```python
import torch

def _softmax(x, axis=-1):
    return torch.softmax(x, dim=axis)
def _gelu(x):
    return torch.nn.functional.gelu(x, approximate="tanh")
def _silu(x):
    return torch.nn.functional.silu(x)
def _sigmoid(x):
    return torch.sigmoid(x)
def _layernorm(x, axis=-1, eps=1e-5):
    m = x.mean(dim=axis, keepdim=True)
    v = x.var(dim=axis, keepdim=True, unbiased=False)
    return (x - m) / torch.sqrt(v + eps)
def _scatter_add(tgt, idx, src, axis=0):
    return tgt.clone().index_add_(axis, idx, src)
def _segment_sum(src, seg, n):
    out = torch.zeros((n,) + src.shape[1:], dtype=src.dtype)
    return out.index_add_(0, seg, src)
def _pad(x, pw):
    flat = tuple(v for pair in reversed(pw) for v in pair)
    return torch.nn.functional.pad(x, flat)

kernel_name = 'concat_heads__compressed_kv_cla__sliding_local_dilated__prompt'

def setup_inputs():

    _w = (lambda a: (lambda t: t.to(torch.bfloat16) if t.is_floating_point() else t)(torch.as_tensor(a)))
    def _A_setup():
        B, H, Sq, Sk, D, M = 1, 32, 2048, 2048, 128, 1024
        Dc = 4 * D
        return {
            "x": _w((lambda *s: torch.randn(*s, dtype=torch.bfloat16) * 0.2)(B, Sq, M)),
            "c_kv": _w((lambda *s: torch.randn(*s, dtype=torch.bfloat16) * 1.6)(B, Sk, Dc)),
            "w_q": _w((lambda *s: torch.randn(*s, dtype=torch.bfloat16) * 3.2)(M, H, D) * (M ** -0.5)),
            "w_uk": _w((lambda *s: torch.randn(*s, dtype=torch.bfloat16) * 3.2)(Dc, H, D) * (Dc ** -0.5)),
            "w_uv": _w((lambda *s: torch.randn(*s, dtype=torch.bfloat16) * 1.6)(Dc, H, D) * (Dc ** -0.5)),
            "w_o": _w((lambda *s: torch.randn(*s, dtype=torch.bfloat16) * 1.6)(H, D, M) * ((H * D) ** -0.5)),
        }
    _A_in = _A_setup()

    def _B_setup():
        B, S, D = 1, 2048, 1024
        return {
            "x": _w((lambda *s: torch.randn(*s, dtype=torch.bfloat16) * 0.2)(B, S, D)),
            "wqkv": _w((lambda *s: torch.randn(*s, dtype=torch.bfloat16) * 0.2)(D, 3 * D)),
            "wo": _w((lambda *s: torch.randn(*s, dtype=torch.bfloat16) * 0.05)(D, D)),
        }
    _B_in = _B_setup()

    return {
        "A_x": _A_in["x"],
        "A_c_kv": _A_in["c_kv"],
        "A_w_q": _A_in["w_q"],
        "A_w_uk": _A_in["w_uk"],
        "A_w_uv": _A_in["w_uv"],
        "A_w_o": _A_in["w_o"],
        "B_x": _B_in["x"],
        "B_wqkv": _B_in["wqkv"],
        "B_wo": _B_in["wo"],
    }

def reference(A_x, A_c_kv, A_w_q, A_w_uk, A_w_uv, A_w_o, B_x, B_wqkv, B_wo):
    def _A_ref(x, c_kv, w_q, w_uk, w_uv, w_o):
        B, Sq, M = x.shape[0], x.shape[1], x.shape[2]
        Sk = c_kv.shape[1]
        H, D = w_q.shape[1], w_q.shape[2]
        q = torch.einsum("bqm,mhd->bhqd", x, w_q)
        k = torch.einsum("bkc,chd->bhkd", c_kv, w_uk)
        v = torch.einsum("bkc,chd->bhkd", c_kv, w_uv)
        scores = torch.einsum("bhqd,bhkd->bhqk", q, k) * (D ** -0.5)
        off = Sk - Sq
        qi = torch.arange(Sq)[:, None]
        kj = torch.arange(Sk)[None, :]
        cmask = kj <= (qi + off)
        scores = torch.where(cmask[None, None], scores, -1e9)
        p = _softmax(scores, axis=-1)
        ctx = torch.einsum("bhqk,bhkd->bhqd", p, v)
        return torch.einsum("bhqd,hdm->bqm", ctx, w_o)

    def _B_ref(x, wqkv, wo):
        B, S, D = x.shape
        H = 32; Dh = D // H
        W, dil = 8, 2
        qkv = (lambda x, *d: x.permute(*d))((x @ wqkv).reshape(B, S, 3, H, Dh), 2, 0, 3, 1, 4)
        q, k, v = qkv[0], qkv[1], qkv[2]
        scale = Dh ** -0.5
        pos = torch.arange(S)
        off = torch.arange(W) * dil
        idx = pos[:, None] - off[None, :]
        mask = idx >= 0
        idx_c = torch.where(mask, idx, 0).reshape(-1)
        kg = k[:, :, idx_c, :].reshape(B, H, S, W, Dh)
        vg = v[:, :, idx_c, :].reshape(B, H, S, W, Dh)
        logits = torch.einsum("bhsd,bhswd->bhsw", q, kg) * scale
        logits = torch.where(mask[None, None, :, :], logits, -1e9)
        attn = _softmax(logits, axis=-1)
        o = torch.einsum("bhsw,bhswd->bhsd", attn, vg)
        return (lambda x, *d: x.permute(*d))(o, 0, 2, 1, 3).reshape(B, S, D) @ wo

    a = _A_ref(A_x, A_c_kv, A_w_q, A_w_uk, A_w_uv, A_w_o)
    b = _B_ref(B_x, B_wqkv, B_wo)
    return torch.concatenate([a, b], axis=-1)
```

```cpp
#include <hip/hip_runtime.h>
#include <stdint.h>
#define LAS __attribute__((address_space(3)))
constexpr size_t LAY_TS = 17408, LAY_HS = 557824, LAY_CRS = 266240, LAY_ORS = 69632, WS_K = 0, WS_V = 17850368, WS_CTX = 35700736, WS_OB = 52740096, WS_FL = 65585152, WS_END = 65634304;
#define MK_ASM_BODY \
"s_load_dwordx16 s[8:23], %0, 0x0\n" \
"s_load_dwordx4 s[24:27], %0, 0x40\n" \
"s_load_dwordx2 s[28:29], %0, 0x50\n" \
"v_readfirstlane_b32 s61, %2\n" \
"s_lshr_b32 s61, s61, 6\n" \
"v_mbcnt_lo_u32_b32 v52, -1, 0\n" \
"v_mbcnt_hi_u32_b32 v52, -1, v52\n" \
"v_lshlrev_b32 v248, 4, v52\n" \
"s_getpc_b64 s[64:65]\n" \
"Lpc_%=:\n" \
"s_lshr_b32 s70, %1, 3\n" \
"s_lshl_b32 s70, s70, 2\n" \
"s_add_u32 s70, s70, s61\n" \
"s_and_b32 s71, s70, 31\n" \
"s_lshl_b32 s71, s71, 12\n" \
"v_lshl_add_u32 v53, v248, 2, s71\n" \
"s_mov_b32 s71, Lcode_end_%=-Lpc_%=-4\n" \
"v_min_u32 v53, s71, v53\n" \
"global_load_dword v253, v53, s[64:65]\n" \
"s_and_b32 s62, %1, 7\n" \
"s_lshl_b32 s62, s62, 2\n" \
"s_lshr_b32 s70, %1, 6\n" \
"s_add_u32 s62, s62, s70\n" \
"s_bfe_u32 s63, %1, 0x30003\n" \
"s_lshl_b32 s87, s61, 13\n" \
"s_lshl_b32 s95, s61, 12\n" \
"v_bfe_u32 v53, v52, 4, 1\n" \
"v_bfe_u32 v54, v52, 2, 2\n" \
"v_lshl_add_u32 v54, v53, 3, v54\n" \
"s_lshl_b32 s71, s61, 4\n" \
"v_add_u32 v54, s71, v54\n" \
"v_lshlrev_b32 v54, 13, v54\n" \
"v_lshlrev_b32 v53, 1, v53\n" \
"v_and_b32 v55, 3, v52\n" \
"v_xor_b32 v53, v53, v55\n" \
"v_lshl_add_u32 v54, v53, 4, v54\n" \
"v_lshrrev_b32 v53, 5, v52\n" \
"v_lshl_add_u32 v54, v53, 6, v54\n" \
"v_add_u32 v40, 4096, v54\n" \
"v_add_u32 v41, 3072, v54\n" \
"v_add_u32 v42, 2176, v54\n" \
"v_add_u32 v43, 1152, v54\n" \
"v_add_u32 v44, 32768, v54\n" \
"v_add_u32 v45, 31744, v54\n" \
"v_add_u32 v46, 30848, v54\n" \
"v_add_u32 v47, 29824, v54\n" \
"v_lshrrev_b32 v55, 3, v52\n" \
"v_and_b32 v56, 7, v52\n" \
"v_xor_b32 v56, v56, v55\n" \
"v_lshlrev_b32 v56, 4, v56\n" \
"v_lshl_add_u32 v57, v55, 10, v56\n" \
"v_add_u32 v32, 528384, v57\n" \
"v_add_u32 v33, 535552, v57\n" \
"v_add_u32 v34, 542720, v57\n" \
"v_add_u32 v35, 549888, v57\n" \
"v_add_u32 v36, 0, v57\n" \
"v_add_u32 v37, 7168, v57\n" \
"v_add_u32 v38, 14336, v57\n" \
"v_add_u32 v39, 21504, v57\n" \
"s_lshl_b32 s88, s62, 8\n" \
"s_lshl_b32 s89, s63, 18\n" \
"s_lshl_b32 s70, s63, 16\n" \
"s_and_b32 s71, s87, 0x4000\n" \
"s_lshl_b32 s71, s71, 1\n" \
"s_add_u32 s70, s70, s71\n" \
"s_andn2_b32 s71, 1, s61\n" \
"s_lshl_b32 s71, s71, 20\n" \
"s_add_u32 s70, s70, s71\n" \
"s_waitcnt lgkmcnt(0)\n" \
"s_add_u32 s52, s14, s88\n" \
"s_addc_u32 s53, s15, 0\n" \
"s_add_u32 s50, s16, s88\n" \
"s_addc_u32 s51, s17, 0\n" \
"s_add_u32 m0, s87, 36864\n" \
"s_nop 0\n" \
"global_load_lds_dwordx4 v40, s[52:53] offset:-4096\n" \
"global_load_lds_dwordx4 v41, s[50:51] offset:-3072\n" \
"global_load_lds_dwordx4 v42, s[52:53] offset:-2048\n" \
"global_load_lds_dwordx4 v43, s[50:51] offset:-1024\n" \
"global_load_lds_dwordx4 v44, s[52:53] offset:0\n" \
"global_load_lds_dwordx4 v45, s[50:51] offset:1024\n" \
"global_load_lds_dwordx4 v46, s[52:53] offset:2048\n" \
"global_load_lds_dwordx4 v47, s[50:51] offset:3072\n" \
"s_add_u32 s52, s52, 0x80000\n" \
"s_addc_u32 s53, s53, 0\n" \
"s_add_u32 s50, s50, 0x80000\n" \
"s_addc_u32 s51, s51, 0\n" \
"s_add_u32 s54, s10, s70\n" \
"s_addc_u32 s55, s11, 0\n" \
"s_add_u32 m0, s87, 4096\n" \
"s_nop 0\n" \
"global_load_lds_dwordx4 v32, s[54:55] offset:-4096\n" \
"global_load_lds_dwordx4 v33, s[54:55] offset:-3072\n" \
"global_load_lds_dwordx4 v34, s[54:55] offset:-2048\n" \
"global_load_lds_dwordx4 v35, s[54:55] offset:-1024\n" \
"global_load_lds_dwordx4 v36, s[54:55] offset:0\n" \
"global_load_lds_dwordx4 v37, s[54:55] offset:1024\n" \
"global_load_lds_dwordx4 v38, s[54:55] offset:2048\n" \
"global_load_lds_dwordx4 v39, s[54:55] offset:3072\n" \
"s_add_u32 s54, s54, 0x80\n" \
"s_addc_u32 s55, s55, 0\n" \
"s_add_u32 m0, s87, 135168\n" \
"s_nop 0\n" \
"global_load_lds_dwordx4 v40, s[52:53] offset:-4096\n" \
"global_load_lds_dwordx4 v41, s[50:51] offset:-3072\n" \
"global_load_lds_dwordx4 v42, s[52:53] offset:-2048\n" \
"global_load_lds_dwordx4 v43, s[50:51] offset:-1024\n" \
"global_load_lds_dwordx4 v44, s[52:53] offset:0\n" \
"global_load_lds_dwordx4 v45, s[50:51] offset:1024\n" \
"global_load_lds_dwordx4 v46, s[52:53] offset:2048\n" \
"global_load_lds_dwordx4 v47, s[50:51] offset:3072\n" \
"s_add_u32 s52, s52, 0x80000\n" \
"s_addc_u32 s53, s53, 0\n" \
"s_add_u32 s50, s50, 0x80000\n" \
"s_addc_u32 s51, s51, 0\n" \
"s_mov_b64 s[56:57], s[28:29]\n" \
"s_and_b32 s57, s57, 0xffff\n" \
"s_mov_b32 s58, 65634304\n" \
"s_mov_b32 s59, 0x20000\n" \
"s_lshl_b32 s66, %1, 2\n" \
"s_add_u32 s66, s66, s61\n" \
"s_lshl_b32 s66, s66, 10\n" \
"s_lshr_b32 s67, %1, 3\n" \
"s_lshl_b32 s67, s67, 2\n" \
"s_add_u32 s67, s67, s61\n" \
"s_lshl_b32 s67, s67, 13\n" \
"s_and_b32 s64, %1, 7\n" \
"s_lshl_b32 s64, s64, 10\n" \
"s_add_u32 s67, s67, s64\n" \
"v_add_u32 v228, 4096, v54\n" \
"v_add_u32 v229, 3200, v54\n" \
"v_lshl_add_u32 v57, v55, 11, v56\n" \
"v_add_u32 v224, 0, v57\n" \
"v_add_u32 v225, 15360, v57\n" \
"v_add_u32 v226, 129024, v57\n" \
"v_add_u32 v227, 144384, v57\n" \
"s_lshl_b32 s70, s61, 15\n" \
"s_add_u32 s70, s70, 0\n" \
"s_add_u32 s71, s70, s89\n" \
"s_add_u32 s71, s71, 4096\n" \
"s_add_u32 s48, s8, s71\n" \
"s_addc_u32 s49, s9, 0\n" \
"s_sub_u32 s71, s70, s89\n" \
"s_add_u32 s71, s71, 3932160\n" \
"s_add_u32 s46, s8, s71\n" \
"s_addc_u32 s47, s9, 0\n" \
"s_add_u32 s70, s88, 0\n" \
"s_add_u32 s44, s12, s70\n" \
"s_addc_u32 s45, s13, 0\n" \
"s_add_u32 s42, s44, 30720\n" \
"s_addc_u32 s43, s45, 0\n" \
"s_lshr_b32 s70, s28, 31\n" \
"s_lshl_b32 s70, s70, 30\n" \
"s_sub_u32 s28, s28, s70\n" \
"v_add_u32 v250, s70, v248\n" \
"v_lshlrev_b32 v245, 2, v52\n" \
"v_mov_b32 v254, 0x30000\n" \
"v_lshrrev_b32 v55, 4, v52\n" \
"v_and_b32 v249, 0xf0, v248\n" \
"v_lshl_add_u32 v249, v55, 9, v249\n" \
"v_and_b32 v53, 15, v52\n" \
"s_lshl_b32 s70, s61, 4\n" \
"v_lshlrev_b32 v54, 2, v55\n" \
"v_sub_u32 v251, v53, v54\n" \
"v_add_u32 v251, s70, v251\n" \
"v_and_b32 v54, 7, v52\n" \
"v_xor_b32 v54, v54, v55\n" \
"v_lshlrev_b32 v53, 7, v53\n" \
"s_add_u32 s71, s87, 131072\n" \
"s_lshr_b32 s70, s61, 1\n" \
"s_lshl_b32 s70, s70, 14\n" \
"s_add_u32 s70, s70, 0\n" \
"v_xor_b32 v56, 0, v54\n" \
"v_lshl_add_u32 v56, v56, 4, v53\n" \
"v_add_u32 v230, s71, v56\n" \
"v_add_u32 v48, s70, v56\n" \
"v_xor_b32 v56, 4, v54\n" \
"v_lshl_add_u32 v56, v56, 4, v53\n" \
"v_add_u32 v231, s71, v56\n" \
"v_add_u32 v49, s70, v56\n" \
"v_and_b32 v53, 3, v52\n" \
"v_lshlrev_b32 v53, 3, v53\n" \
"v_bfe_u32 v54, v52, 2, 2\n" \
"v_lshl_add_u32 v53, v54, 6, v53\n" \
"v_bfe_u32 v54, v52, 4, 1\n" \
"v_lshl_add_u32 v53, v54, 8, v53\n" \
"v_lshl_add_u32 v53, v54, 5, v53\n" \
"v_lshrrev_b32 v54, 5, v52\n" \
"s_and_b32 s70, s61, 1\n" \
"s_lshl_b32 s70, s70, 11\n" \
"s_add_u32 s70, s70, 32768\n" \
"v_lshl_add_u32 v56, v54, 13, v53\n" \
"v_add_u32 v50, s70, v56\n" \
"v_xor_b32 v51, 32, v50\n" \
"v_lshl_add_u32 v232, v54, 12, v53\n" \
"v_xor_b32 v233, 32, v232\n" \
"s_mov_b32 s90, 65593344\n" \
"buffer_load_dword v244, off, s[56:59], s90 sc1\n" \
"s_add_u32 s68, s66, 0x0\n" \
"s_add_u32 s64, s10, s68\n" \
"s_addc_u32 s65, s11, 0\n" \
"global_load_dword v253, v248, s[64:65]\n" \
"s_add_u32 s68, s66, 0x100000\n" \
"s_add_u32 s64, s10, s68\n" \
"s_addc_u32 s65, s11, 0\n" \
"global_load_dword v253, v248, s[64:65]\n" \
"s_add_u32 s68, s67, 0x100000\n" \
"s_add_u32 s64, s14, s68\n" \
"s_addc_u32 s65, s15, 0\n" \
"global_load_dword v253, v248, s[64:65]\n" \
"s_add_u32 s68, s67, 0x100000\n" \
"s_add_u32 s64, s16, s68\n" \
"s_addc_u32 s65, s17, 0\n" \
"global_load_dword v253, v248, s[64:65]\n" \
"s_mov_b64 exec, 0\n" \
"s_cmp_eq_u32 s61, 0\n" \
"s_cbranch_scc1 Lst0x0_%=\n" \
"s_cmp_eq_u32 s61, 1\n" \
"s_cbranch_scc1 Lst0x1_%=\n" \
"s_cmp_eq_u32 s61, 2\n" \
"s_cbranch_scc1 Lst0x2_%=\n" \
"s_branch Lst0x3_%=\n" \
"Lcb0_%=:\n" \
"s_mov_b64 exec, -1\n" \
"s_waitcnt vmcnt(13)\n" \
"s_barrier\n" \
"ds_read_b128 v[64:67], v48 offset:0\n" \
"ds_read_b64_tr_b16 v[128:129], v50 offset:0\n" \
"ds_read_b128 v[68:71], v49 offset:0\n" \
"ds_read_b64_tr_b16 v[130:131], v50 offset:4096\n" \
"ds_read_b64_tr_b16 v[132:133], v50 offset:16384\n" \
"ds_read_b64_tr_b16 v[134:135], v50 offset:20480\n" \
"ds_read_b128 v[72:75], v48 offset:2048\n" \
"ds_read_b64_tr_b16 v[136:137], v51 offset:0\n" \
"ds_read_b128 v[76:79], v49 offset:2048\n" \
"ds_read_b64_tr_b16 v[138:139], v51 offset:4096\n" \
"ds_read_b64_tr_b16 v[140:141], v51 offset:16384\n" \
"ds_read_b64_tr_b16 v[142:143], v51 offset:20480\n" \
"ds_read_b128 v[80:83], v48 offset:4096\n" \
"ds_read_b64_tr_b16 v[144:145], v50 offset:512\n" \
"ds_read_b128 v[84:87], v49 offset:4096\n" \
"ds_read_b64_tr_b16 v[146:147], v50 offset:4608\n" \
"ds_read_b64_tr_b16 v[148:149], v50 offset:16896\n" \
"ds_read_b64_tr_b16 v[150:151], v50 offset:20992\n" \
"ds_read_b128 v[88:91], v48 offset:6144\n" \
"ds_read_b64_tr_b16 v[152:153], v51 offset:512\n" \
"ds_read_b128 v[92:95], v49 offset:6144\n" \
"ds_read_b64_tr_b16 v[154:155], v51 offset:4608\n" \
"ds_read_b64_tr_b16 v[156:157], v51 offset:16896\n" \
"ds_read_b64_tr_b16 v[158:159], v51 offset:20992\n" \
"ds_read_b128 v[96:99], v48 offset:8192\n" \
"ds_read_b128 v[100:103], v49 offset:8192\n" \
"ds_read_b128 v[104:107], v48 offset:10240\n" \
"ds_read_b128 v[108:111], v49 offset:10240\n" \
"ds_read_b128 v[112:115], v48 offset:12288\n" \
"ds_read_b128 v[116:119], v49 offset:12288\n" \
"s_waitcnt lgkmcnt(15)\n" \
"v_mfma_f32_16x16x32_bf16 a[0:3], v[128:131], v[64:67], 0\n" \
"ds_read_b128 v[120:123], v48 offset:14336\n" \
"s_add_u32 m0, s87, 69632\n" \
"v_mfma_f32_16x16x32_bf16 a[0:3], v[132:135], v[68:71], a[0:3]\n" \
"global_load_lds_dwordx4 v32, s[54:55] offset:-4096\n" \
"v_mfma_f32_16x16x32_bf16 a[4:7], v[136:139], v[64:67], 0\n" \
"ds_read_b128 v[124:127], v49 offset:14336\n" \
"v_mfma_f32_16x16x32_bf16 a[4:7], v[140:143], v[68:71], a[4:7]\n" \
"s_waitcnt lgkmcnt(14)\n" \
"v_mfma_f32_16x16x32_bf16 a[8:11], v[144:147], v[64:67], 0\n" \
"ds_read_b64_tr_b16 v[160:161], v50 offset:1024\n" \
"global_load_lds_dwordx4 v33, s[54:55] offset:-3072\n" \
"v_mfma_f32_16x16x32_bf16 a[8:11], v[148:151], v[68:71], a[8:11]\n" \
"s_waitcnt lgkmcnt(2)\n" \
"v_mfma_f32_16x16x32_bf16 a[12:15], v[152:155], v[64:67], 0\n" \
"ds_read_b64_tr_b16 v[162:163], v50 offset:5120\n" \
"v_mfma_f32_16x16x32_bf16 a[12:15], v[156:159], v[68:71], a[12:15]\n" \
"global_load_lds_dwordx4 v34, s[54:55] offset:-2048\n" \
"v_mfma_f32_16x16x32_bf16 a[32:35], v[128:131], v[72:75], 0\n" \
"ds_read_b64_tr_b16 v[164:165], v50 offset:17408\n" \
"v_mfma_f32_16x16x32_bf16 a[32:35], v[132:135], v[76:79], a[32:35]\n" \
"v_mfma_f32_16x16x32_bf16 a[36:39], v[136:139], v[72:75], 0\n" \
"ds_read_b64_tr_b16 v[166:167], v50 offset:21504\n" \
"global_load_lds_dwordx4 v35, s[54:55] offset:-1024\n" \
"v_mfma_f32_16x16x32_bf16 a[36:39], v[140:143], v[76:79], a[36:39]\n" \
"v_mfma_f32_16x16x32_bf16 a[40:43], v[144:147], v[72:75], 0\n" \
"ds_read_b64_tr_b16 v[168:169], v51 offset:1024\n" \
"v_mfma_f32_16x16x32_bf16 a[40:43], v[148:151], v[76:79], a[40:43]\n" \
"global_load_lds_dwordx4 v36, s[54:55] offset:0\n" \
"v_mfma_f32_16x16x32_bf16 a[44:47], v[152:155], v[72:75], 0\n" \
"ds_read_b64_tr_b16 v[170:171], v51 offset:5120\n" \
"v_mfma_f32_16x16x32_bf16 a[44:47], v[156:159], v[76:79], a[44:47]\n" \
"v_mfma_f32_16x16x32_bf16 a[64:67], v[128:131], v[80:83], 0\n" \
"ds_read_b64_tr_b16 v[172:173], v51 offset:17408\n" \
"global_load_lds_dwordx4 v37, s[54:55] offset:1024\n" \
"v_mfma_f32_16x16x32_bf16 a[64:67], v[132:135], v[84:87], a[64:67]\n" \
"v_mfma_f32_16x16x32_bf16 a[68:71], v[136:139], v[80:83], 0\n" \
"ds_read_b64_tr_b16 v[174:175], v51 offset:21504\n" \
"v_mfma_f32_16x16x32_bf16 a[68:71], v[140:143], v[84:87], a[68:71]\n" \
"global_load_lds_dwordx4 v38, s[54:55] offset:2048\n" \
"v_mfma_f32_16x16x32_bf16 a[72:75], v[144:147], v[80:83], 0\n" \
"ds_read_b64_tr_b16 v[176:177], v50 offset:1536\n" \
"v_mfma_f32_16x16x32_bf16 a[72:75], v[148:151], v[84:87], a[72:75]\n" \
"v_mfma_f32_16x16x32_bf16 a[76:79], v[152:155], v[80:83], 0\n" \
"ds_read_b64_tr_b16 v[178:179], v50 offset:5632\n" \
"global_load_lds_dwordx4 v39, s[54:55] offset:3072\n" \
"v_mfma_f32_16x16x32_bf16 a[76:79], v[156:159], v[84:87], a[76:79]\n" \
"s_add_u32 s54, s54, 0x80\n" \
"s_addc_u32 s55, s55, 0\n" \
"v_mfma_f32_16x16x32_bf16 a[96:99], v[128:131], v[88:91], 0\n" \
"s_add_u32 m0, s87, 102400\n" \
"ds_read_b64_tr_b16 v[180:181], v50 offset:17920\n" \
"v_mfma_f32_16x16x32_bf16 a[96:99], v[132:135], v[92:95], a[96:99]\n" \
"global_load_lds_dwordx4 v40, s[52:53] offset:-4096\n" \
"v_mfma_f32_16x16x32_bf16 a[100:103], v[136:139], v[88:91], 0\n" \
"ds_read_b64_tr_b16 v[182:183], v50 offset:22016\n" \
"v_mfma_f32_16x16x32_bf16 a[100:103], v[140:143], v[92:95], a[100:103]\n" \
"v_mfma_f32_16x16x32_bf16 a[104:107], v[144:147], v[88:91], 0\n" \
"ds_read_b64_tr_b16 v[184:185], v51 offset:1536\n" \
"global_load_lds_dwordx4 v41, s[50:51] offset:-3072\n" \
"v_mfma_f32_16x16x32_bf16 a[104:107], v[148:151], v[92:95], a[104:107]\n" \
"v_mfma_f32_16x16x32_bf16 a[108:111], v[152:155], v[88:91], 0\n" \
"ds_read_b64_tr_b16 v[186:187], v51 offset:5632\n" \
"v_mfma_f32_16x16x32_bf16 a[108:111], v[156:159], v[92:95], a[108:111]\n" \
"global_load_lds_dwordx4 v42, s[52:53] offset:-2048\n" \
"v_mfma_f32_16x16x32_bf16 a[128:131], v[128:131], v[96:99], 0\n" \
"ds_read_b64_tr_b16 v[188:189], v51 offset:17920\n" \
"v_mfma_f32_16x16x32_bf16 a[128:131], v[132:135], v[100:103], a[128:131]\n" \
"v_mfma_f32_16x16x32_bf16 a[132:135], v[136:139], v[96:99], 0\n" \
"ds_read_b64_tr_b16 v[190:191], v51 offset:22016\n" \
"global_load_lds_dwordx4 v43, s[50:51] offset:-1024\n" \
"v_mfma_f32_16x16x32_bf16 a[132:135], v[140:143], v[100:103], a[132:135]\n" \
"v_mfma_f32_16x16x32_bf16 a[136:139], v[144:147], v[96:99], 0\n" \
"v_mfma_f32_16x16x32_bf16 a[136:139], v[148:151], v[100:103], a[136:139]\n" \
"global_load_lds_dwordx4 v44, s[52:53] offset:0\n" \
"v_mfma_f32_16x16x32_bf16 a[140:143], v[152:155], v[96:99], 0\n" \
"v_mfma_f32_16x16x32_bf16 a[140:143], v[156:159], v[100:103], a[140:143]\n" \
"global_load_lds_dwordx4 v45, s[50:51] offset:1024\n" \
"v_mfma_f32_16x16x32_bf16 a[160:163], v[128:131], v[104:107], 0\n" \
"v_add_u32 v48, 0x10000, v48\n" \
"v_add_u32 v49, 0x10000, v49\n" \
"v_mfma_f32_16x16x32_bf16 a[160:163], v[132:135], v[108:111], a[160:163]\n" \
"v_add_u32 v50, 0x18000, v50\n" \
"v_add_u32 v51, 0x18000, v51\n" \
"v_mfma_f32_16x16x32_bf16 a[164:167], v[136:139], v[104:107], 0\n" \
"v_mfma_f32_16x16x32_bf16 a[164:167], v[140:143], v[108:111], a[164:167]\n" \
"global_load_lds_dwordx4 v46, s[52:53] offset:2048\n" \
"v_mfma_f32_16x16x32_bf16 a[168:171], v[144:147], v[104:107], 0\n" \
"v_mfma_f32_16x16x32_bf16 a[168:171], v[148:151], v[108:111], a[168:171]\n" \
"v_mfma_f32_16x16x32_bf16 a[172:175], v[152:155], v[104:107], 0\n" \
"global_load_lds_dwordx4 v47, s[50:51] offset:3072\n" \
"v_mfma_f32_16x16x32_bf16 a[172:175], v[156:159], v[108:111], a[172:175]\n" \
"s_add_u32 s52, s52, 0x80000\n" \
"s_addc_u32 s53, s53, 0\n" \
"v_mfma_f32_16x16x32_bf16 a[192:195], v[128:131], v[112:115], 0\n" \
"s_add_u32 s50, s50, 0x80000\n" \
"s_addc_u32 s51, s51, 0\n" \
"v_mfma_f32_16x16x32_bf16 a[192:195], v[132:135], v[116:119], a[192:195]\n" \
"v_mfma_f32_16x16x32_bf16 a[196:199], v[136:139], v[112:115], 0\n" \
"v_mfma_f32_16x16x32_bf16 a[196:199], v[140:143], v[116:119], a[196:199]\n" \
"v_mfma_f32_16x16x32_bf16 a[200:203], v[144:147], v[112:115], 0\n" \
"v_mfma_f32_16x16x32_bf16 a[200:203], v[148:151], v[116:119], a[200:203]\n" \
"v_mfma_f32_16x16x32_bf16 a[204:207], v[152:155], v[112:115], 0\n" \
"v_mfma_f32_16x16x32_bf16 a[204:207], v[156:159], v[116:119], a[204:207]\n" \
"s_waitcnt lgkmcnt(0)\n" \
"v_mfma_f32_16x16x32_bf16 a[224:227], v[128:131], v[120:123], 0\n" \
"v_mfma_f32_16x16x32_bf16 a[224:227], v[132:135], v[124:127], a[224:227]\n" \
"v_mfma_f32_16x16x32_bf16 a[228:231], v[136:139], v[120:123], 0\n" \
"v_mfma_f32_16x16x32_bf16 a[228:231], v[140:143], v[124:127], a[228:231]\n" \
"v_mfma_f32_16x16x32_bf16 a[232:235], v[144:147], v[120:123], 0\n" \
"v_mfma_f32_16x16x32_bf16 a[232:235], v[148:151], v[124:127], a[232:235]\n" \
"v_mfma_f32_16x16x32_bf16 a[236:239], v[152:155], v[120:123], 0\n" \
"s_waitcnt vmcnt(8) lgkmcnt(0)\n" \
"v_mfma_f32_16x16x32_bf16 a[236:239], v[156:159], v[124:127], a[236:239]\n" \
"s_barrier\n" \
"v_mfma_f32_16x16x32_bf16 a[16:19], v[64:67], v[160:163], 0\n" \
"s_add_u32 m0, s87, 36864\n" \
"ds_read_b64_tr_b16 v[128:129], v50 offset:0\n" \
"v_mfma_f32_16x16x32_bf16 a[16:19], v[68:71], v[164:167], a[16:19]\n" \
"global_load_lds_dwordx4 v32, s[54:55] offset:-4096\n" \
"v_mfma_f32_16x16x32_bf16 a[20:23], v[64:67], v[168:171], 0\n" \
"ds_read_b64_tr_b16 v[130:131], v50 offset:4096\n" \
"v_mfma_f32_16x16x32_bf16 a[20:23], v[68:71], v[172:175], a[20:23]\n" \
"v_mfma_f32_16x16x32_bf16 a[24:27], v[64:67], v[176:179], 0\n" \
"ds_read_b64_tr_b16 v[132:133], v50 offset:16384\n" \
"v_mfma_f32_16x16x32_bf16 a[24:27], v[68:71], v[180:183], a[24:27]\n" \
"global_load_lds_dwordx4 v33, s[54:55] offset:-3072\n" \
"v_mfma_f32_16x16x32_bf16 a[28:31], v[64:67], v[184:187], 0\n" \
"ds_read_b64_tr_b16 v[134:135], v50 offset:20480\n" \
"v_mfma_f32_16x16x32_bf16 a[28:31], v[68:71], v[188:191], a[28:31]\n" \
"v_mfma_f32_16x16x32_bf16 a[48:51], v[72:75], v[160:163], 0\n" \
"ds_read_b128 v[64:67], v48 offset:0\n" \
"v_mfma_f32_16x16x32_bf16 a[48:51], v[76:79], v[164:167], a[48:51]\n" \
"global_load_lds_dwordx4 v34, s[54:55] offset:-2048\n" \
"v_mfma_f32_16x16x32_bf16 a[52:55], v[72:75], v[168:171], 0\n" \
"ds_read_b64_tr_b16 v[136:137], v51 offset:0\n" \
"v_mfma_f32_16x16x32_bf16 a[52:55], v[76:79], v[172:175], a[52:55]\n" \
"v_mfma_f32_16x16x32_bf16 a[56:59], v[72:75], v[176:179], 0\n" \
"ds_read_b128 v[68:71], v49 offset:0\n" \
"v_mfma_f32_16x16x32_bf16 a[56:59], v[76:79], v[180:183], a[56:59]\n" \
"global_load_lds_dwordx4 v35, s[54:55] offset:-1024\n" \
"v_mfma_f32_16x16x32_bf16 a[60:63], v[72:75], v[184:187], 0\n" \
"ds_read_b64_tr_b16 v[138:139], v51 offset:4096\n" \
"v_mfma_f32_16x16x32_bf16 a[60:63], v[76:79], v[188:191], a[60:63]\n" \
"v_mfma_f32_16x16x32_bf16 a[80:83], v[80:83], v[160:163], 0\n" \
"ds_read_b128 v[72:75], v48 offset:2048\n" \
"v_mfma_f32_16x16x32_bf16 a[80:83], v[84:87], v[164:167], a[80:83]\n" \
"global_load_lds_dwordx4 v36, s[54:55] offset:0\n" \
"v_mfma_f32_16x16x32_bf16 a[84:87], v[80:83], v[168:171], 0\n" \
"ds_read_b64_tr_b16 v[140:141], v51 offset:16384\n" \
"v_mfma_f32_16x16x32_bf16 a[84:87], v[84:87], v[172:175], a[84:87]\n" \
"v_mfma_f32_16x16x32_bf16 a[88:91], v[80:83], v[176:179], 0\n" \
"ds_read_b128 v[76:79], v49 offset:2048\n" \
"v_mfma_f32_16x16x32_bf16 a[88:91], v[84:87], v[180:183], a[88:91]\n" \
"global_load_lds_dwordx4 v37, s[54:55] offset:1024\n" \
"v_mfma_f32_16x16x32_bf16 a[92:95], v[80:83], v[184:187], 0\n" \
"ds_read_b64_tr_b16 v[142:143], v51 offset:20480\n" \
"v_mfma_f32_16x16x32_bf16 a[92:95], v[84:87], v[188:191], a[92:95]\n" \
"v_mfma_f32_16x16x32_bf16 a[112:115], v[88:91], v[160:163], 0\n" \
"ds_read_b128 v[80:83], v48 offset:4096\n" \
"v_mfma_f32_16x16x32_bf16 a[112:115], v[92:95], v[164:167], a[112:115]\n" \
"global_load_lds_dwordx4 v38, s[54:55] offset:2048\n" \
"v_mfma_f32_16x16x32_bf16 a[116:119], v[88:91], v[168:171], 0\n" \
"ds_read_b64_tr_b16 v[144:145], v50 offset:512\n" \
"v_mfma_f32_16x16x32_bf16 a[116:119], v[92:95], v[172:175], a[116:119]\n" \
"v_mfma_f32_16x16x32_bf16 a[120:123], v[88:91], v[176:179], 0\n" \
"ds_read_b128 v[84:87], v49 offset:4096\n" \
"v_mfma_f32_16x16x32_bf16 a[120:123], v[92:95], v[180:183], a[120:123]\n" \
"global_load_lds_dwordx4 v39, s[54:55] offset:3072\n" \
"v_mfma_f32_16x16x32_bf16 a[124:127], v[88:91], v[184:187], 0\n" \
"s_add_u32 s54, s54, 0x80\n" \
"s_addc_u32 s55, s55, 0\n" \
"ds_read_b64_tr_b16 v[146:147], v50 offset:4608\n" \
"v_mfma_f32_16x16x32_bf16 a[124:127], v[92:95], v[188:191], a[124:127]\n" \
"s_add_u32 s68, s67, 0x200000\n" \
"s_add_u32 s64, s14, s68\n" \
"s_addc_u32 s65, s15, 0\n" \
"v_mfma_f32_16x16x32_bf16 a[144:147], v[96:99], v[160:163], 0\n" \
"ds_read_b128 v[88:91], v48 offset:6144\n" \
"v_mfma_f32_16x16x32_bf16 a[144:147], v[100:103], v[164:167], a[144:147]\n" \
"global_load_dword v253, v248, s[64:65]\n" \
"v_mfma_f32_16x16x32_bf16 a[148:151], v[96:99], v[168:171], 0\n" \
"s_add_u32 s68, s67, 0x200000\n" \
"ds_read_b64_tr_b16 v[148:149], v50 offset:16896\n" \
"v_mfma_f32_16x16x32_bf16 a[148:151], v[100:103], v[172:175], a[148:151]\n" \
"s_add_u32 s64, s16, s68\n" \
"s_addc_u32 s65, s17, 0\n" \
"v_mfma_f32_16x16x32_bf16 a[152:155], v[96:99], v[176:179], 0\n" \
"ds_read_b128 v[92:95], v49 offset:6144\n" \
"v_mfma_f32_16x16x32_bf16 a[152:155], v[100:103], v[180:183], a[152:155]\n" \
"global_load_dword v253, v248, s[64:65]\n" \
"v_mfma_f32_16x16x32_bf16 a[156:159], v[96:99], v[184:187], 0\n" \
"ds_read_b64_tr_b16 v[150:151], v50 offset:20992\n" \
"v_mfma_f32_16x16x32_bf16 a[156:159], v[100:103], v[188:191], a[156:159]\n" \
"v_mfma_f32_16x16x32_bf16 a[176:179], v[104:107], v[160:163], 0\n" \
"ds_read_b128 v[96:99], v48 offset:8192\n" \
"v_mfma_f32_16x16x32_bf16 a[176:179], v[108:111], v[164:167], a[176:179]\n" \
"v_mfma_f32_16x16x32_bf16 a[180:183], v[104:107], v[168:171], 0\n" \
"ds_read_b64_tr_b16 v[152:153], v51 offset:512\n" \
"v_mfma_f32_16x16x32_bf16 a[180:183], v[108:111], v[172:175], a[180:183]\n" \
"v_mfma_f32_16x16x32_bf16 a[184:187], v[104:107], v[176:179], 0\n" \
"ds_read_b128 v[100:103], v49 offset:8192\n" \
"v_mfma_f32_16x16x32_bf16 a[184:187], v[108:111], v[180:183], a[184:187]\n" \
"v_mfma_f32_16x16x32_bf16 a[188:191], v[104:107], v[184:187], 0\n" \
"ds_read_b64_tr_b16 v[154:155], v51 offset:4608\n" \
"v_mfma_f32_16x16x32_bf16 a[188:191], v[108:111], v[188:191], a[188:191]\n" \
"v_mfma_f32_16x16x32_bf16 a[208:211], v[112:115], v[160:163], 0\n" \
"ds_read_b128 v[104:107], v48 offset:10240\n" \
"v_mfma_f32_16x16x32_bf16 a[208:211], v[116:119], v[164:167], a[208:211]\n" \
"v_mfma_f32_16x16x32_bf16 a[212:215], v[112:115], v[168:171], 0\n" \
"ds_read_b64_tr_b16 v[156:157], v51 offset:16896\n" \
"v_mfma_f32_16x16x32_bf16 a[212:215], v[116:119], v[172:175], a[212:215]\n" \
"v_mfma_f32_16x16x32_bf16 a[216:219], v[112:115], v[176:179], 0\n" \
"ds_read_b128 v[108:111], v49 offset:10240\n" \
"v_mfma_f32_16x16x32_bf16 a[216:219], v[116:119], v[180:183], a[216:219]\n" \
"v_mfma_f32_16x16x32_bf16 a[220:223], v[112:115], v[184:187], 0\n" \
"ds_read_b64_tr_b16 v[158:159], v51 offset:20992\n" \
"v_mfma_f32_16x16x32_bf16 a[220:223], v[116:119], v[188:191], a[220:223]\n" \
"v_mfma_f32_16x16x32_bf16 a[240:243], v[120:123], v[160:163], 0\n" \
"ds_read_b128 v[112:115], v48 offset:12288\n" \
"v_mfma_f32_16x16x32_bf16 a[240:243], v[124:127], v[164:167], a[240:243]\n" \
"v_mfma_f32_16x16x32_bf16 a[244:247], v[120:123], v[168:171], 0\n" \
"v_mfma_f32_16x16x32_bf16 a[244:247], v[124:127], v[172:175], a[244:247]\n" \
"v_mfma_f32_16x16x32_bf16 a[248:251], v[120:123], v[176:179], 0\n" \
"ds_read_b128 v[116:119], v49 offset:12288\n" \
"v_mfma_f32_16x16x32_bf16 a[248:251], v[124:127], v[180:183], a[248:251]\n" \
"v_mfma_f32_16x16x32_bf16 a[252:255], v[120:123], v[184:187], 0\n" \
"v_mfma_f32_16x16x32_bf16 a[252:255], v[124:127], v[188:191], a[252:255]\n" \
"s_waitcnt lgkmcnt(1)\n" \
"v_mfma_f32_16x16x32_bf16 a[0:3], v[128:131], v[64:67], a[0:3]\n" \
"ds_read_b128 v[120:123], v48 offset:14336\n" \
"v_mfma_f32_16x16x32_bf16 a[0:3], v[132:135], v[68:71], a[0:3]\n" \
"v_mfma_f32_16x16x32_bf16 a[4:7], v[136:139], v[64:67], a[4:7]\n" \
"ds_read_b128 v[124:127], v49 offset:14336\n" \
"s_add_u32 m0, s87, 4096\n" \
"v_mfma_f32_16x16x32_bf16 a[4:7], v[140:143], v[68:71], a[4:7]\n" \
"global_load_lds_dwordx4 v40, s[52:53] offset:-4096\n" \
"v_mfma_f32_16x16x32_bf16 a[8:11], v[144:147], v[64:67], a[8:11]\n" \
"ds_read_b64_tr_b16 v[160:161], v50 offset:1024\n" \
"v_mfma_f32_16x16x32_bf16 a[8:11], v[148:151], v[68:71], a[8:11]\n" \
"v_mfma_f32_16x16x32_bf16 a[12:15], v[152:155], v[64:67], a[12:15]\n" \
"ds_read_b64_tr_b16 v[162:163], v50 offset:5120\n" \
"v_mfma_f32_16x16x32_bf16 a[12:15], v[156:159], v[68:71], a[12:15]\n" \
"global_load_lds_dwordx4 v41, s[50:51] offset:-3072\n" \
"v_mfma_f32_16x16x32_bf16 a[32:35], v[128:131], v[72:75], a[32:35]\n" \
"ds_read_b64_tr_b16 v[164:165], v50 offset:17408\n" \
"v_mfma_f32_16x16x32_bf16 a[32:35], v[132:135], v[76:79], a[32:35]\n" \
"v_mfma_f32_16x16x32_bf16 a[36:39], v[136:139], v[72:75], a[36:39]\n" \
"ds_read_b64_tr_b16 v[166:167], v50 offset:21504\n" \
"v_mfma_f32_16x16x32_bf16 a[36:39], v[140:143], v[76:79], a[36:39]\n" \
"global_load_lds_dwordx4 v42, s[52:53] offset:-2048\n" \
"v_mfma_f32_16x16x32_bf16 a[40:43], v[144:147], v[72:75], a[40:43]\n" \
"ds_read_b64_tr_b16 v[168:169], v51 offset:1024\n" \
"v_mfma_f32_16x16x32_bf16 a[40:43], v[148:151], v[76:79], a[40:43]\n" \
"v_mfma_f32_16x16x32_bf16 a[44:47], v[152:155], v[72:75], a[44:47]\n" \
"ds_read_b64_tr_b16 v[170:171], v51 offset:5120\n" \
"v_mfma_f32_16x16x32_bf16 a[44:47], v[156:159], v[76:79], a[44:47]\n" \
"global_load_lds_dwordx4 v43, s[50:51] offset:-1024\n" \
"v_mfma_f32_16x16x32_bf16 a[64:67], v[128:131], v[80:83], a[64:67]\n" \
"ds_read_b64_tr_b16 v[172:173], v51 offset:17408\n" \
"v_mfma_f32_16x16x32_bf16 a[64:67], v[132:135], v[84:87], a[64:67]\n" \
"v_mfma_f32_16x16x32_bf16 a[68:71], v[136:139], v[80:83], a[68:71]\n" \
"ds_read_b64_tr_b16 v[174:175], v51 offset:21504\n" \
"v_mfma_f32_16x16x32_bf16 a[68:71], v[140:143], v[84:87], a[68:71]\n" \
"global_load_lds_dwordx4 v44, s[52:53] offset:0\n" \
"v_mfma_f32_16x16x32_bf16 a[72:75], v[144:147], v[80:83], a[72:75]\n" \
"ds_read_b64_tr_b16 v[176:177], v50 offset:1536\n" \
"v_mfma_f32_16x16x32_bf16 a[72:75], v[148:151], v[84:87], a[72:75]\n" \
"v_mfma_f32_16x16x32_bf16 a[76:79], v[152:155], v[80:83], a[76:79]\n" \
"ds_read_b64_tr_b16 v[178:179], v50 offset:5632\n" \
"v_mfma_f32_16x16x32_bf16 a[76:79], v[156:159], v[84:87], a[76:79]\n" \
"global_load_lds_dwordx4 v45, s[50:51] offset:1024\n" \
"v_mfma_f32_16x16x32_bf16 a[96:99], v[128:131], v[88:91], a[96:99]\n" \
"ds_read_b64_tr_b16 v[180:181], v50 offset:17920\n" \
"v_mfma_f32_16x16x32_bf16 a[96:99], v[132:135], v[92:95], a[96:99]\n" \
"v_mfma_f32_16x16x32_bf16 a[100:103], v[136:139], v[88:91], a[100:103]\n" \
"ds_read_b64_tr_b16 v[182:183], v50 offset:22016\n" \
"v_mfma_f32_16x16x32_bf16 a[100:103], v[140:143], v[92:95], a[100:103]\n" \
"global_load_lds_dwordx4 v46, s[52:53] offset:2048\n" \
"v_mfma_f32_16x16x32_bf16 a[104:107], v[144:147], v[88:91], a[104:107]\n" \
"ds_read_b64_tr_b16 v[184:185], v51 offset:1536\n" \
"v_mfma_f32_16x16x32_bf16 a[104:107], v[148:151], v[92:95], a[104:107]\n" \
"v_mfma_f32_16x16x32_bf16 a[108:111], v[152:155], v[88:91], a[108:111]\n" \
"ds_read_b64_tr_b16 v[186:187], v51 offset:5632\n" \
"v_mfma_f32_16x16x32_bf16 a[108:111], v[156:159], v[92:95], a[108:111]\n" \
"global_load_lds_dwordx4 v47, s[50:51] offset:3072\n" \
"v_mfma_f32_16x16x32_bf16 a[128:131], v[128:131], v[96:99], a[128:131]\n" \
"s_add_u32 s52, s52, 0x80000\n" \
"s_addc_u32 s53, s53, 0\n" \
"ds_read_b64_tr_b16 v[188:189], v51 offset:17920\n" \
"v_mfma_f32_16x16x32_bf16 a[128:131], v[132:135], v[100:103], a[128:131]\n" \
"s_add_u32 s50, s50, 0x80000\n" \
"s_addc_u32 s51, s51, 0\n" \
"v_mfma_f32_16x16x32_bf16 a[132:135], v[136:139], v[96:99], a[132:135]\n" \
"ds_read_b64_tr_b16 v[190:191], v51 offset:22016\n" \
"v_mfma_f32_16x16x32_bf16 a[132:135], v[140:143], v[100:103], a[132:135]\n" \
"v_mfma_f32_16x16x32_bf16 a[136:139], v[144:147], v[96:99], a[136:139]\n" \
"v_mfma_f32_16x16x32_bf16 a[136:139], v[148:151], v[100:103], a[136:139]\n" \
"v_mfma_f32_16x16x32_bf16 a[140:143], v[152:155], v[96:99], a[140:143]\n" \
"v_mfma_f32_16x16x32_bf16 a[140:143], v[156:159], v[100:103], a[140:143]\n" \
"v_mfma_f32_16x16x32_bf16 a[160:163], v[128:131], v[104:107], a[160:163]\n" \
"v_add_u32 v48, 0xffff8000, v48\n" \
"v_add_u32 v49, 0xffff8000, v49\n" \
"v_mfma_f32_16x16x32_bf16 a[160:163], v[132:135], v[108:111], a[160:163]\n" \
"v_add_u32 v50, 0xffff8000, v50\n" \
"v_add_u32 v51, 0xffff8000, v51\n" \
"v_mfma_f32_16x16x32_bf16 a[164:167], v[136:139], v[104:107], a[164:167]\n" \
"v_mfma_f32_16x16x32_bf16 a[164:167], v[140:143], v[108:111], a[164:167]\n" \
"v_mfma_f32_16x16x32_bf16 a[168:171], v[144:147], v[104:107], a[168:171]\n" \
"v_mfma_f32_16x16x32_bf16 a[168:171], v[148:151], v[108:111], a[168:171]\n" \
"v_mfma_f32_16x16x32_bf16 a[172:175], v[152:155], v[104:107], a[172:175]\n" \
"v_mfma_f32_16x16x32_bf16 a[172:175], v[156:159], v[108:111], a[172:175]\n" \
"s_waitcnt lgkmcnt(0)\n" \
"v_mfma_f32_16x16x32_bf16 a[192:195], v[128:131], v[112:115], a[192:195]\n" \
"v_mfma_f32_16x16x32_bf16 a[192:195], v[132:135], v[116:119], a[192:195]\n" \
"v_mfma_f32_16x16x32_bf16 a[196:199], v[136:139], v[112:115], a[196:199]\n" \
"v_mfma_f32_16x16x32_bf16 a[196:199], v[140:143], v[116:119], a[196:199]\n" \
"v_mfma_f32_16x16x32_bf16 a[200:203], v[144:147], v[112:115], a[200:203]\n" \
"v_mfma_f32_16x16x32_bf16 a[200:203], v[148:151], v[116:119], a[200:203]\n" \
"v_mfma_f32_16x16x32_bf16 a[204:207], v[152:155], v[112:115], a[204:207]\n" \
"v_mfma_f32_16x16x32_bf16 a[204:207], v[156:159], v[116:119], a[204:207]\n" \
"v_mfma_f32_16x16x32_bf16 a[224:227], v[128:131], v[120:123], a[224:227]\n" \
"v_mfma_f32_16x16x32_bf16 a[224:227], v[132:135], v[124:127], a[224:227]\n" \
"v_mfma_f32_16x16x32_bf16 a[228:231], v[136:139], v[120:123], a[228:231]\n" \
"v_mfma_f32_16x16x32_bf16 a[228:231], v[140:143], v[124:127], a[228:231]\n" \
"v_mfma_f32_16x16x32_bf16 a[232:235], v[144:147], v[120:123], a[232:235]\n" \
"v_mfma_f32_16x16x32_bf16 a[232:235], v[148:151], v[124:127], a[232:235]\n" \
"v_mfma_f32_16x16x32_bf16 a[236:239], v[152:155], v[120:123], a[236:239]\n" \
"s_waitcnt vmcnt(10) lgkmcnt(0)\n" \
"v_mfma_f32_16x16x32_bf16 a[236:239], v[156:159], v[124:127], a[236:239]\n" \
"Lpg587_%=:\n" \
"Lst0x0_%=:\n" \
"s_cbranch_execz Lpg2726_%=\n" \
"s_barrier\n" \
"v_mfma_f32_16x16x32_bf16 a[16:19], v[64:67], v[160:163], a[16:19]\n" \
"s_add_u32 m0, s87, 135168\n" \
"ds_read_b64_tr_b16 v[128:129], v50 offset:0\n" \
"v_readfirstlane_b32 s60, v244\n" \
"v_mfma_f32_16x16x32_bf16 a[16:19], v[68:71], v[164:167], a[16:19]\n" \
"s_add_u32 s60, s60, 0x9e3779b9\n" \
"global_load_lds_dwordx4 v32, s[54:55] offset:-4096\n" \
"v_mfma_f32_16x16x32_bf16 a[20:23], v[64:67], v[168:171], a[20:23]\n" \
"s_mul_i32 s88, s62, 557824\n" \
"s_mul_i32 s89, s63, 17408\n" \
"ds_read_b64_tr_b16 v[130:131], v50 offset:4096\n" \
"v_mfma_f32_16x16x32_bf16 a[20:23], v[68:71], v[172:175], a[20:23]\n" \
"s_add_u32 s88, s88, s89\n" \
"s_add_u32 s88, s88, s95\n" \
"s_add_u32 s89, s88, 17850368\n" \
"v_mfma_f32_16x16x32_bf16 a[24:27], v[64:67], v[176:179], a[24:27]\n" \
"s_lshl_b32 s91, s62, 7\n" \
"s_add_u32 s91, s91, 65585152\n" \
"ds_read_b64_tr_b16 v[132:133], v50 offset:16384\n" \
"v_mfma_f32_16x16x32_bf16 a[24:27], v[68:71], v[180:183], a[24:27]\n" \
"s_mov_b32 s92, 0x3e0293ee\n" \
"global_load_lds_dwordx4 v33, s[54:55] offset:-3072\n" \
"v_mfma_f32_16x16x32_bf16 a[28:31], v[64:67], v[184:187], a[28:31]\n" \
"ds_read_b64_tr_b16 v[134:135], v50 offset:20480\n" \
"v_mfma_f32_16x16x32_bf16 a[28:31], v[68:71], v[188:191], a[28:31]\n" \
"v_mfma_f32_16x16x32_bf16 a[48:51], v[72:75], v[160:163], a[48:51]\n" \
"ds_read_b128 v[64:67], v48 offset:0\n" \
"v_mfma_f32_16x16x32_bf16 a[48:51], v[76:79], v[164:167], a[48:51]\n" \
"global_load_lds_dwordx4 v34, s[54:55] offset:-2048\n" \
"v_mfma_f32_16x16x32_bf16 a[52:55], v[72:75], v[168:171], a[52:55]\n" \
"ds_read_b64_tr_b16 v[136:137], v51 offset:0\n" \
"v_mfma_f32_16x16x32_bf16 a[52:55], v[76:79], v[172:175], a[52:55]\n" \
"v_mfma_f32_16x16x32_bf16 a[56:59], v[72:75], v[176:179], a[56:59]\n" \
"ds_read_b128 v[68:71], v49 offset:0\n" \
"v_mfma_f32_16x16x32_bf16 a[56:59], v[76:79], v[180:183], a[56:59]\n" \
"global_load_lds_dwordx4 v35, s[54:55] offset:-1024\n" \
"v_mfma_f32_16x16x32_bf16 a[60:63], v[72:75], v[184:187], a[60:63]\n" \
"ds_read_b64_tr_b16 v[138:139], v51 offset:4096\n" \
"v_mfma_f32_16x16x32_bf16 a[60:63], v[76:79], v[188:191], a[60:63]\n" \
"v_mfma_f32_16x16x32_bf16 a[80:83], v[80:83], v[160:163], a[80:83]\n" \
"ds_read_b128 v[72:75], v48 offset:2048\n" \
"v_mfma_f32_16x16x32_bf16 a[80:83], v[84:87], v[164:167], a[80:83]\n" \
"global_load_lds_dwordx4 v36, s[54:55] offset:0\n" \
"v_mfma_f32_16x16x32_bf16 a[84:87], v[80:83], v[168:171], a[84:87]\n" \
"ds_read_b64_tr_b16 v[140:141], v51 offset:16384\n" \
"v_mfma_f32_16x16x32_bf16 a[84:87], v[84:87], v[172:175], a[84:87]\n" \
"v_mfma_f32_16x16x32_bf16 a[88:91], v[80:83], v[176:179], a[88:91]\n" \
"ds_read_b128 v[76:79], v49 offset:2048\n" \
"v_mfma_f32_16x16x32_bf16 a[88:91], v[84:87], v[180:183], a[88:91]\n" \
"global_load_lds_dwordx4 v37, s[54:55] offset:1024\n" \
"v_mfma_f32_16x16x32_bf16 a[92:95], v[80:83], v[184:187], a[92:95]\n" \
"ds_read_b64_tr_b16 v[142:143], v51 offset:20480\n" \
"v_mfma_f32_16x16x32_bf16 a[92:95], v[84:87], v[188:191], a[92:95]\n" \
"v_mfma_f32_16x16x32_bf16 a[112:115], v[88:91], v[160:163], a[112:115]\n" \
"ds_read_b128 v[80:83], v48 offset:4096\n" \
"v_mfma_f32_16x16x32_bf16 a[112:115], v[92:95], v[164:167], a[112:115]\n" \
"global_load_lds_dwordx4 v38, s[54:55] offset:2048\n" \
"v_mfma_f32_16x16x32_bf16 a[116:119], v[88:91], v[168:171], a[116:119]\n" \
"ds_read_b64_tr_b16 v[144:145], v50 offset:512\n" \
"v_mfma_f32_16x16x32_bf16 a[116:119], v[92:95], v[172:175], a[116:119]\n" \
"v_mfma_f32_16x16x32_bf16 a[120:123], v[88:91], v[176:179], a[120:123]\n" \
"ds_read_b128 v[84:87], v49 offset:4096\n" \
"v_mfma_f32_16x16x32_bf16 a[120:123], v[92:95], v[180:183], a[120:123]\n" \
"global_load_lds_dwordx4 v39, s[54:55] offset:3072\n" \
"v_mfma_f32_16x16x32_bf16 a[124:127], v[88:91], v[184:187], a[124:127]\n" \
"s_add_u32 s54, s54, 0x80\n" \
"s_addc_u32 s55, s55, 0\n" \
"ds_read_b64_tr_b16 v[146:147], v50 offset:4608\n" \
"v_mfma_f32_16x16x32_bf16 a[124:127], v[92:95], v[188:191], a[124:127]\n" \
"s_add_u32 s68, s67, 0x300000\n" \
"s_add_u32 s64, s14, s68\n" \
"s_addc_u32 s65, s15, 0\n" \
"v_mfma_f32_16x16x32_bf16 a[144:147], v[96:99], v[160:163], a[144:147]\n" \
"ds_read_b128 v[88:91], v48 offset:6144\n" \
"v_mfma_f32_16x16x32_bf16 a[144:147], v[100:103], v[164:167], a[144:147]\n" \
"global_load_dword v253, v248, s[64:65]\n" \
"v_mfma_f32_16x16x32_bf16 a[148:151], v[96:99], v[168:171], a[148:151]\n" \
"s_add_u32 s68, s67, 0x300000\n" \
"ds_read_b64_tr_b16 v[148:149], v50 offset:16896\n" \
"v_mfma_f32_16x16x32_bf16 a[148:151], v[100:103], v[172:175], a[148:151]\n" \
"s_add_u32 s64, s16, s68\n" \
"s_addc_u32 s65, s17, 0\n" \
"v_mfma_f32_16x16x32_bf16 a[152:155], v[96:99], v[176:179], a[152:155]\n" \
"ds_read_b128 v[92:95], v49 offset:6144\n" \
"v_mfma_f32_16x16x32_bf16 a[152:155], v[100:103], v[180:183], a[152:155]\n" \
"global_load_dword v253, v248, s[64:65]\n" \
"v_mfma_f32_16x16x32_bf16 a[156:159], v[96:99], v[184:187], a[156:159]\n" \
"ds_read_b64_tr_b16 v[150:151], v50 offset:20992\n" \
"v_mfma_f32_16x16x32_bf16 a[156:159], v[100:103], v[188:191], a[156:159]\n" \
"v_mfma_f32_16x16x32_bf16 a[176:179], v[104:107], v[160:163], a[176:179]\n" \
"ds_read_b128 v[96:99], v48 offset:8192\n" \
"v_mfma_f32_16x16x32_bf16 a[176:179], v[108:111], v[164:167], a[176:179]\n" \
"v_mfma_f32_16x16x32_bf16 a[180:183], v[104:107], v[168:171], a[180:183]\n" \
"ds_read_b64_tr_b16 v[152:153], v51 offset:512\n" \
"v_mfma_f32_16x16x32_bf16 a[180:183], v[108:111], v[172:175], a[180:183]\n" \
"v_mfma_f32_16x16x32_bf16 a[184:187], v[104:107], v[176:179], a[184:187]\n" \
"ds_read_b128 v[100:103], v49 offset:8192\n" \
"v_mfma_f32_16x16x32_bf16 a[184:187], v[108:111], v[180:183], a[184:187]\n" \
"v_mfma_f32_16x16x32_bf16 a[188:191], v[104:107], v[184:187], a[188:191]\n" \
"ds_read_b64_tr_b16 v[154:155], v51 offset:4608\n" \
"v_mfma_f32_16x16x32_bf16 a[188:191], v[108:111], v[188:191], a[188:191]\n" \
"v_mfma_f32_16x16x32_bf16 a[208:211], v[112:115], v[160:163], a[208:211]\n" \
"ds_read_b128 v[104:107], v48 offset:10240\n" \
"v_mfma_f32_16x16x32_bf16 a[208:211], v[116:119], v[164:167], a[208:211]\n" \
"v_mfma_f32_16x16x32_bf16 a[212:215], v[112:115], v[168:171], a[212:215]\n" \
"ds_read_b64_tr_b16 v[156:157], v51 offset:16896\n" \
"v_mfma_f32_16x16x32_bf16 a[212:215], v[116:119], v[172:175], a[212:215]\n" \
"v_mfma_f32_16x16x32_bf16 a[216:219], v[112:115], v[176:179], a[216:219]\n" \
"ds_read_b128 v[108:111], v49 offset:10240\n" \
"v_mfma_f32_16x16x32_bf16 a[216:219], v[116:119], v[180:183], a[216:219]\n" \
"v_mfma_f32_16x16x32_bf16 a[220:223], v[112:115], v[184:187], a[220:223]\n" \
"ds_read_b64_tr_b16 v[158:159], v51 offset:20992\n" \
"v_mfma_f32_16x16x32_bf16 a[220:223], v[116:119], v[188:191], a[220:223]\n" \
"v_mfma_f32_16x16x32_bf16 a[240:243], v[120:123], v[160:163], a[240:243]\n" \
"ds_read_b128 v[112:115], v48 offset:12288\n" \
"v_mfma_f32_16x16x32_bf16 a[240:243], v[124:127], v[164:167], a[240:243]\n" \
"v_mfma_f32_16x16x32_bf16 a[244:247], v[120:123], v[168:171], a[244:247]\n" \
"v_mfma_f32_16x16x32_bf16 a[244:247], v[124:127], v[172:175], a[244:247]\n" \
"v_mfma_f32_16x16x32_bf16 a[248:251], v[120:123], v[176:179], a[248:251]\n" \
"ds_read_b128 v[116:119], v49 offset:12288\n" \
"v_mfma_f32_16x16x32_bf16 a[248:251], v[124:127], v[180:183], a[248:251]\n" \
"v_mfma_f32_16x16x32_bf16 a[252:255], v[120:123], v[184:187], a[252:255]\n" \
"v_mfma_f32_16x16x32_bf16 a[252:255], v[124:127], v[188:191], a[252:255]\n" \
"s_waitcnt lgkmcnt(1)\n" \
"v_mfma_f32_16x16x32_bf16 a[0:3], v[128:131], v[64:67], a[0:3]\n" \
"ds_read_b128 v[120:123], v48 offset:14336\n" \
"v_mfma_f32_16x16x32_bf16 a[0:3], v[132:135], v[68:71], a[0:3]\n" \
"v_mfma_f32_16x16x32_bf16 a[4:7], v[136:139], v[64:67], a[4:7]\n" \
"ds_read_b128 v[124:127], v49 offset:14336\n" \
"s_add_u32 m0, s87, 69632\n" \
"v_mfma_f32_16x16x32_bf16 a[4:7], v[140:143], v[68:71], a[4:7]\n" \
"global_load_lds_dwordx4 v40, s[52:53] offset:-4096\n" \
"v_mfma_f32_16x16x32_bf16 a[8:11], v[144:147], v[64:67], a[8:11]\n" \
"ds_read_b64_tr_b16 v[160:161], v50 offset:1024\n" \
"v_mfma_f32_16x16x32_bf16 a[8:11], v[148:151], v[68:71], a[8:11]\n" \
"v_mfma_f32_16x16x32_bf16 a[12:15], v[152:155], v[64:67], a[12:15]\n" \
"ds_read_b64_tr_b16 v[162:163], v50 offset:5120\n" \
"v_mfma_f32_16x16x32_bf16 a[12:15], v[156:159], v[68:71], a[12:15]\n" \
"global_load_lds_dwordx4 v41, s[50:51] offset:-3072\n" \
"v_mfma_f32_16x16x32_bf16 a[32:35], v[128:131], v[72:75], a[32:35]\n" \
"ds_read_b64_tr_b16 v[164:165], v50 offset:17408\n" \
"v_mfma_f32_16x16x32_bf16 a[32:35], v[132:135], v[76:79], a[32:35]\n" \
"v_mfma_f32_16x16x32_bf16 a[36:39], v[136:139], v[72:75], a[36:39]\n" \
"ds_read_b64_tr_b16 v[166:167], v50 offset:21504\n" \
"v_mfma_f32_16x16x32_bf16 a[36:39], v[140:143], v[76:79], a[36:39]\n" \
"global_load_lds_dwordx4 v42, s[52:53] offset:-2048\n" \
"v_mfma_f32_16x16x32_bf16 a[40:43], v[144:147], v[72:75], a[40:43]\n" \
"ds_read_b64_tr_b16 v[168:169], v51 offset:1024\n" \
"v_mfma_f32_16x16x32_bf16 a[40:43], v[148:151], v[76:79], a[40:43]\n" \
"v_mfma_f32_16x16x32_bf16 a[44:47], v[152:155], v[72:75], a[44:47]\n" \
"ds_read_b64_tr_b16 v[170:171], v51 offset:5120\n" \
"v_mfma_f32_16x16x32_bf16 a[44:47], v[156:159], v[76:79], a[44:47]\n" \
"global_load_lds_dwordx4 v43, s[50:51] offset:-1024\n" \
"v_mfma_f32_16x16x32_bf16 a[64:67], v[128:131], v[80:83], a[64:67]\n" \
"ds_read_b64_tr_b16 v[172:173], v51 offset:17408\n" \
"v_mfma_f32_16x16x32_bf16 a[64:67], v[132:135], v[84:87], a[64:67]\n" \
"v_mfma_f32_16x16x32_bf16 a[68:71], v[136:139], v[80:83], a[68:71]\n" \
"ds_read_b64_tr_b16 v[174:175], v51 offset:21504\n" \
"v_mfma_f32_16x16x32_bf16 a[68:71], v[140:143], v[84:87], a[68:71]\n" \
"global_load_lds_dwordx4 v44, s[52:53] offset:0\n" \
"v_mfma_f32_16x16x32_bf16 a[72:75], v[144:147], v[80:83], a[72:75]\n" \
"ds_read_b64_tr_b16 v[176:177], v50 offset:1536\n" \
"v_mfma_f32_16x16x32_bf16 a[72:75], v[148:151], v[84:87], a[72:75]\n" \
"v_mfma_f32_16x16x32_bf16 a[76:79], v[152:155], v[80:83], a[76:79]\n" \
"ds_read_b64_tr_b16 v[178:179], v50 offset:5632\n" \
"v_mfma_f32_16x16x32_bf16 a[76:79], v[156:159], v[84:87], a[76:79]\n" \
"global_load_lds_dwordx4 v45, s[50:51] offset:1024\n" \
"v_mfma_f32_16x16x32_bf16 a[96:99], v[128:131], v[88:91], a[96:99]\n" \
"ds_read_b64_tr_b16 v[180:181], v50 offset:17920\n" \
"v_mfma_f32_16x16x32_bf16 a[96:99], v[132:135], v[92:95], a[96:99]\n" \
"v_mfma_f32_16x16x32_bf16 a[100:103], v[136:139], v[88:91], a[100:103]\n" \
"ds_read_b64_tr_b16 v[182:183], v50 offset:22016\n" \
"v_mfma_f32_16x16x32_bf16 a[100:103], v[140:143], v[92:95], a[100:103]\n" \
"global_load_lds_dwordx4 v46, s[52:53] offset:2048\n" \
"v_mfma_f32_16x16x32_bf16 a[104:107], v[144:147], v[88:91], a[104:107]\n" \
"ds_read_b64_tr_b16 v[184:185], v51 offset:1536\n" \
"v_mfma_f32_16x16x32_bf16 a[104:107], v[148:151], v[92:95], a[104:107]\n" \
"v_mfma_f32_16x16x32_bf16 a[108:111], v[152:155], v[88:91], a[108:111]\n" \
"ds_read_b64_tr_b16 v[186:187], v51 offset:5632\n" \
"v_mfma_f32_16x16x32_bf16 a[108:111], v[156:159], v[92:95], a[108:111]\n" \
"global_load_lds_dwordx4 v47, s[50:51] offset:3072\n" \
"v_mfma_f32_16x16x32_bf16 a[128:131], v[128:131], v[96:99], a[128:131]\n" \
"s_add_u32 s52, s52, 0x80000\n" \
"s_addc_u32 s53, s53, 0\n" \
"ds_read_b64_tr_b16 v[188:189], v51 offset:17920\n" \
"v_mfma_f32_16x16x32_bf16 a[128:131], v[132:135], v[100:103], a[128:131]\n" \
"s_add_u32 s50, s50, 0x80000\n" \
"s_addc_u32 s51, s51, 0\n" \
"v_mfma_f32_16x16x32_bf16 a[132:135], v[136:139], v[96:99], a[132:135]\n" \
"ds_read_b64_tr_b16 v[190:191], v51 offset:22016\n" \
"v_mfma_f32_16x16x32_bf16 a[132:135], v[140:143], v[100:103], a[132:135]\n" \
"v_mfma_f32_16x16x32_bf16 a[136:139], v[144:147], v[96:99], a[136:139]\n" \
"v_mfma_f32_16x16x32_bf16 a[136:139], v[148:151], v[100:103], a[136:139]\n" \
"v_mfma_f32_16x16x32_bf16 a[140:143], v[152:155], v[96:99], a[140:143]\n" \
"v_mfma_f32_16x16x32_bf16 a[140:143], v[156:159], v[100:103], a[140:143]\n" \
"v_mfma_f32_16x16x32_bf16 a[160:163], v[128:131], v[104:107], a[160:163]\n" \
"v_add_u32 v48, 0x18000, v48\n" \
"v_add_u32 v49, 0x18000, v49\n" \
"v_mfma_f32_16x16x32_bf16 a[160:163], v[132:135], v[108:111], a[160:163]\n" \
"v_add_u32 v50, 0xfffe8000, v50\n" \
"v_add_u32 v51, 0xfffe8000, v51\n" \
"v_mfma_f32_16x16x32_bf16 a[164:167], v[136:139], v[104:107], a[164:167]\n" \
"v_mfma_f32_16x16x32_bf16 a[164:167], v[140:143], v[108:111], a[164:167]\n" \
"v_mfma_f32_16x16x32_bf16 a[168:171], v[144:147], v[104:107], a[168:171]\n" \
"v_mfma_f32_16x16x32_bf16 a[168:171], v[148:151], v[108:111], a[168:171]\n" \
"v_mfma_f32_16x16x32_bf16 a[172:175], v[152:155], v[104:107], a[172:175]\n" \
"v_mfma_f32_16x16x32_bf16 a[172:175], v[156:159], v[108:111], a[172:175]\n" \
"s_waitcnt lgkmcnt(0)\n" \
"v_mfma_f32_16x16x32_bf16 a[192:195], v[128:131], v[112:115], a[192:195]\n" \
"v_mfma_f32_16x16x32_bf16 a[192:195], v[132:135], v[116:119], a[192:195]\n" \
"v_mfma_f32_16x16x32_bf16 a[196:199], v[136:139], v[112:115], a[196:199]\n" \
"v_mfma_f32_16x16x32_bf16 a[196:199], v[140:143], v[116:119], a[196:199]\n" \
"v_mfma_f32_16x16x32_bf16 a[200:203], v[144:147], v[112:115], a[200:203]\n" \
"v_mfma_f32_16x16x32_bf16 a[200:203], v[148:151], v[116:119], a[200:203]\n" \
"v_mfma_f32_16x16x32_bf16 a[204:207], v[152:155], v[112:115], a[204:207]\n" \
"v_mfma_f32_16x16x32_bf16 a[204:207], v[156:159], v[116:119], a[204:207]\n" \
"v_mfma_f32_16x16x32_bf16 a[224:227], v[128:131], v[120:123], a[224:227]\n" \
"v_mfma_f32_16x16x32_bf16 a[224:227], v[132:135], v[124:127], a[224:227]\n" \
"v_mfma_f32_16x16x32_bf16 a[228:231], v[136:139], v[120:123], a[228:231]\n" \
"v_mfma_f32_16x16x32_bf16 a[228:231], v[140:143], v[124:127], a[228:231]\n" \
"v_mfma_f32_16x16x32_bf16 a[232:235], v[144:147], v[120:123], a[232:235]\n" \
"v_mfma_f32_16x16x32_bf16 a[232:235], v[148:151], v[124:127], a[232:235]\n" \
"v_mfma_f32_16x16x32_bf16 a[236:239], v[152:155], v[120:123], a[236:239]\n" \
"s_waitcnt vmcnt(10) lgkmcnt(0)\n" \
"v_mfma_f32_16x16x32_bf16 a[236:239], v[156:159], v[124:127], a[236:239]\n" \
"s_barrier\n" \
"v_mfma_f32_16x16x32_bf16 a[16:19], v[64:67], v[160:163], a[16:19]\n" \
"s_add_u32 m0, s87, 102400\n" \
"ds_read_b64_tr_b16 v[128:129], v50 offset:0\n" \
"v_mfma_f32_16x16x32_bf16 a[16:19], v[68:71], v[164:167], a[16:19]\n" \
"global_load_lds_dwordx4 v32, s[54:55] offset:-4096\n" \
"v_mfma_f32_16x16x32_bf16 a[20:23], v[64:67], v[168:171], a[20:23]\n" \
"ds_read_b64_tr_b16 v[130:131], v50 offset:4096\n" \
"v_mfma_f32_16x16x32_bf16 a[20:23], v[68:71], v[172:175], a[20:23]\n" \
"v_mfma_f32_16x16x32_bf16 a[24:27], v[64:67], v[176:179], a[24:27]\n" \
"ds_read_b64_tr_b16 v[132:133], v50 offset:16384\n" \
"v_mfma_f32_16x16x32_bf16 a[24:27], v[68:71], v[180:183], a[24:27]\n" \
"global_load_lds_dwordx4 v33, s[54:55] offset:-3072\n" \
"v_mfma_f32_16x16x32_bf16 a[28:31], v[64:67], v[184:187], a[28:31]\n" \
"ds_read_b64_tr_b16 v[134:135], v50 offset:20480\n" \
"v_mfma_f32_16x16x32_bf16 a[28:31], v[68:71], v[188:191], a[28:31]\n" \
"v_mfma_f32_16x16x32_bf16 a[48:51], v[72:75], v[160:163], a[48:51]\n" \
"ds_read_b128 v[64:67], v48 offset:0\n" \
"v_mfma_f32_16x16x32_bf16 a[48:51], v[76:79], v[164:167], a[48:51]\n" \
"global_load_lds_dwordx4 v34, s[54:55] offset:-2048\n" \
"v_mfma_f32_16x16x32_bf16 a[52:55], v[72:75], v[168:171], a[52:55]\n" \
"ds_read_b64_tr_b16 v[136:137], v51 offset:0\n" \
"v_mfma_f32_16x16x32_bf16 a[52:55], v[76:79], v[172:175], a[52:55]\n" \
"v_mfma_f32_16x16x32_bf16 a[56:59], v[72:75], v[176:179], a[56:59]\n" \
"ds_read_b128 v[68:71], v49 offset:0\n" \
"v_mfma_f32_16x16x32_bf16 a[56:59], v[76:79], v[180:183], a[56:59]\n" \
"global_load_lds_dwordx4 v35, s[54:55] offset:-1024\n" \
"v_mfma_f32_16x16x32_bf16 a[60:63], v[72:75], v[184:187], a[60:63]\n" \
"ds_read_b64_tr_b16 v[138:139], v51 offset:4096\n" \
"v_mfma_f32_16x16x32_bf16 a[60:63], v[76:79], v[188:191], a[60:63]\n" \
"v_mfma_f32_16x16x32_bf16 a[80:83], v[80:83], v[160:163], a[80:83]\n" \
"ds_read_b128 v[72:75], v48 offset:2048\n" \
"v_mfma_f32_16x16x32_bf16 a[80:83], v[84:87], v[164:167], a[80:83]\n" \
"global_load_lds_dwordx4 v36, s[54:55] offset:0\n" \
"v_mfma_f32_16x16x32_bf16 a[84:87], v[80:83], v[168:171], a[84:87]\n" \
"ds_read_b64_tr_b16 v[140:141], v51 offset:16384\n" \
"v_mfma_f32_16x16x32_bf16 a[84:87], v[84:87], v[172:175], a[84:87]\n" \
"v_mfma_f32_16x16x32_bf16 a[88:91], v[80:83], v[176:179], a[88:91]\n" \
"ds_read_b128 v[76:79], v49 offset:2048\n" \
"v_mfma_f32_16x16x32_bf16 a[88:91], v[84:87], v[180:183], a[88:91]\n" \
"global_load_lds_dwordx4 v37, s[54:55] offset:1024\n" \
"v_mfma_f32_16x16x32_bf16 a[92:95], v[80:83], v[184:187], a[92:95]\n" \
"ds_read_b64_tr_b16 v[142:143], v51 offset:20480\n" \
"v_mfma_f32_16x16x32_bf16 a[92:95], v[84:87], v[188:191], a[92:95]\n" \
"v_mfma_f32_16x16x32_bf16 a[112:115], v[88:91], v[160:163], a[112:115]\n" \
"ds_read_b128 v[80:83], v48 offset:4096\n" \
"v_mfma_f32_16x16x32_bf16 a[112:115], v[92:95], v[164:167], a[112:115]\n" \
"global_load_lds_dwordx4 v38, s[54:55] offset:2048\n" \
"v_mfma_f32_16x16x32_bf16 a[116:119], v[88:91], v[168:171], a[116:119]\n" \
"ds_read_b64_tr_b16 v[144:145], v50 offset:512\n" \
"v_mfma_f32_16x16x32_bf16 a[116:119], v[92:95], v[172:175], a[116:119]\n" \
"v_mfma_f32_16x16x32_bf16 a[120:123], v[88:91], v[176:179], a[120:123]\n" \
"ds_read_b128 v[84:87], v49 offset:4096\n" \
"v_mfma_f32_16x16x32_bf16 a[120:123], v[92:95], v[180:183], a[120:123]\n" \
"global_load_lds_dwordx4 v39, s[54:55] offset:3072\n" \
"v_mfma_f32_16x16x32_bf16 a[124:127], v[88:91], v[184:187], a[124:127]\n" \
"s_add_u32 s54, s54, 0x80\n" \
"s_addc_u32 s55, s55, 0\n" \
"ds_read_b64_tr_b16 v[146:147], v50 offset:4608\n" \
"v_mfma_f32_16x16x32_bf16 a[124:127], v[92:95], v[188:191], a[124:127]\n" \
"s_add_u32 s68, s66, 0x0\n" \
"s_add_u32 s64, s8, s68\n" \
"s_addc_u32 s65, s9, 0\n" \
"v_mfma_f32_16x16x32_bf16 a[144:147], v[96:99], v[160:163], a[144:147]\n" \
"ds_read_b128 v[88:91], v48 offset:6144\n" \
"v_mfma_f32_16x16x32_bf16 a[144:147], v[100:103], v[164:167], a[144:147]\n" \
"global_load_dword v253, v248, s[64:65]\n" \
"v_mfma_f32_16x16x32_bf16 a[148:151], v[96:99], v[168:171], a[148:151]\n" \
"s_add_u32 s68, s66, 0x100000\n" \
"ds_read_b64_tr_b16 v[148:149], v50 offset:16896\n" \
"v_mfma_f32_16x16x32_bf16 a[148:151], v[100:103], v[172:175], a[148:151]\n" \
"s_add_u32 s64, s8, s68\n" \
"s_addc_u32 s65, s9, 0\n" \
"v_mfma_f32_16x16x32_bf16 a[152:155], v[96:99], v[176:179], a[152:155]\n" \
"ds_read_b128 v[92:95], v49 offset:6144\n" \
"v_mfma_f32_16x16x32_bf16 a[152:155], v[100:103], v[180:183], a[152:155]\n" \
"global_load_dword v253, v248, s[64:65]\n" \
"v_mfma_f32_16x16x32_bf16 a[156:159], v[96:99], v[184:187], a[156:159]\n" \
"ds_read_b64_tr_b16 v[150:151], v50 offset:20992\n" \
"v_mfma_f32_16x16x32_bf16 a[156:159], v[100:103], v[188:191], a[156:159]\n" \
"v_mfma_f32_16x16x32_bf16 a[176:179], v[104:107], v[160:163], a[176:179]\n" \
"ds_read_b128 v[96:99], v48 offset:8192\n" \
"v_mfma_f32_16x16x32_bf16 a[176:179], v[108:111], v[164:167], a[176:179]\n" \
"v_mfma_f32_16x16x32_bf16 a[180:183], v[104:107], v[168:171], a[180:183]\n" \
"ds_read_b64_tr_b16 v[152:153], v51 offset:512\n" \
"v_mfma_f32_16x16x32_bf16 a[180:183], v[108:111], v[172:175], a[180:183]\n" \
"v_mfma_f32_16x16x32_bf16 a[184:187], v[104:107], v[176:179], a[184:187]\n" \
"ds_read_b128 v[100:103], v49 offset:8192\n" \
"v_mfma_f32_16x16x32_bf16 a[184:187], v[108:111], v[180:183], a[184:187]\n" \
"v_mfma_f32_16x16x32_bf16 a[188:191], v[104:107], v[184:187], a[188:191]\n" \
"ds_read_b64_tr_b16 v[154:155], v51 offset:4608\n" \
"v_mfma_f32_16x16x32_bf16 a[188:191], v[108:111], v[188:191], a[188:191]\n" \
"v_mfma_f32_16x16x32_bf16 a[208:211], v[112:115], v[160:163], a[208:211]\n" \
"ds_read_b128 v[104:107], v48 offset:10240\n" \
"v_mfma_f32_16x16x32_bf16 a[208:211], v[116:119], v[164:167], a[208:211]\n" \
"v_mfma_f32_16x16x32_bf16 a[212:215], v[112:115], v[168:171], a[212:215]\n" \
"ds_read_b64_tr_b16 v[156:157], v51 offset:16896\n" \
"v_mfma_f32_16x16x32_bf16 a[212:215], v[116:119], v[172:175], a[212:215]\n" \
"v_mfma_f32_16x16x32_bf16 a[216:219], v[112:115], v[176:179], a[216:219]\n" \
"ds_read_b128 v[108:111], v49 offset:10240\n" \
"v_mfma_f32_16x16x32_bf16 a[216:219], v[116:119], v[180:183], a[216:219]\n" \
"v_mfma_f32_16x16x32_bf16 a[220:223], v[112:115], v[184:187], a[220:223]\n" \
"ds_read_b64_tr_b16 v[158:159], v51 offset:20992\n" \
"v_mfma_f32_16x16x32_bf16 a[220:223], v[116:119], v[188:191], a[220:223]\n" \
"v_mfma_f32_16x16x32_bf16 a[240:243], v[120:123], v[160:163], a[240:243]\n" \
"ds_read_b128 v[112:115], v48 offset:12288\n" \
"v_mfma_f32_16x16x32_bf16 a[240:243], v[124:127], v[164:167], a[240:243]\n" \
"v_mfma_f32_16x16x32_bf16 a[244:247], v[120:123], v[168:171], a[244:247]\n" \
"v_mfma_f32_16x16x32_bf16 a[244:247], v[124:127], v[172:175], a[244:247]\n" \
"v_mfma_f32_16x16x32_bf16 a[248:251], v[120:123], v[176:179], a[248:251]\n" \
"ds_read_b128 v[116:119], v49 offset:12288\n" \
"v_mfma_f32_16x16x32_bf16 a[248:251], v[124:127], v[180:183], a[248:251]\n" \
"v_mfma_f32_16x16x32_bf16 a[252:255], v[120:123], v[184:187], a[252:255]\n" \
"v_mfma_f32_16x16x32_bf16 a[252:255], v[124:127], v[188:191], a[252:255]\n" \
"s_waitcnt lgkmcnt(1)\n" \
"v_mfma_f32_16x16x32_bf16 a[0:3], v[128:131], v[64:67], a[0:3]\n" \
"ds_read_b128 v[120:123], v48 offset:14336\n" \
"v_mfma_f32_16x16x32_bf16 a[0:3], v[132:135], v[68:71], a[0:3]\n" \
"v_mfma_f32_16x16x32_bf16 a[4:7], v[136:139], v[64:67], a[4:7]\n" \
"ds_read_b128 v[124:127], v49 offset:14336\n" \
"s_add_u32 m0, s87, 36864\n" \
"v_mfma_f32_16x16x32_bf16 a[4:7], v[140:143], v[68:71], a[4:7]\n" \
"global_load_lds_dwordx4 v40, s[52:53] offset:-4096\n" \
"v_mfma_f32_16x16x32_bf16 a[8:11], v[144:147], v[64:67], a[8:11]\n" \
"ds_read_b64_tr_b16 v[160:161], v50 offset:1024\n" \
"v_mfma_f32_16x16x32_bf16 a[8:11], v[148:151], v[68:71], a[8:11]\n" \
"v_mfma_f32_16x16x32_bf16 a[12:15], v[152:155], v[64:67], a[12:15]\n" \
"ds_read_b64_tr_b16 v[162:163], v50 offset:5120\n" \
"v_mfma_f32_16x16x32_bf16 a[12:15], v[156:159], v[68:71], a[12:15]\n" \
"global_load_lds_dwordx4 v41, s[50:51] offset:-3072\n" \
"v_mfma_f32_16x16x32_bf16 a[32:35], v[128:131], v[72:75], a[32:35]\n" \
"ds_read_b64_tr_b16 v[164:165], v50 offset:17408\n" \
"v_mfma_f32_16x16x32_bf16 a[32:35], v[132:135], v[76:79], a[32:35]\n" \
"v_mfma_f32_16x16x32_bf16 a[36:39], v[136:139], v[72:75], a[36:39]\n" \
"ds_read_b64_tr_b16 v[166:167], v50 offset:21504\n" \
"v_mfma_f32_16x16x32_bf16 a[36:39], v[140:143], v[76:79], a[36:39]\n" \
"global_load_lds_dwordx4 v42, s[52:53] offset:-2048\n" \
"v_mfma_f32_16x16x32_bf16 a[40:43], v[144:147], v[72:75], a[40:43]\n" \
"ds_read_b64_tr_b16 v[168:169], v51 offset:1024\n" \
"v_mfma_f32_16x16x32_bf16 a[40:43], v[148:151], v[76:79], a[40:43]\n" \
"v_mfma_f32_16x16x32_bf16 a[44:47], v[152:155], v[72:75], a[44:47]\n" \
"ds_read_b64_tr_b16 v[170:171], v51 offset:5120\n" \
"v_mfma_f32_16x16x32_bf16 a[44:47], v[156:159], v[76:79], a[44:47]\n" \
"global_load_lds_dwordx4 v43, s[50:51] offset:-1024\n" \
"v_mfma_f32_16x16x32_bf16 a[64:67], v[128:131], v[80:83], a[64:67]\n" \
"ds_read_b64_tr_b16 v[172:173], v51 offset:17408\n" \
"v_mfma_f32_16x16x32_bf16 a[64:67], v[132:135], v[84:87], a[64:67]\n" \
"v_mfma_f32_16x16x32_bf16 a[68:71], v[136:139], v[80:83], a[68:71]\n" \
"ds_read_b64_tr_b16 v[174:175], v51 offset:21504\n" \
"v_mfma_f32_16x16x32_bf16 a[68:71], v[140:143], v[84:87], a[68:71]\n" \
"global_load_lds_dwordx4 v44, s[52:53] offset:0\n" \
"v_mfma_f32_16x16x32_bf16 a[72:75], v[144:147], v[80:83], a[72:75]\n" \
"ds_read_b64_tr_b16 v[176:177], v50 offset:1536\n" \
"v_mfma_f32_16x16x32_bf16 a[72:75], v[148:151], v[84:87], a[72:75]\n" \
"v_mfma_f32_16x16x32_bf16 a[76:79], v[152:155], v[80:83], a[76:79]\n" \
"ds_read_b64_tr_b16 v[178:179], v50 offset:5632\n" \
"v_mfma_f32_16x16x32_bf16 a[76:79], v[156:159], v[84:87], a[76:79]\n" \
"global_load_lds_dwordx4 v45, s[50:51] offset:1024\n" \
"v_mfma_f32_16x16x32_bf16 a[96:99], v[128:131], v[88:91], a[96:99]\n" \
"ds_read_b64_tr_b16 v[180:181], v50 offset:17920\n" \
"v_mfma_f32_16x16x32_bf16 a[96:99], v[132:135], v[92:95], a[96:99]\n" \
"v_mfma_f32_16x16x32_bf16 a[100:103], v[136:139], v[88:91], a[100:103]\n" \
"ds_read_b64_tr_b16 v[182:183], v50 offset:22016\n" \
"v_mfma_f32_16x16x32_bf16 a[100:103], v[140:143], v[92:95], a[100:103]\n" \
"global_load_lds_dwordx4 v46, s[52:53] offset:2048\n" \
"v_mfma_f32_16x16x32_bf16 a[104:107], v[144:147], v[88:91], a[104:107]\n" \
"ds_read_b64_tr_b16 v[184:185], v51 offset:1536\n" \
"v_mfma_f32_16x16x32_bf16 a[104:107], v[148:151], v[92:95], a[104:107]\n" \
"v_mfma_f32_16x16x32_bf16 a[108:111], v[152:155], v[88:91], a[108:111]\n" \
"ds_read_b64_tr_b16 v[186:187], v51 offset:5632\n" \
"v_mfma_f32_16x16x32_bf16 a[108:111], v[156:159], v[92:95], a[108:111]\n" \
"global_load_lds_dwordx4 v47, s[50:51] offset:3072\n" \
"v_mfma_f32_16x16x32_bf16 a[128:131], v[128:131], v[96:99], a[128:131]\n" \
"s_add_u32 s52, s52, 0x80000\n" \
"s_addc_u32 s53, s53, 0\n" \
"ds_read_b64_tr_b16 v[188:189], v51 offset:17920\n" \
"v_mfma_f32_16x16x32_bf16 a[128:131], v[132:135], v[100:103], a[128:131]\n" \
"s_add_u32 s50, s50, 0x80000\n" \
"s_addc_u32 s51, s51, 0\n" \
"v_mfma_f32_16x16x32_bf16 a[132:135], v[136:139], v[96:99], a[132:135]\n" \
"ds_read_b64_tr_b16 v[190:191], v51 offset:22016\n" \
"v_mfma_f32_16x16x32_bf16 a[132:135], v[140:143], v[100:103], a[132:135]\n" \
"v_mfma_f32_16x16x32_bf16 a[136:139], v[144:147], v[96:99], a[136:139]\n" \
"v_mfma_f32_16x16x32_bf16 a[136:139], v[148:151], v[100:103], a[136:139]\n" \
"v_mfma_f32_16x16x32_bf16 a[140:143], v[152:155], v[96:99], a[140:143]\n" \
"v_mfma_f32_16x16x32_bf16 a[140:143], v[156:159], v[100:103], a[140:143]\n" \
"v_mfma_f32_16x16x32_bf16 a[160:163], v[128:131], v[104:107], a[160:163]\n" \
"v_add_u32 v48, 0xffff8000, v48\n" \
"v_add_u32 v49, 0xffff8000, v49\n" \
"v_mfma_f32_16x16x32_bf16 a[160:163], v[132:135], v[108:111], a[160:163]\n" \
"v_add_u32 v50, 0x10000, v50\n" \
"v_add_u32 v51, 0x10000, v51\n" \
"v_mfma_f32_16x16x32_bf16 a[164:167], v[136:139], v[104:107], a[164:167]\n" \
"v_mfma_f32_16x16x32_bf16 a[164:167], v[140:143], v[108:111], a[164:167]\n" \
"v_mfma_f32_16x16x32_bf16 a[168:171], v[144:147], v[104:107], a[168:171]\n" \
"v_mfma_f32_16x16x32_bf16 a[168:171], v[148:151], v[108:111], a[168:171]\n" \
"v_mfma_f32_16x16x32_bf16 a[172:175], v[152:155], v[104:107], a[172:175]\n" \
"v_mfma_f32_16x16x32_bf16 a[172:175], v[156:159], v[108:111], a[172:175]\n" \
"s_waitcnt lgkmcnt(0)\n" \
"v_mfma_f32_16x16x32_bf16 a[192:195], v[128:131], v[112:115], a[192:195]\n" \
"v_mfma_f32_16x16x32_bf16 a[192:195], v[132:135], v[116:119], a[192:195]\n" \
"v_mfma_f32_16x16x32_bf16 a[196:199], v[136:139], v[112:115], a[196:199]\n" \
"v_mfma_f32_16x16x32_bf16 a[196:199], v[140:143], v[116:119], a[196:199]\n" \
"v_mfma_f32_16x16x32_bf16 a[200:203], v[144:147], v[112:115], a[200:203]\n" \
"v_mfma_f32_16x16x32_bf16 a[200:203], v[148:151], v[116:119], a[200:203]\n" \
"v_mfma_f32_16x16x32_bf16 a[204:207], v[152:155], v[112:115], a[204:207]\n" \
"v_mfma_f32_16x16x32_bf16 a[204:207], v[156:159], v[116:119], a[204:207]\n" \
"v_mfma_f32_16x16x32_bf16 a[224:227], v[128:131], v[120:123], a[224:227]\n" \
"v_mfma_f32_16x16x32_bf16 a[224:227], v[132:135], v[124:127], a[224:227]\n" \
"v_mfma_f32_16x16x32_bf16 a[228:231], v[136:139], v[120:123], a[228:231]\n" \
"v_mfma_f32_16x16x32_bf16 a[228:231], v[140:143], v[124:127], a[228:231]\n" \
"v_mfma_f32_16x16x32_bf16 a[232:235], v[144:147], v[120:123], a[232:235]\n" \
"v_mfma_f32_16x16x32_bf16 a[232:235], v[148:151], v[124:127], a[232:235]\n" \
"v_mfma_f32_16x16x32_bf16 a[236:239], v[152:155], v[120:123], a[236:239]\n" \
"s_waitcnt vmcnt(10) lgkmcnt(0)\n" \
"v_mfma_f32_16x16x32_bf16 a[236:239], v[156:159], v[124:127], a[236:239]\n" \
"s_barrier\n" \
"v_mfma_f32_16x16x32_bf16 a[16:19], v[64:67], v[160:163], a[16:19]\n" \
"s_add_u32 m0, s87, 4096\n" \
"ds_read_b64_tr_b16 v[128:129], v50 offset:0\n" \
"v_mfma_f32_16x16x32_bf16 a[16:19], v[68:71], v[164:167], a[16:19]\n" \
"global_load_lds_dwordx4 v32, s[54:55] offset:-4096\n" \
"v_mfma_f32_16x16x32_bf16 a[20:23], v[64:67], v[168:171], a[20:23]\n" \
"ds_read_b64_tr_b16 v[130:131], v50 offset:4096\n" \
"v_mfma_f32_16x16x32_bf16 a[20:23], v[68:71], v[172:175], a[20:23]\n" \
"v_mfma_f32_16x16x32_bf16 a[24:27], v[64:67], v[176:179], a[24:27]\n" \
"ds_read_b64_tr_b16 v[132:133], v50 offset:16384\n" \
"v_mfma_f32_16x16x32_bf16 a[24:27], v[68:71], v[180:183], a[24:27]\n" \
"global_load_lds_dwordx4 v33, s[54:55] offset:-3072\n" \
"v_mfma_f32_16x16x32_bf16 a[28:31], v[64:67], v[184:187], a[28:31]\n" \
"ds_read_b64_tr_b16 v[134:135], v50 offset:20480\n" \
"v_mfma_f32_16x16x32_bf16 a[28:31], v[68:71], v[188:191], a[28:31]\n" \
"v_mfma_f32_16x16x32_bf16 a[48:51], v[72:75], v[160:163], a[48:51]\n" \
"ds_read_b128 v[64:67], v48 offset:0\n" \
"v_mfma_f32_16x16x32_bf16 a[48:51], v[76:79], v[164:167], a[48:51]\n" \
"global_load_lds_dwordx4 v34, s[54:55] offset:-2048\n" \
"v_mfma_f32_16x16x32_bf16 a[52:55], v[72:75], v[168:171], a[52:55]\n" \
"ds_read_b64_tr_b16 v[136:137], v51 offset:0\n" \
"v_mfma_f32_16x16x32_bf16 a[52:55], v[76:79], v[172:175], a[52:55]\n" \
"v_mfma_f32_16x16x32_bf16 a[56:59], v[72:75], v[176:179], a[56:59]\n" \
"ds_read_b128 v[68:71], v49 offset:0\n" \
"v_mfma_f32_16x16x32_bf16 a[56:59], v[76:79], v[180:183], a[56:59]\n" \
"global_load_lds_dwordx4 v35, s[54:55] offset:-1024\n" \
"v_mfma_f32_16x16x32_bf16 a[60:63], v[72:75], v[184:187], a[60:63]\n" \
"ds_read_b64_tr_b16 v[138:139], v51 offset:4096\n" \
"v_mfma_f32_16x16x32_bf16 a[60:63], v[76:79], v[188:191], a[60:63]\n" \
"v_mfma_f32_16x16x32_bf16 a[80:83], v[80:83], v[160:163], a[80:83]\n" \
"ds_read_b128 v[72:75], v48 offset:2048\n" \
"v_mfma_f32_16x16x32_bf16 a[80:83], v[84:87], v[164:167], a[80:83]\n" \
"global_load_lds_dwordx4 v36, s[54:55] offset:0\n" \
"v_mfma_f32_16x16x32_bf16 a[84:87], v[80:83], v[168:171], a[84:87]\n" \
"ds_read_b64_tr_b16 v[140:141], v51 offset:16384\n" \
"v_mfma_f32_16x16x32_bf16 a[84:87], v[84:87], v[172:175], a[84:87]\n" \
"v_mfma_f32_16x16x32_bf16 a[88:91], v[80:83], v[176:179], a[88:91]\n" \
"ds_read_b128 v[76:79], v49 offset:2048\n" \
"v_mfma_f32_16x16x32_bf16 a[88:91], v[84:87], v[180:183], a[88:91]\n" \
"global_load_lds_dwordx4 v37, s[54:55] offset:1024\n" \
"v_mfma_f32_16x16x32_bf16 a[92:95], v[80:83], v[184:187], a[92:95]\n" \
"ds_read_b64_tr_b16 v[142:143], v51 offset:20480\n" \
"v_mfma_f32_16x16x32_bf16 a[92:95], v[84:87], v[188:191], a[92:95]\n" \
"v_mfma_f32_16x16x32_bf16 a[112:115], v[88:91], v[160:163], a[112:115]\n" \
"ds_read_b128 v[80:83], v48 offset:4096\n" \
"v_mfma_f32_16x16x32_bf16 a[112:115], v[92:95], v[164:167], a[112:115]\n" \
"global_load_lds_dwordx4 v38, s[54:55] offset:2048\n" \
"v_mfma_f32_16x16x32_bf16 a[116:119], v[88:91], v[168:171], a[116:119]\n" \
"ds_read_b64_tr_b16 v[144:145], v50 offset:512\n" \
"v_mfma_f32_16x16x32_bf16 a[116:119], v[92:95], v[172:175], a[116:119]\n" \
"v_mfma_f32_16x16x32_bf16 a[120:123], v[88:91], v[176:179], a[120:123]\n" \
"ds_read_b128 v[84:87], v49 offset:4096\n" \
"v_mfma_f32_16x16x32_bf16 a[120:123], v[92:95], v[180:183], a[120:123]\n" \
"global_load_lds_dwordx4 v39, s[54:55] offset:3072\n" \
"v_mfma_f32_16x16x32_bf16 a[124:127], v[88:91], v[184:187], a[124:127]\n" \
"s_add_u32 s54, s54, 0x80\n" \
"s_addc_u32 s55, s55, 0\n" \
"ds_read_b64_tr_b16 v[146:147], v50 offset:4608\n" \
"v_mfma_f32_16x16x32_bf16 a[124:127], v[92:95], v[188:191], a[124:127]\n" \
"s_add_u32 s68, s66, 0x200000\n" \
"s_add_u32 s64, s8, s68\n" \
"s_addc_u32 s65, s9, 0\n" \
"v_mfma_f32_16x16x32_bf16 a[144:147], v[96:99], v[160:163], a[144:147]\n" \
"ds_read_b128 v[88:91], v48 offset:6144\n" \
"v_mfma_f32_16x16x32_bf16 a[144:147], v[100:103], v[164:167], a[144:147]\n" \
"global_load_dword v253, v248, s[64:65]\n" \
"v_mfma_f32_16x16x32_bf16 a[148:151], v[96:99], v[168:171], a[148:151]\n" \
"s_add_u32 s68, s66, 0x300000\n" \
"ds_read_b64_tr_b16 v[148:149], v50 offset:16896\n" \
"v_mfma_f32_16x16x32_bf16 a[148:151], v[100:103], v[172:175], a[148:151]\n" \
"s_add_u32 s64, s8, s68\n" \
"s_addc_u32 s65, s9, 0\n" \
"v_mfma_f32_16x16x32_bf16 a[152:155], v[96:99], v[176:179], a[152:155]\n" \
"ds_read_b128 v[92:95], v49 offset:6144\n" \
"v_mfma_f32_16x16x32_bf16 a[152:155], v[100:103], v[180:183], a[152:155]\n" \
"global_load_dword v253, v248, s[64:65]\n" \
"v_mfma_f32_16x16x32_bf16 a[156:159], v[96:99], v[184:187], a[156:159]\n" \
"s_add_u32 s68, s67, 0x0\n" \
"ds_read_b64_tr_b16 v[150:151], v50 offset:20992\n" \
"v_mfma_f32_16x16x32_bf16 a[156:159], v[100:103], v[188:191], a[156:159]\n" \
"s_add_u32 s64, s12, s68\n" \
"s_addc_u32 s65, s13, 0\n" \
"v_mfma_f32_16x16x32_bf16 a[176:179], v[104:107], v[160:163], a[176:179]\n" \
"ds_read_b128 v[96:99], v48 offset:8192\n" \
"v_mfma_f32_16x16x32_bf16 a[176:179], v[108:111], v[164:167], a[176:179]\n" \
"global_load_dword v253, v248, s[64:65]\n" \
"v_mfma_f32_16x16x32_bf16 a[180:183], v[104:107], v[168:171], a[180:183]\n" \
"Lpg1118_%=:\n" \
"Lst0x1_%=:\n" \
"s_cbranch_execz Lpg3268_%=\n" \
"ds_read_b64_tr_b16 v[152:153], v51 offset:512\n" \
"v_mfma_f32_16x16x32_bf16 a[180:183], v[108:111], v[172:175], a[180:183]\n" \
"v_mfma_f32_16x16x32_bf16 a[184:187], v[104:107], v[176:179], a[184:187]\n" \
"ds_read_b128 v[100:103], v49 offset:8192\n" \
"v_mfma_f32_16x16x32_bf16 a[184:187], v[108:111], v[180:183], a[184:187]\n" \
"v_mfma_f32_16x16x32_bf16 a[188:191], v[104:107], v[184:187], a[188:191]\n" \
"ds_read_b64_tr_b16 v[154:155], v51 offset:4608\n" \
"v_mfma_f32_16x16x32_bf16 a[188:191], v[108:111], v[188:191], a[188:191]\n" \
"v_mfma_f32_16x16x32_bf16 a[208:211], v[112:115], v[160:163], a[208:211]\n" \
"ds_read_b128 v[104:107], v48 offset:10240\n" \
"v_mfma_f32_16x16x32_bf16 a[208:211], v[116:119], v[164:167], a[208:211]\n" \
"v_mfma_f32_16x16x32_bf16 a[212:215], v[112:115], v[168:171], a[212:215]\n" \
"ds_read_b64_tr_b16 v[156:157], v51 offset:16896\n" \
"v_mfma_f32_16x16x32_bf16 a[212:215], v[116:119], v[172:175], a[212:215]\n" \
"v_mfma_f32_16x16x32_bf16 a[216:219], v[112:115], v[176:179], a[216:219]\n" \
"ds_read_b128 v[108:111], v49 offset:10240\n" \
"v_mfma_f32_16x16x32_bf16 a[216:219], v[116:119], v[180:183], a[216:219]\n" \
"v_mfma_f32_16x16x32_bf16 a[220:223], v[112:115], v[184:187], a[220:223]\n" \
"ds_read_b64_tr_b16 v[158:159], v51 offset:20992\n" \
"v_mfma_f32_16x16x32_bf16 a[220:223], v[116:119], v[188:191], a[220:223]\n" \
"v_mfma_f32_16x16x32_bf16 a[240:243], v[120:123], v[160:163], a[240:243]\n" \
"ds_read_b128 v[112:115], v48 offset:12288\n" \
"v_mfma_f32_16x16x32_bf16 a[240:243], v[124:127], v[164:167], a[240:243]\n" \
"v_mfma_f32_16x16x32_bf16 a[244:247], v[120:123], v[168:171], a[244:247]\n" \
"v_mfma_f32_16x16x32_bf16 a[244:247], v[124:127], v[172:175], a[244:247]\n" \
"v_mfma_f32_16x16x32_bf16 a[248:251], v[120:123], v[176:179], a[248:251]\n" \
"ds_read_b128 v[116:119], v49 offset:12288\n" \
"v_mfma_f32_16x16x32_bf16 a[248:251], v[124:127], v[180:183], a[248:251]\n" \
"v_mfma_f32_16x16x32_bf16 a[252:255], v[120:123], v[184:187], a[252:255]\n" \
"v_mfma_f32_16x16x32_bf16 a[252:255], v[124:127], v[188:191], a[252:255]\n" \
"s_waitcnt lgkmcnt(1)\n" \
"v_mfma_f32_16x16x32_bf16 a[0:3], v[128:131], v[64:67], a[0:3]\n" \
"ds_read_b128 v[120:123], v48 offset:14336\n" \
"v_mfma_f32_16x16x32_bf16 a[0:3], v[132:135], v[68:71], a[0:3]\n" \
"v_mfma_f32_16x16x32_bf16 a[4:7], v[136:139], v[64:67], a[4:7]\n" \
"ds_read_b128 v[124:127], v49 offset:14336\n" \
"s_add_u32 m0, s87, 135168\n" \
"v_mfma_f32_16x16x32_bf16 a[4:7], v[140:143], v[68:71], a[4:7]\n" \
"global_load_lds_dwordx4 v40, s[52:53] offset:-4096\n" \
"v_mfma_f32_16x16x32_bf16 a[8:11], v[144:147], v[64:67], a[8:11]\n" \
"ds_read_b64_tr_b16 v[160:161], v50 offset:1024\n" \
"v_mfma_f32_16x16x32_bf16 a[8:11], v[148:151], v[68:71], a[8:11]\n" \
"v_mfma_f32_16x16x32_bf16 a[12:15], v[152:155], v[64:67], a[12:15]\n" \
"ds_read_b64_tr_b16 v[162:163], v50 offset:5120\n" \
"v_mfma_f32_16x16x32_bf16 a[12:15], v[156:159], v[68:71], a[12:15]\n" \
"global_load_lds_dwordx4 v41, s[50:51] offset:-3072\n" \
"v_mfma_f32_16x16x32_bf16 a[32:35], v[128:131], v[72:75], a[32:35]\n" \
"ds_read_b64_tr_b16 v[164:165], v50 offset:17408\n" \
"v_mfma_f32_16x16x32_bf16 a[32:35], v[132:135], v[76:79], a[32:35]\n" \
"v_mfma_f32_16x16x32_bf16 a[36:39], v[136:139], v[72:75], a[36:39]\n" \
"ds_read_b64_tr_b16 v[166:167], v50 offset:21504\n" \
"v_mfma_f32_16x16x32_bf16 a[36:39], v[140:143], v[76:79], a[36:39]\n" \
"global_load_lds_dwordx4 v42, s[52:53] offset:-2048\n" \
"v_mfma_f32_16x16x32_bf16 a[40:43], v[144:147], v[72:75], a[40:43]\n" \
"ds_read_b64_tr_b16 v[168:169], v51 offset:1024\n" \
"v_mfma_f32_16x16x32_bf16 a[40:43], v[148:151], v[76:79], a[40:43]\n" \
"v_mfma_f32_16x16x32_bf16 a[44:47], v[152:155], v[72:75], a[44:47]\n" \
"ds_read_b64_tr_b16 v[170:171], v51 offset:5120\n" \
"v_mfma_f32_16x16x32_bf16 a[44:47], v[156:159], v[76:79], a[44:47]\n" \
"global_load_lds_dwordx4 v43, s[50:51] offset:-1024\n" \
"v_mfma_f32_16x16x32_bf16 a[64:67], v[128:131], v[80:83], a[64:67]\n" \
"ds_read_b64_tr_b16 v[172:173], v51 offset:17408\n" \
"v_mfma_f32_16x16x32_bf16 a[64:67], v[132:135], v[84:87], a[64:67]\n" \
"v_mfma_f32_16x16x32_bf16 a[68:71], v[136:139], v[80:83], a[68:71]\n" \
"ds_read_b64_tr_b16 v[174:175], v51 offset:21504\n" \
"v_mfma_f32_16x16x32_bf16 a[68:71], v[140:143], v[84:87], a[68:71]\n" \
"global_load_lds_dwordx4 v44, s[52:53] offset:0\n" \
"v_mfma_f32_16x16x32_bf16 a[72:75], v[144:147], v[80:83], a[72:75]\n" \
"ds_read_b64_tr_b16 v[176:177], v50 offset:1536\n" \
"v_mfma_f32_16x16x32_bf16 a[72:75], v[148:151], v[84:87], a[72:75]\n" \
"v_mfma_f32_16x16x32_bf16 a[76:79], v[152:155], v[80:83], a[76:79]\n" \
"ds_read_b64_tr_b16 v[178:179], v50 offset:5632\n" \
"v_mfma_f32_16x16x32_bf16 a[76:79], v[156:159], v[84:87], a[76:79]\n" \
"global_load_lds_dwordx4 v45, s[50:51] offset:1024\n" \
"v_mfma_f32_16x16x32_bf16 a[96:99], v[128:131], v[88:91], a[96:99]\n" \
"ds_read_b64_tr_b16 v[180:181], v50 offset:17920\n" \
"v_mfma_f32_16x16x32_bf16 a[96:99], v[132:135], v[92:95], a[96:99]\n" \
"v_mfma_f32_16x16x32_bf16 a[100:103], v[136:139], v[88:91], a[100:103]\n" \
"ds_read_b64_tr_b16 v[182:183], v50 offset:22016\n" \
"v_mfma_f32_16x16x32_bf16 a[100:103], v[140:143], v[92:95], a[100:103]\n" \
"global_load_lds_dwordx4 v46, s[52:53] offset:2048\n" \
"v_mfma_f32_16x16x32_bf16 a[104:107], v[144:147], v[88:91], a[104:107]\n" \
"ds_read_b64_tr_b16 v[184:185], v51 offset:1536\n" \
"v_mfma_f32_16x16x32_bf16 a[104:107], v[148:151], v[92:95], a[104:107]\n" \
"v_mfma_f32_16x16x32_bf16 a[108:111], v[152:155], v[88:91], a[108:111]\n" \
"ds_read_b64_tr_b16 v[186:187], v51 offset:5632\n" \
"v_mfma_f32_16x16x32_bf16 a[108:111], v[156:159], v[92:95], a[108:111]\n" \
"global_load_lds_dwordx4 v47, s[50:51] offset:3072\n" \
"v_mfma_f32_16x16x32_bf16 a[128:131], v[128:131], v[96:99], a[128:131]\n" \
"s_add_u32 s52, s52, 0x80000\n" \
"s_addc_u32 s53, s53, 0\n" \
"ds_read_b64_tr_b16 v[188:189], v51 offset:17920\n" \
"v_mfma_f32_16x16x32_bf16 a[128:131], v[132:135], v[100:103], a[128:131]\n" \
"s_add_u32 s50, s50, 0x80000\n" \
"s_addc_u32 s51, s51, 0\n" \
"v_mfma_f32_16x16x32_bf16 a[132:135], v[136:139], v[96:99], a[132:135]\n" \
"ds_read_b64_tr_b16 v[190:191], v51 offset:22016\n" \
"v_mfma_f32_16x16x32_bf16 a[132:135], v[140:143], v[100:103], a[132:135]\n" \
"v_mfma_f32_16x16x32_bf16 a[136:139], v[144:147], v[96:99], a[136:139]\n" \
"v_mfma_f32_16x16x32_bf16 a[136:139], v[148:151], v[100:103], a[136:139]\n" \
"v_mfma_f32_16x16x32_bf16 a[140:143], v[152:155], v[96:99], a[140:143]\n" \
"v_mfma_f32_16x16x32_bf16 a[140:143], v[156:159], v[100:103], a[140:143]\n" \
"v_mfma_f32_16x16x32_bf16 a[160:163], v[128:131], v[104:107], a[160:163]\n" \
"v_add_u32 v48, 0xfffe8000, v48\n" \
"v_add_u32 v49, 0xfffe8000, v49\n" \
"v_mfma_f32_16x16x32_bf16 a[160:163], v[132:135], v[108:111], a[160:163]\n" \
"v_add_u32 v50, 0xffff8000, v50\n" \
"v_add_u32 v51, 0xffff8000, v51\n" \
"v_mfma_f32_16x16x32_bf16 a[164:167], v[136:139], v[104:107], a[164:167]\n" \
"v_mfma_f32_16x16x32_bf16 a[164:167], v[140:143], v[108:111], a[164:167]\n" \
"v_mfma_f32_16x16x32_bf16 a[168:171], v[144:147], v[104:107], a[168:171]\n" \
"v_mfma_f32_16x16x32_bf16 a[168:171], v[148:151], v[108:111], a[168:171]\n" \
"v_mfma_f32_16x16x32_bf16 a[172:175], v[152:155], v[104:107], a[172:175]\n" \
"v_mfma_f32_16x16x32_bf16 a[172:175], v[156:159], v[108:111], a[172:175]\n" \
"s_waitcnt lgkmcnt(0)\n" \
"v_mfma_f32_16x16x32_bf16 a[192:195], v[128:131], v[112:115], a[192:195]\n" \
"v_mfma_f32_16x16x32_bf16 a[192:195], v[132:135], v[116:119], a[192:195]\n" \
"v_mfma_f32_16x16x32_bf16 a[196:199], v[136:139], v[112:115], a[196:199]\n" \
"v_mfma_f32_16x16x32_bf16 a[196:199], v[140:143], v[116:119], a[196:199]\n" \
"v_mfma_f32_16x16x32_bf16 a[200:203], v[144:147], v[112:115], a[200:203]\n" \
"v_mfma_f32_16x16x32_bf16 a[200:203], v[148:151], v[116:119], a[200:203]\n" \
"v_mfma_f32_16x16x32_bf16 a[204:207], v[152:155], v[112:115], a[204:207]\n" \
"v_mfma_f32_16x16x32_bf16 a[204:207], v[156:159], v[116:119], a[204:207]\n" \
"v_mfma_f32_16x16x32_bf16 a[224:227], v[128:131], v[120:123], a[224:227]\n" \
"v_mfma_f32_16x16x32_bf16 a[224:227], v[132:135], v[124:127], a[224:227]\n" \
"v_mfma_f32_16x16x32_bf16 a[228:231], v[136:139], v[120:123], a[228:231]\n" \
"v_mfma_f32_16x16x32_bf16 a[228:231], v[140:143], v[124:127], a[228:231]\n" \
"v_mfma_f32_16x16x32_bf16 a[232:235], v[144:147], v[120:123], a[232:235]\n" \
"v_mfma_f32_16x16x32_bf16 a[232:235], v[148:151], v[124:127], a[232:235]\n" \
"v_mfma_f32_16x16x32_bf16 a[236:239], v[152:155], v[120:123], a[236:239]\n" \
"s_waitcnt vmcnt(11) lgkmcnt(0)\n" \
"v_mfma_f32_16x16x32_bf16 a[236:239], v[156:159], v[124:127], a[236:239]\n" \
"s_barrier\n" \
"v_mfma_f32_16x16x32_bf16 a[16:19], v[64:67], v[160:163], a[16:19]\n" \
"s_add_u32 m0, s87, 69632\n" \
"ds_read_b64_tr_b16 v[128:129], v50 offset:0\n" \
"v_mfma_f32_16x16x32_bf16 a[16:19], v[68:71], v[164:167], a[16:19]\n" \
"global_load_lds_dwordx4 v32, s[54:55] offset:-4096\n" \
"v_mfma_f32_16x16x32_bf16 a[20:23], v[64:67], v[168:171], a[20:23]\n" \
"ds_read_b64_tr_b16 v[130:131], v50 offset:4096\n" \
"v_mfma_f32_16x16x32_bf16 a[20:23], v[68:71], v[172:175], a[20:23]\n" \
"v_mfma_f32_16x16x32_bf16 a[24:27], v[64:67], v[176:179], a[24:27]\n" \
"ds_read_b64_tr_b16 v[132:133], v50 offset:16384\n" \
"v_mfma_f32_16x16x32_bf16 a[24:27], v[68:71], v[180:183], a[24:27]\n" \
"global_load_lds_dwordx4 v33, s[54:55] offset:-3072\n" \
"v_mfma_f32_16x16x32_bf16 a[28:31], v[64:67], v[184:187], a[28:31]\n" \
"ds_read_b64_tr_b16 v[134:135], v50 offset:20480\n" \
"v_mfma_f32_16x16x32_bf16 a[28:31], v[68:71], v[188:191], a[28:31]\n" \
"v_mfma_f32_16x16x32_bf16 a[48:51], v[72:75], v[160:163], a[48:51]\n" \
"ds_read_b128 v[64:67], v48 offset:0\n" \
"v_mfma_f32_16x16x32_bf16 a[48:51], v[76:79], v[164:167], a[48:51]\n" \
"global_load_lds_dwordx4 v34, s[54:55] offset:-2048\n" \
"v_mfma_f32_16x16x32_bf16 a[52:55], v[72:75], v[168:171], a[52:55]\n" \
"ds_read_b64_tr_b16 v[136:137], v51 offset:0\n" \
"v_mfma_f32_16x16x32_bf16 a[52:55], v[76:79], v[172:175], a[52:55]\n" \
"v_mfma_f32_16x16x32_bf16 a[56:59], v[72:75], v[176:179], a[56:59]\n" \
"ds_read_b128 v[68:71], v49 offset:0\n" \
"v_mfma_f32_16x16x32_bf16 a[56:59], v[76:79], v[180:183], a[56:59]\n" \
"global_load_lds_dwordx4 v35, s[54:55] offset:-1024\n" \
"v_mfma_f32_16x16x32_bf16 a[60:63], v[72:75], v[184:187], a[60:63]\n" \
"ds_read_b64_tr_b16 v[138:139], v51 offset:4096\n" \
"v_mfma_f32_16x16x32_bf16 a[60:63], v[76:79], v[188:191], a[60:63]\n" \
"v_mfma_f32_16x16x32_bf16 a[80:83], v[80:83], v[160:163], a[80:83]\n" \
"ds_read_b128 v[72:75], v48 offset:2048\n" \
"v_mfma_f32_16x16x32_bf16 a[80:83], v[84:87], v[164:167], a[80:83]\n" \
"global_load_lds_dwordx4 v36, s[54:55] offset:0\n" \
"v_mfma_f32_16x16x32_bf16 a[84:87], v[80:83], v[168:171], a[84:87]\n" \
"ds_read_b64_tr_b16 v[140:141], v51 offset:16384\n" \
"v_mfma_f32_16x16x32_bf16 a[84:87], v[84:87], v[172:175], a[84:87]\n" \
"v_mfma_f32_16x16x32_bf16 a[88:91], v[80:83], v[176:179], a[88:91]\n" \
"ds_read_b128 v[76:79], v49 offset:2048\n" \
"v_mfma_f32_16x16x32_bf16 a[88:91], v[84:87], v[180:183], a[88:91]\n" \
"global_load_lds_dwordx4 v37, s[54:55] offset:1024\n" \
"v_mfma_f32_16x16x32_bf16 a[92:95], v[80:83], v[184:187], a[92:95]\n" \
"ds_read_b64_tr_b16 v[142:143], v51 offset:20480\n" \
"v_mfma_f32_16x16x32_bf16 a[92:95], v[84:87], v[188:191], a[92:95]\n" \
"v_mfma_f32_16x16x32_bf16 a[112:115], v[88:91], v[160:163], a[112:115]\n" \
"ds_read_b128 v[80:83], v48 offset:4096\n" \
"v_mfma_f32_16x16x32_bf16 a[112:115], v[92:95], v[164:167], a[112:115]\n" \
"global_load_lds_dwordx4 v38, s[54:55] offset:2048\n" \
"v_mfma_f32_16x16x32_bf16 a[116:119], v[88:91], v[168:171], a[116:119]\n" \
"ds_read_b64_tr_b16 v[144:145], v50 offset:512\n" \
"v_mfma_f32_16x16x32_bf16 a[116:119], v[92:95], v[172:175], a[116:119]\n" \
"v_mfma_f32_16x16x32_bf16 a[120:123], v[88:91], v[176:179], a[120:123]\n" \
"ds_read_b128 v[84:87], v49 offset:4096\n" \
"v_mfma_f32_16x16x32_bf16 a[120:123], v[92:95], v[180:183], a[120:123]\n" \
"global_load_lds_dwordx4 v39, s[54:55] offset:3072\n" \
"v_mfma_f32_16x16x32_bf16 a[124:127], v[88:91], v[184:187], a[124:127]\n" \
"s_add_u32 s54, s54, 0x80\n" \
"s_addc_u32 s55, s55, 0\n" \
"ds_read_b64_tr_b16 v[146:147], v50 offset:4608\n" \
"v_mfma_f32_16x16x32_bf16 a[124:127], v[92:95], v[188:191], a[124:127]\n" \
"s_add_u32 s68, s67, 0x100000\n" \
"s_add_u32 s64, s12, s68\n" \
"s_addc_u32 s65, s13, 0\n" \
"v_mfma_f32_16x16x32_bf16 a[144:147], v[96:99], v[160:163], a[144:147]\n" \
"ds_read_b128 v[88:91], v48 offset:6144\n" \
"v_mfma_f32_16x16x32_bf16 a[144:147], v[100:103], v[164:167], a[144:147]\n" \
"global_load_dword v253, v248, s[64:65]\n" \
"v_mfma_f32_16x16x32_bf16 a[148:151], v[96:99], v[168:171], a[148:151]\n" \
"s_add_u32 s68, s67, 0x500000\n" \
"ds_read_b64_tr_b16 v[148:149], v50 offset:16896\n" \
"v_mfma_f32_16x16x32_bf16 a[148:151], v[100:103], v[172:175], a[148:151]\n" \
"s_add_u32 s64, s12, s68\n" \
"s_addc_u32 s65, s13, 0\n" \
"v_mfma_f32_16x16x32_bf16 a[152:155], v[96:99], v[176:179], a[152:155]\n" \
"ds_read_b128 v[92:95], v49 offset:6144\n" \
"v_mfma_f32_16x16x32_bf16 a[152:155], v[100:103], v[180:183], a[152:155]\n" \
"global_load_dword v253, v248, s[64:65]\n" \
"v_mfma_f32_16x16x32_bf16 a[156:159], v[96:99], v[184:187], a[156:159]\n" \
"s_add_u32 s68, s67, 0x200000\n" \
"ds_read_b64_tr_b16 v[150:151], v50 offset:20992\n" \
"v_mfma_f32_16x16x32_bf16 a[156:159], v[100:103], v[188:191], a[156:159]\n" \
"s_add_u32 s64, s12, s68\n" \
"s_addc_u32 s65, s13, 0\n" \
"v_mfma_f32_16x16x32_bf16 a[176:179], v[104:107], v[160:163], a[176:179]\n" \
"ds_read_b128 v[96:99], v48 offset:8192\n" \
"v_mfma_f32_16x16x32_bf16 a[176:179], v[108:111], v[164:167], a[176:179]\n" \
"global_load_dword v253, v248, s[64:65]\n" \
"v_mfma_f32_16x16x32_bf16 a[180:183], v[104:107], v[168:171], a[180:183]\n" \
"ds_read_b64_tr_b16 v[152:153], v51 offset:512\n" \
"v_mfma_f32_16x16x32_bf16 a[180:183], v[108:111], v[172:175], a[180:183]\n" \
"v_mfma_f32_16x16x32_bf16 a[184:187], v[104:107], v[176:179], a[184:187]\n" \
"ds_read_b128 v[100:103], v49 offset:8192\n" \
"v_mfma_f32_16x16x32_bf16 a[184:187], v[108:111], v[180:183], a[184:187]\n" \
"v_mfma_f32_16x16x32_bf16 a[188:191], v[104:107], v[184:187], a[188:191]\n" \
"ds_read_b64_tr_b16 v[154:155], v51 offset:4608\n" \
"v_mfma_f32_16x16x32_bf16 a[188:191], v[108:111], v[188:191], a[188:191]\n" \
"v_mfma_f32_16x16x32_bf16 a[208:211], v[112:115], v[160:163], a[208:211]\n" \
"ds_read_b128 v[104:107], v48 offset:10240\n" \
"v_mfma_f32_16x16x32_bf16 a[208:211], v[116:119], v[164:167], a[208:211]\n" \
"v_mfma_f32_16x16x32_bf16 a[212:215], v[112:115], v[168:171], a[212:215]\n" \
"ds_read_b64_tr_b16 v[156:157], v51 offset:16896\n" \
"v_mfma_f32_16x16x32_bf16 a[212:215], v[116:119], v[172:175], a[212:215]\n" \
"v_mfma_f32_16x16x32_bf16 a[216:219], v[112:115], v[176:179], a[216:219]\n" \
"ds_read_b128 v[108:111], v49 offset:10240\n" \
"v_mfma_f32_16x16x32_bf16 a[216:219], v[116:119], v[180:183], a[216:219]\n" \
"v_mfma_f32_16x16x32_bf16 a[220:223], v[112:115], v[184:187], a[220:223]\n" \
"ds_read_b64_tr_b16 v[158:159], v51 offset:20992\n" \
"v_mfma_f32_16x16x32_bf16 a[220:223], v[116:119], v[188:191], a[220:223]\n" \
"v_mfma_f32_16x16x32_bf16 a[240:243], v[120:123], v[160:163], a[240:243]\n" \
"ds_read_b128 v[112:115], v48 offset:12288\n" \
"v_mfma_f32_16x16x32_bf16 a[240:243], v[124:127], v[164:167], a[240:243]\n" \
"v_mfma_f32_16x16x32_bf16 a[244:247], v[120:123], v[168:171], a[244:247]\n" \
"v_mfma_f32_16x16x32_bf16 a[244:247], v[124:127], v[172:175], a[244:247]\n" \
"v_mfma_f32_16x16x32_bf16 a[248:251], v[120:123], v[176:179], a[248:251]\n" \
"ds_read_b128 v[116:119], v49 offset:12288\n" \
"v_mfma_f32_16x16x32_bf16 a[248:251], v[124:127], v[180:183], a[248:251]\n" \
"v_mfma_f32_16x16x32_bf16 a[252:255], v[120:123], v[184:187], a[252:255]\n" \
"v_mfma_f32_16x16x32_bf16 a[252:255], v[124:127], v[188:191], a[252:255]\n" \
"s_waitcnt lgkmcnt(1)\n" \
"v_mfma_f32_16x16x32_bf16 a[0:3], v[128:131], v[64:67], a[0:3]\n" \
"ds_read_b128 v[120:123], v48 offset:14336\n" \
"v_mfma_f32_16x16x32_bf16 a[0:3], v[132:135], v[68:71], a[0:3]\n" \
"v_mfma_f32_16x16x32_bf16 a[4:7], v[136:139], v[64:67], a[4:7]\n" \
"ds_read_b128 v[124:127], v49 offset:14336\n" \
"s_add_u32 m0, s87, 102400\n" \
"v_mfma_f32_16x16x32_bf16 a[4:7], v[140:143], v[68:71], a[4:7]\n" \
"global_load_lds_dwordx4 v40, s[52:53] offset:-4096\n" \
"v_mfma_f32_16x16x32_bf16 a[8:11], v[144:147], v[64:67], a[8:11]\n" \
"ds_read_b64_tr_b16 v[160:161], v50 offset:1024\n" \
"v_mfma_f32_16x16x32_bf16 a[8:11], v[148:151], v[68:71], a[8:11]\n" \
"v_mfma_f32_16x16x32_bf16 a[12:15], v[152:155], v[64:67], a[12:15]\n" \
"ds_read_b64_tr_b16 v[162:163], v50 offset:5120\n" \
"v_mfma_f32_16x16x32_bf16 a[12:15], v[156:159], v[68:71], a[12:15]\n" \
"global_load_lds_dwordx4 v41, s[50:51] offset:-3072\n" \
"v_mfma_f32_16x16x32_bf16 a[32:35], v[128:131], v[72:75], a[32:35]\n" \
"ds_read_b64_tr_b16 v[164:165], v50 offset:17408\n" \
"v_mfma_f32_16x16x32_bf16 a[32:35], v[132:135], v[76:79], a[32:35]\n" \
"v_mfma_f32_16x16x32_bf16 a[36:39], v[136:139], v[72:75], a[36:39]\n" \
"ds_read_b64_tr_b16 v[166:167], v50 offset:21504\n" \
"v_mfma_f32_16x16x32_bf16 a[36:39], v[140:143], v[76:79], a[36:39]\n" \
"global_load_lds_dwordx4 v42, s[52:53] offset:-2048\n" \
"v_mfma_f32_16x16x32_bf16 a[40:43], v[144:147], v[72:75], a[40:43]\n" \
"ds_read_b64_tr_b16 v[168:169], v51 offset:1024\n" \
"v_mfma_f32_16x16x32_bf16 a[40:43], v[148:151], v[76:79], a[40:43]\n" \
"v_mfma_f32_16x16x32_bf16 a[44:47], v[152:155], v[72:75], a[44:47]\n" \
"ds_read_b64_tr_b16 v[170:171], v51 offset:5120\n" \
"v_mfma_f32_16x16x32_bf16 a[44:47], v[156:159], v[76:79], a[44:47]\n" \
"global_load_lds_dwordx4 v43, s[50:51] offset:-1024\n" \
"v_mfma_f32_16x16x32_bf16 a[64:67], v[128:131], v[80:83], a[64:67]\n" \
"ds_read_b64_tr_b16 v[172:173], v51 offset:17408\n" \
"v_mfma_f32_16x16x32_bf16 a[64:67], v[132:135], v[84:87], a[64:67]\n" \
"v_mfma_f32_16x16x32_bf16 a[68:71], v[136:139], v[80:83], a[68:71]\n" \
"ds_read_b64_tr_b16 v[174:175], v51 offset:21504\n" \
"v_mfma_f32_16x16x32_bf16 a[68:71], v[140:143], v[84:87], a[68:71]\n" \
"global_load_lds_dwordx4 v44, s[52:53] offset:0\n" \
"v_mfma_f32_16x16x32_bf16 a[72:75], v[144:147], v[80:83], a[72:75]\n" \
"ds_read_b64_tr_b16 v[176:177], v50 offset:1536\n" \
"v_mfma_f32_16x16x32_bf16 a[72:75], v[148:151], v[84:87], a[72:75]\n" \
"v_mfma_f32_16x16x32_bf16 a[76:79], v[152:155], v[80:83], a[76:79]\n" \
"ds_read_b64_tr_b16 v[178:179], v50 offset:5632\n" \
"v_mfma_f32_16x16x32_bf16 a[76:79], v[156:159], v[84:87], a[76:79]\n" \
"global_load_lds_dwordx4 v45, s[50:51] offset:1024\n" \
"v_mfma_f32_16x16x32_bf16 a[96:99], v[128:131], v[88:91], a[96:99]\n" \
"ds_read_b64_tr_b16 v[180:181], v50 offset:17920\n" \
"v_mfma_f32_16x16x32_bf16 a[96:99], v[132:135], v[92:95], a[96:99]\n" \
"v_mfma_f32_16x16x32_bf16 a[100:103], v[136:139], v[88:91], a[100:103]\n" \
"ds_read_b64_tr_b16 v[182:183], v50 offset:22016\n" \
"v_mfma_f32_16x16x32_bf16 a[100:103], v[140:143], v[92:95], a[100:103]\n" \
"global_load_lds_dwordx4 v46, s[52:53] offset:2048\n" \
"v_mfma_f32_16x16x32_bf16 a[104:107], v[144:147], v[88:91], a[104:107]\n" \
"ds_read_b64_tr_b16 v[184:185], v51 offset:1536\n" \
"v_mfma_f32_16x16x32_bf16 a[104:107], v[148:151], v[92:95], a[104:107]\n" \
"v_mfma_f32_16x16x32_bf16 a[108:111], v[152:155], v[88:91], a[108:111]\n" \
"ds_read_b64_tr_b16 v[186:187], v51 offset:5632\n" \
"v_mfma_f32_16x16x32_bf16 a[108:111], v[156:159], v[92:95], a[108:111]\n" \
"global_load_lds_dwordx4 v47, s[50:51] offset:3072\n" \
"v_mfma_f32_16x16x32_bf16 a[128:131], v[128:131], v[96:99], a[128:131]\n" \
"ds_read_b64_tr_b16 v[188:189], v51 offset:17920\n" \
"v_mfma_f32_16x16x32_bf16 a[128:131], v[132:135], v[100:103], a[128:131]\n" \
"v_mfma_f32_16x16x32_bf16 a[132:135], v[136:139], v[96:99], a[132:135]\n" \
"ds_read_b64_tr_b16 v[190:191], v51 offset:22016\n" \
"v_mfma_f32_16x16x32_bf16 a[132:135], v[140:143], v[100:103], a[132:135]\n" \
"v_mfma_f32_16x16x32_bf16 a[136:139], v[144:147], v[96:99], a[136:139]\n" \
"v_mfma_f32_16x16x32_bf16 a[136:139], v[148:151], v[100:103], a[136:139]\n" \
"v_mfma_f32_16x16x32_bf16 a[140:143], v[152:155], v[96:99], a[140:143]\n" \
"v_mfma_f32_16x16x32_bf16 a[140:143], v[156:159], v[100:103], a[140:143]\n" \
"v_mfma_f32_16x16x32_bf16 a[160:163], v[128:131], v[104:107], a[160:163]\n" \
"v_add_u32 v48, 0x10000, v48\n" \
"v_add_u32 v49, 0x10000, v49\n" \
"v_mfma_f32_16x16x32_bf16 a[160:163], v[132:135], v[108:111], a[160:163]\n" \
"v_add_u32 v50, 0x18000, v50\n" \
"v_add_u32 v51, 0x18000, v51\n" \
"v_mfma_f32_16x16x32_bf16 a[164:167], v[136:139], v[104:107], a[164:167]\n" \
"v_mfma_f32_16x16x32_bf16 a[164:167], v[140:143], v[108:111], a[164:167]\n" \
"v_mfma_f32_16x16x32_bf16 a[168:171], v[144:147], v[104:107], a[168:171]\n" \
"v_mfma_f32_16x16x32_bf16 a[168:171], v[148:151], v[108:111], a[168:171]\n" \
"v_mfma_f32_16x16x32_bf16 a[172:175], v[152:155], v[104:107], a[172:175]\n" \
"v_mfma_f32_16x16x32_bf16 a[172:175], v[156:159], v[108:111], a[172:175]\n" \
"s_waitcnt lgkmcnt(0)\n" \
"v_mfma_f32_16x16x32_bf16 a[192:195], v[128:131], v[112:115], a[192:195]\n" \
"v_mfma_f32_16x16x32_bf16 a[192:195], v[132:135], v[116:119], a[192:195]\n" \
"v_mfma_f32_16x16x32_bf16 a[196:199], v[136:139], v[112:115], a[196:199]\n" \
"v_mfma_f32_16x16x32_bf16 a[196:199], v[140:143], v[116:119], a[196:199]\n" \
"v_mfma_f32_16x16x32_bf16 a[200:203], v[144:147], v[112:115], a[200:203]\n" \
"v_mfma_f32_16x16x32_bf16 a[200:203], v[148:151], v[116:119], a[200:203]\n" \
"v_mfma_f32_16x16x32_bf16 a[204:207], v[152:155], v[112:115], a[204:207]\n" \
"v_mfma_f32_16x16x32_bf16 a[204:207], v[156:159], v[116:119], a[204:207]\n" \
"v_mfma_f32_16x16x32_bf16 a[224:227], v[128:131], v[120:123], a[224:227]\n" \
"v_mfma_f32_16x16x32_bf16 a[224:227], v[132:135], v[124:127], a[224:227]\n" \
"v_mfma_f32_16x16x32_bf16 a[228:231], v[136:139], v[120:123], a[228:231]\n" \
"v_mfma_f32_16x16x32_bf16 a[228:231], v[140:143], v[124:127], a[228:231]\n" \
"v_mfma_f32_16x16x32_bf16 a[232:235], v[144:147], v[120:123], a[232:235]\n" \
"v_mfma_f32_16x16x32_bf16 a[232:235], v[148:151], v[124:127], a[232:235]\n" \
"v_mfma_f32_16x16x32_bf16 a[236:239], v[152:155], v[120:123], a[236:239]\n" \
"s_waitcnt vmcnt(11) lgkmcnt(0)\n" \
"v_mfma_f32_16x16x32_bf16 a[236:239], v[156:159], v[124:127], a[236:239]\n" \
"s_barrier\n" \
"v_mfma_f32_16x16x32_bf16 a[16:19], v[64:67], v[160:163], a[16:19]\n" \
"s_add_u32 m0, s87, 36864\n" \
"ds_read_b64_tr_b16 v[128:129], v50 offset:0\n" \
"v_mfma_f32_16x16x32_bf16 a[16:19], v[68:71], v[164:167], a[16:19]\n" \
"global_load_lds_dwordx4 v32, s[54:55] offset:-4096\n" \
"v_mfma_f32_16x16x32_bf16 a[20:23], v[64:67], v[168:171], a[20:23]\n" \
"ds_read_b64_tr_b16 v[130:131], v50 offset:4096\n" \
"v_mfma_f32_16x16x32_bf16 a[20:23], v[68:71], v[172:175], a[20:23]\n" \
"v_mfma_f32_16x16x32_bf16 a[24:27], v[64:67], v[176:179], a[24:27]\n" \
"ds_read_b64_tr_b16 v[132:133], v50 offset:16384\n" \
"v_mfma_f32_16x16x32_bf16 a[24:27], v[68:71], v[180:183], a[24:27]\n" \
"global_load_lds_dwordx4 v33, s[54:55] offset:-3072\n" \
"v_mfma_f32_16x16x32_bf16 a[28:31], v[64:67], v[184:187], a[28:31]\n" \
"ds_read_b64_tr_b16 v[134:135], v50 offset:20480\n" \
"v_mfma_f32_16x16x32_bf16 a[28:31], v[68:71], v[188:191], a[28:31]\n" \
"v_mfma_f32_16x16x32_bf16 a[48:51], v[72:75], v[160:163], a[48:51]\n" \
"ds_read_b128 v[64:67], v48 offset:0\n" \
"v_mfma_f32_16x16x32_bf16 a[48:51], v[76:79], v[164:167], a[48:51]\n" \
"global_load_lds_dwordx4 v34, s[54:55] offset:-2048\n" \
"v_mfma_f32_16x16x32_bf16 a[52:55], v[72:75], v[168:171], a[52:55]\n" \
"ds_read_b64_tr_b16 v[136:137], v51 offset:0\n" \
"v_mfma_f32_16x16x32_bf16 a[52:55], v[76:79], v[172:175], a[52:55]\n" \
"v_mfma_f32_16x16x32_bf16 a[56:59], v[72:75], v[176:179], a[56:59]\n" \
"ds_read_b128 v[68:71], v49 offset:0\n" \
"v_mfma_f32_16x16x32_bf16 a[56:59], v[76:79], v[180:183], a[56:59]\n" \
"global_load_lds_dwordx4 v35, s[54:55] offset:-1024\n" \
"v_mfma_f32_16x16x32_bf16 a[60:63], v[72:75], v[184:187], a[60:63]\n" \
"ds_read_b64_tr_b16 v[138:139], v51 offset:4096\n" \
"v_mfma_f32_16x16x32_bf16 a[60:63], v[76:79], v[188:191], a[60:63]\n" \
"v_mfma_f32_16x16x32_bf16 a[80:83], v[80:83], v[160:163], a[80:83]\n" \
"ds_read_b128 v[72:75], v48 offset:2048\n" \
"v_mfma_f32_16x16x32_bf16 a[80:83], v[84:87], v[164:167], a[80:83]\n" \
"global_load_lds_dwordx4 v36, s[54:55] offset:0\n" \
"v_mfma_f32_16x16x32_bf16 a[84:87], v[80:83], v[168:171], a[84:87]\n" \
"ds_read_b64_tr_b16 v[140:141], v51 offset:16384\n" \
"v_mfma_f32_16x16x32_bf16 a[84:87], v[84:87], v[172:175], a[84:87]\n" \
"v_mfma_f32_16x16x32_bf16 a[88:91], v[80:83], v[176:179], a[88:91]\n" \
"ds_read_b128 v[76:79], v49 offset:2048\n" \
"v_mfma_f32_16x16x32_bf16 a[88:91], v[84:87], v[180:183], a[88:91]\n" \
"global_load_lds_dwordx4 v37, s[54:55] offset:1024\n" \
"v_mfma_f32_16x16x32_bf16 a[92:95], v[80:83], v[184:187], a[92:95]\n" \
"ds_read_b64_tr_b16 v[142:143], v51 offset:20480\n" \
"v_mfma_f32_16x16x32_bf16 a[92:95], v[84:87], v[188:191], a[92:95]\n" \
"v_mfma_f32_16x16x32_bf16 a[112:115], v[88:91], v[160:163], a[112:115]\n" \
"ds_read_b128 v[80:83], v48 offset:4096\n" \
"v_mfma_f32_16x16x32_bf16 a[112:115], v[92:95], v[164:167], a[112:115]\n" \
"global_load_lds_dwordx4 v38, s[54:55] offset:2048\n" \
"v_mfma_f32_16x16x32_bf16 a[116:119], v[88:91], v[168:171], a[116:119]\n" \
"ds_read_b64_tr_b16 v[144:145], v50 offset:512\n" \
"v_mfma_f32_16x16x32_bf16 a[116:119], v[92:95], v[172:175], a[116:119]\n" \
"v_mfma_f32_16x16x32_bf16 a[120:123], v[88:91], v[176:179], a[120:123]\n" \
"ds_read_b128 v[84:87], v49 offset:4096\n" \
"v_mfma_f32_16x16x32_bf16 a[120:123], v[92:95], v[180:183], a[120:123]\n" \
"global_load_lds_dwordx4 v39, s[54:55] offset:3072\n" \
"s_add_u32 s68, s67, 0x300000\n" \
"v_mfma_f32_16x16x32_bf16 a[124:127], v[88:91], v[184:187], a[124:127]\n" \
"s_add_u32 s64, s12, s68\n" \
"s_addc_u32 s65, s13, 0\n" \
"ds_read_b64_tr_b16 v[146:147], v50 offset:4608\n" \
"v_mfma_f32_16x16x32_bf16 a[124:127], v[92:95], v[188:191], a[124:127]\n" \
"v_mfma_f32_16x16x32_bf16 a[144:147], v[96:99], v[160:163], a[144:147]\n" \
"ds_read_b128 v[88:91], v48 offset:6144\n" \
"v_mfma_f32_16x16x32_bf16 a[144:147], v[100:103], v[164:167], a[144:147]\n" \
"global_load_dword v253, v248, s[64:65]\n" \
"v_mfma_f32_16x16x32_bf16 a[148:151], v[96:99], v[168:171], a[148:151]\n" \
"s_add_u32 s68, s67, 0x400000\n" \
"ds_read_b64_tr_b16 v[148:149], v50 offset:16896\n" \
"v_mfma_f32_16x16x32_bf16 a[148:151], v[100:103], v[172:175], a[148:151]\n" \
"s_add_u32 s64, s12, s68\n" \
"s_addc_u32 s65, s13, 0\n" \
"v_mfma_f32_16x16x32_bf16 a[152:155], v[96:99], v[176:179], a[152:155]\n" \
"ds_read_b128 v[92:95], v49 offset:6144\n" \
"v_mfma_f32_16x16x32_bf16 a[152:155], v[100:103], v[180:183], a[152:155]\n" \
"global_load_dword v253, v248, s[64:65]\n" \
"v_mfma_f32_16x16x32_bf16 a[156:159], v[96:99], v[184:187], a[156:159]\n" \
"s_add_u32 s68, s67, 0x600000\n" \
"ds_read_b64_tr_b16 v[150:151], v50 offset:20992\n" \
"v_mfma_f32_16x16x32_bf16 a[156:159], v[100:103], v[188:191], a[156:159]\n" \
"s_add_u32 s64, s12, s68\n" \
"s_addc_u32 s65, s13, 0\n" \
"v_mfma_f32_16x16x32_bf16 a[176:179], v[104:107], v[160:163], a[176:179]\n" \
"ds_read_b128 v[96:99], v48 offset:8192\n" \
"v_mfma_f32_16x16x32_bf16 a[176:179], v[108:111], v[164:167], a[176:179]\n" \
"global_load_dword v253, v248, s[64:65]\n" \
"v_mfma_f32_16x16x32_bf16 a[180:183], v[104:107], v[168:171], a[180:183]\n" \
"ds_read_b64_tr_b16 v[152:153], v51 offset:512\n" \
"v_mfma_f32_16x16x32_bf16 a[180:183], v[108:111], v[172:175], a[180:183]\n" \
"v_mfma_f32_16x16x32_bf16 a[184:187], v[104:107], v[176:179], a[184:187]\n" \
"ds_read_b128 v[100:103], v49 offset:8192\n" \
"v_mfma_f32_16x16x32_bf16 a[184:187], v[108:111], v[180:183], a[184:187]\n" \
"v_mfma_f32_16x16x32_bf16 a[188:191], v[104:107], v[184:187], a[188:191]\n" \
"ds_read_b64_tr_b16 v[154:155], v51 offset:4608\n" \
"v_mfma_f32_16x16x32_bf16 a[188:191], v[108:111], v[188:191], a[188:191]\n" \
"v_mfma_f32_16x16x32_bf16 a[208:211], v[112:115], v[160:163], a[208:211]\n" \
"ds_read_b128 v[104:107], v48 offset:10240\n" \
"v_mfma_f32_16x16x32_bf16 a[208:211], v[116:119], v[164:167], a[208:211]\n" \
"v_mfma_f32_16x16x32_bf16 a[212:215], v[112:115], v[168:171], a[212:215]\n" \
"ds_read_b64_tr_b16 v[156:157], v51 offset:16896\n" \
"v_mfma_f32_16x16x32_bf16 a[212:215], v[116:119], v[172:175], a[212:215]\n" \
"v_mfma_f32_16x16x32_bf16 a[216:219], v[112:115], v[176:179], a[216:219]\n" \
"ds_read_b128 v[108:111], v49 offset:10240\n" \
"v_mfma_f32_16x16x32_bf16 a[216:219], v[116:119], v[180:183], a[216:219]\n" \
"v_mfma_f32_16x16x32_bf16 a[220:223], v[112:115], v[184:187], a[220:223]\n" \
"ds_read_b64_tr_b16 v[158:159], v51 offset:20992\n" \
"v_mfma_f32_16x16x32_bf16 a[220:223], v[116:119], v[188:191], a[220:223]\n" \
"v_mfma_f32_16x16x32_bf16 a[240:243], v[120:123], v[160:163], a[240:243]\n" \
"ds_read_b128 v[112:115], v48 offset:12288\n" \
"v_mfma_f32_16x16x32_bf16 a[240:243], v[124:127], v[164:167], a[240:243]\n" \
"v_mfma_f32_16x16x32_bf16 a[244:247], v[120:123], v[168:171], a[244:247]\n" \
"v_mfma_f32_16x16x32_bf16 a[244:247], v[124:127], v[172:175], a[244:247]\n" \
"v_mfma_f32_16x16x32_bf16 a[248:251], v[120:123], v[176:179], a[248:251]\n" \
"ds_read_b128 v[116:119], v49 offset:12288\n" \
"v_mfma_f32_16x16x32_bf16 a[248:251], v[124:127], v[180:183], a[248:251]\n" \
"v_mfma_f32_16x16x32_bf16 a[252:255], v[120:123], v[184:187], a[252:255]\n" \
"v_mfma_f32_16x16x32_bf16 a[252:255], v[124:127], v[188:191], a[252:255]\n" \
"s_waitcnt lgkmcnt(1)\n" \
"v_mfma_f32_16x16x32_bf16 a[0:3], v[128:131], v[64:67], a[0:3]\n" \
"ds_read_b128 v[120:123], v48 offset:14336\n" \
"v_mfma_f32_16x16x32_bf16 a[0:3], v[132:135], v[68:71], a[0:3]\n" \
"v_mfma_f32_16x16x32_bf16 a[4:7], v[136:139], v[64:67], a[4:7]\n" \
"ds_read_b128 v[124:127], v49 offset:14336\n" \
"s_add_u32 m0, s95, 4096\n" \
"v_mfma_f32_16x16x32_bf16 a[4:7], v[140:143], v[68:71], a[4:7]\n" \
"global_load_lds_dwordx4 v228, s[44:45] offset:-4096\n" \
"v_mfma_f32_16x16x32_bf16 a[8:11], v[144:147], v[64:67], a[8:11]\n" \
"ds_read_b64_tr_b16 v[160:161], v50 offset:1024\n" \
"v_mfma_f32_16x16x32_bf16 a[8:11], v[148:151], v[68:71], a[8:11]\n" \
"v_mfma_f32_16x16x32_bf16 a[12:15], v[152:155], v[64:67], a[12:15]\n" \
"ds_read_b64_tr_b16 v[162:163], v50 offset:5120\n" \
"v_mfma_f32_16x16x32_bf16 a[12:15], v[156:159], v[68:71], a[12:15]\n" \
"global_load_lds_dwordx4 v229, s[44:45] offset:-3072\n" \
"v_mfma_f32_16x16x32_bf16 a[32:35], v[128:131], v[72:75], a[32:35]\n" \
"s_add_u32 s44, s44, 0x180000\n" \
"s_addc_u32 s45, s45, 0\n" \
"ds_read_b64_tr_b16 v[164:165], v50 offset:17408\n" \
"v_mfma_f32_16x16x32_bf16 a[32:35], v[132:135], v[76:79], a[32:35]\n" \
"v_mfma_f32_16x16x32_bf16 a[36:39], v[136:139], v[72:75], a[36:39]\n" \
"ds_read_b64_tr_b16 v[166:167], v50 offset:21504\n" \
"v_mfma_f32_16x16x32_bf16 a[36:39], v[140:143], v[76:79], a[36:39]\n" \
"global_load_lds_dwordx4 v228, s[42:43] offset:-2048\n" \
"v_mfma_f32_16x16x32_bf16 a[40:43], v[144:147], v[72:75], a[40:43]\n" \
"ds_read_b64_tr_b16 v[168:169], v51 offset:1024\n" \
"v_mfma_f32_16x16x32_bf16 a[40:43], v[148:151], v[76:79], a[40:43]\n" \
"v_mfma_f32_16x16x32_bf16 a[44:47], v[152:155], v[72:75], a[44:47]\n" \
"ds_read_b64_tr_b16 v[170:171], v51 offset:5120\n" \
"v_mfma_f32_16x16x32_bf16 a[44:47], v[156:159], v[76:79], a[44:47]\n" \
"global_load_lds_dwordx4 v229, s[42:43] offset:-1024\n" \
"v_mfma_f32_16x16x32_bf16 a[64:67], v[128:131], v[80:83], a[64:67]\n" \
"s_add_u32 s42, s42, 0x180000\n" \
"s_addc_u32 s43, s43, 0\n" \
"ds_read_b64_tr_b16 v[172:173], v51 offset:17408\n" \
"v_mfma_f32_16x16x32_bf16 a[64:67], v[132:135], v[84:87], a[64:67]\n" \
"v_mfma_f32_16x16x32_bf16 a[68:71], v[136:139], v[80:83], a[68:71]\n" \
"s_add_u32 m0, s95, 20480\n" \
"ds_read_b64_tr_b16 v[174:175], v51 offset:21504\n" \
"v_mfma_f32_16x16x32_bf16 a[68:71], v[140:143], v[84:87], a[68:71]\n" \
"global_load_lds_dwordx4 v228, s[44:45] offset:-4096\n" \
"v_mfma_f32_16x16x32_bf16 a[72:75], v[144:147], v[80:83], a[72:75]\n" \
"ds_read_b64_tr_b16 v[176:177], v50 offset:1536\n" \
"v_mfma_f32_16x16x32_bf16 a[72:75], v[148:151], v[84:87], a[72:75]\n" \
"v_mfma_f32_16x16x32_bf16 a[76:79], v[152:155], v[80:83], a[76:79]\n" \
"ds_read_b64_tr_b16 v[178:179], v50 offset:5632\n" \
"v_mfma_f32_16x16x32_bf16 a[76:79], v[156:159], v[84:87], a[76:79]\n" \
"global_load_lds_dwordx4 v229, s[44:45] offset:-3072\n" \
"v_mfma_f32_16x16x32_bf16 a[96:99], v[128:131], v[88:91], a[96:99]\n" \
"s_add_u32 s44, s44, 0x400000\n" \
"s_addc_u32 s45, s45, 0\n" \
"ds_read_b64_tr_b16 v[180:181], v50 offset:17920\n" \
"v_mfma_f32_16x16x32_bf16 a[96:99], v[132:135], v[92:95], a[96:99]\n" \
"v_mfma_f32_16x16x32_bf16 a[100:103], v[136:139], v[88:91], a[100:103]\n" \
"ds_read_b64_tr_b16 v[182:183], v50 offset:22016\n" \
"v_mfma_f32_16x16x32_bf16 a[100:103], v[140:143], v[92:95], a[100:103]\n" \
"global_load_lds_dwordx4 v228, s[42:43] offset:-2048\n" \
"v_mfma_f32_16x16x32_bf16 a[104:107], v[144:147], v[88:91], a[104:107]\n" \
"ds_read_b64_tr_b16 v[184:185], v51 offset:1536\n" \
"v_mfma_f32_16x16x32_bf16 a[104:107], v[148:151], v[92:95], a[104:107]\n" \
"Lpg1644_%=:\n" \
"Lst0x2_%=:\n" \
"s_cbranch_execz Lpg3809_%=\n" \
"v_mfma_f32_16x16x32_bf16 a[108:111], v[152:155], v[88:91], a[108:111]\n" \
"ds_read_b64_tr_b16 v[186:187], v51 offset:5632\n" \
"v_mfma_f32_16x16x32_bf16 a[108:111], v[156:159], v[92:95], a[108:111]\n" \
"global_load_lds_dwordx4 v229, s[42:43] offset:-1024\n" \
"v_mfma_f32_16x16x32_bf16 a[128:131], v[128:131], v[96:99], a[128:131]\n" \
"s_add_u32 s42, s42, 0x400000\n" \
"s_addc_u32 s43, s43, 0\n" \
"ds_read_b64_tr_b16 v[188:189], v51 offset:17920\n" \
"v_mfma_f32_16x16x32_bf16 a[128:131], v[132:135], v[100:103], a[128:131]\n" \
"v_mfma_f32_16x16x32_bf16 a[132:135], v[136:139], v[96:99], a[132:135]\n" \
"ds_read_b64_tr_b16 v[190:191], v51 offset:22016\n" \
"v_mfma_f32_16x16x32_bf16 a[132:135], v[140:143], v[100:103], a[132:135]\n" \
"v_mfma_f32_16x16x32_bf16 a[136:139], v[144:147], v[96:99], a[136:139]\n" \
"v_mfma_f32_16x16x32_bf16 a[136:139], v[148:151], v[100:103], a[136:139]\n" \
"v_mfma_f32_16x16x32_bf16 a[140:143], v[152:155], v[96:99], a[140:143]\n" \
"v_mfma_f32_16x16x32_bf16 a[140:143], v[156:159], v[100:103], a[140:143]\n" \
"v_mfma_f32_16x16x32_bf16 a[160:163], v[128:131], v[104:107], a[160:163]\n" \
"v_add_u32 v48, 0xffff8000, v48\n" \
"v_add_u32 v49, 0xffff8000, v49\n" \
"v_mfma_f32_16x16x32_bf16 a[160:163], v[132:135], v[108:111], a[160:163]\n" \
"v_add_u32 v50, 0xffff8000, v50\n" \
"v_add_u32 v51, 0xffff8000, v51\n" \
"v_mfma_f32_16x16x32_bf16 a[164:167], v[136:139], v[104:107], a[164:167]\n" \
"v_mfma_f32_16x16x32_bf16 a[164:167], v[140:143], v[108:111], a[164:167]\n" \
"v_mfma_f32_16x16x32_bf16 a[168:171], v[144:147], v[104:107], a[168:171]\n" \
"v_mfma_f32_16x16x32_bf16 a[168:171], v[148:151], v[108:111], a[168:171]\n" \
"v_mfma_f32_16x16x32_bf16 a[172:175], v[152:155], v[104:107], a[172:175]\n" \
"v_mfma_f32_16x16x32_bf16 a[172:175], v[156:159], v[108:111], a[172:175]\n" \
"s_waitcnt lgkmcnt(0)\n" \
"v_mfma_f32_16x16x32_bf16 a[192:195], v[128:131], v[112:115], a[192:195]\n" \
"v_mfma_f32_16x16x32_bf16 a[192:195], v[132:135], v[116:119], a[192:195]\n" \
"v_mfma_f32_16x16x32_bf16 a[196:199], v[136:139], v[112:115], a[196:199]\n" \
"v_mfma_f32_16x16x32_bf16 a[196:199], v[140:143], v[116:119], a[196:199]\n" \
"v_mfma_f32_16x16x32_bf16 a[200:203], v[144:147], v[112:115], a[200:203]\n" \
"v_mfma_f32_16x16x32_bf16 a[200:203], v[148:151], v[116:119], a[200:203]\n" \
"v_mfma_f32_16x16x32_bf16 a[204:207], v[152:155], v[112:115], a[204:207]\n" \
"v_mfma_f32_16x16x32_bf16 a[204:207], v[156:159], v[116:119], a[204:207]\n" \
"v_mfma_f32_16x16x32_bf16 a[224:227], v[128:131], v[120:123], a[224:227]\n" \
"v_mfma_f32_16x16x32_bf16 a[224:227], v[132:135], v[124:127], a[224:227]\n" \
"v_mfma_f32_16x16x32_bf16 a[228:231], v[136:139], v[120:123], a[228:231]\n" \
"v_mfma_f32_16x16x32_bf16 a[228:231], v[140:143], v[124:127], a[228:231]\n" \
"v_mfma_f32_16x16x32_bf16 a[232:235], v[144:147], v[120:123], a[232:235]\n" \
"v_mfma_f32_16x16x32_bf16 a[232:235], v[148:151], v[124:127], a[232:235]\n" \
"v_mfma_f32_16x16x32_bf16 a[236:239], v[152:155], v[120:123], a[236:239]\n" \
"s_waitcnt vmcnt(11) lgkmcnt(0)\n" \
"v_mfma_f32_16x16x32_bf16 a[236:239], v[156:159], v[124:127], a[236:239]\n" \
"s_barrier\n" \
"v_mfma_f32_16x16x32_bf16 a[16:19], v[64:67], v[160:163], a[16:19]\n" \
"s_add_u32 m0, s87, 135168\n" \
"ds_read_b64_tr_b16 v[128:129], v50 offset:0\n" \
"v_mfma_f32_16x16x32_bf16 a[16:19], v[68:71], v[164:167], a[16:19]\n" \
"global_load_lds_dwordx4 v224, s[48:49] offset:-4096\n" \
"v_mfma_f32_16x16x32_bf16 a[20:23], v[64:67], v[168:171], a[20:23]\n" \
"ds_read_b64_tr_b16 v[130:131], v50 offset:4096\n" \
"v_mfma_f32_16x16x32_bf16 a[20:23], v[68:71], v[172:175], a[20:23]\n" \
"v_mfma_f32_16x16x32_bf16 a[24:27], v[64:67], v[176:179], a[24:27]\n" \
"ds_read_b64_tr_b16 v[132:133], v50 offset:16384\n" \
"v_mfma_f32_16x16x32_bf16 a[24:27], v[68:71], v[180:183], a[24:27]\n" \
"global_load_lds_dwordx4 v225, s[48:49] offset:-3072\n" \
"v_mfma_f32_16x16x32_bf16 a[28:31], v[64:67], v[184:187], a[28:31]\n" \
"ds_read_b64_tr_b16 v[134:135], v50 offset:20480\n" \
"v_mfma_f32_16x16x32_bf16 a[28:31], v[68:71], v[188:191], a[28:31]\n" \
"v_mfma_f32_16x16x32_bf16 a[48:51], v[72:75], v[160:163], a[48:51]\n" \
"ds_read_b128 v[64:67], v48 offset:0\n" \
"v_mfma_f32_16x16x32_bf16 a[48:51], v[76:79], v[164:167], a[48:51]\n" \
"global_load_lds_dwordx4 v226, s[48:49] offset:-2048\n" \
"v_mfma_f32_16x16x32_bf16 a[52:55], v[72:75], v[168:171], a[52:55]\n" \
"ds_read_b64_tr_b16 v[136:137], v51 offset:0\n" \
"v_mfma_f32_16x16x32_bf16 a[52:55], v[76:79], v[172:175], a[52:55]\n" \
"v_mfma_f32_16x16x32_bf16 a[56:59], v[72:75], v[176:179], a[56:59]\n" \
"ds_read_b128 v[68:71], v49 offset:0\n" \
"v_mfma_f32_16x16x32_bf16 a[56:59], v[76:79], v[180:183], a[56:59]\n" \
"global_load_lds_dwordx4 v227, s[48:49] offset:-1024\n" \
"v_mfma_f32_16x16x32_bf16 a[60:63], v[72:75], v[184:187], a[60:63]\n" \
"s_add_u32 s48, s48, 0x180\n" \
"s_addc_u32 s49, s49, 0\n" \
"ds_read_b64_tr_b16 v[138:139], v51 offset:4096\n" \
"v_mfma_f32_16x16x32_bf16 a[60:63], v[76:79], v[188:191], a[60:63]\n" \
"v_mfma_f32_16x16x32_bf16 a[80:83], v[80:83], v[160:163], a[80:83]\n" \
"ds_read_b128 v[72:75], v48 offset:2048\n" \
"v_mfma_f32_16x16x32_bf16 a[80:83], v[84:87], v[164:167], a[80:83]\n" \
"global_load_lds_dwordx4 v224, s[46:47] offset:0\n" \
"v_mfma_f32_16x16x32_bf16 a[84:87], v[80:83], v[168:171], a[84:87]\n" \
"ds_read_b64_tr_b16 v[140:141], v51 offset:16384\n" \
"v_mfma_f32_16x16x32_bf16 a[84:87], v[84:87], v[172:175], a[84:87]\n" \
"v_mfma_f32_16x16x32_bf16 a[88:91], v[80:83], v[176:179], a[88:91]\n" \
"ds_read_b128 v[76:79], v49 offset:2048\n" \
"v_mfma_f32_16x16x32_bf16 a[88:91], v[84:87], v[180:183], a[88:91]\n" \
"global_load_lds_dwordx4 v225, s[46:47] offset:1024\n" \
"v_mfma_f32_16x16x32_bf16 a[92:95], v[80:83], v[184:187], a[92:95]\n" \
"ds_read_b64_tr_b16 v[142:143], v51 offset:20480\n" \
"v_mfma_f32_16x16x32_bf16 a[92:95], v[84:87], v[188:191], a[92:95]\n" \
"v_mfma_f32_16x16x32_bf16 a[112:115], v[88:91], v[160:163], a[112:115]\n" \
"ds_read_b128 v[80:83], v48 offset:4096\n" \
"v_mfma_f32_16x16x32_bf16 a[112:115], v[92:95], v[164:167], a[112:115]\n" \
"global_load_lds_dwordx4 v226, s[46:47] offset:2048\n" \
"v_mfma_f32_16x16x32_bf16 a[116:119], v[88:91], v[168:171], a[116:119]\n" \
"ds_read_b64_tr_b16 v[144:145], v50 offset:512\n" \
"v_mfma_f32_16x16x32_bf16 a[116:119], v[92:95], v[172:175], a[116:119]\n" \
"v_mfma_f32_16x16x32_bf16 a[120:123], v[88:91], v[176:179], a[120:123]\n" \
"ds_read_b128 v[84:87], v49 offset:4096\n" \
"v_mfma_f32_16x16x32_bf16 a[120:123], v[92:95], v[180:183], a[120:123]\n" \
"global_load_lds_dwordx4 v227, s[46:47] offset:3072\n" \
"v_mfma_f32_16x16x32_bf16 a[124:127], v[88:91], v[184:187], a[124:127]\n" \
"s_add_u32 s46, s46, 0x180\n" \
"s_addc_u32 s47, s47, 0\n" \
"ds_read_b64_tr_b16 v[146:147], v50 offset:4608\n" \
"v_mfma_f32_16x16x32_bf16 a[124:127], v[92:95], v[188:191], a[124:127]\n" \
"s_add_u32 s68, s67, 0x700000\n" \
"s_add_u32 s64, s12, s68\n" \
"s_addc_u32 s65, s13, 0\n" \
"v_mfma_f32_16x16x32_bf16 a[144:147], v[96:99], v[160:163], a[144:147]\n" \
"ds_read_b128 v[88:91], v48 offset:6144\n" \
"v_mfma_f32_16x16x32_bf16 a[144:147], v[100:103], v[164:167], a[144:147]\n" \
"global_load_dword v253, v248, s[64:65]\n" \
"v_mfma_f32_16x16x32_bf16 a[148:151], v[96:99], v[168:171], a[148:151]\n" \
"s_lshr_b32 s68, s66, 2\n" \
"s_and_b32 s68, s68, 0xffff\n" \
"ds_read_b64_tr_b16 v[148:149], v50 offset:16896\n" \
"v_mfma_f32_16x16x32_bf16 a[148:151], v[100:103], v[172:175], a[148:151]\n" \
"s_add_u32 s68, s68, 65585152\n" \
"v_mfma_f32_16x16x32_bf16 a[152:155], v[96:99], v[176:179], a[152:155]\n" \
"ds_read_b128 v[92:95], v49 offset:6144\n" \
"v_mfma_f32_16x16x32_bf16 a[152:155], v[100:103], v[180:183], a[152:155]\n" \
"buffer_load_dword v253, v245, s[56:59], s68 offen sc1\n" \
"v_mfma_f32_16x16x32_bf16 a[156:159], v[96:99], v[184:187], a[156:159]\n" \
"ds_read_b64_tr_b16 v[150:151], v50 offset:20992\n" \
"v_mfma_f32_16x16x32_bf16 a[156:159], v[100:103], v[188:191], a[156:159]\n" \
"v_mfma_f32_16x16x32_bf16 a[176:179], v[104:107], v[160:163], a[176:179]\n" \
"ds_read_b128 v[96:99], v48 offset:8192\n" \
"v_mfma_f32_16x16x32_bf16 a[176:179], v[108:111], v[164:167], a[176:179]\n" \
"v_mfma_f32_16x16x32_bf16 a[180:183], v[104:107], v[168:171], a[180:183]\n" \
"ds_read_b64_tr_b16 v[152:153], v51 offset:512\n" \
"v_mfma_f32_16x16x32_bf16 a[180:183], v[108:111], v[172:175], a[180:183]\n" \
"v_mfma_f32_16x16x32_bf16 a[184:187], v[104:107], v[176:179], a[184:187]\n" \
"ds_read_b128 v[100:103], v49 offset:8192\n" \
"v_mfma_f32_16x16x32_bf16 a[184:187], v[108:111], v[180:183], a[184:187]\n" \
"v_mfma_f32_16x16x32_bf16 a[188:191], v[104:107], v[184:187], a[188:191]\n" \
"ds_read_b64_tr_b16 v[154:155], v51 offset:4608\n" \
"v_mfma_f32_16x16x32_bf16 a[188:191], v[108:111], v[188:191], a[188:191]\n" \
"v_mfma_f32_16x16x32_bf16 a[208:211], v[112:115], v[160:163], a[208:211]\n" \
"ds_read_b128 v[104:107], v48 offset:10240\n" \
"v_mfma_f32_16x16x32_bf16 a[208:211], v[116:119], v[164:167], a[208:211]\n" \
"v_mfma_f32_16x16x32_bf16 a[212:215], v[112:115], v[168:171], a[212:215]\n" \
"ds_read_b64_tr_b16 v[156:157], v51 offset:16896\n" \
"v_mfma_f32_16x16x32_bf16 a[212:215], v[116:119], v[172:175], a[212:215]\n" \
"v_mfma_f32_16x16x32_bf16 a[216:219], v[112:115], v[176:179], a[216:219]\n" \
"ds_read_b128 v[108:111], v49 offset:10240\n" \
"v_mfma_f32_16x16x32_bf16 a[216:219], v[116:119], v[180:183], a[216:219]\n" \
"v_mfma_f32_16x16x32_bf16 a[220:223], v[112:115], v[184:187], a[220:223]\n" \
"ds_read_b64_tr_b16 v[158:159], v51 offset:20992\n" \
"v_mfma_f32_16x16x32_bf16 a[220:223], v[116:119], v[188:191], a[220:223]\n" \
"v_mfma_f32_16x16x32_bf16 a[240:243], v[120:123], v[160:163], a[240:243]\n" \
"ds_read_b128 v[112:115], v48 offset:12288\n" \
"v_mfma_f32_16x16x32_bf16 a[240:243], v[124:127], v[164:167], a[240:243]\n" \
"v_mfma_f32_16x16x32_bf16 a[244:247], v[120:123], v[168:171], a[244:247]\n" \
"v_mfma_f32_16x16x32_bf16 a[244:247], v[124:127], v[172:175], a[244:247]\n" \
"v_mfma_f32_16x16x32_bf16 a[248:251], v[120:123], v[176:179], a[248:251]\n" \
"ds_read_b128 v[116:119], v49 offset:12288\n" \
"v_mfma_f32_16x16x32_bf16 a[248:251], v[124:127], v[180:183], a[248:251]\n" \
"v_mfma_f32_16x16x32_bf16 a[252:255], v[120:123], v[184:187], a[252:255]\n" \
"v_mfma_f32_16x16x32_bf16 a[252:255], v[124:127], v[188:191], a[252:255]\n" \
"s_waitcnt lgkmcnt(1)\n" \
"v_mfma_f32_16x16x32_bf16 a[0:3], v[128:131], v[64:67], a[0:3]\n" \
"ds_read_b128 v[120:123], v48 offset:14336\n" \
"v_mfma_f32_16x16x32_bf16 a[0:3], v[132:135], v[68:71], a[0:3]\n" \
"v_mfma_f32_16x16x32_bf16 a[4:7], v[136:139], v[64:67], a[4:7]\n" \
"ds_read_b128 v[124:127], v49 offset:14336\n" \
"s_add_u32 m0, s87, 69632\n" \
"v_mfma_f32_16x16x32_bf16 a[4:7], v[140:143], v[68:71], a[4:7]\n" \
"global_load_lds_dwordx4 v224, s[48:49] offset:-4096\n" \
"v_mfma_f32_16x16x32_bf16 a[8:11], v[144:147], v[64:67], a[8:11]\n" \
"ds_read_b64_tr_b16 v[160:161], v50 offset:1024\n" \
"v_mfma_f32_16x16x32_bf16 a[8:11], v[148:151], v[68:71], a[8:11]\n" \
"v_mfma_f32_16x16x32_bf16 a[12:15], v[152:155], v[64:67], a[12:15]\n" \
"ds_read_b64_tr_b16 v[162:163], v50 offset:5120\n" \
"v_mfma_f32_16x16x32_bf16 a[12:15], v[156:159], v[68:71], a[12:15]\n" \
"global_load_lds_dwordx4 v225, s[48:49] offset:-3072\n" \
"v_mfma_f32_16x16x32_bf16 a[32:35], v[128:131], v[72:75], a[32:35]\n" \
"ds_read_b64_tr_b16 v[164:165], v50 offset:17408\n" \
"v_mfma_f32_16x16x32_bf16 a[32:35], v[132:135], v[76:79], a[32:35]\n" \
"v_mfma_f32_16x16x32_bf16 a[36:39], v[136:139], v[72:75], a[36:39]\n" \
"ds_read_b64_tr_b16 v[166:167], v50 offset:21504\n" \
"v_mfma_f32_16x16x32_bf16 a[36:39], v[140:143], v[76:79], a[36:39]\n" \
"global_load_lds_dwordx4 v226, s[48:49] offset:-2048\n" \
"v_mfma_f32_16x16x32_bf16 a[40:43], v[144:147], v[72:75], a[40:43]\n" \
"ds_read_b64_tr_b16 v[168:169], v51 offset:1024\n" \
"v_mfma_f32_16x16x32_bf16 a[40:43], v[148:151], v[76:79], a[40:43]\n" \
"v_mfma_f32_16x16x32_bf16 a[44:47], v[152:155], v[72:75], a[44:47]\n" \
"ds_read_b64_tr_b16 v[170:171], v51 offset:5120\n" \
"v_mfma_f32_16x16x32_bf16 a[44:47], v[156:159], v[76:79], a[44:47]\n" \
"global_load_lds_dwordx4 v227, s[48:49] offset:-1024\n" \
"v_mfma_f32_16x16x32_bf16 a[64:67], v[128:131], v[80:83], a[64:67]\n" \
"s_add_u32 s48, s48, 0x400\n" \
"s_addc_u32 s49, s49, 0\n" \
"ds_read_b64_tr_b16 v[172:173], v51 offset:17408\n" \
"v_mfma_f32_16x16x32_bf16 a[64:67], v[132:135], v[84:87], a[64:67]\n" \
"v_mfma_f32_16x16x32_bf16 a[68:71], v[136:139], v[80:83], a[68:71]\n" \
"ds_read_b64_tr_b16 v[174:175], v51 offset:21504\n" \
"v_mfma_f32_16x16x32_bf16 a[68:71], v[140:143], v[84:87], a[68:71]\n" \
"global_load_lds_dwordx4 v224, s[46:47] offset:0\n" \
"v_mfma_f32_16x16x32_bf16 a[72:75], v[144:147], v[80:83], a[72:75]\n" \
"ds_read_b64_tr_b16 v[176:177], v50 offset:1536\n" \
"v_mfma_f32_16x16x32_bf16 a[72:75], v[148:151], v[84:87], a[72:75]\n" \
"v_mfma_f32_16x16x32_bf16 a[76:79], v[152:155], v[80:83], a[76:79]\n" \
"ds_read_b64_tr_b16 v[178:179], v50 offset:5632\n" \
"v_mfma_f32_16x16x32_bf16 a[76:79], v[156:159], v[84:87], a[76:79]\n" \
"global_load_lds_dwordx4 v225, s[46:47] offset:1024\n" \
"v_mfma_f32_16x16x32_bf16 a[96:99], v[128:131], v[88:91], a[96:99]\n" \
"ds_read_b64_tr_b16 v[180:181], v50 offset:17920\n" \
"v_mfma_f32_16x16x32_bf16 a[96:99], v[132:135], v[92:95], a[96:99]\n" \
"v_mfma_f32_16x16x32_bf16 a[100:103], v[136:139], v[88:91], a[100:103]\n" \
"ds_read_b64_tr_b16 v[182:183], v50 offset:22016\n" \
"v_mfma_f32_16x16x32_bf16 a[100:103], v[140:143], v[92:95], a[100:103]\n" \
"global_load_lds_dwordx4 v226, s[46:47] offset:2048\n" \
"v_mfma_f32_16x16x32_bf16 a[104:107], v[144:147], v[88:91], a[104:107]\n" \
"ds_read_b64_tr_b16 v[184:185], v51 offset:1536\n" \
"v_mfma_f32_16x16x32_bf16 a[104:107], v[148:151], v[92:95], a[104:107]\n" \
"v_mfma_f32_16x16x32_bf16 a[108:111], v[152:155], v[88:91], a[108:111]\n" \
"ds_read_b64_tr_b16 v[186:187], v51 offset:5632\n" \
"v_mfma_f32_16x16x32_bf16 a[108:111], v[156:159], v[92:95], a[108:111]\n" \
"global_load_lds_dwordx4 v227, s[46:47] offset:3072\n" \
"v_mfma_f32_16x16x32_bf16 a[128:131], v[128:131], v[96:99], a[128:131]\n" \
"s_add_u32 s46, s46, 0x400\n" \
"s_addc_u32 s47, s47, 0\n" \
"ds_read_b64_tr_b16 v[188:189], v51 offset:17920\n" \
"v_mfma_f32_16x16x32_bf16 a[128:131], v[132:135], v[100:103], a[128:131]\n" \
"v_mfma_f32_16x16x32_bf16 a[132:135], v[136:139], v[96:99], a[132:135]\n" \
"s_getreg_b32 s93, hwreg(HW_REG_XCC_ID)\n" \
"s_and_b32 s93, s93, 1\n" \
"ds_read_b64_tr_b16 v[190:191], v51 offset:22016\n" \
"v_mfma_f32_16x16x32_bf16 a[132:135], v[140:143], v[100:103], a[132:135]\n" \
"s_lshl_b32 s93, s93, 2\n" \
"s_add_u32 s93, s93, 65593856\n" \
"v_mfma_f32_16x16x32_bf16 a[136:139], v[144:147], v[96:99], a[136:139]\n" \
"s_cmp_eq_u32 %1, 0\n" \
"s_cselect_b32 s93, s93, 0x20000000\n" \
"v_mov_b32 v238, s60\n" \
"v_mfma_f32_16x16x32_bf16 a[136:139], v[148:151], v[100:103], a[136:139]\n" \
"v_lshlrev_b32 v239, 28, s61\n" \
"v_mfma_f32_16x16x32_bf16 a[140:143], v[152:155], v[96:99], a[140:143]\n" \
"buffer_store_dword v238, v239, s[56:59], s93 offen sc1\n" \
"v_mfma_f32_16x16x32_bf16 a[140:143], v[156:159], v[100:103], a[140:143]\n" \
"s_mov_b32 s93, 65593856\n" \
"v_mfma_f32_16x16x32_bf16 a[160:163], v[128:131], v[104:107], a[160:163]\n" \
"v_mfma_f32_16x16x32_bf16 a[160:163], v[132:135], v[108:111], a[160:163]\n" \
"v_mfma_f32_16x16x32_bf16 a[164:167], v[136:139], v[104:107], a[164:167]\n" \
"v_mfma_f32_16x16x32_bf16 a[164:167], v[140:143], v[108:111], a[164:167]\n" \
"v_mfma_f32_16x16x32_bf16 a[168:171], v[144:147], v[104:107], a[168:171]\n" \
"v_mfma_f32_16x16x32_bf16 a[168:171], v[148:151], v[108:111], a[168:171]\n" \
"v_mfma_f32_16x16x32_bf16 a[172:175], v[152:155], v[104:107], a[172:175]\n" \
"v_mfma_f32_16x16x32_bf16 a[172:175], v[156:159], v[108:111], a[172:175]\n" \
"s_waitcnt lgkmcnt(0)\n" \
"v_mfma_f32_16x16x32_bf16 a[192:195], v[128:131], v[112:115], a[192:195]\n" \
"v_mfma_f32_16x16x32_bf16 a[192:195], v[132:135], v[116:119], a[192:195]\n" \
"v_mfma_f32_16x16x32_bf16 a[196:199], v[136:139], v[112:115], a[196:199]\n" \
"v_mfma_f32_16x16x32_bf16 a[196:199], v[140:143], v[116:119], a[196:199]\n" \
"v_mfma_f32_16x16x32_bf16 a[200:203], v[144:147], v[112:115], a[200:203]\n" \
"v_mfma_f32_16x16x32_bf16 a[200:203], v[148:151], v[116:119], a[200:203]\n" \
"v_mfma_f32_16x16x32_bf16 a[204:207], v[152:155], v[112:115], a[204:207]\n" \
"v_mfma_f32_16x16x32_bf16 a[204:207], v[156:159], v[116:119], a[204:207]\n" \
"v_mfma_f32_16x16x32_bf16 a[224:227], v[128:131], v[120:123], a[224:227]\n" \
"v_mfma_f32_16x16x32_bf16 a[224:227], v[132:135], v[124:127], a[224:227]\n" \
"v_mfma_f32_16x16x32_bf16 a[228:231], v[136:139], v[120:123], a[228:231]\n" \
"v_mfma_f32_16x16x32_bf16 a[228:231], v[140:143], v[124:127], a[228:231]\n" \
"v_mfma_f32_16x16x32_bf16 a[232:235], v[144:147], v[120:123], a[232:235]\n" \
"v_mfma_f32_16x16x32_bf16 a[232:235], v[148:151], v[124:127], a[232:235]\n" \
"v_mfma_f32_16x16x32_bf16 a[236:239], v[152:155], v[120:123], a[236:239]\n" \
"s_waitcnt vmcnt(11) lgkmcnt(0)\n" \
"v_mfma_f32_16x16x32_bf16 a[236:239], v[156:159], v[124:127], a[236:239]\n" \
"s_barrier\n" \
"v_mfma_f32_16x16x32_bf16 a[16:19], v[64:67], v[160:163], a[16:19]\n" \
"s_add_u32 m0, s87, 102400\n" \
"v_mfma_f32_16x16x32_bf16 a[16:19], v[68:71], v[164:167], a[16:19]\n" \
"global_load_lds_dwordx4 v224, s[48:49] offset:-4096\n" \
"v_mfma_f32_16x16x32_bf16 a[20:23], v[64:67], v[168:171], a[20:23]\n" \
"ds_read_b64_tr_b16 v[0:1], v232 offset:0\n" \
"v_mfma_f32_16x16x32_bf16 a[20:23], v[68:71], v[172:175], a[20:23]\n" \
"v_accvgpr_read_b32 v234, a192\n" \
"v_accvgpr_read_b32 v235, a193\n" \
"v_mfma_f32_16x16x32_bf16 a[24:27], v[64:67], v[176:179], a[24:27]\n" \
"ds_read_b64_tr_b16 v[2:3], v232 offset:2048\n" \
"v_accvgpr_read_b32 v236, a194\n" \
"v_mfma_f32_16x16x32_bf16 a[24:27], v[68:71], v[180:183], a[24:27]\n" \
"global_load_lds_dwordx4 v225, s[48:49] offset:-3072\n" \
"v_accvgpr_read_b32 v237, a195\n" \
"v_mfma_f32_16x16x32_bf16 a[28:31], v[64:67], v[184:187], a[28:31]\n" \
"ds_read_b64_tr_b16 v[4:5], v232 offset:8192\n" \
"v_mul_f32 v234, s92, v234\n" \
"v_mfma_f32_16x16x32_bf16 a[28:31], v[68:71], v[188:191], a[28:31]\n" \
"v_mul_f32 v235, s92, v235\n" \
"v_mul_f32 v236, s92, v236\n" \
"v_mfma_f32_16x16x32_bf16 a[48:51], v[72:75], v[160:163], a[48:51]\n" \
"ds_read_b64_tr_b16 v[6:7], v232 offset:10240\n" \
"v_mul_f32 v237, s92, v237\n" \
"v_mfma_f32_16x16x32_bf16 a[48:51], v[76:79], v[164:167], a[48:51]\n" \
"global_load_lds_dwordx4 v226, s[48:49] offset:-2048\n" \
"v_cvt_pk_bf16_f32 v234, v234, v235\n" \
"v_mfma_f32_16x16x32_bf16 a[52:55], v[72:75], v[168:171], a[52:55]\n" \
"ds_read_b64_tr_b16 v[8:9], v233 offset:0\n" \
"v_cvt_pk_bf16_f32 v236, v236, v237\n" \
"v_mfma_f32_16x16x32_bf16 a[52:55], v[76:79], v[172:175], a[52:55]\n" \
"v_accvgpr_write_b32 a192, v234\n" \
"v_accvgpr_write_b32 a193, v236\n" \
"v_mfma_f32_16x16x32_bf16 a[56:59], v[72:75], v[176:179], a[56:59]\n" \
"ds_read_b64_tr_b16 v[10:11], v233 offset:2048\n" \
"v_accvgpr_read_b32 v234, a196\n" \
"v_mfma_f32_16x16x32_bf16 a[56:59], v[76:79], v[180:183], a[56:59]\n" \
"global_load_lds_dwordx4 v227, s[48:49] offset:-1024\n" \
"v_accvgpr_read_b32 v235, a197\n" \
"v_mfma_f32_16x16x32_bf16 a[60:63], v[72:75], v[184:187], a[60:63]\n" \
"s_add_u32 s48, s48, 0xfffffb00\n" \
"s_addc_u32 s49, s49, -1\n" \
"ds_read_b64_tr_b16 v[12:13], v233 offset:8192\n" \
"v_mfma_f32_16x16x32_bf16 a[60:63], v[76:79], v[188:191], a[60:63]\n" \
"v_accvgpr_read_b32 v236, a198\n" \
"v_accvgpr_read_b32 v237, a199\n" \
"v_mfma_f32_16x16x32_bf16 a[80:83], v[80:83], v[160:163], a[80:83]\n" \
"ds_read_b64_tr_b16 v[14:15], v233 offset:10240\n" \
"v_mul_f32 v234, s92, v234\n" \
"v_mfma_f32_16x16x32_bf16 a[80:83], v[84:87], v[164:167], a[80:83]\n" \
"global_load_lds_dwordx4 v224, s[46:47] offset:0\n" \
"v_mul_f32 v235, s92, v235\n" \
"v_mfma_f32_16x16x32_bf16 a[84:87], v[80:83], v[168:171], a[84:87]\n" \
"ds_read_b64_tr_b16 v[16:17], v232 offset:512\n" \
"v_mul_f32 v236, s92, v236\n" \
"v_mfma_f32_16x16x32_bf16 a[84:87], v[84:87], v[172:175], a[84:87]\n" \
"v_mul_f32 v237, s92, v237\n" \
"v_cvt_pk_bf16_f32 v234, v234, v235\n" \
"v_mfma_f32_16x16x32_bf16 a[88:91], v[80:83], v[176:179], a[88:91]\n" \
"ds_read_b64_tr_b16 v[18:19], v232 offset:2560\n" \
"v_cvt_pk_bf16_f32 v236, v236, v237\n" \
"v_mfma_f32_16x16x32_bf16 a[88:91], v[84:87], v[180:183], a[88:91]\n" \
"global_load_lds_dwordx4 v225, s[46:47] offset:1024\n" \
"v_accvgpr_write_b32 a194, v234\n" \
"v_mfma_f32_16x16x32_bf16 a[92:95], v[80:83], v[184:187], a[92:95]\n" \
"ds_read_b64_tr_b16 v[20:21], v232 offset:8704\n" \
"v_accvgpr_write_b32 a195, v236\n" \
"v_mfma_f32_16x16x32_bf16 a[92:95], v[84:87], v[188:191], a[92:95]\n" \
"v_accvgpr_read_b32 v234, a224\n" \
"v_accvgpr_read_b32 v235, a225\n" \
"v_mfma_f32_16x16x32_bf16 a[112:115], v[88:91], v[160:163], a[112:115]\n" \
"ds_read_b64_tr_b16 v[22:23], v232 offset:10752\n" \
"v_accvgpr_read_b32 v236, a226\n" \
"v_mfma_f32_16x16x32_bf16 a[112:115], v[92:95], v[164:167], a[112:115]\n" \
"global_load_lds_dwordx4 v226, s[46:47] offset:2048\n" \
"v_accvgpr_read_b32 v237, a227\n" \
"v_mfma_f32_16x16x32_bf16 a[116:119], v[88:91], v[168:171], a[116:119]\n" \
"ds_read_b64_tr_b16 v[24:25], v233 offset:512\n" \
"v_mul_f32 v234, s92, v234\n" \
"v_mfma_f32_16x16x32_bf16 a[116:119], v[92:95], v[172:175], a[116:119]\n" \
"v_mul_f32 v235, s92, v235\n" \
"v_mul_f32 v236, s92, v236\n" \
"v_mfma_f32_16x16x32_bf16 a[120:123], v[88:91], v[176:179], a[120:123]\n" \
"ds_read_b64_tr_b16 v[26:27], v233 offset:2560\n" \
"v_mul_f32 v237, s92, v237\n" \
"v_mfma_f32_16x16x32_bf16 a[120:123], v[92:95], v[180:183], a[120:123]\n" \
"global_load_lds_dwordx4 v227, s[46:47] offset:3072\n" \
"v_cvt_pk_bf16_f32 v234, v234, v235\n" \
"v_mfma_f32_16x16x32_bf16 a[124:127], v[88:91], v[184:187], a[124:127]\n" \
"s_add_u32 s46, s46, 0xfffffb00\n" \
"s_addc_u32 s47, s47, -1\n" \
"ds_read_b64_tr_b16 v[28:29], v233 offset:8704\n" \
"v_mfma_f32_16x16x32_bf16 a[124:127], v[92:95], v[188:191], a[124:127]\n" \
"v_cvt_pk_bf16_f32 v236, v236, v237\n" \
"v_accvgpr_write_b32 a224, v234\n" \
"v_mfma_f32_16x16x32_bf16 a[144:147], v[96:99], v[160:163], a[144:147]\n" \
"s_add_u32 m0, s95, 36864\n" \
"ds_read_b64_tr_b16 v[30:31], v233 offset:10752\n" \
"v_accvgpr_write_b32 a225, v236\n" \
"v_mfma_f32_16x16x32_bf16 a[144:147], v[100:103], v[164:167], a[144:147]\n" \
"global_load_lds_dwordx4 v228, s[44:45] offset:-4096\n" \
"v_accvgpr_read_b32 v234, a228\n" \
"v_mfma_f32_16x16x32_bf16 a[148:151], v[96:99], v[168:171], a[148:151]\n" \
"ds_read_b128 v[192:195], v230 offset:0\n" \
"v_accvgpr_read_b32 v235, a229\n" \
"v_mfma_f32_16x16x32_bf16 a[148:151], v[100:103], v[172:175], a[148:151]\n" \
"v_accvgpr_read_b32 v236, a230\n" \
"v_accvgpr_read_b32 v237, a231\n" \
"v_mfma_f32_16x16x32_bf16 a[152:155], v[96:99], v[176:179], a[152:155]\n" \
"ds_read_b128 v[196:199], v231 offset:0\n" \
"v_mul_f32 v234, s92, v234\n" \
"v_mfma_f32_16x16x32_bf16 a[152:155], v[100:103], v[180:183], a[152:155]\n" \
"global_load_lds_dwordx4 v229, s[44:45] offset:-3072\n" \
"v_mul_f32 v235, s92, v235\n" \
"v_mfma_f32_16x16x32_bf16 a[156:159], v[96:99], v[184:187], a[156:159]\n" \
"s_add_u32 s44, s44, 0xffb00000\n" \
"s_addc_u32 s45, s45, -1\n" \
"ds_read_b128 v[200:203], v230 offset:2048\n" \
"v_mfma_f32_16x16x32_bf16 a[156:159], v[100:103], v[188:191], a[156:159]\n" \
"v_mul_f32 v236, s92, v236\n" \
"v_mul_f32 v237, s92, v237\n" \
"v_mfma_f32_16x16x32_bf16 a[176:179], v[104:107], v[160:163], a[176:179]\n" \
"ds_read_b128 v[204:207], v231 offset:2048\n" \
"v_cvt_pk_bf16_f32 v234, v234, v235\n" \
"v_mfma_f32_16x16x32_bf16 a[176:179], v[108:111], v[164:167], a[176:179]\n" \
"global_load_lds_dwordx4 v228, s[42:43] offset:-2048\n" \
"v_cvt_pk_bf16_f32 v236, v236, v237\n" \
"v_mfma_f32_16x16x32_bf16 a[180:183], v[104:107], v[168:171], a[180:183]\n" \
"ds_read_b128 v[208:211], v230 offset:4096\n" \
"v_accvgpr_write_b32 a226, v234\n" \
"v_mfma_f32_16x16x32_bf16 a[180:183], v[108:111], v[172:175], a[180:183]\n" \
"v_accvgpr_write_b32 a227, v236\n" \
"v_accvgpr_read_b32 v234, a200\n" \
"v_mfma_f32_16x16x32_bf16 a[184:187], v[104:107], v[176:179], a[184:187]\n" \
"ds_read_b128 v[212:215], v231 offset:4096\n" \
"v_accvgpr_read_b32 v235, a201\n" \
"v_mfma_f32_16x16x32_bf16 a[184:187], v[108:111], v[180:183], a[184:187]\n" \
"global_load_lds_dwordx4 v229, s[42:43] offset:-1024\n" \
"v_accvgpr_read_b32 v236, a202\n" \
"v_mfma_f32_16x16x32_bf16 a[188:191], v[104:107], v[184:187], a[188:191]\n" \
"s_add_u32 s42, s42, 0xffb00000\n" \
"s_addc_u32 s43, s43, -1\n" \
"v_accvgpr_read_b32 v237, a203\n" \
"v_mfma_f32_16x16x32_bf16 a[188:191], v[108:111], v[188:191], a[188:191]\n" \
"v_mul_f32 v234, s92, v234\n" \
"v_mul_f32 v235, s92, v235\n" \
"v_mfma_f32_16x16x32_bf16 a[208:211], v[112:115], v[160:163], a[208:211]\n" \
"s_add_u32 m0, s95, 53248\n" \
"v_mul_f32 v236, s92, v236\n" \
"v_mul_f32 v237, s92, v237\n" \
"v_mfma_f32_16x16x32_bf16 a[208:211], v[116:119], v[164:167], a[208:211]\n" \
"global_load_lds_dwordx4 v228, s[44:45] offset:-4096\n" \
"v_cvt_pk_bf16_f32 v234, v234, v235\n" \
"v_mfma_f32_16x16x32_bf16 a[212:215], v[112:115], v[168:171], a[212:215]\n" \
"v_cvt_pk_bf16_f32 v236, v236, v237\n" \
"v_accvgpr_write_b32 a200, v234\n" \
"v_mfma_f32_16x16x32_bf16 a[212:215], v[116:119], v[172:175], a[212:215]\n" \
"v_accvgpr_write_b32 a201, v236\n" \
"v_accvgpr_read_b32 v234, a204\n" \
"v_mfma_f32_16x16x32_bf16 a[216:219], v[112:115], v[176:179], a[216:219]\n" \
"v_accvgpr_read_b32 v235, a205\n" \
"v_accvgpr_read_b32 v236, a206\n" \
"v_mfma_f32_16x16x32_bf16 a[216:219], v[116:119], v[180:183], a[216:219]\n" \
"global_load_lds_dwordx4 v229, s[44:45] offset:-3072\n" \
"v_accvgpr_read_b32 v237, a207\n" \
"v_mfma_f32_16x16x32_bf16 a[220:223], v[112:115], v[184:187], a[220:223]\n" \
"s_add_u32 s44, s44, 0x80000\n" \
"s_addc_u32 s45, s45, 0\n" \
"v_mul_f32 v234, s92, v234\n" \
"v_mfma_f32_16x16x32_bf16 a[220:223], v[116:119], v[188:191], a[220:223]\n" \
"v_mul_f32 v235, s92, v235\n" \
"v_mul_f32 v236, s92, v236\n" \
"v_mfma_f32_16x16x32_bf16 a[240:243], v[120:123], v[160:163], a[240:243]\n" \
"v_mul_f32 v237, s92, v237\n" \
"v_cvt_pk_bf16_f32 v234, v234, v235\n" \
"v_mfma_f32_16x16x32_bf16 a[240:243], v[124:127], v[164:167], a[240:243]\n" \
"global_load_lds_dwordx4 v228, s[42:43] offset:-2048\n" \
"v_cvt_pk_bf16_f32 v236, v236, v237\n" \
"v_mfma_f32_16x16x32_bf16 a[244:247], v[120:123], v[168:171], a[244:247]\n" \
"v_accvgpr_write_b32 a202, v234\n" \
"v_accvgpr_write_b32 a203, v236\n" \
"v_mfma_f32_16x16x32_bf16 a[244:247], v[124:127], v[172:175], a[244:247]\n" \
"v_accvgpr_read_b32 v234, a232\n" \
"v_accvgpr_read_b32 v235, a233\n" \
"v_mfma_f32_16x16x32_bf16 a[248:251], v[120:123], v[176:179], a[248:251]\n" \
"v_accvgpr_read_b32 v236, a234\n" \
"v_accvgpr_read_b32 v237, a235\n" \
"v_mfma_f32_16x16x32_bf16 a[248:251], v[124:127], v[180:183], a[248:251]\n" \
"global_load_lds_dwordx4 v229, s[42:43] offset:-1024\n" \
"v_mul_f32 v234, s92, v234\n" \
"v_mfma_f32_16x16x32_bf16 a[252:255], v[120:123], v[184:187], a[252:255]\n" \
"s_add_u32 s42, s42, 0x80000\n" \
"s_addc_u32 s43, s43, 0\n" \
"v_mul_f32 v235, s92, v235\n" \
"v_mfma_f32_16x16x32_bf16 a[252:255], v[124:127], v[188:191], a[252:255]\n" \
"s_waitcnt lgkmcnt(0)\n" \
"v_mul_f32 v236, s92, v236\n" \
"v_mul_f32 v237, s92, v237\n" \
"v_mfma_f32_16x16x32_bf16 v[64:67], v[0:3], v[192:195], 0\n" \
"ds_read_b128 v[216:219], v230 offset:6144\n" \
"v_cvt_pk_bf16_f32 v234, v234, v235\n" \
"v_mfma_f32_16x16x32_bf16 v[64:67], v[4:7], v[196:199], v[64:67]\n" \
"ds_read_b128 v[220:223], v231 offset:6144\n" \
"v_cvt_pk_bf16_f32 v236, v236, v237\n" \
"v_mfma_f32_16x16x32_bf16 v[68:71], v[8:11], v[192:195], 0\n" \
"s_add_u32 s90, s88, 0\n" \
"ds_read_b64_tr_b16 v[32:33], v232 offset:1024\n" \
"v_accvgpr_write_b32 a232, v234\n" \
"v_mfma_f32_16x16x32_bf16 v[68:71], v[12:15], v[196:199], v[68:71]\n" \
"buffer_store_dwordx4 a[192:195], v249, s[56:59], s90 offen offset:0 sc1\n" \
"v_accvgpr_write_b32 a233, v236\n" \
"v_accvgpr_read_b32 v234, a236\n" \
"v_accvgpr_read_b32 v235, a237\n" \
"v_accvgpr_read_b32 v236, a238\n" \
"v_mfma_f32_16x16x32_bf16 v[72:75], v[16:19], v[192:195], 0\n" \
"ds_read_b64_tr_b16 v[34:35], v232 offset:3072\n" \
"v_accvgpr_read_b32 v237, a239\n" \
"v_mfma_f32_16x16x32_bf16 v[72:75], v[20:23], v[196:199], v[72:75]\n" \
"ds_read_b64_tr_b16 v[36:37], v232 offset:9216\n" \
"v_mul_f32 v234, s92, v234\n" \
"v_mfma_f32_16x16x32_bf16 v[76:79], v[24:27], v[192:195], 0\n" \
"ds_read_b64_tr_b16 v[38:39], v232 offset:11264\n" \
"v_mul_f32 v235, s92, v235\n" \
"v_mfma_f32_16x16x32_bf16 v[76:79], v[28:31], v[196:199], v[76:79]\n" \
"buffer_store_dwordx4 a[224:227], v249, s[56:59], s90 offen offset:256 sc1\n" \
"v_mul_f32 v236, s92, v236\n" \
"v_mul_f32 v237, s92, v237\n" \
"v_cvt_pk_bf16_f32 v234, v234, v235\n" \
"v_cvt_pk_bf16_f32 v236, v236, v237\n" \
"v_mfma_f32_16x16x32_bf16 v[96:99], v[0:3], v[200:203], 0\n" \
"ds_read_b64_tr_b16 v[40:41], v233 offset:1024\n" \
"v_accvgpr_write_b32 a234, v234\n" \
"v_mfma_f32_16x16x32_bf16 v[96:99], v[4:7], v[204:207], v[96:99]\n" \
"ds_read_b64_tr_b16 v[42:43], v233 offset:3072\n" \
"v_accvgpr_write_b32 a235, v236\n" \
"v_mfma_f32_16x16x32_bf16 v[100:103], v[8:11], v[200:203], 0\n" \
"ds_read_b64_tr_b16 v[44:45], v233 offset:9216\n" \
"v_accvgpr_read_b32 v234, a208\n" \
"v_mfma_f32_16x16x32_bf16 v[100:103], v[12:15], v[204:207], v[100:103]\n" \
"v_accvgpr_read_b32 v235, a209\n" \
"v_accvgpr_read_b32 v236, a210\n" \
"v_mfma_f32_16x16x32_bf16 v[104:107], v[16:19], v[200:203], 0\n" \
"ds_read_b64_tr_b16 v[46:47], v233 offset:11264\n" \
"v_accvgpr_read_b32 v237, a211\n" \
"v_mfma_f32_16x16x32_bf16 v[104:107], v[20:23], v[204:207], v[104:107]\n" \
"ds_read_b64_tr_b16 v[48:49], v232 offset:1536\n" \
"v_cvt_pk_bf16_f32 v234, v234, v235\n" \
"v_mfma_f32_16x16x32_bf16 v[108:111], v[24:27], v[200:203], 0\n" \
"ds_read_b64_tr_b16 v[50:51], v232 offset:3584\n" \
"v_cvt_pk_bf16_f32 v236, v236, v237\n" \
"v_mfma_f32_16x16x32_bf16 v[108:111], v[28:31], v[204:207], v[108:111]\n" \
"v_accvgpr_write_b32 a208, v234\n" \
"v_accvgpr_write_b32 a209, v236\n" \
"v_mfma_f32_16x16x32_bf16 v[128:131], v[0:3], v[208:211], 0\n" \
"ds_read_b64_tr_b16 v[52:53], v232 offset:9728\n" \
"v_accvgpr_read_b32 v234, a240\n" \
"v_mfma_f32_16x16x32_bf16 v[128:131], v[4:7], v[212:215], v[128:131]\n" \
"ds_read_b64_tr_b16 v[54:55], v232 offset:11776\n" \
"v_accvgpr_read_b32 v235, a241\n" \
"v_mfma_f32_16x16x32_bf16 v[132:135], v[8:11], v[208:211], 0\n" \
"ds_read_b64_tr_b16 v[56:57], v233 offset:1536\n" \
"v_accvgpr_read_b32 v236, a242\n" \
"v_mfma_f32_16x16x32_bf16 v[132:135], v[12:15], v[212:215], v[132:135]\n" \
"v_accvgpr_read_b32 v237, a243\n" \
"v_cvt_pk_bf16_f32 v234, v234, v235\n" \
"v_mfma_f32_16x16x32_bf16 v[136:139], v[16:19], v[208:211], 0\n" \
"ds_read_b64_tr_b16 v[58:59], v233 offset:3584\n" \
"v_cvt_pk_bf16_f32 v236, v236, v237\n" \
"v_mfma_f32_16x16x32_bf16 v[136:139], v[20:23], v[212:215], v[136:139]\n" \
"ds_read_b64_tr_b16 v[60:61], v233 offset:9728\n" \
"Lpg2186_%=:\n" \
"Lst0x3_%=:\n" \
"s_cbranch_execz Lpg4356_%=\n" \
"v_accvgpr_write_b32 a210, v234\n" \
"v_mfma_f32_16x16x32_bf16 v[140:143], v[24:27], v[208:211], 0\n" \
"ds_read_b64_tr_b16 v[62:63], v233 offset:11776\n" \
"v_accvgpr_write_b32 a211, v236\n" \
"v_mfma_f32_16x16x32_bf16 v[140:143], v[28:31], v[212:215], v[140:143]\n" \
"s_waitcnt lgkmcnt(3)\n" \
"v_accvgpr_read_b32 v234, a212\n" \
"v_accvgpr_read_b32 v235, a213\n" \
"v_mfma_f32_16x16x32_bf16 v[160:163], v[0:3], v[216:219], 0\n" \
"v_add_u32 v230, 0xffff0000, v230\n" \
"v_add_u32 v231, 0xffff0000, v231\n" \
"v_mfma_f32_16x16x32_bf16 v[160:163], v[4:7], v[220:223], v[160:163]\n" \
"v_add_u32 v232, 0x4000, v232\n" \
"v_add_u32 v233, 0x4000, v233\n" \
"v_mfma_f32_16x16x32_bf16 v[164:167], v[8:11], v[216:219], 0\n" \
"v_accvgpr_read_b32 v236, a214\n" \
"v_accvgpr_read_b32 v237, a215\n" \
"v_mfma_f32_16x16x32_bf16 v[164:167], v[12:15], v[220:223], v[164:167]\n" \
"v_cvt_pk_bf16_f32 v234, v234, v235\n" \
"v_cvt_pk_bf16_f32 v236, v236, v237\n" \
"v_mfma_f32_16x16x32_bf16 v[168:171], v[16:19], v[216:219], 0\n" \
"v_accvgpr_write_b32 a212, v234\n" \
"v_accvgpr_write_b32 a213, v236\n" \
"v_mfma_f32_16x16x32_bf16 v[168:171], v[20:23], v[220:223], v[168:171]\n" \
"v_accvgpr_read_b32 v234, a244\n" \
"v_accvgpr_read_b32 v235, a245\n" \
"v_mfma_f32_16x16x32_bf16 v[172:175], v[24:27], v[216:219], 0\n" \
"s_waitcnt vmcnt(19) lgkmcnt(0)\n" \
"v_accvgpr_read_b32 v236, a246\n" \
"v_accvgpr_read_b32 v237, a247\n" \
"v_mfma_f32_16x16x32_bf16 v[172:175], v[28:31], v[220:223], v[172:175]\n" \
"s_barrier\n" \
"v_mfma_f32_16x16x32_bf16 v[80:83], v[32:35], v[192:195], 0\n" \
"ds_read_b64_tr_b16 v[0:1], v232 offset:0\n" \
"v_cvt_pk_bf16_f32 v234, v234, v235\n" \
"v_mfma_f32_16x16x32_bf16 v[80:83], v[36:39], v[196:199], v[80:83]\n" \
"ds_read_b64_tr_b16 v[2:3], v232 offset:2048\n" \
"v_cvt_pk_bf16_f32 v236, v236, v237\n" \
"v_mfma_f32_16x16x32_bf16 v[84:87], v[40:43], v[192:195], 0\n" \
"s_add_u32 m0, s87, 135168\n" \
"ds_read_b64_tr_b16 v[4:5], v232 offset:8192\n" \
"v_accvgpr_write_b32 a214, v234\n" \
"v_mfma_f32_16x16x32_bf16 v[84:87], v[44:47], v[196:199], v[84:87]\n" \
"global_load_lds_dwordx4 v224, s[48:49] offset:-4096\n" \
"v_accvgpr_write_b32 a215, v236\n" \
"v_mfma_f32_16x16x32_bf16 v[88:91], v[48:51], v[192:195], 0\n" \
"ds_read_b64_tr_b16 v[6:7], v232 offset:10240\n" \
"v_accvgpr_read_b32 v234, a216\n" \
"v_mfma_f32_16x16x32_bf16 v[88:91], v[52:55], v[196:199], v[88:91]\n" \
"ds_read_b64_tr_b16 v[8:9], v233 offset:0\n" \
"v_accvgpr_read_b32 v235, a217\n" \
"v_mfma_f32_16x16x32_bf16 v[92:95], v[56:59], v[192:195], 0\n" \
"ds_read_b64_tr_b16 v[10:11], v233 offset:2048\n" \
"v_accvgpr_read_b32 v236, a218\n" \
"v_mfma_f32_16x16x32_bf16 v[92:95], v[60:63], v[196:199], v[92:95]\n" \
"global_load_lds_dwordx4 v225, s[48:49] offset:-3072\n" \
"v_accvgpr_read_b32 v237, a219\n" \
"v_mfma_f32_16x16x32_bf16 v[112:115], v[32:35], v[200:203], 0\n" \
"ds_read_b128 v[192:195], v230 offset:0\n" \
"v_cvt_pk_bf16_f32 v234, v234, v235\n" \
"v_mfma_f32_16x16x32_bf16 v[112:115], v[36:39], v[204:207], v[112:115]\n" \
"ds_read_b128 v[196:199], v231 offset:0\n" \
"v_cvt_pk_bf16_f32 v236, v236, v237\n" \
"v_mfma_f32_16x16x32_bf16 v[116:119], v[40:43], v[200:203], 0\n" \
"ds_read_b64_tr_b16 v[12:13], v233 offset:8192\n" \
"v_accvgpr_write_b32 a216, v234\n" \
"v_mfma_f32_16x16x32_bf16 v[116:119], v[44:47], v[204:207], v[116:119]\n" \
"global_load_lds_dwordx4 v226, s[48:49] offset:-2048\n" \
"v_accvgpr_write_b32 a217, v236\n" \
"v_mfma_f32_16x16x32_bf16 v[120:123], v[48:51], v[200:203], 0\n" \
"ds_read_b64_tr_b16 v[14:15], v233 offset:10240\n" \
"v_accvgpr_read_b32 v234, a248\n" \
"v_mfma_f32_16x16x32_bf16 v[120:123], v[52:55], v[204:207], v[120:123]\n" \
"ds_read_b64_tr_b16 v[16:17], v232 offset:512\n" \
"v_accvgpr_read_b32 v235, a249\n" \
"v_mfma_f32_16x16x32_bf16 v[124:127], v[56:59], v[200:203], 0\n" \
"ds_read_b64_tr_b16 v[18:19], v232 offset:2560\n" \
"v_accvgpr_read_b32 v236, a250\n" \
"v_mfma_f32_16x16x32_bf16 v[124:127], v[60:63], v[204:207], v[124:127]\n" \
"global_load_lds_dwordx4 v227, s[48:49] offset:-1024\n" \
"v_accvgpr_read_b32 v237, a251\n" \
"v_mfma_f32_16x16x32_bf16 v[144:147], v[32:35], v[208:211], 0\n" \
"s_add_u32 s48, s48, 0x80\n" \
"s_addc_u32 s49, s49, 0\n" \
"ds_read_b128 v[200:203], v230 offset:2048\n" \
"v_mfma_f32_16x16x32_bf16 v[144:147], v[36:39], v[212:215], v[144:147]\n" \
"ds_read_b128 v[204:207], v231 offset:2048\n" \
"v_cvt_pk_bf16_f32 v234, v234, v235\n" \
"v_mfma_f32_16x16x32_bf16 v[148:151], v[40:43], v[208:211], 0\n" \
"ds_read_b64_tr_b16 v[20:21], v232 offset:8704\n" \
"v_cvt_pk_bf16_f32 v236, v236, v237\n" \
"v_mfma_f32_16x16x32_bf16 v[148:151], v[44:47], v[212:215], v[148:151]\n" \
"global_load_lds_dwordx4 v224, s[46:47] offset:0\n" \
"v_accvgpr_write_b32 a218, v234\n" \
"v_mfma_f32_16x16x32_bf16 v[152:155], v[48:51], v[208:211], 0\n" \
"ds_read_b64_tr_b16 v[22:23], v232 offset:10752\n" \
"v_accvgpr_write_b32 a219, v236\n" \
"v_mfma_f32_16x16x32_bf16 v[152:155], v[52:55], v[212:215], v[152:155]\n" \
"ds_read_b64_tr_b16 v[24:25], v233 offset:512\n" \
"v_accvgpr_read_b32 v234, a220\n" \
"v_mfma_f32_16x16x32_bf16 v[156:159], v[56:59], v[208:211], 0\n" \
"ds_read_b64_tr_b16 v[26:27], v233 offset:2560\n" \
"v_accvgpr_read_b32 v235, a221\n" \
"v_mfma_f32_16x16x32_bf16 v[156:159], v[60:63], v[212:215], v[156:159]\n" \
"global_load_lds_dwordx4 v225, s[46:47] offset:1024\n" \
"v_accvgpr_read_b32 v236, a222\n" \
"v_mfma_f32_16x16x32_bf16 v[176:179], v[32:35], v[216:219], 0\n" \
"ds_read_b128 v[208:211], v230 offset:4096\n" \
"v_accvgpr_read_b32 v237, a223\n" \
"v_mfma_f32_16x16x32_bf16 v[176:179], v[36:39], v[220:223], v[176:179]\n" \
"ds_read_b128 v[212:215], v231 offset:4096\n" \
"v_cvt_pk_bf16_f32 v234, v234, v235\n" \
"v_mfma_f32_16x16x32_bf16 v[180:183], v[40:43], v[216:219], 0\n" \
"ds_read_b64_tr_b16 v[28:29], v233 offset:8704\n" \
"v_cvt_pk_bf16_f32 v236, v236, v237\n" \
"v_mfma_f32_16x16x32_bf16 v[180:183], v[44:47], v[220:223], v[180:183]\n" \
"global_load_lds_dwordx4 v226, s[46:47] offset:2048\n" \
"v_accvgpr_write_b32 a220, v234\n" \
"v_mfma_f32_16x16x32_bf16 v[184:187], v[48:51], v[216:219], 0\n" \
"ds_read_b64_tr_b16 v[30:31], v233 offset:10752\n" \
"v_accvgpr_write_b32 a221, v236\n" \
"v_mfma_f32_16x16x32_bf16 v[184:187], v[52:55], v[220:223], v[184:187]\n" \
"v_accvgpr_read_b32 v234, a252\n" \
"v_accvgpr_read_b32 v235, a253\n" \
"v_mfma_f32_16x16x32_bf16 v[188:191], v[56:59], v[216:219], 0\n" \
"v_accvgpr_read_b32 v236, a254\n" \
"v_accvgpr_read_b32 v237, a255\n" \
"v_mfma_f32_16x16x32_bf16 v[188:191], v[60:63], v[220:223], v[188:191]\n" \
"s_waitcnt lgkmcnt(1)\n" \
"global_load_lds_dwordx4 v227, s[46:47] offset:3072\n" \
"v_cvt_pk_bf16_f32 v234, v234, v235\n" \
"v_mfma_f32_16x16x32_bf16 v[64:67], v[0:3], v[192:195], v[64:67]\n" \
"s_add_u32 s46, s46, 0x80\n" \
"s_addc_u32 s47, s47, 0\n" \
"ds_read_b128 v[216:219], v230 offset:6144\n" \
"v_mfma_f32_16x16x32_bf16 v[64:67], v[4:7], v[196:199], v[64:67]\n" \
"ds_read_b128 v[220:223], v231 offset:6144\n" \
"v_cvt_pk_bf16_f32 v236, v236, v237\n" \
"v_mfma_f32_16x16x32_bf16 v[68:71], v[8:11], v[192:195], v[68:71]\n" \
"ds_read_b64_tr_b16 v[32:33], v232 offset:1024\n" \
"v_accvgpr_write_b32 a222, v234\n" \
"v_mfma_f32_16x16x32_bf16 v[68:71], v[12:15], v[196:199], v[68:71]\n" \
"buffer_store_dwordx4 a[200:203], v249, s[56:59], s90 offen offset:2048 sc1\n" \
"v_accvgpr_write_b32 a223, v236\n" \
"v_accvgpr_read_b32 v234, a128\n" \
"v_accvgpr_read_b32 v235, a129\n" \
"v_accvgpr_read_b32 v236, a130\n" \
"v_mfma_f32_16x16x32_bf16 v[72:75], v[16:19], v[192:195], v[72:75]\n" \
"ds_read_b64_tr_b16 v[34:35], v232 offset:3072\n" \
"v_accvgpr_read_b32 v237, a131\n" \
"v_mfma_f32_16x16x32_bf16 v[72:75], v[20:23], v[196:199], v[72:75]\n" \
"s_waitcnt lgkmcnt(3)\n" \
"ds_read_b64_tr_b16 v[36:37], v232 offset:9216\n" \
"v_mul_f32 v234, s92, v234\n" \
"v_mfma_f32_16x16x32_bf16 v[76:79], v[24:27], v[192:195], v[76:79]\n" \
"ds_read_b64_tr_b16 v[38:39], v232 offset:11264\n" \
"v_mul_f32 v235, s92, v235\n" \
"v_mfma_f32_16x16x32_bf16 v[76:79], v[28:31], v[196:199], v[76:79]\n" \
"v_mul_f32 v236, s92, v236\n" \
"v_mul_f32 v237, s92, v237\n" \
"v_mfma_f32_16x16x32_bf16 v[96:99], v[0:3], v[200:203], v[96:99]\n" \
"ds_read_b64_tr_b16 v[40:41], v233 offset:1024\n" \
"v_cvt_pk_bf16_f32 v234, v234, v235\n" \
"v_mfma_f32_16x16x32_bf16 v[96:99], v[4:7], v[204:207], v[96:99]\n" \
"ds_read_b64_tr_b16 v[42:43], v233 offset:3072\n" \
"v_cvt_pk_bf16_f32 v236, v236, v237\n" \
"v_mfma_f32_16x16x32_bf16 v[100:103], v[8:11], v[200:203], v[100:103]\n" \
"ds_read_b64_tr_b16 v[44:45], v233 offset:9216\n" \
"v_accvgpr_write_b32 a128, v234\n" \
"v_mfma_f32_16x16x32_bf16 v[100:103], v[12:15], v[204:207], v[100:103]\n" \
"v_accvgpr_write_b32 a129, v236\n" \
"v_accvgpr_read_b32 v234, a132\n" \
"v_mfma_f32_16x16x32_bf16 v[104:107], v[16:19], v[200:203], v[104:107]\n" \
"ds_read_b64_tr_b16 v[46:47], v233 offset:11264\n" \
"v_accvgpr_read_b32 v235, a133\n" \
"v_mfma_f32_16x16x32_bf16 v[104:107], v[20:23], v[204:207], v[104:107]\n" \
"ds_read_b64_tr_b16 v[48:49], v232 offset:1536\n" \
"v_accvgpr_read_b32 v236, a134\n" \
"v_mfma_f32_16x16x32_bf16 v[108:111], v[24:27], v[200:203], v[108:111]\n" \
"s_add_u32 m0, s95, 4096\n" \
"ds_read_b64_tr_b16 v[50:51], v232 offset:3584\n" \
"v_accvgpr_read_b32 v237, a135\n" \
"v_mfma_f32_16x16x32_bf16 v[108:111], v[28:31], v[204:207], v[108:111]\n" \
"global_load_lds_dwordx4 v228, s[44:45] offset:-4096\n" \
"v_mul_f32 v234, s92, v234\n" \
"v_mfma_f32_16x16x32_bf16 v[128:131], v[0:3], v[208:211], v[128:131]\n" \
"ds_read_b64_tr_b16 v[52:53], v232 offset:9728\n" \
"v_mul_f32 v235, s92, v235\n" \
"v_mfma_f32_16x16x32_bf16 v[128:131], v[4:7], v[212:215], v[128:131]\n" \
"ds_read_b64_tr_b16 v[54:55], v232 offset:11776\n" \
"v_mul_f32 v236, s92, v236\n" \
"v_mfma_f32_16x16x32_bf16 v[132:135], v[8:11], v[208:211], v[132:135]\n" \
"ds_read_b64_tr_b16 v[56:57], v233 offset:1536\n" \
"v_mul_f32 v237, s92, v237\n" \
"v_mfma_f32_16x16x32_bf16 v[132:135], v[12:15], v[212:215], v[132:135]\n" \
"global_load_lds_dwordx4 v229, s[44:45] offset:-3072\n" \
"v_cvt_pk_bf16_f32 v234, v234, v235\n" \
"v_mfma_f32_16x16x32_bf16 v[136:139], v[16:19], v[208:211], v[136:139]\n" \
"s_add_u32 s44, s44, 0x100000\n" \
"s_addc_u32 s45, s45, 0\n" \
"ds_read_b64_tr_b16 v[58:59], v233 offset:3584\n" \
"v_mfma_f32_16x16x32_bf16 v[136:139], v[20:23], v[212:215], v[136:139]\n" \
"ds_read_b64_tr_b16 v[60:61], v233 offset:9728\n" \
"v_cvt_pk_bf16_f32 v236, v236, v237\n" \
"v_mfma_f32_16x16x32_bf16 v[140:143], v[24:27], v[208:211], v[140:143]\n" \
"ds_read_b64_tr_b16 v[62:63], v233 offset:11776\n" \
"v_accvgpr_write_b32 a130, v234\n" \
"v_mfma_f32_16x16x32_bf16 v[140:143], v[28:31], v[212:215], v[140:143]\n" \
"s_waitcnt lgkmcnt(3)\n" \
"global_load_lds_dwordx4 v228, s[42:43] offset:-2048\n" \
"v_accvgpr_write_b32 a131, v236\n" \
"v_mfma_f32_16x16x32_bf16 v[160:163], v[0:3], v[216:219], v[160:163]\n" \
"v_add_u32 v230, 0x8000, v230\n" \
"v_add_u32 v231, 0x8000, v231\n" \
"v_mfma_f32_16x16x32_bf16 v[160:163], v[4:7], v[220:223], v[160:163]\n" \
"v_add_u32 v232, 0x4000, v232\n" \
"v_add_u32 v233, 0x4000, v233\n" \
"v_mfma_f32_16x16x32_bf16 v[164:167], v[8:11], v[216:219], v[164:167]\n" \
"v_accvgpr_read_b32 v234, a160\n" \
"v_accvgpr_read_b32 v235, a161\n" \
"v_mfma_f32_16x16x32_bf16 v[164:167], v[12:15], v[220:223], v[164:167]\n" \
"global_load_lds_dwordx4 v229, s[42:43] offset:-1024\n" \
"v_accvgpr_read_b32 v236, a162\n" \
"v_mfma_f32_16x16x32_bf16 v[168:171], v[16:19], v[216:219], v[168:171]\n" \
"s_add_u32 s42, s42, 0x100000\n" \
"s_addc_u32 s43, s43, 0\n" \
"v_accvgpr_read_b32 v237, a163\n" \
"v_mfma_f32_16x16x32_bf16 v[168:171], v[20:23], v[220:223], v[168:171]\n" \
"v_mul_f32 v234, s92, v234\n" \
"v_mul_f32 v235, s92, v235\n" \
"v_mfma_f32_16x16x32_bf16 v[172:175], v[24:27], v[216:219], v[172:175]\n" \
"s_waitcnt vmcnt(19) lgkmcnt(0)\n" \
"v_mul_f32 v236, s92, v236\n" \
"v_mul_f32 v237, s92, v237\n" \
"v_mfma_f32_16x16x32_bf16 v[172:175], v[28:31], v[220:223], v[172:175]\n" \
"s_barrier\n" \
"v_mfma_f32_16x16x32_bf16 v[80:83], v[32:35], v[192:195], v[80:83]\n" \
"ds_read_b64_tr_b16 v[0:1], v232 offset:0\n" \
"v_cvt_pk_bf16_f32 v234, v234, v235\n" \
"v_mfma_f32_16x16x32_bf16 v[80:83], v[36:39], v[196:199], v[80:83]\n" \
"ds_read_b64_tr_b16 v[2:3], v232 offset:2048\n" \
"v_cvt_pk_bf16_f32 v236, v236, v237\n" \
"v_mfma_f32_16x16x32_bf16 v[84:87], v[40:43], v[192:195], v[84:87]\n" \
"s_add_u32 m0, s87, 69632\n" \
"ds_read_b64_tr_b16 v[4:5], v232 offset:8192\n" \
"v_accvgpr_write_b32 a160, v234\n" \
"v_mfma_f32_16x16x32_bf16 v[84:87], v[44:47], v[196:199], v[84:87]\n" \
"global_load_lds_dwordx4 v224, s[48:49] offset:-4096\n" \
"v_accvgpr_write_b32 a161, v236\n" \
"v_mfma_f32_16x16x32_bf16 v[88:91], v[48:51], v[192:195], v[88:91]\n" \
"ds_read_b64_tr_b16 v[6:7], v232 offset:10240\n" \
"v_accvgpr_read_b32 v234, a164\n" \
"v_mfma_f32_16x16x32_bf16 v[88:91], v[52:55], v[196:199], v[88:91]\n" \
"ds_read_b64_tr_b16 v[8:9], v233 offset:0\n" \
"v_accvgpr_read_b32 v235, a165\n" \
"v_mfma_f32_16x16x32_bf16 v[92:95], v[56:59], v[192:195], v[92:95]\n" \
"ds_read_b64_tr_b16 v[10:11], v233 offset:2048\n" \
"v_accvgpr_read_b32 v236, a166\n" \
"v_mfma_f32_16x16x32_bf16 v[92:95], v[60:63], v[196:199], v[92:95]\n" \
"global_load_lds_dwordx4 v225, s[48:49] offset:-3072\n" \
"v_accvgpr_read_b32 v237, a167\n" \
"v_mfma_f32_16x16x32_bf16 v[112:115], v[32:35], v[200:203], v[112:115]\n" \
"ds_read_b128 v[192:195], v230 offset:0\n" \
"v_mul_f32 v234, s92, v234\n" \
"v_mfma_f32_16x16x32_bf16 v[112:115], v[36:39], v[204:207], v[112:115]\n" \
"ds_read_b128 v[196:199], v231 offset:0\n" \
"v_mul_f32 v235, s92, v235\n" \
"v_mfma_f32_16x16x32_bf16 v[116:119], v[40:43], v[200:203], v[116:119]\n" \
"ds_read_b64_tr_b16 v[12:13], v233 offset:8192\n" \
"v_mul_f32 v236, s92, v236\n" \
"v_mfma_f32_16x16x32_bf16 v[116:119], v[44:47], v[204:207], v[116:119]\n" \
"global_load_lds_dwordx4 v226, s[48:49] offset:-2048\n" \
"v_mul_f32 v237, s92, v237\n" \
"v_mfma_f32_16x16x32_bf16 v[120:123], v[48:51], v[200:203], v[120:123]\n" \
"ds_read_b64_tr_b16 v[14:15], v233 offset:10240\n" \
"v_cvt_pk_bf16_f32 v234, v234, v235\n" \
"v_mfma_f32_16x16x32_bf16 v[120:123], v[52:55], v[204:207], v[120:123]\n" \
"ds_read_b64_tr_b16 v[16:17], v232 offset:512\n" \
"v_cvt_pk_bf16_f32 v236, v236, v237\n" \
"v_mfma_f32_16x16x32_bf16 v[124:127], v[56:59], v[200:203], v[124:127]\n" \
"ds_read_b64_tr_b16 v[18:19], v232 offset:2560\n" \
"v_accvgpr_write_b32 a162, v234\n" \
"v_mfma_f32_16x16x32_bf16 v[124:127], v[60:63], v[204:207], v[124:127]\n" \
"global_load_lds_dwordx4 v227, s[48:49] offset:-1024\n" \
"v_accvgpr_write_b32 a163, v236\n" \
"v_mfma_f32_16x16x32_bf16 v[144:147], v[32:35], v[208:211], v[144:147]\n" \
"s_add_u32 s48, s48, 0x100\n" \
"s_addc_u32 s49, s49, 0\n" \
"ds_read_b128 v[200:203], v230 offset:2048\n" \
"v_mfma_f32_16x16x32_bf16 v[144:147], v[36:39], v[212:215], v[144:147]\n" \
"ds_read_b128 v[204:207], v231 offset:2048\n" \
"v_accvgpr_read_b32 v234, a136\n" \
"v_mfma_f32_16x16x32_bf16 v[148:151], v[40:43], v[208:211], v[148:151]\n" \
"ds_read_b64_tr_b16 v[20:21], v232 offset:8704\n" \
"v_accvgpr_read_b32 v235, a137\n" \
"v_mfma_f32_16x16x32_bf16 v[148:151], v[44:47], v[212:215], v[148:151]\n" \
"global_load_lds_dwordx4 v224, s[46:47] offset:0\n" \
"v_accvgpr_read_b32 v236, a138\n" \
"v_mfma_f32_16x16x32_bf16 v[152:155], v[48:51], v[208:211], v[152:155]\n" \
"ds_read_b64_tr_b16 v[22:23], v232 offset:10752\n" \
"v_accvgpr_read_b32 v237, a139\n" \
"v_mfma_f32_16x16x32_bf16 v[152:155], v[52:55], v[212:215], v[152:155]\n" \
"ds_read_b64_tr_b16 v[24:25], v233 offset:512\n" \
"v_mul_f32 v234, s92, v234\n" \
"v_mfma_f32_16x16x32_bf16 v[156:159], v[56:59], v[208:211], v[156:159]\n" \
"ds_read_b64_tr_b16 v[26:27], v233 offset:2560\n" \
"v_mul_f32 v235, s92, v235\n" \
"v_mfma_f32_16x16x32_bf16 v[156:159], v[60:63], v[212:215], v[156:159]\n" \
"global_load_lds_dwordx4 v225, s[46:47] offset:1024\n" \
"v_mul_f32 v236, s92, v236\n" \
"v_mfma_f32_16x16x32_bf16 v[176:179], v[32:35], v[216:219], v[176:179]\n" \
"ds_read_b128 v[208:211], v230 offset:4096\n" \
"v_mul_f32 v237, s92, v237\n" \
"v_mfma_f32_16x16x32_bf16 v[176:179], v[36:39], v[220:223], v[176:179]\n" \
"ds_read_b128 v[212:215], v231 offset:4096\n" \
"v_cvt_pk_bf16_f32 v234, v234, v235\n" \
"v_mfma_f32_16x16x32_bf16 v[180:183], v[40:43], v[216:219], v[180:183]\n" \
"ds_read_b64_tr_b16 v[28:29], v233 offset:8704\n" \
"v_cvt_pk_bf16_f32 v236, v236, v237\n" \
"v_mfma_f32_16x16x32_bf16 v[180:183], v[44:47], v[220:223], v[180:183]\n" \
"global_load_lds_dwordx4 v226, s[46:47] offset:2048\n" \
"v_accvgpr_write_b32 a136, v234\n" \
"v_mfma_f32_16x16x32_bf16 v[184:187], v[48:51], v[216:219], v[184:187]\n" \
"ds_read_b64_tr_b16 v[30:31], v233 offset:10752\n" \
"v_accvgpr_write_b32 a137, v236\n" \
"v_mfma_f32_16x16x32_bf16 v[184:187], v[52:55], v[220:223], v[184:187]\n" \
"v_accvgpr_read_b32 v234, a140\n" \
"v_accvgpr_read_b32 v235, a141\n" \
"v_mfma_f32_16x16x32_bf16 v[188:191], v[56:59], v[216:219], v[188:191]\n" \
"v_accvgpr_read_b32 v236, a142\n" \
"v_accvgpr_read_b32 v237, a143\n" \
"v_mfma_f32_16x16x32_bf16 v[188:191], v[60:63], v[220:223], v[188:191]\n" \
"s_waitcnt lgkmcnt(1)\n" \
"global_load_lds_dwordx4 v227, s[46:47] offset:3072\n" \
"v_mul_f32 v234, s92, v234\n" \
"v_mfma_f32_16x16x32_bf16 v[64:67], v[0:3], v[192:195], v[64:67]\n" \
"s_add_u32 s46, s46, 0x100\n" \
"s_addc_u32 s47, s47, 0\n" \
"ds_read_b128 v[216:219], v230 offset:6144\n" \
"v_mfma_f32_16x16x32_bf16 v[64:67], v[4:7], v[196:199], v[64:67]\n" \
"ds_read_b128 v[220:223], v231 offset:6144\n" \
"v_mul_f32 v235, s92, v235\n" \
"v_mfma_f32_16x16x32_bf16 v[68:71], v[8:11], v[192:195], v[68:71]\n" \
"ds_read_b64_tr_b16 v[32:33], v232 offset:1024\n" \
"v_mul_f32 v236, s92, v236\n" \
"v_mfma_f32_16x16x32_bf16 v[68:71], v[12:15], v[196:199], v[68:71]\n" \
"buffer_store_dwordx4 a[232:235], v249, s[56:59], s90 offen offset:2304 sc1\n" \
"v_mul_f32 v237, s92, v237\n" \
"v_cvt_pk_bf16_f32 v234, v234, v235\n" \
"v_cvt_pk_bf16_f32 v236, v236, v237\n" \
"v_accvgpr_write_b32 a138, v234\n" \
"v_mfma_f32_16x16x32_bf16 v[72:75], v[16:19], v[192:195], v[72:75]\n" \
"s_add_u32 s90, s89, 0\n" \
"ds_read_b64_tr_b16 v[34:35], v232 offset:3072\n" \
"v_accvgpr_write_b32 a139, v236\n" \
"v_mfma_f32_16x16x32_bf16 v[72:75], v[20:23], v[196:199], v[72:75]\n" \
"s_waitcnt lgkmcnt(3)\n" \
"ds_read_b64_tr_b16 v[36:37], v232 offset:9216\n" \
"v_accvgpr_read_b32 v234, a168\n" \
"v_mfma_f32_16x16x32_bf16 v[76:79], v[24:27], v[192:195], v[76:79]\n" \
"ds_read_b64_tr_b16 v[38:39], v232 offset:11264\n" \
"v_accvgpr_read_b32 v235, a169\n" \
"v_mfma_f32_16x16x32_bf16 v[76:79], v[28:31], v[196:199], v[76:79]\n" \
"buffer_store_dwordx4 a[208:211], v248, s[56:59], s90 offen offset:0 sc1\n" \
"v_accvgpr_read_b32 v236, a170\n" \
"v_accvgpr_read_b32 v237, a171\n" \
"v_mul_f32 v234, s92, v234\n" \
"v_mul_f32 v235, s92, v235\n" \
"v_mfma_f32_16x16x32_bf16 v[96:99], v[0:3], v[200:203], v[96:99]\n" \
"ds_read_b64_tr_b16 v[40:41], v233 offset:1024\n" \
"v_mul_f32 v236, s92, v236\n" \
"v_mfma_f32_16x16x32_bf16 v[96:99], v[4:7], v[204:207], v[96:99]\n" \
"ds_read_b64_tr_b16 v[42:43], v233 offset:3072\n" \
"v_mul_f32 v237, s92, v237\n" \
"v_mfma_f32_16x16x32_bf16 v[100:103], v[8:11], v[200:203], v[100:103]\n" \
"ds_read_b64_tr_b16 v[44:45], v233 offset:9216\n" \
"v_cvt_pk_bf16_f32 v234, v234, v235\n" \
"v_mfma_f32_16x16x32_bf16 v[100:103], v[12:15], v[204:207], v[100:103]\n" \
"v_cvt_pk_bf16_f32 v236, v236, v237\n" \
"v_accvgpr_write_b32 a168, v234\n" \
"v_mfma_f32_16x16x32_bf16 v[104:107], v[16:19], v[200:203], v[104:107]\n" \
"ds_read_b64_tr_b16 v[46:47], v233 offset:11264\n" \
"v_accvgpr_write_b32 a169, v236\n" \
"v_mfma_f32_16x16x32_bf16 v[104:107], v[20:23], v[204:207], v[104:107]\n" \
"ds_read_b64_tr_b16 v[48:49], v232 offset:1536\n" \
"v_accvgpr_read_b32 v234, a172\n" \
"v_mfma_f32_16x16x32_bf16 v[108:111], v[24:27], v[200:203], v[108:111]\n" \
"s_add_u32 m0, s95, 20480\n" \
"ds_read_b64_tr_b16 v[50:51], v232 offset:3584\n" \
"v_accvgpr_read_b32 v235, a173\n" \
"v_mfma_f32_16x16x32_bf16 v[108:111], v[28:31], v[204:207], v[108:111]\n" \
"global_load_lds_dwordx4 v228, s[44:45] offset:-4096\n" \
"v_accvgpr_read_b32 v236, a174\n" \
"v_mfma_f32_16x16x32_bf16 v[128:131], v[0:3], v[208:211], v[128:131]\n" \
"ds_read_b64_tr_b16 v[52:53], v232 offset:9728\n" \
"v_accvgpr_read_b32 v237, a175\n" \
"v_mfma_f32_16x16x32_bf16 v[128:131], v[4:7], v[212:215], v[128:131]\n" \
"ds_read_b64_tr_b16 v[54:55], v232 offset:11776\n" \
"v_mul_f32 v234, s92, v234\n" \
"v_mfma_f32_16x16x32_bf16 v[132:135], v[8:11], v[208:211], v[132:135]\n" \
"ds_read_b64_tr_b16 v[56:57], v233 offset:1536\n" \
"v_mul_f32 v235, s92, v235\n" \
"v_mfma_f32_16x16x32_bf16 v[132:135], v[12:15], v[212:215], v[132:135]\n" \
"global_load_lds_dwordx4 v229, s[44:45] offset:-3072\n" \
"v_mul_f32 v236, s92, v236\n" \
"v_mfma_f32_16x16x32_bf16 v[136:139], v[16:19], v[208:211], v[136:139]\n" \
"s_add_u32 s44, s44, 0x80000\n" \
"s_addc_u32 s45, s45, 0\n" \
"ds_read_b64_tr_b16 v[58:59], v233 offset:3584\n" \
"v_mfma_f32_16x16x32_bf16 v[136:139], v[20:23], v[212:215], v[136:139]\n" \
"ds_read_b64_tr_b16 v[60:61], v233 offset:9728\n" \
"v_mul_f32 v237, s92, v237\n" \
"v_mfma_f32_16x16x32_bf16 v[140:143], v[24:27], v[208:211], v[140:143]\n" \
"ds_read_b64_tr_b16 v[62:63], v233 offset:11776\n" \
"v_cvt_pk_bf16_f32 v234, v234, v235\n" \
"v_mfma_f32_16x16x32_bf16 v[140:143], v[28:31], v[212:215], v[140:143]\n" \
"s_waitcnt lgkmcnt(3)\n" \
"global_load_lds_dwordx4 v228, s[42:43] offset:-2048\n" \
"v_cvt_pk_bf16_f32 v236, v236, v237\n" \
"v_mfma_f32_16x16x32_bf16 v[160:163], v[0:3], v[216:219], v[160:163]\n" \
"v_add_u32 v230, 0x8000, v230\n" \
"v_add_u32 v231, 0x8000, v231\n" \
"v_mfma_f32_16x16x32_bf16 v[160:163], v[4:7], v[220:223], v[160:163]\n" \
"v_add_u32 v232, 0x4000, v232\n" \
"v_add_u32 v233, 0x4000, v233\n" \
"v_mfma_f32_16x16x32_bf16 v[164:167], v[8:11], v[216:219], v[164:167]\n" \
"v_accvgpr_write_b32 a170, v234\n" \
"v_accvgpr_write_b32 a171, v236\n" \
"v_mfma_f32_16x16x32_bf16 v[164:167], v[12:15], v[220:223], v[164:167]\n" \
"global_load_lds_dwordx4 v229, s[42:43] offset:-1024\n" \
"v_accvgpr_read_b32 v234, a144\n" \
"v_mfma_f32_16x16x32_bf16 v[168:171], v[16:19], v[216:219], v[168:171]\n" \
"s_add_u32 s42, s42, 0x80000\n" \
"s_addc_u32 s43, s43, 0\n" \
"v_accvgpr_read_b32 v235, a145\n" \
"v_mfma_f32_16x16x32_bf16 v[168:171], v[20:23], v[220:223], v[168:171]\n" \
"v_accvgpr_read_b32 v236, a146\n" \
"v_accvgpr_read_b32 v237, a147\n" \
"v_mfma_f32_16x16x32_bf16 v[172:175], v[24:27], v[216:219], v[172:175]\n" \
"s_waitcnt vmcnt(19) lgkmcnt(0)\n" \
"v_cvt_pk_bf16_f32 v234, v234, v235\n" \
"v_cvt_pk_bf16_f32 v236, v236, v237\n" \
"v_mfma_f32_16x16x32_bf16 v[172:175], v[28:31], v[220:223], v[172:175]\n" \
"s_barrier\n" \
"v_mfma_f32_16x16x32_bf16 v[80:83], v[32:35], v[192:195], v[80:83]\n" \
"ds_read_b64_tr_b16 v[0:1], v232 offset:0\n" \
"v_accvgpr_write_b32 a144, v234\n" \
"v_mfma_f32_16x16x32_bf16 v[80:83], v[36:39], v[196:199], v[80:83]\n" \
"ds_read_b64_tr_b16 v[2:3], v232 offset:2048\n" \
"v_accvgpr_write_b32 a145, v236\n" \
"v_mfma_f32_16x16x32_bf16 v[84:87], v[40:43], v[192:195], v[84:87]\n" \
"s_add_u32 m0, s87, 102400\n" \
"ds_read_b64_tr_b16 v[4:5], v232 offset:8192\n" \
"v_accvgpr_read_b32 v234, a176\n" \
"v_mfma_f32_16x16x32_bf16 v[84:87], v[44:47], v[196:199], v[84:87]\n" \
"global_load_lds_dwordx4 v224, s[48:49] offset:-4096\n" \
"v_accvgpr_read_b32 v235, a177\n" \
"v_mfma_f32_16x16x32_bf16 v[88:91], v[48:51], v[192:195], v[88:91]\n" \
"ds_read_b64_tr_b16 v[6:7], v232 offset:10240\n" \
"v_accvgpr_read_b32 v236, a178\n" \
"v_mfma_f32_16x16x32_bf16 v[88:91], v[52:55], v[196:199], v[88:91]\n" \
"ds_read_b64_tr_b16 v[8:9], v233 offset:0\n" \
"v_accvgpr_read_b32 v237, a179\n" \
"v_mfma_f32_16x16x32_bf16 v[92:95], v[56:59], v[192:195], v[92:95]\n" \
"ds_read_b64_tr_b16 v[10:11], v233 offset:2048\n" \
"v_cvt_pk_bf16_f32 v234, v234, v235\n" \
"v_mfma_f32_16x16x32_bf16 v[92:95], v[60:63], v[196:199], v[92:95]\n" \
"global_load_lds_dwordx4 v225, s[48:49] offset:-3072\n" \
"v_cvt_pk_bf16_f32 v236, v236, v237\n" \
"v_mfma_f32_16x16x32_bf16 v[112:115], v[32:35], v[200:203], v[112:115]\n" \
"ds_read_b128 v[192:195], v230 offset:0\n" \
"v_accvgpr_write_b32 a146, v234\n" \
"v_mfma_f32_16x16x32_bf16 v[112:115], v[36:39], v[204:207], v[112:115]\n" \
"ds_read_b128 v[196:199], v231 offset:0\n" \
"v_accvgpr_write_b32 a147, v236\n" \
"v_mfma_f32_16x16x32_bf16 v[116:119], v[40:43], v[200:203], v[116:119]\n" \
"ds_read_b64_tr_b16 v[12:13], v233 offset:8192\n" \
"v_accvgpr_read_b32 v234, a148\n" \
"v_mfma_f32_16x16x32_bf16 v[116:119], v[44:47], v[204:207], v[116:119]\n" \
"global_load_lds_dwordx4 v226, s[48:49] offset:-2048\n" \
"v_accvgpr_read_b32 v235, a149\n" \
"v_mfma_f32_16x16x32_bf16 v[120:123], v[48:51], v[200:203], v[120:123]\n" \
"ds_read_b64_tr_b16 v[14:15], v233 offset:10240\n" \
"v_accvgpr_read_b32 v236, a150\n" \
"v_mfma_f32_16x16x32_bf16 v[120:123], v[52:55], v[204:207], v[120:123]\n" \
"ds_read_b64_tr_b16 v[16:17], v232 offset:512\n" \
"v_accvgpr_read_b32 v237, a151\n" \
"v_mfma_f32_16x16x32_bf16 v[124:127], v[56:59], v[200:203], v[124:127]\n" \
"ds_read_b64_tr_b16 v[18:19], v232 offset:2560\n" \
"v_cvt_pk_bf16_f32 v234, v234, v235\n" \
"v_mfma_f32_16x16x32_bf16 v[124:127], v[60:63], v[204:207], v[124:127]\n" \
"global_load_lds_dwordx4 v227, s[48:49] offset:-1024\n" \
"v_cvt_pk_bf16_f32 v236, v236, v237\n" \
"v_mfma_f32_16x16x32_bf16 v[144:147], v[32:35], v[208:211], v[144:147]\n" \
"s_add_u32 s48, s48, 0x80\n" \
"s_addc_u32 s49, s49, 0\n" \
"ds_read_b128 v[200:203], v230 offset:2048\n" \
"v_mfma_f32_16x16x32_bf16 v[144:147], v[36:39], v[212:215], v[144:147]\n" \
"ds_read_b128 v[204:207], v231 offset:2048\n" \
"v_accvgpr_write_b32 a148, v234\n" \
"v_mfma_f32_16x16x32_bf16 v[148:151], v[40:43], v[208:211], v[148:151]\n" \
"ds_read_b64_tr_b16 v[20:21], v232 offset:8704\n" \
"v_accvgpr_write_b32 a149, v236\n" \
"v_mfma_f32_16x16x32_bf16 v[148:151], v[44:47], v[212:215], v[148:151]\n" \
"global_load_lds_dwordx4 v224, s[46:47] offset:0\n" \
"v_accvgpr_read_b32 v234, a180\n" \
"v_mfma_f32_16x16x32_bf16 v[152:155], v[48:51], v[208:211], v[152:155]\n" \
"ds_read_b64_tr_b16 v[22:23], v232 offset:10752\n" \
"v_accvgpr_read_b32 v235, a181\n" \
"v_mfma_f32_16x16x32_bf16 v[152:155], v[52:55], v[212:215], v[152:155]\n" \
"ds_read_b64_tr_b16 v[24:25], v233 offset:512\n" \
"v_accvgpr_read_b32 v236, a182\n" \
"v_mfma_f32_16x16x32_bf16 v[156:159], v[56:59], v[208:211], v[156:159]\n" \
"ds_read_b64_tr_b16 v[26:27], v233 offset:2560\n" \
"v_accvgpr_read_b32 v237, a183\n" \
"v_mfma_f32_16x16x32_bf16 v[156:159], v[60:63], v[212:215], v[156:159]\n" \
"global_load_lds_dwordx4 v225, s[46:47] offset:1024\n" \
"v_cvt_pk_bf16_f32 v234, v234, v235\n" \
"v_mfma_f32_16x16x32_bf16 v[176:179], v[32:35], v[216:219], v[176:179]\n" \
"ds_read_b128 v[208:211], v230 offset:4096\n" \
"v_cvt_pk_bf16_f32 v236, v236, v237\n" \
"v_mfma_f32_16x16x32_bf16 v[176:179], v[36:39], v[220:223], v[176:179]\n" \
"ds_read_b128 v[212:215], v231 offset:4096\n" \
"v_accvgpr_write_b32 a150, v234\n" \
"v_mfma_f32_16x16x32_bf16 v[180:183], v[40:43], v[216:219], v[180:183]\n" \
"ds_read_b64_tr_b16 v[28:29], v233 offset:8704\n" \
"v_accvgpr_write_b32 a151, v236\n" \
"v_mfma_f32_16x16x32_bf16 v[180:183], v[44:47], v[220:223], v[180:183]\n" \
"global_load_lds_dwordx4 v226, s[46:47] offset:2048\n" \
"v_accvgpr_read_b32 v234, a152\n" \
"v_mfma_f32_16x16x32_bf16 v[184:187], v[48:51], v[216:219], v[184:187]\n" \
"ds_read_b64_tr_b16 v[30:31], v233 offset:10752\n" \
"v_accvgpr_read_b32 v235, a153\n" \
"v_mfma_f32_16x16x32_bf16 v[184:187], v[52:55], v[220:223], v[184:187]\n" \
"v_accvgpr_read_b32 v236, a154\n" \
"v_accvgpr_read_b32 v237, a155\n" \
"v_mfma_f32_16x16x32_bf16 v[188:191], v[56:59], v[216:219], v[188:191]\n" \
"v_cvt_pk_bf16_f32 v234, v234, v235\n" \
"v_cvt_pk_bf16_f32 v236, v236, v237\n" \
"Lpg2726_%=:\n" \
"s_cbranch_execz Lpg4892_%=\n" \
"v_mfma_f32_16x16x32_bf16 v[188:191], v[60:63], v[220:223], v[188:191]\n" \
"s_waitcnt lgkmcnt(1)\n" \
"global_load_lds_dwordx4 v227, s[46:47] offset:3072\n" \
"v_accvgpr_write_b32 a152, v234\n" \
"v_mfma_f32_16x16x32_bf16 v[64:67], v[0:3], v[192:195], v[64:67]\n" \
"s_add_u32 s46, s46, 0x80\n" \
"s_addc_u32 s47, s47, 0\n" \
"ds_read_b128 v[216:219], v230 offset:6144\n" \
"v_mfma_f32_16x16x32_bf16 v[64:67], v[4:7], v[196:199], v[64:67]\n" \
"ds_read_b128 v[220:223], v231 offset:6144\n" \
"v_accvgpr_write_b32 a153, v236\n" \
"v_mfma_f32_16x16x32_bf16 v[68:71], v[8:11], v[192:195], v[68:71]\n" \
"ds_read_b64_tr_b16 v[32:33], v232 offset:1024\n" \
"v_accvgpr_read_b32 v234, a184\n" \
"v_mfma_f32_16x16x32_bf16 v[68:71], v[12:15], v[196:199], v[68:71]\n" \
"buffer_store_dwordx4 a[212:215], v248, s[56:59], s90 offen offset:1024 sc1\n" \
"v_accvgpr_read_b32 v235, a185\n" \
"v_accvgpr_read_b32 v236, a186\n" \
"v_accvgpr_read_b32 v237, a187\n" \
"v_cvt_pk_bf16_f32 v234, v234, v235\n" \
"v_mfma_f32_16x16x32_bf16 v[72:75], v[16:19], v[192:195], v[72:75]\n" \
"ds_read_b64_tr_b16 v[34:35], v232 offset:3072\n" \
"v_cvt_pk_bf16_f32 v236, v236, v237\n" \
"v_mfma_f32_16x16x32_bf16 v[72:75], v[20:23], v[196:199], v[72:75]\n" \
"s_waitcnt lgkmcnt(3)\n" \
"ds_read_b64_tr_b16 v[36:37], v232 offset:9216\n" \
"v_accvgpr_write_b32 a154, v234\n" \
"v_mfma_f32_16x16x32_bf16 v[76:79], v[24:27], v[192:195], v[76:79]\n" \
"ds_read_b64_tr_b16 v[38:39], v232 offset:11264\n" \
"v_accvgpr_write_b32 a155, v236\n" \
"v_mfma_f32_16x16x32_bf16 v[76:79], v[28:31], v[196:199], v[76:79]\n" \
"v_accvgpr_read_b32 v234, a156\n" \
"v_accvgpr_read_b32 v235, a157\n" \
"v_mfma_f32_16x16x32_bf16 v[96:99], v[0:3], v[200:203], v[96:99]\n" \
"ds_read_b64_tr_b16 v[40:41], v233 offset:1024\n" \
"v_accvgpr_read_b32 v236, a158\n" \
"v_mfma_f32_16x16x32_bf16 v[96:99], v[4:7], v[204:207], v[96:99]\n" \
"ds_read_b64_tr_b16 v[42:43], v233 offset:3072\n" \
"v_accvgpr_read_b32 v237, a159\n" \
"v_mfma_f32_16x16x32_bf16 v[100:103], v[8:11], v[200:203], v[100:103]\n" \
"ds_read_b64_tr_b16 v[44:45], v233 offset:9216\n" \
"v_cvt_pk_bf16_f32 v234, v234, v235\n" \
"v_mfma_f32_16x16x32_bf16 v[100:103], v[12:15], v[204:207], v[100:103]\n" \
"v_cvt_pk_bf16_f32 v236, v236, v237\n" \
"v_accvgpr_write_b32 a156, v234\n" \
"v_mfma_f32_16x16x32_bf16 v[104:107], v[16:19], v[200:203], v[104:107]\n" \
"ds_read_b64_tr_b16 v[46:47], v233 offset:11264\n" \
"v_accvgpr_write_b32 a157, v236\n" \
"v_mfma_f32_16x16x32_bf16 v[104:107], v[20:23], v[204:207], v[104:107]\n" \
"ds_read_b64_tr_b16 v[48:49], v232 offset:1536\n" \
"v_accvgpr_read_b32 v234, a188\n" \
"v_mfma_f32_16x16x32_bf16 v[108:111], v[24:27], v[200:203], v[108:111]\n" \
"s_add_u32 m0, s95, 36864\n" \
"ds_read_b64_tr_b16 v[50:51], v232 offset:3584\n" \
"v_accvgpr_read_b32 v235, a189\n" \
"v_mfma_f32_16x16x32_bf16 v[108:111], v[28:31], v[204:207], v[108:111]\n" \
"global_load_lds_dwordx4 v228, s[44:45] offset:-4096\n" \
"v_accvgpr_read_b32 v236, a190\n" \
"v_mfma_f32_16x16x32_bf16 v[128:131], v[0:3], v[208:211], v[128:131]\n" \
"ds_read_b64_tr_b16 v[52:53], v232 offset:9728\n" \
"v_accvgpr_read_b32 v237, a191\n" \
"v_mfma_f32_16x16x32_bf16 v[128:131], v[4:7], v[212:215], v[128:131]\n" \
"ds_read_b64_tr_b16 v[54:55], v232 offset:11776\n" \
"v_cvt_pk_bf16_f32 v234, v234, v235\n" \
"v_mfma_f32_16x16x32_bf16 v[132:135], v[8:11], v[208:211], v[132:135]\n" \
"ds_read_b64_tr_b16 v[56:57], v233 offset:1536\n" \
"v_cvt_pk_bf16_f32 v236, v236, v237\n" \
"v_mfma_f32_16x16x32_bf16 v[132:135], v[12:15], v[212:215], v[132:135]\n" \
"global_load_lds_dwordx4 v229, s[44:45] offset:-3072\n" \
"v_accvgpr_write_b32 a158, v234\n" \
"v_mfma_f32_16x16x32_bf16 v[136:139], v[16:19], v[208:211], v[136:139]\n" \
"s_add_u32 s44, s44, 0x80000\n" \
"s_addc_u32 s45, s45, 0\n" \
"ds_read_b64_tr_b16 v[58:59], v233 offset:3584\n" \
"v_mfma_f32_16x16x32_bf16 v[136:139], v[20:23], v[212:215], v[136:139]\n" \
"ds_read_b64_tr_b16 v[60:61], v233 offset:9728\n" \
"v_accvgpr_write_b32 a159, v236\n" \
"v_mfma_f32_16x16x32_bf16 v[140:143], v[24:27], v[208:211], v[140:143]\n" \
"ds_read_b64_tr_b16 v[62:63], v233 offset:11776\n" \
"v_accvgpr_read_b32 v234, a64\n" \
"v_mfma_f32_16x16x32_bf16 v[140:143], v[28:31], v[212:215], v[140:143]\n" \
"s_waitcnt lgkmcnt(3)\n" \
"global_load_lds_dwordx4 v228, s[42:43] offset:-2048\n" \
"v_accvgpr_read_b32 v235, a65\n" \
"v_mfma_f32_16x16x32_bf16 v[160:163], v[0:3], v[216:219], v[160:163]\n" \
"v_add_u32 v230, 0xffff0000, v230\n" \
"v_add_u32 v231, 0xffff0000, v231\n" \
"v_mfma_f32_16x16x32_bf16 v[160:163], v[4:7], v[220:223], v[160:163]\n" \
"v_add_u32 v232, 0xffff4000, v232\n" \
"v_add_u32 v233, 0xffff4000, v233\n" \
"v_mfma_f32_16x16x32_bf16 v[164:167], v[8:11], v[216:219], v[164:167]\n" \
"v_accvgpr_read_b32 v236, a66\n" \
"v_accvgpr_read_b32 v237, a67\n" \
"v_mfma_f32_16x16x32_bf16 v[164:167], v[12:15], v[220:223], v[164:167]\n" \
"global_load_lds_dwordx4 v229, s[42:43] offset:-1024\n" \
"v_mul_f32 v234, s92, v234\n" \
"v_mfma_f32_16x16x32_bf16 v[168:171], v[16:19], v[216:219], v[168:171]\n" \
"s_add_u32 s42, s42, 0x80000\n" \
"s_addc_u32 s43, s43, 0\n" \
"v_mul_f32 v235, s92, v235\n" \
"v_mfma_f32_16x16x32_bf16 v[168:171], v[20:23], v[220:223], v[168:171]\n" \
"v_mul_f32 v236, s92, v236\n" \
"v_mul_f32 v237, s92, v237\n" \
"v_mfma_f32_16x16x32_bf16 v[172:175], v[24:27], v[216:219], v[172:175]\n" \
"s_waitcnt vmcnt(19) lgkmcnt(0)\n" \
"v_cvt_pk_bf16_f32 v234, v234, v235\n" \
"v_cvt_pk_bf16_f32 v236, v236, v237\n" \
"v_mfma_f32_16x16x32_bf16 v[172:175], v[28:31], v[220:223], v[172:175]\n" \
"s_barrier\n" \
"v_mfma_f32_16x16x32_bf16 v[80:83], v[32:35], v[192:195], v[80:83]\n" \
"ds_read_b64_tr_b16 v[0:1], v232 offset:0\n" \
"v_accvgpr_write_b32 a64, v234\n" \
"v_mfma_f32_16x16x32_bf16 v[80:83], v[36:39], v[196:199], v[80:83]\n" \
"ds_read_b64_tr_b16 v[2:3], v232 offset:2048\n" \
"v_accvgpr_write_b32 a65, v236\n" \
"v_mfma_f32_16x16x32_bf16 v[84:87], v[40:43], v[192:195], v[84:87]\n" \
"s_add_u32 m0, s87, 135168\n" \
"ds_read_b64_tr_b16 v[4:5], v232 offset:8192\n" \
"v_accvgpr_read_b32 v234, a68\n" \
"v_mfma_f32_16x16x32_bf16 v[84:87], v[44:47], v[196:199], v[84:87]\n" \
"global_load_lds_dwordx4 v224, s[48:49] offset:-4096\n" \
"v_accvgpr_read_b32 v235, a69\n" \
"v_mfma_f32_16x16x32_bf16 v[88:91], v[48:51], v[192:195], v[88:91]\n" \
"ds_read_b64_tr_b16 v[6:7], v232 offset:10240\n" \
"v_accvgpr_read_b32 v236, a70\n" \
"v_mfma_f32_16x16x32_bf16 v[88:91], v[52:55], v[196:199], v[88:91]\n" \
"ds_read_b64_tr_b16 v[8:9], v233 offset:0\n" \
"v_accvgpr_read_b32 v237, a71\n" \
"v_mfma_f32_16x16x32_bf16 v[92:95], v[56:59], v[192:195], v[92:95]\n" \
"ds_read_b64_tr_b16 v[10:11], v233 offset:2048\n" \
"v_mul_f32 v234, s92, v234\n" \
"v_mfma_f32_16x16x32_bf16 v[92:95], v[60:63], v[196:199], v[92:95]\n" \
"global_load_lds_dwordx4 v225, s[48:49] offset:-3072\n" \
"v_mul_f32 v235, s92, v235\n" \
"v_mfma_f32_16x16x32_bf16 v[112:115], v[32:35], v[200:203], v[112:115]\n" \
"ds_read_b128 v[192:195], v230 offset:0\n" \
"v_mul_f32 v236, s92, v236\n" \
"v_mfma_f32_16x16x32_bf16 v[112:115], v[36:39], v[204:207], v[112:115]\n" \
"ds_read_b128 v[196:199], v231 offset:0\n" \
"v_mul_f32 v237, s92, v237\n" \
"v_mfma_f32_16x16x32_bf16 v[116:119], v[40:43], v[200:203], v[116:119]\n" \
"ds_read_b64_tr_b16 v[12:13], v233 offset:8192\n" \
"v_cvt_pk_bf16_f32 v234, v234, v235\n" \
"v_mfma_f32_16x16x32_bf16 v[116:119], v[44:47], v[204:207], v[116:119]\n" \
"global_load_lds_dwordx4 v226, s[48:49] offset:-2048\n" \
"v_cvt_pk_bf16_f32 v236, v236, v237\n" \
"v_mfma_f32_16x16x32_bf16 v[120:123], v[48:51], v[200:203], v[120:123]\n" \
"ds_read_b64_tr_b16 v[14:15], v233 offset:10240\n" \
"v_accvgpr_write_b32 a66, v234\n" \
"v_mfma_f32_16x16x32_bf16 v[120:123], v[52:55], v[204:207], v[120:123]\n" \
"ds_read_b64_tr_b16 v[16:17], v232 offset:512\n" \
"v_accvgpr_write_b32 a67, v236\n" \
"v_mfma_f32_16x16x32_bf16 v[124:127], v[56:59], v[200:203], v[124:127]\n" \
"ds_read_b64_tr_b16 v[18:19], v232 offset:2560\n" \
"v_accvgpr_read_b32 v234, a96\n" \
"v_mfma_f32_16x16x32_bf16 v[124:127], v[60:63], v[204:207], v[124:127]\n" \
"global_load_lds_dwordx4 v227, s[48:49] offset:-1024\n" \
"v_accvgpr_read_b32 v235, a97\n" \
"v_mfma_f32_16x16x32_bf16 v[144:147], v[32:35], v[208:211], v[144:147]\n" \
"s_add_u32 s48, s48, 0x80\n" \
"s_addc_u32 s49, s49, 0\n" \
"ds_read_b128 v[200:203], v230 offset:2048\n" \
"v_mfma_f32_16x16x32_bf16 v[144:147], v[36:39], v[212:215], v[144:147]\n" \
"ds_read_b128 v[204:207], v231 offset:2048\n" \
"v_accvgpr_read_b32 v236, a98\n" \
"v_mfma_f32_16x16x32_bf16 v[148:151], v[40:43], v[208:211], v[148:151]\n" \
"ds_read_b64_tr_b16 v[20:21], v232 offset:8704\n" \
"v_accvgpr_read_b32 v237, a99\n" \
"v_mfma_f32_16x16x32_bf16 v[148:151], v[44:47], v[212:215], v[148:151]\n" \
"global_load_lds_dwordx4 v224, s[46:47] offset:0\n" \
"v_mul_f32 v234, s92, v234\n" \
"v_mfma_f32_16x16x32_bf16 v[152:155], v[48:51], v[208:211], v[152:155]\n" \
"ds_read_b64_tr_b16 v[22:23], v232 offset:10752\n" \
"v_mul_f32 v235, s92, v235\n" \
"v_mfma_f32_16x16x32_bf16 v[152:155], v[52:55], v[212:215], v[152:155]\n" \
"ds_read_b64_tr_b16 v[24:25], v233 offset:512\n" \
"v_mul_f32 v236, s92, v236\n" \
"v_mfma_f32_16x16x32_bf16 v[156:159], v[56:59], v[208:211], v[156:159]\n" \
"ds_read_b64_tr_b16 v[26:27], v233 offset:2560\n" \
"v_mul_f32 v237, s92, v237\n" \
"v_mfma_f32_16x16x32_bf16 v[156:159], v[60:63], v[212:215], v[156:159]\n" \
"global_load_lds_dwordx4 v225, s[46:47] offset:1024\n" \
"v_cvt_pk_bf16_f32 v234, v234, v235\n" \
"v_mfma_f32_16x16x32_bf16 v[176:179], v[32:35], v[216:219], v[176:179]\n" \
"ds_read_b128 v[208:211], v230 offset:4096\n" \
"v_cvt_pk_bf16_f32 v236, v236, v237\n" \
"v_mfma_f32_16x16x32_bf16 v[176:179], v[36:39], v[220:223], v[176:179]\n" \
"ds_read_b128 v[212:215], v231 offset:4096\n" \
"v_accvgpr_write_b32 a96, v234\n" \
"v_mfma_f32_16x16x32_bf16 v[180:183], v[40:43], v[216:219], v[180:183]\n" \
"ds_read_b64_tr_b16 v[28:29], v233 offset:8704\n" \
"v_accvgpr_write_b32 a97, v236\n" \
"v_mfma_f32_16x16x32_bf16 v[180:183], v[44:47], v[220:223], v[180:183]\n" \
"global_load_lds_dwordx4 v226, s[46:47] offset:2048\n" \
"v_accvgpr_read_b32 v234, a100\n" \
"v_mfma_f32_16x16x32_bf16 v[184:187], v[48:51], v[216:219], v[184:187]\n" \
"ds_read_b64_tr_b16 v[30:31], v233 offset:10752\n" \
"v_accvgpr_read_b32 v235, a101\n" \
"v_mfma_f32_16x16x32_bf16 v[184:187], v[52:55], v[220:223], v[184:187]\n" \
"v_accvgpr_read_b32 v236, a102\n" \
"v_accvgpr_read_b32 v237, a103\n" \
"v_mfma_f32_16x16x32_bf16 v[188:191], v[56:59], v[216:219], v[188:191]\n" \
"v_mul_f32 v234, s92, v234\n" \
"v_mul_f32 v235, s92, v235\n" \
"v_mfma_f32_16x16x32_bf16 v[188:191], v[60:63], v[220:223], v[188:191]\n" \
"s_waitcnt lgkmcnt(1)\n" \
"global_load_lds_dwordx4 v227, s[46:47] offset:3072\n" \
"v_mul_f32 v236, s92, v236\n" \
"v_mfma_f32_16x16x32_bf16 v[64:67], v[0:3], v[192:195], v[64:67]\n" \
"s_add_u32 s46, s46, 0x80\n" \
"s_addc_u32 s47, s47, 0\n" \
"ds_read_b128 v[216:219], v230 offset:6144\n" \
"v_mfma_f32_16x16x32_bf16 v[64:67], v[4:7], v[196:199], v[64:67]\n" \
"ds_read_b128 v[220:223], v231 offset:6144\n" \
"v_mul_f32 v237, s92, v237\n" \
"v_mfma_f32_16x16x32_bf16 v[68:71], v[8:11], v[192:195], v[68:71]\n" \
"ds_read_b64_tr_b16 v[32:33], v232 offset:1024\n" \
"v_cvt_pk_bf16_f32 v234, v234, v235\n" \
"v_mfma_f32_16x16x32_bf16 v[68:71], v[12:15], v[196:199], v[68:71]\n" \
"buffer_store_dwordx4 a[216:219], v248, s[56:59], s90 offen offset:2048 sc1\n" \
"v_cvt_pk_bf16_f32 v236, v236, v237\n" \
"v_accvgpr_write_b32 a98, v234\n" \
"v_accvgpr_write_b32 a99, v236\n" \
"v_accvgpr_read_b32 v234, a72\n" \
"v_mfma_f32_16x16x32_bf16 v[72:75], v[16:19], v[192:195], v[72:75]\n" \
"ds_read_b64_tr_b16 v[34:35], v232 offset:3072\n" \
"v_accvgpr_read_b32 v235, a73\n" \
"v_mfma_f32_16x16x32_bf16 v[72:75], v[20:23], v[196:199], v[72:75]\n" \
"s_waitcnt lgkmcnt(3)\n" \
"ds_read_b64_tr_b16 v[36:37], v232 offset:9216\n" \
"v_accvgpr_read_b32 v236, a74\n" \
"v_mfma_f32_16x16x32_bf16 v[76:79], v[24:27], v[192:195], v[76:79]\n" \
"ds_read_b64_tr_b16 v[38:39], v232 offset:11264\n" \
"v_accvgpr_read_b32 v237, a75\n" \
"v_mfma_f32_16x16x32_bf16 v[76:79], v[28:31], v[196:199], v[76:79]\n" \
"v_mul_f32 v234, s92, v234\n" \
"v_mul_f32 v235, s92, v235\n" \
"v_mfma_f32_16x16x32_bf16 v[96:99], v[0:3], v[200:203], v[96:99]\n" \
"ds_read_b64_tr_b16 v[40:41], v233 offset:1024\n" \
"v_mul_f32 v236, s92, v236\n" \
"v_mfma_f32_16x16x32_bf16 v[96:99], v[4:7], v[204:207], v[96:99]\n" \
"ds_read_b64_tr_b16 v[42:43], v233 offset:3072\n" \
"v_mul_f32 v237, s92, v237\n" \
"v_mfma_f32_16x16x32_bf16 v[100:103], v[8:11], v[200:203], v[100:103]\n" \
"ds_read_b64_tr_b16 v[44:45], v233 offset:9216\n" \
"v_cvt_pk_bf16_f32 v234, v234, v235\n" \
"v_mfma_f32_16x16x32_bf16 v[100:103], v[12:15], v[204:207], v[100:103]\n" \
"v_cvt_pk_bf16_f32 v236, v236, v237\n" \
"v_accvgpr_write_b32 a72, v234\n" \
"v_mfma_f32_16x16x32_bf16 v[104:107], v[16:19], v[200:203], v[104:107]\n" \
"ds_read_b64_tr_b16 v[46:47], v233 offset:11264\n" \
"v_accvgpr_write_b32 a73, v236\n" \
"v_mfma_f32_16x16x32_bf16 v[104:107], v[20:23], v[204:207], v[104:107]\n" \
"ds_read_b64_tr_b16 v[48:49], v232 offset:1536\n" \
"v_accvgpr_read_b32 v234, a76\n" \
"v_mfma_f32_16x16x32_bf16 v[108:111], v[24:27], v[200:203], v[108:111]\n" \
"s_add_u32 m0, s95, 53248\n" \
"ds_read_b64_tr_b16 v[50:51], v232 offset:3584\n" \
"v_accvgpr_read_b32 v235, a77\n" \
"v_mfma_f32_16x16x32_bf16 v[108:111], v[28:31], v[204:207], v[108:111]\n" \
"global_load_lds_dwordx4 v228, s[44:45] offset:-4096\n" \
"v_accvgpr_read_b32 v236, a78\n" \
"v_mfma_f32_16x16x32_bf16 v[128:131], v[0:3], v[208:211], v[128:131]\n" \
"ds_read_b64_tr_b16 v[52:53], v232 offset:9728\n" \
"v_accvgpr_read_b32 v237, a79\n" \
"v_mfma_f32_16x16x32_bf16 v[128:131], v[4:7], v[212:215], v[128:131]\n" \
"ds_read_b64_tr_b16 v[54:55], v232 offset:11776\n" \
"v_mul_f32 v234, s92, v234\n" \
"v_mfma_f32_16x16x32_bf16 v[132:135], v[8:11], v[208:211], v[132:135]\n" \
"ds_read_b64_tr_b16 v[56:57], v233 offset:1536\n" \
"v_mul_f32 v235, s92, v235\n" \
"v_mfma_f32_16x16x32_bf16 v[132:135], v[12:15], v[212:215], v[132:135]\n" \
"global_load_lds_dwordx4 v229, s[44:45] offset:-3072\n" \
"v_mul_f32 v236, s92, v236\n" \
"v_mfma_f32_16x16x32_bf16 v[136:139], v[16:19], v[208:211], v[136:139]\n" \
"s_add_u32 s44, s44, 0x80000\n" \
"s_addc_u32 s45, s45, 0\n" \
"ds_read_b64_tr_b16 v[58:59], v233 offset:3584\n" \
"v_mfma_f32_16x16x32_bf16 v[136:139], v[20:23], v[212:215], v[136:139]\n" \
"ds_read_b64_tr_b16 v[60:61], v233 offset:9728\n" \
"v_mul_f32 v237, s92, v237\n" \
"v_mfma_f32_16x16x32_bf16 v[140:143], v[24:27], v[208:211], v[140:143]\n" \
"ds_read_b64_tr_b16 v[62:63], v233 offset:11776\n" \
"v_cvt_pk_bf16_f32 v234, v234, v235\n" \
"v_mfma_f32_16x16x32_bf16 v[140:143], v[28:31], v[212:215], v[140:143]\n" \
"s_waitcnt lgkmcnt(3)\n" \
"global_load_lds_dwordx4 v228, s[42:43] offset:-2048\n" \
"v_cvt_pk_bf16_f32 v236, v236, v237\n" \
"v_mfma_f32_16x16x32_bf16 v[160:163], v[0:3], v[216:219], v[160:163]\n" \
"v_add_u32 v230, 0x8000, v230\n" \
"v_add_u32 v231, 0x8000, v231\n" \
"v_mfma_f32_16x16x32_bf16 v[160:163], v[4:7], v[220:223], v[160:163]\n" \
"v_add_u32 v232, 0x4000, v232\n" \
"v_add_u32 v233, 0x4000, v233\n" \
"v_mfma_f32_16x16x32_bf16 v[164:167], v[8:11], v[216:219], v[164:167]\n" \
"v_accvgpr_write_b32 a74, v234\n" \
"v_accvgpr_write_b32 a75, v236\n" \
"v_mfma_f32_16x16x32_bf16 v[164:167], v[12:15], v[220:223], v[164:167]\n" \
"global_load_lds_dwordx4 v229, s[42:43] offset:-1024\n" \
"v_accvgpr_read_b32 v234, a104\n" \
"v_mfma_f32_16x16x32_bf16 v[168:171], v[16:19], v[216:219], v[168:171]\n" \
"s_add_u32 s42, s42, 0x80000\n" \
"s_addc_u32 s43, s43, 0\n" \
"v_accvgpr_read_b32 v235, a105\n" \
"v_mfma_f32_16x16x32_bf16 v[168:171], v[20:23], v[220:223], v[168:171]\n" \
"v_accvgpr_read_b32 v236, a106\n" \
"v_accvgpr_read_b32 v237, a107\n" \
"v_mfma_f32_16x16x32_bf16 v[172:175], v[24:27], v[216:219], v[172:175]\n" \
"s_waitcnt vmcnt(18) lgkmcnt(0)\n" \
"v_mul_f32 v234, s92, v234\n" \
"v_mul_f32 v235, s92, v235\n" \
"v_mfma_f32_16x16x32_bf16 v[172:175], v[28:31], v[220:223], v[172:175]\n" \
"s_barrier\n" \
"v_mfma_f32_16x16x32_bf16 v[80:83], v[32:35], v[192:195], v[80:83]\n" \
"ds_read_b64_tr_b16 v[0:1], v232 offset:0\n" \
"v_mul_f32 v236, s92, v236\n" \
"v_mfma_f32_16x16x32_bf16 v[80:83], v[36:39], v[196:199], v[80:83]\n" \
"ds_read_b64_tr_b16 v[2:3], v232 offset:2048\n" \
"v_mul_f32 v237, s92, v237\n" \
"v_mfma_f32_16x16x32_bf16 v[84:87], v[40:43], v[192:195], v[84:87]\n" \
"s_add_u32 m0, s87, 69632\n" \
"ds_read_b64_tr_b16 v[4:5], v232 offset:8192\n" \
"v_cvt_pk_bf16_f32 v234, v234, v235\n" \
"v_mfma_f32_16x16x32_bf16 v[84:87], v[44:47], v[196:199], v[84:87]\n" \
"global_load_lds_dwordx4 v224, s[48:49] offset:-4096\n" \
"v_cvt_pk_bf16_f32 v236, v236, v237\n" \
"v_mfma_f32_16x16x32_bf16 v[88:91], v[48:51], v[192:195], v[88:91]\n" \
"ds_read_b64_tr_b16 v[6:7], v232 offset:10240\n" \
"v_accvgpr_write_b32 a104, v234\n" \
"v_mfma_f32_16x16x32_bf16 v[88:91], v[52:55], v[196:199], v[88:91]\n" \
"ds_read_b64_tr_b16 v[8:9], v233 offset:0\n" \
"v_accvgpr_write_b32 a105, v236\n" \
"v_mfma_f32_16x16x32_bf16 v[92:95], v[56:59], v[192:195], v[92:95]\n" \
"ds_read_b64_tr_b16 v[10:11], v233 offset:2048\n" \
"v_accvgpr_read_b32 v234, a108\n" \
"v_mfma_f32_16x16x32_bf16 v[92:95], v[60:63], v[196:199], v[92:95]\n" \
"global_load_lds_dwordx4 v225, s[48:49] offset:-3072\n" \
"v_accvgpr_read_b32 v235, a109\n" \
"v_mfma_f32_16x16x32_bf16 v[112:115], v[32:35], v[200:203], v[112:115]\n" \
"ds_read_b128 v[192:195], v230 offset:0\n" \
"v_accvgpr_read_b32 v236, a110\n" \
"v_mfma_f32_16x16x32_bf16 v[112:115], v[36:39], v[204:207], v[112:115]\n" \
"ds_read_b128 v[196:199], v231 offset:0\n" \
"v_accvgpr_read_b32 v237, a111\n" \
"v_mfma_f32_16x16x32_bf16 v[116:119], v[40:43], v[200:203], v[116:119]\n" \
"ds_read_b64_tr_b16 v[12:13], v233 offset:8192\n" \
"v_mul_f32 v234, s92, v234\n" \
"v_mfma_f32_16x16x32_bf16 v[116:119], v[44:47], v[204:207], v[116:119]\n" \
"global_load_lds_dwordx4 v226, s[48:49] offset:-2048\n" \
"v_mul_f32 v235, s92, v235\n" \
"v_mfma_f32_16x16x32_bf16 v[120:123], v[48:51], v[200:203], v[120:123]\n" \
"ds_read_b64_tr_b16 v[14:15], v233 offset:10240\n" \
"v_mul_f32 v236, s92, v236\n" \
"v_mfma_f32_16x16x32_bf16 v[120:123], v[52:55], v[204:207], v[120:123]\n" \
"ds_read_b64_tr_b16 v[16:17], v232 offset:512\n" \
"v_mul_f32 v237, s92, v237\n" \
"v_mfma_f32_16x16x32_bf16 v[124:127], v[56:59], v[200:203], v[124:127]\n" \
"ds_read_b64_tr_b16 v[18:19], v232 offset:2560\n" \
"v_cvt_pk_bf16_f32 v234, v234, v235\n" \
"v_mfma_f32_16x16x32_bf16 v[124:127], v[60:63], v[204:207], v[124:127]\n" \
"global_load_lds_dwordx4 v227, s[48:49] offset:-1024\n" \
"v_cvt_pk_bf16_f32 v236, v236, v237\n" \
"v_mfma_f32_16x16x32_bf16 v[144:147], v[32:35], v[208:211], v[144:147]\n" \
"s_add_u32 s48, s48, 0x80\n" \
"s_addc_u32 s49, s49, 0\n" \
"ds_read_b128 v[200:203], v230 offset:2048\n" \
"v_mfma_f32_16x16x32_bf16 v[144:147], v[36:39], v[212:215], v[144:147]\n" \
"ds_read_b128 v[204:207], v231 offset:2048\n" \
"v_accvgpr_write_b32 a106, v234\n" \
"v_mfma_f32_16x16x32_bf16 v[148:151], v[40:43], v[208:211], v[148:151]\n" \
"ds_read_b64_tr_b16 v[20:21], v232 offset:8704\n" \
"v_accvgpr_write_b32 a107, v236\n" \
"v_mfma_f32_16x16x32_bf16 v[148:151], v[44:47], v[212:215], v[148:151]\n" \
"global_load_lds_dwordx4 v224, s[46:47] offset:0\n" \
"v_accvgpr_read_b32 v234, a80\n" \
"v_mfma_f32_16x16x32_bf16 v[152:155], v[48:51], v[208:211], v[152:155]\n" \
"ds_read_b64_tr_b16 v[22:23], v232 offset:10752\n" \
"v_accvgpr_read_b32 v235, a81\n" \
"v_mfma_f32_16x16x32_bf16 v[152:155], v[52:55], v[212:215], v[152:155]\n" \
"ds_read_b64_tr_b16 v[24:25], v233 offset:512\n" \
"v_accvgpr_read_b32 v236, a82\n" \
"v_mfma_f32_16x16x32_bf16 v[156:159], v[56:59], v[208:211], v[156:159]\n" \
"ds_read_b64_tr_b16 v[26:27], v233 offset:2560\n" \
"v_accvgpr_read_b32 v237, a83\n" \
"v_mfma_f32_16x16x32_bf16 v[156:159], v[60:63], v[212:215], v[156:159]\n" \
"global_load_lds_dwordx4 v225, s[46:47] offset:1024\n" \
"v_cvt_pk_bf16_f32 v234, v234, v235\n" \
"v_mfma_f32_16x16x32_bf16 v[176:179], v[32:35], v[216:219], v[176:179]\n" \
"ds_read_b128 v[208:211], v230 offset:4096\n" \
"v_cvt_pk_bf16_f32 v236, v236, v237\n" \
"v_mfma_f32_16x16x32_bf16 v[176:179], v[36:39], v[220:223], v[176:179]\n" \
"ds_read_b128 v[212:215], v231 offset:4096\n" \
"v_accvgpr_write_b32 a80, v234\n" \
"v_mfma_f32_16x16x32_bf16 v[180:183], v[40:43], v[216:219], v[180:183]\n" \
"ds_read_b64_tr_b16 v[28:29], v233 offset:8704\n" \
"v_accvgpr_write_b32 a81, v236\n" \
"v_mfma_f32_16x16x32_bf16 v[180:183], v[44:47], v[220:223], v[180:183]\n" \
"global_load_lds_dwordx4 v226, s[46:47] offset:2048\n" \
"v_accvgpr_read_b32 v234, a112\n" \
"v_mfma_f32_16x16x32_bf16 v[184:187], v[48:51], v[216:219], v[184:187]\n" \
"ds_read_b64_tr_b16 v[30:31], v233 offset:10752\n" \
"v_accvgpr_read_b32 v235, a113\n" \
"v_mfma_f32_16x16x32_bf16 v[184:187], v[52:55], v[220:223], v[184:187]\n" \
"v_accvgpr_read_b32 v236, a114\n" \
"v_accvgpr_read_b32 v237, a115\n" \
"v_mfma_f32_16x16x32_bf16 v[188:191], v[56:59], v[216:219], v[188:191]\n" \
"v_cvt_pk_bf16_f32 v234, v234, v235\n" \
"v_cvt_pk_bf16_f32 v236, v236, v237\n" \
"v_mfma_f32_16x16x32_bf16 v[188:191], v[60:63], v[220:223], v[188:191]\n" \
"s_waitcnt lgkmcnt(1)\n" \
"global_load_lds_dwordx4 v227, s[46:47] offset:3072\n" \
"v_accvgpr_write_b32 a82, v234\n" \
"v_mfma_f32_16x16x32_bf16 v[64:67], v[0:3], v[192:195], v[64:67]\n" \
"s_add_u32 s46, s46, 0x80\n" \
"s_addc_u32 s47, s47, 0\n" \
"ds_read_b128 v[216:219], v230 offset:6144\n" \
"v_mfma_f32_16x16x32_bf16 v[64:67], v[4:7], v[196:199], v[64:67]\n" \
"ds_read_b128 v[220:223], v231 offset:6144\n" \
"v_accvgpr_write_b32 a83, v236\n" \
"v_mfma_f32_16x16x32_bf16 v[68:71], v[8:11], v[192:195], v[68:71]\n" \
"ds_read_b64_tr_b16 v[32:33], v232 offset:1024\n" \
"v_accvgpr_read_b32 v234, a84\n" \
"v_mfma_f32_16x16x32_bf16 v[68:71], v[12:15], v[196:199], v[68:71]\n" \
"buffer_store_dwordx4 a[220:223], v248, s[56:59], s90 offen offset:3072 sc1\n" \
"v_accvgpr_read_b32 v235, a85\n" \
"v_accvgpr_read_b32 v236, a86\n" \
"v_accvgpr_read_b32 v237, a87\n" \
"v_cvt_pk_bf16_f32 v234, v234, v235\n" \
"v_mfma_f32_16x16x32_bf16 v[72:75], v[16:19], v[192:195], v[72:75]\n" \
"s_add_u32 s90, s88, 139264\n" \
"ds_read_b64_tr_b16 v[34:35], v232 offset:3072\n" \
"v_cvt_pk_bf16_f32 v236, v236, v237\n" \
"v_mfma_f32_16x16x32_bf16 v[72:75], v[20:23], v[196:199], v[72:75]\n" \
"s_waitcnt lgkmcnt(3)\n" \
"ds_read_b64_tr_b16 v[36:37], v232 offset:9216\n" \
"v_accvgpr_write_b32 a84, v234\n" \
"v_mfma_f32_16x16x32_bf16 v[76:79], v[24:27], v[192:195], v[76:79]\n" \
"ds_read_b64_tr_b16 v[38:39], v232 offset:11264\n" \
"v_accvgpr_write_b32 a85, v236\n" \
"v_mfma_f32_16x16x32_bf16 v[76:79], v[28:31], v[196:199], v[76:79]\n" \
"buffer_store_dwordx4 a[128:131], v249, s[56:59], s90 offen offset:0 sc1\n" \
"v_accvgpr_read_b32 v234, a116\n" \
"v_accvgpr_read_b32 v235, a117\n" \
"v_accvgpr_read_b32 v236, a118\n" \
"v_accvgpr_read_b32 v237, a119\n" \
"v_mfma_f32_16x16x32_bf16 v[96:99], v[0:3], v[200:203], v[96:99]\n" \
"ds_read_b64_tr_b16 v[40:41], v233 offset:1024\n" \
"v_cvt_pk_bf16_f32 v234, v234, v235\n" \
"v_mfma_f32_16x16x32_bf16 v[96:99], v[4:7], v[204:207], v[96:99]\n" \
"ds_read_b64_tr_b16 v[42:43], v233 offset:3072\n" \
"v_cvt_pk_bf16_f32 v236, v236, v237\n" \
"v_mfma_f32_16x16x32_bf16 v[100:103], v[8:11], v[200:203], v[100:103]\n" \
"ds_read_b64_tr_b16 v[44:45], v233 offset:9216\n" \
"v_accvgpr_write_b32 a86, v234\n" \
"v_mfma_f32_16x16x32_bf16 v[100:103], v[12:15], v[204:207], v[100:103]\n" \
"v_accvgpr_write_b32 a87, v236\n" \
"v_accvgpr_read_b32 v234, a88\n" \
"v_mfma_f32_16x16x32_bf16 v[104:107], v[16:19], v[200:203], v[104:107]\n" \
"ds_read_b64_tr_b16 v[46:47], v233 offset:11264\n" \
"v_accvgpr_read_b32 v235, a89\n" \
"v_mfma_f32_16x16x32_bf16 v[104:107], v[20:23], v[204:207], v[104:107]\n" \
"ds_read_b64_tr_b16 v[48:49], v232 offset:1536\n" \
"v_accvgpr_read_b32 v236, a90\n" \
"v_mfma_f32_16x16x32_bf16 v[108:111], v[24:27], v[200:203], v[108:111]\n" \
"s_add_u32 m0, s95, 4096\n" \
"ds_read_b64_tr_b16 v[50:51], v232 offset:3584\n" \
"v_accvgpr_read_b32 v237, a91\n" \
"v_mfma_f32_16x16x32_bf16 v[108:111], v[28:31], v[204:207], v[108:111]\n" \
"global_load_lds_dwordx4 v228, s[44:45] offset:-4096\n" \
"v_cvt_pk_bf16_f32 v234, v234, v235\n" \
"v_mfma_f32_16x16x32_bf16 v[128:131], v[0:3], v[208:211], v[128:131]\n" \
"ds_read_b64_tr_b16 v[52:53], v232 offset:9728\n" \
"v_cvt_pk_bf16_f32 v236, v236, v237\n" \
"v_mfma_f32_16x16x32_bf16 v[128:131], v[4:7], v[212:215], v[128:131]\n" \
"ds_read_b64_tr_b16 v[54:55], v232 offset:11776\n" \
"v_accvgpr_write_b32 a88, v234\n" \
"v_mfma_f32_16x16x32_bf16 v[132:135], v[8:11], v[208:211], v[132:135]\n" \
"ds_read_b64_tr_b16 v[56:57], v233 offset:1536\n" \
"v_accvgpr_write_b32 a89, v236\n" \
"v_mfma_f32_16x16x32_bf16 v[132:135], v[12:15], v[212:215], v[132:135]\n" \
"global_load_lds_dwordx4 v229, s[44:45] offset:-3072\n" \
"v_accvgpr_read_b32 v234, a120\n" \
"v_mfma_f32_16x16x32_bf16 v[136:139], v[16:19], v[208:211], v[136:139]\n" \
"s_add_u32 s44, s44, 0x80000\n" \
"s_addc_u32 s45, s45, 0\n" \
"ds_read_b64_tr_b16 v[58:59], v233 offset:3584\n" \
"v_mfma_f32_16x16x32_bf16 v[136:139], v[20:23], v[212:215], v[136:139]\n" \
"ds_read_b64_tr_b16 v[60:61], v233 offset:9728\n" \
"v_accvgpr_read_b32 v235, a121\n" \
"v_mfma_f32_16x16x32_bf16 v[140:143], v[24:27], v[208:211], v[140:143]\n" \
"ds_read_b64_tr_b16 v[62:63], v233 offset:11776\n" \
"v_accvgpr_read_b32 v236, a122\n" \
"v_mfma_f32_16x16x32_bf16 v[140:143], v[28:31], v[212:215], v[140:143]\n" \
"s_waitcnt lgkmcnt(3)\n" \
"global_load_lds_dwordx4 v228, s[42:43] offset:-2048\n" \
"v_accvgpr_read_b32 v237, a123\n" \
"v_mfma_f32_16x16x32_bf16 v[160:163], v[0:3], v[216:219], v[160:163]\n" \
"v_add_u32 v230, 0x8000, v230\n" \
"v_add_u32 v231, 0x8000, v231\n" \
"v_mfma_f32_16x16x32_bf16 v[160:163], v[4:7], v[220:223], v[160:163]\n" \
"v_add_u32 v232, 0x4000, v232\n" \
"v_add_u32 v233, 0x4000, v233\n" \
"v_mfma_f32_16x16x32_bf16 v[164:167], v[8:11], v[216:219], v[164:167]\n" \
"v_cvt_pk_bf16_f32 v234, v234, v235\n" \
"v_cvt_pk_bf16_f32 v236, v236, v237\n" \
"v_mfma_f32_16x16x32_bf16 v[164:167], v[12:15], v[220:223], v[164:167]\n" \
"global_load_lds_dwordx4 v229, s[42:43] offset:-1024\n" \
"v_accvgpr_write_b32 a90, v234\n" \
"v_mfma_f32_16x16x32_bf16 v[168:171], v[16:19], v[216:219], v[168:171]\n" \
"s_add_u32 s42, s42, 0x80000\n" \
"s_addc_u32 s43, s43, 0\n" \
"v_accvgpr_write_b32 a91, v236\n" \
"v_mfma_f32_16x16x32_bf16 v[168:171], v[20:23], v[220:223], v[168:171]\n" \
"v_accvgpr_read_b32 v234, a92\n" \
"v_accvgpr_read_b32 v235, a93\n" \
"v_mfma_f32_16x16x32_bf16 v[172:175], v[24:27], v[216:219], v[172:175]\n" \
"s_waitcnt vmcnt(19) lgkmcnt(0)\n" \
"v_accvgpr_read_b32 v236, a94\n" \
"v_accvgpr_read_b32 v237, a95\n" \
"v_mfma_f32_16x16x32_bf16 v[172:175], v[28:31], v[220:223], v[172:175]\n" \
"s_barrier\n" \
"v_mfma_f32_16x16x32_bf16 v[80:83], v[32:35], v[192:195], v[80:83]\n" \
"ds_read_b64_tr_b16 v[0:1], v232 offset:0\n" \
"v_cvt_pk_bf16_f32 v234, v234, v235\n" \
"v_mfma_f32_16x16x32_bf16 v[80:83], v[36:39], v[196:199], v[80:83]\n" \
"ds_read_b64_tr_b16 v[2:3], v232 offset:2048\n" \
"v_cvt_pk_bf16_f32 v236, v236, v237\n" \
"v_mfma_f32_16x16x32_bf16 v[84:87], v[40:43], v[192:195], v[84:87]\n" \
"s_add_u32 m0, s87, 102400\n" \
"ds_read_b64_tr_b16 v[4:5], v232 offset:8192\n" \
"v_accvgpr_write_b32 a92, v234\n" \
"v_mfma_f32_16x16x32_bf16 v[84:87], v[44:47], v[196:199], v[84:87]\n" \
"global_load_lds_dwordx4 v224, s[48:49] offset:-4096\n" \
"v_accvgpr_write_b32 a93, v236\n" \
"v_mfma_f32_16x16x32_bf16 v[88:91], v[48:51], v[192:195], v[88:91]\n" \
"ds_read_b64_tr_b16 v[6:7], v232 offset:10240\n" \
"v_accvgpr_read_b32 v234, a124\n" \
"v_mfma_f32_16x16x32_bf16 v[88:91], v[52:55], v[196:199], v[88:91]\n" \
"ds_read_b64_tr_b16 v[8:9], v233 offset:0\n" \
"v_accvgpr_read_b32 v235, a125\n" \
"v_mfma_f32_16x16x32_bf16 v[92:95], v[56:59], v[192:195], v[92:95]\n" \
"Lpg3268_%=:\n" \
"s_cbranch_execz Lpg5362_%=\n" \
"ds_read_b64_tr_b16 v[10:11], v233 offset:2048\n" \
"v_accvgpr_read_b32 v236, a126\n" \
"v_mfma_f32_16x16x32_bf16 v[92:95], v[60:63], v[196:199], v[92:95]\n" \
"global_load_lds_dwordx4 v225, s[48:49] offset:-3072\n" \
"v_accvgpr_read_b32 v237, a127\n" \
"v_mfma_f32_16x16x32_bf16 v[112:115], v[32:35], v[200:203], v[112:115]\n" \
"ds_read_b128 v[192:195], v230 offset:0\n" \
"v_cvt_pk_bf16_f32 v234, v234, v235\n" \
"v_mfma_f32_16x16x32_bf16 v[112:115], v[36:39], v[204:207], v[112:115]\n" \
"ds_read_b128 v[196:199], v231 offset:0\n" \
"v_cvt_pk_bf16_f32 v236, v236, v237\n" \
"v_mfma_f32_16x16x32_bf16 v[116:119], v[40:43], v[200:203], v[116:119]\n" \
"ds_read_b64_tr_b16 v[12:13], v233 offset:8192\n" \
"v_accvgpr_write_b32 a94, v234\n" \
"v_mfma_f32_16x16x32_bf16 v[116:119], v[44:47], v[204:207], v[116:119]\n" \
"global_load_lds_dwordx4 v226, s[48:49] offset:-2048\n" \
"v_accvgpr_write_b32 a95, v236\n" \
"v_mfma_f32_16x16x32_bf16 v[120:123], v[48:51], v[200:203], v[120:123]\n" \
"ds_read_b64_tr_b16 v[14:15], v233 offset:10240\n" \
"v_accvgpr_read_b32 v234, a0\n" \
"v_mfma_f32_16x16x32_bf16 v[120:123], v[52:55], v[204:207], v[120:123]\n" \
"ds_read_b64_tr_b16 v[16:17], v232 offset:512\n" \
"v_accvgpr_read_b32 v235, a1\n" \
"v_mfma_f32_16x16x32_bf16 v[124:127], v[56:59], v[200:203], v[124:127]\n" \
"ds_read_b64_tr_b16 v[18:19], v232 offset:2560\n" \
"v_accvgpr_read_b32 v236, a2\n" \
"v_mfma_f32_16x16x32_bf16 v[124:127], v[60:63], v[204:207], v[124:127]\n" \
"global_load_lds_dwordx4 v227, s[48:49] offset:-1024\n" \
"v_accvgpr_read_b32 v237, a3\n" \
"v_mfma_f32_16x16x32_bf16 v[144:147], v[32:35], v[208:211], v[144:147]\n" \
"s_add_u32 s48, s48, 0x80\n" \
"s_addc_u32 s49, s49, 0\n" \
"ds_read_b128 v[200:203], v230 offset:2048\n" \
"v_mfma_f32_16x16x32_bf16 v[144:147], v[36:39], v[212:215], v[144:147]\n" \
"ds_read_b128 v[204:207], v231 offset:2048\n" \
"v_mul_f32 v234, s92, v234\n" \
"v_mfma_f32_16x16x32_bf16 v[148:151], v[40:43], v[208:211], v[148:151]\n" \
"ds_read_b64_tr_b16 v[20:21], v232 offset:8704\n" \
"v_mul_f32 v235, s92, v235\n" \
"v_mfma_f32_16x16x32_bf16 v[148:151], v[44:47], v[212:215], v[148:151]\n" \
"global_load_lds_dwordx4 v224, s[46:47] offset:0\n" \
"v_mul_f32 v236, s92, v236\n" \
"v_mfma_f32_16x16x32_bf16 v[152:155], v[48:51], v[208:211], v[152:155]\n" \
"ds_read_b64_tr_b16 v[22:23], v232 offset:10752\n" \
"v_mul_f32 v237, s92, v237\n" \
"v_mfma_f32_16x16x32_bf16 v[152:155], v[52:55], v[212:215], v[152:155]\n" \
"ds_read_b64_tr_b16 v[24:25], v233 offset:512\n" \
"v_cvt_pk_bf16_f32 v234, v234, v235\n" \
"v_mfma_f32_16x16x32_bf16 v[156:159], v[56:59], v[208:211], v[156:159]\n" \
"ds_read_b64_tr_b16 v[26:27], v233 offset:2560\n" \
"v_cvt_pk_bf16_f32 v236, v236, v237\n" \
"v_mfma_f32_16x16x32_bf16 v[156:159], v[60:63], v[212:215], v[156:159]\n" \
"global_load_lds_dwordx4 v225, s[46:47] offset:1024\n" \
"v_accvgpr_write_b32 a0, v234\n" \
"v_mfma_f32_16x16x32_bf16 v[176:179], v[32:35], v[216:219], v[176:179]\n" \
"ds_read_b128 v[208:211], v230 offset:4096\n" \
"v_accvgpr_write_b32 a1, v236\n" \
"v_mfma_f32_16x16x32_bf16 v[176:179], v[36:39], v[220:223], v[176:179]\n" \
"ds_read_b128 v[212:215], v231 offset:4096\n" \
"v_accvgpr_read_b32 v234, a4\n" \
"v_mfma_f32_16x16x32_bf16 v[180:183], v[40:43], v[216:219], v[180:183]\n" \
"ds_read_b64_tr_b16 v[28:29], v233 offset:8704\n" \
"v_accvgpr_read_b32 v235, a5\n" \
"v_mfma_f32_16x16x32_bf16 v[180:183], v[44:47], v[220:223], v[180:183]\n" \
"global_load_lds_dwordx4 v226, s[46:47] offset:2048\n" \
"v_accvgpr_read_b32 v236, a6\n" \
"v_mfma_f32_16x16x32_bf16 v[184:187], v[48:51], v[216:219], v[184:187]\n" \
"ds_read_b64_tr_b16 v[30:31], v233 offset:10752\n" \
"v_accvgpr_read_b32 v237, a7\n" \
"v_mfma_f32_16x16x32_bf16 v[184:187], v[52:55], v[220:223], v[184:187]\n" \
"v_mul_f32 v234, s92, v234\n" \
"v_mul_f32 v235, s92, v235\n" \
"v_mfma_f32_16x16x32_bf16 v[188:191], v[56:59], v[216:219], v[188:191]\n" \
"v_mul_f32 v236, s92, v236\n" \
"v_mul_f32 v237, s92, v237\n" \
"v_mfma_f32_16x16x32_bf16 v[188:191], v[60:63], v[220:223], v[188:191]\n" \
"s_waitcnt lgkmcnt(1)\n" \
"global_load_lds_dwordx4 v227, s[46:47] offset:3072\n" \
"v_cvt_pk_bf16_f32 v234, v234, v235\n" \
"v_mfma_f32_16x16x32_bf16 v[64:67], v[0:3], v[192:195], v[64:67]\n" \
"s_add_u32 s46, s46, 0x80\n" \
"s_addc_u32 s47, s47, 0\n" \
"ds_read_b128 v[216:219], v230 offset:6144\n" \
"v_mfma_f32_16x16x32_bf16 v[64:67], v[4:7], v[196:199], v[64:67]\n" \
"ds_read_b128 v[220:223], v231 offset:6144\n" \
"v_cvt_pk_bf16_f32 v236, v236, v237\n" \
"v_mfma_f32_16x16x32_bf16 v[68:71], v[8:11], v[192:195], v[68:71]\n" \
"ds_read_b64_tr_b16 v[32:33], v232 offset:1024\n" \
"v_accvgpr_write_b32 a2, v234\n" \
"v_mfma_f32_16x16x32_bf16 v[68:71], v[12:15], v[196:199], v[68:71]\n" \
"buffer_store_dwordx4 a[160:163], v249, s[56:59], s90 offen offset:256 sc1\n" \
"v_accvgpr_write_b32 a3, v236\n" \
"v_accvgpr_read_b32 v234, a32\n" \
"v_accvgpr_read_b32 v235, a33\n" \
"v_accvgpr_read_b32 v236, a34\n" \
"v_mfma_f32_16x16x32_bf16 v[72:75], v[16:19], v[192:195], v[72:75]\n" \
"ds_read_b64_tr_b16 v[34:35], v232 offset:3072\n" \
"v_accvgpr_read_b32 v237, a35\n" \
"v_mfma_f32_16x16x32_bf16 v[72:75], v[20:23], v[196:199], v[72:75]\n" \
"s_waitcnt lgkmcnt(3)\n" \
"ds_read_b64_tr_b16 v[36:37], v232 offset:9216\n" \
"v_mul_f32 v234, s92, v234\n" \
"v_mfma_f32_16x16x32_bf16 v[76:79], v[24:27], v[192:195], v[76:79]\n" \
"ds_read_b64_tr_b16 v[38:39], v232 offset:11264\n" \
"v_mul_f32 v235, s92, v235\n" \
"v_mfma_f32_16x16x32_bf16 v[76:79], v[28:31], v[196:199], v[76:79]\n" \
"v_mul_f32 v236, s92, v236\n" \
"v_mul_f32 v237, s92, v237\n" \
"v_mfma_f32_16x16x32_bf16 v[96:99], v[0:3], v[200:203], v[96:99]\n" \
"ds_read_b64_tr_b16 v[40:41], v233 offset:1024\n" \
"v_cvt_pk_bf16_f32 v234, v234, v235\n" \
"v_mfma_f32_16x16x32_bf16 v[96:99], v[4:7], v[204:207], v[96:99]\n" \
"ds_read_b64_tr_b16 v[42:43], v233 offset:3072\n" \
"v_cvt_pk_bf16_f32 v236, v236, v237\n" \
"v_mfma_f32_16x16x32_bf16 v[100:103], v[8:11], v[200:203], v[100:103]\n" \
"ds_read_b64_tr_b16 v[44:45], v233 offset:9216\n" \
"v_accvgpr_write_b32 a32, v234\n" \
"v_mfma_f32_16x16x32_bf16 v[100:103], v[12:15], v[204:207], v[100:103]\n" \
"v_accvgpr_write_b32 a33, v236\n" \
"v_accvgpr_read_b32 v234, a36\n" \
"v_mfma_f32_16x16x32_bf16 v[104:107], v[16:19], v[200:203], v[104:107]\n" \
"ds_read_b64_tr_b16 v[46:47], v233 offset:11264\n" \
"v_accvgpr_read_b32 v235, a37\n" \
"v_mfma_f32_16x16x32_bf16 v[104:107], v[20:23], v[204:207], v[104:107]\n" \
"ds_read_b64_tr_b16 v[48:49], v232 offset:1536\n" \
"v_accvgpr_read_b32 v236, a38\n" \
"v_mfma_f32_16x16x32_bf16 v[108:111], v[24:27], v[200:203], v[108:111]\n" \
"s_add_u32 m0, s95, 20480\n" \
"ds_read_b64_tr_b16 v[50:51], v232 offset:3584\n" \
"v_accvgpr_read_b32 v237, a39\n" \
"v_mfma_f32_16x16x32_bf16 v[108:111], v[28:31], v[204:207], v[108:111]\n" \
"global_load_lds_dwordx4 v228, s[44:45] offset:-4096\n" \
"v_mul_f32 v234, s92, v234\n" \
"v_mfma_f32_16x16x32_bf16 v[128:131], v[0:3], v[208:211], v[128:131]\n" \
"ds_read_b64_tr_b16 v[52:53], v232 offset:9728\n" \
"v_mul_f32 v235, s92, v235\n" \
"v_mfma_f32_16x16x32_bf16 v[128:131], v[4:7], v[212:215], v[128:131]\n" \
"ds_read_b64_tr_b16 v[54:55], v232 offset:11776\n" \
"v_mul_f32 v236, s92, v236\n" \
"v_mfma_f32_16x16x32_bf16 v[132:135], v[8:11], v[208:211], v[132:135]\n" \
"ds_read_b64_tr_b16 v[56:57], v233 offset:1536\n" \
"v_mul_f32 v237, s92, v237\n" \
"v_mfma_f32_16x16x32_bf16 v[132:135], v[12:15], v[212:215], v[132:135]\n" \
"global_load_lds_dwordx4 v229, s[44:45] offset:-3072\n" \
"v_cvt_pk_bf16_f32 v234, v234, v235\n" \
"v_mfma_f32_16x16x32_bf16 v[136:139], v[16:19], v[208:211], v[136:139]\n" \
"s_add_u32 s44, s44, 0x80000\n" \
"s_addc_u32 s45, s45, 0\n" \
"ds_read_b64_tr_b16 v[58:59], v233 offset:3584\n" \
"v_mfma_f32_16x16x32_bf16 v[136:139], v[20:23], v[212:215], v[136:139]\n" \
"ds_read_b64_tr_b16 v[60:61], v233 offset:9728\n" \
"v_cvt_pk_bf16_f32 v236, v236, v237\n" \
"v_mfma_f32_16x16x32_bf16 v[140:143], v[24:27], v[208:211], v[140:143]\n" \
"ds_read_b64_tr_b16 v[62:63], v233 offset:11776\n" \
"v_accvgpr_write_b32 a34, v234\n" \
"v_mfma_f32_16x16x32_bf16 v[140:143], v[28:31], v[212:215], v[140:143]\n" \
"s_waitcnt lgkmcnt(3)\n" \
"global_load_lds_dwordx4 v228, s[42:43] offset:-2048\n" \
"v_accvgpr_write_b32 a35, v236\n" \
"v_mfma_f32_16x16x32_bf16 v[160:163], v[0:3], v[216:219], v[160:163]\n" \
"v_add_u32 v230, 0xffff0000, v230\n" \
"v_add_u32 v231, 0xffff0000, v231\n" \
"v_mfma_f32_16x16x32_bf16 v[160:163], v[4:7], v[220:223], v[160:163]\n" \
"v_add_u32 v232, 0x4000, v232\n" \
"v_add_u32 v233, 0x4000, v233\n" \
"v_mfma_f32_16x16x32_bf16 v[164:167], v[8:11], v[216:219], v[164:167]\n" \
"v_accvgpr_read_b32 v234, a8\n" \
"v_accvgpr_read_b32 v235, a9\n" \
"v_mfma_f32_16x16x32_bf16 v[164:167], v[12:15], v[220:223], v[164:167]\n" \
"global_load_lds_dwordx4 v229, s[42:43] offset:-1024\n" \
"v_accvgpr_read_b32 v236, a10\n" \
"v_mfma_f32_16x16x32_bf16 v[168:171], v[16:19], v[216:219], v[168:171]\n" \
"s_add_u32 s42, s42, 0x80000\n" \
"s_addc_u32 s43, s43, 0\n" \
"v_accvgpr_read_b32 v237, a11\n" \
"v_mfma_f32_16x16x32_bf16 v[168:171], v[20:23], v[220:223], v[168:171]\n" \
"v_mul_f32 v234, s92, v234\n" \
"v_mul_f32 v235, s92, v235\n" \
"v_mfma_f32_16x16x32_bf16 v[172:175], v[24:27], v[216:219], v[172:175]\n" \
"s_waitcnt vmcnt(19) lgkmcnt(0)\n" \
"v_mul_f32 v236, s92, v236\n" \
"v_mul_f32 v237, s92, v237\n" \
"v_mfma_f32_16x16x32_bf16 v[172:175], v[28:31], v[220:223], v[172:175]\n" \
"s_barrier\n" \
"v_mfma_f32_16x16x32_bf16 v[80:83], v[32:35], v[192:195], v[80:83]\n" \
"ds_read_b64_tr_b16 v[0:1], v232 offset:0\n" \
"v_cvt_pk_bf16_f32 v234, v234, v235\n" \
"v_mfma_f32_16x16x32_bf16 v[80:83], v[36:39], v[196:199], v[80:83]\n" \
"ds_read_b64_tr_b16 v[2:3], v232 offset:2048\n" \
"v_cvt_pk_bf16_f32 v236, v236, v237\n" \
"v_mfma_f32_16x16x32_bf16 v[84:87], v[40:43], v[192:195], v[84:87]\n" \
"s_add_u32 m0, s87, 135168\n" \
"ds_read_b64_tr_b16 v[4:5], v232 offset:8192\n" \
"v_accvgpr_write_b32 a8, v234\n" \
"v_mfma_f32_16x16x32_bf16 v[84:87], v[44:47], v[196:199], v[84:87]\n" \
"global_load_lds_dwordx4 v224, s[48:49] offset:-4096\n" \
"v_accvgpr_write_b32 a9, v236\n" \
"v_mfma_f32_16x16x32_bf16 v[88:91], v[48:51], v[192:195], v[88:91]\n" \
"ds_read_b64_tr_b16 v[6:7], v232 offset:10240\n" \
"v_accvgpr_read_b32 v234, a12\n" \
"v_mfma_f32_16x16x32_bf16 v[88:91], v[52:55], v[196:199], v[88:91]\n" \
"ds_read_b64_tr_b16 v[8:9], v233 offset:0\n" \
"v_accvgpr_read_b32 v235, a13\n" \
"v_mfma_f32_16x16x32_bf16 v[92:95], v[56:59], v[192:195], v[92:95]\n" \
"ds_read_b64_tr_b16 v[10:11], v233 offset:2048\n" \
"v_accvgpr_read_b32 v236, a14\n" \
"v_mfma_f32_16x16x32_bf16 v[92:95], v[60:63], v[196:199], v[92:95]\n" \
"global_load_lds_dwordx4 v225, s[48:49] offset:-3072\n" \
"v_accvgpr_read_b32 v237, a15\n" \
"v_mfma_f32_16x16x32_bf16 v[112:115], v[32:35], v[200:203], v[112:115]\n" \
"ds_read_b128 v[192:195], v230 offset:0\n" \
"v_mul_f32 v234, s92, v234\n" \
"v_mfma_f32_16x16x32_bf16 v[112:115], v[36:39], v[204:207], v[112:115]\n" \
"ds_read_b128 v[196:199], v231 offset:0\n" \
"v_mul_f32 v235, s92, v235\n" \
"v_mfma_f32_16x16x32_bf16 v[116:119], v[40:43], v[200:203], v[116:119]\n" \
"ds_read_b64_tr_b16 v[12:13], v233 offset:8192\n" \
"v_mul_f32 v236, s92, v236\n" \
"v_mfma_f32_16x16x32_bf16 v[116:119], v[44:47], v[204:207], v[116:119]\n" \
"global_load_lds_dwordx4 v226, s[48:49] offset:-2048\n" \
"v_mul_f32 v237, s92, v237\n" \
"v_mfma_f32_16x16x32_bf16 v[120:123], v[48:51], v[200:203], v[120:123]\n" \
"ds_read_b64_tr_b16 v[14:15], v233 offset:10240\n" \
"v_cvt_pk_bf16_f32 v234, v234, v235\n" \
"v_mfma_f32_16x16x32_bf16 v[120:123], v[52:55], v[204:207], v[120:123]\n" \
"ds_read_b64_tr_b16 v[16:17], v232 offset:512\n" \
"v_cvt_pk_bf16_f32 v236, v236, v237\n" \
"v_mfma_f32_16x16x32_bf16 v[124:127], v[56:59], v[200:203], v[124:127]\n" \
"ds_read_b64_tr_b16 v[18:19], v232 offset:2560\n" \
"v_accvgpr_write_b32 a10, v234\n" \
"v_mfma_f32_16x16x32_bf16 v[124:127], v[60:63], v[204:207], v[124:127]\n" \
"global_load_lds_dwordx4 v227, s[48:49] offset:-1024\n" \
"v_accvgpr_write_b32 a11, v236\n" \
"v_mfma_f32_16x16x32_bf16 v[144:147], v[32:35], v[208:211], v[144:147]\n" \
"s_add_u32 s48, s48, 0x80\n" \
"s_addc_u32 s49, s49, 0\n" \
"ds_read_b128 v[200:203], v230 offset:2048\n" \
"v_mfma_f32_16x16x32_bf16 v[144:147], v[36:39], v[212:215], v[144:147]\n" \
"ds_read_b128 v[204:207], v231 offset:2048\n" \
"v_accvgpr_read_b32 v234, a40\n" \
"v_mfma_f32_16x16x32_bf16 v[148:151], v[40:43], v[208:211], v[148:151]\n" \
"ds_read_b64_tr_b16 v[20:21], v232 offset:8704\n" \
"v_accvgpr_read_b32 v235, a41\n" \
"v_mfma_f32_16x16x32_bf16 v[148:151], v[44:47], v[212:215], v[148:151]\n" \
"global_load_lds_dwordx4 v224, s[46:47] offset:0\n" \
"v_accvgpr_read_b32 v236, a42\n" \
"v_mfma_f32_16x16x32_bf16 v[152:155], v[48:51], v[208:211], v[152:155]\n" \
"ds_read_b64_tr_b16 v[22:23], v232 offset:10752\n" \
"v_accvgpr_read_b32 v237, a43\n" \
"v_mfma_f32_16x16x32_bf16 v[152:155], v[52:55], v[212:215], v[152:155]\n" \
"ds_read_b64_tr_b16 v[24:25], v233 offset:512\n" \
"v_mul_f32 v234, s92, v234\n" \
"v_mfma_f32_16x16x32_bf16 v[156:159], v[56:59], v[208:211], v[156:159]\n" \
"ds_read_b64_tr_b16 v[26:27], v233 offset:2560\n" \
"v_mul_f32 v235, s92, v235\n" \
"v_mfma_f32_16x16x32_bf16 v[156:159], v[60:63], v[212:215], v[156:159]\n" \
"global_load_lds_dwordx4 v225, s[46:47] offset:1024\n" \
"v_mul_f32 v236, s92, v236\n" \
"v_mfma_f32_16x16x32_bf16 v[176:179], v[32:35], v[216:219], v[176:179]\n" \
"ds_read_b128 v[208:211], v230 offset:4096\n" \
"v_mul_f32 v237, s92, v237\n" \
"v_mfma_f32_16x16x32_bf16 v[176:179], v[36:39], v[220:223], v[176:179]\n" \
"ds_read_b128 v[212:215], v231 offset:4096\n" \
"v_cvt_pk_bf16_f32 v234, v234, v235\n" \
"v_mfma_f32_16x16x32_bf16 v[180:183], v[40:43], v[216:219], v[180:183]\n" \
"ds_read_b64_tr_b16 v[28:29], v233 offset:8704\n" \
"v_cvt_pk_bf16_f32 v236, v236, v237\n" \
"v_mfma_f32_16x16x32_bf16 v[180:183], v[44:47], v[220:223], v[180:183]\n" \
"global_load_lds_dwordx4 v226, s[46:47] offset:2048\n" \
"v_accvgpr_write_b32 a40, v234\n" \
"v_mfma_f32_16x16x32_bf16 v[184:187], v[48:51], v[216:219], v[184:187]\n" \
"ds_read_b64_tr_b16 v[30:31], v233 offset:10752\n" \
"v_accvgpr_write_b32 a41, v236\n" \
"v_mfma_f32_16x16x32_bf16 v[184:187], v[52:55], v[220:223], v[184:187]\n" \
"v_accvgpr_read_b32 v234, a44\n" \
"v_accvgpr_read_b32 v235, a45\n" \
"v_mfma_f32_16x16x32_bf16 v[188:191], v[56:59], v[216:219], v[188:191]\n" \
"v_accvgpr_read_b32 v236, a46\n" \
"v_accvgpr_read_b32 v237, a47\n" \
"v_mfma_f32_16x16x32_bf16 v[188:191], v[60:63], v[220:223], v[188:191]\n" \
"s_waitcnt lgkmcnt(1)\n" \
"global_load_lds_dwordx4 v227, s[46:47] offset:3072\n" \
"v_mul_f32 v234, s92, v234\n" \
"v_mfma_f32_16x16x32_bf16 v[64:67], v[0:3], v[192:195], v[64:67]\n" \
"s_add_u32 s46, s46, 0x80\n" \
"s_addc_u32 s47, s47, 0\n" \
"ds_read_b128 v[216:219], v230 offset:6144\n" \
"v_mfma_f32_16x16x32_bf16 v[64:67], v[4:7], v[196:199], v[64:67]\n" \
"ds_read_b128 v[220:223], v231 offset:6144\n" \
"v_mul_f32 v235, s92, v235\n" \
"v_mfma_f32_16x16x32_bf16 v[68:71], v[8:11], v[192:195], v[68:71]\n" \
"ds_read_b64_tr_b16 v[32:33], v232 offset:1024\n" \
"v_mul_f32 v236, s92, v236\n" \
"v_mfma_f32_16x16x32_bf16 v[68:71], v[12:15], v[196:199], v[68:71]\n" \
"buffer_store_dwordx4 a[136:139], v249, s[56:59], s90 offen offset:2048 sc1\n" \
"v_mul_f32 v237, s92, v237\n" \
"v_cvt_pk_bf16_f32 v234, v234, v235\n" \
"v_cvt_pk_bf16_f32 v236, v236, v237\n" \
"v_accvgpr_write_b32 a42, v234\n" \
"v_mfma_f32_16x16x32_bf16 v[72:75], v[16:19], v[192:195], v[72:75]\n" \
"ds_read_b64_tr_b16 v[34:35], v232 offset:3072\n" \
"v_accvgpr_write_b32 a43, v236\n" \
"v_mfma_f32_16x16x32_bf16 v[72:75], v[20:23], v[196:199], v[72:75]\n" \
"s_waitcnt lgkmcnt(3)\n" \
"ds_read_b64_tr_b16 v[36:37], v232 offset:9216\n" \
"v_accvgpr_read_b32 v234, a16\n" \
"v_mfma_f32_16x16x32_bf16 v[76:79], v[24:27], v[192:195], v[76:79]\n" \
"ds_read_b64_tr_b16 v[38:39], v232 offset:11264\n" \
"v_accvgpr_read_b32 v235, a17\n" \
"v_mfma_f32_16x16x32_bf16 v[76:79], v[28:31], v[196:199], v[76:79]\n" \
"v_accvgpr_read_b32 v236, a18\n" \
"v_accvgpr_read_b32 v237, a19\n" \
"v_mfma_f32_16x16x32_bf16 v[96:99], v[0:3], v[200:203], v[96:99]\n" \
"ds_read_b64_tr_b16 v[40:41], v233 offset:1024\n" \
"v_cvt_pk_bf16_f32 v234, v234, v235\n" \
"v_mfma_f32_16x16x32_bf16 v[96:99], v[4:7], v[204:207], v[96:99]\n" \
"ds_read_b64_tr_b16 v[42:43], v233 offset:3072\n" \
"v_cvt_pk_bf16_f32 v236, v236, v237\n" \
"v_mfma_f32_16x16x32_bf16 v[100:103], v[8:11], v[200:203], v[100:103]\n" \
"ds_read_b64_tr_b16 v[44:45], v233 offset:9216\n" \
"v_accvgpr_write_b32 a16, v234\n" \
"v_mfma_f32_16x16x32_bf16 v[100:103], v[12:15], v[204:207], v[100:103]\n" \
"v_accvgpr_write_b32 a17, v236\n" \
"v_accvgpr_read_b32 v234, a48\n" \
"v_mfma_f32_16x16x32_bf16 v[104:107], v[16:19], v[200:203], v[104:107]\n" \
"ds_read_b64_tr_b16 v[46:47], v233 offset:11264\n" \
"v_accvgpr_read_b32 v235, a49\n" \
"v_mfma_f32_16x16x32_bf16 v[104:107], v[20:23], v[204:207], v[104:107]\n" \
"ds_read_b64_tr_b16 v[48:49], v232 offset:1536\n" \
"v_accvgpr_read_b32 v236, a50\n" \
"v_mfma_f32_16x16x32_bf16 v[108:111], v[24:27], v[200:203], v[108:111]\n" \
"s_add_u32 m0, s95, 36864\n" \
"ds_read_b64_tr_b16 v[50:51], v232 offset:3584\n" \
"v_accvgpr_read_b32 v237, a51\n" \
"v_mfma_f32_16x16x32_bf16 v[108:111], v[28:31], v[204:207], v[108:111]\n" \
"global_load_lds_dwordx4 v228, s[44:45] offset:-4096\n" \
"v_cvt_pk_bf16_f32 v234, v234, v235\n" \
"v_mfma_f32_16x16x32_bf16 v[128:131], v[0:3], v[208:211], v[128:131]\n" \
"ds_read_b64_tr_b16 v[52:53], v232 offset:9728\n" \
"v_cvt_pk_bf16_f32 v236, v236, v237\n" \
"v_mfma_f32_16x16x32_bf16 v[128:131], v[4:7], v[212:215], v[128:131]\n" \
"ds_read_b64_tr_b16 v[54:55], v232 offset:11776\n" \
"v_accvgpr_write_b32 a18, v234\n" \
"v_mfma_f32_16x16x32_bf16 v[132:135], v[8:11], v[208:211], v[132:135]\n" \
"ds_read_b64_tr_b16 v[56:57], v233 offset:1536\n" \
"v_accvgpr_write_b32 a19, v236\n" \
"v_mfma_f32_16x16x32_bf16 v[132:135], v[12:15], v[212:215], v[132:135]\n" \
"global_load_lds_dwordx4 v229, s[44:45] offset:-3072\n" \
"v_accvgpr_read_b32 v234, a20\n" \
"v_mfma_f32_16x16x32_bf16 v[136:139], v[16:19], v[208:211], v[136:139]\n" \
"s_add_u32 s44, s44, 0x80000\n" \
"s_addc_u32 s45, s45, 0\n" \
"ds_read_b64_tr_b16 v[58:59], v233 offset:3584\n" \
"v_mfma_f32_16x16x32_bf16 v[136:139], v[20:23], v[212:215], v[136:139]\n" \
"ds_read_b64_tr_b16 v[60:61], v233 offset:9728\n" \
"v_accvgpr_read_b32 v235, a21\n" \
"v_mfma_f32_16x16x32_bf16 v[140:143], v[24:27], v[208:211], v[140:143]\n" \
"ds_read_b64_tr_b16 v[62:63], v233 offset:11776\n" \
"v_accvgpr_read_b32 v236, a22\n" \
"v_mfma_f32_16x16x32_bf16 v[140:143], v[28:31], v[212:215], v[140:143]\n" \
"s_waitcnt lgkmcnt(3)\n" \
"global_load_lds_dwordx4 v228, s[42:43] offset:-2048\n" \
"v_accvgpr_read_b32 v237, a23\n" \
"v_mfma_f32_16x16x32_bf16 v[160:163], v[0:3], v[216:219], v[160:163]\n" \
"v_add_u32 v230, 0x8000, v230\n" \
"v_add_u32 v231, 0x8000, v231\n" \
"v_mfma_f32_16x16x32_bf16 v[160:163], v[4:7], v[220:223], v[160:163]\n" \
"v_add_u32 v232, 0xffff4000, v232\n" \
"v_add_u32 v233, 0xffff4000, v233\n" \
"v_mfma_f32_16x16x32_bf16 v[164:167], v[8:11], v[216:219], v[164:167]\n" \
"v_cvt_pk_bf16_f32 v234, v234, v235\n" \
"v_cvt_pk_bf16_f32 v236, v236, v237\n" \
"v_mfma_f32_16x16x32_bf16 v[164:167], v[12:15], v[220:223], v[164:167]\n" \
"global_load_lds_dwordx4 v229, s[42:43] offset:-1024\n" \
"v_accvgpr_write_b32 a20, v234\n" \
"v_mfma_f32_16x16x32_bf16 v[168:171], v[16:19], v[216:219], v[168:171]\n" \
"s_add_u32 s42, s42, 0x80000\n" \
"s_addc_u32 s43, s43, 0\n" \
"v_accvgpr_write_b32 a21, v236\n" \
"v_mfma_f32_16x16x32_bf16 v[168:171], v[20:23], v[220:223], v[168:171]\n" \
"v_accvgpr_read_b32 v234, a52\n" \
"v_accvgpr_read_b32 v235, a53\n" \
"v_mfma_f32_16x16x32_bf16 v[172:175], v[24:27], v[216:219], v[172:175]\n" \
"s_waitcnt vmcnt(18) lgkmcnt(0)\n" \
"v_accvgpr_read_b32 v236, a54\n" \
"v_accvgpr_read_b32 v237, a55\n" \
"v_mfma_f32_16x16x32_bf16 v[172:175], v[28:31], v[220:223], v[172:175]\n" \
"s_barrier\n" \
"v_mfma_f32_16x16x32_bf16 v[80:83], v[32:35], v[192:195], v[80:83]\n" \
"ds_read_b64_tr_b16 v[0:1], v232 offset:0\n" \
"v_cvt_pk_bf16_f32 v234, v234, v235\n" \
"v_mfma_f32_16x16x32_bf16 v[80:83], v[36:39], v[196:199], v[80:83]\n" \
"ds_read_b64_tr_b16 v[2:3], v232 offset:2048\n" \
"v_cvt_pk_bf16_f32 v236, v236, v237\n" \
"v_mfma_f32_16x16x32_bf16 v[84:87], v[40:43], v[192:195], v[84:87]\n" \
"s_add_u32 m0, s87, 69632\n" \
"ds_read_b64_tr_b16 v[4:5], v232 offset:8192\n" \
"v_accvgpr_write_b32 a22, v234\n" \
"v_mfma_f32_16x16x32_bf16 v[84:87], v[44:47], v[196:199], v[84:87]\n" \
"global_load_lds_dwordx4 v224, s[48:49] offset:-4096\n" \
"v_accvgpr_write_b32 a23, v236\n" \
"v_mfma_f32_16x16x32_bf16 v[88:91], v[48:51], v[192:195], v[88:91]\n" \
"ds_read_b64_tr_b16 v[6:7], v232 offset:10240\n" \
"v_accvgpr_read_b32 v234, a24\n" \
"v_mfma_f32_16x16x32_bf16 v[88:91], v[52:55], v[196:199], v[88:91]\n" \
"ds_read_b64_tr_b16 v[8:9], v233 offset:0\n" \
"v_accvgpr_read_b32 v235, a25\n" \
"v_mfma_f32_16x16x32_bf16 v[92:95], v[56:59], v[192:195], v[92:95]\n" \
"ds_read_b64_tr_b16 v[10:11], v233 offset:2048\n" \
"v_accvgpr_read_b32 v236, a26\n" \
"v_mfma_f32_16x16x32_bf16 v[92:95], v[60:63], v[196:199], v[92:95]\n" \
"global_load_lds_dwordx4 v225, s[48:49] offset:-3072\n" \
"v_accvgpr_read_b32 v237, a27\n" \
"v_mfma_f32_16x16x32_bf16 v[112:115], v[32:35], v[200:203], v[112:115]\n" \
"ds_read_b128 v[192:195], v230 offset:0\n" \
"v_cvt_pk_bf16_f32 v234, v234, v235\n" \
"v_mfma_f32_16x16x32_bf16 v[112:115], v[36:39], v[204:207], v[112:115]\n" \
"ds_read_b128 v[196:199], v231 offset:0\n" \
"v_cvt_pk_bf16_f32 v236, v236, v237\n" \
"v_mfma_f32_16x16x32_bf16 v[116:119], v[40:43], v[200:203], v[116:119]\n" \
"ds_read_b64_tr_b16 v[12:13], v233 offset:8192\n" \
"v_accvgpr_write_b32 a24, v234\n" \
"v_mfma_f32_16x16x32_bf16 v[116:119], v[44:47], v[204:207], v[116:119]\n" \
"global_load_lds_dwordx4 v226, s[48:49] offset:-2048\n" \
"v_accvgpr_write_b32 a25, v236\n" \
"v_mfma_f32_16x16x32_bf16 v[120:123], v[48:51], v[200:203], v[120:123]\n" \
"ds_read_b64_tr_b16 v[14:15], v233 offset:10240\n" \
"v_accvgpr_read_b32 v234, a56\n" \
"v_mfma_f32_16x16x32_bf16 v[120:123], v[52:55], v[204:207], v[120:123]\n" \
"ds_read_b64_tr_b16 v[16:17], v232 offset:512\n" \
"v_accvgpr_read_b32 v235, a57\n" \
"v_mfma_f32_16x16x32_bf16 v[124:127], v[56:59], v[200:203], v[124:127]\n" \
"ds_read_b64_tr_b16 v[18:19], v232 offset:2560\n" \
"v_accvgpr_read_b32 v236, a58\n" \
"v_mfma_f32_16x16x32_bf16 v[124:127], v[60:63], v[204:207], v[124:127]\n" \
"global_load_lds_dwordx4 v227, s[48:49] offset:-1024\n" \
"v_accvgpr_read_b32 v237, a59\n" \
"v_mfma_f32_16x16x32_bf16 v[144:147], v[32:35], v[208:211], v[144:147]\n" \
"s_add_u32 s48, s48, 0x80\n" \
"s_addc_u32 s49, s49, 0\n" \
"ds_read_b128 v[200:203], v230 offset:2048\n" \
"v_mfma_f32_16x16x32_bf16 v[144:147], v[36:39], v[212:215], v[144:147]\n" \
"ds_read_b128 v[204:207], v231 offset:2048\n" \
"v_cvt_pk_bf16_f32 v234, v234, v235\n" \
"v_mfma_f32_16x16x32_bf16 v[148:151], v[40:43], v[208:211], v[148:151]\n" \
"ds_read_b64_tr_b16 v[20:21], v232 offset:8704\n" \
"v_cvt_pk_bf16_f32 v236, v236, v237\n" \
"v_mfma_f32_16x16x32_bf16 v[148:151], v[44:47], v[212:215], v[148:151]\n" \
"global_load_lds_dwordx4 v224, s[46:47] offset:0\n" \
"v_accvgpr_write_b32 a26, v234\n" \
"v_mfma_f32_16x16x32_bf16 v[152:155], v[48:51], v[208:211], v[152:155]\n" \
"ds_read_b64_tr_b16 v[22:23], v232 offset:10752\n" \
"v_accvgpr_write_b32 a27, v236\n" \
"v_mfma_f32_16x16x32_bf16 v[152:155], v[52:55], v[212:215], v[152:155]\n" \
"ds_read_b64_tr_b16 v[24:25], v233 offset:512\n" \
"v_accvgpr_read_b32 v234, a28\n" \
"v_mfma_f32_16x16x32_bf16 v[156:159], v[56:59], v[208:211], v[156:159]\n" \
"ds_read_b64_tr_b16 v[26:27], v233 offset:2560\n" \
"v_accvgpr_read_b32 v235, a29\n" \
"v_mfma_f32_16x16x32_bf16 v[156:159], v[60:63], v[212:215], v[156:159]\n" \
"global_load_lds_dwordx4 v225, s[46:47] offset:1024\n" \
"v_accvgpr_read_b32 v236, a30\n" \
"v_mfma_f32_16x16x32_bf16 v[176:179], v[32:35], v[216:219], v[176:179]\n" \
"ds_read_b128 v[208:211], v230 offset:4096\n" \
"v_accvgpr_read_b32 v237, a31\n" \
"v_mfma_f32_16x16x32_bf16 v[176:179], v[36:39], v[220:223], v[176:179]\n" \
"ds_read_b128 v[212:215], v231 offset:4096\n" \
"v_cvt_pk_bf16_f32 v234, v234, v235\n" \
"v_mfma_f32_16x16x32_bf16 v[180:183], v[40:43], v[216:219], v[180:183]\n" \
"ds_read_b64_tr_b16 v[28:29], v233 offset:8704\n" \
"v_cvt_pk_bf16_f32 v236, v236, v237\n" \
"v_mfma_f32_16x16x32_bf16 v[180:183], v[44:47], v[220:223], v[180:183]\n" \
"global_load_lds_dwordx4 v226, s[46:47] offset:2048\n" \
"v_accvgpr_write_b32 a28, v234\n" \
"v_mfma_f32_16x16x32_bf16 v[184:187], v[48:51], v[216:219], v[184:187]\n" \
"ds_read_b64_tr_b16 v[30:31], v233 offset:10752\n" \
"v_accvgpr_write_b32 a29, v236\n" \
"v_mfma_f32_16x16x32_bf16 v[184:187], v[52:55], v[220:223], v[184:187]\n" \
"v_accvgpr_read_b32 v234, a60\n" \
"v_accvgpr_read_b32 v235, a61\n" \
"v_mfma_f32_16x16x32_bf16 v[188:191], v[56:59], v[216:219], v[188:191]\n" \
"v_accvgpr_read_b32 v236, a62\n" \
"v_accvgpr_read_b32 v237, a63\n" \
"v_mfma_f32_16x16x32_bf16 v[188:191], v[60:63], v[220:223], v[188:191]\n" \
"s_waitcnt lgkmcnt(1)\n" \
"global_load_lds_dwordx4 v227, s[46:47] offset:3072\n" \
"v_cvt_pk_bf16_f32 v234, v234, v235\n" \
"v_mfma_f32_16x16x32_bf16 v[64:67], v[0:3], v[192:195], v[64:67]\n" \
"s_add_u32 s46, s46, 0x80\n" \
"s_addc_u32 s47, s47, 0\n" \
"ds_read_b128 v[216:219], v230 offset:6144\n" \
"v_mfma_f32_16x16x32_bf16 v[64:67], v[4:7], v[196:199], v[64:67]\n" \
"ds_read_b128 v[220:223], v231 offset:6144\n" \
"v_cvt_pk_bf16_f32 v236, v236, v237\n" \
"v_mfma_f32_16x16x32_bf16 v[68:71], v[8:11], v[192:195], v[68:71]\n" \
"ds_read_b64_tr_b16 v[32:33], v232 offset:1024\n" \
"v_accvgpr_write_b32 a30, v234\n" \
"v_mfma_f32_16x16x32_bf16 v[68:71], v[12:15], v[196:199], v[68:71]\n" \
"buffer_store_dwordx4 a[168:171], v249, s[56:59], s90 offen offset:2304 sc1\n" \
"v_accvgpr_write_b32 a31, v236\n" \
"v_mfma_f32_16x16x32_bf16 v[72:75], v[16:19], v[192:195], v[72:75]\n" \
"s_add_u32 s90, s89, 139264\n" \
"ds_read_b64_tr_b16 v[34:35], v232 offset:3072\n" \
"v_mfma_f32_16x16x32_bf16 v[72:75], v[20:23], v[196:199], v[72:75]\n" \
"s_waitcnt lgkmcnt(3)\n" \
"ds_read_b64_tr_b16 v[36:37], v232 offset:9216\n" \
"v_mfma_f32_16x16x32_bf16 v[76:79], v[24:27], v[192:195], v[76:79]\n" \
"ds_read_b64_tr_b16 v[38:39], v232 offset:11264\n" \
"v_mfma_f32_16x16x32_bf16 v[76:79], v[28:31], v[196:199], v[76:79]\n" \
"buffer_store_dwordx4 a[144:147], v248, s[56:59], s90 offen offset:0 sc1\n" \
"v_mfma_f32_16x16x32_bf16 v[96:99], v[0:3], v[200:203], v[96:99]\n" \
"s_lshl_b32 s70, s63, 2\n" \
"ds_read_b64_tr_b16 v[40:41], v233 offset:1024\n" \
"v_mfma_f32_16x16x32_bf16 v[96:99], v[4:7], v[204:207], v[96:99]\n" \
"s_add_u32 s70, s70, s91\n" \
"ds_read_b64_tr_b16 v[42:43], v233 offset:3072\n" \
"v_mov_b32 v238, s60\n" \
"v_mfma_f32_16x16x32_bf16 v[100:103], v[8:11], v[200:203], v[100:103]\n" \
"ds_read_b64_tr_b16 v[44:45], v233 offset:9216\n" \
"v_lshlrev_b32 v239, 28, s61\n" \
"v_mfma_f32_16x16x32_bf16 v[100:103], v[12:15], v[204:207], v[100:103]\n" \
"buffer_store_dword v238, v239, s[56:59], s70 offen sc1\n" \
"v_mfma_f32_16x16x32_bf16 v[104:107], v[16:19], v[200:203], v[104:107]\n" \
"ds_read_b64_tr_b16 v[46:47], v233 offset:11264\n" \
"v_mfma_f32_16x16x32_bf16 v[104:107], v[20:23], v[204:207], v[104:107]\n" \
"ds_read_b64_tr_b16 v[48:49], v232 offset:1536\n" \
"v_mfma_f32_16x16x32_bf16 v[108:111], v[24:27], v[200:203], v[108:111]\n" \
"s_add_u32 m0, s95, 53248\n" \
"ds_read_b64_tr_b16 v[50:51], v232 offset:3584\n" \
"v_mfma_f32_16x16x32_bf16 v[108:111], v[28:31], v[204:207], v[108:111]\n" \
"global_load_lds_dwordx4 v228, s[44:45] offset:-4096\n" \
"v_mfma_f32_16x16x32_bf16 v[128:131], v[0:3], v[208:211], v[128:131]\n" \
"ds_read_b64_tr_b16 v[52:53], v232 offset:9728\n" \
"v_mfma_f32_16x16x32_bf16 v[128:131], v[4:7], v[212:215], v[128:131]\n" \
"ds_read_b64_tr_b16 v[54:55], v232 offset:11776\n" \
"v_mfma_f32_16x16x32_bf16 v[132:135], v[8:11], v[208:211], v[132:135]\n" \
"ds_read_b64_tr_b16 v[56:57], v233 offset:1536\n" \
"v_mfma_f32_16x16x32_bf16 v[132:135], v[12:15], v[212:215], v[132:135]\n" \
"global_load_lds_dwordx4 v229, s[44:45] offset:-3072\n" \
"v_mfma_f32_16x16x32_bf16 v[136:139], v[16:19], v[208:211], v[136:139]\n" \
"Lpg3809_%=:\n" \
"s_cbranch_execz Lpg5524_%=\n" \
"s_add_u32 s44, s44, 0x100000\n" \
"s_addc_u32 s45, s45, 0\n" \
"ds_read_b64_tr_b16 v[58:59], v233 offset:3584\n" \
"v_mfma_f32_16x16x32_bf16 v[136:139], v[20:23], v[212:215], v[136:139]\n" \
"ds_read_b64_tr_b16 v[60:61], v233 offset:9728\n" \
"v_mfma_f32_16x16x32_bf16 v[140:143], v[24:27], v[208:211], v[140:143]\n" \
"ds_read_b64_tr_b16 v[62:63], v233 offset:11776\n" \
"v_mfma_f32_16x16x32_bf16 v[140:143], v[28:31], v[212:215], v[140:143]\n" \
"s_waitcnt lgkmcnt(3)\n" \
"global_load_lds_dwordx4 v228, s[42:43] offset:-2048\n" \
"v_mfma_f32_16x16x32_bf16 v[160:163], v[0:3], v[216:219], v[160:163]\n" \
"v_add_u32 v230, 0x8000, v230\n" \
"v_add_u32 v231, 0x8000, v231\n" \
"v_mfma_f32_16x16x32_bf16 v[160:163], v[4:7], v[220:223], v[160:163]\n" \
"v_add_u32 v232, 0x4000, v232\n" \
"v_add_u32 v233, 0x4000, v233\n" \
"v_mfma_f32_16x16x32_bf16 v[164:167], v[8:11], v[216:219], v[164:167]\n" \
"v_mfma_f32_16x16x32_bf16 v[164:167], v[12:15], v[220:223], v[164:167]\n" \
"global_load_lds_dwordx4 v229, s[42:43] offset:-1024\n" \
"v_mfma_f32_16x16x32_bf16 v[168:171], v[16:19], v[216:219], v[168:171]\n" \
"s_add_u32 s42, s42, 0x100000\n" \
"s_addc_u32 s43, s43, 0\n" \
"v_mfma_f32_16x16x32_bf16 v[168:171], v[20:23], v[220:223], v[168:171]\n" \
"v_mfma_f32_16x16x32_bf16 v[172:175], v[24:27], v[216:219], v[172:175]\n" \
"s_waitcnt vmcnt(20) lgkmcnt(0)\n" \
"v_mfma_f32_16x16x32_bf16 v[172:175], v[28:31], v[220:223], v[172:175]\n" \
"s_barrier\n" \
"v_mfma_f32_16x16x32_bf16 v[80:83], v[32:35], v[192:195], v[80:83]\n" \
"ds_read_b64_tr_b16 v[0:1], v232 offset:0\n" \
"v_mfma_f32_16x16x32_bf16 v[80:83], v[36:39], v[196:199], v[80:83]\n" \
"ds_read_b64_tr_b16 v[2:3], v232 offset:2048\n" \
"v_mfma_f32_16x16x32_bf16 v[84:87], v[40:43], v[192:195], v[84:87]\n" \
"s_add_u32 m0, s87, 102400\n" \
"ds_read_b64_tr_b16 v[4:5], v232 offset:8192\n" \
"v_mfma_f32_16x16x32_bf16 v[84:87], v[44:47], v[196:199], v[84:87]\n" \
"global_load_lds_dwordx4 v224, s[48:49] offset:-4096\n" \
"v_mfma_f32_16x16x32_bf16 v[88:91], v[48:51], v[192:195], v[88:91]\n" \
"ds_read_b64_tr_b16 v[6:7], v232 offset:10240\n" \
"v_mfma_f32_16x16x32_bf16 v[88:91], v[52:55], v[196:199], v[88:91]\n" \
"ds_read_b64_tr_b16 v[8:9], v233 offset:0\n" \
"v_mfma_f32_16x16x32_bf16 v[92:95], v[56:59], v[192:195], v[92:95]\n" \
"ds_read_b64_tr_b16 v[10:11], v233 offset:2048\n" \
"v_mfma_f32_16x16x32_bf16 v[92:95], v[60:63], v[196:199], v[92:95]\n" \
"global_load_lds_dwordx4 v225, s[48:49] offset:-3072\n" \
"v_mfma_f32_16x16x32_bf16 v[112:115], v[32:35], v[200:203], v[112:115]\n" \
"ds_read_b128 v[192:195], v230 offset:0\n" \
"v_mfma_f32_16x16x32_bf16 v[112:115], v[36:39], v[204:207], v[112:115]\n" \
"ds_read_b128 v[196:199], v231 offset:0\n" \
"v_mfma_f32_16x16x32_bf16 v[116:119], v[40:43], v[200:203], v[116:119]\n" \
"ds_read_b64_tr_b16 v[12:13], v233 offset:8192\n" \
"v_mfma_f32_16x16x32_bf16 v[116:119], v[44:47], v[204:207], v[116:119]\n" \
"global_load_lds_dwordx4 v226, s[48:49] offset:-2048\n" \
"v_mfma_f32_16x16x32_bf16 v[120:123], v[48:51], v[200:203], v[120:123]\n" \
"ds_read_b64_tr_b16 v[14:15], v233 offset:10240\n" \
"v_mfma_f32_16x16x32_bf16 v[120:123], v[52:55], v[204:207], v[120:123]\n" \
"ds_read_b64_tr_b16 v[16:17], v232 offset:512\n" \
"v_mfma_f32_16x16x32_bf16 v[124:127], v[56:59], v[200:203], v[124:127]\n" \
"ds_read_b64_tr_b16 v[18:19], v232 offset:2560\n" \
"v_mfma_f32_16x16x32_bf16 v[124:127], v[60:63], v[204:207], v[124:127]\n" \
"global_load_lds_dwordx4 v227, s[48:49] offset:-1024\n" \
"v_mfma_f32_16x16x32_bf16 v[144:147], v[32:35], v[208:211], v[144:147]\n" \
"s_add_u32 s48, s48, 0x100\n" \
"s_addc_u32 s49, s49, 0\n" \
"ds_read_b128 v[200:203], v230 offset:2048\n" \
"v_mfma_f32_16x16x32_bf16 v[144:147], v[36:39], v[212:215], v[144:147]\n" \
"ds_read_b128 v[204:207], v231 offset:2048\n" \
"v_mfma_f32_16x16x32_bf16 v[148:151], v[40:43], v[208:211], v[148:151]\n" \
"ds_read_b64_tr_b16 v[20:21], v232 offset:8704\n" \
"v_mfma_f32_16x16x32_bf16 v[148:151], v[44:47], v[212:215], v[148:151]\n" \
"global_load_lds_dwordx4 v224, s[46:47] offset:0\n" \
"v_mfma_f32_16x16x32_bf16 v[152:155], v[48:51], v[208:211], v[152:155]\n" \
"ds_read_b64_tr_b16 v[22:23], v232 offset:10752\n" \
"v_mfma_f32_16x16x32_bf16 v[152:155], v[52:55], v[212:215], v[152:155]\n" \
"ds_read_b64_tr_b16 v[24:25], v233 offset:512\n" \
"v_mfma_f32_16x16x32_bf16 v[156:159], v[56:59], v[208:211], v[156:159]\n" \
"ds_read_b64_tr_b16 v[26:27], v233 offset:2560\n" \
"v_mfma_f32_16x16x32_bf16 v[156:159], v[60:63], v[212:215], v[156:159]\n" \
"global_load_lds_dwordx4 v225, s[46:47] offset:1024\n" \
"v_mfma_f32_16x16x32_bf16 v[176:179], v[32:35], v[216:219], v[176:179]\n" \
"ds_read_b128 v[208:211], v230 offset:4096\n" \
"v_mfma_f32_16x16x32_bf16 v[176:179], v[36:39], v[220:223], v[176:179]\n" \
"ds_read_b128 v[212:215], v231 offset:4096\n" \
"v_mfma_f32_16x16x32_bf16 v[180:183], v[40:43], v[216:219], v[180:183]\n" \
"ds_read_b64_tr_b16 v[28:29], v233 offset:8704\n" \
"v_mfma_f32_16x16x32_bf16 v[180:183], v[44:47], v[220:223], v[180:183]\n" \
"global_load_lds_dwordx4 v226, s[46:47] offset:2048\n" \
"v_mfma_f32_16x16x32_bf16 v[184:187], v[48:51], v[216:219], v[184:187]\n" \
"ds_read_b64_tr_b16 v[30:31], v233 offset:10752\n" \
"v_mfma_f32_16x16x32_bf16 v[184:187], v[52:55], v[220:223], v[184:187]\n" \
"v_mfma_f32_16x16x32_bf16 v[188:191], v[56:59], v[216:219], v[188:191]\n" \
"v_mfma_f32_16x16x32_bf16 v[188:191], v[60:63], v[220:223], v[188:191]\n" \
"s_waitcnt lgkmcnt(1)\n" \
"global_load_lds_dwordx4 v227, s[46:47] offset:3072\n" \
"v_mfma_f32_16x16x32_bf16 v[64:67], v[0:3], v[192:195], v[64:67]\n" \
"s_add_u32 s46, s46, 0x100\n" \
"s_addc_u32 s47, s47, 0\n" \
"ds_read_b128 v[216:219], v230 offset:6144\n" \
"v_mfma_f32_16x16x32_bf16 v[64:67], v[4:7], v[196:199], v[64:67]\n" \
"ds_read_b128 v[220:223], v231 offset:6144\n" \
"v_mfma_f32_16x16x32_bf16 v[68:71], v[8:11], v[192:195], v[68:71]\n" \
"ds_read_b64_tr_b16 v[32:33], v232 offset:1024\n" \
"v_mfma_f32_16x16x32_bf16 v[68:71], v[12:15], v[196:199], v[68:71]\n" \
"buffer_store_dwordx4 a[148:151], v248, s[56:59], s90 offen offset:1024 sc1\n" \
"v_mfma_f32_16x16x32_bf16 v[72:75], v[16:19], v[192:195], v[72:75]\n" \
"ds_read_b64_tr_b16 v[34:35], v232 offset:3072\n" \
"v_mfma_f32_16x16x32_bf16 v[72:75], v[20:23], v[196:199], v[72:75]\n" \
"s_waitcnt lgkmcnt(3)\n" \
"ds_read_b64_tr_b16 v[36:37], v232 offset:9216\n" \
"v_mfma_f32_16x16x32_bf16 v[76:79], v[24:27], v[192:195], v[76:79]\n" \
"ds_read_b64_tr_b16 v[38:39], v232 offset:11264\n" \
"v_mfma_f32_16x16x32_bf16 v[76:79], v[28:31], v[196:199], v[76:79]\n" \
"v_mfma_f32_16x16x32_bf16 v[96:99], v[0:3], v[200:203], v[96:99]\n" \
"ds_read_b64_tr_b16 v[40:41], v233 offset:1024\n" \
"v_mfma_f32_16x16x32_bf16 v[96:99], v[4:7], v[204:207], v[96:99]\n" \
"ds_read_b64_tr_b16 v[42:43], v233 offset:3072\n" \
"v_mfma_f32_16x16x32_bf16 v[100:103], v[8:11], v[200:203], v[100:103]\n" \
"ds_read_b64_tr_b16 v[44:45], v233 offset:9216\n" \
"v_mfma_f32_16x16x32_bf16 v[100:103], v[12:15], v[204:207], v[100:103]\n" \
"v_mfma_f32_16x16x32_bf16 v[104:107], v[16:19], v[200:203], v[104:107]\n" \
"ds_read_b64_tr_b16 v[46:47], v233 offset:11264\n" \
"v_mfma_f32_16x16x32_bf16 v[104:107], v[20:23], v[204:207], v[104:107]\n" \
"ds_read_b64_tr_b16 v[48:49], v232 offset:1536\n" \
"v_mfma_f32_16x16x32_bf16 v[108:111], v[24:27], v[200:203], v[108:111]\n" \
"s_add_u32 m0, s95, 4096\n" \
"ds_read_b64_tr_b16 v[50:51], v232 offset:3584\n" \
"v_mfma_f32_16x16x32_bf16 v[108:111], v[28:31], v[204:207], v[108:111]\n" \
"global_load_lds_dwordx4 v228, s[44:45] offset:-4096\n" \
"v_mfma_f32_16x16x32_bf16 v[128:131], v[0:3], v[208:211], v[128:131]\n" \
"ds_read_b64_tr_b16 v[52:53], v232 offset:9728\n" \
"v_mfma_f32_16x16x32_bf16 v[128:131], v[4:7], v[212:215], v[128:131]\n" \
"ds_read_b64_tr_b16 v[54:55], v232 offset:11776\n" \
"v_mfma_f32_16x16x32_bf16 v[132:135], v[8:11], v[208:211], v[132:135]\n" \
"ds_read_b64_tr_b16 v[56:57], v233 offset:1536\n" \
"v_mfma_f32_16x16x32_bf16 v[132:135], v[12:15], v[212:215], v[132:135]\n" \
"global_load_lds_dwordx4 v229, s[44:45] offset:-3072\n" \
"v_mfma_f32_16x16x32_bf16 v[136:139], v[16:19], v[208:211], v[136:139]\n" \
"s_add_u32 s44, s44, 0x80000\n" \
"s_addc_u32 s45, s45, 0\n" \
"ds_read_b64_tr_b16 v[58:59], v233 offset:3584\n" \
"v_mfma_f32_16x16x32_bf16 v[136:139], v[20:23], v[212:215], v[136:139]\n" \
"ds_read_b64_tr_b16 v[60:61], v233 offset:9728\n" \
"v_mfma_f32_16x16x32_bf16 v[140:143], v[24:27], v[208:211], v[140:143]\n" \
"ds_read_b64_tr_b16 v[62:63], v233 offset:11776\n" \
"v_mfma_f32_16x16x32_bf16 v[140:143], v[28:31], v[212:215], v[140:143]\n" \
"s_waitcnt lgkmcnt(3)\n" \
"global_load_lds_dwordx4 v228, s[42:43] offset:-2048\n" \
"v_mfma_f32_16x16x32_bf16 v[160:163], v[0:3], v[216:219], v[160:163]\n" \
"v_add_u32 v230, 0xffff0000, v230\n" \
"v_add_u32 v231, 0xffff0000, v231\n" \
"v_mfma_f32_16x16x32_bf16 v[160:163], v[4:7], v[220:223], v[160:163]\n" \
"v_add_u32 v232, 0x4000, v232\n" \
"v_add_u32 v233, 0x4000, v233\n" \
"v_mfma_f32_16x16x32_bf16 v[164:167], v[8:11], v[216:219], v[164:167]\n" \
"v_mfma_f32_16x16x32_bf16 v[164:167], v[12:15], v[220:223], v[164:167]\n" \
"global_load_lds_dwordx4 v229, s[42:43] offset:-1024\n" \
"v_mfma_f32_16x16x32_bf16 v[168:171], v[16:19], v[216:219], v[168:171]\n" \
"s_add_u32 s42, s42, 0x80000\n" \
"s_addc_u32 s43, s43, 0\n" \
"v_mfma_f32_16x16x32_bf16 v[168:171], v[20:23], v[220:223], v[168:171]\n" \
"v_mfma_f32_16x16x32_bf16 v[172:175], v[24:27], v[216:219], v[172:175]\n" \
"s_waitcnt vmcnt(20) lgkmcnt(0)\n" \
"v_mfma_f32_16x16x32_bf16 v[172:175], v[28:31], v[220:223], v[172:175]\n" \
"s_barrier\n" \
"v_mfma_f32_16x16x32_bf16 v[80:83], v[32:35], v[192:195], v[80:83]\n" \
"ds_read_b64_tr_b16 v[0:1], v232 offset:0\n" \
"v_mfma_f32_16x16x32_bf16 v[80:83], v[36:39], v[196:199], v[80:83]\n" \
"ds_read_b64_tr_b16 v[2:3], v232 offset:2048\n" \
"v_mfma_f32_16x16x32_bf16 v[84:87], v[40:43], v[192:195], v[84:87]\n" \
"s_add_u32 m0, s87, 135168\n" \
"ds_read_b64_tr_b16 v[4:5], v232 offset:8192\n" \
"v_mfma_f32_16x16x32_bf16 v[84:87], v[44:47], v[196:199], v[84:87]\n" \
"global_load_lds_dwordx4 v224, s[48:49] offset:-4096\n" \
"v_mfma_f32_16x16x32_bf16 v[88:91], v[48:51], v[192:195], v[88:91]\n" \
"ds_read_b64_tr_b16 v[6:7], v232 offset:10240\n" \
"v_mfma_f32_16x16x32_bf16 v[88:91], v[52:55], v[196:199], v[88:91]\n" \
"ds_read_b64_tr_b16 v[8:9], v233 offset:0\n" \
"v_mfma_f32_16x16x32_bf16 v[92:95], v[56:59], v[192:195], v[92:95]\n" \
"ds_read_b64_tr_b16 v[10:11], v233 offset:2048\n" \
"v_mfma_f32_16x16x32_bf16 v[92:95], v[60:63], v[196:199], v[92:95]\n" \
"global_load_lds_dwordx4 v225, s[48:49] offset:-3072\n" \
"v_mfma_f32_16x16x32_bf16 v[112:115], v[32:35], v[200:203], v[112:115]\n" \
"ds_read_b128 v[192:195], v230 offset:0\n" \
"v_mfma_f32_16x16x32_bf16 v[112:115], v[36:39], v[204:207], v[112:115]\n" \
"ds_read_b128 v[196:199], v231 offset:0\n" \
"v_mfma_f32_16x16x32_bf16 v[116:119], v[40:43], v[200:203], v[116:119]\n" \
"ds_read_b64_tr_b16 v[12:13], v233 offset:8192\n" \
"v_mfma_f32_16x16x32_bf16 v[116:119], v[44:47], v[204:207], v[116:119]\n" \
"global_load_lds_dwordx4 v226, s[48:49] offset:-2048\n" \
"v_mfma_f32_16x16x32_bf16 v[120:123], v[48:51], v[200:203], v[120:123]\n" \
"ds_read_b64_tr_b16 v[14:15], v233 offset:10240\n" \
"v_mfma_f32_16x16x32_bf16 v[120:123], v[52:55], v[204:207], v[120:123]\n" \
"ds_read_b64_tr_b16 v[16:17], v232 offset:512\n" \
"v_mfma_f32_16x16x32_bf16 v[124:127], v[56:59], v[200:203], v[124:127]\n" \
"ds_read_b64_tr_b16 v[18:19], v232 offset:2560\n" \
"v_mfma_f32_16x16x32_bf16 v[124:127], v[60:63], v[204:207], v[124:127]\n" \
"global_load_lds_dwordx4 v227, s[48:49] offset:-1024\n" \
"v_mfma_f32_16x16x32_bf16 v[144:147], v[32:35], v[208:211], v[144:147]\n" \
"s_add_u32 s48, s48, 0x80\n" \
"s_addc_u32 s49, s49, 0\n" \
"ds_read_b128 v[200:203], v230 offset:2048\n" \
"v_mfma_f32_16x16x32_bf16 v[144:147], v[36:39], v[212:215], v[144:147]\n" \
"ds_read_b128 v[204:207], v231 offset:2048\n" \
"v_mfma_f32_16x16x32_bf16 v[148:151], v[40:43], v[208:211], v[148:151]\n" \
"ds_read_b64_tr_b16 v[20:21], v232 offset:8704\n" \
"v_mfma_f32_16x16x32_bf16 v[148:151], v[44:47], v[212:215], v[148:151]\n" \
"global_load_lds_dwordx4 v224, s[46:47] offset:0\n" \
"v_mfma_f32_16x16x32_bf16 v[152:155], v[48:51], v[208:211], v[152:155]\n" \
"ds_read_b64_tr_b16 v[22:23], v232 offset:10752\n" \
"v_mfma_f32_16x16x32_bf16 v[152:155], v[52:55], v[212:215], v[152:155]\n" \
"ds_read_b64_tr_b16 v[24:25], v233 offset:512\n" \
"v_mfma_f32_16x16x32_bf16 v[156:159], v[56:59], v[208:211], v[156:159]\n" \
"ds_read_b64_tr_b16 v[26:27], v233 offset:2560\n" \
"v_mfma_f32_16x16x32_bf16 v[156:159], v[60:63], v[212:215], v[156:159]\n" \
"global_load_lds_dwordx4 v225, s[46:47] offset:1024\n" \
"v_mfma_f32_16x16x32_bf16 v[176:179], v[32:35], v[216:219], v[176:179]\n" \
"ds_read_b128 v[208:211], v230 offset:4096\n" \
"v_mfma_f32_16x16x32_bf16 v[176:179], v[36:39], v[220:223], v[176:179]\n" \
"ds_read_b128 v[212:215], v231 offset:4096\n" \
"v_mfma_f32_16x16x32_bf16 v[180:183], v[40:43], v[216:219], v[180:183]\n" \
"ds_read_b64_tr_b16 v[28:29], v233 offset:8704\n" \
"v_mfma_f32_16x16x32_bf16 v[180:183], v[44:47], v[220:223], v[180:183]\n" \
"global_load_lds_dwordx4 v226, s[46:47] offset:2048\n" \
"v_mfma_f32_16x16x32_bf16 v[184:187], v[48:51], v[216:219], v[184:187]\n" \
"ds_read_b64_tr_b16 v[30:31], v233 offset:10752\n" \
"v_mfma_f32_16x16x32_bf16 v[184:187], v[52:55], v[220:223], v[184:187]\n" \
"v_mfma_f32_16x16x32_bf16 v[188:191], v[56:59], v[216:219], v[188:191]\n" \
"v_mfma_f32_16x16x32_bf16 v[188:191], v[60:63], v[220:223], v[188:191]\n" \
"s_waitcnt lgkmcnt(1)\n" \
"global_load_lds_dwordx4 v227, s[46:47] offset:3072\n" \
"v_mfma_f32_16x16x32_bf16 v[64:67], v[0:3], v[192:195], v[64:67]\n" \
"s_add_u32 s46, s46, 0x80\n" \
"s_addc_u32 s47, s47, 0\n" \
"ds_read_b128 v[216:219], v230 offset:6144\n" \
"v_mfma_f32_16x16x32_bf16 v[64:67], v[4:7], v[196:199], v[64:67]\n" \
"ds_read_b128 v[220:223], v231 offset:6144\n" \
"v_mfma_f32_16x16x32_bf16 v[68:71], v[8:11], v[192:195], v[68:71]\n" \
"ds_read_b64_tr_b16 v[32:33], v232 offset:1024\n" \
"v_mfma_f32_16x16x32_bf16 v[68:71], v[12:15], v[196:199], v[68:71]\n" \
"buffer_store_dwordx4 a[152:155], v248, s[56:59], s90 offen offset:2048 sc1\n" \
"v_mfma_f32_16x16x32_bf16 v[72:75], v[16:19], v[192:195], v[72:75]\n" \
"ds_read_b64_tr_b16 v[34:35], v232 offset:3072\n" \
"v_mfma_f32_16x16x32_bf16 v[72:75], v[20:23], v[196:199], v[72:75]\n" \
"s_waitcnt lgkmcnt(3)\n" \
"ds_read_b64_tr_b16 v[36:37], v232 offset:9216\n" \
"v_mfma_f32_16x16x32_bf16 v[76:79], v[24:27], v[192:195], v[76:79]\n" \
"ds_read_b64_tr_b16 v[38:39], v232 offset:11264\n" \
"v_mfma_f32_16x16x32_bf16 v[76:79], v[28:31], v[196:199], v[76:79]\n" \
"v_mfma_f32_16x16x32_bf16 v[96:99], v[0:3], v[200:203], v[96:99]\n" \
"ds_read_b64_tr_b16 v[40:41], v233 offset:1024\n" \
"v_mfma_f32_16x16x32_bf16 v[96:99], v[4:7], v[204:207], v[96:99]\n" \
"ds_read_b64_tr_b16 v[42:43], v233 offset:3072\n" \
"v_mfma_f32_16x16x32_bf16 v[100:103], v[8:11], v[200:203], v[100:103]\n" \
"ds_read_b64_tr_b16 v[44:45], v233 offset:9216\n" \
"v_mfma_f32_16x16x32_bf16 v[100:103], v[12:15], v[204:207], v[100:103]\n" \
"v_mfma_f32_16x16x32_bf16 v[104:107], v[16:19], v[200:203], v[104:107]\n" \
"ds_read_b64_tr_b16 v[46:47], v233 offset:11264\n" \
"v_mfma_f32_16x16x32_bf16 v[104:107], v[20:23], v[204:207], v[104:107]\n" \
"ds_read_b64_tr_b16 v[48:49], v232 offset:1536\n" \
"v_mfma_f32_16x16x32_bf16 v[108:111], v[24:27], v[200:203], v[108:111]\n" \
"s_add_u32 m0, s95, 20480\n" \
"ds_read_b64_tr_b16 v[50:51], v232 offset:3584\n" \
"v_mfma_f32_16x16x32_bf16 v[108:111], v[28:31], v[204:207], v[108:111]\n" \
"global_load_lds_dwordx4 v228, s[44:45] offset:-4096\n" \
"v_mfma_f32_16x16x32_bf16 v[128:131], v[0:3], v[208:211], v[128:131]\n" \
"ds_read_b64_tr_b16 v[52:53], v232 offset:9728\n" \
"v_mfma_f32_16x16x32_bf16 v[128:131], v[4:7], v[212:215], v[128:131]\n" \
"ds_read_b64_tr_b16 v[54:55], v232 offset:11776\n" \
"v_mfma_f32_16x16x32_bf16 v[132:135], v[8:11], v[208:211], v[132:135]\n" \
"ds_read_b64_tr_b16 v[56:57], v233 offset:1536\n" \
"v_mfma_f32_16x16x32_bf16 v[132:135], v[12:15], v[212:215], v[132:135]\n" \
"global_load_lds_dwordx4 v229, s[44:45] offset:-3072\n" \
"v_mfma_f32_16x16x32_bf16 v[136:139], v[16:19], v[208:211], v[136:139]\n" \
"s_add_u32 s44, s44, 0x80000\n" \
"s_addc_u32 s45, s45, 0\n" \
"ds_read_b64_tr_b16 v[58:59], v233 offset:3584\n" \
"v_mfma_f32_16x16x32_bf16 v[136:139], v[20:23], v[212:215], v[136:139]\n" \
"ds_read_b64_tr_b16 v[60:61], v233 offset:9728\n" \
"v_mfma_f32_16x16x32_bf16 v[140:143], v[24:27], v[208:211], v[140:143]\n" \
"ds_read_b64_tr_b16 v[62:63], v233 offset:11776\n" \
"v_mfma_f32_16x16x32_bf16 v[140:143], v[28:31], v[212:215], v[140:143]\n" \
"s_waitcnt lgkmcnt(3)\n" \
"global_load_lds_dwordx4 v228, s[42:43] offset:-2048\n" \
"v_mfma_f32_16x16x32_bf16 v[160:163], v[0:3], v[216:219], v[160:163]\n" \
"v_add_u32 v230, 0x8000, v230\n" \
"v_add_u32 v231, 0x8000, v231\n" \
"v_mfma_f32_16x16x32_bf16 v[160:163], v[4:7], v[220:223], v[160:163]\n" \
"v_add_u32 v232, 0x4000, v232\n" \
"v_add_u32 v233, 0x4000, v233\n" \
"v_mfma_f32_16x16x32_bf16 v[164:167], v[8:11], v[216:219], v[164:167]\n" \
"v_mfma_f32_16x16x32_bf16 v[164:167], v[12:15], v[220:223], v[164:167]\n" \
"global_load_lds_dwordx4 v229, s[42:43] offset:-1024\n" \
"v_mfma_f32_16x16x32_bf16 v[168:171], v[16:19], v[216:219], v[168:171]\n" \
"s_add_u32 s42, s42, 0x80000\n" \
"s_addc_u32 s43, s43, 0\n" \
"v_mfma_f32_16x16x32_bf16 v[168:171], v[20:23], v[220:223], v[168:171]\n" \
"v_mfma_f32_16x16x32_bf16 v[172:175], v[24:27], v[216:219], v[172:175]\n" \
"s_waitcnt vmcnt(18) lgkmcnt(0)\n" \
"v_mfma_f32_16x16x32_bf16 v[172:175], v[28:31], v[220:223], v[172:175]\n" \
"s_barrier\n" \
"v_mfma_f32_16x16x32_bf16 v[80:83], v[32:35], v[192:195], v[80:83]\n" \
"ds_read_b64_tr_b16 v[0:1], v232 offset:0\n" \
"v_mfma_f32_16x16x32_bf16 v[80:83], v[36:39], v[196:199], v[80:83]\n" \
"ds_read_b64_tr_b16 v[2:3], v232 offset:2048\n" \
"v_mfma_f32_16x16x32_bf16 v[84:87], v[40:43], v[192:195], v[84:87]\n" \
"s_add_u32 m0, s87, 69632\n" \
"ds_read_b64_tr_b16 v[4:5], v232 offset:8192\n" \
"v_mfma_f32_16x16x32_bf16 v[84:87], v[44:47], v[196:199], v[84:87]\n" \
"global_load_lds_dwordx4 v224, s[48:49] offset:-4096\n" \
"v_mfma_f32_16x16x32_bf16 v[88:91], v[48:51], v[192:195], v[88:91]\n" \
"ds_read_b64_tr_b16 v[6:7], v232 offset:10240\n" \
"v_mfma_f32_16x16x32_bf16 v[88:91], v[52:55], v[196:199], v[88:91]\n" \
"ds_read_b64_tr_b16 v[8:9], v233 offset:0\n" \
"v_mfma_f32_16x16x32_bf16 v[92:95], v[56:59], v[192:195], v[92:95]\n" \
"ds_read_b64_tr_b16 v[10:11], v233 offset:2048\n" \
"v_mfma_f32_16x16x32_bf16 v[92:95], v[60:63], v[196:199], v[92:95]\n" \
"global_load_lds_dwordx4 v225, s[48:49] offset:-3072\n" \
"v_mfma_f32_16x16x32_bf16 v[112:115], v[32:35], v[200:203], v[112:115]\n" \
"ds_read_b128 v[192:195], v230 offset:0\n" \
"v_mfma_f32_16x16x32_bf16 v[112:115], v[36:39], v[204:207], v[112:115]\n" \
"ds_read_b128 v[196:199], v231 offset:0\n" \
"v_mfma_f32_16x16x32_bf16 v[116:119], v[40:43], v[200:203], v[116:119]\n" \
"ds_read_b64_tr_b16 v[12:13], v233 offset:8192\n" \
"v_mfma_f32_16x16x32_bf16 v[116:119], v[44:47], v[204:207], v[116:119]\n" \
"global_load_lds_dwordx4 v226, s[48:49] offset:-2048\n" \
"v_mfma_f32_16x16x32_bf16 v[120:123], v[48:51], v[200:203], v[120:123]\n" \
"ds_read_b64_tr_b16 v[14:15], v233 offset:10240\n" \
"v_mfma_f32_16x16x32_bf16 v[120:123], v[52:55], v[204:207], v[120:123]\n" \
"ds_read_b64_tr_b16 v[16:17], v232 offset:512\n" \
"v_mfma_f32_16x16x32_bf16 v[124:127], v[56:59], v[200:203], v[124:127]\n" \
"ds_read_b64_tr_b16 v[18:19], v232 offset:2560\n" \
"v_mfma_f32_16x16x32_bf16 v[124:127], v[60:63], v[204:207], v[124:127]\n" \
"global_load_lds_dwordx4 v227, s[48:49] offset:-1024\n" \
"v_mfma_f32_16x16x32_bf16 v[144:147], v[32:35], v[208:211], v[144:147]\n" \
"s_add_u32 s48, s48, 0x80\n" \
"s_addc_u32 s49, s49, 0\n" \
"ds_read_b128 v[200:203], v230 offset:2048\n" \
"v_mfma_f32_16x16x32_bf16 v[144:147], v[36:39], v[212:215], v[144:147]\n" \
"ds_read_b128 v[204:207], v231 offset:2048\n" \
"v_mfma_f32_16x16x32_bf16 v[148:151], v[40:43], v[208:211], v[148:151]\n" \
"ds_read_b64_tr_b16 v[20:21], v232 offset:8704\n" \
"v_mfma_f32_16x16x32_bf16 v[148:151], v[44:47], v[212:215], v[148:151]\n" \
"global_load_lds_dwordx4 v224, s[46:47] offset:0\n" \
"v_mfma_f32_16x16x32_bf16 v[152:155], v[48:51], v[208:211], v[152:155]\n" \
"ds_read_b64_tr_b16 v[22:23], v232 offset:10752\n" \
"v_mfma_f32_16x16x32_bf16 v[152:155], v[52:55], v[212:215], v[152:155]\n" \
"ds_read_b64_tr_b16 v[24:25], v233 offset:512\n" \
"v_mfma_f32_16x16x32_bf16 v[156:159], v[56:59], v[208:211], v[156:159]\n" \
"ds_read_b64_tr_b16 v[26:27], v233 offset:2560\n" \
"v_mfma_f32_16x16x32_bf16 v[156:159], v[60:63], v[212:215], v[156:159]\n" \
"global_load_lds_dwordx4 v225, s[46:47] offset:1024\n" \
"v_mfma_f32_16x16x32_bf16 v[176:179], v[32:35], v[216:219], v[176:179]\n" \
"ds_read_b128 v[208:211], v230 offset:4096\n" \
"v_mfma_f32_16x16x32_bf16 v[176:179], v[36:39], v[220:223], v[176:179]\n" \
"ds_read_b128 v[212:215], v231 offset:4096\n" \
"v_mfma_f32_16x16x32_bf16 v[180:183], v[40:43], v[216:219], v[180:183]\n" \
"ds_read_b64_tr_b16 v[28:29], v233 offset:8704\n" \
"v_mfma_f32_16x16x32_bf16 v[180:183], v[44:47], v[220:223], v[180:183]\n" \
"global_load_lds_dwordx4 v226, s[46:47] offset:2048\n" \
"v_mfma_f32_16x16x32_bf16 v[184:187], v[48:51], v[216:219], v[184:187]\n" \
"ds_read_b64_tr_b16 v[30:31], v233 offset:10752\n" \
"v_mfma_f32_16x16x32_bf16 v[184:187], v[52:55], v[220:223], v[184:187]\n" \
"v_mfma_f32_16x16x32_bf16 v[188:191], v[56:59], v[216:219], v[188:191]\n" \
"v_mfma_f32_16x16x32_bf16 v[188:191], v[60:63], v[220:223], v[188:191]\n" \
"s_waitcnt lgkmcnt(1)\n" \
"global_load_lds_dwordx4 v227, s[46:47] offset:3072\n" \
"v_mfma_f32_16x16x32_bf16 v[64:67], v[0:3], v[192:195], v[64:67]\n" \
"s_add_u32 s46, s46, 0x80\n" \
"s_addc_u32 s47, s47, 0\n" \
"ds_read_b128 v[216:219], v230 offset:6144\n" \
"v_mfma_f32_16x16x32_bf16 v[64:67], v[4:7], v[196:199], v[64:67]\n" \
"s_mul_i32 s77, s62, 557824\n" \
"ds_read_b128 v[220:223], v231 offset:6144\n" \
"s_add_u32 s77, s77, s95\n" \
"v_mfma_f32_16x16x32_bf16 v[68:71], v[8:11], v[192:195], v[68:71]\n" \
"s_add_u32 s36, s28, s77\n" \
"s_mov_b32 s37, s29\n" \
"ds_read_b64_tr_b16 v[32:33], v232 offset:1024\n" \
"v_mfma_f32_16x16x32_bf16 v[68:71], v[12:15], v[196:199], v[68:71]\n" \
"buffer_store_dwordx4 a[156:159], v248, s[56:59], s90 offen offset:3072 sc1\n" \
"s_add_u32 s77, s77, 17850368\n" \
"s_add_u32 s38, s28, s77\n" \
"s_mov_b32 s39, s29\n" \
"s_and_b32 s74, s63, 1\n" \
"v_mfma_f32_16x16x32_bf16 v[72:75], v[16:19], v[192:195], v[72:75]\n" \
"s_add_u32 s90, s88, 278528\n" \
"s_lshl_b32 s74, s74, 15\n" \
"ds_read_b64_tr_b16 v[34:35], v232 offset:3072\n" \
"v_mfma_f32_16x16x32_bf16 v[72:75], v[20:23], v[196:199], v[72:75]\n" \
"s_waitcnt lgkmcnt(3)\n" \
"s_xor_b32 s74, s74, 0x18000\n" \
"ds_read_b64_tr_b16 v[36:37], v232 offset:9216\n" \
"v_mfma_f32_16x16x32_bf16 v[76:79], v[24:27], v[192:195], v[76:79]\n" \
"ds_read_b64_tr_b16 v[38:39], v232 offset:11264\n" \
"v_add_u32 v247, s74, v248\n" \
"v_mfma_f32_16x16x32_bf16 v[76:79], v[28:31], v[196:199], v[76:79]\n" \
"buffer_store_dwordx4 a[64:67], v249, s[56:59], s90 offen offset:0 sc1\n" \
"s_add_u32 s74, s74, s95\n" \
"s_lshr_b32 s83, s61, 1\n" \
"s_lshl_b32 s75, s63, 2\n" \
"s_add_u32 s77, s83, s75\n" \
"v_mfma_f32_16x16x32_bf16 v[96:99], v[0:3], v[200:203], v[96:99]\n" \
"s_mul_i32 s77, s77, 266240\n" \
"s_sub_u32 s78, s83, s75\n" \
"ds_read_b64_tr_b16 v[40:41], v233 offset:1024\n" \
"v_mfma_f32_16x16x32_bf16 v[96:99], v[4:7], v[204:207], v[96:99]\n" \
"s_add_u32 s78, s78, 60\n" \
"s_mul_i32 s78, s78, 266240\n" \
"ds_read_b64_tr_b16 v[42:43], v233 offset:3072\n" \
"v_mfma_f32_16x16x32_bf16 v[100:103], v[8:11], v[200:203], v[100:103]\n" \
"s_and_b32 s82, s61, 1\n" \
"s_lshl_b32 s75, s82, 8\n" \
"ds_read_b64_tr_b16 v[44:45], v233 offset:9216\n" \
"v_mfma_f32_16x16x32_bf16 v[100:103], v[12:15], v[204:207], v[100:103]\n" \
"buffer_load_dword v244, v245, s[56:59], s91 offen sc1\n" \
"v_mfma_f32_16x16x32_bf16 v[104:107], v[16:19], v[200:203], v[104:107]\n" \
"s_lshl_b32 s76, s62, 13\n" \
"s_add_u32 s75, s75, s76\n" \
"ds_read_b64_tr_b16 v[46:47], v233 offset:11264\n" \
"v_mfma_f32_16x16x32_bf16 v[104:107], v[20:23], v[204:207], v[104:107]\n" \
"s_add_u32 s75, s75, 35700736\n" \
"s_add_u32 s77, s77, s75\n" \
"ds_read_b64_tr_b16 v[48:49], v232 offset:1536\n" \
"v_mfma_f32_16x16x32_bf16 v[108:111], v[24:27], v[200:203], v[108:111]\n" \
"s_add_u32 s78, s78, s75\n" \
"s_mul_i32 s83, s83, 9216\n" \
"ds_read_b64_tr_b16 v[50:51], v232 offset:3584\n" \
"v_mfma_f32_16x16x32_bf16 v[108:111], v[28:31], v[204:207], v[108:111]\n" \
"buffer_load_dword v246, v245, s[56:59], s93 offen sc1\n" \
"v_mfma_f32_16x16x32_bf16 v[128:131], v[0:3], v[208:211], v[128:131]\n" \
"s_lshl_b32 s75, s82, 12\n" \
"s_add_u32 s83, s83, s75\n" \
"ds_read_b64_tr_b16 v[52:53], v232 offset:9728\n" \
"v_mfma_f32_16x16x32_bf16 v[128:131], v[4:7], v[212:215], v[128:131]\n" \
"s_add_u32 s83, s83, 4096\n" \
"s_lshl_b32 s82, s82, 14\n" \
"ds_read_b64_tr_b16 v[54:55], v232 offset:11776\n" \
"v_mfma_f32_16x16x32_bf16 v[132:135], v[8:11], v[208:211], v[132:135]\n" \
"s_add_u32 m0, s95, 36864\n" \
"ds_read_b128 a[64:67], v254\n" \
"ds_read_b64_tr_b16 v[56:57], v233 offset:1536\n" \
"v_mfma_f32_16x16x32_bf16 v[132:135], v[12:15], v[212:215], v[132:135]\n" \
"ds_read_b128 a[68:71], v254\n" \
"global_load_lds_dwordx4 v228, s[44:45] offset:-4096\n" \
"v_mfma_f32_16x16x32_bf16 v[136:139], v[16:19], v[208:211], v[136:139]\n" \
"s_cmp_eq_u32 s63, 7\n" \
"s_cselect_b32 s82, 0, s82\n" \
"ds_read_b64_tr_b16 v[58:59], v233 offset:3584\n" \
"v_mfma_f32_16x16x32_bf16 v[136:139], v[20:23], v[212:215], v[136:139]\n" \
"s_mov_b32 s75, s63\n" \
"s_sub_u32 s76, 7, s63\n" \
"ds_read_b64_tr_b16 v[60:61], v233 offset:9728\n" \
"v_mfma_f32_16x16x32_bf16 v[140:143], v[24:27], v[208:211], v[140:143]\n" \
"s_mov_b32 s73, s95\n" \
"s_mov_b64 s[40:41], s[14:15]\n" \
"ds_read_b64_tr_b16 v[62:63], v233 offset:11776\n" \
"v_mfma_f32_16x16x32_bf16 v[140:143], v[28:31], v[212:215], v[140:143]\n" \
"s_waitcnt lgkmcnt(4)\n" \
"global_load_lds_dwordx4 v229, s[44:45] offset:-3072\n" \
"s_mov_b32 s80, 0\n" \
"v_mfma_f32_16x16x32_bf16 v[160:163], v[0:3], v[216:219], v[160:163]\n" \
"s_add_u32 s44, s44, 0x80000\n" \
"s_addc_u32 s45, s45, 0\n" \
"v_add_u32 v230, 0x8000, v230\n" \
"v_mfma_f32_16x16x32_bf16 v[160:163], v[4:7], v[220:223], v[160:163]\n" \
"s_cmp_eq_u32 s63, 7\n" \
"s_cselect_b64 s[40:41], s[18:19], s[40:41]\n" \
"s_cselect_b32 s80, 12, s80\n" \
"v_mfma_f32_16x16x32_bf16 v[164:167], v[8:11], v[216:219], v[164:167]\n" \
"s_cmp_eq_u32 s63, 6\n" \
"s_cselect_b64 s[40:41], s[22:23], s[40:41]\n" \
"s_cselect_b32 s80, 12, s80\n" \
"v_mfma_f32_16x16x32_bf16 v[164:167], v[12:15], v[220:223], v[164:167]\n" \
"global_load_lds_dwordx4 v228, s[42:43] offset:-2048\n" \
"v_add_u32 v231, 0x8000, v231\n" \
"v_mfma_f32_16x16x32_bf16 v[168:171], v[16:19], v[216:219], v[168:171]\n" \
"global_load_lds_dwordx4 v229, s[42:43] offset:-1024\n" \
"v_add_u32 v232, 0xffff4000, v232\n" \
"v_mfma_f32_16x16x32_bf16 v[168:171], v[20:23], v[220:223], v[168:171]\n" \
"s_add_u32 s42, s42, 0x80000\n" \
"s_addc_u32 s43, s43, 0\n" \
"v_add_u32 v233, 0xffff4000, v233\n" \
"v_mfma_f32_16x16x32_bf16 v[172:175], v[24:27], v[216:219], v[172:175]\n" \
"s_waitcnt vmcnt(21) lgkmcnt(0)\n" \
"v_mfma_f32_16x16x32_bf16 v[172:175], v[28:31], v[220:223], v[172:175]\n" \
"s_barrier\n" \
"v_mfma_f32_16x16x32_bf16 v[80:83], v[32:35], v[192:195], v[80:83]\n" \
"ds_read_b64_tr_b16 v[0:1], v232 offset:0\n" \
"v_mfma_f32_16x16x32_bf16 v[80:83], v[36:39], v[196:199], v[80:83]\n" \
"ds_read_b64_tr_b16 v[2:3], v232 offset:2048\n" \
"v_mfma_f32_16x16x32_bf16 v[84:87], v[40:43], v[192:195], v[84:87]\n" \
"s_add_u32 m0, s87, 102400\n" \
"ds_read_b64_tr_b16 v[4:5], v232 offset:8192\n" \
"v_mfma_f32_16x16x32_bf16 v[84:87], v[44:47], v[196:199], v[84:87]\n" \
"global_load_lds_dwordx4 v224, s[48:49] offset:-4096\n" \
"v_mfma_f32_16x16x32_bf16 v[88:91], v[48:51], v[192:195], v[88:91]\n" \
"ds_read_b64_tr_b16 v[6:7], v232 offset:10240\n" \
"v_mfma_f32_16x16x32_bf16 v[88:91], v[52:55], v[196:199], v[88:91]\n" \
"ds_read_b64_tr_b16 v[8:9], v233 offset:0\n" \
"v_mfma_f32_16x16x32_bf16 v[92:95], v[56:59], v[192:195], v[92:95]\n" \
"ds_read_b64_tr_b16 v[10:11], v233 offset:2048\n" \
"v_mfma_f32_16x16x32_bf16 v[92:95], v[60:63], v[196:199], v[92:95]\n" \
"global_load_lds_dwordx4 v225, s[48:49] offset:-3072\n" \
"v_mfma_f32_16x16x32_bf16 v[112:115], v[32:35], v[200:203], v[112:115]\n" \
"ds_read_b128 v[192:195], v230 offset:0\n" \
"v_mfma_f32_16x16x32_bf16 v[112:115], v[36:39], v[204:207], v[112:115]\n" \
"ds_read_b128 v[196:199], v231 offset:0\n" \
"v_mfma_f32_16x16x32_bf16 v[116:119], v[40:43], v[200:203], v[116:119]\n" \
"ds_read_b64_tr_b16 v[12:13], v233 offset:8192\n" \
"v_mfma_f32_16x16x32_bf16 v[116:119], v[44:47], v[204:207], v[116:119]\n" \
"global_load_lds_dwordx4 v226, s[48:49] offset:-2048\n" \
"v_mfma_f32_16x16x32_bf16 v[120:123], v[48:51], v[200:203], v[120:123]\n" \
"ds_read_b64_tr_b16 v[14:15], v233 offset:10240\n" \
"v_mfma_f32_16x16x32_bf16 v[120:123], v[52:55], v[204:207], v[120:123]\n" \
"ds_read_b64_tr_b16 v[16:17], v232 offset:512\n" \
"v_mfma_f32_16x16x32_bf16 v[124:127], v[56:59], v[200:203], v[124:127]\n" \
"ds_read_b64_tr_b16 v[18:19], v232 offset:2560\n" \
"v_mfma_f32_16x16x32_bf16 v[124:127], v[60:63], v[204:207], v[124:127]\n" \
"global_load_lds_dwordx4 v227, s[48:49] offset:-1024\n" \
"v_mfma_f32_16x16x32_bf16 v[144:147], v[32:35], v[208:211], v[144:147]\n" \
"s_add_u32 s48, s48, 0x80\n" \
"s_addc_u32 s49, s49, 0\n" \
"ds_read_b128 v[200:203], v230 offset:2048\n" \
"v_mfma_f32_16x16x32_bf16 v[144:147], v[36:39], v[212:215], v[144:147]\n" \
"ds_read_b128 v[204:207], v231 offset:2048\n" \
"v_mfma_f32_16x16x32_bf16 v[148:151], v[40:43], v[208:211], v[148:151]\n" \
"ds_read_b64_tr_b16 v[20:21], v232 offset:8704\n" \
"v_mfma_f32_16x16x32_bf16 v[148:151], v[44:47], v[212:215], v[148:151]\n" \
"global_load_lds_dwordx4 v224, s[46:47] offset:0\n" \
"v_mfma_f32_16x16x32_bf16 v[152:155], v[48:51], v[208:211], v[152:155]\n" \
"ds_read_b64_tr_b16 v[22:23], v232 offset:10752\n" \
"v_mfma_f32_16x16x32_bf16 v[152:155], v[52:55], v[212:215], v[152:155]\n" \
"ds_read_b64_tr_b16 v[24:25], v233 offset:512\n" \
"v_mfma_f32_16x16x32_bf16 v[156:159], v[56:59], v[208:211], v[156:159]\n" \
"ds_read_b64_tr_b16 v[26:27], v233 offset:2560\n" \
"v_mfma_f32_16x16x32_bf16 v[156:159], v[60:63], v[212:215], v[156:159]\n" \
"global_load_lds_dwordx4 v225, s[46:47] offset:1024\n" \
"v_mfma_f32_16x16x32_bf16 v[176:179], v[32:35], v[216:219], v[176:179]\n" \
"ds_read_b128 v[208:211], v230 offset:4096\n" \
"v_mfma_f32_16x16x32_bf16 v[176:179], v[36:39], v[220:223], v[176:179]\n" \
"ds_read_b128 v[212:215], v231 offset:4096\n" \
"v_mfma_f32_16x16x32_bf16 v[180:183], v[40:43], v[216:219], v[180:183]\n" \
"ds_read_b64_tr_b16 v[28:29], v233 offset:8704\n" \
"v_mfma_f32_16x16x32_bf16 v[180:183], v[44:47], v[220:223], v[180:183]\n" \
"global_load_lds_dwordx4 v226, s[46:47] offset:2048\n" \
"v_mfma_f32_16x16x32_bf16 v[184:187], v[48:51], v[216:219], v[184:187]\n" \
"ds_read_b64_tr_b16 v[30:31], v233 offset:10752\n" \
"Lpg4356_%=:\n" \
"s_cbranch_execz Lpg5547_%=\n" \
"v_mfma_f32_16x16x32_bf16 v[184:187], v[52:55], v[220:223], v[184:187]\n" \
"s_cmp_eq_u32 s63, 5\n" \
"s_cselect_b64 s[40:41], s[18:19], s[40:41]\n" \
"s_cselect_b32 s80, 4, s80\n" \
"v_mfma_f32_16x16x32_bf16 v[188:191], v[56:59], v[216:219], v[188:191]\n" \
"s_cmp_eq_u32 s63, 4\n" \
"s_cselect_b64 s[40:41], s[20:21], s[40:41]\n" \
"s_cselect_b32 s80, 8, s80\n" \
"v_mfma_f32_16x16x32_bf16 v[188:191], v[60:63], v[220:223], v[188:191]\n" \
"s_waitcnt lgkmcnt(1)\n" \
"global_load_lds_dwordx4 v227, s[46:47] offset:3072\n" \
"v_mfma_f32_16x16x32_bf16 v[64:67], v[0:3], v[192:195], v[64:67]\n" \
"s_add_u32 s46, s46, 0x80\n" \
"s_addc_u32 s47, s47, 0\n" \
"ds_read_b128 v[216:219], v230 offset:6144\n" \
"v_mfma_f32_16x16x32_bf16 v[64:67], v[4:7], v[196:199], v[64:67]\n" \
"ds_read_b128 v[220:223], v231 offset:6144\n" \
"v_mfma_f32_16x16x32_bf16 v[68:71], v[8:11], v[192:195], v[68:71]\n" \
"ds_read_b64_tr_b16 v[32:33], v232 offset:1024\n" \
"v_mfma_f32_16x16x32_bf16 v[68:71], v[12:15], v[196:199], v[68:71]\n" \
"buffer_store_dwordx4 a[96:99], v249, s[56:59], s90 offen offset:256 sc1\n" \
"s_cmp_eq_u32 s63, 2\n" \
"s_cselect_b64 s[40:41], s[24:25], s[40:41]\n" \
"s_cselect_b32 s80, 4, s80\n" \
"v_mfma_f32_16x16x32_bf16 v[72:75], v[16:19], v[192:195], v[72:75]\n" \
"s_cmp_eq_u32 s63, 5\n" \
"s_cselect_b32 s81, 384, 0\n" \
"ds_read_b64_tr_b16 v[34:35], v232 offset:3072\n" \
"v_mfma_f32_16x16x32_bf16 v[72:75], v[20:23], v[196:199], v[72:75]\n" \
"s_waitcnt lgkmcnt(3)\n" \
"s_add_u32 s81, s81, s62\n" \
"ds_read_b64_tr_b16 v[36:37], v232 offset:9216\n" \
"v_mfma_f32_16x16x32_bf16 v[76:79], v[24:27], v[192:195], v[76:79]\n" \
"s_lshl_b32 s81, s81, 14\n" \
"s_add_u32 s81, s81, s95\n" \
"ds_read_b64_tr_b16 v[38:39], v232 offset:11264\n" \
"v_mfma_f32_16x16x32_bf16 v[76:79], v[28:31], v[196:199], v[76:79]\n" \
"buffer_store_dwordx4 a[72:75], v249, s[56:59], s90 offen offset:2048 sc1\n" \
"ds_read_b128 a[96:99], v254\n" \
"s_add_u32 s40, s40, s81\n" \
"s_addc_u32 s41, s41, 0\n" \
"v_lshlrev_b32 v252, 2, v248\n" \
"v_mfma_f32_16x16x32_bf16 v[96:99], v[0:3], v[200:203], v[96:99]\n" \
"ds_read_b128 a[100:103], v254\n" \
"ds_read_b64_tr_b16 v[40:41], v233 offset:1024\n" \
"v_mfma_f32_16x16x32_bf16 v[96:99], v[4:7], v[204:207], v[96:99]\n" \
"ds_read_b64_tr_b16 v[42:43], v233 offset:3072\n" \
"v_mfma_f32_16x16x32_bf16 v[100:103], v[8:11], v[200:203], v[100:103]\n" \
"ds_read_b128 a[72:75], v254\n" \
"ds_read_b64_tr_b16 v[44:45], v233 offset:9216\n" \
"v_mfma_f32_16x16x32_bf16 v[100:103], v[12:15], v[204:207], v[100:103]\n" \
"ds_read_b128 a[76:79], v254\n" \
"v_mfma_f32_16x16x32_bf16 v[104:107], v[16:19], v[200:203], v[104:107]\n" \
"ds_read_b64_tr_b16 v[46:47], v233 offset:11264\n" \
"v_mfma_f32_16x16x32_bf16 v[104:107], v[20:23], v[204:207], v[104:107]\n" \
"ds_read_b64_tr_b16 v[48:49], v232 offset:1536\n" \
"v_mfma_f32_16x16x32_bf16 v[108:111], v[24:27], v[200:203], v[108:111]\n" \
"s_add_u32 m0, s95, 53248\n" \
"ds_read_b64_tr_b16 v[50:51], v232 offset:3584\n" \
"v_mfma_f32_16x16x32_bf16 v[108:111], v[28:31], v[204:207], v[108:111]\n" \
"global_load_lds_dwordx4 v228, s[44:45] offset:-4096\n" \
"v_mfma_f32_16x16x32_bf16 v[128:131], v[0:3], v[208:211], v[128:131]\n" \
"ds_read_b64_tr_b16 v[52:53], v232 offset:9728\n" \
"v_mfma_f32_16x16x32_bf16 v[128:131], v[4:7], v[212:215], v[128:131]\n" \
"ds_read_b64_tr_b16 v[54:55], v232 offset:11776\n" \
"v_mfma_f32_16x16x32_bf16 v[132:135], v[8:11], v[208:211], v[132:135]\n" \
"ds_read_b64_tr_b16 v[56:57], v233 offset:1536\n" \
"v_mfma_f32_16x16x32_bf16 v[132:135], v[12:15], v[212:215], v[132:135]\n" \
"global_load_lds_dwordx4 v229, s[44:45] offset:-3072\n" \
"v_mfma_f32_16x16x32_bf16 v[136:139], v[16:19], v[208:211], v[136:139]\n" \
"ds_read_b64_tr_b16 v[58:59], v233 offset:3584\n" \
"v_mfma_f32_16x16x32_bf16 v[136:139], v[20:23], v[212:215], v[136:139]\n" \
"ds_read_b64_tr_b16 v[60:61], v233 offset:9728\n" \
"v_mfma_f32_16x16x32_bf16 v[140:143], v[24:27], v[208:211], v[140:143]\n" \
"ds_read_b64_tr_b16 v[62:63], v233 offset:11776\n" \
"v_mfma_f32_16x16x32_bf16 v[140:143], v[28:31], v[212:215], v[140:143]\n" \
"s_waitcnt lgkmcnt(3)\n" \
"global_load_lds_dwordx4 v228, s[42:43] offset:-2048\n" \
"v_mfma_f32_16x16x32_bf16 v[160:163], v[0:3], v[216:219], v[160:163]\n" \
"v_add_u32 v230, 0xffff0000, v230\n" \
"v_add_u32 v231, 0xffff0000, v231\n" \
"v_mfma_f32_16x16x32_bf16 v[160:163], v[4:7], v[220:223], v[160:163]\n" \
"v_add_u32 v232, 0x4000, v232\n" \
"v_add_u32 v233, 0x4000, v233\n" \
"v_mfma_f32_16x16x32_bf16 v[164:167], v[8:11], v[216:219], v[164:167]\n" \
"v_mfma_f32_16x16x32_bf16 v[164:167], v[12:15], v[220:223], v[164:167]\n" \
"global_load_lds_dwordx4 v229, s[42:43] offset:-1024\n" \
"v_mfma_f32_16x16x32_bf16 v[168:171], v[16:19], v[216:219], v[168:171]\n" \
"v_mfma_f32_16x16x32_bf16 v[168:171], v[20:23], v[220:223], v[168:171]\n" \
"v_mfma_f32_16x16x32_bf16 v[172:175], v[24:27], v[216:219], v[172:175]\n" \
"s_waitcnt vmcnt(22) lgkmcnt(0)\n" \
"v_mfma_f32_16x16x32_bf16 v[172:175], v[28:31], v[220:223], v[172:175]\n" \
"s_barrier\n" \
"v_mfma_f32_16x16x32_bf16 v[80:83], v[32:35], v[192:195], v[80:83]\n" \
"s_waitcnt vmcnt(19)\n" \
"ds_read_b64_tr_b16 v[0:1], v232 offset:0\n" \
"v_cmp_ne_u32 vcc, s60, v244\n" \
"v_mfma_f32_16x16x32_bf16 v[80:83], v[36:39], v[196:199], v[80:83]\n" \
"ds_read_b64_tr_b16 v[2:3], v232 offset:2048\n" \
"v_mfma_f32_16x16x32_bf16 v[84:87], v[40:43], v[192:195], v[84:87]\n" \
"s_add_u32 m0, s87, 135168\n" \
"ds_read_b64_tr_b16 v[4:5], v232 offset:8192\n" \
"v_mfma_f32_16x16x32_bf16 v[84:87], v[44:47], v[196:199], v[84:87]\n" \
"s_and_b32 vcc_hi, vcc_lo, 255\n" \
"s_cbranch_scc1 Lpl1_%=\n" \
"Lpr1_%=:\n" \
"global_load_lds_dwordx4 v224, s[48:49] offset:-4096\n" \
"v_mfma_f32_16x16x32_bf16 v[88:91], v[48:51], v[192:195], v[88:91]\n" \
"s_add_u32 s79, s91, 12288\n" \
"ds_read_b64_tr_b16 v[6:7], v232 offset:10240\n" \
"v_mfma_f32_16x16x32_bf16 v[88:91], v[52:55], v[196:199], v[88:91]\n" \
"ds_read_b64_tr_b16 v[8:9], v233 offset:0\n" \
"v_mfma_f32_16x16x32_bf16 v[92:95], v[56:59], v[192:195], v[92:95]\n" \
"ds_read_b64_tr_b16 v[10:11], v233 offset:2048\n" \
"v_mfma_f32_16x16x32_bf16 v[92:95], v[60:63], v[196:199], v[92:95]\n" \
"global_load_lds_dwordx4 v225, s[48:49] offset:-3072\n" \
"v_mfma_f32_16x16x32_bf16 v[112:115], v[32:35], v[200:203], v[112:115]\n" \
"ds_read_b128 v[192:195], v230 offset:0\n" \
"v_mfma_f32_16x16x32_bf16 v[112:115], v[36:39], v[204:207], v[112:115]\n" \
"ds_read_b128 v[196:199], v231 offset:0\n" \
"v_mfma_f32_16x16x32_bf16 v[116:119], v[40:43], v[200:203], v[116:119]\n" \
"ds_read_b64_tr_b16 v[12:13], v233 offset:8192\n" \
"v_mfma_f32_16x16x32_bf16 v[116:119], v[44:47], v[204:207], v[116:119]\n" \
"global_load_lds_dwordx4 v226, s[48:49] offset:-2048\n" \
"v_mfma_f32_16x16x32_bf16 v[120:123], v[48:51], v[200:203], v[120:123]\n" \
"ds_read_b64_tr_b16 v[14:15], v233 offset:10240\n" \
"v_mfma_f32_16x16x32_bf16 v[120:123], v[52:55], v[204:207], v[120:123]\n" \
"ds_read_b64_tr_b16 v[16:17], v232 offset:512\n" \
"v_mfma_f32_16x16x32_bf16 v[124:127], v[56:59], v[200:203], v[124:127]\n" \
"ds_read_b64_tr_b16 v[18:19], v232 offset:2560\n" \
"v_mfma_f32_16x16x32_bf16 v[124:127], v[60:63], v[204:207], v[124:127]\n" \
"global_load_lds_dwordx4 v227, s[48:49] offset:-1024\n" \
"v_mfma_f32_16x16x32_bf16 v[144:147], v[32:35], v[208:211], v[144:147]\n" \
"ds_read_b128 v[200:203], v230 offset:2048\n" \
"v_mfma_f32_16x16x32_bf16 v[144:147], v[36:39], v[212:215], v[144:147]\n" \
"ds_read_b128 v[204:207], v231 offset:2048\n" \
"v_mfma_f32_16x16x32_bf16 v[148:151], v[40:43], v[208:211], v[148:151]\n" \
"ds_read_b64_tr_b16 v[20:21], v232 offset:8704\n" \
"v_mfma_f32_16x16x32_bf16 v[148:151], v[44:47], v[212:215], v[148:151]\n" \
"global_load_lds_dwordx4 v224, s[46:47] offset:0\n" \
"v_mfma_f32_16x16x32_bf16 v[152:155], v[48:51], v[208:211], v[152:155]\n" \
"ds_read_b64_tr_b16 v[22:23], v232 offset:10752\n" \
"v_mfma_f32_16x16x32_bf16 v[152:155], v[52:55], v[212:215], v[152:155]\n" \
"ds_read_b64_tr_b16 v[24:25], v233 offset:512\n" \
"v_mfma_f32_16x16x32_bf16 v[156:159], v[56:59], v[208:211], v[156:159]\n" \
"ds_read_b64_tr_b16 v[26:27], v233 offset:2560\n" \
"v_mfma_f32_16x16x32_bf16 v[156:159], v[60:63], v[212:215], v[156:159]\n" \
"global_load_lds_dwordx4 v225, s[46:47] offset:1024\n" \
"v_mfma_f32_16x16x32_bf16 v[176:179], v[32:35], v[216:219], v[176:179]\n" \
"ds_read_b128 v[208:211], v230 offset:4096\n" \
"v_mfma_f32_16x16x32_bf16 v[176:179], v[36:39], v[220:223], v[176:179]\n" \
"ds_read_b128 v[212:215], v231 offset:4096\n" \
"v_mfma_f32_16x16x32_bf16 v[180:183], v[40:43], v[216:219], v[180:183]\n" \
"ds_read_b64_tr_b16 v[28:29], v233 offset:8704\n" \
"v_mfma_f32_16x16x32_bf16 v[180:183], v[44:47], v[220:223], v[180:183]\n" \
"global_load_lds_dwordx4 v226, s[46:47] offset:2048\n" \
"v_mfma_f32_16x16x32_bf16 v[184:187], v[48:51], v[216:219], v[184:187]\n" \
"ds_read_b64_tr_b16 v[30:31], v233 offset:10752\n" \
"v_mfma_f32_16x16x32_bf16 v[184:187], v[52:55], v[220:223], v[184:187]\n" \
"v_mfma_f32_16x16x32_bf16 v[188:191], v[56:59], v[216:219], v[188:191]\n" \
"v_mfma_f32_16x16x32_bf16 v[188:191], v[60:63], v[220:223], v[188:191]\n" \
"s_waitcnt lgkmcnt(1)\n" \
"global_load_lds_dwordx4 v227, s[46:47] offset:3072\n" \
"v_mfma_f32_16x16x32_bf16 v[64:67], v[0:3], v[192:195], v[64:67]\n" \
"ds_read_b128 v[216:219], v230 offset:6144\n" \
"v_mfma_f32_16x16x32_bf16 v[64:67], v[4:7], v[196:199], v[64:67]\n" \
"ds_read_b128 v[220:223], v231 offset:6144\n" \
"v_mfma_f32_16x16x32_bf16 v[68:71], v[8:11], v[192:195], v[68:71]\n" \
"ds_read_b64_tr_b16 v[32:33], v232 offset:1024\n" \
"v_mfma_f32_16x16x32_bf16 v[68:71], v[12:15], v[196:199], v[68:71]\n" \
"buffer_store_dwordx4 a[104:107], v249, s[56:59], s90 offen offset:2304 sc1\n" \
"v_mfma_f32_16x16x32_bf16 v[72:75], v[16:19], v[192:195], v[72:75]\n" \
"s_add_u32 s90, s89, 278528\n" \
"ds_read_b64_tr_b16 v[34:35], v232 offset:3072\n" \
"v_mfma_f32_16x16x32_bf16 v[72:75], v[20:23], v[196:199], v[72:75]\n" \
"s_waitcnt lgkmcnt(3)\n" \
"ds_read_b64_tr_b16 v[36:37], v232 offset:9216\n" \
"v_mfma_f32_16x16x32_bf16 v[76:79], v[24:27], v[192:195], v[76:79]\n" \
"ds_read_b128 a[104:107], v254\n" \
"ds_read_b64_tr_b16 v[38:39], v232 offset:11264\n" \
"v_mfma_f32_16x16x32_bf16 v[76:79], v[28:31], v[196:199], v[76:79]\n" \
"buffer_store_dwordx4 a[80:83], v248, s[56:59], s90 offen offset:0 sc1\n" \
"ds_read_b128 a[108:111], v254\n" \
"v_mfma_f32_16x16x32_bf16 v[96:99], v[0:3], v[200:203], v[96:99]\n" \
"ds_read_b64_tr_b16 v[40:41], v233 offset:1024\n" \
"v_mfma_f32_16x16x32_bf16 v[96:99], v[4:7], v[204:207], v[96:99]\n" \
"ds_read_b64_tr_b16 v[42:43], v233 offset:3072\n" \
"v_mfma_f32_16x16x32_bf16 v[100:103], v[8:11], v[200:203], v[100:103]\n" \
"ds_read_b128 a[80:83], v254\n" \
"ds_read_b64_tr_b16 v[44:45], v233 offset:9216\n" \
"v_mfma_f32_16x16x32_bf16 v[100:103], v[12:15], v[204:207], v[100:103]\n" \
"buffer_store_dwordx4 a[84:87], v248, s[56:59], s90 offen offset:1024 sc1\n" \
"ds_read_b128 a[112:115], v254\n" \
"v_mfma_f32_16x16x32_bf16 v[104:107], v[16:19], v[200:203], v[104:107]\n" \
"ds_read_b64_tr_b16 v[46:47], v233 offset:11264\n" \
"v_mfma_f32_16x16x32_bf16 v[104:107], v[20:23], v[204:207], v[104:107]\n" \
"ds_read_b64_tr_b16 v[48:49], v232 offset:1536\n" \
"v_mfma_f32_16x16x32_bf16 v[108:111], v[24:27], v[200:203], v[108:111]\n" \
"s_add_u32 m0, s95, 0\n" \
"ds_read_b128 a[84:87], v254\n" \
"ds_read_b64_tr_b16 v[50:51], v232 offset:3584\n" \
"v_mfma_f32_16x16x32_bf16 v[108:111], v[28:31], v[204:207], v[108:111]\n" \
"ds_read_b128 a[116:119], v254\n" \
"global_load_lds_dwordx4 v250, s[36:37] offset:0 sc1\n" \
"v_mfma_f32_16x16x32_bf16 v[128:131], v[0:3], v[208:211], v[128:131]\n" \
"ds_read_b64_tr_b16 v[52:53], v232 offset:9728\n" \
"v_mfma_f32_16x16x32_bf16 v[128:131], v[4:7], v[212:215], v[128:131]\n" \
"ds_read_b64_tr_b16 v[54:55], v232 offset:11776\n" \
"v_mfma_f32_16x16x32_bf16 v[132:135], v[8:11], v[208:211], v[132:135]\n" \
"ds_read_b64_tr_b16 v[56:57], v233 offset:1536\n" \
"v_mfma_f32_16x16x32_bf16 v[132:135], v[12:15], v[212:215], v[132:135]\n" \
"global_load_lds_dwordx4 v250, s[36:37] offset:1024 sc1\n" \
"v_mfma_f32_16x16x32_bf16 v[136:139], v[16:19], v[208:211], v[136:139]\n" \
"ds_read_b64_tr_b16 v[58:59], v233 offset:3584\n" \
"v_mfma_f32_16x16x32_bf16 v[136:139], v[20:23], v[212:215], v[136:139]\n" \
"ds_read_b64_tr_b16 v[60:61], v233 offset:9728\n" \
"v_mfma_f32_16x16x32_bf16 v[140:143], v[24:27], v[208:211], v[140:143]\n" \
"ds_read_b64_tr_b16 v[62:63], v233 offset:11776\n" \
"v_mfma_f32_16x16x32_bf16 v[140:143], v[28:31], v[212:215], v[140:143]\n" \
"s_waitcnt lgkmcnt(3)\n" \
"global_load_lds_dwordx4 v250, s[36:37] offset:2048 sc1\n" \
"v_mfma_f32_16x16x32_bf16 v[160:163], v[0:3], v[216:219], v[160:163]\n" \
"v_add_u32 v230, 0x8000, v230\n" \
"v_add_u32 v231, 0x8000, v231\n" \
"v_mfma_f32_16x16x32_bf16 v[160:163], v[4:7], v[220:223], v[160:163]\n" \
"v_add_u32 v232, 0x4000, v232\n" \
"v_add_u32 v233, 0x4000, v233\n" \
"v_mfma_f32_16x16x32_bf16 v[164:167], v[8:11], v[216:219], v[164:167]\n" \
"v_mfma_f32_16x16x32_bf16 v[164:167], v[12:15], v[220:223], v[164:167]\n" \
"global_load_lds_dwordx4 v250, s[36:37] offset:3072 sc1\n" \
"v_mfma_f32_16x16x32_bf16 v[168:171], v[16:19], v[216:219], v[168:171]\n" \
"s_add_u32 s36, s36, 17408\n" \
"v_mfma_f32_16x16x32_bf16 v[168:171], v[20:23], v[220:223], v[168:171]\n" \
"v_mfma_f32_16x16x32_bf16 v[172:175], v[24:27], v[216:219], v[172:175]\n" \
"s_waitcnt vmcnt(21) lgkmcnt(0)\n" \
"v_mfma_f32_16x16x32_bf16 v[172:175], v[28:31], v[220:223], v[172:175]\n" \
"s_barrier\n" \
"v_mfma_f32_16x16x32_bf16 v[80:83], v[32:35], v[192:195], v[80:83]\n" \
"s_waitcnt vmcnt(33)\n" \
"ds_read_b64_tr_b16 v[0:1], v232 offset:0\n" \
"v_cmp_eq_u32 vcc, s60, v246\n" \
"v_mfma_f32_16x16x32_bf16 v[80:83], v[36:39], v[196:199], v[80:83]\n" \
"ds_read_b64_tr_b16 v[2:3], v232 offset:2048\n" \
"v_mfma_f32_16x16x32_bf16 v[84:87], v[40:43], v[192:195], v[84:87]\n" \
"s_add_u32 m0, s95, 16384\n" \
"ds_read_b64_tr_b16 v[4:5], v232 offset:8192\n" \
"v_mfma_f32_16x16x32_bf16 v[84:87], v[44:47], v[196:199], v[84:87]\n" \
"global_load_lds_dwordx4 v250, s[36:37] offset:0 sc1\n" \
"v_mfma_f32_16x16x32_bf16 v[88:91], v[48:51], v[192:195], v[88:91]\n" \
"ds_read_b64_tr_b16 v[6:7], v232 offset:10240\n" \
"v_mfma_f32_16x16x32_bf16 v[88:91], v[52:55], v[196:199], v[88:91]\n" \
"ds_read_b64_tr_b16 v[8:9], v233 offset:0\n" \
"v_mfma_f32_16x16x32_bf16 v[92:95], v[56:59], v[192:195], v[92:95]\n" \
"ds_read_b64_tr_b16 v[10:11], v233 offset:2048\n" \
"v_mfma_f32_16x16x32_bf16 v[92:95], v[60:63], v[196:199], v[92:95]\n" \
"global_load_lds_dwordx4 v250, s[36:37] offset:1024 sc1\n" \
"v_mfma_f32_16x16x32_bf16 v[112:115], v[32:35], v[200:203], v[112:115]\n" \
"ds_read_b128 v[192:195], v230 offset:0\n" \
"v_mfma_f32_16x16x32_bf16 v[112:115], v[36:39], v[204:207], v[112:115]\n" \
"ds_read_b128 v[196:199], v231 offset:0\n" \
"v_mfma_f32_16x16x32_bf16 v[116:119], v[40:43], v[200:203], v[116:119]\n" \
"ds_read_b64_tr_b16 v[12:13], v233 offset:8192\n" \
"v_mfma_f32_16x16x32_bf16 v[116:119], v[44:47], v[204:207], v[116:119]\n" \
"global_load_lds_dwordx4 v250, s[36:37] offset:2048 sc1\n" \
"v_mfma_f32_16x16x32_bf16 v[120:123], v[48:51], v[200:203], v[120:123]\n" \
"ds_read_b64_tr_b16 v[14:15], v233 offset:10240\n" \
"v_mfma_f32_16x16x32_bf16 v[120:123], v[52:55], v[204:207], v[120:123]\n" \
"ds_read_b64_tr_b16 v[16:17], v232 offset:512\n" \
"v_mfma_f32_16x16x32_bf16 v[124:127], v[56:59], v[200:203], v[124:127]\n" \
"ds_read_b64_tr_b16 v[18:19], v232 offset:2560\n" \
"v_mfma_f32_16x16x32_bf16 v[124:127], v[60:63], v[204:207], v[124:127]\n" \
"global_load_lds_dwordx4 v250, s[36:37] offset:3072 sc1\n" \
"v_mfma_f32_16x16x32_bf16 v[144:147], v[32:35], v[208:211], v[144:147]\n" \
"ds_read_b128 v[200:203], v230 offset:2048\n" \
"s_add_u32 s36, s36, 17408\n" \
"v_mfma_f32_16x16x32_bf16 v[144:147], v[36:39], v[212:215], v[144:147]\n" \
"ds_read_b128 v[204:207], v231 offset:2048\n" \
"v_mfma_f32_16x16x32_bf16 v[148:151], v[40:43], v[208:211], v[148:151]\n" \
"ds_read_b64_tr_b16 v[20:21], v232 offset:8704\n" \
"v_mfma_f32_16x16x32_bf16 v[148:151], v[44:47], v[212:215], v[148:151]\n" \
"buffer_store_dwordx4 a[88:91], v248, s[56:59], s90 offen offset:2048 sc1\n" \
"s_and_b32 s30, vcc_lo, 3\n" \
"s_cbranch_scc0 Lpl2_%=\n" \
"Lpr2_%=:\n" \
"v_mfma_f32_16x16x32_bf16 v[152:155], v[48:51], v[208:211], v[152:155]\n" \
"ds_read_b64_tr_b16 v[22:23], v232 offset:10752\n" \
"v_mfma_f32_16x16x32_bf16 v[152:155], v[52:55], v[212:215], v[152:155]\n" \
"ds_read_b64_tr_b16 v[24:25], v233 offset:512\n" \
"v_mfma_f32_16x16x32_bf16 v[156:159], v[56:59], v[208:211], v[156:159]\n" \
"ds_read_b128 a[88:91], v254\n" \
"ds_read_b64_tr_b16 v[26:27], v233 offset:2560\n" \
"v_mfma_f32_16x16x32_bf16 v[156:159], v[60:63], v[212:215], v[156:159]\n" \
"buffer_store_dwordx4 a[92:95], v248, s[56:59], s90 offen offset:3072 sc1\n" \
"ds_read_b128 a[120:123], v254\n" \
"v_mfma_f32_16x16x32_bf16 v[176:179], v[32:35], v[216:219], v[176:179]\n" \
"ds_read_b128 v[208:211], v230 offset:4096\n" \
"s_add_u32 s90, s88, 417792\n" \
"v_mfma_f32_16x16x32_bf16 v[176:179], v[36:39], v[220:223], v[176:179]\n" \
"ds_read_b128 v[212:215], v231 offset:4096\n" \
"v_mfma_f32_16x16x32_bf16 v[180:183], v[40:43], v[216:219], v[180:183]\n" \
"ds_read_b128 a[92:95], v254\n" \
"ds_read_b64_tr_b16 v[28:29], v233 offset:8704\n" \
"v_mfma_f32_16x16x32_bf16 v[180:183], v[44:47], v[220:223], v[180:183]\n" \
"buffer_store_dwordx4 a[0:3], v249, s[56:59], s90 offen offset:0 sc1\n" \
"ds_read_b128 a[124:127], v254\n" \
"v_mfma_f32_16x16x32_bf16 v[184:187], v[48:51], v[216:219], v[184:187]\n" \
"ds_read_b64_tr_b16 v[30:31], v233 offset:10752\n" \
"v_mfma_f32_16x16x32_bf16 v[184:187], v[52:55], v[220:223], v[184:187]\n" \
"v_mfma_f32_16x16x32_bf16 v[188:191], v[56:59], v[216:219], v[188:191]\n" \
"ds_read_b128 a[0:3], v254\n" \
"v_mfma_f32_16x16x32_bf16 v[188:191], v[60:63], v[220:223], v[188:191]\n" \
"s_waitcnt lgkmcnt(3)\n" \
"ds_read_b128 a[4:7], v254\n" \
"v_mfma_f32_16x16x32_bf16 v[64:67], v[0:3], v[192:195], v[64:67]\n" \
"ds_read_b128 v[216:219], v230 offset:6144\n" \
"v_mfma_f32_16x16x32_bf16 v[64:67], v[4:7], v[196:199], v[64:67]\n" \
"ds_read_b128 v[220:223], v231 offset:6144\n" \
"v_mfma_f32_16x16x32_bf16 v[68:71], v[8:11], v[192:195], v[68:71]\n" \
"ds_read_b64_tr_b16 v[32:33], v232 offset:1024\n" \
"v_mfma_f32_16x16x32_bf16 v[68:71], v[12:15], v[196:199], v[68:71]\n" \
"v_mfma_f32_16x16x32_bf16 v[72:75], v[16:19], v[192:195], v[72:75]\n" \
"ds_read_b64_tr_b16 v[34:35], v232 offset:3072\n" \
"v_mfma_f32_16x16x32_bf16 v[72:75], v[20:23], v[196:199], v[72:75]\n" \
"s_waitcnt lgkmcnt(3)\n" \
"ds_read_b64_tr_b16 v[36:37], v232 offset:9216\n" \
"v_mfma_f32_16x16x32_bf16 v[76:79], v[24:27], v[192:195], v[76:79]\n" \
"ds_read_b64_tr_b16 v[38:39], v232 offset:11264\n" \
"v_mfma_f32_16x16x32_bf16 v[76:79], v[28:31], v[196:199], v[76:79]\n" \
"v_mfma_f32_16x16x32_bf16 v[96:99], v[0:3], v[200:203], v[96:99]\n" \
"ds_read_b64_tr_b16 v[40:41], v233 offset:1024\n" \
"v_mfma_f32_16x16x32_bf16 v[96:99], v[4:7], v[204:207], v[96:99]\n" \
"ds_read_b64_tr_b16 v[42:43], v233 offset:3072\n" \
"v_mfma_f32_16x16x32_bf16 v[100:103], v[8:11], v[200:203], v[100:103]\n" \
"ds_read_b64_tr_b16 v[44:45], v233 offset:9216\n" \
"v_mfma_f32_16x16x32_bf16 v[100:103], v[12:15], v[204:207], v[100:103]\n" \
"v_mfma_f32_16x16x32_bf16 v[104:107], v[16:19], v[200:203], v[104:107]\n" \
"ds_read_b64_tr_b16 v[46:47], v233 offset:11264\n" \
"v_mfma_f32_16x16x32_bf16 v[104:107], v[20:23], v[204:207], v[104:107]\n" \
"ds_read_b64_tr_b16 v[48:49], v232 offset:1536\n" \
"v_mfma_f32_16x16x32_bf16 v[108:111], v[24:27], v[200:203], v[108:111]\n" \
"ds_read_b64_tr_b16 v[50:51], v232 offset:3584\n" \
"v_mfma_f32_16x16x32_bf16 v[108:111], v[28:31], v[204:207], v[108:111]\n" \
"v_mfma_f32_16x16x32_bf16 v[128:131], v[0:3], v[208:211], v[128:131]\n" \
"ds_read_b64_tr_b16 v[52:53], v232 offset:9728\n" \
"v_mfma_f32_16x16x32_bf16 v[128:131], v[4:7], v[212:215], v[128:131]\n" \
"ds_read_b64_tr_b16 v[54:55], v232 offset:11776\n" \
"v_mfma_f32_16x16x32_bf16 v[132:135], v[8:11], v[208:211], v[132:135]\n" \
"ds_read_b64_tr_b16 v[56:57], v233 offset:1536\n" \
"v_mfma_f32_16x16x32_bf16 v[132:135], v[12:15], v[212:215], v[132:135]\n" \
"v_mfma_f32_16x16x32_bf16 v[136:139], v[16:19], v[208:211], v[136:139]\n" \
"ds_read_b64_tr_b16 v[58:59], v233 offset:3584\n" \
"v_mfma_f32_16x16x32_bf16 v[136:139], v[20:23], v[212:215], v[136:139]\n" \
"ds_read_b64_tr_b16 v[60:61], v233 offset:9728\n" \
"v_mfma_f32_16x16x32_bf16 v[140:143], v[24:27], v[208:211], v[140:143]\n" \
"ds_read_b64_tr_b16 v[62:63], v233 offset:11776\n" \
"v_mfma_f32_16x16x32_bf16 v[140:143], v[28:31], v[212:215], v[140:143]\n" \
"s_waitcnt lgkmcnt(3)\n" \
"v_mfma_f32_16x16x32_bf16 v[160:163], v[0:3], v[216:219], v[160:163]\n" \
"v_add_u32 v230, 0x8000, v230\n" \
"v_add_u32 v231, 0x8000, v231\n" \
"v_mfma_f32_16x16x32_bf16 v[160:163], v[4:7], v[220:223], v[160:163]\n" \
"v_add_u32 v232, 0x4000, v232\n" \
"v_add_u32 v233, 0x4000, v233\n" \
"v_mfma_f32_16x16x32_bf16 v[164:167], v[8:11], v[216:219], v[164:167]\n" \
"v_mfma_f32_16x16x32_bf16 v[164:167], v[12:15], v[220:223], v[164:167]\n" \
"v_mfma_f32_16x16x32_bf16 v[168:171], v[16:19], v[216:219], v[168:171]\n" \
"v_mfma_f32_16x16x32_bf16 v[168:171], v[20:23], v[220:223], v[168:171]\n" \
"v_mfma_f32_16x16x32_bf16 v[172:175], v[24:27], v[216:219], v[172:175]\n" \
"s_waitcnt vmcnt(14) lgkmcnt(0)\n" \
"v_mfma_f32_16x16x32_bf16 v[172:175], v[28:31], v[220:223], v[172:175]\n" \
"s_barrier\n" \
"v_mfma_f32_16x16x32_bf16 v[80:83], v[32:35], v[192:195], v[80:83]\n" \
"ds_read_b64_tr_b16 v[0:1], v232 offset:0\n" \
"v_mfma_f32_16x16x32_bf16 v[80:83], v[36:39], v[196:199], v[80:83]\n" \
"ds_read_b64_tr_b16 v[2:3], v232 offset:2048\n" \
"v_mfma_f32_16x16x32_bf16 v[84:87], v[40:43], v[192:195], v[84:87]\n" \
"s_add_u32 m0, s95, 32768\n" \
"ds_read_b64_tr_b16 v[4:5], v232 offset:8192\n" \
"v_mfma_f32_16x16x32_bf16 v[84:87], v[44:47], v[196:199], v[84:87]\n" \
"global_load_lds_dwordx4 v250, s[36:37] offset:0 sc1\n" \
"v_mfma_f32_16x16x32_bf16 v[88:91], v[48:51], v[192:195], v[88:91]\n" \
"ds_read_b64_tr_b16 v[6:7], v232 offset:10240\n" \
"v_mfma_f32_16x16x32_bf16 v[88:91], v[52:55], v[196:199], v[88:91]\n" \
"ds_read_b64_tr_b16 v[8:9], v233 offset:0\n" \
"v_mfma_f32_16x16x32_bf16 v[92:95], v[56:59], v[192:195], v[92:95]\n" \
"ds_read_b64_tr_b16 v[10:11], v233 offset:2048\n" \
"v_mfma_f32_16x16x32_bf16 v[92:95], v[60:63], v[196:199], v[92:95]\n" \
"global_load_lds_dwordx4 v250, s[36:37] offset:1024 sc1\n" \
"v_mfma_f32_16x16x32_bf16 v[112:115], v[32:35], v[200:203], v[112:115]\n" \
"ds_read_b128 v[192:195], v230 offset:0\n" \
"v_mfma_f32_16x16x32_bf16 v[112:115], v[36:39], v[204:207], v[112:115]\n" \
"ds_read_b128 v[196:199], v231 offset:0\n" \
"v_mfma_f32_16x16x32_bf16 v[116:119], v[40:43], v[200:203], v[116:119]\n" \
"ds_read_b64_tr_b16 v[12:13], v233 offset:8192\n" \
"v_mfma_f32_16x16x32_bf16 v[116:119], v[44:47], v[204:207], v[116:119]\n" \
"global_load_lds_dwordx4 v250, s[36:37] offset:2048 sc1\n" \
"v_mfma_f32_16x16x32_bf16 v[120:123], v[48:51], v[200:203], v[120:123]\n" \
"ds_read_b64_tr_b16 v[14:15], v233 offset:10240\n" \
"v_mfma_f32_16x16x32_bf16 v[120:123], v[52:55], v[204:207], v[120:123]\n" \
"ds_read_b64_tr_b16 v[16:17], v232 offset:512\n" \
"v_mfma_f32_16x16x32_bf16 v[124:127], v[56:59], v[200:203], v[124:127]\n" \
"ds_read_b64_tr_b16 v[18:19], v232 offset:2560\n" \
"v_mfma_f32_16x16x32_bf16 v[124:127], v[60:63], v[204:207], v[124:127]\n" \
"global_load_lds_dwordx4 v250, s[36:37] offset:3072 sc1\n" \
"v_mfma_f32_16x16x32_bf16 v[144:147], v[32:35], v[208:211], v[144:147]\n" \
"ds_read_b128 v[200:203], v230 offset:2048\n" \
"s_add_u32 s36, s36, 17408\n" \
"v_mfma_f32_16x16x32_bf16 v[144:147], v[36:39], v[212:215], v[144:147]\n" \
"ds_read_b128 v[204:207], v231 offset:2048\n" \
"v_mfma_f32_16x16x32_bf16 v[148:151], v[40:43], v[208:211], v[148:151]\n" \
"ds_read_b64_tr_b16 v[20:21], v232 offset:8704\n" \
"v_mfma_f32_16x16x32_bf16 v[148:151], v[44:47], v[212:215], v[148:151]\n" \
"buffer_store_dwordx4 a[32:35], v249, s[56:59], s90 offen offset:256 sc1\n" \
"v_mfma_f32_16x16x32_bf16 v[152:155], v[48:51], v[208:211], v[152:155]\n" \
"ds_read_b64_tr_b16 v[22:23], v232 offset:10752\n" \
"v_mfma_f32_16x16x32_bf16 v[152:155], v[52:55], v[212:215], v[152:155]\n" \
"ds_read_b64_tr_b16 v[24:25], v233 offset:512\n" \
"v_mfma_f32_16x16x32_bf16 v[156:159], v[56:59], v[208:211], v[156:159]\n" \
"ds_read_b128 a[32:35], v254\n" \
"ds_read_b64_tr_b16 v[26:27], v233 offset:2560\n" \
"v_mfma_f32_16x16x32_bf16 v[156:159], v[60:63], v[212:215], v[156:159]\n" \
"buffer_store_dwordx4 a[8:11], v249, s[56:59], s90 offen offset:2048 sc1\n" \
"ds_read_b128 a[36:39], v254\n" \
"v_mfma_f32_16x16x32_bf16 v[176:179], v[32:35], v[216:219], v[176:179]\n" \
"ds_read_b128 v[208:211], v230 offset:4096\n" \
"v_mfma_f32_16x16x32_bf16 v[176:179], v[36:39], v[220:223], v[176:179]\n" \
"ds_read_b128 v[212:215], v231 offset:4096\n" \
"v_mfma_f32_16x16x32_bf16 v[180:183], v[40:43], v[216:219], v[180:183]\n" \
"ds_read_b128 a[8:11], v254\n" \
"ds_read_b64_tr_b16 v[28:29], v233 offset:8704\n" \
"v_mfma_f32_16x16x32_bf16 v[180:183], v[44:47], v[220:223], v[180:183]\n" \
"buffer_store_dwordx4 a[40:43], v249, s[56:59], s90 offen offset:2304 sc1\n" \
"ds_read_b128 a[12:15], v254\n" \
"v_mfma_f32_16x16x32_bf16 v[184:187], v[48:51], v[216:219], v[184:187]\n" \
"ds_read_b64_tr_b16 v[30:31], v233 offset:10752\n" \
"v_mfma_f32_16x16x32_bf16 v[184:187], v[52:55], v[220:223], v[184:187]\n" \
"v_mfma_f32_16x16x32_bf16 v[188:191], v[56:59], v[216:219], v[188:191]\n" \
"ds_read_b128 a[40:43], v254\n" \
"v_mfma_f32_16x16x32_bf16 v[188:191], v[60:63], v[220:223], v[188:191]\n" \
"s_waitcnt lgkmcnt(3)\n" \
"ds_read_b128 a[44:47], v254\n" \
"v_mfma_f32_16x16x32_bf16 v[64:67], v[0:3], v[192:195], v[64:67]\n" \
"ds_read_b128 v[216:219], v230 offset:6144\n" \
"v_mfma_f32_16x16x32_bf16 v[64:67], v[4:7], v[196:199], v[64:67]\n" \
"ds_read_b128 v[220:223], v231 offset:6144\n" \
"v_mfma_f32_16x16x32_bf16 v[68:71], v[8:11], v[192:195], v[68:71]\n" \
"ds_read_b64_tr_b16 v[32:33], v232 offset:1024\n" \
"v_mfma_f32_16x16x32_bf16 v[68:71], v[12:15], v[196:199], v[68:71]\n" \
"v_mfma_f32_16x16x32_bf16 v[72:75], v[16:19], v[192:195], v[72:75]\n" \
"ds_read_b64_tr_b16 v[34:35], v232 offset:3072\n" \
"v_mfma_f32_16x16x32_bf16 v[72:75], v[20:23], v[196:199], v[72:75]\n" \
"s_waitcnt lgkmcnt(3)\n" \
"ds_read_b64_tr_b16 v[36:37], v232 offset:9216\n" \
"v_mfma_f32_16x16x32_bf16 v[76:79], v[24:27], v[192:195], v[76:79]\n" \
"ds_read_b64_tr_b16 v[38:39], v232 offset:11264\n" \
"v_cvt_pk_bf16_f32 v64, v64, v65\n" \
"v_cvt_pk_bf16_f32 v65, v66, v67\n" \
"v_cvt_pk_bf16_f32 v66, v68, v69\n" \
"v_cvt_pk_bf16_f32 v67, v70, v71\n" \
"v_mfma_f32_16x16x32_bf16 v[76:79], v[28:31], v[196:199], v[76:79]\n" \
"v_accvgpr_write_b32 a128, v64\n" \
"v_accvgpr_write_b32 a129, v65\n" \
"v_mfma_f32_16x16x32_bf16 v[96:99], v[0:3], v[200:203], v[96:99]\n" \
"ds_read_b64_tr_b16 v[40:41], v233 offset:1024\n" \
"v_accvgpr_write_b32 a130, v66\n" \
"v_mfma_f32_16x16x32_bf16 v[96:99], v[4:7], v[204:207], v[96:99]\n" \
"ds_read_b64_tr_b16 v[42:43], v233 offset:3072\n" \
"v_accvgpr_write_b32 a131, v67\n" \
"v_mfma_f32_16x16x32_bf16 v[100:103], v[8:11], v[200:203], v[100:103]\n" \
"ds_read_b64_tr_b16 v[44:45], v233 offset:9216\n" \
"v_cvt_pk_bf16_f32 v72, v72, v73\n" \
"v_cvt_pk_bf16_f32 v73, v74, v75\n" \
"v_cvt_pk_bf16_f32 v74, v76, v77\n" \
"v_cvt_pk_bf16_f32 v75, v78, v79\n" \
"v_mfma_f32_16x16x32_bf16 v[100:103], v[12:15], v[204:207], v[100:103]\n" \
"v_accvgpr_write_b32 a132, v72\n" \
"v_accvgpr_write_b32 a133, v73\n" \
"v_mfma_f32_16x16x32_bf16 v[104:107], v[16:19], v[200:203], v[104:107]\n" \
"ds_read_b64_tr_b16 v[46:47], v233 offset:11264\n" \
"v_accvgpr_write_b32 a134, v74\n" \
"v_mfma_f32_16x16x32_bf16 v[104:107], v[20:23], v[204:207], v[104:107]\n" \
"ds_read_b64_tr_b16 v[48:49], v232 offset:1536\n" \
"v_accvgpr_write_b32 a135, v75\n" \
"v_mfma_f32_16x16x32_bf16 v[108:111], v[24:27], v[200:203], v[108:111]\n" \
"ds_read_b64_tr_b16 v[50:51], v232 offset:3584\n" \
"v_cvt_pk_bf16_f32 v96, v96, v97\n" \
"v_cvt_pk_bf16_f32 v97, v98, v99\n" \
"v_cvt_pk_bf16_f32 v98, v100, v101\n" \
"v_cvt_pk_bf16_f32 v99, v102, v103\n" \
"v_mfma_f32_16x16x32_bf16 v[108:111], v[28:31], v[204:207], v[108:111]\n" \
"v_accvgpr_write_b32 a144, v96\n" \
"v_accvgpr_write_b32 a145, v97\n" \
"v_mfma_f32_16x16x32_bf16 v[128:131], v[0:3], v[208:211], v[128:131]\n" \
"ds_read_b64_tr_b16 v[52:53], v232 offset:9728\n" \
"v_accvgpr_write_b32 a146, v98\n" \
"v_mfma_f32_16x16x32_bf16 v[128:131], v[4:7], v[212:215], v[128:131]\n" \
"ds_read_b64_tr_b16 v[54:55], v232 offset:11776\n" \
"v_accvgpr_write_b32 a147, v99\n" \
"v_mfma_f32_16x16x32_bf16 v[132:135], v[8:11], v[208:211], v[132:135]\n" \
"ds_read_b64_tr_b16 v[56:57], v233 offset:1536\n" \
"v_cvt_pk_bf16_f32 v104, v104, v105\n" \
"v_cvt_pk_bf16_f32 v105, v106, v107\n" \
"v_cvt_pk_bf16_f32 v106, v108, v109\n" \
"v_cvt_pk_bf16_f32 v107, v110, v111\n" \
"v_mfma_f32_16x16x32_bf16 v[132:135], v[12:15], v[212:215], v[132:135]\n" \
"v_accvgpr_write_b32 a148, v104\n" \
"v_accvgpr_write_b32 a149, v105\n" \
"v_mfma_f32_16x16x32_bf16 v[136:139], v[16:19], v[208:211], v[136:139]\n" \
"ds_read_b64_tr_b16 v[58:59], v233 offset:3584\n" \
"v_accvgpr_write_b32 a150, v106\n" \
"v_mfma_f32_16x16x32_bf16 v[136:139], v[20:23], v[212:215], v[136:139]\n" \
"ds_read_b64_tr_b16 v[60:61], v233 offset:9728\n" \
"v_accvgpr_write_b32 a151, v107\n" \
"v_mfma_f32_16x16x32_bf16 v[140:143], v[24:27], v[208:211], v[140:143]\n" \
"ds_read_b64_tr_b16 v[62:63], v233 offset:11776\n" \
"v_cvt_pk_bf16_f32 v128, v128, v129\n" \
"v_cvt_pk_bf16_f32 v129, v130, v131\n" \
"v_mfma_f32_16x16x32_bf16 v[140:143], v[28:31], v[212:215], v[140:143]\n" \
"s_waitcnt lgkmcnt(3)\n" \
"v_cvt_pk_bf16_f32 v130, v132, v133\n" \
"v_cvt_pk_bf16_f32 v131, v134, v135\n" \
"v_mfma_f32_16x16x32_bf16 v[160:163], v[0:3], v[216:219], v[160:163]\n" \
"v_accvgpr_write_b32 a160, v128\n" \
"v_accvgpr_write_b32 a161, v129\n" \
"v_mfma_f32_16x16x32_bf16 v[160:163], v[4:7], v[220:223], v[160:163]\n" \
"v_accvgpr_write_b32 a162, v130\n" \
"v_accvgpr_write_b32 a163, v131\n" \
"v_mfma_f32_16x16x32_bf16 v[164:167], v[8:11], v[216:219], v[164:167]\n" \
"v_cvt_pk_bf16_f32 v136, v136, v137\n" \
"v_cvt_pk_bf16_f32 v137, v138, v139\n" \
"v_mfma_f32_16x16x32_bf16 v[164:167], v[12:15], v[220:223], v[164:167]\n" \
"v_cvt_pk_bf16_f32 v138, v140, v141\n" \
"v_cvt_pk_bf16_f32 v139, v142, v143\n" \
"v_mfma_f32_16x16x32_bf16 v[168:171], v[16:19], v[216:219], v[168:171]\n" \
"v_accvgpr_write_b32 a164, v136\n" \
"Lpg4892_%=:\n" \
"s_cbranch_execz Lpg6170_%=\n" \
"v_accvgpr_write_b32 a165, v137\n" \
"v_mfma_f32_16x16x32_bf16 v[168:171], v[20:23], v[220:223], v[168:171]\n" \
"v_accvgpr_write_b32 a166, v138\n" \
"v_accvgpr_write_b32 a167, v139\n" \
"v_mfma_f32_16x16x32_bf16 v[172:175], v[24:27], v[216:219], v[172:175]\n" \
"s_waitcnt vmcnt(10) lgkmcnt(0)\n" \
"v_cvt_pk_bf16_f32 v160, v160, v161\n" \
"v_cvt_pk_bf16_f32 v161, v162, v163\n" \
"v_cvt_pk_bf16_f32 v162, v164, v165\n" \
"v_cvt_pk_bf16_f32 v163, v166, v167\n" \
"v_accvgpr_write_b32 a176, v160\n" \
"v_mfma_f32_16x16x32_bf16 v[172:175], v[28:31], v[220:223], v[172:175]\n" \
"s_barrier\n" \
"v_accvgpr_write_b32 a177, v161\n" \
"v_mfma_f32_16x16x32_bf16 v[80:83], v[32:35], v[192:195], v[80:83]\n" \
"s_mov_b32 m0, s74\n" \
"v_accvgpr_write_b32 a178, v162\n" \
"v_accvgpr_write_b32 a179, v163\n" \
"v_mfma_f32_16x16x32_bf16 v[80:83], v[36:39], v[196:199], v[80:83]\n" \
"ds_read_b128 a[192:195], v249 offset:0\n" \
"global_load_lds_dwordx4 v250, s[38:39] offset:0 sc1\n" \
"v_mfma_f32_16x16x32_bf16 v[84:87], v[40:43], v[192:195], v[84:87]\n" \
"ds_read_b128 v[0:3], v254\n" \
"v_cvt_pk_bf16_f32 v168, v168, v169\n" \
"v_cvt_pk_bf16_f32 v169, v170, v171\n" \
"v_cvt_pk_bf16_f32 v170, v172, v173\n" \
"v_cvt_pk_bf16_f32 v171, v174, v175\n" \
"v_accvgpr_write_b32 a180, v168\n" \
"v_accvgpr_write_b32 a181, v169\n" \
"v_mfma_f32_16x16x32_bf16 v[84:87], v[44:47], v[196:199], v[84:87]\n" \
"ds_read_b128 a[196:199], v249 offset:2048\n" \
"global_load_lds_dwordx4 v250, s[38:39] offset:1024 sc1\n" \
"v_accvgpr_write_b32 a182, v170\n" \
"v_mfma_f32_16x16x32_bf16 v[88:91], v[48:51], v[192:195], v[88:91]\n" \
"ds_read_b128 v[4:7], v254\n" \
"v_accvgpr_write_b32 a183, v171\n" \
"v_mfma_f32_16x16x32_bf16 v[88:91], v[52:55], v[196:199], v[88:91]\n" \
"ds_read_b128 a[224:227], v249 offset:4096\n" \
"global_load_lds_dwordx4 v250, s[38:39] offset:2048 sc1\n" \
"v_mfma_f32_16x16x32_bf16 v[92:95], v[56:59], v[192:195], v[92:95]\n" \
"v_cvt_pk_bf16_f32 v80, v80, v81\n" \
"v_cvt_pk_bf16_f32 v81, v82, v83\n" \
"v_cvt_pk_bf16_f32 v82, v84, v85\n" \
"v_cvt_pk_bf16_f32 v83, v86, v87\n" \
"v_accvgpr_write_b32 a136, v80\n" \
"v_accvgpr_write_b32 a137, v81\n" \
"v_mfma_f32_16x16x32_bf16 v[92:95], v[60:63], v[196:199], v[92:95]\n" \
"ds_read_b128 a[228:231], v249 offset:6144\n" \
"global_load_lds_dwordx4 v250, s[38:39] offset:3072 sc1\n" \
"v_accvgpr_write_b32 a138, v82\n" \
"v_mfma_f32_16x16x32_bf16 v[112:115], v[32:35], v[200:203], v[112:115]\n" \
"s_add_u32 s38, s38, 17408\n" \
"v_accvgpr_write_b32 a139, v83\n" \
"s_add_u32 m0, m0, 0x4000\n" \
"v_mfma_f32_16x16x32_bf16 v[112:115], v[36:39], v[204:207], v[112:115]\n" \
"ds_read_b128 a[200:203], v249 offset:256\n" \
"global_load_lds_dwordx4 v250, s[38:39] offset:0 sc1\n" \
"v_mfma_f32_16x16x32_bf16 v[116:119], v[40:43], v[200:203], v[116:119]\n" \
"v_cvt_pk_bf16_f32 v88, v88, v89\n" \
"v_cvt_pk_bf16_f32 v89, v90, v91\n" \
"v_cvt_pk_bf16_f32 v90, v92, v93\n" \
"v_cvt_pk_bf16_f32 v91, v94, v95\n" \
"v_accvgpr_write_b32 a140, v88\n" \
"v_accvgpr_write_b32 a141, v89\n" \
"v_mfma_f32_16x16x32_bf16 v[116:119], v[44:47], v[204:207], v[116:119]\n" \
"ds_read_b128 a[204:207], v249 offset:2304\n" \
"global_load_lds_dwordx4 v250, s[38:39] offset:1024 sc1\n" \
"v_mfma_f32_16x16x32_bf16 v[120:123], v[48:51], v[200:203], v[120:123]\n" \
"v_accvgpr_write_b32 a142, v90\n" \
"v_accvgpr_write_b32 a143, v91\n" \
"v_mfma_f32_16x16x32_bf16 v[120:123], v[52:55], v[204:207], v[120:123]\n" \
"ds_read_b128 a[232:235], v249 offset:4352\n" \
"global_load_lds_dwordx4 v250, s[38:39] offset:2048 sc1\n" \
"v_mfma_f32_16x16x32_bf16 v[124:127], v[56:59], v[200:203], v[124:127]\n" \
"v_cvt_pk_bf16_f32 v112, v112, v113\n" \
"v_cvt_pk_bf16_f32 v113, v114, v115\n" \
"v_cvt_pk_bf16_f32 v114, v116, v117\n" \
"v_cvt_pk_bf16_f32 v115, v118, v119\n" \
"v_accvgpr_write_b32 a152, v112\n" \
"v_accvgpr_write_b32 a153, v113\n" \
"v_mfma_f32_16x16x32_bf16 v[124:127], v[60:63], v[204:207], v[124:127]\n" \
"ds_read_b128 a[236:239], v249 offset:6400\n" \
"global_load_lds_dwordx4 v250, s[38:39] offset:3072 sc1\n" \
"v_accvgpr_write_b32 a154, v114\n" \
"v_mfma_f32_16x16x32_bf16 v[144:147], v[32:35], v[208:211], v[144:147]\n" \
"s_add_u32 s38, s38, 17408\n" \
"s_xor_b32 s74, s74, 0x8000\n" \
"s_add_u32 s90, s89, 417792\n" \
"v_mfma_f32_16x16x32_bf16 v[144:147], v[36:39], v[212:215], v[144:147]\n" \
"ds_read_b128 a[208:211], v249 offset:8192\n" \
"v_accvgpr_write_b32 a155, v115\n" \
"v_mfma_f32_16x16x32_bf16 v[148:151], v[40:43], v[208:211], v[148:151]\n" \
"v_cvt_pk_bf16_f32 v120, v120, v121\n" \
"v_cvt_pk_bf16_f32 v121, v122, v123\n" \
"v_mfma_f32_16x16x32_bf16 v[148:151], v[44:47], v[212:215], v[148:151]\n" \
"buffer_store_dwordx4 a[16:19], v248, s[56:59], s90 offen offset:0 sc1\n" \
"ds_read_b128 a[212:215], v249 offset:10240\n" \
"v_cvt_pk_bf16_f32 v122, v124, v125\n" \
"v_cvt_pk_bf16_f32 v123, v126, v127\n" \
"v_accvgpr_write_b32 a156, v120\n" \
"v_mfma_f32_16x16x32_bf16 v[152:155], v[48:51], v[208:211], v[152:155]\n" \
"v_accvgpr_write_b32 a157, v121\n" \
"v_accvgpr_write_b32 a158, v122\n" \
"v_mfma_f32_16x16x32_bf16 v[152:155], v[52:55], v[212:215], v[152:155]\n" \
"ds_read_b128 a[240:243], v249 offset:12288\n" \
"v_accvgpr_write_b32 a159, v123\n" \
"v_mfma_f32_16x16x32_bf16 v[156:159], v[56:59], v[208:211], v[156:159]\n" \
"ds_read_b128 a[16:19], v254\n" \
"v_cvt_pk_bf16_f32 v144, v144, v145\n" \
"v_cvt_pk_bf16_f32 v145, v146, v147\n" \
"v_cvt_pk_bf16_f32 v146, v148, v149\n" \
"v_mfma_f32_16x16x32_bf16 v[156:159], v[60:63], v[212:215], v[156:159]\n" \
"ds_read_b128 a[244:247], v249 offset:14336\n" \
"v_cvt_pk_bf16_f32 v147, v150, v151\n" \
"v_mfma_f32_16x16x32_bf16 v[176:179], v[32:35], v[216:219], v[176:179]\n" \
"ds_read_b128 a[216:219], v249 offset:8448\n" \
"buffer_store_dwordx4 a[20:23], v248, s[56:59], s90 offen offset:1024 sc1\n" \
"ds_read_b128 a[48:51], v254\n" \
"v_accvgpr_write_b32 a168, v144\n" \
"v_accvgpr_write_b32 a169, v145\n" \
"v_accvgpr_write_b32 a170, v146\n" \
"v_mfma_f32_16x16x32_bf16 v[176:179], v[36:39], v[220:223], v[176:179]\n" \
"ds_read_b128 a[220:223], v249 offset:10496\n" \
"v_accvgpr_write_b32 a171, v147\n" \
"v_mfma_f32_16x16x32_bf16 v[180:183], v[40:43], v[216:219], v[180:183]\n" \
"ds_read_b128 a[248:251], v249 offset:12544\n" \
"v_cvt_pk_bf16_f32 v152, v152, v153\n" \
"v_cvt_pk_bf16_f32 v153, v154, v155\n" \
"v_mfma_f32_16x16x32_bf16 v[180:183], v[44:47], v[220:223], v[180:183]\n" \
"ds_read_b128 a[252:255], v249 offset:14592\n" \
"v_cvt_pk_bf16_f32 v154, v156, v157\n" \
"v_mfma_f32_16x16x32_bf16 v[184:187], v[48:51], v[216:219], v[184:187]\n" \
"ds_read_b128 a[20:23], v254\n" \
"v_cvt_pk_bf16_f32 v155, v158, v159\n" \
"v_mfma_f32_16x16x32_bf16 v[184:187], v[52:55], v[220:223], v[184:187]\n" \
"ds_read_b128 v[32:35], v254\n" \
"v_accvgpr_write_b32 a172, v152\n" \
"v_mfma_f32_16x16x32_bf16 v[188:191], v[56:59], v[216:219], v[188:191]\n" \
"ds_read_b128 v[36:39], v254\n" \
"v_accvgpr_write_b32 a173, v153\n" \
"v_mfma_f32_16x16x32_bf16 v[188:191], v[60:63], v[220:223], v[188:191]\n" \
"s_waitcnt lgkmcnt(4)\n" \
"buffer_store_dwordx4 a[24:27], v248, s[56:59], s90 offen offset:2048 sc1\n" \
"v_accvgpr_write_b32 a174, v154\n" \
"s_cmp_eq_u32 s63, 0\n" \
"s_cbranch_scc1 Lmask0_%=\n" \
"Lmask0r_%=:\n" \
"v_mfma_f32_16x16x32_bf16 v[0:3], a[192:195], a[128:131], v[0:3]\n" \
"s_lshl_b32 s70, s95, 1\n" \
"ds_read_b128 a[52:55], v254\n" \
"v_accvgpr_write_b32 a175, v155\n" \
"v_mov_b32 v201, 0xf149f2ca\n" \
"v_mfma_f32_16x16x32_bf16 v[0:3], a[196:199], a[132:135], v[0:3]\n" \
"s_add_u32 s70, s70, 131072\n" \
"v_cvt_pk_bf16_f32 v176, v176, v177\n" \
"v_cvt_pk_bf16_f32 v177, v178, v179\n" \
"v_mfma_f32_16x16x32_bf16 v[0:3], a[224:227], a[136:139], v[0:3]\n" \
"ds_read_b128 a[24:27], v254\n" \
"buffer_store_dwordx4 a[28:31], v248, s[56:59], s90 offen offset:3072 sc1\n" \
"ds_read_b128 a[56:59], v254\n" \
"v_cvt_pk_bf16_f32 v178, v180, v181\n" \
"v_cvt_pk_bf16_f32 v179, v182, v183\n" \
"v_accvgpr_write_b32 a184, v176\n" \
"v_mfma_f32_16x16x32_bf16 v[0:3], a[228:231], a[140:143], v[0:3]\n" \
"v_accvgpr_write_b32 a185, v177\n" \
"v_accvgpr_write_b32 a186, v178\n" \
"v_mfma_f32_16x16x32_bf16 v[8:11], a[192:195], a[144:147], 0\n" \
"global_load_dword v253, v252, s[40:41]\n" \
"v_mfma_f32_16x16x32_bf16 v[8:11], a[196:199], a[148:151], v[8:11]\n" \
"s_sub_u32 s80, s80, 1\n" \
"ds_read_b128 a[28:31], v254\n" \
"s_cmp_gt_i32 s80, 0\n" \
"v_mfma_f32_16x16x32_bf16 v[8:11], a[224:227], a[152:155], v[8:11]\n" \
"s_cselect_b32 s81, 0x80000, 0\n" \
"ds_read_b128 a[60:63], v254\n" \
"v_max3_f32 v192, v0, v1, v2\n" \
"v_max3_f32 v192, v192, v3, v201\n" \
"s_add_u32 s40, s40, s81\n" \
"v_mfma_f32_16x16x32_bf16 v[8:11], a[228:231], a[156:159], v[8:11]\n" \
"s_addc_u32 s41, s41, 0\n" \
"v_accvgpr_write_b32 a187, v179\n" \
"s_add_u32 m0, s95, 49152\n" \
"v_mfma_f32_16x16x32_bf16 v[16:19], a[192:195], a[160:163], 0\n" \
"global_load_lds_dwordx4 v250, s[36:37] offset:0 sc1\n" \
"v_cvt_pk_bf16_f32 v184, v184, v185\n" \
"v_mfma_f32_16x16x32_bf16 v[16:19], a[196:199], a[164:167], v[16:19]\n" \
"v_cvt_pk_bf16_f32 v185, v186, v187\n" \
"v_cvt_pk_bf16_f32 v186, v188, v189\n" \
"v_mfma_f32_16x16x32_bf16 v[16:19], a[224:227], a[168:171], v[16:19]\n" \
"v_max3_f32 v193, v8, v9, v10\n" \
"v_cvt_pk_bf16_f32 v187, v190, v191\n" \
"v_accvgpr_write_b32 a188, v184\n" \
"v_mfma_f32_16x16x32_bf16 v[16:19], a[228:231], a[172:175], v[16:19]\n" \
"v_accvgpr_write_b32 a189, v185\n" \
"v_accvgpr_write_b32 a190, v186\n" \
"v_mfma_f32_16x16x32_bf16 v[24:27], a[192:195], a[176:179], 0\n" \
"global_load_lds_dwordx4 v250, s[36:37] offset:1024 sc1\n" \
"v_accvgpr_write_b32 a191, v187\n" \
"v_mfma_f32_16x16x32_bf16 v[24:27], a[196:199], a[180:183], v[24:27]\n" \
"v_add_u32 v200, s70, v248\n" \
"v_max3_f32 v193, v193, v11, v201\n" \
"v_mfma_f32_16x16x32_bf16 v[24:27], a[224:227], a[184:187], v[24:27]\n" \
"ds_write_b128 v200, a[160:163] offset:0\n" \
"v_mfma_f32_16x16x32_bf16 v[24:27], a[228:231], a[188:191], v[24:27]\n" \
"s_waitcnt lgkmcnt(1)\n" \
"ds_read_b128 v[128:131], v254\n" \
"v_max3_f32 v194, v16, v17, v18\n" \
"v_mfma_f32_16x16x32_bf16 v[4:7], a[200:203], a[128:131], v[4:7]\n" \
"ds_read_b128 v[132:135], v254\n" \
"global_load_lds_dwordx4 v250, s[36:37] offset:2048 sc1\n" \
"v_mfma_f32_16x16x32_bf16 v[4:7], a[204:207], a[132:135], v[4:7]\n" \
"ds_read_b128 a[192:195], v249 offset:16384\n" \
"v_max3_f32 v194, v194, v19, v201\n" \
"v_mfma_f32_16x16x32_bf16 v[4:7], a[232:235], a[136:139], v[4:7]\n" \
"ds_write_b128 v200, a[164:167] offset:1024\n" \
"v_max3_f32 v195, v24, v25, v26\n" \
"v_max3_f32 v195, v195, v27, v201\n" \
"v_mov_b32 v196, v192\n" \
"v_mov_b32 v197, v193\n" \
"v_mov_b32 v198, v194\n" \
"v_mov_b32 v199, v195\n" \
"v_permlane16_swap_b32 v196, v192\n" \
"v_permlane16_swap_b32 v197, v193\n" \
"v_mfma_f32_16x16x32_bf16 v[4:7], a[236:239], a[140:143], v[4:7]\n" \
"ds_read_b128 a[196:199], v249 offset:18432\n" \
"global_load_lds_dwordx4 v250, s[36:37] offset:3072 sc1\n" \
"v_permlane16_swap_b32 v198, v194\n" \
"v_permlane16_swap_b32 v199, v195\n" \
"v_max_f32 v192, v192, v196\n" \
"v_max_f32 v193, v193, v197\n" \
"v_max_f32 v194, v194, v198\n" \
"v_max_f32 v195, v195, v199\n" \
"v_mov_b32 v196, v192\n" \
"v_mov_b32 v197, v193\n" \
"v_mfma_f32_16x16x32_bf16 v[12:15], a[200:203], a[144:147], 0\n" \
"s_add_u32 s36, s36, 17408\n" \
"s_mov_b32 s42, s60\n" \
"ds_read_b128 v[136:139], v254\n" \
"v_mov_b32 v198, v194\n" \
"v_mov_b32 v199, v195\n" \
"v_permlane32_swap_b32 v196, v192\n" \
"v_permlane32_swap_b32 v197, v193\n" \
"v_permlane32_swap_b32 v198, v194\n" \
"v_permlane32_swap_b32 v199, v195\n" \
"v_max_f32 v192, v192, v196\n" \
"v_max_f32 v193, v193, v197\n" \
"v_mfma_f32_16x16x32_bf16 v[12:15], a[204:207], a[148:151], v[12:15]\n" \
"s_mov_b32 s43, s60\n" \
"s_mov_b64 s[8:9], 0\n" \
"ds_read_b128 a[224:227], v249 offset:20480\n" \
"v_max_f32 v194, v194, v198\n" \
"v_max_f32 v195, v195, v199\n" \
"v_sub_f32 v224, 0, v192\n" \
"v_sub_f32 v225, 0, v192\n" \
"v_sub_f32 v226, 0, v192\n" \
"v_sub_f32 v227, 0, v192\n" \
"v_add_f32 v32, v32, v224\n" \
"v_add_f32 v33, v33, v224\n" \
"v_mfma_f32_16x16x32_bf16 v[12:15], a[232:235], a[152:155], v[12:15]\n" \
"s_mov_b64 s[10:11], -1\n" \
"s_lshl_b32 s71, s63, 2\n" \
"s_add_u32 s71, s71, s79\n" \
"v_add_f32 v34, v34, v224\n" \
"v_add_f32 v35, v35, v224\n" \
"v_sub_f32 v228, 0, v193\n" \
"v_sub_f32 v229, 0, v193\n" \
"v_sub_f32 v230, 0, v193\n" \
"v_sub_f32 v231, 0, v193\n" \
"v_sub_f32 v232, 0, v194\n" \
"v_sub_f32 v233, 0, v194\n" \
"v_mfma_f32_16x16x32_bf16 v[12:15], a[236:239], a[156:159], v[12:15]\n" \
"ds_read_b128 a[228:231], v249 offset:22528\n" \
"v_sub_f32 v234, 0, v194\n" \
"v_sub_f32 v235, 0, v194\n" \
"v_sub_f32 v236, 0, v195\n" \
"v_sub_f32 v237, 0, v195\n" \
"v_sub_f32 v238, 0, v195\n" \
"v_sub_f32 v239, 0, v195\n" \
"v_add_f32 v36, v36, v224\n" \
"v_add_f32 v37, v37, v224\n" \
"v_mfma_f32_16x16x32_bf16 v[20:23], a[200:203], a[160:163], 0\n" \
"ds_write_b128 v200, a[168:171] offset:2048\n" \
"v_add_f32 v38, v38, v224\n" \
"v_add_f32 v39, v39, v224\n" \
"v_add_f32 v0, v0, v224\n" \
"v_add_f32 v1, v1, v224\n" \
"v_add_f32 v2, v2, v224\n" \
"v_add_f32 v3, v3, v224\n" \
"v_add_f32 v4, v4, v224\n" \
"v_add_f32 v5, v5, v224\n" \
"v_mfma_f32_16x16x32_bf16 v[20:23], a[204:207], a[164:167], v[20:23]\n" \
"ds_read_b128 v[140:143], v254\n" \
"v_add_f32 v6, v6, v224\n" \
"v_add_f32 v7, v7, v224\n" \
"v_exp_f32 v0, v0\n" \
"v_exp_f32 v1, v1\n" \
"v_exp_f32 v2, v2\n" \
"v_add_f32 v240, v0, v1\n" \
"v_exp_f32 v3, v3\n" \
"v_add_f32 v240, v240, v2\n" \
"v_mfma_f32_16x16x32_bf16 v[20:23], a[232:235], a[168:171], v[20:23]\n" \
"ds_read_b128 v[144:147], v254\n" \
"v_cvt_pk_bf16_f32 v0, v0, v1\n" \
"v_exp_f32 v4, v4\n" \
"v_add_f32 v240, v240, v3\n" \
"v_exp_f32 v5, v5\n" \
"v_add_f32 v240, v240, v4\n" \
"v_cvt_pk_bf16_f32 v1, v2, v3\n" \
"v_exp_f32 v6, v6\n" \
"v_add_f32 v240, v240, v5\n" \
"v_mfma_f32_16x16x32_bf16 v[20:23], a[236:239], a[172:175], v[20:23]\n" \
"ds_write_b128 v200, a[172:175] offset:3072\n" \
"v_exp_f32 v7, v7\n" \
"v_add_f32 v240, v240, v6\n" \
"v_cvt_pk_bf16_f32 v2, v4, v5\n" \
"v_add_f32 v240, v240, v7\n" \
"v_cvt_pk_bf16_f32 v3, v6, v7\n" \
"v_add_f32 v8, v8, v228\n" \
"v_add_f32 v9, v9, v228\n" \
"v_add_f32 v10, v10, v228\n" \
"v_mfma_f32_16x16x32_bf16 v[28:31], a[200:203], a[176:179], 0\n" \
"ds_read_b128 v[148:151], v254\n" \
"v_add_f32 v11, v11, v228\n" \
"v_add_f32 v12, v12, v228\n" \
"v_add_f32 v13, v13, v228\n" \
"v_add_f32 v14, v14, v228\n" \
"v_add_f32 v15, v15, v228\n" \
"v_exp_f32 v8, v8\n" \
"v_exp_f32 v9, v9\n" \
"v_exp_f32 v10, v10\n" \
"v_mfma_f32_16x16x32_bf16 v[28:31], a[204:207], a[180:183], v[28:31]\n" \
"ds_read_b128 v[152:155], v254\n" \
"v_add_f32 v241, v8, v9\n" \
"v_exp_f32 v11, v11\n" \
"v_add_f32 v241, v241, v10\n" \
"v_cvt_pk_bf16_f32 v8, v8, v9\n" \
"v_exp_f32 v12, v12\n" \
"v_add_f32 v241, v241, v11\n" \
"v_exp_f32 v13, v13\n" \
"v_add_f32 v241, v241, v12\n" \
"v_mfma_f32_16x16x32_bf16 v[28:31], a[232:235], a[184:187], v[28:31]\n" \
"ds_write_b128 v200, a[176:179] offset:4096\n" \
"v_cvt_pk_bf16_f32 v9, v10, v11\n" \
"v_exp_f32 v14, v14\n" \
"v_add_f32 v241, v241, v13\n" \
"v_exp_f32 v15, v15\n" \
"v_add_f32 v241, v241, v14\n" \
"v_cvt_pk_bf16_f32 v10, v12, v13\n" \
"v_add_f32 v241, v241, v15\n" \
"v_cvt_pk_bf16_f32 v11, v14, v15\n" \
"v_mfma_f32_16x16x32_bf16 v[28:31], a[236:239], a[188:191], v[28:31]\n" \
"ds_read_b128 v[156:159], v254\n" \
"v_add_f32 v16, v16, v232\n" \
"v_add_f32 v17, v17, v232\n" \
"v_add_f32 v18, v18, v232\n" \
"v_add_f32 v19, v19, v232\n" \
"v_add_f32 v20, v20, v232\n" \
"v_add_f32 v21, v21, v232\n" \
"v_add_f32 v22, v22, v232\n" \
"v_add_f32 v23, v23, v232\n" \
"v_mfma_f32_16x16x32_bf16 v[32:35], a[208:211], a[128:131], v[32:35]\n" \
"ds_read_b128 v[160:163], v254\n" \
"v_exp_f32 v16, v16\n" \
"v_exp_f32 v17, v17\n" \
"v_exp_f32 v18, v18\n" \
"v_add_f32 v242, v16, v17\n" \
"v_exp_f32 v19, v19\n" \
"v_add_f32 v242, v242, v18\n" \
"v_cvt_pk_bf16_f32 v16, v16, v17\n" \
"v_exp_f32 v20, v20\n" \
"v_mfma_f32_16x16x32_bf16 v[32:35], a[212:215], a[132:135], v[32:35]\n" \
"ds_read_b128 a[200:203], v249 offset:16640\n" \
"v_add_f32 v242, v242, v19\n" \
"v_exp_f32 v21, v21\n" \
"v_add_f32 v242, v242, v20\n" \
"v_cvt_pk_bf16_f32 v17, v18, v19\n" \
"v_mfma_f32_16x16x32_bf16 v[32:35], a[240:243], a[136:139], v[32:35]\n" \
"ds_write_b128 v200, a[180:183] offset:5120\n" \
"v_mfma_f32_16x16x32_bf16 v[32:35], a[244:247], a[140:143], v[32:35]\n" \
"ds_read_b128 a[204:207], v249 offset:18688\n" \
"v_exp_f32 v22, v22\n" \
"v_mfma_f32_16x16x32_bf16 v[40:43], a[208:211], a[144:147], v[228:231]\n" \
"ds_read_b128 v[164:167], v254\n" \
"v_add_f32 v242, v242, v21\n" \
"v_mfma_f32_16x16x32_bf16 v[40:43], a[212:215], a[148:151], v[40:43]\n" \
"ds_read_b128 a[232:235], v249 offset:20736\n" \
"v_exp_f32 v23, v23\n" \
"v_mfma_f32_16x16x32_bf16 v[40:43], a[240:243], a[152:155], v[40:43]\n" \
"ds_write_b128 v200, a[184:187] offset:6144\n" \
"v_mfma_f32_16x16x32_bf16 v[40:43], a[244:247], a[156:159], v[40:43]\n" \
"ds_read_b128 a[236:239], v249 offset:22784\n" \
"v_add_f32 v242, v242, v22\n" \
"v_mfma_f32_16x16x32_bf16 v[48:51], a[208:211], a[160:163], v[232:235]\n" \
"ds_read_b128 v[168:171], v254\n" \
"v_cvt_pk_bf16_f32 v18, v20, v21\n" \
"v_mfma_f32_16x16x32_bf16 v[48:51], a[212:215], a[164:167], v[48:51]\n" \
"ds_write_b128 v200, a[188:191] offset:7168\n" \
"v_mfma_f32_16x16x32_bf16 v[48:51], a[240:243], a[168:171], v[48:51]\n" \
"ds_read_b128 v[172:175], v254\n" \
"v_add_f32 v242, v242, v23\n" \
"v_mfma_f32_16x16x32_bf16 v[48:51], a[244:247], a[172:175], v[48:51]\n" \
"ds_read_b128 v[176:179], v254\n" \
"v_cvt_pk_bf16_f32 v19, v22, v23\n" \
"v_mfma_f32_16x16x32_bf16 v[56:59], a[208:211], a[176:179], v[236:239]\n" \
"ds_read_b128 v[180:183], v254\n" \
"v_add_f32 v24, v24, v236\n" \
"v_mfma_f32_16x16x32_bf16 v[56:59], a[212:215], a[180:183], v[56:59]\n" \
"ds_read_b128 v[184:187], v254\n" \
"v_add_f32 v25, v25, v236\n" \
"v_mfma_f32_16x16x32_bf16 v[56:59], a[240:243], a[184:187], v[56:59]\n" \
"ds_read_b128 v[188:191], v254\n" \
"v_add_f32 v26, v26, v236\n" \
"v_mfma_f32_16x16x32_bf16 v[56:59], a[244:247], a[188:191], v[56:59]\n" \
"v_add_f32 v27, v27, v236\n" \
"v_add_f32 v28, v28, v236\n" \
"v_mfma_f32_16x16x32_bf16 v[36:39], a[216:219], a[128:131], v[36:39]\n" \
"v_add_f32 v29, v29, v236\n" \
"v_add_f32 v30, v30, v236\n" \
"v_mfma_f32_16x16x32_bf16 v[36:39], a[220:223], a[132:135], v[36:39]\n" \
"ds_read_b128 a[208:211], v249 offset:24576\n" \
"v_add_f32 v31, v31, v236\n" \
"v_mfma_f32_16x16x32_bf16 v[36:39], a[248:251], a[136:139], v[36:39]\n" \
"v_exp_f32 v24, v24\n" \
"v_mfma_f32_16x16x32_bf16 v[36:39], a[252:255], a[140:143], v[36:39]\n" \
"ds_read_b128 a[212:215], v249 offset:26624\n" \
"v_exp_f32 v25, v25\n" \
"v_mfma_f32_16x16x32_bf16 v[44:47], a[216:219], a[144:147], v[228:231]\n" \
"v_exp_f32 v26, v26\n" \
"v_mfma_f32_16x16x32_bf16 v[44:47], a[220:223], a[148:151], v[44:47]\n" \
"ds_read_b128 a[240:243], v249 offset:28672\n" \
"v_add_f32 v243, v24, v25\n" \
"v_mfma_f32_16x16x32_bf16 v[44:47], a[248:251], a[152:155], v[44:47]\n" \
"v_exp_f32 v27, v27\n" \
"v_add_f32 v243, v243, v26\n" \
"v_mfma_f32_16x16x32_bf16 v[44:47], a[252:255], a[156:159], v[44:47]\n" \
"ds_read_b128 a[244:247], v249 offset:30720\n" \
"v_cvt_pk_bf16_f32 v24, v24, v25\n" \
"v_mfma_f32_16x16x32_bf16 v[52:55], a[216:219], a[160:163], v[232:235]\n" \
"v_exp_f32 v28, v28\n" \
"v_add_f32 v243, v243, v27\n" \
"v_mfma_f32_16x16x32_bf16 v[52:55], a[220:223], a[164:167], v[52:55]\n" \
"v_exp_f32 v29, v29\n" \
"v_mfma_f32_16x16x32_bf16 v[52:55], a[248:251], a[168:171], v[52:55]\n" \
"v_add_f32 v243, v243, v28\n" \
"v_cvt_pk_bf16_f32 v25, v26, v27\n" \
"v_mfma_f32_16x16x32_bf16 v[52:55], a[252:255], a[172:175], v[52:55]\n" \
"v_exp_f32 v30, v30\n" \
"v_add_f32 v243, v243, v29\n" \
"v_mfma_f32_16x16x32_bf16 v[60:63], a[216:219], a[176:179], v[236:239]\n" \
"v_exp_f32 v31, v31\n" \
"v_add_f32 v243, v243, v30\n" \
"v_mfma_f32_16x16x32_bf16 v[60:63], a[220:223], a[180:183], v[60:63]\n" \
"v_cvt_pk_bf16_f32 v26, v28, v29\n" \
"v_add_f32 v243, v243, v31\n" \
"v_mfma_f32_16x16x32_bf16 v[60:63], a[248:251], a[184:187], v[60:63]\n" \
"v_mov_b32 v202, s60\n" \
"v_lshlrev_b32 v203, 28, s61\n" \
"v_mfma_f32_16x16x32_bf16 v[60:63], a[252:255], a[188:191], v[60:63]\n" \
"v_mov_b32 v246, v249\n" \
"v_mov_b32 v254, 0xff800000\n" \
"ds_read_b128 a[216:219], v249 offset:24832\n" \
"ds_read_b128 a[220:223], v249 offset:26880\n" \
"ds_read_b128 a[248:251], v249 offset:28928\n" \
"s_cmp_eq_u32 s63, 0\n" \
"s_waitcnt vmcnt(5) lgkmcnt(1)\n" \
"s_barrier\n" \
"buffer_store_dword v202, v203, s[56:59], s71 offen sc1\n" \
"s_cbranch_scc1 LA1l_%=\n" \
"s_branch LA1_%=\n" \
"Lpg5362_%=:\n" \
"s_cbranch_execz Lpg6287_%=\n" \
"Lloop_%=:\n" \
"v_mfma_f32_16x16x32_bf16 a[0:3], a[192:195], v[64:67], a[0:3]\n" \
"v_exp_f32 v0, v0\n" \
"v_mfma_f32_16x16x32_bf16 a[0:3], a[196:199], v[96:99], a[0:3]\n" \
"v_cvt_pk_bf16_f32 v123, v126, v127\n" \
"v_add_f32 v240, v240, v0\n" \
"v_exp_f32 v1, v1\n" \
"v_mfma_f32_16x16x32_bf16 a[32:35], a[192:195], v[72:75], a[32:35]\n" \
"s_add_u32 m0, s73, 0x4000\n" \
"v_add_f32 v240, v240, v1\n" \
"v_exp_f32 v2, v2\n" \
"v_mfma_f32_16x16x32_bf16 a[32:35], a[196:199], v[104:107], a[32:35]\n" \
"ds_read_b128 a[252:255], v247 offset:31744\n" \
"global_load_lds_dwordx4 v250, s[36:37] offset:0 sc1\n" \
"v_add_f32 v240, v240, v2\n" \
"v_cvt_pk_bf16_f32 v0, v0, v1\n" \
"v_exp_f32 v3, v3\n" \
"v_mfma_f32_16x16x32_bf16 v[128:131], a[192:195], v[80:83], v[128:131]\n" \
"v_xor_b32 v247, 0x8000, v247\n" \
"v_add_f32 v240, v240, v3\n" \
"v_exp_f32 v4, v4\n" \
"v_mfma_f32_16x16x32_bf16 v[128:131], a[196:199], v[112:115], v[128:131]\n" \
"v_add_f32 v240, v240, v4\n" \
"v_cvt_pk_bf16_f32 v1, v2, v3\n" \
"v_mfma_f32_16x16x32_bf16 v[160:163], a[192:195], v[88:91], v[160:163]\n" \
"v_exp_f32 v5, v5\n" \
"v_mfma_f32_16x16x32_bf16 v[160:163], a[196:199], v[120:123], v[160:163]\n" \
"ds_read_b128 a[192:195], v246 offset:16384\n" \
"v_add_f32 v240, v240, v5\n" \
"v_mfma_f32_16x16x32_bf16 a[4:7], a[200:203], v[64:67], a[4:7]\n" \
"v_exp_f32 v6, v6\n" \
"v_mfma_f32_16x16x32_bf16 a[4:7], a[204:207], v[96:99], a[4:7]\n" \
"v_add_f32 v240, v240, v6\n" \
"v_cvt_pk_bf16_f32 v2, v4, v5\n" \
"v_mfma_f32_16x16x32_bf16 a[36:39], a[200:203], v[72:75], a[36:39]\n" \
"v_exp_f32 v7, v7\n" \
"v_mfma_f32_16x16x32_bf16 a[36:39], a[204:207], v[104:107], a[36:39]\n" \
"ds_read_b128 a[196:199], v246 offset:18432\n" \
"v_add_f32 v240, v240, v7\n" \
"v_mfma_f32_16x16x32_bf16 v[132:135], a[200:203], v[80:83], v[132:135]\n" \
"v_exp_f32 v8, v8\n" \
"v_mfma_f32_16x16x32_bf16 v[132:135], a[204:207], v[112:115], v[132:135]\n" \
"v_add_f32 v241, v241, v8\n" \
"v_cvt_pk_bf16_f32 v3, v6, v7\n" \
"v_mfma_f32_16x16x32_bf16 v[164:167], a[200:203], v[88:91], v[164:167]\n" \
"v_exp_f32 v9, v9\n" \
"v_mfma_f32_16x16x32_bf16 v[164:167], a[204:207], v[120:123], v[164:167]\n" \
"ds_read_b128 a[200:203], v246 offset:16640\n" \
"global_load_lds_dwordx4 v250, s[36:37] offset:1024 sc1\n" \
"v_add_f32 v241, v241, v9\n" \
"v_mfma_f32_16x16x32_bf16 a[8:11], a[208:211], v[64:67], a[8:11]\n" \
"v_exp_f32 v10, v10\n" \
"v_mfma_f32_16x16x32_bf16 a[8:11], a[212:215], v[96:99], a[8:11]\n" \
"v_add_f32 v241, v241, v10\n" \
"v_cvt_pk_bf16_f32 v8, v8, v9\n" \
"v_mfma_f32_16x16x32_bf16 a[40:43], a[208:211], v[72:75], a[40:43]\n" \
"v_exp_f32 v11, v11\n" \
"v_mfma_f32_16x16x32_bf16 a[40:43], a[212:215], v[104:107], a[40:43]\n" \
"ds_read_b128 a[204:207], v246 offset:18688\n" \
"v_add_f32 v241, v241, v11\n" \
"v_mfma_f32_16x16x32_bf16 v[136:139], a[208:211], v[80:83], v[136:139]\n" \
"v_exp_f32 v12, v12\n" \
"v_mfma_f32_16x16x32_bf16 v[136:139], a[212:215], v[112:115], v[136:139]\n" \
"v_add_f32 v241, v241, v12\n" \
"v_cvt_pk_bf16_f32 v9, v10, v11\n" \
"v_mfma_f32_16x16x32_bf16 v[168:171], a[208:211], v[88:91], v[168:171]\n" \
"v_exp_f32 v13, v13\n" \
"v_mfma_f32_16x16x32_bf16 v[168:171], a[212:215], v[120:123], v[168:171]\n" \
"ds_read_b128 a[208:211], v246 offset:24576\n" \
"v_add_f32 v241, v241, v13\n" \
"v_mfma_f32_16x16x32_bf16 a[12:15], a[216:219], v[64:67], a[12:15]\n" \
"v_exp_f32 v14, v14\n" \
"v_mfma_f32_16x16x32_bf16 a[12:15], a[220:223], v[96:99], a[12:15]\n" \
"v_add_f32 v241, v241, v14\n" \
"v_cvt_pk_bf16_f32 v10, v12, v13\n" \
"v_mfma_f32_16x16x32_bf16 a[44:47], a[216:219], v[72:75], a[44:47]\n" \
"v_exp_f32 v15, v15\n" \
"v_mfma_f32_16x16x32_bf16 a[44:47], a[220:223], v[104:107], a[44:47]\n" \
"ds_read_b128 a[212:215], v246 offset:26624\n" \
"global_load_lds_dwordx4 v250, s[36:37] offset:2048 sc1\n" \
"v_add_f32 v241, v241, v15\n" \
"v_mfma_f32_16x16x32_bf16 v[140:143], a[216:219], v[80:83], v[140:143]\n" \
"v_exp_f32 v16, v16\n" \
"v_mfma_f32_16x16x32_bf16 v[140:143], a[220:223], v[112:115], v[140:143]\n" \
"v_add_f32 v242, v242, v16\n" \
"v_cvt_pk_bf16_f32 v11, v14, v15\n" \
"v_mfma_f32_16x16x32_bf16 v[172:175], a[216:219], v[88:91], v[172:175]\n" \
"s_waitcnt lgkmcnt(6)\n" \
"v_exp_f32 v17, v17\n" \
"v_mfma_f32_16x16x32_bf16 v[172:175], a[220:223], v[120:123], v[172:175]\n" \
"ds_read_b128 a[216:219], v246 offset:24832\n" \
"v_add_f32 v242, v242, v17\n" \
"v_mfma_f32_16x16x32_bf16 a[16:19], a[224:227], v[64:67], a[16:19]\n" \
"v_exp_f32 v18, v18\n" \
"v_mfma_f32_16x16x32_bf16 a[16:19], a[228:231], v[96:99], a[16:19]\n" \
"v_add_f32 v242, v242, v18\n" \
"v_cvt_pk_bf16_f32 v16, v16, v17\n" \
"v_mfma_f32_16x16x32_bf16 a[48:51], a[224:227], v[72:75], a[48:51]\n" \
"v_exp_f32 v19, v19\n" \
"v_mfma_f32_16x16x32_bf16 a[48:51], a[228:231], v[104:107], a[48:51]\n" \
"ds_read_b128 a[220:223], v246 offset:26880\n" \
"v_add_f32 v242, v242, v19\n" \
"v_exp_f32 v20, v20\n" \
"v_mfma_f32_16x16x32_bf16 v[144:147], a[224:227], v[80:83], v[144:147]\n" \
"v_add_f32 v242, v242, v20\n" \
"v_cvt_pk_bf16_f32 v17, v18, v19\n" \
"v_mfma_f32_16x16x32_bf16 v[144:147], a[228:231], v[112:115], v[144:147]\n" \
"v_exp_f32 v21, v21\n" \
"v_mfma_f32_16x16x32_bf16 v[176:179], a[224:227], v[88:91], v[176:179]\n" \
"global_load_lds_dwordx4 v250, s[36:37] offset:3072 sc1\n" \
"s_add_u32 s36, s36, 17408\n" \
"v_mfma_f32_16x16x32_bf16 v[176:179], a[228:231], v[120:123], v[176:179]\n" \
"s_xor_b32 s73, s73, 0x8000\n" \
"ds_read_b128 a[224:227], v246 offset:20480\n" \
"s_xor_b64 s[52:53], s[44:45], s[42:43]\n" \
"v_mfma_f32_16x16x32_bf16 a[20:23], a[232:235], v[64:67], a[20:23]\n" \
"s_xor_b64 s[54:55], s[46:47], s[42:43]\n" \
"s_or_b64 s[52:53], s[52:53], s[54:55]\n" \
"s_xor_b64 s[54:55], s[48:49], s[42:43]\n" \
"v_mfma_f32_16x16x32_bf16 a[20:23], a[236:239], v[96:99], a[20:23]\n" \
"s_or_b64 s[52:53], s[52:53], s[54:55]\n" \
"s_xor_b64 s[54:55], s[50:51], s[42:43]\n" \
"s_or_b64 s[52:53], s[52:53], s[54:55]\n" \
"v_mfma_f32_16x16x32_bf16 a[52:55], a[232:235], v[72:75], a[52:55]\n" \
"s_and_b64 s[52:53], s[52:53], s[8:9]\n" \
"s_cbranch_scc1 Lrs0_%=\n" \
"Lrr0_%=:\n" \
"v_mfma_f32_16x16x32_bf16 a[52:55], a[236:239], v[104:107], a[52:55]\n" \
"s_mov_b64 s[8:9], s[10:11]\n" \
"ds_read_b128 a[228:231], v246 offset:22528\n" \
"v_add_f32 v242, v242, v21\n" \
"v_mfma_f32_16x16x32_bf16 v[148:151], a[232:235], v[80:83], v[148:151]\n" \
"v_exp_f32 v22, v22\n" \
"v_mfma_f32_16x16x32_bf16 v[148:151], a[236:239], v[112:115], v[148:151]\n" \
"v_add_f32 v242, v242, v22\n" \
"v_cvt_pk_bf16_f32 v18, v20, v21\n" \
"v_mfma_f32_16x16x32_bf16 v[180:183], a[232:235], v[88:91], v[180:183]\n" \
"v_exp_f32 v23, v23\n" \
"v_mfma_f32_16x16x32_bf16 v[180:183], a[236:239], v[120:123], v[180:183]\n" \
"ds_read_b128 a[232:235], v246 offset:20736\n" \
"v_add_f32 v242, v242, v23\n" \
"v_mfma_f32_16x16x32_bf16 a[24:27], a[240:243], v[64:67], a[24:27]\n" \
"v_exp_f32 v24, v24\n" \
"v_mfma_f32_16x16x32_bf16 a[24:27], a[244:247], v[96:99], a[24:27]\n" \
"v_add_f32 v243, v243, v24\n" \
"v_cvt_pk_bf16_f32 v19, v22, v23\n" \
"v_mfma_f32_16x16x32_bf16 a[56:59], a[240:243], v[72:75], a[56:59]\n" \
"v_exp_f32 v25, v25\n" \
"v_mfma_f32_16x16x32_bf16 a[56:59], a[244:247], v[104:107], a[56:59]\n" \
"ds_read_b128 a[236:239], v246 offset:22784\n" \
"v_add_f32 v243, v243, v25\n" \
"v_mfma_f32_16x16x32_bf16 v[152:155], a[240:243], v[80:83], v[152:155]\n" \
"v_exp_f32 v26, v26\n" \
"v_mfma_f32_16x16x32_bf16 v[152:155], a[244:247], v[112:115], v[152:155]\n" \
"v_add_f32 v243, v243, v26\n" \
"v_cvt_pk_bf16_f32 v24, v24, v25\n" \
"v_mfma_f32_16x16x32_bf16 v[184:187], a[240:243], v[88:91], v[184:187]\n" \
"v_exp_f32 v27, v27\n" \
"v_mfma_f32_16x16x32_bf16 v[184:187], a[244:247], v[120:123], v[184:187]\n" \
"ds_read_b128 a[240:243], v246 offset:28672\n" \
"v_add_f32 v243, v243, v27\n" \
"Lpg5524_%=:\n" \
"s_cbranch_execz Lpg6811_%=\n" \
"v_mfma_f32_16x16x32_bf16 a[28:31], a[248:251], v[64:67], a[28:31]\n" \
"v_exp_f32 v28, v28\n" \
"v_mfma_f32_16x16x32_bf16 a[28:31], a[252:255], v[96:99], a[28:31]\n" \
"v_add_f32 v243, v243, v28\n" \
"v_cvt_pk_bf16_f32 v25, v26, v27\n" \
"v_mfma_f32_16x16x32_bf16 a[60:63], a[248:251], v[72:75], a[60:63]\n" \
"v_exp_f32 v29, v29\n" \
"v_mfma_f32_16x16x32_bf16 a[60:63], a[252:255], v[104:107], a[60:63]\n" \
"ds_read_b128 a[244:247], v246 offset:30720\n" \
"v_add_f32 v243, v243, v29\n" \
"v_mfma_f32_16x16x32_bf16 v[156:159], a[248:251], v[80:83], v[156:159]\n" \
"v_exp_f32 v30, v30\n" \
"v_mfma_f32_16x16x32_bf16 v[156:159], a[252:255], v[112:115], v[156:159]\n" \
"v_add_f32 v243, v243, v30\n" \
"v_cvt_pk_bf16_f32 v26, v28, v29\n" \
"v_mfma_f32_16x16x32_bf16 v[188:191], a[248:251], v[88:91], v[188:191]\n" \
"s_waitcnt lgkmcnt(6)\n" \
"v_exp_f32 v31, v31\n" \
"v_mfma_f32_16x16x32_bf16 v[188:191], a[252:255], v[120:123], v[188:191]\n" \
"ds_read_b128 a[248:251], v246 offset:28928\n" \
"v_add_f32 v243, v243, v31\n" \
"LA1_%=:\n" \
"Lpg5547_%=:\n" \
"s_cbranch_execz Lpg7119_%=\n" \
"v_mfma_f32_16x16x32_bf16 v[64:67], a[192:195], a[128:131], v[224:227]\n" \
"v_exp_f32 v32, v32\n" \
"v_mfma_f32_16x16x32_bf16 v[64:67], a[196:199], a[132:135], v[64:67]\n" \
"v_cvt_pk_bf16_f32 v27, v30, v31\n" \
"v_add_f32 v240, v240, v32\n" \
"v_mfma_f32_16x16x32_bf16 v[72:75], a[192:195], a[144:147], v[228:231]\n" \
"s_mov_b32 m0, s74\n" \
"v_exp_f32 v33, v33\n" \
"v_mfma_f32_16x16x32_bf16 v[72:75], a[196:199], a[148:151], v[72:75]\n" \
"ds_read_b128 a[252:255], v246 offset:30976\n" \
"global_load_lds_dwordx4 v250, s[38:39] offset:0 sc1\n" \
"v_add_f32 v240, v240, v33\n" \
"v_mfma_f32_16x16x32_bf16 v[80:83], a[192:195], a[160:163], v[232:235]\n" \
"v_xor_b32 v246, 0x8000, v246\n" \
"v_exp_f32 v34, v34\n" \
"v_mfma_f32_16x16x32_bf16 v[80:83], a[196:199], a[164:167], v[80:83]\n" \
"v_add_f32 v240, v240, v34\n" \
"v_cvt_pk_bf16_f32 v32, v32, v33\n" \
"v_mfma_f32_16x16x32_bf16 v[88:91], a[192:195], a[176:179], v[236:239]\n" \
"v_exp_f32 v35, v35\n" \
"v_mfma_f32_16x16x32_bf16 v[88:91], a[196:199], a[180:183], v[88:91]\n" \
"ds_read_b128 a[192:195], v247 offset:0\n" \
"v_add_f32 v240, v240, v35\n" \
"v_mfma_f32_16x16x32_bf16 v[68:71], a[200:203], a[128:131], v[224:227]\n" \
"v_exp_f32 v36, v36\n" \
"v_mfma_f32_16x16x32_bf16 v[68:71], a[204:207], a[132:135], v[68:71]\n" \
"v_add_f32 v240, v240, v36\n" \
"v_cvt_pk_bf16_f32 v33, v34, v35\n" \
"v_mfma_f32_16x16x32_bf16 v[76:79], a[200:203], a[144:147], v[228:231]\n" \
"v_exp_f32 v37, v37\n" \
"v_mfma_f32_16x16x32_bf16 v[76:79], a[204:207], a[148:151], v[76:79]\n" \
"ds_read_b128 a[196:199], v247 offset:8192\n" \
"v_add_f32 v240, v240, v37\n" \
"v_mfma_f32_16x16x32_bf16 v[84:87], a[200:203], a[160:163], v[232:235]\n" \
"v_exp_f32 v38, v38\n" \
"v_mfma_f32_16x16x32_bf16 v[84:87], a[204:207], a[164:167], v[84:87]\n" \
"v_add_f32 v240, v240, v38\n" \
"v_cvt_pk_bf16_f32 v34, v36, v37\n" \
"v_mfma_f32_16x16x32_bf16 v[92:95], a[200:203], a[176:179], v[236:239]\n" \
"v_exp_f32 v39, v39\n" \
"v_mfma_f32_16x16x32_bf16 v[92:95], a[204:207], a[180:183], v[92:95]\n" \
"ds_read_b128 a[200:203], v247 offset:1024\n" \
"global_load_lds_dwordx4 v250, s[38:39] offset:1024 sc1\n" \
"v_add_f32 v240, v240, v39\n" \
"v_mfma_f32_16x16x32_bf16 v[96:99], a[208:211], a[128:131], v[224:227]\n" \
"v_exp_f32 v40, v40\n" \
"v_mfma_f32_16x16x32_bf16 v[96:99], a[212:215], a[132:135], v[96:99]\n" \
"v_add_f32 v241, v241, v40\n" \
"v_cvt_pk_bf16_f32 v35, v38, v39\n" \
"v_mfma_f32_16x16x32_bf16 v[104:107], a[208:211], a[144:147], v[228:231]\n" \
"v_exp_f32 v41, v41\n" \
"v_mfma_f32_16x16x32_bf16 v[104:107], a[212:215], a[148:151], v[104:107]\n" \
"ds_read_b128 a[204:207], v247 offset:9216\n" \
"v_add_f32 v241, v241, v41\n" \
"v_mfma_f32_16x16x32_bf16 v[112:115], a[208:211], a[160:163], v[232:235]\n" \
"v_exp_f32 v42, v42\n" \
"v_mfma_f32_16x16x32_bf16 v[112:115], a[212:215], a[164:167], v[112:115]\n" \
"v_add_f32 v241, v241, v42\n" \
"v_cvt_pk_bf16_f32 v40, v40, v41\n" \
"v_mfma_f32_16x16x32_bf16 v[120:123], a[208:211], a[176:179], v[236:239]\n" \
"v_exp_f32 v43, v43\n" \
"v_mfma_f32_16x16x32_bf16 v[120:123], a[212:215], a[180:183], v[120:123]\n" \
"ds_read_b128 a[208:211], v247 offset:2048\n" \
"v_add_f32 v241, v241, v43\n" \
"v_mfma_f32_16x16x32_bf16 v[100:103], a[216:219], a[128:131], v[224:227]\n" \
"v_exp_f32 v44, v44\n" \
"v_mfma_f32_16x16x32_bf16 v[100:103], a[220:223], a[132:135], v[100:103]\n" \
"v_add_f32 v241, v241, v44\n" \
"v_cvt_pk_bf16_f32 v41, v42, v43\n" \
"v_mfma_f32_16x16x32_bf16 v[108:111], a[216:219], a[144:147], v[228:231]\n" \
"v_exp_f32 v45, v45\n" \
"v_mfma_f32_16x16x32_bf16 v[108:111], a[220:223], a[148:151], v[108:111]\n" \
"ds_read_b128 a[212:215], v247 offset:10240\n" \
"global_load_lds_dwordx4 v250, s[38:39] offset:2048 sc1\n" \
"v_add_f32 v241, v241, v45\n" \
"v_mfma_f32_16x16x32_bf16 v[116:119], a[216:219], a[160:163], v[232:235]\n" \
"v_exp_f32 v46, v46\n" \
"v_mfma_f32_16x16x32_bf16 v[116:119], a[220:223], a[164:167], v[116:119]\n" \
"v_add_f32 v241, v241, v46\n" \
"v_cvt_pk_bf16_f32 v42, v44, v45\n" \
"v_mfma_f32_16x16x32_bf16 v[124:127], a[216:219], a[176:179], v[236:239]\n" \
"s_waitcnt lgkmcnt(6)\n" \
"v_exp_f32 v47, v47\n" \
"v_mfma_f32_16x16x32_bf16 v[124:127], a[220:223], a[180:183], v[124:127]\n" \
"ds_read_b128 a[216:219], v247 offset:3072\n" \
"v_add_f32 v241, v241, v47\n" \
"v_mfma_f32_16x16x32_bf16 v[64:67], a[224:227], a[136:139], v[64:67]\n" \
"v_exp_f32 v48, v48\n" \
"v_mfma_f32_16x16x32_bf16 v[64:67], a[228:231], a[140:143], v[64:67]\n" \
"v_add_f32 v242, v242, v48\n" \
"v_cvt_pk_bf16_f32 v43, v46, v47\n" \
"v_mfma_f32_16x16x32_bf16 v[72:75], a[224:227], a[152:155], v[72:75]\n" \
"v_exp_f32 v49, v49\n" \
"v_mfma_f32_16x16x32_bf16 v[72:75], a[228:231], a[156:159], v[72:75]\n" \
"ds_read_b128 a[220:223], v247 offset:11264\n" \
"v_add_f32 v242, v242, v49\n" \
"v_exp_f32 v50, v50\n" \
"v_mfma_f32_16x16x32_bf16 v[80:83], a[224:227], a[168:171], v[80:83]\n" \
"v_add_f32 v242, v242, v50\n" \
"v_cvt_pk_bf16_f32 v48, v48, v49\n" \
"v_mfma_f32_16x16x32_bf16 v[80:83], a[228:231], a[172:175], v[80:83]\n" \
"v_exp_f32 v51, v51\n" \
"v_mfma_f32_16x16x32_bf16 v[88:91], a[224:227], a[184:187], v[88:91]\n" \
"v_add_f32 v242, v242, v51\n" \
"v_exp_f32 v52, v52\n" \
"v_mfma_f32_16x16x32_bf16 v[88:91], a[228:231], a[188:191], v[88:91]\n" \
"ds_read_b128 a[224:227], v247 offset:4096\n" \
"global_load_lds_dwordx4 v250, s[38:39] offset:3072 sc1\n" \
"v_mfma_f32_16x16x32_bf16 v[68:71], a[232:235], a[136:139], v[68:71]\n" \
"s_add_u32 s38, s38, 17408\n" \
"v_add_f32 v242, v242, v52\n" \
"v_cvt_pk_bf16_f32 v49, v50, v51\n" \
"v_mfma_f32_16x16x32_bf16 v[68:71], a[236:239], a[140:143], v[68:71]\n" \
"v_exp_f32 v53, v53\n" \
"v_mfma_f32_16x16x32_bf16 v[76:79], a[232:235], a[152:155], v[76:79]\n" \
"v_add_f32 v242, v242, v53\n" \
"v_exp_f32 v54, v54\n" \
"v_mfma_f32_16x16x32_bf16 v[76:79], a[236:239], a[156:159], v[76:79]\n" \
"ds_read_b128 a[228:231], v247 offset:12288\n" \
"v_add_f32 v242, v242, v54\n" \
"v_cvt_pk_bf16_f32 v50, v52, v53\n" \
"v_mfma_f32_16x16x32_bf16 v[84:87], a[232:235], a[168:171], v[84:87]\n" \
"v_exp_f32 v55, v55\n" \
"v_mfma_f32_16x16x32_bf16 v[84:87], a[236:239], a[172:175], v[84:87]\n" \
"global_load_dword v253, v252, s[40:41]\n" \
"v_add_f32 v242, v242, v55\n" \
"v_mfma_f32_16x16x32_bf16 v[92:95], a[232:235], a[184:187], v[92:95]\n" \
"s_sub_u32 s80, s80, 1\n" \
"s_cmp_gt_i32 s80, 0\n" \
"s_cselect_b32 s81, 0x80000, 0\n" \
"v_mfma_f32_16x16x32_bf16 v[92:95], a[236:239], a[188:191], v[92:95]\n" \
"s_add_u32 s40, s40, s81\n" \
"ds_read_b128 a[232:235], v247 offset:5120\n" \
"s_addc_u32 s41, s41, 0\n" \
"v_mfma_f32_16x16x32_bf16 v[96:99], a[240:243], a[136:139], v[96:99]\n" \
"v_exp_f32 v56, v56\n" \
"v_mfma_f32_16x16x32_bf16 v[96:99], a[244:247], a[140:143], v[96:99]\n" \
"v_add_f32 v243, v243, v56\n" \
"v_cvt_pk_bf16_f32 v51, v54, v55\n" \
"v_mfma_f32_16x16x32_bf16 v[104:107], a[240:243], a[152:155], v[104:107]\n" \
"v_exp_f32 v57, v57\n" \
"v_mfma_f32_16x16x32_bf16 v[104:107], a[244:247], a[156:159], v[104:107]\n" \
"ds_read_b128 a[236:239], v247 offset:13312\n" \
"v_add_f32 v243, v243, v57\n" \
"v_mfma_f32_16x16x32_bf16 v[112:115], a[240:243], a[168:171], v[112:115]\n" \
"v_exp_f32 v58, v58\n" \
"v_mfma_f32_16x16x32_bf16 v[112:115], a[244:247], a[172:175], v[112:115]\n" \
"v_add_f32 v243, v243, v58\n" \
"v_cvt_pk_bf16_f32 v56, v56, v57\n" \
"v_mfma_f32_16x16x32_bf16 v[120:123], a[240:243], a[184:187], v[120:123]\n" \
"v_exp_f32 v59, v59\n" \
"v_mfma_f32_16x16x32_bf16 v[120:123], a[244:247], a[188:191], v[120:123]\n" \
"ds_read_b128 a[240:243], v247 offset:6144\n" \
"v_add_f32 v243, v243, v59\n" \
"v_mfma_f32_16x16x32_bf16 v[100:103], a[248:251], a[136:139], v[100:103]\n" \
"v_exp_f32 v60, v60\n" \
"v_mfma_f32_16x16x32_bf16 v[100:103], a[252:255], a[140:143], v[100:103]\n" \
"v_add_f32 v243, v243, v60\n" \
"v_cvt_pk_bf16_f32 v57, v58, v59\n" \
"v_mfma_f32_16x16x32_bf16 v[108:111], a[248:251], a[152:155], v[108:111]\n" \
"v_exp_f32 v61, v61\n" \
"v_mfma_f32_16x16x32_bf16 v[108:111], a[252:255], a[156:159], v[108:111]\n" \
"ds_read_b128 a[244:247], v247 offset:14336\n" \
"v_add_f32 v243, v243, v61\n" \
"v_mfma_f32_16x16x32_bf16 v[116:119], a[248:251], a[168:171], v[116:119]\n" \
"v_exp_f32 v62, v62\n" \
"v_mfma_f32_16x16x32_bf16 v[116:119], a[252:255], a[172:175], v[116:119]\n" \
"v_add_f32 v243, v243, v62\n" \
"v_cvt_pk_bf16_f32 v58, v60, v61\n" \
"v_mfma_f32_16x16x32_bf16 v[124:127], a[248:251], a[184:187], v[124:127]\n" \
"s_waitcnt vmcnt(10) lgkmcnt(6)\n" \
"v_exp_f32 v63, v63\n" \
"v_mfma_f32_16x16x32_bf16 v[124:127], a[252:255], a[188:191], v[124:127]\n" \
"s_barrier\n" \
"ds_read_b128 a[248:251], v247 offset:7168\n" \
"v_add_f32 v243, v243, v63\n" \
"v_mfma_f32_16x16x32_bf16 a[0:3], a[192:195], v[0:3], a[0:3]\n" \
"v_exp_f32 v64, v64\n" \
"v_mfma_f32_16x16x32_bf16 a[0:3], a[196:199], v[32:35], a[0:3]\n" \
"v_cvt_pk_bf16_f32 v59, v62, v63\n" \
"v_add_f32 v240, v240, v64\n" \
"v_mfma_f32_16x16x32_bf16 a[32:35], a[192:195], v[8:11], a[32:35]\n" \
"s_mov_b32 m0, s73\n" \
"v_exp_f32 v65, v65\n" \
"v_mfma_f32_16x16x32_bf16 a[32:35], a[196:199], v[40:43], a[32:35]\n" \
"ds_read_b128 a[252:255], v247 offset:15360\n" \
"global_load_lds_dwordx4 v250, s[36:37] offset:0 sc1\n" \
"v_add_f32 v240, v240, v65\n" \
"v_mfma_f32_16x16x32_bf16 v[128:131], a[192:195], v[16:19], v[128:131]\n" \
"v_exp_f32 v66, v66\n" \
"v_mfma_f32_16x16x32_bf16 v[128:131], a[196:199], v[48:51], v[128:131]\n" \
"v_add_f32 v240, v240, v66\n" \
"v_cvt_pk_bf16_f32 v64, v64, v65\n" \
"v_mfma_f32_16x16x32_bf16 v[160:163], a[192:195], v[24:27], v[160:163]\n" \
"v_exp_f32 v67, v67\n" \
"v_mfma_f32_16x16x32_bf16 v[160:163], a[196:199], v[56:59], v[160:163]\n" \
"ds_read_b128 a[192:195], v246 offset:0\n" \
"v_add_f32 v240, v240, v67\n" \
"v_mfma_f32_16x16x32_bf16 a[4:7], a[200:203], v[0:3], a[4:7]\n" \
"v_exp_f32 v68, v68\n" \
"v_mfma_f32_16x16x32_bf16 a[4:7], a[204:207], v[32:35], a[4:7]\n" \
"v_add_f32 v240, v240, v68\n" \
"v_cvt_pk_bf16_f32 v65, v66, v67\n" \
"v_mfma_f32_16x16x32_bf16 a[36:39], a[200:203], v[8:11], a[36:39]\n" \
"v_exp_f32 v69, v69\n" \
"v_mfma_f32_16x16x32_bf16 a[36:39], a[204:207], v[40:43], a[36:39]\n" \
"ds_read_b128 a[196:199], v246 offset:2048\n" \
"v_add_f32 v240, v240, v69\n" \
"v_mfma_f32_16x16x32_bf16 v[132:135], a[200:203], v[16:19], v[132:135]\n" \
"v_exp_f32 v70, v70\n" \
"v_mfma_f32_16x16x32_bf16 v[132:135], a[204:207], v[48:51], v[132:135]\n" \
"v_add_f32 v240, v240, v70\n" \
"v_cvt_pk_bf16_f32 v66, v68, v69\n" \
"v_mfma_f32_16x16x32_bf16 v[164:167], a[200:203], v[24:27], v[164:167]\n" \
"v_exp_f32 v71, v71\n" \
"v_mfma_f32_16x16x32_bf16 v[164:167], a[204:207], v[56:59], v[164:167]\n" \
"ds_read_b128 a[200:203], v246 offset:256\n" \
"global_load_lds_dwordx4 v250, s[36:37] offset:1024 sc1\n" \
"v_add_f32 v240, v240, v71\n" \
"v_mfma_f32_16x16x32_bf16 a[8:11], a[208:211], v[0:3], a[8:11]\n" \
"v_exp_f32 v72, v72\n" \
"v_mfma_f32_16x16x32_bf16 a[8:11], a[212:215], v[32:35], a[8:11]\n" \
"v_add_f32 v241, v241, v72\n" \
"v_cvt_pk_bf16_f32 v67, v70, v71\n" \
"v_mfma_f32_16x16x32_bf16 a[40:43], a[208:211], v[8:11], a[40:43]\n" \
"v_exp_f32 v73, v73\n" \
"v_mfma_f32_16x16x32_bf16 a[40:43], a[212:215], v[40:43], a[40:43]\n" \
"ds_read_b128 a[204:207], v246 offset:2304\n" \
"v_add_f32 v241, v241, v73\n" \
"v_mfma_f32_16x16x32_bf16 v[136:139], a[208:211], v[16:19], v[136:139]\n" \
"v_exp_f32 v74, v74\n" \
"v_mfma_f32_16x16x32_bf16 v[136:139], a[212:215], v[48:51], v[136:139]\n" \
"v_add_f32 v241, v241, v74\n" \
"v_cvt_pk_bf16_f32 v72, v72, v73\n" \
"v_mfma_f32_16x16x32_bf16 v[168:171], a[208:211], v[24:27], v[168:171]\n" \
"v_exp_f32 v75, v75\n" \
"v_mfma_f32_16x16x32_bf16 v[168:171], a[212:215], v[56:59], v[168:171]\n" \
"ds_read_b128 a[208:211], v246 offset:8192\n" \
"v_add_f32 v241, v241, v75\n" \
"v_mfma_f32_16x16x32_bf16 a[12:15], a[216:219], v[0:3], a[12:15]\n" \
"v_exp_f32 v76, v76\n" \
"v_mfma_f32_16x16x32_bf16 a[12:15], a[220:223], v[32:35], a[12:15]\n" \
"v_add_f32 v241, v241, v76\n" \
"v_cvt_pk_bf16_f32 v73, v74, v75\n" \
"v_mfma_f32_16x16x32_bf16 a[44:47], a[216:219], v[8:11], a[44:47]\n" \
"v_exp_f32 v77, v77\n" \
"v_mfma_f32_16x16x32_bf16 a[44:47], a[220:223], v[40:43], a[44:47]\n" \
"ds_read_b128 a[212:215], v246 offset:10240\n" \
"global_load_lds_dwordx4 v250, s[36:37] offset:2048 sc1\n" \
"v_add_f32 v241, v241, v77\n" \
"v_mfma_f32_16x16x32_bf16 v[140:143], a[216:219], v[16:19], v[140:143]\n" \
"v_exp_f32 v78, v78\n" \
"v_mfma_f32_16x16x32_bf16 v[140:143], a[220:223], v[48:51], v[140:143]\n" \
"v_add_f32 v241, v241, v78\n" \
"v_cvt_pk_bf16_f32 v74, v76, v77\n" \
"v_mfma_f32_16x16x32_bf16 v[172:175], a[216:219], v[24:27], v[172:175]\n" \
"s_waitcnt lgkmcnt(6)\n" \
"v_exp_f32 v79, v79\n" \
"v_mfma_f32_16x16x32_bf16 v[172:175], a[220:223], v[56:59], v[172:175]\n" \
"ds_read_b128 a[216:219], v246 offset:8448\n" \
"v_add_f32 v241, v241, v79\n" \
"v_mfma_f32_16x16x32_bf16 a[16:19], a[224:227], v[0:3], a[16:19]\n" \
"v_exp_f32 v80, v80\n" \
"v_mfma_f32_16x16x32_bf16 a[16:19], a[228:231], v[32:35], a[16:19]\n" \
"v_add_f32 v242, v242, v80\n" \
"v_cvt_pk_bf16_f32 v75, v78, v79\n" \
"v_mfma_f32_16x16x32_bf16 a[48:51], a[224:227], v[8:11], a[48:51]\n" \
"v_exp_f32 v81, v81\n" \
"v_mfma_f32_16x16x32_bf16 a[48:51], a[228:231], v[40:43], a[48:51]\n" \
"ds_read_b128 a[220:223], v246 offset:10496\n" \
"v_add_f32 v242, v242, v81\n" \
"v_mfma_f32_16x16x32_bf16 v[144:147], a[224:227], v[16:19], v[144:147]\n" \
"v_exp_f32 v82, v82\n" \
"v_mfma_f32_16x16x32_bf16 v[144:147], a[228:231], v[48:51], v[144:147]\n" \
"v_add_f32 v242, v242, v82\n" \
"v_cvt_pk_bf16_f32 v80, v80, v81\n" \
"v_mfma_f32_16x16x32_bf16 v[176:179], a[224:227], v[24:27], v[176:179]\n" \
"v_exp_f32 v83, v83\n" \
"v_mfma_f32_16x16x32_bf16 v[176:179], a[228:231], v[56:59], v[176:179]\n" \
"ds_read_b128 a[224:227], v246 offset:4096\n" \
"global_load_lds_dwordx4 v250, s[36:37] offset:3072 sc1\n" \
"v_add_f32 v242, v242, v83\n" \
"v_mfma_f32_16x16x32_bf16 a[20:23], a[232:235], v[0:3], a[20:23]\n" \
"s_add_u32 s36, s36, 17408\n" \
"v_exp_f32 v84, v84\n" \
"v_mfma_f32_16x16x32_bf16 a[20:23], a[236:239], v[32:35], a[20:23]\n" \
"v_add_f32 v242, v242, v84\n" \
"v_cvt_pk_bf16_f32 v81, v82, v83\n" \
"v_mfma_f32_16x16x32_bf16 a[52:55], a[232:235], v[8:11], a[52:55]\n" \
"v_exp_f32 v85, v85\n" \
"v_mfma_f32_16x16x32_bf16 a[52:55], a[236:239], v[40:43], a[52:55]\n" \
"ds_read_b128 a[228:231], v246 offset:6144\n" \
"v_add_f32 v242, v242, v85\n" \
"v_mfma_f32_16x16x32_bf16 v[148:151], a[232:235], v[16:19], v[148:151]\n" \
"v_exp_f32 v86, v86\n" \
"v_mfma_f32_16x16x32_bf16 v[148:151], a[236:239], v[48:51], v[148:151]\n" \
"v_add_f32 v242, v242, v86\n" \
"v_cvt_pk_bf16_f32 v82, v84, v85\n" \
"v_mfma_f32_16x16x32_bf16 v[180:183], a[232:235], v[24:27], v[180:183]\n" \
"v_exp_f32 v87, v87\n" \
"v_mfma_f32_16x16x32_bf16 v[180:183], a[236:239], v[56:59], v[180:183]\n" \
"ds_read_b128 a[232:235], v246 offset:4352\n" \
"v_add_f32 v242, v242, v87\n" \
"v_mfma_f32_16x16x32_bf16 a[24:27], a[240:243], v[0:3], a[24:27]\n" \
"v_exp_f32 v88, v88\n" \
"v_mfma_f32_16x16x32_bf16 a[24:27], a[244:247], v[32:35], a[24:27]\n" \
"v_add_f32 v243, v243, v88\n" \
"v_cvt_pk_bf16_f32 v83, v86, v87\n" \
"v_mfma_f32_16x16x32_bf16 a[56:59], a[240:243], v[8:11], a[56:59]\n" \
"v_exp_f32 v89, v89\n" \
"v_mfma_f32_16x16x32_bf16 a[56:59], a[244:247], v[40:43], a[56:59]\n" \
"ds_read_b128 a[236:239], v246 offset:6400\n" \
"v_add_f32 v243, v243, v89\n" \
"v_mfma_f32_16x16x32_bf16 v[152:155], a[240:243], v[16:19], v[152:155]\n" \
"v_exp_f32 v90, v90\n" \
"v_mfma_f32_16x16x32_bf16 v[152:155], a[244:247], v[48:51], v[152:155]\n" \
"v_add_f32 v243, v243, v90\n" \
"v_cvt_pk_bf16_f32 v88, v88, v89\n" \
"v_mfma_f32_16x16x32_bf16 v[184:187], a[240:243], v[24:27], v[184:187]\n" \
"v_exp_f32 v91, v91\n" \
"v_mfma_f32_16x16x32_bf16 v[184:187], a[244:247], v[56:59], v[184:187]\n" \
"ds_read_b128 a[240:243], v246 offset:12288\n" \
"v_add_f32 v243, v243, v91\n" \
"v_mfma_f32_16x16x32_bf16 a[28:31], a[248:251], v[0:3], a[28:31]\n" \
"v_exp_f32 v92, v92\n" \
"v_mfma_f32_16x16x32_bf16 a[28:31], a[252:255], v[32:35], a[28:31]\n" \
"v_add_f32 v243, v243, v92\n" \
"v_cvt_pk_bf16_f32 v89, v90, v91\n" \
"v_mfma_f32_16x16x32_bf16 a[60:63], a[248:251], v[8:11], a[60:63]\n" \
"v_exp_f32 v93, v93\n" \
"v_mfma_f32_16x16x32_bf16 a[60:63], a[252:255], v[40:43], a[60:63]\n" \
"ds_read_b128 a[244:247], v246 offset:14336\n" \
"v_add_f32 v243, v243, v93\n" \
"v_mfma_f32_16x16x32_bf16 v[156:159], a[248:251], v[16:19], v[156:159]\n" \
"v_exp_f32 v94, v94\n" \
"v_mfma_f32_16x16x32_bf16 v[156:159], a[252:255], v[48:51], v[156:159]\n" \
"v_add_f32 v243, v243, v94\n" \
"v_cvt_pk_bf16_f32 v90, v92, v93\n" \
"v_mfma_f32_16x16x32_bf16 v[188:191], a[248:251], v[24:27], v[188:191]\n" \
"s_waitcnt lgkmcnt(6)\n" \
"v_exp_f32 v95, v95\n" \
"v_mfma_f32_16x16x32_bf16 v[188:191], a[252:255], v[56:59], v[188:191]\n" \
"ds_read_b128 a[248:251], v246 offset:12544\n" \
"v_add_f32 v243, v243, v95\n" \
"v_mfma_f32_16x16x32_bf16 v[0:3], a[192:195], a[128:131], v[224:227]\n" \
"v_exp_f32 v96, v96\n" \
"v_mfma_f32_16x16x32_bf16 v[0:3], a[196:199], a[132:135], v[0:3]\n" \
"v_cvt_pk_bf16_f32 v91, v94, v95\n" \
"v_add_f32 v240, v240, v96\n" \
"v_exp_f32 v97, v97\n" \
"v_mfma_f32_16x16x32_bf16 v[8:11], a[192:195], a[144:147], v[228:231]\n" \
"s_add_u32 m0, s74, 0x4000\n" \
"v_add_f32 v240, v240, v97\n" \
"v_exp_f32 v98, v98\n" \
"v_mfma_f32_16x16x32_bf16 v[8:11], a[196:199], a[148:151], v[8:11]\n" \
"ds_read_b128 a[252:255], v246 offset:14592\n" \
"global_load_lds_dwordx4 v250, s[38:39] offset:0 sc1\n" \
"v_add_f32 v240, v240, v98\n" \
"v_cvt_pk_bf16_f32 v96, v96, v97\n" \
"v_exp_f32 v99, v99\n" \
"v_mfma_f32_16x16x32_bf16 v[16:19], a[192:195], a[160:163], v[232:235]\n" \
"v_add_f32 v240, v240, v99\n" \
"v_exp_f32 v100, v100\n" \
"v_mfma_f32_16x16x32_bf16 v[16:19], a[196:199], a[164:167], v[16:19]\n" \
"v_add_f32 v240, v240, v100\n" \
"v_cvt_pk_bf16_f32 v97, v98, v99\n" \
"v_mfma_f32_16x16x32_bf16 v[24:27], a[192:195], a[176:179], v[236:239]\n" \
"v_exp_f32 v101, v101\n" \
"v_mfma_f32_16x16x32_bf16 v[24:27], a[196:199], a[180:183], v[24:27]\n" \
"ds_read_b128 a[192:195], v247 offset:16384\n" \
"v_add_f32 v240, v240, v101\n" \
"v_mfma_f32_16x16x32_bf16 v[4:7], a[200:203], a[128:131], v[224:227]\n" \
"v_exp_f32 v102, v102\n" \
"v_mfma_f32_16x16x32_bf16 v[4:7], a[204:207], a[132:135], v[4:7]\n" \
"v_add_f32 v240, v240, v102\n" \
"v_cvt_pk_bf16_f32 v98, v100, v101\n" \
"v_mfma_f32_16x16x32_bf16 v[12:15], a[200:203], a[144:147], v[228:231]\n" \
"v_exp_f32 v103, v103\n" \
"v_mfma_f32_16x16x32_bf16 v[12:15], a[204:207], a[148:151], v[12:15]\n" \
"ds_read_b128 a[196:199], v247 offset:24576\n" \
"v_add_f32 v240, v240, v103\n" \
"v_mfma_f32_16x16x32_bf16 v[20:23], a[200:203], a[160:163], v[232:235]\n" \
"v_exp_f32 v104, v104\n" \
"v_mfma_f32_16x16x32_bf16 v[20:23], a[204:207], a[164:167], v[20:23]\n" \
"v_add_f32 v241, v241, v104\n" \
"v_cvt_pk_bf16_f32 v99, v102, v103\n" \
"v_mfma_f32_16x16x32_bf16 v[28:31], a[200:203], a[176:179], v[236:239]\n" \
"v_exp_f32 v105, v105\n" \
"v_mfma_f32_16x16x32_bf16 v[28:31], a[204:207], a[180:183], v[28:31]\n" \
"ds_read_b128 a[200:203], v247 offset:17408\n" \
"global_load_lds_dwordx4 v250, s[38:39] offset:1024 sc1\n" \
"v_add_f32 v241, v241, v105\n" \
"v_mfma_f32_16x16x32_bf16 v[32:35], a[208:211], a[128:131], v[224:227]\n" \
"v_exp_f32 v106, v106\n" \
"v_mfma_f32_16x16x32_bf16 v[32:35], a[212:215], a[132:135], v[32:35]\n" \
"v_add_f32 v241, v241, v106\n" \
"v_cvt_pk_bf16_f32 v104, v104, v105\n" \
"v_mfma_f32_16x16x32_bf16 v[40:43], a[208:211], a[144:147], v[228:231]\n" \
"v_exp_f32 v107, v107\n" \
"v_mfma_f32_16x16x32_bf16 v[40:43], a[212:215], a[148:151], v[40:43]\n" \
"ds_read_b128 a[204:207], v247 offset:25600\n" \
"v_add_f32 v241, v241, v107\n" \
"v_mfma_f32_16x16x32_bf16 v[48:51], a[208:211], a[160:163], v[232:235]\n" \
"v_exp_f32 v108, v108\n" \
"v_mfma_f32_16x16x32_bf16 v[48:51], a[212:215], a[164:167], v[48:51]\n" \
"v_add_f32 v241, v241, v108\n" \
"v_cvt_pk_bf16_f32 v105, v106, v107\n" \
"v_mfma_f32_16x16x32_bf16 v[56:59], a[208:211], a[176:179], v[236:239]\n" \
"v_exp_f32 v109, v109\n" \
"v_mfma_f32_16x16x32_bf16 v[56:59], a[212:215], a[180:183], v[56:59]\n" \
"ds_read_b128 a[208:211], v247 offset:18432\n" \
"v_add_f32 v241, v241, v109\n" \
"v_mfma_f32_16x16x32_bf16 v[36:39], a[216:219], a[128:131], v[224:227]\n" \
"v_exp_f32 v110, v110\n" \
"v_mfma_f32_16x16x32_bf16 v[36:39], a[220:223], a[132:135], v[36:39]\n" \
"v_add_f32 v241, v241, v110\n" \
"v_cvt_pk_bf16_f32 v106, v108, v109\n" \
"v_mfma_f32_16x16x32_bf16 v[44:47], a[216:219], a[144:147], v[228:231]\n" \
"v_exp_f32 v111, v111\n" \
"v_mfma_f32_16x16x32_bf16 v[44:47], a[220:223], a[148:151], v[44:47]\n" \
"ds_read_b128 a[212:215], v247 offset:26624\n" \
"global_load_lds_dwordx4 v250, s[38:39] offset:2048 sc1\n" \
"v_add_f32 v241, v241, v111\n" \
"v_mfma_f32_16x16x32_bf16 v[52:55], a[216:219], a[160:163], v[232:235]\n" \
"v_exp_f32 v112, v112\n" \
"v_mfma_f32_16x16x32_bf16 v[52:55], a[220:223], a[164:167], v[52:55]\n" \
"v_add_f32 v242, v242, v112\n" \
"v_cvt_pk_bf16_f32 v107, v110, v111\n" \
"v_mfma_f32_16x16x32_bf16 v[60:63], a[216:219], a[176:179], v[236:239]\n" \
"s_waitcnt lgkmcnt(6)\n" \
"v_exp_f32 v113, v113\n" \
"v_mfma_f32_16x16x32_bf16 v[60:63], a[220:223], a[180:183], v[60:63]\n" \
"ds_read_b128 a[216:219], v247 offset:19456\n" \
"v_add_f32 v242, v242, v113\n" \
"v_mfma_f32_16x16x32_bf16 v[0:3], a[224:227], a[136:139], v[0:3]\n" \
"v_exp_f32 v114, v114\n" \
"v_mfma_f32_16x16x32_bf16 v[0:3], a[228:231], a[140:143], v[0:3]\n" \
"v_add_f32 v242, v242, v114\n" \
"v_cvt_pk_bf16_f32 v112, v112, v113\n" \
"v_mfma_f32_16x16x32_bf16 v[8:11], a[224:227], a[152:155], v[8:11]\n" \
"v_exp_f32 v115, v115\n" \
"v_mfma_f32_16x16x32_bf16 v[8:11], a[228:231], a[156:159], v[8:11]\n" \
"ds_read_b128 a[220:223], v247 offset:27648\n" \
"v_add_f32 v242, v242, v115\n" \
"v_mfma_f32_16x16x32_bf16 v[16:19], a[224:227], a[168:171], v[16:19]\n" \
"v_exp_f32 v116, v116\n" \
"v_mfma_f32_16x16x32_bf16 v[16:19], a[228:231], a[172:175], v[16:19]\n" \
"v_add_f32 v242, v242, v116\n" \
"v_cvt_pk_bf16_f32 v113, v114, v115\n" \
"v_mfma_f32_16x16x32_bf16 v[24:27], a[224:227], a[184:187], v[24:27]\n" \
"v_exp_f32 v117, v117\n" \
"v_mfma_f32_16x16x32_bf16 v[24:27], a[228:231], a[188:191], v[24:27]\n" \
"ds_read_b128 a[224:227], v247 offset:20480\n" \
"global_load_lds_dwordx4 v250, s[38:39] offset:3072 sc1\n" \
"v_add_f32 v242, v242, v117\n" \
"v_mfma_f32_16x16x32_bf16 v[4:7], a[232:235], a[136:139], v[4:7]\n" \
"s_add_u32 s38, s38, 17408\n" \
"v_exp_f32 v118, v118\n" \
"v_mfma_f32_16x16x32_bf16 v[4:7], a[236:239], a[140:143], v[4:7]\n" \
"v_add_f32 v242, v242, v118\n" \
"v_cvt_pk_bf16_f32 v114, v116, v117\n" \
"v_mfma_f32_16x16x32_bf16 v[12:15], a[232:235], a[152:155], v[12:15]\n" \
"v_exp_f32 v119, v119\n" \
"v_mfma_f32_16x16x32_bf16 v[12:15], a[236:239], a[156:159], v[12:15]\n" \
"ds_read_b128 a[228:231], v247 offset:28672\n" \
"v_add_f32 v242, v242, v119\n" \
"v_mfma_f32_16x16x32_bf16 v[20:23], a[232:235], a[168:171], v[20:23]\n" \
"global_load_dword v253, v252, s[40:41]\n" \
"v_mfma_f32_16x16x32_bf16 v[20:23], a[236:239], a[172:175], v[20:23]\n" \
"s_sub_u32 s80, s80, 1\n" \
"s_cmp_gt_i32 s80, 0\n" \
"s_cselect_b32 s81, 0x80000, 0\n" \
"v_mfma_f32_16x16x32_bf16 v[28:31], a[232:235], a[184:187], v[28:31]\n" \
"s_add_u32 s40, s40, s81\n" \
"s_addc_u32 s41, s41, 0\n" \
"s_load_dwordx8 s[44:51], s[56:57], s79 glc\n" \
"v_mfma_f32_16x16x32_bf16 v[28:31], a[236:239], a[188:191], v[28:31]\n" \
"s_xor_b32 s74, s74, 0x8000\n" \
"ds_read_b128 a[232:235], v247 offset:21504\n" \
"s_sub_u32 s75, s75, 1\n" \
"v_mfma_f32_16x16x32_bf16 v[32:35], a[240:243], a[136:139], v[32:35]\n" \
"s_cmp_lg_u32 s75, 0\n" \
"v_exp_f32 v120, v120\n" \
"v_mfma_f32_16x16x32_bf16 v[32:35], a[244:247], a[140:143], v[32:35]\n" \
"v_add_f32 v243, v243, v120\n" \
"v_cvt_pk_bf16_f32 v115, v118, v119\n" \
"v_mfma_f32_16x16x32_bf16 v[40:43], a[240:243], a[152:155], v[40:43]\n" \
"v_exp_f32 v121, v121\n" \
"v_mfma_f32_16x16x32_bf16 v[40:43], a[244:247], a[156:159], v[40:43]\n" \
"ds_read_b128 a[236:239], v247 offset:29696\n" \
"v_add_f32 v243, v243, v121\n" \
"v_mfma_f32_16x16x32_bf16 v[48:51], a[240:243], a[168:171], v[48:51]\n" \
"v_exp_f32 v122, v122\n" \
"v_mfma_f32_16x16x32_bf16 v[48:51], a[244:247], a[172:175], v[48:51]\n" \
"v_add_f32 v243, v243, v122\n" \
"v_cvt_pk_bf16_f32 v120, v120, v121\n" \
"v_mfma_f32_16x16x32_bf16 v[56:59], a[240:243], a[184:187], v[56:59]\n" \
"v_exp_f32 v123, v123\n" \
"v_mfma_f32_16x16x32_bf16 v[56:59], a[244:247], a[188:191], v[56:59]\n" \
"ds_read_b128 a[240:243], v247 offset:22528\n" \
"v_add_f32 v243, v243, v123\n" \
"v_mfma_f32_16x16x32_bf16 v[36:39], a[248:251], a[136:139], v[36:39]\n" \
"v_exp_f32 v124, v124\n" \
"v_mfma_f32_16x16x32_bf16 v[36:39], a[252:255], a[140:143], v[36:39]\n" \
"v_add_f32 v243, v243, v124\n" \
"v_cvt_pk_bf16_f32 v121, v122, v123\n" \
"v_mfma_f32_16x16x32_bf16 v[44:47], a[248:251], a[152:155], v[44:47]\n" \
"v_exp_f32 v125, v125\n" \
"v_mfma_f32_16x16x32_bf16 v[44:47], a[252:255], a[156:159], v[44:47]\n" \
"ds_read_b128 a[244:247], v247 offset:30720\n" \
"v_add_f32 v243, v243, v125\n" \
"v_mfma_f32_16x16x32_bf16 v[52:55], a[248:251], a[168:171], v[52:55]\n" \
"v_exp_f32 v126, v126\n" \
"v_mfma_f32_16x16x32_bf16 v[52:55], a[252:255], a[172:175], v[52:55]\n" \
"v_add_f32 v243, v243, v126\n" \
"v_cvt_pk_bf16_f32 v122, v124, v125\n" \
"v_mfma_f32_16x16x32_bf16 v[60:63], a[248:251], a[184:187], v[60:63]\n" \
"s_waitcnt vmcnt(10) lgkmcnt(6)\n" \
"v_exp_f32 v127, v127\n" \
"v_mfma_f32_16x16x32_bf16 v[60:63], a[252:255], a[188:191], v[60:63]\n" \
"s_barrier\n" \
"ds_read_b128 a[248:251], v247 offset:23552\n" \
"v_add_f32 v243, v243, v127\n" \
"s_cbranch_scc1 Lloop_%=\n" \
"v_mfma_f32_16x16x32_bf16 a[0:3], a[192:195], v[64:67], a[0:3]\n" \
"v_exp_f32 v8, v8\n" \
"v_mfma_f32_16x16x32_bf16 a[0:3], a[196:199], v[96:99], a[0:3]\n" \
"v_cvt_pk_bf16_f32 v123, v126, v127\n" \
"v_add_f32 v241, v241, v8\n" \
"v_exp_f32 v9, v9\n" \
"v_mfma_f32_16x16x32_bf16 a[32:35], a[192:195], v[72:75], a[32:35]\n" \
"s_add_u32 m0, s73, 0x4000\n" \
"v_add_f32 v241, v241, v9\n" \
"v_exp_f32 v10, v10\n" \
"v_mfma_f32_16x16x32_bf16 a[32:35], a[196:199], v[104:107], a[32:35]\n" \
"ds_read_b128 a[252:255], v247 offset:31744\n" \
"global_load_lds_dwordx4 v250, s[36:37] offset:0 sc1\n" \
"v_add_f32 v241, v241, v10\n" \
"v_cvt_pk_bf16_f32 v8, v8, v9\n" \
"v_exp_f32 v11, v11\n" \
"v_mfma_f32_16x16x32_bf16 v[128:131], a[192:195], v[80:83], v[128:131]\n" \
"v_xor_b32 v247, 0x8000, v247\n" \
"v_add_f32 v241, v241, v11\n" \
"v_exp_f32 v12, v12\n" \
"v_mfma_f32_16x16x32_bf16 v[128:131], a[196:199], v[112:115], v[128:131]\n" \
"v_add_f32 v241, v241, v12\n" \
"v_cvt_pk_bf16_f32 v9, v10, v11\n" \
"v_exp_f32 v13, v13\n" \
"v_mfma_f32_16x16x32_bf16 v[160:163], a[192:195], v[88:91], v[160:163]\n" \
"v_add_f32 v241, v241, v13\n" \
"v_exp_f32 v14, v14\n" \
"v_mfma_f32_16x16x32_bf16 v[160:163], a[196:199], v[120:123], v[160:163]\n" \
"ds_read_b128 a[192:195], v246 offset:16384\n" \
"v_add_f32 v241, v241, v14\n" \
"v_cvt_pk_bf16_f32 v10, v12, v13\n" \
"v_exp_f32 v15, v15\n" \
"v_mfma_f32_16x16x32_bf16 a[4:7], a[200:203], v[64:67], a[4:7]\n" \
"v_add_f32 v241, v241, v15\n" \
"v_exp_f32 v16, v16\n" \
"v_mfma_f32_16x16x32_bf16 a[4:7], a[204:207], v[96:99], a[4:7]\n" \
"v_add_f32 v242, v242, v16\n" \
"v_cvt_pk_bf16_f32 v11, v14, v15\n" \
"v_exp_f32 v17, v17\n" \
"v_mfma_f32_16x16x32_bf16 a[36:39], a[200:203], v[72:75], a[36:39]\n" \
"v_add_f32 v242, v242, v17\n" \
"v_exp_f32 v18, v18\n" \
"v_mfma_f32_16x16x32_bf16 a[36:39], a[204:207], v[104:107], a[36:39]\n" \
"ds_read_b128 a[196:199], v246 offset:18432\n" \
"v_add_f32 v242, v242, v18\n" \
"v_cvt_pk_bf16_f32 v16, v16, v17\n" \
"v_exp_f32 v19, v19\n" \
"v_mfma_f32_16x16x32_bf16 v[132:135], a[200:203], v[80:83], v[132:135]\n" \
"v_add_f32 v242, v242, v19\n" \
"v_exp_f32 v20, v20\n" \
"v_mfma_f32_16x16x32_bf16 v[132:135], a[204:207], v[112:115], v[132:135]\n" \
"v_add_f32 v242, v242, v20\n" \
"v_cvt_pk_bf16_f32 v17, v18, v19\n" \
"v_exp_f32 v21, v21\n" \
"v_mfma_f32_16x16x32_bf16 v[164:167], a[200:203], v[88:91], v[164:167]\n" \
"v_add_f32 v242, v242, v21\n" \
"v_exp_f32 v22, v22\n" \
"v_mfma_f32_16x16x32_bf16 v[164:167], a[204:207], v[120:123], v[164:167]\n" \
"ds_read_b128 a[200:203], v246 offset:16640\n" \
"global_load_lds_dwordx4 v250, s[36:37] offset:1024 sc1\n" \
"v_add_f32 v242, v242, v22\n" \
"v_cvt_pk_bf16_f32 v18, v20, v21\n" \
"v_exp_f32 v23, v23\n" \
"v_mfma_f32_16x16x32_bf16 a[8:11], a[208:211], v[64:67], a[8:11]\n" \
"v_add_f32 v242, v242, v23\n" \
"v_cvt_pk_bf16_f32 v19, v22, v23\n" \
"v_cmp_gt_i32 vcc, 0, v251\n" \
"v_cmp_gt_i32 s[64:65], 1, v251\n" \
"v_cmp_gt_i32 s[66:67], 2, v251\n" \
"v_cndmask_b32 v0, v0, v254, vcc\n" \
"v_mfma_f32_16x16x32_bf16 a[8:11], a[212:215], v[96:99], a[8:11]\n" \
"v_cmp_gt_i32 vcc, 3, v251\n" \
"v_cndmask_b32 v1, v1, v254, s[64:65]\n" \
"v_cmp_gt_i32 s[64:65], 16, v251\n" \
"v_cndmask_b32 v2, v2, v254, s[66:67]\n" \
"v_cmp_gt_i32 s[66:67], 17, v251\n" \
"v_cndmask_b32 v3, v3, v254, vcc\n" \
"v_mfma_f32_16x16x32_bf16 a[40:43], a[208:211], v[72:75], a[40:43]\n" \
"v_cmp_gt_i32 vcc, 18, v251\n" \
"v_cndmask_b32 v4, v4, v254, s[64:65]\n" \
"v_cmp_gt_i32 s[64:65], 19, v251\n" \
"v_cndmask_b32 v5, v5, v254, s[66:67]\n" \
"v_cmp_gt_i32 s[66:67], 32, v251\n" \
"v_cndmask_b32 v6, v6, v254, vcc\n" \
"v_mfma_f32_16x16x32_bf16 a[40:43], a[212:215], v[104:107], a[40:43]\n" \
"ds_read_b128 a[204:207], v246 offset:18688\n" \
"v_cmp_gt_i32 vcc, 33, v251\n" \
"v_mfma_f32_16x16x32_bf16 v[136:139], a[208:211], v[80:83], v[136:139]\n" \
"v_cndmask_b32 v7, v7, v254, s[64:65]\n" \
"v_cmp_gt_i32 s[64:65], 34, v251\n" \
"v_mfma_f32_16x16x32_bf16 v[136:139], a[212:215], v[112:115], v[136:139]\n" \
"v_cndmask_b32 v32, v32, v254, s[66:67]\n" \
"v_cmp_gt_i32 s[66:67], 35, v251\n" \
"v_mfma_f32_16x16x32_bf16 v[168:171], a[208:211], v[88:91], v[168:171]\n" \
"v_cndmask_b32 v33, v33, v254, vcc\n" \
"v_cmp_gt_i32 vcc, 48, v251\n" \
"v_mfma_f32_16x16x32_bf16 v[168:171], a[212:215], v[120:123], v[168:171]\n" \
"ds_read_b128 a[208:211], v246 offset:24576\n" \
"Lpg6170_%=:\n" \
"s_cbranch_execz Lpg7642_%=\n" \
"v_cndmask_b32 v34, v34, v254, s[64:65]\n" \
"v_mfma_f32_16x16x32_bf16 a[12:15], a[216:219], v[64:67], a[12:15]\n" \
"v_cmp_gt_i32 s[64:65], 49, v251\n" \
"v_cndmask_b32 v35, v35, v254, s[66:67]\n" \
"v_mfma_f32_16x16x32_bf16 a[12:15], a[220:223], v[96:99], a[12:15]\n" \
"v_cmp_gt_i32 s[66:67], 50, v251\n" \
"v_cndmask_b32 v36, v36, v254, vcc\n" \
"v_mfma_f32_16x16x32_bf16 a[44:47], a[216:219], v[72:75], a[44:47]\n" \
"v_cmp_gt_i32 vcc, 51, v251\n" \
"v_cndmask_b32 v37, v37, v254, s[64:65]\n" \
"v_mfma_f32_16x16x32_bf16 a[44:47], a[220:223], v[104:107], a[44:47]\n" \
"ds_read_b128 a[212:215], v246 offset:26624\n" \
"global_load_lds_dwordx4 v250, s[36:37] offset:2048 sc1\n" \
"v_mfma_f32_16x16x32_bf16 v[140:143], a[216:219], v[80:83], v[140:143]\n" \
"v_cndmask_b32 v38, v38, v254, s[66:67]\n" \
"v_cndmask_b32 v39, v39, v254, vcc\n" \
"v_mfma_f32_16x16x32_bf16 v[140:143], a[220:223], v[112:115], v[140:143]\n" \
"v_exp_f32 v0, v0\n" \
"v_mfma_f32_16x16x32_bf16 v[172:175], a[216:219], v[88:91], v[172:175]\n" \
"s_waitcnt lgkmcnt(6)\n" \
"v_add_f32 v240, v240, v0\n" \
"v_exp_f32 v1, v1\n" \
"v_mfma_f32_16x16x32_bf16 v[172:175], a[220:223], v[120:123], v[172:175]\n" \
"ds_read_b128 a[216:219], v246 offset:24832\n" \
"v_add_f32 v240, v240, v1\n" \
"v_mfma_f32_16x16x32_bf16 a[16:19], a[224:227], v[64:67], a[16:19]\n" \
"v_exp_f32 v2, v2\n" \
"v_mfma_f32_16x16x32_bf16 a[16:19], a[228:231], v[96:99], a[16:19]\n" \
"v_add_f32 v240, v240, v2\n" \
"v_cvt_pk_bf16_f32 v0, v0, v1\n" \
"v_mfma_f32_16x16x32_bf16 a[48:51], a[224:227], v[72:75], a[48:51]\n" \
"v_exp_f32 v3, v3\n" \
"v_mfma_f32_16x16x32_bf16 a[48:51], a[228:231], v[104:107], a[48:51]\n" \
"ds_read_b128 a[220:223], v246 offset:26880\n" \
"v_add_f32 v240, v240, v3\n" \
"v_exp_f32 v4, v4\n" \
"v_mfma_f32_16x16x32_bf16 v[144:147], a[224:227], v[80:83], v[144:147]\n" \
"v_add_f32 v240, v240, v4\n" \
"v_cvt_pk_bf16_f32 v1, v2, v3\n" \
"v_mfma_f32_16x16x32_bf16 v[144:147], a[228:231], v[112:115], v[144:147]\n" \
"v_exp_f32 v5, v5\n" \
"v_mfma_f32_16x16x32_bf16 v[176:179], a[224:227], v[88:91], v[176:179]\n" \
"global_load_lds_dwordx4 v250, s[36:37] offset:3072 sc1\n" \
"s_add_u32 s36, s36, 17408\n" \
"v_mfma_f32_16x16x32_bf16 v[176:179], a[228:231], v[120:123], v[176:179]\n" \
"s_xor_b32 s73, s73, 0x8000\n" \
"ds_read_b128 a[224:227], v246 offset:20480\n" \
"s_xor_b64 s[52:53], s[44:45], s[42:43]\n" \
"v_mfma_f32_16x16x32_bf16 a[20:23], a[232:235], v[64:67], a[20:23]\n" \
"s_xor_b64 s[54:55], s[46:47], s[42:43]\n" \
"s_or_b64 s[52:53], s[52:53], s[54:55]\n" \
"s_xor_b64 s[54:55], s[48:49], s[42:43]\n" \
"v_mfma_f32_16x16x32_bf16 a[20:23], a[236:239], v[96:99], a[20:23]\n" \
"s_or_b64 s[52:53], s[52:53], s[54:55]\n" \
"s_xor_b64 s[54:55], s[50:51], s[42:43]\n" \
"s_or_b64 s[52:53], s[52:53], s[54:55]\n" \
"v_mfma_f32_16x16x32_bf16 a[52:55], a[232:235], v[72:75], a[52:55]\n" \
"s_and_b64 s[52:53], s[52:53], s[8:9]\n" \
"s_cbranch_scc1 Lrs1_%=\n" \
"Lrr1_%=:\n" \
"v_mfma_f32_16x16x32_bf16 a[52:55], a[236:239], v[104:107], a[52:55]\n" \
"s_mov_b64 s[8:9], s[10:11]\n" \
"ds_read_b128 a[228:231], v246 offset:22528\n" \
"v_add_f32 v240, v240, v5\n" \
"v_mfma_f32_16x16x32_bf16 v[148:151], a[232:235], v[80:83], v[148:151]\n" \
"v_exp_f32 v6, v6\n" \
"v_mfma_f32_16x16x32_bf16 v[148:151], a[236:239], v[112:115], v[148:151]\n" \
"v_add_f32 v240, v240, v6\n" \
"v_cvt_pk_bf16_f32 v2, v4, v5\n" \
"v_mfma_f32_16x16x32_bf16 v[180:183], a[232:235], v[88:91], v[180:183]\n" \
"v_exp_f32 v7, v7\n" \
"v_mfma_f32_16x16x32_bf16 v[180:183], a[236:239], v[120:123], v[180:183]\n" \
"ds_read_b128 a[232:235], v246 offset:20736\n" \
"v_add_f32 v240, v240, v7\n" \
"v_mfma_f32_16x16x32_bf16 a[24:27], a[240:243], v[64:67], a[24:27]\n" \
"v_exp_f32 v24, v24\n" \
"v_mfma_f32_16x16x32_bf16 a[24:27], a[244:247], v[96:99], a[24:27]\n" \
"v_add_f32 v243, v243, v24\n" \
"v_cvt_pk_bf16_f32 v3, v6, v7\n" \
"v_mfma_f32_16x16x32_bf16 a[56:59], a[240:243], v[72:75], a[56:59]\n" \
"v_exp_f32 v25, v25\n" \
"v_mfma_f32_16x16x32_bf16 a[56:59], a[244:247], v[104:107], a[56:59]\n" \
"ds_read_b128 a[236:239], v246 offset:22784\n" \
"v_add_f32 v243, v243, v25\n" \
"v_mfma_f32_16x16x32_bf16 v[152:155], a[240:243], v[80:83], v[152:155]\n" \
"v_exp_f32 v26, v26\n" \
"v_mfma_f32_16x16x32_bf16 v[152:155], a[244:247], v[112:115], v[152:155]\n" \
"v_add_f32 v243, v243, v26\n" \
"v_cvt_pk_bf16_f32 v24, v24, v25\n" \
"v_mfma_f32_16x16x32_bf16 v[184:187], a[240:243], v[88:91], v[184:187]\n" \
"v_exp_f32 v27, v27\n" \
"v_mfma_f32_16x16x32_bf16 v[184:187], a[244:247], v[120:123], v[184:187]\n" \
"ds_read_b128 a[240:243], v246 offset:28672\n" \
"v_add_f32 v243, v243, v27\n" \
"v_mfma_f32_16x16x32_bf16 a[28:31], a[248:251], v[64:67], a[28:31]\n" \
"v_exp_f32 v28, v28\n" \
"v_mfma_f32_16x16x32_bf16 a[28:31], a[252:255], v[96:99], a[28:31]\n" \
"v_add_f32 v243, v243, v28\n" \
"v_cvt_pk_bf16_f32 v25, v26, v27\n" \
"v_mfma_f32_16x16x32_bf16 a[60:63], a[248:251], v[72:75], a[60:63]\n" \
"v_exp_f32 v29, v29\n" \
"v_mfma_f32_16x16x32_bf16 a[60:63], a[252:255], v[104:107], a[60:63]\n" \
"ds_read_b128 a[244:247], v246 offset:30720\n" \
"v_add_f32 v243, v243, v29\n" \
"v_mfma_f32_16x16x32_bf16 v[156:159], a[248:251], v[80:83], v[156:159]\n" \
"v_exp_f32 v30, v30\n" \
"v_mfma_f32_16x16x32_bf16 v[156:159], a[252:255], v[112:115], v[156:159]\n" \
"v_add_f32 v243, v243, v30\n" \
"v_cvt_pk_bf16_f32 v26, v28, v29\n" \
"v_mfma_f32_16x16x32_bf16 v[188:191], a[248:251], v[88:91], v[188:191]\n" \
"s_waitcnt lgkmcnt(6)\n" \
"v_exp_f32 v31, v31\n" \
"v_mfma_f32_16x16x32_bf16 v[188:191], a[252:255], v[120:123], v[188:191]\n" \
"ds_read_b128 a[248:251], v246 offset:28928\n" \
"v_add_f32 v243, v243, v31\n" \
"LA1l_%=:\n" \
"Lpg6287_%=:\n" \
"s_cbranch_execz Lpg13740_%=\n" \
"s_getpc_b64 s[40:41]\n" \
"Lpd_%=:\n" \
"s_lshl_b32 s80, s62, 2\n" \
"s_add_u32 s80, s80, s61\n" \
"s_and_b32 s80, s80, 15\n" \
"s_lshl_b32 s80, s80, 12\n" \
"s_mov_b32 s81, Lcode_end_%=-Lpd_%=-4\n" \
"v_add_u32 v252, s80, v252\n" \
"v_min_u32 v252, s81, v252\n" \
"s_waitcnt lgkmcnt(0)\n" \
"v_mfma_f32_16x16x32_bf16 v[72:75], a[192:195], a[144:147], v[228:231]\n" \
"v_exp_f32 v32, v32\n" \
"v_mfma_f32_16x16x32_bf16 v[72:75], a[196:199], a[148:151], v[72:75]\n" \
"v_cvt_pk_bf16_f32 v27, v30, v31\n" \
"v_add_f32 v240, v240, v32\n" \
"v_exp_f32 v33, v33\n" \
"v_mfma_f32_16x16x32_bf16 v[80:83], a[192:195], a[160:163], v[232:235]\n" \
"s_mov_b32 m0, s74\n" \
"v_add_f32 v240, v240, v33\n" \
"v_exp_f32 v34, v34\n" \
"v_mfma_f32_16x16x32_bf16 v[80:83], a[196:199], a[164:167], v[80:83]\n" \
"ds_read_b128 a[252:255], v246 offset:30976\n" \
"global_load_lds_dwordx4 v250, s[38:39] offset:0 sc1\n" \
"v_add_f32 v240, v240, v34\n" \
"v_cvt_pk_bf16_f32 v32, v32, v33\n" \
"v_exp_f32 v35, v35\n" \
"v_mfma_f32_16x16x32_bf16 v[88:91], a[192:195], a[176:179], v[236:239]\n" \
"s_lshl_b32 s70, s95, 1\n" \
"v_add_f32 v240, v240, v35\n" \
"v_exp_f32 v36, v36\n" \
"s_xor_b32 s70, s70, 139264\n" \
"v_add_u32 v244, s70, v248\n" \
"v_mfma_f32_16x16x32_bf16 v[88:91], a[196:199], a[180:183], v[88:91]\n" \
"ds_read_b128 a[192:195], v247 offset:0\n" \
"v_add_f32 v240, v240, v36\n" \
"v_cvt_pk_bf16_f32 v33, v34, v35\n" \
"v_exp_f32 v37, v37\n" \
"v_mfma_f32_16x16x32_bf16 v[76:79], a[200:203], a[144:147], v[228:231]\n" \
"ds_read_b128 v[192:195], v244 offset:0\n" \
"v_add_f32 v240, v240, v37\n" \
"v_exp_f32 v38, v38\n" \
"v_mfma_f32_16x16x32_bf16 v[76:79], a[204:207], a[148:151], v[76:79]\n" \
"ds_read_b128 v[196:199], v244 offset:1024\n" \
"v_add_f32 v240, v240, v38\n" \
"v_cvt_pk_bf16_f32 v34, v36, v37\n" \
"v_exp_f32 v39, v39\n" \
"v_mfma_f32_16x16x32_bf16 v[84:87], a[200:203], a[160:163], v[232:235]\n" \
"ds_read_b128 a[196:199], v247 offset:8192\n" \
"v_add_f32 v240, v240, v39\n" \
"v_exp_f32 v40, v40\n" \
"v_mfma_f32_16x16x32_bf16 v[84:87], a[204:207], a[164:167], v[84:87]\n" \
"ds_read_b128 v[200:203], v244 offset:2048\n" \
"v_add_f32 v241, v241, v40\n" \
"v_cvt_pk_bf16_f32 v35, v38, v39\n" \
"v_exp_f32 v41, v41\n" \
"v_mfma_f32_16x16x32_bf16 v[92:95], a[200:203], a[176:179], v[236:239]\n" \
"ds_read_b128 v[204:207], v244 offset:3072\n" \
"v_add_f32 v241, v241, v41\n" \
"v_exp_f32 v42, v42\n" \
"v_mfma_f32_16x16x32_bf16 v[92:95], a[204:207], a[180:183], v[92:95]\n" \
"ds_read_b128 a[200:203], v247 offset:1024\n" \
"v_add_f32 v241, v241, v42\n" \
"v_cvt_pk_bf16_f32 v40, v40, v41\n" \
"v_exp_f32 v43, v43\n" \
"v_mfma_f32_16x16x32_bf16 v[104:107], a[208:211], a[144:147], v[228:231]\n" \
"ds_read_b128 v[208:211], v244 offset:4096\n" \
"global_load_lds_dwordx4 v250, s[38:39] offset:1024 sc1\n" \
"v_add_f32 v241, v241, v43\n" \
"v_exp_f32 v44, v44\n" \
"v_mfma_f32_16x16x32_bf16 v[104:107], a[212:215], a[148:151], v[104:107]\n" \
"ds_read_b128 v[212:215], v244 offset:5120\n" \
"v_add_f32 v241, v241, v44\n" \
"v_cvt_pk_bf16_f32 v41, v42, v43\n" \
"v_exp_f32 v45, v45\n" \
"v_mfma_f32_16x16x32_bf16 v[112:115], a[208:211], a[160:163], v[232:235]\n" \
"ds_read_b128 a[204:207], v247 offset:9216\n" \
"v_add_f32 v241, v241, v45\n" \
"v_exp_f32 v46, v46\n" \
"v_mfma_f32_16x16x32_bf16 v[112:115], a[212:215], a[164:167], v[112:115]\n" \
"ds_read_b128 v[216:219], v244 offset:6144\n" \
"v_add_f32 v241, v241, v46\n" \
"v_cvt_pk_bf16_f32 v42, v44, v45\n" \
"v_exp_f32 v47, v47\n" \
"v_mfma_f32_16x16x32_bf16 v[120:123], a[208:211], a[176:179], v[236:239]\n" \
"ds_read_b128 v[220:223], v244 offset:7168\n" \
"v_add_f32 v241, v241, v47\n" \
"v_exp_f32 v48, v48\n" \
"v_mfma_f32_16x16x32_bf16 v[120:123], a[212:215], a[180:183], v[120:123]\n" \
"ds_read_b128 a[208:211], v247 offset:2048\n" \
"v_add_f32 v242, v242, v48\n" \
"v_cvt_pk_bf16_f32 v43, v46, v47\n" \
"v_exp_f32 v49, v49\n" \
"v_mfma_f32_16x16x32_bf16 v[108:111], a[216:219], a[144:147], v[228:231]\n" \
"ds_write_b128 v244, v[232:235]\n" \
"v_mfma_f32_16x16x32_bf16 v[108:111], a[220:223], a[148:151], v[108:111]\n" \
"ds_write_b128 v244, v[236:239] offset:1024\n" \
"v_mfma_f32_16x16x32_bf16 v[116:119], a[216:219], a[160:163], v[232:235]\n" \
"ds_read_b128 a[212:215], v247 offset:10240\n" \
"v_xor_b32 v245, 0x2000, v244\n" \
"v_mfma_f32_16x16x32_bf16 v[116:119], a[220:223], a[164:167], v[116:119]\n" \
"v_xor_b32 v246, 0x8000, v246\n" \
"v_add_u32 v246, s82, v246\n" \
"v_mfma_f32_16x16x32_bf16 v[124:127], a[216:219], a[176:179], v[236:239]\n" \
"s_waitcnt lgkmcnt(6)\n" \
"global_load_lds_dwordx4 v250, s[38:39] offset:2048 sc1\n" \
"v_add_f32 v242, v242, v49\n" \
"v_mfma_f32_16x16x32_bf16 v[124:127], a[220:223], a[180:183], v[124:127]\n" \
"ds_read_b128 a[216:219], v247 offset:3072\n" \
"v_exp_f32 v50, v50\n" \
"v_mfma_f32_16x16x32_bf16 v[72:75], a[224:227], a[152:155], v[72:75]\n" \
"v_add_f32 v242, v242, v50\n" \
"v_cvt_pk_bf16_f32 v48, v48, v49\n" \
"v_mfma_f32_16x16x32_bf16 v[72:75], a[228:231], a[156:159], v[72:75]\n" \
"v_exp_f32 v51, v51\n" \
"v_mfma_f32_16x16x32_bf16 v[80:83], a[224:227], a[168:171], v[80:83]\n" \
"ds_read_b128 a[220:223], v247 offset:11264\n" \
"v_add_f32 v242, v242, v51\n" \
"v_mfma_f32_16x16x32_bf16 v[80:83], a[228:231], a[172:175], v[80:83]\n" \
"v_exp_f32 v52, v52\n" \
"v_mfma_f32_16x16x32_bf16 v[88:91], a[224:227], a[184:187], v[88:91]\n" \
"v_add_f32 v242, v242, v52\n" \
"v_cvt_pk_bf16_f32 v49, v50, v51\n" \
"v_mfma_f32_16x16x32_bf16 v[88:91], a[228:231], a[188:191], v[88:91]\n" \
"ds_read_b128 a[224:227], v247 offset:4096\n" \
"v_exp_f32 v53, v53\n" \
"v_mfma_f32_16x16x32_bf16 v[76:79], a[232:235], a[152:155], v[76:79]\n" \
"global_load_lds_dwordx4 v250, s[38:39] offset:3072 sc1\n" \
"v_add_f32 v242, v242, v53\n" \
"v_mfma_f32_16x16x32_bf16 v[76:79], a[236:239], a[156:159], v[76:79]\n" \
"s_add_u32 s38, s38, 17408\n" \
"v_exp_f32 v54, v54\n" \
"v_mfma_f32_16x16x32_bf16 v[84:87], a[232:235], a[168:171], v[84:87]\n" \
"ds_read_b128 a[228:231], v247 offset:12288\n" \
"v_add_f32 v242, v242, v54\n" \
"v_cvt_pk_bf16_f32 v50, v52, v53\n" \
"v_mfma_f32_16x16x32_bf16 v[84:87], a[236:239], a[172:175], v[84:87]\n" \
"v_exp_f32 v55, v55\n" \
"v_mfma_f32_16x16x32_bf16 v[92:95], a[232:235], a[184:187], v[92:95]\n" \
"v_add_f32 v242, v242, v55\n" \
"v_exp_f32 v56, v56\n" \
"v_mfma_f32_16x16x32_bf16 v[92:95], a[236:239], a[188:191], v[92:95]\n" \
"ds_read_b128 a[232:235], v247 offset:5120\n" \
"v_add_f32 v243, v243, v56\n" \
"v_cvt_pk_bf16_f32 v51, v54, v55\n" \
"v_mfma_f32_16x16x32_bf16 v[104:107], a[240:243], a[152:155], v[104:107]\n" \
"global_load_dword v253, v252, s[40:41]\n" \
"v_mfma_f32_16x16x32_bf16 v[104:107], a[244:247], a[156:159], v[104:107]\n" \
"v_exp_f32 v57, v57\n" \
"v_mfma_f32_16x16x32_bf16 v[112:115], a[240:243], a[168:171], v[112:115]\n" \
"ds_read_b128 a[236:239], v247 offset:13312\n" \
"v_add_f32 v243, v243, v57\n" \
"v_exp_f32 v58, v58\n" \
"v_mfma_f32_16x16x32_bf16 v[112:115], a[244:247], a[172:175], v[112:115]\n" \
"v_add_f32 v243, v243, v58\n" \
"v_cvt_pk_bf16_f32 v56, v56, v57\n" \
"v_mfma_f32_16x16x32_bf16 v[120:123], a[240:243], a[184:187], v[120:123]\n" \
"v_exp_f32 v59, v59\n" \
"v_mfma_f32_16x16x32_bf16 v[120:123], a[244:247], a[188:191], v[120:123]\n" \
"ds_read_b128 a[240:243], v247 offset:6144\n" \
"v_add_f32 v243, v243, v59\n" \
"v_exp_f32 v60, v60\n" \
"v_mfma_f32_16x16x32_bf16 v[108:111], a[248:251], a[152:155], v[108:111]\n" \
"v_add_f32 v243, v243, v60\n" \
"v_cvt_pk_bf16_f32 v57, v58, v59\n" \
"v_mfma_f32_16x16x32_bf16 v[108:111], a[252:255], a[156:159], v[108:111]\n" \
"v_exp_f32 v61, v61\n" \
"v_mfma_f32_16x16x32_bf16 v[116:119], a[248:251], a[168:171], v[116:119]\n" \
"ds_read_b128 a[244:247], v247 offset:14336\n" \
"v_add_f32 v243, v243, v61\n" \
"v_exp_f32 v62, v62\n" \
"v_mfma_f32_16x16x32_bf16 v[116:119], a[252:255], a[172:175], v[116:119]\n" \
"v_add_f32 v243, v243, v62\n" \
"v_cvt_pk_bf16_f32 v58, v60, v61\n" \
"v_mfma_f32_16x16x32_bf16 v[124:127], a[248:251], a[184:187], v[124:127]\n" \
"s_waitcnt vmcnt(5) lgkmcnt(6)\n" \
"v_exp_f32 v63, v63\n" \
"v_mfma_f32_16x16x32_bf16 v[124:127], a[252:255], a[188:191], v[124:127]\n" \
"s_barrier\n" \
"ds_read_b128 a[248:251], v247 offset:7168\n" \
"v_add_f32 v243, v243, v63\n" \
"v_mfma_f32_16x16x32_bf16 a[0:3], a[192:195], v[0:3], a[0:3]\n" \
"v_exp_f32 v80, v80\n" \
"v_mfma_f32_16x16x32_bf16 a[0:3], a[196:199], v[32:35], a[0:3]\n" \
"v_cvt_pk_bf16_f32 v59, v62, v63\n" \
"v_add_f32 v242, v242, v80\n" \
"v_exp_f32 v81, v81\n" \
"v_mfma_f32_16x16x32_bf16 a[32:35], a[192:195], v[8:11], a[32:35]\n" \
"v_add_f32 v242, v242, v81\n" \
"v_exp_f32 v82, v82\n" \
"v_mfma_f32_16x16x32_bf16 a[32:35], a[196:199], v[40:43], a[32:35]\n" \
"ds_read_b128 a[252:255], v247 offset:15360\n" \
"v_add_f32 v242, v242, v82\n" \
"v_cvt_pk_bf16_f32 v80, v80, v81\n" \
"v_exp_f32 v83, v83\n" \
"v_mfma_f32_16x16x32_bf16 v[128:131], a[192:195], v[16:19], v[128:131]\n" \
"ds_read_b128 v[224:227], v245\n" \
"v_add_f32 v242, v242, v83\n" \
"v_exp_f32 v84, v84\n" \
"v_mfma_f32_16x16x32_bf16 v[128:131], a[196:199], v[48:51], v[128:131]\n" \
"ds_read_b128 v[228:231], v245 offset:1024\n" \
"v_add_f32 v242, v242, v84\n" \
"v_cvt_pk_bf16_f32 v81, v82, v83\n" \
"v_exp_f32 v85, v85\n" \
"v_mov_b32 v244, 0\n" \
"v_mfma_f32_16x16x32_bf16 v[160:163], a[192:195], v[24:27], v[160:163]\n" \
"v_mov_b32 v245, 0\n" \
"v_xor_b32 v252, 0x8000, v246\n" \
"v_add_f32 v242, v242, v85\n" \
"v_exp_f32 v86, v86\n" \
"v_mfma_f32_16x16x32_bf16 v[160:163], a[196:199], v[56:59], v[160:163]\n" \
"s_add_u32 s73, s73, s95\n" \
"ds_read_b128 a[192:195], v246 offset:0\n" \
"v_add_f32 v242, v242, v86\n" \
"v_cvt_pk_bf16_f32 v82, v84, v85\n" \
"v_exp_f32 v87, v87\n" \
"v_mfma_f32_16x16x32_bf16 a[4:7], a[200:203], v[0:3], a[4:7]\n" \
"s_add_u32 s36, s36, s83\n" \
"v_add_f32 v242, v242, v87\n" \
"s_add_u32 s73, s73, 4096\n" \
"s_mov_b32 m0, s73\n" \
"v_mfma_f32_16x16x32_bf16 a[4:7], a[204:207], v[32:35], a[4:7]\n" \
"global_load_lds_dwordx4 v250, s[36:37] offset:-4096 sc1\n" \
"v_cvt_pk_bf16_f32 v83, v86, v87\n" \
"v_mfma_f32_16x16x32_bf16 a[36:39], a[200:203], v[8:11], a[36:39]\n" \
"v_cmp_gt_i32 vcc, 0, v251\n" \
"v_cmp_gt_i32 s[64:65], 1, v251\n" \
"v_mfma_f32_16x16x32_bf16 a[36:39], a[204:207], v[40:43], a[36:39]\n" \
"ds_read_b128 a[196:199], v246 offset:2048\n" \
"global_load_lds_dwordx4 v250, s[36:37] offset:-3072 sc1\n" \
"v_mfma_f32_16x16x32_bf16 v[132:135], a[200:203], v[16:19], v[132:135]\n" \
"v_cmp_gt_i32 s[66:67], 2, v251\n" \
"v_cndmask_b32 v72, v72, v254, vcc\n" \
"v_mfma_f32_16x16x32_bf16 v[132:135], a[204:207], v[48:51], v[132:135]\n" \
"v_cmp_gt_i32 vcc, 3, v251\n" \
"v_cndmask_b32 v73, v73, v254, s[64:65]\n" \
"v_mfma_f32_16x16x32_bf16 v[164:167], a[200:203], v[24:27], v[164:167]\n" \
"v_cmp_gt_i32 s[64:65], 16, v251\n" \
"v_cndmask_b32 v74, v74, v254, s[66:67]\n" \
"v_mfma_f32_16x16x32_bf16 v[164:167], a[204:207], v[56:59], v[164:167]\n" \
"ds_read_b128 a[200:203], v246 offset:256\n" \
"global_load_lds_dwordx4 v250, s[36:37] offset:-2048 sc1\n" \
"v_mfma_f32_16x16x32_bf16 a[8:11], a[208:211], v[0:3], a[8:11]\n" \
"v_cmp_gt_i32 s[66:67], 17, v251\n" \
"v_cndmask_b32 v75, v75, v254, vcc\n" \
"v_mfma_f32_16x16x32_bf16 a[8:11], a[212:215], v[32:35], a[8:11]\n" \
"v_cmp_gt_i32 vcc, 18, v251\n" \
"v_cndmask_b32 v76, v76, v254, s[64:65]\n" \
"v_mfma_f32_16x16x32_bf16 a[40:43], a[208:211], v[8:11], a[40:43]\n" \
"v_cmp_gt_i32 s[64:65], 19, v251\n" \
"v_cndmask_b32 v77, v77, v254, s[66:67]\n" \
"v_mfma_f32_16x16x32_bf16 a[40:43], a[212:215], v[40:43], a[40:43]\n" \
"ds_read_b128 a[204:207], v246 offset:2304\n" \
"global_load_lds_dwordx4 v250, s[36:37] offset:-1024 sc1\n" \
"v_mfma_f32_16x16x32_bf16 v[136:139], a[208:211], v[16:19], v[136:139]\n" \
"v_cmp_gt_i32 s[66:67], 32, v251\n" \
"v_cndmask_b32 v78, v78, v254, vcc\n" \
"v_mfma_f32_16x16x32_bf16 v[136:139], a[212:215], v[48:51], v[136:139]\n" \
"v_cmp_gt_i32 vcc, 33, v251\n" \
"v_cndmask_b32 v79, v79, v254, s[64:65]\n" \
"v_mfma_f32_16x16x32_bf16 v[168:171], a[208:211], v[24:27], v[168:171]\n" \
"v_cmp_gt_i32 s[64:65], 34, v251\n" \
"v_cndmask_b32 v104, v104, v254, s[66:67]\n" \
"v_mfma_f32_16x16x32_bf16 v[168:171], a[212:215], v[56:59], v[168:171]\n" \
"ds_read_b128 a[208:211], v246 offset:8192\n" \
"global_load_lds_dwordx4 v250, s[36:37] offset:0 sc1\n" \
"v_mfma_f32_16x16x32_bf16 a[12:15], a[216:219], v[0:3], a[12:15]\n" \
"v_cmp_gt_i32 s[66:67], 35, v251\n" \
"v_cndmask_b32 v105, v105, v254, vcc\n" \
"v_mfma_f32_16x16x32_bf16 a[12:15], a[220:223], v[32:35], a[12:15]\n" \
"v_cmp_gt_i32 vcc, 48, v251\n" \
"v_cndmask_b32 v106, v106, v254, s[64:65]\n" \
"v_mfma_f32_16x16x32_bf16 a[44:47], a[216:219], v[8:11], a[44:47]\n" \
"v_cmp_gt_i32 s[64:65], 49, v251\n" \
"v_cndmask_b32 v107, v107, v254, s[66:67]\n" \
"v_mfma_f32_16x16x32_bf16 a[44:47], a[220:223], v[40:43], a[44:47]\n" \
"ds_read_b128 a[212:215], v246 offset:10240\n" \
"global_load_lds_dwordx4 v250, s[36:37] offset:1024 sc1\n" \
"v_mfma_f32_16x16x32_bf16 v[140:143], a[216:219], v[16:19], v[140:143]\n" \
"v_cmp_gt_i32 s[66:67], 50, v251\n" \
"v_cndmask_b32 v108, v108, v254, vcc\n" \
"v_mfma_f32_16x16x32_bf16 v[140:143], a[220:223], v[48:51], v[140:143]\n" \
"v_cmp_gt_i32 vcc, 51, v251\n" \
"v_cndmask_b32 v109, v109, v254, s[64:65]\n" \
"v_mfma_f32_16x16x32_bf16 v[172:175], a[216:219], v[24:27], v[172:175]\n" \
"s_waitcnt lgkmcnt(6)\n" \
"v_cndmask_b32 v110, v110, v254, s[66:67]\n" \
"v_cndmask_b32 v111, v111, v254, vcc\n" \
"v_mfma_f32_16x16x32_bf16 v[172:175], a[220:223], v[56:59], v[172:175]\n" \
"ds_read_b128 a[216:219], v246 offset:8448\n" \
"global_load_lds_dwordx4 v250, s[36:37] offset:2048 sc1\n" \
"v_mfma_f32_16x16x32_bf16 a[16:19], a[224:227], v[0:3], a[16:19]\n" \
"v_exp_f32 v72, v72\n" \
"v_mfma_f32_16x16x32_bf16 a[16:19], a[228:231], v[32:35], a[16:19]\n" \
"v_add_f32 v241, v241, v72\n" \
"v_exp_f32 v73, v73\n" \
"v_mfma_f32_16x16x32_bf16 a[48:51], a[224:227], v[8:11], a[48:51]\n" \
"v_add_f32 v241, v241, v73\n" \
"v_exp_f32 v74, v74\n" \
"v_mfma_f32_16x16x32_bf16 a[48:51], a[228:231], v[40:43], a[48:51]\n" \
"ds_read_b128 a[220:223], v246 offset:10496\n" \
"global_load_lds_dwordx4 v250, s[36:37] offset:3072 sc1\n" \
"v_mfma_f32_16x16x32_bf16 v[144:147], a[224:227], v[16:19], v[144:147]\n" \
"s_add_u32 s36, s36, 34816\n" \
"v_add_f32 v241, v241, v74\n" \
"v_cvt_pk_bf16_f32 v72, v72, v73\n" \
"v_mfma_f32_16x16x32_bf16 v[144:147], a[228:231], v[48:51], v[144:147]\n" \
"v_exp_f32 v75, v75\n" \
"v_mfma_f32_16x16x32_bf16 v[176:179], a[224:227], v[24:27], v[176:179]\n" \
"s_add_u32 m0, s74, 0x4000\n" \
"v_add_f32 v241, v241, v75\n" \
"v_exp_f32 v76, v76\n" \
"v_mfma_f32_16x16x32_bf16 v[176:179], a[228:231], v[56:59], v[176:179]\n" \
"ds_read_b128 a[224:227], v246 offset:4096\n" \
"global_load_lds_dwordx4 v250, s[38:39] offset:0 sc1\n" \
"v_mfma_f32_16x16x32_bf16 a[20:23], a[232:235], v[0:3], a[20:23]\n" \
"v_add_f32 v241, v241, v76\n" \
"v_cvt_pk_bf16_f32 v73, v74, v75\n" \
"v_mfma_f32_16x16x32_bf16 a[20:23], a[236:239], v[32:35], a[20:23]\n" \
"v_exp_f32 v77, v77\n" \
"v_mfma_f32_16x16x32_bf16 a[52:55], a[232:235], v[8:11], a[52:55]\n" \
"v_add_f32 v241, v241, v77\n" \
"v_exp_f32 v78, v78\n" \
"v_mfma_f32_16x16x32_bf16 a[52:55], a[236:239], v[40:43], a[52:55]\n" \
"ds_read_b128 a[228:231], v246 offset:6144\n" \
"global_load_lds_dwordx4 v250, s[38:39] offset:1024 sc1\n" \
"v_mfma_f32_16x16x32_bf16 v[148:151], a[232:235], v[16:19], v[148:151]\n" \
"v_add_f32 v241, v241, v78\n" \
"v_cvt_pk_bf16_f32 v74, v76, v77\n" \
"v_mfma_f32_16x16x32_bf16 v[148:151], a[236:239], v[48:51], v[148:151]\n" \
"v_exp_f32 v79, v79\n" \
"v_mfma_f32_16x16x32_bf16 v[180:183], a[232:235], v[24:27], v[180:183]\n" \
"v_add_f32 v241, v241, v79\n" \
"v_exp_f32 v88, v88\n" \
"v_mfma_f32_16x16x32_bf16 v[180:183], a[236:239], v[56:59], v[180:183]\n" \
"ds_read_b128 a[232:235], v246 offset:4352\n" \
"global_load_lds_dwordx4 v250, s[38:39] offset:2048 sc1\n" \
"v_mfma_f32_16x16x32_bf16 a[24:27], a[240:243], v[0:3], a[24:27]\n" \
"v_add_f32 v243, v243, v88\n" \
"v_cvt_pk_bf16_f32 v75, v78, v79\n" \
"v_mfma_f32_16x16x32_bf16 a[24:27], a[244:247], v[32:35], a[24:27]\n" \
"v_exp_f32 v89, v89\n" \
"v_mfma_f32_16x16x32_bf16 a[56:59], a[240:243], v[8:11], a[56:59]\n" \
"v_add_f32 v243, v243, v89\n" \
"v_exp_f32 v90, v90\n" \
"v_mfma_f32_16x16x32_bf16 a[56:59], a[244:247], v[40:43], a[56:59]\n" \
"ds_read_b128 a[236:239], v246 offset:6400\n" \
"global_load_lds_dwordx4 v250, s[38:39] offset:3072 sc1\n" \
"v_add_f32 v243, v243, v90\n" \
"v_mfma_f32_16x16x32_bf16 v[152:155], a[240:243], v[16:19], v[152:155]\n" \
"s_add_u32 s38, s38, 17408\n" \
"s_add_u32 s38, s38, s83\n" \
"s_add_u32 s74, s74, s95\n" \
"v_mfma_f32_16x16x32_bf16 v[152:155], a[244:247], v[48:51], v[152:155]\n" \
"s_add_u32 s74, s74, 4096\n" \
"v_cvt_pk_bf16_f32 v88, v88, v89\n" \
"s_cmp_eq_u32 s63, 7\n" \
"v_mfma_f32_16x16x32_bf16 v[184:187], a[240:243], v[24:27], v[184:187]\n" \
"v_exp_f32 v91, v91\n" \
"v_mfma_f32_16x16x32_bf16 v[184:187], a[244:247], v[56:59], v[184:187]\n" \
"ds_read_b128 a[240:243], v246 offset:12288\n" \
"v_add_f32 v243, v243, v91\n" \
"v_mfma_f32_16x16x32_bf16 a[28:31], a[248:251], v[0:3], a[28:31]\n" \
"v_exp_f32 v92, v92\n" \
"v_mfma_f32_16x16x32_bf16 a[28:31], a[252:255], v[32:35], a[28:31]\n" \
"v_add_f32 v243, v243, v92\n" \
"v_cvt_pk_bf16_f32 v89, v90, v91\n" \
"v_mfma_f32_16x16x32_bf16 a[60:63], a[248:251], v[8:11], a[60:63]\n" \
"v_exp_f32 v93, v93\n" \
"v_mfma_f32_16x16x32_bf16 a[60:63], a[252:255], v[40:43], a[60:63]\n" \
"ds_read_b128 a[244:247], v246 offset:14336\n" \
"v_add_f32 v243, v243, v93\n" \
"v_mfma_f32_16x16x32_bf16 v[156:159], a[248:251], v[16:19], v[156:159]\n" \
"v_exp_f32 v94, v94\n" \
"v_mfma_f32_16x16x32_bf16 v[156:159], a[252:255], v[48:51], v[156:159]\n" \
"v_add_f32 v243, v243, v94\n" \
"v_cvt_pk_bf16_f32 v90, v92, v93\n" \
"v_mfma_f32_16x16x32_bf16 v[188:191], a[248:251], v[24:27], v[188:191]\n" \
"s_waitcnt lgkmcnt(6)\n" \
"v_exp_f32 v95, v95\n" \
"v_mfma_f32_16x16x32_bf16 v[188:191], a[252:255], v[56:59], v[188:191]\n" \
"ds_read_b128 a[248:251], v246 offset:12544\n" \
"v_add_f32 v243, v243, v95\n" \
"s_cbranch_scc1 LF7_%=\n" \
"v_mfma_f32_16x16x32_bf16 v[0:3], a[192:195], v[192:195], v[224:227]\n" \
"s_load_dwordx8 s[44:51], s[56:57], s79 glc\n" \
"v_exp_f32 v104, v104\n" \
"v_mfma_f32_16x16x32_bf16 v[0:3], a[196:199], v[196:199], v[0:3]\n" \
"v_cvt_pk_bf16_f32 v91, v94, v95\n" \
"v_add_f32 v241, v241, v104\n" \
"v_mfma_f32_16x16x32_bf16 v[8:11], a[192:195], v[208:211], v[228:231]\n" \
"v_mov_b32 v76, v240\n" \
"v_exp_f32 v105, v105\n" \
"v_mfma_f32_16x16x32_bf16 v[8:11], a[196:199], v[212:215], v[8:11]\n" \
"ds_read_b128 a[252:255], v246 offset:14592\n" \
"v_add_f32 v241, v241, v105\n" \
"v_mfma_f32_16x16x32_bf16 v[16:19], a[192:195], a[160:163], v[232:235]\n" \
"v_exp_f32 v106, v106\n" \
"v_permlane16_swap_b32 v76, v240\n" \
"v_add_f32 v240, v240, v76\n" \
"v_mov_b32 v76, v240\n" \
"v_mfma_f32_16x16x32_bf16 v[16:19], a[196:199], a[164:167], v[16:19]\n" \
"v_add_f32 v241, v241, v106\n" \
"v_cvt_pk_bf16_f32 v104, v104, v105\n" \
"v_mfma_f32_16x16x32_bf16 v[24:27], a[192:195], a[176:179], v[236:239]\n" \
"v_exp_f32 v107, v107\n" \
"v_permlane32_swap_b32 v76, v240\n" \
"v_add_f32 v240, v240, v76\n" \
"v_rcp_f32 v76, v240\n" \
"v_add_u32 v77, 0x1000, v249\n" \
"v_mul_f32 v240, 0x4d800000, v240\n" \
"v_accvgpr_read_b32 v64, a0\n" \
"v_mfma_f32_16x16x32_bf16 v[24:27], a[196:199], a[180:183], v[24:27]\n" \
"ds_read_b128 a[192:195], v247 offset:16384\n" \
"v_add_f32 v241, v241, v107\n" \
"v_accvgpr_read_b32 v65, a1\n" \
"v_accvgpr_read_b32 v66, a2\n" \
"v_accvgpr_read_b32 v67, a3\n" \
"v_accvgpr_read_b32 v68, a4\n" \
"v_accvgpr_read_b32 v69, a5\n" \
"v_accvgpr_read_b32 v70, a6\n" \
"v_mfma_f32_16x16x32_bf16 v[4:7], a[200:203], v[192:195], v[224:227]\n" \
"v_accvgpr_read_b32 v71, a7\n" \
"v_mul_f32 v64, v64, v76\n" \
"v_mul_f32 v65, v65, v76\n" \
"v_mul_f32 v66, v66, v76\n" \
"v_mul_f32 v67, v67, v76\n" \
"v_mul_f32 v68, v68, v76\n" \
"v_exp_f32 v108, v108\n" \
"v_mfma_f32_16x16x32_bf16 v[4:7], a[204:207], v[196:199], v[4:7]\n" \
"v_add_f32 v241, v241, v108\n" \
"v_cvt_pk_bf16_f32 v105, v106, v107\n" \
"v_mul_f32 v69, v69, v76\n" \
"v_mul_f32 v70, v70, v76\n" \
"v_mul_f32 v71, v71, v76\n" \
"v_fma_f32 v240, v64, v65, v240\n" \
"v_cvt_pk_bf16_f32 v96, v64, v65\n" \
"v_fma_f32 v240, v66, v67, v240\n" \
"v_mfma_f32_16x16x32_bf16 v[12:15], a[200:203], v[208:211], v[228:231]\n" \
"v_cvt_pk_bf16_f32 v97, v66, v67\n" \
"v_fma_f32 v240, v68, v69, v240\n" \
"v_cvt_pk_bf16_f32 v98, v68, v69\n" \
"v_fma_f32 v240, v70, v71, v240\n" \
"v_cvt_pk_bf16_f32 v99, v70, v71\n" \
"v_exp_f32 v109, v109\n" \
"v_mfma_f32_16x16x32_bf16 v[12:15], a[204:207], v[212:215], v[12:15]\n" \
"buffer_store_dwordx4 v[96:99], v249, s[56:59], s77 offen offset:0 sc1\n" \
"ds_read_b128 a[196:199], v247 offset:24576\n" \
"v_add_f32 v241, v241, v109\n" \
"v_accvgpr_read_b32 v64, a8\n" \
"v_accvgpr_read_b32 v65, a9\n" \
"v_accvgpr_read_b32 v66, a10\n" \
"v_accvgpr_read_b32 v67, a11\n" \
"v_accvgpr_read_b32 v68, a12\n" \
"v_mfma_f32_16x16x32_bf16 v[20:23], a[200:203], a[160:163], v[232:235]\n" \
"v_accvgpr_read_b32 v69, a13\n" \
"v_accvgpr_read_b32 v70, a14\n" \
"v_accvgpr_read_b32 v71, a15\n" \
"v_mul_f32 v64, v64, v76\n" \
"v_mul_f32 v65, v65, v76\n" \
"v_mul_f32 v66, v66, v76\n" \
"v_exp_f32 v110, v110\n" \
"v_mfma_f32_16x16x32_bf16 v[20:23], a[204:207], a[164:167], v[20:23]\n" \
"v_add_f32 v241, v241, v110\n" \
"v_cvt_pk_bf16_f32 v106, v108, v109\n" \
"v_mul_f32 v67, v67, v76\n" \
"v_mul_f32 v68, v68, v76\n" \
"v_mul_f32 v69, v69, v76\n" \
"v_mul_f32 v70, v70, v76\n" \
"v_mul_f32 v71, v71, v76\n" \
"v_fma_f32 v240, v64, v65, v240\n" \
"v_mfma_f32_16x16x32_bf16 v[28:31], a[200:203], a[176:179], v[236:239]\n" \
"v_cvt_pk_bf16_f32 v100, v64, v65\n" \
"v_fma_f32 v240, v66, v67, v240\n" \
"v_cvt_pk_bf16_f32 v101, v66, v67\n" \
"v_fma_f32 v240, v68, v69, v240\n" \
"v_cvt_pk_bf16_f32 v102, v68, v69\n" \
"v_fma_f32 v240, v70, v71, v240\n" \
"v_exp_f32 v111, v111\n" \
"v_mfma_f32_16x16x32_bf16 v[28:31], a[204:207], a[180:183], v[28:31]\n" \
"ds_read_b128 a[200:203], v247 offset:17408\n" \
"v_add_f32 v241, v241, v111\n" \
"v_cvt_pk_bf16_f32 v103, v70, v71\n" \
"v_mfma_f32_16x16x32_bf16 v[32:35], a[208:211], v[192:195], v[224:227]\n" \
"buffer_store_dwordx4 v[100:103], v249, s[56:59], s77 offen offset:2048 sc1\n" \
"v_exp_f32 v112, v112\n" \
"v_accvgpr_read_b32 v64, a16\n" \
"v_accvgpr_read_b32 v65, a17\n" \
"v_accvgpr_read_b32 v66, a18\n" \
"v_accvgpr_read_b32 v67, a19\n" \
"v_accvgpr_read_b32 v68, a20\n" \
"v_mfma_f32_16x16x32_bf16 v[32:35], a[212:215], v[196:199], v[32:35]\n" \
"v_add_f32 v242, v242, v112\n" \
"v_cvt_pk_bf16_f32 v107, v110, v111\n" \
"v_accvgpr_read_b32 v69, a21\n" \
"v_accvgpr_read_b32 v70, a22\n" \
"v_accvgpr_read_b32 v71, a23\n" \
"v_mul_f32 v64, v64, v76\n" \
"v_mul_f32 v65, v65, v76\n" \
"v_mul_f32 v66, v66, v76\n" \
"v_mfma_f32_16x16x32_bf16 v[40:43], a[208:211], v[208:211], v[228:231]\n" \
"v_mul_f32 v67, v67, v76\n" \
"v_mul_f32 v68, v68, v76\n" \
"v_mul_f32 v69, v69, v76\n" \
"v_mul_f32 v70, v70, v76\n" \
"v_mul_f32 v71, v71, v76\n" \
"v_fma_f32 v240, v64, v65, v240\n" \
"v_exp_f32 v113, v113\n" \
"v_mfma_f32_16x16x32_bf16 v[40:43], a[212:215], v[212:215], v[40:43]\n" \
"ds_read_b128 a[204:207], v247 offset:25600\n" \
"v_add_f32 v242, v242, v113\n" \
"v_cvt_pk_bf16_f32 v96, v64, v65\n" \
"v_fma_f32 v240, v66, v67, v240\n" \
"v_cvt_pk_bf16_f32 v97, v66, v67\n" \
"v_fma_f32 v240, v68, v69, v240\n" \
"v_cvt_pk_bf16_f32 v98, v68, v69\n" \
"v_fma_f32 v240, v70, v71, v240\n" \
"v_mfma_f32_16x16x32_bf16 v[48:51], a[208:211], a[160:163], v[232:235]\n" \
"v_cvt_pk_bf16_f32 v99, v70, v71\n" \
"v_exp_f32 v114, v114\n" \
"v_mfma_f32_16x16x32_bf16 v[48:51], a[212:215], a[164:167], v[48:51]\n" \
"buffer_store_dwordx4 v[96:99], v77, s[56:59], s77 offen offset:0 sc1\n" \
"v_add_f32 v242, v242, v114\n" \
"v_cvt_pk_bf16_f32 v112, v112, v113\n" \
"Lpg6811_%=:\n" \
"s_cbranch_execz Lcb0_%=\n" \
"v_accvgpr_read_b32 v64, a24\n" \
"v_accvgpr_read_b32 v65, a25\n" \
"v_accvgpr_read_b32 v66, a26\n" \
"v_accvgpr_read_b32 v67, a27\n" \
"v_accvgpr_read_b32 v68, a28\n" \
"v_mfma_f32_16x16x32_bf16 v[56:59], a[208:211], a[176:179], v[236:239]\n" \
"v_accvgpr_read_b32 v69, a29\n" \
"v_accvgpr_read_b32 v70, a30\n" \
"v_exp_f32 v115, v115\n" \
"v_mfma_f32_16x16x32_bf16 v[56:59], a[212:215], a[180:183], v[56:59]\n" \
"ds_read_b128 a[208:211], v247 offset:18432\n" \
"v_add_f32 v242, v242, v115\n" \
"v_mfma_f32_16x16x32_bf16 v[36:39], a[216:219], v[192:195], v[224:227]\n" \
"v_exp_f32 v116, v116\n" \
"v_mfma_f32_16x16x32_bf16 v[36:39], a[220:223], v[196:199], v[36:39]\n" \
"v_add_f32 v242, v242, v116\n" \
"v_cvt_pk_bf16_f32 v113, v114, v115\n" \
"v_mfma_f32_16x16x32_bf16 v[44:47], a[216:219], v[208:211], v[228:231]\n" \
"v_exp_f32 v117, v117\n" \
"v_mfma_f32_16x16x32_bf16 v[44:47], a[220:223], v[212:215], v[44:47]\n" \
"ds_read_b128 a[212:215], v247 offset:26624\n" \
"v_add_f32 v242, v242, v117\n" \
"v_mfma_f32_16x16x32_bf16 v[52:55], a[216:219], a[160:163], v[232:235]\n" \
"v_exp_f32 v118, v118\n" \
"v_mfma_f32_16x16x32_bf16 v[52:55], a[220:223], a[164:167], v[52:55]\n" \
"v_add_f32 v242, v242, v118\n" \
"v_cvt_pk_bf16_f32 v114, v116, v117\n" \
"v_mfma_f32_16x16x32_bf16 v[60:63], a[216:219], a[176:179], v[236:239]\n" \
"s_waitcnt lgkmcnt(6)\n" \
"v_exp_f32 v119, v119\n" \
"v_mfma_f32_16x16x32_bf16 v[60:63], a[220:223], a[180:183], v[60:63]\n" \
"ds_read_b128 a[216:219], v247 offset:19456\n" \
"v_add_f32 v242, v242, v119\n" \
"v_mfma_f32_16x16x32_bf16 v[0:3], a[224:227], v[200:203], v[0:3]\n" \
"v_exp_f32 v120, v120\n" \
"v_mfma_f32_16x16x32_bf16 v[0:3], a[228:231], v[204:207], v[0:3]\n" \
"v_add_f32 v243, v243, v120\n" \
"v_cvt_pk_bf16_f32 v115, v118, v119\n" \
"v_mfma_f32_16x16x32_bf16 v[8:11], a[224:227], v[216:219], v[8:11]\n" \
"v_exp_f32 v121, v121\n" \
"v_mfma_f32_16x16x32_bf16 v[8:11], a[228:231], v[220:223], v[8:11]\n" \
"ds_read_b128 a[220:223], v247 offset:27648\n" \
"v_add_f32 v243, v243, v121\n" \
"v_mfma_f32_16x16x32_bf16 v[16:19], a[224:227], a[168:171], v[16:19]\n" \
"v_exp_f32 v122, v122\n" \
"v_mfma_f32_16x16x32_bf16 v[16:19], a[228:231], a[172:175], v[16:19]\n" \
"v_add_f32 v243, v243, v122\n" \
"v_cvt_pk_bf16_f32 v120, v120, v121\n" \
"v_mfma_f32_16x16x32_bf16 v[24:27], a[224:227], a[184:187], v[24:27]\n" \
"v_exp_f32 v123, v123\n" \
"v_mfma_f32_16x16x32_bf16 v[24:27], a[228:231], a[188:191], v[24:27]\n" \
"ds_read_b128 a[224:227], v247 offset:20480\n" \
"v_add_f32 v243, v243, v123\n" \
"v_mfma_f32_16x16x32_bf16 v[4:7], a[232:235], v[200:203], v[4:7]\n" \
"v_exp_f32 v124, v124\n" \
"v_mfma_f32_16x16x32_bf16 v[4:7], a[236:239], v[204:207], v[4:7]\n" \
"v_add_f32 v243, v243, v124\n" \
"v_cvt_pk_bf16_f32 v121, v122, v123\n" \
"v_mfma_f32_16x16x32_bf16 v[12:15], a[232:235], v[216:219], v[12:15]\n" \
"v_exp_f32 v125, v125\n" \
"v_mfma_f32_16x16x32_bf16 v[12:15], a[236:239], v[220:223], v[12:15]\n" \
"ds_read_b128 a[228:231], v247 offset:28672\n" \
"v_add_f32 v243, v243, v125\n" \
"v_mfma_f32_16x16x32_bf16 v[20:23], a[232:235], a[168:171], v[20:23]\n" \
"s_xor_b64 s[52:53], s[44:45], s[42:43]\n" \
"s_xor_b64 s[54:55], s[46:47], s[42:43]\n" \
"s_or_b64 s[52:53], s[52:53], s[54:55]\n" \
"v_mfma_f32_16x16x32_bf16 v[20:23], a[236:239], a[172:175], v[20:23]\n" \
"s_xor_b64 s[54:55], s[48:49], s[42:43]\n" \
"s_or_b64 s[52:53], s[52:53], s[54:55]\n" \
"s_xor_b64 s[54:55], s[50:51], s[42:43]\n" \
"v_mfma_f32_16x16x32_bf16 v[28:31], a[232:235], a[184:187], v[28:31]\n" \
"s_or_b64 s[52:53], s[52:53], s[54:55]\n" \
"v_exp_f32 v126, v126\n" \
"v_mfma_f32_16x16x32_bf16 v[28:31], a[236:239], a[188:191], v[28:31]\n" \
"ds_read_b128 a[232:235], v247 offset:21504\n" \
"v_add_f32 v243, v243, v126\n" \
"v_mfma_f32_16x16x32_bf16 v[32:35], a[240:243], v[200:203], v[32:35]\n" \
"s_and_b64 s[52:53], s[52:53], s[8:9]\n" \
"s_cbranch_scc1 Lrs2_%=\n" \
"Lrr2_%=:\n" \
"v_mfma_f32_16x16x32_bf16 v[32:35], a[244:247], v[204:207], v[32:35]\n" \
"s_mov_b64 s[8:9], s[10:11]\n" \
"v_cvt_pk_bf16_f32 v122, v124, v125\n" \
"v_accvgpr_read_b32 v71, a31\n" \
"v_mfma_f32_16x16x32_bf16 v[40:43], a[240:243], v[216:219], v[40:43]\n" \
"v_exp_f32 v127, v127\n" \
"v_mfma_f32_16x16x32_bf16 v[40:43], a[244:247], v[220:223], v[40:43]\n" \
"ds_read_b128 a[236:239], v247 offset:29696\n" \
"v_add_f32 v243, v243, v127\n" \
"v_mfma_f32_16x16x32_bf16 v[48:51], a[240:243], a[168:171], v[48:51]\n" \
"v_mul_f32 v64, v64, v76\n" \
"v_mul_f32 v65, v65, v76\n" \
"v_mfma_f32_16x16x32_bf16 v[48:51], a[244:247], a[172:175], v[48:51]\n" \
"v_mul_f32 v66, v66, v76\n" \
"v_mul_f32 v67, v67, v76\n" \
"v_mfma_f32_16x16x32_bf16 v[56:59], a[240:243], a[184:187], v[56:59]\n" \
"v_mul_f32 v68, v68, v76\n" \
"v_mul_f32 v69, v69, v76\n" \
"v_mfma_f32_16x16x32_bf16 v[56:59], a[244:247], a[188:191], v[56:59]\n" \
"ds_read_b128 a[240:243], v247 offset:22528\n" \
"v_mul_f32 v70, v70, v76\n" \
"v_mfma_f32_16x16x32_bf16 v[36:39], a[248:251], v[200:203], v[36:39]\n" \
"v_mul_f32 v71, v71, v76\n" \
"v_fma_f32 v240, v64, v65, v240\n" \
"v_mfma_f32_16x16x32_bf16 v[36:39], a[252:255], v[204:207], v[36:39]\n" \
"v_cvt_pk_bf16_f32 v100, v64, v65\n" \
"v_fma_f32 v240, v66, v67, v240\n" \
"v_mfma_f32_16x16x32_bf16 v[44:47], a[248:251], v[216:219], v[44:47]\n" \
"v_cvt_pk_bf16_f32 v101, v66, v67\n" \
"v_fma_f32 v240, v68, v69, v240\n" \
"v_mfma_f32_16x16x32_bf16 v[44:47], a[252:255], v[220:223], v[44:47]\n" \
"ds_read_b128 a[244:247], v247 offset:30720\n" \
"v_cvt_pk_bf16_f32 v102, v68, v69\n" \
"v_mfma_f32_16x16x32_bf16 v[52:55], a[248:251], a[168:171], v[52:55]\n" \
"v_fma_f32 v240, v70, v71, v240\n" \
"v_cvt_pk_bf16_f32 v103, v70, v71\n" \
"v_mfma_f32_16x16x32_bf16 v[52:55], a[252:255], a[172:175], v[52:55]\n" \
"buffer_store_dwordx4 v[100:103], v77, s[56:59], s77 offen offset:2048 sc1\n" \
"s_add_u32 s77, s77, 0x82000\n" \
"v_mfma_f32_16x16x32_bf16 v[60:63], a[248:251], a[184:187], v[60:63]\n" \
"s_waitcnt vmcnt(8) lgkmcnt(6)\n" \
"v_mfma_f32_16x16x32_bf16 v[60:63], a[252:255], a[188:191], v[60:63]\n" \
"s_barrier\n" \
"ds_read_b128 a[248:251], v247 offset:23552\n" \
"v_mfma_f32_16x16x32_bf16 a[32:35], a[192:195], v[72:75], a[32:35]\n" \
"v_exp_f32 v0, v0\n" \
"v_mfma_f32_16x16x32_bf16 a[32:35], a[196:199], v[104:107], a[32:35]\n" \
"v_cvt_pk_bf16_f32 v123, v126, v127\n" \
"v_add_f32 v244, v244, v0\n" \
"v_exp_f32 v1, v1\n" \
"v_mfma_f32_16x16x32_bf16 v[128:131], a[192:195], v[80:83], v[128:131]\n" \
"s_xor_b32 m0, s73, 0x8000\n" \
"v_add_f32 v244, v244, v1\n" \
"v_exp_f32 v2, v2\n" \
"v_mfma_f32_16x16x32_bf16 v[128:131], a[196:199], v[112:115], v[128:131]\n" \
"ds_read_b128 a[252:255], v247 offset:31744\n" \
"global_load_lds_dwordx4 v250, s[36:37] offset:-4096 sc1\n" \
"v_add_f32 v244, v244, v2\n" \
"v_cvt_pk_bf16_f32 v0, v0, v1\n" \
"v_exp_f32 v3, v3\n" \
"v_mfma_f32_16x16x32_bf16 v[160:163], a[192:195], v[88:91], v[160:163]\n" \
"s_add_u32 s70, s82, 0x10000\n" \
"v_add_f32 v244, v244, v3\n" \
"v_exp_f32 v4, v4\n" \
"v_add_u32 v247, s70, v248\n" \
"v_mfma_f32_16x16x32_bf16 v[160:163], a[196:199], v[120:123], v[160:163]\n" \
"ds_read_b128 a[192:195], v252 offset:0\n" \
"v_add_f32 v244, v244, v4\n" \
"v_cvt_pk_bf16_f32 v1, v2, v3\n" \
"v_exp_f32 v5, v5\n" \
"v_mfma_f32_16x16x32_bf16 a[36:39], a[200:203], v[72:75], a[36:39]\n" \
"v_add_f32 v244, v244, v5\n" \
"v_exp_f32 v6, v6\n" \
"v_mfma_f32_16x16x32_bf16 a[36:39], a[204:207], v[104:107], a[36:39]\n" \
"global_load_lds_dwordx4 v250, s[36:37] offset:-3072 sc1\n" \
"v_add_f32 v244, v244, v6\n" \
"v_cvt_pk_bf16_f32 v2, v4, v5\n" \
"v_exp_f32 v7, v7\n" \
"v_mfma_f32_16x16x32_bf16 v[132:135], a[200:203], v[80:83], v[132:135]\n" \
"ds_read_b128 a[196:199], v252 offset:2048\n" \
"v_add_f32 v244, v244, v7\n" \
"v_exp_f32 v8, v8\n" \
"v_mfma_f32_16x16x32_bf16 v[132:135], a[204:207], v[112:115], v[132:135]\n" \
"v_add_f32 v245, v245, v8\n" \
"v_cvt_pk_bf16_f32 v3, v6, v7\n" \
"v_exp_f32 v9, v9\n" \
"v_mfma_f32_16x16x32_bf16 v[164:167], a[200:203], v[88:91], v[164:167]\n" \
"v_add_f32 v245, v245, v9\n" \
"v_exp_f32 v10, v10\n" \
"v_mfma_f32_16x16x32_bf16 v[164:167], a[204:207], v[120:123], v[164:167]\n" \
"ds_read_b128 a[200:203], v252 offset:256\n" \
"v_add_f32 v245, v245, v10\n" \
"v_cvt_pk_bf16_f32 v8, v8, v9\n" \
"v_exp_f32 v11, v11\n" \
"v_mfma_f32_16x16x32_bf16 a[40:43], a[208:211], v[72:75], a[40:43]\n" \
"global_load_lds_dwordx4 v250, s[36:37] offset:-2048 sc1\n" \
"v_add_f32 v245, v245, v11\n" \
"v_exp_f32 v12, v12\n" \
"v_mfma_f32_16x16x32_bf16 a[40:43], a[212:215], v[104:107], a[40:43]\n" \
"v_add_f32 v245, v245, v12\n" \
"v_cvt_pk_bf16_f32 v9, v10, v11\n" \
"v_exp_f32 v13, v13\n" \
"v_mfma_f32_16x16x32_bf16 v[136:139], a[208:211], v[80:83], v[136:139]\n" \
"ds_read_b128 a[204:207], v252 offset:2304\n" \
"v_add_f32 v245, v245, v13\n" \
"v_mfma_f32_16x16x32_bf16 v[136:139], a[212:215], v[112:115], v[136:139]\n" \
"v_exp_f32 v14, v14\n" \
"v_mfma_f32_16x16x32_bf16 v[168:171], a[208:211], v[88:91], v[168:171]\n" \
"global_load_lds_dwordx4 v250, s[36:37] offset:-1024 sc1\n" \
"v_add_f32 v245, v245, v14\n" \
"v_mfma_f32_16x16x32_bf16 v[168:171], a[212:215], v[120:123], v[168:171]\n" \
"ds_read_b128 a[208:211], v252 offset:8192\n" \
"v_cvt_pk_bf16_f32 v10, v12, v13\n" \
"v_mfma_f32_16x16x32_bf16 a[44:47], a[216:219], v[72:75], a[44:47]\n" \
"v_exp_f32 v15, v15\n" \
"v_mfma_f32_16x16x32_bf16 a[44:47], a[220:223], v[104:107], a[44:47]\n" \
"v_add_f32 v245, v245, v15\n" \
"v_exp_f32 v16, v16\n" \
"v_mfma_f32_16x16x32_bf16 v[140:143], a[216:219], v[80:83], v[140:143]\n" \
"ds_read_b128 a[212:215], v252 offset:10240\n" \
"v_add_f32 v242, v242, v16\n" \
"v_mfma_f32_16x16x32_bf16 v[140:143], a[220:223], v[112:115], v[140:143]\n" \
"global_load_lds_dwordx4 v250, s[36:37] offset:0 sc1\n" \
"v_cvt_pk_bf16_f32 v11, v14, v15\n" \
"v_mfma_f32_16x16x32_bf16 v[172:175], a[216:219], v[88:91], v[172:175]\n" \
"s_waitcnt lgkmcnt(6)\n" \
"v_exp_f32 v17, v17\n" \
"v_mfma_f32_16x16x32_bf16 v[172:175], a[220:223], v[120:123], v[172:175]\n" \
"ds_read_b128 a[216:219], v252 offset:8448\n" \
"v_add_f32 v242, v242, v17\n" \
"v_exp_f32 v18, v18\n" \
"v_mfma_f32_16x16x32_bf16 a[48:51], a[224:227], v[72:75], a[48:51]\n" \
"v_add_f32 v242, v242, v18\n" \
"v_cvt_pk_bf16_f32 v16, v16, v17\n" \
"v_mfma_f32_16x16x32_bf16 a[48:51], a[228:231], v[104:107], a[48:51]\n" \
"global_load_lds_dwordx4 v250, s[36:37] offset:1024 sc1\n" \
"v_exp_f32 v19, v19\n" \
"v_mfma_f32_16x16x32_bf16 v[144:147], a[224:227], v[80:83], v[144:147]\n" \
"ds_read_b128 a[220:223], v252 offset:10496\n" \
"v_add_f32 v242, v242, v19\n" \
"v_mfma_f32_16x16x32_bf16 v[144:147], a[228:231], v[112:115], v[144:147]\n" \
"v_exp_f32 v20, v20\n" \
"v_mfma_f32_16x16x32_bf16 v[176:179], a[224:227], v[88:91], v[176:179]\n" \
"v_add_f32 v242, v242, v20\n" \
"v_cvt_pk_bf16_f32 v17, v18, v19\n" \
"v_mfma_f32_16x16x32_bf16 v[176:179], a[228:231], v[120:123], v[176:179]\n" \
"ds_read_b128 a[224:227], v252 offset:4096\n" \
"v_exp_f32 v21, v21\n" \
"v_mfma_f32_16x16x32_bf16 a[52:55], a[232:235], v[72:75], a[52:55]\n" \
"global_load_lds_dwordx4 v250, s[36:37] offset:2048 sc1\n" \
"v_add_f32 v242, v242, v21\n" \
"v_mfma_f32_16x16x32_bf16 a[52:55], a[236:239], v[104:107], a[52:55]\n" \
"v_exp_f32 v22, v22\n" \
"v_mfma_f32_16x16x32_bf16 v[148:151], a[232:235], v[80:83], v[148:151]\n" \
"ds_read_b128 a[228:231], v252 offset:6144\n" \
"v_add_f32 v242, v242, v22\n" \
"v_cvt_pk_bf16_f32 v18, v20, v21\n" \
"v_mfma_f32_16x16x32_bf16 v[148:151], a[236:239], v[112:115], v[148:151]\n" \
"v_exp_f32 v23, v23\n" \
"v_mfma_f32_16x16x32_bf16 v[180:183], a[232:235], v[88:91], v[180:183]\n" \
"global_load_lds_dwordx4 v250, s[36:37] offset:3072 sc1\n" \
"v_add_f32 v242, v242, v23\n" \
"v_mfma_f32_16x16x32_bf16 v[180:183], a[236:239], v[120:123], v[180:183]\n" \
"s_add_u32 s36, s36, 34816\n" \
"ds_read_b128 a[232:235], v252 offset:4352\n" \
"v_exp_f32 v24, v24\n" \
"v_mfma_f32_16x16x32_bf16 a[56:59], a[240:243], v[72:75], a[56:59]\n" \
"v_add_f32 v243, v243, v24\n" \
"v_cvt_pk_bf16_f32 v19, v22, v23\n" \
"v_mfma_f32_16x16x32_bf16 a[56:59], a[244:247], v[104:107], a[56:59]\n" \
"v_exp_f32 v25, v25\n" \
"v_mfma_f32_16x16x32_bf16 v[152:155], a[240:243], v[80:83], v[152:155]\n" \
"ds_read_b128 a[236:239], v252 offset:6400\n" \
"v_add_f32 v243, v243, v25\n" \
"v_exp_f32 v26, v26\n" \
"v_mfma_f32_16x16x32_bf16 v[152:155], a[244:247], v[112:115], v[152:155]\n" \
"v_add_f32 v243, v243, v26\n" \
"v_cvt_pk_bf16_f32 v24, v24, v25\n" \
"v_mfma_f32_16x16x32_bf16 v[184:187], a[240:243], v[88:91], v[184:187]\n" \
"s_load_dwordx8 s[44:51], s[56:57], s79 glc\n" \
"v_exp_f32 v27, v27\n" \
"v_mfma_f32_16x16x32_bf16 v[184:187], a[244:247], v[120:123], v[184:187]\n" \
"ds_read_b128 a[240:243], v252 offset:12288\n" \
"v_add_f32 v243, v243, v27\n" \
"v_exp_f32 v28, v28\n" \
"v_mfma_f32_16x16x32_bf16 a[60:63], a[248:251], v[72:75], a[60:63]\n" \
"v_add_f32 v243, v243, v28\n" \
"v_cvt_pk_bf16_f32 v25, v26, v27\n" \
"v_mfma_f32_16x16x32_bf16 a[60:63], a[252:255], v[104:107], a[60:63]\n" \
"v_exp_f32 v29, v29\n" \
"v_mfma_f32_16x16x32_bf16 v[156:159], a[248:251], v[80:83], v[156:159]\n" \
"ds_read_b128 a[244:247], v252 offset:14336\n" \
"v_add_f32 v243, v243, v29\n" \
"v_exp_f32 v30, v30\n" \
"v_mfma_f32_16x16x32_bf16 v[156:159], a[252:255], v[112:115], v[156:159]\n" \
"v_add_f32 v243, v243, v30\n" \
"v_cvt_pk_bf16_f32 v26, v28, v29\n" \
"v_mfma_f32_16x16x32_bf16 v[188:191], a[248:251], v[88:91], v[188:191]\n" \
"s_waitcnt vmcnt(12) lgkmcnt(6)\n" \
"v_exp_f32 v31, v31\n" \
"v_mfma_f32_16x16x32_bf16 v[188:191], a[252:255], v[120:123], v[188:191]\n" \
"s_barrier\n" \
"ds_read_b128 a[248:251], v252 offset:12544\n" \
"v_add_f32 v243, v243, v31\n" \
"s_mov_b64 exec, 0\n" \
"s_cmp_eq_u32 s61, 0\n" \
"s_cbranch_scc1 Lst1x0_%=\n" \
"s_cmp_eq_u32 s61, 1\n" \
"s_cbranch_scc1 Lst1x1_%=\n" \
"s_cmp_eq_u32 s61, 2\n" \
"s_cbranch_scc1 Lst1x2_%=\n" \
"s_branch Lst1x3_%=\n" \
"Lcb1_%=:\n" \
"s_mov_b64 exec, -1\n" \
"s_waitcnt lgkmcnt(0)\n" \
"s_xor_b64 s[52:53], s[44:45], s[42:43]\n" \
"s_xor_b64 s[54:55], s[46:47], s[42:43]\n" \
"s_or_b64 s[52:53], s[52:53], s[54:55]\n" \
"s_xor_b64 s[54:55], s[48:49], s[42:43]\n" \
"s_or_b64 s[52:53], s[52:53], s[54:55]\n" \
"s_xor_b64 s[54:55], s[50:51], s[42:43]\n" \
"s_or_b64 s[52:53], s[52:53], s[54:55]\n" \
"s_and_b64 s[52:53], s[52:53], s[8:9]\n" \
"s_cbranch_scc1 Lrs3_%=\n" \
"Lrr3_%=:\n" \
"s_branch LA1g_%=\n" \
"Lpg7119_%=:\n" \
"s_cbranch_execz Lcb0_%=\n" \
"Lloop2_%=:\n" \
"v_mfma_f32_16x16x32_bf16 a[64:67], a[192:195], v[0:3], a[64:67]\n" \
"v_exp_f32 v64, v64\n" \
"v_mfma_f32_16x16x32_bf16 a[64:67], a[196:199], v[32:35], a[64:67]\n" \
"v_cvt_pk_bf16_f32 v59, v62, v63\n" \
"v_add_f32 v244, v244, v64\n" \
"v_mfma_f32_16x16x32_bf16 a[96:99], a[192:195], v[8:11], a[96:99]\n" \
"s_mov_b32 m0, s73\n" \
"v_exp_f32 v65, v65\n" \
"v_mfma_f32_16x16x32_bf16 a[96:99], a[196:199], v[40:43], a[96:99]\n" \
"ds_read_b128 a[252:255], v247 offset:15360\n" \
"global_load_lds_dwordx4 v250, s[36:37] offset:-4096 sc1\n" \
"v_add_f32 v244, v244, v65\n" \
"v_mfma_f32_16x16x32_bf16 v[128:131], a[192:195], v[16:19], v[128:131]\n" \
"v_exp_f32 v66, v66\n" \
"v_mfma_f32_16x16x32_bf16 v[128:131], a[196:199], v[48:51], v[128:131]\n" \
"v_add_f32 v244, v244, v66\n" \
"v_cvt_pk_bf16_f32 v64, v64, v65\n" \
"v_mfma_f32_16x16x32_bf16 v[160:163], a[192:195], v[24:27], v[160:163]\n" \
"v_exp_f32 v67, v67\n" \
"v_mfma_f32_16x16x32_bf16 v[160:163], a[196:199], v[56:59], v[160:163]\n" \
"ds_read_b128 a[192:195], v246 offset:0\n" \
"global_load_lds_dwordx4 v250, s[36:37] offset:-3072 sc1\n" \
"v_add_f32 v244, v244, v67\n" \
"v_mfma_f32_16x16x32_bf16 a[68:71], a[200:203], v[0:3], a[68:71]\n" \
"v_exp_f32 v68, v68\n" \
"v_mfma_f32_16x16x32_bf16 a[68:71], a[204:207], v[32:35], a[68:71]\n" \
"v_add_f32 v244, v244, v68\n" \
"v_cvt_pk_bf16_f32 v65, v66, v67\n" \
"v_mfma_f32_16x16x32_bf16 a[100:103], a[200:203], v[8:11], a[100:103]\n" \
"v_exp_f32 v69, v69\n" \
"v_mfma_f32_16x16x32_bf16 a[100:103], a[204:207], v[40:43], a[100:103]\n" \
"ds_read_b128 a[196:199], v246 offset:2048\n" \
"v_add_f32 v244, v244, v69\n" \
"v_mfma_f32_16x16x32_bf16 v[132:135], a[200:203], v[16:19], v[132:135]\n" \
"v_exp_f32 v70, v70\n" \
"v_mfma_f32_16x16x32_bf16 v[132:135], a[204:207], v[48:51], v[132:135]\n" \
"v_add_f32 v244, v244, v70\n" \
"v_cvt_pk_bf16_f32 v66, v68, v69\n" \
"v_mfma_f32_16x16x32_bf16 v[164:167], a[200:203], v[24:27], v[164:167]\n" \
"v_exp_f32 v71, v71\n" \
"v_mfma_f32_16x16x32_bf16 v[164:167], a[204:207], v[56:59], v[164:167]\n" \
"ds_read_b128 a[200:203], v246 offset:256\n" \
"global_load_lds_dwordx4 v250, s[36:37] offset:-2048 sc1\n" \
"v_add_f32 v244, v244, v71\n" \
"v_mfma_f32_16x16x32_bf16 a[72:75], a[208:211], v[0:3], a[72:75]\n" \
"v_exp_f32 v72, v72\n" \
"v_mfma_f32_16x16x32_bf16 a[72:75], a[212:215], v[32:35], a[72:75]\n" \
"v_add_f32 v245, v245, v72\n" \
"v_cvt_pk_bf16_f32 v67, v70, v71\n" \
"v_mfma_f32_16x16x32_bf16 a[104:107], a[208:211], v[8:11], a[104:107]\n" \
"v_exp_f32 v73, v73\n" \
"v_mfma_f32_16x16x32_bf16 a[104:107], a[212:215], v[40:43], a[104:107]\n" \
"ds_read_b128 a[204:207], v246 offset:2304\n" \
"global_load_lds_dwordx4 v250, s[36:37] offset:-1024 sc1\n" \
"v_add_f32 v245, v245, v73\n" \
"v_mfma_f32_16x16x32_bf16 v[136:139], a[208:211], v[16:19], v[136:139]\n" \
"v_exp_f32 v74, v74\n" \
"v_mfma_f32_16x16x32_bf16 v[136:139], a[212:215], v[48:51], v[136:139]\n" \
"v_add_f32 v245, v245, v74\n" \
"v_cvt_pk_bf16_f32 v72, v72, v73\n" \
"v_mfma_f32_16x16x32_bf16 v[168:171], a[208:211], v[24:27], v[168:171]\n" \
"v_exp_f32 v75, v75\n" \
"v_mfma_f32_16x16x32_bf16 v[168:171], a[212:215], v[56:59], v[168:171]\n" \
"ds_read_b128 a[208:211], v246 offset:8192\n" \
"v_add_f32 v245, v245, v75\n" \
"v_mfma_f32_16x16x32_bf16 a[76:79], a[216:219], v[0:3], a[76:79]\n" \
"v_exp_f32 v76, v76\n" \
"v_mfma_f32_16x16x32_bf16 a[76:79], a[220:223], v[32:35], a[76:79]\n" \
"v_add_f32 v245, v245, v76\n" \
"v_cvt_pk_bf16_f32 v73, v74, v75\n" \
"v_mfma_f32_16x16x32_bf16 a[108:111], a[216:219], v[8:11], a[108:111]\n" \
"v_exp_f32 v77, v77\n" \
"v_mfma_f32_16x16x32_bf16 a[108:111], a[220:223], v[40:43], a[108:111]\n" \
"ds_read_b128 a[212:215], v246 offset:10240\n" \
"global_load_lds_dwordx4 v250, s[36:37] offset:0 sc1\n" \
"v_add_f32 v245, v245, v77\n" \
"v_mfma_f32_16x16x32_bf16 v[140:143], a[216:219], v[16:19], v[140:143]\n" \
"v_exp_f32 v78, v78\n" \
"v_mfma_f32_16x16x32_bf16 v[140:143], a[220:223], v[48:51], v[140:143]\n" \
"v_add_f32 v245, v245, v78\n" \
"v_cvt_pk_bf16_f32 v74, v76, v77\n" \
"v_mfma_f32_16x16x32_bf16 v[172:175], a[216:219], v[24:27], v[172:175]\n" \
"s_waitcnt lgkmcnt(6)\n" \
"v_exp_f32 v79, v79\n" \
"v_mfma_f32_16x16x32_bf16 v[172:175], a[220:223], v[56:59], v[172:175]\n" \
"ds_read_b128 a[216:219], v246 offset:8448\n" \
"global_load_lds_dwordx4 v250, s[36:37] offset:1024 sc1\n" \
"v_add_f32 v245, v245, v79\n" \
"v_mfma_f32_16x16x32_bf16 a[80:83], a[224:227], v[0:3], a[80:83]\n" \
"v_exp_f32 v80, v80\n" \
"v_mfma_f32_16x16x32_bf16 a[80:83], a[228:231], v[32:35], a[80:83]\n" \
"v_add_f32 v242, v242, v80\n" \
"v_cvt_pk_bf16_f32 v75, v78, v79\n" \
"v_mfma_f32_16x16x32_bf16 a[112:115], a[224:227], v[8:11], a[112:115]\n" \
"v_exp_f32 v81, v81\n" \
"v_mfma_f32_16x16x32_bf16 a[112:115], a[228:231], v[40:43], a[112:115]\n" \
"ds_read_b128 a[220:223], v246 offset:10496\n" \
"v_add_f32 v242, v242, v81\n" \
"v_mfma_f32_16x16x32_bf16 v[144:147], a[224:227], v[16:19], v[144:147]\n" \
"v_exp_f32 v82, v82\n" \
"v_mfma_f32_16x16x32_bf16 v[144:147], a[228:231], v[48:51], v[144:147]\n" \
"v_add_f32 v242, v242, v82\n" \
"v_cvt_pk_bf16_f32 v80, v80, v81\n" \
"v_mfma_f32_16x16x32_bf16 v[176:179], a[224:227], v[24:27], v[176:179]\n" \
"v_exp_f32 v83, v83\n" \
"v_mfma_f32_16x16x32_bf16 v[176:179], a[228:231], v[56:59], v[176:179]\n" \
"ds_read_b128 a[224:227], v246 offset:4096\n" \
"global_load_lds_dwordx4 v250, s[36:37] offset:2048 sc1\n" \
"v_add_f32 v242, v242, v83\n" \
"v_mfma_f32_16x16x32_bf16 a[84:87], a[232:235], v[0:3], a[84:87]\n" \
"v_exp_f32 v84, v84\n" \
"v_mfma_f32_16x16x32_bf16 a[84:87], a[236:239], v[32:35], a[84:87]\n" \
"v_add_f32 v242, v242, v84\n" \
"v_cvt_pk_bf16_f32 v81, v82, v83\n" \
"v_mfma_f32_16x16x32_bf16 a[116:119], a[232:235], v[8:11], a[116:119]\n" \
"v_exp_f32 v85, v85\n" \
"v_mfma_f32_16x16x32_bf16 a[116:119], a[236:239], v[40:43], a[116:119]\n" \
"ds_read_b128 a[228:231], v246 offset:6144\n" \
"global_load_lds_dwordx4 v250, s[36:37] offset:3072 sc1\n" \
"v_add_f32 v242, v242, v85\n" \
"v_mfma_f32_16x16x32_bf16 v[148:151], a[232:235], v[16:19], v[148:151]\n" \
"s_add_u32 s36, s36, 34816\n" \
"v_exp_f32 v86, v86\n" \
"v_mfma_f32_16x16x32_bf16 v[148:151], a[236:239], v[48:51], v[148:151]\n" \
"v_add_f32 v242, v242, v86\n" \
"v_cvt_pk_bf16_f32 v82, v84, v85\n" \
"v_mfma_f32_16x16x32_bf16 v[180:183], a[232:235], v[24:27], v[180:183]\n" \
"v_exp_f32 v87, v87\n" \
"v_mfma_f32_16x16x32_bf16 v[180:183], a[236:239], v[56:59], v[180:183]\n" \
"ds_read_b128 a[232:235], v246 offset:4352\n" \
"v_add_f32 v242, v242, v87\n" \
"v_mfma_f32_16x16x32_bf16 a[88:91], a[240:243], v[0:3], a[88:91]\n" \
"v_exp_f32 v88, v88\n" \
"v_mfma_f32_16x16x32_bf16 a[88:91], a[244:247], v[32:35], a[88:91]\n" \
"v_add_f32 v243, v243, v88\n" \
"v_cvt_pk_bf16_f32 v83, v86, v87\n" \
"v_mfma_f32_16x16x32_bf16 a[120:123], a[240:243], v[8:11], a[120:123]\n" \
"v_exp_f32 v89, v89\n" \
"v_mfma_f32_16x16x32_bf16 a[120:123], a[244:247], v[40:43], a[120:123]\n" \
"ds_read_b128 a[236:239], v246 offset:6400\n" \
"v_add_f32 v243, v243, v89\n" \
"v_mfma_f32_16x16x32_bf16 v[152:155], a[240:243], v[16:19], v[152:155]\n" \
"v_exp_f32 v90, v90\n" \
"v_mfma_f32_16x16x32_bf16 v[152:155], a[244:247], v[48:51], v[152:155]\n" \
"v_add_f32 v243, v243, v90\n" \
"v_cvt_pk_bf16_f32 v88, v88, v89\n" \
"v_mfma_f32_16x16x32_bf16 v[184:187], a[240:243], v[24:27], v[184:187]\n" \
"v_exp_f32 v91, v91\n" \
"v_mfma_f32_16x16x32_bf16 v[184:187], a[244:247], v[56:59], v[184:187]\n" \
"ds_read_b128 a[240:243], v246 offset:12288\n" \
"v_add_f32 v243, v243, v91\n" \
"v_mfma_f32_16x16x32_bf16 a[92:95], a[248:251], v[0:3], a[92:95]\n" \
"v_exp_f32 v92, v92\n" \
"v_mfma_f32_16x16x32_bf16 a[92:95], a[252:255], v[32:35], a[92:95]\n" \
"v_add_f32 v243, v243, v92\n" \
"v_cvt_pk_bf16_f32 v89, v90, v91\n" \
"v_mfma_f32_16x16x32_bf16 a[124:127], a[248:251], v[8:11], a[124:127]\n" \
"v_exp_f32 v93, v93\n" \
"v_mfma_f32_16x16x32_bf16 a[124:127], a[252:255], v[40:43], a[124:127]\n" \
"ds_read_b128 a[244:247], v246 offset:14336\n" \
"v_add_f32 v243, v243, v93\n" \
"v_mfma_f32_16x16x32_bf16 v[156:159], a[248:251], v[16:19], v[156:159]\n" \
"v_exp_f32 v94, v94\n" \
"v_mfma_f32_16x16x32_bf16 v[156:159], a[252:255], v[48:51], v[156:159]\n" \
"v_add_f32 v243, v243, v94\n" \
"v_cvt_pk_bf16_f32 v90, v92, v93\n" \
"v_mfma_f32_16x16x32_bf16 v[188:191], a[248:251], v[24:27], v[188:191]\n" \
"s_waitcnt vmcnt(8) lgkmcnt(6)\n" \
"v_exp_f32 v95, v95\n" \
"v_mfma_f32_16x16x32_bf16 v[188:191], a[252:255], v[56:59], v[188:191]\n" \
"s_barrier\n" \
"ds_read_b128 a[248:251], v246 offset:12544\n" \
"v_add_f32 v243, v243, v95\n" \
"v_mfma_f32_16x16x32_bf16 v[0:3], a[192:195], v[192:195], v[224:227]\n" \
"v_exp_f32 v96, v96\n" \
"v_mfma_f32_16x16x32_bf16 v[0:3], a[196:199], v[196:199], v[0:3]\n" \
"v_cvt_pk_bf16_f32 v91, v94, v95\n" \
"v_add_f32 v244, v244, v96\n" \
"v_mfma_f32_16x16x32_bf16 v[8:11], a[192:195], v[208:211], v[228:231]\n" \
"s_mov_b32 m0, s74\n" \
"v_exp_f32 v97, v97\n" \
"v_mfma_f32_16x16x32_bf16 v[8:11], a[196:199], v[212:215], v[8:11]\n" \
"ds_read_b128 a[252:255], v246 offset:14592\n" \
"global_load_lds_dwordx4 v250, s[38:39] offset:-4096 sc1\n" \
"v_add_f32 v244, v244, v97\n" \
"v_mfma_f32_16x16x32_bf16 v[16:19], a[192:195], a[160:163], v[232:235]\n" \
"v_exp_f32 v98, v98\n" \
"v_mfma_f32_16x16x32_bf16 v[16:19], a[196:199], a[164:167], v[16:19]\n" \
"v_add_f32 v244, v244, v98\n" \
"v_cvt_pk_bf16_f32 v96, v96, v97\n" \
"v_mfma_f32_16x16x32_bf16 v[24:27], a[192:195], a[176:179], v[236:239]\n" \
"v_exp_f32 v99, v99\n" \
"v_mfma_f32_16x16x32_bf16 v[24:27], a[196:199], a[180:183], v[24:27]\n" \
"ds_read_b128 a[192:195], v247 offset:32768\n" \
"global_load_lds_dwordx4 v250, s[38:39] offset:-3072 sc1\n" \
"v_add_f32 v244, v244, v99\n" \
"v_mfma_f32_16x16x32_bf16 v[4:7], a[200:203], v[192:195], v[224:227]\n" \
"v_exp_f32 v100, v100\n" \
"v_mfma_f32_16x16x32_bf16 v[4:7], a[204:207], v[196:199], v[4:7]\n" \
"v_add_f32 v244, v244, v100\n" \
"v_cvt_pk_bf16_f32 v97, v98, v99\n" \
"v_mfma_f32_16x16x32_bf16 v[12:15], a[200:203], v[208:211], v[228:231]\n" \
"v_exp_f32 v101, v101\n" \
"v_mfma_f32_16x16x32_bf16 v[12:15], a[204:207], v[212:215], v[12:15]\n" \
"ds_read_b128 a[196:199], v247 offset:40960\n" \
"v_add_f32 v244, v244, v101\n" \
"v_mfma_f32_16x16x32_bf16 v[20:23], a[200:203], a[160:163], v[232:235]\n" \
"v_exp_f32 v102, v102\n" \
"v_mfma_f32_16x16x32_bf16 v[20:23], a[204:207], a[164:167], v[20:23]\n" \
"v_add_f32 v244, v244, v102\n" \
"v_cvt_pk_bf16_f32 v98, v100, v101\n" \
"v_mfma_f32_16x16x32_bf16 v[28:31], a[200:203], a[176:179], v[236:239]\n" \
"v_exp_f32 v103, v103\n" \
"v_mfma_f32_16x16x32_bf16 v[28:31], a[204:207], a[180:183], v[28:31]\n" \
"ds_read_b128 a[200:203], v247 offset:33792\n" \
"global_load_lds_dwordx4 v250, s[38:39] offset:-2048 sc1\n" \
"v_add_f32 v244, v244, v103\n" \
"v_mfma_f32_16x16x32_bf16 v[32:35], a[208:211], v[192:195], v[224:227]\n" \
"v_exp_f32 v104, v104\n" \
"v_mfma_f32_16x16x32_bf16 v[32:35], a[212:215], v[196:199], v[32:35]\n" \
"v_add_f32 v245, v245, v104\n" \
"v_cvt_pk_bf16_f32 v99, v102, v103\n" \
"v_mfma_f32_16x16x32_bf16 v[40:43], a[208:211], v[208:211], v[228:231]\n" \
"v_exp_f32 v105, v105\n" \
"v_mfma_f32_16x16x32_bf16 v[40:43], a[212:215], v[212:215], v[40:43]\n" \
"ds_read_b128 a[204:207], v247 offset:41984\n" \
"global_load_lds_dwordx4 v250, s[38:39] offset:-1024 sc1\n" \
"v_add_f32 v245, v245, v105\n" \
"v_mfma_f32_16x16x32_bf16 v[48:51], a[208:211], a[160:163], v[232:235]\n" \
"v_exp_f32 v106, v106\n" \
"v_mfma_f32_16x16x32_bf16 v[48:51], a[212:215], a[164:167], v[48:51]\n" \
"v_add_f32 v245, v245, v106\n" \
"v_cvt_pk_bf16_f32 v104, v104, v105\n" \
"v_mfma_f32_16x16x32_bf16 v[56:59], a[208:211], a[176:179], v[236:239]\n" \
"v_exp_f32 v107, v107\n" \
"v_mfma_f32_16x16x32_bf16 v[56:59], a[212:215], a[180:183], v[56:59]\n" \
"ds_read_b128 a[208:211], v247 offset:34816\n" \
"v_add_f32 v245, v245, v107\n" \
"v_mfma_f32_16x16x32_bf16 v[36:39], a[216:219], v[192:195], v[224:227]\n" \
"v_exp_f32 v108, v108\n" \
"v_mfma_f32_16x16x32_bf16 v[36:39], a[220:223], v[196:199], v[36:39]\n" \
"v_add_f32 v245, v245, v108\n" \
"v_cvt_pk_bf16_f32 v105, v106, v107\n" \
"v_mfma_f32_16x16x32_bf16 v[44:47], a[216:219], v[208:211], v[228:231]\n" \
"v_exp_f32 v109, v109\n" \
"v_mfma_f32_16x16x32_bf16 v[44:47], a[220:223], v[212:215], v[44:47]\n" \
"ds_read_b128 a[212:215], v247 offset:43008\n" \
"global_load_lds_dwordx4 v250, s[38:39] offset:0 sc1\n" \
"v_add_f32 v245, v245, v109\n" \
"v_mfma_f32_16x16x32_bf16 v[52:55], a[216:219], a[160:163], v[232:235]\n" \
"v_exp_f32 v110, v110\n" \
"v_mfma_f32_16x16x32_bf16 v[52:55], a[220:223], a[164:167], v[52:55]\n" \
"v_add_f32 v245, v245, v110\n" \
"v_cvt_pk_bf16_f32 v106, v108, v109\n" \
"v_mfma_f32_16x16x32_bf16 v[60:63], a[216:219], a[176:179], v[236:239]\n" \
"s_waitcnt lgkmcnt(6)\n" \
"v_exp_f32 v111, v111\n" \
"v_mfma_f32_16x16x32_bf16 v[60:63], a[220:223], a[180:183], v[60:63]\n" \
"ds_read_b128 a[216:219], v247 offset:35840\n" \
"global_load_lds_dwordx4 v250, s[38:39] offset:1024 sc1\n" \
"v_add_f32 v245, v245, v111\n" \
"v_mfma_f32_16x16x32_bf16 v[0:3], a[224:227], v[200:203], v[0:3]\n" \
"v_exp_f32 v112, v112\n" \
"v_mfma_f32_16x16x32_bf16 v[0:3], a[228:231], v[204:207], v[0:3]\n" \
"v_add_f32 v242, v242, v112\n" \
"v_cvt_pk_bf16_f32 v107, v110, v111\n" \
"v_mfma_f32_16x16x32_bf16 v[8:11], a[224:227], v[216:219], v[8:11]\n" \
"v_exp_f32 v113, v113\n" \
"v_mfma_f32_16x16x32_bf16 v[8:11], a[228:231], v[220:223], v[8:11]\n" \
"ds_read_b128 a[220:223], v247 offset:44032\n" \
"v_add_f32 v242, v242, v113\n" \
"v_mfma_f32_16x16x32_bf16 v[16:19], a[224:227], a[168:171], v[16:19]\n" \
"v_exp_f32 v114, v114\n" \
"v_mfma_f32_16x16x32_bf16 v[16:19], a[228:231], a[172:175], v[16:19]\n" \
"v_add_f32 v242, v242, v114\n" \
"v_cvt_pk_bf16_f32 v112, v112, v113\n" \
"v_mfma_f32_16x16x32_bf16 v[24:27], a[224:227], a[184:187], v[24:27]\n" \
"v_exp_f32 v115, v115\n" \
"v_mfma_f32_16x16x32_bf16 v[24:27], a[228:231], a[188:191], v[24:27]\n" \
"ds_read_b128 a[224:227], v247 offset:36864\n" \
"global_load_lds_dwordx4 v250, s[38:39] offset:2048 sc1\n" \
"v_add_f32 v242, v242, v115\n" \
"v_mfma_f32_16x16x32_bf16 v[4:7], a[232:235], v[200:203], v[4:7]\n" \
"v_exp_f32 v116, v116\n" \
"v_mfma_f32_16x16x32_bf16 v[4:7], a[236:239], v[204:207], v[4:7]\n" \
"v_add_f32 v242, v242, v116\n" \
"v_cvt_pk_bf16_f32 v113, v114, v115\n" \
"v_mfma_f32_16x16x32_bf16 v[12:15], a[232:235], v[216:219], v[12:15]\n" \
"v_exp_f32 v117, v117\n" \
"v_mfma_f32_16x16x32_bf16 v[12:15], a[236:239], v[220:223], v[12:15]\n" \
"ds_read_b128 a[228:231], v247 offset:45056\n" \
"global_load_lds_dwordx4 v250, s[38:39] offset:3072 sc1\n" \
"v_add_f32 v242, v242, v117\n" \
"v_mfma_f32_16x16x32_bf16 v[20:23], a[232:235], a[168:171], v[20:23]\n" \
"s_add_u32 s38, s38, 34816\n" \
"v_exp_f32 v118, v118\n" \
"v_mfma_f32_16x16x32_bf16 v[20:23], a[236:239], a[172:175], v[20:23]\n" \
"v_add_f32 v242, v242, v118\n" \
"v_cvt_pk_bf16_f32 v114, v116, v117\n" \
"v_mfma_f32_16x16x32_bf16 v[28:31], a[232:235], a[184:187], v[28:31]\n" \
"v_exp_f32 v119, v119\n" \
"v_mfma_f32_16x16x32_bf16 v[28:31], a[236:239], a[188:191], v[28:31]\n" \
"ds_read_b128 a[232:235], v247 offset:37888\n" \
"v_add_f32 v242, v242, v119\n" \
"v_mfma_f32_16x16x32_bf16 v[32:35], a[240:243], v[200:203], v[32:35]\n" \
"v_exp_f32 v120, v120\n" \
"v_mfma_f32_16x16x32_bf16 v[32:35], a[244:247], v[204:207], v[32:35]\n" \
"v_add_f32 v243, v243, v120\n" \
"v_cvt_pk_bf16_f32 v115, v118, v119\n" \
"v_mfma_f32_16x16x32_bf16 v[40:43], a[240:243], v[216:219], v[40:43]\n" \
"v_exp_f32 v121, v121\n" \
"v_mfma_f32_16x16x32_bf16 v[40:43], a[244:247], v[220:223], v[40:43]\n" \
"ds_read_b128 a[236:239], v247 offset:46080\n" \
"v_add_f32 v243, v243, v121\n" \
"v_mfma_f32_16x16x32_bf16 v[48:51], a[240:243], a[168:171], v[48:51]\n" \
"v_exp_f32 v122, v122\n" \
"v_mfma_f32_16x16x32_bf16 v[48:51], a[244:247], a[172:175], v[48:51]\n" \
"v_add_f32 v243, v243, v122\n" \
"v_cvt_pk_bf16_f32 v120, v120, v121\n" \
"v_mfma_f32_16x16x32_bf16 v[56:59], a[240:243], a[184:187], v[56:59]\n" \
"v_exp_f32 v123, v123\n" \
"v_mfma_f32_16x16x32_bf16 v[56:59], a[244:247], a[188:191], v[56:59]\n" \
"ds_read_b128 a[240:243], v247 offset:38912\n" \
"v_add_f32 v243, v243, v123\n" \
"v_mfma_f32_16x16x32_bf16 v[36:39], a[248:251], v[200:203], v[36:39]\n" \
"v_exp_f32 v124, v124\n" \
"v_mfma_f32_16x16x32_bf16 v[36:39], a[252:255], v[204:207], v[36:39]\n" \
"v_add_f32 v243, v243, v124\n" \
"v_cvt_pk_bf16_f32 v121, v122, v123\n" \
"v_mfma_f32_16x16x32_bf16 v[44:47], a[248:251], v[216:219], v[44:47]\n" \
"v_exp_f32 v125, v125\n" \
"v_mfma_f32_16x16x32_bf16 v[44:47], a[252:255], v[220:223], v[44:47]\n" \
"ds_read_b128 a[244:247], v247 offset:47104\n" \
"v_add_f32 v243, v243, v125\n" \
"v_mfma_f32_16x16x32_bf16 v[52:55], a[248:251], a[168:171], v[52:55]\n" \
"v_exp_f32 v126, v126\n" \
"v_mfma_f32_16x16x32_bf16 v[52:55], a[252:255], a[172:175], v[52:55]\n" \
"v_add_f32 v243, v243, v126\n" \
"v_cvt_pk_bf16_f32 v122, v124, v125\n" \
"v_mfma_f32_16x16x32_bf16 v[60:63], a[248:251], a[184:187], v[60:63]\n" \
"Lpg7461_%=:\n" \
"Lst1x0_%=:\n" \
"s_cbranch_execz Lpg9930_%=\n" \
"s_waitcnt vmcnt(8) lgkmcnt(6)\n" \
"v_exp_f32 v127, v127\n" \
"v_mfma_f32_16x16x32_bf16 v[60:63], a[252:255], a[188:191], v[60:63]\n" \
"s_barrier\n" \
"ds_read_b128 a[248:251], v247 offset:39936\n" \
"v_add_f32 v243, v243, v127\n" \
"v_mfma_f32_16x16x32_bf16 a[64:67], a[192:195], v[64:67], a[64:67]\n" \
"v_exp_f32 v0, v0\n" \
"v_mfma_f32_16x16x32_bf16 a[64:67], a[196:199], v[96:99], a[64:67]\n" \
"v_cvt_pk_bf16_f32 v123, v126, v127\n" \
"v_add_f32 v244, v244, v0\n" \
"v_mfma_f32_16x16x32_bf16 a[96:99], a[192:195], v[72:75], a[96:99]\n" \
"s_xor_b32 m0, s73, 0x8000\n" \
"v_exp_f32 v1, v1\n" \
"v_mfma_f32_16x16x32_bf16 a[96:99], a[196:199], v[104:107], a[96:99]\n" \
"ds_read_b128 a[252:255], v247 offset:48128\n" \
"global_load_lds_dwordx4 v250, s[36:37] offset:-4096 sc1\n" \
"v_add_f32 v244, v244, v1\n" \
"v_mfma_f32_16x16x32_bf16 v[128:131], a[192:195], v[80:83], v[128:131]\n" \
"v_exp_f32 v2, v2\n" \
"v_mfma_f32_16x16x32_bf16 v[128:131], a[196:199], v[112:115], v[128:131]\n" \
"v_add_f32 v244, v244, v2\n" \
"v_cvt_pk_bf16_f32 v0, v0, v1\n" \
"v_mfma_f32_16x16x32_bf16 v[160:163], a[192:195], v[88:91], v[160:163]\n" \
"v_exp_f32 v3, v3\n" \
"v_mfma_f32_16x16x32_bf16 v[160:163], a[196:199], v[120:123], v[160:163]\n" \
"ds_read_b128 a[192:195], v252 offset:0\n" \
"global_load_lds_dwordx4 v250, s[36:37] offset:-3072 sc1\n" \
"v_add_f32 v244, v244, v3\n" \
"v_mfma_f32_16x16x32_bf16 a[68:71], a[200:203], v[64:67], a[68:71]\n" \
"v_exp_f32 v4, v4\n" \
"v_mfma_f32_16x16x32_bf16 a[68:71], a[204:207], v[96:99], a[68:71]\n" \
"v_add_f32 v244, v244, v4\n" \
"v_cvt_pk_bf16_f32 v1, v2, v3\n" \
"v_mfma_f32_16x16x32_bf16 a[100:103], a[200:203], v[72:75], a[100:103]\n" \
"v_exp_f32 v5, v5\n" \
"v_mfma_f32_16x16x32_bf16 a[100:103], a[204:207], v[104:107], a[100:103]\n" \
"ds_read_b128 a[196:199], v252 offset:2048\n" \
"v_add_f32 v244, v244, v5\n" \
"v_mfma_f32_16x16x32_bf16 v[132:135], a[200:203], v[80:83], v[132:135]\n" \
"v_exp_f32 v6, v6\n" \
"v_mfma_f32_16x16x32_bf16 v[132:135], a[204:207], v[112:115], v[132:135]\n" \
"v_add_f32 v244, v244, v6\n" \
"v_cvt_pk_bf16_f32 v2, v4, v5\n" \
"v_mfma_f32_16x16x32_bf16 v[164:167], a[200:203], v[88:91], v[164:167]\n" \
"v_exp_f32 v7, v7\n" \
"v_mfma_f32_16x16x32_bf16 v[164:167], a[204:207], v[120:123], v[164:167]\n" \
"ds_read_b128 a[200:203], v252 offset:256\n" \
"global_load_lds_dwordx4 v250, s[36:37] offset:-2048 sc1\n" \
"v_add_f32 v244, v244, v7\n" \
"v_mfma_f32_16x16x32_bf16 a[72:75], a[208:211], v[64:67], a[72:75]\n" \
"v_exp_f32 v8, v8\n" \
"v_mfma_f32_16x16x32_bf16 a[72:75], a[212:215], v[96:99], a[72:75]\n" \
"v_add_f32 v245, v245, v8\n" \
"v_cvt_pk_bf16_f32 v3, v6, v7\n" \
"v_mfma_f32_16x16x32_bf16 a[104:107], a[208:211], v[72:75], a[104:107]\n" \
"v_exp_f32 v9, v9\n" \
"v_mfma_f32_16x16x32_bf16 a[104:107], a[212:215], v[104:107], a[104:107]\n" \
"ds_read_b128 a[204:207], v252 offset:2304\n" \
"global_load_lds_dwordx4 v250, s[36:37] offset:-1024 sc1\n" \
"v_add_f32 v245, v245, v9\n" \
"v_mfma_f32_16x16x32_bf16 v[136:139], a[208:211], v[80:83], v[136:139]\n" \
"v_exp_f32 v10, v10\n" \
"v_mfma_f32_16x16x32_bf16 v[136:139], a[212:215], v[112:115], v[136:139]\n" \
"v_add_f32 v245, v245, v10\n" \
"v_cvt_pk_bf16_f32 v8, v8, v9\n" \
"v_mfma_f32_16x16x32_bf16 v[168:171], a[208:211], v[88:91], v[168:171]\n" \
"v_exp_f32 v11, v11\n" \
"v_mfma_f32_16x16x32_bf16 v[168:171], a[212:215], v[120:123], v[168:171]\n" \
"ds_read_b128 a[208:211], v252 offset:8192\n" \
"v_add_f32 v245, v245, v11\n" \
"v_mfma_f32_16x16x32_bf16 a[76:79], a[216:219], v[64:67], a[76:79]\n" \
"v_exp_f32 v12, v12\n" \
"v_mfma_f32_16x16x32_bf16 a[76:79], a[220:223], v[96:99], a[76:79]\n" \
"v_add_f32 v245, v245, v12\n" \
"v_cvt_pk_bf16_f32 v9, v10, v11\n" \
"v_mfma_f32_16x16x32_bf16 a[108:111], a[216:219], v[72:75], a[108:111]\n" \
"v_exp_f32 v13, v13\n" \
"v_mfma_f32_16x16x32_bf16 a[108:111], a[220:223], v[104:107], a[108:111]\n" \
"ds_read_b128 a[212:215], v252 offset:10240\n" \
"global_load_lds_dwordx4 v250, s[36:37] offset:0 sc1\n" \
"v_add_f32 v245, v245, v13\n" \
"v_mfma_f32_16x16x32_bf16 v[140:143], a[216:219], v[80:83], v[140:143]\n" \
"v_exp_f32 v14, v14\n" \
"v_mfma_f32_16x16x32_bf16 v[140:143], a[220:223], v[112:115], v[140:143]\n" \
"v_add_f32 v245, v245, v14\n" \
"v_cvt_pk_bf16_f32 v10, v12, v13\n" \
"v_mfma_f32_16x16x32_bf16 v[172:175], a[216:219], v[88:91], v[172:175]\n" \
"s_waitcnt lgkmcnt(6)\n" \
"v_exp_f32 v15, v15\n" \
"v_mfma_f32_16x16x32_bf16 v[172:175], a[220:223], v[120:123], v[172:175]\n" \
"ds_read_b128 a[216:219], v252 offset:8448\n" \
"global_load_lds_dwordx4 v250, s[36:37] offset:1024 sc1\n" \
"v_add_f32 v245, v245, v15\n" \
"v_mfma_f32_16x16x32_bf16 a[80:83], a[224:227], v[64:67], a[80:83]\n" \
"v_exp_f32 v16, v16\n" \
"v_mfma_f32_16x16x32_bf16 a[80:83], a[228:231], v[96:99], a[80:83]\n" \
"v_add_f32 v242, v242, v16\n" \
"v_cvt_pk_bf16_f32 v11, v14, v15\n" \
"v_mfma_f32_16x16x32_bf16 a[112:115], a[224:227], v[72:75], a[112:115]\n" \
"v_exp_f32 v17, v17\n" \
"v_mfma_f32_16x16x32_bf16 a[112:115], a[228:231], v[104:107], a[112:115]\n" \
"ds_read_b128 a[220:223], v252 offset:10496\n" \
"v_add_f32 v242, v242, v17\n" \
"v_mfma_f32_16x16x32_bf16 v[144:147], a[224:227], v[80:83], v[144:147]\n" \
"v_exp_f32 v18, v18\n" \
"v_mfma_f32_16x16x32_bf16 v[144:147], a[228:231], v[112:115], v[144:147]\n" \
"v_add_f32 v242, v242, v18\n" \
"v_cvt_pk_bf16_f32 v16, v16, v17\n" \
"v_mfma_f32_16x16x32_bf16 v[176:179], a[224:227], v[88:91], v[176:179]\n" \
"v_exp_f32 v19, v19\n" \
"v_mfma_f32_16x16x32_bf16 v[176:179], a[228:231], v[120:123], v[176:179]\n" \
"ds_read_b128 a[224:227], v252 offset:4096\n" \
"global_load_lds_dwordx4 v250, s[36:37] offset:2048 sc1\n" \
"v_add_f32 v242, v242, v19\n" \
"v_mfma_f32_16x16x32_bf16 a[84:87], a[232:235], v[64:67], a[84:87]\n" \
"v_exp_f32 v20, v20\n" \
"v_mfma_f32_16x16x32_bf16 a[84:87], a[236:239], v[96:99], a[84:87]\n" \
"v_add_f32 v242, v242, v20\n" \
"v_cvt_pk_bf16_f32 v17, v18, v19\n" \
"v_mfma_f32_16x16x32_bf16 a[116:119], a[232:235], v[72:75], a[116:119]\n" \
"v_exp_f32 v21, v21\n" \
"v_mfma_f32_16x16x32_bf16 a[116:119], a[236:239], v[104:107], a[116:119]\n" \
"ds_read_b128 a[228:231], v252 offset:6144\n" \
"global_load_lds_dwordx4 v250, s[36:37] offset:3072 sc1\n" \
"v_add_f32 v242, v242, v21\n" \
"v_mfma_f32_16x16x32_bf16 v[148:151], a[232:235], v[80:83], v[148:151]\n" \
"s_add_u32 s36, s36, 34816\n" \
"v_exp_f32 v22, v22\n" \
"v_mfma_f32_16x16x32_bf16 v[148:151], a[236:239], v[112:115], v[148:151]\n" \
"v_add_f32 v242, v242, v22\n" \
"v_cvt_pk_bf16_f32 v18, v20, v21\n" \
"v_mfma_f32_16x16x32_bf16 v[180:183], a[232:235], v[88:91], v[180:183]\n" \
"v_exp_f32 v23, v23\n" \
"v_mfma_f32_16x16x32_bf16 v[180:183], a[236:239], v[120:123], v[180:183]\n" \
"ds_read_b128 a[232:235], v252 offset:4352\n" \
"v_add_f32 v242, v242, v23\n" \
"v_mfma_f32_16x16x32_bf16 a[88:91], a[240:243], v[64:67], a[88:91]\n" \
"v_exp_f32 v24, v24\n" \
"v_mfma_f32_16x16x32_bf16 a[88:91], a[244:247], v[96:99], a[88:91]\n" \
"v_add_f32 v243, v243, v24\n" \
"v_cvt_pk_bf16_f32 v19, v22, v23\n" \
"v_mfma_f32_16x16x32_bf16 a[120:123], a[240:243], v[72:75], a[120:123]\n" \
"v_exp_f32 v25, v25\n" \
"v_mfma_f32_16x16x32_bf16 a[120:123], a[244:247], v[104:107], a[120:123]\n" \
"ds_read_b128 a[236:239], v252 offset:6400\n" \
"v_add_f32 v243, v243, v25\n" \
"v_mfma_f32_16x16x32_bf16 v[152:155], a[240:243], v[80:83], v[152:155]\n" \
"v_exp_f32 v26, v26\n" \
"v_mfma_f32_16x16x32_bf16 v[152:155], a[244:247], v[112:115], v[152:155]\n" \
"v_add_f32 v243, v243, v26\n" \
"v_cvt_pk_bf16_f32 v24, v24, v25\n" \
"v_mfma_f32_16x16x32_bf16 v[184:187], a[240:243], v[88:91], v[184:187]\n" \
"v_exp_f32 v27, v27\n" \
"v_mfma_f32_16x16x32_bf16 v[184:187], a[244:247], v[120:123], v[184:187]\n" \
"ds_read_b128 a[240:243], v252 offset:12288\n" \
"v_add_f32 v243, v243, v27\n" \
"v_mfma_f32_16x16x32_bf16 a[92:95], a[248:251], v[64:67], a[92:95]\n" \
"v_exp_f32 v28, v28\n" \
"v_mfma_f32_16x16x32_bf16 a[92:95], a[252:255], v[96:99], a[92:95]\n" \
"v_add_f32 v243, v243, v28\n" \
"v_cvt_pk_bf16_f32 v25, v26, v27\n" \
"v_mfma_f32_16x16x32_bf16 a[124:127], a[248:251], v[72:75], a[124:127]\n" \
"v_exp_f32 v29, v29\n" \
"v_mfma_f32_16x16x32_bf16 a[124:127], a[252:255], v[104:107], a[124:127]\n" \
"ds_read_b128 a[244:247], v252 offset:14336\n" \
"v_add_f32 v243, v243, v29\n" \
"v_mfma_f32_16x16x32_bf16 v[156:159], a[248:251], v[80:83], v[156:159]\n" \
"v_exp_f32 v30, v30\n" \
"v_mfma_f32_16x16x32_bf16 v[156:159], a[252:255], v[112:115], v[156:159]\n" \
"v_add_f32 v243, v243, v30\n" \
"v_cvt_pk_bf16_f32 v26, v28, v29\n" \
"v_mfma_f32_16x16x32_bf16 v[188:191], a[248:251], v[88:91], v[188:191]\n" \
"s_waitcnt vmcnt(8) lgkmcnt(6)\n" \
"v_exp_f32 v31, v31\n" \
"v_mfma_f32_16x16x32_bf16 v[188:191], a[252:255], v[120:123], v[188:191]\n" \
"s_barrier\n" \
"ds_read_b128 a[248:251], v252 offset:12544\n" \
"v_add_f32 v243, v243, v31\n" \
"LA1g_%=:\n" \
"Lpg7642_%=:\n" \
"s_cbranch_execz Lcb0_%=\n" \
"v_mfma_f32_16x16x32_bf16 v[64:67], a[192:195], v[192:195], v[224:227]\n" \
"v_exp_f32 v32, v32\n" \
"v_mfma_f32_16x16x32_bf16 v[64:67], a[196:199], v[196:199], v[64:67]\n" \
"v_cvt_pk_bf16_f32 v27, v30, v31\n" \
"v_add_f32 v244, v244, v32\n" \
"v_mfma_f32_16x16x32_bf16 v[72:75], a[192:195], v[208:211], v[228:231]\n" \
"s_add_u32 m0, s74, 0x8000\n" \
"v_exp_f32 v33, v33\n" \
"v_mfma_f32_16x16x32_bf16 v[72:75], a[196:199], v[212:215], v[72:75]\n" \
"ds_read_b128 a[252:255], v252 offset:14592\n" \
"global_load_lds_dwordx4 v250, s[38:39] offset:-4096 sc1\n" \
"v_add_f32 v244, v244, v33\n" \
"v_mfma_f32_16x16x32_bf16 v[80:83], a[192:195], a[160:163], v[232:235]\n" \
"v_exp_f32 v34, v34\n" \
"v_mfma_f32_16x16x32_bf16 v[80:83], a[196:199], a[164:167], v[80:83]\n" \
"v_add_f32 v244, v244, v34\n" \
"v_cvt_pk_bf16_f32 v32, v32, v33\n" \
"v_mfma_f32_16x16x32_bf16 v[88:91], a[192:195], a[176:179], v[236:239]\n" \
"v_exp_f32 v35, v35\n" \
"v_mfma_f32_16x16x32_bf16 v[88:91], a[196:199], a[180:183], v[88:91]\n" \
"ds_read_b128 a[192:195], v247 offset:0\n" \
"global_load_lds_dwordx4 v250, s[38:39] offset:-3072 sc1\n" \
"v_add_f32 v244, v244, v35\n" \
"v_mfma_f32_16x16x32_bf16 v[68:71], a[200:203], v[192:195], v[224:227]\n" \
"v_exp_f32 v36, v36\n" \
"v_mfma_f32_16x16x32_bf16 v[68:71], a[204:207], v[196:199], v[68:71]\n" \
"v_add_f32 v244, v244, v36\n" \
"v_cvt_pk_bf16_f32 v33, v34, v35\n" \
"v_mfma_f32_16x16x32_bf16 v[76:79], a[200:203], v[208:211], v[228:231]\n" \
"v_exp_f32 v37, v37\n" \
"v_mfma_f32_16x16x32_bf16 v[76:79], a[204:207], v[212:215], v[76:79]\n" \
"ds_read_b128 a[196:199], v247 offset:8192\n" \
"v_add_f32 v244, v244, v37\n" \
"v_mfma_f32_16x16x32_bf16 v[84:87], a[200:203], a[160:163], v[232:235]\n" \
"v_exp_f32 v38, v38\n" \
"v_mfma_f32_16x16x32_bf16 v[84:87], a[204:207], a[164:167], v[84:87]\n" \
"v_add_f32 v244, v244, v38\n" \
"v_cvt_pk_bf16_f32 v34, v36, v37\n" \
"v_mfma_f32_16x16x32_bf16 v[92:95], a[200:203], a[176:179], v[236:239]\n" \
"v_exp_f32 v39, v39\n" \
"v_mfma_f32_16x16x32_bf16 v[92:95], a[204:207], a[180:183], v[92:95]\n" \
"ds_read_b128 a[200:203], v247 offset:1024\n" \
"global_load_lds_dwordx4 v250, s[38:39] offset:-2048 sc1\n" \
"v_add_f32 v244, v244, v39\n" \
"v_mfma_f32_16x16x32_bf16 v[96:99], a[208:211], v[192:195], v[224:227]\n" \
"v_exp_f32 v40, v40\n" \
"v_mfma_f32_16x16x32_bf16 v[96:99], a[212:215], v[196:199], v[96:99]\n" \
"v_add_f32 v245, v245, v40\n" \
"v_cvt_pk_bf16_f32 v35, v38, v39\n" \
"v_mfma_f32_16x16x32_bf16 v[104:107], a[208:211], v[208:211], v[228:231]\n" \
"v_exp_f32 v41, v41\n" \
"v_mfma_f32_16x16x32_bf16 v[104:107], a[212:215], v[212:215], v[104:107]\n" \
"ds_read_b128 a[204:207], v247 offset:9216\n" \
"global_load_lds_dwordx4 v250, s[38:39] offset:-1024 sc1\n" \
"v_add_f32 v245, v245, v41\n" \
"v_mfma_f32_16x16x32_bf16 v[112:115], a[208:211], a[160:163], v[232:235]\n" \
"v_exp_f32 v42, v42\n" \
"v_mfma_f32_16x16x32_bf16 v[112:115], a[212:215], a[164:167], v[112:115]\n" \
"v_add_f32 v245, v245, v42\n" \
"v_cvt_pk_bf16_f32 v40, v40, v41\n" \
"v_mfma_f32_16x16x32_bf16 v[120:123], a[208:211], a[176:179], v[236:239]\n" \
"v_exp_f32 v43, v43\n" \
"v_mfma_f32_16x16x32_bf16 v[120:123], a[212:215], a[180:183], v[120:123]\n" \
"ds_read_b128 a[208:211], v247 offset:2048\n" \
"v_add_f32 v245, v245, v43\n" \
"v_mfma_f32_16x16x32_bf16 v[100:103], a[216:219], v[192:195], v[224:227]\n" \
"v_exp_f32 v44, v44\n" \
"v_mfma_f32_16x16x32_bf16 v[100:103], a[220:223], v[196:199], v[100:103]\n" \
"v_add_f32 v245, v245, v44\n" \
"v_cvt_pk_bf16_f32 v41, v42, v43\n" \
"v_mfma_f32_16x16x32_bf16 v[108:111], a[216:219], v[208:211], v[228:231]\n" \
"v_exp_f32 v45, v45\n" \
"v_mfma_f32_16x16x32_bf16 v[108:111], a[220:223], v[212:215], v[108:111]\n" \
"ds_read_b128 a[212:215], v247 offset:10240\n" \
"global_load_lds_dwordx4 v250, s[38:39] offset:0 sc1\n" \
"v_add_f32 v245, v245, v45\n" \
"v_mfma_f32_16x16x32_bf16 v[116:119], a[216:219], a[160:163], v[232:235]\n" \
"v_exp_f32 v46, v46\n" \
"v_mfma_f32_16x16x32_bf16 v[116:119], a[220:223], a[164:167], v[116:119]\n" \
"v_add_f32 v245, v245, v46\n" \
"v_cvt_pk_bf16_f32 v42, v44, v45\n" \
"v_mfma_f32_16x16x32_bf16 v[124:127], a[216:219], a[176:179], v[236:239]\n" \
"s_waitcnt lgkmcnt(6)\n" \
"v_exp_f32 v47, v47\n" \
"v_mfma_f32_16x16x32_bf16 v[124:127], a[220:223], a[180:183], v[124:127]\n" \
"ds_read_b128 a[216:219], v247 offset:3072\n" \
"global_load_lds_dwordx4 v250, s[38:39] offset:1024 sc1\n" \
"v_add_f32 v245, v245, v47\n" \
"v_mfma_f32_16x16x32_bf16 v[64:67], a[224:227], v[200:203], v[64:67]\n" \
"v_exp_f32 v48, v48\n" \
"v_mfma_f32_16x16x32_bf16 v[64:67], a[228:231], v[204:207], v[64:67]\n" \
"v_add_f32 v242, v242, v48\n" \
"v_cvt_pk_bf16_f32 v43, v46, v47\n" \
"v_mfma_f32_16x16x32_bf16 v[72:75], a[224:227], v[216:219], v[72:75]\n" \
"v_exp_f32 v49, v49\n" \
"v_mfma_f32_16x16x32_bf16 v[72:75], a[228:231], v[220:223], v[72:75]\n" \
"ds_read_b128 a[220:223], v247 offset:11264\n" \
"v_add_f32 v242, v242, v49\n" \
"v_exp_f32 v50, v50\n" \
"v_mfma_f32_16x16x32_bf16 v[80:83], a[224:227], a[168:171], v[80:83]\n" \
"v_add_f32 v242, v242, v50\n" \
"v_cvt_pk_bf16_f32 v48, v48, v49\n" \
"v_mfma_f32_16x16x32_bf16 v[80:83], a[228:231], a[172:175], v[80:83]\n" \
"v_exp_f32 v51, v51\n" \
"v_mfma_f32_16x16x32_bf16 v[88:91], a[224:227], a[184:187], v[88:91]\n" \
"v_add_f32 v242, v242, v51\n" \
"v_exp_f32 v52, v52\n" \
"v_mfma_f32_16x16x32_bf16 v[88:91], a[228:231], a[188:191], v[88:91]\n" \
"ds_read_b128 a[224:227], v247 offset:4096\n" \
"global_load_lds_dwordx4 v250, s[38:39] offset:2048 sc1\n" \
"v_mfma_f32_16x16x32_bf16 v[68:71], a[232:235], v[200:203], v[68:71]\n" \
"v_add_f32 v242, v242, v52\n" \
"v_cvt_pk_bf16_f32 v49, v50, v51\n" \
"v_mfma_f32_16x16x32_bf16 v[68:71], a[236:239], v[204:207], v[68:71]\n" \
"v_exp_f32 v53, v53\n" \
"v_mfma_f32_16x16x32_bf16 v[76:79], a[232:235], v[216:219], v[76:79]\n" \
"v_add_f32 v242, v242, v53\n" \
"v_exp_f32 v54, v54\n" \
"v_mfma_f32_16x16x32_bf16 v[76:79], a[236:239], v[220:223], v[76:79]\n" \
"ds_read_b128 a[228:231], v247 offset:12288\n" \
"global_load_lds_dwordx4 v250, s[38:39] offset:3072 sc1\n" \
"v_mfma_f32_16x16x32_bf16 v[84:87], a[232:235], a[168:171], v[84:87]\n" \
"s_add_u32 s38, s38, 34816\n" \
"s_sub_u32 s76, s76, 1\n" \
"s_cmp_lg_u32 s76, 0\n" \
"v_mfma_f32_16x16x32_bf16 v[84:87], a[236:239], a[172:175], v[84:87]\n" \
"v_add_f32 v242, v242, v54\n" \
"v_cvt_pk_bf16_f32 v50, v52, v53\n" \
"v_mfma_f32_16x16x32_bf16 v[92:95], a[232:235], a[184:187], v[92:95]\n" \
"v_exp_f32 v55, v55\n" \
"v_mfma_f32_16x16x32_bf16 v[92:95], a[236:239], a[188:191], v[92:95]\n" \
"ds_read_b128 a[232:235], v247 offset:5120\n" \
"v_add_f32 v242, v242, v55\n" \
"v_mfma_f32_16x16x32_bf16 v[96:99], a[240:243], v[200:203], v[96:99]\n" \
"v_exp_f32 v56, v56\n" \
"v_mfma_f32_16x16x32_bf16 v[96:99], a[244:247], v[204:207], v[96:99]\n" \
"v_add_f32 v243, v243, v56\n" \
"v_cvt_pk_bf16_f32 v51, v54, v55\n" \
"v_mfma_f32_16x16x32_bf16 v[104:107], a[240:243], v[216:219], v[104:107]\n" \
"v_exp_f32 v57, v57\n" \
"v_mfma_f32_16x16x32_bf16 v[104:107], a[244:247], v[220:223], v[104:107]\n" \
"ds_read_b128 a[236:239], v247 offset:13312\n" \
"v_add_f32 v243, v243, v57\n" \
"v_mfma_f32_16x16x32_bf16 v[112:115], a[240:243], a[168:171], v[112:115]\n" \
"v_exp_f32 v58, v58\n" \
"v_mfma_f32_16x16x32_bf16 v[112:115], a[244:247], a[172:175], v[112:115]\n" \
"v_add_f32 v243, v243, v58\n" \
"v_cvt_pk_bf16_f32 v56, v56, v57\n" \
"v_mfma_f32_16x16x32_bf16 v[120:123], a[240:243], a[184:187], v[120:123]\n" \
"v_exp_f32 v59, v59\n" \
"v_mfma_f32_16x16x32_bf16 v[120:123], a[244:247], a[188:191], v[120:123]\n" \
"ds_read_b128 a[240:243], v247 offset:6144\n" \
"v_add_f32 v243, v243, v59\n" \
"v_mfma_f32_16x16x32_bf16 v[100:103], a[248:251], v[200:203], v[100:103]\n" \
"v_exp_f32 v60, v60\n" \
"v_mfma_f32_16x16x32_bf16 v[100:103], a[252:255], v[204:207], v[100:103]\n" \
"v_add_f32 v243, v243, v60\n" \
"v_cvt_pk_bf16_f32 v57, v58, v59\n" \
"v_mfma_f32_16x16x32_bf16 v[108:111], a[248:251], v[216:219], v[108:111]\n" \
"v_exp_f32 v61, v61\n" \
"v_mfma_f32_16x16x32_bf16 v[108:111], a[252:255], v[220:223], v[108:111]\n" \
"ds_read_b128 a[244:247], v247 offset:14336\n" \
"v_add_f32 v243, v243, v61\n" \
"v_mfma_f32_16x16x32_bf16 v[116:119], a[248:251], a[168:171], v[116:119]\n" \
"v_exp_f32 v62, v62\n" \
"v_mfma_f32_16x16x32_bf16 v[116:119], a[252:255], a[172:175], v[116:119]\n" \
"v_add_f32 v243, v243, v62\n" \
"v_cvt_pk_bf16_f32 v58, v60, v61\n" \
"v_mfma_f32_16x16x32_bf16 v[124:127], a[248:251], a[184:187], v[124:127]\n" \
"s_waitcnt vmcnt(8) lgkmcnt(6)\n" \
"v_exp_f32 v63, v63\n" \
"v_mfma_f32_16x16x32_bf16 v[124:127], a[252:255], a[188:191], v[124:127]\n" \
"s_barrier\n" \
"ds_read_b128 a[248:251], v247 offset:7168\n" \
"v_add_f32 v243, v243, v63\n" \
"s_cbranch_scc1 Lloop2_%=\n" \
"v_mfma_f32_16x16x32_bf16 a[64:67], a[192:195], v[0:3], a[64:67]\n" \
"v_subrev_u32 v246, s82, v246\n" \
"v_exp_f32 v64, v64\n" \
"v_mfma_f32_16x16x32_bf16 a[64:67], a[196:199], v[32:35], a[64:67]\n" \
"v_cvt_pk_bf16_f32 v59, v62, v63\n" \
"v_add_f32 v244, v244, v64\n" \
"v_mfma_f32_16x16x32_bf16 a[96:99], a[192:195], v[8:11], a[96:99]\n" \
"v_mov_b32 v208, v241\n" \
"v_exp_f32 v65, v65\n" \
"v_mfma_f32_16x16x32_bf16 a[96:99], a[196:199], v[40:43], a[96:99]\n" \
"ds_read_b128 a[252:255], v247 offset:15360\n" \
"v_add_f32 v244, v244, v65\n" \
"v_mfma_f32_16x16x32_bf16 v[128:131], a[192:195], v[16:19], v[128:131]\n" \
"v_exp_f32 v66, v66\n" \
"v_permlane16_swap_b32 v208, v241\n" \
"v_add_f32 v241, v241, v208\n" \
"v_mov_b32 v208, v241\n" \
"v_mfma_f32_16x16x32_bf16 v[128:131], a[196:199], v[48:51], v[128:131]\n" \
"v_add_f32 v244, v244, v66\n" \
"v_cvt_pk_bf16_f32 v64, v64, v65\n" \
"v_mfma_f32_16x16x32_bf16 v[160:163], a[192:195], v[24:27], v[160:163]\n" \
"v_exp_f32 v67, v67\n" \
"v_permlane32_swap_b32 v208, v241\n" \
"v_add_f32 v241, v241, v208\n" \
"v_rcp_f32 v208, v241\n" \
"v_add_u32 v209, 0x1000, v249\n" \
"v_mul_f32 v241, 0x4d800000, v241\n" \
"v_accvgpr_read_b32 v192, a32\n" \
"v_mfma_f32_16x16x32_bf16 v[160:163], a[196:199], v[56:59], v[160:163]\n" \
"ds_read_b128 a[192:195], v246 offset:0\n" \
"v_add_f32 v244, v244, v67\n" \
"v_accvgpr_read_b32 v193, a33\n" \
"v_accvgpr_read_b32 v194, a34\n" \
"v_accvgpr_read_b32 v195, a35\n" \
"v_accvgpr_read_b32 v196, a36\n" \
"v_accvgpr_read_b32 v197, a37\n" \
"v_accvgpr_read_b32 v198, a38\n" \
"v_mfma_f32_16x16x32_bf16 a[68:71], a[200:203], v[0:3], a[68:71]\n" \
"v_accvgpr_read_b32 v199, a39\n" \
"v_mul_f32 v192, v192, v208\n" \
"v_mul_f32 v193, v193, v208\n" \
"v_mul_f32 v194, v194, v208\n" \
"v_mul_f32 v195, v195, v208\n" \
"v_mul_f32 v196, v196, v208\n" \
"v_exp_f32 v68, v68\n" \
"v_mfma_f32_16x16x32_bf16 a[68:71], a[204:207], v[32:35], a[68:71]\n" \
"s_waitcnt lgkmcnt(1)\n" \
"v_add_f32 v244, v244, v68\n" \
"v_cvt_pk_bf16_f32 v65, v66, v67\n" \
"v_mul_f32 v197, v197, v208\n" \
"v_mul_f32 v198, v198, v208\n" \
"v_mul_f32 v199, v199, v208\n" \
"v_fma_f32 v241, v192, v193, v241\n" \
"v_cvt_pk_bf16_f32 v200, v192, v193\n" \
"v_fma_f32 v241, v194, v195, v241\n" \
"v_mfma_f32_16x16x32_bf16 a[100:103], a[200:203], v[8:11], a[100:103]\n" \
"s_barrier\n" \
"v_cvt_pk_bf16_f32 v201, v194, v195\n" \
"v_fma_f32 v241, v196, v197, v241\n" \
"v_cvt_pk_bf16_f32 v202, v196, v197\n" \
"v_fma_f32 v241, v198, v199, v241\n" \
"v_cvt_pk_bf16_f32 v203, v198, v199\n" \
"v_exp_f32 v69, v69\n" \
"v_mfma_f32_16x16x32_bf16 a[100:103], a[204:207], v[40:43], a[100:103]\n" \
"s_mov_b32 m0, s74\n" \
"ds_read_b128 a[196:199], v246 offset:2048\n" \
"v_add_f32 v244, v244, v69\n" \
"v_mfma_f32_16x16x32_bf16 v[132:135], a[200:203], v[16:19], v[132:135]\n" \
"global_load_lds_dwordx4 v250, s[38:39] offset:-4096 sc1\n" \
"v_exp_f32 v70, v70\n" \
"v_mfma_f32_16x16x32_bf16 v[132:135], a[204:207], v[48:51], v[132:135]\n" \
"v_add_f32 v244, v244, v70\n" \
"v_cvt_pk_bf16_f32 v66, v68, v69\n" \
"v_mfma_f32_16x16x32_bf16 v[164:167], a[200:203], v[24:27], v[164:167]\n" \
"v_exp_f32 v71, v71\n" \
"v_mfma_f32_16x16x32_bf16 v[164:167], a[204:207], v[56:59], v[164:167]\n" \
"ds_read_b128 a[200:203], v246 offset:256\n" \
"global_load_lds_dwordx4 v250, s[38:39] offset:-3072 sc1\n" \
"v_add_f32 v244, v244, v71\n" \
"v_mfma_f32_16x16x32_bf16 a[72:75], a[208:211], v[0:3], a[72:75]\n" \
"v_exp_f32 v72, v72\n" \
"v_mfma_f32_16x16x32_bf16 a[72:75], a[212:215], v[32:35], a[72:75]\n" \
"v_add_f32 v245, v245, v72\n" \
"v_cvt_pk_bf16_f32 v67, v70, v71\n" \
"v_mfma_f32_16x16x32_bf16 a[104:107], a[208:211], v[8:11], a[104:107]\n" \
"global_load_lds_dwordx4 v250, s[38:39] offset:-2048 sc1\n" \
"v_exp_f32 v73, v73\n" \
"v_mfma_f32_16x16x32_bf16 a[104:107], a[212:215], v[40:43], a[104:107]\n" \
"ds_read_b128 a[204:207], v246 offset:2304\n" \
"v_add_f32 v245, v245, v73\n" \
"v_mfma_f32_16x16x32_bf16 v[136:139], a[208:211], v[16:19], v[136:139]\n" \
"v_exp_f32 v74, v74\n" \
"v_mfma_f32_16x16x32_bf16 v[136:139], a[212:215], v[48:51], v[136:139]\n" \
"global_load_lds_dwordx4 v250, s[38:39] offset:-1024 sc1\n" \
"v_add_f32 v245, v245, v74\n" \
"v_cvt_pk_bf16_f32 v72, v72, v73\n" \
"v_mfma_f32_16x16x32_bf16 v[168:171], a[208:211], v[24:27], v[168:171]\n" \
"v_exp_f32 v75, v75\n" \
"v_mfma_f32_16x16x32_bf16 v[168:171], a[212:215], v[56:59], v[168:171]\n" \
"ds_read_b128 a[208:211], v246 offset:8192\n" \
"v_add_f32 v245, v245, v75\n" \
"v_mfma_f32_16x16x32_bf16 a[76:79], a[216:219], v[0:3], a[76:79]\n" \
"global_load_lds_dwordx4 v250, s[38:39] offset:0 sc1\n" \
"v_exp_f32 v76, v76\n" \
"v_mfma_f32_16x16x32_bf16 a[76:79], a[220:223], v[32:35], a[76:79]\n" \
"v_add_f32 v245, v245, v76\n" \
"v_cvt_pk_bf16_f32 v73, v74, v75\n" \
"v_mfma_f32_16x16x32_bf16 a[108:111], a[216:219], v[8:11], a[108:111]\n" \
"v_exp_f32 v77, v77\n" \
"v_mfma_f32_16x16x32_bf16 a[108:111], a[220:223], v[40:43], a[108:111]\n" \
"ds_read_b128 a[212:215], v246 offset:10240\n" \
"global_load_lds_dwordx4 v250, s[38:39] offset:1024 sc1\n" \
"v_add_f32 v245, v245, v77\n" \
"v_mfma_f32_16x16x32_bf16 v[140:143], a[216:219], v[16:19], v[140:143]\n" \
"v_exp_f32 v78, v78\n" \
"v_mfma_f32_16x16x32_bf16 v[140:143], a[220:223], v[48:51], v[140:143]\n" \
"v_add_f32 v245, v245, v78\n" \
"v_cvt_pk_bf16_f32 v74, v76, v77\n" \
"v_mfma_f32_16x16x32_bf16 v[172:175], a[216:219], v[24:27], v[172:175]\n" \
"s_waitcnt lgkmcnt(6)\n" \
"global_load_lds_dwordx4 v250, s[38:39] offset:2048 sc1\n" \
"v_exp_f32 v79, v79\n" \
"v_mfma_f32_16x16x32_bf16 v[172:175], a[220:223], v[56:59], v[172:175]\n" \
"ds_read_b128 a[216:219], v246 offset:8448\n" \
"v_add_f32 v245, v245, v79\n" \
"v_mfma_f32_16x16x32_bf16 a[80:83], a[224:227], v[0:3], a[80:83]\n" \
"v_exp_f32 v80, v80\n" \
"v_mfma_f32_16x16x32_bf16 a[80:83], a[228:231], v[32:35], a[80:83]\n" \
"global_load_lds_dwordx4 v250, s[38:39] offset:3072 sc1\n" \
"v_add_f32 v242, v242, v80\n" \
"v_cvt_pk_bf16_f32 v75, v78, v79\n" \
"v_mfma_f32_16x16x32_bf16 a[112:115], a[224:227], v[8:11], a[112:115]\n" \
"v_exp_f32 v81, v81\n" \
"v_mfma_f32_16x16x32_bf16 a[112:115], a[228:231], v[40:43], a[112:115]\n" \
"ds_read_b128 a[220:223], v246 offset:10496\n" \
"v_add_f32 v242, v242, v81\n" \
"v_mfma_f32_16x16x32_bf16 v[144:147], a[224:227], v[16:19], v[144:147]\n" \
"v_exp_f32 v82, v82\n" \
"v_mfma_f32_16x16x32_bf16 v[144:147], a[228:231], v[48:51], v[144:147]\n" \
"v_add_f32 v242, v242, v82\n" \
"v_cvt_pk_bf16_f32 v80, v80, v81\n" \
"v_mfma_f32_16x16x32_bf16 v[176:179], a[224:227], v[24:27], v[176:179]\n" \
"v_exp_f32 v83, v83\n" \
"v_mfma_f32_16x16x32_bf16 v[176:179], a[228:231], v[56:59], v[176:179]\n" \
"ds_read_b128 a[224:227], v246 offset:4096\n" \
"v_add_f32 v242, v242, v83\n" \
"v_mfma_f32_16x16x32_bf16 a[84:87], a[232:235], v[0:3], a[84:87]\n" \
"buffer_store_dwordx4 v[200:203], v249, s[56:59], s77 offen offset:0 sc1\n" \
"v_exp_f32 v84, v84\n" \
"v_accvgpr_read_b32 v192, a40\n" \
"v_accvgpr_read_b32 v193, a41\n" \
"v_accvgpr_read_b32 v194, a42\n" \
"v_accvgpr_read_b32 v195, a43\n" \
"v_accvgpr_read_b32 v196, a44\n" \
"v_mfma_f32_16x16x32_bf16 a[84:87], a[236:239], v[32:35], a[84:87]\n" \
"v_add_f32 v242, v242, v84\n" \
"v_cvt_pk_bf16_f32 v81, v82, v83\n" \
"v_accvgpr_read_b32 v197, a45\n" \
"v_accvgpr_read_b32 v198, a46\n" \
"v_accvgpr_read_b32 v199, a47\n" \
"v_mul_f32 v192, v192, v208\n" \
"v_mul_f32 v193, v193, v208\n" \
"v_mul_f32 v194, v194, v208\n" \
"v_mfma_f32_16x16x32_bf16 a[116:119], a[232:235], v[8:11], a[116:119]\n" \
"v_mul_f32 v195, v195, v208\n" \
"v_mul_f32 v196, v196, v208\n" \
"v_mul_f32 v197, v197, v208\n" \
"v_mul_f32 v198, v198, v208\n" \
"v_mul_f32 v199, v199, v208\n" \
"v_fma_f32 v241, v192, v193, v241\n" \
"v_exp_f32 v85, v85\n" \
"v_mfma_f32_16x16x32_bf16 a[116:119], a[236:239], v[40:43], a[116:119]\n" \
"ds_read_b128 a[228:231], v246 offset:6144\n" \
"v_add_f32 v242, v242, v85\n" \
"v_cvt_pk_bf16_f32 v204, v192, v193\n" \
"v_fma_f32 v241, v194, v195, v241\n" \
"v_cvt_pk_bf16_f32 v205, v194, v195\n" \
"v_fma_f32 v241, v196, v197, v241\n" \
"v_cvt_pk_bf16_f32 v206, v196, v197\n" \
"v_fma_f32 v241, v198, v199, v241\n" \
"v_mfma_f32_16x16x32_bf16 v[148:151], a[232:235], v[16:19], v[148:151]\n" \
"v_cvt_pk_bf16_f32 v207, v198, v199\n" \
"v_exp_f32 v86, v86\n" \
"v_mfma_f32_16x16x32_bf16 v[148:151], a[236:239], v[48:51], v[148:151]\n" \
"buffer_store_dwordx4 v[204:207], v249, s[56:59], s77 offen offset:2048 sc1\n" \
"v_add_f32 v242, v242, v86\n" \
"v_cvt_pk_bf16_f32 v82, v84, v85\n" \
"v_accvgpr_read_b32 v192, a48\n" \
"v_accvgpr_read_b32 v193, a49\n" \
"v_accvgpr_read_b32 v194, a50\n" \
"v_accvgpr_read_b32 v195, a51\n" \
"v_accvgpr_read_b32 v196, a52\n" \
"v_mfma_f32_16x16x32_bf16 v[180:183], a[232:235], v[24:27], v[180:183]\n" \
"v_accvgpr_read_b32 v197, a53\n" \
"v_accvgpr_read_b32 v198, a54\n" \
"v_accvgpr_read_b32 v199, a55\n" \
"v_mul_f32 v192, v192, v208\n" \
"v_mul_f32 v193, v193, v208\n" \
"v_mul_f32 v194, v194, v208\n" \
"v_exp_f32 v87, v87\n" \
"v_mfma_f32_16x16x32_bf16 v[180:183], a[236:239], v[56:59], v[180:183]\n" \
"ds_read_b128 a[232:235], v246 offset:4352\n" \
"v_add_f32 v242, v242, v87\n" \
"v_mul_f32 v195, v195, v208\n" \
"v_mul_f32 v196, v196, v208\n" \
"v_mul_f32 v197, v197, v208\n" \
"v_mul_f32 v198, v198, v208\n" \
"v_mul_f32 v199, v199, v208\n" \
"v_fma_f32 v241, v192, v193, v241\n" \
"v_mfma_f32_16x16x32_bf16 a[88:91], a[240:243], v[0:3], a[88:91]\n" \
"v_cvt_pk_bf16_f32 v200, v192, v193\n" \
"v_fma_f32 v241, v194, v195, v241\n" \
"v_cvt_pk_bf16_f32 v201, v194, v195\n" \
"v_fma_f32 v241, v196, v197, v241\n" \
"v_cvt_pk_bf16_f32 v202, v196, v197\n" \
"v_fma_f32 v241, v198, v199, v241\n" \
"v_exp_f32 v88, v88\n" \
"v_mfma_f32_16x16x32_bf16 a[88:91], a[244:247], v[32:35], a[88:91]\n" \
"v_cvt_pk_bf16_f32 v203, v198, v199\n" \
"v_add_f32 v243, v243, v88\n" \
"v_cvt_pk_bf16_f32 v83, v86, v87\n" \
"v_mfma_f32_16x16x32_bf16 a[120:123], a[240:243], v[8:11], a[120:123]\n" \
"buffer_store_dwordx4 v[200:203], v209, s[56:59], s77 offen offset:0 sc1\n" \
"v_exp_f32 v89, v89\n" \
"v_accvgpr_read_b32 v192, a56\n" \
"v_accvgpr_read_b32 v193, a57\n" \
"v_accvgpr_read_b32 v194, a58\n" \
"v_accvgpr_read_b32 v195, a59\n" \
"v_accvgpr_read_b32 v196, a60\n" \
"v_mfma_f32_16x16x32_bf16 a[120:123], a[244:247], v[40:43], a[120:123]\n" \
"ds_read_b128 a[236:239], v246 offset:6400\n" \
"v_add_f32 v243, v243, v89\n" \
"v_accvgpr_read_b32 v197, a61\n" \
"v_accvgpr_read_b32 v198, a62\n" \
"v_accvgpr_read_b32 v199, a63\n" \
"v_mul_f32 v192, v192, v208\n" \
"v_mul_f32 v193, v193, v208\n" \
"v_mul_f32 v194, v194, v208\n" \
"v_mfma_f32_16x16x32_bf16 v[152:155], a[240:243], v[16:19], v[152:155]\n" \
"v_mul_f32 v195, v195, v208\n" \
"v_mul_f32 v196, v196, v208\n" \
"v_mul_f32 v197, v197, v208\n" \
"v_mul_f32 v198, v198, v208\n" \
"v_mul_f32 v199, v199, v208\n" \
"v_fma_f32 v241, v192, v193, v241\n" \
"v_exp_f32 v90, v90\n" \
"v_mfma_f32_16x16x32_bf16 v[152:155], a[244:247], v[48:51], v[152:155]\n" \
"v_add_f32 v243, v243, v90\n" \
"v_cvt_pk_bf16_f32 v88, v88, v89\n" \
"v_cvt_pk_bf16_f32 v204, v192, v193\n" \
"v_fma_f32 v241, v194, v195, v241\n" \
"v_cvt_pk_bf16_f32 v205, v194, v195\n" \
"v_fma_f32 v241, v196, v197, v241\n" \
"v_cvt_pk_bf16_f32 v206, v196, v197\n" \
"v_fma_f32 v241, v198, v199, v241\n" \
"v_mfma_f32_16x16x32_bf16 v[184:187], a[240:243], v[24:27], v[184:187]\n" \
"v_cvt_pk_bf16_f32 v207, v198, v199\n" \
"v_exp_f32 v91, v91\n" \
"v_mfma_f32_16x16x32_bf16 v[184:187], a[244:247], v[56:59], v[184:187]\n" \
"buffer_store_dwordx4 v[204:207], v209, s[56:59], s77 offen offset:2048 sc1\n" \
"v_add_f32 v243, v243, v91\n" \
"s_add_u32 s77, s77, 0xfff7e000\n" \
"ds_read_b128 a[240:243], v246 offset:12288\n" \
"v_mfma_f32_16x16x32_bf16 a[92:95], a[248:251], v[0:3], a[92:95]\n" \
"v_exp_f32 v92, v92\n" \
"v_mfma_f32_16x16x32_bf16 a[92:95], a[252:255], v[32:35], a[92:95]\n" \
"v_add_f32 v243, v243, v92\n" \
"v_cvt_pk_bf16_f32 v89, v90, v91\n" \
"v_mfma_f32_16x16x32_bf16 a[124:127], a[248:251], v[8:11], a[124:127]\n" \
"v_exp_f32 v93, v93\n" \
"v_mfma_f32_16x16x32_bf16 a[124:127], a[252:255], v[40:43], a[124:127]\n" \
"ds_read_b128 a[244:247], v246 offset:14336\n" \
"v_add_f32 v243, v243, v93\n" \
"v_mfma_f32_16x16x32_bf16 v[156:159], a[248:251], v[16:19], v[156:159]\n" \
"v_exp_f32 v94, v94\n" \
"v_mfma_f32_16x16x32_bf16 v[156:159], a[252:255], v[48:51], v[156:159]\n" \
"v_add_f32 v243, v243, v94\n" \
"v_cvt_pk_bf16_f32 v90, v92, v93\n" \
"v_mfma_f32_16x16x32_bf16 v[188:191], a[248:251], v[24:27], v[188:191]\n" \
"s_waitcnt vmcnt(12) lgkmcnt(6)\n" \
"Lpg8099_%=:\n" \
"Lst1x1_%=:\n" \
"s_cbranch_execz Lpg10500_%=\n" \
"v_exp_f32 v95, v95\n" \
"v_mfma_f32_16x16x32_bf16 v[188:191], a[252:255], v[56:59], v[188:191]\n" \
"s_barrier\n" \
"ds_read_b128 a[248:251], v246 offset:12544\n" \
"v_add_f32 v243, v243, v95\n" \
"v_mfma_f32_16x16x32_bf16 v[16:19], a[192:195], a[160:163], v[232:235]\n" \
"s_bfe_u32 s88, %1, 0x20001\n" \
"ds_read_b128 v[192:195], v246 offset:16384\n" \
"v_exp_f32 v96, v96\n" \
"s_lshl_b32 s88, s88, 2\n" \
"v_mfma_f32_16x16x32_bf16 v[16:19], a[196:199], a[164:167], v[16:19]\n" \
"s_lshr_b32 s70, %1, 6\n" \
"ds_read_b128 v[196:199], v246 offset:18432\n" \
"v_cvt_pk_bf16_f32 v91, v94, v95\n" \
"v_add_f32 v244, v244, v96\n" \
"v_exp_f32 v97, v97\n" \
"v_mfma_f32_16x16x32_bf16 v[24:27], a[192:195], a[176:179], v[236:239]\n" \
"s_add_u32 s88, s88, s70\n" \
"ds_read_b128 v[200:203], v246 offset:16640\n" \
"v_add_f32 v244, v244, v97\n" \
"v_exp_f32 v98, v98\n" \
"v_bfe_u32 v69, v248, 8, 1\n" \
"v_bfe_u32 v70, v248, 6, 2\n" \
"s_lshr_b32 s71, s22, 7\n" \
"v_mfma_f32_16x16x32_bf16 v[24:27], a[196:199], a[180:183], v[24:27]\n" \
"s_sub_u32 s71, s63, s71\n" \
"ds_read_b128 a[252:255], v246 offset:14592\n" \
"v_add_f32 v244, v244, v98\n" \
"v_cvt_pk_bf16_f32 v96, v96, v97\n" \
"v_exp_f32 v99, v99\n" \
"s_add_u32 s71, s71, 6\n" \
"v_mfma_f32_16x16x32_bf16 v[20:23], a[200:203], a[160:163], v[232:235]\n" \
"s_and_b32 s71, s71, 7\n" \
"ds_read_b128 v[204:207], v246 offset:18688\n" \
"v_add_f32 v244, v244, v99\n" \
"v_exp_f32 v100, v100\n" \
"v_lshl_add_u32 v70, v69, 3, v70\n" \
"v_mul_u32_u24 v70, 6144, v70\n" \
"v_mfma_f32_16x16x32_bf16 v[20:23], a[204:207], a[164:167], v[20:23]\n" \
"s_and_b32 s70, %1, 1\n" \
"ds_read_b128 v[208:211], v246 offset:24576\n" \
"v_add_f32 v244, v244, v100\n" \
"v_cvt_pk_bf16_f32 v97, v98, v99\n" \
"v_exp_f32 v101, v101\n" \
"s_lshl_b32 s70, s70, 3\n" \
"v_mfma_f32_16x16x32_bf16 v[28:31], a[200:203], a[176:179], v[236:239]\n" \
"s_add_u32 s70, s70, s71\n" \
"ds_read_b128 v[212:215], v246 offset:26624\n" \
"v_add_f32 v244, v244, v101\n" \
"v_exp_f32 v102, v102\n" \
"v_lshlrev_b32 v69, 5, v69\n" \
"v_and_b32 v71, 0x30, v248\n" \
"s_lshl_b32 s87, s70, 12\n" \
"v_mfma_f32_16x16x32_bf16 v[28:31], a[204:207], a[180:183], v[28:31]\n" \
"s_lshl_b32 s70, s70, 7\n" \
"ds_read_b128 v[216:219], v246 offset:24832\n" \
"v_add_f32 v244, v244, v102\n" \
"v_cvt_pk_bf16_f32 v98, v100, v101\n" \
"v_exp_f32 v103, v103\n" \
"s_mul_i32 s84, s61, 6144\n" \
"v_mfma_f32_16x16x32_bf16 v[48:51], a[208:211], a[160:163], v[232:235]\n" \
"s_lshl_b32 s71, s84, 4\n" \
"ds_read_b128 v[220:223], v246 offset:26880\n" \
"v_add_f32 v244, v244, v103\n" \
"v_exp_f32 v104, v104\n" \
"v_xor_b32 v69, v69, v71\n" \
"v_mfma_f32_16x16x32_bf16 v[48:51], a[212:215], a[164:167], v[48:51]\n" \
"ds_read_b128 a[192:195], v247 offset:32768\n" \
"v_lshrrev_b32 v71, 9, v248\n" \
"v_add_f32 v245, v245, v104\n" \
"v_cvt_pk_bf16_f32 v99, v102, v103\n" \
"v_exp_f32 v105, v105\n" \
"v_mfma_f32_16x16x32_bf16 v[56:59], a[208:211], a[176:179], v[236:239]\n" \
"s_add_u32 s70, s70, s71\n" \
"ds_read_b128 a[196:199], v247 offset:40960\n" \
"v_add_f32 v245, v245, v105\n" \
"v_exp_f32 v106, v106\n" \
"s_add_u32 s70, s70, 4096\n" \
"v_mfma_f32_16x16x32_bf16 v[56:59], a[212:215], a[180:183], v[56:59]\n" \
"s_waitcnt lgkmcnt(4)\n" \
"ds_read_b128 a[200:203], v247 offset:33792\n" \
"v_lshl_add_u32 v70, v71, 6, v70\n" \
"v_add_f32 v245, v245, v106\n" \
"v_cvt_pk_bf16_f32 v104, v104, v105\n" \
"v_exp_f32 v107, v107\n" \
"v_mfma_f32_16x16x32_bf16 v[8:11], v[192:195], a[176:179], v[236:239]\n" \
"ds_read_b128 a[204:207], v247 offset:41984\n" \
"v_add_u32 v253, v69, v70\n" \
"v_add_f32 v245, v245, v107\n" \
"v_exp_f32 v108, v108\n" \
"v_mfma_f32_16x16x32_bf16 v[8:11], v[196:199], a[180:183], v[8:11]\n" \
"s_add_u32 s48, s22, s70\n" \
"s_addc_u32 s49, s23, 0\n" \
"ds_read_b128 a[208:211], v247 offset:34816\n" \
"v_add_f32 v245, v245, v108\n" \
"v_cvt_pk_bf16_f32 v105, v106, v107\n" \
"v_exp_f32 v109, v109\n" \
"v_mfma_f32_16x16x32_bf16 v[52:55], a[216:219], a[160:163], v[232:235]\n" \
"s_add_u32 s50, s48, 23552\n" \
"s_addc_u32 s51, s49, 0\n" \
"ds_read_b128 a[212:215], v247 offset:43008\n" \
"v_add_f32 v245, v245, v109\n" \
"v_exp_f32 v110, v110\n" \
"v_mfma_f32_16x16x32_bf16 v[52:55], a[220:223], a[164:167], v[52:55]\n" \
"s_lshr_b32 s89, s22, 7\n" \
"ds_read_b128 v[192:195], v246 offset:20480\n" \
"v_add_f32 v245, v245, v110\n" \
"v_cvt_pk_bf16_f32 v106, v108, v109\n" \
"v_exp_f32 v111, v111\n" \
"s_sub_u32 s89, 3, s89\n" \
"v_mfma_f32_16x16x32_bf16 v[60:63], a[216:219], a[176:179], v[236:239]\n" \
"ds_read_b128 v[196:199], v246 offset:22528\n" \
"v_bfe_u32 v84, v248, 7, 2\n" \
"v_add_f32 v245, v245, v111\n" \
"v_exp_f32 v112, v112\n" \
"v_mfma_f32_16x16x32_bf16 v[60:63], a[220:223], a[180:183], v[60:63]\n" \
"s_and_b32 s89, s89, 7\n" \
"ds_read_b128 a[216:219], v247 offset:35840\n" \
"v_add_f32 v242, v242, v112\n" \
"v_cvt_pk_bf16_f32 v107, v110, v111\n" \
"v_exp_f32 v113, v113\n" \
"s_and_b32 s90, %1, 1\n" \
"v_mfma_f32_16x16x32_bf16 v[12:15], v[200:203], a[176:179], v[236:239]\n" \
"ds_read_b128 a[220:223], v247 offset:44032\n" \
"v_bfe_u32 v85, v248, 5, 2\n" \
"v_add_f32 v242, v242, v113\n" \
"v_exp_f32 v114, v114\n" \
"v_mfma_f32_16x16x32_bf16 v[12:15], v[204:207], a[180:183], v[12:15]\n" \
"s_lshl_b32 s90, s90, 3\n" \
"ds_read_b128 v[200:203], v246 offset:20736\n" \
"v_add_f32 v242, v242, v114\n" \
"v_cvt_pk_bf16_f32 v112, v112, v113\n" \
"v_exp_f32 v115, v115\n" \
"s_add_u32 s89, s89, s90\n" \
"v_mfma_f32_16x16x32_bf16 v[16:19], a[224:227], a[168:171], v[16:19]\n" \
"ds_read_b128 v[204:207], v246 offset:22784\n" \
"v_min_u32 v85, 2, v85\n" \
"v_add_f32 v242, v242, v115\n" \
"v_exp_f32 v116, v116\n" \
"v_mfma_f32_16x16x32_bf16 v[16:19], a[228:231], a[172:175], v[16:19]\n" \
"s_lshl_b32 s89, s89, 7\n" \
"v_add_f32 v242, v242, v116\n" \
"v_cvt_pk_bf16_f32 v113, v114, v115\n" \
"v_exp_f32 v117, v117\n" \
"s_lshr_b32 s90, %1, 3\n" \
"v_mad_u32_u24 v84, v84, 3, v85\n" \
"v_mfma_f32_16x16x32_bf16 v[24:27], a[224:227], a[184:187], v[24:27]\n" \
"s_lshl_b32 s90, s90, 2\n" \
"v_add_f32 v242, v242, v117\n" \
"v_exp_f32 v118, v118\n" \
"s_add_u32 s90, s90, s61\n" \
"v_and_b32 v85, 16, v248\n" \
"v_mfma_f32_16x16x32_bf16 v[24:27], a[228:231], a[188:191], v[24:27]\n" \
"s_mul_i32 s90, s90, 24576\n" \
"ds_read_b128 a[224:227], v247 offset:36864\n" \
"v_add_f32 v242, v242, v118\n" \
"v_cvt_pk_bf16_f32 v114, v116, v117\n" \
"s_add_u32 s89, s89, s90\n" \
"v_mfma_f32_16x16x32_bf16 v[40:43], v[208:211], a[176:179], v[236:239]\n" \
"ds_read_b128 a[228:231], v247 offset:45056\n" \
"v_lshl_add_u32 v85, v85, 2, s89\n" \
"v_mfma_f32_16x16x32_bf16 v[40:43], v[212:215], a[180:183], v[40:43]\n" \
"ds_read_b128 v[208:211], v246 offset:28672\n" \
"v_lshl_add_u32 v84, v84, 11, v85\n" \
"v_mfma_f32_16x16x32_bf16 v[20:23], a[232:235], a[168:171], v[20:23]\n" \
"s_add_u32 m0, s84, 143360\n" \
"ds_read_b128 v[212:215], v246 offset:30720\n" \
"v_exp_f32 v119, v119\n" \
"v_mfma_f32_16x16x32_bf16 v[20:23], a[236:239], a[172:175], v[20:23]\n" \
"global_load_lds_dwordx4 v253, s[48:49] offset:-4096\n" \
"v_add_f32 v242, v242, v119\n" \
"v_mfma_f32_16x16x32_bf16 v[28:31], a[232:235], a[184:187], v[28:31]\n" \
"v_exp_f32 v120, v120\n" \
"v_mfma_f32_16x16x32_bf16 v[28:31], a[236:239], a[188:191], v[28:31]\n" \
"s_waitcnt lgkmcnt(2)\n" \
"ds_read_b128 a[232:235], v247 offset:37888\n" \
"v_add_f32 v243, v243, v120\n" \
"v_cvt_pk_bf16_f32 v115, v118, v119\n" \
"v_mfma_f32_16x16x32_bf16 v[44:47], v[216:219], a[176:179], v[236:239]\n" \
"ds_read_b128 a[236:239], v247 offset:46080\n" \
"global_load_lds_dwordx4 v253, s[48:49] offset:-2048\n" \
"v_mfma_f32_16x16x32_bf16 v[44:47], v[220:223], a[180:183], v[44:47]\n" \
"ds_read_b128 v[216:219], v246 offset:28928\n" \
"v_exp_f32 v121, v121\n" \
"v_mfma_f32_16x16x32_bf16 v[48:51], a[240:243], a[168:171], v[48:51]\n" \
"ds_read_b128 v[220:223], v246 offset:30976\n" \
"v_add_f32 v243, v243, v121\n" \
"v_mfma_f32_16x16x32_bf16 v[48:51], a[244:247], a[172:175], v[48:51]\n" \
"global_load_lds_dwordx4 v253, s[48:49] offset:0\n" \
"v_exp_f32 v122, v122\n" \
"v_mfma_f32_16x16x32_bf16 v[56:59], a[240:243], a[184:187], v[56:59]\n" \
"s_add_u32 s48, s48, 0x120000\n" \
"s_addc_u32 s49, s49, 0\n" \
"v_add_f32 v243, v243, v122\n" \
"v_mfma_f32_16x16x32_bf16 v[56:59], a[244:247], a[188:191], v[56:59]\n" \
"ds_read_b128 a[240:243], v247 offset:38912\n" \
"v_cvt_pk_bf16_f32 v120, v120, v121\n" \
"v_mfma_f32_16x16x32_bf16 v[8:11], v[192:195], a[184:187], v[8:11]\n" \
"ds_read_b128 a[244:247], v247 offset:47104\n" \
"global_load_lds_dwordx4 v253, s[50:51] offset:-3072\n" \
"v_mfma_f32_16x16x32_bf16 v[8:11], v[196:199], a[188:191], v[8:11]\n" \
"v_exp_f32 v123, v123\n" \
"v_mfma_f32_16x16x32_bf16 v[52:55], a[248:251], a[168:171], v[52:55]\n" \
"v_add_f32 v243, v243, v123\n" \
"v_exp_f32 v124, v124\n" \
"v_mfma_f32_16x16x32_bf16 v[52:55], a[252:255], a[172:175], v[52:55]\n" \
"global_load_lds_dwordx4 v253, s[50:51] offset:-1024\n" \
"v_add_f32 v243, v243, v124\n" \
"v_cvt_pk_bf16_f32 v121, v122, v123\n" \
"v_mfma_f32_16x16x32_bf16 v[60:63], a[248:251], a[184:187], v[60:63]\n" \
"v_exp_f32 v125, v125\n" \
"v_mfma_f32_16x16x32_bf16 v[60:63], a[252:255], a[188:191], v[60:63]\n" \
"ds_read_b128 a[248:251], v247 offset:39936\n" \
"v_add_f32 v243, v243, v125\n" \
"v_mfma_f32_16x16x32_bf16 v[12:15], v[200:203], a[184:187], v[12:15]\n" \
"ds_read_b128 a[252:255], v247 offset:48128\n" \
"global_load_lds_dwordx4 v253, s[50:51] offset:1024\n" \
"v_mfma_f32_16x16x32_bf16 v[12:15], v[204:207], a[188:191], v[12:15]\n" \
"s_waitcnt lgkmcnt(2)\n" \
"s_add_u32 s50, s50, 0x120000\n" \
"s_addc_u32 s51, s51, 0\n" \
"v_mfma_f32_16x16x32_bf16 v[40:43], v[208:211], a[184:187], v[40:43]\n" \
"v_exp_f32 v126, v126\n" \
"v_mfma_f32_16x16x32_bf16 v[40:43], v[212:215], a[188:191], v[40:43]\n" \
"global_load_dword v85, v84, s[22:23]\n" \
"v_mfma_f32_16x16x32_bf16 v[44:47], v[216:219], a[184:187], v[44:47]\n" \
"s_waitcnt vmcnt(11) lgkmcnt(0)\n" \
"v_add_u32 v247, 0x10000, v248\n" \
"v_add_f32 v243, v243, v126\n" \
"v_mfma_f32_16x16x32_bf16 v[44:47], v[220:223], a[188:191], v[44:47]\n" \
"s_barrier\n" \
"v_cvt_pk_bf16_f32 v122, v124, v125\n" \
"v_mfma_f32_16x16x32_bf16 a[64:67], a[192:195], v[64:67], a[64:67]\n" \
"v_exp_f32 v127, v127\n" \
"v_mfma_f32_16x16x32_bf16 a[64:67], a[196:199], v[96:99], a[64:67]\n" \
"v_add_f32 v243, v243, v127\n" \
"v_cvt_pk_bf16_f32 v123, v126, v127\n" \
"v_mfma_f32_16x16x32_bf16 a[96:99], a[192:195], v[72:75], a[96:99]\n" \
"s_lshl_b32 s70, s61, 14\n" \
"ds_read_b128 v[192:195], v247 offset:16384\n" \
"v_cmp_gt_i32 vcc, 0, v251\n" \
"v_cmp_gt_i32 s[64:65], 1, v251\n" \
"v_cmp_gt_i32 s[66:67], 2, v251\n" \
"v_cndmask_b32 v8, v8, v254, vcc\n" \
"v_cndmask_b32 v16, v16, v254, vcc\n" \
"v_cmp_gt_i32 vcc, 3, v251\n" \
"v_add_u32 v124, s70, v248\n" \
"v_mfma_f32_16x16x32_bf16 a[96:99], a[196:199], v[104:107], a[96:99]\n" \
"v_cndmask_b32 v9, v9, v254, s[64:65]\n" \
"v_cndmask_b32 v17, v17, v254, s[64:65]\n" \
"v_cmp_gt_i32 s[64:65], 16, v251\n" \
"v_cndmask_b32 v10, v10, v254, s[66:67]\n" \
"v_cndmask_b32 v18, v18, v254, s[66:67]\n" \
"v_cmp_gt_i32 s[66:67], 17, v251\n" \
"v_xor_b32 v125, 0x4000, v124\n" \
"s_lshl_b32 s70, s61, 9\n" \
"v_mfma_f32_16x16x32_bf16 v[128:131], a[192:195], v[80:83], v[128:131]\n" \
"s_add_u32 s70, s70, 110592\n" \
"v_cndmask_b32 v11, v11, v254, vcc\n" \
"v_cndmask_b32 v19, v19, v254, vcc\n" \
"v_cmp_gt_i32 vcc, 18, v251\n" \
"v_cndmask_b32 v12, v12, v254, s[64:65]\n" \
"v_cndmask_b32 v20, v20, v254, s[64:65]\n" \
"v_cmp_gt_i32 s[64:65], 19, v251\n" \
"v_lshrrev_b32 v126, 1, v248\n" \
"v_add_u32 v126, s70, v126\n" \
"v_mfma_f32_16x16x32_bf16 v[128:131], a[196:199], v[112:115], v[128:131]\n" \
"v_xor_b32 v127, 0x200, v126\n" \
"v_lshrrev_b32 v71, 7, v248\n" \
"v_cndmask_b32 v13, v13, v254, s[66:67]\n" \
"v_cndmask_b32 v21, v21, v254, s[66:67]\n" \
"v_cmp_gt_i32 s[66:67], 32, v251\n" \
"v_cndmask_b32 v14, v14, v254, vcc\n" \
"v_cndmask_b32 v22, v22, v254, vcc\n" \
"v_cmp_gt_i32 vcc, 33, v251\n" \
"v_mfma_f32_16x16x32_bf16 v[160:163], a[192:195], v[88:91], v[160:163]\n" \
"ds_read_b128 v[196:199], v247 offset:24576\n" \
"v_and_b32 v76, 0x70, v248\n" \
"v_cndmask_b32 v15, v15, v254, s[64:65]\n" \
"v_cndmask_b32 v23, v23, v254, s[64:65]\n" \
"v_cmp_gt_i32 s[64:65], 34, v251\n" \
"v_cndmask_b32 v40, v40, v254, s[66:67]\n" \
"v_cndmask_b32 v48, v48, v254, s[66:67]\n" \
"v_cmp_gt_i32 s[66:67], 35, v251\n" \
"v_mfma_f32_16x16x32_bf16 v[160:163], a[196:199], v[120:123], v[160:163]\n" \
"s_lshl_b32 s70, s88, 18\n" \
"s_lshl_b32 s71, s61, 16\n" \
"ds_read_b128 a[192:195], v247 offset:0\n" \
"v_cndmask_b32 v41, v41, v254, vcc\n" \
"v_cndmask_b32 v49, v49, v254, vcc\n" \
"v_cmp_gt_i32 vcc, 48, v251\n" \
"v_cndmask_b32 v42, v42, v254, s[64:65]\n" \
"v_cndmask_b32 v50, v50, v254, s[64:65]\n" \
"v_cmp_gt_i32 s[64:65], 49, v251\n" \
"v_mfma_f32_16x16x32_bf16 a[68:71], a[200:203], v[64:67], a[68:71]\n" \
"s_add_u32 s71, s71, s70\n" \
"ds_write_b128 v124, a[64:67] offset:0\n" \
"v_cndmask_b32 v43, v43, v254, s[66:67]\n" \
"v_cndmask_b32 v51, v51, v254, s[66:67]\n" \
"v_cmp_gt_i32 s[66:67], 50, v251\n" \
"v_cndmask_b32 v44, v44, v254, vcc\n" \
"v_cndmask_b32 v52, v52, v254, vcc\n" \
"v_cmp_gt_i32 vcc, 51, v251\n" \
"v_mfma_f32_16x16x32_bf16 a[68:71], a[204:207], v[96:99], a[68:71]\n" \
"ds_read_b128 a[196:199], v247 offset:8192\n" \
"v_lshlrev_b32 v77, 4, v71\n" \
"v_cndmask_b32 v45, v45, v254, s[64:65]\n" \
"v_cndmask_b32 v53, v53, v254, s[64:65]\n" \
"v_cndmask_b32 v46, v46, v254, s[66:67]\n" \
"v_cndmask_b32 v54, v54, v254, s[66:67]\n" \
"v_cndmask_b32 v47, v47, v254, vcc\n" \
"v_cndmask_b32 v55, v55, v254, vcc\n" \
"v_mfma_f32_16x16x32_bf16 a[100:103], a[200:203], v[72:75], a[100:103]\n" \
"ds_read_b128 v[200:203], v247 offset:17408\n" \
"v_xor_b32 v77, v77, v76\n" \
"v_exp_f32 v8, v8\n" \
"v_mfma_f32_16x16x32_bf16 a[100:103], a[204:207], v[104:107], a[100:103]\n" \
"s_add_u32 s42, s20, s71\n" \
"s_addc_u32 s43, s21, 0\n" \
"v_add_f32 v243, v243, v8\n" \
"v_exp_f32 v9, v9\n" \
"v_lshl_add_u32 v252, v71, 12, v77\n" \
"v_mfma_f32_16x16x32_bf16 v[132:135], a[200:203], v[80:83], v[132:135]\n" \
"s_add_u32 s44, s42, 31744\n" \
"s_addc_u32 s45, s43, 0\n" \
"ds_write_b128 v124, a[96:99] offset:8192\n" \
"v_add_f32 v243, v243, v9\n" \
"v_exp_f32 v10, v10\n" \
"v_mfma_f32_16x16x32_bf16 v[132:135], a[204:207], v[112:115], v[132:135]\n" \
"s_max_u32 s70, s70, 32768\n" \
"ds_write_b64 v126, v[244:245]\n" \
"v_add_f32 v243, v243, v10\n" \
"v_cvt_pk_bf16_f32 v8, v8, v9\n" \
"v_exp_f32 v11, v11\n" \
"v_mfma_f32_16x16x32_bf16 v[164:167], a[200:203], v[88:91], v[164:167]\n" \
"s_sub_u32 s70, s70, 32768\n" \
"ds_read_b128 v[204:207], v247 offset:25600\n" \
"v_add_f32 v243, v243, v11\n" \
"v_exp_f32 v12, v12\n" \
"s_and_b32 s71, s61, 1\n" \
"v_mfma_f32_16x16x32_bf16 v[164:167], a[204:207], v[120:123], v[164:167]\n" \
"s_lshl_b32 s71, s71, 2\n" \
"ds_read_b128 a[200:203], v247 offset:1024\n" \
"v_add_f32 v243, v243, v12\n" \
"v_cvt_pk_bf16_f32 v9, v10, v11\n" \
"v_exp_f32 v13, v13\n" \
"v_and_or_b32 v71, v71, 3, s71\n" \
"v_mfma_f32_16x16x32_bf16 a[72:75], a[208:211], v[64:67], a[72:75]\n" \
"s_lshr_b32 s71, s61, 1\n" \
"ds_write_b128 v124, a[68:71] offset:1024\n" \
"v_add_f32 v243, v243, v13\n" \
"v_exp_f32 v14, v14\n" \
"v_mfma_f32_16x16x32_bf16 a[72:75], a[212:215], v[96:99], a[72:75]\n" \
"s_lshl_b32 s71, s71, 11\n" \
"ds_read_b128 a[204:207], v247 offset:9216\n" \
"v_add_f32 v243, v243, v14\n" \
"v_cvt_pk_bf16_f32 v10, v12, v13\n" \
"v_exp_f32 v15, v15\n" \
"v_lshlrev_b32 v77, 4, v71\n" \
"v_mfma_f32_16x16x32_bf16 a[104:107], a[208:211], v[72:75], a[104:107]\n" \
"s_add_u32 s70, s70, s71\n" \
"ds_read_b128 v[208:211], v247 offset:18432\n" \
"v_add_f32 v243, v243, v15\n" \
"v_exp_f32 v40, v40\n" \
"v_xor_b32 v77, v77, v76\n" \
"v_mfma_f32_16x16x32_bf16 a[104:107], a[212:215], v[104:107], a[104:107]\n" \
"s_add_u32 s46, s20, s70\n" \
"s_addc_u32 s47, s21, 0\n" \
"v_add_f32 v243, v243, v40\n" \
"v_cvt_pk_bf16_f32 v11, v14, v15\n" \
"v_exp_f32 v41, v41\n" \
"v_lshl_add_u32 v246, v71, 12, v77\n" \
"v_mfma_f32_16x16x32_bf16 v[136:139], a[208:211], v[80:83], v[136:139]\n" \
"s_lshl_b32 s85, s61, 10\n" \
"ds_write_b128 v124, a[100:103] offset:9216\n" \
"v_add_f32 v243, v243, v41\n" \
"v_exp_f32 v42, v42\n" \
"v_mfma_f32_16x16x32_bf16 v[136:139], a[212:215], v[112:115], v[136:139]\n" \
"s_add_u32 m0, s95, 118784\n" \
"v_add_f32 v243, v243, v42\n" \
"v_cvt_pk_bf16_f32 v40, v40, v41\n" \
"v_exp_f32 v43, v43\n" \
"v_mfma_f32_16x16x32_bf16 v[168:171], a[208:211], v[88:91], v[168:171]\n" \
"ds_read_b128 v[212:215], v247 offset:26624\n" \
"global_load_lds_dwordx4 v252, s[42:43] offset:0\n" \
"v_add_f32 v243, v243, v43\n" \
"v_exp_f32 v44, v44\n" \
"v_mfma_f32_16x16x32_bf16 v[168:171], a[212:215], v[120:123], v[168:171]\n" \
"ds_read_b128 a[208:211], v247 offset:2048\n" \
"v_add_f32 v243, v243, v44\n" \
"v_cvt_pk_bf16_f32 v41, v42, v43\n" \
"v_exp_f32 v45, v45\n" \
"v_mfma_f32_16x16x32_bf16 a[76:79], a[216:219], v[64:67], a[76:79]\n" \
"ds_write_b128 v124, a[72:75] offset:2048\n" \
"v_add_f32 v243, v243, v45\n" \
"v_exp_f32 v46, v46\n" \
"v_mfma_f32_16x16x32_bf16 a[76:79], a[220:223], v[96:99], a[76:79]\n" \
"ds_read_b128 a[212:215], v247 offset:10240\n" \
"global_load_lds_dwordx4 v252, s[42:43] offset:2048\n" \
"v_add_f32 v243, v243, v46\n" \
"v_cvt_pk_bf16_f32 v42, v44, v45\n" \
"v_exp_f32 v47, v47\n" \
"v_mfma_f32_16x16x32_bf16 a[108:111], a[216:219], v[72:75], a[108:111]\n" \
"s_add_u32 s42, s42, 0x180\n" \
"s_addc_u32 s43, s43, 0\n" \
"ds_read_b128 v[216:219], v247 offset:19456\n" \
"v_add_f32 v243, v243, v47\n" \
"v_cvt_pk_bf16_f32 v43, v46, v47\n" \
"v_exp_f32 v24, v24\n" \
"v_mfma_f32_16x16x32_bf16 a[108:111], a[220:223], v[104:107], a[108:111]\n" \
"global_load_lds_dwordx4 v252, s[44:45] offset:1024\n" \
"v_add_f32 v243, v243, v24\n" \
"v_exp_f32 v25, v25\n" \
"v_mfma_f32_16x16x32_bf16 v[140:143], a[216:219], v[80:83], v[140:143]\n" \
"ds_write_b128 v124, a[104:107] offset:10240\n" \
"v_add_f32 v243, v243, v25\n" \
"v_exp_f32 v26, v26\n" \
"v_mfma_f32_16x16x32_bf16 v[140:143], a[220:223], v[112:115], v[140:143]\n" \
"v_add_f32 v243, v243, v26\n" \
"v_cvt_pk_bf16_f32 v24, v24, v25\n" \
"v_exp_f32 v27, v27\n" \
"v_mfma_f32_16x16x32_bf16 v[172:175], a[216:219], v[88:91], v[172:175]\n" \
"ds_read_b128 v[220:223], v247 offset:27648\n" \
"global_load_lds_dwordx4 v252, s[44:45] offset:3072\n" \
"v_add_f32 v243, v243, v27\n" \
"v_exp_f32 v28, v28\n" \
"v_mfma_f32_16x16x32_bf16 v[172:175], a[220:223], v[120:123], v[172:175]\n" \
"s_add_u32 s44, s44, 0x180\n" \
"s_addc_u32 s45, s45, 0\n" \
"ds_read_b128 a[216:219], v247 offset:3072\n" \
"v_add_f32 v243, v243, v28\n" \
"v_cvt_pk_bf16_f32 v25, v26, v27\n" \
"v_exp_f32 v29, v29\n" \
"v_mfma_f32_16x16x32_bf16 a[80:83], a[224:227], v[64:67], a[80:83]\n" \
"ds_write_b128 v124, a[76:79] offset:3072\n" \
"v_add_f32 v243, v243, v29\n" \
"v_exp_f32 v30, v30\n" \
"v_mfma_f32_16x16x32_bf16 a[80:83], a[228:231], v[96:99], a[80:83]\n" \
"s_add_u32 m0, s85, 135168\n" \
"ds_read_b128 a[220:223], v247 offset:11264\n" \
"v_add_f32 v243, v243, v30\n" \
"v_cvt_pk_bf16_f32 v26, v28, v29\n" \
"v_exp_f32 v31, v31\n" \
"v_mfma_f32_16x16x32_bf16 a[112:115], a[224:227], v[72:75], a[112:115]\n" \
"ds_read_b128 v[224:227], v247 offset:20480\n" \
"global_load_lds_dwordx4 v246, s[46:47]\n" \
"v_add_f32 v243, v243, v31\n" \
"v_exp_f32 v56, v56\n" \
"v_mfma_f32_16x16x32_bf16 a[112:115], a[228:231], v[104:107], a[112:115]\n" \
"s_add_u32 s46, s46, 0x180\n" \
"s_addc_u32 s47, s47, 0\n" \
"v_add_f32 v243, v243, v56\n" \
"v_cvt_pk_bf16_f32 v27, v30, v31\n" \
"v_exp_f32 v57, v57\n" \
"v_mfma_f32_16x16x32_bf16 v[144:147], a[224:227], v[80:83], v[144:147]\n" \
"ds_write_b128 v124, a[108:111] offset:11264\n" \
"v_add_f32 v243, v243, v57\n" \
"v_exp_f32 v58, v58\n" \
"v_mfma_f32_16x16x32_bf16 v[144:147], a[228:231], v[112:115], v[144:147]\n" \
"v_add_f32 v243, v243, v58\n" \
"v_cvt_pk_bf16_f32 v56, v56, v57\n" \
"v_exp_f32 v59, v59\n" \
"v_mfma_f32_16x16x32_bf16 v[176:179], a[224:227], v[88:91], v[176:179]\n" \
"ds_read_b128 v[228:231], v247 offset:28672\n" \
"v_add_f32 v243, v243, v59\n" \
"v_exp_f32 v60, v60\n" \
"v_mfma_f32_16x16x32_bf16 v[176:179], a[228:231], v[120:123], v[176:179]\n" \
"ds_read_b128 a[224:227], v247 offset:4096\n" \
"v_add_f32 v243, v243, v60\n" \
"v_cvt_pk_bf16_f32 v57, v58, v59\n" \
"v_exp_f32 v61, v61\n" \
"v_mfma_f32_16x16x32_bf16 a[84:87], a[232:235], v[64:67], a[84:87]\n" \
"ds_write_b128 v124, a[80:83] offset:4096\n" \
"v_add_f32 v243, v243, v61\n" \
"v_exp_f32 v62, v62\n" \
"v_mfma_f32_16x16x32_bf16 a[84:87], a[236:239], v[96:99], a[84:87]\n" \
"ds_read_b128 a[228:231], v247 offset:12288\n" \
"v_add_f32 v243, v243, v62\n" \
"v_cvt_pk_bf16_f32 v58, v60, v61\n" \
"v_exp_f32 v63, v63\n" \
"v_mfma_f32_16x16x32_bf16 a[116:119], a[232:235], v[72:75], a[116:119]\n" \
"ds_read_b128 v[232:235], v247 offset:21504\n" \
"v_add_f32 v243, v243, v63\n" \
"v_cvt_pk_bf16_f32 v59, v62, v63\n" \
"v_exp_f32 v16, v16\n" \
"v_mfma_f32_16x16x32_bf16 a[116:119], a[236:239], v[104:107], a[116:119]\n" \
"v_add_f32 v242, v242, v16\n" \
"v_exp_f32 v17, v17\n" \
"v_mfma_f32_16x16x32_bf16 v[148:151], a[232:235], v[80:83], v[148:151]\n" \
"ds_write_b128 v124, a[112:115] offset:12288\n" \
"v_add_f32 v242, v242, v17\n" \
"v_exp_f32 v18, v18\n" \
"v_mfma_f32_16x16x32_bf16 v[148:151], a[236:239], v[112:115], v[148:151]\n" \
"v_add_f32 v242, v242, v18\n" \
"v_cvt_pk_bf16_f32 v16, v16, v17\n" \
"v_exp_f32 v19, v19\n" \
"v_mfma_f32_16x16x32_bf16 v[180:183], a[232:235], v[88:91], v[180:183]\n" \
"ds_read_b128 v[236:239], v247 offset:29696\n" \
"v_add_f32 v242, v242, v19\n" \
"v_exp_f32 v20, v20\n" \
"v_mfma_f32_16x16x32_bf16 v[180:183], a[236:239], v[120:123], v[180:183]\n" \
"ds_read_b128 a[232:235], v247 offset:5120\n" \
"v_add_f32 v242, v242, v20\n" \
"v_cvt_pk_bf16_f32 v17, v18, v19\n" \
"v_exp_f32 v21, v21\n" \
"v_mfma_f32_16x16x32_bf16 a[88:91], a[240:243], v[64:67], a[88:91]\n" \
"ds_write_b128 v124, a[84:87] offset:5120\n" \
"v_add_f32 v242, v242, v21\n" \
"v_exp_f32 v22, v22\n" \
"v_mfma_f32_16x16x32_bf16 a[88:91], a[244:247], v[96:99], a[88:91]\n" \
"ds_read_b128 a[236:239], v247 offset:13312\n" \
"v_add_f32 v242, v242, v22\n" \
"v_mfma_f32_16x16x32_bf16 a[120:123], a[240:243], v[72:75], a[120:123]\n" \
"ds_read_b128 v[0:3], v247 offset:22528\n" \
"v_cvt_pk_bf16_f32 v18, v20, v21\n" \
"v_mfma_f32_16x16x32_bf16 a[120:123], a[244:247], v[104:107], a[120:123]\n" \
"v_exp_f32 v23, v23\n" \
"v_mfma_f32_16x16x32_bf16 v[152:155], a[240:243], v[80:83], v[152:155]\n" \
"ds_write_b128 v124, a[116:119] offset:13312\n" \
"v_add_f32 v242, v242, v23\n" \
"v_mfma_f32_16x16x32_bf16 v[152:155], a[244:247], v[112:115], v[152:155]\n" \
"v_exp_f32 v48, v48\n" \
"v_mfma_f32_16x16x32_bf16 v[184:187], a[240:243], v[88:91], v[184:187]\n" \
"ds_read_b128 v[4:7], v247 offset:30720\n" \
"v_add_f32 v242, v242, v48\n" \
"v_mfma_f32_16x16x32_bf16 v[184:187], a[244:247], v[120:123], v[184:187]\n" \
"ds_read_b128 a[240:243], v247 offset:6144\n" \
"v_cvt_pk_bf16_f32 v19, v22, v23\n" \
"v_mfma_f32_16x16x32_bf16 a[92:95], a[248:251], v[64:67], a[92:95]\n" \
"ds_write_b128 v124, a[88:91] offset:6144\n" \
"v_mfma_f32_16x16x32_bf16 a[92:95], a[252:255], v[96:99], a[92:95]\n" \
"ds_read_b128 a[244:247], v247 offset:14336\n" \
"v_exp_f32 v49, v49\n" \
"v_mfma_f32_16x16x32_bf16 a[124:127], a[248:251], v[72:75], a[124:127]\n" \
"ds_read_b128 v[32:35], v247 offset:23552\n" \
"v_add_f32 v242, v242, v49\n" \
"v_mfma_f32_16x16x32_bf16 a[124:127], a[252:255], v[104:107], a[124:127]\n" \
"v_exp_f32 v50, v50\n" \
"v_mfma_f32_16x16x32_bf16 v[156:159], a[248:251], v[80:83], v[156:159]\n" \
"ds_write_b128 v124, a[120:123] offset:14336\n" \
"v_mfma_f32_16x16x32_bf16 v[156:159], a[252:255], v[112:115], v[156:159]\n" \
"v_add_f32 v242, v242, v50\n" \
"v_cvt_pk_bf16_f32 v48, v48, v49\n" \
"v_mfma_f32_16x16x32_bf16 v[188:191], a[248:251], v[88:91], v[188:191]\n" \
"ds_read_b128 v[36:39], v247 offset:31744\n" \
"ds_write_b128 v124, a[92:95] offset:7168\n" \
"v_mfma_f32_16x16x32_bf16 v[188:191], a[252:255], v[120:123], v[188:191]\n" \
"s_waitcnt lgkmcnt(3)\n" \
"ds_read_b128 a[248:251], v247 offset:7168\n" \
"v_exp_f32 v51, v51\n" \
"v_mfma_f32_16x16x32_bf16 v[160:163], v[192:195], v[8:11], v[160:163]\n" \
"ds_write_b128 v124, a[124:127] offset:15360\n" \
"v_mfma_f32_16x16x32_bf16 v[160:163], v[196:199], v[40:43], v[160:163]\n" \
"ds_read_b128 a[252:255], v247 offset:15360\n" \
"v_add_f32 v242, v242, v51\n" \
"v_mfma_f32_16x16x32_bf16 v[164:167], v[200:203], v[8:11], v[164:167]\n" \
"v_exp_f32 v52, v52\n" \
"v_mfma_f32_16x16x32_bf16 v[164:167], v[204:207], v[40:43], v[164:167]\n" \
"v_add_f32 v242, v242, v52\n" \
"v_cvt_pk_bf16_f32 v49, v50, v51\n" \
"v_mfma_f32_16x16x32_bf16 v[168:171], v[208:211], v[8:11], v[168:171]\n" \
"v_exp_f32 v53, v53\n" \
"v_mfma_f32_16x16x32_bf16 v[168:171], v[212:215], v[40:43], v[168:171]\n" \
"s_waitcnt vmcnt(0) lgkmcnt(0)\n" \
"v_add_f32 v242, v242, v53\n" \
"v_exp_f32 v54, v54\n" \
"v_mfma_f32_16x16x32_bf16 v[172:175], v[216:219], v[8:11], v[172:175]\n" \
"s_barrier\n" \
"v_add_f32 v242, v242, v54\n" \
"v_cvt_pk_bf16_f32 v50, v52, v53\n" \
"v_mfma_f32_16x16x32_bf16 v[172:175], v[220:223], v[40:43], v[172:175]\n" \
"s_add_u32 m0, s84, 90112\n" \
"ds_read_b64 v[244:245], v127\n" \
"v_exp_f32 v55, v55\n" \
"v_mfma_f32_16x16x32_bf16 v[176:179], v[224:227], v[8:11], v[176:179]\n" \
"ds_read_b128 v[64:67], v125 offset:0\n" \
"global_load_lds_dwordx4 v253, s[48:49] offset:-4096\n" \
"v_mfma_f32_16x16x32_bf16 v[176:179], v[228:231], v[40:43], v[176:179]\n" \
"ds_read_b128 v[68:71], v125 offset:1024\n" \
"global_load_lds_dwordx4 v253, s[48:49] offset:-2048\n" \
"v_mfma_f32_16x16x32_bf16 v[180:183], v[232:235], v[8:11], v[180:183]\n" \
"ds_read_b128 v[72:75], v125 offset:2048\n" \
"v_add_f32 v242, v242, v55\n" \
"v_mfma_f32_16x16x32_bf16 v[180:183], v[236:239], v[40:43], v[180:183]\n" \
"ds_read_b128 v[76:79], v125 offset:3072\n" \
"global_load_lds_dwordx4 v253, s[48:49] offset:0\n" \
"v_mfma_f32_16x16x32_bf16 v[184:187], v[0:3], v[8:11], v[184:187]\n" \
"s_add_u32 s48, s48, 0x300000\n" \
"s_addc_u32 s49, s49, 0\n" \
"ds_read_b128 v[80:83], v125 offset:4096\n" \
"v_mfma_f32_16x16x32_bf16 v[184:187], v[4:7], v[40:43], v[184:187]\n" \
"ds_read_b128 v[84:87], v125 offset:5120\n" \
"global_load_lds_dwordx4 v253, s[50:51] offset:-3072\n" \
"v_mfma_f32_16x16x32_bf16 v[188:191], v[32:35], v[8:11], v[188:191]\n" \
"s_waitcnt lgkmcnt(5)\n" \
"ds_read_b128 v[88:91], v125 offset:6144\n" \
"v_cvt_pk_bf16_f32 v51, v54, v55\n" \
"v_add_f32 v128, v128, v64\n" \
"v_add_f32 v129, v129, v65\n" \
"v_add_f32 v130, v130, v66\n" \
"v_add_f32 v131, v131, v67\n" \
"v_mfma_f32_16x16x32_bf16 v[188:191], v[36:39], v[40:43], v[188:191]\n" \
"s_waitcnt lgkmcnt(5)\n" \
"ds_read_b128 v[92:95], v125 offset:7168\n" \
"global_load_lds_dwordx4 v253, s[50:51] offset:-1024\n" \
"v_add_f32 v132, v132, v68\n" \
"v_add_f32 v133, v133, v69\n" \
"v_add_f32 v134, v134, v70\n" \
"v_add_f32 v135, v135, v71\n" \
"v_mfma_f32_16x16x32_bf16 v[160:163], a[192:195], v[24:27], v[160:163]\n" \
"s_waitcnt lgkmcnt(5)\n" \
"ds_read_b128 v[96:99], v125 offset:8192\n" \
"global_load_lds_dwordx4 v253, s[50:51] offset:1024\n" \
"v_add_f32 v136, v136, v72\n" \
"v_add_f32 v137, v137, v73\n" \
"v_add_f32 v138, v138, v74\n" \
"v_add_f32 v139, v139, v75\n" \
"v_mfma_f32_16x16x32_bf16 v[160:163], a[196:199], v[56:59], v[160:163]\n" \
"s_add_u32 s50, s50, 0x300000\n" \
"s_addc_u32 s51, s51, 0\n" \
"ds_read_b128 v[100:103], v125 offset:9216\n" \
"v_mfma_f32_16x16x32_bf16 v[164:167], a[200:203], v[24:27], v[164:167]\n" \
"s_add_u32 m0, s95, 65536\n" \
"ds_read_b128 v[104:107], v125 offset:10240\n" \
"s_movk_i32 s89, 0xc0\n" \
"v_mfma_f32_16x16x32_bf16 v[164:167], a[204:207], v[56:59], v[164:167]\n" \
"s_movk_i32 s90, 288\n" \
"ds_read_b128 v[108:111], v125 offset:11264\n" \
"global_load_lds_dwordx4 v252, s[42:43] offset:0\n" \
"v_lshrrev_b32 v211, 4, v248\n" \
"v_lshrrev_b32 v214, 8, v248\n" \
"v_and_b32 v215, 7, v211\n" \
"v_xor_b32 v214, v214, v215\n" \
"v_mfma_f32_16x16x32_bf16 v[168:171], a[208:211], v[24:27], v[168:171]\n" \
"s_lshr_b32 s71, s61, 1\n" \
"ds_read_b128 v[112:115], v125 offset:12288\n" \
"s_lshl_b32 s70, s71, 11\n" \
"v_mfma_f32_16x16x32_bf16 v[168:171], a[212:215], v[56:59], v[168:171]\n" \
"s_add_u32 s70, s70, 118784\n" \
"ds_read_b128 v[116:119], v125 offset:13312\n" \
"global_load_lds_dwordx4 v252, s[42:43] offset:2048\n" \
"v_and_b32 v216, 15, v211\n" \
"v_lshlrev_b32 v213, 7, v216\n" \
"v_lshl_add_u32 v213, v214, 4, v213\n" \
"v_mfma_f32_16x16x32_bf16 v[172:175], a[216:219], v[24:27], v[172:175]\n" \
"s_add_u32 s42, s42, 0x400\n" \
"s_addc_u32 s43, s43, 0\n" \
"ds_read_b128 v[120:123], v125 offset:14336\n" \
"v_add_u32 v205, s70, v213\n" \
"v_xor_b32 v206, 64, v205\n" \
"v_lshrrev_b32 v216, 2, v216\n" \
"v_lshl_add_u32 v216, v216, 9, v213\n" \
"v_add_u32 v207, 135168, v216\n" \
"v_xor_b32 v208, 64, v207\n" \
"Lpg8754_%=:\n" \
"Lst1x2_%=:\n" \
"s_cbranch_execz Lpg11036_%=\n" \
"v_mfma_f32_16x16x32_bf16 v[172:175], a[220:223], v[56:59], v[172:175]\n" \
"s_and_b32 s70, s61, 1\n" \
"ds_read_b128 v[124:127], v125 offset:15360\n" \
"global_load_lds_dwordx4 v252, s[44:45] offset:1024\n" \
"v_mfma_f32_16x16x32_bf16 v[176:179], a[224:227], v[24:27], v[176:179]\n" \
"s_lshl_b32 s70, s70, 9\n" \
"s_add_u32 s70, s70, 139264\n" \
"s_lshl_b32 s71, s71, 5\n" \
"v_mfma_f32_16x16x32_bf16 v[176:179], a[228:231], v[56:59], v[176:179]\n" \
"s_waitcnt lgkmcnt(12)\n" \
"global_load_lds_dwordx4 v252, s[44:45] offset:3072\n" \
"v_and_b32 v213, 3, v211\n" \
"v_add_f32 v140, v140, v76\n" \
"v_add_f32 v141, v141, v77\n" \
"v_add_f32 v142, v142, v78\n" \
"v_add_f32 v143, v143, v79\n" \
"v_mfma_f32_16x16x32_bf16 v[180:183], a[232:235], v[24:27], v[180:183]\n" \
"s_add_u32 s44, s44, 0x400\n" \
"s_addc_u32 s45, s45, 0\n" \
"s_add_u32 m0, s85, 81920\n" \
"v_mfma_f32_16x16x32_bf16 v[180:183], a[236:239], v[56:59], v[180:183]\n" \
"s_waitcnt lgkmcnt(0)\n" \
"global_load_lds_dwordx4 v246, s[46:47]\n" \
"v_lshlrev_b32 v213, 3, v213\n" \
"v_mfma_f32_16x16x32_bf16 v[184:187], a[240:243], v[24:27], v[184:187]\n" \
"s_barrier\n" \
"v_and_or_b32 v213, v248, s89, v213\n" \
"v_mfma_f32_16x16x32_bf16 v[184:187], a[244:247], v[56:59], v[184:187]\n" \
"s_add_u32 s46, s46, 0x400\n" \
"s_addc_u32 s47, s47, 0\n" \
"v_bfe_u32 v214, v211, 4, 1\n" \
"v_mfma_f32_16x16x32_bf16 v[188:191], a[248:251], v[24:27], v[188:191]\n" \
"s_add_u32 m0, s95, 0\n" \
"v_mad_u32_u24 v213, v214, s90, v213\n" \
"v_lshrrev_b32 v214, 5, v211\n" \
"v_mul_u32_u24 v214, 6144, v214\n" \
"v_add3_u32 v213, v213, v214, s70\n" \
"v_xor_b32 v209, s71, v213\n" \
"v_xor_b32 v210, 32, v209\n" \
"v_mfma_f32_16x16x32_bf16 v[188:191], a[252:255], v[56:59], v[188:191]\n" \
"ds_read_b128 a[64:67], v207 offset:0\n" \
"ds_read_b64_tr_b16 a[96:97], v209 offset:2048\n" \
"v_add_f32 v144, v144, v80\n" \
"v_add_f32 v145, v145, v81\n" \
"v_add_f32 v146, v146, v82\n" \
"v_add_f32 v147, v147, v83\n" \
"v_mfma_f32_16x16x32_bf16 v[128:131], a[192:195], v[16:19], v[128:131]\n" \
"ds_read_b64_tr_b16 a[98:99], v209 offset:3072\n" \
"global_load_lds_dwordx4 v252, s[42:43] offset:0\n" \
"v_add_f32 v148, v148, v84\n" \
"v_add_f32 v149, v149, v85\n" \
"v_add_f32 v150, v150, v86\n" \
"v_add_f32 v151, v151, v87\n" \
"v_mfma_f32_16x16x32_bf16 v[128:131], a[196:199], v[48:51], v[128:131]\n" \
"ds_read_b128 a[68:71], v208 offset:0\n" \
"v_add_f32 v152, v152, v88\n" \
"v_add_f32 v153, v153, v89\n" \
"v_add_f32 v154, v154, v90\n" \
"v_add_f32 v155, v155, v91\n" \
"v_mfma_f32_16x16x32_bf16 v[132:135], a[200:203], v[16:19], v[132:135]\n" \
"ds_read_b64_tr_b16 a[100:101], v209 offset:14336\n" \
"global_load_lds_dwordx4 v252, s[42:43] offset:2048\n" \
"ds_read_b64_tr_b16 a[102:103], v209 offset:15360\n" \
"v_add_f32 v156, v156, v92\n" \
"v_add_f32 v157, v157, v93\n" \
"v_add_f32 v158, v158, v94\n" \
"v_mfma_f32_16x16x32_bf16 v[132:135], a[204:207], v[48:51], v[132:135]\n" \
"s_add_u32 s42, s42, 0xfffffb00\n" \
"s_addc_u32 s43, s43, -1\n" \
"ds_read_b128 a[72:75], v205 offset:0\n" \
"v_add_f32 v159, v159, v95\n" \
"v_mfma_f32_16x16x32_bf16 v[136:139], a[208:211], v[16:19], v[136:139]\n" \
"ds_read_b64_tr_b16 a[104:105], v209 offset:0\n" \
"global_load_lds_dwordx4 v252, s[44:45] offset:1024\n" \
"v_add_f32 v160, v160, v96\n" \
"v_add_f32 v161, v161, v97\n" \
"v_add_f32 v162, v162, v98\n" \
"v_add_f32 v163, v163, v99\n" \
"v_mfma_f32_16x16x32_bf16 v[136:139], a[212:215], v[48:51], v[136:139]\n" \
"ds_read_b128 a[76:79], v206 offset:0\n" \
"ds_read_b64_tr_b16 a[106:107], v209 offset:1024\n" \
"v_add_f32 v164, v164, v100\n" \
"v_add_f32 v165, v165, v101\n" \
"v_add_f32 v166, v166, v102\n" \
"v_add_f32 v167, v167, v103\n" \
"v_mfma_f32_16x16x32_bf16 v[140:143], a[216:219], v[16:19], v[140:143]\n" \
"ds_read_b64_tr_b16 a[108:109], v209 offset:12288\n" \
"v_add_f32 v168, v168, v104\n" \
"v_add_f32 v169, v169, v105\n" \
"v_add_f32 v170, v170, v106\n" \
"v_add_f32 v171, v171, v107\n" \
"v_mfma_f32_16x16x32_bf16 v[140:143], a[220:223], v[48:51], v[140:143]\n" \
"ds_read_b64_tr_b16 a[110:111], v209 offset:13312\n" \
"global_load_lds_dwordx4 v252, s[44:45] offset:3072\n" \
"v_add_f32 v172, v172, v108\n" \
"v_add_f32 v173, v173, v109\n" \
"v_add_f32 v174, v174, v110\n" \
"v_add_f32 v175, v175, v111\n" \
"v_mfma_f32_16x16x32_bf16 v[144:147], a[224:227], v[16:19], v[144:147]\n" \
"ds_read_b128 a[80:83], v205 offset:4096\n" \
"ds_read_b64_tr_b16 a[112:113], v210 offset:0\n" \
"v_add_f32 v176, v176, v112\n" \
"v_add_f32 v177, v177, v113\n" \
"v_add_f32 v178, v178, v114\n" \
"v_mfma_f32_16x16x32_bf16 v[144:147], a[228:231], v[48:51], v[144:147]\n" \
"s_add_u32 s44, s44, 0xfffffb00\n" \
"s_addc_u32 s45, s45, -1\n" \
"ds_read_b64_tr_b16 a[114:115], v210 offset:1024\n" \
"v_mfma_f32_16x16x32_bf16 v[148:151], a[232:235], v[16:19], v[148:151]\n" \
"s_add_u32 m0, s85, 16384\n" \
"ds_read_b64_tr_b16 a[116:117], v210 offset:12288\n" \
"v_add_f32 v179, v179, v115\n" \
"v_mfma_f32_16x16x32_bf16 v[148:151], a[236:239], v[48:51], v[148:151]\n" \
"ds_read_b128 a[84:87], v206 offset:4096\n" \
"ds_read_b64_tr_b16 a[118:119], v210 offset:13312\n" \
"v_add_f32 v180, v180, v116\n" \
"v_add_f32 v181, v181, v117\n" \
"v_add_f32 v182, v182, v118\n" \
"v_add_f32 v183, v183, v119\n" \
"v_mfma_f32_16x16x32_bf16 v[152:155], a[240:243], v[16:19], v[152:155]\n" \
"ds_read_b128 a[88:91], v205 offset:8192\n" \
"global_load_lds_dwordx4 v246, s[46:47]\n" \
"v_add_f32 v184, v184, v120\n" \
"v_add_f32 v185, v185, v121\n" \
"v_add_f32 v186, v186, v122\n" \
"v_add_f32 v187, v187, v123\n" \
"v_mfma_f32_16x16x32_bf16 v[152:155], a[244:247], v[48:51], v[152:155]\n" \
"ds_read_b128 a[92:95], v206 offset:8192\n" \
"v_add_f32 v188, v188, v124\n" \
"v_add_f32 v189, v189, v125\n" \
"v_mfma_f32_16x16x32_bf16 v[156:159], a[248:251], v[16:19], v[156:159]\n" \
"v_add_f32 v190, v190, v126\n" \
"v_add_f32 v191, v191, v127\n" \
"v_mfma_f32_16x16x32_bf16 v[156:159], a[252:255], v[48:51], v[156:159]\n" \
"s_waitcnt lgkmcnt(4)\n" \
"v_add_f32 v242, v242, v244\n" \
"v_add_f32 v243, v243, v245\n" \
"Ljoin_%=:\n" \
"v_mfma_f32_16x16x32_bf16 v[32:35], a[96:99], a[64:67], 0\n" \
"ds_read_b128 a[192:195], v205 offset:12288\n" \
"v_mfma_f32_16x16x32_bf16 v[32:35], a[100:103], a[68:71], v[32:35]\n" \
"s_add_u32 s46, s46, 0xfffffb00\n" \
"s_addc_u32 s47, s47, -1\n" \
"s_add_u32 m0, s84, 24576\n" \
"v_mfma_f32_16x16x32_bf16 v[40:43], a[96:99], a[72:75], 0\n" \
"ds_read_b128 a[196:199], v206 offset:12288\n" \
"global_load_lds_dwordx4 v253, s[48:49] offset:-4096\n" \
"v_mfma_f32_16x16x32_bf16 v[40:43], a[100:103], a[76:79], v[40:43]\n" \
"ds_read_b64_tr_b16 a[200:201], v210 offset:2048\n" \
"v_mov_b32 v232, v242\n" \
"v_mfma_f32_16x16x32_bf16 v[0:3], a[104:107], a[72:75], 0\n" \
"ds_read_b64_tr_b16 a[202:203], v210 offset:3072\n" \
"global_load_lds_dwordx4 v253, s[48:49] offset:-2048\n" \
"v_mfma_f32_16x16x32_bf16 v[0:3], a[108:111], a[76:79], v[0:3]\n" \
"s_waitcnt lgkmcnt(3)\n" \
"v_permlane16_swap_b32 v232, v242\n" \
"v_mfma_f32_16x16x32_bf16 v[4:7], a[112:115], a[72:75], 0\n" \
"ds_read_b64_tr_b16 a[204:205], v210 offset:14336\n" \
"v_add_f32 v242, v242, v232\n" \
"v_mfma_f32_16x16x32_bf16 v[4:7], a[116:119], a[76:79], v[4:7]\n" \
"ds_read_b64_tr_b16 a[206:207], v210 offset:15360\n" \
"global_load_lds_dwordx4 v253, s[48:49] offset:0\n" \
"v_mfma_f32_16x16x32_bf16 v[48:51], a[96:99], a[80:83], 0\n" \
"s_add_u32 s48, s48, 0xffc40000\n" \
"s_addc_u32 s49, s49, -1\n" \
"ds_read_b64_tr_b16 a[208:209], v209 offset:4096\n" \
"v_mfma_f32_16x16x32_bf16 v[48:51], a[100:103], a[84:87], v[48:51]\n" \
"v_mov_b32 v232, v242\n" \
"v_mfma_f32_16x16x32_bf16 v[8:11], a[104:107], a[80:83], 0\n" \
"ds_read_b64_tr_b16 a[210:211], v209 offset:5120\n" \
"global_load_lds_dwordx4 v253, s[50:51] offset:-3072\n" \
"v_mfma_f32_16x16x32_bf16 v[8:11], a[108:111], a[84:87], v[8:11]\n" \
"ds_read_b64_tr_b16 a[212:213], v209 offset:16384\n" \
"v_mfma_f32_16x16x32_bf16 v[12:15], a[112:115], a[80:83], 0\n" \
"ds_read_b64_tr_b16 a[214:215], v209 offset:17408\n" \
"v_mfma_f32_16x16x32_bf16 v[12:15], a[116:119], a[84:87], v[12:15]\n" \
"global_load_lds_dwordx4 v253, s[50:51] offset:-1024\n" \
"v_mfma_f32_16x16x32_bf16 v[56:59], a[96:99], a[88:91], 0\n" \
"ds_read_b64_tr_b16 a[216:217], v210 offset:4096\n" \
"v_mfma_f32_16x16x32_bf16 v[56:59], a[100:103], a[92:95], v[56:59]\n" \
"ds_read_b64_tr_b16 a[218:219], v210 offset:5120\n" \
"v_mfma_f32_16x16x32_bf16 v[16:19], a[104:107], a[88:91], 0\n" \
"ds_read_b64_tr_b16 a[220:221], v210 offset:16384\n" \
"global_load_lds_dwordx4 v253, s[50:51] offset:1024\n" \
"v_mfma_f32_16x16x32_bf16 v[16:19], a[108:111], a[92:95], v[16:19]\n" \
"s_add_u32 s50, s50, 0xffc40000\n" \
"s_addc_u32 s51, s51, -1\n" \
"v_mfma_f32_16x16x32_bf16 v[20:23], a[112:115], a[88:91], 0\n" \
"ds_read_b64_tr_b16 a[222:223], v210 offset:17408\n" \
"v_mfma_f32_16x16x32_bf16 v[20:23], a[116:119], a[92:95], v[20:23]\n" \
"s_waitcnt lgkmcnt(3)\n" \
"v_add_u32 v207, 0xffff3000, v207\n" \
"v_add_u32 v208, 0xffff3000, v208\n" \
"v_mfma_f32_16x16x32_bf16 v[64:67], a[96:99], a[192:195], 0\n" \
"v_add_u32 v205, 0xffff3000, v205\n" \
"v_add_u32 v206, 0xffff3000, v206\n" \
"v_mfma_f32_16x16x32_bf16 v[64:67], a[100:103], a[196:199], v[64:67]\n" \
"v_add_u32 v209, 0xffff3000, v209\n" \
"v_add_u32 v210, 0xffff3000, v210\n" \
"v_mfma_f32_16x16x32_bf16 v[24:27], a[104:107], a[192:195], 0\n" \
"v_permlane32_swap_b32 v232, v242\n" \
"v_mfma_f32_16x16x32_bf16 v[24:27], a[108:111], a[196:199], v[24:27]\n" \
"v_add_f32 v242, v242, v232\n" \
"v_mfma_f32_16x16x32_bf16 v[28:31], a[112:115], a[192:195], 0\n" \
"s_waitcnt vmcnt(11) lgkmcnt(0)\n" \
"v_rcp_f32 v232, v242\n" \
"v_mfma_f32_16x16x32_bf16 v[28:31], a[116:119], a[196:199], v[28:31]\n" \
"s_barrier\n" \
"v_mfma_f32_16x16x32_bf16 v[72:75], a[64:67], a[208:211], 0\n" \
"s_add_u32 m0, s95, 118784\n" \
"ds_read_b64_tr_b16 a[96:97], v209 offset:2048\n" \
"v_add_u32 v233, 0x1000, v249\n" \
"v_mfma_f32_16x16x32_bf16 v[72:75], a[68:71], a[212:215], v[72:75]\n" \
"ds_read_b64_tr_b16 a[98:99], v209 offset:3072\n" \
"global_load_lds_dwordx4 v252, s[42:43] offset:0\n" \
"v_mfma_f32_16x16x32_bf16 v[44:47], a[200:203], a[72:75], 0\n" \
"ds_read_b128 a[64:67], v207 offset:0\n" \
"v_mul_f32 v242, 0x4d800000, v242\n" \
"v_mfma_f32_16x16x32_bf16 v[44:47], a[204:207], a[76:79], v[44:47]\n" \
"v_mul_f32 v128, v128, v232\n" \
"v_mul_f32 v129, v129, v232\n" \
"v_mfma_f32_16x16x32_bf16 v[80:83], a[72:75], a[208:211], 0\n" \
"ds_read_b128 a[68:71], v208 offset:0\n" \
"v_mul_f32 v130, v130, v232\n" \
"v_mfma_f32_16x16x32_bf16 v[80:83], a[76:79], a[212:215], v[80:83]\n" \
"ds_read_b64_tr_b16 a[100:101], v209 offset:14336\n" \
"global_load_lds_dwordx4 v252, s[42:43] offset:2048\n" \
"v_mfma_f32_16x16x32_bf16 v[84:87], a[72:75], a[216:219], 0\n" \
"s_add_u32 s42, s42, 0x80\n" \
"s_addc_u32 s43, s43, 0\n" \
"ds_read_b64_tr_b16 a[102:103], v209 offset:15360\n" \
"v_mfma_f32_16x16x32_bf16 v[84:87], a[76:79], a[220:223], v[84:87]\n" \
"ds_read_b64_tr_b16 a[104:105], v209 offset:0\n" \
"v_mul_f32 v131, v131, v232\n" \
"v_mfma_f32_16x16x32_bf16 v[52:55], a[200:203], a[80:83], 0\n" \
"ds_read_b128 a[72:75], v205 offset:0\n" \
"v_mul_f32 v132, v132, v232\n" \
"v_mfma_f32_16x16x32_bf16 v[52:55], a[204:207], a[84:87], v[52:55]\n" \
"ds_read_b128 a[76:79], v206 offset:0\n" \
"global_load_lds_dwordx4 v252, s[44:45] offset:1024\n" \
"v_mfma_f32_16x16x32_bf16 v[88:91], a[80:83], a[208:211], 0\n" \
"ds_read_b64_tr_b16 a[106:107], v209 offset:1024\n" \
"v_mul_f32 v133, v133, v232\n" \
"v_mfma_f32_16x16x32_bf16 v[88:91], a[84:87], a[212:215], v[88:91]\n" \
"v_mul_f32 v134, v134, v232\n" \
"v_mul_f32 v135, v135, v232\n" \
"v_mfma_f32_16x16x32_bf16 v[92:95], a[80:83], a[216:219], 0\n" \
"ds_read_b64_tr_b16 a[108:109], v209 offset:12288\n" \
"v_fma_f32 v242, v128, v129, v242\n" \
"v_mfma_f32_16x16x32_bf16 v[92:95], a[84:87], a[220:223], v[92:95]\n" \
"ds_read_b64_tr_b16 a[110:111], v209 offset:13312\n" \
"global_load_lds_dwordx4 v252, s[44:45] offset:3072\n" \
"v_mfma_f32_16x16x32_bf16 v[60:63], a[200:203], a[88:91], 0\n" \
"s_add_u32 s44, s44, 0x80\n" \
"s_addc_u32 s45, s45, 0\n" \
"ds_read_b128 a[80:83], v205 offset:4096\n" \
"v_mfma_f32_16x16x32_bf16 v[60:63], a[204:207], a[92:95], v[60:63]\n" \
"ds_read_b64_tr_b16 a[112:113], v210 offset:0\n" \
"v_cvt_pk_bf16_f32 v128, v128, v129\n" \
"v_mfma_f32_16x16x32_bf16 v[96:99], a[88:91], a[208:211], 0\n" \
"s_add_u32 m0, s85, 135168\n" \
"ds_read_b128 a[84:87], v206 offset:4096\n" \
"v_fma_f32 v242, v130, v131, v242\n" \
"v_mfma_f32_16x16x32_bf16 v[96:99], a[92:95], a[212:215], v[96:99]\n" \
"ds_read_b64_tr_b16 a[114:115], v210 offset:1024\n" \
"global_load_lds_dwordx4 v246, s[46:47]\n" \
"v_mfma_f32_16x16x32_bf16 v[100:103], a[88:91], a[216:219], 0\n" \
"s_add_u32 s46, s46, 0x80\n" \
"s_addc_u32 s47, s47, 0\n" \
"ds_read_b64_tr_b16 a[116:117], v210 offset:12288\n" \
"v_mfma_f32_16x16x32_bf16 v[100:103], a[92:95], a[220:223], v[100:103]\n" \
"v_cvt_pk_bf16_f32 v129, v130, v131\n" \
"v_fma_f32 v242, v132, v133, v242\n" \
"v_mfma_f32_16x16x32_bf16 v[68:71], a[200:203], a[192:195], 0\n" \
"s_add_u32 m0, s84, 143360\n" \
"ds_read_b128 a[88:91], v205 offset:8192\n" \
"v_cvt_pk_bf16_f32 v130, v132, v133\n" \
"v_mfma_f32_16x16x32_bf16 v[68:71], a[204:207], a[196:199], v[68:71]\n" \
"ds_read_b128 a[92:95], v206 offset:8192\n" \
"global_load_lds_dwordx4 v253, s[48:49] offset:-4096\n" \
"v_mfma_f32_16x16x32_bf16 v[104:107], a[192:195], a[208:211], 0\n" \
"ds_read_b64_tr_b16 a[118:119], v210 offset:13312\n" \
"v_fma_f32 v242, v134, v135, v242\n" \
"v_mfma_f32_16x16x32_bf16 v[104:107], a[196:199], a[212:215], v[104:107]\n" \
"v_cvt_pk_bf16_f32 v131, v134, v135\n" \
"v_mfma_f32_16x16x32_bf16 v[108:111], a[192:195], a[216:219], 0\n" \
"buffer_store_dwordx4 v[128:131], v249, s[56:59], s78 offen offset:0 sc1\n" \
"v_mul_f32 v136, v136, v232\n" \
"v_mul_f32 v137, v137, v232\n" \
"v_mul_f32 v138, v138, v232\n" \
"v_mul_f32 v139, v139, v232\n" \
"v_mfma_f32_16x16x32_bf16 v[108:111], a[196:199], a[220:223], v[108:111]\n" \
"s_waitcnt lgkmcnt(2)\n" \
"global_load_lds_dwordx4 v253, s[48:49] offset:-2048\n" \
"v_mul_f32 v140, v140, v232\n" \
"v_mfma_f32_16x16x32_bf16 v[32:35], a[96:99], a[64:67], v[32:35]\n" \
"ds_read_b128 a[192:195], v205 offset:12288\n" \
"v_mul_f32 v141, v141, v232\n" \
"v_mfma_f32_16x16x32_bf16 v[32:35], a[100:103], a[68:71], v[32:35]\n" \
"v_mul_f32 v142, v142, v232\n" \
"v_mul_f32 v143, v143, v232\n" \
"v_mfma_f32_16x16x32_bf16 v[40:43], a[96:99], a[72:75], v[40:43]\n" \
"ds_read_b128 a[196:199], v206 offset:12288\n" \
"v_fma_f32 v242, v136, v137, v242\n" \
"v_mfma_f32_16x16x32_bf16 v[40:43], a[100:103], a[76:79], v[40:43]\n" \
"ds_read_b64_tr_b16 a[200:201], v210 offset:2048\n" \
"global_load_lds_dwordx4 v253, s[48:49] offset:0\n" \
"v_mfma_f32_16x16x32_bf16 v[0:3], a[104:107], a[72:75], v[0:3]\n" \
"s_add_u32 s48, s48, 0x60000\n" \
"s_addc_u32 s49, s49, 0\n" \
"ds_read_b64_tr_b16 a[202:203], v210 offset:3072\n" \
"v_mfma_f32_16x16x32_bf16 v[0:3], a[108:111], a[76:79], v[0:3]\n" \
"s_waitcnt lgkmcnt(3)\n" \
"v_cvt_pk_bf16_f32 v136, v136, v137\n" \
"v_fma_f32 v242, v138, v139, v242\n" \
"v_mfma_f32_16x16x32_bf16 v[4:7], a[112:115], a[72:75], v[4:7]\n" \
"ds_read_b64_tr_b16 a[204:205], v210 offset:14336\n" \
"v_cvt_pk_bf16_f32 v137, v138, v139\n" \
"v_mfma_f32_16x16x32_bf16 v[4:7], a[116:119], a[76:79], v[4:7]\n" \
"ds_read_b64_tr_b16 a[206:207], v210 offset:15360\n" \
"global_load_lds_dwordx4 v253, s[50:51] offset:-3072\n" \
"v_mfma_f32_16x16x32_bf16 v[48:51], a[96:99], a[80:83], v[48:51]\n" \
"ds_read_b64_tr_b16 a[208:209], v209 offset:4096\n" \
"v_fma_f32 v242, v140, v141, v242\n" \
"v_mfma_f32_16x16x32_bf16 v[48:51], a[100:103], a[84:87], v[48:51]\n" \
"v_cvt_pk_bf16_f32 v138, v140, v141\n" \
"v_fma_f32 v242, v142, v143, v242\n" \
"v_mfma_f32_16x16x32_bf16 v[8:11], a[104:107], a[80:83], v[8:11]\n" \
"ds_read_b64_tr_b16 a[210:211], v209 offset:5120\n" \
"v_cvt_pk_bf16_f32 v139, v142, v143\n" \
"v_mfma_f32_16x16x32_bf16 v[8:11], a[108:111], a[84:87], v[8:11]\n" \
"buffer_store_dwordx4 v[136:139], v249, s[56:59], s78 offen offset:2048 sc1\n" \
"ds_read_b64_tr_b16 a[212:213], v209 offset:16384\n" \
"global_load_lds_dwordx4 v253, s[50:51] offset:-1024\n" \
"v_mul_f32 v144, v144, v232\n" \
"v_mul_f32 v145, v145, v232\n" \
"v_mfma_f32_16x16x32_bf16 v[12:15], a[112:115], a[80:83], v[12:15]\n" \
"ds_read_b64_tr_b16 a[214:215], v209 offset:17408\n" \
"v_mul_f32 v146, v146, v232\n" \
"v_mfma_f32_16x16x32_bf16 v[12:15], a[116:119], a[84:87], v[12:15]\n" \
"v_mul_f32 v147, v147, v232\n" \
"v_mul_f32 v148, v148, v232\n" \
"v_mfma_f32_16x16x32_bf16 v[56:59], a[96:99], a[88:91], v[56:59]\n" \
"ds_read_b64_tr_b16 a[216:217], v210 offset:4096\n" \
"v_mul_f32 v149, v149, v232\n" \
"v_mfma_f32_16x16x32_bf16 v[56:59], a[100:103], a[92:95], v[56:59]\n" \
"ds_read_b64_tr_b16 a[218:219], v210 offset:5120\n" \
"global_load_lds_dwordx4 v253, s[50:51] offset:1024\n" \
"v_mfma_f32_16x16x32_bf16 v[16:19], a[104:107], a[88:91], v[16:19]\n" \
"s_add_u32 s50, s50, 0x60000\n" \
"s_addc_u32 s51, s51, 0\n" \
"ds_read_b64_tr_b16 a[220:221], v210 offset:16384\n" \
"v_mfma_f32_16x16x32_bf16 v[16:19], a[108:111], a[92:95], v[16:19]\n" \
"v_mul_f32 v150, v150, v232\n" \
"v_mul_f32 v151, v151, v232\n" \
"v_mfma_f32_16x16x32_bf16 v[20:23], a[112:115], a[88:91], v[20:23]\n" \
"ds_read_b64_tr_b16 a[222:223], v210 offset:17408\n" \
"v_fma_f32 v242, v144, v145, v242\n" \
"v_mfma_f32_16x16x32_bf16 v[20:23], a[116:119], a[92:95], v[20:23]\n" \
"s_waitcnt lgkmcnt(3)\n" \
"v_add_u32 v207, 0xffff0000, v207\n" \
"v_add_u32 v208, 0xffff0000, v208\n" \
"v_mfma_f32_16x16x32_bf16 v[64:67], a[96:99], a[192:195], v[64:67]\n" \
"v_add_u32 v205, 0xffff0000, v205\n" \
"v_add_u32 v206, 0xffff0000, v206\n" \
"v_mfma_f32_16x16x32_bf16 v[64:67], a[100:103], a[196:199], v[64:67]\n" \
"v_add_u32 v209, 0xffff0000, v209\n" \
"v_add_u32 v210, 0xffff0000, v210\n" \
"v_mfma_f32_16x16x32_bf16 v[24:27], a[104:107], a[192:195], v[24:27]\n" \
"v_cvt_pk_bf16_f32 v144, v144, v145\n" \
"v_fma_f32 v242, v146, v147, v242\n" \
"v_mfma_f32_16x16x32_bf16 v[24:27], a[108:111], a[196:199], v[24:27]\n" \
"v_cvt_pk_bf16_f32 v145, v146, v147\n" \
"v_fma_f32 v242, v148, v149, v242\n" \
"v_mfma_f32_16x16x32_bf16 v[28:31], a[112:115], a[192:195], v[28:31]\n" \
"s_waitcnt vmcnt(13) lgkmcnt(0)\n" \
"v_cvt_pk_bf16_f32 v146, v148, v149\n" \
"v_fma_f32 v242, v150, v151, v242\n" \
"v_mfma_f32_16x16x32_bf16 v[28:31], a[116:119], a[196:199], v[28:31]\n" \
"s_barrier\n" \
"v_mfma_f32_16x16x32_bf16 v[72:75], a[64:67], a[208:211], v[72:75]\n" \
"s_add_u32 m0, s95, 65536\n" \
"ds_read_b64_tr_b16 a[96:97], v209 offset:2048\n" \
"v_cvt_pk_bf16_f32 v147, v150, v151\n" \
"v_mfma_f32_16x16x32_bf16 v[72:75], a[68:71], a[212:215], v[72:75]\n" \
"buffer_store_dwordx4 v[144:147], v233, s[56:59], s78 offen offset:0 sc1\n" \
"ds_read_b64_tr_b16 a[98:99], v209 offset:3072\n" \
"global_load_lds_dwordx4 v252, s[42:43] offset:0\n" \
"v_mul_f32 v152, v152, v232\n" \
"v_mul_f32 v153, v153, v232\n" \
"v_mfma_f32_16x16x32_bf16 v[44:47], a[200:203], a[72:75], v[44:47]\n" \
"ds_read_b128 a[64:67], v207 offset:0\n" \
"v_mul_f32 v154, v154, v232\n" \
"v_mfma_f32_16x16x32_bf16 v[44:47], a[204:207], a[76:79], v[44:47]\n" \
"v_mul_f32 v155, v155, v232\n" \
"v_mul_f32 v156, v156, v232\n" \
"v_mfma_f32_16x16x32_bf16 v[80:83], a[72:75], a[208:211], v[80:83]\n" \
"ds_read_b128 a[68:71], v208 offset:0\n" \
"v_mul_f32 v157, v157, v232\n" \
"v_mfma_f32_16x16x32_bf16 v[80:83], a[76:79], a[212:215], v[80:83]\n" \
"ds_read_b64_tr_b16 a[100:101], v209 offset:14336\n" \
"global_load_lds_dwordx4 v252, s[42:43] offset:2048\n" \
"v_mfma_f32_16x16x32_bf16 v[84:87], a[72:75], a[216:219], v[84:87]\n" \
"s_add_u32 s42, s42, 0x100\n" \
"s_addc_u32 s43, s43, 0\n" \
"ds_read_b64_tr_b16 a[102:103], v209 offset:15360\n" \
"v_mfma_f32_16x16x32_bf16 v[84:87], a[76:79], a[220:223], v[84:87]\n" \
"ds_read_b64_tr_b16 a[104:105], v209 offset:0\n" \
"v_mul_f32 v158, v158, v232\n" \
"v_mfma_f32_16x16x32_bf16 v[52:55], a[200:203], a[80:83], v[52:55]\n" \
"ds_read_b128 a[72:75], v205 offset:0\n" \
"v_mul_f32 v159, v159, v232\n" \
"v_mfma_f32_16x16x32_bf16 v[52:55], a[204:207], a[84:87], v[52:55]\n" \
"ds_read_b128 a[76:79], v206 offset:0\n" \
"global_load_lds_dwordx4 v252, s[44:45] offset:1024\n" \
"v_mfma_f32_16x16x32_bf16 v[88:91], a[80:83], a[208:211], v[88:91]\n" \
"ds_read_b64_tr_b16 a[106:107], v209 offset:1024\n" \
"v_fma_f32 v242, v152, v153, v242\n" \
"v_mfma_f32_16x16x32_bf16 v[88:91], a[84:87], a[212:215], v[88:91]\n" \
"v_cvt_pk_bf16_f32 v152, v152, v153\n" \
"v_fma_f32 v242, v154, v155, v242\n" \
"v_mfma_f32_16x16x32_bf16 v[92:95], a[80:83], a[216:219], v[92:95]\n" \
"ds_read_b64_tr_b16 a[108:109], v209 offset:12288\n" \
"v_cvt_pk_bf16_f32 v153, v154, v155\n" \
"v_mfma_f32_16x16x32_bf16 v[92:95], a[84:87], a[220:223], v[92:95]\n" \
"ds_read_b64_tr_b16 a[110:111], v209 offset:13312\n" \
"global_load_lds_dwordx4 v252, s[44:45] offset:3072\n" \
"v_mfma_f32_16x16x32_bf16 v[60:63], a[200:203], a[88:91], v[60:63]\n" \
"s_add_u32 s44, s44, 0x100\n" \
"s_addc_u32 s45, s45, 0\n" \
"ds_read_b128 a[80:83], v205 offset:4096\n" \
"v_mfma_f32_16x16x32_bf16 v[60:63], a[204:207], a[92:95], v[60:63]\n" \
"ds_read_b64_tr_b16 a[112:113], v210 offset:0\n" \
"v_fma_f32 v242, v156, v157, v242\n" \
"v_mfma_f32_16x16x32_bf16 v[96:99], a[88:91], a[208:211], v[96:99]\n" \
"s_add_u32 m0, s85, 81920\n" \
"ds_read_b128 a[84:87], v206 offset:4096\n" \
"v_cvt_pk_bf16_f32 v154, v156, v157\n" \
"v_mfma_f32_16x16x32_bf16 v[96:99], a[92:95], a[212:215], v[96:99]\n" \
"ds_read_b64_tr_b16 a[114:115], v210 offset:1024\n" \
"global_load_lds_dwordx4 v246, s[46:47]\n" \
"v_mfma_f32_16x16x32_bf16 v[100:103], a[88:91], a[216:219], v[100:103]\n" \
"s_add_u32 s46, s46, 0x100\n" \
"s_addc_u32 s47, s47, 0\n" \
"ds_read_b64_tr_b16 a[116:117], v210 offset:12288\n" \
"v_mfma_f32_16x16x32_bf16 v[100:103], a[92:95], a[220:223], v[100:103]\n" \
"v_fma_f32 v242, v158, v159, v242\n" \
"v_cvt_pk_bf16_f32 v155, v158, v159\n" \
"v_mfma_f32_16x16x32_bf16 v[68:71], a[200:203], a[192:195], v[68:71]\n" \
"buffer_store_dwordx4 v[152:155], v233, s[56:59], s78 offen offset:2048 sc1\n" \
"ds_read_b128 a[88:91], v205 offset:8192\n" \
"s_add_u32 m0, s84, 90112\n" \
"s_add_u32 s78, s78, 0x82000\n" \
"v_mov_b32 v234, v243\n" \
"v_mfma_f32_16x16x32_bf16 v[68:71], a[204:207], a[196:199], v[68:71]\n" \
"ds_read_b128 a[92:95], v206 offset:8192\n" \
"global_load_lds_dwordx4 v253, s[48:49] offset:-4096\n" \
"v_mfma_f32_16x16x32_bf16 v[104:107], a[192:195], a[208:211], v[104:107]\n" \
"ds_read_b64_tr_b16 a[118:119], v210 offset:13312\n" \
"v_mfma_f32_16x16x32_bf16 v[104:107], a[196:199], a[212:215], v[104:107]\n" \
"v_permlane16_swap_b32 v234, v243\n" \
"v_mfma_f32_16x16x32_bf16 v[108:111], a[192:195], a[216:219], v[108:111]\n" \
"v_add_f32 v243, v243, v234\n" \
"v_mov_b32 v234, v243\n" \
"v_mfma_f32_16x16x32_bf16 v[108:111], a[196:199], a[220:223], v[108:111]\n" \
"s_waitcnt lgkmcnt(2)\n" \
"global_load_lds_dwordx4 v253, s[48:49] offset:-2048\n" \
"v_mfma_f32_16x16x32_bf16 v[32:35], a[96:99], a[64:67], v[32:35]\n" \
"ds_read_b128 a[192:195], v205 offset:12288\n" \
"v_mfma_f32_16x16x32_bf16 v[32:35], a[100:103], a[68:71], v[32:35]\n" \
"v_permlane32_swap_b32 v234, v243\n" \
"v_mfma_f32_16x16x32_bf16 v[40:43], a[96:99], a[72:75], v[40:43]\n" \
"ds_read_b128 a[196:199], v206 offset:12288\n" \
"v_add_f32 v243, v243, v234\n" \
"v_mfma_f32_16x16x32_bf16 v[40:43], a[100:103], a[76:79], v[40:43]\n" \
"ds_read_b64_tr_b16 a[200:201], v210 offset:2048\n" \
"global_load_lds_dwordx4 v253, s[48:49] offset:0\n" \
"v_mfma_f32_16x16x32_bf16 v[0:3], a[104:107], a[72:75], v[0:3]\n" \
"s_add_u32 s48, s48, 0xc0000\n" \
"s_addc_u32 s49, s49, 0\n" \
"ds_read_b64_tr_b16 a[202:203], v210 offset:3072\n" \
"v_mfma_f32_16x16x32_bf16 v[0:3], a[108:111], a[76:79], v[0:3]\n" \
"s_waitcnt lgkmcnt(3)\n" \
"v_rcp_f32 v234, v243\n" \
"v_mfma_f32_16x16x32_bf16 v[4:7], a[112:115], a[72:75], v[4:7]\n" \
"ds_read_b64_tr_b16 a[204:205], v210 offset:14336\n" \
"v_add_u32 v235, 0x1000, v249\n" \
"v_mfma_f32_16x16x32_bf16 v[4:7], a[116:119], a[76:79], v[4:7]\n" \
"ds_read_b64_tr_b16 a[206:207], v210 offset:15360\n" \
"global_load_lds_dwordx4 v253, s[50:51] offset:-3072\n" \
"v_mfma_f32_16x16x32_bf16 v[48:51], a[96:99], a[80:83], v[48:51]\n" \
"ds_read_b64_tr_b16 a[208:209], v209 offset:4096\n" \
"v_mul_f32 v243, 0x4d800000, v243\n" \
"v_mfma_f32_16x16x32_bf16 v[48:51], a[100:103], a[84:87], v[48:51]\n" \
"v_mul_f32 v160, v160, v234\n" \
"v_mul_f32 v161, v161, v234\n" \
"v_mfma_f32_16x16x32_bf16 v[8:11], a[104:107], a[80:83], v[8:11]\n" \
"ds_read_b64_tr_b16 a[210:211], v209 offset:5120\n" \
"v_mul_f32 v162, v162, v234\n" \
"v_mfma_f32_16x16x32_bf16 v[8:11], a[108:111], a[84:87], v[8:11]\n" \
"ds_read_b64_tr_b16 a[212:213], v209 offset:16384\n" \
"global_load_lds_dwordx4 v253, s[50:51] offset:-1024\n" \
"v_mfma_f32_16x16x32_bf16 v[12:15], a[112:115], a[80:83], v[12:15]\n" \
"ds_read_b64_tr_b16 a[214:215], v209 offset:17408\n" \
"v_mul_f32 v163, v163, v234\n" \
"v_mfma_f32_16x16x32_bf16 v[12:15], a[116:119], a[84:87], v[12:15]\n" \
"v_mul_f32 v164, v164, v234\n" \
"v_mul_f32 v165, v165, v234\n" \
"v_mfma_f32_16x16x32_bf16 v[56:59], a[96:99], a[88:91], v[56:59]\n" \
"ds_read_b64_tr_b16 a[216:217], v210 offset:4096\n" \
"v_mul_f32 v166, v166, v234\n" \
"v_mfma_f32_16x16x32_bf16 v[56:59], a[100:103], a[92:95], v[56:59]\n" \
"ds_read_b64_tr_b16 a[218:219], v210 offset:5120\n" \
"global_load_lds_dwordx4 v253, s[50:51] offset:1024\n" \
"v_mfma_f32_16x16x32_bf16 v[16:19], a[104:107], a[88:91], v[16:19]\n" \
"s_add_u32 s50, s50, 0xc0000\n" \
"s_addc_u32 s51, s51, 0\n" \
"ds_read_b64_tr_b16 a[220:221], v210 offset:16384\n" \
"v_mfma_f32_16x16x32_bf16 v[16:19], a[108:111], a[92:95], v[16:19]\n" \
"v_mul_f32 v167, v167, v234\n" \
"v_fma_f32 v243, v160, v161, v243\n" \
"v_mfma_f32_16x16x32_bf16 v[20:23], a[112:115], a[88:91], v[20:23]\n" \
"ds_read_b64_tr_b16 a[222:223], v210 offset:17408\n" \
"v_cvt_pk_bf16_f32 v160, v160, v161\n" \
"v_mfma_f32_16x16x32_bf16 v[20:23], a[116:119], a[92:95], v[20:23]\n" \
"s_waitcnt lgkmcnt(3)\n" \
"v_add_u32 v207, 0x1d000, v207\n" \
"v_add_u32 v208, 0x1d000, v208\n" \
"v_mfma_f32_16x16x32_bf16 v[64:67], a[96:99], a[192:195], v[64:67]\n" \
"v_add_u32 v205, 0x1d000, v205\n" \
"v_add_u32 v206, 0x1d000, v206\n" \
"v_mfma_f32_16x16x32_bf16 v[64:67], a[100:103], a[196:199], v[64:67]\n" \
"v_add_u32 v209, 0x1d000, v209\n" \
"v_add_u32 v210, 0x1d000, v210\n" \
"v_mfma_f32_16x16x32_bf16 v[24:27], a[104:107], a[192:195], v[24:27]\n" \
"v_fma_f32 v243, v162, v163, v243\n" \
"v_cvt_pk_bf16_f32 v161, v162, v163\n" \
"v_mfma_f32_16x16x32_bf16 v[24:27], a[108:111], a[196:199], v[24:27]\n" \
"v_fma_f32 v243, v164, v165, v243\n" \
"v_cvt_pk_bf16_f32 v162, v164, v165\n" \
"v_mfma_f32_16x16x32_bf16 v[28:31], a[112:115], a[192:195], v[28:31]\n" \
"s_waitcnt vmcnt(13) lgkmcnt(0)\n" \
"v_fma_f32 v243, v166, v167, v243\n" \
"v_cvt_pk_bf16_f32 v163, v166, v167\n" \
"v_mfma_f32_16x16x32_bf16 v[28:31], a[116:119], a[196:199], v[28:31]\n" \
"s_barrier\n" \
"v_mfma_f32_16x16x32_bf16 v[72:75], a[64:67], a[208:211], v[72:75]\n" \
"buffer_store_dwordx4 v[160:163], v249, s[56:59], s78 offen offset:0 sc1\n" \
"s_add_u32 m0, s95, 0\n" \
"ds_read_b64_tr_b16 a[96:97], v209 offset:2048\n" \
"v_mul_f32 v168, v168, v234\n" \
"v_mul_f32 v169, v169, v234\n" \
"v_mfma_f32_16x16x32_bf16 v[72:75], a[68:71], a[212:215], v[72:75]\n" \
"ds_read_b64_tr_b16 a[98:99], v209 offset:3072\n" \
"global_load_lds_dwordx4 v252, s[42:43] offset:0\n" \
"v_mfma_f32_16x16x32_bf16 v[44:47], a[200:203], a[72:75], v[44:47]\n" \
"ds_read_b128 a[64:67], v207 offset:0\n" \
"v_mul_f32 v170, v170, v234\n" \
"v_mfma_f32_16x16x32_bf16 v[44:47], a[204:207], a[76:79], v[44:47]\n" \
"v_mul_f32 v171, v171, v234\n" \
"v_mul_f32 v172, v172, v234\n" \
"v_mfma_f32_16x16x32_bf16 v[80:83], a[72:75], a[208:211], v[80:83]\n" \
"ds_read_b128 a[68:71], v208 offset:0\n" \
"v_mul_f32 v173, v173, v234\n" \
"v_mfma_f32_16x16x32_bf16 v[80:83], a[76:79], a[212:215], v[80:83]\n" \
"ds_read_b64_tr_b16 a[100:101], v209 offset:14336\n" \
"global_load_lds_dwordx4 v252, s[42:43] offset:2048\n" \
"v_mfma_f32_16x16x32_bf16 v[84:87], a[72:75], a[216:219], v[84:87]\n" \
"s_add_u32 s42, s42, 0x80\n" \
"s_addc_u32 s43, s43, 0\n" \
"ds_read_b64_tr_b16 a[102:103], v209 offset:15360\n" \
"v_mfma_f32_16x16x32_bf16 v[84:87], a[76:79], a[220:223], v[84:87]\n" \
"ds_read_b64_tr_b16 a[104:105], v209 offset:0\n" \
"v_mul_f32 v174, v174, v234\n" \
"v_mfma_f32_16x16x32_bf16 v[52:55], a[200:203], a[80:83], v[52:55]\n" \
"ds_read_b128 a[72:75], v205 offset:0\n" \
"v_mul_f32 v175, v175, v234\n" \
"v_mfma_f32_16x16x32_bf16 v[52:55], a[204:207], a[84:87], v[52:55]\n" \
"ds_read_b128 a[76:79], v206 offset:0\n" \
"global_load_lds_dwordx4 v252, s[44:45] offset:1024\n" \
"v_mfma_f32_16x16x32_bf16 v[88:91], a[80:83], a[208:211], v[88:91]\n" \
"ds_read_b64_tr_b16 a[106:107], v209 offset:1024\n" \
"v_fma_f32 v243, v168, v169, v243\n" \
"v_mfma_f32_16x16x32_bf16 v[88:91], a[84:87], a[212:215], v[88:91]\n" \
"v_cvt_pk_bf16_f32 v168, v168, v169\n" \
"v_fma_f32 v243, v170, v171, v243\n" \
"v_mfma_f32_16x16x32_bf16 v[92:95], a[80:83], a[216:219], v[92:95]\n" \
"ds_read_b64_tr_b16 a[108:109], v209 offset:12288\n" \
"v_cvt_pk_bf16_f32 v169, v170, v171\n" \
"v_mfma_f32_16x16x32_bf16 v[92:95], a[84:87], a[220:223], v[92:95]\n" \
"ds_read_b64_tr_b16 a[110:111], v209 offset:13312\n" \
"global_load_lds_dwordx4 v252, s[44:45] offset:3072\n" \
"v_mfma_f32_16x16x32_bf16 v[60:63], a[200:203], a[88:91], v[60:63]\n" \
"s_add_u32 s44, s44, 0x80\n" \
"Lpg9349_%=:\n" \
"Lst1x3_%=:\n" \
"s_cbranch_execz Lpg11602_%=\n" \
"s_addc_u32 s45, s45, 0\n" \
"ds_read_b128 a[80:83], v205 offset:4096\n" \
"v_mfma_f32_16x16x32_bf16 v[60:63], a[204:207], a[92:95], v[60:63]\n" \
"ds_read_b64_tr_b16 a[112:113], v210 offset:0\n" \
"v_fma_f32 v243, v172, v173, v243\n" \
"v_mfma_f32_16x16x32_bf16 v[96:99], a[88:91], a[208:211], v[96:99]\n" \
"s_add_u32 m0, s85, 16384\n" \
"ds_read_b128 a[84:87], v206 offset:4096\n" \
"v_cvt_pk_bf16_f32 v170, v172, v173\n" \
"v_mfma_f32_16x16x32_bf16 v[96:99], a[92:95], a[212:215], v[96:99]\n" \
"ds_read_b64_tr_b16 a[114:115], v210 offset:1024\n" \
"global_load_lds_dwordx4 v246, s[46:47]\n" \
"v_mfma_f32_16x16x32_bf16 v[100:103], a[88:91], a[216:219], v[100:103]\n" \
"s_add_u32 s46, s46, 0x80\n" \
"s_addc_u32 s47, s47, 0\n" \
"ds_read_b64_tr_b16 a[116:117], v210 offset:12288\n" \
"v_mfma_f32_16x16x32_bf16 v[100:103], a[92:95], a[220:223], v[100:103]\n" \
"v_fma_f32 v243, v174, v175, v243\n" \
"v_cvt_pk_bf16_f32 v171, v174, v175\n" \
"v_mfma_f32_16x16x32_bf16 v[68:71], a[200:203], a[192:195], v[68:71]\n" \
"buffer_store_dwordx4 v[168:171], v249, s[56:59], s78 offen offset:2048 sc1\n" \
"ds_read_b128 a[88:91], v205 offset:8192\n" \
"s_add_u32 m0, s84, 24576\n" \
"v_mul_f32 v176, v176, v234\n" \
"v_mul_f32 v177, v177, v234\n" \
"v_mfma_f32_16x16x32_bf16 v[68:71], a[204:207], a[196:199], v[68:71]\n" \
"ds_read_b128 a[92:95], v206 offset:8192\n" \
"global_load_lds_dwordx4 v253, s[48:49] offset:-4096\n" \
"v_mfma_f32_16x16x32_bf16 v[104:107], a[192:195], a[208:211], v[104:107]\n" \
"ds_read_b64_tr_b16 a[118:119], v210 offset:13312\n" \
"v_mul_f32 v178, v178, v234\n" \
"v_mfma_f32_16x16x32_bf16 v[104:107], a[196:199], a[212:215], v[104:107]\n" \
"v_mul_f32 v179, v179, v234\n" \
"v_mul_f32 v180, v180, v234\n" \
"v_mfma_f32_16x16x32_bf16 v[108:111], a[192:195], a[216:219], v[108:111]\n" \
"v_mul_f32 v181, v181, v234\n" \
"v_mul_f32 v182, v182, v234\n" \
"v_mfma_f32_16x16x32_bf16 v[108:111], a[196:199], a[220:223], v[108:111]\n" \
"s_waitcnt lgkmcnt(2)\n" \
"global_load_lds_dwordx4 v253, s[48:49] offset:-2048\n" \
"v_mul_f32 v183, v183, v234\n" \
"v_mfma_f32_16x16x32_bf16 v[32:35], a[96:99], a[64:67], v[32:35]\n" \
"ds_read_b128 a[192:195], v205 offset:12288\n" \
"v_fma_f32 v243, v176, v177, v243\n" \
"v_mfma_f32_16x16x32_bf16 v[32:35], a[100:103], a[68:71], v[32:35]\n" \
"v_cvt_pk_bf16_f32 v176, v176, v177\n" \
"v_fma_f32 v243, v178, v179, v243\n" \
"v_mfma_f32_16x16x32_bf16 v[40:43], a[96:99], a[72:75], v[40:43]\n" \
"ds_read_b128 a[196:199], v206 offset:12288\n" \
"v_cvt_pk_bf16_f32 v177, v178, v179\n" \
"v_mfma_f32_16x16x32_bf16 v[40:43], a[100:103], a[76:79], v[40:43]\n" \
"ds_read_b64_tr_b16 a[200:201], v210 offset:2048\n" \
"global_load_lds_dwordx4 v253, s[48:49] offset:0\n" \
"v_mfma_f32_16x16x32_bf16 v[0:3], a[104:107], a[72:75], v[0:3]\n" \
"s_add_u32 s48, s48, 0x60000\n" \
"s_addc_u32 s49, s49, 0\n" \
"ds_read_b64_tr_b16 a[202:203], v210 offset:3072\n" \
"v_mfma_f32_16x16x32_bf16 v[0:3], a[108:111], a[76:79], v[0:3]\n" \
"s_waitcnt lgkmcnt(3)\n" \
"v_fma_f32 v243, v180, v181, v243\n" \
"v_cvt_pk_bf16_f32 v178, v180, v181\n" \
"v_mfma_f32_16x16x32_bf16 v[4:7], a[112:115], a[72:75], v[4:7]\n" \
"ds_read_b64_tr_b16 a[204:205], v210 offset:14336\n" \
"v_fma_f32 v243, v182, v183, v243\n" \
"v_mfma_f32_16x16x32_bf16 v[4:7], a[116:119], a[76:79], v[4:7]\n" \
"ds_read_b64_tr_b16 a[206:207], v210 offset:15360\n" \
"global_load_lds_dwordx4 v253, s[50:51] offset:-3072\n" \
"v_mfma_f32_16x16x32_bf16 v[48:51], a[96:99], a[80:83], v[48:51]\n" \
"ds_read_b64_tr_b16 a[208:209], v209 offset:4096\n" \
"v_cvt_pk_bf16_f32 v179, v182, v183\n" \
"v_mfma_f32_16x16x32_bf16 v[48:51], a[100:103], a[84:87], v[48:51]\n" \
"buffer_store_dwordx4 v[176:179], v235, s[56:59], s78 offen offset:0 sc1\n" \
"v_mul_f32 v184, v184, v234\n" \
"v_mul_f32 v185, v185, v234\n" \
"v_mul_f32 v186, v186, v234\n" \
"v_mul_f32 v187, v187, v234\n" \
"v_mfma_f32_16x16x32_bf16 v[8:11], a[104:107], a[80:83], v[8:11]\n" \
"ds_read_b64_tr_b16 a[210:211], v209 offset:5120\n" \
"v_mul_f32 v188, v188, v234\n" \
"v_mfma_f32_16x16x32_bf16 v[8:11], a[108:111], a[84:87], v[8:11]\n" \
"ds_read_b64_tr_b16 a[212:213], v209 offset:16384\n" \
"global_load_lds_dwordx4 v253, s[50:51] offset:-1024\n" \
"v_mfma_f32_16x16x32_bf16 v[12:15], a[112:115], a[80:83], v[12:15]\n" \
"ds_read_b64_tr_b16 a[214:215], v209 offset:17408\n" \
"v_mul_f32 v189, v189, v234\n" \
"v_mfma_f32_16x16x32_bf16 v[12:15], a[116:119], a[84:87], v[12:15]\n" \
"v_mul_f32 v190, v190, v234\n" \
"v_mul_f32 v191, v191, v234\n" \
"v_mfma_f32_16x16x32_bf16 v[56:59], a[96:99], a[88:91], v[56:59]\n" \
"ds_read_b64_tr_b16 a[216:217], v210 offset:4096\n" \
"v_fma_f32 v243, v184, v185, v243\n" \
"v_mfma_f32_16x16x32_bf16 v[56:59], a[100:103], a[92:95], v[56:59]\n" \
"ds_read_b64_tr_b16 a[218:219], v210 offset:5120\n" \
"global_load_lds_dwordx4 v253, s[50:51] offset:1024\n" \
"v_mfma_f32_16x16x32_bf16 v[16:19], a[104:107], a[88:91], v[16:19]\n" \
"s_add_u32 s50, s50, 0x60000\n" \
"s_addc_u32 s51, s51, 0\n" \
"ds_read_b64_tr_b16 a[220:221], v210 offset:16384\n" \
"v_mfma_f32_16x16x32_bf16 v[16:19], a[108:111], a[92:95], v[16:19]\n" \
"v_cvt_pk_bf16_f32 v184, v184, v185\n" \
"v_fma_f32 v243, v186, v187, v243\n" \
"v_mfma_f32_16x16x32_bf16 v[20:23], a[112:115], a[88:91], v[20:23]\n" \
"ds_read_b64_tr_b16 a[222:223], v210 offset:17408\n" \
"v_cvt_pk_bf16_f32 v185, v186, v187\n" \
"v_mfma_f32_16x16x32_bf16 v[20:23], a[116:119], a[92:95], v[20:23]\n" \
"s_waitcnt lgkmcnt(3)\n" \
"v_add_u32 v207, 0xffff3000, v207\n" \
"v_add_u32 v208, 0xffff3000, v208\n" \
"v_mfma_f32_16x16x32_bf16 v[64:67], a[96:99], a[192:195], v[64:67]\n" \
"v_add_u32 v205, 0xffff3000, v205\n" \
"v_add_u32 v206, 0xffff3000, v206\n" \
"v_mfma_f32_16x16x32_bf16 v[64:67], a[100:103], a[196:199], v[64:67]\n" \
"v_add_u32 v209, 0xffff3000, v209\n" \
"v_add_u32 v210, 0xffff3000, v210\n" \
"v_mfma_f32_16x16x32_bf16 v[24:27], a[104:107], a[192:195], v[24:27]\n" \
"v_fma_f32 v243, v188, v189, v243\n" \
"v_cvt_pk_bf16_f32 v186, v188, v189\n" \
"v_mfma_f32_16x16x32_bf16 v[24:27], a[108:111], a[196:199], v[24:27]\n" \
"v_fma_f32 v243, v190, v191, v243\n" \
"v_cvt_pk_bf16_f32 v187, v190, v191\n" \
"v_mfma_f32_16x16x32_bf16 v[28:31], a[112:115], a[192:195], v[28:31]\n" \
"s_waitcnt vmcnt(14) lgkmcnt(0)\n" \
"buffer_store_dwordx4 v[184:187], v235, s[56:59], s78 offen offset:2048 sc1\n" \
"s_add_u32 s78, s78, 0xfff7e000\n" \
"v_add_f32 v240, v240, v241\n" \
"v_add_f32 v242, v242, v243\n" \
"v_mov_b32 v241, 0x207\n" \
"v_mfma_f32_16x16x32_bf16 v[28:31], a[116:119], a[196:199], v[28:31]\n" \
"s_barrier\n" \
"v_mfma_f32_16x16x32_bf16 v[72:75], a[64:67], a[208:211], v[72:75]\n" \
"s_add_u32 m0, s95, 118784\n" \
"ds_read_b64_tr_b16 a[96:97], v209 offset:2048\n" \
"v_add_f32 v240, v240, v242\n" \
"v_mfma_f32_16x16x32_bf16 v[72:75], a[68:71], a[212:215], v[72:75]\n" \
"ds_read_b64_tr_b16 a[98:99], v209 offset:3072\n" \
"global_load_lds_dwordx4 v252, s[42:43] offset:0\n" \
"v_mfma_f32_16x16x32_bf16 v[44:47], a[200:203], a[72:75], v[44:47]\n" \
"ds_read_b128 a[64:67], v207 offset:0\n" \
"v_cmp_class_f32 vcc, v240, v241\n" \
"v_mfma_f32_16x16x32_bf16 v[44:47], a[204:207], a[76:79], v[44:47]\n" \
"v_mfma_f32_16x16x32_bf16 v[80:83], a[72:75], a[208:211], v[80:83]\n" \
"ds_read_b128 a[68:71], v208 offset:0\n" \
"v_mfma_f32_16x16x32_bf16 v[80:83], a[76:79], a[212:215], v[80:83]\n" \
"ds_read_b64_tr_b16 a[100:101], v209 offset:14336\n" \
"global_load_lds_dwordx4 v252, s[42:43] offset:2048\n" \
"v_mfma_f32_16x16x32_bf16 v[84:87], a[72:75], a[216:219], v[84:87]\n" \
"s_add_u32 s42, s42, 0x80\n" \
"s_addc_u32 s43, s43, 0\n" \
"ds_read_b64_tr_b16 a[102:103], v209 offset:15360\n" \
"v_mfma_f32_16x16x32_bf16 v[84:87], a[76:79], a[220:223], v[84:87]\n" \
"ds_read_b64_tr_b16 a[104:105], v209 offset:0\n" \
"v_mfma_f32_16x16x32_bf16 v[52:55], a[200:203], a[80:83], v[52:55]\n" \
"ds_read_b128 a[72:75], v205 offset:0\n" \
"v_mfma_f32_16x16x32_bf16 v[52:55], a[204:207], a[84:87], v[52:55]\n" \
"ds_read_b128 a[76:79], v206 offset:0\n" \
"global_load_lds_dwordx4 v252, s[44:45] offset:1024\n" \
"v_mfma_f32_16x16x32_bf16 v[88:91], a[80:83], a[208:211], v[88:91]\n" \
"ds_read_b64_tr_b16 a[106:107], v209 offset:1024\n" \
"v_mfma_f32_16x16x32_bf16 v[88:91], a[84:87], a[212:215], v[88:91]\n" \
"s_cbranch_vccnz Lexact_%=\n" \
"Lexr_%=:\n" \
"v_mfma_f32_16x16x32_bf16 v[92:95], a[80:83], a[216:219], v[92:95]\n" \
"ds_read_b64_tr_b16 a[108:109], v209 offset:12288\n" \
"v_mfma_f32_16x16x32_bf16 v[92:95], a[84:87], a[220:223], v[92:95]\n" \
"ds_read_b64_tr_b16 a[110:111], v209 offset:13312\n" \
"global_load_lds_dwordx4 v252, s[44:45] offset:3072\n" \
"v_mfma_f32_16x16x32_bf16 v[60:63], a[200:203], a[88:91], v[60:63]\n" \
"s_add_u32 s44, s44, 0x80\n" \
"s_addc_u32 s45, s45, 0\n" \
"ds_read_b128 a[80:83], v205 offset:4096\n" \
"v_mfma_f32_16x16x32_bf16 v[60:63], a[204:207], a[92:95], v[60:63]\n" \
"ds_read_b64_tr_b16 a[112:113], v210 offset:0\n" \
"v_mfma_f32_16x16x32_bf16 v[96:99], a[88:91], a[208:211], v[96:99]\n" \
"ds_read_b128 a[84:87], v206 offset:4096\n" \
"s_add_u32 m0, s85, 135168\n" \
"v_mfma_f32_16x16x32_bf16 v[96:99], a[92:95], a[212:215], v[96:99]\n" \
"ds_read_b64_tr_b16 a[114:115], v210 offset:1024\n" \
"global_load_lds_dwordx4 v246, s[46:47]\n" \
"v_mfma_f32_16x16x32_bf16 v[100:103], a[88:91], a[216:219], v[100:103]\n" \
"s_add_u32 s46, s46, 0x80\n" \
"s_addc_u32 s47, s47, 0\n" \
"ds_read_b64_tr_b16 a[116:117], v210 offset:12288\n" \
"v_mfma_f32_16x16x32_bf16 v[100:103], a[92:95], a[220:223], v[100:103]\n" \
"v_mfma_f32_16x16x32_bf16 v[68:71], a[200:203], a[192:195], v[68:71]\n" \
"ds_read_b128 a[88:91], v205 offset:8192\n" \
"s_add_u32 m0, s84, 143360\n" \
"v_mfma_f32_16x16x32_bf16 v[68:71], a[204:207], a[196:199], v[68:71]\n" \
"ds_read_b128 a[92:95], v206 offset:8192\n" \
"global_load_lds_dwordx4 v253, s[48:49] offset:-4096\n" \
"v_mfma_f32_16x16x32_bf16 v[104:107], a[192:195], a[208:211], v[104:107]\n" \
"ds_read_b64_tr_b16 a[118:119], v210 offset:13312\n" \
"v_mfma_f32_16x16x32_bf16 v[104:107], a[196:199], a[212:215], v[104:107]\n" \
"v_mfma_f32_16x16x32_bf16 v[108:111], a[192:195], a[216:219], v[108:111]\n" \
"v_mfma_f32_16x16x32_bf16 v[108:111], a[196:199], a[220:223], v[108:111]\n" \
"s_waitcnt lgkmcnt(2)\n" \
"global_load_lds_dwordx4 v253, s[48:49] offset:-2048\n" \
"v_mfma_f32_16x16x32_bf16 v[32:35], a[96:99], a[64:67], v[32:35]\n" \
"ds_read_b128 a[192:195], v205 offset:12288\n" \
"v_mfma_f32_16x16x32_bf16 v[32:35], a[100:103], a[68:71], v[32:35]\n" \
"v_mfma_f32_16x16x32_bf16 v[40:43], a[96:99], a[72:75], v[40:43]\n" \
"ds_read_b128 a[196:199], v206 offset:12288\n" \
"v_mfma_f32_16x16x32_bf16 v[40:43], a[100:103], a[76:79], v[40:43]\n" \
"ds_read_b64_tr_b16 a[200:201], v210 offset:2048\n" \
"global_load_lds_dwordx4 v253, s[48:49] offset:0\n" \
"v_mfma_f32_16x16x32_bf16 v[0:3], a[104:107], a[72:75], v[0:3]\n" \
"s_add_u32 s48, s48, 0x60000\n" \
"s_addc_u32 s49, s49, 0\n" \
"ds_read_b64_tr_b16 a[202:203], v210 offset:3072\n" \
"v_mfma_f32_16x16x32_bf16 v[0:3], a[108:111], a[76:79], v[0:3]\n" \
"s_waitcnt lgkmcnt(3)\n" \
"v_mfma_f32_16x16x32_bf16 v[4:7], a[112:115], a[72:75], v[4:7]\n" \
"ds_read_b64_tr_b16 a[204:205], v210 offset:14336\n" \
"v_mfma_f32_16x16x32_bf16 v[4:7], a[116:119], a[76:79], v[4:7]\n" \
"ds_read_b64_tr_b16 a[206:207], v210 offset:15360\n" \
"global_load_lds_dwordx4 v253, s[50:51] offset:-3072\n" \
"v_mfma_f32_16x16x32_bf16 v[48:51], a[96:99], a[80:83], v[48:51]\n" \
"ds_read_b64_tr_b16 a[208:209], v209 offset:4096\n" \
"v_mfma_f32_16x16x32_bf16 v[48:51], a[100:103], a[84:87], v[48:51]\n" \
"v_mfma_f32_16x16x32_bf16 v[8:11], a[104:107], a[80:83], v[8:11]\n" \
"ds_read_b64_tr_b16 a[210:211], v209 offset:5120\n" \
"v_mfma_f32_16x16x32_bf16 v[8:11], a[108:111], a[84:87], v[8:11]\n" \
"ds_read_b64_tr_b16 a[212:213], v209 offset:16384\n" \
"global_load_lds_dwordx4 v253, s[50:51] offset:-1024\n" \
"v_mfma_f32_16x16x32_bf16 v[12:15], a[112:115], a[80:83], v[12:15]\n" \
"ds_read_b64_tr_b16 a[214:215], v209 offset:17408\n" \
"v_mfma_f32_16x16x32_bf16 v[12:15], a[116:119], a[84:87], v[12:15]\n" \
"v_mfma_f32_16x16x32_bf16 v[56:59], a[96:99], a[88:91], v[56:59]\n" \
"ds_read_b64_tr_b16 a[216:217], v210 offset:4096\n" \
"v_mfma_f32_16x16x32_bf16 v[56:59], a[100:103], a[92:95], v[56:59]\n" \
"ds_read_b64_tr_b16 a[218:219], v210 offset:5120\n" \
"global_load_lds_dwordx4 v253, s[50:51] offset:1024\n" \
"v_mfma_f32_16x16x32_bf16 v[16:19], a[104:107], a[88:91], v[16:19]\n" \
"s_add_u32 s50, s50, 0x60000\n" \
"s_addc_u32 s51, s51, 0\n" \
"ds_read_b64_tr_b16 a[220:221], v210 offset:16384\n" \
"v_mfma_f32_16x16x32_bf16 v[16:19], a[108:111], a[92:95], v[16:19]\n" \
"v_mfma_f32_16x16x32_bf16 v[20:23], a[112:115], a[88:91], v[20:23]\n" \
"ds_read_b64_tr_b16 a[222:223], v210 offset:17408\n" \
"v_mfma_f32_16x16x32_bf16 v[20:23], a[116:119], a[92:95], v[20:23]\n" \
"s_waitcnt lgkmcnt(3)\n" \
"v_add_u32 v207, 0xffff0000, v207\n" \
"v_add_u32 v208, 0xffff0000, v208\n" \
"v_mfma_f32_16x16x32_bf16 v[64:67], a[96:99], a[192:195], v[64:67]\n" \
"v_add_u32 v205, 0xffff0000, v205\n" \
"v_add_u32 v206, 0xffff0000, v206\n" \
"v_mfma_f32_16x16x32_bf16 v[64:67], a[100:103], a[196:199], v[64:67]\n" \
"v_add_u32 v209, 0xffff0000, v209\n" \
"v_add_u32 v210, 0xffff0000, v210\n" \
"v_mfma_f32_16x16x32_bf16 v[24:27], a[104:107], a[192:195], v[24:27]\n" \
"s_lshl_b32 s70, s63, 7\n" \
"v_mfma_f32_16x16x32_bf16 v[24:27], a[108:111], a[196:199], v[24:27]\n" \
"s_lshl_b32 s71, s62, 2\n" \
"s_add_u32 s71, s71, 65589248\n" \
"s_add_u32 s72, s71, s70\n" \
"v_mfma_f32_16x16x32_bf16 v[28:31], a[112:115], a[192:195], v[28:31]\n" \
"s_waitcnt vmcnt(11) lgkmcnt(0)\n" \
"s_sub_u32 s71, s71, s70\n" \
"s_add_u32 s71, s71, 1920\n" \
"v_mfma_f32_16x16x32_bf16 v[28:31], a[116:119], a[196:199], v[28:31]\n" \
"s_barrier\n" \
"v_mfma_f32_16x16x32_bf16 v[72:75], a[64:67], a[208:211], v[72:75]\n" \
"s_add_u32 m0, s95, 65536\n" \
"ds_read_b64_tr_b16 a[96:97], v209 offset:2048\n" \
"v_mov_b32 v236, s60\n" \
"v_mfma_f32_16x16x32_bf16 v[72:75], a[68:71], a[212:215], v[72:75]\n" \
"ds_read_b64_tr_b16 a[98:99], v209 offset:3072\n" \
"global_load_lds_dwordx4 v252, s[42:43] offset:0\n" \
"v_mfma_f32_16x16x32_bf16 v[44:47], a[200:203], a[72:75], v[44:47]\n" \
"ds_read_b128 a[64:67], v207 offset:0\n" \
"v_lshlrev_b32 v237, 28, s61\n" \
"v_mfma_f32_16x16x32_bf16 v[44:47], a[204:207], a[76:79], v[44:47]\n" \
"buffer_store_dword v236, v237, s[56:59], s72 offen sc1\n" \
"v_mfma_f32_16x16x32_bf16 v[80:83], a[72:75], a[208:211], v[80:83]\n" \
"buffer_store_dword v236, v237, s[56:59], s71 offen sc1\n" \
"ds_read_b128 a[68:71], v208 offset:0\n" \
"v_mfma_f32_16x16x32_bf16 v[80:83], a[76:79], a[212:215], v[80:83]\n" \
"ds_read_b64_tr_b16 a[100:101], v209 offset:14336\n" \
"global_load_lds_dwordx4 v252, s[42:43] offset:2048\n" \
"v_mfma_f32_16x16x32_bf16 v[84:87], a[72:75], a[216:219], v[84:87]\n" \
"s_add_u32 s42, s42, 0x80\n" \
"s_addc_u32 s43, s43, 0\n" \
"ds_read_b64_tr_b16 a[102:103], v209 offset:15360\n" \
"v_mfma_f32_16x16x32_bf16 v[84:87], a[76:79], a[220:223], v[84:87]\n" \
"ds_read_b64_tr_b16 a[104:105], v209 offset:0\n" \
"v_lshrrev_b32 v221, 2, v248\n" \
"v_mfma_f32_16x16x32_bf16 v[52:55], a[200:203], a[80:83], v[52:55]\n" \
"s_lshl_b32 s81, s88, 7\n" \
"ds_read_b128 a[72:75], v205 offset:0\n" \
"s_add_u32 s82, s81, 65591296\n" \
"v_mfma_f32_16x16x32_bf16 v[52:55], a[204:207], a[84:87], v[52:55]\n" \
"s_add_u32 s81, s81, 65589248\n" \
"ds_read_b128 a[76:79], v206 offset:0\n" \
"global_load_lds_dwordx4 v252, s[44:45] offset:1024\n" \
"v_mfma_f32_16x16x32_bf16 v[88:91], a[80:83], a[208:211], v[88:91]\n" \
"s_mov_b64 s[52:53], s[26:27]\n" \
"s_and_b32 s53, s53, 0xffff\n" \
"ds_read_b64_tr_b16 a[106:107], v209 offset:1024\n" \
"v_mfma_f32_16x16x32_bf16 v[88:91], a[84:87], a[212:215], v[88:91]\n" \
"s_mov_b32 s54, 8388608\n" \
"s_mov_b32 s55, 0x20000\n" \
"s_lshr_b32 s83, s63, 1\n" \
"v_mfma_f32_16x16x32_bf16 v[92:95], a[80:83], a[216:219], v[92:95]\n" \
"s_add_u32 s83, s83, 2\n" \
"s_lshr_b32 s70, s18, 8\n" \
"ds_read_b64_tr_b16 a[108:109], v209 offset:12288\n" \
"v_mfma_f32_16x16x32_bf16 v[92:95], a[84:87], a[220:223], v[92:95]\n" \
"s_sub_u32 s83, s83, s70\n" \
"ds_read_b64_tr_b16 a[110:111], v209 offset:13312\n" \
"global_load_lds_dwordx4 v252, s[44:45] offset:3072\n" \
"v_mfma_f32_16x16x32_bf16 v[60:63], a[200:203], a[88:91], v[60:63]\n" \
"s_add_u32 s44, s44, 0x80\n" \
"s_addc_u32 s45, s45, 0\n" \
"ds_read_b128 a[80:83], v205 offset:4096\n" \
"v_mfma_f32_16x16x32_bf16 v[60:63], a[204:207], a[92:95], v[60:63]\n" \
"s_and_b32 s83, s83, 3\n" \
"s_and_b32 s70, s63, 1\n" \
"ds_read_b64_tr_b16 a[112:113], v210 offset:0\n" \
"v_mfma_f32_16x16x32_bf16 v[96:99], a[88:91], a[208:211], v[96:99]\n" \
"s_add_u32 m0, s85, 81920\n" \
"ds_read_b128 a[84:87], v206 offset:4096\n" \
"s_lshl_b32 s70, s70, 2\n" \
"v_mfma_f32_16x16x32_bf16 v[96:99], a[92:95], a[212:215], v[96:99]\n" \
"s_or_b32 s83, s83, s70\n" \
"ds_read_b64_tr_b16 a[114:115], v210 offset:1024\n" \
"global_load_lds_dwordx4 v246, s[46:47]\n" \
"v_mfma_f32_16x16x32_bf16 v[100:103], a[88:91], a[216:219], v[100:103]\n" \
"s_add_u32 s46, s46, 0x80\n" \
"s_addc_u32 s47, s47, 0\n" \
"ds_read_b64_tr_b16 a[116:117], v210 offset:12288\n" \
"v_mfma_f32_16x16x32_bf16 v[100:103], a[92:95], a[220:223], v[100:103]\n" \
"s_xor_b32 s78, %1, s30\n" \
"s_and_b32 s78, s78, 1\n" \
"v_lshrrev_b32 v228, 4, v248\n" \
"v_mfma_f32_16x16x32_bf16 v[68:71], a[200:203], a[192:195], v[68:71]\n" \
"s_add_u32 m0, s84, 90112\n" \
"ds_read_b128 a[88:91], v205 offset:8192\n" \
"s_lshl_b32 s70, s88, 2\n" \
"v_mfma_f32_16x16x32_bf16 v[68:71], a[204:207], a[196:199], v[68:71]\n" \
"s_add_u32 s70, s70, s61\n" \
"ds_read_b128 a[92:95], v206 offset:8192\n" \
"global_load_lds_dwordx4 v253, s[48:49] offset:-4096\n" \
"v_mfma_f32_16x16x32_bf16 v[104:107], a[192:195], a[208:211], v[104:107]\n" \
"s_mul_i32 s73, s70, 69632\n" \
"s_add_u32 s73, s73, 52740096\n" \
"ds_read_b64_tr_b16 a[118:119], v210 offset:13312\n" \
"v_mfma_f32_16x16x32_bf16 v[104:107], a[196:199], a[212:215], v[104:107]\n" \
"s_add_u32 s73, s73, s28\n" \
"s_mul_i32 s70, s70, 266240\n" \
"s_mul_i32 s71, s78, 155648\n" \
"v_mfma_f32_16x16x32_bf16 v[108:111], a[192:195], a[216:219], v[108:111]\n" \
"s_add_u32 s70, s70, s71\n" \
"s_add_u32 s70, s70, 35700736\n" \
"s_add_u32 s36, s28, s70\n" \
"v_mfma_f32_16x16x32_bf16 v[108:111], a[196:199], a[220:223], v[108:111]\n" \
"s_waitcnt lgkmcnt(2)\n" \
"global_load_lds_dwordx4 v253, s[48:49] offset:-2048\n" \
"s_mov_b32 s37, s29\n" \
"v_mfma_f32_16x16x32_bf16 v[32:35], a[96:99], a[64:67], v[32:35]\n" \
"s_lshr_b32 s89, s22, 7\n" \
"ds_read_b128 a[192:195], v205 offset:12288\n" \
"s_sub_u32 s89, 3, s89\n" \
"v_mfma_f32_16x16x32_bf16 v[32:35], a[100:103], a[68:71], v[32:35]\n" \
"s_lshr_b32 s74, s61, 1\n" \
"v_bfe_u32 v203, v248, 7, 2\n" \
"s_and_b32 s89, s89, 7\n" \
"v_mfma_f32_16x16x32_bf16 v[40:43], a[96:99], a[72:75], v[40:43]\n" \
"s_and_b32 s90, %1, 1\n" \
"ds_read_b128 a[196:199], v206 offset:12288\n" \
"v_bfe_u32 v204, v248, 5, 2\n" \
"v_mfma_f32_16x16x32_bf16 v[40:43], a[100:103], a[76:79], v[40:43]\n" \
"s_lshl_b32 s90, s90, 3\n" \
"ds_read_b64_tr_b16 a[200:201], v210 offset:2048\n" \
"global_load_lds_dwordx4 v253, s[48:49] offset:0\n" \
"v_mfma_f32_16x16x32_bf16 v[0:3], a[104:107], a[72:75], v[0:3]\n" \
"s_add_u32 s48, s48, 0x60000\n" \
"s_addc_u32 s49, s49, 0\n" \
"ds_read_b64_tr_b16 a[202:203], v210 offset:3072\n" \
"v_mfma_f32_16x16x32_bf16 v[0:3], a[108:111], a[76:79], v[0:3]\n" \
"s_waitcnt lgkmcnt(3)\n" \
"s_add_u32 s89, s89, s90\n" \
"v_min_u32 v204, 2, v204\n" \
"v_mfma_f32_16x16x32_bf16 v[4:7], a[112:115], a[72:75], v[4:7]\n" \
"s_lshl_b32 s89, s89, 7\n" \
"s_lshr_b32 s90, %1, 3\n" \
"ds_read_b64_tr_b16 a[204:205], v210 offset:14336\n" \
"v_mfma_f32_16x16x32_bf16 v[4:7], a[116:119], a[76:79], v[4:7]\n" \
"s_lshl_b32 s74, s74, 16\n" \
"ds_read_b64_tr_b16 a[206:207], v210 offset:15360\n" \
"global_load_lds_dwordx4 v253, s[50:51] offset:-3072\n" \
"v_mfma_f32_16x16x32_bf16 v[48:51], a[96:99], a[80:83], v[48:51]\n" \
"s_and_b32 s70, s61, 1\n" \
"ds_read_b64_tr_b16 a[208:209], v209 offset:4096\n" \
"v_mad_u32_u24 v203, v203, 3, v204\n" \
"v_mfma_f32_16x16x32_bf16 v[48:51], a[100:103], a[84:87], v[48:51]\n" \
"s_lshl_b32 s90, s90, 2\n" \
"s_add_u32 s90, s90, s61\n" \
"v_and_b32 v204, 16, v248\n" \
"v_mfma_f32_16x16x32_bf16 v[8:11], a[104:107], a[80:83], v[8:11]\n" \
"s_add_u32 s90, s90, 128\n" \
"s_mul_i32 s90, s90, 24576\n" \
"ds_read_b64_tr_b16 a[210:211], v209 offset:5120\n" \
"v_mfma_f32_16x16x32_bf16 v[8:11], a[108:111], a[84:87], v[8:11]\n" \
"s_add_u32 s89, s89, s90\n" \
"ds_read_b64_tr_b16 a[212:213], v209 offset:16384\n" \
"global_load_lds_dwordx4 v253, s[50:51] offset:-1024\n" \
"v_mfma_f32_16x16x32_bf16 v[12:15], a[112:115], a[80:83], v[12:15]\n" \
"s_lshl_b32 s70, s70, 14\n" \
"ds_read_b64_tr_b16 a[214:215], v209 offset:17408\n" \
"v_lshl_add_u32 v204, v204, 2, s89\n" \
"v_mfma_f32_16x16x32_bf16 v[12:15], a[116:119], a[84:87], v[12:15]\n" \
"s_add_u32 s74, s74, s70\n" \
"v_lshl_add_u32 v203, v203, 11, v204\n" \
"s_lshl_b32 s71, s83, 8\n" \
"v_mfma_f32_16x16x32_bf16 v[56:59], a[96:99], a[88:91], v[56:59]\n" \
"s_add_u32 s74, s74, s71\n" \
"ds_read_b64_tr_b16 a[216:217], v210 offset:4096\n" \
"v_mfma_f32_16x16x32_bf16 v[56:59], a[100:103], a[92:95], v[56:59]\n" \
"ds_read_b64_tr_b16 a[218:219], v210 offset:5120\n" \
"global_load_lds_dwordx4 v253, s[50:51] offset:1024\n" \
"v_mfma_f32_16x16x32_bf16 v[16:19], a[104:107], a[88:91], v[16:19]\n" \
"s_add_u32 s50, s50, 0x60000\n" \
"s_addc_u32 s51, s51, 0\n" \
"ds_read_b64_tr_b16 a[220:221], v210 offset:16384\n" \
"v_mfma_f32_16x16x32_bf16 v[16:19], a[108:111], a[92:95], v[16:19]\n" \
"s_add_u32 s40, s24, s74\n" \
"s_addc_u32 s41, s25, 0\n" \
"s_mul_i32 s70, s78, 4980736\n" \
"v_mfma_f32_16x16x32_bf16 v[20:23], a[112:115], a[88:91], v[20:23]\n" \
"s_add_u32 s70, s70, s74\n" \
"ds_read_b64_tr_b16 a[222:223], v210 offset:17408\n" \
"v_mfma_f32_16x16x32_bf16 v[20:23], a[116:119], a[92:95], v[20:23]\n" \
"s_waitcnt lgkmcnt(3)\n" \
"v_add_u32 v207, 0x1d000, v207\n" \
"v_add_u32 v208, 0x1d000, v208\n" \
"v_mfma_f32_16x16x32_bf16 v[64:67], a[96:99], a[192:195], v[64:67]\n" \
"v_add_u32 v205, 0x1d000, v205\n" \
"v_add_u32 v206, 0x1d000, v206\n" \
"v_mfma_f32_16x16x32_bf16 v[64:67], a[100:103], a[196:199], v[64:67]\n" \
"v_add_u32 v209, 0x1d000, v209\n" \
"v_add_u32 v210, 0x1d000, v210\n" \
"v_mfma_f32_16x16x32_bf16 v[24:27], a[104:107], a[192:195], v[24:27]\n" \
"s_add_u32 s38, s18, s70\n" \
"s_addc_u32 s39, s19, 0\n" \
"v_bfe_u32 v229, v228, 2, 3\n" \
"v_mfma_f32_16x16x32_bf16 v[24:27], a[108:111], a[196:199], v[24:27]\n" \
"v_lshlrev_b32 v229, 11, v229\n" \
"v_lshrrev_b32 v230, 5, v228\n" \
"v_mfma_f32_16x16x32_bf16 v[28:31], a[112:115], a[192:195], v[28:31]\n" \
"s_waitcnt vmcnt(13) lgkmcnt(0)\n" \
"v_lshl_add_u32 v229, v230, 6, v229\n" \
"v_and_b32 v230, 3, v228\n" \
"v_mfma_f32_16x16x32_bf16 v[28:31], a[116:119], a[196:199], v[28:31]\n" \
"s_barrier\n" \
"v_mfma_f32_16x16x32_bf16 v[72:75], a[64:67], a[208:211], v[72:75]\n" \
"s_add_u32 m0, s95, 0\n" \
"ds_read_b64_tr_b16 a[96:97], v209 offset:2048\n" \
"v_bfe_u32 v231, v228, 4, 1\n" \
"v_mfma_f32_16x16x32_bf16 v[72:75], a[68:71], a[212:215], v[72:75]\n" \
"ds_read_b64_tr_b16 a[98:99], v209 offset:3072\n" \
"global_load_lds_dwordx4 v252, s[42:43] offset:0\n" \
"v_mfma_f32_16x16x32_bf16 v[44:47], a[200:203], a[72:75], v[44:47]\n" \
"ds_read_b128 a[64:67], v207 offset:0\n" \
"v_lshlrev_b32 v231, 1, v231\n" \
"v_mfma_f32_16x16x32_bf16 v[44:47], a[204:207], a[76:79], v[44:47]\n" \
"v_xor_b32 v230, v230, v231\n" \
"v_lshl_add_u32 v229, v230, 4, v229\n" \
"v_mfma_f32_16x16x32_bf16 v[80:83], a[72:75], a[208:211], v[80:83]\n" \
"ds_read_b128 a[68:71], v208 offset:0\n" \
"v_add_u32 v216, 4096, v229\n" \
"v_mfma_f32_16x16x32_bf16 v[80:83], a[76:79], a[212:215], v[80:83]\n" \
"ds_read_b64_tr_b16 a[100:101], v209 offset:14336\n" \
"global_load_lds_dwordx4 v252, s[42:43] offset:2048\n" \
"v_mfma_f32_16x16x32_bf16 v[84:87], a[72:75], a[216:219], v[84:87]\n" \
"s_add_u32 s42, s42, 0x80\n" \
"s_addc_u32 s43, s43, 0\n" \
"ds_read_b64_tr_b16 a[102:103], v209 offset:15360\n" \
"v_mfma_f32_16x16x32_bf16 v[84:87], a[76:79], a[220:223], v[84:87]\n" \
"ds_read_b64_tr_b16 a[104:105], v209 offset:0\n" \
"v_add_u32 v217, 3200, v229\n" \
"v_mfma_f32_16x16x32_bf16 v[52:55], a[200:203], a[80:83], v[52:55]\n" \
"ds_read_b128 a[72:75], v205 offset:0\n" \
"v_add_u32 v218, 34816, v229\n" \
"v_mfma_f32_16x16x32_bf16 v[52:55], a[204:207], a[84:87], v[52:55]\n" \
"ds_read_b128 a[76:79], v206 offset:0\n" \
"global_load_lds_dwordx4 v252, s[44:45] offset:1024\n" \
"v_mfma_f32_16x16x32_bf16 v[88:91], a[80:83], a[208:211], v[88:91]\n" \
"ds_read_b64_tr_b16 a[106:107], v209 offset:1024\n" \
"v_add_u32 v219, 33920, v229\n" \
"v_mfma_f32_16x16x32_bf16 v[88:91], a[84:87], a[212:215], v[88:91]\n" \
"s_mov_b32 s75, 65536\n" \
"s_lshr_b32 s70, s61, 1\n" \
"s_lshl_b32 s71, s70, 13\n" \
"v_mfma_f32_16x16x32_bf16 v[92:95], a[80:83], a[216:219], v[92:95]\n" \
"ds_read_b64_tr_b16 a[108:109], v209 offset:12288\n" \
"v_add_u32 v211, s71, v249\n" \
"v_mfma_f32_16x16x32_bf16 v[92:95], a[84:87], a[220:223], v[92:95]\n" \
"s_lshl_b32 s71, s88, 1\n" \
"ds_read_b64_tr_b16 a[110:111], v209 offset:13312\n" \
"global_load_lds_dwordx4 v252, s[44:45] offset:3072\n" \
"v_mfma_f32_16x16x32_bf16 v[60:63], a[200:203], a[88:91], v[60:63]\n" \
"s_add_u32 s44, s44, 0x80\n" \
"s_addc_u32 s45, s45, 0\n" \
"ds_read_b128 a[80:83], v205 offset:4096\n" \
"v_mfma_f32_16x16x32_bf16 v[60:63], a[204:207], a[92:95], v[60:63]\n" \
"s_add_u32 s70, s70, s71\n" \
"s_lshl_b32 s77, s70, 18\n" \
"ds_read_b64_tr_b16 a[112:113], v210 offset:0\n" \
"v_mfma_f32_16x16x32_bf16 v[96:99], a[88:91], a[208:211], v[96:99]\n" \
"s_add_u32 m0, s85, 16384\n" \
"ds_read_b128 a[84:87], v206 offset:4096\n" \
"s_lshl_b32 s71, s83, 8\n" \
"v_mfma_f32_16x16x32_bf16 v[96:99], a[92:95], a[212:215], v[96:99]\n" \
"s_add_u32 s77, s77, s71\n" \
"ds_read_b64_tr_b16 a[114:115], v210 offset:1024\n" \
"global_load_lds_dwordx4 v246, s[46:47]\n" \
"v_mfma_f32_16x16x32_bf16 v[100:103], a[88:91], a[216:219], v[100:103]\n" \
"s_add_u32 s46, s46, 0x80\n" \
"s_addc_u32 s47, s47, 0\n" \
"ds_read_b64_tr_b16 a[116:117], v210 offset:12288\n" \
"v_mfma_f32_16x16x32_bf16 v[100:103], a[92:95], a[220:223], v[100:103]\n" \
"s_lshl_b32 s71, s78, 11\n" \
"s_add_u32 s77, s77, s71\n" \
"s_and_b32 s70, s61, 1\n" \
"v_mfma_f32_16x16x32_bf16 v[68:71], a[200:203], a[192:195], v[68:71]\n" \
"s_add_u32 m0, s84, 24576\n" \
"ds_read_b128 a[88:91], v205 offset:8192\n" \
"s_lshl_b32 s71, s70, 7\n" \
"v_mfma_f32_16x16x32_bf16 v[68:71], a[204:207], a[196:199], v[68:71]\n" \
"s_add_u32 s77, s77, s71\n" \
"ds_read_b128 a[92:95], v206 offset:8192\n" \
"global_load_lds_dwordx4 v253, s[48:49] offset:-4096\n" \
"v_mfma_f32_16x16x32_bf16 v[104:107], a[192:195], a[208:211], v[104:107]\n" \
"ds_read_b64_tr_b16 a[118:119], v210 offset:13312\n" \
"v_and_b32 v229, 3, v228\n" \
"v_mfma_f32_16x16x32_bf16 v[104:107], a[196:199], a[212:215], v[104:107]\n" \
"v_lshlrev_b32 v229, 3, v229\n" \
"v_bfe_u32 v230, v228, 2, 2\n" \
"v_mfma_f32_16x16x32_bf16 v[108:111], a[192:195], a[216:219], v[108:111]\n" \
"v_lshl_add_u32 v229, v230, 6, v229\n" \
"v_bfe_u32 v230, v228, 4, 1\n" \
"v_mfma_f32_16x16x32_bf16 v[108:111], a[196:199], a[220:223], v[108:111]\n" \
"s_waitcnt lgkmcnt(2)\n" \
"global_load_lds_dwordx4 v253, s[48:49] offset:-2048\n" \
"v_lshl_add_u32 v229, v230, 5, v229\n" \
"v_mfma_f32_16x16x32_bf16 v[32:35], a[96:99], a[64:67], v[32:35]\n" \
"ds_read_b128 a[192:195], v205 offset:12288\n" \
"v_lshl_add_u32 v229, v230, 8, v229\n" \
"v_mfma_f32_16x16x32_bf16 v[32:35], a[100:103], a[68:71], v[32:35]\n" \
"v_lshrrev_b32 v230, 5, v228\n" \
"v_lshl_add_u32 v229, v230, 12, v229\n" \
"v_mfma_f32_16x16x32_bf16 v[40:43], a[96:99], a[72:75], v[40:43]\n" \
"s_lshl_b32 s71, s70, 10\n" \
"ds_read_b128 a[196:199], v206 offset:12288\n" \
"v_add_u32 v212, s71, v229\n" \
"v_mfma_f32_16x16x32_bf16 v[40:43], a[100:103], a[76:79], v[40:43]\n" \
"s_lshl_b32 s70, s88, 3\n" \
"ds_read_b64_tr_b16 a[200:201], v210 offset:2048\n" \
"global_load_lds_dwordx4 v253, s[48:49] offset:0\n" \
"v_mfma_f32_16x16x32_bf16 v[0:3], a[104:107], a[72:75], v[0:3]\n" \
"s_add_u32 s48, s48, 0x60000\n" \
"s_addc_u32 s49, s49, 0\n" \
"ds_read_b64_tr_b16 a[202:203], v210 offset:3072\n" \
"v_mfma_f32_16x16x32_bf16 v[0:3], a[108:111], a[76:79], v[0:3]\n" \
"s_waitcnt lgkmcnt(3)\n" \
"s_add_u32 s70, s70, s83\n" \
"s_lshl_b32 s76, s70, 16\n" \
"v_mfma_f32_16x16x32_bf16 v[4:7], a[112:115], a[72:75], v[4:7]\n" \
"s_lshl_b32 s71, s61, 14\n" \
"s_add_u32 s76, s76, s71\n" \
"ds_read_b64_tr_b16 a[204:205], v210 offset:14336\n" \
"v_mfma_f32_16x16x32_bf16 v[4:7], a[116:119], a[76:79], v[4:7]\n" \
"s_add_u32 s76, s76, 57196544\n" \
"ds_read_b64_tr_b16 a[206:207], v210 offset:15360\n" \
"global_load_lds_dwordx4 v253, s[50:51] offset:-3072\n" \
"v_mfma_f32_16x16x32_bf16 v[48:51], a[96:99], a[80:83], v[48:51]\n" \
"s_lshl_b32 s79, s70, 7\n" \
"s_add_u32 s79, s79, 65601536\n" \
"ds_read_b64_tr_b16 a[208:209], v209 offset:4096\n" \
"v_mfma_f32_16x16x32_bf16 v[48:51], a[100:103], a[84:87], v[48:51]\n" \
"s_mov_b32 s70, 0xfff80000\n" \
"Lpg9930_%=:\n" \
"s_cbranch_execz Lpg12185_%=\n" \
"s_cmp_eq_u32 s78, 0\n" \
"s_cselect_b32 s80, 0x7ffff, s70\n" \
"v_mfma_f32_16x16x32_bf16 v[8:11], a[104:107], a[80:83], v[8:11]\n" \
"s_mov_b32 s74, 98304\n" \
"ds_read_b64_tr_b16 a[210:211], v209 offset:5120\n" \
"v_mfma_f32_16x16x32_bf16 v[8:11], a[108:111], a[84:87], v[8:11]\n" \
"ds_read_b64_tr_b16 a[212:213], v209 offset:16384\n" \
"global_load_lds_dwordx4 v253, s[50:51] offset:-1024\n" \
"v_mfma_f32_16x16x32_bf16 v[12:15], a[112:115], a[80:83], v[12:15]\n" \
"s_sub_u32 s74, s74, 32768\n" \
"s_cselect_b32 s74, 131072, s74\n" \
"ds_read_b64_tr_b16 a[214:215], v209 offset:17408\n" \
"v_mfma_f32_16x16x32_bf16 v[12:15], a[116:119], a[84:87], v[12:15]\n" \
"v_add_u32 v213, s74, v211\n" \
"v_add_u32 v214, s74, v212\n" \
"v_mfma_f32_16x16x32_bf16 v[56:59], a[96:99], a[88:91], v[56:59]\n" \
"ds_read_b64_tr_b16 a[216:217], v210 offset:4096\n" \
"v_xor_b32 v215, 32, v214\n" \
"v_mfma_f32_16x16x32_bf16 v[56:59], a[100:103], a[92:95], v[56:59]\n" \
"s_and_b32 s89, s61, 1\n" \
"ds_read_b64_tr_b16 a[218:219], v210 offset:5120\n" \
"global_load_lds_dwordx4 v253, s[50:51] offset:1024\n" \
"v_mfma_f32_16x16x32_bf16 v[16:19], a[104:107], a[88:91], v[16:19]\n" \
"s_add_u32 s50, s50, 0x60000\n" \
"s_addc_u32 s51, s51, 0\n" \
"ds_read_b64_tr_b16 a[220:221], v210 offset:16384\n" \
"v_mfma_f32_16x16x32_bf16 v[16:19], a[108:111], a[92:95], v[16:19]\n" \
"s_lshl_b32 s89, s89, 11\n" \
"s_add_u32 s87, s87, s89\n" \
"s_add_u32 s87, s87, 52740096\n" \
"v_mfma_f32_16x16x32_bf16 v[20:23], a[112:115], a[88:91], v[20:23]\n" \
"s_cmp_eq_u32 s88, 0\n" \
"s_cselect_b32 s86, -1, 0\n" \
"ds_read_b64_tr_b16 a[222:223], v210 offset:17408\n" \
"v_mfma_f32_16x16x32_bf16 v[20:23], a[116:119], a[92:95], v[20:23]\n" \
"s_waitcnt lgkmcnt(3)\n" \
"global_load_dword v204, v203, s[22:23]\n" \
"v_mfma_f32_16x16x32_bf16 v[64:67], a[96:99], a[192:195], v[64:67]\n" \
"s_mul_i32 s89, s88, 278528\n" \
"v_add_u32 v207, 0xffff3000, v207\n" \
"v_add_u32 v208, 0xffff3000, v208\n" \
"v_mfma_f32_16x16x32_bf16 v[64:67], a[100:103], a[196:199], v[64:67]\n" \
"s_add_u32 s87, s87, s89\n" \
"v_add_u32 v205, 0xffff3000, v205\n" \
"v_add_u32 v206, 0xffff3000, v206\n" \
"v_mfma_f32_16x16x32_bf16 v[24:27], a[104:107], a[192:195], v[24:27]\n" \
"s_lshr_b32 s89, s61, 1\n" \
"v_add_u32 v209, 0xffff3000, v209\n" \
"v_add_u32 v210, 0xffff3000, v210\n" \
"v_mfma_f32_16x16x32_bf16 v[24:27], a[108:111], a[196:199], v[24:27]\n" \
"s_lshl_b32 s89, s89, 8\n" \
"s_add_u32 s87, s87, s89\n" \
"s_lshl_b32 s89, s61, 4\n" \
"v_mfma_f32_16x16x32_bf16 v[28:31], a[112:115], a[192:195], v[28:31]\n" \
"s_waitcnt vmcnt(12) lgkmcnt(0)\n" \
"v_subrev_u32 v147, s89, v251\n" \
"s_lshr_b32 s89, s61, 1\n" \
"v_mfma_f32_16x16x32_bf16 v[28:31], a[116:119], a[196:199], v[28:31]\n" \
"s_barrier\n" \
"v_mfma_f32_16x16x32_bf16 v[72:75], a[64:67], a[208:211], v[72:75]\n" \
"s_add_u32 m0, s95, 118784\n" \
"s_lshl_b32 s90, s89, 9\n" \
"ds_read_b64_tr_b16 a[96:97], v209 offset:2048\n" \
"v_mfma_f32_16x16x32_bf16 v[72:75], a[68:71], a[212:215], v[72:75]\n" \
"s_add_u32 s90, s90, 511\n" \
"ds_read_b64_tr_b16 a[98:99], v209 offset:3072\n" \
"global_load_lds_dwordx4 v252, s[42:43] offset:0\n" \
"v_mfma_f32_16x16x32_bf16 v[44:47], a[200:203], a[72:75], v[44:47]\n" \
"ds_read_b128 a[64:67], v207 offset:0\n" \
"v_sub_u32 v145, s90, v248\n" \
"v_mfma_f32_16x16x32_bf16 v[44:47], a[204:207], a[76:79], v[44:47]\n" \
"s_lshl_b32 s90, s89, 3\n" \
"s_not_b32 s90, s90\n" \
"v_sub_u32 v146, s90, v147\n" \
"v_mfma_f32_16x16x32_bf16 v[80:83], a[72:75], a[208:211], v[80:83]\n" \
"ds_read_b128 a[68:71], v208 offset:0\n" \
"v_subrev_u32 v152, 0, v147\n" \
"v_mfma_f32_16x16x32_bf16 v[80:83], a[76:79], a[212:215], v[80:83]\n" \
"ds_read_b64_tr_b16 a[100:101], v209 offset:14336\n" \
"global_load_lds_dwordx4 v252, s[42:43] offset:2048\n" \
"v_mfma_f32_16x16x32_bf16 v[84:87], a[72:75], a[216:219], v[84:87]\n" \
"s_add_u32 s42, s42, 0x80\n" \
"s_addc_u32 s43, s43, 0\n" \
"ds_read_b64_tr_b16 a[102:103], v209 offset:15360\n" \
"v_mfma_f32_16x16x32_bf16 v[84:87], a[76:79], a[220:223], v[84:87]\n" \
"ds_read_b64_tr_b16 a[104:105], v209 offset:0\n" \
"v_sub_u32 v144, 7, v147\n" \
"v_mfma_f32_16x16x32_bf16 v[52:55], a[200:203], a[80:83], v[52:55]\n" \
"ds_read_b128 a[72:75], v205 offset:0\n" \
"v_or_b32 v152, v152, v144\n" \
"v_mfma_f32_16x16x32_bf16 v[52:55], a[204:207], a[84:87], v[52:55]\n" \
"ds_read_b128 a[76:79], v206 offset:0\n" \
"global_load_lds_dwordx4 v252, s[44:45] offset:1024\n" \
"v_mfma_f32_16x16x32_bf16 v[88:91], a[80:83], a[208:211], v[88:91]\n" \
"ds_read_b64_tr_b16 a[106:107], v209 offset:1024\n" \
"v_ashrrev_i32 v152, 31, v152\n" \
"v_mfma_f32_16x16x32_bf16 v[88:91], a[84:87], a[212:215], v[88:91]\n" \
"v_and_b32 v152, 0xff800000, v152\n" \
"v_mov_b32 v160, v152\n" \
"v_mfma_f32_16x16x32_bf16 v[92:95], a[80:83], a[216:219], v[92:95]\n" \
"ds_read_b64_tr_b16 a[108:109], v209 offset:12288\n" \
"v_mov_b32 v168, v152\n" \
"v_mfma_f32_16x16x32_bf16 v[92:95], a[84:87], a[220:223], v[92:95]\n" \
"ds_read_b64_tr_b16 a[110:111], v209 offset:13312\n" \
"global_load_lds_dwordx4 v252, s[44:45] offset:3072\n" \
"v_mfma_f32_16x16x32_bf16 v[60:63], a[200:203], a[88:91], v[60:63]\n" \
"s_add_u32 s44, s44, 0x80\n" \
"s_addc_u32 s45, s45, 0\n" \
"ds_read_b128 a[80:83], v205 offset:4096\n" \
"v_mfma_f32_16x16x32_bf16 v[60:63], a[204:207], a[92:95], v[60:63]\n" \
"ds_read_b64_tr_b16 a[112:113], v210 offset:0\n" \
"v_mov_b32 v176, v152\n" \
"v_mfma_f32_16x16x32_bf16 v[96:99], a[88:91], a[208:211], v[96:99]\n" \
"s_add_u32 m0, s85, 135168\n" \
"ds_read_b128 a[84:87], v206 offset:4096\n" \
"v_sub_u32 v156, -9, v147\n" \
"v_mfma_f32_16x16x32_bf16 v[96:99], a[92:95], a[212:215], v[96:99]\n" \
"ds_read_b64_tr_b16 a[114:115], v210 offset:1024\n" \
"global_load_lds_dwordx4 v246, s[46:47]\n" \
"v_mfma_f32_16x16x32_bf16 v[100:103], a[88:91], a[216:219], v[100:103]\n" \
"s_add_u32 s46, s46, 0x80\n" \
"s_addc_u32 s47, s47, 0\n" \
"ds_read_b64_tr_b16 a[116:117], v210 offset:12288\n" \
"v_mfma_f32_16x16x32_bf16 v[100:103], a[92:95], a[220:223], v[100:103]\n" \
"v_ashrrev_i32 v156, 31, v156\n" \
"v_and_b32 v156, 0xff800000, v156\n" \
"v_mfma_f32_16x16x32_bf16 v[68:71], a[200:203], a[192:195], v[68:71]\n" \
"s_add_u32 m0, s84, 143360\n" \
"ds_read_b128 a[88:91], v205 offset:8192\n" \
"v_mov_b32 v164, v156\n" \
"v_mfma_f32_16x16x32_bf16 v[68:71], a[204:207], a[196:199], v[68:71]\n" \
"ds_read_b128 a[92:95], v206 offset:8192\n" \
"global_load_lds_dwordx4 v253, s[48:49] offset:-4096\n" \
"v_mfma_f32_16x16x32_bf16 v[104:107], a[192:195], a[208:211], v[104:107]\n" \
"ds_read_b64_tr_b16 a[118:119], v210 offset:13312\n" \
"v_mov_b32 v172, v156\n" \
"v_mfma_f32_16x16x32_bf16 v[104:107], a[196:199], a[212:215], v[104:107]\n" \
"v_add_u32 v148, 0, v146\n" \
"v_or3_b32 v148, v148, v145, s86\n" \
"v_mfma_f32_16x16x32_bf16 v[108:111], a[192:195], a[216:219], v[108:111]\n" \
"v_ashrrev_i32 v148, 31, v148\n" \
"v_and_b32 v148, 0xff800000, v148\n" \
"v_mfma_f32_16x16x32_bf16 v[108:111], a[196:199], a[220:223], v[108:111]\n" \
"s_waitcnt lgkmcnt(2)\n" \
"global_load_lds_dwordx4 v253, s[48:49] offset:-2048\n" \
"v_subrev_u32 v153, 1, v147\n" \
"v_mfma_f32_16x16x32_bf16 v[32:35], a[96:99], a[64:67], v[32:35]\n" \
"ds_read_b128 a[192:195], v205 offset:12288\n" \
"v_sub_u32 v144, 8, v147\n" \
"v_mfma_f32_16x16x32_bf16 v[32:35], a[100:103], a[68:71], v[32:35]\n" \
"v_or_b32 v153, v153, v144\n" \
"v_ashrrev_i32 v153, 31, v153\n" \
"v_mfma_f32_16x16x32_bf16 v[40:43], a[96:99], a[72:75], v[40:43]\n" \
"ds_read_b128 a[196:199], v206 offset:12288\n" \
"v_and_b32 v153, 0xff800000, v153\n" \
"v_mfma_f32_16x16x32_bf16 v[40:43], a[100:103], a[76:79], v[40:43]\n" \
"ds_read_b64_tr_b16 a[200:201], v210 offset:2048\n" \
"global_load_lds_dwordx4 v253, s[48:49] offset:0\n" \
"v_mfma_f32_16x16x32_bf16 v[0:3], a[104:107], a[72:75], v[0:3]\n" \
"s_add_u32 s48, s48, 0x60000\n" \
"s_addc_u32 s49, s49, 0\n" \
"ds_read_b64_tr_b16 a[202:203], v210 offset:3072\n" \
"v_mfma_f32_16x16x32_bf16 v[0:3], a[108:111], a[76:79], v[0:3]\n" \
"s_waitcnt lgkmcnt(3)\n" \
"v_mov_b32 v161, v153\n" \
"v_mov_b32 v169, v153\n" \
"v_mfma_f32_16x16x32_bf16 v[4:7], a[112:115], a[72:75], v[4:7]\n" \
"ds_read_b64_tr_b16 a[204:205], v210 offset:14336\n" \
"v_mov_b32 v177, v153\n" \
"v_mfma_f32_16x16x32_bf16 v[4:7], a[116:119], a[76:79], v[4:7]\n" \
"ds_read_b64_tr_b16 a[206:207], v210 offset:15360\n" \
"global_load_lds_dwordx4 v253, s[50:51] offset:-3072\n" \
"v_mfma_f32_16x16x32_bf16 v[48:51], a[96:99], a[80:83], v[48:51]\n" \
"ds_read_b64_tr_b16 a[208:209], v209 offset:4096\n" \
"v_sub_u32 v157, -8, v147\n" \
"v_mfma_f32_16x16x32_bf16 v[48:51], a[100:103], a[84:87], v[48:51]\n" \
"v_ashrrev_i32 v157, 31, v157\n" \
"v_and_b32 v157, 0xff800000, v157\n" \
"v_mfma_f32_16x16x32_bf16 v[8:11], a[104:107], a[80:83], v[8:11]\n" \
"ds_read_b64_tr_b16 a[210:211], v209 offset:5120\n" \
"v_mov_b32 v165, v157\n" \
"v_mfma_f32_16x16x32_bf16 v[8:11], a[108:111], a[84:87], v[8:11]\n" \
"ds_read_b64_tr_b16 a[212:213], v209 offset:16384\n" \
"global_load_lds_dwordx4 v253, s[50:51] offset:-1024\n" \
"v_mfma_f32_16x16x32_bf16 v[12:15], a[112:115], a[80:83], v[12:15]\n" \
"ds_read_b64_tr_b16 a[214:215], v209 offset:17408\n" \
"v_mov_b32 v173, v157\n" \
"v_mfma_f32_16x16x32_bf16 v[12:15], a[116:119], a[84:87], v[12:15]\n" \
"v_add_u32 v149, 1, v146\n" \
"v_or3_b32 v149, v149, v145, s86\n" \
"v_mfma_f32_16x16x32_bf16 v[56:59], a[96:99], a[88:91], v[56:59]\n" \
"ds_read_b64_tr_b16 a[216:217], v210 offset:4096\n" \
"v_ashrrev_i32 v149, 31, v149\n" \
"v_mfma_f32_16x16x32_bf16 v[56:59], a[100:103], a[92:95], v[56:59]\n" \
"ds_read_b64_tr_b16 a[218:219], v210 offset:5120\n" \
"global_load_lds_dwordx4 v253, s[50:51] offset:1024\n" \
"v_mfma_f32_16x16x32_bf16 v[16:19], a[104:107], a[88:91], v[16:19]\n" \
"s_add_u32 s50, s50, 0x60000\n" \
"s_addc_u32 s51, s51, 0\n" \
"ds_read_b64_tr_b16 a[220:221], v210 offset:16384\n" \
"v_mfma_f32_16x16x32_bf16 v[16:19], a[108:111], a[92:95], v[16:19]\n" \
"v_and_b32 v149, 0xff800000, v149\n" \
"v_subrev_u32 v154, 2, v147\n" \
"v_mfma_f32_16x16x32_bf16 v[20:23], a[112:115], a[88:91], v[20:23]\n" \
"ds_read_b64_tr_b16 a[222:223], v210 offset:17408\n" \
"v_sub_u32 v144, 9, v147\n" \
"v_mfma_f32_16x16x32_bf16 v[20:23], a[116:119], a[92:95], v[20:23]\n" \
"s_waitcnt lgkmcnt(3)\n" \
"v_add_u32 v207, 0xffff0000, v207\n" \
"v_add_u32 v208, 0xffff0000, v208\n" \
"v_mfma_f32_16x16x32_bf16 v[64:67], a[96:99], a[192:195], v[64:67]\n" \
"v_add_u32 v205, 0xffff0000, v205\n" \
"v_add_u32 v206, 0xffff0000, v206\n" \
"v_mfma_f32_16x16x32_bf16 v[64:67], a[100:103], a[196:199], v[64:67]\n" \
"v_add_u32 v209, 0xffff0000, v209\n" \
"v_add_u32 v210, 0xffff0000, v210\n" \
"v_mfma_f32_16x16x32_bf16 v[24:27], a[104:107], a[192:195], v[24:27]\n" \
"v_or_b32 v154, v154, v144\n" \
"v_ashrrev_i32 v154, 31, v154\n" \
"v_mfma_f32_16x16x32_bf16 v[24:27], a[108:111], a[196:199], v[24:27]\n" \
"v_and_b32 v154, 0xff800000, v154\n" \
"v_mov_b32 v162, v154\n" \
"v_mfma_f32_16x16x32_bf16 v[28:31], a[112:115], a[192:195], v[28:31]\n" \
"s_waitcnt vmcnt(12) lgkmcnt(0)\n" \
"v_mov_b32 v170, v154\n" \
"v_mov_b32 v178, v154\n" \
"v_mfma_f32_16x16x32_bf16 v[28:31], a[116:119], a[196:199], v[28:31]\n" \
"s_barrier\n" \
"v_mfma_f32_16x16x32_bf16 v[72:75], a[64:67], a[208:211], v[72:75]\n" \
"s_add_u32 m0, s95, 65536\n" \
"ds_read_b64_tr_b16 a[96:97], v209 offset:2048\n" \
"v_sub_u32 v158, -7, v147\n" \
"v_mfma_f32_16x16x32_bf16 v[72:75], a[68:71], a[212:215], v[72:75]\n" \
"ds_read_b64_tr_b16 a[98:99], v209 offset:3072\n" \
"global_load_lds_dwordx4 v252, s[42:43] offset:0\n" \
"v_mfma_f32_16x16x32_bf16 v[44:47], a[200:203], a[72:75], v[44:47]\n" \
"ds_read_b128 a[64:67], v207 offset:0\n" \
"v_ashrrev_i32 v158, 31, v158\n" \
"v_mfma_f32_16x16x32_bf16 v[44:47], a[204:207], a[76:79], v[44:47]\n" \
"v_and_b32 v158, 0xff800000, v158\n" \
"v_mov_b32 v166, v158\n" \
"v_mfma_f32_16x16x32_bf16 v[80:83], a[72:75], a[208:211], v[80:83]\n" \
"ds_read_b128 a[68:71], v208 offset:0\n" \
"v_mov_b32 v174, v158\n" \
"v_mfma_f32_16x16x32_bf16 v[80:83], a[76:79], a[212:215], v[80:83]\n" \
"ds_read_b64_tr_b16 a[100:101], v209 offset:14336\n" \
"global_load_lds_dwordx4 v252, s[42:43] offset:2048\n" \
"v_mfma_f32_16x16x32_bf16 v[84:87], a[72:75], a[216:219], v[84:87]\n" \
"s_add_u32 s42, s42, 0x80\n" \
"s_addc_u32 s43, s43, 0\n" \
"ds_read_b64_tr_b16 a[102:103], v209 offset:15360\n" \
"v_mfma_f32_16x16x32_bf16 v[84:87], a[76:79], a[220:223], v[84:87]\n" \
"ds_read_b64_tr_b16 a[104:105], v209 offset:0\n" \
"v_add_u32 v150, 2, v146\n" \
"v_mfma_f32_16x16x32_bf16 v[52:55], a[200:203], a[80:83], v[52:55]\n" \
"ds_read_b128 a[72:75], v205 offset:0\n" \
"v_or3_b32 v150, v150, v145, s86\n" \
"v_mfma_f32_16x16x32_bf16 v[52:55], a[204:207], a[84:87], v[52:55]\n" \
"ds_read_b128 a[76:79], v206 offset:0\n" \
"global_load_lds_dwordx4 v252, s[44:45] offset:1024\n" \
"v_mfma_f32_16x16x32_bf16 v[88:91], a[80:83], a[208:211], v[88:91]\n" \
"ds_read_b64_tr_b16 a[106:107], v209 offset:1024\n" \
"v_ashrrev_i32 v150, 31, v150\n" \
"v_mfma_f32_16x16x32_bf16 v[88:91], a[84:87], a[212:215], v[88:91]\n" \
"v_and_b32 v150, 0xff800000, v150\n" \
"v_subrev_u32 v155, 3, v147\n" \
"v_mfma_f32_16x16x32_bf16 v[92:95], a[80:83], a[216:219], v[92:95]\n" \
"ds_read_b64_tr_b16 a[108:109], v209 offset:12288\n" \
"v_sub_u32 v144, 10, v147\n" \
"v_mfma_f32_16x16x32_bf16 v[92:95], a[84:87], a[220:223], v[92:95]\n" \
"ds_read_b64_tr_b16 a[110:111], v209 offset:13312\n" \
"global_load_lds_dwordx4 v252, s[44:45] offset:3072\n" \
"v_mfma_f32_16x16x32_bf16 v[60:63], a[200:203], a[88:91], v[60:63]\n" \
"s_add_u32 s44, s44, 0x80\n" \
"s_addc_u32 s45, s45, 0\n" \
"ds_read_b128 a[80:83], v205 offset:4096\n" \
"v_mfma_f32_16x16x32_bf16 v[60:63], a[204:207], a[92:95], v[60:63]\n" \
"ds_read_b64_tr_b16 a[112:113], v210 offset:0\n" \
"v_or_b32 v155, v155, v144\n" \
"v_mfma_f32_16x16x32_bf16 v[96:99], a[88:91], a[208:211], v[96:99]\n" \
"s_add_u32 m0, s85, 81920\n" \
"ds_read_b128 a[84:87], v206 offset:4096\n" \
"v_ashrrev_i32 v155, 31, v155\n" \
"v_mfma_f32_16x16x32_bf16 v[96:99], a[92:95], a[212:215], v[96:99]\n" \
"ds_read_b64_tr_b16 a[114:115], v210 offset:1024\n" \
"global_load_lds_dwordx4 v246, s[46:47]\n" \
"v_mfma_f32_16x16x32_bf16 v[100:103], a[88:91], a[216:219], v[100:103]\n" \
"s_add_u32 s46, s46, 0x80\n" \
"s_addc_u32 s47, s47, 0\n" \
"ds_read_b64_tr_b16 a[116:117], v210 offset:12288\n" \
"v_mfma_f32_16x16x32_bf16 v[100:103], a[92:95], a[220:223], v[100:103]\n" \
"v_and_b32 v155, 0xff800000, v155\n" \
"v_mov_b32 v163, v155\n" \
"v_mfma_f32_16x16x32_bf16 v[68:71], a[200:203], a[192:195], v[68:71]\n" \
"s_add_u32 m0, s84, 90112\n" \
"ds_read_b128 a[88:91], v205 offset:8192\n" \
"v_mov_b32 v171, v155\n" \
"v_mfma_f32_16x16x32_bf16 v[68:71], a[204:207], a[196:199], v[68:71]\n" \
"ds_read_b128 a[92:95], v206 offset:8192\n" \
"global_load_lds_dwordx4 v253, s[48:49] offset:-4096\n" \
"v_mfma_f32_16x16x32_bf16 v[104:107], a[192:195], a[208:211], v[104:107]\n" \
"ds_read_b64_tr_b16 a[118:119], v210 offset:13312\n" \
"v_mov_b32 v179, v155\n" \
"v_mfma_f32_16x16x32_bf16 v[104:107], a[196:199], a[212:215], v[104:107]\n" \
"v_sub_u32 v159, -6, v147\n" \
"v_ashrrev_i32 v159, 31, v159\n" \
"v_mfma_f32_16x16x32_bf16 v[108:111], a[192:195], a[216:219], v[108:111]\n" \
"v_and_b32 v159, 0xff800000, v159\n" \
"v_mov_b32 v167, v159\n" \
"v_mfma_f32_16x16x32_bf16 v[108:111], a[196:199], a[220:223], v[108:111]\n" \
"s_waitcnt lgkmcnt(2)\n" \
"global_load_lds_dwordx4 v253, s[48:49] offset:-2048\n" \
"v_mov_b32 v175, v159\n" \
"v_mfma_f32_16x16x32_bf16 v[32:35], a[96:99], a[64:67], v[32:35]\n" \
"ds_read_b128 a[192:195], v205 offset:12288\n" \
"v_add_u32 v151, 3, v146\n" \
"v_mfma_f32_16x16x32_bf16 v[32:35], a[100:103], a[68:71], v[32:35]\n" \
"v_or3_b32 v151, v151, v145, s86\n" \
"v_ashrrev_i32 v151, 31, v151\n" \
"v_mfma_f32_16x16x32_bf16 v[40:43], a[96:99], a[72:75], v[40:43]\n" \
"ds_read_b128 a[196:199], v206 offset:12288\n" \
"v_and_b32 v151, 0xff800000, v151\n" \
"v_mfma_f32_16x16x32_bf16 v[40:43], a[100:103], a[76:79], v[40:43]\n" \
"ds_read_b64_tr_b16 a[200:201], v210 offset:2048\n" \
"global_load_lds_dwordx4 v253, s[48:49] offset:0\n" \
"v_mfma_f32_16x16x32_bf16 v[0:3], a[104:107], a[72:75], v[0:3]\n" \
"s_add_u32 s48, s48, 0x60000\n" \
"s_addc_u32 s49, s49, 0\n" \
"ds_read_b64_tr_b16 a[202:203], v210 offset:3072\n" \
"v_mfma_f32_16x16x32_bf16 v[0:3], a[108:111], a[76:79], v[0:3]\n" \
"s_waitcnt lgkmcnt(3)\n" \
"v_mov_b32 v144, 0x3e8293ee\n" \
"s_add_u32 s90, s95, 117760\n" \
"v_mfma_f32_16x16x32_bf16 v[4:7], a[112:115], a[72:75], v[4:7]\n" \
"ds_read_b64_tr_b16 a[204:205], v210 offset:14336\n" \
"v_add_u32 v223, s90, v248\n" \
"v_mfma_f32_16x16x32_bf16 v[4:7], a[116:119], a[76:79], v[4:7]\n" \
"ds_read_b64_tr_b16 a[206:207], v210 offset:15360\n" \
"global_load_lds_dwordx4 v253, s[50:51] offset:-3072\n" \
"v_mfma_f32_16x16x32_bf16 v[48:51], a[96:99], a[80:83], v[48:51]\n" \
"s_cmp_lg_u32 s89, 0\n" \
"s_cselect_b64 s[92:93], -1, 0\n" \
"ds_read_b64_tr_b16 a[208:209], v209 offset:4096\n" \
"v_mfma_f32_16x16x32_bf16 v[48:51], a[100:103], a[84:87], v[48:51]\n" \
"v_xor_b32 v145, 0x2000, v223\n" \
"v_mfma_f32_16x16x32_bf16 v[8:11], a[104:107], a[80:83], v[8:11]\n" \
"ds_read_b64_tr_b16 a[210:211], v209 offset:5120\n" \
"v_mfma_f32_16x16x32_bf16 v[8:11], a[108:111], a[84:87], v[8:11]\n" \
"ds_read_b64_tr_b16 a[212:213], v209 offset:16384\n" \
"global_load_lds_dwordx4 v253, s[50:51] offset:-1024\n" \
"v_mfma_f32_16x16x32_bf16 v[12:15], a[112:115], a[80:83], v[12:15]\n" \
"ds_read_b64_tr_b16 a[214:215], v209 offset:17408\n" \
"v_mfma_f32_16x16x32_bf16 v[12:15], a[116:119], a[84:87], v[12:15]\n" \
"v_mfma_f32_16x16x32_bf16 v[56:59], a[96:99], a[88:91], v[56:59]\n" \
"ds_read_b64_tr_b16 a[216:217], v210 offset:4096\n" \
"v_mfma_f32_16x16x32_bf16 v[56:59], a[100:103], a[92:95], v[56:59]\n" \
"ds_read_b64_tr_b16 a[218:219], v210 offset:5120\n" \
"global_load_lds_dwordx4 v253, s[50:51] offset:1024\n" \
"v_mfma_f32_16x16x32_bf16 v[16:19], a[104:107], a[88:91], v[16:19]\n" \
"s_add_u32 s50, s50, 0x60000\n" \
"s_addc_u32 s51, s51, 0\n" \
"ds_read_b64_tr_b16 a[220:221], v210 offset:16384\n" \
"v_mfma_f32_16x16x32_bf16 v[16:19], a[108:111], a[92:95], v[16:19]\n" \
"v_mfma_f32_16x16x32_bf16 v[20:23], a[112:115], a[88:91], v[20:23]\n" \
"ds_read_b64_tr_b16 a[222:223], v210 offset:17408\n" \
"v_mfma_f32_16x16x32_bf16 v[20:23], a[116:119], a[92:95], v[20:23]\n" \
"s_waitcnt lgkmcnt(3)\n" \
"v_add_u32 v207, 0x1d000, v207\n" \
"v_add_u32 v208, 0x1d000, v208\n" \
"v_mfma_f32_16x16x32_bf16 v[64:67], a[96:99], a[192:195], v[64:67]\n" \
"v_add_u32 v205, 0x1d000, v205\n" \
"v_add_u32 v206, 0x1d000, v206\n" \
"v_mfma_f32_16x16x32_bf16 v[64:67], a[100:103], a[196:199], v[64:67]\n" \
"v_add_u32 v209, 0x1d000, v209\n" \
"v_add_u32 v210, 0x1d000, v210\n" \
"v_mfma_f32_16x16x32_bf16 v[24:27], a[104:107], a[192:195], v[24:27]\n" \
"v_mfma_f32_16x16x32_bf16 v[24:27], a[108:111], a[196:199], v[24:27]\n" \
"v_mfma_f32_16x16x32_bf16 v[28:31], a[112:115], a[192:195], v[28:31]\n" \
"s_waitcnt vmcnt(11) lgkmcnt(0)\n" \
"v_mfma_f32_16x16x32_bf16 v[28:31], a[116:119], a[196:199], v[28:31]\n" \
"s_barrier\n" \
"v_mfma_f32_16x16x32_bf16 v[72:75], a[64:67], a[208:211], v[72:75]\n" \
"s_add_u32 m0, s95, 0\n" \
"ds_read_b64_tr_b16 a[96:97], v209 offset:2048\n" \
"v_mfma_f32_16x16x32_bf16 v[72:75], a[68:71], a[212:215], v[72:75]\n" \
"ds_read_b64_tr_b16 a[98:99], v209 offset:3072\n" \
"global_load_lds_dwordx4 v252, s[42:43] offset:0\n" \
"v_mfma_f32_16x16x32_bf16 v[44:47], a[200:203], a[72:75], v[44:47]\n" \
"ds_read_b128 a[64:67], v207 offset:0\n" \
"v_mfma_f32_16x16x32_bf16 v[44:47], a[204:207], a[76:79], v[44:47]\n" \
"v_mfma_f32_16x16x32_bf16 v[80:83], a[72:75], a[208:211], v[80:83]\n" \
"ds_read_b128 a[68:71], v208 offset:0\n" \
"v_mfma_f32_16x16x32_bf16 v[80:83], a[76:79], a[212:215], v[80:83]\n" \
"ds_read_b64_tr_b16 a[100:101], v209 offset:14336\n" \
"global_load_lds_dwordx4 v252, s[42:43] offset:2048\n" \
"v_mfma_f32_16x16x32_bf16 v[84:87], a[72:75], a[216:219], v[84:87]\n" \
"s_add_u32 s42, s42, 0x100\n" \
"s_addc_u32 s43, s43, 0\n" \
"ds_read_b64_tr_b16 a[102:103], v209 offset:15360\n" \
"v_mfma_f32_16x16x32_bf16 v[84:87], a[76:79], a[220:223], v[84:87]\n" \
"ds_read_b64_tr_b16 a[104:105], v209 offset:0\n" \
"v_mfma_f32_16x16x32_bf16 v[52:55], a[200:203], a[80:83], v[52:55]\n" \
"ds_read_b128 a[72:75], v205 offset:0\n" \
"v_mfma_f32_16x16x32_bf16 v[52:55], a[204:207], a[84:87], v[52:55]\n" \
"ds_read_b128 a[76:79], v206 offset:0\n" \
"global_load_lds_dwordx4 v252, s[44:45] offset:1024\n" \
"v_mfma_f32_16x16x32_bf16 v[88:91], a[80:83], a[208:211], v[88:91]\n" \
"ds_read_b64_tr_b16 a[106:107], v209 offset:1024\n" \
"v_mfma_f32_16x16x32_bf16 v[88:91], a[84:87], a[212:215], v[88:91]\n" \
"v_mfma_f32_16x16x32_bf16 v[92:95], a[80:83], a[216:219], v[92:95]\n" \
"ds_read_b64_tr_b16 a[108:109], v209 offset:12288\n" \
"v_mfma_f32_16x16x32_bf16 v[92:95], a[84:87], a[220:223], v[92:95]\n" \
"ds_read_b64_tr_b16 a[110:111], v209 offset:13312\n" \
"global_load_lds_dwordx4 v252, s[44:45] offset:3072\n" \
"v_mfma_f32_16x16x32_bf16 v[60:63], a[200:203], a[88:91], v[60:63]\n" \
"s_add_u32 s44, s44, 0x100\n" \
"s_addc_u32 s45, s45, 0\n" \
"ds_read_b128 a[80:83], v205 offset:4096\n" \
"v_mfma_f32_16x16x32_bf16 v[60:63], a[204:207], a[92:95], v[60:63]\n" \
"ds_read_b64_tr_b16 a[112:113], v210 offset:0\n" \
"v_mfma_f32_16x16x32_bf16 v[96:99], a[88:91], a[208:211], v[96:99]\n" \
"ds_read_b128 a[84:87], v206 offset:4096\n" \
"s_add_u32 m0, s85, 16384\n" \
"v_mfma_f32_16x16x32_bf16 v[96:99], a[92:95], a[212:215], v[96:99]\n" \
"ds_read_b64_tr_b16 a[114:115], v210 offset:1024\n" \
"global_load_lds_dwordx4 v246, s[46:47]\n" \
"v_mfma_f32_16x16x32_bf16 v[100:103], a[88:91], a[216:219], v[100:103]\n" \
"s_add_u32 s46, s46, 0x100\n" \
"s_addc_u32 s47, s47, 0\n" \
"ds_read_b64_tr_b16 a[116:117], v210 offset:12288\n" \
"v_mfma_f32_16x16x32_bf16 v[100:103], a[92:95], a[220:223], v[100:103]\n" \
"v_mfma_f32_16x16x32_bf16 v[68:71], a[200:203], a[192:195], v[68:71]\n" \
"ds_read_b128 a[88:91], v205 offset:8192\n" \
"s_add_u32 m0, s84, 24576\n" \
"v_mfma_f32_16x16x32_bf16 v[68:71], a[204:207], a[196:199], v[68:71]\n" \
"ds_read_b128 a[92:95], v206 offset:8192\n" \
"global_load_lds_dwordx4 v253, s[48:49] offset:-4096\n" \
"v_mfma_f32_16x16x32_bf16 v[104:107], a[192:195], a[208:211], v[104:107]\n" \
"ds_read_b64_tr_b16 a[118:119], v210 offset:13312\n" \
"v_mfma_f32_16x16x32_bf16 v[104:107], a[196:199], a[212:215], v[104:107]\n" \
"v_mfma_f32_16x16x32_bf16 v[108:111], a[192:195], a[216:219], v[108:111]\n" \
"v_mfma_f32_16x16x32_bf16 v[108:111], a[196:199], a[220:223], v[108:111]\n" \
"s_waitcnt lgkmcnt(2)\n" \
"global_load_lds_dwordx4 v253, s[48:49] offset:-2048\n" \
"v_mfma_f32_16x16x32_bf16 v[32:35], a[96:99], a[64:67], v[32:35]\n" \
"ds_read_b128 a[192:195], v205 offset:12288\n" \
"v_mfma_f32_16x16x32_bf16 v[32:35], a[100:103], a[68:71], v[32:35]\n" \
"v_mfma_f32_16x16x32_bf16 v[40:43], a[96:99], a[72:75], v[40:43]\n" \
"ds_read_b128 a[196:199], v206 offset:12288\n" \
"v_mfma_f32_16x16x32_bf16 v[40:43], a[100:103], a[76:79], v[40:43]\n" \
"ds_read_b64_tr_b16 a[200:201], v210 offset:2048\n" \
"global_load_lds_dwordx4 v253, s[48:49] offset:0\n" \
"v_mfma_f32_16x16x32_bf16 v[0:3], a[104:107], a[72:75], v[0:3]\n" \
"s_add_u32 s48, s48, 0xc0000\n" \
"s_addc_u32 s49, s49, 0\n" \
"ds_read_b64_tr_b16 a[202:203], v210 offset:3072\n" \
"v_mfma_f32_16x16x32_bf16 v[0:3], a[108:111], a[76:79], v[0:3]\n" \
"s_waitcnt lgkmcnt(3)\n" \
"v_mfma_f32_16x16x32_bf16 v[4:7], a[112:115], a[72:75], v[4:7]\n" \
"ds_read_b64_tr_b16 a[204:205], v210 offset:14336\n" \
"v_mfma_f32_16x16x32_bf16 v[4:7], a[116:119], a[76:79], v[4:7]\n" \
"ds_read_b64_tr_b16 a[206:207], v210 offset:15360\n" \
"global_load_lds_dwordx4 v253, s[50:51] offset:-3072\n" \
"v_mfma_f32_16x16x32_bf16 v[48:51], a[96:99], a[80:83], v[48:51]\n" \
"ds_read_b64_tr_b16 a[208:209], v209 offset:4096\n" \
"v_mfma_f32_16x16x32_bf16 v[48:51], a[100:103], a[84:87], v[48:51]\n" \
"v_mfma_f32_16x16x32_bf16 v[8:11], a[104:107], a[80:83], v[8:11]\n" \
"ds_read_b64_tr_b16 a[210:211], v209 offset:5120\n" \
"v_mfma_f32_16x16x32_bf16 v[8:11], a[108:111], a[84:87], v[8:11]\n" \
"ds_read_b64_tr_b16 a[212:213], v209 offset:16384\n" \
"global_load_lds_dwordx4 v253, s[50:51] offset:-1024\n" \
"v_mfma_f32_16x16x32_bf16 v[12:15], a[112:115], a[80:83], v[12:15]\n" \
"ds_read_b64_tr_b16 a[214:215], v209 offset:17408\n" \
"v_mfma_f32_16x16x32_bf16 v[12:15], a[116:119], a[84:87], v[12:15]\n" \
"v_mfma_f32_16x16x32_bf16 v[56:59], a[96:99], a[88:91], v[56:59]\n" \
"ds_read_b64_tr_b16 a[216:217], v210 offset:4096\n" \
"v_mfma_f32_16x16x32_bf16 v[56:59], a[100:103], a[92:95], v[56:59]\n" \
"ds_read_b64_tr_b16 a[218:219], v210 offset:5120\n" \
"global_load_lds_dwordx4 v253, s[50:51] offset:1024\n" \
"v_mfma_f32_16x16x32_bf16 v[16:19], a[104:107], a[88:91], v[16:19]\n" \
"s_add_u32 s50, s50, 0xc0000\n" \
"s_addc_u32 s51, s51, 0\n" \
"ds_read_b64_tr_b16 a[220:221], v210 offset:16384\n" \
"v_mfma_f32_16x16x32_bf16 v[16:19], a[108:111], a[92:95], v[16:19]\n" \
"v_mfma_f32_16x16x32_bf16 v[20:23], a[112:115], a[88:91], v[20:23]\n" \
"ds_read_b64_tr_b16 a[222:223], v210 offset:17408\n" \
"v_mfma_f32_16x16x32_bf16 v[20:23], a[116:119], a[92:95], v[20:23]\n" \
"s_waitcnt lgkmcnt(3)\n" \
"v_add_u32 v207, 0xffff3000, v207\n" \
"v_add_u32 v208, 0xffff3000, v208\n" \
"v_mfma_f32_16x16x32_bf16 v[64:67], a[96:99], a[192:195], v[64:67]\n" \
"v_add_u32 v205, 0xffff3000, v205\n" \
"v_add_u32 v206, 0xffff3000, v206\n" \
"v_mfma_f32_16x16x32_bf16 v[64:67], a[100:103], a[196:199], v[64:67]\n" \
"v_add_u32 v209, 0xffff3000, v209\n" \
"v_add_u32 v210, 0xffff3000, v210\n" \
"v_mfma_f32_16x16x32_bf16 v[24:27], a[104:107], a[192:195], v[24:27]\n" \
"v_mfma_f32_16x16x32_bf16 v[24:27], a[108:111], a[196:199], v[24:27]\n" \
"v_mfma_f32_16x16x32_bf16 v[28:31], a[112:115], a[192:195], v[28:31]\n" \
"s_waitcnt vmcnt(11) lgkmcnt(0)\n" \
"v_mfma_f32_16x16x32_bf16 v[28:31], a[116:119], a[196:199], v[28:31]\n" \
"s_barrier\n" \
"v_mfma_f32_16x16x32_bf16 v[72:75], a[64:67], a[208:211], v[72:75]\n" \
"s_add_u32 m0, s95, 118784\n" \
"ds_read_b64_tr_b16 a[96:97], v209 offset:2048\n" \
"v_mfma_f32_16x16x32_bf16 v[72:75], a[68:71], a[212:215], v[72:75]\n" \
"ds_read_b64_tr_b16 a[98:99], v209 offset:3072\n" \
"global_load_lds_dwordx4 v252, s[42:43] offset:0\n" \
"v_mfma_f32_16x16x32_bf16 v[44:47], a[200:203], a[72:75], v[44:47]\n" \
"ds_read_b128 a[64:67], v207 offset:0\n" \
"v_mfma_f32_16x16x32_bf16 v[44:47], a[204:207], a[76:79], v[44:47]\n" \
"v_mfma_f32_16x16x32_bf16 v[80:83], a[72:75], a[208:211], v[80:83]\n" \
"ds_read_b128 a[68:71], v208 offset:0\n" \
"v_mfma_f32_16x16x32_bf16 v[80:83], a[76:79], a[212:215], v[80:83]\n" \
"ds_read_b64_tr_b16 a[100:101], v209 offset:14336\n" \
"global_load_lds_dwordx4 v252, s[42:43] offset:2048\n" \
"v_mfma_f32_16x16x32_bf16 v[84:87], a[72:75], a[216:219], v[84:87]\n" \
"s_add_u32 s42, s42, 0x80\n" \
"s_addc_u32 s43, s43, 0\n" \
"ds_read_b64_tr_b16 a[102:103], v209 offset:15360\n" \
"v_mfma_f32_16x16x32_bf16 v[84:87], a[76:79], a[220:223], v[84:87]\n" \
"ds_read_b64_tr_b16 a[104:105], v209 offset:0\n" \
"v_mfma_f32_16x16x32_bf16 v[52:55], a[200:203], a[80:83], v[52:55]\n" \
"ds_read_b128 a[72:75], v205 offset:0\n" \
"v_mfma_f32_16x16x32_bf16 v[52:55], a[204:207], a[84:87], v[52:55]\n" \
"ds_read_b128 a[76:79], v206 offset:0\n" \
"global_load_lds_dwordx4 v252, s[44:45] offset:1024\n" \
"v_mfma_f32_16x16x32_bf16 v[88:91], a[80:83], a[208:211], v[88:91]\n" \
"ds_read_b64_tr_b16 a[106:107], v209 offset:1024\n" \
"v_mfma_f32_16x16x32_bf16 v[88:91], a[84:87], a[212:215], v[88:91]\n" \
"v_mfma_f32_16x16x32_bf16 v[92:95], a[80:83], a[216:219], v[92:95]\n" \
"ds_read_b64_tr_b16 a[108:109], v209 offset:12288\n" \
"v_mfma_f32_16x16x32_bf16 v[92:95], a[84:87], a[220:223], v[92:95]\n" \
"ds_read_b64_tr_b16 a[110:111], v209 offset:13312\n" \
"global_load_lds_dwordx4 v252, s[44:45] offset:3072\n" \
"v_mfma_f32_16x16x32_bf16 v[60:63], a[200:203], a[88:91], v[60:63]\n" \
"s_add_u32 s44, s44, 0x80\n" \
"s_addc_u32 s45, s45, 0\n" \
"ds_read_b128 a[80:83], v205 offset:4096\n" \
"v_mfma_f32_16x16x32_bf16 v[60:63], a[204:207], a[92:95], v[60:63]\n" \
"ds_read_b64_tr_b16 a[112:113], v210 offset:0\n" \
"v_mfma_f32_16x16x32_bf16 v[96:99], a[88:91], a[208:211], v[96:99]\n" \
"ds_read_b128 a[84:87], v206 offset:4096\n" \
"s_add_u32 m0, s85, 135168\n" \
"v_mfma_f32_16x16x32_bf16 v[96:99], a[92:95], a[212:215], v[96:99]\n" \
"ds_read_b64_tr_b16 a[114:115], v210 offset:1024\n" \
"global_load_lds_dwordx4 v246, s[46:47]\n" \
"v_mfma_f32_16x16x32_bf16 v[100:103], a[88:91], a[216:219], v[100:103]\n" \
"s_add_u32 s46, s46, 0x80\n" \
"s_addc_u32 s47, s47, 0\n" \
"ds_read_b64_tr_b16 a[116:117], v210 offset:12288\n" \
"v_mfma_f32_16x16x32_bf16 v[100:103], a[92:95], a[220:223], v[100:103]\n" \
"v_mfma_f32_16x16x32_bf16 v[68:71], a[200:203], a[192:195], v[68:71]\n" \
"ds_read_b128 a[88:91], v205 offset:8192\n" \
"s_add_u32 m0, s84, 143360\n" \
"v_mfma_f32_16x16x32_bf16 v[68:71], a[204:207], a[196:199], v[68:71]\n" \
"ds_read_b128 a[92:95], v206 offset:8192\n" \
"global_load_lds_dwordx4 v253, s[48:49] offset:-4096\n" \
"v_mfma_f32_16x16x32_bf16 v[104:107], a[192:195], a[208:211], v[104:107]\n" \
"ds_read_b64_tr_b16 a[118:119], v210 offset:13312\n" \
"v_mfma_f32_16x16x32_bf16 v[104:107], a[196:199], a[212:215], v[104:107]\n" \
"v_mfma_f32_16x16x32_bf16 v[108:111], a[192:195], a[216:219], v[108:111]\n" \
"v_mfma_f32_16x16x32_bf16 v[108:111], a[196:199], a[220:223], v[108:111]\n" \
"s_waitcnt lgkmcnt(2)\n" \
"global_load_lds_dwordx4 v253, s[48:49] offset:-2048\n" \
"v_mfma_f32_16x16x32_bf16 v[32:35], a[96:99], a[64:67], v[32:35]\n" \
"ds_read_b128 a[192:195], v205 offset:12288\n" \
"v_mfma_f32_16x16x32_bf16 v[32:35], a[100:103], a[68:71], v[32:35]\n" \
"v_mfma_f32_16x16x32_bf16 v[40:43], a[96:99], a[72:75], v[40:43]\n" \
"Lpg10500_%=:\n" \
"s_cbranch_execz Lpg12679_%=\n" \
"ds_read_b128 a[196:199], v206 offset:12288\n" \
"v_mfma_f32_16x16x32_bf16 v[40:43], a[100:103], a[76:79], v[40:43]\n" \
"ds_read_b64_tr_b16 a[200:201], v210 offset:2048\n" \
"global_load_lds_dwordx4 v253, s[48:49] offset:0\n" \
"v_mfma_f32_16x16x32_bf16 v[0:3], a[104:107], a[72:75], v[0:3]\n" \
"s_add_u32 s48, s48, 0x60000\n" \
"s_addc_u32 s49, s49, 0\n" \
"ds_read_b64_tr_b16 a[202:203], v210 offset:3072\n" \
"v_mfma_f32_16x16x32_bf16 v[0:3], a[108:111], a[76:79], v[0:3]\n" \
"s_waitcnt lgkmcnt(3)\n" \
"v_mfma_f32_16x16x32_bf16 v[4:7], a[112:115], a[72:75], v[4:7]\n" \
"ds_read_b64_tr_b16 a[204:205], v210 offset:14336\n" \
"v_mfma_f32_16x16x32_bf16 v[4:7], a[116:119], a[76:79], v[4:7]\n" \
"ds_read_b64_tr_b16 a[206:207], v210 offset:15360\n" \
"global_load_lds_dwordx4 v253, s[50:51] offset:-3072\n" \
"v_mfma_f32_16x16x32_bf16 v[48:51], a[96:99], a[80:83], v[48:51]\n" \
"ds_read_b64_tr_b16 a[208:209], v209 offset:4096\n" \
"v_mfma_f32_16x16x32_bf16 v[48:51], a[100:103], a[84:87], v[48:51]\n" \
"v_mfma_f32_16x16x32_bf16 v[8:11], a[104:107], a[80:83], v[8:11]\n" \
"ds_read_b64_tr_b16 a[210:211], v209 offset:5120\n" \
"v_mfma_f32_16x16x32_bf16 v[8:11], a[108:111], a[84:87], v[8:11]\n" \
"ds_read_b64_tr_b16 a[212:213], v209 offset:16384\n" \
"global_load_lds_dwordx4 v253, s[50:51] offset:-1024\n" \
"v_mfma_f32_16x16x32_bf16 v[12:15], a[112:115], a[80:83], v[12:15]\n" \
"ds_read_b64_tr_b16 a[214:215], v209 offset:17408\n" \
"v_mfma_f32_16x16x32_bf16 v[12:15], a[116:119], a[84:87], v[12:15]\n" \
"v_mfma_f32_16x16x32_bf16 v[56:59], a[96:99], a[88:91], v[56:59]\n" \
"ds_read_b64_tr_b16 a[216:217], v210 offset:4096\n" \
"v_mfma_f32_16x16x32_bf16 v[56:59], a[100:103], a[92:95], v[56:59]\n" \
"ds_read_b64_tr_b16 a[218:219], v210 offset:5120\n" \
"global_load_lds_dwordx4 v253, s[50:51] offset:1024\n" \
"v_mfma_f32_16x16x32_bf16 v[16:19], a[104:107], a[88:91], v[16:19]\n" \
"s_add_u32 s50, s50, 0x60000\n" \
"s_addc_u32 s51, s51, 0\n" \
"ds_read_b64_tr_b16 a[220:221], v210 offset:16384\n" \
"v_mfma_f32_16x16x32_bf16 v[16:19], a[108:111], a[92:95], v[16:19]\n" \
"v_mfma_f32_16x16x32_bf16 v[20:23], a[112:115], a[88:91], v[20:23]\n" \
"ds_read_b64_tr_b16 a[222:223], v210 offset:17408\n" \
"v_mfma_f32_16x16x32_bf16 v[20:23], a[116:119], a[92:95], v[20:23]\n" \
"s_waitcnt lgkmcnt(3)\n" \
"v_add_u32 v207, 0xffff0000, v207\n" \
"v_add_u32 v208, 0xffff0000, v208\n" \
"v_mfma_f32_16x16x32_bf16 v[64:67], a[96:99], a[192:195], v[64:67]\n" \
"v_add_u32 v205, 0xffff0000, v205\n" \
"v_add_u32 v206, 0xffff0000, v206\n" \
"v_mfma_f32_16x16x32_bf16 v[64:67], a[100:103], a[196:199], v[64:67]\n" \
"v_add_u32 v209, 0xffff0000, v209\n" \
"v_add_u32 v210, 0xffff0000, v210\n" \
"v_mfma_f32_16x16x32_bf16 v[24:27], a[104:107], a[192:195], v[24:27]\n" \
"v_mfma_f32_16x16x32_bf16 v[24:27], a[108:111], a[196:199], v[24:27]\n" \
"v_mfma_f32_16x16x32_bf16 v[28:31], a[112:115], a[192:195], v[28:31]\n" \
"s_waitcnt vmcnt(11) lgkmcnt(0)\n" \
"v_mfma_f32_16x16x32_bf16 v[28:31], a[116:119], a[196:199], v[28:31]\n" \
"s_barrier\n" \
"v_mfma_f32_16x16x32_bf16 v[72:75], a[64:67], a[208:211], v[72:75]\n" \
"s_add_u32 m0, s95, 65536\n" \
"ds_read_b64_tr_b16 a[96:97], v209 offset:2048\n" \
"v_mfma_f32_16x16x32_bf16 v[72:75], a[68:71], a[212:215], v[72:75]\n" \
"ds_read_b64_tr_b16 a[98:99], v209 offset:3072\n" \
"global_load_lds_dwordx4 v252, s[42:43] offset:0\n" \
"v_mfma_f32_16x16x32_bf16 v[44:47], a[200:203], a[72:75], v[44:47]\n" \
"ds_read_b128 a[64:67], v207 offset:0\n" \
"v_mfma_f32_16x16x32_bf16 v[44:47], a[204:207], a[76:79], v[44:47]\n" \
"v_mfma_f32_16x16x32_bf16 v[80:83], a[72:75], a[208:211], v[80:83]\n" \
"ds_read_b128 a[68:71], v208 offset:0\n" \
"v_mfma_f32_16x16x32_bf16 v[80:83], a[76:79], a[212:215], v[80:83]\n" \
"ds_read_b64_tr_b16 a[100:101], v209 offset:14336\n" \
"global_load_lds_dwordx4 v252, s[42:43] offset:2048\n" \
"v_mfma_f32_16x16x32_bf16 v[84:87], a[72:75], a[216:219], v[84:87]\n" \
"s_add_u32 s42, s42, 0x80\n" \
"s_addc_u32 s43, s43, 0\n" \
"ds_read_b64_tr_b16 a[102:103], v209 offset:15360\n" \
"v_mfma_f32_16x16x32_bf16 v[84:87], a[76:79], a[220:223], v[84:87]\n" \
"ds_read_b64_tr_b16 a[104:105], v209 offset:0\n" \
"v_mfma_f32_16x16x32_bf16 v[52:55], a[200:203], a[80:83], v[52:55]\n" \
"ds_read_b128 a[72:75], v205 offset:0\n" \
"v_mfma_f32_16x16x32_bf16 v[52:55], a[204:207], a[84:87], v[52:55]\n" \
"ds_read_b128 a[76:79], v206 offset:0\n" \
"global_load_lds_dwordx4 v252, s[44:45] offset:1024\n" \
"v_mfma_f32_16x16x32_bf16 v[88:91], a[80:83], a[208:211], v[88:91]\n" \
"ds_read_b64_tr_b16 a[106:107], v209 offset:1024\n" \
"v_mfma_f32_16x16x32_bf16 v[88:91], a[84:87], a[212:215], v[88:91]\n" \
"v_mfma_f32_16x16x32_bf16 v[92:95], a[80:83], a[216:219], v[92:95]\n" \
"ds_read_b64_tr_b16 a[108:109], v209 offset:12288\n" \
"v_mfma_f32_16x16x32_bf16 v[92:95], a[84:87], a[220:223], v[92:95]\n" \
"ds_read_b64_tr_b16 a[110:111], v209 offset:13312\n" \
"global_load_lds_dwordx4 v252, s[44:45] offset:3072\n" \
"v_mfma_f32_16x16x32_bf16 v[60:63], a[200:203], a[88:91], v[60:63]\n" \
"s_add_u32 s44, s44, 0x80\n" \
"s_addc_u32 s45, s45, 0\n" \
"ds_read_b128 a[80:83], v205 offset:4096\n" \
"v_mfma_f32_16x16x32_bf16 v[60:63], a[204:207], a[92:95], v[60:63]\n" \
"ds_read_b64_tr_b16 a[112:113], v210 offset:0\n" \
"v_mfma_f32_16x16x32_bf16 v[96:99], a[88:91], a[208:211], v[96:99]\n" \
"ds_read_b128 a[84:87], v206 offset:4096\n" \
"s_add_u32 m0, s85, 81920\n" \
"v_mfma_f32_16x16x32_bf16 v[96:99], a[92:95], a[212:215], v[96:99]\n" \
"ds_read_b64_tr_b16 a[114:115], v210 offset:1024\n" \
"global_load_lds_dwordx4 v246, s[46:47]\n" \
"v_mfma_f32_16x16x32_bf16 v[100:103], a[88:91], a[216:219], v[100:103]\n" \
"s_add_u32 s46, s46, 0x80\n" \
"s_addc_u32 s47, s47, 0\n" \
"ds_read_b64_tr_b16 a[116:117], v210 offset:12288\n" \
"v_mfma_f32_16x16x32_bf16 v[100:103], a[92:95], a[220:223], v[100:103]\n" \
"v_mfma_f32_16x16x32_bf16 v[68:71], a[200:203], a[192:195], v[68:71]\n" \
"ds_read_b128 a[88:91], v205 offset:8192\n" \
"s_add_u32 m0, s84, 90112\n" \
"v_mfma_f32_16x16x32_bf16 v[68:71], a[204:207], a[196:199], v[68:71]\n" \
"ds_read_b128 a[92:95], v206 offset:8192\n" \
"global_load_lds_dwordx4 v253, s[48:49] offset:-4096\n" \
"v_mfma_f32_16x16x32_bf16 v[104:107], a[192:195], a[208:211], v[104:107]\n" \
"ds_read_b64_tr_b16 a[118:119], v210 offset:13312\n" \
"v_mfma_f32_16x16x32_bf16 v[104:107], a[196:199], a[212:215], v[104:107]\n" \
"v_mfma_f32_16x16x32_bf16 v[108:111], a[192:195], a[216:219], v[108:111]\n" \
"v_mfma_f32_16x16x32_bf16 v[108:111], a[196:199], a[220:223], v[108:111]\n" \
"s_waitcnt lgkmcnt(2)\n" \
"global_load_lds_dwordx4 v253, s[48:49] offset:-2048\n" \
"v_mfma_f32_16x16x32_bf16 v[32:35], a[96:99], a[64:67], v[32:35]\n" \
"ds_read_b128 a[192:195], v205 offset:12288\n" \
"v_mfma_f32_16x16x32_bf16 v[32:35], a[100:103], a[68:71], v[32:35]\n" \
"v_mfma_f32_16x16x32_bf16 v[40:43], a[96:99], a[72:75], v[40:43]\n" \
"ds_read_b128 a[196:199], v206 offset:12288\n" \
"v_mfma_f32_16x16x32_bf16 v[40:43], a[100:103], a[76:79], v[40:43]\n" \
"ds_read_b64_tr_b16 a[200:201], v210 offset:2048\n" \
"global_load_lds_dwordx4 v253, s[48:49] offset:0\n" \
"v_mfma_f32_16x16x32_bf16 v[0:3], a[104:107], a[72:75], v[0:3]\n" \
"s_add_u32 s48, s48, 0x60000\n" \
"s_addc_u32 s49, s49, 0\n" \
"ds_read_b64_tr_b16 a[202:203], v210 offset:3072\n" \
"v_mfma_f32_16x16x32_bf16 v[0:3], a[108:111], a[76:79], v[0:3]\n" \
"s_waitcnt lgkmcnt(3)\n" \
"v_mfma_f32_16x16x32_bf16 v[4:7], a[112:115], a[72:75], v[4:7]\n" \
"ds_read_b64_tr_b16 a[204:205], v210 offset:14336\n" \
"v_mfma_f32_16x16x32_bf16 v[4:7], a[116:119], a[76:79], v[4:7]\n" \
"ds_read_b64_tr_b16 a[206:207], v210 offset:15360\n" \
"global_load_lds_dwordx4 v253, s[50:51] offset:-3072\n" \
"v_mfma_f32_16x16x32_bf16 v[48:51], a[96:99], a[80:83], v[48:51]\n" \
"ds_read_b64_tr_b16 a[208:209], v209 offset:4096\n" \
"v_mfma_f32_16x16x32_bf16 v[48:51], a[100:103], a[84:87], v[48:51]\n" \
"v_mfma_f32_16x16x32_bf16 v[8:11], a[104:107], a[80:83], v[8:11]\n" \
"ds_read_b64_tr_b16 a[210:211], v209 offset:5120\n" \
"v_mfma_f32_16x16x32_bf16 v[8:11], a[108:111], a[84:87], v[8:11]\n" \
"ds_read_b64_tr_b16 a[212:213], v209 offset:16384\n" \
"global_load_lds_dwordx4 v253, s[50:51] offset:-1024\n" \
"v_mfma_f32_16x16x32_bf16 v[12:15], a[112:115], a[80:83], v[12:15]\n" \
"ds_read_b64_tr_b16 a[214:215], v209 offset:17408\n" \
"v_mfma_f32_16x16x32_bf16 v[12:15], a[116:119], a[84:87], v[12:15]\n" \
"v_mfma_f32_16x16x32_bf16 v[56:59], a[96:99], a[88:91], v[56:59]\n" \
"ds_read_b64_tr_b16 a[216:217], v210 offset:4096\n" \
"v_mfma_f32_16x16x32_bf16 v[56:59], a[100:103], a[92:95], v[56:59]\n" \
"ds_read_b64_tr_b16 a[218:219], v210 offset:5120\n" \
"global_load_lds_dwordx4 v253, s[50:51] offset:1024\n" \
"v_mfma_f32_16x16x32_bf16 v[16:19], a[104:107], a[88:91], v[16:19]\n" \
"s_add_u32 s50, s50, 0x60000\n" \
"s_addc_u32 s51, s51, 0\n" \
"ds_read_b64_tr_b16 a[220:221], v210 offset:16384\n" \
"v_mfma_f32_16x16x32_bf16 v[16:19], a[108:111], a[92:95], v[16:19]\n" \
"v_mfma_f32_16x16x32_bf16 v[20:23], a[112:115], a[88:91], v[20:23]\n" \
"ds_read_b64_tr_b16 a[222:223], v210 offset:17408\n" \
"v_mfma_f32_16x16x32_bf16 v[20:23], a[116:119], a[92:95], v[20:23]\n" \
"s_waitcnt lgkmcnt(3)\n" \
"v_add_u32 v207, 0x1d000, v207\n" \
"v_add_u32 v208, 0x1d000, v208\n" \
"v_mfma_f32_16x16x32_bf16 v[64:67], a[96:99], a[192:195], v[64:67]\n" \
"v_add_u32 v205, 0x1d000, v205\n" \
"v_add_u32 v206, 0x1d000, v206\n" \
"v_mfma_f32_16x16x32_bf16 v[64:67], a[100:103], a[196:199], v[64:67]\n" \
"v_add_u32 v209, 0x1d000, v209\n" \
"v_add_u32 v210, 0x1d000, v210\n" \
"v_mfma_f32_16x16x32_bf16 v[24:27], a[104:107], a[192:195], v[24:27]\n" \
"v_mfma_f32_16x16x32_bf16 v[24:27], a[108:111], a[196:199], v[24:27]\n" \
"v_mfma_f32_16x16x32_bf16 v[28:31], a[112:115], a[192:195], v[28:31]\n" \
"s_waitcnt vmcnt(11) lgkmcnt(0)\n" \
"v_mfma_f32_16x16x32_bf16 v[28:31], a[116:119], a[196:199], v[28:31]\n" \
"s_barrier\n" \
"v_mfma_f32_16x16x32_bf16 v[72:75], a[64:67], a[208:211], v[72:75]\n" \
"s_add_u32 m0, s95, 0\n" \
"ds_read_b64_tr_b16 a[96:97], v209 offset:2048\n" \
"v_mfma_f32_16x16x32_bf16 v[72:75], a[68:71], a[212:215], v[72:75]\n" \
"ds_read_b64_tr_b16 a[98:99], v209 offset:3072\n" \
"global_load_lds_dwordx4 v252, s[42:43] offset:0\n" \
"v_mfma_f32_16x16x32_bf16 v[44:47], a[200:203], a[72:75], v[44:47]\n" \
"ds_read_b128 a[64:67], v207 offset:0\n" \
"v_mfma_f32_16x16x32_bf16 v[44:47], a[204:207], a[76:79], v[44:47]\n" \
"v_mfma_f32_16x16x32_bf16 v[80:83], a[72:75], a[208:211], v[80:83]\n" \
"ds_read_b128 a[68:71], v208 offset:0\n" \
"v_mfma_f32_16x16x32_bf16 v[80:83], a[76:79], a[212:215], v[80:83]\n" \
"ds_read_b64_tr_b16 a[100:101], v209 offset:14336\n" \
"global_load_lds_dwordx4 v252, s[42:43] offset:2048\n" \
"v_mfma_f32_16x16x32_bf16 v[84:87], a[72:75], a[216:219], v[84:87]\n" \
"s_add_u32 s42, s42, 0x80\n" \
"s_addc_u32 s43, s43, 0\n" \
"ds_read_b64_tr_b16 a[102:103], v209 offset:15360\n" \
"v_mfma_f32_16x16x32_bf16 v[84:87], a[76:79], a[220:223], v[84:87]\n" \
"ds_read_b64_tr_b16 a[104:105], v209 offset:0\n" \
"v_mfma_f32_16x16x32_bf16 v[52:55], a[200:203], a[80:83], v[52:55]\n" \
"ds_read_b128 a[72:75], v205 offset:0\n" \
"v_mfma_f32_16x16x32_bf16 v[52:55], a[204:207], a[84:87], v[52:55]\n" \
"ds_read_b128 a[76:79], v206 offset:0\n" \
"global_load_lds_dwordx4 v252, s[44:45] offset:1024\n" \
"v_mfma_f32_16x16x32_bf16 v[88:91], a[80:83], a[208:211], v[88:91]\n" \
"ds_read_b64_tr_b16 a[106:107], v209 offset:1024\n" \
"v_mfma_f32_16x16x32_bf16 v[88:91], a[84:87], a[212:215], v[88:91]\n" \
"v_mfma_f32_16x16x32_bf16 v[92:95], a[80:83], a[216:219], v[92:95]\n" \
"ds_read_b64_tr_b16 a[108:109], v209 offset:12288\n" \
"v_mfma_f32_16x16x32_bf16 v[92:95], a[84:87], a[220:223], v[92:95]\n" \
"ds_read_b64_tr_b16 a[110:111], v209 offset:13312\n" \
"global_load_lds_dwordx4 v252, s[44:45] offset:3072\n" \
"v_mfma_f32_16x16x32_bf16 v[60:63], a[200:203], a[88:91], v[60:63]\n" \
"s_add_u32 s44, s44, 0x80\n" \
"s_addc_u32 s45, s45, 0\n" \
"ds_read_b128 a[80:83], v205 offset:4096\n" \
"v_mfma_f32_16x16x32_bf16 v[60:63], a[204:207], a[92:95], v[60:63]\n" \
"ds_read_b64_tr_b16 a[112:113], v210 offset:0\n" \
"v_mfma_f32_16x16x32_bf16 v[96:99], a[88:91], a[208:211], v[96:99]\n" \
"ds_read_b128 a[84:87], v206 offset:4096\n" \
"s_add_u32 m0, s85, 16384\n" \
"v_mfma_f32_16x16x32_bf16 v[96:99], a[92:95], a[212:215], v[96:99]\n" \
"ds_read_b64_tr_b16 a[114:115], v210 offset:1024\n" \
"global_load_lds_dwordx4 v246, s[46:47]\n" \
"v_mfma_f32_16x16x32_bf16 v[100:103], a[88:91], a[216:219], v[100:103]\n" \
"s_add_u32 s46, s46, 0x80\n" \
"s_addc_u32 s47, s47, 0\n" \
"ds_read_b64_tr_b16 a[116:117], v210 offset:12288\n" \
"v_mfma_f32_16x16x32_bf16 v[100:103], a[92:95], a[220:223], v[100:103]\n" \
"v_mfma_f32_16x16x32_bf16 v[68:71], a[200:203], a[192:195], v[68:71]\n" \
"ds_read_b128 a[88:91], v205 offset:8192\n" \
"s_add_u32 m0, s84, 24576\n" \
"v_mfma_f32_16x16x32_bf16 v[68:71], a[204:207], a[196:199], v[68:71]\n" \
"ds_read_b128 a[92:95], v206 offset:8192\n" \
"global_load_lds_dwordx4 v253, s[48:49] offset:-4096\n" \
"v_mfma_f32_16x16x32_bf16 v[104:107], a[192:195], a[208:211], v[104:107]\n" \
"ds_read_b64_tr_b16 a[118:119], v210 offset:13312\n" \
"v_mfma_f32_16x16x32_bf16 v[104:107], a[196:199], a[212:215], v[104:107]\n" \
"v_mfma_f32_16x16x32_bf16 v[108:111], a[192:195], a[216:219], v[108:111]\n" \
"v_mfma_f32_16x16x32_bf16 v[108:111], a[196:199], a[220:223], v[108:111]\n" \
"s_waitcnt lgkmcnt(2)\n" \
"global_load_lds_dwordx4 v253, s[48:49] offset:-2048\n" \
"v_mfma_f32_16x16x32_bf16 v[32:35], a[96:99], a[64:67], v[32:35]\n" \
"ds_read_b128 a[192:195], v205 offset:12288\n" \
"v_mfma_f32_16x16x32_bf16 v[32:35], a[100:103], a[68:71], v[32:35]\n" \
"v_mfma_f32_16x16x32_bf16 v[40:43], a[96:99], a[72:75], v[40:43]\n" \
"ds_read_b128 a[196:199], v206 offset:12288\n" \
"v_mfma_f32_16x16x32_bf16 v[40:43], a[100:103], a[76:79], v[40:43]\n" \
"ds_read_b64_tr_b16 a[200:201], v210 offset:2048\n" \
"global_load_lds_dwordx4 v253, s[48:49] offset:0\n" \
"v_mfma_f32_16x16x32_bf16 v[0:3], a[104:107], a[72:75], v[0:3]\n" \
"s_add_u32 s48, s48, 0x60000\n" \
"s_addc_u32 s49, s49, 0\n" \
"ds_read_b64_tr_b16 a[202:203], v210 offset:3072\n" \
"v_mfma_f32_16x16x32_bf16 v[0:3], a[108:111], a[76:79], v[0:3]\n" \
"s_waitcnt lgkmcnt(3)\n" \
"v_mfma_f32_16x16x32_bf16 v[4:7], a[112:115], a[72:75], v[4:7]\n" \
"ds_read_b64_tr_b16 a[204:205], v210 offset:14336\n" \
"v_mfma_f32_16x16x32_bf16 v[4:7], a[116:119], a[76:79], v[4:7]\n" \
"ds_read_b64_tr_b16 a[206:207], v210 offset:15360\n" \
"global_load_lds_dwordx4 v253, s[50:51] offset:-3072\n" \
"v_mfma_f32_16x16x32_bf16 v[48:51], a[96:99], a[80:83], v[48:51]\n" \
"ds_read_b64_tr_b16 a[208:209], v209 offset:4096\n" \
"v_mfma_f32_16x16x32_bf16 v[48:51], a[100:103], a[84:87], v[48:51]\n" \
"v_mfma_f32_16x16x32_bf16 v[8:11], a[104:107], a[80:83], v[8:11]\n" \
"ds_read_b64_tr_b16 a[210:211], v209 offset:5120\n" \
"v_mfma_f32_16x16x32_bf16 v[8:11], a[108:111], a[84:87], v[8:11]\n" \
"ds_read_b64_tr_b16 a[212:213], v209 offset:16384\n" \
"global_load_lds_dwordx4 v253, s[50:51] offset:-1024\n" \
"v_mfma_f32_16x16x32_bf16 v[12:15], a[112:115], a[80:83], v[12:15]\n" \
"ds_read_b64_tr_b16 a[214:215], v209 offset:17408\n" \
"v_mfma_f32_16x16x32_bf16 v[12:15], a[116:119], a[84:87], v[12:15]\n" \
"v_mfma_f32_16x16x32_bf16 v[56:59], a[96:99], a[88:91], v[56:59]\n" \
"ds_read_b64_tr_b16 a[216:217], v210 offset:4096\n" \
"v_mfma_f32_16x16x32_bf16 v[56:59], a[100:103], a[92:95], v[56:59]\n" \
"ds_read_b64_tr_b16 a[218:219], v210 offset:5120\n" \
"global_load_lds_dwordx4 v253, s[50:51] offset:1024\n" \
"v_mfma_f32_16x16x32_bf16 v[16:19], a[104:107], a[88:91], v[16:19]\n" \
"s_add_u32 s50, s50, 0x60000\n" \
"s_addc_u32 s51, s51, 0\n" \
"ds_read_b64_tr_b16 a[220:221], v210 offset:16384\n" \
"v_mfma_f32_16x16x32_bf16 v[16:19], a[108:111], a[92:95], v[16:19]\n" \
"v_mfma_f32_16x16x32_bf16 v[20:23], a[112:115], a[88:91], v[20:23]\n" \
"ds_read_b64_tr_b16 a[222:223], v210 offset:17408\n" \
"v_mfma_f32_16x16x32_bf16 v[20:23], a[116:119], a[92:95], v[20:23]\n" \
"s_waitcnt lgkmcnt(3)\n" \
"buffer_load_dword v220, v221, s[56:59], s81 offen sc1\n" \
"v_mfma_f32_16x16x32_bf16 v[64:67], a[96:99], a[192:195], v[64:67]\n" \
"v_add_u32 v207, 0xffff3000, v207\n" \
"v_add_u32 v208, 0xffff3000, v208\n" \
"v_mfma_f32_16x16x32_bf16 v[64:67], a[100:103], a[196:199], v[64:67]\n" \
"v_add_u32 v205, 0xffff3000, v205\n" \
"v_add_u32 v206, 0xffff3000, v206\n" \
"v_mfma_f32_16x16x32_bf16 v[24:27], a[104:107], a[192:195], v[24:27]\n" \
"v_add_u32 v209, 0xffff3000, v209\n" \
"v_add_u32 v210, 0xffff3000, v210\n" \
"v_mfma_f32_16x16x32_bf16 v[24:27], a[108:111], a[196:199], v[24:27]\n" \
"v_mfma_f32_16x16x32_bf16 v[28:31], a[112:115], a[192:195], v[28:31]\n" \
"s_waitcnt vmcnt(12) lgkmcnt(0)\n" \
"v_mfma_f32_16x16x32_bf16 v[28:31], a[116:119], a[196:199], v[28:31]\n" \
"s_barrier\n" \
"v_mfma_f32_16x16x32_bf16 v[72:75], a[64:67], a[208:211], v[72:75]\n" \
"s_add_u32 m0, s95, 118784\n" \
"ds_read_b64_tr_b16 a[96:97], v209 offset:2048\n" \
"v_mfma_f32_16x16x32_bf16 v[72:75], a[68:71], a[212:215], v[72:75]\n" \
"ds_read_b64_tr_b16 a[98:99], v209 offset:3072\n" \
"global_load_lds_dwordx4 v252, s[42:43] offset:0\n" \
"v_mfma_f32_16x16x32_bf16 v[44:47], a[200:203], a[72:75], v[44:47]\n" \
"ds_read_b128 a[64:67], v207 offset:0\n" \
"v_mfma_f32_16x16x32_bf16 v[44:47], a[204:207], a[76:79], v[44:47]\n" \
"v_mfma_f32_16x16x32_bf16 v[80:83], a[72:75], a[208:211], v[80:83]\n" \
"ds_read_b128 a[68:71], v208 offset:0\n" \
"v_mfma_f32_16x16x32_bf16 v[80:83], a[76:79], a[212:215], v[80:83]\n" \
"ds_read_b64_tr_b16 a[100:101], v209 offset:14336\n" \
"global_load_lds_dwordx4 v252, s[42:43] offset:2048\n" \
"v_mfma_f32_16x16x32_bf16 v[84:87], a[72:75], a[216:219], v[84:87]\n" \
"ds_read_b64_tr_b16 a[102:103], v209 offset:15360\n" \
"v_mfma_f32_16x16x32_bf16 v[84:87], a[76:79], a[220:223], v[84:87]\n" \
"ds_read_b64_tr_b16 a[104:105], v209 offset:0\n" \
"v_mfma_f32_16x16x32_bf16 v[52:55], a[200:203], a[80:83], v[52:55]\n" \
"ds_read_b128 a[72:75], v205 offset:0\n" \
"v_mfma_f32_16x16x32_bf16 v[52:55], a[204:207], a[84:87], v[52:55]\n" \
"ds_read_b128 a[76:79], v206 offset:0\n" \
"global_load_lds_dwordx4 v252, s[44:45] offset:1024\n" \
"v_mfma_f32_16x16x32_bf16 v[88:91], a[80:83], a[208:211], v[88:91]\n" \
"ds_read_b64_tr_b16 a[106:107], v209 offset:1024\n" \
"v_mfma_f32_16x16x32_bf16 v[88:91], a[84:87], a[212:215], v[88:91]\n" \
"v_mfma_f32_16x16x32_bf16 v[92:95], a[80:83], a[216:219], v[92:95]\n" \
"ds_read_b64_tr_b16 a[108:109], v209 offset:12288\n" \
"v_mfma_f32_16x16x32_bf16 v[92:95], a[84:87], a[220:223], v[92:95]\n" \
"ds_read_b64_tr_b16 a[110:111], v209 offset:13312\n" \
"global_load_lds_dwordx4 v252, s[44:45] offset:3072\n" \
"v_mfma_f32_16x16x32_bf16 v[60:63], a[200:203], a[88:91], v[60:63]\n" \
"ds_read_b128 a[80:83], v205 offset:4096\n" \
"v_mfma_f32_16x16x32_bf16 v[60:63], a[204:207], a[92:95], v[60:63]\n" \
"ds_read_b64_tr_b16 a[112:113], v210 offset:0\n" \
"v_mfma_f32_16x16x32_bf16 v[96:99], a[88:91], a[208:211], v[96:99]\n" \
"ds_read_b128 a[84:87], v206 offset:4096\n" \
"s_add_u32 m0, s85, 135168\n" \
"v_mfma_f32_16x16x32_bf16 v[96:99], a[92:95], a[212:215], v[96:99]\n" \
"ds_read_b64_tr_b16 a[114:115], v210 offset:1024\n" \
"global_load_lds_dwordx4 v246, s[46:47]\n" \
"v_mfma_f32_16x16x32_bf16 v[100:103], a[88:91], a[216:219], v[100:103]\n" \
"ds_read_b64_tr_b16 a[116:117], v210 offset:12288\n" \
"v_mfma_f32_16x16x32_bf16 v[100:103], a[92:95], a[220:223], v[100:103]\n" \
"v_mfma_f32_16x16x32_bf16 v[68:71], a[200:203], a[192:195], v[68:71]\n" \
"ds_read_b128 a[88:91], v205 offset:8192\n" \
"s_add_u32 m0, s84, 143360\n" \
"v_mfma_f32_16x16x32_bf16 v[68:71], a[204:207], a[196:199], v[68:71]\n" \
"ds_read_b128 a[92:95], v206 offset:8192\n" \
"global_load_lds_dwordx4 v253, s[48:49] offset:-4096\n" \
"v_mfma_f32_16x16x32_bf16 v[104:107], a[192:195], a[208:211], v[104:107]\n" \
"ds_read_b64_tr_b16 a[118:119], v210 offset:13312\n" \
"v_mfma_f32_16x16x32_bf16 v[104:107], a[196:199], a[212:215], v[104:107]\n" \
"v_mfma_f32_16x16x32_bf16 v[108:111], a[192:195], a[216:219], v[108:111]\n" \
"v_mfma_f32_16x16x32_bf16 v[108:111], a[196:199], a[220:223], v[108:111]\n" \
"s_waitcnt lgkmcnt(2)\n" \
"global_load_lds_dwordx4 v253, s[48:49] offset:-2048\n" \
"v_mfma_f32_16x16x32_bf16 v[32:35], a[96:99], a[64:67], v[32:35]\n" \
"ds_read_b128 a[192:195], v205 offset:12288\n" \
"v_mfma_f32_16x16x32_bf16 v[32:35], a[100:103], a[68:71], v[32:35]\n" \
"v_mfma_f32_16x16x32_bf16 v[40:43], a[96:99], a[72:75], v[40:43]\n" \
"ds_read_b128 a[196:199], v206 offset:12288\n" \
"v_mfma_f32_16x16x32_bf16 v[40:43], a[100:103], a[76:79], v[40:43]\n" \
"ds_read_b64_tr_b16 a[200:201], v210 offset:2048\n" \
"global_load_lds_dwordx4 v253, s[48:49] offset:0\n" \
"v_mfma_f32_16x16x32_bf16 v[0:3], a[104:107], a[72:75], v[0:3]\n" \
"ds_read_b64_tr_b16 a[202:203], v210 offset:3072\n" \
"v_mfma_f32_16x16x32_bf16 v[0:3], a[108:111], a[76:79], v[0:3]\n" \
"s_waitcnt lgkmcnt(3)\n" \
"v_mfma_f32_16x16x32_bf16 v[4:7], a[112:115], a[72:75], v[4:7]\n" \
"ds_read_b64_tr_b16 a[204:205], v210 offset:14336\n" \
"v_mfma_f32_16x16x32_bf16 v[4:7], a[116:119], a[76:79], v[4:7]\n" \
"ds_read_b64_tr_b16 a[206:207], v210 offset:15360\n" \
"global_load_lds_dwordx4 v253, s[50:51] offset:-3072\n" \
"v_mfma_f32_16x16x32_bf16 v[48:51], a[96:99], a[80:83], v[48:51]\n" \
"ds_read_b64_tr_b16 a[208:209], v209 offset:4096\n" \
"v_mfma_f32_16x16x32_bf16 v[48:51], a[100:103], a[84:87], v[48:51]\n" \
"v_mfma_f32_16x16x32_bf16 v[8:11], a[104:107], a[80:83], v[8:11]\n" \
"ds_read_b64_tr_b16 a[210:211], v209 offset:5120\n" \
"v_mfma_f32_16x16x32_bf16 v[8:11], a[108:111], a[84:87], v[8:11]\n" \
"ds_read_b64_tr_b16 a[212:213], v209 offset:16384\n" \
"global_load_lds_dwordx4 v253, s[50:51] offset:-1024\n" \
"v_mfma_f32_16x16x32_bf16 v[12:15], a[112:115], a[80:83], v[12:15]\n" \
"ds_read_b64_tr_b16 a[214:215], v209 offset:17408\n" \
"v_mfma_f32_16x16x32_bf16 v[12:15], a[116:119], a[84:87], v[12:15]\n" \
"v_mfma_f32_16x16x32_bf16 v[56:59], a[96:99], a[88:91], v[56:59]\n" \
"ds_read_b64_tr_b16 a[216:217], v210 offset:4096\n" \
"v_mfma_f32_16x16x32_bf16 v[56:59], a[100:103], a[92:95], v[56:59]\n" \
"ds_read_b64_tr_b16 a[218:219], v210 offset:5120\n" \
"global_load_lds_dwordx4 v253, s[50:51] offset:1024\n" \
"v_mfma_f32_16x16x32_bf16 v[16:19], a[104:107], a[88:91], v[16:19]\n" \
"ds_read_b64_tr_b16 a[220:221], v210 offset:16384\n" \
"v_mfma_f32_16x16x32_bf16 v[16:19], a[108:111], a[92:95], v[16:19]\n" \
"v_mfma_f32_16x16x32_bf16 v[20:23], a[112:115], a[88:91], v[20:23]\n" \
"ds_read_b64_tr_b16 a[222:223], v210 offset:17408\n" \
"v_mfma_f32_16x16x32_bf16 v[20:23], a[116:119], a[92:95], v[20:23]\n" \
"s_waitcnt lgkmcnt(3)\n" \
"v_add_u32 v207, 0xffff0000, v207\n" \
"v_add_u32 v208, 0xffff0000, v208\n" \
"v_mfma_f32_16x16x32_bf16 v[64:67], a[96:99], a[192:195], v[64:67]\n" \
"v_add_u32 v205, 0xffff0000, v205\n" \
"v_add_u32 v206, 0xffff0000, v206\n" \
"v_mfma_f32_16x16x32_bf16 v[64:67], a[100:103], a[196:199], v[64:67]\n" \
"v_add_u32 v209, 0xffff0000, v209\n" \
"v_add_u32 v210, 0xffff0000, v210\n" \
"v_mfma_f32_16x16x32_bf16 v[24:27], a[104:107], a[192:195], v[24:27]\n" \
"v_mfma_f32_16x16x32_bf16 v[24:27], a[108:111], a[196:199], v[24:27]\n" \
"v_mfma_f32_16x16x32_bf16 v[28:31], a[112:115], a[192:195], v[28:31]\n" \
"s_waitcnt vmcnt(12) lgkmcnt(0)\n" \
"v_mfma_f32_16x16x32_bf16 v[28:31], a[116:119], a[196:199], v[28:31]\n" \
"s_barrier\n" \
"v_mfma_f32_16x16x32_bf16 v[72:75], a[64:67], a[208:211], v[72:75]\n" \
"s_waitcnt vmcnt(11)\n" \
"ds_read_b64_tr_b16 a[96:97], v209 offset:2048\n" \
"v_cmp_ne_u32 vcc, s60, v220\n" \
"v_mfma_f32_16x16x32_bf16 v[72:75], a[68:71], a[212:215], v[72:75]\n" \
"ds_read_b64_tr_b16 a[98:99], v209 offset:3072\n" \
"v_mfma_f32_16x16x32_bf16 v[44:47], a[200:203], a[72:75], v[44:47]\n" \
"ds_read_b128 a[64:67], v207 offset:0\n" \
"v_mfma_f32_16x16x32_bf16 v[44:47], a[204:207], a[76:79], v[44:47]\n" \
"s_and_b32 vcc_hi, vcc_lo, s80\n" \
"s_cbranch_scc1 Lpl3_%=\n" \
"Lpr3_%=:\n" \
"v_mfma_f32_16x16x32_bf16 v[80:83], a[72:75], a[208:211], v[80:83]\n" \
"ds_read_b128 a[68:71], v208 offset:0\n" \
"s_add_u32 m0, s75, s95\n" \
"v_mfma_f32_16x16x32_bf16 v[80:83], a[76:79], a[212:215], v[80:83]\n" \
"ds_read_b64_tr_b16 a[100:101], v209 offset:14336\n" \
"global_load_lds_dwordx4 v250, s[36:37] offset:0 sc1\n" \
"v_mfma_f32_16x16x32_bf16 v[84:87], a[72:75], a[216:219], v[84:87]\n" \
"ds_read_b64_tr_b16 a[102:103], v209 offset:15360\n" \
"v_mfma_f32_16x16x32_bf16 v[84:87], a[76:79], a[220:223], v[84:87]\n" \
"ds_read_b64_tr_b16 a[104:105], v209 offset:0\n" \
"v_mfma_f32_16x16x32_bf16 v[52:55], a[200:203], a[80:83], v[52:55]\n" \
"ds_read_b128 a[72:75], v205 offset:0\n" \
"v_mfma_f32_16x16x32_bf16 v[52:55], a[204:207], a[84:87], v[52:55]\n" \
"ds_read_b128 a[76:79], v206 offset:0\n" \
"global_load_lds_dwordx4 v250, s[36:37] offset:1024 sc1\n" \
"v_mfma_f32_16x16x32_bf16 v[88:91], a[80:83], a[208:211], v[88:91]\n" \
"ds_read_b64_tr_b16 a[106:107], v209 offset:1024\n" \
"v_mfma_f32_16x16x32_bf16 v[88:91], a[84:87], a[212:215], v[88:91]\n" \
"v_mfma_f32_16x16x32_bf16 v[92:95], a[80:83], a[216:219], v[92:95]\n" \
"ds_read_b64_tr_b16 a[108:109], v209 offset:12288\n" \
"v_mfma_f32_16x16x32_bf16 v[92:95], a[84:87], a[220:223], v[92:95]\n" \
"ds_read_b64_tr_b16 a[110:111], v209 offset:13312\n" \
"global_load_lds_dwordx4 v250, s[36:37] offset:2048 sc1\n" \
"v_mfma_f32_16x16x32_bf16 v[60:63], a[200:203], a[88:91], v[60:63]\n" \
"ds_read_b128 a[80:83], v205 offset:4096\n" \
"v_mfma_f32_16x16x32_bf16 v[60:63], a[204:207], a[92:95], v[60:63]\n" \
"ds_read_b64_tr_b16 a[112:113], v210 offset:0\n" \
"v_mfma_f32_16x16x32_bf16 v[96:99], a[88:91], a[208:211], v[96:99]\n" \
"ds_read_b128 a[84:87], v206 offset:4096\n" \
"v_mfma_f32_16x16x32_bf16 v[96:99], a[92:95], a[212:215], v[96:99]\n" \
"ds_read_b64_tr_b16 a[114:115], v210 offset:1024\n" \
"global_load_lds_dwordx4 v250, s[36:37] offset:3072 sc1\n" \
"v_mfma_f32_16x16x32_bf16 v[100:103], a[88:91], a[216:219], v[100:103]\n" \
"s_add_u32 s36, s36, 0x1000\n" \
"ds_read_b64_tr_b16 a[116:117], v210 offset:12288\n" \
"v_mfma_f32_16x16x32_bf16 v[100:103], a[92:95], a[220:223], v[100:103]\n" \
"v_mfma_f32_16x16x32_bf16 v[68:71], a[200:203], a[192:195], v[68:71]\n" \
"ds_read_b128 a[88:91], v205 offset:8192\n" \
"s_add_u32 m0, m0, 0x5000\n" \
"v_mfma_f32_16x16x32_bf16 v[68:71], a[204:207], a[196:199], v[68:71]\n" \
"ds_read_b128 a[92:95], v206 offset:8192\n" \
"global_load_lds_dwordx4 v216, s[38:39] offset:-4096\n" \
"v_mfma_f32_16x16x32_bf16 v[104:107], a[192:195], a[208:211], v[104:107]\n" \
"ds_read_b64_tr_b16 a[118:119], v210 offset:13312\n" \
"v_mfma_f32_16x16x32_bf16 v[104:107], a[196:199], a[212:215], v[104:107]\n" \
"v_mfma_f32_16x16x32_bf16 v[108:111], a[192:195], a[216:219], v[108:111]\n" \
"v_mfma_f32_16x16x32_bf16 v[108:111], a[196:199], a[220:223], v[108:111]\n" \
"s_waitcnt lgkmcnt(2)\n" \
"global_load_lds_dwordx4 v217, s[38:39] offset:-3072\n" \
"v_mfma_f32_16x16x32_bf16 v[32:35], a[96:99], a[64:67], v[32:35]\n" \
"ds_read_b128 a[192:195], v205 offset:12288\n" \
"v_mfma_f32_16x16x32_bf16 v[32:35], a[100:103], a[68:71], v[32:35]\n" \
"v_mfma_f32_16x16x32_bf16 v[40:43], a[96:99], a[72:75], v[40:43]\n" \
"ds_read_b128 a[196:199], v206 offset:12288\n" \
"v_mfma_f32_16x16x32_bf16 v[40:43], a[100:103], a[76:79], v[40:43]\n" \
"ds_read_b64_tr_b16 a[200:201], v210 offset:2048\n" \
"global_load_lds_dwordx4 v218, s[38:39] offset:-2048\n" \
"v_mfma_f32_16x16x32_bf16 v[0:3], a[104:107], a[72:75], v[0:3]\n" \
"ds_read_b64_tr_b16 a[202:203], v210 offset:3072\n" \
"v_mfma_f32_16x16x32_bf16 v[0:3], a[108:111], a[76:79], v[0:3]\n" \
"s_waitcnt lgkmcnt(3)\n" \
"v_mfma_f32_16x16x32_bf16 v[4:7], a[112:115], a[72:75], v[4:7]\n" \
"ds_read_b64_tr_b16 a[204:205], v210 offset:14336\n" \
"v_mfma_f32_16x16x32_bf16 v[4:7], a[116:119], a[76:79], v[4:7]\n" \
"ds_read_b64_tr_b16 a[206:207], v210 offset:15360\n" \
"global_load_lds_dwordx4 v219, s[38:39] offset:-1024\n" \
"v_mfma_f32_16x16x32_bf16 v[48:51], a[96:99], a[80:83], v[48:51]\n" \
"s_add_u32 s38, s38, 0x20000\n" \
"s_addc_u32 s39, s39, 0\n" \
"ds_read_b64_tr_b16 a[208:209], v209 offset:4096\n" \
"v_mfma_f32_16x16x32_bf16 v[48:51], a[100:103], a[84:87], v[48:51]\n" \
"s_sub_u32 s75, s75, 32768\n" \
"s_cselect_b32 s75, 131072, s75\n" \
"v_mfma_f32_16x16x32_bf16 v[8:11], a[104:107], a[80:83], v[8:11]\n" \
"ds_read_b64_tr_b16 a[210:211], v209 offset:5120\n" \
"v_mfma_f32_16x16x32_bf16 v[8:11], a[108:111], a[84:87], v[8:11]\n" \
"ds_read_b64_tr_b16 a[212:213], v209 offset:16384\n" \
"v_mfma_f32_16x16x32_bf16 v[12:15], a[112:115], a[80:83], v[12:15]\n" \
"ds_read_b64_tr_b16 a[214:215], v209 offset:17408\n" \
"v_mfma_f32_16x16x32_bf16 v[12:15], a[116:119], a[84:87], v[12:15]\n" \
"v_mfma_f32_16x16x32_bf16 v[56:59], a[96:99], a[88:91], v[56:59]\n" \
"ds_read_b64_tr_b16 a[216:217], v210 offset:4096\n" \
"v_mfma_f32_16x16x32_bf16 v[56:59], a[100:103], a[92:95], v[56:59]\n" \
"ds_read_b64_tr_b16 a[218:219], v210 offset:5120\n" \
"v_mfma_f32_16x16x32_bf16 v[16:19], a[104:107], a[88:91], v[16:19]\n" \
"ds_read_b64_tr_b16 a[220:221], v210 offset:16384\n" \
"v_mfma_f32_16x16x32_bf16 v[16:19], a[108:111], a[92:95], v[16:19]\n" \
"v_mfma_f32_16x16x32_bf16 v[20:23], a[112:115], a[88:91], v[20:23]\n" \
"ds_read_b64_tr_b16 a[222:223], v210 offset:17408\n" \
"v_mfma_f32_16x16x32_bf16 v[20:23], a[116:119], a[92:95], v[20:23]\n" \
"s_waitcnt lgkmcnt(3)\n" \
"v_add_u32 v207, 0x1d000, v207\n" \
"v_add_u32 v208, 0x1d000, v208\n" \
"v_mfma_f32_16x16x32_bf16 v[64:67], a[96:99], a[192:195], v[64:67]\n" \
"v_add_u32 v205, 0x1d000, v205\n" \
"v_add_u32 v206, 0x1d000, v206\n" \
"v_mfma_f32_16x16x32_bf16 v[64:67], a[100:103], a[196:199], v[64:67]\n" \
"v_add_u32 v209, 0x1d000, v209\n" \
"v_add_u32 v210, 0x1d000, v210\n" \
"v_mfma_f32_16x16x32_bf16 v[24:27], a[104:107], a[192:195], v[24:27]\n" \
"v_mfma_f32_16x16x32_bf16 v[24:27], a[108:111], a[196:199], v[24:27]\n" \
"v_mfma_f32_16x16x32_bf16 v[28:31], a[112:115], a[192:195], v[28:31]\n" \
"s_waitcnt vmcnt(8) lgkmcnt(0)\n" \
"v_mfma_f32_16x16x32_bf16 v[28:31], a[116:119], a[196:199], v[28:31]\n" \
"s_barrier\n" \
"v_mfma_f32_16x16x32_bf16 v[72:75], a[64:67], a[208:211], v[72:75]\n" \
"s_add_u32 m0, s75, s95\n" \
"ds_read_b64_tr_b16 a[96:97], v209 offset:2048\n" \
"v_mfma_f32_16x16x32_bf16 v[72:75], a[68:71], a[212:215], v[72:75]\n" \
"ds_read_b64_tr_b16 a[98:99], v209 offset:3072\n" \
"global_load_lds_dwordx4 v250, s[36:37] offset:0 sc1\n" \
"v_mfma_f32_16x16x32_bf16 v[44:47], a[200:203], a[72:75], v[44:47]\n" \
"ds_read_b128 a[64:67], v207 offset:0\n" \
"v_mfma_f32_16x16x32_bf16 v[44:47], a[204:207], a[76:79], v[44:47]\n" \
"v_mfma_f32_16x16x32_bf16 v[80:83], a[72:75], a[208:211], v[80:83]\n" \
"Lpg11036_%=:\n" \
"s_cbranch_execz Lpg12767_%=\n" \
"ds_read_b128 a[68:71], v208 offset:0\n" \
"v_mfma_f32_16x16x32_bf16 v[80:83], a[76:79], a[212:215], v[80:83]\n" \
"ds_read_b64_tr_b16 a[100:101], v209 offset:14336\n" \
"global_load_lds_dwordx4 v250, s[36:37] offset:1024 sc1\n" \
"v_mfma_f32_16x16x32_bf16 v[84:87], a[72:75], a[216:219], v[84:87]\n" \
"ds_read_b64_tr_b16 a[102:103], v209 offset:15360\n" \
"v_mfma_f32_16x16x32_bf16 v[84:87], a[76:79], a[220:223], v[84:87]\n" \
"ds_read_b64_tr_b16 a[104:105], v209 offset:0\n" \
"v_mfma_f32_16x16x32_bf16 v[52:55], a[200:203], a[80:83], v[52:55]\n" \
"ds_read_b128 a[72:75], v205 offset:0\n" \
"v_mfma_f32_16x16x32_bf16 v[52:55], a[204:207], a[84:87], v[52:55]\n" \
"ds_read_b128 a[76:79], v206 offset:0\n" \
"global_load_lds_dwordx4 v250, s[36:37] offset:2048 sc1\n" \
"v_mfma_f32_16x16x32_bf16 v[88:91], a[80:83], a[208:211], v[88:91]\n" \
"ds_read_b64_tr_b16 a[106:107], v209 offset:1024\n" \
"v_mfma_f32_16x16x32_bf16 v[88:91], a[84:87], a[212:215], v[88:91]\n" \
"v_mfma_f32_16x16x32_bf16 v[92:95], a[80:83], a[216:219], v[92:95]\n" \
"ds_read_b64_tr_b16 a[108:109], v209 offset:12288\n" \
"v_mfma_f32_16x16x32_bf16 v[92:95], a[84:87], a[220:223], v[92:95]\n" \
"ds_read_b64_tr_b16 a[110:111], v209 offset:13312\n" \
"global_load_lds_dwordx4 v250, s[36:37] offset:3072 sc1\n" \
"v_mfma_f32_16x16x32_bf16 v[60:63], a[200:203], a[88:91], v[60:63]\n" \
"ds_read_b128 a[80:83], v205 offset:4096\n" \
"s_add_u32 s36, s36, 0x1000\n" \
"v_mfma_f32_16x16x32_bf16 v[60:63], a[204:207], a[92:95], v[60:63]\n" \
"ds_read_b64_tr_b16 a[112:113], v210 offset:0\n" \
"v_mfma_f32_16x16x32_bf16 v[96:99], a[88:91], a[208:211], v[96:99]\n" \
"ds_read_b128 a[84:87], v206 offset:4096\n" \
"s_add_u32 m0, m0, 0x5000\n" \
"v_mfma_f32_16x16x32_bf16 v[96:99], a[92:95], a[212:215], v[96:99]\n" \
"ds_read_b64_tr_b16 a[114:115], v210 offset:1024\n" \
"global_load_lds_dwordx4 v216, s[38:39] offset:-4096\n" \
"v_mfma_f32_16x16x32_bf16 v[100:103], a[88:91], a[216:219], v[100:103]\n" \
"ds_read_b64_tr_b16 a[116:117], v210 offset:12288\n" \
"v_mfma_f32_16x16x32_bf16 v[100:103], a[92:95], a[220:223], v[100:103]\n" \
"v_mfma_f32_16x16x32_bf16 v[68:71], a[200:203], a[192:195], v[68:71]\n" \
"ds_read_b128 a[88:91], v205 offset:8192\n" \
"v_mfma_f32_16x16x32_bf16 v[68:71], a[204:207], a[196:199], v[68:71]\n" \
"ds_read_b128 a[92:95], v206 offset:8192\n" \
"global_load_lds_dwordx4 v217, s[38:39] offset:-3072\n" \
"v_mfma_f32_16x16x32_bf16 v[104:107], a[192:195], a[208:211], v[104:107]\n" \
"ds_read_b64_tr_b16 a[118:119], v210 offset:13312\n" \
"v_mfma_f32_16x16x32_bf16 v[104:107], a[196:199], a[212:215], v[104:107]\n" \
"v_mfma_f32_16x16x32_bf16 v[108:111], a[192:195], a[216:219], v[108:111]\n" \
"v_mfma_f32_16x16x32_bf16 v[108:111], a[196:199], a[220:223], v[108:111]\n" \
"s_waitcnt lgkmcnt(2)\n" \
"global_load_lds_dwordx4 v218, s[38:39] offset:-2048\n" \
"v_mfma_f32_16x16x32_bf16 v[32:35], a[96:99], a[64:67], v[32:35]\n" \
"ds_read_b128 a[192:195], v205 offset:12288\n" \
"v_mfma_f32_16x16x32_bf16 v[32:35], a[100:103], a[68:71], v[32:35]\n" \
"v_mfma_f32_16x16x32_bf16 v[40:43], a[96:99], a[72:75], v[40:43]\n" \
"ds_read_b128 a[196:199], v206 offset:12288\n" \
"v_mfma_f32_16x16x32_bf16 v[40:43], a[100:103], a[76:79], v[40:43]\n" \
"ds_read_b64_tr_b16 a[200:201], v210 offset:2048\n" \
"global_load_lds_dwordx4 v219, s[38:39] offset:-1024\n" \
"v_mfma_f32_16x16x32_bf16 v[0:3], a[104:107], a[72:75], v[0:3]\n" \
"s_add_u32 s38, s38, 0x20000\n" \
"s_addc_u32 s39, s39, 0\n" \
"ds_read_b64_tr_b16 a[202:203], v210 offset:3072\n" \
"v_mfma_f32_16x16x32_bf16 v[0:3], a[108:111], a[76:79], v[0:3]\n" \
"s_waitcnt lgkmcnt(3)\n" \
"s_sub_u32 s75, s75, 32768\n" \
"s_cselect_b32 s75, 131072, s75\n" \
"v_mfma_f32_16x16x32_bf16 v[4:7], a[112:115], a[72:75], v[4:7]\n" \
"s_add_u32 m0, s75, s95\n" \
"ds_read_b64_tr_b16 a[204:205], v210 offset:14336\n" \
"v_mfma_f32_16x16x32_bf16 v[4:7], a[116:119], a[76:79], v[4:7]\n" \
"ds_read_b64_tr_b16 a[206:207], v210 offset:15360\n" \
"global_load_lds_dwordx4 v250, s[36:37] offset:0 sc1\n" \
"v_mfma_f32_16x16x32_bf16 v[48:51], a[96:99], a[80:83], v[48:51]\n" \
"ds_read_b64_tr_b16 a[208:209], v209 offset:4096\n" \
"v_mfma_f32_16x16x32_bf16 v[48:51], a[100:103], a[84:87], v[48:51]\n" \
"v_mfma_f32_16x16x32_bf16 v[8:11], a[104:107], a[80:83], v[8:11]\n" \
"ds_read_b64_tr_b16 a[210:211], v209 offset:5120\n" \
"v_cvt_pk_bf16_f32 v112, v0, v1\n" \
"v_mfma_f32_16x16x32_bf16 v[8:11], a[108:111], a[84:87], v[8:11]\n" \
"ds_read_b64_tr_b16 a[212:213], v209 offset:16384\n" \
"global_load_lds_dwordx4 v250, s[36:37] offset:1024 sc1\n" \
"v_mfma_f32_16x16x32_bf16 v[12:15], a[112:115], a[80:83], v[12:15]\n" \
"ds_read_b64_tr_b16 a[214:215], v209 offset:17408\n" \
"v_cvt_pk_bf16_f32 v113, v2, v3\n" \
"v_mfma_f32_16x16x32_bf16 v[12:15], a[116:119], a[84:87], v[12:15]\n" \
"v_cvt_pk_bf16_f32 v114, v4, v5\n" \
"v_cvt_pk_bf16_f32 v115, v6, v7\n" \
"v_mfma_f32_16x16x32_bf16 v[56:59], a[96:99], a[88:91], v[56:59]\n" \
"ds_read_b64_tr_b16 a[216:217], v210 offset:4096\n" \
"v_mfma_f32_16x16x32_bf16 v[56:59], a[100:103], a[92:95], v[56:59]\n" \
"ds_read_b64_tr_b16 a[218:219], v210 offset:5120\n" \
"global_load_lds_dwordx4 v250, s[36:37] offset:2048 sc1\n" \
"v_mfma_f32_16x16x32_bf16 v[16:19], a[104:107], a[88:91], v[16:19]\n" \
"ds_read_b64_tr_b16 a[220:221], v210 offset:16384\n" \
"v_cvt_pk_bf16_f32 v116, v8, v9\n" \
"v_mfma_f32_16x16x32_bf16 v[16:19], a[108:111], a[92:95], v[16:19]\n" \
"v_cvt_pk_bf16_f32 v117, v10, v11\n" \
"v_cvt_pk_bf16_f32 v118, v12, v13\n" \
"v_mfma_f32_16x16x32_bf16 v[20:23], a[112:115], a[88:91], v[20:23]\n" \
"ds_read_b64_tr_b16 a[222:223], v210 offset:17408\n" \
"v_cvt_pk_bf16_f32 v119, v14, v15\n" \
"v_mfma_f32_16x16x32_bf16 v[20:23], a[116:119], a[92:95], v[20:23]\n" \
"s_waitcnt lgkmcnt(3)\n" \
"global_load_lds_dwordx4 v250, s[36:37] offset:3072 sc1\n" \
"v_mfma_f32_16x16x32_bf16 v[64:67], a[96:99], a[192:195], v[64:67]\n" \
"v_mfma_f32_16x16x32_bf16 v[64:67], a[100:103], a[196:199], v[64:67]\n" \
"v_mfma_f32_16x16x32_bf16 v[24:27], a[104:107], a[192:195], v[24:27]\n" \
"v_cvt_pk_bf16_f32 v120, v16, v17\n" \
"v_cvt_pk_bf16_f32 v121, v18, v19\n" \
"v_mfma_f32_16x16x32_bf16 v[24:27], a[108:111], a[196:199], v[24:27]\n" \
"v_cvt_pk_bf16_f32 v122, v20, v21\n" \
"v_cvt_pk_bf16_f32 v123, v22, v23\n" \
"v_mfma_f32_16x16x32_bf16 v[28:31], a[112:115], a[192:195], v[28:31]\n" \
"s_waitcnt vmcnt(12) lgkmcnt(0)\n" \
"v_mfma_f32_16x16x32_bf16 v[28:31], a[116:119], a[196:199], v[28:31]\n" \
"s_barrier\n" \
"v_mfma_f32_16x16x32_bf16 v[72:75], a[64:67], a[208:211], v[72:75]\n" \
"s_add_u32 s36, s36, 0x1000\n" \
"s_add_u32 m0, m0, 0x5000\n" \
"ds_read_b64_tr_b16 a[32:33], v214 offset:16384\n" \
"v_mfma_f32_16x16x32_bf16 v[72:75], a[68:71], a[212:215], v[72:75]\n" \
"global_load_lds_dwordx4 v216, s[38:39] offset:-4096\n" \
"v_mfma_f32_16x16x32_bf16 v[44:47], a[200:203], a[72:75], v[44:47]\n" \
"ds_read_b64_tr_b16 a[34:35], v214 offset:18432\n" \
"v_cvt_pk_bf16_f32 v124, v24, v25\n" \
"v_mfma_f32_16x16x32_bf16 v[44:47], a[204:207], a[76:79], v[44:47]\n" \
"ds_read_b64_tr_b16 a[36:37], v214 offset:24576\n" \
"v_cvt_pk_bf16_f32 v125, v26, v27\n" \
"v_mfma_f32_16x16x32_bf16 v[80:83], a[72:75], a[208:211], v[80:83]\n" \
"v_cvt_pk_bf16_f32 v126, v28, v29\n" \
"v_cvt_pk_bf16_f32 v127, v30, v31\n" \
"v_mfma_f32_16x16x32_bf16 v[80:83], a[76:79], a[212:215], v[80:83]\n" \
"ds_read_b64_tr_b16 a[38:39], v214 offset:26624\n" \
"global_load_lds_dwordx4 v217, s[38:39] offset:-3072\n" \
"v_mfma_f32_16x16x32_bf16 v[84:87], a[72:75], a[216:219], v[84:87]\n" \
"ds_read_b128 a[0:3], v213 offset:0\n" \
"v_cvt_pk_bf16_f32 v128, v32, v33\n" \
"v_mfma_f32_16x16x32_bf16 v[84:87], a[76:79], a[220:223], v[84:87]\n" \
"v_cvt_pk_bf16_f32 v129, v34, v35\n" \
"v_cvt_pk_bf16_f32 v130, v72, v73\n" \
"v_mfma_f32_16x16x32_bf16 v[52:55], a[200:203], a[80:83], v[52:55]\n" \
"ds_read_b128 a[4:7], v213 offset:2048\n" \
"v_cvt_pk_bf16_f32 v131, v74, v75\n" \
"v_mfma_f32_16x16x32_bf16 v[52:55], a[204:207], a[84:87], v[52:55]\n" \
"ds_read_b64_tr_b16 a[40:41], v215 offset:16384\n" \
"global_load_lds_dwordx4 v218, s[38:39] offset:-2048\n" \
"v_mfma_f32_16x16x32_bf16 v[88:91], a[80:83], a[208:211], v[88:91]\n" \
"ds_write_b128 v145, v[128:131]\n" \
"v_mfma_f32_16x16x32_bf16 v[88:91], a[84:87], a[212:215], v[88:91]\n" \
"ds_read_b64_tr_b16 a[42:43], v215 offset:18432\n" \
"v_cvt_pk_bf16_f32 v132, v40, v41\n" \
"v_mfma_f32_16x16x32_bf16 v[92:95], a[80:83], a[216:219], v[92:95]\n" \
"ds_read_b64_tr_b16 a[44:45], v215 offset:24576\n" \
"v_cvt_pk_bf16_f32 v133, v42, v43\n" \
"v_mfma_f32_16x16x32_bf16 v[92:95], a[84:87], a[220:223], v[92:95]\n" \
"global_load_lds_dwordx4 v219, s[38:39] offset:-1024\n" \
"v_cvt_pk_bf16_f32 v134, v44, v45\n" \
"v_mfma_f32_16x16x32_bf16 v[60:63], a[200:203], a[88:91], v[60:63]\n" \
"s_add_u32 s38, s38, 0x20000\n" \
"s_addc_u32 s39, s39, 0\n" \
"ds_read_b64_tr_b16 a[46:47], v215 offset:26624\n" \
"v_mfma_f32_16x16x32_bf16 v[60:63], a[204:207], a[92:95], v[60:63]\n" \
"s_sub_u32 s75, s75, 32768\n" \
"s_cselect_b32 s75, 131072, s75\n" \
"ds_read_b128 a[8:11], v213 offset:256\n" \
"v_mfma_f32_16x16x32_bf16 v[96:99], a[88:91], a[208:211], v[96:99]\n" \
"s_add_u32 m0, s75, s95\n" \
"v_cvt_pk_bf16_f32 v135, v46, v47\n" \
"v_cvt_pk_bf16_f32 v136, v48, v49\n" \
"v_mfma_f32_16x16x32_bf16 v[96:99], a[92:95], a[212:215], v[96:99]\n" \
"ds_read_b128 a[12:15], v213 offset:2304\n" \
"global_load_lds_dwordx4 v250, s[36:37] offset:0 sc1\n" \
"v_mfma_f32_16x16x32_bf16 v[100:103], a[88:91], a[216:219], v[100:103]\n" \
"ds_read_b128 a[16:19], v213 offset:4096\n" \
"v_cvt_pk_bf16_f32 v137, v50, v51\n" \
"v_mfma_f32_16x16x32_bf16 v[100:103], a[92:95], a[220:223], v[100:103]\n" \
"v_cvt_pk_bf16_f32 v138, v52, v53\n" \
"v_cvt_pk_bf16_f32 v139, v54, v55\n" \
"v_mfma_f32_16x16x32_bf16 v[68:71], a[200:203], a[192:195], v[68:71]\n" \
"ds_read_b128 a[20:23], v213 offset:6144\n" \
"v_cvt_pk_bf16_f32 v140, v56, v57\n" \
"v_mfma_f32_16x16x32_bf16 v[68:71], a[204:207], a[196:199], v[68:71]\n" \
"global_load_lds_dwordx4 v250, s[36:37] offset:1024 sc1\n" \
"v_cvt_pk_bf16_f32 v141, v58, v59\n" \
"v_mfma_f32_16x16x32_bf16 v[104:107], a[192:195], a[208:211], v[104:107]\n" \
"v_cvt_pk_bf16_f32 v142, v60, v61\n" \
"v_cvt_pk_bf16_f32 v143, v62, v63\n" \
"v_mfma_f32_16x16x32_bf16 v[104:107], a[196:199], a[212:215], v[104:107]\n" \
"v_mfma_f32_16x16x32_bf16 v[108:111], a[192:195], a[216:219], v[108:111]\n" \
"v_mfma_f32_16x16x32_bf16 v[108:111], a[196:199], a[220:223], v[108:111]\n" \
"s_waitcnt lgkmcnt(0)\n" \
"global_load_lds_dwordx4 v250, s[36:37] offset:2048 sc1\n" \
"v_mfma_f32_16x16x32_bf16 v[0:3], a[32:35], a[0:3], 0\n" \
"ds_read_b128 a[24:27], v213 offset:4352\n" \
"v_mfma_f32_16x16x32_bf16 v[0:3], a[36:39], a[4:7], v[0:3]\n" \
"ds_read_b128 a[28:31], v213 offset:6400\n" \
"global_load_lds_dwordx4 v250, s[36:37] offset:3072 sc1\n" \
"v_mfma_f32_16x16x32_bf16 v[4:7], a[40:43], a[0:3], 0\n" \
"s_add_u32 s36, s36, 0x1000\n" \
"ds_read_b64_tr_b16 a[48:49], v214 offset:16896\n" \
"v_mfma_f32_16x16x32_bf16 v[4:7], a[44:47], a[4:7], v[4:7]\n" \
"s_add_u32 m0, m0, 0x5000\n" \
"ds_read_b64_tr_b16 a[50:51], v214 offset:18944\n" \
"v_cvt_pk_bf16_f32 v64, v64, v65\n" \
"v_cvt_pk_bf16_f32 v65, v66, v67\n" \
"v_cvt_pk_bf16_f32 v66, v68, v69\n" \
"v_cvt_pk_bf16_f32 v67, v70, v71\n" \
"v_cvt_pk_bf16_f32 v68, v72, v73\n" \
"v_cvt_pk_bf16_f32 v69, v74, v75\n" \
"v_mfma_f32_16x16x32_bf16 v[16:19], a[32:35], a[8:11], 0\n" \
"ds_read_b64_tr_b16 a[52:53], v214 offset:25088\n" \
"global_load_lds_dwordx4 v216, s[38:39] offset:-4096\n" \
"v_cvt_pk_bf16_f32 v70, v80, v81\n" \
"v_cvt_pk_bf16_f32 v71, v82, v83\n" \
"v_cvt_pk_bf16_f32 v194, v84, v85\n" \
"v_mfma_f32_16x16x32_bf16 v[16:19], a[36:39], a[12:15], v[16:19]\n" \
"s_sub_u32 s74, s74, 32768\n" \
"s_cselect_b32 s74, 131072, s74\n" \
"ds_read_b64_tr_b16 a[54:55], v214 offset:27136\n" \
"v_mfma_f32_16x16x32_bf16 v[20:23], a[40:43], a[8:11], 0\n" \
"ds_read_b64_tr_b16 a[56:57], v215 offset:16896\n" \
"v_cvt_pk_bf16_f32 v195, v86, v87\n" \
"v_mfma_f32_16x16x32_bf16 v[20:23], a[44:47], a[12:15], v[20:23]\n" \
"ds_read_b64_tr_b16 a[58:59], v215 offset:18944\n" \
"global_load_lds_dwordx4 v217, s[38:39] offset:-3072\n" \
"v_mfma_f32_16x16x32_bf16 v[32:35], a[32:35], a[16:19], 0\n" \
"ds_read_b64_tr_b16 a[60:61], v215 offset:25088\n" \
"v_cvt_pk_bf16_f32 v72, v88, v89\n" \
"v_mfma_f32_16x16x32_bf16 v[32:35], a[36:39], a[20:23], v[32:35]\n" \
"ds_read_b64_tr_b16 a[62:63], v215 offset:27136\n" \
"v_cvt_pk_bf16_f32 v73, v90, v91\n" \
"v_mfma_f32_16x16x32_bf16 v[36:39], a[40:43], a[16:19], 0\n" \
"global_load_lds_dwordx4 v218, s[38:39] offset:-2048\n" \
"v_add_u32 v213, s74, v211\n" \
"v_mfma_f32_16x16x32_bf16 v[36:39], a[44:47], a[20:23], v[36:39]\n" \
"s_waitcnt lgkmcnt(3)\n" \
"v_add_u32 v214, s74, v212\n" \
"v_xor_b32 v215, 32, v214\n" \
"v_mfma_f32_16x16x32_bf16 v[48:51], a[32:35], a[24:27], 0\n" \
"global_load_lds_dwordx4 v219, s[38:39] offset:-1024\n" \
"v_cvt_pk_bf16_f32 v196, v92, v93\n" \
"v_mfma_f32_16x16x32_bf16 v[48:51], a[36:39], a[28:31], v[48:51]\n" \
"s_add_u32 s38, s38, 0x20000\n" \
"s_addc_u32 s39, s39, 0\n" \
"v_cvt_pk_bf16_f32 v197, v94, v95\n" \
"v_mfma_f32_16x16x32_bf16 v[52:55], a[40:43], a[24:27], 0\n" \
"s_waitcnt vmcnt(16) lgkmcnt(0)\n" \
"s_sub_u32 s75, s75, 32768\n" \
"s_cselect_b32 s75, 131072, s75\n" \
"v_mfma_f32_16x16x32_bf16 v[52:55], a[44:47], a[28:31], v[52:55]\n" \
"s_barrier\n" \
"v_cvt_pk_bf16_f32 v74, v96, v97\n" \
"v_mfma_f32_16x16x32_bf16 v[8:11], a[48:51], a[0:3], 0\n" \
"s_add_u32 m0, s75, s95\n" \
"ds_read_b64 v[130:131], v223\n" \
"ds_read_b64_tr_b16 a[32:33], v214 offset:16384\n" \
"v_mfma_f32_16x16x32_bf16 v[8:11], a[52:55], a[4:7], v[8:11]\n" \
"ds_read_b64 v[192:193], v223 offset:8\n" \
"ds_read_b64_tr_b16 a[34:35], v214 offset:18432\n" \
"v_mfma_f32_16x16x32_bf16 v[12:15], a[56:59], a[0:3], 0\n" \
"ds_read_b64_tr_b16 a[36:37], v214 offset:24576\n" \
"global_load_lds_dwordx4 v250, s[36:37] offset:0 sc1\n" \
"v_mfma_f32_16x16x32_bf16 v[12:15], a[60:63], a[4:7], v[12:15]\n" \
"ds_read_b128 a[0:3], v213 offset:0\n" \
"v_cvt_pk_bf16_f32 v75, v98, v99\n" \
"v_mfma_f32_16x16x32_bf16 v[24:27], a[48:51], a[8:11], 0\n" \
"ds_read_b128 a[4:7], v213 offset:2048\n" \
"global_load_lds_dwordx4 v250, s[36:37] offset:1024 sc1\n" \
"v_mfma_f32_16x16x32_bf16 v[24:27], a[52:55], a[12:15], v[24:27]\n" \
"ds_read_b64_tr_b16 a[38:39], v214 offset:26624\n" \
"v_cvt_pk_bf16_f32 v198, v100, v101\n" \
"v_mfma_f32_16x16x32_bf16 v[28:31], a[56:59], a[8:11], 0\n" \
"ds_read_b64_tr_b16 a[40:41], v215 offset:16384\n" \
"v_cvt_pk_bf16_f32 v199, v102, v103\n" \
"v_mfma_f32_16x16x32_bf16 v[28:31], a[60:63], a[12:15], v[28:31]\n" \
"s_waitcnt lgkmcnt(4)\n" \
"ds_read_b128 a[8:11], v213 offset:256\n" \
"global_load_lds_dwordx4 v250, s[36:37] offset:2048 sc1\n" \
"v_mfma_f32_16x16x32_bf16 v[40:43], a[48:51], a[16:19], 0\n" \
"ds_read_b128 a[12:15], v213 offset:2304\n" \
"v_cvt_pk_bf16_f32 v76, v104, v105\n" \
"v_mfma_f32_16x16x32_bf16 v[40:43], a[52:55], a[20:23], v[40:43]\n" \
"ds_read_b64_tr_b16 a[42:43], v215 offset:18432\n" \
"v_cvt_pk_bf16_f32 v77, v106, v107\n" \
"v_mfma_f32_16x16x32_bf16 v[44:47], a[56:59], a[16:19], 0\n" \
"v_cvt_pk_bf16_f32 v200, v108, v109\n" \
"v_cvt_pk_bf16_f32 v201, v110, v111\n" \
"v_mfma_f32_16x16x32_bf16 v[176:179], v[64:67], v[124:127], v[176:179]\n" \
"v_mfma_f32_16x16x32_bf16 v[172:175], v[140:143], v[124:127], v[172:175]\n" \
"v_mfma_f32_16x16x32_bf16 v[168:171], v[140:143], v[120:123], v[168:171]\n" \
"ds_read_b64_tr_b16 a[44:45], v215 offset:24576\n" \
"global_load_lds_dwordx4 v250, s[36:37] offset:3072 sc1\n" \
"v_mfma_f32_16x16x32_bf16 v[44:47], a[60:63], a[20:23], v[44:47]\n" \
"s_add_u32 s36, s36, 0x1000\n" \
"s_add_u32 m0, m0, 0x5000\n" \
"ds_read_b128 a[16:19], v213 offset:4096\n" \
"v_mfma_f32_16x16x32_bf16 v[164:167], v[136:139], v[120:123], v[164:167]\n" \
"v_mfma_f32_16x16x32_bf16 v[160:163], v[136:139], v[116:119], v[160:163]\n" \
"v_mfma_f32_16x16x32_bf16 v[156:159], v[132:135], v[116:119], v[156:159]\n" \
"v_mfma_f32_16x16x32_bf16 v[152:155], v[132:135], v[112:115], v[152:155]\n" \
"v_mfma_f32_16x16x32_bf16 v[148:151], v[128:131], v[112:115], v[148:151]\n" \
"v_mfma_f32_16x16x32_bf16 v[56:59], a[48:51], a[24:27], 0\n" \
"ds_read_b128 a[20:23], v213 offset:6144\n" \
"global_load_lds_dwordx4 v216, s[38:39] offset:-4096\n" \
"v_mfma_f32_16x16x32_bf16 v[56:59], a[52:55], a[28:31], v[56:59]\n" \
"ds_read_b64_tr_b16 a[46:47], v215 offset:26624\n" \
"v_max3_f32 v183, v172, v173, v174\n" \
"v_max3_f32 v182, v164, v165, v166\n" \
"v_max3_f32 v181, v156, v157, v158\n" \
"v_max3_f32 v180, v148, v149, v150\n" \
"v_max3_f32 v183, v183, v175, v176\n" \
"v_max3_f32 v182, v182, v167, v168\n" \
"v_mfma_f32_16x16x32_bf16 v[60:63], a[56:59], a[24:27], 0\n" \
"v_max3_f32 v181, v181, v159, v160\n" \
"v_max3_f32 v180, v180, v151, v152\n" \
"v_max3_f32 v183, v183, v177, v178\n" \
"v_max3_f32 v182, v182, v169, v170\n" \
"v_max3_f32 v181, v181, v161, v162\n" \
"v_max3_f32 v180, v180, v153, v154\n" \
"v_mfma_f32_16x16x32_bf16 v[60:63], a[60:63], a[28:31], v[60:63]\n" \
"s_waitcnt lgkmcnt(3)\n" \
"v_max_f32 v183, v183, v179\n" \
"v_max_f32 v182, v182, v171\n" \
"v_max_f32 v181, v181, v163\n" \
"v_max_f32 v180, v180, v155\n" \
"v_mov_b32 v187, v183\n" \
"v_mov_b32 v186, v182\n" \
"v_mfma_f32_16x16x32_bf16 v[0:3], a[32:35], a[0:3], v[0:3]\n" \
"ds_read_b128 a[24:27], v213 offset:4352\n" \
"v_mov_b32 v185, v181\n" \
"v_mov_b32 v184, v180\n" \
"v_permlane16_swap_b32 v187, v183\n" \
"v_permlane16_swap_b32 v186, v182\n" \
"v_permlane16_swap_b32 v185, v181\n" \
"v_permlane16_swap_b32 v184, v180\n" \
"v_mfma_f32_16x16x32_bf16 v[0:3], a[36:39], a[4:7], v[0:3]\n" \
"s_waitcnt lgkmcnt(1)\n" \
"ds_read_b128 a[28:31], v213 offset:6400\n" \
"global_load_lds_dwordx4 v217, s[38:39] offset:-3072\n" \
"v_max_f32 v183, v183, v187\n" \
"v_max_f32 v182, v182, v186\n" \
"v_max_f32 v181, v181, v185\n" \
"v_max_f32 v180, v180, v184\n" \
"v_mov_b32 v187, v183\n" \
"v_mov_b32 v186, v182\n" \
"v_mfma_f32_16x16x32_bf16 v[4:7], a[40:43], a[0:3], v[4:7]\n" \
"s_sub_u32 s74, s74, 32768\n" \
"s_cselect_b32 s74, 131072, s74\n" \
"ds_read_b64_tr_b16 a[48:49], v214 offset:16896\n" \
"v_mov_b32 v185, v181\n" \
"v_mov_b32 v184, v180\n" \
"v_permlane32_swap_b32 v187, v183\n" \
"v_permlane32_swap_b32 v186, v182\n" \
"v_permlane32_swap_b32 v185, v181\n" \
"v_mfma_f32_16x16x32_bf16 v[4:7], a[44:47], a[4:7], v[4:7]\n" \
"ds_read_b64_tr_b16 a[50:51], v214 offset:18944\n" \
"v_permlane32_swap_b32 v184, v180\n" \
"v_mfma_f32_16x16x32_bf16 v[16:19], a[32:35], a[8:11], v[16:19]\n" \
"ds_read_b64_tr_b16 a[52:53], v214 offset:25088\n" \
"global_load_lds_dwordx4 v218, s[38:39] offset:-2048\n" \
"v_max_f32 v183, v183, v187\n" \
"v_mfma_f32_16x16x32_bf16 v[16:19], a[36:39], a[12:15], v[16:19]\n" \
"ds_read_b64_tr_b16 a[54:55], v214 offset:27136\n" \
"v_max_f32 v182, v182, v186\n" \
"v_max_f32 v181, v181, v185\n" \
"v_mfma_f32_16x16x32_bf16 v[20:23], a[40:43], a[8:11], v[20:23]\n" \
"ds_read_b64_tr_b16 a[56:57], v215 offset:16896\n" \
"global_load_lds_dwordx4 v219, s[38:39] offset:-1024\n" \
"v_max_f32 v180, v180, v184\n" \
"v_mul_f32 v183, v183, v144\n" \
"v_mfma_f32_16x16x32_bf16 v[20:23], a[44:47], a[12:15], v[20:23]\n" \
"s_add_u32 s38, s38, 0x20000\n" \
"s_addc_u32 s39, s39, 0\n" \
"ds_read_b64_tr_b16 a[58:59], v215 offset:18944\n" \
"v_mul_f32 v182, v182, v144\n" \
"v_mul_f32 v181, v181, v144\n" \
"v_mfma_f32_16x16x32_bf16 v[32:35], a[32:35], a[16:19], v[32:35]\n" \
"s_sub_u32 s75, s75, 32768\n" \
"s_cselect_b32 s75, 131072, s75\n" \
"ds_read_b64_tr_b16 a[60:61], v215 offset:25088\n" \
"v_mul_f32 v180, v180, v144\n" \
"v_fma_f32 v172, v172, v144, -v183\n" \
"v_mfma_f32_16x16x32_bf16 v[32:35], a[36:39], a[20:23], v[32:35]\n" \
"s_add_u32 m0, s75, s95\n" \
"ds_read_b64_tr_b16 a[62:63], v215 offset:27136\n" \
"v_fma_f32 v164, v164, v144, -v182\n" \
"v_mfma_f32_16x16x32_bf16 v[36:39], a[40:43], a[16:19], v[36:39]\n" \
"global_load_lds_dwordx4 v250, s[36:37] offset:0 sc1\n" \
"v_add_u32 v213, s74, v211\n" \
"v_fma_f32 v156, v156, v144, -v181\n" \
"v_mfma_f32_16x16x32_bf16 v[36:39], a[44:47], a[20:23], v[36:39]\n" \
"s_waitcnt lgkmcnt(3)\n" \
"v_add_u32 v214, s74, v212\n" \
"v_xor_b32 v215, 32, v214\n" \
"v_exp_f32 v172, v172\n" \
"v_fma_f32 v148, v148, v144, -v180\n" \
"v_mfma_f32_16x16x32_bf16 v[48:51], a[32:35], a[24:27], v[48:51]\n" \
"v_exp_f32 v164, v164\n" \
"v_mfma_f32_16x16x32_bf16 v[48:51], a[36:39], a[28:31], v[48:51]\n" \
"global_load_lds_dwordx4 v250, s[36:37] offset:1024 sc1\n" \
"v_fma_f32 v173, v173, v144, -v183\n" \
"v_mfma_f32_16x16x32_bf16 v[52:55], a[40:43], a[24:27], v[52:55]\n" \
"s_waitcnt vmcnt(18) lgkmcnt(0)\n" \
"v_exp_f32 v156, v156\n" \
"v_mfma_f32_16x16x32_bf16 v[52:55], a[44:47], a[28:31], v[52:55]\n" \
"s_barrier\n" \
"v_fma_f32 v165, v165, v144, -v182\n" \
"v_mfma_f32_16x16x32_bf16 v[8:11], a[48:51], a[0:3], v[8:11]\n" \
"ds_read_b64_tr_b16 a[32:33], v214 offset:16384\n" \
"v_exp_f32 v148, v148\n" \
"v_fma_f32 v157, v157, v144, -v181\n" \
"v_mfma_f32_16x16x32_bf16 v[8:11], a[52:55], a[4:7], v[8:11]\n" \
"ds_read_b64_tr_b16 a[34:35], v214 offset:18432\n" \
"global_load_lds_dwordx4 v250, s[36:37] offset:2048 sc1\n" \
"v_mfma_f32_16x16x32_bf16 v[12:15], a[56:59], a[0:3], v[12:15]\n" \
"ds_read_b64_tr_b16 a[36:37], v214 offset:24576\n" \
"v_exp_f32 v173, v173\n" \
"v_fma_f32 v149, v149, v144, -v180\n" \
"v_mfma_f32_16x16x32_bf16 v[12:15], a[60:63], a[4:7], v[12:15]\n" \
"ds_read_b128 a[0:3], v213 offset:0\n" \
"v_exp_f32 v165, v165\n" \
"v_mfma_f32_16x16x32_bf16 v[24:27], a[48:51], a[8:11], v[24:27]\n" \
"ds_read_b128 a[4:7], v213 offset:2048\n" \
"global_load_lds_dwordx4 v250, s[36:37] offset:3072 sc1\n" \
"v_fma_f32 v174, v174, v144, -v183\n" \
"v_mfma_f32_16x16x32_bf16 v[24:27], a[52:55], a[12:15], v[24:27]\n" \
"s_add_u32 s36, s36, 0x1000\n" \
"ds_read_b64_tr_b16 a[38:39], v214 offset:26624\n" \
"v_exp_f32 v157, v157\n" \
"v_mfma_f32_16x16x32_bf16 v[28:31], a[56:59], a[8:11], v[28:31]\n" \
"s_add_u32 m0, m0, 0x5000\n" \
"ds_read_b64_tr_b16 a[40:41], v215 offset:16384\n" \
"v_fma_f32 v166, v166, v144, -v182\n" \
"v_mfma_f32_16x16x32_bf16 v[28:31], a[60:63], a[12:15], v[28:31]\n" \
"ds_read_b128 a[8:11], v213 offset:256\n" \
"global_load_lds_dwordx4 v216, s[38:39] offset:-4096\n" \
"v_exp_f32 v149, v149\n" \
"v_fma_f32 v158, v158, v144, -v181\n" \
"v_mfma_f32_16x16x32_bf16 v[40:43], a[48:51], a[16:19], v[40:43]\n" \
"ds_read_b128 a[12:15], v213 offset:2304\n" \
"v_exp_f32 v174, v174\n" \
"v_mfma_f32_16x16x32_bf16 v[40:43], a[52:55], a[20:23], v[40:43]\n" \
"ds_read_b64_tr_b16 a[42:43], v215 offset:18432\n" \
"v_add_f32 v191, v172, v173\n" \
"v_fma_f32 v150, v150, v144, -v180\n" \
"v_mfma_f32_16x16x32_bf16 v[44:47], a[56:59], a[16:19], v[44:47]\n" \
"ds_read_b64_tr_b16 a[44:45], v215 offset:24576\n" \
"global_load_lds_dwordx4 v217, s[38:39] offset:-3072\n" \
"v_mfma_f32_16x16x32_bf16 v[44:47], a[60:63], a[20:23], v[44:47]\n" \
"ds_read_b128 a[16:19], v213 offset:4096\n" \
"v_exp_f32 v166, v166\n" \
"v_add_f32 v190, v164, v165\n" \
"v_mfma_f32_16x16x32_bf16 v[56:59], a[48:51], a[24:27], v[56:59]\n" \
"ds_read_b128 a[20:23], v213 offset:6144\n" \
"global_load_lds_dwordx4 v218, s[38:39] offset:-2048\n" \
"v_fma_f32 v175, v175, v144, -v183\n" \
"v_mfma_f32_16x16x32_bf16 v[56:59], a[52:55], a[28:31], v[56:59]\n" \
"ds_read_b64_tr_b16 a[46:47], v215 offset:26624\n" \
"v_exp_f32 v158, v158\n" \
"v_mfma_f32_16x16x32_bf16 v[60:63], a[56:59], a[24:27], v[60:63]\n" \
"v_add_f32 v189, v156, v157\n" \
"v_fma_f32 v167, v167, v144, -v182\n" \
"v_mfma_f32_16x16x32_bf16 v[60:63], a[60:63], a[28:31], v[60:63]\n" \
"s_waitcnt lgkmcnt(3)\n" \
"v_exp_f32 v150, v150\n" \
"v_mfma_f32_16x16x32_bf16 v[0:3], a[32:35], a[0:3], v[0:3]\n" \
"ds_read_b128 a[24:27], v213 offset:4352\n" \
"v_add_f32 v188, v148, v149\n" \
"v_fma_f32 v159, v159, v144, -v181\n" \
"v_mfma_f32_16x16x32_bf16 v[0:3], a[36:39], a[4:7], v[0:3]\n" \
"s_waitcnt lgkmcnt(1)\n" \
"ds_read_b128 a[28:31], v213 offset:6400\n" \
"global_load_lds_dwordx4 v219, s[38:39] offset:-1024\n" \
"v_exp_f32 v175, v175\n" \
"v_mfma_f32_16x16x32_bf16 v[4:7], a[40:43], a[0:3], v[4:7]\n" \
"s_add_u32 s38, s38, 0x20000\n" \
"s_addc_u32 s39, s39, 0\n" \
"ds_read_b64_tr_b16 a[48:49], v214 offset:16896\n" \
"v_add_f32 v191, v191, v174\n" \
"v_fma_f32 v151, v151, v144, -v180\n" \
"v_mfma_f32_16x16x32_bf16 v[4:7], a[44:47], a[4:7], v[4:7]\n" \
"s_sub_u32 s75, s75, 32768\n" \
"s_cselect_b32 s75, 131072, s75\n" \
"ds_read_b64_tr_b16 a[50:51], v214 offset:18944\n" \
"v_mfma_f32_16x16x32_bf16 v[16:19], a[32:35], a[8:11], v[16:19]\n" \
"s_add_u32 m0, s75, s95\n" \
"ds_read_b64_tr_b16 a[52:53], v214 offset:25088\n" \
"v_exp_f32 v167, v167\n" \
"v_add_f32 v190, v190, v166\n" \
"v_mfma_f32_16x16x32_bf16 v[16:19], a[36:39], a[12:15], v[16:19]\n" \
"ds_read_b64_tr_b16 a[54:55], v214 offset:27136\n" \
"global_load_lds_dwordx4 v250, s[36:37] offset:0 sc1\n" \
"v_fma_f32 v176, v176, v144, -v183\n" \
"v_mfma_f32_16x16x32_bf16 v[20:23], a[40:43], a[8:11], v[20:23]\n" \
"s_sub_u32 s74, s74, 32768\n" \
"s_cselect_b32 s74, 131072, s74\n" \
"ds_read_b64_tr_b16 a[56:57], v215 offset:16896\n" \
"v_exp_f32 v159, v159\n" \
"v_add_f32 v189, v189, v158\n" \
"v_mfma_f32_16x16x32_bf16 v[20:23], a[44:47], a[12:15], v[20:23]\n" \
"ds_read_b64_tr_b16 a[58:59], v215 offset:18944\n" \
"global_load_lds_dwordx4 v250, s[36:37] offset:1024 sc1\n" \
"v_fma_f32 v168, v168, v144, -v182\n" \
"v_mfma_f32_16x16x32_bf16 v[32:35], a[32:35], a[16:19], v[32:35]\n" \
"ds_read_b64_tr_b16 a[60:61], v215 offset:25088\n" \
"v_exp_f32 v151, v151\n" \
"v_mfma_f32_16x16x32_bf16 v[32:35], a[36:39], a[20:23], v[32:35]\n" \
"ds_read_b64_tr_b16 a[62:63], v215 offset:27136\n" \
"v_add_f32 v188, v188, v150\n" \
"v_fma_f32 v160, v160, v144, -v181\n" \
"v_mfma_f32_16x16x32_bf16 v[36:39], a[40:43], a[16:19], v[36:39]\n" \
"global_load_lds_dwordx4 v250, s[36:37] offset:2048 sc1\n" \
"v_add_u32 v213, s74, v211\n" \
"v_exp_f32 v176, v176\n" \
"v_mfma_f32_16x16x32_bf16 v[36:39], a[44:47], a[20:23], v[36:39]\n" \
"s_waitcnt lgkmcnt(3)\n" \
"v_add_u32 v214, s74, v212\n" \
"v_xor_b32 v215, 32, v214\n" \
"v_mfma_f32_16x16x32_bf16 v[48:51], a[32:35], a[24:27], v[48:51]\n" \
"v_add_f32 v191, v191, v175\n" \
"v_fma_f32 v152, v152, v144, -v180\n" \
"v_mfma_f32_16x16x32_bf16 v[48:51], a[36:39], a[28:31], v[48:51]\n" \
"global_load_lds_dwordx4 v250, s[36:37] offset:3072 sc1\n" \
"v_exp_f32 v168, v168\n" \
"v_add_f32 v190, v190, v167\n" \
"v_mfma_f32_16x16x32_bf16 v[52:55], a[40:43], a[24:27], v[52:55]\n" \
"s_waitcnt vmcnt(20) lgkmcnt(0)\n" \
"v_fma_f32 v177, v177, v144, -v183\n" \
"s_add_u32 s36, s36, 0x1000\n" \
"v_mfma_f32_16x16x32_bf16 v[52:55], a[44:47], a[28:31], v[52:55]\n" \
"s_barrier\n" \
"v_exp_f32 v160, v160\n" \
"v_mfma_f32_16x16x32_bf16 v[8:11], a[48:51], a[0:3], v[8:11]\n" \
"s_add_u32 m0, m0, 0x5000\n" \
"ds_read_b64_tr_b16 a[32:33], v214 offset:16384\n" \
"v_add_f32 v189, v189, v159\n" \
"v_fma_f32 v169, v169, v144, -v182\n" \
"v_mfma_f32_16x16x32_bf16 v[8:11], a[52:55], a[4:7], v[8:11]\n" \
"ds_read_b64_tr_b16 a[34:35], v214 offset:18432\n" \
"global_load_lds_dwordx4 v216, s[38:39] offset:-4096\n" \
"v_exp_f32 v152, v152\n" \
"v_mfma_f32_16x16x32_bf16 v[12:15], a[56:59], a[0:3], v[12:15]\n" \
"ds_read_b64_tr_b16 a[36:37], v214 offset:24576\n" \
"v_add_f32 v188, v188, v151\n" \
"v_fma_f32 v161, v161, v144, -v181\n" \
"v_mfma_f32_16x16x32_bf16 v[12:15], a[60:63], a[4:7], v[12:15]\n" \
"ds_read_b128 a[0:3], v213 offset:0\n" \
"v_exp_f32 v177, v177\n" \
"v_mfma_f32_16x16x32_bf16 v[24:27], a[48:51], a[8:11], v[24:27]\n" \
"ds_read_b128 a[4:7], v213 offset:2048\n" \
"global_load_lds_dwordx4 v217, s[38:39] offset:-3072\n" \
"v_add_f32 v191, v191, v176\n" \
"v_mfma_f32_16x16x32_bf16 v[24:27], a[52:55], a[12:15], v[24:27]\n" \
"ds_read_b64_tr_b16 a[38:39], v214 offset:26624\n" \
"v_fma_f32 v153, v153, v144, -v180\n" \
"v_mfma_f32_16x16x32_bf16 v[28:31], a[56:59], a[8:11], v[28:31]\n" \
"ds_read_b64_tr_b16 a[40:41], v215 offset:16384\n" \
"v_exp_f32 v169, v169\n" \
"v_add_f32 v190, v190, v168\n" \
"v_mfma_f32_16x16x32_bf16 v[28:31], a[60:63], a[12:15], v[28:31]\n" \
"ds_read_b128 a[8:11], v213 offset:256\n" \
"global_load_lds_dwordx4 v218, s[38:39] offset:-2048\n" \
"v_fma_f32 v178, v178, v144, -v183\n" \
"v_mfma_f32_16x16x32_bf16 v[40:43], a[48:51], a[16:19], v[40:43]\n" \
"ds_read_b128 a[12:15], v213 offset:2304\n" \
"v_exp_f32 v161, v161\n" \
"v_mfma_f32_16x16x32_bf16 v[40:43], a[52:55], a[20:23], v[40:43]\n" \
"ds_read_b64_tr_b16 a[42:43], v215 offset:18432\n" \
"Lpg11602_%=:\n" \
"s_cbranch_execz Lpg12794_%=\n" \
"v_add_f32 v189, v189, v160\n" \
"v_fma_f32 v170, v170, v144, -v182\n" \
"v_mfma_f32_16x16x32_bf16 v[44:47], a[56:59], a[16:19], v[44:47]\n" \
"ds_read_b64_tr_b16 a[44:45], v215 offset:24576\n" \
"global_load_lds_dwordx4 v219, s[38:39] offset:-1024\n" \
"v_exp_f32 v153, v153\n" \
"v_mfma_f32_16x16x32_bf16 v[44:47], a[60:63], a[20:23], v[44:47]\n" \
"s_add_u32 s38, s38, 0x20000\n" \
"s_addc_u32 s39, s39, 0\n" \
"ds_read_b128 a[16:19], v213 offset:4096\n" \
"v_add_f32 v188, v188, v152\n" \
"v_fma_f32 v162, v162, v144, -v181\n" \
"v_mfma_f32_16x16x32_bf16 v[56:59], a[48:51], a[24:27], v[56:59]\n" \
"s_sub_u32 s75, s75, 32768\n" \
"s_cselect_b32 s75, 131072, s75\n" \
"ds_read_b128 a[20:23], v213 offset:6144\n" \
"v_mfma_f32_16x16x32_bf16 v[56:59], a[52:55], a[28:31], v[56:59]\n" \
"ds_read_b64_tr_b16 a[46:47], v215 offset:26624\n" \
"v_exp_f32 v178, v178\n" \
"v_mfma_f32_16x16x32_bf16 v[60:63], a[56:59], a[24:27], v[60:63]\n" \
"v_add_f32 v191, v191, v177\n" \
"v_fma_f32 v154, v154, v144, -v180\n" \
"v_mfma_f32_16x16x32_bf16 v[60:63], a[60:63], a[28:31], v[60:63]\n" \
"s_waitcnt lgkmcnt(3)\n" \
"v_exp_f32 v170, v170\n" \
"v_add_f32 v190, v190, v169\n" \
"v_mfma_f32_16x16x32_bf16 v[0:3], a[32:35], a[0:3], v[0:3]\n" \
"s_add_u32 m0, s75, s95\n" \
"ds_read_b128 a[24:27], v213 offset:4352\n" \
"v_fma_f32 v179, v179, v144, -v183\n" \
"v_mfma_f32_16x16x32_bf16 v[0:3], a[36:39], a[4:7], v[0:3]\n" \
"s_waitcnt lgkmcnt(1)\n" \
"ds_read_b128 a[28:31], v213 offset:6400\n" \
"global_load_lds_dwordx4 v250, s[36:37] offset:0 sc1\n" \
"v_exp_f32 v162, v162\n" \
"v_mfma_f32_16x16x32_bf16 v[4:7], a[40:43], a[0:3], v[4:7]\n" \
"s_sub_u32 s74, s74, 32768\n" \
"s_cselect_b32 s74, 131072, s74\n" \
"ds_read_b64_tr_b16 a[48:49], v214 offset:16896\n" \
"v_add_f32 v189, v189, v161\n" \
"v_fma_f32 v171, v171, v144, -v182\n" \
"v_mfma_f32_16x16x32_bf16 v[4:7], a[44:47], a[4:7], v[4:7]\n" \
"ds_read_b64_tr_b16 a[50:51], v214 offset:18944\n" \
"v_exp_f32 v154, v154\n" \
"v_mfma_f32_16x16x32_bf16 v[16:19], a[32:35], a[8:11], v[16:19]\n" \
"ds_read_b64_tr_b16 a[52:53], v214 offset:25088\n" \
"v_add_f32 v188, v188, v153\n" \
"v_fma_f32 v163, v163, v144, -v181\n" \
"v_mfma_f32_16x16x32_bf16 v[16:19], a[36:39], a[12:15], v[16:19]\n" \
"ds_read_b64_tr_b16 a[54:55], v214 offset:27136\n" \
"global_load_lds_dwordx4 v250, s[36:37] offset:1024 sc1\n" \
"v_mfma_f32_16x16x32_bf16 v[20:23], a[40:43], a[8:11], v[20:23]\n" \
"ds_read_b64_tr_b16 a[56:57], v215 offset:16896\n" \
"v_exp_f32 v179, v179\n" \
"v_add_f32 v191, v191, v178\n" \
"v_mfma_f32_16x16x32_bf16 v[20:23], a[44:47], a[12:15], v[20:23]\n" \
"ds_read_b64_tr_b16 a[58:59], v215 offset:18944\n" \
"v_fma_f32 v155, v155, v144, -v180\n" \
"v_mfma_f32_16x16x32_bf16 v[32:35], a[32:35], a[16:19], v[32:35]\n" \
"ds_read_b64_tr_b16 a[60:61], v215 offset:25088\n" \
"v_exp_f32 v171, v171\n" \
"v_add_f32 v190, v190, v170\n" \
"v_mfma_f32_16x16x32_bf16 v[32:35], a[36:39], a[20:23], v[32:35]\n" \
"ds_read_b64_tr_b16 a[62:63], v215 offset:27136\n" \
"global_load_lds_dwordx4 v250, s[36:37] offset:2048 sc1\n" \
"v_mfma_f32_16x16x32_bf16 v[36:39], a[40:43], a[16:19], v[36:39]\n" \
"v_add_u32 v213, s74, v211\n" \
"v_add_u32 v214, s74, v212\n" \
"v_exp_f32 v163, v163\n" \
"v_add_f32 v189, v189, v162\n" \
"v_mfma_f32_16x16x32_bf16 v[36:39], a[44:47], a[20:23], v[36:39]\n" \
"s_waitcnt lgkmcnt(3)\n" \
"v_xor_b32 v215, 32, v214\n" \
"v_exp_f32 v155, v155\n" \
"v_mfma_f32_16x16x32_bf16 v[48:51], a[32:35], a[24:27], v[48:51]\n" \
"v_add_f32 v188, v188, v154\n" \
"v_add_f32 v191, v191, v179\n" \
"v_mfma_f32_16x16x32_bf16 v[48:51], a[36:39], a[28:31], v[48:51]\n" \
"global_load_lds_dwordx4 v250, s[36:37] offset:3072 sc1\n" \
"v_add_f32 v190, v190, v171\n" \
"v_mfma_f32_16x16x32_bf16 v[52:55], a[40:43], a[24:27], v[52:55]\n" \
"s_waitcnt vmcnt(20) lgkmcnt(0)\n" \
"v_add_f32 v189, v189, v163\n" \
"v_add_f32 v188, v188, v155\n" \
"s_add_u32 s36, s36, 0x1000\n" \
"v_mfma_f32_16x16x32_bf16 v[52:55], a[44:47], a[28:31], v[52:55]\n" \
"s_barrier\n" \
"v_mov_b32 v187, v191\n" \
"v_mov_b32 v186, v190\n" \
"v_mfma_f32_16x16x32_bf16 v[8:11], a[48:51], a[0:3], v[8:11]\n" \
"s_add_u32 m0, m0, 0x5000\n" \
"ds_read_b64_tr_b16 a[32:33], v214 offset:16384\n" \
"v_mov_b32 v185, v189\n" \
"v_mov_b32 v184, v188\n" \
"v_mfma_f32_16x16x32_bf16 v[8:11], a[52:55], a[4:7], v[8:11]\n" \
"ds_read_b64_tr_b16 a[34:35], v214 offset:18432\n" \
"global_load_lds_dwordx4 v216, s[38:39] offset:-4096\n" \
"v_mfma_f32_16x16x32_bf16 v[12:15], a[56:59], a[0:3], v[12:15]\n" \
"ds_read_b64_tr_b16 a[36:37], v214 offset:24576\n" \
"v_permlane16_swap_b32 v187, v191\n" \
"v_mfma_f32_16x16x32_bf16 v[12:15], a[60:63], a[4:7], v[12:15]\n" \
"ds_read_b128 a[0:3], v213 offset:0\n" \
"v_permlane16_swap_b32 v186, v190\n" \
"v_mfma_f32_16x16x32_bf16 v[24:27], a[48:51], a[8:11], v[24:27]\n" \
"ds_read_b128 a[4:7], v213 offset:2048\n" \
"v_permlane16_swap_b32 v185, v189\n" \
"v_mfma_f32_16x16x32_bf16 v[24:27], a[52:55], a[12:15], v[24:27]\n" \
"ds_read_b64_tr_b16 a[38:39], v214 offset:26624\n" \
"global_load_lds_dwordx4 v217, s[38:39] offset:-3072\n" \
"v_permlane16_swap_b32 v184, v188\n" \
"v_add_f32 v191, v191, v187\n" \
"v_mfma_f32_16x16x32_bf16 v[28:31], a[56:59], a[8:11], v[28:31]\n" \
"ds_read_b64_tr_b16 a[40:41], v215 offset:16384\n" \
"v_add_f32 v190, v190, v186\n" \
"v_mfma_f32_16x16x32_bf16 v[28:31], a[60:63], a[12:15], v[28:31]\n" \
"ds_read_b128 a[8:11], v213 offset:256\n" \
"v_add_f32 v189, v189, v185\n" \
"v_add_f32 v188, v188, v184\n" \
"v_mfma_f32_16x16x32_bf16 v[40:43], a[48:51], a[16:19], v[40:43]\n" \
"ds_read_b128 a[12:15], v213 offset:2304\n" \
"v_mov_b32 v187, v191\n" \
"v_mov_b32 v186, v190\n" \
"v_mfma_f32_16x16x32_bf16 v[40:43], a[52:55], a[20:23], v[40:43]\n" \
"ds_read_b64_tr_b16 a[42:43], v215 offset:18432\n" \
"global_load_lds_dwordx4 v218, s[38:39] offset:-2048\n" \
"v_mov_b32 v185, v189\n" \
"v_mfma_f32_16x16x32_bf16 v[44:47], a[56:59], a[16:19], v[44:47]\n" \
"ds_read_b64_tr_b16 a[44:45], v215 offset:24576\n" \
"v_mov_b32 v184, v188\n" \
"v_mfma_f32_16x16x32_bf16 v[44:47], a[60:63], a[20:23], v[44:47]\n" \
"ds_read_b128 a[16:19], v213 offset:4096\n" \
"v_permlane32_swap_b32 v187, v191\n" \
"v_mfma_f32_16x16x32_bf16 v[56:59], a[48:51], a[24:27], v[56:59]\n" \
"ds_read_b128 a[20:23], v213 offset:6144\n" \
"global_load_lds_dwordx4 v219, s[38:39] offset:-1024\n" \
"v_permlane32_swap_b32 v186, v190\n" \
"v_mfma_f32_16x16x32_bf16 v[56:59], a[52:55], a[28:31], v[56:59]\n" \
"s_add_u32 s38, s38, 0x20000\n" \
"s_addc_u32 s39, s39, 0\n" \
"ds_read_b64_tr_b16 a[46:47], v215 offset:26624\n" \
"v_permlane32_swap_b32 v185, v189\n" \
"v_mfma_f32_16x16x32_bf16 v[60:63], a[56:59], a[24:27], v[60:63]\n" \
"s_sub_u32 s75, s75, 32768\n" \
"s_cselect_b32 s75, 131072, s75\n" \
"v_permlane32_swap_b32 v184, v188\n" \
"v_mfma_f32_16x16x32_bf16 v[60:63], a[60:63], a[28:31], v[60:63]\n" \
"s_waitcnt lgkmcnt(3)\n" \
"v_add_f32 v191, v191, v187\n" \
"v_add_f32 v190, v190, v186\n" \
"v_mfma_f32_16x16x32_bf16 v[0:3], a[32:35], a[0:3], v[0:3]\n" \
"s_add_u32 m0, s75, s95\n" \
"ds_read_b128 a[24:27], v213 offset:4352\n" \
"v_add_f32 v189, v189, v185\n" \
"v_add_f32 v188, v188, v184\n" \
"v_mfma_f32_16x16x32_bf16 v[0:3], a[36:39], a[4:7], v[0:3]\n" \
"s_waitcnt lgkmcnt(1)\n" \
"ds_read_b128 a[28:31], v213 offset:6400\n" \
"global_load_lds_dwordx4 v250, s[36:37] offset:0 sc1\n" \
"v_rcp_f32 v191, v191\n" \
"v_mfma_f32_16x16x32_bf16 v[4:7], a[40:43], a[0:3], v[4:7]\n" \
"s_sub_u32 s74, s74, 32768\n" \
"s_cselect_b32 s74, 131072, s74\n" \
"ds_read_b64_tr_b16 a[48:49], v214 offset:16896\n" \
"v_mfma_f32_16x16x32_bf16 v[4:7], a[44:47], a[4:7], v[4:7]\n" \
"ds_read_b64_tr_b16 a[50:51], v214 offset:18944\n" \
"v_rcp_f32 v190, v190\n" \
"v_mfma_f32_16x16x32_bf16 v[16:19], a[32:35], a[8:11], v[16:19]\n" \
"ds_read_b64_tr_b16 a[52:53], v214 offset:25088\n" \
"v_rcp_f32 v189, v189\n" \
"v_mfma_f32_16x16x32_bf16 v[16:19], a[36:39], a[12:15], v[16:19]\n" \
"ds_read_b64_tr_b16 a[54:55], v214 offset:27136\n" \
"global_load_lds_dwordx4 v250, s[36:37] offset:1024 sc1\n" \
"v_rcp_f32 v188, v188\n" \
"v_mul_f32 v172, v172, v191\n" \
"v_mfma_f32_16x16x32_bf16 v[20:23], a[40:43], a[8:11], v[20:23]\n" \
"ds_read_b64_tr_b16 a[56:57], v215 offset:16896\n" \
"v_mul_f32 v164, v164, v190\n" \
"v_mul_f32 v156, v156, v189\n" \
"v_mfma_f32_16x16x32_bf16 v[20:23], a[44:47], a[12:15], v[20:23]\n" \
"ds_read_b64_tr_b16 a[58:59], v215 offset:18944\n" \
"v_mul_f32 v148, v148, v188\n" \
"v_mfma_f32_16x16x32_bf16 v[32:35], a[32:35], a[16:19], v[32:35]\n" \
"ds_read_b64_tr_b16 a[60:61], v215 offset:25088\n" \
"v_mul_f32 v173, v173, v191\n" \
"v_mul_f32 v165, v165, v190\n" \
"v_mfma_f32_16x16x32_bf16 v[32:35], a[36:39], a[20:23], v[32:35]\n" \
"ds_read_b64_tr_b16 a[62:63], v215 offset:27136\n" \
"global_load_lds_dwordx4 v250, s[36:37] offset:2048 sc1\n" \
"v_mul_f32 v157, v157, v189\n" \
"v_mul_f32 v149, v149, v188\n" \
"v_mfma_f32_16x16x32_bf16 v[36:39], a[40:43], a[16:19], v[36:39]\n" \
"v_add_u32 v213, s74, v211\n" \
"v_add_u32 v214, s74, v212\n" \
"v_mul_f32 v174, v174, v191\n" \
"v_mul_f32 v166, v166, v190\n" \
"v_mfma_f32_16x16x32_bf16 v[36:39], a[44:47], a[20:23], v[36:39]\n" \
"s_waitcnt lgkmcnt(3)\n" \
"v_xor_b32 v215, 32, v214\n" \
"v_mul_f32 v158, v158, v189\n" \
"v_mfma_f32_16x16x32_bf16 v[48:51], a[32:35], a[24:27], v[48:51]\n" \
"v_mul_f32 v150, v150, v188\n" \
"v_mul_f32 v175, v175, v191\n" \
"v_mfma_f32_16x16x32_bf16 v[48:51], a[36:39], a[28:31], v[48:51]\n" \
"global_load_lds_dwordx4 v250, s[36:37] offset:3072 sc1\n" \
"v_mul_f32 v167, v167, v190\n" \
"v_mul_f32 v159, v159, v189\n" \
"v_mfma_f32_16x16x32_bf16 v[52:55], a[40:43], a[24:27], v[52:55]\n" \
"s_waitcnt vmcnt(20) lgkmcnt(0)\n" \
"v_mul_f32 v151, v151, v188\n" \
"v_mul_f32 v176, v176, v191\n" \
"s_add_u32 s36, s36, 0x1000\n" \
"v_mfma_f32_16x16x32_bf16 v[52:55], a[44:47], a[28:31], v[52:55]\n" \
"s_barrier\n" \
"v_mul_f32 v168, v168, v190\n" \
"v_mul_f32 v160, v160, v189\n" \
"v_mfma_f32_16x16x32_bf16 v[8:11], a[48:51], a[0:3], v[8:11]\n" \
"s_add_u32 m0, m0, 0x5000\n" \
"ds_read_b64_tr_b16 a[32:33], v214 offset:16384\n" \
"v_mul_f32 v152, v152, v188\n" \
"v_mfma_f32_16x16x32_bf16 v[8:11], a[52:55], a[4:7], v[8:11]\n" \
"ds_read_b64_tr_b16 a[34:35], v214 offset:18432\n" \
"global_load_lds_dwordx4 v216, s[38:39] offset:-4096\n" \
"v_mul_f32 v177, v177, v191\n" \
"v_mul_f32 v169, v169, v190\n" \
"v_mfma_f32_16x16x32_bf16 v[12:15], a[56:59], a[0:3], v[12:15]\n" \
"ds_read_b64_tr_b16 a[36:37], v214 offset:24576\n" \
"v_mul_f32 v161, v161, v189\n" \
"v_mul_f32 v153, v153, v188\n" \
"v_mfma_f32_16x16x32_bf16 v[12:15], a[60:63], a[4:7], v[12:15]\n" \
"ds_read_b128 a[0:3], v213 offset:0\n" \
"v_mul_f32 v178, v178, v191\n" \
"v_mul_f32 v170, v170, v190\n" \
"v_mfma_f32_16x16x32_bf16 v[24:27], a[48:51], a[8:11], v[24:27]\n" \
"ds_read_b128 a[4:7], v213 offset:2048\n" \
"v_mul_f32 v162, v162, v189\n" \
"v_mfma_f32_16x16x32_bf16 v[24:27], a[52:55], a[12:15], v[24:27]\n" \
"ds_read_b64_tr_b16 a[38:39], v214 offset:26624\n" \
"global_load_lds_dwordx4 v217, s[38:39] offset:-3072\n" \
"v_mul_f32 v154, v154, v188\n" \
"v_mul_f32 v179, v179, v191\n" \
"v_mfma_f32_16x16x32_bf16 v[28:31], a[56:59], a[8:11], v[28:31]\n" \
"ds_read_b64_tr_b16 a[40:41], v215 offset:16384\n" \
"v_mul_f32 v171, v171, v190\n" \
"v_mul_f32 v163, v163, v189\n" \
"v_mfma_f32_16x16x32_bf16 v[28:31], a[60:63], a[12:15], v[28:31]\n" \
"ds_read_b128 a[8:11], v213 offset:256\n" \
"v_mul_f32 v155, v155, v188\n" \
"v_cvt_pk_bf16_f32 v172, v172, v173\n" \
"v_mfma_f32_16x16x32_bf16 v[40:43], a[48:51], a[16:19], v[40:43]\n" \
"ds_read_b128 a[12:15], v213 offset:2304\n" \
"v_cvt_pk_bf16_f32 v173, v174, v175\n" \
"v_mfma_f32_16x16x32_bf16 v[40:43], a[52:55], a[20:23], v[40:43]\n" \
"ds_read_b64_tr_b16 a[42:43], v215 offset:18432\n" \
"global_load_lds_dwordx4 v218, s[38:39] offset:-2048\n" \
"v_cvt_pk_bf16_f32 v174, v176, v177\n" \
"v_cvt_pk_bf16_f32 v175, v178, v179\n" \
"v_mfma_f32_16x16x32_bf16 v[44:47], a[56:59], a[16:19], v[44:47]\n" \
"ds_read_b64_tr_b16 a[44:45], v215 offset:24576\n" \
"v_cvt_pk_bf16_f32 v164, v164, v165\n" \
"v_cvt_pk_bf16_f32 v165, v166, v167\n" \
"v_mfma_f32_16x16x32_bf16 v[44:47], a[60:63], a[20:23], v[44:47]\n" \
"ds_read_b128 a[16:19], v213 offset:4096\n" \
"v_cvt_pk_bf16_f32 v166, v168, v169\n" \
"v_cvt_pk_bf16_f32 v167, v170, v171\n" \
"v_mfma_f32_16x16x32_bf16 v[56:59], a[48:51], a[24:27], v[56:59]\n" \
"ds_read_b128 a[20:23], v213 offset:6144\n" \
"global_load_lds_dwordx4 v219, s[38:39] offset:-1024\n" \
"v_mfma_f32_16x16x32_bf16 v[56:59], a[52:55], a[28:31], v[56:59]\n" \
"s_add_u32 s38, s38, 0x20000\n" \
"s_addc_u32 s39, s39, 0\n" \
"ds_read_b64_tr_b16 a[46:47], v215 offset:26624\n" \
"v_mfma_f32_16x16x32_bf16 v[60:63], a[56:59], a[24:27], v[60:63]\n" \
"s_sub_u32 s75, s75, 32768\n" \
"s_cselect_b32 s75, 131072, s75\n" \
"v_cvt_pk_bf16_f32 v156, v156, v157\n" \
"v_mfma_f32_16x16x32_bf16 v[60:63], a[60:63], a[28:31], v[60:63]\n" \
"s_waitcnt lgkmcnt(3)\n" \
"v_cvt_pk_bf16_f32 v157, v158, v159\n" \
"v_cvt_pk_bf16_f32 v158, v160, v161\n" \
"v_mfma_f32_16x16x32_bf16 v[0:3], a[32:35], a[0:3], v[0:3]\n" \
"s_add_u32 m0, s75, s95\n" \
"ds_read_b128 a[24:27], v213 offset:4352\n" \
"v_cvt_pk_bf16_f32 v159, v162, v163\n" \
"v_mfma_f32_16x16x32_bf16 v[0:3], a[36:39], a[4:7], v[0:3]\n" \
"s_waitcnt lgkmcnt(1)\n" \
"ds_read_b128 a[28:31], v213 offset:6400\n" \
"global_load_lds_dwordx4 v250, s[36:37] offset:0 sc1\n" \
"v_mfma_f32_16x16x32_bf16 v[4:7], a[40:43], a[0:3], v[4:7]\n" \
"s_sub_u32 s74, s74, 32768\n" \
"s_cselect_b32 s74, 131072, s74\n" \
"ds_read_b64_tr_b16 a[48:49], v214 offset:16896\n" \
"v_mfma_f32_16x16x32_bf16 v[4:7], a[44:47], a[4:7], v[4:7]\n" \
"ds_read_b64_tr_b16 a[50:51], v214 offset:18944\n" \
"v_cvt_pk_bf16_f32 v148, v148, v149\n" \
"v_mfma_f32_16x16x32_bf16 v[16:19], a[32:35], a[8:11], v[16:19]\n" \
"ds_read_b64_tr_b16 a[52:53], v214 offset:25088\n" \
"v_cvt_pk_bf16_f32 v149, v150, v151\n" \
"v_mfma_f32_16x16x32_bf16 v[16:19], a[36:39], a[12:15], v[16:19]\n" \
"ds_read_b64_tr_b16 a[54:55], v214 offset:27136\n" \
"global_load_lds_dwordx4 v250, s[36:37] offset:1024 sc1\n" \
"v_mfma_f32_16x16x32_bf16 v[20:23], a[40:43], a[8:11], v[20:23]\n" \
"ds_read_b64_tr_b16 a[56:57], v215 offset:16896\n" \
"v_cvt_pk_bf16_f32 v150, v152, v153\n" \
"v_mfma_f32_16x16x32_bf16 v[20:23], a[44:47], a[12:15], v[20:23]\n" \
"ds_read_b64_tr_b16 a[58:59], v215 offset:18944\n" \
"v_cvt_pk_bf16_f32 v151, v154, v155\n" \
"v_mfma_f32_16x16x32_bf16 v[32:35], a[32:35], a[16:19], v[32:35]\n" \
"v_mfma_f32_16x16x32_bf16 v[136:139], v[74:77], v[172:175], 0\n" \
"v_mfma_f32_16x16x32_bf16 v[140:143], v[198:201], v[172:175], 0\n" \
"v_mfma_f32_16x16x32_bf16 v[128:131], v[72:75], v[164:167], 0\n" \
"v_mfma_f32_16x16x32_bf16 v[132:135], v[196:199], v[164:167], 0\n" \
"v_mfma_f32_16x16x32_bf16 v[120:123], v[70:73], v[156:159], 0\n" \
"v_mfma_f32_16x16x32_bf16 v[124:127], v[194:197], v[156:159], 0\n" \
"ds_read_b64_tr_b16 a[60:61], v215 offset:25088\n" \
"v_mfma_f32_16x16x32_bf16 v[32:35], a[36:39], a[20:23], v[32:35]\n" \
"v_mfma_f32_16x16x32_bf16 v[112:115], v[68:71], v[148:151], 0\n" \
"v_mfma_f32_16x16x32_bf16 v[116:119], v[192:195], v[148:151], 0\n" \
"ds_read_b64_tr_b16 a[62:63], v215 offset:27136\n" \
"global_load_lds_dwordx4 v250, s[36:37] offset:2048 sc1\n" \
"v_mfma_f32_16x16x32_bf16 v[36:39], a[40:43], a[16:19], v[36:39]\n" \
"v_add_u32 v213, s74, v211\n" \
"v_add_u32 v214, s74, v212\n" \
"v_mfma_f32_16x16x32_bf16 v[36:39], a[44:47], a[20:23], v[36:39]\n" \
"s_waitcnt lgkmcnt(3)\n" \
"v_xor_b32 v215, 32, v214\n" \
"v_cvt_pk_bf16_f32 v136, v136, v137\n" \
"v_cvt_pk_bf16_f32 v137, v138, v139\n" \
"v_mfma_f32_16x16x32_bf16 v[48:51], a[32:35], a[24:27], v[48:51]\n" \
"v_cvt_pk_bf16_f32 v138, v140, v141\n" \
"v_cvt_pk_bf16_f32 v139, v142, v143\n" \
"v_mfma_f32_16x16x32_bf16 v[48:51], a[36:39], a[28:31], v[48:51]\n" \
"global_load_lds_dwordx4 v250, s[36:37] offset:3072 sc1\n" \
"v_cndmask_b32 v140, v136, v138, s[92:93]\n" \
"v_mfma_f32_16x16x32_bf16 v[52:55], a[40:43], a[24:27], v[52:55]\n" \
"s_waitcnt vmcnt(20) lgkmcnt(0)\n" \
"v_cndmask_b32 v141, v137, v139, s[92:93]\n" \
"s_add_u32 s36, s36, 0x1000\n" \
"v_mfma_f32_16x16x32_bf16 v[52:55], a[44:47], a[28:31], v[52:55]\n" \
"s_barrier\n" \
"v_cndmask_b32 v142, v138, v136, s[92:93]\n" \
"v_mfma_f32_16x16x32_bf16 v[8:11], a[48:51], a[0:3], v[8:11]\n" \
"s_add_u32 m0, m0, 0x5000\n" \
"ds_read_b64_tr_b16 a[32:33], v214 offset:16384\n" \
"v_cndmask_b32 v143, v139, v137, s[92:93]\n" \
"v_mfma_f32_16x16x32_bf16 v[8:11], a[52:55], a[4:7], v[8:11]\n" \
"s_add_u32 s71, s87, 208896\n" \
"ds_read_b64_tr_b16 a[34:35], v214 offset:18432\n" \
"global_load_lds_dwordx4 v216, s[38:39] offset:-4096\n" \
"v_mfma_f32_16x16x32_bf16 v[12:15], a[56:59], a[0:3], v[12:15]\n" \
"ds_read_b64_tr_b16 a[36:37], v214 offset:24576\n" \
"buffer_store_dwordx4 v[140:143], v249, s[56:59], s71 offen sc1\n" \
"v_cvt_pk_bf16_f32 v128, v128, v129\n" \
"v_cvt_pk_bf16_f32 v129, v130, v131\n" \
"v_cvt_pk_bf16_f32 v130, v132, v133\n" \
"v_cvt_pk_bf16_f32 v131, v134, v135\n" \
"v_cndmask_b32 v132, v128, v130, s[92:93]\n" \
"v_mfma_f32_16x16x32_bf16 v[12:15], a[60:63], a[4:7], v[12:15]\n" \
"ds_read_b128 a[0:3], v213 offset:0\n" \
"v_cndmask_b32 v133, v129, v131, s[92:93]\n" \
"v_cndmask_b32 v134, v130, v128, s[92:93]\n" \
"v_mfma_f32_16x16x32_bf16 v[24:27], a[48:51], a[8:11], v[24:27]\n" \
"ds_read_b128 a[4:7], v213 offset:2048\n" \
"v_cndmask_b32 v135, v131, v129, s[92:93]\n" \
"v_mfma_f32_16x16x32_bf16 v[24:27], a[52:55], a[12:15], v[24:27]\n" \
"s_add_u32 s71, s87, 139264\n" \
"ds_read_b64_tr_b16 a[38:39], v214 offset:26624\n" \
"global_load_lds_dwordx4 v217, s[38:39] offset:-3072\n" \
"v_mfma_f32_16x16x32_bf16 v[28:31], a[56:59], a[8:11], v[28:31]\n" \
"ds_read_b64_tr_b16 a[40:41], v215 offset:16384\n" \
"buffer_store_dwordx4 v[132:135], v249, s[56:59], s71 offen sc1\n" \
"v_cvt_pk_bf16_f32 v120, v120, v121\n" \
"v_cvt_pk_bf16_f32 v121, v122, v123\n" \
"v_cvt_pk_bf16_f32 v122, v124, v125\n" \
"v_cvt_pk_bf16_f32 v123, v126, v127\n" \
"v_cndmask_b32 v124, v120, v122, s[92:93]\n" \
"v_mfma_f32_16x16x32_bf16 v[28:31], a[60:63], a[12:15], v[28:31]\n" \
"ds_read_b128 a[8:11], v213 offset:256\n" \
"v_cndmask_b32 v125, v121, v123, s[92:93]\n" \
"v_cndmask_b32 v126, v122, v120, s[92:93]\n" \
"v_mfma_f32_16x16x32_bf16 v[40:43], a[48:51], a[16:19], v[40:43]\n" \
"ds_read_b128 a[12:15], v213 offset:2304\n" \
"v_cndmask_b32 v127, v123, v121, s[92:93]\n" \
"v_mfma_f32_16x16x32_bf16 v[40:43], a[52:55], a[20:23], v[40:43]\n" \
"s_add_u32 s71, s87, 69632\n" \
"ds_read_b64_tr_b16 a[42:43], v215 offset:18432\n" \
"global_load_lds_dwordx4 v218, s[38:39] offset:-2048\n" \
"v_mfma_f32_16x16x32_bf16 v[44:47], a[56:59], a[16:19], v[44:47]\n" \
"ds_read_b64_tr_b16 a[44:45], v215 offset:24576\n" \
"buffer_store_dwordx4 v[124:127], v249, s[56:59], s71 offen sc1\n" \
"v_cvt_pk_bf16_f32 v112, v112, v113\n" \
"v_cvt_pk_bf16_f32 v113, v114, v115\n" \
"v_cvt_pk_bf16_f32 v114, v116, v117\n" \
"v_cvt_pk_bf16_f32 v115, v118, v119\n" \
"v_cndmask_b32 v116, v112, v114, s[92:93]\n" \
"v_mfma_f32_16x16x32_bf16 v[44:47], a[60:63], a[20:23], v[44:47]\n" \
"ds_read_b128 a[16:19], v213 offset:4096\n" \
"v_cndmask_b32 v117, v113, v115, s[92:93]\n" \
"v_cndmask_b32 v118, v114, v112, s[92:93]\n" \
"v_cndmask_b32 v119, v115, v113, s[92:93]\n" \
"v_mfma_f32_16x16x32_bf16 v[56:59], a[48:51], a[24:27], v[56:59]\n" \
"s_add_u32 s71, s87, 0\n" \
"ds_read_b128 a[20:23], v213 offset:6144\n" \
"global_load_lds_dwordx4 v219, s[38:39] offset:-1024\n" \
"v_mfma_f32_16x16x32_bf16 v[56:59], a[52:55], a[28:31], v[56:59]\n" \
"buffer_store_dwordx4 v[116:119], v249, s[56:59], s71 offen sc1\n" \
"s_add_u32 s38, s38, 0x20000\n" \
"s_addc_u32 s39, s39, 0\n" \
"ds_read_b64_tr_b16 a[46:47], v215 offset:26624\n" \
"v_mfma_f32_16x16x32_bf16 v[60:63], a[56:59], a[24:27], v[60:63]\n" \
"s_sub_u32 s75, s75, 32768\n" \
"s_cselect_b32 s75, 131072, s75\n" \
"v_mfma_f32_16x16x32_bf16 v[60:63], a[60:63], a[28:31], v[60:63]\n" \
"s_waitcnt lgkmcnt(3)\n" \
"v_mfma_f32_16x16x32_bf16 v[0:3], a[32:35], a[0:3], v[0:3]\n" \
"s_add_u32 m0, s75, s95\n" \
"ds_read_b128 a[24:27], v213 offset:4352\n" \
"s_and_b32 s70, %1, 1\n" \
"v_mfma_f32_16x16x32_bf16 v[0:3], a[36:39], a[4:7], v[0:3]\n" \
"s_waitcnt lgkmcnt(1)\n" \
"ds_read_b128 a[28:31], v213 offset:6400\n" \
"global_load_lds_dwordx4 v250, s[36:37] offset:0 sc1\n" \
"v_mfma_f32_16x16x32_bf16 v[4:7], a[40:43], a[0:3], v[4:7]\n" \
"s_sub_u32 s74, s74, 32768\n" \
"s_cselect_b32 s74, 131072, s74\n" \
"ds_read_b64_tr_b16 a[48:49], v214 offset:16896\n" \
"v_mfma_f32_16x16x32_bf16 v[4:7], a[44:47], a[4:7], v[4:7]\n" \
"s_lshl_b32 s70, s70, 3\n" \
"s_add_u32 s70, s70, s63\n" \
"ds_read_b64_tr_b16 a[50:51], v214 offset:18944\n" \
"v_mfma_f32_16x16x32_bf16 v[16:19], a[32:35], a[8:11], v[16:19]\n" \
"s_lshl_b32 s70, s70, 2\n" \
"s_add_u32 s70, s70, s82\n" \
"ds_read_b64_tr_b16 a[52:53], v214 offset:25088\n" \
"v_mfma_f32_16x16x32_bf16 v[16:19], a[36:39], a[12:15], v[16:19]\n" \
"ds_read_b64_tr_b16 a[54:55], v214 offset:27136\n" \
"global_load_lds_dwordx4 v250, s[36:37] offset:1024 sc1\n" \
"v_mfma_f32_16x16x32_bf16 v[20:23], a[40:43], a[8:11], v[20:23]\n" \
"ds_read_b64_tr_b16 a[56:57], v215 offset:16896\n" \
"v_mov_b32 v224, s60\n" \
"v_mfma_f32_16x16x32_bf16 v[20:23], a[44:47], a[12:15], v[20:23]\n" \
"ds_read_b64_tr_b16 a[58:59], v215 offset:18944\n" \
"v_lshlrev_b32 v225, 28, s61\n" \
"v_mfma_f32_16x16x32_bf16 v[32:35], a[32:35], a[16:19], v[32:35]\n" \
"ds_read_b64_tr_b16 a[60:61], v215 offset:25088\n" \
"v_mfma_f32_16x16x32_bf16 v[32:35], a[36:39], a[20:23], v[32:35]\n" \
"ds_read_b64_tr_b16 a[62:63], v215 offset:27136\n" \
"global_load_lds_dwordx4 v250, s[36:37] offset:2048 sc1\n" \
"v_mfma_f32_16x16x32_bf16 v[36:39], a[40:43], a[16:19], v[36:39]\n" \
"v_add_u32 v213, s74, v211\n" \
"v_add_u32 v214, s74, v212\n" \
"v_mfma_f32_16x16x32_bf16 v[36:39], a[44:47], a[20:23], v[36:39]\n" \
"s_waitcnt lgkmcnt(3)\n" \
"v_xor_b32 v215, 32, v214\n" \
"v_mfma_f32_16x16x32_bf16 v[48:51], a[32:35], a[24:27], v[48:51]\n" \
"v_mfma_f32_16x16x32_bf16 v[48:51], a[36:39], a[28:31], v[48:51]\n" \
"global_load_lds_dwordx4 v250, s[36:37] offset:3072 sc1\n" \
"v_mfma_f32_16x16x32_bf16 v[52:55], a[40:43], a[24:27], v[52:55]\n" \
"s_waitcnt vmcnt(24) lgkmcnt(0)\n" \
"s_add_u32 s36, s36, 0x1000\n" \
"v_mfma_f32_16x16x32_bf16 v[52:55], a[44:47], a[28:31], v[52:55]\n" \
"s_barrier\n" \
"v_mfma_f32_16x16x32_bf16 v[8:11], a[48:51], a[0:3], v[8:11]\n" \
"s_add_u32 m0, m0, 0x5000\n" \
"ds_read_b64_tr_b16 a[32:33], v214 offset:16384\n" \
"v_mfma_f32_16x16x32_bf16 v[8:11], a[52:55], a[4:7], v[8:11]\n" \
"ds_read_b64_tr_b16 a[34:35], v214 offset:18432\n" \
"global_load_lds_dwordx4 v216, s[38:39] offset:-4096\n" \
"v_mfma_f32_16x16x32_bf16 v[12:15], a[56:59], a[0:3], v[12:15]\n" \
"ds_read_b64_tr_b16 a[36:37], v214 offset:24576\n" \
"v_mfma_f32_16x16x32_bf16 v[12:15], a[60:63], a[4:7], v[12:15]\n" \
"ds_read_b128 a[0:3], v213 offset:0\n" \
"v_mfma_f32_16x16x32_bf16 v[24:27], a[48:51], a[8:11], v[24:27]\n" \
"ds_read_b128 a[4:7], v213 offset:2048\n" \
"v_mfma_f32_16x16x32_bf16 v[24:27], a[52:55], a[12:15], v[24:27]\n" \
"ds_read_b64_tr_b16 a[38:39], v214 offset:26624\n" \
"global_load_lds_dwordx4 v217, s[38:39] offset:-3072\n" \
"v_mfma_f32_16x16x32_bf16 v[28:31], a[56:59], a[8:11], v[28:31]\n" \
"ds_read_b64_tr_b16 a[40:41], v215 offset:16384\n" \
"v_mfma_f32_16x16x32_bf16 v[28:31], a[60:63], a[12:15], v[28:31]\n" \
"ds_read_b128 a[8:11], v213 offset:256\n" \
"v_mfma_f32_16x16x32_bf16 v[40:43], a[48:51], a[16:19], v[40:43]\n" \
"ds_read_b128 a[12:15], v213 offset:2304\n" \
"v_mfma_f32_16x16x32_bf16 v[40:43], a[52:55], a[20:23], v[40:43]\n" \
"ds_read_b64_tr_b16 a[42:43], v215 offset:18432\n" \
"global_load_lds_dwordx4 v218, s[38:39] offset:-2048\n" \
"v_mfma_f32_16x16x32_bf16 v[44:47], a[56:59], a[16:19], v[44:47]\n" \
"ds_read_b64_tr_b16 a[44:45], v215 offset:24576\n" \
"v_mfma_f32_16x16x32_bf16 v[44:47], a[60:63], a[20:23], v[44:47]\n" \
"ds_read_b128 a[16:19], v213 offset:4096\n" \
"v_mfma_f32_16x16x32_bf16 v[56:59], a[48:51], a[24:27], v[56:59]\n" \
"ds_read_b128 a[20:23], v213 offset:6144\n" \
"global_load_lds_dwordx4 v219, s[38:39] offset:-1024\n" \
"v_mfma_f32_16x16x32_bf16 v[56:59], a[52:55], a[28:31], v[56:59]\n" \
"s_add_u32 s38, s38, 0x20000\n" \
"s_addc_u32 s39, s39, 0\n" \
"ds_read_b64_tr_b16 a[46:47], v215 offset:26624\n" \
"v_mfma_f32_16x16x32_bf16 v[60:63], a[56:59], a[24:27], v[60:63]\n" \
"s_sub_u32 s75, s75, 32768\n" \
"s_cselect_b32 s75, 131072, s75\n" \
"v_mfma_f32_16x16x32_bf16 v[60:63], a[60:63], a[28:31], v[60:63]\n" \
"s_waitcnt lgkmcnt(3)\n" \
"v_mfma_f32_16x16x32_bf16 v[0:3], a[32:35], a[0:3], v[0:3]\n" \
"ds_read_b128 a[24:27], v213 offset:4352\n" \
"s_add_u32 m0, s75, s95\n" \
"v_mfma_f32_16x16x32_bf16 v[0:3], a[36:39], a[4:7], v[0:3]\n" \
"s_waitcnt lgkmcnt(1)\n" \
"ds_read_b128 a[28:31], v213 offset:6400\n" \
"global_load_lds_dwordx4 v250, s[36:37] offset:0 sc1\n" \
"v_mfma_f32_16x16x32_bf16 v[4:7], a[40:43], a[0:3], v[4:7]\n" \
"s_sub_u32 s74, s74, 32768\n" \
"s_cselect_b32 s74, 131072, s74\n" \
"ds_read_b64_tr_b16 a[48:49], v214 offset:16896\n" \
"v_mfma_f32_16x16x32_bf16 v[4:7], a[44:47], a[4:7], v[4:7]\n" \
"ds_read_b64_tr_b16 a[50:51], v214 offset:18944\n" \
"v_mfma_f32_16x16x32_bf16 v[16:19], a[32:35], a[8:11], v[16:19]\n" \
"ds_read_b64_tr_b16 a[52:53], v214 offset:25088\n" \
"v_mfma_f32_16x16x32_bf16 v[16:19], a[36:39], a[12:15], v[16:19]\n" \
"ds_read_b64_tr_b16 a[54:55], v214 offset:27136\n" \
"global_load_lds_dwordx4 v250, s[36:37] offset:1024 sc1\n" \
"v_mfma_f32_16x16x32_bf16 v[20:23], a[40:43], a[8:11], v[20:23]\n" \
"ds_read_b64_tr_b16 a[56:57], v215 offset:16896\n" \
"v_mfma_f32_16x16x32_bf16 v[20:23], a[44:47], a[12:15], v[20:23]\n" \
"ds_read_b64_tr_b16 a[58:59], v215 offset:18944\n" \
"v_mfma_f32_16x16x32_bf16 v[32:35], a[32:35], a[16:19], v[32:35]\n" \
"ds_read_b64_tr_b16 a[60:61], v215 offset:25088\n" \
"v_mfma_f32_16x16x32_bf16 v[32:35], a[36:39], a[20:23], v[32:35]\n" \
"ds_read_b64_tr_b16 a[62:63], v215 offset:27136\n" \
"global_load_lds_dwordx4 v250, s[36:37] offset:2048 sc1\n" \
"v_mfma_f32_16x16x32_bf16 v[36:39], a[40:43], a[16:19], v[36:39]\n" \
"v_add_u32 v213, s74, v211\n" \
"v_add_u32 v214, s74, v212\n" \
"v_mfma_f32_16x16x32_bf16 v[36:39], a[44:47], a[20:23], v[36:39]\n" \
"s_waitcnt lgkmcnt(3)\n" \
"v_xor_b32 v215, 32, v214\n" \
"v_mfma_f32_16x16x32_bf16 v[48:51], a[32:35], a[24:27], v[48:51]\n" \
"v_mfma_f32_16x16x32_bf16 v[48:51], a[36:39], a[28:31], v[48:51]\n" \
"global_load_lds_dwordx4 v250, s[36:37] offset:3072 sc1\n" \
"v_mfma_f32_16x16x32_bf16 v[52:55], a[40:43], a[24:27], v[52:55]\n" \
"s_waitcnt vmcnt(12) lgkmcnt(0)\n" \
"s_add_u32 s36, s36, 0x1000\n" \
"v_mfma_f32_16x16x32_bf16 v[52:55], a[44:47], a[28:31], v[52:55]\n" \
"s_barrier\n" \
"v_mfma_f32_16x16x32_bf16 v[8:11], a[48:51], a[0:3], v[8:11]\n" \
"s_add_u32 m0, m0, 0x5000\n" \
"ds_read_b64_tr_b16 a[32:33], v214 offset:16384\n" \
"buffer_store_dword v224, v225, s[56:59], s70 offen sc1\n" \
"v_mfma_f32_16x16x32_bf16 v[8:11], a[52:55], a[4:7], v[8:11]\n" \
"ds_read_b64_tr_b16 a[34:35], v214 offset:18432\n" \
"global_load_lds_dwordx4 v216, s[38:39] offset:-4096\n" \
"v_mfma_f32_16x16x32_bf16 v[12:15], a[56:59], a[0:3], v[12:15]\n" \
"ds_read_b64_tr_b16 a[36:37], v214 offset:24576\n" \
"v_mfma_f32_16x16x32_bf16 v[12:15], a[60:63], a[4:7], v[12:15]\n" \
"ds_read_b128 a[0:3], v213 offset:0\n" \
"v_mfma_f32_16x16x32_bf16 v[24:27], a[48:51], a[8:11], v[24:27]\n" \
"ds_read_b128 a[4:7], v213 offset:2048\n" \
"v_mfma_f32_16x16x32_bf16 v[24:27], a[52:55], a[12:15], v[24:27]\n" \
"ds_read_b64_tr_b16 a[38:39], v214 offset:26624\n" \
"global_load_lds_dwordx4 v217, s[38:39] offset:-3072\n" \
"v_mfma_f32_16x16x32_bf16 v[28:31], a[56:59], a[8:11], v[28:31]\n" \
"ds_read_b64_tr_b16 a[40:41], v215 offset:16384\n" \
"v_mfma_f32_16x16x32_bf16 v[28:31], a[60:63], a[12:15], v[28:31]\n" \
"ds_read_b128 a[8:11], v213 offset:256\n" \
"v_mfma_f32_16x16x32_bf16 v[40:43], a[48:51], a[16:19], v[40:43]\n" \
"ds_read_b128 a[12:15], v213 offset:2304\n" \
"v_mfma_f32_16x16x32_bf16 v[40:43], a[52:55], a[20:23], v[40:43]\n" \
"ds_read_b64_tr_b16 a[42:43], v215 offset:18432\n" \
"global_load_lds_dwordx4 v218, s[38:39] offset:-2048\n" \
"v_mfma_f32_16x16x32_bf16 v[44:47], a[56:59], a[16:19], v[44:47]\n" \
"ds_read_b64_tr_b16 a[44:45], v215 offset:24576\n" \
"v_mfma_f32_16x16x32_bf16 v[44:47], a[60:63], a[20:23], v[44:47]\n" \
"ds_read_b128 a[16:19], v213 offset:4096\n" \
"v_mfma_f32_16x16x32_bf16 v[56:59], a[48:51], a[24:27], v[56:59]\n" \
"ds_read_b128 a[20:23], v213 offset:6144\n" \
"global_load_lds_dwordx4 v219, s[38:39] offset:-1024\n" \
"v_mfma_f32_16x16x32_bf16 v[56:59], a[52:55], a[28:31], v[56:59]\n" \
"s_add_u32 s38, s38, 0x20000\n" \
"s_addc_u32 s39, s39, 0\n" \
"ds_read_b64_tr_b16 a[46:47], v215 offset:26624\n" \
"v_mfma_f32_16x16x32_bf16 v[60:63], a[56:59], a[24:27], v[60:63]\n" \
"s_sub_u32 s75, s75, 32768\n" \
"s_cselect_b32 s75, 131072, s75\n" \
"v_mfma_f32_16x16x32_bf16 v[60:63], a[60:63], a[28:31], v[60:63]\n" \
"Lpg12185_%=:\n" \
"s_cbranch_execz Lpg13337_%=\n" \
"s_waitcnt lgkmcnt(3)\n" \
"s_cmp_eq_u32 s78, 0\n" \
"s_cbranch_scc0 Lrole1_%=\n" \
"s_mov_b32 s72, 24\n" \
"s_call_b64 s[68:69], Lo0_%=\n" \
"buffer_load_dword v220, off, s[56:59], s79 sc1\n" \
"s_mov_b32 s72, 2\n" \
"s_call_b64 s[68:69], Lo0_%=\n" \
"v_mfma_f32_16x16x32_bf16 v[0:3], a[32:35], a[0:3], v[0:3]\n" \
"ds_read_b128 a[24:27], v213 offset:4352\n" \
"v_mfma_f32_16x16x32_bf16 v[0:3], a[36:39], a[4:7], v[0:3]\n" \
"s_waitcnt lgkmcnt(1)\n" \
"ds_read_b128 a[28:31], v213 offset:6400\n" \
"v_mfma_f32_16x16x32_bf16 v[4:7], a[40:43], a[0:3], v[4:7]\n" \
"s_sub_u32 s74, s74, 32768\n" \
"s_cselect_b32 s74, 131072, s74\n" \
"ds_read_b64_tr_b16 a[48:49], v214 offset:16896\n" \
"v_mfma_f32_16x16x32_bf16 v[4:7], a[44:47], a[4:7], v[4:7]\n" \
"ds_read_b64_tr_b16 a[50:51], v214 offset:18944\n" \
"v_mfma_f32_16x16x32_bf16 v[16:19], a[32:35], a[8:11], v[16:19]\n" \
"ds_read_b64_tr_b16 a[52:53], v214 offset:25088\n" \
"v_mfma_f32_16x16x32_bf16 v[16:19], a[36:39], a[12:15], v[16:19]\n" \
"ds_read_b64_tr_b16 a[54:55], v214 offset:27136\n" \
"v_mfma_f32_16x16x32_bf16 v[20:23], a[40:43], a[8:11], v[20:23]\n" \
"ds_read_b64_tr_b16 a[56:57], v215 offset:16896\n" \
"v_mfma_f32_16x16x32_bf16 v[20:23], a[44:47], a[12:15], v[20:23]\n" \
"ds_read_b64_tr_b16 a[58:59], v215 offset:18944\n" \
"v_mfma_f32_16x16x32_bf16 v[32:35], a[32:35], a[16:19], v[32:35]\n" \
"ds_read_b64_tr_b16 a[60:61], v215 offset:25088\n" \
"v_mfma_f32_16x16x32_bf16 v[32:35], a[36:39], a[20:23], v[32:35]\n" \
"ds_read_b64_tr_b16 a[62:63], v215 offset:27136\n" \
"v_mfma_f32_16x16x32_bf16 v[36:39], a[40:43], a[16:19], v[36:39]\n" \
"v_add_u32 v213, s74, v211\n" \
"v_add_u32 v214, s74, v212\n" \
"v_mfma_f32_16x16x32_bf16 v[36:39], a[44:47], a[20:23], v[36:39]\n" \
"s_waitcnt lgkmcnt(3)\n" \
"v_xor_b32 v215, 32, v214\n" \
"v_mfma_f32_16x16x32_bf16 v[48:51], a[32:35], a[24:27], v[48:51]\n" \
"v_mfma_f32_16x16x32_bf16 v[48:51], a[36:39], a[28:31], v[48:51]\n" \
"v_mfma_f32_16x16x32_bf16 v[52:55], a[40:43], a[24:27], v[52:55]\n" \
"s_waitcnt vmcnt(17) lgkmcnt(0)\n" \
"v_mfma_f32_16x16x32_bf16 v[52:55], a[44:47], a[28:31], v[52:55]\n" \
"s_barrier\n" \
"v_mfma_f32_16x16x32_bf16 v[8:11], a[48:51], a[0:3], v[8:11]\n" \
"s_lshl_b32 s83, s95, 1\n" \
"s_add_u32 s83, s83, s74\n" \
"ds_read_b64_tr_b16 a[32:33], v214 offset:16384\n" \
"v_mfma_f32_16x16x32_bf16 v[8:11], a[52:55], a[4:7], v[8:11]\n" \
"s_waitcnt vmcnt(16)\n" \
"ds_read_b64_tr_b16 a[34:35], v214 offset:18432\n" \
"v_cmp_ne_u32 vcc, s60, v220\n" \
"v_mfma_f32_16x16x32_bf16 v[12:15], a[56:59], a[0:3], v[12:15]\n" \
"ds_read_b64_tr_b16 a[36:37], v214 offset:24576\n" \
"v_mfma_f32_16x16x32_bf16 v[12:15], a[60:63], a[4:7], v[12:15]\n" \
"s_and_b32 vcc_hi, vcc_lo, 1\n" \
"s_cbranch_scc1 Lpl4_%=\n" \
"Lpr4_%=:\n" \
"ds_read_b128 a[0:3], v213 offset:0\n" \
"v_mfma_f32_16x16x32_bf16 v[24:27], a[48:51], a[8:11], v[24:27]\n" \
"ds_read_b128 a[4:7], v213 offset:2048\n" \
"v_mfma_f32_16x16x32_bf16 v[24:27], a[52:55], a[12:15], v[24:27]\n" \
"s_add_u32 s71, s76, 0\n" \
"ds_read_b64_tr_b16 a[38:39], v214 offset:26624\n" \
"buffer_load_dwordx4 v[176:179], v248, s[56:59], s71 offen offset:0 sc1\n" \
"v_mfma_f32_16x16x32_bf16 v[28:31], a[56:59], a[8:11], v[28:31]\n" \
"ds_read_b64_tr_b16 a[40:41], v215 offset:16384\n" \
"v_mfma_f32_16x16x32_bf16 v[28:31], a[60:63], a[12:15], v[28:31]\n" \
"buffer_load_dwordx4 v[180:183], v248, s[56:59], s71 offen offset:1024 sc1\n" \
"ds_read_b128 a[8:11], v213 offset:256\n" \
"v_mfma_f32_16x16x32_bf16 v[40:43], a[48:51], a[16:19], v[40:43]\n" \
"ds_read_b128 a[12:15], v213 offset:2304\n" \
"v_mfma_f32_16x16x32_bf16 v[40:43], a[52:55], a[20:23], v[40:43]\n" \
"ds_read_b64_tr_b16 a[42:43], v215 offset:18432\n" \
"v_mfma_f32_16x16x32_bf16 v[44:47], a[56:59], a[16:19], v[44:47]\n" \
"ds_read_b64_tr_b16 a[44:45], v215 offset:24576\n" \
"v_mfma_f32_16x16x32_bf16 v[44:47], a[60:63], a[20:23], v[44:47]\n" \
"ds_read_b128 a[16:19], v213 offset:4096\n" \
"v_mfma_f32_16x16x32_bf16 v[56:59], a[48:51], a[24:27], v[56:59]\n" \
"ds_read_b128 a[20:23], v213 offset:6144\n" \
"v_mfma_f32_16x16x32_bf16 v[56:59], a[52:55], a[28:31], v[56:59]\n" \
"ds_read_b64_tr_b16 a[46:47], v215 offset:26624\n" \
"v_mfma_f32_16x16x32_bf16 v[60:63], a[56:59], a[24:27], v[60:63]\n" \
"buffer_load_dwordx4 v[184:187], v248, s[56:59], s71 offen offset:2048 sc1\n" \
"v_mfma_f32_16x16x32_bf16 v[60:63], a[60:63], a[28:31], v[60:63]\n" \
"s_waitcnt lgkmcnt(3)\n" \
"buffer_load_dwordx4 v[188:191], v248, s[56:59], s71 offen offset:3072 sc1\n" \
"v_mfma_f32_16x16x32_bf16 v[0:3], a[32:35], a[0:3], v[0:3]\n" \
"s_add_u32 s71, s76, 4096\n" \
"ds_read_b128 a[24:27], v213 offset:4352\n" \
"buffer_load_dwordx4 v[192:195], v248, s[56:59], s71 offen offset:0 sc1\n" \
"v_mfma_f32_16x16x32_bf16 v[0:3], a[36:39], a[4:7], v[0:3]\n" \
"s_waitcnt lgkmcnt(1)\n" \
"ds_read_b128 a[28:31], v213 offset:6400\n" \
"v_lshrrev_b32 v132, 4, v248\n" \
"v_mfma_f32_16x16x32_bf16 v[4:7], a[40:43], a[0:3], v[4:7]\n" \
"s_sub_u32 s74, s74, 32768\n" \
"s_cselect_b32 s74, 131072, s74\n" \
"ds_read_b64_tr_b16 a[48:49], v214 offset:16896\n" \
"v_mfma_f32_16x16x32_bf16 v[4:7], a[44:47], a[4:7], v[4:7]\n" \
"ds_read_b64_tr_b16 a[50:51], v214 offset:18944\n" \
"v_and_b32 v133, 15, v132\n" \
"v_mfma_f32_16x16x32_bf16 v[16:19], a[32:35], a[8:11], v[16:19]\n" \
"ds_read_b64_tr_b16 a[52:53], v214 offset:25088\n" \
"v_mul_u32_u24 v128, 144, v133\n" \
"v_mfma_f32_16x16x32_bf16 v[16:19], a[36:39], a[12:15], v[16:19]\n" \
"ds_read_b64_tr_b16 a[54:55], v214 offset:27136\n" \
"v_lshrrev_b32 v133, 4, v132\n" \
"v_mfma_f32_16x16x32_bf16 v[20:23], a[40:43], a[8:11], v[20:23]\n" \
"ds_read_b64_tr_b16 a[56:57], v215 offset:16896\n" \
"v_lshl_add_u32 v128, v133, 3, v128\n" \
"v_mfma_f32_16x16x32_bf16 v[20:23], a[44:47], a[12:15], v[20:23]\n" \
"ds_read_b64_tr_b16 a[58:59], v215 offset:18944\n" \
"v_add_u32 v128, s83, v128\n" \
"v_mfma_f32_16x16x32_bf16 v[32:35], a[32:35], a[16:19], v[32:35]\n" \
"ds_read_b64_tr_b16 a[60:61], v215 offset:25088\n" \
"v_lshrrev_b32 v133, 3, v132\n" \
"v_mfma_f32_16x16x32_bf16 v[32:35], a[36:39], a[20:23], v[32:35]\n" \
"ds_read_b64_tr_b16 a[62:63], v215 offset:27136\n" \
"v_and_b32 v134, 7, v132\n" \
"v_mfma_f32_16x16x32_bf16 v[36:39], a[40:43], a[16:19], v[36:39]\n" \
"v_add_u32 v213, s74, v211\n" \
"v_add_u32 v214, s74, v212\n" \
"v_mfma_f32_16x16x32_bf16 v[36:39], a[44:47], a[20:23], v[36:39]\n" \
"s_waitcnt lgkmcnt(3)\n" \
"v_xor_b32 v215, 32, v214\n" \
"v_mul_u32_u24 v129, 144, v133\n" \
"v_mfma_f32_16x16x32_bf16 v[48:51], a[32:35], a[24:27], v[48:51]\n" \
"buffer_load_dwordx4 v[196:199], v248, s[56:59], s71 offen offset:1024 sc1\n" \
"v_mfma_f32_16x16x32_bf16 v[48:51], a[36:39], a[28:31], v[48:51]\n" \
"buffer_load_dwordx4 v[200:203], v248, s[56:59], s71 offen offset:2048 sc1\n" \
"v_mfma_f32_16x16x32_bf16 v[52:55], a[40:43], a[24:27], v[52:55]\n" \
"s_waitcnt vmcnt(7) lgkmcnt(0)\n" \
"buffer_load_dwordx4 v[204:207], v248, s[56:59], s71 offen offset:3072 sc1\n" \
"v_mfma_f32_16x16x32_bf16 v[52:55], a[44:47], a[28:31], v[52:55]\n" \
"s_barrier\n" \
"v_mfma_f32_16x16x32_bf16 v[8:11], a[48:51], a[0:3], v[8:11]\n" \
"s_mov_b32 s70, 0x20000000\n" \
"ds_read_b128 a[192:195], v213 offset:0\n" \
"ds_read_b64_tr_b16 a[128:129], v214 offset:16384\n" \
"v_mfma_f32_16x16x32_bf16 v[8:11], a[52:55], a[4:7], v[8:11]\n" \
"s_sub_u32 s74, s74, 32768\n" \
"s_cselect_b32 s74, 131072, s74\n" \
"ds_read_b64_tr_b16 a[130:131], v214 offset:18432\n" \
"v_mfma_f32_16x16x32_bf16 v[12:15], a[56:59], a[0:3], v[12:15]\n" \
"s_lshr_b32 s72, %1, 1\n" \
"ds_read_b128 a[196:199], v213 offset:2048\n" \
"ds_read_b64_tr_b16 a[132:133], v214 offset:24576\n" \
"v_mfma_f32_16x16x32_bf16 v[12:15], a[60:63], a[4:7], v[12:15]\n" \
"ds_read_b64_tr_b16 a[134:135], v214 offset:26624\n" \
"v_add_u32 v226, s74, v211\n" \
"v_mfma_f32_16x16x32_bf16 v[24:27], a[48:51], a[8:11], v[24:27]\n" \
"ds_read_b128 a[200:203], v213 offset:256\n" \
"ds_read_b64_tr_b16 a[136:137], v215 offset:16384\n" \
"v_mfma_f32_16x16x32_bf16 v[24:27], a[52:55], a[12:15], v[24:27]\n" \
"ds_read_b64_tr_b16 a[138:139], v215 offset:18432\n" \
"v_add_u32 v227, s74, v212\n" \
"v_mfma_f32_16x16x32_bf16 v[28:31], a[56:59], a[8:11], v[28:31]\n" \
"ds_read_b128 a[204:207], v213 offset:2304\n" \
"ds_read_b64_tr_b16 a[140:141], v215 offset:24576\n" \
"v_mfma_f32_16x16x32_bf16 v[28:31], a[60:63], a[12:15], v[28:31]\n" \
"ds_read_b64_tr_b16 a[142:143], v215 offset:26624\n" \
"v_xor_b32 v228, 32, v227\n" \
"v_mfma_f32_16x16x32_bf16 v[40:43], a[48:51], a[16:19], v[40:43]\n" \
"s_cmp_eq_u32 s72, 99\n" \
"s_cselect_b32 s70, 65593344, s70\n" \
"ds_read_b64_tr_b16 a[144:145], v214 offset:16896\n" \
"v_mfma_f32_16x16x32_bf16 v[40:43], a[52:55], a[20:23], v[40:43]\n" \
"buffer_store_dword v224, v225, s[56:59], s70 offen sc1\n" \
"ds_read_b64_tr_b16 a[146:147], v214 offset:18944\n" \
"v_mfma_f32_16x16x32_bf16 v[44:47], a[56:59], a[16:19], v[44:47]\n" \
"ds_read_b64_tr_b16 a[148:149], v214 offset:25088\n" \
"v_lshl_add_u32 v129, v134, 4, v129\n" \
"v_mfma_f32_16x16x32_bf16 v[44:47], a[60:63], a[20:23], v[44:47]\n" \
"ds_read_b64_tr_b16 a[150:151], v214 offset:27136\n" \
"v_add_u32 v129, s83, v129\n" \
"v_mfma_f32_16x16x32_bf16 v[56:59], a[48:51], a[24:27], v[56:59]\n" \
"ds_read_b64_tr_b16 a[152:153], v215 offset:16896\n" \
"v_lshlrev_b32 v134, 4, v134\n" \
"v_mfma_f32_16x16x32_bf16 v[56:59], a[52:55], a[28:31], v[56:59]\n" \
"ds_read_b64_tr_b16 a[154:155], v215 offset:18944\n" \
"v_lshl_add_u32 v130, v133, 12, v134\n" \
"v_mfma_f32_16x16x32_bf16 v[60:63], a[56:59], a[24:27], v[60:63]\n" \
"s_waitcnt vmcnt(1)\n" \
"ds_read_b64_tr_b16 a[156:157], v215 offset:25088\n" \
"v_lshlrev_b32 v64, 16, v176\n" \
"v_and_b32 v65, 0xffff0000, v176\n" \
"v_lshlrev_b32 v66, 16, v177\n" \
"v_and_b32 v67, 0xffff0000, v177\n" \
"v_lshlrev_b32 v68, 16, v178\n" \
"v_mfma_f32_16x16x32_bf16 v[60:63], a[60:63], a[28:31], v[60:63]\n" \
"s_waitcnt lgkmcnt(4)\n" \
"ds_read_b64_tr_b16 a[158:159], v215 offset:27136\n" \
"v_and_b32 v69, 0xffff0000, v178\n" \
"v_lshlrev_b32 v70, 16, v179\n" \
"v_and_b32 v71, 0xffff0000, v179\n" \
"v_lshlrev_b32 v72, 16, v180\n" \
"v_and_b32 v73, 0xffff0000, v180\n" \
"v_lshlrev_b32 v74, 16, v181\n" \
"v_mfma_f32_16x16x32_bf16 v[0:3], a[128:131], a[192:195], v[0:3]\n" \
"ds_read_b128 a[224:227], v226 offset:0\n" \
"ds_read_b64_tr_b16 a[160:161], v227 offset:16384\n" \
"v_and_b32 v75, 0xffff0000, v181\n" \
"v_lshlrev_b32 v76, 16, v182\n" \
"v_and_b32 v77, 0xffff0000, v182\n" \
"v_lshlrev_b32 v78, 16, v183\n" \
"v_and_b32 v79, 0xffff0000, v183\n" \
"v_lshlrev_b32 v80, 16, v184\n" \
"v_mfma_f32_16x16x32_bf16 v[0:3], a[132:135], a[196:199], v[0:3]\n" \
"ds_read_b64_tr_b16 a[162:163], v227 offset:18432\n" \
"v_and_b32 v81, 0xffff0000, v184\n" \
"v_lshlrev_b32 v82, 16, v185\n" \
"v_and_b32 v83, 0xffff0000, v185\n" \
"v_lshlrev_b32 v84, 16, v186\n" \
"v_and_b32 v85, 0xffff0000, v186\n" \
"v_lshlrev_b32 v86, 16, v187\n" \
"v_mfma_f32_16x16x32_bf16 v[16:19], a[128:131], a[200:203], v[16:19]\n" \
"ds_read_b64_tr_b16 a[164:165], v227 offset:24576\n" \
"v_and_b32 v87, 0xffff0000, v187\n" \
"v_lshlrev_b32 v88, 16, v188\n" \
"v_and_b32 v89, 0xffff0000, v188\n" \
"v_lshlrev_b32 v90, 16, v189\n" \
"v_and_b32 v91, 0xffff0000, v189\n" \
"v_lshlrev_b32 v92, 16, v190\n" \
"v_mfma_f32_16x16x32_bf16 v[16:19], a[132:135], a[204:207], v[16:19]\n" \
"ds_read_b128 a[228:231], v226 offset:2048\n" \
"ds_read_b64_tr_b16 a[166:167], v227 offset:26624\n" \
"v_and_b32 v93, 0xffff0000, v190\n" \
"v_lshlrev_b32 v94, 16, v191\n" \
"v_and_b32 v95, 0xffff0000, v191\n" \
"v_lshlrev_b32 v96, 16, v192\n" \
"v_and_b32 v97, 0xffff0000, v192\n" \
"v_lshlrev_b32 v98, 16, v193\n" \
"v_mfma_f32_16x16x32_bf16 v[4:7], a[136:139], a[192:195], v[4:7]\n" \
"ds_read_b64_tr_b16 a[168:169], v228 offset:16384\n" \
"v_and_b32 v99, 0xffff0000, v193\n" \
"v_lshlrev_b32 v100, 16, v194\n" \
"v_and_b32 v101, 0xffff0000, v194\n" \
"v_lshlrev_b32 v102, 16, v195\n" \
"v_and_b32 v103, 0xffff0000, v195\n" \
"v_lshlrev_b32 v104, 16, v196\n" \
"v_mfma_f32_16x16x32_bf16 v[4:7], a[140:143], a[196:199], v[4:7]\n" \
"ds_read_b64_tr_b16 a[170:171], v228 offset:18432\n" \
"v_and_b32 v105, 0xffff0000, v196\n" \
"v_lshlrev_b32 v106, 16, v197\n" \
"v_and_b32 v107, 0xffff0000, v197\n" \
"v_lshlrev_b32 v108, 16, v198\n" \
"v_and_b32 v109, 0xffff0000, v198\n" \
"v_lshlrev_b32 v110, 16, v199\n" \
"v_mfma_f32_16x16x32_bf16 v[20:23], a[136:139], a[200:203], v[20:23]\n" \
"ds_read_b128 a[232:235], v226 offset:256\n" \
"ds_read_b64_tr_b16 a[172:173], v228 offset:24576\n" \
"v_and_b32 v111, 0xffff0000, v199\n" \
"v_lshlrev_b32 v112, 16, v200\n" \
"v_and_b32 v113, 0xffff0000, v200\n" \
"v_lshlrev_b32 v114, 16, v201\n" \
"v_and_b32 v115, 0xffff0000, v201\n" \
"v_lshlrev_b32 v116, 16, v202\n" \
"v_mfma_f32_16x16x32_bf16 v[20:23], a[140:143], a[204:207], v[20:23]\n" \
"s_waitcnt lgkmcnt(6)\n" \
"ds_read_b64_tr_b16 a[174:175], v228 offset:26624\n" \
"v_and_b32 v117, 0xffff0000, v202\n" \
"v_lshlrev_b32 v118, 16, v203\n" \
"v_and_b32 v119, 0xffff0000, v203\n" \
"v_lshlrev_b32 v120, 16, v204\n" \
"v_and_b32 v121, 0xffff0000, v204\n" \
"v_lshlrev_b32 v122, 16, v205\n" \
"v_mfma_f32_16x16x32_bf16 v[8:11], a[144:147], a[192:195], v[8:11]\n" \
"ds_read_b64_tr_b16 a[176:177], v227 offset:16896\n" \
"v_and_b32 v123, 0xffff0000, v205\n" \
"v_lshlrev_b32 v124, 16, v206\n" \
"v_and_b32 v125, 0xffff0000, v206\n" \
"v_lshlrev_b32 v126, 16, v207\n" \
"v_and_b32 v127, 0xffff0000, v207\n" \
"v_add_f32 v32, v32, v96\n" \
"v_mfma_f32_16x16x32_bf16 v[8:11], a[148:151], a[196:199], v[8:11]\n" \
"ds_read_b128 a[236:239], v226 offset:2304\n" \
"ds_read_b64_tr_b16 a[178:179], v227 offset:18944\n" \
"v_add_f32 v33, v33, v97\n" \
"v_add_f32 v34, v34, v98\n" \
"v_add_f32 v35, v35, v99\n" \
"v_mfma_f32_16x16x32_bf16 v[24:27], a[144:147], a[200:203], v[24:27]\n" \
"ds_read_b64_tr_b16 a[180:181], v227 offset:25088\n" \
"v_add_f32 v36, v36, v100\n" \
"v_mfma_f32_16x16x32_bf16 v[24:27], a[148:151], a[204:207], v[24:27]\n" \
"ds_read_b64_tr_b16 a[182:183], v227 offset:27136\n" \
"v_add_f32 v37, v37, v101\n" \
"v_mfma_f32_16x16x32_bf16 v[12:15], a[152:155], a[192:195], v[12:15]\n" \
"ds_read_b64_tr_b16 a[184:185], v228 offset:16896\n" \
"v_add_f32 v38, v38, v102\n" \
"v_mfma_f32_16x16x32_bf16 v[12:15], a[156:159], a[196:199], v[12:15]\n" \
"ds_read_b64_tr_b16 a[186:187], v228 offset:18944\n" \
"v_add_f32 v39, v39, v103\n" \
"v_mfma_f32_16x16x32_bf16 v[28:31], a[152:155], a[200:203], v[28:31]\n" \
"ds_read_b64_tr_b16 a[188:189], v228 offset:25088\n" \
"v_add_f32 v40, v40, v104\n" \
"v_mfma_f32_16x16x32_bf16 v[28:31], a[156:159], a[204:207], v[28:31]\n" \
"s_waitcnt lgkmcnt(4)\n" \
"ds_read_b64_tr_b16 a[190:191], v228 offset:27136\n" \
"v_add_f32 v41, v41, v105\n" \
"v_mfma_f32_16x16x32_bf16 v[0:3], a[160:163], a[224:227], v[0:3]\n" \
"ds_read_b128 a[208:211], v213 offset:4096\n" \
"v_add_f32 v42, v42, v106\n" \
"v_mfma_f32_16x16x32_bf16 v[0:3], a[164:167], a[228:231], v[0:3]\n" \
"v_add_f32 v43, v43, v107\n" \
"v_add_f32 v44, v44, v108\n" \
"v_mfma_f32_16x16x32_bf16 v[16:19], a[160:163], a[232:235], v[16:19]\n" \
"ds_read_b128 a[212:215], v213 offset:6144\n" \
"v_add_f32 v45, v45, v109\n" \
"v_mfma_f32_16x16x32_bf16 v[16:19], a[164:167], a[236:239], v[16:19]\n" \
"v_add_f32 v46, v46, v110\n" \
"v_add_f32 v47, v47, v111\n" \
"v_mfma_f32_16x16x32_bf16 v[4:7], a[168:171], a[224:227], v[4:7]\n" \
"ds_read_b128 a[216:219], v213 offset:4352\n" \
"v_add_f32 v48, v48, v112\n" \
"v_add_f32 v0, v0, v64\n" \
"v_add_f32 v1, v1, v65\n" \
"v_add_f32 v2, v2, v66\n" \
"v_add_f32 v3, v3, v67\n" \
"v_cvt_pk_bf16_f32 v0, v0, v1\n" \
"v_cvt_pk_bf16_f32 v1, v2, v3\n" \
"v_mfma_f32_16x16x32_bf16 v[4:7], a[172:175], a[228:231], v[4:7]\n" \
"ds_write_b64 v128, v[0:1] offset:0\n" \
"v_add_f32 v49, v49, v113\n" \
"v_add_f32 v50, v50, v114\n" \
"v_mfma_f32_16x16x32_bf16 v[20:23], a[168:171], a[232:235], v[20:23]\n" \
"ds_read_b128 a[220:223], v213 offset:6400\n" \
"v_add_f32 v51, v51, v115\n" \
"v_add_f32 v16, v16, v80\n" \
"v_add_f32 v17, v17, v81\n" \
"v_add_f32 v18, v18, v82\n" \
"v_add_f32 v19, v19, v83\n" \
"v_cvt_pk_bf16_f32 v16, v16, v17\n" \
"v_cvt_pk_bf16_f32 v17, v18, v19\n" \
"v_mfma_f32_16x16x32_bf16 v[20:23], a[172:175], a[236:239], v[20:23]\n" \
"s_waitcnt lgkmcnt(3)\n" \
"ds_write_b64 v128, v[16:17] offset:2304\n" \
"v_add_f32 v52, v52, v116\n" \
"v_add_f32 v53, v53, v117\n" \
"v_mfma_f32_16x16x32_bf16 v[8:11], a[176:179], a[224:227], v[8:11]\n" \
"ds_read_b128 a[240:243], v226 offset:4096\n" \
"v_add_f32 v54, v54, v118\n" \
"v_add_f32 v4, v4, v68\n" \
"v_add_f32 v5, v5, v69\n" \
"v_add_f32 v6, v6, v70\n" \
"v_add_f32 v7, v7, v71\n" \
"v_cvt_pk_bf16_f32 v4, v4, v5\n" \
"v_cvt_pk_bf16_f32 v5, v6, v7\n" \
"v_mfma_f32_16x16x32_bf16 v[8:11], a[180:183], a[228:231], v[8:11]\n" \
"ds_write_b64 v128, v[4:5] offset:32\n" \
"v_add_f32 v55, v55, v119\n" \
"v_add_f32 v56, v56, v120\n" \
"v_mfma_f32_16x16x32_bf16 v[24:27], a[176:179], a[232:235], v[24:27]\n" \
"ds_read_b128 a[244:247], v226 offset:6144\n" \
"v_add_f32 v57, v57, v121\n" \
"v_add_f32 v20, v20, v84\n" \
"v_add_f32 v21, v21, v85\n" \
"v_add_f32 v22, v22, v86\n" \
"v_add_f32 v23, v23, v87\n" \
"v_cvt_pk_bf16_f32 v20, v20, v21\n" \
"v_cvt_pk_bf16_f32 v21, v22, v23\n" \
"v_mfma_f32_16x16x32_bf16 v[24:27], a[180:183], a[236:239], v[24:27]\n" \
"ds_write_b64 v128, v[20:21] offset:2336\n" \
"v_add_f32 v58, v58, v122\n" \
"v_add_f32 v59, v59, v123\n" \
"v_mfma_f32_16x16x32_bf16 v[12:15], a[184:187], a[224:227], v[12:15]\n" \
"ds_read_b128 a[248:251], v226 offset:4352\n" \
"v_add_f32 v60, v60, v124\n" \
"v_add_f32 v8, v8, v72\n" \
"v_add_f32 v9, v9, v73\n" \
"v_mfma_f32_16x16x32_bf16 v[12:15], a[188:191], a[228:231], v[12:15]\n" \
"v_add_f32 v61, v61, v125\n" \
"v_add_f32 v62, v62, v126\n" \
"v_mfma_f32_16x16x32_bf16 v[28:31], a[184:187], a[232:235], v[28:31]\n" \
"ds_read_b128 a[252:255], v226 offset:6400\n" \
"v_add_f32 v63, v63, v127\n" \
"v_mfma_f32_16x16x32_bf16 v[28:31], a[188:191], a[236:239], v[28:31]\n" \
"v_add_f32 v10, v10, v74\n" \
"v_add_f32 v11, v11, v75\n" \
"v_mfma_f32_16x16x32_bf16 v[32:35], a[128:131], a[208:211], v[32:35]\n" \
"v_cvt_pk_bf16_f32 v8, v8, v9\n" \
"v_cvt_pk_bf16_f32 v9, v10, v11\n" \
"v_mfma_f32_16x16x32_bf16 v[32:35], a[132:135], a[212:215], v[32:35]\n" \
"s_waitcnt lgkmcnt(1)\n" \
"ds_write_b64 v128, v[8:9] offset:64\n" \
"v_mfma_f32_16x16x32_bf16 v[48:51], a[128:131], a[216:219], v[48:51]\n" \
"v_add_f32 v24, v24, v88\n" \
"v_add_f32 v25, v25, v89\n" \
"v_mfma_f32_16x16x32_bf16 v[48:51], a[132:135], a[220:223], v[48:51]\n" \
"v_add_f32 v26, v26, v90\n" \
"v_add_f32 v27, v27, v91\n" \
"v_mfma_f32_16x16x32_bf16 v[36:39], a[136:139], a[208:211], v[36:39]\n" \
"v_cvt_pk_bf16_f32 v24, v24, v25\n" \
"v_cvt_pk_bf16_f32 v25, v26, v27\n" \
"v_mfma_f32_16x16x32_bf16 v[36:39], a[140:143], a[212:215], v[36:39]\n" \
"ds_write_b64 v128, v[24:25] offset:2368\n" \
"v_mfma_f32_16x16x32_bf16 v[52:55], a[136:139], a[216:219], v[52:55]\n" \
"v_add_f32 v12, v12, v76\n" \
"v_add_f32 v13, v13, v77\n" \
"v_mfma_f32_16x16x32_bf16 v[52:55], a[140:143], a[220:223], v[52:55]\n" \
"v_add_f32 v14, v14, v78\n" \
"v_add_f32 v15, v15, v79\n" \
"v_mfma_f32_16x16x32_bf16 v[40:43], a[144:147], a[208:211], v[40:43]\n" \
"v_cvt_pk_bf16_f32 v12, v12, v13\n" \
"v_cvt_pk_bf16_f32 v13, v14, v15\n" \
"v_mfma_f32_16x16x32_bf16 v[40:43], a[148:151], a[212:215], v[40:43]\n" \
"ds_write_b64 v128, v[12:13] offset:96\n" \
"v_mfma_f32_16x16x32_bf16 v[56:59], a[144:147], a[216:219], v[56:59]\n" \
"v_add_f32 v28, v28, v92\n" \
"v_add_f32 v29, v29, v93\n" \
"v_mfma_f32_16x16x32_bf16 v[56:59], a[148:151], a[220:223], v[56:59]\n" \
"v_add_f32 v30, v30, v94\n" \
"v_add_f32 v31, v31, v95\n" \
"v_mfma_f32_16x16x32_bf16 v[44:47], a[152:155], a[208:211], v[44:47]\n" \
"v_cvt_pk_bf16_f32 v28, v28, v29\n" \
"v_cvt_pk_bf16_f32 v29, v30, v31\n" \
"v_mfma_f32_16x16x32_bf16 v[44:47], a[156:159], a[212:215], v[44:47]\n" \
"ds_write_b64 v128, v[28:29] offset:2400\n" \
"ds_read_b128 v[140:143], v129 offset:0\n" \
"v_mfma_f32_16x16x32_bf16 v[60:63], a[152:155], a[216:219], v[60:63]\n" \
"ds_read_b128 v[144:147], v129 offset:1152\n" \
"ds_read_b128 v[148:151], v129 offset:2304\n" \
"v_mfma_f32_16x16x32_bf16 v[60:63], a[156:159], a[220:223], v[60:63]\n" \
"ds_read_b128 v[152:155], v129 offset:3456\n" \
"v_mfma_f32_16x16x32_bf16 v[32:35], a[160:163], a[240:243], v[32:35]\n" \
"v_mfma_f32_16x16x32_bf16 v[32:35], a[164:167], a[244:247], v[32:35]\n" \
"s_waitcnt lgkmcnt(5)\n" \
"v_mfma_f32_16x16x32_bf16 v[48:51], a[160:163], a[248:251], v[48:51]\n" \
"v_mfma_f32_16x16x32_bf16 v[48:51], a[164:167], a[252:255], v[48:51]\n" \
"s_add_u32 s70, s77, 0\n" \
"s_waitcnt lgkmcnt(3)\n" \
"buffer_store_dwordx4 v[140:143], v130, s[52:55], s70 offen sc1\n" \
"v_mfma_f32_16x16x32_bf16 v[36:39], a[168:171], a[240:243], v[36:39]\n" \
"v_mfma_f32_16x16x32_bf16 v[36:39], a[172:175], a[244:247], v[36:39]\n" \
"v_mfma_f32_16x16x32_bf16 v[52:55], a[168:171], a[248:251], v[52:55]\n" \
"v_mfma_f32_16x16x32_bf16 v[52:55], a[172:175], a[252:255], v[52:55]\n" \
"s_add_u32 s70, s77, 32768\n" \
"s_waitcnt lgkmcnt(2)\n" \
"buffer_store_dwordx4 v[144:147], v130, s[52:55], s70 offen sc1\n" \
"v_mfma_f32_16x16x32_bf16 v[40:43], a[176:179], a[240:243], v[40:43]\n" \
"v_mfma_f32_16x16x32_bf16 v[40:43], a[180:183], a[244:247], v[40:43]\n" \
"v_mfma_f32_16x16x32_bf16 v[56:59], a[176:179], a[248:251], v[56:59]\n" \
"v_mfma_f32_16x16x32_bf16 v[56:59], a[180:183], a[252:255], v[56:59]\n" \
"s_add_u32 s70, s77, 65536\n" \
"s_waitcnt lgkmcnt(1)\n" \
"buffer_store_dwordx4 v[148:151], v130, s[52:55], s70 offen sc1\n" \
"v_mfma_f32_16x16x32_bf16 v[44:47], a[184:187], a[240:243], v[44:47]\n" \
"v_mfma_f32_16x16x32_bf16 v[44:47], a[188:191], a[244:247], v[44:47]\n" \
"v_mfma_f32_16x16x32_bf16 v[60:63], a[184:187], a[248:251], v[60:63]\n" \
"v_mfma_f32_16x16x32_bf16 v[60:63], a[188:191], a[252:255], v[60:63]\n" \
"s_add_u32 s70, s77, 98304\n" \
"s_waitcnt lgkmcnt(0)\n" \
"buffer_store_dwordx4 v[152:155], v130, s[52:55], s70 offen sc1\n" \
"v_cvt_pk_bf16_f32 v32, v32, v33\n" \
"v_cvt_pk_bf16_f32 v33, v34, v35\n" \
"v_cvt_pk_bf16_f32 v48, v48, v49\n" \
"v_cvt_pk_bf16_f32 v49, v50, v51\n" \
"v_cvt_pk_bf16_f32 v36, v36, v37\n" \
"v_cvt_pk_bf16_f32 v37, v38, v39\n" \
"v_cvt_pk_bf16_f32 v52, v52, v53\n" \
"v_cvt_pk_bf16_f32 v53, v54, v55\n" \
"v_cvt_pk_bf16_f32 v40, v40, v41\n" \
"v_cvt_pk_bf16_f32 v41, v42, v43\n" \
"v_cvt_pk_bf16_f32 v56, v56, v57\n" \
"v_cvt_pk_bf16_f32 v57, v58, v59\n" \
"ds_write_b64 v128, v[32:33] offset:0\n" \
"ds_write_b64 v128, v[48:49] offset:2304\n" \
"ds_write_b64 v128, v[36:37] offset:32\n" \
"ds_write_b64 v128, v[52:53] offset:2336\n" \
"ds_write_b64 v128, v[40:41] offset:64\n" \
"ds_write_b64 v128, v[56:57] offset:2368\n" \
"v_cvt_pk_bf16_f32 v44, v44, v45\n" \
"v_cvt_pk_bf16_f32 v45, v46, v47\n" \
"v_cvt_pk_bf16_f32 v60, v60, v61\n" \
"v_cvt_pk_bf16_f32 v61, v62, v63\n" \
"ds_write_b64 v128, v[44:45] offset:96\n" \
"ds_write_b64 v128, v[60:61] offset:2400\n" \
"ds_read_b128 v[156:159], v129 offset:0\n" \
"ds_read_b128 v[160:163], v129 offset:1152\n" \
"ds_read_b128 v[164:167], v129 offset:2304\n" \
"ds_read_b128 v[168:171], v129 offset:3456\n" \
"s_add_u32 s70, s77, 131072\n" \
"s_waitcnt lgkmcnt(3)\n" \
"buffer_store_dwordx4 v[156:159], v130, s[52:55], s70 offen sc1\n" \
"s_add_u32 s70, s77, 163840\n" \
"s_waitcnt lgkmcnt(2)\n" \
"buffer_store_dwordx4 v[160:163], v130, s[52:55], s70 offen sc1\n" \
"s_add_u32 s70, s77, 196608\n" \
"s_waitcnt lgkmcnt(1)\n" \
"buffer_store_dwordx4 v[164:167], v130, s[52:55], s70 offen sc1\n" \
"s_add_u32 s70, s77, 229376\n" \
"s_waitcnt lgkmcnt(0)\n" \
"buffer_store_dwordx4 v[168:171], v130, s[52:55], s70 offen sc1\n" \
"s_endpgm\n" \
"Lpg12679_%=:\n" \
"s_cbranch_execz Lpg13651_%=\n" \
"Lo0_%=:\n" \
"v_mfma_f32_16x16x32_bf16 v[0:3], a[32:35], a[0:3], v[0:3]\n" \
"ds_read_b128 a[24:27], v213 offset:4352\n" \
"s_add_u32 m0, s75, s95\n" \
"v_mfma_f32_16x16x32_bf16 v[0:3], a[36:39], a[4:7], v[0:3]\n" \
"s_waitcnt lgkmcnt(1)\n" \
"ds_read_b128 a[28:31], v213 offset:6400\n" \
"global_load_lds_dwordx4 v250, s[36:37] offset:0 sc1\n" \
"v_mfma_f32_16x16x32_bf16 v[4:7], a[40:43], a[0:3], v[4:7]\n" \
"s_sub_u32 s74, s74, 32768\n" \
"s_cselect_b32 s74, 131072, s74\n" \
"ds_read_b64_tr_b16 a[48:49], v214 offset:16896\n" \
"v_mfma_f32_16x16x32_bf16 v[4:7], a[44:47], a[4:7], v[4:7]\n" \
"ds_read_b64_tr_b16 a[50:51], v214 offset:18944\n" \
"v_mfma_f32_16x16x32_bf16 v[16:19], a[32:35], a[8:11], v[16:19]\n" \
"ds_read_b64_tr_b16 a[52:53], v214 offset:25088\n" \
"v_mfma_f32_16x16x32_bf16 v[16:19], a[36:39], a[12:15], v[16:19]\n" \
"ds_read_b64_tr_b16 a[54:55], v214 offset:27136\n" \
"global_load_lds_dwordx4 v250, s[36:37] offset:1024 sc1\n" \
"v_mfma_f32_16x16x32_bf16 v[20:23], a[40:43], a[8:11], v[20:23]\n" \
"ds_read_b64_tr_b16 a[56:57], v215 offset:16896\n" \
"v_mfma_f32_16x16x32_bf16 v[20:23], a[44:47], a[12:15], v[20:23]\n" \
"ds_read_b64_tr_b16 a[58:59], v215 offset:18944\n" \
"v_mfma_f32_16x16x32_bf16 v[32:35], a[32:35], a[16:19], v[32:35]\n" \
"ds_read_b64_tr_b16 a[60:61], v215 offset:25088\n" \
"v_mfma_f32_16x16x32_bf16 v[32:35], a[36:39], a[20:23], v[32:35]\n" \
"ds_read_b64_tr_b16 a[62:63], v215 offset:27136\n" \
"global_load_lds_dwordx4 v250, s[36:37] offset:2048 sc1\n" \
"v_mfma_f32_16x16x32_bf16 v[36:39], a[40:43], a[16:19], v[36:39]\n" \
"v_add_u32 v213, s74, v211\n" \
"v_add_u32 v214, s74, v212\n" \
"v_mfma_f32_16x16x32_bf16 v[36:39], a[44:47], a[20:23], v[36:39]\n" \
"s_waitcnt lgkmcnt(3)\n" \
"v_xor_b32 v215, 32, v214\n" \
"v_mfma_f32_16x16x32_bf16 v[48:51], a[32:35], a[24:27], v[48:51]\n" \
"v_mfma_f32_16x16x32_bf16 v[48:51], a[36:39], a[28:31], v[48:51]\n" \
"global_load_lds_dwordx4 v250, s[36:37] offset:3072 sc1\n" \
"v_mfma_f32_16x16x32_bf16 v[52:55], a[40:43], a[24:27], v[52:55]\n" \
"s_waitcnt vmcnt(20) lgkmcnt(0)\n" \
"s_add_u32 s36, s36, 0x1000\n" \
"v_mfma_f32_16x16x32_bf16 v[52:55], a[44:47], a[28:31], v[52:55]\n" \
"s_barrier\n" \
"v_mfma_f32_16x16x32_bf16 v[8:11], a[48:51], a[0:3], v[8:11]\n" \
"s_add_u32 m0, m0, 0x5000\n" \
"ds_read_b64_tr_b16 a[32:33], v214 offset:16384\n" \
"v_mfma_f32_16x16x32_bf16 v[8:11], a[52:55], a[4:7], v[8:11]\n" \
"ds_read_b64_tr_b16 a[34:35], v214 offset:18432\n" \
"global_load_lds_dwordx4 v216, s[38:39] offset:-4096\n" \
"v_mfma_f32_16x16x32_bf16 v[12:15], a[56:59], a[0:3], v[12:15]\n" \
"ds_read_b64_tr_b16 a[36:37], v214 offset:24576\n" \
"v_mfma_f32_16x16x32_bf16 v[12:15], a[60:63], a[4:7], v[12:15]\n" \
"ds_read_b128 a[0:3], v213 offset:0\n" \
"v_mfma_f32_16x16x32_bf16 v[24:27], a[48:51], a[8:11], v[24:27]\n" \
"ds_read_b128 a[4:7], v213 offset:2048\n" \
"v_mfma_f32_16x16x32_bf16 v[24:27], a[52:55], a[12:15], v[24:27]\n" \
"ds_read_b64_tr_b16 a[38:39], v214 offset:26624\n" \
"global_load_lds_dwordx4 v217, s[38:39] offset:-3072\n" \
"v_mfma_f32_16x16x32_bf16 v[28:31], a[56:59], a[8:11], v[28:31]\n" \
"ds_read_b64_tr_b16 a[40:41], v215 offset:16384\n" \
"v_mfma_f32_16x16x32_bf16 v[28:31], a[60:63], a[12:15], v[28:31]\n" \
"ds_read_b128 a[8:11], v213 offset:256\n" \
"v_mfma_f32_16x16x32_bf16 v[40:43], a[48:51], a[16:19], v[40:43]\n" \
"ds_read_b128 a[12:15], v213 offset:2304\n" \
"v_mfma_f32_16x16x32_bf16 v[40:43], a[52:55], a[20:23], v[40:43]\n" \
"ds_read_b64_tr_b16 a[42:43], v215 offset:18432\n" \
"global_load_lds_dwordx4 v218, s[38:39] offset:-2048\n" \
"v_mfma_f32_16x16x32_bf16 v[44:47], a[56:59], a[16:19], v[44:47]\n" \
"ds_read_b64_tr_b16 a[44:45], v215 offset:24576\n" \
"v_mfma_f32_16x16x32_bf16 v[44:47], a[60:63], a[20:23], v[44:47]\n" \
"ds_read_b128 a[16:19], v213 offset:4096\n" \
"global_load_lds_dwordx4 v219, s[38:39] offset:-1024\n" \
"v_mfma_f32_16x16x32_bf16 v[56:59], a[48:51], a[24:27], v[56:59]\n" \
"s_add_u32 s38, s38, 0x20000\n" \
"s_addc_u32 s39, s39, 0\n" \
"ds_read_b128 a[20:23], v213 offset:6144\n" \
"v_mfma_f32_16x16x32_bf16 v[56:59], a[52:55], a[28:31], v[56:59]\n" \
"s_sub_u32 s75, s75, 32768\n" \
"s_cselect_b32 s75, 131072, s75\n" \
"ds_read_b64_tr_b16 a[46:47], v215 offset:26624\n" \
"v_mfma_f32_16x16x32_bf16 v[60:63], a[56:59], a[24:27], v[60:63]\n" \
"s_sub_u32 s72, s72, 1\n" \
"s_cmp_lg_u32 s72, 0\n" \
"v_mfma_f32_16x16x32_bf16 v[60:63], a[60:63], a[28:31], v[60:63]\n" \
"s_waitcnt lgkmcnt(3)\n" \
"s_cbranch_scc1 Lo0_%=\n" \
"s_setpc_b64 s[68:69]\n" \
"Lrole1_%=:\n" \
"Lpg12767_%=:\n" \
"s_cbranch_execz Lcb1_%=\n" \
"s_mov_b32 s72, 12\n" \
"s_call_b64 s[68:69], Lo0_%=\n" \
"buffer_load_dword v220, v221, s[56:59], s82 offen sc1\n" \
"s_mov_b32 s72, 2\n" \
"s_call_b64 s[68:69], Lo0_%=\n" \
"s_waitcnt vmcnt(16)\n" \
"v_cmp_ne_u32 vcc, s60, v220\n" \
"s_nop 1\n" \
"s_and_b32 vcc_hi, vcc_lo, 65535\n" \
"s_cbranch_scc1 Lpl5_%=\n" \
"Lpr5_%=:\n" \
"s_mov_b32 s36, s73\n" \
"s_mov_b64 s[38:39], s[40:41]\n" \
"s_mov_b32 s72, 4\n" \
"s_call_b64 s[68:69], Lo0_%=\n" \
"v_mfma_f32_16x16x32_bf16 v[64:67], a[32:35], a[0:3], 0\n" \
"ds_read_b128 a[24:27], v213 offset:4352\n" \
"s_add_u32 m0, s75, s95\n" \
"v_mfma_f32_16x16x32_bf16 v[64:67], a[36:39], a[4:7], v[64:67]\n" \
"s_waitcnt lgkmcnt(1)\n" \
"ds_read_b128 a[28:31], v213 offset:6400\n" \
"global_load_lds_dwordx4 v250, s[36:37] offset:0 sc1\n" \
"v_mfma_f32_16x16x32_bf16 v[68:71], a[40:43], a[0:3], 0\n" \
"s_sub_u32 s74, s74, 32768\n" \
"s_cselect_b32 s74, 131072, s74\n" \
"ds_read_b64_tr_b16 a[48:49], v214 offset:16896\n" \
"Lpg12794_%=:\n" \
"s_cbranch_execz Lcb1_%=\n" \
"v_mfma_f32_16x16x32_bf16 v[68:71], a[44:47], a[4:7], v[68:71]\n" \
"ds_read_b64_tr_b16 a[50:51], v214 offset:18944\n" \
"v_cvt_pk_bf16_f32 v0, v0, v1\n" \
"v_mfma_f32_16x16x32_bf16 v[80:83], a[32:35], a[8:11], 0\n" \
"ds_read_b64_tr_b16 a[52:53], v214 offset:25088\n" \
"v_cvt_pk_bf16_f32 v1, v2, v3\n" \
"v_mfma_f32_16x16x32_bf16 v[80:83], a[36:39], a[12:15], v[80:83]\n" \
"ds_read_b64_tr_b16 a[54:55], v214 offset:27136\n" \
"global_load_lds_dwordx4 v250, s[36:37] offset:1024 sc1\n" \
"v_mfma_f32_16x16x32_bf16 v[84:87], a[40:43], a[8:11], 0\n" \
"ds_read_b64_tr_b16 a[56:57], v215 offset:16896\n" \
"v_cvt_pk_bf16_f32 v2, v4, v5\n" \
"v_mfma_f32_16x16x32_bf16 v[84:87], a[44:47], a[12:15], v[84:87]\n" \
"ds_read_b64_tr_b16 a[58:59], v215 offset:18944\n" \
"v_cvt_pk_bf16_f32 v3, v6, v7\n" \
"v_mfma_f32_16x16x32_bf16 v[96:99], a[32:35], a[16:19], 0\n" \
"s_add_u32 s71, s76, 0\n" \
"ds_read_b64_tr_b16 a[60:61], v215 offset:25088\n" \
"v_mfma_f32_16x16x32_bf16 v[96:99], a[36:39], a[20:23], v[96:99]\n" \
"ds_read_b64_tr_b16 a[62:63], v215 offset:27136\n" \
"global_load_lds_dwordx4 v250, s[36:37] offset:2048 sc1\n" \
"v_mfma_f32_16x16x32_bf16 v[100:103], a[40:43], a[16:19], 0\n" \
"v_add_u32 v213, s74, v211\n" \
"v_add_u32 v214, s74, v212\n" \
"v_mfma_f32_16x16x32_bf16 v[100:103], a[44:47], a[20:23], v[100:103]\n" \
"s_waitcnt lgkmcnt(3)\n" \
"v_xor_b32 v215, 32, v214\n" \
"v_mfma_f32_16x16x32_bf16 v[112:115], a[32:35], a[24:27], 0\n" \
"v_mfma_f32_16x16x32_bf16 v[112:115], a[36:39], a[28:31], v[112:115]\n" \
"global_load_lds_dwordx4 v250, s[36:37] offset:3072 sc1\n" \
"v_mfma_f32_16x16x32_bf16 v[116:119], a[40:43], a[24:27], 0\n" \
"s_waitcnt vmcnt(20) lgkmcnt(0)\n" \
"s_add_u32 s36, s36, 0x1000\n" \
"v_mfma_f32_16x16x32_bf16 v[116:119], a[44:47], a[28:31], v[116:119]\n" \
"s_barrier\n" \
"v_mfma_f32_16x16x32_bf16 v[72:75], a[48:51], a[0:3], 0\n" \
"s_add_u32 m0, m0, 0x5000\n" \
"ds_read_b64_tr_b16 a[32:33], v214 offset:16384\n" \
"v_mfma_f32_16x16x32_bf16 v[72:75], a[52:55], a[4:7], v[72:75]\n" \
"ds_read_b64_tr_b16 a[34:35], v214 offset:18432\n" \
"global_load_lds_dwordx4 v216, s[38:39] offset:-4096\n" \
"v_mfma_f32_16x16x32_bf16 v[76:79], a[56:59], a[0:3], 0\n" \
"ds_read_b64_tr_b16 a[36:37], v214 offset:24576\n" \
"v_mfma_f32_16x16x32_bf16 v[76:79], a[60:63], a[4:7], v[76:79]\n" \
"ds_read_b128 a[0:3], v213 offset:0\n" \
"v_mfma_f32_16x16x32_bf16 v[88:91], a[48:51], a[8:11], 0\n" \
"ds_read_b128 a[4:7], v213 offset:2048\n" \
"v_mfma_f32_16x16x32_bf16 v[88:91], a[52:55], a[12:15], v[88:91]\n" \
"ds_read_b64_tr_b16 a[38:39], v214 offset:26624\n" \
"global_load_lds_dwordx4 v217, s[38:39] offset:-3072\n" \
"v_mfma_f32_16x16x32_bf16 v[92:95], a[56:59], a[8:11], 0\n" \
"buffer_store_dwordx4 v[0:3], v248, s[56:59], s71 offen offset:0 sc1\n" \
"ds_read_b64_tr_b16 a[40:41], v215 offset:16384\n" \
"v_cvt_pk_bf16_f32 v4, v8, v9\n" \
"v_cvt_pk_bf16_f32 v5, v10, v11\n" \
"v_cvt_pk_bf16_f32 v6, v12, v13\n" \
"v_mfma_f32_16x16x32_bf16 v[92:95], a[60:63], a[12:15], v[92:95]\n" \
"ds_read_b128 a[8:11], v213 offset:256\n" \
"v_cvt_pk_bf16_f32 v7, v14, v15\n" \
"v_mfma_f32_16x16x32_bf16 v[104:107], a[48:51], a[16:19], 0\n" \
"ds_read_b128 a[12:15], v213 offset:2304\n" \
"v_mfma_f32_16x16x32_bf16 v[104:107], a[52:55], a[20:23], v[104:107]\n" \
"ds_read_b64_tr_b16 a[42:43], v215 offset:18432\n" \
"global_load_lds_dwordx4 v218, s[38:39] offset:-2048\n" \
"v_mfma_f32_16x16x32_bf16 v[108:111], a[56:59], a[16:19], 0\n" \
"buffer_store_dwordx4 v[4:7], v248, s[56:59], s71 offen offset:1024 sc1\n" \
"ds_read_b64_tr_b16 a[44:45], v215 offset:24576\n" \
"v_cvt_pk_bf16_f32 v8, v16, v17\n" \
"v_cvt_pk_bf16_f32 v9, v18, v19\n" \
"v_cvt_pk_bf16_f32 v10, v20, v21\n" \
"v_mfma_f32_16x16x32_bf16 v[108:111], a[60:63], a[20:23], v[108:111]\n" \
"ds_read_b128 a[16:19], v213 offset:4096\n" \
"v_cvt_pk_bf16_f32 v11, v22, v23\n" \
"v_mfma_f32_16x16x32_bf16 v[120:123], a[48:51], a[24:27], 0\n" \
"ds_read_b128 a[20:23], v213 offset:6144\n" \
"global_load_lds_dwordx4 v219, s[38:39] offset:-1024\n" \
"v_mfma_f32_16x16x32_bf16 v[120:123], a[52:55], a[28:31], v[120:123]\n" \
"s_add_u32 s38, s38, 0x20000\n" \
"s_addc_u32 s39, s39, 0\n" \
"ds_read_b64_tr_b16 a[46:47], v215 offset:26624\n" \
"v_mfma_f32_16x16x32_bf16 v[124:127], a[56:59], a[24:27], 0\n" \
"buffer_store_dwordx4 v[8:11], v248, s[56:59], s71 offen offset:2048 sc1\n" \
"s_sub_u32 s75, s75, 32768\n" \
"s_cselect_b32 s75, 131072, s75\n" \
"v_cvt_pk_bf16_f32 v12, v24, v25\n" \
"v_cvt_pk_bf16_f32 v13, v26, v27\n" \
"v_mfma_f32_16x16x32_bf16 v[124:127], a[60:63], a[28:31], v[124:127]\n" \
"s_waitcnt lgkmcnt(3)\n" \
"v_cvt_pk_bf16_f32 v14, v28, v29\n" \
"v_cvt_pk_bf16_f32 v15, v30, v31\n" \
"v_mfma_f32_16x16x32_bf16 v[64:67], a[32:35], a[0:3], v[64:67]\n" \
"ds_read_b128 a[24:27], v213 offset:4352\n" \
"s_add_u32 m0, s75, s95\n" \
"v_mfma_f32_16x16x32_bf16 v[64:67], a[36:39], a[4:7], v[64:67]\n" \
"s_waitcnt lgkmcnt(1)\n" \
"ds_read_b128 a[28:31], v213 offset:6400\n" \
"global_load_lds_dwordx4 v250, s[36:37] offset:0 sc1\n" \
"v_mfma_f32_16x16x32_bf16 v[68:71], a[40:43], a[0:3], v[68:71]\n" \
"s_sub_u32 s74, s74, 32768\n" \
"s_cselect_b32 s74, 131072, s74\n" \
"ds_read_b64_tr_b16 a[48:49], v214 offset:16896\n" \
"v_mfma_f32_16x16x32_bf16 v[68:71], a[44:47], a[4:7], v[68:71]\n" \
"ds_read_b64_tr_b16 a[50:51], v214 offset:18944\n" \
"v_mfma_f32_16x16x32_bf16 v[80:83], a[32:35], a[8:11], v[80:83]\n" \
"ds_read_b64_tr_b16 a[52:53], v214 offset:25088\n" \
"v_mfma_f32_16x16x32_bf16 v[80:83], a[36:39], a[12:15], v[80:83]\n" \
"ds_read_b64_tr_b16 a[54:55], v214 offset:27136\n" \
"global_load_lds_dwordx4 v250, s[36:37] offset:1024 sc1\n" \
"v_mfma_f32_16x16x32_bf16 v[84:87], a[40:43], a[8:11], v[84:87]\n" \
"ds_read_b64_tr_b16 a[56:57], v215 offset:16896\n" \
"v_mfma_f32_16x16x32_bf16 v[84:87], a[44:47], a[12:15], v[84:87]\n" \
"ds_read_b64_tr_b16 a[58:59], v215 offset:18944\n" \
"v_mfma_f32_16x16x32_bf16 v[96:99], a[32:35], a[16:19], v[96:99]\n" \
"ds_read_b64_tr_b16 a[60:61], v215 offset:25088\n" \
"v_mfma_f32_16x16x32_bf16 v[96:99], a[36:39], a[20:23], v[96:99]\n" \
"ds_read_b64_tr_b16 a[62:63], v215 offset:27136\n" \
"global_load_lds_dwordx4 v250, s[36:37] offset:2048 sc1\n" \
"v_mfma_f32_16x16x32_bf16 v[100:103], a[40:43], a[16:19], v[100:103]\n" \
"v_add_u32 v213, s74, v211\n" \
"v_add_u32 v214, s74, v212\n" \
"v_mfma_f32_16x16x32_bf16 v[100:103], a[44:47], a[20:23], v[100:103]\n" \
"s_waitcnt lgkmcnt(3)\n" \
"v_xor_b32 v215, 32, v214\n" \
"v_mfma_f32_16x16x32_bf16 v[112:115], a[32:35], a[24:27], v[112:115]\n" \
"v_mfma_f32_16x16x32_bf16 v[112:115], a[36:39], a[28:31], v[112:115]\n" \
"global_load_lds_dwordx4 v250, s[36:37] offset:3072 sc1\n" \
"v_mfma_f32_16x16x32_bf16 v[116:119], a[40:43], a[24:27], v[116:119]\n" \
"s_waitcnt vmcnt(23) lgkmcnt(0)\n" \
"s_add_u32 s36, s36, 0x1000\n" \
"v_mfma_f32_16x16x32_bf16 v[116:119], a[44:47], a[28:31], v[116:119]\n" \
"s_barrier\n" \
"v_mfma_f32_16x16x32_bf16 v[72:75], a[48:51], a[0:3], v[72:75]\n" \
"s_add_u32 m0, m0, 0x5000\n" \
"ds_read_b64_tr_b16 a[32:33], v214 offset:16384\n" \
"v_mfma_f32_16x16x32_bf16 v[72:75], a[52:55], a[4:7], v[72:75]\n" \
"ds_read_b64_tr_b16 a[34:35], v214 offset:18432\n" \
"global_load_lds_dwordx4 v216, s[38:39] offset:-4096\n" \
"v_mfma_f32_16x16x32_bf16 v[76:79], a[56:59], a[0:3], v[76:79]\n" \
"ds_read_b64_tr_b16 a[36:37], v214 offset:24576\n" \
"v_mfma_f32_16x16x32_bf16 v[76:79], a[60:63], a[4:7], v[76:79]\n" \
"ds_read_b128 a[0:3], v213 offset:0\n" \
"v_mfma_f32_16x16x32_bf16 v[88:91], a[48:51], a[8:11], v[88:91]\n" \
"ds_read_b128 a[4:7], v213 offset:2048\n" \
"v_mfma_f32_16x16x32_bf16 v[88:91], a[52:55], a[12:15], v[88:91]\n" \
"ds_read_b64_tr_b16 a[38:39], v214 offset:26624\n" \
"global_load_lds_dwordx4 v217, s[38:39] offset:-3072\n" \
"v_mfma_f32_16x16x32_bf16 v[92:95], a[56:59], a[8:11], v[92:95]\n" \
"buffer_store_dwordx4 v[12:15], v248, s[56:59], s71 offen offset:3072 sc1\n" \
"ds_read_b64_tr_b16 a[40:41], v215 offset:16384\n" \
"v_cvt_pk_bf16_f32 v16, v32, v33\n" \
"v_cvt_pk_bf16_f32 v17, v34, v35\n" \
"v_cvt_pk_bf16_f32 v18, v36, v37\n" \
"v_mfma_f32_16x16x32_bf16 v[92:95], a[60:63], a[12:15], v[92:95]\n" \
"ds_read_b128 a[8:11], v213 offset:256\n" \
"v_cvt_pk_bf16_f32 v19, v38, v39\n" \
"v_mfma_f32_16x16x32_bf16 v[104:107], a[48:51], a[16:19], v[104:107]\n" \
"ds_read_b128 a[12:15], v213 offset:2304\n" \
"s_add_u32 s71, s76, 4096\n" \
"v_mfma_f32_16x16x32_bf16 v[104:107], a[52:55], a[20:23], v[104:107]\n" \
"ds_read_b64_tr_b16 a[42:43], v215 offset:18432\n" \
"global_load_lds_dwordx4 v218, s[38:39] offset:-2048\n" \
"v_mfma_f32_16x16x32_bf16 v[108:111], a[56:59], a[16:19], v[108:111]\n" \
"buffer_store_dwordx4 v[16:19], v248, s[56:59], s71 offen offset:0 sc1\n" \
"ds_read_b64_tr_b16 a[44:45], v215 offset:24576\n" \
"v_cvt_pk_bf16_f32 v20, v40, v41\n" \
"v_cvt_pk_bf16_f32 v21, v42, v43\n" \
"v_cvt_pk_bf16_f32 v22, v44, v45\n" \
"v_mfma_f32_16x16x32_bf16 v[108:111], a[60:63], a[20:23], v[108:111]\n" \
"ds_read_b128 a[16:19], v213 offset:4096\n" \
"v_cvt_pk_bf16_f32 v23, v46, v47\n" \
"v_mfma_f32_16x16x32_bf16 v[120:123], a[48:51], a[24:27], v[120:123]\n" \
"ds_read_b128 a[20:23], v213 offset:6144\n" \
"global_load_lds_dwordx4 v219, s[38:39] offset:-1024\n" \
"v_mfma_f32_16x16x32_bf16 v[120:123], a[52:55], a[28:31], v[120:123]\n" \
"s_add_u32 s38, s38, 0x20000\n" \
"s_addc_u32 s39, s39, 0\n" \
"ds_read_b64_tr_b16 a[46:47], v215 offset:26624\n" \
"v_mfma_f32_16x16x32_bf16 v[124:127], a[56:59], a[24:27], v[124:127]\n" \
"buffer_store_dwordx4 v[20:23], v248, s[56:59], s71 offen offset:1024 sc1\n" \
"s_sub_u32 s75, s75, 32768\n" \
"s_cselect_b32 s75, 131072, s75\n" \
"v_cvt_pk_bf16_f32 v24, v48, v49\n" \
"v_cvt_pk_bf16_f32 v25, v50, v51\n" \
"v_mfma_f32_16x16x32_bf16 v[124:127], a[60:63], a[28:31], v[124:127]\n" \
"s_waitcnt lgkmcnt(3)\n" \
"v_cvt_pk_bf16_f32 v26, v52, v53\n" \
"v_cvt_pk_bf16_f32 v27, v54, v55\n" \
"v_mfma_f32_16x16x32_bf16 v[64:67], a[32:35], a[0:3], v[64:67]\n" \
"ds_read_b128 a[24:27], v213 offset:4352\n" \
"s_add_u32 m0, s75, s95\n" \
"v_mfma_f32_16x16x32_bf16 v[64:67], a[36:39], a[4:7], v[64:67]\n" \
"s_waitcnt lgkmcnt(1)\n" \
"ds_read_b128 a[28:31], v213 offset:6400\n" \
"global_load_lds_dwordx4 v250, s[36:37] offset:0 sc1\n" \
"v_mfma_f32_16x16x32_bf16 v[68:71], a[40:43], a[0:3], v[68:71]\n" \
"s_sub_u32 s74, s74, 32768\n" \
"s_cselect_b32 s74, 131072, s74\n" \
"ds_read_b64_tr_b16 a[48:49], v214 offset:16896\n" \
"v_mfma_f32_16x16x32_bf16 v[68:71], a[44:47], a[4:7], v[68:71]\n" \
"ds_read_b64_tr_b16 a[50:51], v214 offset:18944\n" \
"v_mfma_f32_16x16x32_bf16 v[80:83], a[32:35], a[8:11], v[80:83]\n" \
"ds_read_b64_tr_b16 a[52:53], v214 offset:25088\n" \
"v_mfma_f32_16x16x32_bf16 v[80:83], a[36:39], a[12:15], v[80:83]\n" \
"ds_read_b64_tr_b16 a[54:55], v214 offset:27136\n" \
"global_load_lds_dwordx4 v250, s[36:37] offset:1024 sc1\n" \
"v_mfma_f32_16x16x32_bf16 v[84:87], a[40:43], a[8:11], v[84:87]\n" \
"ds_read_b64_tr_b16 a[56:57], v215 offset:16896\n" \
"v_mfma_f32_16x16x32_bf16 v[84:87], a[44:47], a[12:15], v[84:87]\n" \
"ds_read_b64_tr_b16 a[58:59], v215 offset:18944\n" \
"v_mfma_f32_16x16x32_bf16 v[96:99], a[32:35], a[16:19], v[96:99]\n" \
"ds_read_b64_tr_b16 a[60:61], v215 offset:25088\n" \
"v_mfma_f32_16x16x32_bf16 v[96:99], a[36:39], a[20:23], v[96:99]\n" \
"ds_read_b64_tr_b16 a[62:63], v215 offset:27136\n" \
"global_load_lds_dwordx4 v250, s[36:37] offset:2048 sc1\n" \
"v_mfma_f32_16x16x32_bf16 v[100:103], a[40:43], a[16:19], v[100:103]\n" \
"v_add_u32 v213, s74, v211\n" \
"v_add_u32 v214, s74, v212\n" \
"v_mfma_f32_16x16x32_bf16 v[100:103], a[44:47], a[20:23], v[100:103]\n" \
"s_waitcnt lgkmcnt(3)\n" \
"v_xor_b32 v215, 32, v214\n" \
"v_mfma_f32_16x16x32_bf16 v[112:115], a[32:35], a[24:27], v[112:115]\n" \
"v_mfma_f32_16x16x32_bf16 v[112:115], a[36:39], a[28:31], v[112:115]\n" \
"global_load_lds_dwordx4 v250, s[36:37] offset:3072 sc1\n" \
"v_mfma_f32_16x16x32_bf16 v[116:119], a[40:43], a[24:27], v[116:119]\n" \
"s_waitcnt vmcnt(26) lgkmcnt(0)\n" \
"s_add_u32 s36, s36, 0x1000\n" \
"v_mfma_f32_16x16x32_bf16 v[116:119], a[44:47], a[28:31], v[116:119]\n" \
"s_barrier\n" \
"v_mfma_f32_16x16x32_bf16 v[72:75], a[48:51], a[0:3], v[72:75]\n" \
"s_add_u32 m0, m0, 0x5000\n" \
"ds_read_b64_tr_b16 a[32:33], v214 offset:16384\n" \
"v_mfma_f32_16x16x32_bf16 v[72:75], a[52:55], a[4:7], v[72:75]\n" \
"ds_read_b64_tr_b16 a[34:35], v214 offset:18432\n" \
"global_load_lds_dwordx4 v216, s[38:39] offset:-4096\n" \
"v_mfma_f32_16x16x32_bf16 v[76:79], a[56:59], a[0:3], v[76:79]\n" \
"ds_read_b64_tr_b16 a[36:37], v214 offset:24576\n" \
"v_mfma_f32_16x16x32_bf16 v[76:79], a[60:63], a[4:7], v[76:79]\n" \
"ds_read_b128 a[0:3], v213 offset:0\n" \
"v_mfma_f32_16x16x32_bf16 v[88:91], a[48:51], a[8:11], v[88:91]\n" \
"ds_read_b128 a[4:7], v213 offset:2048\n" \
"v_mfma_f32_16x16x32_bf16 v[88:91], a[52:55], a[12:15], v[88:91]\n" \
"ds_read_b64_tr_b16 a[38:39], v214 offset:26624\n" \
"global_load_lds_dwordx4 v217, s[38:39] offset:-3072\n" \
"v_mfma_f32_16x16x32_bf16 v[92:95], a[56:59], a[8:11], v[92:95]\n" \
"buffer_store_dwordx4 v[24:27], v248, s[56:59], s71 offen offset:2048 sc1\n" \
"ds_read_b64_tr_b16 a[40:41], v215 offset:16384\n" \
"v_cvt_pk_bf16_f32 v28, v56, v57\n" \
"v_cvt_pk_bf16_f32 v29, v58, v59\n" \
"v_cvt_pk_bf16_f32 v30, v60, v61\n" \
"v_mfma_f32_16x16x32_bf16 v[92:95], a[60:63], a[12:15], v[92:95]\n" \
"ds_read_b128 a[8:11], v213 offset:256\n" \
"v_cvt_pk_bf16_f32 v31, v62, v63\n" \
"v_mfma_f32_16x16x32_bf16 v[104:107], a[48:51], a[16:19], v[104:107]\n" \
"ds_read_b128 a[12:15], v213 offset:2304\n" \
"v_mfma_f32_16x16x32_bf16 v[104:107], a[52:55], a[20:23], v[104:107]\n" \
"ds_read_b64_tr_b16 a[42:43], v215 offset:18432\n" \
"global_load_lds_dwordx4 v218, s[38:39] offset:-2048\n" \
"v_mfma_f32_16x16x32_bf16 v[108:111], a[56:59], a[16:19], v[108:111]\n" \
"buffer_store_dwordx4 v[28:31], v248, s[56:59], s71 offen offset:3072 sc1\n" \
"ds_read_b64_tr_b16 a[44:45], v215 offset:24576\n" \
"v_mfma_f32_16x16x32_bf16 v[108:111], a[60:63], a[20:23], v[108:111]\n" \
"ds_read_b128 a[16:19], v213 offset:4096\n" \
"v_mfma_f32_16x16x32_bf16 v[120:123], a[48:51], a[24:27], v[120:123]\n" \
"ds_read_b128 a[20:23], v213 offset:6144\n" \
"global_load_lds_dwordx4 v219, s[38:39] offset:-1024\n" \
"v_mfma_f32_16x16x32_bf16 v[120:123], a[52:55], a[28:31], v[120:123]\n" \
"s_add_u32 s38, s38, 0x20000\n" \
"s_addc_u32 s39, s39, 0\n" \
"ds_read_b64_tr_b16 a[46:47], v215 offset:26624\n" \
"v_mfma_f32_16x16x32_bf16 v[124:127], a[56:59], a[24:27], v[124:127]\n" \
"s_sub_u32 s75, s75, 32768\n" \
"s_cselect_b32 s75, 131072, s75\n" \
"v_mfma_f32_16x16x32_bf16 v[124:127], a[60:63], a[28:31], v[124:127]\n" \
"s_waitcnt lgkmcnt(3)\n" \
"v_mfma_f32_16x16x32_bf16 v[64:67], a[32:35], a[0:3], v[64:67]\n" \
"ds_read_b128 a[24:27], v213 offset:4352\n" \
"s_add_u32 m0, s75, s95\n" \
"v_mfma_f32_16x16x32_bf16 v[64:67], a[36:39], a[4:7], v[64:67]\n" \
"s_waitcnt lgkmcnt(1)\n" \
"ds_read_b128 a[28:31], v213 offset:6400\n" \
"global_load_lds_dwordx4 v250, s[36:37] offset:0 sc1\n" \
"v_mfma_f32_16x16x32_bf16 v[68:71], a[40:43], a[0:3], v[68:71]\n" \
"s_sub_u32 s74, s74, 32768\n" \
"s_cselect_b32 s74, 131072, s74\n" \
"ds_read_b64_tr_b16 a[48:49], v214 offset:16896\n" \
"v_mfma_f32_16x16x32_bf16 v[68:71], a[44:47], a[4:7], v[68:71]\n" \
"ds_read_b64_tr_b16 a[50:51], v214 offset:18944\n" \
"v_mfma_f32_16x16x32_bf16 v[80:83], a[32:35], a[8:11], v[80:83]\n" \
"ds_read_b64_tr_b16 a[52:53], v214 offset:25088\n" \
"v_mfma_f32_16x16x32_bf16 v[80:83], a[36:39], a[12:15], v[80:83]\n" \
"ds_read_b64_tr_b16 a[54:55], v214 offset:27136\n" \
"global_load_lds_dwordx4 v250, s[36:37] offset:1024 sc1\n" \
"v_mfma_f32_16x16x32_bf16 v[84:87], a[40:43], a[8:11], v[84:87]\n" \
"ds_read_b64_tr_b16 a[56:57], v215 offset:16896\n" \
"v_mfma_f32_16x16x32_bf16 v[84:87], a[44:47], a[12:15], v[84:87]\n" \
"ds_read_b64_tr_b16 a[58:59], v215 offset:18944\n" \
"v_mfma_f32_16x16x32_bf16 v[96:99], a[32:35], a[16:19], v[96:99]\n" \
"ds_read_b64_tr_b16 a[60:61], v215 offset:25088\n" \
"v_mfma_f32_16x16x32_bf16 v[96:99], a[36:39], a[20:23], v[96:99]\n" \
"ds_read_b64_tr_b16 a[62:63], v215 offset:27136\n" \
"global_load_lds_dwordx4 v250, s[36:37] offset:2048 sc1\n" \
"v_mfma_f32_16x16x32_bf16 v[100:103], a[40:43], a[16:19], v[100:103]\n" \
"v_add_u32 v213, s74, v211\n" \
"v_add_u32 v214, s74, v212\n" \
"v_mfma_f32_16x16x32_bf16 v[100:103], a[44:47], a[20:23], v[100:103]\n" \
"s_waitcnt lgkmcnt(3)\n" \
"v_xor_b32 v215, 32, v214\n" \
"v_mfma_f32_16x16x32_bf16 v[112:115], a[32:35], a[24:27], v[112:115]\n" \
"v_mfma_f32_16x16x32_bf16 v[112:115], a[36:39], a[28:31], v[112:115]\n" \
"global_load_lds_dwordx4 v250, s[36:37] offset:3072 sc1\n" \
"v_mfma_f32_16x16x32_bf16 v[116:119], a[40:43], a[24:27], v[116:119]\n" \
"s_waitcnt vmcnt(26) lgkmcnt(0)\n" \
"s_add_u32 s36, s36, 0x1000\n" \
"v_mfma_f32_16x16x32_bf16 v[116:119], a[44:47], a[28:31], v[116:119]\n" \
"s_barrier\n" \
"v_mfma_f32_16x16x32_bf16 v[72:75], a[48:51], a[0:3], v[72:75]\n" \
"s_add_u32 m0, m0, 0x5000\n" \
"ds_read_b64_tr_b16 a[32:33], v214 offset:16384\n" \
"v_mfma_f32_16x16x32_bf16 v[72:75], a[52:55], a[4:7], v[72:75]\n" \
"ds_read_b64_tr_b16 a[34:35], v214 offset:18432\n" \
"global_load_lds_dwordx4 v216, s[38:39] offset:-4096\n" \
"v_mfma_f32_16x16x32_bf16 v[76:79], a[56:59], a[0:3], v[76:79]\n" \
"ds_read_b64_tr_b16 a[36:37], v214 offset:24576\n" \
"v_mfma_f32_16x16x32_bf16 v[76:79], a[60:63], a[4:7], v[76:79]\n" \
"ds_read_b128 a[0:3], v213 offset:0\n" \
"v_mfma_f32_16x16x32_bf16 v[88:91], a[48:51], a[8:11], v[88:91]\n" \
"ds_read_b128 a[4:7], v213 offset:2048\n" \
"v_mfma_f32_16x16x32_bf16 v[88:91], a[52:55], a[12:15], v[88:91]\n" \
"ds_read_b64_tr_b16 a[38:39], v214 offset:26624\n" \
"global_load_lds_dwordx4 v217, s[38:39] offset:-3072\n" \
"v_mfma_f32_16x16x32_bf16 v[92:95], a[56:59], a[8:11], v[92:95]\n" \
"ds_read_b64_tr_b16 a[40:41], v215 offset:16384\n" \
"v_mfma_f32_16x16x32_bf16 v[92:95], a[60:63], a[12:15], v[92:95]\n" \
"ds_read_b128 a[8:11], v213 offset:256\n" \
"v_mfma_f32_16x16x32_bf16 v[104:107], a[48:51], a[16:19], v[104:107]\n" \
"ds_read_b128 a[12:15], v213 offset:2304\n" \
"v_mfma_f32_16x16x32_bf16 v[104:107], a[52:55], a[20:23], v[104:107]\n" \
"ds_read_b64_tr_b16 a[42:43], v215 offset:18432\n" \
"global_load_lds_dwordx4 v218, s[38:39] offset:-2048\n" \
"v_mfma_f32_16x16x32_bf16 v[108:111], a[56:59], a[16:19], v[108:111]\n" \
"ds_read_b64_tr_b16 a[44:45], v215 offset:24576\n" \
"v_mfma_f32_16x16x32_bf16 v[108:111], a[60:63], a[20:23], v[108:111]\n" \
"ds_read_b128 a[16:19], v213 offset:4096\n" \
"v_mfma_f32_16x16x32_bf16 v[120:123], a[48:51], a[24:27], v[120:123]\n" \
"ds_read_b128 a[20:23], v213 offset:6144\n" \
"global_load_lds_dwordx4 v219, s[38:39] offset:-1024\n" \
"v_mfma_f32_16x16x32_bf16 v[120:123], a[52:55], a[28:31], v[120:123]\n" \
"s_add_u32 s38, s38, 0x20000\n" \
"s_addc_u32 s39, s39, 0\n" \
"ds_read_b64_tr_b16 a[46:47], v215 offset:26624\n" \
"v_mfma_f32_16x16x32_bf16 v[124:127], a[56:59], a[24:27], v[124:127]\n" \
"s_sub_u32 s75, s75, 32768\n" \
"s_cselect_b32 s75, 131072, s75\n" \
"v_mfma_f32_16x16x32_bf16 v[124:127], a[60:63], a[28:31], v[124:127]\n" \
"s_waitcnt lgkmcnt(3)\n" \
"v_mfma_f32_16x16x32_bf16 v[64:67], a[32:35], a[0:3], v[64:67]\n" \
"ds_read_b128 a[24:27], v213 offset:4352\n" \
"s_add_u32 m0, s75, s95\n" \
"v_mfma_f32_16x16x32_bf16 v[64:67], a[36:39], a[4:7], v[64:67]\n" \
"s_waitcnt lgkmcnt(1)\n" \
"ds_read_b128 a[28:31], v213 offset:6400\n" \
"global_load_lds_dwordx4 v250, s[36:37] offset:0 sc1\n" \
"v_mfma_f32_16x16x32_bf16 v[68:71], a[40:43], a[0:3], v[68:71]\n" \
"s_sub_u32 s74, s74, 32768\n" \
"s_cselect_b32 s74, 131072, s74\n" \
"ds_read_b64_tr_b16 a[48:49], v214 offset:16896\n" \
"v_mfma_f32_16x16x32_bf16 v[68:71], a[44:47], a[4:7], v[68:71]\n" \
"ds_read_b64_tr_b16 a[50:51], v214 offset:18944\n" \
"v_mfma_f32_16x16x32_bf16 v[80:83], a[32:35], a[8:11], v[80:83]\n" \
"ds_read_b64_tr_b16 a[52:53], v214 offset:25088\n" \
"v_mfma_f32_16x16x32_bf16 v[80:83], a[36:39], a[12:15], v[80:83]\n" \
"ds_read_b64_tr_b16 a[54:55], v214 offset:27136\n" \
"global_load_lds_dwordx4 v250, s[36:37] offset:1024 sc1\n" \
"v_mfma_f32_16x16x32_bf16 v[84:87], a[40:43], a[8:11], v[84:87]\n" \
"ds_read_b64_tr_b16 a[56:57], v215 offset:16896\n" \
"v_mfma_f32_16x16x32_bf16 v[84:87], a[44:47], a[12:15], v[84:87]\n" \
"ds_read_b64_tr_b16 a[58:59], v215 offset:18944\n" \
"v_mfma_f32_16x16x32_bf16 v[96:99], a[32:35], a[16:19], v[96:99]\n" \
"ds_read_b64_tr_b16 a[60:61], v215 offset:25088\n" \
"v_mfma_f32_16x16x32_bf16 v[96:99], a[36:39], a[20:23], v[96:99]\n" \
"ds_read_b64_tr_b16 a[62:63], v215 offset:27136\n" \
"global_load_lds_dwordx4 v250, s[36:37] offset:2048 sc1\n" \
"v_mfma_f32_16x16x32_bf16 v[100:103], a[40:43], a[16:19], v[100:103]\n" \
"v_add_u32 v213, s74, v211\n" \
"v_add_u32 v214, s74, v212\n" \
"v_mfma_f32_16x16x32_bf16 v[100:103], a[44:47], a[20:23], v[100:103]\n" \
"s_waitcnt lgkmcnt(3)\n" \
"v_xor_b32 v215, 32, v214\n" \
"v_mfma_f32_16x16x32_bf16 v[112:115], a[32:35], a[24:27], v[112:115]\n" \
"v_mfma_f32_16x16x32_bf16 v[112:115], a[36:39], a[28:31], v[112:115]\n" \
"global_load_lds_dwordx4 v250, s[36:37] offset:3072 sc1\n" \
"v_mfma_f32_16x16x32_bf16 v[116:119], a[40:43], a[24:27], v[116:119]\n" \
"s_waitcnt vmcnt(23) lgkmcnt(0)\n" \
"s_add_u32 s36, s36, 0x1000\n" \
"v_mfma_f32_16x16x32_bf16 v[116:119], a[44:47], a[28:31], v[116:119]\n" \
"s_barrier\n" \
"v_mfma_f32_16x16x32_bf16 v[72:75], a[48:51], a[0:3], v[72:75]\n" \
"s_add_u32 m0, m0, 0x5000\n" \
"ds_read_b64_tr_b16 a[32:33], v214 offset:16384\n" \
"v_mfma_f32_16x16x32_bf16 v[72:75], a[52:55], a[4:7], v[72:75]\n" \
"ds_read_b64_tr_b16 a[34:35], v214 offset:18432\n" \
"global_load_lds_dwordx4 v216, s[38:39] offset:-4096\n" \
"v_mfma_f32_16x16x32_bf16 v[76:79], a[56:59], a[0:3], v[76:79]\n" \
"ds_read_b64_tr_b16 a[36:37], v214 offset:24576\n" \
"v_mfma_f32_16x16x32_bf16 v[76:79], a[60:63], a[4:7], v[76:79]\n" \
"ds_read_b128 a[0:3], v213 offset:0\n" \
"v_mfma_f32_16x16x32_bf16 v[88:91], a[48:51], a[8:11], v[88:91]\n" \
"ds_read_b128 a[4:7], v213 offset:2048\n" \
"v_mfma_f32_16x16x32_bf16 v[88:91], a[52:55], a[12:15], v[88:91]\n" \
"ds_read_b64_tr_b16 a[38:39], v214 offset:26624\n" \
"global_load_lds_dwordx4 v217, s[38:39] offset:-3072\n" \
"v_mfma_f32_16x16x32_bf16 v[92:95], a[56:59], a[8:11], v[92:95]\n" \
"ds_read_b64_tr_b16 a[40:41], v215 offset:16384\n" \
"v_mfma_f32_16x16x32_bf16 v[92:95], a[60:63], a[12:15], v[92:95]\n" \
"ds_read_b128 a[8:11], v213 offset:256\n" \
"v_mfma_f32_16x16x32_bf16 v[104:107], a[48:51], a[16:19], v[104:107]\n" \
"ds_read_b128 a[12:15], v213 offset:2304\n" \
"v_mfma_f32_16x16x32_bf16 v[104:107], a[52:55], a[20:23], v[104:107]\n" \
"ds_read_b64_tr_b16 a[42:43], v215 offset:18432\n" \
"global_load_lds_dwordx4 v218, s[38:39] offset:-2048\n" \
"v_mfma_f32_16x16x32_bf16 v[108:111], a[56:59], a[16:19], v[108:111]\n" \
"ds_read_b64_tr_b16 a[44:45], v215 offset:24576\n" \
"v_mfma_f32_16x16x32_bf16 v[108:111], a[60:63], a[20:23], v[108:111]\n" \
"ds_read_b128 a[16:19], v213 offset:4096\n" \
"v_mfma_f32_16x16x32_bf16 v[120:123], a[48:51], a[24:27], v[120:123]\n" \
"ds_read_b128 a[20:23], v213 offset:6144\n" \
"global_load_lds_dwordx4 v219, s[38:39] offset:-1024\n" \
"v_mfma_f32_16x16x32_bf16 v[120:123], a[52:55], a[28:31], v[120:123]\n" \
"s_add_u32 s38, s38, 0x20000\n" \
"s_addc_u32 s39, s39, 0\n" \
"ds_read_b64_tr_b16 a[46:47], v215 offset:26624\n" \
"v_mfma_f32_16x16x32_bf16 v[124:127], a[56:59], a[24:27], v[124:127]\n" \
"s_sub_u32 s75, s75, 32768\n" \
"s_cselect_b32 s75, 131072, s75\n" \
"v_mfma_f32_16x16x32_bf16 v[124:127], a[60:63], a[28:31], v[124:127]\n" \
"s_waitcnt lgkmcnt(3)\n" \
"v_mfma_f32_16x16x32_bf16 v[64:67], a[32:35], a[0:3], v[64:67]\n" \
"ds_read_b128 a[24:27], v213 offset:4352\n" \
"s_add_u32 m0, s75, s95\n" \
"v_mfma_f32_16x16x32_bf16 v[64:67], a[36:39], a[4:7], v[64:67]\n" \
"s_waitcnt lgkmcnt(1)\n" \
"ds_read_b128 a[28:31], v213 offset:6400\n" \
"global_load_lds_dwordx4 v250, s[36:37] offset:0 sc1\n" \
"v_mfma_f32_16x16x32_bf16 v[68:71], a[40:43], a[0:3], v[68:71]\n" \
"s_sub_u32 s74, s74, 32768\n" \
"s_cselect_b32 s74, 131072, s74\n" \
"ds_read_b64_tr_b16 a[48:49], v214 offset:16896\n" \
"v_mfma_f32_16x16x32_bf16 v[68:71], a[44:47], a[4:7], v[68:71]\n" \
"ds_read_b64_tr_b16 a[50:51], v214 offset:18944\n" \
"v_mfma_f32_16x16x32_bf16 v[80:83], a[32:35], a[8:11], v[80:83]\n" \
"ds_read_b64_tr_b16 a[52:53], v214 offset:25088\n" \
"v_mfma_f32_16x16x32_bf16 v[80:83], a[36:39], a[12:15], v[80:83]\n" \
"ds_read_b64_tr_b16 a[54:55], v214 offset:27136\n" \
"global_load_lds_dwordx4 v250, s[36:37] offset:1024 sc1\n" \
"v_mfma_f32_16x16x32_bf16 v[84:87], a[40:43], a[8:11], v[84:87]\n" \
"ds_read_b64_tr_b16 a[56:57], v215 offset:16896\n" \
"v_mfma_f32_16x16x32_bf16 v[84:87], a[44:47], a[12:15], v[84:87]\n" \
"ds_read_b64_tr_b16 a[58:59], v215 offset:18944\n" \
"v_mfma_f32_16x16x32_bf16 v[96:99], a[32:35], a[16:19], v[96:99]\n" \
"ds_read_b64_tr_b16 a[60:61], v215 offset:25088\n" \
"v_mfma_f32_16x16x32_bf16 v[96:99], a[36:39], a[20:23], v[96:99]\n" \
"ds_read_b64_tr_b16 a[62:63], v215 offset:27136\n" \
"global_load_lds_dwordx4 v250, s[36:37] offset:2048 sc1\n" \
"v_mfma_f32_16x16x32_bf16 v[100:103], a[40:43], a[16:19], v[100:103]\n" \
"v_add_u32 v213, s74, v211\n" \
"v_add_u32 v214, s74, v212\n" \
"v_mfma_f32_16x16x32_bf16 v[100:103], a[44:47], a[20:23], v[100:103]\n" \
"s_waitcnt lgkmcnt(3)\n" \
"v_xor_b32 v215, 32, v214\n" \
"v_mfma_f32_16x16x32_bf16 v[112:115], a[32:35], a[24:27], v[112:115]\n" \
"v_mfma_f32_16x16x32_bf16 v[112:115], a[36:39], a[28:31], v[112:115]\n" \
"global_load_lds_dwordx4 v250, s[36:37] offset:3072 sc1\n" \
"v_mfma_f32_16x16x32_bf16 v[116:119], a[40:43], a[24:27], v[116:119]\n" \
"s_waitcnt vmcnt(20) lgkmcnt(0)\n" \
"s_add_u32 s36, s36, 0x1000\n" \
"v_mfma_f32_16x16x32_bf16 v[116:119], a[44:47], a[28:31], v[116:119]\n" \
"s_barrier\n" \
"v_mfma_f32_16x16x32_bf16 v[72:75], a[48:51], a[0:3], v[72:75]\n" \
"s_add_u32 m0, m0, 0x5000\n" \
"ds_read_b64_tr_b16 a[32:33], v214 offset:16384\n" \
"buffer_store_dword v224, v225, s[56:59], s79 offen sc1\n" \
"v_mfma_f32_16x16x32_bf16 v[72:75], a[52:55], a[4:7], v[72:75]\n" \
"ds_read_b64_tr_b16 a[34:35], v214 offset:18432\n" \
"global_load_lds_dwordx4 v216, s[38:39] offset:-4096\n" \
"v_mfma_f32_16x16x32_bf16 v[76:79], a[56:59], a[0:3], v[76:79]\n" \
"ds_read_b64_tr_b16 a[36:37], v214 offset:24576\n" \
"v_mfma_f32_16x16x32_bf16 v[76:79], a[60:63], a[4:7], v[76:79]\n" \
"ds_read_b128 a[0:3], v213 offset:0\n" \
"v_mfma_f32_16x16x32_bf16 v[88:91], a[48:51], a[8:11], v[88:91]\n" \
"ds_read_b128 a[4:7], v213 offset:2048\n" \
"v_mfma_f32_16x16x32_bf16 v[88:91], a[52:55], a[12:15], v[88:91]\n" \
"ds_read_b64_tr_b16 a[38:39], v214 offset:26624\n" \
"global_load_lds_dwordx4 v217, s[38:39] offset:-3072\n" \
"v_mfma_f32_16x16x32_bf16 v[92:95], a[56:59], a[8:11], v[92:95]\n" \
"ds_read_b64_tr_b16 a[40:41], v215 offset:16384\n" \
"v_mfma_f32_16x16x32_bf16 v[92:95], a[60:63], a[12:15], v[92:95]\n" \
"ds_read_b128 a[8:11], v213 offset:256\n" \
"v_mfma_f32_16x16x32_bf16 v[104:107], a[48:51], a[16:19], v[104:107]\n" \
"ds_read_b128 a[12:15], v213 offset:2304\n" \
"v_mfma_f32_16x16x32_bf16 v[104:107], a[52:55], a[20:23], v[104:107]\n" \
"ds_read_b64_tr_b16 a[42:43], v215 offset:18432\n" \
"global_load_lds_dwordx4 v218, s[38:39] offset:-2048\n" \
"v_mfma_f32_16x16x32_bf16 v[108:111], a[56:59], a[16:19], v[108:111]\n" \
"ds_read_b64_tr_b16 a[44:45], v215 offset:24576\n" \
"v_mfma_f32_16x16x32_bf16 v[108:111], a[60:63], a[20:23], v[108:111]\n" \
"ds_read_b128 a[16:19], v213 offset:4096\n" \
"v_mfma_f32_16x16x32_bf16 v[120:123], a[48:51], a[24:27], v[120:123]\n" \
"ds_read_b128 a[20:23], v213 offset:6144\n" \
"global_load_lds_dwordx4 v219, s[38:39] offset:-1024\n" \
"v_mfma_f32_16x16x32_bf16 v[120:123], a[52:55], a[28:31], v[120:123]\n" \
"s_add_u32 s38, s38, 0x20000\n" \
"s_addc_u32 s39, s39, 0\n" \
"ds_read_b64_tr_b16 a[46:47], v215 offset:26624\n" \
"v_mfma_f32_16x16x32_bf16 v[124:127], a[56:59], a[24:27], v[124:127]\n" \
"s_sub_u32 s75, s75, 32768\n" \
"s_cselect_b32 s75, 131072, s75\n" \
"v_mfma_f32_16x16x32_bf16 v[124:127], a[60:63], a[28:31], v[124:127]\n" \
"s_waitcnt lgkmcnt(3)\n" \
"s_mov_b32 s72, 6\n" \
"s_call_b64 s[68:69], Lo64_%=\n" \
"v_mfma_f32_16x16x32_bf16 v[64:67], a[32:35], a[0:3], v[64:67]\n" \
"ds_read_b128 a[24:27], v213 offset:4352\n" \
"v_mfma_f32_16x16x32_bf16 v[64:67], a[36:39], a[4:7], v[64:67]\n" \
"s_waitcnt lgkmcnt(1)\n" \
"ds_read_b128 a[28:31], v213 offset:6400\n" \
"v_mfma_f32_16x16x32_bf16 v[68:71], a[40:43], a[0:3], v[68:71]\n" \
"s_sub_u32 s74, s74, 32768\n" \
"s_cselect_b32 s74, 131072, s74\n" \
"ds_read_b64_tr_b16 a[48:49], v214 offset:16896\n" \
"v_mfma_f32_16x16x32_bf16 v[68:71], a[44:47], a[4:7], v[68:71]\n" \
"ds_read_b64_tr_b16 a[50:51], v214 offset:18944\n" \
"v_mfma_f32_16x16x32_bf16 v[80:83], a[32:35], a[8:11], v[80:83]\n" \
"ds_read_b64_tr_b16 a[52:53], v214 offset:25088\n" \
"v_mfma_f32_16x16x32_bf16 v[80:83], a[36:39], a[12:15], v[80:83]\n" \
"ds_read_b64_tr_b16 a[54:55], v214 offset:27136\n" \
"v_mfma_f32_16x16x32_bf16 v[84:87], a[40:43], a[8:11], v[84:87]\n" \
"ds_read_b64_tr_b16 a[56:57], v215 offset:16896\n" \
"v_mfma_f32_16x16x32_bf16 v[84:87], a[44:47], a[12:15], v[84:87]\n" \
"ds_read_b64_tr_b16 a[58:59], v215 offset:18944\n" \
"v_mfma_f32_16x16x32_bf16 v[96:99], a[32:35], a[16:19], v[96:99]\n" \
"ds_read_b64_tr_b16 a[60:61], v215 offset:25088\n" \
"v_mfma_f32_16x16x32_bf16 v[96:99], a[36:39], a[20:23], v[96:99]\n" \
"Lpg13337_%=:\n" \
"s_cbranch_execz Lcb1_%=\n" \
"ds_read_b64_tr_b16 a[62:63], v215 offset:27136\n" \
"v_mfma_f32_16x16x32_bf16 v[100:103], a[40:43], a[16:19], v[100:103]\n" \
"v_add_u32 v213, s74, v211\n" \
"v_add_u32 v214, s74, v212\n" \
"v_mfma_f32_16x16x32_bf16 v[100:103], a[44:47], a[20:23], v[100:103]\n" \
"s_waitcnt lgkmcnt(3)\n" \
"v_xor_b32 v215, 32, v214\n" \
"v_mfma_f32_16x16x32_bf16 v[112:115], a[32:35], a[24:27], v[112:115]\n" \
"v_mfma_f32_16x16x32_bf16 v[112:115], a[36:39], a[28:31], v[112:115]\n" \
"v_mfma_f32_16x16x32_bf16 v[116:119], a[40:43], a[24:27], v[116:119]\n" \
"s_waitcnt vmcnt(16) lgkmcnt(0)\n" \
"v_mfma_f32_16x16x32_bf16 v[116:119], a[44:47], a[28:31], v[116:119]\n" \
"s_barrier\n" \
"v_mfma_f32_16x16x32_bf16 v[72:75], a[48:51], a[0:3], v[72:75]\n" \
"s_lshl_b32 s83, s95, 1\n" \
"s_add_u32 s83, s83, s74\n" \
"ds_read_b64_tr_b16 a[32:33], v214 offset:16384\n" \
"v_mfma_f32_16x16x32_bf16 v[72:75], a[52:55], a[4:7], v[72:75]\n" \
"ds_read_b64_tr_b16 a[34:35], v214 offset:18432\n" \
"v_mfma_f32_16x16x32_bf16 v[76:79], a[56:59], a[0:3], v[76:79]\n" \
"ds_read_b64_tr_b16 a[36:37], v214 offset:24576\n" \
"v_mfma_f32_16x16x32_bf16 v[76:79], a[60:63], a[4:7], v[76:79]\n" \
"ds_read_b128 a[0:3], v213 offset:0\n" \
"v_mfma_f32_16x16x32_bf16 v[88:91], a[48:51], a[8:11], v[88:91]\n" \
"ds_read_b128 a[4:7], v213 offset:2048\n" \
"v_mfma_f32_16x16x32_bf16 v[88:91], a[52:55], a[12:15], v[88:91]\n" \
"ds_read_b64_tr_b16 a[38:39], v214 offset:26624\n" \
"v_mfma_f32_16x16x32_bf16 v[92:95], a[56:59], a[8:11], v[92:95]\n" \
"ds_read_b64_tr_b16 a[40:41], v215 offset:16384\n" \
"v_mfma_f32_16x16x32_bf16 v[92:95], a[60:63], a[12:15], v[92:95]\n" \
"ds_read_b128 a[8:11], v213 offset:256\n" \
"v_mfma_f32_16x16x32_bf16 v[104:107], a[48:51], a[16:19], v[104:107]\n" \
"ds_read_b128 a[12:15], v213 offset:2304\n" \
"v_mfma_f32_16x16x32_bf16 v[104:107], a[52:55], a[20:23], v[104:107]\n" \
"ds_read_b64_tr_b16 a[42:43], v215 offset:18432\n" \
"v_mfma_f32_16x16x32_bf16 v[108:111], a[56:59], a[16:19], v[108:111]\n" \
"ds_read_b64_tr_b16 a[44:45], v215 offset:24576\n" \
"v_mfma_f32_16x16x32_bf16 v[108:111], a[60:63], a[20:23], v[108:111]\n" \
"ds_read_b128 a[16:19], v213 offset:4096\n" \
"v_mfma_f32_16x16x32_bf16 v[120:123], a[48:51], a[24:27], v[120:123]\n" \
"ds_read_b128 a[20:23], v213 offset:6144\n" \
"v_mfma_f32_16x16x32_bf16 v[120:123], a[52:55], a[28:31], v[120:123]\n" \
"ds_read_b64_tr_b16 a[46:47], v215 offset:26624\n" \
"v_mfma_f32_16x16x32_bf16 v[124:127], a[56:59], a[24:27], v[124:127]\n" \
"v_mfma_f32_16x16x32_bf16 v[124:127], a[60:63], a[28:31], v[124:127]\n" \
"s_waitcnt lgkmcnt(3)\n" \
"v_mfma_f32_16x16x32_bf16 v[64:67], a[32:35], a[0:3], v[64:67]\n" \
"ds_read_b128 a[24:27], v213 offset:4352\n" \
"v_mfma_f32_16x16x32_bf16 v[64:67], a[36:39], a[4:7], v[64:67]\n" \
"s_waitcnt lgkmcnt(1)\n" \
"ds_read_b128 a[28:31], v213 offset:6400\n" \
"v_lshrrev_b32 v132, 4, v248\n" \
"v_mfma_f32_16x16x32_bf16 v[68:71], a[40:43], a[0:3], v[68:71]\n" \
"s_sub_u32 s74, s74, 32768\n" \
"s_cselect_b32 s74, 131072, s74\n" \
"ds_read_b64_tr_b16 a[48:49], v214 offset:16896\n" \
"v_mfma_f32_16x16x32_bf16 v[68:71], a[44:47], a[4:7], v[68:71]\n" \
"ds_read_b64_tr_b16 a[50:51], v214 offset:18944\n" \
"v_and_b32 v133, 15, v132\n" \
"v_mfma_f32_16x16x32_bf16 v[80:83], a[32:35], a[8:11], v[80:83]\n" \
"ds_read_b64_tr_b16 a[52:53], v214 offset:25088\n" \
"v_mul_u32_u24 v128, 144, v133\n" \
"v_mfma_f32_16x16x32_bf16 v[80:83], a[36:39], a[12:15], v[80:83]\n" \
"ds_read_b64_tr_b16 a[54:55], v214 offset:27136\n" \
"v_lshrrev_b32 v133, 4, v132\n" \
"v_mfma_f32_16x16x32_bf16 v[84:87], a[40:43], a[8:11], v[84:87]\n" \
"ds_read_b64_tr_b16 a[56:57], v215 offset:16896\n" \
"v_lshl_add_u32 v128, v133, 3, v128\n" \
"v_mfma_f32_16x16x32_bf16 v[84:87], a[44:47], a[12:15], v[84:87]\n" \
"ds_read_b64_tr_b16 a[58:59], v215 offset:18944\n" \
"v_add_u32 v128, s83, v128\n" \
"v_mfma_f32_16x16x32_bf16 v[96:99], a[32:35], a[16:19], v[96:99]\n" \
"ds_read_b64_tr_b16 a[60:61], v215 offset:25088\n" \
"v_lshrrev_b32 v133, 3, v132\n" \
"v_mfma_f32_16x16x32_bf16 v[96:99], a[36:39], a[20:23], v[96:99]\n" \
"ds_read_b64_tr_b16 a[62:63], v215 offset:27136\n" \
"v_and_b32 v134, 7, v132\n" \
"v_mfma_f32_16x16x32_bf16 v[100:103], a[40:43], a[16:19], v[100:103]\n" \
"v_add_u32 v213, s74, v211\n" \
"v_add_u32 v214, s74, v212\n" \
"v_mfma_f32_16x16x32_bf16 v[100:103], a[44:47], a[20:23], v[100:103]\n" \
"s_waitcnt lgkmcnt(3)\n" \
"v_xor_b32 v215, 32, v214\n" \
"v_mul_u32_u24 v129, 144, v133\n" \
"v_mfma_f32_16x16x32_bf16 v[112:115], a[32:35], a[24:27], v[112:115]\n" \
"v_lshl_add_u32 v129, v134, 4, v129\n" \
"v_add_u32 v129, s83, v129\n" \
"v_mfma_f32_16x16x32_bf16 v[112:115], a[36:39], a[28:31], v[112:115]\n" \
"v_lshlrev_b32 v134, 4, v134\n" \
"v_lshl_add_u32 v130, v133, 13, v134\n" \
"v_mfma_f32_16x16x32_bf16 v[116:119], a[40:43], a[24:27], v[116:119]\n" \
"s_waitcnt vmcnt(0) lgkmcnt(0)\n" \
"v_mfma_f32_16x16x32_bf16 v[116:119], a[44:47], a[28:31], v[116:119]\n" \
"s_barrier\n" \
"v_mfma_f32_16x16x32_bf16 v[72:75], a[48:51], a[0:3], v[72:75]\n" \
"ds_read_b128 a[192:195], v213 offset:0\n" \
"ds_read_b64_tr_b16 a[128:129], v214 offset:16384\n" \
"v_mfma_f32_16x16x32_bf16 v[72:75], a[52:55], a[4:7], v[72:75]\n" \
"s_sub_u32 s74, s74, 32768\n" \
"s_cselect_b32 s74, 131072, s74\n" \
"ds_read_b64_tr_b16 a[130:131], v214 offset:18432\n" \
"v_mfma_f32_16x16x32_bf16 v[76:79], a[56:59], a[0:3], v[76:79]\n" \
"ds_read_b128 a[196:199], v213 offset:2048\n" \
"ds_read_b64_tr_b16 a[132:133], v214 offset:24576\n" \
"v_mfma_f32_16x16x32_bf16 v[76:79], a[60:63], a[4:7], v[76:79]\n" \
"ds_read_b64_tr_b16 a[134:135], v214 offset:26624\n" \
"v_add_u32 v226, s74, v211\n" \
"v_mfma_f32_16x16x32_bf16 v[88:91], a[48:51], a[8:11], v[88:91]\n" \
"ds_read_b128 a[200:203], v213 offset:256\n" \
"ds_read_b64_tr_b16 a[136:137], v215 offset:16384\n" \
"v_mfma_f32_16x16x32_bf16 v[88:91], a[52:55], a[12:15], v[88:91]\n" \
"ds_read_b64_tr_b16 a[138:139], v215 offset:18432\n" \
"v_add_u32 v227, s74, v212\n" \
"v_mfma_f32_16x16x32_bf16 v[92:95], a[56:59], a[8:11], v[92:95]\n" \
"ds_read_b128 a[204:207], v213 offset:2304\n" \
"ds_read_b64_tr_b16 a[140:141], v215 offset:24576\n" \
"v_mfma_f32_16x16x32_bf16 v[92:95], a[60:63], a[12:15], v[92:95]\n" \
"ds_read_b64_tr_b16 a[142:143], v215 offset:26624\n" \
"v_xor_b32 v228, 32, v227\n" \
"v_mfma_f32_16x16x32_bf16 v[104:107], a[48:51], a[16:19], v[104:107]\n" \
"ds_read_b64_tr_b16 a[144:145], v214 offset:16896\n" \
"v_mfma_f32_16x16x32_bf16 v[104:107], a[52:55], a[20:23], v[104:107]\n" \
"ds_read_b64_tr_b16 a[146:147], v214 offset:18944\n" \
"v_mfma_f32_16x16x32_bf16 v[108:111], a[56:59], a[16:19], v[108:111]\n" \
"ds_read_b64_tr_b16 a[148:149], v214 offset:25088\n" \
"v_mfma_f32_16x16x32_bf16 v[108:111], a[60:63], a[20:23], v[108:111]\n" \
"ds_read_b64_tr_b16 a[150:151], v214 offset:27136\n" \
"v_mfma_f32_16x16x32_bf16 v[120:123], a[48:51], a[24:27], v[120:123]\n" \
"ds_read_b64_tr_b16 a[152:153], v215 offset:16896\n" \
"v_mfma_f32_16x16x32_bf16 v[120:123], a[52:55], a[28:31], v[120:123]\n" \
"ds_read_b64_tr_b16 a[154:155], v215 offset:18944\n" \
"v_mfma_f32_16x16x32_bf16 v[124:127], a[56:59], a[24:27], v[124:127]\n" \
"ds_read_b64_tr_b16 a[156:157], v215 offset:25088\n" \
"v_mfma_f32_16x16x32_bf16 v[124:127], a[60:63], a[28:31], v[124:127]\n" \
"s_waitcnt lgkmcnt(4)\n" \
"ds_read_b64_tr_b16 a[158:159], v215 offset:27136\n" \
"v_mfma_f32_16x16x32_bf16 v[64:67], a[128:131], a[192:195], v[64:67]\n" \
"ds_read_b128 a[224:227], v226 offset:0\n" \
"ds_read_b64_tr_b16 a[160:161], v227 offset:16384\n" \
"v_mfma_f32_16x16x32_bf16 v[64:67], a[132:135], a[196:199], v[64:67]\n" \
"ds_read_b64_tr_b16 a[162:163], v227 offset:18432\n" \
"v_mfma_f32_16x16x32_bf16 v[80:83], a[128:131], a[200:203], v[80:83]\n" \
"ds_read_b64_tr_b16 a[164:165], v227 offset:24576\n" \
"v_mfma_f32_16x16x32_bf16 v[80:83], a[132:135], a[204:207], v[80:83]\n" \
"ds_read_b128 a[228:231], v226 offset:2048\n" \
"ds_read_b64_tr_b16 a[166:167], v227 offset:26624\n" \
"v_mfma_f32_16x16x32_bf16 v[68:71], a[136:139], a[192:195], v[68:71]\n" \
"ds_read_b64_tr_b16 a[168:169], v228 offset:16384\n" \
"v_mfma_f32_16x16x32_bf16 v[68:71], a[140:143], a[196:199], v[68:71]\n" \
"ds_read_b64_tr_b16 a[170:171], v228 offset:18432\n" \
"v_mfma_f32_16x16x32_bf16 v[84:87], a[136:139], a[200:203], v[84:87]\n" \
"ds_read_b128 a[232:235], v226 offset:256\n" \
"ds_read_b64_tr_b16 a[172:173], v228 offset:24576\n" \
"v_mfma_f32_16x16x32_bf16 v[84:87], a[140:143], a[204:207], v[84:87]\n" \
"s_waitcnt lgkmcnt(6)\n" \
"ds_read_b64_tr_b16 a[174:175], v228 offset:26624\n" \
"v_mfma_f32_16x16x32_bf16 v[72:75], a[144:147], a[192:195], v[72:75]\n" \
"ds_read_b64_tr_b16 a[176:177], v227 offset:16896\n" \
"v_mfma_f32_16x16x32_bf16 v[72:75], a[148:151], a[196:199], v[72:75]\n" \
"ds_read_b128 a[236:239], v226 offset:2304\n" \
"ds_read_b64_tr_b16 a[178:179], v227 offset:18944\n" \
"v_mfma_f32_16x16x32_bf16 v[88:91], a[144:147], a[200:203], v[88:91]\n" \
"ds_read_b64_tr_b16 a[180:181], v227 offset:25088\n" \
"v_mfma_f32_16x16x32_bf16 v[88:91], a[148:151], a[204:207], v[88:91]\n" \
"ds_read_b64_tr_b16 a[182:183], v227 offset:27136\n" \
"v_mfma_f32_16x16x32_bf16 v[76:79], a[152:155], a[192:195], v[76:79]\n" \
"ds_read_b64_tr_b16 a[184:185], v228 offset:16896\n" \
"v_mfma_f32_16x16x32_bf16 v[76:79], a[156:159], a[196:199], v[76:79]\n" \
"ds_read_b64_tr_b16 a[186:187], v228 offset:18944\n" \
"v_mfma_f32_16x16x32_bf16 v[92:95], a[152:155], a[200:203], v[92:95]\n" \
"ds_read_b64_tr_b16 a[188:189], v228 offset:25088\n" \
"v_mfma_f32_16x16x32_bf16 v[92:95], a[156:159], a[204:207], v[92:95]\n" \
"s_waitcnt lgkmcnt(4)\n" \
"ds_read_b64_tr_b16 a[190:191], v228 offset:27136\n" \
"v_mfma_f32_16x16x32_bf16 v[64:67], a[160:163], a[224:227], v[64:67]\n" \
"ds_read_b128 a[208:211], v213 offset:4096\n" \
"v_mfma_f32_16x16x32_bf16 v[64:67], a[164:167], a[228:231], v[64:67]\n" \
"v_mfma_f32_16x16x32_bf16 v[80:83], a[160:163], a[232:235], v[80:83]\n" \
"ds_read_b128 a[212:215], v213 offset:6144\n" \
"v_mfma_f32_16x16x32_bf16 v[80:83], a[164:167], a[236:239], v[80:83]\n" \
"v_mfma_f32_16x16x32_bf16 v[68:71], a[168:171], a[224:227], v[68:71]\n" \
"ds_read_b128 a[216:219], v213 offset:4352\n" \
"v_cvt_pk_bf16_f32 v64, v64, v65\n" \
"v_cvt_pk_bf16_f32 v65, v66, v67\n" \
"v_mfma_f32_16x16x32_bf16 v[68:71], a[172:175], a[228:231], v[68:71]\n" \
"ds_write_b64 v128, v[64:65] offset:0\n" \
"v_mfma_f32_16x16x32_bf16 v[84:87], a[168:171], a[232:235], v[84:87]\n" \
"ds_read_b128 a[220:223], v213 offset:6400\n" \
"v_cvt_pk_bf16_f32 v80, v80, v81\n" \
"v_cvt_pk_bf16_f32 v81, v82, v83\n" \
"v_mfma_f32_16x16x32_bf16 v[84:87], a[172:175], a[236:239], v[84:87]\n" \
"s_waitcnt lgkmcnt(3)\n" \
"ds_write_b64 v128, v[80:81] offset:2304\n" \
"v_mfma_f32_16x16x32_bf16 v[72:75], a[176:179], a[224:227], v[72:75]\n" \
"ds_read_b128 a[240:243], v226 offset:4096\n" \
"v_cvt_pk_bf16_f32 v68, v68, v69\n" \
"v_mfma_f32_16x16x32_bf16 v[72:75], a[180:183], a[228:231], v[72:75]\n" \
"v_cvt_pk_bf16_f32 v69, v70, v71\n" \
"v_mfma_f32_16x16x32_bf16 v[88:91], a[176:179], a[232:235], v[88:91]\n" \
"ds_read_b128 a[244:247], v226 offset:6144\n" \
"v_mfma_f32_16x16x32_bf16 v[88:91], a[180:183], a[236:239], v[88:91]\n" \
"ds_write_b64 v128, v[68:69] offset:32\n" \
"v_mfma_f32_16x16x32_bf16 v[76:79], a[184:187], a[224:227], v[76:79]\n" \
"ds_read_b128 a[248:251], v226 offset:4352\n" \
"v_cvt_pk_bf16_f32 v84, v84, v85\n" \
"v_mfma_f32_16x16x32_bf16 v[76:79], a[188:191], a[228:231], v[76:79]\n" \
"v_cvt_pk_bf16_f32 v85, v86, v87\n" \
"v_mfma_f32_16x16x32_bf16 v[92:95], a[184:187], a[232:235], v[92:95]\n" \
"ds_read_b128 a[252:255], v226 offset:6400\n" \
"v_mfma_f32_16x16x32_bf16 v[92:95], a[188:191], a[236:239], v[92:95]\n" \
"ds_write_b64 v128, v[84:85] offset:2336\n" \
"v_mfma_f32_16x16x32_bf16 v[96:99], a[128:131], a[208:211], v[96:99]\n" \
"v_cvt_pk_bf16_f32 v72, v72, v73\n" \
"v_cvt_pk_bf16_f32 v73, v74, v75\n" \
"v_mfma_f32_16x16x32_bf16 v[96:99], a[132:135], a[212:215], v[96:99]\n" \
"s_waitcnt lgkmcnt(2)\n" \
"ds_write_b64 v128, v[72:73] offset:64\n" \
"v_mfma_f32_16x16x32_bf16 v[112:115], a[128:131], a[216:219], v[112:115]\n" \
"v_cvt_pk_bf16_f32 v88, v88, v89\n" \
"v_cvt_pk_bf16_f32 v89, v90, v91\n" \
"v_mfma_f32_16x16x32_bf16 v[112:115], a[132:135], a[220:223], v[112:115]\n" \
"ds_write_b64 v128, v[88:89] offset:2368\n" \
"v_mfma_f32_16x16x32_bf16 v[100:103], a[136:139], a[208:211], v[100:103]\n" \
"v_cvt_pk_bf16_f32 v76, v76, v77\n" \
"v_cvt_pk_bf16_f32 v77, v78, v79\n" \
"v_mfma_f32_16x16x32_bf16 v[100:103], a[140:143], a[212:215], v[100:103]\n" \
"ds_write_b64 v128, v[76:77] offset:96\n" \
"v_mfma_f32_16x16x32_bf16 v[116:119], a[136:139], a[216:219], v[116:119]\n" \
"v_cvt_pk_bf16_f32 v92, v92, v93\n" \
"v_cvt_pk_bf16_f32 v93, v94, v95\n" \
"v_mfma_f32_16x16x32_bf16 v[116:119], a[140:143], a[220:223], v[116:119]\n" \
"ds_write_b64 v128, v[92:93] offset:2400\n" \
"v_mfma_f32_16x16x32_bf16 v[104:107], a[144:147], a[208:211], v[104:107]\n" \
"ds_read_b128 v[140:143], v129 offset:0\n" \
"v_mfma_f32_16x16x32_bf16 v[104:107], a[148:151], a[212:215], v[104:107]\n" \
"ds_read_b128 v[144:147], v129 offset:1152\n" \
"v_mfma_f32_16x16x32_bf16 v[120:123], a[144:147], a[216:219], v[120:123]\n" \
"ds_read_b128 v[148:151], v129 offset:2304\n" \
"v_mfma_f32_16x16x32_bf16 v[120:123], a[148:151], a[220:223], v[120:123]\n" \
"ds_read_b128 v[152:155], v129 offset:3456\n" \
"v_mfma_f32_16x16x32_bf16 v[108:111], a[152:155], a[208:211], v[108:111]\n" \
"v_mfma_f32_16x16x32_bf16 v[108:111], a[156:159], a[212:215], v[108:111]\n" \
"v_mfma_f32_16x16x32_bf16 v[124:127], a[152:155], a[216:219], v[124:127]\n" \
"v_mfma_f32_16x16x32_bf16 v[124:127], a[156:159], a[220:223], v[124:127]\n" \
"s_add_u32 s70, s77, 0\n" \
"s_waitcnt lgkmcnt(3)\n" \
"v_mfma_f32_16x16x32_bf16 v[96:99], a[160:163], a[240:243], v[96:99]\n" \
"buffer_store_dwordx4 v[140:143], v130, s[52:55], s70 offen sc1\n" \
"v_mfma_f32_16x16x32_bf16 v[96:99], a[164:167], a[244:247], v[96:99]\n" \
"v_mfma_f32_16x16x32_bf16 v[112:115], a[160:163], a[248:251], v[112:115]\n" \
"v_mfma_f32_16x16x32_bf16 v[112:115], a[164:167], a[252:255], v[112:115]\n" \
"v_mfma_f32_16x16x32_bf16 v[100:103], a[168:171], a[240:243], v[100:103]\n" \
"s_add_u32 s70, s77, 65536\n" \
"s_waitcnt lgkmcnt(2)\n" \
"v_mfma_f32_16x16x32_bf16 v[100:103], a[172:175], a[244:247], v[100:103]\n" \
"buffer_store_dwordx4 v[144:147], v130, s[52:55], s70 offen sc1\n" \
"v_mfma_f32_16x16x32_bf16 v[116:119], a[168:171], a[248:251], v[116:119]\n" \
"v_mfma_f32_16x16x32_bf16 v[116:119], a[172:175], a[252:255], v[116:119]\n" \
"v_mfma_f32_16x16x32_bf16 v[104:107], a[176:179], a[240:243], v[104:107]\n" \
"v_mfma_f32_16x16x32_bf16 v[104:107], a[180:183], a[244:247], v[104:107]\n" \
"s_add_u32 s70, s77, 4096\n" \
"s_waitcnt lgkmcnt(1)\n" \
"v_mfma_f32_16x16x32_bf16 v[120:123], a[176:179], a[248:251], v[120:123]\n" \
"buffer_store_dwordx4 v[148:151], v130, s[52:55], s70 offen sc1\n" \
"v_mfma_f32_16x16x32_bf16 v[120:123], a[180:183], a[252:255], v[120:123]\n" \
"v_mfma_f32_16x16x32_bf16 v[108:111], a[184:187], a[240:243], v[108:111]\n" \
"v_mfma_f32_16x16x32_bf16 v[108:111], a[188:191], a[244:247], v[108:111]\n" \
"v_mfma_f32_16x16x32_bf16 v[124:127], a[184:187], a[248:251], v[124:127]\n" \
"s_add_u32 s70, s77, 69632\n" \
"s_waitcnt lgkmcnt(0)\n" \
"v_mfma_f32_16x16x32_bf16 v[124:127], a[188:191], a[252:255], v[124:127]\n" \
"buffer_store_dwordx4 v[152:155], v130, s[52:55], s70 offen sc1\n" \
"v_cvt_pk_bf16_f32 v96, v96, v97\n" \
"v_cvt_pk_bf16_f32 v97, v98, v99\n" \
"v_cvt_pk_bf16_f32 v112, v112, v113\n" \
"v_cvt_pk_bf16_f32 v113, v114, v115\n" \
"v_cvt_pk_bf16_f32 v100, v100, v101\n" \
"v_cvt_pk_bf16_f32 v101, v102, v103\n" \
"v_cvt_pk_bf16_f32 v116, v116, v117\n" \
"v_cvt_pk_bf16_f32 v117, v118, v119\n" \
"v_cvt_pk_bf16_f32 v104, v104, v105\n" \
"v_cvt_pk_bf16_f32 v105, v106, v107\n" \
"v_cvt_pk_bf16_f32 v120, v120, v121\n" \
"v_cvt_pk_bf16_f32 v121, v122, v123\n" \
"ds_write_b64 v128, v[96:97] offset:0\n" \
"ds_write_b64 v128, v[112:113] offset:2304\n" \
"ds_write_b64 v128, v[100:101] offset:32\n" \
"ds_write_b64 v128, v[116:117] offset:2336\n" \
"ds_write_b64 v128, v[104:105] offset:64\n" \
"ds_write_b64 v128, v[120:121] offset:2368\n" \
"v_cvt_pk_bf16_f32 v108, v108, v109\n" \
"v_cvt_pk_bf16_f32 v109, v110, v111\n" \
"v_cvt_pk_bf16_f32 v124, v124, v125\n" \
"v_cvt_pk_bf16_f32 v125, v126, v127\n" \
"ds_write_b64 v128, v[108:109] offset:96\n" \
"ds_write_b64 v128, v[124:125] offset:2400\n" \
"ds_read_b128 v[156:159], v129 offset:0\n" \
"ds_read_b128 v[160:163], v129 offset:1152\n" \
"ds_read_b128 v[164:167], v129 offset:2304\n" \
"ds_read_b128 v[168:171], v129 offset:3456\n" \
"s_add_u32 s70, s77, 131072\n" \
"s_waitcnt lgkmcnt(3)\n" \
"buffer_store_dwordx4 v[156:159], v130, s[52:55], s70 offen sc1\n" \
"s_add_u32 s70, s77, 196608\n" \
"s_waitcnt lgkmcnt(2)\n" \
"buffer_store_dwordx4 v[160:163], v130, s[52:55], s70 offen sc1\n" \
"s_add_u32 s70, s77, 135168\n" \
"s_waitcnt lgkmcnt(1)\n" \
"buffer_store_dwordx4 v[164:167], v130, s[52:55], s70 offen sc1\n" \
"s_add_u32 s70, s77, 200704\n" \
"s_waitcnt lgkmcnt(0)\n" \
"buffer_store_dwordx4 v[168:171], v130, s[52:55], s70 offen sc1\n" \
"s_endpgm\n" \
"Lpg13651_%=:\n" \
"s_cbranch_execz Lcb1_%=\n" \
"Lo64_%=:\n" \
"v_mfma_f32_16x16x32_bf16 v[64:67], a[32:35], a[0:3], v[64:67]\n" \
"ds_read_b128 a[24:27], v213 offset:4352\n" \
"s_add_u32 m0, s75, s95\n" \
"v_mfma_f32_16x16x32_bf16 v[64:67], a[36:39], a[4:7], v[64:67]\n" \
"s_waitcnt lgkmcnt(1)\n" \
"ds_read_b128 a[28:31], v213 offset:6400\n" \
"global_load_lds_dwordx4 v250, s[36:37] offset:0 sc1\n" \
"v_mfma_f32_16x16x32_bf16 v[68:71], a[40:43], a[0:3], v[68:71]\n" \
"s_sub_u32 s74, s74, 32768\n" \
"s_cselect_b32 s74, 131072, s74\n" \
"ds_read_b64_tr_b16 a[48:49], v214 offset:16896\n" \
"v_mfma_f32_16x16x32_bf16 v[68:71], a[44:47], a[4:7], v[68:71]\n" \
"ds_read_b64_tr_b16 a[50:51], v214 offset:18944\n" \
"v_mfma_f32_16x16x32_bf16 v[80:83], a[32:35], a[8:11], v[80:83]\n" \
"ds_read_b64_tr_b16 a[52:53], v214 offset:25088\n" \
"v_mfma_f32_16x16x32_bf16 v[80:83], a[36:39], a[12:15], v[80:83]\n" \
"ds_read_b64_tr_b16 a[54:55], v214 offset:27136\n" \
"global_load_lds_dwordx4 v250, s[36:37] offset:1024 sc1\n" \
"v_mfma_f32_16x16x32_bf16 v[84:87], a[40:43], a[8:11], v[84:87]\n" \
"ds_read_b64_tr_b16 a[56:57], v215 offset:16896\n" \
"v_mfma_f32_16x16x32_bf16 v[84:87], a[44:47], a[12:15], v[84:87]\n" \
"ds_read_b64_tr_b16 a[58:59], v215 offset:18944\n" \
"v_mfma_f32_16x16x32_bf16 v[96:99], a[32:35], a[16:19], v[96:99]\n" \
"ds_read_b64_tr_b16 a[60:61], v215 offset:25088\n" \
"v_mfma_f32_16x16x32_bf16 v[96:99], a[36:39], a[20:23], v[96:99]\n" \
"ds_read_b64_tr_b16 a[62:63], v215 offset:27136\n" \
"global_load_lds_dwordx4 v250, s[36:37] offset:2048 sc1\n" \
"v_mfma_f32_16x16x32_bf16 v[100:103], a[40:43], a[16:19], v[100:103]\n" \
"v_add_u32 v213, s74, v211\n" \
"v_add_u32 v214, s74, v212\n" \
"v_mfma_f32_16x16x32_bf16 v[100:103], a[44:47], a[20:23], v[100:103]\n" \
"s_waitcnt lgkmcnt(3)\n" \
"v_xor_b32 v215, 32, v214\n" \
"v_mfma_f32_16x16x32_bf16 v[112:115], a[32:35], a[24:27], v[112:115]\n" \
"v_mfma_f32_16x16x32_bf16 v[112:115], a[36:39], a[28:31], v[112:115]\n" \
"global_load_lds_dwordx4 v250, s[36:37] offset:3072 sc1\n" \
"v_mfma_f32_16x16x32_bf16 v[116:119], a[40:43], a[24:27], v[116:119]\n" \
"s_waitcnt vmcnt(20) lgkmcnt(0)\n" \
"s_add_u32 s36, s36, 0x1000\n" \
"v_mfma_f32_16x16x32_bf16 v[116:119], a[44:47], a[28:31], v[116:119]\n" \
"s_barrier\n" \
"v_mfma_f32_16x16x32_bf16 v[72:75], a[48:51], a[0:3], v[72:75]\n" \
"s_add_u32 m0, m0, 0x5000\n" \
"ds_read_b64_tr_b16 a[32:33], v214 offset:16384\n" \
"v_mfma_f32_16x16x32_bf16 v[72:75], a[52:55], a[4:7], v[72:75]\n" \
"ds_read_b64_tr_b16 a[34:35], v214 offset:18432\n" \
"global_load_lds_dwordx4 v216, s[38:39] offset:-4096\n" \
"v_mfma_f32_16x16x32_bf16 v[76:79], a[56:59], a[0:3], v[76:79]\n" \
"ds_read_b64_tr_b16 a[36:37], v214 offset:24576\n" \
"v_mfma_f32_16x16x32_bf16 v[76:79], a[60:63], a[4:7], v[76:79]\n" \
"ds_read_b128 a[0:3], v213 offset:0\n" \
"v_mfma_f32_16x16x32_bf16 v[88:91], a[48:51], a[8:11], v[88:91]\n" \
"ds_read_b128 a[4:7], v213 offset:2048\n" \
"v_mfma_f32_16x16x32_bf16 v[88:91], a[52:55], a[12:15], v[88:91]\n" \
"ds_read_b64_tr_b16 a[38:39], v214 offset:26624\n" \
"global_load_lds_dwordx4 v217, s[38:39] offset:-3072\n" \
"v_mfma_f32_16x16x32_bf16 v[92:95], a[56:59], a[8:11], v[92:95]\n" \
"ds_read_b64_tr_b16 a[40:41], v215 offset:16384\n" \
"v_mfma_f32_16x16x32_bf16 v[92:95], a[60:63], a[12:15], v[92:95]\n" \
"ds_read_b128 a[8:11], v213 offset:256\n" \
"v_mfma_f32_16x16x32_bf16 v[104:107], a[48:51], a[16:19], v[104:107]\n" \
"ds_read_b128 a[12:15], v213 offset:2304\n" \
"v_mfma_f32_16x16x32_bf16 v[104:107], a[52:55], a[20:23], v[104:107]\n" \
"ds_read_b64_tr_b16 a[42:43], v215 offset:18432\n" \
"global_load_lds_dwordx4 v218, s[38:39] offset:-2048\n" \
"v_mfma_f32_16x16x32_bf16 v[108:111], a[56:59], a[16:19], v[108:111]\n" \
"ds_read_b64_tr_b16 a[44:45], v215 offset:24576\n" \
"v_mfma_f32_16x16x32_bf16 v[108:111], a[60:63], a[20:23], v[108:111]\n" \
"ds_read_b128 a[16:19], v213 offset:4096\n" \
"global_load_lds_dwordx4 v219, s[38:39] offset:-1024\n" \
"v_mfma_f32_16x16x32_bf16 v[120:123], a[48:51], a[24:27], v[120:123]\n" \
"s_add_u32 s38, s38, 0x20000\n" \
"s_addc_u32 s39, s39, 0\n" \
"ds_read_b128 a[20:23], v213 offset:6144\n" \
"v_mfma_f32_16x16x32_bf16 v[120:123], a[52:55], a[28:31], v[120:123]\n" \
"s_sub_u32 s75, s75, 32768\n" \
"s_cselect_b32 s75, 131072, s75\n" \
"ds_read_b64_tr_b16 a[46:47], v215 offset:26624\n" \
"v_mfma_f32_16x16x32_bf16 v[124:127], a[56:59], a[24:27], v[124:127]\n" \
"s_sub_u32 s72, s72, 1\n" \
"s_cmp_lg_u32 s72, 0\n" \
"v_mfma_f32_16x16x32_bf16 v[124:127], a[60:63], a[28:31], v[124:127]\n" \
"s_waitcnt lgkmcnt(3)\n" \
"s_cbranch_scc1 Lo64_%=\n" \
"s_setpc_b64 s[68:69]\n" \
"Lhot_end_%=:\n" \
"Lmask0_%=:\n" \
"Lpg13740_%=:\n" \
"s_cbranch_execz Lcb0_%=\n" \
"s_waitcnt lgkmcnt(0)\n" \
"v_subrev_u32 v0, 0, v251\n" \
"v_subrev_u32 v1, 1, v251\n" \
"v_subrev_u32 v2, 2, v251\n" \
"v_subrev_u32 v3, 3, v251\n" \
"v_subrev_u32 v4, 16, v251\n" \
"v_subrev_u32 v5, 17, v251\n" \
"v_subrev_u32 v6, 18, v251\n" \
"v_subrev_u32 v7, 19, v251\n" \
"v_subrev_u32 v32, 32, v251\n" \
"v_subrev_u32 v33, 33, v251\n" \
"v_subrev_u32 v34, 34, v251\n" \
"v_subrev_u32 v35, 35, v251\n" \
"v_subrev_u32 v36, 48, v251\n" \
"v_subrev_u32 v37, 49, v251\n" \
"v_subrev_u32 v38, 50, v251\n" \
"v_subrev_u32 v39, 51, v251\n" \
"v_ashrrev_i32 v0, 31, v0\n" \
"v_ashrrev_i32 v1, 31, v1\n" \
"v_ashrrev_i32 v2, 31, v2\n" \
"v_ashrrev_i32 v3, 31, v3\n" \
"v_ashrrev_i32 v4, 31, v4\n" \
"v_ashrrev_i32 v5, 31, v5\n" \
"v_ashrrev_i32 v6, 31, v6\n" \
"v_ashrrev_i32 v7, 31, v7\n" \
"v_ashrrev_i32 v32, 31, v32\n" \
"v_ashrrev_i32 v33, 31, v33\n" \
"v_ashrrev_i32 v34, 31, v34\n" \
"v_ashrrev_i32 v35, 31, v35\n" \
"v_ashrrev_i32 v36, 31, v36\n" \
"v_ashrrev_i32 v37, 31, v37\n" \
"v_ashrrev_i32 v38, 31, v38\n" \
"v_ashrrev_i32 v39, 31, v39\n" \
"v_and_b32 v0, 0xff800000, v0\n" \
"v_and_b32 v1, 0xff800000, v1\n" \
"v_and_b32 v2, 0xff800000, v2\n" \
"v_and_b32 v3, 0xff800000, v3\n" \
"v_and_b32 v4, 0xff800000, v4\n" \
"v_and_b32 v5, 0xff800000, v5\n" \
"v_and_b32 v6, 0xff800000, v6\n" \
"v_and_b32 v7, 0xff800000, v7\n" \
"v_and_b32 v32, 0xff800000, v32\n" \
"v_and_b32 v33, 0xff800000, v33\n" \
"v_and_b32 v34, 0xff800000, v34\n" \
"v_and_b32 v35, 0xff800000, v35\n" \
"v_and_b32 v36, 0xff800000, v36\n" \
"v_and_b32 v37, 0xff800000, v37\n" \
"v_and_b32 v38, 0xff800000, v38\n" \
"v_and_b32 v39, 0xff800000, v39\n" \
"s_branch Lmask0r_%=\n" \
"Lpl1_%=:\n" \
"s_mov_b32 s31, 0\n" \
"Lpk1_%=:\n" \
"s_sleep 2\n" \
"buffer_load_dword v244, v245, s[56:59], s91 offen sc1\n" \
"s_waitcnt vmcnt(0)\n" \
"v_cmp_ne_u32 vcc, s60, v244\n" \
"s_nop 1\n" \
"s_and_b32 vcc_hi, vcc_lo, 255\n" \
"s_cbranch_scc0 Lpr1_%=\n" \
"s_add_u32 s31, s31, 1\n" \
"s_cmp_lt_u32 s31, 0x40000\n" \
"s_cbranch_scc1 Lpk1_%=\n" \
"s_branch Lpr1_%=\n" \
"Lpl2_%=:\n" \
"s_mov_b32 s31, 0\n" \
"Lpk2_%=:\n" \
"s_sleep 2\n" \
"buffer_load_dword v246, v245, s[56:59], s93 offen sc1\n" \
"s_waitcnt vmcnt(0)\n" \
"v_cmp_eq_u32 vcc, s60, v246\n" \
"s_nop 1\n" \
"s_and_b32 s30, vcc_lo, 3\n" \
"s_cbranch_scc1 Lpr2_%=\n" \
"s_add_u32 s31, s31, 1\n" \
"s_cmp_lt_u32 s31, 0x40000\n" \
"s_cbranch_scc1 Lpk2_%=\n" \
"s_branch Lpr2_%=\n" \
"Lrs0_%=:\n" \
"s_call_b64 s[68:69], Lrest_%=\n" \
"s_branch Lrr0_%=\n" \
"Lrest_%=:\n" \
"s_mov_b32 s31, 0\n" \
"s_waitcnt vmcnt(0)\n" \
"Lrk_%=:\n" \
"v_lshrrev_b32 v253, 2, v248\n" \
"s_sleep 2\n" \
"buffer_load_dword v253, v253, s[56:59], s79 offen sc1\n" \
"s_waitcnt vmcnt(0)\n" \
"v_cmp_ne_u32 s[52:53], s60, v253\n" \
"s_add_u32 s31, s31, 1\n" \
"s_cmp_lt_u32 s31, 0x40000\n" \
"s_cselect_b32 s54, 0xff, 0\n" \
"s_and_b32 s52, s52, s54\n" \
"s_cmp_lg_u32 s52, 0\n" \
"s_cbranch_scc1 Lrk_%=\n" \
"s_mov_b64 s[10:11], 0\n" \
"s_setpc_b64 s[68:69]\n" \
"Lrs1_%=:\n" \
"s_call_b64 s[68:69], Lrest_%=\n" \
"s_branch Lrr1_%=\n" \
"Lrs2_%=:\n" \
"s_call_b64 s[68:69], Lrest_%=\n" \
"s_branch Lrr2_%=\n" \
"Lrs3_%=:\n" \
"s_call_b64 s[68:69], Lrest_%=\n" \
"s_branch Lrr3_%=\n" \
"LF7_%=:\n" \
"v_mfma_f32_16x16x32_bf16 v[16:19], a[192:195], a[160:163], v[232:235]\n" \
"s_bfe_u32 s88, %1, 0x20001\n" \
"ds_read_b128 v[192:195], v246 offset:16384\n" \
"v_exp_f32 v104, v104\n" \
"s_lshl_b32 s88, s88, 2\n" \
"v_mfma_f32_16x16x32_bf16 v[16:19], a[196:199], a[164:167], v[16:19]\n" \
"s_lshr_b32 s70, %1, 6\n" \
"ds_read_b128 v[196:199], v246 offset:18432\n" \
"v_cvt_pk_bf16_f32 v91, v94, v95\n" \
"v_add_f32 v241, v241, v104\n" \
"v_exp_f32 v105, v105\n" \
"v_mfma_f32_16x16x32_bf16 v[24:27], a[192:195], a[176:179], v[236:239]\n" \
"s_add_u32 s88, s88, s70\n" \
"ds_read_b128 v[200:203], v246 offset:16640\n" \
"v_mov_b32 v78, v240\n" \
"v_add_f32 v241, v241, v105\n" \
"v_bfe_u32 v69, v248, 8, 1\n" \
"v_bfe_u32 v70, v248, 6, 2\n" \
"s_lshr_b32 s71, s22, 7\n" \
"v_exp_f32 v106, v106\n" \
"v_mfma_f32_16x16x32_bf16 v[24:27], a[196:199], a[180:183], v[24:27]\n" \
"s_sub_u32 s71, s63, s71\n" \
"ds_read_b128 a[252:255], v246 offset:14592\n" \
"v_add_f32 v241, v241, v106\n" \
"v_cvt_pk_bf16_f32 v104, v104, v105\n" \
"v_exp_f32 v107, v107\n" \
"s_add_u32 s71, s71, 6\n" \
"v_mfma_f32_16x16x32_bf16 v[20:23], a[200:203], a[160:163], v[232:235]\n" \
"s_and_b32 s71, s71, 7\n" \
"ds_read_b128 v[204:207], v246 offset:18688\n" \
"v_add_f32 v241, v241, v107\n" \
"v_lshl_add_u32 v70, v69, 3, v70\n" \
"v_mul_u32_u24 v70, 6144, v70\n" \
"v_exp_f32 v108, v108\n" \
"v_permlane16_swap_b32 v78, v240\n" \
"v_add_f32 v240, v240, v78\n" \
"v_mov_b32 v78, v240\n" \
"v_mfma_f32_16x16x32_bf16 v[20:23], a[204:207], a[164:167], v[20:23]\n" \
"s_and_b32 s70, %1, 1\n" \
"ds_read_b128 v[208:211], v246 offset:24576\n" \
"v_add_f32 v241, v241, v108\n" \
"v_cvt_pk_bf16_f32 v105, v106, v107\n" \
"v_exp_f32 v109, v109\n" \
"s_lshl_b32 s70, s70, 3\n" \
"v_mfma_f32_16x16x32_bf16 v[28:31], a[200:203], a[176:179], v[236:239]\n" \
"s_add_u32 s70, s70, s71\n" \
"ds_read_b128 v[212:215], v246 offset:26624\n" \
"v_add_f32 v241, v241, v109\n" \
"v_lshlrev_b32 v69, 5, v69\n" \
"v_and_b32 v71, 0x30, v248\n" \
"s_lshl_b32 s87, s70, 12\n" \
"v_exp_f32 v110, v110\n" \
"v_permlane32_swap_b32 v78, v240\n" \
"v_add_f32 v240, v240, v78\n" \
"v_rcp_f32 v78, v240\n" \
"v_add_u32 v79, 0x1000, v249\n" \
"v_mul_f32 v240, 0x4d800000, v240\n" \
"v_accvgpr_read_b32 v96, a0\n" \
"v_mfma_f32_16x16x32_bf16 v[28:31], a[204:207], a[180:183], v[28:31]\n" \
"s_lshl_b32 s70, s70, 7\n" \
"s_mul_i32 s84, s61, 6144\n" \
"ds_read_b128 v[216:219], v246 offset:24832\n" \
"v_accvgpr_read_b32 v97, a1\n" \
"v_accvgpr_read_b32 v98, a2\n" \
"v_accvgpr_read_b32 v99, a3\n" \
"v_accvgpr_read_b32 v100, a4\n" \
"v_accvgpr_read_b32 v101, a5\n" \
"v_accvgpr_read_b32 v102, a6\n" \
"v_add_f32 v241, v241, v110\n" \
"v_cvt_pk_bf16_f32 v106, v108, v109\n" \
"v_exp_f32 v111, v111\n" \
"v_mfma_f32_16x16x32_bf16 v[48:51], a[208:211], a[160:163], v[232:235]\n" \
"s_lshl_b32 s71, s84, 4\n" \
"ds_read_b128 v[220:223], v246 offset:26880\n" \
"v_accvgpr_read_b32 v103, a7\n" \
"v_mul_f32 v96, v96, v78\n" \
"v_mul_f32 v97, v97, v78\n" \
"v_mul_f32 v98, v98, v78\n" \
"v_mul_f32 v99, v99, v78\n" \
"v_mul_f32 v100, v100, v78\n" \
"v_add_f32 v241, v241, v111\n" \
"v_xor_b32 v69, v69, v71\n" \
"v_exp_f32 v112, v112\n" \
"v_mfma_f32_16x16x32_bf16 v[48:51], a[212:215], a[164:167], v[48:51]\n" \
"ds_read_b128 a[192:195], v247 offset:16384\n" \
"v_lshrrev_b32 v71, 9, v248\n" \
"v_mul_f32 v101, v101, v78\n" \
"v_mul_f32 v102, v102, v78\n" \
"v_mul_f32 v103, v103, v78\n" \
"v_fma_f32 v240, v96, v97, v240\n" \
"v_cvt_pk_bf16_f32 v224, v96, v97\n" \
"v_fma_f32 v240, v98, v99, v240\n" \
"v_add_f32 v242, v242, v112\n" \
"v_cvt_pk_bf16_f32 v107, v110, v111\n" \
"v_exp_f32 v113, v113\n" \
"v_mfma_f32_16x16x32_bf16 v[56:59], a[208:211], a[176:179], v[236:239]\n" \
"s_add_u32 s70, s70, s71\n" \
"s_add_u32 s70, s70, 4096\n" \
"ds_read_b128 a[196:199], v247 offset:24576\n" \
"v_cvt_pk_bf16_f32 v225, v98, v99\n" \
"v_fma_f32 v240, v100, v101, v240\n" \
"v_cvt_pk_bf16_f32 v226, v100, v101\n" \
"v_fma_f32 v240, v102, v103, v240\n" \
"v_cvt_pk_bf16_f32 v227, v102, v103\n" \
"v_add_f32 v242, v242, v113\n" \
"v_exp_f32 v114, v114\n" \
"v_mfma_f32_16x16x32_bf16 v[56:59], a[212:215], a[180:183], v[56:59]\n" \
"s_waitcnt lgkmcnt(4)\n" \
"buffer_store_dwordx4 v[224:227], v249, s[56:59], s77 offen offset:0 sc1\n" \
"ds_read_b128 a[200:203], v247 offset:17408\n" \
"v_lshl_add_u32 v70, v71, 6, v70\n" \
"v_add_f32 v242, v242, v114\n" \
"v_cvt_pk_bf16_f32 v112, v112, v113\n" \
"v_accvgpr_read_b32 v96, a8\n" \
"v_accvgpr_read_b32 v97, a9\n" \
"v_accvgpr_read_b32 v98, a10\n" \
"v_accvgpr_read_b32 v99, a11\n" \
"v_accvgpr_read_b32 v100, a12\n" \
"v_exp_f32 v115, v115\n" \
"v_mfma_f32_16x16x32_bf16 v[8:11], v[192:195], a[176:179], v[236:239]\n" \
"ds_read_b128 a[204:207], v247 offset:25600\n" \
"v_add_u32 v253, v69, v70\n" \
"v_accvgpr_read_b32 v101, a13\n" \
"v_accvgpr_read_b32 v102, a14\n" \
"v_accvgpr_read_b32 v103, a15\n" \
"v_mul_f32 v96, v96, v78\n" \
"v_mul_f32 v97, v97, v78\n" \
"v_mul_f32 v98, v98, v78\n" \
"v_add_f32 v242, v242, v115\n" \
"v_exp_f32 v116, v116\n" \
"v_mfma_f32_16x16x32_bf16 v[8:11], v[196:199], a[180:183], v[8:11]\n" \
"s_add_u32 s48, s22, s70\n" \
"s_addc_u32 s49, s23, 0\n" \
"ds_read_b128 a[208:211], v247 offset:18432\n" \
"v_mul_f32 v99, v99, v78\n" \
"v_mul_f32 v100, v100, v78\n" \
"v_mul_f32 v101, v101, v78\n" \
"v_mul_f32 v102, v102, v78\n" \
"v_mul_f32 v103, v103, v78\n" \
"v_fma_f32 v240, v96, v97, v240\n" \
"v_add_f32 v242, v242, v116\n" \
"v_cvt_pk_bf16_f32 v113, v114, v115\n" \
"v_exp_f32 v117, v117\n" \
"v_mfma_f32_16x16x32_bf16 v[52:55], a[216:219], a[160:163], v[232:235]\n" \
"s_add_u32 s50, s48, 23552\n" \
"s_addc_u32 s51, s49, 0\n" \
"ds_read_b128 a[212:215], v247 offset:26624\n" \
"v_cvt_pk_bf16_f32 v228, v96, v97\n" \
"v_fma_f32 v240, v98, v99, v240\n" \
"v_cvt_pk_bf16_f32 v229, v98, v99\n" \
"v_fma_f32 v240, v100, v101, v240\n" \
"v_cvt_pk_bf16_f32 v230, v100, v101\n" \
"v_fma_f32 v240, v102, v103, v240\n" \
"v_add_f32 v242, v242, v117\n" \
"v_exp_f32 v118, v118\n" \
"v_mfma_f32_16x16x32_bf16 v[52:55], a[220:223], a[164:167], v[52:55]\n" \
"s_lshr_b32 s89, s22, 7\n" \
"ds_read_b128 v[192:195], v246 offset:20480\n" \
"v_cvt_pk_bf16_f32 v231, v102, v103\n" \
"v_add_f32 v242, v242, v118\n" \
"v_cvt_pk_bf16_f32 v114, v116, v117\n" \
"s_sub_u32 s89, 3, s89\n" \
"v_mfma_f32_16x16x32_bf16 v[60:63], a[216:219], a[176:179], v[236:239]\n" \
"buffer_store_dwordx4 v[228:231], v249, s[56:59], s77 offen offset:2048 sc1\n" \
"ds_read_b128 v[196:199], v246 offset:22528\n" \
"v_bfe_u32 v84, v248, 7, 2\n" \
"v_accvgpr_read_b32 v96, a16\n" \
"v_accvgpr_read_b32 v97, a17\n" \
"v_accvgpr_read_b32 v98, a18\n" \
"v_accvgpr_read_b32 v99, a19\n" \
"v_accvgpr_read_b32 v100, a20\n" \
"v_mfma_f32_16x16x32_bf16 v[60:63], a[220:223], a[180:183], v[60:63]\n" \
"s_and_b32 s89, s89, 7\n" \
"s_and_b32 s90, %1, 1\n" \
"ds_read_b128 a[216:219], v247 offset:19456\n" \
"v_accvgpr_read_b32 v101, a21\n" \
"v_accvgpr_read_b32 v102, a22\n" \
"v_accvgpr_read_b32 v103, a23\n" \
"v_mul_f32 v96, v96, v78\n" \
"v_mul_f32 v97, v97, v78\n" \
"v_mul_f32 v98, v98, v78\n" \
"v_mfma_f32_16x16x32_bf16 v[12:15], v[200:203], a[176:179], v[236:239]\n" \
"ds_read_b128 a[220:223], v247 offset:27648\n" \
"v_bfe_u32 v85, v248, 5, 2\n" \
"v_mul_f32 v99, v99, v78\n" \
"v_mul_f32 v100, v100, v78\n" \
"v_mul_f32 v101, v101, v78\n" \
"v_mul_f32 v102, v102, v78\n" \
"v_mul_f32 v103, v103, v78\n" \
"v_fma_f32 v240, v96, v97, v240\n" \
"v_mfma_f32_16x16x32_bf16 v[12:15], v[204:207], a[180:183], v[12:15]\n" \
"s_lshl_b32 s90, s90, 3\n" \
"s_add_u32 s89, s89, s90\n" \
"ds_read_b128 v[200:203], v246 offset:20736\n" \
"v_cvt_pk_bf16_f32 v224, v96, v97\n" \
"v_fma_f32 v240, v98, v99, v240\n" \
"v_cvt_pk_bf16_f32 v225, v98, v99\n" \
"v_fma_f32 v240, v100, v101, v240\n" \
"v_cvt_pk_bf16_f32 v226, v100, v101\n" \
"v_fma_f32 v240, v102, v103, v240\n" \
"v_mfma_f32_16x16x32_bf16 v[16:19], a[224:227], a[168:171], v[16:19]\n" \
"ds_read_b128 v[204:207], v246 offset:22784\n" \
"v_min_u32 v85, 2, v85\n" \
"v_cvt_pk_bf16_f32 v227, v102, v103\n" \
"v_mfma_f32_16x16x32_bf16 v[16:19], a[228:231], a[172:175], v[16:19]\n" \
"s_lshl_b32 s89, s89, 7\n" \
"s_lshr_b32 s90, %1, 3\n" \
"v_mad_u32_u24 v84, v84, 3, v85\n" \
"v_mfma_f32_16x16x32_bf16 v[24:27], a[224:227], a[184:187], v[24:27]\n" \
"s_lshl_b32 s90, s90, 2\n" \
"s_add_u32 s90, s90, s61\n" \
"v_and_b32 v85, 16, v248\n" \
"v_mfma_f32_16x16x32_bf16 v[24:27], a[228:231], a[188:191], v[24:27]\n" \
"s_mul_i32 s90, s90, 24576\n" \
"ds_read_b128 a[224:227], v247 offset:20480\n" \
"s_add_u32 s89, s89, s90\n" \
"v_mfma_f32_16x16x32_bf16 v[40:43], v[208:211], a[176:179], v[236:239]\n" \
"ds_read_b128 a[228:231], v247 offset:28672\n" \
"v_lshl_add_u32 v85, v85, 2, s89\n" \
"v_mfma_f32_16x16x32_bf16 v[40:43], v[212:215], a[180:183], v[40:43]\n" \
"ds_read_b128 v[208:211], v246 offset:28672\n" \
"v_lshl_add_u32 v84, v84, 11, v85\n" \
"v_mfma_f32_16x16x32_bf16 v[20:23], a[232:235], a[168:171], v[20:23]\n" \
"s_add_u32 m0, s84, 143360\n" \
"ds_read_b128 v[212:215], v246 offset:30720\n" \
"v_exp_f32 v119, v119\n" \
"v_mfma_f32_16x16x32_bf16 v[20:23], a[236:239], a[172:175], v[20:23]\n" \
"global_load_lds_dwordx4 v253, s[48:49] offset:-4096\n" \
"v_add_f32 v242, v242, v119\n" \
"v_mfma_f32_16x16x32_bf16 v[28:31], a[232:235], a[184:187], v[28:31]\n" \
"v_exp_f32 v120, v120\n" \
"v_mfma_f32_16x16x32_bf16 v[28:31], a[236:239], a[188:191], v[28:31]\n" \
"s_waitcnt lgkmcnt(2)\n" \
"ds_read_b128 a[232:235], v247 offset:21504\n" \
"v_add_f32 v243, v243, v120\n" \
"v_cvt_pk_bf16_f32 v115, v118, v119\n" \
"v_mfma_f32_16x16x32_bf16 v[44:47], v[216:219], a[176:179], v[236:239]\n" \
"ds_read_b128 a[236:239], v247 offset:29696\n" \
"global_load_lds_dwordx4 v253, s[48:49] offset:-2048\n" \
"v_mfma_f32_16x16x32_bf16 v[44:47], v[220:223], a[180:183], v[44:47]\n" \
"ds_read_b128 v[216:219], v246 offset:28928\n" \
"v_exp_f32 v121, v121\n" \
"v_mfma_f32_16x16x32_bf16 v[48:51], a[240:243], a[168:171], v[48:51]\n" \
"ds_read_b128 v[220:223], v246 offset:30976\n" \
"v_add_f32 v243, v243, v121\n" \
"v_mfma_f32_16x16x32_bf16 v[48:51], a[244:247], a[172:175], v[48:51]\n" \
"global_load_lds_dwordx4 v253, s[48:49] offset:0\n" \
"v_exp_f32 v122, v122\n" \
"v_mfma_f32_16x16x32_bf16 v[56:59], a[240:243], a[184:187], v[56:59]\n" \
"s_add_u32 s48, s48, 0x120000\n" \
"s_addc_u32 s49, s49, 0\n" \
"v_add_f32 v243, v243, v122\n" \
"v_mfma_f32_16x16x32_bf16 v[56:59], a[244:247], a[188:191], v[56:59]\n" \
"ds_read_b128 a[240:243], v247 offset:22528\n" \
"v_cvt_pk_bf16_f32 v120, v120, v121\n" \
"v_mfma_f32_16x16x32_bf16 v[8:11], v[192:195], a[184:187], v[8:11]\n" \
"ds_read_b128 a[244:247], v247 offset:30720\n" \
"global_load_lds_dwordx4 v253, s[50:51] offset:-3072\n" \
"v_mfma_f32_16x16x32_bf16 v[8:11], v[196:199], a[188:191], v[8:11]\n" \
"v_exp_f32 v123, v123\n" \
"v_mfma_f32_16x16x32_bf16 v[52:55], a[248:251], a[168:171], v[52:55]\n" \
"v_add_f32 v243, v243, v123\n" \
"v_exp_f32 v124, v124\n" \
"v_mfma_f32_16x16x32_bf16 v[52:55], a[252:255], a[172:175], v[52:55]\n" \
"global_load_lds_dwordx4 v253, s[50:51] offset:-1024\n" \
"v_add_f32 v243, v243, v124\n" \
"v_cvt_pk_bf16_f32 v121, v122, v123\n" \
"v_mfma_f32_16x16x32_bf16 v[60:63], a[248:251], a[184:187], v[60:63]\n" \
"v_exp_f32 v125, v125\n" \
"v_mfma_f32_16x16x32_bf16 v[60:63], a[252:255], a[188:191], v[60:63]\n" \
"ds_read_b128 a[248:251], v247 offset:23552\n" \
"v_add_f32 v243, v243, v125\n" \
"v_mfma_f32_16x16x32_bf16 v[12:15], v[200:203], a[184:187], v[12:15]\n" \
"ds_read_b128 a[252:255], v247 offset:31744\n" \
"global_load_lds_dwordx4 v253, s[50:51] offset:1024\n" \
"v_mfma_f32_16x16x32_bf16 v[12:15], v[204:207], a[188:191], v[12:15]\n" \
"s_waitcnt lgkmcnt(2)\n" \
"s_add_u32 s50, s50, 0x120000\n" \
"s_addc_u32 s51, s51, 0\n" \
"v_mfma_f32_16x16x32_bf16 v[40:43], v[208:211], a[184:187], v[40:43]\n" \
"v_exp_f32 v126, v126\n" \
"v_mfma_f32_16x16x32_bf16 v[40:43], v[212:215], a[188:191], v[40:43]\n" \
"global_load_dword v85, v84, s[22:23]\n" \
"v_mfma_f32_16x16x32_bf16 v[44:47], v[216:219], a[184:187], v[44:47]\n" \
"s_waitcnt vmcnt(9) lgkmcnt(0)\n" \
"v_add_u32 v247, 0x10000, v248\n" \
"v_add_f32 v243, v243, v126\n" \
"v_mfma_f32_16x16x32_bf16 v[44:47], v[220:223], a[188:191], v[44:47]\n" \
"s_barrier\n" \
"v_cvt_pk_bf16_f32 v122, v124, v125\n" \
"v_mfma_f32_16x16x32_bf16 a[32:35], a[192:195], v[72:75], a[32:35]\n" \
"v_exp_f32 v127, v127\n" \
"v_mfma_f32_16x16x32_bf16 a[32:35], a[196:199], v[104:107], a[32:35]\n" \
"v_add_f32 v243, v243, v127\n" \
"v_cvt_pk_bf16_f32 v123, v126, v127\n" \
"v_mfma_f32_16x16x32_bf16 v[128:131], a[192:195], v[80:83], v[128:131]\n" \
"ds_read_b128 v[192:195], v247 offset:16384\n" \
"v_lshrrev_b32 v71, 7, v248\n" \
"v_cmp_gt_i32 vcc, 0, v251\n" \
"v_cmp_gt_i32 s[64:65], 1, v251\n" \
"v_cmp_gt_i32 s[66:67], 2, v251\n" \
"v_cndmask_b32 v8, v8, v254, vcc\n" \
"v_cndmask_b32 v16, v16, v254, vcc\n" \
"v_cmp_gt_i32 vcc, 3, v251\n" \
"v_mfma_f32_16x16x32_bf16 v[128:131], a[196:199], v[112:115], v[128:131]\n" \
"v_cndmask_b32 v9, v9, v254, s[64:65]\n" \
"v_cndmask_b32 v17, v17, v254, s[64:65]\n" \
"v_cmp_gt_i32 s[64:65], 16, v251\n" \
"v_cndmask_b32 v10, v10, v254, s[66:67]\n" \
"v_cndmask_b32 v18, v18, v254, s[66:67]\n" \
"v_cmp_gt_i32 s[66:67], 17, v251\n" \
"v_and_b32 v76, 0x70, v248\n" \
"s_lshl_b32 s70, s88, 18\n" \
"v_mfma_f32_16x16x32_bf16 v[160:163], a[192:195], v[88:91], v[160:163]\n" \
"s_lshl_b32 s71, s61, 16\n" \
"s_add_u32 s71, s71, s70\n" \
"v_cndmask_b32 v11, v11, v254, vcc\n" \
"v_cndmask_b32 v19, v19, v254, vcc\n" \
"v_cmp_gt_i32 vcc, 18, v251\n" \
"v_cndmask_b32 v12, v12, v254, s[64:65]\n" \
"v_cndmask_b32 v20, v20, v254, s[64:65]\n" \
"v_cmp_gt_i32 s[64:65], 19, v251\n" \
"v_lshlrev_b32 v77, 4, v71\n" \
"v_mfma_f32_16x16x32_bf16 v[160:163], a[196:199], v[120:123], v[160:163]\n" \
"ds_read_b128 a[192:195], v247 offset:0\n" \
"ds_read_b128 v[196:199], v247 offset:24576\n" \
"v_cndmask_b32 v13, v13, v254, s[66:67]\n" \
"v_cndmask_b32 v21, v21, v254, s[66:67]\n" \
"v_cmp_gt_i32 s[66:67], 32, v251\n" \
"v_cndmask_b32 v14, v14, v254, vcc\n" \
"v_cndmask_b32 v22, v22, v254, vcc\n" \
"v_cmp_gt_i32 vcc, 33, v251\n" \
"v_mfma_f32_16x16x32_bf16 a[36:39], a[200:203], v[72:75], a[36:39]\n" \
"v_xor_b32 v77, v77, v76\n" \
"v_cndmask_b32 v15, v15, v254, s[64:65]\n" \
"v_cndmask_b32 v23, v23, v254, s[64:65]\n" \
"v_cmp_gt_i32 s[64:65], 34, v251\n" \
"v_cndmask_b32 v40, v40, v254, s[66:67]\n" \
"v_cndmask_b32 v48, v48, v254, s[66:67]\n" \
"v_cmp_gt_i32 s[66:67], 35, v251\n" \
"v_mfma_f32_16x16x32_bf16 a[36:39], a[204:207], v[104:107], a[36:39]\n" \
"s_add_u32 s42, s20, s71\n" \
"s_addc_u32 s43, s21, 0\n" \
"ds_read_b128 a[196:199], v247 offset:8192\n" \
"v_cndmask_b32 v41, v41, v254, vcc\n" \
"v_cndmask_b32 v49, v49, v254, vcc\n" \
"v_cmp_gt_i32 vcc, 48, v251\n" \
"v_cndmask_b32 v42, v42, v254, s[64:65]\n" \
"v_cndmask_b32 v50, v50, v254, s[64:65]\n" \
"v_cmp_gt_i32 s[64:65], 49, v251\n" \
"v_mfma_f32_16x16x32_bf16 v[132:135], a[200:203], v[80:83], v[132:135]\n" \
"ds_read_b128 v[200:203], v247 offset:17408\n" \
"v_lshl_add_u32 v252, v71, 12, v77\n" \
"v_cndmask_b32 v43, v43, v254, s[66:67]\n" \
"v_cndmask_b32 v51, v51, v254, s[66:67]\n" \
"v_cmp_gt_i32 s[66:67], 50, v251\n" \
"v_cndmask_b32 v44, v44, v254, vcc\n" \
"v_cndmask_b32 v52, v52, v254, vcc\n" \
"v_cmp_gt_i32 vcc, 51, v251\n" \
"v_mfma_f32_16x16x32_bf16 v[132:135], a[204:207], v[112:115], v[132:135]\n" \
"s_add_u32 s44, s42, 31744\n" \
"s_addc_u32 s45, s43, 0\n" \
"s_max_u32 s70, s70, 32768\n" \
"v_cndmask_b32 v45, v45, v254, s[64:65]\n" \
"v_cndmask_b32 v53, v53, v254, s[64:65]\n" \
"v_cndmask_b32 v46, v46, v254, s[66:67]\n" \
"v_cndmask_b32 v54, v54, v254, s[66:67]\n" \
"v_cndmask_b32 v47, v47, v254, vcc\n" \
"v_cndmask_b32 v55, v55, v254, vcc\n" \
"v_mfma_f32_16x16x32_bf16 v[164:167], a[200:203], v[88:91], v[164:167]\n" \
"s_sub_u32 s70, s70, 32768\n" \
"v_exp_f32 v8, v8\n" \
"s_and_b32 s71, s61, 1\n" \
"s_lshl_b32 s71, s71, 2\n" \
"v_mfma_f32_16x16x32_bf16 v[164:167], a[204:207], v[120:123], v[164:167]\n" \
"ds_read_b128 a[200:203], v247 offset:1024\n" \
"buffer_store_dwordx4 v[224:227], v79, s[56:59], s77 offen offset:0 sc1\n" \
"ds_read_b128 v[204:207], v247 offset:25600\n" \
"v_add_f32 v243, v243, v8\n" \
"v_exp_f32 v9, v9\n" \
"v_accvgpr_read_b32 v96, a24\n" \
"v_accvgpr_read_b32 v97, a25\n" \
"v_mfma_f32_16x16x32_bf16 a[40:43], a[208:211], v[72:75], a[40:43]\n" \
"v_accvgpr_read_b32 v98, a26\n" \
"v_accvgpr_read_b32 v99, a27\n" \
"v_accvgpr_read_b32 v100, a28\n" \
"v_accvgpr_read_b32 v101, a29\n" \
"v_accvgpr_read_b32 v102, a30\n" \
"v_accvgpr_read_b32 v103, a31\n" \
"v_add_f32 v243, v243, v9\n" \
"v_and_or_b32 v71, v71, 3, s71\n" \
"s_lshr_b32 s71, s61, 1\n" \
"v_exp_f32 v10, v10\n" \
"v_mfma_f32_16x16x32_bf16 a[40:43], a[212:215], v[104:107], a[40:43]\n" \
"s_lshl_b32 s71, s71, 11\n" \
"ds_read_b128 a[204:207], v247 offset:9216\n" \
"v_mul_f32 v96, v96, v78\n" \
"v_mul_f32 v97, v97, v78\n" \
"v_mul_f32 v98, v98, v78\n" \
"v_mul_f32 v99, v99, v78\n" \
"v_mul_f32 v100, v100, v78\n" \
"v_mul_f32 v101, v101, v78\n" \
"v_add_f32 v243, v243, v10\n" \
"v_cvt_pk_bf16_f32 v8, v8, v9\n" \
"v_lshlrev_b32 v77, 4, v71\n" \
"v_exp_f32 v11, v11\n" \
"v_mfma_f32_16x16x32_bf16 v[136:139], a[208:211], v[80:83], v[136:139]\n" \
"s_add_u32 s70, s70, s71\n" \
"ds_read_b128 v[208:211], v247 offset:18432\n" \
"v_mul_f32 v102, v102, v78\n" \
"v_mul_f32 v103, v103, v78\n" \
"v_fma_f32 v240, v96, v97, v240\n" \
"v_cvt_pk_bf16_f32 v228, v96, v97\n" \
"v_fma_f32 v240, v98, v99, v240\n" \
"v_cvt_pk_bf16_f32 v229, v98, v99\n" \
"v_add_f32 v243, v243, v11\n" \
"v_xor_b32 v77, v77, v76\n" \
"v_exp_f32 v12, v12\n" \
"v_mfma_f32_16x16x32_bf16 v[136:139], a[212:215], v[112:115], v[136:139]\n" \
"s_add_u32 s46, s20, s70\n" \
"s_addc_u32 s47, s21, 0\n" \
"v_fma_f32 v240, v100, v101, v240\n" \
"v_cvt_pk_bf16_f32 v230, v100, v101\n" \
"v_fma_f32 v240, v102, v103, v240\n" \
"v_cvt_pk_bf16_f32 v231, v102, v103\n" \
"v_add_f32 v243, v243, v12\n" \
"v_cvt_pk_bf16_f32 v9, v10, v11\n" \
"v_lshl_add_u32 v246, v71, 12, v77\n" \
"v_exp_f32 v13, v13\n" \
"v_mfma_f32_16x16x32_bf16 v[168:171], a[208:211], v[88:91], v[168:171]\n" \
"s_lshl_b32 s85, s61, 10\n" \
"v_add_f32 v243, v243, v13\n" \
"v_exp_f32 v14, v14\n" \
"v_mfma_f32_16x16x32_bf16 v[168:171], a[212:215], v[120:123], v[168:171]\n" \
"s_add_u32 m0, s95, 118784\n" \
"ds_read_b128 a[208:211], v247 offset:2048\n" \
"buffer_store_dwordx4 v[228:231], v79, s[56:59], s77 offen offset:2048 sc1\n" \
"ds_read_b128 v[212:215], v247 offset:26624\n" \
"v_add_f32 v243, v243, v14\n" \
"v_cvt_pk_bf16_f32 v10, v12, v13\n" \
"v_exp_f32 v15, v15\n" \
"v_mfma_f32_16x16x32_bf16 a[44:47], a[216:219], v[72:75], a[44:47]\n" \
"global_load_lds_dwordx4 v252, s[42:43] offset:0\n" \
"v_add_f32 v243, v243, v15\n" \
"v_exp_f32 v40, v40\n" \
"v_mfma_f32_16x16x32_bf16 a[44:47], a[220:223], v[104:107], a[44:47]\n" \
"s_add_u32 s77, s77, 0x82000\n" \
"ds_read_b128 a[212:215], v247 offset:10240\n" \
"v_add_f32 v243, v243, v40\n" \
"v_cvt_pk_bf16_f32 v11, v14, v15\n" \
"v_exp_f32 v41, v41\n" \
"v_mfma_f32_16x16x32_bf16 v[140:143], a[216:219], v[80:83], v[140:143]\n" \
"ds_read_b128 v[216:219], v247 offset:19456\n" \
"v_add_f32 v243, v243, v41\n" \
"v_exp_f32 v42, v42\n" \
"v_mfma_f32_16x16x32_bf16 v[140:143], a[220:223], v[112:115], v[140:143]\n" \
"global_load_lds_dwordx4 v252, s[42:43] offset:2048\n" \
"v_add_f32 v243, v243, v42\n" \
"v_cvt_pk_bf16_f32 v40, v40, v41\n" \
"v_exp_f32 v43, v43\n" \
"v_mfma_f32_16x16x32_bf16 v[172:175], a[216:219], v[88:91], v[172:175]\n" \
"s_add_u32 s42, s42, 0x180\n" \
"s_addc_u32 s43, s43, 0\n" \
"v_add_f32 v243, v243, v43\n" \
"v_exp_f32 v44, v44\n" \
"v_mfma_f32_16x16x32_bf16 v[172:175], a[220:223], v[120:123], v[172:175]\n" \
"ds_read_b128 a[216:219], v247 offset:3072\n" \
"ds_read_b128 v[220:223], v247 offset:27648\n" \
"v_add_f32 v243, v243, v44\n" \
"v_cvt_pk_bf16_f32 v41, v42, v43\n" \
"v_exp_f32 v45, v45\n" \
"v_mfma_f32_16x16x32_bf16 a[48:51], a[224:227], v[72:75], a[48:51]\n" \
"global_load_lds_dwordx4 v252, s[44:45] offset:1024\n" \
"v_add_f32 v243, v243, v45\n" \
"v_exp_f32 v46, v46\n" \
"v_mfma_f32_16x16x32_bf16 a[48:51], a[228:231], v[104:107], a[48:51]\n" \
"ds_read_b128 a[220:223], v247 offset:11264\n" \
"v_add_f32 v243, v243, v46\n" \
"v_cvt_pk_bf16_f32 v42, v44, v45\n" \
"v_exp_f32 v47, v47\n" \
"v_mfma_f32_16x16x32_bf16 v[144:147], a[224:227], v[80:83], v[144:147]\n" \
"ds_read_b128 v[224:227], v247 offset:20480\n" \
"v_add_f32 v243, v243, v47\n" \
"v_cvt_pk_bf16_f32 v43, v46, v47\n" \
"v_exp_f32 v24, v24\n" \
"v_mfma_f32_16x16x32_bf16 v[144:147], a[228:231], v[112:115], v[144:147]\n" \
"global_load_lds_dwordx4 v252, s[44:45] offset:3072\n" \
"v_add_f32 v243, v243, v24\n" \
"v_exp_f32 v25, v25\n" \
"v_mfma_f32_16x16x32_bf16 v[176:179], a[224:227], v[88:91], v[176:179]\n" \
"s_add_u32 s44, s44, 0x180\n" \
"s_addc_u32 s45, s45, 0\n" \
"v_add_f32 v243, v243, v25\n" \
"v_exp_f32 v26, v26\n" \
"v_mfma_f32_16x16x32_bf16 v[176:179], a[228:231], v[120:123], v[176:179]\n" \
"ds_read_b128 a[224:227], v247 offset:4096\n" \
"s_add_u32 m0, s85, 135168\n" \
"ds_read_b128 v[228:231], v247 offset:28672\n" \
"v_add_f32 v243, v243, v26\n" \
"v_cvt_pk_bf16_f32 v24, v24, v25\n" \
"v_exp_f32 v27, v27\n" \
"v_mfma_f32_16x16x32_bf16 a[52:55], a[232:235], v[72:75], a[52:55]\n" \
"global_load_lds_dwordx4 v246, s[46:47]\n" \
"v_add_f32 v243, v243, v27\n" \
"v_exp_f32 v28, v28\n" \
"v_mfma_f32_16x16x32_bf16 a[52:55], a[236:239], v[104:107], a[52:55]\n" \
"s_add_u32 s46, s46, 0x180\n" \
"s_addc_u32 s47, s47, 0\n" \
"ds_read_b128 a[228:231], v247 offset:12288\n" \
"v_add_f32 v243, v243, v28\n" \
"v_cvt_pk_bf16_f32 v25, v26, v27\n" \
"v_exp_f32 v29, v29\n" \
"v_mfma_f32_16x16x32_bf16 v[148:151], a[232:235], v[80:83], v[148:151]\n" \
"ds_read_b128 v[232:235], v247 offset:21504\n" \
"v_add_f32 v243, v243, v29\n" \
"v_exp_f32 v30, v30\n" \
"v_mfma_f32_16x16x32_bf16 v[148:151], a[236:239], v[112:115], v[148:151]\n" \
"v_add_f32 v243, v243, v30\n" \
"v_cvt_pk_bf16_f32 v26, v28, v29\n" \
"v_exp_f32 v31, v31\n" \
"v_mfma_f32_16x16x32_bf16 v[180:183], a[232:235], v[88:91], v[180:183]\n" \
"v_add_f32 v243, v243, v31\n" \
"v_exp_f32 v56, v56\n" \
"v_mfma_f32_16x16x32_bf16 v[180:183], a[236:239], v[120:123], v[180:183]\n" \
"ds_read_b128 a[232:235], v247 offset:5120\n" \
"ds_read_b128 v[236:239], v247 offset:29696\n" \
"v_add_f32 v243, v243, v56\n" \
"v_cvt_pk_bf16_f32 v27, v30, v31\n" \
"v_exp_f32 v57, v57\n" \
"v_mfma_f32_16x16x32_bf16 a[56:59], a[240:243], v[72:75], a[56:59]\n" \
"v_add_f32 v243, v243, v57\n" \
"v_exp_f32 v58, v58\n" \
"v_mfma_f32_16x16x32_bf16 a[56:59], a[244:247], v[104:107], a[56:59]\n" \
"ds_read_b128 a[236:239], v247 offset:13312\n" \
"v_add_f32 v243, v243, v58\n" \
"v_cvt_pk_bf16_f32 v56, v56, v57\n" \
"v_exp_f32 v59, v59\n" \
"v_mfma_f32_16x16x32_bf16 v[152:155], a[240:243], v[80:83], v[152:155]\n" \
"ds_read_b128 v[0:3], v247 offset:22528\n" \
"v_add_f32 v243, v243, v59\n" \
"v_exp_f32 v60, v60\n" \
"v_mfma_f32_16x16x32_bf16 v[152:155], a[244:247], v[112:115], v[152:155]\n" \
"v_add_f32 v243, v243, v60\n" \
"v_cvt_pk_bf16_f32 v57, v58, v59\n" \
"v_exp_f32 v61, v61\n" \
"v_mfma_f32_16x16x32_bf16 v[184:187], a[240:243], v[88:91], v[184:187]\n" \
"v_add_f32 v243, v243, v61\n" \
"v_exp_f32 v62, v62\n" \
"v_mfma_f32_16x16x32_bf16 v[184:187], a[244:247], v[120:123], v[184:187]\n" \
"ds_read_b128 a[240:243], v247 offset:6144\n" \
"ds_read_b128 v[4:7], v247 offset:30720\n" \
"v_add_f32 v243, v243, v62\n" \
"v_cvt_pk_bf16_f32 v58, v60, v61\n" \
"v_exp_f32 v63, v63\n" \
"v_mfma_f32_16x16x32_bf16 a[60:63], a[248:251], v[72:75], a[60:63]\n" \
"v_add_f32 v243, v243, v63\n" \
"v_cvt_pk_bf16_f32 v59, v62, v63\n" \
"v_exp_f32 v16, v16\n" \
"v_mfma_f32_16x16x32_bf16 a[60:63], a[252:255], v[104:107], a[60:63]\n" \
"ds_read_b128 a[244:247], v247 offset:14336\n" \
"v_add_f32 v242, v242, v16\n" \
"v_exp_f32 v17, v17\n" \
"v_mfma_f32_16x16x32_bf16 v[156:159], a[248:251], v[80:83], v[156:159]\n" \
"ds_read_b128 v[32:35], v247 offset:23552\n" \
"v_add_f32 v242, v242, v17\n" \
"v_exp_f32 v18, v18\n" \
"v_mfma_f32_16x16x32_bf16 v[156:159], a[252:255], v[112:115], v[156:159]\n" \
"v_add_f32 v242, v242, v18\n" \
"v_cvt_pk_bf16_f32 v16, v16, v17\n" \
"v_exp_f32 v19, v19\n" \
"v_mfma_f32_16x16x32_bf16 v[188:191], a[248:251], v[88:91], v[188:191]\n" \
"v_add_f32 v242, v242, v19\n" \
"v_exp_f32 v20, v20\n" \
"v_mfma_f32_16x16x32_bf16 v[188:191], a[252:255], v[120:123], v[188:191]\n" \
"s_waitcnt lgkmcnt(2)\n" \
"ds_read_b128 a[248:251], v247 offset:7168\n" \
"ds_read_b128 v[36:39], v247 offset:31744\n" \
"v_add_f32 v242, v242, v20\n" \
"v_cvt_pk_bf16_f32 v17, v18, v19\n" \
"v_exp_f32 v21, v21\n" \
"v_mfma_f32_16x16x32_bf16 v[160:163], v[192:195], v[8:11], v[160:163]\n" \
"v_add_f32 v242, v242, v21\n" \
"v_exp_f32 v22, v22\n" \
"v_mfma_f32_16x16x32_bf16 v[160:163], v[196:199], v[40:43], v[160:163]\n" \
"ds_read_b128 a[252:255], v247 offset:15360\n" \
"v_add_f32 v242, v242, v22\n" \
"v_cvt_pk_bf16_f32 v18, v20, v21\n" \
"v_exp_f32 v23, v23\n" \
"v_mfma_f32_16x16x32_bf16 v[164:167], v[200:203], v[8:11], v[164:167]\n" \
"v_mov_b32 v112, v241\n" \
"v_add_f32 v242, v242, v23\n" \
"v_exp_f32 v48, v48\n" \
"v_mfma_f32_16x16x32_bf16 v[164:167], v[204:207], v[40:43], v[164:167]\n" \
"v_add_f32 v242, v242, v48\n" \
"v_cvt_pk_bf16_f32 v19, v22, v23\n" \
"v_mfma_f32_16x16x32_bf16 v[168:171], v[208:211], v[8:11], v[168:171]\n" \
"v_exp_f32 v49, v49\n" \
"v_permlane16_swap_b32 v112, v241\n" \
"v_add_f32 v241, v241, v112\n" \
"v_mov_b32 v112, v241\n" \
"v_mfma_f32_16x16x32_bf16 v[168:171], v[212:215], v[40:43], v[168:171]\n" \
"s_waitcnt vmcnt(0) lgkmcnt(0)\n" \
"v_add_f32 v242, v242, v49\n" \
"v_exp_f32 v50, v50\n" \
"v_mfma_f32_16x16x32_bf16 v[172:175], v[216:219], v[8:11], v[172:175]\n" \
"s_barrier\n" \
"v_add_f32 v242, v242, v50\n" \
"v_cvt_pk_bf16_f32 v48, v48, v49\n" \
"v_permlane32_swap_b32 v112, v241\n" \
"v_add_f32 v241, v241, v112\n" \
"v_rcp_f32 v112, v241\n" \
"v_add_u32 v113, 0x1000, v249\n" \
"v_mul_f32 v241, 0x4d800000, v241\n" \
"v_accvgpr_read_b32 v96, a32\n" \
"v_mfma_f32_16x16x32_bf16 v[172:175], v[220:223], v[40:43], v[172:175]\n" \
"s_add_u32 m0, s84, 90112\n" \
"v_accvgpr_read_b32 v97, a33\n" \
"v_accvgpr_read_b32 v98, a34\n" \
"v_accvgpr_read_b32 v99, a35\n" \
"v_accvgpr_read_b32 v100, a36\n" \
"v_accvgpr_read_b32 v101, a37\n" \
"v_accvgpr_read_b32 v102, a38\n" \
"v_exp_f32 v51, v51\n" \
"v_mfma_f32_16x16x32_bf16 v[176:179], v[224:227], v[8:11], v[176:179]\n" \
"global_load_lds_dwordx4 v253, s[48:49] offset:-4096\n" \
"v_add_f32 v242, v242, v51\n" \
"v_accvgpr_read_b32 v103, a39\n" \
"v_mul_f32 v96, v96, v112\n" \
"v_mul_f32 v97, v97, v112\n" \
"v_mul_f32 v98, v98, v112\n" \
"v_mul_f32 v99, v99, v112\n" \
"v_mul_f32 v100, v100, v112\n" \
"v_mfma_f32_16x16x32_bf16 v[176:179], v[228:231], v[40:43], v[176:179]\n" \
"global_load_lds_dwordx4 v253, s[48:49] offset:-2048\n" \
"v_mul_f32 v101, v101, v112\n" \
"v_mul_f32 v102, v102, v112\n" \
"v_mul_f32 v103, v103, v112\n" \
"v_fma_f32 v241, v96, v97, v241\n" \
"v_cvt_pk_bf16_f32 v104, v96, v97\n" \
"v_fma_f32 v241, v98, v99, v241\n" \
"v_exp_f32 v52, v52\n" \
"v_mfma_f32_16x16x32_bf16 v[180:183], v[232:235], v[8:11], v[180:183]\n" \
"v_add_f32 v242, v242, v52\n" \
"v_cvt_pk_bf16_f32 v49, v50, v51\n" \
"v_cvt_pk_bf16_f32 v105, v98, v99\n" \
"v_fma_f32 v241, v100, v101, v241\n" \
"v_cvt_pk_bf16_f32 v106, v100, v101\n" \
"v_fma_f32 v241, v102, v103, v241\n" \
"v_cvt_pk_bf16_f32 v107, v102, v103\n" \
"v_mfma_f32_16x16x32_bf16 v[180:183], v[236:239], v[40:43], v[180:183]\n" \
"buffer_store_dwordx4 v[104:107], v249, s[56:59], s77 offen offset:0 sc1\n" \
"global_load_lds_dwordx4 v253, s[48:49] offset:0\n" \
"v_accvgpr_read_b32 v96, a40\n" \
"v_accvgpr_read_b32 v97, a41\n" \
"v_accvgpr_read_b32 v98, a42\n" \
"v_accvgpr_read_b32 v99, a43\n" \
"v_accvgpr_read_b32 v100, a44\n" \
"v_exp_f32 v53, v53\n" \
"v_mfma_f32_16x16x32_bf16 v[184:187], v[0:3], v[8:11], v[184:187]\n" \
"s_add_u32 s48, s48, 0x300000\n" \
"s_addc_u32 s49, s49, 0\n" \
"v_add_f32 v242, v242, v53\n" \
"v_accvgpr_read_b32 v101, a45\n" \
"v_accvgpr_read_b32 v102, a46\n" \
"v_accvgpr_read_b32 v103, a47\n" \
"v_mul_f32 v96, v96, v112\n" \
"v_mul_f32 v97, v97, v112\n" \
"v_mul_f32 v98, v98, v112\n" \
"v_mfma_f32_16x16x32_bf16 v[184:187], v[4:7], v[40:43], v[184:187]\n" \
"global_load_lds_dwordx4 v253, s[50:51] offset:-3072\n" \
"v_mul_f32 v99, v99, v112\n" \
"v_mul_f32 v100, v100, v112\n" \
"v_mul_f32 v101, v101, v112\n" \
"v_mul_f32 v102, v102, v112\n" \
"v_mul_f32 v103, v103, v112\n" \
"v_fma_f32 v241, v96, v97, v241\n" \
"v_exp_f32 v54, v54\n" \
"v_mfma_f32_16x16x32_bf16 v[188:191], v[32:35], v[8:11], v[188:191]\n" \
"v_add_f32 v242, v242, v54\n" \
"v_cvt_pk_bf16_f32 v50, v52, v53\n" \
"v_cvt_pk_bf16_f32 v108, v96, v97\n" \
"v_fma_f32 v241, v98, v99, v241\n" \
"v_cvt_pk_bf16_f32 v109, v98, v99\n" \
"v_fma_f32 v241, v100, v101, v241\n" \
"v_cvt_pk_bf16_f32 v110, v100, v101\n" \
"v_fma_f32 v241, v102, v103, v241\n" \
"v_mfma_f32_16x16x32_bf16 v[188:191], v[36:39], v[40:43], v[188:191]\n" \
"global_load_lds_dwordx4 v253, s[50:51] offset:-1024\n" \
"v_cvt_pk_bf16_f32 v111, v102, v103\n" \
"v_exp_f32 v55, v55\n" \
"v_mfma_f32_16x16x32_bf16 v[160:163], a[192:195], v[24:27], v[160:163]\n" \
"buffer_store_dwordx4 v[108:111], v249, s[56:59], s77 offen offset:2048 sc1\n" \
"v_add_f32 v242, v242, v55\n" \
"v_cvt_pk_bf16_f32 v51, v54, v55\n" \
"v_accvgpr_read_b32 v96, a48\n" \
"v_accvgpr_read_b32 v97, a49\n" \
"v_accvgpr_read_b32 v98, a50\n" \
"v_accvgpr_read_b32 v99, a51\n" \
"v_accvgpr_read_b32 v100, a52\n" \
"v_mfma_f32_16x16x32_bf16 v[160:163], a[196:199], v[56:59], v[160:163]\n" \
"s_movk_i32 s89, 0xc0\n" \
"global_load_lds_dwordx4 v253, s[50:51] offset:1024\n" \
"v_accvgpr_read_b32 v101, a53\n" \
"v_accvgpr_read_b32 v102, a54\n" \
"v_accvgpr_read_b32 v103, a55\n" \
"v_mul_f32 v96, v96, v112\n" \
"v_mul_f32 v97, v97, v112\n" \
"v_mul_f32 v98, v98, v112\n" \
"v_mfma_f32_16x16x32_bf16 v[164:167], a[200:203], v[24:27], v[164:167]\n" \
"s_add_u32 s50, s50, 0x300000\n" \
"s_addc_u32 s51, s51, 0\n" \
"s_add_u32 m0, s95, 65536\n" \
"v_mul_f32 v99, v99, v112\n" \
"v_mul_f32 v100, v100, v112\n" \
"v_mul_f32 v101, v101, v112\n" \
"v_mul_f32 v102, v102, v112\n" \
"v_mul_f32 v103, v103, v112\n" \
"v_fma_f32 v241, v96, v97, v241\n" \
"v_mfma_f32_16x16x32_bf16 v[164:167], a[204:207], v[56:59], v[164:167]\n" \
"s_movk_i32 s90, 288\n" \
"global_load_lds_dwordx4 v252, s[42:43] offset:0\n" \
"v_cvt_pk_bf16_f32 v104, v96, v97\n" \
"v_fma_f32 v241, v98, v99, v241\n" \
"v_cvt_pk_bf16_f32 v105, v98, v99\n" \
"v_fma_f32 v241, v100, v101, v241\n" \
"v_cvt_pk_bf16_f32 v106, v100, v101\n" \
"v_fma_f32 v241, v102, v103, v241\n" \
"v_lshrrev_b32 v211, 4, v248\n" \
"v_lshrrev_b32 v214, 8, v248\n" \
"v_and_b32 v215, 7, v211\n" \
"v_xor_b32 v214, v214, v215\n" \
"v_mfma_f32_16x16x32_bf16 v[168:171], a[208:211], v[24:27], v[168:171]\n" \
"s_lshr_b32 s71, s61, 1\n" \
"s_lshl_b32 s70, s71, 11\n" \
"s_add_u32 s70, s70, 118784\n" \
"v_mfma_f32_16x16x32_bf16 v[168:171], a[212:215], v[56:59], v[168:171]\n" \
"global_load_lds_dwordx4 v252, s[42:43] offset:2048\n" \
"v_and_b32 v216, 15, v211\n" \
"v_mfma_f32_16x16x32_bf16 v[172:175], a[216:219], v[24:27], v[172:175]\n" \
"s_add_u32 s42, s42, 0x400\n" \
"s_addc_u32 s43, s43, 0\n" \
"v_lshlrev_b32 v213, 7, v216\n" \
"v_lshl_add_u32 v213, v214, 4, v213\n" \
"v_mfma_f32_16x16x32_bf16 v[172:175], a[220:223], v[56:59], v[172:175]\n" \
"global_load_lds_dwordx4 v252, s[44:45] offset:1024\n" \
"v_add_u32 v205, s70, v213\n" \
"v_xor_b32 v206, 64, v205\n" \
"v_lshrrev_b32 v216, 2, v216\n" \
"v_lshl_add_u32 v216, v216, 9, v213\n" \
"v_add_u32 v207, 135168, v216\n" \
"v_xor_b32 v208, 64, v207\n" \
"v_mfma_f32_16x16x32_bf16 v[176:179], a[224:227], v[24:27], v[176:179]\n" \
"s_and_b32 s70, s61, 1\n" \
"s_lshl_b32 s70, s70, 9\n" \
"s_add_u32 s70, s70, 139264\n" \
"v_mfma_f32_16x16x32_bf16 v[176:179], a[228:231], v[56:59], v[176:179]\n" \
"s_lshl_b32 s71, s71, 5\n" \
"global_load_lds_dwordx4 v252, s[44:45] offset:3072\n" \
"v_mfma_f32_16x16x32_bf16 v[180:183], a[232:235], v[24:27], v[180:183]\n" \
"s_add_u32 s44, s44, 0x400\n" \
"s_addc_u32 s45, s45, 0\n" \
"s_add_u32 m0, s85, 81920\n" \
"v_mfma_f32_16x16x32_bf16 v[180:183], a[236:239], v[56:59], v[180:183]\n" \
"global_load_lds_dwordx4 v246, s[46:47]\n" \
"v_and_b32 v213, 3, v211\n" \
"v_mfma_f32_16x16x32_bf16 v[184:187], a[240:243], v[24:27], v[184:187]\n" \
"s_add_u32 s46, s46, 0x400\n" \
"s_addc_u32 s47, s47, 0\n" \
"v_lshlrev_b32 v213, 3, v213\n" \
"v_mfma_f32_16x16x32_bf16 v[184:187], a[244:247], v[56:59], v[184:187]\n" \
"s_add_u32 m0, s95, 0\n" \
"v_and_or_b32 v213, v248, s89, v213\n" \
"v_bfe_u32 v214, v211, 4, 1\n" \
"v_mfma_f32_16x16x32_bf16 v[188:191], a[248:251], v[24:27], v[188:191]\n" \
"global_load_lds_dwordx4 v252, s[42:43] offset:0\n" \
"v_mad_u32_u24 v213, v214, s90, v213\n" \
"v_lshrrev_b32 v214, 5, v211\n" \
"v_mul_u32_u24 v214, 6144, v214\n" \
"v_add3_u32 v213, v213, v214, s70\n" \
"v_xor_b32 v209, s71, v213\n" \
"v_xor_b32 v210, 32, v209\n" \
"v_mfma_f32_16x16x32_bf16 v[188:191], a[252:255], v[56:59], v[188:191]\n" \
"ds_read_b128 a[64:67], v207 offset:0\n" \
"ds_read_b64_tr_b16 a[96:97], v209 offset:2048\n" \
"v_mfma_f32_16x16x32_bf16 v[128:131], a[192:195], v[16:19], v[128:131]\n" \
"ds_read_b64_tr_b16 a[98:99], v209 offset:3072\n" \
"v_cvt_pk_bf16_f32 v107, v102, v103\n" \
"v_mfma_f32_16x16x32_bf16 v[128:131], a[196:199], v[48:51], v[128:131]\n" \
"ds_read_b128 a[68:71], v208 offset:0\n" \
"global_load_lds_dwordx4 v252, s[42:43] offset:2048\n" \
"v_mfma_f32_16x16x32_bf16 v[132:135], a[200:203], v[16:19], v[132:135]\n" \
"ds_read_b64_tr_b16 a[100:101], v209 offset:14336\n" \
"buffer_store_dwordx4 v[104:107], v113, s[56:59], s77 offen offset:0 sc1\n" \
"ds_read_b64_tr_b16 a[102:103], v209 offset:15360\n" \
"v_accvgpr_read_b32 v96, a56\n" \
"v_accvgpr_read_b32 v97, a57\n" \
"v_accvgpr_read_b32 v98, a58\n" \
"v_accvgpr_read_b32 v99, a59\n" \
"v_mfma_f32_16x16x32_bf16 v[132:135], a[204:207], v[48:51], v[132:135]\n" \
"s_add_u32 s42, s42, 0xfffffb00\n" \
"s_addc_u32 s43, s43, -1\n" \
"ds_read_b128 a[72:75], v205 offset:0\n" \
"v_accvgpr_read_b32 v100, a60\n" \
"v_accvgpr_read_b32 v101, a61\n" \
"v_accvgpr_read_b32 v102, a62\n" \
"v_accvgpr_read_b32 v103, a63\n" \
"v_mul_f32 v96, v96, v112\n" \
"v_mul_f32 v97, v97, v112\n" \
"v_mfma_f32_16x16x32_bf16 v[136:139], a[208:211], v[16:19], v[136:139]\n" \
"ds_read_b64_tr_b16 a[104:105], v209 offset:0\n" \
"global_load_lds_dwordx4 v252, s[44:45] offset:1024\n" \
"v_mul_f32 v98, v98, v112\n" \
"v_mul_f32 v99, v99, v112\n" \
"v_mul_f32 v100, v100, v112\n" \
"v_mul_f32 v101, v101, v112\n" \
"v_mul_f32 v102, v102, v112\n" \
"v_mul_f32 v103, v103, v112\n" \
"v_mfma_f32_16x16x32_bf16 v[136:139], a[212:215], v[48:51], v[136:139]\n" \
"ds_read_b128 a[76:79], v206 offset:0\n" \
"ds_read_b64_tr_b16 a[106:107], v209 offset:1024\n" \
"v_fma_f32 v241, v96, v97, v241\n" \
"v_cvt_pk_bf16_f32 v108, v96, v97\n" \
"v_fma_f32 v241, v98, v99, v241\n" \
"v_mfma_f32_16x16x32_bf16 v[140:143], a[216:219], v[16:19], v[140:143]\n" \
"ds_read_b64_tr_b16 a[108:109], v209 offset:12288\n" \
"global_load_lds_dwordx4 v252, s[44:45] offset:3072\n" \
"v_mfma_f32_16x16x32_bf16 v[140:143], a[220:223], v[48:51], v[140:143]\n" \
"s_add_u32 s44, s44, 0xfffffb00\n" \
"s_addc_u32 s45, s45, -1\n" \
"ds_read_b64_tr_b16 a[110:111], v209 offset:13312\n" \
"v_mfma_f32_16x16x32_bf16 v[144:147], a[224:227], v[16:19], v[144:147]\n" \
"ds_read_b128 a[80:83], v205 offset:4096\n" \
"ds_read_b64_tr_b16 a[112:113], v210 offset:0\n" \
"v_mfma_f32_16x16x32_bf16 v[144:147], a[228:231], v[48:51], v[144:147]\n" \
"s_add_u32 m0, s85, 16384\n" \
"ds_read_b64_tr_b16 a[114:115], v210 offset:1024\n" \
"v_cvt_pk_bf16_f32 v109, v98, v99\n" \
"v_mfma_f32_16x16x32_bf16 v[148:151], a[232:235], v[16:19], v[148:151]\n" \
"ds_read_b64_tr_b16 a[116:117], v210 offset:12288\n" \
"global_load_lds_dwordx4 v246, s[46:47]\n" \
"v_mfma_f32_16x16x32_bf16 v[148:151], a[236:239], v[48:51], v[148:151]\n" \
"ds_read_b128 a[84:87], v206 offset:4096\n" \
"ds_read_b64_tr_b16 a[118:119], v210 offset:13312\n" \
"v_mfma_f32_16x16x32_bf16 v[152:155], a[240:243], v[16:19], v[152:155]\n" \
"ds_read_b128 a[88:91], v205 offset:8192\n" \
"v_fma_f32 v241, v100, v101, v241\n" \
"v_mfma_f32_16x16x32_bf16 v[152:155], a[244:247], v[48:51], v[152:155]\n" \
"ds_read_b128 a[92:95], v206 offset:8192\n" \
"v_cvt_pk_bf16_f32 v110, v100, v101\n" \
"v_mfma_f32_16x16x32_bf16 v[156:159], a[248:251], v[16:19], v[156:159]\n" \
"v_fma_f32 v241, v102, v103, v241\n" \
"v_cvt_pk_bf16_f32 v111, v102, v103\n" \
"v_mfma_f32_16x16x32_bf16 v[156:159], a[252:255], v[48:51], v[156:159]\n" \
"s_waitcnt lgkmcnt(4)\n" \
"buffer_store_dwordx4 v[108:111], v113, s[56:59], s77 offen offset:2048 sc1\n" \
"s_add_u32 s77, s77, 0xfff7e000\n" \
"s_branch Ljoin_%=\n" \
"Lexact_%=:\n" \
"v_mov_b32 v190, 0xff800000\n" \
"s_and_b32 s80, s61, 2\n" \
"s_lshl_b32 s80, s80, 4\n" \
"v_subrev_u32 v186, s80, v251\n" \
"v_accvgpr_read_b32 v160, a128\n" \
"v_accvgpr_read_b32 v161, a129\n" \
"v_accvgpr_read_b32 v162, a130\n" \
"v_accvgpr_read_b32 v163, a131\n" \
"v_accvgpr_read_b32 v164, a132\n" \
"v_accvgpr_read_b32 v165, a133\n" \
"v_accvgpr_read_b32 v166, a134\n" \
"v_accvgpr_read_b32 v167, a135\n" \
"v_accvgpr_read_b32 v168, a136\n" \
"v_accvgpr_read_b32 v169, a137\n" \
"v_accvgpr_read_b32 v170, a138\n" \
"v_accvgpr_read_b32 v171, a139\n" \
"v_accvgpr_read_b32 v172, a140\n" \
"v_accvgpr_read_b32 v173, a141\n" \
"v_accvgpr_read_b32 v174, a142\n" \
"v_accvgpr_read_b32 v175, a143\n" \
"s_lshl_b32 s75, s63, 2\n" \
"s_lshr_b32 s80, s61, 1\n" \
"s_add_u32 s75, s75, s80\n" \
"s_add_u32 s75, s75, 0\n" \
"s_add_u32 s76, s77, 0\n" \
"s_call_b64 s[68:69], Lex1_%=\n" \
"v_accvgpr_read_b32 v160, a144\n" \
"v_accvgpr_read_b32 v161, a145\n" \
"v_accvgpr_read_b32 v162, a146\n" \
"v_accvgpr_read_b32 v163, a147\n" \
"v_accvgpr_read_b32 v164, a148\n" \
"v_accvgpr_read_b32 v165, a149\n" \
"v_accvgpr_read_b32 v166, a150\n" \
"v_accvgpr_read_b32 v167, a151\n" \
"v_accvgpr_read_b32 v168, a152\n" \
"v_accvgpr_read_b32 v169, a153\n" \
"v_accvgpr_read_b32 v170, a154\n" \
"v_accvgpr_read_b32 v171, a155\n" \
"v_accvgpr_read_b32 v172, a156\n" \
"v_accvgpr_read_b32 v173, a157\n" \
"v_accvgpr_read_b32 v174, a158\n" \
"v_accvgpr_read_b32 v175, a159\n" \
"s_lshl_b32 s75, s63, 2\n" \
"s_lshr_b32 s80, s61, 1\n" \
"s_add_u32 s75, s75, s80\n" \
"s_add_u32 s75, s75, 2\n" \
"s_add_u32 s76, s77, 532480\n" \
"s_call_b64 s[68:69], Lex1_%=\n" \
"v_accvgpr_read_b32 v160, a160\n" \
"v_accvgpr_read_b32 v161, a161\n" \
"v_accvgpr_read_b32 v162, a162\n" \
"v_accvgpr_read_b32 v163, a163\n" \
"v_accvgpr_read_b32 v164, a164\n" \
"v_accvgpr_read_b32 v165, a165\n" \
"v_accvgpr_read_b32 v166, a166\n" \
"v_accvgpr_read_b32 v167, a167\n" \
"v_accvgpr_read_b32 v168, a168\n" \
"v_accvgpr_read_b32 v169, a169\n" \
"v_accvgpr_read_b32 v170, a170\n" \
"v_accvgpr_read_b32 v171, a171\n" \
"v_accvgpr_read_b32 v172, a172\n" \
"v_accvgpr_read_b32 v173, a173\n" \
"v_accvgpr_read_b32 v174, a174\n" \
"v_accvgpr_read_b32 v175, a175\n" \
"s_lshl_b32 s75, s63, 2\n" \
"s_sub_u32 s75, 60, s75\n" \
"s_lshr_b32 s80, s61, 1\n" \
"s_add_u32 s75, s75, s80\n" \
"s_add_u32 s75, s75, 0\n" \
"s_add_u32 s76, s78, 0\n" \
"s_call_b64 s[68:69], Lex1_%=\n" \
"v_accvgpr_read_b32 v160, a176\n" \
"v_accvgpr_read_b32 v161, a177\n" \
"v_accvgpr_read_b32 v162, a178\n" \
"v_accvgpr_read_b32 v163, a179\n" \
"v_accvgpr_read_b32 v164, a180\n" \
"v_accvgpr_read_b32 v165, a181\n" \
"v_accvgpr_read_b32 v166, a182\n" \
"v_accvgpr_read_b32 v167, a183\n" \
"v_accvgpr_read_b32 v168, a184\n" \
"v_accvgpr_read_b32 v169, a185\n" \
"v_accvgpr_read_b32 v170, a186\n" \
"v_accvgpr_read_b32 v171, a187\n" \
"v_accvgpr_read_b32 v172, a188\n" \
"v_accvgpr_read_b32 v173, a189\n" \
"v_accvgpr_read_b32 v174, a190\n" \
"v_accvgpr_read_b32 v175, a191\n" \
"s_lshl_b32 s75, s63, 2\n" \
"s_sub_u32 s75, 60, s75\n" \
"s_lshr_b32 s80, s61, 1\n" \
"s_add_u32 s75, s75, s80\n" \
"s_add_u32 s75, s75, 2\n" \
"s_add_u32 s76, s78, 532480\n" \
"s_call_b64 s[68:69], Lex1_%=\n" \
"s_branch Lexr_%=\n" \
"Lex1_%=:\n" \
"v_mov_b32 v128, 0\n" \
"v_mov_b32 v129, 0\n" \
"v_mov_b32 v130, 0\n" \
"v_mov_b32 v131, 0\n" \
"v_mov_b32 v132, 0\n" \
"v_mov_b32 v133, 0\n" \
"v_mov_b32 v134, 0\n" \
"v_mov_b32 v135, 0\n" \
"v_mov_b32 v136, 0\n" \
"v_mov_b32 v137, 0\n" \
"v_mov_b32 v138, 0\n" \
"v_mov_b32 v139, 0\n" \
"v_mov_b32 v140, 0\n" \
"v_mov_b32 v141, 0\n" \
"v_mov_b32 v142, 0\n" \
"v_mov_b32 v143, 0\n" \
"v_mov_b32 v144, 0\n" \
"v_mov_b32 v145, 0\n" \
"v_mov_b32 v146, 0\n" \
"v_mov_b32 v147, 0\n" \
"v_mov_b32 v148, 0\n" \
"v_mov_b32 v149, 0\n" \
"v_mov_b32 v150, 0\n" \
"v_mov_b32 v151, 0\n" \
"v_mov_b32 v152, 0\n" \
"v_mov_b32 v153, 0\n" \
"v_mov_b32 v154, 0\n" \
"v_mov_b32 v155, 0\n" \
"v_mov_b32 v156, 0\n" \
"v_mov_b32 v157, 0\n" \
"v_mov_b32 v158, 0\n" \
"v_mov_b32 v159, 0\n" \
"v_mov_b32 v184, 0xf149f2ca\n" \
"v_mov_b32 v185, 0\n" \
"s_mov_b32 s73, 0\n" \
"s_mul_i32 s74, s62, 557824\n" \
"Lex2_%=:\n" \
"s_add_u32 s80, s74, 4096\n" \
"buffer_load_dwordx4 a[0:3], v249, s[56:59], s74 offen offset:0 sc1\n" \
"buffer_load_dwordx4 a[4:7], v249, s[56:59], s74 offen offset:2048 sc1\n" \
"buffer_load_dwordx4 a[8:11], v249, s[56:59], s80 offen offset:0 sc1\n" \
"buffer_load_dwordx4 a[12:15], v249, s[56:59], s80 offen offset:2048 sc1\n" \
"buffer_load_dwordx4 a[16:19], v249, s[56:59], s74 offen offset:256 sc1\n" \
"buffer_load_dwordx4 a[20:23], v249, s[56:59], s74 offen offset:2304 sc1\n" \
"buffer_load_dwordx4 a[24:27], v249, s[56:59], s80 offen offset:256 sc1\n" \
"buffer_load_dwordx4 a[28:31], v249, s[56:59], s80 offen offset:2304 sc1\n" \
"s_add_u32 s80, s74, 17850368\n" \
"s_add_u32 s81, s80, 4096\n" \
"buffer_load_dwordx4 a[32:35], v248, s[56:59], s80 offen offset:0 sc1\n" \
"buffer_load_dwordx4 a[36:39], v248, s[56:59], s80 offen offset:1024 sc1\n" \
"buffer_load_dwordx4 a[40:43], v248, s[56:59], s80 offen offset:2048 sc1\n" \
"buffer_load_dwordx4 a[44:47], v248, s[56:59], s80 offen offset:3072 sc1\n" \
"buffer_load_dwordx4 a[48:51], v248, s[56:59], s81 offen offset:0 sc1\n" \
"buffer_load_dwordx4 a[52:55], v248, s[56:59], s81 offen offset:1024 sc1\n" \
"buffer_load_dwordx4 a[56:59], v248, s[56:59], s81 offen offset:2048 sc1\n" \
"buffer_load_dwordx4 a[60:63], v248, s[56:59], s81 offen offset:3072 sc1\n" \
"s_waitcnt vmcnt(0)\n" \
"v_mfma_f32_16x16x32_bf16 v[176:179], a[0:3], v[160:163], 0\n" \
"v_mfma_f32_16x16x32_bf16 v[176:179], a[4:7], v[164:167], v[176:179]\n" \
"v_mfma_f32_16x16x32_bf16 v[176:179], a[8:11], v[168:171], v[176:179]\n" \
"v_mfma_f32_16x16x32_bf16 v[176:179], a[12:15], v[172:175], v[176:179]\n" \
"v_mfma_f32_16x16x32_bf16 v[180:183], a[16:19], v[160:163], 0\n" \
"v_mfma_f32_16x16x32_bf16 v[180:183], a[20:23], v[164:167], v[180:183]\n" \
"v_mfma_f32_16x16x32_bf16 v[180:183], a[24:27], v[168:171], v[180:183]\n" \
"v_mfma_f32_16x16x32_bf16 v[180:183], a[28:31], v[172:175], v[180:183]\n" \
"s_nop 7\n" \
"s_nop 1\n" \
"s_cmp_eq_u32 s73, s75\n" \
"s_cbranch_scc0 Lex3_%=\n" \
"v_cmp_gt_i32 vcc, 0, v186\n" \
"s_nop 1\n" \
"v_cndmask_b32 v176, v176, v190, vcc\n" \
"v_cmp_gt_i32 vcc, 1, v186\n" \
"s_nop 1\n" \
"v_cndmask_b32 v177, v177, v190, vcc\n" \
"v_cmp_gt_i32 vcc, 2, v186\n" \
"s_nop 1\n" \
"v_cndmask_b32 v178, v178, v190, vcc\n" \
"v_cmp_gt_i32 vcc, 3, v186\n" \
"s_nop 1\n" \
"v_cndmask_b32 v179, v179, v190, vcc\n" \
"v_cmp_gt_i32 vcc, 16, v186\n" \
"s_nop 1\n" \
"v_cndmask_b32 v180, v180, v190, vcc\n" \
"v_cmp_gt_i32 vcc, 17, v186\n" \
"s_nop 1\n" \
"v_cndmask_b32 v181, v181, v190, vcc\n" \
"v_cmp_gt_i32 vcc, 18, v186\n" \
"s_nop 1\n" \
"v_cndmask_b32 v182, v182, v190, vcc\n" \
"v_cmp_gt_i32 vcc, 19, v186\n" \
"s_nop 1\n" \
"v_cndmask_b32 v183, v183, v190, vcc\n" \
"Lex3_%=:\n" \
"v_max3_f32 v187, v176, v177, v178\n" \
"v_max3_f32 v187, v187, v179, v180\n" \
"v_max3_f32 v187, v187, v181, v182\n" \
"v_max3_f32 v187, v187, v183, v184\n" \
"v_mov_b32 v188, v187\n" \
"s_nop 1\n" \
"v_permlane16_swap_b32 v188, v187\n" \
"v_max_f32 v187, v187, v188\n" \
"v_mov_b32 v188, v187\n" \
"s_nop 1\n" \
"v_permlane32_swap_b32 v188, v187\n" \
"v_max_f32 v187, v187, v188\n" \
"v_sub_f32 v189, v184, v187\n" \
"v_exp_f32 v189, v189\n" \
"v_mov_b32 v184, v187\n" \
"v_sub_f32 v176, v176, v187\n" \
"v_sub_f32 v177, v177, v187\n" \
"v_sub_f32 v178, v178, v187\n" \
"v_sub_f32 v179, v179, v187\n" \
"v_sub_f32 v180, v180, v187\n" \
"v_sub_f32 v181, v181, v187\n" \
"v_sub_f32 v182, v182, v187\n" \
"v_sub_f32 v183, v183, v187\n" \
"v_exp_f32 v176, v176\n" \
"v_exp_f32 v177, v177\n" \
"v_exp_f32 v178, v178\n" \
"v_exp_f32 v179, v179\n" \
"v_exp_f32 v180, v180\n" \
"v_exp_f32 v181, v181\n" \
"v_exp_f32 v182, v182\n" \
"v_exp_f32 v183, v183\n" \
"v_mul_f32 v185, v185, v189\n" \
"v_add_f32 v185, v185, v176\n" \
"v_add_f32 v185, v185, v177\n" \
"v_add_f32 v185, v185, v178\n" \
"v_add_f32 v185, v185, v179\n" \
"v_add_f32 v185, v185, v180\n" \
"v_add_f32 v185, v185, v181\n" \
"v_add_f32 v185, v185, v182\n" \
"v_add_f32 v185, v185, v183\n" \
"v_cvt_pk_bf16_f32 v112, v176, v177\n" \
"v_cvt_pk_bf16_f32 v113, v178, v179\n" \
"v_cvt_pk_bf16_f32 v114, v180, v181\n" \
"v_cvt_pk_bf16_f32 v115, v182, v183\n" \
"v_mul_f32 v128, v128, v189\n" \
"v_mul_f32 v129, v129, v189\n" \
"v_mul_f32 v130, v130, v189\n" \
"v_mul_f32 v131, v131, v189\n" \
"v_mul_f32 v132, v132, v189\n" \
"v_mul_f32 v133, v133, v189\n" \
"v_mul_f32 v134, v134, v189\n" \
"v_mul_f32 v135, v135, v189\n" \
"v_mul_f32 v136, v136, v189\n" \
"v_mul_f32 v137, v137, v189\n" \
"v_mul_f32 v138, v138, v189\n" \
"v_mul_f32 v139, v139, v189\n" \
"v_mul_f32 v140, v140, v189\n" \
"v_mul_f32 v141, v141, v189\n" \
"v_mul_f32 v142, v142, v189\n" \
"v_mul_f32 v143, v143, v189\n" \
"v_mul_f32 v144, v144, v189\n" \
"v_mul_f32 v145, v145, v189\n" \
"v_mul_f32 v146, v146, v189\n" \
"v_mul_f32 v147, v147, v189\n" \
"v_mul_f32 v148, v148, v189\n" \
"v_mul_f32 v149, v149, v189\n" \
"v_mul_f32 v150, v150, v189\n" \
"v_mul_f32 v151, v151, v189\n" \
"v_mul_f32 v152, v152, v189\n" \
"v_mul_f32 v153, v153, v189\n" \
"v_mul_f32 v154, v154, v189\n" \
"v_mul_f32 v155, v155, v189\n" \
"v_mul_f32 v156, v156, v189\n" \
"v_mul_f32 v157, v157, v189\n" \
"v_mul_f32 v158, v158, v189\n" \
"v_mul_f32 v159, v159, v189\n" \
"s_nop 1\n" \
"v_mfma_f32_16x16x32_bf16 v[128:131], a[32:35], v[112:115], v[128:131]\n" \
"v_mfma_f32_16x16x32_bf16 v[132:135], a[36:39], v[112:115], v[132:135]\n" \
"v_mfma_f32_16x16x32_bf16 v[136:139], a[40:43], v[112:115], v[136:139]\n" \
"v_mfma_f32_16x16x32_bf16 v[140:143], a[44:47], v[112:115], v[140:143]\n" \
"v_mfma_f32_16x16x32_bf16 v[144:147], a[48:51], v[112:115], v[144:147]\n" \
"v_mfma_f32_16x16x32_bf16 v[148:151], a[52:55], v[112:115], v[148:151]\n" \
"v_mfma_f32_16x16x32_bf16 v[152:155], a[56:59], v[112:115], v[152:155]\n" \
"v_mfma_f32_16x16x32_bf16 v[156:159], a[60:63], v[112:115], v[156:159]\n" \
"s_nop 7\n" \
"s_nop 1\n" \
"s_and_b32 s80, s73, 1\n" \
"s_mul_i32 s80, s80, 1024\n" \
"s_add_u32 s74, s74, s80\n" \
"s_add_u32 s74, s74, 8192\n" \
"s_add_u32 s73, s73, 1\n" \
"s_cmp_le_u32 s73, s75\n" \
"s_cbranch_scc1 Lex2_%=\n" \
"v_mov_b32 v187, v185\n" \
"s_nop 1\n" \
"v_permlane16_swap_b32 v187, v185\n" \
"v_add_f32 v185, v185, v187\n" \
"v_mov_b32 v187, v185\n" \
"s_nop 1\n" \
"v_permlane32_swap_b32 v187, v185\n" \
"v_add_f32 v185, v185, v187\n" \
"v_rcp_f32 v185, v185\n" \
"s_nop 0\n" \
"v_mul_f32 v128, v128, v185\n" \
"v_mul_f32 v129, v129, v185\n" \
"v_mul_f32 v130, v130, v185\n" \
"v_mul_f32 v131, v131, v185\n" \
"v_mul_f32 v132, v132, v185\n" \
"v_mul_f32 v133, v133, v185\n" \
"v_mul_f32 v134, v134, v185\n" \
"v_mul_f32 v135, v135, v185\n" \
"v_mul_f32 v136, v136, v185\n" \
"v_mul_f32 v137, v137, v185\n" \
"v_mul_f32 v138, v138, v185\n" \
"v_mul_f32 v139, v139, v185\n" \
"v_mul_f32 v140, v140, v185\n" \
"v_mul_f32 v141, v141, v185\n" \
"v_mul_f32 v142, v142, v185\n" \
"v_mul_f32 v143, v143, v185\n" \
"v_mul_f32 v144, v144, v185\n" \
"v_mul_f32 v145, v145, v185\n" \
"v_mul_f32 v146, v146, v185\n" \
"v_mul_f32 v147, v147, v185\n" \
"v_mul_f32 v148, v148, v185\n" \
"v_mul_f32 v149, v149, v185\n" \
"v_mul_f32 v150, v150, v185\n" \
"v_mul_f32 v151, v151, v185\n" \
"v_mul_f32 v152, v152, v185\n" \
"v_mul_f32 v153, v153, v185\n" \
"v_mul_f32 v154, v154, v185\n" \
"v_mul_f32 v155, v155, v185\n" \
"v_mul_f32 v156, v156, v185\n" \
"v_mul_f32 v157, v157, v185\n" \
"v_mul_f32 v158, v158, v185\n" \
"v_mul_f32 v159, v159, v185\n" \
"s_add_u32 s80, s76, 4096\n" \
"v_cvt_pk_bf16_f32 v112, v128, v129\n" \
"v_cvt_pk_bf16_f32 v113, v130, v131\n" \
"v_cvt_pk_bf16_f32 v114, v132, v133\n" \
"v_cvt_pk_bf16_f32 v115, v134, v135\n" \
"buffer_store_dwordx4 v[112:115], v249, s[56:59], s76 offen offset:0 sc1\n" \
"v_cvt_pk_bf16_f32 v116, v136, v137\n" \
"v_cvt_pk_bf16_f32 v117, v138, v139\n" \
"v_cvt_pk_bf16_f32 v118, v140, v141\n" \
"v_cvt_pk_bf16_f32 v119, v142, v143\n" \
"buffer_store_dwordx4 v[116:119], v249, s[56:59], s76 offen offset:2048 sc1\n" \
"v_cvt_pk_bf16_f32 v120, v144, v145\n" \
"v_cvt_pk_bf16_f32 v121, v146, v147\n" \
"v_cvt_pk_bf16_f32 v122, v148, v149\n" \
"v_cvt_pk_bf16_f32 v123, v150, v151\n" \
"buffer_store_dwordx4 v[120:123], v249, s[56:59], s80 offen offset:0 sc1\n" \
"v_cvt_pk_bf16_f32 v124, v152, v153\n" \
"v_cvt_pk_bf16_f32 v125, v154, v155\n" \
"v_cvt_pk_bf16_f32 v126, v156, v157\n" \
"v_cvt_pk_bf16_f32 v127, v158, v159\n" \
"buffer_store_dwordx4 v[124:127], v249, s[56:59], s80 offen offset:2048 sc1\n" \
"s_waitcnt vmcnt(0)\n" \
"s_setpc_b64 s[68:69]\n" \
"Lpl3_%=:\n" \
"s_mov_b32 s31, 0\n" \
"Lpk3_%=:\n" \
"s_sleep 2\n" \
"buffer_load_dword v220, v221, s[56:59], s81 offen sc1\n" \
"s_waitcnt vmcnt(0)\n" \
"v_cmp_ne_u32 vcc, s60, v220\n" \
"s_nop 1\n" \
"s_and_b32 vcc_hi, vcc_lo, s80\n" \
"s_cbranch_scc0 Lpr3_%=\n" \
"s_add_u32 s31, s31, 1\n" \
"s_cmp_lt_u32 s31, 0x40000\n" \
"s_cbranch_scc1 Lpk3_%=\n" \
"s_branch Lpr3_%=\n" \
"Lpl4_%=:\n" \
"s_mov_b32 s31, 0\n" \
"Lpk4_%=:\n" \
"s_sleep 2\n" \
"buffer_load_dword v220, off, s[56:59], s79 sc1\n" \
"s_waitcnt vmcnt(0)\n" \
"v_cmp_ne_u32 vcc, s60, v220\n" \
"s_nop 1\n" \
"s_and_b32 vcc_hi, vcc_lo, 1\n" \
"s_cbranch_scc0 Lpr4_%=\n" \
"s_add_u32 s31, s31, 1\n" \
"s_cmp_lt_u32 s31, 0x40000\n" \
"s_cbranch_scc1 Lpk4_%=\n" \
"s_branch Lpr4_%=\n" \
"Lpl5_%=:\n" \
"s_mov_b32 s31, 0\n" \
"Lpk5_%=:\n" \
"s_sleep 2\n" \
"buffer_load_dword v220, v221, s[56:59], s82 offen sc1\n" \
"s_waitcnt vmcnt(0)\n" \
"v_cmp_ne_u32 vcc, s60, v220\n" \
"s_nop 1\n" \
"s_and_b32 vcc_hi, vcc_lo, 65535\n" \
"s_cbranch_scc0 Lpr5_%=\n" \
"s_add_u32 s31, s31, 1\n" \
"s_cmp_lt_u32 s31, 0x40000\n" \
"s_cbranch_scc1 Lpk5_%=\n" \
"s_branch Lpr5_%=\n" \
"Lcode_end_%=:\n" \
".p2align 12\n" \
""
#define MK_ASM_CLOBBERS "v1", "v2", "v3", "v4", "v5", "v6", "v7", "v8", "v9", "v10", "v11", "v12", "v13", "v14", "v15", "v16", "v17", "v18", "v19", "v20", "v21", "v22", "v23", "v24", "v25", "v26", "v27", "v28", "v29", "v30", "v31", "v32", "v33", "v34", "v35", "v36", "v37", "v38", "v39", "v40", "v41", "v42", "v43", "v44", "v45", "v46", "v47", "v48", "v49", "v50", "v51", "v52", "v53", "v54", "v55", "v56", "v57", "v58", "v59", "v60", "v61", "v62", "v63", "v64", "v65", "v66", "v67", "v68", "v69", "v70", "v71", "v72", "v73", "v74", "v75", "v76", "v77", "v78", "v79", "v80", "v81", "v82", "v83", "v84", "v85", "v86", "v87", "v88", "v89", "v90", "v91", "v92", "v93", "v94", "v95", "v96", "v97", "v98", "v99", "v100", "v101", "v102", "v103", "v104", "v105", "v106", "v107", "v108", "v109", "v110", "v111", "v112", "v113", "v114", "v115", "v116", "v117", "v118", "v119", "v120", "v121", "v122", "v123", "v124", "v125", "v126", "v127", "v128", "v129", "v130", "v131", "v132", "v133", "v134", "v135", "v136", "v137", "v138", "v139", "v140", "v141", "v142", "v143", "v144", "v145", "v146", "v147", "v148", "v149", "v150", "v151", "v152", "v153", "v154", "v155", "v156", "v157", "v158", "v159", "v160", "v161", "v162", "v163", "v164", "v165", "v166", "v167", "v168", "v169", "v170", "v171", "v172", "v173", "v174", "v175", "v176", "v177", "v178", "v179", "v180", "v181", "v182", "v183", "v184", "v185", "v186", "v187", "v188", "v189", "v190", "v191", "v192", "v193", "v194", "v195", "v196", "v197", "v198", "v199", "v200", "v201", "v202", "v203", "v204", "v205", "v206", "v207", "v208", "v209", "v210", "v211", "v212", "v213", "v214", "v215", "v216", "v217", "v218", "v219", "v220", "v221", "v222", "v223", "v224", "v225", "v226", "v227", "v228", "v229", "v230", "v231", "v232", "v233", "v234", "v235", "v236", "v237", "v238", "v239", "v240", "v241", "v242", "v243", "v244", "v245", "v246", "v247", "v248", "v249", "v250", "v251", "v252", "v253", "v254", "v255", "a0", "a1", "a2", "a3", "a4", "a5", "a6", "a7", "a8", "a9", "a10", "a11", "a12", "a13", "a14", "a15", "a16", "a17", "a18", "a19", "a20", "a21", "a22", "a23", "a24", "a25", "a26", "a27", "a28", "a29", "a30", "a31", "a32", "a33", "a34", "a35", "a36", "a37", "a38", "a39", "a40", "a41", "a42", "a43", "a44", "a45", "a46", "a47", "a48", "a49", "a50", "a51", "a52", "a53", "a54", "a55", "a56", "a57", "a58", "a59", "a60", "a61", "a62", "a63", "a64", "a65", "a66", "a67", "a68", "a69", "a70", "a71", "a72", "a73", "a74", "a75", "a76", "a77", "a78", "a79", "a80", "a81", "a82", "a83", "a84", "a85", "a86", "a87", "a88", "a89", "a90", "a91", "a92", "a93", "a94", "a95", "a96", "a97", "a98", "a99", "a100", "a101", "a102", "a103", "a104", "a105", "a106", "a107", "a108", "a109", "a110", "a111", "a112", "a113", "a114", "a115", "a116", "a117", "a118", "a119", "a120", "a121", "a122", "a123", "a124", "a125", "a126", "a127", "a128", "a129", "a130", "a131", "a132", "a133", "a134", "a135", "a136", "a137", "a138", "a139", "a140", "a141", "a142", "a143", "a144", "a145", "a146", "a147", "a148", "a149", "a150", "a151", "a152", "a153", "a154", "a155", "a156", "a157", "a158", "a159", "a160", "a161", "a162", "a163", "a164", "a165", "a166", "a167", "a168", "a169", "a170", "a171", "a172", "a173", "a174", "a175", "a176", "a177", "a178", "a179", "a180", "a181", "a182", "a183", "a184", "a185", "a186", "a187", "a188", "a189", "a190", "a191", "a192", "a193", "a194", "a195", "a196", "a197", "a198", "a199", "a200", "a201", "a202", "a203", "a204", "a205", "a206", "a207", "a208", "a209", "a210", "a211", "a212", "a213", "a214", "a215", "a216", "a217", "a218", "a219", "a220", "a221", "a222", "a223", "a224", "a225", "a226", "a227", "a228", "a229", "a230", "a231", "a232", "a233", "a234", "a235", "a236", "a237", "a238", "a239", "a240", "a241", "a242", "a243", "a244", "a245", "a246", "a247", "a248", "a249", "a250", "a251", "a252", "a253", "a254", "a255", "s8", "s9", "s10", "s11", "s12", "s13", "s14", "s15", "s16", "s17", "s18", "s19", "s20", "s21", "s22", "s23", "s24", "s25", "s26", "s27", "s28", "s29", "s30", "s31", "s36", "s37", "s38", "s39", "s40", "s41", "s42", "s43", "s44", "s45", "s46", "s47", "s48", "s49", "s50", "s51", "s52", "s53", "s54", "s55", "s56", "s57", "s58", "s59", "s60", "s61", "s62", "s63", "s64", "s65", "s66", "s67", "s68", "s69", "s70", "s71", "s72", "s73", "s74", "s75", "s76", "s77", "s78", "s79", "s80", "s81", "s82", "s83", "s84", "s85", "s86", "s87", "s88", "s89", "s90", "s91", "s92", "s93", "s94", "s95", "vcc", "scc", "memory"
namespace mk {
constexpr int LDS_BYTES = 163840;
struct Args { const void* in[9]; void* out; void* ws; };
__global__ __launch_bounds__(256, 1) void mega_k(const Args a) {
    extern __shared__ __attribute__((aligned(16))) unsigned char shm[];
    const unsigned ldsb = (unsigned)(size_t)(LAS unsigned char*)shm;
    asm volatile(MK_ASM_BODY
                 :
                 : "s"(__builtin_amdgcn_kernarg_segment_ptr()), "s"(blockIdx.x), "v"(threadIdx.x), "s"(ldsb)
                 : MK_ASM_CLOBBERS);
}
}
extern "C" void kernel_launch(void* const* d_in, const int* in_sizes, int n_in, void* d_out, int out_size, void* d_ws, size_t ws_size, hipStream_t stream) {
    (void)hipFuncSetAttribute((const void*)mk::mega_k, hipFuncAttributeMaxDynamicSharedMemorySize, mk::LDS_BYTES);
    mk::Args a;
    for (int i = 0; i < 9; ++i) a.in[i] = d_in[i];
    a.out = d_out; a.ws = (char*)d_ws + ((ws_size - WS_END) & ~(size_t)0x1fffff);
    mk::mega_k<<<256, 256, mk::LDS_BYTES, stream>>>(a);
}
```

```cpp
#include <hip/hip_runtime.h>
#include <stdint.h>
#define LAS __attribute__((address_space(3)))
constexpr size_t LAY_TS = 17408, LAY_HS = 557824, LAY_CRS = 266240, LAY_ORS = 69632, WS_K = 0, WS_V = 17850368, WS_CTX = 35700736, WS_OB = 52740096, WS_FL = 65585152, LAY_EXD = 29950080, WS_END = 87147648;
#define MK_ASM_BODY \
"s_load_dwordx16 s[8:23], %0, 0x0\n" \
"s_load_dwordx4 s[24:27], %0, 0x40\n" \
"s_load_dwordx2 s[28:29], %0, 0x50\n" \
"v_readfirstlane_b32 s61, %2\n" \
"s_lshr_b32 s61, s61, 6\n" \
"v_mbcnt_lo_u32_b32 v52, -1, 0\n" \
"v_mbcnt_hi_u32_b32 v52, -1, v52\n" \
"v_lshlrev_b32 v248, 4, v52\n" \
"s_getpc_b64 s[64:65]\n" \
"Lpc_%=:\n" \
"s_lshr_b32 s70, %1, 3\n" \
"s_lshl_b32 s70, s70, 2\n" \
"s_add_u32 s70, s70, s61\n" \
"s_and_b32 s71, s70, 31\n" \
"s_lshl_b32 s71, s71, 12\n" \
"v_lshl_add_u32 v53, v248, 2, s71\n" \
"s_mov_b32 s71, Lcode_end_%=-Lpc_%=-4\n" \
"v_min_u32 v53, s71, v53\n" \
"global_load_dword v253, v53, s[64:65]\n" \
"s_and_b32 s62, %1, 7\n" \
"s_lshl_b32 s62, s62, 2\n" \
"s_lshr_b32 s70, %1, 6\n" \
"s_add_u32 s62, s62, s70\n" \
"s_bfe_u32 s63, %1, 0x30003\n" \
"s_lshl_b32 s87, s61, 13\n" \
"s_lshl_b32 s95, s61, 12\n" \
"v_bfe_u32 v53, v52, 4, 1\n" \
"v_bfe_u32 v54, v52, 2, 2\n" \
"v_lshl_add_u32 v54, v53, 3, v54\n" \
"s_lshl_b32 s71, s61, 4\n" \
"v_add_u32 v54, s71, v54\n" \
"v_lshlrev_b32 v54, 13, v54\n" \
"v_lshlrev_b32 v53, 1, v53\n" \
"v_and_b32 v55, 3, v52\n" \
"v_xor_b32 v53, v53, v55\n" \
"v_lshl_add_u32 v54, v53, 4, v54\n" \
"v_lshrrev_b32 v53, 5, v52\n" \
"v_lshl_add_u32 v54, v53, 6, v54\n" \
"v_add_u32 v40, 4096, v54\n" \
"v_add_u32 v41, 3072, v54\n" \
"v_add_u32 v42, 2176, v54\n" \
"v_add_u32 v43, 1152, v54\n" \
"v_add_u32 v44, 32768, v54\n" \
"v_add_u32 v45, 31744, v54\n" \
"v_add_u32 v46, 30848, v54\n" \
"v_add_u32 v47, 29824, v54\n" \
"v_lshrrev_b32 v55, 3, v52\n" \
"v_and_b32 v56, 7, v52\n" \
"v_xor_b32 v56, v56, v55\n" \
"v_lshlrev_b32 v56, 4, v56\n" \
"v_lshl_add_u32 v57, v55, 10, v56\n" \
"v_add_u32 v32, 528384, v57\n" \
"v_add_u32 v33, 535552, v57\n" \
"v_add_u32 v34, 542720, v57\n" \
"v_add_u32 v35, 549888, v57\n" \
"v_add_u32 v36, 0, v57\n" \
"v_add_u32 v37, 7168, v57\n" \
"v_add_u32 v38, 14336, v57\n" \
"v_add_u32 v39, 21504, v57\n" \
"s_lshl_b32 s88, s62, 8\n" \
"s_lshl_b32 s89, s63, 18\n" \
"s_lshl_b32 s70, s63, 16\n" \
"s_and_b32 s71, s87, 0x4000\n" \
"s_lshl_b32 s71, s71, 1\n" \
"s_add_u32 s70, s70, s71\n" \
"s_andn2_b32 s71, 1, s61\n" \
"s_lshl_b32 s71, s71, 20\n" \
"s_add_u32 s70, s70, s71\n" \
"s_lshl_b32 s66, %1, 2\n" \
"s_add_u32 s66, s66, s61\n" \
"s_lshl_b32 s66, s66, 10\n" \
"s_lshr_b32 s67, %1, 3\n" \
"s_lshl_b32 s67, s67, 2\n" \
"s_add_u32 s67, s67, s61\n" \
"s_lshl_b32 s67, s67, 13\n" \
"v_lshrrev_b32 v63, 6, v248\n" \
"v_lshlrev_b32 v63, 8, v63\n" \
"v_and_or_b32 v63, v248, 48, v63\n" \
"s_lshl_b32 s64, s67, 1\n" \
"v_lshl_add_u32 v63, v63, 2, s64\n" \
"s_and_b32 s64, %1, 7\n" \
"s_lshl_b32 s64, s64, 10\n" \
"s_add_u32 s67, s67, s64\n" \
"s_waitcnt lgkmcnt(0)\n" \
"s_add_u32 s52, s14, s88\n" \
"s_addc_u32 s53, s15, 0\n" \
"s_add_u32 s50, s16, s88\n" \
"s_addc_u32 s51, s17, 0\n" \
"s_add_u32 m0, s87, 36864\n" \
"s_nop 0\n" \
"global_load_lds_dwordx4 v40, s[52:53] offset:-4096\n" \
"global_load_lds_dwordx4 v41, s[50:51] offset:-3072\n" \
"global_load_lds_dwordx4 v42, s[52:53] offset:-2048\n" \
"global_load_lds_dwordx4 v43, s[50:51] offset:-1024\n" \
"global_load_lds_dwordx4 v44, s[52:53] offset:0\n" \
"global_load_lds_dwordx4 v45, s[50:51] offset:1024\n" \
"global_load_lds_dwordx4 v46, s[52:53] offset:2048\n" \
"global_load_lds_dwordx4 v47, s[50:51] offset:3072\n" \
"s_add_u32 s52, s52, 0x80000\n" \
"s_addc_u32 s53, s53, 0\n" \
"s_add_u32 s50, s50, 0x80000\n" \
"s_addc_u32 s51, s51, 0\n" \
"s_add_u32 s54, s10, s70\n" \
"s_addc_u32 s55, s11, 0\n" \
"s_add_u32 m0, s87, 4096\n" \
"s_nop 0\n" \
"global_load_lds_dwordx4 v32, s[54:55] offset:-4096\n" \
"global_load_lds_dwordx4 v33, s[54:55] offset:-3072\n" \
"global_load_lds_dwordx4 v34, s[54:55] offset:-2048\n" \
"global_load_lds_dwordx4 v35, s[54:55] offset:-1024\n" \
"global_load_lds_dwordx4 v36, s[54:55] offset:0\n" \
"global_load_lds_dwordx4 v37, s[54:55] offset:1024\n" \
"global_load_lds_dwordx4 v38, s[54:55] offset:2048\n" \
"global_load_lds_dwordx4 v39, s[54:55] offset:3072\n" \
"s_add_u32 s54, s54, 0x80\n" \
"s_addc_u32 s55, s55, 0\n" \
"s_add_u32 m0, s87, 135168\n" \
"s_nop 0\n" \
"global_load_lds_dwordx4 v40, s[52:53] offset:-4096\n" \
"global_load_lds_dwordx4 v41, s[50:51] offset:-3072\n" \
"global_load_lds_dwordx4 v42, s[52:53] offset:-2048\n" \
"global_load_lds_dwordx4 v43, s[50:51] offset:-1024\n" \
"global_load_lds_dwordx4 v44, s[52:53] offset:0\n" \
"global_load_lds_dwordx4 v45, s[50:51] offset:1024\n" \
"global_load_lds_dwordx4 v46, s[52:53] offset:2048\n" \
"global_load_lds_dwordx4 v47, s[50:51] offset:3072\n" \
"s_add_u32 s52, s52, 0x80000\n" \
"s_addc_u32 s53, s53, 0\n" \
"s_add_u32 s50, s50, 0x80000\n" \
"s_addc_u32 s51, s51, 0\n" \
"s_mov_b64 s[56:57], s[28:29]\n" \
"s_and_b32 s57, s57, 0xffff\n" \
"s_mov_b32 s58, 87147648\n" \
"s_mov_b32 s59, 0x20000\n" \
"v_add_u32 v228, 4096, v54\n" \
"v_add_u32 v229, 3200, v54\n" \
"v_lshl_add_u32 v57, v55, 11, v56\n" \
"v_add_u32 v224, 0, v57\n" \
"v_add_u32 v225, 15360, v57\n" \
"v_add_u32 v226, 129024, v57\n" \
"v_add_u32 v227, 144384, v57\n" \
"s_lshl_b32 s70, s61, 15\n" \
"s_add_u32 s70, s70, 0\n" \
"s_add_u32 s71, s70, s89\n" \
"s_add_u32 s71, s71, 4096\n" \
"s_add_u32 s48, s8, s71\n" \
"s_addc_u32 s49, s9, 0\n" \
"s_sub_u32 s71, s70, s89\n" \
"s_add_u32 s71, s71, 3932160\n" \
"s_add_u32 s46, s8, s71\n" \
"s_addc_u32 s47, s9, 0\n" \
"s_add_u32 s70, s88, 0\n" \
"s_add_u32 s44, s12, s70\n" \
"s_addc_u32 s45, s13, 0\n" \
"s_add_u32 s42, s44, 30720\n" \
"s_addc_u32 s43, s45, 0\n" \
"s_lshr_b32 s70, s28, 31\n" \
"s_lshl_b32 s70, s70, 30\n" \
"s_sub_u32 s28, s28, s70\n" \
"v_add_u32 v250, s70, v248\n" \
"v_lshlrev_b32 v245, 2, v52\n" \
"v_mov_b32 v254, 0x30000\n" \
"v_lshrrev_b32 v55, 4, v52\n" \
"v_and_b32 v249, 0xf0, v248\n" \
"v_lshl_add_u32 v249, v55, 9, v249\n" \
"v_and_b32 v53, 15, v52\n" \
"s_lshl_b32 s70, s61, 4\n" \
"v_lshlrev_b32 v54, 2, v55\n" \
"v_sub_u32 v251, v53, v54\n" \
"v_add_u32 v251, s70, v251\n" \
"v_and_b32 v54, 7, v52\n" \
"v_xor_b32 v54, v54, v55\n" \
"v_lshlrev_b32 v53, 7, v53\n" \
"s_add_u32 s71, s87, 131072\n" \
"s_lshr_b32 s70, s61, 1\n" \
"s_lshl_b32 s70, s70, 14\n" \
"s_add_u32 s70, s70, 0\n" \
"v_xor_b32 v56, 0, v54\n" \
"v_lshl_add_u32 v56, v56, 4, v53\n" \
"v_add_u32 v230, s71, v56\n" \
"v_add_u32 v48, s70, v56\n" \
"v_xor_b32 v56, 4, v54\n" \
"v_lshl_add_u32 v56, v56, 4, v53\n" \
"v_add_u32 v231, s71, v56\n" \
"v_add_u32 v49, s70, v56\n" \
"v_and_b32 v53, 3, v52\n" \
"v_lshlrev_b32 v53, 3, v53\n" \
"v_bfe_u32 v54, v52, 2, 2\n" \
"v_lshl_add_u32 v53, v54, 6, v53\n" \
"v_bfe_u32 v54, v52, 4, 1\n" \
"v_lshl_add_u32 v53, v54, 8, v53\n" \
"v_lshl_add_u32 v53, v54, 5, v53\n" \
"v_lshrrev_b32 v54, 5, v52\n" \
"s_and_b32 s70, s61, 1\n" \
"s_lshl_b32 s70, s70, 11\n" \
"s_add_u32 s70, s70, 32768\n" \
"v_lshl_add_u32 v56, v54, 13, v53\n" \
"v_add_u32 v50, s70, v56\n" \
"v_xor_b32 v51, 32, v50\n" \
"v_lshl_add_u32 v232, v54, 12, v53\n" \
"v_xor_b32 v233, 32, v232\n" \
"s_mov_b32 s90, 65593344\n" \
"buffer_load_dword v244, off, s[56:59], s90 sc1\n" \
"s_add_u32 s68, s66, 0x0\n" \
"s_add_u32 s64, s10, s68\n" \
"s_addc_u32 s65, s11, 0\n" \
"global_load_dword v253, v248, s[64:65]\n" \
"s_add_u32 s68, s66, 0x100000\n" \
"s_add_u32 s64, s10, s68\n" \
"s_addc_u32 s65, s11, 0\n" \
"global_load_dword v253, v248, s[64:65]\n" \
"s_add_u32 s68, s67, 0x100000\n" \
"s_add_u32 s64, s14, s68\n" \
"s_addc_u32 s65, s15, 0\n" \
"global_load_dword v253, v248, s[64:65]\n" \
"s_add_u32 s68, s67, 0x100000\n" \
"s_add_u32 s64, s16, s68\n" \
"s_addc_u32 s65, s17, 0\n" \
"global_load_dword v253, v248, s[64:65]\n" \
"s_mov_b64 exec, 0\n" \
"s_cmp_eq_u32 s61, 0\n" \
"s_cbranch_scc1 Lst0x0_%=\n" \
"s_cmp_eq_u32 s61, 1\n" \
"s_cbranch_scc1 Lst0x1_%=\n" \
"s_cmp_eq_u32 s61, 2\n" \
"s_cbranch_scc1 Lst0x2_%=\n" \
"s_branch Lst0x3_%=\n" \
"Lcb0_%=:\n" \
"s_mov_b64 exec, -1\n" \
"s_waitcnt vmcnt(13)\n" \
"s_barrier\n" \
"ds_read_b128 v[64:67], v48 offset:0\n" \
"ds_read_b64_tr_b16 v[128:129], v50 offset:0\n" \
"ds_read_b128 v[68:71], v49 offset:0\n" \
"ds_read_b64_tr_b16 v[130:131], v50 offset:4096\n" \
"ds_read_b64_tr_b16 v[132:133], v50 offset:16384\n" \
"ds_read_b64_tr_b16 v[134:135], v50 offset:20480\n" \
"ds_read_b128 v[72:75], v48 offset:2048\n" \
"ds_read_b64_tr_b16 v[136:137], v51 offset:0\n" \
"ds_read_b128 v[76:79], v49 offset:2048\n" \
"ds_read_b64_tr_b16 v[138:139], v51 offset:4096\n" \
"ds_read_b64_tr_b16 v[140:141], v51 offset:16384\n" \
"ds_read_b64_tr_b16 v[142:143], v51 offset:20480\n" \
"ds_read_b128 v[80:83], v48 offset:4096\n" \
"ds_read_b64_tr_b16 v[144:145], v50 offset:512\n" \
"ds_read_b128 v[84:87], v49 offset:4096\n" \
"ds_read_b64_tr_b16 v[146:147], v50 offset:4608\n" \
"ds_read_b64_tr_b16 v[148:149], v50 offset:16896\n" \
"ds_read_b64_tr_b16 v[150:151], v50 offset:20992\n" \
"ds_read_b128 v[88:91], v48 offset:6144\n" \
"ds_read_b64_tr_b16 v[152:153], v51 offset:512\n" \
"ds_read_b128 v[92:95], v49 offset:6144\n" \
"ds_read_b64_tr_b16 v[154:155], v51 offset:4608\n" \
"ds_read_b64_tr_b16 v[156:157], v51 offset:16896\n" \
"ds_read_b64_tr_b16 v[158:159], v51 offset:20992\n" \
"ds_read_b128 v[96:99], v48 offset:8192\n" \
"ds_read_b128 v[100:103], v49 offset:8192\n" \
"ds_read_b128 v[104:107], v48 offset:10240\n" \
"ds_read_b128 v[108:111], v49 offset:10240\n" \
"ds_read_b128 v[112:115], v48 offset:12288\n" \
"ds_read_b128 v[116:119], v49 offset:12288\n" \
"s_waitcnt lgkmcnt(15)\n" \
"v_mfma_f32_16x16x32_bf16 a[0:3], v[128:131], v[64:67], 0\n" \
"ds_read_b128 v[120:123], v48 offset:14336\n" \
"s_add_u32 m0, s87, 69632\n" \
"v_mfma_f32_16x16x32_bf16 a[0:3], v[132:135], v[68:71], a[0:3]\n" \
"global_load_lds_dwordx4 v32, s[54:55] offset:-4096\n" \
"v_mfma_f32_16x16x32_bf16 a[4:7], v[136:139], v[64:67], 0\n" \
"ds_read_b128 v[124:127], v49 offset:14336\n" \
"v_mfma_f32_16x16x32_bf16 a[4:7], v[140:143], v[68:71], a[4:7]\n" \
"s_waitcnt lgkmcnt(14)\n" \
"v_mfma_f32_16x16x32_bf16 a[8:11], v[144:147], v[64:67], 0\n" \
"ds_read_b64_tr_b16 v[160:161], v50 offset:1024\n" \
"global_load_lds_dwordx4 v33, s[54:55] offset:-3072\n" \
"v_mfma_f32_16x16x32_bf16 a[8:11], v[148:151], v[68:71], a[8:11]\n" \
"s_waitcnt lgkmcnt(2)\n" \
"v_mfma_f32_16x16x32_bf16 a[12:15], v[152:155], v[64:67], 0\n" \
"ds_read_b64_tr_b16 v[162:163], v50 offset:5120\n" \
"v_mfma_f32_16x16x32_bf16 a[12:15], v[156:159], v[68:71], a[12:15]\n" \
"global_load_lds_dwordx4 v34, s[54:55] offset:-2048\n" \
"v_mfma_f32_16x16x32_bf16 a[32:35], v[128:131], v[72:75], 0\n" \
"ds_read_b64_tr_b16 v[164:165], v50 offset:17408\n" \
"v_mfma_f32_16x16x32_bf16 a[32:35], v[132:135], v[76:79], a[32:35]\n" \
"v_mfma_f32_16x16x32_bf16 a[36:39], v[136:139], v[72:75], 0\n" \
"ds_read_b64_tr_b16 v[166:167], v50 offset:21504\n" \
"global_load_lds_dwordx4 v35, s[54:55] offset:-1024\n" \
"v_mfma_f32_16x16x32_bf16 a[36:39], v[140:143], v[76:79], a[36:39]\n" \
"v_mfma_f32_16x16x32_bf16 a[40:43], v[144:147], v[72:75], 0\n" \
"ds_read_b64_tr_b16 v[168:169], v51 offset:1024\n" \
"v_mfma_f32_16x16x32_bf16 a[40:43], v[148:151], v[76:79], a[40:43]\n" \
"global_load_lds_dwordx4 v36, s[54:55] offset:0\n" \
"v_mfma_f32_16x16x32_bf16 a[44:47], v[152:155], v[72:75], 0\n" \
"ds_read_b64_tr_b16 v[170:171], v51 offset:5120\n" \
"v_mfma_f32_16x16x32_bf16 a[44:47], v[156:159], v[76:79], a[44:47]\n" \
"v_mfma_f32_16x16x32_bf16 a[64:67], v[128:131], v[80:83], 0\n" \
"ds_read_b64_tr_b16 v[172:173], v51 offset:17408\n" \
"global_load_lds_dwordx4 v37, s[54:55] offset:1024\n" \
"v_mfma_f32_16x16x32_bf16 a[64:67], v[132:135], v[84:87], a[64:67]\n" \
"v_mfma_f32_16x16x32_bf16 a[68:71], v[136:139], v[80:83], 0\n" \
"ds_read_b64_tr_b16 v[174:175], v51 offset:21504\n" \
"v_mfma_f32_16x16x32_bf16 a[68:71], v[140:143], v[84:87], a[68:71]\n" \
"global_load_lds_dwordx4 v38, s[54:55] offset:2048\n" \
"v_mfma_f32_16x16x32_bf16 a[72:75], v[144:147], v[80:83], 0\n" \
"ds_read_b64_tr_b16 v[176:177], v50 offset:1536\n" \
"v_mfma_f32_16x16x32_bf16 a[72:75], v[148:151], v[84:87], a[72:75]\n" \
"v_mfma_f32_16x16x32_bf16 a[76:79], v[152:155], v[80:83], 0\n" \
"ds_read_b64_tr_b16 v[178:179], v50 offset:5632\n" \
"global_load_lds_dwordx4 v39, s[54:55] offset:3072\n" \
"v_mfma_f32_16x16x32_bf16 a[76:79], v[156:159], v[84:87], a[76:79]\n" \
"s_add_u32 s54, s54, 0x80\n" \
"s_addc_u32 s55, s55, 0\n" \
"v_mfma_f32_16x16x32_bf16 a[96:99], v[128:131], v[88:91], 0\n" \
"s_add_u32 m0, s87, 102400\n" \
"ds_read_b64_tr_b16 v[180:181], v50 offset:17920\n" \
"v_mfma_f32_16x16x32_bf16 a[96:99], v[132:135], v[92:95], a[96:99]\n" \
"global_load_lds_dwordx4 v40, s[52:53] offset:-4096\n" \
"v_mfma_f32_16x16x32_bf16 a[100:103], v[136:139], v[88:91], 0\n" \
"ds_read_b64_tr_b16 v[182:183], v50 offset:22016\n" \
"v_mfma_f32_16x16x32_bf16 a[100:103], v[140:143], v[92:95], a[100:103]\n" \
"v_mfma_f32_16x16x32_bf16 a[104:107], v[144:147], v[88:91], 0\n" \
"ds_read_b64_tr_b16 v[184:185], v51 offset:1536\n" \
"global_load_lds_dwordx4 v41, s[50:51] offset:-3072\n" \
"v_mfma_f32_16x16x32_bf16 a[104:107], v[148:151], v[92:95], a[104:107]\n" \
"v_mfma_f32_16x16x32_bf16 a[108:111], v[152:155], v[88:91], 0\n" \
"ds_read_b64_tr_b16 v[186:187], v51 offset:5632\n" \
"v_mfma_f32_16x16x32_bf16 a[108:111], v[156:159], v[92:95], a[108:111]\n" \
"global_load_lds_dwordx4 v42, s[52:53] offset:-2048\n" \
"v_mfma_f32_16x16x32_bf16 a[128:131], v[128:131], v[96:99], 0\n" \
"ds_read_b64_tr_b16 v[188:189], v51 offset:17920\n" \
"v_mfma_f32_16x16x32_bf16 a[128:131], v[132:135], v[100:103], a[128:131]\n" \
"v_mfma_f32_16x16x32_bf16 a[132:135], v[136:139], v[96:99], 0\n" \
"ds_read_b64_tr_b16 v[190:191], v51 offset:22016\n" \
"global_load_lds_dwordx4 v43, s[50:51] offset:-1024\n" \
"v_mfma_f32_16x16x32_bf16 a[132:135], v[140:143], v[100:103], a[132:135]\n" \
"v_mfma_f32_16x16x32_bf16 a[136:139], v[144:147], v[96:99], 0\n" \
"v_mfma_f32_16x16x32_bf16 a[136:139], v[148:151], v[100:103], a[136:139]\n" \
"global_load_lds_dwordx4 v44, s[52:53] offset:0\n" \
"v_mfma_f32_16x16x32_bf16 a[140:143], v[152:155], v[96:99], 0\n" \
"v_mfma_f32_16x16x32_bf16 a[140:143], v[156:159], v[100:103], a[140:143]\n" \
"global_load_lds_dwordx4 v45, s[50:51] offset:1024\n" \
"v_mfma_f32_16x16x32_bf16 a[160:163], v[128:131], v[104:107], 0\n" \
"v_add_u32 v48, 0x10000, v48\n" \
"v_add_u32 v49, 0x10000, v49\n" \
"v_mfma_f32_16x16x32_bf16 a[160:163], v[132:135], v[108:111], a[160:163]\n" \
"v_add_u32 v50, 0x18000, v50\n" \
"v_add_u32 v51, 0x18000, v51\n" \
"v_mfma_f32_16x16x32_bf16 a[164:167], v[136:139], v[104:107], 0\n" \
"v_mfma_f32_16x16x32_bf16 a[164:167], v[140:143], v[108:111], a[164:167]\n" \
"global_load_lds_dwordx4 v46, s[52:53] offset:2048\n" \
"v_mfma_f32_16x16x32_bf16 a[168:171], v[144:147], v[104:107], 0\n" \
"v_mfma_f32_16x16x32_bf16 a[168:171], v[148:151], v[108:111], a[168:171]\n" \
"v_mfma_f32_16x16x32_bf16 a[172:175], v[152:155], v[104:107], 0\n" \
"global_load_lds_dwordx4 v47, s[50:51] offset:3072\n" \
"v_mfma_f32_16x16x32_bf16 a[172:175], v[156:159], v[108:111], a[172:175]\n" \
"s_add_u32 s52, s52, 0x80000\n" \
"s_addc_u32 s53, s53, 0\n" \
"v_mfma_f32_16x16x32_bf16 a[192:195], v[128:131], v[112:115], 0\n" \
"s_add_u32 s50, s50, 0x80000\n" \
"s_addc_u32 s51, s51, 0\n" \
"v_mfma_f32_16x16x32_bf16 a[192:195], v[132:135], v[116:119], a[192:195]\n" \
"v_mfma_f32_16x16x32_bf16 a[196:199], v[136:139], v[112:115], 0\n" \
"v_mfma_f32_16x16x32_bf16 a[196:199], v[140:143], v[116:119], a[196:199]\n" \
"v_mfma_f32_16x16x32_bf16 a[200:203], v[144:147], v[112:115], 0\n" \
"v_mfma_f32_16x16x32_bf16 a[200:203], v[148:151], v[116:119], a[200:203]\n" \
"v_mfma_f32_16x16x32_bf16 a[204:207], v[152:155], v[112:115], 0\n" \
"v_mfma_f32_16x16x32_bf16 a[204:207], v[156:159], v[116:119], a[204:207]\n" \
"s_waitcnt lgkmcnt(0)\n" \
"v_mfma_f32_16x16x32_bf16 a[224:227], v[128:131], v[120:123], 0\n" \
"v_mfma_f32_16x16x32_bf16 a[224:227], v[132:135], v[124:127], a[224:227]\n" \
"v_mfma_f32_16x16x32_bf16 a[228:231], v[136:139], v[120:123], 0\n" \
"v_mfma_f32_16x16x32_bf16 a[228:231], v[140:143], v[124:127], a[228:231]\n" \
"v_mfma_f32_16x16x32_bf16 a[232:235], v[144:147], v[120:123], 0\n" \
"v_mfma_f32_16x16x32_bf16 a[232:235], v[148:151], v[124:127], a[232:235]\n" \
"v_mfma_f32_16x16x32_bf16 a[236:239], v[152:155], v[120:123], 0\n" \
"s_waitcnt vmcnt(8) lgkmcnt(0)\n" \
"v_mfma_f32_16x16x32_bf16 a[236:239], v[156:159], v[124:127], a[236:239]\n" \
"s_barrier\n" \
"v_mfma_f32_16x16x32_bf16 a[16:19], v[64:67], v[160:163], 0\n" \
"s_add_u32 m0, s87, 36864\n" \
"ds_read_b64_tr_b16 v[128:129], v50 offset:0\n" \
"v_mfma_f32_16x16x32_bf16 a[16:19], v[68:71], v[164:167], a[16:19]\n" \
"global_load_lds_dwordx4 v32, s[54:55] offset:-4096\n" \
"v_mfma_f32_16x16x32_bf16 a[20:23], v[64:67], v[168:171], 0\n" \
"ds_read_b64_tr_b16 v[130:131], v50 offset:4096\n" \
"v_mfma_f32_16x16x32_bf16 a[20:23], v[68:71], v[172:175], a[20:23]\n" \
"v_mfma_f32_16x16x32_bf16 a[24:27], v[64:67], v[176:179], 0\n" \
"ds_read_b64_tr_b16 v[132:133], v50 offset:16384\n" \
"v_mfma_f32_16x16x32_bf16 a[24:27], v[68:71], v[180:183], a[24:27]\n" \
"global_load_lds_dwordx4 v33, s[54:55] offset:-3072\n" \
"v_mfma_f32_16x16x32_bf16 a[28:31], v[64:67], v[184:187], 0\n" \
"ds_read_b64_tr_b16 v[134:135], v50 offset:20480\n" \
"v_mfma_f32_16x16x32_bf16 a[28:31], v[68:71], v[188:191], a[28:31]\n" \
"v_mfma_f32_16x16x32_bf16 a[48:51], v[72:75], v[160:163], 0\n" \
"ds_read_b128 v[64:67], v48 offset:0\n" \
"v_mfma_f32_16x16x32_bf16 a[48:51], v[76:79], v[164:167], a[48:51]\n" \
"global_load_lds_dwordx4 v34, s[54:55] offset:-2048\n" \
"v_mfma_f32_16x16x32_bf16 a[52:55], v[72:75], v[168:171], 0\n" \
"ds_read_b64_tr_b16 v[136:137], v51 offset:0\n" \
"v_mfma_f32_16x16x32_bf16 a[52:55], v[76:79], v[172:175], a[52:55]\n" \
"v_mfma_f32_16x16x32_bf16 a[56:59], v[72:75], v[176:179], 0\n" \
"ds_read_b128 v[68:71], v49 offset:0\n" \
"v_mfma_f32_16x16x32_bf16 a[56:59], v[76:79], v[180:183], a[56:59]\n" \
"global_load_lds_dwordx4 v35, s[54:55] offset:-1024\n" \
"v_mfma_f32_16x16x32_bf16 a[60:63], v[72:75], v[184:187], 0\n" \
"ds_read_b64_tr_b16 v[138:139], v51 offset:4096\n" \
"v_mfma_f32_16x16x32_bf16 a[60:63], v[76:79], v[188:191], a[60:63]\n" \
"v_mfma_f32_16x16x32_bf16 a[80:83], v[80:83], v[160:163], 0\n" \
"ds_read_b128 v[72:75], v48 offset:2048\n" \
"v_mfma_f32_16x16x32_bf16 a[80:83], v[84:87], v[164:167], a[80:83]\n" \
"global_load_lds_dwordx4 v36, s[54:55] offset:0\n" \
"v_mfma_f32_16x16x32_bf16 a[84:87], v[80:83], v[168:171], 0\n" \
"ds_read_b64_tr_b16 v[140:141], v51 offset:16384\n" \
"v_mfma_f32_16x16x32_bf16 a[84:87], v[84:87], v[172:175], a[84:87]\n" \
"v_mfma_f32_16x16x32_bf16 a[88:91], v[80:83], v[176:179], 0\n" \
"ds_read_b128 v[76:79], v49 offset:2048\n" \
"v_mfma_f32_16x16x32_bf16 a[88:91], v[84:87], v[180:183], a[88:91]\n" \
"global_load_lds_dwordx4 v37, s[54:55] offset:1024\n" \
"v_mfma_f32_16x16x32_bf16 a[92:95], v[80:83], v[184:187], 0\n" \
"ds_read_b64_tr_b16 v[142:143], v51 offset:20480\n" \
"v_mfma_f32_16x16x32_bf16 a[92:95], v[84:87], v[188:191], a[92:95]\n" \
"v_mfma_f32_16x16x32_bf16 a[112:115], v[88:91], v[160:163], 0\n" \
"ds_read_b128 v[80:83], v48 offset:4096\n" \
"v_mfma_f32_16x16x32_bf16 a[112:115], v[92:95], v[164:167], a[112:115]\n" \
"global_load_lds_dwordx4 v38, s[54:55] offset:2048\n" \
"v_mfma_f32_16x16x32_bf16 a[116:119], v[88:91], v[168:171], 0\n" \
"ds_read_b64_tr_b16 v[144:145], v50 offset:512\n" \
"v_mfma_f32_16x16x32_bf16 a[116:119], v[92:95], v[172:175], a[116:119]\n" \
"v_mfma_f32_16x16x32_bf16 a[120:123], v[88:91], v[176:179], 0\n" \
"ds_read_b128 v[84:87], v49 offset:4096\n" \
"v_mfma_f32_16x16x32_bf16 a[120:123], v[92:95], v[180:183], a[120:123]\n" \
"global_load_lds_dwordx4 v39, s[54:55] offset:3072\n" \
"v_mfma_f32_16x16x32_bf16 a[124:127], v[88:91], v[184:187], 0\n" \
"s_add_u32 s54, s54, 0x80\n" \
"s_addc_u32 s55, s55, 0\n" \
"ds_read_b64_tr_b16 v[146:147], v50 offset:4608\n" \
"v_mfma_f32_16x16x32_bf16 a[124:127], v[92:95], v[188:191], a[124:127]\n" \
"s_add_u32 s68, s67, 0x200000\n" \
"s_add_u32 s64, s14, s68\n" \
"s_addc_u32 s65, s15, 0\n" \
"v_mfma_f32_16x16x32_bf16 a[144:147], v[96:99], v[160:163], 0\n" \
"ds_read_b128 v[88:91], v48 offset:6144\n" \
"v_mfma_f32_16x16x32_bf16 a[144:147], v[100:103], v[164:167], a[144:147]\n" \
"global_load_dword v253, v248, s[64:65]\n" \
"v_mfma_f32_16x16x32_bf16 a[148:151], v[96:99], v[168:171], 0\n" \
"s_add_u32 s68, s67, 0x200000\n" \
"ds_read_b64_tr_b16 v[148:149], v50 offset:16896\n" \
"v_mfma_f32_16x16x32_bf16 a[148:151], v[100:103], v[172:175], a[148:151]\n" \
"s_add_u32 s64, s16, s68\n" \
"s_addc_u32 s65, s17, 0\n" \
"v_mfma_f32_16x16x32_bf16 a[152:155], v[96:99], v[176:179], 0\n" \
"ds_read_b128 v[92:95], v49 offset:6144\n" \
"v_mfma_f32_16x16x32_bf16 a[152:155], v[100:103], v[180:183], a[152:155]\n" \
"global_load_dword v253, v248, s[64:65]\n" \
"v_mfma_f32_16x16x32_bf16 a[156:159], v[96:99], v[184:187], 0\n" \
"ds_read_b64_tr_b16 v[150:151], v50 offset:20992\n" \
"v_mfma_f32_16x16x32_bf16 a[156:159], v[100:103], v[188:191], a[156:159]\n" \
"v_mfma_f32_16x16x32_bf16 a[176:179], v[104:107], v[160:163], 0\n" \
"ds_read_b128 v[96:99], v48 offset:8192\n" \
"v_mfma_f32_16x16x32_bf16 a[176:179], v[108:111], v[164:167], a[176:179]\n" \
"v_mfma_f32_16x16x32_bf16 a[180:183], v[104:107], v[168:171], 0\n" \
"ds_read_b64_tr_b16 v[152:153], v51 offset:512\n" \
"v_mfma_f32_16x16x32_bf16 a[180:183], v[108:111], v[172:175], a[180:183]\n" \
"v_mfma_f32_16x16x32_bf16 a[184:187], v[104:107], v[176:179], 0\n" \
"ds_read_b128 v[100:103], v49 offset:8192\n" \
"v_mfma_f32_16x16x32_bf16 a[184:187], v[108:111], v[180:183], a[184:187]\n" \
"v_mfma_f32_16x16x32_bf16 a[188:191], v[104:107], v[184:187], 0\n" \
"ds_read_b64_tr_b16 v[154:155], v51 offset:4608\n" \
"v_mfma_f32_16x16x32_bf16 a[188:191], v[108:111], v[188:191], a[188:191]\n" \
"v_mfma_f32_16x16x32_bf16 a[208:211], v[112:115], v[160:163], 0\n" \
"ds_read_b128 v[104:107], v48 offset:10240\n" \
"v_mfma_f32_16x16x32_bf16 a[208:211], v[116:119], v[164:167], a[208:211]\n" \
"v_mfma_f32_16x16x32_bf16 a[212:215], v[112:115], v[168:171], 0\n" \
"ds_read_b64_tr_b16 v[156:157], v51 offset:16896\n" \
"v_mfma_f32_16x16x32_bf16 a[212:215], v[116:119], v[172:175], a[212:215]\n" \
"v_mfma_f32_16x16x32_bf16 a[216:219], v[112:115], v[176:179], 0\n" \
"ds_read_b128 v[108:111], v49 offset:10240\n" \
"v_mfma_f32_16x16x32_bf16 a[216:219], v[116:119], v[180:183], a[216:219]\n" \
"v_mfma_f32_16x16x32_bf16 a[220:223], v[112:115], v[184:187], 0\n" \
"ds_read_b64_tr_b16 v[158:159], v51 offset:20992\n" \
"v_mfma_f32_16x16x32_bf16 a[220:223], v[116:119], v[188:191], a[220:223]\n" \
"v_mfma_f32_16x16x32_bf16 a[240:243], v[120:123], v[160:163], 0\n" \
"ds_read_b128 v[112:115], v48 offset:12288\n" \
"v_mfma_f32_16x16x32_bf16 a[240:243], v[124:127], v[164:167], a[240:243]\n" \
"v_mfma_f32_16x16x32_bf16 a[244:247], v[120:123], v[168:171], 0\n" \
"v_mfma_f32_16x16x32_bf16 a[244:247], v[124:127], v[172:175], a[244:247]\n" \
"v_mfma_f32_16x16x32_bf16 a[248:251], v[120:123], v[176:179], 0\n" \
"ds_read_b128 v[116:119], v49 offset:12288\n" \
"v_mfma_f32_16x16x32_bf16 a[248:251], v[124:127], v[180:183], a[248:251]\n" \
"v_mfma_f32_16x16x32_bf16 a[252:255], v[120:123], v[184:187], 0\n" \
"v_mfma_f32_16x16x32_bf16 a[252:255], v[124:127], v[188:191], a[252:255]\n" \
"s_waitcnt lgkmcnt(1)\n" \
"v_mfma_f32_16x16x32_bf16 a[0:3], v[128:131], v[64:67], a[0:3]\n" \
"ds_read_b128 v[120:123], v48 offset:14336\n" \
"v_mfma_f32_16x16x32_bf16 a[0:3], v[132:135], v[68:71], a[0:3]\n" \
"v_mfma_f32_16x16x32_bf16 a[4:7], v[136:139], v[64:67], a[4:7]\n" \
"ds_read_b128 v[124:127], v49 offset:14336\n" \
"s_add_u32 m0, s87, 4096\n" \
"v_mfma_f32_16x16x32_bf16 a[4:7], v[140:143], v[68:71], a[4:7]\n" \
"global_load_lds_dwordx4 v40, s[52:53] offset:-4096\n" \
"v_mfma_f32_16x16x32_bf16 a[8:11], v[144:147], v[64:67], a[8:11]\n" \
"ds_read_b64_tr_b16 v[160:161], v50 offset:1024\n" \
"v_mfma_f32_16x16x32_bf16 a[8:11], v[148:151], v[68:71], a[8:11]\n" \
"v_mfma_f32_16x16x32_bf16 a[12:15], v[152:155], v[64:67], a[12:15]\n" \
"ds_read_b64_tr_b16 v[162:163], v50 offset:5120\n" \
"v_mfma_f32_16x16x32_bf16 a[12:15], v[156:159], v[68:71], a[12:15]\n" \
"global_load_lds_dwordx4 v41, s[50:51] offset:-3072\n" \
"v_mfma_f32_16x16x32_bf16 a[32:35], v[128:131], v[72:75], a[32:35]\n" \
"ds_read_b64_tr_b16 v[164:165], v50 offset:17408\n" \
"v_mfma_f32_16x16x32_bf16 a[32:35], v[132:135], v[76:79], a[32:35]\n" \
"v_mfma_f32_16x16x32_bf16 a[36:39], v[136:139], v[72:75], a[36:39]\n" \
"ds_read_b64_tr_b16 v[166:167], v50 offset:21504\n" \
"v_mfma_f32_16x16x32_bf16 a[36:39], v[140:143], v[76:79], a[36:39]\n" \
"global_load_lds_dwordx4 v42, s[52:53] offset:-2048\n" \
"v_mfma_f32_16x16x32_bf16 a[40:43], v[144:147], v[72:75], a[40:43]\n" \
"ds_read_b64_tr_b16 v[168:169], v51 offset:1024\n" \
"v_mfma_f32_16x16x32_bf16 a[40:43], v[148:151], v[76:79], a[40:43]\n" \
"v_mfma_f32_16x16x32_bf16 a[44:47], v[152:155], v[72:75], a[44:47]\n" \
"ds_read_b64_tr_b16 v[170:171], v51 offset:5120\n" \
"v_mfma_f32_16x16x32_bf16 a[44:47], v[156:159], v[76:79], a[44:47]\n" \
"global_load_lds_dwordx4 v43, s[50:51] offset:-1024\n" \
"v_mfma_f32_16x16x32_bf16 a[64:67], v[128:131], v[80:83], a[64:67]\n" \
"ds_read_b64_tr_b16 v[172:173], v51 offset:17408\n" \
"v_mfma_f32_16x16x32_bf16 a[64:67], v[132:135], v[84:87], a[64:67]\n" \
"v_mfma_f32_16x16x32_bf16 a[68:71], v[136:139], v[80:83], a[68:71]\n" \
"ds_read_b64_tr_b16 v[174:175], v51 offset:21504\n" \
"v_mfma_f32_16x16x32_bf16 a[68:71], v[140:143], v[84:87], a[68:71]\n" \
"global_load_lds_dwordx4 v44, s[52:53] offset:0\n" \
"v_mfma_f32_16x16x32_bf16 a[72:75], v[144:147], v[80:83], a[72:75]\n" \
"ds_read_b64_tr_b16 v[176:177], v50 offset:1536\n" \
"v_mfma_f32_16x16x32_bf16 a[72:75], v[148:151], v[84:87], a[72:75]\n" \
"v_mfma_f32_16x16x32_bf16 a[76:79], v[152:155], v[80:83], a[76:79]\n" \
"ds_read_b64_tr_b16 v[178:179], v50 offset:5632\n" \
"v_mfma_f32_16x16x32_bf16 a[76:79], v[156:159], v[84:87], a[76:79]\n" \
"global_load_lds_dwordx4 v45, s[50:51] offset:1024\n" \
"v_mfma_f32_16x16x32_bf16 a[96:99], v[128:131], v[88:91], a[96:99]\n" \
"ds_read_b64_tr_b16 v[180:181], v50 offset:17920\n" \
"v_mfma_f32_16x16x32_bf16 a[96:99], v[132:135], v[92:95], a[96:99]\n" \
"v_mfma_f32_16x16x32_bf16 a[100:103], v[136:139], v[88:91], a[100:103]\n" \
"ds_read_b64_tr_b16 v[182:183], v50 offset:22016\n" \
"v_mfma_f32_16x16x32_bf16 a[100:103], v[140:143], v[92:95], a[100:103]\n" \
"global_load_lds_dwordx4 v46, s[52:53] offset:2048\n" \
"v_mfma_f32_16x16x32_bf16 a[104:107], v[144:147], v[88:91], a[104:107]\n" \
"ds_read_b64_tr_b16 v[184:185], v51 offset:1536\n" \
"v_mfma_f32_16x16x32_bf16 a[104:107], v[148:151], v[92:95], a[104:107]\n" \
"v_mfma_f32_16x16x32_bf16 a[108:111], v[152:155], v[88:91], a[108:111]\n" \
"ds_read_b64_tr_b16 v[186:187], v51 offset:5632\n" \
"v_mfma_f32_16x16x32_bf16 a[108:111], v[156:159], v[92:95], a[108:111]\n" \
"global_load_lds_dwordx4 v47, s[50:51] offset:3072\n" \
"v_mfma_f32_16x16x32_bf16 a[128:131], v[128:131], v[96:99], a[128:131]\n" \
"s_add_u32 s52, s52, 0x80000\n" \
"s_addc_u32 s53, s53, 0\n" \
"ds_read_b64_tr_b16 v[188:189], v51 offset:17920\n" \
"v_mfma_f32_16x16x32_bf16 a[128:131], v[132:135], v[100:103], a[128:131]\n" \
"s_add_u32 s50, s50, 0x80000\n" \
"s_addc_u32 s51, s51, 0\n" \
"v_mfma_f32_16x16x32_bf16 a[132:135], v[136:139], v[96:99], a[132:135]\n" \
"ds_read_b64_tr_b16 v[190:191], v51 offset:22016\n" \
"v_mfma_f32_16x16x32_bf16 a[132:135], v[140:143], v[100:103], a[132:135]\n" \
"v_mfma_f32_16x16x32_bf16 a[136:139], v[144:147], v[96:99], a[136:139]\n" \
"v_mfma_f32_16x16x32_bf16 a[136:139], v[148:151], v[100:103], a[136:139]\n" \
"v_mfma_f32_16x16x32_bf16 a[140:143], v[152:155], v[96:99], a[140:143]\n" \
"v_mfma_f32_16x16x32_bf16 a[140:143], v[156:159], v[100:103], a[140:143]\n" \
"v_mfma_f32_16x16x32_bf16 a[160:163], v[128:131], v[104:107], a[160:163]\n" \
"v_add_u32 v48, 0xffff8000, v48\n" \
"v_add_u32 v49, 0xffff8000, v49\n" \
"v_mfma_f32_16x16x32_bf16 a[160:163], v[132:135], v[108:111], a[160:163]\n" \
"v_add_u32 v50, 0xffff8000, v50\n" \
"v_add_u32 v51, 0xffff8000, v51\n" \
"v_mfma_f32_16x16x32_bf16 a[164:167], v[136:139], v[104:107], a[164:167]\n" \
"v_mfma_f32_16x16x32_bf16 a[164:167], v[140:143], v[108:111], a[164:167]\n" \
"v_mfma_f32_16x16x32_bf16 a[168:171], v[144:147], v[104:107], a[168:171]\n" \
"v_mfma_f32_16x16x32_bf16 a[168:171], v[148:151], v[108:111], a[168:171]\n" \
"v_mfma_f32_16x16x32_bf16 a[172:175], v[152:155], v[104:107], a[172:175]\n" \
"v_mfma_f32_16x16x32_bf16 a[172:175], v[156:159], v[108:111], a[172:175]\n" \
"s_waitcnt lgkmcnt(0)\n" \
"v_mfma_f32_16x16x32_bf16 a[192:195], v[128:131], v[112:115], a[192:195]\n" \
"v_mfma_f32_16x16x32_bf16 a[192:195], v[132:135], v[116:119], a[192:195]\n" \
"v_mfma_f32_16x16x32_bf16 a[196:199], v[136:139], v[112:115], a[196:199]\n" \
"v_mfma_f32_16x16x32_bf16 a[196:199], v[140:143], v[116:119], a[196:199]\n" \
"v_mfma_f32_16x16x32_bf16 a[200:203], v[144:147], v[112:115], a[200:203]\n" \
"v_mfma_f32_16x16x32_bf16 a[200:203], v[148:151], v[116:119], a[200:203]\n" \
"v_mfma_f32_16x16x32_bf16 a[204:207], v[152:155], v[112:115], a[204:207]\n" \
"v_mfma_f32_16x16x32_bf16 a[204:207], v[156:159], v[116:119], a[204:207]\n" \
"v_mfma_f32_16x16x32_bf16 a[224:227], v[128:131], v[120:123], a[224:227]\n" \
"v_mfma_f32_16x16x32_bf16 a[224:227], v[132:135], v[124:127], a[224:227]\n" \
"v_mfma_f32_16x16x32_bf16 a[228:231], v[136:139], v[120:123], a[228:231]\n" \
"v_mfma_f32_16x16x32_bf16 a[228:231], v[140:143], v[124:127], a[228:231]\n" \
"v_mfma_f32_16x16x32_bf16 a[232:235], v[144:147], v[120:123], a[232:235]\n" \
"Lpg588_%=:\n" \
"Lst0x0_%=:\n" \
"s_cbranch_execz Lpg2726_%=\n" \
"v_mfma_f32_16x16x32_bf16 a[232:235], v[148:151], v[124:127], a[232:235]\n" \
"v_mfma_f32_16x16x32_bf16 a[236:239], v[152:155], v[120:123], a[236:239]\n" \
"s_waitcnt vmcnt(10) lgkmcnt(0)\n" \
"v_mfma_f32_16x16x32_bf16 a[236:239], v[156:159], v[124:127], a[236:239]\n" \
"s_barrier\n" \
"v_mfma_f32_16x16x32_bf16 a[16:19], v[64:67], v[160:163], a[16:19]\n" \
"s_add_u32 m0, s87, 135168\n" \
"ds_read_b64_tr_b16 v[128:129], v50 offset:0\n" \
"v_readfirstlane_b32 s60, v244\n" \
"v_mfma_f32_16x16x32_bf16 a[16:19], v[68:71], v[164:167], a[16:19]\n" \
"s_add_u32 s60, s60, 0x9e3779b9\n" \
"global_load_lds_dwordx4 v32, s[54:55] offset:-4096\n" \
"v_mfma_f32_16x16x32_bf16 a[20:23], v[64:67], v[168:171], a[20:23]\n" \
"s_mul_i32 s88, s62, 557824\n" \
"s_mul_i32 s89, s63, 17408\n" \
"ds_read_b64_tr_b16 v[130:131], v50 offset:4096\n" \
"v_mfma_f32_16x16x32_bf16 a[20:23], v[68:71], v[172:175], a[20:23]\n" \
"s_add_u32 s88, s88, s89\n" \
"s_add_u32 s88, s88, s95\n" \
"s_add_u32 s89, s88, 17850368\n" \
"v_mfma_f32_16x16x32_bf16 a[24:27], v[64:67], v[176:179], a[24:27]\n" \
"s_lshl_b32 s91, s62, 7\n" \
"s_add_u32 s91, s91, 65585152\n" \
"ds_read_b64_tr_b16 v[132:133], v50 offset:16384\n" \
"v_mfma_f32_16x16x32_bf16 a[24:27], v[68:71], v[180:183], a[24:27]\n" \
"s_mov_b32 s92, 0x3e0293ee\n" \
"global_load_lds_dwordx4 v33, s[54:55] offset:-3072\n" \
"v_mfma_f32_16x16x32_bf16 a[28:31], v[64:67], v[184:187], a[28:31]\n" \
"ds_read_b64_tr_b16 v[134:135], v50 offset:20480\n" \
"v_mfma_f32_16x16x32_bf16 a[28:31], v[68:71], v[188:191], a[28:31]\n" \
"v_mfma_f32_16x16x32_bf16 a[48:51], v[72:75], v[160:163], a[48:51]\n" \
"ds_read_b128 v[64:67], v48 offset:0\n" \
"v_mfma_f32_16x16x32_bf16 a[48:51], v[76:79], v[164:167], a[48:51]\n" \
"global_load_lds_dwordx4 v34, s[54:55] offset:-2048\n" \
"v_mfma_f32_16x16x32_bf16 a[52:55], v[72:75], v[168:171], a[52:55]\n" \
"ds_read_b64_tr_b16 v[136:137], v51 offset:0\n" \
"v_mfma_f32_16x16x32_bf16 a[52:55], v[76:79], v[172:175], a[52:55]\n" \
"v_mfma_f32_16x16x32_bf16 a[56:59], v[72:75], v[176:179], a[56:59]\n" \
"ds_read_b128 v[68:71], v49 offset:0\n" \
"v_mfma_f32_16x16x32_bf16 a[56:59], v[76:79], v[180:183], a[56:59]\n" \
"global_load_lds_dwordx4 v35, s[54:55] offset:-1024\n" \
"v_mfma_f32_16x16x32_bf16 a[60:63], v[72:75], v[184:187], a[60:63]\n" \
"ds_read_b64_tr_b16 v[138:139], v51 offset:4096\n" \
"v_mfma_f32_16x16x32_bf16 a[60:63], v[76:79], v[188:191], a[60:63]\n" \
"v_mfma_f32_16x16x32_bf16 a[80:83], v[80:83], v[160:163], a[80:83]\n" \
"ds_read_b128 v[72:75], v48 offset:2048\n" \
"v_mfma_f32_16x16x32_bf16 a[80:83], v[84:87], v[164:167], a[80:83]\n" \
"global_load_lds_dwordx4 v36, s[54:55] offset:0\n" \
"v_mfma_f32_16x16x32_bf16 a[84:87], v[80:83], v[168:171], a[84:87]\n" \
"ds_read_b64_tr_b16 v[140:141], v51 offset:16384\n" \
"v_mfma_f32_16x16x32_bf16 a[84:87], v[84:87], v[172:175], a[84:87]\n" \
"v_mfma_f32_16x16x32_bf16 a[88:91], v[80:83], v[176:179], a[88:91]\n" \
"ds_read_b128 v[76:79], v49 offset:2048\n" \
"v_mfma_f32_16x16x32_bf16 a[88:91], v[84:87], v[180:183], a[88:91]\n" \
"global_load_lds_dwordx4 v37, s[54:55] offset:1024\n" \
"v_mfma_f32_16x16x32_bf16 a[92:95], v[80:83], v[184:187], a[92:95]\n" \
"ds_read_b64_tr_b16 v[142:143], v51 offset:20480\n" \
"v_mfma_f32_16x16x32_bf16 a[92:95], v[84:87], v[188:191], a[92:95]\n" \
"v_mfma_f32_16x16x32_bf16 a[112:115], v[88:91], v[160:163], a[112:115]\n" \
"ds_read_b128 v[80:83], v48 offset:4096\n" \
"v_mfma_f32_16x16x32_bf16 a[112:115], v[92:95], v[164:167], a[112:115]\n" \
"global_load_lds_dwordx4 v38, s[54:55] offset:2048\n" \
"v_mfma_f32_16x16x32_bf16 a[116:119], v[88:91], v[168:171], a[116:119]\n" \
"ds_read_b64_tr_b16 v[144:145], v50 offset:512\n" \
"v_mfma_f32_16x16x32_bf16 a[116:119], v[92:95], v[172:175], a[116:119]\n" \
"v_mfma_f32_16x16x32_bf16 a[120:123], v[88:91], v[176:179], a[120:123]\n" \
"ds_read_b128 v[84:87], v49 offset:4096\n" \
"v_mfma_f32_16x16x32_bf16 a[120:123], v[92:95], v[180:183], a[120:123]\n" \
"global_load_lds_dwordx4 v39, s[54:55] offset:3072\n" \
"v_mfma_f32_16x16x32_bf16 a[124:127], v[88:91], v[184:187], a[124:127]\n" \
"s_add_u32 s54, s54, 0x80\n" \
"s_addc_u32 s55, s55, 0\n" \
"ds_read_b64_tr_b16 v[146:147], v50 offset:4608\n" \
"v_mfma_f32_16x16x32_bf16 a[124:127], v[92:95], v[188:191], a[124:127]\n" \
"s_add_u32 s68, s67, 0x300000\n" \
"s_add_u32 s64, s14, s68\n" \
"s_addc_u32 s65, s15, 0\n" \
"v_mfma_f32_16x16x32_bf16 a[144:147], v[96:99], v[160:163], a[144:147]\n" \
"ds_read_b128 v[88:91], v48 offset:6144\n" \
"v_mfma_f32_16x16x32_bf16 a[144:147], v[100:103], v[164:167], a[144:147]\n" \
"global_load_dword v253, v248, s[64:65]\n" \
"v_mfma_f32_16x16x32_bf16 a[148:151], v[96:99], v[168:171], a[148:151]\n" \
"s_add_u32 s68, s67, 0x300000\n" \
"ds_read_b64_tr_b16 v[148:149], v50 offset:16896\n" \
"v_mfma_f32_16x16x32_bf16 a[148:151], v[100:103], v[172:175], a[148:151]\n" \
"s_add_u32 s64, s16, s68\n" \
"s_addc_u32 s65, s17, 0\n" \
"v_mfma_f32_16x16x32_bf16 a[152:155], v[96:99], v[176:179], a[152:155]\n" \
"ds_read_b128 v[92:95], v49 offset:6144\n" \
"v_mfma_f32_16x16x32_bf16 a[152:155], v[100:103], v[180:183], a[152:155]\n" \
"global_load_dword v253, v248, s[64:65]\n" \
"v_mfma_f32_16x16x32_bf16 a[156:159], v[96:99], v[184:187], a[156:159]\n" \
"ds_read_b64_tr_b16 v[150:151], v50 offset:20992\n" \
"v_mfma_f32_16x16x32_bf16 a[156:159], v[100:103], v[188:191], a[156:159]\n" \
"v_mfma_f32_16x16x32_bf16 a[176:179], v[104:107], v[160:163], a[176:179]\n" \
"ds_read_b128 v[96:99], v48 offset:8192\n" \
"v_mfma_f32_16x16x32_bf16 a[176:179], v[108:111], v[164:167], a[176:179]\n" \
"v_mfma_f32_16x16x32_bf16 a[180:183], v[104:107], v[168:171], a[180:183]\n" \
"ds_read_b64_tr_b16 v[152:153], v51 offset:512\n" \
"v_mfma_f32_16x16x32_bf16 a[180:183], v[108:111], v[172:175], a[180:183]\n" \
"v_mfma_f32_16x16x32_bf16 a[184:187], v[104:107], v[176:179], a[184:187]\n" \
"ds_read_b128 v[100:103], v49 offset:8192\n" \
"v_mfma_f32_16x16x32_bf16 a[184:187], v[108:111], v[180:183], a[184:187]\n" \
"v_mfma_f32_16x16x32_bf16 a[188:191], v[104:107], v[184:187], a[188:191]\n" \
"ds_read_b64_tr_b16 v[154:155], v51 offset:4608\n" \
"v_mfma_f32_16x16x32_bf16 a[188:191], v[108:111], v[188:191], a[188:191]\n" \
"v_mfma_f32_16x16x32_bf16 a[208:211], v[112:115], v[160:163], a[208:211]\n" \
"ds_read_b128 v[104:107], v48 offset:10240\n" \
"v_mfma_f32_16x16x32_bf16 a[208:211], v[116:119], v[164:167], a[208:211]\n" \
"v_mfma_f32_16x16x32_bf16 a[212:215], v[112:115], v[168:171], a[212:215]\n" \
"ds_read_b64_tr_b16 v[156:157], v51 offset:16896\n" \
"v_mfma_f32_16x16x32_bf16 a[212:215], v[116:119], v[172:175], a[212:215]\n" \
"v_mfma_f32_16x16x32_bf16 a[216:219], v[112:115], v[176:179], a[216:219]\n" \
"ds_read_b128 v[108:111], v49 offset:10240\n" \
"v_mfma_f32_16x16x32_bf16 a[216:219], v[116:119], v[180:183], a[216:219]\n" \
"v_mfma_f32_16x16x32_bf16 a[220:223], v[112:115], v[184:187], a[220:223]\n" \
"ds_read_b64_tr_b16 v[158:159], v51 offset:20992\n" \
"v_mfma_f32_16x16x32_bf16 a[220:223], v[116:119], v[188:191], a[220:223]\n" \
"v_mfma_f32_16x16x32_bf16 a[240:243], v[120:123], v[160:163], a[240:243]\n" \
"ds_read_b128 v[112:115], v48 offset:12288\n" \
"v_mfma_f32_16x16x32_bf16 a[240:243], v[124:127], v[164:167], a[240:243]\n" \
"v_mfma_f32_16x16x32_bf16 a[244:247], v[120:123], v[168:171], a[244:247]\n" \
"v_mfma_f32_16x16x32_bf16 a[244:247], v[124:127], v[172:175], a[244:247]\n" \
"v_mfma_f32_16x16x32_bf16 a[248:251], v[120:123], v[176:179], a[248:251]\n" \
"ds_read_b128 v[116:119], v49 offset:12288\n" \
"v_mfma_f32_16x16x32_bf16 a[248:251], v[124:127], v[180:183], a[248:251]\n" \
"v_mfma_f32_16x16x32_bf16 a[252:255], v[120:123], v[184:187], a[252:255]\n" \
"v_mfma_f32_16x16x32_bf16 a[252:255], v[124:127], v[188:191], a[252:255]\n" \
"s_waitcnt lgkmcnt(1)\n" \
"v_mfma_f32_16x16x32_bf16 a[0:3], v[128:131], v[64:67], a[0:3]\n" \
"ds_read_b128 v[120:123], v48 offset:14336\n" \
"v_mfma_f32_16x16x32_bf16 a[0:3], v[132:135], v[68:71], a[0:3]\n" \
"v_mfma_f32_16x16x32_bf16 a[4:7], v[136:139], v[64:67], a[4:7]\n" \
"ds_read_b128 v[124:127], v49 offset:14336\n" \
"s_add_u32 m0, s87, 69632\n" \
"v_mfma_f32_16x16x32_bf16 a[4:7], v[140:143], v[68:71], a[4:7]\n" \
"global_load_lds_dwordx4 v40, s[52:53] offset:-4096\n" \
"v_mfma_f32_16x16x32_bf16 a[8:11], v[144:147], v[64:67], a[8:11]\n" \
"ds_read_b64_tr_b16 v[160:161], v50 offset:1024\n" \
"v_mfma_f32_16x16x32_bf16 a[8:11], v[148:151], v[68:71], a[8:11]\n" \
"v_mfma_f32_16x16x32_bf16 a[12:15], v[152:155], v[64:67], a[12:15]\n" \
"ds_read_b64_tr_b16 v[162:163], v50 offset:5120\n" \
"v_mfma_f32_16x16x32_bf16 a[12:15], v[156:159], v[68:71], a[12:15]\n" \
"global_load_lds_dwordx4 v41, s[50:51] offset:-3072\n" \
"v_mfma_f32_16x16x32_bf16 a[32:35], v[128:131], v[72:75], a[32:35]\n" \
"ds_read_b64_tr_b16 v[164:165], v50 offset:17408\n" \
"v_mfma_f32_16x16x32_bf16 a[32:35], v[132:135], v[76:79], a[32:35]\n" \
"v_mfma_f32_16x16x32_bf16 a[36:39], v[136:139], v[72:75], a[36:39]\n" \
"ds_read_b64_tr_b16 v[166:167], v50 offset:21504\n" \
"v_mfma_f32_16x16x32_bf16 a[36:39], v[140:143], v[76:79], a[36:39]\n" \
"global_load_lds_dwordx4 v42, s[52:53] offset:-2048\n" \
"v_mfma_f32_16x16x32_bf16 a[40:43], v[144:147], v[72:75], a[40:43]\n" \
"ds_read_b64_tr_b16 v[168:169], v51 offset:1024\n" \
"v_mfma_f32_16x16x32_bf16 a[40:43], v[148:151], v[76:79], a[40:43]\n" \
"v_mfma_f32_16x16x32_bf16 a[44:47], v[152:155], v[72:75], a[44:47]\n" \
"ds_read_b64_tr_b16 v[170:171], v51 offset:5120\n" \
"v_mfma_f32_16x16x32_bf16 a[44:47], v[156:159], v[76:79], a[44:47]\n" \
"global_load_lds_dwordx4 v43, s[50:51] offset:-1024\n" \
"v_mfma_f32_16x16x32_bf16 a[64:67], v[128:131], v[80:83], a[64:67]\n" \
"ds_read_b64_tr_b16 v[172:173], v51 offset:17408\n" \
"v_mfma_f32_16x16x32_bf16 a[64:67], v[132:135], v[84:87], a[64:67]\n" \
"v_mfma_f32_16x16x32_bf16 a[68:71], v[136:139], v[80:83], a[68:71]\n" \
"ds_read_b64_tr_b16 v[174:175], v51 offset:21504\n" \
"v_mfma_f32_16x16x32_bf16 a[68:71], v[140:143], v[84:87], a[68:71]\n" \
"global_load_lds_dwordx4 v44, s[52:53] offset:0\n" \
"v_mfma_f32_16x16x32_bf16 a[72:75], v[144:147], v[80:83], a[72:75]\n" \
"ds_read_b64_tr_b16 v[176:177], v50 offset:1536\n" \
"v_mfma_f32_16x16x32_bf16 a[72:75], v[148:151], v[84:87], a[72:75]\n" \
"v_mfma_f32_16x16x32_bf16 a[76:79], v[152:155], v[80:83], a[76:79]\n" \
"ds_read_b64_tr_b16 v[178:179], v50 offset:5632\n" \
"v_mfma_f32_16x16x32_bf16 a[76:79], v[156:159], v[84:87], a[76:79]\n" \
"global_load_lds_dwordx4 v45, s[50:51] offset:1024\n" \
"v_mfma_f32_16x16x32_bf16 a[96:99], v[128:131], v[88:91], a[96:99]\n" \
"ds_read_b64_tr_b16 v[180:181], v50 offset:17920\n" \
"v_mfma_f32_16x16x32_bf16 a[96:99], v[132:135], v[92:95], a[96:99]\n" \
"v_mfma_f32_16x16x32_bf16 a[100:103], v[136:139], v[88:91], a[100:103]\n" \
"ds_read_b64_tr_b16 v[182:183], v50 offset:22016\n" \
"v_mfma_f32_16x16x32_bf16 a[100:103], v[140:143], v[92:95], a[100:103]\n" \
"global_load_lds_dwordx4 v46, s[52:53] offset:2048\n" \
"v_mfma_f32_16x16x32_bf16 a[104:107], v[144:147], v[88:91], a[104:107]\n" \
"ds_read_b64_tr_b16 v[184:185], v51 offset:1536\n" \
"v_mfma_f32_16x16x32_bf16 a[104:107], v[148:151], v[92:95], a[104:107]\n" \
"v_mfma_f32_16x16x32_bf16 a[108:111], v[152:155], v[88:91], a[108:111]\n" \
"ds_read_b64_tr_b16 v[186:187], v51 offset:5632\n" \
"v_mfma_f32_16x16x32_bf16 a[108:111], v[156:159], v[92:95], a[108:111]\n" \
"global_load_lds_dwordx4 v47, s[50:51] offset:3072\n" \
"v_mfma_f32_16x16x32_bf16 a[128:131], v[128:131], v[96:99], a[128:131]\n" \
"s_add_u32 s52, s52, 0x80000\n" \
"s_addc_u32 s53, s53, 0\n" \
"ds_read_b64_tr_b16 v[188:189], v51 offset:17920\n" \
"v_mfma_f32_16x16x32_bf16 a[128:131], v[132:135], v[100:103], a[128:131]\n" \
"s_add_u32 s50, s50, 0x80000\n" \
"s_addc_u32 s51, s51, 0\n" \
"v_mfma_f32_16x16x32_bf16 a[132:135], v[136:139], v[96:99], a[132:135]\n" \
"ds_read_b64_tr_b16 v[190:191], v51 offset:22016\n" \
"v_mfma_f32_16x16x32_bf16 a[132:135], v[140:143], v[100:103], a[132:135]\n" \
"v_mfma_f32_16x16x32_bf16 a[136:139], v[144:147], v[96:99], a[136:139]\n" \
"v_mfma_f32_16x16x32_bf16 a[136:139], v[148:151], v[100:103], a[136:139]\n" \
"v_mfma_f32_16x16x32_bf16 a[140:143], v[152:155], v[96:99], a[140:143]\n" \
"v_mfma_f32_16x16x32_bf16 a[140:143], v[156:159], v[100:103], a[140:143]\n" \
"v_mfma_f32_16x16x32_bf16 a[160:163], v[128:131], v[104:107], a[160:163]\n" \
"v_add_u32 v48, 0x18000, v48\n" \
"v_add_u32 v49, 0x18000, v49\n" \
"v_mfma_f32_16x16x32_bf16 a[160:163], v[132:135], v[108:111], a[160:163]\n" \
"v_add_u32 v50, 0xfffe8000, v50\n" \
"v_add_u32 v51, 0xfffe8000, v51\n" \
"v_mfma_f32_16x16x32_bf16 a[164:167], v[136:139], v[104:107], a[164:167]\n" \
"v_mfma_f32_16x16x32_bf16 a[164:167], v[140:143], v[108:111], a[164:167]\n" \
"v_mfma_f32_16x16x32_bf16 a[168:171], v[144:147], v[104:107], a[168:171]\n" \
"v_mfma_f32_16x16x32_bf16 a[168:171], v[148:151], v[108:111], a[168:171]\n" \
"v_mfma_f32_16x16x32_bf16 a[172:175], v[152:155], v[104:107], a[172:175]\n" \
"v_mfma_f32_16x16x32_bf16 a[172:175], v[156:159], v[108:111], a[172:175]\n" \
"s_waitcnt lgkmcnt(0)\n" \
"v_mfma_f32_16x16x32_bf16 a[192:195], v[128:131], v[112:115], a[192:195]\n" \
"v_mfma_f32_16x16x32_bf16 a[192:195], v[132:135], v[116:119], a[192:195]\n" \
"v_mfma_f32_16x16x32_bf16 a[196:199], v[136:139], v[112:115], a[196:199]\n" \
"v_mfma_f32_16x16x32_bf16 a[196:199], v[140:143], v[116:119], a[196:199]\n" \
"v_mfma_f32_16x16x32_bf16 a[200:203], v[144:147], v[112:115], a[200:203]\n" \
"v_mfma_f32_16x16x32_bf16 a[200:203], v[148:151], v[116:119], a[200:203]\n" \
"v_mfma_f32_16x16x32_bf16 a[204:207], v[152:155], v[112:115], a[204:207]\n" \
"v_mfma_f32_16x16x32_bf16 a[204:207], v[156:159], v[116:119], a[204:207]\n" \
"v_mfma_f32_16x16x32_bf16 a[224:227], v[128:131], v[120:123], a[224:227]\n" \
"v_mfma_f32_16x16x32_bf16 a[224:227], v[132:135], v[124:127], a[224:227]\n" \
"v_mfma_f32_16x16x32_bf16 a[228:231], v[136:139], v[120:123], a[228:231]\n" \
"v_mfma_f32_16x16x32_bf16 a[228:231], v[140:143], v[124:127], a[228:231]\n" \
"v_mfma_f32_16x16x32_bf16 a[232:235], v[144:147], v[120:123], a[232:235]\n" \
"v_mfma_f32_16x16x32_bf16 a[232:235], v[148:151], v[124:127], a[232:235]\n" \
"v_mfma_f32_16x16x32_bf16 a[236:239], v[152:155], v[120:123], a[236:239]\n" \
"s_waitcnt vmcnt(10) lgkmcnt(0)\n" \
"v_mfma_f32_16x16x32_bf16 a[236:239], v[156:159], v[124:127], a[236:239]\n" \
"s_barrier\n" \
"v_mfma_f32_16x16x32_bf16 a[16:19], v[64:67], v[160:163], a[16:19]\n" \
"s_add_u32 m0, s87, 102400\n" \
"ds_read_b64_tr_b16 v[128:129], v50 offset:0\n" \
"v_mfma_f32_16x16x32_bf16 a[16:19], v[68:71], v[164:167], a[16:19]\n" \
"global_load_lds_dwordx4 v32, s[54:55] offset:-4096\n" \
"v_mfma_f32_16x16x32_bf16 a[20:23], v[64:67], v[168:171], a[20:23]\n" \
"ds_read_b64_tr_b16 v[130:131], v50 offset:4096\n" \
"v_mfma_f32_16x16x32_bf16 a[20:23], v[68:71], v[172:175], a[20:23]\n" \
"v_mfma_f32_16x16x32_bf16 a[24:27], v[64:67], v[176:179], a[24:27]\n" \
"ds_read_b64_tr_b16 v[132:133], v50 offset:16384\n" \
"v_mfma_f32_16x16x32_bf16 a[24:27], v[68:71], v[180:183], a[24:27]\n" \
"global_load_lds_dwordx4 v33, s[54:55] offset:-3072\n" \
"v_mfma_f32_16x16x32_bf16 a[28:31], v[64:67], v[184:187], a[28:31]\n" \
"ds_read_b64_tr_b16 v[134:135], v50 offset:20480\n" \
"v_mfma_f32_16x16x32_bf16 a[28:31], v[68:71], v[188:191], a[28:31]\n" \
"v_mfma_f32_16x16x32_bf16 a[48:51], v[72:75], v[160:163], a[48:51]\n" \
"ds_read_b128 v[64:67], v48 offset:0\n" \
"v_mfma_f32_16x16x32_bf16 a[48:51], v[76:79], v[164:167], a[48:51]\n" \
"global_load_lds_dwordx4 v34, s[54:55] offset:-2048\n" \
"v_mfma_f32_16x16x32_bf16 a[52:55], v[72:75], v[168:171], a[52:55]\n" \
"ds_read_b64_tr_b16 v[136:137], v51 offset:0\n" \
"v_mfma_f32_16x16x32_bf16 a[52:55], v[76:79], v[172:175], a[52:55]\n" \
"v_mfma_f32_16x16x32_bf16 a[56:59], v[72:75], v[176:179], a[56:59]\n" \
"ds_read_b128 v[68:71], v49 offset:0\n" \
"v_mfma_f32_16x16x32_bf16 a[56:59], v[76:79], v[180:183], a[56:59]\n" \
"global_load_lds_dwordx4 v35, s[54:55] offset:-1024\n" \
"v_mfma_f32_16x16x32_bf16 a[60:63], v[72:75], v[184:187], a[60:63]\n" \
"ds_read_b64_tr_b16 v[138:139], v51 offset:4096\n" \
"v_mfma_f32_16x16x32_bf16 a[60:63], v[76:79], v[188:191], a[60:63]\n" \
"v_mfma_f32_16x16x32_bf16 a[80:83], v[80:83], v[160:163], a[80:83]\n" \
"ds_read_b128 v[72:75], v48 offset:2048\n" \
"v_mfma_f32_16x16x32_bf16 a[80:83], v[84:87], v[164:167], a[80:83]\n" \
"global_load_lds_dwordx4 v36, s[54:55] offset:0\n" \
"v_mfma_f32_16x16x32_bf16 a[84:87], v[80:83], v[168:171], a[84:87]\n" \
"ds_read_b64_tr_b16 v[140:141], v51 offset:16384\n" \
"v_mfma_f32_16x16x32_bf16 a[84:87], v[84:87], v[172:175], a[84:87]\n" \
"v_mfma_f32_16x16x32_bf16 a[88:91], v[80:83], v[176:179], a[88:91]\n" \
"ds_read_b128 v[76:79], v49 offset:2048\n" \
"v_mfma_f32_16x16x32_bf16 a[88:91], v[84:87], v[180:183], a[88:91]\n" \
"global_load_lds_dwordx4 v37, s[54:55] offset:1024\n" \
"v_mfma_f32_16x16x32_bf16 a[92:95], v[80:83], v[184:187], a[92:95]\n" \
"ds_read_b64_tr_b16 v[142:143], v51 offset:20480\n" \
"v_mfma_f32_16x16x32_bf16 a[92:95], v[84:87], v[188:191], a[92:95]\n" \
"v_mfma_f32_16x16x32_bf16 a[112:115], v[88:91], v[160:163], a[112:115]\n" \
"ds_read_b128 v[80:83], v48 offset:4096\n" \
"v_mfma_f32_16x16x32_bf16 a[112:115], v[92:95], v[164:167], a[112:115]\n" \
"global_load_lds_dwordx4 v38, s[54:55] offset:2048\n" \
"v_mfma_f32_16x16x32_bf16 a[116:119], v[88:91], v[168:171], a[116:119]\n" \
"ds_read_b64_tr_b16 v[144:145], v50 offset:512\n" \
"v_mfma_f32_16x16x32_bf16 a[116:119], v[92:95], v[172:175], a[116:119]\n" \
"v_mfma_f32_16x16x32_bf16 a[120:123], v[88:91], v[176:179], a[120:123]\n" \
"ds_read_b128 v[84:87], v49 offset:4096\n" \
"v_mfma_f32_16x16x32_bf16 a[120:123], v[92:95], v[180:183], a[120:123]\n" \
"global_load_lds_dwordx4 v39, s[54:55] offset:3072\n" \
"v_mfma_f32_16x16x32_bf16 a[124:127], v[88:91], v[184:187], a[124:127]\n" \
"s_add_u32 s54, s54, 0x80\n" \
"s_addc_u32 s55, s55, 0\n" \
"ds_read_b64_tr_b16 v[146:147], v50 offset:4608\n" \
"v_mfma_f32_16x16x32_bf16 a[124:127], v[92:95], v[188:191], a[124:127]\n" \
"s_add_u32 s68, s66, 0x0\n" \
"s_add_u32 s64, s8, s68\n" \
"s_addc_u32 s65, s9, 0\n" \
"v_mfma_f32_16x16x32_bf16 a[144:147], v[96:99], v[160:163], a[144:147]\n" \
"ds_read_b128 v[88:91], v48 offset:6144\n" \
"v_mfma_f32_16x16x32_bf16 a[144:147], v[100:103], v[164:167], a[144:147]\n" \
"global_load_dword v253, v248, s[64:65]\n" \
"v_mfma_f32_16x16x32_bf16 a[148:151], v[96:99], v[168:171], a[148:151]\n" \
"s_add_u32 s68, s66, 0x100000\n" \
"ds_read_b64_tr_b16 v[148:149], v50 offset:16896\n" \
"v_mfma_f32_16x16x32_bf16 a[148:151], v[100:103], v[172:175], a[148:151]\n" \
"s_add_u32 s64, s8, s68\n" \
"s_addc_u32 s65, s9, 0\n" \
"v_mfma_f32_16x16x32_bf16 a[152:155], v[96:99], v[176:179], a[152:155]\n" \
"ds_read_b128 v[92:95], v49 offset:6144\n" \
"v_mfma_f32_16x16x32_bf16 a[152:155], v[100:103], v[180:183], a[152:155]\n" \
"global_load_dword v253, v248, s[64:65]\n" \
"v_mfma_f32_16x16x32_bf16 a[156:159], v[96:99], v[184:187], a[156:159]\n" \
"ds_read_b64_tr_b16 v[150:151], v50 offset:20992\n" \
"v_mfma_f32_16x16x32_bf16 a[156:159], v[100:103], v[188:191], a[156:159]\n" \
"v_mfma_f32_16x16x32_bf16 a[176:179], v[104:107], v[160:163], a[176:179]\n" \
"ds_read_b128 v[96:99], v48 offset:8192\n" \
"v_mfma_f32_16x16x32_bf16 a[176:179], v[108:111], v[164:167], a[176:179]\n" \
"v_mfma_f32_16x16x32_bf16 a[180:183], v[104:107], v[168:171], a[180:183]\n" \
"ds_read_b64_tr_b16 v[152:153], v51 offset:512\n" \
"v_mfma_f32_16x16x32_bf16 a[180:183], v[108:111], v[172:175], a[180:183]\n" \
"v_mfma_f32_16x16x32_bf16 a[184:187], v[104:107], v[176:179], a[184:187]\n" \
"ds_read_b128 v[100:103], v49 offset:8192\n" \
"v_mfma_f32_16x16x32_bf16 a[184:187], v[108:111], v[180:183], a[184:187]\n" \
"v_mfma_f32_16x16x32_bf16 a[188:191], v[104:107], v[184:187], a[188:191]\n" \
"ds_read_b64_tr_b16 v[154:155], v51 offset:4608\n" \
"v_mfma_f32_16x16x32_bf16 a[188:191], v[108:111], v[188:191], a[188:191]\n" \
"v_mfma_f32_16x16x32_bf16 a[208:211], v[112:115], v[160:163], a[208:211]\n" \
"ds_read_b128 v[104:107], v48 offset:10240\n" \
"v_mfma_f32_16x16x32_bf16 a[208:211], v[116:119], v[164:167], a[208:211]\n" \
"v_mfma_f32_16x16x32_bf16 a[212:215], v[112:115], v[168:171], a[212:215]\n" \
"ds_read_b64_tr_b16 v[156:157], v51 offset:16896\n" \
"v_mfma_f32_16x16x32_bf16 a[212:215], v[116:119], v[172:175], a[212:215]\n" \
"v_mfma_f32_16x16x32_bf16 a[216:219], v[112:115], v[176:179], a[216:219]\n" \
"ds_read_b128 v[108:111], v49 offset:10240\n" \
"v_mfma_f32_16x16x32_bf16 a[216:219], v[116:119], v[180:183], a[216:219]\n" \
"v_mfma_f32_16x16x32_bf16 a[220:223], v[112:115], v[184:187], a[220:223]\n" \
"ds_read_b64_tr_b16 v[158:159], v51 offset:20992\n" \
"v_mfma_f32_16x16x32_bf16 a[220:223], v[116:119], v[188:191], a[220:223]\n" \
"v_mfma_f32_16x16x32_bf16 a[240:243], v[120:123], v[160:163], a[240:243]\n" \
"ds_read_b128 v[112:115], v48 offset:12288\n" \
"v_mfma_f32_16x16x32_bf16 a[240:243], v[124:127], v[164:167], a[240:243]\n" \
"v_mfma_f32_16x16x32_bf16 a[244:247], v[120:123], v[168:171], a[244:247]\n" \
"v_mfma_f32_16x16x32_bf16 a[244:247], v[124:127], v[172:175], a[244:247]\n" \
"v_mfma_f32_16x16x32_bf16 a[248:251], v[120:123], v[176:179], a[248:251]\n" \
"ds_read_b128 v[116:119], v49 offset:12288\n" \
"v_mfma_f32_16x16x32_bf16 a[248:251], v[124:127], v[180:183], a[248:251]\n" \
"v_mfma_f32_16x16x32_bf16 a[252:255], v[120:123], v[184:187], a[252:255]\n" \
"v_mfma_f32_16x16x32_bf16 a[252:255], v[124:127], v[188:191], a[252:255]\n" \
"s_waitcnt lgkmcnt(1)\n" \
"v_mfma_f32_16x16x32_bf16 a[0:3], v[128:131], v[64:67], a[0:3]\n" \
"ds_read_b128 v[120:123], v48 offset:14336\n" \
"v_mfma_f32_16x16x32_bf16 a[0:3], v[132:135], v[68:71], a[0:3]\n" \
"v_mfma_f32_16x16x32_bf16 a[4:7], v[136:139], v[64:67], a[4:7]\n" \
"ds_read_b128 v[124:127], v49 offset:14336\n" \
"s_add_u32 m0, s87, 36864\n" \
"v_mfma_f32_16x16x32_bf16 a[4:7], v[140:143], v[68:71], a[4:7]\n" \
"global_load_lds_dwordx4 v40, s[52:53] offset:-4096\n" \
"v_mfma_f32_16x16x32_bf16 a[8:11], v[144:147], v[64:67], a[8:11]\n" \
"ds_read_b64_tr_b16 v[160:161], v50 offset:1024\n" \
"v_mfma_f32_16x16x32_bf16 a[8:11], v[148:151], v[68:71], a[8:11]\n" \
"v_mfma_f32_16x16x32_bf16 a[12:15], v[152:155], v[64:67], a[12:15]\n" \
"ds_read_b64_tr_b16 v[162:163], v50 offset:5120\n" \
"v_mfma_f32_16x16x32_bf16 a[12:15], v[156:159], v[68:71], a[12:15]\n" \
"global_load_lds_dwordx4 v41, s[50:51] offset:-3072\n" \
"v_mfma_f32_16x16x32_bf16 a[32:35], v[128:131], v[72:75], a[32:35]\n" \
"ds_read_b64_tr_b16 v[164:165], v50 offset:17408\n" \
"v_mfma_f32_16x16x32_bf16 a[32:35], v[132:135], v[76:79], a[32:35]\n" \
"v_mfma_f32_16x16x32_bf16 a[36:39], v[136:139], v[72:75], a[36:39]\n" \
"ds_read_b64_tr_b16 v[166:167], v50 offset:21504\n" \
"v_mfma_f32_16x16x32_bf16 a[36:39], v[140:143], v[76:79], a[36:39]\n" \
"global_load_lds_dwordx4 v42, s[52:53] offset:-2048\n" \
"v_mfma_f32_16x16x32_bf16 a[40:43], v[144:147], v[72:75], a[40:43]\n" \
"ds_read_b64_tr_b16 v[168:169], v51 offset:1024\n" \
"v_mfma_f32_16x16x32_bf16 a[40:43], v[148:151], v[76:79], a[40:43]\n" \
"v_mfma_f32_16x16x32_bf16 a[44:47], v[152:155], v[72:75], a[44:47]\n" \
"ds_read_b64_tr_b16 v[170:171], v51 offset:5120\n" \
"v_mfma_f32_16x16x32_bf16 a[44:47], v[156:159], v[76:79], a[44:47]\n" \
"global_load_lds_dwordx4 v43, s[50:51] offset:-1024\n" \
"v_mfma_f32_16x16x32_bf16 a[64:67], v[128:131], v[80:83], a[64:67]\n" \
"ds_read_b64_tr_b16 v[172:173], v51 offset:17408\n" \
"v_mfma_f32_16x16x32_bf16 a[64:67], v[132:135], v[84:87], a[64:67]\n" \
"v_mfma_f32_16x16x32_bf16 a[68:71], v[136:139], v[80:83], a[68:71]\n" \
"ds_read_b64_tr_b16 v[174:175], v51 offset:21504\n" \
"v_mfma_f32_16x16x32_bf16 a[68:71], v[140:143], v[84:87], a[68:71]\n" \
"global_load_lds_dwordx4 v44, s[52:53] offset:0\n" \
"v_mfma_f32_16x16x32_bf16 a[72:75], v[144:147], v[80:83], a[72:75]\n" \
"ds_read_b64_tr_b16 v[176:177], v50 offset:1536\n" \
"v_mfma_f32_16x16x32_bf16 a[72:75], v[148:151], v[84:87], a[72:75]\n" \
"v_mfma_f32_16x16x32_bf16 a[76:79], v[152:155], v[80:83], a[76:79]\n" \
"ds_read_b64_tr_b16 v[178:179], v50 offset:5632\n" \
"v_mfma_f32_16x16x32_bf16 a[76:79], v[156:159], v[84:87], a[76:79]\n" \
"global_load_lds_dwordx4 v45, s[50:51] offset:1024\n" \
"v_mfma_f32_16x16x32_bf16 a[96:99], v[128:131], v[88:91], a[96:99]\n" \
"ds_read_b64_tr_b16 v[180:181], v50 offset:17920\n" \
"v_mfma_f32_16x16x32_bf16 a[96:99], v[132:135], v[92:95], a[96:99]\n" \
"v_mfma_f32_16x16x32_bf16 a[100:103], v[136:139], v[88:91], a[100:103]\n" \
"ds_read_b64_tr_b16 v[182:183], v50 offset:22016\n" \
"v_mfma_f32_16x16x32_bf16 a[100:103], v[140:143], v[92:95], a[100:103]\n" \
"global_load_lds_dwordx4 v46, s[52:53] offset:2048\n" \
"v_mfma_f32_16x16x32_bf16 a[104:107], v[144:147], v[88:91], a[104:107]\n" \
"ds_read_b64_tr_b16 v[184:185], v51 offset:1536\n" \
"v_mfma_f32_16x16x32_bf16 a[104:107], v[148:151], v[92:95], a[104:107]\n" \
"v_mfma_f32_16x16x32_bf16 a[108:111], v[152:155], v[88:91], a[108:111]\n" \
"ds_read_b64_tr_b16 v[186:187], v51 offset:5632\n" \
"v_mfma_f32_16x16x32_bf16 a[108:111], v[156:159], v[92:95], a[108:111]\n" \
"global_load_lds_dwordx4 v47, s[50:51] offset:3072\n" \
"v_mfma_f32_16x16x32_bf16 a[128:131], v[128:131], v[96:99], a[128:131]\n" \
"s_add_u32 s52, s52, 0x80000\n" \
"s_addc_u32 s53, s53, 0\n" \
"ds_read_b64_tr_b16 v[188:189], v51 offset:17920\n" \
"v_mfma_f32_16x16x32_bf16 a[128:131], v[132:135], v[100:103], a[128:131]\n" \
"s_add_u32 s50, s50, 0x80000\n" \
"s_addc_u32 s51, s51, 0\n" \
"v_mfma_f32_16x16x32_bf16 a[132:135], v[136:139], v[96:99], a[132:135]\n" \
"ds_read_b64_tr_b16 v[190:191], v51 offset:22016\n" \
"v_mfma_f32_16x16x32_bf16 a[132:135], v[140:143], v[100:103], a[132:135]\n" \
"v_mfma_f32_16x16x32_bf16 a[136:139], v[144:147], v[96:99], a[136:139]\n" \
"v_mfma_f32_16x16x32_bf16 a[136:139], v[148:151], v[100:103], a[136:139]\n" \
"v_mfma_f32_16x16x32_bf16 a[140:143], v[152:155], v[96:99], a[140:143]\n" \
"v_mfma_f32_16x16x32_bf16 a[140:143], v[156:159], v[100:103], a[140:143]\n" \
"v_mfma_f32_16x16x32_bf16 a[160:163], v[128:131], v[104:107], a[160:163]\n" \
"v_add_u32 v48, 0xffff8000, v48\n" \
"v_add_u32 v49, 0xffff8000, v49\n" \
"v_mfma_f32_16x16x32_bf16 a[160:163], v[132:135], v[108:111], a[160:163]\n" \
"v_add_u32 v50, 0x10000, v50\n" \
"v_add_u32 v51, 0x10000, v51\n" \
"v_mfma_f32_16x16x32_bf16 a[164:167], v[136:139], v[104:107], a[164:167]\n" \
"v_mfma_f32_16x16x32_bf16 a[164:167], v[140:143], v[108:111], a[164:167]\n" \
"v_mfma_f32_16x16x32_bf16 a[168:171], v[144:147], v[104:107], a[168:171]\n" \
"v_mfma_f32_16x16x32_bf16 a[168:171], v[148:151], v[108:111], a[168:171]\n" \
"v_mfma_f32_16x16x32_bf16 a[172:175], v[152:155], v[104:107], a[172:175]\n" \
"v_mfma_f32_16x16x32_bf16 a[172:175], v[156:159], v[108:111], a[172:175]\n" \
"s_waitcnt lgkmcnt(0)\n" \
"v_mfma_f32_16x16x32_bf16 a[192:195], v[128:131], v[112:115], a[192:195]\n" \
"v_mfma_f32_16x16x32_bf16 a[192:195], v[132:135], v[116:119], a[192:195]\n" \
"v_mfma_f32_16x16x32_bf16 a[196:199], v[136:139], v[112:115], a[196:199]\n" \
"v_mfma_f32_16x16x32_bf16 a[196:199], v[140:143], v[116:119], a[196:199]\n" \
"v_mfma_f32_16x16x32_bf16 a[200:203], v[144:147], v[112:115], a[200:203]\n" \
"v_mfma_f32_16x16x32_bf16 a[200:203], v[148:151], v[116:119], a[200:203]\n" \
"v_mfma_f32_16x16x32_bf16 a[204:207], v[152:155], v[112:115], a[204:207]\n" \
"v_mfma_f32_16x16x32_bf16 a[204:207], v[156:159], v[116:119], a[204:207]\n" \
"v_mfma_f32_16x16x32_bf16 a[224:227], v[128:131], v[120:123], a[224:227]\n" \
"v_mfma_f32_16x16x32_bf16 a[224:227], v[132:135], v[124:127], a[224:227]\n" \
"v_mfma_f32_16x16x32_bf16 a[228:231], v[136:139], v[120:123], a[228:231]\n" \
"v_mfma_f32_16x16x32_bf16 a[228:231], v[140:143], v[124:127], a[228:231]\n" \
"v_mfma_f32_16x16x32_bf16 a[232:235], v[144:147], v[120:123], a[232:235]\n" \
"v_mfma_f32_16x16x32_bf16 a[232:235], v[148:151], v[124:127], a[232:235]\n" \
"v_mfma_f32_16x16x32_bf16 a[236:239], v[152:155], v[120:123], a[236:239]\n" \
"s_waitcnt vmcnt(10) lgkmcnt(0)\n" \
"v_mfma_f32_16x16x32_bf16 a[236:239], v[156:159], v[124:127], a[236:239]\n" \
"s_barrier\n" \
"v_mfma_f32_16x16x32_bf16 a[16:19], v[64:67], v[160:163], a[16:19]\n" \
"s_add_u32 m0, s87, 4096\n" \
"ds_read_b64_tr_b16 v[128:129], v50 offset:0\n" \
"v_mfma_f32_16x16x32_bf16 a[16:19], v[68:71], v[164:167], a[16:19]\n" \
"global_load_lds_dwordx4 v32, s[54:55] offset:-4096\n" \
"v_mfma_f32_16x16x32_bf16 a[20:23], v[64:67], v[168:171], a[20:23]\n" \
"ds_read_b64_tr_b16 v[130:131], v50 offset:4096\n" \
"v_mfma_f32_16x16x32_bf16 a[20:23], v[68:71], v[172:175], a[20:23]\n" \
"v_mfma_f32_16x16x32_bf16 a[24:27], v[64:67], v[176:179], a[24:27]\n" \
"ds_read_b64_tr_b16 v[132:133], v50 offset:16384\n" \
"v_mfma_f32_16x16x32_bf16 a[24:27], v[68:71], v[180:183], a[24:27]\n" \
"global_load_lds_dwordx4 v33, s[54:55] offset:-3072\n" \
"v_mfma_f32_16x16x32_bf16 a[28:31], v[64:67], v[184:187], a[28:31]\n" \
"ds_read_b64_tr_b16 v[134:135], v50 offset:20480\n" \
"v_mfma_f32_16x16x32_bf16 a[28:31], v[68:71], v[188:191], a[28:31]\n" \
"v_mfma_f32_16x16x32_bf16 a[48:51], v[72:75], v[160:163], a[48:51]\n" \
"ds_read_b128 v[64:67], v48 offset:0\n" \
"v_mfma_f32_16x16x32_bf16 a[48:51], v[76:79], v[164:167], a[48:51]\n" \
"global_load_lds_dwordx4 v34, s[54:55] offset:-2048\n" \
"v_mfma_f32_16x16x32_bf16 a[52:55], v[72:75], v[168:171], a[52:55]\n" \
"ds_read_b64_tr_b16 v[136:137], v51 offset:0\n" \
"v_mfma_f32_16x16x32_bf16 a[52:55], v[76:79], v[172:175], a[52:55]\n" \
"v_mfma_f32_16x16x32_bf16 a[56:59], v[72:75], v[176:179], a[56:59]\n" \
"ds_read_b128 v[68:71], v49 offset:0\n" \
"v_mfma_f32_16x16x32_bf16 a[56:59], v[76:79], v[180:183], a[56:59]\n" \
"global_load_lds_dwordx4 v35, s[54:55] offset:-1024\n" \
"v_mfma_f32_16x16x32_bf16 a[60:63], v[72:75], v[184:187], a[60:63]\n" \
"ds_read_b64_tr_b16 v[138:139], v51 offset:4096\n" \
"v_mfma_f32_16x16x32_bf16 a[60:63], v[76:79], v[188:191], a[60:63]\n" \
"v_mfma_f32_16x16x32_bf16 a[80:83], v[80:83], v[160:163], a[80:83]\n" \
"ds_read_b128 v[72:75], v48 offset:2048\n" \
"v_mfma_f32_16x16x32_bf16 a[80:83], v[84:87], v[164:167], a[80:83]\n" \
"global_load_lds_dwordx4 v36, s[54:55] offset:0\n" \
"v_mfma_f32_16x16x32_bf16 a[84:87], v[80:83], v[168:171], a[84:87]\n" \
"ds_read_b64_tr_b16 v[140:141], v51 offset:16384\n" \
"v_mfma_f32_16x16x32_bf16 a[84:87], v[84:87], v[172:175], a[84:87]\n" \
"v_mfma_f32_16x16x32_bf16 a[88:91], v[80:83], v[176:179], a[88:91]\n" \
"ds_read_b128 v[76:79], v49 offset:2048\n" \
"v_mfma_f32_16x16x32_bf16 a[88:91], v[84:87], v[180:183], a[88:91]\n" \
"global_load_lds_dwordx4 v37, s[54:55] offset:1024\n" \
"v_mfma_f32_16x16x32_bf16 a[92:95], v[80:83], v[184:187], a[92:95]\n" \
"ds_read_b64_tr_b16 v[142:143], v51 offset:20480\n" \
"v_mfma_f32_16x16x32_bf16 a[92:95], v[84:87], v[188:191], a[92:95]\n" \
"v_mfma_f32_16x16x32_bf16 a[112:115], v[88:91], v[160:163], a[112:115]\n" \
"ds_read_b128 v[80:83], v48 offset:4096\n" \
"v_mfma_f32_16x16x32_bf16 a[112:115], v[92:95], v[164:167], a[112:115]\n" \
"global_load_lds_dwordx4 v38, s[54:55] offset:2048\n" \
"v_mfma_f32_16x16x32_bf16 a[116:119], v[88:91], v[168:171], a[116:119]\n" \
"ds_read_b64_tr_b16 v[144:145], v50 offset:512\n" \
"v_mfma_f32_16x16x32_bf16 a[116:119], v[92:95], v[172:175], a[116:119]\n" \
"v_mfma_f32_16x16x32_bf16 a[120:123], v[88:91], v[176:179], a[120:123]\n" \
"ds_read_b128 v[84:87], v49 offset:4096\n" \
"v_mfma_f32_16x16x32_bf16 a[120:123], v[92:95], v[180:183], a[120:123]\n" \
"global_load_lds_dwordx4 v39, s[54:55] offset:3072\n" \
"v_mfma_f32_16x16x32_bf16 a[124:127], v[88:91], v[184:187], a[124:127]\n" \
"s_add_u32 s54, s54, 0x80\n" \
"s_addc_u32 s55, s55, 0\n" \
"ds_read_b64_tr_b16 v[146:147], v50 offset:4608\n" \
"v_mfma_f32_16x16x32_bf16 a[124:127], v[92:95], v[188:191], a[124:127]\n" \
"s_add_u32 s68, s66, 0x200000\n" \
"s_add_u32 s64, s8, s68\n" \
"s_addc_u32 s65, s9, 0\n" \
"v_mfma_f32_16x16x32_bf16 a[144:147], v[96:99], v[160:163], a[144:147]\n" \
"ds_read_b128 v[88:91], v48 offset:6144\n" \
"v_mfma_f32_16x16x32_bf16 a[144:147], v[100:103], v[164:167], a[144:147]\n" \
"global_load_dword v253, v248, s[64:65]\n" \
"v_mfma_f32_16x16x32_bf16 a[148:151], v[96:99], v[168:171], a[148:151]\n" \
"s_add_u32 s68, s66, 0x300000\n" \
"ds_read_b64_tr_b16 v[148:149], v50 offset:16896\n" \
"v_mfma_f32_16x16x32_bf16 a[148:151], v[100:103], v[172:175], a[148:151]\n" \
"s_add_u32 s64, s8, s68\n" \
"s_addc_u32 s65, s9, 0\n" \
"v_mfma_f32_16x16x32_bf16 a[152:155], v[96:99], v[176:179], a[152:155]\n" \
"ds_read_b128 v[92:95], v49 offset:6144\n" \
"v_mfma_f32_16x16x32_bf16 a[152:155], v[100:103], v[180:183], a[152:155]\n" \
"global_load_dword v253, v248, s[64:65]\n" \
"v_mfma_f32_16x16x32_bf16 a[156:159], v[96:99], v[184:187], a[156:159]\n" \
"s_add_u32 s68, s67, 0x0\n" \
"ds_read_b64_tr_b16 v[150:151], v50 offset:20992\n" \
"v_mfma_f32_16x16x32_bf16 a[156:159], v[100:103], v[188:191], a[156:159]\n" \
"s_add_u32 s64, s12, s68\n" \
"s_addc_u32 s65, s13, 0\n" \
"v_mfma_f32_16x16x32_bf16 a[176:179], v[104:107], v[160:163], a[176:179]\n" \
"Lpg1119_%=:\n" \
"Lst0x1_%=:\n" \
"s_cbranch_execz Lpg3267_%=\n" \
"ds_read_b128 v[96:99], v48 offset:8192\n" \
"v_mfma_f32_16x16x32_bf16 a[176:179], v[108:111], v[164:167], a[176:179]\n" \
"global_load_dword v253, v248, s[64:65]\n" \
"v_mfma_f32_16x16x32_bf16 a[180:183], v[104:107], v[168:171], a[180:183]\n" \
"ds_read_b64_tr_b16 v[152:153], v51 offset:512\n" \
"v_mfma_f32_16x16x32_bf16 a[180:183], v[108:111], v[172:175], a[180:183]\n" \
"v_mfma_f32_16x16x32_bf16 a[184:187], v[104:107], v[176:179], a[184:187]\n" \
"ds_read_b128 v[100:103], v49 offset:8192\n" \
"v_mfma_f32_16x16x32_bf16 a[184:187], v[108:111], v[180:183], a[184:187]\n" \
"v_mfma_f32_16x16x32_bf16 a[188:191], v[104:107], v[184:187], a[188:191]\n" \
"ds_read_b64_tr_b16 v[154:155], v51 offset:4608\n" \
"v_mfma_f32_16x16x32_bf16 a[188:191], v[108:111], v[188:191], a[188:191]\n" \
"v_mfma_f32_16x16x32_bf16 a[208:211], v[112:115], v[160:163], a[208:211]\n" \
"ds_read_b128 v[104:107], v48 offset:10240\n" \
"v_mfma_f32_16x16x32_bf16 a[208:211], v[116:119], v[164:167], a[208:211]\n" \
"v_mfma_f32_16x16x32_bf16 a[212:215], v[112:115], v[168:171], a[212:215]\n" \
"ds_read_b64_tr_b16 v[156:157], v51 offset:16896\n" \
"v_mfma_f32_16x16x32_bf16 a[212:215], v[116:119], v[172:175], a[212:215]\n" \
"v_mfma_f32_16x16x32_bf16 a[216:219], v[112:115], v[176:179], a[216:219]\n" \
"ds_read_b128 v[108:111], v49 offset:10240\n" \
"v_mfma_f32_16x16x32_bf16 a[216:219], v[116:119], v[180:183], a[216:219]\n" \
"v_mfma_f32_16x16x32_bf16 a[220:223], v[112:115], v[184:187], a[220:223]\n" \
"ds_read_b64_tr_b16 v[158:159], v51 offset:20992\n" \
"v_mfma_f32_16x16x32_bf16 a[220:223], v[116:119], v[188:191], a[220:223]\n" \
"v_mfma_f32_16x16x32_bf16 a[240:243], v[120:123], v[160:163], a[240:243]\n" \
"ds_read_b128 v[112:115], v48 offset:12288\n" \
"v_mfma_f32_16x16x32_bf16 a[240:243], v[124:127], v[164:167], a[240:243]\n" \
"v_mfma_f32_16x16x32_bf16 a[244:247], v[120:123], v[168:171], a[244:247]\n" \
"v_mfma_f32_16x16x32_bf16 a[244:247], v[124:127], v[172:175], a[244:247]\n" \
"v_mfma_f32_16x16x32_bf16 a[248:251], v[120:123], v[176:179], a[248:251]\n" \
"ds_read_b128 v[116:119], v49 offset:12288\n" \
"v_mfma_f32_16x16x32_bf16 a[248:251], v[124:127], v[180:183], a[248:251]\n" \
"v_mfma_f32_16x16x32_bf16 a[252:255], v[120:123], v[184:187], a[252:255]\n" \
"v_mfma_f32_16x16x32_bf16 a[252:255], v[124:127], v[188:191], a[252:255]\n" \
"s_waitcnt lgkmcnt(1)\n" \
"v_mfma_f32_16x16x32_bf16 a[0:3], v[128:131], v[64:67], a[0:3]\n" \
"ds_read_b128 v[120:123], v48 offset:14336\n" \
"v_mfma_f32_16x16x32_bf16 a[0:3], v[132:135], v[68:71], a[0:3]\n" \
"v_mfma_f32_16x16x32_bf16 a[4:7], v[136:139], v[64:67], a[4:7]\n" \
"ds_read_b128 v[124:127], v49 offset:14336\n" \
"s_add_u32 m0, s87, 135168\n" \
"v_mfma_f32_16x16x32_bf16 a[4:7], v[140:143], v[68:71], a[4:7]\n" \
"global_load_lds_dwordx4 v40, s[52:53] offset:-4096\n" \
"v_mfma_f32_16x16x32_bf16 a[8:11], v[144:147], v[64:67], a[8:11]\n" \
"ds_read_b64_tr_b16 v[160:161], v50 offset:1024\n" \
"v_mfma_f32_16x16x32_bf16 a[8:11], v[148:151], v[68:71], a[8:11]\n" \
"v_mfma_f32_16x16x32_bf16 a[12:15], v[152:155], v[64:67], a[12:15]\n" \
"ds_read_b64_tr_b16 v[162:163], v50 offset:5120\n" \
"v_mfma_f32_16x16x32_bf16 a[12:15], v[156:159], v[68:71], a[12:15]\n" \
"global_load_lds_dwordx4 v41, s[50:51] offset:-3072\n" \
"v_mfma_f32_16x16x32_bf16 a[32:35], v[128:131], v[72:75], a[32:35]\n" \
"ds_read_b64_tr_b16 v[164:165], v50 offset:17408\n" \
"v_mfma_f32_16x16x32_bf16 a[32:35], v[132:135], v[76:79], a[32:35]\n" \
"v_mfma_f32_16x16x32_bf16 a[36:39], v[136:139], v[72:75], a[36:39]\n" \
"ds_read_b64_tr_b16 v[166:167], v50 offset:21504\n" \
"v_mfma_f32_16x16x32_bf16 a[36:39], v[140:143], v[76:79], a[36:39]\n" \
"global_load_lds_dwordx4 v42, s[52:53] offset:-2048\n" \
"v_mfma_f32_16x16x32_bf16 a[40:43], v[144:147], v[72:75], a[40:43]\n" \
"ds_read_b64_tr_b16 v[168:169], v51 offset:1024\n" \
"v_mfma_f32_16x16x32_bf16 a[40:43], v[148:151], v[76:79], a[40:43]\n" \
"v_mfma_f32_16x16x32_bf16 a[44:47], v[152:155], v[72:75], a[44:47]\n" \
"ds_read_b64_tr_b16 v[170:171], v51 offset:5120\n" \
"v_mfma_f32_16x16x32_bf16 a[44:47], v[156:159], v[76:79], a[44:47]\n" \
"global_load_lds_dwordx4 v43, s[50:51] offset:-1024\n" \
"v_mfma_f32_16x16x32_bf16 a[64:67], v[128:131], v[80:83], a[64:67]\n" \
"ds_read_b64_tr_b16 v[172:173], v51 offset:17408\n" \
"v_mfma_f32_16x16x32_bf16 a[64:67], v[132:135], v[84:87], a[64:67]\n" \
"v_mfma_f32_16x16x32_bf16 a[68:71], v[136:139], v[80:83], a[68:71]\n" \
"ds_read_b64_tr_b16 v[174:175], v51 offset:21504\n" \
"v_mfma_f32_16x16x32_bf16 a[68:71], v[140:143], v[84:87], a[68:71]\n" \
"global_load_lds_dwordx4 v44, s[52:53] offset:0\n" \
"v_mfma_f32_16x16x32_bf16 a[72:75], v[144:147], v[80:83], a[72:75]\n" \
"ds_read_b64_tr_b16 v[176:177], v50 offset:1536\n" \
"v_mfma_f32_16x16x32_bf16 a[72:75], v[148:151], v[84:87], a[72:75]\n" \
"v_mfma_f32_16x16x32_bf16 a[76:79], v[152:155], v[80:83], a[76:79]\n" \
"ds_read_b64_tr_b16 v[178:179], v50 offset:5632\n" \
"v_mfma_f32_16x16x32_bf16 a[76:79], v[156:159], v[84:87], a[76:79]\n" \
"global_load_lds_dwordx4 v45, s[50:51] offset:1024\n" \
"v_mfma_f32_16x16x32_bf16 a[96:99], v[128:131], v[88:91], a[96:99]\n" \
"ds_read_b64_tr_b16 v[180:181], v50 offset:17920\n" \
"v_mfma_f32_16x16x32_bf16 a[96:99], v[132:135], v[92:95], a[96:99]\n" \
"v_mfma_f32_16x16x32_bf16 a[100:103], v[136:139], v[88:91], a[100:103]\n" \
"ds_read_b64_tr_b16 v[182:183], v50 offset:22016\n" \
"v_mfma_f32_16x16x32_bf16 a[100:103], v[140:143], v[92:95], a[100:103]\n" \
"global_load_lds_dwordx4 v46, s[52:53] offset:2048\n" \
"v_mfma_f32_16x16x32_bf16 a[104:107], v[144:147], v[88:91], a[104:107]\n" \
"ds_read_b64_tr_b16 v[184:185], v51 offset:1536\n" \
"v_mfma_f32_16x16x32_bf16 a[104:107], v[148:151], v[92:95], a[104:107]\n" \
"v_mfma_f32_16x16x32_bf16 a[108:111], v[152:155], v[88:91], a[108:111]\n" \
"ds_read_b64_tr_b16 v[186:187], v51 offset:5632\n" \
"v_mfma_f32_16x16x32_bf16 a[108:111], v[156:159], v[92:95], a[108:111]\n" \
"global_load_lds_dwordx4 v47, s[50:51] offset:3072\n" \
"v_mfma_f32_16x16x32_bf16 a[128:131], v[128:131], v[96:99], a[128:131]\n" \
"s_add_u32 s52, s52, 0x80000\n" \
"s_addc_u32 s53, s53, 0\n" \
"ds_read_b64_tr_b16 v[188:189], v51 offset:17920\n" \
"v_mfma_f32_16x16x32_bf16 a[128:131], v[132:135], v[100:103], a[128:131]\n" \
"s_add_u32 s50, s50, 0x80000\n" \
"s_addc_u32 s51, s51, 0\n" \
"v_mfma_f32_16x16x32_bf16 a[132:135], v[136:139], v[96:99], a[132:135]\n" \
"ds_read_b64_tr_b16 v[190:191], v51 offset:22016\n" \
"v_mfma_f32_16x16x32_bf16 a[132:135], v[140:143], v[100:103], a[132:135]\n" \
"v_mfma_f32_16x16x32_bf16 a[136:139], v[144:147], v[96:99], a[136:139]\n" \
"v_mfma_f32_16x16x32_bf16 a[136:139], v[148:151], v[100:103], a[136:139]\n" \
"v_mfma_f32_16x16x32_bf16 a[140:143], v[152:155], v[96:99], a[140:143]\n" \
"v_mfma_f32_16x16x32_bf16 a[140:143], v[156:159], v[100:103], a[140:143]\n" \
"v_mfma_f32_16x16x32_bf16 a[160:163], v[128:131], v[104:107], a[160:163]\n" \
"v_add_u32 v48, 0xfffe8000, v48\n" \
"v_add_u32 v49, 0xfffe8000, v49\n" \
"v_mfma_f32_16x16x32_bf16 a[160:163], v[132:135], v[108:111], a[160:163]\n" \
"v_add_u32 v50, 0xffff8000, v50\n" \
"v_add_u32 v51, 0xffff8000, v51\n" \
"v_mfma_f32_16x16x32_bf16 a[164:167], v[136:139], v[104:107], a[164:167]\n" \
"v_mfma_f32_16x16x32_bf16 a[164:167], v[140:143], v[108:111], a[164:167]\n" \
"v_mfma_f32_16x16x32_bf16 a[168:171], v[144:147], v[104:107], a[168:171]\n" \
"v_mfma_f32_16x16x32_bf16 a[168:171], v[148:151], v[108:111], a[168:171]\n" \
"v_mfma_f32_16x16x32_bf16 a[172:175], v[152:155], v[104:107], a[172:175]\n" \
"v_mfma_f32_16x16x32_bf16 a[172:175], v[156:159], v[108:111], a[172:175]\n" \
"s_waitcnt lgkmcnt(0)\n" \
"v_mfma_f32_16x16x32_bf16 a[192:195], v[128:131], v[112:115], a[192:195]\n" \
"v_mfma_f32_16x16x32_bf16 a[192:195], v[132:135], v[116:119], a[192:195]\n" \
"v_mfma_f32_16x16x32_bf16 a[196:199], v[136:139], v[112:115], a[196:199]\n" \
"v_mfma_f32_16x16x32_bf16 a[196:199], v[140:143], v[116:119], a[196:199]\n" \
"v_mfma_f32_16x16x32_bf16 a[200:203], v[144:147], v[112:115], a[200:203]\n" \
"v_mfma_f32_16x16x32_bf16 a[200:203], v[148:151], v[116:119], a[200:203]\n" \
"v_mfma_f32_16x16x32_bf16 a[204:207], v[152:155], v[112:115], a[204:207]\n" \
"v_mfma_f32_16x16x32_bf16 a[204:207], v[156:159], v[116:119], a[204:207]\n" \
"v_mfma_f32_16x16x32_bf16 a[224:227], v[128:131], v[120:123], a[224:227]\n" \
"v_mfma_f32_16x16x32_bf16 a[224:227], v[132:135], v[124:127], a[224:227]\n" \
"v_mfma_f32_16x16x32_bf16 a[228:231], v[136:139], v[120:123], a[228:231]\n" \
"v_mfma_f32_16x16x32_bf16 a[228:231], v[140:143], v[124:127], a[228:231]\n" \
"v_mfma_f32_16x16x32_bf16 a[232:235], v[144:147], v[120:123], a[232:235]\n" \
"v_mfma_f32_16x16x32_bf16 a[232:235], v[148:151], v[124:127], a[232:235]\n" \
"v_mfma_f32_16x16x32_bf16 a[236:239], v[152:155], v[120:123], a[236:239]\n" \
"s_waitcnt vmcnt(11) lgkmcnt(0)\n" \
"v_mfma_f32_16x16x32_bf16 a[236:239], v[156:159], v[124:127], a[236:239]\n" \
"s_barrier\n" \
"v_mfma_f32_16x16x32_bf16 a[16:19], v[64:67], v[160:163], a[16:19]\n" \
"s_add_u32 m0, s87, 69632\n" \
"ds_read_b64_tr_b16 v[128:129], v50 offset:0\n" \
"v_mfma_f32_16x16x32_bf16 a[16:19], v[68:71], v[164:167], a[16:19]\n" \
"global_load_lds_dwordx4 v32, s[54:55] offset:-4096\n" \
"v_mfma_f32_16x16x32_bf16 a[20:23], v[64:67], v[168:171], a[20:23]\n" \
"ds_read_b64_tr_b16 v[130:131], v50 offset:4096\n" \
"v_mfma_f32_16x16x32_bf16 a[20:23], v[68:71], v[172:175], a[20:23]\n" \
"v_mfma_f32_16x16x32_bf16 a[24:27], v[64:67], v[176:179], a[24:27]\n" \
"ds_read_b64_tr_b16 v[132:133], v50 offset:16384\n" \
"v_mfma_f32_16x16x32_bf16 a[24:27], v[68:71], v[180:183], a[24:27]\n" \
"global_load_lds_dwordx4 v33, s[54:55] offset:-3072\n" \
"v_mfma_f32_16x16x32_bf16 a[28:31], v[64:67], v[184:187], a[28:31]\n" \
"ds_read_b64_tr_b16 v[134:135], v50 offset:20480\n" \
"v_mfma_f32_16x16x32_bf16 a[28:31], v[68:71], v[188:191], a[28:31]\n" \
"v_mfma_f32_16x16x32_bf16 a[48:51], v[72:75], v[160:163], a[48:51]\n" \
"ds_read_b128 v[64:67], v48 offset:0\n" \
"v_mfma_f32_16x16x32_bf16 a[48:51], v[76:79], v[164:167], a[48:51]\n" \
"global_load_lds_dwordx4 v34, s[54:55] offset:-2048\n" \
"v_mfma_f32_16x16x32_bf16 a[52:55], v[72:75], v[168:171], a[52:55]\n" \
"ds_read_b64_tr_b16 v[136:137], v51 offset:0\n" \
"v_mfma_f32_16x16x32_bf16 a[52:55], v[76:79], v[172:175], a[52:55]\n" \
"v_mfma_f32_16x16x32_bf16 a[56:59], v[72:75], v[176:179], a[56:59]\n" \
"ds_read_b128 v[68:71], v49 offset:0\n" \
"v_mfma_f32_16x16x32_bf16 a[56:59], v[76:79], v[180:183], a[56:59]\n" \
"global_load_lds_dwordx4 v35, s[54:55] offset:-1024\n" \
"v_mfma_f32_16x16x32_bf16 a[60:63], v[72:75], v[184:187], a[60:63]\n" \
"ds_read_b64_tr_b16 v[138:139], v51 offset:4096\n" \
"v_mfma_f32_16x16x32_bf16 a[60:63], v[76:79], v[188:191], a[60:63]\n" \
"v_mfma_f32_16x16x32_bf16 a[80:83], v[80:83], v[160:163], a[80:83]\n" \
"ds_read_b128 v[72:75], v48 offset:2048\n" \
"v_mfma_f32_16x16x32_bf16 a[80:83], v[84:87], v[164:167], a[80:83]\n" \
"global_load_lds_dwordx4 v36, s[54:55] offset:0\n" \
"v_mfma_f32_16x16x32_bf16 a[84:87], v[80:83], v[168:171], a[84:87]\n" \
"ds_read_b64_tr_b16 v[140:141], v51 offset:16384\n" \
"v_mfma_f32_16x16x32_bf16 a[84:87], v[84:87], v[172:175], a[84:87]\n" \
"v_mfma_f32_16x16x32_bf16 a[88:91], v[80:83], v[176:179], a[88:91]\n" \
"ds_read_b128 v[76:79], v49 offset:2048\n" \
"v_mfma_f32_16x16x32_bf16 a[88:91], v[84:87], v[180:183], a[88:91]\n" \
"global_load_lds_dwordx4 v37, s[54:55] offset:1024\n" \
"v_mfma_f32_16x16x32_bf16 a[92:95], v[80:83], v[184:187], a[92:95]\n" \
"ds_read_b64_tr_b16 v[142:143], v51 offset:20480\n" \
"v_mfma_f32_16x16x32_bf16 a[92:95], v[84:87], v[188:191], a[92:95]\n" \
"v_mfma_f32_16x16x32_bf16 a[112:115], v[88:91], v[160:163], a[112:115]\n" \
"ds_read_b128 v[80:83], v48 offset:4096\n" \
"v_mfma_f32_16x16x32_bf16 a[112:115], v[92:95], v[164:167], a[112:115]\n" \
"global_load_lds_dwordx4 v38, s[54:55] offset:2048\n" \
"v_mfma_f32_16x16x32_bf16 a[116:119], v[88:91], v[168:171], a[116:119]\n" \
"ds_read_b64_tr_b16 v[144:145], v50 offset:512\n" \
"v_mfma_f32_16x16x32_bf16 a[116:119], v[92:95], v[172:175], a[116:119]\n" \
"v_mfma_f32_16x16x32_bf16 a[120:123], v[88:91], v[176:179], a[120:123]\n" \
"ds_read_b128 v[84:87], v49 offset:4096\n" \
"v_mfma_f32_16x16x32_bf16 a[120:123], v[92:95], v[180:183], a[120:123]\n" \
"global_load_lds_dwordx4 v39, s[54:55] offset:3072\n" \
"v_mfma_f32_16x16x32_bf16 a[124:127], v[88:91], v[184:187], a[124:127]\n" \
"s_add_u32 s54, s54, 0x80\n" \
"s_addc_u32 s55, s55, 0\n" \
"ds_read_b64_tr_b16 v[146:147], v50 offset:4608\n" \
"v_mfma_f32_16x16x32_bf16 a[124:127], v[92:95], v[188:191], a[124:127]\n" \
"s_add_u32 s68, s67, 0x100000\n" \
"s_add_u32 s64, s12, s68\n" \
"s_addc_u32 s65, s13, 0\n" \
"v_mfma_f32_16x16x32_bf16 a[144:147], v[96:99], v[160:163], a[144:147]\n" \
"ds_read_b128 v[88:91], v48 offset:6144\n" \
"v_mfma_f32_16x16x32_bf16 a[144:147], v[100:103], v[164:167], a[144:147]\n" \
"global_load_dword v253, v248, s[64:65]\n" \
"v_mfma_f32_16x16x32_bf16 a[148:151], v[96:99], v[168:171], a[148:151]\n" \
"s_add_u32 s68, s67, 0x500000\n" \
"ds_read_b64_tr_b16 v[148:149], v50 offset:16896\n" \
"v_mfma_f32_16x16x32_bf16 a[148:151], v[100:103], v[172:175], a[148:151]\n" \
"s_add_u32 s64, s12, s68\n" \
"s_addc_u32 s65, s13, 0\n" \
"v_mfma_f32_16x16x32_bf16 a[152:155], v[96:99], v[176:179], a[152:155]\n" \
"ds_read_b128 v[92:95], v49 offset:6144\n" \
"v_mfma_f32_16x16x32_bf16 a[152:155], v[100:103], v[180:183], a[152:155]\n" \
"global_load_dword v253, v248, s[64:65]\n" \
"v_mfma_f32_16x16x32_bf16 a[156:159], v[96:99], v[184:187], a[156:159]\n" \
"s_add_u32 s68, s67, 0x200000\n" \
"ds_read_b64_tr_b16 v[150:151], v50 offset:20992\n" \
"v_mfma_f32_16x16x32_bf16 a[156:159], v[100:103], v[188:191], a[156:159]\n" \
"s_add_u32 s64, s12, s68\n" \
"s_addc_u32 s65, s13, 0\n" \
"v_mfma_f32_16x16x32_bf16 a[176:179], v[104:107], v[160:163], a[176:179]\n" \
"ds_read_b128 v[96:99], v48 offset:8192\n" \
"v_mfma_f32_16x16x32_bf16 a[176:179], v[108:111], v[164:167], a[176:179]\n" \
"global_load_dword v253, v248, s[64:65]\n" \
"v_mfma_f32_16x16x32_bf16 a[180:183], v[104:107], v[168:171], a[180:183]\n" \
"ds_read_b64_tr_b16 v[152:153], v51 offset:512\n" \
"v_mfma_f32_16x16x32_bf16 a[180:183], v[108:111], v[172:175], a[180:183]\n" \
"v_mfma_f32_16x16x32_bf16 a[184:187], v[104:107], v[176:179], a[184:187]\n" \
"ds_read_b128 v[100:103], v49 offset:8192\n" \
"v_mfma_f32_16x16x32_bf16 a[184:187], v[108:111], v[180:183], a[184:187]\n" \
"v_mfma_f32_16x16x32_bf16 a[188:191], v[104:107], v[184:187], a[188:191]\n" \
"ds_read_b64_tr_b16 v[154:155], v51 offset:4608\n" \
"v_mfma_f32_16x16x32_bf16 a[188:191], v[108:111], v[188:191], a[188:191]\n" \
"v_mfma_f32_16x16x32_bf16 a[208:211], v[112:115], v[160:163], a[208:211]\n" \
"ds_read_b128 v[104:107], v48 offset:10240\n" \
"v_mfma_f32_16x16x32_bf16 a[208:211], v[116:119], v[164:167], a[208:211]\n" \
"v_mfma_f32_16x16x32_bf16 a[212:215], v[112:115], v[168:171], a[212:215]\n" \
"ds_read_b64_tr_b16 v[156:157], v51 offset:16896\n" \
"v_mfma_f32_16x16x32_bf16 a[212:215], v[116:119], v[172:175], a[212:215]\n" \
"v_mfma_f32_16x16x32_bf16 a[216:219], v[112:115], v[176:179], a[216:219]\n" \
"ds_read_b128 v[108:111], v49 offset:10240\n" \
"v_mfma_f32_16x16x32_bf16 a[216:219], v[116:119], v[180:183], a[216:219]\n" \
"v_mfma_f32_16x16x32_bf16 a[220:223], v[112:115], v[184:187], a[220:223]\n" \
"ds_read_b64_tr_b16 v[158:159], v51 offset:20992\n" \
"v_mfma_f32_16x16x32_bf16 a[220:223], v[116:119], v[188:191], a[220:223]\n" \
"v_mfma_f32_16x16x32_bf16 a[240:243], v[120:123], v[160:163], a[240:243]\n" \
"ds_read_b128 v[112:115], v48 offset:12288\n" \
"v_mfma_f32_16x16x32_bf16 a[240:243], v[124:127], v[164:167], a[240:243]\n" \
"v_mfma_f32_16x16x32_bf16 a[244:247], v[120:123], v[168:171], a[244:247]\n" \
"v_mfma_f32_16x16x32_bf16 a[244:247], v[124:127], v[172:175], a[244:247]\n" \
"v_mfma_f32_16x16x32_bf16 a[248:251], v[120:123], v[176:179], a[248:251]\n" \
"ds_read_b128 v[116:119], v49 offset:12288\n" \
"v_mfma_f32_16x16x32_bf16 a[248:251], v[124:127], v[180:183], a[248:251]\n" \
"v_mfma_f32_16x16x32_bf16 a[252:255], v[120:123], v[184:187], a[252:255]\n" \
"v_mfma_f32_16x16x32_bf16 a[252:255], v[124:127], v[188:191], a[252:255]\n" \
"s_waitcnt lgkmcnt(1)\n" \
"v_mfma_f32_16x16x32_bf16 a[0:3], v[128:131], v[64:67], a[0:3]\n" \
"ds_read_b128 v[120:123], v48 offset:14336\n" \
"v_mfma_f32_16x16x32_bf16 a[0:3], v[132:135], v[68:71], a[0:3]\n" \
"v_mfma_f32_16x16x32_bf16 a[4:7], v[136:139], v[64:67], a[4:7]\n" \
"ds_read_b128 v[124:127], v49 offset:14336\n" \
"s_add_u32 m0, s87, 102400\n" \
"v_mfma_f32_16x16x32_bf16 a[4:7], v[140:143], v[68:71], a[4:7]\n" \
"global_load_lds_dwordx4 v40, s[52:53] offset:-4096\n" \
"v_mfma_f32_16x16x32_bf16 a[8:11], v[144:147], v[64:67], a[8:11]\n" \
"ds_read_b64_tr_b16 v[160:161], v50 offset:1024\n" \
"v_mfma_f32_16x16x32_bf16 a[8:11], v[148:151], v[68:71], a[8:11]\n" \
"v_mfma_f32_16x16x32_bf16 a[12:15], v[152:155], v[64:67], a[12:15]\n" \
"ds_read_b64_tr_b16 v[162:163], v50 offset:5120\n" \
"v_mfma_f32_16x16x32_bf16 a[12:15], v[156:159], v[68:71], a[12:15]\n" \
"global_load_lds_dwordx4 v41, s[50:51] offset:-3072\n" \
"v_mfma_f32_16x16x32_bf16 a[32:35], v[128:131], v[72:75], a[32:35]\n" \
"ds_read_b64_tr_b16 v[164:165], v50 offset:17408\n" \
"v_mfma_f32_16x16x32_bf16 a[32:35], v[132:135], v[76:79], a[32:35]\n" \
"v_mfma_f32_16x16x32_bf16 a[36:39], v[136:139], v[72:75], a[36:39]\n" \
"ds_read_b64_tr_b16 v[166:167], v50 offset:21504\n" \
"v_mfma_f32_16x16x32_bf16 a[36:39], v[140:143], v[76:79], a[36:39]\n" \
"global_load_lds_dwordx4 v42, s[52:53] offset:-2048\n" \
"v_mfma_f32_16x16x32_bf16 a[40:43], v[144:147], v[72:75], a[40:43]\n" \
"ds_read_b64_tr_b16 v[168:169], v51 offset:1024\n" \
"v_mfma_f32_16x16x32_bf16 a[40:43], v[148:151], v[76:79], a[40:43]\n" \
"v_mfma_f32_16x16x32_bf16 a[44:47], v[152:155], v[72:75], a[44:47]\n" \
"ds_read_b64_tr_b16 v[170:171], v51 offset:5120\n" \
"v_mfma_f32_16x16x32_bf16 a[44:47], v[156:159], v[76:79], a[44:47]\n" \
"global_load_lds_dwordx4 v43, s[50:51] offset:-1024\n" \
"v_mfma_f32_16x16x32_bf16 a[64:67], v[128:131], v[80:83], a[64:67]\n" \
"ds_read_b64_tr_b16 v[172:173], v51 offset:17408\n" \
"v_mfma_f32_16x16x32_bf16 a[64:67], v[132:135], v[84:87], a[64:67]\n" \
"v_mfma_f32_16x16x32_bf16 a[68:71], v[136:139], v[80:83], a[68:71]\n" \
"ds_read_b64_tr_b16 v[174:175], v51 offset:21504\n" \
"v_mfma_f32_16x16x32_bf16 a[68:71], v[140:143], v[84:87], a[68:71]\n" \
"global_load_lds_dwordx4 v44, s[52:53] offset:0\n" \
"v_mfma_f32_16x16x32_bf16 a[72:75], v[144:147], v[80:83], a[72:75]\n" \
"ds_read_b64_tr_b16 v[176:177], v50 offset:1536\n" \
"v_mfma_f32_16x16x32_bf16 a[72:75], v[148:151], v[84:87], a[72:75]\n" \
"v_mfma_f32_16x16x32_bf16 a[76:79], v[152:155], v[80:83], a[76:79]\n" \
"ds_read_b64_tr_b16 v[178:179], v50 offset:5632\n" \
"v_mfma_f32_16x16x32_bf16 a[76:79], v[156:159], v[84:87], a[76:79]\n" \
"global_load_lds_dwordx4 v45, s[50:51] offset:1024\n" \
"v_mfma_f32_16x16x32_bf16 a[96:99], v[128:131], v[88:91], a[96:99]\n" \
"ds_read_b64_tr_b16 v[180:181], v50 offset:17920\n" \
"v_mfma_f32_16x16x32_bf16 a[96:99], v[132:135], v[92:95], a[96:99]\n" \
"v_mfma_f32_16x16x32_bf16 a[100:103], v[136:139], v[88:91], a[100:103]\n" \
"ds_read_b64_tr_b16 v[182:183], v50 offset:22016\n" \
"v_mfma_f32_16x16x32_bf16 a[100:103], v[140:143], v[92:95], a[100:103]\n" \
"global_load_lds_dwordx4 v46, s[52:53] offset:2048\n" \
"v_mfma_f32_16x16x32_bf16 a[104:107], v[144:147], v[88:91], a[104:107]\n" \
"ds_read_b64_tr_b16 v[184:185], v51 offset:1536\n" \
"v_mfma_f32_16x16x32_bf16 a[104:107], v[148:151], v[92:95], a[104:107]\n" \
"v_mfma_f32_16x16x32_bf16 a[108:111], v[152:155], v[88:91], a[108:111]\n" \
"ds_read_b64_tr_b16 v[186:187], v51 offset:5632\n" \
"v_mfma_f32_16x16x32_bf16 a[108:111], v[156:159], v[92:95], a[108:111]\n" \
"global_load_lds_dwordx4 v47, s[50:51] offset:3072\n" \
"v_mfma_f32_16x16x32_bf16 a[128:131], v[128:131], v[96:99], a[128:131]\n" \
"ds_read_b64_tr_b16 v[188:189], v51 offset:17920\n" \
"v_mfma_f32_16x16x32_bf16 a[128:131], v[132:135], v[100:103], a[128:131]\n" \
"v_mfma_f32_16x16x32_bf16 a[132:135], v[136:139], v[96:99], a[132:135]\n" \
"ds_read_b64_tr_b16 v[190:191], v51 offset:22016\n" \
"v_mfma_f32_16x16x32_bf16 a[132:135], v[140:143], v[100:103], a[132:135]\n" \
"v_mfma_f32_16x16x32_bf16 a[136:139], v[144:147], v[96:99], a[136:139]\n" \
"v_mfma_f32_16x16x32_bf16 a[136:139], v[148:151], v[100:103], a[136:139]\n" \
"v_mfma_f32_16x16x32_bf16 a[140:143], v[152:155], v[96:99], a[140:143]\n" \
"v_mfma_f32_16x16x32_bf16 a[140:143], v[156:159], v[100:103], a[140:143]\n" \
"v_mfma_f32_16x16x32_bf16 a[160:163], v[128:131], v[104:107], a[160:163]\n" \
"v_add_u32 v48, 0x10000, v48\n" \
"v_add_u32 v49, 0x10000, v49\n" \
"v_mfma_f32_16x16x32_bf16 a[160:163], v[132:135], v[108:111], a[160:163]\n" \
"v_add_u32 v50, 0x18000, v50\n" \
"v_add_u32 v51, 0x18000, v51\n" \
"v_mfma_f32_16x16x32_bf16 a[164:167], v[136:139], v[104:107], a[164:167]\n" \
"v_mfma_f32_16x16x32_bf16 a[164:167], v[140:143], v[108:111], a[164:167]\n" \
"v_mfma_f32_16x16x32_bf16 a[168:171], v[144:147], v[104:107], a[168:171]\n" \
"v_mfma_f32_16x16x32_bf16 a[168:171], v[148:151], v[108:111], a[168:171]\n" \
"v_mfma_f32_16x16x32_bf16 a[172:175], v[152:155], v[104:107], a[172:175]\n" \
"v_mfma_f32_16x16x32_bf16 a[172:175], v[156:159], v[108:111], a[172:175]\n" \
"s_waitcnt lgkmcnt(0)\n" \
"v_mfma_f32_16x16x32_bf16 a[192:195], v[128:131], v[112:115], a[192:195]\n" \
"v_mfma_f32_16x16x32_bf16 a[192:195], v[132:135], v[116:119], a[192:195]\n" \
"v_mfma_f32_16x16x32_bf16 a[196:199], v[136:139], v[112:115], a[196:199]\n" \
"v_mfma_f32_16x16x32_bf16 a[196:199], v[140:143], v[116:119], a[196:199]\n" \
"v_mfma_f32_16x16x32_bf16 a[200:203], v[144:147], v[112:115], a[200:203]\n" \
"v_mfma_f32_16x16x32_bf16 a[200:203], v[148:151], v[116:119], a[200:203]\n" \
"v_mfma_f32_16x16x32_bf16 a[204:207], v[152:155], v[112:115], a[204:207]\n" \
"v_mfma_f32_16x16x32_bf16 a[204:207], v[156:159], v[116:119], a[204:207]\n" \
"v_mfma_f32_16x16x32_bf16 a[224:227], v[128:131], v[120:123], a[224:227]\n" \
"v_mfma_f32_16x16x32_bf16 a[224:227], v[132:135], v[124:127], a[224:227]\n" \
"v_mfma_f32_16x16x32_bf16 a[228:231], v[136:139], v[120:123], a[228:231]\n" \
"v_mfma_f32_16x16x32_bf16 a[228:231], v[140:143], v[124:127], a[228:231]\n" \
"v_mfma_f32_16x16x32_bf16 a[232:235], v[144:147], v[120:123], a[232:235]\n" \
"v_mfma_f32_16x16x32_bf16 a[232:235], v[148:151], v[124:127], a[232:235]\n" \
"v_mfma_f32_16x16x32_bf16 a[236:239], v[152:155], v[120:123], a[236:239]\n" \
"s_waitcnt vmcnt(11) lgkmcnt(0)\n" \
"v_mfma_f32_16x16x32_bf16 a[236:239], v[156:159], v[124:127], a[236:239]\n" \
"s_barrier\n" \
"v_mfma_f32_16x16x32_bf16 a[16:19], v[64:67], v[160:163], a[16:19]\n" \
"s_add_u32 m0, s87, 36864\n" \
"ds_read_b64_tr_b16 v[128:129], v50 offset:0\n" \
"v_mfma_f32_16x16x32_bf16 a[16:19], v[68:71], v[164:167], a[16:19]\n" \
"global_load_lds_dwordx4 v32, s[54:55] offset:-4096\n" \
"v_mfma_f32_16x16x32_bf16 a[20:23], v[64:67], v[168:171], a[20:23]\n" \
"ds_read_b64_tr_b16 v[130:131], v50 offset:4096\n" \
"v_mfma_f32_16x16x32_bf16 a[20:23], v[68:71], v[172:175], a[20:23]\n" \
"v_mfma_f32_16x16x32_bf16 a[24:27], v[64:67], v[176:179], a[24:27]\n" \
"ds_read_b64_tr_b16 v[132:133], v50 offset:16384\n" \
"v_mfma_f32_16x16x32_bf16 a[24:27], v[68:71], v[180:183], a[24:27]\n" \
"global_load_lds_dwordx4 v33, s[54:55] offset:-3072\n" \
"v_mfma_f32_16x16x32_bf16 a[28:31], v[64:67], v[184:187], a[28:31]\n" \
"ds_read_b64_tr_b16 v[134:135], v50 offset:20480\n" \
"v_mfma_f32_16x16x32_bf16 a[28:31], v[68:71], v[188:191], a[28:31]\n" \
"v_mfma_f32_16x16x32_bf16 a[48:51], v[72:75], v[160:163], a[48:51]\n" \
"ds_read_b128 v[64:67], v48 offset:0\n" \
"v_mfma_f32_16x16x32_bf16 a[48:51], v[76:79], v[164:167], a[48:51]\n" \
"global_load_lds_dwordx4 v34, s[54:55] offset:-2048\n" \
"v_mfma_f32_16x16x32_bf16 a[52:55], v[72:75], v[168:171], a[52:55]\n" \
"ds_read_b64_tr_b16 v[136:137], v51 offset:0\n" \
"v_mfma_f32_16x16x32_bf16 a[52:55], v[76:79], v[172:175], a[52:55]\n" \
"v_mfma_f32_16x16x32_bf16 a[56:59], v[72:75], v[176:179], a[56:59]\n" \
"ds_read_b128 v[68:71], v49 offset:0\n" \
"v_mfma_f32_16x16x32_bf16 a[56:59], v[76:79], v[180:183], a[56:59]\n" \
"global_load_lds_dwordx4 v35, s[54:55] offset:-1024\n" \
"v_mfma_f32_16x16x32_bf16 a[60:63], v[72:75], v[184:187], a[60:63]\n" \
"ds_read_b64_tr_b16 v[138:139], v51 offset:4096\n" \
"v_mfma_f32_16x16x32_bf16 a[60:63], v[76:79], v[188:191], a[60:63]\n" \
"v_mfma_f32_16x16x32_bf16 a[80:83], v[80:83], v[160:163], a[80:83]\n" \
"ds_read_b128 v[72:75], v48 offset:2048\n" \
"v_mfma_f32_16x16x32_bf16 a[80:83], v[84:87], v[164:167], a[80:83]\n" \
"global_load_lds_dwordx4 v36, s[54:55] offset:0\n" \
"v_mfma_f32_16x16x32_bf16 a[84:87], v[80:83], v[168:171], a[84:87]\n" \
"ds_read_b64_tr_b16 v[140:141], v51 offset:16384\n" \
"v_mfma_f32_16x16x32_bf16 a[84:87], v[84:87], v[172:175], a[84:87]\n" \
"v_mfma_f32_16x16x32_bf16 a[88:91], v[80:83], v[176:179], a[88:91]\n" \
"ds_read_b128 v[76:79], v49 offset:2048\n" \
"v_mfma_f32_16x16x32_bf16 a[88:91], v[84:87], v[180:183], a[88:91]\n" \
"global_load_lds_dwordx4 v37, s[54:55] offset:1024\n" \
"v_mfma_f32_16x16x32_bf16 a[92:95], v[80:83], v[184:187], a[92:95]\n" \
"ds_read_b64_tr_b16 v[142:143], v51 offset:20480\n" \
"v_mfma_f32_16x16x32_bf16 a[92:95], v[84:87], v[188:191], a[92:95]\n" \
"v_mfma_f32_16x16x32_bf16 a[112:115], v[88:91], v[160:163], a[112:115]\n" \
"ds_read_b128 v[80:83], v48 offset:4096\n" \
"v_mfma_f32_16x16x32_bf16 a[112:115], v[92:95], v[164:167], a[112:115]\n" \
"global_load_lds_dwordx4 v38, s[54:55] offset:2048\n" \
"v_mfma_f32_16x16x32_bf16 a[116:119], v[88:91], v[168:171], a[116:119]\n" \
"ds_read_b64_tr_b16 v[144:145], v50 offset:512\n" \
"v_mfma_f32_16x16x32_bf16 a[116:119], v[92:95], v[172:175], a[116:119]\n" \
"v_mfma_f32_16x16x32_bf16 a[120:123], v[88:91], v[176:179], a[120:123]\n" \
"ds_read_b128 v[84:87], v49 offset:4096\n" \
"v_mfma_f32_16x16x32_bf16 a[120:123], v[92:95], v[180:183], a[120:123]\n" \
"global_load_lds_dwordx4 v39, s[54:55] offset:3072\n" \
"s_add_u32 s68, s67, 0x300000\n" \
"v_mfma_f32_16x16x32_bf16 a[124:127], v[88:91], v[184:187], a[124:127]\n" \
"s_add_u32 s64, s12, s68\n" \
"s_addc_u32 s65, s13, 0\n" \
"ds_read_b64_tr_b16 v[146:147], v50 offset:4608\n" \
"v_mfma_f32_16x16x32_bf16 a[124:127], v[92:95], v[188:191], a[124:127]\n" \
"v_mfma_f32_16x16x32_bf16 a[144:147], v[96:99], v[160:163], a[144:147]\n" \
"ds_read_b128 v[88:91], v48 offset:6144\n" \
"v_mfma_f32_16x16x32_bf16 a[144:147], v[100:103], v[164:167], a[144:147]\n" \
"global_load_dword v253, v248, s[64:65]\n" \
"v_mfma_f32_16x16x32_bf16 a[148:151], v[96:99], v[168:171], a[148:151]\n" \
"s_add_u32 s68, s67, 0x400000\n" \
"ds_read_b64_tr_b16 v[148:149], v50 offset:16896\n" \
"v_mfma_f32_16x16x32_bf16 a[148:151], v[100:103], v[172:175], a[148:151]\n" \
"s_add_u32 s64, s12, s68\n" \
"s_addc_u32 s65, s13, 0\n" \
"v_mfma_f32_16x16x32_bf16 a[152:155], v[96:99], v[176:179], a[152:155]\n" \
"ds_read_b128 v[92:95], v49 offset:6144\n" \
"v_mfma_f32_16x16x32_bf16 a[152:155], v[100:103], v[180:183], a[152:155]\n" \
"global_load_dword v253, v248, s[64:65]\n" \
"v_mfma_f32_16x16x32_bf16 a[156:159], v[96:99], v[184:187], a[156:159]\n" \
"s_add_u32 s68, s67, 0x600000\n" \
"ds_read_b64_tr_b16 v[150:151], v50 offset:20992\n" \
"v_mfma_f32_16x16x32_bf16 a[156:159], v[100:103], v[188:191], a[156:159]\n" \
"s_add_u32 s64, s12, s68\n" \
"s_addc_u32 s65, s13, 0\n" \
"v_mfma_f32_16x16x32_bf16 a[176:179], v[104:107], v[160:163], a[176:179]\n" \
"ds_read_b128 v[96:99], v48 offset:8192\n" \
"v_mfma_f32_16x16x32_bf16 a[176:179], v[108:111], v[164:167], a[176:179]\n" \
"global_load_dword v253, v248, s[64:65]\n" \
"v_mfma_f32_16x16x32_bf16 a[180:183], v[104:107], v[168:171], a[180:183]\n" \
"ds_read_b64_tr_b16 v[152:153], v51 offset:512\n" \
"v_mfma_f32_16x16x32_bf16 a[180:183], v[108:111], v[172:175], a[180:183]\n" \
"v_mfma_f32_16x16x32_bf16 a[184:187], v[104:107], v[176:179], a[184:187]\n" \
"ds_read_b128 v[100:103], v49 offset:8192\n" \
"v_mfma_f32_16x16x32_bf16 a[184:187], v[108:111], v[180:183], a[184:187]\n" \
"v_mfma_f32_16x16x32_bf16 a[188:191], v[104:107], v[184:187], a[188:191]\n" \
"ds_read_b64_tr_b16 v[154:155], v51 offset:4608\n" \
"v_mfma_f32_16x16x32_bf16 a[188:191], v[108:111], v[188:191], a[188:191]\n" \
"v_mfma_f32_16x16x32_bf16 a[208:211], v[112:115], v[160:163], a[208:211]\n" \
"ds_read_b128 v[104:107], v48 offset:10240\n" \
"v_mfma_f32_16x16x32_bf16 a[208:211], v[116:119], v[164:167], a[208:211]\n" \
"v_mfma_f32_16x16x32_bf16 a[212:215], v[112:115], v[168:171], a[212:215]\n" \
"ds_read_b64_tr_b16 v[156:157], v51 offset:16896\n" \
"v_mfma_f32_16x16x32_bf16 a[212:215], v[116:119], v[172:175], a[212:215]\n" \
"v_mfma_f32_16x16x32_bf16 a[216:219], v[112:115], v[176:179], a[216:219]\n" \
"ds_read_b128 v[108:111], v49 offset:10240\n" \
"v_mfma_f32_16x16x32_bf16 a[216:219], v[116:119], v[180:183], a[216:219]\n" \
"v_mfma_f32_16x16x32_bf16 a[220:223], v[112:115], v[184:187], a[220:223]\n" \
"ds_read_b64_tr_b16 v[158:159], v51 offset:20992\n" \
"v_mfma_f32_16x16x32_bf16 a[220:223], v[116:119], v[188:191], a[220:223]\n" \
"v_mfma_f32_16x16x32_bf16 a[240:243], v[120:123], v[160:163], a[240:243]\n" \
"ds_read_b128 v[112:115], v48 offset:12288\n" \
"v_mfma_f32_16x16x32_bf16 a[240:243], v[124:127], v[164:167], a[240:243]\n" \
"v_mfma_f32_16x16x32_bf16 a[244:247], v[120:123], v[168:171], a[244:247]\n" \
"v_mfma_f32_16x16x32_bf16 a[244:247], v[124:127], v[172:175], a[244:247]\n" \
"v_mfma_f32_16x16x32_bf16 a[248:251], v[120:123], v[176:179], a[248:251]\n" \
"ds_read_b128 v[116:119], v49 offset:12288\n" \
"v_mfma_f32_16x16x32_bf16 a[248:251], v[124:127], v[180:183], a[248:251]\n" \
"v_mfma_f32_16x16x32_bf16 a[252:255], v[120:123], v[184:187], a[252:255]\n" \
"v_mfma_f32_16x16x32_bf16 a[252:255], v[124:127], v[188:191], a[252:255]\n" \
"s_waitcnt lgkmcnt(1)\n" \
"v_mfma_f32_16x16x32_bf16 a[0:3], v[128:131], v[64:67], a[0:3]\n" \
"ds_read_b128 v[120:123], v48 offset:14336\n" \
"v_mfma_f32_16x16x32_bf16 a[0:3], v[132:135], v[68:71], a[0:3]\n" \
"v_mfma_f32_16x16x32_bf16 a[4:7], v[136:139], v[64:67], a[4:7]\n" \
"ds_read_b128 v[124:127], v49 offset:14336\n" \
"s_add_u32 m0, s95, 4096\n" \
"v_mfma_f32_16x16x32_bf16 a[4:7], v[140:143], v[68:71], a[4:7]\n" \
"global_load_lds_dwordx4 v228, s[44:45] offset:-4096\n" \
"v_mfma_f32_16x16x32_bf16 a[8:11], v[144:147], v[64:67], a[8:11]\n" \
"ds_read_b64_tr_b16 v[160:161], v50 offset:1024\n" \
"v_mfma_f32_16x16x32_bf16 a[8:11], v[148:151], v[68:71], a[8:11]\n" \
"v_mfma_f32_16x16x32_bf16 a[12:15], v[152:155], v[64:67], a[12:15]\n" \
"ds_read_b64_tr_b16 v[162:163], v50 offset:5120\n" \
"v_mfma_f32_16x16x32_bf16 a[12:15], v[156:159], v[68:71], a[12:15]\n" \
"global_load_lds_dwordx4 v229, s[44:45] offset:-3072\n" \
"v_mfma_f32_16x16x32_bf16 a[32:35], v[128:131], v[72:75], a[32:35]\n" \
"s_add_u32 s44, s44, 0x180000\n" \
"s_addc_u32 s45, s45, 0\n" \
"ds_read_b64_tr_b16 v[164:165], v50 offset:17408\n" \
"v_mfma_f32_16x16x32_bf16 a[32:35], v[132:135], v[76:79], a[32:35]\n" \
"v_mfma_f32_16x16x32_bf16 a[36:39], v[136:139], v[72:75], a[36:39]\n" \
"ds_read_b64_tr_b16 v[166:167], v50 offset:21504\n" \
"v_mfma_f32_16x16x32_bf16 a[36:39], v[140:143], v[76:79], a[36:39]\n" \
"global_load_lds_dwordx4 v228, s[42:43] offset:-2048\n" \
"v_mfma_f32_16x16x32_bf16 a[40:43], v[144:147], v[72:75], a[40:43]\n" \
"ds_read_b64_tr_b16 v[168:169], v51 offset:1024\n" \
"v_mfma_f32_16x16x32_bf16 a[40:43], v[148:151], v[76:79], a[40:43]\n" \
"v_mfma_f32_16x16x32_bf16 a[44:47], v[152:155], v[72:75], a[44:47]\n" \
"ds_read_b64_tr_b16 v[170:171], v51 offset:5120\n" \
"v_mfma_f32_16x16x32_bf16 a[44:47], v[156:159], v[76:79], a[44:47]\n" \
"global_load_lds_dwordx4 v229, s[42:43] offset:-1024\n" \
"v_mfma_f32_16x16x32_bf16 a[64:67], v[128:131], v[80:83], a[64:67]\n" \
"s_add_u32 s42, s42, 0x180000\n" \
"s_addc_u32 s43, s43, 0\n" \
"ds_read_b64_tr_b16 v[172:173], v51 offset:17408\n" \
"v_mfma_f32_16x16x32_bf16 a[64:67], v[132:135], v[84:87], a[64:67]\n" \
"v_mfma_f32_16x16x32_bf16 a[68:71], v[136:139], v[80:83], a[68:71]\n" \
"s_add_u32 m0, s95, 20480\n" \
"ds_read_b64_tr_b16 v[174:175], v51 offset:21504\n" \
"v_mfma_f32_16x16x32_bf16 a[68:71], v[140:143], v[84:87], a[68:71]\n" \
"global_load_lds_dwordx4 v228, s[44:45] offset:-4096\n" \
"v_mfma_f32_16x16x32_bf16 a[72:75], v[144:147], v[80:83], a[72:75]\n" \
"ds_read_b64_tr_b16 v[176:177], v50 offset:1536\n" \
"v_mfma_f32_16x16x32_bf16 a[72:75], v[148:151], v[84:87], a[72:75]\n" \
"v_mfma_f32_16x16x32_bf16 a[76:79], v[152:155], v[80:83], a[76:79]\n" \
"ds_read_b64_tr_b16 v[178:179], v50 offset:5632\n" \
"v_mfma_f32_16x16x32_bf16 a[76:79], v[156:159], v[84:87], a[76:79]\n" \
"global_load_lds_dwordx4 v229, s[44:45] offset:-3072\n" \
"v_mfma_f32_16x16x32_bf16 a[96:99], v[128:131], v[88:91], a[96:99]\n" \
"s_add_u32 s44, s44, 0x400000\n" \
"s_addc_u32 s45, s45, 0\n" \
"ds_read_b64_tr_b16 v[180:181], v50 offset:17920\n" \
"v_mfma_f32_16x16x32_bf16 a[96:99], v[132:135], v[92:95], a[96:99]\n" \
"v_mfma_f32_16x16x32_bf16 a[100:103], v[136:139], v[88:91], a[100:103]\n" \
"ds_read_b64_tr_b16 v[182:183], v50 offset:22016\n" \
"v_mfma_f32_16x16x32_bf16 a[100:103], v[140:143], v[92:95], a[100:103]\n" \
"Lpg1645_%=:\n" \
"Lst0x2_%=:\n" \
"s_cbranch_execz Lpg3808_%=\n" \
"global_load_lds_dwordx4 v228, s[42:43] offset:-2048\n" \
"v_mfma_f32_16x16x32_bf16 a[104:107], v[144:147], v[88:91], a[104:107]\n" \
"ds_read_b64_tr_b16 v[184:185], v51 offset:1536\n" \
"v_mfma_f32_16x16x32_bf16 a[104:107], v[148:151], v[92:95], a[104:107]\n" \
"v_mfma_f32_16x16x32_bf16 a[108:111], v[152:155], v[88:91], a[108:111]\n" \
"ds_read_b64_tr_b16 v[186:187], v51 offset:5632\n" \
"v_mfma_f32_16x16x32_bf16 a[108:111], v[156:159], v[92:95], a[108:111]\n" \
"global_load_lds_dwordx4 v229, s[42:43] offset:-1024\n" \
"v_mfma_f32_16x16x32_bf16 a[128:131], v[128:131], v[96:99], a[128:131]\n" \
"s_add_u32 s42, s42, 0x400000\n" \
"s_addc_u32 s43, s43, 0\n" \
"ds_read_b64_tr_b16 v[188:189], v51 offset:17920\n" \
"v_mfma_f32_16x16x32_bf16 a[128:131], v[132:135], v[100:103], a[128:131]\n" \
"v_mfma_f32_16x16x32_bf16 a[132:135], v[136:139], v[96:99], a[132:135]\n" \
"ds_read_b64_tr_b16 v[190:191], v51 offset:22016\n" \
"v_mfma_f32_16x16x32_bf16 a[132:135], v[140:143], v[100:103], a[132:135]\n" \
"v_mfma_f32_16x16x32_bf16 a[136:139], v[144:147], v[96:99], a[136:139]\n" \
"v_mfma_f32_16x16x32_bf16 a[136:139], v[148:151], v[100:103], a[136:139]\n" \
"v_mfma_f32_16x16x32_bf16 a[140:143], v[152:155], v[96:99], a[140:143]\n" \
"v_mfma_f32_16x16x32_bf16 a[140:143], v[156:159], v[100:103], a[140:143]\n" \
"v_mfma_f32_16x16x32_bf16 a[160:163], v[128:131], v[104:107], a[160:163]\n" \
"v_add_u32 v48, 0xffff8000, v48\n" \
"v_add_u32 v49, 0xffff8000, v49\n" \
"v_mfma_f32_16x16x32_bf16 a[160:163], v[132:135], v[108:111], a[160:163]\n" \
"v_add_u32 v50, 0xffff8000, v50\n" \
"v_add_u32 v51, 0xffff8000, v51\n" \
"v_mfma_f32_16x16x32_bf16 a[164:167], v[136:139], v[104:107], a[164:167]\n" \
"v_mfma_f32_16x16x32_bf16 a[164:167], v[140:143], v[108:111], a[164:167]\n" \
"v_mfma_f32_16x16x32_bf16 a[168:171], v[144:147], v[104:107], a[168:171]\n" \
"v_mfma_f32_16x16x32_bf16 a[168:171], v[148:151], v[108:111], a[168:171]\n" \
"v_mfma_f32_16x16x32_bf16 a[172:175], v[152:155], v[104:107], a[172:175]\n" \
"v_mfma_f32_16x16x32_bf16 a[172:175], v[156:159], v[108:111], a[172:175]\n" \
"s_waitcnt lgkmcnt(0)\n" \
"v_mfma_f32_16x16x32_bf16 a[192:195], v[128:131], v[112:115], a[192:195]\n" \
"v_mfma_f32_16x16x32_bf16 a[192:195], v[132:135], v[116:119], a[192:195]\n" \
"v_mfma_f32_16x16x32_bf16 a[196:199], v[136:139], v[112:115], a[196:199]\n" \
"v_mfma_f32_16x16x32_bf16 a[196:199], v[140:143], v[116:119], a[196:199]\n" \
"v_mfma_f32_16x16x32_bf16 a[200:203], v[144:147], v[112:115], a[200:203]\n" \
"v_mfma_f32_16x16x32_bf16 a[200:203], v[148:151], v[116:119], a[200:203]\n" \
"v_mfma_f32_16x16x32_bf16 a[204:207], v[152:155], v[112:115], a[204:207]\n" \
"v_mfma_f32_16x16x32_bf16 a[204:207], v[156:159], v[116:119], a[204:207]\n" \
"v_mfma_f32_16x16x32_bf16 a[224:227], v[128:131], v[120:123], a[224:227]\n" \
"v_mfma_f32_16x16x32_bf16 a[224:227], v[132:135], v[124:127], a[224:227]\n" \
"v_mfma_f32_16x16x32_bf16 a[228:231], v[136:139], v[120:123], a[228:231]\n" \
"v_mfma_f32_16x16x32_bf16 a[228:231], v[140:143], v[124:127], a[228:231]\n" \
"v_mfma_f32_16x16x32_bf16 a[232:235], v[144:147], v[120:123], a[232:235]\n" \
"v_mfma_f32_16x16x32_bf16 a[232:235], v[148:151], v[124:127], a[232:235]\n" \
"v_mfma_f32_16x16x32_bf16 a[236:239], v[152:155], v[120:123], a[236:239]\n" \
"s_waitcnt vmcnt(11) lgkmcnt(0)\n" \
"v_mfma_f32_16x16x32_bf16 a[236:239], v[156:159], v[124:127], a[236:239]\n" \
"s_barrier\n" \
"v_mfma_f32_16x16x32_bf16 a[16:19], v[64:67], v[160:163], a[16:19]\n" \
"s_add_u32 m0, s87, 135168\n" \
"ds_read_b64_tr_b16 v[128:129], v50 offset:0\n" \
"v_mfma_f32_16x16x32_bf16 a[16:19], v[68:71], v[164:167], a[16:19]\n" \
"global_load_lds_dwordx4 v224, s[48:49] offset:-4096\n" \
"v_mfma_f32_16x16x32_bf16 a[20:23], v[64:67], v[168:171], a[20:23]\n" \
"ds_read_b64_tr_b16 v[130:131], v50 offset:4096\n" \
"v_mfma_f32_16x16x32_bf16 a[20:23], v[68:71], v[172:175], a[20:23]\n" \
"v_mfma_f32_16x16x32_bf16 a[24:27], v[64:67], v[176:179], a[24:27]\n" \
"ds_read_b64_tr_b16 v[132:133], v50 offset:16384\n" \
"v_mfma_f32_16x16x32_bf16 a[24:27], v[68:71], v[180:183], a[24:27]\n" \
"global_load_lds_dwordx4 v225, s[48:49] offset:-3072\n" \
"v_mfma_f32_16x16x32_bf16 a[28:31], v[64:67], v[184:187], a[28:31]\n" \
"ds_read_b64_tr_b16 v[134:135], v50 offset:20480\n" \
"v_mfma_f32_16x16x32_bf16 a[28:31], v[68:71], v[188:191], a[28:31]\n" \
"v_mfma_f32_16x16x32_bf16 a[48:51], v[72:75], v[160:163], a[48:51]\n" \
"ds_read_b128 v[64:67], v48 offset:0\n" \
"v_mfma_f32_16x16x32_bf16 a[48:51], v[76:79], v[164:167], a[48:51]\n" \
"global_load_lds_dwordx4 v226, s[48:49] offset:-2048\n" \
"v_mfma_f32_16x16x32_bf16 a[52:55], v[72:75], v[168:171], a[52:55]\n" \
"ds_read_b64_tr_b16 v[136:137], v51 offset:0\n" \
"v_mfma_f32_16x16x32_bf16 a[52:55], v[76:79], v[172:175], a[52:55]\n" \
"v_mfma_f32_16x16x32_bf16 a[56:59], v[72:75], v[176:179], a[56:59]\n" \
"ds_read_b128 v[68:71], v49 offset:0\n" \
"v_mfma_f32_16x16x32_bf16 a[56:59], v[76:79], v[180:183], a[56:59]\n" \
"global_load_lds_dwordx4 v227, s[48:49] offset:-1024\n" \
"v_mfma_f32_16x16x32_bf16 a[60:63], v[72:75], v[184:187], a[60:63]\n" \
"s_add_u32 s48, s48, 0x180\n" \
"s_addc_u32 s49, s49, 0\n" \
"ds_read_b64_tr_b16 v[138:139], v51 offset:4096\n" \
"v_mfma_f32_16x16x32_bf16 a[60:63], v[76:79], v[188:191], a[60:63]\n" \
"v_mfma_f32_16x16x32_bf16 a[80:83], v[80:83], v[160:163], a[80:83]\n" \
"ds_read_b128 v[72:75], v48 offset:2048\n" \
"v_mfma_f32_16x16x32_bf16 a[80:83], v[84:87], v[164:167], a[80:83]\n" \
"global_load_lds_dwordx4 v224, s[46:47] offset:0\n" \
"v_mfma_f32_16x16x32_bf16 a[84:87], v[80:83], v[168:171], a[84:87]\n" \
"ds_read_b64_tr_b16 v[140:141], v51 offset:16384\n" \
"v_mfma_f32_16x16x32_bf16 a[84:87], v[84:87], v[172:175], a[84:87]\n" \
"v_mfma_f32_16x16x32_bf16 a[88:91], v[80:83], v[176:179], a[88:91]\n" \
"ds_read_b128 v[76:79], v49 offset:2048\n" \
"v_mfma_f32_16x16x32_bf16 a[88:91], v[84:87], v[180:183], a[88:91]\n" \
"global_load_lds_dwordx4 v225, s[46:47] offset:1024\n" \
"v_mfma_f32_16x16x32_bf16 a[92:95], v[80:83], v[184:187], a[92:95]\n" \
"ds_read_b64_tr_b16 v[142:143], v51 offset:20480\n" \
"v_mfma_f32_16x16x32_bf16 a[92:95], v[84:87], v[188:191], a[92:95]\n" \
"v_mfma_f32_16x16x32_bf16 a[112:115], v[88:91], v[160:163], a[112:115]\n" \
"ds_read_b128 v[80:83], v48 offset:4096\n" \
"v_mfma_f32_16x16x32_bf16 a[112:115], v[92:95], v[164:167], a[112:115]\n" \
"global_load_lds_dwordx4 v226, s[46:47] offset:2048\n" \
"v_mfma_f32_16x16x32_bf16 a[116:119], v[88:91], v[168:171], a[116:119]\n" \
"ds_read_b64_tr_b16 v[144:145], v50 offset:512\n" \
"v_mfma_f32_16x16x32_bf16 a[116:119], v[92:95], v[172:175], a[116:119]\n" \
"v_mfma_f32_16x16x32_bf16 a[120:123], v[88:91], v[176:179], a[120:123]\n" \
"ds_read_b128 v[84:87], v49 offset:4096\n" \
"v_mfma_f32_16x16x32_bf16 a[120:123], v[92:95], v[180:183], a[120:123]\n" \
"global_load_lds_dwordx4 v227, s[46:47] offset:3072\n" \
"v_mfma_f32_16x16x32_bf16 a[124:127], v[88:91], v[184:187], a[124:127]\n" \
"s_add_u32 s46, s46, 0x180\n" \
"s_addc_u32 s47, s47, 0\n" \
"ds_read_b64_tr_b16 v[146:147], v50 offset:4608\n" \
"v_mfma_f32_16x16x32_bf16 a[124:127], v[92:95], v[188:191], a[124:127]\n" \
"s_add_u32 s68, s67, 0x700000\n" \
"s_add_u32 s64, s12, s68\n" \
"s_addc_u32 s65, s13, 0\n" \
"v_mfma_f32_16x16x32_bf16 a[144:147], v[96:99], v[160:163], a[144:147]\n" \
"ds_read_b128 v[88:91], v48 offset:6144\n" \
"v_mfma_f32_16x16x32_bf16 a[144:147], v[100:103], v[164:167], a[144:147]\n" \
"global_load_dword v253, v248, s[64:65]\n" \
"v_mfma_f32_16x16x32_bf16 a[148:151], v[96:99], v[168:171], a[148:151]\n" \
"s_lshr_b32 s68, s66, 2\n" \
"s_and_b32 s68, s68, 0xffff\n" \
"ds_read_b64_tr_b16 v[148:149], v50 offset:16896\n" \
"v_mfma_f32_16x16x32_bf16 a[148:151], v[100:103], v[172:175], a[148:151]\n" \
"s_add_u32 s68, s68, 65585152\n" \
"v_mfma_f32_16x16x32_bf16 a[152:155], v[96:99], v[176:179], a[152:155]\n" \
"ds_read_b128 v[92:95], v49 offset:6144\n" \
"v_mfma_f32_16x16x32_bf16 a[152:155], v[100:103], v[180:183], a[152:155]\n" \
"buffer_load_dword v253, v245, s[56:59], s68 offen sc1\n" \
"v_mfma_f32_16x16x32_bf16 a[156:159], v[96:99], v[184:187], a[156:159]\n" \
"ds_read_b64_tr_b16 v[150:151], v50 offset:20992\n" \
"v_mfma_f32_16x16x32_bf16 a[156:159], v[100:103], v[188:191], a[156:159]\n" \
"v_mfma_f32_16x16x32_bf16 a[176:179], v[104:107], v[160:163], a[176:179]\n" \
"ds_read_b128 v[96:99], v48 offset:8192\n" \
"v_mfma_f32_16x16x32_bf16 a[176:179], v[108:111], v[164:167], a[176:179]\n" \
"v_mfma_f32_16x16x32_bf16 a[180:183], v[104:107], v[168:171], a[180:183]\n" \
"ds_read_b64_tr_b16 v[152:153], v51 offset:512\n" \
"v_mfma_f32_16x16x32_bf16 a[180:183], v[108:111], v[172:175], a[180:183]\n" \
"v_mfma_f32_16x16x32_bf16 a[184:187], v[104:107], v[176:179], a[184:187]\n" \
"ds_read_b128 v[100:103], v49 offset:8192\n" \
"v_mfma_f32_16x16x32_bf16 a[184:187], v[108:111], v[180:183], a[184:187]\n" \
"v_mfma_f32_16x16x32_bf16 a[188:191], v[104:107], v[184:187], a[188:191]\n" \
"ds_read_b64_tr_b16 v[154:155], v51 offset:4608\n" \
"v_mfma_f32_16x16x32_bf16 a[188:191], v[108:111], v[188:191], a[188:191]\n" \
"v_mfma_f32_16x16x32_bf16 a[208:211], v[112:115], v[160:163], a[208:211]\n" \
"ds_read_b128 v[104:107], v48 offset:10240\n" \
"v_mfma_f32_16x16x32_bf16 a[208:211], v[116:119], v[164:167], a[208:211]\n" \
"v_mfma_f32_16x16x32_bf16 a[212:215], v[112:115], v[168:171], a[212:215]\n" \
"ds_read_b64_tr_b16 v[156:157], v51 offset:16896\n" \
"v_mfma_f32_16x16x32_bf16 a[212:215], v[116:119], v[172:175], a[212:215]\n" \
"v_mfma_f32_16x16x32_bf16 a[216:219], v[112:115], v[176:179], a[216:219]\n" \
"ds_read_b128 v[108:111], v49 offset:10240\n" \
"v_mfma_f32_16x16x32_bf16 a[216:219], v[116:119], v[180:183], a[216:219]\n" \
"v_mfma_f32_16x16x32_bf16 a[220:223], v[112:115], v[184:187], a[220:223]\n" \
"ds_read_b64_tr_b16 v[158:159], v51 offset:20992\n" \
"v_mfma_f32_16x16x32_bf16 a[220:223], v[116:119], v[188:191], a[220:223]\n" \
"v_mfma_f32_16x16x32_bf16 a[240:243], v[120:123], v[160:163], a[240:243]\n" \
"ds_read_b128 v[112:115], v48 offset:12288\n" \
"v_mfma_f32_16x16x32_bf16 a[240:243], v[124:127], v[164:167], a[240:243]\n" \
"v_mfma_f32_16x16x32_bf16 a[244:247], v[120:123], v[168:171], a[244:247]\n" \
"v_mfma_f32_16x16x32_bf16 a[244:247], v[124:127], v[172:175], a[244:247]\n" \
"v_mfma_f32_16x16x32_bf16 a[248:251], v[120:123], v[176:179], a[248:251]\n" \
"ds_read_b128 v[116:119], v49 offset:12288\n" \
"v_mfma_f32_16x16x32_bf16 a[248:251], v[124:127], v[180:183], a[248:251]\n" \
"v_mfma_f32_16x16x32_bf16 a[252:255], v[120:123], v[184:187], a[252:255]\n" \
"v_mfma_f32_16x16x32_bf16 a[252:255], v[124:127], v[188:191], a[252:255]\n" \
"s_waitcnt lgkmcnt(1)\n" \
"v_mfma_f32_16x16x32_bf16 a[0:3], v[128:131], v[64:67], a[0:3]\n" \
"ds_read_b128 v[120:123], v48 offset:14336\n" \
"v_mfma_f32_16x16x32_bf16 a[0:3], v[132:135], v[68:71], a[0:3]\n" \
"v_mfma_f32_16x16x32_bf16 a[4:7], v[136:139], v[64:67], a[4:7]\n" \
"ds_read_b128 v[124:127], v49 offset:14336\n" \
"s_add_u32 m0, s87, 69632\n" \
"v_mfma_f32_16x16x32_bf16 a[4:7], v[140:143], v[68:71], a[4:7]\n" \
"global_load_lds_dwordx4 v224, s[48:49] offset:-4096\n" \
"v_mfma_f32_16x16x32_bf16 a[8:11], v[144:147], v[64:67], a[8:11]\n" \
"ds_read_b64_tr_b16 v[160:161], v50 offset:1024\n" \
"v_mfma_f32_16x16x32_bf16 a[8:11], v[148:151], v[68:71], a[8:11]\n" \
"v_mfma_f32_16x16x32_bf16 a[12:15], v[152:155], v[64:67], a[12:15]\n" \
"ds_read_b64_tr_b16 v[162:163], v50 offset:5120\n" \
"v_mfma_f32_16x16x32_bf16 a[12:15], v[156:159], v[68:71], a[12:15]\n" \
"global_load_lds_dwordx4 v225, s[48:49] offset:-3072\n" \
"v_mfma_f32_16x16x32_bf16 a[32:35], v[128:131], v[72:75], a[32:35]\n" \
"ds_read_b64_tr_b16 v[164:165], v50 offset:17408\n" \
"v_mfma_f32_16x16x32_bf16 a[32:35], v[132:135], v[76:79], a[32:35]\n" \
"v_mfma_f32_16x16x32_bf16 a[36:39], v[136:139], v[72:75], a[36:39]\n" \
"ds_read_b64_tr_b16 v[166:167], v50 offset:21504\n" \
"v_mfma_f32_16x16x32_bf16 a[36:39], v[140:143], v[76:79], a[36:39]\n" \
"global_load_lds_dwordx4 v226, s[48:49] offset:-2048\n" \
"v_mfma_f32_16x16x32_bf16 a[40:43], v[144:147], v[72:75], a[40:43]\n" \
"ds_read_b64_tr_b16 v[168:169], v51 offset:1024\n" \
"v_mfma_f32_16x16x32_bf16 a[40:43], v[148:151], v[76:79], a[40:43]\n" \
"v_mfma_f32_16x16x32_bf16 a[44:47], v[152:155], v[72:75], a[44:47]\n" \
"ds_read_b64_tr_b16 v[170:171], v51 offset:5120\n" \
"v_mfma_f32_16x16x32_bf16 a[44:47], v[156:159], v[76:79], a[44:47]\n" \
"global_load_lds_dwordx4 v227, s[48:49] offset:-1024\n" \
"v_mfma_f32_16x16x32_bf16 a[64:67], v[128:131], v[80:83], a[64:67]\n" \
"s_add_u32 s48, s48, 0x400\n" \
"s_addc_u32 s49, s49, 0\n" \
"ds_read_b64_tr_b16 v[172:173], v51 offset:17408\n" \
"v_mfma_f32_16x16x32_bf16 a[64:67], v[132:135], v[84:87], a[64:67]\n" \
"v_mfma_f32_16x16x32_bf16 a[68:71], v[136:139], v[80:83], a[68:71]\n" \
"ds_read_b64_tr_b16 v[174:175], v51 offset:21504\n" \
"v_mfma_f32_16x16x32_bf16 a[68:71], v[140:143], v[84:87], a[68:71]\n" \
"global_load_lds_dwordx4 v224, s[46:47] offset:0\n" \
"v_mfma_f32_16x16x32_bf16 a[72:75], v[144:147], v[80:83], a[72:75]\n" \
"ds_read_b64_tr_b16 v[176:177], v50 offset:1536\n" \
"v_mfma_f32_16x16x32_bf16 a[72:75], v[148:151], v[84:87], a[72:75]\n" \
"v_mfma_f32_16x16x32_bf16 a[76:79], v[152:155], v[80:83], a[76:79]\n" \
"ds_read_b64_tr_b16 v[178:179], v50 offset:5632\n" \
"v_mfma_f32_16x16x32_bf16 a[76:79], v[156:159], v[84:87], a[76:79]\n" \
"global_load_lds_dwordx4 v225, s[46:47] offset:1024\n" \
"v_mfma_f32_16x16x32_bf16 a[96:99], v[128:131], v[88:91], a[96:99]\n" \
"ds_read_b64_tr_b16 v[180:181], v50 offset:17920\n" \
"v_mfma_f32_16x16x32_bf16 a[96:99], v[132:135], v[92:95], a[96:99]\n" \
"v_mfma_f32_16x16x32_bf16 a[100:103], v[136:139], v[88:91], a[100:103]\n" \
"ds_read_b64_tr_b16 v[182:183], v50 offset:22016\n" \
"v_mfma_f32_16x16x32_bf16 a[100:103], v[140:143], v[92:95], a[100:103]\n" \
"global_load_lds_dwordx4 v226, s[46:47] offset:2048\n" \
"v_mfma_f32_16x16x32_bf16 a[104:107], v[144:147], v[88:91], a[104:107]\n" \
"ds_read_b64_tr_b16 v[184:185], v51 offset:1536\n" \
"v_mfma_f32_16x16x32_bf16 a[104:107], v[148:151], v[92:95], a[104:107]\n" \
"v_mfma_f32_16x16x32_bf16 a[108:111], v[152:155], v[88:91], a[108:111]\n" \
"ds_read_b64_tr_b16 v[186:187], v51 offset:5632\n" \
"v_mfma_f32_16x16x32_bf16 a[108:111], v[156:159], v[92:95], a[108:111]\n" \
"global_load_lds_dwordx4 v227, s[46:47] offset:3072\n" \
"v_mfma_f32_16x16x32_bf16 a[128:131], v[128:131], v[96:99], a[128:131]\n" \
"s_add_u32 s46, s46, 0x400\n" \
"s_addc_u32 s47, s47, 0\n" \
"ds_read_b64_tr_b16 v[188:189], v51 offset:17920\n" \
"v_mfma_f32_16x16x32_bf16 a[128:131], v[132:135], v[100:103], a[128:131]\n" \
"v_mfma_f32_16x16x32_bf16 a[132:135], v[136:139], v[96:99], a[132:135]\n" \
"ds_read_b64_tr_b16 v[190:191], v51 offset:22016\n" \
"v_mfma_f32_16x16x32_bf16 a[132:135], v[140:143], v[100:103], a[132:135]\n" \
"v_mfma_f32_16x16x32_bf16 a[136:139], v[144:147], v[96:99], a[136:139]\n" \
"v_mfma_f32_16x16x32_bf16 a[136:139], v[148:151], v[100:103], a[136:139]\n" \
"v_mfma_f32_16x16x32_bf16 a[140:143], v[152:155], v[96:99], a[140:143]\n" \
"v_mfma_f32_16x16x32_bf16 a[140:143], v[156:159], v[100:103], a[140:143]\n" \
"v_mfma_f32_16x16x32_bf16 a[160:163], v[128:131], v[104:107], a[160:163]\n" \
"v_mfma_f32_16x16x32_bf16 a[160:163], v[132:135], v[108:111], a[160:163]\n" \
"v_mfma_f32_16x16x32_bf16 a[164:167], v[136:139], v[104:107], a[164:167]\n" \
"v_mfma_f32_16x16x32_bf16 a[164:167], v[140:143], v[108:111], a[164:167]\n" \
"v_mfma_f32_16x16x32_bf16 a[168:171], v[144:147], v[104:107], a[168:171]\n" \
"v_mfma_f32_16x16x32_bf16 a[168:171], v[148:151], v[108:111], a[168:171]\n" \
"v_mfma_f32_16x16x32_bf16 a[172:175], v[152:155], v[104:107], a[172:175]\n" \
"v_mfma_f32_16x16x32_bf16 a[172:175], v[156:159], v[108:111], a[172:175]\n" \
"s_waitcnt lgkmcnt(0)\n" \
"v_mfma_f32_16x16x32_bf16 a[192:195], v[128:131], v[112:115], a[192:195]\n" \
"v_mfma_f32_16x16x32_bf16 a[192:195], v[132:135], v[116:119], a[192:195]\n" \
"v_mfma_f32_16x16x32_bf16 a[196:199], v[136:139], v[112:115], a[196:199]\n" \
"v_mfma_f32_16x16x32_bf16 a[196:199], v[140:143], v[116:119], a[196:199]\n" \
"v_mfma_f32_16x16x32_bf16 a[200:203], v[144:147], v[112:115], a[200:203]\n" \
"v_mfma_f32_16x16x32_bf16 a[200:203], v[148:151], v[116:119], a[200:203]\n" \
"v_mfma_f32_16x16x32_bf16 a[204:207], v[152:155], v[112:115], a[204:207]\n" \
"v_mfma_f32_16x16x32_bf16 a[204:207], v[156:159], v[116:119], a[204:207]\n" \
"v_mfma_f32_16x16x32_bf16 a[224:227], v[128:131], v[120:123], a[224:227]\n" \
"v_mfma_f32_16x16x32_bf16 a[224:227], v[132:135], v[124:127], a[224:227]\n" \
"v_mfma_f32_16x16x32_bf16 a[228:231], v[136:139], v[120:123], a[228:231]\n" \
"v_mfma_f32_16x16x32_bf16 a[228:231], v[140:143], v[124:127], a[228:231]\n" \
"v_mfma_f32_16x16x32_bf16 a[232:235], v[144:147], v[120:123], a[232:235]\n" \
"v_mfma_f32_16x16x32_bf16 a[232:235], v[148:151], v[124:127], a[232:235]\n" \
"v_mfma_f32_16x16x32_bf16 a[236:239], v[152:155], v[120:123], a[236:239]\n" \
"s_waitcnt vmcnt(10) lgkmcnt(0)\n" \
"v_mfma_f32_16x16x32_bf16 a[236:239], v[156:159], v[124:127], a[236:239]\n" \
"s_barrier\n" \
"v_mfma_f32_16x16x32_bf16 a[16:19], v[64:67], v[160:163], a[16:19]\n" \
"s_add_u32 m0, s87, 102400\n" \
"v_mfma_f32_16x16x32_bf16 a[16:19], v[68:71], v[164:167], a[16:19]\n" \
"global_load_lds_dwordx4 v224, s[48:49] offset:-4096\n" \
"v_mfma_f32_16x16x32_bf16 a[20:23], v[64:67], v[168:171], a[20:23]\n" \
"ds_read_b64_tr_b16 v[0:1], v232 offset:0\n" \
"v_mfma_f32_16x16x32_bf16 a[20:23], v[68:71], v[172:175], a[20:23]\n" \
"v_accvgpr_read_b32 v234, a192\n" \
"v_accvgpr_read_b32 v235, a193\n" \
"v_mfma_f32_16x16x32_bf16 a[24:27], v[64:67], v[176:179], a[24:27]\n" \
"ds_read_b64_tr_b16 v[2:3], v232 offset:2048\n" \
"v_accvgpr_read_b32 v236, a194\n" \
"v_mfma_f32_16x16x32_bf16 a[24:27], v[68:71], v[180:183], a[24:27]\n" \
"global_load_lds_dwordx4 v225, s[48:49] offset:-3072\n" \
"v_accvgpr_read_b32 v237, a195\n" \
"v_mfma_f32_16x16x32_bf16 a[28:31], v[64:67], v[184:187], a[28:31]\n" \
"ds_read_b64_tr_b16 v[4:5], v232 offset:8192\n" \
"v_mul_f32 v234, s92, v234\n" \
"v_mfma_f32_16x16x32_bf16 a[28:31], v[68:71], v[188:191], a[28:31]\n" \
"v_mul_f32 v235, s92, v235\n" \
"v_mul_f32 v236, s92, v236\n" \
"v_mfma_f32_16x16x32_bf16 a[48:51], v[72:75], v[160:163], a[48:51]\n" \
"ds_read_b64_tr_b16 v[6:7], v232 offset:10240\n" \
"v_mul_f32 v237, s92, v237\n" \
"v_mfma_f32_16x16x32_bf16 a[48:51], v[76:79], v[164:167], a[48:51]\n" \
"global_load_lds_dwordx4 v226, s[48:49] offset:-2048\n" \
"v_cvt_pk_bf16_f32 v234, v234, v235\n" \
"v_mfma_f32_16x16x32_bf16 a[52:55], v[72:75], v[168:171], a[52:55]\n" \
"ds_read_b64_tr_b16 v[8:9], v233 offset:0\n" \
"v_cvt_pk_bf16_f32 v236, v236, v237\n" \
"v_mfma_f32_16x16x32_bf16 a[52:55], v[76:79], v[172:175], a[52:55]\n" \
"v_accvgpr_write_b32 a192, v234\n" \
"v_accvgpr_write_b32 a193, v236\n" \
"v_mfma_f32_16x16x32_bf16 a[56:59], v[72:75], v[176:179], a[56:59]\n" \
"ds_read_b64_tr_b16 v[10:11], v233 offset:2048\n" \
"v_accvgpr_read_b32 v234, a196\n" \
"v_mfma_f32_16x16x32_bf16 a[56:59], v[76:79], v[180:183], a[56:59]\n" \
"global_load_lds_dwordx4 v227, s[48:49] offset:-1024\n" \
"v_accvgpr_read_b32 v235, a197\n" \
"v_mfma_f32_16x16x32_bf16 a[60:63], v[72:75], v[184:187], a[60:63]\n" \
"s_add_u32 s48, s48, 0xfffffb00\n" \
"s_addc_u32 s49, s49, -1\n" \
"ds_read_b64_tr_b16 v[12:13], v233 offset:8192\n" \
"v_mfma_f32_16x16x32_bf16 a[60:63], v[76:79], v[188:191], a[60:63]\n" \
"v_accvgpr_read_b32 v236, a198\n" \
"v_accvgpr_read_b32 v237, a199\n" \
"v_mfma_f32_16x16x32_bf16 a[80:83], v[80:83], v[160:163], a[80:83]\n" \
"ds_read_b64_tr_b16 v[14:15], v233 offset:10240\n" \
"v_mul_f32 v234, s92, v234\n" \
"v_mfma_f32_16x16x32_bf16 a[80:83], v[84:87], v[164:167], a[80:83]\n" \
"global_load_lds_dwordx4 v224, s[46:47] offset:0\n" \
"v_mul_f32 v235, s92, v235\n" \
"v_mfma_f32_16x16x32_bf16 a[84:87], v[80:83], v[168:171], a[84:87]\n" \
"ds_read_b64_tr_b16 v[16:17], v232 offset:512\n" \
"v_mul_f32 v236, s92, v236\n" \
"v_mfma_f32_16x16x32_bf16 a[84:87], v[84:87], v[172:175], a[84:87]\n" \
"v_mul_f32 v237, s92, v237\n" \
"v_cvt_pk_bf16_f32 v234, v234, v235\n" \
"v_mfma_f32_16x16x32_bf16 a[88:91], v[80:83], v[176:179], a[88:91]\n" \
"ds_read_b64_tr_b16 v[18:19], v232 offset:2560\n" \
"v_cvt_pk_bf16_f32 v236, v236, v237\n" \
"v_mfma_f32_16x16x32_bf16 a[88:91], v[84:87], v[180:183], a[88:91]\n" \
"global_load_lds_dwordx4 v225, s[46:47] offset:1024\n" \
"v_accvgpr_write_b32 a194, v234\n" \
"v_mfma_f32_16x16x32_bf16 a[92:95], v[80:83], v[184:187], a[92:95]\n" \
"ds_read_b64_tr_b16 v[20:21], v232 offset:8704\n" \
"v_accvgpr_write_b32 a195, v236\n" \
"v_mfma_f32_16x16x32_bf16 a[92:95], v[84:87], v[188:191], a[92:95]\n" \
"v_accvgpr_read_b32 v234, a224\n" \
"v_accvgpr_read_b32 v235, a225\n" \
"v_mfma_f32_16x16x32_bf16 a[112:115], v[88:91], v[160:163], a[112:115]\n" \
"ds_read_b64_tr_b16 v[22:23], v232 offset:10752\n" \
"v_accvgpr_read_b32 v236, a226\n" \
"v_mfma_f32_16x16x32_bf16 a[112:115], v[92:95], v[164:167], a[112:115]\n" \
"global_load_lds_dwordx4 v226, s[46:47] offset:2048\n" \
"v_accvgpr_read_b32 v237, a227\n" \
"v_mfma_f32_16x16x32_bf16 a[116:119], v[88:91], v[168:171], a[116:119]\n" \
"ds_read_b64_tr_b16 v[24:25], v233 offset:512\n" \
"v_mul_f32 v234, s92, v234\n" \
"v_mfma_f32_16x16x32_bf16 a[116:119], v[92:95], v[172:175], a[116:119]\n" \
"v_mul_f32 v235, s92, v235\n" \
"v_mul_f32 v236, s92, v236\n" \
"v_mfma_f32_16x16x32_bf16 a[120:123], v[88:91], v[176:179], a[120:123]\n" \
"ds_read_b64_tr_b16 v[26:27], v233 offset:2560\n" \
"v_mul_f32 v237, s92, v237\n" \
"v_mfma_f32_16x16x32_bf16 a[120:123], v[92:95], v[180:183], a[120:123]\n" \
"global_load_lds_dwordx4 v227, s[46:47] offset:3072\n" \
"v_cvt_pk_bf16_f32 v234, v234, v235\n" \
"v_mfma_f32_16x16x32_bf16 a[124:127], v[88:91], v[184:187], a[124:127]\n" \
"s_add_u32 s46, s46, 0xfffffb00\n" \
"s_addc_u32 s47, s47, -1\n" \
"ds_read_b64_tr_b16 v[28:29], v233 offset:8704\n" \
"v_mfma_f32_16x16x32_bf16 a[124:127], v[92:95], v[188:191], a[124:127]\n" \
"v_cvt_pk_bf16_f32 v236, v236, v237\n" \
"v_accvgpr_write_b32 a224, v234\n" \
"v_mfma_f32_16x16x32_bf16 a[144:147], v[96:99], v[160:163], a[144:147]\n" \
"s_add_u32 m0, s95, 36864\n" \
"ds_read_b64_tr_b16 v[30:31], v233 offset:10752\n" \
"v_accvgpr_write_b32 a225, v236\n" \
"v_mfma_f32_16x16x32_bf16 a[144:147], v[100:103], v[164:167], a[144:147]\n" \
"global_load_lds_dwordx4 v228, s[44:45] offset:-4096\n" \
"v_accvgpr_read_b32 v234, a228\n" \
"v_mfma_f32_16x16x32_bf16 a[148:151], v[96:99], v[168:171], a[148:151]\n" \
"ds_read_b128 v[192:195], v230 offset:0\n" \
"v_accvgpr_read_b32 v235, a229\n" \
"v_mfma_f32_16x16x32_bf16 a[148:151], v[100:103], v[172:175], a[148:151]\n" \
"v_accvgpr_read_b32 v236, a230\n" \
"v_accvgpr_read_b32 v237, a231\n" \
"v_mfma_f32_16x16x32_bf16 a[152:155], v[96:99], v[176:179], a[152:155]\n" \
"ds_read_b128 v[196:199], v231 offset:0\n" \
"v_mul_f32 v234, s92, v234\n" \
"v_mfma_f32_16x16x32_bf16 a[152:155], v[100:103], v[180:183], a[152:155]\n" \
"global_load_lds_dwordx4 v229, s[44:45] offset:-3072\n" \
"v_mul_f32 v235, s92, v235\n" \
"v_mfma_f32_16x16x32_bf16 a[156:159], v[96:99], v[184:187], a[156:159]\n" \
"s_add_u32 s44, s44, 0xffb00000\n" \
"s_addc_u32 s45, s45, -1\n" \
"ds_read_b128 v[200:203], v230 offset:2048\n" \
"v_mfma_f32_16x16x32_bf16 a[156:159], v[100:103], v[188:191], a[156:159]\n" \
"v_mul_f32 v236, s92, v236\n" \
"v_mul_f32 v237, s92, v237\n" \
"v_mfma_f32_16x16x32_bf16 a[176:179], v[104:107], v[160:163], a[176:179]\n" \
"ds_read_b128 v[204:207], v231 offset:2048\n" \
"v_cvt_pk_bf16_f32 v234, v234, v235\n" \
"v_mfma_f32_16x16x32_bf16 a[176:179], v[108:111], v[164:167], a[176:179]\n" \
"global_load_lds_dwordx4 v228, s[42:43] offset:-2048\n" \
"v_cvt_pk_bf16_f32 v236, v236, v237\n" \
"v_mfma_f32_16x16x32_bf16 a[180:183], v[104:107], v[168:171], a[180:183]\n" \
"ds_read_b128 v[208:211], v230 offset:4096\n" \
"v_accvgpr_write_b32 a226, v234\n" \
"v_mfma_f32_16x16x32_bf16 a[180:183], v[108:111], v[172:175], a[180:183]\n" \
"v_accvgpr_write_b32 a227, v236\n" \
"v_accvgpr_read_b32 v234, a200\n" \
"v_mfma_f32_16x16x32_bf16 a[184:187], v[104:107], v[176:179], a[184:187]\n" \
"ds_read_b128 v[212:215], v231 offset:4096\n" \
"v_accvgpr_read_b32 v235, a201\n" \
"v_mfma_f32_16x16x32_bf16 a[184:187], v[108:111], v[180:183], a[184:187]\n" \
"global_load_lds_dwordx4 v229, s[42:43] offset:-1024\n" \
"v_accvgpr_read_b32 v236, a202\n" \
"v_mfma_f32_16x16x32_bf16 a[188:191], v[104:107], v[184:187], a[188:191]\n" \
"s_add_u32 s42, s42, 0xffb00000\n" \
"s_addc_u32 s43, s43, -1\n" \
"v_accvgpr_read_b32 v237, a203\n" \
"v_mfma_f32_16x16x32_bf16 a[188:191], v[108:111], v[188:191], a[188:191]\n" \
"v_mul_f32 v234, s92, v234\n" \
"v_mul_f32 v235, s92, v235\n" \
"v_mfma_f32_16x16x32_bf16 a[208:211], v[112:115], v[160:163], a[208:211]\n" \
"s_add_u32 m0, s95, 53248\n" \
"v_mul_f32 v236, s92, v236\n" \
"v_mul_f32 v237, s92, v237\n" \
"v_mfma_f32_16x16x32_bf16 a[208:211], v[116:119], v[164:167], a[208:211]\n" \
"global_load_lds_dwordx4 v228, s[44:45] offset:-4096\n" \
"v_cvt_pk_bf16_f32 v234, v234, v235\n" \
"v_mfma_f32_16x16x32_bf16 a[212:215], v[112:115], v[168:171], a[212:215]\n" \
"v_cvt_pk_bf16_f32 v236, v236, v237\n" \
"v_accvgpr_write_b32 a200, v234\n" \
"v_mfma_f32_16x16x32_bf16 a[212:215], v[116:119], v[172:175], a[212:215]\n" \
"v_accvgpr_write_b32 a201, v236\n" \
"v_accvgpr_read_b32 v234, a204\n" \
"v_mfma_f32_16x16x32_bf16 a[216:219], v[112:115], v[176:179], a[216:219]\n" \
"v_accvgpr_read_b32 v235, a205\n" \
"v_accvgpr_read_b32 v236, a206\n" \
"v_mfma_f32_16x16x32_bf16 a[216:219], v[116:119], v[180:183], a[216:219]\n" \
"global_load_lds_dwordx4 v229, s[44:45] offset:-3072\n" \
"v_accvgpr_read_b32 v237, a207\n" \
"v_mfma_f32_16x16x32_bf16 a[220:223], v[112:115], v[184:187], a[220:223]\n" \
"s_add_u32 s44, s44, 0x80000\n" \
"s_addc_u32 s45, s45, 0\n" \
"v_mul_f32 v234, s92, v234\n" \
"v_mfma_f32_16x16x32_bf16 a[220:223], v[116:119], v[188:191], a[220:223]\n" \
"v_mul_f32 v235, s92, v235\n" \
"v_mul_f32 v236, s92, v236\n" \
"v_mfma_f32_16x16x32_bf16 a[240:243], v[120:123], v[160:163], a[240:243]\n" \
"v_mul_f32 v237, s92, v237\n" \
"v_cvt_pk_bf16_f32 v234, v234, v235\n" \
"v_mfma_f32_16x16x32_bf16 a[240:243], v[124:127], v[164:167], a[240:243]\n" \
"global_load_lds_dwordx4 v228, s[42:43] offset:-2048\n" \
"v_cvt_pk_bf16_f32 v236, v236, v237\n" \
"v_mfma_f32_16x16x32_bf16 a[244:247], v[120:123], v[168:171], a[244:247]\n" \
"v_accvgpr_write_b32 a202, v234\n" \
"v_accvgpr_write_b32 a203, v236\n" \
"v_mfma_f32_16x16x32_bf16 a[244:247], v[124:127], v[172:175], a[244:247]\n" \
"v_accvgpr_read_b32 v234, a232\n" \
"v_accvgpr_read_b32 v235, a233\n" \
"v_mfma_f32_16x16x32_bf16 a[248:251], v[120:123], v[176:179], a[248:251]\n" \
"v_accvgpr_read_b32 v236, a234\n" \
"v_accvgpr_read_b32 v237, a235\n" \
"v_mfma_f32_16x16x32_bf16 a[248:251], v[124:127], v[180:183], a[248:251]\n" \
"global_load_lds_dwordx4 v229, s[42:43] offset:-1024\n" \
"v_mul_f32 v234, s92, v234\n" \
"v_mfma_f32_16x16x32_bf16 a[252:255], v[120:123], v[184:187], a[252:255]\n" \
"s_add_u32 s42, s42, 0x80000\n" \
"s_addc_u32 s43, s43, 0\n" \
"v_mul_f32 v235, s92, v235\n" \
"v_mfma_f32_16x16x32_bf16 a[252:255], v[124:127], v[188:191], a[252:255]\n" \
"s_waitcnt lgkmcnt(0)\n" \
"v_mul_f32 v236, s92, v236\n" \
"v_mul_f32 v237, s92, v237\n" \
"v_mfma_f32_16x16x32_bf16 v[64:67], v[0:3], v[192:195], 0\n" \
"ds_read_b128 v[216:219], v230 offset:6144\n" \
"v_cvt_pk_bf16_f32 v234, v234, v235\n" \
"v_mfma_f32_16x16x32_bf16 v[64:67], v[4:7], v[196:199], v[64:67]\n" \
"ds_read_b128 v[220:223], v231 offset:6144\n" \
"v_cvt_pk_bf16_f32 v236, v236, v237\n" \
"v_mfma_f32_16x16x32_bf16 v[68:71], v[8:11], v[192:195], 0\n" \
"s_add_u32 s90, s88, 0\n" \
"ds_read_b64_tr_b16 v[32:33], v232 offset:1024\n" \
"v_accvgpr_write_b32 a232, v234\n" \
"v_mfma_f32_16x16x32_bf16 v[68:71], v[12:15], v[196:199], v[68:71]\n" \
"buffer_store_dwordx4 a[192:195], v249, s[56:59], s90 offen offset:0 sc1\n" \
"v_accvgpr_write_b32 a233, v236\n" \
"v_accvgpr_read_b32 v234, a236\n" \
"v_accvgpr_read_b32 v235, a237\n" \
"v_accvgpr_read_b32 v236, a238\n" \
"v_mfma_f32_16x16x32_bf16 v[72:75], v[16:19], v[192:195], 0\n" \
"ds_read_b64_tr_b16 v[34:35], v232 offset:3072\n" \
"v_accvgpr_read_b32 v237, a239\n" \
"v_mfma_f32_16x16x32_bf16 v[72:75], v[20:23], v[196:199], v[72:75]\n" \
"ds_read_b64_tr_b16 v[36:37], v232 offset:9216\n" \
"v_mul_f32 v234, s92, v234\n" \
"v_mfma_f32_16x16x32_bf16 v[76:79], v[24:27], v[192:195], 0\n" \
"ds_read_b64_tr_b16 v[38:39], v232 offset:11264\n" \
"v_mul_f32 v235, s92, v235\n" \
"v_mfma_f32_16x16x32_bf16 v[76:79], v[28:31], v[196:199], v[76:79]\n" \
"buffer_store_dwordx4 a[224:227], v249, s[56:59], s90 offen offset:256 sc1\n" \
"v_mul_f32 v236, s92, v236\n" \
"v_mul_f32 v237, s92, v237\n" \
"v_cvt_pk_bf16_f32 v234, v234, v235\n" \
"v_cvt_pk_bf16_f32 v236, v236, v237\n" \
"v_mfma_f32_16x16x32_bf16 v[96:99], v[0:3], v[200:203], 0\n" \
"ds_read_b64_tr_b16 v[40:41], v233 offset:1024\n" \
"v_accvgpr_write_b32 a234, v234\n" \
"v_mfma_f32_16x16x32_bf16 v[96:99], v[4:7], v[204:207], v[96:99]\n" \
"ds_read_b64_tr_b16 v[42:43], v233 offset:3072\n" \
"v_accvgpr_write_b32 a235, v236\n" \
"v_mfma_f32_16x16x32_bf16 v[100:103], v[8:11], v[200:203], 0\n" \
"ds_read_b64_tr_b16 v[44:45], v233 offset:9216\n" \
"v_accvgpr_read_b32 v234, a208\n" \
"v_mfma_f32_16x16x32_bf16 v[100:103], v[12:15], v[204:207], v[100:103]\n" \
"v_accvgpr_read_b32 v235, a209\n" \
"v_accvgpr_read_b32 v236, a210\n" \
"v_mfma_f32_16x16x32_bf16 v[104:107], v[16:19], v[200:203], 0\n" \
"ds_read_b64_tr_b16 v[46:47], v233 offset:11264\n" \
"v_accvgpr_read_b32 v237, a211\n" \
"v_mfma_f32_16x16x32_bf16 v[104:107], v[20:23], v[204:207], v[104:107]\n" \
"ds_read_b64_tr_b16 v[48:49], v232 offset:1536\n" \
"v_cvt_pk_bf16_f32 v234, v234, v235\n" \
"v_mfma_f32_16x16x32_bf16 v[108:111], v[24:27], v[200:203], 0\n" \
"ds_read_b64_tr_b16 v[50:51], v232 offset:3584\n" \
"v_cvt_pk_bf16_f32 v236, v236, v237\n" \
"v_mfma_f32_16x16x32_bf16 v[108:111], v[28:31], v[204:207], v[108:111]\n" \
"v_accvgpr_write_b32 a208, v234\n" \
"v_accvgpr_write_b32 a209, v236\n" \
"v_mfma_f32_16x16x32_bf16 v[128:131], v[0:3], v[208:211], 0\n" \
"ds_read_b64_tr_b16 v[52:53], v232 offset:9728\n" \
"v_accvgpr_read_b32 v234, a240\n" \
"v_mfma_f32_16x16x32_bf16 v[128:131], v[4:7], v[212:215], v[128:131]\n" \
"ds_read_b64_tr_b16 v[54:55], v232 offset:11776\n" \
"v_accvgpr_read_b32 v235, a241\n" \
"v_mfma_f32_16x16x32_bf16 v[132:135], v[8:11], v[208:211], 0\n" \
"ds_read_b64_tr_b16 v[56:57], v233 offset:1536\n" \
"v_accvgpr_read_b32 v236, a242\n" \
"v_mfma_f32_16x16x32_bf16 v[132:135], v[12:15], v[212:215], v[132:135]\n" \
"v_accvgpr_read_b32 v237, a243\n" \
"v_cvt_pk_bf16_f32 v234, v234, v235\n" \
"v_mfma_f32_16x16x32_bf16 v[136:139], v[16:19], v[208:211], 0\n" \
"ds_read_b64_tr_b16 v[58:59], v233 offset:3584\n" \
"v_cvt_pk_bf16_f32 v236, v236, v237\n" \
"v_mfma_f32_16x16x32_bf16 v[136:139], v[20:23], v[212:215], v[136:139]\n" \
"ds_read_b64_tr_b16 v[60:61], v233 offset:9728\n" \
"v_accvgpr_write_b32 a210, v234\n" \
"v_mfma_f32_16x16x32_bf16 v[140:143], v[24:27], v[208:211], 0\n" \
"ds_read_b64_tr_b16 v[62:63], v233 offset:11776\n" \
"v_accvgpr_write_b32 a211, v236\n" \
"Lpg2185_%=:\n" \
"Lst0x3_%=:\n" \
"s_cbranch_execz Lpg4357_%=\n" \
"v_mfma_f32_16x16x32_bf16 v[140:143], v[28:31], v[212:215], v[140:143]\n" \
"s_waitcnt lgkmcnt(3)\n" \
"v_accvgpr_read_b32 v234, a212\n" \
"v_accvgpr_read_b32 v235, a213\n" \
"v_mfma_f32_16x16x32_bf16 v[160:163], v[0:3], v[216:219], 0\n" \
"v_add_u32 v230, 0xffff0000, v230\n" \
"v_add_u32 v231, 0xffff0000, v231\n" \
"v_mfma_f32_16x16x32_bf16 v[160:163], v[4:7], v[220:223], v[160:163]\n" \
"v_add_u32 v232, 0x4000, v232\n" \
"v_add_u32 v233, 0x4000, v233\n" \
"v_mfma_f32_16x16x32_bf16 v[164:167], v[8:11], v[216:219], 0\n" \
"v_accvgpr_read_b32 v236, a214\n" \
"v_accvgpr_read_b32 v237, a215\n" \
"v_mfma_f32_16x16x32_bf16 v[164:167], v[12:15], v[220:223], v[164:167]\n" \
"v_cvt_pk_bf16_f32 v234, v234, v235\n" \
"v_cvt_pk_bf16_f32 v236, v236, v237\n" \
"v_mfma_f32_16x16x32_bf16 v[168:171], v[16:19], v[216:219], 0\n" \
"v_accvgpr_write_b32 a212, v234\n" \
"v_accvgpr_write_b32 a213, v236\n" \
"v_mfma_f32_16x16x32_bf16 v[168:171], v[20:23], v[220:223], v[168:171]\n" \
"v_accvgpr_read_b32 v234, a244\n" \
"v_accvgpr_read_b32 v235, a245\n" \
"v_mfma_f32_16x16x32_bf16 v[172:175], v[24:27], v[216:219], 0\n" \
"s_waitcnt vmcnt(18) lgkmcnt(0)\n" \
"v_accvgpr_read_b32 v236, a246\n" \
"v_accvgpr_read_b32 v237, a247\n" \
"v_mfma_f32_16x16x32_bf16 v[172:175], v[28:31], v[220:223], v[172:175]\n" \
"s_barrier\n" \
"v_mfma_f32_16x16x32_bf16 v[80:83], v[32:35], v[192:195], 0\n" \
"ds_read_b64_tr_b16 v[0:1], v232 offset:0\n" \
"v_cvt_pk_bf16_f32 v234, v234, v235\n" \
"v_mfma_f32_16x16x32_bf16 v[80:83], v[36:39], v[196:199], v[80:83]\n" \
"ds_read_b64_tr_b16 v[2:3], v232 offset:2048\n" \
"v_cvt_pk_bf16_f32 v236, v236, v237\n" \
"v_mfma_f32_16x16x32_bf16 v[84:87], v[40:43], v[192:195], 0\n" \
"s_add_u32 m0, s87, 135168\n" \
"ds_read_b64_tr_b16 v[4:5], v232 offset:8192\n" \
"v_accvgpr_write_b32 a214, v234\n" \
"v_mfma_f32_16x16x32_bf16 v[84:87], v[44:47], v[196:199], v[84:87]\n" \
"global_load_lds_dwordx4 v224, s[48:49] offset:-4096\n" \
"v_accvgpr_write_b32 a215, v236\n" \
"v_mfma_f32_16x16x32_bf16 v[88:91], v[48:51], v[192:195], 0\n" \
"ds_read_b64_tr_b16 v[6:7], v232 offset:10240\n" \
"v_accvgpr_read_b32 v234, a216\n" \
"v_mfma_f32_16x16x32_bf16 v[88:91], v[52:55], v[196:199], v[88:91]\n" \
"ds_read_b64_tr_b16 v[8:9], v233 offset:0\n" \
"v_accvgpr_read_b32 v235, a217\n" \
"v_mfma_f32_16x16x32_bf16 v[92:95], v[56:59], v[192:195], 0\n" \
"ds_read_b64_tr_b16 v[10:11], v233 offset:2048\n" \
"v_accvgpr_read_b32 v236, a218\n" \
"v_mfma_f32_16x16x32_bf16 v[92:95], v[60:63], v[196:199], v[92:95]\n" \
"global_load_lds_dwordx4 v225, s[48:49] offset:-3072\n" \
"v_accvgpr_read_b32 v237, a219\n" \
"v_mfma_f32_16x16x32_bf16 v[112:115], v[32:35], v[200:203], 0\n" \
"ds_read_b128 v[192:195], v230 offset:0\n" \
"v_cvt_pk_bf16_f32 v234, v234, v235\n" \
"v_mfma_f32_16x16x32_bf16 v[112:115], v[36:39], v[204:207], v[112:115]\n" \
"ds_read_b128 v[196:199], v231 offset:0\n" \
"v_cvt_pk_bf16_f32 v236, v236, v237\n" \
"v_mfma_f32_16x16x32_bf16 v[116:119], v[40:43], v[200:203], 0\n" \
"ds_read_b64_tr_b16 v[12:13], v233 offset:8192\n" \
"v_accvgpr_write_b32 a216, v234\n" \
"v_mfma_f32_16x16x32_bf16 v[116:119], v[44:47], v[204:207], v[116:119]\n" \
"global_load_lds_dwordx4 v226, s[48:49] offset:-2048\n" \
"v_accvgpr_write_b32 a217, v236\n" \
"v_mfma_f32_16x16x32_bf16 v[120:123], v[48:51], v[200:203], 0\n" \
"ds_read_b64_tr_b16 v[14:15], v233 offset:10240\n" \
"v_accvgpr_read_b32 v234, a248\n" \
"v_mfma_f32_16x16x32_bf16 v[120:123], v[52:55], v[204:207], v[120:123]\n" \
"ds_read_b64_tr_b16 v[16:17], v232 offset:512\n" \
"v_accvgpr_read_b32 v235, a249\n" \
"v_mfma_f32_16x16x32_bf16 v[124:127], v[56:59], v[200:203], 0\n" \
"ds_read_b64_tr_b16 v[18:19], v232 offset:2560\n" \
"v_accvgpr_read_b32 v236, a250\n" \
"v_mfma_f32_16x16x32_bf16 v[124:127], v[60:63], v[204:207], v[124:127]\n" \
"global_load_lds_dwordx4 v227, s[48:49] offset:-1024\n" \
"v_accvgpr_read_b32 v237, a251\n" \
"v_mfma_f32_16x16x32_bf16 v[144:147], v[32:35], v[208:211], 0\n" \
"s_add_u32 s48, s48, 0x80\n" \
"s_addc_u32 s49, s49, 0\n" \
"ds_read_b128 v[200:203], v230 offset:2048\n" \
"v_mfma_f32_16x16x32_bf16 v[144:147], v[36:39], v[212:215], v[144:147]\n" \
"ds_read_b128 v[204:207], v231 offset:2048\n" \
"v_cvt_pk_bf16_f32 v234, v234, v235\n" \
"v_mfma_f32_16x16x32_bf16 v[148:151], v[40:43], v[208:211], 0\n" \
"ds_read_b64_tr_b16 v[20:21], v232 offset:8704\n" \
"v_cvt_pk_bf16_f32 v236, v236, v237\n" \
"v_mfma_f32_16x16x32_bf16 v[148:151], v[44:47], v[212:215], v[148:151]\n" \
"global_load_lds_dwordx4 v224, s[46:47] offset:0\n" \
"v_accvgpr_write_b32 a218, v234\n" \
"v_mfma_f32_16x16x32_bf16 v[152:155], v[48:51], v[208:211], 0\n" \
"ds_read_b64_tr_b16 v[22:23], v232 offset:10752\n" \
"v_accvgpr_write_b32 a219, v236\n" \
"v_mfma_f32_16x16x32_bf16 v[152:155], v[52:55], v[212:215], v[152:155]\n" \
"ds_read_b64_tr_b16 v[24:25], v233 offset:512\n" \
"v_accvgpr_read_b32 v234, a220\n" \
"v_mfma_f32_16x16x32_bf16 v[156:159], v[56:59], v[208:211], 0\n" \
"ds_read_b64_tr_b16 v[26:27], v233 offset:2560\n" \
"v_accvgpr_read_b32 v235, a221\n" \
"v_mfma_f32_16x16x32_bf16 v[156:159], v[60:63], v[212:215], v[156:159]\n" \
"global_load_lds_dwordx4 v225, s[46:47] offset:1024\n" \
"v_accvgpr_read_b32 v236, a222\n" \
"v_mfma_f32_16x16x32_bf16 v[176:179], v[32:35], v[216:219], 0\n" \
"ds_read_b128 v[208:211], v230 offset:4096\n" \
"v_accvgpr_read_b32 v237, a223\n" \
"v_mfma_f32_16x16x32_bf16 v[176:179], v[36:39], v[220:223], v[176:179]\n" \
"ds_read_b128 v[212:215], v231 offset:4096\n" \
"v_cvt_pk_bf16_f32 v234, v234, v235\n" \
"v_mfma_f32_16x16x32_bf16 v[180:183], v[40:43], v[216:219], 0\n" \
"ds_read_b64_tr_b16 v[28:29], v233 offset:8704\n" \
"v_cvt_pk_bf16_f32 v236, v236, v237\n" \
"v_mfma_f32_16x16x32_bf16 v[180:183], v[44:47], v[220:223], v[180:183]\n" \
"global_load_lds_dwordx4 v226, s[46:47] offset:2048\n" \
"v_accvgpr_write_b32 a220, v234\n" \
"v_mfma_f32_16x16x32_bf16 v[184:187], v[48:51], v[216:219], 0\n" \
"ds_read_b64_tr_b16 v[30:31], v233 offset:10752\n" \
"v_accvgpr_write_b32 a221, v236\n" \
"v_mfma_f32_16x16x32_bf16 v[184:187], v[52:55], v[220:223], v[184:187]\n" \
"v_accvgpr_read_b32 v234, a252\n" \
"v_accvgpr_read_b32 v235, a253\n" \
"v_mfma_f32_16x16x32_bf16 v[188:191], v[56:59], v[216:219], 0\n" \
"v_accvgpr_read_b32 v236, a254\n" \
"v_accvgpr_read_b32 v237, a255\n" \
"v_mfma_f32_16x16x32_bf16 v[188:191], v[60:63], v[220:223], v[188:191]\n" \
"s_waitcnt lgkmcnt(1)\n" \
"global_load_lds_dwordx4 v227, s[46:47] offset:3072\n" \
"v_cvt_pk_bf16_f32 v234, v234, v235\n" \
"v_mfma_f32_16x16x32_bf16 v[64:67], v[0:3], v[192:195], v[64:67]\n" \
"s_add_u32 s46, s46, 0x80\n" \
"s_addc_u32 s47, s47, 0\n" \
"ds_read_b128 v[216:219], v230 offset:6144\n" \
"v_mfma_f32_16x16x32_bf16 v[64:67], v[4:7], v[196:199], v[64:67]\n" \
"ds_read_b128 v[220:223], v231 offset:6144\n" \
"v_cvt_pk_bf16_f32 v236, v236, v237\n" \
"v_mfma_f32_16x16x32_bf16 v[68:71], v[8:11], v[192:195], v[68:71]\n" \
"ds_read_b64_tr_b16 v[32:33], v232 offset:1024\n" \
"v_accvgpr_write_b32 a222, v234\n" \
"v_mfma_f32_16x16x32_bf16 v[68:71], v[12:15], v[196:199], v[68:71]\n" \
"buffer_store_dwordx4 a[200:203], v249, s[56:59], s90 offen offset:2048 sc1\n" \
"v_accvgpr_write_b32 a223, v236\n" \
"v_accvgpr_read_b32 v234, a128\n" \
"v_accvgpr_read_b32 v235, a129\n" \
"v_accvgpr_read_b32 v236, a130\n" \
"v_mfma_f32_16x16x32_bf16 v[72:75], v[16:19], v[192:195], v[72:75]\n" \
"ds_read_b64_tr_b16 v[34:35], v232 offset:3072\n" \
"v_accvgpr_read_b32 v237, a131\n" \
"v_mfma_f32_16x16x32_bf16 v[72:75], v[20:23], v[196:199], v[72:75]\n" \
"s_waitcnt lgkmcnt(3)\n" \
"ds_read_b64_tr_b16 v[36:37], v232 offset:9216\n" \
"v_mul_f32 v234, s92, v234\n" \
"v_mfma_f32_16x16x32_bf16 v[76:79], v[24:27], v[192:195], v[76:79]\n" \
"ds_read_b64_tr_b16 v[38:39], v232 offset:11264\n" \
"v_mul_f32 v235, s92, v235\n" \
"v_mfma_f32_16x16x32_bf16 v[76:79], v[28:31], v[196:199], v[76:79]\n" \
"v_mul_f32 v236, s92, v236\n" \
"v_mul_f32 v237, s92, v237\n" \
"v_mfma_f32_16x16x32_bf16 v[96:99], v[0:3], v[200:203], v[96:99]\n" \
"ds_read_b64_tr_b16 v[40:41], v233 offset:1024\n" \
"v_cvt_pk_bf16_f32 v234, v234, v235\n" \
"v_mfma_f32_16x16x32_bf16 v[96:99], v[4:7], v[204:207], v[96:99]\n" \
"ds_read_b64_tr_b16 v[42:43], v233 offset:3072\n" \
"v_cvt_pk_bf16_f32 v236, v236, v237\n" \
"v_mfma_f32_16x16x32_bf16 v[100:103], v[8:11], v[200:203], v[100:103]\n" \
"ds_read_b64_tr_b16 v[44:45], v233 offset:9216\n" \
"v_accvgpr_write_b32 a128, v234\n" \
"v_mfma_f32_16x16x32_bf16 v[100:103], v[12:15], v[204:207], v[100:103]\n" \
"v_accvgpr_write_b32 a129, v236\n" \
"v_accvgpr_read_b32 v234, a132\n" \
"v_mfma_f32_16x16x32_bf16 v[104:107], v[16:19], v[200:203], v[104:107]\n" \
"ds_read_b64_tr_b16 v[46:47], v233 offset:11264\n" \
"v_accvgpr_read_b32 v235, a133\n" \
"v_mfma_f32_16x16x32_bf16 v[104:107], v[20:23], v[204:207], v[104:107]\n" \
"ds_read_b64_tr_b16 v[48:49], v232 offset:1536\n" \
"v_accvgpr_read_b32 v236, a134\n" \
"v_mfma_f32_16x16x32_bf16 v[108:111], v[24:27], v[200:203], v[108:111]\n" \
"s_add_u32 m0, s95, 4096\n" \
"ds_read_b64_tr_b16 v[50:51], v232 offset:3584\n" \
"v_accvgpr_read_b32 v237, a135\n" \
"v_mfma_f32_16x16x32_bf16 v[108:111], v[28:31], v[204:207], v[108:111]\n" \
"global_load_lds_dwordx4 v228, s[44:45] offset:-4096\n" \
"v_mul_f32 v234, s92, v234\n" \
"v_mfma_f32_16x16x32_bf16 v[128:131], v[0:3], v[208:211], v[128:131]\n" \
"ds_read_b64_tr_b16 v[52:53], v232 offset:9728\n" \
"v_mul_f32 v235, s92, v235\n" \
"v_mfma_f32_16x16x32_bf16 v[128:131], v[4:7], v[212:215], v[128:131]\n" \
"ds_read_b64_tr_b16 v[54:55], v232 offset:11776\n" \
"v_mul_f32 v236, s92, v236\n" \
"v_mfma_f32_16x16x32_bf16 v[132:135], v[8:11], v[208:211], v[132:135]\n" \
"ds_read_b64_tr_b16 v[56:57], v233 offset:1536\n" \
"v_mul_f32 v237, s92, v237\n" \
"v_mfma_f32_16x16x32_bf16 v[132:135], v[12:15], v[212:215], v[132:135]\n" \
"global_load_lds_dwordx4 v229, s[44:45] offset:-3072\n" \
"v_cvt_pk_bf16_f32 v234, v234, v235\n" \
"v_mfma_f32_16x16x32_bf16 v[136:139], v[16:19], v[208:211], v[136:139]\n" \
"s_add_u32 s44, s44, 0x100000\n" \
"s_addc_u32 s45, s45, 0\n" \
"ds_read_b64_tr_b16 v[58:59], v233 offset:3584\n" \
"v_mfma_f32_16x16x32_bf16 v[136:139], v[20:23], v[212:215], v[136:139]\n" \
"ds_read_b64_tr_b16 v[60:61], v233 offset:9728\n" \
"v_cvt_pk_bf16_f32 v236, v236, v237\n" \
"v_mfma_f32_16x16x32_bf16 v[140:143], v[24:27], v[208:211], v[140:143]\n" \
"ds_read_b64_tr_b16 v[62:63], v233 offset:11776\n" \
"v_accvgpr_write_b32 a130, v234\n" \
"v_mfma_f32_16x16x32_bf16 v[140:143], v[28:31], v[212:215], v[140:143]\n" \
"s_waitcnt lgkmcnt(3)\n" \
"global_load_lds_dwordx4 v228, s[42:43] offset:-2048\n" \
"v_accvgpr_write_b32 a131, v236\n" \
"v_mfma_f32_16x16x32_bf16 v[160:163], v[0:3], v[216:219], v[160:163]\n" \
"v_add_u32 v230, 0x8000, v230\n" \
"v_add_u32 v231, 0x8000, v231\n" \
"v_mfma_f32_16x16x32_bf16 v[160:163], v[4:7], v[220:223], v[160:163]\n" \
"v_add_u32 v232, 0x4000, v232\n" \
"v_add_u32 v233, 0x4000, v233\n" \
"v_mfma_f32_16x16x32_bf16 v[164:167], v[8:11], v[216:219], v[164:167]\n" \
"v_accvgpr_read_b32 v234, a160\n" \
"v_accvgpr_read_b32 v235, a161\n" \
"v_mfma_f32_16x16x32_bf16 v[164:167], v[12:15], v[220:223], v[164:167]\n" \
"global_load_lds_dwordx4 v229, s[42:43] offset:-1024\n" \
"v_accvgpr_read_b32 v236, a162\n" \
"v_mfma_f32_16x16x32_bf16 v[168:171], v[16:19], v[216:219], v[168:171]\n" \
"s_add_u32 s42, s42, 0x100000\n" \
"s_addc_u32 s43, s43, 0\n" \
"v_accvgpr_read_b32 v237, a163\n" \
"v_mfma_f32_16x16x32_bf16 v[168:171], v[20:23], v[220:223], v[168:171]\n" \
"v_mul_f32 v234, s92, v234\n" \
"v_mul_f32 v235, s92, v235\n" \
"v_mfma_f32_16x16x32_bf16 v[172:175], v[24:27], v[216:219], v[172:175]\n" \
"s_waitcnt vmcnt(19) lgkmcnt(0)\n" \
"v_mul_f32 v236, s92, v236\n" \
"v_mul_f32 v237, s92, v237\n" \
"v_mfma_f32_16x16x32_bf16 v[172:175], v[28:31], v[220:223], v[172:175]\n" \
"s_barrier\n" \
"v_mfma_f32_16x16x32_bf16 v[80:83], v[32:35], v[192:195], v[80:83]\n" \
"ds_read_b64_tr_b16 v[0:1], v232 offset:0\n" \
"v_cvt_pk_bf16_f32 v234, v234, v235\n" \
"v_mfma_f32_16x16x32_bf16 v[80:83], v[36:39], v[196:199], v[80:83]\n" \
"ds_read_b64_tr_b16 v[2:3], v232 offset:2048\n" \
"v_cvt_pk_bf16_f32 v236, v236, v237\n" \
"v_mfma_f32_16x16x32_bf16 v[84:87], v[40:43], v[192:195], v[84:87]\n" \
"s_add_u32 m0, s87, 69632\n" \
"ds_read_b64_tr_b16 v[4:5], v232 offset:8192\n" \
"v_accvgpr_write_b32 a160, v234\n" \
"v_mfma_f32_16x16x32_bf16 v[84:87], v[44:47], v[196:199], v[84:87]\n" \
"global_load_lds_dwordx4 v224, s[48:49] offset:-4096\n" \
"v_accvgpr_write_b32 a161, v236\n" \
"v_mfma_f32_16x16x32_bf16 v[88:91], v[48:51], v[192:195], v[88:91]\n" \
"ds_read_b64_tr_b16 v[6:7], v232 offset:10240\n" \
"v_accvgpr_read_b32 v234, a164\n" \
"v_mfma_f32_16x16x32_bf16 v[88:91], v[52:55], v[196:199], v[88:91]\n" \
"ds_read_b64_tr_b16 v[8:9], v233 offset:0\n" \
"v_accvgpr_read_b32 v235, a165\n" \
"v_mfma_f32_16x16x32_bf16 v[92:95], v[56:59], v[192:195], v[92:95]\n" \
"ds_read_b64_tr_b16 v[10:11], v233 offset:2048\n" \
"v_accvgpr_read_b32 v236, a166\n" \
"v_mfma_f32_16x16x32_bf16 v[92:95], v[60:63], v[196:199], v[92:95]\n" \
"global_load_lds_dwordx4 v225, s[48:49] offset:-3072\n" \
"v_accvgpr_read_b32 v237, a167\n" \
"v_mfma_f32_16x16x32_bf16 v[112:115], v[32:35], v[200:203], v[112:115]\n" \
"ds_read_b128 v[192:195], v230 offset:0\n" \
"v_mul_f32 v234, s92, v234\n" \
"v_mfma_f32_16x16x32_bf16 v[112:115], v[36:39], v[204:207], v[112:115]\n" \
"ds_read_b128 v[196:199], v231 offset:0\n" \
"v_mul_f32 v235, s92, v235\n" \
"v_mfma_f32_16x16x32_bf16 v[116:119], v[40:43], v[200:203], v[116:119]\n" \
"ds_read_b64_tr_b16 v[12:13], v233 offset:8192\n" \
"v_mul_f32 v236, s92, v236\n" \
"v_mfma_f32_16x16x32_bf16 v[116:119], v[44:47], v[204:207], v[116:119]\n" \
"global_load_lds_dwordx4 v226, s[48:49] offset:-2048\n" \
"v_mul_f32 v237, s92, v237\n" \
"v_mfma_f32_16x16x32_bf16 v[120:123], v[48:51], v[200:203], v[120:123]\n" \
"ds_read_b64_tr_b16 v[14:15], v233 offset:10240\n" \
"v_cvt_pk_bf16_f32 v234, v234, v235\n" \
"v_mfma_f32_16x16x32_bf16 v[120:123], v[52:55], v[204:207], v[120:123]\n" \
"ds_read_b64_tr_b16 v[16:17], v232 offset:512\n" \
"v_cvt_pk_bf16_f32 v236, v236, v237\n" \
"v_mfma_f32_16x16x32_bf16 v[124:127], v[56:59], v[200:203], v[124:127]\n" \
"ds_read_b64_tr_b16 v[18:19], v232 offset:2560\n" \
"v_accvgpr_write_b32 a162, v234\n" \
"v_mfma_f32_16x16x32_bf16 v[124:127], v[60:63], v[204:207], v[124:127]\n" \
"global_load_lds_dwordx4 v227, s[48:49] offset:-1024\n" \
"v_accvgpr_write_b32 a163, v236\n" \
"v_mfma_f32_16x16x32_bf16 v[144:147], v[32:35], v[208:211], v[144:147]\n" \
"s_add_u32 s48, s48, 0x100\n" \
"s_addc_u32 s49, s49, 0\n" \
"ds_read_b128 v[200:203], v230 offset:2048\n" \
"v_mfma_f32_16x16x32_bf16 v[144:147], v[36:39], v[212:215], v[144:147]\n" \
"ds_read_b128 v[204:207], v231 offset:2048\n" \
"v_accvgpr_read_b32 v234, a136\n" \
"v_mfma_f32_16x16x32_bf16 v[148:151], v[40:43], v[208:211], v[148:151]\n" \
"ds_read_b64_tr_b16 v[20:21], v232 offset:8704\n" \
"v_accvgpr_read_b32 v235, a137\n" \
"v_mfma_f32_16x16x32_bf16 v[148:151], v[44:47], v[212:215], v[148:151]\n" \
"global_load_lds_dwordx4 v224, s[46:47] offset:0\n" \
"v_accvgpr_read_b32 v236, a138\n" \
"v_mfma_f32_16x16x32_bf16 v[152:155], v[48:51], v[208:211], v[152:155]\n" \
"ds_read_b64_tr_b16 v[22:23], v232 offset:10752\n" \
"v_accvgpr_read_b32 v237, a139\n" \
"v_mfma_f32_16x16x32_bf16 v[152:155], v[52:55], v[212:215], v[152:155]\n" \
"ds_read_b64_tr_b16 v[24:25], v233 offset:512\n" \
"v_mul_f32 v234, s92, v234\n" \
"v_mfma_f32_16x16x32_bf16 v[156:159], v[56:59], v[208:211], v[156:159]\n" \
"ds_read_b64_tr_b16 v[26:27], v233 offset:2560\n" \
"v_mul_f32 v235, s92, v235\n" \
"v_mfma_f32_16x16x32_bf16 v[156:159], v[60:63], v[212:215], v[156:159]\n" \
"global_load_lds_dwordx4 v225, s[46:47] offset:1024\n" \
"v_mul_f32 v236, s92, v236\n" \
"v_mfma_f32_16x16x32_bf16 v[176:179], v[32:35], v[216:219], v[176:179]\n" \
"ds_read_b128 v[208:211], v230 offset:4096\n" \
"v_mul_f32 v237, s92, v237\n" \
"v_mfma_f32_16x16x32_bf16 v[176:179], v[36:39], v[220:223], v[176:179]\n" \
"ds_read_b128 v[212:215], v231 offset:4096\n" \
"v_cvt_pk_bf16_f32 v234, v234, v235\n" \
"v_mfma_f32_16x16x32_bf16 v[180:183], v[40:43], v[216:219], v[180:183]\n" \
"ds_read_b64_tr_b16 v[28:29], v233 offset:8704\n" \
"v_cvt_pk_bf16_f32 v236, v236, v237\n" \
"v_mfma_f32_16x16x32_bf16 v[180:183], v[44:47], v[220:223], v[180:183]\n" \
"global_load_lds_dwordx4 v226, s[46:47] offset:2048\n" \
"v_accvgpr_write_b32 a136, v234\n" \
"v_mfma_f32_16x16x32_bf16 v[184:187], v[48:51], v[216:219], v[184:187]\n" \
"ds_read_b64_tr_b16 v[30:31], v233 offset:10752\n" \
"v_accvgpr_write_b32 a137, v236\n" \
"v_mfma_f32_16x16x32_bf16 v[184:187], v[52:55], v[220:223], v[184:187]\n" \
"v_accvgpr_read_b32 v234, a140\n" \
"v_accvgpr_read_b32 v235, a141\n" \
"v_mfma_f32_16x16x32_bf16 v[188:191], v[56:59], v[216:219], v[188:191]\n" \
"v_accvgpr_read_b32 v236, a142\n" \
"v_accvgpr_read_b32 v237, a143\n" \
"v_mfma_f32_16x16x32_bf16 v[188:191], v[60:63], v[220:223], v[188:191]\n" \
"s_waitcnt lgkmcnt(1)\n" \
"global_load_lds_dwordx4 v227, s[46:47] offset:3072\n" \
"v_mul_f32 v234, s92, v234\n" \
"v_mfma_f32_16x16x32_bf16 v[64:67], v[0:3], v[192:195], v[64:67]\n" \
"s_add_u32 s46, s46, 0x100\n" \
"s_addc_u32 s47, s47, 0\n" \
"ds_read_b128 v[216:219], v230 offset:6144\n" \
"v_mfma_f32_16x16x32_bf16 v[64:67], v[4:7], v[196:199], v[64:67]\n" \
"ds_read_b128 v[220:223], v231 offset:6144\n" \
"v_mul_f32 v235, s92, v235\n" \
"v_mfma_f32_16x16x32_bf16 v[68:71], v[8:11], v[192:195], v[68:71]\n" \
"ds_read_b64_tr_b16 v[32:33], v232 offset:1024\n" \
"v_mul_f32 v236, s92, v236\n" \
"v_mfma_f32_16x16x32_bf16 v[68:71], v[12:15], v[196:199], v[68:71]\n" \
"buffer_store_dwordx4 a[232:235], v249, s[56:59], s90 offen offset:2304 sc1\n" \
"v_mul_f32 v237, s92, v237\n" \
"v_cvt_pk_bf16_f32 v234, v234, v235\n" \
"v_cvt_pk_bf16_f32 v236, v236, v237\n" \
"v_accvgpr_write_b32 a138, v234\n" \
"v_mfma_f32_16x16x32_bf16 v[72:75], v[16:19], v[192:195], v[72:75]\n" \
"s_add_u32 s90, s89, 0\n" \
"ds_read_b64_tr_b16 v[34:35], v232 offset:3072\n" \
"v_accvgpr_write_b32 a139, v236\n" \
"v_mfma_f32_16x16x32_bf16 v[72:75], v[20:23], v[196:199], v[72:75]\n" \
"s_waitcnt lgkmcnt(3)\n" \
"ds_read_b64_tr_b16 v[36:37], v232 offset:9216\n" \
"v_accvgpr_read_b32 v234, a168\n" \
"v_mfma_f32_16x16x32_bf16 v[76:79], v[24:27], v[192:195], v[76:79]\n" \
"ds_read_b64_tr_b16 v[38:39], v232 offset:11264\n" \
"v_accvgpr_read_b32 v235, a169\n" \
"v_mfma_f32_16x16x32_bf16 v[76:79], v[28:31], v[196:199], v[76:79]\n" \
"buffer_store_dwordx4 a[208:211], v248, s[56:59], s90 offen offset:0 sc1\n" \
"v_accvgpr_read_b32 v236, a170\n" \
"v_accvgpr_read_b32 v237, a171\n" \
"v_mul_f32 v234, s92, v234\n" \
"v_mul_f32 v235, s92, v235\n" \
"v_mfma_f32_16x16x32_bf16 v[96:99], v[0:3], v[200:203], v[96:99]\n" \
"ds_read_b64_tr_b16 v[40:41], v233 offset:1024\n" \
"v_mul_f32 v236, s92, v236\n" \
"v_mfma_f32_16x16x32_bf16 v[96:99], v[4:7], v[204:207], v[96:99]\n" \
"ds_read_b64_tr_b16 v[42:43], v233 offset:3072\n" \
"v_mul_f32 v237, s92, v237\n" \
"v_mfma_f32_16x16x32_bf16 v[100:103], v[8:11], v[200:203], v[100:103]\n" \
"ds_read_b64_tr_b16 v[44:45], v233 offset:9216\n" \
"v_cvt_pk_bf16_f32 v234, v234, v235\n" \
"v_mfma_f32_16x16x32_bf16 v[100:103], v[12:15], v[204:207], v[100:103]\n" \
"v_cvt_pk_bf16_f32 v236, v236, v237\n" \
"v_accvgpr_write_b32 a168, v234\n" \
"v_mfma_f32_16x16x32_bf16 v[104:107], v[16:19], v[200:203], v[104:107]\n" \
"ds_read_b64_tr_b16 v[46:47], v233 offset:11264\n" \
"v_accvgpr_write_b32 a169, v236\n" \
"v_mfma_f32_16x16x32_bf16 v[104:107], v[20:23], v[204:207], v[104:107]\n" \
"ds_read_b64_tr_b16 v[48:49], v232 offset:1536\n" \
"v_accvgpr_read_b32 v234, a172\n" \
"v_mfma_f32_16x16x32_bf16 v[108:111], v[24:27], v[200:203], v[108:111]\n" \
"s_add_u32 m0, s95, 20480\n" \
"ds_read_b64_tr_b16 v[50:51], v232 offset:3584\n" \
"v_accvgpr_read_b32 v235, a173\n" \
"v_mfma_f32_16x16x32_bf16 v[108:111], v[28:31], v[204:207], v[108:111]\n" \
"global_load_lds_dwordx4 v228, s[44:45] offset:-4096\n" \
"v_accvgpr_read_b32 v236, a174\n" \
"v_mfma_f32_16x16x32_bf16 v[128:131], v[0:3], v[208:211], v[128:131]\n" \
"ds_read_b64_tr_b16 v[52:53], v232 offset:9728\n" \
"v_accvgpr_read_b32 v237, a175\n" \
"v_mfma_f32_16x16x32_bf16 v[128:131], v[4:7], v[212:215], v[128:131]\n" \
"ds_read_b64_tr_b16 v[54:55], v232 offset:11776\n" \
"v_mul_f32 v234, s92, v234\n" \
"v_mfma_f32_16x16x32_bf16 v[132:135], v[8:11], v[208:211], v[132:135]\n" \
"ds_read_b64_tr_b16 v[56:57], v233 offset:1536\n" \
"v_mul_f32 v235, s92, v235\n" \
"v_mfma_f32_16x16x32_bf16 v[132:135], v[12:15], v[212:215], v[132:135]\n" \
"global_load_lds_dwordx4 v229, s[44:45] offset:-3072\n" \
"v_mul_f32 v236, s92, v236\n" \
"v_mfma_f32_16x16x32_bf16 v[136:139], v[16:19], v[208:211], v[136:139]\n" \
"s_add_u32 s44, s44, 0x80000\n" \
"s_addc_u32 s45, s45, 0\n" \
"ds_read_b64_tr_b16 v[58:59], v233 offset:3584\n" \
"v_mfma_f32_16x16x32_bf16 v[136:139], v[20:23], v[212:215], v[136:139]\n" \
"ds_read_b64_tr_b16 v[60:61], v233 offset:9728\n" \
"v_mul_f32 v237, s92, v237\n" \
"v_mfma_f32_16x16x32_bf16 v[140:143], v[24:27], v[208:211], v[140:143]\n" \
"ds_read_b64_tr_b16 v[62:63], v233 offset:11776\n" \
"v_cvt_pk_bf16_f32 v234, v234, v235\n" \
"v_mfma_f32_16x16x32_bf16 v[140:143], v[28:31], v[212:215], v[140:143]\n" \
"s_waitcnt lgkmcnt(3)\n" \
"global_load_lds_dwordx4 v228, s[42:43] offset:-2048\n" \
"v_cvt_pk_bf16_f32 v236, v236, v237\n" \
"v_mfma_f32_16x16x32_bf16 v[160:163], v[0:3], v[216:219], v[160:163]\n" \
"v_add_u32 v230, 0x8000, v230\n" \
"v_add_u32 v231, 0x8000, v231\n" \
"v_mfma_f32_16x16x32_bf16 v[160:163], v[4:7], v[220:223], v[160:163]\n" \
"v_add_u32 v232, 0x4000, v232\n" \
"v_add_u32 v233, 0x4000, v233\n" \
"v_mfma_f32_16x16x32_bf16 v[164:167], v[8:11], v[216:219], v[164:167]\n" \
"v_accvgpr_write_b32 a170, v234\n" \
"v_accvgpr_write_b32 a171, v236\n" \
"v_mfma_f32_16x16x32_bf16 v[164:167], v[12:15], v[220:223], v[164:167]\n" \
"global_load_lds_dwordx4 v229, s[42:43] offset:-1024\n" \
"v_accvgpr_read_b32 v234, a144\n" \
"v_mfma_f32_16x16x32_bf16 v[168:171], v[16:19], v[216:219], v[168:171]\n" \
"s_add_u32 s42, s42, 0x80000\n" \
"s_addc_u32 s43, s43, 0\n" \
"v_accvgpr_read_b32 v235, a145\n" \
"v_mfma_f32_16x16x32_bf16 v[168:171], v[20:23], v[220:223], v[168:171]\n" \
"v_accvgpr_read_b32 v236, a146\n" \
"v_accvgpr_read_b32 v237, a147\n" \
"v_mfma_f32_16x16x32_bf16 v[172:175], v[24:27], v[216:219], v[172:175]\n" \
"s_waitcnt vmcnt(19) lgkmcnt(0)\n" \
"v_cvt_pk_bf16_f32 v234, v234, v235\n" \
"v_cvt_pk_bf16_f32 v236, v236, v237\n" \
"v_mfma_f32_16x16x32_bf16 v[172:175], v[28:31], v[220:223], v[172:175]\n" \
"s_barrier\n" \
"v_mfma_f32_16x16x32_bf16 v[80:83], v[32:35], v[192:195], v[80:83]\n" \
"ds_read_b64_tr_b16 v[0:1], v232 offset:0\n" \
"v_accvgpr_write_b32 a144, v234\n" \
"v_mfma_f32_16x16x32_bf16 v[80:83], v[36:39], v[196:199], v[80:83]\n" \
"ds_read_b64_tr_b16 v[2:3], v232 offset:2048\n" \
"v_accvgpr_write_b32 a145, v236\n" \
"v_mfma_f32_16x16x32_bf16 v[84:87], v[40:43], v[192:195], v[84:87]\n" \
"s_add_u32 m0, s87, 102400\n" \
"ds_read_b64_tr_b16 v[4:5], v232 offset:8192\n" \
"v_accvgpr_read_b32 v234, a176\n" \
"v_mfma_f32_16x16x32_bf16 v[84:87], v[44:47], v[196:199], v[84:87]\n" \
"global_load_lds_dwordx4 v224, s[48:49] offset:-4096\n" \
"v_accvgpr_read_b32 v235, a177\n" \
"v_mfma_f32_16x16x32_bf16 v[88:91], v[48:51], v[192:195], v[88:91]\n" \
"ds_read_b64_tr_b16 v[6:7], v232 offset:10240\n" \
"v_accvgpr_read_b32 v236, a178\n" \
"v_mfma_f32_16x16x32_bf16 v[88:91], v[52:55], v[196:199], v[88:91]\n" \
"ds_read_b64_tr_b16 v[8:9], v233 offset:0\n" \
"v_accvgpr_read_b32 v237, a179\n" \
"v_mfma_f32_16x16x32_bf16 v[92:95], v[56:59], v[192:195], v[92:95]\n" \
"ds_read_b64_tr_b16 v[10:11], v233 offset:2048\n" \
"v_cvt_pk_bf16_f32 v234, v234, v235\n" \
"v_mfma_f32_16x16x32_bf16 v[92:95], v[60:63], v[196:199], v[92:95]\n" \
"global_load_lds_dwordx4 v225, s[48:49] offset:-3072\n" \
"v_cvt_pk_bf16_f32 v236, v236, v237\n" \
"v_mfma_f32_16x16x32_bf16 v[112:115], v[32:35], v[200:203], v[112:115]\n" \
"ds_read_b128 v[192:195], v230 offset:0\n" \
"v_accvgpr_write_b32 a146, v234\n" \
"v_mfma_f32_16x16x32_bf16 v[112:115], v[36:39], v[204:207], v[112:115]\n" \
"ds_read_b128 v[196:199], v231 offset:0\n" \
"v_accvgpr_write_b32 a147, v236\n" \
"v_mfma_f32_16x16x32_bf16 v[116:119], v[40:43], v[200:203], v[116:119]\n" \
"ds_read_b64_tr_b16 v[12:13], v233 offset:8192\n" \
"v_accvgpr_read_b32 v234, a148\n" \
"v_mfma_f32_16x16x32_bf16 v[116:119], v[44:47], v[204:207], v[116:119]\n" \
"global_load_lds_dwordx4 v226, s[48:49] offset:-2048\n" \
"v_accvgpr_read_b32 v235, a149\n" \
"v_mfma_f32_16x16x32_bf16 v[120:123], v[48:51], v[200:203], v[120:123]\n" \
"ds_read_b64_tr_b16 v[14:15], v233 offset:10240\n" \
"v_accvgpr_read_b32 v236, a150\n" \
"v_mfma_f32_16x16x32_bf16 v[120:123], v[52:55], v[204:207], v[120:123]\n" \
"ds_read_b64_tr_b16 v[16:17], v232 offset:512\n" \
"v_accvgpr_read_b32 v237, a151\n" \
"v_mfma_f32_16x16x32_bf16 v[124:127], v[56:59], v[200:203], v[124:127]\n" \
"ds_read_b64_tr_b16 v[18:19], v232 offset:2560\n" \
"v_cvt_pk_bf16_f32 v234, v234, v235\n" \
"v_mfma_f32_16x16x32_bf16 v[124:127], v[60:63], v[204:207], v[124:127]\n" \
"global_load_lds_dwordx4 v227, s[48:49] offset:-1024\n" \
"v_cvt_pk_bf16_f32 v236, v236, v237\n" \
"v_mfma_f32_16x16x32_bf16 v[144:147], v[32:35], v[208:211], v[144:147]\n" \
"s_add_u32 s48, s48, 0x80\n" \
"s_addc_u32 s49, s49, 0\n" \
"ds_read_b128 v[200:203], v230 offset:2048\n" \
"v_mfma_f32_16x16x32_bf16 v[144:147], v[36:39], v[212:215], v[144:147]\n" \
"ds_read_b128 v[204:207], v231 offset:2048\n" \
"v_accvgpr_write_b32 a148, v234\n" \
"v_mfma_f32_16x16x32_bf16 v[148:151], v[40:43], v[208:211], v[148:151]\n" \
"ds_read_b64_tr_b16 v[20:21], v232 offset:8704\n" \
"v_accvgpr_write_b32 a149, v236\n" \
"v_mfma_f32_16x16x32_bf16 v[148:151], v[44:47], v[212:215], v[148:151]\n" \
"global_load_lds_dwordx4 v224, s[46:47] offset:0\n" \
"v_accvgpr_read_b32 v234, a180\n" \
"v_mfma_f32_16x16x32_bf16 v[152:155], v[48:51], v[208:211], v[152:155]\n" \
"ds_read_b64_tr_b16 v[22:23], v232 offset:10752\n" \
"v_accvgpr_read_b32 v235, a181\n" \
"v_mfma_f32_16x16x32_bf16 v[152:155], v[52:55], v[212:215], v[152:155]\n" \
"ds_read_b64_tr_b16 v[24:25], v233 offset:512\n" \
"v_accvgpr_read_b32 v236, a182\n" \
"v_mfma_f32_16x16x32_bf16 v[156:159], v[56:59], v[208:211], v[156:159]\n" \
"ds_read_b64_tr_b16 v[26:27], v233 offset:2560\n" \
"v_accvgpr_read_b32 v237, a183\n" \
"v_mfma_f32_16x16x32_bf16 v[156:159], v[60:63], v[212:215], v[156:159]\n" \
"global_load_lds_dwordx4 v225, s[46:47] offset:1024\n" \
"v_cvt_pk_bf16_f32 v234, v234, v235\n" \
"v_mfma_f32_16x16x32_bf16 v[176:179], v[32:35], v[216:219], v[176:179]\n" \
"ds_read_b128 v[208:211], v230 offset:4096\n" \
"v_cvt_pk_bf16_f32 v236, v236, v237\n" \
"v_mfma_f32_16x16x32_bf16 v[176:179], v[36:39], v[220:223], v[176:179]\n" \
"ds_read_b128 v[212:215], v231 offset:4096\n" \
"v_accvgpr_write_b32 a150, v234\n" \
"v_mfma_f32_16x16x32_bf16 v[180:183], v[40:43], v[216:219], v[180:183]\n" \
"ds_read_b64_tr_b16 v[28:29], v233 offset:8704\n" \
"v_accvgpr_write_b32 a151, v236\n" \
"v_mfma_f32_16x16x32_bf16 v[180:183], v[44:47], v[220:223], v[180:183]\n" \
"global_load_lds_dwordx4 v226, s[46:47] offset:2048\n" \
"v_accvgpr_read_b32 v234, a152\n" \
"v_mfma_f32_16x16x32_bf16 v[184:187], v[48:51], v[216:219], v[184:187]\n" \
"ds_read_b64_tr_b16 v[30:31], v233 offset:10752\n" \
"v_accvgpr_read_b32 v235, a153\n" \
"v_mfma_f32_16x16x32_bf16 v[184:187], v[52:55], v[220:223], v[184:187]\n" \
"v_accvgpr_read_b32 v236, a154\n" \
"v_accvgpr_read_b32 v237, a155\n" \
"v_mfma_f32_16x16x32_bf16 v[188:191], v[56:59], v[216:219], v[188:191]\n" \
"v_cvt_pk_bf16_f32 v234, v234, v235\n" \
"v_cvt_pk_bf16_f32 v236, v236, v237\n" \
"v_mfma_f32_16x16x32_bf16 v[188:191], v[60:63], v[220:223], v[188:191]\n" \
"s_waitcnt lgkmcnt(1)\n" \
"global_load_lds_dwordx4 v227, s[46:47] offset:3072\n" \
"v_accvgpr_write_b32 a152, v234\n" \
"v_mfma_f32_16x16x32_bf16 v[64:67], v[0:3], v[192:195], v[64:67]\n" \
"Lpg2726_%=:\n" \
"s_cbranch_execz Lpg4891_%=\n" \
"s_add_u32 s46, s46, 0x80\n" \
"s_addc_u32 s47, s47, 0\n" \
"ds_read_b128 v[216:219], v230 offset:6144\n" \
"v_mfma_f32_16x16x32_bf16 v[64:67], v[4:7], v[196:199], v[64:67]\n" \
"ds_read_b128 v[220:223], v231 offset:6144\n" \
"v_accvgpr_write_b32 a153, v236\n" \
"v_mfma_f32_16x16x32_bf16 v[68:71], v[8:11], v[192:195], v[68:71]\n" \
"ds_read_b64_tr_b16 v[32:33], v232 offset:1024\n" \
"v_accvgpr_read_b32 v234, a184\n" \
"v_mfma_f32_16x16x32_bf16 v[68:71], v[12:15], v[196:199], v[68:71]\n" \
"buffer_store_dwordx4 a[212:215], v248, s[56:59], s90 offen offset:1024 sc1\n" \
"v_accvgpr_read_b32 v235, a185\n" \
"v_accvgpr_read_b32 v236, a186\n" \
"v_accvgpr_read_b32 v237, a187\n" \
"v_cvt_pk_bf16_f32 v234, v234, v235\n" \
"v_mfma_f32_16x16x32_bf16 v[72:75], v[16:19], v[192:195], v[72:75]\n" \
"ds_read_b64_tr_b16 v[34:35], v232 offset:3072\n" \
"v_cvt_pk_bf16_f32 v236, v236, v237\n" \
"v_mfma_f32_16x16x32_bf16 v[72:75], v[20:23], v[196:199], v[72:75]\n" \
"s_waitcnt lgkmcnt(3)\n" \
"ds_read_b64_tr_b16 v[36:37], v232 offset:9216\n" \
"v_accvgpr_write_b32 a154, v234\n" \
"v_mfma_f32_16x16x32_bf16 v[76:79], v[24:27], v[192:195], v[76:79]\n" \
"ds_read_b64_tr_b16 v[38:39], v232 offset:11264\n" \
"v_accvgpr_write_b32 a155, v236\n" \
"v_mfma_f32_16x16x32_bf16 v[76:79], v[28:31], v[196:199], v[76:79]\n" \
"v_accvgpr_read_b32 v234, a156\n" \
"v_accvgpr_read_b32 v235, a157\n" \
"v_mfma_f32_16x16x32_bf16 v[96:99], v[0:3], v[200:203], v[96:99]\n" \
"ds_read_b64_tr_b16 v[40:41], v233 offset:1024\n" \
"v_accvgpr_read_b32 v236, a158\n" \
"v_mfma_f32_16x16x32_bf16 v[96:99], v[4:7], v[204:207], v[96:99]\n" \
"ds_read_b64_tr_b16 v[42:43], v233 offset:3072\n" \
"v_accvgpr_read_b32 v237, a159\n" \
"v_mfma_f32_16x16x32_bf16 v[100:103], v[8:11], v[200:203], v[100:103]\n" \
"ds_read_b64_tr_b16 v[44:45], v233 offset:9216\n" \
"v_cvt_pk_bf16_f32 v234, v234, v235\n" \
"v_mfma_f32_16x16x32_bf16 v[100:103], v[12:15], v[204:207], v[100:103]\n" \
"v_cvt_pk_bf16_f32 v236, v236, v237\n" \
"v_accvgpr_write_b32 a156, v234\n" \
"v_mfma_f32_16x16x32_bf16 v[104:107], v[16:19], v[200:203], v[104:107]\n" \
"ds_read_b64_tr_b16 v[46:47], v233 offset:11264\n" \
"v_accvgpr_write_b32 a157, v236\n" \
"v_mfma_f32_16x16x32_bf16 v[104:107], v[20:23], v[204:207], v[104:107]\n" \
"ds_read_b64_tr_b16 v[48:49], v232 offset:1536\n" \
"v_accvgpr_read_b32 v234, a188\n" \
"v_mfma_f32_16x16x32_bf16 v[108:111], v[24:27], v[200:203], v[108:111]\n" \
"s_add_u32 m0, s95, 36864\n" \
"ds_read_b64_tr_b16 v[50:51], v232 offset:3584\n" \
"v_accvgpr_read_b32 v235, a189\n" \
"v_mfma_f32_16x16x32_bf16 v[108:111], v[28:31], v[204:207], v[108:111]\n" \
"global_load_lds_dwordx4 v228, s[44:45] offset:-4096\n" \
"v_accvgpr_read_b32 v236, a190\n" \
"v_mfma_f32_16x16x32_bf16 v[128:131], v[0:3], v[208:211], v[128:131]\n" \
"ds_read_b64_tr_b16 v[52:53], v232 offset:9728\n" \
"v_accvgpr_read_b32 v237, a191\n" \
"v_mfma_f32_16x16x32_bf16 v[128:131], v[4:7], v[212:215], v[128:131]\n" \
"ds_read_b64_tr_b16 v[54:55], v232 offset:11776\n" \
"v_cvt_pk_bf16_f32 v234, v234, v235\n" \
"v_mfma_f32_16x16x32_bf16 v[132:135], v[8:11], v[208:211], v[132:135]\n" \
"ds_read_b64_tr_b16 v[56:57], v233 offset:1536\n" \
"v_cvt_pk_bf16_f32 v236, v236, v237\n" \
"v_mfma_f32_16x16x32_bf16 v[132:135], v[12:15], v[212:215], v[132:135]\n" \
"global_load_lds_dwordx4 v229, s[44:45] offset:-3072\n" \
"v_accvgpr_write_b32 a158, v234\n" \
"v_mfma_f32_16x16x32_bf16 v[136:139], v[16:19], v[208:211], v[136:139]\n" \
"s_add_u32 s44, s44, 0x80000\n" \
"s_addc_u32 s45, s45, 0\n" \
"ds_read_b64_tr_b16 v[58:59], v233 offset:3584\n" \
"v_mfma_f32_16x16x32_bf16 v[136:139], v[20:23], v[212:215], v[136:139]\n" \
"ds_read_b64_tr_b16 v[60:61], v233 offset:9728\n" \
"v_accvgpr_write_b32 a159, v236\n" \
"v_mfma_f32_16x16x32_bf16 v[140:143], v[24:27], v[208:211], v[140:143]\n" \
"ds_read_b64_tr_b16 v[62:63], v233 offset:11776\n" \
"v_accvgpr_read_b32 v234, a64\n" \
"v_mfma_f32_16x16x32_bf16 v[140:143], v[28:31], v[212:215], v[140:143]\n" \
"s_waitcnt lgkmcnt(3)\n" \
"global_load_lds_dwordx4 v228, s[42:43] offset:-2048\n" \
"v_accvgpr_read_b32 v235, a65\n" \
"v_mfma_f32_16x16x32_bf16 v[160:163], v[0:3], v[216:219], v[160:163]\n" \
"v_add_u32 v230, 0xffff0000, v230\n" \
"v_add_u32 v231, 0xffff0000, v231\n" \
"v_mfma_f32_16x16x32_bf16 v[160:163], v[4:7], v[220:223], v[160:163]\n" \
"v_add_u32 v232, 0xffff4000, v232\n" \
"v_add_u32 v233, 0xffff4000, v233\n" \
"v_mfma_f32_16x16x32_bf16 v[164:167], v[8:11], v[216:219], v[164:167]\n" \
"v_accvgpr_read_b32 v236, a66\n" \
"v_accvgpr_read_b32 v237, a67\n" \
"v_mfma_f32_16x16x32_bf16 v[164:167], v[12:15], v[220:223], v[164:167]\n" \
"global_load_lds_dwordx4 v229, s[42:43] offset:-1024\n" \
"v_mul_f32 v234, s92, v234\n" \
"v_mfma_f32_16x16x32_bf16 v[168:171], v[16:19], v[216:219], v[168:171]\n" \
"s_add_u32 s42, s42, 0x80000\n" \
"s_addc_u32 s43, s43, 0\n" \
"v_mul_f32 v235, s92, v235\n" \
"v_mfma_f32_16x16x32_bf16 v[168:171], v[20:23], v[220:223], v[168:171]\n" \
"v_mul_f32 v236, s92, v236\n" \
"v_mul_f32 v237, s92, v237\n" \
"v_mfma_f32_16x16x32_bf16 v[172:175], v[24:27], v[216:219], v[172:175]\n" \
"s_waitcnt vmcnt(19) lgkmcnt(0)\n" \
"v_cvt_pk_bf16_f32 v234, v234, v235\n" \
"v_cvt_pk_bf16_f32 v236, v236, v237\n" \
"v_mfma_f32_16x16x32_bf16 v[172:175], v[28:31], v[220:223], v[172:175]\n" \
"s_barrier\n" \
"v_mfma_f32_16x16x32_bf16 v[80:83], v[32:35], v[192:195], v[80:83]\n" \
"ds_read_b64_tr_b16 v[0:1], v232 offset:0\n" \
"v_accvgpr_write_b32 a64, v234\n" \
"v_mfma_f32_16x16x32_bf16 v[80:83], v[36:39], v[196:199], v[80:83]\n" \
"ds_read_b64_tr_b16 v[2:3], v232 offset:2048\n" \
"v_accvgpr_write_b32 a65, v236\n" \
"v_mfma_f32_16x16x32_bf16 v[84:87], v[40:43], v[192:195], v[84:87]\n" \
"s_add_u32 m0, s87, 135168\n" \
"ds_read_b64_tr_b16 v[4:5], v232 offset:8192\n" \
"v_accvgpr_read_b32 v234, a68\n" \
"v_mfma_f32_16x16x32_bf16 v[84:87], v[44:47], v[196:199], v[84:87]\n" \
"global_load_lds_dwordx4 v224, s[48:49] offset:-4096\n" \
"v_accvgpr_read_b32 v235, a69\n" \
"v_mfma_f32_16x16x32_bf16 v[88:91], v[48:51], v[192:195], v[88:91]\n" \
"ds_read_b64_tr_b16 v[6:7], v232 offset:10240\n" \
"v_accvgpr_read_b32 v236, a70\n" \
"v_mfma_f32_16x16x32_bf16 v[88:91], v[52:55], v[196:199], v[88:91]\n" \
"ds_read_b64_tr_b16 v[8:9], v233 offset:0\n" \
"v_accvgpr_read_b32 v237, a71\n" \
"v_mfma_f32_16x16x32_bf16 v[92:95], v[56:59], v[192:195], v[92:95]\n" \
"ds_read_b64_tr_b16 v[10:11], v233 offset:2048\n" \
"v_mul_f32 v234, s92, v234\n" \
"v_mfma_f32_16x16x32_bf16 v[92:95], v[60:63], v[196:199], v[92:95]\n" \
"global_load_lds_dwordx4 v225, s[48:49] offset:-3072\n" \
"v_mul_f32 v235, s92, v235\n" \
"v_mfma_f32_16x16x32_bf16 v[112:115], v[32:35], v[200:203], v[112:115]\n" \
"ds_read_b128 v[192:195], v230 offset:0\n" \
"v_mul_f32 v236, s92, v236\n" \
"v_mfma_f32_16x16x32_bf16 v[112:115], v[36:39], v[204:207], v[112:115]\n" \
"ds_read_b128 v[196:199], v231 offset:0\n" \
"v_mul_f32 v237, s92, v237\n" \
"v_mfma_f32_16x16x32_bf16 v[116:119], v[40:43], v[200:203], v[116:119]\n" \
"ds_read_b64_tr_b16 v[12:13], v233 offset:8192\n" \
"v_cvt_pk_bf16_f32 v234, v234, v235\n" \
"v_mfma_f32_16x16x32_bf16 v[116:119], v[44:47], v[204:207], v[116:119]\n" \
"global_load_lds_dwordx4 v226, s[48:49] offset:-2048\n" \
"v_cvt_pk_bf16_f32 v236, v236, v237\n" \
"v_mfma_f32_16x16x32_bf16 v[120:123], v[48:51], v[200:203], v[120:123]\n" \
"ds_read_b64_tr_b16 v[14:15], v233 offset:10240\n" \
"v_accvgpr_write_b32 a66, v234\n" \
"v_mfma_f32_16x16x32_bf16 v[120:123], v[52:55], v[204:207], v[120:123]\n" \
"ds_read_b64_tr_b16 v[16:17], v232 offset:512\n" \
"v_accvgpr_write_b32 a67, v236\n" \
"v_mfma_f32_16x16x32_bf16 v[124:127], v[56:59], v[200:203], v[124:127]\n" \
"ds_read_b64_tr_b16 v[18:19], v232 offset:2560\n" \
"v_accvgpr_read_b32 v234, a96\n" \
"v_mfma_f32_16x16x32_bf16 v[124:127], v[60:63], v[204:207], v[124:127]\n" \
"global_load_lds_dwordx4 v227, s[48:49] offset:-1024\n" \
"v_accvgpr_read_b32 v235, a97\n" \
"v_mfma_f32_16x16x32_bf16 v[144:147], v[32:35], v[208:211], v[144:147]\n" \
"s_add_u32 s48, s48, 0x80\n" \
"s_addc_u32 s49, s49, 0\n" \
"ds_read_b128 v[200:203], v230 offset:2048\n" \
"v_mfma_f32_16x16x32_bf16 v[144:147], v[36:39], v[212:215], v[144:147]\n" \
"ds_read_b128 v[204:207], v231 offset:2048\n" \
"v_accvgpr_read_b32 v236, a98\n" \
"v_mfma_f32_16x16x32_bf16 v[148:151], v[40:43], v[208:211], v[148:151]\n" \
"ds_read_b64_tr_b16 v[20:21], v232 offset:8704\n" \
"v_accvgpr_read_b32 v237, a99\n" \
"v_mfma_f32_16x16x32_bf16 v[148:151], v[44:47], v[212:215], v[148:151]\n" \
"global_load_lds_dwordx4 v224, s[46:47] offset:0\n" \
"v_mul_f32 v234, s92, v234\n" \
"v_mfma_f32_16x16x32_bf16 v[152:155], v[48:51], v[208:211], v[152:155]\n" \
"ds_read_b64_tr_b16 v[22:23], v232 offset:10752\n" \
"v_mul_f32 v235, s92, v235\n" \
"v_mfma_f32_16x16x32_bf16 v[152:155], v[52:55], v[212:215], v[152:155]\n" \
"ds_read_b64_tr_b16 v[24:25], v233 offset:512\n" \
"v_mul_f32 v236, s92, v236\n" \
"v_mfma_f32_16x16x32_bf16 v[156:159], v[56:59], v[208:211], v[156:159]\n" \
"ds_read_b64_tr_b16 v[26:27], v233 offset:2560\n" \
"v_mul_f32 v237, s92, v237\n" \
"v_mfma_f32_16x16x32_bf16 v[156:159], v[60:63], v[212:215], v[156:159]\n" \
"global_load_lds_dwordx4 v225, s[46:47] offset:1024\n" \
"v_cvt_pk_bf16_f32 v234, v234, v235\n" \
"v_mfma_f32_16x16x32_bf16 v[176:179], v[32:35], v[216:219], v[176:179]\n" \
"ds_read_b128 v[208:211], v230 offset:4096\n" \
"v_cvt_pk_bf16_f32 v236, v236, v237\n" \
"v_mfma_f32_16x16x32_bf16 v[176:179], v[36:39], v[220:223], v[176:179]\n" \
"ds_read_b128 v[212:215], v231 offset:4096\n" \
"v_accvgpr_write_b32 a96, v234\n" \
"v_mfma_f32_16x16x32_bf16 v[180:183], v[40:43], v[216:219], v[180:183]\n" \
"ds_read_b64_tr_b16 v[28:29], v233 offset:8704\n" \
"v_accvgpr_write_b32 a97, v236\n" \
"v_mfma_f32_16x16x32_bf16 v[180:183], v[44:47], v[220:223], v[180:183]\n" \
"global_load_lds_dwordx4 v226, s[46:47] offset:2048\n" \
"v_accvgpr_read_b32 v234, a100\n" \
"v_mfma_f32_16x16x32_bf16 v[184:187], v[48:51], v[216:219], v[184:187]\n" \
"ds_read_b64_tr_b16 v[30:31], v233 offset:10752\n" \
"v_accvgpr_read_b32 v235, a101\n" \
"v_mfma_f32_16x16x32_bf16 v[184:187], v[52:55], v[220:223], v[184:187]\n" \
"v_accvgpr_read_b32 v236, a102\n" \
"v_accvgpr_read_b32 v237, a103\n" \
"v_mfma_f32_16x16x32_bf16 v[188:191], v[56:59], v[216:219], v[188:191]\n" \
"v_mul_f32 v234, s92, v234\n" \
"v_mul_f32 v235, s92, v235\n" \
"v_mfma_f32_16x16x32_bf16 v[188:191], v[60:63], v[220:223], v[188:191]\n" \
"s_waitcnt lgkmcnt(1)\n" \
"global_load_lds_dwordx4 v227, s[46:47] offset:3072\n" \
"v_mul_f32 v236, s92, v236\n" \
"v_mfma_f32_16x16x32_bf16 v[64:67], v[0:3], v[192:195], v[64:67]\n" \
"s_add_u32 s46, s46, 0x80\n" \
"s_addc_u32 s47, s47, 0\n" \
"ds_read_b128 v[216:219], v230 offset:6144\n" \
"v_mfma_f32_16x16x32_bf16 v[64:67], v[4:7], v[196:199], v[64:67]\n" \
"ds_read_b128 v[220:223], v231 offset:6144\n" \
"v_mul_f32 v237, s92, v237\n" \
"v_mfma_f32_16x16x32_bf16 v[68:71], v[8:11], v[192:195], v[68:71]\n" \
"ds_read_b64_tr_b16 v[32:33], v232 offset:1024\n" \
"v_cvt_pk_bf16_f32 v234, v234, v235\n" \
"v_mfma_f32_16x16x32_bf16 v[68:71], v[12:15], v[196:199], v[68:71]\n" \
"buffer_store_dwordx4 a[216:219], v248, s[56:59], s90 offen offset:2048 sc1\n" \
"v_cvt_pk_bf16_f32 v236, v236, v237\n" \
"v_accvgpr_write_b32 a98, v234\n" \
"v_accvgpr_write_b32 a99, v236\n" \
"v_accvgpr_read_b32 v234, a72\n" \
"v_mfma_f32_16x16x32_bf16 v[72:75], v[16:19], v[192:195], v[72:75]\n" \
"ds_read_b64_tr_b16 v[34:35], v232 offset:3072\n" \
"v_accvgpr_read_b32 v235, a73\n" \
"v_mfma_f32_16x16x32_bf16 v[72:75], v[20:23], v[196:199], v[72:75]\n" \
"s_waitcnt lgkmcnt(3)\n" \
"ds_read_b64_tr_b16 v[36:37], v232 offset:9216\n" \
"v_accvgpr_read_b32 v236, a74\n" \
"v_mfma_f32_16x16x32_bf16 v[76:79], v[24:27], v[192:195], v[76:79]\n" \
"ds_read_b64_tr_b16 v[38:39], v232 offset:11264\n" \
"v_accvgpr_read_b32 v237, a75\n" \
"v_mfma_f32_16x16x32_bf16 v[76:79], v[28:31], v[196:199], v[76:79]\n" \
"v_mul_f32 v234, s92, v234\n" \
"v_mul_f32 v235, s92, v235\n" \
"v_mfma_f32_16x16x32_bf16 v[96:99], v[0:3], v[200:203], v[96:99]\n" \
"ds_read_b64_tr_b16 v[40:41], v233 offset:1024\n" \
"v_mul_f32 v236, s92, v236\n" \
"v_mfma_f32_16x16x32_bf16 v[96:99], v[4:7], v[204:207], v[96:99]\n" \
"ds_read_b64_tr_b16 v[42:43], v233 offset:3072\n" \
"v_mul_f32 v237, s92, v237\n" \
"v_mfma_f32_16x16x32_bf16 v[100:103], v[8:11], v[200:203], v[100:103]\n" \
"ds_read_b64_tr_b16 v[44:45], v233 offset:9216\n" \
"v_cvt_pk_bf16_f32 v234, v234, v235\n" \
"v_mfma_f32_16x16x32_bf16 v[100:103], v[12:15], v[204:207], v[100:103]\n" \
"v_cvt_pk_bf16_f32 v236, v236, v237\n" \
"v_accvgpr_write_b32 a72, v234\n" \
"v_mfma_f32_16x16x32_bf16 v[104:107], v[16:19], v[200:203], v[104:107]\n" \
"ds_read_b64_tr_b16 v[46:47], v233 offset:11264\n" \
"v_accvgpr_write_b32 a73, v236\n" \
"v_mfma_f32_16x16x32_bf16 v[104:107], v[20:23], v[204:207], v[104:107]\n" \
"ds_read_b64_tr_b16 v[48:49], v232 offset:1536\n" \
"v_accvgpr_read_b32 v234, a76\n" \
"v_mfma_f32_16x16x32_bf16 v[108:111], v[24:27], v[200:203], v[108:111]\n" \
"s_add_u32 m0, s95, 53248\n" \
"ds_read_b64_tr_b16 v[50:51], v232 offset:3584\n" \
"v_accvgpr_read_b32 v235, a77\n" \
"v_mfma_f32_16x16x32_bf16 v[108:111], v[28:31], v[204:207], v[108:111]\n" \
"global_load_lds_dwordx4 v228, s[44:45] offset:-4096\n" \
"v_accvgpr_read_b32 v236, a78\n" \
"v_mfma_f32_16x16x32_bf16 v[128:131], v[0:3], v[208:211], v[128:131]\n" \
"ds_read_b64_tr_b16 v[52:53], v232 offset:9728\n" \
"v_accvgpr_read_b32 v237, a79\n" \
"v_mfma_f32_16x16x32_bf16 v[128:131], v[4:7], v[212:215], v[128:131]\n" \
"ds_read_b64_tr_b16 v[54:55], v232 offset:11776\n" \
"v_mul_f32 v234, s92, v234\n" \
"v_mfma_f32_16x16x32_bf16 v[132:135], v[8:11], v[208:211], v[132:135]\n" \
"ds_read_b64_tr_b16 v[56:57], v233 offset:1536\n" \
"v_mul_f32 v235, s92, v235\n" \
"v_mfma_f32_16x16x32_bf16 v[132:135], v[12:15], v[212:215], v[132:135]\n" \
"global_load_lds_dwordx4 v229, s[44:45] offset:-3072\n" \
"v_mul_f32 v236, s92, v236\n" \
"v_mfma_f32_16x16x32_bf16 v[136:139], v[16:19], v[208:211], v[136:139]\n" \
"s_add_u32 s44, s44, 0x80000\n" \
"s_addc_u32 s45, s45, 0\n" \
"ds_read_b64_tr_b16 v[58:59], v233 offset:3584\n" \
"v_mfma_f32_16x16x32_bf16 v[136:139], v[20:23], v[212:215], v[136:139]\n" \
"ds_read_b64_tr_b16 v[60:61], v233 offset:9728\n" \
"v_mul_f32 v237, s92, v237\n" \
"v_mfma_f32_16x16x32_bf16 v[140:143], v[24:27], v[208:211], v[140:143]\n" \
"ds_read_b64_tr_b16 v[62:63], v233 offset:11776\n" \
"v_cvt_pk_bf16_f32 v234, v234, v235\n" \
"v_mfma_f32_16x16x32_bf16 v[140:143], v[28:31], v[212:215], v[140:143]\n" \
"s_waitcnt lgkmcnt(3)\n" \
"global_load_lds_dwordx4 v228, s[42:43] offset:-2048\n" \
"v_cvt_pk_bf16_f32 v236, v236, v237\n" \
"v_mfma_f32_16x16x32_bf16 v[160:163], v[0:3], v[216:219], v[160:163]\n" \
"v_add_u32 v230, 0x8000, v230\n" \
"v_add_u32 v231, 0x8000, v231\n" \
"v_mfma_f32_16x16x32_bf16 v[160:163], v[4:7], v[220:223], v[160:163]\n" \
"v_add_u32 v232, 0x4000, v232\n" \
"v_add_u32 v233, 0x4000, v233\n" \
"v_mfma_f32_16x16x32_bf16 v[164:167], v[8:11], v[216:219], v[164:167]\n" \
"v_accvgpr_write_b32 a74, v234\n" \
"v_accvgpr_write_b32 a75, v236\n" \
"v_mfma_f32_16x16x32_bf16 v[164:167], v[12:15], v[220:223], v[164:167]\n" \
"global_load_lds_dwordx4 v229, s[42:43] offset:-1024\n" \
"v_accvgpr_read_b32 v234, a104\n" \
"v_mfma_f32_16x16x32_bf16 v[168:171], v[16:19], v[216:219], v[168:171]\n" \
"s_add_u32 s42, s42, 0x80000\n" \
"s_addc_u32 s43, s43, 0\n" \
"v_accvgpr_read_b32 v235, a105\n" \
"v_mfma_f32_16x16x32_bf16 v[168:171], v[20:23], v[220:223], v[168:171]\n" \
"v_accvgpr_read_b32 v236, a106\n" \
"v_accvgpr_read_b32 v237, a107\n" \
"v_mfma_f32_16x16x32_bf16 v[172:175], v[24:27], v[216:219], v[172:175]\n" \
"s_waitcnt vmcnt(18) lgkmcnt(0)\n" \
"v_mul_f32 v234, s92, v234\n" \
"v_mul_f32 v235, s92, v235\n" \
"v_mfma_f32_16x16x32_bf16 v[172:175], v[28:31], v[220:223], v[172:175]\n" \
"s_barrier\n" \
"v_mfma_f32_16x16x32_bf16 v[80:83], v[32:35], v[192:195], v[80:83]\n" \
"ds_read_b64_tr_b16 v[0:1], v232 offset:0\n" \
"v_mul_f32 v236, s92, v236\n" \
"v_mfma_f32_16x16x32_bf16 v[80:83], v[36:39], v[196:199], v[80:83]\n" \
"ds_read_b64_tr_b16 v[2:3], v232 offset:2048\n" \
"v_mul_f32 v237, s92, v237\n" \
"v_mfma_f32_16x16x32_bf16 v[84:87], v[40:43], v[192:195], v[84:87]\n" \
"s_add_u32 m0, s87, 69632\n" \
"ds_read_b64_tr_b16 v[4:5], v232 offset:8192\n" \
"v_cvt_pk_bf16_f32 v234, v234, v235\n" \
"v_mfma_f32_16x16x32_bf16 v[84:87], v[44:47], v[196:199], v[84:87]\n" \
"global_load_lds_dwordx4 v224, s[48:49] offset:-4096\n" \
"v_cvt_pk_bf16_f32 v236, v236, v237\n" \
"v_mfma_f32_16x16x32_bf16 v[88:91], v[48:51], v[192:195], v[88:91]\n" \
"ds_read_b64_tr_b16 v[6:7], v232 offset:10240\n" \
"v_accvgpr_write_b32 a104, v234\n" \
"v_mfma_f32_16x16x32_bf16 v[88:91], v[52:55], v[196:199], v[88:91]\n" \
"ds_read_b64_tr_b16 v[8:9], v233 offset:0\n" \
"v_accvgpr_write_b32 a105, v236\n" \
"v_mfma_f32_16x16x32_bf16 v[92:95], v[56:59], v[192:195], v[92:95]\n" \
"ds_read_b64_tr_b16 v[10:11], v233 offset:2048\n" \
"v_accvgpr_read_b32 v234, a108\n" \
"v_mfma_f32_16x16x32_bf16 v[92:95], v[60:63], v[196:199], v[92:95]\n" \
"global_load_lds_dwordx4 v225, s[48:49] offset:-3072\n" \
"v_accvgpr_read_b32 v235, a109\n" \
"v_mfma_f32_16x16x32_bf16 v[112:115], v[32:35], v[200:203], v[112:115]\n" \
"ds_read_b128 v[192:195], v230 offset:0\n" \
"v_accvgpr_read_b32 v236, a110\n" \
"v_mfma_f32_16x16x32_bf16 v[112:115], v[36:39], v[204:207], v[112:115]\n" \
"ds_read_b128 v[196:199], v231 offset:0\n" \
"v_accvgpr_read_b32 v237, a111\n" \
"v_mfma_f32_16x16x32_bf16 v[116:119], v[40:43], v[200:203], v[116:119]\n" \
"ds_read_b64_tr_b16 v[12:13], v233 offset:8192\n" \
"v_mul_f32 v234, s92, v234\n" \
"v_mfma_f32_16x16x32_bf16 v[116:119], v[44:47], v[204:207], v[116:119]\n" \
"global_load_lds_dwordx4 v226, s[48:49] offset:-2048\n" \
"v_mul_f32 v235, s92, v235\n" \
"v_mfma_f32_16x16x32_bf16 v[120:123], v[48:51], v[200:203], v[120:123]\n" \
"ds_read_b64_tr_b16 v[14:15], v233 offset:10240\n" \
"v_mul_f32 v236, s92, v236\n" \
"v_mfma_f32_16x16x32_bf16 v[120:123], v[52:55], v[204:207], v[120:123]\n" \
"ds_read_b64_tr_b16 v[16:17], v232 offset:512\n" \
"v_mul_f32 v237, s92, v237\n" \
"v_mfma_f32_16x16x32_bf16 v[124:127], v[56:59], v[200:203], v[124:127]\n" \
"ds_read_b64_tr_b16 v[18:19], v232 offset:2560\n" \
"v_cvt_pk_bf16_f32 v234, v234, v235\n" \
"v_mfma_f32_16x16x32_bf16 v[124:127], v[60:63], v[204:207], v[124:127]\n" \
"global_load_lds_dwordx4 v227, s[48:49] offset:-1024\n" \
"v_cvt_pk_bf16_f32 v236, v236, v237\n" \
"v_mfma_f32_16x16x32_bf16 v[144:147], v[32:35], v[208:211], v[144:147]\n" \
"s_add_u32 s48, s48, 0x80\n" \
"s_addc_u32 s49, s49, 0\n" \
"ds_read_b128 v[200:203], v230 offset:2048\n" \
"v_mfma_f32_16x16x32_bf16 v[144:147], v[36:39], v[212:215], v[144:147]\n" \
"ds_read_b128 v[204:207], v231 offset:2048\n" \
"v_accvgpr_write_b32 a106, v234\n" \
"v_mfma_f32_16x16x32_bf16 v[148:151], v[40:43], v[208:211], v[148:151]\n" \
"ds_read_b64_tr_b16 v[20:21], v232 offset:8704\n" \
"v_accvgpr_write_b32 a107, v236\n" \
"v_mfma_f32_16x16x32_bf16 v[148:151], v[44:47], v[212:215], v[148:151]\n" \
"global_load_lds_dwordx4 v224, s[46:47] offset:0\n" \
"v_accvgpr_read_b32 v234, a80\n" \
"v_mfma_f32_16x16x32_bf16 v[152:155], v[48:51], v[208:211], v[152:155]\n" \
"ds_read_b64_tr_b16 v[22:23], v232 offset:10752\n" \
"v_accvgpr_read_b32 v235, a81\n" \
"v_mfma_f32_16x16x32_bf16 v[152:155], v[52:55], v[212:215], v[152:155]\n" \
"ds_read_b64_tr_b16 v[24:25], v233 offset:512\n" \
"v_accvgpr_read_b32 v236, a82\n" \
"v_mfma_f32_16x16x32_bf16 v[156:159], v[56:59], v[208:211], v[156:159]\n" \
"ds_read_b64_tr_b16 v[26:27], v233 offset:2560\n" \
"v_accvgpr_read_b32 v237, a83\n" \
"v_mfma_f32_16x16x32_bf16 v[156:159], v[60:63], v[212:215], v[156:159]\n" \
"global_load_lds_dwordx4 v225, s[46:47] offset:1024\n" \
"v_cvt_pk_bf16_f32 v234, v234, v235\n" \
"v_mfma_f32_16x16x32_bf16 v[176:179], v[32:35], v[216:219], v[176:179]\n" \
"ds_read_b128 v[208:211], v230 offset:4096\n" \
"v_cvt_pk_bf16_f32 v236, v236, v237\n" \
"v_mfma_f32_16x16x32_bf16 v[176:179], v[36:39], v[220:223], v[176:179]\n" \
"ds_read_b128 v[212:215], v231 offset:4096\n" \
"v_accvgpr_write_b32 a80, v234\n" \
"v_mfma_f32_16x16x32_bf16 v[180:183], v[40:43], v[216:219], v[180:183]\n" \
"ds_read_b64_tr_b16 v[28:29], v233 offset:8704\n" \
"v_accvgpr_write_b32 a81, v236\n" \
"v_mfma_f32_16x16x32_bf16 v[180:183], v[44:47], v[220:223], v[180:183]\n" \
"global_load_lds_dwordx4 v226, s[46:47] offset:2048\n" \
"v_accvgpr_read_b32 v234, a112\n" \
"v_mfma_f32_16x16x32_bf16 v[184:187], v[48:51], v[216:219], v[184:187]\n" \
"ds_read_b64_tr_b16 v[30:31], v233 offset:10752\n" \
"v_accvgpr_read_b32 v235, a113\n" \
"v_mfma_f32_16x16x32_bf16 v[184:187], v[52:55], v[220:223], v[184:187]\n" \
"v_accvgpr_read_b32 v236, a114\n" \
"v_accvgpr_read_b32 v237, a115\n" \
"v_mfma_f32_16x16x32_bf16 v[188:191], v[56:59], v[216:219], v[188:191]\n" \
"v_cvt_pk_bf16_f32 v234, v234, v235\n" \
"v_cvt_pk_bf16_f32 v236, v236, v237\n" \
"v_mfma_f32_16x16x32_bf16 v[188:191], v[60:63], v[220:223], v[188:191]\n" \
"s_waitcnt lgkmcnt(1)\n" \
"global_load_lds_dwordx4 v227, s[46:47] offset:3072\n" \
"v_accvgpr_write_b32 a82, v234\n" \
"v_mfma_f32_16x16x32_bf16 v[64:67], v[0:3], v[192:195], v[64:67]\n" \
"s_add_u32 s46, s46, 0x80\n" \
"s_addc_u32 s47, s47, 0\n" \
"ds_read_b128 v[216:219], v230 offset:6144\n" \
"v_mfma_f32_16x16x32_bf16 v[64:67], v[4:7], v[196:199], v[64:67]\n" \
"ds_read_b128 v[220:223], v231 offset:6144\n" \
"v_accvgpr_write_b32 a83, v236\n" \
"v_mfma_f32_16x16x32_bf16 v[68:71], v[8:11], v[192:195], v[68:71]\n" \
"ds_read_b64_tr_b16 v[32:33], v232 offset:1024\n" \
"v_accvgpr_read_b32 v234, a84\n" \
"v_mfma_f32_16x16x32_bf16 v[68:71], v[12:15], v[196:199], v[68:71]\n" \
"buffer_store_dwordx4 a[220:223], v248, s[56:59], s90 offen offset:3072 sc1\n" \
"v_accvgpr_read_b32 v235, a85\n" \
"v_accvgpr_read_b32 v236, a86\n" \
"v_accvgpr_read_b32 v237, a87\n" \
"v_cvt_pk_bf16_f32 v234, v234, v235\n" \
"v_mfma_f32_16x16x32_bf16 v[72:75], v[16:19], v[192:195], v[72:75]\n" \
"s_add_u32 s90, s88, 139264\n" \
"ds_read_b64_tr_b16 v[34:35], v232 offset:3072\n" \
"v_cvt_pk_bf16_f32 v236, v236, v237\n" \
"v_mfma_f32_16x16x32_bf16 v[72:75], v[20:23], v[196:199], v[72:75]\n" \
"s_waitcnt lgkmcnt(3)\n" \
"ds_read_b64_tr_b16 v[36:37], v232 offset:9216\n" \
"v_accvgpr_write_b32 a84, v234\n" \
"v_mfma_f32_16x16x32_bf16 v[76:79], v[24:27], v[192:195], v[76:79]\n" \
"ds_read_b64_tr_b16 v[38:39], v232 offset:11264\n" \
"v_accvgpr_write_b32 a85, v236\n" \
"v_mfma_f32_16x16x32_bf16 v[76:79], v[28:31], v[196:199], v[76:79]\n" \
"buffer_store_dwordx4 a[128:131], v249, s[56:59], s90 offen offset:0 sc1\n" \
"v_accvgpr_read_b32 v234, a116\n" \
"v_accvgpr_read_b32 v235, a117\n" \
"v_accvgpr_read_b32 v236, a118\n" \
"v_accvgpr_read_b32 v237, a119\n" \
"v_mfma_f32_16x16x32_bf16 v[96:99], v[0:3], v[200:203], v[96:99]\n" \
"ds_read_b64_tr_b16 v[40:41], v233 offset:1024\n" \
"v_cvt_pk_bf16_f32 v234, v234, v235\n" \
"v_mfma_f32_16x16x32_bf16 v[96:99], v[4:7], v[204:207], v[96:99]\n" \
"ds_read_b64_tr_b16 v[42:43], v233 offset:3072\n" \
"v_cvt_pk_bf16_f32 v236, v236, v237\n" \
"v_mfma_f32_16x16x32_bf16 v[100:103], v[8:11], v[200:203], v[100:103]\n" \
"ds_read_b64_tr_b16 v[44:45], v233 offset:9216\n" \
"v_accvgpr_write_b32 a86, v234\n" \
"v_mfma_f32_16x16x32_bf16 v[100:103], v[12:15], v[204:207], v[100:103]\n" \
"v_accvgpr_write_b32 a87, v236\n" \
"v_accvgpr_read_b32 v234, a88\n" \
"v_mfma_f32_16x16x32_bf16 v[104:107], v[16:19], v[200:203], v[104:107]\n" \
"ds_read_b64_tr_b16 v[46:47], v233 offset:11264\n" \
"v_accvgpr_read_b32 v235, a89\n" \
"v_mfma_f32_16x16x32_bf16 v[104:107], v[20:23], v[204:207], v[104:107]\n" \
"ds_read_b64_tr_b16 v[48:49], v232 offset:1536\n" \
"v_accvgpr_read_b32 v236, a90\n" \
"v_mfma_f32_16x16x32_bf16 v[108:111], v[24:27], v[200:203], v[108:111]\n" \
"s_add_u32 m0, s95, 4096\n" \
"ds_read_b64_tr_b16 v[50:51], v232 offset:3584\n" \
"v_accvgpr_read_b32 v237, a91\n" \
"v_mfma_f32_16x16x32_bf16 v[108:111], v[28:31], v[204:207], v[108:111]\n" \
"global_load_lds_dwordx4 v228, s[44:45] offset:-4096\n" \
"v_cvt_pk_bf16_f32 v234, v234, v235\n" \
"v_mfma_f32_16x16x32_bf16 v[128:131], v[0:3], v[208:211], v[128:131]\n" \
"ds_read_b64_tr_b16 v[52:53], v232 offset:9728\n" \
"v_cvt_pk_bf16_f32 v236, v236, v237\n" \
"v_mfma_f32_16x16x32_bf16 v[128:131], v[4:7], v[212:215], v[128:131]\n" \
"ds_read_b64_tr_b16 v[54:55], v232 offset:11776\n" \
"v_accvgpr_write_b32 a88, v234\n" \
"v_mfma_f32_16x16x32_bf16 v[132:135], v[8:11], v[208:211], v[132:135]\n" \
"ds_read_b64_tr_b16 v[56:57], v233 offset:1536\n" \
"v_accvgpr_write_b32 a89, v236\n" \
"v_mfma_f32_16x16x32_bf16 v[132:135], v[12:15], v[212:215], v[132:135]\n" \
"global_load_lds_dwordx4 v229, s[44:45] offset:-3072\n" \
"v_accvgpr_read_b32 v234, a120\n" \
"v_mfma_f32_16x16x32_bf16 v[136:139], v[16:19], v[208:211], v[136:139]\n" \
"s_add_u32 s44, s44, 0x80000\n" \
"s_addc_u32 s45, s45, 0\n" \
"ds_read_b64_tr_b16 v[58:59], v233 offset:3584\n" \
"v_mfma_f32_16x16x32_bf16 v[136:139], v[20:23], v[212:215], v[136:139]\n" \
"ds_read_b64_tr_b16 v[60:61], v233 offset:9728\n" \
"v_accvgpr_read_b32 v235, a121\n" \
"v_mfma_f32_16x16x32_bf16 v[140:143], v[24:27], v[208:211], v[140:143]\n" \
"ds_read_b64_tr_b16 v[62:63], v233 offset:11776\n" \
"v_accvgpr_read_b32 v236, a122\n" \
"v_mfma_f32_16x16x32_bf16 v[140:143], v[28:31], v[212:215], v[140:143]\n" \
"s_waitcnt lgkmcnt(3)\n" \
"global_load_lds_dwordx4 v228, s[42:43] offset:-2048\n" \
"v_accvgpr_read_b32 v237, a123\n" \
"v_mfma_f32_16x16x32_bf16 v[160:163], v[0:3], v[216:219], v[160:163]\n" \
"v_add_u32 v230, 0x8000, v230\n" \
"v_add_u32 v231, 0x8000, v231\n" \
"v_mfma_f32_16x16x32_bf16 v[160:163], v[4:7], v[220:223], v[160:163]\n" \
"v_add_u32 v232, 0x4000, v232\n" \
"v_add_u32 v233, 0x4000, v233\n" \
"v_mfma_f32_16x16x32_bf16 v[164:167], v[8:11], v[216:219], v[164:167]\n" \
"v_cvt_pk_bf16_f32 v234, v234, v235\n" \
"v_cvt_pk_bf16_f32 v236, v236, v237\n" \
"v_mfma_f32_16x16x32_bf16 v[164:167], v[12:15], v[220:223], v[164:167]\n" \
"global_load_lds_dwordx4 v229, s[42:43] offset:-1024\n" \
"v_accvgpr_write_b32 a90, v234\n" \
"v_mfma_f32_16x16x32_bf16 v[168:171], v[16:19], v[216:219], v[168:171]\n" \
"s_add_u32 s42, s42, 0x80000\n" \
"s_addc_u32 s43, s43, 0\n" \
"v_accvgpr_write_b32 a91, v236\n" \
"v_mfma_f32_16x16x32_bf16 v[168:171], v[20:23], v[220:223], v[168:171]\n" \
"v_accvgpr_read_b32 v234, a92\n" \
"v_accvgpr_read_b32 v235, a93\n" \
"v_mfma_f32_16x16x32_bf16 v[172:175], v[24:27], v[216:219], v[172:175]\n" \
"s_waitcnt vmcnt(19) lgkmcnt(0)\n" \
"v_accvgpr_read_b32 v236, a94\n" \
"v_accvgpr_read_b32 v237, a95\n" \
"v_mfma_f32_16x16x32_bf16 v[172:175], v[28:31], v[220:223], v[172:175]\n" \
"s_barrier\n" \
"v_mfma_f32_16x16x32_bf16 v[80:83], v[32:35], v[192:195], v[80:83]\n" \
"ds_read_b64_tr_b16 v[0:1], v232 offset:0\n" \
"v_cvt_pk_bf16_f32 v234, v234, v235\n" \
"v_mfma_f32_16x16x32_bf16 v[80:83], v[36:39], v[196:199], v[80:83]\n" \
"ds_read_b64_tr_b16 v[2:3], v232 offset:2048\n" \
"v_cvt_pk_bf16_f32 v236, v236, v237\n" \
"v_mfma_f32_16x16x32_bf16 v[84:87], v[40:43], v[192:195], v[84:87]\n" \
"s_add_u32 m0, s87, 102400\n" \
"ds_read_b64_tr_b16 v[4:5], v232 offset:8192\n" \
"v_accvgpr_write_b32 a92, v234\n" \
"v_mfma_f32_16x16x32_bf16 v[84:87], v[44:47], v[196:199], v[84:87]\n" \
"global_load_lds_dwordx4 v224, s[48:49] offset:-4096\n" \
"v_accvgpr_write_b32 a93, v236\n" \
"v_mfma_f32_16x16x32_bf16 v[88:91], v[48:51], v[192:195], v[88:91]\n" \
"ds_read_b64_tr_b16 v[6:7], v232 offset:10240\n" \
"v_accvgpr_read_b32 v234, a124\n" \
"v_mfma_f32_16x16x32_bf16 v[88:91], v[52:55], v[196:199], v[88:91]\n" \
"ds_read_b64_tr_b16 v[8:9], v233 offset:0\n" \
"v_accvgpr_read_b32 v235, a125\n" \
"v_mfma_f32_16x16x32_bf16 v[92:95], v[56:59], v[192:195], v[92:95]\n" \
"ds_read_b64_tr_b16 v[10:11], v233 offset:2048\n" \
"v_accvgpr_read_b32 v236, a126\n" \
"v_mfma_f32_16x16x32_bf16 v[92:95], v[60:63], v[196:199], v[92:95]\n" \
"global_load_lds_dwordx4 v225, s[48:49] offset:-3072\n" \
"Lpg3267_%=:\n" \
"s_cbranch_execz Lpg5359_%=\n" \
"v_accvgpr_read_b32 v237, a127\n" \
"v_mfma_f32_16x16x32_bf16 v[112:115], v[32:35], v[200:203], v[112:115]\n" \
"ds_read_b128 v[192:195], v230 offset:0\n" \
"v_cvt_pk_bf16_f32 v234, v234, v235\n" \
"v_mfma_f32_16x16x32_bf16 v[112:115], v[36:39], v[204:207], v[112:115]\n" \
"ds_read_b128 v[196:199], v231 offset:0\n" \
"v_cvt_pk_bf16_f32 v236, v236, v237\n" \
"v_mfma_f32_16x16x32_bf16 v[116:119], v[40:43], v[200:203], v[116:119]\n" \
"ds_read_b64_tr_b16 v[12:13], v233 offset:8192\n" \
"v_accvgpr_write_b32 a94, v234\n" \
"v_mfma_f32_16x16x32_bf16 v[116:119], v[44:47], v[204:207], v[116:119]\n" \
"global_load_lds_dwordx4 v226, s[48:49] offset:-2048\n" \
"v_accvgpr_write_b32 a95, v236\n" \
"v_mfma_f32_16x16x32_bf16 v[120:123], v[48:51], v[200:203], v[120:123]\n" \
"ds_read_b64_tr_b16 v[14:15], v233 offset:10240\n" \
"v_accvgpr_read_b32 v234, a0\n" \
"v_mfma_f32_16x16x32_bf16 v[120:123], v[52:55], v[204:207], v[120:123]\n" \
"ds_read_b64_tr_b16 v[16:17], v232 offset:512\n" \
"v_accvgpr_read_b32 v235, a1\n" \
"v_mfma_f32_16x16x32_bf16 v[124:127], v[56:59], v[200:203], v[124:127]\n" \
"ds_read_b64_tr_b16 v[18:19], v232 offset:2560\n" \
"v_accvgpr_read_b32 v236, a2\n" \
"v_mfma_f32_16x16x32_bf16 v[124:127], v[60:63], v[204:207], v[124:127]\n" \
"global_load_lds_dwordx4 v227, s[48:49] offset:-1024\n" \
"v_accvgpr_read_b32 v237, a3\n" \
"v_mfma_f32_16x16x32_bf16 v[144:147], v[32:35], v[208:211], v[144:147]\n" \
"s_add_u32 s48, s48, 0x80\n" \
"s_addc_u32 s49, s49, 0\n" \
"ds_read_b128 v[200:203], v230 offset:2048\n" \
"v_mfma_f32_16x16x32_bf16 v[144:147], v[36:39], v[212:215], v[144:147]\n" \
"ds_read_b128 v[204:207], v231 offset:2048\n" \
"v_mul_f32 v234, s92, v234\n" \
"v_mfma_f32_16x16x32_bf16 v[148:151], v[40:43], v[208:211], v[148:151]\n" \
"ds_read_b64_tr_b16 v[20:21], v232 offset:8704\n" \
"v_mul_f32 v235, s92, v235\n" \
"v_mfma_f32_16x16x32_bf16 v[148:151], v[44:47], v[212:215], v[148:151]\n" \
"global_load_lds_dwordx4 v224, s[46:47] offset:0\n" \
"v_mul_f32 v236, s92, v236\n" \
"v_mfma_f32_16x16x32_bf16 v[152:155], v[48:51], v[208:211], v[152:155]\n" \
"ds_read_b64_tr_b16 v[22:23], v232 offset:10752\n" \
"v_mul_f32 v237, s92, v237\n" \
"v_mfma_f32_16x16x32_bf16 v[152:155], v[52:55], v[212:215], v[152:155]\n" \
"ds_read_b64_tr_b16 v[24:25], v233 offset:512\n" \
"v_cvt_pk_bf16_f32 v234, v234, v235\n" \
"v_mfma_f32_16x16x32_bf16 v[156:159], v[56:59], v[208:211], v[156:159]\n" \
"ds_read_b64_tr_b16 v[26:27], v233 offset:2560\n" \
"v_cvt_pk_bf16_f32 v236, v236, v237\n" \
"v_mfma_f32_16x16x32_bf16 v[156:159], v[60:63], v[212:215], v[156:159]\n" \
"global_load_lds_dwordx4 v225, s[46:47] offset:1024\n" \
"v_accvgpr_write_b32 a0, v234\n" \
"v_mfma_f32_16x16x32_bf16 v[176:179], v[32:35], v[216:219], v[176:179]\n" \
"ds_read_b128 v[208:211], v230 offset:4096\n" \
"v_accvgpr_write_b32 a1, v236\n" \
"v_mfma_f32_16x16x32_bf16 v[176:179], v[36:39], v[220:223], v[176:179]\n" \
"ds_read_b128 v[212:215], v231 offset:4096\n" \
"v_accvgpr_read_b32 v234, a4\n" \
"v_mfma_f32_16x16x32_bf16 v[180:183], v[40:43], v[216:219], v[180:183]\n" \
"ds_read_b64_tr_b16 v[28:29], v233 offset:8704\n" \
"v_accvgpr_read_b32 v235, a5\n" \
"v_mfma_f32_16x16x32_bf16 v[180:183], v[44:47], v[220:223], v[180:183]\n" \
"global_load_lds_dwordx4 v226, s[46:47] offset:2048\n" \
"v_accvgpr_read_b32 v236, a6\n" \
"v_mfma_f32_16x16x32_bf16 v[184:187], v[48:51], v[216:219], v[184:187]\n" \
"ds_read_b64_tr_b16 v[30:31], v233 offset:10752\n" \
"v_accvgpr_read_b32 v237, a7\n" \
"v_mfma_f32_16x16x32_bf16 v[184:187], v[52:55], v[220:223], v[184:187]\n" \
"v_mul_f32 v234, s92, v234\n" \
"v_mul_f32 v235, s92, v235\n" \
"v_mfma_f32_16x16x32_bf16 v[188:191], v[56:59], v[216:219], v[188:191]\n" \
"v_mul_f32 v236, s92, v236\n" \
"v_mul_f32 v237, s92, v237\n" \
"v_mfma_f32_16x16x32_bf16 v[188:191], v[60:63], v[220:223], v[188:191]\n" \
"s_waitcnt lgkmcnt(1)\n" \
"global_load_lds_dwordx4 v227, s[46:47] offset:3072\n" \
"v_cvt_pk_bf16_f32 v234, v234, v235\n" \
"v_mfma_f32_16x16x32_bf16 v[64:67], v[0:3], v[192:195], v[64:67]\n" \
"s_add_u32 s46, s46, 0x80\n" \
"s_addc_u32 s47, s47, 0\n" \
"ds_read_b128 v[216:219], v230 offset:6144\n" \
"v_mfma_f32_16x16x32_bf16 v[64:67], v[4:7], v[196:199], v[64:67]\n" \
"ds_read_b128 v[220:223], v231 offset:6144\n" \
"v_cvt_pk_bf16_f32 v236, v236, v237\n" \
"v_mfma_f32_16x16x32_bf16 v[68:71], v[8:11], v[192:195], v[68:71]\n" \
"ds_read_b64_tr_b16 v[32:33], v232 offset:1024\n" \
"v_accvgpr_write_b32 a2, v234\n" \
"v_mfma_f32_16x16x32_bf16 v[68:71], v[12:15], v[196:199], v[68:71]\n" \
"buffer_store_dwordx4 a[160:163], v249, s[56:59], s90 offen offset:256 sc1\n" \
"v_accvgpr_write_b32 a3, v236\n" \
"v_accvgpr_read_b32 v234, a32\n" \
"v_accvgpr_read_b32 v235, a33\n" \
"v_accvgpr_read_b32 v236, a34\n" \
"v_mfma_f32_16x16x32_bf16 v[72:75], v[16:19], v[192:195], v[72:75]\n" \
"ds_read_b64_tr_b16 v[34:35], v232 offset:3072\n" \
"v_accvgpr_read_b32 v237, a35\n" \
"v_mfma_f32_16x16x32_bf16 v[72:75], v[20:23], v[196:199], v[72:75]\n" \
"s_waitcnt lgkmcnt(3)\n" \
"ds_read_b64_tr_b16 v[36:37], v232 offset:9216\n" \
"v_mul_f32 v234, s92, v234\n" \
"v_mfma_f32_16x16x32_bf16 v[76:79], v[24:27], v[192:195], v[76:79]\n" \
"ds_read_b64_tr_b16 v[38:39], v232 offset:11264\n" \
"v_mul_f32 v235, s92, v235\n" \
"v_mfma_f32_16x16x32_bf16 v[76:79], v[28:31], v[196:199], v[76:79]\n" \
"v_mul_f32 v236, s92, v236\n" \
"v_mul_f32 v237, s92, v237\n" \
"v_mfma_f32_16x16x32_bf16 v[96:99], v[0:3], v[200:203], v[96:99]\n" \
"ds_read_b64_tr_b16 v[40:41], v233 offset:1024\n" \
"v_cvt_pk_bf16_f32 v234, v234, v235\n" \
"v_mfma_f32_16x16x32_bf16 v[96:99], v[4:7], v[204:207], v[96:99]\n" \
"ds_read_b64_tr_b16 v[42:43], v233 offset:3072\n" \
"v_cvt_pk_bf16_f32 v236, v236, v237\n" \
"v_mfma_f32_16x16x32_bf16 v[100:103], v[8:11], v[200:203], v[100:103]\n" \
"ds_read_b64_tr_b16 v[44:45], v233 offset:9216\n" \
"v_accvgpr_write_b32 a32, v234\n" \
"v_mfma_f32_16x16x32_bf16 v[100:103], v[12:15], v[204:207], v[100:103]\n" \
"v_accvgpr_write_b32 a33, v236\n" \
"v_accvgpr_read_b32 v234, a36\n" \
"v_mfma_f32_16x16x32_bf16 v[104:107], v[16:19], v[200:203], v[104:107]\n" \
"ds_read_b64_tr_b16 v[46:47], v233 offset:11264\n" \
"v_accvgpr_read_b32 v235, a37\n" \
"v_mfma_f32_16x16x32_bf16 v[104:107], v[20:23], v[204:207], v[104:107]\n" \
"ds_read_b64_tr_b16 v[48:49], v232 offset:1536\n" \
"v_accvgpr_read_b32 v236, a38\n" \
"v_mfma_f32_16x16x32_bf16 v[108:111], v[24:27], v[200:203], v[108:111]\n" \
"s_add_u32 m0, s95, 20480\n" \
"ds_read_b64_tr_b16 v[50:51], v232 offset:3584\n" \
"v_accvgpr_read_b32 v237, a39\n" \
"v_mfma_f32_16x16x32_bf16 v[108:111], v[28:31], v[204:207], v[108:111]\n" \
"global_load_lds_dwordx4 v228, s[44:45] offset:-4096\n" \
"v_mul_f32 v234, s92, v234\n" \
"v_mfma_f32_16x16x32_bf16 v[128:131], v[0:3], v[208:211], v[128:131]\n" \
"ds_read_b64_tr_b16 v[52:53], v232 offset:9728\n" \
"v_mul_f32 v235, s92, v235\n" \
"v_mfma_f32_16x16x32_bf16 v[128:131], v[4:7], v[212:215], v[128:131]\n" \
"ds_read_b64_tr_b16 v[54:55], v232 offset:11776\n" \
"v_mul_f32 v236, s92, v236\n" \
"v_mfma_f32_16x16x32_bf16 v[132:135], v[8:11], v[208:211], v[132:135]\n" \
"ds_read_b64_tr_b16 v[56:57], v233 offset:1536\n" \
"v_mul_f32 v237, s92, v237\n" \
"v_mfma_f32_16x16x32_bf16 v[132:135], v[12:15], v[212:215], v[132:135]\n" \
"global_load_lds_dwordx4 v229, s[44:45] offset:-3072\n" \
"v_cvt_pk_bf16_f32 v234, v234, v235\n" \
"v_mfma_f32_16x16x32_bf16 v[136:139], v[16:19], v[208:211], v[136:139]\n" \
"s_add_u32 s44, s44, 0x80000\n" \
"s_addc_u32 s45, s45, 0\n" \
"ds_read_b64_tr_b16 v[58:59], v233 offset:3584\n" \
"v_mfma_f32_16x16x32_bf16 v[136:139], v[20:23], v[212:215], v[136:139]\n" \
"ds_read_b64_tr_b16 v[60:61], v233 offset:9728\n" \
"v_cvt_pk_bf16_f32 v236, v236, v237\n" \
"v_mfma_f32_16x16x32_bf16 v[140:143], v[24:27], v[208:211], v[140:143]\n" \
"ds_read_b64_tr_b16 v[62:63], v233 offset:11776\n" \
"v_accvgpr_write_b32 a34, v234\n" \
"v_mfma_f32_16x16x32_bf16 v[140:143], v[28:31], v[212:215], v[140:143]\n" \
"s_waitcnt lgkmcnt(3)\n" \
"global_load_lds_dwordx4 v228, s[42:43] offset:-2048\n" \
"v_accvgpr_write_b32 a35, v236\n" \
"v_mfma_f32_16x16x32_bf16 v[160:163], v[0:3], v[216:219], v[160:163]\n" \
"v_add_u32 v230, 0xffff0000, v230\n" \
"v_add_u32 v231, 0xffff0000, v231\n" \
"v_mfma_f32_16x16x32_bf16 v[160:163], v[4:7], v[220:223], v[160:163]\n" \
"v_add_u32 v232, 0x4000, v232\n" \
"v_add_u32 v233, 0x4000, v233\n" \
"v_mfma_f32_16x16x32_bf16 v[164:167], v[8:11], v[216:219], v[164:167]\n" \
"v_accvgpr_read_b32 v234, a8\n" \
"v_accvgpr_read_b32 v235, a9\n" \
"v_mfma_f32_16x16x32_bf16 v[164:167], v[12:15], v[220:223], v[164:167]\n" \
"global_load_lds_dwordx4 v229, s[42:43] offset:-1024\n" \
"v_accvgpr_read_b32 v236, a10\n" \
"v_mfma_f32_16x16x32_bf16 v[168:171], v[16:19], v[216:219], v[168:171]\n" \
"s_add_u32 s42, s42, 0x80000\n" \
"s_addc_u32 s43, s43, 0\n" \
"v_accvgpr_read_b32 v237, a11\n" \
"v_mfma_f32_16x16x32_bf16 v[168:171], v[20:23], v[220:223], v[168:171]\n" \
"v_mul_f32 v234, s92, v234\n" \
"v_mul_f32 v235, s92, v235\n" \
"v_mfma_f32_16x16x32_bf16 v[172:175], v[24:27], v[216:219], v[172:175]\n" \
"s_waitcnt vmcnt(19) lgkmcnt(0)\n" \
"v_mul_f32 v236, s92, v236\n" \
"v_mul_f32 v237, s92, v237\n" \
"v_mfma_f32_16x16x32_bf16 v[172:175], v[28:31], v[220:223], v[172:175]\n" \
"s_barrier\n" \
"v_mfma_f32_16x16x32_bf16 v[80:83], v[32:35], v[192:195], v[80:83]\n" \
"ds_read_b64_tr_b16 v[0:1], v232 offset:0\n" \
"v_cvt_pk_bf16_f32 v234, v234, v235\n" \
"v_mfma_f32_16x16x32_bf16 v[80:83], v[36:39], v[196:199], v[80:83]\n" \
"ds_read_b64_tr_b16 v[2:3], v232 offset:2048\n" \
"v_cvt_pk_bf16_f32 v236, v236, v237\n" \
"v_mfma_f32_16x16x32_bf16 v[84:87], v[40:43], v[192:195], v[84:87]\n" \
"s_add_u32 m0, s87, 135168\n" \
"ds_read_b64_tr_b16 v[4:5], v232 offset:8192\n" \
"v_accvgpr_write_b32 a8, v234\n" \
"v_mfma_f32_16x16x32_bf16 v[84:87], v[44:47], v[196:199], v[84:87]\n" \
"global_load_lds_dwordx4 v224, s[48:49] offset:-4096\n" \
"v_accvgpr_write_b32 a9, v236\n" \
"v_mfma_f32_16x16x32_bf16 v[88:91], v[48:51], v[192:195], v[88:91]\n" \
"ds_read_b64_tr_b16 v[6:7], v232 offset:10240\n" \
"v_accvgpr_read_b32 v234, a12\n" \
"v_mfma_f32_16x16x32_bf16 v[88:91], v[52:55], v[196:199], v[88:91]\n" \
"ds_read_b64_tr_b16 v[8:9], v233 offset:0\n" \
"v_accvgpr_read_b32 v235, a13\n" \
"v_mfma_f32_16x16x32_bf16 v[92:95], v[56:59], v[192:195], v[92:95]\n" \
"ds_read_b64_tr_b16 v[10:11], v233 offset:2048\n" \
"v_accvgpr_read_b32 v236, a14\n" \
"v_mfma_f32_16x16x32_bf16 v[92:95], v[60:63], v[196:199], v[92:95]\n" \
"global_load_lds_dwordx4 v225, s[48:49] offset:-3072\n" \
"v_accvgpr_read_b32 v237, a15\n" \
"v_mfma_f32_16x16x32_bf16 v[112:115], v[32:35], v[200:203], v[112:115]\n" \
"ds_read_b128 v[192:195], v230 offset:0\n" \
"v_mul_f32 v234, s92, v234\n" \
"v_mfma_f32_16x16x32_bf16 v[112:115], v[36:39], v[204:207], v[112:115]\n" \
"ds_read_b128 v[196:199], v231 offset:0\n" \
"v_mul_f32 v235, s92, v235\n" \
"v_mfma_f32_16x16x32_bf16 v[116:119], v[40:43], v[200:203], v[116:119]\n" \
"ds_read_b64_tr_b16 v[12:13], v233 offset:8192\n" \
"v_mul_f32 v236, s92, v236\n" \
"v_mfma_f32_16x16x32_bf16 v[116:119], v[44:47], v[204:207], v[116:119]\n" \
"global_load_lds_dwordx4 v226, s[48:49] offset:-2048\n" \
"v_mul_f32 v237, s92, v237\n" \
"v_mfma_f32_16x16x32_bf16 v[120:123], v[48:51], v[200:203], v[120:123]\n" \
"ds_read_b64_tr_b16 v[14:15], v233 offset:10240\n" \
"v_cvt_pk_bf16_f32 v234, v234, v235\n" \
"v_mfma_f32_16x16x32_bf16 v[120:123], v[52:55], v[204:207], v[120:123]\n" \
"ds_read_b64_tr_b16 v[16:17], v232 offset:512\n" \
"v_cvt_pk_bf16_f32 v236, v236, v237\n" \
"v_mfma_f32_16x16x32_bf16 v[124:127], v[56:59], v[200:203], v[124:127]\n" \
"ds_read_b64_tr_b16 v[18:19], v232 offset:2560\n" \
"v_accvgpr_write_b32 a10, v234\n" \
"v_mfma_f32_16x16x32_bf16 v[124:127], v[60:63], v[204:207], v[124:127]\n" \
"global_load_lds_dwordx4 v227, s[48:49] offset:-1024\n" \
"v_accvgpr_write_b32 a11, v236\n" \
"v_mfma_f32_16x16x32_bf16 v[144:147], v[32:35], v[208:211], v[144:147]\n" \
"s_add_u32 s48, s48, 0x80\n" \
"s_addc_u32 s49, s49, 0\n" \
"ds_read_b128 v[200:203], v230 offset:2048\n" \
"v_mfma_f32_16x16x32_bf16 v[144:147], v[36:39], v[212:215], v[144:147]\n" \
"ds_read_b128 v[204:207], v231 offset:2048\n" \
"v_accvgpr_read_b32 v234, a40\n" \
"v_mfma_f32_16x16x32_bf16 v[148:151], v[40:43], v[208:211], v[148:151]\n" \
"ds_read_b64_tr_b16 v[20:21], v232 offset:8704\n" \
"v_accvgpr_read_b32 v235, a41\n" \
"v_mfma_f32_16x16x32_bf16 v[148:151], v[44:47], v[212:215], v[148:151]\n" \
"global_load_lds_dwordx4 v224, s[46:47] offset:0\n" \
"v_accvgpr_read_b32 v236, a42\n" \
"v_mfma_f32_16x16x32_bf16 v[152:155], v[48:51], v[208:211], v[152:155]\n" \
"ds_read_b64_tr_b16 v[22:23], v232 offset:10752\n" \
"v_accvgpr_read_b32 v237, a43\n" \
"v_mfma_f32_16x16x32_bf16 v[152:155], v[52:55], v[212:215], v[152:155]\n" \
"ds_read_b64_tr_b16 v[24:25], v233 offset:512\n" \
"v_mul_f32 v234, s92, v234\n" \
"v_mfma_f32_16x16x32_bf16 v[156:159], v[56:59], v[208:211], v[156:159]\n" \
"ds_read_b64_tr_b16 v[26:27], v233 offset:2560\n" \
"v_mul_f32 v235, s92, v235\n" \
"v_mfma_f32_16x16x32_bf16 v[156:159], v[60:63], v[212:215], v[156:159]\n" \
"global_load_lds_dwordx4 v225, s[46:47] offset:1024\n" \
"v_mul_f32 v236, s92, v236\n" \
"v_mfma_f32_16x16x32_bf16 v[176:179], v[32:35], v[216:219], v[176:179]\n" \
"ds_read_b128 v[208:211], v230 offset:4096\n" \
"v_mul_f32 v237, s92, v237\n" \
"v_mfma_f32_16x16x32_bf16 v[176:179], v[36:39], v[220:223], v[176:179]\n" \
"ds_read_b128 v[212:215], v231 offset:4096\n" \
"v_cvt_pk_bf16_f32 v234, v234, v235\n" \
"v_mfma_f32_16x16x32_bf16 v[180:183], v[40:43], v[216:219], v[180:183]\n" \
"ds_read_b64_tr_b16 v[28:29], v233 offset:8704\n" \
"v_cvt_pk_bf16_f32 v236, v236, v237\n" \
"v_mfma_f32_16x16x32_bf16 v[180:183], v[44:47], v[220:223], v[180:183]\n" \
"global_load_lds_dwordx4 v226, s[46:47] offset:2048\n" \
"v_accvgpr_write_b32 a40, v234\n" \
"v_mfma_f32_16x16x32_bf16 v[184:187], v[48:51], v[216:219], v[184:187]\n" \
"ds_read_b64_tr_b16 v[30:31], v233 offset:10752\n" \
"v_accvgpr_write_b32 a41, v236\n" \
"v_mfma_f32_16x16x32_bf16 v[184:187], v[52:55], v[220:223], v[184:187]\n" \
"v_accvgpr_read_b32 v234, a44\n" \
"v_accvgpr_read_b32 v235, a45\n" \
"v_mfma_f32_16x16x32_bf16 v[188:191], v[56:59], v[216:219], v[188:191]\n" \
"v_accvgpr_read_b32 v236, a46\n" \
"v_accvgpr_read_b32 v237, a47\n" \
"v_mfma_f32_16x16x32_bf16 v[188:191], v[60:63], v[220:223], v[188:191]\n" \
"s_waitcnt lgkmcnt(1)\n" \
"global_load_lds_dwordx4 v227, s[46:47] offset:3072\n" \
"v_mul_f32 v234, s92, v234\n" \
"v_mfma_f32_16x16x32_bf16 v[64:67], v[0:3], v[192:195], v[64:67]\n" \
"s_add_u32 s46, s46, 0x80\n" \
"s_addc_u32 s47, s47, 0\n" \
"ds_read_b128 v[216:219], v230 offset:6144\n" \
"v_mfma_f32_16x16x32_bf16 v[64:67], v[4:7], v[196:199], v[64:67]\n" \
"ds_read_b128 v[220:223], v231 offset:6144\n" \
"v_mul_f32 v235, s92, v235\n" \
"v_mfma_f32_16x16x32_bf16 v[68:71], v[8:11], v[192:195], v[68:71]\n" \
"ds_read_b64_tr_b16 v[32:33], v232 offset:1024\n" \
"v_mul_f32 v236, s92, v236\n" \
"v_mfma_f32_16x16x32_bf16 v[68:71], v[12:15], v[196:199], v[68:71]\n" \
"buffer_store_dwordx4 a[136:139], v249, s[56:59], s90 offen offset:2048 sc1\n" \
"v_mul_f32 v237, s92, v237\n" \
"v_cvt_pk_bf16_f32 v234, v234, v235\n" \
"v_cvt_pk_bf16_f32 v236, v236, v237\n" \
"v_accvgpr_write_b32 a42, v234\n" \
"v_mfma_f32_16x16x32_bf16 v[72:75], v[16:19], v[192:195], v[72:75]\n" \
"ds_read_b64_tr_b16 v[34:35], v232 offset:3072\n" \
"v_accvgpr_write_b32 a43, v236\n" \
"v_mfma_f32_16x16x32_bf16 v[72:75], v[20:23], v[196:199], v[72:75]\n" \
"s_waitcnt lgkmcnt(3)\n" \
"ds_read_b64_tr_b16 v[36:37], v232 offset:9216\n" \
"v_accvgpr_read_b32 v234, a16\n" \
"v_mfma_f32_16x16x32_bf16 v[76:79], v[24:27], v[192:195], v[76:79]\n" \
"ds_read_b64_tr_b16 v[38:39], v232 offset:11264\n" \
"v_accvgpr_read_b32 v235, a17\n" \
"v_mfma_f32_16x16x32_bf16 v[76:79], v[28:31], v[196:199], v[76:79]\n" \
"v_accvgpr_read_b32 v236, a18\n" \
"v_accvgpr_read_b32 v237, a19\n" \
"v_mfma_f32_16x16x32_bf16 v[96:99], v[0:3], v[200:203], v[96:99]\n" \
"ds_read_b64_tr_b16 v[40:41], v233 offset:1024\n" \
"v_cvt_pk_bf16_f32 v234, v234, v235\n" \
"v_mfma_f32_16x16x32_bf16 v[96:99], v[4:7], v[204:207], v[96:99]\n" \
"ds_read_b64_tr_b16 v[42:43], v233 offset:3072\n" \
"v_cvt_pk_bf16_f32 v236, v236, v237\n" \
"v_mfma_f32_16x16x32_bf16 v[100:103], v[8:11], v[200:203], v[100:103]\n" \
"ds_read_b64_tr_b16 v[44:45], v233 offset:9216\n" \
"v_accvgpr_write_b32 a16, v234\n" \
"v_mfma_f32_16x16x32_bf16 v[100:103], v[12:15], v[204:207], v[100:103]\n" \
"v_accvgpr_write_b32 a17, v236\n" \
"v_accvgpr_read_b32 v234, a48\n" \
"v_mfma_f32_16x16x32_bf16 v[104:107], v[16:19], v[200:203], v[104:107]\n" \
"ds_read_b64_tr_b16 v[46:47], v233 offset:11264\n" \
"v_accvgpr_read_b32 v235, a49\n" \
"v_mfma_f32_16x16x32_bf16 v[104:107], v[20:23], v[204:207], v[104:107]\n" \
"ds_read_b64_tr_b16 v[48:49], v232 offset:1536\n" \
"v_accvgpr_read_b32 v236, a50\n" \
"v_mfma_f32_16x16x32_bf16 v[108:111], v[24:27], v[200:203], v[108:111]\n" \
"s_add_u32 m0, s95, 36864\n" \
"ds_read_b64_tr_b16 v[50:51], v232 offset:3584\n" \
"v_accvgpr_read_b32 v237, a51\n" \
"v_mfma_f32_16x16x32_bf16 v[108:111], v[28:31], v[204:207], v[108:111]\n" \
"global_load_lds_dwordx4 v228, s[44:45] offset:-4096\n" \
"v_cvt_pk_bf16_f32 v234, v234, v235\n" \
"v_mfma_f32_16x16x32_bf16 v[128:131], v[0:3], v[208:211], v[128:131]\n" \
"ds_read_b64_tr_b16 v[52:53], v232 offset:9728\n" \
"v_cvt_pk_bf16_f32 v236, v236, v237\n" \
"v_mfma_f32_16x16x32_bf16 v[128:131], v[4:7], v[212:215], v[128:131]\n" \
"ds_read_b64_tr_b16 v[54:55], v232 offset:11776\n" \
"v_accvgpr_write_b32 a18, v234\n" \
"v_mfma_f32_16x16x32_bf16 v[132:135], v[8:11], v[208:211], v[132:135]\n" \
"ds_read_b64_tr_b16 v[56:57], v233 offset:1536\n" \
"v_accvgpr_write_b32 a19, v236\n" \
"v_mfma_f32_16x16x32_bf16 v[132:135], v[12:15], v[212:215], v[132:135]\n" \
"global_load_lds_dwordx4 v229, s[44:45] offset:-3072\n" \
"v_accvgpr_read_b32 v234, a20\n" \
"v_mfma_f32_16x16x32_bf16 v[136:139], v[16:19], v[208:211], v[136:139]\n" \
"s_add_u32 s44, s44, 0x80000\n" \
"s_addc_u32 s45, s45, 0\n" \
"ds_read_b64_tr_b16 v[58:59], v233 offset:3584\n" \
"v_mfma_f32_16x16x32_bf16 v[136:139], v[20:23], v[212:215], v[136:139]\n" \
"ds_read_b64_tr_b16 v[60:61], v233 offset:9728\n" \
"v_accvgpr_read_b32 v235, a21\n" \
"v_mfma_f32_16x16x32_bf16 v[140:143], v[24:27], v[208:211], v[140:143]\n" \
"ds_read_b64_tr_b16 v[62:63], v233 offset:11776\n" \
"v_accvgpr_read_b32 v236, a22\n" \
"v_mfma_f32_16x16x32_bf16 v[140:143], v[28:31], v[212:215], v[140:143]\n" \
"s_waitcnt lgkmcnt(3)\n" \
"global_load_lds_dwordx4 v228, s[42:43] offset:-2048\n" \
"v_accvgpr_read_b32 v237, a23\n" \
"v_mfma_f32_16x16x32_bf16 v[160:163], v[0:3], v[216:219], v[160:163]\n" \
"v_add_u32 v230, 0x8000, v230\n" \
"v_add_u32 v231, 0x8000, v231\n" \
"v_mfma_f32_16x16x32_bf16 v[160:163], v[4:7], v[220:223], v[160:163]\n" \
"v_add_u32 v232, 0xffff4000, v232\n" \
"v_add_u32 v233, 0xffff4000, v233\n" \
"v_mfma_f32_16x16x32_bf16 v[164:167], v[8:11], v[216:219], v[164:167]\n" \
"v_cvt_pk_bf16_f32 v234, v234, v235\n" \
"v_cvt_pk_bf16_f32 v236, v236, v237\n" \
"v_mfma_f32_16x16x32_bf16 v[164:167], v[12:15], v[220:223], v[164:167]\n" \
"global_load_lds_dwordx4 v229, s[42:43] offset:-1024\n" \
"v_accvgpr_write_b32 a20, v234\n" \
"v_mfma_f32_16x16x32_bf16 v[168:171], v[16:19], v[216:219], v[168:171]\n" \
"s_add_u32 s42, s42, 0x80000\n" \
"s_addc_u32 s43, s43, 0\n" \
"v_accvgpr_write_b32 a21, v236\n" \
"v_mfma_f32_16x16x32_bf16 v[168:171], v[20:23], v[220:223], v[168:171]\n" \
"v_accvgpr_read_b32 v234, a52\n" \
"v_accvgpr_read_b32 v235, a53\n" \
"v_mfma_f32_16x16x32_bf16 v[172:175], v[24:27], v[216:219], v[172:175]\n" \
"s_waitcnt vmcnt(18) lgkmcnt(0)\n" \
"v_accvgpr_read_b32 v236, a54\n" \
"v_accvgpr_read_b32 v237, a55\n" \
"v_mfma_f32_16x16x32_bf16 v[172:175], v[28:31], v[220:223], v[172:175]\n" \
"s_barrier\n" \
"v_mfma_f32_16x16x32_bf16 v[80:83], v[32:35], v[192:195], v[80:83]\n" \
"ds_read_b64_tr_b16 v[0:1], v232 offset:0\n" \
"v_cvt_pk_bf16_f32 v234, v234, v235\n" \
"v_mfma_f32_16x16x32_bf16 v[80:83], v[36:39], v[196:199], v[80:83]\n" \
"ds_read_b64_tr_b16 v[2:3], v232 offset:2048\n" \
"v_cvt_pk_bf16_f32 v236, v236, v237\n" \
"v_mfma_f32_16x16x32_bf16 v[84:87], v[40:43], v[192:195], v[84:87]\n" \
"s_add_u32 m0, s87, 69632\n" \
"ds_read_b64_tr_b16 v[4:5], v232 offset:8192\n" \
"v_accvgpr_write_b32 a22, v234\n" \
"v_mfma_f32_16x16x32_bf16 v[84:87], v[44:47], v[196:199], v[84:87]\n" \
"global_load_lds_dwordx4 v224, s[48:49] offset:-4096\n" \
"v_accvgpr_write_b32 a23, v236\n" \
"v_mfma_f32_16x16x32_bf16 v[88:91], v[48:51], v[192:195], v[88:91]\n" \
"ds_read_b64_tr_b16 v[6:7], v232 offset:10240\n" \
"v_accvgpr_read_b32 v234, a24\n" \
"v_mfma_f32_16x16x32_bf16 v[88:91], v[52:55], v[196:199], v[88:91]\n" \
"ds_read_b64_tr_b16 v[8:9], v233 offset:0\n" \
"v_accvgpr_read_b32 v235, a25\n" \
"v_mfma_f32_16x16x32_bf16 v[92:95], v[56:59], v[192:195], v[92:95]\n" \
"ds_read_b64_tr_b16 v[10:11], v233 offset:2048\n" \
"v_accvgpr_read_b32 v236, a26\n" \
"v_mfma_f32_16x16x32_bf16 v[92:95], v[60:63], v[196:199], v[92:95]\n" \
"global_load_lds_dwordx4 v225, s[48:49] offset:-3072\n" \
"v_accvgpr_read_b32 v237, a27\n" \
"v_mfma_f32_16x16x32_bf16 v[112:115], v[32:35], v[200:203], v[112:115]\n" \
"ds_read_b128 v[192:195], v230 offset:0\n" \
"v_cvt_pk_bf16_f32 v234, v234, v235\n" \
"v_mfma_f32_16x16x32_bf16 v[112:115], v[36:39], v[204:207], v[112:115]\n" \
"ds_read_b128 v[196:199], v231 offset:0\n" \
"v_cvt_pk_bf16_f32 v236, v236, v237\n" \
"v_mfma_f32_16x16x32_bf16 v[116:119], v[40:43], v[200:203], v[116:119]\n" \
"ds_read_b64_tr_b16 v[12:13], v233 offset:8192\n" \
"v_accvgpr_write_b32 a24, v234\n" \
"v_mfma_f32_16x16x32_bf16 v[116:119], v[44:47], v[204:207], v[116:119]\n" \
"global_load_lds_dwordx4 v226, s[48:49] offset:-2048\n" \
"v_accvgpr_write_b32 a25, v236\n" \
"v_mfma_f32_16x16x32_bf16 v[120:123], v[48:51], v[200:203], v[120:123]\n" \
"ds_read_b64_tr_b16 v[14:15], v233 offset:10240\n" \
"v_accvgpr_read_b32 v234, a56\n" \
"v_mfma_f32_16x16x32_bf16 v[120:123], v[52:55], v[204:207], v[120:123]\n" \
"ds_read_b64_tr_b16 v[16:17], v232 offset:512\n" \
"v_accvgpr_read_b32 v235, a57\n" \
"v_mfma_f32_16x16x32_bf16 v[124:127], v[56:59], v[200:203], v[124:127]\n" \
"ds_read_b64_tr_b16 v[18:19], v232 offset:2560\n" \
"v_accvgpr_read_b32 v236, a58\n" \
"v_mfma_f32_16x16x32_bf16 v[124:127], v[60:63], v[204:207], v[124:127]\n" \
"global_load_lds_dwordx4 v227, s[48:49] offset:-1024\n" \
"v_accvgpr_read_b32 v237, a59\n" \
"v_mfma_f32_16x16x32_bf16 v[144:147], v[32:35], v[208:211], v[144:147]\n" \
"s_add_u32 s48, s48, 0x80\n" \
"s_addc_u32 s49, s49, 0\n" \
"ds_read_b128 v[200:203], v230 offset:2048\n" \
"v_mfma_f32_16x16x32_bf16 v[144:147], v[36:39], v[212:215], v[144:147]\n" \
"ds_read_b128 v[204:207], v231 offset:2048\n" \
"v_cvt_pk_bf16_f32 v234, v234, v235\n" \
"v_mfma_f32_16x16x32_bf16 v[148:151], v[40:43], v[208:211], v[148:151]\n" \
"ds_read_b64_tr_b16 v[20:21], v232 offset:8704\n" \
"v_cvt_pk_bf16_f32 v236, v236, v237\n" \
"v_mfma_f32_16x16x32_bf16 v[148:151], v[44:47], v[212:215], v[148:151]\n" \
"global_load_lds_dwordx4 v224, s[46:47] offset:0\n" \
"v_accvgpr_write_b32 a26, v234\n" \
"v_mfma_f32_16x16x32_bf16 v[152:155], v[48:51], v[208:211], v[152:155]\n" \
"ds_read_b64_tr_b16 v[22:23], v232 offset:10752\n" \
"v_accvgpr_write_b32 a27, v236\n" \
"v_mfma_f32_16x16x32_bf16 v[152:155], v[52:55], v[212:215], v[152:155]\n" \
"ds_read_b64_tr_b16 v[24:25], v233 offset:512\n" \
"v_accvgpr_read_b32 v234, a28\n" \
"v_mfma_f32_16x16x32_bf16 v[156:159], v[56:59], v[208:211], v[156:159]\n" \
"ds_read_b64_tr_b16 v[26:27], v233 offset:2560\n" \
"v_accvgpr_read_b32 v235, a29\n" \
"v_mfma_f32_16x16x32_bf16 v[156:159], v[60:63], v[212:215], v[156:159]\n" \
"global_load_lds_dwordx4 v225, s[46:47] offset:1024\n" \
"v_accvgpr_read_b32 v236, a30\n" \
"v_mfma_f32_16x16x32_bf16 v[176:179], v[32:35], v[216:219], v[176:179]\n" \
"ds_read_b128 v[208:211], v230 offset:4096\n" \
"v_accvgpr_read_b32 v237, a31\n" \
"v_mfma_f32_16x16x32_bf16 v[176:179], v[36:39], v[220:223], v[176:179]\n" \
"ds_read_b128 v[212:215], v231 offset:4096\n" \
"v_cvt_pk_bf16_f32 v234, v234, v235\n" \
"v_mfma_f32_16x16x32_bf16 v[180:183], v[40:43], v[216:219], v[180:183]\n" \
"ds_read_b64_tr_b16 v[28:29], v233 offset:8704\n" \
"v_cvt_pk_bf16_f32 v236, v236, v237\n" \
"v_mfma_f32_16x16x32_bf16 v[180:183], v[44:47], v[220:223], v[180:183]\n" \
"global_load_lds_dwordx4 v226, s[46:47] offset:2048\n" \
"v_accvgpr_write_b32 a28, v234\n" \
"v_mfma_f32_16x16x32_bf16 v[184:187], v[48:51], v[216:219], v[184:187]\n" \
"ds_read_b64_tr_b16 v[30:31], v233 offset:10752\n" \
"v_accvgpr_write_b32 a29, v236\n" \
"v_mfma_f32_16x16x32_bf16 v[184:187], v[52:55], v[220:223], v[184:187]\n" \
"v_accvgpr_read_b32 v234, a60\n" \
"v_accvgpr_read_b32 v235, a61\n" \
"v_mfma_f32_16x16x32_bf16 v[188:191], v[56:59], v[216:219], v[188:191]\n" \
"v_accvgpr_read_b32 v236, a62\n" \
"v_accvgpr_read_b32 v237, a63\n" \
"v_mfma_f32_16x16x32_bf16 v[188:191], v[60:63], v[220:223], v[188:191]\n" \
"s_waitcnt lgkmcnt(1)\n" \
"global_load_lds_dwordx4 v227, s[46:47] offset:3072\n" \
"v_cvt_pk_bf16_f32 v234, v234, v235\n" \
"v_mfma_f32_16x16x32_bf16 v[64:67], v[0:3], v[192:195], v[64:67]\n" \
"s_add_u32 s46, s46, 0x80\n" \
"s_addc_u32 s47, s47, 0\n" \
"ds_read_b128 v[216:219], v230 offset:6144\n" \
"v_mfma_f32_16x16x32_bf16 v[64:67], v[4:7], v[196:199], v[64:67]\n" \
"ds_read_b128 v[220:223], v231 offset:6144\n" \
"v_cvt_pk_bf16_f32 v236, v236, v237\n" \
"v_mfma_f32_16x16x32_bf16 v[68:71], v[8:11], v[192:195], v[68:71]\n" \
"ds_read_b64_tr_b16 v[32:33], v232 offset:1024\n" \
"v_accvgpr_write_b32 a30, v234\n" \
"v_mfma_f32_16x16x32_bf16 v[68:71], v[12:15], v[196:199], v[68:71]\n" \
"buffer_store_dwordx4 a[168:171], v249, s[56:59], s90 offen offset:2304 sc1\n" \
"v_accvgpr_write_b32 a31, v236\n" \
"v_mfma_f32_16x16x32_bf16 v[72:75], v[16:19], v[192:195], v[72:75]\n" \
"s_add_u32 s90, s89, 139264\n" \
"ds_read_b64_tr_b16 v[34:35], v232 offset:3072\n" \
"v_mfma_f32_16x16x32_bf16 v[72:75], v[20:23], v[196:199], v[72:75]\n" \
"s_waitcnt lgkmcnt(3)\n" \
"ds_read_b64_tr_b16 v[36:37], v232 offset:9216\n" \
"v_mfma_f32_16x16x32_bf16 v[76:79], v[24:27], v[192:195], v[76:79]\n" \
"ds_read_b64_tr_b16 v[38:39], v232 offset:11264\n" \
"v_mfma_f32_16x16x32_bf16 v[76:79], v[28:31], v[196:199], v[76:79]\n" \
"buffer_store_dwordx4 a[144:147], v248, s[56:59], s90 offen offset:0 sc1\n" \
"v_mfma_f32_16x16x32_bf16 v[96:99], v[0:3], v[200:203], v[96:99]\n" \
"s_lshl_b32 s70, s63, 2\n" \
"ds_read_b64_tr_b16 v[40:41], v233 offset:1024\n" \
"v_mfma_f32_16x16x32_bf16 v[96:99], v[4:7], v[204:207], v[96:99]\n" \
"s_add_u32 s70, s70, s91\n" \
"ds_read_b64_tr_b16 v[42:43], v233 offset:3072\n" \
"v_mov_b32 v238, s60\n" \
"v_mfma_f32_16x16x32_bf16 v[100:103], v[8:11], v[200:203], v[100:103]\n" \
"ds_read_b64_tr_b16 v[44:45], v233 offset:9216\n" \
"v_lshlrev_b32 v239, 28, s61\n" \
"v_mfma_f32_16x16x32_bf16 v[100:103], v[12:15], v[204:207], v[100:103]\n" \
"buffer_store_dword v238, v239, s[56:59], s70 offen sc1\n" \
"v_mfma_f32_16x16x32_bf16 v[104:107], v[16:19], v[200:203], v[104:107]\n" \
"ds_read_b64_tr_b16 v[46:47], v233 offset:11264\n" \
"v_mfma_f32_16x16x32_bf16 v[104:107], v[20:23], v[204:207], v[104:107]\n" \
"ds_read_b64_tr_b16 v[48:49], v232 offset:1536\n" \
"v_mfma_f32_16x16x32_bf16 v[108:111], v[24:27], v[200:203], v[108:111]\n" \
"s_add_u32 m0, s95, 53248\n" \
"ds_read_b64_tr_b16 v[50:51], v232 offset:3584\n" \
"v_mfma_f32_16x16x32_bf16 v[108:111], v[28:31], v[204:207], v[108:111]\n" \
"global_load_lds_dwordx4 v228, s[44:45] offset:-4096\n" \
"v_mfma_f32_16x16x32_bf16 v[128:131], v[0:3], v[208:211], v[128:131]\n" \
"ds_read_b64_tr_b16 v[52:53], v232 offset:9728\n" \
"v_mfma_f32_16x16x32_bf16 v[128:131], v[4:7], v[212:215], v[128:131]\n" \
"ds_read_b64_tr_b16 v[54:55], v232 offset:11776\n" \
"v_mfma_f32_16x16x32_bf16 v[132:135], v[8:11], v[208:211], v[132:135]\n" \
"ds_read_b64_tr_b16 v[56:57], v233 offset:1536\n" \
"v_mfma_f32_16x16x32_bf16 v[132:135], v[12:15], v[212:215], v[132:135]\n" \
"global_load_lds_dwordx4 v229, s[44:45] offset:-3072\n" \
"v_mfma_f32_16x16x32_bf16 v[136:139], v[16:19], v[208:211], v[136:139]\n" \
"s_add_u32 s44, s44, 0x100000\n" \
"s_addc_u32 s45, s45, 0\n" \
"ds_read_b64_tr_b16 v[58:59], v233 offset:3584\n" \
"v_mfma_f32_16x16x32_bf16 v[136:139], v[20:23], v[212:215], v[136:139]\n" \
"Lpg3808_%=:\n" \
"s_cbranch_execz Lpg5524_%=\n" \
"ds_read_b64_tr_b16 v[60:61], v233 offset:9728\n" \
"v_mfma_f32_16x16x32_bf16 v[140:143], v[24:27], v[208:211], v[140:143]\n" \
"ds_read_b64_tr_b16 v[62:63], v233 offset:11776\n" \
"v_mfma_f32_16x16x32_bf16 v[140:143], v[28:31], v[212:215], v[140:143]\n" \
"s_waitcnt lgkmcnt(3)\n" \
"global_load_lds_dwordx4 v228, s[42:43] offset:-2048\n" \
"v_mfma_f32_16x16x32_bf16 v[160:163], v[0:3], v[216:219], v[160:163]\n" \
"v_add_u32 v230, 0x8000, v230\n" \
"v_add_u32 v231, 0x8000, v231\n" \
"v_mfma_f32_16x16x32_bf16 v[160:163], v[4:7], v[220:223], v[160:163]\n" \
"v_add_u32 v232, 0x4000, v232\n" \
"v_add_u32 v233, 0x4000, v233\n" \
"v_mfma_f32_16x16x32_bf16 v[164:167], v[8:11], v[216:219], v[164:167]\n" \
"v_mfma_f32_16x16x32_bf16 v[164:167], v[12:15], v[220:223], v[164:167]\n" \
"global_load_lds_dwordx4 v229, s[42:43] offset:-1024\n" \
"v_mfma_f32_16x16x32_bf16 v[168:171], v[16:19], v[216:219], v[168:171]\n" \
"s_add_u32 s42, s42, 0x100000\n" \
"s_addc_u32 s43, s43, 0\n" \
"v_mfma_f32_16x16x32_bf16 v[168:171], v[20:23], v[220:223], v[168:171]\n" \
"v_mfma_f32_16x16x32_bf16 v[172:175], v[24:27], v[216:219], v[172:175]\n" \
"s_waitcnt vmcnt(20) lgkmcnt(0)\n" \
"v_mfma_f32_16x16x32_bf16 v[172:175], v[28:31], v[220:223], v[172:175]\n" \
"s_barrier\n" \
"v_mfma_f32_16x16x32_bf16 v[80:83], v[32:35], v[192:195], v[80:83]\n" \
"ds_read_b64_tr_b16 v[0:1], v232 offset:0\n" \
"v_mfma_f32_16x16x32_bf16 v[80:83], v[36:39], v[196:199], v[80:83]\n" \
"ds_read_b64_tr_b16 v[2:3], v232 offset:2048\n" \
"v_mfma_f32_16x16x32_bf16 v[84:87], v[40:43], v[192:195], v[84:87]\n" \
"s_add_u32 m0, s87, 102400\n" \
"ds_read_b64_tr_b16 v[4:5], v232 offset:8192\n" \
"v_mfma_f32_16x16x32_bf16 v[84:87], v[44:47], v[196:199], v[84:87]\n" \
"global_load_lds_dwordx4 v224, s[48:49] offset:-4096\n" \
"v_mfma_f32_16x16x32_bf16 v[88:91], v[48:51], v[192:195], v[88:91]\n" \
"ds_read_b64_tr_b16 v[6:7], v232 offset:10240\n" \
"v_mfma_f32_16x16x32_bf16 v[88:91], v[52:55], v[196:199], v[88:91]\n" \
"ds_read_b64_tr_b16 v[8:9], v233 offset:0\n" \
"v_mfma_f32_16x16x32_bf16 v[92:95], v[56:59], v[192:195], v[92:95]\n" \
"ds_read_b64_tr_b16 v[10:11], v233 offset:2048\n" \
"v_mfma_f32_16x16x32_bf16 v[92:95], v[60:63], v[196:199], v[92:95]\n" \
"global_load_lds_dwordx4 v225, s[48:49] offset:-3072\n" \
"v_mfma_f32_16x16x32_bf16 v[112:115], v[32:35], v[200:203], v[112:115]\n" \
"ds_read_b128 v[192:195], v230 offset:0\n" \
"v_mfma_f32_16x16x32_bf16 v[112:115], v[36:39], v[204:207], v[112:115]\n" \
"ds_read_b128 v[196:199], v231 offset:0\n" \
"v_mfma_f32_16x16x32_bf16 v[116:119], v[40:43], v[200:203], v[116:119]\n" \
"ds_read_b64_tr_b16 v[12:13], v233 offset:8192\n" \
"v_mfma_f32_16x16x32_bf16 v[116:119], v[44:47], v[204:207], v[116:119]\n" \
"global_load_lds_dwordx4 v226, s[48:49] offset:-2048\n" \
"v_mfma_f32_16x16x32_bf16 v[120:123], v[48:51], v[200:203], v[120:123]\n" \
"ds_read_b64_tr_b16 v[14:15], v233 offset:10240\n" \
"v_mfma_f32_16x16x32_bf16 v[120:123], v[52:55], v[204:207], v[120:123]\n" \
"ds_read_b64_tr_b16 v[16:17], v232 offset:512\n" \
"v_mfma_f32_16x16x32_bf16 v[124:127], v[56:59], v[200:203], v[124:127]\n" \
"ds_read_b64_tr_b16 v[18:19], v232 offset:2560\n" \
"v_mfma_f32_16x16x32_bf16 v[124:127], v[60:63], v[204:207], v[124:127]\n" \
"global_load_lds_dwordx4 v227, s[48:49] offset:-1024\n" \
"v_mfma_f32_16x16x32_bf16 v[144:147], v[32:35], v[208:211], v[144:147]\n" \
"s_add_u32 s48, s48, 0x100\n" \
"s_addc_u32 s49, s49, 0\n" \
"ds_read_b128 v[200:203], v230 offset:2048\n" \
"v_mfma_f32_16x16x32_bf16 v[144:147], v[36:39], v[212:215], v[144:147]\n" \
"ds_read_b128 v[204:207], v231 offset:2048\n" \
"v_mfma_f32_16x16x32_bf16 v[148:151], v[40:43], v[208:211], v[148:151]\n" \
"ds_read_b64_tr_b16 v[20:21], v232 offset:8704\n" \
"v_mfma_f32_16x16x32_bf16 v[148:151], v[44:47], v[212:215], v[148:151]\n" \
"global_load_lds_dwordx4 v224, s[46:47] offset:0\n" \
"v_mfma_f32_16x16x32_bf16 v[152:155], v[48:51], v[208:211], v[152:155]\n" \
"ds_read_b64_tr_b16 v[22:23], v232 offset:10752\n" \
"v_mfma_f32_16x16x32_bf16 v[152:155], v[52:55], v[212:215], v[152:155]\n" \
"ds_read_b64_tr_b16 v[24:25], v233 offset:512\n" \
"v_mfma_f32_16x16x32_bf16 v[156:159], v[56:59], v[208:211], v[156:159]\n" \
"ds_read_b64_tr_b16 v[26:27], v233 offset:2560\n" \
"v_mfma_f32_16x16x32_bf16 v[156:159], v[60:63], v[212:215], v[156:159]\n" \
"global_load_lds_dwordx4 v225, s[46:47] offset:1024\n" \
"v_mfma_f32_16x16x32_bf16 v[176:179], v[32:35], v[216:219], v[176:179]\n" \
"ds_read_b128 v[208:211], v230 offset:4096\n" \
"v_mfma_f32_16x16x32_bf16 v[176:179], v[36:39], v[220:223], v[176:179]\n" \
"ds_read_b128 v[212:215], v231 offset:4096\n" \
"v_mfma_f32_16x16x32_bf16 v[180:183], v[40:43], v[216:219], v[180:183]\n" \
"ds_read_b64_tr_b16 v[28:29], v233 offset:8704\n" \
"v_mfma_f32_16x16x32_bf16 v[180:183], v[44:47], v[220:223], v[180:183]\n" \
"global_load_lds_dwordx4 v226, s[46:47] offset:2048\n" \
"v_mfma_f32_16x16x32_bf16 v[184:187], v[48:51], v[216:219], v[184:187]\n" \
"ds_read_b64_tr_b16 v[30:31], v233 offset:10752\n" \
"v_mfma_f32_16x16x32_bf16 v[184:187], v[52:55], v[220:223], v[184:187]\n" \
"v_mfma_f32_16x16x32_bf16 v[188:191], v[56:59], v[216:219], v[188:191]\n" \
"v_mfma_f32_16x16x32_bf16 v[188:191], v[60:63], v[220:223], v[188:191]\n" \
"s_waitcnt lgkmcnt(1)\n" \
"global_load_lds_dwordx4 v227, s[46:47] offset:3072\n" \
"v_mfma_f32_16x16x32_bf16 v[64:67], v[0:3], v[192:195], v[64:67]\n" \
"s_add_u32 s46, s46, 0x100\n" \
"s_addc_u32 s47, s47, 0\n" \
"ds_read_b128 v[216:219], v230 offset:6144\n" \
"v_mfma_f32_16x16x32_bf16 v[64:67], v[4:7], v[196:199], v[64:67]\n" \
"ds_read_b128 v[220:223], v231 offset:6144\n" \
"v_mfma_f32_16x16x32_bf16 v[68:71], v[8:11], v[192:195], v[68:71]\n" \
"ds_read_b64_tr_b16 v[32:33], v232 offset:1024\n" \
"v_mfma_f32_16x16x32_bf16 v[68:71], v[12:15], v[196:199], v[68:71]\n" \
"buffer_store_dwordx4 a[148:151], v248, s[56:59], s90 offen offset:1024 sc1\n" \
"v_mfma_f32_16x16x32_bf16 v[72:75], v[16:19], v[192:195], v[72:75]\n" \
"ds_read_b64_tr_b16 v[34:35], v232 offset:3072\n" \
"v_mfma_f32_16x16x32_bf16 v[72:75], v[20:23], v[196:199], v[72:75]\n" \
"s_waitcnt lgkmcnt(3)\n" \
"ds_read_b64_tr_b16 v[36:37], v232 offset:9216\n" \
"v_mfma_f32_16x16x32_bf16 v[76:79], v[24:27], v[192:195], v[76:79]\n" \
"ds_read_b64_tr_b16 v[38:39], v232 offset:11264\n" \
"v_mfma_f32_16x16x32_bf16 v[76:79], v[28:31], v[196:199], v[76:79]\n" \
"v_mfma_f32_16x16x32_bf16 v[96:99], v[0:3], v[200:203], v[96:99]\n" \
"ds_read_b64_tr_b16 v[40:41], v233 offset:1024\n" \
"v_mfma_f32_16x16x32_bf16 v[96:99], v[4:7], v[204:207], v[96:99]\n" \
"ds_read_b64_tr_b16 v[42:43], v233 offset:3072\n" \
"v_mfma_f32_16x16x32_bf16 v[100:103], v[8:11], v[200:203], v[100:103]\n" \
"ds_read_b64_tr_b16 v[44:45], v233 offset:9216\n" \
"v_mfma_f32_16x16x32_bf16 v[100:103], v[12:15], v[204:207], v[100:103]\n" \
"v_mfma_f32_16x16x32_bf16 v[104:107], v[16:19], v[200:203], v[104:107]\n" \
"ds_read_b64_tr_b16 v[46:47], v233 offset:11264\n" \
"v_mfma_f32_16x16x32_bf16 v[104:107], v[20:23], v[204:207], v[104:107]\n" \
"ds_read_b64_tr_b16 v[48:49], v232 offset:1536\n" \
"v_mfma_f32_16x16x32_bf16 v[108:111], v[24:27], v[200:203], v[108:111]\n" \
"s_add_u32 m0, s95, 4096\n" \
"ds_read_b64_tr_b16 v[50:51], v232 offset:3584\n" \
"v_mfma_f32_16x16x32_bf16 v[108:111], v[28:31], v[204:207], v[108:111]\n" \
"global_load_lds_dwordx4 v228, s[44:45] offset:-4096\n" \
"v_mfma_f32_16x16x32_bf16 v[128:131], v[0:3], v[208:211], v[128:131]\n" \
"ds_read_b64_tr_b16 v[52:53], v232 offset:9728\n" \
"v_mfma_f32_16x16x32_bf16 v[128:131], v[4:7], v[212:215], v[128:131]\n" \
"ds_read_b64_tr_b16 v[54:55], v232 offset:11776\n" \
"v_mfma_f32_16x16x32_bf16 v[132:135], v[8:11], v[208:211], v[132:135]\n" \
"ds_read_b64_tr_b16 v[56:57], v233 offset:1536\n" \
"v_mfma_f32_16x16x32_bf16 v[132:135], v[12:15], v[212:215], v[132:135]\n" \
"global_load_lds_dwordx4 v229, s[44:45] offset:-3072\n" \
"v_mfma_f32_16x16x32_bf16 v[136:139], v[16:19], v[208:211], v[136:139]\n" \
"s_add_u32 s44, s44, 0x80000\n" \
"s_addc_u32 s45, s45, 0\n" \
"ds_read_b64_tr_b16 v[58:59], v233 offset:3584\n" \
"v_mfma_f32_16x16x32_bf16 v[136:139], v[20:23], v[212:215], v[136:139]\n" \
"ds_read_b64_tr_b16 v[60:61], v233 offset:9728\n" \
"v_mfma_f32_16x16x32_bf16 v[140:143], v[24:27], v[208:211], v[140:143]\n" \
"ds_read_b64_tr_b16 v[62:63], v233 offset:11776\n" \
"v_mfma_f32_16x16x32_bf16 v[140:143], v[28:31], v[212:215], v[140:143]\n" \
"s_waitcnt lgkmcnt(3)\n" \
"global_load_lds_dwordx4 v228, s[42:43] offset:-2048\n" \
"v_mfma_f32_16x16x32_bf16 v[160:163], v[0:3], v[216:219], v[160:163]\n" \
"v_add_u32 v230, 0xffff0000, v230\n" \
"v_add_u32 v231, 0xffff0000, v231\n" \
"v_mfma_f32_16x16x32_bf16 v[160:163], v[4:7], v[220:223], v[160:163]\n" \
"v_add_u32 v232, 0x4000, v232\n" \
"v_add_u32 v233, 0x4000, v233\n" \
"v_mfma_f32_16x16x32_bf16 v[164:167], v[8:11], v[216:219], v[164:167]\n" \
"v_mfma_f32_16x16x32_bf16 v[164:167], v[12:15], v[220:223], v[164:167]\n" \
"global_load_lds_dwordx4 v229, s[42:43] offset:-1024\n" \
"v_mfma_f32_16x16x32_bf16 v[168:171], v[16:19], v[216:219], v[168:171]\n" \
"s_add_u32 s42, s42, 0x80000\n" \
"s_addc_u32 s43, s43, 0\n" \
"v_mfma_f32_16x16x32_bf16 v[168:171], v[20:23], v[220:223], v[168:171]\n" \
"v_mfma_f32_16x16x32_bf16 v[172:175], v[24:27], v[216:219], v[172:175]\n" \
"s_waitcnt vmcnt(20) lgkmcnt(0)\n" \
"v_mfma_f32_16x16x32_bf16 v[172:175], v[28:31], v[220:223], v[172:175]\n" \
"s_barrier\n" \
"v_mfma_f32_16x16x32_bf16 v[80:83], v[32:35], v[192:195], v[80:83]\n" \
"ds_read_b64_tr_b16 v[0:1], v232 offset:0\n" \
"v_mfma_f32_16x16x32_bf16 v[80:83], v[36:39], v[196:199], v[80:83]\n" \
"ds_read_b64_tr_b16 v[2:3], v232 offset:2048\n" \
"v_mfma_f32_16x16x32_bf16 v[84:87], v[40:43], v[192:195], v[84:87]\n" \
"s_add_u32 m0, s87, 135168\n" \
"ds_read_b64_tr_b16 v[4:5], v232 offset:8192\n" \
"v_mfma_f32_16x16x32_bf16 v[84:87], v[44:47], v[196:199], v[84:87]\n" \
"global_load_lds_dwordx4 v224, s[48:49] offset:-4096\n" \
"v_mfma_f32_16x16x32_bf16 v[88:91], v[48:51], v[192:195], v[88:91]\n" \
"ds_read_b64_tr_b16 v[6:7], v232 offset:10240\n" \
"v_mfma_f32_16x16x32_bf16 v[88:91], v[52:55], v[196:199], v[88:91]\n" \
"ds_read_b64_tr_b16 v[8:9], v233 offset:0\n" \
"v_mfma_f32_16x16x32_bf16 v[92:95], v[56:59], v[192:195], v[92:95]\n" \
"ds_read_b64_tr_b16 v[10:11], v233 offset:2048\n" \
"v_mfma_f32_16x16x32_bf16 v[92:95], v[60:63], v[196:199], v[92:95]\n" \
"global_load_lds_dwordx4 v225, s[48:49] offset:-3072\n" \
"v_mfma_f32_16x16x32_bf16 v[112:115], v[32:35], v[200:203], v[112:115]\n" \
"ds_read_b128 v[192:195], v230 offset:0\n" \
"v_mfma_f32_16x16x32_bf16 v[112:115], v[36:39], v[204:207], v[112:115]\n" \
"ds_read_b128 v[196:199], v231 offset:0\n" \
"v_mfma_f32_16x16x32_bf16 v[116:119], v[40:43], v[200:203], v[116:119]\n" \
"ds_read_b64_tr_b16 v[12:13], v233 offset:8192\n" \
"v_mfma_f32_16x16x32_bf16 v[116:119], v[44:47], v[204:207], v[116:119]\n" \
"global_load_lds_dwordx4 v226, s[48:49] offset:-2048\n" \
"v_mfma_f32_16x16x32_bf16 v[120:123], v[48:51], v[200:203], v[120:123]\n" \
"ds_read_b64_tr_b16 v[14:15], v233 offset:10240\n" \
"v_mfma_f32_16x16x32_bf16 v[120:123], v[52:55], v[204:207], v[120:123]\n" \
"ds_read_b64_tr_b16 v[16:17], v232 offset:512\n" \
"v_mfma_f32_16x16x32_bf16 v[124:127], v[56:59], v[200:203], v[124:127]\n" \
"ds_read_b64_tr_b16 v[18:19], v232 offset:2560\n" \
"v_mfma_f32_16x16x32_bf16 v[124:127], v[60:63], v[204:207], v[124:127]\n" \
"global_load_lds_dwordx4 v227, s[48:49] offset:-1024\n" \
"v_mfma_f32_16x16x32_bf16 v[144:147], v[32:35], v[208:211], v[144:147]\n" \
"s_add_u32 s48, s48, 0x80\n" \
"s_addc_u32 s49, s49, 0\n" \
"ds_read_b128 v[200:203], v230 offset:2048\n" \
"v_mfma_f32_16x16x32_bf16 v[144:147], v[36:39], v[212:215], v[144:147]\n" \
"ds_read_b128 v[204:207], v231 offset:2048\n" \
"v_mfma_f32_16x16x32_bf16 v[148:151], v[40:43], v[208:211], v[148:151]\n" \
"ds_read_b64_tr_b16 v[20:21], v232 offset:8704\n" \
"v_mfma_f32_16x16x32_bf16 v[148:151], v[44:47], v[212:215], v[148:151]\n" \
"global_load_lds_dwordx4 v224, s[46:47] offset:0\n" \
"v_mfma_f32_16x16x32_bf16 v[152:155], v[48:51], v[208:211], v[152:155]\n" \
"ds_read_b64_tr_b16 v[22:23], v232 offset:10752\n" \
"v_mfma_f32_16x16x32_bf16 v[152:155], v[52:55], v[212:215], v[152:155]\n" \
"ds_read_b64_tr_b16 v[24:25], v233 offset:512\n" \
"v_mfma_f32_16x16x32_bf16 v[156:159], v[56:59], v[208:211], v[156:159]\n" \
"ds_read_b64_tr_b16 v[26:27], v233 offset:2560\n" \
"v_mfma_f32_16x16x32_bf16 v[156:159], v[60:63], v[212:215], v[156:159]\n" \
"global_load_lds_dwordx4 v225, s[46:47] offset:1024\n" \
"v_mfma_f32_16x16x32_bf16 v[176:179], v[32:35], v[216:219], v[176:179]\n" \
"ds_read_b128 v[208:211], v230 offset:4096\n" \
"v_mfma_f32_16x16x32_bf16 v[176:179], v[36:39], v[220:223], v[176:179]\n" \
"ds_read_b128 v[212:215], v231 offset:4096\n" \
"v_mfma_f32_16x16x32_bf16 v[180:183], v[40:43], v[216:219], v[180:183]\n" \
"ds_read_b64_tr_b16 v[28:29], v233 offset:8704\n" \
"v_mfma_f32_16x16x32_bf16 v[180:183], v[44:47], v[220:223], v[180:183]\n" \
"global_load_lds_dwordx4 v226, s[46:47] offset:2048\n" \
"v_mfma_f32_16x16x32_bf16 v[184:187], v[48:51], v[216:219], v[184:187]\n" \
"ds_read_b64_tr_b16 v[30:31], v233 offset:10752\n" \
"v_mfma_f32_16x16x32_bf16 v[184:187], v[52:55], v[220:223], v[184:187]\n" \
"v_mfma_f32_16x16x32_bf16 v[188:191], v[56:59], v[216:219], v[188:191]\n" \
"v_mfma_f32_16x16x32_bf16 v[188:191], v[60:63], v[220:223], v[188:191]\n" \
"s_waitcnt lgkmcnt(1)\n" \
"global_load_lds_dwordx4 v227, s[46:47] offset:3072\n" \
"v_mfma_f32_16x16x32_bf16 v[64:67], v[0:3], v[192:195], v[64:67]\n" \
"s_add_u32 s46, s46, 0x80\n" \
"s_addc_u32 s47, s47, 0\n" \
"ds_read_b128 v[216:219], v230 offset:6144\n" \
"v_mfma_f32_16x16x32_bf16 v[64:67], v[4:7], v[196:199], v[64:67]\n" \
"ds_read_b128 v[220:223], v231 offset:6144\n" \
"v_mfma_f32_16x16x32_bf16 v[68:71], v[8:11], v[192:195], v[68:71]\n" \
"ds_read_b64_tr_b16 v[32:33], v232 offset:1024\n" \
"v_mfma_f32_16x16x32_bf16 v[68:71], v[12:15], v[196:199], v[68:71]\n" \
"buffer_store_dwordx4 a[152:155], v248, s[56:59], s90 offen offset:2048 sc1\n" \
"v_mfma_f32_16x16x32_bf16 v[72:75], v[16:19], v[192:195], v[72:75]\n" \
"ds_read_b64_tr_b16 v[34:35], v232 offset:3072\n" \
"v_mfma_f32_16x16x32_bf16 v[72:75], v[20:23], v[196:199], v[72:75]\n" \
"s_waitcnt lgkmcnt(3)\n" \
"ds_read_b64_tr_b16 v[36:37], v232 offset:9216\n" \
"v_mfma_f32_16x16x32_bf16 v[76:79], v[24:27], v[192:195], v[76:79]\n" \
"ds_read_b64_tr_b16 v[38:39], v232 offset:11264\n" \
"v_mfma_f32_16x16x32_bf16 v[76:79], v[28:31], v[196:199], v[76:79]\n" \
"v_mfma_f32_16x16x32_bf16 v[96:99], v[0:3], v[200:203], v[96:99]\n" \
"ds_read_b64_tr_b16 v[40:41], v233 offset:1024\n" \
"v_mfma_f32_16x16x32_bf16 v[96:99], v[4:7], v[204:207], v[96:99]\n" \
"ds_read_b64_tr_b16 v[42:43], v233 offset:3072\n" \
"v_mfma_f32_16x16x32_bf16 v[100:103], v[8:11], v[200:203], v[100:103]\n" \
"ds_read_b64_tr_b16 v[44:45], v233 offset:9216\n" \
"v_mfma_f32_16x16x32_bf16 v[100:103], v[12:15], v[204:207], v[100:103]\n" \
"v_mfma_f32_16x16x32_bf16 v[104:107], v[16:19], v[200:203], v[104:107]\n" \
"ds_read_b64_tr_b16 v[46:47], v233 offset:11264\n" \
"v_mfma_f32_16x16x32_bf16 v[104:107], v[20:23], v[204:207], v[104:107]\n" \
"ds_read_b64_tr_b16 v[48:49], v232 offset:1536\n" \
"v_mfma_f32_16x16x32_bf16 v[108:111], v[24:27], v[200:203], v[108:111]\n" \
"s_add_u32 m0, s95, 20480\n" \
"ds_read_b64_tr_b16 v[50:51], v232 offset:3584\n" \
"v_mfma_f32_16x16x32_bf16 v[108:111], v[28:31], v[204:207], v[108:111]\n" \
"global_load_lds_dwordx4 v228, s[44:45] offset:-4096\n" \
"v_mfma_f32_16x16x32_bf16 v[128:131], v[0:3], v[208:211], v[128:131]\n" \
"ds_read_b64_tr_b16 v[52:53], v232 offset:9728\n" \
"v_mfma_f32_16x16x32_bf16 v[128:131], v[4:7], v[212:215], v[128:131]\n" \
"ds_read_b64_tr_b16 v[54:55], v232 offset:11776\n" \
"v_mfma_f32_16x16x32_bf16 v[132:135], v[8:11], v[208:211], v[132:135]\n" \
"ds_read_b64_tr_b16 v[56:57], v233 offset:1536\n" \
"v_mfma_f32_16x16x32_bf16 v[132:135], v[12:15], v[212:215], v[132:135]\n" \
"global_load_lds_dwordx4 v229, s[44:45] offset:-3072\n" \
"v_mfma_f32_16x16x32_bf16 v[136:139], v[16:19], v[208:211], v[136:139]\n" \
"s_add_u32 s44, s44, 0x80000\n" \
"s_addc_u32 s45, s45, 0\n" \
"ds_read_b64_tr_b16 v[58:59], v233 offset:3584\n" \
"v_mfma_f32_16x16x32_bf16 v[136:139], v[20:23], v[212:215], v[136:139]\n" \
"ds_read_b64_tr_b16 v[60:61], v233 offset:9728\n" \
"v_mfma_f32_16x16x32_bf16 v[140:143], v[24:27], v[208:211], v[140:143]\n" \
"ds_read_b64_tr_b16 v[62:63], v233 offset:11776\n" \
"v_mfma_f32_16x16x32_bf16 v[140:143], v[28:31], v[212:215], v[140:143]\n" \
"s_waitcnt lgkmcnt(3)\n" \
"global_load_lds_dwordx4 v228, s[42:43] offset:-2048\n" \
"v_mfma_f32_16x16x32_bf16 v[160:163], v[0:3], v[216:219], v[160:163]\n" \
"v_add_u32 v230, 0x8000, v230\n" \
"v_add_u32 v231, 0x8000, v231\n" \
"v_mfma_f32_16x16x32_bf16 v[160:163], v[4:7], v[220:223], v[160:163]\n" \
"v_add_u32 v232, 0x4000, v232\n" \
"v_add_u32 v233, 0x4000, v233\n" \
"v_mfma_f32_16x16x32_bf16 v[164:167], v[8:11], v[216:219], v[164:167]\n" \
"v_mfma_f32_16x16x32_bf16 v[164:167], v[12:15], v[220:223], v[164:167]\n" \
"global_load_lds_dwordx4 v229, s[42:43] offset:-1024\n" \
"v_mfma_f32_16x16x32_bf16 v[168:171], v[16:19], v[216:219], v[168:171]\n" \
"s_add_u32 s42, s42, 0x80000\n" \
"s_addc_u32 s43, s43, 0\n" \
"v_mfma_f32_16x16x32_bf16 v[168:171], v[20:23], v[220:223], v[168:171]\n" \
"v_mfma_f32_16x16x32_bf16 v[172:175], v[24:27], v[216:219], v[172:175]\n" \
"s_waitcnt vmcnt(18) lgkmcnt(0)\n" \
"v_mfma_f32_16x16x32_bf16 v[172:175], v[28:31], v[220:223], v[172:175]\n" \
"s_barrier\n" \
"v_mfma_f32_16x16x32_bf16 v[80:83], v[32:35], v[192:195], v[80:83]\n" \
"ds_read_b64_tr_b16 v[0:1], v232 offset:0\n" \
"v_mfma_f32_16x16x32_bf16 v[80:83], v[36:39], v[196:199], v[80:83]\n" \
"ds_read_b64_tr_b16 v[2:3], v232 offset:2048\n" \
"v_mfma_f32_16x16x32_bf16 v[84:87], v[40:43], v[192:195], v[84:87]\n" \
"s_add_u32 m0, s87, 69632\n" \
"ds_read_b64_tr_b16 v[4:5], v232 offset:8192\n" \
"v_mfma_f32_16x16x32_bf16 v[84:87], v[44:47], v[196:199], v[84:87]\n" \
"global_load_lds_dwordx4 v224, s[48:49] offset:-4096\n" \
"v_mfma_f32_16x16x32_bf16 v[88:91], v[48:51], v[192:195], v[88:91]\n" \
"ds_read_b64_tr_b16 v[6:7], v232 offset:10240\n" \
"v_mfma_f32_16x16x32_bf16 v[88:91], v[52:55], v[196:199], v[88:91]\n" \
"ds_read_b64_tr_b16 v[8:9], v233 offset:0\n" \
"v_mfma_f32_16x16x32_bf16 v[92:95], v[56:59], v[192:195], v[92:95]\n" \
"ds_read_b64_tr_b16 v[10:11], v233 offset:2048\n" \
"v_mfma_f32_16x16x32_bf16 v[92:95], v[60:63], v[196:199], v[92:95]\n" \
"global_load_lds_dwordx4 v225, s[48:49] offset:-3072\n" \
"v_mfma_f32_16x16x32_bf16 v[112:115], v[32:35], v[200:203], v[112:115]\n" \
"ds_read_b128 v[192:195], v230 offset:0\n" \
"v_mfma_f32_16x16x32_bf16 v[112:115], v[36:39], v[204:207], v[112:115]\n" \
"ds_read_b128 v[196:199], v231 offset:0\n" \
"v_mfma_f32_16x16x32_bf16 v[116:119], v[40:43], v[200:203], v[116:119]\n" \
"ds_read_b64_tr_b16 v[12:13], v233 offset:8192\n" \
"v_mfma_f32_16x16x32_bf16 v[116:119], v[44:47], v[204:207], v[116:119]\n" \
"global_load_lds_dwordx4 v226, s[48:49] offset:-2048\n" \
"v_mfma_f32_16x16x32_bf16 v[120:123], v[48:51], v[200:203], v[120:123]\n" \
"ds_read_b64_tr_b16 v[14:15], v233 offset:10240\n" \
"v_mfma_f32_16x16x32_bf16 v[120:123], v[52:55], v[204:207], v[120:123]\n" \
"ds_read_b64_tr_b16 v[16:17], v232 offset:512\n" \
"v_mfma_f32_16x16x32_bf16 v[124:127], v[56:59], v[200:203], v[124:127]\n" \
"ds_read_b64_tr_b16 v[18:19], v232 offset:2560\n" \
"v_mfma_f32_16x16x32_bf16 v[124:127], v[60:63], v[204:207], v[124:127]\n" \
"global_load_lds_dwordx4 v227, s[48:49] offset:-1024\n" \
"v_mfma_f32_16x16x32_bf16 v[144:147], v[32:35], v[208:211], v[144:147]\n" \
"s_add_u32 s48, s48, 0x80\n" \
"s_addc_u32 s49, s49, 0\n" \
"ds_read_b128 v[200:203], v230 offset:2048\n" \
"v_mfma_f32_16x16x32_bf16 v[144:147], v[36:39], v[212:215], v[144:147]\n" \
"ds_read_b128 v[204:207], v231 offset:2048\n" \
"v_mfma_f32_16x16x32_bf16 v[148:151], v[40:43], v[208:211], v[148:151]\n" \
"ds_read_b64_tr_b16 v[20:21], v232 offset:8704\n" \
"v_mfma_f32_16x16x32_bf16 v[148:151], v[44:47], v[212:215], v[148:151]\n" \
"global_load_lds_dwordx4 v224, s[46:47] offset:0\n" \
"v_mfma_f32_16x16x32_bf16 v[152:155], v[48:51], v[208:211], v[152:155]\n" \
"ds_read_b64_tr_b16 v[22:23], v232 offset:10752\n" \
"v_mfma_f32_16x16x32_bf16 v[152:155], v[52:55], v[212:215], v[152:155]\n" \
"ds_read_b64_tr_b16 v[24:25], v233 offset:512\n" \
"v_mfma_f32_16x16x32_bf16 v[156:159], v[56:59], v[208:211], v[156:159]\n" \
"ds_read_b64_tr_b16 v[26:27], v233 offset:2560\n" \
"v_mfma_f32_16x16x32_bf16 v[156:159], v[60:63], v[212:215], v[156:159]\n" \
"global_load_lds_dwordx4 v225, s[46:47] offset:1024\n" \
"v_mfma_f32_16x16x32_bf16 v[176:179], v[32:35], v[216:219], v[176:179]\n" \
"ds_read_b128 v[208:211], v230 offset:4096\n" \
"v_mfma_f32_16x16x32_bf16 v[176:179], v[36:39], v[220:223], v[176:179]\n" \
"ds_read_b128 v[212:215], v231 offset:4096\n" \
"v_mfma_f32_16x16x32_bf16 v[180:183], v[40:43], v[216:219], v[180:183]\n" \
"ds_read_b64_tr_b16 v[28:29], v233 offset:8704\n" \
"v_mfma_f32_16x16x32_bf16 v[180:183], v[44:47], v[220:223], v[180:183]\n" \
"global_load_lds_dwordx4 v226, s[46:47] offset:2048\n" \
"v_mfma_f32_16x16x32_bf16 v[184:187], v[48:51], v[216:219], v[184:187]\n" \
"ds_read_b64_tr_b16 v[30:31], v233 offset:10752\n" \
"v_mfma_f32_16x16x32_bf16 v[184:187], v[52:55], v[220:223], v[184:187]\n" \
"v_mfma_f32_16x16x32_bf16 v[188:191], v[56:59], v[216:219], v[188:191]\n" \
"v_mfma_f32_16x16x32_bf16 v[188:191], v[60:63], v[220:223], v[188:191]\n" \
"s_waitcnt lgkmcnt(1)\n" \
"global_load_lds_dwordx4 v227, s[46:47] offset:3072\n" \
"v_mfma_f32_16x16x32_bf16 v[64:67], v[0:3], v[192:195], v[64:67]\n" \
"s_add_u32 s46, s46, 0x80\n" \
"s_addc_u32 s47, s47, 0\n" \
"ds_read_b128 v[216:219], v230 offset:6144\n" \
"v_mfma_f32_16x16x32_bf16 v[64:67], v[4:7], v[196:199], v[64:67]\n" \
"s_mul_i32 s77, s62, 557824\n" \
"ds_read_b128 v[220:223], v231 offset:6144\n" \
"s_add_u32 s77, s77, s95\n" \
"v_mfma_f32_16x16x32_bf16 v[68:71], v[8:11], v[192:195], v[68:71]\n" \
"s_add_u32 s36, s28, s77\n" \
"s_mov_b32 s37, s29\n" \
"ds_read_b64_tr_b16 v[32:33], v232 offset:1024\n" \
"v_mfma_f32_16x16x32_bf16 v[68:71], v[12:15], v[196:199], v[68:71]\n" \
"buffer_store_dwordx4 a[156:159], v248, s[56:59], s90 offen offset:3072 sc1\n" \
"s_add_u32 s77, s77, 17850368\n" \
"s_add_u32 s38, s28, s77\n" \
"s_mov_b32 s39, s29\n" \
"s_and_b32 s74, s63, 1\n" \
"v_mfma_f32_16x16x32_bf16 v[72:75], v[16:19], v[192:195], v[72:75]\n" \
"s_add_u32 s90, s88, 278528\n" \
"s_lshl_b32 s74, s74, 15\n" \
"ds_read_b64_tr_b16 v[34:35], v232 offset:3072\n" \
"v_mfma_f32_16x16x32_bf16 v[72:75], v[20:23], v[196:199], v[72:75]\n" \
"s_waitcnt lgkmcnt(3)\n" \
"s_xor_b32 s74, s74, 0x18000\n" \
"ds_read_b64_tr_b16 v[36:37], v232 offset:9216\n" \
"v_mfma_f32_16x16x32_bf16 v[76:79], v[24:27], v[192:195], v[76:79]\n" \
"ds_read_b64_tr_b16 v[38:39], v232 offset:11264\n" \
"v_add_u32 v247, s74, v248\n" \
"v_mfma_f32_16x16x32_bf16 v[76:79], v[28:31], v[196:199], v[76:79]\n" \
"buffer_store_dwordx4 a[64:67], v249, s[56:59], s90 offen offset:0 sc1\n" \
"s_add_u32 s74, s74, s95\n" \
"s_lshr_b32 s83, s61, 1\n" \
"s_lshl_b32 s75, s63, 2\n" \
"s_add_u32 s77, s83, s75\n" \
"v_mfma_f32_16x16x32_bf16 v[96:99], v[0:3], v[200:203], v[96:99]\n" \
"s_mul_i32 s77, s77, 266240\n" \
"s_sub_u32 s78, s83, s75\n" \
"ds_read_b64_tr_b16 v[40:41], v233 offset:1024\n" \
"v_mfma_f32_16x16x32_bf16 v[96:99], v[4:7], v[204:207], v[96:99]\n" \
"s_add_u32 s78, s78, 60\n" \
"s_mul_i32 s78, s78, 266240\n" \
"ds_read_b64_tr_b16 v[42:43], v233 offset:3072\n" \
"v_mfma_f32_16x16x32_bf16 v[100:103], v[8:11], v[200:203], v[100:103]\n" \
"s_and_b32 s82, s61, 1\n" \
"ds_read_b128 a[64:67], v254\n" \
"ds_read_b64_tr_b16 v[44:45], v233 offset:9216\n" \
"v_mfma_f32_16x16x32_bf16 v[100:103], v[12:15], v[204:207], v[100:103]\n" \
"buffer_load_dword v244, v245, s[56:59], s91 offen sc1\n" \
"v_mfma_f32_16x16x32_bf16 v[104:107], v[16:19], v[200:203], v[104:107]\n" \
"s_cmp_eq_u32 s82, 0\n" \
"s_cselect_b32 s16, 0, 0xff00\n" \
"ds_read_b64_tr_b16 v[46:47], v233 offset:11264\n" \
"v_mfma_f32_16x16x32_bf16 v[104:107], v[20:23], v[204:207], v[104:107]\n" \
"s_mov_b32 s17, s16\n" \
"s_lshl_b32 s75, s82, 8\n" \
"ds_read_b64_tr_b16 v[48:49], v232 offset:1536\n" \
"v_mfma_f32_16x16x32_bf16 v[108:111], v[24:27], v[200:203], v[108:111]\n" \
"s_add_u32 m0, s95, 36864\n" \
"s_lshl_b32 s76, s62, 13\n" \
"ds_read_b64_tr_b16 v[50:51], v232 offset:3584\n" \
"v_mfma_f32_16x16x32_bf16 v[108:111], v[28:31], v[204:207], v[108:111]\n" \
"s_add_u32 s75, s75, s76\n" \
"ds_read_b128 a[68:71], v254\n" \
"global_load_lds_dwordx4 v228, s[44:45] offset:-4096\n" \
"v_mfma_f32_16x16x32_bf16 v[128:131], v[0:3], v[208:211], v[128:131]\n" \
"s_add_u32 s75, s75, 35700736\n" \
"s_add_u32 s77, s77, s75\n" \
"ds_read_b64_tr_b16 v[52:53], v232 offset:9728\n" \
"v_mfma_f32_16x16x32_bf16 v[128:131], v[4:7], v[212:215], v[128:131]\n" \
"s_add_u32 s78, s78, s75\n" \
"s_mul_i32 s83, s83, 9216\n" \
"ds_read_b64_tr_b16 v[54:55], v232 offset:11776\n" \
"v_mfma_f32_16x16x32_bf16 v[132:135], v[8:11], v[208:211], v[132:135]\n" \
"s_lshl_b32 s75, s82, 12\n" \
"s_add_u32 s83, s83, s75\n" \
"ds_read_b64_tr_b16 v[56:57], v233 offset:1536\n" \
"v_mfma_f32_16x16x32_bf16 v[132:135], v[12:15], v[212:215], v[132:135]\n" \
"s_add_u32 s83, s83, 4096\n" \
"global_load_lds_dwordx4 v229, s[44:45] offset:-3072\n" \
"v_mfma_f32_16x16x32_bf16 v[136:139], v[16:19], v[208:211], v[136:139]\n" \
"s_add_u32 s44, s44, 0x80000\n" \
"s_addc_u32 s45, s45, 0\n" \
"ds_read_b64_tr_b16 v[58:59], v233 offset:3584\n" \
"v_mfma_f32_16x16x32_bf16 v[136:139], v[20:23], v[212:215], v[136:139]\n" \
"s_lshl_b32 s82, s82, 14\n" \
"ds_read_b64_tr_b16 v[60:61], v233 offset:9728\n" \
"v_mfma_f32_16x16x32_bf16 v[140:143], v[24:27], v[208:211], v[140:143]\n" \
"s_cmp_eq_u32 s63, 7\n" \
"s_cselect_b32 s82, 0, s82\n" \
"ds_read_b64_tr_b16 v[62:63], v233 offset:11776\n" \
"v_mfma_f32_16x16x32_bf16 v[140:143], v[28:31], v[212:215], v[140:143]\n" \
"s_waitcnt lgkmcnt(3)\n" \
"global_load_lds_dwordx4 v228, s[42:43] offset:-2048\n" \
"s_mov_b32 s75, s63\n" \
"v_mfma_f32_16x16x32_bf16 v[160:163], v[0:3], v[216:219], v[160:163]\n" \
"s_sub_u32 s76, 7, s63\n" \
"s_mov_b32 s73, s95\n" \
"s_mov_b64 s[40:41], s[14:15]\n" \
"v_mfma_f32_16x16x32_bf16 v[160:163], v[4:7], v[220:223], v[160:163]\n" \
"s_mov_b32 s80, 0\n" \
"v_add_u32 v230, 0x8000, v230\n" \
"s_mov_b32 s81, 0\n" \
"v_mfma_f32_16x16x32_bf16 v[164:167], v[8:11], v[216:219], v[164:167]\n" \
"s_cmp_eq_u32 s63, 7\n" \
"s_cselect_b64 s[40:41], s[18:19], s[40:41]\n" \
"s_cselect_b32 s80, 12, s80\n" \
"v_mfma_f32_16x16x32_bf16 v[164:167], v[12:15], v[220:223], v[164:167]\n" \
"global_load_lds_dwordx4 v229, s[42:43] offset:-1024\n" \
"v_add_u32 v231, 0x8000, v231\n" \
"v_mfma_f32_16x16x32_bf16 v[168:171], v[16:19], v[216:219], v[168:171]\n" \
"s_add_u32 s42, s42, 0x80000\n" \
"s_addc_u32 s43, s43, 0\n" \
"v_add_u32 v232, 0xffff4000, v232\n" \
"v_mfma_f32_16x16x32_bf16 v[168:171], v[20:23], v[220:223], v[168:171]\n" \
"s_cmp_eq_u32 s63, 6\n" \
"s_cselect_b64 s[40:41], s[22:23], s[40:41]\n" \
"s_cselect_b32 s80, 12, s80\n" \
"v_mfma_f32_16x16x32_bf16 v[172:175], v[24:27], v[216:219], v[172:175]\n" \
"s_waitcnt vmcnt(20) lgkmcnt(0)\n" \
"v_add_u32 v233, 0xffff4000, v233\n" \
"v_mfma_f32_16x16x32_bf16 v[172:175], v[28:31], v[220:223], v[172:175]\n" \
"s_barrier\n" \
"v_mfma_f32_16x16x32_bf16 v[80:83], v[32:35], v[192:195], v[80:83]\n" \
"ds_read_b64_tr_b16 v[0:1], v232 offset:0\n" \
"v_mfma_f32_16x16x32_bf16 v[80:83], v[36:39], v[196:199], v[80:83]\n" \
"ds_read_b64_tr_b16 v[2:3], v232 offset:2048\n" \
"v_mfma_f32_16x16x32_bf16 v[84:87], v[40:43], v[192:195], v[84:87]\n" \
"s_add_u32 m0, s87, 102400\n" \
"ds_read_b64_tr_b16 v[4:5], v232 offset:8192\n" \
"v_mfma_f32_16x16x32_bf16 v[84:87], v[44:47], v[196:199], v[84:87]\n" \
"global_load_lds_dwordx4 v224, s[48:49] offset:-4096\n" \
"v_mfma_f32_16x16x32_bf16 v[88:91], v[48:51], v[192:195], v[88:91]\n" \
"ds_read_b64_tr_b16 v[6:7], v232 offset:10240\n" \
"v_mfma_f32_16x16x32_bf16 v[88:91], v[52:55], v[196:199], v[88:91]\n" \
"ds_read_b64_tr_b16 v[8:9], v233 offset:0\n" \
"v_mfma_f32_16x16x32_bf16 v[92:95], v[56:59], v[192:195], v[92:95]\n" \
"ds_read_b64_tr_b16 v[10:11], v233 offset:2048\n" \
"v_mfma_f32_16x16x32_bf16 v[92:95], v[60:63], v[196:199], v[92:95]\n" \
"global_load_lds_dwordx4 v225, s[48:49] offset:-3072\n" \
"v_mfma_f32_16x16x32_bf16 v[112:115], v[32:35], v[200:203], v[112:115]\n" \
"ds_read_b128 v[192:195], v230 offset:0\n" \
"v_mfma_f32_16x16x32_bf16 v[112:115], v[36:39], v[204:207], v[112:115]\n" \
"ds_read_b128 v[196:199], v231 offset:0\n" \
"v_mfma_f32_16x16x32_bf16 v[116:119], v[40:43], v[200:203], v[116:119]\n" \
"ds_read_b64_tr_b16 v[12:13], v233 offset:8192\n" \
"v_mfma_f32_16x16x32_bf16 v[116:119], v[44:47], v[204:207], v[116:119]\n" \
"global_load_lds_dwordx4 v226, s[48:49] offset:-2048\n" \
"v_mfma_f32_16x16x32_bf16 v[120:123], v[48:51], v[200:203], v[120:123]\n" \
"ds_read_b64_tr_b16 v[14:15], v233 offset:10240\n" \
"v_mfma_f32_16x16x32_bf16 v[120:123], v[52:55], v[204:207], v[120:123]\n" \
"ds_read_b64_tr_b16 v[16:17], v232 offset:512\n" \
"v_mfma_f32_16x16x32_bf16 v[124:127], v[56:59], v[200:203], v[124:127]\n" \
"ds_read_b64_tr_b16 v[18:19], v232 offset:2560\n" \
"v_mfma_f32_16x16x32_bf16 v[124:127], v[60:63], v[204:207], v[124:127]\n" \
"global_load_lds_dwordx4 v227, s[48:49] offset:-1024\n" \
"v_mfma_f32_16x16x32_bf16 v[144:147], v[32:35], v[208:211], v[144:147]\n" \
"s_add_u32 s48, s48, 0x80\n" \
"s_addc_u32 s49, s49, 0\n" \
"ds_read_b128 v[200:203], v230 offset:2048\n" \
"v_mfma_f32_16x16x32_bf16 v[144:147], v[36:39], v[212:215], v[144:147]\n" \
"ds_read_b128 v[204:207], v231 offset:2048\n" \
"v_mfma_f32_16x16x32_bf16 v[148:151], v[40:43], v[208:211], v[148:151]\n" \
"ds_read_b64_tr_b16 v[20:21], v232 offset:8704\n" \
"v_mfma_f32_16x16x32_bf16 v[148:151], v[44:47], v[212:215], v[148:151]\n" \
"global_load_lds_dwordx4 v224, s[46:47] offset:0\n" \
"v_mfma_f32_16x16x32_bf16 v[152:155], v[48:51], v[208:211], v[152:155]\n" \
"ds_read_b64_tr_b16 v[22:23], v232 offset:10752\n" \
"v_mfma_f32_16x16x32_bf16 v[152:155], v[52:55], v[212:215], v[152:155]\n" \
"ds_read_b64_tr_b16 v[24:25], v233 offset:512\n" \
"v_mfma_f32_16x16x32_bf16 v[156:159], v[56:59], v[208:211], v[156:159]\n" \
"ds_read_b64_tr_b16 v[26:27], v233 offset:2560\n" \
"v_mfma_f32_16x16x32_bf16 v[156:159], v[60:63], v[212:215], v[156:159]\n" \
"global_load_lds_dwordx4 v225, s[46:47] offset:1024\n" \
"v_mfma_f32_16x16x32_bf16 v[176:179], v[32:35], v[216:219], v[176:179]\n" \
"ds_read_b128 v[208:211], v230 offset:4096\n" \
"v_mfma_f32_16x16x32_bf16 v[176:179], v[36:39], v[220:223], v[176:179]\n" \
"ds_read_b128 v[212:215], v231 offset:4096\n" \
"v_mfma_f32_16x16x32_bf16 v[180:183], v[40:43], v[216:219], v[180:183]\n" \
"ds_read_b64_tr_b16 v[28:29], v233 offset:8704\n" \
"v_mfma_f32_16x16x32_bf16 v[180:183], v[44:47], v[220:223], v[180:183]\n" \
"global_load_lds_dwordx4 v226, s[46:47] offset:2048\n" \
"v_mfma_f32_16x16x32_bf16 v[184:187], v[48:51], v[216:219], v[184:187]\n" \
"ds_read_b64_tr_b16 v[30:31], v233 offset:10752\n" \
"v_mfma_f32_16x16x32_bf16 v[184:187], v[52:55], v[220:223], v[184:187]\n" \
"s_cmp_eq_u32 s63, 5\n" \
"s_cselect_b64 s[40:41], s[18:19], s[40:41]\n" \
"Lpg4357_%=:\n" \
"s_cbranch_execz Lpg5544_%=\n" \
"s_cselect_b32 s80, 4, s80\n" \
"v_mfma_f32_16x16x32_bf16 v[188:191], v[56:59], v[216:219], v[188:191]\n" \
"s_cmp_eq_u32 s63, 5\n" \
"s_cselect_b32 s81, 384, s81\n" \
"v_mfma_f32_16x16x32_bf16 v[188:191], v[60:63], v[220:223], v[188:191]\n" \
"s_waitcnt lgkmcnt(1)\n" \
"global_load_lds_dwordx4 v227, s[46:47] offset:3072\n" \
"v_mfma_f32_16x16x32_bf16 v[64:67], v[0:3], v[192:195], v[64:67]\n" \
"s_add_u32 s46, s46, 0x80\n" \
"s_addc_u32 s47, s47, 0\n" \
"ds_read_b128 v[216:219], v230 offset:6144\n" \
"v_mfma_f32_16x16x32_bf16 v[64:67], v[4:7], v[196:199], v[64:67]\n" \
"ds_read_b128 v[220:223], v231 offset:6144\n" \
"v_mfma_f32_16x16x32_bf16 v[68:71], v[8:11], v[192:195], v[68:71]\n" \
"ds_read_b64_tr_b16 v[32:33], v232 offset:1024\n" \
"v_mfma_f32_16x16x32_bf16 v[68:71], v[12:15], v[196:199], v[68:71]\n" \
"buffer_store_dwordx4 a[96:99], v249, s[56:59], s90 offen offset:256 sc1\n" \
"s_cmp_eq_u32 s63, 4\n" \
"s_cselect_b64 s[40:41], s[20:21], s[40:41]\n" \
"s_cselect_b32 s80, 8, s80\n" \
"v_mfma_f32_16x16x32_bf16 v[72:75], v[16:19], v[192:195], v[72:75]\n" \
"ds_read_b64_tr_b16 v[34:35], v232 offset:3072\n" \
"v_mfma_f32_16x16x32_bf16 v[72:75], v[20:23], v[196:199], v[72:75]\n" \
"s_waitcnt lgkmcnt(3)\n" \
"ds_read_b64_tr_b16 v[36:37], v232 offset:9216\n" \
"v_mfma_f32_16x16x32_bf16 v[76:79], v[24:27], v[192:195], v[76:79]\n" \
"ds_read_b128 a[96:99], v254\n" \
"ds_read_b64_tr_b16 v[38:39], v232 offset:11264\n" \
"v_mfma_f32_16x16x32_bf16 v[76:79], v[28:31], v[196:199], v[76:79]\n" \
"buffer_store_dwordx4 a[72:75], v249, s[56:59], s90 offen offset:2048 sc1\n" \
"s_cmp_eq_u32 s63, 2\n" \
"s_cselect_b64 s[40:41], s[24:25], s[40:41]\n" \
"s_cselect_b32 s80, 4, s80\n" \
"s_add_u32 s81, s81, s62\n" \
"v_mfma_f32_16x16x32_bf16 v[96:99], v[0:3], v[200:203], v[96:99]\n" \
"s_lshl_b32 s81, s81, 2\n" \
"s_add_u32 s81, s81, s61\n" \
"ds_read_b64_tr_b16 v[40:41], v233 offset:1024\n" \
"v_mfma_f32_16x16x32_bf16 v[96:99], v[4:7], v[204:207], v[96:99]\n" \
"s_lshl_b32 s81, s81, 12\n" \
"ds_read_b128 a[100:103], v254\n" \
"ds_read_b64_tr_b16 v[42:43], v233 offset:3072\n" \
"v_mfma_f32_16x16x32_bf16 v[100:103], v[8:11], v[200:203], v[100:103]\n" \
"s_add_u32 s40, s40, s81\n" \
"s_addc_u32 s41, s41, 0\n" \
"ds_read_b64_tr_b16 v[44:45], v233 offset:9216\n" \
"v_mfma_f32_16x16x32_bf16 v[100:103], v[12:15], v[204:207], v[100:103]\n" \
"s_min_u32 s81, s80, 1\n" \
"s_lshl_b32 s81, s81, 2\n" \
"v_mul_u32_u24 v252, s81, v248\n" \
"v_mfma_f32_16x16x32_bf16 v[104:107], v[16:19], v[200:203], v[104:107]\n" \
"ds_read_b128 a[72:75], v254\n" \
"ds_read_b64_tr_b16 v[46:47], v233 offset:11264\n" \
"v_mfma_f32_16x16x32_bf16 v[104:107], v[20:23], v[204:207], v[104:107]\n" \
"ds_read_b128 a[76:79], v254\n" \
"ds_read_b64_tr_b16 v[48:49], v232 offset:1536\n" \
"v_mfma_f32_16x16x32_bf16 v[108:111], v[24:27], v[200:203], v[108:111]\n" \
"s_add_u32 m0, s95, 53248\n" \
"ds_read_b64_tr_b16 v[50:51], v232 offset:3584\n" \
"v_mfma_f32_16x16x32_bf16 v[108:111], v[28:31], v[204:207], v[108:111]\n" \
"global_load_lds_dwordx4 v228, s[44:45] offset:-4096\n" \
"v_mfma_f32_16x16x32_bf16 v[128:131], v[0:3], v[208:211], v[128:131]\n" \
"ds_read_b64_tr_b16 v[52:53], v232 offset:9728\n" \
"v_mfma_f32_16x16x32_bf16 v[128:131], v[4:7], v[212:215], v[128:131]\n" \
"ds_read_b64_tr_b16 v[54:55], v232 offset:11776\n" \
"v_mfma_f32_16x16x32_bf16 v[132:135], v[8:11], v[208:211], v[132:135]\n" \
"ds_read_b64_tr_b16 v[56:57], v233 offset:1536\n" \
"v_mfma_f32_16x16x32_bf16 v[132:135], v[12:15], v[212:215], v[132:135]\n" \
"global_load_lds_dwordx4 v229, s[44:45] offset:-3072\n" \
"v_mfma_f32_16x16x32_bf16 v[136:139], v[16:19], v[208:211], v[136:139]\n" \
"ds_read_b64_tr_b16 v[58:59], v233 offset:3584\n" \
"v_mfma_f32_16x16x32_bf16 v[136:139], v[20:23], v[212:215], v[136:139]\n" \
"ds_read_b64_tr_b16 v[60:61], v233 offset:9728\n" \
"v_mfma_f32_16x16x32_bf16 v[140:143], v[24:27], v[208:211], v[140:143]\n" \
"ds_read_b64_tr_b16 v[62:63], v233 offset:11776\n" \
"v_mfma_f32_16x16x32_bf16 v[140:143], v[28:31], v[212:215], v[140:143]\n" \
"s_waitcnt lgkmcnt(3)\n" \
"global_load_lds_dwordx4 v228, s[42:43] offset:-2048\n" \
"v_mfma_f32_16x16x32_bf16 v[160:163], v[0:3], v[216:219], v[160:163]\n" \
"v_add_u32 v230, 0xffff0000, v230\n" \
"v_add_u32 v231, 0xffff0000, v231\n" \
"v_mfma_f32_16x16x32_bf16 v[160:163], v[4:7], v[220:223], v[160:163]\n" \
"v_add_u32 v232, 0x4000, v232\n" \
"v_add_u32 v233, 0x4000, v233\n" \
"v_mfma_f32_16x16x32_bf16 v[164:167], v[8:11], v[216:219], v[164:167]\n" \
"v_mfma_f32_16x16x32_bf16 v[164:167], v[12:15], v[220:223], v[164:167]\n" \
"global_load_lds_dwordx4 v229, s[42:43] offset:-1024\n" \
"v_mfma_f32_16x16x32_bf16 v[168:171], v[16:19], v[216:219], v[168:171]\n" \
"v_mfma_f32_16x16x32_bf16 v[168:171], v[20:23], v[220:223], v[168:171]\n" \
"v_mfma_f32_16x16x32_bf16 v[172:175], v[24:27], v[216:219], v[172:175]\n" \
"s_waitcnt vmcnt(21) lgkmcnt(0)\n" \
"v_mfma_f32_16x16x32_bf16 v[172:175], v[28:31], v[220:223], v[172:175]\n" \
"s_barrier\n" \
"v_mfma_f32_16x16x32_bf16 v[80:83], v[32:35], v[192:195], v[80:83]\n" \
"s_waitcnt vmcnt(18)\n" \
"ds_read_b64_tr_b16 v[0:1], v232 offset:0\n" \
"v_cmp_ne_u32 vcc, s60, v244\n" \
"v_mfma_f32_16x16x32_bf16 v[80:83], v[36:39], v[196:199], v[80:83]\n" \
"ds_read_b64_tr_b16 v[2:3], v232 offset:2048\n" \
"v_mfma_f32_16x16x32_bf16 v[84:87], v[40:43], v[192:195], v[84:87]\n" \
"s_add_u32 m0, s87, 135168\n" \
"ds_read_b64_tr_b16 v[4:5], v232 offset:8192\n" \
"v_mfma_f32_16x16x32_bf16 v[84:87], v[44:47], v[196:199], v[84:87]\n" \
"s_and_b32 vcc_hi, vcc_lo, 255\n" \
"s_cbranch_scc1 Lpl1_%=\n" \
"Lpr1_%=:\n" \
"global_load_lds_dwordx4 v224, s[48:49] offset:-4096\n" \
"v_mfma_f32_16x16x32_bf16 v[88:91], v[48:51], v[192:195], v[88:91]\n" \
"s_add_u32 s79, s91, 12288\n" \
"ds_read_b64_tr_b16 v[6:7], v232 offset:10240\n" \
"v_mfma_f32_16x16x32_bf16 v[88:91], v[52:55], v[196:199], v[88:91]\n" \
"ds_read_b64_tr_b16 v[8:9], v233 offset:0\n" \
"v_mfma_f32_16x16x32_bf16 v[92:95], v[56:59], v[192:195], v[92:95]\n" \
"ds_read_b64_tr_b16 v[10:11], v233 offset:2048\n" \
"v_mfma_f32_16x16x32_bf16 v[92:95], v[60:63], v[196:199], v[92:95]\n" \
"global_load_lds_dwordx4 v225, s[48:49] offset:-3072\n" \
"v_mfma_f32_16x16x32_bf16 v[112:115], v[32:35], v[200:203], v[112:115]\n" \
"ds_read_b128 v[192:195], v230 offset:0\n" \
"v_mfma_f32_16x16x32_bf16 v[112:115], v[36:39], v[204:207], v[112:115]\n" \
"ds_read_b128 v[196:199], v231 offset:0\n" \
"v_mfma_f32_16x16x32_bf16 v[116:119], v[40:43], v[200:203], v[116:119]\n" \
"ds_read_b64_tr_b16 v[12:13], v233 offset:8192\n" \
"v_mfma_f32_16x16x32_bf16 v[116:119], v[44:47], v[204:207], v[116:119]\n" \
"global_load_lds_dwordx4 v226, s[48:49] offset:-2048\n" \
"v_mfma_f32_16x16x32_bf16 v[120:123], v[48:51], v[200:203], v[120:123]\n" \
"ds_read_b64_tr_b16 v[14:15], v233 offset:10240\n" \
"v_mfma_f32_16x16x32_bf16 v[120:123], v[52:55], v[204:207], v[120:123]\n" \
"ds_read_b64_tr_b16 v[16:17], v232 offset:512\n" \
"v_mfma_f32_16x16x32_bf16 v[124:127], v[56:59], v[200:203], v[124:127]\n" \
"ds_read_b64_tr_b16 v[18:19], v232 offset:2560\n" \
"v_mfma_f32_16x16x32_bf16 v[124:127], v[60:63], v[204:207], v[124:127]\n" \
"global_load_lds_dwordx4 v227, s[48:49] offset:-1024\n" \
"v_mfma_f32_16x16x32_bf16 v[144:147], v[32:35], v[208:211], v[144:147]\n" \
"ds_read_b128 v[200:203], v230 offset:2048\n" \
"v_mfma_f32_16x16x32_bf16 v[144:147], v[36:39], v[212:215], v[144:147]\n" \
"ds_read_b128 v[204:207], v231 offset:2048\n" \
"v_mfma_f32_16x16x32_bf16 v[148:151], v[40:43], v[208:211], v[148:151]\n" \
"ds_read_b64_tr_b16 v[20:21], v232 offset:8704\n" \
"v_mfma_f32_16x16x32_bf16 v[148:151], v[44:47], v[212:215], v[148:151]\n" \
"global_load_lds_dwordx4 v224, s[46:47] offset:0\n" \
"v_mfma_f32_16x16x32_bf16 v[152:155], v[48:51], v[208:211], v[152:155]\n" \
"ds_read_b64_tr_b16 v[22:23], v232 offset:10752\n" \
"v_mfma_f32_16x16x32_bf16 v[152:155], v[52:55], v[212:215], v[152:155]\n" \
"ds_read_b64_tr_b16 v[24:25], v233 offset:512\n" \
"v_mfma_f32_16x16x32_bf16 v[156:159], v[56:59], v[208:211], v[156:159]\n" \
"ds_read_b64_tr_b16 v[26:27], v233 offset:2560\n" \
"v_mfma_f32_16x16x32_bf16 v[156:159], v[60:63], v[212:215], v[156:159]\n" \
"global_load_lds_dwordx4 v225, s[46:47] offset:1024\n" \
"v_mfma_f32_16x16x32_bf16 v[176:179], v[32:35], v[216:219], v[176:179]\n" \
"ds_read_b128 v[208:211], v230 offset:4096\n" \
"v_mfma_f32_16x16x32_bf16 v[176:179], v[36:39], v[220:223], v[176:179]\n" \
"ds_read_b128 v[212:215], v231 offset:4096\n" \
"v_mfma_f32_16x16x32_bf16 v[180:183], v[40:43], v[216:219], v[180:183]\n" \
"ds_read_b64_tr_b16 v[28:29], v233 offset:8704\n" \
"v_mfma_f32_16x16x32_bf16 v[180:183], v[44:47], v[220:223], v[180:183]\n" \
"global_load_lds_dwordx4 v226, s[46:47] offset:2048\n" \
"v_mfma_f32_16x16x32_bf16 v[184:187], v[48:51], v[216:219], v[184:187]\n" \
"ds_read_b64_tr_b16 v[30:31], v233 offset:10752\n" \
"v_mfma_f32_16x16x32_bf16 v[184:187], v[52:55], v[220:223], v[184:187]\n" \
"v_mfma_f32_16x16x32_bf16 v[188:191], v[56:59], v[216:219], v[188:191]\n" \
"v_mfma_f32_16x16x32_bf16 v[188:191], v[60:63], v[220:223], v[188:191]\n" \
"s_waitcnt lgkmcnt(1)\n" \
"global_load_lds_dwordx4 v227, s[46:47] offset:3072\n" \
"v_mfma_f32_16x16x32_bf16 v[64:67], v[0:3], v[192:195], v[64:67]\n" \
"ds_read_b128 v[216:219], v230 offset:6144\n" \
"v_mfma_f32_16x16x32_bf16 v[64:67], v[4:7], v[196:199], v[64:67]\n" \
"ds_read_b128 v[220:223], v231 offset:6144\n" \
"v_mfma_f32_16x16x32_bf16 v[68:71], v[8:11], v[192:195], v[68:71]\n" \
"ds_read_b64_tr_b16 v[32:33], v232 offset:1024\n" \
"v_mfma_f32_16x16x32_bf16 v[68:71], v[12:15], v[196:199], v[68:71]\n" \
"buffer_store_dwordx4 a[104:107], v249, s[56:59], s90 offen offset:2304 sc1\n" \
"v_mfma_f32_16x16x32_bf16 v[72:75], v[16:19], v[192:195], v[72:75]\n" \
"s_add_u32 s90, s89, 278528\n" \
"ds_read_b64_tr_b16 v[34:35], v232 offset:3072\n" \
"v_mfma_f32_16x16x32_bf16 v[72:75], v[20:23], v[196:199], v[72:75]\n" \
"s_waitcnt lgkmcnt(3)\n" \
"ds_read_b64_tr_b16 v[36:37], v232 offset:9216\n" \
"v_mfma_f32_16x16x32_bf16 v[76:79], v[24:27], v[192:195], v[76:79]\n" \
"ds_read_b128 a[104:107], v254\n" \
"ds_read_b64_tr_b16 v[38:39], v232 offset:11264\n" \
"v_mfma_f32_16x16x32_bf16 v[76:79], v[28:31], v[196:199], v[76:79]\n" \
"buffer_store_dwordx4 a[80:83], v248, s[56:59], s90 offen offset:0 sc1\n" \
"ds_read_b128 a[108:111], v254\n" \
"v_mfma_f32_16x16x32_bf16 v[96:99], v[0:3], v[200:203], v[96:99]\n" \
"ds_read_b64_tr_b16 v[40:41], v233 offset:1024\n" \
"v_mfma_f32_16x16x32_bf16 v[96:99], v[4:7], v[204:207], v[96:99]\n" \
"ds_read_b64_tr_b16 v[42:43], v233 offset:3072\n" \
"v_mfma_f32_16x16x32_bf16 v[100:103], v[8:11], v[200:203], v[100:103]\n" \
"ds_read_b128 a[80:83], v254\n" \
"ds_read_b64_tr_b16 v[44:45], v233 offset:9216\n" \
"v_mfma_f32_16x16x32_bf16 v[100:103], v[12:15], v[204:207], v[100:103]\n" \
"buffer_store_dwordx4 a[84:87], v248, s[56:59], s90 offen offset:1024 sc1\n" \
"ds_read_b128 a[112:115], v254\n" \
"v_mfma_f32_16x16x32_bf16 v[104:107], v[16:19], v[200:203], v[104:107]\n" \
"ds_read_b64_tr_b16 v[46:47], v233 offset:11264\n" \
"v_mfma_f32_16x16x32_bf16 v[104:107], v[20:23], v[204:207], v[104:107]\n" \
"ds_read_b64_tr_b16 v[48:49], v232 offset:1536\n" \
"v_mfma_f32_16x16x32_bf16 v[108:111], v[24:27], v[200:203], v[108:111]\n" \
"s_add_u32 m0, s95, 0\n" \
"ds_read_b128 a[84:87], v254\n" \
"ds_read_b64_tr_b16 v[50:51], v232 offset:3584\n" \
"v_mfma_f32_16x16x32_bf16 v[108:111], v[28:31], v[204:207], v[108:111]\n" \
"ds_read_b128 a[116:119], v254\n" \
"global_load_lds_dwordx4 v250, s[36:37] offset:0 sc1\n" \
"v_mfma_f32_16x16x32_bf16 v[128:131], v[0:3], v[208:211], v[128:131]\n" \
"ds_read_b64_tr_b16 v[52:53], v232 offset:9728\n" \
"v_mfma_f32_16x16x32_bf16 v[128:131], v[4:7], v[212:215], v[128:131]\n" \
"ds_read_b64_tr_b16 v[54:55], v232 offset:11776\n" \
"v_mfma_f32_16x16x32_bf16 v[132:135], v[8:11], v[208:211], v[132:135]\n" \
"ds_read_b64_tr_b16 v[56:57], v233 offset:1536\n" \
"v_mfma_f32_16x16x32_bf16 v[132:135], v[12:15], v[212:215], v[132:135]\n" \
"global_load_lds_dwordx4 v250, s[36:37] offset:1024 sc1\n" \
"v_mfma_f32_16x16x32_bf16 v[136:139], v[16:19], v[208:211], v[136:139]\n" \
"ds_read_b64_tr_b16 v[58:59], v233 offset:3584\n" \
"v_mfma_f32_16x16x32_bf16 v[136:139], v[20:23], v[212:215], v[136:139]\n" \
"ds_read_b64_tr_b16 v[60:61], v233 offset:9728\n" \
"v_mfma_f32_16x16x32_bf16 v[140:143], v[24:27], v[208:211], v[140:143]\n" \
"ds_read_b64_tr_b16 v[62:63], v233 offset:11776\n" \
"v_mfma_f32_16x16x32_bf16 v[140:143], v[28:31], v[212:215], v[140:143]\n" \
"s_waitcnt lgkmcnt(3)\n" \
"global_load_lds_dwordx4 v250, s[36:37] offset:2048 sc1\n" \
"v_mfma_f32_16x16x32_bf16 v[160:163], v[0:3], v[216:219], v[160:163]\n" \
"v_add_u32 v230, 0x8000, v230\n" \
"v_add_u32 v231, 0x8000, v231\n" \
"v_mfma_f32_16x16x32_bf16 v[160:163], v[4:7], v[220:223], v[160:163]\n" \
"v_add_u32 v232, 0x4000, v232\n" \
"v_add_u32 v233, 0x4000, v233\n" \
"v_mfma_f32_16x16x32_bf16 v[164:167], v[8:11], v[216:219], v[164:167]\n" \
"v_mfma_f32_16x16x32_bf16 v[164:167], v[12:15], v[220:223], v[164:167]\n" \
"global_load_lds_dwordx4 v250, s[36:37] offset:3072 sc1\n" \
"v_mfma_f32_16x16x32_bf16 v[168:171], v[16:19], v[216:219], v[168:171]\n" \
"s_add_u32 s36, s36, 17408\n" \
"v_mfma_f32_16x16x32_bf16 v[168:171], v[20:23], v[220:223], v[168:171]\n" \
"v_mfma_f32_16x16x32_bf16 v[172:175], v[24:27], v[216:219], v[172:175]\n" \
"s_waitcnt vmcnt(21) lgkmcnt(0)\n" \
"v_mfma_f32_16x16x32_bf16 v[172:175], v[28:31], v[220:223], v[172:175]\n" \
"s_barrier\n" \
"v_mfma_f32_16x16x32_bf16 v[80:83], v[32:35], v[192:195], v[80:83]\n" \
"ds_read_b64_tr_b16 v[0:1], v232 offset:0\n" \
"v_mfma_f32_16x16x32_bf16 v[80:83], v[36:39], v[196:199], v[80:83]\n" \
"ds_read_b64_tr_b16 v[2:3], v232 offset:2048\n" \
"v_mfma_f32_16x16x32_bf16 v[84:87], v[40:43], v[192:195], v[84:87]\n" \
"s_add_u32 m0, s95, 16384\n" \
"ds_read_b64_tr_b16 v[4:5], v232 offset:8192\n" \
"v_mfma_f32_16x16x32_bf16 v[84:87], v[44:47], v[196:199], v[84:87]\n" \
"global_load_lds_dwordx4 v250, s[36:37] offset:0 sc1\n" \
"v_mfma_f32_16x16x32_bf16 v[88:91], v[48:51], v[192:195], v[88:91]\n" \
"ds_read_b64_tr_b16 v[6:7], v232 offset:10240\n" \
"v_mfma_f32_16x16x32_bf16 v[88:91], v[52:55], v[196:199], v[88:91]\n" \
"ds_read_b64_tr_b16 v[8:9], v233 offset:0\n" \
"v_mfma_f32_16x16x32_bf16 v[92:95], v[56:59], v[192:195], v[92:95]\n" \
"ds_read_b64_tr_b16 v[10:11], v233 offset:2048\n" \
"v_mfma_f32_16x16x32_bf16 v[92:95], v[60:63], v[196:199], v[92:95]\n" \
"global_load_lds_dwordx4 v250, s[36:37] offset:1024 sc1\n" \
"v_mfma_f32_16x16x32_bf16 v[112:115], v[32:35], v[200:203], v[112:115]\n" \
"ds_read_b128 v[192:195], v230 offset:0\n" \
"v_mfma_f32_16x16x32_bf16 v[112:115], v[36:39], v[204:207], v[112:115]\n" \
"ds_read_b128 v[196:199], v231 offset:0\n" \
"v_mfma_f32_16x16x32_bf16 v[116:119], v[40:43], v[200:203], v[116:119]\n" \
"ds_read_b64_tr_b16 v[12:13], v233 offset:8192\n" \
"v_mfma_f32_16x16x32_bf16 v[116:119], v[44:47], v[204:207], v[116:119]\n" \
"global_load_lds_dwordx4 v250, s[36:37] offset:2048 sc1\n" \
"v_mfma_f32_16x16x32_bf16 v[120:123], v[48:51], v[200:203], v[120:123]\n" \
"ds_read_b64_tr_b16 v[14:15], v233 offset:10240\n" \
"v_mfma_f32_16x16x32_bf16 v[120:123], v[52:55], v[204:207], v[120:123]\n" \
"ds_read_b64_tr_b16 v[16:17], v232 offset:512\n" \
"v_mfma_f32_16x16x32_bf16 v[124:127], v[56:59], v[200:203], v[124:127]\n" \
"ds_read_b64_tr_b16 v[18:19], v232 offset:2560\n" \
"v_mfma_f32_16x16x32_bf16 v[124:127], v[60:63], v[204:207], v[124:127]\n" \
"global_load_lds_dwordx4 v250, s[36:37] offset:3072 sc1\n" \
"v_mfma_f32_16x16x32_bf16 v[144:147], v[32:35], v[208:211], v[144:147]\n" \
"ds_read_b128 v[200:203], v230 offset:2048\n" \
"s_add_u32 s36, s36, 17408\n" \
"v_mfma_f32_16x16x32_bf16 v[144:147], v[36:39], v[212:215], v[144:147]\n" \
"ds_read_b128 v[204:207], v231 offset:2048\n" \
"v_mfma_f32_16x16x32_bf16 v[148:151], v[40:43], v[208:211], v[148:151]\n" \
"ds_read_b64_tr_b16 v[20:21], v232 offset:8704\n" \
"v_mfma_f32_16x16x32_bf16 v[148:151], v[44:47], v[212:215], v[148:151]\n" \
"buffer_store_dwordx4 a[88:91], v248, s[56:59], s90 offen offset:2048 sc1\n" \
"v_mfma_f32_16x16x32_bf16 v[152:155], v[48:51], v[208:211], v[152:155]\n" \
"ds_read_b64_tr_b16 v[22:23], v232 offset:10752\n" \
"v_mfma_f32_16x16x32_bf16 v[152:155], v[52:55], v[212:215], v[152:155]\n" \
"ds_read_b64_tr_b16 v[24:25], v233 offset:512\n" \
"v_mfma_f32_16x16x32_bf16 v[156:159], v[56:59], v[208:211], v[156:159]\n" \
"ds_read_b128 a[88:91], v254\n" \
"ds_read_b64_tr_b16 v[26:27], v233 offset:2560\n" \
"v_mfma_f32_16x16x32_bf16 v[156:159], v[60:63], v[212:215], v[156:159]\n" \
"buffer_store_dwordx4 a[92:95], v248, s[56:59], s90 offen offset:3072 sc1\n" \
"ds_read_b128 a[120:123], v254\n" \
"v_mfma_f32_16x16x32_bf16 v[176:179], v[32:35], v[216:219], v[176:179]\n" \
"ds_read_b128 v[208:211], v230 offset:4096\n" \
"s_add_u32 s90, s88, 417792\n" \
"v_mfma_f32_16x16x32_bf16 v[176:179], v[36:39], v[220:223], v[176:179]\n" \
"ds_read_b128 v[212:215], v231 offset:4096\n" \
"v_mfma_f32_16x16x32_bf16 v[180:183], v[40:43], v[216:219], v[180:183]\n" \
"ds_read_b128 a[92:95], v254\n" \
"ds_read_b64_tr_b16 v[28:29], v233 offset:8704\n" \
"v_mfma_f32_16x16x32_bf16 v[180:183], v[44:47], v[220:223], v[180:183]\n" \
"buffer_store_dwordx4 a[0:3], v249, s[56:59], s90 offen offset:0 sc1\n" \
"ds_read_b128 a[124:127], v254\n" \
"v_mfma_f32_16x16x32_bf16 v[184:187], v[48:51], v[216:219], v[184:187]\n" \
"ds_read_b64_tr_b16 v[30:31], v233 offset:10752\n" \
"v_mfma_f32_16x16x32_bf16 v[184:187], v[52:55], v[220:223], v[184:187]\n" \
"v_mfma_f32_16x16x32_bf16 v[188:191], v[56:59], v[216:219], v[188:191]\n" \
"ds_read_b128 a[0:3], v254\n" \
"v_mfma_f32_16x16x32_bf16 v[188:191], v[60:63], v[220:223], v[188:191]\n" \
"s_waitcnt lgkmcnt(3)\n" \
"ds_read_b128 a[4:7], v254\n" \
"v_mfma_f32_16x16x32_bf16 v[64:67], v[0:3], v[192:195], v[64:67]\n" \
"ds_read_b128 v[216:219], v230 offset:6144\n" \
"v_mfma_f32_16x16x32_bf16 v[64:67], v[4:7], v[196:199], v[64:67]\n" \
"ds_read_b128 v[220:223], v231 offset:6144\n" \
"v_mfma_f32_16x16x32_bf16 v[68:71], v[8:11], v[192:195], v[68:71]\n" \
"ds_read_b64_tr_b16 v[32:33], v232 offset:1024\n" \
"v_mfma_f32_16x16x32_bf16 v[68:71], v[12:15], v[196:199], v[68:71]\n" \
"v_mfma_f32_16x16x32_bf16 v[72:75], v[16:19], v[192:195], v[72:75]\n" \
"ds_read_b64_tr_b16 v[34:35], v232 offset:3072\n" \
"v_mfma_f32_16x16x32_bf16 v[72:75], v[20:23], v[196:199], v[72:75]\n" \
"s_waitcnt lgkmcnt(3)\n" \
"ds_read_b64_tr_b16 v[36:37], v232 offset:9216\n" \
"v_mfma_f32_16x16x32_bf16 v[76:79], v[24:27], v[192:195], v[76:79]\n" \
"ds_read_b64_tr_b16 v[38:39], v232 offset:11264\n" \
"v_mfma_f32_16x16x32_bf16 v[76:79], v[28:31], v[196:199], v[76:79]\n" \
"v_mfma_f32_16x16x32_bf16 v[96:99], v[0:3], v[200:203], v[96:99]\n" \
"ds_read_b64_tr_b16 v[40:41], v233 offset:1024\n" \
"v_mfma_f32_16x16x32_bf16 v[96:99], v[4:7], v[204:207], v[96:99]\n" \
"ds_read_b64_tr_b16 v[42:43], v233 offset:3072\n" \
"v_mfma_f32_16x16x32_bf16 v[100:103], v[8:11], v[200:203], v[100:103]\n" \
"ds_read_b64_tr_b16 v[44:45], v233 offset:9216\n" \
"v_mfma_f32_16x16x32_bf16 v[100:103], v[12:15], v[204:207], v[100:103]\n" \
"v_mfma_f32_16x16x32_bf16 v[104:107], v[16:19], v[200:203], v[104:107]\n" \
"ds_read_b64_tr_b16 v[46:47], v233 offset:11264\n" \
"v_mfma_f32_16x16x32_bf16 v[104:107], v[20:23], v[204:207], v[104:107]\n" \
"ds_read_b64_tr_b16 v[48:49], v232 offset:1536\n" \
"v_mfma_f32_16x16x32_bf16 v[108:111], v[24:27], v[200:203], v[108:111]\n" \
"ds_read_b64_tr_b16 v[50:51], v232 offset:3584\n" \
"v_mfma_f32_16x16x32_bf16 v[108:111], v[28:31], v[204:207], v[108:111]\n" \
"v_mfma_f32_16x16x32_bf16 v[128:131], v[0:3], v[208:211], v[128:131]\n" \
"ds_read_b64_tr_b16 v[52:53], v232 offset:9728\n" \
"v_mfma_f32_16x16x32_bf16 v[128:131], v[4:7], v[212:215], v[128:131]\n" \
"ds_read_b64_tr_b16 v[54:55], v232 offset:11776\n" \
"v_mfma_f32_16x16x32_bf16 v[132:135], v[8:11], v[208:211], v[132:135]\n" \
"ds_read_b64_tr_b16 v[56:57], v233 offset:1536\n" \
"v_mfma_f32_16x16x32_bf16 v[132:135], v[12:15], v[212:215], v[132:135]\n" \
"v_mfma_f32_16x16x32_bf16 v[136:139], v[16:19], v[208:211], v[136:139]\n" \
"ds_read_b64_tr_b16 v[58:59], v233 offset:3584\n" \
"v_mfma_f32_16x16x32_bf16 v[136:139], v[20:23], v[212:215], v[136:139]\n" \
"ds_read_b64_tr_b16 v[60:61], v233 offset:9728\n" \
"v_mfma_f32_16x16x32_bf16 v[140:143], v[24:27], v[208:211], v[140:143]\n" \
"ds_read_b64_tr_b16 v[62:63], v233 offset:11776\n" \
"v_mfma_f32_16x16x32_bf16 v[140:143], v[28:31], v[212:215], v[140:143]\n" \
"s_waitcnt lgkmcnt(3)\n" \
"v_mfma_f32_16x16x32_bf16 v[160:163], v[0:3], v[216:219], v[160:163]\n" \
"v_add_u32 v230, 0x8000, v230\n" \
"v_add_u32 v231, 0x8000, v231\n" \
"v_mfma_f32_16x16x32_bf16 v[160:163], v[4:7], v[220:223], v[160:163]\n" \
"v_add_u32 v232, 0x4000, v232\n" \
"v_add_u32 v233, 0x4000, v233\n" \
"v_mfma_f32_16x16x32_bf16 v[164:167], v[8:11], v[216:219], v[164:167]\n" \
"v_mfma_f32_16x16x32_bf16 v[164:167], v[12:15], v[220:223], v[164:167]\n" \
"v_mfma_f32_16x16x32_bf16 v[168:171], v[16:19], v[216:219], v[168:171]\n" \
"v_mfma_f32_16x16x32_bf16 v[168:171], v[20:23], v[220:223], v[168:171]\n" \
"v_mfma_f32_16x16x32_bf16 v[172:175], v[24:27], v[216:219], v[172:175]\n" \
"s_waitcnt vmcnt(14) lgkmcnt(0)\n" \
"v_mfma_f32_16x16x32_bf16 v[172:175], v[28:31], v[220:223], v[172:175]\n" \
"s_barrier\n" \
"v_mfma_f32_16x16x32_bf16 v[80:83], v[32:35], v[192:195], v[80:83]\n" \
"ds_read_b64_tr_b16 v[0:1], v232 offset:0\n" \
"v_mfma_f32_16x16x32_bf16 v[80:83], v[36:39], v[196:199], v[80:83]\n" \
"ds_read_b64_tr_b16 v[2:3], v232 offset:2048\n" \
"v_mfma_f32_16x16x32_bf16 v[84:87], v[40:43], v[192:195], v[84:87]\n" \
"s_add_u32 m0, s95, 32768\n" \
"ds_read_b64_tr_b16 v[4:5], v232 offset:8192\n" \
"v_mfma_f32_16x16x32_bf16 v[84:87], v[44:47], v[196:199], v[84:87]\n" \
"global_load_lds_dwordx4 v250, s[36:37] offset:0 sc1\n" \
"v_mfma_f32_16x16x32_bf16 v[88:91], v[48:51], v[192:195], v[88:91]\n" \
"ds_read_b64_tr_b16 v[6:7], v232 offset:10240\n" \
"v_mfma_f32_16x16x32_bf16 v[88:91], v[52:55], v[196:199], v[88:91]\n" \
"ds_read_b64_tr_b16 v[8:9], v233 offset:0\n" \
"v_mfma_f32_16x16x32_bf16 v[92:95], v[56:59], v[192:195], v[92:95]\n" \
"ds_read_b64_tr_b16 v[10:11], v233 offset:2048\n" \
"v_mfma_f32_16x16x32_bf16 v[92:95], v[60:63], v[196:199], v[92:95]\n" \
"global_load_lds_dwordx4 v250, s[36:37] offset:1024 sc1\n" \
"v_mfma_f32_16x16x32_bf16 v[112:115], v[32:35], v[200:203], v[112:115]\n" \
"ds_read_b128 v[192:195], v230 offset:0\n" \
"v_mfma_f32_16x16x32_bf16 v[112:115], v[36:39], v[204:207], v[112:115]\n" \
"ds_read_b128 v[196:199], v231 offset:0\n" \
"v_mfma_f32_16x16x32_bf16 v[116:119], v[40:43], v[200:203], v[116:119]\n" \
"ds_read_b64_tr_b16 v[12:13], v233 offset:8192\n" \
"v_mfma_f32_16x16x32_bf16 v[116:119], v[44:47], v[204:207], v[116:119]\n" \
"global_load_lds_dwordx4 v250, s[36:37] offset:2048 sc1\n" \
"v_mfma_f32_16x16x32_bf16 v[120:123], v[48:51], v[200:203], v[120:123]\n" \
"ds_read_b64_tr_b16 v[14:15], v233 offset:10240\n" \
"v_mfma_f32_16x16x32_bf16 v[120:123], v[52:55], v[204:207], v[120:123]\n" \
"ds_read_b64_tr_b16 v[16:17], v232 offset:512\n" \
"v_mfma_f32_16x16x32_bf16 v[124:127], v[56:59], v[200:203], v[124:127]\n" \
"ds_read_b64_tr_b16 v[18:19], v232 offset:2560\n" \
"v_mfma_f32_16x16x32_bf16 v[124:127], v[60:63], v[204:207], v[124:127]\n" \
"global_load_lds_dwordx4 v250, s[36:37] offset:3072 sc1\n" \
"v_mfma_f32_16x16x32_bf16 v[144:147], v[32:35], v[208:211], v[144:147]\n" \
"ds_read_b128 v[200:203], v230 offset:2048\n" \
"s_add_u32 s36, s36, 17408\n" \
"v_mfma_f32_16x16x32_bf16 v[144:147], v[36:39], v[212:215], v[144:147]\n" \
"ds_read_b128 v[204:207], v231 offset:2048\n" \
"v_mfma_f32_16x16x32_bf16 v[148:151], v[40:43], v[208:211], v[148:151]\n" \
"ds_read_b64_tr_b16 v[20:21], v232 offset:8704\n" \
"v_mfma_f32_16x16x32_bf16 v[148:151], v[44:47], v[212:215], v[148:151]\n" \
"buffer_store_dwordx4 a[32:35], v249, s[56:59], s90 offen offset:256 sc1\n" \
"v_mfma_f32_16x16x32_bf16 v[152:155], v[48:51], v[208:211], v[152:155]\n" \
"ds_read_b64_tr_b16 v[22:23], v232 offset:10752\n" \
"v_mfma_f32_16x16x32_bf16 v[152:155], v[52:55], v[212:215], v[152:155]\n" \
"ds_read_b64_tr_b16 v[24:25], v233 offset:512\n" \
"v_mfma_f32_16x16x32_bf16 v[156:159], v[56:59], v[208:211], v[156:159]\n" \
"ds_read_b128 a[32:35], v254\n" \
"ds_read_b64_tr_b16 v[26:27], v233 offset:2560\n" \
"v_mfma_f32_16x16x32_bf16 v[156:159], v[60:63], v[212:215], v[156:159]\n" \
"buffer_store_dwordx4 a[8:11], v249, s[56:59], s90 offen offset:2048 sc1\n" \
"ds_read_b128 a[36:39], v254\n" \
"v_mfma_f32_16x16x32_bf16 v[176:179], v[32:35], v[216:219], v[176:179]\n" \
"ds_read_b128 v[208:211], v230 offset:4096\n" \
"v_mfma_f32_16x16x32_bf16 v[176:179], v[36:39], v[220:223], v[176:179]\n" \
"ds_read_b128 v[212:215], v231 offset:4096\n" \
"v_mfma_f32_16x16x32_bf16 v[180:183], v[40:43], v[216:219], v[180:183]\n" \
"ds_read_b128 a[8:11], v254\n" \
"ds_read_b64_tr_b16 v[28:29], v233 offset:8704\n" \
"v_mfma_f32_16x16x32_bf16 v[180:183], v[44:47], v[220:223], v[180:183]\n" \
"buffer_store_dwordx4 a[40:43], v249, s[56:59], s90 offen offset:2304 sc1\n" \
"ds_read_b128 a[12:15], v254\n" \
"v_mfma_f32_16x16x32_bf16 v[184:187], v[48:51], v[216:219], v[184:187]\n" \
"ds_read_b64_tr_b16 v[30:31], v233 offset:10752\n" \
"v_mfma_f32_16x16x32_bf16 v[184:187], v[52:55], v[220:223], v[184:187]\n" \
"v_mfma_f32_16x16x32_bf16 v[188:191], v[56:59], v[216:219], v[188:191]\n" \
"ds_read_b128 a[40:43], v254\n" \
"v_mfma_f32_16x16x32_bf16 v[188:191], v[60:63], v[220:223], v[188:191]\n" \
"s_waitcnt lgkmcnt(3)\n" \
"ds_read_b128 a[44:47], v254\n" \
"v_mfma_f32_16x16x32_bf16 v[64:67], v[0:3], v[192:195], v[64:67]\n" \
"ds_read_b128 v[216:219], v230 offset:6144\n" \
"v_mfma_f32_16x16x32_bf16 v[64:67], v[4:7], v[196:199], v[64:67]\n" \
"ds_read_b128 v[220:223], v231 offset:6144\n" \
"v_mfma_f32_16x16x32_bf16 v[68:71], v[8:11], v[192:195], v[68:71]\n" \
"ds_read_b64_tr_b16 v[32:33], v232 offset:1024\n" \
"v_mfma_f32_16x16x32_bf16 v[68:71], v[12:15], v[196:199], v[68:71]\n" \
"v_mfma_f32_16x16x32_bf16 v[72:75], v[16:19], v[192:195], v[72:75]\n" \
"ds_read_b64_tr_b16 v[34:35], v232 offset:3072\n" \
"v_mfma_f32_16x16x32_bf16 v[72:75], v[20:23], v[196:199], v[72:75]\n" \
"s_waitcnt lgkmcnt(3)\n" \
"ds_read_b64_tr_b16 v[36:37], v232 offset:9216\n" \
"v_mfma_f32_16x16x32_bf16 v[76:79], v[24:27], v[192:195], v[76:79]\n" \
"ds_read_b64_tr_b16 v[38:39], v232 offset:11264\n" \
"v_cvt_pk_bf16_f32 v64, v64, v65\n" \
"v_cvt_pk_bf16_f32 v65, v66, v67\n" \
"v_cvt_pk_bf16_f32 v66, v68, v69\n" \
"v_cvt_pk_bf16_f32 v67, v70, v71\n" \
"v_mfma_f32_16x16x32_bf16 v[76:79], v[28:31], v[196:199], v[76:79]\n" \
"v_accvgpr_write_b32 a128, v64\n" \
"v_accvgpr_write_b32 a129, v65\n" \
"v_mfma_f32_16x16x32_bf16 v[96:99], v[0:3], v[200:203], v[96:99]\n" \
"ds_read_b64_tr_b16 v[40:41], v233 offset:1024\n" \
"v_accvgpr_write_b32 a130, v66\n" \
"v_mfma_f32_16x16x32_bf16 v[96:99], v[4:7], v[204:207], v[96:99]\n" \
"ds_read_b64_tr_b16 v[42:43], v233 offset:3072\n" \
"v_accvgpr_write_b32 a131, v67\n" \
"v_mfma_f32_16x16x32_bf16 v[100:103], v[8:11], v[200:203], v[100:103]\n" \
"ds_read_b64_tr_b16 v[44:45], v233 offset:9216\n" \
"v_cvt_pk_bf16_f32 v72, v72, v73\n" \
"v_cvt_pk_bf16_f32 v73, v74, v75\n" \
"v_cvt_pk_bf16_f32 v74, v76, v77\n" \
"v_cvt_pk_bf16_f32 v75, v78, v79\n" \
"v_mfma_f32_16x16x32_bf16 v[100:103], v[12:15], v[204:207], v[100:103]\n" \
"v_accvgpr_write_b32 a132, v72\n" \
"v_accvgpr_write_b32 a133, v73\n" \
"v_mfma_f32_16x16x32_bf16 v[104:107], v[16:19], v[200:203], v[104:107]\n" \
"ds_read_b64_tr_b16 v[46:47], v233 offset:11264\n" \
"v_accvgpr_write_b32 a134, v74\n" \
"v_mfma_f32_16x16x32_bf16 v[104:107], v[20:23], v[204:207], v[104:107]\n" \
"ds_read_b64_tr_b16 v[48:49], v232 offset:1536\n" \
"v_accvgpr_write_b32 a135, v75\n" \
"v_mfma_f32_16x16x32_bf16 v[108:111], v[24:27], v[200:203], v[108:111]\n" \
"ds_read_b64_tr_b16 v[50:51], v232 offset:3584\n" \
"v_cvt_pk_bf16_f32 v96, v96, v97\n" \
"v_cvt_pk_bf16_f32 v97, v98, v99\n" \
"v_cvt_pk_bf16_f32 v98, v100, v101\n" \
"v_cvt_pk_bf16_f32 v99, v102, v103\n" \
"v_mfma_f32_16x16x32_bf16 v[108:111], v[28:31], v[204:207], v[108:111]\n" \
"v_accvgpr_write_b32 a144, v96\n" \
"v_accvgpr_write_b32 a145, v97\n" \
"v_mfma_f32_16x16x32_bf16 v[128:131], v[0:3], v[208:211], v[128:131]\n" \
"ds_read_b64_tr_b16 v[52:53], v232 offset:9728\n" \
"v_accvgpr_write_b32 a146, v98\n" \
"v_mfma_f32_16x16x32_bf16 v[128:131], v[4:7], v[212:215], v[128:131]\n" \
"ds_read_b64_tr_b16 v[54:55], v232 offset:11776\n" \
"v_accvgpr_write_b32 a147, v99\n" \
"v_mfma_f32_16x16x32_bf16 v[132:135], v[8:11], v[208:211], v[132:135]\n" \
"ds_read_b64_tr_b16 v[56:57], v233 offset:1536\n" \
"v_cvt_pk_bf16_f32 v104, v104, v105\n" \
"v_cvt_pk_bf16_f32 v105, v106, v107\n" \
"v_cvt_pk_bf16_f32 v106, v108, v109\n" \
"v_cvt_pk_bf16_f32 v107, v110, v111\n" \
"v_mfma_f32_16x16x32_bf16 v[132:135], v[12:15], v[212:215], v[132:135]\n" \
"v_accvgpr_write_b32 a148, v104\n" \
"v_accvgpr_write_b32 a149, v105\n" \
"v_mfma_f32_16x16x32_bf16 v[136:139], v[16:19], v[208:211], v[136:139]\n" \
"ds_read_b64_tr_b16 v[58:59], v233 offset:3584\n" \
"v_accvgpr_write_b32 a150, v106\n" \
"v_mfma_f32_16x16x32_bf16 v[136:139], v[20:23], v[212:215], v[136:139]\n" \
"ds_read_b64_tr_b16 v[60:61], v233 offset:9728\n" \
"v_accvgpr_write_b32 a151, v107\n" \
"v_mfma_f32_16x16x32_bf16 v[140:143], v[24:27], v[208:211], v[140:143]\n" \
"ds_read_b64_tr_b16 v[62:63], v233 offset:11776\n" \
"v_cvt_pk_bf16_f32 v128, v128, v129\n" \
"v_cvt_pk_bf16_f32 v129, v130, v131\n" \
"v_mfma_f32_16x16x32_bf16 v[140:143], v[28:31], v[212:215], v[140:143]\n" \
"s_waitcnt lgkmcnt(3)\n" \
"v_cvt_pk_bf16_f32 v130, v132, v133\n" \
"v_cvt_pk_bf16_f32 v131, v134, v135\n" \
"v_mfma_f32_16x16x32_bf16 v[160:163], v[0:3], v[216:219], v[160:163]\n" \
"v_accvgpr_write_b32 a160, v128\n" \
"v_accvgpr_write_b32 a161, v129\n" \
"v_mfma_f32_16x16x32_bf16 v[160:163], v[4:7], v[220:223], v[160:163]\n" \
"v_accvgpr_write_b32 a162, v130\n" \
"v_accvgpr_write_b32 a163, v131\n" \
"v_mfma_f32_16x16x32_bf16 v[164:167], v[8:11], v[216:219], v[164:167]\n" \
"v_cvt_pk_bf16_f32 v136, v136, v137\n" \
"v_cvt_pk_bf16_f32 v137, v138, v139\n" \
"v_mfma_f32_16x16x32_bf16 v[164:167], v[12:15], v[220:223], v[164:167]\n" \
"v_cvt_pk_bf16_f32 v138, v140, v141\n" \
"v_cvt_pk_bf16_f32 v139, v142, v143\n" \
"v_mfma_f32_16x16x32_bf16 v[168:171], v[16:19], v[216:219], v[168:171]\n" \
"v_accvgpr_write_b32 a164, v136\n" \
"v_accvgpr_write_b32 a165, v137\n" \
"v_mfma_f32_16x16x32_bf16 v[168:171], v[20:23], v[220:223], v[168:171]\n" \
"v_accvgpr_write_b32 a166, v138\n" \
"Lpg4891_%=:\n" \
"s_cbranch_execz Lpg6169_%=\n" \
"v_accvgpr_write_b32 a167, v139\n" \
"v_mfma_f32_16x16x32_bf16 v[172:175], v[24:27], v[216:219], v[172:175]\n" \
"s_waitcnt vmcnt(10) lgkmcnt(0)\n" \
"v_cvt_pk_bf16_f32 v160, v160, v161\n" \
"v_cvt_pk_bf16_f32 v161, v162, v163\n" \
"v_cvt_pk_bf16_f32 v162, v164, v165\n" \
"v_cvt_pk_bf16_f32 v163, v166, v167\n" \
"v_accvgpr_write_b32 a176, v160\n" \
"v_mfma_f32_16x16x32_bf16 v[172:175], v[28:31], v[220:223], v[172:175]\n" \
"s_barrier\n" \
"v_accvgpr_write_b32 a177, v161\n" \
"v_mfma_f32_16x16x32_bf16 v[80:83], v[32:35], v[192:195], v[80:83]\n" \
"s_mov_b32 m0, s74\n" \
"v_accvgpr_write_b32 a178, v162\n" \
"v_accvgpr_write_b32 a179, v163\n" \
"v_mfma_f32_16x16x32_bf16 v[80:83], v[36:39], v[196:199], v[80:83]\n" \
"ds_read_b128 a[192:195], v249 offset:0\n" \
"global_load_lds_dwordx4 v250, s[38:39] offset:0 sc1\n" \
"v_mfma_f32_16x16x32_bf16 v[84:87], v[40:43], v[192:195], v[84:87]\n" \
"ds_read_b128 v[0:3], v254\n" \
"v_cvt_pk_bf16_f32 v168, v168, v169\n" \
"v_cvt_pk_bf16_f32 v169, v170, v171\n" \
"v_cvt_pk_bf16_f32 v170, v172, v173\n" \
"v_cvt_pk_bf16_f32 v171, v174, v175\n" \
"v_accvgpr_write_b32 a180, v168\n" \
"v_accvgpr_write_b32 a181, v169\n" \
"v_mfma_f32_16x16x32_bf16 v[84:87], v[44:47], v[196:199], v[84:87]\n" \
"ds_read_b128 a[196:199], v249 offset:2048\n" \
"global_load_lds_dwordx4 v250, s[38:39] offset:1024 sc1\n" \
"v_accvgpr_write_b32 a182, v170\n" \
"v_mfma_f32_16x16x32_bf16 v[88:91], v[48:51], v[192:195], v[88:91]\n" \
"ds_read_b128 v[4:7], v254\n" \
"v_accvgpr_write_b32 a183, v171\n" \
"v_mfma_f32_16x16x32_bf16 v[88:91], v[52:55], v[196:199], v[88:91]\n" \
"ds_read_b128 a[224:227], v249 offset:4096\n" \
"global_load_lds_dwordx4 v250, s[38:39] offset:2048 sc1\n" \
"v_mfma_f32_16x16x32_bf16 v[92:95], v[56:59], v[192:195], v[92:95]\n" \
"v_cvt_pk_bf16_f32 v80, v80, v81\n" \
"v_cvt_pk_bf16_f32 v81, v82, v83\n" \
"v_cvt_pk_bf16_f32 v82, v84, v85\n" \
"v_cvt_pk_bf16_f32 v83, v86, v87\n" \
"v_accvgpr_write_b32 a136, v80\n" \
"v_accvgpr_write_b32 a137, v81\n" \
"v_mfma_f32_16x16x32_bf16 v[92:95], v[60:63], v[196:199], v[92:95]\n" \
"ds_read_b128 a[228:231], v249 offset:6144\n" \
"global_load_lds_dwordx4 v250, s[38:39] offset:3072 sc1\n" \
"v_accvgpr_write_b32 a138, v82\n" \
"v_mfma_f32_16x16x32_bf16 v[112:115], v[32:35], v[200:203], v[112:115]\n" \
"s_add_u32 s38, s38, 17408\n" \
"v_accvgpr_write_b32 a139, v83\n" \
"s_add_u32 m0, m0, 0x4000\n" \
"v_mfma_f32_16x16x32_bf16 v[112:115], v[36:39], v[204:207], v[112:115]\n" \
"ds_read_b128 a[200:203], v249 offset:256\n" \
"global_load_lds_dwordx4 v250, s[38:39] offset:0 sc1\n" \
"v_mfma_f32_16x16x32_bf16 v[116:119], v[40:43], v[200:203], v[116:119]\n" \
"v_cvt_pk_bf16_f32 v88, v88, v89\n" \
"v_cvt_pk_bf16_f32 v89, v90, v91\n" \
"v_cvt_pk_bf16_f32 v90, v92, v93\n" \
"v_cvt_pk_bf16_f32 v91, v94, v95\n" \
"v_accvgpr_write_b32 a140, v88\n" \
"v_accvgpr_write_b32 a141, v89\n" \
"v_mfma_f32_16x16x32_bf16 v[116:119], v[44:47], v[204:207], v[116:119]\n" \
"ds_read_b128 a[204:207], v249 offset:2304\n" \
"global_load_lds_dwordx4 v250, s[38:39] offset:1024 sc1\n" \
"v_mfma_f32_16x16x32_bf16 v[120:123], v[48:51], v[200:203], v[120:123]\n" \
"v_accvgpr_write_b32 a142, v90\n" \
"v_accvgpr_write_b32 a143, v91\n" \
"v_mfma_f32_16x16x32_bf16 v[120:123], v[52:55], v[204:207], v[120:123]\n" \
"ds_read_b128 a[232:235], v249 offset:4352\n" \
"global_load_lds_dwordx4 v250, s[38:39] offset:2048 sc1\n" \
"v_mfma_f32_16x16x32_bf16 v[124:127], v[56:59], v[200:203], v[124:127]\n" \
"v_cvt_pk_bf16_f32 v112, v112, v113\n" \
"v_cvt_pk_bf16_f32 v113, v114, v115\n" \
"v_cvt_pk_bf16_f32 v114, v116, v117\n" \
"v_cvt_pk_bf16_f32 v115, v118, v119\n" \
"v_accvgpr_write_b32 a152, v112\n" \
"v_accvgpr_write_b32 a153, v113\n" \
"v_mfma_f32_16x16x32_bf16 v[124:127], v[60:63], v[204:207], v[124:127]\n" \
"ds_read_b128 a[236:239], v249 offset:6400\n" \
"global_load_lds_dwordx4 v250, s[38:39] offset:3072 sc1\n" \
"v_accvgpr_write_b32 a154, v114\n" \
"v_mfma_f32_16x16x32_bf16 v[144:147], v[32:35], v[208:211], v[144:147]\n" \
"s_add_u32 s38, s38, 17408\n" \
"s_xor_b32 s74, s74, 0x8000\n" \
"s_add_u32 s90, s89, 417792\n" \
"v_mfma_f32_16x16x32_bf16 v[144:147], v[36:39], v[212:215], v[144:147]\n" \
"ds_read_b128 a[208:211], v249 offset:8192\n" \
"v_accvgpr_write_b32 a155, v115\n" \
"v_mfma_f32_16x16x32_bf16 v[148:151], v[40:43], v[208:211], v[148:151]\n" \
"v_cvt_pk_bf16_f32 v120, v120, v121\n" \
"v_cvt_pk_bf16_f32 v121, v122, v123\n" \
"v_mfma_f32_16x16x32_bf16 v[148:151], v[44:47], v[212:215], v[148:151]\n" \
"buffer_store_dwordx4 a[16:19], v248, s[56:59], s90 offen offset:0 sc1\n" \
"ds_read_b128 a[212:215], v249 offset:10240\n" \
"v_cvt_pk_bf16_f32 v122, v124, v125\n" \
"v_cvt_pk_bf16_f32 v123, v126, v127\n" \
"v_accvgpr_write_b32 a156, v120\n" \
"v_mfma_f32_16x16x32_bf16 v[152:155], v[48:51], v[208:211], v[152:155]\n" \
"v_accvgpr_write_b32 a157, v121\n" \
"v_accvgpr_write_b32 a158, v122\n" \
"v_mfma_f32_16x16x32_bf16 v[152:155], v[52:55], v[212:215], v[152:155]\n" \
"ds_read_b128 a[240:243], v249 offset:12288\n" \
"v_accvgpr_write_b32 a159, v123\n" \
"v_mfma_f32_16x16x32_bf16 v[156:159], v[56:59], v[208:211], v[156:159]\n" \
"ds_read_b128 a[16:19], v254\n" \
"v_cvt_pk_bf16_f32 v144, v144, v145\n" \
"v_cvt_pk_bf16_f32 v145, v146, v147\n" \
"v_cvt_pk_bf16_f32 v146, v148, v149\n" \
"v_mfma_f32_16x16x32_bf16 v[156:159], v[60:63], v[212:215], v[156:159]\n" \
"ds_read_b128 a[244:247], v249 offset:14336\n" \
"v_cvt_pk_bf16_f32 v147, v150, v151\n" \
"v_mfma_f32_16x16x32_bf16 v[176:179], v[32:35], v[216:219], v[176:179]\n" \
"ds_read_b128 a[216:219], v249 offset:8448\n" \
"buffer_store_dwordx4 a[20:23], v248, s[56:59], s90 offen offset:1024 sc1\n" \
"ds_read_b128 a[48:51], v254\n" \
"v_accvgpr_write_b32 a168, v144\n" \
"v_accvgpr_write_b32 a169, v145\n" \
"v_accvgpr_write_b32 a170, v146\n" \
"v_mfma_f32_16x16x32_bf16 v[176:179], v[36:39], v[220:223], v[176:179]\n" \
"ds_read_b128 a[220:223], v249 offset:10496\n" \
"v_accvgpr_write_b32 a171, v147\n" \
"v_mfma_f32_16x16x32_bf16 v[180:183], v[40:43], v[216:219], v[180:183]\n" \
"ds_read_b128 a[248:251], v249 offset:12544\n" \
"v_cvt_pk_bf16_f32 v152, v152, v153\n" \
"v_cvt_pk_bf16_f32 v153, v154, v155\n" \
"v_mfma_f32_16x16x32_bf16 v[180:183], v[44:47], v[220:223], v[180:183]\n" \
"ds_read_b128 a[252:255], v249 offset:14592\n" \
"v_cvt_pk_bf16_f32 v154, v156, v157\n" \
"v_mfma_f32_16x16x32_bf16 v[184:187], v[48:51], v[216:219], v[184:187]\n" \
"ds_read_b128 a[20:23], v254\n" \
"v_cvt_pk_bf16_f32 v155, v158, v159\n" \
"v_mfma_f32_16x16x32_bf16 v[184:187], v[52:55], v[220:223], v[184:187]\n" \
"ds_read_b128 v[32:35], v254\n" \
"v_accvgpr_write_b32 a172, v152\n" \
"v_mfma_f32_16x16x32_bf16 v[188:191], v[56:59], v[216:219], v[188:191]\n" \
"ds_read_b128 v[36:39], v254\n" \
"v_accvgpr_write_b32 a173, v153\n" \
"v_mfma_f32_16x16x32_bf16 v[188:191], v[60:63], v[220:223], v[188:191]\n" \
"s_waitcnt lgkmcnt(4)\n" \
"buffer_store_dwordx4 a[24:27], v248, s[56:59], s90 offen offset:2048 sc1\n" \
"v_accvgpr_write_b32 a174, v154\n" \
"s_cmp_eq_u32 s63, 0\n" \
"s_cbranch_scc1 Lmask0_%=\n" \
"Lmask0r_%=:\n" \
"v_mfma_f32_16x16x32_bf16 v[0:3], a[192:195], a[128:131], v[0:3]\n" \
"s_lshl_b32 s70, s95, 1\n" \
"ds_read_b128 a[52:55], v254\n" \
"v_accvgpr_write_b32 a175, v155\n" \
"v_mov_b32 v201, 0xf149f2ca\n" \
"v_mfma_f32_16x16x32_bf16 v[0:3], a[196:199], a[132:135], v[0:3]\n" \
"s_add_u32 s70, s70, 131072\n" \
"v_cvt_pk_bf16_f32 v176, v176, v177\n" \
"v_cvt_pk_bf16_f32 v177, v178, v179\n" \
"v_mfma_f32_16x16x32_bf16 v[0:3], a[224:227], a[136:139], v[0:3]\n" \
"ds_read_b128 a[24:27], v254\n" \
"buffer_store_dwordx4 a[28:31], v248, s[56:59], s90 offen offset:3072 sc1\n" \
"ds_read_b128 a[56:59], v254\n" \
"v_cvt_pk_bf16_f32 v178, v180, v181\n" \
"v_cvt_pk_bf16_f32 v179, v182, v183\n" \
"v_accvgpr_write_b32 a184, v176\n" \
"v_mfma_f32_16x16x32_bf16 v[0:3], a[228:231], a[140:143], v[0:3]\n" \
"v_accvgpr_write_b32 a185, v177\n" \
"v_accvgpr_write_b32 a186, v178\n" \
"v_mfma_f32_16x16x32_bf16 v[8:11], a[192:195], a[144:147], 0\n" \
"global_load_dword v253, v252, s[40:41]\n" \
"v_mfma_f32_16x16x32_bf16 v[8:11], a[196:199], a[148:151], v[8:11]\n" \
"s_sub_u32 s80, s80, 1\n" \
"ds_read_b128 a[28:31], v254\n" \
"s_cmp_gt_i32 s80, 0\n" \
"v_mfma_f32_16x16x32_bf16 v[8:11], a[224:227], a[152:155], v[8:11]\n" \
"s_cselect_b32 s81, 0x80000, 0\n" \
"ds_read_b128 a[60:63], v254\n" \
"v_max3_f32 v192, v0, v1, v2\n" \
"v_max3_f32 v192, v192, v3, v201\n" \
"s_add_u32 s40, s40, s81\n" \
"v_mfma_f32_16x16x32_bf16 v[8:11], a[228:231], a[156:159], v[8:11]\n" \
"s_addc_u32 s41, s41, 0\n" \
"v_accvgpr_write_b32 a187, v179\n" \
"s_add_u32 m0, s95, 49152\n" \
"v_mfma_f32_16x16x32_bf16 v[16:19], a[192:195], a[160:163], 0\n" \
"global_load_lds_dwordx4 v250, s[36:37] offset:0 sc1\n" \
"v_cvt_pk_bf16_f32 v184, v184, v185\n" \
"v_mfma_f32_16x16x32_bf16 v[16:19], a[196:199], a[164:167], v[16:19]\n" \
"v_cvt_pk_bf16_f32 v185, v186, v187\n" \
"v_cvt_pk_bf16_f32 v186, v188, v189\n" \
"v_mfma_f32_16x16x32_bf16 v[16:19], a[224:227], a[168:171], v[16:19]\n" \
"v_max3_f32 v193, v8, v9, v10\n" \
"v_cvt_pk_bf16_f32 v187, v190, v191\n" \
"v_accvgpr_write_b32 a188, v184\n" \
"v_mfma_f32_16x16x32_bf16 v[16:19], a[228:231], a[172:175], v[16:19]\n" \
"v_accvgpr_write_b32 a189, v185\n" \
"v_accvgpr_write_b32 a190, v186\n" \
"v_mfma_f32_16x16x32_bf16 v[24:27], a[192:195], a[176:179], 0\n" \
"global_load_lds_dwordx4 v250, s[36:37] offset:1024 sc1\n" \
"v_accvgpr_write_b32 a191, v187\n" \
"v_mfma_f32_16x16x32_bf16 v[24:27], a[196:199], a[180:183], v[24:27]\n" \
"v_add_u32 v200, s70, v248\n" \
"v_max3_f32 v193, v193, v11, v201\n" \
"v_mfma_f32_16x16x32_bf16 v[24:27], a[224:227], a[184:187], v[24:27]\n" \
"ds_write_b128 v200, a[160:163] offset:0\n" \
"v_mfma_f32_16x16x32_bf16 v[24:27], a[228:231], a[188:191], v[24:27]\n" \
"s_waitcnt lgkmcnt(1)\n" \
"ds_read_b128 v[128:131], v254\n" \
"v_max3_f32 v194, v16, v17, v18\n" \
"v_mfma_f32_16x16x32_bf16 v[4:7], a[200:203], a[128:131], v[4:7]\n" \
"ds_read_b128 v[132:135], v254\n" \
"global_load_lds_dwordx4 v250, s[36:37] offset:2048 sc1\n" \
"v_mfma_f32_16x16x32_bf16 v[4:7], a[204:207], a[132:135], v[4:7]\n" \
"ds_read_b128 a[192:195], v249 offset:16384\n" \
"v_max3_f32 v194, v194, v19, v201\n" \
"v_mfma_f32_16x16x32_bf16 v[4:7], a[232:235], a[136:139], v[4:7]\n" \
"ds_write_b128 v200, a[164:167] offset:1024\n" \
"v_max3_f32 v195, v24, v25, v26\n" \
"v_max3_f32 v195, v195, v27, v201\n" \
"v_mov_b32 v196, v192\n" \
"v_mov_b32 v197, v193\n" \
"v_mov_b32 v198, v194\n" \
"v_mov_b32 v199, v195\n" \
"v_permlane16_swap_b32 v196, v192\n" \
"v_permlane16_swap_b32 v197, v193\n" \
"v_mfma_f32_16x16x32_bf16 v[4:7], a[236:239], a[140:143], v[4:7]\n" \
"ds_read_b128 a[196:199], v249 offset:18432\n" \
"global_load_lds_dwordx4 v250, s[36:37] offset:3072 sc1\n" \
"v_permlane16_swap_b32 v198, v194\n" \
"v_permlane16_swap_b32 v199, v195\n" \
"v_max_f32 v192, v192, v196\n" \
"v_max_f32 v193, v193, v197\n" \
"v_max_f32 v194, v194, v198\n" \
"v_max_f32 v195, v195, v199\n" \
"v_mov_b32 v196, v192\n" \
"v_mov_b32 v197, v193\n" \
"v_mfma_f32_16x16x32_bf16 v[12:15], a[200:203], a[144:147], 0\n" \
"s_add_u32 s36, s36, 17408\n" \
"s_mov_b32 s42, s60\n" \
"ds_read_b128 v[136:139], v254\n" \
"v_mov_b32 v198, v194\n" \
"v_mov_b32 v199, v195\n" \
"v_permlane32_swap_b32 v196, v192\n" \
"v_permlane32_swap_b32 v197, v193\n" \
"v_permlane32_swap_b32 v198, v194\n" \
"v_permlane32_swap_b32 v199, v195\n" \
"v_max_f32 v192, v192, v196\n" \
"v_max_f32 v193, v193, v197\n" \
"v_mfma_f32_16x16x32_bf16 v[12:15], a[204:207], a[148:151], v[12:15]\n" \
"s_mov_b32 s43, s60\n" \
"s_mov_b64 s[8:9], 0\n" \
"ds_read_b128 a[224:227], v249 offset:20480\n" \
"v_max_f32 v194, v194, v198\n" \
"v_max_f32 v195, v195, v199\n" \
"v_sub_f32 v224, 0, v192\n" \
"v_sub_f32 v225, 0, v192\n" \
"v_sub_f32 v226, 0, v192\n" \
"v_sub_f32 v227, 0, v192\n" \
"v_add_f32 v32, v32, v224\n" \
"v_add_f32 v33, v33, v224\n" \
"v_mfma_f32_16x16x32_bf16 v[12:15], a[232:235], a[152:155], v[12:15]\n" \
"s_mov_b64 s[10:11], -1\n" \
"s_lshl_b32 s71, s63, 2\n" \
"s_add_u32 s71, s71, s79\n" \
"v_add_f32 v34, v34, v224\n" \
"v_add_f32 v35, v35, v224\n" \
"v_sub_f32 v228, 0, v193\n" \
"v_sub_f32 v229, 0, v193\n" \
"v_sub_f32 v230, 0, v193\n" \
"v_sub_f32 v231, 0, v193\n" \
"v_sub_f32 v232, 0, v194\n" \
"v_sub_f32 v233, 0, v194\n" \
"v_mfma_f32_16x16x32_bf16 v[12:15], a[236:239], a[156:159], v[12:15]\n" \
"ds_read_b128 a[228:231], v249 offset:22528\n" \
"v_sub_f32 v234, 0, v194\n" \
"v_sub_f32 v235, 0, v194\n" \
"v_sub_f32 v236, 0, v195\n" \
"v_sub_f32 v237, 0, v195\n" \
"v_sub_f32 v238, 0, v195\n" \
"v_sub_f32 v239, 0, v195\n" \
"v_add_f32 v36, v36, v224\n" \
"v_add_f32 v37, v37, v224\n" \
"v_mfma_f32_16x16x32_bf16 v[20:23], a[200:203], a[160:163], 0\n" \
"ds_write_b128 v200, a[168:171] offset:2048\n" \
"v_add_f32 v38, v38, v224\n" \
"v_add_f32 v39, v39, v224\n" \
"v_add_f32 v0, v0, v224\n" \
"v_add_f32 v1, v1, v224\n" \
"v_add_f32 v2, v2, v224\n" \
"v_add_f32 v3, v3, v224\n" \
"v_add_f32 v4, v4, v224\n" \
"v_add_f32 v5, v5, v224\n" \
"v_mfma_f32_16x16x32_bf16 v[20:23], a[204:207], a[164:167], v[20:23]\n" \
"ds_read_b128 v[140:143], v254\n" \
"v_add_f32 v6, v6, v224\n" \
"v_add_f32 v7, v7, v224\n" \
"v_exp_f32 v0, v0\n" \
"v_exp_f32 v1, v1\n" \
"v_exp_f32 v2, v2\n" \
"v_add_f32 v240, v0, v1\n" \
"v_exp_f32 v3, v3\n" \
"v_add_f32 v240, v240, v2\n" \
"v_mfma_f32_16x16x32_bf16 v[20:23], a[232:235], a[168:171], v[20:23]\n" \
"ds_read_b128 v[144:147], v254\n" \
"v_cvt_pk_bf16_f32 v0, v0, v1\n" \
"v_exp_f32 v4, v4\n" \
"v_add_f32 v240, v240, v3\n" \
"v_exp_f32 v5, v5\n" \
"v_add_f32 v240, v240, v4\n" \
"v_cvt_pk_bf16_f32 v1, v2, v3\n" \
"v_exp_f32 v6, v6\n" \
"v_add_f32 v240, v240, v5\n" \
"v_mfma_f32_16x16x32_bf16 v[20:23], a[236:239], a[172:175], v[20:23]\n" \
"ds_write_b128 v200, a[172:175] offset:3072\n" \
"v_exp_f32 v7, v7\n" \
"v_add_f32 v240, v240, v6\n" \
"v_cvt_pk_bf16_f32 v2, v4, v5\n" \
"v_add_f32 v240, v240, v7\n" \
"v_cvt_pk_bf16_f32 v3, v6, v7\n" \
"v_add_f32 v8, v8, v228\n" \
"v_add_f32 v9, v9, v228\n" \
"v_add_f32 v10, v10, v228\n" \
"v_mfma_f32_16x16x32_bf16 v[28:31], a[200:203], a[176:179], 0\n" \
"ds_read_b128 v[148:151], v254\n" \
"v_add_f32 v11, v11, v228\n" \
"v_add_f32 v12, v12, v228\n" \
"v_add_f32 v13, v13, v228\n" \
"v_add_f32 v14, v14, v228\n" \
"v_add_f32 v15, v15, v228\n" \
"v_exp_f32 v8, v8\n" \
"v_exp_f32 v9, v9\n" \
"v_exp_f32 v10, v10\n" \
"v_mfma_f32_16x16x32_bf16 v[28:31], a[204:207], a[180:183], v[28:31]\n" \
"ds_read_b128 v[152:155], v254\n" \
"v_add_f32 v241, v8, v9\n" \
"v_exp_f32 v11, v11\n" \
"v_add_f32 v241, v241, v10\n" \
"v_cvt_pk_bf16_f32 v8, v8, v9\n" \
"v_exp_f32 v12, v12\n" \
"v_add_f32 v241, v241, v11\n" \
"v_exp_f32 v13, v13\n" \
"v_add_f32 v241, v241, v12\n" \
"v_mfma_f32_16x16x32_bf16 v[28:31], a[232:235], a[184:187], v[28:31]\n" \
"ds_write_b128 v200, a[176:179] offset:4096\n" \
"v_cvt_pk_bf16_f32 v9, v10, v11\n" \
"v_exp_f32 v14, v14\n" \
"v_add_f32 v241, v241, v13\n" \
"v_exp_f32 v15, v15\n" \
"v_add_f32 v241, v241, v14\n" \
"v_cvt_pk_bf16_f32 v10, v12, v13\n" \
"v_add_f32 v241, v241, v15\n" \
"v_cvt_pk_bf16_f32 v11, v14, v15\n" \
"v_mfma_f32_16x16x32_bf16 v[28:31], a[236:239], a[188:191], v[28:31]\n" \
"ds_read_b128 v[156:159], v254\n" \
"v_add_f32 v16, v16, v232\n" \
"v_add_f32 v17, v17, v232\n" \
"v_add_f32 v18, v18, v232\n" \
"v_add_f32 v19, v19, v232\n" \
"v_add_f32 v20, v20, v232\n" \
"v_add_f32 v21, v21, v232\n" \
"v_add_f32 v22, v22, v232\n" \
"v_add_f32 v23, v23, v232\n" \
"v_mfma_f32_16x16x32_bf16 v[32:35], a[208:211], a[128:131], v[32:35]\n" \
"ds_read_b128 v[160:163], v254\n" \
"v_exp_f32 v16, v16\n" \
"v_exp_f32 v17, v17\n" \
"v_exp_f32 v18, v18\n" \
"v_add_f32 v242, v16, v17\n" \
"v_exp_f32 v19, v19\n" \
"v_add_f32 v242, v242, v18\n" \
"v_cvt_pk_bf16_f32 v16, v16, v17\n" \
"v_exp_f32 v20, v20\n" \
"v_mfma_f32_16x16x32_bf16 v[32:35], a[212:215], a[132:135], v[32:35]\n" \
"ds_read_b128 a[200:203], v249 offset:16640\n" \
"v_add_f32 v242, v242, v19\n" \
"v_exp_f32 v21, v21\n" \
"v_add_f32 v242, v242, v20\n" \
"v_cvt_pk_bf16_f32 v17, v18, v19\n" \
"v_mfma_f32_16x16x32_bf16 v[32:35], a[240:243], a[136:139], v[32:35]\n" \
"ds_write_b128 v200, a[180:183] offset:5120\n" \
"v_mfma_f32_16x16x32_bf16 v[32:35], a[244:247], a[140:143], v[32:35]\n" \
"ds_read_b128 a[204:207], v249 offset:18688\n" \
"v_exp_f32 v22, v22\n" \
"v_mfma_f32_16x16x32_bf16 v[40:43], a[208:211], a[144:147], v[228:231]\n" \
"ds_read_b128 v[164:167], v254\n" \
"v_add_f32 v242, v242, v21\n" \
"v_mfma_f32_16x16x32_bf16 v[40:43], a[212:215], a[148:151], v[40:43]\n" \
"ds_read_b128 a[232:235], v249 offset:20736\n" \
"v_exp_f32 v23, v23\n" \
"v_mfma_f32_16x16x32_bf16 v[40:43], a[240:243], a[152:155], v[40:43]\n" \
"ds_write_b128 v200, a[184:187] offset:6144\n" \
"v_mfma_f32_16x16x32_bf16 v[40:43], a[244:247], a[156:159], v[40:43]\n" \
"ds_read_b128 a[236:239], v249 offset:22784\n" \
"v_add_f32 v242, v242, v22\n" \
"v_mfma_f32_16x16x32_bf16 v[48:51], a[208:211], a[160:163], v[232:235]\n" \
"ds_read_b128 v[168:171], v254\n" \
"v_cvt_pk_bf16_f32 v18, v20, v21\n" \
"v_mfma_f32_16x16x32_bf16 v[48:51], a[212:215], a[164:167], v[48:51]\n" \
"ds_write_b128 v200, a[188:191] offset:7168\n" \
"v_mfma_f32_16x16x32_bf16 v[48:51], a[240:243], a[168:171], v[48:51]\n" \
"ds_read_b128 v[172:175], v254\n" \
"v_add_f32 v242, v242, v23\n" \
"v_mfma_f32_16x16x32_bf16 v[48:51], a[244:247], a[172:175], v[48:51]\n" \
"ds_read_b128 v[176:179], v254\n" \
"v_cvt_pk_bf16_f32 v19, v22, v23\n" \
"v_mfma_f32_16x16x32_bf16 v[56:59], a[208:211], a[176:179], v[236:239]\n" \
"ds_read_b128 v[180:183], v254\n" \
"v_add_f32 v24, v24, v236\n" \
"v_mfma_f32_16x16x32_bf16 v[56:59], a[212:215], a[180:183], v[56:59]\n" \
"ds_read_b128 v[184:187], v254\n" \
"v_add_f32 v25, v25, v236\n" \
"v_mfma_f32_16x16x32_bf16 v[56:59], a[240:243], a[184:187], v[56:59]\n" \
"ds_read_b128 v[188:191], v254\n" \
"v_add_f32 v26, v26, v236\n" \
"v_mfma_f32_16x16x32_bf16 v[56:59], a[244:247], a[188:191], v[56:59]\n" \
"v_add_f32 v27, v27, v236\n" \
"v_add_f32 v28, v28, v236\n" \
"v_mfma_f32_16x16x32_bf16 v[36:39], a[216:219], a[128:131], v[36:39]\n" \
"v_add_f32 v29, v29, v236\n" \
"v_add_f32 v30, v30, v236\n" \
"v_mfma_f32_16x16x32_bf16 v[36:39], a[220:223], a[132:135], v[36:39]\n" \
"ds_read_b128 a[208:211], v249 offset:24576\n" \
"v_add_f32 v31, v31, v236\n" \
"v_mfma_f32_16x16x32_bf16 v[36:39], a[248:251], a[136:139], v[36:39]\n" \
"v_exp_f32 v24, v24\n" \
"v_mfma_f32_16x16x32_bf16 v[36:39], a[252:255], a[140:143], v[36:39]\n" \
"ds_read_b128 a[212:215], v249 offset:26624\n" \
"v_exp_f32 v25, v25\n" \
"v_mfma_f32_16x16x32_bf16 v[44:47], a[216:219], a[144:147], v[228:231]\n" \
"v_exp_f32 v26, v26\n" \
"v_mfma_f32_16x16x32_bf16 v[44:47], a[220:223], a[148:151], v[44:47]\n" \
"ds_read_b128 a[240:243], v249 offset:28672\n" \
"v_add_f32 v243, v24, v25\n" \
"v_mfma_f32_16x16x32_bf16 v[44:47], a[248:251], a[152:155], v[44:47]\n" \
"v_exp_f32 v27, v27\n" \
"v_add_f32 v243, v243, v26\n" \
"v_mfma_f32_16x16x32_bf16 v[44:47], a[252:255], a[156:159], v[44:47]\n" \
"ds_read_b128 a[244:247], v249 offset:30720\n" \
"v_cvt_pk_bf16_f32 v24, v24, v25\n" \
"v_mfma_f32_16x16x32_bf16 v[52:55], a[216:219], a[160:163], v[232:235]\n" \
"v_exp_f32 v28, v28\n" \
"v_add_f32 v243, v243, v27\n" \
"v_mfma_f32_16x16x32_bf16 v[52:55], a[220:223], a[164:167], v[52:55]\n" \
"v_exp_f32 v29, v29\n" \
"v_mfma_f32_16x16x32_bf16 v[52:55], a[248:251], a[168:171], v[52:55]\n" \
"v_add_f32 v243, v243, v28\n" \
"v_cvt_pk_bf16_f32 v25, v26, v27\n" \
"v_mfma_f32_16x16x32_bf16 v[52:55], a[252:255], a[172:175], v[52:55]\n" \
"v_exp_f32 v30, v30\n" \
"v_add_f32 v243, v243, v29\n" \
"v_mfma_f32_16x16x32_bf16 v[60:63], a[216:219], a[176:179], v[236:239]\n" \
"v_exp_f32 v31, v31\n" \
"v_add_f32 v243, v243, v30\n" \
"v_mfma_f32_16x16x32_bf16 v[60:63], a[220:223], a[180:183], v[60:63]\n" \
"v_cvt_pk_bf16_f32 v26, v28, v29\n" \
"v_add_f32 v243, v243, v31\n" \
"v_mfma_f32_16x16x32_bf16 v[60:63], a[248:251], a[184:187], v[60:63]\n" \
"v_mov_b32 v202, s60\n" \
"v_lshlrev_b32 v203, 28, s61\n" \
"v_mfma_f32_16x16x32_bf16 v[60:63], a[252:255], a[188:191], v[60:63]\n" \
"v_mov_b32 v246, v249\n" \
"v_mov_b32 v254, 0xff800000\n" \
"ds_read_b128 a[216:219], v249 offset:24832\n" \
"ds_read_b128 a[220:223], v249 offset:26880\n" \
"ds_read_b128 a[248:251], v249 offset:28928\n" \
"s_cmp_eq_u32 s63, 0\n" \
"s_waitcnt vmcnt(5) lgkmcnt(1)\n" \
"s_barrier\n" \
"buffer_store_dword v202, v203, s[56:59], s71 offen sc1\n" \
"s_memrealtime s[12:13]\n" \
"s_cbranch_scc1 LA1l_%=\n" \
"s_branch LA1_%=\n" \
"Lpg5359_%=:\n" \
"s_cbranch_execz Lpg6284_%=\n" \
"Lloop_%=:\n" \
"v_mfma_f32_16x16x32_bf16 a[0:3], a[192:195], v[64:67], a[0:3]\n" \
"v_exp_f32 v0, v0\n" \
"v_mfma_f32_16x16x32_bf16 a[0:3], a[196:199], v[96:99], a[0:3]\n" \
"v_cvt_pk_bf16_f32 v123, v126, v127\n" \
"v_add_f32 v240, v240, v0\n" \
"v_exp_f32 v1, v1\n" \
"v_mfma_f32_16x16x32_bf16 a[32:35], a[192:195], v[72:75], a[32:35]\n" \
"s_add_u32 m0, s73, 0x4000\n" \
"v_add_f32 v240, v240, v1\n" \
"v_exp_f32 v2, v2\n" \
"v_mfma_f32_16x16x32_bf16 a[32:35], a[196:199], v[104:107], a[32:35]\n" \
"ds_read_b128 a[252:255], v247 offset:31744\n" \
"global_load_lds_dwordx4 v250, s[36:37] offset:0 sc1\n" \
"v_add_f32 v240, v240, v2\n" \
"v_cvt_pk_bf16_f32 v0, v0, v1\n" \
"v_exp_f32 v3, v3\n" \
"v_mfma_f32_16x16x32_bf16 v[128:131], a[192:195], v[80:83], v[128:131]\n" \
"v_xor_b32 v247, 0x8000, v247\n" \
"v_add_f32 v240, v240, v3\n" \
"v_exp_f32 v4, v4\n" \
"v_mfma_f32_16x16x32_bf16 v[128:131], a[196:199], v[112:115], v[128:131]\n" \
"v_add_f32 v240, v240, v4\n" \
"v_cvt_pk_bf16_f32 v1, v2, v3\n" \
"v_mfma_f32_16x16x32_bf16 v[160:163], a[192:195], v[88:91], v[160:163]\n" \
"v_exp_f32 v5, v5\n" \
"v_mfma_f32_16x16x32_bf16 v[160:163], a[196:199], v[120:123], v[160:163]\n" \
"ds_read_b128 a[192:195], v246 offset:16384\n" \
"v_add_f32 v240, v240, v5\n" \
"v_mfma_f32_16x16x32_bf16 a[4:7], a[200:203], v[64:67], a[4:7]\n" \
"v_exp_f32 v6, v6\n" \
"v_mfma_f32_16x16x32_bf16 a[4:7], a[204:207], v[96:99], a[4:7]\n" \
"v_add_f32 v240, v240, v6\n" \
"v_cvt_pk_bf16_f32 v2, v4, v5\n" \
"v_mfma_f32_16x16x32_bf16 a[36:39], a[200:203], v[72:75], a[36:39]\n" \
"v_exp_f32 v7, v7\n" \
"v_mfma_f32_16x16x32_bf16 a[36:39], a[204:207], v[104:107], a[36:39]\n" \
"ds_read_b128 a[196:199], v246 offset:18432\n" \
"v_add_f32 v240, v240, v7\n" \
"v_mfma_f32_16x16x32_bf16 v[132:135], a[200:203], v[80:83], v[132:135]\n" \
"v_exp_f32 v8, v8\n" \
"v_mfma_f32_16x16x32_bf16 v[132:135], a[204:207], v[112:115], v[132:135]\n" \
"v_add_f32 v241, v241, v8\n" \
"v_cvt_pk_bf16_f32 v3, v6, v7\n" \
"v_mfma_f32_16x16x32_bf16 v[164:167], a[200:203], v[88:91], v[164:167]\n" \
"v_exp_f32 v9, v9\n" \
"v_mfma_f32_16x16x32_bf16 v[164:167], a[204:207], v[120:123], v[164:167]\n" \
"ds_read_b128 a[200:203], v246 offset:16640\n" \
"global_load_lds_dwordx4 v250, s[36:37] offset:1024 sc1\n" \
"v_add_f32 v241, v241, v9\n" \
"v_mfma_f32_16x16x32_bf16 a[8:11], a[208:211], v[64:67], a[8:11]\n" \
"v_exp_f32 v10, v10\n" \
"v_mfma_f32_16x16x32_bf16 a[8:11], a[212:215], v[96:99], a[8:11]\n" \
"v_add_f32 v241, v241, v10\n" \
"v_cvt_pk_bf16_f32 v8, v8, v9\n" \
"v_mfma_f32_16x16x32_bf16 a[40:43], a[208:211], v[72:75], a[40:43]\n" \
"v_exp_f32 v11, v11\n" \
"v_mfma_f32_16x16x32_bf16 a[40:43], a[212:215], v[104:107], a[40:43]\n" \
"ds_read_b128 a[204:207], v246 offset:18688\n" \
"v_add_f32 v241, v241, v11\n" \
"v_mfma_f32_16x16x32_bf16 v[136:139], a[208:211], v[80:83], v[136:139]\n" \
"v_exp_f32 v12, v12\n" \
"v_mfma_f32_16x16x32_bf16 v[136:139], a[212:215], v[112:115], v[136:139]\n" \
"v_add_f32 v241, v241, v12\n" \
"v_cvt_pk_bf16_f32 v9, v10, v11\n" \
"v_mfma_f32_16x16x32_bf16 v[168:171], a[208:211], v[88:91], v[168:171]\n" \
"v_exp_f32 v13, v13\n" \
"v_mfma_f32_16x16x32_bf16 v[168:171], a[212:215], v[120:123], v[168:171]\n" \
"ds_read_b128 a[208:211], v246 offset:24576\n" \
"v_add_f32 v241, v241, v13\n" \
"v_mfma_f32_16x16x32_bf16 a[12:15], a[216:219], v[64:67], a[12:15]\n" \
"v_exp_f32 v14, v14\n" \
"v_mfma_f32_16x16x32_bf16 a[12:15], a[220:223], v[96:99], a[12:15]\n" \
"v_add_f32 v241, v241, v14\n" \
"v_cvt_pk_bf16_f32 v10, v12, v13\n" \
"v_mfma_f32_16x16x32_bf16 a[44:47], a[216:219], v[72:75], a[44:47]\n" \
"v_exp_f32 v15, v15\n" \
"v_mfma_f32_16x16x32_bf16 a[44:47], a[220:223], v[104:107], a[44:47]\n" \
"ds_read_b128 a[212:215], v246 offset:26624\n" \
"global_load_lds_dwordx4 v250, s[36:37] offset:2048 sc1\n" \
"v_add_f32 v241, v241, v15\n" \
"v_mfma_f32_16x16x32_bf16 v[140:143], a[216:219], v[80:83], v[140:143]\n" \
"v_exp_f32 v16, v16\n" \
"v_mfma_f32_16x16x32_bf16 v[140:143], a[220:223], v[112:115], v[140:143]\n" \
"v_add_f32 v242, v242, v16\n" \
"v_cvt_pk_bf16_f32 v11, v14, v15\n" \
"v_mfma_f32_16x16x32_bf16 v[172:175], a[216:219], v[88:91], v[172:175]\n" \
"s_waitcnt lgkmcnt(6)\n" \
"v_exp_f32 v17, v17\n" \
"v_mfma_f32_16x16x32_bf16 v[172:175], a[220:223], v[120:123], v[172:175]\n" \
"ds_read_b128 a[216:219], v246 offset:24832\n" \
"v_add_f32 v242, v242, v17\n" \
"v_mfma_f32_16x16x32_bf16 a[16:19], a[224:227], v[64:67], a[16:19]\n" \
"v_exp_f32 v18, v18\n" \
"v_mfma_f32_16x16x32_bf16 a[16:19], a[228:231], v[96:99], a[16:19]\n" \
"v_add_f32 v242, v242, v18\n" \
"v_cvt_pk_bf16_f32 v16, v16, v17\n" \
"v_mfma_f32_16x16x32_bf16 a[48:51], a[224:227], v[72:75], a[48:51]\n" \
"v_exp_f32 v19, v19\n" \
"v_mfma_f32_16x16x32_bf16 a[48:51], a[228:231], v[104:107], a[48:51]\n" \
"ds_read_b128 a[220:223], v246 offset:26880\n" \
"v_add_f32 v242, v242, v19\n" \
"v_exp_f32 v20, v20\n" \
"v_mfma_f32_16x16x32_bf16 v[144:147], a[224:227], v[80:83], v[144:147]\n" \
"v_add_f32 v242, v242, v20\n" \
"v_cvt_pk_bf16_f32 v17, v18, v19\n" \
"v_mfma_f32_16x16x32_bf16 v[144:147], a[228:231], v[112:115], v[144:147]\n" \
"v_exp_f32 v21, v21\n" \
"v_mfma_f32_16x16x32_bf16 v[176:179], a[224:227], v[88:91], v[176:179]\n" \
"global_load_lds_dwordx4 v250, s[36:37] offset:3072 sc1\n" \
"s_add_u32 s36, s36, 17408\n" \
"v_mfma_f32_16x16x32_bf16 v[176:179], a[228:231], v[120:123], v[176:179]\n" \
"s_xor_b32 s73, s73, 0x8000\n" \
"ds_read_b128 a[224:227], v246 offset:20480\n" \
"s_xor_b64 s[52:53], s[44:45], s[42:43]\n" \
"v_mfma_f32_16x16x32_bf16 a[20:23], a[232:235], v[64:67], a[20:23]\n" \
"s_xor_b64 s[54:55], s[46:47], s[42:43]\n" \
"s_or_b64 s[52:53], s[52:53], s[54:55]\n" \
"s_xor_b64 s[54:55], s[48:49], s[42:43]\n" \
"v_mfma_f32_16x16x32_bf16 a[20:23], a[236:239], v[96:99], a[20:23]\n" \
"s_or_b64 s[52:53], s[52:53], s[54:55]\n" \
"s_xor_b64 s[54:55], s[50:51], s[42:43]\n" \
"s_or_b64 s[52:53], s[52:53], s[54:55]\n" \
"v_mfma_f32_16x16x32_bf16 a[52:55], a[232:235], v[72:75], a[52:55]\n" \
"s_and_b64 s[52:53], s[52:53], s[8:9]\n" \
"s_cbranch_scc1 Lrs0_%=\n" \
"Lrr0_%=:\n" \
"v_mfma_f32_16x16x32_bf16 a[52:55], a[236:239], v[104:107], a[52:55]\n" \
"s_mov_b64 s[8:9], s[10:11]\n" \
"ds_read_b128 a[228:231], v246 offset:22528\n" \
"v_add_f32 v242, v242, v21\n" \
"v_mfma_f32_16x16x32_bf16 v[148:151], a[232:235], v[80:83], v[148:151]\n" \
"v_exp_f32 v22, v22\n" \
"v_mfma_f32_16x16x32_bf16 v[148:151], a[236:239], v[112:115], v[148:151]\n" \
"v_add_f32 v242, v242, v22\n" \
"v_cvt_pk_bf16_f32 v18, v20, v21\n" \
"v_mfma_f32_16x16x32_bf16 v[180:183], a[232:235], v[88:91], v[180:183]\n" \
"v_exp_f32 v23, v23\n" \
"v_mfma_f32_16x16x32_bf16 v[180:183], a[236:239], v[120:123], v[180:183]\n" \
"ds_read_b128 a[232:235], v246 offset:20736\n" \
"v_add_f32 v242, v242, v23\n" \
"v_mfma_f32_16x16x32_bf16 a[24:27], a[240:243], v[64:67], a[24:27]\n" \
"v_exp_f32 v24, v24\n" \
"v_mfma_f32_16x16x32_bf16 a[24:27], a[244:247], v[96:99], a[24:27]\n" \
"v_add_f32 v243, v243, v24\n" \
"v_cvt_pk_bf16_f32 v19, v22, v23\n" \
"v_mfma_f32_16x16x32_bf16 a[56:59], a[240:243], v[72:75], a[56:59]\n" \
"v_exp_f32 v25, v25\n" \
"v_mfma_f32_16x16x32_bf16 a[56:59], a[244:247], v[104:107], a[56:59]\n" \
"ds_read_b128 a[236:239], v246 offset:22784\n" \
"v_add_f32 v243, v243, v25\n" \
"v_mfma_f32_16x16x32_bf16 v[152:155], a[240:243], v[80:83], v[152:155]\n" \
"v_exp_f32 v26, v26\n" \
"v_mfma_f32_16x16x32_bf16 v[152:155], a[244:247], v[112:115], v[152:155]\n" \
"v_add_f32 v243, v243, v26\n" \
"v_cvt_pk_bf16_f32 v24, v24, v25\n" \
"v_mfma_f32_16x16x32_bf16 v[184:187], a[240:243], v[88:91], v[184:187]\n" \
"v_exp_f32 v27, v27\n" \
"v_mfma_f32_16x16x32_bf16 v[184:187], a[244:247], v[120:123], v[184:187]\n" \
"ds_read_b128 a[240:243], v246 offset:28672\n" \
"v_add_f32 v243, v243, v27\n" \
"v_mfma_f32_16x16x32_bf16 a[28:31], a[248:251], v[64:67], a[28:31]\n" \
"v_exp_f32 v28, v28\n" \
"v_mfma_f32_16x16x32_bf16 a[28:31], a[252:255], v[96:99], a[28:31]\n" \
"Lpg5524_%=:\n" \
"s_cbranch_execz Lpg6809_%=\n" \
"v_add_f32 v243, v243, v28\n" \
"v_cvt_pk_bf16_f32 v25, v26, v27\n" \
"v_mfma_f32_16x16x32_bf16 a[60:63], a[248:251], v[72:75], a[60:63]\n" \
"v_exp_f32 v29, v29\n" \
"v_mfma_f32_16x16x32_bf16 a[60:63], a[252:255], v[104:107], a[60:63]\n" \
"ds_read_b128 a[244:247], v246 offset:30720\n" \
"v_add_f32 v243, v243, v29\n" \
"v_mfma_f32_16x16x32_bf16 v[156:159], a[248:251], v[80:83], v[156:159]\n" \
"v_exp_f32 v30, v30\n" \
"v_mfma_f32_16x16x32_bf16 v[156:159], a[252:255], v[112:115], v[156:159]\n" \
"v_add_f32 v243, v243, v30\n" \
"v_cvt_pk_bf16_f32 v26, v28, v29\n" \
"v_mfma_f32_16x16x32_bf16 v[188:191], a[248:251], v[88:91], v[188:191]\n" \
"s_waitcnt lgkmcnt(6)\n" \
"v_exp_f32 v31, v31\n" \
"v_mfma_f32_16x16x32_bf16 v[188:191], a[252:255], v[120:123], v[188:191]\n" \
"ds_read_b128 a[248:251], v246 offset:28928\n" \
"v_add_f32 v243, v243, v31\n" \
"LA1_%=:\n" \
"Lpg5544_%=:\n" \
"s_cbranch_execz Lpg7118_%=\n" \
"v_mfma_f32_16x16x32_bf16 v[64:67], a[192:195], a[128:131], v[224:227]\n" \
"v_exp_f32 v32, v32\n" \
"v_mfma_f32_16x16x32_bf16 v[64:67], a[196:199], a[132:135], v[64:67]\n" \
"v_cvt_pk_bf16_f32 v27, v30, v31\n" \
"v_add_f32 v240, v240, v32\n" \
"v_mfma_f32_16x16x32_bf16 v[72:75], a[192:195], a[144:147], v[228:231]\n" \
"s_mov_b32 m0, s74\n" \
"v_exp_f32 v33, v33\n" \
"v_mfma_f32_16x16x32_bf16 v[72:75], a[196:199], a[148:151], v[72:75]\n" \
"ds_read_b128 a[252:255], v246 offset:30976\n" \
"global_load_lds_dwordx4 v250, s[38:39] offset:0 sc1\n" \
"v_add_f32 v240, v240, v33\n" \
"v_mfma_f32_16x16x32_bf16 v[80:83], a[192:195], a[160:163], v[232:235]\n" \
"v_xor_b32 v246, 0x8000, v246\n" \
"v_exp_f32 v34, v34\n" \
"v_mfma_f32_16x16x32_bf16 v[80:83], a[196:199], a[164:167], v[80:83]\n" \
"v_add_f32 v240, v240, v34\n" \
"v_cvt_pk_bf16_f32 v32, v32, v33\n" \
"v_mfma_f32_16x16x32_bf16 v[88:91], a[192:195], a[176:179], v[236:239]\n" \
"v_exp_f32 v35, v35\n" \
"v_mfma_f32_16x16x32_bf16 v[88:91], a[196:199], a[180:183], v[88:91]\n" \
"ds_read_b128 a[192:195], v247 offset:0\n" \
"v_add_f32 v240, v240, v35\n" \
"v_mfma_f32_16x16x32_bf16 v[68:71], a[200:203], a[128:131], v[224:227]\n" \
"v_exp_f32 v36, v36\n" \
"v_mfma_f32_16x16x32_bf16 v[68:71], a[204:207], a[132:135], v[68:71]\n" \
"v_add_f32 v240, v240, v36\n" \
"v_cvt_pk_bf16_f32 v33, v34, v35\n" \
"v_mfma_f32_16x16x32_bf16 v[76:79], a[200:203], a[144:147], v[228:231]\n" \
"v_exp_f32 v37, v37\n" \
"v_mfma_f32_16x16x32_bf16 v[76:79], a[204:207], a[148:151], v[76:79]\n" \
"ds_read_b128 a[196:199], v247 offset:8192\n" \
"v_add_f32 v240, v240, v37\n" \
"v_mfma_f32_16x16x32_bf16 v[84:87], a[200:203], a[160:163], v[232:235]\n" \
"v_exp_f32 v38, v38\n" \
"v_mfma_f32_16x16x32_bf16 v[84:87], a[204:207], a[164:167], v[84:87]\n" \
"v_add_f32 v240, v240, v38\n" \
"v_cvt_pk_bf16_f32 v34, v36, v37\n" \
"v_mfma_f32_16x16x32_bf16 v[92:95], a[200:203], a[176:179], v[236:239]\n" \
"v_exp_f32 v39, v39\n" \
"v_mfma_f32_16x16x32_bf16 v[92:95], a[204:207], a[180:183], v[92:95]\n" \
"ds_read_b128 a[200:203], v247 offset:1024\n" \
"global_load_lds_dwordx4 v250, s[38:39] offset:1024 sc1\n" \
"v_add_f32 v240, v240, v39\n" \
"v_mfma_f32_16x16x32_bf16 v[96:99], a[208:211], a[128:131], v[224:227]\n" \
"v_exp_f32 v40, v40\n" \
"v_mfma_f32_16x16x32_bf16 v[96:99], a[212:215], a[132:135], v[96:99]\n" \
"v_add_f32 v241, v241, v40\n" \
"v_cvt_pk_bf16_f32 v35, v38, v39\n" \
"v_mfma_f32_16x16x32_bf16 v[104:107], a[208:211], a[144:147], v[228:231]\n" \
"v_exp_f32 v41, v41\n" \
"v_mfma_f32_16x16x32_bf16 v[104:107], a[212:215], a[148:151], v[104:107]\n" \
"ds_read_b128 a[204:207], v247 offset:9216\n" \
"v_add_f32 v241, v241, v41\n" \
"v_mfma_f32_16x16x32_bf16 v[112:115], a[208:211], a[160:163], v[232:235]\n" \
"v_exp_f32 v42, v42\n" \
"v_mfma_f32_16x16x32_bf16 v[112:115], a[212:215], a[164:167], v[112:115]\n" \
"v_add_f32 v241, v241, v42\n" \
"v_cvt_pk_bf16_f32 v40, v40, v41\n" \
"v_mfma_f32_16x16x32_bf16 v[120:123], a[208:211], a[176:179], v[236:239]\n" \
"v_exp_f32 v43, v43\n" \
"v_mfma_f32_16x16x32_bf16 v[120:123], a[212:215], a[180:183], v[120:123]\n" \
"ds_read_b128 a[208:211], v247 offset:2048\n" \
"v_add_f32 v241, v241, v43\n" \
"v_mfma_f32_16x16x32_bf16 v[100:103], a[216:219], a[128:131], v[224:227]\n" \
"v_exp_f32 v44, v44\n" \
"v_mfma_f32_16x16x32_bf16 v[100:103], a[220:223], a[132:135], v[100:103]\n" \
"v_add_f32 v241, v241, v44\n" \
"v_cvt_pk_bf16_f32 v41, v42, v43\n" \
"v_mfma_f32_16x16x32_bf16 v[108:111], a[216:219], a[144:147], v[228:231]\n" \
"v_exp_f32 v45, v45\n" \
"v_mfma_f32_16x16x32_bf16 v[108:111], a[220:223], a[148:151], v[108:111]\n" \
"ds_read_b128 a[212:215], v247 offset:10240\n" \
"global_load_lds_dwordx4 v250, s[38:39] offset:2048 sc1\n" \
"v_add_f32 v241, v241, v45\n" \
"v_mfma_f32_16x16x32_bf16 v[116:119], a[216:219], a[160:163], v[232:235]\n" \
"v_exp_f32 v46, v46\n" \
"v_mfma_f32_16x16x32_bf16 v[116:119], a[220:223], a[164:167], v[116:119]\n" \
"v_add_f32 v241, v241, v46\n" \
"v_cvt_pk_bf16_f32 v42, v44, v45\n" \
"v_mfma_f32_16x16x32_bf16 v[124:127], a[216:219], a[176:179], v[236:239]\n" \
"s_waitcnt lgkmcnt(6)\n" \
"v_exp_f32 v47, v47\n" \
"v_mfma_f32_16x16x32_bf16 v[124:127], a[220:223], a[180:183], v[124:127]\n" \
"ds_read_b128 a[216:219], v247 offset:3072\n" \
"v_add_f32 v241, v241, v47\n" \
"v_mfma_f32_16x16x32_bf16 v[64:67], a[224:227], a[136:139], v[64:67]\n" \
"v_exp_f32 v48, v48\n" \
"v_mfma_f32_16x16x32_bf16 v[64:67], a[228:231], a[140:143], v[64:67]\n" \
"v_add_f32 v242, v242, v48\n" \
"v_cvt_pk_bf16_f32 v43, v46, v47\n" \
"v_mfma_f32_16x16x32_bf16 v[72:75], a[224:227], a[152:155], v[72:75]\n" \
"v_exp_f32 v49, v49\n" \
"v_mfma_f32_16x16x32_bf16 v[72:75], a[228:231], a[156:159], v[72:75]\n" \
"ds_read_b128 a[220:223], v247 offset:11264\n" \
"v_add_f32 v242, v242, v49\n" \
"v_exp_f32 v50, v50\n" \
"v_mfma_f32_16x16x32_bf16 v[80:83], a[224:227], a[168:171], v[80:83]\n" \
"v_add_f32 v242, v242, v50\n" \
"v_cvt_pk_bf16_f32 v48, v48, v49\n" \
"v_mfma_f32_16x16x32_bf16 v[80:83], a[228:231], a[172:175], v[80:83]\n" \
"v_exp_f32 v51, v51\n" \
"v_mfma_f32_16x16x32_bf16 v[88:91], a[224:227], a[184:187], v[88:91]\n" \
"v_add_f32 v242, v242, v51\n" \
"v_exp_f32 v52, v52\n" \
"v_mfma_f32_16x16x32_bf16 v[88:91], a[228:231], a[188:191], v[88:91]\n" \
"ds_read_b128 a[224:227], v247 offset:4096\n" \
"global_load_lds_dwordx4 v250, s[38:39] offset:3072 sc1\n" \
"v_mfma_f32_16x16x32_bf16 v[68:71], a[232:235], a[136:139], v[68:71]\n" \
"s_add_u32 s38, s38, 17408\n" \
"v_add_f32 v242, v242, v52\n" \
"v_cvt_pk_bf16_f32 v49, v50, v51\n" \
"v_mfma_f32_16x16x32_bf16 v[68:71], a[236:239], a[140:143], v[68:71]\n" \
"v_exp_f32 v53, v53\n" \
"v_mfma_f32_16x16x32_bf16 v[76:79], a[232:235], a[152:155], v[76:79]\n" \
"v_add_f32 v242, v242, v53\n" \
"v_exp_f32 v54, v54\n" \
"v_mfma_f32_16x16x32_bf16 v[76:79], a[236:239], a[156:159], v[76:79]\n" \
"ds_read_b128 a[228:231], v247 offset:12288\n" \
"v_add_f32 v242, v242, v54\n" \
"v_cvt_pk_bf16_f32 v50, v52, v53\n" \
"v_mfma_f32_16x16x32_bf16 v[84:87], a[232:235], a[168:171], v[84:87]\n" \
"v_exp_f32 v55, v55\n" \
"v_mfma_f32_16x16x32_bf16 v[84:87], a[236:239], a[172:175], v[84:87]\n" \
"global_load_dword v253, v252, s[40:41]\n" \
"v_add_f32 v242, v242, v55\n" \
"v_mfma_f32_16x16x32_bf16 v[92:95], a[232:235], a[184:187], v[92:95]\n" \
"s_sub_u32 s80, s80, 1\n" \
"s_cmp_gt_i32 s80, 0\n" \
"s_cselect_b32 s81, 0x80000, 0\n" \
"v_mfma_f32_16x16x32_bf16 v[92:95], a[236:239], a[188:191], v[92:95]\n" \
"s_add_u32 s40, s40, s81\n" \
"ds_read_b128 a[232:235], v247 offset:5120\n" \
"s_addc_u32 s41, s41, 0\n" \
"v_mfma_f32_16x16x32_bf16 v[96:99], a[240:243], a[136:139], v[96:99]\n" \
"v_exp_f32 v56, v56\n" \
"v_mfma_f32_16x16x32_bf16 v[96:99], a[244:247], a[140:143], v[96:99]\n" \
"v_add_f32 v243, v243, v56\n" \
"v_cvt_pk_bf16_f32 v51, v54, v55\n" \
"v_mfma_f32_16x16x32_bf16 v[104:107], a[240:243], a[152:155], v[104:107]\n" \
"v_exp_f32 v57, v57\n" \
"v_mfma_f32_16x16x32_bf16 v[104:107], a[244:247], a[156:159], v[104:107]\n" \
"ds_read_b128 a[236:239], v247 offset:13312\n" \
"v_add_f32 v243, v243, v57\n" \
"v_mfma_f32_16x16x32_bf16 v[112:115], a[240:243], a[168:171], v[112:115]\n" \
"v_exp_f32 v58, v58\n" \
"v_mfma_f32_16x16x32_bf16 v[112:115], a[244:247], a[172:175], v[112:115]\n" \
"v_add_f32 v243, v243, v58\n" \
"v_cvt_pk_bf16_f32 v56, v56, v57\n" \
"v_mfma_f32_16x16x32_bf16 v[120:123], a[240:243], a[184:187], v[120:123]\n" \
"v_exp_f32 v59, v59\n" \
"v_mfma_f32_16x16x32_bf16 v[120:123], a[244:247], a[188:191], v[120:123]\n" \
"ds_read_b128 a[240:243], v247 offset:6144\n" \
"v_add_f32 v243, v243, v59\n" \
"v_mfma_f32_16x16x32_bf16 v[100:103], a[248:251], a[136:139], v[100:103]\n" \
"v_exp_f32 v60, v60\n" \
"v_mfma_f32_16x16x32_bf16 v[100:103], a[252:255], a[140:143], v[100:103]\n" \
"v_add_f32 v243, v243, v60\n" \
"v_cvt_pk_bf16_f32 v57, v58, v59\n" \
"v_mfma_f32_16x16x32_bf16 v[108:111], a[248:251], a[152:155], v[108:111]\n" \
"v_exp_f32 v61, v61\n" \
"v_mfma_f32_16x16x32_bf16 v[108:111], a[252:255], a[156:159], v[108:111]\n" \
"ds_read_b128 a[244:247], v247 offset:14336\n" \
"v_add_f32 v243, v243, v61\n" \
"v_mfma_f32_16x16x32_bf16 v[116:119], a[248:251], a[168:171], v[116:119]\n" \
"v_exp_f32 v62, v62\n" \
"v_mfma_f32_16x16x32_bf16 v[116:119], a[252:255], a[172:175], v[116:119]\n" \
"v_add_f32 v243, v243, v62\n" \
"v_cvt_pk_bf16_f32 v58, v60, v61\n" \
"v_mfma_f32_16x16x32_bf16 v[124:127], a[248:251], a[184:187], v[124:127]\n" \
"s_waitcnt vmcnt(10) lgkmcnt(6)\n" \
"v_exp_f32 v63, v63\n" \
"v_mfma_f32_16x16x32_bf16 v[124:127], a[252:255], a[188:191], v[124:127]\n" \
"s_barrier\n" \
"ds_read_b128 a[248:251], v247 offset:7168\n" \
"v_add_f32 v243, v243, v63\n" \
"v_mfma_f32_16x16x32_bf16 a[0:3], a[192:195], v[0:3], a[0:3]\n" \
"v_exp_f32 v64, v64\n" \
"v_mfma_f32_16x16x32_bf16 a[0:3], a[196:199], v[32:35], a[0:3]\n" \
"v_cvt_pk_bf16_f32 v59, v62, v63\n" \
"v_add_f32 v240, v240, v64\n" \
"v_mfma_f32_16x16x32_bf16 a[32:35], a[192:195], v[8:11], a[32:35]\n" \
"s_mov_b32 m0, s73\n" \
"v_exp_f32 v65, v65\n" \
"v_mfma_f32_16x16x32_bf16 a[32:35], a[196:199], v[40:43], a[32:35]\n" \
"ds_read_b128 a[252:255], v247 offset:15360\n" \
"global_load_lds_dwordx4 v250, s[36:37] offset:0 sc1\n" \
"v_add_f32 v240, v240, v65\n" \
"v_mfma_f32_16x16x32_bf16 v[128:131], a[192:195], v[16:19], v[128:131]\n" \
"v_exp_f32 v66, v66\n" \
"v_mfma_f32_16x16x32_bf16 v[128:131], a[196:199], v[48:51], v[128:131]\n" \
"v_add_f32 v240, v240, v66\n" \
"v_cvt_pk_bf16_f32 v64, v64, v65\n" \
"v_mfma_f32_16x16x32_bf16 v[160:163], a[192:195], v[24:27], v[160:163]\n" \
"v_exp_f32 v67, v67\n" \
"v_mfma_f32_16x16x32_bf16 v[160:163], a[196:199], v[56:59], v[160:163]\n" \
"ds_read_b128 a[192:195], v246 offset:0\n" \
"v_add_f32 v240, v240, v67\n" \
"v_mfma_f32_16x16x32_bf16 a[4:7], a[200:203], v[0:3], a[4:7]\n" \
"v_exp_f32 v68, v68\n" \
"v_mfma_f32_16x16x32_bf16 a[4:7], a[204:207], v[32:35], a[4:7]\n" \
"v_add_f32 v240, v240, v68\n" \
"v_cvt_pk_bf16_f32 v65, v66, v67\n" \
"v_mfma_f32_16x16x32_bf16 a[36:39], a[200:203], v[8:11], a[36:39]\n" \
"v_exp_f32 v69, v69\n" \
"v_mfma_f32_16x16x32_bf16 a[36:39], a[204:207], v[40:43], a[36:39]\n" \
"ds_read_b128 a[196:199], v246 offset:2048\n" \
"v_add_f32 v240, v240, v69\n" \
"v_mfma_f32_16x16x32_bf16 v[132:135], a[200:203], v[16:19], v[132:135]\n" \
"v_exp_f32 v70, v70\n" \
"v_mfma_f32_16x16x32_bf16 v[132:135], a[204:207], v[48:51], v[132:135]\n" \
"v_add_f32 v240, v240, v70\n" \
"v_cvt_pk_bf16_f32 v66, v68, v69\n" \
"v_mfma_f32_16x16x32_bf16 v[164:167], a[200:203], v[24:27], v[164:167]\n" \
"v_exp_f32 v71, v71\n" \
"v_mfma_f32_16x16x32_bf16 v[164:167], a[204:207], v[56:59], v[164:167]\n" \
"ds_read_b128 a[200:203], v246 offset:256\n" \
"global_load_lds_dwordx4 v250, s[36:37] offset:1024 sc1\n" \
"v_add_f32 v240, v240, v71\n" \
"v_mfma_f32_16x16x32_bf16 a[8:11], a[208:211], v[0:3], a[8:11]\n" \
"v_exp_f32 v72, v72\n" \
"v_mfma_f32_16x16x32_bf16 a[8:11], a[212:215], v[32:35], a[8:11]\n" \
"v_add_f32 v241, v241, v72\n" \
"v_cvt_pk_bf16_f32 v67, v70, v71\n" \
"v_mfma_f32_16x16x32_bf16 a[40:43], a[208:211], v[8:11], a[40:43]\n" \
"v_exp_f32 v73, v73\n" \
"v_mfma_f32_16x16x32_bf16 a[40:43], a[212:215], v[40:43], a[40:43]\n" \
"ds_read_b128 a[204:207], v246 offset:2304\n" \
"v_add_f32 v241, v241, v73\n" \
"v_mfma_f32_16x16x32_bf16 v[136:139], a[208:211], v[16:19], v[136:139]\n" \
"v_exp_f32 v74, v74\n" \
"v_mfma_f32_16x16x32_bf16 v[136:139], a[212:215], v[48:51], v[136:139]\n" \
"v_add_f32 v241, v241, v74\n" \
"v_cvt_pk_bf16_f32 v72, v72, v73\n" \
"v_mfma_f32_16x16x32_bf16 v[168:171], a[208:211], v[24:27], v[168:171]\n" \
"v_exp_f32 v75, v75\n" \
"v_mfma_f32_16x16x32_bf16 v[168:171], a[212:215], v[56:59], v[168:171]\n" \
"ds_read_b128 a[208:211], v246 offset:8192\n" \
"v_add_f32 v241, v241, v75\n" \
"v_mfma_f32_16x16x32_bf16 a[12:15], a[216:219], v[0:3], a[12:15]\n" \
"v_exp_f32 v76, v76\n" \
"v_mfma_f32_16x16x32_bf16 a[12:15], a[220:223], v[32:35], a[12:15]\n" \
"v_add_f32 v241, v241, v76\n" \
"v_cvt_pk_bf16_f32 v73, v74, v75\n" \
"v_mfma_f32_16x16x32_bf16 a[44:47], a[216:219], v[8:11], a[44:47]\n" \
"v_exp_f32 v77, v77\n" \
"v_mfma_f32_16x16x32_bf16 a[44:47], a[220:223], v[40:43], a[44:47]\n" \
"ds_read_b128 a[212:215], v246 offset:10240\n" \
"global_load_lds_dwordx4 v250, s[36:37] offset:2048 sc1\n" \
"v_add_f32 v241, v241, v77\n" \
"v_mfma_f32_16x16x32_bf16 v[140:143], a[216:219], v[16:19], v[140:143]\n" \
"v_exp_f32 v78, v78\n" \
"v_mfma_f32_16x16x32_bf16 v[140:143], a[220:223], v[48:51], v[140:143]\n" \
"v_add_f32 v241, v241, v78\n" \
"v_cvt_pk_bf16_f32 v74, v76, v77\n" \
"v_mfma_f32_16x16x32_bf16 v[172:175], a[216:219], v[24:27], v[172:175]\n" \
"s_waitcnt lgkmcnt(6)\n" \
"v_exp_f32 v79, v79\n" \
"v_mfma_f32_16x16x32_bf16 v[172:175], a[220:223], v[56:59], v[172:175]\n" \
"ds_read_b128 a[216:219], v246 offset:8448\n" \
"v_add_f32 v241, v241, v79\n" \
"v_mfma_f32_16x16x32_bf16 a[16:19], a[224:227], v[0:3], a[16:19]\n" \
"v_exp_f32 v80, v80\n" \
"v_mfma_f32_16x16x32_bf16 a[16:19], a[228:231], v[32:35], a[16:19]\n" \
"v_add_f32 v242, v242, v80\n" \
"v_cvt_pk_bf16_f32 v75, v78, v79\n" \
"v_mfma_f32_16x16x32_bf16 a[48:51], a[224:227], v[8:11], a[48:51]\n" \
"v_exp_f32 v81, v81\n" \
"v_mfma_f32_16x16x32_bf16 a[48:51], a[228:231], v[40:43], a[48:51]\n" \
"ds_read_b128 a[220:223], v246 offset:10496\n" \
"v_add_f32 v242, v242, v81\n" \
"v_mfma_f32_16x16x32_bf16 v[144:147], a[224:227], v[16:19], v[144:147]\n" \
"v_exp_f32 v82, v82\n" \
"v_mfma_f32_16x16x32_bf16 v[144:147], a[228:231], v[48:51], v[144:147]\n" \
"v_add_f32 v242, v242, v82\n" \
"v_cvt_pk_bf16_f32 v80, v80, v81\n" \
"v_mfma_f32_16x16x32_bf16 v[176:179], a[224:227], v[24:27], v[176:179]\n" \
"v_exp_f32 v83, v83\n" \
"v_mfma_f32_16x16x32_bf16 v[176:179], a[228:231], v[56:59], v[176:179]\n" \
"ds_read_b128 a[224:227], v246 offset:4096\n" \
"global_load_lds_dwordx4 v250, s[36:37] offset:3072 sc1\n" \
"v_add_f32 v242, v242, v83\n" \
"v_mfma_f32_16x16x32_bf16 a[20:23], a[232:235], v[0:3], a[20:23]\n" \
"s_add_u32 s36, s36, 17408\n" \
"v_exp_f32 v84, v84\n" \
"v_mfma_f32_16x16x32_bf16 a[20:23], a[236:239], v[32:35], a[20:23]\n" \
"v_add_f32 v242, v242, v84\n" \
"v_cvt_pk_bf16_f32 v81, v82, v83\n" \
"v_mfma_f32_16x16x32_bf16 a[52:55], a[232:235], v[8:11], a[52:55]\n" \
"v_exp_f32 v85, v85\n" \
"v_mfma_f32_16x16x32_bf16 a[52:55], a[236:239], v[40:43], a[52:55]\n" \
"ds_read_b128 a[228:231], v246 offset:6144\n" \
"v_add_f32 v242, v242, v85\n" \
"v_mfma_f32_16x16x32_bf16 v[148:151], a[232:235], v[16:19], v[148:151]\n" \
"v_exp_f32 v86, v86\n" \
"v_mfma_f32_16x16x32_bf16 v[148:151], a[236:239], v[48:51], v[148:151]\n" \
"v_add_f32 v242, v242, v86\n" \
"v_cvt_pk_bf16_f32 v82, v84, v85\n" \
"v_mfma_f32_16x16x32_bf16 v[180:183], a[232:235], v[24:27], v[180:183]\n" \
"v_exp_f32 v87, v87\n" \
"v_mfma_f32_16x16x32_bf16 v[180:183], a[236:239], v[56:59], v[180:183]\n" \
"ds_read_b128 a[232:235], v246 offset:4352\n" \
"v_add_f32 v242, v242, v87\n" \
"v_mfma_f32_16x16x32_bf16 a[24:27], a[240:243], v[0:3], a[24:27]\n" \
"v_exp_f32 v88, v88\n" \
"v_mfma_f32_16x16x32_bf16 a[24:27], a[244:247], v[32:35], a[24:27]\n" \
"v_add_f32 v243, v243, v88\n" \
"v_cvt_pk_bf16_f32 v83, v86, v87\n" \
"v_mfma_f32_16x16x32_bf16 a[56:59], a[240:243], v[8:11], a[56:59]\n" \
"v_exp_f32 v89, v89\n" \
"v_mfma_f32_16x16x32_bf16 a[56:59], a[244:247], v[40:43], a[56:59]\n" \
"ds_read_b128 a[236:239], v246 offset:6400\n" \
"v_add_f32 v243, v243, v89\n" \
"v_mfma_f32_16x16x32_bf16 v[152:155], a[240:243], v[16:19], v[152:155]\n" \
"v_exp_f32 v90, v90\n" \
"v_mfma_f32_16x16x32_bf16 v[152:155], a[244:247], v[48:51], v[152:155]\n" \
"v_add_f32 v243, v243, v90\n" \
"v_cvt_pk_bf16_f32 v88, v88, v89\n" \
"v_mfma_f32_16x16x32_bf16 v[184:187], a[240:243], v[24:27], v[184:187]\n" \
"v_exp_f32 v91, v91\n" \
"v_mfma_f32_16x16x32_bf16 v[184:187], a[244:247], v[56:59], v[184:187]\n" \
"ds_read_b128 a[240:243], v246 offset:12288\n" \
"v_add_f32 v243, v243, v91\n" \
"v_mfma_f32_16x16x32_bf16 a[28:31], a[248:251], v[0:3], a[28:31]\n" \
"v_exp_f32 v92, v92\n" \
"v_mfma_f32_16x16x32_bf16 a[28:31], a[252:255], v[32:35], a[28:31]\n" \
"v_add_f32 v243, v243, v92\n" \
"v_cvt_pk_bf16_f32 v89, v90, v91\n" \
"v_mfma_f32_16x16x32_bf16 a[60:63], a[248:251], v[8:11], a[60:63]\n" \
"v_exp_f32 v93, v93\n" \
"v_mfma_f32_16x16x32_bf16 a[60:63], a[252:255], v[40:43], a[60:63]\n" \
"ds_read_b128 a[244:247], v246 offset:14336\n" \
"v_add_f32 v243, v243, v93\n" \
"v_mfma_f32_16x16x32_bf16 v[156:159], a[248:251], v[16:19], v[156:159]\n" \
"v_exp_f32 v94, v94\n" \
"v_mfma_f32_16x16x32_bf16 v[156:159], a[252:255], v[48:51], v[156:159]\n" \
"v_add_f32 v243, v243, v94\n" \
"v_cvt_pk_bf16_f32 v90, v92, v93\n" \
"v_mfma_f32_16x16x32_bf16 v[188:191], a[248:251], v[24:27], v[188:191]\n" \
"s_waitcnt lgkmcnt(6)\n" \
"v_exp_f32 v95, v95\n" \
"v_mfma_f32_16x16x32_bf16 v[188:191], a[252:255], v[56:59], v[188:191]\n" \
"ds_read_b128 a[248:251], v246 offset:12544\n" \
"v_add_f32 v243, v243, v95\n" \
"v_mfma_f32_16x16x32_bf16 v[0:3], a[192:195], a[128:131], v[224:227]\n" \
"v_exp_f32 v96, v96\n" \
"v_mfma_f32_16x16x32_bf16 v[0:3], a[196:199], a[132:135], v[0:3]\n" \
"v_cvt_pk_bf16_f32 v91, v94, v95\n" \
"v_add_f32 v240, v240, v96\n" \
"v_exp_f32 v97, v97\n" \
"v_mfma_f32_16x16x32_bf16 v[8:11], a[192:195], a[144:147], v[228:231]\n" \
"s_add_u32 m0, s74, 0x4000\n" \
"v_add_f32 v240, v240, v97\n" \
"v_exp_f32 v98, v98\n" \
"v_mfma_f32_16x16x32_bf16 v[8:11], a[196:199], a[148:151], v[8:11]\n" \
"ds_read_b128 a[252:255], v246 offset:14592\n" \
"global_load_lds_dwordx4 v250, s[38:39] offset:0 sc1\n" \
"v_add_f32 v240, v240, v98\n" \
"v_cvt_pk_bf16_f32 v96, v96, v97\n" \
"v_exp_f32 v99, v99\n" \
"v_mfma_f32_16x16x32_bf16 v[16:19], a[192:195], a[160:163], v[232:235]\n" \
"v_add_f32 v240, v240, v99\n" \
"v_exp_f32 v100, v100\n" \
"v_mfma_f32_16x16x32_bf16 v[16:19], a[196:199], a[164:167], v[16:19]\n" \
"v_add_f32 v240, v240, v100\n" \
"v_cvt_pk_bf16_f32 v97, v98, v99\n" \
"v_mfma_f32_16x16x32_bf16 v[24:27], a[192:195], a[176:179], v[236:239]\n" \
"v_exp_f32 v101, v101\n" \
"v_mfma_f32_16x16x32_bf16 v[24:27], a[196:199], a[180:183], v[24:27]\n" \
"ds_read_b128 a[192:195], v247 offset:16384\n" \
"v_add_f32 v240, v240, v101\n" \
"v_mfma_f32_16x16x32_bf16 v[4:7], a[200:203], a[128:131], v[224:227]\n" \
"v_exp_f32 v102, v102\n" \
"v_mfma_f32_16x16x32_bf16 v[4:7], a[204:207], a[132:135], v[4:7]\n" \
"v_add_f32 v240, v240, v102\n" \
"v_cvt_pk_bf16_f32 v98, v100, v101\n" \
"v_mfma_f32_16x16x32_bf16 v[12:15], a[200:203], a[144:147], v[228:231]\n" \
"v_exp_f32 v103, v103\n" \
"v_mfma_f32_16x16x32_bf16 v[12:15], a[204:207], a[148:151], v[12:15]\n" \
"ds_read_b128 a[196:199], v247 offset:24576\n" \
"v_add_f32 v240, v240, v103\n" \
"v_mfma_f32_16x16x32_bf16 v[20:23], a[200:203], a[160:163], v[232:235]\n" \
"v_exp_f32 v104, v104\n" \
"v_mfma_f32_16x16x32_bf16 v[20:23], a[204:207], a[164:167], v[20:23]\n" \
"v_add_f32 v241, v241, v104\n" \
"v_cvt_pk_bf16_f32 v99, v102, v103\n" \
"v_mfma_f32_16x16x32_bf16 v[28:31], a[200:203], a[176:179], v[236:239]\n" \
"v_exp_f32 v105, v105\n" \
"v_mfma_f32_16x16x32_bf16 v[28:31], a[204:207], a[180:183], v[28:31]\n" \
"ds_read_b128 a[200:203], v247 offset:17408\n" \
"global_load_lds_dwordx4 v250, s[38:39] offset:1024 sc1\n" \
"v_add_f32 v241, v241, v105\n" \
"v_mfma_f32_16x16x32_bf16 v[32:35], a[208:211], a[128:131], v[224:227]\n" \
"v_exp_f32 v106, v106\n" \
"v_mfma_f32_16x16x32_bf16 v[32:35], a[212:215], a[132:135], v[32:35]\n" \
"v_add_f32 v241, v241, v106\n" \
"v_cvt_pk_bf16_f32 v104, v104, v105\n" \
"v_mfma_f32_16x16x32_bf16 v[40:43], a[208:211], a[144:147], v[228:231]\n" \
"v_exp_f32 v107, v107\n" \
"v_mfma_f32_16x16x32_bf16 v[40:43], a[212:215], a[148:151], v[40:43]\n" \
"ds_read_b128 a[204:207], v247 offset:25600\n" \
"v_add_f32 v241, v241, v107\n" \
"v_mfma_f32_16x16x32_bf16 v[48:51], a[208:211], a[160:163], v[232:235]\n" \
"v_exp_f32 v108, v108\n" \
"v_mfma_f32_16x16x32_bf16 v[48:51], a[212:215], a[164:167], v[48:51]\n" \
"v_add_f32 v241, v241, v108\n" \
"v_cvt_pk_bf16_f32 v105, v106, v107\n" \
"v_mfma_f32_16x16x32_bf16 v[56:59], a[208:211], a[176:179], v[236:239]\n" \
"v_exp_f32 v109, v109\n" \
"v_mfma_f32_16x16x32_bf16 v[56:59], a[212:215], a[180:183], v[56:59]\n" \
"ds_read_b128 a[208:211], v247 offset:18432\n" \
"v_add_f32 v241, v241, v109\n" \
"v_mfma_f32_16x16x32_bf16 v[36:39], a[216:219], a[128:131], v[224:227]\n" \
"v_exp_f32 v110, v110\n" \
"v_mfma_f32_16x16x32_bf16 v[36:39], a[220:223], a[132:135], v[36:39]\n" \
"v_add_f32 v241, v241, v110\n" \
"v_cvt_pk_bf16_f32 v106, v108, v109\n" \
"v_mfma_f32_16x16x32_bf16 v[44:47], a[216:219], a[144:147], v[228:231]\n" \
"v_exp_f32 v111, v111\n" \
"v_mfma_f32_16x16x32_bf16 v[44:47], a[220:223], a[148:151], v[44:47]\n" \
"ds_read_b128 a[212:215], v247 offset:26624\n" \
"global_load_lds_dwordx4 v250, s[38:39] offset:2048 sc1\n" \
"v_add_f32 v241, v241, v111\n" \
"v_mfma_f32_16x16x32_bf16 v[52:55], a[216:219], a[160:163], v[232:235]\n" \
"v_exp_f32 v112, v112\n" \
"v_mfma_f32_16x16x32_bf16 v[52:55], a[220:223], a[164:167], v[52:55]\n" \
"v_add_f32 v242, v242, v112\n" \
"v_cvt_pk_bf16_f32 v107, v110, v111\n" \
"v_mfma_f32_16x16x32_bf16 v[60:63], a[216:219], a[176:179], v[236:239]\n" \
"s_waitcnt lgkmcnt(6)\n" \
"v_exp_f32 v113, v113\n" \
"v_mfma_f32_16x16x32_bf16 v[60:63], a[220:223], a[180:183], v[60:63]\n" \
"ds_read_b128 a[216:219], v247 offset:19456\n" \
"v_add_f32 v242, v242, v113\n" \
"v_mfma_f32_16x16x32_bf16 v[0:3], a[224:227], a[136:139], v[0:3]\n" \
"v_exp_f32 v114, v114\n" \
"v_mfma_f32_16x16x32_bf16 v[0:3], a[228:231], a[140:143], v[0:3]\n" \
"v_add_f32 v242, v242, v114\n" \
"v_cvt_pk_bf16_f32 v112, v112, v113\n" \
"v_mfma_f32_16x16x32_bf16 v[8:11], a[224:227], a[152:155], v[8:11]\n" \
"v_exp_f32 v115, v115\n" \
"v_mfma_f32_16x16x32_bf16 v[8:11], a[228:231], a[156:159], v[8:11]\n" \
"ds_read_b128 a[220:223], v247 offset:27648\n" \
"v_add_f32 v242, v242, v115\n" \
"v_mfma_f32_16x16x32_bf16 v[16:19], a[224:227], a[168:171], v[16:19]\n" \
"v_exp_f32 v116, v116\n" \
"v_mfma_f32_16x16x32_bf16 v[16:19], a[228:231], a[172:175], v[16:19]\n" \
"v_add_f32 v242, v242, v116\n" \
"v_cvt_pk_bf16_f32 v113, v114, v115\n" \
"v_mfma_f32_16x16x32_bf16 v[24:27], a[224:227], a[184:187], v[24:27]\n" \
"v_exp_f32 v117, v117\n" \
"v_mfma_f32_16x16x32_bf16 v[24:27], a[228:231], a[188:191], v[24:27]\n" \
"ds_read_b128 a[224:227], v247 offset:20480\n" \
"global_load_lds_dwordx4 v250, s[38:39] offset:3072 sc1\n" \
"v_add_f32 v242, v242, v117\n" \
"v_mfma_f32_16x16x32_bf16 v[4:7], a[232:235], a[136:139], v[4:7]\n" \
"s_add_u32 s38, s38, 17408\n" \
"v_exp_f32 v118, v118\n" \
"v_mfma_f32_16x16x32_bf16 v[4:7], a[236:239], a[140:143], v[4:7]\n" \
"v_add_f32 v242, v242, v118\n" \
"v_cvt_pk_bf16_f32 v114, v116, v117\n" \
"v_mfma_f32_16x16x32_bf16 v[12:15], a[232:235], a[152:155], v[12:15]\n" \
"v_exp_f32 v119, v119\n" \
"v_mfma_f32_16x16x32_bf16 v[12:15], a[236:239], a[156:159], v[12:15]\n" \
"ds_read_b128 a[228:231], v247 offset:28672\n" \
"v_add_f32 v242, v242, v119\n" \
"v_mfma_f32_16x16x32_bf16 v[20:23], a[232:235], a[168:171], v[20:23]\n" \
"global_load_dword v253, v252, s[40:41]\n" \
"v_mfma_f32_16x16x32_bf16 v[20:23], a[236:239], a[172:175], v[20:23]\n" \
"s_sub_u32 s80, s80, 1\n" \
"s_cmp_gt_i32 s80, 0\n" \
"s_cselect_b32 s81, 0x80000, 0\n" \
"v_mfma_f32_16x16x32_bf16 v[28:31], a[232:235], a[184:187], v[28:31]\n" \
"s_add_u32 s40, s40, s81\n" \
"s_addc_u32 s41, s41, 0\n" \
"s_load_dwordx8 s[44:51], s[56:57], s79 glc\n" \
"v_mfma_f32_16x16x32_bf16 v[28:31], a[236:239], a[188:191], v[28:31]\n" \
"s_xor_b32 s74, s74, 0x8000\n" \
"ds_read_b128 a[232:235], v247 offset:21504\n" \
"s_sub_u32 s75, s75, 1\n" \
"v_mfma_f32_16x16x32_bf16 v[32:35], a[240:243], a[136:139], v[32:35]\n" \
"s_cmp_lg_u32 s75, 0\n" \
"v_exp_f32 v120, v120\n" \
"v_mfma_f32_16x16x32_bf16 v[32:35], a[244:247], a[140:143], v[32:35]\n" \
"v_add_f32 v243, v243, v120\n" \
"v_cvt_pk_bf16_f32 v115, v118, v119\n" \
"v_mfma_f32_16x16x32_bf16 v[40:43], a[240:243], a[152:155], v[40:43]\n" \
"v_exp_f32 v121, v121\n" \
"v_mfma_f32_16x16x32_bf16 v[40:43], a[244:247], a[156:159], v[40:43]\n" \
"ds_read_b128 a[236:239], v247 offset:29696\n" \
"v_add_f32 v243, v243, v121\n" \
"v_mfma_f32_16x16x32_bf16 v[48:51], a[240:243], a[168:171], v[48:51]\n" \
"v_exp_f32 v122, v122\n" \
"v_mfma_f32_16x16x32_bf16 v[48:51], a[244:247], a[172:175], v[48:51]\n" \
"v_add_f32 v243, v243, v122\n" \
"v_cvt_pk_bf16_f32 v120, v120, v121\n" \
"v_mfma_f32_16x16x32_bf16 v[56:59], a[240:243], a[184:187], v[56:59]\n" \
"v_exp_f32 v123, v123\n" \
"v_mfma_f32_16x16x32_bf16 v[56:59], a[244:247], a[188:191], v[56:59]\n" \
"ds_read_b128 a[240:243], v247 offset:22528\n" \
"v_add_f32 v243, v243, v123\n" \
"v_mfma_f32_16x16x32_bf16 v[36:39], a[248:251], a[136:139], v[36:39]\n" \
"v_exp_f32 v124, v124\n" \
"v_mfma_f32_16x16x32_bf16 v[36:39], a[252:255], a[140:143], v[36:39]\n" \
"v_add_f32 v243, v243, v124\n" \
"v_cvt_pk_bf16_f32 v121, v122, v123\n" \
"v_mfma_f32_16x16x32_bf16 v[44:47], a[248:251], a[152:155], v[44:47]\n" \
"v_exp_f32 v125, v125\n" \
"v_mfma_f32_16x16x32_bf16 v[44:47], a[252:255], a[156:159], v[44:47]\n" \
"ds_read_b128 a[244:247], v247 offset:30720\n" \
"v_add_f32 v243, v243, v125\n" \
"v_mfma_f32_16x16x32_bf16 v[52:55], a[248:251], a[168:171], v[52:55]\n" \
"v_exp_f32 v126, v126\n" \
"v_mfma_f32_16x16x32_bf16 v[52:55], a[252:255], a[172:175], v[52:55]\n" \
"v_add_f32 v243, v243, v126\n" \
"v_cvt_pk_bf16_f32 v122, v124, v125\n" \
"v_mfma_f32_16x16x32_bf16 v[60:63], a[248:251], a[184:187], v[60:63]\n" \
"s_waitcnt vmcnt(10) lgkmcnt(6)\n" \
"v_exp_f32 v127, v127\n" \
"v_mfma_f32_16x16x32_bf16 v[60:63], a[252:255], a[188:191], v[60:63]\n" \
"s_barrier\n" \
"ds_read_b128 a[248:251], v247 offset:23552\n" \
"v_add_f32 v243, v243, v127\n" \
"s_cbranch_scc1 Lloop_%=\n" \
"v_mfma_f32_16x16x32_bf16 a[0:3], a[192:195], v[64:67], a[0:3]\n" \
"v_exp_f32 v8, v8\n" \
"v_mfma_f32_16x16x32_bf16 a[0:3], a[196:199], v[96:99], a[0:3]\n" \
"v_cvt_pk_bf16_f32 v123, v126, v127\n" \
"v_add_f32 v241, v241, v8\n" \
"v_exp_f32 v9, v9\n" \
"v_mfma_f32_16x16x32_bf16 a[32:35], a[192:195], v[72:75], a[32:35]\n" \
"s_add_u32 m0, s73, 0x4000\n" \
"v_add_f32 v241, v241, v9\n" \
"v_exp_f32 v10, v10\n" \
"v_mfma_f32_16x16x32_bf16 a[32:35], a[196:199], v[104:107], a[32:35]\n" \
"ds_read_b128 a[252:255], v247 offset:31744\n" \
"global_load_lds_dwordx4 v250, s[36:37] offset:0 sc1\n" \
"v_add_f32 v241, v241, v10\n" \
"v_cvt_pk_bf16_f32 v8, v8, v9\n" \
"v_exp_f32 v11, v11\n" \
"v_mfma_f32_16x16x32_bf16 v[128:131], a[192:195], v[80:83], v[128:131]\n" \
"v_xor_b32 v247, 0x8000, v247\n" \
"v_add_f32 v241, v241, v11\n" \
"v_exp_f32 v12, v12\n" \
"v_mfma_f32_16x16x32_bf16 v[128:131], a[196:199], v[112:115], v[128:131]\n" \
"v_add_f32 v241, v241, v12\n" \
"v_cvt_pk_bf16_f32 v9, v10, v11\n" \
"v_exp_f32 v13, v13\n" \
"v_mfma_f32_16x16x32_bf16 v[160:163], a[192:195], v[88:91], v[160:163]\n" \
"v_add_f32 v241, v241, v13\n" \
"v_exp_f32 v14, v14\n" \
"v_mfma_f32_16x16x32_bf16 v[160:163], a[196:199], v[120:123], v[160:163]\n" \
"ds_read_b128 a[192:195], v246 offset:16384\n" \
"v_add_f32 v241, v241, v14\n" \
"v_cvt_pk_bf16_f32 v10, v12, v13\n" \
"v_exp_f32 v15, v15\n" \
"v_mfma_f32_16x16x32_bf16 a[4:7], a[200:203], v[64:67], a[4:7]\n" \
"v_add_f32 v241, v241, v15\n" \
"v_exp_f32 v16, v16\n" \
"v_mfma_f32_16x16x32_bf16 a[4:7], a[204:207], v[96:99], a[4:7]\n" \
"v_add_f32 v242, v242, v16\n" \
"v_cvt_pk_bf16_f32 v11, v14, v15\n" \
"v_exp_f32 v17, v17\n" \
"v_mfma_f32_16x16x32_bf16 a[36:39], a[200:203], v[72:75], a[36:39]\n" \
"v_add_f32 v242, v242, v17\n" \
"v_exp_f32 v18, v18\n" \
"v_mfma_f32_16x16x32_bf16 a[36:39], a[204:207], v[104:107], a[36:39]\n" \
"ds_read_b128 a[196:199], v246 offset:18432\n" \
"v_add_f32 v242, v242, v18\n" \
"v_cvt_pk_bf16_f32 v16, v16, v17\n" \
"v_exp_f32 v19, v19\n" \
"v_mfma_f32_16x16x32_bf16 v[132:135], a[200:203], v[80:83], v[132:135]\n" \
"v_add_f32 v242, v242, v19\n" \
"v_exp_f32 v20, v20\n" \
"v_mfma_f32_16x16x32_bf16 v[132:135], a[204:207], v[112:115], v[132:135]\n" \
"v_add_f32 v242, v242, v20\n" \
"v_cvt_pk_bf16_f32 v17, v18, v19\n" \
"v_exp_f32 v21, v21\n" \
"v_mfma_f32_16x16x32_bf16 v[164:167], a[200:203], v[88:91], v[164:167]\n" \
"v_add_f32 v242, v242, v21\n" \
"v_exp_f32 v22, v22\n" \
"v_mfma_f32_16x16x32_bf16 v[164:167], a[204:207], v[120:123], v[164:167]\n" \
"ds_read_b128 a[200:203], v246 offset:16640\n" \
"global_load_lds_dwordx4 v250, s[36:37] offset:1024 sc1\n" \
"v_add_f32 v242, v242, v22\n" \
"v_cvt_pk_bf16_f32 v18, v20, v21\n" \
"v_exp_f32 v23, v23\n" \
"v_mfma_f32_16x16x32_bf16 a[8:11], a[208:211], v[64:67], a[8:11]\n" \
"v_add_f32 v242, v242, v23\n" \
"v_cvt_pk_bf16_f32 v19, v22, v23\n" \
"v_cmp_gt_i32 vcc, 0, v251\n" \
"v_cmp_gt_i32 s[64:65], 1, v251\n" \
"v_cmp_gt_i32 s[66:67], 2, v251\n" \
"v_cndmask_b32 v0, v0, v254, vcc\n" \
"v_mfma_f32_16x16x32_bf16 a[8:11], a[212:215], v[96:99], a[8:11]\n" \
"v_cmp_gt_i32 vcc, 3, v251\n" \
"v_cndmask_b32 v1, v1, v254, s[64:65]\n" \
"v_cmp_gt_i32 s[64:65], 16, v251\n" \
"v_cndmask_b32 v2, v2, v254, s[66:67]\n" \
"v_cmp_gt_i32 s[66:67], 17, v251\n" \
"v_cndmask_b32 v3, v3, v254, vcc\n" \
"v_mfma_f32_16x16x32_bf16 a[40:43], a[208:211], v[72:75], a[40:43]\n" \
"v_cmp_gt_i32 vcc, 18, v251\n" \
"v_cndmask_b32 v4, v4, v254, s[64:65]\n" \
"v_cmp_gt_i32 s[64:65], 19, v251\n" \
"v_cndmask_b32 v5, v5, v254, s[66:67]\n" \
"v_cmp_gt_i32 s[66:67], 32, v251\n" \
"v_cndmask_b32 v6, v6, v254, vcc\n" \
"v_mfma_f32_16x16x32_bf16 a[40:43], a[212:215], v[104:107], a[40:43]\n" \
"ds_read_b128 a[204:207], v246 offset:18688\n" \
"v_cmp_gt_i32 vcc, 33, v251\n" \
"v_mfma_f32_16x16x32_bf16 v[136:139], a[208:211], v[80:83], v[136:139]\n" \
"v_cndmask_b32 v7, v7, v254, s[64:65]\n" \
"v_cmp_gt_i32 s[64:65], 34, v251\n" \
"v_mfma_f32_16x16x32_bf16 v[136:139], a[212:215], v[112:115], v[136:139]\n" \
"v_cndmask_b32 v32, v32, v254, s[66:67]\n" \
"v_cmp_gt_i32 s[66:67], 35, v251\n" \
"v_mfma_f32_16x16x32_bf16 v[168:171], a[208:211], v[88:91], v[168:171]\n" \
"v_cndmask_b32 v33, v33, v254, vcc\n" \
"v_cmp_gt_i32 vcc, 48, v251\n" \
"v_mfma_f32_16x16x32_bf16 v[168:171], a[212:215], v[120:123], v[168:171]\n" \
"ds_read_b128 a[208:211], v246 offset:24576\n" \
"v_cndmask_b32 v34, v34, v254, s[64:65]\n" \
"v_mfma_f32_16x16x32_bf16 a[12:15], a[216:219], v[64:67], a[12:15]\n" \
"Lpg6169_%=:\n" \
"s_cbranch_execz Lpg7641_%=\n" \
"v_cmp_gt_i32 s[64:65], 49, v251\n" \
"v_cndmask_b32 v35, v35, v254, s[66:67]\n" \
"v_mfma_f32_16x16x32_bf16 a[12:15], a[220:223], v[96:99], a[12:15]\n" \
"v_cmp_gt_i32 s[66:67], 50, v251\n" \
"v_cndmask_b32 v36, v36, v254, vcc\n" \
"v_mfma_f32_16x16x32_bf16 a[44:47], a[216:219], v[72:75], a[44:47]\n" \
"v_cmp_gt_i32 vcc, 51, v251\n" \
"v_cndmask_b32 v37, v37, v254, s[64:65]\n" \
"v_mfma_f32_16x16x32_bf16 a[44:47], a[220:223], v[104:107], a[44:47]\n" \
"ds_read_b128 a[212:215], v246 offset:26624\n" \
"global_load_lds_dwordx4 v250, s[36:37] offset:2048 sc1\n" \
"v_mfma_f32_16x16x32_bf16 v[140:143], a[216:219], v[80:83], v[140:143]\n" \
"v_cndmask_b32 v38, v38, v254, s[66:67]\n" \
"v_cndmask_b32 v39, v39, v254, vcc\n" \
"v_mfma_f32_16x16x32_bf16 v[140:143], a[220:223], v[112:115], v[140:143]\n" \
"v_exp_f32 v0, v0\n" \
"v_mfma_f32_16x16x32_bf16 v[172:175], a[216:219], v[88:91], v[172:175]\n" \
"s_waitcnt lgkmcnt(6)\n" \
"v_add_f32 v240, v240, v0\n" \
"v_exp_f32 v1, v1\n" \
"v_mfma_f32_16x16x32_bf16 v[172:175], a[220:223], v[120:123], v[172:175]\n" \
"ds_read_b128 a[216:219], v246 offset:24832\n" \
"v_add_f32 v240, v240, v1\n" \
"v_mfma_f32_16x16x32_bf16 a[16:19], a[224:227], v[64:67], a[16:19]\n" \
"v_exp_f32 v2, v2\n" \
"v_mfma_f32_16x16x32_bf16 a[16:19], a[228:231], v[96:99], a[16:19]\n" \
"v_add_f32 v240, v240, v2\n" \
"v_cvt_pk_bf16_f32 v0, v0, v1\n" \
"v_mfma_f32_16x16x32_bf16 a[48:51], a[224:227], v[72:75], a[48:51]\n" \
"v_exp_f32 v3, v3\n" \
"v_mfma_f32_16x16x32_bf16 a[48:51], a[228:231], v[104:107], a[48:51]\n" \
"ds_read_b128 a[220:223], v246 offset:26880\n" \
"v_add_f32 v240, v240, v3\n" \
"v_exp_f32 v4, v4\n" \
"v_mfma_f32_16x16x32_bf16 v[144:147], a[224:227], v[80:83], v[144:147]\n" \
"v_add_f32 v240, v240, v4\n" \
"v_cvt_pk_bf16_f32 v1, v2, v3\n" \
"v_mfma_f32_16x16x32_bf16 v[144:147], a[228:231], v[112:115], v[144:147]\n" \
"v_exp_f32 v5, v5\n" \
"v_mfma_f32_16x16x32_bf16 v[176:179], a[224:227], v[88:91], v[176:179]\n" \
"global_load_lds_dwordx4 v250, s[36:37] offset:3072 sc1\n" \
"s_add_u32 s36, s36, 17408\n" \
"v_mfma_f32_16x16x32_bf16 v[176:179], a[228:231], v[120:123], v[176:179]\n" \
"s_xor_b32 s73, s73, 0x8000\n" \
"ds_read_b128 a[224:227], v246 offset:20480\n" \
"s_xor_b64 s[52:53], s[44:45], s[42:43]\n" \
"v_mfma_f32_16x16x32_bf16 a[20:23], a[232:235], v[64:67], a[20:23]\n" \
"s_xor_b64 s[54:55], s[46:47], s[42:43]\n" \
"s_or_b64 s[52:53], s[52:53], s[54:55]\n" \
"s_xor_b64 s[54:55], s[48:49], s[42:43]\n" \
"v_mfma_f32_16x16x32_bf16 a[20:23], a[236:239], v[96:99], a[20:23]\n" \
"s_or_b64 s[52:53], s[52:53], s[54:55]\n" \
"s_xor_b64 s[54:55], s[50:51], s[42:43]\n" \
"s_or_b64 s[52:53], s[52:53], s[54:55]\n" \
"v_mfma_f32_16x16x32_bf16 a[52:55], a[232:235], v[72:75], a[52:55]\n" \
"s_and_b64 s[52:53], s[52:53], s[8:9]\n" \
"s_cbranch_scc1 Lrs1_%=\n" \
"Lrr1_%=:\n" \
"v_mfma_f32_16x16x32_bf16 a[52:55], a[236:239], v[104:107], a[52:55]\n" \
"s_mov_b64 s[8:9], s[10:11]\n" \
"ds_read_b128 a[228:231], v246 offset:22528\n" \
"v_add_f32 v240, v240, v5\n" \
"v_mfma_f32_16x16x32_bf16 v[148:151], a[232:235], v[80:83], v[148:151]\n" \
"v_exp_f32 v6, v6\n" \
"v_mfma_f32_16x16x32_bf16 v[148:151], a[236:239], v[112:115], v[148:151]\n" \
"v_add_f32 v240, v240, v6\n" \
"v_cvt_pk_bf16_f32 v2, v4, v5\n" \
"v_mfma_f32_16x16x32_bf16 v[180:183], a[232:235], v[88:91], v[180:183]\n" \
"v_exp_f32 v7, v7\n" \
"v_mfma_f32_16x16x32_bf16 v[180:183], a[236:239], v[120:123], v[180:183]\n" \
"ds_read_b128 a[232:235], v246 offset:20736\n" \
"v_add_f32 v240, v240, v7\n" \
"v_mfma_f32_16x16x32_bf16 a[24:27], a[240:243], v[64:67], a[24:27]\n" \
"v_exp_f32 v24, v24\n" \
"v_mfma_f32_16x16x32_bf16 a[24:27], a[244:247], v[96:99], a[24:27]\n" \
"v_add_f32 v243, v243, v24\n" \
"v_cvt_pk_bf16_f32 v3, v6, v7\n" \
"v_mfma_f32_16x16x32_bf16 a[56:59], a[240:243], v[72:75], a[56:59]\n" \
"v_exp_f32 v25, v25\n" \
"v_mfma_f32_16x16x32_bf16 a[56:59], a[244:247], v[104:107], a[56:59]\n" \
"ds_read_b128 a[236:239], v246 offset:22784\n" \
"v_add_f32 v243, v243, v25\n" \
"v_mfma_f32_16x16x32_bf16 v[152:155], a[240:243], v[80:83], v[152:155]\n" \
"v_exp_f32 v26, v26\n" \
"v_mfma_f32_16x16x32_bf16 v[152:155], a[244:247], v[112:115], v[152:155]\n" \
"v_add_f32 v243, v243, v26\n" \
"v_cvt_pk_bf16_f32 v24, v24, v25\n" \
"v_mfma_f32_16x16x32_bf16 v[184:187], a[240:243], v[88:91], v[184:187]\n" \
"v_exp_f32 v27, v27\n" \
"v_mfma_f32_16x16x32_bf16 v[184:187], a[244:247], v[120:123], v[184:187]\n" \
"ds_read_b128 a[240:243], v246 offset:28672\n" \
"v_add_f32 v243, v243, v27\n" \
"v_mfma_f32_16x16x32_bf16 a[28:31], a[248:251], v[64:67], a[28:31]\n" \
"v_exp_f32 v28, v28\n" \
"v_mfma_f32_16x16x32_bf16 a[28:31], a[252:255], v[96:99], a[28:31]\n" \
"v_add_f32 v243, v243, v28\n" \
"v_cvt_pk_bf16_f32 v25, v26, v27\n" \
"v_mfma_f32_16x16x32_bf16 a[60:63], a[248:251], v[72:75], a[60:63]\n" \
"v_exp_f32 v29, v29\n" \
"v_mfma_f32_16x16x32_bf16 a[60:63], a[252:255], v[104:107], a[60:63]\n" \
"ds_read_b128 a[244:247], v246 offset:30720\n" \
"v_add_f32 v243, v243, v29\n" \
"v_mfma_f32_16x16x32_bf16 v[156:159], a[248:251], v[80:83], v[156:159]\n" \
"v_exp_f32 v30, v30\n" \
"v_mfma_f32_16x16x32_bf16 v[156:159], a[252:255], v[112:115], v[156:159]\n" \
"v_add_f32 v243, v243, v30\n" \
"v_cvt_pk_bf16_f32 v26, v28, v29\n" \
"v_mfma_f32_16x16x32_bf16 v[188:191], a[248:251], v[88:91], v[188:191]\n" \
"s_waitcnt lgkmcnt(6)\n" \
"v_exp_f32 v31, v31\n" \
"v_mfma_f32_16x16x32_bf16 v[188:191], a[252:255], v[120:123], v[188:191]\n" \
"ds_read_b128 a[248:251], v246 offset:28928\n" \
"v_add_f32 v243, v243, v31\n" \
"LA1l_%=:\n" \
"Lpg6284_%=:\n" \
"s_cbranch_execz Lpg13705_%=\n" \
"s_getpc_b64 s[40:41]\n" \
"Lpd_%=:\n" \
"s_lshl_b32 s80, s62, 2\n" \
"s_add_u32 s80, s80, s61\n" \
"s_and_b32 s80, s80, 15\n" \
"s_lshl_b32 s80, s80, 12\n" \
"s_mov_b32 s81, Lcode_end_%=-Lpd_%=-4\n" \
"v_lshl_add_u32 v252, v248, 2, s80\n" \
"v_min_u32 v252, s81, v252\n" \
"s_waitcnt lgkmcnt(0)\n" \
"v_mfma_f32_16x16x32_bf16 v[72:75], a[192:195], a[144:147], v[228:231]\n" \
"v_exp_f32 v32, v32\n" \
"v_mfma_f32_16x16x32_bf16 v[72:75], a[196:199], a[148:151], v[72:75]\n" \
"v_cvt_pk_bf16_f32 v27, v30, v31\n" \
"v_add_f32 v240, v240, v32\n" \
"v_exp_f32 v33, v33\n" \
"v_mfma_f32_16x16x32_bf16 v[80:83], a[192:195], a[160:163], v[232:235]\n" \
"s_mov_b32 m0, s74\n" \
"v_add_f32 v240, v240, v33\n" \
"v_exp_f32 v34, v34\n" \
"v_mfma_f32_16x16x32_bf16 v[80:83], a[196:199], a[164:167], v[80:83]\n" \
"ds_read_b128 a[252:255], v246 offset:30976\n" \
"global_load_lds_dwordx4 v250, s[38:39] offset:0 sc1\n" \
"v_add_f32 v240, v240, v34\n" \
"v_cvt_pk_bf16_f32 v32, v32, v33\n" \
"v_exp_f32 v35, v35\n" \
"v_mfma_f32_16x16x32_bf16 v[88:91], a[192:195], a[176:179], v[236:239]\n" \
"s_lshl_b32 s70, s95, 1\n" \
"v_add_f32 v240, v240, v35\n" \
"v_exp_f32 v36, v36\n" \
"s_xor_b32 s70, s70, 139264\n" \
"v_add_u32 v244, s70, v248\n" \
"v_mfma_f32_16x16x32_bf16 v[88:91], a[196:199], a[180:183], v[88:91]\n" \
"ds_read_b128 a[192:195], v247 offset:0\n" \
"v_add_f32 v240, v240, v36\n" \
"v_cvt_pk_bf16_f32 v33, v34, v35\n" \
"v_exp_f32 v37, v37\n" \
"v_mfma_f32_16x16x32_bf16 v[76:79], a[200:203], a[144:147], v[228:231]\n" \
"ds_read_b128 v[192:195], v244 offset:0\n" \
"v_add_f32 v240, v240, v37\n" \
"v_exp_f32 v38, v38\n" \
"v_mfma_f32_16x16x32_bf16 v[76:79], a[204:207], a[148:151], v[76:79]\n" \
"ds_read_b128 v[196:199], v244 offset:1024\n" \
"v_add_f32 v240, v240, v38\n" \
"v_cvt_pk_bf16_f32 v34, v36, v37\n" \
"v_exp_f32 v39, v39\n" \
"v_mfma_f32_16x16x32_bf16 v[84:87], a[200:203], a[160:163], v[232:235]\n" \
"ds_read_b128 a[196:199], v247 offset:8192\n" \
"v_add_f32 v240, v240, v39\n" \
"v_exp_f32 v40, v40\n" \
"v_mfma_f32_16x16x32_bf16 v[84:87], a[204:207], a[164:167], v[84:87]\n" \
"ds_read_b128 v[200:203], v244 offset:2048\n" \
"v_add_f32 v241, v241, v40\n" \
"v_cvt_pk_bf16_f32 v35, v38, v39\n" \
"v_exp_f32 v41, v41\n" \
"v_mfma_f32_16x16x32_bf16 v[92:95], a[200:203], a[176:179], v[236:239]\n" \
"ds_read_b128 v[204:207], v244 offset:3072\n" \
"v_add_f32 v241, v241, v41\n" \
"v_exp_f32 v42, v42\n" \
"v_mfma_f32_16x16x32_bf16 v[92:95], a[204:207], a[180:183], v[92:95]\n" \
"ds_read_b128 a[200:203], v247 offset:1024\n" \
"v_add_f32 v241, v241, v42\n" \
"v_cvt_pk_bf16_f32 v40, v40, v41\n" \
"v_exp_f32 v43, v43\n" \
"v_mfma_f32_16x16x32_bf16 v[104:107], a[208:211], a[144:147], v[228:231]\n" \
"ds_read_b128 v[208:211], v244 offset:4096\n" \
"global_load_lds_dwordx4 v250, s[38:39] offset:1024 sc1\n" \
"v_add_f32 v241, v241, v43\n" \
"v_exp_f32 v44, v44\n" \
"v_mfma_f32_16x16x32_bf16 v[104:107], a[212:215], a[148:151], v[104:107]\n" \
"ds_read_b128 v[212:215], v244 offset:5120\n" \
"v_add_f32 v241, v241, v44\n" \
"v_cvt_pk_bf16_f32 v41, v42, v43\n" \
"v_exp_f32 v45, v45\n" \
"v_mfma_f32_16x16x32_bf16 v[112:115], a[208:211], a[160:163], v[232:235]\n" \
"ds_read_b128 a[204:207], v247 offset:9216\n" \
"v_add_f32 v241, v241, v45\n" \
"v_exp_f32 v46, v46\n" \
"v_mfma_f32_16x16x32_bf16 v[112:115], a[212:215], a[164:167], v[112:115]\n" \
"ds_read_b128 v[216:219], v244 offset:6144\n" \
"v_add_f32 v241, v241, v46\n" \
"v_cvt_pk_bf16_f32 v42, v44, v45\n" \
"v_exp_f32 v47, v47\n" \
"v_mfma_f32_16x16x32_bf16 v[120:123], a[208:211], a[176:179], v[236:239]\n" \
"ds_read_b128 v[220:223], v244 offset:7168\n" \
"v_add_f32 v241, v241, v47\n" \
"v_exp_f32 v48, v48\n" \
"v_mfma_f32_16x16x32_bf16 v[120:123], a[212:215], a[180:183], v[120:123]\n" \
"ds_read_b128 a[208:211], v247 offset:2048\n" \
"v_add_f32 v242, v242, v48\n" \
"v_cvt_pk_bf16_f32 v43, v46, v47\n" \
"v_exp_f32 v49, v49\n" \
"v_mfma_f32_16x16x32_bf16 v[108:111], a[216:219], a[144:147], v[228:231]\n" \
"ds_write_b128 v244, v[232:235]\n" \
"v_mfma_f32_16x16x32_bf16 v[108:111], a[220:223], a[148:151], v[108:111]\n" \
"ds_write_b128 v244, v[236:239] offset:1024\n" \
"v_mfma_f32_16x16x32_bf16 v[116:119], a[216:219], a[160:163], v[232:235]\n" \
"ds_read_b128 a[212:215], v247 offset:10240\n" \
"v_xor_b32 v245, 0x2000, v244\n" \
"v_mfma_f32_16x16x32_bf16 v[116:119], a[220:223], a[164:167], v[116:119]\n" \
"v_xor_b32 v246, 0x8000, v246\n" \
"v_add_u32 v246, s82, v246\n" \
"v_mfma_f32_16x16x32_bf16 v[124:127], a[216:219], a[176:179], v[236:239]\n" \
"s_waitcnt lgkmcnt(6)\n" \
"global_load_lds_dwordx4 v250, s[38:39] offset:2048 sc1\n" \
"v_add_f32 v242, v242, v49\n" \
"v_mfma_f32_16x16x32_bf16 v[124:127], a[220:223], a[180:183], v[124:127]\n" \
"ds_read_b128 a[216:219], v247 offset:3072\n" \
"v_exp_f32 v50, v50\n" \
"v_mfma_f32_16x16x32_bf16 v[72:75], a[224:227], a[152:155], v[72:75]\n" \
"v_add_f32 v242, v242, v50\n" \
"v_cvt_pk_bf16_f32 v48, v48, v49\n" \
"v_mfma_f32_16x16x32_bf16 v[72:75], a[228:231], a[156:159], v[72:75]\n" \
"v_exp_f32 v51, v51\n" \
"v_mfma_f32_16x16x32_bf16 v[80:83], a[224:227], a[168:171], v[80:83]\n" \
"ds_read_b128 a[220:223], v247 offset:11264\n" \
"v_add_f32 v242, v242, v51\n" \
"v_mfma_f32_16x16x32_bf16 v[80:83], a[228:231], a[172:175], v[80:83]\n" \
"v_exp_f32 v52, v52\n" \
"v_mfma_f32_16x16x32_bf16 v[88:91], a[224:227], a[184:187], v[88:91]\n" \
"v_add_f32 v242, v242, v52\n" \
"v_cvt_pk_bf16_f32 v49, v50, v51\n" \
"v_mfma_f32_16x16x32_bf16 v[88:91], a[228:231], a[188:191], v[88:91]\n" \
"ds_read_b128 a[224:227], v247 offset:4096\n" \
"v_exp_f32 v53, v53\n" \
"v_mfma_f32_16x16x32_bf16 v[76:79], a[232:235], a[152:155], v[76:79]\n" \
"global_load_lds_dwordx4 v250, s[38:39] offset:3072 sc1\n" \
"v_add_f32 v242, v242, v53\n" \
"v_mfma_f32_16x16x32_bf16 v[76:79], a[236:239], a[156:159], v[76:79]\n" \
"s_add_u32 s38, s38, 17408\n" \
"v_exp_f32 v54, v54\n" \
"v_mfma_f32_16x16x32_bf16 v[84:87], a[232:235], a[168:171], v[84:87]\n" \
"ds_read_b128 a[228:231], v247 offset:12288\n" \
"v_add_f32 v242, v242, v54\n" \
"v_cvt_pk_bf16_f32 v50, v52, v53\n" \
"v_mfma_f32_16x16x32_bf16 v[84:87], a[236:239], a[172:175], v[84:87]\n" \
"v_exp_f32 v55, v55\n" \
"v_mfma_f32_16x16x32_bf16 v[92:95], a[232:235], a[184:187], v[92:95]\n" \
"v_add_f32 v242, v242, v55\n" \
"v_exp_f32 v56, v56\n" \
"v_mfma_f32_16x16x32_bf16 v[92:95], a[236:239], a[188:191], v[92:95]\n" \
"ds_read_b128 a[232:235], v247 offset:5120\n" \
"v_add_f32 v243, v243, v56\n" \
"v_cvt_pk_bf16_f32 v51, v54, v55\n" \
"v_mfma_f32_16x16x32_bf16 v[104:107], a[240:243], a[152:155], v[104:107]\n" \
"global_load_dword v253, v252, s[40:41]\n" \
"v_mfma_f32_16x16x32_bf16 v[104:107], a[244:247], a[156:159], v[104:107]\n" \
"v_exp_f32 v57, v57\n" \
"v_mfma_f32_16x16x32_bf16 v[112:115], a[240:243], a[168:171], v[112:115]\n" \
"ds_read_b128 a[236:239], v247 offset:13312\n" \
"v_add_f32 v243, v243, v57\n" \
"v_exp_f32 v58, v58\n" \
"v_mfma_f32_16x16x32_bf16 v[112:115], a[244:247], a[172:175], v[112:115]\n" \
"v_add_f32 v243, v243, v58\n" \
"v_cvt_pk_bf16_f32 v56, v56, v57\n" \
"v_mfma_f32_16x16x32_bf16 v[120:123], a[240:243], a[184:187], v[120:123]\n" \
"v_exp_f32 v59, v59\n" \
"v_mfma_f32_16x16x32_bf16 v[120:123], a[244:247], a[188:191], v[120:123]\n" \
"ds_read_b128 a[240:243], v247 offset:6144\n" \
"v_add_f32 v243, v243, v59\n" \
"v_exp_f32 v60, v60\n" \
"v_mfma_f32_16x16x32_bf16 v[108:111], a[248:251], a[152:155], v[108:111]\n" \
"v_add_f32 v243, v243, v60\n" \
"v_cvt_pk_bf16_f32 v57, v58, v59\n" \
"v_mfma_f32_16x16x32_bf16 v[108:111], a[252:255], a[156:159], v[108:111]\n" \
"v_exp_f32 v61, v61\n" \
"v_mfma_f32_16x16x32_bf16 v[116:119], a[248:251], a[168:171], v[116:119]\n" \
"ds_read_b128 a[244:247], v247 offset:14336\n" \
"v_add_f32 v243, v243, v61\n" \
"v_exp_f32 v62, v62\n" \
"v_mfma_f32_16x16x32_bf16 v[116:119], a[252:255], a[172:175], v[116:119]\n" \
"v_add_f32 v243, v243, v62\n" \
"v_cvt_pk_bf16_f32 v58, v60, v61\n" \
"v_mfma_f32_16x16x32_bf16 v[124:127], a[248:251], a[184:187], v[124:127]\n" \
"s_waitcnt vmcnt(5) lgkmcnt(6)\n" \
"v_exp_f32 v63, v63\n" \
"v_mfma_f32_16x16x32_bf16 v[124:127], a[252:255], a[188:191], v[124:127]\n" \
"s_barrier\n" \
"ds_read_b128 a[248:251], v247 offset:7168\n" \
"v_add_f32 v243, v243, v63\n" \
"v_mfma_f32_16x16x32_bf16 a[0:3], a[192:195], v[0:3], a[0:3]\n" \
"v_exp_f32 v80, v80\n" \
"v_mfma_f32_16x16x32_bf16 a[0:3], a[196:199], v[32:35], a[0:3]\n" \
"v_cvt_pk_bf16_f32 v59, v62, v63\n" \
"v_add_f32 v242, v242, v80\n" \
"v_exp_f32 v81, v81\n" \
"v_mfma_f32_16x16x32_bf16 a[32:35], a[192:195], v[8:11], a[32:35]\n" \
"v_add_f32 v242, v242, v81\n" \
"v_exp_f32 v82, v82\n" \
"v_mfma_f32_16x16x32_bf16 a[32:35], a[196:199], v[40:43], a[32:35]\n" \
"ds_read_b128 a[252:255], v247 offset:15360\n" \
"v_add_f32 v242, v242, v82\n" \
"v_cvt_pk_bf16_f32 v80, v80, v81\n" \
"v_exp_f32 v83, v83\n" \
"v_mfma_f32_16x16x32_bf16 v[128:131], a[192:195], v[16:19], v[128:131]\n" \
"ds_read_b128 v[224:227], v245\n" \
"v_add_f32 v242, v242, v83\n" \
"v_exp_f32 v84, v84\n" \
"v_mfma_f32_16x16x32_bf16 v[128:131], a[196:199], v[48:51], v[128:131]\n" \
"ds_read_b128 v[228:231], v245 offset:1024\n" \
"v_add_f32 v242, v242, v84\n" \
"v_cvt_pk_bf16_f32 v81, v82, v83\n" \
"v_exp_f32 v85, v85\n" \
"v_mov_b32 v244, 0\n" \
"v_mfma_f32_16x16x32_bf16 v[160:163], a[192:195], v[24:27], v[160:163]\n" \
"v_mov_b32 v245, 0\n" \
"v_xor_b32 v252, 0x8000, v246\n" \
"v_add_f32 v242, v242, v85\n" \
"v_exp_f32 v86, v86\n" \
"v_mfma_f32_16x16x32_bf16 v[160:163], a[196:199], v[56:59], v[160:163]\n" \
"s_add_u32 s73, s73, s95\n" \
"ds_read_b128 a[192:195], v246 offset:0\n" \
"v_add_f32 v242, v242, v86\n" \
"v_cvt_pk_bf16_f32 v82, v84, v85\n" \
"v_exp_f32 v87, v87\n" \
"v_mfma_f32_16x16x32_bf16 a[4:7], a[200:203], v[0:3], a[4:7]\n" \
"s_add_u32 s36, s36, s83\n" \
"v_add_f32 v242, v242, v87\n" \
"s_add_u32 s73, s73, 4096\n" \
"s_mov_b32 m0, s73\n" \
"v_mfma_f32_16x16x32_bf16 a[4:7], a[204:207], v[32:35], a[4:7]\n" \
"global_load_lds_dwordx4 v250, s[36:37] offset:-4096 sc1\n" \
"v_cvt_pk_bf16_f32 v83, v86, v87\n" \
"v_mfma_f32_16x16x32_bf16 a[36:39], a[200:203], v[8:11], a[36:39]\n" \
"v_cmp_gt_i32 vcc, 0, v251\n" \
"v_cmp_gt_i32 s[64:65], 1, v251\n" \
"v_mfma_f32_16x16x32_bf16 a[36:39], a[204:207], v[40:43], a[36:39]\n" \
"ds_read_b128 a[196:199], v246 offset:2048\n" \
"global_load_lds_dwordx4 v250, s[36:37] offset:-3072 sc1\n" \
"v_mfma_f32_16x16x32_bf16 v[132:135], a[200:203], v[16:19], v[132:135]\n" \
"v_cmp_gt_i32 s[66:67], 2, v251\n" \
"v_cndmask_b32 v72, v72, v254, vcc\n" \
"v_mfma_f32_16x16x32_bf16 v[132:135], a[204:207], v[48:51], v[132:135]\n" \
"v_cmp_gt_i32 vcc, 3, v251\n" \
"v_cndmask_b32 v73, v73, v254, s[64:65]\n" \
"v_mfma_f32_16x16x32_bf16 v[164:167], a[200:203], v[24:27], v[164:167]\n" \
"v_cmp_gt_i32 s[64:65], 16, v251\n" \
"v_cndmask_b32 v74, v74, v254, s[66:67]\n" \
"v_mfma_f32_16x16x32_bf16 v[164:167], a[204:207], v[56:59], v[164:167]\n" \
"ds_read_b128 a[200:203], v246 offset:256\n" \
"global_load_lds_dwordx4 v250, s[36:37] offset:-2048 sc1\n" \
"v_mfma_f32_16x16x32_bf16 a[8:11], a[208:211], v[0:3], a[8:11]\n" \
"v_cmp_gt_i32 s[66:67], 17, v251\n" \
"v_cndmask_b32 v75, v75, v254, vcc\n" \
"v_mfma_f32_16x16x32_bf16 a[8:11], a[212:215], v[32:35], a[8:11]\n" \
"v_cmp_gt_i32 vcc, 18, v251\n" \
"v_cndmask_b32 v76, v76, v254, s[64:65]\n" \
"v_mfma_f32_16x16x32_bf16 a[40:43], a[208:211], v[8:11], a[40:43]\n" \
"v_cmp_gt_i32 s[64:65], 19, v251\n" \
"v_cndmask_b32 v77, v77, v254, s[66:67]\n" \
"v_mfma_f32_16x16x32_bf16 a[40:43], a[212:215], v[40:43], a[40:43]\n" \
"ds_read_b128 a[204:207], v246 offset:2304\n" \
"global_load_lds_dwordx4 v250, s[36:37] offset:-1024 sc1\n" \
"v_mfma_f32_16x16x32_bf16 v[136:139], a[208:211], v[16:19], v[136:139]\n" \
"v_cmp_gt_i32 s[66:67], 32, v251\n" \
"v_cndmask_b32 v78, v78, v254, vcc\n" \
"v_mfma_f32_16x16x32_bf16 v[136:139], a[212:215], v[48:51], v[136:139]\n" \
"v_cmp_gt_i32 vcc, 33, v251\n" \
"v_cndmask_b32 v79, v79, v254, s[64:65]\n" \
"v_mfma_f32_16x16x32_bf16 v[168:171], a[208:211], v[24:27], v[168:171]\n" \
"v_cmp_gt_i32 s[64:65], 34, v251\n" \
"v_cndmask_b32 v104, v104, v254, s[66:67]\n" \
"v_mfma_f32_16x16x32_bf16 v[168:171], a[212:215], v[56:59], v[168:171]\n" \
"ds_read_b128 a[208:211], v246 offset:8192\n" \
"global_load_lds_dwordx4 v250, s[36:37] offset:0 sc1\n" \
"v_mfma_f32_16x16x32_bf16 a[12:15], a[216:219], v[0:3], a[12:15]\n" \
"v_cmp_gt_i32 s[66:67], 35, v251\n" \
"v_cndmask_b32 v105, v105, v254, vcc\n" \
"v_mfma_f32_16x16x32_bf16 a[12:15], a[220:223], v[32:35], a[12:15]\n" \
"v_cmp_gt_i32 vcc, 48, v251\n" \
"v_cndmask_b32 v106, v106, v254, s[64:65]\n" \
"v_mfma_f32_16x16x32_bf16 a[44:47], a[216:219], v[8:11], a[44:47]\n" \
"v_cmp_gt_i32 s[64:65], 49, v251\n" \
"v_cndmask_b32 v107, v107, v254, s[66:67]\n" \
"v_mfma_f32_16x16x32_bf16 a[44:47], a[220:223], v[40:43], a[44:47]\n" \
"ds_read_b128 a[212:215], v246 offset:10240\n" \
"global_load_lds_dwordx4 v250, s[36:37] offset:1024 sc1\n" \
"v_mfma_f32_16x16x32_bf16 v[140:143], a[216:219], v[16:19], v[140:143]\n" \
"v_cmp_gt_i32 s[66:67], 50, v251\n" \
"v_cndmask_b32 v108, v108, v254, vcc\n" \
"v_mfma_f32_16x16x32_bf16 v[140:143], a[220:223], v[48:51], v[140:143]\n" \
"v_cmp_gt_i32 vcc, 51, v251\n" \
"v_cndmask_b32 v109, v109, v254, s[64:65]\n" \
"v_mfma_f32_16x16x32_bf16 v[172:175], a[216:219], v[24:27], v[172:175]\n" \
"s_waitcnt lgkmcnt(6)\n" \
"v_cndmask_b32 v110, v110, v254, s[66:67]\n" \
"v_cndmask_b32 v111, v111, v254, vcc\n" \
"v_mfma_f32_16x16x32_bf16 v[172:175], a[220:223], v[56:59], v[172:175]\n" \
"ds_read_b128 a[216:219], v246 offset:8448\n" \
"global_load_lds_dwordx4 v250, s[36:37] offset:2048 sc1\n" \
"v_mfma_f32_16x16x32_bf16 a[16:19], a[224:227], v[0:3], a[16:19]\n" \
"v_exp_f32 v72, v72\n" \
"v_mfma_f32_16x16x32_bf16 a[16:19], a[228:231], v[32:35], a[16:19]\n" \
"v_add_f32 v241, v241, v72\n" \
"v_exp_f32 v73, v73\n" \
"v_mfma_f32_16x16x32_bf16 a[48:51], a[224:227], v[8:11], a[48:51]\n" \
"v_add_f32 v241, v241, v73\n" \
"v_exp_f32 v74, v74\n" \
"v_mfma_f32_16x16x32_bf16 a[48:51], a[228:231], v[40:43], a[48:51]\n" \
"ds_read_b128 a[220:223], v246 offset:10496\n" \
"global_load_lds_dwordx4 v250, s[36:37] offset:3072 sc1\n" \
"v_mfma_f32_16x16x32_bf16 v[144:147], a[224:227], v[16:19], v[144:147]\n" \
"s_add_u32 s36, s36, 34816\n" \
"v_add_f32 v241, v241, v74\n" \
"v_cvt_pk_bf16_f32 v72, v72, v73\n" \
"v_mfma_f32_16x16x32_bf16 v[144:147], a[228:231], v[48:51], v[144:147]\n" \
"v_exp_f32 v75, v75\n" \
"v_mfma_f32_16x16x32_bf16 v[176:179], a[224:227], v[24:27], v[176:179]\n" \
"s_add_u32 m0, s74, 0x4000\n" \
"v_add_f32 v241, v241, v75\n" \
"v_exp_f32 v76, v76\n" \
"v_mfma_f32_16x16x32_bf16 v[176:179], a[228:231], v[56:59], v[176:179]\n" \
"ds_read_b128 a[224:227], v246 offset:4096\n" \
"global_load_lds_dwordx4 v250, s[38:39] offset:0 sc1\n" \
"v_mfma_f32_16x16x32_bf16 a[20:23], a[232:235], v[0:3], a[20:23]\n" \
"v_add_f32 v241, v241, v76\n" \
"v_cvt_pk_bf16_f32 v73, v74, v75\n" \
"v_mfma_f32_16x16x32_bf16 a[20:23], a[236:239], v[32:35], a[20:23]\n" \
"v_exp_f32 v77, v77\n" \
"v_mfma_f32_16x16x32_bf16 a[52:55], a[232:235], v[8:11], a[52:55]\n" \
"v_add_f32 v241, v241, v77\n" \
"v_exp_f32 v78, v78\n" \
"v_mfma_f32_16x16x32_bf16 a[52:55], a[236:239], v[40:43], a[52:55]\n" \
"ds_read_b128 a[228:231], v246 offset:6144\n" \
"global_load_lds_dwordx4 v250, s[38:39] offset:1024 sc1\n" \
"v_mfma_f32_16x16x32_bf16 v[148:151], a[232:235], v[16:19], v[148:151]\n" \
"v_add_f32 v241, v241, v78\n" \
"v_cvt_pk_bf16_f32 v74, v76, v77\n" \
"v_mfma_f32_16x16x32_bf16 v[148:151], a[236:239], v[48:51], v[148:151]\n" \
"v_exp_f32 v79, v79\n" \
"v_mfma_f32_16x16x32_bf16 v[180:183], a[232:235], v[24:27], v[180:183]\n" \
"v_add_f32 v241, v241, v79\n" \
"v_exp_f32 v88, v88\n" \
"v_mfma_f32_16x16x32_bf16 v[180:183], a[236:239], v[56:59], v[180:183]\n" \
"ds_read_b128 a[232:235], v246 offset:4352\n" \
"global_load_lds_dwordx4 v250, s[38:39] offset:2048 sc1\n" \
"v_mfma_f32_16x16x32_bf16 a[24:27], a[240:243], v[0:3], a[24:27]\n" \
"v_add_f32 v243, v243, v88\n" \
"v_cvt_pk_bf16_f32 v75, v78, v79\n" \
"v_mfma_f32_16x16x32_bf16 a[24:27], a[244:247], v[32:35], a[24:27]\n" \
"v_exp_f32 v89, v89\n" \
"v_mfma_f32_16x16x32_bf16 a[56:59], a[240:243], v[8:11], a[56:59]\n" \
"v_add_f32 v243, v243, v89\n" \
"v_exp_f32 v90, v90\n" \
"v_mfma_f32_16x16x32_bf16 a[56:59], a[244:247], v[40:43], a[56:59]\n" \
"ds_read_b128 a[236:239], v246 offset:6400\n" \
"global_load_lds_dwordx4 v250, s[38:39] offset:3072 sc1\n" \
"v_add_f32 v243, v243, v90\n" \
"v_mfma_f32_16x16x32_bf16 v[152:155], a[240:243], v[16:19], v[152:155]\n" \
"s_add_u32 s38, s38, 17408\n" \
"s_add_u32 s38, s38, s83\n" \
"s_add_u32 s74, s74, s95\n" \
"v_mfma_f32_16x16x32_bf16 v[152:155], a[244:247], v[48:51], v[152:155]\n" \
"s_add_u32 s74, s74, 4096\n" \
"v_cvt_pk_bf16_f32 v88, v88, v89\n" \
"s_cmp_eq_u32 s63, 7\n" \
"v_mfma_f32_16x16x32_bf16 v[184:187], a[240:243], v[24:27], v[184:187]\n" \
"v_exp_f32 v91, v91\n" \
"v_mfma_f32_16x16x32_bf16 v[184:187], a[244:247], v[56:59], v[184:187]\n" \
"ds_read_b128 a[240:243], v246 offset:12288\n" \
"v_add_f32 v243, v243, v91\n" \
"v_mfma_f32_16x16x32_bf16 a[28:31], a[248:251], v[0:3], a[28:31]\n" \
"v_exp_f32 v92, v92\n" \
"v_mfma_f32_16x16x32_bf16 a[28:31], a[252:255], v[32:35], a[28:31]\n" \
"v_add_f32 v243, v243, v92\n" \
"v_cvt_pk_bf16_f32 v89, v90, v91\n" \
"v_mfma_f32_16x16x32_bf16 a[60:63], a[248:251], v[8:11], a[60:63]\n" \
"v_exp_f32 v93, v93\n" \
"v_mfma_f32_16x16x32_bf16 a[60:63], a[252:255], v[40:43], a[60:63]\n" \
"ds_read_b128 a[244:247], v246 offset:14336\n" \
"v_add_f32 v243, v243, v93\n" \
"v_mfma_f32_16x16x32_bf16 v[156:159], a[248:251], v[16:19], v[156:159]\n" \
"v_exp_f32 v94, v94\n" \
"v_mfma_f32_16x16x32_bf16 v[156:159], a[252:255], v[48:51], v[156:159]\n" \
"v_add_f32 v243, v243, v94\n" \
"v_cvt_pk_bf16_f32 v90, v92, v93\n" \
"v_mfma_f32_16x16x32_bf16 v[188:191], a[248:251], v[24:27], v[188:191]\n" \
"s_waitcnt lgkmcnt(6)\n" \
"v_exp_f32 v95, v95\n" \
"v_mfma_f32_16x16x32_bf16 v[188:191], a[252:255], v[56:59], v[188:191]\n" \
"ds_read_b128 a[248:251], v246 offset:12544\n" \
"v_add_f32 v243, v243, v95\n" \
"s_cbranch_scc1 LF7_%=\n" \
"v_mfma_f32_16x16x32_bf16 v[0:3], a[192:195], v[192:195], v[224:227]\n" \
"s_load_dwordx8 s[44:51], s[56:57], s79 glc\n" \
"v_exp_f32 v104, v104\n" \
"v_mfma_f32_16x16x32_bf16 v[0:3], a[196:199], v[196:199], v[0:3]\n" \
"v_cvt_pk_bf16_f32 v91, v94, v95\n" \
"v_add_f32 v241, v241, v104\n" \
"v_mfma_f32_16x16x32_bf16 v[8:11], a[192:195], v[208:211], v[228:231]\n" \
"v_mov_b32 v76, v240\n" \
"v_exp_f32 v105, v105\n" \
"v_mfma_f32_16x16x32_bf16 v[8:11], a[196:199], v[212:215], v[8:11]\n" \
"ds_read_b128 a[252:255], v246 offset:14592\n" \
"v_add_f32 v241, v241, v105\n" \
"v_mfma_f32_16x16x32_bf16 v[16:19], a[192:195], a[160:163], v[232:235]\n" \
"v_exp_f32 v106, v106\n" \
"v_permlane16_swap_b32 v76, v240\n" \
"v_add_f32 v240, v240, v76\n" \
"v_mov_b32 v76, v240\n" \
"v_mfma_f32_16x16x32_bf16 v[16:19], a[196:199], a[164:167], v[16:19]\n" \
"v_add_f32 v241, v241, v106\n" \
"v_cvt_pk_bf16_f32 v104, v104, v105\n" \
"v_mfma_f32_16x16x32_bf16 v[24:27], a[192:195], a[176:179], v[236:239]\n" \
"v_exp_f32 v107, v107\n" \
"v_permlane32_swap_b32 v76, v240\n" \
"v_add_f32 v240, v240, v76\n" \
"v_rcp_f32 v76, v240\n" \
"v_mul_f32 v240, 0x4d800000, v240\n" \
"v_add_u32 v77, 0x1c90080, v249\n" \
"v_cndmask_b32 v77, v249, v77, s[16:17]\n" \
"v_mfma_f32_16x16x32_bf16 v[24:27], a[196:199], a[180:183], v[24:27]\n" \
"ds_read_b128 a[192:195], v247 offset:16384\n" \
"v_add_f32 v241, v241, v107\n" \
"v_accvgpr_read_b32 v64, a0\n" \
"v_accvgpr_read_b32 v65, a1\n" \
"v_accvgpr_read_b32 v66, a2\n" \
"v_accvgpr_read_b32 v67, a3\n" \
"v_accvgpr_read_b32 v68, a4\n" \
"v_accvgpr_read_b32 v69, a5\n" \
"v_mfma_f32_16x16x32_bf16 v[4:7], a[200:203], v[192:195], v[224:227]\n" \
"v_accvgpr_read_b32 v70, a6\n" \
"v_accvgpr_read_b32 v71, a7\n" \
"v_mul_f32 v64, v64, v76\n" \
"v_mul_f32 v65, v65, v76\n" \
"v_mul_f32 v66, v66, v76\n" \
"v_mul_f32 v67, v67, v76\n" \
"v_exp_f32 v108, v108\n" \
"v_mfma_f32_16x16x32_bf16 v[4:7], a[204:207], v[196:199], v[4:7]\n" \
"v_add_f32 v241, v241, v108\n" \
"v_cvt_pk_bf16_f32 v105, v106, v107\n" \
"v_mul_f32 v68, v68, v76\n" \
"v_mul_f32 v69, v69, v76\n" \
"v_mul_f32 v70, v70, v76\n" \
"v_mul_f32 v71, v71, v76\n" \
"v_fma_f32 v240, v64, v65, v240\n" \
"v_cvt_pk_bf16_f32 v96, v64, v65\n" \
"v_mfma_f32_16x16x32_bf16 v[12:15], a[200:203], v[208:211], v[228:231]\n" \
"v_fma_f32 v240, v66, v67, v240\n" \
"v_cvt_pk_bf16_f32 v97, v66, v67\n" \
"v_fma_f32 v240, v68, v69, v240\n" \
"v_cvt_pk_bf16_f32 v98, v68, v69\n" \
"v_fma_f32 v240, v70, v71, v240\n" \
"v_cvt_pk_bf16_f32 v99, v70, v71\n" \
"v_exp_f32 v109, v109\n" \
"v_mfma_f32_16x16x32_bf16 v[12:15], a[204:207], v[212:215], v[12:15]\n" \
"buffer_store_dwordx4 v[96:99], v77, s[56:59], s77 offen offset:0 sc1\n" \
"ds_read_b128 a[196:199], v247 offset:24576\n" \
"v_add_f32 v241, v241, v109\n" \
"v_accvgpr_read_b32 v64, a8\n" \
"v_accvgpr_read_b32 v65, a9\n" \
"v_accvgpr_read_b32 v66, a10\n" \
"v_accvgpr_read_b32 v67, a11\n" \
"v_accvgpr_read_b32 v68, a12\n" \
"v_mfma_f32_16x16x32_bf16 v[20:23], a[200:203], a[160:163], v[232:235]\n" \
"v_accvgpr_read_b32 v69, a13\n" \
"v_accvgpr_read_b32 v70, a14\n" \
"v_accvgpr_read_b32 v71, a15\n" \
"v_mul_f32 v64, v64, v76\n" \
"v_mul_f32 v65, v65, v76\n" \
"v_mul_f32 v66, v66, v76\n" \
"v_exp_f32 v110, v110\n" \
"v_mfma_f32_16x16x32_bf16 v[20:23], a[204:207], a[164:167], v[20:23]\n" \
"v_add_f32 v241, v241, v110\n" \
"v_cvt_pk_bf16_f32 v106, v108, v109\n" \
"v_mul_f32 v67, v67, v76\n" \
"v_mul_f32 v68, v68, v76\n" \
"v_mul_f32 v69, v69, v76\n" \
"v_mul_f32 v70, v70, v76\n" \
"v_mul_f32 v71, v71, v76\n" \
"v_fma_f32 v240, v64, v65, v240\n" \
"v_mfma_f32_16x16x32_bf16 v[28:31], a[200:203], a[176:179], v[236:239]\n" \
"v_cvt_pk_bf16_f32 v100, v64, v65\n" \
"v_fma_f32 v240, v66, v67, v240\n" \
"v_cvt_pk_bf16_f32 v101, v66, v67\n" \
"v_fma_f32 v240, v68, v69, v240\n" \
"v_cvt_pk_bf16_f32 v102, v68, v69\n" \
"v_fma_f32 v240, v70, v71, v240\n" \
"v_exp_f32 v111, v111\n" \
"v_mfma_f32_16x16x32_bf16 v[28:31], a[204:207], a[180:183], v[28:31]\n" \
"ds_read_b128 a[200:203], v247 offset:17408\n" \
"v_add_f32 v241, v241, v111\n" \
"v_cvt_pk_bf16_f32 v103, v70, v71\n" \
"v_mfma_f32_16x16x32_bf16 v[32:35], a[208:211], v[192:195], v[224:227]\n" \
"buffer_store_dwordx4 v[100:103], v77, s[56:59], s77 offen offset:2048 sc1\n" \
"v_exp_f32 v112, v112\n" \
"s_add_u32 s77, s77, 0x1000\n" \
"v_accvgpr_read_b32 v64, a16\n" \
"v_accvgpr_read_b32 v65, a17\n" \
"v_accvgpr_read_b32 v66, a18\n" \
"v_accvgpr_read_b32 v67, a19\n" \
"v_mfma_f32_16x16x32_bf16 v[32:35], a[212:215], v[196:199], v[32:35]\n" \
"v_add_f32 v242, v242, v112\n" \
"v_cvt_pk_bf16_f32 v107, v110, v111\n" \
"v_accvgpr_read_b32 v68, a20\n" \
"v_accvgpr_read_b32 v69, a21\n" \
"v_accvgpr_read_b32 v70, a22\n" \
"v_accvgpr_read_b32 v71, a23\n" \
"v_mul_f32 v64, v64, v76\n" \
"v_mul_f32 v65, v65, v76\n" \
"v_mfma_f32_16x16x32_bf16 v[40:43], a[208:211], v[208:211], v[228:231]\n" \
"v_mul_f32 v66, v66, v76\n" \
"v_mul_f32 v67, v67, v76\n" \
"v_mul_f32 v68, v68, v76\n" \
"v_mul_f32 v69, v69, v76\n" \
"v_mul_f32 v70, v70, v76\n" \
"v_mul_f32 v71, v71, v76\n" \
"v_exp_f32 v113, v113\n" \
"v_mfma_f32_16x16x32_bf16 v[40:43], a[212:215], v[212:215], v[40:43]\n" \
"ds_read_b128 a[204:207], v247 offset:25600\n" \
"v_add_f32 v242, v242, v113\n" \
"v_fma_f32 v240, v64, v65, v240\n" \
"v_cvt_pk_bf16_f32 v96, v64, v65\n" \
"v_fma_f32 v240, v66, v67, v240\n" \
"v_cvt_pk_bf16_f32 v97, v66, v67\n" \
"v_fma_f32 v240, v68, v69, v240\n" \
"v_cvt_pk_bf16_f32 v98, v68, v69\n" \
"v_mfma_f32_16x16x32_bf16 v[48:51], a[208:211], a[160:163], v[232:235]\n" \
"v_fma_f32 v240, v70, v71, v240\n" \
"v_cvt_pk_bf16_f32 v99, v70, v71\n" \
"v_exp_f32 v114, v114\n" \
"v_mfma_f32_16x16x32_bf16 v[48:51], a[212:215], a[164:167], v[48:51]\n" \
"buffer_store_dwordx4 v[96:99], v77, s[56:59], s77 offen offset:0 sc1\n" \
"v_add_f32 v242, v242, v114\n" \
"Lpg6809_%=:\n" \
"s_cbranch_execz Lcb0_%=\n" \
"v_cvt_pk_bf16_f32 v112, v112, v113\n" \
"v_accvgpr_read_b32 v64, a24\n" \
"v_accvgpr_read_b32 v65, a25\n" \
"v_accvgpr_read_b32 v66, a26\n" \
"v_accvgpr_read_b32 v67, a27\n" \
"v_accvgpr_read_b32 v68, a28\n" \
"v_mfma_f32_16x16x32_bf16 v[56:59], a[208:211], a[176:179], v[236:239]\n" \
"v_accvgpr_read_b32 v69, a29\n" \
"v_accvgpr_read_b32 v70, a30\n" \
"v_exp_f32 v115, v115\n" \
"v_mfma_f32_16x16x32_bf16 v[56:59], a[212:215], a[180:183], v[56:59]\n" \
"ds_read_b128 a[208:211], v247 offset:18432\n" \
"v_add_f32 v242, v242, v115\n" \
"v_mfma_f32_16x16x32_bf16 v[36:39], a[216:219], v[192:195], v[224:227]\n" \
"v_exp_f32 v116, v116\n" \
"v_mfma_f32_16x16x32_bf16 v[36:39], a[220:223], v[196:199], v[36:39]\n" \
"v_add_f32 v242, v242, v116\n" \
"v_cvt_pk_bf16_f32 v113, v114, v115\n" \
"v_mfma_f32_16x16x32_bf16 v[44:47], a[216:219], v[208:211], v[228:231]\n" \
"v_exp_f32 v117, v117\n" \
"v_mfma_f32_16x16x32_bf16 v[44:47], a[220:223], v[212:215], v[44:47]\n" \
"ds_read_b128 a[212:215], v247 offset:26624\n" \
"v_add_f32 v242, v242, v117\n" \
"v_mfma_f32_16x16x32_bf16 v[52:55], a[216:219], a[160:163], v[232:235]\n" \
"v_exp_f32 v118, v118\n" \
"v_mfma_f32_16x16x32_bf16 v[52:55], a[220:223], a[164:167], v[52:55]\n" \
"v_add_f32 v242, v242, v118\n" \
"v_cvt_pk_bf16_f32 v114, v116, v117\n" \
"v_mfma_f32_16x16x32_bf16 v[60:63], a[216:219], a[176:179], v[236:239]\n" \
"s_waitcnt lgkmcnt(6)\n" \
"v_exp_f32 v119, v119\n" \
"v_mfma_f32_16x16x32_bf16 v[60:63], a[220:223], a[180:183], v[60:63]\n" \
"ds_read_b128 a[216:219], v247 offset:19456\n" \
"v_add_f32 v242, v242, v119\n" \
"v_mfma_f32_16x16x32_bf16 v[0:3], a[224:227], v[200:203], v[0:3]\n" \
"v_exp_f32 v120, v120\n" \
"v_mfma_f32_16x16x32_bf16 v[0:3], a[228:231], v[204:207], v[0:3]\n" \
"v_add_f32 v243, v243, v120\n" \
"v_cvt_pk_bf16_f32 v115, v118, v119\n" \
"v_mfma_f32_16x16x32_bf16 v[8:11], a[224:227], v[216:219], v[8:11]\n" \
"v_exp_f32 v121, v121\n" \
"v_mfma_f32_16x16x32_bf16 v[8:11], a[228:231], v[220:223], v[8:11]\n" \
"ds_read_b128 a[220:223], v247 offset:27648\n" \
"v_add_f32 v243, v243, v121\n" \
"v_mfma_f32_16x16x32_bf16 v[16:19], a[224:227], a[168:171], v[16:19]\n" \
"v_exp_f32 v122, v122\n" \
"v_mfma_f32_16x16x32_bf16 v[16:19], a[228:231], a[172:175], v[16:19]\n" \
"v_add_f32 v243, v243, v122\n" \
"v_cvt_pk_bf16_f32 v120, v120, v121\n" \
"v_mfma_f32_16x16x32_bf16 v[24:27], a[224:227], a[184:187], v[24:27]\n" \
"v_exp_f32 v123, v123\n" \
"v_mfma_f32_16x16x32_bf16 v[24:27], a[228:231], a[188:191], v[24:27]\n" \
"ds_read_b128 a[224:227], v247 offset:20480\n" \
"v_add_f32 v243, v243, v123\n" \
"v_mfma_f32_16x16x32_bf16 v[4:7], a[232:235], v[200:203], v[4:7]\n" \
"v_exp_f32 v124, v124\n" \
"v_mfma_f32_16x16x32_bf16 v[4:7], a[236:239], v[204:207], v[4:7]\n" \
"v_add_f32 v243, v243, v124\n" \
"v_cvt_pk_bf16_f32 v121, v122, v123\n" \
"v_mfma_f32_16x16x32_bf16 v[12:15], a[232:235], v[216:219], v[12:15]\n" \
"v_exp_f32 v125, v125\n" \
"v_mfma_f32_16x16x32_bf16 v[12:15], a[236:239], v[220:223], v[12:15]\n" \
"ds_read_b128 a[228:231], v247 offset:28672\n" \
"v_add_f32 v243, v243, v125\n" \
"v_mfma_f32_16x16x32_bf16 v[20:23], a[232:235], a[168:171], v[20:23]\n" \
"s_xor_b64 s[52:53], s[44:45], s[42:43]\n" \
"s_xor_b64 s[54:55], s[46:47], s[42:43]\n" \
"s_or_b64 s[52:53], s[52:53], s[54:55]\n" \
"v_mfma_f32_16x16x32_bf16 v[20:23], a[236:239], a[172:175], v[20:23]\n" \
"s_xor_b64 s[54:55], s[48:49], s[42:43]\n" \
"s_or_b64 s[52:53], s[52:53], s[54:55]\n" \
"s_xor_b64 s[54:55], s[50:51], s[42:43]\n" \
"v_mfma_f32_16x16x32_bf16 v[28:31], a[232:235], a[184:187], v[28:31]\n" \
"s_or_b64 s[52:53], s[52:53], s[54:55]\n" \
"v_exp_f32 v126, v126\n" \
"v_mfma_f32_16x16x32_bf16 v[28:31], a[236:239], a[188:191], v[28:31]\n" \
"ds_read_b128 a[232:235], v247 offset:21504\n" \
"v_add_f32 v243, v243, v126\n" \
"v_mfma_f32_16x16x32_bf16 v[32:35], a[240:243], v[200:203], v[32:35]\n" \
"s_and_b64 s[52:53], s[52:53], s[8:9]\n" \
"s_cbranch_scc1 Lrs2_%=\n" \
"Lrr2_%=:\n" \
"v_mfma_f32_16x16x32_bf16 v[32:35], a[244:247], v[204:207], v[32:35]\n" \
"s_mov_b64 s[8:9], s[10:11]\n" \
"v_cvt_pk_bf16_f32 v122, v124, v125\n" \
"v_accvgpr_read_b32 v71, a31\n" \
"v_mfma_f32_16x16x32_bf16 v[40:43], a[240:243], v[216:219], v[40:43]\n" \
"v_exp_f32 v127, v127\n" \
"v_mfma_f32_16x16x32_bf16 v[40:43], a[244:247], v[220:223], v[40:43]\n" \
"ds_read_b128 a[236:239], v247 offset:29696\n" \
"v_add_f32 v243, v243, v127\n" \
"v_mfma_f32_16x16x32_bf16 v[48:51], a[240:243], a[168:171], v[48:51]\n" \
"v_mul_f32 v64, v64, v76\n" \
"v_mul_f32 v65, v65, v76\n" \
"v_mfma_f32_16x16x32_bf16 v[48:51], a[244:247], a[172:175], v[48:51]\n" \
"v_mul_f32 v66, v66, v76\n" \
"v_mul_f32 v67, v67, v76\n" \
"v_mfma_f32_16x16x32_bf16 v[56:59], a[240:243], a[184:187], v[56:59]\n" \
"v_mul_f32 v68, v68, v76\n" \
"v_mul_f32 v69, v69, v76\n" \
"v_mfma_f32_16x16x32_bf16 v[56:59], a[244:247], a[188:191], v[56:59]\n" \
"ds_read_b128 a[240:243], v247 offset:22528\n" \
"v_mul_f32 v70, v70, v76\n" \
"v_mfma_f32_16x16x32_bf16 v[36:39], a[248:251], v[200:203], v[36:39]\n" \
"v_mul_f32 v71, v71, v76\n" \
"v_fma_f32 v240, v64, v65, v240\n" \
"v_mfma_f32_16x16x32_bf16 v[36:39], a[252:255], v[204:207], v[36:39]\n" \
"v_cvt_pk_bf16_f32 v100, v64, v65\n" \
"v_fma_f32 v240, v66, v67, v240\n" \
"v_mfma_f32_16x16x32_bf16 v[44:47], a[248:251], v[216:219], v[44:47]\n" \
"v_cvt_pk_bf16_f32 v101, v66, v67\n" \
"v_fma_f32 v240, v68, v69, v240\n" \
"v_mfma_f32_16x16x32_bf16 v[44:47], a[252:255], v[220:223], v[44:47]\n" \
"ds_read_b128 a[244:247], v247 offset:30720\n" \
"v_cvt_pk_bf16_f32 v102, v68, v69\n" \
"v_mfma_f32_16x16x32_bf16 v[52:55], a[248:251], a[168:171], v[52:55]\n" \
"v_fma_f32 v240, v70, v71, v240\n" \
"v_cvt_pk_bf16_f32 v103, v70, v71\n" \
"v_mfma_f32_16x16x32_bf16 v[52:55], a[252:255], a[172:175], v[52:55]\n" \
"buffer_store_dwordx4 v[100:103], v77, s[56:59], s77 offen offset:2048 sc1\n" \
"s_add_u32 s77, s77, 0x81000\n" \
"v_mfma_f32_16x16x32_bf16 v[60:63], a[248:251], a[184:187], v[60:63]\n" \
"s_waitcnt vmcnt(8) lgkmcnt(6)\n" \
"v_mfma_f32_16x16x32_bf16 v[60:63], a[252:255], a[188:191], v[60:63]\n" \
"s_barrier\n" \
"ds_read_b128 a[248:251], v247 offset:23552\n" \
"v_mfma_f32_16x16x32_bf16 a[32:35], a[192:195], v[72:75], a[32:35]\n" \
"v_exp_f32 v0, v0\n" \
"v_mfma_f32_16x16x32_bf16 a[32:35], a[196:199], v[104:107], a[32:35]\n" \
"v_cvt_pk_bf16_f32 v123, v126, v127\n" \
"v_add_f32 v244, v244, v0\n" \
"v_exp_f32 v1, v1\n" \
"v_mfma_f32_16x16x32_bf16 v[128:131], a[192:195], v[80:83], v[128:131]\n" \
"s_xor_b32 m0, s73, 0x8000\n" \
"v_add_f32 v244, v244, v1\n" \
"v_exp_f32 v2, v2\n" \
"v_mfma_f32_16x16x32_bf16 v[128:131], a[196:199], v[112:115], v[128:131]\n" \
"ds_read_b128 a[252:255], v247 offset:31744\n" \
"global_load_lds_dwordx4 v250, s[36:37] offset:-4096 sc1\n" \
"v_add_f32 v244, v244, v2\n" \
"v_cvt_pk_bf16_f32 v0, v0, v1\n" \
"v_exp_f32 v3, v3\n" \
"v_mfma_f32_16x16x32_bf16 v[160:163], a[192:195], v[88:91], v[160:163]\n" \
"s_add_u32 s70, s82, 0x10000\n" \
"v_add_f32 v244, v244, v3\n" \
"v_exp_f32 v4, v4\n" \
"v_add_u32 v247, s70, v248\n" \
"v_mfma_f32_16x16x32_bf16 v[160:163], a[196:199], v[120:123], v[160:163]\n" \
"ds_read_b128 a[192:195], v252 offset:0\n" \
"v_add_f32 v244, v244, v4\n" \
"v_cvt_pk_bf16_f32 v1, v2, v3\n" \
"v_exp_f32 v5, v5\n" \
"v_mfma_f32_16x16x32_bf16 a[36:39], a[200:203], v[72:75], a[36:39]\n" \
"v_add_f32 v244, v244, v5\n" \
"v_exp_f32 v6, v6\n" \
"v_mfma_f32_16x16x32_bf16 a[36:39], a[204:207], v[104:107], a[36:39]\n" \
"global_load_lds_dwordx4 v250, s[36:37] offset:-3072 sc1\n" \
"v_add_f32 v244, v244, v6\n" \
"v_cvt_pk_bf16_f32 v2, v4, v5\n" \
"v_exp_f32 v7, v7\n" \
"v_mfma_f32_16x16x32_bf16 v[132:135], a[200:203], v[80:83], v[132:135]\n" \
"ds_read_b128 a[196:199], v252 offset:2048\n" \
"v_add_f32 v244, v244, v7\n" \
"v_exp_f32 v8, v8\n" \
"v_mfma_f32_16x16x32_bf16 v[132:135], a[204:207], v[112:115], v[132:135]\n" \
"v_add_f32 v245, v245, v8\n" \
"v_cvt_pk_bf16_f32 v3, v6, v7\n" \
"v_exp_f32 v9, v9\n" \
"v_mfma_f32_16x16x32_bf16 v[164:167], a[200:203], v[88:91], v[164:167]\n" \
"v_add_f32 v245, v245, v9\n" \
"v_exp_f32 v10, v10\n" \
"v_mfma_f32_16x16x32_bf16 v[164:167], a[204:207], v[120:123], v[164:167]\n" \
"ds_read_b128 a[200:203], v252 offset:256\n" \
"v_add_f32 v245, v245, v10\n" \
"v_cvt_pk_bf16_f32 v8, v8, v9\n" \
"v_exp_f32 v11, v11\n" \
"v_mfma_f32_16x16x32_bf16 a[40:43], a[208:211], v[72:75], a[40:43]\n" \
"global_load_lds_dwordx4 v250, s[36:37] offset:-2048 sc1\n" \
"v_add_f32 v245, v245, v11\n" \
"v_exp_f32 v12, v12\n" \
"v_mfma_f32_16x16x32_bf16 a[40:43], a[212:215], v[104:107], a[40:43]\n" \
"v_add_f32 v245, v245, v12\n" \
"v_cvt_pk_bf16_f32 v9, v10, v11\n" \
"v_exp_f32 v13, v13\n" \
"v_mfma_f32_16x16x32_bf16 v[136:139], a[208:211], v[80:83], v[136:139]\n" \
"ds_read_b128 a[204:207], v252 offset:2304\n" \
"v_add_f32 v245, v245, v13\n" \
"v_mfma_f32_16x16x32_bf16 v[136:139], a[212:215], v[112:115], v[136:139]\n" \
"v_exp_f32 v14, v14\n" \
"v_mfma_f32_16x16x32_bf16 v[168:171], a[208:211], v[88:91], v[168:171]\n" \
"global_load_lds_dwordx4 v250, s[36:37] offset:-1024 sc1\n" \
"v_add_f32 v245, v245, v14\n" \
"v_mfma_f32_16x16x32_bf16 v[168:171], a[212:215], v[120:123], v[168:171]\n" \
"ds_read_b128 a[208:211], v252 offset:8192\n" \
"v_cvt_pk_bf16_f32 v10, v12, v13\n" \
"v_mfma_f32_16x16x32_bf16 a[44:47], a[216:219], v[72:75], a[44:47]\n" \
"v_exp_f32 v15, v15\n" \
"v_mfma_f32_16x16x32_bf16 a[44:47], a[220:223], v[104:107], a[44:47]\n" \
"v_add_f32 v245, v245, v15\n" \
"v_exp_f32 v16, v16\n" \
"v_mfma_f32_16x16x32_bf16 v[140:143], a[216:219], v[80:83], v[140:143]\n" \
"ds_read_b128 a[212:215], v252 offset:10240\n" \
"v_add_f32 v242, v242, v16\n" \
"v_mfma_f32_16x16x32_bf16 v[140:143], a[220:223], v[112:115], v[140:143]\n" \
"global_load_lds_dwordx4 v250, s[36:37] offset:0 sc1\n" \
"v_cvt_pk_bf16_f32 v11, v14, v15\n" \
"v_mfma_f32_16x16x32_bf16 v[172:175], a[216:219], v[88:91], v[172:175]\n" \
"s_waitcnt lgkmcnt(6)\n" \
"v_exp_f32 v17, v17\n" \
"v_mfma_f32_16x16x32_bf16 v[172:175], a[220:223], v[120:123], v[172:175]\n" \
"ds_read_b128 a[216:219], v252 offset:8448\n" \
"v_add_f32 v242, v242, v17\n" \
"v_exp_f32 v18, v18\n" \
"v_mfma_f32_16x16x32_bf16 a[48:51], a[224:227], v[72:75], a[48:51]\n" \
"v_add_f32 v242, v242, v18\n" \
"v_cvt_pk_bf16_f32 v16, v16, v17\n" \
"v_mfma_f32_16x16x32_bf16 a[48:51], a[228:231], v[104:107], a[48:51]\n" \
"global_load_lds_dwordx4 v250, s[36:37] offset:1024 sc1\n" \
"v_exp_f32 v19, v19\n" \
"v_mfma_f32_16x16x32_bf16 v[144:147], a[224:227], v[80:83], v[144:147]\n" \
"ds_read_b128 a[220:223], v252 offset:10496\n" \
"v_add_f32 v242, v242, v19\n" \
"v_mfma_f32_16x16x32_bf16 v[144:147], a[228:231], v[112:115], v[144:147]\n" \
"v_exp_f32 v20, v20\n" \
"v_mfma_f32_16x16x32_bf16 v[176:179], a[224:227], v[88:91], v[176:179]\n" \
"v_add_f32 v242, v242, v20\n" \
"v_cvt_pk_bf16_f32 v17, v18, v19\n" \
"v_mfma_f32_16x16x32_bf16 v[176:179], a[228:231], v[120:123], v[176:179]\n" \
"ds_read_b128 a[224:227], v252 offset:4096\n" \
"v_exp_f32 v21, v21\n" \
"v_mfma_f32_16x16x32_bf16 a[52:55], a[232:235], v[72:75], a[52:55]\n" \
"global_load_lds_dwordx4 v250, s[36:37] offset:2048 sc1\n" \
"v_add_f32 v242, v242, v21\n" \
"v_mfma_f32_16x16x32_bf16 a[52:55], a[236:239], v[104:107], a[52:55]\n" \
"v_exp_f32 v22, v22\n" \
"v_mfma_f32_16x16x32_bf16 v[148:151], a[232:235], v[80:83], v[148:151]\n" \
"ds_read_b128 a[228:231], v252 offset:6144\n" \
"v_add_f32 v242, v242, v22\n" \
"v_cvt_pk_bf16_f32 v18, v20, v21\n" \
"v_mfma_f32_16x16x32_bf16 v[148:151], a[236:239], v[112:115], v[148:151]\n" \
"v_exp_f32 v23, v23\n" \
"v_mfma_f32_16x16x32_bf16 v[180:183], a[232:235], v[88:91], v[180:183]\n" \
"global_load_lds_dwordx4 v250, s[36:37] offset:3072 sc1\n" \
"v_add_f32 v242, v242, v23\n" \
"v_mfma_f32_16x16x32_bf16 v[180:183], a[236:239], v[120:123], v[180:183]\n" \
"s_add_u32 s36, s36, 34816\n" \
"ds_read_b128 a[232:235], v252 offset:4352\n" \
"v_exp_f32 v24, v24\n" \
"v_mfma_f32_16x16x32_bf16 a[56:59], a[240:243], v[72:75], a[56:59]\n" \
"v_add_f32 v243, v243, v24\n" \
"v_cvt_pk_bf16_f32 v19, v22, v23\n" \
"v_mfma_f32_16x16x32_bf16 a[56:59], a[244:247], v[104:107], a[56:59]\n" \
"v_exp_f32 v25, v25\n" \
"v_mfma_f32_16x16x32_bf16 v[152:155], a[240:243], v[80:83], v[152:155]\n" \
"ds_read_b128 a[236:239], v252 offset:6400\n" \
"v_add_f32 v243, v243, v25\n" \
"v_exp_f32 v26, v26\n" \
"v_mfma_f32_16x16x32_bf16 v[152:155], a[244:247], v[112:115], v[152:155]\n" \
"v_add_f32 v243, v243, v26\n" \
"v_cvt_pk_bf16_f32 v24, v24, v25\n" \
"v_mfma_f32_16x16x32_bf16 v[184:187], a[240:243], v[88:91], v[184:187]\n" \
"s_load_dwordx8 s[44:51], s[56:57], s79 glc\n" \
"v_exp_f32 v27, v27\n" \
"v_mfma_f32_16x16x32_bf16 v[184:187], a[244:247], v[120:123], v[184:187]\n" \
"ds_read_b128 a[240:243], v252 offset:12288\n" \
"v_add_f32 v243, v243, v27\n" \
"v_exp_f32 v28, v28\n" \
"v_mfma_f32_16x16x32_bf16 a[60:63], a[248:251], v[72:75], a[60:63]\n" \
"v_add_f32 v243, v243, v28\n" \
"v_cvt_pk_bf16_f32 v25, v26, v27\n" \
"v_mfma_f32_16x16x32_bf16 a[60:63], a[252:255], v[104:107], a[60:63]\n" \
"v_exp_f32 v29, v29\n" \
"v_mfma_f32_16x16x32_bf16 v[156:159], a[248:251], v[80:83], v[156:159]\n" \
"ds_read_b128 a[244:247], v252 offset:14336\n" \
"v_add_f32 v243, v243, v29\n" \
"v_exp_f32 v30, v30\n" \
"v_mfma_f32_16x16x32_bf16 v[156:159], a[252:255], v[112:115], v[156:159]\n" \
"v_add_f32 v243, v243, v30\n" \
"v_cvt_pk_bf16_f32 v26, v28, v29\n" \
"v_mfma_f32_16x16x32_bf16 v[188:191], a[248:251], v[88:91], v[188:191]\n" \
"s_waitcnt vmcnt(12) lgkmcnt(6)\n" \
"v_exp_f32 v31, v31\n" \
"v_mfma_f32_16x16x32_bf16 v[188:191], a[252:255], v[120:123], v[188:191]\n" \
"s_barrier\n" \
"ds_read_b128 a[248:251], v252 offset:12544\n" \
"v_add_f32 v243, v243, v31\n" \
"s_mov_b64 exec, 0\n" \
"s_cmp_eq_u32 s61, 0\n" \
"s_cbranch_scc1 Lst1x0_%=\n" \
"s_cmp_eq_u32 s61, 1\n" \
"s_cbranch_scc1 Lst1x1_%=\n" \
"s_cmp_eq_u32 s61, 2\n" \
"s_cbranch_scc1 Lst1x2_%=\n" \
"s_branch Lst1x3_%=\n" \
"Lcb1_%=:\n" \
"s_mov_b64 exec, -1\n" \
"s_waitcnt lgkmcnt(0)\n" \
"s_xor_b64 s[52:53], s[44:45], s[42:43]\n" \
"s_xor_b64 s[54:55], s[46:47], s[42:43]\n" \
"s_or_b64 s[52:53], s[52:53], s[54:55]\n" \
"s_xor_b64 s[54:55], s[48:49], s[42:43]\n" \
"s_or_b64 s[52:53], s[52:53], s[54:55]\n" \
"s_xor_b64 s[54:55], s[50:51], s[42:43]\n" \
"s_or_b64 s[52:53], s[52:53], s[54:55]\n" \
"s_and_b64 s[52:53], s[52:53], s[8:9]\n" \
"s_cbranch_scc1 Lrs3_%=\n" \
"Lrr3_%=:\n" \
"s_branch LA1g_%=\n" \
"Lpg7118_%=:\n" \
"s_cbranch_execz Lcb0_%=\n" \
"Lloop2_%=:\n" \
"v_mfma_f32_16x16x32_bf16 a[64:67], a[192:195], v[0:3], a[64:67]\n" \
"v_exp_f32 v64, v64\n" \
"v_mfma_f32_16x16x32_bf16 a[64:67], a[196:199], v[32:35], a[64:67]\n" \
"v_cvt_pk_bf16_f32 v59, v62, v63\n" \
"v_add_f32 v244, v244, v64\n" \
"v_mfma_f32_16x16x32_bf16 a[96:99], a[192:195], v[8:11], a[96:99]\n" \
"s_mov_b32 m0, s73\n" \
"v_exp_f32 v65, v65\n" \
"v_mfma_f32_16x16x32_bf16 a[96:99], a[196:199], v[40:43], a[96:99]\n" \
"ds_read_b128 a[252:255], v247 offset:15360\n" \
"global_load_lds_dwordx4 v250, s[36:37] offset:-4096 sc1\n" \
"v_add_f32 v244, v244, v65\n" \
"v_mfma_f32_16x16x32_bf16 v[128:131], a[192:195], v[16:19], v[128:131]\n" \
"v_exp_f32 v66, v66\n" \
"v_mfma_f32_16x16x32_bf16 v[128:131], a[196:199], v[48:51], v[128:131]\n" \
"v_add_f32 v244, v244, v66\n" \
"v_cvt_pk_bf16_f32 v64, v64, v65\n" \
"v_mfma_f32_16x16x32_bf16 v[160:163], a[192:195], v[24:27], v[160:163]\n" \
"v_exp_f32 v67, v67\n" \
"v_mfma_f32_16x16x32_bf16 v[160:163], a[196:199], v[56:59], v[160:163]\n" \
"ds_read_b128 a[192:195], v246 offset:0\n" \
"global_load_lds_dwordx4 v250, s[36:37] offset:-3072 sc1\n" \
"v_add_f32 v244, v244, v67\n" \
"v_mfma_f32_16x16x32_bf16 a[68:71], a[200:203], v[0:3], a[68:71]\n" \
"v_exp_f32 v68, v68\n" \
"v_mfma_f32_16x16x32_bf16 a[68:71], a[204:207], v[32:35], a[68:71]\n" \
"v_add_f32 v244, v244, v68\n" \
"v_cvt_pk_bf16_f32 v65, v66, v67\n" \
"v_mfma_f32_16x16x32_bf16 a[100:103], a[200:203], v[8:11], a[100:103]\n" \
"v_exp_f32 v69, v69\n" \
"v_mfma_f32_16x16x32_bf16 a[100:103], a[204:207], v[40:43], a[100:103]\n" \
"ds_read_b128 a[196:199], v246 offset:2048\n" \
"v_add_f32 v244, v244, v69\n" \
"v_mfma_f32_16x16x32_bf16 v[132:135], a[200:203], v[16:19], v[132:135]\n" \
"v_exp_f32 v70, v70\n" \
"v_mfma_f32_16x16x32_bf16 v[132:135], a[204:207], v[48:51], v[132:135]\n" \
"v_add_f32 v244, v244, v70\n" \
"v_cvt_pk_bf16_f32 v66, v68, v69\n" \
"v_mfma_f32_16x16x32_bf16 v[164:167], a[200:203], v[24:27], v[164:167]\n" \
"v_exp_f32 v71, v71\n" \
"v_mfma_f32_16x16x32_bf16 v[164:167], a[204:207], v[56:59], v[164:167]\n" \
"ds_read_b128 a[200:203], v246 offset:256\n" \
"global_load_lds_dwordx4 v250, s[36:37] offset:-2048 sc1\n" \
"v_add_f32 v244, v244, v71\n" \
"v_mfma_f32_16x16x32_bf16 a[72:75], a[208:211], v[0:3], a[72:75]\n" \
"v_exp_f32 v72, v72\n" \
"v_mfma_f32_16x16x32_bf16 a[72:75], a[212:215], v[32:35], a[72:75]\n" \
"v_add_f32 v245, v245, v72\n" \
"v_cvt_pk_bf16_f32 v67, v70, v71\n" \
"v_mfma_f32_16x16x32_bf16 a[104:107], a[208:211], v[8:11], a[104:107]\n" \
"v_exp_f32 v73, v73\n" \
"v_mfma_f32_16x16x32_bf16 a[104:107], a[212:215], v[40:43], a[104:107]\n" \
"ds_read_b128 a[204:207], v246 offset:2304\n" \
"global_load_lds_dwordx4 v250, s[36:37] offset:-1024 sc1\n" \
"v_add_f32 v245, v245, v73\n" \
"v_mfma_f32_16x16x32_bf16 v[136:139], a[208:211], v[16:19], v[136:139]\n" \
"v_exp_f32 v74, v74\n" \
"v_mfma_f32_16x16x32_bf16 v[136:139], a[212:215], v[48:51], v[136:139]\n" \
"v_add_f32 v245, v245, v74\n" \
"v_cvt_pk_bf16_f32 v72, v72, v73\n" \
"v_mfma_f32_16x16x32_bf16 v[168:171], a[208:211], v[24:27], v[168:171]\n" \
"v_exp_f32 v75, v75\n" \
"v_mfma_f32_16x16x32_bf16 v[168:171], a[212:215], v[56:59], v[168:171]\n" \
"ds_read_b128 a[208:211], v246 offset:8192\n" \
"v_add_f32 v245, v245, v75\n" \
"v_mfma_f32_16x16x32_bf16 a[76:79], a[216:219], v[0:3], a[76:79]\n" \
"v_exp_f32 v76, v76\n" \
"v_mfma_f32_16x16x32_bf16 a[76:79], a[220:223], v[32:35], a[76:79]\n" \
"v_add_f32 v245, v245, v76\n" \
"v_cvt_pk_bf16_f32 v73, v74, v75\n" \
"v_mfma_f32_16x16x32_bf16 a[108:111], a[216:219], v[8:11], a[108:111]\n" \
"v_exp_f32 v77, v77\n" \
"v_mfma_f32_16x16x32_bf16 a[108:111], a[220:223], v[40:43], a[108:111]\n" \
"ds_read_b128 a[212:215], v246 offset:10240\n" \
"global_load_lds_dwordx4 v250, s[36:37] offset:0 sc1\n" \
"v_add_f32 v245, v245, v77\n" \
"v_mfma_f32_16x16x32_bf16 v[140:143], a[216:219], v[16:19], v[140:143]\n" \
"v_exp_f32 v78, v78\n" \
"v_mfma_f32_16x16x32_bf16 v[140:143], a[220:223], v[48:51], v[140:143]\n" \
"v_add_f32 v245, v245, v78\n" \
"v_cvt_pk_bf16_f32 v74, v76, v77\n" \
"v_mfma_f32_16x16x32_bf16 v[172:175], a[216:219], v[24:27], v[172:175]\n" \
"s_waitcnt lgkmcnt(6)\n" \
"v_exp_f32 v79, v79\n" \
"v_mfma_f32_16x16x32_bf16 v[172:175], a[220:223], v[56:59], v[172:175]\n" \
"ds_read_b128 a[216:219], v246 offset:8448\n" \
"global_load_lds_dwordx4 v250, s[36:37] offset:1024 sc1\n" \
"v_add_f32 v245, v245, v79\n" \
"v_mfma_f32_16x16x32_bf16 a[80:83], a[224:227], v[0:3], a[80:83]\n" \
"v_exp_f32 v80, v80\n" \
"v_mfma_f32_16x16x32_bf16 a[80:83], a[228:231], v[32:35], a[80:83]\n" \
"v_add_f32 v242, v242, v80\n" \
"v_cvt_pk_bf16_f32 v75, v78, v79\n" \
"v_mfma_f32_16x16x32_bf16 a[112:115], a[224:227], v[8:11], a[112:115]\n" \
"v_exp_f32 v81, v81\n" \
"v_mfma_f32_16x16x32_bf16 a[112:115], a[228:231], v[40:43], a[112:115]\n" \
"ds_read_b128 a[220:223], v246 offset:10496\n" \
"v_add_f32 v242, v242, v81\n" \
"v_mfma_f32_16x16x32_bf16 v[144:147], a[224:227], v[16:19], v[144:147]\n" \
"v_exp_f32 v82, v82\n" \
"v_mfma_f32_16x16x32_bf16 v[144:147], a[228:231], v[48:51], v[144:147]\n" \
"v_add_f32 v242, v242, v82\n" \
"v_cvt_pk_bf16_f32 v80, v80, v81\n" \
"v_mfma_f32_16x16x32_bf16 v[176:179], a[224:227], v[24:27], v[176:179]\n" \
"v_exp_f32 v83, v83\n" \
"v_mfma_f32_16x16x32_bf16 v[176:179], a[228:231], v[56:59], v[176:179]\n" \
"ds_read_b128 a[224:227], v246 offset:4096\n" \
"global_load_lds_dwordx4 v250, s[36:37] offset:2048 sc1\n" \
"v_add_f32 v242, v242, v83\n" \
"v_mfma_f32_16x16x32_bf16 a[84:87], a[232:235], v[0:3], a[84:87]\n" \
"v_exp_f32 v84, v84\n" \
"v_mfma_f32_16x16x32_bf16 a[84:87], a[236:239], v[32:35], a[84:87]\n" \
"v_add_f32 v242, v242, v84\n" \
"v_cvt_pk_bf16_f32 v81, v82, v83\n" \
"v_mfma_f32_16x16x32_bf16 a[116:119], a[232:235], v[8:11], a[116:119]\n" \
"v_exp_f32 v85, v85\n" \
"v_mfma_f32_16x16x32_bf16 a[116:119], a[236:239], v[40:43], a[116:119]\n" \
"ds_read_b128 a[228:231], v246 offset:6144\n" \
"global_load_lds_dwordx4 v250, s[36:37] offset:3072 sc1\n" \
"v_add_f32 v242, v242, v85\n" \
"v_mfma_f32_16x16x32_bf16 v[148:151], a[232:235], v[16:19], v[148:151]\n" \
"s_add_u32 s36, s36, 34816\n" \
"v_exp_f32 v86, v86\n" \
"v_mfma_f32_16x16x32_bf16 v[148:151], a[236:239], v[48:51], v[148:151]\n" \
"v_add_f32 v242, v242, v86\n" \
"v_cvt_pk_bf16_f32 v82, v84, v85\n" \
"v_mfma_f32_16x16x32_bf16 v[180:183], a[232:235], v[24:27], v[180:183]\n" \
"v_exp_f32 v87, v87\n" \
"v_mfma_f32_16x16x32_bf16 v[180:183], a[236:239], v[56:59], v[180:183]\n" \
"ds_read_b128 a[232:235], v246 offset:4352\n" \
"v_add_f32 v242, v242, v87\n" \
"v_mfma_f32_16x16x32_bf16 a[88:91], a[240:243], v[0:3], a[88:91]\n" \
"v_exp_f32 v88, v88\n" \
"v_mfma_f32_16x16x32_bf16 a[88:91], a[244:247], v[32:35], a[88:91]\n" \
"v_add_f32 v243, v243, v88\n" \
"v_cvt_pk_bf16_f32 v83, v86, v87\n" \
"v_mfma_f32_16x16x32_bf16 a[120:123], a[240:243], v[8:11], a[120:123]\n" \
"v_exp_f32 v89, v89\n" \
"v_mfma_f32_16x16x32_bf16 a[120:123], a[244:247], v[40:43], a[120:123]\n" \
"ds_read_b128 a[236:239], v246 offset:6400\n" \
"v_add_f32 v243, v243, v89\n" \
"v_mfma_f32_16x16x32_bf16 v[152:155], a[240:243], v[16:19], v[152:155]\n" \
"v_exp_f32 v90, v90\n" \
"v_mfma_f32_16x16x32_bf16 v[152:155], a[244:247], v[48:51], v[152:155]\n" \
"v_add_f32 v243, v243, v90\n" \
"v_cvt_pk_bf16_f32 v88, v88, v89\n" \
"v_mfma_f32_16x16x32_bf16 v[184:187], a[240:243], v[24:27], v[184:187]\n" \
"v_exp_f32 v91, v91\n" \
"v_mfma_f32_16x16x32_bf16 v[184:187], a[244:247], v[56:59], v[184:187]\n" \
"ds_read_b128 a[240:243], v246 offset:12288\n" \
"v_add_f32 v243, v243, v91\n" \
"v_mfma_f32_16x16x32_bf16 a[92:95], a[248:251], v[0:3], a[92:95]\n" \
"v_exp_f32 v92, v92\n" \
"v_mfma_f32_16x16x32_bf16 a[92:95], a[252:255], v[32:35], a[92:95]\n" \
"v_add_f32 v243, v243, v92\n" \
"v_cvt_pk_bf16_f32 v89, v90, v91\n" \
"v_mfma_f32_16x16x32_bf16 a[124:127], a[248:251], v[8:11], a[124:127]\n" \
"v_exp_f32 v93, v93\n" \
"v_mfma_f32_16x16x32_bf16 a[124:127], a[252:255], v[40:43], a[124:127]\n" \
"ds_read_b128 a[244:247], v246 offset:14336\n" \
"v_add_f32 v243, v243, v93\n" \
"v_mfma_f32_16x16x32_bf16 v[156:159], a[248:251], v[16:19], v[156:159]\n" \
"v_exp_f32 v94, v94\n" \
"v_mfma_f32_16x16x32_bf16 v[156:159], a[252:255], v[48:51], v[156:159]\n" \
"v_add_f32 v243, v243, v94\n" \
"v_cvt_pk_bf16_f32 v90, v92, v93\n" \
"v_mfma_f32_16x16x32_bf16 v[188:191], a[248:251], v[24:27], v[188:191]\n" \
"s_waitcnt vmcnt(8) lgkmcnt(6)\n" \
"v_exp_f32 v95, v95\n" \
"v_mfma_f32_16x16x32_bf16 v[188:191], a[252:255], v[56:59], v[188:191]\n" \
"s_barrier\n" \
"ds_read_b128 a[248:251], v246 offset:12544\n" \
"v_add_f32 v243, v243, v95\n" \
"v_mfma_f32_16x16x32_bf16 v[0:3], a[192:195], v[192:195], v[224:227]\n" \
"v_exp_f32 v96, v96\n" \
"v_mfma_f32_16x16x32_bf16 v[0:3], a[196:199], v[196:199], v[0:3]\n" \
"v_cvt_pk_bf16_f32 v91, v94, v95\n" \
"v_add_f32 v244, v244, v96\n" \
"v_mfma_f32_16x16x32_bf16 v[8:11], a[192:195], v[208:211], v[228:231]\n" \
"s_mov_b32 m0, s74\n" \
"v_exp_f32 v97, v97\n" \
"v_mfma_f32_16x16x32_bf16 v[8:11], a[196:199], v[212:215], v[8:11]\n" \
"ds_read_b128 a[252:255], v246 offset:14592\n" \
"global_load_lds_dwordx4 v250, s[38:39] offset:-4096 sc1\n" \
"v_add_f32 v244, v244, v97\n" \
"v_mfma_f32_16x16x32_bf16 v[16:19], a[192:195], a[160:163], v[232:235]\n" \
"v_exp_f32 v98, v98\n" \
"v_mfma_f32_16x16x32_bf16 v[16:19], a[196:199], a[164:167], v[16:19]\n" \
"v_add_f32 v244, v244, v98\n" \
"v_cvt_pk_bf16_f32 v96, v96, v97\n" \
"v_mfma_f32_16x16x32_bf16 v[24:27], a[192:195], a[176:179], v[236:239]\n" \
"v_exp_f32 v99, v99\n" \
"v_mfma_f32_16x16x32_bf16 v[24:27], a[196:199], a[180:183], v[24:27]\n" \
"ds_read_b128 a[192:195], v247 offset:32768\n" \
"global_load_lds_dwordx4 v250, s[38:39] offset:-3072 sc1\n" \
"v_add_f32 v244, v244, v99\n" \
"v_mfma_f32_16x16x32_bf16 v[4:7], a[200:203], v[192:195], v[224:227]\n" \
"v_exp_f32 v100, v100\n" \
"v_mfma_f32_16x16x32_bf16 v[4:7], a[204:207], v[196:199], v[4:7]\n" \
"v_add_f32 v244, v244, v100\n" \
"v_cvt_pk_bf16_f32 v97, v98, v99\n" \
"v_mfma_f32_16x16x32_bf16 v[12:15], a[200:203], v[208:211], v[228:231]\n" \
"v_exp_f32 v101, v101\n" \
"v_mfma_f32_16x16x32_bf16 v[12:15], a[204:207], v[212:215], v[12:15]\n" \
"ds_read_b128 a[196:199], v247 offset:40960\n" \
"v_add_f32 v244, v244, v101\n" \
"v_mfma_f32_16x16x32_bf16 v[20:23], a[200:203], a[160:163], v[232:235]\n" \
"v_exp_f32 v102, v102\n" \
"v_mfma_f32_16x16x32_bf16 v[20:23], a[204:207], a[164:167], v[20:23]\n" \
"v_add_f32 v244, v244, v102\n" \
"v_cvt_pk_bf16_f32 v98, v100, v101\n" \
"v_mfma_f32_16x16x32_bf16 v[28:31], a[200:203], a[176:179], v[236:239]\n" \
"v_exp_f32 v103, v103\n" \
"v_mfma_f32_16x16x32_bf16 v[28:31], a[204:207], a[180:183], v[28:31]\n" \
"ds_read_b128 a[200:203], v247 offset:33792\n" \
"global_load_lds_dwordx4 v250, s[38:39] offset:-2048 sc1\n" \
"v_add_f32 v244, v244, v103\n" \
"v_mfma_f32_16x16x32_bf16 v[32:35], a[208:211], v[192:195], v[224:227]\n" \
"v_exp_f32 v104, v104\n" \
"v_mfma_f32_16x16x32_bf16 v[32:35], a[212:215], v[196:199], v[32:35]\n" \
"v_add_f32 v245, v245, v104\n" \
"v_cvt_pk_bf16_f32 v99, v102, v103\n" \
"v_mfma_f32_16x16x32_bf16 v[40:43], a[208:211], v[208:211], v[228:231]\n" \
"v_exp_f32 v105, v105\n" \
"v_mfma_f32_16x16x32_bf16 v[40:43], a[212:215], v[212:215], v[40:43]\n" \
"ds_read_b128 a[204:207], v247 offset:41984\n" \
"global_load_lds_dwordx4 v250, s[38:39] offset:-1024 sc1\n" \
"v_add_f32 v245, v245, v105\n" \
"v_mfma_f32_16x16x32_bf16 v[48:51], a[208:211], a[160:163], v[232:235]\n" \
"v_exp_f32 v106, v106\n" \
"v_mfma_f32_16x16x32_bf16 v[48:51], a[212:215], a[164:167], v[48:51]\n" \
"v_add_f32 v245, v245, v106\n" \
"v_cvt_pk_bf16_f32 v104, v104, v105\n" \
"v_mfma_f32_16x16x32_bf16 v[56:59], a[208:211], a[176:179], v[236:239]\n" \
"v_exp_f32 v107, v107\n" \
"v_mfma_f32_16x16x32_bf16 v[56:59], a[212:215], a[180:183], v[56:59]\n" \
"ds_read_b128 a[208:211], v247 offset:34816\n" \
"v_add_f32 v245, v245, v107\n" \
"v_mfma_f32_16x16x32_bf16 v[36:39], a[216:219], v[192:195], v[224:227]\n" \
"v_exp_f32 v108, v108\n" \
"v_mfma_f32_16x16x32_bf16 v[36:39], a[220:223], v[196:199], v[36:39]\n" \
"v_add_f32 v245, v245, v108\n" \
"v_cvt_pk_bf16_f32 v105, v106, v107\n" \
"v_mfma_f32_16x16x32_bf16 v[44:47], a[216:219], v[208:211], v[228:231]\n" \
"v_exp_f32 v109, v109\n" \
"v_mfma_f32_16x16x32_bf16 v[44:47], a[220:223], v[212:215], v[44:47]\n" \
"ds_read_b128 a[212:215], v247 offset:43008\n" \
"global_load_lds_dwordx4 v250, s[38:39] offset:0 sc1\n" \
"v_add_f32 v245, v245, v109\n" \
"v_mfma_f32_16x16x32_bf16 v[52:55], a[216:219], a[160:163], v[232:235]\n" \
"v_exp_f32 v110, v110\n" \
"v_mfma_f32_16x16x32_bf16 v[52:55], a[220:223], a[164:167], v[52:55]\n" \
"v_add_f32 v245, v245, v110\n" \
"v_cvt_pk_bf16_f32 v106, v108, v109\n" \
"v_mfma_f32_16x16x32_bf16 v[60:63], a[216:219], a[176:179], v[236:239]\n" \
"s_waitcnt lgkmcnt(6)\n" \
"v_exp_f32 v111, v111\n" \
"v_mfma_f32_16x16x32_bf16 v[60:63], a[220:223], a[180:183], v[60:63]\n" \
"ds_read_b128 a[216:219], v247 offset:35840\n" \
"global_load_lds_dwordx4 v250, s[38:39] offset:1024 sc1\n" \
"v_add_f32 v245, v245, v111\n" \
"v_mfma_f32_16x16x32_bf16 v[0:3], a[224:227], v[200:203], v[0:3]\n" \
"v_exp_f32 v112, v112\n" \
"v_mfma_f32_16x16x32_bf16 v[0:3], a[228:231], v[204:207], v[0:3]\n" \
"v_add_f32 v242, v242, v112\n" \
"v_cvt_pk_bf16_f32 v107, v110, v111\n" \
"v_mfma_f32_16x16x32_bf16 v[8:11], a[224:227], v[216:219], v[8:11]\n" \
"v_exp_f32 v113, v113\n" \
"v_mfma_f32_16x16x32_bf16 v[8:11], a[228:231], v[220:223], v[8:11]\n" \
"ds_read_b128 a[220:223], v247 offset:44032\n" \
"v_add_f32 v242, v242, v113\n" \
"v_mfma_f32_16x16x32_bf16 v[16:19], a[224:227], a[168:171], v[16:19]\n" \
"v_exp_f32 v114, v114\n" \
"v_mfma_f32_16x16x32_bf16 v[16:19], a[228:231], a[172:175], v[16:19]\n" \
"v_add_f32 v242, v242, v114\n" \
"v_cvt_pk_bf16_f32 v112, v112, v113\n" \
"v_mfma_f32_16x16x32_bf16 v[24:27], a[224:227], a[184:187], v[24:27]\n" \
"v_exp_f32 v115, v115\n" \
"v_mfma_f32_16x16x32_bf16 v[24:27], a[228:231], a[188:191], v[24:27]\n" \
"ds_read_b128 a[224:227], v247 offset:36864\n" \
"global_load_lds_dwordx4 v250, s[38:39] offset:2048 sc1\n" \
"v_add_f32 v242, v242, v115\n" \
"v_mfma_f32_16x16x32_bf16 v[4:7], a[232:235], v[200:203], v[4:7]\n" \
"v_exp_f32 v116, v116\n" \
"v_mfma_f32_16x16x32_bf16 v[4:7], a[236:239], v[204:207], v[4:7]\n" \
"v_add_f32 v242, v242, v116\n" \
"v_cvt_pk_bf16_f32 v113, v114, v115\n" \
"v_mfma_f32_16x16x32_bf16 v[12:15], a[232:235], v[216:219], v[12:15]\n" \
"v_exp_f32 v117, v117\n" \
"v_mfma_f32_16x16x32_bf16 v[12:15], a[236:239], v[220:223], v[12:15]\n" \
"ds_read_b128 a[228:231], v247 offset:45056\n" \
"global_load_lds_dwordx4 v250, s[38:39] offset:3072 sc1\n" \
"v_add_f32 v242, v242, v117\n" \
"v_mfma_f32_16x16x32_bf16 v[20:23], a[232:235], a[168:171], v[20:23]\n" \
"s_add_u32 s38, s38, 34816\n" \
"v_exp_f32 v118, v118\n" \
"v_mfma_f32_16x16x32_bf16 v[20:23], a[236:239], a[172:175], v[20:23]\n" \
"v_add_f32 v242, v242, v118\n" \
"v_cvt_pk_bf16_f32 v114, v116, v117\n" \
"v_mfma_f32_16x16x32_bf16 v[28:31], a[232:235], a[184:187], v[28:31]\n" \
"v_exp_f32 v119, v119\n" \
"v_mfma_f32_16x16x32_bf16 v[28:31], a[236:239], a[188:191], v[28:31]\n" \
"ds_read_b128 a[232:235], v247 offset:37888\n" \
"v_add_f32 v242, v242, v119\n" \
"v_mfma_f32_16x16x32_bf16 v[32:35], a[240:243], v[200:203], v[32:35]\n" \
"v_exp_f32 v120, v120\n" \
"v_mfma_f32_16x16x32_bf16 v[32:35], a[244:247], v[204:207], v[32:35]\n" \
"v_add_f32 v243, v243, v120\n" \
"v_cvt_pk_bf16_f32 v115, v118, v119\n" \
"v_mfma_f32_16x16x32_bf16 v[40:43], a[240:243], v[216:219], v[40:43]\n" \
"v_exp_f32 v121, v121\n" \
"v_mfma_f32_16x16x32_bf16 v[40:43], a[244:247], v[220:223], v[40:43]\n" \
"ds_read_b128 a[236:239], v247 offset:46080\n" \
"v_add_f32 v243, v243, v121\n" \
"v_mfma_f32_16x16x32_bf16 v[48:51], a[240:243], a[168:171], v[48:51]\n" \
"v_exp_f32 v122, v122\n" \
"v_mfma_f32_16x16x32_bf16 v[48:51], a[244:247], a[172:175], v[48:51]\n" \
"v_add_f32 v243, v243, v122\n" \
"v_cvt_pk_bf16_f32 v120, v120, v121\n" \
"v_mfma_f32_16x16x32_bf16 v[56:59], a[240:243], a[184:187], v[56:59]\n" \
"v_exp_f32 v123, v123\n" \
"v_mfma_f32_16x16x32_bf16 v[56:59], a[244:247], a[188:191], v[56:59]\n" \
"ds_read_b128 a[240:243], v247 offset:38912\n" \
"v_add_f32 v243, v243, v123\n" \
"v_mfma_f32_16x16x32_bf16 v[36:39], a[248:251], v[200:203], v[36:39]\n" \
"v_exp_f32 v124, v124\n" \
"v_mfma_f32_16x16x32_bf16 v[36:39], a[252:255], v[204:207], v[36:39]\n" \
"v_add_f32 v243, v243, v124\n" \
"v_cvt_pk_bf16_f32 v121, v122, v123\n" \
"v_mfma_f32_16x16x32_bf16 v[44:47], a[248:251], v[216:219], v[44:47]\n" \
"v_exp_f32 v125, v125\n" \
"v_mfma_f32_16x16x32_bf16 v[44:47], a[252:255], v[220:223], v[44:47]\n" \
"ds_read_b128 a[244:247], v247 offset:47104\n" \
"v_add_f32 v243, v243, v125\n" \
"v_mfma_f32_16x16x32_bf16 v[52:55], a[248:251], a[168:171], v[52:55]\n" \
"v_exp_f32 v126, v126\n" \
"v_mfma_f32_16x16x32_bf16 v[52:55], a[252:255], a[172:175], v[52:55]\n" \
"v_add_f32 v243, v243, v126\n" \
"v_cvt_pk_bf16_f32 v122, v124, v125\n" \
"v_mfma_f32_16x16x32_bf16 v[60:63], a[248:251], a[184:187], v[60:63]\n" \
"Lpg7460_%=:\n" \
"Lst1x0_%=:\n" \
"s_cbranch_execz Lpg9904_%=\n" \
"s_waitcnt vmcnt(8) lgkmcnt(6)\n" \
"v_exp_f32 v127, v127\n" \
"v_mfma_f32_16x16x32_bf16 v[60:63], a[252:255], a[188:191], v[60:63]\n" \
"s_barrier\n" \
"ds_read_b128 a[248:251], v247 offset:39936\n" \
"v_add_f32 v243, v243, v127\n" \
"v_mfma_f32_16x16x32_bf16 a[64:67], a[192:195], v[64:67], a[64:67]\n" \
"v_exp_f32 v0, v0\n" \
"v_mfma_f32_16x16x32_bf16 a[64:67], a[196:199], v[96:99], a[64:67]\n" \
"v_cvt_pk_bf16_f32 v123, v126, v127\n" \
"v_add_f32 v244, v244, v0\n" \
"v_mfma_f32_16x16x32_bf16 a[96:99], a[192:195], v[72:75], a[96:99]\n" \
"s_xor_b32 m0, s73, 0x8000\n" \
"v_exp_f32 v1, v1\n" \
"v_mfma_f32_16x16x32_bf16 a[96:99], a[196:199], v[104:107], a[96:99]\n" \
"ds_read_b128 a[252:255], v247 offset:48128\n" \
"global_load_lds_dwordx4 v250, s[36:37] offset:-4096 sc1\n" \
"v_add_f32 v244, v244, v1\n" \
"v_mfma_f32_16x16x32_bf16 v[128:131], a[192:195], v[80:83], v[128:131]\n" \
"v_exp_f32 v2, v2\n" \
"v_mfma_f32_16x16x32_bf16 v[128:131], a[196:199], v[112:115], v[128:131]\n" \
"v_add_f32 v244, v244, v2\n" \
"v_cvt_pk_bf16_f32 v0, v0, v1\n" \
"v_mfma_f32_16x16x32_bf16 v[160:163], a[192:195], v[88:91], v[160:163]\n" \
"v_exp_f32 v3, v3\n" \
"v_mfma_f32_16x16x32_bf16 v[160:163], a[196:199], v[120:123], v[160:163]\n" \
"ds_read_b128 a[192:195], v252 offset:0\n" \
"global_load_lds_dwordx4 v250, s[36:37] offset:-3072 sc1\n" \
"v_add_f32 v244, v244, v3\n" \
"v_mfma_f32_16x16x32_bf16 a[68:71], a[200:203], v[64:67], a[68:71]\n" \
"v_exp_f32 v4, v4\n" \
"v_mfma_f32_16x16x32_bf16 a[68:71], a[204:207], v[96:99], a[68:71]\n" \
"v_add_f32 v244, v244, v4\n" \
"v_cvt_pk_bf16_f32 v1, v2, v3\n" \
"v_mfma_f32_16x16x32_bf16 a[100:103], a[200:203], v[72:75], a[100:103]\n" \
"v_exp_f32 v5, v5\n" \
"v_mfma_f32_16x16x32_bf16 a[100:103], a[204:207], v[104:107], a[100:103]\n" \
"ds_read_b128 a[196:199], v252 offset:2048\n" \
"v_add_f32 v244, v244, v5\n" \
"v_mfma_f32_16x16x32_bf16 v[132:135], a[200:203], v[80:83], v[132:135]\n" \
"v_exp_f32 v6, v6\n" \
"v_mfma_f32_16x16x32_bf16 v[132:135], a[204:207], v[112:115], v[132:135]\n" \
"v_add_f32 v244, v244, v6\n" \
"v_cvt_pk_bf16_f32 v2, v4, v5\n" \
"v_mfma_f32_16x16x32_bf16 v[164:167], a[200:203], v[88:91], v[164:167]\n" \
"v_exp_f32 v7, v7\n" \
"v_mfma_f32_16x16x32_bf16 v[164:167], a[204:207], v[120:123], v[164:167]\n" \
"ds_read_b128 a[200:203], v252 offset:256\n" \
"global_load_lds_dwordx4 v250, s[36:37] offset:-2048 sc1\n" \
"v_add_f32 v244, v244, v7\n" \
"v_mfma_f32_16x16x32_bf16 a[72:75], a[208:211], v[64:67], a[72:75]\n" \
"v_exp_f32 v8, v8\n" \
"v_mfma_f32_16x16x32_bf16 a[72:75], a[212:215], v[96:99], a[72:75]\n" \
"v_add_f32 v245, v245, v8\n" \
"v_cvt_pk_bf16_f32 v3, v6, v7\n" \
"v_mfma_f32_16x16x32_bf16 a[104:107], a[208:211], v[72:75], a[104:107]\n" \
"v_exp_f32 v9, v9\n" \
"v_mfma_f32_16x16x32_bf16 a[104:107], a[212:215], v[104:107], a[104:107]\n" \
"ds_read_b128 a[204:207], v252 offset:2304\n" \
"global_load_lds_dwordx4 v250, s[36:37] offset:-1024 sc1\n" \
"v_add_f32 v245, v245, v9\n" \
"v_mfma_f32_16x16x32_bf16 v[136:139], a[208:211], v[80:83], v[136:139]\n" \
"v_exp_f32 v10, v10\n" \
"v_mfma_f32_16x16x32_bf16 v[136:139], a[212:215], v[112:115], v[136:139]\n" \
"v_add_f32 v245, v245, v10\n" \
"v_cvt_pk_bf16_f32 v8, v8, v9\n" \
"v_mfma_f32_16x16x32_bf16 v[168:171], a[208:211], v[88:91], v[168:171]\n" \
"v_exp_f32 v11, v11\n" \
"v_mfma_f32_16x16x32_bf16 v[168:171], a[212:215], v[120:123], v[168:171]\n" \
"ds_read_b128 a[208:211], v252 offset:8192\n" \
"v_add_f32 v245, v245, v11\n" \
"v_mfma_f32_16x16x32_bf16 a[76:79], a[216:219], v[64:67], a[76:79]\n" \
"v_exp_f32 v12, v12\n" \
"v_mfma_f32_16x16x32_bf16 a[76:79], a[220:223], v[96:99], a[76:79]\n" \
"v_add_f32 v245, v245, v12\n" \
"v_cvt_pk_bf16_f32 v9, v10, v11\n" \
"v_mfma_f32_16x16x32_bf16 a[108:111], a[216:219], v[72:75], a[108:111]\n" \
"v_exp_f32 v13, v13\n" \
"v_mfma_f32_16x16x32_bf16 a[108:111], a[220:223], v[104:107], a[108:111]\n" \
"ds_read_b128 a[212:215], v252 offset:10240\n" \
"global_load_lds_dwordx4 v250, s[36:37] offset:0 sc1\n" \
"v_add_f32 v245, v245, v13\n" \
"v_mfma_f32_16x16x32_bf16 v[140:143], a[216:219], v[80:83], v[140:143]\n" \
"v_exp_f32 v14, v14\n" \
"v_mfma_f32_16x16x32_bf16 v[140:143], a[220:223], v[112:115], v[140:143]\n" \
"v_add_f32 v245, v245, v14\n" \
"v_cvt_pk_bf16_f32 v10, v12, v13\n" \
"v_mfma_f32_16x16x32_bf16 v[172:175], a[216:219], v[88:91], v[172:175]\n" \
"s_waitcnt lgkmcnt(6)\n" \
"v_exp_f32 v15, v15\n" \
"v_mfma_f32_16x16x32_bf16 v[172:175], a[220:223], v[120:123], v[172:175]\n" \
"ds_read_b128 a[216:219], v252 offset:8448\n" \
"global_load_lds_dwordx4 v250, s[36:37] offset:1024 sc1\n" \
"v_add_f32 v245, v245, v15\n" \
"v_mfma_f32_16x16x32_bf16 a[80:83], a[224:227], v[64:67], a[80:83]\n" \
"v_exp_f32 v16, v16\n" \
"v_mfma_f32_16x16x32_bf16 a[80:83], a[228:231], v[96:99], a[80:83]\n" \
"v_add_f32 v242, v242, v16\n" \
"v_cvt_pk_bf16_f32 v11, v14, v15\n" \
"v_mfma_f32_16x16x32_bf16 a[112:115], a[224:227], v[72:75], a[112:115]\n" \
"v_exp_f32 v17, v17\n" \
"v_mfma_f32_16x16x32_bf16 a[112:115], a[228:231], v[104:107], a[112:115]\n" \
"ds_read_b128 a[220:223], v252 offset:10496\n" \
"v_add_f32 v242, v242, v17\n" \
"v_mfma_f32_16x16x32_bf16 v[144:147], a[224:227], v[80:83], v[144:147]\n" \
"v_exp_f32 v18, v18\n" \
"v_mfma_f32_16x16x32_bf16 v[144:147], a[228:231], v[112:115], v[144:147]\n" \
"v_add_f32 v242, v242, v18\n" \
"v_cvt_pk_bf16_f32 v16, v16, v17\n" \
"v_mfma_f32_16x16x32_bf16 v[176:179], a[224:227], v[88:91], v[176:179]\n" \
"v_exp_f32 v19, v19\n" \
"v_mfma_f32_16x16x32_bf16 v[176:179], a[228:231], v[120:123], v[176:179]\n" \
"ds_read_b128 a[224:227], v252 offset:4096\n" \
"global_load_lds_dwordx4 v250, s[36:37] offset:2048 sc1\n" \
"v_add_f32 v242, v242, v19\n" \
"v_mfma_f32_16x16x32_bf16 a[84:87], a[232:235], v[64:67], a[84:87]\n" \
"v_exp_f32 v20, v20\n" \
"v_mfma_f32_16x16x32_bf16 a[84:87], a[236:239], v[96:99], a[84:87]\n" \
"v_add_f32 v242, v242, v20\n" \
"v_cvt_pk_bf16_f32 v17, v18, v19\n" \
"v_mfma_f32_16x16x32_bf16 a[116:119], a[232:235], v[72:75], a[116:119]\n" \
"v_exp_f32 v21, v21\n" \
"v_mfma_f32_16x16x32_bf16 a[116:119], a[236:239], v[104:107], a[116:119]\n" \
"ds_read_b128 a[228:231], v252 offset:6144\n" \
"global_load_lds_dwordx4 v250, s[36:37] offset:3072 sc1\n" \
"v_add_f32 v242, v242, v21\n" \
"v_mfma_f32_16x16x32_bf16 v[148:151], a[232:235], v[80:83], v[148:151]\n" \
"s_add_u32 s36, s36, 34816\n" \
"v_exp_f32 v22, v22\n" \
"v_mfma_f32_16x16x32_bf16 v[148:151], a[236:239], v[112:115], v[148:151]\n" \
"v_add_f32 v242, v242, v22\n" \
"v_cvt_pk_bf16_f32 v18, v20, v21\n" \
"v_mfma_f32_16x16x32_bf16 v[180:183], a[232:235], v[88:91], v[180:183]\n" \
"v_exp_f32 v23, v23\n" \
"v_mfma_f32_16x16x32_bf16 v[180:183], a[236:239], v[120:123], v[180:183]\n" \
"ds_read_b128 a[232:235], v252 offset:4352\n" \
"v_add_f32 v242, v242, v23\n" \
"v_mfma_f32_16x16x32_bf16 a[88:91], a[240:243], v[64:67], a[88:91]\n" \
"v_exp_f32 v24, v24\n" \
"v_mfma_f32_16x16x32_bf16 a[88:91], a[244:247], v[96:99], a[88:91]\n" \
"v_add_f32 v243, v243, v24\n" \
"v_cvt_pk_bf16_f32 v19, v22, v23\n" \
"v_mfma_f32_16x16x32_bf16 a[120:123], a[240:243], v[72:75], a[120:123]\n" \
"v_exp_f32 v25, v25\n" \
"v_mfma_f32_16x16x32_bf16 a[120:123], a[244:247], v[104:107], a[120:123]\n" \
"ds_read_b128 a[236:239], v252 offset:6400\n" \
"v_add_f32 v243, v243, v25\n" \
"v_mfma_f32_16x16x32_bf16 v[152:155], a[240:243], v[80:83], v[152:155]\n" \
"v_exp_f32 v26, v26\n" \
"v_mfma_f32_16x16x32_bf16 v[152:155], a[244:247], v[112:115], v[152:155]\n" \
"v_add_f32 v243, v243, v26\n" \
"v_cvt_pk_bf16_f32 v24, v24, v25\n" \
"v_mfma_f32_16x16x32_bf16 v[184:187], a[240:243], v[88:91], v[184:187]\n" \
"v_exp_f32 v27, v27\n" \
"v_mfma_f32_16x16x32_bf16 v[184:187], a[244:247], v[120:123], v[184:187]\n" \
"ds_read_b128 a[240:243], v252 offset:12288\n" \
"v_add_f32 v243, v243, v27\n" \
"v_mfma_f32_16x16x32_bf16 a[92:95], a[248:251], v[64:67], a[92:95]\n" \
"v_exp_f32 v28, v28\n" \
"v_mfma_f32_16x16x32_bf16 a[92:95], a[252:255], v[96:99], a[92:95]\n" \
"v_add_f32 v243, v243, v28\n" \
"v_cvt_pk_bf16_f32 v25, v26, v27\n" \
"v_mfma_f32_16x16x32_bf16 a[124:127], a[248:251], v[72:75], a[124:127]\n" \
"v_exp_f32 v29, v29\n" \
"v_mfma_f32_16x16x32_bf16 a[124:127], a[252:255], v[104:107], a[124:127]\n" \
"ds_read_b128 a[244:247], v252 offset:14336\n" \
"v_add_f32 v243, v243, v29\n" \
"v_mfma_f32_16x16x32_bf16 v[156:159], a[248:251], v[80:83], v[156:159]\n" \
"v_exp_f32 v30, v30\n" \
"v_mfma_f32_16x16x32_bf16 v[156:159], a[252:255], v[112:115], v[156:159]\n" \
"v_add_f32 v243, v243, v30\n" \
"v_cvt_pk_bf16_f32 v26, v28, v29\n" \
"v_mfma_f32_16x16x32_bf16 v[188:191], a[248:251], v[88:91], v[188:191]\n" \
"s_waitcnt vmcnt(8) lgkmcnt(6)\n" \
"v_exp_f32 v31, v31\n" \
"v_mfma_f32_16x16x32_bf16 v[188:191], a[252:255], v[120:123], v[188:191]\n" \
"s_barrier\n" \
"ds_read_b128 a[248:251], v252 offset:12544\n" \
"v_add_f32 v243, v243, v31\n" \
"LA1g_%=:\n" \
"Lpg7641_%=:\n" \
"s_cbranch_execz Lcb0_%=\n" \
"v_mfma_f32_16x16x32_bf16 v[64:67], a[192:195], v[192:195], v[224:227]\n" \
"v_exp_f32 v32, v32\n" \
"v_mfma_f32_16x16x32_bf16 v[64:67], a[196:199], v[196:199], v[64:67]\n" \
"v_cvt_pk_bf16_f32 v27, v30, v31\n" \
"v_add_f32 v244, v244, v32\n" \
"v_mfma_f32_16x16x32_bf16 v[72:75], a[192:195], v[208:211], v[228:231]\n" \
"s_add_u32 m0, s74, 0x8000\n" \
"v_exp_f32 v33, v33\n" \
"v_mfma_f32_16x16x32_bf16 v[72:75], a[196:199], v[212:215], v[72:75]\n" \
"ds_read_b128 a[252:255], v252 offset:14592\n" \
"global_load_lds_dwordx4 v250, s[38:39] offset:-4096 sc1\n" \
"v_add_f32 v244, v244, v33\n" \
"v_mfma_f32_16x16x32_bf16 v[80:83], a[192:195], a[160:163], v[232:235]\n" \
"v_exp_f32 v34, v34\n" \
"v_mfma_f32_16x16x32_bf16 v[80:83], a[196:199], a[164:167], v[80:83]\n" \
"v_add_f32 v244, v244, v34\n" \
"v_cvt_pk_bf16_f32 v32, v32, v33\n" \
"v_mfma_f32_16x16x32_bf16 v[88:91], a[192:195], a[176:179], v[236:239]\n" \
"v_exp_f32 v35, v35\n" \
"v_mfma_f32_16x16x32_bf16 v[88:91], a[196:199], a[180:183], v[88:91]\n" \
"ds_read_b128 a[192:195], v247 offset:0\n" \
"global_load_lds_dwordx4 v250, s[38:39] offset:-3072 sc1\n" \
"v_add_f32 v244, v244, v35\n" \
"v_mfma_f32_16x16x32_bf16 v[68:71], a[200:203], v[192:195], v[224:227]\n" \
"v_exp_f32 v36, v36\n" \
"v_mfma_f32_16x16x32_bf16 v[68:71], a[204:207], v[196:199], v[68:71]\n" \
"v_add_f32 v244, v244, v36\n" \
"v_cvt_pk_bf16_f32 v33, v34, v35\n" \
"v_mfma_f32_16x16x32_bf16 v[76:79], a[200:203], v[208:211], v[228:231]\n" \
"v_exp_f32 v37, v37\n" \
"v_mfma_f32_16x16x32_bf16 v[76:79], a[204:207], v[212:215], v[76:79]\n" \
"ds_read_b128 a[196:199], v247 offset:8192\n" \
"v_add_f32 v244, v244, v37\n" \
"v_mfma_f32_16x16x32_bf16 v[84:87], a[200:203], a[160:163], v[232:235]\n" \
"v_exp_f32 v38, v38\n" \
"v_mfma_f32_16x16x32_bf16 v[84:87], a[204:207], a[164:167], v[84:87]\n" \
"v_add_f32 v244, v244, v38\n" \
"v_cvt_pk_bf16_f32 v34, v36, v37\n" \
"v_mfma_f32_16x16x32_bf16 v[92:95], a[200:203], a[176:179], v[236:239]\n" \
"v_exp_f32 v39, v39\n" \
"v_mfma_f32_16x16x32_bf16 v[92:95], a[204:207], a[180:183], v[92:95]\n" \
"ds_read_b128 a[200:203], v247 offset:1024\n" \
"global_load_lds_dwordx4 v250, s[38:39] offset:-2048 sc1\n" \
"v_add_f32 v244, v244, v39\n" \
"v_mfma_f32_16x16x32_bf16 v[96:99], a[208:211], v[192:195], v[224:227]\n" \
"v_exp_f32 v40, v40\n" \
"v_mfma_f32_16x16x32_bf16 v[96:99], a[212:215], v[196:199], v[96:99]\n" \
"v_add_f32 v245, v245, v40\n" \
"v_cvt_pk_bf16_f32 v35, v38, v39\n" \
"v_mfma_f32_16x16x32_bf16 v[104:107], a[208:211], v[208:211], v[228:231]\n" \
"v_exp_f32 v41, v41\n" \
"v_mfma_f32_16x16x32_bf16 v[104:107], a[212:215], v[212:215], v[104:107]\n" \
"ds_read_b128 a[204:207], v247 offset:9216\n" \
"global_load_lds_dwordx4 v250, s[38:39] offset:-1024 sc1\n" \
"v_add_f32 v245, v245, v41\n" \
"v_mfma_f32_16x16x32_bf16 v[112:115], a[208:211], a[160:163], v[232:235]\n" \
"v_exp_f32 v42, v42\n" \
"v_mfma_f32_16x16x32_bf16 v[112:115], a[212:215], a[164:167], v[112:115]\n" \
"v_add_f32 v245, v245, v42\n" \
"v_cvt_pk_bf16_f32 v40, v40, v41\n" \
"v_mfma_f32_16x16x32_bf16 v[120:123], a[208:211], a[176:179], v[236:239]\n" \
"v_exp_f32 v43, v43\n" \
"v_mfma_f32_16x16x32_bf16 v[120:123], a[212:215], a[180:183], v[120:123]\n" \
"ds_read_b128 a[208:211], v247 offset:2048\n" \
"v_add_f32 v245, v245, v43\n" \
"v_mfma_f32_16x16x32_bf16 v[100:103], a[216:219], v[192:195], v[224:227]\n" \
"v_exp_f32 v44, v44\n" \
"v_mfma_f32_16x16x32_bf16 v[100:103], a[220:223], v[196:199], v[100:103]\n" \
"v_add_f32 v245, v245, v44\n" \
"v_cvt_pk_bf16_f32 v41, v42, v43\n" \
"v_mfma_f32_16x16x32_bf16 v[108:111], a[216:219], v[208:211], v[228:231]\n" \
"v_exp_f32 v45, v45\n" \
"v_mfma_f32_16x16x32_bf16 v[108:111], a[220:223], v[212:215], v[108:111]\n" \
"ds_read_b128 a[212:215], v247 offset:10240\n" \
"global_load_lds_dwordx4 v250, s[38:39] offset:0 sc1\n" \
"v_add_f32 v245, v245, v45\n" \
"v_mfma_f32_16x16x32_bf16 v[116:119], a[216:219], a[160:163], v[232:235]\n" \
"v_exp_f32 v46, v46\n" \
"v_mfma_f32_16x16x32_bf16 v[116:119], a[220:223], a[164:167], v[116:119]\n" \
"v_add_f32 v245, v245, v46\n" \
"v_cvt_pk_bf16_f32 v42, v44, v45\n" \
"v_mfma_f32_16x16x32_bf16 v[124:127], a[216:219], a[176:179], v[236:239]\n" \
"s_waitcnt lgkmcnt(6)\n" \
"v_exp_f32 v47, v47\n" \
"v_mfma_f32_16x16x32_bf16 v[124:127], a[220:223], a[180:183], v[124:127]\n" \
"ds_read_b128 a[216:219], v247 offset:3072\n" \
"global_load_lds_dwordx4 v250, s[38:39] offset:1024 sc1\n" \
"v_add_f32 v245, v245, v47\n" \
"v_mfma_f32_16x16x32_bf16 v[64:67], a[224:227], v[200:203], v[64:67]\n" \
"v_exp_f32 v48, v48\n" \
"v_mfma_f32_16x16x32_bf16 v[64:67], a[228:231], v[204:207], v[64:67]\n" \
"v_add_f32 v242, v242, v48\n" \
"v_cvt_pk_bf16_f32 v43, v46, v47\n" \
"v_mfma_f32_16x16x32_bf16 v[72:75], a[224:227], v[216:219], v[72:75]\n" \
"v_exp_f32 v49, v49\n" \
"v_mfma_f32_16x16x32_bf16 v[72:75], a[228:231], v[220:223], v[72:75]\n" \
"ds_read_b128 a[220:223], v247 offset:11264\n" \
"v_add_f32 v242, v242, v49\n" \
"v_exp_f32 v50, v50\n" \
"v_mfma_f32_16x16x32_bf16 v[80:83], a[224:227], a[168:171], v[80:83]\n" \
"v_add_f32 v242, v242, v50\n" \
"v_cvt_pk_bf16_f32 v48, v48, v49\n" \
"v_mfma_f32_16x16x32_bf16 v[80:83], a[228:231], a[172:175], v[80:83]\n" \
"v_exp_f32 v51, v51\n" \
"v_mfma_f32_16x16x32_bf16 v[88:91], a[224:227], a[184:187], v[88:91]\n" \
"v_add_f32 v242, v242, v51\n" \
"v_exp_f32 v52, v52\n" \
"v_mfma_f32_16x16x32_bf16 v[88:91], a[228:231], a[188:191], v[88:91]\n" \
"ds_read_b128 a[224:227], v247 offset:4096\n" \
"global_load_lds_dwordx4 v250, s[38:39] offset:2048 sc1\n" \
"v_mfma_f32_16x16x32_bf16 v[68:71], a[232:235], v[200:203], v[68:71]\n" \
"v_add_f32 v242, v242, v52\n" \
"v_cvt_pk_bf16_f32 v49, v50, v51\n" \
"v_mfma_f32_16x16x32_bf16 v[68:71], a[236:239], v[204:207], v[68:71]\n" \
"v_exp_f32 v53, v53\n" \
"v_mfma_f32_16x16x32_bf16 v[76:79], a[232:235], v[216:219], v[76:79]\n" \
"v_add_f32 v242, v242, v53\n" \
"v_exp_f32 v54, v54\n" \
"v_mfma_f32_16x16x32_bf16 v[76:79], a[236:239], v[220:223], v[76:79]\n" \
"ds_read_b128 a[228:231], v247 offset:12288\n" \
"global_load_lds_dwordx4 v250, s[38:39] offset:3072 sc1\n" \
"v_mfma_f32_16x16x32_bf16 v[84:87], a[232:235], a[168:171], v[84:87]\n" \
"s_add_u32 s38, s38, 34816\n" \
"s_sub_u32 s76, s76, 1\n" \
"s_cmp_lg_u32 s76, 0\n" \
"v_mfma_f32_16x16x32_bf16 v[84:87], a[236:239], a[172:175], v[84:87]\n" \
"v_add_f32 v242, v242, v54\n" \
"v_cvt_pk_bf16_f32 v50, v52, v53\n" \
"v_mfma_f32_16x16x32_bf16 v[92:95], a[232:235], a[184:187], v[92:95]\n" \
"v_exp_f32 v55, v55\n" \
"v_mfma_f32_16x16x32_bf16 v[92:95], a[236:239], a[188:191], v[92:95]\n" \
"ds_read_b128 a[232:235], v247 offset:5120\n" \
"v_add_f32 v242, v242, v55\n" \
"v_mfma_f32_16x16x32_bf16 v[96:99], a[240:243], v[200:203], v[96:99]\n" \
"v_exp_f32 v56, v56\n" \
"v_mfma_f32_16x16x32_bf16 v[96:99], a[244:247], v[204:207], v[96:99]\n" \
"v_add_f32 v243, v243, v56\n" \
"v_cvt_pk_bf16_f32 v51, v54, v55\n" \
"v_mfma_f32_16x16x32_bf16 v[104:107], a[240:243], v[216:219], v[104:107]\n" \
"v_exp_f32 v57, v57\n" \
"v_mfma_f32_16x16x32_bf16 v[104:107], a[244:247], v[220:223], v[104:107]\n" \
"ds_read_b128 a[236:239], v247 offset:13312\n" \
"v_add_f32 v243, v243, v57\n" \
"v_mfma_f32_16x16x32_bf16 v[112:115], a[240:243], a[168:171], v[112:115]\n" \
"v_exp_f32 v58, v58\n" \
"v_mfma_f32_16x16x32_bf16 v[112:115], a[244:247], a[172:175], v[112:115]\n" \
"v_add_f32 v243, v243, v58\n" \
"v_cvt_pk_bf16_f32 v56, v56, v57\n" \
"v_mfma_f32_16x16x32_bf16 v[120:123], a[240:243], a[184:187], v[120:123]\n" \
"v_exp_f32 v59, v59\n" \
"v_mfma_f32_16x16x32_bf16 v[120:123], a[244:247], a[188:191], v[120:123]\n" \
"ds_read_b128 a[240:243], v247 offset:6144\n" \
"v_add_f32 v243, v243, v59\n" \
"v_mfma_f32_16x16x32_bf16 v[100:103], a[248:251], v[200:203], v[100:103]\n" \
"v_exp_f32 v60, v60\n" \
"v_mfma_f32_16x16x32_bf16 v[100:103], a[252:255], v[204:207], v[100:103]\n" \
"v_add_f32 v243, v243, v60\n" \
"v_cvt_pk_bf16_f32 v57, v58, v59\n" \
"v_mfma_f32_16x16x32_bf16 v[108:111], a[248:251], v[216:219], v[108:111]\n" \
"v_exp_f32 v61, v61\n" \
"v_mfma_f32_16x16x32_bf16 v[108:111], a[252:255], v[220:223], v[108:111]\n" \
"ds_read_b128 a[244:247], v247 offset:14336\n" \
"v_add_f32 v243, v243, v61\n" \
"v_mfma_f32_16x16x32_bf16 v[116:119], a[248:251], a[168:171], v[116:119]\n" \
"v_exp_f32 v62, v62\n" \
"v_mfma_f32_16x16x32_bf16 v[116:119], a[252:255], a[172:175], v[116:119]\n" \
"v_add_f32 v243, v243, v62\n" \
"v_cvt_pk_bf16_f32 v58, v60, v61\n" \
"v_mfma_f32_16x16x32_bf16 v[124:127], a[248:251], a[184:187], v[124:127]\n" \
"s_waitcnt vmcnt(8) lgkmcnt(6)\n" \
"v_exp_f32 v63, v63\n" \
"v_mfma_f32_16x16x32_bf16 v[124:127], a[252:255], a[188:191], v[124:127]\n" \
"s_barrier\n" \
"ds_read_b128 a[248:251], v247 offset:7168\n" \
"v_add_f32 v243, v243, v63\n" \
"s_cbranch_scc1 Lloop2_%=\n" \
"v_mfma_f32_16x16x32_bf16 a[64:67], a[192:195], v[0:3], a[64:67]\n" \
"v_subrev_u32 v246, s82, v246\n" \
"v_exp_f32 v64, v64\n" \
"v_mfma_f32_16x16x32_bf16 a[64:67], a[196:199], v[32:35], a[64:67]\n" \
"s_memrealtime s[44:45]\n" \
"v_cvt_pk_bf16_f32 v59, v62, v63\n" \
"v_add_f32 v244, v244, v64\n" \
"v_exp_f32 v65, v65\n" \
"v_mfma_f32_16x16x32_bf16 a[96:99], a[192:195], v[8:11], a[96:99]\n" \
"v_mov_b32 v208, v241\n" \
"v_add_f32 v244, v244, v65\n" \
"v_exp_f32 v66, v66\n" \
"v_mfma_f32_16x16x32_bf16 a[96:99], a[196:199], v[40:43], a[96:99]\n" \
"ds_read_b128 a[252:255], v247 offset:15360\n" \
"v_add_f32 v244, v244, v66\n" \
"v_cvt_pk_bf16_f32 v64, v64, v65\n" \
"v_exp_f32 v67, v67\n" \
"v_mfma_f32_16x16x32_bf16 v[128:131], a[192:195], v[16:19], v[128:131]\n" \
"v_add_f32 v244, v244, v67\n" \
"v_exp_f32 v68, v68\n" \
"v_permlane16_swap_b32 v208, v241\n" \
"v_add_f32 v241, v241, v208\n" \
"v_mov_b32 v208, v241\n" \
"v_mfma_f32_16x16x32_bf16 v[128:131], a[196:199], v[48:51], v[128:131]\n" \
"v_add_f32 v244, v244, v68\n" \
"v_cvt_pk_bf16_f32 v65, v66, v67\n" \
"v_exp_f32 v69, v69\n" \
"v_mfma_f32_16x16x32_bf16 v[160:163], a[192:195], v[24:27], v[160:163]\n" \
"v_add_f32 v244, v244, v69\n" \
"v_exp_f32 v70, v70\n" \
"v_permlane32_swap_b32 v208, v241\n" \
"v_add_f32 v241, v241, v208\n" \
"v_rcp_f32 v208, v241\n" \
"v_mul_f32 v241, 0x4d800000, v241\n" \
"v_add_u32 v209, 0x1c90080, v249\n" \
"v_cndmask_b32 v209, v249, v209, s[16:17]\n" \
"v_mfma_f32_16x16x32_bf16 v[160:163], a[196:199], v[56:59], v[160:163]\n" \
"ds_read_b128 a[192:195], v246 offset:0\n" \
"v_add_f32 v244, v244, v70\n" \
"v_cvt_pk_bf16_f32 v66, v68, v69\n" \
"v_accvgpr_read_b32 v192, a32\n" \
"v_accvgpr_read_b32 v193, a33\n" \
"v_accvgpr_read_b32 v194, a34\n" \
"v_accvgpr_read_b32 v195, a35\n" \
"v_accvgpr_read_b32 v196, a36\n" \
"v_accvgpr_read_b32 v197, a37\n" \
"v_mfma_f32_16x16x32_bf16 a[68:71], a[200:203], v[0:3], a[68:71]\n" \
"v_accvgpr_read_b32 v198, a38\n" \
"v_accvgpr_read_b32 v199, a39\n" \
"v_mul_f32 v192, v192, v208\n" \
"v_mul_f32 v193, v193, v208\n" \
"v_mul_f32 v194, v194, v208\n" \
"v_mul_f32 v195, v195, v208\n" \
"v_exp_f32 v71, v71\n" \
"v_mfma_f32_16x16x32_bf16 a[68:71], a[204:207], v[32:35], a[68:71]\n" \
"s_waitcnt lgkmcnt(0)\n" \
"v_mul_f32 v196, v196, v208\n" \
"v_mul_f32 v197, v197, v208\n" \
"v_mul_f32 v198, v198, v208\n" \
"v_mul_f32 v199, v199, v208\n" \
"v_fma_f32 v241, v192, v193, v241\n" \
"v_cvt_pk_bf16_f32 v200, v192, v193\n" \
"v_add_f32 v244, v244, v71\n" \
"v_exp_f32 v72, v72\n" \
"v_mfma_f32_16x16x32_bf16 a[100:103], a[200:203], v[8:11], a[100:103]\n" \
"s_barrier\n" \
"v_add_f32 v245, v245, v72\n" \
"v_cvt_pk_bf16_f32 v67, v70, v71\n" \
"v_fma_f32 v241, v194, v195, v241\n" \
"v_cvt_pk_bf16_f32 v201, v194, v195\n" \
"v_fma_f32 v241, v196, v197, v241\n" \
"v_cvt_pk_bf16_f32 v202, v196, v197\n" \
"v_fma_f32 v241, v198, v199, v241\n" \
"v_cvt_pk_bf16_f32 v203, v198, v199\n" \
"v_mfma_f32_16x16x32_bf16 a[100:103], a[204:207], v[40:43], a[100:103]\n" \
"s_and_b32 s46, %1, 7\n" \
"ds_read_b128 a[196:199], v246 offset:2048\n" \
"s_lshl_b32 s46, s46, 2\n" \
"v_mfma_f32_16x16x32_bf16 v[132:135], a[200:203], v[16:19], v[132:135]\n" \
"s_lshr_b32 s47, %1, 6\n" \
"s_add_u32 s46, s46, s47\n" \
"s_lshl_b32 s46, s46, 3\n" \
"v_mfma_f32_16x16x32_bf16 v[132:135], a[204:207], v[48:51], v[132:135]\n" \
"s_add_u32 s46, s46, 65593856\n" \
"s_sub_u32 s45, s44, s12\n" \
"s_mul_i32 s45, s45, 3\n" \
"v_mfma_f32_16x16x32_bf16 v[164:167], a[200:203], v[24:27], v[164:167]\n" \
"s_lshl_b32 s44, s44, 1\n" \
"s_add_u32 s44, s44, s45\n" \
"v_mov_b32 v211, s44\n" \
"v_mfma_f32_16x16x32_bf16 v[164:167], a[204:207], v[56:59], v[164:167]\n" \
"s_mov_b32 m0, s74\n" \
"ds_read_b128 a[200:203], v246 offset:256\n" \
"v_xor_b32 v210, s60, v211\n" \
"v_mfma_f32_16x16x32_bf16 a[72:75], a[208:211], v[0:3], a[72:75]\n" \
"global_load_lds_dwordx4 v250, s[38:39] offset:-4096 sc1\n" \
"v_lshlrev_b32 v212, 28, s61\n" \
"v_mfma_f32_16x16x32_bf16 a[72:75], a[212:215], v[32:35], a[72:75]\n" \
"s_cmp_eq_u32 s63, 3\n" \
"s_cselect_b32 s46, s46, 0x20000000\n" \
"v_exp_f32 v73, v73\n" \
"v_mfma_f32_16x16x32_bf16 a[104:107], a[208:211], v[8:11], a[104:107]\n" \
"global_load_lds_dwordx4 v250, s[38:39] offset:-3072 sc1\n" \
"global_load_lds_dwordx4 v250, s[38:39] offset:-2048 sc1\n" \
"v_mfma_f32_16x16x32_bf16 a[104:107], a[212:215], v[40:43], a[104:107]\n" \
"ds_read_b128 a[204:207], v246 offset:2304\n" \
"v_add_f32 v245, v245, v73\n" \
"v_mfma_f32_16x16x32_bf16 v[136:139], a[208:211], v[16:19], v[136:139]\n" \
"v_exp_f32 v74, v74\n" \
"v_mfma_f32_16x16x32_bf16 v[136:139], a[212:215], v[48:51], v[136:139]\n" \
"global_load_lds_dwordx4 v250, s[38:39] offset:-1024 sc1\n" \
"v_add_f32 v245, v245, v74\n" \
"v_cvt_pk_bf16_f32 v72, v72, v73\n" \
"v_mfma_f32_16x16x32_bf16 v[168:171], a[208:211], v[24:27], v[168:171]\n" \
"v_exp_f32 v75, v75\n" \
"v_mfma_f32_16x16x32_bf16 v[168:171], a[212:215], v[56:59], v[168:171]\n" \
"ds_read_b128 a[208:211], v246 offset:8192\n" \
"v_add_f32 v245, v245, v75\n" \
"v_mfma_f32_16x16x32_bf16 a[76:79], a[216:219], v[0:3], a[76:79]\n" \
"global_load_lds_dwordx4 v250, s[38:39] offset:0 sc1\n" \
"v_exp_f32 v76, v76\n" \
"v_mfma_f32_16x16x32_bf16 a[76:79], a[220:223], v[32:35], a[76:79]\n" \
"v_add_f32 v245, v245, v76\n" \
"v_cvt_pk_bf16_f32 v73, v74, v75\n" \
"v_mfma_f32_16x16x32_bf16 a[108:111], a[216:219], v[8:11], a[108:111]\n" \
"v_exp_f32 v77, v77\n" \
"v_mfma_f32_16x16x32_bf16 a[108:111], a[220:223], v[40:43], a[108:111]\n" \
"ds_read_b128 a[212:215], v246 offset:10240\n" \
"global_load_lds_dwordx4 v250, s[38:39] offset:1024 sc1\n" \
"v_add_f32 v245, v245, v77\n" \
"v_mfma_f32_16x16x32_bf16 v[140:143], a[216:219], v[16:19], v[140:143]\n" \
"v_exp_f32 v78, v78\n" \
"v_mfma_f32_16x16x32_bf16 v[140:143], a[220:223], v[48:51], v[140:143]\n" \
"v_add_f32 v245, v245, v78\n" \
"v_cvt_pk_bf16_f32 v74, v76, v77\n" \
"v_mfma_f32_16x16x32_bf16 v[172:175], a[216:219], v[24:27], v[172:175]\n" \
"s_waitcnt lgkmcnt(6)\n" \
"global_load_lds_dwordx4 v250, s[38:39] offset:2048 sc1\n" \
"v_exp_f32 v79, v79\n" \
"v_mfma_f32_16x16x32_bf16 v[172:175], a[220:223], v[56:59], v[172:175]\n" \
"ds_read_b128 a[216:219], v246 offset:8448\n" \
"v_add_f32 v245, v245, v79\n" \
"v_mfma_f32_16x16x32_bf16 a[80:83], a[224:227], v[0:3], a[80:83]\n" \
"v_exp_f32 v80, v80\n" \
"v_mfma_f32_16x16x32_bf16 a[80:83], a[228:231], v[32:35], a[80:83]\n" \
"global_load_lds_dwordx4 v250, s[38:39] offset:3072 sc1\n" \
"v_add_f32 v242, v242, v80\n" \
"v_cvt_pk_bf16_f32 v75, v78, v79\n" \
"v_mfma_f32_16x16x32_bf16 a[112:115], a[224:227], v[8:11], a[112:115]\n" \
"v_exp_f32 v81, v81\n" \
"v_mfma_f32_16x16x32_bf16 a[112:115], a[228:231], v[40:43], a[112:115]\n" \
"ds_read_b128 a[220:223], v246 offset:10496\n" \
"v_add_f32 v242, v242, v81\n" \
"v_mfma_f32_16x16x32_bf16 v[144:147], a[224:227], v[16:19], v[144:147]\n" \
"v_exp_f32 v82, v82\n" \
"v_mfma_f32_16x16x32_bf16 v[144:147], a[228:231], v[48:51], v[144:147]\n" \
"v_add_f32 v242, v242, v82\n" \
"v_cvt_pk_bf16_f32 v80, v80, v81\n" \
"v_mfma_f32_16x16x32_bf16 v[176:179], a[224:227], v[24:27], v[176:179]\n" \
"v_exp_f32 v83, v83\n" \
"v_mfma_f32_16x16x32_bf16 v[176:179], a[228:231], v[56:59], v[176:179]\n" \
"ds_read_b128 a[224:227], v246 offset:4096\n" \
"v_add_f32 v242, v242, v83\n" \
"v_exp_f32 v84, v84\n" \
"v_mfma_f32_16x16x32_bf16 a[84:87], a[232:235], v[0:3], a[84:87]\n" \
"buffer_store_dwordx4 v[200:203], v209, s[56:59], s77 offen offset:0 sc1\n" \
"v_add_f32 v242, v242, v84\n" \
"v_cvt_pk_bf16_f32 v81, v82, v83\n" \
"v_accvgpr_read_b32 v192, a40\n" \
"v_accvgpr_read_b32 v193, a41\n" \
"v_accvgpr_read_b32 v194, a42\n" \
"v_accvgpr_read_b32 v195, a43\n" \
"v_accvgpr_read_b32 v196, a44\n" \
"v_mfma_f32_16x16x32_bf16 a[84:87], a[236:239], v[32:35], a[84:87]\n" \
"buffer_store_dwordx2 v[210:211], v212, s[56:59], s46 offen sc1\n" \
"v_accvgpr_read_b32 v197, a45\n" \
"v_accvgpr_read_b32 v198, a46\n" \
"v_accvgpr_read_b32 v199, a47\n" \
"v_mul_f32 v192, v192, v208\n" \
"v_mul_f32 v193, v193, v208\n" \
"v_mul_f32 v194, v194, v208\n" \
"v_mfma_f32_16x16x32_bf16 a[116:119], a[232:235], v[8:11], a[116:119]\n" \
"v_mul_f32 v195, v195, v208\n" \
"v_mul_f32 v196, v196, v208\n" \
"v_mul_f32 v197, v197, v208\n" \
"v_mul_f32 v198, v198, v208\n" \
"v_mul_f32 v199, v199, v208\n" \
"v_fma_f32 v241, v192, v193, v241\n" \
"v_exp_f32 v85, v85\n" \
"v_mfma_f32_16x16x32_bf16 a[116:119], a[236:239], v[40:43], a[116:119]\n" \
"ds_read_b128 a[228:231], v246 offset:6144\n" \
"v_add_f32 v242, v242, v85\n" \
"v_cvt_pk_bf16_f32 v204, v192, v193\n" \
"v_fma_f32 v241, v194, v195, v241\n" \
"v_cvt_pk_bf16_f32 v205, v194, v195\n" \
"v_fma_f32 v241, v196, v197, v241\n" \
"v_cvt_pk_bf16_f32 v206, v196, v197\n" \
"v_fma_f32 v241, v198, v199, v241\n" \
"v_mfma_f32_16x16x32_bf16 v[148:151], a[232:235], v[16:19], v[148:151]\n" \
"v_cvt_pk_bf16_f32 v207, v198, v199\n" \
"v_exp_f32 v86, v86\n" \
"v_mfma_f32_16x16x32_bf16 v[148:151], a[236:239], v[48:51], v[148:151]\n" \
"buffer_store_dwordx4 v[204:207], v209, s[56:59], s77 offen offset:2048 sc1\n" \
"s_add_u32 s77, s77, 0x1000\n" \
"v_add_f32 v242, v242, v86\n" \
"v_cvt_pk_bf16_f32 v82, v84, v85\n" \
"v_accvgpr_read_b32 v192, a48\n" \
"v_accvgpr_read_b32 v193, a49\n" \
"v_accvgpr_read_b32 v194, a50\n" \
"v_accvgpr_read_b32 v195, a51\n" \
"v_mfma_f32_16x16x32_bf16 v[180:183], a[232:235], v[24:27], v[180:183]\n" \
"v_accvgpr_read_b32 v196, a52\n" \
"v_accvgpr_read_b32 v197, a53\n" \
"v_accvgpr_read_b32 v198, a54\n" \
"v_accvgpr_read_b32 v199, a55\n" \
"v_mul_f32 v192, v192, v208\n" \
"v_mul_f32 v193, v193, v208\n" \
"v_exp_f32 v87, v87\n" \
"v_mfma_f32_16x16x32_bf16 v[180:183], a[236:239], v[56:59], v[180:183]\n" \
"ds_read_b128 a[232:235], v246 offset:4352\n" \
"v_add_f32 v242, v242, v87\n" \
"v_mul_f32 v194, v194, v208\n" \
"v_mul_f32 v195, v195, v208\n" \
"v_mul_f32 v196, v196, v208\n" \
"v_mul_f32 v197, v197, v208\n" \
"v_mul_f32 v198, v198, v208\n" \
"v_mul_f32 v199, v199, v208\n" \
"v_mfma_f32_16x16x32_bf16 a[88:91], a[240:243], v[0:3], a[88:91]\n" \
"v_fma_f32 v241, v192, v193, v241\n" \
"v_cvt_pk_bf16_f32 v200, v192, v193\n" \
"v_fma_f32 v241, v194, v195, v241\n" \
"v_cvt_pk_bf16_f32 v201, v194, v195\n" \
"v_fma_f32 v241, v196, v197, v241\n" \
"v_cvt_pk_bf16_f32 v202, v196, v197\n" \
"v_exp_f32 v88, v88\n" \
"v_mfma_f32_16x16x32_bf16 a[88:91], a[244:247], v[32:35], a[88:91]\n" \
"v_add_f32 v243, v243, v88\n" \
"v_cvt_pk_bf16_f32 v83, v86, v87\n" \
"v_fma_f32 v241, v198, v199, v241\n" \
"v_cvt_pk_bf16_f32 v203, v198, v199\n" \
"v_mfma_f32_16x16x32_bf16 a[120:123], a[240:243], v[8:11], a[120:123]\n" \
"buffer_store_dwordx4 v[200:203], v209, s[56:59], s77 offen offset:0 sc1\n" \
"v_exp_f32 v89, v89\n" \
"v_accvgpr_read_b32 v192, a56\n" \
"v_accvgpr_read_b32 v193, a57\n" \
"v_accvgpr_read_b32 v194, a58\n" \
"v_accvgpr_read_b32 v195, a59\n" \
"v_accvgpr_read_b32 v196, a60\n" \
"v_mfma_f32_16x16x32_bf16 a[120:123], a[244:247], v[40:43], a[120:123]\n" \
"ds_read_b128 a[236:239], v246 offset:6400\n" \
"v_add_f32 v243, v243, v89\n" \
"v_accvgpr_read_b32 v197, a61\n" \
"v_accvgpr_read_b32 v198, a62\n" \
"v_accvgpr_read_b32 v199, a63\n" \
"v_mul_f32 v192, v192, v208\n" \
"v_mul_f32 v193, v193, v208\n" \
"v_mul_f32 v194, v194, v208\n" \
"v_mfma_f32_16x16x32_bf16 v[152:155], a[240:243], v[16:19], v[152:155]\n" \
"v_mul_f32 v195, v195, v208\n" \
"v_mul_f32 v196, v196, v208\n" \
"v_mul_f32 v197, v197, v208\n" \
"v_mul_f32 v198, v198, v208\n" \
"v_mul_f32 v199, v199, v208\n" \
"v_fma_f32 v241, v192, v193, v241\n" \
"v_exp_f32 v90, v90\n" \
"v_mfma_f32_16x16x32_bf16 v[152:155], a[244:247], v[48:51], v[152:155]\n" \
"v_add_f32 v243, v243, v90\n" \
"v_cvt_pk_bf16_f32 v88, v88, v89\n" \
"v_cvt_pk_bf16_f32 v204, v192, v193\n" \
"v_fma_f32 v241, v194, v195, v241\n" \
"v_cvt_pk_bf16_f32 v205, v194, v195\n" \
"v_fma_f32 v241, v196, v197, v241\n" \
"v_cvt_pk_bf16_f32 v206, v196, v197\n" \
"v_fma_f32 v241, v198, v199, v241\n" \
"v_mfma_f32_16x16x32_bf16 v[184:187], a[240:243], v[24:27], v[184:187]\n" \
"v_cvt_pk_bf16_f32 v207, v198, v199\n" \
"v_exp_f32 v91, v91\n" \
"v_mfma_f32_16x16x32_bf16 v[184:187], a[244:247], v[56:59], v[184:187]\n" \
"buffer_store_dwordx4 v[204:207], v209, s[56:59], s77 offen offset:2048 sc1\n" \
"v_add_f32 v243, v243, v91\n" \
"s_add_u32 s77, s77, 0xfff7d000\n" \
"ds_read_b128 a[240:243], v246 offset:12288\n" \
"Lpg8100_%=:\n" \
"Lst1x1_%=:\n" \
"s_cbranch_execz Lpg10467_%=\n" \
"v_mfma_f32_16x16x32_bf16 a[92:95], a[248:251], v[0:3], a[92:95]\n" \
"v_exp_f32 v92, v92\n" \
"v_mfma_f32_16x16x32_bf16 a[92:95], a[252:255], v[32:35], a[92:95]\n" \
"v_add_f32 v243, v243, v92\n" \
"v_cvt_pk_bf16_f32 v89, v90, v91\n" \
"v_mfma_f32_16x16x32_bf16 a[124:127], a[248:251], v[8:11], a[124:127]\n" \
"v_exp_f32 v93, v93\n" \
"v_mfma_f32_16x16x32_bf16 a[124:127], a[252:255], v[40:43], a[124:127]\n" \
"ds_read_b128 a[244:247], v246 offset:14336\n" \
"v_add_f32 v243, v243, v93\n" \
"v_mfma_f32_16x16x32_bf16 v[156:159], a[248:251], v[16:19], v[156:159]\n" \
"v_exp_f32 v94, v94\n" \
"v_mfma_f32_16x16x32_bf16 v[156:159], a[252:255], v[48:51], v[156:159]\n" \
"v_add_f32 v243, v243, v94\n" \
"v_cvt_pk_bf16_f32 v90, v92, v93\n" \
"v_mfma_f32_16x16x32_bf16 v[188:191], a[248:251], v[24:27], v[188:191]\n" \
"s_waitcnt vmcnt(13) lgkmcnt(6)\n" \
"v_exp_f32 v95, v95\n" \
"v_mfma_f32_16x16x32_bf16 v[188:191], a[252:255], v[56:59], v[188:191]\n" \
"s_barrier\n" \
"ds_read_b128 a[248:251], v246 offset:12544\n" \
"v_add_f32 v243, v243, v95\n" \
"v_mfma_f32_16x16x32_bf16 v[16:19], a[192:195], a[160:163], v[232:235]\n" \
"s_bfe_u32 s88, %1, 0x20001\n" \
"ds_read_b128 v[192:195], v246 offset:16384\n" \
"v_exp_f32 v96, v96\n" \
"s_lshl_b32 s88, s88, 2\n" \
"v_mfma_f32_16x16x32_bf16 v[16:19], a[196:199], a[164:167], v[16:19]\n" \
"s_lshr_b32 s70, %1, 6\n" \
"ds_read_b128 v[196:199], v246 offset:18432\n" \
"v_cvt_pk_bf16_f32 v91, v94, v95\n" \
"v_add_f32 v244, v244, v96\n" \
"v_exp_f32 v97, v97\n" \
"v_mfma_f32_16x16x32_bf16 v[24:27], a[192:195], a[176:179], v[236:239]\n" \
"s_add_u32 s88, s88, s70\n" \
"ds_read_b128 v[200:203], v246 offset:16640\n" \
"v_add_f32 v244, v244, v97\n" \
"v_exp_f32 v98, v98\n" \
"v_bfe_u32 v69, v248, 8, 1\n" \
"v_bfe_u32 v70, v248, 6, 2\n" \
"s_lshr_b32 s71, s22, 7\n" \
"v_mfma_f32_16x16x32_bf16 v[24:27], a[196:199], a[180:183], v[24:27]\n" \
"s_sub_u32 s71, s63, s71\n" \
"ds_read_b128 a[252:255], v246 offset:14592\n" \
"v_add_f32 v244, v244, v98\n" \
"v_cvt_pk_bf16_f32 v96, v96, v97\n" \
"v_exp_f32 v99, v99\n" \
"s_add_u32 s71, s71, 6\n" \
"v_mfma_f32_16x16x32_bf16 v[20:23], a[200:203], a[160:163], v[232:235]\n" \
"s_and_b32 s71, s71, 7\n" \
"ds_read_b128 v[204:207], v246 offset:18688\n" \
"v_add_f32 v244, v244, v99\n" \
"v_exp_f32 v100, v100\n" \
"v_lshl_add_u32 v70, v69, 3, v70\n" \
"v_mul_u32_u24 v70, 6144, v70\n" \
"v_mfma_f32_16x16x32_bf16 v[20:23], a[204:207], a[164:167], v[20:23]\n" \
"s_and_b32 s70, %1, 1\n" \
"ds_read_b128 v[208:211], v246 offset:24576\n" \
"v_add_f32 v244, v244, v100\n" \
"v_cvt_pk_bf16_f32 v97, v98, v99\n" \
"v_exp_f32 v101, v101\n" \
"s_lshl_b32 s70, s70, 3\n" \
"v_mfma_f32_16x16x32_bf16 v[28:31], a[200:203], a[176:179], v[236:239]\n" \
"s_add_u32 s70, s70, s71\n" \
"ds_read_b128 v[212:215], v246 offset:26624\n" \
"v_add_f32 v244, v244, v101\n" \
"v_exp_f32 v102, v102\n" \
"v_lshlrev_b32 v69, 5, v69\n" \
"v_and_b32 v71, 0x30, v248\n" \
"s_lshl_b32 s87, s70, 12\n" \
"v_mfma_f32_16x16x32_bf16 v[28:31], a[204:207], a[180:183], v[28:31]\n" \
"s_lshl_b32 s70, s70, 7\n" \
"ds_read_b128 v[216:219], v246 offset:24832\n" \
"v_add_f32 v244, v244, v102\n" \
"v_cvt_pk_bf16_f32 v98, v100, v101\n" \
"v_exp_f32 v103, v103\n" \
"s_mul_i32 s84, s61, 6144\n" \
"v_mfma_f32_16x16x32_bf16 v[48:51], a[208:211], a[160:163], v[232:235]\n" \
"s_lshl_b32 s71, s84, 4\n" \
"ds_read_b128 v[220:223], v246 offset:26880\n" \
"v_add_f32 v244, v244, v103\n" \
"v_exp_f32 v104, v104\n" \
"v_xor_b32 v69, v69, v71\n" \
"v_mfma_f32_16x16x32_bf16 v[48:51], a[212:215], a[164:167], v[48:51]\n" \
"ds_read_b128 a[192:195], v247 offset:32768\n" \
"v_lshrrev_b32 v71, 9, v248\n" \
"v_add_f32 v245, v245, v104\n" \
"v_cvt_pk_bf16_f32 v99, v102, v103\n" \
"v_exp_f32 v105, v105\n" \
"v_mfma_f32_16x16x32_bf16 v[56:59], a[208:211], a[176:179], v[236:239]\n" \
"s_add_u32 s70, s70, s71\n" \
"ds_read_b128 a[196:199], v247 offset:40960\n" \
"v_add_f32 v245, v245, v105\n" \
"v_exp_f32 v106, v106\n" \
"s_add_u32 s70, s70, 4096\n" \
"v_mfma_f32_16x16x32_bf16 v[56:59], a[212:215], a[180:183], v[56:59]\n" \
"s_waitcnt lgkmcnt(4)\n" \
"ds_read_b128 a[200:203], v247 offset:33792\n" \
"v_lshl_add_u32 v70, v71, 6, v70\n" \
"v_add_f32 v245, v245, v106\n" \
"v_cvt_pk_bf16_f32 v104, v104, v105\n" \
"v_exp_f32 v107, v107\n" \
"v_mfma_f32_16x16x32_bf16 v[8:11], v[192:195], a[176:179], v[236:239]\n" \
"ds_read_b128 a[204:207], v247 offset:41984\n" \
"v_add_u32 v253, v69, v70\n" \
"v_add_f32 v245, v245, v107\n" \
"v_exp_f32 v108, v108\n" \
"v_mfma_f32_16x16x32_bf16 v[8:11], v[196:199], a[180:183], v[8:11]\n" \
"s_add_u32 s48, s22, s70\n" \
"s_addc_u32 s49, s23, 0\n" \
"ds_read_b128 a[208:211], v247 offset:34816\n" \
"v_add_f32 v245, v245, v108\n" \
"v_cvt_pk_bf16_f32 v105, v106, v107\n" \
"v_exp_f32 v109, v109\n" \
"v_mfma_f32_16x16x32_bf16 v[52:55], a[216:219], a[160:163], v[232:235]\n" \
"s_add_u32 s50, s48, 23552\n" \
"s_addc_u32 s51, s49, 0\n" \
"ds_read_b128 a[212:215], v247 offset:43008\n" \
"v_add_f32 v245, v245, v109\n" \
"v_exp_f32 v110, v110\n" \
"v_mfma_f32_16x16x32_bf16 v[52:55], a[220:223], a[164:167], v[52:55]\n" \
"s_lshr_b32 s89, s22, 7\n" \
"ds_read_b128 v[192:195], v246 offset:20480\n" \
"v_add_f32 v245, v245, v110\n" \
"v_cvt_pk_bf16_f32 v106, v108, v109\n" \
"v_exp_f32 v111, v111\n" \
"s_sub_u32 s89, 3, s89\n" \
"v_mfma_f32_16x16x32_bf16 v[60:63], a[216:219], a[176:179], v[236:239]\n" \
"ds_read_b128 v[196:199], v246 offset:22528\n" \
"v_bfe_u32 v84, v248, 7, 2\n" \
"v_add_f32 v245, v245, v111\n" \
"v_exp_f32 v112, v112\n" \
"v_mfma_f32_16x16x32_bf16 v[60:63], a[220:223], a[180:183], v[60:63]\n" \
"s_and_b32 s89, s89, 7\n" \
"ds_read_b128 a[216:219], v247 offset:35840\n" \
"v_add_f32 v242, v242, v112\n" \
"v_cvt_pk_bf16_f32 v107, v110, v111\n" \
"v_exp_f32 v113, v113\n" \
"s_and_b32 s90, %1, 1\n" \
"v_mfma_f32_16x16x32_bf16 v[12:15], v[200:203], a[176:179], v[236:239]\n" \
"ds_read_b128 a[220:223], v247 offset:44032\n" \
"v_bfe_u32 v85, v248, 5, 2\n" \
"v_add_f32 v242, v242, v113\n" \
"v_exp_f32 v114, v114\n" \
"v_mfma_f32_16x16x32_bf16 v[12:15], v[204:207], a[180:183], v[12:15]\n" \
"s_lshl_b32 s90, s90, 3\n" \
"ds_read_b128 v[200:203], v246 offset:20736\n" \
"v_add_f32 v242, v242, v114\n" \
"v_cvt_pk_bf16_f32 v112, v112, v113\n" \
"v_exp_f32 v115, v115\n" \
"s_add_u32 s89, s89, s90\n" \
"v_mfma_f32_16x16x32_bf16 v[16:19], a[224:227], a[168:171], v[16:19]\n" \
"ds_read_b128 v[204:207], v246 offset:22784\n" \
"v_min_u32 v85, 2, v85\n" \
"v_add_f32 v242, v242, v115\n" \
"v_exp_f32 v116, v116\n" \
"v_mfma_f32_16x16x32_bf16 v[16:19], a[228:231], a[172:175], v[16:19]\n" \
"s_lshl_b32 s89, s89, 7\n" \
"v_add_f32 v242, v242, v116\n" \
"v_cvt_pk_bf16_f32 v113, v114, v115\n" \
"v_exp_f32 v117, v117\n" \
"s_lshr_b32 s90, %1, 3\n" \
"v_mad_u32_u24 v84, v84, 3, v85\n" \
"v_mfma_f32_16x16x32_bf16 v[24:27], a[224:227], a[184:187], v[24:27]\n" \
"s_lshl_b32 s90, s90, 2\n" \
"v_add_f32 v242, v242, v117\n" \
"v_exp_f32 v118, v118\n" \
"s_add_u32 s90, s90, s61\n" \
"v_and_b32 v85, 16, v248\n" \
"v_mfma_f32_16x16x32_bf16 v[24:27], a[228:231], a[188:191], v[24:27]\n" \
"s_mul_i32 s90, s90, 24576\n" \
"ds_read_b128 a[224:227], v247 offset:36864\n" \
"v_add_f32 v242, v242, v118\n" \
"v_cvt_pk_bf16_f32 v114, v116, v117\n" \
"s_add_u32 s89, s89, s90\n" \
"v_mfma_f32_16x16x32_bf16 v[40:43], v[208:211], a[176:179], v[236:239]\n" \
"ds_read_b128 a[228:231], v247 offset:45056\n" \
"v_lshl_add_u32 v85, v85, 2, s89\n" \
"v_mfma_f32_16x16x32_bf16 v[40:43], v[212:215], a[180:183], v[40:43]\n" \
"ds_read_b128 v[208:211], v246 offset:28672\n" \
"v_lshl_add_u32 v84, v84, 11, v85\n" \
"v_mfma_f32_16x16x32_bf16 v[20:23], a[232:235], a[168:171], v[20:23]\n" \
"s_add_u32 m0, s84, 143360\n" \
"ds_read_b128 v[212:215], v246 offset:30720\n" \
"v_exp_f32 v119, v119\n" \
"v_mfma_f32_16x16x32_bf16 v[20:23], a[236:239], a[172:175], v[20:23]\n" \
"global_load_lds_dwordx4 v253, s[48:49] offset:-4096\n" \
"v_add_f32 v242, v242, v119\n" \
"v_mfma_f32_16x16x32_bf16 v[28:31], a[232:235], a[184:187], v[28:31]\n" \
"v_exp_f32 v120, v120\n" \
"v_mfma_f32_16x16x32_bf16 v[28:31], a[236:239], a[188:191], v[28:31]\n" \
"s_waitcnt lgkmcnt(2)\n" \
"ds_read_b128 a[232:235], v247 offset:37888\n" \
"v_add_f32 v243, v243, v120\n" \
"v_cvt_pk_bf16_f32 v115, v118, v119\n" \
"v_mfma_f32_16x16x32_bf16 v[44:47], v[216:219], a[176:179], v[236:239]\n" \
"ds_read_b128 a[236:239], v247 offset:46080\n" \
"global_load_lds_dwordx4 v253, s[48:49] offset:-2048\n" \
"v_mfma_f32_16x16x32_bf16 v[44:47], v[220:223], a[180:183], v[44:47]\n" \
"ds_read_b128 v[216:219], v246 offset:28928\n" \
"v_exp_f32 v121, v121\n" \
"v_mfma_f32_16x16x32_bf16 v[48:51], a[240:243], a[168:171], v[48:51]\n" \
"ds_read_b128 v[220:223], v246 offset:30976\n" \
"v_add_f32 v243, v243, v121\n" \
"v_mfma_f32_16x16x32_bf16 v[48:51], a[244:247], a[172:175], v[48:51]\n" \
"global_load_lds_dwordx4 v253, s[48:49] offset:0\n" \
"v_exp_f32 v122, v122\n" \
"v_mfma_f32_16x16x32_bf16 v[56:59], a[240:243], a[184:187], v[56:59]\n" \
"s_add_u32 s48, s48, 0x120000\n" \
"s_addc_u32 s49, s49, 0\n" \
"v_add_f32 v243, v243, v122\n" \
"v_mfma_f32_16x16x32_bf16 v[56:59], a[244:247], a[188:191], v[56:59]\n" \
"ds_read_b128 a[240:243], v247 offset:38912\n" \
"v_cvt_pk_bf16_f32 v120, v120, v121\n" \
"v_mfma_f32_16x16x32_bf16 v[8:11], v[192:195], a[184:187], v[8:11]\n" \
"ds_read_b128 a[244:247], v247 offset:47104\n" \
"global_load_lds_dwordx4 v253, s[50:51] offset:-3072\n" \
"v_mfma_f32_16x16x32_bf16 v[8:11], v[196:199], a[188:191], v[8:11]\n" \
"v_exp_f32 v123, v123\n" \
"v_mfma_f32_16x16x32_bf16 v[52:55], a[248:251], a[168:171], v[52:55]\n" \
"v_add_f32 v243, v243, v123\n" \
"v_exp_f32 v124, v124\n" \
"v_mfma_f32_16x16x32_bf16 v[52:55], a[252:255], a[172:175], v[52:55]\n" \
"global_load_lds_dwordx4 v253, s[50:51] offset:-1024\n" \
"v_add_f32 v243, v243, v124\n" \
"v_cvt_pk_bf16_f32 v121, v122, v123\n" \
"v_mfma_f32_16x16x32_bf16 v[60:63], a[248:251], a[184:187], v[60:63]\n" \
"v_exp_f32 v125, v125\n" \
"v_mfma_f32_16x16x32_bf16 v[60:63], a[252:255], a[188:191], v[60:63]\n" \
"ds_read_b128 a[248:251], v247 offset:39936\n" \
"v_add_f32 v243, v243, v125\n" \
"v_mfma_f32_16x16x32_bf16 v[12:15], v[200:203], a[184:187], v[12:15]\n" \
"ds_read_b128 a[252:255], v247 offset:48128\n" \
"global_load_lds_dwordx4 v253, s[50:51] offset:1024\n" \
"v_mfma_f32_16x16x32_bf16 v[12:15], v[204:207], a[188:191], v[12:15]\n" \
"s_waitcnt lgkmcnt(2)\n" \
"s_add_u32 s50, s50, 0x120000\n" \
"s_addc_u32 s51, s51, 0\n" \
"v_mfma_f32_16x16x32_bf16 v[40:43], v[208:211], a[184:187], v[40:43]\n" \
"v_exp_f32 v126, v126\n" \
"v_mfma_f32_16x16x32_bf16 v[40:43], v[212:215], a[188:191], v[40:43]\n" \
"global_load_dword v85, v84, s[22:23]\n" \
"v_mfma_f32_16x16x32_bf16 v[44:47], v[216:219], a[184:187], v[44:47]\n" \
"s_waitcnt vmcnt(12) lgkmcnt(0)\n" \
"v_add_u32 v247, 0x10000, v248\n" \
"v_add_f32 v243, v243, v126\n" \
"v_mfma_f32_16x16x32_bf16 v[44:47], v[220:223], a[188:191], v[44:47]\n" \
"s_barrier\n" \
"v_cvt_pk_bf16_f32 v122, v124, v125\n" \
"v_mfma_f32_16x16x32_bf16 a[64:67], a[192:195], v[64:67], a[64:67]\n" \
"v_exp_f32 v127, v127\n" \
"v_mfma_f32_16x16x32_bf16 a[64:67], a[196:199], v[96:99], a[64:67]\n" \
"v_add_f32 v243, v243, v127\n" \
"v_cvt_pk_bf16_f32 v123, v126, v127\n" \
"v_mfma_f32_16x16x32_bf16 a[96:99], a[192:195], v[72:75], a[96:99]\n" \
"s_lshl_b32 s70, s61, 14\n" \
"ds_read_b128 v[192:195], v247 offset:16384\n" \
"v_cmp_gt_i32 vcc, 0, v251\n" \
"v_cmp_gt_i32 s[64:65], 1, v251\n" \
"v_cmp_gt_i32 s[66:67], 2, v251\n" \
"v_cndmask_b32 v8, v8, v254, vcc\n" \
"v_cndmask_b32 v16, v16, v254, vcc\n" \
"v_cmp_gt_i32 vcc, 3, v251\n" \
"v_add_u32 v124, s70, v248\n" \
"v_xor_b32 v125, 0x4000, v124\n" \
"v_mfma_f32_16x16x32_bf16 a[96:99], a[196:199], v[104:107], a[96:99]\n" \
"s_lshl_b32 s70, s61, 9\n" \
"s_add_u32 s70, s70, 110592\n" \
"v_cndmask_b32 v9, v9, v254, s[64:65]\n" \
"v_cndmask_b32 v17, v17, v254, s[64:65]\n" \
"v_cmp_gt_i32 s[64:65], 16, v251\n" \
"v_cndmask_b32 v10, v10, v254, s[66:67]\n" \
"v_cndmask_b32 v18, v18, v254, s[66:67]\n" \
"v_cmp_gt_i32 s[66:67], 17, v251\n" \
"v_lshrrev_b32 v126, 1, v248\n" \
"v_mfma_f32_16x16x32_bf16 v[128:131], a[192:195], v[80:83], v[128:131]\n" \
"v_add_u32 v126, s70, v126\n" \
"v_xor_b32 v127, 0x200, v126\n" \
"v_cndmask_b32 v11, v11, v254, vcc\n" \
"v_cndmask_b32 v19, v19, v254, vcc\n" \
"v_cmp_gt_i32 vcc, 18, v251\n" \
"v_cndmask_b32 v12, v12, v254, s[64:65]\n" \
"v_cndmask_b32 v20, v20, v254, s[64:65]\n" \
"v_cmp_gt_i32 s[64:65], 19, v251\n" \
"v_mfma_f32_16x16x32_bf16 v[128:131], a[196:199], v[112:115], v[128:131]\n" \
"v_lshrrev_b32 v71, 7, v248\n" \
"v_and_b32 v76, 0x70, v248\n" \
"v_cndmask_b32 v13, v13, v254, s[66:67]\n" \
"v_cndmask_b32 v21, v21, v254, s[66:67]\n" \
"v_cmp_gt_i32 s[66:67], 32, v251\n" \
"v_cndmask_b32 v14, v14, v254, vcc\n" \
"v_cndmask_b32 v22, v22, v254, vcc\n" \
"v_cmp_gt_i32 vcc, 33, v251\n" \
"v_mfma_f32_16x16x32_bf16 v[160:163], a[192:195], v[88:91], v[160:163]\n" \
"s_lshl_b32 s70, s88, 18\n" \
"s_lshl_b32 s71, s61, 16\n" \
"ds_read_b128 v[196:199], v247 offset:24576\n" \
"v_cndmask_b32 v15, v15, v254, s[64:65]\n" \
"v_cndmask_b32 v23, v23, v254, s[64:65]\n" \
"v_cmp_gt_i32 s[64:65], 34, v251\n" \
"v_cndmask_b32 v40, v40, v254, s[66:67]\n" \
"v_cndmask_b32 v48, v48, v254, s[66:67]\n" \
"v_cmp_gt_i32 s[66:67], 35, v251\n" \
"v_mfma_f32_16x16x32_bf16 v[160:163], a[196:199], v[120:123], v[160:163]\n" \
"s_add_u32 s71, s71, s70\n" \
"ds_read_b128 a[192:195], v247 offset:0\n" \
"v_cndmask_b32 v41, v41, v254, vcc\n" \
"v_cndmask_b32 v49, v49, v254, vcc\n" \
"v_cmp_gt_i32 vcc, 48, v251\n" \
"v_cndmask_b32 v42, v42, v254, s[64:65]\n" \
"v_cndmask_b32 v50, v50, v254, s[64:65]\n" \
"v_cmp_gt_i32 s[64:65], 49, v251\n" \
"v_lshlrev_b32 v77, 4, v71\n" \
"v_xor_b32 v77, v77, v76\n" \
"v_mfma_f32_16x16x32_bf16 a[68:71], a[200:203], v[64:67], a[68:71]\n" \
"s_add_u32 s42, s20, s71\n" \
"s_addc_u32 s43, s21, 0\n" \
"ds_write_b128 v124, a[96:99] offset:8192\n" \
"v_cndmask_b32 v43, v43, v254, s[66:67]\n" \
"v_cndmask_b32 v51, v51, v254, s[66:67]\n" \
"v_cmp_gt_i32 s[66:67], 50, v251\n" \
"v_cndmask_b32 v44, v44, v254, vcc\n" \
"v_cndmask_b32 v52, v52, v254, vcc\n" \
"v_cmp_gt_i32 vcc, 51, v251\n" \
"v_mfma_f32_16x16x32_bf16 a[68:71], a[204:207], v[96:99], a[68:71]\n" \
"ds_write_b128 v124, a[64:67] offset:0\n" \
"v_cndmask_b32 v45, v45, v254, s[64:65]\n" \
"v_cndmask_b32 v53, v53, v254, s[64:65]\n" \
"v_cndmask_b32 v46, v46, v254, s[66:67]\n" \
"v_cndmask_b32 v54, v54, v254, s[66:67]\n" \
"v_cndmask_b32 v47, v47, v254, vcc\n" \
"v_cndmask_b32 v55, v55, v254, vcc\n" \
"v_mfma_f32_16x16x32_bf16 a[100:103], a[200:203], v[72:75], a[100:103]\n" \
"ds_read_b128 v[200:203], v247 offset:17408\n" \
"v_lshl_add_u32 v252, v71, 12, v77\n" \
"v_exp_f32 v8, v8\n" \
"v_mfma_f32_16x16x32_bf16 a[100:103], a[204:207], v[104:107], a[100:103]\n" \
"s_add_u32 s44, s42, 31744\n" \
"s_addc_u32 s45, s43, 0\n" \
"ds_read_b128 a[196:199], v247 offset:8192\n" \
"v_add_f32 v243, v243, v8\n" \
"v_exp_f32 v9, v9\n" \
"v_mfma_f32_16x16x32_bf16 v[132:135], a[200:203], v[80:83], v[132:135]\n" \
"s_max_u32 s70, s70, 32768\n" \
"ds_write_b128 v124, a[68:71] offset:1024\n" \
"v_add_f32 v243, v243, v9\n" \
"v_exp_f32 v10, v10\n" \
"v_mfma_f32_16x16x32_bf16 v[132:135], a[204:207], v[112:115], v[132:135]\n" \
"s_sub_u32 s70, s70, 32768\n" \
"ds_write_b64 v126, v[244:245]\n" \
"v_add_f32 v243, v243, v10\n" \
"v_cvt_pk_bf16_f32 v8, v8, v9\n" \
"v_exp_f32 v11, v11\n" \
"v_mfma_f32_16x16x32_bf16 v[164:167], a[200:203], v[88:91], v[164:167]\n" \
"s_and_b32 s71, s61, 1\n" \
"ds_read_b128 v[204:207], v247 offset:25600\n" \
"v_add_f32 v243, v243, v11\n" \
"v_exp_f32 v12, v12\n" \
"s_lshl_b32 s71, s71, 2\n" \
"v_mfma_f32_16x16x32_bf16 v[164:167], a[204:207], v[120:123], v[164:167]\n" \
"ds_read_b128 a[200:203], v247 offset:1024\n" \
"v_and_or_b32 v71, v71, 3, s71\n" \
"v_add_f32 v243, v243, v12\n" \
"v_cvt_pk_bf16_f32 v9, v10, v11\n" \
"v_exp_f32 v13, v13\n" \
"v_mfma_f32_16x16x32_bf16 a[72:75], a[208:211], v[64:67], a[72:75]\n" \
"s_lshr_b32 s71, s61, 1\n" \
"ds_write_b128 v124, a[100:103] offset:9216\n" \
"v_add_f32 v243, v243, v13\n" \
"v_exp_f32 v14, v14\n" \
"v_mfma_f32_16x16x32_bf16 a[72:75], a[212:215], v[96:99], a[72:75]\n" \
"s_lshl_b32 s71, s71, 11\n" \
"v_add_f32 v243, v243, v14\n" \
"v_cvt_pk_bf16_f32 v10, v12, v13\n" \
"v_exp_f32 v15, v15\n" \
"v_lshlrev_b32 v77, 4, v71\n" \
"s_add_u32 s70, s70, s71\n" \
"v_xor_b32 v77, v77, v76\n" \
"v_mfma_f32_16x16x32_bf16 a[104:107], a[208:211], v[72:75], a[104:107]\n" \
"s_add_u32 s46, s20, s70\n" \
"s_addc_u32 s47, s21, 0\n" \
"ds_read_b128 v[208:211], v247 offset:18432\n" \
"v_add_f32 v243, v243, v15\n" \
"v_exp_f32 v40, v40\n" \
"v_lshl_add_u32 v246, v71, 12, v77\n" \
"v_mfma_f32_16x16x32_bf16 a[104:107], a[212:215], v[104:107], a[104:107]\n" \
"s_lshl_b32 s85, s61, 10\n" \
"ds_read_b128 a[204:207], v247 offset:9216\n" \
"v_add_f32 v243, v243, v40\n" \
"v_cvt_pk_bf16_f32 v11, v14, v15\n" \
"v_exp_f32 v41, v41\n" \
"s_mov_b64 s[12:13], s[20:21]\n" \
"v_mfma_f32_16x16x32_bf16 v[136:139], a[208:211], v[80:83], v[136:139]\n" \
"s_and_b32 s13, s13, 0xffff\n" \
"ds_write_b128 v124, a[72:75] offset:2048\n" \
"v_add_f32 v243, v243, v41\n" \
"v_exp_f32 v42, v42\n" \
"v_mfma_f32_16x16x32_bf16 v[136:139], a[212:215], v[112:115], v[136:139]\n" \
"s_mov_b32 s14, 4194304\n" \
"v_add_f32 v243, v243, v42\n" \
"v_cvt_pk_bf16_f32 v40, v40, v41\n" \
"v_exp_f32 v43, v43\n" \
"s_mov_b32 s15, 0x20000\n" \
"s_mov_b32 s46, s70\n" \
"v_mfma_f32_16x16x32_bf16 v[168:171], a[208:211], v[88:91], v[168:171]\n" \
"ds_read_b128 v[212:215], v247 offset:26624\n" \
"v_lshrrev_b32 v71, 9, v248\n" \
"v_add_f32 v243, v243, v43\n" \
"v_exp_f32 v44, v44\n" \
"v_mfma_f32_16x16x32_bf16 v[168:171], a[212:215], v[120:123], v[168:171]\n" \
"ds_read_b128 a[208:211], v247 offset:2048\n" \
"v_lshl_add_u32 v246, v71, 30, v246\n" \
"v_add_f32 v243, v243, v44\n" \
"v_cvt_pk_bf16_f32 v41, v42, v43\n" \
"v_exp_f32 v45, v45\n" \
"v_mfma_f32_16x16x32_bf16 a[76:79], a[216:219], v[64:67], a[76:79]\n" \
"s_add_u32 m0, s95, 118784\n" \
"ds_write_b128 v124, a[104:107] offset:10240\n" \
"v_add_f32 v243, v243, v45\n" \
"v_exp_f32 v46, v46\n" \
"v_mfma_f32_16x16x32_bf16 a[76:79], a[220:223], v[96:99], a[76:79]\n" \
"global_load_lds_dwordx4 v252, s[42:43] offset:0\n" \
"v_add_f32 v243, v243, v46\n" \
"v_cvt_pk_bf16_f32 v42, v44, v45\n" \
"v_exp_f32 v47, v47\n" \
"v_mfma_f32_16x16x32_bf16 a[108:111], a[216:219], v[72:75], a[108:111]\n" \
"ds_read_b128 v[216:219], v247 offset:19456\n" \
"v_add_f32 v243, v243, v47\n" \
"v_cvt_pk_bf16_f32 v43, v46, v47\n" \
"v_exp_f32 v24, v24\n" \
"v_mfma_f32_16x16x32_bf16 a[108:111], a[220:223], v[104:107], a[108:111]\n" \
"ds_read_b128 a[212:215], v247 offset:10240\n" \
"global_load_lds_dwordx4 v252, s[42:43] offset:2048\n" \
"v_add_f32 v243, v243, v24\n" \
"v_exp_f32 v25, v25\n" \
"v_mfma_f32_16x16x32_bf16 v[140:143], a[216:219], v[80:83], v[140:143]\n" \
"ds_write_b128 v124, a[76:79] offset:3072\n" \
"v_add_f32 v243, v243, v25\n" \
"v_exp_f32 v26, v26\n" \
"v_mfma_f32_16x16x32_bf16 v[140:143], a[220:223], v[112:115], v[140:143]\n" \
"s_add_u32 s42, s42, 0x180\n" \
"s_addc_u32 s43, s43, 0\n" \
"v_add_f32 v243, v243, v26\n" \
"v_cvt_pk_bf16_f32 v24, v24, v25\n" \
"v_exp_f32 v27, v27\n" \
"v_mfma_f32_16x16x32_bf16 v[172:175], a[216:219], v[88:91], v[172:175]\n" \
"ds_read_b128 v[220:223], v247 offset:27648\n" \
"v_add_f32 v243, v243, v27\n" \
"v_exp_f32 v28, v28\n" \
"v_mfma_f32_16x16x32_bf16 v[172:175], a[220:223], v[120:123], v[172:175]\n" \
"ds_read_b128 a[216:219], v247 offset:3072\n" \
"global_load_lds_dwordx4 v252, s[44:45] offset:1024\n" \
"v_add_f32 v243, v243, v28\n" \
"v_cvt_pk_bf16_f32 v25, v26, v27\n" \
"v_exp_f32 v29, v29\n" \
"v_mfma_f32_16x16x32_bf16 a[80:83], a[224:227], v[64:67], a[80:83]\n" \
"ds_write_b128 v124, a[108:111] offset:11264\n" \
"v_add_f32 v243, v243, v29\n" \
"v_exp_f32 v30, v30\n" \
"v_mfma_f32_16x16x32_bf16 a[80:83], a[228:231], v[96:99], a[80:83]\n" \
"v_add_f32 v243, v243, v30\n" \
"v_cvt_pk_bf16_f32 v26, v28, v29\n" \
"v_exp_f32 v31, v31\n" \
"v_mfma_f32_16x16x32_bf16 a[112:115], a[224:227], v[72:75], a[112:115]\n" \
"ds_read_b128 v[224:227], v247 offset:20480\n" \
"global_load_lds_dwordx4 v252, s[44:45] offset:3072\n" \
"v_add_f32 v243, v243, v31\n" \
"v_exp_f32 v56, v56\n" \
"v_mfma_f32_16x16x32_bf16 a[112:115], a[228:231], v[104:107], a[112:115]\n" \
"s_add_u32 s44, s44, 0x180\n" \
"s_addc_u32 s45, s45, 0\n" \
"ds_read_b128 a[220:223], v247 offset:11264\n" \
"v_add_f32 v243, v243, v56\n" \
"v_cvt_pk_bf16_f32 v27, v30, v31\n" \
"v_exp_f32 v57, v57\n" \
"v_mfma_f32_16x16x32_bf16 v[144:147], a[224:227], v[80:83], v[144:147]\n" \
"s_add_u32 m0, s85, 135168\n" \
"ds_write_b128 v124, a[80:83] offset:4096\n" \
"v_add_f32 v243, v243, v57\n" \
"v_exp_f32 v58, v58\n" \
"v_mfma_f32_16x16x32_bf16 v[144:147], a[228:231], v[112:115], v[144:147]\n" \
"buffer_load_dwordx4 v246, s[12:15], s46 offen lds\n" \
"v_add_f32 v243, v243, v58\n" \
"v_cvt_pk_bf16_f32 v56, v56, v57\n" \
"v_exp_f32 v59, v59\n" \
"v_mfma_f32_16x16x32_bf16 v[176:179], a[224:227], v[88:91], v[176:179]\n" \
"s_add_u32 s46, s46, 0x180\n" \
"ds_read_b128 v[228:231], v247 offset:28672\n" \
"v_add_f32 v243, v243, v59\n" \
"v_exp_f32 v60, v60\n" \
"v_mfma_f32_16x16x32_bf16 v[176:179], a[228:231], v[120:123], v[176:179]\n" \
"ds_read_b128 a[224:227], v247 offset:4096\n" \
"v_add_f32 v243, v243, v60\n" \
"v_cvt_pk_bf16_f32 v57, v58, v59\n" \
"v_exp_f32 v61, v61\n" \
"v_mfma_f32_16x16x32_bf16 a[84:87], a[232:235], v[64:67], a[84:87]\n" \
"ds_write_b128 v124, a[112:115] offset:12288\n" \
"v_add_f32 v243, v243, v61\n" \
"v_exp_f32 v62, v62\n" \
"v_mfma_f32_16x16x32_bf16 a[84:87], a[236:239], v[96:99], a[84:87]\n" \
"v_add_f32 v243, v243, v62\n" \
"v_cvt_pk_bf16_f32 v58, v60, v61\n" \
"v_exp_f32 v63, v63\n" \
"v_mfma_f32_16x16x32_bf16 a[116:119], a[232:235], v[72:75], a[116:119]\n" \
"ds_read_b128 v[232:235], v247 offset:21504\n" \
"v_add_f32 v243, v243, v63\n" \
"v_cvt_pk_bf16_f32 v59, v62, v63\n" \
"v_exp_f32 v16, v16\n" \
"v_mfma_f32_16x16x32_bf16 a[116:119], a[236:239], v[104:107], a[116:119]\n" \
"ds_read_b128 a[228:231], v247 offset:12288\n" \
"v_add_f32 v242, v242, v16\n" \
"v_exp_f32 v17, v17\n" \
"v_mfma_f32_16x16x32_bf16 v[148:151], a[232:235], v[80:83], v[148:151]\n" \
"ds_write_b128 v124, a[84:87] offset:5120\n" \
"v_add_f32 v242, v242, v17\n" \
"v_exp_f32 v18, v18\n" \
"v_mfma_f32_16x16x32_bf16 v[148:151], a[236:239], v[112:115], v[148:151]\n" \
"v_add_f32 v242, v242, v18\n" \
"v_cvt_pk_bf16_f32 v16, v16, v17\n" \
"v_exp_f32 v19, v19\n" \
"v_mfma_f32_16x16x32_bf16 v[180:183], a[232:235], v[88:91], v[180:183]\n" \
"ds_read_b128 v[236:239], v247 offset:29696\n" \
"v_add_f32 v242, v242, v19\n" \
"v_exp_f32 v20, v20\n" \
"v_mfma_f32_16x16x32_bf16 v[180:183], a[236:239], v[120:123], v[180:183]\n" \
"ds_read_b128 a[232:235], v247 offset:5120\n" \
"v_add_f32 v242, v242, v20\n" \
"v_cvt_pk_bf16_f32 v17, v18, v19\n" \
"v_exp_f32 v21, v21\n" \
"v_mfma_f32_16x16x32_bf16 a[88:91], a[240:243], v[64:67], a[88:91]\n" \
"ds_write_b128 v124, a[116:119] offset:13312\n" \
"v_add_f32 v242, v242, v21\n" \
"v_mfma_f32_16x16x32_bf16 a[88:91], a[244:247], v[96:99], a[88:91]\n" \
"v_exp_f32 v22, v22\n" \
"v_mfma_f32_16x16x32_bf16 a[120:123], a[240:243], v[72:75], a[120:123]\n" \
"ds_read_b128 v[0:3], v247 offset:22528\n" \
"v_add_f32 v242, v242, v22\n" \
"v_mfma_f32_16x16x32_bf16 a[120:123], a[244:247], v[104:107], a[120:123]\n" \
"ds_read_b128 a[236:239], v247 offset:13312\n" \
"v_cvt_pk_bf16_f32 v18, v20, v21\n" \
"v_mfma_f32_16x16x32_bf16 v[152:155], a[240:243], v[80:83], v[152:155]\n" \
"ds_write_b128 v124, a[88:91] offset:6144\n" \
"v_mfma_f32_16x16x32_bf16 v[152:155], a[244:247], v[112:115], v[152:155]\n" \
"v_exp_f32 v23, v23\n" \
"v_mfma_f32_16x16x32_bf16 v[184:187], a[240:243], v[88:91], v[184:187]\n" \
"ds_read_b128 v[4:7], v247 offset:30720\n" \
"v_add_f32 v242, v242, v23\n" \
"v_mfma_f32_16x16x32_bf16 v[184:187], a[244:247], v[120:123], v[184:187]\n" \
"ds_read_b128 a[240:243], v247 offset:6144\n" \
"v_exp_f32 v48, v48\n" \
"v_mfma_f32_16x16x32_bf16 a[92:95], a[248:251], v[64:67], a[92:95]\n" \
"ds_write_b128 v124, a[120:123] offset:14336\n" \
"v_mfma_f32_16x16x32_bf16 a[92:95], a[252:255], v[96:99], a[92:95]\n" \
"v_add_f32 v242, v242, v48\n" \
"v_cvt_pk_bf16_f32 v19, v22, v23\n" \
"v_mfma_f32_16x16x32_bf16 a[124:127], a[248:251], v[72:75], a[124:127]\n" \
"ds_read_b128 v[32:35], v247 offset:23552\n" \
"v_exp_f32 v49, v49\n" \
"v_mfma_f32_16x16x32_bf16 a[124:127], a[252:255], v[104:107], a[124:127]\n" \
"ds_read_b128 a[244:247], v247 offset:14336\n" \
"v_add_f32 v242, v242, v49\n" \
"v_mfma_f32_16x16x32_bf16 v[156:159], a[248:251], v[80:83], v[156:159]\n" \
"ds_write_b128 v124, a[92:95] offset:7168\n" \
"v_mfma_f32_16x16x32_bf16 v[156:159], a[252:255], v[112:115], v[156:159]\n" \
"v_exp_f32 v50, v50\n" \
"v_mfma_f32_16x16x32_bf16 v[188:191], a[248:251], v[88:91], v[188:191]\n" \
"ds_read_b128 v[36:39], v247 offset:31744\n" \
"v_add_f32 v242, v242, v50\n" \
"v_mfma_f32_16x16x32_bf16 v[188:191], a[252:255], v[120:123], v[188:191]\n" \
"s_waitcnt lgkmcnt(3)\n" \
"ds_read_b128 a[248:251], v247 offset:7168\n" \
"v_cvt_pk_bf16_f32 v48, v48, v49\n" \
"v_mfma_f32_16x16x32_bf16 v[160:163], v[192:195], v[8:11], v[160:163]\n" \
"v_exp_f32 v51, v51\n" \
"v_mfma_f32_16x16x32_bf16 v[160:163], v[196:199], v[40:43], v[160:163]\n" \
"ds_read_b128 a[252:255], v247 offset:15360\n" \
"v_add_f32 v242, v242, v51\n" \
"v_exp_f32 v52, v52\n" \
"v_mfma_f32_16x16x32_bf16 v[164:167], v[200:203], v[8:11], v[164:167]\n" \
"ds_write_b128 v124, a[124:127] offset:15360\n" \
"v_mfma_f32_16x16x32_bf16 v[164:167], v[204:207], v[40:43], v[164:167]\n" \
"v_add_f32 v242, v242, v52\n" \
"v_cvt_pk_bf16_f32 v49, v50, v51\n" \
"v_mfma_f32_16x16x32_bf16 v[168:171], v[208:211], v[8:11], v[168:171]\n" \
"v_exp_f32 v53, v53\n" \
"v_mfma_f32_16x16x32_bf16 v[168:171], v[212:215], v[40:43], v[168:171]\n" \
"s_waitcnt vmcnt(0) lgkmcnt(0)\n" \
"v_add_f32 v242, v242, v53\n" \
"v_exp_f32 v54, v54\n" \
"v_mfma_f32_16x16x32_bf16 v[172:175], v[216:219], v[8:11], v[172:175]\n" \
"s_barrier\n" \
"v_add_f32 v242, v242, v54\n" \
"v_cvt_pk_bf16_f32 v50, v52, v53\n" \
"v_mfma_f32_16x16x32_bf16 v[172:175], v[220:223], v[40:43], v[172:175]\n" \
"s_add_u32 m0, s84, 90112\n" \
"ds_read_b64 v[244:245], v127\n" \
"v_exp_f32 v55, v55\n" \
"v_mfma_f32_16x16x32_bf16 v[176:179], v[224:227], v[8:11], v[176:179]\n" \
"ds_read_b128 v[64:67], v125 offset:0\n" \
"global_load_lds_dwordx4 v253, s[48:49] offset:-4096\n" \
"v_mfma_f32_16x16x32_bf16 v[176:179], v[228:231], v[40:43], v[176:179]\n" \
"ds_read_b128 v[68:71], v125 offset:1024\n" \
"global_load_lds_dwordx4 v253, s[48:49] offset:-2048\n" \
"v_mfma_f32_16x16x32_bf16 v[180:183], v[232:235], v[8:11], v[180:183]\n" \
"ds_read_b128 v[72:75], v125 offset:2048\n" \
"v_add_f32 v242, v242, v55\n" \
"v_mfma_f32_16x16x32_bf16 v[180:183], v[236:239], v[40:43], v[180:183]\n" \
"ds_read_b128 v[76:79], v125 offset:3072\n" \
"global_load_lds_dwordx4 v253, s[48:49] offset:0\n" \
"v_mfma_f32_16x16x32_bf16 v[184:187], v[0:3], v[8:11], v[184:187]\n" \
"s_add_u32 s48, s48, 0x300000\n" \
"s_addc_u32 s49, s49, 0\n" \
"ds_read_b128 v[80:83], v125 offset:4096\n" \
"v_mfma_f32_16x16x32_bf16 v[184:187], v[4:7], v[40:43], v[184:187]\n" \
"ds_read_b128 v[84:87], v125 offset:5120\n" \
"global_load_lds_dwordx4 v253, s[50:51] offset:-3072\n" \
"v_mfma_f32_16x16x32_bf16 v[188:191], v[32:35], v[8:11], v[188:191]\n" \
"s_waitcnt lgkmcnt(5)\n" \
"ds_read_b128 v[88:91], v125 offset:6144\n" \
"v_cvt_pk_bf16_f32 v51, v54, v55\n" \
"v_add_f32 v128, v128, v64\n" \
"v_add_f32 v129, v129, v65\n" \
"v_add_f32 v130, v130, v66\n" \
"v_add_f32 v131, v131, v67\n" \
"v_mfma_f32_16x16x32_bf16 v[188:191], v[36:39], v[40:43], v[188:191]\n" \
"s_waitcnt lgkmcnt(5)\n" \
"ds_read_b128 v[92:95], v125 offset:7168\n" \
"global_load_lds_dwordx4 v253, s[50:51] offset:-1024\n" \
"v_add_f32 v132, v132, v68\n" \
"v_add_f32 v133, v133, v69\n" \
"v_add_f32 v134, v134, v70\n" \
"v_add_f32 v135, v135, v71\n" \
"v_mfma_f32_16x16x32_bf16 v[160:163], a[192:195], v[24:27], v[160:163]\n" \
"s_waitcnt lgkmcnt(5)\n" \
"ds_read_b128 v[96:99], v125 offset:8192\n" \
"global_load_lds_dwordx4 v253, s[50:51] offset:1024\n" \
"v_add_f32 v136, v136, v72\n" \
"v_add_f32 v137, v137, v73\n" \
"v_add_f32 v138, v138, v74\n" \
"v_add_f32 v139, v139, v75\n" \
"v_mfma_f32_16x16x32_bf16 v[160:163], a[196:199], v[56:59], v[160:163]\n" \
"s_add_u32 s50, s50, 0x300000\n" \
"s_addc_u32 s51, s51, 0\n" \
"ds_read_b128 v[100:103], v125 offset:9216\n" \
"v_mfma_f32_16x16x32_bf16 v[164:167], a[200:203], v[24:27], v[164:167]\n" \
"s_add_u32 m0, s95, 65536\n" \
"ds_read_b128 v[104:107], v125 offset:10240\n" \
"s_movk_i32 s89, 0xc0\n" \
"v_mfma_f32_16x16x32_bf16 v[164:167], a[204:207], v[56:59], v[164:167]\n" \
"s_movk_i32 s90, 288\n" \
"ds_read_b128 v[108:111], v125 offset:11264\n" \
"global_load_lds_dwordx4 v252, s[42:43] offset:0\n" \
"Lpg8753_%=:\n" \
"Lst1x2_%=:\n" \
"s_cbranch_execz Lpg11053_%=\n" \
"v_lshrrev_b32 v211, 4, v248\n" \
"v_lshrrev_b32 v214, 8, v248\n" \
"v_and_b32 v215, 7, v211\n" \
"v_xor_b32 v214, v214, v215\n" \
"v_mfma_f32_16x16x32_bf16 v[168:171], a[208:211], v[24:27], v[168:171]\n" \
"s_lshr_b32 s71, s61, 1\n" \
"ds_read_b128 v[112:115], v125 offset:12288\n" \
"s_lshl_b32 s70, s71, 11\n" \
"v_mfma_f32_16x16x32_bf16 v[168:171], a[212:215], v[56:59], v[168:171]\n" \
"s_add_u32 s70, s70, 118784\n" \
"ds_read_b128 v[116:119], v125 offset:13312\n" \
"global_load_lds_dwordx4 v252, s[42:43] offset:2048\n" \
"v_and_b32 v216, 15, v211\n" \
"v_lshlrev_b32 v213, 7, v216\n" \
"v_lshl_add_u32 v213, v214, 4, v213\n" \
"v_mfma_f32_16x16x32_bf16 v[172:175], a[216:219], v[24:27], v[172:175]\n" \
"s_add_u32 s42, s42, 0x400\n" \
"s_addc_u32 s43, s43, 0\n" \
"ds_read_b128 v[120:123], v125 offset:14336\n" \
"v_add_u32 v205, s70, v213\n" \
"v_xor_b32 v206, 64, v205\n" \
"v_lshrrev_b32 v216, 2, v216\n" \
"v_lshl_add_u32 v216, v216, 9, v213\n" \
"v_add_u32 v207, 135168, v216\n" \
"v_xor_b32 v208, 64, v207\n" \
"v_mfma_f32_16x16x32_bf16 v[172:175], a[220:223], v[56:59], v[172:175]\n" \
"s_and_b32 s70, s61, 1\n" \
"ds_read_b128 v[124:127], v125 offset:15360\n" \
"global_load_lds_dwordx4 v252, s[44:45] offset:1024\n" \
"v_mfma_f32_16x16x32_bf16 v[176:179], a[224:227], v[24:27], v[176:179]\n" \
"s_lshl_b32 s70, s70, 9\n" \
"s_add_u32 s70, s70, 139264\n" \
"s_lshl_b32 s71, s71, 5\n" \
"v_mfma_f32_16x16x32_bf16 v[176:179], a[228:231], v[56:59], v[176:179]\n" \
"s_waitcnt lgkmcnt(12)\n" \
"global_load_lds_dwordx4 v252, s[44:45] offset:3072\n" \
"v_and_b32 v213, 3, v211\n" \
"v_add_f32 v140, v140, v76\n" \
"v_add_f32 v141, v141, v77\n" \
"v_add_f32 v142, v142, v78\n" \
"v_add_f32 v143, v143, v79\n" \
"v_mfma_f32_16x16x32_bf16 v[180:183], a[232:235], v[24:27], v[180:183]\n" \
"s_add_u32 s44, s44, 0x400\n" \
"s_addc_u32 s45, s45, 0\n" \
"s_add_u32 m0, s85, 81920\n" \
"v_mfma_f32_16x16x32_bf16 v[180:183], a[236:239], v[56:59], v[180:183]\n" \
"s_waitcnt lgkmcnt(0)\n" \
"buffer_load_dwordx4 v246, s[12:15], s46 offen lds\n" \
"v_mfma_f32_16x16x32_bf16 v[184:187], a[240:243], v[24:27], v[184:187]\n" \
"s_barrier\n" \
"v_lshlrev_b32 v213, 3, v213\n" \
"v_mfma_f32_16x16x32_bf16 v[184:187], a[244:247], v[56:59], v[184:187]\n" \
"s_add_u32 s46, s46, 0x400\n" \
"v_and_or_b32 v213, v248, s89, v213\n" \
"v_bfe_u32 v214, v211, 4, 1\n" \
"v_mfma_f32_16x16x32_bf16 v[188:191], a[248:251], v[24:27], v[188:191]\n" \
"s_add_u32 m0, s95, 0\n" \
"v_mad_u32_u24 v213, v214, s90, v213\n" \
"v_lshrrev_b32 v214, 5, v211\n" \
"v_mul_u32_u24 v214, 6144, v214\n" \
"v_add3_u32 v213, v213, v214, s70\n" \
"v_xor_b32 v209, s71, v213\n" \
"v_xor_b32 v210, 32, v209\n" \
"v_mfma_f32_16x16x32_bf16 v[188:191], a[252:255], v[56:59], v[188:191]\n" \
"ds_read_b128 a[64:67], v207 offset:0\n" \
"ds_read_b64_tr_b16 a[96:97], v209 offset:2048\n" \
"v_add_f32 v144, v144, v80\n" \
"v_add_f32 v145, v145, v81\n" \
"v_add_f32 v146, v146, v82\n" \
"v_add_f32 v147, v147, v83\n" \
"v_mfma_f32_16x16x32_bf16 v[128:131], a[192:195], v[16:19], v[128:131]\n" \
"ds_read_b64_tr_b16 a[98:99], v209 offset:3072\n" \
"global_load_lds_dwordx4 v252, s[42:43] offset:0\n" \
"v_add_f32 v148, v148, v84\n" \
"v_add_f32 v149, v149, v85\n" \
"v_add_f32 v150, v150, v86\n" \
"v_add_f32 v151, v151, v87\n" \
"v_mfma_f32_16x16x32_bf16 v[128:131], a[196:199], v[48:51], v[128:131]\n" \
"ds_read_b128 a[68:71], v208 offset:0\n" \
"v_add_f32 v152, v152, v88\n" \
"v_add_f32 v153, v153, v89\n" \
"v_add_f32 v154, v154, v90\n" \
"v_add_f32 v155, v155, v91\n" \
"v_mfma_f32_16x16x32_bf16 v[132:135], a[200:203], v[16:19], v[132:135]\n" \
"ds_read_b64_tr_b16 a[100:101], v209 offset:14336\n" \
"global_load_lds_dwordx4 v252, s[42:43] offset:2048\n" \
"ds_read_b64_tr_b16 a[102:103], v209 offset:15360\n" \
"v_add_f32 v156, v156, v92\n" \
"v_add_f32 v157, v157, v93\n" \
"v_add_f32 v158, v158, v94\n" \
"v_mfma_f32_16x16x32_bf16 v[132:135], a[204:207], v[48:51], v[132:135]\n" \
"s_add_u32 s42, s42, 0xfffffb00\n" \
"s_addc_u32 s43, s43, -1\n" \
"ds_read_b128 a[72:75], v205 offset:0\n" \
"v_add_f32 v159, v159, v95\n" \
"v_mfma_f32_16x16x32_bf16 v[136:139], a[208:211], v[16:19], v[136:139]\n" \
"ds_read_b64_tr_b16 a[104:105], v209 offset:0\n" \
"global_load_lds_dwordx4 v252, s[44:45] offset:1024\n" \
"v_add_f32 v160, v160, v96\n" \
"v_add_f32 v161, v161, v97\n" \
"v_add_f32 v162, v162, v98\n" \
"v_add_f32 v163, v163, v99\n" \
"v_mfma_f32_16x16x32_bf16 v[136:139], a[212:215], v[48:51], v[136:139]\n" \
"ds_read_b128 a[76:79], v206 offset:0\n" \
"ds_read_b64_tr_b16 a[106:107], v209 offset:1024\n" \
"v_add_f32 v164, v164, v100\n" \
"v_add_f32 v165, v165, v101\n" \
"v_add_f32 v166, v166, v102\n" \
"v_add_f32 v167, v167, v103\n" \
"v_mfma_f32_16x16x32_bf16 v[140:143], a[216:219], v[16:19], v[140:143]\n" \
"ds_read_b64_tr_b16 a[108:109], v209 offset:12288\n" \
"v_add_f32 v168, v168, v104\n" \
"v_add_f32 v169, v169, v105\n" \
"v_add_f32 v170, v170, v106\n" \
"v_add_f32 v171, v171, v107\n" \
"v_mfma_f32_16x16x32_bf16 v[140:143], a[220:223], v[48:51], v[140:143]\n" \
"ds_read_b64_tr_b16 a[110:111], v209 offset:13312\n" \
"global_load_lds_dwordx4 v252, s[44:45] offset:3072\n" \
"v_add_f32 v172, v172, v108\n" \
"v_add_f32 v173, v173, v109\n" \
"v_add_f32 v174, v174, v110\n" \
"v_add_f32 v175, v175, v111\n" \
"v_mfma_f32_16x16x32_bf16 v[144:147], a[224:227], v[16:19], v[144:147]\n" \
"ds_read_b128 a[80:83], v205 offset:4096\n" \
"ds_read_b64_tr_b16 a[112:113], v210 offset:0\n" \
"v_add_f32 v176, v176, v112\n" \
"v_add_f32 v177, v177, v113\n" \
"v_add_f32 v178, v178, v114\n" \
"v_mfma_f32_16x16x32_bf16 v[144:147], a[228:231], v[48:51], v[144:147]\n" \
"s_add_u32 s44, s44, 0xfffffb00\n" \
"s_addc_u32 s45, s45, -1\n" \
"ds_read_b64_tr_b16 a[114:115], v210 offset:1024\n" \
"v_mfma_f32_16x16x32_bf16 v[148:151], a[232:235], v[16:19], v[148:151]\n" \
"s_add_u32 m0, s85, 16384\n" \
"ds_read_b64_tr_b16 a[116:117], v210 offset:12288\n" \
"v_add_f32 v179, v179, v115\n" \
"v_mfma_f32_16x16x32_bf16 v[148:151], a[236:239], v[48:51], v[148:151]\n" \
"ds_read_b128 a[84:87], v206 offset:4096\n" \
"ds_read_b64_tr_b16 a[118:119], v210 offset:13312\n" \
"v_add_f32 v180, v180, v116\n" \
"v_add_f32 v181, v181, v117\n" \
"v_add_f32 v182, v182, v118\n" \
"v_add_f32 v183, v183, v119\n" \
"v_mfma_f32_16x16x32_bf16 v[152:155], a[240:243], v[16:19], v[152:155]\n" \
"ds_read_b128 a[88:91], v205 offset:8192\n" \
"v_add_f32 v184, v184, v120\n" \
"v_add_f32 v185, v185, v121\n" \
"v_add_f32 v186, v186, v122\n" \
"v_add_f32 v187, v187, v123\n" \
"v_mfma_f32_16x16x32_bf16 v[152:155], a[244:247], v[48:51], v[152:155]\n" \
"ds_read_b128 a[92:95], v206 offset:8192\n" \
"v_add_f32 v188, v188, v124\n" \
"v_add_f32 v189, v189, v125\n" \
"v_add_f32 v190, v190, v126\n" \
"v_add_f32 v191, v191, v127\n" \
"v_mfma_f32_16x16x32_bf16 v[156:159], a[248:251], v[16:19], v[156:159]\n" \
"buffer_load_dwordx4 v246, s[12:15], s46 offen lds\n" \
"v_mfma_f32_16x16x32_bf16 v[156:159], a[252:255], v[48:51], v[156:159]\n" \
"s_waitcnt lgkmcnt(4)\n" \
"v_add_f32 v242, v242, v244\n" \
"v_add_f32 v243, v243, v245\n" \
"Ljoin_%=:\n" \
"v_mfma_f32_16x16x32_bf16 v[32:35], a[96:99], a[64:67], 0\n" \
"s_add_u32 s46, s46, 0xfffffb00\n" \
"ds_read_b128 a[192:195], v205 offset:12288\n" \
"s_add_u32 m0, s84, 24576\n" \
"v_mfma_f32_16x16x32_bf16 v[32:35], a[100:103], a[68:71], v[32:35]\n" \
"global_load_lds_dwordx4 v253, s[48:49] offset:-4096\n" \
"v_mfma_f32_16x16x32_bf16 v[40:43], a[96:99], a[72:75], 0\n" \
"ds_read_b128 a[196:199], v206 offset:12288\n" \
"v_mfma_f32_16x16x32_bf16 v[40:43], a[100:103], a[76:79], v[40:43]\n" \
"ds_read_b64_tr_b16 a[200:201], v210 offset:2048\n" \
"v_mov_b32 v232, v242\n" \
"v_mfma_f32_16x16x32_bf16 v[0:3], a[104:107], a[72:75], 0\n" \
"ds_read_b64_tr_b16 a[202:203], v210 offset:3072\n" \
"global_load_lds_dwordx4 v253, s[48:49] offset:-2048\n" \
"v_mfma_f32_16x16x32_bf16 v[0:3], a[108:111], a[76:79], v[0:3]\n" \
"s_waitcnt lgkmcnt(3)\n" \
"v_permlane16_swap_b32 v232, v242\n" \
"v_mfma_f32_16x16x32_bf16 v[4:7], a[112:115], a[72:75], 0\n" \
"ds_read_b64_tr_b16 a[204:205], v210 offset:14336\n" \
"v_add_f32 v242, v242, v232\n" \
"v_mfma_f32_16x16x32_bf16 v[4:7], a[116:119], a[76:79], v[4:7]\n" \
"ds_read_b64_tr_b16 a[206:207], v210 offset:15360\n" \
"global_load_lds_dwordx4 v253, s[48:49] offset:0\n" \
"v_mfma_f32_16x16x32_bf16 v[48:51], a[96:99], a[80:83], 0\n" \
"s_add_u32 s48, s48, 0xffc40000\n" \
"s_addc_u32 s49, s49, -1\n" \
"ds_read_b64_tr_b16 a[208:209], v209 offset:4096\n" \
"v_mfma_f32_16x16x32_bf16 v[48:51], a[100:103], a[84:87], v[48:51]\n" \
"v_mov_b32 v232, v242\n" \
"v_mfma_f32_16x16x32_bf16 v[8:11], a[104:107], a[80:83], 0\n" \
"ds_read_b64_tr_b16 a[210:211], v209 offset:5120\n" \
"global_load_lds_dwordx4 v253, s[50:51] offset:-3072\n" \
"v_mfma_f32_16x16x32_bf16 v[8:11], a[108:111], a[84:87], v[8:11]\n" \
"ds_read_b64_tr_b16 a[212:213], v209 offset:16384\n" \
"v_mfma_f32_16x16x32_bf16 v[12:15], a[112:115], a[80:83], 0\n" \
"ds_read_b64_tr_b16 a[214:215], v209 offset:17408\n" \
"v_mfma_f32_16x16x32_bf16 v[12:15], a[116:119], a[84:87], v[12:15]\n" \
"global_load_lds_dwordx4 v253, s[50:51] offset:-1024\n" \
"v_mfma_f32_16x16x32_bf16 v[56:59], a[96:99], a[88:91], 0\n" \
"ds_read_b64_tr_b16 a[216:217], v210 offset:4096\n" \
"v_mfma_f32_16x16x32_bf16 v[56:59], a[100:103], a[92:95], v[56:59]\n" \
"ds_read_b64_tr_b16 a[218:219], v210 offset:5120\n" \
"v_mfma_f32_16x16x32_bf16 v[16:19], a[104:107], a[88:91], 0\n" \
"ds_read_b64_tr_b16 a[220:221], v210 offset:16384\n" \
"global_load_lds_dwordx4 v253, s[50:51] offset:1024\n" \
"v_mfma_f32_16x16x32_bf16 v[16:19], a[108:111], a[92:95], v[16:19]\n" \
"s_add_u32 s50, s50, 0xffc40000\n" \
"s_addc_u32 s51, s51, -1\n" \
"v_mfma_f32_16x16x32_bf16 v[20:23], a[112:115], a[88:91], 0\n" \
"ds_read_b64_tr_b16 a[222:223], v210 offset:17408\n" \
"v_mfma_f32_16x16x32_bf16 v[20:23], a[116:119], a[92:95], v[20:23]\n" \
"s_waitcnt lgkmcnt(3)\n" \
"v_add_u32 v207, 0xffff3000, v207\n" \
"v_add_u32 v208, 0xffff3000, v208\n" \
"v_mfma_f32_16x16x32_bf16 v[64:67], a[96:99], a[192:195], 0\n" \
"v_add_u32 v205, 0xffff3000, v205\n" \
"v_add_u32 v206, 0xffff3000, v206\n" \
"v_mfma_f32_16x16x32_bf16 v[64:67], a[100:103], a[196:199], v[64:67]\n" \
"v_add_u32 v209, 0xffff3000, v209\n" \
"v_add_u32 v210, 0xffff3000, v210\n" \
"v_mfma_f32_16x16x32_bf16 v[24:27], a[104:107], a[192:195], 0\n" \
"v_permlane32_swap_b32 v232, v242\n" \
"v_mfma_f32_16x16x32_bf16 v[24:27], a[108:111], a[196:199], v[24:27]\n" \
"v_add_f32 v242, v242, v232\n" \
"v_mfma_f32_16x16x32_bf16 v[28:31], a[112:115], a[192:195], 0\n" \
"s_waitcnt vmcnt(11) lgkmcnt(0)\n" \
"v_rcp_f32 v232, v242\n" \
"v_mfma_f32_16x16x32_bf16 v[28:31], a[116:119], a[196:199], v[28:31]\n" \
"s_barrier\n" \
"v_mul_f32 v242, 0x4d800000, v242\n" \
"v_mfma_f32_16x16x32_bf16 v[72:75], a[64:67], a[208:211], 0\n" \
"ds_read_b64_tr_b16 a[96:97], v209 offset:2048\n" \
"v_add_u32 v233, 0x1c90080, v249\n" \
"v_mfma_f32_16x16x32_bf16 v[72:75], a[68:71], a[212:215], v[72:75]\n" \
"ds_read_b64_tr_b16 a[98:99], v209 offset:3072\n" \
"v_cndmask_b32 v233, v249, v233, s[16:17]\n" \
"v_mfma_f32_16x16x32_bf16 v[44:47], a[200:203], a[72:75], 0\n" \
"s_add_u32 m0, s95, 118784\n" \
"ds_read_b128 a[64:67], v207 offset:0\n" \
"ds_read_b64_tr_b16 a[100:101], v209 offset:14336\n" \
"v_mfma_f32_16x16x32_bf16 v[44:47], a[204:207], a[76:79], v[44:47]\n" \
"global_load_lds_dwordx4 v252, s[42:43] offset:0\n" \
"v_mul_f32 v128, v128, v232\n" \
"v_mfma_f32_16x16x32_bf16 v[80:83], a[72:75], a[208:211], 0\n" \
"ds_read_b128 a[68:71], v208 offset:0\n" \
"ds_read_b64_tr_b16 a[102:103], v209 offset:15360\n" \
"v_mfma_f32_16x16x32_bf16 v[80:83], a[76:79], a[212:215], v[80:83]\n" \
"ds_read_b64_tr_b16 a[104:105], v209 offset:0\n" \
"v_mul_f32 v129, v129, v232\n" \
"v_mfma_f32_16x16x32_bf16 v[84:87], a[72:75], a[216:219], 0\n" \
"ds_read_b64_tr_b16 a[106:107], v209 offset:1024\n" \
"v_mul_f32 v130, v130, v232\n" \
"v_mfma_f32_16x16x32_bf16 v[84:87], a[76:79], a[220:223], v[84:87]\n" \
"global_load_lds_dwordx4 v252, s[42:43] offset:2048\n" \
"v_mul_f32 v131, v131, v232\n" \
"v_mfma_f32_16x16x32_bf16 v[52:55], a[200:203], a[80:83], 0\n" \
"s_add_u32 s42, s42, 0x80\n" \
"s_addc_u32 s43, s43, 0\n" \
"ds_read_b128 a[72:75], v205 offset:0\n" \
"v_mfma_f32_16x16x32_bf16 v[52:55], a[204:207], a[84:87], v[52:55]\n" \
"ds_read_b128 a[76:79], v206 offset:0\n" \
"v_mul_f32 v132, v132, v232\n" \
"v_mfma_f32_16x16x32_bf16 v[88:91], a[80:83], a[208:211], 0\n" \
"ds_read_b64_tr_b16 a[108:109], v209 offset:12288\n" \
"v_mul_f32 v133, v133, v232\n" \
"v_mfma_f32_16x16x32_bf16 v[88:91], a[84:87], a[212:215], v[88:91]\n" \
"global_load_lds_dwordx4 v252, s[44:45] offset:1024\n" \
"v_mul_f32 v134, v134, v232\n" \
"v_mfma_f32_16x16x32_bf16 v[92:95], a[80:83], a[216:219], 0\n" \
"ds_read_b64_tr_b16 a[110:111], v209 offset:13312\n" \
"v_mul_f32 v135, v135, v232\n" \
"v_mfma_f32_16x16x32_bf16 v[92:95], a[84:87], a[220:223], v[92:95]\n" \
"ds_read_b64_tr_b16 a[112:113], v210 offset:0\n" \
"v_fma_f32 v242, v128, v129, v242\n" \
"v_mfma_f32_16x16x32_bf16 v[60:63], a[200:203], a[88:91], 0\n" \
"ds_read_b128 a[80:83], v205 offset:4096\n" \
"v_cvt_pk_bf16_f32 v128, v128, v129\n" \
"v_mfma_f32_16x16x32_bf16 v[60:63], a[204:207], a[92:95], v[60:63]\n" \
"global_load_lds_dwordx4 v252, s[44:45] offset:3072\n" \
"v_fma_f32 v242, v130, v131, v242\n" \
"v_mfma_f32_16x16x32_bf16 v[96:99], a[88:91], a[208:211], 0\n" \
"s_add_u32 s44, s44, 0x80\n" \
"s_addc_u32 s45, s45, 0\n" \
"ds_read_b128 a[84:87], v206 offset:4096\n" \
"v_mfma_f32_16x16x32_bf16 v[96:99], a[92:95], a[212:215], v[96:99]\n" \
"ds_read_b64_tr_b16 a[114:115], v210 offset:1024\n" \
"v_cvt_pk_bf16_f32 v129, v130, v131\n" \
"v_mfma_f32_16x16x32_bf16 v[100:103], a[88:91], a[216:219], 0\n" \
"s_add_u32 m0, s85, 135168\n" \
"ds_read_b64_tr_b16 a[116:117], v210 offset:12288\n" \
"v_fma_f32 v242, v132, v133, v242\n" \
"v_mfma_f32_16x16x32_bf16 v[100:103], a[92:95], a[220:223], v[100:103]\n" \
"buffer_load_dwordx4 v246, s[12:15], s46 offen lds\n" \
"v_mfma_f32_16x16x32_bf16 v[68:71], a[200:203], a[192:195], 0\n" \
"s_add_u32 s46, s46, 0x80\n" \
"ds_read_b128 a[88:91], v205 offset:8192\n" \
"v_cvt_pk_bf16_f32 v130, v132, v133\n" \
"v_mfma_f32_16x16x32_bf16 v[68:71], a[204:207], a[196:199], v[68:71]\n" \
"ds_read_b128 a[92:95], v206 offset:8192\n" \
"v_fma_f32 v242, v134, v135, v242\n" \
"v_mfma_f32_16x16x32_bf16 v[104:107], a[192:195], a[208:211], 0\n" \
"s_add_u32 m0, s84, 143360\n" \
"ds_read_b64_tr_b16 a[118:119], v210 offset:13312\n" \
"v_cvt_pk_bf16_f32 v131, v134, v135\n" \
"v_mfma_f32_16x16x32_bf16 v[104:107], a[196:199], a[212:215], v[104:107]\n" \
"buffer_store_dwordx4 v[128:131], v233, s[56:59], s78 offen offset:0 sc1\n" \
"global_load_lds_dwordx4 v253, s[48:49] offset:-4096\n" \
"v_mul_f32 v136, v136, v232\n" \
"v_mul_f32 v137, v137, v232\n" \
"v_mul_f32 v138, v138, v232\n" \
"v_mfma_f32_16x16x32_bf16 v[108:111], a[192:195], a[216:219], 0\n" \
"v_mul_f32 v139, v139, v232\n" \
"v_mul_f32 v140, v140, v232\n" \
"v_mfma_f32_16x16x32_bf16 v[108:111], a[196:199], a[220:223], v[108:111]\n" \
"s_waitcnt lgkmcnt(2)\n" \
"v_mul_f32 v141, v141, v232\n" \
"v_mul_f32 v142, v142, v232\n" \
"v_mfma_f32_16x16x32_bf16 v[32:35], a[96:99], a[64:67], v[32:35]\n" \
"ds_read_b128 a[192:195], v205 offset:12288\n" \
"v_mul_f32 v143, v143, v232\n" \
"v_mfma_f32_16x16x32_bf16 v[32:35], a[100:103], a[68:71], v[32:35]\n" \
"global_load_lds_dwordx4 v253, s[48:49] offset:-2048\n" \
"v_fma_f32 v242, v136, v137, v242\n" \
"v_mfma_f32_16x16x32_bf16 v[40:43], a[96:99], a[72:75], v[40:43]\n" \
"ds_read_b128 a[196:199], v206 offset:12288\n" \
"v_cvt_pk_bf16_f32 v136, v136, v137\n" \
"v_mfma_f32_16x16x32_bf16 v[40:43], a[100:103], a[76:79], v[40:43]\n" \
"ds_read_b64_tr_b16 a[200:201], v210 offset:2048\n" \
"v_fma_f32 v242, v138, v139, v242\n" \
"v_mfma_f32_16x16x32_bf16 v[0:3], a[104:107], a[72:75], v[0:3]\n" \
"ds_read_b64_tr_b16 a[202:203], v210 offset:3072\n" \
"v_cvt_pk_bf16_f32 v137, v138, v139\n" \
"v_mfma_f32_16x16x32_bf16 v[0:3], a[108:111], a[76:79], v[0:3]\n" \
"s_waitcnt lgkmcnt(3)\n" \
"global_load_lds_dwordx4 v253, s[48:49] offset:0\n" \
"v_fma_f32 v242, v140, v141, v242\n" \
"v_mfma_f32_16x16x32_bf16 v[4:7], a[112:115], a[72:75], v[4:7]\n" \
"s_add_u32 s48, s48, 0x60000\n" \
"s_addc_u32 s49, s49, 0\n" \
"ds_read_b64_tr_b16 a[204:205], v210 offset:14336\n" \
"v_mfma_f32_16x16x32_bf16 v[4:7], a[116:119], a[76:79], v[4:7]\n" \
"ds_read_b64_tr_b16 a[206:207], v210 offset:15360\n" \
"v_cvt_pk_bf16_f32 v138, v140, v141\n" \
"v_mfma_f32_16x16x32_bf16 v[48:51], a[96:99], a[80:83], v[48:51]\n" \
"ds_read_b64_tr_b16 a[208:209], v209 offset:4096\n" \
"v_fma_f32 v242, v142, v143, v242\n" \
"v_mfma_f32_16x16x32_bf16 v[48:51], a[100:103], a[84:87], v[48:51]\n" \
"global_load_lds_dwordx4 v253, s[50:51] offset:-3072\n" \
"v_cvt_pk_bf16_f32 v139, v142, v143\n" \
"v_mfma_f32_16x16x32_bf16 v[8:11], a[104:107], a[80:83], v[8:11]\n" \
"buffer_store_dwordx4 v[136:139], v233, s[56:59], s78 offen offset:2048 sc1\n" \
"ds_read_b64_tr_b16 a[210:211], v209 offset:5120\n" \
"s_add_u32 s78, s78, 0x1000\n" \
"v_mul_f32 v144, v144, v232\n" \
"v_mul_f32 v145, v145, v232\n" \
"v_mfma_f32_16x16x32_bf16 v[8:11], a[108:111], a[84:87], v[8:11]\n" \
"ds_read_b64_tr_b16 a[212:213], v209 offset:16384\n" \
"v_mul_f32 v146, v146, v232\n" \
"v_mfma_f32_16x16x32_bf16 v[12:15], a[112:115], a[80:83], v[12:15]\n" \
"ds_read_b64_tr_b16 a[214:215], v209 offset:17408\n" \
"v_mul_f32 v147, v147, v232\n" \
"v_mfma_f32_16x16x32_bf16 v[12:15], a[116:119], a[84:87], v[12:15]\n" \
"global_load_lds_dwordx4 v253, s[50:51] offset:-1024\n" \
"v_mul_f32 v148, v148, v232\n" \
"v_mfma_f32_16x16x32_bf16 v[56:59], a[96:99], a[88:91], v[56:59]\n" \
"ds_read_b64_tr_b16 a[216:217], v210 offset:4096\n" \
"v_mul_f32 v149, v149, v232\n" \
"v_mfma_f32_16x16x32_bf16 v[56:59], a[100:103], a[92:95], v[56:59]\n" \
"ds_read_b64_tr_b16 a[218:219], v210 offset:5120\n" \
"v_mul_f32 v150, v150, v232\n" \
"v_mfma_f32_16x16x32_bf16 v[16:19], a[104:107], a[88:91], v[16:19]\n" \
"ds_read_b64_tr_b16 a[220:221], v210 offset:16384\n" \
"v_mul_f32 v151, v151, v232\n" \
"v_mfma_f32_16x16x32_bf16 v[16:19], a[108:111], a[92:95], v[16:19]\n" \
"global_load_lds_dwordx4 v253, s[50:51] offset:1024\n" \
"v_fma_f32 v242, v144, v145, v242\n" \
"v_mfma_f32_16x16x32_bf16 v[20:23], a[112:115], a[88:91], v[20:23]\n" \
"s_add_u32 s50, s50, 0x60000\n" \
"s_addc_u32 s51, s51, 0\n" \
"ds_read_b64_tr_b16 a[222:223], v210 offset:17408\n" \
"v_mfma_f32_16x16x32_bf16 v[20:23], a[116:119], a[92:95], v[20:23]\n" \
"s_waitcnt lgkmcnt(3)\n" \
"v_add_u32 v207, 0xffff0000, v207\n" \
"v_add_u32 v208, 0xffff0000, v208\n" \
"v_mfma_f32_16x16x32_bf16 v[64:67], a[96:99], a[192:195], v[64:67]\n" \
"v_add_u32 v205, 0xffff0000, v205\n" \
"v_add_u32 v206, 0xffff0000, v206\n" \
"v_mfma_f32_16x16x32_bf16 v[64:67], a[100:103], a[196:199], v[64:67]\n" \
"v_add_u32 v209, 0xffff0000, v209\n" \
"v_add_u32 v210, 0xffff0000, v210\n" \
"v_mfma_f32_16x16x32_bf16 v[24:27], a[104:107], a[192:195], v[24:27]\n" \
"v_cvt_pk_bf16_f32 v144, v144, v145\n" \
"v_fma_f32 v242, v146, v147, v242\n" \
"v_mfma_f32_16x16x32_bf16 v[24:27], a[108:111], a[196:199], v[24:27]\n" \
"v_cvt_pk_bf16_f32 v145, v146, v147\n" \
"v_fma_f32 v242, v148, v149, v242\n" \
"v_mfma_f32_16x16x32_bf16 v[28:31], a[112:115], a[192:195], v[28:31]\n" \
"s_waitcnt vmcnt(13) lgkmcnt(0)\n" \
"v_cvt_pk_bf16_f32 v146, v148, v149\n" \
"v_fma_f32 v242, v150, v151, v242\n" \
"v_mfma_f32_16x16x32_bf16 v[28:31], a[116:119], a[196:199], v[28:31]\n" \
"s_barrier\n" \
"v_cvt_pk_bf16_f32 v147, v150, v151\n" \
"v_mfma_f32_16x16x32_bf16 v[72:75], a[64:67], a[208:211], v[72:75]\n" \
"buffer_store_dwordx4 v[144:147], v233, s[56:59], s78 offen offset:0 sc1\n" \
"ds_read_b64_tr_b16 a[96:97], v209 offset:2048\n" \
"v_mul_f32 v152, v152, v232\n" \
"v_mul_f32 v153, v153, v232\n" \
"v_mul_f32 v154, v154, v232\n" \
"v_mfma_f32_16x16x32_bf16 v[72:75], a[68:71], a[212:215], v[72:75]\n" \
"ds_read_b64_tr_b16 a[98:99], v209 offset:3072\n" \
"v_mul_f32 v155, v155, v232\n" \
"v_mfma_f32_16x16x32_bf16 v[44:47], a[200:203], a[72:75], v[44:47]\n" \
"s_add_u32 m0, s95, 65536\n" \
"ds_read_b128 a[64:67], v207 offset:0\n" \
"ds_read_b64_tr_b16 a[100:101], v209 offset:14336\n" \
"v_mfma_f32_16x16x32_bf16 v[44:47], a[204:207], a[76:79], v[44:47]\n" \
"global_load_lds_dwordx4 v252, s[42:43] offset:0\n" \
"v_mul_f32 v156, v156, v232\n" \
"v_mfma_f32_16x16x32_bf16 v[80:83], a[72:75], a[208:211], v[80:83]\n" \
"ds_read_b128 a[68:71], v208 offset:0\n" \
"ds_read_b64_tr_b16 a[102:103], v209 offset:15360\n" \
"v_mfma_f32_16x16x32_bf16 v[80:83], a[76:79], a[212:215], v[80:83]\n" \
"ds_read_b64_tr_b16 a[104:105], v209 offset:0\n" \
"v_mul_f32 v157, v157, v232\n" \
"v_mfma_f32_16x16x32_bf16 v[84:87], a[72:75], a[216:219], v[84:87]\n" \
"ds_read_b64_tr_b16 a[106:107], v209 offset:1024\n" \
"v_mul_f32 v158, v158, v232\n" \
"v_mfma_f32_16x16x32_bf16 v[84:87], a[76:79], a[220:223], v[84:87]\n" \
"global_load_lds_dwordx4 v252, s[42:43] offset:2048\n" \
"v_mul_f32 v159, v159, v232\n" \
"v_mfma_f32_16x16x32_bf16 v[52:55], a[200:203], a[80:83], v[52:55]\n" \
"s_add_u32 s42, s42, 0x100\n" \
"s_addc_u32 s43, s43, 0\n" \
"ds_read_b128 a[72:75], v205 offset:0\n" \
"v_mfma_f32_16x16x32_bf16 v[52:55], a[204:207], a[84:87], v[52:55]\n" \
"ds_read_b128 a[76:79], v206 offset:0\n" \
"v_fma_f32 v242, v152, v153, v242\n" \
"v_mfma_f32_16x16x32_bf16 v[88:91], a[80:83], a[208:211], v[88:91]\n" \
"ds_read_b64_tr_b16 a[108:109], v209 offset:12288\n" \
"v_cvt_pk_bf16_f32 v152, v152, v153\n" \
"v_mfma_f32_16x16x32_bf16 v[88:91], a[84:87], a[212:215], v[88:91]\n" \
"global_load_lds_dwordx4 v252, s[44:45] offset:1024\n" \
"v_fma_f32 v242, v154, v155, v242\n" \
"v_mfma_f32_16x16x32_bf16 v[92:95], a[80:83], a[216:219], v[92:95]\n" \
"ds_read_b64_tr_b16 a[110:111], v209 offset:13312\n" \
"v_cvt_pk_bf16_f32 v153, v154, v155\n" \
"v_mfma_f32_16x16x32_bf16 v[92:95], a[84:87], a[220:223], v[92:95]\n" \
"ds_read_b64_tr_b16 a[112:113], v210 offset:0\n" \
"v_fma_f32 v242, v156, v157, v242\n" \
"v_mfma_f32_16x16x32_bf16 v[60:63], a[200:203], a[88:91], v[60:63]\n" \
"ds_read_b128 a[80:83], v205 offset:4096\n" \
"v_cvt_pk_bf16_f32 v154, v156, v157\n" \
"v_mfma_f32_16x16x32_bf16 v[60:63], a[204:207], a[92:95], v[60:63]\n" \
"global_load_lds_dwordx4 v252, s[44:45] offset:3072\n" \
"v_fma_f32 v242, v158, v159, v242\n" \
"v_mfma_f32_16x16x32_bf16 v[96:99], a[88:91], a[208:211], v[96:99]\n" \
"s_add_u32 s44, s44, 0x100\n" \
"s_addc_u32 s45, s45, 0\n" \
"ds_read_b128 a[84:87], v206 offset:4096\n" \
"v_mfma_f32_16x16x32_bf16 v[96:99], a[92:95], a[212:215], v[96:99]\n" \
"ds_read_b64_tr_b16 a[114:115], v210 offset:1024\n" \
"v_cvt_pk_bf16_f32 v155, v158, v159\n" \
"v_mfma_f32_16x16x32_bf16 v[100:103], a[88:91], a[216:219], v[100:103]\n" \
"buffer_store_dwordx4 v[152:155], v233, s[56:59], s78 offen offset:2048 sc1\n" \
"s_add_u32 m0, s85, 81920\n" \
"ds_read_b64_tr_b16 a[116:117], v210 offset:12288\n" \
"s_add_u32 s78, s78, 0x81000\n" \
"v_mov_b32 v234, v243\n" \
"v_mfma_f32_16x16x32_bf16 v[100:103], a[92:95], a[220:223], v[100:103]\n" \
"buffer_load_dwordx4 v246, s[12:15], s46 offen lds\n" \
"v_mfma_f32_16x16x32_bf16 v[68:71], a[200:203], a[192:195], v[68:71]\n" \
"s_add_u32 s46, s46, 0x100\n" \
"ds_read_b128 a[88:91], v205 offset:8192\n" \
"v_permlane16_swap_b32 v234, v243\n" \
"v_mfma_f32_16x16x32_bf16 v[68:71], a[204:207], a[196:199], v[68:71]\n" \
"ds_read_b128 a[92:95], v206 offset:8192\n" \
"v_add_f32 v243, v243, v234\n" \
"v_mfma_f32_16x16x32_bf16 v[104:107], a[192:195], a[208:211], v[104:107]\n" \
"s_add_u32 m0, s84, 90112\n" \
"ds_read_b64_tr_b16 a[118:119], v210 offset:13312\n" \
"v_mov_b32 v234, v243\n" \
"v_mfma_f32_16x16x32_bf16 v[104:107], a[196:199], a[212:215], v[104:107]\n" \
"global_load_lds_dwordx4 v253, s[48:49] offset:-4096\n" \
"v_mfma_f32_16x16x32_bf16 v[108:111], a[192:195], a[216:219], v[108:111]\n" \
"v_permlane32_swap_b32 v234, v243\n" \
"v_add_f32 v243, v243, v234\n" \
"v_mfma_f32_16x16x32_bf16 v[108:111], a[196:199], a[220:223], v[108:111]\n" \
"s_waitcnt lgkmcnt(2)\n" \
"v_rcp_f32 v234, v243\n" \
"v_mfma_f32_16x16x32_bf16 v[32:35], a[96:99], a[64:67], v[32:35]\n" \
"ds_read_b128 a[192:195], v205 offset:12288\n" \
"v_mul_f32 v243, 0x4d800000, v243\n" \
"v_mfma_f32_16x16x32_bf16 v[32:35], a[100:103], a[68:71], v[32:35]\n" \
"global_load_lds_dwordx4 v253, s[48:49] offset:-2048\n" \
"v_add_u32 v235, 0x1c90080, v249\n" \
"v_mfma_f32_16x16x32_bf16 v[40:43], a[96:99], a[72:75], v[40:43]\n" \
"ds_read_b128 a[196:199], v206 offset:12288\n" \
"v_cndmask_b32 v235, v249, v235, s[16:17]\n" \
"v_mfma_f32_16x16x32_bf16 v[40:43], a[100:103], a[76:79], v[40:43]\n" \
"ds_read_b64_tr_b16 a[200:201], v210 offset:2048\n" \
"v_mul_f32 v160, v160, v234\n" \
"v_mfma_f32_16x16x32_bf16 v[0:3], a[104:107], a[72:75], v[0:3]\n" \
"ds_read_b64_tr_b16 a[202:203], v210 offset:3072\n" \
"v_mul_f32 v161, v161, v234\n" \
"v_mfma_f32_16x16x32_bf16 v[0:3], a[108:111], a[76:79], v[0:3]\n" \
"s_waitcnt lgkmcnt(3)\n" \
"global_load_lds_dwordx4 v253, s[48:49] offset:0\n" \
"v_mul_f32 v162, v162, v234\n" \
"v_mfma_f32_16x16x32_bf16 v[4:7], a[112:115], a[72:75], v[4:7]\n" \
"s_add_u32 s48, s48, 0xc0000\n" \
"s_addc_u32 s49, s49, 0\n" \
"ds_read_b64_tr_b16 a[204:205], v210 offset:14336\n" \
"v_mfma_f32_16x16x32_bf16 v[4:7], a[116:119], a[76:79], v[4:7]\n" \
"ds_read_b64_tr_b16 a[206:207], v210 offset:15360\n" \
"v_mul_f32 v163, v163, v234\n" \
"v_mfma_f32_16x16x32_bf16 v[48:51], a[96:99], a[80:83], v[48:51]\n" \
"ds_read_b64_tr_b16 a[208:209], v209 offset:4096\n" \
"v_mul_f32 v164, v164, v234\n" \
"v_mfma_f32_16x16x32_bf16 v[48:51], a[100:103], a[84:87], v[48:51]\n" \
"global_load_lds_dwordx4 v253, s[50:51] offset:-3072\n" \
"v_mul_f32 v165, v165, v234\n" \
"v_mfma_f32_16x16x32_bf16 v[8:11], a[104:107], a[80:83], v[8:11]\n" \
"ds_read_b64_tr_b16 a[210:211], v209 offset:5120\n" \
"v_mul_f32 v166, v166, v234\n" \
"v_mfma_f32_16x16x32_bf16 v[8:11], a[108:111], a[84:87], v[8:11]\n" \
"ds_read_b64_tr_b16 a[212:213], v209 offset:16384\n" \
"v_mul_f32 v167, v167, v234\n" \
"v_mfma_f32_16x16x32_bf16 v[12:15], a[112:115], a[80:83], v[12:15]\n" \
"ds_read_b64_tr_b16 a[214:215], v209 offset:17408\n" \
"v_fma_f32 v243, v160, v161, v243\n" \
"v_mfma_f32_16x16x32_bf16 v[12:15], a[116:119], a[84:87], v[12:15]\n" \
"global_load_lds_dwordx4 v253, s[50:51] offset:-1024\n" \
"v_cvt_pk_bf16_f32 v160, v160, v161\n" \
"v_mfma_f32_16x16x32_bf16 v[56:59], a[96:99], a[88:91], v[56:59]\n" \
"ds_read_b64_tr_b16 a[216:217], v210 offset:4096\n" \
"v_fma_f32 v243, v162, v163, v243\n" \
"v_mfma_f32_16x16x32_bf16 v[56:59], a[100:103], a[92:95], v[56:59]\n" \
"ds_read_b64_tr_b16 a[218:219], v210 offset:5120\n" \
"v_cvt_pk_bf16_f32 v161, v162, v163\n" \
"v_mfma_f32_16x16x32_bf16 v[16:19], a[104:107], a[88:91], v[16:19]\n" \
"ds_read_b64_tr_b16 a[220:221], v210 offset:16384\n" \
"v_fma_f32 v243, v164, v165, v243\n" \
"v_mfma_f32_16x16x32_bf16 v[16:19], a[108:111], a[92:95], v[16:19]\n" \
"global_load_lds_dwordx4 v253, s[50:51] offset:1024\n" \
"v_cvt_pk_bf16_f32 v162, v164, v165\n" \
"v_mfma_f32_16x16x32_bf16 v[20:23], a[112:115], a[88:91], v[20:23]\n" \
"s_add_u32 s50, s50, 0xc0000\n" \
"s_addc_u32 s51, s51, 0\n" \
"ds_read_b64_tr_b16 a[222:223], v210 offset:17408\n" \
"v_mfma_f32_16x16x32_bf16 v[20:23], a[116:119], a[92:95], v[20:23]\n" \
"s_waitcnt lgkmcnt(3)\n" \
"v_add_u32 v207, 0x1d000, v207\n" \
"v_add_u32 v208, 0x1d000, v208\n" \
"v_mfma_f32_16x16x32_bf16 v[64:67], a[96:99], a[192:195], v[64:67]\n" \
"v_add_u32 v205, 0x1d000, v205\n" \
"v_add_u32 v206, 0x1d000, v206\n" \
"v_mfma_f32_16x16x32_bf16 v[64:67], a[100:103], a[196:199], v[64:67]\n" \
"v_add_u32 v209, 0x1d000, v209\n" \
"v_add_u32 v210, 0x1d000, v210\n" \
"v_mfma_f32_16x16x32_bf16 v[24:27], a[104:107], a[192:195], v[24:27]\n" \
"v_fma_f32 v243, v166, v167, v243\n" \
"v_cvt_pk_bf16_f32 v163, v166, v167\n" \
"v_mfma_f32_16x16x32_bf16 v[24:27], a[108:111], a[196:199], v[24:27]\n" \
"buffer_store_dwordx4 v[160:163], v235, s[56:59], s78 offen offset:0 sc1\n" \
"v_mul_f32 v168, v168, v234\n" \
"v_mul_f32 v169, v169, v234\n" \
"v_mul_f32 v170, v170, v234\n" \
"v_mul_f32 v171, v171, v234\n" \
"v_mfma_f32_16x16x32_bf16 v[28:31], a[112:115], a[192:195], v[28:31]\n" \
"s_waitcnt vmcnt(14) lgkmcnt(0)\n" \
"v_mul_f32 v172, v172, v234\n" \
"v_mul_f32 v173, v173, v234\n" \
"v_mfma_f32_16x16x32_bf16 v[28:31], a[116:119], a[196:199], v[28:31]\n" \
"s_barrier\n" \
"v_mul_f32 v174, v174, v234\n" \
"v_mfma_f32_16x16x32_bf16 v[72:75], a[64:67], a[208:211], v[72:75]\n" \
"ds_read_b64_tr_b16 a[96:97], v209 offset:2048\n" \
"v_mul_f32 v175, v175, v234\n" \
"v_mfma_f32_16x16x32_bf16 v[72:75], a[68:71], a[212:215], v[72:75]\n" \
"ds_read_b64_tr_b16 a[98:99], v209 offset:3072\n" \
"v_fma_f32 v243, v168, v169, v243\n" \
"v_mfma_f32_16x16x32_bf16 v[44:47], a[200:203], a[72:75], v[44:47]\n" \
"s_add_u32 m0, s95, 0\n" \
"ds_read_b128 a[64:67], v207 offset:0\n" \
"ds_read_b64_tr_b16 a[100:101], v209 offset:14336\n" \
"v_mfma_f32_16x16x32_bf16 v[44:47], a[204:207], a[76:79], v[44:47]\n" \
"global_load_lds_dwordx4 v252, s[42:43] offset:0\n" \
"v_cvt_pk_bf16_f32 v168, v168, v169\n" \
"v_mfma_f32_16x16x32_bf16 v[80:83], a[72:75], a[208:211], v[80:83]\n" \
"ds_read_b128 a[68:71], v208 offset:0\n" \
"ds_read_b64_tr_b16 a[102:103], v209 offset:15360\n" \
"v_mfma_f32_16x16x32_bf16 v[80:83], a[76:79], a[212:215], v[80:83]\n" \
"ds_read_b64_tr_b16 a[104:105], v209 offset:0\n" \
"v_fma_f32 v243, v170, v171, v243\n" \
"v_mfma_f32_16x16x32_bf16 v[84:87], a[72:75], a[216:219], v[84:87]\n" \
"Lpg9352_%=:\n" \
"Lst1x3_%=:\n" \
"s_cbranch_execz Lpg11613_%=\n" \
"ds_read_b64_tr_b16 a[106:107], v209 offset:1024\n" \
"v_cvt_pk_bf16_f32 v169, v170, v171\n" \
"v_mfma_f32_16x16x32_bf16 v[84:87], a[76:79], a[220:223], v[84:87]\n" \
"global_load_lds_dwordx4 v252, s[42:43] offset:2048\n" \
"v_fma_f32 v243, v172, v173, v243\n" \
"v_mfma_f32_16x16x32_bf16 v[52:55], a[200:203], a[80:83], v[52:55]\n" \
"s_add_u32 s42, s42, 0x80\n" \
"s_addc_u32 s43, s43, 0\n" \
"ds_read_b128 a[72:75], v205 offset:0\n" \
"v_mfma_f32_16x16x32_bf16 v[52:55], a[204:207], a[84:87], v[52:55]\n" \
"ds_read_b128 a[76:79], v206 offset:0\n" \
"v_cvt_pk_bf16_f32 v170, v172, v173\n" \
"v_mfma_f32_16x16x32_bf16 v[88:91], a[80:83], a[208:211], v[88:91]\n" \
"ds_read_b64_tr_b16 a[108:109], v209 offset:12288\n" \
"v_fma_f32 v243, v174, v175, v243\n" \
"v_mfma_f32_16x16x32_bf16 v[88:91], a[84:87], a[212:215], v[88:91]\n" \
"global_load_lds_dwordx4 v252, s[44:45] offset:1024\n" \
"v_cvt_pk_bf16_f32 v171, v174, v175\n" \
"v_mfma_f32_16x16x32_bf16 v[92:95], a[80:83], a[216:219], v[92:95]\n" \
"buffer_store_dwordx4 v[168:171], v235, s[56:59], s78 offen offset:2048 sc1\n" \
"ds_read_b64_tr_b16 a[110:111], v209 offset:13312\n" \
"s_add_u32 s78, s78, 0x1000\n" \
"v_mul_f32 v176, v176, v234\n" \
"v_mul_f32 v177, v177, v234\n" \
"v_mfma_f32_16x16x32_bf16 v[92:95], a[84:87], a[220:223], v[92:95]\n" \
"ds_read_b64_tr_b16 a[112:113], v210 offset:0\n" \
"v_mul_f32 v178, v178, v234\n" \
"v_mfma_f32_16x16x32_bf16 v[60:63], a[200:203], a[88:91], v[60:63]\n" \
"ds_read_b128 a[80:83], v205 offset:4096\n" \
"v_mul_f32 v179, v179, v234\n" \
"v_mfma_f32_16x16x32_bf16 v[60:63], a[204:207], a[92:95], v[60:63]\n" \
"global_load_lds_dwordx4 v252, s[44:45] offset:3072\n" \
"v_mul_f32 v180, v180, v234\n" \
"v_mfma_f32_16x16x32_bf16 v[96:99], a[88:91], a[208:211], v[96:99]\n" \
"s_add_u32 s44, s44, 0x80\n" \
"s_addc_u32 s45, s45, 0\n" \
"ds_read_b128 a[84:87], v206 offset:4096\n" \
"v_mfma_f32_16x16x32_bf16 v[96:99], a[92:95], a[212:215], v[96:99]\n" \
"ds_read_b64_tr_b16 a[114:115], v210 offset:1024\n" \
"v_mul_f32 v181, v181, v234\n" \
"v_mfma_f32_16x16x32_bf16 v[100:103], a[88:91], a[216:219], v[100:103]\n" \
"s_add_u32 m0, s85, 16384\n" \
"ds_read_b64_tr_b16 a[116:117], v210 offset:12288\n" \
"v_mul_f32 v182, v182, v234\n" \
"v_mfma_f32_16x16x32_bf16 v[100:103], a[92:95], a[220:223], v[100:103]\n" \
"buffer_load_dwordx4 v246, s[12:15], s46 offen lds\n" \
"v_mfma_f32_16x16x32_bf16 v[68:71], a[200:203], a[192:195], v[68:71]\n" \
"s_add_u32 s46, s46, 0x80\n" \
"ds_read_b128 a[88:91], v205 offset:8192\n" \
"v_mul_f32 v183, v183, v234\n" \
"v_mfma_f32_16x16x32_bf16 v[68:71], a[204:207], a[196:199], v[68:71]\n" \
"ds_read_b128 a[92:95], v206 offset:8192\n" \
"v_fma_f32 v243, v176, v177, v243\n" \
"v_mfma_f32_16x16x32_bf16 v[104:107], a[192:195], a[208:211], v[104:107]\n" \
"s_add_u32 m0, s84, 24576\n" \
"ds_read_b64_tr_b16 a[118:119], v210 offset:13312\n" \
"v_cvt_pk_bf16_f32 v176, v176, v177\n" \
"v_mfma_f32_16x16x32_bf16 v[104:107], a[196:199], a[212:215], v[104:107]\n" \
"global_load_lds_dwordx4 v253, s[48:49] offset:-4096\n" \
"v_fma_f32 v243, v178, v179, v243\n" \
"v_mfma_f32_16x16x32_bf16 v[108:111], a[192:195], a[216:219], v[108:111]\n" \
"v_cvt_pk_bf16_f32 v177, v178, v179\n" \
"v_fma_f32 v243, v180, v181, v243\n" \
"v_mfma_f32_16x16x32_bf16 v[108:111], a[196:199], a[220:223], v[108:111]\n" \
"s_waitcnt lgkmcnt(2)\n" \
"v_cvt_pk_bf16_f32 v178, v180, v181\n" \
"v_fma_f32 v243, v182, v183, v243\n" \
"v_mfma_f32_16x16x32_bf16 v[32:35], a[96:99], a[64:67], v[32:35]\n" \
"ds_read_b128 a[192:195], v205 offset:12288\n" \
"v_cvt_pk_bf16_f32 v179, v182, v183\n" \
"v_mfma_f32_16x16x32_bf16 v[32:35], a[100:103], a[68:71], v[32:35]\n" \
"buffer_store_dwordx4 v[176:179], v235, s[56:59], s78 offen offset:0 sc1\n" \
"global_load_lds_dwordx4 v253, s[48:49] offset:-2048\n" \
"v_mul_f32 v184, v184, v234\n" \
"v_mul_f32 v185, v185, v234\n" \
"v_mul_f32 v186, v186, v234\n" \
"v_mfma_f32_16x16x32_bf16 v[40:43], a[96:99], a[72:75], v[40:43]\n" \
"ds_read_b128 a[196:199], v206 offset:12288\n" \
"v_mul_f32 v187, v187, v234\n" \
"v_mfma_f32_16x16x32_bf16 v[40:43], a[100:103], a[76:79], v[40:43]\n" \
"ds_read_b64_tr_b16 a[200:201], v210 offset:2048\n" \
"v_mul_f32 v188, v188, v234\n" \
"v_mfma_f32_16x16x32_bf16 v[0:3], a[104:107], a[72:75], v[0:3]\n" \
"ds_read_b64_tr_b16 a[202:203], v210 offset:3072\n" \
"v_mul_f32 v189, v189, v234\n" \
"v_mfma_f32_16x16x32_bf16 v[0:3], a[108:111], a[76:79], v[0:3]\n" \
"s_waitcnt lgkmcnt(3)\n" \
"global_load_lds_dwordx4 v253, s[48:49] offset:0\n" \
"v_mul_f32 v190, v190, v234\n" \
"v_mfma_f32_16x16x32_bf16 v[4:7], a[112:115], a[72:75], v[4:7]\n" \
"s_add_u32 s48, s48, 0x60000\n" \
"s_addc_u32 s49, s49, 0\n" \
"ds_read_b64_tr_b16 a[204:205], v210 offset:14336\n" \
"v_mfma_f32_16x16x32_bf16 v[4:7], a[116:119], a[76:79], v[4:7]\n" \
"ds_read_b64_tr_b16 a[206:207], v210 offset:15360\n" \
"v_mul_f32 v191, v191, v234\n" \
"v_mfma_f32_16x16x32_bf16 v[48:51], a[96:99], a[80:83], v[48:51]\n" \
"ds_read_b64_tr_b16 a[208:209], v209 offset:4096\n" \
"v_fma_f32 v243, v184, v185, v243\n" \
"v_mfma_f32_16x16x32_bf16 v[48:51], a[100:103], a[84:87], v[48:51]\n" \
"global_load_lds_dwordx4 v253, s[50:51] offset:-3072\n" \
"v_cvt_pk_bf16_f32 v184, v184, v185\n" \
"v_mfma_f32_16x16x32_bf16 v[8:11], a[104:107], a[80:83], v[8:11]\n" \
"ds_read_b64_tr_b16 a[210:211], v209 offset:5120\n" \
"v_fma_f32 v243, v186, v187, v243\n" \
"v_mfma_f32_16x16x32_bf16 v[8:11], a[108:111], a[84:87], v[8:11]\n" \
"ds_read_b64_tr_b16 a[212:213], v209 offset:16384\n" \
"v_cvt_pk_bf16_f32 v185, v186, v187\n" \
"v_mfma_f32_16x16x32_bf16 v[12:15], a[112:115], a[80:83], v[12:15]\n" \
"ds_read_b64_tr_b16 a[214:215], v209 offset:17408\n" \
"v_fma_f32 v243, v188, v189, v243\n" \
"v_mfma_f32_16x16x32_bf16 v[12:15], a[116:119], a[84:87], v[12:15]\n" \
"global_load_lds_dwordx4 v253, s[50:51] offset:-1024\n" \
"v_cvt_pk_bf16_f32 v186, v188, v189\n" \
"v_mfma_f32_16x16x32_bf16 v[56:59], a[96:99], a[88:91], v[56:59]\n" \
"ds_read_b64_tr_b16 a[216:217], v210 offset:4096\n" \
"v_fma_f32 v243, v190, v191, v243\n" \
"v_mfma_f32_16x16x32_bf16 v[56:59], a[100:103], a[92:95], v[56:59]\n" \
"ds_read_b64_tr_b16 a[218:219], v210 offset:5120\n" \
"v_cvt_pk_bf16_f32 v187, v190, v191\n" \
"v_mfma_f32_16x16x32_bf16 v[16:19], a[104:107], a[88:91], v[16:19]\n" \
"buffer_store_dwordx4 v[184:187], v235, s[56:59], s78 offen offset:2048 sc1\n" \
"ds_read_b64_tr_b16 a[220:221], v210 offset:16384\n" \
"s_add_u32 s78, s78, 0xfff7d000\n" \
"v_add_f32 v240, v240, v241\n" \
"v_add_f32 v242, v242, v243\n" \
"v_mfma_f32_16x16x32_bf16 v[16:19], a[108:111], a[92:95], v[16:19]\n" \
"global_load_lds_dwordx4 v253, s[50:51] offset:1024\n" \
"v_mov_b32 v241, 0x207\n" \
"v_mfma_f32_16x16x32_bf16 v[20:23], a[112:115], a[88:91], v[20:23]\n" \
"s_add_u32 s50, s50, 0x60000\n" \
"s_addc_u32 s51, s51, 0\n" \
"ds_read_b64_tr_b16 a[222:223], v210 offset:17408\n" \
"v_mfma_f32_16x16x32_bf16 v[20:23], a[116:119], a[92:95], v[20:23]\n" \
"s_waitcnt lgkmcnt(3)\n" \
"v_add_u32 v207, 0xffff3000, v207\n" \
"v_add_u32 v208, 0xffff3000, v208\n" \
"v_mfma_f32_16x16x32_bf16 v[64:67], a[96:99], a[192:195], v[64:67]\n" \
"v_add_u32 v205, 0xffff3000, v205\n" \
"v_add_u32 v206, 0xffff3000, v206\n" \
"v_mfma_f32_16x16x32_bf16 v[64:67], a[100:103], a[196:199], v[64:67]\n" \
"v_add_u32 v209, 0xffff3000, v209\n" \
"v_add_u32 v210, 0xffff3000, v210\n" \
"v_mfma_f32_16x16x32_bf16 v[24:27], a[104:107], a[192:195], v[24:27]\n" \
"v_add_f32 v240, v240, v242\n" \
"v_cmp_class_f32 vcc, v240, v241\n" \
"v_mfma_f32_16x16x32_bf16 v[24:27], a[108:111], a[196:199], v[24:27]\n" \
"v_mfma_f32_16x16x32_bf16 v[28:31], a[112:115], a[192:195], v[28:31]\n" \
"s_waitcnt vmcnt(15) lgkmcnt(0)\n" \
"s_cbranch_vccnz Lexact_%=\n" \
"Lexr_%=:\n" \
"v_mfma_f32_16x16x32_bf16 v[28:31], a[116:119], a[196:199], v[28:31]\n" \
"s_barrier\n" \
"v_mfma_f32_16x16x32_bf16 v[72:75], a[64:67], a[208:211], v[72:75]\n" \
"ds_read_b64_tr_b16 a[96:97], v209 offset:2048\n" \
"v_mfma_f32_16x16x32_bf16 v[72:75], a[68:71], a[212:215], v[72:75]\n" \
"ds_read_b64_tr_b16 a[98:99], v209 offset:3072\n" \
"v_mfma_f32_16x16x32_bf16 v[44:47], a[200:203], a[72:75], v[44:47]\n" \
"s_add_u32 m0, s95, 118784\n" \
"ds_read_b128 a[64:67], v207 offset:0\n" \
"ds_read_b64_tr_b16 a[100:101], v209 offset:14336\n" \
"v_mfma_f32_16x16x32_bf16 v[44:47], a[204:207], a[76:79], v[44:47]\n" \
"global_load_lds_dwordx4 v252, s[42:43] offset:0\n" \
"v_mfma_f32_16x16x32_bf16 v[80:83], a[72:75], a[208:211], v[80:83]\n" \
"ds_read_b128 a[68:71], v208 offset:0\n" \
"ds_read_b64_tr_b16 a[102:103], v209 offset:15360\n" \
"v_mfma_f32_16x16x32_bf16 v[80:83], a[76:79], a[212:215], v[80:83]\n" \
"ds_read_b64_tr_b16 a[104:105], v209 offset:0\n" \
"v_mfma_f32_16x16x32_bf16 v[84:87], a[72:75], a[216:219], v[84:87]\n" \
"ds_read_b64_tr_b16 a[106:107], v209 offset:1024\n" \
"v_mfma_f32_16x16x32_bf16 v[84:87], a[76:79], a[220:223], v[84:87]\n" \
"global_load_lds_dwordx4 v252, s[42:43] offset:2048\n" \
"v_mfma_f32_16x16x32_bf16 v[52:55], a[200:203], a[80:83], v[52:55]\n" \
"s_add_u32 s42, s42, 0x80\n" \
"s_addc_u32 s43, s43, 0\n" \
"ds_read_b128 a[72:75], v205 offset:0\n" \
"v_mfma_f32_16x16x32_bf16 v[52:55], a[204:207], a[84:87], v[52:55]\n" \
"ds_read_b128 a[76:79], v206 offset:0\n" \
"v_mfma_f32_16x16x32_bf16 v[88:91], a[80:83], a[208:211], v[88:91]\n" \
"ds_read_b64_tr_b16 a[108:109], v209 offset:12288\n" \
"v_mfma_f32_16x16x32_bf16 v[88:91], a[84:87], a[212:215], v[88:91]\n" \
"global_load_lds_dwordx4 v252, s[44:45] offset:1024\n" \
"v_mfma_f32_16x16x32_bf16 v[92:95], a[80:83], a[216:219], v[92:95]\n" \
"ds_read_b64_tr_b16 a[110:111], v209 offset:13312\n" \
"v_mfma_f32_16x16x32_bf16 v[92:95], a[84:87], a[220:223], v[92:95]\n" \
"ds_read_b64_tr_b16 a[112:113], v210 offset:0\n" \
"v_mfma_f32_16x16x32_bf16 v[60:63], a[200:203], a[88:91], v[60:63]\n" \
"ds_read_b128 a[80:83], v205 offset:4096\n" \
"v_mfma_f32_16x16x32_bf16 v[60:63], a[204:207], a[92:95], v[60:63]\n" \
"global_load_lds_dwordx4 v252, s[44:45] offset:3072\n" \
"v_mfma_f32_16x16x32_bf16 v[96:99], a[88:91], a[208:211], v[96:99]\n" \
"s_add_u32 s44, s44, 0x80\n" \
"s_addc_u32 s45, s45, 0\n" \
"ds_read_b128 a[84:87], v206 offset:4096\n" \
"v_mfma_f32_16x16x32_bf16 v[96:99], a[92:95], a[212:215], v[96:99]\n" \
"ds_read_b64_tr_b16 a[114:115], v210 offset:1024\n" \
"v_mfma_f32_16x16x32_bf16 v[100:103], a[88:91], a[216:219], v[100:103]\n" \
"s_add_u32 m0, s85, 135168\n" \
"ds_read_b64_tr_b16 a[116:117], v210 offset:12288\n" \
"v_mfma_f32_16x16x32_bf16 v[100:103], a[92:95], a[220:223], v[100:103]\n" \
"buffer_load_dwordx4 v246, s[12:15], s46 offen lds\n" \
"v_mfma_f32_16x16x32_bf16 v[68:71], a[200:203], a[192:195], v[68:71]\n" \
"ds_read_b128 a[88:91], v205 offset:8192\n" \
"s_add_u32 s46, s46, 0x80\n" \
"v_mfma_f32_16x16x32_bf16 v[68:71], a[204:207], a[196:199], v[68:71]\n" \
"ds_read_b128 a[92:95], v206 offset:8192\n" \
"v_mfma_f32_16x16x32_bf16 v[104:107], a[192:195], a[208:211], v[104:107]\n" \
"s_add_u32 m0, s84, 143360\n" \
"ds_read_b64_tr_b16 a[118:119], v210 offset:13312\n" \
"v_mfma_f32_16x16x32_bf16 v[104:107], a[196:199], a[212:215], v[104:107]\n" \
"global_load_lds_dwordx4 v253, s[48:49] offset:-4096\n" \
"v_mfma_f32_16x16x32_bf16 v[108:111], a[192:195], a[216:219], v[108:111]\n" \
"v_mfma_f32_16x16x32_bf16 v[108:111], a[196:199], a[220:223], v[108:111]\n" \
"s_waitcnt lgkmcnt(2)\n" \
"v_mfma_f32_16x16x32_bf16 v[32:35], a[96:99], a[64:67], v[32:35]\n" \
"ds_read_b128 a[192:195], v205 offset:12288\n" \
"v_mfma_f32_16x16x32_bf16 v[32:35], a[100:103], a[68:71], v[32:35]\n" \
"global_load_lds_dwordx4 v253, s[48:49] offset:-2048\n" \
"v_mfma_f32_16x16x32_bf16 v[40:43], a[96:99], a[72:75], v[40:43]\n" \
"ds_read_b128 a[196:199], v206 offset:12288\n" \
"v_mfma_f32_16x16x32_bf16 v[40:43], a[100:103], a[76:79], v[40:43]\n" \
"ds_read_b64_tr_b16 a[200:201], v210 offset:2048\n" \
"v_mfma_f32_16x16x32_bf16 v[0:3], a[104:107], a[72:75], v[0:3]\n" \
"ds_read_b64_tr_b16 a[202:203], v210 offset:3072\n" \
"v_mfma_f32_16x16x32_bf16 v[0:3], a[108:111], a[76:79], v[0:3]\n" \
"s_waitcnt lgkmcnt(3)\n" \
"global_load_lds_dwordx4 v253, s[48:49] offset:0\n" \
"v_mfma_f32_16x16x32_bf16 v[4:7], a[112:115], a[72:75], v[4:7]\n" \
"s_add_u32 s48, s48, 0x60000\n" \
"s_addc_u32 s49, s49, 0\n" \
"ds_read_b64_tr_b16 a[204:205], v210 offset:14336\n" \
"v_mfma_f32_16x16x32_bf16 v[4:7], a[116:119], a[76:79], v[4:7]\n" \
"ds_read_b64_tr_b16 a[206:207], v210 offset:15360\n" \
"v_mfma_f32_16x16x32_bf16 v[48:51], a[96:99], a[80:83], v[48:51]\n" \
"ds_read_b64_tr_b16 a[208:209], v209 offset:4096\n" \
"v_mfma_f32_16x16x32_bf16 v[48:51], a[100:103], a[84:87], v[48:51]\n" \
"global_load_lds_dwordx4 v253, s[50:51] offset:-3072\n" \
"v_mfma_f32_16x16x32_bf16 v[8:11], a[104:107], a[80:83], v[8:11]\n" \
"ds_read_b64_tr_b16 a[210:211], v209 offset:5120\n" \
"v_mfma_f32_16x16x32_bf16 v[8:11], a[108:111], a[84:87], v[8:11]\n" \
"ds_read_b64_tr_b16 a[212:213], v209 offset:16384\n" \
"v_mfma_f32_16x16x32_bf16 v[12:15], a[112:115], a[80:83], v[12:15]\n" \
"ds_read_b64_tr_b16 a[214:215], v209 offset:17408\n" \
"v_mfma_f32_16x16x32_bf16 v[12:15], a[116:119], a[84:87], v[12:15]\n" \
"global_load_lds_dwordx4 v253, s[50:51] offset:-1024\n" \
"v_mfma_f32_16x16x32_bf16 v[56:59], a[96:99], a[88:91], v[56:59]\n" \
"ds_read_b64_tr_b16 a[216:217], v210 offset:4096\n" \
"v_mfma_f32_16x16x32_bf16 v[56:59], a[100:103], a[92:95], v[56:59]\n" \
"ds_read_b64_tr_b16 a[218:219], v210 offset:5120\n" \
"v_mfma_f32_16x16x32_bf16 v[16:19], a[104:107], a[88:91], v[16:19]\n" \
"ds_read_b64_tr_b16 a[220:221], v210 offset:16384\n" \
"v_mfma_f32_16x16x32_bf16 v[16:19], a[108:111], a[92:95], v[16:19]\n" \
"global_load_lds_dwordx4 v253, s[50:51] offset:1024\n" \
"v_mfma_f32_16x16x32_bf16 v[20:23], a[112:115], a[88:91], v[20:23]\n" \
"s_add_u32 s50, s50, 0x60000\n" \
"s_addc_u32 s51, s51, 0\n" \
"ds_read_b64_tr_b16 a[222:223], v210 offset:17408\n" \
"v_mfma_f32_16x16x32_bf16 v[20:23], a[116:119], a[92:95], v[20:23]\n" \
"s_waitcnt lgkmcnt(3)\n" \
"v_add_u32 v207, 0xffff0000, v207\n" \
"v_add_u32 v208, 0xffff0000, v208\n" \
"v_mfma_f32_16x16x32_bf16 v[64:67], a[96:99], a[192:195], v[64:67]\n" \
"v_add_u32 v205, 0xffff0000, v205\n" \
"v_add_u32 v206, 0xffff0000, v206\n" \
"v_mfma_f32_16x16x32_bf16 v[64:67], a[100:103], a[196:199], v[64:67]\n" \
"v_add_u32 v209, 0xffff0000, v209\n" \
"v_add_u32 v210, 0xffff0000, v210\n" \
"v_mfma_f32_16x16x32_bf16 v[24:27], a[104:107], a[192:195], v[24:27]\n" \
"v_mfma_f32_16x16x32_bf16 v[24:27], a[108:111], a[196:199], v[24:27]\n" \
"s_lshl_b32 s70, s63, 7\n" \
"s_lshl_b32 s71, s62, 2\n" \
"s_add_u32 s71, s71, 65589248\n" \
"v_mfma_f32_16x16x32_bf16 v[28:31], a[112:115], a[192:195], v[28:31]\n" \
"s_waitcnt vmcnt(11) lgkmcnt(0)\n" \
"s_add_u32 s72, s71, s70\n" \
"s_sub_u32 s71, s71, s70\n" \
"v_mfma_f32_16x16x32_bf16 v[28:31], a[116:119], a[196:199], v[28:31]\n" \
"s_barrier\n" \
"v_mfma_f32_16x16x32_bf16 v[72:75], a[64:67], a[208:211], v[72:75]\n" \
"s_add_u32 s71, s71, 1920\n" \
"ds_read_b64_tr_b16 a[96:97], v209 offset:2048\n" \
"v_mov_b32 v236, s60\n" \
"v_mfma_f32_16x16x32_bf16 v[72:75], a[68:71], a[212:215], v[72:75]\n" \
"ds_read_b64_tr_b16 a[98:99], v209 offset:3072\n" \
"v_lshlrev_b32 v237, 28, s61\n" \
"v_mfma_f32_16x16x32_bf16 v[44:47], a[200:203], a[72:75], v[44:47]\n" \
"s_add_u32 m0, s95, 65536\n" \
"ds_read_b128 a[64:67], v207 offset:0\n" \
"ds_read_b64_tr_b16 a[100:101], v209 offset:14336\n" \
"v_mfma_f32_16x16x32_bf16 v[44:47], a[204:207], a[76:79], v[44:47]\n" \
"buffer_store_dword v236, v237, s[56:59], s72 offen sc1\n" \
"global_load_lds_dwordx4 v252, s[42:43] offset:0\n" \
"v_mfma_f32_16x16x32_bf16 v[80:83], a[72:75], a[208:211], v[80:83]\n" \
"ds_read_b128 a[68:71], v208 offset:0\n" \
"ds_read_b64_tr_b16 a[102:103], v209 offset:15360\n" \
"v_mfma_f32_16x16x32_bf16 v[80:83], a[76:79], a[212:215], v[80:83]\n" \
"buffer_store_dword v236, v237, s[56:59], s71 offen sc1\n" \
"ds_read_b64_tr_b16 a[104:105], v209 offset:0\n" \
"v_mfma_f32_16x16x32_bf16 v[84:87], a[72:75], a[216:219], v[84:87]\n" \
"ds_read_b64_tr_b16 a[106:107], v209 offset:1024\n" \
"v_mfma_f32_16x16x32_bf16 v[84:87], a[76:79], a[220:223], v[84:87]\n" \
"global_load_lds_dwordx4 v252, s[42:43] offset:2048\n" \
"v_mfma_f32_16x16x32_bf16 v[52:55], a[200:203], a[80:83], v[52:55]\n" \
"s_add_u32 s42, s42, 0x80\n" \
"s_addc_u32 s43, s43, 0\n" \
"ds_read_b128 a[72:75], v205 offset:0\n" \
"v_mfma_f32_16x16x32_bf16 v[52:55], a[204:207], a[84:87], v[52:55]\n" \
"ds_read_b128 a[76:79], v206 offset:0\n" \
"v_mfma_f32_16x16x32_bf16 v[88:91], a[80:83], a[208:211], v[88:91]\n" \
"ds_read_b64_tr_b16 a[108:109], v209 offset:12288\n" \
"v_mfma_f32_16x16x32_bf16 v[88:91], a[84:87], a[212:215], v[88:91]\n" \
"global_load_lds_dwordx4 v252, s[44:45] offset:1024\n" \
"v_mfma_f32_16x16x32_bf16 v[92:95], a[80:83], a[216:219], v[92:95]\n" \
"ds_read_b64_tr_b16 a[110:111], v209 offset:13312\n" \
"v_mfma_f32_16x16x32_bf16 v[92:95], a[84:87], a[220:223], v[92:95]\n" \
"ds_read_b64_tr_b16 a[112:113], v210 offset:0\n" \
"v_mfma_f32_16x16x32_bf16 v[60:63], a[200:203], a[88:91], v[60:63]\n" \
"ds_read_b128 a[80:83], v205 offset:4096\n" \
"v_mfma_f32_16x16x32_bf16 v[60:63], a[204:207], a[92:95], v[60:63]\n" \
"global_load_lds_dwordx4 v252, s[44:45] offset:3072\n" \
"v_mfma_f32_16x16x32_bf16 v[96:99], a[88:91], a[208:211], v[96:99]\n" \
"s_add_u32 s44, s44, 0x80\n" \
"s_addc_u32 s45, s45, 0\n" \
"ds_read_b128 a[84:87], v206 offset:4096\n" \
"v_mfma_f32_16x16x32_bf16 v[96:99], a[92:95], a[212:215], v[96:99]\n" \
"ds_read_b64_tr_b16 a[114:115], v210 offset:1024\n" \
"v_mfma_f32_16x16x32_bf16 v[100:103], a[88:91], a[216:219], v[100:103]\n" \
"s_add_u32 m0, s85, 81920\n" \
"ds_read_b64_tr_b16 a[116:117], v210 offset:12288\n" \
"v_mfma_f32_16x16x32_bf16 v[100:103], a[92:95], a[220:223], v[100:103]\n" \
"buffer_load_dwordx4 v246, s[12:15], s46 offen lds\n" \
"v_mfma_f32_16x16x32_bf16 v[68:71], a[200:203], a[192:195], v[68:71]\n" \
"ds_read_b128 a[88:91], v205 offset:8192\n" \
"s_add_u32 s46, s46, 0x80\n" \
"v_mfma_f32_16x16x32_bf16 v[68:71], a[204:207], a[196:199], v[68:71]\n" \
"ds_read_b128 a[92:95], v206 offset:8192\n" \
"v_mfma_f32_16x16x32_bf16 v[104:107], a[192:195], a[208:211], v[104:107]\n" \
"s_add_u32 m0, s84, 90112\n" \
"ds_read_b64_tr_b16 a[118:119], v210 offset:13312\n" \
"v_mfma_f32_16x16x32_bf16 v[104:107], a[196:199], a[212:215], v[104:107]\n" \
"global_load_lds_dwordx4 v253, s[48:49] offset:-4096\n" \
"v_mfma_f32_16x16x32_bf16 v[108:111], a[192:195], a[216:219], v[108:111]\n" \
"v_mfma_f32_16x16x32_bf16 v[108:111], a[196:199], a[220:223], v[108:111]\n" \
"s_waitcnt lgkmcnt(2)\n" \
"v_mfma_f32_16x16x32_bf16 v[32:35], a[96:99], a[64:67], v[32:35]\n" \
"s_lshr_b32 s89, s22, 7\n" \
"ds_read_b128 a[192:195], v205 offset:12288\n" \
"s_sub_u32 s89, 3, s89\n" \
"v_mfma_f32_16x16x32_bf16 v[32:35], a[100:103], a[68:71], v[32:35]\n" \
"global_load_lds_dwordx4 v253, s[48:49] offset:-2048\n" \
"v_bfe_u32 v203, v248, 7, 2\n" \
"v_mfma_f32_16x16x32_bf16 v[40:43], a[96:99], a[72:75], v[40:43]\n" \
"s_and_b32 s89, s89, 7\n" \
"ds_read_b128 a[196:199], v206 offset:12288\n" \
"s_and_b32 s90, %1, 1\n" \
"v_mfma_f32_16x16x32_bf16 v[40:43], a[100:103], a[76:79], v[40:43]\n" \
"ds_read_b64_tr_b16 a[200:201], v210 offset:2048\n" \
"v_bfe_u32 v204, v248, 5, 2\n" \
"v_mfma_f32_16x16x32_bf16 v[0:3], a[104:107], a[72:75], v[0:3]\n" \
"s_lshl_b32 s90, s90, 3\n" \
"s_add_u32 s89, s89, s90\n" \
"ds_read_b64_tr_b16 a[202:203], v210 offset:3072\n" \
"v_mfma_f32_16x16x32_bf16 v[0:3], a[108:111], a[76:79], v[0:3]\n" \
"s_waitcnt lgkmcnt(3)\n" \
"global_load_lds_dwordx4 v253, s[48:49] offset:0\n" \
"v_min_u32 v204, 2, v204\n" \
"v_mfma_f32_16x16x32_bf16 v[4:7], a[112:115], a[72:75], v[4:7]\n" \
"s_add_u32 s48, s48, 0x60000\n" \
"s_addc_u32 s49, s49, 0\n" \
"ds_read_b64_tr_b16 a[204:205], v210 offset:14336\n" \
"v_mfma_f32_16x16x32_bf16 v[4:7], a[116:119], a[76:79], v[4:7]\n" \
"s_lshl_b32 s89, s89, 7\n" \
"s_lshr_b32 s90, %1, 3\n" \
"ds_read_b64_tr_b16 a[206:207], v210 offset:15360\n" \
"v_mfma_f32_16x16x32_bf16 v[48:51], a[96:99], a[80:83], v[48:51]\n" \
"ds_read_b64_tr_b16 a[208:209], v209 offset:4096\n" \
"v_mad_u32_u24 v203, v203, 3, v204\n" \
"v_mfma_f32_16x16x32_bf16 v[48:51], a[100:103], a[84:87], v[48:51]\n" \
"s_lshl_b32 s90, s90, 2\n" \
"global_load_lds_dwordx4 v253, s[50:51] offset:-3072\n" \
"v_mfma_f32_16x16x32_bf16 v[8:11], a[104:107], a[80:83], v[8:11]\n" \
"s_add_u32 s90, s90, s61\n" \
"ds_read_b64_tr_b16 a[210:211], v209 offset:5120\n" \
"v_and_b32 v204, 16, v248\n" \
"v_mfma_f32_16x16x32_bf16 v[8:11], a[108:111], a[84:87], v[8:11]\n" \
"s_add_u32 s90, s90, 128\n" \
"s_mul_i32 s90, s90, 24576\n" \
"ds_read_b64_tr_b16 a[212:213], v209 offset:16384\n" \
"v_mfma_f32_16x16x32_bf16 v[12:15], a[112:115], a[80:83], v[12:15]\n" \
"s_add_u32 s89, s89, s90\n" \
"ds_read_b64_tr_b16 a[214:215], v209 offset:17408\n" \
"v_lshl_add_u32 v204, v204, 2, s89\n" \
"v_mfma_f32_16x16x32_bf16 v[12:15], a[116:119], a[84:87], v[12:15]\n" \
"global_load_lds_dwordx4 v253, s[50:51] offset:-1024\n" \
"v_lshl_add_u32 v203, v203, 11, v204\n" \
"v_mfma_f32_16x16x32_bf16 v[56:59], a[96:99], a[88:91], v[56:59]\n" \
"ds_read_b64_tr_b16 a[216:217], v210 offset:4096\n" \
"v_mfma_f32_16x16x32_bf16 v[56:59], a[100:103], a[92:95], v[56:59]\n" \
"ds_read_b64_tr_b16 a[218:219], v210 offset:5120\n" \
"v_mfma_f32_16x16x32_bf16 v[16:19], a[104:107], a[88:91], v[16:19]\n" \
"ds_read_b64_tr_b16 a[220:221], v210 offset:16384\n" \
"v_mfma_f32_16x16x32_bf16 v[16:19], a[108:111], a[92:95], v[16:19]\n" \
"global_load_lds_dwordx4 v253, s[50:51] offset:1024\n" \
"v_mfma_f32_16x16x32_bf16 v[20:23], a[112:115], a[88:91], v[20:23]\n" \
"s_add_u32 s50, s50, 0x60000\n" \
"s_addc_u32 s51, s51, 0\n" \
"ds_read_b64_tr_b16 a[222:223], v210 offset:17408\n" \
"v_mfma_f32_16x16x32_bf16 v[20:23], a[116:119], a[92:95], v[20:23]\n" \
"s_waitcnt lgkmcnt(3)\n" \
"v_add_u32 v207, 0x1d000, v207\n" \
"v_add_u32 v208, 0x1d000, v208\n" \
"v_mfma_f32_16x16x32_bf16 v[64:67], a[96:99], a[192:195], v[64:67]\n" \
"v_add_u32 v205, 0x1d000, v205\n" \
"v_add_u32 v206, 0x1d000, v206\n" \
"v_mfma_f32_16x16x32_bf16 v[64:67], a[100:103], a[196:199], v[64:67]\n" \
"v_add_u32 v209, 0x1d000, v209\n" \
"v_add_u32 v210, 0x1d000, v210\n" \
"v_mfma_f32_16x16x32_bf16 v[24:27], a[104:107], a[192:195], v[24:27]\n" \
"v_mfma_f32_16x16x32_bf16 v[24:27], a[108:111], a[196:199], v[24:27]\n" \
"v_mfma_f32_16x16x32_bf16 v[28:31], a[112:115], a[192:195], v[28:31]\n" \
"s_waitcnt vmcnt(13) lgkmcnt(0)\n" \
"v_mfma_f32_16x16x32_bf16 v[28:31], a[116:119], a[196:199], v[28:31]\n" \
"s_barrier\n" \
"v_mfma_f32_16x16x32_bf16 v[72:75], a[64:67], a[208:211], v[72:75]\n" \
"ds_read_b64_tr_b16 a[96:97], v209 offset:2048\n" \
"v_mfma_f32_16x16x32_bf16 v[72:75], a[68:71], a[212:215], v[72:75]\n" \
"ds_read_b64_tr_b16 a[98:99], v209 offset:3072\n" \
"v_mfma_f32_16x16x32_bf16 v[44:47], a[200:203], a[72:75], v[44:47]\n" \
"s_add_u32 m0, s95, 0\n" \
"ds_read_b128 a[64:67], v207 offset:0\n" \
"ds_read_b64_tr_b16 a[100:101], v209 offset:14336\n" \
"v_mfma_f32_16x16x32_bf16 v[44:47], a[204:207], a[76:79], v[44:47]\n" \
"global_load_lds_dwordx4 v252, s[42:43] offset:0\n" \
"v_mfma_f32_16x16x32_bf16 v[80:83], a[72:75], a[208:211], v[80:83]\n" \
"ds_read_b128 a[68:71], v208 offset:0\n" \
"ds_read_b64_tr_b16 a[102:103], v209 offset:15360\n" \
"v_mfma_f32_16x16x32_bf16 v[80:83], a[76:79], a[212:215], v[80:83]\n" \
"ds_read_b64_tr_b16 a[104:105], v209 offset:0\n" \
"v_mfma_f32_16x16x32_bf16 v[84:87], a[72:75], a[216:219], v[84:87]\n" \
"ds_read_b64_tr_b16 a[106:107], v209 offset:1024\n" \
"v_mfma_f32_16x16x32_bf16 v[84:87], a[76:79], a[220:223], v[84:87]\n" \
"global_load_lds_dwordx4 v252, s[42:43] offset:2048\n" \
"v_mfma_f32_16x16x32_bf16 v[52:55], a[200:203], a[80:83], v[52:55]\n" \
"s_add_u32 s42, s42, 0x80\n" \
"s_addc_u32 s43, s43, 0\n" \
"ds_read_b128 a[72:75], v205 offset:0\n" \
"v_mfma_f32_16x16x32_bf16 v[52:55], a[204:207], a[84:87], v[52:55]\n" \
"ds_read_b128 a[76:79], v206 offset:0\n" \
"v_mfma_f32_16x16x32_bf16 v[88:91], a[80:83], a[208:211], v[88:91]\n" \
"ds_read_b64_tr_b16 a[108:109], v209 offset:12288\n" \
"v_mfma_f32_16x16x32_bf16 v[88:91], a[84:87], a[212:215], v[88:91]\n" \
"global_load_lds_dwordx4 v252, s[44:45] offset:1024\n" \
"v_mfma_f32_16x16x32_bf16 v[92:95], a[80:83], a[216:219], v[92:95]\n" \
"ds_read_b64_tr_b16 a[110:111], v209 offset:13312\n" \
"v_mfma_f32_16x16x32_bf16 v[92:95], a[84:87], a[220:223], v[92:95]\n" \
"ds_read_b64_tr_b16 a[112:113], v210 offset:0\n" \
"v_mfma_f32_16x16x32_bf16 v[60:63], a[200:203], a[88:91], v[60:63]\n" \
"ds_read_b128 a[80:83], v205 offset:4096\n" \
"v_mfma_f32_16x16x32_bf16 v[60:63], a[204:207], a[92:95], v[60:63]\n" \
"global_load_lds_dwordx4 v252, s[44:45] offset:3072\n" \
"v_mfma_f32_16x16x32_bf16 v[96:99], a[88:91], a[208:211], v[96:99]\n" \
"s_add_u32 s44, s44, 0x80\n" \
"s_addc_u32 s45, s45, 0\n" \
"ds_read_b128 a[84:87], v206 offset:4096\n" \
"v_mfma_f32_16x16x32_bf16 v[96:99], a[92:95], a[212:215], v[96:99]\n" \
"ds_read_b64_tr_b16 a[114:115], v210 offset:1024\n" \
"v_mfma_f32_16x16x32_bf16 v[100:103], a[88:91], a[216:219], v[100:103]\n" \
"s_add_u32 m0, s85, 16384\n" \
"ds_read_b64_tr_b16 a[116:117], v210 offset:12288\n" \
"v_mfma_f32_16x16x32_bf16 v[100:103], a[92:95], a[220:223], v[100:103]\n" \
"buffer_load_dwordx4 v246, s[12:15], s46 offen lds\n" \
"v_mfma_f32_16x16x32_bf16 v[68:71], a[200:203], a[192:195], v[68:71]\n" \
"ds_read_b128 a[88:91], v205 offset:8192\n" \
"s_add_u32 s46, s46, 0x80\n" \
"v_mfma_f32_16x16x32_bf16 v[68:71], a[204:207], a[196:199], v[68:71]\n" \
"ds_read_b128 a[92:95], v206 offset:8192\n" \
"v_mfma_f32_16x16x32_bf16 v[104:107], a[192:195], a[208:211], v[104:107]\n" \
"s_add_u32 m0, s84, 24576\n" \
"ds_read_b64_tr_b16 a[118:119], v210 offset:13312\n" \
"v_mfma_f32_16x16x32_bf16 v[104:107], a[196:199], a[212:215], v[104:107]\n" \
"global_load_lds_dwordx4 v253, s[48:49] offset:-4096\n" \
"v_mfma_f32_16x16x32_bf16 v[108:111], a[192:195], a[216:219], v[108:111]\n" \
"v_mfma_f32_16x16x32_bf16 v[108:111], a[196:199], a[220:223], v[108:111]\n" \
"s_waitcnt lgkmcnt(2)\n" \
"v_mfma_f32_16x16x32_bf16 v[32:35], a[96:99], a[64:67], v[32:35]\n" \
"s_mov_b32 s30, 65593856\n" \
"ds_read_b128 a[192:195], v205 offset:12288\n" \
"v_lshrrev_b32 v228, 1, v248\n" \
"v_mfma_f32_16x16x32_bf16 v[32:35], a[100:103], a[68:71], v[32:35]\n" \
"global_load_lds_dwordx4 v253, s[48:49] offset:-2048\n" \
"v_mfma_f32_16x16x32_bf16 v[40:43], a[96:99], a[72:75], v[40:43]\n" \
"ds_read_b128 a[196:199], v206 offset:12288\n" \
"v_mfma_f32_16x16x32_bf16 v[40:43], a[100:103], a[76:79], v[40:43]\n" \
"ds_read_b64_tr_b16 a[200:201], v210 offset:2048\n" \
"v_mfma_f32_16x16x32_bf16 v[0:3], a[104:107], a[72:75], v[0:3]\n" \
"ds_read_b64_tr_b16 a[202:203], v210 offset:3072\n" \
"v_mfma_f32_16x16x32_bf16 v[0:3], a[108:111], a[76:79], v[0:3]\n" \
"s_waitcnt lgkmcnt(3)\n" \
"global_load_lds_dwordx4 v253, s[48:49] offset:0\n" \
"v_mfma_f32_16x16x32_bf16 v[4:7], a[112:115], a[72:75], v[4:7]\n" \
"s_add_u32 s48, s48, 0x60000\n" \
"s_addc_u32 s49, s49, 0\n" \
"ds_read_b64_tr_b16 a[204:205], v210 offset:14336\n" \
"v_mfma_f32_16x16x32_bf16 v[4:7], a[116:119], a[76:79], v[4:7]\n" \
"ds_read_b64_tr_b16 a[206:207], v210 offset:15360\n" \
"v_mfma_f32_16x16x32_bf16 v[48:51], a[96:99], a[80:83], v[48:51]\n" \
"ds_read_b64_tr_b16 a[208:209], v209 offset:4096\n" \
"v_mfma_f32_16x16x32_bf16 v[48:51], a[100:103], a[84:87], v[48:51]\n" \
"global_load_lds_dwordx4 v253, s[50:51] offset:-3072\n" \
"v_mfma_f32_16x16x32_bf16 v[8:11], a[104:107], a[80:83], v[8:11]\n" \
"ds_read_b64_tr_b16 a[210:211], v209 offset:5120\n" \
"v_mfma_f32_16x16x32_bf16 v[8:11], a[108:111], a[84:87], v[8:11]\n" \
"ds_read_b64_tr_b16 a[212:213], v209 offset:16384\n" \
"v_mfma_f32_16x16x32_bf16 v[12:15], a[112:115], a[80:83], v[12:15]\n" \
"ds_read_b64_tr_b16 a[214:215], v209 offset:17408\n" \
"v_mfma_f32_16x16x32_bf16 v[12:15], a[116:119], a[84:87], v[12:15]\n" \
"global_load_lds_dwordx4 v253, s[50:51] offset:-1024\n" \
"v_mfma_f32_16x16x32_bf16 v[56:59], a[96:99], a[88:91], v[56:59]\n" \
"ds_read_b64_tr_b16 a[216:217], v210 offset:4096\n" \
"v_mfma_f32_16x16x32_bf16 v[56:59], a[100:103], a[92:95], v[56:59]\n" \
"ds_read_b64_tr_b16 a[218:219], v210 offset:5120\n" \
"v_mfma_f32_16x16x32_bf16 v[16:19], a[104:107], a[88:91], v[16:19]\n" \
"ds_read_b64_tr_b16 a[220:221], v210 offset:16384\n" \
"v_mfma_f32_16x16x32_bf16 v[16:19], a[108:111], a[92:95], v[16:19]\n" \
"global_load_lds_dwordx4 v253, s[50:51] offset:1024\n" \
"v_mfma_f32_16x16x32_bf16 v[20:23], a[112:115], a[88:91], v[20:23]\n" \
"s_add_u32 s50, s50, 0x60000\n" \
"s_addc_u32 s51, s51, 0\n" \
"ds_read_b64_tr_b16 a[222:223], v210 offset:17408\n" \
"v_mfma_f32_16x16x32_bf16 v[20:23], a[116:119], a[92:95], v[20:23]\n" \
"s_waitcnt lgkmcnt(3)\n" \
"v_add_u32 v207, 0xffff3000, v207\n" \
"v_add_u32 v208, 0xffff3000, v208\n" \
"v_mfma_f32_16x16x32_bf16 v[64:67], a[96:99], a[192:195], v[64:67]\n" \
"v_add_u32 v205, 0xffff3000, v205\n" \
"v_add_u32 v206, 0xffff3000, v206\n" \
"v_mfma_f32_16x16x32_bf16 v[64:67], a[100:103], a[196:199], v[64:67]\n" \
"global_load_dword v204, v203, s[22:23]\n" \
"v_mfma_f32_16x16x32_bf16 v[24:27], a[104:107], a[192:195], v[24:27]\n" \
"v_add_u32 v209, 0xffff3000, v209\n" \
"v_add_u32 v210, 0xffff3000, v210\n" \
"v_mfma_f32_16x16x32_bf16 v[24:27], a[108:111], a[196:199], v[24:27]\n" \
"v_mfma_f32_16x16x32_bf16 v[28:31], a[112:115], a[192:195], v[28:31]\n" \
"s_waitcnt vmcnt(12) lgkmcnt(0)\n" \
"v_mfma_f32_16x16x32_bf16 v[28:31], a[116:119], a[196:199], v[28:31]\n" \
"s_barrier\n" \
"v_mfma_f32_16x16x32_bf16 v[72:75], a[64:67], a[208:211], v[72:75]\n" \
"ds_read_b64_tr_b16 a[96:97], v209 offset:2048\n" \
"v_mfma_f32_16x16x32_bf16 v[72:75], a[68:71], a[212:215], v[72:75]\n" \
"ds_read_b64_tr_b16 a[98:99], v209 offset:3072\n" \
"v_mfma_f32_16x16x32_bf16 v[44:47], a[200:203], a[72:75], v[44:47]\n" \
"Lpg9904_%=:\n" \
"s_cbranch_execz Lpg12201_%=\n" \
"s_add_u32 m0, s95, 118784\n" \
"ds_read_b128 a[64:67], v207 offset:0\n" \
"ds_read_b64_tr_b16 a[100:101], v209 offset:14336\n" \
"v_mfma_f32_16x16x32_bf16 v[44:47], a[204:207], a[76:79], v[44:47]\n" \
"global_load_lds_dwordx4 v252, s[42:43] offset:0\n" \
"v_mfma_f32_16x16x32_bf16 v[80:83], a[72:75], a[208:211], v[80:83]\n" \
"ds_read_b128 a[68:71], v208 offset:0\n" \
"ds_read_b64_tr_b16 a[102:103], v209 offset:15360\n" \
"v_mfma_f32_16x16x32_bf16 v[80:83], a[76:79], a[212:215], v[80:83]\n" \
"ds_read_b64_tr_b16 a[104:105], v209 offset:0\n" \
"v_mfma_f32_16x16x32_bf16 v[84:87], a[72:75], a[216:219], v[84:87]\n" \
"ds_read_b64_tr_b16 a[106:107], v209 offset:1024\n" \
"v_mfma_f32_16x16x32_bf16 v[84:87], a[76:79], a[220:223], v[84:87]\n" \
"global_load_lds_dwordx4 v252, s[42:43] offset:2048\n" \
"v_mfma_f32_16x16x32_bf16 v[52:55], a[200:203], a[80:83], v[52:55]\n" \
"s_add_u32 s42, s42, 0x80\n" \
"s_addc_u32 s43, s43, 0\n" \
"ds_read_b128 a[72:75], v205 offset:0\n" \
"v_mfma_f32_16x16x32_bf16 v[52:55], a[204:207], a[84:87], v[52:55]\n" \
"ds_read_b128 a[76:79], v206 offset:0\n" \
"v_mfma_f32_16x16x32_bf16 v[88:91], a[80:83], a[208:211], v[88:91]\n" \
"ds_read_b64_tr_b16 a[108:109], v209 offset:12288\n" \
"v_mfma_f32_16x16x32_bf16 v[88:91], a[84:87], a[212:215], v[88:91]\n" \
"global_load_lds_dwordx4 v252, s[44:45] offset:1024\n" \
"v_mfma_f32_16x16x32_bf16 v[92:95], a[80:83], a[216:219], v[92:95]\n" \
"ds_read_b64_tr_b16 a[110:111], v209 offset:13312\n" \
"v_mfma_f32_16x16x32_bf16 v[92:95], a[84:87], a[220:223], v[92:95]\n" \
"ds_read_b64_tr_b16 a[112:113], v210 offset:0\n" \
"v_mfma_f32_16x16x32_bf16 v[60:63], a[200:203], a[88:91], v[60:63]\n" \
"ds_read_b128 a[80:83], v205 offset:4096\n" \
"v_mfma_f32_16x16x32_bf16 v[60:63], a[204:207], a[92:95], v[60:63]\n" \
"global_load_lds_dwordx4 v252, s[44:45] offset:3072\n" \
"v_mfma_f32_16x16x32_bf16 v[96:99], a[88:91], a[208:211], v[96:99]\n" \
"s_add_u32 s44, s44, 0x80\n" \
"s_addc_u32 s45, s45, 0\n" \
"ds_read_b128 a[84:87], v206 offset:4096\n" \
"v_mfma_f32_16x16x32_bf16 v[96:99], a[92:95], a[212:215], v[96:99]\n" \
"ds_read_b64_tr_b16 a[114:115], v210 offset:1024\n" \
"v_mfma_f32_16x16x32_bf16 v[100:103], a[88:91], a[216:219], v[100:103]\n" \
"s_add_u32 m0, s85, 135168\n" \
"ds_read_b64_tr_b16 a[116:117], v210 offset:12288\n" \
"v_mfma_f32_16x16x32_bf16 v[100:103], a[92:95], a[220:223], v[100:103]\n" \
"buffer_load_dwordx4 v246, s[12:15], s46 offen lds\n" \
"v_mfma_f32_16x16x32_bf16 v[68:71], a[200:203], a[192:195], v[68:71]\n" \
"ds_read_b128 a[88:91], v205 offset:8192\n" \
"s_add_u32 s46, s46, 0x80\n" \
"v_mfma_f32_16x16x32_bf16 v[68:71], a[204:207], a[196:199], v[68:71]\n" \
"ds_read_b128 a[92:95], v206 offset:8192\n" \
"v_mfma_f32_16x16x32_bf16 v[104:107], a[192:195], a[208:211], v[104:107]\n" \
"s_add_u32 m0, s84, 143360\n" \
"ds_read_b64_tr_b16 a[118:119], v210 offset:13312\n" \
"v_mfma_f32_16x16x32_bf16 v[104:107], a[196:199], a[212:215], v[104:107]\n" \
"global_load_lds_dwordx4 v253, s[48:49] offset:-4096\n" \
"v_mfma_f32_16x16x32_bf16 v[108:111], a[192:195], a[216:219], v[108:111]\n" \
"v_mfma_f32_16x16x32_bf16 v[108:111], a[196:199], a[220:223], v[108:111]\n" \
"s_waitcnt lgkmcnt(2)\n" \
"v_mfma_f32_16x16x32_bf16 v[32:35], a[96:99], a[64:67], v[32:35]\n" \
"ds_read_b128 a[192:195], v205 offset:12288\n" \
"v_mfma_f32_16x16x32_bf16 v[32:35], a[100:103], a[68:71], v[32:35]\n" \
"global_load_lds_dwordx4 v253, s[48:49] offset:-2048\n" \
"v_mfma_f32_16x16x32_bf16 v[40:43], a[96:99], a[72:75], v[40:43]\n" \
"ds_read_b128 a[196:199], v206 offset:12288\n" \
"v_mfma_f32_16x16x32_bf16 v[40:43], a[100:103], a[76:79], v[40:43]\n" \
"ds_read_b64_tr_b16 a[200:201], v210 offset:2048\n" \
"v_mfma_f32_16x16x32_bf16 v[0:3], a[104:107], a[72:75], v[0:3]\n" \
"ds_read_b64_tr_b16 a[202:203], v210 offset:3072\n" \
"v_mfma_f32_16x16x32_bf16 v[0:3], a[108:111], a[76:79], v[0:3]\n" \
"s_waitcnt lgkmcnt(3)\n" \
"global_load_lds_dwordx4 v253, s[48:49] offset:0\n" \
"v_mfma_f32_16x16x32_bf16 v[4:7], a[112:115], a[72:75], v[4:7]\n" \
"s_add_u32 s48, s48, 0x60000\n" \
"s_addc_u32 s49, s49, 0\n" \
"ds_read_b64_tr_b16 a[204:205], v210 offset:14336\n" \
"v_mfma_f32_16x16x32_bf16 v[4:7], a[116:119], a[76:79], v[4:7]\n" \
"ds_read_b64_tr_b16 a[206:207], v210 offset:15360\n" \
"v_mfma_f32_16x16x32_bf16 v[48:51], a[96:99], a[80:83], v[48:51]\n" \
"ds_read_b64_tr_b16 a[208:209], v209 offset:4096\n" \
"v_mfma_f32_16x16x32_bf16 v[48:51], a[100:103], a[84:87], v[48:51]\n" \
"global_load_lds_dwordx4 v253, s[50:51] offset:-3072\n" \
"v_mfma_f32_16x16x32_bf16 v[8:11], a[104:107], a[80:83], v[8:11]\n" \
"ds_read_b64_tr_b16 a[210:211], v209 offset:5120\n" \
"v_mfma_f32_16x16x32_bf16 v[8:11], a[108:111], a[84:87], v[8:11]\n" \
"ds_read_b64_tr_b16 a[212:213], v209 offset:16384\n" \
"v_mfma_f32_16x16x32_bf16 v[12:15], a[112:115], a[80:83], v[12:15]\n" \
"ds_read_b64_tr_b16 a[214:215], v209 offset:17408\n" \
"v_mfma_f32_16x16x32_bf16 v[12:15], a[116:119], a[84:87], v[12:15]\n" \
"global_load_lds_dwordx4 v253, s[50:51] offset:-1024\n" \
"v_mfma_f32_16x16x32_bf16 v[56:59], a[96:99], a[88:91], v[56:59]\n" \
"ds_read_b64_tr_b16 a[216:217], v210 offset:4096\n" \
"v_mfma_f32_16x16x32_bf16 v[56:59], a[100:103], a[92:95], v[56:59]\n" \
"ds_read_b64_tr_b16 a[218:219], v210 offset:5120\n" \
"v_mfma_f32_16x16x32_bf16 v[16:19], a[104:107], a[88:91], v[16:19]\n" \
"ds_read_b64_tr_b16 a[220:221], v210 offset:16384\n" \
"v_mfma_f32_16x16x32_bf16 v[16:19], a[108:111], a[92:95], v[16:19]\n" \
"global_load_lds_dwordx4 v253, s[50:51] offset:1024\n" \
"v_mfma_f32_16x16x32_bf16 v[20:23], a[112:115], a[88:91], v[20:23]\n" \
"s_add_u32 s50, s50, 0x60000\n" \
"s_addc_u32 s51, s51, 0\n" \
"ds_read_b64_tr_b16 a[222:223], v210 offset:17408\n" \
"v_mfma_f32_16x16x32_bf16 v[20:23], a[116:119], a[92:95], v[20:23]\n" \
"s_waitcnt lgkmcnt(3)\n" \
"v_add_u32 v207, 0xffff0000, v207\n" \
"v_add_u32 v208, 0xffff0000, v208\n" \
"v_mfma_f32_16x16x32_bf16 v[64:67], a[96:99], a[192:195], v[64:67]\n" \
"v_add_u32 v205, 0xffff0000, v205\n" \
"v_add_u32 v206, 0xffff0000, v206\n" \
"v_mfma_f32_16x16x32_bf16 v[64:67], a[100:103], a[196:199], v[64:67]\n" \
"buffer_load_dwordx2 v[226:227], v228, s[56:59], s30 offen sc1\n" \
"v_mfma_f32_16x16x32_bf16 v[24:27], a[104:107], a[192:195], v[24:27]\n" \
"v_add_u32 v209, 0xffff0000, v209\n" \
"v_add_u32 v210, 0xffff0000, v210\n" \
"v_mfma_f32_16x16x32_bf16 v[24:27], a[108:111], a[196:199], v[24:27]\n" \
"v_mfma_f32_16x16x32_bf16 v[28:31], a[112:115], a[192:195], v[28:31]\n" \
"s_waitcnt vmcnt(13) lgkmcnt(0)\n" \
"v_mfma_f32_16x16x32_bf16 v[28:31], a[116:119], a[196:199], v[28:31]\n" \
"s_barrier\n" \
"v_mfma_f32_16x16x32_bf16 v[72:75], a[64:67], a[208:211], v[72:75]\n" \
"ds_read_b64_tr_b16 a[96:97], v209 offset:2048\n" \
"v_mfma_f32_16x16x32_bf16 v[72:75], a[68:71], a[212:215], v[72:75]\n" \
"ds_read_b64_tr_b16 a[98:99], v209 offset:3072\n" \
"v_mfma_f32_16x16x32_bf16 v[44:47], a[200:203], a[72:75], v[44:47]\n" \
"s_add_u32 m0, s95, 65536\n" \
"ds_read_b128 a[64:67], v207 offset:0\n" \
"ds_read_b64_tr_b16 a[100:101], v209 offset:14336\n" \
"v_mfma_f32_16x16x32_bf16 v[44:47], a[204:207], a[76:79], v[44:47]\n" \
"global_load_lds_dwordx4 v252, s[42:43] offset:0\n" \
"v_mfma_f32_16x16x32_bf16 v[80:83], a[72:75], a[208:211], v[80:83]\n" \
"ds_read_b128 a[68:71], v208 offset:0\n" \
"ds_read_b64_tr_b16 a[102:103], v209 offset:15360\n" \
"v_mfma_f32_16x16x32_bf16 v[80:83], a[76:79], a[212:215], v[80:83]\n" \
"ds_read_b64_tr_b16 a[104:105], v209 offset:0\n" \
"v_mfma_f32_16x16x32_bf16 v[84:87], a[72:75], a[216:219], v[84:87]\n" \
"ds_read_b64_tr_b16 a[106:107], v209 offset:1024\n" \
"v_mfma_f32_16x16x32_bf16 v[84:87], a[76:79], a[220:223], v[84:87]\n" \
"global_load_lds_dwordx4 v252, s[42:43] offset:2048\n" \
"v_mfma_f32_16x16x32_bf16 v[52:55], a[200:203], a[80:83], v[52:55]\n" \
"s_add_u32 s42, s42, 0x80\n" \
"s_addc_u32 s43, s43, 0\n" \
"ds_read_b128 a[72:75], v205 offset:0\n" \
"v_mfma_f32_16x16x32_bf16 v[52:55], a[204:207], a[84:87], v[52:55]\n" \
"ds_read_b128 a[76:79], v206 offset:0\n" \
"v_mfma_f32_16x16x32_bf16 v[88:91], a[80:83], a[208:211], v[88:91]\n" \
"ds_read_b64_tr_b16 a[108:109], v209 offset:12288\n" \
"v_mfma_f32_16x16x32_bf16 v[88:91], a[84:87], a[212:215], v[88:91]\n" \
"global_load_lds_dwordx4 v252, s[44:45] offset:1024\n" \
"v_mfma_f32_16x16x32_bf16 v[92:95], a[80:83], a[216:219], v[92:95]\n" \
"ds_read_b64_tr_b16 a[110:111], v209 offset:13312\n" \
"v_mfma_f32_16x16x32_bf16 v[92:95], a[84:87], a[220:223], v[92:95]\n" \
"ds_read_b64_tr_b16 a[112:113], v210 offset:0\n" \
"v_mfma_f32_16x16x32_bf16 v[60:63], a[200:203], a[88:91], v[60:63]\n" \
"ds_read_b128 a[80:83], v205 offset:4096\n" \
"v_mfma_f32_16x16x32_bf16 v[60:63], a[204:207], a[92:95], v[60:63]\n" \
"global_load_lds_dwordx4 v252, s[44:45] offset:3072\n" \
"v_mfma_f32_16x16x32_bf16 v[96:99], a[88:91], a[208:211], v[96:99]\n" \
"s_add_u32 s44, s44, 0x80\n" \
"s_addc_u32 s45, s45, 0\n" \
"ds_read_b128 a[84:87], v206 offset:4096\n" \
"v_mfma_f32_16x16x32_bf16 v[96:99], a[92:95], a[212:215], v[96:99]\n" \
"ds_read_b64_tr_b16 a[114:115], v210 offset:1024\n" \
"v_mfma_f32_16x16x32_bf16 v[100:103], a[88:91], a[216:219], v[100:103]\n" \
"s_add_u32 m0, s85, 81920\n" \
"ds_read_b64_tr_b16 a[116:117], v210 offset:12288\n" \
"v_mfma_f32_16x16x32_bf16 v[100:103], a[92:95], a[220:223], v[100:103]\n" \
"buffer_load_dwordx4 v246, s[12:15], s46 offen lds\n" \
"v_mfma_f32_16x16x32_bf16 v[68:71], a[200:203], a[192:195], v[68:71]\n" \
"ds_read_b128 a[88:91], v205 offset:8192\n" \
"s_add_u32 s46, s46, 0x80\n" \
"v_mfma_f32_16x16x32_bf16 v[68:71], a[204:207], a[196:199], v[68:71]\n" \
"ds_read_b128 a[92:95], v206 offset:8192\n" \
"v_mfma_f32_16x16x32_bf16 v[104:107], a[192:195], a[208:211], v[104:107]\n" \
"s_add_u32 m0, s84, 90112\n" \
"ds_read_b64_tr_b16 a[118:119], v210 offset:13312\n" \
"v_mfma_f32_16x16x32_bf16 v[104:107], a[196:199], a[212:215], v[104:107]\n" \
"global_load_lds_dwordx4 v253, s[48:49] offset:-4096\n" \
"v_mfma_f32_16x16x32_bf16 v[108:111], a[192:195], a[216:219], v[108:111]\n" \
"v_mfma_f32_16x16x32_bf16 v[108:111], a[196:199], a[220:223], v[108:111]\n" \
"s_waitcnt lgkmcnt(2)\n" \
"v_mfma_f32_16x16x32_bf16 v[32:35], a[96:99], a[64:67], v[32:35]\n" \
"ds_read_b128 a[192:195], v205 offset:12288\n" \
"v_mfma_f32_16x16x32_bf16 v[32:35], a[100:103], a[68:71], v[32:35]\n" \
"global_load_lds_dwordx4 v253, s[48:49] offset:-2048\n" \
"v_mfma_f32_16x16x32_bf16 v[40:43], a[96:99], a[72:75], v[40:43]\n" \
"ds_read_b128 a[196:199], v206 offset:12288\n" \
"v_mfma_f32_16x16x32_bf16 v[40:43], a[100:103], a[76:79], v[40:43]\n" \
"ds_read_b64_tr_b16 a[200:201], v210 offset:2048\n" \
"v_mfma_f32_16x16x32_bf16 v[0:3], a[104:107], a[72:75], v[0:3]\n" \
"ds_read_b64_tr_b16 a[202:203], v210 offset:3072\n" \
"v_mfma_f32_16x16x32_bf16 v[0:3], a[108:111], a[76:79], v[0:3]\n" \
"s_waitcnt lgkmcnt(3)\n" \
"global_load_lds_dwordx4 v253, s[48:49] offset:0\n" \
"v_mfma_f32_16x16x32_bf16 v[4:7], a[112:115], a[72:75], v[4:7]\n" \
"s_add_u32 s48, s48, 0x60000\n" \
"s_addc_u32 s49, s49, 0\n" \
"ds_read_b64_tr_b16 a[204:205], v210 offset:14336\n" \
"v_mfma_f32_16x16x32_bf16 v[4:7], a[116:119], a[76:79], v[4:7]\n" \
"ds_read_b64_tr_b16 a[206:207], v210 offset:15360\n" \
"v_mfma_f32_16x16x32_bf16 v[48:51], a[96:99], a[80:83], v[48:51]\n" \
"ds_read_b64_tr_b16 a[208:209], v209 offset:4096\n" \
"v_mfma_f32_16x16x32_bf16 v[48:51], a[100:103], a[84:87], v[48:51]\n" \
"global_load_lds_dwordx4 v253, s[50:51] offset:-3072\n" \
"v_mfma_f32_16x16x32_bf16 v[8:11], a[104:107], a[80:83], v[8:11]\n" \
"ds_read_b64_tr_b16 a[210:211], v209 offset:5120\n" \
"v_mfma_f32_16x16x32_bf16 v[8:11], a[108:111], a[84:87], v[8:11]\n" \
"ds_read_b64_tr_b16 a[212:213], v209 offset:16384\n" \
"v_mfma_f32_16x16x32_bf16 v[12:15], a[112:115], a[80:83], v[12:15]\n" \
"ds_read_b64_tr_b16 a[214:215], v209 offset:17408\n" \
"v_mfma_f32_16x16x32_bf16 v[12:15], a[116:119], a[84:87], v[12:15]\n" \
"global_load_lds_dwordx4 v253, s[50:51] offset:-1024\n" \
"v_mfma_f32_16x16x32_bf16 v[56:59], a[96:99], a[88:91], v[56:59]\n" \
"ds_read_b64_tr_b16 a[216:217], v210 offset:4096\n" \
"v_mfma_f32_16x16x32_bf16 v[56:59], a[100:103], a[92:95], v[56:59]\n" \
"ds_read_b64_tr_b16 a[218:219], v210 offset:5120\n" \
"v_mfma_f32_16x16x32_bf16 v[16:19], a[104:107], a[88:91], v[16:19]\n" \
"ds_read_b64_tr_b16 a[220:221], v210 offset:16384\n" \
"v_mfma_f32_16x16x32_bf16 v[16:19], a[108:111], a[92:95], v[16:19]\n" \
"global_load_lds_dwordx4 v253, s[50:51] offset:1024\n" \
"v_mfma_f32_16x16x32_bf16 v[20:23], a[112:115], a[88:91], v[20:23]\n" \
"s_add_u32 s50, s50, 0x60000\n" \
"s_addc_u32 s51, s51, 0\n" \
"ds_read_b64_tr_b16 a[222:223], v210 offset:17408\n" \
"v_mfma_f32_16x16x32_bf16 v[20:23], a[116:119], a[92:95], v[20:23]\n" \
"s_waitcnt lgkmcnt(3)\n" \
"v_add_u32 v207, 0x1d000, v207\n" \
"v_add_u32 v208, 0x1d000, v208\n" \
"v_mfma_f32_16x16x32_bf16 v[64:67], a[96:99], a[192:195], v[64:67]\n" \
"v_add_u32 v205, 0x1d000, v205\n" \
"v_add_u32 v206, 0x1d000, v206\n" \
"v_mfma_f32_16x16x32_bf16 v[64:67], a[100:103], a[196:199], v[64:67]\n" \
"v_add_u32 v209, 0x1d000, v209\n" \
"v_add_u32 v210, 0x1d000, v210\n" \
"v_mfma_f32_16x16x32_bf16 v[24:27], a[104:107], a[192:195], v[24:27]\n" \
"v_mfma_f32_16x16x32_bf16 v[24:27], a[108:111], a[196:199], v[24:27]\n" \
"v_mfma_f32_16x16x32_bf16 v[28:31], a[112:115], a[192:195], v[28:31]\n" \
"s_waitcnt vmcnt(12) lgkmcnt(0)\n" \
"v_mfma_f32_16x16x32_bf16 v[28:31], a[116:119], a[196:199], v[28:31]\n" \
"s_barrier\n" \
"v_mfma_f32_16x16x32_bf16 v[72:75], a[64:67], a[208:211], v[72:75]\n" \
"ds_read_b64_tr_b16 a[96:97], v209 offset:2048\n" \
"s_waitcnt vmcnt(11)\n" \
"v_mfma_f32_16x16x32_bf16 v[72:75], a[68:71], a[212:215], v[72:75]\n" \
"ds_read_b64_tr_b16 a[98:99], v209 offset:3072\n" \
"v_mfma_f32_16x16x32_bf16 v[44:47], a[200:203], a[72:75], v[44:47]\n" \
"s_add_u32 m0, s95, 0\n" \
"ds_read_b128 a[64:67], v207 offset:0\n" \
"ds_read_b64_tr_b16 a[100:101], v209 offset:14336\n" \
"v_mfma_f32_16x16x32_bf16 v[44:47], a[204:207], a[76:79], v[44:47]\n" \
"global_load_lds_dwordx4 v252, s[42:43] offset:0\n" \
"v_mfma_f32_16x16x32_bf16 v[80:83], a[72:75], a[208:211], v[80:83]\n" \
"ds_read_b128 a[68:71], v208 offset:0\n" \
"ds_read_b64_tr_b16 a[102:103], v209 offset:15360\n" \
"v_mfma_f32_16x16x32_bf16 v[80:83], a[76:79], a[212:215], v[80:83]\n" \
"ds_read_b64_tr_b16 a[104:105], v209 offset:0\n" \
"v_mfma_f32_16x16x32_bf16 v[84:87], a[72:75], a[216:219], v[84:87]\n" \
"ds_read_b64_tr_b16 a[106:107], v209 offset:1024\n" \
"v_mfma_f32_16x16x32_bf16 v[84:87], a[76:79], a[220:223], v[84:87]\n" \
"global_load_lds_dwordx4 v252, s[42:43] offset:2048\n" \
"v_mfma_f32_16x16x32_bf16 v[52:55], a[200:203], a[80:83], v[52:55]\n" \
"s_add_u32 s42, s42, 0x100\n" \
"s_addc_u32 s43, s43, 0\n" \
"ds_read_b128 a[72:75], v205 offset:0\n" \
"v_mfma_f32_16x16x32_bf16 v[52:55], a[204:207], a[84:87], v[52:55]\n" \
"ds_read_b128 a[76:79], v206 offset:0\n" \
"v_mfma_f32_16x16x32_bf16 v[88:91], a[80:83], a[208:211], v[88:91]\n" \
"ds_read_b64_tr_b16 a[108:109], v209 offset:12288\n" \
"v_mfma_f32_16x16x32_bf16 v[88:91], a[84:87], a[212:215], v[88:91]\n" \
"global_load_lds_dwordx4 v252, s[44:45] offset:1024\n" \
"v_mfma_f32_16x16x32_bf16 v[92:95], a[80:83], a[216:219], v[92:95]\n" \
"ds_read_b64_tr_b16 a[110:111], v209 offset:13312\n" \
"v_mfma_f32_16x16x32_bf16 v[92:95], a[84:87], a[220:223], v[92:95]\n" \
"ds_read_b64_tr_b16 a[112:113], v210 offset:0\n" \
"v_mfma_f32_16x16x32_bf16 v[60:63], a[200:203], a[88:91], v[60:63]\n" \
"ds_read_b128 a[80:83], v205 offset:4096\n" \
"v_mfma_f32_16x16x32_bf16 v[60:63], a[204:207], a[92:95], v[60:63]\n" \
"global_load_lds_dwordx4 v252, s[44:45] offset:3072\n" \
"v_mfma_f32_16x16x32_bf16 v[96:99], a[88:91], a[208:211], v[96:99]\n" \
"s_add_u32 s44, s44, 0x100\n" \
"s_addc_u32 s45, s45, 0\n" \
"ds_read_b128 a[84:87], v206 offset:4096\n" \
"v_mfma_f32_16x16x32_bf16 v[96:99], a[92:95], a[212:215], v[96:99]\n" \
"ds_read_b64_tr_b16 a[114:115], v210 offset:1024\n" \
"v_mfma_f32_16x16x32_bf16 v[100:103], a[88:91], a[216:219], v[100:103]\n" \
"s_add_u32 m0, s85, 16384\n" \
"ds_read_b64_tr_b16 a[116:117], v210 offset:12288\n" \
"v_mfma_f32_16x16x32_bf16 v[100:103], a[92:95], a[220:223], v[100:103]\n" \
"buffer_load_dwordx4 v246, s[12:15], s46 offen lds\n" \
"v_mfma_f32_16x16x32_bf16 v[68:71], a[200:203], a[192:195], v[68:71]\n" \
"ds_read_b128 a[88:91], v205 offset:8192\n" \
"s_add_u32 s46, s46, 0x100\n" \
"v_mfma_f32_16x16x32_bf16 v[68:71], a[204:207], a[196:199], v[68:71]\n" \
"ds_read_b128 a[92:95], v206 offset:8192\n" \
"v_mfma_f32_16x16x32_bf16 v[104:107], a[192:195], a[208:211], v[104:107]\n" \
"s_add_u32 m0, s84, 24576\n" \
"ds_read_b64_tr_b16 a[118:119], v210 offset:13312\n" \
"v_mfma_f32_16x16x32_bf16 v[104:107], a[196:199], a[212:215], v[104:107]\n" \
"global_load_lds_dwordx4 v253, s[48:49] offset:-4096\n" \
"v_mfma_f32_16x16x32_bf16 v[108:111], a[192:195], a[216:219], v[108:111]\n" \
"v_xor_b32 v226, v226, v227\n" \
"v_cmp_ne_u32 vcc, s60, v226\n" \
"v_mfma_f32_16x16x32_bf16 v[108:111], a[196:199], a[220:223], v[108:111]\n" \
"s_waitcnt lgkmcnt(2)\n" \
"v_mfma_f32_16x16x32_bf16 v[32:35], a[96:99], a[64:67], v[32:35]\n" \
"ds_read_b128 a[192:195], v205 offset:12288\n" \
"v_mfma_f32_16x16x32_bf16 v[32:35], a[100:103], a[68:71], v[32:35]\n" \
"global_load_lds_dwordx4 v253, s[48:49] offset:-2048\n" \
"v_mfma_f32_16x16x32_bf16 v[40:43], a[96:99], a[72:75], v[40:43]\n" \
"ds_read_b128 a[196:199], v206 offset:12288\n" \
"v_mfma_f32_16x16x32_bf16 v[40:43], a[100:103], a[76:79], v[40:43]\n" \
"ds_read_b64_tr_b16 a[200:201], v210 offset:2048\n" \
"v_mfma_f32_16x16x32_bf16 v[0:3], a[104:107], a[72:75], v[0:3]\n" \
"ds_read_b64_tr_b16 a[202:203], v210 offset:3072\n" \
"v_mfma_f32_16x16x32_bf16 v[0:3], a[108:111], a[76:79], v[0:3]\n" \
"s_waitcnt lgkmcnt(3)\n" \
"global_load_lds_dwordx4 v253, s[48:49] offset:0\n" \
"v_mfma_f32_16x16x32_bf16 v[4:7], a[112:115], a[72:75], v[4:7]\n" \
"s_add_u32 s48, s48, 0xc0000\n" \
"s_addc_u32 s49, s49, 0\n" \
"ds_read_b64_tr_b16 a[204:205], v210 offset:14336\n" \
"v_mfma_f32_16x16x32_bf16 v[4:7], a[116:119], a[76:79], v[4:7]\n" \
"ds_read_b64_tr_b16 a[206:207], v210 offset:15360\n" \
"v_mfma_f32_16x16x32_bf16 v[48:51], a[96:99], a[80:83], v[48:51]\n" \
"ds_read_b64_tr_b16 a[208:209], v209 offset:4096\n" \
"v_mfma_f32_16x16x32_bf16 v[48:51], a[100:103], a[84:87], v[48:51]\n" \
"global_load_lds_dwordx4 v253, s[50:51] offset:-3072\n" \
"v_mfma_f32_16x16x32_bf16 v[8:11], a[104:107], a[80:83], v[8:11]\n" \
"ds_read_b64_tr_b16 a[210:211], v209 offset:5120\n" \
"v_mfma_f32_16x16x32_bf16 v[8:11], a[108:111], a[84:87], v[8:11]\n" \
"ds_read_b64_tr_b16 a[212:213], v209 offset:16384\n" \
"v_mfma_f32_16x16x32_bf16 v[12:15], a[112:115], a[80:83], v[12:15]\n" \
"ds_read_b64_tr_b16 a[214:215], v209 offset:17408\n" \
"v_mfma_f32_16x16x32_bf16 v[12:15], a[116:119], a[84:87], v[12:15]\n" \
"global_load_lds_dwordx4 v253, s[50:51] offset:-1024\n" \
"v_mfma_f32_16x16x32_bf16 v[56:59], a[96:99], a[88:91], v[56:59]\n" \
"ds_read_b64_tr_b16 a[216:217], v210 offset:4096\n" \
"v_mfma_f32_16x16x32_bf16 v[56:59], a[100:103], a[92:95], v[56:59]\n" \
"ds_read_b64_tr_b16 a[218:219], v210 offset:5120\n" \
"v_mfma_f32_16x16x32_bf16 v[16:19], a[104:107], a[88:91], v[16:19]\n" \
"ds_read_b64_tr_b16 a[220:221], v210 offset:16384\n" \
"v_mfma_f32_16x16x32_bf16 v[16:19], a[108:111], a[92:95], v[16:19]\n" \
"global_load_lds_dwordx4 v253, s[50:51] offset:1024\n" \
"v_mfma_f32_16x16x32_bf16 v[20:23], a[112:115], a[88:91], v[20:23]\n" \
"s_add_u32 s50, s50, 0xc0000\n" \
"s_addc_u32 s51, s51, 0\n" \
"ds_read_b64_tr_b16 a[222:223], v210 offset:17408\n" \
"v_mfma_f32_16x16x32_bf16 v[20:23], a[116:119], a[92:95], v[20:23]\n" \
"s_waitcnt lgkmcnt(3)\n" \
"v_add_u32 v207, 0xffff3000, v207\n" \
"v_add_u32 v208, 0xffff3000, v208\n" \
"v_mfma_f32_16x16x32_bf16 v[64:67], a[96:99], a[192:195], v[64:67]\n" \
"v_add_u32 v205, 0xffff3000, v205\n" \
"v_add_u32 v206, 0xffff3000, v206\n" \
"v_mfma_f32_16x16x32_bf16 v[64:67], a[100:103], a[196:199], v[64:67]\n" \
"v_add_u32 v209, 0xffff3000, v209\n" \
"v_add_u32 v210, 0xffff3000, v210\n" \
"v_mfma_f32_16x16x32_bf16 v[24:27], a[104:107], a[192:195], v[24:27]\n" \
"s_and_b32 vcc_hi, vcc_lo, -1\n" \
"s_cbranch_scc1 Lpl3_%=\n" \
"Lpr3_%=:\n" \
"v_mfma_f32_16x16x32_bf16 v[24:27], a[108:111], a[196:199], v[24:27]\n" \
"v_readfirstlane_b32 s71, v227\n" \
"s_and_b32 s70, %1, 7\n" \
"v_mfma_f32_16x16x32_bf16 v[28:31], a[112:115], a[192:195], v[28:31]\n" \
"s_waitcnt vmcnt(11) lgkmcnt(0)\n" \
"s_lshl_b32 s78, s70, 2\n" \
"v_mfma_f32_16x16x32_bf16 v[28:31], a[116:119], a[196:199], v[28:31]\n" \
"s_barrier\n" \
"v_mfma_f32_16x16x32_bf16 v[72:75], a[64:67], a[208:211], v[72:75]\n" \
"ds_read_b64_tr_b16 a[96:97], v209 offset:2048\n" \
"v_subrev_u32 v227, s71, v227\n" \
"v_mfma_f32_16x16x32_bf16 v[72:75], a[68:71], a[212:215], v[72:75]\n" \
"ds_read_b64_tr_b16 a[98:99], v209 offset:3072\n" \
"v_mfma_f32_16x16x32_bf16 v[44:47], a[200:203], a[72:75], v[44:47]\n" \
"s_add_u32 m0, s95, 118784\n" \
"ds_read_b128 a[64:67], v207 offset:0\n" \
"ds_read_b64_tr_b16 a[100:101], v209 offset:14336\n" \
"v_mfma_f32_16x16x32_bf16 v[44:47], a[204:207], a[76:79], v[44:47]\n" \
"global_load_lds_dwordx4 v252, s[42:43] offset:0\n" \
"v_add_u32_dpp v227, v227, v227 quad_perm:[1,0,3,2] row_mask:0xf bank_mask:0xf\n" \
"v_mfma_f32_16x16x32_bf16 v[80:83], a[72:75], a[208:211], v[80:83]\n" \
"ds_read_b128 a[68:71], v208 offset:0\n" \
"ds_read_b64_tr_b16 a[102:103], v209 offset:15360\n" \
"v_mfma_f32_16x16x32_bf16 v[80:83], a[76:79], a[212:215], v[80:83]\n" \
"ds_read_b64_tr_b16 a[104:105], v209 offset:0\n" \
"v_add_u32_dpp v227, v227, v227 quad_perm:[2,3,0,1] row_mask:0xf bank_mask:0xf\n" \
"v_mfma_f32_16x16x32_bf16 v[84:87], a[72:75], a[216:219], v[84:87]\n" \
"ds_read_b64_tr_b16 a[106:107], v209 offset:1024\n" \
"v_readlane_b32 s71, v227, s78\n" \
"v_mfma_f32_16x16x32_bf16 v[84:87], a[76:79], a[220:223], v[84:87]\n" \
"global_load_lds_dwordx4 v252, s[42:43] offset:2048\n" \
"v_lshrrev_b32 v226, 6, v248\n" \
"v_mfma_f32_16x16x32_bf16 v[52:55], a[200:203], a[80:83], v[52:55]\n" \
"s_add_u32 s42, s42, 0x80\n" \
"s_addc_u32 s43, s43, 0\n" \
"ds_read_b128 a[72:75], v205 offset:0\n" \
"v_mfma_f32_16x16x32_bf16 v[52:55], a[204:207], a[84:87], v[52:55]\n" \
"ds_read_b128 a[76:79], v206 offset:0\n" \
"v_subrev_u32 v227, s71, v227\n" \
"v_mfma_f32_16x16x32_bf16 v[88:91], a[80:83], a[208:211], v[88:91]\n" \
"ds_read_b64_tr_b16 a[108:109], v209 offset:12288\n" \
"v_lshl_add_u32 v227, v227, 3, v226\n" \
"v_mfma_f32_16x16x32_bf16 v[88:91], a[84:87], a[212:215], v[88:91]\n" \
"global_load_lds_dwordx4 v252, s[44:45] offset:1024\n" \
"v_cmp_gt_i32 vcc, s70, v227\n" \
"v_mfma_f32_16x16x32_bf16 v[92:95], a[80:83], a[216:219], v[92:95]\n" \
"ds_read_b64_tr_b16 a[110:111], v209 offset:13312\n" \
"v_mfma_f32_16x16x32_bf16 v[92:95], a[84:87], a[220:223], v[92:95]\n" \
"s_bcnt1_i32_b32 s70, vcc_lo\n" \
"s_lshr_b32 s70, s70, 2\n" \
"ds_read_b64_tr_b16 a[112:113], v210 offset:0\n" \
"v_mfma_f32_16x16x32_bf16 v[60:63], a[200:203], a[88:91], v[60:63]\n" \
"s_lshr_b32 s78, s70, 2\n" \
"ds_read_b128 a[80:83], v205 offset:4096\n" \
"s_mul_i32 s71, s78, 7\n" \
"v_mfma_f32_16x16x32_bf16 v[60:63], a[204:207], a[92:95], v[60:63]\n" \
"s_xor_b32 s70, s70, s71\n" \
"global_load_lds_dwordx4 v252, s[44:45] offset:3072\n" \
"v_mfma_f32_16x16x32_bf16 v[96:99], a[88:91], a[208:211], v[96:99]\n" \
"s_add_u32 s44, s44, 0x80\n" \
"s_addc_u32 s45, s45, 0\n" \
"ds_read_b128 a[84:87], v206 offset:4096\n" \
"v_mfma_f32_16x16x32_bf16 v[96:99], a[92:95], a[212:215], v[96:99]\n" \
"s_xor_b32 s78, s78, 1\n" \
"s_lshl_b32 s65, s70, 2\n" \
"ds_read_b64_tr_b16 a[114:115], v210 offset:1024\n" \
"v_mfma_f32_16x16x32_bf16 v[100:103], a[88:91], a[216:219], v[100:103]\n" \
"s_add_u32 m0, s85, 135168\n" \
"s_lshr_b32 s64, 0x8654, s65\n" \
"ds_read_b64_tr_b16 a[116:117], v210 offset:12288\n" \
"v_mfma_f32_16x16x32_bf16 v[100:103], a[92:95], a[220:223], v[100:103]\n" \
"buffer_load_dwordx4 v246, s[12:15], s46 offen lds\n" \
"v_mfma_f32_16x16x32_bf16 v[68:71], a[200:203], a[192:195], v[68:71]\n" \
"s_add_u32 s46, s46, 0x80\n" \
"ds_read_b128 a[88:91], v205 offset:8192\n" \
"s_and_b32 s64, s64, 15\n" \
"v_mfma_f32_16x16x32_bf16 v[68:71], a[204:207], a[196:199], v[68:71]\n" \
"s_add_u32 s64, s64, 32\n" \
"ds_read_b128 a[92:95], v206 offset:8192\n" \
"s_lshr_b32 s70, %1, 6\n" \
"v_mfma_f32_16x16x32_bf16 v[104:107], a[192:195], a[208:211], v[104:107]\n" \
"s_add_u32 m0, s84, 143360\n" \
"s_add_u32 s65, s65, s70\n" \
"ds_read_b64_tr_b16 a[118:119], v210 offset:13312\n" \
"v_mfma_f32_16x16x32_bf16 v[104:107], a[196:199], a[212:215], v[104:107]\n" \
"s_and_b32 s30, %1, 1\n" \
"global_load_lds_dwordx4 v253, s[48:49] offset:-4096\n" \
"v_mfma_f32_16x16x32_bf16 v[108:111], a[192:195], a[216:219], v[108:111]\n" \
"s_lshl_b32 s30, s30, 3\n" \
"s_add_u32 s30, s30, s63\n" \
"s_lshl_b32 s30, s30, 2\n" \
"v_mfma_f32_16x16x32_bf16 v[108:111], a[196:199], a[220:223], v[108:111]\n" \
"s_waitcnt lgkmcnt(2)\n" \
"s_lshl_b32 s70, s88, 7\n" \
"s_add_u32 s30, s30, s70\n" \
"v_mfma_f32_16x16x32_bf16 v[32:35], a[96:99], a[64:67], v[32:35]\n" \
"s_add_u32 s30, s30, 65591296\n" \
"ds_read_b128 a[192:195], v205 offset:12288\n" \
"v_lshrrev_b32 v221, 2, v248\n" \
"v_mfma_f32_16x16x32_bf16 v[32:35], a[100:103], a[68:71], v[32:35]\n" \
"s_lshl_b32 s81, s65, 7\n" \
"global_load_lds_dwordx4 v253, s[48:49] offset:-2048\n" \
"v_mfma_f32_16x16x32_bf16 v[40:43], a[96:99], a[72:75], v[40:43]\n" \
"s_add_u32 s82, s81, 65591296\n" \
"ds_read_b128 a[196:199], v206 offset:12288\n" \
"s_add_u32 s81, s81, 65589248\n" \
"v_mfma_f32_16x16x32_bf16 v[40:43], a[100:103], a[76:79], v[40:43]\n" \
"s_mov_b64 s[52:53], s[26:27]\n" \
"s_and_b32 s53, s53, 0xffff\n" \
"ds_read_b64_tr_b16 a[200:201], v210 offset:2048\n" \
"v_mfma_f32_16x16x32_bf16 v[0:3], a[104:107], a[72:75], v[0:3]\n" \
"s_mov_b32 s54, 8388608\n" \
"s_mov_b32 s55, 0x20000\n" \
"ds_read_b64_tr_b16 a[202:203], v210 offset:3072\n" \
"v_mfma_f32_16x16x32_bf16 v[0:3], a[108:111], a[76:79], v[0:3]\n" \
"s_waitcnt lgkmcnt(3)\n" \
"global_load_lds_dwordx4 v253, s[48:49] offset:0\n" \
"s_lshr_b32 s83, s63, 1\n" \
"v_mfma_f32_16x16x32_bf16 v[4:7], a[112:115], a[72:75], v[4:7]\n" \
"s_add_u32 s48, s48, 0x60000\n" \
"s_addc_u32 s49, s49, 0\n" \
"ds_read_b64_tr_b16 a[204:205], v210 offset:14336\n" \
"v_mfma_f32_16x16x32_bf16 v[4:7], a[116:119], a[76:79], v[4:7]\n" \
"s_add_u32 s83, s83, 2\n" \
"s_lshr_b32 s70, s18, 8\n" \
"ds_read_b64_tr_b16 a[206:207], v210 offset:15360\n" \
"v_mfma_f32_16x16x32_bf16 v[48:51], a[96:99], a[80:83], v[48:51]\n" \
"s_sub_u32 s83, s83, s70\n" \
"s_and_b32 s83, s83, 3\n" \
"ds_read_b64_tr_b16 a[208:209], v209 offset:4096\n" \
"v_mfma_f32_16x16x32_bf16 v[48:51], a[100:103], a[84:87], v[48:51]\n" \
"s_and_b32 s70, s63, 1\n" \
"global_load_lds_dwordx4 v253, s[50:51] offset:-3072\n" \
"v_mfma_f32_16x16x32_bf16 v[8:11], a[104:107], a[80:83], v[8:11]\n" \
"s_lshl_b32 s70, s70, 2\n" \
"s_or_b32 s83, s83, s70\n" \
"ds_read_b64_tr_b16 a[210:211], v209 offset:5120\n" \
"v_mfma_f32_16x16x32_bf16 v[8:11], a[108:111], a[84:87], v[8:11]\n" \
"s_mul_i32 s75, s78, s64\n" \
"ds_read_b64_tr_b16 a[212:213], v209 offset:16384\n" \
"v_lshrrev_b32 v228, 4, v248\n" \
"v_mfma_f32_16x16x32_bf16 v[12:15], a[112:115], a[80:83], v[12:15]\n" \
"s_lshl_b32 s70, s65, 2\n" \
"s_add_u32 s70, s70, s61\n" \
"ds_read_b64_tr_b16 a[214:215], v209 offset:17408\n" \
"v_mfma_f32_16x16x32_bf16 v[12:15], a[116:119], a[84:87], v[12:15]\n" \
"s_mul_i32 s73, s70, 69632\n" \
"global_load_lds_dwordx4 v253, s[50:51] offset:-1024\n" \
"v_mfma_f32_16x16x32_bf16 v[56:59], a[96:99], a[88:91], v[56:59]\n" \
"s_add_u32 s73, s73, 52740096\n" \
"s_add_u32 s73, s73, s28\n" \
"ds_read_b64_tr_b16 a[216:217], v210 offset:4096\n" \
"v_mfma_f32_16x16x32_bf16 v[56:59], a[100:103], a[92:95], v[56:59]\n" \
"s_mul_i32 s70, s70, 266240\n" \
"s_lshl_b32 s71, s75, 12\n" \
"ds_read_b64_tr_b16 a[218:219], v210 offset:5120\n" \
"v_mfma_f32_16x16x32_bf16 v[16:19], a[104:107], a[88:91], v[16:19]\n" \
"s_add_u32 s70, s70, s71\n" \
"s_add_u32 s70, s70, 35700736\n" \
"ds_read_b64_tr_b16 a[220:221], v210 offset:16384\n" \
"v_mfma_f32_16x16x32_bf16 v[16:19], a[108:111], a[92:95], v[16:19]\n" \
"s_add_u32 s36, s28, s70\n" \
"global_load_lds_dwordx4 v253, s[50:51] offset:1024\n" \
"v_mfma_f32_16x16x32_bf16 v[20:23], a[112:115], a[88:91], v[20:23]\n" \
"s_add_u32 s50, s50, 0x60000\n" \
"s_addc_u32 s51, s51, 0\n" \
"ds_read_b64_tr_b16 a[222:223], v210 offset:17408\n" \
"v_mfma_f32_16x16x32_bf16 v[20:23], a[116:119], a[92:95], v[20:23]\n" \
"s_waitcnt lgkmcnt(3)\n" \
"v_add_u32 v207, 0xffff0000, v207\n" \
"v_add_u32 v208, 0xffff0000, v208\n" \
"v_mfma_f32_16x16x32_bf16 v[64:67], a[96:99], a[192:195], v[64:67]\n" \
"s_mov_b32 s37, s29\n" \
"v_add_u32 v205, 0xffff0000, v205\n" \
"v_add_u32 v206, 0xffff0000, v206\n" \
"v_mfma_f32_16x16x32_bf16 v[64:67], a[100:103], a[196:199], v[64:67]\n" \
"s_lshr_b32 s74, s61, 1\n" \
"v_add_u32 v209, 0xffff0000, v209\n" \
"v_add_u32 v210, 0xffff0000, v210\n" \
"v_mfma_f32_16x16x32_bf16 v[24:27], a[104:107], a[192:195], v[24:27]\n" \
"s_lshl_b32 s74, s74, 16\n" \
"s_and_b32 s70, s61, 1\n" \
"s_lshl_b32 s70, s70, 14\n" \
"v_mfma_f32_16x16x32_bf16 v[24:27], a[108:111], a[196:199], v[24:27]\n" \
"s_add_u32 s74, s74, s70\n" \
"s_lshl_b32 s71, s83, 8\n" \
"s_add_u32 s74, s74, s71\n" \
"v_mfma_f32_16x16x32_bf16 v[28:31], a[112:115], a[192:195], v[28:31]\n" \
"s_waitcnt vmcnt(11) lgkmcnt(0)\n" \
"s_add_u32 s40, s24, s74\n" \
"s_addc_u32 s41, s25, 0\n" \
"v_mfma_f32_16x16x32_bf16 v[28:31], a[116:119], a[196:199], v[28:31]\n" \
"s_barrier\n" \
"v_mfma_f32_16x16x32_bf16 v[72:75], a[64:67], a[208:211], v[72:75]\n" \
"s_lshl_b32 s70, s75, 17\n" \
"s_add_u32 s70, s70, s74\n" \
"ds_read_b64_tr_b16 a[96:97], v209 offset:2048\n" \
"v_mfma_f32_16x16x32_bf16 v[72:75], a[68:71], a[212:215], v[72:75]\n" \
"s_add_u32 s38, s18, s70\n" \
"s_addc_u32 s39, s19, 0\n" \
"ds_read_b64_tr_b16 a[98:99], v209 offset:3072\n" \
"v_mfma_f32_16x16x32_bf16 v[44:47], a[200:203], a[72:75], v[44:47]\n" \
"s_add_u32 m0, s95, 65536\n" \
"ds_read_b128 a[64:67], v207 offset:0\n" \
"ds_read_b64_tr_b16 a[100:101], v209 offset:14336\n" \
"Lpg10467_%=:\n" \
"s_cbranch_execz Lpg12643_%=\n" \
"v_mfma_f32_16x16x32_bf16 v[44:47], a[204:207], a[76:79], v[44:47]\n" \
"global_load_lds_dwordx4 v252, s[42:43] offset:0\n" \
"v_bfe_u32 v229, v228, 2, 3\n" \
"v_mfma_f32_16x16x32_bf16 v[80:83], a[72:75], a[208:211], v[80:83]\n" \
"ds_read_b128 a[68:71], v208 offset:0\n" \
"ds_read_b64_tr_b16 a[102:103], v209 offset:15360\n" \
"v_mfma_f32_16x16x32_bf16 v[80:83], a[76:79], a[212:215], v[80:83]\n" \
"ds_read_b64_tr_b16 a[104:105], v209 offset:0\n" \
"v_lshlrev_b32 v229, 11, v229\n" \
"v_mfma_f32_16x16x32_bf16 v[84:87], a[72:75], a[216:219], v[84:87]\n" \
"ds_read_b64_tr_b16 a[106:107], v209 offset:1024\n" \
"v_lshrrev_b32 v230, 5, v228\n" \
"v_mfma_f32_16x16x32_bf16 v[84:87], a[76:79], a[220:223], v[84:87]\n" \
"global_load_lds_dwordx4 v252, s[42:43] offset:2048\n" \
"v_lshl_add_u32 v229, v230, 6, v229\n" \
"v_mfma_f32_16x16x32_bf16 v[52:55], a[200:203], a[80:83], v[52:55]\n" \
"s_add_u32 s42, s42, 0x80\n" \
"s_addc_u32 s43, s43, 0\n" \
"ds_read_b128 a[72:75], v205 offset:0\n" \
"v_mfma_f32_16x16x32_bf16 v[52:55], a[204:207], a[84:87], v[52:55]\n" \
"ds_read_b128 a[76:79], v206 offset:0\n" \
"v_and_b32 v230, 3, v228\n" \
"v_mfma_f32_16x16x32_bf16 v[88:91], a[80:83], a[208:211], v[88:91]\n" \
"ds_read_b64_tr_b16 a[108:109], v209 offset:12288\n" \
"v_bfe_u32 v231, v228, 4, 1\n" \
"v_mfma_f32_16x16x32_bf16 v[88:91], a[84:87], a[212:215], v[88:91]\n" \
"global_load_lds_dwordx4 v252, s[44:45] offset:1024\n" \
"v_lshlrev_b32 v231, 1, v231\n" \
"v_mfma_f32_16x16x32_bf16 v[92:95], a[80:83], a[216:219], v[92:95]\n" \
"ds_read_b64_tr_b16 a[110:111], v209 offset:13312\n" \
"v_xor_b32 v230, v230, v231\n" \
"v_mfma_f32_16x16x32_bf16 v[92:95], a[84:87], a[220:223], v[92:95]\n" \
"ds_read_b64_tr_b16 a[112:113], v210 offset:0\n" \
"v_lshl_add_u32 v229, v230, 4, v229\n" \
"v_mfma_f32_16x16x32_bf16 v[60:63], a[200:203], a[88:91], v[60:63]\n" \
"ds_read_b128 a[80:83], v205 offset:4096\n" \
"v_add_u32 v216, 4096, v229\n" \
"v_mfma_f32_16x16x32_bf16 v[60:63], a[204:207], a[92:95], v[60:63]\n" \
"global_load_lds_dwordx4 v252, s[44:45] offset:3072\n" \
"v_add_u32 v217, 3200, v229\n" \
"v_mfma_f32_16x16x32_bf16 v[96:99], a[88:91], a[208:211], v[96:99]\n" \
"s_add_u32 s44, s44, 0x80\n" \
"s_addc_u32 s45, s45, 0\n" \
"ds_read_b128 a[84:87], v206 offset:4096\n" \
"v_mfma_f32_16x16x32_bf16 v[96:99], a[92:95], a[212:215], v[96:99]\n" \
"ds_read_b64_tr_b16 a[114:115], v210 offset:1024\n" \
"v_add_u32 v218, 34816, v229\n" \
"v_mfma_f32_16x16x32_bf16 v[100:103], a[88:91], a[216:219], v[100:103]\n" \
"s_add_u32 m0, s85, 81920\n" \
"ds_read_b64_tr_b16 a[116:117], v210 offset:12288\n" \
"v_add_u32 v219, 33920, v229\n" \
"v_mfma_f32_16x16x32_bf16 v[100:103], a[92:95], a[220:223], v[100:103]\n" \
"buffer_load_dwordx4 v246, s[12:15], s46 offen lds\n" \
"v_mfma_f32_16x16x32_bf16 v[68:71], a[200:203], a[192:195], v[68:71]\n" \
"s_add_u32 s46, s46, 0x80\n" \
"ds_read_b128 a[88:91], v205 offset:8192\n" \
"s_mov_b32 s75, 65536\n" \
"v_mfma_f32_16x16x32_bf16 v[68:71], a[204:207], a[196:199], v[68:71]\n" \
"s_lshr_b32 s70, s61, 1\n" \
"ds_read_b128 a[92:95], v206 offset:8192\n" \
"s_lshl_b32 s71, s70, 13\n" \
"v_mfma_f32_16x16x32_bf16 v[104:107], a[192:195], a[208:211], v[104:107]\n" \
"s_add_u32 m0, s84, 90112\n" \
"ds_read_b64_tr_b16 a[118:119], v210 offset:13312\n" \
"v_add_u32 v211, s71, v249\n" \
"v_mfma_f32_16x16x32_bf16 v[104:107], a[196:199], a[212:215], v[104:107]\n" \
"s_lshl_b32 s71, s65, 1\n" \
"global_load_lds_dwordx4 v253, s[48:49] offset:-4096\n" \
"v_mfma_f32_16x16x32_bf16 v[108:111], a[192:195], a[216:219], v[108:111]\n" \
"s_add_u32 s70, s70, s71\n" \
"s_lshl_b32 s77, s70, 18\n" \
"s_lshl_b32 s71, s83, 8\n" \
"v_mfma_f32_16x16x32_bf16 v[108:111], a[196:199], a[220:223], v[108:111]\n" \
"s_waitcnt lgkmcnt(2)\n" \
"s_add_u32 s77, s77, s71\n" \
"s_lshl_b32 s71, s78, 11\n" \
"v_mfma_f32_16x16x32_bf16 v[32:35], a[96:99], a[64:67], v[32:35]\n" \
"s_add_u32 s77, s77, s71\n" \
"ds_read_b128 a[192:195], v205 offset:12288\n" \
"s_and_b32 s70, s61, 1\n" \
"v_mfma_f32_16x16x32_bf16 v[32:35], a[100:103], a[68:71], v[32:35]\n" \
"s_lshl_b32 s71, s70, 7\n" \
"global_load_lds_dwordx4 v253, s[48:49] offset:-2048\n" \
"v_mfma_f32_16x16x32_bf16 v[40:43], a[96:99], a[72:75], v[40:43]\n" \
"s_add_u32 s77, s77, s71\n" \
"ds_read_b128 a[196:199], v206 offset:12288\n" \
"v_and_b32 v229, 3, v228\n" \
"v_mfma_f32_16x16x32_bf16 v[40:43], a[100:103], a[76:79], v[40:43]\n" \
"ds_read_b64_tr_b16 a[200:201], v210 offset:2048\n" \
"v_lshlrev_b32 v229, 3, v229\n" \
"v_mfma_f32_16x16x32_bf16 v[0:3], a[104:107], a[72:75], v[0:3]\n" \
"ds_read_b64_tr_b16 a[202:203], v210 offset:3072\n" \
"v_bfe_u32 v230, v228, 2, 2\n" \
"v_mfma_f32_16x16x32_bf16 v[0:3], a[108:111], a[76:79], v[0:3]\n" \
"s_waitcnt lgkmcnt(3)\n" \
"global_load_lds_dwordx4 v253, s[48:49] offset:0\n" \
"v_lshl_add_u32 v229, v230, 6, v229\n" \
"v_mfma_f32_16x16x32_bf16 v[4:7], a[112:115], a[72:75], v[4:7]\n" \
"s_add_u32 s48, s48, 0x60000\n" \
"s_addc_u32 s49, s49, 0\n" \
"ds_read_b64_tr_b16 a[204:205], v210 offset:14336\n" \
"v_mfma_f32_16x16x32_bf16 v[4:7], a[116:119], a[76:79], v[4:7]\n" \
"ds_read_b64_tr_b16 a[206:207], v210 offset:15360\n" \
"v_bfe_u32 v230, v228, 4, 1\n" \
"v_mfma_f32_16x16x32_bf16 v[48:51], a[96:99], a[80:83], v[48:51]\n" \
"ds_read_b64_tr_b16 a[208:209], v209 offset:4096\n" \
"v_lshl_add_u32 v229, v230, 5, v229\n" \
"v_mfma_f32_16x16x32_bf16 v[48:51], a[100:103], a[84:87], v[48:51]\n" \
"global_load_lds_dwordx4 v253, s[50:51] offset:-3072\n" \
"v_lshl_add_u32 v229, v230, 8, v229\n" \
"v_mfma_f32_16x16x32_bf16 v[8:11], a[104:107], a[80:83], v[8:11]\n" \
"ds_read_b64_tr_b16 a[210:211], v209 offset:5120\n" \
"v_lshrrev_b32 v230, 5, v228\n" \
"v_mfma_f32_16x16x32_bf16 v[8:11], a[108:111], a[84:87], v[8:11]\n" \
"ds_read_b64_tr_b16 a[212:213], v209 offset:16384\n" \
"v_lshl_add_u32 v229, v230, 12, v229\n" \
"v_mfma_f32_16x16x32_bf16 v[12:15], a[112:115], a[80:83], v[12:15]\n" \
"s_lshl_b32 s71, s70, 10\n" \
"ds_read_b64_tr_b16 a[214:215], v209 offset:17408\n" \
"v_add_u32 v212, s71, v229\n" \
"v_mfma_f32_16x16x32_bf16 v[12:15], a[116:119], a[84:87], v[12:15]\n" \
"s_lshl_b32 s70, s65, 3\n" \
"global_load_lds_dwordx4 v253, s[50:51] offset:-1024\n" \
"v_mfma_f32_16x16x32_bf16 v[56:59], a[96:99], a[88:91], v[56:59]\n" \
"s_add_u32 s70, s70, s83\n" \
"s_lshl_b32 s76, s70, 16\n" \
"ds_read_b64_tr_b16 a[216:217], v210 offset:4096\n" \
"v_mfma_f32_16x16x32_bf16 v[56:59], a[100:103], a[92:95], v[56:59]\n" \
"s_lshl_b32 s71, s61, 14\n" \
"s_add_u32 s76, s76, s71\n" \
"ds_read_b64_tr_b16 a[218:219], v210 offset:5120\n" \
"v_mfma_f32_16x16x32_bf16 v[16:19], a[104:107], a[88:91], v[16:19]\n" \
"s_add_u32 s76, s76, 57196544\n" \
"s_lshl_b32 s79, s70, 7\n" \
"ds_read_b64_tr_b16 a[220:221], v210 offset:16384\n" \
"v_mfma_f32_16x16x32_bf16 v[16:19], a[108:111], a[92:95], v[16:19]\n" \
"s_add_u32 s79, s79, 65601536\n" \
"global_load_lds_dwordx4 v253, s[50:51] offset:1024\n" \
"v_mfma_f32_16x16x32_bf16 v[20:23], a[112:115], a[88:91], v[20:23]\n" \
"s_add_u32 s50, s50, 0x60000\n" \
"s_addc_u32 s51, s51, 0\n" \
"ds_read_b64_tr_b16 a[222:223], v210 offset:17408\n" \
"v_mfma_f32_16x16x32_bf16 v[20:23], a[116:119], a[92:95], v[20:23]\n" \
"s_waitcnt lgkmcnt(3)\n" \
"v_add_u32 v207, 0x1d000, v207\n" \
"v_add_u32 v208, 0x1d000, v208\n" \
"v_mfma_f32_16x16x32_bf16 v[64:67], a[96:99], a[192:195], v[64:67]\n" \
"s_lshr_b32 s70, s64, 1\n" \
"v_add_u32 v205, 0x1d000, v205\n" \
"v_add_u32 v206, 0x1d000, v206\n" \
"v_mfma_f32_16x16x32_bf16 v[64:67], a[100:103], a[196:199], v[64:67]\n" \
"s_lshl_b32 s70, -1, s70\n" \
"v_add_u32 v209, 0x1d000, v209\n" \
"v_add_u32 v210, 0x1d000, v210\n" \
"v_mfma_f32_16x16x32_bf16 v[24:27], a[104:107], a[192:195], v[24:27]\n" \
"s_add_u32 s71, s64, 1\n" \
"s_lshr_b32 s71, s71, 1\n" \
"s_bfm_b32 s71, s71, 0\n" \
"v_mfma_f32_16x16x32_bf16 v[24:27], a[108:111], a[196:199], v[24:27]\n" \
"s_cmp_eq_u32 s78, 0\n" \
"s_cselect_b32 s80, s71, s70\n" \
"s_mov_b32 s16, 0xff000000\n" \
"v_mfma_f32_16x16x32_bf16 v[28:31], a[112:115], a[192:195], v[28:31]\n" \
"s_waitcnt vmcnt(11) lgkmcnt(0)\n" \
"s_mov_b32 s17, 0\n" \
"v_add_u32 v222, 0x1c90080, v250\n" \
"v_mfma_f32_16x16x32_bf16 v[28:31], a[116:119], a[196:199], v[28:31]\n" \
"s_barrier\n" \
"v_mfma_f32_16x16x32_bf16 v[72:75], a[64:67], a[208:211], v[72:75]\n" \
"ds_read_b64_tr_b16 a[96:97], v209 offset:2048\n" \
"v_cndmask_b32 v222, v250, v222, s[16:17]\n" \
"v_mfma_f32_16x16x32_bf16 v[72:75], a[68:71], a[212:215], v[72:75]\n" \
"s_mov_b32 s74, 98304\n" \
"ds_read_b64_tr_b16 a[98:99], v209 offset:3072\n" \
"v_mfma_f32_16x16x32_bf16 v[44:47], a[200:203], a[72:75], v[44:47]\n" \
"s_add_u32 m0, s95, 0\n" \
"ds_read_b128 a[64:67], v207 offset:0\n" \
"ds_read_b64_tr_b16 a[100:101], v209 offset:14336\n" \
"v_mfma_f32_16x16x32_bf16 v[44:47], a[204:207], a[76:79], v[44:47]\n" \
"global_load_lds_dwordx4 v252, s[42:43] offset:0\n" \
"v_mfma_f32_16x16x32_bf16 v[80:83], a[72:75], a[208:211], v[80:83]\n" \
"ds_read_b128 a[68:71], v208 offset:0\n" \
"ds_read_b64_tr_b16 a[102:103], v209 offset:15360\n" \
"v_mfma_f32_16x16x32_bf16 v[80:83], a[76:79], a[212:215], v[80:83]\n" \
"s_sub_u32 s74, s74, 32768\n" \
"s_cselect_b32 s74, 131072, s74\n" \
"ds_read_b64_tr_b16 a[104:105], v209 offset:0\n" \
"v_mfma_f32_16x16x32_bf16 v[84:87], a[72:75], a[216:219], v[84:87]\n" \
"ds_read_b64_tr_b16 a[106:107], v209 offset:1024\n" \
"v_add_u32 v213, s74, v211\n" \
"v_mfma_f32_16x16x32_bf16 v[84:87], a[76:79], a[220:223], v[84:87]\n" \
"global_load_lds_dwordx4 v252, s[42:43] offset:2048\n" \
"v_add_u32 v214, s74, v212\n" \
"v_mfma_f32_16x16x32_bf16 v[52:55], a[200:203], a[80:83], v[52:55]\n" \
"s_add_u32 s42, s42, 0x80\n" \
"s_addc_u32 s43, s43, 0\n" \
"ds_read_b128 a[72:75], v205 offset:0\n" \
"v_mfma_f32_16x16x32_bf16 v[52:55], a[204:207], a[84:87], v[52:55]\n" \
"ds_read_b128 a[76:79], v206 offset:0\n" \
"v_xor_b32 v215, 32, v214\n" \
"v_mfma_f32_16x16x32_bf16 v[88:91], a[80:83], a[208:211], v[88:91]\n" \
"s_and_b32 s89, s61, 1\n" \
"s_lshl_b32 s89, s89, 11\n" \
"ds_read_b64_tr_b16 a[108:109], v209 offset:12288\n" \
"v_mfma_f32_16x16x32_bf16 v[88:91], a[84:87], a[212:215], v[88:91]\n" \
"s_add_u32 s87, s87, s89\n" \
"global_load_lds_dwordx4 v252, s[44:45] offset:1024\n" \
"v_mfma_f32_16x16x32_bf16 v[92:95], a[80:83], a[216:219], v[92:95]\n" \
"s_add_u32 s87, s87, 52740096\n" \
"ds_read_b64_tr_b16 a[110:111], v209 offset:13312\n" \
"v_mfma_f32_16x16x32_bf16 v[92:95], a[84:87], a[220:223], v[92:95]\n" \
"s_cmp_eq_u32 s88, 0\n" \
"s_cselect_b32 s86, -1, 0\n" \
"ds_read_b64_tr_b16 a[112:113], v210 offset:0\n" \
"v_mfma_f32_16x16x32_bf16 v[60:63], a[200:203], a[88:91], v[60:63]\n" \
"s_mul_i32 s89, s88, 278528\n" \
"ds_read_b128 a[80:83], v205 offset:4096\n" \
"s_add_u32 s87, s87, s89\n" \
"v_mfma_f32_16x16x32_bf16 v[60:63], a[204:207], a[92:95], v[60:63]\n" \
"s_lshr_b32 s89, s61, 1\n" \
"global_load_lds_dwordx4 v252, s[44:45] offset:3072\n" \
"v_mfma_f32_16x16x32_bf16 v[96:99], a[88:91], a[208:211], v[96:99]\n" \
"s_add_u32 s44, s44, 0x80\n" \
"s_addc_u32 s45, s45, 0\n" \
"ds_read_b128 a[84:87], v206 offset:4096\n" \
"v_mfma_f32_16x16x32_bf16 v[96:99], a[92:95], a[212:215], v[96:99]\n" \
"s_lshl_b32 s89, s89, 8\n" \
"s_add_u32 s87, s87, s89\n" \
"ds_read_b64_tr_b16 a[114:115], v210 offset:1024\n" \
"v_mfma_f32_16x16x32_bf16 v[100:103], a[88:91], a[216:219], v[100:103]\n" \
"s_add_u32 m0, s85, 16384\n" \
"s_lshl_b32 s89, s61, 4\n" \
"ds_read_b64_tr_b16 a[116:117], v210 offset:12288\n" \
"v_mfma_f32_16x16x32_bf16 v[100:103], a[92:95], a[220:223], v[100:103]\n" \
"buffer_load_dwordx4 v246, s[12:15], s46 offen lds\n" \
"v_mfma_f32_16x16x32_bf16 v[68:71], a[200:203], a[192:195], v[68:71]\n" \
"s_add_u32 s46, s46, 0x80\n" \
"ds_read_b128 a[88:91], v205 offset:8192\n" \
"v_subrev_u32 v147, s89, v251\n" \
"v_mfma_f32_16x16x32_bf16 v[68:71], a[204:207], a[196:199], v[68:71]\n" \
"s_lshr_b32 s89, s61, 1\n" \
"ds_read_b128 a[92:95], v206 offset:8192\n" \
"s_lshl_b32 s90, s89, 9\n" \
"v_mfma_f32_16x16x32_bf16 v[104:107], a[192:195], a[208:211], v[104:107]\n" \
"s_add_u32 m0, s84, 24576\n" \
"s_add_u32 s90, s90, 511\n" \
"ds_read_b64_tr_b16 a[118:119], v210 offset:13312\n" \
"v_mfma_f32_16x16x32_bf16 v[104:107], a[196:199], a[212:215], v[104:107]\n" \
"global_load_lds_dwordx4 v253, s[48:49] offset:-4096\n" \
"v_sub_u32 v145, s90, v248\n" \
"v_mfma_f32_16x16x32_bf16 v[108:111], a[192:195], a[216:219], v[108:111]\n" \
"s_lshl_b32 s90, s89, 3\n" \
"s_not_b32 s90, s90\n" \
"v_sub_u32 v146, s90, v147\n" \
"v_mfma_f32_16x16x32_bf16 v[108:111], a[196:199], a[220:223], v[108:111]\n" \
"s_waitcnt lgkmcnt(2)\n" \
"v_subrev_u32 v152, 0, v147\n" \
"v_sub_u32 v144, 7, v147\n" \
"v_mfma_f32_16x16x32_bf16 v[32:35], a[96:99], a[64:67], v[32:35]\n" \
"ds_read_b128 a[192:195], v205 offset:12288\n" \
"v_or_b32 v152, v152, v144\n" \
"v_mfma_f32_16x16x32_bf16 v[32:35], a[100:103], a[68:71], v[32:35]\n" \
"global_load_lds_dwordx4 v253, s[48:49] offset:-2048\n" \
"v_ashrrev_i32 v152, 31, v152\n" \
"v_mfma_f32_16x16x32_bf16 v[40:43], a[96:99], a[72:75], v[40:43]\n" \
"ds_read_b128 a[196:199], v206 offset:12288\n" \
"v_and_b32 v152, 0xff800000, v152\n" \
"v_mfma_f32_16x16x32_bf16 v[40:43], a[100:103], a[76:79], v[40:43]\n" \
"ds_read_b64_tr_b16 a[200:201], v210 offset:2048\n" \
"v_mov_b32 v160, v152\n" \
"v_mfma_f32_16x16x32_bf16 v[0:3], a[104:107], a[72:75], v[0:3]\n" \
"ds_read_b64_tr_b16 a[202:203], v210 offset:3072\n" \
"v_mov_b32 v168, v152\n" \
"v_mfma_f32_16x16x32_bf16 v[0:3], a[108:111], a[76:79], v[0:3]\n" \
"s_waitcnt lgkmcnt(3)\n" \
"global_load_lds_dwordx4 v253, s[48:49] offset:0\n" \
"v_mov_b32 v176, v152\n" \
"v_mfma_f32_16x16x32_bf16 v[4:7], a[112:115], a[72:75], v[4:7]\n" \
"s_add_u32 s48, s48, 0x60000\n" \
"s_addc_u32 s49, s49, 0\n" \
"ds_read_b64_tr_b16 a[204:205], v210 offset:14336\n" \
"v_mfma_f32_16x16x32_bf16 v[4:7], a[116:119], a[76:79], v[4:7]\n" \
"ds_read_b64_tr_b16 a[206:207], v210 offset:15360\n" \
"v_sub_u32 v156, -9, v147\n" \
"v_mfma_f32_16x16x32_bf16 v[48:51], a[96:99], a[80:83], v[48:51]\n" \
"ds_read_b64_tr_b16 a[208:209], v209 offset:4096\n" \
"v_ashrrev_i32 v156, 31, v156\n" \
"v_mfma_f32_16x16x32_bf16 v[48:51], a[100:103], a[84:87], v[48:51]\n" \
"global_load_lds_dwordx4 v253, s[50:51] offset:-3072\n" \
"v_and_b32 v156, 0xff800000, v156\n" \
"v_mfma_f32_16x16x32_bf16 v[8:11], a[104:107], a[80:83], v[8:11]\n" \
"ds_read_b64_tr_b16 a[210:211], v209 offset:5120\n" \
"v_mov_b32 v164, v156\n" \
"v_mfma_f32_16x16x32_bf16 v[8:11], a[108:111], a[84:87], v[8:11]\n" \
"ds_read_b64_tr_b16 a[212:213], v209 offset:16384\n" \
"v_mov_b32 v172, v156\n" \
"v_mfma_f32_16x16x32_bf16 v[12:15], a[112:115], a[80:83], v[12:15]\n" \
"ds_read_b64_tr_b16 a[214:215], v209 offset:17408\n" \
"v_add_u32 v148, 0, v146\n" \
"v_mfma_f32_16x16x32_bf16 v[12:15], a[116:119], a[84:87], v[12:15]\n" \
"global_load_lds_dwordx4 v253, s[50:51] offset:-1024\n" \
"v_or3_b32 v148, v148, v145, s86\n" \
"v_mfma_f32_16x16x32_bf16 v[56:59], a[96:99], a[88:91], v[56:59]\n" \
"ds_read_b64_tr_b16 a[216:217], v210 offset:4096\n" \
"v_ashrrev_i32 v148, 31, v148\n" \
"v_mfma_f32_16x16x32_bf16 v[56:59], a[100:103], a[92:95], v[56:59]\n" \
"ds_read_b64_tr_b16 a[218:219], v210 offset:5120\n" \
"v_and_b32 v148, 0xff800000, v148\n" \
"v_mfma_f32_16x16x32_bf16 v[16:19], a[104:107], a[88:91], v[16:19]\n" \
"ds_read_b64_tr_b16 a[220:221], v210 offset:16384\n" \
"v_subrev_u32 v153, 1, v147\n" \
"v_mfma_f32_16x16x32_bf16 v[16:19], a[108:111], a[92:95], v[16:19]\n" \
"global_load_lds_dwordx4 v253, s[50:51] offset:1024\n" \
"v_sub_u32 v144, 8, v147\n" \
"v_mfma_f32_16x16x32_bf16 v[20:23], a[112:115], a[88:91], v[20:23]\n" \
"s_add_u32 s50, s50, 0x60000\n" \
"s_addc_u32 s51, s51, 0\n" \
"ds_read_b64_tr_b16 a[222:223], v210 offset:17408\n" \
"v_mfma_f32_16x16x32_bf16 v[20:23], a[116:119], a[92:95], v[20:23]\n" \
"s_waitcnt lgkmcnt(3)\n" \
"v_add_u32 v207, 0xffff3000, v207\n" \
"v_add_u32 v208, 0xffff3000, v208\n" \
"v_mfma_f32_16x16x32_bf16 v[64:67], a[96:99], a[192:195], v[64:67]\n" \
"v_add_u32 v205, 0xffff3000, v205\n" \
"v_add_u32 v206, 0xffff3000, v206\n" \
"v_mfma_f32_16x16x32_bf16 v[64:67], a[100:103], a[196:199], v[64:67]\n" \
"buffer_load_dword v220, v221, s[56:59], s81 offen sc1\n" \
"v_mfma_f32_16x16x32_bf16 v[24:27], a[104:107], a[192:195], v[24:27]\n" \
"v_add_u32 v209, 0xffff3000, v209\n" \
"v_add_u32 v210, 0xffff3000, v210\n" \
"v_mfma_f32_16x16x32_bf16 v[24:27], a[108:111], a[196:199], v[24:27]\n" \
"v_or_b32 v153, v153, v144\n" \
"v_ashrrev_i32 v153, 31, v153\n" \
"v_mfma_f32_16x16x32_bf16 v[28:31], a[112:115], a[192:195], v[28:31]\n" \
"s_waitcnt vmcnt(12) lgkmcnt(0)\n" \
"v_and_b32 v153, 0xff800000, v153\n" \
"v_mov_b32 v161, v153\n" \
"v_mfma_f32_16x16x32_bf16 v[28:31], a[116:119], a[196:199], v[28:31]\n" \
"s_barrier\n" \
"v_mfma_f32_16x16x32_bf16 v[72:75], a[64:67], a[208:211], v[72:75]\n" \
"ds_read_b64_tr_b16 a[96:97], v209 offset:2048\n" \
"v_mov_b32 v169, v153\n" \
"v_mfma_f32_16x16x32_bf16 v[72:75], a[68:71], a[212:215], v[72:75]\n" \
"ds_read_b64_tr_b16 a[98:99], v209 offset:3072\n" \
"v_mov_b32 v177, v153\n" \
"v_mfma_f32_16x16x32_bf16 v[44:47], a[200:203], a[72:75], v[44:47]\n" \
"s_add_u32 m0, s95, 118784\n" \
"ds_read_b128 a[64:67], v207 offset:0\n" \
"ds_read_b64_tr_b16 a[100:101], v209 offset:14336\n" \
"v_mfma_f32_16x16x32_bf16 v[44:47], a[204:207], a[76:79], v[44:47]\n" \
"global_load_lds_dwordx4 v252, s[42:43] offset:0\n" \
"v_sub_u32 v157, -8, v147\n" \
"v_mfma_f32_16x16x32_bf16 v[80:83], a[72:75], a[208:211], v[80:83]\n" \
"ds_read_b128 a[68:71], v208 offset:0\n" \
"ds_read_b64_tr_b16 a[102:103], v209 offset:15360\n" \
"v_mfma_f32_16x16x32_bf16 v[80:83], a[76:79], a[212:215], v[80:83]\n" \
"ds_read_b64_tr_b16 a[104:105], v209 offset:0\n" \
"v_ashrrev_i32 v157, 31, v157\n" \
"v_mfma_f32_16x16x32_bf16 v[84:87], a[72:75], a[216:219], v[84:87]\n" \
"ds_read_b64_tr_b16 a[106:107], v209 offset:1024\n" \
"v_and_b32 v157, 0xff800000, v157\n" \
"v_mfma_f32_16x16x32_bf16 v[84:87], a[76:79], a[220:223], v[84:87]\n" \
"global_load_lds_dwordx4 v252, s[42:43] offset:2048\n" \
"v_mov_b32 v165, v157\n" \
"v_mfma_f32_16x16x32_bf16 v[52:55], a[200:203], a[80:83], v[52:55]\n" \
"ds_read_b128 a[72:75], v205 offset:0\n" \
"v_mov_b32 v173, v157\n" \
"v_mfma_f32_16x16x32_bf16 v[52:55], a[204:207], a[84:87], v[52:55]\n" \
"ds_read_b128 a[76:79], v206 offset:0\n" \
"v_add_u32 v149, 1, v146\n" \
"v_mfma_f32_16x16x32_bf16 v[88:91], a[80:83], a[208:211], v[88:91]\n" \
"ds_read_b64_tr_b16 a[108:109], v209 offset:12288\n" \
"v_or3_b32 v149, v149, v145, s86\n" \
"v_mfma_f32_16x16x32_bf16 v[88:91], a[84:87], a[212:215], v[88:91]\n" \
"global_load_lds_dwordx4 v252, s[44:45] offset:1024\n" \
"v_ashrrev_i32 v149, 31, v149\n" \
"v_mfma_f32_16x16x32_bf16 v[92:95], a[80:83], a[216:219], v[92:95]\n" \
"ds_read_b64_tr_b16 a[110:111], v209 offset:13312\n" \
"v_and_b32 v149, 0xff800000, v149\n" \
"v_mfma_f32_16x16x32_bf16 v[92:95], a[84:87], a[220:223], v[92:95]\n" \
"ds_read_b64_tr_b16 a[112:113], v210 offset:0\n" \
"v_subrev_u32 v154, 2, v147\n" \
"v_mfma_f32_16x16x32_bf16 v[60:63], a[200:203], a[88:91], v[60:63]\n" \
"ds_read_b128 a[80:83], v205 offset:4096\n" \
"v_sub_u32 v144, 9, v147\n" \
"v_mfma_f32_16x16x32_bf16 v[60:63], a[204:207], a[92:95], v[60:63]\n" \
"global_load_lds_dwordx4 v252, s[44:45] offset:3072\n" \
"v_or_b32 v154, v154, v144\n" \
"v_mfma_f32_16x16x32_bf16 v[96:99], a[88:91], a[208:211], v[96:99]\n" \
"ds_read_b128 a[84:87], v206 offset:4096\n" \
"v_ashrrev_i32 v154, 31, v154\n" \
"v_mfma_f32_16x16x32_bf16 v[96:99], a[92:95], a[212:215], v[96:99]\n" \
"ds_read_b64_tr_b16 a[114:115], v210 offset:1024\n" \
"v_and_b32 v154, 0xff800000, v154\n" \
"v_mfma_f32_16x16x32_bf16 v[100:103], a[88:91], a[216:219], v[100:103]\n" \
"s_add_u32 m0, s85, 135168\n" \
"ds_read_b64_tr_b16 a[116:117], v210 offset:12288\n" \
"v_mov_b32 v162, v154\n" \
"v_mfma_f32_16x16x32_bf16 v[100:103], a[92:95], a[220:223], v[100:103]\n" \
"buffer_load_dwordx4 v246, s[12:15], s46 offen lds\n" \
"v_mfma_f32_16x16x32_bf16 v[68:71], a[200:203], a[192:195], v[68:71]\n" \
"ds_read_b128 a[88:91], v205 offset:8192\n" \
"v_mov_b32 v170, v154\n" \
"v_mfma_f32_16x16x32_bf16 v[68:71], a[204:207], a[196:199], v[68:71]\n" \
"ds_read_b128 a[92:95], v206 offset:8192\n" \
"v_mov_b32 v178, v154\n" \
"v_mfma_f32_16x16x32_bf16 v[104:107], a[192:195], a[208:211], v[104:107]\n" \
"s_add_u32 m0, s84, 143360\n" \
"ds_read_b64_tr_b16 a[118:119], v210 offset:13312\n" \
"v_sub_u32 v158, -7, v147\n" \
"v_mfma_f32_16x16x32_bf16 v[104:107], a[196:199], a[212:215], v[104:107]\n" \
"global_load_lds_dwordx4 v253, s[48:49] offset:-4096\n" \
"v_ashrrev_i32 v158, 31, v158\n" \
"v_mfma_f32_16x16x32_bf16 v[108:111], a[192:195], a[216:219], v[108:111]\n" \
"v_and_b32 v158, 0xff800000, v158\n" \
"v_mov_b32 v166, v158\n" \
"v_mfma_f32_16x16x32_bf16 v[108:111], a[196:199], a[220:223], v[108:111]\n" \
"s_waitcnt lgkmcnt(2)\n" \
"v_mov_b32 v174, v158\n" \
"v_add_u32 v150, 2, v146\n" \
"v_mfma_f32_16x16x32_bf16 v[32:35], a[96:99], a[64:67], v[32:35]\n" \
"ds_read_b128 a[192:195], v205 offset:12288\n" \
"v_or3_b32 v150, v150, v145, s86\n" \
"v_mfma_f32_16x16x32_bf16 v[32:35], a[100:103], a[68:71], v[32:35]\n" \
"global_load_lds_dwordx4 v253, s[48:49] offset:-2048\n" \
"v_ashrrev_i32 v150, 31, v150\n" \
"v_mfma_f32_16x16x32_bf16 v[40:43], a[96:99], a[72:75], v[40:43]\n" \
"ds_read_b128 a[196:199], v206 offset:12288\n" \
"v_and_b32 v150, 0xff800000, v150\n" \
"v_mfma_f32_16x16x32_bf16 v[40:43], a[100:103], a[76:79], v[40:43]\n" \
"ds_read_b64_tr_b16 a[200:201], v210 offset:2048\n" \
"v_subrev_u32 v155, 3, v147\n" \
"v_mfma_f32_16x16x32_bf16 v[0:3], a[104:107], a[72:75], v[0:3]\n" \
"ds_read_b64_tr_b16 a[202:203], v210 offset:3072\n" \
"v_sub_u32 v144, 10, v147\n" \
"v_mfma_f32_16x16x32_bf16 v[0:3], a[108:111], a[76:79], v[0:3]\n" \
"s_waitcnt lgkmcnt(3)\n" \
"global_load_lds_dwordx4 v253, s[48:49] offset:0\n" \
"v_or_b32 v155, v155, v144\n" \
"v_mfma_f32_16x16x32_bf16 v[4:7], a[112:115], a[72:75], v[4:7]\n" \
"ds_read_b64_tr_b16 a[204:205], v210 offset:14336\n" \
"v_ashrrev_i32 v155, 31, v155\n" \
"v_mfma_f32_16x16x32_bf16 v[4:7], a[116:119], a[76:79], v[4:7]\n" \
"ds_read_b64_tr_b16 a[206:207], v210 offset:15360\n" \
"v_and_b32 v155, 0xff800000, v155\n" \
"v_mfma_f32_16x16x32_bf16 v[48:51], a[96:99], a[80:83], v[48:51]\n" \
"ds_read_b64_tr_b16 a[208:209], v209 offset:4096\n" \
"v_mov_b32 v163, v155\n" \
"v_mfma_f32_16x16x32_bf16 v[48:51], a[100:103], a[84:87], v[48:51]\n" \
"global_load_lds_dwordx4 v253, s[50:51] offset:-3072\n" \
"v_mov_b32 v171, v155\n" \
"v_mfma_f32_16x16x32_bf16 v[8:11], a[104:107], a[80:83], v[8:11]\n" \
"ds_read_b64_tr_b16 a[210:211], v209 offset:5120\n" \
"v_mov_b32 v179, v155\n" \
"v_mfma_f32_16x16x32_bf16 v[8:11], a[108:111], a[84:87], v[8:11]\n" \
"ds_read_b64_tr_b16 a[212:213], v209 offset:16384\n" \
"v_sub_u32 v159, -6, v147\n" \
"v_mfma_f32_16x16x32_bf16 v[12:15], a[112:115], a[80:83], v[12:15]\n" \
"ds_read_b64_tr_b16 a[214:215], v209 offset:17408\n" \
"v_ashrrev_i32 v159, 31, v159\n" \
"v_mfma_f32_16x16x32_bf16 v[12:15], a[116:119], a[84:87], v[12:15]\n" \
"global_load_lds_dwordx4 v253, s[50:51] offset:-1024\n" \
"v_and_b32 v159, 0xff800000, v159\n" \
"v_mfma_f32_16x16x32_bf16 v[56:59], a[96:99], a[88:91], v[56:59]\n" \
"ds_read_b64_tr_b16 a[216:217], v210 offset:4096\n" \
"v_mov_b32 v167, v159\n" \
"v_mfma_f32_16x16x32_bf16 v[56:59], a[100:103], a[92:95], v[56:59]\n" \
"ds_read_b64_tr_b16 a[218:219], v210 offset:5120\n" \
"v_mov_b32 v175, v159\n" \
"v_mfma_f32_16x16x32_bf16 v[16:19], a[104:107], a[88:91], v[16:19]\n" \
"ds_read_b64_tr_b16 a[220:221], v210 offset:16384\n" \
"v_add_u32 v151, 3, v146\n" \
"v_mfma_f32_16x16x32_bf16 v[16:19], a[108:111], a[92:95], v[16:19]\n" \
"global_load_lds_dwordx4 v253, s[50:51] offset:1024\n" \
"v_or3_b32 v151, v151, v145, s86\n" \
"v_mfma_f32_16x16x32_bf16 v[20:23], a[112:115], a[88:91], v[20:23]\n" \
"ds_read_b64_tr_b16 a[222:223], v210 offset:17408\n" \
"v_ashrrev_i32 v151, 31, v151\n" \
"v_mfma_f32_16x16x32_bf16 v[20:23], a[116:119], a[92:95], v[20:23]\n" \
"s_waitcnt lgkmcnt(3)\n" \
"v_add_u32 v207, 0xffff0000, v207\n" \
"v_add_u32 v208, 0xffff0000, v208\n" \
"v_mfma_f32_16x16x32_bf16 v[64:67], a[96:99], a[192:195], v[64:67]\n" \
"v_add_u32 v205, 0xffff0000, v205\n" \
"v_add_u32 v206, 0xffff0000, v206\n" \
"v_mfma_f32_16x16x32_bf16 v[64:67], a[100:103], a[196:199], v[64:67]\n" \
"v_add_u32 v209, 0xffff0000, v209\n" \
"v_add_u32 v210, 0xffff0000, v210\n" \
"v_mfma_f32_16x16x32_bf16 v[24:27], a[104:107], a[192:195], v[24:27]\n" \
"v_and_b32 v151, 0xff800000, v151\n" \
"v_mov_b32 v144, 0x3e8293ee\n" \
"v_mfma_f32_16x16x32_bf16 v[24:27], a[108:111], a[196:199], v[24:27]\n" \
"s_add_u32 s90, s95, 117760\n" \
"v_add_u32 v223, s90, v248\n" \
"v_mfma_f32_16x16x32_bf16 v[28:31], a[112:115], a[192:195], v[28:31]\n" \
"s_waitcnt vmcnt(12) lgkmcnt(0)\n" \
"s_cmp_lg_u32 s89, 0\n" \
"s_cselect_b64 s[92:93], -1, 0\n" \
"v_mfma_f32_16x16x32_bf16 v[28:31], a[116:119], a[196:199], v[28:31]\n" \
"s_barrier\n" \
"v_mfma_f32_16x16x32_bf16 v[72:75], a[64:67], a[208:211], v[72:75]\n" \
"s_waitcnt vmcnt(11)\n" \
"ds_read_b64_tr_b16 a[96:97], v209 offset:2048\n" \
"v_cmp_ne_u32 vcc, s60, v220\n" \
"v_mfma_f32_16x16x32_bf16 v[72:75], a[68:71], a[212:215], v[72:75]\n" \
"ds_read_b64_tr_b16 a[98:99], v209 offset:3072\n" \
"v_xor_b32 v145, 0x2000, v223\n" \
"v_mfma_f32_16x16x32_bf16 v[44:47], a[200:203], a[72:75], v[44:47]\n" \
"ds_read_b128 a[64:67], v207 offset:0\n" \
"ds_read_b64_tr_b16 a[100:101], v209 offset:14336\n" \
"v_mfma_f32_16x16x32_bf16 v[44:47], a[204:207], a[76:79], v[44:47]\n" \
"s_and_b32 vcc_hi, vcc_lo, s80\n" \
"s_cbranch_scc1 Lpl2_%=\n" \
"Lpr2_%=:\n" \
"v_mfma_f32_16x16x32_bf16 v[80:83], a[72:75], a[208:211], v[80:83]\n" \
"s_add_u32 m0, s75, s95\n" \
"ds_read_b128 a[68:71], v208 offset:0\n" \
"ds_read_b64_tr_b16 a[102:103], v209 offset:15360\n" \
"v_mfma_f32_16x16x32_bf16 v[80:83], a[76:79], a[212:215], v[80:83]\n" \
"ds_read_b64_tr_b16 a[104:105], v209 offset:0\n" \
"global_load_lds_dwordx4 v222, s[36:37] offset:0 sc1\n" \
"v_mfma_f32_16x16x32_bf16 v[84:87], a[72:75], a[216:219], v[84:87]\n" \
"s_cmp_eq_u32 s89, 0\n" \
"s_cselect_b32 s16, 0, 0xff00\n" \
"ds_read_b64_tr_b16 a[106:107], v209 offset:1024\n" \
"v_mfma_f32_16x16x32_bf16 v[84:87], a[76:79], a[220:223], v[84:87]\n" \
"s_mov_b32 s17, s16\n" \
"v_add_u32 v146, 0x1c90080, v249\n" \
"v_cndmask_b32 v146, v249, v146, s[16:17]\n" \
"v_mfma_f32_16x16x32_bf16 v[52:55], a[200:203], a[80:83], v[52:55]\n" \
"ds_read_b128 a[72:75], v205 offset:0\n" \
"v_mfma_f32_16x16x32_bf16 v[52:55], a[204:207], a[84:87], v[52:55]\n" \
"ds_read_b128 a[76:79], v206 offset:0\n" \
"global_load_lds_dwordx4 v222, s[36:37] offset:1024 sc1\n" \
"v_mfma_f32_16x16x32_bf16 v[88:91], a[80:83], a[208:211], v[88:91]\n" \
"ds_read_b64_tr_b16 a[108:109], v209 offset:12288\n" \
"v_mfma_f32_16x16x32_bf16 v[88:91], a[84:87], a[212:215], v[88:91]\n" \
"v_mfma_f32_16x16x32_bf16 v[92:95], a[80:83], a[216:219], v[92:95]\n" \
"ds_read_b64_tr_b16 a[110:111], v209 offset:13312\n" \
"v_mfma_f32_16x16x32_bf16 v[92:95], a[84:87], a[220:223], v[92:95]\n" \
"ds_read_b64_tr_b16 a[112:113], v210 offset:0\n" \
"global_load_lds_dwordx4 v222, s[36:37] offset:2048 sc1\n" \
"v_mfma_f32_16x16x32_bf16 v[60:63], a[200:203], a[88:91], v[60:63]\n" \
"ds_read_b128 a[80:83], v205 offset:4096\n" \
"v_mfma_f32_16x16x32_bf16 v[60:63], a[204:207], a[92:95], v[60:63]\n" \
"v_mfma_f32_16x16x32_bf16 v[96:99], a[88:91], a[208:211], v[96:99]\n" \
"ds_read_b128 a[84:87], v206 offset:4096\n" \
"v_mfma_f32_16x16x32_bf16 v[96:99], a[92:95], a[212:215], v[96:99]\n" \
"ds_read_b64_tr_b16 a[114:115], v210 offset:1024\n" \
"global_load_lds_dwordx4 v222, s[36:37] offset:3072 sc1\n" \
"v_mfma_f32_16x16x32_bf16 v[100:103], a[88:91], a[216:219], v[100:103]\n" \
"s_add_u32 s36, s36, 0x1000\n" \
"ds_read_b64_tr_b16 a[116:117], v210 offset:12288\n" \
"v_mfma_f32_16x16x32_bf16 v[100:103], a[92:95], a[220:223], v[100:103]\n" \
"v_mfma_f32_16x16x32_bf16 v[68:71], a[200:203], a[192:195], v[68:71]\n" \
"ds_read_b128 a[88:91], v205 offset:8192\n" \
"s_add_u32 m0, m0, 0x5000\n" \
"v_mfma_f32_16x16x32_bf16 v[68:71], a[204:207], a[196:199], v[68:71]\n" \
"ds_read_b128 a[92:95], v206 offset:8192\n" \
"global_load_lds_dwordx4 v216, s[38:39] offset:-4096\n" \
"v_mfma_f32_16x16x32_bf16 v[104:107], a[192:195], a[208:211], v[104:107]\n" \
"ds_read_b64_tr_b16 a[118:119], v210 offset:13312\n" \
"v_mfma_f32_16x16x32_bf16 v[104:107], a[196:199], a[212:215], v[104:107]\n" \
"v_mfma_f32_16x16x32_bf16 v[108:111], a[192:195], a[216:219], v[108:111]\n" \
"v_mfma_f32_16x16x32_bf16 v[108:111], a[196:199], a[220:223], v[108:111]\n" \
"s_waitcnt lgkmcnt(2)\n" \
"global_load_lds_dwordx4 v217, s[38:39] offset:-3072\n" \
"v_mfma_f32_16x16x32_bf16 v[32:35], a[96:99], a[64:67], v[32:35]\n" \
"ds_read_b128 a[192:195], v205 offset:12288\n" \
"v_mfma_f32_16x16x32_bf16 v[32:35], a[100:103], a[68:71], v[32:35]\n" \
"v_mfma_f32_16x16x32_bf16 v[40:43], a[96:99], a[72:75], v[40:43]\n" \
"ds_read_b128 a[196:199], v206 offset:12288\n" \
"v_mfma_f32_16x16x32_bf16 v[40:43], a[100:103], a[76:79], v[40:43]\n" \
"ds_read_b64_tr_b16 a[200:201], v210 offset:2048\n" \
"global_load_lds_dwordx4 v218, s[38:39] offset:-2048\n" \
"v_mfma_f32_16x16x32_bf16 v[0:3], a[104:107], a[72:75], v[0:3]\n" \
"ds_read_b64_tr_b16 a[202:203], v210 offset:3072\n" \
"v_mfma_f32_16x16x32_bf16 v[0:3], a[108:111], a[76:79], v[0:3]\n" \
"s_waitcnt lgkmcnt(3)\n" \
"v_mfma_f32_16x16x32_bf16 v[4:7], a[112:115], a[72:75], v[4:7]\n" \
"ds_read_b64_tr_b16 a[204:205], v210 offset:14336\n" \
"v_mfma_f32_16x16x32_bf16 v[4:7], a[116:119], a[76:79], v[4:7]\n" \
"ds_read_b64_tr_b16 a[206:207], v210 offset:15360\n" \
"global_load_lds_dwordx4 v219, s[38:39] offset:-1024\n" \
"v_mfma_f32_16x16x32_bf16 v[48:51], a[96:99], a[80:83], v[48:51]\n" \
"Lpg11053_%=:\n" \
"s_cbranch_execz Lpg12731_%=\n" \
"s_add_u32 s38, s38, 0x20000\n" \
"s_addc_u32 s39, s39, 0\n" \
"ds_read_b64_tr_b16 a[208:209], v209 offset:4096\n" \
"v_mfma_f32_16x16x32_bf16 v[48:51], a[100:103], a[84:87], v[48:51]\n" \
"s_sub_u32 s75, s75, 32768\n" \
"s_cselect_b32 s75, 131072, s75\n" \
"v_mfma_f32_16x16x32_bf16 v[8:11], a[104:107], a[80:83], v[8:11]\n" \
"ds_read_b64_tr_b16 a[210:211], v209 offset:5120\n" \
"v_mfma_f32_16x16x32_bf16 v[8:11], a[108:111], a[84:87], v[8:11]\n" \
"ds_read_b64_tr_b16 a[212:213], v209 offset:16384\n" \
"v_mfma_f32_16x16x32_bf16 v[12:15], a[112:115], a[80:83], v[12:15]\n" \
"ds_read_b64_tr_b16 a[214:215], v209 offset:17408\n" \
"v_mfma_f32_16x16x32_bf16 v[12:15], a[116:119], a[84:87], v[12:15]\n" \
"v_mfma_f32_16x16x32_bf16 v[56:59], a[96:99], a[88:91], v[56:59]\n" \
"ds_read_b64_tr_b16 a[216:217], v210 offset:4096\n" \
"v_mfma_f32_16x16x32_bf16 v[56:59], a[100:103], a[92:95], v[56:59]\n" \
"ds_read_b64_tr_b16 a[218:219], v210 offset:5120\n" \
"v_mfma_f32_16x16x32_bf16 v[16:19], a[104:107], a[88:91], v[16:19]\n" \
"ds_read_b64_tr_b16 a[220:221], v210 offset:16384\n" \
"v_mfma_f32_16x16x32_bf16 v[16:19], a[108:111], a[92:95], v[16:19]\n" \
"v_mfma_f32_16x16x32_bf16 v[20:23], a[112:115], a[88:91], v[20:23]\n" \
"ds_read_b64_tr_b16 a[222:223], v210 offset:17408\n" \
"v_mfma_f32_16x16x32_bf16 v[20:23], a[116:119], a[92:95], v[20:23]\n" \
"s_waitcnt lgkmcnt(3)\n" \
"v_add_u32 v207, 0x1d000, v207\n" \
"v_add_u32 v208, 0x1d000, v208\n" \
"v_mfma_f32_16x16x32_bf16 v[64:67], a[96:99], a[192:195], v[64:67]\n" \
"v_add_u32 v205, 0x1d000, v205\n" \
"v_add_u32 v206, 0x1d000, v206\n" \
"v_mfma_f32_16x16x32_bf16 v[64:67], a[100:103], a[196:199], v[64:67]\n" \
"v_add_u32 v209, 0x1d000, v209\n" \
"v_add_u32 v210, 0x1d000, v210\n" \
"v_mfma_f32_16x16x32_bf16 v[24:27], a[104:107], a[192:195], v[24:27]\n" \
"v_mfma_f32_16x16x32_bf16 v[24:27], a[108:111], a[196:199], v[24:27]\n" \
"v_mfma_f32_16x16x32_bf16 v[28:31], a[112:115], a[192:195], v[28:31]\n" \
"s_waitcnt vmcnt(8) lgkmcnt(0)\n" \
"v_mfma_f32_16x16x32_bf16 v[28:31], a[116:119], a[196:199], v[28:31]\n" \
"s_barrier\n" \
"v_mfma_f32_16x16x32_bf16 v[72:75], a[64:67], a[208:211], v[72:75]\n" \
"ds_read_b64_tr_b16 a[96:97], v209 offset:2048\n" \
"v_mfma_f32_16x16x32_bf16 v[72:75], a[68:71], a[212:215], v[72:75]\n" \
"ds_read_b64_tr_b16 a[98:99], v209 offset:3072\n" \
"v_mfma_f32_16x16x32_bf16 v[44:47], a[200:203], a[72:75], v[44:47]\n" \
"s_add_u32 m0, s75, s95\n" \
"ds_read_b128 a[64:67], v207 offset:0\n" \
"ds_read_b64_tr_b16 a[100:101], v209 offset:14336\n" \
"v_mfma_f32_16x16x32_bf16 v[44:47], a[204:207], a[76:79], v[44:47]\n" \
"global_load_lds_dwordx4 v222, s[36:37] offset:0 sc1\n" \
"v_mfma_f32_16x16x32_bf16 v[80:83], a[72:75], a[208:211], v[80:83]\n" \
"ds_read_b128 a[68:71], v208 offset:0\n" \
"ds_read_b64_tr_b16 a[102:103], v209 offset:15360\n" \
"v_mfma_f32_16x16x32_bf16 v[80:83], a[76:79], a[212:215], v[80:83]\n" \
"ds_read_b64_tr_b16 a[104:105], v209 offset:0\n" \
"v_mfma_f32_16x16x32_bf16 v[84:87], a[72:75], a[216:219], v[84:87]\n" \
"ds_read_b64_tr_b16 a[106:107], v209 offset:1024\n" \
"v_mfma_f32_16x16x32_bf16 v[84:87], a[76:79], a[220:223], v[84:87]\n" \
"global_load_lds_dwordx4 v222, s[36:37] offset:1024 sc1\n" \
"v_mfma_f32_16x16x32_bf16 v[52:55], a[200:203], a[80:83], v[52:55]\n" \
"ds_read_b128 a[72:75], v205 offset:0\n" \
"v_mfma_f32_16x16x32_bf16 v[52:55], a[204:207], a[84:87], v[52:55]\n" \
"ds_read_b128 a[76:79], v206 offset:0\n" \
"v_mfma_f32_16x16x32_bf16 v[88:91], a[80:83], a[208:211], v[88:91]\n" \
"ds_read_b64_tr_b16 a[108:109], v209 offset:12288\n" \
"v_mfma_f32_16x16x32_bf16 v[88:91], a[84:87], a[212:215], v[88:91]\n" \
"global_load_lds_dwordx4 v222, s[36:37] offset:2048 sc1\n" \
"v_mfma_f32_16x16x32_bf16 v[92:95], a[80:83], a[216:219], v[92:95]\n" \
"ds_read_b64_tr_b16 a[110:111], v209 offset:13312\n" \
"v_mfma_f32_16x16x32_bf16 v[92:95], a[84:87], a[220:223], v[92:95]\n" \
"ds_read_b64_tr_b16 a[112:113], v210 offset:0\n" \
"v_mfma_f32_16x16x32_bf16 v[60:63], a[200:203], a[88:91], v[60:63]\n" \
"ds_read_b128 a[80:83], v205 offset:4096\n" \
"v_mfma_f32_16x16x32_bf16 v[60:63], a[204:207], a[92:95], v[60:63]\n" \
"global_load_lds_dwordx4 v222, s[36:37] offset:3072 sc1\n" \
"v_mfma_f32_16x16x32_bf16 v[96:99], a[88:91], a[208:211], v[96:99]\n" \
"ds_read_b128 a[84:87], v206 offset:4096\n" \
"s_add_u32 s36, s36, 0x1000\n" \
"v_mfma_f32_16x16x32_bf16 v[96:99], a[92:95], a[212:215], v[96:99]\n" \
"ds_read_b64_tr_b16 a[114:115], v210 offset:1024\n" \
"v_mfma_f32_16x16x32_bf16 v[100:103], a[88:91], a[216:219], v[100:103]\n" \
"s_add_u32 m0, m0, 0x5000\n" \
"ds_read_b64_tr_b16 a[116:117], v210 offset:12288\n" \
"v_mfma_f32_16x16x32_bf16 v[100:103], a[92:95], a[220:223], v[100:103]\n" \
"global_load_lds_dwordx4 v216, s[38:39] offset:-4096\n" \
"v_mfma_f32_16x16x32_bf16 v[68:71], a[200:203], a[192:195], v[68:71]\n" \
"ds_read_b128 a[88:91], v205 offset:8192\n" \
"v_mfma_f32_16x16x32_bf16 v[68:71], a[204:207], a[196:199], v[68:71]\n" \
"ds_read_b128 a[92:95], v206 offset:8192\n" \
"v_mfma_f32_16x16x32_bf16 v[104:107], a[192:195], a[208:211], v[104:107]\n" \
"ds_read_b64_tr_b16 a[118:119], v210 offset:13312\n" \
"v_mfma_f32_16x16x32_bf16 v[104:107], a[196:199], a[212:215], v[104:107]\n" \
"global_load_lds_dwordx4 v217, s[38:39] offset:-3072\n" \
"v_mfma_f32_16x16x32_bf16 v[108:111], a[192:195], a[216:219], v[108:111]\n" \
"v_mfma_f32_16x16x32_bf16 v[108:111], a[196:199], a[220:223], v[108:111]\n" \
"s_waitcnt lgkmcnt(2)\n" \
"v_mfma_f32_16x16x32_bf16 v[32:35], a[96:99], a[64:67], v[32:35]\n" \
"ds_read_b128 a[192:195], v205 offset:12288\n" \
"v_mfma_f32_16x16x32_bf16 v[32:35], a[100:103], a[68:71], v[32:35]\n" \
"global_load_lds_dwordx4 v218, s[38:39] offset:-2048\n" \
"v_mfma_f32_16x16x32_bf16 v[40:43], a[96:99], a[72:75], v[40:43]\n" \
"ds_read_b128 a[196:199], v206 offset:12288\n" \
"v_mfma_f32_16x16x32_bf16 v[40:43], a[100:103], a[76:79], v[40:43]\n" \
"ds_read_b64_tr_b16 a[200:201], v210 offset:2048\n" \
"v_mfma_f32_16x16x32_bf16 v[0:3], a[104:107], a[72:75], v[0:3]\n" \
"ds_read_b64_tr_b16 a[202:203], v210 offset:3072\n" \
"v_mfma_f32_16x16x32_bf16 v[0:3], a[108:111], a[76:79], v[0:3]\n" \
"s_waitcnt lgkmcnt(3)\n" \
"global_load_lds_dwordx4 v219, s[38:39] offset:-1024\n" \
"v_mfma_f32_16x16x32_bf16 v[4:7], a[112:115], a[72:75], v[4:7]\n" \
"s_add_u32 s38, s38, 0x20000\n" \
"s_addc_u32 s39, s39, 0\n" \
"ds_read_b64_tr_b16 a[204:205], v210 offset:14336\n" \
"v_mfma_f32_16x16x32_bf16 v[4:7], a[116:119], a[76:79], v[4:7]\n" \
"s_sub_u32 s75, s75, 32768\n" \
"s_cselect_b32 s75, 131072, s75\n" \
"ds_read_b64_tr_b16 a[206:207], v210 offset:15360\n" \
"v_mfma_f32_16x16x32_bf16 v[48:51], a[96:99], a[80:83], v[48:51]\n" \
"s_add_u32 m0, s75, s95\n" \
"ds_read_b64_tr_b16 a[208:209], v209 offset:4096\n" \
"v_mfma_f32_16x16x32_bf16 v[48:51], a[100:103], a[84:87], v[48:51]\n" \
"global_load_lds_dwordx4 v222, s[36:37] offset:0 sc1\n" \
"v_mfma_f32_16x16x32_bf16 v[8:11], a[104:107], a[80:83], v[8:11]\n" \
"ds_read_b64_tr_b16 a[210:211], v209 offset:5120\n" \
"v_cvt_pk_bf16_f32 v112, v0, v1\n" \
"v_mfma_f32_16x16x32_bf16 v[8:11], a[108:111], a[84:87], v[8:11]\n" \
"ds_read_b64_tr_b16 a[212:213], v209 offset:16384\n" \
"v_cvt_pk_bf16_f32 v113, v2, v3\n" \
"v_mfma_f32_16x16x32_bf16 v[12:15], a[112:115], a[80:83], v[12:15]\n" \
"ds_read_b64_tr_b16 a[214:215], v209 offset:17408\n" \
"v_cvt_pk_bf16_f32 v114, v4, v5\n" \
"v_mfma_f32_16x16x32_bf16 v[12:15], a[116:119], a[84:87], v[12:15]\n" \
"global_load_lds_dwordx4 v222, s[36:37] offset:1024 sc1\n" \
"v_cvt_pk_bf16_f32 v115, v6, v7\n" \
"v_mfma_f32_16x16x32_bf16 v[56:59], a[96:99], a[88:91], v[56:59]\n" \
"ds_read_b64_tr_b16 a[216:217], v210 offset:4096\n" \
"v_mfma_f32_16x16x32_bf16 v[56:59], a[100:103], a[92:95], v[56:59]\n" \
"ds_read_b64_tr_b16 a[218:219], v210 offset:5120\n" \
"v_mfma_f32_16x16x32_bf16 v[16:19], a[104:107], a[88:91], v[16:19]\n" \
"ds_read_b64_tr_b16 a[220:221], v210 offset:16384\n" \
"v_cvt_pk_bf16_f32 v116, v8, v9\n" \
"v_mfma_f32_16x16x32_bf16 v[16:19], a[108:111], a[92:95], v[16:19]\n" \
"global_load_lds_dwordx4 v222, s[36:37] offset:2048 sc1\n" \
"v_cvt_pk_bf16_f32 v117, v10, v11\n" \
"v_mfma_f32_16x16x32_bf16 v[20:23], a[112:115], a[88:91], v[20:23]\n" \
"ds_read_b64_tr_b16 a[222:223], v210 offset:17408\n" \
"v_cvt_pk_bf16_f32 v118, v12, v13\n" \
"v_mfma_f32_16x16x32_bf16 v[20:23], a[116:119], a[92:95], v[20:23]\n" \
"s_waitcnt lgkmcnt(3)\n" \
"v_cvt_pk_bf16_f32 v119, v14, v15\n" \
"v_mfma_f32_16x16x32_bf16 v[64:67], a[96:99], a[192:195], v[64:67]\n" \
"v_mfma_f32_16x16x32_bf16 v[64:67], a[100:103], a[196:199], v[64:67]\n" \
"global_load_lds_dwordx4 v222, s[36:37] offset:3072 sc1\n" \
"v_mfma_f32_16x16x32_bf16 v[24:27], a[104:107], a[192:195], v[24:27]\n" \
"v_cvt_pk_bf16_f32 v120, v16, v17\n" \
"v_cvt_pk_bf16_f32 v121, v18, v19\n" \
"v_mfma_f32_16x16x32_bf16 v[24:27], a[108:111], a[196:199], v[24:27]\n" \
"v_cvt_pk_bf16_f32 v122, v20, v21\n" \
"v_cvt_pk_bf16_f32 v123, v22, v23\n" \
"v_mfma_f32_16x16x32_bf16 v[28:31], a[112:115], a[192:195], v[28:31]\n" \
"s_waitcnt vmcnt(12) lgkmcnt(0)\n" \
"v_mfma_f32_16x16x32_bf16 v[28:31], a[116:119], a[196:199], v[28:31]\n" \
"s_barrier\n" \
"v_mfma_f32_16x16x32_bf16 v[72:75], a[64:67], a[208:211], v[72:75]\n" \
"ds_read_b64_tr_b16 a[32:33], v214 offset:16384\n" \
"v_mfma_f32_16x16x32_bf16 v[72:75], a[68:71], a[212:215], v[72:75]\n" \
"v_mfma_f32_16x16x32_bf16 v[44:47], a[200:203], a[72:75], v[44:47]\n" \
"s_add_u32 s36, s36, 0x1000\n" \
"s_add_u32 m0, m0, 0x5000\n" \
"ds_read_b64_tr_b16 a[34:35], v214 offset:18432\n" \
"v_mfma_f32_16x16x32_bf16 v[44:47], a[204:207], a[76:79], v[44:47]\n" \
"ds_read_b64_tr_b16 a[36:37], v214 offset:24576\n" \
"global_load_lds_dwordx4 v216, s[38:39] offset:-4096\n" \
"v_mfma_f32_16x16x32_bf16 v[80:83], a[72:75], a[208:211], v[80:83]\n" \
"v_cvt_pk_bf16_f32 v124, v24, v25\n" \
"v_cvt_pk_bf16_f32 v125, v26, v27\n" \
"v_mfma_f32_16x16x32_bf16 v[80:83], a[76:79], a[212:215], v[80:83]\n" \
"ds_read_b64_tr_b16 a[38:39], v214 offset:26624\n" \
"v_cvt_pk_bf16_f32 v126, v28, v29\n" \
"v_mfma_f32_16x16x32_bf16 v[84:87], a[72:75], a[216:219], v[84:87]\n" \
"ds_read_b128 a[0:3], v213 offset:0\n" \
"v_cvt_pk_bf16_f32 v127, v30, v31\n" \
"v_mfma_f32_16x16x32_bf16 v[84:87], a[76:79], a[220:223], v[84:87]\n" \
"global_load_lds_dwordx4 v217, s[38:39] offset:-3072\n" \
"v_cvt_pk_bf16_f32 v128, v32, v33\n" \
"v_mfma_f32_16x16x32_bf16 v[52:55], a[200:203], a[80:83], v[52:55]\n" \
"ds_read_b128 a[4:7], v213 offset:2048\n" \
"v_cvt_pk_bf16_f32 v129, v34, v35\n" \
"v_mfma_f32_16x16x32_bf16 v[52:55], a[204:207], a[84:87], v[52:55]\n" \
"ds_read_b64_tr_b16 a[40:41], v215 offset:16384\n" \
"v_cvt_pk_bf16_f32 v130, v72, v73\n" \
"v_mfma_f32_16x16x32_bf16 v[88:91], a[80:83], a[208:211], v[88:91]\n" \
"v_cvt_pk_bf16_f32 v131, v74, v75\n" \
"v_mfma_f32_16x16x32_bf16 v[88:91], a[84:87], a[212:215], v[88:91]\n" \
"ds_read_b64_tr_b16 a[42:43], v215 offset:18432\n" \
"global_load_lds_dwordx4 v218, s[38:39] offset:-2048\n" \
"v_mfma_f32_16x16x32_bf16 v[92:95], a[80:83], a[216:219], v[92:95]\n" \
"ds_read_b64_tr_b16 a[44:45], v215 offset:24576\n" \
"v_mfma_f32_16x16x32_bf16 v[92:95], a[84:87], a[220:223], v[92:95]\n" \
"ds_write_b128 v145, v[128:131]\n" \
"v_mfma_f32_16x16x32_bf16 v[60:63], a[200:203], a[88:91], v[60:63]\n" \
"ds_read_b64_tr_b16 a[46:47], v215 offset:26624\n" \
"v_cvt_pk_bf16_f32 v132, v40, v41\n" \
"v_mfma_f32_16x16x32_bf16 v[60:63], a[204:207], a[92:95], v[60:63]\n" \
"ds_read_b128 a[8:11], v213 offset:256\n" \
"global_load_lds_dwordx4 v219, s[38:39] offset:-1024\n" \
"v_mfma_f32_16x16x32_bf16 v[96:99], a[88:91], a[208:211], v[96:99]\n" \
"s_add_u32 s38, s38, 0x20000\n" \
"s_addc_u32 s39, s39, 0\n" \
"v_cvt_pk_bf16_f32 v133, v42, v43\n" \
"v_mfma_f32_16x16x32_bf16 v[96:99], a[92:95], a[212:215], v[96:99]\n" \
"s_sub_u32 s75, s75, 32768\n" \
"s_cselect_b32 s75, 131072, s75\n" \
"ds_read_b128 a[12:15], v213 offset:2304\n" \
"v_mfma_f32_16x16x32_bf16 v[100:103], a[88:91], a[216:219], v[100:103]\n" \
"s_add_u32 m0, s75, s95\n" \
"ds_read_b128 a[16:19], v213 offset:4096\n" \
"v_cvt_pk_bf16_f32 v134, v44, v45\n" \
"v_mfma_f32_16x16x32_bf16 v[100:103], a[92:95], a[220:223], v[100:103]\n" \
"global_load_lds_dwordx4 v222, s[36:37] offset:0 sc1\n" \
"v_cvt_pk_bf16_f32 v135, v46, v47\n" \
"v_mfma_f32_16x16x32_bf16 v[68:71], a[200:203], a[192:195], v[68:71]\n" \
"ds_read_b128 a[20:23], v213 offset:6144\n" \
"v_cvt_pk_bf16_f32 v136, v48, v49\n" \
"v_mfma_f32_16x16x32_bf16 v[68:71], a[204:207], a[196:199], v[68:71]\n" \
"v_cvt_pk_bf16_f32 v137, v50, v51\n" \
"v_cvt_pk_bf16_f32 v138, v52, v53\n" \
"v_mfma_f32_16x16x32_bf16 v[104:107], a[192:195], a[208:211], v[104:107]\n" \
"v_cvt_pk_bf16_f32 v139, v54, v55\n" \
"v_cvt_pk_bf16_f32 v140, v56, v57\n" \
"v_mfma_f32_16x16x32_bf16 v[104:107], a[196:199], a[212:215], v[104:107]\n" \
"global_load_lds_dwordx4 v222, s[36:37] offset:1024 sc1\n" \
"v_cvt_pk_bf16_f32 v141, v58, v59\n" \
"v_mfma_f32_16x16x32_bf16 v[108:111], a[192:195], a[216:219], v[108:111]\n" \
"v_cvt_pk_bf16_f32 v142, v60, v61\n" \
"v_cvt_pk_bf16_f32 v143, v62, v63\n" \
"v_mfma_f32_16x16x32_bf16 v[108:111], a[196:199], a[220:223], v[108:111]\n" \
"s_waitcnt lgkmcnt(0)\n" \
"v_mfma_f32_16x16x32_bf16 v[0:3], a[32:35], a[0:3], 0\n" \
"ds_read_b128 a[24:27], v213 offset:4352\n" \
"v_mfma_f32_16x16x32_bf16 v[0:3], a[36:39], a[4:7], v[0:3]\n" \
"ds_read_b128 a[28:31], v213 offset:6400\n" \
"global_load_lds_dwordx4 v222, s[36:37] offset:2048 sc1\n" \
"v_mfma_f32_16x16x32_bf16 v[4:7], a[40:43], a[0:3], 0\n" \
"s_sub_u32 s74, s74, 32768\n" \
"s_cselect_b32 s74, 131072, s74\n" \
"ds_read_b64_tr_b16 a[48:49], v214 offset:16896\n" \
"v_mfma_f32_16x16x32_bf16 v[4:7], a[44:47], a[4:7], v[4:7]\n" \
"ds_read_b64_tr_b16 a[50:51], v214 offset:18944\n" \
"v_cvt_pk_bf16_f32 v64, v64, v65\n" \
"v_mfma_f32_16x16x32_bf16 v[16:19], a[32:35], a[8:11], 0\n" \
"ds_read_b64_tr_b16 a[52:53], v214 offset:25088\n" \
"global_load_lds_dwordx4 v222, s[36:37] offset:3072 sc1\n" \
"v_mfma_f32_16x16x32_bf16 v[16:19], a[36:39], a[12:15], v[16:19]\n" \
"s_add_u32 s36, s36, 0x1000\n" \
"ds_read_b64_tr_b16 a[54:55], v214 offset:27136\n" \
"v_cvt_pk_bf16_f32 v65, v66, v67\n" \
"v_mfma_f32_16x16x32_bf16 v[20:23], a[40:43], a[8:11], 0\n" \
"s_add_u32 m0, m0, 0x5000\n" \
"ds_read_b64_tr_b16 a[56:57], v215 offset:16896\n" \
"v_cvt_pk_bf16_f32 v66, v68, v69\n" \
"v_mfma_f32_16x16x32_bf16 v[20:23], a[44:47], a[12:15], v[20:23]\n" \
"ds_read_b64_tr_b16 a[58:59], v215 offset:18944\n" \
"global_load_lds_dwordx4 v216, s[38:39] offset:-4096\n" \
"v_mfma_f32_16x16x32_bf16 v[32:35], a[32:35], a[16:19], 0\n" \
"ds_read_b64_tr_b16 a[60:61], v215 offset:25088\n" \
"v_cvt_pk_bf16_f32 v67, v70, v71\n" \
"v_mfma_f32_16x16x32_bf16 v[32:35], a[36:39], a[20:23], v[32:35]\n" \
"ds_read_b64_tr_b16 a[62:63], v215 offset:27136\n" \
"v_cvt_pk_bf16_f32 v68, v72, v73\n" \
"v_mfma_f32_16x16x32_bf16 v[36:39], a[40:43], a[16:19], 0\n" \
"global_load_lds_dwordx4 v217, s[38:39] offset:-3072\n" \
"v_add_u32 v213, s74, v211\n" \
"v_mfma_f32_16x16x32_bf16 v[36:39], a[44:47], a[20:23], v[36:39]\n" \
"s_waitcnt lgkmcnt(3)\n" \
"v_add_u32 v214, s74, v212\n" \
"v_xor_b32 v215, 32, v214\n" \
"v_mfma_f32_16x16x32_bf16 v[48:51], a[32:35], a[24:27], 0\n" \
"v_cvt_pk_bf16_f32 v69, v74, v75\n" \
"v_cvt_pk_bf16_f32 v70, v80, v81\n" \
"v_mfma_f32_16x16x32_bf16 v[48:51], a[36:39], a[28:31], v[48:51]\n" \
"global_load_lds_dwordx4 v218, s[38:39] offset:-2048\n" \
"v_cvt_pk_bf16_f32 v71, v82, v83\n" \
"v_mfma_f32_16x16x32_bf16 v[52:55], a[40:43], a[24:27], 0\n" \
"s_waitcnt vmcnt(15) lgkmcnt(0)\n" \
"global_load_lds_dwordx4 v219, s[38:39] offset:-1024\n" \
"v_cvt_pk_bf16_f32 v194, v84, v85\n" \
"v_mfma_f32_16x16x32_bf16 v[52:55], a[44:47], a[28:31], v[52:55]\n" \
"s_barrier\n" \
"v_cvt_pk_bf16_f32 v195, v86, v87\n" \
"v_mfma_f32_16x16x32_bf16 v[8:11], a[48:51], a[0:3], 0\n" \
"ds_read_b64 v[130:131], v223\n" \
"ds_read_b64_tr_b16 a[32:33], v214 offset:16384\n" \
"v_mfma_f32_16x16x32_bf16 v[8:11], a[52:55], a[4:7], v[8:11]\n" \
"ds_read_b64 v[192:193], v223 offset:8\n" \
"ds_read_b64_tr_b16 a[34:35], v214 offset:18432\n" \
"v_mfma_f32_16x16x32_bf16 v[12:15], a[56:59], a[0:3], 0\n" \
"s_add_u32 s38, s38, 0x20000\n" \
"s_addc_u32 s39, s39, 0\n" \
"ds_read_b64_tr_b16 a[36:37], v214 offset:24576\n" \
"v_mfma_f32_16x16x32_bf16 v[12:15], a[60:63], a[4:7], v[12:15]\n" \
"s_sub_u32 s75, s75, 32768\n" \
"s_cselect_b32 s75, 131072, s75\n" \
"ds_read_b128 a[0:3], v213 offset:0\n" \
"v_mfma_f32_16x16x32_bf16 v[24:27], a[48:51], a[8:11], 0\n" \
"s_add_u32 m0, s75, s95\n" \
"ds_read_b128 a[4:7], v213 offset:2048\n" \
"v_cvt_pk_bf16_f32 v72, v88, v89\n" \
"v_mfma_f32_16x16x32_bf16 v[24:27], a[52:55], a[12:15], v[24:27]\n" \
"ds_read_b64_tr_b16 a[38:39], v214 offset:26624\n" \
"global_load_lds_dwordx4 v222, s[36:37] offset:0 sc1\n" \
"v_mfma_f32_16x16x32_bf16 v[28:31], a[56:59], a[8:11], 0\n" \
"ds_read_b64_tr_b16 a[40:41], v215 offset:16384\n" \
"v_cvt_pk_bf16_f32 v73, v90, v91\n" \
"v_mfma_f32_16x16x32_bf16 v[28:31], a[60:63], a[12:15], v[28:31]\n" \
"s_waitcnt lgkmcnt(4)\n" \
"ds_read_b128 a[8:11], v213 offset:256\n" \
"global_load_lds_dwordx4 v222, s[36:37] offset:1024 sc1\n" \
"v_mfma_f32_16x16x32_bf16 v[40:43], a[48:51], a[16:19], 0\n" \
"ds_read_b128 a[12:15], v213 offset:2304\n" \
"v_cvt_pk_bf16_f32 v196, v92, v93\n" \
"v_mfma_f32_16x16x32_bf16 v[40:43], a[52:55], a[20:23], v[40:43]\n" \
"ds_read_b64_tr_b16 a[42:43], v215 offset:18432\n" \
"v_cvt_pk_bf16_f32 v197, v94, v95\n" \
"v_mfma_f32_16x16x32_bf16 v[44:47], a[56:59], a[16:19], 0\n" \
"ds_read_b64_tr_b16 a[44:45], v215 offset:24576\n" \
"global_load_lds_dwordx4 v222, s[36:37] offset:2048 sc1\n" \
"v_mfma_f32_16x16x32_bf16 v[44:47], a[60:63], a[20:23], v[44:47]\n" \
"ds_read_b128 a[16:19], v213 offset:4096\n" \
"v_cvt_pk_bf16_f32 v74, v96, v97\n" \
"v_mfma_f32_16x16x32_bf16 v[56:59], a[48:51], a[24:27], 0\n" \
"ds_read_b128 a[20:23], v213 offset:6144\n" \
"global_load_lds_dwordx4 v222, s[36:37] offset:3072 sc1\n" \
"v_mfma_f32_16x16x32_bf16 v[56:59], a[52:55], a[28:31], v[56:59]\n" \
"ds_read_b64_tr_b16 a[46:47], v215 offset:26624\n" \
"v_cvt_pk_bf16_f32 v75, v98, v99\n" \
"v_mfma_f32_16x16x32_bf16 v[60:63], a[56:59], a[24:27], 0\n" \
"v_cvt_pk_bf16_f32 v198, v100, v101\n" \
"v_cvt_pk_bf16_f32 v199, v102, v103\n" \
"v_mfma_f32_16x16x32_bf16 v[60:63], a[60:63], a[28:31], v[60:63]\n" \
"s_waitcnt lgkmcnt(3)\n" \
"v_cvt_pk_bf16_f32 v76, v104, v105\n" \
"v_cvt_pk_bf16_f32 v77, v106, v107\n" \
"v_mfma_f32_16x16x32_bf16 v[0:3], a[32:35], a[0:3], v[0:3]\n" \
"v_cvt_pk_bf16_f32 v200, v108, v109\n" \
"v_cvt_pk_bf16_f32 v201, v110, v111\n" \
"v_mfma_f32_16x16x32_bf16 v[176:179], v[64:67], v[124:127], v[176:179]\n" \
"v_mfma_f32_16x16x32_bf16 v[172:175], v[140:143], v[124:127], v[172:175]\n" \
"s_add_u32 s36, s36, 0x1000\n" \
"ds_read_b128 a[24:27], v213 offset:4352\n" \
"s_add_u32 m0, m0, 0x5000\n" \
"v_mfma_f32_16x16x32_bf16 v[0:3], a[36:39], a[4:7], v[0:3]\n" \
"s_waitcnt lgkmcnt(1)\n" \
"ds_read_b128 a[28:31], v213 offset:6400\n" \
"global_load_lds_dwordx4 v216, s[38:39] offset:-4096\n" \
"v_mfma_f32_16x16x32_bf16 v[168:171], v[140:143], v[120:123], v[168:171]\n" \
"v_mfma_f32_16x16x32_bf16 v[164:167], v[136:139], v[120:123], v[164:167]\n" \
"v_mfma_f32_16x16x32_bf16 v[160:163], v[136:139], v[116:119], v[160:163]\n" \
"v_mfma_f32_16x16x32_bf16 v[156:159], v[132:135], v[116:119], v[156:159]\n" \
"v_mfma_f32_16x16x32_bf16 v[152:155], v[132:135], v[112:115], v[152:155]\n" \
"v_mfma_f32_16x16x32_bf16 v[148:151], v[128:131], v[112:115], v[148:151]\n" \
"v_mfma_f32_16x16x32_bf16 v[4:7], a[40:43], a[0:3], v[4:7]\n" \
"s_sub_u32 s74, s74, 32768\n" \
"s_cselect_b32 s74, 131072, s74\n" \
"ds_read_b64_tr_b16 a[48:49], v214 offset:16896\n" \
"v_mfma_f32_16x16x32_bf16 v[4:7], a[44:47], a[4:7], v[4:7]\n" \
"ds_read_b64_tr_b16 a[50:51], v214 offset:18944\n" \
"v_max3_f32 v183, v172, v173, v174\n" \
"v_max3_f32 v182, v164, v165, v166\n" \
"v_max3_f32 v181, v156, v157, v158\n" \
"v_max3_f32 v180, v148, v149, v150\n" \
"v_max3_f32 v183, v183, v175, v176\n" \
"v_max3_f32 v182, v182, v167, v168\n" \
"v_mfma_f32_16x16x32_bf16 v[16:19], a[32:35], a[8:11], v[16:19]\n" \
"ds_read_b64_tr_b16 a[52:53], v214 offset:25088\n" \
"global_load_lds_dwordx4 v217, s[38:39] offset:-3072\n" \
"v_max3_f32 v181, v181, v159, v160\n" \
"v_max3_f32 v180, v180, v151, v152\n" \
"v_max3_f32 v183, v183, v177, v178\n" \
"v_max3_f32 v182, v182, v169, v170\n" \
"v_max3_f32 v181, v181, v161, v162\n" \
"v_max3_f32 v180, v180, v153, v154\n" \
"v_mfma_f32_16x16x32_bf16 v[16:19], a[36:39], a[12:15], v[16:19]\n" \
"ds_read_b64_tr_b16 a[54:55], v214 offset:27136\n" \
"v_max_f32 v183, v183, v179\n" \
"v_max_f32 v182, v182, v171\n" \
"v_max_f32 v181, v181, v163\n" \
"v_max_f32 v180, v180, v155\n" \
"v_mov_b32 v187, v183\n" \
"v_mov_b32 v186, v182\n" \
"v_mfma_f32_16x16x32_bf16 v[20:23], a[40:43], a[8:11], v[20:23]\n" \
"ds_read_b64_tr_b16 a[56:57], v215 offset:16896\n" \
"v_mov_b32 v185, v181\n" \
"v_mov_b32 v184, v180\n" \
"v_permlane16_swap_b32 v187, v183\n" \
"v_permlane16_swap_b32 v186, v182\n" \
"v_permlane16_swap_b32 v185, v181\n" \
"v_permlane16_swap_b32 v184, v180\n" \
"v_mfma_f32_16x16x32_bf16 v[20:23], a[44:47], a[12:15], v[20:23]\n" \
"ds_read_b64_tr_b16 a[58:59], v215 offset:18944\n" \
"global_load_lds_dwordx4 v218, s[38:39] offset:-2048\n" \
"v_max_f32 v183, v183, v187\n" \
"v_max_f32 v182, v182, v186\n" \
"v_max_f32 v181, v181, v185\n" \
"v_max_f32 v180, v180, v184\n" \
"v_mov_b32 v187, v183\n" \
"v_mov_b32 v186, v182\n" \
"v_mfma_f32_16x16x32_bf16 v[32:35], a[32:35], a[16:19], v[32:35]\n" \
"ds_read_b64_tr_b16 a[60:61], v215 offset:25088\n" \
"v_mov_b32 v185, v181\n" \
"v_mov_b32 v184, v180\n" \
"v_permlane32_swap_b32 v187, v183\n" \
"v_permlane32_swap_b32 v186, v182\n" \
"v_permlane32_swap_b32 v185, v181\n" \
"v_permlane32_swap_b32 v184, v180\n" \
"v_mfma_f32_16x16x32_bf16 v[32:35], a[36:39], a[20:23], v[32:35]\n" \
"ds_read_b64_tr_b16 a[62:63], v215 offset:27136\n" \
"v_max_f32 v183, v183, v187\n" \
"v_max_f32 v182, v182, v186\n" \
"v_max_f32 v181, v181, v185\n" \
"v_max_f32 v180, v180, v184\n" \
"v_mul_f32 v183, v183, v144\n" \
"v_mul_f32 v182, v182, v144\n" \
"v_mfma_f32_16x16x32_bf16 v[36:39], a[40:43], a[16:19], v[36:39]\n" \
"global_load_lds_dwordx4 v219, s[38:39] offset:-1024\n" \
"v_add_u32 v213, s74, v211\n" \
"v_mul_f32 v181, v181, v144\n" \
"v_mul_f32 v180, v180, v144\n" \
"v_fma_f32 v172, v172, v144, -v183\n" \
"v_fma_f32 v164, v164, v144, -v182\n" \
"v_fma_f32 v156, v156, v144, -v181\n" \
"v_mfma_f32_16x16x32_bf16 v[36:39], a[44:47], a[20:23], v[36:39]\n" \
"s_waitcnt lgkmcnt(3)\n" \
"v_exp_f32 v172, v172\n" \
"v_fma_f32 v148, v148, v144, -v180\n" \
"s_add_u32 s38, s38, 0x20000\n" \
"s_addc_u32 s39, s39, 0\n" \
"v_mfma_f32_16x16x32_bf16 v[48:51], a[32:35], a[24:27], v[48:51]\n" \
"s_sub_u32 s75, s75, 32768\n" \
"s_cselect_b32 s75, 131072, s75\n" \
"v_exp_f32 v164, v164\n" \
"s_add_u32 m0, s75, s95\n" \
"v_mfma_f32_16x16x32_bf16 v[48:51], a[36:39], a[28:31], v[48:51]\n" \
"global_load_lds_dwordx4 v222, s[36:37] offset:0 sc1\n" \
"v_add_u32 v214, s74, v212\n" \
"v_fma_f32 v173, v173, v144, -v183\n" \
"v_mfma_f32_16x16x32_bf16 v[52:55], a[40:43], a[24:27], v[52:55]\n" \
"s_waitcnt vmcnt(17) lgkmcnt(0)\n" \
"v_xor_b32 v215, 32, v214\n" \
"v_exp_f32 v156, v156\n" \
"v_mfma_f32_16x16x32_bf16 v[52:55], a[44:47], a[28:31], v[52:55]\n" \
"s_barrier\n" \
"v_fma_f32 v165, v165, v144, -v182\n" \
"v_mfma_f32_16x16x32_bf16 v[8:11], a[48:51], a[0:3], v[8:11]\n" \
"ds_read_b64_tr_b16 a[32:33], v214 offset:16384\n" \
"v_exp_f32 v148, v148\n" \
"v_fma_f32 v157, v157, v144, -v181\n" \
"v_mfma_f32_16x16x32_bf16 v[8:11], a[52:55], a[4:7], v[8:11]\n" \
"ds_read_b64_tr_b16 a[34:35], v214 offset:18432\n" \
"global_load_lds_dwordx4 v222, s[36:37] offset:1024 sc1\n" \
"v_mfma_f32_16x16x32_bf16 v[12:15], a[56:59], a[0:3], v[12:15]\n" \
"ds_read_b64_tr_b16 a[36:37], v214 offset:24576\n" \
"v_exp_f32 v173, v173\n" \
"v_fma_f32 v149, v149, v144, -v180\n" \
"v_mfma_f32_16x16x32_bf16 v[12:15], a[60:63], a[4:7], v[12:15]\n" \
"ds_read_b128 a[0:3], v213 offset:0\n" \
"v_exp_f32 v165, v165\n" \
"v_mfma_f32_16x16x32_bf16 v[24:27], a[48:51], a[8:11], v[24:27]\n" \
"ds_read_b128 a[4:7], v213 offset:2048\n" \
"global_load_lds_dwordx4 v222, s[36:37] offset:2048 sc1\n" \
"v_fma_f32 v174, v174, v144, -v183\n" \
"v_mfma_f32_16x16x32_bf16 v[24:27], a[52:55], a[12:15], v[24:27]\n" \
"ds_read_b64_tr_b16 a[38:39], v214 offset:26624\n" \
"v_exp_f32 v157, v157\n" \
"v_mfma_f32_16x16x32_bf16 v[28:31], a[56:59], a[8:11], v[28:31]\n" \
"ds_read_b64_tr_b16 a[40:41], v215 offset:16384\n" \
"v_fma_f32 v166, v166, v144, -v182\n" \
"v_mfma_f32_16x16x32_bf16 v[28:31], a[60:63], a[12:15], v[28:31]\n" \
"ds_read_b128 a[8:11], v213 offset:256\n" \
"global_load_lds_dwordx4 v222, s[36:37] offset:3072 sc1\n" \
"v_exp_f32 v149, v149\n" \
"v_fma_f32 v158, v158, v144, -v181\n" \
"v_mfma_f32_16x16x32_bf16 v[40:43], a[48:51], a[16:19], v[40:43]\n" \
"s_add_u32 s36, s36, 0x1000\n" \
"ds_read_b128 a[12:15], v213 offset:2304\n" \
"v_exp_f32 v174, v174\n" \
"v_mfma_f32_16x16x32_bf16 v[40:43], a[52:55], a[20:23], v[40:43]\n" \
"s_add_u32 m0, m0, 0x5000\n" \
"ds_read_b64_tr_b16 a[42:43], v215 offset:18432\n" \
"v_add_f32 v191, v172, v173\n" \
"v_fma_f32 v150, v150, v144, -v180\n" \
"v_mfma_f32_16x16x32_bf16 v[44:47], a[56:59], a[16:19], v[44:47]\n" \
"ds_read_b64_tr_b16 a[44:45], v215 offset:24576\n" \
"global_load_lds_dwordx4 v216, s[38:39] offset:-4096\n" \
"v_mfma_f32_16x16x32_bf16 v[44:47], a[60:63], a[20:23], v[44:47]\n" \
"ds_read_b128 a[16:19], v213 offset:4096\n" \
"v_exp_f32 v166, v166\n" \
"v_add_f32 v190, v164, v165\n" \
"v_mfma_f32_16x16x32_bf16 v[56:59], a[48:51], a[24:27], v[56:59]\n" \
"ds_read_b128 a[20:23], v213 offset:6144\n" \
"global_load_lds_dwordx4 v217, s[38:39] offset:-3072\n" \
"v_fma_f32 v175, v175, v144, -v183\n" \
"v_mfma_f32_16x16x32_bf16 v[56:59], a[52:55], a[28:31], v[56:59]\n" \
"ds_read_b64_tr_b16 a[46:47], v215 offset:26624\n" \
"v_exp_f32 v158, v158\n" \
"v_mfma_f32_16x16x32_bf16 v[60:63], a[56:59], a[24:27], v[60:63]\n" \
"v_add_f32 v189, v156, v157\n" \
"v_fma_f32 v167, v167, v144, -v182\n" \
"v_mfma_f32_16x16x32_bf16 v[60:63], a[60:63], a[28:31], v[60:63]\n" \
"s_waitcnt lgkmcnt(3)\n" \
"v_exp_f32 v150, v150\n" \
"v_mfma_f32_16x16x32_bf16 v[0:3], a[32:35], a[0:3], v[0:3]\n" \
"ds_read_b128 a[24:27], v213 offset:4352\n" \
"v_add_f32 v188, v148, v149\n" \
"v_fma_f32 v159, v159, v144, -v181\n" \
"v_mfma_f32_16x16x32_bf16 v[0:3], a[36:39], a[4:7], v[0:3]\n" \
"s_waitcnt lgkmcnt(1)\n" \
"ds_read_b128 a[28:31], v213 offset:6400\n" \
"global_load_lds_dwordx4 v218, s[38:39] offset:-2048\n" \
"v_exp_f32 v175, v175\n" \
"v_mfma_f32_16x16x32_bf16 v[4:7], a[40:43], a[0:3], v[4:7]\n" \
"s_sub_u32 s74, s74, 32768\n" \
"s_cselect_b32 s74, 131072, s74\n" \
"ds_read_b64_tr_b16 a[48:49], v214 offset:16896\n" \
"v_add_f32 v191, v191, v174\n" \
"v_fma_f32 v151, v151, v144, -v180\n" \
"v_mfma_f32_16x16x32_bf16 v[4:7], a[44:47], a[4:7], v[4:7]\n" \
"ds_read_b64_tr_b16 a[50:51], v214 offset:18944\n" \
"global_load_lds_dwordx4 v219, s[38:39] offset:-1024\n" \
"v_mfma_f32_16x16x32_bf16 v[16:19], a[32:35], a[8:11], v[16:19]\n" \
"s_add_u32 s38, s38, 0x20000\n" \
"s_addc_u32 s39, s39, 0\n" \
"ds_read_b64_tr_b16 a[52:53], v214 offset:25088\n" \
"v_exp_f32 v167, v167\n" \
"v_add_f32 v190, v190, v166\n" \
"v_mfma_f32_16x16x32_bf16 v[16:19], a[36:39], a[12:15], v[16:19]\n" \
"s_sub_u32 s75, s75, 32768\n" \
"s_cselect_b32 s75, 131072, s75\n" \
"ds_read_b64_tr_b16 a[54:55], v214 offset:27136\n" \
"v_fma_f32 v176, v176, v144, -v183\n" \
"v_mfma_f32_16x16x32_bf16 v[20:23], a[40:43], a[8:11], v[20:23]\n" \
"s_add_u32 m0, s75, s95\n" \
"ds_read_b64_tr_b16 a[56:57], v215 offset:16896\n" \
"v_exp_f32 v159, v159\n" \
"v_add_f32 v189, v189, v158\n" \
"v_mfma_f32_16x16x32_bf16 v[20:23], a[44:47], a[12:15], v[20:23]\n" \
"ds_read_b64_tr_b16 a[58:59], v215 offset:18944\n" \
"global_load_lds_dwordx4 v222, s[36:37] offset:0 sc1\n" \
"v_fma_f32 v168, v168, v144, -v182\n" \
"v_mfma_f32_16x16x32_bf16 v[32:35], a[32:35], a[16:19], v[32:35]\n" \
"ds_read_b64_tr_b16 a[60:61], v215 offset:25088\n" \
"v_exp_f32 v151, v151\n" \
"v_mfma_f32_16x16x32_bf16 v[32:35], a[36:39], a[20:23], v[32:35]\n" \
"ds_read_b64_tr_b16 a[62:63], v215 offset:27136\n" \
"v_add_f32 v188, v188, v150\n" \
"v_fma_f32 v160, v160, v144, -v181\n" \
"v_mfma_f32_16x16x32_bf16 v[36:39], a[40:43], a[16:19], v[36:39]\n" \
"global_load_lds_dwordx4 v222, s[36:37] offset:1024 sc1\n" \
"v_add_u32 v213, s74, v211\n" \
"v_exp_f32 v176, v176\n" \
"v_mfma_f32_16x16x32_bf16 v[36:39], a[44:47], a[20:23], v[36:39]\n" \
"Lpg11613_%=:\n" \
"s_cbranch_execz Lpg12768_%=\n" \
"s_waitcnt lgkmcnt(3)\n" \
"v_add_u32 v214, s74, v212\n" \
"v_xor_b32 v215, 32, v214\n" \
"v_mfma_f32_16x16x32_bf16 v[48:51], a[32:35], a[24:27], v[48:51]\n" \
"v_add_f32 v191, v191, v175\n" \
"v_fma_f32 v152, v152, v144, -v180\n" \
"v_mfma_f32_16x16x32_bf16 v[48:51], a[36:39], a[28:31], v[48:51]\n" \
"global_load_lds_dwordx4 v222, s[36:37] offset:2048 sc1\n" \
"v_exp_f32 v168, v168\n" \
"v_mfma_f32_16x16x32_bf16 v[52:55], a[40:43], a[24:27], v[52:55]\n" \
"s_waitcnt vmcnt(19) lgkmcnt(0)\n" \
"v_add_f32 v190, v190, v167\n" \
"v_fma_f32 v177, v177, v144, -v183\n" \
"v_mfma_f32_16x16x32_bf16 v[52:55], a[44:47], a[28:31], v[52:55]\n" \
"s_barrier\n" \
"v_exp_f32 v160, v160\n" \
"v_mfma_f32_16x16x32_bf16 v[8:11], a[48:51], a[0:3], v[8:11]\n" \
"ds_read_b64_tr_b16 a[32:33], v214 offset:16384\n" \
"v_add_f32 v189, v189, v159\n" \
"v_fma_f32 v169, v169, v144, -v182\n" \
"v_mfma_f32_16x16x32_bf16 v[8:11], a[52:55], a[4:7], v[8:11]\n" \
"ds_read_b64_tr_b16 a[34:35], v214 offset:18432\n" \
"global_load_lds_dwordx4 v222, s[36:37] offset:3072 sc1\n" \
"v_exp_f32 v152, v152\n" \
"v_mfma_f32_16x16x32_bf16 v[12:15], a[56:59], a[0:3], v[12:15]\n" \
"s_add_u32 s36, s36, 0x1000\n" \
"ds_read_b64_tr_b16 a[36:37], v214 offset:24576\n" \
"v_add_f32 v188, v188, v151\n" \
"v_fma_f32 v161, v161, v144, -v181\n" \
"v_mfma_f32_16x16x32_bf16 v[12:15], a[60:63], a[4:7], v[12:15]\n" \
"s_add_u32 m0, m0, 0x5000\n" \
"ds_read_b128 a[0:3], v213 offset:0\n" \
"v_exp_f32 v177, v177\n" \
"v_mfma_f32_16x16x32_bf16 v[24:27], a[48:51], a[8:11], v[24:27]\n" \
"ds_read_b128 a[4:7], v213 offset:2048\n" \
"global_load_lds_dwordx4 v216, s[38:39] offset:-4096\n" \
"v_add_f32 v191, v191, v176\n" \
"v_mfma_f32_16x16x32_bf16 v[24:27], a[52:55], a[12:15], v[24:27]\n" \
"ds_read_b64_tr_b16 a[38:39], v214 offset:26624\n" \
"v_fma_f32 v153, v153, v144, -v180\n" \
"v_mfma_f32_16x16x32_bf16 v[28:31], a[56:59], a[8:11], v[28:31]\n" \
"ds_read_b64_tr_b16 a[40:41], v215 offset:16384\n" \
"v_exp_f32 v169, v169\n" \
"v_add_f32 v190, v190, v168\n" \
"v_mfma_f32_16x16x32_bf16 v[28:31], a[60:63], a[12:15], v[28:31]\n" \
"ds_read_b128 a[8:11], v213 offset:256\n" \
"global_load_lds_dwordx4 v217, s[38:39] offset:-3072\n" \
"v_fma_f32 v178, v178, v144, -v183\n" \
"v_mfma_f32_16x16x32_bf16 v[40:43], a[48:51], a[16:19], v[40:43]\n" \
"ds_read_b128 a[12:15], v213 offset:2304\n" \
"v_exp_f32 v161, v161\n" \
"v_mfma_f32_16x16x32_bf16 v[40:43], a[52:55], a[20:23], v[40:43]\n" \
"ds_read_b64_tr_b16 a[42:43], v215 offset:18432\n" \
"v_add_f32 v189, v189, v160\n" \
"v_fma_f32 v170, v170, v144, -v182\n" \
"v_mfma_f32_16x16x32_bf16 v[44:47], a[56:59], a[16:19], v[44:47]\n" \
"ds_read_b64_tr_b16 a[44:45], v215 offset:24576\n" \
"global_load_lds_dwordx4 v218, s[38:39] offset:-2048\n" \
"v_exp_f32 v153, v153\n" \
"v_mfma_f32_16x16x32_bf16 v[44:47], a[60:63], a[20:23], v[44:47]\n" \
"ds_read_b128 a[16:19], v213 offset:4096\n" \
"v_add_f32 v188, v188, v152\n" \
"v_fma_f32 v162, v162, v144, -v181\n" \
"v_mfma_f32_16x16x32_bf16 v[56:59], a[48:51], a[24:27], v[56:59]\n" \
"ds_read_b128 a[20:23], v213 offset:6144\n" \
"global_load_lds_dwordx4 v219, s[38:39] offset:-1024\n" \
"v_mfma_f32_16x16x32_bf16 v[56:59], a[52:55], a[28:31], v[56:59]\n" \
"s_add_u32 s38, s38, 0x20000\n" \
"s_addc_u32 s39, s39, 0\n" \
"ds_read_b64_tr_b16 a[46:47], v215 offset:26624\n" \
"v_exp_f32 v178, v178\n" \
"v_add_f32 v191, v191, v177\n" \
"v_mfma_f32_16x16x32_bf16 v[60:63], a[56:59], a[24:27], v[60:63]\n" \
"s_sub_u32 s75, s75, 32768\n" \
"s_cselect_b32 s75, 131072, s75\n" \
"v_fma_f32 v154, v154, v144, -v180\n" \
"v_mfma_f32_16x16x32_bf16 v[60:63], a[60:63], a[28:31], v[60:63]\n" \
"s_waitcnt lgkmcnt(3)\n" \
"v_exp_f32 v170, v170\n" \
"v_add_f32 v190, v190, v169\n" \
"v_mfma_f32_16x16x32_bf16 v[0:3], a[32:35], a[0:3], v[0:3]\n" \
"s_add_u32 m0, s75, s95\n" \
"ds_read_b128 a[24:27], v213 offset:4352\n" \
"v_fma_f32 v179, v179, v144, -v183\n" \
"v_mfma_f32_16x16x32_bf16 v[0:3], a[36:39], a[4:7], v[0:3]\n" \
"s_waitcnt lgkmcnt(1)\n" \
"ds_read_b128 a[28:31], v213 offset:6400\n" \
"global_load_lds_dwordx4 v222, s[36:37] offset:0 sc1\n" \
"v_exp_f32 v162, v162\n" \
"v_mfma_f32_16x16x32_bf16 v[4:7], a[40:43], a[0:3], v[4:7]\n" \
"s_sub_u32 s74, s74, 32768\n" \
"s_cselect_b32 s74, 131072, s74\n" \
"ds_read_b64_tr_b16 a[48:49], v214 offset:16896\n" \
"v_add_f32 v189, v189, v161\n" \
"v_fma_f32 v171, v171, v144, -v182\n" \
"v_mfma_f32_16x16x32_bf16 v[4:7], a[44:47], a[4:7], v[4:7]\n" \
"ds_read_b64_tr_b16 a[50:51], v214 offset:18944\n" \
"v_exp_f32 v154, v154\n" \
"v_mfma_f32_16x16x32_bf16 v[16:19], a[32:35], a[8:11], v[16:19]\n" \
"ds_read_b64_tr_b16 a[52:53], v214 offset:25088\n" \
"v_add_f32 v188, v188, v153\n" \
"v_fma_f32 v163, v163, v144, -v181\n" \
"v_mfma_f32_16x16x32_bf16 v[16:19], a[36:39], a[12:15], v[16:19]\n" \
"ds_read_b64_tr_b16 a[54:55], v214 offset:27136\n" \
"global_load_lds_dwordx4 v222, s[36:37] offset:1024 sc1\n" \
"v_mfma_f32_16x16x32_bf16 v[20:23], a[40:43], a[8:11], v[20:23]\n" \
"ds_read_b64_tr_b16 a[56:57], v215 offset:16896\n" \
"v_exp_f32 v179, v179\n" \
"v_add_f32 v191, v191, v178\n" \
"v_mfma_f32_16x16x32_bf16 v[20:23], a[44:47], a[12:15], v[20:23]\n" \
"ds_read_b64_tr_b16 a[58:59], v215 offset:18944\n" \
"v_fma_f32 v155, v155, v144, -v180\n" \
"v_mfma_f32_16x16x32_bf16 v[32:35], a[32:35], a[16:19], v[32:35]\n" \
"ds_read_b64_tr_b16 a[60:61], v215 offset:25088\n" \
"v_exp_f32 v171, v171\n" \
"v_add_f32 v190, v190, v170\n" \
"v_mfma_f32_16x16x32_bf16 v[32:35], a[36:39], a[20:23], v[32:35]\n" \
"ds_read_b64_tr_b16 a[62:63], v215 offset:27136\n" \
"global_load_lds_dwordx4 v222, s[36:37] offset:2048 sc1\n" \
"v_mfma_f32_16x16x32_bf16 v[36:39], a[40:43], a[16:19], v[36:39]\n" \
"v_add_u32 v213, s74, v211\n" \
"v_add_u32 v214, s74, v212\n" \
"v_exp_f32 v163, v163\n" \
"v_add_f32 v189, v189, v162\n" \
"v_mfma_f32_16x16x32_bf16 v[36:39], a[44:47], a[20:23], v[36:39]\n" \
"s_waitcnt lgkmcnt(3)\n" \
"v_xor_b32 v215, 32, v214\n" \
"v_exp_f32 v155, v155\n" \
"v_mfma_f32_16x16x32_bf16 v[48:51], a[32:35], a[24:27], v[48:51]\n" \
"v_add_f32 v188, v188, v154\n" \
"v_add_f32 v191, v191, v179\n" \
"v_mfma_f32_16x16x32_bf16 v[48:51], a[36:39], a[28:31], v[48:51]\n" \
"global_load_lds_dwordx4 v222, s[36:37] offset:3072 sc1\n" \
"v_add_f32 v190, v190, v171\n" \
"v_mfma_f32_16x16x32_bf16 v[52:55], a[40:43], a[24:27], v[52:55]\n" \
"s_waitcnt vmcnt(20) lgkmcnt(0)\n" \
"v_add_f32 v189, v189, v163\n" \
"v_add_f32 v188, v188, v155\n" \
"s_add_u32 s36, s36, 0x1000\n" \
"v_mfma_f32_16x16x32_bf16 v[52:55], a[44:47], a[28:31], v[52:55]\n" \
"s_barrier\n" \
"v_mov_b32 v187, v191\n" \
"v_mov_b32 v186, v190\n" \
"v_mfma_f32_16x16x32_bf16 v[8:11], a[48:51], a[0:3], v[8:11]\n" \
"s_add_u32 m0, m0, 0x5000\n" \
"ds_read_b64_tr_b16 a[32:33], v214 offset:16384\n" \
"v_mov_b32 v185, v189\n" \
"v_mov_b32 v184, v188\n" \
"v_mfma_f32_16x16x32_bf16 v[8:11], a[52:55], a[4:7], v[8:11]\n" \
"ds_read_b64_tr_b16 a[34:35], v214 offset:18432\n" \
"global_load_lds_dwordx4 v216, s[38:39] offset:-4096\n" \
"v_mfma_f32_16x16x32_bf16 v[12:15], a[56:59], a[0:3], v[12:15]\n" \
"ds_read_b64_tr_b16 a[36:37], v214 offset:24576\n" \
"v_permlane16_swap_b32 v187, v191\n" \
"v_mfma_f32_16x16x32_bf16 v[12:15], a[60:63], a[4:7], v[12:15]\n" \
"ds_read_b128 a[0:3], v213 offset:0\n" \
"v_permlane16_swap_b32 v186, v190\n" \
"v_mfma_f32_16x16x32_bf16 v[24:27], a[48:51], a[8:11], v[24:27]\n" \
"ds_read_b128 a[4:7], v213 offset:2048\n" \
"v_permlane16_swap_b32 v185, v189\n" \
"v_mfma_f32_16x16x32_bf16 v[24:27], a[52:55], a[12:15], v[24:27]\n" \
"ds_read_b64_tr_b16 a[38:39], v214 offset:26624\n" \
"global_load_lds_dwordx4 v217, s[38:39] offset:-3072\n" \
"v_permlane16_swap_b32 v184, v188\n" \
"v_add_f32 v191, v191, v187\n" \
"v_mfma_f32_16x16x32_bf16 v[28:31], a[56:59], a[8:11], v[28:31]\n" \
"ds_read_b64_tr_b16 a[40:41], v215 offset:16384\n" \
"v_add_f32 v190, v190, v186\n" \
"v_mfma_f32_16x16x32_bf16 v[28:31], a[60:63], a[12:15], v[28:31]\n" \
"ds_read_b128 a[8:11], v213 offset:256\n" \
"v_add_f32 v189, v189, v185\n" \
"v_add_f32 v188, v188, v184\n" \
"v_mfma_f32_16x16x32_bf16 v[40:43], a[48:51], a[16:19], v[40:43]\n" \
"ds_read_b128 a[12:15], v213 offset:2304\n" \
"v_mov_b32 v187, v191\n" \
"v_mov_b32 v186, v190\n" \
"v_mfma_f32_16x16x32_bf16 v[40:43], a[52:55], a[20:23], v[40:43]\n" \
"ds_read_b64_tr_b16 a[42:43], v215 offset:18432\n" \
"global_load_lds_dwordx4 v218, s[38:39] offset:-2048\n" \
"v_mov_b32 v185, v189\n" \
"v_mfma_f32_16x16x32_bf16 v[44:47], a[56:59], a[16:19], v[44:47]\n" \
"ds_read_b64_tr_b16 a[44:45], v215 offset:24576\n" \
"v_mov_b32 v184, v188\n" \
"v_mfma_f32_16x16x32_bf16 v[44:47], a[60:63], a[20:23], v[44:47]\n" \
"ds_read_b128 a[16:19], v213 offset:4096\n" \
"v_permlane32_swap_b32 v187, v191\n" \
"v_mfma_f32_16x16x32_bf16 v[56:59], a[48:51], a[24:27], v[56:59]\n" \
"ds_read_b128 a[20:23], v213 offset:6144\n" \
"global_load_lds_dwordx4 v219, s[38:39] offset:-1024\n" \
"v_permlane32_swap_b32 v186, v190\n" \
"v_mfma_f32_16x16x32_bf16 v[56:59], a[52:55], a[28:31], v[56:59]\n" \
"s_add_u32 s38, s38, 0x20000\n" \
"s_addc_u32 s39, s39, 0\n" \
"ds_read_b64_tr_b16 a[46:47], v215 offset:26624\n" \
"v_permlane32_swap_b32 v185, v189\n" \
"v_mfma_f32_16x16x32_bf16 v[60:63], a[56:59], a[24:27], v[60:63]\n" \
"s_sub_u32 s75, s75, 32768\n" \
"s_cselect_b32 s75, 131072, s75\n" \
"v_permlane32_swap_b32 v184, v188\n" \
"v_mfma_f32_16x16x32_bf16 v[60:63], a[60:63], a[28:31], v[60:63]\n" \
"s_waitcnt lgkmcnt(3)\n" \
"v_add_f32 v191, v191, v187\n" \
"v_add_f32 v190, v190, v186\n" \
"v_mfma_f32_16x16x32_bf16 v[0:3], a[32:35], a[0:3], v[0:3]\n" \
"s_add_u32 m0, s75, s95\n" \
"ds_read_b128 a[24:27], v213 offset:4352\n" \
"v_add_f32 v189, v189, v185\n" \
"v_add_f32 v188, v188, v184\n" \
"v_mfma_f32_16x16x32_bf16 v[0:3], a[36:39], a[4:7], v[0:3]\n" \
"s_waitcnt lgkmcnt(1)\n" \
"ds_read_b128 a[28:31], v213 offset:6400\n" \
"global_load_lds_dwordx4 v222, s[36:37] offset:0 sc1\n" \
"v_rcp_f32 v191, v191\n" \
"v_mfma_f32_16x16x32_bf16 v[4:7], a[40:43], a[0:3], v[4:7]\n" \
"s_sub_u32 s74, s74, 32768\n" \
"s_cselect_b32 s74, 131072, s74\n" \
"ds_read_b64_tr_b16 a[48:49], v214 offset:16896\n" \
"v_mfma_f32_16x16x32_bf16 v[4:7], a[44:47], a[4:7], v[4:7]\n" \
"ds_read_b64_tr_b16 a[50:51], v214 offset:18944\n" \
"v_rcp_f32 v190, v190\n" \
"v_mfma_f32_16x16x32_bf16 v[16:19], a[32:35], a[8:11], v[16:19]\n" \
"ds_read_b64_tr_b16 a[52:53], v214 offset:25088\n" \
"v_rcp_f32 v189, v189\n" \
"v_mfma_f32_16x16x32_bf16 v[16:19], a[36:39], a[12:15], v[16:19]\n" \
"ds_read_b64_tr_b16 a[54:55], v214 offset:27136\n" \
"global_load_lds_dwordx4 v222, s[36:37] offset:1024 sc1\n" \
"v_rcp_f32 v188, v188\n" \
"v_mul_f32 v172, v172, v191\n" \
"v_mfma_f32_16x16x32_bf16 v[20:23], a[40:43], a[8:11], v[20:23]\n" \
"ds_read_b64_tr_b16 a[56:57], v215 offset:16896\n" \
"v_mul_f32 v164, v164, v190\n" \
"v_mul_f32 v156, v156, v189\n" \
"v_mfma_f32_16x16x32_bf16 v[20:23], a[44:47], a[12:15], v[20:23]\n" \
"ds_read_b64_tr_b16 a[58:59], v215 offset:18944\n" \
"v_mul_f32 v148, v148, v188\n" \
"v_mfma_f32_16x16x32_bf16 v[32:35], a[32:35], a[16:19], v[32:35]\n" \
"ds_read_b64_tr_b16 a[60:61], v215 offset:25088\n" \
"v_mul_f32 v173, v173, v191\n" \
"v_mul_f32 v165, v165, v190\n" \
"v_mfma_f32_16x16x32_bf16 v[32:35], a[36:39], a[20:23], v[32:35]\n" \
"ds_read_b64_tr_b16 a[62:63], v215 offset:27136\n" \
"global_load_lds_dwordx4 v222, s[36:37] offset:2048 sc1\n" \
"v_mul_f32 v157, v157, v189\n" \
"v_mul_f32 v149, v149, v188\n" \
"v_mfma_f32_16x16x32_bf16 v[36:39], a[40:43], a[16:19], v[36:39]\n" \
"v_add_u32 v213, s74, v211\n" \
"v_add_u32 v214, s74, v212\n" \
"v_mul_f32 v174, v174, v191\n" \
"v_mul_f32 v166, v166, v190\n" \
"v_mfma_f32_16x16x32_bf16 v[36:39], a[44:47], a[20:23], v[36:39]\n" \
"s_waitcnt lgkmcnt(3)\n" \
"v_xor_b32 v215, 32, v214\n" \
"v_mul_f32 v158, v158, v189\n" \
"v_mfma_f32_16x16x32_bf16 v[48:51], a[32:35], a[24:27], v[48:51]\n" \
"v_mul_f32 v150, v150, v188\n" \
"v_mul_f32 v175, v175, v191\n" \
"v_mfma_f32_16x16x32_bf16 v[48:51], a[36:39], a[28:31], v[48:51]\n" \
"global_load_lds_dwordx4 v222, s[36:37] offset:3072 sc1\n" \
"v_mul_f32 v167, v167, v190\n" \
"v_mul_f32 v159, v159, v189\n" \
"v_mfma_f32_16x16x32_bf16 v[52:55], a[40:43], a[24:27], v[52:55]\n" \
"s_waitcnt vmcnt(20) lgkmcnt(0)\n" \
"v_mul_f32 v151, v151, v188\n" \
"v_mul_f32 v176, v176, v191\n" \
"s_add_u32 s36, s36, 0x1000\n" \
"v_mfma_f32_16x16x32_bf16 v[52:55], a[44:47], a[28:31], v[52:55]\n" \
"s_barrier\n" \
"v_mul_f32 v168, v168, v190\n" \
"v_mul_f32 v160, v160, v189\n" \
"v_mfma_f32_16x16x32_bf16 v[8:11], a[48:51], a[0:3], v[8:11]\n" \
"s_add_u32 m0, m0, 0x5000\n" \
"ds_read_b64_tr_b16 a[32:33], v214 offset:16384\n" \
"v_mul_f32 v152, v152, v188\n" \
"v_mfma_f32_16x16x32_bf16 v[8:11], a[52:55], a[4:7], v[8:11]\n" \
"ds_read_b64_tr_b16 a[34:35], v214 offset:18432\n" \
"global_load_lds_dwordx4 v216, s[38:39] offset:-4096\n" \
"v_mul_f32 v177, v177, v191\n" \
"v_mul_f32 v169, v169, v190\n" \
"v_mfma_f32_16x16x32_bf16 v[12:15], a[56:59], a[0:3], v[12:15]\n" \
"ds_read_b64_tr_b16 a[36:37], v214 offset:24576\n" \
"v_mul_f32 v161, v161, v189\n" \
"v_mul_f32 v153, v153, v188\n" \
"v_mfma_f32_16x16x32_bf16 v[12:15], a[60:63], a[4:7], v[12:15]\n" \
"ds_read_b128 a[0:3], v213 offset:0\n" \
"v_mul_f32 v178, v178, v191\n" \
"v_mul_f32 v170, v170, v190\n" \
"v_mfma_f32_16x16x32_bf16 v[24:27], a[48:51], a[8:11], v[24:27]\n" \
"ds_read_b128 a[4:7], v213 offset:2048\n" \
"v_mul_f32 v162, v162, v189\n" \
"v_mfma_f32_16x16x32_bf16 v[24:27], a[52:55], a[12:15], v[24:27]\n" \
"ds_read_b64_tr_b16 a[38:39], v214 offset:26624\n" \
"global_load_lds_dwordx4 v217, s[38:39] offset:-3072\n" \
"v_mul_f32 v154, v154, v188\n" \
"v_mul_f32 v179, v179, v191\n" \
"v_mfma_f32_16x16x32_bf16 v[28:31], a[56:59], a[8:11], v[28:31]\n" \
"ds_read_b64_tr_b16 a[40:41], v215 offset:16384\n" \
"v_mul_f32 v171, v171, v190\n" \
"v_mul_f32 v163, v163, v189\n" \
"v_mfma_f32_16x16x32_bf16 v[28:31], a[60:63], a[12:15], v[28:31]\n" \
"ds_read_b128 a[8:11], v213 offset:256\n" \
"v_mul_f32 v155, v155, v188\n" \
"v_cvt_pk_bf16_f32 v172, v172, v173\n" \
"v_mfma_f32_16x16x32_bf16 v[40:43], a[48:51], a[16:19], v[40:43]\n" \
"ds_read_b128 a[12:15], v213 offset:2304\n" \
"v_cvt_pk_bf16_f32 v173, v174, v175\n" \
"v_mfma_f32_16x16x32_bf16 v[40:43], a[52:55], a[20:23], v[40:43]\n" \
"ds_read_b64_tr_b16 a[42:43], v215 offset:18432\n" \
"global_load_lds_dwordx4 v218, s[38:39] offset:-2048\n" \
"v_cvt_pk_bf16_f32 v174, v176, v177\n" \
"v_cvt_pk_bf16_f32 v175, v178, v179\n" \
"v_mfma_f32_16x16x32_bf16 v[44:47], a[56:59], a[16:19], v[44:47]\n" \
"ds_read_b64_tr_b16 a[44:45], v215 offset:24576\n" \
"v_cvt_pk_bf16_f32 v164, v164, v165\n" \
"v_cvt_pk_bf16_f32 v165, v166, v167\n" \
"v_mfma_f32_16x16x32_bf16 v[44:47], a[60:63], a[20:23], v[44:47]\n" \
"ds_read_b128 a[16:19], v213 offset:4096\n" \
"v_cvt_pk_bf16_f32 v166, v168, v169\n" \
"v_cvt_pk_bf16_f32 v167, v170, v171\n" \
"v_mfma_f32_16x16x32_bf16 v[56:59], a[48:51], a[24:27], v[56:59]\n" \
"ds_read_b128 a[20:23], v213 offset:6144\n" \
"global_load_lds_dwordx4 v219, s[38:39] offset:-1024\n" \
"v_mfma_f32_16x16x32_bf16 v[56:59], a[52:55], a[28:31], v[56:59]\n" \
"s_add_u32 s38, s38, 0x20000\n" \
"s_addc_u32 s39, s39, 0\n" \
"ds_read_b64_tr_b16 a[46:47], v215 offset:26624\n" \
"v_mfma_f32_16x16x32_bf16 v[60:63], a[56:59], a[24:27], v[60:63]\n" \
"s_sub_u32 s75, s75, 32768\n" \
"s_cselect_b32 s75, 131072, s75\n" \
"v_cvt_pk_bf16_f32 v156, v156, v157\n" \
"v_mfma_f32_16x16x32_bf16 v[60:63], a[60:63], a[28:31], v[60:63]\n" \
"s_waitcnt lgkmcnt(3)\n" \
"v_cvt_pk_bf16_f32 v157, v158, v159\n" \
"v_cvt_pk_bf16_f32 v158, v160, v161\n" \
"v_mfma_f32_16x16x32_bf16 v[0:3], a[32:35], a[0:3], v[0:3]\n" \
"s_add_u32 m0, s75, s95\n" \
"ds_read_b128 a[24:27], v213 offset:4352\n" \
"v_cvt_pk_bf16_f32 v159, v162, v163\n" \
"v_mfma_f32_16x16x32_bf16 v[0:3], a[36:39], a[4:7], v[0:3]\n" \
"s_waitcnt lgkmcnt(1)\n" \
"ds_read_b128 a[28:31], v213 offset:6400\n" \
"global_load_lds_dwordx4 v222, s[36:37] offset:0 sc1\n" \
"v_mfma_f32_16x16x32_bf16 v[4:7], a[40:43], a[0:3], v[4:7]\n" \
"s_sub_u32 s74, s74, 32768\n" \
"s_cselect_b32 s74, 131072, s74\n" \
"ds_read_b64_tr_b16 a[48:49], v214 offset:16896\n" \
"v_mfma_f32_16x16x32_bf16 v[4:7], a[44:47], a[4:7], v[4:7]\n" \
"ds_read_b64_tr_b16 a[50:51], v214 offset:18944\n" \
"v_cvt_pk_bf16_f32 v148, v148, v149\n" \
"v_mfma_f32_16x16x32_bf16 v[16:19], a[32:35], a[8:11], v[16:19]\n" \
"ds_read_b64_tr_b16 a[52:53], v214 offset:25088\n" \
"v_cvt_pk_bf16_f32 v149, v150, v151\n" \
"v_mfma_f32_16x16x32_bf16 v[16:19], a[36:39], a[12:15], v[16:19]\n" \
"ds_read_b64_tr_b16 a[54:55], v214 offset:27136\n" \
"global_load_lds_dwordx4 v222, s[36:37] offset:1024 sc1\n" \
"v_mfma_f32_16x16x32_bf16 v[20:23], a[40:43], a[8:11], v[20:23]\n" \
"ds_read_b64_tr_b16 a[56:57], v215 offset:16896\n" \
"v_cvt_pk_bf16_f32 v150, v152, v153\n" \
"v_mfma_f32_16x16x32_bf16 v[20:23], a[44:47], a[12:15], v[20:23]\n" \
"ds_read_b64_tr_b16 a[58:59], v215 offset:18944\n" \
"v_cvt_pk_bf16_f32 v151, v154, v155\n" \
"v_mfma_f32_16x16x32_bf16 v[32:35], a[32:35], a[16:19], v[32:35]\n" \
"v_mfma_f32_16x16x32_bf16 v[136:139], v[74:77], v[172:175], 0\n" \
"v_mfma_f32_16x16x32_bf16 v[140:143], v[198:201], v[172:175], 0\n" \
"v_mfma_f32_16x16x32_bf16 v[128:131], v[72:75], v[164:167], 0\n" \
"v_mfma_f32_16x16x32_bf16 v[132:135], v[196:199], v[164:167], 0\n" \
"v_mfma_f32_16x16x32_bf16 v[120:123], v[70:73], v[156:159], 0\n" \
"v_mfma_f32_16x16x32_bf16 v[124:127], v[194:197], v[156:159], 0\n" \
"ds_read_b64_tr_b16 a[60:61], v215 offset:25088\n" \
"v_mfma_f32_16x16x32_bf16 v[32:35], a[36:39], a[20:23], v[32:35]\n" \
"v_mfma_f32_16x16x32_bf16 v[112:115], v[68:71], v[148:151], 0\n" \
"v_mfma_f32_16x16x32_bf16 v[116:119], v[192:195], v[148:151], 0\n" \
"ds_read_b64_tr_b16 a[62:63], v215 offset:27136\n" \
"global_load_lds_dwordx4 v222, s[36:37] offset:2048 sc1\n" \
"v_mfma_f32_16x16x32_bf16 v[36:39], a[40:43], a[16:19], v[36:39]\n" \
"v_add_u32 v213, s74, v211\n" \
"v_add_u32 v214, s74, v212\n" \
"v_mfma_f32_16x16x32_bf16 v[36:39], a[44:47], a[20:23], v[36:39]\n" \
"s_waitcnt lgkmcnt(3)\n" \
"v_xor_b32 v215, 32, v214\n" \
"v_cvt_pk_bf16_f32 v136, v136, v137\n" \
"v_cvt_pk_bf16_f32 v137, v138, v139\n" \
"v_mfma_f32_16x16x32_bf16 v[48:51], a[32:35], a[24:27], v[48:51]\n" \
"v_cvt_pk_bf16_f32 v138, v140, v141\n" \
"v_cvt_pk_bf16_f32 v139, v142, v143\n" \
"v_mfma_f32_16x16x32_bf16 v[48:51], a[36:39], a[28:31], v[48:51]\n" \
"global_load_lds_dwordx4 v222, s[36:37] offset:3072 sc1\n" \
"v_cndmask_b32 v140, v136, v138, s[92:93]\n" \
"v_mfma_f32_16x16x32_bf16 v[52:55], a[40:43], a[24:27], v[52:55]\n" \
"s_waitcnt vmcnt(20) lgkmcnt(0)\n" \
"v_cndmask_b32 v141, v137, v139, s[92:93]\n" \
"s_add_u32 s36, s36, 0x1000\n" \
"v_mfma_f32_16x16x32_bf16 v[52:55], a[44:47], a[28:31], v[52:55]\n" \
"s_barrier\n" \
"v_cndmask_b32 v142, v138, v136, s[92:93]\n" \
"v_mfma_f32_16x16x32_bf16 v[8:11], a[48:51], a[0:3], v[8:11]\n" \
"s_add_u32 m0, m0, 0x5000\n" \
"ds_read_b64_tr_b16 a[32:33], v214 offset:16384\n" \
"v_cndmask_b32 v143, v139, v137, s[92:93]\n" \
"v_mfma_f32_16x16x32_bf16 v[8:11], a[52:55], a[4:7], v[8:11]\n" \
"s_add_u32 s71, s87, 208896\n" \
"ds_read_b64_tr_b16 a[34:35], v214 offset:18432\n" \
"global_load_lds_dwordx4 v216, s[38:39] offset:-4096\n" \
"v_mfma_f32_16x16x32_bf16 v[12:15], a[56:59], a[0:3], v[12:15]\n" \
"ds_read_b64_tr_b16 a[36:37], v214 offset:24576\n" \
"buffer_store_dwordx4 v[140:143], v146, s[56:59], s71 offen sc1\n" \
"v_cvt_pk_bf16_f32 v128, v128, v129\n" \
"v_cvt_pk_bf16_f32 v129, v130, v131\n" \
"v_cvt_pk_bf16_f32 v130, v132, v133\n" \
"v_cvt_pk_bf16_f32 v131, v134, v135\n" \
"v_cndmask_b32 v132, v128, v130, s[92:93]\n" \
"v_mfma_f32_16x16x32_bf16 v[12:15], a[60:63], a[4:7], v[12:15]\n" \
"ds_read_b128 a[0:3], v213 offset:0\n" \
"v_cndmask_b32 v133, v129, v131, s[92:93]\n" \
"v_cndmask_b32 v134, v130, v128, s[92:93]\n" \
"v_mfma_f32_16x16x32_bf16 v[24:27], a[48:51], a[8:11], v[24:27]\n" \
"ds_read_b128 a[4:7], v213 offset:2048\n" \
"v_cndmask_b32 v135, v131, v129, s[92:93]\n" \
"v_mfma_f32_16x16x32_bf16 v[24:27], a[52:55], a[12:15], v[24:27]\n" \
"s_add_u32 s71, s87, 139264\n" \
"ds_read_b64_tr_b16 a[38:39], v214 offset:26624\n" \
"global_load_lds_dwordx4 v217, s[38:39] offset:-3072\n" \
"v_mfma_f32_16x16x32_bf16 v[28:31], a[56:59], a[8:11], v[28:31]\n" \
"ds_read_b64_tr_b16 a[40:41], v215 offset:16384\n" \
"buffer_store_dwordx4 v[132:135], v146, s[56:59], s71 offen sc1\n" \
"v_cvt_pk_bf16_f32 v120, v120, v121\n" \
"v_cvt_pk_bf16_f32 v121, v122, v123\n" \
"v_cvt_pk_bf16_f32 v122, v124, v125\n" \
"v_cvt_pk_bf16_f32 v123, v126, v127\n" \
"v_cndmask_b32 v124, v120, v122, s[92:93]\n" \
"v_mfma_f32_16x16x32_bf16 v[28:31], a[60:63], a[12:15], v[28:31]\n" \
"ds_read_b128 a[8:11], v213 offset:256\n" \
"v_cndmask_b32 v125, v121, v123, s[92:93]\n" \
"v_cndmask_b32 v126, v122, v120, s[92:93]\n" \
"v_mfma_f32_16x16x32_bf16 v[40:43], a[48:51], a[16:19], v[40:43]\n" \
"ds_read_b128 a[12:15], v213 offset:2304\n" \
"v_cndmask_b32 v127, v123, v121, s[92:93]\n" \
"v_mfma_f32_16x16x32_bf16 v[40:43], a[52:55], a[20:23], v[40:43]\n" \
"s_add_u32 s71, s87, 69632\n" \
"ds_read_b64_tr_b16 a[42:43], v215 offset:18432\n" \
"global_load_lds_dwordx4 v218, s[38:39] offset:-2048\n" \
"v_mfma_f32_16x16x32_bf16 v[44:47], a[56:59], a[16:19], v[44:47]\n" \
"ds_read_b64_tr_b16 a[44:45], v215 offset:24576\n" \
"buffer_store_dwordx4 v[124:127], v146, s[56:59], s71 offen sc1\n" \
"v_cvt_pk_bf16_f32 v112, v112, v113\n" \
"v_cvt_pk_bf16_f32 v113, v114, v115\n" \
"v_cvt_pk_bf16_f32 v114, v116, v117\n" \
"v_cvt_pk_bf16_f32 v115, v118, v119\n" \
"v_cndmask_b32 v116, v112, v114, s[92:93]\n" \
"v_mfma_f32_16x16x32_bf16 v[44:47], a[60:63], a[20:23], v[44:47]\n" \
"ds_read_b128 a[16:19], v213 offset:4096\n" \
"v_cndmask_b32 v117, v113, v115, s[92:93]\n" \
"v_cndmask_b32 v118, v114, v112, s[92:93]\n" \
"v_cndmask_b32 v119, v115, v113, s[92:93]\n" \
"v_mfma_f32_16x16x32_bf16 v[56:59], a[48:51], a[24:27], v[56:59]\n" \
"s_add_u32 s71, s87, 0\n" \
"ds_read_b128 a[20:23], v213 offset:6144\n" \
"global_load_lds_dwordx4 v219, s[38:39] offset:-1024\n" \
"v_mfma_f32_16x16x32_bf16 v[56:59], a[52:55], a[28:31], v[56:59]\n" \
"buffer_store_dwordx4 v[116:119], v146, s[56:59], s71 offen sc1\n" \
"s_add_u32 s38, s38, 0x20000\n" \
"s_addc_u32 s39, s39, 0\n" \
"ds_read_b64_tr_b16 a[46:47], v215 offset:26624\n" \
"v_mfma_f32_16x16x32_bf16 v[60:63], a[56:59], a[24:27], v[60:63]\n" \
"s_sub_u32 s75, s75, 32768\n" \
"s_cselect_b32 s75, 131072, s75\n" \
"v_mfma_f32_16x16x32_bf16 v[60:63], a[60:63], a[28:31], v[60:63]\n" \
"s_waitcnt lgkmcnt(3)\n" \
"v_mfma_f32_16x16x32_bf16 v[0:3], a[32:35], a[0:3], v[0:3]\n" \
"s_add_u32 m0, s75, s95\n" \
"ds_read_b128 a[24:27], v213 offset:4352\n" \
"v_mov_b32 v224, s60\n" \
"v_mfma_f32_16x16x32_bf16 v[0:3], a[36:39], a[4:7], v[0:3]\n" \
"s_waitcnt lgkmcnt(1)\n" \
"ds_read_b128 a[28:31], v213 offset:6400\n" \
"global_load_lds_dwordx4 v222, s[36:37] offset:0 sc1\n" \
"v_mfma_f32_16x16x32_bf16 v[4:7], a[40:43], a[0:3], v[4:7]\n" \
"s_sub_u32 s74, s74, 32768\n" \
"s_cselect_b32 s74, 131072, s74\n" \
"ds_read_b64_tr_b16 a[48:49], v214 offset:16896\n" \
"v_mfma_f32_16x16x32_bf16 v[4:7], a[44:47], a[4:7], v[4:7]\n" \
"ds_read_b64_tr_b16 a[50:51], v214 offset:18944\n" \
"v_lshlrev_b32 v225, 28, s61\n" \
"v_mfma_f32_16x16x32_bf16 v[16:19], a[32:35], a[8:11], v[16:19]\n" \
"ds_read_b64_tr_b16 a[52:53], v214 offset:25088\n" \
"v_mfma_f32_16x16x32_bf16 v[16:19], a[36:39], a[12:15], v[16:19]\n" \
"ds_read_b64_tr_b16 a[54:55], v214 offset:27136\n" \
"global_load_lds_dwordx4 v222, s[36:37] offset:1024 sc1\n" \
"v_mfma_f32_16x16x32_bf16 v[20:23], a[40:43], a[8:11], v[20:23]\n" \
"ds_read_b64_tr_b16 a[56:57], v215 offset:16896\n" \
"v_mfma_f32_16x16x32_bf16 v[20:23], a[44:47], a[12:15], v[20:23]\n" \
"ds_read_b64_tr_b16 a[58:59], v215 offset:18944\n" \
"v_mfma_f32_16x16x32_bf16 v[32:35], a[32:35], a[16:19], v[32:35]\n" \
"ds_read_b64_tr_b16 a[60:61], v215 offset:25088\n" \
"v_mfma_f32_16x16x32_bf16 v[32:35], a[36:39], a[20:23], v[32:35]\n" \
"ds_read_b64_tr_b16 a[62:63], v215 offset:27136\n" \
"global_load_lds_dwordx4 v222, s[36:37] offset:2048 sc1\n" \
"v_mfma_f32_16x16x32_bf16 v[36:39], a[40:43], a[16:19], v[36:39]\n" \
"v_add_u32 v213, s74, v211\n" \
"v_add_u32 v214, s74, v212\n" \
"v_mfma_f32_16x16x32_bf16 v[36:39], a[44:47], a[20:23], v[36:39]\n" \
"s_waitcnt lgkmcnt(3)\n" \
"v_xor_b32 v215, 32, v214\n" \
"v_mfma_f32_16x16x32_bf16 v[48:51], a[32:35], a[24:27], v[48:51]\n" \
"v_mfma_f32_16x16x32_bf16 v[48:51], a[36:39], a[28:31], v[48:51]\n" \
"global_load_lds_dwordx4 v222, s[36:37] offset:3072 sc1\n" \
"v_mfma_f32_16x16x32_bf16 v[52:55], a[40:43], a[24:27], v[52:55]\n" \
"s_waitcnt vmcnt(24) lgkmcnt(0)\n" \
"s_add_u32 s36, s36, 0x1000\n" \
"v_mfma_f32_16x16x32_bf16 v[52:55], a[44:47], a[28:31], v[52:55]\n" \
"s_barrier\n" \
"v_mfma_f32_16x16x32_bf16 v[8:11], a[48:51], a[0:3], v[8:11]\n" \
"s_add_u32 m0, m0, 0x5000\n" \
"ds_read_b64_tr_b16 a[32:33], v214 offset:16384\n" \
"v_mfma_f32_16x16x32_bf16 v[8:11], a[52:55], a[4:7], v[8:11]\n" \
"ds_read_b64_tr_b16 a[34:35], v214 offset:18432\n" \
"global_load_lds_dwordx4 v216, s[38:39] offset:-4096\n" \
"v_mfma_f32_16x16x32_bf16 v[12:15], a[56:59], a[0:3], v[12:15]\n" \
"ds_read_b64_tr_b16 a[36:37], v214 offset:24576\n" \
"v_mfma_f32_16x16x32_bf16 v[12:15], a[60:63], a[4:7], v[12:15]\n" \
"ds_read_b128 a[0:3], v213 offset:0\n" \
"v_mfma_f32_16x16x32_bf16 v[24:27], a[48:51], a[8:11], v[24:27]\n" \
"ds_read_b128 a[4:7], v213 offset:2048\n" \
"v_mfma_f32_16x16x32_bf16 v[24:27], a[52:55], a[12:15], v[24:27]\n" \
"ds_read_b64_tr_b16 a[38:39], v214 offset:26624\n" \
"global_load_lds_dwordx4 v217, s[38:39] offset:-3072\n" \
"v_mfma_f32_16x16x32_bf16 v[28:31], a[56:59], a[8:11], v[28:31]\n" \
"ds_read_b64_tr_b16 a[40:41], v215 offset:16384\n" \
"v_mfma_f32_16x16x32_bf16 v[28:31], a[60:63], a[12:15], v[28:31]\n" \
"ds_read_b128 a[8:11], v213 offset:256\n" \
"v_mfma_f32_16x16x32_bf16 v[40:43], a[48:51], a[16:19], v[40:43]\n" \
"ds_read_b128 a[12:15], v213 offset:2304\n" \
"v_mfma_f32_16x16x32_bf16 v[40:43], a[52:55], a[20:23], v[40:43]\n" \
"ds_read_b64_tr_b16 a[42:43], v215 offset:18432\n" \
"global_load_lds_dwordx4 v218, s[38:39] offset:-2048\n" \
"v_mfma_f32_16x16x32_bf16 v[44:47], a[56:59], a[16:19], v[44:47]\n" \
"ds_read_b64_tr_b16 a[44:45], v215 offset:24576\n" \
"v_mfma_f32_16x16x32_bf16 v[44:47], a[60:63], a[20:23], v[44:47]\n" \
"ds_read_b128 a[16:19], v213 offset:4096\n" \
"v_mfma_f32_16x16x32_bf16 v[56:59], a[48:51], a[24:27], v[56:59]\n" \
"ds_read_b128 a[20:23], v213 offset:6144\n" \
"global_load_lds_dwordx4 v219, s[38:39] offset:-1024\n" \
"v_mfma_f32_16x16x32_bf16 v[56:59], a[52:55], a[28:31], v[56:59]\n" \
"s_add_u32 s38, s38, 0x20000\n" \
"s_addc_u32 s39, s39, 0\n" \
"ds_read_b64_tr_b16 a[46:47], v215 offset:26624\n" \
"v_mfma_f32_16x16x32_bf16 v[60:63], a[56:59], a[24:27], v[60:63]\n" \
"s_sub_u32 s75, s75, 32768\n" \
"s_cselect_b32 s75, 131072, s75\n" \
"v_mfma_f32_16x16x32_bf16 v[60:63], a[60:63], a[28:31], v[60:63]\n" \
"s_waitcnt lgkmcnt(3)\n" \
"v_mfma_f32_16x16x32_bf16 v[0:3], a[32:35], a[0:3], v[0:3]\n" \
"ds_read_b128 a[24:27], v213 offset:4352\n" \
"s_add_u32 m0, s75, s95\n" \
"v_mfma_f32_16x16x32_bf16 v[0:3], a[36:39], a[4:7], v[0:3]\n" \
"s_waitcnt lgkmcnt(1)\n" \
"ds_read_b128 a[28:31], v213 offset:6400\n" \
"global_load_lds_dwordx4 v222, s[36:37] offset:0 sc1\n" \
"v_mfma_f32_16x16x32_bf16 v[4:7], a[40:43], a[0:3], v[4:7]\n" \
"s_sub_u32 s74, s74, 32768\n" \
"s_cselect_b32 s74, 131072, s74\n" \
"ds_read_b64_tr_b16 a[48:49], v214 offset:16896\n" \
"v_mfma_f32_16x16x32_bf16 v[4:7], a[44:47], a[4:7], v[4:7]\n" \
"ds_read_b64_tr_b16 a[50:51], v214 offset:18944\n" \
"v_mfma_f32_16x16x32_bf16 v[16:19], a[32:35], a[8:11], v[16:19]\n" \
"ds_read_b64_tr_b16 a[52:53], v214 offset:25088\n" \
"v_mfma_f32_16x16x32_bf16 v[16:19], a[36:39], a[12:15], v[16:19]\n" \
"ds_read_b64_tr_b16 a[54:55], v214 offset:27136\n" \
"global_load_lds_dwordx4 v222, s[36:37] offset:1024 sc1\n" \
"v_mfma_f32_16x16x32_bf16 v[20:23], a[40:43], a[8:11], v[20:23]\n" \
"ds_read_b64_tr_b16 a[56:57], v215 offset:16896\n" \
"v_mfma_f32_16x16x32_bf16 v[20:23], a[44:47], a[12:15], v[20:23]\n" \
"ds_read_b64_tr_b16 a[58:59], v215 offset:18944\n" \
"v_mfma_f32_16x16x32_bf16 v[32:35], a[32:35], a[16:19], v[32:35]\n" \
"ds_read_b64_tr_b16 a[60:61], v215 offset:25088\n" \
"v_mfma_f32_16x16x32_bf16 v[32:35], a[36:39], a[20:23], v[32:35]\n" \
"ds_read_b64_tr_b16 a[62:63], v215 offset:27136\n" \
"global_load_lds_dwordx4 v222, s[36:37] offset:2048 sc1\n" \
"v_mfma_f32_16x16x32_bf16 v[36:39], a[40:43], a[16:19], v[36:39]\n" \
"v_add_u32 v213, s74, v211\n" \
"v_add_u32 v214, s74, v212\n" \
"v_mfma_f32_16x16x32_bf16 v[36:39], a[44:47], a[20:23], v[36:39]\n" \
"s_waitcnt lgkmcnt(3)\n" \
"v_xor_b32 v215, 32, v214\n" \
"v_mfma_f32_16x16x32_bf16 v[48:51], a[32:35], a[24:27], v[48:51]\n" \
"v_mfma_f32_16x16x32_bf16 v[48:51], a[36:39], a[28:31], v[48:51]\n" \
"global_load_lds_dwordx4 v222, s[36:37] offset:3072 sc1\n" \
"v_mfma_f32_16x16x32_bf16 v[52:55], a[40:43], a[24:27], v[52:55]\n" \
"Lpg12201_%=:\n" \
"s_cbranch_execz Lpg13315_%=\n" \
"s_waitcnt vmcnt(12) lgkmcnt(0)\n" \
"s_add_u32 s36, s36, 0x1000\n" \
"v_mfma_f32_16x16x32_bf16 v[52:55], a[44:47], a[28:31], v[52:55]\n" \
"s_barrier\n" \
"v_mfma_f32_16x16x32_bf16 v[8:11], a[48:51], a[0:3], v[8:11]\n" \
"s_add_u32 m0, m0, 0x5000\n" \
"ds_read_b64_tr_b16 a[32:33], v214 offset:16384\n" \
"buffer_store_dword v224, v225, s[56:59], s30 offen sc1\n" \
"v_mfma_f32_16x16x32_bf16 v[8:11], a[52:55], a[4:7], v[8:11]\n" \
"ds_read_b64_tr_b16 a[34:35], v214 offset:18432\n" \
"global_load_lds_dwordx4 v216, s[38:39] offset:-4096\n" \
"v_mfma_f32_16x16x32_bf16 v[12:15], a[56:59], a[0:3], v[12:15]\n" \
"ds_read_b64_tr_b16 a[36:37], v214 offset:24576\n" \
"v_mfma_f32_16x16x32_bf16 v[12:15], a[60:63], a[4:7], v[12:15]\n" \
"ds_read_b128 a[0:3], v213 offset:0\n" \
"v_mfma_f32_16x16x32_bf16 v[24:27], a[48:51], a[8:11], v[24:27]\n" \
"ds_read_b128 a[4:7], v213 offset:2048\n" \
"v_mfma_f32_16x16x32_bf16 v[24:27], a[52:55], a[12:15], v[24:27]\n" \
"ds_read_b64_tr_b16 a[38:39], v214 offset:26624\n" \
"global_load_lds_dwordx4 v217, s[38:39] offset:-3072\n" \
"v_mfma_f32_16x16x32_bf16 v[28:31], a[56:59], a[8:11], v[28:31]\n" \
"ds_read_b64_tr_b16 a[40:41], v215 offset:16384\n" \
"v_mfma_f32_16x16x32_bf16 v[28:31], a[60:63], a[12:15], v[28:31]\n" \
"ds_read_b128 a[8:11], v213 offset:256\n" \
"v_mfma_f32_16x16x32_bf16 v[40:43], a[48:51], a[16:19], v[40:43]\n" \
"ds_read_b128 a[12:15], v213 offset:2304\n" \
"v_mfma_f32_16x16x32_bf16 v[40:43], a[52:55], a[20:23], v[40:43]\n" \
"ds_read_b64_tr_b16 a[42:43], v215 offset:18432\n" \
"global_load_lds_dwordx4 v218, s[38:39] offset:-2048\n" \
"v_mfma_f32_16x16x32_bf16 v[44:47], a[56:59], a[16:19], v[44:47]\n" \
"ds_read_b64_tr_b16 a[44:45], v215 offset:24576\n" \
"v_mfma_f32_16x16x32_bf16 v[44:47], a[60:63], a[20:23], v[44:47]\n" \
"ds_read_b128 a[16:19], v213 offset:4096\n" \
"v_mfma_f32_16x16x32_bf16 v[56:59], a[48:51], a[24:27], v[56:59]\n" \
"ds_read_b128 a[20:23], v213 offset:6144\n" \
"global_load_lds_dwordx4 v219, s[38:39] offset:-1024\n" \
"v_mfma_f32_16x16x32_bf16 v[56:59], a[52:55], a[28:31], v[56:59]\n" \
"s_add_u32 s38, s38, 0x20000\n" \
"s_addc_u32 s39, s39, 0\n" \
"ds_read_b64_tr_b16 a[46:47], v215 offset:26624\n" \
"v_mfma_f32_16x16x32_bf16 v[60:63], a[56:59], a[24:27], v[60:63]\n" \
"s_sub_u32 s75, s75, 32768\n" \
"s_cselect_b32 s75, 131072, s75\n" \
"v_mfma_f32_16x16x32_bf16 v[60:63], a[60:63], a[28:31], v[60:63]\n" \
"s_waitcnt lgkmcnt(3)\n" \
"s_cmp_eq_u32 s78, 0\n" \
"s_cbranch_scc0 Lrole1_%=\n" \
"s_sub_u32 s72, s64, 14\n" \
"s_call_b64 s[68:69], Lo0_%=\n" \
"buffer_load_dword v220, off, s[56:59], s79 sc1\n" \
"s_mov_b32 s72, 2\n" \
"s_call_b64 s[68:69], Lo0_%=\n" \
"v_mfma_f32_16x16x32_bf16 v[0:3], a[32:35], a[0:3], v[0:3]\n" \
"s_lshl_b32 s71, s61, 4\n" \
"ds_read_b128 a[24:27], v213 offset:4352\n" \
"v_subrev_u32 v72, s71, v251\n" \
"v_mfma_f32_16x16x32_bf16 v[0:3], a[36:39], a[4:7], v[0:3]\n" \
"s_waitcnt lgkmcnt(1)\n" \
"ds_read_b128 a[28:31], v213 offset:6400\n" \
"v_mov_b32 v73, 0x3f80\n" \
"v_mfma_f32_16x16x32_bf16 v[4:7], a[40:43], a[0:3], v[4:7]\n" \
"s_sub_u32 s74, s74, 32768\n" \
"s_cselect_b32 s74, 131072, s74\n" \
"ds_read_b64_tr_b16 a[48:49], v214 offset:16896\n" \
"v_mfma_f32_16x16x32_bf16 v[4:7], a[44:47], a[4:7], v[4:7]\n" \
"ds_read_b64_tr_b16 a[50:51], v214 offset:18944\n" \
"v_mov_b32 v74, 0x3f800000\n" \
"v_mfma_f32_16x16x32_bf16 v[16:19], a[32:35], a[8:11], v[16:19]\n" \
"ds_read_b64_tr_b16 a[52:53], v214 offset:25088\n" \
"v_cmp_eq_u32 vcc, 0, v72\n" \
"v_mfma_f32_16x16x32_bf16 v[16:19], a[36:39], a[12:15], v[16:19]\n" \
"ds_read_b64_tr_b16 a[54:55], v214 offset:27136\n" \
"v_cmp_eq_u32 s[66:67], 1, v72\n" \
"v_mfma_f32_16x16x32_bf16 v[20:23], a[40:43], a[8:11], v[20:23]\n" \
"ds_read_b64_tr_b16 a[56:57], v215 offset:16896\n" \
"v_mov_b32 v66, 0\n" \
"v_mfma_f32_16x16x32_bf16 v[20:23], a[44:47], a[12:15], v[20:23]\n" \
"ds_read_b64_tr_b16 a[58:59], v215 offset:18944\n" \
"v_mov_b32 v68, 0\n" \
"v_mfma_f32_16x16x32_bf16 v[32:35], a[32:35], a[16:19], v[32:35]\n" \
"ds_read_b64_tr_b16 a[60:61], v215 offset:25088\n" \
"v_cndmask_b32 v64, 0, v73, vcc\n" \
"v_mfma_f32_16x16x32_bf16 v[32:35], a[36:39], a[20:23], v[32:35]\n" \
"ds_read_b64_tr_b16 a[62:63], v215 offset:27136\n" \
"v_cndmask_b32 v64, v64, v74, s[66:67]\n" \
"v_mfma_f32_16x16x32_bf16 v[36:39], a[40:43], a[16:19], v[36:39]\n" \
"v_add_u32 v213, s74, v211\n" \
"v_add_u32 v214, s74, v212\n" \
"v_mfma_f32_16x16x32_bf16 v[36:39], a[44:47], a[20:23], v[36:39]\n" \
"s_waitcnt lgkmcnt(3)\n" \
"v_xor_b32 v215, 32, v214\n" \
"v_mov_b32 v70, v64\n" \
"v_mfma_f32_16x16x32_bf16 v[48:51], a[32:35], a[24:27], v[48:51]\n" \
"v_cmp_eq_u32 vcc, 2, v72\n" \
"v_cmp_eq_u32 s[66:67], 3, v72\n" \
"v_mfma_f32_16x16x32_bf16 v[48:51], a[36:39], a[28:31], v[48:51]\n" \
"v_mov_b32 v67, 0\n" \
"v_mov_b32 v69, 0\n" \
"v_mfma_f32_16x16x32_bf16 v[52:55], a[40:43], a[24:27], v[52:55]\n" \
"s_waitcnt vmcnt(17) lgkmcnt(0)\n" \
"v_cndmask_b32 v65, 0, v73, vcc\n" \
"v_cndmask_b32 v65, v65, v74, s[66:67]\n" \
"v_mfma_f32_16x16x32_bf16 v[52:55], a[44:47], a[28:31], v[52:55]\n" \
"s_barrier\n" \
"v_mov_b32 v71, v65\n" \
"v_mfma_f32_16x16x32_bf16 v[8:11], a[48:51], a[0:3], v[8:11]\n" \
"s_lshl_b32 s83, s95, 1\n" \
"s_add_u32 s83, s83, s74\n" \
"ds_read_b64_tr_b16 a[32:33], v214 offset:16384\n" \
"v_mfma_f32_16x16x32_bf16 v[8:11], a[52:55], a[4:7], v[8:11]\n" \
"s_waitcnt vmcnt(16)\n" \
"ds_read_b64_tr_b16 a[34:35], v214 offset:18432\n" \
"v_cmp_ne_u32 vcc, s60, v220\n" \
"v_mfma_f32_16x16x32_bf16 v[12:15], a[56:59], a[0:3], v[12:15]\n" \
"ds_read_b64_tr_b16 a[36:37], v214 offset:24576\n" \
"v_mfma_f32_16x16x32_bf16 v[12:15], a[60:63], a[4:7], v[12:15]\n" \
"s_and_b32 vcc_hi, vcc_lo, 1\n" \
"s_cbranch_scc1 Lpl4_%=\n" \
"Lpr4_%=:\n" \
"ds_read_b128 a[0:3], v213 offset:0\n" \
"v_mfma_f32_16x16x32_bf16 v[24:27], a[48:51], a[8:11], v[24:27]\n" \
"ds_read_b128 a[4:7], v213 offset:2048\n" \
"v_mfma_f32_16x16x32_bf16 v[24:27], a[52:55], a[12:15], v[24:27]\n" \
"s_add_u32 s71, s76, 0\n" \
"ds_read_b64_tr_b16 a[38:39], v214 offset:26624\n" \
"buffer_load_dwordx4 v[176:179], v248, s[56:59], s71 offen offset:0 sc1\n" \
"v_mfma_f32_16x16x32_bf16 v[28:31], a[56:59], a[8:11], v[28:31]\n" \
"ds_read_b64_tr_b16 a[40:41], v215 offset:16384\n" \
"v_mfma_f32_16x16x32_bf16 v[28:31], a[60:63], a[12:15], v[28:31]\n" \
"buffer_load_dwordx4 v[180:183], v248, s[56:59], s71 offen offset:1024 sc1\n" \
"ds_read_b128 a[8:11], v213 offset:256\n" \
"v_mfma_f32_16x16x32_bf16 v[40:43], a[48:51], a[16:19], v[40:43]\n" \
"ds_read_b128 a[12:15], v213 offset:2304\n" \
"v_mfma_f32_16x16x32_bf16 v[40:43], a[52:55], a[20:23], v[40:43]\n" \
"ds_read_b64_tr_b16 a[42:43], v215 offset:18432\n" \
"v_mfma_f32_16x16x32_bf16 v[44:47], a[56:59], a[16:19], v[44:47]\n" \
"ds_read_b64_tr_b16 a[44:45], v215 offset:24576\n" \
"v_mfma_f32_16x16x32_bf16 v[44:47], a[60:63], a[20:23], v[44:47]\n" \
"ds_read_b128 a[16:19], v213 offset:4096\n" \
"v_mfma_f32_16x16x32_bf16 v[56:59], a[48:51], a[24:27], v[56:59]\n" \
"ds_read_b128 a[20:23], v213 offset:6144\n" \
"v_mfma_f32_16x16x32_bf16 v[56:59], a[52:55], a[28:31], v[56:59]\n" \
"ds_read_b64_tr_b16 a[46:47], v215 offset:26624\n" \
"v_mfma_f32_16x16x32_bf16 v[60:63], a[56:59], a[24:27], v[60:63]\n" \
"buffer_load_dwordx4 v[184:187], v248, s[56:59], s71 offen offset:2048 sc1\n" \
"v_mfma_f32_16x16x32_bf16 v[60:63], a[60:63], a[28:31], v[60:63]\n" \
"s_waitcnt lgkmcnt(3)\n" \
"buffer_load_dwordx4 v[188:191], v248, s[56:59], s71 offen offset:3072 sc1\n" \
"v_mfma_f32_16x16x32_bf16 v[0:3], a[32:35], a[0:3], v[0:3]\n" \
"s_add_u32 s71, s76, 4096\n" \
"ds_read_b128 a[24:27], v213 offset:4352\n" \
"buffer_load_dwordx4 v[192:195], v248, s[56:59], s71 offen offset:0 sc1\n" \
"v_mfma_f32_16x16x32_bf16 v[0:3], a[36:39], a[4:7], v[0:3]\n" \
"s_waitcnt lgkmcnt(1)\n" \
"ds_read_b128 a[28:31], v213 offset:6400\n" \
"v_lshrrev_b32 v132, 4, v248\n" \
"v_mfma_f32_16x16x32_bf16 v[4:7], a[40:43], a[0:3], v[4:7]\n" \
"s_sub_u32 s74, s74, 32768\n" \
"s_cselect_b32 s74, 131072, s74\n" \
"ds_read_b64_tr_b16 a[48:49], v214 offset:16896\n" \
"v_mfma_f32_16x16x32_bf16 v[4:7], a[44:47], a[4:7], v[4:7]\n" \
"ds_read_b64_tr_b16 a[50:51], v214 offset:18944\n" \
"v_and_b32 v133, 15, v132\n" \
"v_mfma_f32_16x16x32_bf16 v[16:19], a[32:35], a[8:11], v[16:19]\n" \
"ds_read_b64_tr_b16 a[52:53], v214 offset:25088\n" \
"v_mul_u32_u24 v128, 144, v133\n" \
"v_mfma_f32_16x16x32_bf16 v[16:19], a[36:39], a[12:15], v[16:19]\n" \
"ds_read_b64_tr_b16 a[54:55], v214 offset:27136\n" \
"v_lshrrev_b32 v133, 4, v132\n" \
"v_mfma_f32_16x16x32_bf16 v[20:23], a[40:43], a[8:11], v[20:23]\n" \
"ds_read_b64_tr_b16 a[56:57], v215 offset:16896\n" \
"v_lshl_add_u32 v128, v133, 3, v128\n" \
"v_mfma_f32_16x16x32_bf16 v[20:23], a[44:47], a[12:15], v[20:23]\n" \
"ds_read_b64_tr_b16 a[58:59], v215 offset:18944\n" \
"v_add_u32 v128, s83, v128\n" \
"v_mfma_f32_16x16x32_bf16 v[32:35], a[32:35], a[16:19], v[32:35]\n" \
"ds_read_b64_tr_b16 a[60:61], v215 offset:25088\n" \
"v_lshrrev_b32 v133, 3, v132\n" \
"v_mfma_f32_16x16x32_bf16 v[32:35], a[36:39], a[20:23], v[32:35]\n" \
"ds_read_b64_tr_b16 a[62:63], v215 offset:27136\n" \
"v_and_b32 v134, 7, v132\n" \
"v_mfma_f32_16x16x32_bf16 v[36:39], a[40:43], a[16:19], v[36:39]\n" \
"v_add_u32 v213, s74, v211\n" \
"v_add_u32 v214, s74, v212\n" \
"v_mfma_f32_16x16x32_bf16 v[36:39], a[44:47], a[20:23], v[36:39]\n" \
"s_waitcnt lgkmcnt(3)\n" \
"v_xor_b32 v215, 32, v214\n" \
"v_mul_u32_u24 v129, 144, v133\n" \
"v_mfma_f32_16x16x32_bf16 v[48:51], a[32:35], a[24:27], v[48:51]\n" \
"buffer_load_dwordx4 v[196:199], v248, s[56:59], s71 offen offset:1024 sc1\n" \
"v_mfma_f32_16x16x32_bf16 v[48:51], a[36:39], a[28:31], v[48:51]\n" \
"buffer_load_dwordx4 v[200:203], v248, s[56:59], s71 offen offset:2048 sc1\n" \
"v_mfma_f32_16x16x32_bf16 v[52:55], a[40:43], a[24:27], v[52:55]\n" \
"s_waitcnt vmcnt(7) lgkmcnt(0)\n" \
"buffer_load_dwordx4 v[204:207], v248, s[56:59], s71 offen offset:3072 sc1\n" \
"v_mfma_f32_16x16x32_bf16 v[52:55], a[44:47], a[28:31], v[52:55]\n" \
"s_barrier\n" \
"v_mfma_f32_16x16x32_bf16 v[8:11], a[48:51], a[0:3], v[8:11]\n" \
"s_mov_b32 s70, 0x20000000\n" \
"ds_read_b128 a[192:195], v213 offset:0\n" \
"ds_read_b64_tr_b16 a[128:129], v214 offset:16384\n" \
"v_mfma_f32_16x16x32_bf16 v[8:11], a[52:55], a[4:7], v[8:11]\n" \
"s_sub_u32 s74, s74, 32768\n" \
"s_cselect_b32 s74, 131072, s74\n" \
"ds_read_b64_tr_b16 a[130:131], v214 offset:18432\n" \
"v_mfma_f32_16x16x32_bf16 v[12:15], a[56:59], a[0:3], v[12:15]\n" \
"ds_read_b128 a[196:199], v213 offset:2048\n" \
"ds_read_b64_tr_b16 a[132:133], v214 offset:24576\n" \
"v_mfma_f32_16x16x32_bf16 v[12:15], a[60:63], a[4:7], v[12:15]\n" \
"ds_read_b64_tr_b16 a[134:135], v214 offset:26624\n" \
"v_add_u32 v226, s74, v211\n" \
"v_mfma_f32_16x16x32_bf16 v[24:27], a[48:51], a[8:11], v[24:27]\n" \
"ds_read_b128 a[200:203], v213 offset:256\n" \
"ds_read_b64_tr_b16 a[136:137], v215 offset:16384\n" \
"v_mfma_f32_16x16x32_bf16 v[24:27], a[52:55], a[12:15], v[24:27]\n" \
"ds_read_b64_tr_b16 a[138:139], v215 offset:18432\n" \
"v_add_u32 v227, s74, v212\n" \
"v_mfma_f32_16x16x32_bf16 v[28:31], a[56:59], a[8:11], v[28:31]\n" \
"ds_read_b128 a[204:207], v213 offset:2304\n" \
"ds_read_b64_tr_b16 a[140:141], v215 offset:24576\n" \
"v_mfma_f32_16x16x32_bf16 v[28:31], a[60:63], a[12:15], v[28:31]\n" \
"ds_read_b64_tr_b16 a[142:143], v215 offset:26624\n" \
"v_xor_b32 v228, 32, v227\n" \
"v_mfma_f32_16x16x32_bf16 v[40:43], a[48:51], a[16:19], v[40:43]\n" \
"s_cmp_eq_u32 s79, 65601536\n" \
"s_cselect_b32 s70, 65593344, s70\n" \
"ds_read_b64_tr_b16 a[144:145], v214 offset:16896\n" \
"v_mfma_f32_16x16x32_bf16 v[40:43], a[52:55], a[20:23], v[40:43]\n" \
"buffer_store_dword v224, v225, s[56:59], s70 offen sc1\n" \
"ds_read_b64_tr_b16 a[146:147], v214 offset:18944\n" \
"v_mfma_f32_16x16x32_bf16 v[44:47], a[56:59], a[16:19], v[44:47]\n" \
"ds_read_b64_tr_b16 a[148:149], v214 offset:25088\n" \
"v_lshl_add_u32 v129, v134, 4, v129\n" \
"v_mfma_f32_16x16x32_bf16 v[44:47], a[60:63], a[20:23], v[44:47]\n" \
"ds_read_b64_tr_b16 a[150:151], v214 offset:27136\n" \
"v_add_u32 v129, s83, v129\n" \
"v_mfma_f32_16x16x32_bf16 v[56:59], a[48:51], a[24:27], v[56:59]\n" \
"ds_read_b64_tr_b16 a[152:153], v215 offset:16896\n" \
"v_lshlrev_b32 v134, 4, v134\n" \
"v_mfma_f32_16x16x32_bf16 v[56:59], a[52:55], a[28:31], v[56:59]\n" \
"ds_read_b64_tr_b16 a[154:155], v215 offset:18944\n" \
"v_lshl_add_u32 v130, v133, 12, v134\n" \
"v_mfma_f32_16x16x32_bf16 v[60:63], a[56:59], a[24:27], v[60:63]\n" \
"ds_read_b64_tr_b16 a[156:157], v215 offset:25088\n" \
"v_mfma_f32_16x16x32_bf16 v[60:63], a[60:63], a[28:31], v[60:63]\n" \
"s_waitcnt lgkmcnt(4)\n" \
"ds_read_b64_tr_b16 a[158:159], v215 offset:27136\n" \
"v_mfma_f32_16x16x32_bf16 v[0:3], a[128:131], a[192:195], v[0:3]\n" \
"ds_read_b128 a[224:227], v226 offset:0\n" \
"ds_read_b64_tr_b16 a[160:161], v227 offset:16384\n" \
"v_mfma_f32_16x16x32_bf16 v[0:3], a[132:135], a[196:199], v[0:3]\n" \
"ds_read_b64_tr_b16 a[162:163], v227 offset:18432\n" \
"v_mfma_f32_16x16x32_bf16 v[16:19], a[128:131], a[200:203], v[16:19]\n" \
"ds_read_b64_tr_b16 a[164:165], v227 offset:24576\n" \
"v_mfma_f32_16x16x32_bf16 v[16:19], a[132:135], a[204:207], v[16:19]\n" \
"ds_read_b128 a[228:231], v226 offset:2048\n" \
"ds_read_b64_tr_b16 a[166:167], v227 offset:26624\n" \
"v_mfma_f32_16x16x32_bf16 v[4:7], a[136:139], a[192:195], v[4:7]\n" \
"ds_read_b64_tr_b16 a[168:169], v228 offset:16384\n" \
"v_mfma_f32_16x16x32_bf16 v[4:7], a[140:143], a[196:199], v[4:7]\n" \
"ds_read_b64_tr_b16 a[170:171], v228 offset:18432\n" \
"v_mfma_f32_16x16x32_bf16 v[20:23], a[136:139], a[200:203], v[20:23]\n" \
"ds_read_b128 a[232:235], v226 offset:256\n" \
"ds_read_b64_tr_b16 a[172:173], v228 offset:24576\n" \
"v_mfma_f32_16x16x32_bf16 v[20:23], a[140:143], a[204:207], v[20:23]\n" \
"s_waitcnt lgkmcnt(6)\n" \
"ds_read_b64_tr_b16 a[174:175], v228 offset:26624\n" \
"v_mfma_f32_16x16x32_bf16 v[8:11], a[144:147], a[192:195], v[8:11]\n" \
"ds_read_b64_tr_b16 a[176:177], v227 offset:16896\n" \
"v_mfma_f32_16x16x32_bf16 v[8:11], a[148:151], a[196:199], v[8:11]\n" \
"ds_read_b128 a[236:239], v226 offset:2304\n" \
"ds_read_b64_tr_b16 a[178:179], v227 offset:18944\n" \
"v_mfma_f32_16x16x32_bf16 v[24:27], a[144:147], a[200:203], v[24:27]\n" \
"ds_read_b64_tr_b16 a[180:181], v227 offset:25088\n" \
"v_mfma_f32_16x16x32_bf16 v[24:27], a[148:151], a[204:207], v[24:27]\n" \
"ds_read_b64_tr_b16 a[182:183], v227 offset:27136\n" \
"v_mfma_f32_16x16x32_bf16 v[12:15], a[152:155], a[192:195], v[12:15]\n" \
"ds_read_b64_tr_b16 a[184:185], v228 offset:16896\n" \
"v_mfma_f32_16x16x32_bf16 v[12:15], a[156:159], a[196:199], v[12:15]\n" \
"ds_read_b64_tr_b16 a[186:187], v228 offset:18944\n" \
"v_mfma_f32_16x16x32_bf16 v[28:31], a[152:155], a[200:203], v[28:31]\n" \
"ds_read_b64_tr_b16 a[188:189], v228 offset:25088\n" \
"v_mfma_f32_16x16x32_bf16 v[28:31], a[156:159], a[204:207], v[28:31]\n" \
"s_waitcnt lgkmcnt(4)\n" \
"ds_read_b64_tr_b16 a[190:191], v228 offset:27136\n" \
"v_mfma_f32_16x16x32_bf16 v[0:3], a[160:163], a[224:227], v[0:3]\n" \
"s_waitcnt vmcnt(1)\n" \
"ds_read_b128 a[208:211], v213 offset:4096\n" \
"v_mfma_f32_16x16x32_bf16 v[0:3], a[164:167], a[228:231], v[0:3]\n" \
"ds_read_b128 a[212:215], v213 offset:6144\n" \
"v_mfma_f32_16x16x32_bf16 v[0:3], v[64:67], v[176:179], v[0:3]\n" \
"ds_read_b128 a[240:243], v226 offset:4096\n" \
"v_mfma_f32_16x16x32_bf16 v[16:19], a[160:163], a[232:235], v[16:19]\n" \
"ds_read_b128 a[244:247], v226 offset:6144\n" \
"v_mfma_f32_16x16x32_bf16 v[16:19], a[164:167], a[236:239], v[16:19]\n" \
"ds_read_b128 a[216:219], v213 offset:4352\n" \
"v_mfma_f32_16x16x32_bf16 v[16:19], v[64:67], v[184:187], v[16:19]\n" \
"ds_read_b128 a[220:223], v213 offset:6400\n" \
"v_cvt_pk_bf16_f32 v0, v0, v1\n" \
"v_mfma_f32_16x16x32_bf16 v[4:7], a[168:171], a[224:227], v[4:7]\n" \
"ds_read_b128 a[248:251], v226 offset:4352\n" \
"v_cvt_pk_bf16_f32 v1, v2, v3\n" \
"v_mfma_f32_16x16x32_bf16 v[4:7], a[172:175], a[228:231], v[4:7]\n" \
"ds_read_b128 a[252:255], v226 offset:6400\n" \
"v_mfma_f32_16x16x32_bf16 v[4:7], v[68:71], v[176:179], v[4:7]\n" \
"ds_write_b64 v128, v[0:1] offset:0\n" \
"v_mfma_f32_16x16x32_bf16 v[20:23], a[168:171], a[232:235], v[20:23]\n" \
"v_cvt_pk_bf16_f32 v16, v16, v17\n" \
"v_cvt_pk_bf16_f32 v17, v18, v19\n" \
"v_mfma_f32_16x16x32_bf16 v[20:23], a[172:175], a[236:239], v[20:23]\n" \
"ds_write_b64 v128, v[16:17] offset:2304\n" \
"v_mfma_f32_16x16x32_bf16 v[20:23], v[68:71], v[184:187], v[20:23]\n" \
"s_waitcnt lgkmcnt(3)\n" \
"v_cvt_pk_bf16_f32 v4, v4, v5\n" \
"v_cvt_pk_bf16_f32 v5, v6, v7\n" \
"v_mfma_f32_16x16x32_bf16 v[8:11], a[176:179], a[224:227], v[8:11]\n" \
"ds_write_b64 v128, v[4:5] offset:32\n" \
"v_mfma_f32_16x16x32_bf16 v[8:11], a[180:183], a[228:231], v[8:11]\n" \
"v_mfma_f32_16x16x32_bf16 v[8:11], v[64:67], v[180:183], v[8:11]\n" \
"v_cvt_pk_bf16_f32 v20, v20, v21\n" \
"v_cvt_pk_bf16_f32 v21, v22, v23\n" \
"v_mfma_f32_16x16x32_bf16 v[24:27], a[176:179], a[232:235], v[24:27]\n" \
"ds_write_b64 v128, v[20:21] offset:2336\n" \
"v_mfma_f32_16x16x32_bf16 v[24:27], a[180:183], a[236:239], v[24:27]\n" \
"v_mfma_f32_16x16x32_bf16 v[24:27], v[64:67], v[188:191], v[24:27]\n" \
"v_cvt_pk_bf16_f32 v8, v8, v9\n" \
"v_cvt_pk_bf16_f32 v9, v10, v11\n" \
"v_mfma_f32_16x16x32_bf16 v[12:15], a[184:187], a[224:227], v[12:15]\n" \
"ds_write_b64 v128, v[8:9] offset:64\n" \
"v_mfma_f32_16x16x32_bf16 v[12:15], a[188:191], a[228:231], v[12:15]\n" \
"v_mfma_f32_16x16x32_bf16 v[12:15], v[68:71], v[180:183], v[12:15]\n" \
"v_cvt_pk_bf16_f32 v24, v24, v25\n" \
"v_cvt_pk_bf16_f32 v25, v26, v27\n" \
"v_mfma_f32_16x16x32_bf16 v[28:31], a[184:187], a[232:235], v[28:31]\n" \
"ds_write_b64 v128, v[24:25] offset:2368\n" \
"v_mfma_f32_16x16x32_bf16 v[28:31], a[188:191], a[236:239], v[28:31]\n" \
"v_mfma_f32_16x16x32_bf16 v[28:31], v[68:71], v[188:191], v[28:31]\n" \
"v_cvt_pk_bf16_f32 v12, v12, v13\n" \
"v_cvt_pk_bf16_f32 v13, v14, v15\n" \
"v_mfma_f32_16x16x32_bf16 v[32:35], a[128:131], a[208:211], v[32:35]\n" \
"ds_write_b64 v128, v[12:13] offset:96\n" \
"v_mfma_f32_16x16x32_bf16 v[32:35], a[132:135], a[212:215], v[32:35]\n" \
"v_mfma_f32_16x16x32_bf16 v[32:35], a[160:163], a[240:243], v[32:35]\n" \
"v_cvt_pk_bf16_f32 v28, v28, v29\n" \
"v_cvt_pk_bf16_f32 v29, v30, v31\n" \
"v_mfma_f32_16x16x32_bf16 v[32:35], a[164:167], a[244:247], v[32:35]\n" \
"ds_write_b64 v128, v[28:29] offset:2400\n" \
"v_mfma_f32_16x16x32_bf16 v[32:35], v[64:67], v[192:195], v[32:35]\n" \
"ds_read_b128 v[140:143], v129 offset:0\n" \
"v_mfma_f32_16x16x32_bf16 v[36:39], a[136:139], a[208:211], v[36:39]\n" \
"ds_read_b128 v[144:147], v129 offset:1152\n" \
"v_mfma_f32_16x16x32_bf16 v[36:39], a[140:143], a[212:215], v[36:39]\n" \
"ds_read_b128 v[148:151], v129 offset:2304\n" \
"v_mfma_f32_16x16x32_bf16 v[36:39], a[168:171], a[240:243], v[36:39]\n" \
"ds_read_b128 v[152:155], v129 offset:3456\n" \
"v_mfma_f32_16x16x32_bf16 v[36:39], a[172:175], a[244:247], v[36:39]\n" \
"v_cvt_pk_bf16_f32 v32, v32, v33\n" \
"v_cvt_pk_bf16_f32 v33, v34, v35\n" \
"v_mfma_f32_16x16x32_bf16 v[36:39], v[68:71], v[192:195], v[36:39]\n" \
"ds_write_b64 v128, v[32:33] offset:0\n" \
"v_mfma_f32_16x16x32_bf16 v[40:43], a[144:147], a[208:211], v[40:43]\n" \
"v_mfma_f32_16x16x32_bf16 v[40:43], a[148:151], a[212:215], v[40:43]\n" \
"s_add_u32 s70, s77, 0\n" \
"s_waitcnt lgkmcnt(4)\n" \
"v_mfma_f32_16x16x32_bf16 v[40:43], a[176:179], a[240:243], v[40:43]\n" \
"v_cvt_pk_bf16_f32 v36, v36, v37\n" \
"v_cvt_pk_bf16_f32 v37, v38, v39\n" \
"v_mfma_f32_16x16x32_bf16 v[40:43], a[180:183], a[244:247], v[40:43]\n" \
"buffer_store_dwordx4 v[140:143], v130, s[52:55], s70 offen sc1\n" \
"ds_write_b64 v128, v[36:37] offset:32\n" \
"v_mfma_f32_16x16x32_bf16 v[40:43], v[64:67], v[196:199], v[40:43]\n" \
"v_mfma_f32_16x16x32_bf16 v[44:47], a[152:155], a[208:211], v[44:47]\n" \
"v_mfma_f32_16x16x32_bf16 v[44:47], a[156:159], a[212:215], v[44:47]\n" \
"v_mfma_f32_16x16x32_bf16 v[44:47], a[184:187], a[240:243], v[44:47]\n" \
"s_add_u32 s70, s77, 32768\n" \
"v_cvt_pk_bf16_f32 v40, v40, v41\n" \
"v_cvt_pk_bf16_f32 v41, v42, v43\n" \
"v_mfma_f32_16x16x32_bf16 v[44:47], a[188:191], a[244:247], v[44:47]\n" \
"s_waitcnt lgkmcnt(4)\n" \
"buffer_store_dwordx4 v[144:147], v130, s[52:55], s70 offen sc1\n" \
"ds_write_b64 v128, v[40:41] offset:64\n" \
"v_mfma_f32_16x16x32_bf16 v[44:47], v[68:71], v[196:199], v[44:47]\n" \
"v_mfma_f32_16x16x32_bf16 v[48:51], a[128:131], a[216:219], v[48:51]\n" \
"v_mfma_f32_16x16x32_bf16 v[48:51], a[132:135], a[220:223], v[48:51]\n" \
"v_mfma_f32_16x16x32_bf16 v[48:51], a[160:163], a[248:251], v[48:51]\n" \
"s_add_u32 s70, s77, 65536\n" \
"v_cvt_pk_bf16_f32 v44, v44, v45\n" \
"v_cvt_pk_bf16_f32 v45, v46, v47\n" \
"v_mfma_f32_16x16x32_bf16 v[48:51], a[164:167], a[252:255], v[48:51]\n" \
"s_waitcnt lgkmcnt(4)\n" \
"buffer_store_dwordx4 v[148:151], v130, s[52:55], s70 offen sc1\n" \
"ds_write_b64 v128, v[44:45] offset:96\n" \
"v_mfma_f32_16x16x32_bf16 v[48:51], v[64:67], v[200:203], v[48:51]\n" \
"ds_read_b128 v[156:159], v129 offset:0\n" \
"v_mfma_f32_16x16x32_bf16 v[52:55], a[136:139], a[216:219], v[52:55]\n" \
"ds_read_b128 v[160:163], v129 offset:1152\n" \
"v_mfma_f32_16x16x32_bf16 v[52:55], a[140:143], a[220:223], v[52:55]\n" \
"v_mfma_f32_16x16x32_bf16 v[52:55], a[168:171], a[248:251], v[52:55]\n" \
"s_add_u32 s70, s77, 98304\n" \
"v_cvt_pk_bf16_f32 v48, v48, v49\n" \
"v_cvt_pk_bf16_f32 v49, v50, v51\n" \
"v_mfma_f32_16x16x32_bf16 v[52:55], a[172:175], a[252:255], v[52:55]\n" \
"s_waitcnt lgkmcnt(6)\n" \
"buffer_store_dwordx4 v[152:155], v130, s[52:55], s70 offen sc1\n" \
"ds_write_b64 v128, v[48:49] offset:2304\n" \
"v_mfma_f32_16x16x32_bf16 v[52:55], v[68:71], v[200:203], v[52:55]\n" \
"v_mfma_f32_16x16x32_bf16 v[56:59], a[144:147], a[216:219], v[56:59]\n" \
"v_mfma_f32_16x16x32_bf16 v[56:59], a[148:151], a[220:223], v[56:59]\n" \
"v_mfma_f32_16x16x32_bf16 v[56:59], a[176:179], a[248:251], v[56:59]\n" \
"s_add_u32 s70, s77, 131072\n" \
"v_cvt_pk_bf16_f32 v52, v52, v53\n" \
"v_cvt_pk_bf16_f32 v53, v54, v55\n" \
"v_mfma_f32_16x16x32_bf16 v[56:59], a[180:183], a[252:255], v[56:59]\n" \
"s_waitcnt lgkmcnt(2)\n" \
"buffer_store_dwordx4 v[156:159], v130, s[52:55], s70 offen sc1\n" \
"ds_write_b64 v128, v[52:53] offset:2336\n" \
"v_mfma_f32_16x16x32_bf16 v[56:59], v[64:67], v[204:207], v[56:59]\n" \
"v_mfma_f32_16x16x32_bf16 v[60:63], a[152:155], a[216:219], v[60:63]\n" \
"v_mfma_f32_16x16x32_bf16 v[60:63], a[156:159], a[220:223], v[60:63]\n" \
"v_mfma_f32_16x16x32_bf16 v[60:63], a[184:187], a[248:251], v[60:63]\n" \
"s_add_u32 s70, s77, 163840\n" \
"v_cvt_pk_bf16_f32 v56, v56, v57\n" \
"v_cvt_pk_bf16_f32 v57, v58, v59\n" \
"v_mfma_f32_16x16x32_bf16 v[60:63], a[188:191], a[252:255], v[60:63]\n" \
"s_waitcnt lgkmcnt(2)\n" \
"buffer_store_dwordx4 v[160:163], v130, s[52:55], s70 offen sc1\n" \
"ds_write_b64 v128, v[56:57] offset:2368\n" \
"v_mfma_f32_16x16x32_bf16 v[60:63], v[68:71], v[204:207], v[60:63]\n" \
"s_nop 7\n" \
"v_cvt_pk_bf16_f32 v60, v60, v61\n" \
"v_cvt_pk_bf16_f32 v61, v62, v63\n" \
"ds_write_b64 v128, v[60:61] offset:2400\n" \
"ds_read_b128 v[164:167], v129 offset:2304\n" \
"ds_read_b128 v[168:171], v129 offset:3456\n" \
"s_add_u32 s70, s77, 196608\n" \
"s_waitcnt lgkmcnt(1)\n" \
"buffer_store_dwordx4 v[164:167], v130, s[52:55], s70 offen sc1\n" \
"s_add_u32 s70, s77, 229376\n" \
"s_waitcnt lgkmcnt(0)\n" \
"buffer_store_dwordx4 v[168:171], v130, s[52:55], s70 offen sc1\n" \
"s_endpgm\n" \
"Lpg12643_%=:\n" \
"s_cbranch_execz Lpg13616_%=\n" \
"Lo0_%=:\n" \
"v_mfma_f32_16x16x32_bf16 v[0:3], a[32:35], a[0:3], v[0:3]\n" \
"ds_read_b128 a[24:27], v213 offset:4352\n" \
"s_add_u32 m0, s75, s95\n" \
"v_mfma_f32_16x16x32_bf16 v[0:3], a[36:39], a[4:7], v[0:3]\n" \
"s_waitcnt lgkmcnt(1)\n" \
"ds_read_b128 a[28:31], v213 offset:6400\n" \
"global_load_lds_dwordx4 v222, s[36:37] offset:0 sc1\n" \
"v_mfma_f32_16x16x32_bf16 v[4:7], a[40:43], a[0:3], v[4:7]\n" \
"s_sub_u32 s74, s74, 32768\n" \
"s_cselect_b32 s74, 131072, s74\n" \
"ds_read_b64_tr_b16 a[48:49], v214 offset:16896\n" \
"v_mfma_f32_16x16x32_bf16 v[4:7], a[44:47], a[4:7], v[4:7]\n" \
"ds_read_b64_tr_b16 a[50:51], v214 offset:18944\n" \
"v_mfma_f32_16x16x32_bf16 v[16:19], a[32:35], a[8:11], v[16:19]\n" \
"ds_read_b64_tr_b16 a[52:53], v214 offset:25088\n" \
"v_mfma_f32_16x16x32_bf16 v[16:19], a[36:39], a[12:15], v[16:19]\n" \
"ds_read_b64_tr_b16 a[54:55], v214 offset:27136\n" \
"global_load_lds_dwordx4 v222, s[36:37] offset:1024 sc1\n" \
"v_mfma_f32_16x16x32_bf16 v[20:23], a[40:43], a[8:11], v[20:23]\n" \
"ds_read_b64_tr_b16 a[56:57], v215 offset:16896\n" \
"v_mfma_f32_16x16x32_bf16 v[20:23], a[44:47], a[12:15], v[20:23]\n" \
"ds_read_b64_tr_b16 a[58:59], v215 offset:18944\n" \
"v_mfma_f32_16x16x32_bf16 v[32:35], a[32:35], a[16:19], v[32:35]\n" \
"ds_read_b64_tr_b16 a[60:61], v215 offset:25088\n" \
"v_mfma_f32_16x16x32_bf16 v[32:35], a[36:39], a[20:23], v[32:35]\n" \
"ds_read_b64_tr_b16 a[62:63], v215 offset:27136\n" \
"global_load_lds_dwordx4 v222, s[36:37] offset:2048 sc1\n" \
"v_mfma_f32_16x16x32_bf16 v[36:39], a[40:43], a[16:19], v[36:39]\n" \
"v_add_u32 v213, s74, v211\n" \
"v_add_u32 v214, s74, v212\n" \
"v_mfma_f32_16x16x32_bf16 v[36:39], a[44:47], a[20:23], v[36:39]\n" \
"s_waitcnt lgkmcnt(3)\n" \
"v_xor_b32 v215, 32, v214\n" \
"v_mfma_f32_16x16x32_bf16 v[48:51], a[32:35], a[24:27], v[48:51]\n" \
"v_mfma_f32_16x16x32_bf16 v[48:51], a[36:39], a[28:31], v[48:51]\n" \
"global_load_lds_dwordx4 v222, s[36:37] offset:3072 sc1\n" \
"v_mfma_f32_16x16x32_bf16 v[52:55], a[40:43], a[24:27], v[52:55]\n" \
"s_waitcnt vmcnt(20) lgkmcnt(0)\n" \
"s_add_u32 s36, s36, 0x1000\n" \
"v_mfma_f32_16x16x32_bf16 v[52:55], a[44:47], a[28:31], v[52:55]\n" \
"s_barrier\n" \
"v_mfma_f32_16x16x32_bf16 v[8:11], a[48:51], a[0:3], v[8:11]\n" \
"s_add_u32 m0, m0, 0x5000\n" \
"ds_read_b64_tr_b16 a[32:33], v214 offset:16384\n" \
"v_mfma_f32_16x16x32_bf16 v[8:11], a[52:55], a[4:7], v[8:11]\n" \
"ds_read_b64_tr_b16 a[34:35], v214 offset:18432\n" \
"global_load_lds_dwordx4 v216, s[38:39] offset:-4096\n" \
"v_mfma_f32_16x16x32_bf16 v[12:15], a[56:59], a[0:3], v[12:15]\n" \
"ds_read_b64_tr_b16 a[36:37], v214 offset:24576\n" \
"v_mfma_f32_16x16x32_bf16 v[12:15], a[60:63], a[4:7], v[12:15]\n" \
"ds_read_b128 a[0:3], v213 offset:0\n" \
"v_mfma_f32_16x16x32_bf16 v[24:27], a[48:51], a[8:11], v[24:27]\n" \
"ds_read_b128 a[4:7], v213 offset:2048\n" \
"v_mfma_f32_16x16x32_bf16 v[24:27], a[52:55], a[12:15], v[24:27]\n" \
"ds_read_b64_tr_b16 a[38:39], v214 offset:26624\n" \
"global_load_lds_dwordx4 v217, s[38:39] offset:-3072\n" \
"v_mfma_f32_16x16x32_bf16 v[28:31], a[56:59], a[8:11], v[28:31]\n" \
"ds_read_b64_tr_b16 a[40:41], v215 offset:16384\n" \
"v_mfma_f32_16x16x32_bf16 v[28:31], a[60:63], a[12:15], v[28:31]\n" \
"ds_read_b128 a[8:11], v213 offset:256\n" \
"v_mfma_f32_16x16x32_bf16 v[40:43], a[48:51], a[16:19], v[40:43]\n" \
"ds_read_b128 a[12:15], v213 offset:2304\n" \
"v_mfma_f32_16x16x32_bf16 v[40:43], a[52:55], a[20:23], v[40:43]\n" \
"ds_read_b64_tr_b16 a[42:43], v215 offset:18432\n" \
"global_load_lds_dwordx4 v218, s[38:39] offset:-2048\n" \
"v_mfma_f32_16x16x32_bf16 v[44:47], a[56:59], a[16:19], v[44:47]\n" \
"ds_read_b64_tr_b16 a[44:45], v215 offset:24576\n" \
"v_mfma_f32_16x16x32_bf16 v[44:47], a[60:63], a[20:23], v[44:47]\n" \
"ds_read_b128 a[16:19], v213 offset:4096\n" \
"global_load_lds_dwordx4 v219, s[38:39] offset:-1024\n" \
"v_mfma_f32_16x16x32_bf16 v[56:59], a[48:51], a[24:27], v[56:59]\n" \
"s_add_u32 s38, s38, 0x20000\n" \
"s_addc_u32 s39, s39, 0\n" \
"ds_read_b128 a[20:23], v213 offset:6144\n" \
"v_mfma_f32_16x16x32_bf16 v[56:59], a[52:55], a[28:31], v[56:59]\n" \
"s_sub_u32 s75, s75, 32768\n" \
"s_cselect_b32 s75, 131072, s75\n" \
"ds_read_b64_tr_b16 a[46:47], v215 offset:26624\n" \
"v_mfma_f32_16x16x32_bf16 v[60:63], a[56:59], a[24:27], v[60:63]\n" \
"s_sub_u32 s72, s72, 1\n" \
"s_cmp_lg_u32 s72, 0\n" \
"v_mfma_f32_16x16x32_bf16 v[60:63], a[60:63], a[28:31], v[60:63]\n" \
"s_waitcnt lgkmcnt(3)\n" \
"s_cbranch_scc1 Lo0_%=\n" \
"s_setpc_b64 s[68:69]\n" \
"Lrole1_%=:\n" \
"Lpg12731_%=:\n" \
"s_cbranch_execz Lcb1_%=\n" \
"s_sub_u32 s72, 50, s64\n" \
"s_call_b64 s[68:69], Lo0_%=\n" \
"buffer_load_dword v220, v221, s[56:59], s82 offen sc1\n" \
"s_mov_b32 s72, 2\n" \
"s_call_b64 s[68:69], Lo0_%=\n" \
"s_waitcnt vmcnt(16)\n" \
"v_cmp_ne_u32 vcc, s60, v220\n" \
"s_nop 1\n" \
"s_and_b32 vcc_hi, vcc_lo, 65535\n" \
"s_cbranch_scc1 Lpl5_%=\n" \
"Lpr5_%=:\n" \
"s_mov_b32 s36, s73\n" \
"s_mov_b64 s[38:39], s[40:41]\n" \
"s_mov_b32 s72, 4\n" \
"s_call_b64 s[68:69], Lo0_%=\n" \
"v_mfma_f32_16x16x32_bf16 v[64:67], a[32:35], a[0:3], 0\n" \
"ds_read_b128 a[24:27], v213 offset:4352\n" \
"s_add_u32 m0, s75, s95\n" \
"v_mfma_f32_16x16x32_bf16 v[64:67], a[36:39], a[4:7], v[64:67]\n" \
"s_waitcnt lgkmcnt(1)\n" \
"ds_read_b128 a[28:31], v213 offset:6400\n" \
"global_load_lds_dwordx4 v222, s[36:37] offset:0 sc1\n" \
"v_mfma_f32_16x16x32_bf16 v[68:71], a[40:43], a[0:3], 0\n" \
"s_sub_u32 s74, s74, 32768\n" \
"s_cselect_b32 s74, 131072, s74\n" \
"ds_read_b64_tr_b16 a[48:49], v214 offset:16896\n" \
"v_mfma_f32_16x16x32_bf16 v[68:71], a[44:47], a[4:7], v[68:71]\n" \
"ds_read_b64_tr_b16 a[50:51], v214 offset:18944\n" \
"v_cvt_pk_bf16_f32 v0, v0, v1\n" \
"v_mfma_f32_16x16x32_bf16 v[80:83], a[32:35], a[8:11], 0\n" \
"ds_read_b64_tr_b16 a[52:53], v214 offset:25088\n" \
"v_cvt_pk_bf16_f32 v1, v2, v3\n" \
"v_mfma_f32_16x16x32_bf16 v[80:83], a[36:39], a[12:15], v[80:83]\n" \
"ds_read_b64_tr_b16 a[54:55], v214 offset:27136\n" \
"global_load_lds_dwordx4 v222, s[36:37] offset:1024 sc1\n" \
"v_mfma_f32_16x16x32_bf16 v[84:87], a[40:43], a[8:11], 0\n" \
"Lpg12768_%=:\n" \
"s_cbranch_execz Lcb1_%=\n" \
"ds_read_b64_tr_b16 a[56:57], v215 offset:16896\n" \
"v_cvt_pk_bf16_f32 v2, v4, v5\n" \
"v_mfma_f32_16x16x32_bf16 v[84:87], a[44:47], a[12:15], v[84:87]\n" \
"ds_read_b64_tr_b16 a[58:59], v215 offset:18944\n" \
"v_cvt_pk_bf16_f32 v3, v6, v7\n" \
"v_mfma_f32_16x16x32_bf16 v[96:99], a[32:35], a[16:19], 0\n" \
"s_add_u32 s71, s76, 0\n" \
"ds_read_b64_tr_b16 a[60:61], v215 offset:25088\n" \
"v_mfma_f32_16x16x32_bf16 v[96:99], a[36:39], a[20:23], v[96:99]\n" \
"ds_read_b64_tr_b16 a[62:63], v215 offset:27136\n" \
"global_load_lds_dwordx4 v222, s[36:37] offset:2048 sc1\n" \
"v_mfma_f32_16x16x32_bf16 v[100:103], a[40:43], a[16:19], 0\n" \
"v_add_u32 v213, s74, v211\n" \
"v_add_u32 v214, s74, v212\n" \
"v_mfma_f32_16x16x32_bf16 v[100:103], a[44:47], a[20:23], v[100:103]\n" \
"s_waitcnt lgkmcnt(3)\n" \
"v_xor_b32 v215, 32, v214\n" \
"v_mfma_f32_16x16x32_bf16 v[112:115], a[32:35], a[24:27], 0\n" \
"v_mfma_f32_16x16x32_bf16 v[112:115], a[36:39], a[28:31], v[112:115]\n" \
"global_load_lds_dwordx4 v222, s[36:37] offset:3072 sc1\n" \
"v_mfma_f32_16x16x32_bf16 v[116:119], a[40:43], a[24:27], 0\n" \
"s_waitcnt vmcnt(20) lgkmcnt(0)\n" \
"s_add_u32 s36, s36, 0x1000\n" \
"v_mfma_f32_16x16x32_bf16 v[116:119], a[44:47], a[28:31], v[116:119]\n" \
"s_barrier\n" \
"v_mfma_f32_16x16x32_bf16 v[72:75], a[48:51], a[0:3], 0\n" \
"s_add_u32 m0, m0, 0x5000\n" \
"ds_read_b64_tr_b16 a[32:33], v214 offset:16384\n" \
"v_mfma_f32_16x16x32_bf16 v[72:75], a[52:55], a[4:7], v[72:75]\n" \
"ds_read_b64_tr_b16 a[34:35], v214 offset:18432\n" \
"global_load_lds_dwordx4 v216, s[38:39] offset:-4096\n" \
"v_mfma_f32_16x16x32_bf16 v[76:79], a[56:59], a[0:3], 0\n" \
"ds_read_b64_tr_b16 a[36:37], v214 offset:24576\n" \
"v_mfma_f32_16x16x32_bf16 v[76:79], a[60:63], a[4:7], v[76:79]\n" \
"ds_read_b128 a[0:3], v213 offset:0\n" \
"v_mfma_f32_16x16x32_bf16 v[88:91], a[48:51], a[8:11], 0\n" \
"ds_read_b128 a[4:7], v213 offset:2048\n" \
"v_mfma_f32_16x16x32_bf16 v[88:91], a[52:55], a[12:15], v[88:91]\n" \
"ds_read_b64_tr_b16 a[38:39], v214 offset:26624\n" \
"global_load_lds_dwordx4 v217, s[38:39] offset:-3072\n" \
"v_mfma_f32_16x16x32_bf16 v[92:95], a[56:59], a[8:11], 0\n" \
"buffer_store_dwordx4 v[0:3], v248, s[56:59], s71 offen offset:0 sc1\n" \
"ds_read_b64_tr_b16 a[40:41], v215 offset:16384\n" \
"v_cvt_pk_bf16_f32 v4, v8, v9\n" \
"v_cvt_pk_bf16_f32 v5, v10, v11\n" \
"v_cvt_pk_bf16_f32 v6, v12, v13\n" \
"v_mfma_f32_16x16x32_bf16 v[92:95], a[60:63], a[12:15], v[92:95]\n" \
"ds_read_b128 a[8:11], v213 offset:256\n" \
"v_cvt_pk_bf16_f32 v7, v14, v15\n" \
"v_mfma_f32_16x16x32_bf16 v[104:107], a[48:51], a[16:19], 0\n" \
"ds_read_b128 a[12:15], v213 offset:2304\n" \
"v_mfma_f32_16x16x32_bf16 v[104:107], a[52:55], a[20:23], v[104:107]\n" \
"ds_read_b64_tr_b16 a[42:43], v215 offset:18432\n" \
"global_load_lds_dwordx4 v218, s[38:39] offset:-2048\n" \
"v_mfma_f32_16x16x32_bf16 v[108:111], a[56:59], a[16:19], 0\n" \
"buffer_store_dwordx4 v[4:7], v248, s[56:59], s71 offen offset:1024 sc1\n" \
"ds_read_b64_tr_b16 a[44:45], v215 offset:24576\n" \
"v_cvt_pk_bf16_f32 v8, v16, v17\n" \
"v_cvt_pk_bf16_f32 v9, v18, v19\n" \
"v_cvt_pk_bf16_f32 v10, v20, v21\n" \
"v_mfma_f32_16x16x32_bf16 v[108:111], a[60:63], a[20:23], v[108:111]\n" \
"ds_read_b128 a[16:19], v213 offset:4096\n" \
"v_cvt_pk_bf16_f32 v11, v22, v23\n" \
"v_mfma_f32_16x16x32_bf16 v[120:123], a[48:51], a[24:27], 0\n" \
"ds_read_b128 a[20:23], v213 offset:6144\n" \
"global_load_lds_dwordx4 v219, s[38:39] offset:-1024\n" \
"v_mfma_f32_16x16x32_bf16 v[120:123], a[52:55], a[28:31], v[120:123]\n" \
"s_add_u32 s38, s38, 0x20000\n" \
"s_addc_u32 s39, s39, 0\n" \
"ds_read_b64_tr_b16 a[46:47], v215 offset:26624\n" \
"v_mfma_f32_16x16x32_bf16 v[124:127], a[56:59], a[24:27], 0\n" \
"buffer_store_dwordx4 v[8:11], v248, s[56:59], s71 offen offset:2048 sc1\n" \
"s_sub_u32 s75, s75, 32768\n" \
"s_cselect_b32 s75, 131072, s75\n" \
"v_cvt_pk_bf16_f32 v12, v24, v25\n" \
"v_cvt_pk_bf16_f32 v13, v26, v27\n" \
"v_mfma_f32_16x16x32_bf16 v[124:127], a[60:63], a[28:31], v[124:127]\n" \
"s_waitcnt lgkmcnt(3)\n" \
"v_cvt_pk_bf16_f32 v14, v28, v29\n" \
"v_cvt_pk_bf16_f32 v15, v30, v31\n" \
"v_mfma_f32_16x16x32_bf16 v[64:67], a[32:35], a[0:3], v[64:67]\n" \
"ds_read_b128 a[24:27], v213 offset:4352\n" \
"s_add_u32 m0, s75, s95\n" \
"v_mfma_f32_16x16x32_bf16 v[64:67], a[36:39], a[4:7], v[64:67]\n" \
"s_waitcnt lgkmcnt(1)\n" \
"ds_read_b128 a[28:31], v213 offset:6400\n" \
"global_load_lds_dwordx4 v222, s[36:37] offset:0 sc1\n" \
"v_mfma_f32_16x16x32_bf16 v[68:71], a[40:43], a[0:3], v[68:71]\n" \
"s_sub_u32 s74, s74, 32768\n" \
"s_cselect_b32 s74, 131072, s74\n" \
"ds_read_b64_tr_b16 a[48:49], v214 offset:16896\n" \
"v_mfma_f32_16x16x32_bf16 v[68:71], a[44:47], a[4:7], v[68:71]\n" \
"ds_read_b64_tr_b16 a[50:51], v214 offset:18944\n" \
"v_mfma_f32_16x16x32_bf16 v[80:83], a[32:35], a[8:11], v[80:83]\n" \
"ds_read_b64_tr_b16 a[52:53], v214 offset:25088\n" \
"v_mfma_f32_16x16x32_bf16 v[80:83], a[36:39], a[12:15], v[80:83]\n" \
"ds_read_b64_tr_b16 a[54:55], v214 offset:27136\n" \
"global_load_lds_dwordx4 v222, s[36:37] offset:1024 sc1\n" \
"v_mfma_f32_16x16x32_bf16 v[84:87], a[40:43], a[8:11], v[84:87]\n" \
"ds_read_b64_tr_b16 a[56:57], v215 offset:16896\n" \
"v_mfma_f32_16x16x32_bf16 v[84:87], a[44:47], a[12:15], v[84:87]\n" \
"ds_read_b64_tr_b16 a[58:59], v215 offset:18944\n" \
"v_mfma_f32_16x16x32_bf16 v[96:99], a[32:35], a[16:19], v[96:99]\n" \
"ds_read_b64_tr_b16 a[60:61], v215 offset:25088\n" \
"v_mfma_f32_16x16x32_bf16 v[96:99], a[36:39], a[20:23], v[96:99]\n" \
"ds_read_b64_tr_b16 a[62:63], v215 offset:27136\n" \
"global_load_lds_dwordx4 v222, s[36:37] offset:2048 sc1\n" \
"v_mfma_f32_16x16x32_bf16 v[100:103], a[40:43], a[16:19], v[100:103]\n" \
"v_add_u32 v213, s74, v211\n" \
"v_add_u32 v214, s74, v212\n" \
"v_mfma_f32_16x16x32_bf16 v[100:103], a[44:47], a[20:23], v[100:103]\n" \
"s_waitcnt lgkmcnt(3)\n" \
"v_xor_b32 v215, 32, v214\n" \
"v_mfma_f32_16x16x32_bf16 v[112:115], a[32:35], a[24:27], v[112:115]\n" \
"v_mfma_f32_16x16x32_bf16 v[112:115], a[36:39], a[28:31], v[112:115]\n" \
"global_load_lds_dwordx4 v222, s[36:37] offset:3072 sc1\n" \
"v_mfma_f32_16x16x32_bf16 v[116:119], a[40:43], a[24:27], v[116:119]\n" \
"s_waitcnt vmcnt(23) lgkmcnt(0)\n" \
"s_add_u32 s36, s36, 0x1000\n" \
"v_mfma_f32_16x16x32_bf16 v[116:119], a[44:47], a[28:31], v[116:119]\n" \
"s_barrier\n" \
"v_mfma_f32_16x16x32_bf16 v[72:75], a[48:51], a[0:3], v[72:75]\n" \
"s_add_u32 m0, m0, 0x5000\n" \
"ds_read_b64_tr_b16 a[32:33], v214 offset:16384\n" \
"v_mfma_f32_16x16x32_bf16 v[72:75], a[52:55], a[4:7], v[72:75]\n" \
"ds_read_b64_tr_b16 a[34:35], v214 offset:18432\n" \
"global_load_lds_dwordx4 v216, s[38:39] offset:-4096\n" \
"v_mfma_f32_16x16x32_bf16 v[76:79], a[56:59], a[0:3], v[76:79]\n" \
"ds_read_b64_tr_b16 a[36:37], v214 offset:24576\n" \
"v_mfma_f32_16x16x32_bf16 v[76:79], a[60:63], a[4:7], v[76:79]\n" \
"ds_read_b128 a[0:3], v213 offset:0\n" \
"v_mfma_f32_16x16x32_bf16 v[88:91], a[48:51], a[8:11], v[88:91]\n" \
"ds_read_b128 a[4:7], v213 offset:2048\n" \
"v_mfma_f32_16x16x32_bf16 v[88:91], a[52:55], a[12:15], v[88:91]\n" \
"ds_read_b64_tr_b16 a[38:39], v214 offset:26624\n" \
"global_load_lds_dwordx4 v217, s[38:39] offset:-3072\n" \
"v_mfma_f32_16x16x32_bf16 v[92:95], a[56:59], a[8:11], v[92:95]\n" \
"buffer_store_dwordx4 v[12:15], v248, s[56:59], s71 offen offset:3072 sc1\n" \
"ds_read_b64_tr_b16 a[40:41], v215 offset:16384\n" \
"v_cvt_pk_bf16_f32 v16, v32, v33\n" \
"v_cvt_pk_bf16_f32 v17, v34, v35\n" \
"v_cvt_pk_bf16_f32 v18, v36, v37\n" \
"v_mfma_f32_16x16x32_bf16 v[92:95], a[60:63], a[12:15], v[92:95]\n" \
"ds_read_b128 a[8:11], v213 offset:256\n" \
"v_cvt_pk_bf16_f32 v19, v38, v39\n" \
"v_mfma_f32_16x16x32_bf16 v[104:107], a[48:51], a[16:19], v[104:107]\n" \
"ds_read_b128 a[12:15], v213 offset:2304\n" \
"s_add_u32 s71, s76, 4096\n" \
"v_mfma_f32_16x16x32_bf16 v[104:107], a[52:55], a[20:23], v[104:107]\n" \
"ds_read_b64_tr_b16 a[42:43], v215 offset:18432\n" \
"global_load_lds_dwordx4 v218, s[38:39] offset:-2048\n" \
"v_mfma_f32_16x16x32_bf16 v[108:111], a[56:59], a[16:19], v[108:111]\n" \
"buffer_store_dwordx4 v[16:19], v248, s[56:59], s71 offen offset:0 sc1\n" \
"ds_read_b64_tr_b16 a[44:45], v215 offset:24576\n" \
"v_cvt_pk_bf16_f32 v20, v40, v41\n" \
"v_cvt_pk_bf16_f32 v21, v42, v43\n" \
"v_cvt_pk_bf16_f32 v22, v44, v45\n" \
"v_mfma_f32_16x16x32_bf16 v[108:111], a[60:63], a[20:23], v[108:111]\n" \
"ds_read_b128 a[16:19], v213 offset:4096\n" \
"v_cvt_pk_bf16_f32 v23, v46, v47\n" \
"v_mfma_f32_16x16x32_bf16 v[120:123], a[48:51], a[24:27], v[120:123]\n" \
"ds_read_b128 a[20:23], v213 offset:6144\n" \
"global_load_lds_dwordx4 v219, s[38:39] offset:-1024\n" \
"v_mfma_f32_16x16x32_bf16 v[120:123], a[52:55], a[28:31], v[120:123]\n" \
"s_add_u32 s38, s38, 0x20000\n" \
"s_addc_u32 s39, s39, 0\n" \
"ds_read_b64_tr_b16 a[46:47], v215 offset:26624\n" \
"v_mfma_f32_16x16x32_bf16 v[124:127], a[56:59], a[24:27], v[124:127]\n" \
"buffer_store_dwordx4 v[20:23], v248, s[56:59], s71 offen offset:1024 sc1\n" \
"s_sub_u32 s75, s75, 32768\n" \
"s_cselect_b32 s75, 131072, s75\n" \
"v_cvt_pk_bf16_f32 v24, v48, v49\n" \
"v_cvt_pk_bf16_f32 v25, v50, v51\n" \
"v_mfma_f32_16x16x32_bf16 v[124:127], a[60:63], a[28:31], v[124:127]\n" \
"s_waitcnt lgkmcnt(3)\n" \
"v_cvt_pk_bf16_f32 v26, v52, v53\n" \
"v_cvt_pk_bf16_f32 v27, v54, v55\n" \
"v_mfma_f32_16x16x32_bf16 v[64:67], a[32:35], a[0:3], v[64:67]\n" \
"ds_read_b128 a[24:27], v213 offset:4352\n" \
"s_add_u32 m0, s75, s95\n" \
"v_mfma_f32_16x16x32_bf16 v[64:67], a[36:39], a[4:7], v[64:67]\n" \
"s_waitcnt lgkmcnt(1)\n" \
"ds_read_b128 a[28:31], v213 offset:6400\n" \
"global_load_lds_dwordx4 v222, s[36:37] offset:0 sc1\n" \
"v_mfma_f32_16x16x32_bf16 v[68:71], a[40:43], a[0:3], v[68:71]\n" \
"s_sub_u32 s74, s74, 32768\n" \
"s_cselect_b32 s74, 131072, s74\n" \
"ds_read_b64_tr_b16 a[48:49], v214 offset:16896\n" \
"v_mfma_f32_16x16x32_bf16 v[68:71], a[44:47], a[4:7], v[68:71]\n" \
"ds_read_b64_tr_b16 a[50:51], v214 offset:18944\n" \
"v_mfma_f32_16x16x32_bf16 v[80:83], a[32:35], a[8:11], v[80:83]\n" \
"ds_read_b64_tr_b16 a[52:53], v214 offset:25088\n" \
"v_mfma_f32_16x16x32_bf16 v[80:83], a[36:39], a[12:15], v[80:83]\n" \
"ds_read_b64_tr_b16 a[54:55], v214 offset:27136\n" \
"global_load_lds_dwordx4 v222, s[36:37] offset:1024 sc1\n" \
"v_mfma_f32_16x16x32_bf16 v[84:87], a[40:43], a[8:11], v[84:87]\n" \
"ds_read_b64_tr_b16 a[56:57], v215 offset:16896\n" \
"v_mfma_f32_16x16x32_bf16 v[84:87], a[44:47], a[12:15], v[84:87]\n" \
"ds_read_b64_tr_b16 a[58:59], v215 offset:18944\n" \
"v_mfma_f32_16x16x32_bf16 v[96:99], a[32:35], a[16:19], v[96:99]\n" \
"ds_read_b64_tr_b16 a[60:61], v215 offset:25088\n" \
"v_mfma_f32_16x16x32_bf16 v[96:99], a[36:39], a[20:23], v[96:99]\n" \
"ds_read_b64_tr_b16 a[62:63], v215 offset:27136\n" \
"global_load_lds_dwordx4 v222, s[36:37] offset:2048 sc1\n" \
"v_mfma_f32_16x16x32_bf16 v[100:103], a[40:43], a[16:19], v[100:103]\n" \
"v_add_u32 v213, s74, v211\n" \
"v_add_u32 v214, s74, v212\n" \
"v_mfma_f32_16x16x32_bf16 v[100:103], a[44:47], a[20:23], v[100:103]\n" \
"s_waitcnt lgkmcnt(3)\n" \
"v_xor_b32 v215, 32, v214\n" \
"v_mfma_f32_16x16x32_bf16 v[112:115], a[32:35], a[24:27], v[112:115]\n" \
"v_mfma_f32_16x16x32_bf16 v[112:115], a[36:39], a[28:31], v[112:115]\n" \
"global_load_lds_dwordx4 v222, s[36:37] offset:3072 sc1\n" \
"v_mfma_f32_16x16x32_bf16 v[116:119], a[40:43], a[24:27], v[116:119]\n" \
"s_waitcnt vmcnt(26) lgkmcnt(0)\n" \
"s_add_u32 s36, s36, 0x1000\n" \
"v_mfma_f32_16x16x32_bf16 v[116:119], a[44:47], a[28:31], v[116:119]\n" \
"s_barrier\n" \
"v_mfma_f32_16x16x32_bf16 v[72:75], a[48:51], a[0:3], v[72:75]\n" \
"s_add_u32 m0, m0, 0x5000\n" \
"ds_read_b64_tr_b16 a[32:33], v214 offset:16384\n" \
"v_mfma_f32_16x16x32_bf16 v[72:75], a[52:55], a[4:7], v[72:75]\n" \
"ds_read_b64_tr_b16 a[34:35], v214 offset:18432\n" \
"global_load_lds_dwordx4 v216, s[38:39] offset:-4096\n" \
"v_mfma_f32_16x16x32_bf16 v[76:79], a[56:59], a[0:3], v[76:79]\n" \
"ds_read_b64_tr_b16 a[36:37], v214 offset:24576\n" \
"v_mfma_f32_16x16x32_bf16 v[76:79], a[60:63], a[4:7], v[76:79]\n" \
"ds_read_b128 a[0:3], v213 offset:0\n" \
"v_mfma_f32_16x16x32_bf16 v[88:91], a[48:51], a[8:11], v[88:91]\n" \
"ds_read_b128 a[4:7], v213 offset:2048\n" \
"v_mfma_f32_16x16x32_bf16 v[88:91], a[52:55], a[12:15], v[88:91]\n" \
"ds_read_b64_tr_b16 a[38:39], v214 offset:26624\n" \
"global_load_lds_dwordx4 v217, s[38:39] offset:-3072\n" \
"v_mfma_f32_16x16x32_bf16 v[92:95], a[56:59], a[8:11], v[92:95]\n" \
"buffer_store_dwordx4 v[24:27], v248, s[56:59], s71 offen offset:2048 sc1\n" \
"ds_read_b64_tr_b16 a[40:41], v215 offset:16384\n" \
"v_cvt_pk_bf16_f32 v28, v56, v57\n" \
"v_cvt_pk_bf16_f32 v29, v58, v59\n" \
"v_cvt_pk_bf16_f32 v30, v60, v61\n" \
"v_mfma_f32_16x16x32_bf16 v[92:95], a[60:63], a[12:15], v[92:95]\n" \
"ds_read_b128 a[8:11], v213 offset:256\n" \
"v_cvt_pk_bf16_f32 v31, v62, v63\n" \
"v_mfma_f32_16x16x32_bf16 v[104:107], a[48:51], a[16:19], v[104:107]\n" \
"ds_read_b128 a[12:15], v213 offset:2304\n" \
"v_mfma_f32_16x16x32_bf16 v[104:107], a[52:55], a[20:23], v[104:107]\n" \
"ds_read_b64_tr_b16 a[42:43], v215 offset:18432\n" \
"global_load_lds_dwordx4 v218, s[38:39] offset:-2048\n" \
"v_mfma_f32_16x16x32_bf16 v[108:111], a[56:59], a[16:19], v[108:111]\n" \
"buffer_store_dwordx4 v[28:31], v248, s[56:59], s71 offen offset:3072 sc1\n" \
"ds_read_b64_tr_b16 a[44:45], v215 offset:24576\n" \
"v_mfma_f32_16x16x32_bf16 v[108:111], a[60:63], a[20:23], v[108:111]\n" \
"ds_read_b128 a[16:19], v213 offset:4096\n" \
"v_mfma_f32_16x16x32_bf16 v[120:123], a[48:51], a[24:27], v[120:123]\n" \
"ds_read_b128 a[20:23], v213 offset:6144\n" \
"global_load_lds_dwordx4 v219, s[38:39] offset:-1024\n" \
"v_mfma_f32_16x16x32_bf16 v[120:123], a[52:55], a[28:31], v[120:123]\n" \
"s_add_u32 s38, s38, 0x20000\n" \
"s_addc_u32 s39, s39, 0\n" \
"ds_read_b64_tr_b16 a[46:47], v215 offset:26624\n" \
"v_mfma_f32_16x16x32_bf16 v[124:127], a[56:59], a[24:27], v[124:127]\n" \
"s_sub_u32 s75, s75, 32768\n" \
"s_cselect_b32 s75, 131072, s75\n" \
"v_mfma_f32_16x16x32_bf16 v[124:127], a[60:63], a[28:31], v[124:127]\n" \
"s_waitcnt lgkmcnt(3)\n" \
"v_mfma_f32_16x16x32_bf16 v[64:67], a[32:35], a[0:3], v[64:67]\n" \
"ds_read_b128 a[24:27], v213 offset:4352\n" \
"s_add_u32 m0, s75, s95\n" \
"v_mfma_f32_16x16x32_bf16 v[64:67], a[36:39], a[4:7], v[64:67]\n" \
"s_waitcnt lgkmcnt(1)\n" \
"ds_read_b128 a[28:31], v213 offset:6400\n" \
"global_load_lds_dwordx4 v222, s[36:37] offset:0 sc1\n" \
"v_mfma_f32_16x16x32_bf16 v[68:71], a[40:43], a[0:3], v[68:71]\n" \
"s_sub_u32 s74, s74, 32768\n" \
"s_cselect_b32 s74, 131072, s74\n" \
"ds_read_b64_tr_b16 a[48:49], v214 offset:16896\n" \
"v_mfma_f32_16x16x32_bf16 v[68:71], a[44:47], a[4:7], v[68:71]\n" \
"ds_read_b64_tr_b16 a[50:51], v214 offset:18944\n" \
"v_mfma_f32_16x16x32_bf16 v[80:83], a[32:35], a[8:11], v[80:83]\n" \
"ds_read_b64_tr_b16 a[52:53], v214 offset:25088\n" \
"v_mfma_f32_16x16x32_bf16 v[80:83], a[36:39], a[12:15], v[80:83]\n" \
"ds_read_b64_tr_b16 a[54:55], v214 offset:27136\n" \
"global_load_lds_dwordx4 v222, s[36:37] offset:1024 sc1\n" \
"v_mfma_f32_16x16x32_bf16 v[84:87], a[40:43], a[8:11], v[84:87]\n" \
"ds_read_b64_tr_b16 a[56:57], v215 offset:16896\n" \
"v_mfma_f32_16x16x32_bf16 v[84:87], a[44:47], a[12:15], v[84:87]\n" \
"ds_read_b64_tr_b16 a[58:59], v215 offset:18944\n" \
"v_mfma_f32_16x16x32_bf16 v[96:99], a[32:35], a[16:19], v[96:99]\n" \
"ds_read_b64_tr_b16 a[60:61], v215 offset:25088\n" \
"v_mfma_f32_16x16x32_bf16 v[96:99], a[36:39], a[20:23], v[96:99]\n" \
"ds_read_b64_tr_b16 a[62:63], v215 offset:27136\n" \
"global_load_lds_dwordx4 v222, s[36:37] offset:2048 sc1\n" \
"v_mfma_f32_16x16x32_bf16 v[100:103], a[40:43], a[16:19], v[100:103]\n" \
"v_add_u32 v213, s74, v211\n" \
"v_add_u32 v214, s74, v212\n" \
"v_mfma_f32_16x16x32_bf16 v[100:103], a[44:47], a[20:23], v[100:103]\n" \
"s_waitcnt lgkmcnt(3)\n" \
"v_xor_b32 v215, 32, v214\n" \
"v_mfma_f32_16x16x32_bf16 v[112:115], a[32:35], a[24:27], v[112:115]\n" \
"v_mfma_f32_16x16x32_bf16 v[112:115], a[36:39], a[28:31], v[112:115]\n" \
"global_load_lds_dwordx4 v222, s[36:37] offset:3072 sc1\n" \
"v_mfma_f32_16x16x32_bf16 v[116:119], a[40:43], a[24:27], v[116:119]\n" \
"s_waitcnt vmcnt(26) lgkmcnt(0)\n" \
"s_add_u32 s36, s36, 0x1000\n" \
"v_mfma_f32_16x16x32_bf16 v[116:119], a[44:47], a[28:31], v[116:119]\n" \
"s_barrier\n" \
"v_mfma_f32_16x16x32_bf16 v[72:75], a[48:51], a[0:3], v[72:75]\n" \
"s_add_u32 m0, m0, 0x5000\n" \
"ds_read_b64_tr_b16 a[32:33], v214 offset:16384\n" \
"v_mfma_f32_16x16x32_bf16 v[72:75], a[52:55], a[4:7], v[72:75]\n" \
"ds_read_b64_tr_b16 a[34:35], v214 offset:18432\n" \
"global_load_lds_dwordx4 v216, s[38:39] offset:-4096\n" \
"v_mfma_f32_16x16x32_bf16 v[76:79], a[56:59], a[0:3], v[76:79]\n" \
"ds_read_b64_tr_b16 a[36:37], v214 offset:24576\n" \
"v_mfma_f32_16x16x32_bf16 v[76:79], a[60:63], a[4:7], v[76:79]\n" \
"ds_read_b128 a[0:3], v213 offset:0\n" \
"v_mfma_f32_16x16x32_bf16 v[88:91], a[48:51], a[8:11], v[88:91]\n" \
"ds_read_b128 a[4:7], v213 offset:2048\n" \
"v_mfma_f32_16x16x32_bf16 v[88:91], a[52:55], a[12:15], v[88:91]\n" \
"ds_read_b64_tr_b16 a[38:39], v214 offset:26624\n" \
"global_load_lds_dwordx4 v217, s[38:39] offset:-3072\n" \
"v_mfma_f32_16x16x32_bf16 v[92:95], a[56:59], a[8:11], v[92:95]\n" \
"ds_read_b64_tr_b16 a[40:41], v215 offset:16384\n" \
"v_mfma_f32_16x16x32_bf16 v[92:95], a[60:63], a[12:15], v[92:95]\n" \
"ds_read_b128 a[8:11], v213 offset:256\n" \
"v_mfma_f32_16x16x32_bf16 v[104:107], a[48:51], a[16:19], v[104:107]\n" \
"ds_read_b128 a[12:15], v213 offset:2304\n" \
"v_mfma_f32_16x16x32_bf16 v[104:107], a[52:55], a[20:23], v[104:107]\n" \
"ds_read_b64_tr_b16 a[42:43], v215 offset:18432\n" \
"global_load_lds_dwordx4 v218, s[38:39] offset:-2048\n" \
"v_mfma_f32_16x16x32_bf16 v[108:111], a[56:59], a[16:19], v[108:111]\n" \
"ds_read_b64_tr_b16 a[44:45], v215 offset:24576\n" \
"v_mfma_f32_16x16x32_bf16 v[108:111], a[60:63], a[20:23], v[108:111]\n" \
"ds_read_b128 a[16:19], v213 offset:4096\n" \
"v_mfma_f32_16x16x32_bf16 v[120:123], a[48:51], a[24:27], v[120:123]\n" \
"ds_read_b128 a[20:23], v213 offset:6144\n" \
"global_load_lds_dwordx4 v219, s[38:39] offset:-1024\n" \
"v_mfma_f32_16x16x32_bf16 v[120:123], a[52:55], a[28:31], v[120:123]\n" \
"s_add_u32 s38, s38, 0x20000\n" \
"s_addc_u32 s39, s39, 0\n" \
"ds_read_b64_tr_b16 a[46:47], v215 offset:26624\n" \
"v_mfma_f32_16x16x32_bf16 v[124:127], a[56:59], a[24:27], v[124:127]\n" \
"s_sub_u32 s75, s75, 32768\n" \
"s_cselect_b32 s75, 131072, s75\n" \
"v_mfma_f32_16x16x32_bf16 v[124:127], a[60:63], a[28:31], v[124:127]\n" \
"s_waitcnt lgkmcnt(3)\n" \
"v_mfma_f32_16x16x32_bf16 v[64:67], a[32:35], a[0:3], v[64:67]\n" \
"ds_read_b128 a[24:27], v213 offset:4352\n" \
"s_add_u32 m0, s75, s95\n" \
"v_mfma_f32_16x16x32_bf16 v[64:67], a[36:39], a[4:7], v[64:67]\n" \
"s_waitcnt lgkmcnt(1)\n" \
"ds_read_b128 a[28:31], v213 offset:6400\n" \
"global_load_lds_dwordx4 v222, s[36:37] offset:0 sc1\n" \
"v_mfma_f32_16x16x32_bf16 v[68:71], a[40:43], a[0:3], v[68:71]\n" \
"s_sub_u32 s74, s74, 32768\n" \
"s_cselect_b32 s74, 131072, s74\n" \
"ds_read_b64_tr_b16 a[48:49], v214 offset:16896\n" \
"v_mfma_f32_16x16x32_bf16 v[68:71], a[44:47], a[4:7], v[68:71]\n" \
"ds_read_b64_tr_b16 a[50:51], v214 offset:18944\n" \
"v_mfma_f32_16x16x32_bf16 v[80:83], a[32:35], a[8:11], v[80:83]\n" \
"ds_read_b64_tr_b16 a[52:53], v214 offset:25088\n" \
"v_mfma_f32_16x16x32_bf16 v[80:83], a[36:39], a[12:15], v[80:83]\n" \
"ds_read_b64_tr_b16 a[54:55], v214 offset:27136\n" \
"global_load_lds_dwordx4 v222, s[36:37] offset:1024 sc1\n" \
"v_mfma_f32_16x16x32_bf16 v[84:87], a[40:43], a[8:11], v[84:87]\n" \
"ds_read_b64_tr_b16 a[56:57], v215 offset:16896\n" \
"v_mfma_f32_16x16x32_bf16 v[84:87], a[44:47], a[12:15], v[84:87]\n" \
"ds_read_b64_tr_b16 a[58:59], v215 offset:18944\n" \
"v_mfma_f32_16x16x32_bf16 v[96:99], a[32:35], a[16:19], v[96:99]\n" \
"ds_read_b64_tr_b16 a[60:61], v215 offset:25088\n" \
"v_mfma_f32_16x16x32_bf16 v[96:99], a[36:39], a[20:23], v[96:99]\n" \
"ds_read_b64_tr_b16 a[62:63], v215 offset:27136\n" \
"global_load_lds_dwordx4 v222, s[36:37] offset:2048 sc1\n" \
"v_mfma_f32_16x16x32_bf16 v[100:103], a[40:43], a[16:19], v[100:103]\n" \
"v_add_u32 v213, s74, v211\n" \
"v_add_u32 v214, s74, v212\n" \
"v_mfma_f32_16x16x32_bf16 v[100:103], a[44:47], a[20:23], v[100:103]\n" \
"s_waitcnt lgkmcnt(3)\n" \
"v_xor_b32 v215, 32, v214\n" \
"v_mfma_f32_16x16x32_bf16 v[112:115], a[32:35], a[24:27], v[112:115]\n" \
"v_mfma_f32_16x16x32_bf16 v[112:115], a[36:39], a[28:31], v[112:115]\n" \
"global_load_lds_dwordx4 v222, s[36:37] offset:3072 sc1\n" \
"v_mfma_f32_16x16x32_bf16 v[116:119], a[40:43], a[24:27], v[116:119]\n" \
"s_waitcnt vmcnt(23) lgkmcnt(0)\n" \
"s_add_u32 s36, s36, 0x1000\n" \
"v_mfma_f32_16x16x32_bf16 v[116:119], a[44:47], a[28:31], v[116:119]\n" \
"s_barrier\n" \
"v_mfma_f32_16x16x32_bf16 v[72:75], a[48:51], a[0:3], v[72:75]\n" \
"s_add_u32 m0, m0, 0x5000\n" \
"ds_read_b64_tr_b16 a[32:33], v214 offset:16384\n" \
"v_mfma_f32_16x16x32_bf16 v[72:75], a[52:55], a[4:7], v[72:75]\n" \
"ds_read_b64_tr_b16 a[34:35], v214 offset:18432\n" \
"global_load_lds_dwordx4 v216, s[38:39] offset:-4096\n" \
"v_mfma_f32_16x16x32_bf16 v[76:79], a[56:59], a[0:3], v[76:79]\n" \
"ds_read_b64_tr_b16 a[36:37], v214 offset:24576\n" \
"v_mfma_f32_16x16x32_bf16 v[76:79], a[60:63], a[4:7], v[76:79]\n" \
"ds_read_b128 a[0:3], v213 offset:0\n" \
"v_mfma_f32_16x16x32_bf16 v[88:91], a[48:51], a[8:11], v[88:91]\n" \
"ds_read_b128 a[4:7], v213 offset:2048\n" \
"v_mfma_f32_16x16x32_bf16 v[88:91], a[52:55], a[12:15], v[88:91]\n" \
"ds_read_b64_tr_b16 a[38:39], v214 offset:26624\n" \
"global_load_lds_dwordx4 v217, s[38:39] offset:-3072\n" \
"v_mfma_f32_16x16x32_bf16 v[92:95], a[56:59], a[8:11], v[92:95]\n" \
"ds_read_b64_tr_b16 a[40:41], v215 offset:16384\n" \
"v_mfma_f32_16x16x32_bf16 v[92:95], a[60:63], a[12:15], v[92:95]\n" \
"ds_read_b128 a[8:11], v213 offset:256\n" \
"v_mfma_f32_16x16x32_bf16 v[104:107], a[48:51], a[16:19], v[104:107]\n" \
"ds_read_b128 a[12:15], v213 offset:2304\n" \
"v_mfma_f32_16x16x32_bf16 v[104:107], a[52:55], a[20:23], v[104:107]\n" \
"ds_read_b64_tr_b16 a[42:43], v215 offset:18432\n" \
"global_load_lds_dwordx4 v218, s[38:39] offset:-2048\n" \
"v_mfma_f32_16x16x32_bf16 v[108:111], a[56:59], a[16:19], v[108:111]\n" \
"ds_read_b64_tr_b16 a[44:45], v215 offset:24576\n" \
"v_mfma_f32_16x16x32_bf16 v[108:111], a[60:63], a[20:23], v[108:111]\n" \
"ds_read_b128 a[16:19], v213 offset:4096\n" \
"v_mfma_f32_16x16x32_bf16 v[120:123], a[48:51], a[24:27], v[120:123]\n" \
"ds_read_b128 a[20:23], v213 offset:6144\n" \
"global_load_lds_dwordx4 v219, s[38:39] offset:-1024\n" \
"v_mfma_f32_16x16x32_bf16 v[120:123], a[52:55], a[28:31], v[120:123]\n" \
"s_add_u32 s38, s38, 0x20000\n" \
"s_addc_u32 s39, s39, 0\n" \
"ds_read_b64_tr_b16 a[46:47], v215 offset:26624\n" \
"v_mfma_f32_16x16x32_bf16 v[124:127], a[56:59], a[24:27], v[124:127]\n" \
"s_sub_u32 s75, s75, 32768\n" \
"s_cselect_b32 s75, 131072, s75\n" \
"v_mfma_f32_16x16x32_bf16 v[124:127], a[60:63], a[28:31], v[124:127]\n" \
"s_waitcnt lgkmcnt(3)\n" \
"v_mfma_f32_16x16x32_bf16 v[64:67], a[32:35], a[0:3], v[64:67]\n" \
"ds_read_b128 a[24:27], v213 offset:4352\n" \
"s_add_u32 m0, s75, s95\n" \
"v_mfma_f32_16x16x32_bf16 v[64:67], a[36:39], a[4:7], v[64:67]\n" \
"s_waitcnt lgkmcnt(1)\n" \
"ds_read_b128 a[28:31], v213 offset:6400\n" \
"global_load_lds_dwordx4 v222, s[36:37] offset:0 sc1\n" \
"v_mfma_f32_16x16x32_bf16 v[68:71], a[40:43], a[0:3], v[68:71]\n" \
"s_sub_u32 s74, s74, 32768\n" \
"s_cselect_b32 s74, 131072, s74\n" \
"ds_read_b64_tr_b16 a[48:49], v214 offset:16896\n" \
"v_mfma_f32_16x16x32_bf16 v[68:71], a[44:47], a[4:7], v[68:71]\n" \
"ds_read_b64_tr_b16 a[50:51], v214 offset:18944\n" \
"v_mfma_f32_16x16x32_bf16 v[80:83], a[32:35], a[8:11], v[80:83]\n" \
"ds_read_b64_tr_b16 a[52:53], v214 offset:25088\n" \
"v_mfma_f32_16x16x32_bf16 v[80:83], a[36:39], a[12:15], v[80:83]\n" \
"ds_read_b64_tr_b16 a[54:55], v214 offset:27136\n" \
"global_load_lds_dwordx4 v222, s[36:37] offset:1024 sc1\n" \
"v_mfma_f32_16x16x32_bf16 v[84:87], a[40:43], a[8:11], v[84:87]\n" \
"ds_read_b64_tr_b16 a[56:57], v215 offset:16896\n" \
"v_mfma_f32_16x16x32_bf16 v[84:87], a[44:47], a[12:15], v[84:87]\n" \
"ds_read_b64_tr_b16 a[58:59], v215 offset:18944\n" \
"v_mfma_f32_16x16x32_bf16 v[96:99], a[32:35], a[16:19], v[96:99]\n" \
"ds_read_b64_tr_b16 a[60:61], v215 offset:25088\n" \
"v_mfma_f32_16x16x32_bf16 v[96:99], a[36:39], a[20:23], v[96:99]\n" \
"ds_read_b64_tr_b16 a[62:63], v215 offset:27136\n" \
"global_load_lds_dwordx4 v222, s[36:37] offset:2048 sc1\n" \
"v_mfma_f32_16x16x32_bf16 v[100:103], a[40:43], a[16:19], v[100:103]\n" \
"v_add_u32 v213, s74, v211\n" \
"v_add_u32 v214, s74, v212\n" \
"v_mfma_f32_16x16x32_bf16 v[100:103], a[44:47], a[20:23], v[100:103]\n" \
"s_waitcnt lgkmcnt(3)\n" \
"v_xor_b32 v215, 32, v214\n" \
"v_mfma_f32_16x16x32_bf16 v[112:115], a[32:35], a[24:27], v[112:115]\n" \
"v_mfma_f32_16x16x32_bf16 v[112:115], a[36:39], a[28:31], v[112:115]\n" \
"global_load_lds_dwordx4 v222, s[36:37] offset:3072 sc1\n" \
"v_mfma_f32_16x16x32_bf16 v[116:119], a[40:43], a[24:27], v[116:119]\n" \
"s_waitcnt vmcnt(20) lgkmcnt(0)\n" \
"s_add_u32 s36, s36, 0x1000\n" \
"v_mfma_f32_16x16x32_bf16 v[116:119], a[44:47], a[28:31], v[116:119]\n" \
"s_barrier\n" \
"v_mfma_f32_16x16x32_bf16 v[72:75], a[48:51], a[0:3], v[72:75]\n" \
"s_add_u32 m0, m0, 0x5000\n" \
"ds_read_b64_tr_b16 a[32:33], v214 offset:16384\n" \
"buffer_store_dword v224, v225, s[56:59], s79 offen sc1\n" \
"v_mfma_f32_16x16x32_bf16 v[72:75], a[52:55], a[4:7], v[72:75]\n" \
"ds_read_b64_tr_b16 a[34:35], v214 offset:18432\n" \
"global_load_lds_dwordx4 v216, s[38:39] offset:-4096\n" \
"v_mfma_f32_16x16x32_bf16 v[76:79], a[56:59], a[0:3], v[76:79]\n" \
"ds_read_b64_tr_b16 a[36:37], v214 offset:24576\n" \
"v_mfma_f32_16x16x32_bf16 v[76:79], a[60:63], a[4:7], v[76:79]\n" \
"ds_read_b128 a[0:3], v213 offset:0\n" \
"v_mfma_f32_16x16x32_bf16 v[88:91], a[48:51], a[8:11], v[88:91]\n" \
"ds_read_b128 a[4:7], v213 offset:2048\n" \
"v_mfma_f32_16x16x32_bf16 v[88:91], a[52:55], a[12:15], v[88:91]\n" \
"ds_read_b64_tr_b16 a[38:39], v214 offset:26624\n" \
"global_load_lds_dwordx4 v217, s[38:39] offset:-3072\n" \
"v_mfma_f32_16x16x32_bf16 v[92:95], a[56:59], a[8:11], v[92:95]\n" \
"ds_read_b64_tr_b16 a[40:41], v215 offset:16384\n" \
"v_mfma_f32_16x16x32_bf16 v[92:95], a[60:63], a[12:15], v[92:95]\n" \
"ds_read_b128 a[8:11], v213 offset:256\n" \
"v_mfma_f32_16x16x32_bf16 v[104:107], a[48:51], a[16:19], v[104:107]\n" \
"ds_read_b128 a[12:15], v213 offset:2304\n" \
"v_mfma_f32_16x16x32_bf16 v[104:107], a[52:55], a[20:23], v[104:107]\n" \
"ds_read_b64_tr_b16 a[42:43], v215 offset:18432\n" \
"global_load_lds_dwordx4 v218, s[38:39] offset:-2048\n" \
"v_mfma_f32_16x16x32_bf16 v[108:111], a[56:59], a[16:19], v[108:111]\n" \
"ds_read_b64_tr_b16 a[44:45], v215 offset:24576\n" \
"v_mfma_f32_16x16x32_bf16 v[108:111], a[60:63], a[20:23], v[108:111]\n" \
"ds_read_b128 a[16:19], v213 offset:4096\n" \
"v_mfma_f32_16x16x32_bf16 v[120:123], a[48:51], a[24:27], v[120:123]\n" \
"ds_read_b128 a[20:23], v213 offset:6144\n" \
"global_load_lds_dwordx4 v219, s[38:39] offset:-1024\n" \
"v_mfma_f32_16x16x32_bf16 v[120:123], a[52:55], a[28:31], v[120:123]\n" \
"s_add_u32 s38, s38, 0x20000\n" \
"s_addc_u32 s39, s39, 0\n" \
"ds_read_b64_tr_b16 a[46:47], v215 offset:26624\n" \
"v_mfma_f32_16x16x32_bf16 v[124:127], a[56:59], a[24:27], v[124:127]\n" \
"s_sub_u32 s75, s75, 32768\n" \
"s_cselect_b32 s75, 131072, s75\n" \
"v_mfma_f32_16x16x32_bf16 v[124:127], a[60:63], a[28:31], v[124:127]\n" \
"s_waitcnt lgkmcnt(3)\n" \
"s_mov_b32 s72, 6\n" \
"s_call_b64 s[68:69], Lo64_%=\n" \
"v_mfma_f32_16x16x32_bf16 v[64:67], a[32:35], a[0:3], v[64:67]\n" \
"ds_read_b128 a[24:27], v213 offset:4352\n" \
"v_mfma_f32_16x16x32_bf16 v[64:67], a[36:39], a[4:7], v[64:67]\n" \
"s_waitcnt lgkmcnt(1)\n" \
"ds_read_b128 a[28:31], v213 offset:6400\n" \
"v_mfma_f32_16x16x32_bf16 v[68:71], a[40:43], a[0:3], v[68:71]\n" \
"s_sub_u32 s74, s74, 32768\n" \
"s_cselect_b32 s74, 131072, s74\n" \
"ds_read_b64_tr_b16 a[48:49], v214 offset:16896\n" \
"v_mfma_f32_16x16x32_bf16 v[68:71], a[44:47], a[4:7], v[68:71]\n" \
"ds_read_b64_tr_b16 a[50:51], v214 offset:18944\n" \
"v_mfma_f32_16x16x32_bf16 v[80:83], a[32:35], a[8:11], v[80:83]\n" \
"ds_read_b64_tr_b16 a[52:53], v214 offset:25088\n" \
"v_mfma_f32_16x16x32_bf16 v[80:83], a[36:39], a[12:15], v[80:83]\n" \
"ds_read_b64_tr_b16 a[54:55], v214 offset:27136\n" \
"v_mfma_f32_16x16x32_bf16 v[84:87], a[40:43], a[8:11], v[84:87]\n" \
"ds_read_b64_tr_b16 a[56:57], v215 offset:16896\n" \
"v_mfma_f32_16x16x32_bf16 v[84:87], a[44:47], a[12:15], v[84:87]\n" \
"ds_read_b64_tr_b16 a[58:59], v215 offset:18944\n" \
"v_mfma_f32_16x16x32_bf16 v[96:99], a[32:35], a[16:19], v[96:99]\n" \
"ds_read_b64_tr_b16 a[60:61], v215 offset:25088\n" \
"v_mfma_f32_16x16x32_bf16 v[96:99], a[36:39], a[20:23], v[96:99]\n" \
"ds_read_b64_tr_b16 a[62:63], v215 offset:27136\n" \
"v_mfma_f32_16x16x32_bf16 v[100:103], a[40:43], a[16:19], v[100:103]\n" \
"v_add_u32 v213, s74, v211\n" \
"v_add_u32 v214, s74, v212\n" \
"v_mfma_f32_16x16x32_bf16 v[100:103], a[44:47], a[20:23], v[100:103]\n" \
"s_waitcnt lgkmcnt(3)\n" \
"v_xor_b32 v215, 32, v214\n" \
"v_mfma_f32_16x16x32_bf16 v[112:115], a[32:35], a[24:27], v[112:115]\n" \
"v_mfma_f32_16x16x32_bf16 v[112:115], a[36:39], a[28:31], v[112:115]\n" \
"v_mfma_f32_16x16x32_bf16 v[116:119], a[40:43], a[24:27], v[116:119]\n" \
"s_waitcnt vmcnt(16) lgkmcnt(0)\n" \
"v_mfma_f32_16x16x32_bf16 v[116:119], a[44:47], a[28:31], v[116:119]\n" \
"s_barrier\n" \
"v_mfma_f32_16x16x32_bf16 v[72:75], a[48:51], a[0:3], v[72:75]\n" \
"Lpg13315_%=:\n" \
"s_cbranch_execz Lcb1_%=\n" \
"s_lshl_b32 s83, s95, 1\n" \
"s_add_u32 s83, s83, s74\n" \
"ds_read_b64_tr_b16 a[32:33], v214 offset:16384\n" \
"v_mfma_f32_16x16x32_bf16 v[72:75], a[52:55], a[4:7], v[72:75]\n" \
"ds_read_b64_tr_b16 a[34:35], v214 offset:18432\n" \
"v_mfma_f32_16x16x32_bf16 v[76:79], a[56:59], a[0:3], v[76:79]\n" \
"ds_read_b64_tr_b16 a[36:37], v214 offset:24576\n" \
"v_mfma_f32_16x16x32_bf16 v[76:79], a[60:63], a[4:7], v[76:79]\n" \
"ds_read_b128 a[0:3], v213 offset:0\n" \
"v_mfma_f32_16x16x32_bf16 v[88:91], a[48:51], a[8:11], v[88:91]\n" \
"ds_read_b128 a[4:7], v213 offset:2048\n" \
"v_mfma_f32_16x16x32_bf16 v[88:91], a[52:55], a[12:15], v[88:91]\n" \
"ds_read_b64_tr_b16 a[38:39], v214 offset:26624\n" \
"v_mfma_f32_16x16x32_bf16 v[92:95], a[56:59], a[8:11], v[92:95]\n" \
"ds_read_b64_tr_b16 a[40:41], v215 offset:16384\n" \
"v_mfma_f32_16x16x32_bf16 v[92:95], a[60:63], a[12:15], v[92:95]\n" \
"ds_read_b128 a[8:11], v213 offset:256\n" \
"v_mfma_f32_16x16x32_bf16 v[104:107], a[48:51], a[16:19], v[104:107]\n" \
"ds_read_b128 a[12:15], v213 offset:2304\n" \
"v_mfma_f32_16x16x32_bf16 v[104:107], a[52:55], a[20:23], v[104:107]\n" \
"ds_read_b64_tr_b16 a[42:43], v215 offset:18432\n" \
"v_mfma_f32_16x16x32_bf16 v[108:111], a[56:59], a[16:19], v[108:111]\n" \
"ds_read_b64_tr_b16 a[44:45], v215 offset:24576\n" \
"v_mfma_f32_16x16x32_bf16 v[108:111], a[60:63], a[20:23], v[108:111]\n" \
"ds_read_b128 a[16:19], v213 offset:4096\n" \
"v_mfma_f32_16x16x32_bf16 v[120:123], a[48:51], a[24:27], v[120:123]\n" \
"ds_read_b128 a[20:23], v213 offset:6144\n" \
"v_mfma_f32_16x16x32_bf16 v[120:123], a[52:55], a[28:31], v[120:123]\n" \
"ds_read_b64_tr_b16 a[46:47], v215 offset:26624\n" \
"v_mfma_f32_16x16x32_bf16 v[124:127], a[56:59], a[24:27], v[124:127]\n" \
"v_mfma_f32_16x16x32_bf16 v[124:127], a[60:63], a[28:31], v[124:127]\n" \
"s_waitcnt lgkmcnt(3)\n" \
"v_mfma_f32_16x16x32_bf16 v[64:67], a[32:35], a[0:3], v[64:67]\n" \
"ds_read_b128 a[24:27], v213 offset:4352\n" \
"v_mfma_f32_16x16x32_bf16 v[64:67], a[36:39], a[4:7], v[64:67]\n" \
"s_waitcnt lgkmcnt(1)\n" \
"ds_read_b128 a[28:31], v213 offset:6400\n" \
"v_lshrrev_b32 v132, 4, v248\n" \
"v_mfma_f32_16x16x32_bf16 v[68:71], a[40:43], a[0:3], v[68:71]\n" \
"s_sub_u32 s74, s74, 32768\n" \
"s_cselect_b32 s74, 131072, s74\n" \
"ds_read_b64_tr_b16 a[48:49], v214 offset:16896\n" \
"v_mfma_f32_16x16x32_bf16 v[68:71], a[44:47], a[4:7], v[68:71]\n" \
"ds_read_b64_tr_b16 a[50:51], v214 offset:18944\n" \
"v_and_b32 v133, 15, v132\n" \
"v_mfma_f32_16x16x32_bf16 v[80:83], a[32:35], a[8:11], v[80:83]\n" \
"ds_read_b64_tr_b16 a[52:53], v214 offset:25088\n" \
"v_mul_u32_u24 v128, 144, v133\n" \
"v_mfma_f32_16x16x32_bf16 v[80:83], a[36:39], a[12:15], v[80:83]\n" \
"ds_read_b64_tr_b16 a[54:55], v214 offset:27136\n" \
"v_lshrrev_b32 v133, 4, v132\n" \
"v_mfma_f32_16x16x32_bf16 v[84:87], a[40:43], a[8:11], v[84:87]\n" \
"ds_read_b64_tr_b16 a[56:57], v215 offset:16896\n" \
"v_lshl_add_u32 v128, v133, 3, v128\n" \
"v_mfma_f32_16x16x32_bf16 v[84:87], a[44:47], a[12:15], v[84:87]\n" \
"ds_read_b64_tr_b16 a[58:59], v215 offset:18944\n" \
"v_add_u32 v128, s83, v128\n" \
"v_mfma_f32_16x16x32_bf16 v[96:99], a[32:35], a[16:19], v[96:99]\n" \
"ds_read_b64_tr_b16 a[60:61], v215 offset:25088\n" \
"v_lshrrev_b32 v133, 3, v132\n" \
"v_mfma_f32_16x16x32_bf16 v[96:99], a[36:39], a[20:23], v[96:99]\n" \
"ds_read_b64_tr_b16 a[62:63], v215 offset:27136\n" \
"v_and_b32 v134, 7, v132\n" \
"v_mfma_f32_16x16x32_bf16 v[100:103], a[40:43], a[16:19], v[100:103]\n" \
"v_add_u32 v213, s74, v211\n" \
"v_add_u32 v214, s74, v212\n" \
"v_mfma_f32_16x16x32_bf16 v[100:103], a[44:47], a[20:23], v[100:103]\n" \
"s_waitcnt lgkmcnt(3)\n" \
"v_xor_b32 v215, 32, v214\n" \
"v_mul_u32_u24 v129, 144, v133\n" \
"v_mfma_f32_16x16x32_bf16 v[112:115], a[32:35], a[24:27], v[112:115]\n" \
"v_lshl_add_u32 v129, v134, 4, v129\n" \
"v_add_u32 v129, s83, v129\n" \
"v_mfma_f32_16x16x32_bf16 v[112:115], a[36:39], a[28:31], v[112:115]\n" \
"v_lshlrev_b32 v134, 4, v134\n" \
"v_lshl_add_u32 v130, v133, 13, v134\n" \
"v_mfma_f32_16x16x32_bf16 v[116:119], a[40:43], a[24:27], v[116:119]\n" \
"s_waitcnt vmcnt(0) lgkmcnt(0)\n" \
"v_mfma_f32_16x16x32_bf16 v[116:119], a[44:47], a[28:31], v[116:119]\n" \
"s_barrier\n" \
"v_mfma_f32_16x16x32_bf16 v[72:75], a[48:51], a[0:3], v[72:75]\n" \
"ds_read_b128 a[192:195], v213 offset:0\n" \
"ds_read_b64_tr_b16 a[128:129], v214 offset:16384\n" \
"v_mfma_f32_16x16x32_bf16 v[72:75], a[52:55], a[4:7], v[72:75]\n" \
"s_sub_u32 s74, s74, 32768\n" \
"s_cselect_b32 s74, 131072, s74\n" \
"ds_read_b64_tr_b16 a[130:131], v214 offset:18432\n" \
"v_mfma_f32_16x16x32_bf16 v[76:79], a[56:59], a[0:3], v[76:79]\n" \
"ds_read_b128 a[196:199], v213 offset:2048\n" \
"ds_read_b64_tr_b16 a[132:133], v214 offset:24576\n" \
"v_mfma_f32_16x16x32_bf16 v[76:79], a[60:63], a[4:7], v[76:79]\n" \
"ds_read_b64_tr_b16 a[134:135], v214 offset:26624\n" \
"v_add_u32 v226, s74, v211\n" \
"v_mfma_f32_16x16x32_bf16 v[88:91], a[48:51], a[8:11], v[88:91]\n" \
"ds_read_b128 a[200:203], v213 offset:256\n" \
"ds_read_b64_tr_b16 a[136:137], v215 offset:16384\n" \
"v_mfma_f32_16x16x32_bf16 v[88:91], a[52:55], a[12:15], v[88:91]\n" \
"ds_read_b64_tr_b16 a[138:139], v215 offset:18432\n" \
"v_add_u32 v227, s74, v212\n" \
"v_mfma_f32_16x16x32_bf16 v[92:95], a[56:59], a[8:11], v[92:95]\n" \
"ds_read_b128 a[204:207], v213 offset:2304\n" \
"ds_read_b64_tr_b16 a[140:141], v215 offset:24576\n" \
"v_mfma_f32_16x16x32_bf16 v[92:95], a[60:63], a[12:15], v[92:95]\n" \
"ds_read_b64_tr_b16 a[142:143], v215 offset:26624\n" \
"v_xor_b32 v228, 32, v227\n" \
"v_mfma_f32_16x16x32_bf16 v[104:107], a[48:51], a[16:19], v[104:107]\n" \
"ds_read_b64_tr_b16 a[144:145], v214 offset:16896\n" \
"v_mfma_f32_16x16x32_bf16 v[104:107], a[52:55], a[20:23], v[104:107]\n" \
"ds_read_b64_tr_b16 a[146:147], v214 offset:18944\n" \
"v_mfma_f32_16x16x32_bf16 v[108:111], a[56:59], a[16:19], v[108:111]\n" \
"ds_read_b64_tr_b16 a[148:149], v214 offset:25088\n" \
"v_mfma_f32_16x16x32_bf16 v[108:111], a[60:63], a[20:23], v[108:111]\n" \
"ds_read_b64_tr_b16 a[150:151], v214 offset:27136\n" \
"v_mfma_f32_16x16x32_bf16 v[120:123], a[48:51], a[24:27], v[120:123]\n" \
"ds_read_b64_tr_b16 a[152:153], v215 offset:16896\n" \
"v_mfma_f32_16x16x32_bf16 v[120:123], a[52:55], a[28:31], v[120:123]\n" \
"ds_read_b64_tr_b16 a[154:155], v215 offset:18944\n" \
"v_mfma_f32_16x16x32_bf16 v[124:127], a[56:59], a[24:27], v[124:127]\n" \
"ds_read_b64_tr_b16 a[156:157], v215 offset:25088\n" \
"v_mfma_f32_16x16x32_bf16 v[124:127], a[60:63], a[28:31], v[124:127]\n" \
"s_waitcnt lgkmcnt(4)\n" \
"ds_read_b64_tr_b16 a[158:159], v215 offset:27136\n" \
"v_mfma_f32_16x16x32_bf16 v[64:67], a[128:131], a[192:195], v[64:67]\n" \
"ds_read_b128 a[224:227], v226 offset:0\n" \
"ds_read_b64_tr_b16 a[160:161], v227 offset:16384\n" \
"v_mfma_f32_16x16x32_bf16 v[64:67], a[132:135], a[196:199], v[64:67]\n" \
"ds_read_b64_tr_b16 a[162:163], v227 offset:18432\n" \
"v_mfma_f32_16x16x32_bf16 v[80:83], a[128:131], a[200:203], v[80:83]\n" \
"ds_read_b64_tr_b16 a[164:165], v227 offset:24576\n" \
"v_mfma_f32_16x16x32_bf16 v[80:83], a[132:135], a[204:207], v[80:83]\n" \
"ds_read_b128 a[228:231], v226 offset:2048\n" \
"ds_read_b64_tr_b16 a[166:167], v227 offset:26624\n" \
"v_mfma_f32_16x16x32_bf16 v[68:71], a[136:139], a[192:195], v[68:71]\n" \
"ds_read_b64_tr_b16 a[168:169], v228 offset:16384\n" \
"v_mfma_f32_16x16x32_bf16 v[68:71], a[140:143], a[196:199], v[68:71]\n" \
"ds_read_b64_tr_b16 a[170:171], v228 offset:18432\n" \
"v_mfma_f32_16x16x32_bf16 v[84:87], a[136:139], a[200:203], v[84:87]\n" \
"ds_read_b128 a[232:235], v226 offset:256\n" \
"ds_read_b64_tr_b16 a[172:173], v228 offset:24576\n" \
"v_mfma_f32_16x16x32_bf16 v[84:87], a[140:143], a[204:207], v[84:87]\n" \
"s_waitcnt lgkmcnt(6)\n" \
"ds_read_b64_tr_b16 a[174:175], v228 offset:26624\n" \
"v_mfma_f32_16x16x32_bf16 v[72:75], a[144:147], a[192:195], v[72:75]\n" \
"ds_read_b64_tr_b16 a[176:177], v227 offset:16896\n" \
"v_mfma_f32_16x16x32_bf16 v[72:75], a[148:151], a[196:199], v[72:75]\n" \
"ds_read_b128 a[236:239], v226 offset:2304\n" \
"ds_read_b64_tr_b16 a[178:179], v227 offset:18944\n" \
"v_mfma_f32_16x16x32_bf16 v[88:91], a[144:147], a[200:203], v[88:91]\n" \
"ds_read_b64_tr_b16 a[180:181], v227 offset:25088\n" \
"v_mfma_f32_16x16x32_bf16 v[88:91], a[148:151], a[204:207], v[88:91]\n" \
"ds_read_b64_tr_b16 a[182:183], v227 offset:27136\n" \
"v_mfma_f32_16x16x32_bf16 v[76:79], a[152:155], a[192:195], v[76:79]\n" \
"ds_read_b64_tr_b16 a[184:185], v228 offset:16896\n" \
"v_mfma_f32_16x16x32_bf16 v[76:79], a[156:159], a[196:199], v[76:79]\n" \
"ds_read_b64_tr_b16 a[186:187], v228 offset:18944\n" \
"v_mfma_f32_16x16x32_bf16 v[92:95], a[152:155], a[200:203], v[92:95]\n" \
"ds_read_b64_tr_b16 a[188:189], v228 offset:25088\n" \
"v_mfma_f32_16x16x32_bf16 v[92:95], a[156:159], a[204:207], v[92:95]\n" \
"s_waitcnt lgkmcnt(4)\n" \
"ds_read_b64_tr_b16 a[190:191], v228 offset:27136\n" \
"v_mfma_f32_16x16x32_bf16 v[64:67], a[160:163], a[224:227], v[64:67]\n" \
"ds_read_b128 a[208:211], v213 offset:4096\n" \
"v_mfma_f32_16x16x32_bf16 v[64:67], a[164:167], a[228:231], v[64:67]\n" \
"ds_read_b128 a[212:215], v213 offset:6144\n" \
"v_mfma_f32_16x16x32_bf16 v[80:83], a[160:163], a[232:235], v[80:83]\n" \
"ds_read_b128 a[240:243], v226 offset:4096\n" \
"v_mfma_f32_16x16x32_bf16 v[80:83], a[164:167], a[236:239], v[80:83]\n" \
"ds_read_b128 a[244:247], v226 offset:6144\n" \
"v_mfma_f32_16x16x32_bf16 v[68:71], a[168:171], a[224:227], v[68:71]\n" \
"ds_read_b128 a[216:219], v213 offset:4352\n" \
"v_cvt_pk_bf16_f32 v64, v64, v65\n" \
"v_mfma_f32_16x16x32_bf16 v[68:71], a[172:175], a[228:231], v[68:71]\n" \
"ds_read_b128 a[220:223], v213 offset:6400\n" \
"v_cvt_pk_bf16_f32 v65, v66, v67\n" \
"v_mfma_f32_16x16x32_bf16 v[84:87], a[168:171], a[232:235], v[84:87]\n" \
"ds_read_b128 a[248:251], v226 offset:4352\n" \
"v_mfma_f32_16x16x32_bf16 v[84:87], a[172:175], a[236:239], v[84:87]\n" \
"s_waitcnt lgkmcnt(4)\n" \
"ds_read_b128 a[252:255], v226 offset:6400\n" \
"v_mfma_f32_16x16x32_bf16 v[72:75], a[176:179], a[224:227], v[72:75]\n" \
"ds_write_b64 v128, v[64:65] offset:0\n" \
"v_mfma_f32_16x16x32_bf16 v[72:75], a[180:183], a[228:231], v[72:75]\n" \
"v_cvt_pk_bf16_f32 v80, v80, v81\n" \
"v_cvt_pk_bf16_f32 v81, v82, v83\n" \
"v_mfma_f32_16x16x32_bf16 v[88:91], a[176:179], a[232:235], v[88:91]\n" \
"ds_write_b64 v128, v[80:81] offset:2304\n" \
"v_mfma_f32_16x16x32_bf16 v[88:91], a[180:183], a[236:239], v[88:91]\n" \
"v_cvt_pk_bf16_f32 v68, v68, v69\n" \
"v_cvt_pk_bf16_f32 v69, v70, v71\n" \
"v_mfma_f32_16x16x32_bf16 v[76:79], a[184:187], a[224:227], v[76:79]\n" \
"ds_write_b64 v128, v[68:69] offset:32\n" \
"v_mfma_f32_16x16x32_bf16 v[76:79], a[188:191], a[228:231], v[76:79]\n" \
"v_cvt_pk_bf16_f32 v84, v84, v85\n" \
"v_cvt_pk_bf16_f32 v85, v86, v87\n" \
"v_mfma_f32_16x16x32_bf16 v[92:95], a[184:187], a[232:235], v[92:95]\n" \
"ds_write_b64 v128, v[84:85] offset:2336\n" \
"v_mfma_f32_16x16x32_bf16 v[92:95], a[188:191], a[236:239], v[92:95]\n" \
"v_cvt_pk_bf16_f32 v72, v72, v73\n" \
"v_cvt_pk_bf16_f32 v73, v74, v75\n" \
"v_mfma_f32_16x16x32_bf16 v[96:99], a[128:131], a[208:211], v[96:99]\n" \
"ds_write_b64 v128, v[72:73] offset:64\n" \
"v_mfma_f32_16x16x32_bf16 v[96:99], a[132:135], a[212:215], v[96:99]\n" \
"s_waitcnt lgkmcnt(2)\n" \
"v_cvt_pk_bf16_f32 v88, v88, v89\n" \
"v_cvt_pk_bf16_f32 v89, v90, v91\n" \
"v_mfma_f32_16x16x32_bf16 v[96:99], a[160:163], a[240:243], v[96:99]\n" \
"ds_write_b64 v128, v[88:89] offset:2368\n" \
"v_mfma_f32_16x16x32_bf16 v[96:99], a[164:167], a[244:247], v[96:99]\n" \
"v_cvt_pk_bf16_f32 v76, v76, v77\n" \
"v_cvt_pk_bf16_f32 v77, v78, v79\n" \
"v_mfma_f32_16x16x32_bf16 v[100:103], a[136:139], a[208:211], v[100:103]\n" \
"ds_write_b64 v128, v[76:77] offset:96\n" \
"v_mfma_f32_16x16x32_bf16 v[100:103], a[140:143], a[212:215], v[100:103]\n" \
"v_cvt_pk_bf16_f32 v92, v92, v93\n" \
"v_cvt_pk_bf16_f32 v93, v94, v95\n" \
"v_mfma_f32_16x16x32_bf16 v[100:103], a[168:171], a[240:243], v[100:103]\n" \
"ds_write_b64 v128, v[92:93] offset:2400\n" \
"v_mfma_f32_16x16x32_bf16 v[100:103], a[172:175], a[244:247], v[100:103]\n" \
"ds_read_b128 v[140:143], v129 offset:0\n" \
"v_mfma_f32_16x16x32_bf16 v[104:107], a[144:147], a[208:211], v[104:107]\n" \
"ds_read_b128 v[144:147], v129 offset:1152\n" \
"v_mfma_f32_16x16x32_bf16 v[104:107], a[148:151], a[212:215], v[104:107]\n" \
"ds_read_b128 v[148:151], v129 offset:2304\n" \
"v_mfma_f32_16x16x32_bf16 v[104:107], a[176:179], a[240:243], v[104:107]\n" \
"ds_read_b128 v[152:155], v129 offset:3456\n" \
"v_mfma_f32_16x16x32_bf16 v[104:107], a[180:183], a[244:247], v[104:107]\n" \
"v_cvt_pk_bf16_f32 v96, v96, v97\n" \
"v_cvt_pk_bf16_f32 v97, v98, v99\n" \
"v_mfma_f32_16x16x32_bf16 v[108:111], a[152:155], a[208:211], v[108:111]\n" \
"ds_write_b64 v128, v[96:97] offset:0\n" \
"v_mfma_f32_16x16x32_bf16 v[108:111], a[156:159], a[212:215], v[108:111]\n" \
"v_cvt_pk_bf16_f32 v100, v100, v101\n" \
"v_cvt_pk_bf16_f32 v101, v102, v103\n" \
"v_mfma_f32_16x16x32_bf16 v[108:111], a[184:187], a[240:243], v[108:111]\n" \
"s_add_u32 s70, s77, 0\n" \
"ds_write_b64 v128, v[100:101] offset:32\n" \
"v_mfma_f32_16x16x32_bf16 v[108:111], a[188:191], a[244:247], v[108:111]\n" \
"s_waitcnt lgkmcnt(5)\n" \
"v_cvt_pk_bf16_f32 v104, v104, v105\n" \
"v_cvt_pk_bf16_f32 v105, v106, v107\n" \
"v_mfma_f32_16x16x32_bf16 v[112:115], a[128:131], a[216:219], v[112:115]\n" \
"buffer_store_dwordx4 v[140:143], v130, s[52:55], s70 offen sc1\n" \
"ds_write_b64 v128, v[104:105] offset:64\n" \
"v_mfma_f32_16x16x32_bf16 v[112:115], a[132:135], a[220:223], v[112:115]\n" \
"v_mfma_f32_16x16x32_bf16 v[112:115], a[160:163], a[248:251], v[112:115]\n" \
"v_cvt_pk_bf16_f32 v108, v108, v109\n" \
"v_cvt_pk_bf16_f32 v109, v110, v111\n" \
"v_mfma_f32_16x16x32_bf16 v[112:115], a[164:167], a[252:255], v[112:115]\n" \
"ds_write_b64 v128, v[108:109] offset:96\n" \
"v_mfma_f32_16x16x32_bf16 v[116:119], a[136:139], a[216:219], v[116:119]\n" \
"s_add_u32 s70, s77, 65536\n" \
"ds_read_b128 v[156:159], v129 offset:0\n" \
"s_waitcnt lgkmcnt(7)\n" \
"v_mfma_f32_16x16x32_bf16 v[116:119], a[140:143], a[220:223], v[116:119]\n" \
"buffer_store_dwordx4 v[144:147], v130, s[52:55], s70 offen sc1\n" \
"ds_read_b128 v[160:163], v129 offset:1152\n" \
"v_mfma_f32_16x16x32_bf16 v[116:119], a[168:171], a[248:251], v[116:119]\n" \
"v_cvt_pk_bf16_f32 v112, v112, v113\n" \
"v_cvt_pk_bf16_f32 v113, v114, v115\n" \
"v_mfma_f32_16x16x32_bf16 v[116:119], a[172:175], a[252:255], v[116:119]\n" \
"ds_write_b64 v128, v[112:113] offset:2304\n" \
"v_mfma_f32_16x16x32_bf16 v[120:123], a[144:147], a[216:219], v[120:123]\n" \
"v_mfma_f32_16x16x32_bf16 v[120:123], a[148:151], a[220:223], v[120:123]\n" \
"s_add_u32 s70, s77, 4096\n" \
"s_waitcnt lgkmcnt(8)\n" \
"v_mfma_f32_16x16x32_bf16 v[120:123], a[176:179], a[248:251], v[120:123]\n" \
"buffer_store_dwordx4 v[148:151], v130, s[52:55], s70 offen sc1\n" \
"v_cvt_pk_bf16_f32 v116, v116, v117\n" \
"v_cvt_pk_bf16_f32 v117, v118, v119\n" \
"v_mfma_f32_16x16x32_bf16 v[120:123], a[180:183], a[252:255], v[120:123]\n" \
"ds_write_b64 v128, v[116:117] offset:2336\n" \
"v_mfma_f32_16x16x32_bf16 v[124:127], a[152:155], a[216:219], v[124:127]\n" \
"v_mfma_f32_16x16x32_bf16 v[124:127], a[156:159], a[220:223], v[124:127]\n" \
"v_mfma_f32_16x16x32_bf16 v[124:127], a[184:187], a[248:251], v[124:127]\n" \
"s_add_u32 s70, s77, 69632\n" \
"v_cvt_pk_bf16_f32 v120, v120, v121\n" \
"v_cvt_pk_bf16_f32 v121, v122, v123\n" \
"v_mfma_f32_16x16x32_bf16 v[124:127], a[188:191], a[252:255], v[124:127]\n" \
"s_waitcnt lgkmcnt(8)\n" \
"ds_write_b64 v128, v[120:121] offset:2368\n" \
"buffer_store_dwordx4 v[152:155], v130, s[52:55], s70 offen sc1\n" \
"s_add_u32 s70, s77, 131072\n" \
"s_waitcnt lgkmcnt(4)\n" \
"s_nop 2\n" \
"v_cvt_pk_bf16_f32 v124, v124, v125\n" \
"v_cvt_pk_bf16_f32 v125, v126, v127\n" \
"ds_write_b64 v128, v[124:125] offset:2400\n" \
"ds_read_b128 v[164:167], v129 offset:2304\n" \
"ds_read_b128 v[168:171], v129 offset:3456\n" \
"buffer_store_dwordx4 v[156:159], v130, s[52:55], s70 offen sc1\n" \
"s_add_u32 s70, s77, 196608\n" \
"s_waitcnt lgkmcnt(6)\n" \
"buffer_store_dwordx4 v[160:163], v130, s[52:55], s70 offen sc1\n" \
"s_add_u32 s70, s77, 135168\n" \
"s_waitcnt lgkmcnt(1)\n" \
"buffer_store_dwordx4 v[164:167], v130, s[52:55], s70 offen sc1\n" \
"s_add_u32 s70, s77, 200704\n" \
"s_waitcnt lgkmcnt(0)\n" \
"buffer_store_dwordx4 v[168:171], v130, s[52:55], s70 offen sc1\n" \
"s_endpgm\n" \
"Lpg13616_%=:\n" \
"s_cbranch_execz Lcb1_%=\n" \
"Lo64_%=:\n" \
"v_mfma_f32_16x16x32_bf16 v[64:67], a[32:35], a[0:3], v[64:67]\n" \
"ds_read_b128 a[24:27], v213 offset:4352\n" \
"s_add_u32 m0, s75, s95\n" \
"v_mfma_f32_16x16x32_bf16 v[64:67], a[36:39], a[4:7], v[64:67]\n" \
"s_waitcnt lgkmcnt(1)\n" \
"ds_read_b128 a[28:31], v213 offset:6400\n" \
"global_load_lds_dwordx4 v222, s[36:37] offset:0 sc1\n" \
"v_mfma_f32_16x16x32_bf16 v[68:71], a[40:43], a[0:3], v[68:71]\n" \
"s_sub_u32 s74, s74, 32768\n" \
"s_cselect_b32 s74, 131072, s74\n" \
"ds_read_b64_tr_b16 a[48:49], v214 offset:16896\n" \
"v_mfma_f32_16x16x32_bf16 v[68:71], a[44:47], a[4:7], v[68:71]\n" \
"ds_read_b64_tr_b16 a[50:51], v214 offset:18944\n" \
"v_mfma_f32_16x16x32_bf16 v[80:83], a[32:35], a[8:11], v[80:83]\n" \
"ds_read_b64_tr_b16 a[52:53], v214 offset:25088\n" \
"v_mfma_f32_16x16x32_bf16 v[80:83], a[36:39], a[12:15], v[80:83]\n" \
"ds_read_b64_tr_b16 a[54:55], v214 offset:27136\n" \
"global_load_lds_dwordx4 v222, s[36:37] offset:1024 sc1\n" \
"v_mfma_f32_16x16x32_bf16 v[84:87], a[40:43], a[8:11], v[84:87]\n" \
"ds_read_b64_tr_b16 a[56:57], v215 offset:16896\n" \
"v_mfma_f32_16x16x32_bf16 v[84:87], a[44:47], a[12:15], v[84:87]\n" \
"ds_read_b64_tr_b16 a[58:59], v215 offset:18944\n" \
"v_mfma_f32_16x16x32_bf16 v[96:99], a[32:35], a[16:19], v[96:99]\n" \
"ds_read_b64_tr_b16 a[60:61], v215 offset:25088\n" \
"v_mfma_f32_16x16x32_bf16 v[96:99], a[36:39], a[20:23], v[96:99]\n" \
"ds_read_b64_tr_b16 a[62:63], v215 offset:27136\n" \
"global_load_lds_dwordx4 v222, s[36:37] offset:2048 sc1\n" \
"v_mfma_f32_16x16x32_bf16 v[100:103], a[40:43], a[16:19], v[100:103]\n" \
"v_add_u32 v213, s74, v211\n" \
"v_add_u32 v214, s74, v212\n" \
"v_mfma_f32_16x16x32_bf16 v[100:103], a[44:47], a[20:23], v[100:103]\n" \
"s_waitcnt lgkmcnt(3)\n" \
"v_xor_b32 v215, 32, v214\n" \
"v_mfma_f32_16x16x32_bf16 v[112:115], a[32:35], a[24:27], v[112:115]\n" \
"v_mfma_f32_16x16x32_bf16 v[112:115], a[36:39], a[28:31], v[112:115]\n" \
"global_load_lds_dwordx4 v222, s[36:37] offset:3072 sc1\n" \
"v_mfma_f32_16x16x32_bf16 v[116:119], a[40:43], a[24:27], v[116:119]\n" \
"s_waitcnt vmcnt(20) lgkmcnt(0)\n" \
"s_add_u32 s36, s36, 0x1000\n" \
"v_mfma_f32_16x16x32_bf16 v[116:119], a[44:47], a[28:31], v[116:119]\n" \
"s_barrier\n" \
"v_mfma_f32_16x16x32_bf16 v[72:75], a[48:51], a[0:3], v[72:75]\n" \
"s_add_u32 m0, m0, 0x5000\n" \
"ds_read_b64_tr_b16 a[32:33], v214 offset:16384\n" \
"v_mfma_f32_16x16x32_bf16 v[72:75], a[52:55], a[4:7], v[72:75]\n" \
"ds_read_b64_tr_b16 a[34:35], v214 offset:18432\n" \
"global_load_lds_dwordx4 v216, s[38:39] offset:-4096\n" \
"v_mfma_f32_16x16x32_bf16 v[76:79], a[56:59], a[0:3], v[76:79]\n" \
"ds_read_b64_tr_b16 a[36:37], v214 offset:24576\n" \
"v_mfma_f32_16x16x32_bf16 v[76:79], a[60:63], a[4:7], v[76:79]\n" \
"ds_read_b128 a[0:3], v213 offset:0\n" \
"v_mfma_f32_16x16x32_bf16 v[88:91], a[48:51], a[8:11], v[88:91]\n" \
"ds_read_b128 a[4:7], v213 offset:2048\n" \
"v_mfma_f32_16x16x32_bf16 v[88:91], a[52:55], a[12:15], v[88:91]\n" \
"ds_read_b64_tr_b16 a[38:39], v214 offset:26624\n" \
"global_load_lds_dwordx4 v217, s[38:39] offset:-3072\n" \
"v_mfma_f32_16x16x32_bf16 v[92:95], a[56:59], a[8:11], v[92:95]\n" \
"ds_read_b64_tr_b16 a[40:41], v215 offset:16384\n" \
"v_mfma_f32_16x16x32_bf16 v[92:95], a[60:63], a[12:15], v[92:95]\n" \
"ds_read_b128 a[8:11], v213 offset:256\n" \
"v_mfma_f32_16x16x32_bf16 v[104:107], a[48:51], a[16:19], v[104:107]\n" \
"ds_read_b128 a[12:15], v213 offset:2304\n" \
"v_mfma_f32_16x16x32_bf16 v[104:107], a[52:55], a[20:23], v[104:107]\n" \
"ds_read_b64_tr_b16 a[42:43], v215 offset:18432\n" \
"global_load_lds_dwordx4 v218, s[38:39] offset:-2048\n" \
"v_mfma_f32_16x16x32_bf16 v[108:111], a[56:59], a[16:19], v[108:111]\n" \
"ds_read_b64_tr_b16 a[44:45], v215 offset:24576\n" \
"v_mfma_f32_16x16x32_bf16 v[108:111], a[60:63], a[20:23], v[108:111]\n" \
"ds_read_b128 a[16:19], v213 offset:4096\n" \
"global_load_lds_dwordx4 v219, s[38:39] offset:-1024\n" \
"v_mfma_f32_16x16x32_bf16 v[120:123], a[48:51], a[24:27], v[120:123]\n" \
"s_add_u32 s38, s38, 0x20000\n" \
"s_addc_u32 s39, s39, 0\n" \
"ds_read_b128 a[20:23], v213 offset:6144\n" \
"v_mfma_f32_16x16x32_bf16 v[120:123], a[52:55], a[28:31], v[120:123]\n" \
"s_sub_u32 s75, s75, 32768\n" \
"s_cselect_b32 s75, 131072, s75\n" \
"ds_read_b64_tr_b16 a[46:47], v215 offset:26624\n" \
"v_mfma_f32_16x16x32_bf16 v[124:127], a[56:59], a[24:27], v[124:127]\n" \
"s_sub_u32 s72, s72, 1\n" \
"s_cmp_lg_u32 s72, 0\n" \
"v_mfma_f32_16x16x32_bf16 v[124:127], a[60:63], a[28:31], v[124:127]\n" \
"s_waitcnt lgkmcnt(3)\n" \
"s_cbranch_scc1 Lo64_%=\n" \
"s_setpc_b64 s[68:69]\n" \
"Lhot_end_%=:\n" \
"Lmask0_%=:\n" \
"Lpg13705_%=:\n" \
"s_cbranch_execz Lcb0_%=\n" \
"s_waitcnt lgkmcnt(0)\n" \
"v_subrev_u32 v0, 0, v251\n" \
"v_subrev_u32 v1, 1, v251\n" \
"v_subrev_u32 v2, 2, v251\n" \
"v_subrev_u32 v3, 3, v251\n" \
"v_subrev_u32 v4, 16, v251\n" \
"v_subrev_u32 v5, 17, v251\n" \
"v_subrev_u32 v6, 18, v251\n" \
"v_subrev_u32 v7, 19, v251\n" \
"v_subrev_u32 v32, 32, v251\n" \
"v_subrev_u32 v33, 33, v251\n" \
"v_subrev_u32 v34, 34, v251\n" \
"v_subrev_u32 v35, 35, v251\n" \
"v_subrev_u32 v36, 48, v251\n" \
"v_subrev_u32 v37, 49, v251\n" \
"v_subrev_u32 v38, 50, v251\n" \
"v_subrev_u32 v39, 51, v251\n" \
"v_ashrrev_i32 v0, 31, v0\n" \
"v_ashrrev_i32 v1, 31, v1\n" \
"v_ashrrev_i32 v2, 31, v2\n" \
"v_ashrrev_i32 v3, 31, v3\n" \
"v_ashrrev_i32 v4, 31, v4\n" \
"v_ashrrev_i32 v5, 31, v5\n" \
"v_ashrrev_i32 v6, 31, v6\n" \
"v_ashrrev_i32 v7, 31, v7\n" \
"v_ashrrev_i32 v32, 31, v32\n" \
"v_ashrrev_i32 v33, 31, v33\n" \
"v_ashrrev_i32 v34, 31, v34\n" \
"v_ashrrev_i32 v35, 31, v35\n" \
"v_ashrrev_i32 v36, 31, v36\n" \
"v_ashrrev_i32 v37, 31, v37\n" \
"v_ashrrev_i32 v38, 31, v38\n" \
"v_ashrrev_i32 v39, 31, v39\n" \
"v_and_b32 v0, 0xff800000, v0\n" \
"v_and_b32 v1, 0xff800000, v1\n" \
"v_and_b32 v2, 0xff800000, v2\n" \
"v_and_b32 v3, 0xff800000, v3\n" \
"v_and_b32 v4, 0xff800000, v4\n" \
"v_and_b32 v5, 0xff800000, v5\n" \
"v_and_b32 v6, 0xff800000, v6\n" \
"v_and_b32 v7, 0xff800000, v7\n" \
"v_and_b32 v32, 0xff800000, v32\n" \
"v_and_b32 v33, 0xff800000, v33\n" \
"v_and_b32 v34, 0xff800000, v34\n" \
"v_and_b32 v35, 0xff800000, v35\n" \
"v_and_b32 v36, 0xff800000, v36\n" \
"v_and_b32 v37, 0xff800000, v37\n" \
"v_and_b32 v38, 0xff800000, v38\n" \
"v_and_b32 v39, 0xff800000, v39\n" \
"s_branch Lmask0r_%=\n" \
"Lpl1_%=:\n" \
"s_mov_b32 s31, 0\n" \
"Lpk1_%=:\n" \
"s_sleep 2\n" \
"buffer_load_dword v244, v245, s[56:59], s91 offen sc1\n" \
"s_waitcnt vmcnt(0)\n" \
"v_cmp_ne_u32 vcc, s60, v244\n" \
"s_nop 1\n" \
"s_and_b32 vcc_hi, vcc_lo, 255\n" \
"s_cbranch_scc0 Lpr1_%=\n" \
"s_add_u32 s31, s31, 1\n" \
"s_cmp_lt_u32 s31, 0x40000\n" \
"s_cbranch_scc1 Lpk1_%=\n" \
"s_branch Lpr1_%=\n" \
"Lrs0_%=:\n" \
"s_call_b64 s[68:69], Lrest_%=\n" \
"s_branch Lrr0_%=\n" \
"Lrest_%=:\n" \
"s_mov_b32 s31, 0\n" \
"s_waitcnt vmcnt(0)\n" \
"Lrk_%=:\n" \
"v_lshrrev_b32 v253, 2, v248\n" \
"s_sleep 2\n" \
"buffer_load_dword v253, v253, s[56:59], s79 offen sc1\n" \
"s_waitcnt vmcnt(0)\n" \
"v_cmp_ne_u32 s[52:53], s60, v253\n" \
"s_add_u32 s31, s31, 1\n" \
"s_cmp_lt_u32 s31, 0x40000\n" \
"s_cselect_b32 s54, 0xff, 0\n" \
"s_and_b32 s52, s52, s54\n" \
"s_cmp_lg_u32 s52, 0\n" \
"s_cbranch_scc1 Lrk_%=\n" \
"s_mov_b64 s[10:11], 0\n" \
"s_setpc_b64 s[68:69]\n" \
"Lrs1_%=:\n" \
"s_call_b64 s[68:69], Lrest_%=\n" \
"s_branch Lrr1_%=\n" \
"Lrs2_%=:\n" \
"s_call_b64 s[68:69], Lrest_%=\n" \
"s_branch Lrr2_%=\n" \
"Lrs3_%=:\n" \
"s_call_b64 s[68:69], Lrest_%=\n" \
"s_branch Lrr3_%=\n" \
"LF7_%=:\n" \
"v_mfma_f32_16x16x32_bf16 v[16:19], a[192:195], a[160:163], v[232:235]\n" \
"s_bfe_u32 s88, %1, 0x20001\n" \
"ds_read_b128 v[192:195], v246 offset:16384\n" \
"v_exp_f32 v104, v104\n" \
"s_lshl_b32 s88, s88, 2\n" \
"v_mfma_f32_16x16x32_bf16 v[16:19], a[196:199], a[164:167], v[16:19]\n" \
"s_lshr_b32 s70, %1, 6\n" \
"ds_read_b128 v[196:199], v246 offset:18432\n" \
"v_cvt_pk_bf16_f32 v91, v94, v95\n" \
"v_add_f32 v241, v241, v104\n" \
"v_exp_f32 v105, v105\n" \
"v_mfma_f32_16x16x32_bf16 v[24:27], a[192:195], a[176:179], v[236:239]\n" \
"s_add_u32 s88, s88, s70\n" \
"ds_read_b128 v[200:203], v246 offset:16640\n" \
"v_mov_b32 v78, v240\n" \
"v_add_f32 v241, v241, v105\n" \
"v_bfe_u32 v69, v248, 8, 1\n" \
"v_bfe_u32 v70, v248, 6, 2\n" \
"s_lshr_b32 s71, s22, 7\n" \
"v_exp_f32 v106, v106\n" \
"v_mfma_f32_16x16x32_bf16 v[24:27], a[196:199], a[180:183], v[24:27]\n" \
"s_sub_u32 s71, s63, s71\n" \
"ds_read_b128 a[252:255], v246 offset:14592\n" \
"v_add_f32 v241, v241, v106\n" \
"v_cvt_pk_bf16_f32 v104, v104, v105\n" \
"v_exp_f32 v107, v107\n" \
"s_add_u32 s71, s71, 6\n" \
"v_mfma_f32_16x16x32_bf16 v[20:23], a[200:203], a[160:163], v[232:235]\n" \
"s_and_b32 s71, s71, 7\n" \
"ds_read_b128 v[204:207], v246 offset:18688\n" \
"v_add_f32 v241, v241, v107\n" \
"v_lshl_add_u32 v70, v69, 3, v70\n" \
"v_mul_u32_u24 v70, 6144, v70\n" \
"v_exp_f32 v108, v108\n" \
"v_permlane16_swap_b32 v78, v240\n" \
"v_add_f32 v240, v240, v78\n" \
"v_mov_b32 v78, v240\n" \
"v_mfma_f32_16x16x32_bf16 v[20:23], a[204:207], a[164:167], v[20:23]\n" \
"s_and_b32 s70, %1, 1\n" \
"ds_read_b128 v[208:211], v246 offset:24576\n" \
"v_add_f32 v241, v241, v108\n" \
"v_cvt_pk_bf16_f32 v105, v106, v107\n" \
"v_exp_f32 v109, v109\n" \
"s_lshl_b32 s70, s70, 3\n" \
"v_mfma_f32_16x16x32_bf16 v[28:31], a[200:203], a[176:179], v[236:239]\n" \
"s_add_u32 s70, s70, s71\n" \
"ds_read_b128 v[212:215], v246 offset:26624\n" \
"v_add_f32 v241, v241, v109\n" \
"v_lshlrev_b32 v69, 5, v69\n" \
"v_and_b32 v71, 0x30, v248\n" \
"s_lshl_b32 s87, s70, 12\n" \
"v_exp_f32 v110, v110\n" \
"v_permlane32_swap_b32 v78, v240\n" \
"v_add_f32 v240, v240, v78\n" \
"v_rcp_f32 v78, v240\n" \
"v_mul_f32 v240, 0x4d800000, v240\n" \
"v_add_u32 v79, 0x1c90080, v249\n" \
"v_cndmask_b32 v79, v249, v79, s[16:17]\n" \
"v_mfma_f32_16x16x32_bf16 v[28:31], a[204:207], a[180:183], v[28:31]\n" \
"s_lshl_b32 s70, s70, 7\n" \
"s_mul_i32 s84, s61, 6144\n" \
"ds_read_b128 v[216:219], v246 offset:24832\n" \
"v_accvgpr_read_b32 v96, a0\n" \
"v_accvgpr_read_b32 v97, a1\n" \
"v_accvgpr_read_b32 v98, a2\n" \
"v_accvgpr_read_b32 v99, a3\n" \
"v_accvgpr_read_b32 v100, a4\n" \
"v_accvgpr_read_b32 v101, a5\n" \
"v_add_f32 v241, v241, v110\n" \
"v_cvt_pk_bf16_f32 v106, v108, v109\n" \
"v_exp_f32 v111, v111\n" \
"v_mfma_f32_16x16x32_bf16 v[48:51], a[208:211], a[160:163], v[232:235]\n" \
"s_lshl_b32 s71, s84, 4\n" \
"ds_read_b128 v[220:223], v246 offset:26880\n" \
"v_accvgpr_read_b32 v102, a6\n" \
"v_accvgpr_read_b32 v103, a7\n" \
"v_mul_f32 v96, v96, v78\n" \
"v_mul_f32 v97, v97, v78\n" \
"v_mul_f32 v98, v98, v78\n" \
"v_mul_f32 v99, v99, v78\n" \
"v_add_f32 v241, v241, v111\n" \
"v_xor_b32 v69, v69, v71\n" \
"v_exp_f32 v112, v112\n" \
"v_mfma_f32_16x16x32_bf16 v[48:51], a[212:215], a[164:167], v[48:51]\n" \
"ds_read_b128 a[192:195], v247 offset:16384\n" \
"v_lshrrev_b32 v71, 9, v248\n" \
"v_mul_f32 v100, v100, v78\n" \
"v_mul_f32 v101, v101, v78\n" \
"v_mul_f32 v102, v102, v78\n" \
"v_mul_f32 v103, v103, v78\n" \
"v_fma_f32 v240, v96, v97, v240\n" \
"v_cvt_pk_bf16_f32 v224, v96, v97\n" \
"v_add_f32 v242, v242, v112\n" \
"v_cvt_pk_bf16_f32 v107, v110, v111\n" \
"v_exp_f32 v113, v113\n" \
"v_mfma_f32_16x16x32_bf16 v[56:59], a[208:211], a[176:179], v[236:239]\n" \
"s_add_u32 s70, s70, s71\n" \
"s_add_u32 s70, s70, 4096\n" \
"ds_read_b128 a[196:199], v247 offset:24576\n" \
"v_fma_f32 v240, v98, v99, v240\n" \
"v_cvt_pk_bf16_f32 v225, v98, v99\n" \
"v_fma_f32 v240, v100, v101, v240\n" \
"v_cvt_pk_bf16_f32 v226, v100, v101\n" \
"v_fma_f32 v240, v102, v103, v240\n" \
"v_cvt_pk_bf16_f32 v227, v102, v103\n" \
"v_add_f32 v242, v242, v113\n" \
"v_exp_f32 v114, v114\n" \
"v_mfma_f32_16x16x32_bf16 v[56:59], a[212:215], a[180:183], v[56:59]\n" \
"s_waitcnt lgkmcnt(4)\n" \
"buffer_store_dwordx4 v[224:227], v79, s[56:59], s77 offen offset:0 sc1\n" \
"ds_read_b128 a[200:203], v247 offset:17408\n" \
"v_lshl_add_u32 v70, v71, 6, v70\n" \
"v_add_f32 v242, v242, v114\n" \
"v_cvt_pk_bf16_f32 v112, v112, v113\n" \
"v_accvgpr_read_b32 v96, a8\n" \
"v_accvgpr_read_b32 v97, a9\n" \
"v_accvgpr_read_b32 v98, a10\n" \
"v_accvgpr_read_b32 v99, a11\n" \
"v_accvgpr_read_b32 v100, a12\n" \
"v_exp_f32 v115, v115\n" \
"v_mfma_f32_16x16x32_bf16 v[8:11], v[192:195], a[176:179], v[236:239]\n" \
"ds_read_b128 a[204:207], v247 offset:25600\n" \
"v_add_u32 v253, v69, v70\n" \
"v_accvgpr_read_b32 v101, a13\n" \
"v_accvgpr_read_b32 v102, a14\n" \
"v_accvgpr_read_b32 v103, a15\n" \
"v_mul_f32 v96, v96, v78\n" \
"v_mul_f32 v97, v97, v78\n" \
"v_mul_f32 v98, v98, v78\n" \
"v_add_f32 v242, v242, v115\n" \
"v_exp_f32 v116, v116\n" \
"v_mfma_f32_16x16x32_bf16 v[8:11], v[196:199], a[180:183], v[8:11]\n" \
"s_add_u32 s48, s22, s70\n" \
"s_addc_u32 s49, s23, 0\n" \
"ds_read_b128 a[208:211], v247 offset:18432\n" \
"v_mul_f32 v99, v99, v78\n" \
"v_mul_f32 v100, v100, v78\n" \
"v_mul_f32 v101, v101, v78\n" \
"v_mul_f32 v102, v102, v78\n" \
"v_mul_f32 v103, v103, v78\n" \
"v_fma_f32 v240, v96, v97, v240\n" \
"v_add_f32 v242, v242, v116\n" \
"v_cvt_pk_bf16_f32 v113, v114, v115\n" \
"v_exp_f32 v117, v117\n" \
"v_mfma_f32_16x16x32_bf16 v[52:55], a[216:219], a[160:163], v[232:235]\n" \
"s_add_u32 s50, s48, 23552\n" \
"s_addc_u32 s51, s49, 0\n" \
"ds_read_b128 a[212:215], v247 offset:26624\n" \
"v_cvt_pk_bf16_f32 v228, v96, v97\n" \
"v_fma_f32 v240, v98, v99, v240\n" \
"v_cvt_pk_bf16_f32 v229, v98, v99\n" \
"v_fma_f32 v240, v100, v101, v240\n" \
"v_cvt_pk_bf16_f32 v230, v100, v101\n" \
"v_fma_f32 v240, v102, v103, v240\n" \
"v_add_f32 v242, v242, v117\n" \
"v_exp_f32 v118, v118\n" \
"v_mfma_f32_16x16x32_bf16 v[52:55], a[220:223], a[164:167], v[52:55]\n" \
"s_lshr_b32 s89, s22, 7\n" \
"ds_read_b128 v[192:195], v246 offset:20480\n" \
"v_cvt_pk_bf16_f32 v231, v102, v103\n" \
"v_add_f32 v242, v242, v118\n" \
"v_cvt_pk_bf16_f32 v114, v116, v117\n" \
"s_sub_u32 s89, 3, s89\n" \
"v_mfma_f32_16x16x32_bf16 v[60:63], a[216:219], a[176:179], v[236:239]\n" \
"buffer_store_dwordx4 v[228:231], v79, s[56:59], s77 offen offset:2048 sc1\n" \
"s_add_u32 s77, s77, 0x1000\n" \
"ds_read_b128 v[196:199], v246 offset:22528\n" \
"v_bfe_u32 v84, v248, 7, 2\n" \
"v_accvgpr_read_b32 v96, a16\n" \
"v_accvgpr_read_b32 v97, a17\n" \
"v_accvgpr_read_b32 v98, a18\n" \
"v_accvgpr_read_b32 v99, a19\n" \
"v_mfma_f32_16x16x32_bf16 v[60:63], a[220:223], a[180:183], v[60:63]\n" \
"s_and_b32 s89, s89, 7\n" \
"s_and_b32 s90, %1, 1\n" \
"ds_read_b128 a[216:219], v247 offset:19456\n" \
"v_accvgpr_read_b32 v100, a20\n" \
"v_accvgpr_read_b32 v101, a21\n" \
"v_accvgpr_read_b32 v102, a22\n" \
"v_accvgpr_read_b32 v103, a23\n" \
"v_mul_f32 v96, v96, v78\n" \
"v_mul_f32 v97, v97, v78\n" \
"v_mfma_f32_16x16x32_bf16 v[12:15], v[200:203], a[176:179], v[236:239]\n" \
"ds_read_b128 a[220:223], v247 offset:27648\n" \
"v_bfe_u32 v85, v248, 5, 2\n" \
"v_mul_f32 v98, v98, v78\n" \
"v_mul_f32 v99, v99, v78\n" \
"v_mul_f32 v100, v100, v78\n" \
"v_mul_f32 v101, v101, v78\n" \
"v_mul_f32 v102, v102, v78\n" \
"v_mul_f32 v103, v103, v78\n" \
"v_mfma_f32_16x16x32_bf16 v[12:15], v[204:207], a[180:183], v[12:15]\n" \
"s_lshl_b32 s90, s90, 3\n" \
"s_add_u32 s89, s89, s90\n" \
"ds_read_b128 v[200:203], v246 offset:20736\n" \
"v_fma_f32 v240, v96, v97, v240\n" \
"v_cvt_pk_bf16_f32 v224, v96, v97\n" \
"v_fma_f32 v240, v98, v99, v240\n" \
"v_cvt_pk_bf16_f32 v225, v98, v99\n" \
"v_fma_f32 v240, v100, v101, v240\n" \
"v_cvt_pk_bf16_f32 v226, v100, v101\n" \
"v_mfma_f32_16x16x32_bf16 v[16:19], a[224:227], a[168:171], v[16:19]\n" \
"ds_read_b128 v[204:207], v246 offset:22784\n" \
"v_min_u32 v85, 2, v85\n" \
"v_fma_f32 v240, v102, v103, v240\n" \
"v_cvt_pk_bf16_f32 v227, v102, v103\n" \
"v_mfma_f32_16x16x32_bf16 v[16:19], a[228:231], a[172:175], v[16:19]\n" \
"s_lshl_b32 s89, s89, 7\n" \
"s_lshr_b32 s90, %1, 3\n" \
"v_mad_u32_u24 v84, v84, 3, v85\n" \
"v_mfma_f32_16x16x32_bf16 v[24:27], a[224:227], a[184:187], v[24:27]\n" \
"s_lshl_b32 s90, s90, 2\n" \
"s_add_u32 s90, s90, s61\n" \
"v_and_b32 v85, 16, v248\n" \
"v_mfma_f32_16x16x32_bf16 v[24:27], a[228:231], a[188:191], v[24:27]\n" \
"s_mul_i32 s90, s90, 24576\n" \
"ds_read_b128 a[224:227], v247 offset:20480\n" \
"buffer_store_dwordx4 v[224:227], v79, s[56:59], s77 offen offset:0 sc1\n" \
"s_add_u32 s89, s89, s90\n" \
"v_accvgpr_read_b32 v96, a24\n" \
"v_accvgpr_read_b32 v97, a25\n" \
"v_accvgpr_read_b32 v98, a26\n" \
"v_mfma_f32_16x16x32_bf16 v[40:43], v[208:211], a[176:179], v[236:239]\n" \
"ds_read_b128 a[228:231], v247 offset:28672\n" \
"v_lshl_add_u32 v85, v85, 2, s89\n" \
"v_accvgpr_read_b32 v99, a27\n" \
"v_accvgpr_read_b32 v100, a28\n" \
"v_accvgpr_read_b32 v101, a29\n" \
"v_accvgpr_read_b32 v102, a30\n" \
"v_accvgpr_read_b32 v103, a31\n" \
"v_mul_f32 v96, v96, v78\n" \
"v_mfma_f32_16x16x32_bf16 v[40:43], v[212:215], a[180:183], v[40:43]\n" \
"ds_read_b128 v[208:211], v246 offset:28672\n" \
"v_lshl_add_u32 v84, v84, 11, v85\n" \
"v_mul_f32 v97, v97, v78\n" \
"v_mul_f32 v98, v98, v78\n" \
"v_mul_f32 v99, v99, v78\n" \
"v_mul_f32 v100, v100, v78\n" \
"v_mul_f32 v101, v101, v78\n" \
"v_mul_f32 v102, v102, v78\n" \
"v_mfma_f32_16x16x32_bf16 v[20:23], a[232:235], a[168:171], v[20:23]\n" \
"s_add_u32 m0, s84, 143360\n" \
"ds_read_b128 v[212:215], v246 offset:30720\n" \
"v_mul_f32 v103, v103, v78\n" \
"v_fma_f32 v240, v96, v97, v240\n" \
"v_cvt_pk_bf16_f32 v228, v96, v97\n" \
"v_fma_f32 v240, v98, v99, v240\n" \
"v_cvt_pk_bf16_f32 v229, v98, v99\n" \
"v_fma_f32 v240, v100, v101, v240\n" \
"v_exp_f32 v119, v119\n" \
"v_mfma_f32_16x16x32_bf16 v[20:23], a[236:239], a[172:175], v[20:23]\n" \
"global_load_lds_dwordx4 v253, s[48:49] offset:-4096\n" \
"v_add_f32 v242, v242, v119\n" \
"v_cvt_pk_bf16_f32 v230, v100, v101\n" \
"v_fma_f32 v240, v102, v103, v240\n" \
"v_cvt_pk_bf16_f32 v231, v102, v103\n" \
"v_mfma_f32_16x16x32_bf16 v[28:31], a[232:235], a[184:187], v[28:31]\n" \
"v_exp_f32 v120, v120\n" \
"v_mfma_f32_16x16x32_bf16 v[28:31], a[236:239], a[188:191], v[28:31]\n" \
"s_waitcnt lgkmcnt(2)\n" \
"ds_read_b128 a[232:235], v247 offset:21504\n" \
"v_add_f32 v243, v243, v120\n" \
"v_cvt_pk_bf16_f32 v115, v118, v119\n" \
"v_mfma_f32_16x16x32_bf16 v[44:47], v[216:219], a[176:179], v[236:239]\n" \
"ds_read_b128 a[236:239], v247 offset:29696\n" \
"global_load_lds_dwordx4 v253, s[48:49] offset:-2048\n" \
"v_mfma_f32_16x16x32_bf16 v[44:47], v[220:223], a[180:183], v[44:47]\n" \
"ds_read_b128 v[216:219], v246 offset:28928\n" \
"v_exp_f32 v121, v121\n" \
"v_mfma_f32_16x16x32_bf16 v[48:51], a[240:243], a[168:171], v[48:51]\n" \
"ds_read_b128 v[220:223], v246 offset:30976\n" \
"v_add_f32 v243, v243, v121\n" \
"v_mfma_f32_16x16x32_bf16 v[48:51], a[244:247], a[172:175], v[48:51]\n" \
"global_load_lds_dwordx4 v253, s[48:49] offset:0\n" \
"v_exp_f32 v122, v122\n" \
"v_mfma_f32_16x16x32_bf16 v[56:59], a[240:243], a[184:187], v[56:59]\n" \
"s_add_u32 s48, s48, 0x120000\n" \
"s_addc_u32 s49, s49, 0\n" \
"v_add_f32 v243, v243, v122\n" \
"v_mfma_f32_16x16x32_bf16 v[56:59], a[244:247], a[188:191], v[56:59]\n" \
"ds_read_b128 a[240:243], v247 offset:22528\n" \
"v_cvt_pk_bf16_f32 v120, v120, v121\n" \
"v_mfma_f32_16x16x32_bf16 v[8:11], v[192:195], a[184:187], v[8:11]\n" \
"ds_read_b128 a[244:247], v247 offset:30720\n" \
"global_load_lds_dwordx4 v253, s[50:51] offset:-3072\n" \
"v_mfma_f32_16x16x32_bf16 v[8:11], v[196:199], a[188:191], v[8:11]\n" \
"v_exp_f32 v123, v123\n" \
"v_mfma_f32_16x16x32_bf16 v[52:55], a[248:251], a[168:171], v[52:55]\n" \
"v_add_f32 v243, v243, v123\n" \
"v_exp_f32 v124, v124\n" \
"v_mfma_f32_16x16x32_bf16 v[52:55], a[252:255], a[172:175], v[52:55]\n" \
"global_load_lds_dwordx4 v253, s[50:51] offset:-1024\n" \
"v_add_f32 v243, v243, v124\n" \
"v_cvt_pk_bf16_f32 v121, v122, v123\n" \
"v_mfma_f32_16x16x32_bf16 v[60:63], a[248:251], a[184:187], v[60:63]\n" \
"v_exp_f32 v125, v125\n" \
"v_mfma_f32_16x16x32_bf16 v[60:63], a[252:255], a[188:191], v[60:63]\n" \
"ds_read_b128 a[248:251], v247 offset:23552\n" \
"v_add_f32 v243, v243, v125\n" \
"v_mfma_f32_16x16x32_bf16 v[12:15], v[200:203], a[184:187], v[12:15]\n" \
"ds_read_b128 a[252:255], v247 offset:31744\n" \
"global_load_lds_dwordx4 v253, s[50:51] offset:1024\n" \
"v_mfma_f32_16x16x32_bf16 v[12:15], v[204:207], a[188:191], v[12:15]\n" \
"s_waitcnt lgkmcnt(2)\n" \
"s_add_u32 s50, s50, 0x120000\n" \
"s_addc_u32 s51, s51, 0\n" \
"v_mfma_f32_16x16x32_bf16 v[40:43], v[208:211], a[184:187], v[40:43]\n" \
"v_exp_f32 v126, v126\n" \
"v_mfma_f32_16x16x32_bf16 v[40:43], v[212:215], a[188:191], v[40:43]\n" \
"global_load_dword v85, v84, s[22:23]\n" \
"v_mfma_f32_16x16x32_bf16 v[44:47], v[216:219], a[184:187], v[44:47]\n" \
"s_waitcnt vmcnt(10) lgkmcnt(0)\n" \
"v_add_u32 v247, 0x10000, v248\n" \
"v_add_f32 v243, v243, v126\n" \
"v_mfma_f32_16x16x32_bf16 v[44:47], v[220:223], a[188:191], v[44:47]\n" \
"s_barrier\n" \
"v_cvt_pk_bf16_f32 v122, v124, v125\n" \
"v_mfma_f32_16x16x32_bf16 a[32:35], a[192:195], v[72:75], a[32:35]\n" \
"v_exp_f32 v127, v127\n" \
"v_mfma_f32_16x16x32_bf16 a[32:35], a[196:199], v[104:107], a[32:35]\n" \
"v_add_f32 v243, v243, v127\n" \
"v_cvt_pk_bf16_f32 v123, v126, v127\n" \
"v_mfma_f32_16x16x32_bf16 v[128:131], a[192:195], v[80:83], v[128:131]\n" \
"ds_read_b128 v[192:195], v247 offset:16384\n" \
"v_lshrrev_b32 v71, 7, v248\n" \
"v_cmp_gt_i32 vcc, 0, v251\n" \
"v_cmp_gt_i32 s[64:65], 1, v251\n" \
"v_cmp_gt_i32 s[66:67], 2, v251\n" \
"v_cndmask_b32 v8, v8, v254, vcc\n" \
"v_cndmask_b32 v16, v16, v254, vcc\n" \
"v_cmp_gt_i32 vcc, 3, v251\n" \
"v_mfma_f32_16x16x32_bf16 v[128:131], a[196:199], v[112:115], v[128:131]\n" \
"v_cndmask_b32 v9, v9, v254, s[64:65]\n" \
"v_cndmask_b32 v17, v17, v254, s[64:65]\n" \
"v_cmp_gt_i32 s[64:65], 16, v251\n" \
"v_cndmask_b32 v10, v10, v254, s[66:67]\n" \
"v_cndmask_b32 v18, v18, v254, s[66:67]\n" \
"v_cmp_gt_i32 s[66:67], 17, v251\n" \
"v_and_b32 v76, 0x70, v248\n" \
"s_lshl_b32 s70, s88, 18\n" \
"v_mfma_f32_16x16x32_bf16 v[160:163], a[192:195], v[88:91], v[160:163]\n" \
"s_lshl_b32 s71, s61, 16\n" \
"s_add_u32 s71, s71, s70\n" \
"v_cndmask_b32 v11, v11, v254, vcc\n" \
"v_cndmask_b32 v19, v19, v254, vcc\n" \
"v_cmp_gt_i32 vcc, 18, v251\n" \
"v_cndmask_b32 v12, v12, v254, s[64:65]\n" \
"v_cndmask_b32 v20, v20, v254, s[64:65]\n" \
"v_cmp_gt_i32 s[64:65], 19, v251\n" \
"v_lshlrev_b32 v77, 4, v71\n" \
"v_mfma_f32_16x16x32_bf16 v[160:163], a[196:199], v[120:123], v[160:163]\n" \
"ds_read_b128 a[192:195], v247 offset:0\n" \
"ds_read_b128 v[196:199], v247 offset:24576\n" \
"v_cndmask_b32 v13, v13, v254, s[66:67]\n" \
"v_cndmask_b32 v21, v21, v254, s[66:67]\n" \
"v_cmp_gt_i32 s[66:67], 32, v251\n" \
"v_cndmask_b32 v14, v14, v254, vcc\n" \
"v_cndmask_b32 v22, v22, v254, vcc\n" \
"v_cmp_gt_i32 vcc, 33, v251\n" \
"v_mfma_f32_16x16x32_bf16 a[36:39], a[200:203], v[72:75], a[36:39]\n" \
"v_xor_b32 v77, v77, v76\n" \
"v_cndmask_b32 v15, v15, v254, s[64:65]\n" \
"v_cndmask_b32 v23, v23, v254, s[64:65]\n" \
"v_cmp_gt_i32 s[64:65], 34, v251\n" \
"v_cndmask_b32 v40, v40, v254, s[66:67]\n" \
"v_cndmask_b32 v48, v48, v254, s[66:67]\n" \
"v_cmp_gt_i32 s[66:67], 35, v251\n" \
"v_mfma_f32_16x16x32_bf16 a[36:39], a[204:207], v[104:107], a[36:39]\n" \
"s_add_u32 s42, s20, s71\n" \
"s_addc_u32 s43, s21, 0\n" \
"ds_read_b128 a[196:199], v247 offset:8192\n" \
"v_cndmask_b32 v41, v41, v254, vcc\n" \
"v_cndmask_b32 v49, v49, v254, vcc\n" \
"v_cmp_gt_i32 vcc, 48, v251\n" \
"v_cndmask_b32 v42, v42, v254, s[64:65]\n" \
"v_cndmask_b32 v50, v50, v254, s[64:65]\n" \
"v_cmp_gt_i32 s[64:65], 49, v251\n" \
"v_mfma_f32_16x16x32_bf16 v[132:135], a[200:203], v[80:83], v[132:135]\n" \
"ds_read_b128 v[200:203], v247 offset:17408\n" \
"v_lshl_add_u32 v252, v71, 12, v77\n" \
"v_cndmask_b32 v43, v43, v254, s[66:67]\n" \
"v_cndmask_b32 v51, v51, v254, s[66:67]\n" \
"v_cmp_gt_i32 s[66:67], 50, v251\n" \
"v_cndmask_b32 v44, v44, v254, vcc\n" \
"v_cndmask_b32 v52, v52, v254, vcc\n" \
"v_cmp_gt_i32 vcc, 51, v251\n" \
"v_mfma_f32_16x16x32_bf16 v[132:135], a[204:207], v[112:115], v[132:135]\n" \
"s_add_u32 s44, s42, 31744\n" \
"s_addc_u32 s45, s43, 0\n" \
"s_max_u32 s70, s70, 32768\n" \
"v_cndmask_b32 v45, v45, v254, s[64:65]\n" \
"v_cndmask_b32 v53, v53, v254, s[64:65]\n" \
"v_cndmask_b32 v46, v46, v254, s[66:67]\n" \
"v_cndmask_b32 v54, v54, v254, s[66:67]\n" \
"v_cndmask_b32 v47, v47, v254, vcc\n" \
"v_cndmask_b32 v55, v55, v254, vcc\n" \
"v_mfma_f32_16x16x32_bf16 v[164:167], a[200:203], v[88:91], v[164:167]\n" \
"s_sub_u32 s70, s70, 32768\n" \
"v_exp_f32 v8, v8\n" \
"s_and_b32 s71, s61, 1\n" \
"s_lshl_b32 s71, s71, 2\n" \
"v_mfma_f32_16x16x32_bf16 v[164:167], a[204:207], v[120:123], v[164:167]\n" \
"ds_read_b128 a[200:203], v247 offset:1024\n" \
"buffer_store_dwordx4 v[228:231], v79, s[56:59], s77 offen offset:2048 sc1\n" \
"ds_read_b128 v[204:207], v247 offset:25600\n" \
"v_add_f32 v243, v243, v8\n" \
"v_exp_f32 v9, v9\n" \
"v_mfma_f32_16x16x32_bf16 a[40:43], a[208:211], v[72:75], a[40:43]\n" \
"s_add_u32 s77, s77, 0x81000\n" \
"v_add_f32 v243, v243, v9\n" \
"v_exp_f32 v10, v10\n" \
"v_and_or_b32 v71, v71, 3, s71\n" \
"s_lshr_b32 s71, s61, 1\n" \
"v_mfma_f32_16x16x32_bf16 a[40:43], a[212:215], v[104:107], a[40:43]\n" \
"s_lshl_b32 s71, s71, 11\n" \
"ds_read_b128 a[204:207], v247 offset:9216\n" \
"v_add_f32 v243, v243, v10\n" \
"v_cvt_pk_bf16_f32 v8, v8, v9\n" \
"v_exp_f32 v11, v11\n" \
"v_lshlrev_b32 v77, 4, v71\n" \
"v_mfma_f32_16x16x32_bf16 v[136:139], a[208:211], v[80:83], v[136:139]\n" \
"s_add_u32 s70, s70, s71\n" \
"ds_read_b128 v[208:211], v247 offset:18432\n" \
"v_add_f32 v243, v243, v11\n" \
"v_exp_f32 v12, v12\n" \
"v_xor_b32 v77, v77, v76\n" \
"v_mfma_f32_16x16x32_bf16 v[136:139], a[212:215], v[112:115], v[136:139]\n" \
"s_add_u32 s46, s20, s70\n" \
"s_addc_u32 s47, s21, 0\n" \
"v_add_f32 v243, v243, v12\n" \
"v_cvt_pk_bf16_f32 v9, v10, v11\n" \
"v_exp_f32 v13, v13\n" \
"v_lshl_add_u32 v246, v71, 12, v77\n" \
"v_mfma_f32_16x16x32_bf16 v[168:171], a[208:211], v[88:91], v[168:171]\n" \
"s_lshl_b32 s85, s61, 10\n" \
"v_add_f32 v243, v243, v13\n" \
"v_exp_f32 v14, v14\n" \
"s_mov_b64 s[12:13], s[20:21]\n" \
"s_and_b32 s13, s13, 0xffff\n" \
"v_mfma_f32_16x16x32_bf16 v[168:171], a[212:215], v[120:123], v[168:171]\n" \
"s_mov_b32 s14, 4194304\n" \
"ds_read_b128 a[208:211], v247 offset:2048\n" \
"s_mov_b32 s15, 0x20000\n" \
"ds_read_b128 v[212:215], v247 offset:26624\n" \
"v_add_f32 v243, v243, v14\n" \
"v_cvt_pk_bf16_f32 v10, v12, v13\n" \
"v_exp_f32 v15, v15\n" \
"v_mfma_f32_16x16x32_bf16 a[44:47], a[216:219], v[72:75], a[44:47]\n" \
"s_mov_b32 s46, s70\n" \
"v_add_f32 v243, v243, v15\n" \
"v_exp_f32 v40, v40\n" \
"v_lshrrev_b32 v71, 9, v248\n" \
"v_lshl_add_u32 v246, v71, 30, v246\n" \
"v_mfma_f32_16x16x32_bf16 a[44:47], a[220:223], v[104:107], a[44:47]\n" \
"s_add_u32 m0, s95, 118784\n" \
"ds_read_b128 a[212:215], v247 offset:10240\n" \
"v_add_f32 v243, v243, v40\n" \
"v_cvt_pk_bf16_f32 v11, v14, v15\n" \
"v_exp_f32 v41, v41\n" \
"v_mfma_f32_16x16x32_bf16 v[140:143], a[216:219], v[80:83], v[140:143]\n" \
"ds_read_b128 v[216:219], v247 offset:19456\n" \
"global_load_lds_dwordx4 v252, s[42:43] offset:0\n" \
"v_add_f32 v243, v243, v41\n" \
"v_exp_f32 v42, v42\n" \
"v_mfma_f32_16x16x32_bf16 v[140:143], a[220:223], v[112:115], v[140:143]\n" \
"v_add_f32 v243, v243, v42\n" \
"v_cvt_pk_bf16_f32 v40, v40, v41\n" \
"v_exp_f32 v43, v43\n" \
"v_mfma_f32_16x16x32_bf16 v[172:175], a[216:219], v[88:91], v[172:175]\n" \
"v_add_f32 v243, v243, v43\n" \
"v_exp_f32 v44, v44\n" \
"v_mfma_f32_16x16x32_bf16 v[172:175], a[220:223], v[120:123], v[172:175]\n" \
"ds_read_b128 a[216:219], v247 offset:3072\n" \
"global_load_lds_dwordx4 v252, s[42:43] offset:2048\n" \
"ds_read_b128 v[220:223], v247 offset:27648\n" \
"v_add_f32 v243, v243, v44\n" \
"v_cvt_pk_bf16_f32 v41, v42, v43\n" \
"v_exp_f32 v45, v45\n" \
"v_mfma_f32_16x16x32_bf16 a[48:51], a[224:227], v[72:75], a[48:51]\n" \
"s_add_u32 s42, s42, 0x180\n" \
"s_addc_u32 s43, s43, 0\n" \
"v_add_f32 v243, v243, v45\n" \
"v_exp_f32 v46, v46\n" \
"v_mfma_f32_16x16x32_bf16 a[48:51], a[228:231], v[104:107], a[48:51]\n" \
"ds_read_b128 a[220:223], v247 offset:11264\n" \
"v_add_f32 v243, v243, v46\n" \
"v_cvt_pk_bf16_f32 v42, v44, v45\n" \
"v_exp_f32 v47, v47\n" \
"v_mfma_f32_16x16x32_bf16 v[144:147], a[224:227], v[80:83], v[144:147]\n" \
"ds_read_b128 v[224:227], v247 offset:20480\n" \
"global_load_lds_dwordx4 v252, s[44:45] offset:1024\n" \
"v_add_f32 v243, v243, v47\n" \
"v_cvt_pk_bf16_f32 v43, v46, v47\n" \
"v_exp_f32 v24, v24\n" \
"v_mfma_f32_16x16x32_bf16 v[144:147], a[228:231], v[112:115], v[144:147]\n" \
"v_add_f32 v243, v243, v24\n" \
"v_exp_f32 v25, v25\n" \
"v_mfma_f32_16x16x32_bf16 v[176:179], a[224:227], v[88:91], v[176:179]\n" \
"v_add_f32 v243, v243, v25\n" \
"v_exp_f32 v26, v26\n" \
"v_mfma_f32_16x16x32_bf16 v[176:179], a[228:231], v[120:123], v[176:179]\n" \
"ds_read_b128 a[224:227], v247 offset:4096\n" \
"global_load_lds_dwordx4 v252, s[44:45] offset:3072\n" \
"ds_read_b128 v[228:231], v247 offset:28672\n" \
"v_add_f32 v243, v243, v26\n" \
"v_cvt_pk_bf16_f32 v24, v24, v25\n" \
"v_exp_f32 v27, v27\n" \
"v_mfma_f32_16x16x32_bf16 a[52:55], a[232:235], v[72:75], a[52:55]\n" \
"s_add_u32 s44, s44, 0x180\n" \
"s_addc_u32 s45, s45, 0\n" \
"v_add_f32 v243, v243, v27\n" \
"v_exp_f32 v28, v28\n" \
"v_mfma_f32_16x16x32_bf16 a[52:55], a[236:239], v[104:107], a[52:55]\n" \
"s_add_u32 m0, s85, 135168\n" \
"ds_read_b128 a[228:231], v247 offset:12288\n" \
"v_add_f32 v243, v243, v28\n" \
"v_cvt_pk_bf16_f32 v25, v26, v27\n" \
"v_exp_f32 v29, v29\n" \
"v_mfma_f32_16x16x32_bf16 v[148:151], a[232:235], v[80:83], v[148:151]\n" \
"ds_read_b128 v[232:235], v247 offset:21504\n" \
"v_add_f32 v243, v243, v29\n" \
"v_exp_f32 v30, v30\n" \
"v_mfma_f32_16x16x32_bf16 v[148:151], a[236:239], v[112:115], v[148:151]\n" \
"buffer_load_dwordx4 v246, s[12:15], s46 offen lds\n" \
"v_add_f32 v243, v243, v30\n" \
"v_cvt_pk_bf16_f32 v26, v28, v29\n" \
"v_exp_f32 v31, v31\n" \
"v_mfma_f32_16x16x32_bf16 v[180:183], a[232:235], v[88:91], v[180:183]\n" \
"s_add_u32 s46, s46, 0x180\n" \
"v_add_f32 v243, v243, v31\n" \
"v_exp_f32 v56, v56\n" \
"v_mfma_f32_16x16x32_bf16 v[180:183], a[236:239], v[120:123], v[180:183]\n" \
"ds_read_b128 a[232:235], v247 offset:5120\n" \
"ds_read_b128 v[236:239], v247 offset:29696\n" \
"v_add_f32 v243, v243, v56\n" \
"v_cvt_pk_bf16_f32 v27, v30, v31\n" \
"v_exp_f32 v57, v57\n" \
"v_mfma_f32_16x16x32_bf16 a[56:59], a[240:243], v[72:75], a[56:59]\n" \
"v_add_f32 v243, v243, v57\n" \
"v_exp_f32 v58, v58\n" \
"v_mfma_f32_16x16x32_bf16 a[56:59], a[244:247], v[104:107], a[56:59]\n" \
"ds_read_b128 a[236:239], v247 offset:13312\n" \
"v_add_f32 v243, v243, v58\n" \
"v_cvt_pk_bf16_f32 v56, v56, v57\n" \
"v_exp_f32 v59, v59\n" \
"v_mfma_f32_16x16x32_bf16 v[152:155], a[240:243], v[80:83], v[152:155]\n" \
"ds_read_b128 v[0:3], v247 offset:22528\n" \
"v_add_f32 v243, v243, v59\n" \
"v_exp_f32 v60, v60\n" \
"v_mfma_f32_16x16x32_bf16 v[152:155], a[244:247], v[112:115], v[152:155]\n" \
"v_add_f32 v243, v243, v60\n" \
"v_cvt_pk_bf16_f32 v57, v58, v59\n" \
"v_exp_f32 v61, v61\n" \
"v_mfma_f32_16x16x32_bf16 v[184:187], a[240:243], v[88:91], v[184:187]\n" \
"v_add_f32 v243, v243, v61\n" \
"v_exp_f32 v62, v62\n" \
"v_mfma_f32_16x16x32_bf16 v[184:187], a[244:247], v[120:123], v[184:187]\n" \
"ds_read_b128 a[240:243], v247 offset:6144\n" \
"ds_read_b128 v[4:7], v247 offset:30720\n" \
"v_add_f32 v243, v243, v62\n" \
"v_cvt_pk_bf16_f32 v58, v60, v61\n" \
"v_exp_f32 v63, v63\n" \
"v_mfma_f32_16x16x32_bf16 a[60:63], a[248:251], v[72:75], a[60:63]\n" \
"v_add_f32 v243, v243, v63\n" \
"v_cvt_pk_bf16_f32 v59, v62, v63\n" \
"v_exp_f32 v16, v16\n" \
"v_mfma_f32_16x16x32_bf16 a[60:63], a[252:255], v[104:107], a[60:63]\n" \
"ds_read_b128 a[244:247], v247 offset:14336\n" \
"v_add_f32 v242, v242, v16\n" \
"v_exp_f32 v17, v17\n" \
"v_mfma_f32_16x16x32_bf16 v[156:159], a[248:251], v[80:83], v[156:159]\n" \
"ds_read_b128 v[32:35], v247 offset:23552\n" \
"v_add_f32 v242, v242, v17\n" \
"v_exp_f32 v18, v18\n" \
"v_mfma_f32_16x16x32_bf16 v[156:159], a[252:255], v[112:115], v[156:159]\n" \
"v_add_f32 v242, v242, v18\n" \
"v_cvt_pk_bf16_f32 v16, v16, v17\n" \
"v_exp_f32 v19, v19\n" \
"v_mfma_f32_16x16x32_bf16 v[188:191], a[248:251], v[88:91], v[188:191]\n" \
"v_add_f32 v242, v242, v19\n" \
"v_exp_f32 v20, v20\n" \
"v_mfma_f32_16x16x32_bf16 v[188:191], a[252:255], v[120:123], v[188:191]\n" \
"s_waitcnt lgkmcnt(2)\n" \
"ds_read_b128 a[248:251], v247 offset:7168\n" \
"ds_read_b128 v[36:39], v247 offset:31744\n" \
"v_add_f32 v242, v242, v20\n" \
"v_cvt_pk_bf16_f32 v17, v18, v19\n" \
"v_exp_f32 v21, v21\n" \
"v_mfma_f32_16x16x32_bf16 v[160:163], v[192:195], v[8:11], v[160:163]\n" \
"v_add_f32 v242, v242, v21\n" \
"v_exp_f32 v22, v22\n" \
"v_mfma_f32_16x16x32_bf16 v[160:163], v[196:199], v[40:43], v[160:163]\n" \
"ds_read_b128 a[252:255], v247 offset:15360\n" \
"v_add_f32 v242, v242, v22\n" \
"v_cvt_pk_bf16_f32 v18, v20, v21\n" \
"v_exp_f32 v23, v23\n" \
"v_mfma_f32_16x16x32_bf16 v[164:167], v[200:203], v[8:11], v[164:167]\n" \
"v_mov_b32 v112, v241\n" \
"v_add_f32 v242, v242, v23\n" \
"v_exp_f32 v48, v48\n" \
"v_mfma_f32_16x16x32_bf16 v[164:167], v[204:207], v[40:43], v[164:167]\n" \
"v_add_f32 v242, v242, v48\n" \
"v_cvt_pk_bf16_f32 v19, v22, v23\n" \
"v_mfma_f32_16x16x32_bf16 v[168:171], v[208:211], v[8:11], v[168:171]\n" \
"v_exp_f32 v49, v49\n" \
"v_permlane16_swap_b32 v112, v241\n" \
"v_add_f32 v241, v241, v112\n" \
"v_mov_b32 v112, v241\n" \
"v_mfma_f32_16x16x32_bf16 v[168:171], v[212:215], v[40:43], v[168:171]\n" \
"s_waitcnt vmcnt(0) lgkmcnt(0)\n" \
"v_add_f32 v242, v242, v49\n" \
"v_exp_f32 v50, v50\n" \
"v_mfma_f32_16x16x32_bf16 v[172:175], v[216:219], v[8:11], v[172:175]\n" \
"s_barrier\n" \
"v_add_f32 v242, v242, v50\n" \
"v_cvt_pk_bf16_f32 v48, v48, v49\n" \
"v_permlane32_swap_b32 v112, v241\n" \
"v_add_f32 v241, v241, v112\n" \
"v_rcp_f32 v112, v241\n" \
"v_mul_f32 v241, 0x4d800000, v241\n" \
"v_add_u32 v113, 0x1c90080, v249\n" \
"v_cndmask_b32 v113, v249, v113, s[16:17]\n" \
"v_mfma_f32_16x16x32_bf16 v[172:175], v[220:223], v[40:43], v[172:175]\n" \
"s_add_u32 m0, s84, 90112\n" \
"v_accvgpr_read_b32 v96, a32\n" \
"v_accvgpr_read_b32 v97, a33\n" \
"v_accvgpr_read_b32 v98, a34\n" \
"v_accvgpr_read_b32 v99, a35\n" \
"v_accvgpr_read_b32 v100, a36\n" \
"v_accvgpr_read_b32 v101, a37\n" \
"v_exp_f32 v51, v51\n" \
"v_mfma_f32_16x16x32_bf16 v[176:179], v[224:227], v[8:11], v[176:179]\n" \
"global_load_lds_dwordx4 v253, s[48:49] offset:-4096\n" \
"v_add_f32 v242, v242, v51\n" \
"v_accvgpr_read_b32 v102, a38\n" \
"v_accvgpr_read_b32 v103, a39\n" \
"v_mul_f32 v96, v96, v112\n" \
"v_mul_f32 v97, v97, v112\n" \
"v_mul_f32 v98, v98, v112\n" \
"v_mul_f32 v99, v99, v112\n" \
"v_mfma_f32_16x16x32_bf16 v[176:179], v[228:231], v[40:43], v[176:179]\n" \
"global_load_lds_dwordx4 v253, s[48:49] offset:-2048\n" \
"v_mul_f32 v100, v100, v112\n" \
"v_mul_f32 v101, v101, v112\n" \
"v_mul_f32 v102, v102, v112\n" \
"v_mul_f32 v103, v103, v112\n" \
"v_fma_f32 v241, v96, v97, v241\n" \
"v_cvt_pk_bf16_f32 v104, v96, v97\n" \
"v_exp_f32 v52, v52\n" \
"v_mfma_f32_16x16x32_bf16 v[180:183], v[232:235], v[8:11], v[180:183]\n" \
"v_add_f32 v242, v242, v52\n" \
"v_cvt_pk_bf16_f32 v49, v50, v51\n" \
"v_fma_f32 v241, v98, v99, v241\n" \
"v_cvt_pk_bf16_f32 v105, v98, v99\n" \
"v_fma_f32 v241, v100, v101, v241\n" \
"v_cvt_pk_bf16_f32 v106, v100, v101\n" \
"v_fma_f32 v241, v102, v103, v241\n" \
"v_cvt_pk_bf16_f32 v107, v102, v103\n" \
"v_mfma_f32_16x16x32_bf16 v[180:183], v[236:239], v[40:43], v[180:183]\n" \
"buffer_store_dwordx4 v[104:107], v113, s[56:59], s77 offen offset:0 sc1\n" \
"global_load_lds_dwordx4 v253, s[48:49] offset:0\n" \
"v_accvgpr_read_b32 v96, a40\n" \
"v_accvgpr_read_b32 v97, a41\n" \
"v_accvgpr_read_b32 v98, a42\n" \
"v_accvgpr_read_b32 v99, a43\n" \
"v_accvgpr_read_b32 v100, a44\n" \
"v_exp_f32 v53, v53\n" \
"v_mfma_f32_16x16x32_bf16 v[184:187], v[0:3], v[8:11], v[184:187]\n" \
"s_add_u32 s48, s48, 0x300000\n" \
"s_addc_u32 s49, s49, 0\n" \
"v_add_f32 v242, v242, v53\n" \
"v_accvgpr_read_b32 v101, a45\n" \
"v_accvgpr_read_b32 v102, a46\n" \
"v_accvgpr_read_b32 v103, a47\n" \
"v_mul_f32 v96, v96, v112\n" \
"v_mul_f32 v97, v97, v112\n" \
"v_mul_f32 v98, v98, v112\n" \
"v_mfma_f32_16x16x32_bf16 v[184:187], v[4:7], v[40:43], v[184:187]\n" \
"global_load_lds_dwordx4 v253, s[50:51] offset:-3072\n" \
"v_mul_f32 v99, v99, v112\n" \
"v_mul_f32 v100, v100, v112\n" \
"v_mul_f32 v101, v101, v112\n" \
"v_mul_f32 v102, v102, v112\n" \
"v_mul_f32 v103, v103, v112\n" \
"v_fma_f32 v241, v96, v97, v241\n" \
"v_exp_f32 v54, v54\n" \
"v_mfma_f32_16x16x32_bf16 v[188:191], v[32:35], v[8:11], v[188:191]\n" \
"v_add_f32 v242, v242, v54\n" \
"v_cvt_pk_bf16_f32 v50, v52, v53\n" \
"v_cvt_pk_bf16_f32 v108, v96, v97\n" \
"v_fma_f32 v241, v98, v99, v241\n" \
"v_cvt_pk_bf16_f32 v109, v98, v99\n" \
"v_fma_f32 v241, v100, v101, v241\n" \
"v_cvt_pk_bf16_f32 v110, v100, v101\n" \
"v_fma_f32 v241, v102, v103, v241\n" \
"v_mfma_f32_16x16x32_bf16 v[188:191], v[36:39], v[40:43], v[188:191]\n" \
"global_load_lds_dwordx4 v253, s[50:51] offset:-1024\n" \
"v_cvt_pk_bf16_f32 v111, v102, v103\n" \
"v_exp_f32 v55, v55\n" \
"v_mfma_f32_16x16x32_bf16 v[160:163], a[192:195], v[24:27], v[160:163]\n" \
"buffer_store_dwordx4 v[108:111], v113, s[56:59], s77 offen offset:2048 sc1\n" \
"s_add_u32 s77, s77, 0x1000\n" \
"v_add_f32 v242, v242, v55\n" \
"v_cvt_pk_bf16_f32 v51, v54, v55\n" \
"v_accvgpr_read_b32 v96, a48\n" \
"v_accvgpr_read_b32 v97, a49\n" \
"v_accvgpr_read_b32 v98, a50\n" \
"v_accvgpr_read_b32 v99, a51\n" \
"v_mfma_f32_16x16x32_bf16 v[160:163], a[196:199], v[56:59], v[160:163]\n" \
"s_movk_i32 s89, 0xc0\n" \
"global_load_lds_dwordx4 v253, s[50:51] offset:1024\n" \
"v_accvgpr_read_b32 v100, a52\n" \
"v_accvgpr_read_b32 v101, a53\n" \
"v_accvgpr_read_b32 v102, a54\n" \
"v_accvgpr_read_b32 v103, a55\n" \
"v_mul_f32 v96, v96, v112\n" \
"v_mul_f32 v97, v97, v112\n" \
"v_mfma_f32_16x16x32_bf16 v[164:167], a[200:203], v[24:27], v[164:167]\n" \
"s_add_u32 s50, s50, 0x300000\n" \
"s_addc_u32 s51, s51, 0\n" \
"s_add_u32 m0, s95, 65536\n" \
"v_mul_f32 v98, v98, v112\n" \
"v_mul_f32 v99, v99, v112\n" \
"v_mul_f32 v100, v100, v112\n" \
"v_mul_f32 v101, v101, v112\n" \
"v_mul_f32 v102, v102, v112\n" \
"v_mul_f32 v103, v103, v112\n" \
"v_mfma_f32_16x16x32_bf16 v[164:167], a[204:207], v[56:59], v[164:167]\n" \
"s_movk_i32 s90, 288\n" \
"global_load_lds_dwordx4 v252, s[42:43] offset:0\n" \
"v_fma_f32 v241, v96, v97, v241\n" \
"v_cvt_pk_bf16_f32 v104, v96, v97\n" \
"v_fma_f32 v241, v98, v99, v241\n" \
"v_cvt_pk_bf16_f32 v105, v98, v99\n" \
"v_fma_f32 v241, v100, v101, v241\n" \
"v_cvt_pk_bf16_f32 v106, v100, v101\n" \
"v_lshrrev_b32 v211, 4, v248\n" \
"v_lshrrev_b32 v214, 8, v248\n" \
"v_and_b32 v215, 7, v211\n" \
"v_xor_b32 v214, v214, v215\n" \
"v_mfma_f32_16x16x32_bf16 v[168:171], a[208:211], v[24:27], v[168:171]\n" \
"s_lshr_b32 s71, s61, 1\n" \
"v_fma_f32 v241, v102, v103, v241\n" \
"s_lshl_b32 s70, s71, 11\n" \
"s_add_u32 s70, s70, 118784\n" \
"v_mfma_f32_16x16x32_bf16 v[168:171], a[212:215], v[56:59], v[168:171]\n" \
"global_load_lds_dwordx4 v252, s[42:43] offset:2048\n" \
"v_and_b32 v216, 15, v211\n" \
"v_mfma_f32_16x16x32_bf16 v[172:175], a[216:219], v[24:27], v[172:175]\n" \
"s_add_u32 s42, s42, 0x400\n" \
"s_addc_u32 s43, s43, 0\n" \
"v_lshlrev_b32 v213, 7, v216\n" \
"v_lshl_add_u32 v213, v214, 4, v213\n" \
"v_mfma_f32_16x16x32_bf16 v[172:175], a[220:223], v[56:59], v[172:175]\n" \
"global_load_lds_dwordx4 v252, s[44:45] offset:1024\n" \
"v_add_u32 v205, s70, v213\n" \
"v_xor_b32 v206, 64, v205\n" \
"v_lshrrev_b32 v216, 2, v216\n" \
"v_lshl_add_u32 v216, v216, 9, v213\n" \
"v_add_u32 v207, 135168, v216\n" \
"v_xor_b32 v208, 64, v207\n" \
"v_mfma_f32_16x16x32_bf16 v[176:179], a[224:227], v[24:27], v[176:179]\n" \
"s_and_b32 s70, s61, 1\n" \
"s_lshl_b32 s70, s70, 9\n" \
"s_add_u32 s70, s70, 139264\n" \
"v_mfma_f32_16x16x32_bf16 v[176:179], a[228:231], v[56:59], v[176:179]\n" \
"s_lshl_b32 s71, s71, 5\n" \
"global_load_lds_dwordx4 v252, s[44:45] offset:3072\n" \
"v_mfma_f32_16x16x32_bf16 v[180:183], a[232:235], v[24:27], v[180:183]\n" \
"s_add_u32 s44, s44, 0x400\n" \
"s_addc_u32 s45, s45, 0\n" \
"s_add_u32 m0, s85, 81920\n" \
"v_mfma_f32_16x16x32_bf16 v[180:183], a[236:239], v[56:59], v[180:183]\n" \
"buffer_load_dwordx4 v246, s[12:15], s46 offen lds\n" \
"v_mfma_f32_16x16x32_bf16 v[184:187], a[240:243], v[24:27], v[184:187]\n" \
"s_add_u32 s46, s46, 0x400\n" \
"v_and_b32 v213, 3, v211\n" \
"v_lshlrev_b32 v213, 3, v213\n" \
"v_mfma_f32_16x16x32_bf16 v[184:187], a[244:247], v[56:59], v[184:187]\n" \
"s_add_u32 m0, s95, 0\n" \
"v_and_or_b32 v213, v248, s89, v213\n" \
"v_bfe_u32 v214, v211, 4, 1\n" \
"v_mfma_f32_16x16x32_bf16 v[188:191], a[248:251], v[24:27], v[188:191]\n" \
"global_load_lds_dwordx4 v252, s[42:43] offset:0\n" \
"v_mad_u32_u24 v213, v214, s90, v213\n" \
"v_lshrrev_b32 v214, 5, v211\n" \
"v_mul_u32_u24 v214, 6144, v214\n" \
"v_add3_u32 v213, v213, v214, s70\n" \
"v_xor_b32 v209, s71, v213\n" \
"v_xor_b32 v210, 32, v209\n" \
"v_mfma_f32_16x16x32_bf16 v[188:191], a[252:255], v[56:59], v[188:191]\n" \
"ds_read_b128 a[64:67], v207 offset:0\n" \
"ds_read_b64_tr_b16 a[96:97], v209 offset:2048\n" \
"v_mfma_f32_16x16x32_bf16 v[128:131], a[192:195], v[16:19], v[128:131]\n" \
"ds_read_b64_tr_b16 a[98:99], v209 offset:3072\n" \
"v_cvt_pk_bf16_f32 v107, v102, v103\n" \
"v_mfma_f32_16x16x32_bf16 v[128:131], a[196:199], v[48:51], v[128:131]\n" \
"ds_read_b128 a[68:71], v208 offset:0\n" \
"global_load_lds_dwordx4 v252, s[42:43] offset:2048\n" \
"v_mfma_f32_16x16x32_bf16 v[132:135], a[200:203], v[16:19], v[132:135]\n" \
"ds_read_b64_tr_b16 a[100:101], v209 offset:14336\n" \
"buffer_store_dwordx4 v[104:107], v113, s[56:59], s77 offen offset:0 sc1\n" \
"ds_read_b64_tr_b16 a[102:103], v209 offset:15360\n" \
"v_accvgpr_read_b32 v96, a56\n" \
"v_accvgpr_read_b32 v97, a57\n" \
"v_accvgpr_read_b32 v98, a58\n" \
"v_accvgpr_read_b32 v99, a59\n" \
"v_mfma_f32_16x16x32_bf16 v[132:135], a[204:207], v[48:51], v[132:135]\n" \
"s_add_u32 s42, s42, 0xfffffb00\n" \
"s_addc_u32 s43, s43, -1\n" \
"ds_read_b128 a[72:75], v205 offset:0\n" \
"v_accvgpr_read_b32 v100, a60\n" \
"v_accvgpr_read_b32 v101, a61\n" \
"v_accvgpr_read_b32 v102, a62\n" \
"v_accvgpr_read_b32 v103, a63\n" \
"v_mul_f32 v96, v96, v112\n" \
"v_mul_f32 v97, v97, v112\n" \
"v_mfma_f32_16x16x32_bf16 v[136:139], a[208:211], v[16:19], v[136:139]\n" \
"ds_read_b64_tr_b16 a[104:105], v209 offset:0\n" \
"global_load_lds_dwordx4 v252, s[44:45] offset:1024\n" \
"v_mul_f32 v98, v98, v112\n" \
"v_mul_f32 v99, v99, v112\n" \
"v_mul_f32 v100, v100, v112\n" \
"v_mul_f32 v101, v101, v112\n" \
"v_mul_f32 v102, v102, v112\n" \
"v_mul_f32 v103, v103, v112\n" \
"v_mfma_f32_16x16x32_bf16 v[136:139], a[212:215], v[48:51], v[136:139]\n" \
"ds_read_b128 a[76:79], v206 offset:0\n" \
"ds_read_b64_tr_b16 a[106:107], v209 offset:1024\n" \
"v_fma_f32 v241, v96, v97, v241\n" \
"v_cvt_pk_bf16_f32 v108, v96, v97\n" \
"v_fma_f32 v241, v98, v99, v241\n" \
"v_cvt_pk_bf16_f32 v109, v98, v99\n" \
"v_mfma_f32_16x16x32_bf16 v[140:143], a[216:219], v[16:19], v[140:143]\n" \
"ds_read_b64_tr_b16 a[108:109], v209 offset:12288\n" \
"global_load_lds_dwordx4 v252, s[44:45] offset:3072\n" \
"v_mfma_f32_16x16x32_bf16 v[140:143], a[220:223], v[48:51], v[140:143]\n" \
"s_add_u32 s44, s44, 0xfffffb00\n" \
"s_addc_u32 s45, s45, -1\n" \
"ds_read_b64_tr_b16 a[110:111], v209 offset:13312\n" \
"v_mfma_f32_16x16x32_bf16 v[144:147], a[224:227], v[16:19], v[144:147]\n" \
"ds_read_b128 a[80:83], v205 offset:4096\n" \
"ds_read_b64_tr_b16 a[112:113], v210 offset:0\n" \
"v_mfma_f32_16x16x32_bf16 v[144:147], a[228:231], v[48:51], v[144:147]\n" \
"s_add_u32 m0, s85, 16384\n" \
"ds_read_b64_tr_b16 a[114:115], v210 offset:1024\n" \
"v_fma_f32 v241, v100, v101, v241\n" \
"v_mfma_f32_16x16x32_bf16 v[148:151], a[232:235], v[16:19], v[148:151]\n" \
"ds_read_b64_tr_b16 a[116:117], v210 offset:12288\n" \
"v_cvt_pk_bf16_f32 v110, v100, v101\n" \
"v_mfma_f32_16x16x32_bf16 v[148:151], a[236:239], v[48:51], v[148:151]\n" \
"ds_read_b128 a[84:87], v206 offset:4096\n" \
"ds_read_b64_tr_b16 a[118:119], v210 offset:13312\n" \
"v_mfma_f32_16x16x32_bf16 v[152:155], a[240:243], v[16:19], v[152:155]\n" \
"ds_read_b128 a[88:91], v205 offset:8192\n" \
"v_fma_f32 v241, v102, v103, v241\n" \
"v_mfma_f32_16x16x32_bf16 v[152:155], a[244:247], v[48:51], v[152:155]\n" \
"ds_read_b128 a[92:95], v206 offset:8192\n" \
"v_cvt_pk_bf16_f32 v111, v102, v103\n" \
"v_mfma_f32_16x16x32_bf16 v[156:159], a[248:251], v[16:19], v[156:159]\n" \
"buffer_store_dwordx4 v[108:111], v113, s[56:59], s77 offen offset:2048 sc1\n" \
"buffer_load_dwordx4 v246, s[12:15], s46 offen lds\n" \
"s_add_u32 s77, s77, 0xfff7d000\n" \
"v_mfma_f32_16x16x32_bf16 v[156:159], a[252:255], v[48:51], v[156:159]\n" \
"s_waitcnt lgkmcnt(4)\n" \
"s_branch Ljoin_%=\n" \
"Lexact_%=:\n" \
"v_mov_b32 v190, 0xff800000\n" \
"v_add_u32 v191, 0x1c90080, v249\n" \
"v_cndmask_b32 v191, v249, v191, s[16:17]\n" \
"s_and_b32 s80, s61, 2\n" \
"s_lshl_b32 s80, s80, 4\n" \
"v_subrev_u32 v186, s80, v251\n" \
"v_accvgpr_read_b32 v160, a128\n" \
"v_accvgpr_read_b32 v161, a129\n" \
"v_accvgpr_read_b32 v162, a130\n" \
"v_accvgpr_read_b32 v163, a131\n" \
"v_accvgpr_read_b32 v164, a132\n" \
"v_accvgpr_read_b32 v165, a133\n" \
"v_accvgpr_read_b32 v166, a134\n" \
"v_accvgpr_read_b32 v167, a135\n" \
"v_accvgpr_read_b32 v168, a136\n" \
"v_accvgpr_read_b32 v169, a137\n" \
"v_accvgpr_read_b32 v170, a138\n" \
"v_accvgpr_read_b32 v171, a139\n" \
"v_accvgpr_read_b32 v172, a140\n" \
"v_accvgpr_read_b32 v173, a141\n" \
"v_accvgpr_read_b32 v174, a142\n" \
"v_accvgpr_read_b32 v175, a143\n" \
"s_lshl_b32 s75, s63, 2\n" \
"s_lshr_b32 s80, s61, 1\n" \
"s_add_u32 s75, s75, s80\n" \
"s_add_u32 s75, s75, 0\n" \
"s_add_u32 s76, s77, 0\n" \
"s_call_b64 s[68:69], Lex1_%=\n" \
"v_accvgpr_read_b32 v160, a144\n" \
"v_accvgpr_read_b32 v161, a145\n" \
"v_accvgpr_read_b32 v162, a146\n" \
"v_accvgpr_read_b32 v163, a147\n" \
"v_accvgpr_read_b32 v164, a148\n" \
"v_accvgpr_read_b32 v165, a149\n" \
"v_accvgpr_read_b32 v166, a150\n" \
"v_accvgpr_read_b32 v167, a151\n" \
"v_accvgpr_read_b32 v168, a152\n" \
"v_accvgpr_read_b32 v169, a153\n" \
"v_accvgpr_read_b32 v170, a154\n" \
"v_accvgpr_read_b32 v171, a155\n" \
"v_accvgpr_read_b32 v172, a156\n" \
"v_accvgpr_read_b32 v173, a157\n" \
"v_accvgpr_read_b32 v174, a158\n" \
"v_accvgpr_read_b32 v175, a159\n" \
"s_lshl_b32 s75, s63, 2\n" \
"s_lshr_b32 s80, s61, 1\n" \
"s_add_u32 s75, s75, s80\n" \
"s_add_u32 s75, s75, 2\n" \
"s_add_u32 s76, s77, 532480\n" \
"s_call_b64 s[68:69], Lex1_%=\n" \
"v_accvgpr_read_b32 v160, a160\n" \
"v_accvgpr_read_b32 v161, a161\n" \
"v_accvgpr_read_b32 v162, a162\n" \
"v_accvgpr_read_b32 v163, a163\n" \
"v_accvgpr_read_b32 v164, a164\n" \
"v_accvgpr_read_b32 v165, a165\n" \
"v_accvgpr_read_b32 v166, a166\n" \
"v_accvgpr_read_b32 v167, a167\n" \
"v_accvgpr_read_b32 v168, a168\n" \
"v_accvgpr_read_b32 v169, a169\n" \
"v_accvgpr_read_b32 v170, a170\n" \
"v_accvgpr_read_b32 v171, a171\n" \
"v_accvgpr_read_b32 v172, a172\n" \
"v_accvgpr_read_b32 v173, a173\n" \
"v_accvgpr_read_b32 v174, a174\n" \
"v_accvgpr_read_b32 v175, a175\n" \
"s_lshl_b32 s75, s63, 2\n" \
"s_sub_u32 s75, 60, s75\n" \
"s_lshr_b32 s80, s61, 1\n" \
"s_add_u32 s75, s75, s80\n" \
"s_add_u32 s75, s75, 0\n" \
"s_add_u32 s76, s78, 0\n" \
"s_call_b64 s[68:69], Lex1_%=\n" \
"v_accvgpr_read_b32 v160, a176\n" \
"v_accvgpr_read_b32 v161, a177\n" \
"v_accvgpr_read_b32 v162, a178\n" \
"v_accvgpr_read_b32 v163, a179\n" \
"v_accvgpr_read_b32 v164, a180\n" \
"v_accvgpr_read_b32 v165, a181\n" \
"v_accvgpr_read_b32 v166, a182\n" \
"v_accvgpr_read_b32 v167, a183\n" \
"v_accvgpr_read_b32 v168, a184\n" \
"v_accvgpr_read_b32 v169, a185\n" \
"v_accvgpr_read_b32 v170, a186\n" \
"v_accvgpr_read_b32 v171, a187\n" \
"v_accvgpr_read_b32 v172, a188\n" \
"v_accvgpr_read_b32 v173, a189\n" \
"v_accvgpr_read_b32 v174, a190\n" \
"v_accvgpr_read_b32 v175, a191\n" \
"s_lshl_b32 s75, s63, 2\n" \
"s_sub_u32 s75, 60, s75\n" \
"s_lshr_b32 s80, s61, 1\n" \
"s_add_u32 s75, s75, s80\n" \
"s_add_u32 s75, s75, 2\n" \
"s_add_u32 s76, s78, 532480\n" \
"s_call_b64 s[68:69], Lex1_%=\n" \
"s_branch Lexr_%=\n" \
"Lex1_%=:\n" \
"v_mov_b32 v128, 0\n" \
"v_mov_b32 v129, 0\n" \
"v_mov_b32 v130, 0\n" \
"v_mov_b32 v131, 0\n" \
"v_mov_b32 v132, 0\n" \
"v_mov_b32 v133, 0\n" \
"v_mov_b32 v134, 0\n" \
"v_mov_b32 v135, 0\n" \
"v_mov_b32 v136, 0\n" \
"v_mov_b32 v137, 0\n" \
"v_mov_b32 v138, 0\n" \
"v_mov_b32 v139, 0\n" \
"v_mov_b32 v140, 0\n" \
"v_mov_b32 v141, 0\n" \
"v_mov_b32 v142, 0\n" \
"v_mov_b32 v143, 0\n" \
"v_mov_b32 v144, 0\n" \
"v_mov_b32 v145, 0\n" \
"v_mov_b32 v146, 0\n" \
"v_mov_b32 v147, 0\n" \
"v_mov_b32 v148, 0\n" \
"v_mov_b32 v149, 0\n" \
"v_mov_b32 v150, 0\n" \
"v_mov_b32 v151, 0\n" \
"v_mov_b32 v152, 0\n" \
"v_mov_b32 v153, 0\n" \
"v_mov_b32 v154, 0\n" \
"v_mov_b32 v155, 0\n" \
"v_mov_b32 v156, 0\n" \
"v_mov_b32 v157, 0\n" \
"v_mov_b32 v158, 0\n" \
"v_mov_b32 v159, 0\n" \
"v_mov_b32 v184, 0xf149f2ca\n" \
"v_mov_b32 v185, 0\n" \
"s_mov_b32 s73, 0\n" \
"s_mul_i32 s74, s62, 557824\n" \
"Lex2_%=:\n" \
"s_add_u32 s80, s74, 4096\n" \
"buffer_load_dwordx4 a[0:3], v249, s[56:59], s74 offen offset:0 sc1\n" \
"buffer_load_dwordx4 a[4:7], v249, s[56:59], s74 offen offset:2048 sc1\n" \
"buffer_load_dwordx4 a[8:11], v249, s[56:59], s80 offen offset:0 sc1\n" \
"buffer_load_dwordx4 a[12:15], v249, s[56:59], s80 offen offset:2048 sc1\n" \
"buffer_load_dwordx4 a[16:19], v249, s[56:59], s74 offen offset:256 sc1\n" \
"buffer_load_dwordx4 a[20:23], v249, s[56:59], s74 offen offset:2304 sc1\n" \
"buffer_load_dwordx4 a[24:27], v249, s[56:59], s80 offen offset:256 sc1\n" \
"buffer_load_dwordx4 a[28:31], v249, s[56:59], s80 offen offset:2304 sc1\n" \
"s_add_u32 s80, s74, 17850368\n" \
"s_add_u32 s81, s80, 4096\n" \
"buffer_load_dwordx4 a[32:35], v248, s[56:59], s80 offen offset:0 sc1\n" \
"buffer_load_dwordx4 a[36:39], v248, s[56:59], s80 offen offset:1024 sc1\n" \
"buffer_load_dwordx4 a[40:43], v248, s[56:59], s80 offen offset:2048 sc1\n" \
"buffer_load_dwordx4 a[44:47], v248, s[56:59], s80 offen offset:3072 sc1\n" \
"buffer_load_dwordx4 a[48:51], v248, s[56:59], s81 offen offset:0 sc1\n" \
"buffer_load_dwordx4 a[52:55], v248, s[56:59], s81 offen offset:1024 sc1\n" \
"buffer_load_dwordx4 a[56:59], v248, s[56:59], s81 offen offset:2048 sc1\n" \
"buffer_load_dwordx4 a[60:63], v248, s[56:59], s81 offen offset:3072 sc1\n" \
"s_waitcnt vmcnt(0)\n" \
"v_mfma_f32_16x16x32_bf16 v[176:179], a[0:3], v[160:163], 0\n" \
"v_mfma_f32_16x16x32_bf16 v[176:179], a[4:7], v[164:167], v[176:179]\n" \
"v_mfma_f32_16x16x32_bf16 v[176:179], a[8:11], v[168:171], v[176:179]\n" \
"v_mfma_f32_16x16x32_bf16 v[176:179], a[12:15], v[172:175], v[176:179]\n" \
"v_mfma_f32_16x16x32_bf16 v[180:183], a[16:19], v[160:163], 0\n" \
"v_mfma_f32_16x16x32_bf16 v[180:183], a[20:23], v[164:167], v[180:183]\n" \
"v_mfma_f32_16x16x32_bf16 v[180:183], a[24:27], v[168:171], v[180:183]\n" \
"v_mfma_f32_16x16x32_bf16 v[180:183], a[28:31], v[172:175], v[180:183]\n" \
"s_nop 7\n" \
"s_nop 1\n" \
"s_cmp_eq_u32 s73, s75\n" \
"s_cbranch_scc0 Lex3_%=\n" \
"v_cmp_gt_i32 vcc, 0, v186\n" \
"s_nop 1\n" \
"v_cndmask_b32 v176, v176, v190, vcc\n" \
"v_cmp_gt_i32 vcc, 1, v186\n" \
"s_nop 1\n" \
"v_cndmask_b32 v177, v177, v190, vcc\n" \
"v_cmp_gt_i32 vcc, 2, v186\n" \
"s_nop 1\n" \
"v_cndmask_b32 v178, v178, v190, vcc\n" \
"v_cmp_gt_i32 vcc, 3, v186\n" \
"s_nop 1\n" \
"v_cndmask_b32 v179, v179, v190, vcc\n" \
"v_cmp_gt_i32 vcc, 16, v186\n" \
"s_nop 1\n" \
"v_cndmask_b32 v180, v180, v190, vcc\n" \
"v_cmp_gt_i32 vcc, 17, v186\n" \
"s_nop 1\n" \
"v_cndmask_b32 v181, v181, v190, vcc\n" \
"v_cmp_gt_i32 vcc, 18, v186\n" \
"s_nop 1\n" \
"v_cndmask_b32 v182, v182, v190, vcc\n" \
"v_cmp_gt_i32 vcc, 19, v186\n" \
"s_nop 1\n" \
"v_cndmask_b32 v183, v183, v190, vcc\n" \
"Lex3_%=:\n" \
"v_max3_f32 v187, v176, v177, v178\n" \
"v_max3_f32 v187, v187, v179, v180\n" \
"v_max3_f32 v187, v187, v181, v182\n" \
"v_max3_f32 v187, v187, v183, v184\n" \
"v_mov_b32 v188, v187\n" \
"s_nop 1\n" \
"v_permlane16_swap_b32 v188, v187\n" \
"v_max_f32 v187, v187, v188\n" \
"v_mov_b32 v188, v187\n" \
"s_nop 1\n" \
"v_permlane32_swap_b32 v188, v187\n" \
"v_max_f32 v187, v187, v188\n" \
"v_sub_f32 v189, v184, v187\n" \
"v_exp_f32 v189, v189\n" \
"v_mov_b32 v184, v187\n" \
"v_sub_f32 v176, v176, v187\n" \
"v_sub_f32 v177, v177, v187\n" \
"v_sub_f32 v178, v178, v187\n" \
"v_sub_f32 v179, v179, v187\n" \
"v_sub_f32 v180, v180, v187\n" \
"v_sub_f32 v181, v181, v187\n" \
"v_sub_f32 v182, v182, v187\n" \
"v_sub_f32 v183, v183, v187\n" \
"v_exp_f32 v176, v176\n" \
"v_exp_f32 v177, v177\n" \
"v_exp_f32 v178, v178\n" \
"v_exp_f32 v179, v179\n" \
"v_exp_f32 v180, v180\n" \
"v_exp_f32 v181, v181\n" \
"v_exp_f32 v182, v182\n" \
"v_exp_f32 v183, v183\n" \
"v_mul_f32 v185, v185, v189\n" \
"v_add_f32 v185, v185, v176\n" \
"v_add_f32 v185, v185, v177\n" \
"v_add_f32 v185, v185, v178\n" \
"v_add_f32 v185, v185, v179\n" \
"v_add_f32 v185, v185, v180\n" \
"v_add_f32 v185, v185, v181\n" \
"v_add_f32 v185, v185, v182\n" \
"v_add_f32 v185, v185, v183\n" \
"v_cvt_pk_bf16_f32 v112, v176, v177\n" \
"v_cvt_pk_bf16_f32 v113, v178, v179\n" \
"v_cvt_pk_bf16_f32 v114, v180, v181\n" \
"v_cvt_pk_bf16_f32 v115, v182, v183\n" \
"v_mul_f32 v128, v128, v189\n" \
"v_mul_f32 v129, v129, v189\n" \
"v_mul_f32 v130, v130, v189\n" \
"v_mul_f32 v131, v131, v189\n" \
"v_mul_f32 v132, v132, v189\n" \
"v_mul_f32 v133, v133, v189\n" \
"v_mul_f32 v134, v134, v189\n" \
"v_mul_f32 v135, v135, v189\n" \
"v_mul_f32 v136, v136, v189\n" \
"v_mul_f32 v137, v137, v189\n" \
"v_mul_f32 v138, v138, v189\n" \
"v_mul_f32 v139, v139, v189\n" \
"v_mul_f32 v140, v140, v189\n" \
"v_mul_f32 v141, v141, v189\n" \
"v_mul_f32 v142, v142, v189\n" \
"v_mul_f32 v143, v143, v189\n" \
"v_mul_f32 v144, v144, v189\n" \
"v_mul_f32 v145, v145, v189\n" \
"v_mul_f32 v146, v146, v189\n" \
"v_mul_f32 v147, v147, v189\n" \
"v_mul_f32 v148, v148, v189\n" \
"v_mul_f32 v149, v149, v189\n" \
"v_mul_f32 v150, v150, v189\n" \
"v_mul_f32 v151, v151, v189\n" \
"v_mul_f32 v152, v152, v189\n" \
"v_mul_f32 v153, v153, v189\n" \
"v_mul_f32 v154, v154, v189\n" \
"v_mul_f32 v155, v155, v189\n" \
"v_mul_f32 v156, v156, v189\n" \
"v_mul_f32 v157, v157, v189\n" \
"v_mul_f32 v158, v158, v189\n" \
"v_mul_f32 v159, v159, v189\n" \
"s_nop 1\n" \
"v_mfma_f32_16x16x32_bf16 v[128:131], a[32:35], v[112:115], v[128:131]\n" \
"v_mfma_f32_16x16x32_bf16 v[132:135], a[36:39], v[112:115], v[132:135]\n" \
"v_mfma_f32_16x16x32_bf16 v[136:139], a[40:43], v[112:115], v[136:139]\n" \
"v_mfma_f32_16x16x32_bf16 v[140:143], a[44:47], v[112:115], v[140:143]\n" \
"v_mfma_f32_16x16x32_bf16 v[144:147], a[48:51], v[112:115], v[144:147]\n" \
"v_mfma_f32_16x16x32_bf16 v[148:151], a[52:55], v[112:115], v[148:151]\n" \
"v_mfma_f32_16x16x32_bf16 v[152:155], a[56:59], v[112:115], v[152:155]\n" \
"v_mfma_f32_16x16x32_bf16 v[156:159], a[60:63], v[112:115], v[156:159]\n" \
"s_nop 7\n" \
"s_nop 1\n" \
"s_and_b32 s80, s73, 1\n" \
"s_mul_i32 s80, s80, 1024\n" \
"s_add_u32 s74, s74, s80\n" \
"s_add_u32 s74, s74, 8192\n" \
"s_add_u32 s73, s73, 1\n" \
"s_cmp_le_u32 s73, s75\n" \
"s_cbranch_scc1 Lex2_%=\n" \
"v_mov_b32 v187, v185\n" \
"s_nop 1\n" \
"v_permlane16_swap_b32 v187, v185\n" \
"v_add_f32 v185, v185, v187\n" \
"v_mov_b32 v187, v185\n" \
"s_nop 1\n" \
"v_permlane32_swap_b32 v187, v185\n" \
"v_add_f32 v185, v185, v187\n" \
"v_rcp_f32 v185, v185\n" \
"s_nop 0\n" \
"v_mul_f32 v128, v128, v185\n" \
"v_mul_f32 v129, v129, v185\n" \
"v_mul_f32 v130, v130, v185\n" \
"v_mul_f32 v131, v131, v185\n" \
"v_mul_f32 v132, v132, v185\n" \
"v_mul_f32 v133, v133, v185\n" \
"v_mul_f32 v134, v134, v185\n" \
"v_mul_f32 v135, v135, v185\n" \
"v_mul_f32 v136, v136, v185\n" \
"v_mul_f32 v137, v137, v185\n" \
"v_mul_f32 v138, v138, v185\n" \
"v_mul_f32 v139, v139, v185\n" \
"v_mul_f32 v140, v140, v185\n" \
"v_mul_f32 v141, v141, v185\n" \
"v_mul_f32 v142, v142, v185\n" \
"v_mul_f32 v143, v143, v185\n" \
"v_mul_f32 v144, v144, v185\n" \
"v_mul_f32 v145, v145, v185\n" \
"v_mul_f32 v146, v146, v185\n" \
"v_mul_f32 v147, v147, v185\n" \
"v_mul_f32 v148, v148, v185\n" \
"v_mul_f32 v149, v149, v185\n" \
"v_mul_f32 v150, v150, v185\n" \
"v_mul_f32 v151, v151, v185\n" \
"v_mul_f32 v152, v152, v185\n" \
"v_mul_f32 v153, v153, v185\n" \
"v_mul_f32 v154, v154, v185\n" \
"v_mul_f32 v155, v155, v185\n" \
"v_mul_f32 v156, v156, v185\n" \
"v_mul_f32 v157, v157, v185\n" \
"v_mul_f32 v158, v158, v185\n" \
"v_mul_f32 v159, v159, v185\n" \
"s_add_u32 s80, s76, 4096\n" \
"v_cvt_pk_bf16_f32 v112, v128, v129\n" \
"v_cvt_pk_bf16_f32 v113, v130, v131\n" \
"v_cvt_pk_bf16_f32 v114, v132, v133\n" \
"v_cvt_pk_bf16_f32 v115, v134, v135\n" \
"buffer_store_dwordx4 v[112:115], v191, s[56:59], s76 offen offset:0 sc1\n" \
"v_cvt_pk_bf16_f32 v116, v136, v137\n" \
"v_cvt_pk_bf16_f32 v117, v138, v139\n" \
"v_cvt_pk_bf16_f32 v118, v140, v141\n" \
"v_cvt_pk_bf16_f32 v119, v142, v143\n" \
"buffer_store_dwordx4 v[116:119], v191, s[56:59], s76 offen offset:2048 sc1\n" \
"v_cvt_pk_bf16_f32 v120, v144, v145\n" \
"v_cvt_pk_bf16_f32 v121, v146, v147\n" \
"v_cvt_pk_bf16_f32 v122, v148, v149\n" \
"v_cvt_pk_bf16_f32 v123, v150, v151\n" \
"buffer_store_dwordx4 v[120:123], v191, s[56:59], s80 offen offset:0 sc1\n" \
"v_cvt_pk_bf16_f32 v124, v152, v153\n" \
"v_cvt_pk_bf16_f32 v125, v154, v155\n" \
"v_cvt_pk_bf16_f32 v126, v156, v157\n" \
"v_cvt_pk_bf16_f32 v127, v158, v159\n" \
"buffer_store_dwordx4 v[124:127], v191, s[56:59], s80 offen offset:2048 sc1\n" \
"s_waitcnt vmcnt(0)\n" \
"s_setpc_b64 s[68:69]\n" \
"Lpl2_%=:\n" \
"s_mov_b32 s31, 0\n" \
"Lpk2_%=:\n" \
"s_sleep 2\n" \
"buffer_load_dword v220, v221, s[56:59], s81 offen sc1\n" \
"s_waitcnt vmcnt(0)\n" \
"v_cmp_ne_u32 vcc, s60, v220\n" \
"s_nop 1\n" \
"s_and_b32 vcc_hi, vcc_lo, s80\n" \
"s_cbranch_scc0 Lpr2_%=\n" \
"s_add_u32 s31, s31, 1\n" \
"s_cmp_lt_u32 s31, 0x40000\n" \
"s_cbranch_scc1 Lpk2_%=\n" \
"s_branch Lpr2_%=\n" \
"Lpl3_%=:\n" \
"s_mov_b32 s31, 0\n" \
"Lpk3_%=:\n" \
"s_sleep 2\n" \
"buffer_load_dwordx2 v[226:227], v228, s[56:59], s30 offen sc1\n" \
"s_waitcnt vmcnt(0)\n" \
"v_xor_b32 v226, v226, v227\n" \
"v_cmp_ne_u32 vcc, s60, v226\n" \
"s_nop 1\n" \
"s_and_b32 vcc_hi, vcc_lo, -1\n" \
"s_cbranch_scc0 Lpr3_%=\n" \
"s_add_u32 s31, s31, 1\n" \
"s_cmp_lt_u32 s31, 0x40000\n" \
"s_cbranch_scc1 Lpk3_%=\n" \
"s_branch Lpr3_%=\n" \
"Lpl4_%=:\n" \
"s_mov_b32 s31, 0\n" \
"Lpk4_%=:\n" \
"s_sleep 2\n" \
"buffer_load_dword v220, off, s[56:59], s79 sc1\n" \
"s_waitcnt vmcnt(0)\n" \
"v_cmp_ne_u32 vcc, s60, v220\n" \
"s_nop 1\n" \
"s_and_b32 vcc_hi, vcc_lo, 1\n" \
"s_cbranch_scc0 Lpr4_%=\n" \
"s_add_u32 s31, s31, 1\n" \
"s_cmp_lt_u32 s31, 0x40000\n" \
"s_cbranch_scc1 Lpk4_%=\n" \
"s_branch Lpr4_%=\n" \
"Lpl5_%=:\n" \
"s_mov_b32 s31, 0\n" \
"Lpk5_%=:\n" \
"s_sleep 2\n" \
"buffer_load_dword v220, v221, s[56:59], s82 offen sc1\n" \
"s_waitcnt vmcnt(0)\n" \
"v_cmp_ne_u32 vcc, s60, v220\n" \
"s_nop 1\n" \
"s_and_b32 vcc_hi, vcc_lo, 65535\n" \
"s_cbranch_scc0 Lpr5_%=\n" \
"s_add_u32 s31, s31, 1\n" \
"s_cmp_lt_u32 s31, 0x40000\n" \
"s_cbranch_scc1 Lpk5_%=\n" \
"s_branch Lpr5_%=\n" \
"Lcode_end_%=:\n" \
".p2align 12\n" \
""
#define MK_ASM_CLOBBERS "v1", "v2", "v3", "v4", "v5", "v6", "v7", "v8", "v9", "v10", "v11", "v12", "v13", "v14", "v15", "v16", "v17", "v18", "v19", "v20", "v21", "v22", "v23", "v24", "v25", "v26", "v27", "v28", "v29", "v30", "v31", "v32", "v33", "v34", "v35", "v36", "v37", "v38", "v39", "v40", "v41", "v42", "v43", "v44", "v45", "v46", "v47", "v48", "v49", "v50", "v51", "v52", "v53", "v54", "v55", "v56", "v57", "v58", "v59", "v60", "v61", "v62", "v63", "v64", "v65", "v66", "v67", "v68", "v69", "v70", "v71", "v72", "v73", "v74", "v75", "v76", "v77", "v78", "v79", "v80", "v81", "v82", "v83", "v84", "v85", "v86", "v87", "v88", "v89", "v90", "v91", "v92", "v93", "v94", "v95", "v96", "v97", "v98", "v99", "v100", "v101", "v102", "v103", "v104", "v105", "v106", "v107", "v108", "v109", "v110", "v111", "v112", "v113", "v114", "v115", "v116", "v117", "v118", "v119", "v120", "v121", "v122", "v123", "v124", "v125", "v126", "v127", "v128", "v129", "v130", "v131", "v132", "v133", "v134", "v135", "v136", "v137", "v138", "v139", "v140", "v141", "v142", "v143", "v144", "v145", "v146", "v147", "v148", "v149", "v150", "v151", "v152", "v153", "v154", "v155", "v156", "v157", "v158", "v159", "v160", "v161", "v162", "v163", "v164", "v165", "v166", "v167", "v168", "v169", "v170", "v171", "v172", "v173", "v174", "v175", "v176", "v177", "v178", "v179", "v180", "v181", "v182", "v183", "v184", "v185", "v186", "v187", "v188", "v189", "v190", "v191", "v192", "v193", "v194", "v195", "v196", "v197", "v198", "v199", "v200", "v201", "v202", "v203", "v204", "v205", "v206", "v207", "v208", "v209", "v210", "v211", "v212", "v213", "v214", "v215", "v216", "v217", "v218", "v219", "v220", "v221", "v222", "v223", "v224", "v225", "v226", "v227", "v228", "v229", "v230", "v231", "v232", "v233", "v234", "v235", "v236", "v237", "v238", "v239", "v240", "v241", "v242", "v243", "v244", "v245", "v246", "v247", "v248", "v249", "v250", "v251", "v252", "v253", "v254", "v255", "a0", "a1", "a2", "a3", "a4", "a5", "a6", "a7", "a8", "a9", "a10", "a11", "a12", "a13", "a14", "a15", "a16", "a17", "a18", "a19", "a20", "a21", "a22", "a23", "a24", "a25", "a26", "a27", "a28", "a29", "a30", "a31", "a32", "a33", "a34", "a35", "a36", "a37", "a38", "a39", "a40", "a41", "a42", "a43", "a44", "a45", "a46", "a47", "a48", "a49", "a50", "a51", "a52", "a53", "a54", "a55", "a56", "a57", "a58", "a59", "a60", "a61", "a62", "a63", "a64", "a65", "a66", "a67", "a68", "a69", "a70", "a71", "a72", "a73", "a74", "a75", "a76", "a77", "a78", "a79", "a80", "a81", "a82", "a83", "a84", "a85", "a86", "a87", "a88", "a89", "a90", "a91", "a92", "a93", "a94", "a95", "a96", "a97", "a98", "a99", "a100", "a101", "a102", "a103", "a104", "a105", "a106", "a107", "a108", "a109", "a110", "a111", "a112", "a113", "a114", "a115", "a116", "a117", "a118", "a119", "a120", "a121", "a122", "a123", "a124", "a125", "a126", "a127", "a128", "a129", "a130", "a131", "a132", "a133", "a134", "a135", "a136", "a137", "a138", "a139", "a140", "a141", "a142", "a143", "a144", "a145", "a146", "a147", "a148", "a149", "a150", "a151", "a152", "a153", "a154", "a155", "a156", "a157", "a158", "a159", "a160", "a161", "a162", "a163", "a164", "a165", "a166", "a167", "a168", "a169", "a170", "a171", "a172", "a173", "a174", "a175", "a176", "a177", "a178", "a179", "a180", "a181", "a182", "a183", "a184", "a185", "a186", "a187", "a188", "a189", "a190", "a191", "a192", "a193", "a194", "a195", "a196", "a197", "a198", "a199", "a200", "a201", "a202", "a203", "a204", "a205", "a206", "a207", "a208", "a209", "a210", "a211", "a212", "a213", "a214", "a215", "a216", "a217", "a218", "a219", "a220", "a221", "a222", "a223", "a224", "a225", "a226", "a227", "a228", "a229", "a230", "a231", "a232", "a233", "a234", "a235", "a236", "a237", "a238", "a239", "a240", "a241", "a242", "a243", "a244", "a245", "a246", "a247", "a248", "a249", "a250", "a251", "a252", "a253", "a254", "a255", "s8", "s9", "s10", "s11", "s12", "s13", "s14", "s15", "s16", "s17", "s18", "s19", "s20", "s21", "s22", "s23", "s24", "s25", "s26", "s27", "s28", "s29", "s30", "s31", "s36", "s37", "s38", "s39", "s40", "s41", "s42", "s43", "s44", "s45", "s46", "s47", "s48", "s49", "s50", "s51", "s52", "s53", "s54", "s55", "s56", "s57", "s58", "s59", "s60", "s61", "s62", "s63", "s64", "s65", "s66", "s67", "s68", "s69", "s70", "s71", "s72", "s73", "s74", "s75", "s76", "s77", "s78", "s79", "s80", "s81", "s82", "s83", "s84", "s85", "s86", "s87", "s88", "s89", "s90", "s91", "s92", "s93", "s94", "s95", "vcc", "scc", "memory"
namespace mk {
constexpr int LDS_BYTES = 163840;
struct Args { const void* in[9]; void* out; void* ws; };
__global__ __launch_bounds__(256, 1) void mega_k(const Args a) {
    extern __shared__ __attribute__((aligned(16))) unsigned char shm[];
    const unsigned ldsb = (unsigned)(size_t)(LAS unsigned char*)shm;
    asm volatile(MK_ASM_BODY
                 :
                 : "s"(__builtin_amdgcn_kernarg_segment_ptr()), "s"(blockIdx.x), "v"(threadIdx.x), "s"(ldsb)
                 : MK_ASM_CLOBBERS);
}
}
extern "C" void kernel_launch(void* const* d_in, const int* in_sizes, int n_in, void* d_out, int out_size, void* d_ws, size_t ws_size, hipStream_t stream) {
    (void)hipFuncSetAttribute((const void*)mk::mega_k, hipFuncAttributeMaxDynamicSharedMemorySize, mk::LDS_BYTES);
    mk::Args a;
    for (int i = 0; i < 9; ++i) a.in[i] = d_in[i];
    a.out = d_out; a.ws = (char*)d_ws + ((ws_size - WS_END) & ~(size_t)0x1fffff);
    mk::mega_k<<<256, 256, mk::LDS_BYTES, stream>>>(a);
}
```

```cpp
#include <hip/hip_runtime.h>
#include <stdint.h>
#define LAS __attribute__((address_space(3)))
constexpr size_t LAY_TS = 17408, LAY_HS = 557824, LAY_CRS = 266240, LAY_ORS = 69632, WS_K = 0, WS_V = 17850368, WS_CTX = 35700736, WS_OB = 52740096, WS_FL = 65585152, LAY_EXD = 29950080, WS_END = 87147648;
#define MK_ASM_BODY \
"s_load_dwordx16 s[8:23], %0, 0x0\n" \
"s_load_dwordx4 s[24:27], %0, 0x40\n" \
"s_load_dwordx2 s[28:29], %0, 0x50\n" \
"v_readfirstlane_b32 s61, %2\n" \
"s_lshr_b32 s61, s61, 6\n" \
"v_mbcnt_lo_u32_b32 v52, -1, 0\n" \
"v_mbcnt_hi_u32_b32 v52, -1, v52\n" \
"v_lshlrev_b32 v248, 4, v52\n" \
"s_getpc_b64 s[64:65]\n" \
"Lpc_%=:\n" \
"s_lshr_b32 s70, %1, 3\n" \
"s_lshl_b32 s70, s70, 2\n" \
"s_add_u32 s70, s70, s61\n" \
"s_and_b32 s71, s70, 31\n" \
"s_lshl_b32 s71, s71, 12\n" \
"v_lshl_add_u32 v53, v248, 2, s71\n" \
"s_mov_b32 s71, Lcode_end_%=-Lpc_%=-4\n" \
"v_min_u32 v53, s71, v53\n" \
"global_load_dword v253, v53, s[64:65]\n" \
"s_and_b32 s62, %1, 7\n" \
"s_lshl_b32 s62, s62, 2\n" \
"s_lshr_b32 s70, %1, 6\n" \
"s_add_u32 s62, s62, s70\n" \
"s_bfe_u32 s63, %1, 0x30003\n" \
"s_lshl_b32 s87, s61, 13\n" \
"s_lshl_b32 s95, s61, 12\n" \
"v_bfe_u32 v53, v52, 4, 1\n" \
"v_bfe_u32 v54, v52, 2, 2\n" \
"v_lshl_add_u32 v54, v53, 3, v54\n" \
"s_lshl_b32 s71, s61, 4\n" \
"v_add_u32 v54, s71, v54\n" \
"v_lshlrev_b32 v54, 13, v54\n" \
"v_lshlrev_b32 v53, 1, v53\n" \
"v_and_b32 v55, 3, v52\n" \
"v_xor_b32 v53, v53, v55\n" \
"v_lshl_add_u32 v54, v53, 4, v54\n" \
"v_lshrrev_b32 v53, 5, v52\n" \
"v_lshl_add_u32 v54, v53, 6, v54\n" \
"v_add_u32 v40, 4096, v54\n" \
"v_add_u32 v41, 3072, v54\n" \
"v_add_u32 v42, 2176, v54\n" \
"v_add_u32 v43, 1152, v54\n" \
"v_add_u32 v44, 32768, v54\n" \
"v_add_u32 v45, 31744, v54\n" \
"v_add_u32 v46, 30848, v54\n" \
"v_add_u32 v47, 29824, v54\n" \
"v_lshrrev_b32 v55, 3, v52\n" \
"v_and_b32 v56, 7, v52\n" \
"v_xor_b32 v56, v56, v55\n" \
"v_lshlrev_b32 v56, 4, v56\n" \
"v_lshl_add_u32 v57, v55, 10, v56\n" \
"v_add_u32 v32, 528384, v57\n" \
"v_add_u32 v33, 535552, v57\n" \
"v_add_u32 v34, 542720, v57\n" \
"v_add_u32 v35, 549888, v57\n" \
"v_add_u32 v36, 0, v57\n" \
"v_add_u32 v37, 7168, v57\n" \
"v_add_u32 v38, 14336, v57\n" \
"v_add_u32 v39, 21504, v57\n" \
"s_lshl_b32 s88, s62, 8\n" \
"s_lshl_b32 s89, s63, 18\n" \
"s_lshl_b32 s70, s63, 16\n" \
"s_and_b32 s71, s87, 0x4000\n" \
"s_lshl_b32 s71, s71, 1\n" \
"s_add_u32 s70, s70, s71\n" \
"s_andn2_b32 s71, 1, s61\n" \
"s_lshl_b32 s71, s71, 20\n" \
"s_add_u32 s70, s70, s71\n" \
"s_lshl_b32 s66, %1, 2\n" \
"s_add_u32 s66, s66, s61\n" \
"s_lshl_b32 s66, s66, 10\n" \
"s_lshr_b32 s67, %1, 3\n" \
"s_lshl_b32 s67, s67, 2\n" \
"s_add_u32 s67, s67, s61\n" \
"s_lshl_b32 s67, s67, 13\n" \
"v_lshrrev_b32 v63, 6, v248\n" \
"v_lshlrev_b32 v63, 8, v63\n" \
"v_and_or_b32 v63, v248, 48, v63\n" \
"s_lshl_b32 s64, s67, 1\n" \
"v_lshl_add_u32 v63, v63, 2, s64\n" \
"s_and_b32 s64, %1, 7\n" \
"s_lshl_b32 s64, s64, 10\n" \
"s_add_u32 s67, s67, s64\n" \
"s_add_u32 s73, s63, s62\n" \
"s_and_b32 s73, s73, 1\n" \
"s_mov_b32 s74, 128\n" \
"s_cmp_lg_u32 s73, 0\n" \
"s_cselect_b64 exec, -1, 0\n" \
"v_swap_b32 v32, v40\n" \
"v_swap_b32 v33, v41\n" \
"v_swap_b32 v34, v42\n" \
"v_swap_b32 v35, v43\n" \
"v_swap_b32 v36, v44\n" \
"v_swap_b32 v37, v45\n" \
"v_swap_b32 v38, v46\n" \
"v_swap_b32 v39, v47\n" \
"s_mov_b64 exec, -1\n" \
"s_cselect_b32 s76, s70, s88\n" \
"s_cselect_b32 s77, s88, s70\n" \
"s_cselect_b32 s75, 524288, s74\n" \
"s_cselect_b32 s74, s74, 524288\n" \
"s_waitcnt lgkmcnt(0)\n" \
"s_cselect_b64 s[52:53], s[10:11], s[14:15]\n" \
"s_cselect_b64 s[54:55], s[14:15], s[10:11]\n" \
"s_cselect_b64 s[50:51], s[10:11], s[16:17]\n" \
"s_cselect_b64 s[40:41], s[16:17], s[10:11]\n" \
"s_add_u32 s52, s52, s76\n" \
"s_addc_u32 s53, s53, 0\n" \
"s_add_u32 s50, s50, s76\n" \
"s_addc_u32 s51, s51, 0\n" \
"s_add_u32 m0, s87, 36864\n" \
"s_nop 0\n" \
"global_load_lds_dwordx4 v40, s[52:53] offset:-4096\n" \
"global_load_lds_dwordx4 v41, s[50:51] offset:-3072\n" \
"global_load_lds_dwordx4 v42, s[52:53] offset:-2048\n" \
"global_load_lds_dwordx4 v43, s[50:51] offset:-1024\n" \
"global_load_lds_dwordx4 v44, s[52:53] offset:0\n" \
"global_load_lds_dwordx4 v45, s[50:51] offset:1024\n" \
"global_load_lds_dwordx4 v46, s[52:53] offset:2048\n" \
"global_load_lds_dwordx4 v47, s[50:51] offset:3072\n" \
"s_add_u32 s52, s52, s74\n" \
"s_addc_u32 s53, s53, 0\n" \
"s_add_u32 s50, s50, s74\n" \
"s_addc_u32 s51, s51, 0\n" \
"s_add_u32 s54, s54, s77\n" \
"s_addc_u32 s55, s55, 0\n" \
"s_add_u32 s40, s40, s77\n" \
"s_addc_u32 s41, s41, 0\n" \
"s_add_u32 m0, s87, 4096\n" \
"s_nop 0\n" \
"global_load_lds_dwordx4 v32, s[54:55] offset:-4096\n" \
"global_load_lds_dwordx4 v33, s[40:41] offset:-3072\n" \
"global_load_lds_dwordx4 v34, s[54:55] offset:-2048\n" \
"global_load_lds_dwordx4 v35, s[40:41] offset:-1024\n" \
"global_load_lds_dwordx4 v36, s[54:55] offset:0\n" \
"global_load_lds_dwordx4 v37, s[40:41] offset:1024\n" \
"global_load_lds_dwordx4 v38, s[54:55] offset:2048\n" \
"global_load_lds_dwordx4 v39, s[40:41] offset:3072\n" \
"s_add_u32 s54, s54, s75\n" \
"s_addc_u32 s55, s55, 0\n" \
"s_add_u32 s40, s40, s75\n" \
"s_addc_u32 s41, s41, 0\n" \
"s_mov_b64 s[56:57], s[28:29]\n" \
"s_and_b32 s57, s57, 0xffff\n" \
"s_mov_b32 s58, 87147648\n" \
"s_mov_b32 s59, 0x20000\n" \
"v_add_u32 v228, 4096, v54\n" \
"v_add_u32 v229, 3200, v54\n" \
"v_lshl_add_u32 v57, v55, 11, v56\n" \
"v_add_u32 v224, 0, v57\n" \
"v_add_u32 v225, 15360, v57\n" \
"v_add_u32 v226, 129024, v57\n" \
"v_add_u32 v227, 144384, v57\n" \
"s_lshl_b32 s70, s61, 15\n" \
"s_add_u32 s70, s70, 0\n" \
"s_add_u32 s71, s70, s89\n" \
"s_add_u32 s71, s71, 4096\n" \
"s_add_u32 s48, s8, s71\n" \
"s_addc_u32 s49, s9, 0\n" \
"s_sub_u32 s71, s70, s89\n" \
"s_add_u32 s71, s71, 3932160\n" \
"s_add_u32 s46, s8, s71\n" \
"s_addc_u32 s47, s9, 0\n" \
"s_add_u32 s70, s88, 0\n" \
"s_add_u32 s44, s12, s70\n" \
"s_addc_u32 s45, s13, 0\n" \
"s_add_u32 s42, s44, 30720\n" \
"s_addc_u32 s43, s45, 0\n" \
"s_lshr_b32 s70, s28, 31\n" \
"s_lshl_b32 s70, s70, 30\n" \
"s_sub_u32 s28, s28, s70\n" \
"v_add_u32 v250, s70, v248\n" \
"v_lshlrev_b32 v245, 2, v52\n" \
"v_mov_b32 v254, 0x30000\n" \
"v_lshrrev_b32 v55, 4, v52\n" \
"v_and_b32 v249, 0xf0, v248\n" \
"v_lshl_add_u32 v249, v55, 9, v249\n" \
"v_and_b32 v53, 15, v52\n" \
"s_lshl_b32 s70, s61, 4\n" \
"v_lshlrev_b32 v54, 2, v55\n" \
"v_sub_u32 v251, v53, v54\n" \
"v_add_u32 v251, s70, v251\n" \
"v_and_b32 v54, 7, v52\n" \
"v_xor_b32 v54, v54, v55\n" \
"v_lshlrev_b32 v53, 7, v53\n" \
"s_add_u32 s71, s87, 131072\n" \
"s_lshr_b32 s70, s61, 1\n" \
"s_lshl_b32 s70, s70, 14\n" \
"s_lshl_b32 s76, s73, 15\n" \
"s_add_u32 s70, s70, s76\n" \
"v_xor_b32 v56, 0, v54\n" \
"v_lshl_add_u32 v56, v56, 4, v53\n" \
"v_add_u32 v230, s71, v56\n" \
"v_add_u32 v48, s70, v56\n" \
"v_xor_b32 v56, 4, v54\n" \
"v_lshl_add_u32 v56, v56, 4, v53\n" \
"v_add_u32 v231, s71, v56\n" \
"v_add_u32 v49, s70, v56\n" \
"v_and_b32 v53, 3, v52\n" \
"v_lshlrev_b32 v53, 3, v53\n" \
"v_bfe_u32 v54, v52, 2, 2\n" \
"v_lshl_add_u32 v53, v54, 6, v53\n" \
"v_bfe_u32 v54, v52, 4, 1\n" \
"v_lshl_add_u32 v53, v54, 8, v53\n" \
"v_lshl_add_u32 v53, v54, 5, v53\n" \
"v_lshrrev_b32 v54, 5, v52\n" \
"s_and_b32 s70, s61, 1\n" \
"s_lshl_b32 s70, s70, 11\n" \
"s_xor_b32 s76, s76, 0x8000\n" \
"s_add_u32 s70, s70, s76\n" \
"v_lshl_add_u32 v56, v54, 13, v53\n" \
"v_add_u32 v50, s70, v56\n" \
"v_xor_b32 v51, 32, v50\n" \
"v_lshl_add_u32 v232, v54, 12, v53\n" \
"v_xor_b32 v233, 32, v232\n" \
"s_mov_b64 exec, 0\n" \
"s_cmp_eq_u32 s61, 0\n" \
"s_cbranch_scc1 Lst0x0_%=\n" \
"s_cmp_eq_u32 s61, 1\n" \
"s_cbranch_scc1 Lst0x1_%=\n" \
"s_cmp_eq_u32 s61, 2\n" \
"s_cbranch_scc1 Lst0x2_%=\n" \
"s_branch Lst0x3_%=\n" \
"Lcb0_%=:\n" \
"s_mov_b64 exec, -1\n" \
"s_add_u32 m0, s87, 135168\n" \
"s_nop 0\n" \
"global_load_lds_dwordx4 v40, s[52:53] offset:-4096\n" \
"global_load_lds_dwordx4 v41, s[50:51] offset:-3072\n" \
"global_load_lds_dwordx4 v42, s[52:53] offset:-2048\n" \
"global_load_lds_dwordx4 v43, s[50:51] offset:-1024\n" \
"global_load_lds_dwordx4 v44, s[52:53] offset:0\n" \
"global_load_lds_dwordx4 v45, s[50:51] offset:1024\n" \
"global_load_lds_dwordx4 v46, s[52:53] offset:2048\n" \
"global_load_lds_dwordx4 v47, s[50:51] offset:3072\n" \
"s_add_u32 s52, s52, s74\n" \
"s_addc_u32 s53, s53, 0\n" \
"s_add_u32 s50, s50, s74\n" \
"s_addc_u32 s51, s51, 0\n" \
"s_add_u32 m0, s87, 69632\n" \
"s_nop 0\n" \
"global_load_lds_dwordx4 v32, s[54:55] offset:-4096\n" \
"global_load_lds_dwordx4 v33, s[40:41] offset:-3072\n" \
"global_load_lds_dwordx4 v34, s[54:55] offset:-2048\n" \
"global_load_lds_dwordx4 v35, s[40:41] offset:-1024\n" \
"global_load_lds_dwordx4 v36, s[54:55] offset:0\n" \
"global_load_lds_dwordx4 v37, s[40:41] offset:1024\n" \
"global_load_lds_dwordx4 v38, s[54:55] offset:2048\n" \
"global_load_lds_dwordx4 v39, s[40:41] offset:3072\n" \
"s_add_u32 s54, s54, s75\n" \
"s_addc_u32 s55, s55, 0\n" \
"s_add_u32 s40, s40, s75\n" \
"s_addc_u32 s41, s41, 0\n" \
"s_waitcnt vmcnt(16)\n" \
"s_barrier\n" \
"ds_read_b128 v[64:67], v48 offset:0\n" \
"ds_read_b64_tr_b16 v[128:129], v50 offset:0\n" \
"ds_read_b64_tr_b16 v[130:131], v50 offset:4096\n" \
"ds_read_b128 v[68:71], v49 offset:0\n" \
"ds_read_b64_tr_b16 v[132:133], v50 offset:16384\n" \
"ds_read_b64_tr_b16 v[134:135], v50 offset:20480\n" \
"ds_read_b128 v[72:75], v48 offset:2048\n" \
"ds_read_b64_tr_b16 v[136:137], v51 offset:0\n" \
"ds_read_b64_tr_b16 v[138:139], v51 offset:4096\n" \
"ds_read_b128 v[76:79], v49 offset:2048\n" \
"ds_read_b64_tr_b16 v[140:141], v51 offset:16384\n" \
"ds_read_b64_tr_b16 v[142:143], v51 offset:20480\n" \
"ds_read_b64_tr_b16 v[144:145], v50 offset:512\n" \
"ds_read_b64_tr_b16 v[146:147], v50 offset:4608\n" \
"ds_read_b64_tr_b16 v[148:149], v50 offset:16896\n" \
"ds_read_b64_tr_b16 v[150:151], v50 offset:20992\n" \
"ds_read_b64_tr_b16 v[152:153], v51 offset:512\n" \
"ds_read_b64_tr_b16 v[154:155], v51 offset:4608\n" \
"ds_read_b64_tr_b16 v[156:157], v51 offset:16896\n" \
"ds_read_b64_tr_b16 v[158:159], v51 offset:20992\n" \
"s_waitcnt lgkmcnt(14)\n" \
"v_mfma_f32_16x16x32_bf16 a[0:3], v[128:131], v[64:67], 0\n" \
"ds_read_b128 v[120:123], v48 offset:14336\n" \
"v_mfma_f32_16x16x32_bf16 a[0:3], v[132:135], v[68:71], a[0:3]\n" \
"s_waitcnt lgkmcnt(9)\n" \
"ds_read_b128 v[80:83], v48 offset:4096\n" \
"v_mfma_f32_16x16x32_bf16 a[4:7], v[136:139], v[64:67], 0\n" \
"ds_read_b128 v[124:127], v49 offset:14336\n" \
"s_mov_b32 s90, 65593344\n" \
"v_mfma_f32_16x16x32_bf16 a[4:7], v[140:143], v[68:71], a[4:7]\n" \
"s_waitcnt lgkmcnt(7)\n" \
"ds_read_b128 v[84:87], v49 offset:4096\n" \
"v_mfma_f32_16x16x32_bf16 a[8:11], v[144:147], v[64:67], 0\n" \
"ds_read_b64_tr_b16 v[160:161], v50 offset:1024\n" \
"v_mfma_f32_16x16x32_bf16 a[8:11], v[148:151], v[68:71], a[8:11]\n" \
"s_waitcnt lgkmcnt(4)\n" \
"ds_read_b128 v[88:91], v48 offset:6144\n" \
"v_mfma_f32_16x16x32_bf16 a[12:15], v[152:155], v[64:67], 0\n" \
"ds_read_b64_tr_b16 v[162:163], v50 offset:5120\n" \
"v_mfma_f32_16x16x32_bf16 a[12:15], v[156:159], v[68:71], a[12:15]\n" \
"ds_read_b128 v[92:95], v49 offset:6144\n" \
"v_mfma_f32_16x16x32_bf16 a[32:35], v[128:131], v[72:75], 0\n" \
"ds_read_b64_tr_b16 v[164:165], v50 offset:17408\n" \
"v_mfma_f32_16x16x32_bf16 a[32:35], v[132:135], v[76:79], a[32:35]\n" \
"ds_read_b128 v[96:99], v48 offset:8192\n" \
"v_mfma_f32_16x16x32_bf16 a[36:39], v[136:139], v[72:75], 0\n" \
"ds_read_b64_tr_b16 v[166:167], v50 offset:21504\n" \
"v_mfma_f32_16x16x32_bf16 a[36:39], v[140:143], v[76:79], a[36:39]\n" \
"ds_read_b128 v[100:103], v49 offset:8192\n" \
"v_mfma_f32_16x16x32_bf16 a[40:43], v[144:147], v[72:75], 0\n" \
"ds_read_b64_tr_b16 v[168:169], v51 offset:1024\n" \
"v_mfma_f32_16x16x32_bf16 a[40:43], v[148:151], v[76:79], a[40:43]\n" \
"ds_read_b128 v[104:107], v48 offset:10240\n" \
"v_mfma_f32_16x16x32_bf16 a[44:47], v[152:155], v[72:75], 0\n" \
"ds_read_b64_tr_b16 v[170:171], v51 offset:5120\n" \
"v_mfma_f32_16x16x32_bf16 a[44:47], v[156:159], v[76:79], a[44:47]\n" \
"s_waitcnt lgkmcnt(4)\n" \
"ds_read_b128 v[108:111], v49 offset:10240\n" \
"v_mfma_f32_16x16x32_bf16 a[64:67], v[128:131], v[80:83], 0\n" \
"ds_read_b64_tr_b16 v[172:173], v51 offset:17408\n" \
"v_mfma_f32_16x16x32_bf16 a[64:67], v[132:135], v[84:87], a[64:67]\n" \
"ds_read_b128 v[112:115], v48 offset:12288\n" \
"v_mfma_f32_16x16x32_bf16 a[68:71], v[136:139], v[80:83], 0\n" \
"ds_read_b64_tr_b16 v[174:175], v51 offset:21504\n" \
"v_mfma_f32_16x16x32_bf16 a[68:71], v[140:143], v[84:87], a[68:71]\n" \
"ds_read_b128 v[116:119], v49 offset:12288\n" \
"v_mfma_f32_16x16x32_bf16 a[72:75], v[144:147], v[80:83], 0\n" \
"ds_read_b64_tr_b16 v[176:177], v50 offset:1536\n" \
"v_mfma_f32_16x16x32_bf16 a[72:75], v[148:151], v[84:87], a[72:75]\n" \
"buffer_load_dword v244, off, s[56:59], s90 sc1\n" \
"v_mfma_f32_16x16x32_bf16 a[76:79], v[152:155], v[80:83], 0\n" \
"s_add_u32 m0, s87, 102400\n" \
"ds_read_b64_tr_b16 v[178:179], v50 offset:5632\n" \
"v_mfma_f32_16x16x32_bf16 a[76:79], v[156:159], v[84:87], a[76:79]\n" \
"global_load_lds_dwordx4 v40, s[52:53] offset:-4096\n" \
"global_load_lds_dwordx4 v41, s[50:51] offset:-3072\n" \
"v_mfma_f32_16x16x32_bf16 a[96:99], v[128:131], v[88:91], 0\n" \
"ds_read_b64_tr_b16 v[180:181], v50 offset:17920\n" \
"global_load_lds_dwordx4 v42, s[52:53] offset:-2048\n" \
"v_mfma_f32_16x16x32_bf16 a[96:99], v[132:135], v[92:95], a[96:99]\n" \
"global_load_lds_dwordx4 v43, s[50:51] offset:-1024\n" \
"global_load_lds_dwordx4 v44, s[52:53] offset:0\n" \
"v_mfma_f32_16x16x32_bf16 a[100:103], v[136:139], v[88:91], 0\n" \
"ds_read_b64_tr_b16 v[182:183], v50 offset:22016\n" \
"global_load_lds_dwordx4 v45, s[50:51] offset:1024\n" \
"v_mfma_f32_16x16x32_bf16 a[100:103], v[140:143], v[92:95], a[100:103]\n" \
"global_load_lds_dwordx4 v46, s[52:53] offset:2048\n" \
"global_load_lds_dwordx4 v47, s[50:51] offset:3072\n" \
"v_mfma_f32_16x16x32_bf16 a[104:107], v[144:147], v[88:91], 0\n" \
"s_add_u32 s52, s52, s74\n" \
"s_addc_u32 s53, s53, 0\n" \
"ds_read_b64_tr_b16 v[184:185], v51 offset:1536\n" \
"v_mfma_f32_16x16x32_bf16 a[104:107], v[148:151], v[92:95], a[104:107]\n" \
"s_add_u32 s50, s50, s74\n" \
"s_addc_u32 s51, s51, 0\n" \
"v_mfma_f32_16x16x32_bf16 a[108:111], v[152:155], v[88:91], 0\n" \
"ds_read_b64_tr_b16 v[186:187], v51 offset:5632\n" \
"v_mfma_f32_16x16x32_bf16 a[108:111], v[156:159], v[92:95], a[108:111]\n" \
"s_waitcnt lgkmcnt(2)\n" \
"v_mfma_f32_16x16x32_bf16 a[128:131], v[128:131], v[96:99], 0\n" \
"ds_read_b64_tr_b16 v[188:189], v51 offset:17920\n" \
"v_mfma_f32_16x16x32_bf16 a[128:131], v[132:135], v[100:103], a[128:131]\n" \
"v_mfma_f32_16x16x32_bf16 a[132:135], v[136:139], v[96:99], 0\n" \
"ds_read_b64_tr_b16 v[190:191], v51 offset:22016\n" \
"v_mfma_f32_16x16x32_bf16 a[132:135], v[140:143], v[100:103], a[132:135]\n" \
"v_mfma_f32_16x16x32_bf16 a[136:139], v[144:147], v[96:99], 0\n" \
"v_mfma_f32_16x16x32_bf16 a[136:139], v[148:151], v[100:103], a[136:139]\n" \
"v_mfma_f32_16x16x32_bf16 a[140:143], v[152:155], v[96:99], 0\n" \
"v_mfma_f32_16x16x32_bf16 a[140:143], v[156:159], v[100:103], a[140:143]\n" \
"v_mfma_f32_16x16x32_bf16 a[160:163], v[128:131], v[104:107], 0\n" \
"s_mul_i32 s76, s73, 0x8000\n" \
"s_add_u32 s77, s76, 0x10000\n" \
"s_sub_u32 s78, 0x18000, s76\n" \
"v_mfma_f32_16x16x32_bf16 a[160:163], v[132:135], v[108:111], a[160:163]\n" \
"v_add_u32 v48, s77, v48\n" \
"v_add_u32 v49, s77, v49\n" \
"v_mfma_f32_16x16x32_bf16 a[164:167], v[136:139], v[104:107], 0\n" \
"v_add_u32 v50, s78, v50\n" \
"v_add_u32 v51, s78, v51\n" \
"v_mfma_f32_16x16x32_bf16 a[164:167], v[140:143], v[108:111], a[164:167]\n" \
"v_mfma_f32_16x16x32_bf16 a[168:171], v[144:147], v[104:107], 0\n" \
"v_mfma_f32_16x16x32_bf16 a[168:171], v[148:151], v[108:111], a[168:171]\n" \
"v_mfma_f32_16x16x32_bf16 a[172:175], v[152:155], v[104:107], 0\n" \
"v_mfma_f32_16x16x32_bf16 a[172:175], v[156:159], v[108:111], a[172:175]\n" \
"v_mfma_f32_16x16x32_bf16 a[192:195], v[128:131], v[112:115], 0\n" \
"v_mfma_f32_16x16x32_bf16 a[192:195], v[132:135], v[116:119], a[192:195]\n" \
"v_mfma_f32_16x16x32_bf16 a[196:199], v[136:139], v[112:115], 0\n" \
"v_mfma_f32_16x16x32_bf16 a[196:199], v[140:143], v[116:119], a[196:199]\n" \
"v_mfma_f32_16x16x32_bf16 a[200:203], v[144:147], v[112:115], 0\n" \
"v_mfma_f32_16x16x32_bf16 a[200:203], v[148:151], v[116:119], a[200:203]\n" \
"v_mfma_f32_16x16x32_bf16 a[204:207], v[152:155], v[112:115], 0\n" \
"v_mfma_f32_16x16x32_bf16 a[204:207], v[156:159], v[116:119], a[204:207]\n" \
"v_mfma_f32_16x16x32_bf16 a[224:227], v[128:131], v[120:123], 0\n" \
"v_mfma_f32_16x16x32_bf16 a[224:227], v[132:135], v[124:127], a[224:227]\n" \
"v_mfma_f32_16x16x32_bf16 a[228:231], v[136:139], v[120:123], 0\n" \
"v_mfma_f32_16x16x32_bf16 a[228:231], v[140:143], v[124:127], a[228:231]\n" \
"v_mfma_f32_16x16x32_bf16 a[232:235], v[144:147], v[120:123], 0\n" \
"v_mfma_f32_16x16x32_bf16 a[232:235], v[148:151], v[124:127], a[232:235]\n" \
"v_mfma_f32_16x16x32_bf16 a[236:239], v[152:155], v[120:123], 0\n" \
"s_waitcnt vmcnt(9) lgkmcnt(0)\n" \
"v_mfma_f32_16x16x32_bf16 a[236:239], v[156:159], v[124:127], a[236:239]\n" \
"s_barrier\n" \
"v_mfma_f32_16x16x32_bf16 a[16:19], v[64:67], v[160:163], 0\n" \
"s_add_u32 m0, s87, 36864\n" \
"ds_read_b64_tr_b16 v[128:129], v50 offset:0\n" \
"v_mfma_f32_16x16x32_bf16 a[16:19], v[68:71], v[164:167], a[16:19]\n" \
"global_load_lds_dwordx4 v32, s[54:55] offset:-4096\n" \
"v_mfma_f32_16x16x32_bf16 a[20:23], v[64:67], v[168:171], 0\n" \
"ds_read_b64_tr_b16 v[130:131], v50 offset:4096\n" \
"v_mfma_f32_16x16x32_bf16 a[20:23], v[68:71], v[172:175], a[20:23]\n" \
"v_mfma_f32_16x16x32_bf16 a[24:27], v[64:67], v[176:179], 0\n" \
"ds_read_b64_tr_b16 v[132:133], v50 offset:16384\n" \
"v_mfma_f32_16x16x32_bf16 a[24:27], v[68:71], v[180:183], a[24:27]\n" \
"global_load_lds_dwordx4 v33, s[40:41] offset:-3072\n" \
"v_mfma_f32_16x16x32_bf16 a[28:31], v[64:67], v[184:187], 0\n" \
"ds_read_b64_tr_b16 v[134:135], v50 offset:20480\n" \
"v_mfma_f32_16x16x32_bf16 a[28:31], v[68:71], v[188:191], a[28:31]\n" \
"v_mfma_f32_16x16x32_bf16 a[48:51], v[72:75], v[160:163], 0\n" \
"ds_read_b128 v[64:67], v48 offset:0\n" \
"v_mfma_f32_16x16x32_bf16 a[48:51], v[76:79], v[164:167], a[48:51]\n" \
"global_load_lds_dwordx4 v34, s[54:55] offset:-2048\n" \
"v_mfma_f32_16x16x32_bf16 a[52:55], v[72:75], v[168:171], 0\n" \
"ds_read_b64_tr_b16 v[136:137], v51 offset:0\n" \
"v_mfma_f32_16x16x32_bf16 a[52:55], v[76:79], v[172:175], a[52:55]\n" \
"v_mfma_f32_16x16x32_bf16 a[56:59], v[72:75], v[176:179], 0\n" \
"ds_read_b128 v[68:71], v49 offset:0\n" \
"v_mfma_f32_16x16x32_bf16 a[56:59], v[76:79], v[180:183], a[56:59]\n" \
"global_load_lds_dwordx4 v35, s[40:41] offset:-1024\n" \
"v_mfma_f32_16x16x32_bf16 a[60:63], v[72:75], v[184:187], 0\n" \
"ds_read_b64_tr_b16 v[138:139], v51 offset:4096\n" \
"v_mfma_f32_16x16x32_bf16 a[60:63], v[76:79], v[188:191], a[60:63]\n" \
"v_mfma_f32_16x16x32_bf16 a[80:83], v[80:83], v[160:163], 0\n" \
"ds_read_b128 v[72:75], v48 offset:2048\n" \
"v_mfma_f32_16x16x32_bf16 a[80:83], v[84:87], v[164:167], a[80:83]\n" \
"global_load_lds_dwordx4 v36, s[54:55] offset:0\n" \
"v_mfma_f32_16x16x32_bf16 a[84:87], v[80:83], v[168:171], 0\n" \
"ds_read_b64_tr_b16 v[140:141], v51 offset:16384\n" \
"v_mfma_f32_16x16x32_bf16 a[84:87], v[84:87], v[172:175], a[84:87]\n" \
"v_mfma_f32_16x16x32_bf16 a[88:91], v[80:83], v[176:179], 0\n" \
"ds_read_b128 v[76:79], v49 offset:2048\n" \
"v_mfma_f32_16x16x32_bf16 a[88:91], v[84:87], v[180:183], a[88:91]\n" \
"global_load_lds_dwordx4 v37, s[40:41] offset:1024\n" \
"v_mfma_f32_16x16x32_bf16 a[92:95], v[80:83], v[184:187], 0\n" \
"ds_read_b64_tr_b16 v[142:143], v51 offset:20480\n" \
"v_mfma_f32_16x16x32_bf16 a[92:95], v[84:87], v[188:191], a[92:95]\n" \
"v_mfma_f32_16x16x32_bf16 a[112:115], v[88:91], v[160:163], 0\n" \
"ds_read_b128 v[80:83], v48 offset:4096\n" \
"v_mfma_f32_16x16x32_bf16 a[112:115], v[92:95], v[164:167], a[112:115]\n" \
"global_load_lds_dwordx4 v38, s[54:55] offset:2048\n" \
"v_mfma_f32_16x16x32_bf16 a[116:119], v[88:91], v[168:171], 0\n" \
"ds_read_b64_tr_b16 v[144:145], v50 offset:512\n" \
"v_mfma_f32_16x16x32_bf16 a[116:119], v[92:95], v[172:175], a[116:119]\n" \
"v_mfma_f32_16x16x32_bf16 a[120:123], v[88:91], v[176:179], 0\n" \
"ds_read_b128 v[84:87], v49 offset:4096\n" \
"global_load_lds_dwordx4 v39, s[40:41] offset:3072\n" \
"v_mfma_f32_16x16x32_bf16 a[120:123], v[92:95], v[180:183], a[120:123]\n" \
"s_add_u32 s54, s54, s75\n" \
"s_addc_u32 s55, s55, 0\n" \
"v_mfma_f32_16x16x32_bf16 a[124:127], v[88:91], v[184:187], 0\n" \
"s_add_u32 s40, s40, s75\n" \
"s_addc_u32 s41, s41, 0\n" \
"ds_read_b64_tr_b16 v[146:147], v50 offset:4608\n" \
"v_mfma_f32_16x16x32_bf16 a[124:127], v[92:95], v[188:191], a[124:127]\n" \
"s_add_u32 s68, s66, 0x0\n" \
"s_add_u32 s64, s10, s68\n" \
"s_addc_u32 s65, s11, 0\n" \
"v_mfma_f32_16x16x32_bf16 a[144:147], v[96:99], v[160:163], 0\n" \
"ds_read_b128 v[88:91], v48 offset:6144\n" \
"v_mfma_f32_16x16x32_bf16 a[144:147], v[100:103], v[164:167], a[144:147]\n" \
"global_load_dword v253, v248, s[64:65]\n" \
"v_mfma_f32_16x16x32_bf16 a[148:151], v[96:99], v[168:171], 0\n" \
"ds_read_b64_tr_b16 v[148:149], v50 offset:16896\n" \
"v_mfma_f32_16x16x32_bf16 a[148:151], v[100:103], v[172:175], a[148:151]\n" \
"v_mfma_f32_16x16x32_bf16 a[152:155], v[96:99], v[176:179], 0\n" \
"ds_read_b128 v[92:95], v49 offset:6144\n" \
"v_mfma_f32_16x16x32_bf16 a[152:155], v[100:103], v[180:183], a[152:155]\n" \
"v_mfma_f32_16x16x32_bf16 a[156:159], v[96:99], v[184:187], 0\n" \
"ds_read_b64_tr_b16 v[150:151], v50 offset:20992\n" \
"v_mfma_f32_16x16x32_bf16 a[156:159], v[100:103], v[188:191], a[156:159]\n" \
"v_mfma_f32_16x16x32_bf16 a[176:179], v[104:107], v[160:163], 0\n" \
"ds_read_b128 v[96:99], v48 offset:8192\n" \
"v_mfma_f32_16x16x32_bf16 a[176:179], v[108:111], v[164:167], a[176:179]\n" \
"v_mfma_f32_16x16x32_bf16 a[180:183], v[104:107], v[168:171], 0\n" \
"ds_read_b64_tr_b16 v[152:153], v51 offset:512\n" \
"v_mfma_f32_16x16x32_bf16 a[180:183], v[108:111], v[172:175], a[180:183]\n" \
"v_mfma_f32_16x16x32_bf16 a[184:187], v[104:107], v[176:179], 0\n" \
"ds_read_b128 v[100:103], v49 offset:8192\n" \
"v_mfma_f32_16x16x32_bf16 a[184:187], v[108:111], v[180:183], a[184:187]\n" \
"v_mfma_f32_16x16x32_bf16 a[188:191], v[104:107], v[184:187], 0\n" \
"ds_read_b64_tr_b16 v[154:155], v51 offset:4608\n" \
"v_mfma_f32_16x16x32_bf16 a[188:191], v[108:111], v[188:191], a[188:191]\n" \
"v_mfma_f32_16x16x32_bf16 a[208:211], v[112:115], v[160:163], 0\n" \
"ds_read_b128 v[104:107], v48 offset:10240\n" \
"v_mfma_f32_16x16x32_bf16 a[208:211], v[116:119], v[164:167], a[208:211]\n" \
"v_mfma_f32_16x16x32_bf16 a[212:215], v[112:115], v[168:171], 0\n" \
"ds_read_b64_tr_b16 v[156:157], v51 offset:16896\n" \
"v_mfma_f32_16x16x32_bf16 a[212:215], v[116:119], v[172:175], a[212:215]\n" \
"v_mfma_f32_16x16x32_bf16 a[216:219], v[112:115], v[176:179], 0\n" \
"ds_read_b128 v[108:111], v49 offset:10240\n" \
"v_mfma_f32_16x16x32_bf16 a[216:219], v[116:119], v[180:183], a[216:219]\n" \
"v_mfma_f32_16x16x32_bf16 a[220:223], v[112:115], v[184:187], 0\n" \
"ds_read_b64_tr_b16 v[158:159], v51 offset:20992\n" \
"v_mfma_f32_16x16x32_bf16 a[220:223], v[116:119], v[188:191], a[220:223]\n" \
"v_mfma_f32_16x16x32_bf16 a[240:243], v[120:123], v[160:163], 0\n" \
"ds_read_b128 v[112:115], v48 offset:12288\n" \
"v_mfma_f32_16x16x32_bf16 a[240:243], v[124:127], v[164:167], a[240:243]\n" \
"v_mfma_f32_16x16x32_bf16 a[244:247], v[120:123], v[168:171], 0\n" \
"v_mfma_f32_16x16x32_bf16 a[244:247], v[124:127], v[172:175], a[244:247]\n" \
"v_mfma_f32_16x16x32_bf16 a[248:251], v[120:123], v[176:179], 0\n" \
"ds_read_b128 v[116:119], v49 offset:12288\n" \
"v_mfma_f32_16x16x32_bf16 a[248:251], v[124:127], v[180:183], a[248:251]\n" \
"v_mfma_f32_16x16x32_bf16 a[252:255], v[120:123], v[184:187], 0\n" \
"v_mfma_f32_16x16x32_bf16 a[252:255], v[124:127], v[188:191], a[252:255]\n" \
"s_waitcnt lgkmcnt(1)\n" \
"v_mfma_f32_16x16x32_bf16 a[0:3], v[128:131], v[64:67], a[0:3]\n" \
"ds_read_b128 v[120:123], v48 offset:14336\n" \
"v_mfma_f32_16x16x32_bf16 a[0:3], v[132:135], v[68:71], a[0:3]\n" \
"v_mfma_f32_16x16x32_bf16 a[4:7], v[136:139], v[64:67], a[4:7]\n" \
"ds_read_b128 v[124:127], v49 offset:14336\n" \
"v_mfma_f32_16x16x32_bf16 a[4:7], v[140:143], v[68:71], a[4:7]\n" \
"s_add_u32 s68, s66, 0x100000\n" \
"v_mfma_f32_16x16x32_bf16 a[8:11], v[144:147], v[64:67], a[8:11]\n" \
"s_add_u32 s64, s10, s68\n" \
"s_addc_u32 s65, s11, 0\n" \
"ds_read_b64_tr_b16 v[160:161], v50 offset:1024\n" \
"v_mfma_f32_16x16x32_bf16 a[8:11], v[148:151], v[68:71], a[8:11]\n" \
"global_load_dword v253, v248, s[64:65]\n" \
"v_mfma_f32_16x16x32_bf16 a[12:15], v[152:155], v[64:67], a[12:15]\n" \
"s_add_u32 m0, s87, 4096\n" \
"ds_read_b64_tr_b16 v[162:163], v50 offset:5120\n" \
"v_mfma_f32_16x16x32_bf16 a[12:15], v[156:159], v[68:71], a[12:15]\n" \
"global_load_lds_dwordx4 v40, s[52:53] offset:-4096\n" \
"v_mfma_f32_16x16x32_bf16 a[32:35], v[128:131], v[72:75], a[32:35]\n" \
"ds_read_b64_tr_b16 v[164:165], v50 offset:17408\n" \
"v_mfma_f32_16x16x32_bf16 a[32:35], v[132:135], v[76:79], a[32:35]\n" \
"v_mfma_f32_16x16x32_bf16 a[36:39], v[136:139], v[72:75], a[36:39]\n" \
"ds_read_b64_tr_b16 v[166:167], v50 offset:21504\n" \
"v_mfma_f32_16x16x32_bf16 a[36:39], v[140:143], v[76:79], a[36:39]\n" \
"global_load_lds_dwordx4 v41, s[50:51] offset:-3072\n" \
"v_mfma_f32_16x16x32_bf16 a[40:43], v[144:147], v[72:75], a[40:43]\n" \
"ds_read_b64_tr_b16 v[168:169], v51 offset:1024\n" \
"v_mfma_f32_16x16x32_bf16 a[40:43], v[148:151], v[76:79], a[40:43]\n" \
"v_mfma_f32_16x16x32_bf16 a[44:47], v[152:155], v[72:75], a[44:47]\n" \
"ds_read_b64_tr_b16 v[170:171], v51 offset:5120\n" \
"v_mfma_f32_16x16x32_bf16 a[44:47], v[156:159], v[76:79], a[44:47]\n" \
"global_load_lds_dwordx4 v42, s[52:53] offset:-2048\n" \
"v_mfma_f32_16x16x32_bf16 a[64:67], v[128:131], v[80:83], a[64:67]\n" \
"ds_read_b64_tr_b16 v[172:173], v51 offset:17408\n" \
"v_mfma_f32_16x16x32_bf16 a[64:67], v[132:135], v[84:87], a[64:67]\n" \
"v_mfma_f32_16x16x32_bf16 a[68:71], v[136:139], v[80:83], a[68:71]\n" \
"ds_read_b64_tr_b16 v[174:175], v51 offset:21504\n" \
"v_mfma_f32_16x16x32_bf16 a[68:71], v[140:143], v[84:87], a[68:71]\n" \
"global_load_lds_dwordx4 v43, s[50:51] offset:-1024\n" \
"v_mfma_f32_16x16x32_bf16 a[72:75], v[144:147], v[80:83], a[72:75]\n" \
"ds_read_b64_tr_b16 v[176:177], v50 offset:1536\n" \
"v_mfma_f32_16x16x32_bf16 a[72:75], v[148:151], v[84:87], a[72:75]\n" \
"v_mfma_f32_16x16x32_bf16 a[76:79], v[152:155], v[80:83], a[76:79]\n" \
"ds_read_b64_tr_b16 v[178:179], v50 offset:5632\n" \
"v_mfma_f32_16x16x32_bf16 a[76:79], v[156:159], v[84:87], a[76:79]\n" \
"global_load_lds_dwordx4 v44, s[52:53] offset:0\n" \
"v_mfma_f32_16x16x32_bf16 a[96:99], v[128:131], v[88:91], a[96:99]\n" \
"ds_read_b64_tr_b16 v[180:181], v50 offset:17920\n" \
"v_mfma_f32_16x16x32_bf16 a[96:99], v[132:135], v[92:95], a[96:99]\n" \
"v_mfma_f32_16x16x32_bf16 a[100:103], v[136:139], v[88:91], a[100:103]\n" \
"ds_read_b64_tr_b16 v[182:183], v50 offset:22016\n" \
"v_mfma_f32_16x16x32_bf16 a[100:103], v[140:143], v[92:95], a[100:103]\n" \
"global_load_lds_dwordx4 v45, s[50:51] offset:1024\n" \
"v_mfma_f32_16x16x32_bf16 a[104:107], v[144:147], v[88:91], a[104:107]\n" \
"ds_read_b64_tr_b16 v[184:185], v51 offset:1536\n" \
"v_mfma_f32_16x16x32_bf16 a[104:107], v[148:151], v[92:95], a[104:107]\n" \
"v_mfma_f32_16x16x32_bf16 a[108:111], v[152:155], v[88:91], a[108:111]\n" \
"ds_read_b64_tr_b16 v[186:187], v51 offset:5632\n" \
"v_mfma_f32_16x16x32_bf16 a[108:111], v[156:159], v[92:95], a[108:111]\n" \
"global_load_lds_dwordx4 v46, s[52:53] offset:2048\n" \
"v_mfma_f32_16x16x32_bf16 a[128:131], v[128:131], v[96:99], a[128:131]\n" \
"ds_read_b64_tr_b16 v[188:189], v51 offset:17920\n" \
"v_mfma_f32_16x16x32_bf16 a[128:131], v[132:135], v[100:103], a[128:131]\n" \
"v_mfma_f32_16x16x32_bf16 a[132:135], v[136:139], v[96:99], a[132:135]\n" \
"ds_read_b64_tr_b16 v[190:191], v51 offset:22016\n" \
"v_mfma_f32_16x16x32_bf16 a[132:135], v[140:143], v[100:103], a[132:135]\n" \
"global_load_lds_dwordx4 v47, s[50:51] offset:3072\n" \
"v_mfma_f32_16x16x32_bf16 a[136:139], v[144:147], v[96:99], a[136:139]\n" \
"s_add_u32 s52, s52, s74\n" \
"s_addc_u32 s53, s53, 0\n" \
"v_mfma_f32_16x16x32_bf16 a[136:139], v[148:151], v[100:103], a[136:139]\n" \
"s_add_u32 s50, s50, s74\n" \
"s_addc_u32 s51, s51, 0\n" \
"v_mfma_f32_16x16x32_bf16 a[140:143], v[152:155], v[96:99], a[140:143]\n" \
"v_mfma_f32_16x16x32_bf16 a[140:143], v[156:159], v[100:103], a[140:143]\n" \
"v_mfma_f32_16x16x32_bf16 a[160:163], v[128:131], v[104:107], a[160:163]\n" \
"s_mul_i32 s76, s73, 0x0\n" \
"s_add_u32 s77, s76, 0xffff8000\n" \
"s_sub_u32 s78, 0xffff8000, s76\n" \
"v_mfma_f32_16x16x32_bf16 a[160:163], v[132:135], v[108:111], a[160:163]\n" \
"v_add_u32 v48, s77, v48\n" \
"v_add_u32 v49, s77, v49\n" \
"v_mfma_f32_16x16x32_bf16 a[164:167], v[136:139], v[104:107], a[164:167]\n" \
"v_add_u32 v50, s78, v50\n" \
"v_add_u32 v51, s78, v51\n" \
"v_mfma_f32_16x16x32_bf16 a[164:167], v[140:143], v[108:111], a[164:167]\n" \
"v_mfma_f32_16x16x32_bf16 a[168:171], v[144:147], v[104:107], a[168:171]\n" \
"v_mfma_f32_16x16x32_bf16 a[168:171], v[148:151], v[108:111], a[168:171]\n" \
"v_mfma_f32_16x16x32_bf16 a[172:175], v[152:155], v[104:107], a[172:175]\n" \
"v_mfma_f32_16x16x32_bf16 a[172:175], v[156:159], v[108:111], a[172:175]\n" \
"s_waitcnt lgkmcnt(0)\n" \
"v_mfma_f32_16x16x32_bf16 a[192:195], v[128:131], v[112:115], a[192:195]\n" \
"v_mfma_f32_16x16x32_bf16 a[192:195], v[132:135], v[116:119], a[192:195]\n" \
"v_mfma_f32_16x16x32_bf16 a[196:199], v[136:139], v[112:115], a[196:199]\n" \
"v_mfma_f32_16x16x32_bf16 a[196:199], v[140:143], v[116:119], a[196:199]\n" \
"v_mfma_f32_16x16x32_bf16 a[200:203], v[144:147], v[112:115], a[200:203]\n" \
"v_mfma_f32_16x16x32_bf16 a[200:203], v[148:151], v[116:119], a[200:203]\n" \
"v_mfma_f32_16x16x32_bf16 a[204:207], v[152:155], v[112:115], a[204:207]\n" \
"v_mfma_f32_16x16x32_bf16 a[204:207], v[156:159], v[116:119], a[204:207]\n" \
"v_mfma_f32_16x16x32_bf16 a[224:227], v[128:131], v[120:123], a[224:227]\n" \
"v_mfma_f32_16x16x32_bf16 a[224:227], v[132:135], v[124:127], a[224:227]\n" \
"Lpg610_%=:\n" \
"Lst0x0_%=:\n" \
"s_cbranch_execz Lpg2769_%=\n" \
"v_mfma_f32_16x16x32_bf16 a[228:231], v[136:139], v[120:123], a[228:231]\n" \
"v_mfma_f32_16x16x32_bf16 a[228:231], v[140:143], v[124:127], a[228:231]\n" \
"v_mfma_f32_16x16x32_bf16 a[232:235], v[144:147], v[120:123], a[232:235]\n" \
"v_mfma_f32_16x16x32_bf16 a[232:235], v[148:151], v[124:127], a[232:235]\n" \
"v_mfma_f32_16x16x32_bf16 a[236:239], v[152:155], v[120:123], a[236:239]\n" \
"s_waitcnt vmcnt(10) lgkmcnt(0)\n" \
"v_mfma_f32_16x16x32_bf16 a[236:239], v[156:159], v[124:127], a[236:239]\n" \
"s_barrier\n" \
"v_mfma_f32_16x16x32_bf16 a[16:19], v[64:67], v[160:163], a[16:19]\n" \
"s_add_u32 m0, s87, 135168\n" \
"ds_read_b64_tr_b16 v[128:129], v50 offset:0\n" \
"v_readfirstlane_b32 s60, v244\n" \
"v_mfma_f32_16x16x32_bf16 a[16:19], v[68:71], v[164:167], a[16:19]\n" \
"s_add_u32 s60, s60, 0x9e3779b9\n" \
"global_load_lds_dwordx4 v32, s[54:55] offset:-4096\n" \
"v_mfma_f32_16x16x32_bf16 a[20:23], v[64:67], v[168:171], a[20:23]\n" \
"s_mul_i32 s88, s62, 557824\n" \
"s_mul_i32 s89, s63, 17408\n" \
"ds_read_b64_tr_b16 v[130:131], v50 offset:4096\n" \
"v_mfma_f32_16x16x32_bf16 a[20:23], v[68:71], v[172:175], a[20:23]\n" \
"s_add_u32 s88, s88, s89\n" \
"s_add_u32 s88, s88, s95\n" \
"s_add_u32 s89, s88, 17850368\n" \
"v_mfma_f32_16x16x32_bf16 a[24:27], v[64:67], v[176:179], a[24:27]\n" \
"s_lshl_b32 s91, s62, 7\n" \
"s_add_u32 s91, s91, 65585152\n" \
"ds_read_b64_tr_b16 v[132:133], v50 offset:16384\n" \
"v_mfma_f32_16x16x32_bf16 a[24:27], v[68:71], v[180:183], a[24:27]\n" \
"s_mov_b32 s92, 0x3e0293ee\n" \
"global_load_lds_dwordx4 v33, s[40:41] offset:-3072\n" \
"v_mfma_f32_16x16x32_bf16 a[28:31], v[64:67], v[184:187], a[28:31]\n" \
"ds_read_b64_tr_b16 v[134:135], v50 offset:20480\n" \
"v_mfma_f32_16x16x32_bf16 a[28:31], v[68:71], v[188:191], a[28:31]\n" \
"v_mfma_f32_16x16x32_bf16 a[48:51], v[72:75], v[160:163], a[48:51]\n" \
"ds_read_b128 v[64:67], v48 offset:0\n" \
"v_mfma_f32_16x16x32_bf16 a[48:51], v[76:79], v[164:167], a[48:51]\n" \
"global_load_lds_dwordx4 v34, s[54:55] offset:-2048\n" \
"v_mfma_f32_16x16x32_bf16 a[52:55], v[72:75], v[168:171], a[52:55]\n" \
"ds_read_b64_tr_b16 v[136:137], v51 offset:0\n" \
"v_mfma_f32_16x16x32_bf16 a[52:55], v[76:79], v[172:175], a[52:55]\n" \
"v_mfma_f32_16x16x32_bf16 a[56:59], v[72:75], v[176:179], a[56:59]\n" \
"ds_read_b128 v[68:71], v49 offset:0\n" \
"v_mfma_f32_16x16x32_bf16 a[56:59], v[76:79], v[180:183], a[56:59]\n" \
"global_load_lds_dwordx4 v35, s[40:41] offset:-1024\n" \
"v_mfma_f32_16x16x32_bf16 a[60:63], v[72:75], v[184:187], a[60:63]\n" \
"ds_read_b64_tr_b16 v[138:139], v51 offset:4096\n" \
"v_mfma_f32_16x16x32_bf16 a[60:63], v[76:79], v[188:191], a[60:63]\n" \
"v_mfma_f32_16x16x32_bf16 a[80:83], v[80:83], v[160:163], a[80:83]\n" \
"ds_read_b128 v[72:75], v48 offset:2048\n" \
"v_mfma_f32_16x16x32_bf16 a[80:83], v[84:87], v[164:167], a[80:83]\n" \
"global_load_lds_dwordx4 v36, s[54:55] offset:0\n" \
"v_mfma_f32_16x16x32_bf16 a[84:87], v[80:83], v[168:171], a[84:87]\n" \
"ds_read_b64_tr_b16 v[140:141], v51 offset:16384\n" \
"v_mfma_f32_16x16x32_bf16 a[84:87], v[84:87], v[172:175], a[84:87]\n" \
"v_mfma_f32_16x16x32_bf16 a[88:91], v[80:83], v[176:179], a[88:91]\n" \
"ds_read_b128 v[76:79], v49 offset:2048\n" \
"v_mfma_f32_16x16x32_bf16 a[88:91], v[84:87], v[180:183], a[88:91]\n" \
"global_load_lds_dwordx4 v37, s[40:41] offset:1024\n" \
"v_mfma_f32_16x16x32_bf16 a[92:95], v[80:83], v[184:187], a[92:95]\n" \
"ds_read_b64_tr_b16 v[142:143], v51 offset:20480\n" \
"v_mfma_f32_16x16x32_bf16 a[92:95], v[84:87], v[188:191], a[92:95]\n" \
"v_mfma_f32_16x16x32_bf16 a[112:115], v[88:91], v[160:163], a[112:115]\n" \
"ds_read_b128 v[80:83], v48 offset:4096\n" \
"v_mfma_f32_16x16x32_bf16 a[112:115], v[92:95], v[164:167], a[112:115]\n" \
"global_load_lds_dwordx4 v38, s[54:55] offset:2048\n" \
"v_mfma_f32_16x16x32_bf16 a[116:119], v[88:91], v[168:171], a[116:119]\n" \
"ds_read_b64_tr_b16 v[144:145], v50 offset:512\n" \
"v_mfma_f32_16x16x32_bf16 a[116:119], v[92:95], v[172:175], a[116:119]\n" \
"v_mfma_f32_16x16x32_bf16 a[120:123], v[88:91], v[176:179], a[120:123]\n" \
"ds_read_b128 v[84:87], v49 offset:4096\n" \
"global_load_lds_dwordx4 v39, s[40:41] offset:3072\n" \
"v_mfma_f32_16x16x32_bf16 a[120:123], v[92:95], v[180:183], a[120:123]\n" \
"s_add_u32 s54, s54, s75\n" \
"s_addc_u32 s55, s55, 0\n" \
"v_mfma_f32_16x16x32_bf16 a[124:127], v[88:91], v[184:187], a[124:127]\n" \
"s_add_u32 s40, s40, s75\n" \
"s_addc_u32 s41, s41, 0\n" \
"ds_read_b64_tr_b16 v[146:147], v50 offset:4608\n" \
"v_mfma_f32_16x16x32_bf16 a[124:127], v[92:95], v[188:191], a[124:127]\n" \
"s_add_u32 s68, s67, 0x200000\n" \
"s_add_u32 s64, s14, s68\n" \
"s_addc_u32 s65, s15, 0\n" \
"v_mfma_f32_16x16x32_bf16 a[144:147], v[96:99], v[160:163], a[144:147]\n" \
"ds_read_b128 v[88:91], v48 offset:6144\n" \
"v_mfma_f32_16x16x32_bf16 a[144:147], v[100:103], v[164:167], a[144:147]\n" \
"global_load_dword v253, v248, s[64:65]\n" \
"v_mfma_f32_16x16x32_bf16 a[148:151], v[96:99], v[168:171], a[148:151]\n" \
"s_add_u32 s68, s67, 0x200000\n" \
"ds_read_b64_tr_b16 v[148:149], v50 offset:16896\n" \
"v_mfma_f32_16x16x32_bf16 a[148:151], v[100:103], v[172:175], a[148:151]\n" \
"s_add_u32 s64, s16, s68\n" \
"s_addc_u32 s65, s17, 0\n" \
"v_mfma_f32_16x16x32_bf16 a[152:155], v[96:99], v[176:179], a[152:155]\n" \
"ds_read_b128 v[92:95], v49 offset:6144\n" \
"v_mfma_f32_16x16x32_bf16 a[152:155], v[100:103], v[180:183], a[152:155]\n" \
"global_load_dword v253, v248, s[64:65]\n" \
"v_mfma_f32_16x16x32_bf16 a[156:159], v[96:99], v[184:187], a[156:159]\n" \
"ds_read_b64_tr_b16 v[150:151], v50 offset:20992\n" \
"v_mfma_f32_16x16x32_bf16 a[156:159], v[100:103], v[188:191], a[156:159]\n" \
"v_mfma_f32_16x16x32_bf16 a[176:179], v[104:107], v[160:163], a[176:179]\n" \
"ds_read_b128 v[96:99], v48 offset:8192\n" \
"v_mfma_f32_16x16x32_bf16 a[176:179], v[108:111], v[164:167], a[176:179]\n" \
"v_mfma_f32_16x16x32_bf16 a[180:183], v[104:107], v[168:171], a[180:183]\n" \
"ds_read_b64_tr_b16 v[152:153], v51 offset:512\n" \
"v_mfma_f32_16x16x32_bf16 a[180:183], v[108:111], v[172:175], a[180:183]\n" \
"v_mfma_f32_16x16x32_bf16 a[184:187], v[104:107], v[176:179], a[184:187]\n" \
"ds_read_b128 v[100:103], v49 offset:8192\n" \
"v_mfma_f32_16x16x32_bf16 a[184:187], v[108:111], v[180:183], a[184:187]\n" \
"v_mfma_f32_16x16x32_bf16 a[188:191], v[104:107], v[184:187], a[188:191]\n" \
"ds_read_b64_tr_b16 v[154:155], v51 offset:4608\n" \
"v_mfma_f32_16x16x32_bf16 a[188:191], v[108:111], v[188:191], a[188:191]\n" \
"v_mfma_f32_16x16x32_bf16 a[208:211], v[112:115], v[160:163], a[208:211]\n" \
"ds_read_b128 v[104:107], v48 offset:10240\n" \
"v_mfma_f32_16x16x32_bf16 a[208:211], v[116:119], v[164:167], a[208:211]\n" \
"v_mfma_f32_16x16x32_bf16 a[212:215], v[112:115], v[168:171], a[212:215]\n" \
"ds_read_b64_tr_b16 v[156:157], v51 offset:16896\n" \
"v_mfma_f32_16x16x32_bf16 a[212:215], v[116:119], v[172:175], a[212:215]\n" \
"v_mfma_f32_16x16x32_bf16 a[216:219], v[112:115], v[176:179], a[216:219]\n" \
"ds_read_b128 v[108:111], v49 offset:10240\n" \
"v_mfma_f32_16x16x32_bf16 a[216:219], v[116:119], v[180:183], a[216:219]\n" \
"v_mfma_f32_16x16x32_bf16 a[220:223], v[112:115], v[184:187], a[220:223]\n" \
"ds_read_b64_tr_b16 v[158:159], v51 offset:20992\n" \
"v_mfma_f32_16x16x32_bf16 a[220:223], v[116:119], v[188:191], a[220:223]\n" \
"v_mfma_f32_16x16x32_bf16 a[240:243], v[120:123], v[160:163], a[240:243]\n" \
"ds_read_b128 v[112:115], v48 offset:12288\n" \
"v_mfma_f32_16x16x32_bf16 a[240:243], v[124:127], v[164:167], a[240:243]\n" \
"v_mfma_f32_16x16x32_bf16 a[244:247], v[120:123], v[168:171], a[244:247]\n" \
"v_mfma_f32_16x16x32_bf16 a[244:247], v[124:127], v[172:175], a[244:247]\n" \
"v_mfma_f32_16x16x32_bf16 a[248:251], v[120:123], v[176:179], a[248:251]\n" \
"ds_read_b128 v[116:119], v49 offset:12288\n" \
"v_mfma_f32_16x16x32_bf16 a[248:251], v[124:127], v[180:183], a[248:251]\n" \
"v_mfma_f32_16x16x32_bf16 a[252:255], v[120:123], v[184:187], a[252:255]\n" \
"v_mfma_f32_16x16x32_bf16 a[252:255], v[124:127], v[188:191], a[252:255]\n" \
"s_waitcnt lgkmcnt(1)\n" \
"v_mfma_f32_16x16x32_bf16 a[0:3], v[128:131], v[64:67], a[0:3]\n" \
"ds_read_b128 v[120:123], v48 offset:14336\n" \
"v_mfma_f32_16x16x32_bf16 a[0:3], v[132:135], v[68:71], a[0:3]\n" \
"v_mfma_f32_16x16x32_bf16 a[4:7], v[136:139], v[64:67], a[4:7]\n" \
"ds_read_b128 v[124:127], v49 offset:14336\n" \
"v_mfma_f32_16x16x32_bf16 a[4:7], v[140:143], v[68:71], a[4:7]\n" \
"s_add_u32 s68, s67, 0x300000\n" \
"v_mfma_f32_16x16x32_bf16 a[8:11], v[144:147], v[64:67], a[8:11]\n" \
"s_add_u32 s64, s14, s68\n" \
"s_addc_u32 s65, s15, 0\n" \
"ds_read_b64_tr_b16 v[160:161], v50 offset:1024\n" \
"v_mfma_f32_16x16x32_bf16 a[8:11], v[148:151], v[68:71], a[8:11]\n" \
"global_load_dword v253, v248, s[64:65]\n" \
"v_mfma_f32_16x16x32_bf16 a[12:15], v[152:155], v[64:67], a[12:15]\n" \
"s_add_u32 m0, s87, 69632\n" \
"ds_read_b64_tr_b16 v[162:163], v50 offset:5120\n" \
"v_mfma_f32_16x16x32_bf16 a[12:15], v[156:159], v[68:71], a[12:15]\n" \
"global_load_lds_dwordx4 v40, s[52:53] offset:-4096\n" \
"v_mfma_f32_16x16x32_bf16 a[32:35], v[128:131], v[72:75], a[32:35]\n" \
"ds_read_b64_tr_b16 v[164:165], v50 offset:17408\n" \
"v_mfma_f32_16x16x32_bf16 a[32:35], v[132:135], v[76:79], a[32:35]\n" \
"v_mfma_f32_16x16x32_bf16 a[36:39], v[136:139], v[72:75], a[36:39]\n" \
"ds_read_b64_tr_b16 v[166:167], v50 offset:21504\n" \
"v_mfma_f32_16x16x32_bf16 a[36:39], v[140:143], v[76:79], a[36:39]\n" \
"global_load_lds_dwordx4 v41, s[50:51] offset:-3072\n" \
"v_mfma_f32_16x16x32_bf16 a[40:43], v[144:147], v[72:75], a[40:43]\n" \
"ds_read_b64_tr_b16 v[168:169], v51 offset:1024\n" \
"v_mfma_f32_16x16x32_bf16 a[40:43], v[148:151], v[76:79], a[40:43]\n" \
"v_mfma_f32_16x16x32_bf16 a[44:47], v[152:155], v[72:75], a[44:47]\n" \
"ds_read_b64_tr_b16 v[170:171], v51 offset:5120\n" \
"v_mfma_f32_16x16x32_bf16 a[44:47], v[156:159], v[76:79], a[44:47]\n" \
"global_load_lds_dwordx4 v42, s[52:53] offset:-2048\n" \
"v_mfma_f32_16x16x32_bf16 a[64:67], v[128:131], v[80:83], a[64:67]\n" \
"ds_read_b64_tr_b16 v[172:173], v51 offset:17408\n" \
"v_mfma_f32_16x16x32_bf16 a[64:67], v[132:135], v[84:87], a[64:67]\n" \
"v_mfma_f32_16x16x32_bf16 a[68:71], v[136:139], v[80:83], a[68:71]\n" \
"ds_read_b64_tr_b16 v[174:175], v51 offset:21504\n" \
"v_mfma_f32_16x16x32_bf16 a[68:71], v[140:143], v[84:87], a[68:71]\n" \
"global_load_lds_dwordx4 v43, s[50:51] offset:-1024\n" \
"v_mfma_f32_16x16x32_bf16 a[72:75], v[144:147], v[80:83], a[72:75]\n" \
"ds_read_b64_tr_b16 v[176:177], v50 offset:1536\n" \
"v_mfma_f32_16x16x32_bf16 a[72:75], v[148:151], v[84:87], a[72:75]\n" \
"v_mfma_f32_16x16x32_bf16 a[76:79], v[152:155], v[80:83], a[76:79]\n" \
"ds_read_b64_tr_b16 v[178:179], v50 offset:5632\n" \
"v_mfma_f32_16x16x32_bf16 a[76:79], v[156:159], v[84:87], a[76:79]\n" \
"global_load_lds_dwordx4 v44, s[52:53] offset:0\n" \
"v_mfma_f32_16x16x32_bf16 a[96:99], v[128:131], v[88:91], a[96:99]\n" \
"ds_read_b64_tr_b16 v[180:181], v50 offset:17920\n" \
"v_mfma_f32_16x16x32_bf16 a[96:99], v[132:135], v[92:95], a[96:99]\n" \
"v_mfma_f32_16x16x32_bf16 a[100:103], v[136:139], v[88:91], a[100:103]\n" \
"ds_read_b64_tr_b16 v[182:183], v50 offset:22016\n" \
"v_mfma_f32_16x16x32_bf16 a[100:103], v[140:143], v[92:95], a[100:103]\n" \
"global_load_lds_dwordx4 v45, s[50:51] offset:1024\n" \
"v_mfma_f32_16x16x32_bf16 a[104:107], v[144:147], v[88:91], a[104:107]\n" \
"ds_read_b64_tr_b16 v[184:185], v51 offset:1536\n" \
"v_mfma_f32_16x16x32_bf16 a[104:107], v[148:151], v[92:95], a[104:107]\n" \
"v_mfma_f32_16x16x32_bf16 a[108:111], v[152:155], v[88:91], a[108:111]\n" \
"ds_read_b64_tr_b16 v[186:187], v51 offset:5632\n" \
"v_mfma_f32_16x16x32_bf16 a[108:111], v[156:159], v[92:95], a[108:111]\n" \
"global_load_lds_dwordx4 v46, s[52:53] offset:2048\n" \
"v_mfma_f32_16x16x32_bf16 a[128:131], v[128:131], v[96:99], a[128:131]\n" \
"ds_read_b64_tr_b16 v[188:189], v51 offset:17920\n" \
"v_mfma_f32_16x16x32_bf16 a[128:131], v[132:135], v[100:103], a[128:131]\n" \
"v_mfma_f32_16x16x32_bf16 a[132:135], v[136:139], v[96:99], a[132:135]\n" \
"ds_read_b64_tr_b16 v[190:191], v51 offset:22016\n" \
"v_mfma_f32_16x16x32_bf16 a[132:135], v[140:143], v[100:103], a[132:135]\n" \
"global_load_lds_dwordx4 v47, s[50:51] offset:3072\n" \
"v_mfma_f32_16x16x32_bf16 a[136:139], v[144:147], v[96:99], a[136:139]\n" \
"s_add_u32 s52, s52, s74\n" \
"s_addc_u32 s53, s53, 0\n" \
"v_mfma_f32_16x16x32_bf16 a[136:139], v[148:151], v[100:103], a[136:139]\n" \
"s_add_u32 s50, s50, s74\n" \
"s_addc_u32 s51, s51, 0\n" \
"v_mfma_f32_16x16x32_bf16 a[140:143], v[152:155], v[96:99], a[140:143]\n" \
"v_mfma_f32_16x16x32_bf16 a[140:143], v[156:159], v[100:103], a[140:143]\n" \
"v_mfma_f32_16x16x32_bf16 a[160:163], v[128:131], v[104:107], a[160:163]\n" \
"s_mul_i32 s76, s73, 0xfffd0000\n" \
"s_add_u32 s77, s76, 0x18000\n" \
"s_sub_u32 s78, 0xfffe8000, s76\n" \
"v_mfma_f32_16x16x32_bf16 a[160:163], v[132:135], v[108:111], a[160:163]\n" \
"v_add_u32 v48, s77, v48\n" \
"v_add_u32 v49, s77, v49\n" \
"v_mfma_f32_16x16x32_bf16 a[164:167], v[136:139], v[104:107], a[164:167]\n" \
"v_add_u32 v50, s78, v50\n" \
"v_add_u32 v51, s78, v51\n" \
"v_mfma_f32_16x16x32_bf16 a[164:167], v[140:143], v[108:111], a[164:167]\n" \
"v_mfma_f32_16x16x32_bf16 a[168:171], v[144:147], v[104:107], a[168:171]\n" \
"v_mfma_f32_16x16x32_bf16 a[168:171], v[148:151], v[108:111], a[168:171]\n" \
"v_mfma_f32_16x16x32_bf16 a[172:175], v[152:155], v[104:107], a[172:175]\n" \
"v_mfma_f32_16x16x32_bf16 a[172:175], v[156:159], v[108:111], a[172:175]\n" \
"s_waitcnt lgkmcnt(0)\n" \
"v_mfma_f32_16x16x32_bf16 a[192:195], v[128:131], v[112:115], a[192:195]\n" \
"v_mfma_f32_16x16x32_bf16 a[192:195], v[132:135], v[116:119], a[192:195]\n" \
"v_mfma_f32_16x16x32_bf16 a[196:199], v[136:139], v[112:115], a[196:199]\n" \
"v_mfma_f32_16x16x32_bf16 a[196:199], v[140:143], v[116:119], a[196:199]\n" \
"v_mfma_f32_16x16x32_bf16 a[200:203], v[144:147], v[112:115], a[200:203]\n" \
"v_mfma_f32_16x16x32_bf16 a[200:203], v[148:151], v[116:119], a[200:203]\n" \
"v_mfma_f32_16x16x32_bf16 a[204:207], v[152:155], v[112:115], a[204:207]\n" \
"v_mfma_f32_16x16x32_bf16 a[204:207], v[156:159], v[116:119], a[204:207]\n" \
"v_mfma_f32_16x16x32_bf16 a[224:227], v[128:131], v[120:123], a[224:227]\n" \
"v_mfma_f32_16x16x32_bf16 a[224:227], v[132:135], v[124:127], a[224:227]\n" \
"v_mfma_f32_16x16x32_bf16 a[228:231], v[136:139], v[120:123], a[228:231]\n" \
"v_mfma_f32_16x16x32_bf16 a[228:231], v[140:143], v[124:127], a[228:231]\n" \
"v_mfma_f32_16x16x32_bf16 a[232:235], v[144:147], v[120:123], a[232:235]\n" \
"v_mfma_f32_16x16x32_bf16 a[232:235], v[148:151], v[124:127], a[232:235]\n" \
"v_mfma_f32_16x16x32_bf16 a[236:239], v[152:155], v[120:123], a[236:239]\n" \
"s_waitcnt vmcnt(11) lgkmcnt(0)\n" \
"v_mfma_f32_16x16x32_bf16 a[236:239], v[156:159], v[124:127], a[236:239]\n" \
"s_barrier\n" \
"v_mfma_f32_16x16x32_bf16 a[16:19], v[64:67], v[160:163], a[16:19]\n" \
"s_add_u32 m0, s87, 102400\n" \
"ds_read_b64_tr_b16 v[128:129], v50 offset:0\n" \
"v_mfma_f32_16x16x32_bf16 a[16:19], v[68:71], v[164:167], a[16:19]\n" \
"global_load_lds_dwordx4 v32, s[54:55] offset:-4096\n" \
"v_mfma_f32_16x16x32_bf16 a[20:23], v[64:67], v[168:171], a[20:23]\n" \
"ds_read_b64_tr_b16 v[130:131], v50 offset:4096\n" \
"v_mfma_f32_16x16x32_bf16 a[20:23], v[68:71], v[172:175], a[20:23]\n" \
"v_mfma_f32_16x16x32_bf16 a[24:27], v[64:67], v[176:179], a[24:27]\n" \
"ds_read_b64_tr_b16 v[132:133], v50 offset:16384\n" \
"v_mfma_f32_16x16x32_bf16 a[24:27], v[68:71], v[180:183], a[24:27]\n" \
"global_load_lds_dwordx4 v33, s[40:41] offset:-3072\n" \
"v_mfma_f32_16x16x32_bf16 a[28:31], v[64:67], v[184:187], a[28:31]\n" \
"ds_read_b64_tr_b16 v[134:135], v50 offset:20480\n" \
"v_mfma_f32_16x16x32_bf16 a[28:31], v[68:71], v[188:191], a[28:31]\n" \
"v_mfma_f32_16x16x32_bf16 a[48:51], v[72:75], v[160:163], a[48:51]\n" \
"ds_read_b128 v[64:67], v48 offset:0\n" \
"v_mfma_f32_16x16x32_bf16 a[48:51], v[76:79], v[164:167], a[48:51]\n" \
"global_load_lds_dwordx4 v34, s[54:55] offset:-2048\n" \
"v_mfma_f32_16x16x32_bf16 a[52:55], v[72:75], v[168:171], a[52:55]\n" \
"ds_read_b64_tr_b16 v[136:137], v51 offset:0\n" \
"v_mfma_f32_16x16x32_bf16 a[52:55], v[76:79], v[172:175], a[52:55]\n" \
"v_mfma_f32_16x16x32_bf16 a[56:59], v[72:75], v[176:179], a[56:59]\n" \
"ds_read_b128 v[68:71], v49 offset:0\n" \
"v_mfma_f32_16x16x32_bf16 a[56:59], v[76:79], v[180:183], a[56:59]\n" \
"global_load_lds_dwordx4 v35, s[40:41] offset:-1024\n" \
"v_mfma_f32_16x16x32_bf16 a[60:63], v[72:75], v[184:187], a[60:63]\n" \
"ds_read_b64_tr_b16 v[138:139], v51 offset:4096\n" \
"v_mfma_f32_16x16x32_bf16 a[60:63], v[76:79], v[188:191], a[60:63]\n" \
"v_mfma_f32_16x16x32_bf16 a[80:83], v[80:83], v[160:163], a[80:83]\n" \
"ds_read_b128 v[72:75], v48 offset:2048\n" \
"v_mfma_f32_16x16x32_bf16 a[80:83], v[84:87], v[164:167], a[80:83]\n" \
"global_load_lds_dwordx4 v36, s[54:55] offset:0\n" \
"v_mfma_f32_16x16x32_bf16 a[84:87], v[80:83], v[168:171], a[84:87]\n" \
"ds_read_b64_tr_b16 v[140:141], v51 offset:16384\n" \
"v_mfma_f32_16x16x32_bf16 a[84:87], v[84:87], v[172:175], a[84:87]\n" \
"v_mfma_f32_16x16x32_bf16 a[88:91], v[80:83], v[176:179], a[88:91]\n" \
"ds_read_b128 v[76:79], v49 offset:2048\n" \
"v_mfma_f32_16x16x32_bf16 a[88:91], v[84:87], v[180:183], a[88:91]\n" \
"global_load_lds_dwordx4 v37, s[40:41] offset:1024\n" \
"v_mfma_f32_16x16x32_bf16 a[92:95], v[80:83], v[184:187], a[92:95]\n" \
"ds_read_b64_tr_b16 v[142:143], v51 offset:20480\n" \
"v_mfma_f32_16x16x32_bf16 a[92:95], v[84:87], v[188:191], a[92:95]\n" \
"v_mfma_f32_16x16x32_bf16 a[112:115], v[88:91], v[160:163], a[112:115]\n" \
"ds_read_b128 v[80:83], v48 offset:4096\n" \
"v_mfma_f32_16x16x32_bf16 a[112:115], v[92:95], v[164:167], a[112:115]\n" \
"global_load_lds_dwordx4 v38, s[54:55] offset:2048\n" \
"v_mfma_f32_16x16x32_bf16 a[116:119], v[88:91], v[168:171], a[116:119]\n" \
"ds_read_b64_tr_b16 v[144:145], v50 offset:512\n" \
"v_mfma_f32_16x16x32_bf16 a[116:119], v[92:95], v[172:175], a[116:119]\n" \
"v_mfma_f32_16x16x32_bf16 a[120:123], v[88:91], v[176:179], a[120:123]\n" \
"ds_read_b128 v[84:87], v49 offset:4096\n" \
"global_load_lds_dwordx4 v39, s[40:41] offset:3072\n" \
"v_mfma_f32_16x16x32_bf16 a[120:123], v[92:95], v[180:183], a[120:123]\n" \
"s_add_u32 s54, s54, s75\n" \
"s_addc_u32 s55, s55, 0\n" \
"v_mfma_f32_16x16x32_bf16 a[124:127], v[88:91], v[184:187], a[124:127]\n" \
"s_add_u32 s40, s40, s75\n" \
"s_addc_u32 s41, s41, 0\n" \
"ds_read_b64_tr_b16 v[146:147], v50 offset:4608\n" \
"v_mfma_f32_16x16x32_bf16 a[124:127], v[92:95], v[188:191], a[124:127]\n" \
"s_add_u32 s68, s67, 0x300000\n" \
"s_add_u32 s64, s16, s68\n" \
"s_addc_u32 s65, s17, 0\n" \
"v_mfma_f32_16x16x32_bf16 a[144:147], v[96:99], v[160:163], a[144:147]\n" \
"ds_read_b128 v[88:91], v48 offset:6144\n" \
"v_mfma_f32_16x16x32_bf16 a[144:147], v[100:103], v[164:167], a[144:147]\n" \
"global_load_dword v253, v248, s[64:65]\n" \
"v_mfma_f32_16x16x32_bf16 a[148:151], v[96:99], v[168:171], a[148:151]\n" \
"s_add_u32 s68, s66, 0x0\n" \
"ds_read_b64_tr_b16 v[148:149], v50 offset:16896\n" \
"v_mfma_f32_16x16x32_bf16 a[148:151], v[100:103], v[172:175], a[148:151]\n" \
"s_add_u32 s64, s8, s68\n" \
"s_addc_u32 s65, s9, 0\n" \
"v_mfma_f32_16x16x32_bf16 a[152:155], v[96:99], v[176:179], a[152:155]\n" \
"ds_read_b128 v[92:95], v49 offset:6144\n" \
"v_mfma_f32_16x16x32_bf16 a[152:155], v[100:103], v[180:183], a[152:155]\n" \
"global_load_dword v253, v248, s[64:65]\n" \
"v_mfma_f32_16x16x32_bf16 a[156:159], v[96:99], v[184:187], a[156:159]\n" \
"ds_read_b64_tr_b16 v[150:151], v50 offset:20992\n" \
"v_mfma_f32_16x16x32_bf16 a[156:159], v[100:103], v[188:191], a[156:159]\n" \
"v_mfma_f32_16x16x32_bf16 a[176:179], v[104:107], v[160:163], a[176:179]\n" \
"ds_read_b128 v[96:99], v48 offset:8192\n" \
"v_mfma_f32_16x16x32_bf16 a[176:179], v[108:111], v[164:167], a[176:179]\n" \
"v_mfma_f32_16x16x32_bf16 a[180:183], v[104:107], v[168:171], a[180:183]\n" \
"ds_read_b64_tr_b16 v[152:153], v51 offset:512\n" \
"v_mfma_f32_16x16x32_bf16 a[180:183], v[108:111], v[172:175], a[180:183]\n" \
"v_mfma_f32_16x16x32_bf16 a[184:187], v[104:107], v[176:179], a[184:187]\n" \
"ds_read_b128 v[100:103], v49 offset:8192\n" \
"v_mfma_f32_16x16x32_bf16 a[184:187], v[108:111], v[180:183], a[184:187]\n" \
"v_mfma_f32_16x16x32_bf16 a[188:191], v[104:107], v[184:187], a[188:191]\n" \
"ds_read_b64_tr_b16 v[154:155], v51 offset:4608\n" \
"v_mfma_f32_16x16x32_bf16 a[188:191], v[108:111], v[188:191], a[188:191]\n" \
"v_mfma_f32_16x16x32_bf16 a[208:211], v[112:115], v[160:163], a[208:211]\n" \
"ds_read_b128 v[104:107], v48 offset:10240\n" \
"v_mfma_f32_16x16x32_bf16 a[208:211], v[116:119], v[164:167], a[208:211]\n" \
"v_mfma_f32_16x16x32_bf16 a[212:215], v[112:115], v[168:171], a[212:215]\n" \
"ds_read_b64_tr_b16 v[156:157], v51 offset:16896\n" \
"v_mfma_f32_16x16x32_bf16 a[212:215], v[116:119], v[172:175], a[212:215]\n" \
"v_mfma_f32_16x16x32_bf16 a[216:219], v[112:115], v[176:179], a[216:219]\n" \
"ds_read_b128 v[108:111], v49 offset:10240\n" \
"v_mfma_f32_16x16x32_bf16 a[216:219], v[116:119], v[180:183], a[216:219]\n" \
"v_mfma_f32_16x16x32_bf16 a[220:223], v[112:115], v[184:187], a[220:223]\n" \
"ds_read_b64_tr_b16 v[158:159], v51 offset:20992\n" \
"v_mfma_f32_16x16x32_bf16 a[220:223], v[116:119], v[188:191], a[220:223]\n" \
"v_mfma_f32_16x16x32_bf16 a[240:243], v[120:123], v[160:163], a[240:243]\n" \
"ds_read_b128 v[112:115], v48 offset:12288\n" \
"v_mfma_f32_16x16x32_bf16 a[240:243], v[124:127], v[164:167], a[240:243]\n" \
"v_mfma_f32_16x16x32_bf16 a[244:247], v[120:123], v[168:171], a[244:247]\n" \
"v_mfma_f32_16x16x32_bf16 a[244:247], v[124:127], v[172:175], a[244:247]\n" \
"v_mfma_f32_16x16x32_bf16 a[248:251], v[120:123], v[176:179], a[248:251]\n" \
"ds_read_b128 v[116:119], v49 offset:12288\n" \
"v_mfma_f32_16x16x32_bf16 a[248:251], v[124:127], v[180:183], a[248:251]\n" \
"v_mfma_f32_16x16x32_bf16 a[252:255], v[120:123], v[184:187], a[252:255]\n" \
"v_mfma_f32_16x16x32_bf16 a[252:255], v[124:127], v[188:191], a[252:255]\n" \
"s_waitcnt lgkmcnt(1)\n" \
"v_mfma_f32_16x16x32_bf16 a[0:3], v[128:131], v[64:67], a[0:3]\n" \
"ds_read_b128 v[120:123], v48 offset:14336\n" \
"v_mfma_f32_16x16x32_bf16 a[0:3], v[132:135], v[68:71], a[0:3]\n" \
"v_mfma_f32_16x16x32_bf16 a[4:7], v[136:139], v[64:67], a[4:7]\n" \
"ds_read_b128 v[124:127], v49 offset:14336\n" \
"v_mfma_f32_16x16x32_bf16 a[4:7], v[140:143], v[68:71], a[4:7]\n" \
"s_add_u32 s68, s66, 0x100000\n" \
"v_mfma_f32_16x16x32_bf16 a[8:11], v[144:147], v[64:67], a[8:11]\n" \
"s_add_u32 s64, s8, s68\n" \
"s_addc_u32 s65, s9, 0\n" \
"ds_read_b64_tr_b16 v[160:161], v50 offset:1024\n" \
"v_mfma_f32_16x16x32_bf16 a[8:11], v[148:151], v[68:71], a[8:11]\n" \
"global_load_dword v253, v248, s[64:65]\n" \
"v_mfma_f32_16x16x32_bf16 a[12:15], v[152:155], v[64:67], a[12:15]\n" \
"s_add_u32 m0, s87, 36864\n" \
"ds_read_b64_tr_b16 v[162:163], v50 offset:5120\n" \
"v_mfma_f32_16x16x32_bf16 a[12:15], v[156:159], v[68:71], a[12:15]\n" \
"global_load_lds_dwordx4 v40, s[52:53] offset:-4096\n" \
"v_mfma_f32_16x16x32_bf16 a[32:35], v[128:131], v[72:75], a[32:35]\n" \
"ds_read_b64_tr_b16 v[164:165], v50 offset:17408\n" \
"v_mfma_f32_16x16x32_bf16 a[32:35], v[132:135], v[76:79], a[32:35]\n" \
"v_mfma_f32_16x16x32_bf16 a[36:39], v[136:139], v[72:75], a[36:39]\n" \
"ds_read_b64_tr_b16 v[166:167], v50 offset:21504\n" \
"v_mfma_f32_16x16x32_bf16 a[36:39], v[140:143], v[76:79], a[36:39]\n" \
"global_load_lds_dwordx4 v41, s[50:51] offset:-3072\n" \
"v_mfma_f32_16x16x32_bf16 a[40:43], v[144:147], v[72:75], a[40:43]\n" \
"ds_read_b64_tr_b16 v[168:169], v51 offset:1024\n" \
"v_mfma_f32_16x16x32_bf16 a[40:43], v[148:151], v[76:79], a[40:43]\n" \
"v_mfma_f32_16x16x32_bf16 a[44:47], v[152:155], v[72:75], a[44:47]\n" \
"ds_read_b64_tr_b16 v[170:171], v51 offset:5120\n" \
"v_mfma_f32_16x16x32_bf16 a[44:47], v[156:159], v[76:79], a[44:47]\n" \
"global_load_lds_dwordx4 v42, s[52:53] offset:-2048\n" \
"v_mfma_f32_16x16x32_bf16 a[64:67], v[128:131], v[80:83], a[64:67]\n" \
"ds_read_b64_tr_b16 v[172:173], v51 offset:17408\n" \
"v_mfma_f32_16x16x32_bf16 a[64:67], v[132:135], v[84:87], a[64:67]\n" \
"v_mfma_f32_16x16x32_bf16 a[68:71], v[136:139], v[80:83], a[68:71]\n" \
"ds_read_b64_tr_b16 v[174:175], v51 offset:21504\n" \
"v_mfma_f32_16x16x32_bf16 a[68:71], v[140:143], v[84:87], a[68:71]\n" \
"global_load_lds_dwordx4 v43, s[50:51] offset:-1024\n" \
"v_mfma_f32_16x16x32_bf16 a[72:75], v[144:147], v[80:83], a[72:75]\n" \
"ds_read_b64_tr_b16 v[176:177], v50 offset:1536\n" \
"v_mfma_f32_16x16x32_bf16 a[72:75], v[148:151], v[84:87], a[72:75]\n" \
"v_mfma_f32_16x16x32_bf16 a[76:79], v[152:155], v[80:83], a[76:79]\n" \
"ds_read_b64_tr_b16 v[178:179], v50 offset:5632\n" \
"v_mfma_f32_16x16x32_bf16 a[76:79], v[156:159], v[84:87], a[76:79]\n" \
"global_load_lds_dwordx4 v44, s[52:53] offset:0\n" \
"v_mfma_f32_16x16x32_bf16 a[96:99], v[128:131], v[88:91], a[96:99]\n" \
"ds_read_b64_tr_b16 v[180:181], v50 offset:17920\n" \
"v_mfma_f32_16x16x32_bf16 a[96:99], v[132:135], v[92:95], a[96:99]\n" \
"v_mfma_f32_16x16x32_bf16 a[100:103], v[136:139], v[88:91], a[100:103]\n" \
"ds_read_b64_tr_b16 v[182:183], v50 offset:22016\n" \
"v_mfma_f32_16x16x32_bf16 a[100:103], v[140:143], v[92:95], a[100:103]\n" \
"global_load_lds_dwordx4 v45, s[50:51] offset:1024\n" \
"v_mfma_f32_16x16x32_bf16 a[104:107], v[144:147], v[88:91], a[104:107]\n" \
"ds_read_b64_tr_b16 v[184:185], v51 offset:1536\n" \
"v_mfma_f32_16x16x32_bf16 a[104:107], v[148:151], v[92:95], a[104:107]\n" \
"v_mfma_f32_16x16x32_bf16 a[108:111], v[152:155], v[88:91], a[108:111]\n" \
"ds_read_b64_tr_b16 v[186:187], v51 offset:5632\n" \
"v_mfma_f32_16x16x32_bf16 a[108:111], v[156:159], v[92:95], a[108:111]\n" \
"global_load_lds_dwordx4 v46, s[52:53] offset:2048\n" \
"v_mfma_f32_16x16x32_bf16 a[128:131], v[128:131], v[96:99], a[128:131]\n" \
"ds_read_b64_tr_b16 v[188:189], v51 offset:17920\n" \
"v_mfma_f32_16x16x32_bf16 a[128:131], v[132:135], v[100:103], a[128:131]\n" \
"v_mfma_f32_16x16x32_bf16 a[132:135], v[136:139], v[96:99], a[132:135]\n" \
"ds_read_b64_tr_b16 v[190:191], v51 offset:22016\n" \
"v_mfma_f32_16x16x32_bf16 a[132:135], v[140:143], v[100:103], a[132:135]\n" \
"global_load_lds_dwordx4 v47, s[50:51] offset:3072\n" \
"v_mfma_f32_16x16x32_bf16 a[136:139], v[144:147], v[96:99], a[136:139]\n" \
"s_add_u32 s52, s52, s74\n" \
"s_addc_u32 s53, s53, 0\n" \
"v_mfma_f32_16x16x32_bf16 a[136:139], v[148:151], v[100:103], a[136:139]\n" \
"s_add_u32 s50, s50, s74\n" \
"s_addc_u32 s51, s51, 0\n" \
"v_mfma_f32_16x16x32_bf16 a[140:143], v[152:155], v[96:99], a[140:143]\n" \
"v_mfma_f32_16x16x32_bf16 a[140:143], v[156:159], v[100:103], a[140:143]\n" \
"v_mfma_f32_16x16x32_bf16 a[160:163], v[128:131], v[104:107], a[160:163]\n" \
"s_mul_i32 s76, s73, 0x18000\n" \
"s_add_u32 s77, s76, 0xffff8000\n" \
"s_sub_u32 s78, 0x10000, s76\n" \
"v_mfma_f32_16x16x32_bf16 a[160:163], v[132:135], v[108:111], a[160:163]\n" \
"v_add_u32 v48, s77, v48\n" \
"v_add_u32 v49, s77, v49\n" \
"v_mfma_f32_16x16x32_bf16 a[164:167], v[136:139], v[104:107], a[164:167]\n" \
"v_add_u32 v50, s78, v50\n" \
"v_add_u32 v51, s78, v51\n" \
"v_mfma_f32_16x16x32_bf16 a[164:167], v[140:143], v[108:111], a[164:167]\n" \
"v_mfma_f32_16x16x32_bf16 a[168:171], v[144:147], v[104:107], a[168:171]\n" \
"v_mfma_f32_16x16x32_bf16 a[168:171], v[148:151], v[108:111], a[168:171]\n" \
"v_mfma_f32_16x16x32_bf16 a[172:175], v[152:155], v[104:107], a[172:175]\n" \
"v_mfma_f32_16x16x32_bf16 a[172:175], v[156:159], v[108:111], a[172:175]\n" \
"s_waitcnt lgkmcnt(0)\n" \
"v_mfma_f32_16x16x32_bf16 a[192:195], v[128:131], v[112:115], a[192:195]\n" \
"v_mfma_f32_16x16x32_bf16 a[192:195], v[132:135], v[116:119], a[192:195]\n" \
"v_mfma_f32_16x16x32_bf16 a[196:199], v[136:139], v[112:115], a[196:199]\n" \
"v_mfma_f32_16x16x32_bf16 a[196:199], v[140:143], v[116:119], a[196:199]\n" \
"v_mfma_f32_16x16x32_bf16 a[200:203], v[144:147], v[112:115], a[200:203]\n" \
"v_mfma_f32_16x16x32_bf16 a[200:203], v[148:151], v[116:119], a[200:203]\n" \
"v_mfma_f32_16x16x32_bf16 a[204:207], v[152:155], v[112:115], a[204:207]\n" \
"v_mfma_f32_16x16x32_bf16 a[204:207], v[156:159], v[116:119], a[204:207]\n" \
"v_mfma_f32_16x16x32_bf16 a[224:227], v[128:131], v[120:123], a[224:227]\n" \
"v_mfma_f32_16x16x32_bf16 a[224:227], v[132:135], v[124:127], a[224:227]\n" \
"v_mfma_f32_16x16x32_bf16 a[228:231], v[136:139], v[120:123], a[228:231]\n" \
"v_mfma_f32_16x16x32_bf16 a[228:231], v[140:143], v[124:127], a[228:231]\n" \
"v_mfma_f32_16x16x32_bf16 a[232:235], v[144:147], v[120:123], a[232:235]\n" \
"v_mfma_f32_16x16x32_bf16 a[232:235], v[148:151], v[124:127], a[232:235]\n" \
"v_mfma_f32_16x16x32_bf16 a[236:239], v[152:155], v[120:123], a[236:239]\n" \
"s_waitcnt vmcnt(11) lgkmcnt(0)\n" \
"v_mfma_f32_16x16x32_bf16 a[236:239], v[156:159], v[124:127], a[236:239]\n" \
"s_barrier\n" \
"v_mfma_f32_16x16x32_bf16 a[16:19], v[64:67], v[160:163], a[16:19]\n" \
"s_add_u32 m0, s87, 4096\n" \
"ds_read_b64_tr_b16 v[128:129], v50 offset:0\n" \
"v_mfma_f32_16x16x32_bf16 a[16:19], v[68:71], v[164:167], a[16:19]\n" \
"global_load_lds_dwordx4 v32, s[54:55] offset:-4096\n" \
"v_mfma_f32_16x16x32_bf16 a[20:23], v[64:67], v[168:171], a[20:23]\n" \
"ds_read_b64_tr_b16 v[130:131], v50 offset:4096\n" \
"v_mfma_f32_16x16x32_bf16 a[20:23], v[68:71], v[172:175], a[20:23]\n" \
"v_mfma_f32_16x16x32_bf16 a[24:27], v[64:67], v[176:179], a[24:27]\n" \
"ds_read_b64_tr_b16 v[132:133], v50 offset:16384\n" \
"v_mfma_f32_16x16x32_bf16 a[24:27], v[68:71], v[180:183], a[24:27]\n" \
"global_load_lds_dwordx4 v33, s[40:41] offset:-3072\n" \
"v_mfma_f32_16x16x32_bf16 a[28:31], v[64:67], v[184:187], a[28:31]\n" \
"ds_read_b64_tr_b16 v[134:135], v50 offset:20480\n" \
"v_mfma_f32_16x16x32_bf16 a[28:31], v[68:71], v[188:191], a[28:31]\n" \
"v_mfma_f32_16x16x32_bf16 a[48:51], v[72:75], v[160:163], a[48:51]\n" \
"ds_read_b128 v[64:67], v48 offset:0\n" \
"v_mfma_f32_16x16x32_bf16 a[48:51], v[76:79], v[164:167], a[48:51]\n" \
"global_load_lds_dwordx4 v34, s[54:55] offset:-2048\n" \
"v_mfma_f32_16x16x32_bf16 a[52:55], v[72:75], v[168:171], a[52:55]\n" \
"ds_read_b64_tr_b16 v[136:137], v51 offset:0\n" \
"v_mfma_f32_16x16x32_bf16 a[52:55], v[76:79], v[172:175], a[52:55]\n" \
"v_mfma_f32_16x16x32_bf16 a[56:59], v[72:75], v[176:179], a[56:59]\n" \
"ds_read_b128 v[68:71], v49 offset:0\n" \
"v_mfma_f32_16x16x32_bf16 a[56:59], v[76:79], v[180:183], a[56:59]\n" \
"global_load_lds_dwordx4 v35, s[40:41] offset:-1024\n" \
"v_mfma_f32_16x16x32_bf16 a[60:63], v[72:75], v[184:187], a[60:63]\n" \
"ds_read_b64_tr_b16 v[138:139], v51 offset:4096\n" \
"v_mfma_f32_16x16x32_bf16 a[60:63], v[76:79], v[188:191], a[60:63]\n" \
"v_mfma_f32_16x16x32_bf16 a[80:83], v[80:83], v[160:163], a[80:83]\n" \
"ds_read_b128 v[72:75], v48 offset:2048\n" \
"v_mfma_f32_16x16x32_bf16 a[80:83], v[84:87], v[164:167], a[80:83]\n" \
"global_load_lds_dwordx4 v36, s[54:55] offset:0\n" \
"v_mfma_f32_16x16x32_bf16 a[84:87], v[80:83], v[168:171], a[84:87]\n" \
"ds_read_b64_tr_b16 v[140:141], v51 offset:16384\n" \
"v_mfma_f32_16x16x32_bf16 a[84:87], v[84:87], v[172:175], a[84:87]\n" \
"v_mfma_f32_16x16x32_bf16 a[88:91], v[80:83], v[176:179], a[88:91]\n" \
"ds_read_b128 v[76:79], v49 offset:2048\n" \
"v_mfma_f32_16x16x32_bf16 a[88:91], v[84:87], v[180:183], a[88:91]\n" \
"global_load_lds_dwordx4 v37, s[40:41] offset:1024\n" \
"v_mfma_f32_16x16x32_bf16 a[92:95], v[80:83], v[184:187], a[92:95]\n" \
"ds_read_b64_tr_b16 v[142:143], v51 offset:20480\n" \
"v_mfma_f32_16x16x32_bf16 a[92:95], v[84:87], v[188:191], a[92:95]\n" \
"v_mfma_f32_16x16x32_bf16 a[112:115], v[88:91], v[160:163], a[112:115]\n" \
"ds_read_b128 v[80:83], v48 offset:4096\n" \
"v_mfma_f32_16x16x32_bf16 a[112:115], v[92:95], v[164:167], a[112:115]\n" \
"global_load_lds_dwordx4 v38, s[54:55] offset:2048\n" \
"v_mfma_f32_16x16x32_bf16 a[116:119], v[88:91], v[168:171], a[116:119]\n" \
"ds_read_b64_tr_b16 v[144:145], v50 offset:512\n" \
"v_mfma_f32_16x16x32_bf16 a[116:119], v[92:95], v[172:175], a[116:119]\n" \
"v_mfma_f32_16x16x32_bf16 a[120:123], v[88:91], v[176:179], a[120:123]\n" \
"ds_read_b128 v[84:87], v49 offset:4096\n" \
"global_load_lds_dwordx4 v39, s[40:41] offset:3072\n" \
"v_mfma_f32_16x16x32_bf16 a[120:123], v[92:95], v[180:183], a[120:123]\n" \
"s_add_u32 s54, s54, s75\n" \
"s_addc_u32 s55, s55, 0\n" \
"v_mfma_f32_16x16x32_bf16 a[124:127], v[88:91], v[184:187], a[124:127]\n" \
"s_add_u32 s40, s40, s75\n" \
"s_addc_u32 s41, s41, 0\n" \
"ds_read_b64_tr_b16 v[146:147], v50 offset:4608\n" \
"v_mfma_f32_16x16x32_bf16 a[124:127], v[92:95], v[188:191], a[124:127]\n" \
"s_add_u32 s68, s66, 0x200000\n" \
"s_add_u32 s64, s8, s68\n" \
"s_addc_u32 s65, s9, 0\n" \
"v_mfma_f32_16x16x32_bf16 a[144:147], v[96:99], v[160:163], a[144:147]\n" \
"ds_read_b128 v[88:91], v48 offset:6144\n" \
"v_mfma_f32_16x16x32_bf16 a[144:147], v[100:103], v[164:167], a[144:147]\n" \
"global_load_dword v253, v248, s[64:65]\n" \
"v_mfma_f32_16x16x32_bf16 a[148:151], v[96:99], v[168:171], a[148:151]\n" \
"s_add_u32 s68, s67, 0x0\n" \
"ds_read_b64_tr_b16 v[148:149], v50 offset:16896\n" \
"v_mfma_f32_16x16x32_bf16 a[148:151], v[100:103], v[172:175], a[148:151]\n" \
"s_add_u32 s64, s12, s68\n" \
"Lpg1152_%=:\n" \
"Lst0x1_%=:\n" \
"s_cbranch_execz Lpg3310_%=\n" \
"s_addc_u32 s65, s13, 0\n" \
"v_mfma_f32_16x16x32_bf16 a[152:155], v[96:99], v[176:179], a[152:155]\n" \
"ds_read_b128 v[92:95], v49 offset:6144\n" \
"v_mfma_f32_16x16x32_bf16 a[152:155], v[100:103], v[180:183], a[152:155]\n" \
"global_load_dword v253, v248, s[64:65]\n" \
"v_mfma_f32_16x16x32_bf16 a[156:159], v[96:99], v[184:187], a[156:159]\n" \
"ds_read_b64_tr_b16 v[150:151], v50 offset:20992\n" \
"v_mfma_f32_16x16x32_bf16 a[156:159], v[100:103], v[188:191], a[156:159]\n" \
"v_mfma_f32_16x16x32_bf16 a[176:179], v[104:107], v[160:163], a[176:179]\n" \
"ds_read_b128 v[96:99], v48 offset:8192\n" \
"v_mfma_f32_16x16x32_bf16 a[176:179], v[108:111], v[164:167], a[176:179]\n" \
"v_mfma_f32_16x16x32_bf16 a[180:183], v[104:107], v[168:171], a[180:183]\n" \
"ds_read_b64_tr_b16 v[152:153], v51 offset:512\n" \
"v_mfma_f32_16x16x32_bf16 a[180:183], v[108:111], v[172:175], a[180:183]\n" \
"v_mfma_f32_16x16x32_bf16 a[184:187], v[104:107], v[176:179], a[184:187]\n" \
"ds_read_b128 v[100:103], v49 offset:8192\n" \
"v_mfma_f32_16x16x32_bf16 a[184:187], v[108:111], v[180:183], a[184:187]\n" \
"v_mfma_f32_16x16x32_bf16 a[188:191], v[104:107], v[184:187], a[188:191]\n" \
"ds_read_b64_tr_b16 v[154:155], v51 offset:4608\n" \
"v_mfma_f32_16x16x32_bf16 a[188:191], v[108:111], v[188:191], a[188:191]\n" \
"v_mfma_f32_16x16x32_bf16 a[208:211], v[112:115], v[160:163], a[208:211]\n" \
"ds_read_b128 v[104:107], v48 offset:10240\n" \
"v_mfma_f32_16x16x32_bf16 a[208:211], v[116:119], v[164:167], a[208:211]\n" \
"v_mfma_f32_16x16x32_bf16 a[212:215], v[112:115], v[168:171], a[212:215]\n" \
"ds_read_b64_tr_b16 v[156:157], v51 offset:16896\n" \
"v_mfma_f32_16x16x32_bf16 a[212:215], v[116:119], v[172:175], a[212:215]\n" \
"v_mfma_f32_16x16x32_bf16 a[216:219], v[112:115], v[176:179], a[216:219]\n" \
"ds_read_b128 v[108:111], v49 offset:10240\n" \
"v_mfma_f32_16x16x32_bf16 a[216:219], v[116:119], v[180:183], a[216:219]\n" \
"v_mfma_f32_16x16x32_bf16 a[220:223], v[112:115], v[184:187], a[220:223]\n" \
"ds_read_b64_tr_b16 v[158:159], v51 offset:20992\n" \
"v_mfma_f32_16x16x32_bf16 a[220:223], v[116:119], v[188:191], a[220:223]\n" \
"v_mfma_f32_16x16x32_bf16 a[240:243], v[120:123], v[160:163], a[240:243]\n" \
"ds_read_b128 v[112:115], v48 offset:12288\n" \
"v_mfma_f32_16x16x32_bf16 a[240:243], v[124:127], v[164:167], a[240:243]\n" \
"v_mfma_f32_16x16x32_bf16 a[244:247], v[120:123], v[168:171], a[244:247]\n" \
"v_mfma_f32_16x16x32_bf16 a[244:247], v[124:127], v[172:175], a[244:247]\n" \
"v_mfma_f32_16x16x32_bf16 a[248:251], v[120:123], v[176:179], a[248:251]\n" \
"ds_read_b128 v[116:119], v49 offset:12288\n" \
"v_mfma_f32_16x16x32_bf16 a[248:251], v[124:127], v[180:183], a[248:251]\n" \
"v_mfma_f32_16x16x32_bf16 a[252:255], v[120:123], v[184:187], a[252:255]\n" \
"v_mfma_f32_16x16x32_bf16 a[252:255], v[124:127], v[188:191], a[252:255]\n" \
"s_waitcnt lgkmcnt(1)\n" \
"v_mfma_f32_16x16x32_bf16 a[0:3], v[128:131], v[64:67], a[0:3]\n" \
"ds_read_b128 v[120:123], v48 offset:14336\n" \
"v_mfma_f32_16x16x32_bf16 a[0:3], v[132:135], v[68:71], a[0:3]\n" \
"v_mfma_f32_16x16x32_bf16 a[4:7], v[136:139], v[64:67], a[4:7]\n" \
"ds_read_b128 v[124:127], v49 offset:14336\n" \
"v_mfma_f32_16x16x32_bf16 a[4:7], v[140:143], v[68:71], a[4:7]\n" \
"s_add_u32 s68, s66, 0x300000\n" \
"v_mfma_f32_16x16x32_bf16 a[8:11], v[144:147], v[64:67], a[8:11]\n" \
"s_add_u32 s64, s8, s68\n" \
"s_addc_u32 s65, s9, 0\n" \
"ds_read_b64_tr_b16 v[160:161], v50 offset:1024\n" \
"v_mfma_f32_16x16x32_bf16 a[8:11], v[148:151], v[68:71], a[8:11]\n" \
"global_load_dword v253, v248, s[64:65]\n" \
"v_mfma_f32_16x16x32_bf16 a[12:15], v[152:155], v[64:67], a[12:15]\n" \
"s_add_u32 m0, s87, 135168\n" \
"ds_read_b64_tr_b16 v[162:163], v50 offset:5120\n" \
"v_mfma_f32_16x16x32_bf16 a[12:15], v[156:159], v[68:71], a[12:15]\n" \
"global_load_lds_dwordx4 v40, s[52:53] offset:-4096\n" \
"v_mfma_f32_16x16x32_bf16 a[32:35], v[128:131], v[72:75], a[32:35]\n" \
"ds_read_b64_tr_b16 v[164:165], v50 offset:17408\n" \
"v_mfma_f32_16x16x32_bf16 a[32:35], v[132:135], v[76:79], a[32:35]\n" \
"v_mfma_f32_16x16x32_bf16 a[36:39], v[136:139], v[72:75], a[36:39]\n" \
"ds_read_b64_tr_b16 v[166:167], v50 offset:21504\n" \
"v_mfma_f32_16x16x32_bf16 a[36:39], v[140:143], v[76:79], a[36:39]\n" \
"global_load_lds_dwordx4 v41, s[50:51] offset:-3072\n" \
"v_mfma_f32_16x16x32_bf16 a[40:43], v[144:147], v[72:75], a[40:43]\n" \
"ds_read_b64_tr_b16 v[168:169], v51 offset:1024\n" \
"v_mfma_f32_16x16x32_bf16 a[40:43], v[148:151], v[76:79], a[40:43]\n" \
"v_mfma_f32_16x16x32_bf16 a[44:47], v[152:155], v[72:75], a[44:47]\n" \
"ds_read_b64_tr_b16 v[170:171], v51 offset:5120\n" \
"v_mfma_f32_16x16x32_bf16 a[44:47], v[156:159], v[76:79], a[44:47]\n" \
"global_load_lds_dwordx4 v42, s[52:53] offset:-2048\n" \
"v_mfma_f32_16x16x32_bf16 a[64:67], v[128:131], v[80:83], a[64:67]\n" \
"ds_read_b64_tr_b16 v[172:173], v51 offset:17408\n" \
"v_mfma_f32_16x16x32_bf16 a[64:67], v[132:135], v[84:87], a[64:67]\n" \
"v_mfma_f32_16x16x32_bf16 a[68:71], v[136:139], v[80:83], a[68:71]\n" \
"ds_read_b64_tr_b16 v[174:175], v51 offset:21504\n" \
"v_mfma_f32_16x16x32_bf16 a[68:71], v[140:143], v[84:87], a[68:71]\n" \
"global_load_lds_dwordx4 v43, s[50:51] offset:-1024\n" \
"v_mfma_f32_16x16x32_bf16 a[72:75], v[144:147], v[80:83], a[72:75]\n" \
"ds_read_b64_tr_b16 v[176:177], v50 offset:1536\n" \
"v_mfma_f32_16x16x32_bf16 a[72:75], v[148:151], v[84:87], a[72:75]\n" \
"v_mfma_f32_16x16x32_bf16 a[76:79], v[152:155], v[80:83], a[76:79]\n" \
"ds_read_b64_tr_b16 v[178:179], v50 offset:5632\n" \
"v_mfma_f32_16x16x32_bf16 a[76:79], v[156:159], v[84:87], a[76:79]\n" \
"global_load_lds_dwordx4 v44, s[52:53] offset:0\n" \
"v_mfma_f32_16x16x32_bf16 a[96:99], v[128:131], v[88:91], a[96:99]\n" \
"ds_read_b64_tr_b16 v[180:181], v50 offset:17920\n" \
"v_mfma_f32_16x16x32_bf16 a[96:99], v[132:135], v[92:95], a[96:99]\n" \
"v_mfma_f32_16x16x32_bf16 a[100:103], v[136:139], v[88:91], a[100:103]\n" \
"ds_read_b64_tr_b16 v[182:183], v50 offset:22016\n" \
"v_mfma_f32_16x16x32_bf16 a[100:103], v[140:143], v[92:95], a[100:103]\n" \
"global_load_lds_dwordx4 v45, s[50:51] offset:1024\n" \
"v_mfma_f32_16x16x32_bf16 a[104:107], v[144:147], v[88:91], a[104:107]\n" \
"ds_read_b64_tr_b16 v[184:185], v51 offset:1536\n" \
"v_mfma_f32_16x16x32_bf16 a[104:107], v[148:151], v[92:95], a[104:107]\n" \
"v_mfma_f32_16x16x32_bf16 a[108:111], v[152:155], v[88:91], a[108:111]\n" \
"ds_read_b64_tr_b16 v[186:187], v51 offset:5632\n" \
"v_mfma_f32_16x16x32_bf16 a[108:111], v[156:159], v[92:95], a[108:111]\n" \
"global_load_lds_dwordx4 v46, s[52:53] offset:2048\n" \
"v_mfma_f32_16x16x32_bf16 a[128:131], v[128:131], v[96:99], a[128:131]\n" \
"ds_read_b64_tr_b16 v[188:189], v51 offset:17920\n" \
"v_mfma_f32_16x16x32_bf16 a[128:131], v[132:135], v[100:103], a[128:131]\n" \
"v_mfma_f32_16x16x32_bf16 a[132:135], v[136:139], v[96:99], a[132:135]\n" \
"ds_read_b64_tr_b16 v[190:191], v51 offset:22016\n" \
"v_mfma_f32_16x16x32_bf16 a[132:135], v[140:143], v[100:103], a[132:135]\n" \
"global_load_lds_dwordx4 v47, s[50:51] offset:3072\n" \
"v_mfma_f32_16x16x32_bf16 a[136:139], v[144:147], v[96:99], a[136:139]\n" \
"s_add_u32 s52, s52, s74\n" \
"s_addc_u32 s53, s53, 0\n" \
"v_mfma_f32_16x16x32_bf16 a[136:139], v[148:151], v[100:103], a[136:139]\n" \
"s_add_u32 s50, s50, s74\n" \
"s_addc_u32 s51, s51, 0\n" \
"v_mfma_f32_16x16x32_bf16 a[140:143], v[152:155], v[96:99], a[140:143]\n" \
"v_mfma_f32_16x16x32_bf16 a[140:143], v[156:159], v[100:103], a[140:143]\n" \
"v_mfma_f32_16x16x32_bf16 a[160:163], v[128:131], v[104:107], a[160:163]\n" \
"s_mul_i32 s76, s73, 0x10000\n" \
"s_add_u32 s77, s76, 0xfffe8000\n" \
"s_sub_u32 s78, 0xffff8000, s76\n" \
"v_mfma_f32_16x16x32_bf16 a[160:163], v[132:135], v[108:111], a[160:163]\n" \
"v_add_u32 v48, s77, v48\n" \
"v_add_u32 v49, s77, v49\n" \
"v_mfma_f32_16x16x32_bf16 a[164:167], v[136:139], v[104:107], a[164:167]\n" \
"v_add_u32 v50, s78, v50\n" \
"v_add_u32 v51, s78, v51\n" \
"v_mfma_f32_16x16x32_bf16 a[164:167], v[140:143], v[108:111], a[164:167]\n" \
"v_mfma_f32_16x16x32_bf16 a[168:171], v[144:147], v[104:107], a[168:171]\n" \
"v_mfma_f32_16x16x32_bf16 a[168:171], v[148:151], v[108:111], a[168:171]\n" \
"v_mfma_f32_16x16x32_bf16 a[172:175], v[152:155], v[104:107], a[172:175]\n" \
"v_mfma_f32_16x16x32_bf16 a[172:175], v[156:159], v[108:111], a[172:175]\n" \
"s_waitcnt lgkmcnt(0)\n" \
"v_mfma_f32_16x16x32_bf16 a[192:195], v[128:131], v[112:115], a[192:195]\n" \
"v_mfma_f32_16x16x32_bf16 a[192:195], v[132:135], v[116:119], a[192:195]\n" \
"v_mfma_f32_16x16x32_bf16 a[196:199], v[136:139], v[112:115], a[196:199]\n" \
"v_mfma_f32_16x16x32_bf16 a[196:199], v[140:143], v[116:119], a[196:199]\n" \
"v_mfma_f32_16x16x32_bf16 a[200:203], v[144:147], v[112:115], a[200:203]\n" \
"v_mfma_f32_16x16x32_bf16 a[200:203], v[148:151], v[116:119], a[200:203]\n" \
"v_mfma_f32_16x16x32_bf16 a[204:207], v[152:155], v[112:115], a[204:207]\n" \
"v_mfma_f32_16x16x32_bf16 a[204:207], v[156:159], v[116:119], a[204:207]\n" \
"v_mfma_f32_16x16x32_bf16 a[224:227], v[128:131], v[120:123], a[224:227]\n" \
"v_mfma_f32_16x16x32_bf16 a[224:227], v[132:135], v[124:127], a[224:227]\n" \
"v_mfma_f32_16x16x32_bf16 a[228:231], v[136:139], v[120:123], a[228:231]\n" \
"v_mfma_f32_16x16x32_bf16 a[228:231], v[140:143], v[124:127], a[228:231]\n" \
"v_mfma_f32_16x16x32_bf16 a[232:235], v[144:147], v[120:123], a[232:235]\n" \
"v_mfma_f32_16x16x32_bf16 a[232:235], v[148:151], v[124:127], a[232:235]\n" \
"v_mfma_f32_16x16x32_bf16 a[236:239], v[152:155], v[120:123], a[236:239]\n" \
"s_waitcnt vmcnt(11) lgkmcnt(0)\n" \
"v_mfma_f32_16x16x32_bf16 a[236:239], v[156:159], v[124:127], a[236:239]\n" \
"s_barrier\n" \
"v_mfma_f32_16x16x32_bf16 a[16:19], v[64:67], v[160:163], a[16:19]\n" \
"s_add_u32 m0, s87, 69632\n" \
"ds_read_b64_tr_b16 v[128:129], v50 offset:0\n" \
"v_mfma_f32_16x16x32_bf16 a[16:19], v[68:71], v[164:167], a[16:19]\n" \
"global_load_lds_dwordx4 v32, s[54:55] offset:-4096\n" \
"v_mfma_f32_16x16x32_bf16 a[20:23], v[64:67], v[168:171], a[20:23]\n" \
"ds_read_b64_tr_b16 v[130:131], v50 offset:4096\n" \
"v_mfma_f32_16x16x32_bf16 a[20:23], v[68:71], v[172:175], a[20:23]\n" \
"v_mfma_f32_16x16x32_bf16 a[24:27], v[64:67], v[176:179], a[24:27]\n" \
"ds_read_b64_tr_b16 v[132:133], v50 offset:16384\n" \
"v_mfma_f32_16x16x32_bf16 a[24:27], v[68:71], v[180:183], a[24:27]\n" \
"global_load_lds_dwordx4 v33, s[40:41] offset:-3072\n" \
"v_mfma_f32_16x16x32_bf16 a[28:31], v[64:67], v[184:187], a[28:31]\n" \
"ds_read_b64_tr_b16 v[134:135], v50 offset:20480\n" \
"v_mfma_f32_16x16x32_bf16 a[28:31], v[68:71], v[188:191], a[28:31]\n" \
"v_mfma_f32_16x16x32_bf16 a[48:51], v[72:75], v[160:163], a[48:51]\n" \
"ds_read_b128 v[64:67], v48 offset:0\n" \
"v_mfma_f32_16x16x32_bf16 a[48:51], v[76:79], v[164:167], a[48:51]\n" \
"global_load_lds_dwordx4 v34, s[54:55] offset:-2048\n" \
"v_mfma_f32_16x16x32_bf16 a[52:55], v[72:75], v[168:171], a[52:55]\n" \
"ds_read_b64_tr_b16 v[136:137], v51 offset:0\n" \
"v_mfma_f32_16x16x32_bf16 a[52:55], v[76:79], v[172:175], a[52:55]\n" \
"v_mfma_f32_16x16x32_bf16 a[56:59], v[72:75], v[176:179], a[56:59]\n" \
"ds_read_b128 v[68:71], v49 offset:0\n" \
"v_mfma_f32_16x16x32_bf16 a[56:59], v[76:79], v[180:183], a[56:59]\n" \
"global_load_lds_dwordx4 v35, s[40:41] offset:-1024\n" \
"v_mfma_f32_16x16x32_bf16 a[60:63], v[72:75], v[184:187], a[60:63]\n" \
"ds_read_b64_tr_b16 v[138:139], v51 offset:4096\n" \
"v_mfma_f32_16x16x32_bf16 a[60:63], v[76:79], v[188:191], a[60:63]\n" \
"v_mfma_f32_16x16x32_bf16 a[80:83], v[80:83], v[160:163], a[80:83]\n" \
"ds_read_b128 v[72:75], v48 offset:2048\n" \
"v_mfma_f32_16x16x32_bf16 a[80:83], v[84:87], v[164:167], a[80:83]\n" \
"global_load_lds_dwordx4 v36, s[54:55] offset:0\n" \
"v_mfma_f32_16x16x32_bf16 a[84:87], v[80:83], v[168:171], a[84:87]\n" \
"ds_read_b64_tr_b16 v[140:141], v51 offset:16384\n" \
"v_mfma_f32_16x16x32_bf16 a[84:87], v[84:87], v[172:175], a[84:87]\n" \
"v_mfma_f32_16x16x32_bf16 a[88:91], v[80:83], v[176:179], a[88:91]\n" \
"ds_read_b128 v[76:79], v49 offset:2048\n" \
"v_mfma_f32_16x16x32_bf16 a[88:91], v[84:87], v[180:183], a[88:91]\n" \
"global_load_lds_dwordx4 v37, s[40:41] offset:1024\n" \
"v_mfma_f32_16x16x32_bf16 a[92:95], v[80:83], v[184:187], a[92:95]\n" \
"ds_read_b64_tr_b16 v[142:143], v51 offset:20480\n" \
"v_mfma_f32_16x16x32_bf16 a[92:95], v[84:87], v[188:191], a[92:95]\n" \
"v_mfma_f32_16x16x32_bf16 a[112:115], v[88:91], v[160:163], a[112:115]\n" \
"ds_read_b128 v[80:83], v48 offset:4096\n" \
"v_mfma_f32_16x16x32_bf16 a[112:115], v[92:95], v[164:167], a[112:115]\n" \
"global_load_lds_dwordx4 v38, s[54:55] offset:2048\n" \
"v_mfma_f32_16x16x32_bf16 a[116:119], v[88:91], v[168:171], a[116:119]\n" \
"ds_read_b64_tr_b16 v[144:145], v50 offset:512\n" \
"v_mfma_f32_16x16x32_bf16 a[116:119], v[92:95], v[172:175], a[116:119]\n" \
"v_mfma_f32_16x16x32_bf16 a[120:123], v[88:91], v[176:179], a[120:123]\n" \
"ds_read_b128 v[84:87], v49 offset:4096\n" \
"global_load_lds_dwordx4 v39, s[40:41] offset:3072\n" \
"v_mfma_f32_16x16x32_bf16 a[120:123], v[92:95], v[180:183], a[120:123]\n" \
"s_add_u32 s54, s54, s75\n" \
"s_addc_u32 s55, s55, 0\n" \
"v_mfma_f32_16x16x32_bf16 a[124:127], v[88:91], v[184:187], a[124:127]\n" \
"s_add_u32 s40, s40, s75\n" \
"s_addc_u32 s41, s41, 0\n" \
"ds_read_b64_tr_b16 v[146:147], v50 offset:4608\n" \
"v_mfma_f32_16x16x32_bf16 a[124:127], v[92:95], v[188:191], a[124:127]\n" \
"s_add_u32 s68, s67, 0x100000\n" \
"s_add_u32 s64, s12, s68\n" \
"s_addc_u32 s65, s13, 0\n" \
"v_mfma_f32_16x16x32_bf16 a[144:147], v[96:99], v[160:163], a[144:147]\n" \
"ds_read_b128 v[88:91], v48 offset:6144\n" \
"v_mfma_f32_16x16x32_bf16 a[144:147], v[100:103], v[164:167], a[144:147]\n" \
"global_load_dword v253, v248, s[64:65]\n" \
"v_mfma_f32_16x16x32_bf16 a[148:151], v[96:99], v[168:171], a[148:151]\n" \
"s_add_u32 s68, s67, 0x500000\n" \
"ds_read_b64_tr_b16 v[148:149], v50 offset:16896\n" \
"v_mfma_f32_16x16x32_bf16 a[148:151], v[100:103], v[172:175], a[148:151]\n" \
"s_add_u32 s64, s12, s68\n" \
"s_addc_u32 s65, s13, 0\n" \
"v_mfma_f32_16x16x32_bf16 a[152:155], v[96:99], v[176:179], a[152:155]\n" \
"ds_read_b128 v[92:95], v49 offset:6144\n" \
"v_mfma_f32_16x16x32_bf16 a[152:155], v[100:103], v[180:183], a[152:155]\n" \
"global_load_dword v253, v248, s[64:65]\n" \
"v_mfma_f32_16x16x32_bf16 a[156:159], v[96:99], v[184:187], a[156:159]\n" \
"ds_read_b64_tr_b16 v[150:151], v50 offset:20992\n" \
"v_mfma_f32_16x16x32_bf16 a[156:159], v[100:103], v[188:191], a[156:159]\n" \
"v_mfma_f32_16x16x32_bf16 a[176:179], v[104:107], v[160:163], a[176:179]\n" \
"ds_read_b128 v[96:99], v48 offset:8192\n" \
"v_mfma_f32_16x16x32_bf16 a[176:179], v[108:111], v[164:167], a[176:179]\n" \
"v_mfma_f32_16x16x32_bf16 a[180:183], v[104:107], v[168:171], a[180:183]\n" \
"ds_read_b64_tr_b16 v[152:153], v51 offset:512\n" \
"v_mfma_f32_16x16x32_bf16 a[180:183], v[108:111], v[172:175], a[180:183]\n" \
"v_mfma_f32_16x16x32_bf16 a[184:187], v[104:107], v[176:179], a[184:187]\n" \
"ds_read_b128 v[100:103], v49 offset:8192\n" \
"v_mfma_f32_16x16x32_bf16 a[184:187], v[108:111], v[180:183], a[184:187]\n" \
"v_mfma_f32_16x16x32_bf16 a[188:191], v[104:107], v[184:187], a[188:191]\n" \
"ds_read_b64_tr_b16 v[154:155], v51 offset:4608\n" \
"v_mfma_f32_16x16x32_bf16 a[188:191], v[108:111], v[188:191], a[188:191]\n" \
"v_mfma_f32_16x16x32_bf16 a[208:211], v[112:115], v[160:163], a[208:211]\n" \
"ds_read_b128 v[104:107], v48 offset:10240\n" \
"v_mfma_f32_16x16x32_bf16 a[208:211], v[116:119], v[164:167], a[208:211]\n" \
"v_mfma_f32_16x16x32_bf16 a[212:215], v[112:115], v[168:171], a[212:215]\n" \
"ds_read_b64_tr_b16 v[156:157], v51 offset:16896\n" \
"v_mfma_f32_16x16x32_bf16 a[212:215], v[116:119], v[172:175], a[212:215]\n" \
"v_mfma_f32_16x16x32_bf16 a[216:219], v[112:115], v[176:179], a[216:219]\n" \
"ds_read_b128 v[108:111], v49 offset:10240\n" \
"v_mfma_f32_16x16x32_bf16 a[216:219], v[116:119], v[180:183], a[216:219]\n" \
"v_mfma_f32_16x16x32_bf16 a[220:223], v[112:115], v[184:187], a[220:223]\n" \
"ds_read_b64_tr_b16 v[158:159], v51 offset:20992\n" \
"v_mfma_f32_16x16x32_bf16 a[220:223], v[116:119], v[188:191], a[220:223]\n" \
"v_mfma_f32_16x16x32_bf16 a[240:243], v[120:123], v[160:163], a[240:243]\n" \
"ds_read_b128 v[112:115], v48 offset:12288\n" \
"v_mfma_f32_16x16x32_bf16 a[240:243], v[124:127], v[164:167], a[240:243]\n" \
"v_mfma_f32_16x16x32_bf16 a[244:247], v[120:123], v[168:171], a[244:247]\n" \
"v_mfma_f32_16x16x32_bf16 a[244:247], v[124:127], v[172:175], a[244:247]\n" \
"v_mfma_f32_16x16x32_bf16 a[248:251], v[120:123], v[176:179], a[248:251]\n" \
"ds_read_b128 v[116:119], v49 offset:12288\n" \
"v_mfma_f32_16x16x32_bf16 a[248:251], v[124:127], v[180:183], a[248:251]\n" \
"v_mfma_f32_16x16x32_bf16 a[252:255], v[120:123], v[184:187], a[252:255]\n" \
"v_mfma_f32_16x16x32_bf16 a[252:255], v[124:127], v[188:191], a[252:255]\n" \
"s_waitcnt lgkmcnt(1)\n" \
"v_mfma_f32_16x16x32_bf16 a[0:3], v[128:131], v[64:67], a[0:3]\n" \
"ds_read_b128 v[120:123], v48 offset:14336\n" \
"v_mfma_f32_16x16x32_bf16 a[0:3], v[132:135], v[68:71], a[0:3]\n" \
"v_mfma_f32_16x16x32_bf16 a[4:7], v[136:139], v[64:67], a[4:7]\n" \
"ds_read_b128 v[124:127], v49 offset:14336\n" \
"v_mfma_f32_16x16x32_bf16 a[4:7], v[140:143], v[68:71], a[4:7]\n" \
"s_add_u32 s68, s67, 0x200000\n" \
"v_mfma_f32_16x16x32_bf16 a[8:11], v[144:147], v[64:67], a[8:11]\n" \
"s_add_u32 s64, s12, s68\n" \
"s_addc_u32 s65, s13, 0\n" \
"ds_read_b64_tr_b16 v[160:161], v50 offset:1024\n" \
"v_mfma_f32_16x16x32_bf16 a[8:11], v[148:151], v[68:71], a[8:11]\n" \
"global_load_dword v253, v248, s[64:65]\n" \
"v_mfma_f32_16x16x32_bf16 a[12:15], v[152:155], v[64:67], a[12:15]\n" \
"s_add_u32 m0, s87, 102400\n" \
"ds_read_b64_tr_b16 v[162:163], v50 offset:5120\n" \
"v_mfma_f32_16x16x32_bf16 a[12:15], v[156:159], v[68:71], a[12:15]\n" \
"global_load_lds_dwordx4 v40, s[52:53] offset:-4096\n" \
"v_mfma_f32_16x16x32_bf16 a[32:35], v[128:131], v[72:75], a[32:35]\n" \
"ds_read_b64_tr_b16 v[164:165], v50 offset:17408\n" \
"v_mfma_f32_16x16x32_bf16 a[32:35], v[132:135], v[76:79], a[32:35]\n" \
"v_mfma_f32_16x16x32_bf16 a[36:39], v[136:139], v[72:75], a[36:39]\n" \
"ds_read_b64_tr_b16 v[166:167], v50 offset:21504\n" \
"v_mfma_f32_16x16x32_bf16 a[36:39], v[140:143], v[76:79], a[36:39]\n" \
"global_load_lds_dwordx4 v41, s[50:51] offset:-3072\n" \
"v_mfma_f32_16x16x32_bf16 a[40:43], v[144:147], v[72:75], a[40:43]\n" \
"ds_read_b64_tr_b16 v[168:169], v51 offset:1024\n" \
"v_mfma_f32_16x16x32_bf16 a[40:43], v[148:151], v[76:79], a[40:43]\n" \
"v_mfma_f32_16x16x32_bf16 a[44:47], v[152:155], v[72:75], a[44:47]\n" \
"ds_read_b64_tr_b16 v[170:171], v51 offset:5120\n" \
"v_mfma_f32_16x16x32_bf16 a[44:47], v[156:159], v[76:79], a[44:47]\n" \
"global_load_lds_dwordx4 v42, s[52:53] offset:-2048\n" \
"v_mfma_f32_16x16x32_bf16 a[64:67], v[128:131], v[80:83], a[64:67]\n" \
"ds_read_b64_tr_b16 v[172:173], v51 offset:17408\n" \
"v_mfma_f32_16x16x32_bf16 a[64:67], v[132:135], v[84:87], a[64:67]\n" \
"v_mfma_f32_16x16x32_bf16 a[68:71], v[136:139], v[80:83], a[68:71]\n" \
"ds_read_b64_tr_b16 v[174:175], v51 offset:21504\n" \
"v_mfma_f32_16x16x32_bf16 a[68:71], v[140:143], v[84:87], a[68:71]\n" \
"global_load_lds_dwordx4 v43, s[50:51] offset:-1024\n" \
"v_mfma_f32_16x16x32_bf16 a[72:75], v[144:147], v[80:83], a[72:75]\n" \
"ds_read_b64_tr_b16 v[176:177], v50 offset:1536\n" \
"v_mfma_f32_16x16x32_bf16 a[72:75], v[148:151], v[84:87], a[72:75]\n" \
"v_mfma_f32_16x16x32_bf16 a[76:79], v[152:155], v[80:83], a[76:79]\n" \
"ds_read_b64_tr_b16 v[178:179], v50 offset:5632\n" \
"v_mfma_f32_16x16x32_bf16 a[76:79], v[156:159], v[84:87], a[76:79]\n" \
"global_load_lds_dwordx4 v44, s[52:53] offset:0\n" \
"v_mfma_f32_16x16x32_bf16 a[96:99], v[128:131], v[88:91], a[96:99]\n" \
"ds_read_b64_tr_b16 v[180:181], v50 offset:17920\n" \
"v_mfma_f32_16x16x32_bf16 a[96:99], v[132:135], v[92:95], a[96:99]\n" \
"v_mfma_f32_16x16x32_bf16 a[100:103], v[136:139], v[88:91], a[100:103]\n" \
"ds_read_b64_tr_b16 v[182:183], v50 offset:22016\n" \
"v_mfma_f32_16x16x32_bf16 a[100:103], v[140:143], v[92:95], a[100:103]\n" \
"global_load_lds_dwordx4 v45, s[50:51] offset:1024\n" \
"v_mfma_f32_16x16x32_bf16 a[104:107], v[144:147], v[88:91], a[104:107]\n" \
"ds_read_b64_tr_b16 v[184:185], v51 offset:1536\n" \
"v_mfma_f32_16x16x32_bf16 a[104:107], v[148:151], v[92:95], a[104:107]\n" \
"v_mfma_f32_16x16x32_bf16 a[108:111], v[152:155], v[88:91], a[108:111]\n" \
"ds_read_b64_tr_b16 v[186:187], v51 offset:5632\n" \
"v_mfma_f32_16x16x32_bf16 a[108:111], v[156:159], v[92:95], a[108:111]\n" \
"global_load_lds_dwordx4 v46, s[52:53] offset:2048\n" \
"v_mfma_f32_16x16x32_bf16 a[128:131], v[128:131], v[96:99], a[128:131]\n" \
"ds_read_b64_tr_b16 v[188:189], v51 offset:17920\n" \
"v_mfma_f32_16x16x32_bf16 a[128:131], v[132:135], v[100:103], a[128:131]\n" \
"v_mfma_f32_16x16x32_bf16 a[132:135], v[136:139], v[96:99], a[132:135]\n" \
"ds_read_b64_tr_b16 v[190:191], v51 offset:22016\n" \
"v_mfma_f32_16x16x32_bf16 a[132:135], v[140:143], v[100:103], a[132:135]\n" \
"global_load_lds_dwordx4 v47, s[50:51] offset:3072\n" \
"v_mfma_f32_16x16x32_bf16 a[136:139], v[144:147], v[96:99], a[136:139]\n" \
"v_mfma_f32_16x16x32_bf16 a[136:139], v[148:151], v[100:103], a[136:139]\n" \
"v_mfma_f32_16x16x32_bf16 a[140:143], v[152:155], v[96:99], a[140:143]\n" \
"v_mfma_f32_16x16x32_bf16 a[140:143], v[156:159], v[100:103], a[140:143]\n" \
"v_mfma_f32_16x16x32_bf16 a[160:163], v[128:131], v[104:107], a[160:163]\n" \
"s_mul_i32 s76, s73, 0x8000\n" \
"s_add_u32 s77, s76, 0x10000\n" \
"s_sub_u32 s78, 0x18000, s76\n" \
"v_mfma_f32_16x16x32_bf16 a[160:163], v[132:135], v[108:111], a[160:163]\n" \
"v_add_u32 v48, s77, v48\n" \
"v_add_u32 v49, s77, v49\n" \
"v_mfma_f32_16x16x32_bf16 a[164:167], v[136:139], v[104:107], a[164:167]\n" \
"v_add_u32 v50, s78, v50\n" \
"v_add_u32 v51, s78, v51\n" \
"v_mfma_f32_16x16x32_bf16 a[164:167], v[140:143], v[108:111], a[164:167]\n" \
"v_mfma_f32_16x16x32_bf16 a[168:171], v[144:147], v[104:107], a[168:171]\n" \
"v_mfma_f32_16x16x32_bf16 a[168:171], v[148:151], v[108:111], a[168:171]\n" \
"v_mfma_f32_16x16x32_bf16 a[172:175], v[152:155], v[104:107], a[172:175]\n" \
"v_mfma_f32_16x16x32_bf16 a[172:175], v[156:159], v[108:111], a[172:175]\n" \
"s_waitcnt lgkmcnt(0)\n" \
"v_mfma_f32_16x16x32_bf16 a[192:195], v[128:131], v[112:115], a[192:195]\n" \
"v_mfma_f32_16x16x32_bf16 a[192:195], v[132:135], v[116:119], a[192:195]\n" \
"v_mfma_f32_16x16x32_bf16 a[196:199], v[136:139], v[112:115], a[196:199]\n" \
"v_mfma_f32_16x16x32_bf16 a[196:199], v[140:143], v[116:119], a[196:199]\n" \
"v_mfma_f32_16x16x32_bf16 a[200:203], v[144:147], v[112:115], a[200:203]\n" \
"v_mfma_f32_16x16x32_bf16 a[200:203], v[148:151], v[116:119], a[200:203]\n" \
"v_mfma_f32_16x16x32_bf16 a[204:207], v[152:155], v[112:115], a[204:207]\n" \
"v_mfma_f32_16x16x32_bf16 a[204:207], v[156:159], v[116:119], a[204:207]\n" \
"v_mfma_f32_16x16x32_bf16 a[224:227], v[128:131], v[120:123], a[224:227]\n" \
"v_mfma_f32_16x16x32_bf16 a[224:227], v[132:135], v[124:127], a[224:227]\n" \
"v_mfma_f32_16x16x32_bf16 a[228:231], v[136:139], v[120:123], a[228:231]\n" \
"v_mfma_f32_16x16x32_bf16 a[228:231], v[140:143], v[124:127], a[228:231]\n" \
"v_mfma_f32_16x16x32_bf16 a[232:235], v[144:147], v[120:123], a[232:235]\n" \
"v_mfma_f32_16x16x32_bf16 a[232:235], v[148:151], v[124:127], a[232:235]\n" \
"v_mfma_f32_16x16x32_bf16 a[236:239], v[152:155], v[120:123], a[236:239]\n" \
"s_waitcnt vmcnt(11) lgkmcnt(0)\n" \
"v_mfma_f32_16x16x32_bf16 a[236:239], v[156:159], v[124:127], a[236:239]\n" \
"s_barrier\n" \
"v_mfma_f32_16x16x32_bf16 a[16:19], v[64:67], v[160:163], a[16:19]\n" \
"s_add_u32 m0, s87, 36864\n" \
"ds_read_b64_tr_b16 v[128:129], v50 offset:0\n" \
"v_mfma_f32_16x16x32_bf16 a[16:19], v[68:71], v[164:167], a[16:19]\n" \
"global_load_lds_dwordx4 v32, s[54:55] offset:-4096\n" \
"v_mfma_f32_16x16x32_bf16 a[20:23], v[64:67], v[168:171], a[20:23]\n" \
"ds_read_b64_tr_b16 v[130:131], v50 offset:4096\n" \
"v_mfma_f32_16x16x32_bf16 a[20:23], v[68:71], v[172:175], a[20:23]\n" \
"v_mfma_f32_16x16x32_bf16 a[24:27], v[64:67], v[176:179], a[24:27]\n" \
"ds_read_b64_tr_b16 v[132:133], v50 offset:16384\n" \
"v_mfma_f32_16x16x32_bf16 a[24:27], v[68:71], v[180:183], a[24:27]\n" \
"global_load_lds_dwordx4 v33, s[40:41] offset:-3072\n" \
"v_mfma_f32_16x16x32_bf16 a[28:31], v[64:67], v[184:187], a[28:31]\n" \
"ds_read_b64_tr_b16 v[134:135], v50 offset:20480\n" \
"v_mfma_f32_16x16x32_bf16 a[28:31], v[68:71], v[188:191], a[28:31]\n" \
"v_mfma_f32_16x16x32_bf16 a[48:51], v[72:75], v[160:163], a[48:51]\n" \
"ds_read_b128 v[64:67], v48 offset:0\n" \
"v_mfma_f32_16x16x32_bf16 a[48:51], v[76:79], v[164:167], a[48:51]\n" \
"global_load_lds_dwordx4 v34, s[54:55] offset:-2048\n" \
"v_mfma_f32_16x16x32_bf16 a[52:55], v[72:75], v[168:171], a[52:55]\n" \
"ds_read_b64_tr_b16 v[136:137], v51 offset:0\n" \
"v_mfma_f32_16x16x32_bf16 a[52:55], v[76:79], v[172:175], a[52:55]\n" \
"v_mfma_f32_16x16x32_bf16 a[56:59], v[72:75], v[176:179], a[56:59]\n" \
"ds_read_b128 v[68:71], v49 offset:0\n" \
"v_mfma_f32_16x16x32_bf16 a[56:59], v[76:79], v[180:183], a[56:59]\n" \
"global_load_lds_dwordx4 v35, s[40:41] offset:-1024\n" \
"v_mfma_f32_16x16x32_bf16 a[60:63], v[72:75], v[184:187], a[60:63]\n" \
"ds_read_b64_tr_b16 v[138:139], v51 offset:4096\n" \
"v_mfma_f32_16x16x32_bf16 a[60:63], v[76:79], v[188:191], a[60:63]\n" \
"v_mfma_f32_16x16x32_bf16 a[80:83], v[80:83], v[160:163], a[80:83]\n" \
"ds_read_b128 v[72:75], v48 offset:2048\n" \
"v_mfma_f32_16x16x32_bf16 a[80:83], v[84:87], v[164:167], a[80:83]\n" \
"global_load_lds_dwordx4 v36, s[54:55] offset:0\n" \
"v_mfma_f32_16x16x32_bf16 a[84:87], v[80:83], v[168:171], a[84:87]\n" \
"ds_read_b64_tr_b16 v[140:141], v51 offset:16384\n" \
"v_mfma_f32_16x16x32_bf16 a[84:87], v[84:87], v[172:175], a[84:87]\n" \
"v_mfma_f32_16x16x32_bf16 a[88:91], v[80:83], v[176:179], a[88:91]\n" \
"ds_read_b128 v[76:79], v49 offset:2048\n" \
"v_mfma_f32_16x16x32_bf16 a[88:91], v[84:87], v[180:183], a[88:91]\n" \
"global_load_lds_dwordx4 v37, s[40:41] offset:1024\n" \
"v_mfma_f32_16x16x32_bf16 a[92:95], v[80:83], v[184:187], a[92:95]\n" \
"ds_read_b64_tr_b16 v[142:143], v51 offset:20480\n" \
"v_mfma_f32_16x16x32_bf16 a[92:95], v[84:87], v[188:191], a[92:95]\n" \
"v_mfma_f32_16x16x32_bf16 a[112:115], v[88:91], v[160:163], a[112:115]\n" \
"ds_read_b128 v[80:83], v48 offset:4096\n" \
"v_mfma_f32_16x16x32_bf16 a[112:115], v[92:95], v[164:167], a[112:115]\n" \
"global_load_lds_dwordx4 v38, s[54:55] offset:2048\n" \
"v_mfma_f32_16x16x32_bf16 a[116:119], v[88:91], v[168:171], a[116:119]\n" \
"ds_read_b64_tr_b16 v[144:145], v50 offset:512\n" \
"v_mfma_f32_16x16x32_bf16 a[116:119], v[92:95], v[172:175], a[116:119]\n" \
"v_mfma_f32_16x16x32_bf16 a[120:123], v[88:91], v[176:179], a[120:123]\n" \
"ds_read_b128 v[84:87], v49 offset:4096\n" \
"v_mfma_f32_16x16x32_bf16 a[120:123], v[92:95], v[180:183], a[120:123]\n" \
"global_load_lds_dwordx4 v39, s[40:41] offset:3072\n" \
"s_add_u32 s68, s67, 0x300000\n" \
"v_mfma_f32_16x16x32_bf16 a[124:127], v[88:91], v[184:187], a[124:127]\n" \
"s_add_u32 s64, s12, s68\n" \
"s_addc_u32 s65, s13, 0\n" \
"ds_read_b64_tr_b16 v[146:147], v50 offset:4608\n" \
"v_mfma_f32_16x16x32_bf16 a[124:127], v[92:95], v[188:191], a[124:127]\n" \
"v_mfma_f32_16x16x32_bf16 a[144:147], v[96:99], v[160:163], a[144:147]\n" \
"ds_read_b128 v[88:91], v48 offset:6144\n" \
"v_mfma_f32_16x16x32_bf16 a[144:147], v[100:103], v[164:167], a[144:147]\n" \
"global_load_dword v253, v248, s[64:65]\n" \
"v_mfma_f32_16x16x32_bf16 a[148:151], v[96:99], v[168:171], a[148:151]\n" \
"s_add_u32 s68, s67, 0x400000\n" \
"ds_read_b64_tr_b16 v[148:149], v50 offset:16896\n" \
"v_mfma_f32_16x16x32_bf16 a[148:151], v[100:103], v[172:175], a[148:151]\n" \
"s_add_u32 s64, s12, s68\n" \
"s_addc_u32 s65, s13, 0\n" \
"v_mfma_f32_16x16x32_bf16 a[152:155], v[96:99], v[176:179], a[152:155]\n" \
"ds_read_b128 v[92:95], v49 offset:6144\n" \
"v_mfma_f32_16x16x32_bf16 a[152:155], v[100:103], v[180:183], a[152:155]\n" \
"global_load_dword v253, v248, s[64:65]\n" \
"v_mfma_f32_16x16x32_bf16 a[156:159], v[96:99], v[184:187], a[156:159]\n" \
"ds_read_b64_tr_b16 v[150:151], v50 offset:20992\n" \
"v_mfma_f32_16x16x32_bf16 a[156:159], v[100:103], v[188:191], a[156:159]\n" \
"v_mfma_f32_16x16x32_bf16 a[176:179], v[104:107], v[160:163], a[176:179]\n" \
"ds_read_b128 v[96:99], v48 offset:8192\n" \
"v_mfma_f32_16x16x32_bf16 a[176:179], v[108:111], v[164:167], a[176:179]\n" \
"v_mfma_f32_16x16x32_bf16 a[180:183], v[104:107], v[168:171], a[180:183]\n" \
"ds_read_b64_tr_b16 v[152:153], v51 offset:512\n" \
"v_mfma_f32_16x16x32_bf16 a[180:183], v[108:111], v[172:175], a[180:183]\n" \
"v_mfma_f32_16x16x32_bf16 a[184:187], v[104:107], v[176:179], a[184:187]\n" \
"ds_read_b128 v[100:103], v49 offset:8192\n" \
"v_mfma_f32_16x16x32_bf16 a[184:187], v[108:111], v[180:183], a[184:187]\n" \
"v_mfma_f32_16x16x32_bf16 a[188:191], v[104:107], v[184:187], a[188:191]\n" \
"ds_read_b64_tr_b16 v[154:155], v51 offset:4608\n" \
"v_mfma_f32_16x16x32_bf16 a[188:191], v[108:111], v[188:191], a[188:191]\n" \
"v_mfma_f32_16x16x32_bf16 a[208:211], v[112:115], v[160:163], a[208:211]\n" \
"ds_read_b128 v[104:107], v48 offset:10240\n" \
"v_mfma_f32_16x16x32_bf16 a[208:211], v[116:119], v[164:167], a[208:211]\n" \
"v_mfma_f32_16x16x32_bf16 a[212:215], v[112:115], v[168:171], a[212:215]\n" \
"ds_read_b64_tr_b16 v[156:157], v51 offset:16896\n" \
"v_mfma_f32_16x16x32_bf16 a[212:215], v[116:119], v[172:175], a[212:215]\n" \
"v_mfma_f32_16x16x32_bf16 a[216:219], v[112:115], v[176:179], a[216:219]\n" \
"ds_read_b128 v[108:111], v49 offset:10240\n" \
"v_mfma_f32_16x16x32_bf16 a[216:219], v[116:119], v[180:183], a[216:219]\n" \
"v_mfma_f32_16x16x32_bf16 a[220:223], v[112:115], v[184:187], a[220:223]\n" \
"ds_read_b64_tr_b16 v[158:159], v51 offset:20992\n" \
"v_mfma_f32_16x16x32_bf16 a[220:223], v[116:119], v[188:191], a[220:223]\n" \
"v_mfma_f32_16x16x32_bf16 a[240:243], v[120:123], v[160:163], a[240:243]\n" \
"ds_read_b128 v[112:115], v48 offset:12288\n" \
"v_mfma_f32_16x16x32_bf16 a[240:243], v[124:127], v[164:167], a[240:243]\n" \
"v_mfma_f32_16x16x32_bf16 a[244:247], v[120:123], v[168:171], a[244:247]\n" \
"v_mfma_f32_16x16x32_bf16 a[244:247], v[124:127], v[172:175], a[244:247]\n" \
"v_mfma_f32_16x16x32_bf16 a[248:251], v[120:123], v[176:179], a[248:251]\n" \
"ds_read_b128 v[116:119], v49 offset:12288\n" \
"v_mfma_f32_16x16x32_bf16 a[248:251], v[124:127], v[180:183], a[248:251]\n" \
"v_mfma_f32_16x16x32_bf16 a[252:255], v[120:123], v[184:187], a[252:255]\n" \
"v_mfma_f32_16x16x32_bf16 a[252:255], v[124:127], v[188:191], a[252:255]\n" \
"s_waitcnt lgkmcnt(1)\n" \
"v_mfma_f32_16x16x32_bf16 a[0:3], v[128:131], v[64:67], a[0:3]\n" \
"ds_read_b128 v[120:123], v48 offset:14336\n" \
"v_mfma_f32_16x16x32_bf16 a[0:3], v[132:135], v[68:71], a[0:3]\n" \
"v_mfma_f32_16x16x32_bf16 a[4:7], v[136:139], v[64:67], a[4:7]\n" \
"ds_read_b128 v[124:127], v49 offset:14336\n" \
"s_add_u32 m0, s95, 4096\n" \
"v_mfma_f32_16x16x32_bf16 a[4:7], v[140:143], v[68:71], a[4:7]\n" \
"global_load_lds_dwordx4 v228, s[44:45] offset:-4096\n" \
"v_mfma_f32_16x16x32_bf16 a[8:11], v[144:147], v[64:67], a[8:11]\n" \
"ds_read_b64_tr_b16 v[160:161], v50 offset:1024\n" \
"v_mfma_f32_16x16x32_bf16 a[8:11], v[148:151], v[68:71], a[8:11]\n" \
"v_mfma_f32_16x16x32_bf16 a[12:15], v[152:155], v[64:67], a[12:15]\n" \
"ds_read_b64_tr_b16 v[162:163], v50 offset:5120\n" \
"v_mfma_f32_16x16x32_bf16 a[12:15], v[156:159], v[68:71], a[12:15]\n" \
"global_load_lds_dwordx4 v229, s[44:45] offset:-3072\n" \
"v_mfma_f32_16x16x32_bf16 a[32:35], v[128:131], v[72:75], a[32:35]\n" \
"s_add_u32 s44, s44, 0x180000\n" \
"s_addc_u32 s45, s45, 0\n" \
"ds_read_b64_tr_b16 v[164:165], v50 offset:17408\n" \
"v_mfma_f32_16x16x32_bf16 a[32:35], v[132:135], v[76:79], a[32:35]\n" \
"v_mfma_f32_16x16x32_bf16 a[36:39], v[136:139], v[72:75], a[36:39]\n" \
"ds_read_b64_tr_b16 v[166:167], v50 offset:21504\n" \
"v_mfma_f32_16x16x32_bf16 a[36:39], v[140:143], v[76:79], a[36:39]\n" \
"global_load_lds_dwordx4 v228, s[42:43] offset:-2048\n" \
"v_mfma_f32_16x16x32_bf16 a[40:43], v[144:147], v[72:75], a[40:43]\n" \
"ds_read_b64_tr_b16 v[168:169], v51 offset:1024\n" \
"v_mfma_f32_16x16x32_bf16 a[40:43], v[148:151], v[76:79], a[40:43]\n" \
"v_mfma_f32_16x16x32_bf16 a[44:47], v[152:155], v[72:75], a[44:47]\n" \
"ds_read_b64_tr_b16 v[170:171], v51 offset:5120\n" \
"v_mfma_f32_16x16x32_bf16 a[44:47], v[156:159], v[76:79], a[44:47]\n" \
"global_load_lds_dwordx4 v229, s[42:43] offset:-1024\n" \
"v_mfma_f32_16x16x32_bf16 a[64:67], v[128:131], v[80:83], a[64:67]\n" \
"s_add_u32 s42, s42, 0x180000\n" \
"s_addc_u32 s43, s43, 0\n" \
"ds_read_b64_tr_b16 v[172:173], v51 offset:17408\n" \
"v_mfma_f32_16x16x32_bf16 a[64:67], v[132:135], v[84:87], a[64:67]\n" \
"v_mfma_f32_16x16x32_bf16 a[68:71], v[136:139], v[80:83], a[68:71]\n" \
"s_add_u32 m0, s95, 20480\n" \
"ds_read_b64_tr_b16 v[174:175], v51 offset:21504\n" \
"v_mfma_f32_16x16x32_bf16 a[68:71], v[140:143], v[84:87], a[68:71]\n" \
"global_load_lds_dwordx4 v228, s[44:45] offset:-4096\n" \
"v_mfma_f32_16x16x32_bf16 a[72:75], v[144:147], v[80:83], a[72:75]\n" \
"ds_read_b64_tr_b16 v[176:177], v50 offset:1536\n" \
"v_mfma_f32_16x16x32_bf16 a[72:75], v[148:151], v[84:87], a[72:75]\n" \
"v_mfma_f32_16x16x32_bf16 a[76:79], v[152:155], v[80:83], a[76:79]\n" \
"ds_read_b64_tr_b16 v[178:179], v50 offset:5632\n" \
"v_mfma_f32_16x16x32_bf16 a[76:79], v[156:159], v[84:87], a[76:79]\n" \
"Lpg1685_%=:\n" \
"Lst0x2_%=:\n" \
"s_cbranch_execz Lpg3851_%=\n" \
"global_load_lds_dwordx4 v229, s[44:45] offset:-3072\n" \
"v_mfma_f32_16x16x32_bf16 a[96:99], v[128:131], v[88:91], a[96:99]\n" \
"s_add_u32 s44, s44, 0x400000\n" \
"s_addc_u32 s45, s45, 0\n" \
"ds_read_b64_tr_b16 v[180:181], v50 offset:17920\n" \
"v_mfma_f32_16x16x32_bf16 a[96:99], v[132:135], v[92:95], a[96:99]\n" \
"v_mfma_f32_16x16x32_bf16 a[100:103], v[136:139], v[88:91], a[100:103]\n" \
"ds_read_b64_tr_b16 v[182:183], v50 offset:22016\n" \
"v_mfma_f32_16x16x32_bf16 a[100:103], v[140:143], v[92:95], a[100:103]\n" \
"global_load_lds_dwordx4 v228, s[42:43] offset:-2048\n" \
"v_mfma_f32_16x16x32_bf16 a[104:107], v[144:147], v[88:91], a[104:107]\n" \
"ds_read_b64_tr_b16 v[184:185], v51 offset:1536\n" \
"v_mfma_f32_16x16x32_bf16 a[104:107], v[148:151], v[92:95], a[104:107]\n" \
"v_mfma_f32_16x16x32_bf16 a[108:111], v[152:155], v[88:91], a[108:111]\n" \
"ds_read_b64_tr_b16 v[186:187], v51 offset:5632\n" \
"v_mfma_f32_16x16x32_bf16 a[108:111], v[156:159], v[92:95], a[108:111]\n" \
"global_load_lds_dwordx4 v229, s[42:43] offset:-1024\n" \
"v_mfma_f32_16x16x32_bf16 a[128:131], v[128:131], v[96:99], a[128:131]\n" \
"s_add_u32 s42, s42, 0x400000\n" \
"s_addc_u32 s43, s43, 0\n" \
"ds_read_b64_tr_b16 v[188:189], v51 offset:17920\n" \
"v_mfma_f32_16x16x32_bf16 a[128:131], v[132:135], v[100:103], a[128:131]\n" \
"v_mfma_f32_16x16x32_bf16 a[132:135], v[136:139], v[96:99], a[132:135]\n" \
"ds_read_b64_tr_b16 v[190:191], v51 offset:22016\n" \
"v_mfma_f32_16x16x32_bf16 a[132:135], v[140:143], v[100:103], a[132:135]\n" \
"v_mfma_f32_16x16x32_bf16 a[136:139], v[144:147], v[96:99], a[136:139]\n" \
"v_mfma_f32_16x16x32_bf16 a[136:139], v[148:151], v[100:103], a[136:139]\n" \
"v_mfma_f32_16x16x32_bf16 a[140:143], v[152:155], v[96:99], a[140:143]\n" \
"v_mfma_f32_16x16x32_bf16 a[140:143], v[156:159], v[100:103], a[140:143]\n" \
"v_mfma_f32_16x16x32_bf16 a[160:163], v[128:131], v[104:107], a[160:163]\n" \
"s_mul_i32 s76, s73, 0x0\n" \
"s_add_u32 s77, s76, 0xffff8000\n" \
"s_sub_u32 s78, 0xffff8000, s76\n" \
"v_mfma_f32_16x16x32_bf16 a[160:163], v[132:135], v[108:111], a[160:163]\n" \
"v_add_u32 v48, s77, v48\n" \
"v_add_u32 v49, s77, v49\n" \
"v_mfma_f32_16x16x32_bf16 a[164:167], v[136:139], v[104:107], a[164:167]\n" \
"v_add_u32 v50, s78, v50\n" \
"v_add_u32 v51, s78, v51\n" \
"v_mfma_f32_16x16x32_bf16 a[164:167], v[140:143], v[108:111], a[164:167]\n" \
"v_mfma_f32_16x16x32_bf16 a[168:171], v[144:147], v[104:107], a[168:171]\n" \
"v_mfma_f32_16x16x32_bf16 a[168:171], v[148:151], v[108:111], a[168:171]\n" \
"v_mfma_f32_16x16x32_bf16 a[172:175], v[152:155], v[104:107], a[172:175]\n" \
"v_mfma_f32_16x16x32_bf16 a[172:175], v[156:159], v[108:111], a[172:175]\n" \
"s_waitcnt lgkmcnt(0)\n" \
"v_mfma_f32_16x16x32_bf16 a[192:195], v[128:131], v[112:115], a[192:195]\n" \
"v_mfma_f32_16x16x32_bf16 a[192:195], v[132:135], v[116:119], a[192:195]\n" \
"v_mfma_f32_16x16x32_bf16 a[196:199], v[136:139], v[112:115], a[196:199]\n" \
"v_mfma_f32_16x16x32_bf16 a[196:199], v[140:143], v[116:119], a[196:199]\n" \
"v_mfma_f32_16x16x32_bf16 a[200:203], v[144:147], v[112:115], a[200:203]\n" \
"v_mfma_f32_16x16x32_bf16 a[200:203], v[148:151], v[116:119], a[200:203]\n" \
"v_mfma_f32_16x16x32_bf16 a[204:207], v[152:155], v[112:115], a[204:207]\n" \
"v_mfma_f32_16x16x32_bf16 a[204:207], v[156:159], v[116:119], a[204:207]\n" \
"v_mfma_f32_16x16x32_bf16 a[224:227], v[128:131], v[120:123], a[224:227]\n" \
"v_mfma_f32_16x16x32_bf16 a[224:227], v[132:135], v[124:127], a[224:227]\n" \
"v_mfma_f32_16x16x32_bf16 a[228:231], v[136:139], v[120:123], a[228:231]\n" \
"v_mfma_f32_16x16x32_bf16 a[228:231], v[140:143], v[124:127], a[228:231]\n" \
"v_mfma_f32_16x16x32_bf16 a[232:235], v[144:147], v[120:123], a[232:235]\n" \
"v_mfma_f32_16x16x32_bf16 a[232:235], v[148:151], v[124:127], a[232:235]\n" \
"v_mfma_f32_16x16x32_bf16 a[236:239], v[152:155], v[120:123], a[236:239]\n" \
"s_waitcnt vmcnt(10) lgkmcnt(0)\n" \
"v_mfma_f32_16x16x32_bf16 a[236:239], v[156:159], v[124:127], a[236:239]\n" \
"s_barrier\n" \
"v_mfma_f32_16x16x32_bf16 a[16:19], v[64:67], v[160:163], a[16:19]\n" \
"s_add_u32 m0, s87, 135168\n" \
"ds_read_b64_tr_b16 v[128:129], v50 offset:0\n" \
"v_mfma_f32_16x16x32_bf16 a[16:19], v[68:71], v[164:167], a[16:19]\n" \
"global_load_lds_dwordx4 v224, s[48:49] offset:-4096\n" \
"v_mfma_f32_16x16x32_bf16 a[20:23], v[64:67], v[168:171], a[20:23]\n" \
"ds_read_b64_tr_b16 v[130:131], v50 offset:4096\n" \
"v_mfma_f32_16x16x32_bf16 a[20:23], v[68:71], v[172:175], a[20:23]\n" \
"v_mfma_f32_16x16x32_bf16 a[24:27], v[64:67], v[176:179], a[24:27]\n" \
"ds_read_b64_tr_b16 v[132:133], v50 offset:16384\n" \
"v_mfma_f32_16x16x32_bf16 a[24:27], v[68:71], v[180:183], a[24:27]\n" \
"global_load_lds_dwordx4 v225, s[48:49] offset:-3072\n" \
"v_mfma_f32_16x16x32_bf16 a[28:31], v[64:67], v[184:187], a[28:31]\n" \
"ds_read_b64_tr_b16 v[134:135], v50 offset:20480\n" \
"v_mfma_f32_16x16x32_bf16 a[28:31], v[68:71], v[188:191], a[28:31]\n" \
"v_mfma_f32_16x16x32_bf16 a[48:51], v[72:75], v[160:163], a[48:51]\n" \
"ds_read_b128 v[64:67], v48 offset:0\n" \
"v_mfma_f32_16x16x32_bf16 a[48:51], v[76:79], v[164:167], a[48:51]\n" \
"global_load_lds_dwordx4 v226, s[48:49] offset:-2048\n" \
"v_mfma_f32_16x16x32_bf16 a[52:55], v[72:75], v[168:171], a[52:55]\n" \
"ds_read_b64_tr_b16 v[136:137], v51 offset:0\n" \
"v_mfma_f32_16x16x32_bf16 a[52:55], v[76:79], v[172:175], a[52:55]\n" \
"v_mfma_f32_16x16x32_bf16 a[56:59], v[72:75], v[176:179], a[56:59]\n" \
"ds_read_b128 v[68:71], v49 offset:0\n" \
"v_mfma_f32_16x16x32_bf16 a[56:59], v[76:79], v[180:183], a[56:59]\n" \
"global_load_lds_dwordx4 v227, s[48:49] offset:-1024\n" \
"v_mfma_f32_16x16x32_bf16 a[60:63], v[72:75], v[184:187], a[60:63]\n" \
"s_add_u32 s48, s48, 0x180\n" \
"s_addc_u32 s49, s49, 0\n" \
"ds_read_b64_tr_b16 v[138:139], v51 offset:4096\n" \
"v_mfma_f32_16x16x32_bf16 a[60:63], v[76:79], v[188:191], a[60:63]\n" \
"v_mfma_f32_16x16x32_bf16 a[80:83], v[80:83], v[160:163], a[80:83]\n" \
"ds_read_b128 v[72:75], v48 offset:2048\n" \
"v_mfma_f32_16x16x32_bf16 a[80:83], v[84:87], v[164:167], a[80:83]\n" \
"global_load_lds_dwordx4 v224, s[46:47] offset:0\n" \
"v_mfma_f32_16x16x32_bf16 a[84:87], v[80:83], v[168:171], a[84:87]\n" \
"ds_read_b64_tr_b16 v[140:141], v51 offset:16384\n" \
"v_mfma_f32_16x16x32_bf16 a[84:87], v[84:87], v[172:175], a[84:87]\n" \
"v_mfma_f32_16x16x32_bf16 a[88:91], v[80:83], v[176:179], a[88:91]\n" \
"ds_read_b128 v[76:79], v49 offset:2048\n" \
"v_mfma_f32_16x16x32_bf16 a[88:91], v[84:87], v[180:183], a[88:91]\n" \
"global_load_lds_dwordx4 v225, s[46:47] offset:1024\n" \
"v_mfma_f32_16x16x32_bf16 a[92:95], v[80:83], v[184:187], a[92:95]\n" \
"ds_read_b64_tr_b16 v[142:143], v51 offset:20480\n" \
"v_mfma_f32_16x16x32_bf16 a[92:95], v[84:87], v[188:191], a[92:95]\n" \
"v_mfma_f32_16x16x32_bf16 a[112:115], v[88:91], v[160:163], a[112:115]\n" \
"ds_read_b128 v[80:83], v48 offset:4096\n" \
"v_mfma_f32_16x16x32_bf16 a[112:115], v[92:95], v[164:167], a[112:115]\n" \
"global_load_lds_dwordx4 v226, s[46:47] offset:2048\n" \
"v_mfma_f32_16x16x32_bf16 a[116:119], v[88:91], v[168:171], a[116:119]\n" \
"ds_read_b64_tr_b16 v[144:145], v50 offset:512\n" \
"v_mfma_f32_16x16x32_bf16 a[116:119], v[92:95], v[172:175], a[116:119]\n" \
"v_mfma_f32_16x16x32_bf16 a[120:123], v[88:91], v[176:179], a[120:123]\n" \
"ds_read_b128 v[84:87], v49 offset:4096\n" \
"v_mfma_f32_16x16x32_bf16 a[120:123], v[92:95], v[180:183], a[120:123]\n" \
"global_load_lds_dwordx4 v227, s[46:47] offset:3072\n" \
"v_mfma_f32_16x16x32_bf16 a[124:127], v[88:91], v[184:187], a[124:127]\n" \
"s_add_u32 s46, s46, 0x180\n" \
"s_addc_u32 s47, s47, 0\n" \
"ds_read_b64_tr_b16 v[146:147], v50 offset:4608\n" \
"v_mfma_f32_16x16x32_bf16 a[124:127], v[92:95], v[188:191], a[124:127]\n" \
"s_add_u32 s68, s67, 0x600000\n" \
"s_add_u32 s64, s12, s68\n" \
"s_addc_u32 s65, s13, 0\n" \
"v_mfma_f32_16x16x32_bf16 a[144:147], v[96:99], v[160:163], a[144:147]\n" \
"ds_read_b128 v[88:91], v48 offset:6144\n" \
"v_mfma_f32_16x16x32_bf16 a[144:147], v[100:103], v[164:167], a[144:147]\n" \
"global_load_dword v253, v248, s[64:65]\n" \
"v_mfma_f32_16x16x32_bf16 a[148:151], v[96:99], v[168:171], a[148:151]\n" \
"s_add_u32 s68, s67, 0x700000\n" \
"ds_read_b64_tr_b16 v[148:149], v50 offset:16896\n" \
"v_mfma_f32_16x16x32_bf16 a[148:151], v[100:103], v[172:175], a[148:151]\n" \
"s_add_u32 s64, s12, s68\n" \
"s_addc_u32 s65, s13, 0\n" \
"v_mfma_f32_16x16x32_bf16 a[152:155], v[96:99], v[176:179], a[152:155]\n" \
"ds_read_b128 v[92:95], v49 offset:6144\n" \
"v_mfma_f32_16x16x32_bf16 a[152:155], v[100:103], v[180:183], a[152:155]\n" \
"global_load_dword v253, v248, s[64:65]\n" \
"v_mfma_f32_16x16x32_bf16 a[156:159], v[96:99], v[184:187], a[156:159]\n" \
"s_lshr_b32 s68, s66, 2\n" \
"s_and_b32 s68, s68, 0xffff\n" \
"ds_read_b64_tr_b16 v[150:151], v50 offset:20992\n" \
"v_mfma_f32_16x16x32_bf16 a[156:159], v[100:103], v[188:191], a[156:159]\n" \
"s_add_u32 s68, s68, 65585152\n" \
"v_mfma_f32_16x16x32_bf16 a[176:179], v[104:107], v[160:163], a[176:179]\n" \
"ds_read_b128 v[96:99], v48 offset:8192\n" \
"v_mfma_f32_16x16x32_bf16 a[176:179], v[108:111], v[164:167], a[176:179]\n" \
"buffer_load_dword v253, v245, s[56:59], s68 offen sc1\n" \
"v_mfma_f32_16x16x32_bf16 a[180:183], v[104:107], v[168:171], a[180:183]\n" \
"ds_read_b64_tr_b16 v[152:153], v51 offset:512\n" \
"v_mfma_f32_16x16x32_bf16 a[180:183], v[108:111], v[172:175], a[180:183]\n" \
"v_mfma_f32_16x16x32_bf16 a[184:187], v[104:107], v[176:179], a[184:187]\n" \
"ds_read_b128 v[100:103], v49 offset:8192\n" \
"v_mfma_f32_16x16x32_bf16 a[184:187], v[108:111], v[180:183], a[184:187]\n" \
"v_mfma_f32_16x16x32_bf16 a[188:191], v[104:107], v[184:187], a[188:191]\n" \
"ds_read_b64_tr_b16 v[154:155], v51 offset:4608\n" \
"v_mfma_f32_16x16x32_bf16 a[188:191], v[108:111], v[188:191], a[188:191]\n" \
"v_mfma_f32_16x16x32_bf16 a[208:211], v[112:115], v[160:163], a[208:211]\n" \
"ds_read_b128 v[104:107], v48 offset:10240\n" \
"v_mfma_f32_16x16x32_bf16 a[208:211], v[116:119], v[164:167], a[208:211]\n" \
"v_mfma_f32_16x16x32_bf16 a[212:215], v[112:115], v[168:171], a[212:215]\n" \
"ds_read_b64_tr_b16 v[156:157], v51 offset:16896\n" \
"v_mfma_f32_16x16x32_bf16 a[212:215], v[116:119], v[172:175], a[212:215]\n" \
"v_mfma_f32_16x16x32_bf16 a[216:219], v[112:115], v[176:179], a[216:219]\n" \
"ds_read_b128 v[108:111], v49 offset:10240\n" \
"v_mfma_f32_16x16x32_bf16 a[216:219], v[116:119], v[180:183], a[216:219]\n" \
"v_mfma_f32_16x16x32_bf16 a[220:223], v[112:115], v[184:187], a[220:223]\n" \
"ds_read_b64_tr_b16 v[158:159], v51 offset:20992\n" \
"v_mfma_f32_16x16x32_bf16 a[220:223], v[116:119], v[188:191], a[220:223]\n" \
"v_mfma_f32_16x16x32_bf16 a[240:243], v[120:123], v[160:163], a[240:243]\n" \
"ds_read_b128 v[112:115], v48 offset:12288\n" \
"v_mfma_f32_16x16x32_bf16 a[240:243], v[124:127], v[164:167], a[240:243]\n" \
"v_mfma_f32_16x16x32_bf16 a[244:247], v[120:123], v[168:171], a[244:247]\n" \
"v_mfma_f32_16x16x32_bf16 a[244:247], v[124:127], v[172:175], a[244:247]\n" \
"v_mfma_f32_16x16x32_bf16 a[248:251], v[120:123], v[176:179], a[248:251]\n" \
"ds_read_b128 v[116:119], v49 offset:12288\n" \
"v_mfma_f32_16x16x32_bf16 a[248:251], v[124:127], v[180:183], a[248:251]\n" \
"v_mfma_f32_16x16x32_bf16 a[252:255], v[120:123], v[184:187], a[252:255]\n" \
"v_mfma_f32_16x16x32_bf16 a[252:255], v[124:127], v[188:191], a[252:255]\n" \
"s_waitcnt lgkmcnt(1)\n" \
"v_mfma_f32_16x16x32_bf16 a[0:3], v[128:131], v[64:67], a[0:3]\n" \
"ds_read_b128 v[120:123], v48 offset:14336\n" \
"v_mfma_f32_16x16x32_bf16 a[0:3], v[132:135], v[68:71], a[0:3]\n" \
"v_mfma_f32_16x16x32_bf16 a[4:7], v[136:139], v[64:67], a[4:7]\n" \
"ds_read_b128 v[124:127], v49 offset:14336\n" \
"s_add_u32 m0, s87, 69632\n" \
"v_mfma_f32_16x16x32_bf16 a[4:7], v[140:143], v[68:71], a[4:7]\n" \
"global_load_lds_dwordx4 v224, s[48:49] offset:-4096\n" \
"v_mfma_f32_16x16x32_bf16 a[8:11], v[144:147], v[64:67], a[8:11]\n" \
"ds_read_b64_tr_b16 v[160:161], v50 offset:1024\n" \
"v_mfma_f32_16x16x32_bf16 a[8:11], v[148:151], v[68:71], a[8:11]\n" \
"v_mfma_f32_16x16x32_bf16 a[12:15], v[152:155], v[64:67], a[12:15]\n" \
"ds_read_b64_tr_b16 v[162:163], v50 offset:5120\n" \
"v_mfma_f32_16x16x32_bf16 a[12:15], v[156:159], v[68:71], a[12:15]\n" \
"global_load_lds_dwordx4 v225, s[48:49] offset:-3072\n" \
"v_mfma_f32_16x16x32_bf16 a[32:35], v[128:131], v[72:75], a[32:35]\n" \
"ds_read_b64_tr_b16 v[164:165], v50 offset:17408\n" \
"v_mfma_f32_16x16x32_bf16 a[32:35], v[132:135], v[76:79], a[32:35]\n" \
"v_mfma_f32_16x16x32_bf16 a[36:39], v[136:139], v[72:75], a[36:39]\n" \
"ds_read_b64_tr_b16 v[166:167], v50 offset:21504\n" \
"v_mfma_f32_16x16x32_bf16 a[36:39], v[140:143], v[76:79], a[36:39]\n" \
"global_load_lds_dwordx4 v226, s[48:49] offset:-2048\n" \
"v_mfma_f32_16x16x32_bf16 a[40:43], v[144:147], v[72:75], a[40:43]\n" \
"ds_read_b64_tr_b16 v[168:169], v51 offset:1024\n" \
"v_mfma_f32_16x16x32_bf16 a[40:43], v[148:151], v[76:79], a[40:43]\n" \
"v_mfma_f32_16x16x32_bf16 a[44:47], v[152:155], v[72:75], a[44:47]\n" \
"ds_read_b64_tr_b16 v[170:171], v51 offset:5120\n" \
"v_mfma_f32_16x16x32_bf16 a[44:47], v[156:159], v[76:79], a[44:47]\n" \
"global_load_lds_dwordx4 v227, s[48:49] offset:-1024\n" \
"v_mfma_f32_16x16x32_bf16 a[64:67], v[128:131], v[80:83], a[64:67]\n" \
"s_add_u32 s48, s48, 0x400\n" \
"s_addc_u32 s49, s49, 0\n" \
"ds_read_b64_tr_b16 v[172:173], v51 offset:17408\n" \
"v_mfma_f32_16x16x32_bf16 a[64:67], v[132:135], v[84:87], a[64:67]\n" \
"v_mfma_f32_16x16x32_bf16 a[68:71], v[136:139], v[80:83], a[68:71]\n" \
"ds_read_b64_tr_b16 v[174:175], v51 offset:21504\n" \
"v_mfma_f32_16x16x32_bf16 a[68:71], v[140:143], v[84:87], a[68:71]\n" \
"global_load_lds_dwordx4 v224, s[46:47] offset:0\n" \
"v_mfma_f32_16x16x32_bf16 a[72:75], v[144:147], v[80:83], a[72:75]\n" \
"ds_read_b64_tr_b16 v[176:177], v50 offset:1536\n" \
"v_mfma_f32_16x16x32_bf16 a[72:75], v[148:151], v[84:87], a[72:75]\n" \
"v_mfma_f32_16x16x32_bf16 a[76:79], v[152:155], v[80:83], a[76:79]\n" \
"ds_read_b64_tr_b16 v[178:179], v50 offset:5632\n" \
"v_mfma_f32_16x16x32_bf16 a[76:79], v[156:159], v[84:87], a[76:79]\n" \
"global_load_lds_dwordx4 v225, s[46:47] offset:1024\n" \
"v_mfma_f32_16x16x32_bf16 a[96:99], v[128:131], v[88:91], a[96:99]\n" \
"ds_read_b64_tr_b16 v[180:181], v50 offset:17920\n" \
"v_mfma_f32_16x16x32_bf16 a[96:99], v[132:135], v[92:95], a[96:99]\n" \
"v_mfma_f32_16x16x32_bf16 a[100:103], v[136:139], v[88:91], a[100:103]\n" \
"ds_read_b64_tr_b16 v[182:183], v50 offset:22016\n" \
"v_mfma_f32_16x16x32_bf16 a[100:103], v[140:143], v[92:95], a[100:103]\n" \
"global_load_lds_dwordx4 v226, s[46:47] offset:2048\n" \
"v_mfma_f32_16x16x32_bf16 a[104:107], v[144:147], v[88:91], a[104:107]\n" \
"ds_read_b64_tr_b16 v[184:185], v51 offset:1536\n" \
"v_mfma_f32_16x16x32_bf16 a[104:107], v[148:151], v[92:95], a[104:107]\n" \
"v_mfma_f32_16x16x32_bf16 a[108:111], v[152:155], v[88:91], a[108:111]\n" \
"ds_read_b64_tr_b16 v[186:187], v51 offset:5632\n" \
"v_mfma_f32_16x16x32_bf16 a[108:111], v[156:159], v[92:95], a[108:111]\n" \
"global_load_lds_dwordx4 v227, s[46:47] offset:3072\n" \
"v_mfma_f32_16x16x32_bf16 a[128:131], v[128:131], v[96:99], a[128:131]\n" \
"s_add_u32 s46, s46, 0x400\n" \
"s_addc_u32 s47, s47, 0\n" \
"ds_read_b64_tr_b16 v[188:189], v51 offset:17920\n" \
"v_mfma_f32_16x16x32_bf16 a[128:131], v[132:135], v[100:103], a[128:131]\n" \
"v_mfma_f32_16x16x32_bf16 a[132:135], v[136:139], v[96:99], a[132:135]\n" \
"ds_read_b64_tr_b16 v[190:191], v51 offset:22016\n" \
"v_mfma_f32_16x16x32_bf16 a[132:135], v[140:143], v[100:103], a[132:135]\n" \
"v_mfma_f32_16x16x32_bf16 a[136:139], v[144:147], v[96:99], a[136:139]\n" \
"v_mfma_f32_16x16x32_bf16 a[136:139], v[148:151], v[100:103], a[136:139]\n" \
"v_mfma_f32_16x16x32_bf16 a[140:143], v[152:155], v[96:99], a[140:143]\n" \
"v_mfma_f32_16x16x32_bf16 a[140:143], v[156:159], v[100:103], a[140:143]\n" \
"v_mfma_f32_16x16x32_bf16 a[160:163], v[128:131], v[104:107], a[160:163]\n" \
"v_mfma_f32_16x16x32_bf16 a[160:163], v[132:135], v[108:111], a[160:163]\n" \
"v_mfma_f32_16x16x32_bf16 a[164:167], v[136:139], v[104:107], a[164:167]\n" \
"v_mfma_f32_16x16x32_bf16 a[164:167], v[140:143], v[108:111], a[164:167]\n" \
"v_mfma_f32_16x16x32_bf16 a[168:171], v[144:147], v[104:107], a[168:171]\n" \
"v_mfma_f32_16x16x32_bf16 a[168:171], v[148:151], v[108:111], a[168:171]\n" \
"v_mfma_f32_16x16x32_bf16 a[172:175], v[152:155], v[104:107], a[172:175]\n" \
"v_mfma_f32_16x16x32_bf16 a[172:175], v[156:159], v[108:111], a[172:175]\n" \
"s_waitcnt lgkmcnt(0)\n" \
"v_mfma_f32_16x16x32_bf16 a[192:195], v[128:131], v[112:115], a[192:195]\n" \
"v_mfma_f32_16x16x32_bf16 a[192:195], v[132:135], v[116:119], a[192:195]\n" \
"v_mfma_f32_16x16x32_bf16 a[196:199], v[136:139], v[112:115], a[196:199]\n" \
"v_mfma_f32_16x16x32_bf16 a[196:199], v[140:143], v[116:119], a[196:199]\n" \
"v_mfma_f32_16x16x32_bf16 a[200:203], v[144:147], v[112:115], a[200:203]\n" \
"v_mfma_f32_16x16x32_bf16 a[200:203], v[148:151], v[116:119], a[200:203]\n" \
"v_mfma_f32_16x16x32_bf16 a[204:207], v[152:155], v[112:115], a[204:207]\n" \
"v_mfma_f32_16x16x32_bf16 a[204:207], v[156:159], v[116:119], a[204:207]\n" \
"v_mfma_f32_16x16x32_bf16 a[224:227], v[128:131], v[120:123], a[224:227]\n" \
"v_mfma_f32_16x16x32_bf16 a[224:227], v[132:135], v[124:127], a[224:227]\n" \
"v_mfma_f32_16x16x32_bf16 a[228:231], v[136:139], v[120:123], a[228:231]\n" \
"v_mfma_f32_16x16x32_bf16 a[228:231], v[140:143], v[124:127], a[228:231]\n" \
"v_mfma_f32_16x16x32_bf16 a[232:235], v[144:147], v[120:123], a[232:235]\n" \
"v_mfma_f32_16x16x32_bf16 a[232:235], v[148:151], v[124:127], a[232:235]\n" \
"v_mfma_f32_16x16x32_bf16 a[236:239], v[152:155], v[120:123], a[236:239]\n" \
"s_waitcnt vmcnt(11) lgkmcnt(0)\n" \
"v_mfma_f32_16x16x32_bf16 a[236:239], v[156:159], v[124:127], a[236:239]\n" \
"s_barrier\n" \
"v_mfma_f32_16x16x32_bf16 a[16:19], v[64:67], v[160:163], a[16:19]\n" \
"s_add_u32 m0, s87, 102400\n" \
"v_mfma_f32_16x16x32_bf16 a[16:19], v[68:71], v[164:167], a[16:19]\n" \
"global_load_lds_dwordx4 v224, s[48:49] offset:-4096\n" \
"v_mfma_f32_16x16x32_bf16 a[20:23], v[64:67], v[168:171], a[20:23]\n" \
"ds_read_b64_tr_b16 v[0:1], v232 offset:0\n" \
"v_mfma_f32_16x16x32_bf16 a[20:23], v[68:71], v[172:175], a[20:23]\n" \
"v_accvgpr_read_b32 v234, a192\n" \
"v_accvgpr_read_b32 v235, a193\n" \
"v_mfma_f32_16x16x32_bf16 a[24:27], v[64:67], v[176:179], a[24:27]\n" \
"ds_read_b64_tr_b16 v[2:3], v232 offset:2048\n" \
"v_accvgpr_read_b32 v236, a194\n" \
"v_mfma_f32_16x16x32_bf16 a[24:27], v[68:71], v[180:183], a[24:27]\n" \
"global_load_lds_dwordx4 v225, s[48:49] offset:-3072\n" \
"v_accvgpr_read_b32 v237, a195\n" \
"v_mfma_f32_16x16x32_bf16 a[28:31], v[64:67], v[184:187], a[28:31]\n" \
"ds_read_b64_tr_b16 v[4:5], v232 offset:8192\n" \
"v_mul_f32 v234, s92, v234\n" \
"v_mfma_f32_16x16x32_bf16 a[28:31], v[68:71], v[188:191], a[28:31]\n" \
"v_mul_f32 v235, s92, v235\n" \
"v_mul_f32 v236, s92, v236\n" \
"v_mfma_f32_16x16x32_bf16 a[48:51], v[72:75], v[160:163], a[48:51]\n" \
"ds_read_b64_tr_b16 v[6:7], v232 offset:10240\n" \
"v_mul_f32 v237, s92, v237\n" \
"v_mfma_f32_16x16x32_bf16 a[48:51], v[76:79], v[164:167], a[48:51]\n" \
"global_load_lds_dwordx4 v226, s[48:49] offset:-2048\n" \
"v_cvt_pk_bf16_f32 v234, v234, v235\n" \
"v_mfma_f32_16x16x32_bf16 a[52:55], v[72:75], v[168:171], a[52:55]\n" \
"ds_read_b64_tr_b16 v[8:9], v233 offset:0\n" \
"v_cvt_pk_bf16_f32 v236, v236, v237\n" \
"v_mfma_f32_16x16x32_bf16 a[52:55], v[76:79], v[172:175], a[52:55]\n" \
"v_accvgpr_write_b32 a192, v234\n" \
"v_accvgpr_write_b32 a193, v236\n" \
"v_mfma_f32_16x16x32_bf16 a[56:59], v[72:75], v[176:179], a[56:59]\n" \
"ds_read_b64_tr_b16 v[10:11], v233 offset:2048\n" \
"v_accvgpr_read_b32 v234, a196\n" \
"v_mfma_f32_16x16x32_bf16 a[56:59], v[76:79], v[180:183], a[56:59]\n" \
"global_load_lds_dwordx4 v227, s[48:49] offset:-1024\n" \
"v_accvgpr_read_b32 v235, a197\n" \
"v_mfma_f32_16x16x32_bf16 a[60:63], v[72:75], v[184:187], a[60:63]\n" \
"s_add_u32 s48, s48, 0xfffffb00\n" \
"s_addc_u32 s49, s49, -1\n" \
"ds_read_b64_tr_b16 v[12:13], v233 offset:8192\n" \
"v_mfma_f32_16x16x32_bf16 a[60:63], v[76:79], v[188:191], a[60:63]\n" \
"v_accvgpr_read_b32 v236, a198\n" \
"v_accvgpr_read_b32 v237, a199\n" \
"v_mfma_f32_16x16x32_bf16 a[80:83], v[80:83], v[160:163], a[80:83]\n" \
"ds_read_b64_tr_b16 v[14:15], v233 offset:10240\n" \
"v_mul_f32 v234, s92, v234\n" \
"v_mfma_f32_16x16x32_bf16 a[80:83], v[84:87], v[164:167], a[80:83]\n" \
"global_load_lds_dwordx4 v224, s[46:47] offset:0\n" \
"v_mul_f32 v235, s92, v235\n" \
"v_mfma_f32_16x16x32_bf16 a[84:87], v[80:83], v[168:171], a[84:87]\n" \
"ds_read_b64_tr_b16 v[16:17], v232 offset:512\n" \
"v_mul_f32 v236, s92, v236\n" \
"v_mfma_f32_16x16x32_bf16 a[84:87], v[84:87], v[172:175], a[84:87]\n" \
"v_mul_f32 v237, s92, v237\n" \
"v_cvt_pk_bf16_f32 v234, v234, v235\n" \
"v_mfma_f32_16x16x32_bf16 a[88:91], v[80:83], v[176:179], a[88:91]\n" \
"ds_read_b64_tr_b16 v[18:19], v232 offset:2560\n" \
"v_cvt_pk_bf16_f32 v236, v236, v237\n" \
"v_mfma_f32_16x16x32_bf16 a[88:91], v[84:87], v[180:183], a[88:91]\n" \
"global_load_lds_dwordx4 v225, s[46:47] offset:1024\n" \
"v_accvgpr_write_b32 a194, v234\n" \
"v_mfma_f32_16x16x32_bf16 a[92:95], v[80:83], v[184:187], a[92:95]\n" \
"ds_read_b64_tr_b16 v[20:21], v232 offset:8704\n" \
"v_accvgpr_write_b32 a195, v236\n" \
"v_mfma_f32_16x16x32_bf16 a[92:95], v[84:87], v[188:191], a[92:95]\n" \
"v_accvgpr_read_b32 v234, a224\n" \
"v_accvgpr_read_b32 v235, a225\n" \
"v_mfma_f32_16x16x32_bf16 a[112:115], v[88:91], v[160:163], a[112:115]\n" \
"ds_read_b64_tr_b16 v[22:23], v232 offset:10752\n" \
"v_accvgpr_read_b32 v236, a226\n" \
"v_mfma_f32_16x16x32_bf16 a[112:115], v[92:95], v[164:167], a[112:115]\n" \
"global_load_lds_dwordx4 v226, s[46:47] offset:2048\n" \
"v_accvgpr_read_b32 v237, a227\n" \
"v_mfma_f32_16x16x32_bf16 a[116:119], v[88:91], v[168:171], a[116:119]\n" \
"ds_read_b64_tr_b16 v[24:25], v233 offset:512\n" \
"v_mul_f32 v234, s92, v234\n" \
"v_mfma_f32_16x16x32_bf16 a[116:119], v[92:95], v[172:175], a[116:119]\n" \
"v_mul_f32 v235, s92, v235\n" \
"v_mul_f32 v236, s92, v236\n" \
"v_mfma_f32_16x16x32_bf16 a[120:123], v[88:91], v[176:179], a[120:123]\n" \
"ds_read_b64_tr_b16 v[26:27], v233 offset:2560\n" \
"v_mul_f32 v237, s92, v237\n" \
"v_mfma_f32_16x16x32_bf16 a[120:123], v[92:95], v[180:183], a[120:123]\n" \
"global_load_lds_dwordx4 v227, s[46:47] offset:3072\n" \
"v_cvt_pk_bf16_f32 v234, v234, v235\n" \
"v_mfma_f32_16x16x32_bf16 a[124:127], v[88:91], v[184:187], a[124:127]\n" \
"s_add_u32 s46, s46, 0xfffffb00\n" \
"s_addc_u32 s47, s47, -1\n" \
"ds_read_b64_tr_b16 v[28:29], v233 offset:8704\n" \
"v_mfma_f32_16x16x32_bf16 a[124:127], v[92:95], v[188:191], a[124:127]\n" \
"v_cvt_pk_bf16_f32 v236, v236, v237\n" \
"v_accvgpr_write_b32 a224, v234\n" \
"v_mfma_f32_16x16x32_bf16 a[144:147], v[96:99], v[160:163], a[144:147]\n" \
"s_add_u32 m0, s95, 36864\n" \
"ds_read_b64_tr_b16 v[30:31], v233 offset:10752\n" \
"v_accvgpr_write_b32 a225, v236\n" \
"v_mfma_f32_16x16x32_bf16 a[144:147], v[100:103], v[164:167], a[144:147]\n" \
"global_load_lds_dwordx4 v228, s[44:45] offset:-4096\n" \
"v_accvgpr_read_b32 v234, a228\n" \
"v_mfma_f32_16x16x32_bf16 a[148:151], v[96:99], v[168:171], a[148:151]\n" \
"ds_read_b128 v[192:195], v230 offset:0\n" \
"v_accvgpr_read_b32 v235, a229\n" \
"v_mfma_f32_16x16x32_bf16 a[148:151], v[100:103], v[172:175], a[148:151]\n" \
"v_accvgpr_read_b32 v236, a230\n" \
"v_accvgpr_read_b32 v237, a231\n" \
"v_mfma_f32_16x16x32_bf16 a[152:155], v[96:99], v[176:179], a[152:155]\n" \
"ds_read_b128 v[196:199], v231 offset:0\n" \
"v_mul_f32 v234, s92, v234\n" \
"v_mfma_f32_16x16x32_bf16 a[152:155], v[100:103], v[180:183], a[152:155]\n" \
"global_load_lds_dwordx4 v229, s[44:45] offset:-3072\n" \
"v_mul_f32 v235, s92, v235\n" \
"v_mfma_f32_16x16x32_bf16 a[156:159], v[96:99], v[184:187], a[156:159]\n" \
"s_add_u32 s44, s44, 0xffb00000\n" \
"s_addc_u32 s45, s45, -1\n" \
"ds_read_b128 v[200:203], v230 offset:2048\n" \
"v_mfma_f32_16x16x32_bf16 a[156:159], v[100:103], v[188:191], a[156:159]\n" \
"v_mul_f32 v236, s92, v236\n" \
"v_mul_f32 v237, s92, v237\n" \
"v_mfma_f32_16x16x32_bf16 a[176:179], v[104:107], v[160:163], a[176:179]\n" \
"ds_read_b128 v[204:207], v231 offset:2048\n" \
"v_cvt_pk_bf16_f32 v234, v234, v235\n" \
"v_mfma_f32_16x16x32_bf16 a[176:179], v[108:111], v[164:167], a[176:179]\n" \
"global_load_lds_dwordx4 v228, s[42:43] offset:-2048\n" \
"v_cvt_pk_bf16_f32 v236, v236, v237\n" \
"v_mfma_f32_16x16x32_bf16 a[180:183], v[104:107], v[168:171], a[180:183]\n" \
"ds_read_b128 v[208:211], v230 offset:4096\n" \
"v_accvgpr_write_b32 a226, v234\n" \
"v_mfma_f32_16x16x32_bf16 a[180:183], v[108:111], v[172:175], a[180:183]\n" \
"v_accvgpr_write_b32 a227, v236\n" \
"v_accvgpr_read_b32 v234, a200\n" \
"v_mfma_f32_16x16x32_bf16 a[184:187], v[104:107], v[176:179], a[184:187]\n" \
"ds_read_b128 v[212:215], v231 offset:4096\n" \
"v_accvgpr_read_b32 v235, a201\n" \
"v_mfma_f32_16x16x32_bf16 a[184:187], v[108:111], v[180:183], a[184:187]\n" \
"global_load_lds_dwordx4 v229, s[42:43] offset:-1024\n" \
"v_accvgpr_read_b32 v236, a202\n" \
"v_mfma_f32_16x16x32_bf16 a[188:191], v[104:107], v[184:187], a[188:191]\n" \
"s_add_u32 s42, s42, 0xffb00000\n" \
"s_addc_u32 s43, s43, -1\n" \
"v_accvgpr_read_b32 v237, a203\n" \
"v_mfma_f32_16x16x32_bf16 a[188:191], v[108:111], v[188:191], a[188:191]\n" \
"v_mul_f32 v234, s92, v234\n" \
"v_mul_f32 v235, s92, v235\n" \
"v_mfma_f32_16x16x32_bf16 a[208:211], v[112:115], v[160:163], a[208:211]\n" \
"s_add_u32 m0, s95, 53248\n" \
"v_mul_f32 v236, s92, v236\n" \
"v_mul_f32 v237, s92, v237\n" \
"v_mfma_f32_16x16x32_bf16 a[208:211], v[116:119], v[164:167], a[208:211]\n" \
"global_load_lds_dwordx4 v228, s[44:45] offset:-4096\n" \
"v_cvt_pk_bf16_f32 v234, v234, v235\n" \
"v_mfma_f32_16x16x32_bf16 a[212:215], v[112:115], v[168:171], a[212:215]\n" \
"v_cvt_pk_bf16_f32 v236, v236, v237\n" \
"v_accvgpr_write_b32 a200, v234\n" \
"v_mfma_f32_16x16x32_bf16 a[212:215], v[116:119], v[172:175], a[212:215]\n" \
"v_accvgpr_write_b32 a201, v236\n" \
"v_accvgpr_read_b32 v234, a204\n" \
"v_mfma_f32_16x16x32_bf16 a[216:219], v[112:115], v[176:179], a[216:219]\n" \
"v_accvgpr_read_b32 v235, a205\n" \
"v_accvgpr_read_b32 v236, a206\n" \
"v_mfma_f32_16x16x32_bf16 a[216:219], v[116:119], v[180:183], a[216:219]\n" \
"global_load_lds_dwordx4 v229, s[44:45] offset:-3072\n" \
"v_accvgpr_read_b32 v237, a207\n" \
"v_mfma_f32_16x16x32_bf16 a[220:223], v[112:115], v[184:187], a[220:223]\n" \
"s_add_u32 s44, s44, 0x80000\n" \
"s_addc_u32 s45, s45, 0\n" \
"v_mul_f32 v234, s92, v234\n" \
"v_mfma_f32_16x16x32_bf16 a[220:223], v[116:119], v[188:191], a[220:223]\n" \
"v_mul_f32 v235, s92, v235\n" \
"v_mul_f32 v236, s92, v236\n" \
"v_mfma_f32_16x16x32_bf16 a[240:243], v[120:123], v[160:163], a[240:243]\n" \
"v_mul_f32 v237, s92, v237\n" \
"v_cvt_pk_bf16_f32 v234, v234, v235\n" \
"v_mfma_f32_16x16x32_bf16 a[240:243], v[124:127], v[164:167], a[240:243]\n" \
"global_load_lds_dwordx4 v228, s[42:43] offset:-2048\n" \
"v_cvt_pk_bf16_f32 v236, v236, v237\n" \
"v_mfma_f32_16x16x32_bf16 a[244:247], v[120:123], v[168:171], a[244:247]\n" \
"v_accvgpr_write_b32 a202, v234\n" \
"v_accvgpr_write_b32 a203, v236\n" \
"v_mfma_f32_16x16x32_bf16 a[244:247], v[124:127], v[172:175], a[244:247]\n" \
"v_accvgpr_read_b32 v234, a232\n" \
"v_accvgpr_read_b32 v235, a233\n" \
"v_mfma_f32_16x16x32_bf16 a[248:251], v[120:123], v[176:179], a[248:251]\n" \
"v_accvgpr_read_b32 v236, a234\n" \
"v_accvgpr_read_b32 v237, a235\n" \
"v_mfma_f32_16x16x32_bf16 a[248:251], v[124:127], v[180:183], a[248:251]\n" \
"global_load_lds_dwordx4 v229, s[42:43] offset:-1024\n" \
"v_mul_f32 v234, s92, v234\n" \
"v_mfma_f32_16x16x32_bf16 a[252:255], v[120:123], v[184:187], a[252:255]\n" \
"s_add_u32 s42, s42, 0x80000\n" \
"s_addc_u32 s43, s43, 0\n" \
"v_mul_f32 v235, s92, v235\n" \
"v_mfma_f32_16x16x32_bf16 a[252:255], v[124:127], v[188:191], a[252:255]\n" \
"s_waitcnt lgkmcnt(0)\n" \
"v_mul_f32 v236, s92, v236\n" \
"v_mul_f32 v237, s92, v237\n" \
"v_mfma_f32_16x16x32_bf16 v[64:67], v[0:3], v[192:195], 0\n" \
"ds_read_b128 v[216:219], v230 offset:6144\n" \
"v_cvt_pk_bf16_f32 v234, v234, v235\n" \
"v_mfma_f32_16x16x32_bf16 v[64:67], v[4:7], v[196:199], v[64:67]\n" \
"ds_read_b128 v[220:223], v231 offset:6144\n" \
"v_cvt_pk_bf16_f32 v236, v236, v237\n" \
"v_mfma_f32_16x16x32_bf16 v[68:71], v[8:11], v[192:195], 0\n" \
"s_add_u32 s90, s88, 0\n" \
"ds_read_b64_tr_b16 v[32:33], v232 offset:1024\n" \
"v_accvgpr_write_b32 a232, v234\n" \
"v_mfma_f32_16x16x32_bf16 v[68:71], v[12:15], v[196:199], v[68:71]\n" \
"buffer_store_dwordx4 a[192:195], v249, s[56:59], s90 offen offset:0 sc1\n" \
"v_accvgpr_write_b32 a233, v236\n" \
"v_accvgpr_read_b32 v234, a236\n" \
"v_accvgpr_read_b32 v235, a237\n" \
"v_accvgpr_read_b32 v236, a238\n" \
"v_mfma_f32_16x16x32_bf16 v[72:75], v[16:19], v[192:195], 0\n" \
"ds_read_b64_tr_b16 v[34:35], v232 offset:3072\n" \
"v_accvgpr_read_b32 v237, a239\n" \
"v_mfma_f32_16x16x32_bf16 v[72:75], v[20:23], v[196:199], v[72:75]\n" \
"ds_read_b64_tr_b16 v[36:37], v232 offset:9216\n" \
"v_mul_f32 v234, s92, v234\n" \
"v_mfma_f32_16x16x32_bf16 v[76:79], v[24:27], v[192:195], 0\n" \
"ds_read_b64_tr_b16 v[38:39], v232 offset:11264\n" \
"v_mul_f32 v235, s92, v235\n" \
"v_mfma_f32_16x16x32_bf16 v[76:79], v[28:31], v[196:199], v[76:79]\n" \
"buffer_store_dwordx4 a[224:227], v249, s[56:59], s90 offen offset:256 sc1\n" \
"v_mul_f32 v236, s92, v236\n" \
"v_mul_f32 v237, s92, v237\n" \
"v_cvt_pk_bf16_f32 v234, v234, v235\n" \
"v_cvt_pk_bf16_f32 v236, v236, v237\n" \
"v_mfma_f32_16x16x32_bf16 v[96:99], v[0:3], v[200:203], 0\n" \
"ds_read_b64_tr_b16 v[40:41], v233 offset:1024\n" \
"v_accvgpr_write_b32 a234, v234\n" \
"v_mfma_f32_16x16x32_bf16 v[96:99], v[4:7], v[204:207], v[96:99]\n" \
"ds_read_b64_tr_b16 v[42:43], v233 offset:3072\n" \
"v_accvgpr_write_b32 a235, v236\n" \
"v_mfma_f32_16x16x32_bf16 v[100:103], v[8:11], v[200:203], 0\n" \
"ds_read_b64_tr_b16 v[44:45], v233 offset:9216\n" \
"v_accvgpr_read_b32 v234, a208\n" \
"v_mfma_f32_16x16x32_bf16 v[100:103], v[12:15], v[204:207], v[100:103]\n" \
"v_accvgpr_read_b32 v235, a209\n" \
"v_accvgpr_read_b32 v236, a210\n" \
"v_mfma_f32_16x16x32_bf16 v[104:107], v[16:19], v[200:203], 0\n" \
"ds_read_b64_tr_b16 v[46:47], v233 offset:11264\n" \
"v_accvgpr_read_b32 v237, a211\n" \
"v_mfma_f32_16x16x32_bf16 v[104:107], v[20:23], v[204:207], v[104:107]\n" \
"ds_read_b64_tr_b16 v[48:49], v232 offset:1536\n" \
"v_cvt_pk_bf16_f32 v234, v234, v235\n" \
"v_mfma_f32_16x16x32_bf16 v[108:111], v[24:27], v[200:203], 0\n" \
"ds_read_b64_tr_b16 v[50:51], v232 offset:3584\n" \
"v_cvt_pk_bf16_f32 v236, v236, v237\n" \
"v_mfma_f32_16x16x32_bf16 v[108:111], v[28:31], v[204:207], v[108:111]\n" \
"v_accvgpr_write_b32 a208, v234\n" \
"v_accvgpr_write_b32 a209, v236\n" \
"v_mfma_f32_16x16x32_bf16 v[128:131], v[0:3], v[208:211], 0\n" \
"ds_read_b64_tr_b16 v[52:53], v232 offset:9728\n" \
"v_accvgpr_read_b32 v234, a240\n" \
"v_mfma_f32_16x16x32_bf16 v[128:131], v[4:7], v[212:215], v[128:131]\n" \
"ds_read_b64_tr_b16 v[54:55], v232 offset:11776\n" \
"v_accvgpr_read_b32 v235, a241\n" \
"v_mfma_f32_16x16x32_bf16 v[132:135], v[8:11], v[208:211], 0\n" \
"ds_read_b64_tr_b16 v[56:57], v233 offset:1536\n" \
"v_accvgpr_read_b32 v236, a242\n" \
"Lpg2229_%=:\n" \
"Lst0x3_%=:\n" \
"s_cbranch_execz Lpg4400_%=\n" \
"v_mfma_f32_16x16x32_bf16 v[132:135], v[12:15], v[212:215], v[132:135]\n" \
"v_accvgpr_read_b32 v237, a243\n" \
"v_cvt_pk_bf16_f32 v234, v234, v235\n" \
"v_mfma_f32_16x16x32_bf16 v[136:139], v[16:19], v[208:211], 0\n" \
"ds_read_b64_tr_b16 v[58:59], v233 offset:3584\n" \
"v_cvt_pk_bf16_f32 v236, v236, v237\n" \
"v_mfma_f32_16x16x32_bf16 v[136:139], v[20:23], v[212:215], v[136:139]\n" \
"ds_read_b64_tr_b16 v[60:61], v233 offset:9728\n" \
"v_accvgpr_write_b32 a210, v234\n" \
"v_mfma_f32_16x16x32_bf16 v[140:143], v[24:27], v[208:211], 0\n" \
"ds_read_b64_tr_b16 v[62:63], v233 offset:11776\n" \
"v_accvgpr_write_b32 a211, v236\n" \
"v_mfma_f32_16x16x32_bf16 v[140:143], v[28:31], v[212:215], v[140:143]\n" \
"s_waitcnt lgkmcnt(3)\n" \
"v_accvgpr_read_b32 v234, a212\n" \
"v_accvgpr_read_b32 v235, a213\n" \
"v_mfma_f32_16x16x32_bf16 v[160:163], v[0:3], v[216:219], 0\n" \
"v_add_u32 v230, 0xffff0000, v230\n" \
"v_add_u32 v231, 0xffff0000, v231\n" \
"v_mfma_f32_16x16x32_bf16 v[160:163], v[4:7], v[220:223], v[160:163]\n" \
"v_add_u32 v232, 0x4000, v232\n" \
"v_add_u32 v233, 0x4000, v233\n" \
"v_mfma_f32_16x16x32_bf16 v[164:167], v[8:11], v[216:219], 0\n" \
"v_accvgpr_read_b32 v236, a214\n" \
"v_accvgpr_read_b32 v237, a215\n" \
"v_mfma_f32_16x16x32_bf16 v[164:167], v[12:15], v[220:223], v[164:167]\n" \
"v_cvt_pk_bf16_f32 v234, v234, v235\n" \
"v_cvt_pk_bf16_f32 v236, v236, v237\n" \
"v_mfma_f32_16x16x32_bf16 v[168:171], v[16:19], v[216:219], 0\n" \
"v_accvgpr_write_b32 a212, v234\n" \
"v_accvgpr_write_b32 a213, v236\n" \
"v_mfma_f32_16x16x32_bf16 v[168:171], v[20:23], v[220:223], v[168:171]\n" \
"v_accvgpr_read_b32 v234, a244\n" \
"v_accvgpr_read_b32 v235, a245\n" \
"v_mfma_f32_16x16x32_bf16 v[172:175], v[24:27], v[216:219], 0\n" \
"s_waitcnt vmcnt(18) lgkmcnt(0)\n" \
"v_accvgpr_read_b32 v236, a246\n" \
"v_accvgpr_read_b32 v237, a247\n" \
"v_mfma_f32_16x16x32_bf16 v[172:175], v[28:31], v[220:223], v[172:175]\n" \
"s_barrier\n" \
"v_mfma_f32_16x16x32_bf16 v[80:83], v[32:35], v[192:195], 0\n" \
"ds_read_b64_tr_b16 v[0:1], v232 offset:0\n" \
"v_cvt_pk_bf16_f32 v234, v234, v235\n" \
"v_mfma_f32_16x16x32_bf16 v[80:83], v[36:39], v[196:199], v[80:83]\n" \
"ds_read_b64_tr_b16 v[2:3], v232 offset:2048\n" \
"v_cvt_pk_bf16_f32 v236, v236, v237\n" \
"v_mfma_f32_16x16x32_bf16 v[84:87], v[40:43], v[192:195], 0\n" \
"s_add_u32 m0, s87, 135168\n" \
"ds_read_b64_tr_b16 v[4:5], v232 offset:8192\n" \
"v_accvgpr_write_b32 a214, v234\n" \
"v_mfma_f32_16x16x32_bf16 v[84:87], v[44:47], v[196:199], v[84:87]\n" \
"global_load_lds_dwordx4 v224, s[48:49] offset:-4096\n" \
"v_accvgpr_write_b32 a215, v236\n" \
"v_mfma_f32_16x16x32_bf16 v[88:91], v[48:51], v[192:195], 0\n" \
"ds_read_b64_tr_b16 v[6:7], v232 offset:10240\n" \
"v_accvgpr_read_b32 v234, a216\n" \
"v_mfma_f32_16x16x32_bf16 v[88:91], v[52:55], v[196:199], v[88:91]\n" \
"ds_read_b64_tr_b16 v[8:9], v233 offset:0\n" \
"v_accvgpr_read_b32 v235, a217\n" \
"v_mfma_f32_16x16x32_bf16 v[92:95], v[56:59], v[192:195], 0\n" \
"ds_read_b64_tr_b16 v[10:11], v233 offset:2048\n" \
"v_accvgpr_read_b32 v236, a218\n" \
"v_mfma_f32_16x16x32_bf16 v[92:95], v[60:63], v[196:199], v[92:95]\n" \
"global_load_lds_dwordx4 v225, s[48:49] offset:-3072\n" \
"v_accvgpr_read_b32 v237, a219\n" \
"v_mfma_f32_16x16x32_bf16 v[112:115], v[32:35], v[200:203], 0\n" \
"ds_read_b128 v[192:195], v230 offset:0\n" \
"v_cvt_pk_bf16_f32 v234, v234, v235\n" \
"v_mfma_f32_16x16x32_bf16 v[112:115], v[36:39], v[204:207], v[112:115]\n" \
"ds_read_b128 v[196:199], v231 offset:0\n" \
"v_cvt_pk_bf16_f32 v236, v236, v237\n" \
"v_mfma_f32_16x16x32_bf16 v[116:119], v[40:43], v[200:203], 0\n" \
"ds_read_b64_tr_b16 v[12:13], v233 offset:8192\n" \
"v_accvgpr_write_b32 a216, v234\n" \
"v_mfma_f32_16x16x32_bf16 v[116:119], v[44:47], v[204:207], v[116:119]\n" \
"global_load_lds_dwordx4 v226, s[48:49] offset:-2048\n" \
"v_accvgpr_write_b32 a217, v236\n" \
"v_mfma_f32_16x16x32_bf16 v[120:123], v[48:51], v[200:203], 0\n" \
"ds_read_b64_tr_b16 v[14:15], v233 offset:10240\n" \
"v_accvgpr_read_b32 v234, a248\n" \
"v_mfma_f32_16x16x32_bf16 v[120:123], v[52:55], v[204:207], v[120:123]\n" \
"ds_read_b64_tr_b16 v[16:17], v232 offset:512\n" \
"v_accvgpr_read_b32 v235, a249\n" \
"v_mfma_f32_16x16x32_bf16 v[124:127], v[56:59], v[200:203], 0\n" \
"ds_read_b64_tr_b16 v[18:19], v232 offset:2560\n" \
"v_accvgpr_read_b32 v236, a250\n" \
"v_mfma_f32_16x16x32_bf16 v[124:127], v[60:63], v[204:207], v[124:127]\n" \
"global_load_lds_dwordx4 v227, s[48:49] offset:-1024\n" \
"v_accvgpr_read_b32 v237, a251\n" \
"v_mfma_f32_16x16x32_bf16 v[144:147], v[32:35], v[208:211], 0\n" \
"s_add_u32 s48, s48, 0x80\n" \
"s_addc_u32 s49, s49, 0\n" \
"ds_read_b128 v[200:203], v230 offset:2048\n" \
"v_mfma_f32_16x16x32_bf16 v[144:147], v[36:39], v[212:215], v[144:147]\n" \
"ds_read_b128 v[204:207], v231 offset:2048\n" \
"v_cvt_pk_bf16_f32 v234, v234, v235\n" \
"v_mfma_f32_16x16x32_bf16 v[148:151], v[40:43], v[208:211], 0\n" \
"ds_read_b64_tr_b16 v[20:21], v232 offset:8704\n" \
"v_cvt_pk_bf16_f32 v236, v236, v237\n" \
"v_mfma_f32_16x16x32_bf16 v[148:151], v[44:47], v[212:215], v[148:151]\n" \
"global_load_lds_dwordx4 v224, s[46:47] offset:0\n" \
"v_accvgpr_write_b32 a218, v234\n" \
"v_mfma_f32_16x16x32_bf16 v[152:155], v[48:51], v[208:211], 0\n" \
"ds_read_b64_tr_b16 v[22:23], v232 offset:10752\n" \
"v_accvgpr_write_b32 a219, v236\n" \
"v_mfma_f32_16x16x32_bf16 v[152:155], v[52:55], v[212:215], v[152:155]\n" \
"ds_read_b64_tr_b16 v[24:25], v233 offset:512\n" \
"v_accvgpr_read_b32 v234, a220\n" \
"v_mfma_f32_16x16x32_bf16 v[156:159], v[56:59], v[208:211], 0\n" \
"ds_read_b64_tr_b16 v[26:27], v233 offset:2560\n" \
"v_accvgpr_read_b32 v235, a221\n" \
"v_mfma_f32_16x16x32_bf16 v[156:159], v[60:63], v[212:215], v[156:159]\n" \
"global_load_lds_dwordx4 v225, s[46:47] offset:1024\n" \
"v_accvgpr_read_b32 v236, a222\n" \
"v_mfma_f32_16x16x32_bf16 v[176:179], v[32:35], v[216:219], 0\n" \
"ds_read_b128 v[208:211], v230 offset:4096\n" \
"v_accvgpr_read_b32 v237, a223\n" \
"v_mfma_f32_16x16x32_bf16 v[176:179], v[36:39], v[220:223], v[176:179]\n" \
"ds_read_b128 v[212:215], v231 offset:4096\n" \
"v_cvt_pk_bf16_f32 v234, v234, v235\n" \
"v_mfma_f32_16x16x32_bf16 v[180:183], v[40:43], v[216:219], 0\n" \
"ds_read_b64_tr_b16 v[28:29], v233 offset:8704\n" \
"v_cvt_pk_bf16_f32 v236, v236, v237\n" \
"v_mfma_f32_16x16x32_bf16 v[180:183], v[44:47], v[220:223], v[180:183]\n" \
"global_load_lds_dwordx4 v226, s[46:47] offset:2048\n" \
"v_accvgpr_write_b32 a220, v234\n" \
"v_mfma_f32_16x16x32_bf16 v[184:187], v[48:51], v[216:219], 0\n" \
"ds_read_b64_tr_b16 v[30:31], v233 offset:10752\n" \
"v_accvgpr_write_b32 a221, v236\n" \
"v_mfma_f32_16x16x32_bf16 v[184:187], v[52:55], v[220:223], v[184:187]\n" \
"v_accvgpr_read_b32 v234, a252\n" \
"v_accvgpr_read_b32 v235, a253\n" \
"v_mfma_f32_16x16x32_bf16 v[188:191], v[56:59], v[216:219], 0\n" \
"v_accvgpr_read_b32 v236, a254\n" \
"v_accvgpr_read_b32 v237, a255\n" \
"v_mfma_f32_16x16x32_bf16 v[188:191], v[60:63], v[220:223], v[188:191]\n" \
"s_waitcnt lgkmcnt(1)\n" \
"global_load_lds_dwordx4 v227, s[46:47] offset:3072\n" \
"v_cvt_pk_bf16_f32 v234, v234, v235\n" \
"v_mfma_f32_16x16x32_bf16 v[64:67], v[0:3], v[192:195], v[64:67]\n" \
"s_add_u32 s46, s46, 0x80\n" \
"s_addc_u32 s47, s47, 0\n" \
"ds_read_b128 v[216:219], v230 offset:6144\n" \
"v_mfma_f32_16x16x32_bf16 v[64:67], v[4:7], v[196:199], v[64:67]\n" \
"ds_read_b128 v[220:223], v231 offset:6144\n" \
"v_cvt_pk_bf16_f32 v236, v236, v237\n" \
"v_mfma_f32_16x16x32_bf16 v[68:71], v[8:11], v[192:195], v[68:71]\n" \
"ds_read_b64_tr_b16 v[32:33], v232 offset:1024\n" \
"v_accvgpr_write_b32 a222, v234\n" \
"v_mfma_f32_16x16x32_bf16 v[68:71], v[12:15], v[196:199], v[68:71]\n" \
"buffer_store_dwordx4 a[200:203], v249, s[56:59], s90 offen offset:2048 sc1\n" \
"v_accvgpr_write_b32 a223, v236\n" \
"v_accvgpr_read_b32 v234, a128\n" \
"v_accvgpr_read_b32 v235, a129\n" \
"v_accvgpr_read_b32 v236, a130\n" \
"v_mfma_f32_16x16x32_bf16 v[72:75], v[16:19], v[192:195], v[72:75]\n" \
"ds_read_b64_tr_b16 v[34:35], v232 offset:3072\n" \
"v_accvgpr_read_b32 v237, a131\n" \
"v_mfma_f32_16x16x32_bf16 v[72:75], v[20:23], v[196:199], v[72:75]\n" \
"s_waitcnt lgkmcnt(3)\n" \
"ds_read_b64_tr_b16 v[36:37], v232 offset:9216\n" \
"v_mul_f32 v234, s92, v234\n" \
"v_mfma_f32_16x16x32_bf16 v[76:79], v[24:27], v[192:195], v[76:79]\n" \
"ds_read_b64_tr_b16 v[38:39], v232 offset:11264\n" \
"v_mul_f32 v235, s92, v235\n" \
"v_mfma_f32_16x16x32_bf16 v[76:79], v[28:31], v[196:199], v[76:79]\n" \
"v_mul_f32 v236, s92, v236\n" \
"v_mul_f32 v237, s92, v237\n" \
"v_mfma_f32_16x16x32_bf16 v[96:99], v[0:3], v[200:203], v[96:99]\n" \
"ds_read_b64_tr_b16 v[40:41], v233 offset:1024\n" \
"v_cvt_pk_bf16_f32 v234, v234, v235\n" \
"v_mfma_f32_16x16x32_bf16 v[96:99], v[4:7], v[204:207], v[96:99]\n" \
"ds_read_b64_tr_b16 v[42:43], v233 offset:3072\n" \
"v_cvt_pk_bf16_f32 v236, v236, v237\n" \
"v_mfma_f32_16x16x32_bf16 v[100:103], v[8:11], v[200:203], v[100:103]\n" \
"ds_read_b64_tr_b16 v[44:45], v233 offset:9216\n" \
"v_accvgpr_write_b32 a128, v234\n" \
"v_mfma_f32_16x16x32_bf16 v[100:103], v[12:15], v[204:207], v[100:103]\n" \
"v_accvgpr_write_b32 a129, v236\n" \
"v_accvgpr_read_b32 v234, a132\n" \
"v_mfma_f32_16x16x32_bf16 v[104:107], v[16:19], v[200:203], v[104:107]\n" \
"ds_read_b64_tr_b16 v[46:47], v233 offset:11264\n" \
"v_accvgpr_read_b32 v235, a133\n" \
"v_mfma_f32_16x16x32_bf16 v[104:107], v[20:23], v[204:207], v[104:107]\n" \
"ds_read_b64_tr_b16 v[48:49], v232 offset:1536\n" \
"v_accvgpr_read_b32 v236, a134\n" \
"v_mfma_f32_16x16x32_bf16 v[108:111], v[24:27], v[200:203], v[108:111]\n" \
"s_add_u32 m0, s95, 4096\n" \
"ds_read_b64_tr_b16 v[50:51], v232 offset:3584\n" \
"v_accvgpr_read_b32 v237, a135\n" \
"v_mfma_f32_16x16x32_bf16 v[108:111], v[28:31], v[204:207], v[108:111]\n" \
"global_load_lds_dwordx4 v228, s[44:45] offset:-4096\n" \
"v_mul_f32 v234, s92, v234\n" \
"v_mfma_f32_16x16x32_bf16 v[128:131], v[0:3], v[208:211], v[128:131]\n" \
"ds_read_b64_tr_b16 v[52:53], v232 offset:9728\n" \
"v_mul_f32 v235, s92, v235\n" \
"v_mfma_f32_16x16x32_bf16 v[128:131], v[4:7], v[212:215], v[128:131]\n" \
"ds_read_b64_tr_b16 v[54:55], v232 offset:11776\n" \
"v_mul_f32 v236, s92, v236\n" \
"v_mfma_f32_16x16x32_bf16 v[132:135], v[8:11], v[208:211], v[132:135]\n" \
"ds_read_b64_tr_b16 v[56:57], v233 offset:1536\n" \
"v_mul_f32 v237, s92, v237\n" \
"v_mfma_f32_16x16x32_bf16 v[132:135], v[12:15], v[212:215], v[132:135]\n" \
"global_load_lds_dwordx4 v229, s[44:45] offset:-3072\n" \
"v_cvt_pk_bf16_f32 v234, v234, v235\n" \
"v_mfma_f32_16x16x32_bf16 v[136:139], v[16:19], v[208:211], v[136:139]\n" \
"s_add_u32 s44, s44, 0x100000\n" \
"s_addc_u32 s45, s45, 0\n" \
"ds_read_b64_tr_b16 v[58:59], v233 offset:3584\n" \
"v_mfma_f32_16x16x32_bf16 v[136:139], v[20:23], v[212:215], v[136:139]\n" \
"ds_read_b64_tr_b16 v[60:61], v233 offset:9728\n" \
"v_cvt_pk_bf16_f32 v236, v236, v237\n" \
"v_mfma_f32_16x16x32_bf16 v[140:143], v[24:27], v[208:211], v[140:143]\n" \
"ds_read_b64_tr_b16 v[62:63], v233 offset:11776\n" \
"v_accvgpr_write_b32 a130, v234\n" \
"v_mfma_f32_16x16x32_bf16 v[140:143], v[28:31], v[212:215], v[140:143]\n" \
"s_waitcnt lgkmcnt(3)\n" \
"global_load_lds_dwordx4 v228, s[42:43] offset:-2048\n" \
"v_accvgpr_write_b32 a131, v236\n" \
"v_mfma_f32_16x16x32_bf16 v[160:163], v[0:3], v[216:219], v[160:163]\n" \
"v_add_u32 v230, 0x8000, v230\n" \
"v_add_u32 v231, 0x8000, v231\n" \
"v_mfma_f32_16x16x32_bf16 v[160:163], v[4:7], v[220:223], v[160:163]\n" \
"v_add_u32 v232, 0x4000, v232\n" \
"v_add_u32 v233, 0x4000, v233\n" \
"v_mfma_f32_16x16x32_bf16 v[164:167], v[8:11], v[216:219], v[164:167]\n" \
"v_accvgpr_read_b32 v234, a160\n" \
"v_accvgpr_read_b32 v235, a161\n" \
"v_mfma_f32_16x16x32_bf16 v[164:167], v[12:15], v[220:223], v[164:167]\n" \
"global_load_lds_dwordx4 v229, s[42:43] offset:-1024\n" \
"v_accvgpr_read_b32 v236, a162\n" \
"v_mfma_f32_16x16x32_bf16 v[168:171], v[16:19], v[216:219], v[168:171]\n" \
"s_add_u32 s42, s42, 0x100000\n" \
"s_addc_u32 s43, s43, 0\n" \
"v_accvgpr_read_b32 v237, a163\n" \
"v_mfma_f32_16x16x32_bf16 v[168:171], v[20:23], v[220:223], v[168:171]\n" \
"v_mul_f32 v234, s92, v234\n" \
"v_mul_f32 v235, s92, v235\n" \
"v_mfma_f32_16x16x32_bf16 v[172:175], v[24:27], v[216:219], v[172:175]\n" \
"s_waitcnt vmcnt(19) lgkmcnt(0)\n" \
"v_mul_f32 v236, s92, v236\n" \
"v_mul_f32 v237, s92, v237\n" \
"v_mfma_f32_16x16x32_bf16 v[172:175], v[28:31], v[220:223], v[172:175]\n" \
"s_barrier\n" \
"v_mfma_f32_16x16x32_bf16 v[80:83], v[32:35], v[192:195], v[80:83]\n" \
"ds_read_b64_tr_b16 v[0:1], v232 offset:0\n" \
"v_cvt_pk_bf16_f32 v234, v234, v235\n" \
"v_mfma_f32_16x16x32_bf16 v[80:83], v[36:39], v[196:199], v[80:83]\n" \
"ds_read_b64_tr_b16 v[2:3], v232 offset:2048\n" \
"v_cvt_pk_bf16_f32 v236, v236, v237\n" \
"v_mfma_f32_16x16x32_bf16 v[84:87], v[40:43], v[192:195], v[84:87]\n" \
"s_add_u32 m0, s87, 69632\n" \
"ds_read_b64_tr_b16 v[4:5], v232 offset:8192\n" \
"v_accvgpr_write_b32 a160, v234\n" \
"v_mfma_f32_16x16x32_bf16 v[84:87], v[44:47], v[196:199], v[84:87]\n" \
"global_load_lds_dwordx4 v224, s[48:49] offset:-4096\n" \
"v_accvgpr_write_b32 a161, v236\n" \
"v_mfma_f32_16x16x32_bf16 v[88:91], v[48:51], v[192:195], v[88:91]\n" \
"ds_read_b64_tr_b16 v[6:7], v232 offset:10240\n" \
"v_accvgpr_read_b32 v234, a164\n" \
"v_mfma_f32_16x16x32_bf16 v[88:91], v[52:55], v[196:199], v[88:91]\n" \
"ds_read_b64_tr_b16 v[8:9], v233 offset:0\n" \
"v_accvgpr_read_b32 v235, a165\n" \
"v_mfma_f32_16x16x32_bf16 v[92:95], v[56:59], v[192:195], v[92:95]\n" \
"ds_read_b64_tr_b16 v[10:11], v233 offset:2048\n" \
"v_accvgpr_read_b32 v236, a166\n" \
"v_mfma_f32_16x16x32_bf16 v[92:95], v[60:63], v[196:199], v[92:95]\n" \
"global_load_lds_dwordx4 v225, s[48:49] offset:-3072\n" \
"v_accvgpr_read_b32 v237, a167\n" \
"v_mfma_f32_16x16x32_bf16 v[112:115], v[32:35], v[200:203], v[112:115]\n" \
"ds_read_b128 v[192:195], v230 offset:0\n" \
"v_mul_f32 v234, s92, v234\n" \
"v_mfma_f32_16x16x32_bf16 v[112:115], v[36:39], v[204:207], v[112:115]\n" \
"ds_read_b128 v[196:199], v231 offset:0\n" \
"v_mul_f32 v235, s92, v235\n" \
"v_mfma_f32_16x16x32_bf16 v[116:119], v[40:43], v[200:203], v[116:119]\n" \
"ds_read_b64_tr_b16 v[12:13], v233 offset:8192\n" \
"v_mul_f32 v236, s92, v236\n" \
"v_mfma_f32_16x16x32_bf16 v[116:119], v[44:47], v[204:207], v[116:119]\n" \
"global_load_lds_dwordx4 v226, s[48:49] offset:-2048\n" \
"v_mul_f32 v237, s92, v237\n" \
"v_mfma_f32_16x16x32_bf16 v[120:123], v[48:51], v[200:203], v[120:123]\n" \
"ds_read_b64_tr_b16 v[14:15], v233 offset:10240\n" \
"v_cvt_pk_bf16_f32 v234, v234, v235\n" \
"v_mfma_f32_16x16x32_bf16 v[120:123], v[52:55], v[204:207], v[120:123]\n" \
"ds_read_b64_tr_b16 v[16:17], v232 offset:512\n" \
"v_cvt_pk_bf16_f32 v236, v236, v237\n" \
"v_mfma_f32_16x16x32_bf16 v[124:127], v[56:59], v[200:203], v[124:127]\n" \
"ds_read_b64_tr_b16 v[18:19], v232 offset:2560\n" \
"v_accvgpr_write_b32 a162, v234\n" \
"v_mfma_f32_16x16x32_bf16 v[124:127], v[60:63], v[204:207], v[124:127]\n" \
"global_load_lds_dwordx4 v227, s[48:49] offset:-1024\n" \
"v_accvgpr_write_b32 a163, v236\n" \
"v_mfma_f32_16x16x32_bf16 v[144:147], v[32:35], v[208:211], v[144:147]\n" \
"s_add_u32 s48, s48, 0x100\n" \
"s_addc_u32 s49, s49, 0\n" \
"ds_read_b128 v[200:203], v230 offset:2048\n" \
"v_mfma_f32_16x16x32_bf16 v[144:147], v[36:39], v[212:215], v[144:147]\n" \
"ds_read_b128 v[204:207], v231 offset:2048\n" \
"v_accvgpr_read_b32 v234, a136\n" \
"v_mfma_f32_16x16x32_bf16 v[148:151], v[40:43], v[208:211], v[148:151]\n" \
"ds_read_b64_tr_b16 v[20:21], v232 offset:8704\n" \
"v_accvgpr_read_b32 v235, a137\n" \
"v_mfma_f32_16x16x32_bf16 v[148:151], v[44:47], v[212:215], v[148:151]\n" \
"global_load_lds_dwordx4 v224, s[46:47] offset:0\n" \
"v_accvgpr_read_b32 v236, a138\n" \
"v_mfma_f32_16x16x32_bf16 v[152:155], v[48:51], v[208:211], v[152:155]\n" \
"ds_read_b64_tr_b16 v[22:23], v232 offset:10752\n" \
"v_accvgpr_read_b32 v237, a139\n" \
"v_mfma_f32_16x16x32_bf16 v[152:155], v[52:55], v[212:215], v[152:155]\n" \
"ds_read_b64_tr_b16 v[24:25], v233 offset:512\n" \
"v_mul_f32 v234, s92, v234\n" \
"v_mfma_f32_16x16x32_bf16 v[156:159], v[56:59], v[208:211], v[156:159]\n" \
"ds_read_b64_tr_b16 v[26:27], v233 offset:2560\n" \
"v_mul_f32 v235, s92, v235\n" \
"v_mfma_f32_16x16x32_bf16 v[156:159], v[60:63], v[212:215], v[156:159]\n" \
"global_load_lds_dwordx4 v225, s[46:47] offset:1024\n" \
"v_mul_f32 v236, s92, v236\n" \
"v_mfma_f32_16x16x32_bf16 v[176:179], v[32:35], v[216:219], v[176:179]\n" \
"ds_read_b128 v[208:211], v230 offset:4096\n" \
"v_mul_f32 v237, s92, v237\n" \
"v_mfma_f32_16x16x32_bf16 v[176:179], v[36:39], v[220:223], v[176:179]\n" \
"ds_read_b128 v[212:215], v231 offset:4096\n" \
"v_cvt_pk_bf16_f32 v234, v234, v235\n" \
"v_mfma_f32_16x16x32_bf16 v[180:183], v[40:43], v[216:219], v[180:183]\n" \
"ds_read_b64_tr_b16 v[28:29], v233 offset:8704\n" \
"v_cvt_pk_bf16_f32 v236, v236, v237\n" \
"v_mfma_f32_16x16x32_bf16 v[180:183], v[44:47], v[220:223], v[180:183]\n" \
"global_load_lds_dwordx4 v226, s[46:47] offset:2048\n" \
"v_accvgpr_write_b32 a136, v234\n" \
"v_mfma_f32_16x16x32_bf16 v[184:187], v[48:51], v[216:219], v[184:187]\n" \
"ds_read_b64_tr_b16 v[30:31], v233 offset:10752\n" \
"v_accvgpr_write_b32 a137, v236\n" \
"v_mfma_f32_16x16x32_bf16 v[184:187], v[52:55], v[220:223], v[184:187]\n" \
"v_accvgpr_read_b32 v234, a140\n" \
"v_accvgpr_read_b32 v235, a141\n" \
"v_mfma_f32_16x16x32_bf16 v[188:191], v[56:59], v[216:219], v[188:191]\n" \
"v_accvgpr_read_b32 v236, a142\n" \
"v_accvgpr_read_b32 v237, a143\n" \
"v_mfma_f32_16x16x32_bf16 v[188:191], v[60:63], v[220:223], v[188:191]\n" \
"s_waitcnt lgkmcnt(1)\n" \
"global_load_lds_dwordx4 v227, s[46:47] offset:3072\n" \
"v_mul_f32 v234, s92, v234\n" \
"v_mfma_f32_16x16x32_bf16 v[64:67], v[0:3], v[192:195], v[64:67]\n" \
"s_add_u32 s46, s46, 0x100\n" \
"s_addc_u32 s47, s47, 0\n" \
"ds_read_b128 v[216:219], v230 offset:6144\n" \
"v_mfma_f32_16x16x32_bf16 v[64:67], v[4:7], v[196:199], v[64:67]\n" \
"ds_read_b128 v[220:223], v231 offset:6144\n" \
"v_mul_f32 v235, s92, v235\n" \
"v_mfma_f32_16x16x32_bf16 v[68:71], v[8:11], v[192:195], v[68:71]\n" \
"ds_read_b64_tr_b16 v[32:33], v232 offset:1024\n" \
"v_mul_f32 v236, s92, v236\n" \
"v_mfma_f32_16x16x32_bf16 v[68:71], v[12:15], v[196:199], v[68:71]\n" \
"buffer_store_dwordx4 a[232:235], v249, s[56:59], s90 offen offset:2304 sc1\n" \
"v_mul_f32 v237, s92, v237\n" \
"v_cvt_pk_bf16_f32 v234, v234, v235\n" \
"v_cvt_pk_bf16_f32 v236, v236, v237\n" \
"v_accvgpr_write_b32 a138, v234\n" \
"v_mfma_f32_16x16x32_bf16 v[72:75], v[16:19], v[192:195], v[72:75]\n" \
"s_add_u32 s90, s89, 0\n" \
"ds_read_b64_tr_b16 v[34:35], v232 offset:3072\n" \
"v_accvgpr_write_b32 a139, v236\n" \
"v_mfma_f32_16x16x32_bf16 v[72:75], v[20:23], v[196:199], v[72:75]\n" \
"s_waitcnt lgkmcnt(3)\n" \
"ds_read_b64_tr_b16 v[36:37], v232 offset:9216\n" \
"v_accvgpr_read_b32 v234, a168\n" \
"v_mfma_f32_16x16x32_bf16 v[76:79], v[24:27], v[192:195], v[76:79]\n" \
"ds_read_b64_tr_b16 v[38:39], v232 offset:11264\n" \
"v_accvgpr_read_b32 v235, a169\n" \
"v_mfma_f32_16x16x32_bf16 v[76:79], v[28:31], v[196:199], v[76:79]\n" \
"buffer_store_dwordx4 a[208:211], v248, s[56:59], s90 offen offset:0 sc1\n" \
"v_accvgpr_read_b32 v236, a170\n" \
"v_accvgpr_read_b32 v237, a171\n" \
"v_mul_f32 v234, s92, v234\n" \
"v_mul_f32 v235, s92, v235\n" \
"v_mfma_f32_16x16x32_bf16 v[96:99], v[0:3], v[200:203], v[96:99]\n" \
"ds_read_b64_tr_b16 v[40:41], v233 offset:1024\n" \
"v_mul_f32 v236, s92, v236\n" \
"v_mfma_f32_16x16x32_bf16 v[96:99], v[4:7], v[204:207], v[96:99]\n" \
"ds_read_b64_tr_b16 v[42:43], v233 offset:3072\n" \
"v_mul_f32 v237, s92, v237\n" \
"v_mfma_f32_16x16x32_bf16 v[100:103], v[8:11], v[200:203], v[100:103]\n" \
"ds_read_b64_tr_b16 v[44:45], v233 offset:9216\n" \
"v_cvt_pk_bf16_f32 v234, v234, v235\n" \
"v_mfma_f32_16x16x32_bf16 v[100:103], v[12:15], v[204:207], v[100:103]\n" \
"v_cvt_pk_bf16_f32 v236, v236, v237\n" \
"v_accvgpr_write_b32 a168, v234\n" \
"v_mfma_f32_16x16x32_bf16 v[104:107], v[16:19], v[200:203], v[104:107]\n" \
"ds_read_b64_tr_b16 v[46:47], v233 offset:11264\n" \
"v_accvgpr_write_b32 a169, v236\n" \
"v_mfma_f32_16x16x32_bf16 v[104:107], v[20:23], v[204:207], v[104:107]\n" \
"ds_read_b64_tr_b16 v[48:49], v232 offset:1536\n" \
"v_accvgpr_read_b32 v234, a172\n" \
"v_mfma_f32_16x16x32_bf16 v[108:111], v[24:27], v[200:203], v[108:111]\n" \
"s_add_u32 m0, s95, 20480\n" \
"ds_read_b64_tr_b16 v[50:51], v232 offset:3584\n" \
"v_accvgpr_read_b32 v235, a173\n" \
"v_mfma_f32_16x16x32_bf16 v[108:111], v[28:31], v[204:207], v[108:111]\n" \
"global_load_lds_dwordx4 v228, s[44:45] offset:-4096\n" \
"v_accvgpr_read_b32 v236, a174\n" \
"v_mfma_f32_16x16x32_bf16 v[128:131], v[0:3], v[208:211], v[128:131]\n" \
"ds_read_b64_tr_b16 v[52:53], v232 offset:9728\n" \
"v_accvgpr_read_b32 v237, a175\n" \
"v_mfma_f32_16x16x32_bf16 v[128:131], v[4:7], v[212:215], v[128:131]\n" \
"ds_read_b64_tr_b16 v[54:55], v232 offset:11776\n" \
"v_mul_f32 v234, s92, v234\n" \
"v_mfma_f32_16x16x32_bf16 v[132:135], v[8:11], v[208:211], v[132:135]\n" \
"ds_read_b64_tr_b16 v[56:57], v233 offset:1536\n" \
"v_mul_f32 v235, s92, v235\n" \
"v_mfma_f32_16x16x32_bf16 v[132:135], v[12:15], v[212:215], v[132:135]\n" \
"global_load_lds_dwordx4 v229, s[44:45] offset:-3072\n" \
"v_mul_f32 v236, s92, v236\n" \
"v_mfma_f32_16x16x32_bf16 v[136:139], v[16:19], v[208:211], v[136:139]\n" \
"s_add_u32 s44, s44, 0x80000\n" \
"s_addc_u32 s45, s45, 0\n" \
"ds_read_b64_tr_b16 v[58:59], v233 offset:3584\n" \
"v_mfma_f32_16x16x32_bf16 v[136:139], v[20:23], v[212:215], v[136:139]\n" \
"ds_read_b64_tr_b16 v[60:61], v233 offset:9728\n" \
"v_mul_f32 v237, s92, v237\n" \
"v_mfma_f32_16x16x32_bf16 v[140:143], v[24:27], v[208:211], v[140:143]\n" \
"ds_read_b64_tr_b16 v[62:63], v233 offset:11776\n" \
"v_cvt_pk_bf16_f32 v234, v234, v235\n" \
"v_mfma_f32_16x16x32_bf16 v[140:143], v[28:31], v[212:215], v[140:143]\n" \
"s_waitcnt lgkmcnt(3)\n" \
"global_load_lds_dwordx4 v228, s[42:43] offset:-2048\n" \
"v_cvt_pk_bf16_f32 v236, v236, v237\n" \
"v_mfma_f32_16x16x32_bf16 v[160:163], v[0:3], v[216:219], v[160:163]\n" \
"v_add_u32 v230, 0x8000, v230\n" \
"v_add_u32 v231, 0x8000, v231\n" \
"v_mfma_f32_16x16x32_bf16 v[160:163], v[4:7], v[220:223], v[160:163]\n" \
"v_add_u32 v232, 0x4000, v232\n" \
"v_add_u32 v233, 0x4000, v233\n" \
"v_mfma_f32_16x16x32_bf16 v[164:167], v[8:11], v[216:219], v[164:167]\n" \
"v_accvgpr_write_b32 a170, v234\n" \
"v_accvgpr_write_b32 a171, v236\n" \
"v_mfma_f32_16x16x32_bf16 v[164:167], v[12:15], v[220:223], v[164:167]\n" \
"global_load_lds_dwordx4 v229, s[42:43] offset:-1024\n" \
"v_accvgpr_read_b32 v234, a144\n" \
"v_mfma_f32_16x16x32_bf16 v[168:171], v[16:19], v[216:219], v[168:171]\n" \
"s_add_u32 s42, s42, 0x80000\n" \
"s_addc_u32 s43, s43, 0\n" \
"v_accvgpr_read_b32 v235, a145\n" \
"v_mfma_f32_16x16x32_bf16 v[168:171], v[20:23], v[220:223], v[168:171]\n" \
"v_accvgpr_read_b32 v236, a146\n" \
"v_accvgpr_read_b32 v237, a147\n" \
"v_mfma_f32_16x16x32_bf16 v[172:175], v[24:27], v[216:219], v[172:175]\n" \
"s_waitcnt vmcnt(19) lgkmcnt(0)\n" \
"v_cvt_pk_bf16_f32 v234, v234, v235\n" \
"v_cvt_pk_bf16_f32 v236, v236, v237\n" \
"v_mfma_f32_16x16x32_bf16 v[172:175], v[28:31], v[220:223], v[172:175]\n" \
"s_barrier\n" \
"v_mfma_f32_16x16x32_bf16 v[80:83], v[32:35], v[192:195], v[80:83]\n" \
"ds_read_b64_tr_b16 v[0:1], v232 offset:0\n" \
"v_accvgpr_write_b32 a144, v234\n" \
"v_mfma_f32_16x16x32_bf16 v[80:83], v[36:39], v[196:199], v[80:83]\n" \
"ds_read_b64_tr_b16 v[2:3], v232 offset:2048\n" \
"v_accvgpr_write_b32 a145, v236\n" \
"v_mfma_f32_16x16x32_bf16 v[84:87], v[40:43], v[192:195], v[84:87]\n" \
"s_add_u32 m0, s87, 102400\n" \
"ds_read_b64_tr_b16 v[4:5], v232 offset:8192\n" \
"v_accvgpr_read_b32 v234, a176\n" \
"v_mfma_f32_16x16x32_bf16 v[84:87], v[44:47], v[196:199], v[84:87]\n" \
"global_load_lds_dwordx4 v224, s[48:49] offset:-4096\n" \
"v_accvgpr_read_b32 v235, a177\n" \
"v_mfma_f32_16x16x32_bf16 v[88:91], v[48:51], v[192:195], v[88:91]\n" \
"ds_read_b64_tr_b16 v[6:7], v232 offset:10240\n" \
"v_accvgpr_read_b32 v236, a178\n" \
"v_mfma_f32_16x16x32_bf16 v[88:91], v[52:55], v[196:199], v[88:91]\n" \
"ds_read_b64_tr_b16 v[8:9], v233 offset:0\n" \
"v_accvgpr_read_b32 v237, a179\n" \
"v_mfma_f32_16x16x32_bf16 v[92:95], v[56:59], v[192:195], v[92:95]\n" \
"ds_read_b64_tr_b16 v[10:11], v233 offset:2048\n" \
"v_cvt_pk_bf16_f32 v234, v234, v235\n" \
"v_mfma_f32_16x16x32_bf16 v[92:95], v[60:63], v[196:199], v[92:95]\n" \
"global_load_lds_dwordx4 v225, s[48:49] offset:-3072\n" \
"v_cvt_pk_bf16_f32 v236, v236, v237\n" \
"v_mfma_f32_16x16x32_bf16 v[112:115], v[32:35], v[200:203], v[112:115]\n" \
"ds_read_b128 v[192:195], v230 offset:0\n" \
"v_accvgpr_write_b32 a146, v234\n" \
"v_mfma_f32_16x16x32_bf16 v[112:115], v[36:39], v[204:207], v[112:115]\n" \
"ds_read_b128 v[196:199], v231 offset:0\n" \
"v_accvgpr_write_b32 a147, v236\n" \
"v_mfma_f32_16x16x32_bf16 v[116:119], v[40:43], v[200:203], v[116:119]\n" \
"ds_read_b64_tr_b16 v[12:13], v233 offset:8192\n" \
"v_accvgpr_read_b32 v234, a148\n" \
"v_mfma_f32_16x16x32_bf16 v[116:119], v[44:47], v[204:207], v[116:119]\n" \
"global_load_lds_dwordx4 v226, s[48:49] offset:-2048\n" \
"v_accvgpr_read_b32 v235, a149\n" \
"v_mfma_f32_16x16x32_bf16 v[120:123], v[48:51], v[200:203], v[120:123]\n" \
"ds_read_b64_tr_b16 v[14:15], v233 offset:10240\n" \
"v_accvgpr_read_b32 v236, a150\n" \
"v_mfma_f32_16x16x32_bf16 v[120:123], v[52:55], v[204:207], v[120:123]\n" \
"ds_read_b64_tr_b16 v[16:17], v232 offset:512\n" \
"v_accvgpr_read_b32 v237, a151\n" \
"v_mfma_f32_16x16x32_bf16 v[124:127], v[56:59], v[200:203], v[124:127]\n" \
"ds_read_b64_tr_b16 v[18:19], v232 offset:2560\n" \
"v_cvt_pk_bf16_f32 v234, v234, v235\n" \
"v_mfma_f32_16x16x32_bf16 v[124:127], v[60:63], v[204:207], v[124:127]\n" \
"global_load_lds_dwordx4 v227, s[48:49] offset:-1024\n" \
"v_cvt_pk_bf16_f32 v236, v236, v237\n" \
"v_mfma_f32_16x16x32_bf16 v[144:147], v[32:35], v[208:211], v[144:147]\n" \
"s_add_u32 s48, s48, 0x80\n" \
"s_addc_u32 s49, s49, 0\n" \
"ds_read_b128 v[200:203], v230 offset:2048\n" \
"v_mfma_f32_16x16x32_bf16 v[144:147], v[36:39], v[212:215], v[144:147]\n" \
"ds_read_b128 v[204:207], v231 offset:2048\n" \
"v_accvgpr_write_b32 a148, v234\n" \
"v_mfma_f32_16x16x32_bf16 v[148:151], v[40:43], v[208:211], v[148:151]\n" \
"ds_read_b64_tr_b16 v[20:21], v232 offset:8704\n" \
"v_accvgpr_write_b32 a149, v236\n" \
"v_mfma_f32_16x16x32_bf16 v[148:151], v[44:47], v[212:215], v[148:151]\n" \
"global_load_lds_dwordx4 v224, s[46:47] offset:0\n" \
"v_accvgpr_read_b32 v234, a180\n" \
"v_mfma_f32_16x16x32_bf16 v[152:155], v[48:51], v[208:211], v[152:155]\n" \
"ds_read_b64_tr_b16 v[22:23], v232 offset:10752\n" \
"v_accvgpr_read_b32 v235, a181\n" \
"v_mfma_f32_16x16x32_bf16 v[152:155], v[52:55], v[212:215], v[152:155]\n" \
"ds_read_b64_tr_b16 v[24:25], v233 offset:512\n" \
"v_accvgpr_read_b32 v236, a182\n" \
"v_mfma_f32_16x16x32_bf16 v[156:159], v[56:59], v[208:211], v[156:159]\n" \
"ds_read_b64_tr_b16 v[26:27], v233 offset:2560\n" \
"v_accvgpr_read_b32 v237, a183\n" \
"v_mfma_f32_16x16x32_bf16 v[156:159], v[60:63], v[212:215], v[156:159]\n" \
"global_load_lds_dwordx4 v225, s[46:47] offset:1024\n" \
"v_cvt_pk_bf16_f32 v234, v234, v235\n" \
"v_mfma_f32_16x16x32_bf16 v[176:179], v[32:35], v[216:219], v[176:179]\n" \
"ds_read_b128 v[208:211], v230 offset:4096\n" \
"v_cvt_pk_bf16_f32 v236, v236, v237\n" \
"v_mfma_f32_16x16x32_bf16 v[176:179], v[36:39], v[220:223], v[176:179]\n" \
"ds_read_b128 v[212:215], v231 offset:4096\n" \
"v_accvgpr_write_b32 a150, v234\n" \
"v_mfma_f32_16x16x32_bf16 v[180:183], v[40:43], v[216:219], v[180:183]\n" \
"ds_read_b64_tr_b16 v[28:29], v233 offset:8704\n" \
"v_accvgpr_write_b32 a151, v236\n" \
"v_mfma_f32_16x16x32_bf16 v[180:183], v[44:47], v[220:223], v[180:183]\n" \
"global_load_lds_dwordx4 v226, s[46:47] offset:2048\n" \
"v_accvgpr_read_b32 v234, a152\n" \
"v_mfma_f32_16x16x32_bf16 v[184:187], v[48:51], v[216:219], v[184:187]\n" \
"Lpg2769_%=:\n" \
"s_cbranch_execz Lpg4934_%=\n" \
"ds_read_b64_tr_b16 v[30:31], v233 offset:10752\n" \
"v_accvgpr_read_b32 v235, a153\n" \
"v_mfma_f32_16x16x32_bf16 v[184:187], v[52:55], v[220:223], v[184:187]\n" \
"v_accvgpr_read_b32 v236, a154\n" \
"v_accvgpr_read_b32 v237, a155\n" \
"v_mfma_f32_16x16x32_bf16 v[188:191], v[56:59], v[216:219], v[188:191]\n" \
"v_cvt_pk_bf16_f32 v234, v234, v235\n" \
"v_cvt_pk_bf16_f32 v236, v236, v237\n" \
"v_mfma_f32_16x16x32_bf16 v[188:191], v[60:63], v[220:223], v[188:191]\n" \
"s_waitcnt lgkmcnt(1)\n" \
"global_load_lds_dwordx4 v227, s[46:47] offset:3072\n" \
"v_accvgpr_write_b32 a152, v234\n" \
"v_mfma_f32_16x16x32_bf16 v[64:67], v[0:3], v[192:195], v[64:67]\n" \
"s_add_u32 s46, s46, 0x80\n" \
"s_addc_u32 s47, s47, 0\n" \
"ds_read_b128 v[216:219], v230 offset:6144\n" \
"v_mfma_f32_16x16x32_bf16 v[64:67], v[4:7], v[196:199], v[64:67]\n" \
"ds_read_b128 v[220:223], v231 offset:6144\n" \
"v_accvgpr_write_b32 a153, v236\n" \
"v_mfma_f32_16x16x32_bf16 v[68:71], v[8:11], v[192:195], v[68:71]\n" \
"ds_read_b64_tr_b16 v[32:33], v232 offset:1024\n" \
"v_accvgpr_read_b32 v234, a184\n" \
"v_mfma_f32_16x16x32_bf16 v[68:71], v[12:15], v[196:199], v[68:71]\n" \
"buffer_store_dwordx4 a[212:215], v248, s[56:59], s90 offen offset:1024 sc1\n" \
"v_accvgpr_read_b32 v235, a185\n" \
"v_accvgpr_read_b32 v236, a186\n" \
"v_accvgpr_read_b32 v237, a187\n" \
"v_cvt_pk_bf16_f32 v234, v234, v235\n" \
"v_mfma_f32_16x16x32_bf16 v[72:75], v[16:19], v[192:195], v[72:75]\n" \
"ds_read_b64_tr_b16 v[34:35], v232 offset:3072\n" \
"v_cvt_pk_bf16_f32 v236, v236, v237\n" \
"v_mfma_f32_16x16x32_bf16 v[72:75], v[20:23], v[196:199], v[72:75]\n" \
"s_waitcnt lgkmcnt(3)\n" \
"ds_read_b64_tr_b16 v[36:37], v232 offset:9216\n" \
"v_accvgpr_write_b32 a154, v234\n" \
"v_mfma_f32_16x16x32_bf16 v[76:79], v[24:27], v[192:195], v[76:79]\n" \
"ds_read_b64_tr_b16 v[38:39], v232 offset:11264\n" \
"v_accvgpr_write_b32 a155, v236\n" \
"v_mfma_f32_16x16x32_bf16 v[76:79], v[28:31], v[196:199], v[76:79]\n" \
"v_accvgpr_read_b32 v234, a156\n" \
"v_accvgpr_read_b32 v235, a157\n" \
"v_mfma_f32_16x16x32_bf16 v[96:99], v[0:3], v[200:203], v[96:99]\n" \
"ds_read_b64_tr_b16 v[40:41], v233 offset:1024\n" \
"v_accvgpr_read_b32 v236, a158\n" \
"v_mfma_f32_16x16x32_bf16 v[96:99], v[4:7], v[204:207], v[96:99]\n" \
"ds_read_b64_tr_b16 v[42:43], v233 offset:3072\n" \
"v_accvgpr_read_b32 v237, a159\n" \
"v_mfma_f32_16x16x32_bf16 v[100:103], v[8:11], v[200:203], v[100:103]\n" \
"ds_read_b64_tr_b16 v[44:45], v233 offset:9216\n" \
"v_cvt_pk_bf16_f32 v234, v234, v235\n" \
"v_mfma_f32_16x16x32_bf16 v[100:103], v[12:15], v[204:207], v[100:103]\n" \
"v_cvt_pk_bf16_f32 v236, v236, v237\n" \
"v_accvgpr_write_b32 a156, v234\n" \
"v_mfma_f32_16x16x32_bf16 v[104:107], v[16:19], v[200:203], v[104:107]\n" \
"ds_read_b64_tr_b16 v[46:47], v233 offset:11264\n" \
"v_accvgpr_write_b32 a157, v236\n" \
"v_mfma_f32_16x16x32_bf16 v[104:107], v[20:23], v[204:207], v[104:107]\n" \
"ds_read_b64_tr_b16 v[48:49], v232 offset:1536\n" \
"v_accvgpr_read_b32 v234, a188\n" \
"v_mfma_f32_16x16x32_bf16 v[108:111], v[24:27], v[200:203], v[108:111]\n" \
"s_add_u32 m0, s95, 36864\n" \
"ds_read_b64_tr_b16 v[50:51], v232 offset:3584\n" \
"v_accvgpr_read_b32 v235, a189\n" \
"v_mfma_f32_16x16x32_bf16 v[108:111], v[28:31], v[204:207], v[108:111]\n" \
"global_load_lds_dwordx4 v228, s[44:45] offset:-4096\n" \
"v_accvgpr_read_b32 v236, a190\n" \
"v_mfma_f32_16x16x32_bf16 v[128:131], v[0:3], v[208:211], v[128:131]\n" \
"ds_read_b64_tr_b16 v[52:53], v232 offset:9728\n" \
"v_accvgpr_read_b32 v237, a191\n" \
"v_mfma_f32_16x16x32_bf16 v[128:131], v[4:7], v[212:215], v[128:131]\n" \
"ds_read_b64_tr_b16 v[54:55], v232 offset:11776\n" \
"v_cvt_pk_bf16_f32 v234, v234, v235\n" \
"v_mfma_f32_16x16x32_bf16 v[132:135], v[8:11], v[208:211], v[132:135]\n" \
"ds_read_b64_tr_b16 v[56:57], v233 offset:1536\n" \
"v_cvt_pk_bf16_f32 v236, v236, v237\n" \
"v_mfma_f32_16x16x32_bf16 v[132:135], v[12:15], v[212:215], v[132:135]\n" \
"global_load_lds_dwordx4 v229, s[44:45] offset:-3072\n" \
"v_accvgpr_write_b32 a158, v234\n" \
"v_mfma_f32_16x16x32_bf16 v[136:139], v[16:19], v[208:211], v[136:139]\n" \
"s_add_u32 s44, s44, 0x80000\n" \
"s_addc_u32 s45, s45, 0\n" \
"ds_read_b64_tr_b16 v[58:59], v233 offset:3584\n" \
"v_mfma_f32_16x16x32_bf16 v[136:139], v[20:23], v[212:215], v[136:139]\n" \
"ds_read_b64_tr_b16 v[60:61], v233 offset:9728\n" \
"v_accvgpr_write_b32 a159, v236\n" \
"v_mfma_f32_16x16x32_bf16 v[140:143], v[24:27], v[208:211], v[140:143]\n" \
"ds_read_b64_tr_b16 v[62:63], v233 offset:11776\n" \
"v_accvgpr_read_b32 v234, a64\n" \
"v_mfma_f32_16x16x32_bf16 v[140:143], v[28:31], v[212:215], v[140:143]\n" \
"s_waitcnt lgkmcnt(3)\n" \
"global_load_lds_dwordx4 v228, s[42:43] offset:-2048\n" \
"v_accvgpr_read_b32 v235, a65\n" \
"v_mfma_f32_16x16x32_bf16 v[160:163], v[0:3], v[216:219], v[160:163]\n" \
"v_add_u32 v230, 0xffff0000, v230\n" \
"v_add_u32 v231, 0xffff0000, v231\n" \
"v_mfma_f32_16x16x32_bf16 v[160:163], v[4:7], v[220:223], v[160:163]\n" \
"v_add_u32 v232, 0xffff4000, v232\n" \
"v_add_u32 v233, 0xffff4000, v233\n" \
"v_mfma_f32_16x16x32_bf16 v[164:167], v[8:11], v[216:219], v[164:167]\n" \
"v_accvgpr_read_b32 v236, a66\n" \
"v_accvgpr_read_b32 v237, a67\n" \
"v_mfma_f32_16x16x32_bf16 v[164:167], v[12:15], v[220:223], v[164:167]\n" \
"global_load_lds_dwordx4 v229, s[42:43] offset:-1024\n" \
"v_mul_f32 v234, s92, v234\n" \
"v_mfma_f32_16x16x32_bf16 v[168:171], v[16:19], v[216:219], v[168:171]\n" \
"s_add_u32 s42, s42, 0x80000\n" \
"s_addc_u32 s43, s43, 0\n" \
"v_mul_f32 v235, s92, v235\n" \
"v_mfma_f32_16x16x32_bf16 v[168:171], v[20:23], v[220:223], v[168:171]\n" \
"v_mul_f32 v236, s92, v236\n" \
"v_mul_f32 v237, s92, v237\n" \
"v_mfma_f32_16x16x32_bf16 v[172:175], v[24:27], v[216:219], v[172:175]\n" \
"s_waitcnt vmcnt(19) lgkmcnt(0)\n" \
"v_cvt_pk_bf16_f32 v234, v234, v235\n" \
"v_cvt_pk_bf16_f32 v236, v236, v237\n" \
"v_mfma_f32_16x16x32_bf16 v[172:175], v[28:31], v[220:223], v[172:175]\n" \
"s_barrier\n" \
"v_mfma_f32_16x16x32_bf16 v[80:83], v[32:35], v[192:195], v[80:83]\n" \
"ds_read_b64_tr_b16 v[0:1], v232 offset:0\n" \
"v_accvgpr_write_b32 a64, v234\n" \
"v_mfma_f32_16x16x32_bf16 v[80:83], v[36:39], v[196:199], v[80:83]\n" \
"ds_read_b64_tr_b16 v[2:3], v232 offset:2048\n" \
"v_accvgpr_write_b32 a65, v236\n" \
"v_mfma_f32_16x16x32_bf16 v[84:87], v[40:43], v[192:195], v[84:87]\n" \
"s_add_u32 m0, s87, 135168\n" \
"ds_read_b64_tr_b16 v[4:5], v232 offset:8192\n" \
"v_accvgpr_read_b32 v234, a68\n" \
"v_mfma_f32_16x16x32_bf16 v[84:87], v[44:47], v[196:199], v[84:87]\n" \
"global_load_lds_dwordx4 v224, s[48:49] offset:-4096\n" \
"v_accvgpr_read_b32 v235, a69\n" \
"v_mfma_f32_16x16x32_bf16 v[88:91], v[48:51], v[192:195], v[88:91]\n" \
"ds_read_b64_tr_b16 v[6:7], v232 offset:10240\n" \
"v_accvgpr_read_b32 v236, a70\n" \
"v_mfma_f32_16x16x32_bf16 v[88:91], v[52:55], v[196:199], v[88:91]\n" \
"ds_read_b64_tr_b16 v[8:9], v233 offset:0\n" \
"v_accvgpr_read_b32 v237, a71\n" \
"v_mfma_f32_16x16x32_bf16 v[92:95], v[56:59], v[192:195], v[92:95]\n" \
"ds_read_b64_tr_b16 v[10:11], v233 offset:2048\n" \
"v_mul_f32 v234, s92, v234\n" \
"v_mfma_f32_16x16x32_bf16 v[92:95], v[60:63], v[196:199], v[92:95]\n" \
"global_load_lds_dwordx4 v225, s[48:49] offset:-3072\n" \
"v_mul_f32 v235, s92, v235\n" \
"v_mfma_f32_16x16x32_bf16 v[112:115], v[32:35], v[200:203], v[112:115]\n" \
"ds_read_b128 v[192:195], v230 offset:0\n" \
"v_mul_f32 v236, s92, v236\n" \
"v_mfma_f32_16x16x32_bf16 v[112:115], v[36:39], v[204:207], v[112:115]\n" \
"ds_read_b128 v[196:199], v231 offset:0\n" \
"v_mul_f32 v237, s92, v237\n" \
"v_mfma_f32_16x16x32_bf16 v[116:119], v[40:43], v[200:203], v[116:119]\n" \
"ds_read_b64_tr_b16 v[12:13], v233 offset:8192\n" \
"v_cvt_pk_bf16_f32 v234, v234, v235\n" \
"v_mfma_f32_16x16x32_bf16 v[116:119], v[44:47], v[204:207], v[116:119]\n" \
"global_load_lds_dwordx4 v226, s[48:49] offset:-2048\n" \
"v_cvt_pk_bf16_f32 v236, v236, v237\n" \
"v_mfma_f32_16x16x32_bf16 v[120:123], v[48:51], v[200:203], v[120:123]\n" \
"ds_read_b64_tr_b16 v[14:15], v233 offset:10240\n" \
"v_accvgpr_write_b32 a66, v234\n" \
"v_mfma_f32_16x16x32_bf16 v[120:123], v[52:55], v[204:207], v[120:123]\n" \
"ds_read_b64_tr_b16 v[16:17], v232 offset:512\n" \
"v_accvgpr_write_b32 a67, v236\n" \
"v_mfma_f32_16x16x32_bf16 v[124:127], v[56:59], v[200:203], v[124:127]\n" \
"ds_read_b64_tr_b16 v[18:19], v232 offset:2560\n" \
"v_accvgpr_read_b32 v234, a96\n" \
"v_mfma_f32_16x16x32_bf16 v[124:127], v[60:63], v[204:207], v[124:127]\n" \
"global_load_lds_dwordx4 v227, s[48:49] offset:-1024\n" \
"v_accvgpr_read_b32 v235, a97\n" \
"v_mfma_f32_16x16x32_bf16 v[144:147], v[32:35], v[208:211], v[144:147]\n" \
"s_add_u32 s48, s48, 0x80\n" \
"s_addc_u32 s49, s49, 0\n" \
"ds_read_b128 v[200:203], v230 offset:2048\n" \
"v_mfma_f32_16x16x32_bf16 v[144:147], v[36:39], v[212:215], v[144:147]\n" \
"ds_read_b128 v[204:207], v231 offset:2048\n" \
"v_accvgpr_read_b32 v236, a98\n" \
"v_mfma_f32_16x16x32_bf16 v[148:151], v[40:43], v[208:211], v[148:151]\n" \
"ds_read_b64_tr_b16 v[20:21], v232 offset:8704\n" \
"v_accvgpr_read_b32 v237, a99\n" \
"v_mfma_f32_16x16x32_bf16 v[148:151], v[44:47], v[212:215], v[148:151]\n" \
"global_load_lds_dwordx4 v224, s[46:47] offset:0\n" \
"v_mul_f32 v234, s92, v234\n" \
"v_mfma_f32_16x16x32_bf16 v[152:155], v[48:51], v[208:211], v[152:155]\n" \
"ds_read_b64_tr_b16 v[22:23], v232 offset:10752\n" \
"v_mul_f32 v235, s92, v235\n" \
"v_mfma_f32_16x16x32_bf16 v[152:155], v[52:55], v[212:215], v[152:155]\n" \
"ds_read_b64_tr_b16 v[24:25], v233 offset:512\n" \
"v_mul_f32 v236, s92, v236\n" \
"v_mfma_f32_16x16x32_bf16 v[156:159], v[56:59], v[208:211], v[156:159]\n" \
"ds_read_b64_tr_b16 v[26:27], v233 offset:2560\n" \
"v_mul_f32 v237, s92, v237\n" \
"v_mfma_f32_16x16x32_bf16 v[156:159], v[60:63], v[212:215], v[156:159]\n" \
"global_load_lds_dwordx4 v225, s[46:47] offset:1024\n" \
"v_cvt_pk_bf16_f32 v234, v234, v235\n" \
"v_mfma_f32_16x16x32_bf16 v[176:179], v[32:35], v[216:219], v[176:179]\n" \
"ds_read_b128 v[208:211], v230 offset:4096\n" \
"v_cvt_pk_bf16_f32 v236, v236, v237\n" \
"v_mfma_f32_16x16x32_bf16 v[176:179], v[36:39], v[220:223], v[176:179]\n" \
"ds_read_b128 v[212:215], v231 offset:4096\n" \
"v_accvgpr_write_b32 a96, v234\n" \
"v_mfma_f32_16x16x32_bf16 v[180:183], v[40:43], v[216:219], v[180:183]\n" \
"ds_read_b64_tr_b16 v[28:29], v233 offset:8704\n" \
"v_accvgpr_write_b32 a97, v236\n" \
"v_mfma_f32_16x16x32_bf16 v[180:183], v[44:47], v[220:223], v[180:183]\n" \
"global_load_lds_dwordx4 v226, s[46:47] offset:2048\n" \
"v_accvgpr_read_b32 v234, a100\n" \
"v_mfma_f32_16x16x32_bf16 v[184:187], v[48:51], v[216:219], v[184:187]\n" \
"ds_read_b64_tr_b16 v[30:31], v233 offset:10752\n" \
"v_accvgpr_read_b32 v235, a101\n" \
"v_mfma_f32_16x16x32_bf16 v[184:187], v[52:55], v[220:223], v[184:187]\n" \
"v_accvgpr_read_b32 v236, a102\n" \
"v_accvgpr_read_b32 v237, a103\n" \
"v_mfma_f32_16x16x32_bf16 v[188:191], v[56:59], v[216:219], v[188:191]\n" \
"v_mul_f32 v234, s92, v234\n" \
"v_mul_f32 v235, s92, v235\n" \
"v_mfma_f32_16x16x32_bf16 v[188:191], v[60:63], v[220:223], v[188:191]\n" \
"s_waitcnt lgkmcnt(1)\n" \
"global_load_lds_dwordx4 v227, s[46:47] offset:3072\n" \
"v_mul_f32 v236, s92, v236\n" \
"v_mfma_f32_16x16x32_bf16 v[64:67], v[0:3], v[192:195], v[64:67]\n" \
"s_add_u32 s46, s46, 0x80\n" \
"s_addc_u32 s47, s47, 0\n" \
"ds_read_b128 v[216:219], v230 offset:6144\n" \
"v_mfma_f32_16x16x32_bf16 v[64:67], v[4:7], v[196:199], v[64:67]\n" \
"ds_read_b128 v[220:223], v231 offset:6144\n" \
"v_mul_f32 v237, s92, v237\n" \
"v_mfma_f32_16x16x32_bf16 v[68:71], v[8:11], v[192:195], v[68:71]\n" \
"ds_read_b64_tr_b16 v[32:33], v232 offset:1024\n" \
"v_cvt_pk_bf16_f32 v234, v234, v235\n" \
"v_mfma_f32_16x16x32_bf16 v[68:71], v[12:15], v[196:199], v[68:71]\n" \
"buffer_store_dwordx4 a[216:219], v248, s[56:59], s90 offen offset:2048 sc1\n" \
"v_cvt_pk_bf16_f32 v236, v236, v237\n" \
"v_accvgpr_write_b32 a98, v234\n" \
"v_accvgpr_write_b32 a99, v236\n" \
"v_accvgpr_read_b32 v234, a72\n" \
"v_mfma_f32_16x16x32_bf16 v[72:75], v[16:19], v[192:195], v[72:75]\n" \
"ds_read_b64_tr_b16 v[34:35], v232 offset:3072\n" \
"v_accvgpr_read_b32 v235, a73\n" \
"v_mfma_f32_16x16x32_bf16 v[72:75], v[20:23], v[196:199], v[72:75]\n" \
"s_waitcnt lgkmcnt(3)\n" \
"ds_read_b64_tr_b16 v[36:37], v232 offset:9216\n" \
"v_accvgpr_read_b32 v236, a74\n" \
"v_mfma_f32_16x16x32_bf16 v[76:79], v[24:27], v[192:195], v[76:79]\n" \
"ds_read_b64_tr_b16 v[38:39], v232 offset:11264\n" \
"v_accvgpr_read_b32 v237, a75\n" \
"v_mfma_f32_16x16x32_bf16 v[76:79], v[28:31], v[196:199], v[76:79]\n" \
"v_mul_f32 v234, s92, v234\n" \
"v_mul_f32 v235, s92, v235\n" \
"v_mfma_f32_16x16x32_bf16 v[96:99], v[0:3], v[200:203], v[96:99]\n" \
"ds_read_b64_tr_b16 v[40:41], v233 offset:1024\n" \
"v_mul_f32 v236, s92, v236\n" \
"v_mfma_f32_16x16x32_bf16 v[96:99], v[4:7], v[204:207], v[96:99]\n" \
"ds_read_b64_tr_b16 v[42:43], v233 offset:3072\n" \
"v_mul_f32 v237, s92, v237\n" \
"v_mfma_f32_16x16x32_bf16 v[100:103], v[8:11], v[200:203], v[100:103]\n" \
"ds_read_b64_tr_b16 v[44:45], v233 offset:9216\n" \
"v_cvt_pk_bf16_f32 v234, v234, v235\n" \
"v_mfma_f32_16x16x32_bf16 v[100:103], v[12:15], v[204:207], v[100:103]\n" \
"v_cvt_pk_bf16_f32 v236, v236, v237\n" \
"v_accvgpr_write_b32 a72, v234\n" \
"v_mfma_f32_16x16x32_bf16 v[104:107], v[16:19], v[200:203], v[104:107]\n" \
"ds_read_b64_tr_b16 v[46:47], v233 offset:11264\n" \
"v_accvgpr_write_b32 a73, v236\n" \
"v_mfma_f32_16x16x32_bf16 v[104:107], v[20:23], v[204:207], v[104:107]\n" \
"ds_read_b64_tr_b16 v[48:49], v232 offset:1536\n" \
"v_accvgpr_read_b32 v234, a76\n" \
"v_mfma_f32_16x16x32_bf16 v[108:111], v[24:27], v[200:203], v[108:111]\n" \
"s_add_u32 m0, s95, 53248\n" \
"ds_read_b64_tr_b16 v[50:51], v232 offset:3584\n" \
"v_accvgpr_read_b32 v235, a77\n" \
"v_mfma_f32_16x16x32_bf16 v[108:111], v[28:31], v[204:207], v[108:111]\n" \
"global_load_lds_dwordx4 v228, s[44:45] offset:-4096\n" \
"v_accvgpr_read_b32 v236, a78\n" \
"v_mfma_f32_16x16x32_bf16 v[128:131], v[0:3], v[208:211], v[128:131]\n" \
"ds_read_b64_tr_b16 v[52:53], v232 offset:9728\n" \
"v_accvgpr_read_b32 v237, a79\n" \
"v_mfma_f32_16x16x32_bf16 v[128:131], v[4:7], v[212:215], v[128:131]\n" \
"ds_read_b64_tr_b16 v[54:55], v232 offset:11776\n" \
"v_mul_f32 v234, s92, v234\n" \
"v_mfma_f32_16x16x32_bf16 v[132:135], v[8:11], v[208:211], v[132:135]\n" \
"ds_read_b64_tr_b16 v[56:57], v233 offset:1536\n" \
"v_mul_f32 v235, s92, v235\n" \
"v_mfma_f32_16x16x32_bf16 v[132:135], v[12:15], v[212:215], v[132:135]\n" \
"global_load_lds_dwordx4 v229, s[44:45] offset:-3072\n" \
"v_mul_f32 v236, s92, v236\n" \
"v_mfma_f32_16x16x32_bf16 v[136:139], v[16:19], v[208:211], v[136:139]\n" \
"s_add_u32 s44, s44, 0x80000\n" \
"s_addc_u32 s45, s45, 0\n" \
"ds_read_b64_tr_b16 v[58:59], v233 offset:3584\n" \
"v_mfma_f32_16x16x32_bf16 v[136:139], v[20:23], v[212:215], v[136:139]\n" \
"ds_read_b64_tr_b16 v[60:61], v233 offset:9728\n" \
"v_mul_f32 v237, s92, v237\n" \
"v_mfma_f32_16x16x32_bf16 v[140:143], v[24:27], v[208:211], v[140:143]\n" \
"ds_read_b64_tr_b16 v[62:63], v233 offset:11776\n" \
"v_cvt_pk_bf16_f32 v234, v234, v235\n" \
"v_mfma_f32_16x16x32_bf16 v[140:143], v[28:31], v[212:215], v[140:143]\n" \
"s_waitcnt lgkmcnt(3)\n" \
"global_load_lds_dwordx4 v228, s[42:43] offset:-2048\n" \
"v_cvt_pk_bf16_f32 v236, v236, v237\n" \
"v_mfma_f32_16x16x32_bf16 v[160:163], v[0:3], v[216:219], v[160:163]\n" \
"v_add_u32 v230, 0x8000, v230\n" \
"v_add_u32 v231, 0x8000, v231\n" \
"v_mfma_f32_16x16x32_bf16 v[160:163], v[4:7], v[220:223], v[160:163]\n" \
"v_add_u32 v232, 0x4000, v232\n" \
"v_add_u32 v233, 0x4000, v233\n" \
"v_mfma_f32_16x16x32_bf16 v[164:167], v[8:11], v[216:219], v[164:167]\n" \
"v_accvgpr_write_b32 a74, v234\n" \
"v_accvgpr_write_b32 a75, v236\n" \
"v_mfma_f32_16x16x32_bf16 v[164:167], v[12:15], v[220:223], v[164:167]\n" \
"global_load_lds_dwordx4 v229, s[42:43] offset:-1024\n" \
"v_accvgpr_read_b32 v234, a104\n" \
"v_mfma_f32_16x16x32_bf16 v[168:171], v[16:19], v[216:219], v[168:171]\n" \
"s_add_u32 s42, s42, 0x80000\n" \
"s_addc_u32 s43, s43, 0\n" \
"v_accvgpr_read_b32 v235, a105\n" \
"v_mfma_f32_16x16x32_bf16 v[168:171], v[20:23], v[220:223], v[168:171]\n" \
"v_accvgpr_read_b32 v236, a106\n" \
"v_accvgpr_read_b32 v237, a107\n" \
"v_mfma_f32_16x16x32_bf16 v[172:175], v[24:27], v[216:219], v[172:175]\n" \
"s_waitcnt vmcnt(18) lgkmcnt(0)\n" \
"v_mul_f32 v234, s92, v234\n" \
"v_mul_f32 v235, s92, v235\n" \
"v_mfma_f32_16x16x32_bf16 v[172:175], v[28:31], v[220:223], v[172:175]\n" \
"s_barrier\n" \
"v_mfma_f32_16x16x32_bf16 v[80:83], v[32:35], v[192:195], v[80:83]\n" \
"ds_read_b64_tr_b16 v[0:1], v232 offset:0\n" \
"v_mul_f32 v236, s92, v236\n" \
"v_mfma_f32_16x16x32_bf16 v[80:83], v[36:39], v[196:199], v[80:83]\n" \
"ds_read_b64_tr_b16 v[2:3], v232 offset:2048\n" \
"v_mul_f32 v237, s92, v237\n" \
"v_mfma_f32_16x16x32_bf16 v[84:87], v[40:43], v[192:195], v[84:87]\n" \
"s_add_u32 m0, s87, 69632\n" \
"ds_read_b64_tr_b16 v[4:5], v232 offset:8192\n" \
"v_cvt_pk_bf16_f32 v234, v234, v235\n" \
"v_mfma_f32_16x16x32_bf16 v[84:87], v[44:47], v[196:199], v[84:87]\n" \
"global_load_lds_dwordx4 v224, s[48:49] offset:-4096\n" \
"v_cvt_pk_bf16_f32 v236, v236, v237\n" \
"v_mfma_f32_16x16x32_bf16 v[88:91], v[48:51], v[192:195], v[88:91]\n" \
"ds_read_b64_tr_b16 v[6:7], v232 offset:10240\n" \
"v_accvgpr_write_b32 a104, v234\n" \
"v_mfma_f32_16x16x32_bf16 v[88:91], v[52:55], v[196:199], v[88:91]\n" \
"ds_read_b64_tr_b16 v[8:9], v233 offset:0\n" \
"v_accvgpr_write_b32 a105, v236\n" \
"v_mfma_f32_16x16x32_bf16 v[92:95], v[56:59], v[192:195], v[92:95]\n" \
"ds_read_b64_tr_b16 v[10:11], v233 offset:2048\n" \
"v_accvgpr_read_b32 v234, a108\n" \
"v_mfma_f32_16x16x32_bf16 v[92:95], v[60:63], v[196:199], v[92:95]\n" \
"global_load_lds_dwordx4 v225, s[48:49] offset:-3072\n" \
"v_accvgpr_read_b32 v235, a109\n" \
"v_mfma_f32_16x16x32_bf16 v[112:115], v[32:35], v[200:203], v[112:115]\n" \
"ds_read_b128 v[192:195], v230 offset:0\n" \
"v_accvgpr_read_b32 v236, a110\n" \
"v_mfma_f32_16x16x32_bf16 v[112:115], v[36:39], v[204:207], v[112:115]\n" \
"ds_read_b128 v[196:199], v231 offset:0\n" \
"v_accvgpr_read_b32 v237, a111\n" \
"v_mfma_f32_16x16x32_bf16 v[116:119], v[40:43], v[200:203], v[116:119]\n" \
"ds_read_b64_tr_b16 v[12:13], v233 offset:8192\n" \
"v_mul_f32 v234, s92, v234\n" \
"v_mfma_f32_16x16x32_bf16 v[116:119], v[44:47], v[204:207], v[116:119]\n" \
"global_load_lds_dwordx4 v226, s[48:49] offset:-2048\n" \
"v_mul_f32 v235, s92, v235\n" \
"v_mfma_f32_16x16x32_bf16 v[120:123], v[48:51], v[200:203], v[120:123]\n" \
"ds_read_b64_tr_b16 v[14:15], v233 offset:10240\n" \
"v_mul_f32 v236, s92, v236\n" \
"v_mfma_f32_16x16x32_bf16 v[120:123], v[52:55], v[204:207], v[120:123]\n" \
"ds_read_b64_tr_b16 v[16:17], v232 offset:512\n" \
"v_mul_f32 v237, s92, v237\n" \
"v_mfma_f32_16x16x32_bf16 v[124:127], v[56:59], v[200:203], v[124:127]\n" \
"ds_read_b64_tr_b16 v[18:19], v232 offset:2560\n" \
"v_cvt_pk_bf16_f32 v234, v234, v235\n" \
"v_mfma_f32_16x16x32_bf16 v[124:127], v[60:63], v[204:207], v[124:127]\n" \
"global_load_lds_dwordx4 v227, s[48:49] offset:-1024\n" \
"v_cvt_pk_bf16_f32 v236, v236, v237\n" \
"v_mfma_f32_16x16x32_bf16 v[144:147], v[32:35], v[208:211], v[144:147]\n" \
"s_add_u32 s48, s48, 0x80\n" \
"s_addc_u32 s49, s49, 0\n" \
"ds_read_b128 v[200:203], v230 offset:2048\n" \
"v_mfma_f32_16x16x32_bf16 v[144:147], v[36:39], v[212:215], v[144:147]\n" \
"ds_read_b128 v[204:207], v231 offset:2048\n" \
"v_accvgpr_write_b32 a106, v234\n" \
"v_mfma_f32_16x16x32_bf16 v[148:151], v[40:43], v[208:211], v[148:151]\n" \
"ds_read_b64_tr_b16 v[20:21], v232 offset:8704\n" \
"v_accvgpr_write_b32 a107, v236\n" \
"v_mfma_f32_16x16x32_bf16 v[148:151], v[44:47], v[212:215], v[148:151]\n" \
"global_load_lds_dwordx4 v224, s[46:47] offset:0\n" \
"v_accvgpr_read_b32 v234, a80\n" \
"v_mfma_f32_16x16x32_bf16 v[152:155], v[48:51], v[208:211], v[152:155]\n" \
"ds_read_b64_tr_b16 v[22:23], v232 offset:10752\n" \
"v_accvgpr_read_b32 v235, a81\n" \
"v_mfma_f32_16x16x32_bf16 v[152:155], v[52:55], v[212:215], v[152:155]\n" \
"ds_read_b64_tr_b16 v[24:25], v233 offset:512\n" \
"v_accvgpr_read_b32 v236, a82\n" \
"v_mfma_f32_16x16x32_bf16 v[156:159], v[56:59], v[208:211], v[156:159]\n" \
"ds_read_b64_tr_b16 v[26:27], v233 offset:2560\n" \
"v_accvgpr_read_b32 v237, a83\n" \
"v_mfma_f32_16x16x32_bf16 v[156:159], v[60:63], v[212:215], v[156:159]\n" \
"global_load_lds_dwordx4 v225, s[46:47] offset:1024\n" \
"v_cvt_pk_bf16_f32 v234, v234, v235\n" \
"v_mfma_f32_16x16x32_bf16 v[176:179], v[32:35], v[216:219], v[176:179]\n" \
"ds_read_b128 v[208:211], v230 offset:4096\n" \
"v_cvt_pk_bf16_f32 v236, v236, v237\n" \
"v_mfma_f32_16x16x32_bf16 v[176:179], v[36:39], v[220:223], v[176:179]\n" \
"ds_read_b128 v[212:215], v231 offset:4096\n" \
"v_accvgpr_write_b32 a80, v234\n" \
"v_mfma_f32_16x16x32_bf16 v[180:183], v[40:43], v[216:219], v[180:183]\n" \
"ds_read_b64_tr_b16 v[28:29], v233 offset:8704\n" \
"v_accvgpr_write_b32 a81, v236\n" \
"v_mfma_f32_16x16x32_bf16 v[180:183], v[44:47], v[220:223], v[180:183]\n" \
"global_load_lds_dwordx4 v226, s[46:47] offset:2048\n" \
"v_accvgpr_read_b32 v234, a112\n" \
"v_mfma_f32_16x16x32_bf16 v[184:187], v[48:51], v[216:219], v[184:187]\n" \
"ds_read_b64_tr_b16 v[30:31], v233 offset:10752\n" \
"v_accvgpr_read_b32 v235, a113\n" \
"v_mfma_f32_16x16x32_bf16 v[184:187], v[52:55], v[220:223], v[184:187]\n" \
"v_accvgpr_read_b32 v236, a114\n" \
"v_accvgpr_read_b32 v237, a115\n" \
"v_mfma_f32_16x16x32_bf16 v[188:191], v[56:59], v[216:219], v[188:191]\n" \
"v_cvt_pk_bf16_f32 v234, v234, v235\n" \
"v_cvt_pk_bf16_f32 v236, v236, v237\n" \
"v_mfma_f32_16x16x32_bf16 v[188:191], v[60:63], v[220:223], v[188:191]\n" \
"s_waitcnt lgkmcnt(1)\n" \
"global_load_lds_dwordx4 v227, s[46:47] offset:3072\n" \
"v_accvgpr_write_b32 a82, v234\n" \
"v_mfma_f32_16x16x32_bf16 v[64:67], v[0:3], v[192:195], v[64:67]\n" \
"s_add_u32 s46, s46, 0x80\n" \
"s_addc_u32 s47, s47, 0\n" \
"ds_read_b128 v[216:219], v230 offset:6144\n" \
"v_mfma_f32_16x16x32_bf16 v[64:67], v[4:7], v[196:199], v[64:67]\n" \
"ds_read_b128 v[220:223], v231 offset:6144\n" \
"v_accvgpr_write_b32 a83, v236\n" \
"v_mfma_f32_16x16x32_bf16 v[68:71], v[8:11], v[192:195], v[68:71]\n" \
"ds_read_b64_tr_b16 v[32:33], v232 offset:1024\n" \
"v_accvgpr_read_b32 v234, a84\n" \
"v_mfma_f32_16x16x32_bf16 v[68:71], v[12:15], v[196:199], v[68:71]\n" \
"buffer_store_dwordx4 a[220:223], v248, s[56:59], s90 offen offset:3072 sc1\n" \
"v_accvgpr_read_b32 v235, a85\n" \
"v_accvgpr_read_b32 v236, a86\n" \
"v_accvgpr_read_b32 v237, a87\n" \
"v_cvt_pk_bf16_f32 v234, v234, v235\n" \
"v_mfma_f32_16x16x32_bf16 v[72:75], v[16:19], v[192:195], v[72:75]\n" \
"s_add_u32 s90, s88, 139264\n" \
"ds_read_b64_tr_b16 v[34:35], v232 offset:3072\n" \
"v_cvt_pk_bf16_f32 v236, v236, v237\n" \
"v_mfma_f32_16x16x32_bf16 v[72:75], v[20:23], v[196:199], v[72:75]\n" \
"s_waitcnt lgkmcnt(3)\n" \
"ds_read_b64_tr_b16 v[36:37], v232 offset:9216\n" \
"v_accvgpr_write_b32 a84, v234\n" \
"v_mfma_f32_16x16x32_bf16 v[76:79], v[24:27], v[192:195], v[76:79]\n" \
"ds_read_b64_tr_b16 v[38:39], v232 offset:11264\n" \
"v_accvgpr_write_b32 a85, v236\n" \
"v_mfma_f32_16x16x32_bf16 v[76:79], v[28:31], v[196:199], v[76:79]\n" \
"buffer_store_dwordx4 a[128:131], v249, s[56:59], s90 offen offset:0 sc1\n" \
"v_accvgpr_read_b32 v234, a116\n" \
"v_accvgpr_read_b32 v235, a117\n" \
"v_accvgpr_read_b32 v236, a118\n" \
"v_accvgpr_read_b32 v237, a119\n" \
"v_mfma_f32_16x16x32_bf16 v[96:99], v[0:3], v[200:203], v[96:99]\n" \
"ds_read_b64_tr_b16 v[40:41], v233 offset:1024\n" \
"v_cvt_pk_bf16_f32 v234, v234, v235\n" \
"v_mfma_f32_16x16x32_bf16 v[96:99], v[4:7], v[204:207], v[96:99]\n" \
"ds_read_b64_tr_b16 v[42:43], v233 offset:3072\n" \
"v_cvt_pk_bf16_f32 v236, v236, v237\n" \
"v_mfma_f32_16x16x32_bf16 v[100:103], v[8:11], v[200:203], v[100:103]\n" \
"ds_read_b64_tr_b16 v[44:45], v233 offset:9216\n" \
"v_accvgpr_write_b32 a86, v234\n" \
"v_mfma_f32_16x16x32_bf16 v[100:103], v[12:15], v[204:207], v[100:103]\n" \
"v_accvgpr_write_b32 a87, v236\n" \
"v_accvgpr_read_b32 v234, a88\n" \
"v_mfma_f32_16x16x32_bf16 v[104:107], v[16:19], v[200:203], v[104:107]\n" \
"ds_read_b64_tr_b16 v[46:47], v233 offset:11264\n" \
"v_accvgpr_read_b32 v235, a89\n" \
"v_mfma_f32_16x16x32_bf16 v[104:107], v[20:23], v[204:207], v[104:107]\n" \
"ds_read_b64_tr_b16 v[48:49], v232 offset:1536\n" \
"v_accvgpr_read_b32 v236, a90\n" \
"v_mfma_f32_16x16x32_bf16 v[108:111], v[24:27], v[200:203], v[108:111]\n" \
"s_add_u32 m0, s95, 4096\n" \
"ds_read_b64_tr_b16 v[50:51], v232 offset:3584\n" \
"v_accvgpr_read_b32 v237, a91\n" \
"v_mfma_f32_16x16x32_bf16 v[108:111], v[28:31], v[204:207], v[108:111]\n" \
"global_load_lds_dwordx4 v228, s[44:45] offset:-4096\n" \
"v_cvt_pk_bf16_f32 v234, v234, v235\n" \
"v_mfma_f32_16x16x32_bf16 v[128:131], v[0:3], v[208:211], v[128:131]\n" \
"ds_read_b64_tr_b16 v[52:53], v232 offset:9728\n" \
"v_cvt_pk_bf16_f32 v236, v236, v237\n" \
"v_mfma_f32_16x16x32_bf16 v[128:131], v[4:7], v[212:215], v[128:131]\n" \
"ds_read_b64_tr_b16 v[54:55], v232 offset:11776\n" \
"v_accvgpr_write_b32 a88, v234\n" \
"v_mfma_f32_16x16x32_bf16 v[132:135], v[8:11], v[208:211], v[132:135]\n" \
"ds_read_b64_tr_b16 v[56:57], v233 offset:1536\n" \
"v_accvgpr_write_b32 a89, v236\n" \
"v_mfma_f32_16x16x32_bf16 v[132:135], v[12:15], v[212:215], v[132:135]\n" \
"global_load_lds_dwordx4 v229, s[44:45] offset:-3072\n" \
"v_accvgpr_read_b32 v234, a120\n" \
"v_mfma_f32_16x16x32_bf16 v[136:139], v[16:19], v[208:211], v[136:139]\n" \
"s_add_u32 s44, s44, 0x80000\n" \
"s_addc_u32 s45, s45, 0\n" \
"ds_read_b64_tr_b16 v[58:59], v233 offset:3584\n" \
"v_mfma_f32_16x16x32_bf16 v[136:139], v[20:23], v[212:215], v[136:139]\n" \
"ds_read_b64_tr_b16 v[60:61], v233 offset:9728\n" \
"v_accvgpr_read_b32 v235, a121\n" \
"v_mfma_f32_16x16x32_bf16 v[140:143], v[24:27], v[208:211], v[140:143]\n" \
"ds_read_b64_tr_b16 v[62:63], v233 offset:11776\n" \
"v_accvgpr_read_b32 v236, a122\n" \
"v_mfma_f32_16x16x32_bf16 v[140:143], v[28:31], v[212:215], v[140:143]\n" \
"s_waitcnt lgkmcnt(3)\n" \
"global_load_lds_dwordx4 v228, s[42:43] offset:-2048\n" \
"v_accvgpr_read_b32 v237, a123\n" \
"v_mfma_f32_16x16x32_bf16 v[160:163], v[0:3], v[216:219], v[160:163]\n" \
"v_add_u32 v230, 0x8000, v230\n" \
"v_add_u32 v231, 0x8000, v231\n" \
"v_mfma_f32_16x16x32_bf16 v[160:163], v[4:7], v[220:223], v[160:163]\n" \
"v_add_u32 v232, 0x4000, v232\n" \
"v_add_u32 v233, 0x4000, v233\n" \
"v_mfma_f32_16x16x32_bf16 v[164:167], v[8:11], v[216:219], v[164:167]\n" \
"v_cvt_pk_bf16_f32 v234, v234, v235\n" \
"v_cvt_pk_bf16_f32 v236, v236, v237\n" \
"v_mfma_f32_16x16x32_bf16 v[164:167], v[12:15], v[220:223], v[164:167]\n" \
"global_load_lds_dwordx4 v229, s[42:43] offset:-1024\n" \
"v_accvgpr_write_b32 a90, v234\n" \
"v_mfma_f32_16x16x32_bf16 v[168:171], v[16:19], v[216:219], v[168:171]\n" \
"s_add_u32 s42, s42, 0x80000\n" \
"s_addc_u32 s43, s43, 0\n" \
"v_accvgpr_write_b32 a91, v236\n" \
"v_mfma_f32_16x16x32_bf16 v[168:171], v[20:23], v[220:223], v[168:171]\n" \
"v_accvgpr_read_b32 v234, a92\n" \
"v_accvgpr_read_b32 v235, a93\n" \
"v_mfma_f32_16x16x32_bf16 v[172:175], v[24:27], v[216:219], v[172:175]\n" \
"s_waitcnt vmcnt(19) lgkmcnt(0)\n" \
"v_accvgpr_read_b32 v236, a94\n" \
"v_accvgpr_read_b32 v237, a95\n" \
"v_mfma_f32_16x16x32_bf16 v[172:175], v[28:31], v[220:223], v[172:175]\n" \
"s_barrier\n" \
"v_mfma_f32_16x16x32_bf16 v[80:83], v[32:35], v[192:195], v[80:83]\n" \
"ds_read_b64_tr_b16 v[0:1], v232 offset:0\n" \
"v_cvt_pk_bf16_f32 v234, v234, v235\n" \
"v_mfma_f32_16x16x32_bf16 v[80:83], v[36:39], v[196:199], v[80:83]\n" \
"ds_read_b64_tr_b16 v[2:3], v232 offset:2048\n" \
"v_cvt_pk_bf16_f32 v236, v236, v237\n" \
"v_mfma_f32_16x16x32_bf16 v[84:87], v[40:43], v[192:195], v[84:87]\n" \
"s_add_u32 m0, s87, 102400\n" \
"ds_read_b64_tr_b16 v[4:5], v232 offset:8192\n" \
"v_accvgpr_write_b32 a92, v234\n" \
"v_mfma_f32_16x16x32_bf16 v[84:87], v[44:47], v[196:199], v[84:87]\n" \
"Lpg3310_%=:\n" \
"s_cbranch_execz Lpg5399_%=\n" \
"global_load_lds_dwordx4 v224, s[48:49] offset:-4096\n" \
"v_accvgpr_write_b32 a93, v236\n" \
"v_mfma_f32_16x16x32_bf16 v[88:91], v[48:51], v[192:195], v[88:91]\n" \
"ds_read_b64_tr_b16 v[6:7], v232 offset:10240\n" \
"v_accvgpr_read_b32 v234, a124\n" \
"v_mfma_f32_16x16x32_bf16 v[88:91], v[52:55], v[196:199], v[88:91]\n" \
"ds_read_b64_tr_b16 v[8:9], v233 offset:0\n" \
"v_accvgpr_read_b32 v235, a125\n" \
"v_mfma_f32_16x16x32_bf16 v[92:95], v[56:59], v[192:195], v[92:95]\n" \
"ds_read_b64_tr_b16 v[10:11], v233 offset:2048\n" \
"v_accvgpr_read_b32 v236, a126\n" \
"v_mfma_f32_16x16x32_bf16 v[92:95], v[60:63], v[196:199], v[92:95]\n" \
"global_load_lds_dwordx4 v225, s[48:49] offset:-3072\n" \
"v_accvgpr_read_b32 v237, a127\n" \
"v_mfma_f32_16x16x32_bf16 v[112:115], v[32:35], v[200:203], v[112:115]\n" \
"ds_read_b128 v[192:195], v230 offset:0\n" \
"v_cvt_pk_bf16_f32 v234, v234, v235\n" \
"v_mfma_f32_16x16x32_bf16 v[112:115], v[36:39], v[204:207], v[112:115]\n" \
"ds_read_b128 v[196:199], v231 offset:0\n" \
"v_cvt_pk_bf16_f32 v236, v236, v237\n" \
"v_mfma_f32_16x16x32_bf16 v[116:119], v[40:43], v[200:203], v[116:119]\n" \
"ds_read_b64_tr_b16 v[12:13], v233 offset:8192\n" \
"v_accvgpr_write_b32 a94, v234\n" \
"v_mfma_f32_16x16x32_bf16 v[116:119], v[44:47], v[204:207], v[116:119]\n" \
"global_load_lds_dwordx4 v226, s[48:49] offset:-2048\n" \
"v_accvgpr_write_b32 a95, v236\n" \
"v_mfma_f32_16x16x32_bf16 v[120:123], v[48:51], v[200:203], v[120:123]\n" \
"ds_read_b64_tr_b16 v[14:15], v233 offset:10240\n" \
"v_accvgpr_read_b32 v234, a0\n" \
"v_mfma_f32_16x16x32_bf16 v[120:123], v[52:55], v[204:207], v[120:123]\n" \
"ds_read_b64_tr_b16 v[16:17], v232 offset:512\n" \
"v_accvgpr_read_b32 v235, a1\n" \
"v_mfma_f32_16x16x32_bf16 v[124:127], v[56:59], v[200:203], v[124:127]\n" \
"ds_read_b64_tr_b16 v[18:19], v232 offset:2560\n" \
"v_accvgpr_read_b32 v236, a2\n" \
"v_mfma_f32_16x16x32_bf16 v[124:127], v[60:63], v[204:207], v[124:127]\n" \
"global_load_lds_dwordx4 v227, s[48:49] offset:-1024\n" \
"v_accvgpr_read_b32 v237, a3\n" \
"v_mfma_f32_16x16x32_bf16 v[144:147], v[32:35], v[208:211], v[144:147]\n" \
"s_add_u32 s48, s48, 0x80\n" \
"s_addc_u32 s49, s49, 0\n" \
"ds_read_b128 v[200:203], v230 offset:2048\n" \
"v_mfma_f32_16x16x32_bf16 v[144:147], v[36:39], v[212:215], v[144:147]\n" \
"ds_read_b128 v[204:207], v231 offset:2048\n" \
"v_mul_f32 v234, s92, v234\n" \
"v_mfma_f32_16x16x32_bf16 v[148:151], v[40:43], v[208:211], v[148:151]\n" \
"ds_read_b64_tr_b16 v[20:21], v232 offset:8704\n" \
"v_mul_f32 v235, s92, v235\n" \
"v_mfma_f32_16x16x32_bf16 v[148:151], v[44:47], v[212:215], v[148:151]\n" \
"global_load_lds_dwordx4 v224, s[46:47] offset:0\n" \
"v_mul_f32 v236, s92, v236\n" \
"v_mfma_f32_16x16x32_bf16 v[152:155], v[48:51], v[208:211], v[152:155]\n" \
"ds_read_b64_tr_b16 v[22:23], v232 offset:10752\n" \
"v_mul_f32 v237, s92, v237\n" \
"v_mfma_f32_16x16x32_bf16 v[152:155], v[52:55], v[212:215], v[152:155]\n" \
"ds_read_b64_tr_b16 v[24:25], v233 offset:512\n" \
"v_cvt_pk_bf16_f32 v234, v234, v235\n" \
"v_mfma_f32_16x16x32_bf16 v[156:159], v[56:59], v[208:211], v[156:159]\n" \
"ds_read_b64_tr_b16 v[26:27], v233 offset:2560\n" \
"v_cvt_pk_bf16_f32 v236, v236, v237\n" \
"v_mfma_f32_16x16x32_bf16 v[156:159], v[60:63], v[212:215], v[156:159]\n" \
"global_load_lds_dwordx4 v225, s[46:47] offset:1024\n" \
"v_accvgpr_write_b32 a0, v234\n" \
"v_mfma_f32_16x16x32_bf16 v[176:179], v[32:35], v[216:219], v[176:179]\n" \
"ds_read_b128 v[208:211], v230 offset:4096\n" \
"v_accvgpr_write_b32 a1, v236\n" \
"v_mfma_f32_16x16x32_bf16 v[176:179], v[36:39], v[220:223], v[176:179]\n" \
"ds_read_b128 v[212:215], v231 offset:4096\n" \
"v_accvgpr_read_b32 v234, a4\n" \
"v_mfma_f32_16x16x32_bf16 v[180:183], v[40:43], v[216:219], v[180:183]\n" \
"ds_read_b64_tr_b16 v[28:29], v233 offset:8704\n" \
"v_accvgpr_read_b32 v235, a5\n" \
"v_mfma_f32_16x16x32_bf16 v[180:183], v[44:47], v[220:223], v[180:183]\n" \
"global_load_lds_dwordx4 v226, s[46:47] offset:2048\n" \
"v_accvgpr_read_b32 v236, a6\n" \
"v_mfma_f32_16x16x32_bf16 v[184:187], v[48:51], v[216:219], v[184:187]\n" \
"ds_read_b64_tr_b16 v[30:31], v233 offset:10752\n" \
"v_accvgpr_read_b32 v237, a7\n" \
"v_mfma_f32_16x16x32_bf16 v[184:187], v[52:55], v[220:223], v[184:187]\n" \
"v_mul_f32 v234, s92, v234\n" \
"v_mul_f32 v235, s92, v235\n" \
"v_mfma_f32_16x16x32_bf16 v[188:191], v[56:59], v[216:219], v[188:191]\n" \
"v_mul_f32 v236, s92, v236\n" \
"v_mul_f32 v237, s92, v237\n" \
"v_mfma_f32_16x16x32_bf16 v[188:191], v[60:63], v[220:223], v[188:191]\n" \
"s_waitcnt lgkmcnt(1)\n" \
"global_load_lds_dwordx4 v227, s[46:47] offset:3072\n" \
"v_cvt_pk_bf16_f32 v234, v234, v235\n" \
"v_mfma_f32_16x16x32_bf16 v[64:67], v[0:3], v[192:195], v[64:67]\n" \
"s_add_u32 s46, s46, 0x80\n" \
"s_addc_u32 s47, s47, 0\n" \
"ds_read_b128 v[216:219], v230 offset:6144\n" \
"v_mfma_f32_16x16x32_bf16 v[64:67], v[4:7], v[196:199], v[64:67]\n" \
"ds_read_b128 v[220:223], v231 offset:6144\n" \
"v_cvt_pk_bf16_f32 v236, v236, v237\n" \
"v_mfma_f32_16x16x32_bf16 v[68:71], v[8:11], v[192:195], v[68:71]\n" \
"ds_read_b64_tr_b16 v[32:33], v232 offset:1024\n" \
"v_accvgpr_write_b32 a2, v234\n" \
"v_mfma_f32_16x16x32_bf16 v[68:71], v[12:15], v[196:199], v[68:71]\n" \
"buffer_store_dwordx4 a[160:163], v249, s[56:59], s90 offen offset:256 sc1\n" \
"v_accvgpr_write_b32 a3, v236\n" \
"v_accvgpr_read_b32 v234, a32\n" \
"v_accvgpr_read_b32 v235, a33\n" \
"v_accvgpr_read_b32 v236, a34\n" \
"v_mfma_f32_16x16x32_bf16 v[72:75], v[16:19], v[192:195], v[72:75]\n" \
"ds_read_b64_tr_b16 v[34:35], v232 offset:3072\n" \
"v_accvgpr_read_b32 v237, a35\n" \
"v_mfma_f32_16x16x32_bf16 v[72:75], v[20:23], v[196:199], v[72:75]\n" \
"s_waitcnt lgkmcnt(3)\n" \
"ds_read_b64_tr_b16 v[36:37], v232 offset:9216\n" \
"v_mul_f32 v234, s92, v234\n" \
"v_mfma_f32_16x16x32_bf16 v[76:79], v[24:27], v[192:195], v[76:79]\n" \
"ds_read_b64_tr_b16 v[38:39], v232 offset:11264\n" \
"v_mul_f32 v235, s92, v235\n" \
"v_mfma_f32_16x16x32_bf16 v[76:79], v[28:31], v[196:199], v[76:79]\n" \
"v_mul_f32 v236, s92, v236\n" \
"v_mul_f32 v237, s92, v237\n" \
"v_mfma_f32_16x16x32_bf16 v[96:99], v[0:3], v[200:203], v[96:99]\n" \
"ds_read_b64_tr_b16 v[40:41], v233 offset:1024\n" \
"v_cvt_pk_bf16_f32 v234, v234, v235\n" \
"v_mfma_f32_16x16x32_bf16 v[96:99], v[4:7], v[204:207], v[96:99]\n" \
"ds_read_b64_tr_b16 v[42:43], v233 offset:3072\n" \
"v_cvt_pk_bf16_f32 v236, v236, v237\n" \
"v_mfma_f32_16x16x32_bf16 v[100:103], v[8:11], v[200:203], v[100:103]\n" \
"ds_read_b64_tr_b16 v[44:45], v233 offset:9216\n" \
"v_accvgpr_write_b32 a32, v234\n" \
"v_mfma_f32_16x16x32_bf16 v[100:103], v[12:15], v[204:207], v[100:103]\n" \
"v_accvgpr_write_b32 a33, v236\n" \
"v_accvgpr_read_b32 v234, a36\n" \
"v_mfma_f32_16x16x32_bf16 v[104:107], v[16:19], v[200:203], v[104:107]\n" \
"ds_read_b64_tr_b16 v[46:47], v233 offset:11264\n" \
"v_accvgpr_read_b32 v235, a37\n" \
"v_mfma_f32_16x16x32_bf16 v[104:107], v[20:23], v[204:207], v[104:107]\n" \
"ds_read_b64_tr_b16 v[48:49], v232 offset:1536\n" \
"v_accvgpr_read_b32 v236, a38\n" \
"v_mfma_f32_16x16x32_bf16 v[108:111], v[24:27], v[200:203], v[108:111]\n" \
"s_add_u32 m0, s95, 20480\n" \
"ds_read_b64_tr_b16 v[50:51], v232 offset:3584\n" \
"v_accvgpr_read_b32 v237, a39\n" \
"v_mfma_f32_16x16x32_bf16 v[108:111], v[28:31], v[204:207], v[108:111]\n" \
"global_load_lds_dwordx4 v228, s[44:45] offset:-4096\n" \
"v_mul_f32 v234, s92, v234\n" \
"v_mfma_f32_16x16x32_bf16 v[128:131], v[0:3], v[208:211], v[128:131]\n" \
"ds_read_b64_tr_b16 v[52:53], v232 offset:9728\n" \
"v_mul_f32 v235, s92, v235\n" \
"v_mfma_f32_16x16x32_bf16 v[128:131], v[4:7], v[212:215], v[128:131]\n" \
"ds_read_b64_tr_b16 v[54:55], v232 offset:11776\n" \
"v_mul_f32 v236, s92, v236\n" \
"v_mfma_f32_16x16x32_bf16 v[132:135], v[8:11], v[208:211], v[132:135]\n" \
"ds_read_b64_tr_b16 v[56:57], v233 offset:1536\n" \
"v_mul_f32 v237, s92, v237\n" \
"v_mfma_f32_16x16x32_bf16 v[132:135], v[12:15], v[212:215], v[132:135]\n" \
"global_load_lds_dwordx4 v229, s[44:45] offset:-3072\n" \
"v_cvt_pk_bf16_f32 v234, v234, v235\n" \
"v_mfma_f32_16x16x32_bf16 v[136:139], v[16:19], v[208:211], v[136:139]\n" \
"s_add_u32 s44, s44, 0x80000\n" \
"s_addc_u32 s45, s45, 0\n" \
"ds_read_b64_tr_b16 v[58:59], v233 offset:3584\n" \
"v_mfma_f32_16x16x32_bf16 v[136:139], v[20:23], v[212:215], v[136:139]\n" \
"ds_read_b64_tr_b16 v[60:61], v233 offset:9728\n" \
"v_cvt_pk_bf16_f32 v236, v236, v237\n" \
"v_mfma_f32_16x16x32_bf16 v[140:143], v[24:27], v[208:211], v[140:143]\n" \
"ds_read_b64_tr_b16 v[62:63], v233 offset:11776\n" \
"v_accvgpr_write_b32 a34, v234\n" \
"v_mfma_f32_16x16x32_bf16 v[140:143], v[28:31], v[212:215], v[140:143]\n" \
"s_waitcnt lgkmcnt(3)\n" \
"global_load_lds_dwordx4 v228, s[42:43] offset:-2048\n" \
"v_accvgpr_write_b32 a35, v236\n" \
"v_mfma_f32_16x16x32_bf16 v[160:163], v[0:3], v[216:219], v[160:163]\n" \
"v_add_u32 v230, 0xffff0000, v230\n" \
"v_add_u32 v231, 0xffff0000, v231\n" \
"v_mfma_f32_16x16x32_bf16 v[160:163], v[4:7], v[220:223], v[160:163]\n" \
"v_add_u32 v232, 0x4000, v232\n" \
"v_add_u32 v233, 0x4000, v233\n" \
"v_mfma_f32_16x16x32_bf16 v[164:167], v[8:11], v[216:219], v[164:167]\n" \
"v_accvgpr_read_b32 v234, a8\n" \
"v_accvgpr_read_b32 v235, a9\n" \
"v_mfma_f32_16x16x32_bf16 v[164:167], v[12:15], v[220:223], v[164:167]\n" \
"global_load_lds_dwordx4 v229, s[42:43] offset:-1024\n" \
"v_accvgpr_read_b32 v236, a10\n" \
"v_mfma_f32_16x16x32_bf16 v[168:171], v[16:19], v[216:219], v[168:171]\n" \
"s_add_u32 s42, s42, 0x80000\n" \
"s_addc_u32 s43, s43, 0\n" \
"v_accvgpr_read_b32 v237, a11\n" \
"v_mfma_f32_16x16x32_bf16 v[168:171], v[20:23], v[220:223], v[168:171]\n" \
"v_mul_f32 v234, s92, v234\n" \
"v_mul_f32 v235, s92, v235\n" \
"v_mfma_f32_16x16x32_bf16 v[172:175], v[24:27], v[216:219], v[172:175]\n" \
"s_waitcnt vmcnt(19) lgkmcnt(0)\n" \
"v_mul_f32 v236, s92, v236\n" \
"v_mul_f32 v237, s92, v237\n" \
"v_mfma_f32_16x16x32_bf16 v[172:175], v[28:31], v[220:223], v[172:175]\n" \
"s_barrier\n" \
"v_mfma_f32_16x16x32_bf16 v[80:83], v[32:35], v[192:195], v[80:83]\n" \
"ds_read_b64_tr_b16 v[0:1], v232 offset:0\n" \
"v_cvt_pk_bf16_f32 v234, v234, v235\n" \
"v_mfma_f32_16x16x32_bf16 v[80:83], v[36:39], v[196:199], v[80:83]\n" \
"ds_read_b64_tr_b16 v[2:3], v232 offset:2048\n" \
"v_cvt_pk_bf16_f32 v236, v236, v237\n" \
"v_mfma_f32_16x16x32_bf16 v[84:87], v[40:43], v[192:195], v[84:87]\n" \
"s_add_u32 m0, s87, 135168\n" \
"ds_read_b64_tr_b16 v[4:5], v232 offset:8192\n" \
"v_accvgpr_write_b32 a8, v234\n" \
"v_mfma_f32_16x16x32_bf16 v[84:87], v[44:47], v[196:199], v[84:87]\n" \
"global_load_lds_dwordx4 v224, s[48:49] offset:-4096\n" \
"v_accvgpr_write_b32 a9, v236\n" \
"v_mfma_f32_16x16x32_bf16 v[88:91], v[48:51], v[192:195], v[88:91]\n" \
"ds_read_b64_tr_b16 v[6:7], v232 offset:10240\n" \
"v_accvgpr_read_b32 v234, a12\n" \
"v_mfma_f32_16x16x32_bf16 v[88:91], v[52:55], v[196:199], v[88:91]\n" \
"ds_read_b64_tr_b16 v[8:9], v233 offset:0\n" \
"v_accvgpr_read_b32 v235, a13\n" \
"v_mfma_f32_16x16x32_bf16 v[92:95], v[56:59], v[192:195], v[92:95]\n" \
"ds_read_b64_tr_b16 v[10:11], v233 offset:2048\n" \
"v_accvgpr_read_b32 v236, a14\n" \
"v_mfma_f32_16x16x32_bf16 v[92:95], v[60:63], v[196:199], v[92:95]\n" \
"global_load_lds_dwordx4 v225, s[48:49] offset:-3072\n" \
"v_accvgpr_read_b32 v237, a15\n" \
"v_mfma_f32_16x16x32_bf16 v[112:115], v[32:35], v[200:203], v[112:115]\n" \
"ds_read_b128 v[192:195], v230 offset:0\n" \
"v_mul_f32 v234, s92, v234\n" \
"v_mfma_f32_16x16x32_bf16 v[112:115], v[36:39], v[204:207], v[112:115]\n" \
"ds_read_b128 v[196:199], v231 offset:0\n" \
"v_mul_f32 v235, s92, v235\n" \
"v_mfma_f32_16x16x32_bf16 v[116:119], v[40:43], v[200:203], v[116:119]\n" \
"ds_read_b64_tr_b16 v[12:13], v233 offset:8192\n" \
"v_mul_f32 v236, s92, v236\n" \
"v_mfma_f32_16x16x32_bf16 v[116:119], v[44:47], v[204:207], v[116:119]\n" \
"global_load_lds_dwordx4 v226, s[48:49] offset:-2048\n" \
"v_mul_f32 v237, s92, v237\n" \
"v_mfma_f32_16x16x32_bf16 v[120:123], v[48:51], v[200:203], v[120:123]\n" \
"ds_read_b64_tr_b16 v[14:15], v233 offset:10240\n" \
"v_cvt_pk_bf16_f32 v234, v234, v235\n" \
"v_mfma_f32_16x16x32_bf16 v[120:123], v[52:55], v[204:207], v[120:123]\n" \
"ds_read_b64_tr_b16 v[16:17], v232 offset:512\n" \
"v_cvt_pk_bf16_f32 v236, v236, v237\n" \
"v_mfma_f32_16x16x32_bf16 v[124:127], v[56:59], v[200:203], v[124:127]\n" \
"ds_read_b64_tr_b16 v[18:19], v232 offset:2560\n" \
"v_accvgpr_write_b32 a10, v234\n" \
"v_mfma_f32_16x16x32_bf16 v[124:127], v[60:63], v[204:207], v[124:127]\n" \
"global_load_lds_dwordx4 v227, s[48:49] offset:-1024\n" \
"v_accvgpr_write_b32 a11, v236\n" \
"v_mfma_f32_16x16x32_bf16 v[144:147], v[32:35], v[208:211], v[144:147]\n" \
"s_add_u32 s48, s48, 0x80\n" \
"s_addc_u32 s49, s49, 0\n" \
"ds_read_b128 v[200:203], v230 offset:2048\n" \
"v_mfma_f32_16x16x32_bf16 v[144:147], v[36:39], v[212:215], v[144:147]\n" \
"ds_read_b128 v[204:207], v231 offset:2048\n" \
"v_accvgpr_read_b32 v234, a40\n" \
"v_mfma_f32_16x16x32_bf16 v[148:151], v[40:43], v[208:211], v[148:151]\n" \
"ds_read_b64_tr_b16 v[20:21], v232 offset:8704\n" \
"v_accvgpr_read_b32 v235, a41\n" \
"v_mfma_f32_16x16x32_bf16 v[148:151], v[44:47], v[212:215], v[148:151]\n" \
"global_load_lds_dwordx4 v224, s[46:47] offset:0\n" \
"v_accvgpr_read_b32 v236, a42\n" \
"v_mfma_f32_16x16x32_bf16 v[152:155], v[48:51], v[208:211], v[152:155]\n" \
"ds_read_b64_tr_b16 v[22:23], v232 offset:10752\n" \
"v_accvgpr_read_b32 v237, a43\n" \
"v_mfma_f32_16x16x32_bf16 v[152:155], v[52:55], v[212:215], v[152:155]\n" \
"ds_read_b64_tr_b16 v[24:25], v233 offset:512\n" \
"v_mul_f32 v234, s92, v234\n" \
"v_mfma_f32_16x16x32_bf16 v[156:159], v[56:59], v[208:211], v[156:159]\n" \
"ds_read_b64_tr_b16 v[26:27], v233 offset:2560\n" \
"v_mul_f32 v235, s92, v235\n" \
"v_mfma_f32_16x16x32_bf16 v[156:159], v[60:63], v[212:215], v[156:159]\n" \
"global_load_lds_dwordx4 v225, s[46:47] offset:1024\n" \
"v_mul_f32 v236, s92, v236\n" \
"v_mfma_f32_16x16x32_bf16 v[176:179], v[32:35], v[216:219], v[176:179]\n" \
"ds_read_b128 v[208:211], v230 offset:4096\n" \
"v_mul_f32 v237, s92, v237\n" \
"v_mfma_f32_16x16x32_bf16 v[176:179], v[36:39], v[220:223], v[176:179]\n" \
"ds_read_b128 v[212:215], v231 offset:4096\n" \
"v_cvt_pk_bf16_f32 v234, v234, v235\n" \
"v_mfma_f32_16x16x32_bf16 v[180:183], v[40:43], v[216:219], v[180:183]\n" \
"ds_read_b64_tr_b16 v[28:29], v233 offset:8704\n" \
"v_cvt_pk_bf16_f32 v236, v236, v237\n" \
"v_mfma_f32_16x16x32_bf16 v[180:183], v[44:47], v[220:223], v[180:183]\n" \
"global_load_lds_dwordx4 v226, s[46:47] offset:2048\n" \
"v_accvgpr_write_b32 a40, v234\n" \
"v_mfma_f32_16x16x32_bf16 v[184:187], v[48:51], v[216:219], v[184:187]\n" \
"ds_read_b64_tr_b16 v[30:31], v233 offset:10752\n" \
"v_accvgpr_write_b32 a41, v236\n" \
"v_mfma_f32_16x16x32_bf16 v[184:187], v[52:55], v[220:223], v[184:187]\n" \
"v_accvgpr_read_b32 v234, a44\n" \
"v_accvgpr_read_b32 v235, a45\n" \
"v_mfma_f32_16x16x32_bf16 v[188:191], v[56:59], v[216:219], v[188:191]\n" \
"v_accvgpr_read_b32 v236, a46\n" \
"v_accvgpr_read_b32 v237, a47\n" \
"v_mfma_f32_16x16x32_bf16 v[188:191], v[60:63], v[220:223], v[188:191]\n" \
"s_waitcnt lgkmcnt(1)\n" \
"global_load_lds_dwordx4 v227, s[46:47] offset:3072\n" \
"v_mul_f32 v234, s92, v234\n" \
"v_mfma_f32_16x16x32_bf16 v[64:67], v[0:3], v[192:195], v[64:67]\n" \
"s_add_u32 s46, s46, 0x80\n" \
"s_addc_u32 s47, s47, 0\n" \
"ds_read_b128 v[216:219], v230 offset:6144\n" \
"v_mfma_f32_16x16x32_bf16 v[64:67], v[4:7], v[196:199], v[64:67]\n" \
"ds_read_b128 v[220:223], v231 offset:6144\n" \
"v_mul_f32 v235, s92, v235\n" \
"v_mfma_f32_16x16x32_bf16 v[68:71], v[8:11], v[192:195], v[68:71]\n" \
"ds_read_b64_tr_b16 v[32:33], v232 offset:1024\n" \
"v_mul_f32 v236, s92, v236\n" \
"v_mfma_f32_16x16x32_bf16 v[68:71], v[12:15], v[196:199], v[68:71]\n" \
"buffer_store_dwordx4 a[136:139], v249, s[56:59], s90 offen offset:2048 sc1\n" \
"v_mul_f32 v237, s92, v237\n" \
"v_cvt_pk_bf16_f32 v234, v234, v235\n" \
"v_cvt_pk_bf16_f32 v236, v236, v237\n" \
"v_accvgpr_write_b32 a42, v234\n" \
"v_mfma_f32_16x16x32_bf16 v[72:75], v[16:19], v[192:195], v[72:75]\n" \
"ds_read_b64_tr_b16 v[34:35], v232 offset:3072\n" \
"v_accvgpr_write_b32 a43, v236\n" \
"v_mfma_f32_16x16x32_bf16 v[72:75], v[20:23], v[196:199], v[72:75]\n" \
"s_waitcnt lgkmcnt(3)\n" \
"ds_read_b64_tr_b16 v[36:37], v232 offset:9216\n" \
"v_accvgpr_read_b32 v234, a16\n" \
"v_mfma_f32_16x16x32_bf16 v[76:79], v[24:27], v[192:195], v[76:79]\n" \
"ds_read_b64_tr_b16 v[38:39], v232 offset:11264\n" \
"v_accvgpr_read_b32 v235, a17\n" \
"v_mfma_f32_16x16x32_bf16 v[76:79], v[28:31], v[196:199], v[76:79]\n" \
"v_accvgpr_read_b32 v236, a18\n" \
"v_accvgpr_read_b32 v237, a19\n" \
"v_mfma_f32_16x16x32_bf16 v[96:99], v[0:3], v[200:203], v[96:99]\n" \
"ds_read_b64_tr_b16 v[40:41], v233 offset:1024\n" \
"v_cvt_pk_bf16_f32 v234, v234, v235\n" \
"v_mfma_f32_16x16x32_bf16 v[96:99], v[4:7], v[204:207], v[96:99]\n" \
"ds_read_b64_tr_b16 v[42:43], v233 offset:3072\n" \
"v_cvt_pk_bf16_f32 v236, v236, v237\n" \
"v_mfma_f32_16x16x32_bf16 v[100:103], v[8:11], v[200:203], v[100:103]\n" \
"ds_read_b64_tr_b16 v[44:45], v233 offset:9216\n" \
"v_accvgpr_write_b32 a16, v234\n" \
"v_mfma_f32_16x16x32_bf16 v[100:103], v[12:15], v[204:207], v[100:103]\n" \
"v_accvgpr_write_b32 a17, v236\n" \
"v_accvgpr_read_b32 v234, a48\n" \
"v_mfma_f32_16x16x32_bf16 v[104:107], v[16:19], v[200:203], v[104:107]\n" \
"ds_read_b64_tr_b16 v[46:47], v233 offset:11264\n" \
"v_accvgpr_read_b32 v235, a49\n" \
"v_mfma_f32_16x16x32_bf16 v[104:107], v[20:23], v[204:207], v[104:107]\n" \
"ds_read_b64_tr_b16 v[48:49], v232 offset:1536\n" \
"v_accvgpr_read_b32 v236, a50\n" \
"v_mfma_f32_16x16x32_bf16 v[108:111], v[24:27], v[200:203], v[108:111]\n" \
"s_add_u32 m0, s95, 36864\n" \
"ds_read_b64_tr_b16 v[50:51], v232 offset:3584\n" \
"v_accvgpr_read_b32 v237, a51\n" \
"v_mfma_f32_16x16x32_bf16 v[108:111], v[28:31], v[204:207], v[108:111]\n" \
"global_load_lds_dwordx4 v228, s[44:45] offset:-4096\n" \
"v_cvt_pk_bf16_f32 v234, v234, v235\n" \
"v_mfma_f32_16x16x32_bf16 v[128:131], v[0:3], v[208:211], v[128:131]\n" \
"ds_read_b64_tr_b16 v[52:53], v232 offset:9728\n" \
"v_cvt_pk_bf16_f32 v236, v236, v237\n" \
"v_mfma_f32_16x16x32_bf16 v[128:131], v[4:7], v[212:215], v[128:131]\n" \
"ds_read_b64_tr_b16 v[54:55], v232 offset:11776\n" \
"v_accvgpr_write_b32 a18, v234\n" \
"v_mfma_f32_16x16x32_bf16 v[132:135], v[8:11], v[208:211], v[132:135]\n" \
"ds_read_b64_tr_b16 v[56:57], v233 offset:1536\n" \
"v_accvgpr_write_b32 a19, v236\n" \
"v_mfma_f32_16x16x32_bf16 v[132:135], v[12:15], v[212:215], v[132:135]\n" \
"global_load_lds_dwordx4 v229, s[44:45] offset:-3072\n" \
"v_accvgpr_read_b32 v234, a20\n" \
"v_mfma_f32_16x16x32_bf16 v[136:139], v[16:19], v[208:211], v[136:139]\n" \
"s_add_u32 s44, s44, 0x80000\n" \
"s_addc_u32 s45, s45, 0\n" \
"ds_read_b64_tr_b16 v[58:59], v233 offset:3584\n" \
"v_mfma_f32_16x16x32_bf16 v[136:139], v[20:23], v[212:215], v[136:139]\n" \
"ds_read_b64_tr_b16 v[60:61], v233 offset:9728\n" \
"v_accvgpr_read_b32 v235, a21\n" \
"v_mfma_f32_16x16x32_bf16 v[140:143], v[24:27], v[208:211], v[140:143]\n" \
"ds_read_b64_tr_b16 v[62:63], v233 offset:11776\n" \
"v_accvgpr_read_b32 v236, a22\n" \
"v_mfma_f32_16x16x32_bf16 v[140:143], v[28:31], v[212:215], v[140:143]\n" \
"s_waitcnt lgkmcnt(3)\n" \
"global_load_lds_dwordx4 v228, s[42:43] offset:-2048\n" \
"v_accvgpr_read_b32 v237, a23\n" \
"v_mfma_f32_16x16x32_bf16 v[160:163], v[0:3], v[216:219], v[160:163]\n" \
"v_add_u32 v230, 0x8000, v230\n" \
"v_add_u32 v231, 0x8000, v231\n" \
"v_mfma_f32_16x16x32_bf16 v[160:163], v[4:7], v[220:223], v[160:163]\n" \
"v_add_u32 v232, 0xffff4000, v232\n" \
"v_add_u32 v233, 0xffff4000, v233\n" \
"v_mfma_f32_16x16x32_bf16 v[164:167], v[8:11], v[216:219], v[164:167]\n" \
"v_cvt_pk_bf16_f32 v234, v234, v235\n" \
"v_cvt_pk_bf16_f32 v236, v236, v237\n" \
"v_mfma_f32_16x16x32_bf16 v[164:167], v[12:15], v[220:223], v[164:167]\n" \
"global_load_lds_dwordx4 v229, s[42:43] offset:-1024\n" \
"v_accvgpr_write_b32 a20, v234\n" \
"v_mfma_f32_16x16x32_bf16 v[168:171], v[16:19], v[216:219], v[168:171]\n" \
"s_add_u32 s42, s42, 0x80000\n" \
"s_addc_u32 s43, s43, 0\n" \
"v_accvgpr_write_b32 a21, v236\n" \
"v_mfma_f32_16x16x32_bf16 v[168:171], v[20:23], v[220:223], v[168:171]\n" \
"v_accvgpr_read_b32 v234, a52\n" \
"v_accvgpr_read_b32 v235, a53\n" \
"v_mfma_f32_16x16x32_bf16 v[172:175], v[24:27], v[216:219], v[172:175]\n" \
"s_waitcnt vmcnt(18) lgkmcnt(0)\n" \
"v_accvgpr_read_b32 v236, a54\n" \
"v_accvgpr_read_b32 v237, a55\n" \
"v_mfma_f32_16x16x32_bf16 v[172:175], v[28:31], v[220:223], v[172:175]\n" \
"s_barrier\n" \
"v_mfma_f32_16x16x32_bf16 v[80:83], v[32:35], v[192:195], v[80:83]\n" \
"ds_read_b64_tr_b16 v[0:1], v232 offset:0\n" \
"v_cvt_pk_bf16_f32 v234, v234, v235\n" \
"v_mfma_f32_16x16x32_bf16 v[80:83], v[36:39], v[196:199], v[80:83]\n" \
"ds_read_b64_tr_b16 v[2:3], v232 offset:2048\n" \
"v_cvt_pk_bf16_f32 v236, v236, v237\n" \
"v_mfma_f32_16x16x32_bf16 v[84:87], v[40:43], v[192:195], v[84:87]\n" \
"s_add_u32 m0, s87, 69632\n" \
"ds_read_b64_tr_b16 v[4:5], v232 offset:8192\n" \
"v_accvgpr_write_b32 a22, v234\n" \
"v_mfma_f32_16x16x32_bf16 v[84:87], v[44:47], v[196:199], v[84:87]\n" \
"global_load_lds_dwordx4 v224, s[48:49] offset:-4096\n" \
"v_accvgpr_write_b32 a23, v236\n" \
"v_mfma_f32_16x16x32_bf16 v[88:91], v[48:51], v[192:195], v[88:91]\n" \
"ds_read_b64_tr_b16 v[6:7], v232 offset:10240\n" \
"v_accvgpr_read_b32 v234, a24\n" \
"v_mfma_f32_16x16x32_bf16 v[88:91], v[52:55], v[196:199], v[88:91]\n" \
"ds_read_b64_tr_b16 v[8:9], v233 offset:0\n" \
"v_accvgpr_read_b32 v235, a25\n" \
"v_mfma_f32_16x16x32_bf16 v[92:95], v[56:59], v[192:195], v[92:95]\n" \
"ds_read_b64_tr_b16 v[10:11], v233 offset:2048\n" \
"v_accvgpr_read_b32 v236, a26\n" \
"v_mfma_f32_16x16x32_bf16 v[92:95], v[60:63], v[196:199], v[92:95]\n" \
"global_load_lds_dwordx4 v225, s[48:49] offset:-3072\n" \
"v_accvgpr_read_b32 v237, a27\n" \
"v_mfma_f32_16x16x32_bf16 v[112:115], v[32:35], v[200:203], v[112:115]\n" \
"ds_read_b128 v[192:195], v230 offset:0\n" \
"v_cvt_pk_bf16_f32 v234, v234, v235\n" \
"v_mfma_f32_16x16x32_bf16 v[112:115], v[36:39], v[204:207], v[112:115]\n" \
"ds_read_b128 v[196:199], v231 offset:0\n" \
"v_cvt_pk_bf16_f32 v236, v236, v237\n" \
"v_mfma_f32_16x16x32_bf16 v[116:119], v[40:43], v[200:203], v[116:119]\n" \
"ds_read_b64_tr_b16 v[12:13], v233 offset:8192\n" \
"v_accvgpr_write_b32 a24, v234\n" \
"v_mfma_f32_16x16x32_bf16 v[116:119], v[44:47], v[204:207], v[116:119]\n" \
"global_load_lds_dwordx4 v226, s[48:49] offset:-2048\n" \
"v_accvgpr_write_b32 a25, v236\n" \
"v_mfma_f32_16x16x32_bf16 v[120:123], v[48:51], v[200:203], v[120:123]\n" \
"ds_read_b64_tr_b16 v[14:15], v233 offset:10240\n" \
"v_accvgpr_read_b32 v234, a56\n" \
"v_mfma_f32_16x16x32_bf16 v[120:123], v[52:55], v[204:207], v[120:123]\n" \
"ds_read_b64_tr_b16 v[16:17], v232 offset:512\n" \
"v_accvgpr_read_b32 v235, a57\n" \
"v_mfma_f32_16x16x32_bf16 v[124:127], v[56:59], v[200:203], v[124:127]\n" \
"ds_read_b64_tr_b16 v[18:19], v232 offset:2560\n" \
"v_accvgpr_read_b32 v236, a58\n" \
"v_mfma_f32_16x16x32_bf16 v[124:127], v[60:63], v[204:207], v[124:127]\n" \
"global_load_lds_dwordx4 v227, s[48:49] offset:-1024\n" \
"v_accvgpr_read_b32 v237, a59\n" \
"v_mfma_f32_16x16x32_bf16 v[144:147], v[32:35], v[208:211], v[144:147]\n" \
"s_add_u32 s48, s48, 0x80\n" \
"s_addc_u32 s49, s49, 0\n" \
"ds_read_b128 v[200:203], v230 offset:2048\n" \
"v_mfma_f32_16x16x32_bf16 v[144:147], v[36:39], v[212:215], v[144:147]\n" \
"ds_read_b128 v[204:207], v231 offset:2048\n" \
"v_cvt_pk_bf16_f32 v234, v234, v235\n" \
"v_mfma_f32_16x16x32_bf16 v[148:151], v[40:43], v[208:211], v[148:151]\n" \
"ds_read_b64_tr_b16 v[20:21], v232 offset:8704\n" \
"v_cvt_pk_bf16_f32 v236, v236, v237\n" \
"v_mfma_f32_16x16x32_bf16 v[148:151], v[44:47], v[212:215], v[148:151]\n" \
"global_load_lds_dwordx4 v224, s[46:47] offset:0\n" \
"v_accvgpr_write_b32 a26, v234\n" \
"v_mfma_f32_16x16x32_bf16 v[152:155], v[48:51], v[208:211], v[152:155]\n" \
"ds_read_b64_tr_b16 v[22:23], v232 offset:10752\n" \
"v_accvgpr_write_b32 a27, v236\n" \
"v_mfma_f32_16x16x32_bf16 v[152:155], v[52:55], v[212:215], v[152:155]\n" \
"ds_read_b64_tr_b16 v[24:25], v233 offset:512\n" \
"v_accvgpr_read_b32 v234, a28\n" \
"v_mfma_f32_16x16x32_bf16 v[156:159], v[56:59], v[208:211], v[156:159]\n" \
"ds_read_b64_tr_b16 v[26:27], v233 offset:2560\n" \
"v_accvgpr_read_b32 v235, a29\n" \
"v_mfma_f32_16x16x32_bf16 v[156:159], v[60:63], v[212:215], v[156:159]\n" \
"global_load_lds_dwordx4 v225, s[46:47] offset:1024\n" \
"v_accvgpr_read_b32 v236, a30\n" \
"v_mfma_f32_16x16x32_bf16 v[176:179], v[32:35], v[216:219], v[176:179]\n" \
"ds_read_b128 v[208:211], v230 offset:4096\n" \
"v_accvgpr_read_b32 v237, a31\n" \
"v_mfma_f32_16x16x32_bf16 v[176:179], v[36:39], v[220:223], v[176:179]\n" \
"ds_read_b128 v[212:215], v231 offset:4096\n" \
"v_cvt_pk_bf16_f32 v234, v234, v235\n" \
"v_mfma_f32_16x16x32_bf16 v[180:183], v[40:43], v[216:219], v[180:183]\n" \
"ds_read_b64_tr_b16 v[28:29], v233 offset:8704\n" \
"v_cvt_pk_bf16_f32 v236, v236, v237\n" \
"v_mfma_f32_16x16x32_bf16 v[180:183], v[44:47], v[220:223], v[180:183]\n" \
"global_load_lds_dwordx4 v226, s[46:47] offset:2048\n" \
"v_accvgpr_write_b32 a28, v234\n" \
"v_mfma_f32_16x16x32_bf16 v[184:187], v[48:51], v[216:219], v[184:187]\n" \
"ds_read_b64_tr_b16 v[30:31], v233 offset:10752\n" \
"v_accvgpr_write_b32 a29, v236\n" \
"v_mfma_f32_16x16x32_bf16 v[184:187], v[52:55], v[220:223], v[184:187]\n" \
"v_accvgpr_read_b32 v234, a60\n" \
"v_accvgpr_read_b32 v235, a61\n" \
"v_mfma_f32_16x16x32_bf16 v[188:191], v[56:59], v[216:219], v[188:191]\n" \
"v_accvgpr_read_b32 v236, a62\n" \
"v_accvgpr_read_b32 v237, a63\n" \
"v_mfma_f32_16x16x32_bf16 v[188:191], v[60:63], v[220:223], v[188:191]\n" \
"s_waitcnt lgkmcnt(1)\n" \
"global_load_lds_dwordx4 v227, s[46:47] offset:3072\n" \
"v_cvt_pk_bf16_f32 v234, v234, v235\n" \
"v_mfma_f32_16x16x32_bf16 v[64:67], v[0:3], v[192:195], v[64:67]\n" \
"s_add_u32 s46, s46, 0x80\n" \
"s_addc_u32 s47, s47, 0\n" \
"ds_read_b128 v[216:219], v230 offset:6144\n" \
"v_mfma_f32_16x16x32_bf16 v[64:67], v[4:7], v[196:199], v[64:67]\n" \
"ds_read_b128 v[220:223], v231 offset:6144\n" \
"v_cvt_pk_bf16_f32 v236, v236, v237\n" \
"v_mfma_f32_16x16x32_bf16 v[68:71], v[8:11], v[192:195], v[68:71]\n" \
"ds_read_b64_tr_b16 v[32:33], v232 offset:1024\n" \
"v_accvgpr_write_b32 a30, v234\n" \
"v_mfma_f32_16x16x32_bf16 v[68:71], v[12:15], v[196:199], v[68:71]\n" \
"buffer_store_dwordx4 a[168:171], v249, s[56:59], s90 offen offset:2304 sc1\n" \
"v_accvgpr_write_b32 a31, v236\n" \
"v_mfma_f32_16x16x32_bf16 v[72:75], v[16:19], v[192:195], v[72:75]\n" \
"s_add_u32 s90, s89, 139264\n" \
"ds_read_b64_tr_b16 v[34:35], v232 offset:3072\n" \
"v_mfma_f32_16x16x32_bf16 v[72:75], v[20:23], v[196:199], v[72:75]\n" \
"s_waitcnt lgkmcnt(3)\n" \
"ds_read_b64_tr_b16 v[36:37], v232 offset:9216\n" \
"v_mfma_f32_16x16x32_bf16 v[76:79], v[24:27], v[192:195], v[76:79]\n" \
"ds_read_b64_tr_b16 v[38:39], v232 offset:11264\n" \
"v_mfma_f32_16x16x32_bf16 v[76:79], v[28:31], v[196:199], v[76:79]\n" \
"buffer_store_dwordx4 a[144:147], v248, s[56:59], s90 offen offset:0 sc1\n" \
"v_mfma_f32_16x16x32_bf16 v[96:99], v[0:3], v[200:203], v[96:99]\n" \
"s_lshl_b32 s70, s63, 2\n" \
"ds_read_b64_tr_b16 v[40:41], v233 offset:1024\n" \
"v_mfma_f32_16x16x32_bf16 v[96:99], v[4:7], v[204:207], v[96:99]\n" \
"s_add_u32 s70, s70, s91\n" \
"ds_read_b64_tr_b16 v[42:43], v233 offset:3072\n" \
"v_mov_b32 v238, s60\n" \
"v_mfma_f32_16x16x32_bf16 v[100:103], v[8:11], v[200:203], v[100:103]\n" \
"ds_read_b64_tr_b16 v[44:45], v233 offset:9216\n" \
"v_lshlrev_b32 v239, 28, s61\n" \
"v_mfma_f32_16x16x32_bf16 v[100:103], v[12:15], v[204:207], v[100:103]\n" \
"buffer_store_dword v238, v239, s[56:59], s70 offen sc1\n" \
"v_mfma_f32_16x16x32_bf16 v[104:107], v[16:19], v[200:203], v[104:107]\n" \
"ds_read_b64_tr_b16 v[46:47], v233 offset:11264\n" \
"v_mfma_f32_16x16x32_bf16 v[104:107], v[20:23], v[204:207], v[104:107]\n" \
"ds_read_b64_tr_b16 v[48:49], v232 offset:1536\n" \
"v_mfma_f32_16x16x32_bf16 v[108:111], v[24:27], v[200:203], v[108:111]\n" \
"s_add_u32 m0, s95, 53248\n" \
"ds_read_b64_tr_b16 v[50:51], v232 offset:3584\n" \
"v_mfma_f32_16x16x32_bf16 v[108:111], v[28:31], v[204:207], v[108:111]\n" \
"global_load_lds_dwordx4 v228, s[44:45] offset:-4096\n" \
"Lpg3851_%=:\n" \
"s_cbranch_execz Lpg5548_%=\n" \
"v_mfma_f32_16x16x32_bf16 v[128:131], v[0:3], v[208:211], v[128:131]\n" \
"ds_read_b64_tr_b16 v[52:53], v232 offset:9728\n" \
"v_mfma_f32_16x16x32_bf16 v[128:131], v[4:7], v[212:215], v[128:131]\n" \
"ds_read_b64_tr_b16 v[54:55], v232 offset:11776\n" \
"v_mfma_f32_16x16x32_bf16 v[132:135], v[8:11], v[208:211], v[132:135]\n" \
"ds_read_b64_tr_b16 v[56:57], v233 offset:1536\n" \
"v_mfma_f32_16x16x32_bf16 v[132:135], v[12:15], v[212:215], v[132:135]\n" \
"global_load_lds_dwordx4 v229, s[44:45] offset:-3072\n" \
"v_mfma_f32_16x16x32_bf16 v[136:139], v[16:19], v[208:211], v[136:139]\n" \
"s_add_u32 s44, s44, 0x100000\n" \
"s_addc_u32 s45, s45, 0\n" \
"ds_read_b64_tr_b16 v[58:59], v233 offset:3584\n" \
"v_mfma_f32_16x16x32_bf16 v[136:139], v[20:23], v[212:215], v[136:139]\n" \
"ds_read_b64_tr_b16 v[60:61], v233 offset:9728\n" \
"v_mfma_f32_16x16x32_bf16 v[140:143], v[24:27], v[208:211], v[140:143]\n" \
"ds_read_b64_tr_b16 v[62:63], v233 offset:11776\n" \
"v_mfma_f32_16x16x32_bf16 v[140:143], v[28:31], v[212:215], v[140:143]\n" \
"s_waitcnt lgkmcnt(3)\n" \
"global_load_lds_dwordx4 v228, s[42:43] offset:-2048\n" \
"v_mfma_f32_16x16x32_bf16 v[160:163], v[0:3], v[216:219], v[160:163]\n" \
"v_add_u32 v230, 0x8000, v230\n" \
"v_add_u32 v231, 0x8000, v231\n" \
"v_mfma_f32_16x16x32_bf16 v[160:163], v[4:7], v[220:223], v[160:163]\n" \
"v_add_u32 v232, 0x4000, v232\n" \
"v_add_u32 v233, 0x4000, v233\n" \
"v_mfma_f32_16x16x32_bf16 v[164:167], v[8:11], v[216:219], v[164:167]\n" \
"v_mfma_f32_16x16x32_bf16 v[164:167], v[12:15], v[220:223], v[164:167]\n" \
"global_load_lds_dwordx4 v229, s[42:43] offset:-1024\n" \
"v_mfma_f32_16x16x32_bf16 v[168:171], v[16:19], v[216:219], v[168:171]\n" \
"s_add_u32 s42, s42, 0x100000\n" \
"s_addc_u32 s43, s43, 0\n" \
"v_mfma_f32_16x16x32_bf16 v[168:171], v[20:23], v[220:223], v[168:171]\n" \
"v_mfma_f32_16x16x32_bf16 v[172:175], v[24:27], v[216:219], v[172:175]\n" \
"s_waitcnt vmcnt(20) lgkmcnt(0)\n" \
"v_mfma_f32_16x16x32_bf16 v[172:175], v[28:31], v[220:223], v[172:175]\n" \
"s_barrier\n" \
"v_mfma_f32_16x16x32_bf16 v[80:83], v[32:35], v[192:195], v[80:83]\n" \
"ds_read_b64_tr_b16 v[0:1], v232 offset:0\n" \
"v_mfma_f32_16x16x32_bf16 v[80:83], v[36:39], v[196:199], v[80:83]\n" \
"ds_read_b64_tr_b16 v[2:3], v232 offset:2048\n" \
"v_mfma_f32_16x16x32_bf16 v[84:87], v[40:43], v[192:195], v[84:87]\n" \
"s_add_u32 m0, s87, 102400\n" \
"ds_read_b64_tr_b16 v[4:5], v232 offset:8192\n" \
"v_mfma_f32_16x16x32_bf16 v[84:87], v[44:47], v[196:199], v[84:87]\n" \
"global_load_lds_dwordx4 v224, s[48:49] offset:-4096\n" \
"v_mfma_f32_16x16x32_bf16 v[88:91], v[48:51], v[192:195], v[88:91]\n" \
"ds_read_b64_tr_b16 v[6:7], v232 offset:10240\n" \
"v_mfma_f32_16x16x32_bf16 v[88:91], v[52:55], v[196:199], v[88:91]\n" \
"ds_read_b64_tr_b16 v[8:9], v233 offset:0\n" \
"v_mfma_f32_16x16x32_bf16 v[92:95], v[56:59], v[192:195], v[92:95]\n" \
"ds_read_b64_tr_b16 v[10:11], v233 offset:2048\n" \
"v_mfma_f32_16x16x32_bf16 v[92:95], v[60:63], v[196:199], v[92:95]\n" \
"global_load_lds_dwordx4 v225, s[48:49] offset:-3072\n" \
"v_mfma_f32_16x16x32_bf16 v[112:115], v[32:35], v[200:203], v[112:115]\n" \
"ds_read_b128 v[192:195], v230 offset:0\n" \
"v_mfma_f32_16x16x32_bf16 v[112:115], v[36:39], v[204:207], v[112:115]\n" \
"ds_read_b128 v[196:199], v231 offset:0\n" \
"v_mfma_f32_16x16x32_bf16 v[116:119], v[40:43], v[200:203], v[116:119]\n" \
"ds_read_b64_tr_b16 v[12:13], v233 offset:8192\n" \
"v_mfma_f32_16x16x32_bf16 v[116:119], v[44:47], v[204:207], v[116:119]\n" \
"global_load_lds_dwordx4 v226, s[48:49] offset:-2048\n" \
"v_mfma_f32_16x16x32_bf16 v[120:123], v[48:51], v[200:203], v[120:123]\n" \
"ds_read_b64_tr_b16 v[14:15], v233 offset:10240\n" \
"v_mfma_f32_16x16x32_bf16 v[120:123], v[52:55], v[204:207], v[120:123]\n" \
"ds_read_b64_tr_b16 v[16:17], v232 offset:512\n" \
"v_mfma_f32_16x16x32_bf16 v[124:127], v[56:59], v[200:203], v[124:127]\n" \
"ds_read_b64_tr_b16 v[18:19], v232 offset:2560\n" \
"v_mfma_f32_16x16x32_bf16 v[124:127], v[60:63], v[204:207], v[124:127]\n" \
"global_load_lds_dwordx4 v227, s[48:49] offset:-1024\n" \
"v_mfma_f32_16x16x32_bf16 v[144:147], v[32:35], v[208:211], v[144:147]\n" \
"s_add_u32 s48, s48, 0x100\n" \
"s_addc_u32 s49, s49, 0\n" \
"ds_read_b128 v[200:203], v230 offset:2048\n" \
"v_mfma_f32_16x16x32_bf16 v[144:147], v[36:39], v[212:215], v[144:147]\n" \
"ds_read_b128 v[204:207], v231 offset:2048\n" \
"v_mfma_f32_16x16x32_bf16 v[148:151], v[40:43], v[208:211], v[148:151]\n" \
"ds_read_b64_tr_b16 v[20:21], v232 offset:8704\n" \
"v_mfma_f32_16x16x32_bf16 v[148:151], v[44:47], v[212:215], v[148:151]\n" \
"global_load_lds_dwordx4 v224, s[46:47] offset:0\n" \
"v_mfma_f32_16x16x32_bf16 v[152:155], v[48:51], v[208:211], v[152:155]\n" \
"ds_read_b64_tr_b16 v[22:23], v232 offset:10752\n" \
"v_mfma_f32_16x16x32_bf16 v[152:155], v[52:55], v[212:215], v[152:155]\n" \
"ds_read_b64_tr_b16 v[24:25], v233 offset:512\n" \
"v_mfma_f32_16x16x32_bf16 v[156:159], v[56:59], v[208:211], v[156:159]\n" \
"ds_read_b64_tr_b16 v[26:27], v233 offset:2560\n" \
"v_mfma_f32_16x16x32_bf16 v[156:159], v[60:63], v[212:215], v[156:159]\n" \
"global_load_lds_dwordx4 v225, s[46:47] offset:1024\n" \
"v_mfma_f32_16x16x32_bf16 v[176:179], v[32:35], v[216:219], v[176:179]\n" \
"ds_read_b128 v[208:211], v230 offset:4096\n" \
"v_mfma_f32_16x16x32_bf16 v[176:179], v[36:39], v[220:223], v[176:179]\n" \
"ds_read_b128 v[212:215], v231 offset:4096\n" \
"v_mfma_f32_16x16x32_bf16 v[180:183], v[40:43], v[216:219], v[180:183]\n" \
"ds_read_b64_tr_b16 v[28:29], v233 offset:8704\n" \
"v_mfma_f32_16x16x32_bf16 v[180:183], v[44:47], v[220:223], v[180:183]\n" \
"global_load_lds_dwordx4 v226, s[46:47] offset:2048\n" \
"v_mfma_f32_16x16x32_bf16 v[184:187], v[48:51], v[216:219], v[184:187]\n" \
"ds_read_b64_tr_b16 v[30:31], v233 offset:10752\n" \
"v_mfma_f32_16x16x32_bf16 v[184:187], v[52:55], v[220:223], v[184:187]\n" \
"v_mfma_f32_16x16x32_bf16 v[188:191], v[56:59], v[216:219], v[188:191]\n" \
"v_mfma_f32_16x16x32_bf16 v[188:191], v[60:63], v[220:223], v[188:191]\n" \
"s_waitcnt lgkmcnt(1)\n" \
"global_load_lds_dwordx4 v227, s[46:47] offset:3072\n" \
"v_mfma_f32_16x16x32_bf16 v[64:67], v[0:3], v[192:195], v[64:67]\n" \
"s_add_u32 s46, s46, 0x100\n" \
"s_addc_u32 s47, s47, 0\n" \
"ds_read_b128 v[216:219], v230 offset:6144\n" \
"v_mfma_f32_16x16x32_bf16 v[64:67], v[4:7], v[196:199], v[64:67]\n" \
"ds_read_b128 v[220:223], v231 offset:6144\n" \
"v_mfma_f32_16x16x32_bf16 v[68:71], v[8:11], v[192:195], v[68:71]\n" \
"ds_read_b64_tr_b16 v[32:33], v232 offset:1024\n" \
"v_mfma_f32_16x16x32_bf16 v[68:71], v[12:15], v[196:199], v[68:71]\n" \
"buffer_store_dwordx4 a[148:151], v248, s[56:59], s90 offen offset:1024 sc1\n" \
"v_mfma_f32_16x16x32_bf16 v[72:75], v[16:19], v[192:195], v[72:75]\n" \
"ds_read_b64_tr_b16 v[34:35], v232 offset:3072\n" \
"v_mfma_f32_16x16x32_bf16 v[72:75], v[20:23], v[196:199], v[72:75]\n" \
"s_waitcnt lgkmcnt(3)\n" \
"ds_read_b64_tr_b16 v[36:37], v232 offset:9216\n" \
"v_mfma_f32_16x16x32_bf16 v[76:79], v[24:27], v[192:195], v[76:79]\n" \
"ds_read_b64_tr_b16 v[38:39], v232 offset:11264\n" \
"v_mfma_f32_16x16x32_bf16 v[76:79], v[28:31], v[196:199], v[76:79]\n" \
"v_mfma_f32_16x16x32_bf16 v[96:99], v[0:3], v[200:203], v[96:99]\n" \
"ds_read_b64_tr_b16 v[40:41], v233 offset:1024\n" \
"v_mfma_f32_16x16x32_bf16 v[96:99], v[4:7], v[204:207], v[96:99]\n" \
"ds_read_b64_tr_b16 v[42:43], v233 offset:3072\n" \
"v_mfma_f32_16x16x32_bf16 v[100:103], v[8:11], v[200:203], v[100:103]\n" \
"ds_read_b64_tr_b16 v[44:45], v233 offset:9216\n" \
"v_mfma_f32_16x16x32_bf16 v[100:103], v[12:15], v[204:207], v[100:103]\n" \
"v_mfma_f32_16x16x32_bf16 v[104:107], v[16:19], v[200:203], v[104:107]\n" \
"ds_read_b64_tr_b16 v[46:47], v233 offset:11264\n" \
"v_mfma_f32_16x16x32_bf16 v[104:107], v[20:23], v[204:207], v[104:107]\n" \
"ds_read_b64_tr_b16 v[48:49], v232 offset:1536\n" \
"v_mfma_f32_16x16x32_bf16 v[108:111], v[24:27], v[200:203], v[108:111]\n" \
"s_add_u32 m0, s95, 4096\n" \
"ds_read_b64_tr_b16 v[50:51], v232 offset:3584\n" \
"v_mfma_f32_16x16x32_bf16 v[108:111], v[28:31], v[204:207], v[108:111]\n" \
"global_load_lds_dwordx4 v228, s[44:45] offset:-4096\n" \
"v_mfma_f32_16x16x32_bf16 v[128:131], v[0:3], v[208:211], v[128:131]\n" \
"ds_read_b64_tr_b16 v[52:53], v232 offset:9728\n" \
"v_mfma_f32_16x16x32_bf16 v[128:131], v[4:7], v[212:215], v[128:131]\n" \
"ds_read_b64_tr_b16 v[54:55], v232 offset:11776\n" \
"v_mfma_f32_16x16x32_bf16 v[132:135], v[8:11], v[208:211], v[132:135]\n" \
"ds_read_b64_tr_b16 v[56:57], v233 offset:1536\n" \
"v_mfma_f32_16x16x32_bf16 v[132:135], v[12:15], v[212:215], v[132:135]\n" \
"global_load_lds_dwordx4 v229, s[44:45] offset:-3072\n" \
"v_mfma_f32_16x16x32_bf16 v[136:139], v[16:19], v[208:211], v[136:139]\n" \
"s_add_u32 s44, s44, 0x80000\n" \
"s_addc_u32 s45, s45, 0\n" \
"ds_read_b64_tr_b16 v[58:59], v233 offset:3584\n" \
"v_mfma_f32_16x16x32_bf16 v[136:139], v[20:23], v[212:215], v[136:139]\n" \
"ds_read_b64_tr_b16 v[60:61], v233 offset:9728\n" \
"v_mfma_f32_16x16x32_bf16 v[140:143], v[24:27], v[208:211], v[140:143]\n" \
"ds_read_b64_tr_b16 v[62:63], v233 offset:11776\n" \
"v_mfma_f32_16x16x32_bf16 v[140:143], v[28:31], v[212:215], v[140:143]\n" \
"s_waitcnt lgkmcnt(3)\n" \
"global_load_lds_dwordx4 v228, s[42:43] offset:-2048\n" \
"v_mfma_f32_16x16x32_bf16 v[160:163], v[0:3], v[216:219], v[160:163]\n" \
"v_add_u32 v230, 0xffff0000, v230\n" \
"v_add_u32 v231, 0xffff0000, v231\n" \
"v_mfma_f32_16x16x32_bf16 v[160:163], v[4:7], v[220:223], v[160:163]\n" \
"v_add_u32 v232, 0x4000, v232\n" \
"v_add_u32 v233, 0x4000, v233\n" \
"v_mfma_f32_16x16x32_bf16 v[164:167], v[8:11], v[216:219], v[164:167]\n" \
"v_mfma_f32_16x16x32_bf16 v[164:167], v[12:15], v[220:223], v[164:167]\n" \
"global_load_lds_dwordx4 v229, s[42:43] offset:-1024\n" \
"v_mfma_f32_16x16x32_bf16 v[168:171], v[16:19], v[216:219], v[168:171]\n" \
"s_add_u32 s42, s42, 0x80000\n" \
"s_addc_u32 s43, s43, 0\n" \
"v_mfma_f32_16x16x32_bf16 v[168:171], v[20:23], v[220:223], v[168:171]\n" \
"v_mfma_f32_16x16x32_bf16 v[172:175], v[24:27], v[216:219], v[172:175]\n" \
"s_waitcnt vmcnt(20) lgkmcnt(0)\n" \
"v_mfma_f32_16x16x32_bf16 v[172:175], v[28:31], v[220:223], v[172:175]\n" \
"s_barrier\n" \
"v_mfma_f32_16x16x32_bf16 v[80:83], v[32:35], v[192:195], v[80:83]\n" \
"ds_read_b64_tr_b16 v[0:1], v232 offset:0\n" \
"v_mfma_f32_16x16x32_bf16 v[80:83], v[36:39], v[196:199], v[80:83]\n" \
"ds_read_b64_tr_b16 v[2:3], v232 offset:2048\n" \
"v_mfma_f32_16x16x32_bf16 v[84:87], v[40:43], v[192:195], v[84:87]\n" \
"s_add_u32 m0, s87, 135168\n" \
"ds_read_b64_tr_b16 v[4:5], v232 offset:8192\n" \
"v_mfma_f32_16x16x32_bf16 v[84:87], v[44:47], v[196:199], v[84:87]\n" \
"global_load_lds_dwordx4 v224, s[48:49] offset:-4096\n" \
"v_mfma_f32_16x16x32_bf16 v[88:91], v[48:51], v[192:195], v[88:91]\n" \
"ds_read_b64_tr_b16 v[6:7], v232 offset:10240\n" \
"v_mfma_f32_16x16x32_bf16 v[88:91], v[52:55], v[196:199], v[88:91]\n" \
"ds_read_b64_tr_b16 v[8:9], v233 offset:0\n" \
"v_mfma_f32_16x16x32_bf16 v[92:95], v[56:59], v[192:195], v[92:95]\n" \
"ds_read_b64_tr_b16 v[10:11], v233 offset:2048\n" \
"v_mfma_f32_16x16x32_bf16 v[92:95], v[60:63], v[196:199], v[92:95]\n" \
"global_load_lds_dwordx4 v225, s[48:49] offset:-3072\n" \
"v_mfma_f32_16x16x32_bf16 v[112:115], v[32:35], v[200:203], v[112:115]\n" \
"ds_read_b128 v[192:195], v230 offset:0\n" \
"v_mfma_f32_16x16x32_bf16 v[112:115], v[36:39], v[204:207], v[112:115]\n" \
"ds_read_b128 v[196:199], v231 offset:0\n" \
"v_mfma_f32_16x16x32_bf16 v[116:119], v[40:43], v[200:203], v[116:119]\n" \
"ds_read_b64_tr_b16 v[12:13], v233 offset:8192\n" \
"v_mfma_f32_16x16x32_bf16 v[116:119], v[44:47], v[204:207], v[116:119]\n" \
"global_load_lds_dwordx4 v226, s[48:49] offset:-2048\n" \
"v_mfma_f32_16x16x32_bf16 v[120:123], v[48:51], v[200:203], v[120:123]\n" \
"ds_read_b64_tr_b16 v[14:15], v233 offset:10240\n" \
"v_mfma_f32_16x16x32_bf16 v[120:123], v[52:55], v[204:207], v[120:123]\n" \
"ds_read_b64_tr_b16 v[16:17], v232 offset:512\n" \
"v_mfma_f32_16x16x32_bf16 v[124:127], v[56:59], v[200:203], v[124:127]\n" \
"ds_read_b64_tr_b16 v[18:19], v232 offset:2560\n" \
"v_mfma_f32_16x16x32_bf16 v[124:127], v[60:63], v[204:207], v[124:127]\n" \
"global_load_lds_dwordx4 v227, s[48:49] offset:-1024\n" \
"v_mfma_f32_16x16x32_bf16 v[144:147], v[32:35], v[208:211], v[144:147]\n" \
"s_add_u32 s48, s48, 0x80\n" \
"s_addc_u32 s49, s49, 0\n" \
"ds_read_b128 v[200:203], v230 offset:2048\n" \
"v_mfma_f32_16x16x32_bf16 v[144:147], v[36:39], v[212:215], v[144:147]\n" \
"ds_read_b128 v[204:207], v231 offset:2048\n" \
"v_mfma_f32_16x16x32_bf16 v[148:151], v[40:43], v[208:211], v[148:151]\n" \
"ds_read_b64_tr_b16 v[20:21], v232 offset:8704\n" \
"v_mfma_f32_16x16x32_bf16 v[148:151], v[44:47], v[212:215], v[148:151]\n" \
"global_load_lds_dwordx4 v224, s[46:47] offset:0\n" \
"v_mfma_f32_16x16x32_bf16 v[152:155], v[48:51], v[208:211], v[152:155]\n" \
"ds_read_b64_tr_b16 v[22:23], v232 offset:10752\n" \
"v_mfma_f32_16x16x32_bf16 v[152:155], v[52:55], v[212:215], v[152:155]\n" \
"ds_read_b64_tr_b16 v[24:25], v233 offset:512\n" \
"v_mfma_f32_16x16x32_bf16 v[156:159], v[56:59], v[208:211], v[156:159]\n" \
"ds_read_b64_tr_b16 v[26:27], v233 offset:2560\n" \
"v_mfma_f32_16x16x32_bf16 v[156:159], v[60:63], v[212:215], v[156:159]\n" \
"global_load_lds_dwordx4 v225, s[46:47] offset:1024\n" \
"v_mfma_f32_16x16x32_bf16 v[176:179], v[32:35], v[216:219], v[176:179]\n" \
"ds_read_b128 v[208:211], v230 offset:4096\n" \
"v_mfma_f32_16x16x32_bf16 v[176:179], v[36:39], v[220:223], v[176:179]\n" \
"ds_read_b128 v[212:215], v231 offset:4096\n" \
"v_mfma_f32_16x16x32_bf16 v[180:183], v[40:43], v[216:219], v[180:183]\n" \
"ds_read_b64_tr_b16 v[28:29], v233 offset:8704\n" \
"v_mfma_f32_16x16x32_bf16 v[180:183], v[44:47], v[220:223], v[180:183]\n" \
"global_load_lds_dwordx4 v226, s[46:47] offset:2048\n" \
"v_mfma_f32_16x16x32_bf16 v[184:187], v[48:51], v[216:219], v[184:187]\n" \
"ds_read_b64_tr_b16 v[30:31], v233 offset:10752\n" \
"v_mfma_f32_16x16x32_bf16 v[184:187], v[52:55], v[220:223], v[184:187]\n" \
"v_mfma_f32_16x16x32_bf16 v[188:191], v[56:59], v[216:219], v[188:191]\n" \
"v_mfma_f32_16x16x32_bf16 v[188:191], v[60:63], v[220:223], v[188:191]\n" \
"s_waitcnt lgkmcnt(1)\n" \
"global_load_lds_dwordx4 v227, s[46:47] offset:3072\n" \
"v_mfma_f32_16x16x32_bf16 v[64:67], v[0:3], v[192:195], v[64:67]\n" \
"s_add_u32 s46, s46, 0x80\n" \
"s_addc_u32 s47, s47, 0\n" \
"ds_read_b128 v[216:219], v230 offset:6144\n" \
"v_mfma_f32_16x16x32_bf16 v[64:67], v[4:7], v[196:199], v[64:67]\n" \
"ds_read_b128 v[220:223], v231 offset:6144\n" \
"v_mfma_f32_16x16x32_bf16 v[68:71], v[8:11], v[192:195], v[68:71]\n" \
"ds_read_b64_tr_b16 v[32:33], v232 offset:1024\n" \
"v_mfma_f32_16x16x32_bf16 v[68:71], v[12:15], v[196:199], v[68:71]\n" \
"buffer_store_dwordx4 a[152:155], v248, s[56:59], s90 offen offset:2048 sc1\n" \
"v_mfma_f32_16x16x32_bf16 v[72:75], v[16:19], v[192:195], v[72:75]\n" \
"ds_read_b64_tr_b16 v[34:35], v232 offset:3072\n" \
"v_mfma_f32_16x16x32_bf16 v[72:75], v[20:23], v[196:199], v[72:75]\n" \
"s_waitcnt lgkmcnt(3)\n" \
"ds_read_b64_tr_b16 v[36:37], v232 offset:9216\n" \
"v_mfma_f32_16x16x32_bf16 v[76:79], v[24:27], v[192:195], v[76:79]\n" \
"ds_read_b64_tr_b16 v[38:39], v232 offset:11264\n" \
"v_mfma_f32_16x16x32_bf16 v[76:79], v[28:31], v[196:199], v[76:79]\n" \
"v_mfma_f32_16x16x32_bf16 v[96:99], v[0:3], v[200:203], v[96:99]\n" \
"ds_read_b64_tr_b16 v[40:41], v233 offset:1024\n" \
"v_mfma_f32_16x16x32_bf16 v[96:99], v[4:7], v[204:207], v[96:99]\n" \
"ds_read_b64_tr_b16 v[42:43], v233 offset:3072\n" \
"v_mfma_f32_16x16x32_bf16 v[100:103], v[8:11], v[200:203], v[100:103]\n" \
"ds_read_b64_tr_b16 v[44:45], v233 offset:9216\n" \
"v_mfma_f32_16x16x32_bf16 v[100:103], v[12:15], v[204:207], v[100:103]\n" \
"v_mfma_f32_16x16x32_bf16 v[104:107], v[16:19], v[200:203], v[104:107]\n" \
"ds_read_b64_tr_b16 v[46:47], v233 offset:11264\n" \
"v_mfma_f32_16x16x32_bf16 v[104:107], v[20:23], v[204:207], v[104:107]\n" \
"ds_read_b64_tr_b16 v[48:49], v232 offset:1536\n" \
"v_mfma_f32_16x16x32_bf16 v[108:111], v[24:27], v[200:203], v[108:111]\n" \
"s_add_u32 m0, s95, 20480\n" \
"ds_read_b64_tr_b16 v[50:51], v232 offset:3584\n" \
"v_mfma_f32_16x16x32_bf16 v[108:111], v[28:31], v[204:207], v[108:111]\n" \
"global_load_lds_dwordx4 v228, s[44:45] offset:-4096\n" \
"v_mfma_f32_16x16x32_bf16 v[128:131], v[0:3], v[208:211], v[128:131]\n" \
"ds_read_b64_tr_b16 v[52:53], v232 offset:9728\n" \
"v_mfma_f32_16x16x32_bf16 v[128:131], v[4:7], v[212:215], v[128:131]\n" \
"ds_read_b64_tr_b16 v[54:55], v232 offset:11776\n" \
"v_mfma_f32_16x16x32_bf16 v[132:135], v[8:11], v[208:211], v[132:135]\n" \
"ds_read_b64_tr_b16 v[56:57], v233 offset:1536\n" \
"v_mfma_f32_16x16x32_bf16 v[132:135], v[12:15], v[212:215], v[132:135]\n" \
"global_load_lds_dwordx4 v229, s[44:45] offset:-3072\n" \
"v_mfma_f32_16x16x32_bf16 v[136:139], v[16:19], v[208:211], v[136:139]\n" \
"s_add_u32 s44, s44, 0x80000\n" \
"s_addc_u32 s45, s45, 0\n" \
"ds_read_b64_tr_b16 v[58:59], v233 offset:3584\n" \
"v_mfma_f32_16x16x32_bf16 v[136:139], v[20:23], v[212:215], v[136:139]\n" \
"ds_read_b64_tr_b16 v[60:61], v233 offset:9728\n" \
"v_mfma_f32_16x16x32_bf16 v[140:143], v[24:27], v[208:211], v[140:143]\n" \
"ds_read_b64_tr_b16 v[62:63], v233 offset:11776\n" \
"v_mfma_f32_16x16x32_bf16 v[140:143], v[28:31], v[212:215], v[140:143]\n" \
"s_waitcnt lgkmcnt(3)\n" \
"global_load_lds_dwordx4 v228, s[42:43] offset:-2048\n" \
"v_mfma_f32_16x16x32_bf16 v[160:163], v[0:3], v[216:219], v[160:163]\n" \
"v_add_u32 v230, 0x8000, v230\n" \
"v_add_u32 v231, 0x8000, v231\n" \
"v_mfma_f32_16x16x32_bf16 v[160:163], v[4:7], v[220:223], v[160:163]\n" \
"v_add_u32 v232, 0x4000, v232\n" \
"v_add_u32 v233, 0x4000, v233\n" \
"v_mfma_f32_16x16x32_bf16 v[164:167], v[8:11], v[216:219], v[164:167]\n" \
"v_mfma_f32_16x16x32_bf16 v[164:167], v[12:15], v[220:223], v[164:167]\n" \
"global_load_lds_dwordx4 v229, s[42:43] offset:-1024\n" \
"v_mfma_f32_16x16x32_bf16 v[168:171], v[16:19], v[216:219], v[168:171]\n" \
"s_add_u32 s42, s42, 0x80000\n" \
"s_addc_u32 s43, s43, 0\n" \
"v_mfma_f32_16x16x32_bf16 v[168:171], v[20:23], v[220:223], v[168:171]\n" \
"v_mfma_f32_16x16x32_bf16 v[172:175], v[24:27], v[216:219], v[172:175]\n" \
"s_waitcnt vmcnt(18) lgkmcnt(0)\n" \
"v_mfma_f32_16x16x32_bf16 v[172:175], v[28:31], v[220:223], v[172:175]\n" \
"s_barrier\n" \
"v_mfma_f32_16x16x32_bf16 v[80:83], v[32:35], v[192:195], v[80:83]\n" \
"ds_read_b64_tr_b16 v[0:1], v232 offset:0\n" \
"v_mfma_f32_16x16x32_bf16 v[80:83], v[36:39], v[196:199], v[80:83]\n" \
"ds_read_b64_tr_b16 v[2:3], v232 offset:2048\n" \
"v_mfma_f32_16x16x32_bf16 v[84:87], v[40:43], v[192:195], v[84:87]\n" \
"s_add_u32 m0, s87, 69632\n" \
"ds_read_b64_tr_b16 v[4:5], v232 offset:8192\n" \
"v_mfma_f32_16x16x32_bf16 v[84:87], v[44:47], v[196:199], v[84:87]\n" \
"global_load_lds_dwordx4 v224, s[48:49] offset:-4096\n" \
"v_mfma_f32_16x16x32_bf16 v[88:91], v[48:51], v[192:195], v[88:91]\n" \
"ds_read_b64_tr_b16 v[6:7], v232 offset:10240\n" \
"v_mfma_f32_16x16x32_bf16 v[88:91], v[52:55], v[196:199], v[88:91]\n" \
"ds_read_b64_tr_b16 v[8:9], v233 offset:0\n" \
"v_mfma_f32_16x16x32_bf16 v[92:95], v[56:59], v[192:195], v[92:95]\n" \
"ds_read_b64_tr_b16 v[10:11], v233 offset:2048\n" \
"v_mfma_f32_16x16x32_bf16 v[92:95], v[60:63], v[196:199], v[92:95]\n" \
"global_load_lds_dwordx4 v225, s[48:49] offset:-3072\n" \
"v_mfma_f32_16x16x32_bf16 v[112:115], v[32:35], v[200:203], v[112:115]\n" \
"ds_read_b128 v[192:195], v230 offset:0\n" \
"v_mfma_f32_16x16x32_bf16 v[112:115], v[36:39], v[204:207], v[112:115]\n" \
"ds_read_b128 v[196:199], v231 offset:0\n" \
"v_mfma_f32_16x16x32_bf16 v[116:119], v[40:43], v[200:203], v[116:119]\n" \
"ds_read_b64_tr_b16 v[12:13], v233 offset:8192\n" \
"v_mfma_f32_16x16x32_bf16 v[116:119], v[44:47], v[204:207], v[116:119]\n" \
"global_load_lds_dwordx4 v226, s[48:49] offset:-2048\n" \
"v_mfma_f32_16x16x32_bf16 v[120:123], v[48:51], v[200:203], v[120:123]\n" \
"ds_read_b64_tr_b16 v[14:15], v233 offset:10240\n" \
"v_mfma_f32_16x16x32_bf16 v[120:123], v[52:55], v[204:207], v[120:123]\n" \
"ds_read_b64_tr_b16 v[16:17], v232 offset:512\n" \
"v_mfma_f32_16x16x32_bf16 v[124:127], v[56:59], v[200:203], v[124:127]\n" \
"ds_read_b64_tr_b16 v[18:19], v232 offset:2560\n" \
"v_mfma_f32_16x16x32_bf16 v[124:127], v[60:63], v[204:207], v[124:127]\n" \
"global_load_lds_dwordx4 v227, s[48:49] offset:-1024\n" \
"v_mfma_f32_16x16x32_bf16 v[144:147], v[32:35], v[208:211], v[144:147]\n" \
"s_add_u32 s48, s48, 0x80\n" \
"s_addc_u32 s49, s49, 0\n" \
"ds_read_b128 v[200:203], v230 offset:2048\n" \
"v_mfma_f32_16x16x32_bf16 v[144:147], v[36:39], v[212:215], v[144:147]\n" \
"ds_read_b128 v[204:207], v231 offset:2048\n" \
"v_mfma_f32_16x16x32_bf16 v[148:151], v[40:43], v[208:211], v[148:151]\n" \
"ds_read_b64_tr_b16 v[20:21], v232 offset:8704\n" \
"v_mfma_f32_16x16x32_bf16 v[148:151], v[44:47], v[212:215], v[148:151]\n" \
"global_load_lds_dwordx4 v224, s[46:47] offset:0\n" \
"v_mfma_f32_16x16x32_bf16 v[152:155], v[48:51], v[208:211], v[152:155]\n" \
"ds_read_b64_tr_b16 v[22:23], v232 offset:10752\n" \
"v_mfma_f32_16x16x32_bf16 v[152:155], v[52:55], v[212:215], v[152:155]\n" \
"ds_read_b64_tr_b16 v[24:25], v233 offset:512\n" \
"v_mfma_f32_16x16x32_bf16 v[156:159], v[56:59], v[208:211], v[156:159]\n" \
"ds_read_b64_tr_b16 v[26:27], v233 offset:2560\n" \
"v_mfma_f32_16x16x32_bf16 v[156:159], v[60:63], v[212:215], v[156:159]\n" \
"global_load_lds_dwordx4 v225, s[46:47] offset:1024\n" \
"v_mfma_f32_16x16x32_bf16 v[176:179], v[32:35], v[216:219], v[176:179]\n" \
"ds_read_b128 v[208:211], v230 offset:4096\n" \
"v_mfma_f32_16x16x32_bf16 v[176:179], v[36:39], v[220:223], v[176:179]\n" \
"ds_read_b128 v[212:215], v231 offset:4096\n" \
"v_mfma_f32_16x16x32_bf16 v[180:183], v[40:43], v[216:219], v[180:183]\n" \
"ds_read_b64_tr_b16 v[28:29], v233 offset:8704\n" \
"v_mfma_f32_16x16x32_bf16 v[180:183], v[44:47], v[220:223], v[180:183]\n" \
"global_load_lds_dwordx4 v226, s[46:47] offset:2048\n" \
"v_mfma_f32_16x16x32_bf16 v[184:187], v[48:51], v[216:219], v[184:187]\n" \
"ds_read_b64_tr_b16 v[30:31], v233 offset:10752\n" \
"v_mfma_f32_16x16x32_bf16 v[184:187], v[52:55], v[220:223], v[184:187]\n" \
"v_mfma_f32_16x16x32_bf16 v[188:191], v[56:59], v[216:219], v[188:191]\n" \
"v_mfma_f32_16x16x32_bf16 v[188:191], v[60:63], v[220:223], v[188:191]\n" \
"s_waitcnt lgkmcnt(1)\n" \
"global_load_lds_dwordx4 v227, s[46:47] offset:3072\n" \
"v_mfma_f32_16x16x32_bf16 v[64:67], v[0:3], v[192:195], v[64:67]\n" \
"s_add_u32 s46, s46, 0x80\n" \
"s_addc_u32 s47, s47, 0\n" \
"ds_read_b128 v[216:219], v230 offset:6144\n" \
"v_mfma_f32_16x16x32_bf16 v[64:67], v[4:7], v[196:199], v[64:67]\n" \
"s_mul_i32 s77, s62, 557824\n" \
"ds_read_b128 v[220:223], v231 offset:6144\n" \
"s_add_u32 s77, s77, s95\n" \
"v_mfma_f32_16x16x32_bf16 v[68:71], v[8:11], v[192:195], v[68:71]\n" \
"s_add_u32 s36, s28, s77\n" \
"s_mov_b32 s37, s29\n" \
"ds_read_b64_tr_b16 v[32:33], v232 offset:1024\n" \
"v_mfma_f32_16x16x32_bf16 v[68:71], v[12:15], v[196:199], v[68:71]\n" \
"buffer_store_dwordx4 a[156:159], v248, s[56:59], s90 offen offset:3072 sc1\n" \
"s_add_u32 s77, s77, 17850368\n" \
"s_add_u32 s38, s28, s77\n" \
"s_mov_b32 s39, s29\n" \
"s_and_b32 s74, s63, 1\n" \
"v_mfma_f32_16x16x32_bf16 v[72:75], v[16:19], v[192:195], v[72:75]\n" \
"s_add_u32 s90, s88, 278528\n" \
"s_lshl_b32 s74, s74, 15\n" \
"ds_read_b64_tr_b16 v[34:35], v232 offset:3072\n" \
"v_mfma_f32_16x16x32_bf16 v[72:75], v[20:23], v[196:199], v[72:75]\n" \
"s_waitcnt lgkmcnt(3)\n" \
"s_xor_b32 s74, s74, 0x18000\n" \
"ds_read_b64_tr_b16 v[36:37], v232 offset:9216\n" \
"v_mfma_f32_16x16x32_bf16 v[76:79], v[24:27], v[192:195], v[76:79]\n" \
"ds_read_b64_tr_b16 v[38:39], v232 offset:11264\n" \
"v_add_u32 v247, s74, v248\n" \
"v_mfma_f32_16x16x32_bf16 v[76:79], v[28:31], v[196:199], v[76:79]\n" \
"buffer_store_dwordx4 a[64:67], v249, s[56:59], s90 offen offset:0 sc1\n" \
"s_add_u32 s74, s74, s95\n" \
"s_lshr_b32 s83, s61, 1\n" \
"s_lshl_b32 s75, s63, 2\n" \
"s_add_u32 s77, s83, s75\n" \
"v_mfma_f32_16x16x32_bf16 v[96:99], v[0:3], v[200:203], v[96:99]\n" \
"s_mul_i32 s77, s77, 266240\n" \
"s_sub_u32 s78, s83, s75\n" \
"ds_read_b64_tr_b16 v[40:41], v233 offset:1024\n" \
"v_mfma_f32_16x16x32_bf16 v[96:99], v[4:7], v[204:207], v[96:99]\n" \
"s_add_u32 s78, s78, 60\n" \
"s_mul_i32 s78, s78, 266240\n" \
"ds_read_b64_tr_b16 v[42:43], v233 offset:3072\n" \
"v_mfma_f32_16x16x32_bf16 v[100:103], v[8:11], v[200:203], v[100:103]\n" \
"s_and_b32 s82, s61, 1\n" \
"ds_read_b128 a[64:67], v254\n" \
"ds_read_b64_tr_b16 v[44:45], v233 offset:9216\n" \
"v_mfma_f32_16x16x32_bf16 v[100:103], v[12:15], v[204:207], v[100:103]\n" \
"buffer_load_dword v244, v245, s[56:59], s91 offen sc1\n" \
"v_mfma_f32_16x16x32_bf16 v[104:107], v[16:19], v[200:203], v[104:107]\n" \
"s_cmp_eq_u32 s82, 0\n" \
"s_cselect_b32 s16, 0, 0xff00\n" \
"ds_read_b64_tr_b16 v[46:47], v233 offset:11264\n" \
"v_mfma_f32_16x16x32_bf16 v[104:107], v[20:23], v[204:207], v[104:107]\n" \
"s_mov_b32 s17, s16\n" \
"s_lshl_b32 s75, s82, 8\n" \
"ds_read_b64_tr_b16 v[48:49], v232 offset:1536\n" \
"v_mfma_f32_16x16x32_bf16 v[108:111], v[24:27], v[200:203], v[108:111]\n" \
"s_add_u32 m0, s95, 36864\n" \
"s_lshl_b32 s76, s62, 13\n" \
"ds_read_b64_tr_b16 v[50:51], v232 offset:3584\n" \
"v_mfma_f32_16x16x32_bf16 v[108:111], v[28:31], v[204:207], v[108:111]\n" \
"s_add_u32 s75, s75, s76\n" \
"ds_read_b128 a[68:71], v254\n" \
"global_load_lds_dwordx4 v228, s[44:45] offset:-4096\n" \
"v_mfma_f32_16x16x32_bf16 v[128:131], v[0:3], v[208:211], v[128:131]\n" \
"s_add_u32 s75, s75, 35700736\n" \
"s_add_u32 s77, s77, s75\n" \
"ds_read_b64_tr_b16 v[52:53], v232 offset:9728\n" \
"v_mfma_f32_16x16x32_bf16 v[128:131], v[4:7], v[212:215], v[128:131]\n" \
"s_add_u32 s78, s78, s75\n" \
"s_mul_i32 s83, s83, 9216\n" \
"ds_read_b64_tr_b16 v[54:55], v232 offset:11776\n" \
"v_mfma_f32_16x16x32_bf16 v[132:135], v[8:11], v[208:211], v[132:135]\n" \
"s_lshl_b32 s75, s82, 12\n" \
"s_add_u32 s83, s83, s75\n" \
"ds_read_b64_tr_b16 v[56:57], v233 offset:1536\n" \
"v_mfma_f32_16x16x32_bf16 v[132:135], v[12:15], v[212:215], v[132:135]\n" \
"s_add_u32 s83, s83, 4096\n" \
"global_load_lds_dwordx4 v229, s[44:45] offset:-3072\n" \
"v_mfma_f32_16x16x32_bf16 v[136:139], v[16:19], v[208:211], v[136:139]\n" \
"s_add_u32 s44, s44, 0x80000\n" \
"s_addc_u32 s45, s45, 0\n" \
"ds_read_b64_tr_b16 v[58:59], v233 offset:3584\n" \
"v_mfma_f32_16x16x32_bf16 v[136:139], v[20:23], v[212:215], v[136:139]\n" \
"s_lshl_b32 s82, s82, 14\n" \
"ds_read_b64_tr_b16 v[60:61], v233 offset:9728\n" \
"v_mfma_f32_16x16x32_bf16 v[140:143], v[24:27], v[208:211], v[140:143]\n" \
"s_cmp_eq_u32 s63, 7\n" \
"s_cselect_b32 s82, 0, s82\n" \
"ds_read_b64_tr_b16 v[62:63], v233 offset:11776\n" \
"v_mfma_f32_16x16x32_bf16 v[140:143], v[28:31], v[212:215], v[140:143]\n" \
"s_waitcnt lgkmcnt(3)\n" \
"global_load_lds_dwordx4 v228, s[42:43] offset:-2048\n" \
"s_mov_b32 s75, s63\n" \
"v_mfma_f32_16x16x32_bf16 v[160:163], v[0:3], v[216:219], v[160:163]\n" \
"s_sub_u32 s76, 7, s63\n" \
"s_mov_b32 s73, s95\n" \
"s_mov_b64 s[40:41], s[14:15]\n" \
"v_mfma_f32_16x16x32_bf16 v[160:163], v[4:7], v[220:223], v[160:163]\n" \
"s_mov_b32 s80, 0\n" \
"v_add_u32 v230, 0x8000, v230\n" \
"s_mov_b32 s81, 0\n" \
"v_mfma_f32_16x16x32_bf16 v[164:167], v[8:11], v[216:219], v[164:167]\n" \
"s_cmp_eq_u32 s63, 7\n" \
"s_cselect_b64 s[40:41], s[18:19], s[40:41]\n" \
"s_cselect_b32 s80, 12, s80\n" \
"v_mfma_f32_16x16x32_bf16 v[164:167], v[12:15], v[220:223], v[164:167]\n" \
"global_load_lds_dwordx4 v229, s[42:43] offset:-1024\n" \
"v_add_u32 v231, 0x8000, v231\n" \
"v_mfma_f32_16x16x32_bf16 v[168:171], v[16:19], v[216:219], v[168:171]\n" \
"s_add_u32 s42, s42, 0x80000\n" \
"s_addc_u32 s43, s43, 0\n" \
"v_add_u32 v232, 0xffff4000, v232\n" \
"v_mfma_f32_16x16x32_bf16 v[168:171], v[20:23], v[220:223], v[168:171]\n" \
"s_cmp_eq_u32 s63, 6\n" \
"s_cselect_b64 s[40:41], s[22:23], s[40:41]\n" \
"s_cselect_b32 s80, 12, s80\n" \
"v_mfma_f32_16x16x32_bf16 v[172:175], v[24:27], v[216:219], v[172:175]\n" \
"s_waitcnt vmcnt(20) lgkmcnt(0)\n" \
"v_add_u32 v233, 0xffff4000, v233\n" \
"v_mfma_f32_16x16x32_bf16 v[172:175], v[28:31], v[220:223], v[172:175]\n" \
"s_barrier\n" \
"v_mfma_f32_16x16x32_bf16 v[80:83], v[32:35], v[192:195], v[80:83]\n" \
"ds_read_b64_tr_b16 v[0:1], v232 offset:0\n" \
"v_mfma_f32_16x16x32_bf16 v[80:83], v[36:39], v[196:199], v[80:83]\n" \
"ds_read_b64_tr_b16 v[2:3], v232 offset:2048\n" \
"v_mfma_f32_16x16x32_bf16 v[84:87], v[40:43], v[192:195], v[84:87]\n" \
"s_add_u32 m0, s87, 102400\n" \
"ds_read_b64_tr_b16 v[4:5], v232 offset:8192\n" \
"v_mfma_f32_16x16x32_bf16 v[84:87], v[44:47], v[196:199], v[84:87]\n" \
"global_load_lds_dwordx4 v224, s[48:49] offset:-4096\n" \
"v_mfma_f32_16x16x32_bf16 v[88:91], v[48:51], v[192:195], v[88:91]\n" \
"ds_read_b64_tr_b16 v[6:7], v232 offset:10240\n" \
"v_mfma_f32_16x16x32_bf16 v[88:91], v[52:55], v[196:199], v[88:91]\n" \
"ds_read_b64_tr_b16 v[8:9], v233 offset:0\n" \
"v_mfma_f32_16x16x32_bf16 v[92:95], v[56:59], v[192:195], v[92:95]\n" \
"ds_read_b64_tr_b16 v[10:11], v233 offset:2048\n" \
"v_mfma_f32_16x16x32_bf16 v[92:95], v[60:63], v[196:199], v[92:95]\n" \
"global_load_lds_dwordx4 v225, s[48:49] offset:-3072\n" \
"v_mfma_f32_16x16x32_bf16 v[112:115], v[32:35], v[200:203], v[112:115]\n" \
"ds_read_b128 v[192:195], v230 offset:0\n" \
"v_mfma_f32_16x16x32_bf16 v[112:115], v[36:39], v[204:207], v[112:115]\n" \
"ds_read_b128 v[196:199], v231 offset:0\n" \
"v_mfma_f32_16x16x32_bf16 v[116:119], v[40:43], v[200:203], v[116:119]\n" \
"ds_read_b64_tr_b16 v[12:13], v233 offset:8192\n" \
"v_mfma_f32_16x16x32_bf16 v[116:119], v[44:47], v[204:207], v[116:119]\n" \
"global_load_lds_dwordx4 v226, s[48:49] offset:-2048\n" \
"v_mfma_f32_16x16x32_bf16 v[120:123], v[48:51], v[200:203], v[120:123]\n" \
"ds_read_b64_tr_b16 v[14:15], v233 offset:10240\n" \
"v_mfma_f32_16x16x32_bf16 v[120:123], v[52:55], v[204:207], v[120:123]\n" \
"ds_read_b64_tr_b16 v[16:17], v232 offset:512\n" \
"v_mfma_f32_16x16x32_bf16 v[124:127], v[56:59], v[200:203], v[124:127]\n" \
"ds_read_b64_tr_b16 v[18:19], v232 offset:2560\n" \
"v_mfma_f32_16x16x32_bf16 v[124:127], v[60:63], v[204:207], v[124:127]\n" \
"global_load_lds_dwordx4 v227, s[48:49] offset:-1024\n" \
"v_mfma_f32_16x16x32_bf16 v[144:147], v[32:35], v[208:211], v[144:147]\n" \
"s_add_u32 s48, s48, 0x80\n" \
"s_addc_u32 s49, s49, 0\n" \
"ds_read_b128 v[200:203], v230 offset:2048\n" \
"v_mfma_f32_16x16x32_bf16 v[144:147], v[36:39], v[212:215], v[144:147]\n" \
"ds_read_b128 v[204:207], v231 offset:2048\n" \
"v_mfma_f32_16x16x32_bf16 v[148:151], v[40:43], v[208:211], v[148:151]\n" \
"ds_read_b64_tr_b16 v[20:21], v232 offset:8704\n" \
"v_mfma_f32_16x16x32_bf16 v[148:151], v[44:47], v[212:215], v[148:151]\n" \
"global_load_lds_dwordx4 v224, s[46:47] offset:0\n" \
"v_mfma_f32_16x16x32_bf16 v[152:155], v[48:51], v[208:211], v[152:155]\n" \
"ds_read_b64_tr_b16 v[22:23], v232 offset:10752\n" \
"v_mfma_f32_16x16x32_bf16 v[152:155], v[52:55], v[212:215], v[152:155]\n" \
"ds_read_b64_tr_b16 v[24:25], v233 offset:512\n" \
"v_mfma_f32_16x16x32_bf16 v[156:159], v[56:59], v[208:211], v[156:159]\n" \
"ds_read_b64_tr_b16 v[26:27], v233 offset:2560\n" \
"v_mfma_f32_16x16x32_bf16 v[156:159], v[60:63], v[212:215], v[156:159]\n" \
"global_load_lds_dwordx4 v225, s[46:47] offset:1024\n" \
"Lpg4400_%=:\n" \
"s_cbranch_execz Lpg5584_%=\n" \
"v_mfma_f32_16x16x32_bf16 v[176:179], v[32:35], v[216:219], v[176:179]\n" \
"ds_read_b128 v[208:211], v230 offset:4096\n" \
"v_mfma_f32_16x16x32_bf16 v[176:179], v[36:39], v[220:223], v[176:179]\n" \
"ds_read_b128 v[212:215], v231 offset:4096\n" \
"v_mfma_f32_16x16x32_bf16 v[180:183], v[40:43], v[216:219], v[180:183]\n" \
"ds_read_b64_tr_b16 v[28:29], v233 offset:8704\n" \
"v_mfma_f32_16x16x32_bf16 v[180:183], v[44:47], v[220:223], v[180:183]\n" \
"global_load_lds_dwordx4 v226, s[46:47] offset:2048\n" \
"v_mfma_f32_16x16x32_bf16 v[184:187], v[48:51], v[216:219], v[184:187]\n" \
"ds_read_b64_tr_b16 v[30:31], v233 offset:10752\n" \
"v_mfma_f32_16x16x32_bf16 v[184:187], v[52:55], v[220:223], v[184:187]\n" \
"s_cmp_eq_u32 s63, 5\n" \
"s_cselect_b64 s[40:41], s[18:19], s[40:41]\n" \
"s_cselect_b32 s80, 4, s80\n" \
"v_mfma_f32_16x16x32_bf16 v[188:191], v[56:59], v[216:219], v[188:191]\n" \
"s_cmp_eq_u32 s63, 5\n" \
"s_cselect_b32 s81, 384, s81\n" \
"v_mfma_f32_16x16x32_bf16 v[188:191], v[60:63], v[220:223], v[188:191]\n" \
"s_waitcnt lgkmcnt(1)\n" \
"global_load_lds_dwordx4 v227, s[46:47] offset:3072\n" \
"v_mfma_f32_16x16x32_bf16 v[64:67], v[0:3], v[192:195], v[64:67]\n" \
"s_add_u32 s46, s46, 0x80\n" \
"s_addc_u32 s47, s47, 0\n" \
"ds_read_b128 v[216:219], v230 offset:6144\n" \
"v_mfma_f32_16x16x32_bf16 v[64:67], v[4:7], v[196:199], v[64:67]\n" \
"ds_read_b128 v[220:223], v231 offset:6144\n" \
"v_mfma_f32_16x16x32_bf16 v[68:71], v[8:11], v[192:195], v[68:71]\n" \
"ds_read_b64_tr_b16 v[32:33], v232 offset:1024\n" \
"v_mfma_f32_16x16x32_bf16 v[68:71], v[12:15], v[196:199], v[68:71]\n" \
"buffer_store_dwordx4 a[96:99], v249, s[56:59], s90 offen offset:256 sc1\n" \
"s_cmp_eq_u32 s63, 4\n" \
"s_cselect_b64 s[40:41], s[20:21], s[40:41]\n" \
"s_cselect_b32 s80, 8, s80\n" \
"v_mfma_f32_16x16x32_bf16 v[72:75], v[16:19], v[192:195], v[72:75]\n" \
"ds_read_b64_tr_b16 v[34:35], v232 offset:3072\n" \
"v_mfma_f32_16x16x32_bf16 v[72:75], v[20:23], v[196:199], v[72:75]\n" \
"s_waitcnt lgkmcnt(3)\n" \
"ds_read_b64_tr_b16 v[36:37], v232 offset:9216\n" \
"v_mfma_f32_16x16x32_bf16 v[76:79], v[24:27], v[192:195], v[76:79]\n" \
"ds_read_b128 a[96:99], v254\n" \
"ds_read_b64_tr_b16 v[38:39], v232 offset:11264\n" \
"v_mfma_f32_16x16x32_bf16 v[76:79], v[28:31], v[196:199], v[76:79]\n" \
"buffer_store_dwordx4 a[72:75], v249, s[56:59], s90 offen offset:2048 sc1\n" \
"s_cmp_eq_u32 s63, 2\n" \
"s_cselect_b64 s[40:41], s[24:25], s[40:41]\n" \
"s_cselect_b32 s80, 4, s80\n" \
"s_add_u32 s81, s81, s62\n" \
"v_mfma_f32_16x16x32_bf16 v[96:99], v[0:3], v[200:203], v[96:99]\n" \
"s_lshl_b32 s81, s81, 2\n" \
"s_add_u32 s81, s81, s61\n" \
"ds_read_b64_tr_b16 v[40:41], v233 offset:1024\n" \
"v_mfma_f32_16x16x32_bf16 v[96:99], v[4:7], v[204:207], v[96:99]\n" \
"s_lshl_b32 s81, s81, 12\n" \
"ds_read_b128 a[100:103], v254\n" \
"ds_read_b64_tr_b16 v[42:43], v233 offset:3072\n" \
"v_mfma_f32_16x16x32_bf16 v[100:103], v[8:11], v[200:203], v[100:103]\n" \
"s_add_u32 s40, s40, s81\n" \
"s_addc_u32 s41, s41, 0\n" \
"ds_read_b64_tr_b16 v[44:45], v233 offset:9216\n" \
"v_mfma_f32_16x16x32_bf16 v[100:103], v[12:15], v[204:207], v[100:103]\n" \
"s_min_u32 s81, s80, 1\n" \
"s_lshl_b32 s81, s81, 2\n" \
"v_mul_u32_u24 v252, s81, v248\n" \
"v_mfma_f32_16x16x32_bf16 v[104:107], v[16:19], v[200:203], v[104:107]\n" \
"ds_read_b128 a[72:75], v254\n" \
"ds_read_b64_tr_b16 v[46:47], v233 offset:11264\n" \
"v_mfma_f32_16x16x32_bf16 v[104:107], v[20:23], v[204:207], v[104:107]\n" \
"ds_read_b128 a[76:79], v254\n" \
"ds_read_b64_tr_b16 v[48:49], v232 offset:1536\n" \
"v_mfma_f32_16x16x32_bf16 v[108:111], v[24:27], v[200:203], v[108:111]\n" \
"s_add_u32 m0, s95, 53248\n" \
"ds_read_b64_tr_b16 v[50:51], v232 offset:3584\n" \
"v_mfma_f32_16x16x32_bf16 v[108:111], v[28:31], v[204:207], v[108:111]\n" \
"global_load_lds_dwordx4 v228, s[44:45] offset:-4096\n" \
"v_mfma_f32_16x16x32_bf16 v[128:131], v[0:3], v[208:211], v[128:131]\n" \
"ds_read_b64_tr_b16 v[52:53], v232 offset:9728\n" \
"v_mfma_f32_16x16x32_bf16 v[128:131], v[4:7], v[212:215], v[128:131]\n" \
"ds_read_b64_tr_b16 v[54:55], v232 offset:11776\n" \
"v_mfma_f32_16x16x32_bf16 v[132:135], v[8:11], v[208:211], v[132:135]\n" \
"ds_read_b64_tr_b16 v[56:57], v233 offset:1536\n" \
"v_mfma_f32_16x16x32_bf16 v[132:135], v[12:15], v[212:215], v[132:135]\n" \
"global_load_lds_dwordx4 v229, s[44:45] offset:-3072\n" \
"v_mfma_f32_16x16x32_bf16 v[136:139], v[16:19], v[208:211], v[136:139]\n" \
"ds_read_b64_tr_b16 v[58:59], v233 offset:3584\n" \
"v_mfma_f32_16x16x32_bf16 v[136:139], v[20:23], v[212:215], v[136:139]\n" \
"ds_read_b64_tr_b16 v[60:61], v233 offset:9728\n" \
"v_mfma_f32_16x16x32_bf16 v[140:143], v[24:27], v[208:211], v[140:143]\n" \
"ds_read_b64_tr_b16 v[62:63], v233 offset:11776\n" \
"v_mfma_f32_16x16x32_bf16 v[140:143], v[28:31], v[212:215], v[140:143]\n" \
"s_waitcnt lgkmcnt(3)\n" \
"global_load_lds_dwordx4 v228, s[42:43] offset:-2048\n" \
"v_mfma_f32_16x16x32_bf16 v[160:163], v[0:3], v[216:219], v[160:163]\n" \
"v_add_u32 v230, 0xffff0000, v230\n" \
"v_add_u32 v231, 0xffff0000, v231\n" \
"v_mfma_f32_16x16x32_bf16 v[160:163], v[4:7], v[220:223], v[160:163]\n" \
"v_add_u32 v232, 0x4000, v232\n" \
"v_add_u32 v233, 0x4000, v233\n" \
"v_mfma_f32_16x16x32_bf16 v[164:167], v[8:11], v[216:219], v[164:167]\n" \
"v_mfma_f32_16x16x32_bf16 v[164:167], v[12:15], v[220:223], v[164:167]\n" \
"global_load_lds_dwordx4 v229, s[42:43] offset:-1024\n" \
"v_mfma_f32_16x16x32_bf16 v[168:171], v[16:19], v[216:219], v[168:171]\n" \
"v_mfma_f32_16x16x32_bf16 v[168:171], v[20:23], v[220:223], v[168:171]\n" \
"v_mfma_f32_16x16x32_bf16 v[172:175], v[24:27], v[216:219], v[172:175]\n" \
"s_waitcnt vmcnt(21) lgkmcnt(0)\n" \
"v_mfma_f32_16x16x32_bf16 v[172:175], v[28:31], v[220:223], v[172:175]\n" \
"s_barrier\n" \
"v_mfma_f32_16x16x32_bf16 v[80:83], v[32:35], v[192:195], v[80:83]\n" \
"s_waitcnt vmcnt(18)\n" \
"ds_read_b64_tr_b16 v[0:1], v232 offset:0\n" \
"v_cmp_ne_u32 vcc, s60, v244\n" \
"v_mfma_f32_16x16x32_bf16 v[80:83], v[36:39], v[196:199], v[80:83]\n" \
"ds_read_b64_tr_b16 v[2:3], v232 offset:2048\n" \
"v_mfma_f32_16x16x32_bf16 v[84:87], v[40:43], v[192:195], v[84:87]\n" \
"s_add_u32 m0, s87, 135168\n" \
"ds_read_b64_tr_b16 v[4:5], v232 offset:8192\n" \
"v_mfma_f32_16x16x32_bf16 v[84:87], v[44:47], v[196:199], v[84:87]\n" \
"s_and_b32 vcc_hi, vcc_lo, 255\n" \
"s_cbranch_scc1 Lpl1_%=\n" \
"Lpr1_%=:\n" \
"global_load_lds_dwordx4 v224, s[48:49] offset:-4096\n" \
"v_mfma_f32_16x16x32_bf16 v[88:91], v[48:51], v[192:195], v[88:91]\n" \
"s_add_u32 s79, s91, 12288\n" \
"ds_read_b64_tr_b16 v[6:7], v232 offset:10240\n" \
"v_mfma_f32_16x16x32_bf16 v[88:91], v[52:55], v[196:199], v[88:91]\n" \
"ds_read_b64_tr_b16 v[8:9], v233 offset:0\n" \
"v_mfma_f32_16x16x32_bf16 v[92:95], v[56:59], v[192:195], v[92:95]\n" \
"ds_read_b64_tr_b16 v[10:11], v233 offset:2048\n" \
"v_mfma_f32_16x16x32_bf16 v[92:95], v[60:63], v[196:199], v[92:95]\n" \
"global_load_lds_dwordx4 v225, s[48:49] offset:-3072\n" \
"v_mfma_f32_16x16x32_bf16 v[112:115], v[32:35], v[200:203], v[112:115]\n" \
"ds_read_b128 v[192:195], v230 offset:0\n" \
"v_mfma_f32_16x16x32_bf16 v[112:115], v[36:39], v[204:207], v[112:115]\n" \
"ds_read_b128 v[196:199], v231 offset:0\n" \
"v_mfma_f32_16x16x32_bf16 v[116:119], v[40:43], v[200:203], v[116:119]\n" \
"ds_read_b64_tr_b16 v[12:13], v233 offset:8192\n" \
"v_mfma_f32_16x16x32_bf16 v[116:119], v[44:47], v[204:207], v[116:119]\n" \
"global_load_lds_dwordx4 v226, s[48:49] offset:-2048\n" \
"v_mfma_f32_16x16x32_bf16 v[120:123], v[48:51], v[200:203], v[120:123]\n" \
"ds_read_b64_tr_b16 v[14:15], v233 offset:10240\n" \
"v_mfma_f32_16x16x32_bf16 v[120:123], v[52:55], v[204:207], v[120:123]\n" \
"ds_read_b64_tr_b16 v[16:17], v232 offset:512\n" \
"v_mfma_f32_16x16x32_bf16 v[124:127], v[56:59], v[200:203], v[124:127]\n" \
"ds_read_b64_tr_b16 v[18:19], v232 offset:2560\n" \
"v_mfma_f32_16x16x32_bf16 v[124:127], v[60:63], v[204:207], v[124:127]\n" \
"global_load_lds_dwordx4 v227, s[48:49] offset:-1024\n" \
"v_mfma_f32_16x16x32_bf16 v[144:147], v[32:35], v[208:211], v[144:147]\n" \
"ds_read_b128 v[200:203], v230 offset:2048\n" \
"v_mfma_f32_16x16x32_bf16 v[144:147], v[36:39], v[212:215], v[144:147]\n" \
"ds_read_b128 v[204:207], v231 offset:2048\n" \
"v_mfma_f32_16x16x32_bf16 v[148:151], v[40:43], v[208:211], v[148:151]\n" \
"ds_read_b64_tr_b16 v[20:21], v232 offset:8704\n" \
"v_mfma_f32_16x16x32_bf16 v[148:151], v[44:47], v[212:215], v[148:151]\n" \
"global_load_lds_dwordx4 v224, s[46:47] offset:0\n" \
"v_mfma_f32_16x16x32_bf16 v[152:155], v[48:51], v[208:211], v[152:155]\n" \
"ds_read_b64_tr_b16 v[22:23], v232 offset:10752\n" \
"v_mfma_f32_16x16x32_bf16 v[152:155], v[52:55], v[212:215], v[152:155]\n" \
"ds_read_b64_tr_b16 v[24:25], v233 offset:512\n" \
"v_mfma_f32_16x16x32_bf16 v[156:159], v[56:59], v[208:211], v[156:159]\n" \
"ds_read_b64_tr_b16 v[26:27], v233 offset:2560\n" \
"v_mfma_f32_16x16x32_bf16 v[156:159], v[60:63], v[212:215], v[156:159]\n" \
"global_load_lds_dwordx4 v225, s[46:47] offset:1024\n" \
"v_mfma_f32_16x16x32_bf16 v[176:179], v[32:35], v[216:219], v[176:179]\n" \
"ds_read_b128 v[208:211], v230 offset:4096\n" \
"v_mfma_f32_16x16x32_bf16 v[176:179], v[36:39], v[220:223], v[176:179]\n" \
"ds_read_b128 v[212:215], v231 offset:4096\n" \
"v_mfma_f32_16x16x32_bf16 v[180:183], v[40:43], v[216:219], v[180:183]\n" \
"ds_read_b64_tr_b16 v[28:29], v233 offset:8704\n" \
"v_mfma_f32_16x16x32_bf16 v[180:183], v[44:47], v[220:223], v[180:183]\n" \
"global_load_lds_dwordx4 v226, s[46:47] offset:2048\n" \
"v_mfma_f32_16x16x32_bf16 v[184:187], v[48:51], v[216:219], v[184:187]\n" \
"ds_read_b64_tr_b16 v[30:31], v233 offset:10752\n" \
"v_mfma_f32_16x16x32_bf16 v[184:187], v[52:55], v[220:223], v[184:187]\n" \
"v_mfma_f32_16x16x32_bf16 v[188:191], v[56:59], v[216:219], v[188:191]\n" \
"v_mfma_f32_16x16x32_bf16 v[188:191], v[60:63], v[220:223], v[188:191]\n" \
"s_waitcnt lgkmcnt(1)\n" \
"global_load_lds_dwordx4 v227, s[46:47] offset:3072\n" \
"v_mfma_f32_16x16x32_bf16 v[64:67], v[0:3], v[192:195], v[64:67]\n" \
"ds_read_b128 v[216:219], v230 offset:6144\n" \
"v_mfma_f32_16x16x32_bf16 v[64:67], v[4:7], v[196:199], v[64:67]\n" \
"ds_read_b128 v[220:223], v231 offset:6144\n" \
"v_mfma_f32_16x16x32_bf16 v[68:71], v[8:11], v[192:195], v[68:71]\n" \
"ds_read_b64_tr_b16 v[32:33], v232 offset:1024\n" \
"v_mfma_f32_16x16x32_bf16 v[68:71], v[12:15], v[196:199], v[68:71]\n" \
"buffer_store_dwordx4 a[104:107], v249, s[56:59], s90 offen offset:2304 sc1\n" \
"v_mfma_f32_16x16x32_bf16 v[72:75], v[16:19], v[192:195], v[72:75]\n" \
"s_add_u32 s90, s89, 278528\n" \
"ds_read_b64_tr_b16 v[34:35], v232 offset:3072\n" \
"v_mfma_f32_16x16x32_bf16 v[72:75], v[20:23], v[196:199], v[72:75]\n" \
"s_waitcnt lgkmcnt(3)\n" \
"ds_read_b64_tr_b16 v[36:37], v232 offset:9216\n" \
"v_mfma_f32_16x16x32_bf16 v[76:79], v[24:27], v[192:195], v[76:79]\n" \
"ds_read_b128 a[104:107], v254\n" \
"ds_read_b64_tr_b16 v[38:39], v232 offset:11264\n" \
"v_mfma_f32_16x16x32_bf16 v[76:79], v[28:31], v[196:199], v[76:79]\n" \
"buffer_store_dwordx4 a[80:83], v248, s[56:59], s90 offen offset:0 sc1\n" \
"ds_read_b128 a[108:111], v254\n" \
"v_mfma_f32_16x16x32_bf16 v[96:99], v[0:3], v[200:203], v[96:99]\n" \
"ds_read_b64_tr_b16 v[40:41], v233 offset:1024\n" \
"v_mfma_f32_16x16x32_bf16 v[96:99], v[4:7], v[204:207], v[96:99]\n" \
"ds_read_b64_tr_b16 v[42:43], v233 offset:3072\n" \
"v_mfma_f32_16x16x32_bf16 v[100:103], v[8:11], v[200:203], v[100:103]\n" \
"ds_read_b128 a[80:83], v254\n" \
"ds_read_b64_tr_b16 v[44:45], v233 offset:9216\n" \
"v_mfma_f32_16x16x32_bf16 v[100:103], v[12:15], v[204:207], v[100:103]\n" \
"buffer_store_dwordx4 a[84:87], v248, s[56:59], s90 offen offset:1024 sc1\n" \
"ds_read_b128 a[112:115], v254\n" \
"v_mfma_f32_16x16x32_bf16 v[104:107], v[16:19], v[200:203], v[104:107]\n" \
"ds_read_b64_tr_b16 v[46:47], v233 offset:11264\n" \
"v_mfma_f32_16x16x32_bf16 v[104:107], v[20:23], v[204:207], v[104:107]\n" \
"ds_read_b64_tr_b16 v[48:49], v232 offset:1536\n" \
"v_mfma_f32_16x16x32_bf16 v[108:111], v[24:27], v[200:203], v[108:111]\n" \
"s_add_u32 m0, s95, 0\n" \
"ds_read_b128 a[84:87], v254\n" \
"ds_read_b64_tr_b16 v[50:51], v232 offset:3584\n" \
"v_mfma_f32_16x16x32_bf16 v[108:111], v[28:31], v[204:207], v[108:111]\n" \
"ds_read_b128 a[116:119], v254\n" \
"global_load_lds_dwordx4 v250, s[36:37] offset:0 sc1\n" \
"v_mfma_f32_16x16x32_bf16 v[128:131], v[0:3], v[208:211], v[128:131]\n" \
"ds_read_b64_tr_b16 v[52:53], v232 offset:9728\n" \
"v_mfma_f32_16x16x32_bf16 v[128:131], v[4:7], v[212:215], v[128:131]\n" \
"ds_read_b64_tr_b16 v[54:55], v232 offset:11776\n" \
"v_mfma_f32_16x16x32_bf16 v[132:135], v[8:11], v[208:211], v[132:135]\n" \
"ds_read_b64_tr_b16 v[56:57], v233 offset:1536\n" \
"v_mfma_f32_16x16x32_bf16 v[132:135], v[12:15], v[212:215], v[132:135]\n" \
"global_load_lds_dwordx4 v250, s[36:37] offset:1024 sc1\n" \
"v_mfma_f32_16x16x32_bf16 v[136:139], v[16:19], v[208:211], v[136:139]\n" \
"ds_read_b64_tr_b16 v[58:59], v233 offset:3584\n" \
"v_mfma_f32_16x16x32_bf16 v[136:139], v[20:23], v[212:215], v[136:139]\n" \
"ds_read_b64_tr_b16 v[60:61], v233 offset:9728\n" \
"v_mfma_f32_16x16x32_bf16 v[140:143], v[24:27], v[208:211], v[140:143]\n" \
"ds_read_b64_tr_b16 v[62:63], v233 offset:11776\n" \
"v_mfma_f32_16x16x32_bf16 v[140:143], v[28:31], v[212:215], v[140:143]\n" \
"s_waitcnt lgkmcnt(3)\n" \
"global_load_lds_dwordx4 v250, s[36:37] offset:2048 sc1\n" \
"v_mfma_f32_16x16x32_bf16 v[160:163], v[0:3], v[216:219], v[160:163]\n" \
"v_add_u32 v230, 0x8000, v230\n" \
"v_add_u32 v231, 0x8000, v231\n" \
"v_mfma_f32_16x16x32_bf16 v[160:163], v[4:7], v[220:223], v[160:163]\n" \
"v_add_u32 v232, 0x4000, v232\n" \
"v_add_u32 v233, 0x4000, v233\n" \
"v_mfma_f32_16x16x32_bf16 v[164:167], v[8:11], v[216:219], v[164:167]\n" \
"v_mfma_f32_16x16x32_bf16 v[164:167], v[12:15], v[220:223], v[164:167]\n" \
"global_load_lds_dwordx4 v250, s[36:37] offset:3072 sc1\n" \
"v_mfma_f32_16x16x32_bf16 v[168:171], v[16:19], v[216:219], v[168:171]\n" \
"s_add_u32 s36, s36, 17408\n" \
"v_mfma_f32_16x16x32_bf16 v[168:171], v[20:23], v[220:223], v[168:171]\n" \
"v_mfma_f32_16x16x32_bf16 v[172:175], v[24:27], v[216:219], v[172:175]\n" \
"s_waitcnt vmcnt(21) lgkmcnt(0)\n" \
"v_mfma_f32_16x16x32_bf16 v[172:175], v[28:31], v[220:223], v[172:175]\n" \
"s_barrier\n" \
"v_mfma_f32_16x16x32_bf16 v[80:83], v[32:35], v[192:195], v[80:83]\n" \
"ds_read_b64_tr_b16 v[0:1], v232 offset:0\n" \
"v_mfma_f32_16x16x32_bf16 v[80:83], v[36:39], v[196:199], v[80:83]\n" \
"ds_read_b64_tr_b16 v[2:3], v232 offset:2048\n" \
"v_mfma_f32_16x16x32_bf16 v[84:87], v[40:43], v[192:195], v[84:87]\n" \
"s_add_u32 m0, s95, 16384\n" \
"ds_read_b64_tr_b16 v[4:5], v232 offset:8192\n" \
"v_mfma_f32_16x16x32_bf16 v[84:87], v[44:47], v[196:199], v[84:87]\n" \
"global_load_lds_dwordx4 v250, s[36:37] offset:0 sc1\n" \
"v_mfma_f32_16x16x32_bf16 v[88:91], v[48:51], v[192:195], v[88:91]\n" \
"ds_read_b64_tr_b16 v[6:7], v232 offset:10240\n" \
"v_mfma_f32_16x16x32_bf16 v[88:91], v[52:55], v[196:199], v[88:91]\n" \
"ds_read_b64_tr_b16 v[8:9], v233 offset:0\n" \
"v_mfma_f32_16x16x32_bf16 v[92:95], v[56:59], v[192:195], v[92:95]\n" \
"ds_read_b64_tr_b16 v[10:11], v233 offset:2048\n" \
"v_mfma_f32_16x16x32_bf16 v[92:95], v[60:63], v[196:199], v[92:95]\n" \
"global_load_lds_dwordx4 v250, s[36:37] offset:1024 sc1\n" \
"v_mfma_f32_16x16x32_bf16 v[112:115], v[32:35], v[200:203], v[112:115]\n" \
"ds_read_b128 v[192:195], v230 offset:0\n" \
"v_mfma_f32_16x16x32_bf16 v[112:115], v[36:39], v[204:207], v[112:115]\n" \
"ds_read_b128 v[196:199], v231 offset:0\n" \
"v_mfma_f32_16x16x32_bf16 v[116:119], v[40:43], v[200:203], v[116:119]\n" \
"ds_read_b64_tr_b16 v[12:13], v233 offset:8192\n" \
"v_mfma_f32_16x16x32_bf16 v[116:119], v[44:47], v[204:207], v[116:119]\n" \
"global_load_lds_dwordx4 v250, s[36:37] offset:2048 sc1\n" \
"v_mfma_f32_16x16x32_bf16 v[120:123], v[48:51], v[200:203], v[120:123]\n" \
"ds_read_b64_tr_b16 v[14:15], v233 offset:10240\n" \
"v_mfma_f32_16x16x32_bf16 v[120:123], v[52:55], v[204:207], v[120:123]\n" \
"ds_read_b64_tr_b16 v[16:17], v232 offset:512\n" \
"v_mfma_f32_16x16x32_bf16 v[124:127], v[56:59], v[200:203], v[124:127]\n" \
"ds_read_b64_tr_b16 v[18:19], v232 offset:2560\n" \
"v_mfma_f32_16x16x32_bf16 v[124:127], v[60:63], v[204:207], v[124:127]\n" \
"global_load_lds_dwordx4 v250, s[36:37] offset:3072 sc1\n" \
"v_mfma_f32_16x16x32_bf16 v[144:147], v[32:35], v[208:211], v[144:147]\n" \
"ds_read_b128 v[200:203], v230 offset:2048\n" \
"s_add_u32 s36, s36, 17408\n" \
"v_mfma_f32_16x16x32_bf16 v[144:147], v[36:39], v[212:215], v[144:147]\n" \
"ds_read_b128 v[204:207], v231 offset:2048\n" \
"v_mfma_f32_16x16x32_bf16 v[148:151], v[40:43], v[208:211], v[148:151]\n" \
"ds_read_b64_tr_b16 v[20:21], v232 offset:8704\n" \
"v_mfma_f32_16x16x32_bf16 v[148:151], v[44:47], v[212:215], v[148:151]\n" \
"buffer_store_dwordx4 a[88:91], v248, s[56:59], s90 offen offset:2048 sc1\n" \
"v_mfma_f32_16x16x32_bf16 v[152:155], v[48:51], v[208:211], v[152:155]\n" \
"ds_read_b64_tr_b16 v[22:23], v232 offset:10752\n" \
"v_mfma_f32_16x16x32_bf16 v[152:155], v[52:55], v[212:215], v[152:155]\n" \
"ds_read_b64_tr_b16 v[24:25], v233 offset:512\n" \
"v_mfma_f32_16x16x32_bf16 v[156:159], v[56:59], v[208:211], v[156:159]\n" \
"ds_read_b128 a[88:91], v254\n" \
"ds_read_b64_tr_b16 v[26:27], v233 offset:2560\n" \
"v_mfma_f32_16x16x32_bf16 v[156:159], v[60:63], v[212:215], v[156:159]\n" \
"buffer_store_dwordx4 a[92:95], v248, s[56:59], s90 offen offset:3072 sc1\n" \
"ds_read_b128 a[120:123], v254\n" \
"v_mfma_f32_16x16x32_bf16 v[176:179], v[32:35], v[216:219], v[176:179]\n" \
"ds_read_b128 v[208:211], v230 offset:4096\n" \
"s_add_u32 s90, s88, 417792\n" \
"v_mfma_f32_16x16x32_bf16 v[176:179], v[36:39], v[220:223], v[176:179]\n" \
"ds_read_b128 v[212:215], v231 offset:4096\n" \
"v_mfma_f32_16x16x32_bf16 v[180:183], v[40:43], v[216:219], v[180:183]\n" \
"ds_read_b128 a[92:95], v254\n" \
"ds_read_b64_tr_b16 v[28:29], v233 offset:8704\n" \
"v_mfma_f32_16x16x32_bf16 v[180:183], v[44:47], v[220:223], v[180:183]\n" \
"buffer_store_dwordx4 a[0:3], v249, s[56:59], s90 offen offset:0 sc1\n" \
"ds_read_b128 a[124:127], v254\n" \
"v_mfma_f32_16x16x32_bf16 v[184:187], v[48:51], v[216:219], v[184:187]\n" \
"ds_read_b64_tr_b16 v[30:31], v233 offset:10752\n" \
"v_mfma_f32_16x16x32_bf16 v[184:187], v[52:55], v[220:223], v[184:187]\n" \
"v_mfma_f32_16x16x32_bf16 v[188:191], v[56:59], v[216:219], v[188:191]\n" \
"ds_read_b128 a[0:3], v254\n" \
"v_mfma_f32_16x16x32_bf16 v[188:191], v[60:63], v[220:223], v[188:191]\n" \
"s_waitcnt lgkmcnt(3)\n" \
"ds_read_b128 a[4:7], v254\n" \
"v_mfma_f32_16x16x32_bf16 v[64:67], v[0:3], v[192:195], v[64:67]\n" \
"ds_read_b128 v[216:219], v230 offset:6144\n" \
"v_mfma_f32_16x16x32_bf16 v[64:67], v[4:7], v[196:199], v[64:67]\n" \
"ds_read_b128 v[220:223], v231 offset:6144\n" \
"v_mfma_f32_16x16x32_bf16 v[68:71], v[8:11], v[192:195], v[68:71]\n" \
"ds_read_b64_tr_b16 v[32:33], v232 offset:1024\n" \
"v_mfma_f32_16x16x32_bf16 v[68:71], v[12:15], v[196:199], v[68:71]\n" \
"v_mfma_f32_16x16x32_bf16 v[72:75], v[16:19], v[192:195], v[72:75]\n" \
"ds_read_b64_tr_b16 v[34:35], v232 offset:3072\n" \
"v_mfma_f32_16x16x32_bf16 v[72:75], v[20:23], v[196:199], v[72:75]\n" \
"s_waitcnt lgkmcnt(3)\n" \
"ds_read_b64_tr_b16 v[36:37], v232 offset:9216\n" \
"v_mfma_f32_16x16x32_bf16 v[76:79], v[24:27], v[192:195], v[76:79]\n" \
"ds_read_b64_tr_b16 v[38:39], v232 offset:11264\n" \
"v_mfma_f32_16x16x32_bf16 v[76:79], v[28:31], v[196:199], v[76:79]\n" \
"v_mfma_f32_16x16x32_bf16 v[96:99], v[0:3], v[200:203], v[96:99]\n" \
"ds_read_b64_tr_b16 v[40:41], v233 offset:1024\n" \
"v_mfma_f32_16x16x32_bf16 v[96:99], v[4:7], v[204:207], v[96:99]\n" \
"ds_read_b64_tr_b16 v[42:43], v233 offset:3072\n" \
"v_mfma_f32_16x16x32_bf16 v[100:103], v[8:11], v[200:203], v[100:103]\n" \
"ds_read_b64_tr_b16 v[44:45], v233 offset:9216\n" \
"v_mfma_f32_16x16x32_bf16 v[100:103], v[12:15], v[204:207], v[100:103]\n" \
"v_mfma_f32_16x16x32_bf16 v[104:107], v[16:19], v[200:203], v[104:107]\n" \
"ds_read_b64_tr_b16 v[46:47], v233 offset:11264\n" \
"v_mfma_f32_16x16x32_bf16 v[104:107], v[20:23], v[204:207], v[104:107]\n" \
"ds_read_b64_tr_b16 v[48:49], v232 offset:1536\n" \
"v_mfma_f32_16x16x32_bf16 v[108:111], v[24:27], v[200:203], v[108:111]\n" \
"ds_read_b64_tr_b16 v[50:51], v232 offset:3584\n" \
"v_mfma_f32_16x16x32_bf16 v[108:111], v[28:31], v[204:207], v[108:111]\n" \
"v_mfma_f32_16x16x32_bf16 v[128:131], v[0:3], v[208:211], v[128:131]\n" \
"ds_read_b64_tr_b16 v[52:53], v232 offset:9728\n" \
"v_mfma_f32_16x16x32_bf16 v[128:131], v[4:7], v[212:215], v[128:131]\n" \
"ds_read_b64_tr_b16 v[54:55], v232 offset:11776\n" \
"v_mfma_f32_16x16x32_bf16 v[132:135], v[8:11], v[208:211], v[132:135]\n" \
"ds_read_b64_tr_b16 v[56:57], v233 offset:1536\n" \
"v_mfma_f32_16x16x32_bf16 v[132:135], v[12:15], v[212:215], v[132:135]\n" \
"v_mfma_f32_16x16x32_bf16 v[136:139], v[16:19], v[208:211], v[136:139]\n" \
"ds_read_b64_tr_b16 v[58:59], v233 offset:3584\n" \
"v_mfma_f32_16x16x32_bf16 v[136:139], v[20:23], v[212:215], v[136:139]\n" \
"ds_read_b64_tr_b16 v[60:61], v233 offset:9728\n" \
"v_mfma_f32_16x16x32_bf16 v[140:143], v[24:27], v[208:211], v[140:143]\n" \
"ds_read_b64_tr_b16 v[62:63], v233 offset:11776\n" \
"v_mfma_f32_16x16x32_bf16 v[140:143], v[28:31], v[212:215], v[140:143]\n" \
"s_waitcnt lgkmcnt(3)\n" \
"v_mfma_f32_16x16x32_bf16 v[160:163], v[0:3], v[216:219], v[160:163]\n" \
"v_add_u32 v230, 0x8000, v230\n" \
"v_add_u32 v231, 0x8000, v231\n" \
"v_mfma_f32_16x16x32_bf16 v[160:163], v[4:7], v[220:223], v[160:163]\n" \
"v_add_u32 v232, 0x4000, v232\n" \
"v_add_u32 v233, 0x4000, v233\n" \
"v_mfma_f32_16x16x32_bf16 v[164:167], v[8:11], v[216:219], v[164:167]\n" \
"v_mfma_f32_16x16x32_bf16 v[164:167], v[12:15], v[220:223], v[164:167]\n" \
"v_mfma_f32_16x16x32_bf16 v[168:171], v[16:19], v[216:219], v[168:171]\n" \
"v_mfma_f32_16x16x32_bf16 v[168:171], v[20:23], v[220:223], v[168:171]\n" \
"v_mfma_f32_16x16x32_bf16 v[172:175], v[24:27], v[216:219], v[172:175]\n" \
"s_waitcnt vmcnt(14) lgkmcnt(0)\n" \
"v_mfma_f32_16x16x32_bf16 v[172:175], v[28:31], v[220:223], v[172:175]\n" \
"s_barrier\n" \
"v_mfma_f32_16x16x32_bf16 v[80:83], v[32:35], v[192:195], v[80:83]\n" \
"ds_read_b64_tr_b16 v[0:1], v232 offset:0\n" \
"v_mfma_f32_16x16x32_bf16 v[80:83], v[36:39], v[196:199], v[80:83]\n" \
"ds_read_b64_tr_b16 v[2:3], v232 offset:2048\n" \
"v_mfma_f32_16x16x32_bf16 v[84:87], v[40:43], v[192:195], v[84:87]\n" \
"s_add_u32 m0, s95, 32768\n" \
"ds_read_b64_tr_b16 v[4:5], v232 offset:8192\n" \
"v_mfma_f32_16x16x32_bf16 v[84:87], v[44:47], v[196:199], v[84:87]\n" \
"global_load_lds_dwordx4 v250, s[36:37] offset:0 sc1\n" \
"v_mfma_f32_16x16x32_bf16 v[88:91], v[48:51], v[192:195], v[88:91]\n" \
"ds_read_b64_tr_b16 v[6:7], v232 offset:10240\n" \
"v_mfma_f32_16x16x32_bf16 v[88:91], v[52:55], v[196:199], v[88:91]\n" \
"ds_read_b64_tr_b16 v[8:9], v233 offset:0\n" \
"v_mfma_f32_16x16x32_bf16 v[92:95], v[56:59], v[192:195], v[92:95]\n" \
"ds_read_b64_tr_b16 v[10:11], v233 offset:2048\n" \
"v_mfma_f32_16x16x32_bf16 v[92:95], v[60:63], v[196:199], v[92:95]\n" \
"global_load_lds_dwordx4 v250, s[36:37] offset:1024 sc1\n" \
"v_mfma_f32_16x16x32_bf16 v[112:115], v[32:35], v[200:203], v[112:115]\n" \
"ds_read_b128 v[192:195], v230 offset:0\n" \
"v_mfma_f32_16x16x32_bf16 v[112:115], v[36:39], v[204:207], v[112:115]\n" \
"ds_read_b128 v[196:199], v231 offset:0\n" \
"v_mfma_f32_16x16x32_bf16 v[116:119], v[40:43], v[200:203], v[116:119]\n" \
"ds_read_b64_tr_b16 v[12:13], v233 offset:8192\n" \
"v_mfma_f32_16x16x32_bf16 v[116:119], v[44:47], v[204:207], v[116:119]\n" \
"global_load_lds_dwordx4 v250, s[36:37] offset:2048 sc1\n" \
"v_mfma_f32_16x16x32_bf16 v[120:123], v[48:51], v[200:203], v[120:123]\n" \
"ds_read_b64_tr_b16 v[14:15], v233 offset:10240\n" \
"v_mfma_f32_16x16x32_bf16 v[120:123], v[52:55], v[204:207], v[120:123]\n" \
"ds_read_b64_tr_b16 v[16:17], v232 offset:512\n" \
"v_mfma_f32_16x16x32_bf16 v[124:127], v[56:59], v[200:203], v[124:127]\n" \
"ds_read_b64_tr_b16 v[18:19], v232 offset:2560\n" \
"v_mfma_f32_16x16x32_bf16 v[124:127], v[60:63], v[204:207], v[124:127]\n" \
"global_load_lds_dwordx4 v250, s[36:37] offset:3072 sc1\n" \
"v_mfma_f32_16x16x32_bf16 v[144:147], v[32:35], v[208:211], v[144:147]\n" \
"ds_read_b128 v[200:203], v230 offset:2048\n" \
"s_add_u32 s36, s36, 17408\n" \
"v_mfma_f32_16x16x32_bf16 v[144:147], v[36:39], v[212:215], v[144:147]\n" \
"ds_read_b128 v[204:207], v231 offset:2048\n" \
"v_mfma_f32_16x16x32_bf16 v[148:151], v[40:43], v[208:211], v[148:151]\n" \
"ds_read_b64_tr_b16 v[20:21], v232 offset:8704\n" \
"v_mfma_f32_16x16x32_bf16 v[148:151], v[44:47], v[212:215], v[148:151]\n" \
"buffer_store_dwordx4 a[32:35], v249, s[56:59], s90 offen offset:256 sc1\n" \
"v_mfma_f32_16x16x32_bf16 v[152:155], v[48:51], v[208:211], v[152:155]\n" \
"ds_read_b64_tr_b16 v[22:23], v232 offset:10752\n" \
"v_mfma_f32_16x16x32_bf16 v[152:155], v[52:55], v[212:215], v[152:155]\n" \
"ds_read_b64_tr_b16 v[24:25], v233 offset:512\n" \
"v_mfma_f32_16x16x32_bf16 v[156:159], v[56:59], v[208:211], v[156:159]\n" \
"ds_read_b128 a[32:35], v254\n" \
"ds_read_b64_tr_b16 v[26:27], v233 offset:2560\n" \
"v_mfma_f32_16x16x32_bf16 v[156:159], v[60:63], v[212:215], v[156:159]\n" \
"buffer_store_dwordx4 a[8:11], v249, s[56:59], s90 offen offset:2048 sc1\n" \
"ds_read_b128 a[36:39], v254\n" \
"v_mfma_f32_16x16x32_bf16 v[176:179], v[32:35], v[216:219], v[176:179]\n" \
"ds_read_b128 v[208:211], v230 offset:4096\n" \
"v_mfma_f32_16x16x32_bf16 v[176:179], v[36:39], v[220:223], v[176:179]\n" \
"ds_read_b128 v[212:215], v231 offset:4096\n" \
"v_mfma_f32_16x16x32_bf16 v[180:183], v[40:43], v[216:219], v[180:183]\n" \
"ds_read_b128 a[8:11], v254\n" \
"ds_read_b64_tr_b16 v[28:29], v233 offset:8704\n" \
"v_mfma_f32_16x16x32_bf16 v[180:183], v[44:47], v[220:223], v[180:183]\n" \
"buffer_store_dwordx4 a[40:43], v249, s[56:59], s90 offen offset:2304 sc1\n" \
"ds_read_b128 a[12:15], v254\n" \
"v_mfma_f32_16x16x32_bf16 v[184:187], v[48:51], v[216:219], v[184:187]\n" \
"ds_read_b64_tr_b16 v[30:31], v233 offset:10752\n" \
"v_mfma_f32_16x16x32_bf16 v[184:187], v[52:55], v[220:223], v[184:187]\n" \
"v_mfma_f32_16x16x32_bf16 v[188:191], v[56:59], v[216:219], v[188:191]\n" \
"ds_read_b128 a[40:43], v254\n" \
"v_mfma_f32_16x16x32_bf16 v[188:191], v[60:63], v[220:223], v[188:191]\n" \
"s_waitcnt lgkmcnt(3)\n" \
"ds_read_b128 a[44:47], v254\n" \
"v_mfma_f32_16x16x32_bf16 v[64:67], v[0:3], v[192:195], v[64:67]\n" \
"ds_read_b128 v[216:219], v230 offset:6144\n" \
"v_mfma_f32_16x16x32_bf16 v[64:67], v[4:7], v[196:199], v[64:67]\n" \
"ds_read_b128 v[220:223], v231 offset:6144\n" \
"v_mfma_f32_16x16x32_bf16 v[68:71], v[8:11], v[192:195], v[68:71]\n" \
"ds_read_b64_tr_b16 v[32:33], v232 offset:1024\n" \
"v_mfma_f32_16x16x32_bf16 v[68:71], v[12:15], v[196:199], v[68:71]\n" \
"v_mfma_f32_16x16x32_bf16 v[72:75], v[16:19], v[192:195], v[72:75]\n" \
"ds_read_b64_tr_b16 v[34:35], v232 offset:3072\n" \
"v_mfma_f32_16x16x32_bf16 v[72:75], v[20:23], v[196:199], v[72:75]\n" \
"s_waitcnt lgkmcnt(3)\n" \
"ds_read_b64_tr_b16 v[36:37], v232 offset:9216\n" \
"v_mfma_f32_16x16x32_bf16 v[76:79], v[24:27], v[192:195], v[76:79]\n" \
"ds_read_b64_tr_b16 v[38:39], v232 offset:11264\n" \
"v_cvt_pk_bf16_f32 v64, v64, v65\n" \
"v_cvt_pk_bf16_f32 v65, v66, v67\n" \
"v_cvt_pk_bf16_f32 v66, v68, v69\n" \
"v_cvt_pk_bf16_f32 v67, v70, v71\n" \
"v_mfma_f32_16x16x32_bf16 v[76:79], v[28:31], v[196:199], v[76:79]\n" \
"v_accvgpr_write_b32 a128, v64\n" \
"v_accvgpr_write_b32 a129, v65\n" \
"v_mfma_f32_16x16x32_bf16 v[96:99], v[0:3], v[200:203], v[96:99]\n" \
"ds_read_b64_tr_b16 v[40:41], v233 offset:1024\n" \
"v_accvgpr_write_b32 a130, v66\n" \
"v_mfma_f32_16x16x32_bf16 v[96:99], v[4:7], v[204:207], v[96:99]\n" \
"ds_read_b64_tr_b16 v[42:43], v233 offset:3072\n" \
"v_accvgpr_write_b32 a131, v67\n" \
"v_mfma_f32_16x16x32_bf16 v[100:103], v[8:11], v[200:203], v[100:103]\n" \
"ds_read_b64_tr_b16 v[44:45], v233 offset:9216\n" \
"v_cvt_pk_bf16_f32 v72, v72, v73\n" \
"v_cvt_pk_bf16_f32 v73, v74, v75\n" \
"v_cvt_pk_bf16_f32 v74, v76, v77\n" \
"v_cvt_pk_bf16_f32 v75, v78, v79\n" \
"v_mfma_f32_16x16x32_bf16 v[100:103], v[12:15], v[204:207], v[100:103]\n" \
"v_accvgpr_write_b32 a132, v72\n" \
"v_accvgpr_write_b32 a133, v73\n" \
"v_mfma_f32_16x16x32_bf16 v[104:107], v[16:19], v[200:203], v[104:107]\n" \
"ds_read_b64_tr_b16 v[46:47], v233 offset:11264\n" \
"v_accvgpr_write_b32 a134, v74\n" \
"v_mfma_f32_16x16x32_bf16 v[104:107], v[20:23], v[204:207], v[104:107]\n" \
"ds_read_b64_tr_b16 v[48:49], v232 offset:1536\n" \
"v_accvgpr_write_b32 a135, v75\n" \
"v_mfma_f32_16x16x32_bf16 v[108:111], v[24:27], v[200:203], v[108:111]\n" \
"ds_read_b64_tr_b16 v[50:51], v232 offset:3584\n" \
"v_cvt_pk_bf16_f32 v96, v96, v97\n" \
"v_cvt_pk_bf16_f32 v97, v98, v99\n" \
"v_cvt_pk_bf16_f32 v98, v100, v101\n" \
"v_cvt_pk_bf16_f32 v99, v102, v103\n" \
"v_mfma_f32_16x16x32_bf16 v[108:111], v[28:31], v[204:207], v[108:111]\n" \
"v_accvgpr_write_b32 a144, v96\n" \
"v_accvgpr_write_b32 a145, v97\n" \
"v_mfma_f32_16x16x32_bf16 v[128:131], v[0:3], v[208:211], v[128:131]\n" \
"ds_read_b64_tr_b16 v[52:53], v232 offset:9728\n" \
"v_accvgpr_write_b32 a146, v98\n" \
"v_mfma_f32_16x16x32_bf16 v[128:131], v[4:7], v[212:215], v[128:131]\n" \
"ds_read_b64_tr_b16 v[54:55], v232 offset:11776\n" \
"v_accvgpr_write_b32 a147, v99\n" \
"v_mfma_f32_16x16x32_bf16 v[132:135], v[8:11], v[208:211], v[132:135]\n" \
"ds_read_b64_tr_b16 v[56:57], v233 offset:1536\n" \
"v_cvt_pk_bf16_f32 v104, v104, v105\n" \
"v_cvt_pk_bf16_f32 v105, v106, v107\n" \
"v_cvt_pk_bf16_f32 v106, v108, v109\n" \
"v_cvt_pk_bf16_f32 v107, v110, v111\n" \
"v_mfma_f32_16x16x32_bf16 v[132:135], v[12:15], v[212:215], v[132:135]\n" \
"v_accvgpr_write_b32 a148, v104\n" \
"v_accvgpr_write_b32 a149, v105\n" \
"v_mfma_f32_16x16x32_bf16 v[136:139], v[16:19], v[208:211], v[136:139]\n" \
"ds_read_b64_tr_b16 v[58:59], v233 offset:3584\n" \
"v_accvgpr_write_b32 a150, v106\n" \
"v_mfma_f32_16x16x32_bf16 v[136:139], v[20:23], v[212:215], v[136:139]\n" \
"ds_read_b64_tr_b16 v[60:61], v233 offset:9728\n" \
"v_accvgpr_write_b32 a151, v107\n" \
"v_mfma_f32_16x16x32_bf16 v[140:143], v[24:27], v[208:211], v[140:143]\n" \
"ds_read_b64_tr_b16 v[62:63], v233 offset:11776\n" \
"v_cvt_pk_bf16_f32 v128, v128, v129\n" \
"v_cvt_pk_bf16_f32 v129, v130, v131\n" \
"v_mfma_f32_16x16x32_bf16 v[140:143], v[28:31], v[212:215], v[140:143]\n" \
"s_waitcnt lgkmcnt(3)\n" \
"v_cvt_pk_bf16_f32 v130, v132, v133\n" \
"v_cvt_pk_bf16_f32 v131, v134, v135\n" \
"v_mfma_f32_16x16x32_bf16 v[160:163], v[0:3], v[216:219], v[160:163]\n" \
"v_accvgpr_write_b32 a160, v128\n" \
"v_accvgpr_write_b32 a161, v129\n" \
"v_mfma_f32_16x16x32_bf16 v[160:163], v[4:7], v[220:223], v[160:163]\n" \
"Lpg4934_%=:\n" \
"s_cbranch_execz Lpg6195_%=\n" \
"v_accvgpr_write_b32 a162, v130\n" \
"v_accvgpr_write_b32 a163, v131\n" \
"v_mfma_f32_16x16x32_bf16 v[164:167], v[8:11], v[216:219], v[164:167]\n" \
"v_cvt_pk_bf16_f32 v136, v136, v137\n" \
"v_cvt_pk_bf16_f32 v137, v138, v139\n" \
"v_mfma_f32_16x16x32_bf16 v[164:167], v[12:15], v[220:223], v[164:167]\n" \
"v_cvt_pk_bf16_f32 v138, v140, v141\n" \
"v_cvt_pk_bf16_f32 v139, v142, v143\n" \
"v_mfma_f32_16x16x32_bf16 v[168:171], v[16:19], v[216:219], v[168:171]\n" \
"v_accvgpr_write_b32 a164, v136\n" \
"v_accvgpr_write_b32 a165, v137\n" \
"v_mfma_f32_16x16x32_bf16 v[168:171], v[20:23], v[220:223], v[168:171]\n" \
"v_accvgpr_write_b32 a166, v138\n" \
"v_accvgpr_write_b32 a167, v139\n" \
"v_mfma_f32_16x16x32_bf16 v[172:175], v[24:27], v[216:219], v[172:175]\n" \
"s_waitcnt vmcnt(10) lgkmcnt(0)\n" \
"v_cvt_pk_bf16_f32 v160, v160, v161\n" \
"v_cvt_pk_bf16_f32 v161, v162, v163\n" \
"v_cvt_pk_bf16_f32 v162, v164, v165\n" \
"v_cvt_pk_bf16_f32 v163, v166, v167\n" \
"v_accvgpr_write_b32 a176, v160\n" \
"v_mfma_f32_16x16x32_bf16 v[172:175], v[28:31], v[220:223], v[172:175]\n" \
"s_barrier\n" \
"v_accvgpr_write_b32 a177, v161\n" \
"v_mfma_f32_16x16x32_bf16 v[80:83], v[32:35], v[192:195], v[80:83]\n" \
"s_mov_b32 m0, s74\n" \
"v_accvgpr_write_b32 a178, v162\n" \
"v_accvgpr_write_b32 a179, v163\n" \
"v_mfma_f32_16x16x32_bf16 v[80:83], v[36:39], v[196:199], v[80:83]\n" \
"ds_read_b128 a[192:195], v249 offset:0\n" \
"global_load_lds_dwordx4 v250, s[38:39] offset:0 sc1\n" \
"v_mfma_f32_16x16x32_bf16 v[84:87], v[40:43], v[192:195], v[84:87]\n" \
"ds_read_b128 v[0:3], v254\n" \
"v_cvt_pk_bf16_f32 v168, v168, v169\n" \
"v_cvt_pk_bf16_f32 v169, v170, v171\n" \
"v_cvt_pk_bf16_f32 v170, v172, v173\n" \
"v_cvt_pk_bf16_f32 v171, v174, v175\n" \
"v_accvgpr_write_b32 a180, v168\n" \
"v_accvgpr_write_b32 a181, v169\n" \
"v_mfma_f32_16x16x32_bf16 v[84:87], v[44:47], v[196:199], v[84:87]\n" \
"ds_read_b128 a[196:199], v249 offset:2048\n" \
"global_load_lds_dwordx4 v250, s[38:39] offset:1024 sc1\n" \
"v_accvgpr_write_b32 a182, v170\n" \
"v_mfma_f32_16x16x32_bf16 v[88:91], v[48:51], v[192:195], v[88:91]\n" \
"ds_read_b128 v[4:7], v254\n" \
"v_accvgpr_write_b32 a183, v171\n" \
"v_mfma_f32_16x16x32_bf16 v[88:91], v[52:55], v[196:199], v[88:91]\n" \
"ds_read_b128 a[224:227], v249 offset:4096\n" \
"global_load_lds_dwordx4 v250, s[38:39] offset:2048 sc1\n" \
"v_mfma_f32_16x16x32_bf16 v[92:95], v[56:59], v[192:195], v[92:95]\n" \
"v_cvt_pk_bf16_f32 v80, v80, v81\n" \
"v_cvt_pk_bf16_f32 v81, v82, v83\n" \
"v_cvt_pk_bf16_f32 v82, v84, v85\n" \
"v_cvt_pk_bf16_f32 v83, v86, v87\n" \
"v_accvgpr_write_b32 a136, v80\n" \
"v_accvgpr_write_b32 a137, v81\n" \
"v_mfma_f32_16x16x32_bf16 v[92:95], v[60:63], v[196:199], v[92:95]\n" \
"ds_read_b128 a[228:231], v249 offset:6144\n" \
"global_load_lds_dwordx4 v250, s[38:39] offset:3072 sc1\n" \
"v_accvgpr_write_b32 a138, v82\n" \
"v_mfma_f32_16x16x32_bf16 v[112:115], v[32:35], v[200:203], v[112:115]\n" \
"s_add_u32 s38, s38, 17408\n" \
"v_accvgpr_write_b32 a139, v83\n" \
"s_add_u32 m0, m0, 0x4000\n" \
"v_mfma_f32_16x16x32_bf16 v[112:115], v[36:39], v[204:207], v[112:115]\n" \
"ds_read_b128 a[200:203], v249 offset:256\n" \
"global_load_lds_dwordx4 v250, s[38:39] offset:0 sc1\n" \
"v_mfma_f32_16x16x32_bf16 v[116:119], v[40:43], v[200:203], v[116:119]\n" \
"v_cvt_pk_bf16_f32 v88, v88, v89\n" \
"v_cvt_pk_bf16_f32 v89, v90, v91\n" \
"v_cvt_pk_bf16_f32 v90, v92, v93\n" \
"v_cvt_pk_bf16_f32 v91, v94, v95\n" \
"v_accvgpr_write_b32 a140, v88\n" \
"v_accvgpr_write_b32 a141, v89\n" \
"v_mfma_f32_16x16x32_bf16 v[116:119], v[44:47], v[204:207], v[116:119]\n" \
"ds_read_b128 a[204:207], v249 offset:2304\n" \
"global_load_lds_dwordx4 v250, s[38:39] offset:1024 sc1\n" \
"v_mfma_f32_16x16x32_bf16 v[120:123], v[48:51], v[200:203], v[120:123]\n" \
"v_accvgpr_write_b32 a142, v90\n" \
"v_accvgpr_write_b32 a143, v91\n" \
"v_mfma_f32_16x16x32_bf16 v[120:123], v[52:55], v[204:207], v[120:123]\n" \
"ds_read_b128 a[232:235], v249 offset:4352\n" \
"global_load_lds_dwordx4 v250, s[38:39] offset:2048 sc1\n" \
"v_mfma_f32_16x16x32_bf16 v[124:127], v[56:59], v[200:203], v[124:127]\n" \
"v_cvt_pk_bf16_f32 v112, v112, v113\n" \
"v_cvt_pk_bf16_f32 v113, v114, v115\n" \
"v_cvt_pk_bf16_f32 v114, v116, v117\n" \
"v_cvt_pk_bf16_f32 v115, v118, v119\n" \
"v_accvgpr_write_b32 a152, v112\n" \
"v_mfma_f32_16x16x32_bf16 v[124:127], v[60:63], v[204:207], v[124:127]\n" \
"ds_read_b128 a[236:239], v249 offset:6400\n" \
"global_load_lds_dwordx4 v250, s[38:39] offset:3072 sc1\n" \
"v_mfma_f32_16x16x32_bf16 v[144:147], v[32:35], v[208:211], v[144:147]\n" \
"s_add_u32 s38, s38, 17408\n" \
"s_xor_b32 s74, s74, 0x8000\n" \
"s_add_u32 s90, s89, 417792\n" \
"v_mfma_f32_16x16x32_bf16 v[144:147], v[36:39], v[212:215], v[144:147]\n" \
"ds_read_b128 a[208:211], v249 offset:8192\n" \
"v_accvgpr_write_b32 a153, v113\n" \
"v_mfma_f32_16x16x32_bf16 v[148:151], v[40:43], v[208:211], v[148:151]\n" \
"v_accvgpr_write_b32 a154, v114\n" \
"v_accvgpr_write_b32 a155, v115\n" \
"v_mfma_f32_16x16x32_bf16 v[148:151], v[44:47], v[212:215], v[148:151]\n" \
"buffer_store_dwordx4 a[16:19], v248, s[56:59], s90 offen offset:0 sc1\n" \
"ds_read_b128 a[212:215], v249 offset:10240\n" \
"v_cvt_pk_bf16_f32 v120, v120, v121\n" \
"v_cvt_pk_bf16_f32 v121, v122, v123\n" \
"v_cvt_pk_bf16_f32 v122, v124, v125\n" \
"v_mfma_f32_16x16x32_bf16 v[152:155], v[48:51], v[208:211], v[152:155]\n" \
"v_cvt_pk_bf16_f32 v123, v126, v127\n" \
"v_accvgpr_write_b32 a156, v120\n" \
"v_mfma_f32_16x16x32_bf16 v[152:155], v[52:55], v[212:215], v[152:155]\n" \
"ds_read_b128 a[240:243], v249 offset:12288\n" \
"v_accvgpr_write_b32 a157, v121\n" \
"v_mfma_f32_16x16x32_bf16 v[156:159], v[56:59], v[208:211], v[156:159]\n" \
"ds_read_b128 a[16:19], v254\n" \
"v_accvgpr_write_b32 a158, v122\n" \
"v_mfma_f32_16x16x32_bf16 v[156:159], v[60:63], v[212:215], v[156:159]\n" \
"ds_read_b128 a[244:247], v249 offset:14336\n" \
"v_accvgpr_write_b32 a159, v123\n" \
"v_mfma_f32_16x16x32_bf16 v[176:179], v[32:35], v[216:219], v[176:179]\n" \
"ds_read_b128 a[216:219], v249 offset:8448\n" \
"v_cvt_pk_bf16_f32 v144, v144, v145\n" \
"v_mfma_f32_16x16x32_bf16 v[176:179], v[36:39], v[220:223], v[176:179]\n" \
"ds_read_b128 a[220:223], v249 offset:10496\n" \
"v_cvt_pk_bf16_f32 v145, v146, v147\n" \
"v_mfma_f32_16x16x32_bf16 v[180:183], v[40:43], v[216:219], v[180:183]\n" \
"ds_read_b128 a[248:251], v249 offset:12544\n" \
"v_cvt_pk_bf16_f32 v146, v148, v149\n" \
"v_mfma_f32_16x16x32_bf16 v[180:183], v[44:47], v[220:223], v[180:183]\n" \
"ds_read_b128 a[252:255], v249 offset:14592\n" \
"v_cvt_pk_bf16_f32 v147, v150, v151\n" \
"v_mfma_f32_16x16x32_bf16 v[184:187], v[48:51], v[216:219], v[184:187]\n" \
"ds_read_b128 a[48:51], v254\n" \
"v_accvgpr_write_b32 a168, v144\n" \
"v_mfma_f32_16x16x32_bf16 v[184:187], v[52:55], v[220:223], v[184:187]\n" \
"ds_read_b128 v[32:35], v254\n" \
"v_accvgpr_write_b32 a169, v145\n" \
"v_mfma_f32_16x16x32_bf16 v[188:191], v[56:59], v[216:219], v[188:191]\n" \
"ds_read_b128 v[36:39], v254\n" \
"v_accvgpr_write_b32 a170, v146\n" \
"v_mfma_f32_16x16x32_bf16 v[188:191], v[60:63], v[220:223], v[188:191]\n" \
"s_waitcnt lgkmcnt(4)\n" \
"buffer_store_dwordx4 a[20:23], v248, s[56:59], s90 offen offset:1024 sc1\n" \
"v_accvgpr_write_b32 a171, v147\n" \
"s_cmp_eq_u32 s63, 0\n" \
"s_cbranch_scc1 Lmask0_%=\n" \
"Lmask0r_%=:\n" \
"v_mfma_f32_16x16x32_bf16 v[0:3], a[192:195], a[128:131], v[0:3]\n" \
"s_lshl_b32 s70, s95, 1\n" \
"v_mov_b32 v201, 0xf149f2ca\n" \
"v_cvt_pk_bf16_f32 v152, v152, v153\n" \
"v_cvt_pk_bf16_f32 v153, v154, v155\n" \
"v_mfma_f32_16x16x32_bf16 v[0:3], a[196:199], a[132:135], v[0:3]\n" \
"s_add_u32 s70, s70, 131072\n" \
"v_cvt_pk_bf16_f32 v154, v156, v157\n" \
"v_cvt_pk_bf16_f32 v155, v158, v159\n" \
"v_mfma_f32_16x16x32_bf16 v[0:3], a[224:227], a[136:139], v[0:3]\n" \
"ds_read_b128 a[20:23], v254\n" \
"v_accvgpr_write_b32 a172, v152\n" \
"v_mfma_f32_16x16x32_bf16 v[0:3], a[228:231], a[140:143], v[0:3]\n" \
"ds_read_b128 a[52:55], v254\n" \
"v_accvgpr_write_b32 a173, v153\n" \
"v_mfma_f32_16x16x32_bf16 v[8:11], a[192:195], a[144:147], 0\n" \
"v_accvgpr_write_b32 a174, v154\n" \
"v_accvgpr_write_b32 a175, v155\n" \
"v_mfma_f32_16x16x32_bf16 v[8:11], a[196:199], a[148:151], v[8:11]\n" \
"v_cvt_pk_bf16_f32 v176, v176, v177\n" \
"v_cvt_pk_bf16_f32 v177, v178, v179\n" \
"v_mfma_f32_16x16x32_bf16 v[8:11], a[224:227], a[152:155], v[8:11]\n" \
"v_cvt_pk_bf16_f32 v178, v180, v181\n" \
"v_cvt_pk_bf16_f32 v179, v182, v183\n" \
"v_max3_f32 v192, v0, v1, v2\n" \
"v_max3_f32 v192, v192, v3, v201\n" \
"v_mfma_f32_16x16x32_bf16 v[8:11], a[228:231], a[156:159], v[8:11]\n" \
"v_accvgpr_write_b32 a184, v176\n" \
"v_accvgpr_write_b32 a185, v177\n" \
"v_mfma_f32_16x16x32_bf16 v[16:19], a[192:195], a[160:163], 0\n" \
"v_accvgpr_write_b32 a186, v178\n" \
"v_accvgpr_write_b32 a187, v179\n" \
"v_mfma_f32_16x16x32_bf16 v[16:19], a[196:199], a[164:167], v[16:19]\n" \
"v_cvt_pk_bf16_f32 v184, v184, v185\n" \
"v_cvt_pk_bf16_f32 v185, v186, v187\n" \
"v_mfma_f32_16x16x32_bf16 v[16:19], a[224:227], a[168:171], v[16:19]\n" \
"v_max3_f32 v193, v8, v9, v10\n" \
"v_cvt_pk_bf16_f32 v186, v188, v189\n" \
"v_cvt_pk_bf16_f32 v187, v190, v191\n" \
"v_mfma_f32_16x16x32_bf16 v[16:19], a[228:231], a[172:175], v[16:19]\n" \
"v_accvgpr_write_b32 a188, v184\n" \
"v_accvgpr_write_b32 a189, v185\n" \
"v_mfma_f32_16x16x32_bf16 v[24:27], a[192:195], a[176:179], 0\n" \
"v_accvgpr_write_b32 a190, v186\n" \
"v_accvgpr_write_b32 a191, v187\n" \
"v_mfma_f32_16x16x32_bf16 v[24:27], a[196:199], a[180:183], v[24:27]\n" \
"v_add_u32 v200, s70, v248\n" \
"v_max3_f32 v193, v193, v11, v201\n" \
"v_mfma_f32_16x16x32_bf16 v[24:27], a[224:227], a[184:187], v[24:27]\n" \
"ds_read_b128 v[128:131], v254\n" \
"ds_read_b128 v[132:135], v254\n" \
"v_max3_f32 v194, v16, v17, v18\n" \
"v_max3_f32 v194, v194, v19, v201\n" \
"v_mfma_f32_16x16x32_bf16 v[24:27], a[228:231], a[188:191], v[24:27]\n" \
"s_waitcnt lgkmcnt(2)\n" \
"ds_read_b128 v[136:139], v254\n" \
"ds_read_b128 v[140:143], v254\n" \
"v_mfma_f32_16x16x32_bf16 v[4:7], a[200:203], a[128:131], v[4:7]\n" \
"ds_read_b128 v[144:147], v254\n" \
"ds_read_b128 v[148:151], v254\n" \
"v_mfma_f32_16x16x32_bf16 v[4:7], a[204:207], a[132:135], v[4:7]\n" \
"ds_read_b128 a[192:195], v249 offset:16384\n" \
"ds_read_b128 v[152:155], v254\n" \
"v_mfma_f32_16x16x32_bf16 v[4:7], a[232:235], a[136:139], v[4:7]\n" \
"ds_read_b128 v[156:159], v254\n" \
"ds_read_b128 v[160:163], v254\n" \
"v_max3_f32 v195, v24, v25, v26\n" \
"v_max3_f32 v195, v195, v27, v201\n" \
"v_mov_b32 v196, v192\n" \
"v_mov_b32 v197, v193\n" \
"v_mov_b32 v198, v194\n" \
"v_mov_b32 v199, v195\n" \
"v_permlane16_swap_b32 v196, v192\n" \
"v_permlane16_swap_b32 v197, v193\n" \
"v_permlane16_swap_b32 v198, v194\n" \
"v_permlane16_swap_b32 v199, v195\n" \
"v_max_f32 v192, v192, v196\n" \
"v_max_f32 v193, v193, v197\n" \
"v_mfma_f32_16x16x32_bf16 v[4:7], a[236:239], a[140:143], v[4:7]\n" \
"ds_read_b128 a[196:199], v249 offset:18432\n" \
"ds_read_b128 v[164:167], v254\n" \
"v_max_f32 v194, v194, v198\n" \
"v_max_f32 v195, v195, v199\n" \
"v_mov_b32 v196, v192\n" \
"v_mov_b32 v197, v193\n" \
"v_mov_b32 v198, v194\n" \
"v_mov_b32 v199, v195\n" \
"v_permlane32_swap_b32 v196, v192\n" \
"v_permlane32_swap_b32 v197, v193\n" \
"v_permlane32_swap_b32 v198, v194\n" \
"v_permlane32_swap_b32 v199, v195\n" \
"v_max_f32 v192, v192, v196\n" \
"v_max_f32 v193, v193, v197\n" \
"v_mfma_f32_16x16x32_bf16 v[12:15], a[200:203], a[144:147], 0\n" \
"ds_read_b128 v[168:171], v254\n" \
"ds_read_b128 v[172:175], v254\n" \
"v_max_f32 v194, v194, v198\n" \
"v_max_f32 v195, v195, v199\n" \
"v_sub_f32 v224, 0, v192\n" \
"v_sub_f32 v225, 0, v192\n" \
"v_sub_f32 v226, 0, v192\n" \
"v_sub_f32 v227, 0, v192\n" \
"v_add_f32 v32, v32, v224\n" \
"v_add_f32 v33, v33, v224\n" \
"v_add_f32 v34, v34, v224\n" \
"v_add_f32 v35, v35, v224\n" \
"v_sub_f32 v228, 0, v193\n" \
"v_sub_f32 v229, 0, v193\n" \
"v_mfma_f32_16x16x32_bf16 v[12:15], a[204:207], a[148:151], v[12:15]\n" \
"ds_read_b128 a[224:227], v249 offset:20480\n" \
"ds_read_b128 v[176:179], v254\n" \
"v_sub_f32 v230, 0, v193\n" \
"v_sub_f32 v231, 0, v193\n" \
"v_sub_f32 v232, 0, v194\n" \
"v_sub_f32 v233, 0, v194\n" \
"v_sub_f32 v234, 0, v194\n" \
"v_sub_f32 v235, 0, v194\n" \
"v_sub_f32 v236, 0, v195\n" \
"v_sub_f32 v237, 0, v195\n" \
"v_sub_f32 v238, 0, v195\n" \
"v_sub_f32 v239, 0, v195\n" \
"v_mfma_f32_16x16x32_bf16 v[12:15], a[232:235], a[152:155], v[12:15]\n" \
"ds_read_b128 v[180:183], v254\n" \
"v_add_f32 v36, v36, v224\n" \
"v_add_f32 v37, v37, v224\n" \
"v_add_f32 v38, v38, v224\n" \
"v_add_f32 v39, v39, v224\n" \
"v_pk_add_f32 v[0:1], v[0:1], v[224:225]\n" \
"v_pk_add_f32 v[2:3], v[2:3], v[224:225]\n" \
"v_pk_add_f32 v[4:5], v[4:5], v[224:225]\n" \
"v_pk_add_f32 v[6:7], v[6:7], v[224:225]\n" \
"v_exp_f32 v0, v0\n" \
"v_exp_f32 v1, v1\n" \
"v_exp_f32 v2, v2\n" \
"v_add_f32 v240, v0, v1\n" \
"v_mfma_f32_16x16x32_bf16 v[12:15], a[236:239], a[156:159], v[12:15]\n" \
"ds_read_b128 a[228:231], v249 offset:22528\n" \
"v_exp_f32 v3, v3\n" \
"v_add_f32 v240, v240, v2\n" \
"v_cvt_pk_bf16_f32 v0, v0, v1\n" \
"v_exp_f32 v4, v4\n" \
"v_add_f32 v240, v240, v3\n" \
"v_exp_f32 v5, v5\n" \
"v_add_f32 v240, v240, v4\n" \
"v_cvt_pk_bf16_f32 v1, v2, v3\n" \
"v_mfma_f32_16x16x32_bf16 v[20:23], a[200:203], a[160:163], 0\n" \
"buffer_store_dwordx4 a[24:27], v248, s[56:59], s90 offen offset:2048 sc1\n" \
"ds_write_b128 v200, a[160:163] offset:0\n" \
"v_mfma_f32_16x16x32_bf16 v[20:23], a[204:207], a[164:167], v[20:23]\n" \
"v_exp_f32 v6, v6\n" \
"v_add_f32 v240, v240, v5\n" \
"v_mfma_f32_16x16x32_bf16 v[20:23], a[232:235], a[168:171], v[20:23]\n" \
"v_exp_f32 v7, v7\n" \
"v_add_f32 v240, v240, v6\n" \
"v_cvt_pk_bf16_f32 v2, v4, v5\n" \
"v_add_f32 v240, v240, v7\n" \
"v_cvt_pk_bf16_f32 v3, v6, v7\n" \
"v_pk_add_f32 v[8:9], v[8:9], v[228:229]\n" \
"v_pk_add_f32 v[10:11], v[10:11], v[228:229]\n" \
"v_pk_add_f32 v[12:13], v[12:13], v[228:229]\n" \
"v_pk_add_f32 v[14:15], v[14:15], v[228:229]\n" \
"v_exp_f32 v8, v8\n" \
"v_exp_f32 v9, v9\n" \
"v_exp_f32 v10, v10\n" \
"v_mfma_f32_16x16x32_bf16 v[20:23], a[236:239], a[172:175], v[20:23]\n" \
"buffer_store_dwordx4 a[28:31], v248, s[56:59], s90 offen offset:3072 sc1\n" \
"ds_write_b128 v200, a[164:167] offset:1024\n" \
"v_add_f32 v241, v8, v9\n" \
"v_exp_f32 v11, v11\n" \
"v_add_f32 v241, v241, v10\n" \
"v_cvt_pk_bf16_f32 v8, v8, v9\n" \
"v_exp_f32 v12, v12\n" \
"v_add_f32 v241, v241, v11\n" \
"v_exp_f32 v13, v13\n" \
"v_add_f32 v241, v241, v12\n" \
"v_cvt_pk_bf16_f32 v9, v10, v11\n" \
"v_exp_f32 v14, v14\n" \
"v_add_f32 v241, v241, v13\n" \
"v_exp_f32 v15, v15\n" \
"v_mfma_f32_16x16x32_bf16 v[28:31], a[200:203], a[176:179], 0\n" \
"v_add_f32 v241, v241, v14\n" \
"v_cvt_pk_bf16_f32 v10, v12, v13\n" \
"v_add_f32 v241, v241, v15\n" \
"v_cvt_pk_bf16_f32 v11, v14, v15\n" \
"v_mfma_f32_16x16x32_bf16 v[28:31], a[204:207], a[180:183], v[28:31]\n" \
"global_load_dword v253, v252, s[40:41]\n" \
"v_mfma_f32_16x16x32_bf16 v[28:31], a[232:235], a[184:187], v[28:31]\n" \
"s_sub_u32 s80, s80, 1\n" \
"ds_write_b128 v200, a[168:171] offset:2048\n" \
"v_pk_add_f32 v[16:17], v[16:17], v[232:233]\n" \
"v_pk_add_f32 v[18:19], v[18:19], v[232:233]\n" \
"v_pk_add_f32 v[20:21], v[20:21], v[232:233]\n" \
"v_pk_add_f32 v[22:23], v[22:23], v[232:233]\n" \
"v_exp_f32 v16, v16\n" \
"v_exp_f32 v17, v17\n" \
"v_exp_f32 v18, v18\n" \
"v_add_f32 v242, v16, v17\n" \
"v_exp_f32 v19, v19\n" \
"v_add_f32 v242, v242, v18\n" \
"v_cvt_pk_bf16_f32 v16, v16, v17\n" \
"v_exp_f32 v20, v20\n" \
"v_mfma_f32_16x16x32_bf16 v[28:31], a[236:239], a[188:191], v[28:31]\n" \
"s_cmp_gt_i32 s80, 0\n" \
"v_add_f32 v242, v242, v19\n" \
"s_cselect_b32 s81, 0x80000, 0\n" \
"s_add_u32 s40, s40, s81\n" \
"v_mfma_f32_16x16x32_bf16 v[32:35], a[208:211], a[128:131], v[32:35]\n" \
"s_addc_u32 s41, s41, 0\n" \
"v_exp_f32 v21, v21\n" \
"s_add_u32 m0, s95, 49152\n" \
"v_mfma_f32_16x16x32_bf16 v[32:35], a[212:215], a[132:135], v[32:35]\n" \
"ds_read_b128 a[200:203], v249 offset:16640\n" \
"global_load_lds_dwordx4 v250, s[36:37] offset:0 sc1\n" \
"v_mfma_f32_16x16x32_bf16 v[32:35], a[240:243], a[136:139], v[32:35]\n" \
"ds_read_b128 a[24:27], v254\n" \
"v_add_f32 v242, v242, v20\n" \
"v_mfma_f32_16x16x32_bf16 v[32:35], a[244:247], a[140:143], v[32:35]\n" \
"ds_read_b128 a[204:207], v249 offset:18688\n" \
"v_cvt_pk_bf16_f32 v17, v18, v19\n" \
"v_mfma_f32_16x16x32_bf16 v[40:43], a[208:211], a[144:147], v[228:231]\n" \
"ds_read_b128 a[56:59], v254\n" \
"v_exp_f32 v22, v22\n" \
"v_mfma_f32_16x16x32_bf16 v[40:43], a[212:215], a[148:151], v[40:43]\n" \
"ds_read_b128 a[232:235], v249 offset:20736\n" \
"global_load_lds_dwordx4 v250, s[36:37] offset:1024 sc1\n" \
"v_mfma_f32_16x16x32_bf16 v[40:43], a[240:243], a[152:155], v[40:43]\n" \
"ds_read_b128 a[28:31], v254\n" \
"v_add_f32 v242, v242, v21\n" \
"v_mfma_f32_16x16x32_bf16 v[40:43], a[244:247], a[156:159], v[40:43]\n" \
"ds_read_b128 a[236:239], v249 offset:22784\n" \
"v_exp_f32 v23, v23\n" \
"v_mfma_f32_16x16x32_bf16 v[48:51], a[208:211], a[160:163], v[232:235]\n" \
"ds_write_b128 v200, a[172:175] offset:3072\n" \
"v_mfma_f32_16x16x32_bf16 v[48:51], a[212:215], a[164:167], v[48:51]\n" \
"global_load_lds_dwordx4 v250, s[36:37] offset:2048 sc1\n" \
"v_add_f32 v242, v242, v22\n" \
"v_cvt_pk_bf16_f32 v18, v20, v21\n" \
"v_add_f32 v242, v242, v23\n" \
"v_cvt_pk_bf16_f32 v19, v22, v23\n" \
"v_pk_add_f32 v[24:25], v[24:25], v[236:237]\n" \
"v_pk_add_f32 v[26:27], v[26:27], v[236:237]\n" \
"v_pk_add_f32 v[28:29], v[28:29], v[236:237]\n" \
"v_pk_add_f32 v[30:31], v[30:31], v[236:237]\n" \
"v_mfma_f32_16x16x32_bf16 v[48:51], a[240:243], a[168:171], v[48:51]\n" \
"v_exp_f32 v24, v24\n" \
"v_mfma_f32_16x16x32_bf16 v[48:51], a[244:247], a[172:175], v[48:51]\n" \
"ds_write_b128 v200, a[176:179] offset:4096\n" \
"v_mfma_f32_16x16x32_bf16 v[56:59], a[208:211], a[176:179], v[236:239]\n" \
"global_load_lds_dwordx4 v250, s[36:37] offset:3072 sc1\n" \
"v_exp_f32 v25, v25\n" \
"v_mfma_f32_16x16x32_bf16 v[56:59], a[212:215], a[180:183], v[56:59]\n" \
"s_add_u32 s36, s36, 17408\n" \
"s_mov_b32 s42, s60\n" \
"s_mov_b32 s43, s60\n" \
"v_mfma_f32_16x16x32_bf16 v[56:59], a[240:243], a[184:187], v[56:59]\n" \
"s_mov_b64 s[8:9], 0\n" \
"ds_write_b128 v200, a[180:183] offset:5120\n" \
"v_mfma_f32_16x16x32_bf16 v[56:59], a[244:247], a[188:191], v[56:59]\n" \
"s_mov_b64 s[10:11], -1\n" \
"s_lshl_b32 s71, s63, 2\n" \
"s_add_u32 s71, s71, s79\n" \
"v_mfma_f32_16x16x32_bf16 v[36:39], a[216:219], a[128:131], v[36:39]\n" \
"v_exp_f32 v26, v26\n" \
"v_mfma_f32_16x16x32_bf16 v[36:39], a[220:223], a[132:135], v[36:39]\n" \
"ds_read_b128 a[208:211], v249 offset:24576\n" \
"v_add_f32 v243, v24, v25\n" \
"v_mfma_f32_16x16x32_bf16 v[36:39], a[248:251], a[136:139], v[36:39]\n" \
"ds_read_b128 a[60:63], v254\n" \
"v_exp_f32 v27, v27\n" \
"v_mfma_f32_16x16x32_bf16 v[36:39], a[252:255], a[140:143], v[36:39]\n" \
"ds_read_b128 a[212:215], v249 offset:26624\n" \
"v_add_f32 v243, v243, v26\n" \
"v_mfma_f32_16x16x32_bf16 v[44:47], a[216:219], a[144:147], v[228:231]\n" \
"ds_read_b128 v[184:187], v254\n" \
"v_cvt_pk_bf16_f32 v24, v24, v25\n" \
"v_mfma_f32_16x16x32_bf16 v[44:47], a[220:223], a[148:151], v[44:47]\n" \
"ds_read_b128 a[240:243], v249 offset:28672\n" \
"v_exp_f32 v28, v28\n" \
"v_mfma_f32_16x16x32_bf16 v[44:47], a[248:251], a[152:155], v[44:47]\n" \
"ds_read_b128 v[188:191], v254\n" \
"v_add_f32 v243, v243, v27\n" \
"v_mfma_f32_16x16x32_bf16 v[44:47], a[252:255], a[156:159], v[44:47]\n" \
"ds_read_b128 a[244:247], v249 offset:30720\n" \
"v_exp_f32 v29, v29\n" \
"v_mfma_f32_16x16x32_bf16 v[52:55], a[216:219], a[160:163], v[232:235]\n" \
"ds_write_b128 v200, a[184:187] offset:6144\n" \
"v_mfma_f32_16x16x32_bf16 v[52:55], a[220:223], a[164:167], v[52:55]\n" \
"v_add_f32 v243, v243, v28\n" \
"v_cvt_pk_bf16_f32 v25, v26, v27\n" \
"v_mfma_f32_16x16x32_bf16 v[52:55], a[248:251], a[168:171], v[52:55]\n" \
"v_exp_f32 v30, v30\n" \
"v_add_f32 v243, v243, v29\n" \
"v_mfma_f32_16x16x32_bf16 v[52:55], a[252:255], a[172:175], v[52:55]\n" \
"ds_write_b128 v200, a[188:191] offset:7168\n" \
"v_mfma_f32_16x16x32_bf16 v[60:63], a[216:219], a[176:179], v[236:239]\n" \
"v_exp_f32 v31, v31\n" \
"v_add_f32 v243, v243, v30\n" \
"v_mfma_f32_16x16x32_bf16 v[60:63], a[220:223], a[180:183], v[60:63]\n" \
"v_cvt_pk_bf16_f32 v26, v28, v29\n" \
"v_add_f32 v243, v243, v31\n" \
"v_mfma_f32_16x16x32_bf16 v[60:63], a[248:251], a[184:187], v[60:63]\n" \
"v_mov_b32 v202, s60\n" \
"v_lshlrev_b32 v203, 28, s61\n" \
"v_mfma_f32_16x16x32_bf16 v[60:63], a[252:255], a[188:191], v[60:63]\n" \
"v_mov_b32 v246, v249\n" \
"v_mov_b32 v254, 0xff800000\n" \
"ds_read_b128 a[216:219], v249 offset:24832\n" \
"ds_read_b128 a[220:223], v249 offset:26880\n" \
"ds_read_b128 a[248:251], v249 offset:28928\n" \
"s_cmp_eq_u32 s63, 0\n" \
"s_waitcnt vmcnt(5) lgkmcnt(1)\n" \
"s_barrier\n" \
"buffer_store_dword v202, v203, s[56:59], s71 offen sc1\n" \
"s_memrealtime s[12:13]\n" \
"s_cbranch_scc1 LA1l_%=\n" \
"s_branch LA1_%=\n" \
"Lpg5399_%=:\n" \
"s_cbranch_execz Lpg6324_%=\n" \
"Lloop_%=:\n" \
"v_mfma_f32_16x16x32_bf16 a[0:3], a[192:195], v[64:67], a[0:3]\n" \
"v_exp_f32 v0, v0\n" \
"v_mfma_f32_16x16x32_bf16 a[0:3], a[196:199], v[96:99], a[0:3]\n" \
"v_cvt_pk_bf16_f32 v123, v126, v127\n" \
"v_add_f32 v240, v240, v0\n" \
"v_exp_f32 v1, v1\n" \
"v_mfma_f32_16x16x32_bf16 a[32:35], a[192:195], v[72:75], a[32:35]\n" \
"s_add_u32 m0, s73, 0x4000\n" \
"v_add_f32 v240, v240, v1\n" \
"v_exp_f32 v2, v2\n" \
"v_mfma_f32_16x16x32_bf16 a[32:35], a[196:199], v[104:107], a[32:35]\n" \
"ds_read_b128 a[252:255], v247 offset:31744\n" \
"global_load_lds_dwordx4 v250, s[36:37] offset:0 sc1\n" \
"v_add_f32 v240, v240, v2\n" \
"v_cvt_pk_bf16_f32 v0, v0, v1\n" \
"v_exp_f32 v3, v3\n" \
"v_mfma_f32_16x16x32_bf16 v[128:131], a[192:195], v[80:83], v[128:131]\n" \
"v_xor_b32 v247, 0x8000, v247\n" \
"v_add_f32 v240, v240, v3\n" \
"v_exp_f32 v4, v4\n" \
"v_mfma_f32_16x16x32_bf16 v[128:131], a[196:199], v[112:115], v[128:131]\n" \
"v_add_f32 v240, v240, v4\n" \
"v_cvt_pk_bf16_f32 v1, v2, v3\n" \
"v_mfma_f32_16x16x32_bf16 v[160:163], a[192:195], v[88:91], v[160:163]\n" \
"v_exp_f32 v5, v5\n" \
"v_mfma_f32_16x16x32_bf16 v[160:163], a[196:199], v[120:123], v[160:163]\n" \
"ds_read_b128 a[192:195], v246 offset:16384\n" \
"v_add_f32 v240, v240, v5\n" \
"v_mfma_f32_16x16x32_bf16 a[4:7], a[200:203], v[64:67], a[4:7]\n" \
"v_exp_f32 v6, v6\n" \
"v_mfma_f32_16x16x32_bf16 a[4:7], a[204:207], v[96:99], a[4:7]\n" \
"v_add_f32 v240, v240, v6\n" \
"v_cvt_pk_bf16_f32 v2, v4, v5\n" \
"v_mfma_f32_16x16x32_bf16 a[36:39], a[200:203], v[72:75], a[36:39]\n" \
"v_exp_f32 v7, v7\n" \
"v_mfma_f32_16x16x32_bf16 a[36:39], a[204:207], v[104:107], a[36:39]\n" \
"ds_read_b128 a[196:199], v246 offset:18432\n" \
"v_add_f32 v240, v240, v7\n" \
"v_mfma_f32_16x16x32_bf16 v[132:135], a[200:203], v[80:83], v[132:135]\n" \
"v_exp_f32 v8, v8\n" \
"v_mfma_f32_16x16x32_bf16 v[132:135], a[204:207], v[112:115], v[132:135]\n" \
"v_add_f32 v241, v241, v8\n" \
"v_cvt_pk_bf16_f32 v3, v6, v7\n" \
"v_mfma_f32_16x16x32_bf16 v[164:167], a[200:203], v[88:91], v[164:167]\n" \
"v_exp_f32 v9, v9\n" \
"v_mfma_f32_16x16x32_bf16 v[164:167], a[204:207], v[120:123], v[164:167]\n" \
"ds_read_b128 a[200:203], v246 offset:16640\n" \
"global_load_lds_dwordx4 v250, s[36:37] offset:1024 sc1\n" \
"v_add_f32 v241, v241, v9\n" \
"v_mfma_f32_16x16x32_bf16 a[8:11], a[208:211], v[64:67], a[8:11]\n" \
"v_exp_f32 v10, v10\n" \
"v_mfma_f32_16x16x32_bf16 a[8:11], a[212:215], v[96:99], a[8:11]\n" \
"v_add_f32 v241, v241, v10\n" \
"v_cvt_pk_bf16_f32 v8, v8, v9\n" \
"v_mfma_f32_16x16x32_bf16 a[40:43], a[208:211], v[72:75], a[40:43]\n" \
"v_exp_f32 v11, v11\n" \
"v_mfma_f32_16x16x32_bf16 a[40:43], a[212:215], v[104:107], a[40:43]\n" \
"ds_read_b128 a[204:207], v246 offset:18688\n" \
"v_add_f32 v241, v241, v11\n" \
"v_mfma_f32_16x16x32_bf16 v[136:139], a[208:211], v[80:83], v[136:139]\n" \
"v_exp_f32 v12, v12\n" \
"v_mfma_f32_16x16x32_bf16 v[136:139], a[212:215], v[112:115], v[136:139]\n" \
"v_add_f32 v241, v241, v12\n" \
"v_cvt_pk_bf16_f32 v9, v10, v11\n" \
"v_mfma_f32_16x16x32_bf16 v[168:171], a[208:211], v[88:91], v[168:171]\n" \
"v_exp_f32 v13, v13\n" \
"v_mfma_f32_16x16x32_bf16 v[168:171], a[212:215], v[120:123], v[168:171]\n" \
"ds_read_b128 a[208:211], v246 offset:24576\n" \
"v_add_f32 v241, v241, v13\n" \
"v_mfma_f32_16x16x32_bf16 a[12:15], a[216:219], v[64:67], a[12:15]\n" \
"v_exp_f32 v14, v14\n" \
"v_mfma_f32_16x16x32_bf16 a[12:15], a[220:223], v[96:99], a[12:15]\n" \
"v_add_f32 v241, v241, v14\n" \
"v_cvt_pk_bf16_f32 v10, v12, v13\n" \
"v_mfma_f32_16x16x32_bf16 a[44:47], a[216:219], v[72:75], a[44:47]\n" \
"v_exp_f32 v15, v15\n" \
"v_mfma_f32_16x16x32_bf16 a[44:47], a[220:223], v[104:107], a[44:47]\n" \
"ds_read_b128 a[212:215], v246 offset:26624\n" \
"global_load_lds_dwordx4 v250, s[36:37] offset:2048 sc1\n" \
"v_add_f32 v241, v241, v15\n" \
"v_mfma_f32_16x16x32_bf16 v[140:143], a[216:219], v[80:83], v[140:143]\n" \
"v_exp_f32 v16, v16\n" \
"v_mfma_f32_16x16x32_bf16 v[140:143], a[220:223], v[112:115], v[140:143]\n" \
"v_add_f32 v242, v242, v16\n" \
"v_cvt_pk_bf16_f32 v11, v14, v15\n" \
"v_mfma_f32_16x16x32_bf16 v[172:175], a[216:219], v[88:91], v[172:175]\n" \
"s_waitcnt lgkmcnt(6)\n" \
"v_exp_f32 v17, v17\n" \
"v_mfma_f32_16x16x32_bf16 v[172:175], a[220:223], v[120:123], v[172:175]\n" \
"ds_read_b128 a[216:219], v246 offset:24832\n" \
"v_add_f32 v242, v242, v17\n" \
"v_mfma_f32_16x16x32_bf16 a[16:19], a[224:227], v[64:67], a[16:19]\n" \
"v_exp_f32 v18, v18\n" \
"v_mfma_f32_16x16x32_bf16 a[16:19], a[228:231], v[96:99], a[16:19]\n" \
"v_add_f32 v242, v242, v18\n" \
"v_cvt_pk_bf16_f32 v16, v16, v17\n" \
"v_mfma_f32_16x16x32_bf16 a[48:51], a[224:227], v[72:75], a[48:51]\n" \
"v_exp_f32 v19, v19\n" \
"v_mfma_f32_16x16x32_bf16 a[48:51], a[228:231], v[104:107], a[48:51]\n" \
"ds_read_b128 a[220:223], v246 offset:26880\n" \
"v_add_f32 v242, v242, v19\n" \
"v_exp_f32 v20, v20\n" \
"v_mfma_f32_16x16x32_bf16 v[144:147], a[224:227], v[80:83], v[144:147]\n" \
"v_add_f32 v242, v242, v20\n" \
"v_cvt_pk_bf16_f32 v17, v18, v19\n" \
"v_mfma_f32_16x16x32_bf16 v[144:147], a[228:231], v[112:115], v[144:147]\n" \
"v_exp_f32 v21, v21\n" \
"v_mfma_f32_16x16x32_bf16 v[176:179], a[224:227], v[88:91], v[176:179]\n" \
"global_load_lds_dwordx4 v250, s[36:37] offset:3072 sc1\n" \
"s_add_u32 s36, s36, 17408\n" \
"v_mfma_f32_16x16x32_bf16 v[176:179], a[228:231], v[120:123], v[176:179]\n" \
"s_xor_b32 s73, s73, 0x8000\n" \
"ds_read_b128 a[224:227], v246 offset:20480\n" \
"s_xor_b64 s[52:53], s[44:45], s[42:43]\n" \
"v_mfma_f32_16x16x32_bf16 a[20:23], a[232:235], v[64:67], a[20:23]\n" \
"s_xor_b64 s[54:55], s[46:47], s[42:43]\n" \
"s_or_b64 s[52:53], s[52:53], s[54:55]\n" \
"s_xor_b64 s[54:55], s[48:49], s[42:43]\n" \
"v_mfma_f32_16x16x32_bf16 a[20:23], a[236:239], v[96:99], a[20:23]\n" \
"s_or_b64 s[52:53], s[52:53], s[54:55]\n" \
"s_xor_b64 s[54:55], s[50:51], s[42:43]\n" \
"s_or_b64 s[52:53], s[52:53], s[54:55]\n" \
"v_mfma_f32_16x16x32_bf16 a[52:55], a[232:235], v[72:75], a[52:55]\n" \
"s_and_b64 s[52:53], s[52:53], s[8:9]\n" \
"s_cbranch_scc1 Lrs0_%=\n" \
"Lrr0_%=:\n" \
"v_mfma_f32_16x16x32_bf16 a[52:55], a[236:239], v[104:107], a[52:55]\n" \
"s_mov_b64 s[8:9], s[10:11]\n" \
"ds_read_b128 a[228:231], v246 offset:22528\n" \
"v_add_f32 v242, v242, v21\n" \
"v_mfma_f32_16x16x32_bf16 v[148:151], a[232:235], v[80:83], v[148:151]\n" \
"v_exp_f32 v22, v22\n" \
"v_mfma_f32_16x16x32_bf16 v[148:151], a[236:239], v[112:115], v[148:151]\n" \
"v_add_f32 v242, v242, v22\n" \
"v_cvt_pk_bf16_f32 v18, v20, v21\n" \
"v_mfma_f32_16x16x32_bf16 v[180:183], a[232:235], v[88:91], v[180:183]\n" \
"v_exp_f32 v23, v23\n" \
"v_mfma_f32_16x16x32_bf16 v[180:183], a[236:239], v[120:123], v[180:183]\n" \
"ds_read_b128 a[232:235], v246 offset:20736\n" \
"v_add_f32 v242, v242, v23\n" \
"v_mfma_f32_16x16x32_bf16 a[24:27], a[240:243], v[64:67], a[24:27]\n" \
"v_exp_f32 v24, v24\n" \
"v_mfma_f32_16x16x32_bf16 a[24:27], a[244:247], v[96:99], a[24:27]\n" \
"v_add_f32 v243, v243, v24\n" \
"v_cvt_pk_bf16_f32 v19, v22, v23\n" \
"v_mfma_f32_16x16x32_bf16 a[56:59], a[240:243], v[72:75], a[56:59]\n" \
"v_exp_f32 v25, v25\n" \
"Lpg5548_%=:\n" \
"s_cbranch_execz Lpg6840_%=\n" \
"v_mfma_f32_16x16x32_bf16 a[56:59], a[244:247], v[104:107], a[56:59]\n" \
"ds_read_b128 a[236:239], v246 offset:22784\n" \
"v_add_f32 v243, v243, v25\n" \
"v_mfma_f32_16x16x32_bf16 v[152:155], a[240:243], v[80:83], v[152:155]\n" \
"v_exp_f32 v26, v26\n" \
"v_mfma_f32_16x16x32_bf16 v[152:155], a[244:247], v[112:115], v[152:155]\n" \
"v_add_f32 v243, v243, v26\n" \
"v_cvt_pk_bf16_f32 v24, v24, v25\n" \
"v_mfma_f32_16x16x32_bf16 v[184:187], a[240:243], v[88:91], v[184:187]\n" \
"v_exp_f32 v27, v27\n" \
"v_mfma_f32_16x16x32_bf16 v[184:187], a[244:247], v[120:123], v[184:187]\n" \
"ds_read_b128 a[240:243], v246 offset:28672\n" \
"v_add_f32 v243, v243, v27\n" \
"v_mfma_f32_16x16x32_bf16 a[28:31], a[248:251], v[64:67], a[28:31]\n" \
"v_exp_f32 v28, v28\n" \
"v_mfma_f32_16x16x32_bf16 a[28:31], a[252:255], v[96:99], a[28:31]\n" \
"v_add_f32 v243, v243, v28\n" \
"v_cvt_pk_bf16_f32 v25, v26, v27\n" \
"v_mfma_f32_16x16x32_bf16 a[60:63], a[248:251], v[72:75], a[60:63]\n" \
"v_exp_f32 v29, v29\n" \
"v_mfma_f32_16x16x32_bf16 a[60:63], a[252:255], v[104:107], a[60:63]\n" \
"ds_read_b128 a[244:247], v246 offset:30720\n" \
"v_add_f32 v243, v243, v29\n" \
"v_mfma_f32_16x16x32_bf16 v[156:159], a[248:251], v[80:83], v[156:159]\n" \
"v_exp_f32 v30, v30\n" \
"v_mfma_f32_16x16x32_bf16 v[156:159], a[252:255], v[112:115], v[156:159]\n" \
"v_add_f32 v243, v243, v30\n" \
"v_cvt_pk_bf16_f32 v26, v28, v29\n" \
"v_mfma_f32_16x16x32_bf16 v[188:191], a[248:251], v[88:91], v[188:191]\n" \
"s_waitcnt lgkmcnt(6)\n" \
"v_exp_f32 v31, v31\n" \
"v_mfma_f32_16x16x32_bf16 v[188:191], a[252:255], v[120:123], v[188:191]\n" \
"ds_read_b128 a[248:251], v246 offset:28928\n" \
"v_add_f32 v243, v243, v31\n" \
"LA1_%=:\n" \
"Lpg5584_%=:\n" \
"s_cbranch_execz Lpg7140_%=\n" \
"v_mfma_f32_16x16x32_bf16 v[64:67], a[192:195], a[128:131], v[224:227]\n" \
"v_exp_f32 v32, v32\n" \
"v_mfma_f32_16x16x32_bf16 v[64:67], a[196:199], a[132:135], v[64:67]\n" \
"v_cvt_pk_bf16_f32 v27, v30, v31\n" \
"v_add_f32 v240, v240, v32\n" \
"v_mfma_f32_16x16x32_bf16 v[72:75], a[192:195], a[144:147], v[228:231]\n" \
"s_mov_b32 m0, s74\n" \
"v_exp_f32 v33, v33\n" \
"v_mfma_f32_16x16x32_bf16 v[72:75], a[196:199], a[148:151], v[72:75]\n" \
"ds_read_b128 a[252:255], v246 offset:30976\n" \
"global_load_lds_dwordx4 v250, s[38:39] offset:0 sc1\n" \
"v_add_f32 v240, v240, v33\n" \
"v_mfma_f32_16x16x32_bf16 v[80:83], a[192:195], a[160:163], v[232:235]\n" \
"v_xor_b32 v246, 0x8000, v246\n" \
"v_exp_f32 v34, v34\n" \
"v_mfma_f32_16x16x32_bf16 v[80:83], a[196:199], a[164:167], v[80:83]\n" \
"v_add_f32 v240, v240, v34\n" \
"v_cvt_pk_bf16_f32 v32, v32, v33\n" \
"v_mfma_f32_16x16x32_bf16 v[88:91], a[192:195], a[176:179], v[236:239]\n" \
"v_exp_f32 v35, v35\n" \
"v_mfma_f32_16x16x32_bf16 v[88:91], a[196:199], a[180:183], v[88:91]\n" \
"ds_read_b128 a[192:195], v247 offset:0\n" \
"v_add_f32 v240, v240, v35\n" \
"v_mfma_f32_16x16x32_bf16 v[68:71], a[200:203], a[128:131], v[224:227]\n" \
"v_exp_f32 v36, v36\n" \
"v_mfma_f32_16x16x32_bf16 v[68:71], a[204:207], a[132:135], v[68:71]\n" \
"v_add_f32 v240, v240, v36\n" \
"v_cvt_pk_bf16_f32 v33, v34, v35\n" \
"v_mfma_f32_16x16x32_bf16 v[76:79], a[200:203], a[144:147], v[228:231]\n" \
"v_exp_f32 v37, v37\n" \
"v_mfma_f32_16x16x32_bf16 v[76:79], a[204:207], a[148:151], v[76:79]\n" \
"ds_read_b128 a[196:199], v247 offset:8192\n" \
"v_add_f32 v240, v240, v37\n" \
"v_mfma_f32_16x16x32_bf16 v[84:87], a[200:203], a[160:163], v[232:235]\n" \
"v_exp_f32 v38, v38\n" \
"v_mfma_f32_16x16x32_bf16 v[84:87], a[204:207], a[164:167], v[84:87]\n" \
"v_add_f32 v240, v240, v38\n" \
"v_cvt_pk_bf16_f32 v34, v36, v37\n" \
"v_mfma_f32_16x16x32_bf16 v[92:95], a[200:203], a[176:179], v[236:239]\n" \
"v_exp_f32 v39, v39\n" \
"v_mfma_f32_16x16x32_bf16 v[92:95], a[204:207], a[180:183], v[92:95]\n" \
"ds_read_b128 a[200:203], v247 offset:1024\n" \
"global_load_lds_dwordx4 v250, s[38:39] offset:1024 sc1\n" \
"v_add_f32 v240, v240, v39\n" \
"v_mfma_f32_16x16x32_bf16 v[96:99], a[208:211], a[128:131], v[224:227]\n" \
"v_exp_f32 v40, v40\n" \
"v_mfma_f32_16x16x32_bf16 v[96:99], a[212:215], a[132:135], v[96:99]\n" \
"v_add_f32 v241, v241, v40\n" \
"v_cvt_pk_bf16_f32 v35, v38, v39\n" \
"v_mfma_f32_16x16x32_bf16 v[104:107], a[208:211], a[144:147], v[228:231]\n" \
"v_exp_f32 v41, v41\n" \
"v_mfma_f32_16x16x32_bf16 v[104:107], a[212:215], a[148:151], v[104:107]\n" \
"ds_read_b128 a[204:207], v247 offset:9216\n" \
"v_add_f32 v241, v241, v41\n" \
"v_mfma_f32_16x16x32_bf16 v[112:115], a[208:211], a[160:163], v[232:235]\n" \
"v_exp_f32 v42, v42\n" \
"v_mfma_f32_16x16x32_bf16 v[112:115], a[212:215], a[164:167], v[112:115]\n" \
"v_add_f32 v241, v241, v42\n" \
"v_cvt_pk_bf16_f32 v40, v40, v41\n" \
"v_mfma_f32_16x16x32_bf16 v[120:123], a[208:211], a[176:179], v[236:239]\n" \
"v_exp_f32 v43, v43\n" \
"v_mfma_f32_16x16x32_bf16 v[120:123], a[212:215], a[180:183], v[120:123]\n" \
"ds_read_b128 a[208:211], v247 offset:2048\n" \
"v_add_f32 v241, v241, v43\n" \
"v_mfma_f32_16x16x32_bf16 v[100:103], a[216:219], a[128:131], v[224:227]\n" \
"v_exp_f32 v44, v44\n" \
"v_mfma_f32_16x16x32_bf16 v[100:103], a[220:223], a[132:135], v[100:103]\n" \
"v_add_f32 v241, v241, v44\n" \
"v_cvt_pk_bf16_f32 v41, v42, v43\n" \
"v_mfma_f32_16x16x32_bf16 v[108:111], a[216:219], a[144:147], v[228:231]\n" \
"v_exp_f32 v45, v45\n" \
"v_mfma_f32_16x16x32_bf16 v[108:111], a[220:223], a[148:151], v[108:111]\n" \
"ds_read_b128 a[212:215], v247 offset:10240\n" \
"global_load_lds_dwordx4 v250, s[38:39] offset:2048 sc1\n" \
"v_add_f32 v241, v241, v45\n" \
"v_mfma_f32_16x16x32_bf16 v[116:119], a[216:219], a[160:163], v[232:235]\n" \
"v_exp_f32 v46, v46\n" \
"v_mfma_f32_16x16x32_bf16 v[116:119], a[220:223], a[164:167], v[116:119]\n" \
"v_add_f32 v241, v241, v46\n" \
"v_cvt_pk_bf16_f32 v42, v44, v45\n" \
"v_mfma_f32_16x16x32_bf16 v[124:127], a[216:219], a[176:179], v[236:239]\n" \
"s_waitcnt lgkmcnt(6)\n" \
"v_exp_f32 v47, v47\n" \
"v_mfma_f32_16x16x32_bf16 v[124:127], a[220:223], a[180:183], v[124:127]\n" \
"ds_read_b128 a[216:219], v247 offset:3072\n" \
"v_add_f32 v241, v241, v47\n" \
"v_mfma_f32_16x16x32_bf16 v[64:67], a[224:227], a[136:139], v[64:67]\n" \
"v_exp_f32 v48, v48\n" \
"v_mfma_f32_16x16x32_bf16 v[64:67], a[228:231], a[140:143], v[64:67]\n" \
"v_add_f32 v242, v242, v48\n" \
"v_cvt_pk_bf16_f32 v43, v46, v47\n" \
"v_mfma_f32_16x16x32_bf16 v[72:75], a[224:227], a[152:155], v[72:75]\n" \
"v_exp_f32 v49, v49\n" \
"v_mfma_f32_16x16x32_bf16 v[72:75], a[228:231], a[156:159], v[72:75]\n" \
"ds_read_b128 a[220:223], v247 offset:11264\n" \
"v_add_f32 v242, v242, v49\n" \
"v_exp_f32 v50, v50\n" \
"v_mfma_f32_16x16x32_bf16 v[80:83], a[224:227], a[168:171], v[80:83]\n" \
"v_add_f32 v242, v242, v50\n" \
"v_cvt_pk_bf16_f32 v48, v48, v49\n" \
"v_mfma_f32_16x16x32_bf16 v[80:83], a[228:231], a[172:175], v[80:83]\n" \
"v_exp_f32 v51, v51\n" \
"v_mfma_f32_16x16x32_bf16 v[88:91], a[224:227], a[184:187], v[88:91]\n" \
"v_add_f32 v242, v242, v51\n" \
"v_exp_f32 v52, v52\n" \
"v_mfma_f32_16x16x32_bf16 v[88:91], a[228:231], a[188:191], v[88:91]\n" \
"ds_read_b128 a[224:227], v247 offset:4096\n" \
"global_load_lds_dwordx4 v250, s[38:39] offset:3072 sc1\n" \
"v_mfma_f32_16x16x32_bf16 v[68:71], a[232:235], a[136:139], v[68:71]\n" \
"s_add_u32 s38, s38, 17408\n" \
"v_add_f32 v242, v242, v52\n" \
"v_cvt_pk_bf16_f32 v49, v50, v51\n" \
"v_mfma_f32_16x16x32_bf16 v[68:71], a[236:239], a[140:143], v[68:71]\n" \
"v_exp_f32 v53, v53\n" \
"v_mfma_f32_16x16x32_bf16 v[76:79], a[232:235], a[152:155], v[76:79]\n" \
"v_add_f32 v242, v242, v53\n" \
"v_exp_f32 v54, v54\n" \
"v_mfma_f32_16x16x32_bf16 v[76:79], a[236:239], a[156:159], v[76:79]\n" \
"ds_read_b128 a[228:231], v247 offset:12288\n" \
"v_add_f32 v242, v242, v54\n" \
"v_cvt_pk_bf16_f32 v50, v52, v53\n" \
"v_mfma_f32_16x16x32_bf16 v[84:87], a[232:235], a[168:171], v[84:87]\n" \
"v_exp_f32 v55, v55\n" \
"v_mfma_f32_16x16x32_bf16 v[84:87], a[236:239], a[172:175], v[84:87]\n" \
"global_load_dword v253, v252, s[40:41]\n" \
"v_add_f32 v242, v242, v55\n" \
"v_mfma_f32_16x16x32_bf16 v[92:95], a[232:235], a[184:187], v[92:95]\n" \
"s_sub_u32 s80, s80, 1\n" \
"s_cmp_gt_i32 s80, 0\n" \
"s_cselect_b32 s81, 0x80000, 0\n" \
"v_mfma_f32_16x16x32_bf16 v[92:95], a[236:239], a[188:191], v[92:95]\n" \
"s_add_u32 s40, s40, s81\n" \
"ds_read_b128 a[232:235], v247 offset:5120\n" \
"s_addc_u32 s41, s41, 0\n" \
"v_mfma_f32_16x16x32_bf16 v[96:99], a[240:243], a[136:139], v[96:99]\n" \
"v_exp_f32 v56, v56\n" \
"v_mfma_f32_16x16x32_bf16 v[96:99], a[244:247], a[140:143], v[96:99]\n" \
"v_add_f32 v243, v243, v56\n" \
"v_cvt_pk_bf16_f32 v51, v54, v55\n" \
"v_mfma_f32_16x16x32_bf16 v[104:107], a[240:243], a[152:155], v[104:107]\n" \
"v_exp_f32 v57, v57\n" \
"v_mfma_f32_16x16x32_bf16 v[104:107], a[244:247], a[156:159], v[104:107]\n" \
"ds_read_b128 a[236:239], v247 offset:13312\n" \
"v_add_f32 v243, v243, v57\n" \
"v_mfma_f32_16x16x32_bf16 v[112:115], a[240:243], a[168:171], v[112:115]\n" \
"v_exp_f32 v58, v58\n" \
"v_mfma_f32_16x16x32_bf16 v[112:115], a[244:247], a[172:175], v[112:115]\n" \
"v_add_f32 v243, v243, v58\n" \
"v_cvt_pk_bf16_f32 v56, v56, v57\n" \
"v_mfma_f32_16x16x32_bf16 v[120:123], a[240:243], a[184:187], v[120:123]\n" \
"v_exp_f32 v59, v59\n" \
"v_mfma_f32_16x16x32_bf16 v[120:123], a[244:247], a[188:191], v[120:123]\n" \
"ds_read_b128 a[240:243], v247 offset:6144\n" \
"v_add_f32 v243, v243, v59\n" \
"v_mfma_f32_16x16x32_bf16 v[100:103], a[248:251], a[136:139], v[100:103]\n" \
"v_exp_f32 v60, v60\n" \
"v_mfma_f32_16x16x32_bf16 v[100:103], a[252:255], a[140:143], v[100:103]\n" \
"v_add_f32 v243, v243, v60\n" \
"v_cvt_pk_bf16_f32 v57, v58, v59\n" \
"v_mfma_f32_16x16x32_bf16 v[108:111], a[248:251], a[152:155], v[108:111]\n" \
"v_exp_f32 v61, v61\n" \
"v_mfma_f32_16x16x32_bf16 v[108:111], a[252:255], a[156:159], v[108:111]\n" \
"ds_read_b128 a[244:247], v247 offset:14336\n" \
"v_add_f32 v243, v243, v61\n" \
"v_mfma_f32_16x16x32_bf16 v[116:119], a[248:251], a[168:171], v[116:119]\n" \
"v_exp_f32 v62, v62\n" \
"v_mfma_f32_16x16x32_bf16 v[116:119], a[252:255], a[172:175], v[116:119]\n" \
"v_add_f32 v243, v243, v62\n" \
"v_cvt_pk_bf16_f32 v58, v60, v61\n" \
"v_mfma_f32_16x16x32_bf16 v[124:127], a[248:251], a[184:187], v[124:127]\n" \
"s_waitcnt vmcnt(10) lgkmcnt(6)\n" \
"v_exp_f32 v63, v63\n" \
"v_mfma_f32_16x16x32_bf16 v[124:127], a[252:255], a[188:191], v[124:127]\n" \
"s_barrier\n" \
"ds_read_b128 a[248:251], v247 offset:7168\n" \
"v_add_f32 v243, v243, v63\n" \
"v_mfma_f32_16x16x32_bf16 a[0:3], a[192:195], v[0:3], a[0:3]\n" \
"v_exp_f32 v64, v64\n" \
"v_mfma_f32_16x16x32_bf16 a[0:3], a[196:199], v[32:35], a[0:3]\n" \
"v_cvt_pk_bf16_f32 v59, v62, v63\n" \
"v_add_f32 v240, v240, v64\n" \
"v_mfma_f32_16x16x32_bf16 a[32:35], a[192:195], v[8:11], a[32:35]\n" \
"s_mov_b32 m0, s73\n" \
"v_exp_f32 v65, v65\n" \
"v_mfma_f32_16x16x32_bf16 a[32:35], a[196:199], v[40:43], a[32:35]\n" \
"ds_read_b128 a[252:255], v247 offset:15360\n" \
"global_load_lds_dwordx4 v250, s[36:37] offset:0 sc1\n" \
"v_add_f32 v240, v240, v65\n" \
"v_mfma_f32_16x16x32_bf16 v[128:131], a[192:195], v[16:19], v[128:131]\n" \
"v_exp_f32 v66, v66\n" \
"v_mfma_f32_16x16x32_bf16 v[128:131], a[196:199], v[48:51], v[128:131]\n" \
"v_add_f32 v240, v240, v66\n" \
"v_cvt_pk_bf16_f32 v64, v64, v65\n" \
"v_mfma_f32_16x16x32_bf16 v[160:163], a[192:195], v[24:27], v[160:163]\n" \
"v_exp_f32 v67, v67\n" \
"v_mfma_f32_16x16x32_bf16 v[160:163], a[196:199], v[56:59], v[160:163]\n" \
"ds_read_b128 a[192:195], v246 offset:0\n" \
"v_add_f32 v240, v240, v67\n" \
"v_mfma_f32_16x16x32_bf16 a[4:7], a[200:203], v[0:3], a[4:7]\n" \
"v_exp_f32 v68, v68\n" \
"v_mfma_f32_16x16x32_bf16 a[4:7], a[204:207], v[32:35], a[4:7]\n" \
"v_add_f32 v240, v240, v68\n" \
"v_cvt_pk_bf16_f32 v65, v66, v67\n" \
"v_mfma_f32_16x16x32_bf16 a[36:39], a[200:203], v[8:11], a[36:39]\n" \
"v_exp_f32 v69, v69\n" \
"v_mfma_f32_16x16x32_bf16 a[36:39], a[204:207], v[40:43], a[36:39]\n" \
"ds_read_b128 a[196:199], v246 offset:2048\n" \
"v_add_f32 v240, v240, v69\n" \
"v_mfma_f32_16x16x32_bf16 v[132:135], a[200:203], v[16:19], v[132:135]\n" \
"v_exp_f32 v70, v70\n" \
"v_mfma_f32_16x16x32_bf16 v[132:135], a[204:207], v[48:51], v[132:135]\n" \
"v_add_f32 v240, v240, v70\n" \
"v_cvt_pk_bf16_f32 v66, v68, v69\n" \
"v_mfma_f32_16x16x32_bf16 v[164:167], a[200:203], v[24:27], v[164:167]\n" \
"v_exp_f32 v71, v71\n" \
"v_mfma_f32_16x16x32_bf16 v[164:167], a[204:207], v[56:59], v[164:167]\n" \
"ds_read_b128 a[200:203], v246 offset:256\n" \
"global_load_lds_dwordx4 v250, s[36:37] offset:1024 sc1\n" \
"v_add_f32 v240, v240, v71\n" \
"v_mfma_f32_16x16x32_bf16 a[8:11], a[208:211], v[0:3], a[8:11]\n" \
"v_exp_f32 v72, v72\n" \
"v_mfma_f32_16x16x32_bf16 a[8:11], a[212:215], v[32:35], a[8:11]\n" \
"v_add_f32 v241, v241, v72\n" \
"v_cvt_pk_bf16_f32 v67, v70, v71\n" \
"v_mfma_f32_16x16x32_bf16 a[40:43], a[208:211], v[8:11], a[40:43]\n" \
"v_exp_f32 v73, v73\n" \
"v_mfma_f32_16x16x32_bf16 a[40:43], a[212:215], v[40:43], a[40:43]\n" \
"ds_read_b128 a[204:207], v246 offset:2304\n" \
"v_add_f32 v241, v241, v73\n" \
"v_mfma_f32_16x16x32_bf16 v[136:139], a[208:211], v[16:19], v[136:139]\n" \
"v_exp_f32 v74, v74\n" \
"v_mfma_f32_16x16x32_bf16 v[136:139], a[212:215], v[48:51], v[136:139]\n" \
"v_add_f32 v241, v241, v74\n" \
"v_cvt_pk_bf16_f32 v72, v72, v73\n" \
"v_mfma_f32_16x16x32_bf16 v[168:171], a[208:211], v[24:27], v[168:171]\n" \
"v_exp_f32 v75, v75\n" \
"v_mfma_f32_16x16x32_bf16 v[168:171], a[212:215], v[56:59], v[168:171]\n" \
"ds_read_b128 a[208:211], v246 offset:8192\n" \
"v_add_f32 v241, v241, v75\n" \
"v_mfma_f32_16x16x32_bf16 a[12:15], a[216:219], v[0:3], a[12:15]\n" \
"v_exp_f32 v76, v76\n" \
"v_mfma_f32_16x16x32_bf16 a[12:15], a[220:223], v[32:35], a[12:15]\n" \
"v_add_f32 v241, v241, v76\n" \
"v_cvt_pk_bf16_f32 v73, v74, v75\n" \
"v_mfma_f32_16x16x32_bf16 a[44:47], a[216:219], v[8:11], a[44:47]\n" \
"v_exp_f32 v77, v77\n" \
"v_mfma_f32_16x16x32_bf16 a[44:47], a[220:223], v[40:43], a[44:47]\n" \
"ds_read_b128 a[212:215], v246 offset:10240\n" \
"global_load_lds_dwordx4 v250, s[36:37] offset:2048 sc1\n" \
"v_add_f32 v241, v241, v77\n" \
"v_mfma_f32_16x16x32_bf16 v[140:143], a[216:219], v[16:19], v[140:143]\n" \
"v_exp_f32 v78, v78\n" \
"v_mfma_f32_16x16x32_bf16 v[140:143], a[220:223], v[48:51], v[140:143]\n" \
"v_add_f32 v241, v241, v78\n" \
"v_cvt_pk_bf16_f32 v74, v76, v77\n" \
"v_mfma_f32_16x16x32_bf16 v[172:175], a[216:219], v[24:27], v[172:175]\n" \
"s_waitcnt lgkmcnt(6)\n" \
"v_exp_f32 v79, v79\n" \
"v_mfma_f32_16x16x32_bf16 v[172:175], a[220:223], v[56:59], v[172:175]\n" \
"ds_read_b128 a[216:219], v246 offset:8448\n" \
"v_add_f32 v241, v241, v79\n" \
"v_mfma_f32_16x16x32_bf16 a[16:19], a[224:227], v[0:3], a[16:19]\n" \
"v_exp_f32 v80, v80\n" \
"v_mfma_f32_16x16x32_bf16 a[16:19], a[228:231], v[32:35], a[16:19]\n" \
"v_add_f32 v242, v242, v80\n" \
"v_cvt_pk_bf16_f32 v75, v78, v79\n" \
"v_mfma_f32_16x16x32_bf16 a[48:51], a[224:227], v[8:11], a[48:51]\n" \
"v_exp_f32 v81, v81\n" \
"v_mfma_f32_16x16x32_bf16 a[48:51], a[228:231], v[40:43], a[48:51]\n" \
"ds_read_b128 a[220:223], v246 offset:10496\n" \
"v_add_f32 v242, v242, v81\n" \
"v_mfma_f32_16x16x32_bf16 v[144:147], a[224:227], v[16:19], v[144:147]\n" \
"v_exp_f32 v82, v82\n" \
"v_mfma_f32_16x16x32_bf16 v[144:147], a[228:231], v[48:51], v[144:147]\n" \
"v_add_f32 v242, v242, v82\n" \
"v_cvt_pk_bf16_f32 v80, v80, v81\n" \
"v_mfma_f32_16x16x32_bf16 v[176:179], a[224:227], v[24:27], v[176:179]\n" \
"v_exp_f32 v83, v83\n" \
"v_mfma_f32_16x16x32_bf16 v[176:179], a[228:231], v[56:59], v[176:179]\n" \
"ds_read_b128 a[224:227], v246 offset:4096\n" \
"global_load_lds_dwordx4 v250, s[36:37] offset:3072 sc1\n" \
"v_add_f32 v242, v242, v83\n" \
"v_mfma_f32_16x16x32_bf16 a[20:23], a[232:235], v[0:3], a[20:23]\n" \
"s_add_u32 s36, s36, 17408\n" \
"v_exp_f32 v84, v84\n" \
"v_mfma_f32_16x16x32_bf16 a[20:23], a[236:239], v[32:35], a[20:23]\n" \
"v_add_f32 v242, v242, v84\n" \
"v_cvt_pk_bf16_f32 v81, v82, v83\n" \
"v_mfma_f32_16x16x32_bf16 a[52:55], a[232:235], v[8:11], a[52:55]\n" \
"v_exp_f32 v85, v85\n" \
"v_mfma_f32_16x16x32_bf16 a[52:55], a[236:239], v[40:43], a[52:55]\n" \
"ds_read_b128 a[228:231], v246 offset:6144\n" \
"v_add_f32 v242, v242, v85\n" \
"v_mfma_f32_16x16x32_bf16 v[148:151], a[232:235], v[16:19], v[148:151]\n" \
"v_exp_f32 v86, v86\n" \
"v_mfma_f32_16x16x32_bf16 v[148:151], a[236:239], v[48:51], v[148:151]\n" \
"v_add_f32 v242, v242, v86\n" \
"v_cvt_pk_bf16_f32 v82, v84, v85\n" \
"v_mfma_f32_16x16x32_bf16 v[180:183], a[232:235], v[24:27], v[180:183]\n" \
"v_exp_f32 v87, v87\n" \
"v_mfma_f32_16x16x32_bf16 v[180:183], a[236:239], v[56:59], v[180:183]\n" \
"ds_read_b128 a[232:235], v246 offset:4352\n" \
"v_add_f32 v242, v242, v87\n" \
"v_mfma_f32_16x16x32_bf16 a[24:27], a[240:243], v[0:3], a[24:27]\n" \
"v_exp_f32 v88, v88\n" \
"v_mfma_f32_16x16x32_bf16 a[24:27], a[244:247], v[32:35], a[24:27]\n" \
"v_add_f32 v243, v243, v88\n" \
"v_cvt_pk_bf16_f32 v83, v86, v87\n" \
"v_mfma_f32_16x16x32_bf16 a[56:59], a[240:243], v[8:11], a[56:59]\n" \
"v_exp_f32 v89, v89\n" \
"v_mfma_f32_16x16x32_bf16 a[56:59], a[244:247], v[40:43], a[56:59]\n" \
"ds_read_b128 a[236:239], v246 offset:6400\n" \
"v_add_f32 v243, v243, v89\n" \
"v_mfma_f32_16x16x32_bf16 v[152:155], a[240:243], v[16:19], v[152:155]\n" \
"v_exp_f32 v90, v90\n" \
"v_mfma_f32_16x16x32_bf16 v[152:155], a[244:247], v[48:51], v[152:155]\n" \
"v_add_f32 v243, v243, v90\n" \
"v_cvt_pk_bf16_f32 v88, v88, v89\n" \
"v_mfma_f32_16x16x32_bf16 v[184:187], a[240:243], v[24:27], v[184:187]\n" \
"v_exp_f32 v91, v91\n" \
"v_mfma_f32_16x16x32_bf16 v[184:187], a[244:247], v[56:59], v[184:187]\n" \
"ds_read_b128 a[240:243], v246 offset:12288\n" \
"v_add_f32 v243, v243, v91\n" \
"v_mfma_f32_16x16x32_bf16 a[28:31], a[248:251], v[0:3], a[28:31]\n" \
"v_exp_f32 v92, v92\n" \
"v_mfma_f32_16x16x32_bf16 a[28:31], a[252:255], v[32:35], a[28:31]\n" \
"v_add_f32 v243, v243, v92\n" \
"v_cvt_pk_bf16_f32 v89, v90, v91\n" \
"v_mfma_f32_16x16x32_bf16 a[60:63], a[248:251], v[8:11], a[60:63]\n" \
"v_exp_f32 v93, v93\n" \
"v_mfma_f32_16x16x32_bf16 a[60:63], a[252:255], v[40:43], a[60:63]\n" \
"ds_read_b128 a[244:247], v246 offset:14336\n" \
"v_add_f32 v243, v243, v93\n" \
"v_mfma_f32_16x16x32_bf16 v[156:159], a[248:251], v[16:19], v[156:159]\n" \
"v_exp_f32 v94, v94\n" \
"v_mfma_f32_16x16x32_bf16 v[156:159], a[252:255], v[48:51], v[156:159]\n" \
"v_add_f32 v243, v243, v94\n" \
"v_cvt_pk_bf16_f32 v90, v92, v93\n" \
"v_mfma_f32_16x16x32_bf16 v[188:191], a[248:251], v[24:27], v[188:191]\n" \
"s_waitcnt lgkmcnt(6)\n" \
"v_exp_f32 v95, v95\n" \
"v_mfma_f32_16x16x32_bf16 v[188:191], a[252:255], v[56:59], v[188:191]\n" \
"ds_read_b128 a[248:251], v246 offset:12544\n" \
"v_add_f32 v243, v243, v95\n" \
"v_mfma_f32_16x16x32_bf16 v[0:3], a[192:195], a[128:131], v[224:227]\n" \
"v_exp_f32 v96, v96\n" \
"v_mfma_f32_16x16x32_bf16 v[0:3], a[196:199], a[132:135], v[0:3]\n" \
"v_cvt_pk_bf16_f32 v91, v94, v95\n" \
"v_add_f32 v240, v240, v96\n" \
"v_exp_f32 v97, v97\n" \
"v_mfma_f32_16x16x32_bf16 v[8:11], a[192:195], a[144:147], v[228:231]\n" \
"s_add_u32 m0, s74, 0x4000\n" \
"v_add_f32 v240, v240, v97\n" \
"v_exp_f32 v98, v98\n" \
"v_mfma_f32_16x16x32_bf16 v[8:11], a[196:199], a[148:151], v[8:11]\n" \
"ds_read_b128 a[252:255], v246 offset:14592\n" \
"global_load_lds_dwordx4 v250, s[38:39] offset:0 sc1\n" \
"v_add_f32 v240, v240, v98\n" \
"v_cvt_pk_bf16_f32 v96, v96, v97\n" \
"v_exp_f32 v99, v99\n" \
"v_mfma_f32_16x16x32_bf16 v[16:19], a[192:195], a[160:163], v[232:235]\n" \
"v_add_f32 v240, v240, v99\n" \
"v_exp_f32 v100, v100\n" \
"v_mfma_f32_16x16x32_bf16 v[16:19], a[196:199], a[164:167], v[16:19]\n" \
"v_add_f32 v240, v240, v100\n" \
"v_cvt_pk_bf16_f32 v97, v98, v99\n" \
"v_mfma_f32_16x16x32_bf16 v[24:27], a[192:195], a[176:179], v[236:239]\n" \
"v_exp_f32 v101, v101\n" \
"v_mfma_f32_16x16x32_bf16 v[24:27], a[196:199], a[180:183], v[24:27]\n" \
"ds_read_b128 a[192:195], v247 offset:16384\n" \
"v_add_f32 v240, v240, v101\n" \
"v_mfma_f32_16x16x32_bf16 v[4:7], a[200:203], a[128:131], v[224:227]\n" \
"v_exp_f32 v102, v102\n" \
"v_mfma_f32_16x16x32_bf16 v[4:7], a[204:207], a[132:135], v[4:7]\n" \
"v_add_f32 v240, v240, v102\n" \
"v_cvt_pk_bf16_f32 v98, v100, v101\n" \
"v_mfma_f32_16x16x32_bf16 v[12:15], a[200:203], a[144:147], v[228:231]\n" \
"v_exp_f32 v103, v103\n" \
"v_mfma_f32_16x16x32_bf16 v[12:15], a[204:207], a[148:151], v[12:15]\n" \
"ds_read_b128 a[196:199], v247 offset:24576\n" \
"v_add_f32 v240, v240, v103\n" \
"v_mfma_f32_16x16x32_bf16 v[20:23], a[200:203], a[160:163], v[232:235]\n" \
"v_exp_f32 v104, v104\n" \
"v_mfma_f32_16x16x32_bf16 v[20:23], a[204:207], a[164:167], v[20:23]\n" \
"v_add_f32 v241, v241, v104\n" \
"v_cvt_pk_bf16_f32 v99, v102, v103\n" \
"v_mfma_f32_16x16x32_bf16 v[28:31], a[200:203], a[176:179], v[236:239]\n" \
"v_exp_f32 v105, v105\n" \
"v_mfma_f32_16x16x32_bf16 v[28:31], a[204:207], a[180:183], v[28:31]\n" \
"ds_read_b128 a[200:203], v247 offset:17408\n" \
"global_load_lds_dwordx4 v250, s[38:39] offset:1024 sc1\n" \
"v_add_f32 v241, v241, v105\n" \
"v_mfma_f32_16x16x32_bf16 v[32:35], a[208:211], a[128:131], v[224:227]\n" \
"v_exp_f32 v106, v106\n" \
"v_mfma_f32_16x16x32_bf16 v[32:35], a[212:215], a[132:135], v[32:35]\n" \
"v_add_f32 v241, v241, v106\n" \
"v_cvt_pk_bf16_f32 v104, v104, v105\n" \
"v_mfma_f32_16x16x32_bf16 v[40:43], a[208:211], a[144:147], v[228:231]\n" \
"v_exp_f32 v107, v107\n" \
"v_mfma_f32_16x16x32_bf16 v[40:43], a[212:215], a[148:151], v[40:43]\n" \
"ds_read_b128 a[204:207], v247 offset:25600\n" \
"v_add_f32 v241, v241, v107\n" \
"v_mfma_f32_16x16x32_bf16 v[48:51], a[208:211], a[160:163], v[232:235]\n" \
"v_exp_f32 v108, v108\n" \
"v_mfma_f32_16x16x32_bf16 v[48:51], a[212:215], a[164:167], v[48:51]\n" \
"v_add_f32 v241, v241, v108\n" \
"v_cvt_pk_bf16_f32 v105, v106, v107\n" \
"v_mfma_f32_16x16x32_bf16 v[56:59], a[208:211], a[176:179], v[236:239]\n" \
"v_exp_f32 v109, v109\n" \
"v_mfma_f32_16x16x32_bf16 v[56:59], a[212:215], a[180:183], v[56:59]\n" \
"ds_read_b128 a[208:211], v247 offset:18432\n" \
"v_add_f32 v241, v241, v109\n" \
"v_mfma_f32_16x16x32_bf16 v[36:39], a[216:219], a[128:131], v[224:227]\n" \
"v_exp_f32 v110, v110\n" \
"v_mfma_f32_16x16x32_bf16 v[36:39], a[220:223], a[132:135], v[36:39]\n" \
"v_add_f32 v241, v241, v110\n" \
"v_cvt_pk_bf16_f32 v106, v108, v109\n" \
"v_mfma_f32_16x16x32_bf16 v[44:47], a[216:219], a[144:147], v[228:231]\n" \
"v_exp_f32 v111, v111\n" \
"v_mfma_f32_16x16x32_bf16 v[44:47], a[220:223], a[148:151], v[44:47]\n" \
"ds_read_b128 a[212:215], v247 offset:26624\n" \
"global_load_lds_dwordx4 v250, s[38:39] offset:2048 sc1\n" \
"v_add_f32 v241, v241, v111\n" \
"v_mfma_f32_16x16x32_bf16 v[52:55], a[216:219], a[160:163], v[232:235]\n" \
"v_exp_f32 v112, v112\n" \
"v_mfma_f32_16x16x32_bf16 v[52:55], a[220:223], a[164:167], v[52:55]\n" \
"v_add_f32 v242, v242, v112\n" \
"v_cvt_pk_bf16_f32 v107, v110, v111\n" \
"v_mfma_f32_16x16x32_bf16 v[60:63], a[216:219], a[176:179], v[236:239]\n" \
"s_waitcnt lgkmcnt(6)\n" \
"v_exp_f32 v113, v113\n" \
"v_mfma_f32_16x16x32_bf16 v[60:63], a[220:223], a[180:183], v[60:63]\n" \
"ds_read_b128 a[216:219], v247 offset:19456\n" \
"v_add_f32 v242, v242, v113\n" \
"v_mfma_f32_16x16x32_bf16 v[0:3], a[224:227], a[136:139], v[0:3]\n" \
"v_exp_f32 v114, v114\n" \
"v_mfma_f32_16x16x32_bf16 v[0:3], a[228:231], a[140:143], v[0:3]\n" \
"v_add_f32 v242, v242, v114\n" \
"v_cvt_pk_bf16_f32 v112, v112, v113\n" \
"v_mfma_f32_16x16x32_bf16 v[8:11], a[224:227], a[152:155], v[8:11]\n" \
"v_exp_f32 v115, v115\n" \
"v_mfma_f32_16x16x32_bf16 v[8:11], a[228:231], a[156:159], v[8:11]\n" \
"ds_read_b128 a[220:223], v247 offset:27648\n" \
"v_add_f32 v242, v242, v115\n" \
"v_mfma_f32_16x16x32_bf16 v[16:19], a[224:227], a[168:171], v[16:19]\n" \
"v_exp_f32 v116, v116\n" \
"v_mfma_f32_16x16x32_bf16 v[16:19], a[228:231], a[172:175], v[16:19]\n" \
"v_add_f32 v242, v242, v116\n" \
"v_cvt_pk_bf16_f32 v113, v114, v115\n" \
"v_mfma_f32_16x16x32_bf16 v[24:27], a[224:227], a[184:187], v[24:27]\n" \
"v_exp_f32 v117, v117\n" \
"v_mfma_f32_16x16x32_bf16 v[24:27], a[228:231], a[188:191], v[24:27]\n" \
"ds_read_b128 a[224:227], v247 offset:20480\n" \
"global_load_lds_dwordx4 v250, s[38:39] offset:3072 sc1\n" \
"v_add_f32 v242, v242, v117\n" \
"v_mfma_f32_16x16x32_bf16 v[4:7], a[232:235], a[136:139], v[4:7]\n" \
"s_add_u32 s38, s38, 17408\n" \
"v_exp_f32 v118, v118\n" \
"v_mfma_f32_16x16x32_bf16 v[4:7], a[236:239], a[140:143], v[4:7]\n" \
"v_add_f32 v242, v242, v118\n" \
"v_cvt_pk_bf16_f32 v114, v116, v117\n" \
"v_mfma_f32_16x16x32_bf16 v[12:15], a[232:235], a[152:155], v[12:15]\n" \
"v_exp_f32 v119, v119\n" \
"v_mfma_f32_16x16x32_bf16 v[12:15], a[236:239], a[156:159], v[12:15]\n" \
"ds_read_b128 a[228:231], v247 offset:28672\n" \
"v_add_f32 v242, v242, v119\n" \
"v_mfma_f32_16x16x32_bf16 v[20:23], a[232:235], a[168:171], v[20:23]\n" \
"global_load_dword v253, v252, s[40:41]\n" \
"v_mfma_f32_16x16x32_bf16 v[20:23], a[236:239], a[172:175], v[20:23]\n" \
"s_sub_u32 s80, s80, 1\n" \
"s_cmp_gt_i32 s80, 0\n" \
"s_cselect_b32 s81, 0x80000, 0\n" \
"v_mfma_f32_16x16x32_bf16 v[28:31], a[232:235], a[184:187], v[28:31]\n" \
"s_add_u32 s40, s40, s81\n" \
"s_addc_u32 s41, s41, 0\n" \
"s_load_dwordx8 s[44:51], s[56:57], s79 glc\n" \
"v_mfma_f32_16x16x32_bf16 v[28:31], a[236:239], a[188:191], v[28:31]\n" \
"s_xor_b32 s74, s74, 0x8000\n" \
"ds_read_b128 a[232:235], v247 offset:21504\n" \
"s_sub_u32 s75, s75, 1\n" \
"v_mfma_f32_16x16x32_bf16 v[32:35], a[240:243], a[136:139], v[32:35]\n" \
"s_cmp_lg_u32 s75, 0\n" \
"v_exp_f32 v120, v120\n" \
"v_mfma_f32_16x16x32_bf16 v[32:35], a[244:247], a[140:143], v[32:35]\n" \
"v_add_f32 v243, v243, v120\n" \
"v_cvt_pk_bf16_f32 v115, v118, v119\n" \
"v_mfma_f32_16x16x32_bf16 v[40:43], a[240:243], a[152:155], v[40:43]\n" \
"v_exp_f32 v121, v121\n" \
"v_mfma_f32_16x16x32_bf16 v[40:43], a[244:247], a[156:159], v[40:43]\n" \
"ds_read_b128 a[236:239], v247 offset:29696\n" \
"v_add_f32 v243, v243, v121\n" \
"v_mfma_f32_16x16x32_bf16 v[48:51], a[240:243], a[168:171], v[48:51]\n" \
"v_exp_f32 v122, v122\n" \
"v_mfma_f32_16x16x32_bf16 v[48:51], a[244:247], a[172:175], v[48:51]\n" \
"v_add_f32 v243, v243, v122\n" \
"v_cvt_pk_bf16_f32 v120, v120, v121\n" \
"v_mfma_f32_16x16x32_bf16 v[56:59], a[240:243], a[184:187], v[56:59]\n" \
"v_exp_f32 v123, v123\n" \
"v_mfma_f32_16x16x32_bf16 v[56:59], a[244:247], a[188:191], v[56:59]\n" \
"ds_read_b128 a[240:243], v247 offset:22528\n" \
"v_add_f32 v243, v243, v123\n" \
"v_mfma_f32_16x16x32_bf16 v[36:39], a[248:251], a[136:139], v[36:39]\n" \
"v_exp_f32 v124, v124\n" \
"v_mfma_f32_16x16x32_bf16 v[36:39], a[252:255], a[140:143], v[36:39]\n" \
"v_add_f32 v243, v243, v124\n" \
"v_cvt_pk_bf16_f32 v121, v122, v123\n" \
"v_mfma_f32_16x16x32_bf16 v[44:47], a[248:251], a[152:155], v[44:47]\n" \
"v_exp_f32 v125, v125\n" \
"v_mfma_f32_16x16x32_bf16 v[44:47], a[252:255], a[156:159], v[44:47]\n" \
"ds_read_b128 a[244:247], v247 offset:30720\n" \
"v_add_f32 v243, v243, v125\n" \
"v_mfma_f32_16x16x32_bf16 v[52:55], a[248:251], a[168:171], v[52:55]\n" \
"v_exp_f32 v126, v126\n" \
"v_mfma_f32_16x16x32_bf16 v[52:55], a[252:255], a[172:175], v[52:55]\n" \
"v_add_f32 v243, v243, v126\n" \
"v_cvt_pk_bf16_f32 v122, v124, v125\n" \
"v_mfma_f32_16x16x32_bf16 v[60:63], a[248:251], a[184:187], v[60:63]\n" \
"s_waitcnt vmcnt(10) lgkmcnt(6)\n" \
"v_exp_f32 v127, v127\n" \
"v_mfma_f32_16x16x32_bf16 v[60:63], a[252:255], a[188:191], v[60:63]\n" \
"s_barrier\n" \
"ds_read_b128 a[248:251], v247 offset:23552\n" \
"v_add_f32 v243, v243, v127\n" \
"s_cbranch_scc1 Lloop_%=\n" \
"v_mfma_f32_16x16x32_bf16 a[0:3], a[192:195], v[64:67], a[0:3]\n" \
"v_exp_f32 v8, v8\n" \
"v_mfma_f32_16x16x32_bf16 a[0:3], a[196:199], v[96:99], a[0:3]\n" \
"v_cvt_pk_bf16_f32 v123, v126, v127\n" \
"v_add_f32 v241, v241, v8\n" \
"v_exp_f32 v9, v9\n" \
"v_mfma_f32_16x16x32_bf16 a[32:35], a[192:195], v[72:75], a[32:35]\n" \
"s_add_u32 m0, s73, 0x4000\n" \
"v_add_f32 v241, v241, v9\n" \
"v_exp_f32 v10, v10\n" \
"v_mfma_f32_16x16x32_bf16 a[32:35], a[196:199], v[104:107], a[32:35]\n" \
"ds_read_b128 a[252:255], v247 offset:31744\n" \
"global_load_lds_dwordx4 v250, s[36:37] offset:0 sc1\n" \
"v_add_f32 v241, v241, v10\n" \
"v_cvt_pk_bf16_f32 v8, v8, v9\n" \
"v_exp_f32 v11, v11\n" \
"v_mfma_f32_16x16x32_bf16 v[128:131], a[192:195], v[80:83], v[128:131]\n" \
"v_xor_b32 v247, 0x8000, v247\n" \
"v_add_f32 v241, v241, v11\n" \
"v_exp_f32 v12, v12\n" \
"v_mfma_f32_16x16x32_bf16 v[128:131], a[196:199], v[112:115], v[128:131]\n" \
"v_add_f32 v241, v241, v12\n" \
"v_cvt_pk_bf16_f32 v9, v10, v11\n" \
"v_exp_f32 v13, v13\n" \
"v_mfma_f32_16x16x32_bf16 v[160:163], a[192:195], v[88:91], v[160:163]\n" \
"v_add_f32 v241, v241, v13\n" \
"v_exp_f32 v14, v14\n" \
"v_mfma_f32_16x16x32_bf16 v[160:163], a[196:199], v[120:123], v[160:163]\n" \
"ds_read_b128 a[192:195], v246 offset:16384\n" \
"v_add_f32 v241, v241, v14\n" \
"v_cvt_pk_bf16_f32 v10, v12, v13\n" \
"v_exp_f32 v15, v15\n" \
"v_mfma_f32_16x16x32_bf16 a[4:7], a[200:203], v[64:67], a[4:7]\n" \
"v_add_f32 v241, v241, v15\n" \
"v_exp_f32 v16, v16\n" \
"v_mfma_f32_16x16x32_bf16 a[4:7], a[204:207], v[96:99], a[4:7]\n" \
"v_add_f32 v242, v242, v16\n" \
"v_cvt_pk_bf16_f32 v11, v14, v15\n" \
"v_exp_f32 v17, v17\n" \
"v_mfma_f32_16x16x32_bf16 a[36:39], a[200:203], v[72:75], a[36:39]\n" \
"v_add_f32 v242, v242, v17\n" \
"v_exp_f32 v18, v18\n" \
"v_mfma_f32_16x16x32_bf16 a[36:39], a[204:207], v[104:107], a[36:39]\n" \
"ds_read_b128 a[196:199], v246 offset:18432\n" \
"v_add_f32 v242, v242, v18\n" \
"v_cvt_pk_bf16_f32 v16, v16, v17\n" \
"v_exp_f32 v19, v19\n" \
"v_mfma_f32_16x16x32_bf16 v[132:135], a[200:203], v[80:83], v[132:135]\n" \
"v_add_f32 v242, v242, v19\n" \
"v_exp_f32 v20, v20\n" \
"v_mfma_f32_16x16x32_bf16 v[132:135], a[204:207], v[112:115], v[132:135]\n" \
"v_add_f32 v242, v242, v20\n" \
"v_cvt_pk_bf16_f32 v17, v18, v19\n" \
"v_exp_f32 v21, v21\n" \
"v_mfma_f32_16x16x32_bf16 v[164:167], a[200:203], v[88:91], v[164:167]\n" \
"v_add_f32 v242, v242, v21\n" \
"v_exp_f32 v22, v22\n" \
"v_mfma_f32_16x16x32_bf16 v[164:167], a[204:207], v[120:123], v[164:167]\n" \
"ds_read_b128 a[200:203], v246 offset:16640\n" \
"global_load_lds_dwordx4 v250, s[36:37] offset:1024 sc1\n" \
"v_add_f32 v242, v242, v22\n" \
"v_cvt_pk_bf16_f32 v18, v20, v21\n" \
"v_exp_f32 v23, v23\n" \
"v_mfma_f32_16x16x32_bf16 a[8:11], a[208:211], v[64:67], a[8:11]\n" \
"v_add_f32 v242, v242, v23\n" \
"v_cvt_pk_bf16_f32 v19, v22, v23\n" \
"v_cmp_gt_i32 vcc, 0, v251\n" \
"v_cmp_gt_i32 s[64:65], 1, v251\n" \
"v_cmp_gt_i32 s[66:67], 2, v251\n" \
"v_cndmask_b32 v0, v0, v254, vcc\n" \
"v_mfma_f32_16x16x32_bf16 a[8:11], a[212:215], v[96:99], a[8:11]\n" \
"v_cmp_gt_i32 vcc, 3, v251\n" \
"v_cndmask_b32 v1, v1, v254, s[64:65]\n" \
"v_cmp_gt_i32 s[64:65], 16, v251\n" \
"v_cndmask_b32 v2, v2, v254, s[66:67]\n" \
"v_cmp_gt_i32 s[66:67], 17, v251\n" \
"v_cndmask_b32 v3, v3, v254, vcc\n" \
"v_mfma_f32_16x16x32_bf16 a[40:43], a[208:211], v[72:75], a[40:43]\n" \
"v_cmp_gt_i32 vcc, 18, v251\n" \
"v_cndmask_b32 v4, v4, v254, s[64:65]\n" \
"v_cmp_gt_i32 s[64:65], 19, v251\n" \
"v_cndmask_b32 v5, v5, v254, s[66:67]\n" \
"v_cmp_gt_i32 s[66:67], 32, v251\n" \
"v_cndmask_b32 v6, v6, v254, vcc\n" \
"v_mfma_f32_16x16x32_bf16 a[40:43], a[212:215], v[104:107], a[40:43]\n" \
"ds_read_b128 a[204:207], v246 offset:18688\n" \
"Lpg6195_%=:\n" \
"s_cbranch_execz Lpg7663_%=\n" \
"v_cmp_gt_i32 vcc, 33, v251\n" \
"v_mfma_f32_16x16x32_bf16 v[136:139], a[208:211], v[80:83], v[136:139]\n" \
"v_cndmask_b32 v7, v7, v254, s[64:65]\n" \
"v_cmp_gt_i32 s[64:65], 34, v251\n" \
"v_mfma_f32_16x16x32_bf16 v[136:139], a[212:215], v[112:115], v[136:139]\n" \
"v_cndmask_b32 v32, v32, v254, s[66:67]\n" \
"v_cmp_gt_i32 s[66:67], 35, v251\n" \
"v_mfma_f32_16x16x32_bf16 v[168:171], a[208:211], v[88:91], v[168:171]\n" \
"v_cndmask_b32 v33, v33, v254, vcc\n" \
"v_cmp_gt_i32 vcc, 48, v251\n" \
"v_mfma_f32_16x16x32_bf16 v[168:171], a[212:215], v[120:123], v[168:171]\n" \
"ds_read_b128 a[208:211], v246 offset:24576\n" \
"v_cndmask_b32 v34, v34, v254, s[64:65]\n" \
"v_mfma_f32_16x16x32_bf16 a[12:15], a[216:219], v[64:67], a[12:15]\n" \
"v_cmp_gt_i32 s[64:65], 49, v251\n" \
"v_cndmask_b32 v35, v35, v254, s[66:67]\n" \
"v_mfma_f32_16x16x32_bf16 a[12:15], a[220:223], v[96:99], a[12:15]\n" \
"v_cmp_gt_i32 s[66:67], 50, v251\n" \
"v_cndmask_b32 v36, v36, v254, vcc\n" \
"v_mfma_f32_16x16x32_bf16 a[44:47], a[216:219], v[72:75], a[44:47]\n" \
"v_cmp_gt_i32 vcc, 51, v251\n" \
"v_cndmask_b32 v37, v37, v254, s[64:65]\n" \
"v_mfma_f32_16x16x32_bf16 a[44:47], a[220:223], v[104:107], a[44:47]\n" \
"ds_read_b128 a[212:215], v246 offset:26624\n" \
"global_load_lds_dwordx4 v250, s[36:37] offset:2048 sc1\n" \
"v_mfma_f32_16x16x32_bf16 v[140:143], a[216:219], v[80:83], v[140:143]\n" \
"v_cndmask_b32 v38, v38, v254, s[66:67]\n" \
"v_cndmask_b32 v39, v39, v254, vcc\n" \
"v_mfma_f32_16x16x32_bf16 v[140:143], a[220:223], v[112:115], v[140:143]\n" \
"v_exp_f32 v0, v0\n" \
"v_mfma_f32_16x16x32_bf16 v[172:175], a[216:219], v[88:91], v[172:175]\n" \
"s_waitcnt lgkmcnt(6)\n" \
"v_add_f32 v240, v240, v0\n" \
"v_exp_f32 v1, v1\n" \
"v_mfma_f32_16x16x32_bf16 v[172:175], a[220:223], v[120:123], v[172:175]\n" \
"ds_read_b128 a[216:219], v246 offset:24832\n" \
"v_add_f32 v240, v240, v1\n" \
"v_mfma_f32_16x16x32_bf16 a[16:19], a[224:227], v[64:67], a[16:19]\n" \
"v_exp_f32 v2, v2\n" \
"v_mfma_f32_16x16x32_bf16 a[16:19], a[228:231], v[96:99], a[16:19]\n" \
"v_add_f32 v240, v240, v2\n" \
"v_cvt_pk_bf16_f32 v0, v0, v1\n" \
"v_mfma_f32_16x16x32_bf16 a[48:51], a[224:227], v[72:75], a[48:51]\n" \
"v_exp_f32 v3, v3\n" \
"v_mfma_f32_16x16x32_bf16 a[48:51], a[228:231], v[104:107], a[48:51]\n" \
"ds_read_b128 a[220:223], v246 offset:26880\n" \
"v_add_f32 v240, v240, v3\n" \
"v_exp_f32 v4, v4\n" \
"v_mfma_f32_16x16x32_bf16 v[144:147], a[224:227], v[80:83], v[144:147]\n" \
"v_add_f32 v240, v240, v4\n" \
"v_cvt_pk_bf16_f32 v1, v2, v3\n" \
"v_mfma_f32_16x16x32_bf16 v[144:147], a[228:231], v[112:115], v[144:147]\n" \
"v_exp_f32 v5, v5\n" \
"v_mfma_f32_16x16x32_bf16 v[176:179], a[224:227], v[88:91], v[176:179]\n" \
"global_load_lds_dwordx4 v250, s[36:37] offset:3072 sc1\n" \
"s_add_u32 s36, s36, 17408\n" \
"v_mfma_f32_16x16x32_bf16 v[176:179], a[228:231], v[120:123], v[176:179]\n" \
"s_xor_b32 s73, s73, 0x8000\n" \
"ds_read_b128 a[224:227], v246 offset:20480\n" \
"s_xor_b64 s[52:53], s[44:45], s[42:43]\n" \
"v_mfma_f32_16x16x32_bf16 a[20:23], a[232:235], v[64:67], a[20:23]\n" \
"s_xor_b64 s[54:55], s[46:47], s[42:43]\n" \
"s_or_b64 s[52:53], s[52:53], s[54:55]\n" \
"s_xor_b64 s[54:55], s[48:49], s[42:43]\n" \
"v_mfma_f32_16x16x32_bf16 a[20:23], a[236:239], v[96:99], a[20:23]\n" \
"s_or_b64 s[52:53], s[52:53], s[54:55]\n" \
"s_xor_b64 s[54:55], s[50:51], s[42:43]\n" \
"s_or_b64 s[52:53], s[52:53], s[54:55]\n" \
"v_mfma_f32_16x16x32_bf16 a[52:55], a[232:235], v[72:75], a[52:55]\n" \
"s_and_b64 s[52:53], s[52:53], s[8:9]\n" \
"s_cbranch_scc1 Lrs1_%=\n" \
"Lrr1_%=:\n" \
"v_mfma_f32_16x16x32_bf16 a[52:55], a[236:239], v[104:107], a[52:55]\n" \
"s_mov_b64 s[8:9], s[10:11]\n" \
"ds_read_b128 a[228:231], v246 offset:22528\n" \
"v_add_f32 v240, v240, v5\n" \
"v_mfma_f32_16x16x32_bf16 v[148:151], a[232:235], v[80:83], v[148:151]\n" \
"v_exp_f32 v6, v6\n" \
"v_mfma_f32_16x16x32_bf16 v[148:151], a[236:239], v[112:115], v[148:151]\n" \
"v_add_f32 v240, v240, v6\n" \
"v_cvt_pk_bf16_f32 v2, v4, v5\n" \
"v_mfma_f32_16x16x32_bf16 v[180:183], a[232:235], v[88:91], v[180:183]\n" \
"v_exp_f32 v7, v7\n" \
"v_mfma_f32_16x16x32_bf16 v[180:183], a[236:239], v[120:123], v[180:183]\n" \
"ds_read_b128 a[232:235], v246 offset:20736\n" \
"v_add_f32 v240, v240, v7\n" \
"v_mfma_f32_16x16x32_bf16 a[24:27], a[240:243], v[64:67], a[24:27]\n" \
"v_exp_f32 v24, v24\n" \
"v_mfma_f32_16x16x32_bf16 a[24:27], a[244:247], v[96:99], a[24:27]\n" \
"v_add_f32 v243, v243, v24\n" \
"v_cvt_pk_bf16_f32 v3, v6, v7\n" \
"v_mfma_f32_16x16x32_bf16 a[56:59], a[240:243], v[72:75], a[56:59]\n" \
"v_exp_f32 v25, v25\n" \
"v_mfma_f32_16x16x32_bf16 a[56:59], a[244:247], v[104:107], a[56:59]\n" \
"ds_read_b128 a[236:239], v246 offset:22784\n" \
"v_add_f32 v243, v243, v25\n" \
"v_mfma_f32_16x16x32_bf16 v[152:155], a[240:243], v[80:83], v[152:155]\n" \
"v_exp_f32 v26, v26\n" \
"v_mfma_f32_16x16x32_bf16 v[152:155], a[244:247], v[112:115], v[152:155]\n" \
"v_add_f32 v243, v243, v26\n" \
"v_cvt_pk_bf16_f32 v24, v24, v25\n" \
"v_mfma_f32_16x16x32_bf16 v[184:187], a[240:243], v[88:91], v[184:187]\n" \
"v_exp_f32 v27, v27\n" \
"v_mfma_f32_16x16x32_bf16 v[184:187], a[244:247], v[120:123], v[184:187]\n" \
"ds_read_b128 a[240:243], v246 offset:28672\n" \
"v_add_f32 v243, v243, v27\n" \
"v_mfma_f32_16x16x32_bf16 a[28:31], a[248:251], v[64:67], a[28:31]\n" \
"v_exp_f32 v28, v28\n" \
"v_mfma_f32_16x16x32_bf16 a[28:31], a[252:255], v[96:99], a[28:31]\n" \
"v_add_f32 v243, v243, v28\n" \
"v_cvt_pk_bf16_f32 v25, v26, v27\n" \
"v_mfma_f32_16x16x32_bf16 a[60:63], a[248:251], v[72:75], a[60:63]\n" \
"v_exp_f32 v29, v29\n" \
"v_mfma_f32_16x16x32_bf16 a[60:63], a[252:255], v[104:107], a[60:63]\n" \
"ds_read_b128 a[244:247], v246 offset:30720\n" \
"v_add_f32 v243, v243, v29\n" \
"v_mfma_f32_16x16x32_bf16 v[156:159], a[248:251], v[80:83], v[156:159]\n" \
"v_exp_f32 v30, v30\n" \
"v_mfma_f32_16x16x32_bf16 v[156:159], a[252:255], v[112:115], v[156:159]\n" \
"v_add_f32 v243, v243, v30\n" \
"v_cvt_pk_bf16_f32 v26, v28, v29\n" \
"v_mfma_f32_16x16x32_bf16 v[188:191], a[248:251], v[88:91], v[188:191]\n" \
"s_waitcnt lgkmcnt(6)\n" \
"v_exp_f32 v31, v31\n" \
"v_mfma_f32_16x16x32_bf16 v[188:191], a[252:255], v[120:123], v[188:191]\n" \
"ds_read_b128 a[248:251], v246 offset:28928\n" \
"v_add_f32 v243, v243, v31\n" \
"LA1l_%=:\n" \
"Lpg6324_%=:\n" \
"s_cbranch_execz Lpg13682_%=\n" \
"s_getpc_b64 s[40:41]\n" \
"Lpd_%=:\n" \
"s_lshl_b32 s80, s62, 2\n" \
"s_add_u32 s80, s80, s61\n" \
"s_and_b32 s80, s80, 15\n" \
"s_lshl_b32 s80, s80, 12\n" \
"s_mov_b32 s81, Lcode_end_%=-Lpd_%=-4\n" \
"v_lshl_add_u32 v252, v248, 2, s80\n" \
"v_min_u32 v252, s81, v252\n" \
"s_waitcnt lgkmcnt(0)\n" \
"v_mfma_f32_16x16x32_bf16 v[72:75], a[192:195], a[144:147], v[228:231]\n" \
"v_exp_f32 v32, v32\n" \
"v_mfma_f32_16x16x32_bf16 v[72:75], a[196:199], a[148:151], v[72:75]\n" \
"v_cvt_pk_bf16_f32 v27, v30, v31\n" \
"v_add_f32 v240, v240, v32\n" \
"v_exp_f32 v33, v33\n" \
"v_mfma_f32_16x16x32_bf16 v[80:83], a[192:195], a[160:163], v[232:235]\n" \
"s_mov_b32 m0, s74\n" \
"v_add_f32 v240, v240, v33\n" \
"v_exp_f32 v34, v34\n" \
"v_mfma_f32_16x16x32_bf16 v[80:83], a[196:199], a[164:167], v[80:83]\n" \
"ds_read_b128 a[252:255], v246 offset:30976\n" \
"global_load_lds_dwordx4 v250, s[38:39] offset:0 sc1\n" \
"v_add_f32 v240, v240, v34\n" \
"v_cvt_pk_bf16_f32 v32, v32, v33\n" \
"v_exp_f32 v35, v35\n" \
"v_mfma_f32_16x16x32_bf16 v[88:91], a[192:195], a[176:179], v[236:239]\n" \
"s_lshl_b32 s70, s95, 1\n" \
"v_add_f32 v240, v240, v35\n" \
"v_exp_f32 v36, v36\n" \
"s_xor_b32 s70, s70, 139264\n" \
"v_add_u32 v244, s70, v248\n" \
"v_mfma_f32_16x16x32_bf16 v[88:91], a[196:199], a[180:183], v[88:91]\n" \
"ds_read_b128 a[192:195], v247 offset:0\n" \
"v_add_f32 v240, v240, v36\n" \
"v_cvt_pk_bf16_f32 v33, v34, v35\n" \
"v_exp_f32 v37, v37\n" \
"v_mfma_f32_16x16x32_bf16 v[76:79], a[200:203], a[144:147], v[228:231]\n" \
"ds_read_b128 v[192:195], v244 offset:0\n" \
"v_add_f32 v240, v240, v37\n" \
"v_exp_f32 v38, v38\n" \
"v_mfma_f32_16x16x32_bf16 v[76:79], a[204:207], a[148:151], v[76:79]\n" \
"ds_read_b128 v[196:199], v244 offset:1024\n" \
"v_add_f32 v240, v240, v38\n" \
"v_cvt_pk_bf16_f32 v34, v36, v37\n" \
"v_exp_f32 v39, v39\n" \
"v_mfma_f32_16x16x32_bf16 v[84:87], a[200:203], a[160:163], v[232:235]\n" \
"ds_read_b128 a[196:199], v247 offset:8192\n" \
"v_add_f32 v240, v240, v39\n" \
"v_exp_f32 v40, v40\n" \
"v_mfma_f32_16x16x32_bf16 v[84:87], a[204:207], a[164:167], v[84:87]\n" \
"ds_read_b128 v[200:203], v244 offset:2048\n" \
"v_add_f32 v241, v241, v40\n" \
"v_cvt_pk_bf16_f32 v35, v38, v39\n" \
"v_exp_f32 v41, v41\n" \
"v_mfma_f32_16x16x32_bf16 v[92:95], a[200:203], a[176:179], v[236:239]\n" \
"ds_read_b128 v[204:207], v244 offset:3072\n" \
"v_add_f32 v241, v241, v41\n" \
"v_exp_f32 v42, v42\n" \
"v_mfma_f32_16x16x32_bf16 v[92:95], a[204:207], a[180:183], v[92:95]\n" \
"ds_read_b128 a[200:203], v247 offset:1024\n" \
"v_add_f32 v241, v241, v42\n" \
"v_cvt_pk_bf16_f32 v40, v40, v41\n" \
"v_exp_f32 v43, v43\n" \
"v_mfma_f32_16x16x32_bf16 v[104:107], a[208:211], a[144:147], v[228:231]\n" \
"ds_read_b128 v[208:211], v244 offset:4096\n" \
"global_load_lds_dwordx4 v250, s[38:39] offset:1024 sc1\n" \
"v_add_f32 v241, v241, v43\n" \
"v_exp_f32 v44, v44\n" \
"v_mfma_f32_16x16x32_bf16 v[104:107], a[212:215], a[148:151], v[104:107]\n" \
"ds_read_b128 v[212:215], v244 offset:5120\n" \
"v_add_f32 v241, v241, v44\n" \
"v_cvt_pk_bf16_f32 v41, v42, v43\n" \
"v_exp_f32 v45, v45\n" \
"v_mfma_f32_16x16x32_bf16 v[112:115], a[208:211], a[160:163], v[232:235]\n" \
"ds_read_b128 a[204:207], v247 offset:9216\n" \
"v_add_f32 v241, v241, v45\n" \
"v_exp_f32 v46, v46\n" \
"v_mfma_f32_16x16x32_bf16 v[112:115], a[212:215], a[164:167], v[112:115]\n" \
"ds_read_b128 v[216:219], v244 offset:6144\n" \
"v_add_f32 v241, v241, v46\n" \
"v_cvt_pk_bf16_f32 v42, v44, v45\n" \
"v_exp_f32 v47, v47\n" \
"v_mfma_f32_16x16x32_bf16 v[120:123], a[208:211], a[176:179], v[236:239]\n" \
"ds_read_b128 v[220:223], v244 offset:7168\n" \
"v_add_f32 v241, v241, v47\n" \
"v_exp_f32 v48, v48\n" \
"v_mfma_f32_16x16x32_bf16 v[120:123], a[212:215], a[180:183], v[120:123]\n" \
"ds_read_b128 a[208:211], v247 offset:2048\n" \
"v_add_f32 v242, v242, v48\n" \
"v_cvt_pk_bf16_f32 v43, v46, v47\n" \
"v_exp_f32 v49, v49\n" \
"v_mfma_f32_16x16x32_bf16 v[108:111], a[216:219], a[144:147], v[228:231]\n" \
"ds_write_b128 v244, v[232:235]\n" \
"v_mfma_f32_16x16x32_bf16 v[108:111], a[220:223], a[148:151], v[108:111]\n" \
"ds_write_b128 v244, v[236:239] offset:1024\n" \
"v_mfma_f32_16x16x32_bf16 v[116:119], a[216:219], a[160:163], v[232:235]\n" \
"ds_read_b128 a[212:215], v247 offset:10240\n" \
"v_xor_b32 v245, 0x2000, v244\n" \
"v_mfma_f32_16x16x32_bf16 v[116:119], a[220:223], a[164:167], v[116:119]\n" \
"v_xor_b32 v246, 0x8000, v246\n" \
"v_add_u32 v246, s82, v246\n" \
"v_mfma_f32_16x16x32_bf16 v[124:127], a[216:219], a[176:179], v[236:239]\n" \
"s_waitcnt lgkmcnt(6)\n" \
"global_load_lds_dwordx4 v250, s[38:39] offset:2048 sc1\n" \
"v_add_f32 v242, v242, v49\n" \
"v_mfma_f32_16x16x32_bf16 v[124:127], a[220:223], a[180:183], v[124:127]\n" \
"ds_read_b128 a[216:219], v247 offset:3072\n" \
"v_exp_f32 v50, v50\n" \
"v_mfma_f32_16x16x32_bf16 v[72:75], a[224:227], a[152:155], v[72:75]\n" \
"v_add_f32 v242, v242, v50\n" \
"v_cvt_pk_bf16_f32 v48, v48, v49\n" \
"v_mfma_f32_16x16x32_bf16 v[72:75], a[228:231], a[156:159], v[72:75]\n" \
"v_exp_f32 v51, v51\n" \
"v_mfma_f32_16x16x32_bf16 v[80:83], a[224:227], a[168:171], v[80:83]\n" \
"ds_read_b128 a[220:223], v247 offset:11264\n" \
"v_add_f32 v242, v242, v51\n" \
"v_mfma_f32_16x16x32_bf16 v[80:83], a[228:231], a[172:175], v[80:83]\n" \
"v_exp_f32 v52, v52\n" \
"v_mfma_f32_16x16x32_bf16 v[88:91], a[224:227], a[184:187], v[88:91]\n" \
"v_add_f32 v242, v242, v52\n" \
"v_cvt_pk_bf16_f32 v49, v50, v51\n" \
"v_mfma_f32_16x16x32_bf16 v[88:91], a[228:231], a[188:191], v[88:91]\n" \
"ds_read_b128 a[224:227], v247 offset:4096\n" \
"v_exp_f32 v53, v53\n" \
"v_mfma_f32_16x16x32_bf16 v[76:79], a[232:235], a[152:155], v[76:79]\n" \
"global_load_lds_dwordx4 v250, s[38:39] offset:3072 sc1\n" \
"v_add_f32 v242, v242, v53\n" \
"v_mfma_f32_16x16x32_bf16 v[76:79], a[236:239], a[156:159], v[76:79]\n" \
"s_add_u32 s38, s38, 17408\n" \
"v_exp_f32 v54, v54\n" \
"v_mfma_f32_16x16x32_bf16 v[84:87], a[232:235], a[168:171], v[84:87]\n" \
"ds_read_b128 a[228:231], v247 offset:12288\n" \
"v_add_f32 v242, v242, v54\n" \
"v_cvt_pk_bf16_f32 v50, v52, v53\n" \
"v_mfma_f32_16x16x32_bf16 v[84:87], a[236:239], a[172:175], v[84:87]\n" \
"v_exp_f32 v55, v55\n" \
"v_mfma_f32_16x16x32_bf16 v[92:95], a[232:235], a[184:187], v[92:95]\n" \
"v_add_f32 v242, v242, v55\n" \
"v_exp_f32 v56, v56\n" \
"v_mfma_f32_16x16x32_bf16 v[92:95], a[236:239], a[188:191], v[92:95]\n" \
"ds_read_b128 a[232:235], v247 offset:5120\n" \
"v_add_f32 v243, v243, v56\n" \
"v_cvt_pk_bf16_f32 v51, v54, v55\n" \
"v_mfma_f32_16x16x32_bf16 v[104:107], a[240:243], a[152:155], v[104:107]\n" \
"global_load_dword v253, v252, s[40:41]\n" \
"v_mfma_f32_16x16x32_bf16 v[104:107], a[244:247], a[156:159], v[104:107]\n" \
"v_exp_f32 v57, v57\n" \
"v_mfma_f32_16x16x32_bf16 v[112:115], a[240:243], a[168:171], v[112:115]\n" \
"ds_read_b128 a[236:239], v247 offset:13312\n" \
"v_add_f32 v243, v243, v57\n" \
"v_exp_f32 v58, v58\n" \
"v_mfma_f32_16x16x32_bf16 v[112:115], a[244:247], a[172:175], v[112:115]\n" \
"v_add_f32 v243, v243, v58\n" \
"v_cvt_pk_bf16_f32 v56, v56, v57\n" \
"v_mfma_f32_16x16x32_bf16 v[120:123], a[240:243], a[184:187], v[120:123]\n" \
"v_exp_f32 v59, v59\n" \
"v_mfma_f32_16x16x32_bf16 v[120:123], a[244:247], a[188:191], v[120:123]\n" \
"ds_read_b128 a[240:243], v247 offset:6144\n" \
"v_add_f32 v243, v243, v59\n" \
"v_exp_f32 v60, v60\n" \
"v_mfma_f32_16x16x32_bf16 v[108:111], a[248:251], a[152:155], v[108:111]\n" \
"v_add_f32 v243, v243, v60\n" \
"v_cvt_pk_bf16_f32 v57, v58, v59\n" \
"v_mfma_f32_16x16x32_bf16 v[108:111], a[252:255], a[156:159], v[108:111]\n" \
"v_exp_f32 v61, v61\n" \
"v_mfma_f32_16x16x32_bf16 v[116:119], a[248:251], a[168:171], v[116:119]\n" \
"ds_read_b128 a[244:247], v247 offset:14336\n" \
"v_add_f32 v243, v243, v61\n" \
"v_exp_f32 v62, v62\n" \
"v_mfma_f32_16x16x32_bf16 v[116:119], a[252:255], a[172:175], v[116:119]\n" \
"v_add_f32 v243, v243, v62\n" \
"v_cvt_pk_bf16_f32 v58, v60, v61\n" \
"v_mfma_f32_16x16x32_bf16 v[124:127], a[248:251], a[184:187], v[124:127]\n" \
"s_waitcnt vmcnt(5) lgkmcnt(6)\n" \
"v_exp_f32 v63, v63\n" \
"v_mfma_f32_16x16x32_bf16 v[124:127], a[252:255], a[188:191], v[124:127]\n" \
"s_barrier\n" \
"ds_read_b128 a[248:251], v247 offset:7168\n" \
"v_add_f32 v243, v243, v63\n" \
"v_mfma_f32_16x16x32_bf16 a[0:3], a[192:195], v[0:3], a[0:3]\n" \
"v_exp_f32 v80, v80\n" \
"v_mfma_f32_16x16x32_bf16 a[0:3], a[196:199], v[32:35], a[0:3]\n" \
"v_cvt_pk_bf16_f32 v59, v62, v63\n" \
"v_add_f32 v242, v242, v80\n" \
"v_exp_f32 v81, v81\n" \
"v_mfma_f32_16x16x32_bf16 a[32:35], a[192:195], v[8:11], a[32:35]\n" \
"v_add_f32 v242, v242, v81\n" \
"v_exp_f32 v82, v82\n" \
"v_mfma_f32_16x16x32_bf16 a[32:35], a[196:199], v[40:43], a[32:35]\n" \
"ds_read_b128 a[252:255], v247 offset:15360\n" \
"v_add_f32 v242, v242, v82\n" \
"v_cvt_pk_bf16_f32 v80, v80, v81\n" \
"v_exp_f32 v83, v83\n" \
"v_mfma_f32_16x16x32_bf16 v[128:131], a[192:195], v[16:19], v[128:131]\n" \
"ds_read_b128 v[224:227], v245\n" \
"v_add_f32 v242, v242, v83\n" \
"v_exp_f32 v84, v84\n" \
"v_mfma_f32_16x16x32_bf16 v[128:131], a[196:199], v[48:51], v[128:131]\n" \
"ds_read_b128 v[228:231], v245 offset:1024\n" \
"v_add_f32 v242, v242, v84\n" \
"v_cvt_pk_bf16_f32 v81, v82, v83\n" \
"v_exp_f32 v85, v85\n" \
"v_mov_b32 v244, 0\n" \
"v_mfma_f32_16x16x32_bf16 v[160:163], a[192:195], v[24:27], v[160:163]\n" \
"v_mov_b32 v245, 0\n" \
"v_xor_b32 v252, 0x8000, v246\n" \
"v_add_f32 v242, v242, v85\n" \
"v_exp_f32 v86, v86\n" \
"v_mfma_f32_16x16x32_bf16 v[160:163], a[196:199], v[56:59], v[160:163]\n" \
"s_add_u32 s73, s73, s95\n" \
"ds_read_b128 a[192:195], v246 offset:0\n" \
"v_add_f32 v242, v242, v86\n" \
"v_cvt_pk_bf16_f32 v82, v84, v85\n" \
"v_exp_f32 v87, v87\n" \
"v_mfma_f32_16x16x32_bf16 a[4:7], a[200:203], v[0:3], a[4:7]\n" \
"s_add_u32 s36, s36, s83\n" \
"v_add_f32 v242, v242, v87\n" \
"s_add_u32 s73, s73, 4096\n" \
"s_mov_b32 m0, s73\n" \
"v_mfma_f32_16x16x32_bf16 a[4:7], a[204:207], v[32:35], a[4:7]\n" \
"global_load_lds_dwordx4 v250, s[36:37] offset:-4096 sc1\n" \
"v_cvt_pk_bf16_f32 v83, v86, v87\n" \
"v_mfma_f32_16x16x32_bf16 a[36:39], a[200:203], v[8:11], a[36:39]\n" \
"v_cmp_gt_i32 vcc, 0, v251\n" \
"v_cmp_gt_i32 s[64:65], 1, v251\n" \
"v_mfma_f32_16x16x32_bf16 a[36:39], a[204:207], v[40:43], a[36:39]\n" \
"ds_read_b128 a[196:199], v246 offset:2048\n" \
"global_load_lds_dwordx4 v250, s[36:37] offset:-3072 sc1\n" \
"v_mfma_f32_16x16x32_bf16 v[132:135], a[200:203], v[16:19], v[132:135]\n" \
"v_cmp_gt_i32 s[66:67], 2, v251\n" \
"v_cndmask_b32 v72, v72, v254, vcc\n" \
"v_mfma_f32_16x16x32_bf16 v[132:135], a[204:207], v[48:51], v[132:135]\n" \
"v_cmp_gt_i32 vcc, 3, v251\n" \
"v_cndmask_b32 v73, v73, v254, s[64:65]\n" \
"v_mfma_f32_16x16x32_bf16 v[164:167], a[200:203], v[24:27], v[164:167]\n" \
"v_cmp_gt_i32 s[64:65], 16, v251\n" \
"v_cndmask_b32 v74, v74, v254, s[66:67]\n" \
"v_mfma_f32_16x16x32_bf16 v[164:167], a[204:207], v[56:59], v[164:167]\n" \
"ds_read_b128 a[200:203], v246 offset:256\n" \
"global_load_lds_dwordx4 v250, s[36:37] offset:-2048 sc1\n" \
"v_mfma_f32_16x16x32_bf16 a[8:11], a[208:211], v[0:3], a[8:11]\n" \
"v_cmp_gt_i32 s[66:67], 17, v251\n" \
"v_cndmask_b32 v75, v75, v254, vcc\n" \
"v_mfma_f32_16x16x32_bf16 a[8:11], a[212:215], v[32:35], a[8:11]\n" \
"v_cmp_gt_i32 vcc, 18, v251\n" \
"v_cndmask_b32 v76, v76, v254, s[64:65]\n" \
"v_mfma_f32_16x16x32_bf16 a[40:43], a[208:211], v[8:11], a[40:43]\n" \
"v_cmp_gt_i32 s[64:65], 19, v251\n" \
"v_cndmask_b32 v77, v77, v254, s[66:67]\n" \
"v_mfma_f32_16x16x32_bf16 a[40:43], a[212:215], v[40:43], a[40:43]\n" \
"ds_read_b128 a[204:207], v246 offset:2304\n" \
"global_load_lds_dwordx4 v250, s[36:37] offset:-1024 sc1\n" \
"v_mfma_f32_16x16x32_bf16 v[136:139], a[208:211], v[16:19], v[136:139]\n" \
"v_cmp_gt_i32 s[66:67], 32, v251\n" \
"v_cndmask_b32 v78, v78, v254, vcc\n" \
"v_mfma_f32_16x16x32_bf16 v[136:139], a[212:215], v[48:51], v[136:139]\n" \
"v_cmp_gt_i32 vcc, 33, v251\n" \
"v_cndmask_b32 v79, v79, v254, s[64:65]\n" \
"v_mfma_f32_16x16x32_bf16 v[168:171], a[208:211], v[24:27], v[168:171]\n" \
"v_cmp_gt_i32 s[64:65], 34, v251\n" \
"v_cndmask_b32 v104, v104, v254, s[66:67]\n" \
"v_mfma_f32_16x16x32_bf16 v[168:171], a[212:215], v[56:59], v[168:171]\n" \
"ds_read_b128 a[208:211], v246 offset:8192\n" \
"global_load_lds_dwordx4 v250, s[36:37] offset:0 sc1\n" \
"v_mfma_f32_16x16x32_bf16 a[12:15], a[216:219], v[0:3], a[12:15]\n" \
"v_cmp_gt_i32 s[66:67], 35, v251\n" \
"v_cndmask_b32 v105, v105, v254, vcc\n" \
"v_mfma_f32_16x16x32_bf16 a[12:15], a[220:223], v[32:35], a[12:15]\n" \
"v_cmp_gt_i32 vcc, 48, v251\n" \
"v_cndmask_b32 v106, v106, v254, s[64:65]\n" \
"v_mfma_f32_16x16x32_bf16 a[44:47], a[216:219], v[8:11], a[44:47]\n" \
"v_cmp_gt_i32 s[64:65], 49, v251\n" \
"v_cndmask_b32 v107, v107, v254, s[66:67]\n" \
"v_mfma_f32_16x16x32_bf16 a[44:47], a[220:223], v[40:43], a[44:47]\n" \
"ds_read_b128 a[212:215], v246 offset:10240\n" \
"global_load_lds_dwordx4 v250, s[36:37] offset:1024 sc1\n" \
"v_mfma_f32_16x16x32_bf16 v[140:143], a[216:219], v[16:19], v[140:143]\n" \
"v_cmp_gt_i32 s[66:67], 50, v251\n" \
"v_cndmask_b32 v108, v108, v254, vcc\n" \
"v_mfma_f32_16x16x32_bf16 v[140:143], a[220:223], v[48:51], v[140:143]\n" \
"v_cmp_gt_i32 vcc, 51, v251\n" \
"v_cndmask_b32 v109, v109, v254, s[64:65]\n" \
"v_mfma_f32_16x16x32_bf16 v[172:175], a[216:219], v[24:27], v[172:175]\n" \
"s_waitcnt lgkmcnt(6)\n" \
"v_cndmask_b32 v110, v110, v254, s[66:67]\n" \
"v_cndmask_b32 v111, v111, v254, vcc\n" \
"v_mfma_f32_16x16x32_bf16 v[172:175], a[220:223], v[56:59], v[172:175]\n" \
"ds_read_b128 a[216:219], v246 offset:8448\n" \
"global_load_lds_dwordx4 v250, s[36:37] offset:2048 sc1\n" \
"v_mfma_f32_16x16x32_bf16 a[16:19], a[224:227], v[0:3], a[16:19]\n" \
"v_exp_f32 v72, v72\n" \
"v_mfma_f32_16x16x32_bf16 a[16:19], a[228:231], v[32:35], a[16:19]\n" \
"v_add_f32 v241, v241, v72\n" \
"v_exp_f32 v73, v73\n" \
"v_mfma_f32_16x16x32_bf16 a[48:51], a[224:227], v[8:11], a[48:51]\n" \
"v_add_f32 v241, v241, v73\n" \
"v_exp_f32 v74, v74\n" \
"v_mfma_f32_16x16x32_bf16 a[48:51], a[228:231], v[40:43], a[48:51]\n" \
"ds_read_b128 a[220:223], v246 offset:10496\n" \
"global_load_lds_dwordx4 v250, s[36:37] offset:3072 sc1\n" \
"v_mfma_f32_16x16x32_bf16 v[144:147], a[224:227], v[16:19], v[144:147]\n" \
"s_add_u32 s36, s36, 34816\n" \
"v_add_f32 v241, v241, v74\n" \
"v_cvt_pk_bf16_f32 v72, v72, v73\n" \
"v_mfma_f32_16x16x32_bf16 v[144:147], a[228:231], v[48:51], v[144:147]\n" \
"v_exp_f32 v75, v75\n" \
"v_mfma_f32_16x16x32_bf16 v[176:179], a[224:227], v[24:27], v[176:179]\n" \
"s_add_u32 m0, s74, 0x4000\n" \
"v_add_f32 v241, v241, v75\n" \
"v_exp_f32 v76, v76\n" \
"v_mfma_f32_16x16x32_bf16 v[176:179], a[228:231], v[56:59], v[176:179]\n" \
"ds_read_b128 a[224:227], v246 offset:4096\n" \
"global_load_lds_dwordx4 v250, s[38:39] offset:0 sc1\n" \
"v_mfma_f32_16x16x32_bf16 a[20:23], a[232:235], v[0:3], a[20:23]\n" \
"v_add_f32 v241, v241, v76\n" \
"v_cvt_pk_bf16_f32 v73, v74, v75\n" \
"v_mfma_f32_16x16x32_bf16 a[20:23], a[236:239], v[32:35], a[20:23]\n" \
"v_exp_f32 v77, v77\n" \
"v_mfma_f32_16x16x32_bf16 a[52:55], a[232:235], v[8:11], a[52:55]\n" \
"v_add_f32 v241, v241, v77\n" \
"v_exp_f32 v78, v78\n" \
"v_mfma_f32_16x16x32_bf16 a[52:55], a[236:239], v[40:43], a[52:55]\n" \
"ds_read_b128 a[228:231], v246 offset:6144\n" \
"global_load_lds_dwordx4 v250, s[38:39] offset:1024 sc1\n" \
"v_mfma_f32_16x16x32_bf16 v[148:151], a[232:235], v[16:19], v[148:151]\n" \
"v_add_f32 v241, v241, v78\n" \
"v_cvt_pk_bf16_f32 v74, v76, v77\n" \
"v_mfma_f32_16x16x32_bf16 v[148:151], a[236:239], v[48:51], v[148:151]\n" \
"v_exp_f32 v79, v79\n" \
"v_mfma_f32_16x16x32_bf16 v[180:183], a[232:235], v[24:27], v[180:183]\n" \
"v_add_f32 v241, v241, v79\n" \
"v_exp_f32 v88, v88\n" \
"v_mfma_f32_16x16x32_bf16 v[180:183], a[236:239], v[56:59], v[180:183]\n" \
"ds_read_b128 a[232:235], v246 offset:4352\n" \
"global_load_lds_dwordx4 v250, s[38:39] offset:2048 sc1\n" \
"v_mfma_f32_16x16x32_bf16 a[24:27], a[240:243], v[0:3], a[24:27]\n" \
"v_add_f32 v243, v243, v88\n" \
"v_cvt_pk_bf16_f32 v75, v78, v79\n" \
"v_mfma_f32_16x16x32_bf16 a[24:27], a[244:247], v[32:35], a[24:27]\n" \
"v_exp_f32 v89, v89\n" \
"v_mfma_f32_16x16x32_bf16 a[56:59], a[240:243], v[8:11], a[56:59]\n" \
"v_add_f32 v243, v243, v89\n" \
"v_exp_f32 v90, v90\n" \
"v_mfma_f32_16x16x32_bf16 a[56:59], a[244:247], v[40:43], a[56:59]\n" \
"ds_read_b128 a[236:239], v246 offset:6400\n" \
"global_load_lds_dwordx4 v250, s[38:39] offset:3072 sc1\n" \
"v_add_f32 v243, v243, v90\n" \
"v_mfma_f32_16x16x32_bf16 v[152:155], a[240:243], v[16:19], v[152:155]\n" \
"s_add_u32 s38, s38, 17408\n" \
"s_add_u32 s38, s38, s83\n" \
"s_add_u32 s74, s74, s95\n" \
"v_mfma_f32_16x16x32_bf16 v[152:155], a[244:247], v[48:51], v[152:155]\n" \
"s_add_u32 s74, s74, 4096\n" \
"v_cvt_pk_bf16_f32 v88, v88, v89\n" \
"s_cmp_eq_u32 s63, 7\n" \
"v_mfma_f32_16x16x32_bf16 v[184:187], a[240:243], v[24:27], v[184:187]\n" \
"v_exp_f32 v91, v91\n" \
"v_mfma_f32_16x16x32_bf16 v[184:187], a[244:247], v[56:59], v[184:187]\n" \
"ds_read_b128 a[240:243], v246 offset:12288\n" \
"v_add_f32 v243, v243, v91\n" \
"v_mfma_f32_16x16x32_bf16 a[28:31], a[248:251], v[0:3], a[28:31]\n" \
"v_exp_f32 v92, v92\n" \
"v_mfma_f32_16x16x32_bf16 a[28:31], a[252:255], v[32:35], a[28:31]\n" \
"v_add_f32 v243, v243, v92\n" \
"v_cvt_pk_bf16_f32 v89, v90, v91\n" \
"v_mfma_f32_16x16x32_bf16 a[60:63], a[248:251], v[8:11], a[60:63]\n" \
"v_exp_f32 v93, v93\n" \
"v_mfma_f32_16x16x32_bf16 a[60:63], a[252:255], v[40:43], a[60:63]\n" \
"ds_read_b128 a[244:247], v246 offset:14336\n" \
"v_add_f32 v243, v243, v93\n" \
"v_mfma_f32_16x16x32_bf16 v[156:159], a[248:251], v[16:19], v[156:159]\n" \
"v_exp_f32 v94, v94\n" \
"v_mfma_f32_16x16x32_bf16 v[156:159], a[252:255], v[48:51], v[156:159]\n" \
"v_add_f32 v243, v243, v94\n" \
"v_cvt_pk_bf16_f32 v90, v92, v93\n" \
"v_mfma_f32_16x16x32_bf16 v[188:191], a[248:251], v[24:27], v[188:191]\n" \
"s_waitcnt lgkmcnt(6)\n" \
"v_exp_f32 v95, v95\n" \
"v_mfma_f32_16x16x32_bf16 v[188:191], a[252:255], v[56:59], v[188:191]\n" \
"ds_read_b128 a[248:251], v246 offset:12544\n" \
"v_add_f32 v243, v243, v95\n" \
"s_cbranch_scc1 LF7_%=\n" \
"v_mfma_f32_16x16x32_bf16 v[0:3], a[192:195], v[192:195], v[224:227]\n" \
"s_load_dwordx8 s[44:51], s[56:57], s79 glc\n" \
"v_exp_f32 v104, v104\n" \
"v_mfma_f32_16x16x32_bf16 v[0:3], a[196:199], v[196:199], v[0:3]\n" \
"v_cvt_pk_bf16_f32 v91, v94, v95\n" \
"v_add_f32 v241, v241, v104\n" \
"v_mfma_f32_16x16x32_bf16 v[8:11], a[192:195], v[208:211], v[228:231]\n" \
"v_mov_b32 v76, v240\n" \
"v_exp_f32 v105, v105\n" \
"v_mfma_f32_16x16x32_bf16 v[8:11], a[196:199], v[212:215], v[8:11]\n" \
"ds_read_b128 a[252:255], v246 offset:14592\n" \
"v_add_f32 v241, v241, v105\n" \
"v_mfma_f32_16x16x32_bf16 v[16:19], a[192:195], a[160:163], v[232:235]\n" \
"v_exp_f32 v106, v106\n" \
"v_permlane16_swap_b32 v76, v240\n" \
"v_add_f32 v240, v240, v76\n" \
"v_mov_b32 v76, v240\n" \
"v_mfma_f32_16x16x32_bf16 v[16:19], a[196:199], a[164:167], v[16:19]\n" \
"v_add_f32 v241, v241, v106\n" \
"v_cvt_pk_bf16_f32 v104, v104, v105\n" \
"v_mfma_f32_16x16x32_bf16 v[24:27], a[192:195], a[176:179], v[236:239]\n" \
"v_exp_f32 v107, v107\n" \
"v_permlane32_swap_b32 v76, v240\n" \
"v_add_f32 v240, v240, v76\n" \
"v_rcp_f32 v76, v240\n" \
"v_mul_f32 v240, 0x4d800000, v240\n" \
"v_mfma_f32_16x16x32_bf16 v[24:27], a[196:199], a[180:183], v[24:27]\n" \
"ds_read_b128 a[192:195], v247 offset:16384\n" \
"v_add_f32 v241, v241, v107\n" \
"v_mfma_f32_16x16x32_bf16 v[4:7], a[200:203], v[192:195], v[224:227]\n" \
"v_exp_f32 v108, v108\n" \
"v_mfma_f32_16x16x32_bf16 v[4:7], a[204:207], v[196:199], v[4:7]\n" \
"v_add_f32 v241, v241, v108\n" \
"v_cvt_pk_bf16_f32 v105, v106, v107\n" \
"v_mfma_f32_16x16x32_bf16 v[12:15], a[200:203], v[208:211], v[228:231]\n" \
"v_exp_f32 v109, v109\n" \
"v_mfma_f32_16x16x32_bf16 v[12:15], a[204:207], v[212:215], v[12:15]\n" \
"ds_read_b128 a[196:199], v247 offset:24576\n" \
"v_add_f32 v241, v241, v109\n" \
"v_mfma_f32_16x16x32_bf16 v[20:23], a[200:203], a[160:163], v[232:235]\n" \
"v_exp_f32 v110, v110\n" \
"v_mfma_f32_16x16x32_bf16 v[20:23], a[204:207], a[164:167], v[20:23]\n" \
"v_add_f32 v241, v241, v110\n" \
"v_cvt_pk_bf16_f32 v106, v108, v109\n" \
"v_mfma_f32_16x16x32_bf16 v[28:31], a[200:203], a[176:179], v[236:239]\n" \
"v_exp_f32 v111, v111\n" \
"v_mfma_f32_16x16x32_bf16 v[28:31], a[204:207], a[180:183], v[28:31]\n" \
"ds_read_b128 a[200:203], v247 offset:17408\n" \
"v_add_f32 v241, v241, v111\n" \
"v_mfma_f32_16x16x32_bf16 v[32:35], a[208:211], v[192:195], v[224:227]\n" \
"v_exp_f32 v112, v112\n" \
"v_mfma_f32_16x16x32_bf16 v[32:35], a[212:215], v[196:199], v[32:35]\n" \
"v_add_f32 v242, v242, v112\n" \
"v_cvt_pk_bf16_f32 v107, v110, v111\n" \
"v_mfma_f32_16x16x32_bf16 v[40:43], a[208:211], v[208:211], v[228:231]\n" \
"v_exp_f32 v113, v113\n" \
"v_mfma_f32_16x16x32_bf16 v[40:43], a[212:215], v[212:215], v[40:43]\n" \
"ds_read_b128 a[204:207], v247 offset:25600\n" \
"v_add_f32 v242, v242, v113\n" \
"v_mfma_f32_16x16x32_bf16 v[48:51], a[208:211], a[160:163], v[232:235]\n" \
"v_exp_f32 v114, v114\n" \
"v_mfma_f32_16x16x32_bf16 v[48:51], a[212:215], a[164:167], v[48:51]\n" \
"v_add_f32 v242, v242, v114\n" \
"v_cvt_pk_bf16_f32 v112, v112, v113\n" \
"v_mfma_f32_16x16x32_bf16 v[56:59], a[208:211], a[176:179], v[236:239]\n" \
"v_exp_f32 v115, v115\n" \
"v_mfma_f32_16x16x32_bf16 v[56:59], a[212:215], a[180:183], v[56:59]\n" \
"ds_read_b128 a[208:211], v247 offset:18432\n" \
"v_add_f32 v242, v242, v115\n" \
"v_mfma_f32_16x16x32_bf16 v[36:39], a[216:219], v[192:195], v[224:227]\n" \
"v_exp_f32 v116, v116\n" \
"v_mfma_f32_16x16x32_bf16 v[36:39], a[220:223], v[196:199], v[36:39]\n" \
"v_add_f32 v242, v242, v116\n" \
"v_cvt_pk_bf16_f32 v113, v114, v115\n" \
"v_mfma_f32_16x16x32_bf16 v[44:47], a[216:219], v[208:211], v[228:231]\n" \
"v_exp_f32 v117, v117\n" \
"v_mfma_f32_16x16x32_bf16 v[44:47], a[220:223], v[212:215], v[44:47]\n" \
"ds_read_b128 a[212:215], v247 offset:26624\n" \
"v_add_f32 v242, v242, v117\n" \
"v_mfma_f32_16x16x32_bf16 v[52:55], a[216:219], a[160:163], v[232:235]\n" \
"v_exp_f32 v118, v118\n" \
"v_mfma_f32_16x16x32_bf16 v[52:55], a[220:223], a[164:167], v[52:55]\n" \
"v_add_f32 v242, v242, v118\n" \
"v_cvt_pk_bf16_f32 v114, v116, v117\n" \
"v_mfma_f32_16x16x32_bf16 v[60:63], a[216:219], a[176:179], v[236:239]\n" \
"s_waitcnt lgkmcnt(6)\n" \
"v_exp_f32 v119, v119\n" \
"v_mfma_f32_16x16x32_bf16 v[60:63], a[220:223], a[180:183], v[60:63]\n" \
"ds_read_b128 a[216:219], v247 offset:19456\n" \
"v_add_f32 v242, v242, v119\n" \
"v_mfma_f32_16x16x32_bf16 v[0:3], a[224:227], v[200:203], v[0:3]\n" \
"v_exp_f32 v120, v120\n" \
"v_mfma_f32_16x16x32_bf16 v[0:3], a[228:231], v[204:207], v[0:3]\n" \
"v_add_f32 v243, v243, v120\n" \
"v_cvt_pk_bf16_f32 v115, v118, v119\n" \
"v_mfma_f32_16x16x32_bf16 v[8:11], a[224:227], v[216:219], v[8:11]\n" \
"v_exp_f32 v121, v121\n" \
"v_mfma_f32_16x16x32_bf16 v[8:11], a[228:231], v[220:223], v[8:11]\n" \
"ds_read_b128 a[220:223], v247 offset:27648\n" \
"v_add_f32 v243, v243, v121\n" \
"v_mfma_f32_16x16x32_bf16 v[16:19], a[224:227], a[168:171], v[16:19]\n" \
"v_exp_f32 v122, v122\n" \
"v_mfma_f32_16x16x32_bf16 v[16:19], a[228:231], a[172:175], v[16:19]\n" \
"v_add_f32 v243, v243, v122\n" \
"v_cvt_pk_bf16_f32 v120, v120, v121\n" \
"v_mfma_f32_16x16x32_bf16 v[24:27], a[224:227], a[184:187], v[24:27]\n" \
"v_exp_f32 v123, v123\n" \
"v_mfma_f32_16x16x32_bf16 v[24:27], a[228:231], a[188:191], v[24:27]\n" \
"ds_read_b128 a[224:227], v247 offset:20480\n" \
"v_add_f32 v243, v243, v123\n" \
"v_mfma_f32_16x16x32_bf16 v[4:7], a[232:235], v[200:203], v[4:7]\n" \
"v_exp_f32 v124, v124\n" \
"v_mfma_f32_16x16x32_bf16 v[4:7], a[236:239], v[204:207], v[4:7]\n" \
"v_add_f32 v243, v243, v124\n" \
"v_cvt_pk_bf16_f32 v121, v122, v123\n" \
"v_mfma_f32_16x16x32_bf16 v[12:15], a[232:235], v[216:219], v[12:15]\n" \
"v_exp_f32 v125, v125\n" \
"v_mfma_f32_16x16x32_bf16 v[12:15], a[236:239], v[220:223], v[12:15]\n" \
"ds_read_b128 a[228:231], v247 offset:28672\n" \
"v_add_f32 v243, v243, v125\n" \
"v_mfma_f32_16x16x32_bf16 v[20:23], a[232:235], a[168:171], v[20:23]\n" \
"s_xor_b64 s[52:53], s[44:45], s[42:43]\n" \
"s_xor_b64 s[54:55], s[46:47], s[42:43]\n" \
"s_or_b64 s[52:53], s[52:53], s[54:55]\n" \
"v_mfma_f32_16x16x32_bf16 v[20:23], a[236:239], a[172:175], v[20:23]\n" \
"s_xor_b64 s[54:55], s[48:49], s[42:43]\n" \
"s_or_b64 s[52:53], s[52:53], s[54:55]\n" \
"s_xor_b64 s[54:55], s[50:51], s[42:43]\n" \
"v_mfma_f32_16x16x32_bf16 v[28:31], a[232:235], a[184:187], v[28:31]\n" \
"s_or_b64 s[52:53], s[52:53], s[54:55]\n" \
"v_exp_f32 v126, v126\n" \
"v_mfma_f32_16x16x32_bf16 v[28:31], a[236:239], a[188:191], v[28:31]\n" \
"Lpg6840_%=:\n" \
"s_cbranch_execz Lcb0_%=\n" \
"ds_read_b128 a[232:235], v247 offset:21504\n" \
"v_add_f32 v243, v243, v126\n" \
"v_mfma_f32_16x16x32_bf16 v[32:35], a[240:243], v[200:203], v[32:35]\n" \
"s_and_b64 s[52:53], s[52:53], s[8:9]\n" \
"s_cbranch_scc1 Lrs2_%=\n" \
"Lrr2_%=:\n" \
"v_mfma_f32_16x16x32_bf16 v[32:35], a[244:247], v[204:207], v[32:35]\n" \
"s_mov_b64 s[8:9], s[10:11]\n" \
"v_cvt_pk_bf16_f32 v122, v124, v125\n" \
"v_add_u32 v77, 0x1c90080, v249\n" \
"v_mfma_f32_16x16x32_bf16 v[40:43], a[240:243], v[216:219], v[40:43]\n" \
"v_exp_f32 v127, v127\n" \
"v_mfma_f32_16x16x32_bf16 v[40:43], a[244:247], v[220:223], v[40:43]\n" \
"ds_read_b128 a[236:239], v247 offset:29696\n" \
"v_add_f32 v243, v243, v127\n" \
"v_mfma_f32_16x16x32_bf16 v[48:51], a[240:243], a[168:171], v[48:51]\n" \
"v_cndmask_b32 v77, v249, v77, s[16:17]\n" \
"v_accvgpr_read_b32 v64, a0\n" \
"v_mfma_f32_16x16x32_bf16 v[48:51], a[244:247], a[172:175], v[48:51]\n" \
"v_accvgpr_read_b32 v65, a1\n" \
"v_accvgpr_read_b32 v66, a2\n" \
"v_mfma_f32_16x16x32_bf16 v[56:59], a[240:243], a[184:187], v[56:59]\n" \
"v_accvgpr_read_b32 v67, a3\n" \
"v_accvgpr_read_b32 v68, a4\n" \
"v_accvgpr_read_b32 v69, a5\n" \
"v_accvgpr_read_b32 v70, a6\n" \
"v_accvgpr_read_b32 v71, a7\n" \
"v_pk_mul_f32 v[64:65], v[64:65], v[76:77] op_sel_hi:[1,0]\n" \
"v_pk_mul_f32 v[66:67], v[66:67], v[76:77] op_sel_hi:[1,0]\n" \
"v_pk_mul_f32 v[68:69], v[68:69], v[76:77] op_sel_hi:[1,0]\n" \
"v_pk_mul_f32 v[70:71], v[70:71], v[76:77] op_sel_hi:[1,0]\n" \
"v_cvt_pk_bf16_f32 v96, v64, v65\n" \
"v_cvt_pk_bf16_f32 v97, v66, v67\n" \
"v_cvt_pk_bf16_f32 v98, v68, v69\n" \
"v_cvt_pk_bf16_f32 v99, v70, v71\n" \
"v_pk_mul_f32 v[64:65], v[64:65], v[66:67]\n" \
"v_pk_fma_f32 v[64:65], v[68:69], v[70:71], v[64:65]\n" \
"v_fma_f32 v240, v64, v65, v240\n" \
"v_mfma_f32_16x16x32_bf16 v[56:59], a[244:247], a[188:191], v[56:59]\n" \
"buffer_store_dwordx4 v[96:99], v77, s[56:59], s77 offen offset:0 sc1\n" \
"ds_read_b128 a[240:243], v247 offset:22528\n" \
"v_accvgpr_read_b32 v64, a8\n" \
"v_accvgpr_read_b32 v65, a9\n" \
"v_accvgpr_read_b32 v66, a10\n" \
"v_mfma_f32_16x16x32_bf16 v[36:39], a[248:251], v[200:203], v[36:39]\n" \
"v_accvgpr_read_b32 v67, a11\n" \
"v_accvgpr_read_b32 v68, a12\n" \
"v_accvgpr_read_b32 v69, a13\n" \
"v_accvgpr_read_b32 v70, a14\n" \
"v_accvgpr_read_b32 v71, a15\n" \
"v_pk_mul_f32 v[64:65], v[64:65], v[76:77] op_sel_hi:[1,0]\n" \
"v_pk_mul_f32 v[66:67], v[66:67], v[76:77] op_sel_hi:[1,0]\n" \
"v_pk_mul_f32 v[68:69], v[68:69], v[76:77] op_sel_hi:[1,0]\n" \
"v_pk_mul_f32 v[70:71], v[70:71], v[76:77] op_sel_hi:[1,0]\n" \
"v_cvt_pk_bf16_f32 v100, v64, v65\n" \
"v_cvt_pk_bf16_f32 v101, v66, v67\n" \
"v_cvt_pk_bf16_f32 v102, v68, v69\n" \
"v_cvt_pk_bf16_f32 v103, v70, v71\n" \
"v_pk_mul_f32 v[64:65], v[64:65], v[66:67]\n" \
"v_pk_fma_f32 v[64:65], v[68:69], v[70:71], v[64:65]\n" \
"v_fma_f32 v240, v64, v65, v240\n" \
"v_mfma_f32_16x16x32_bf16 v[36:39], a[252:255], v[204:207], v[36:39]\n" \
"buffer_store_dwordx4 v[100:103], v77, s[56:59], s77 offen offset:2048 sc1\n" \
"s_add_u32 s77, s77, 0x1000\n" \
"v_accvgpr_read_b32 v64, a16\n" \
"v_accvgpr_read_b32 v65, a17\n" \
"v_accvgpr_read_b32 v66, a18\n" \
"v_mfma_f32_16x16x32_bf16 v[44:47], a[248:251], v[216:219], v[44:47]\n" \
"v_accvgpr_read_b32 v67, a19\n" \
"v_accvgpr_read_b32 v68, a20\n" \
"v_accvgpr_read_b32 v69, a21\n" \
"v_accvgpr_read_b32 v70, a22\n" \
"v_accvgpr_read_b32 v71, a23\n" \
"v_pk_mul_f32 v[64:65], v[64:65], v[76:77] op_sel_hi:[1,0]\n" \
"v_pk_mul_f32 v[66:67], v[66:67], v[76:77] op_sel_hi:[1,0]\n" \
"v_pk_mul_f32 v[68:69], v[68:69], v[76:77] op_sel_hi:[1,0]\n" \
"v_pk_mul_f32 v[70:71], v[70:71], v[76:77] op_sel_hi:[1,0]\n" \
"v_cvt_pk_bf16_f32 v96, v64, v65\n" \
"v_cvt_pk_bf16_f32 v97, v66, v67\n" \
"v_cvt_pk_bf16_f32 v98, v68, v69\n" \
"v_cvt_pk_bf16_f32 v99, v70, v71\n" \
"v_pk_mul_f32 v[64:65], v[64:65], v[66:67]\n" \
"v_pk_fma_f32 v[64:65], v[68:69], v[70:71], v[64:65]\n" \
"v_fma_f32 v240, v64, v65, v240\n" \
"v_mfma_f32_16x16x32_bf16 v[44:47], a[252:255], v[220:223], v[44:47]\n" \
"buffer_store_dwordx4 v[96:99], v77, s[56:59], s77 offen offset:0 sc1\n" \
"ds_read_b128 a[244:247], v247 offset:30720\n" \
"v_accvgpr_read_b32 v64, a24\n" \
"v_accvgpr_read_b32 v65, a25\n" \
"v_accvgpr_read_b32 v66, a26\n" \
"v_mfma_f32_16x16x32_bf16 v[52:55], a[248:251], a[168:171], v[52:55]\n" \
"v_accvgpr_read_b32 v67, a27\n" \
"v_accvgpr_read_b32 v68, a28\n" \
"v_accvgpr_read_b32 v69, a29\n" \
"v_accvgpr_read_b32 v70, a30\n" \
"v_accvgpr_read_b32 v71, a31\n" \
"v_pk_mul_f32 v[64:65], v[64:65], v[76:77] op_sel_hi:[1,0]\n" \
"v_pk_mul_f32 v[66:67], v[66:67], v[76:77] op_sel_hi:[1,0]\n" \
"v_pk_mul_f32 v[68:69], v[68:69], v[76:77] op_sel_hi:[1,0]\n" \
"v_pk_mul_f32 v[70:71], v[70:71], v[76:77] op_sel_hi:[1,0]\n" \
"v_cvt_pk_bf16_f32 v100, v64, v65\n" \
"v_cvt_pk_bf16_f32 v101, v66, v67\n" \
"v_cvt_pk_bf16_f32 v102, v68, v69\n" \
"v_cvt_pk_bf16_f32 v103, v70, v71\n" \
"v_pk_mul_f32 v[64:65], v[64:65], v[66:67]\n" \
"v_pk_fma_f32 v[64:65], v[68:69], v[70:71], v[64:65]\n" \
"v_fma_f32 v240, v64, v65, v240\n" \
"v_mfma_f32_16x16x32_bf16 v[52:55], a[252:255], a[172:175], v[52:55]\n" \
"buffer_store_dwordx4 v[100:103], v77, s[56:59], s77 offen offset:2048 sc1\n" \
"s_add_u32 s77, s77, 0x81000\n" \
"v_mfma_f32_16x16x32_bf16 v[60:63], a[248:251], a[184:187], v[60:63]\n" \
"s_waitcnt vmcnt(8) lgkmcnt(6)\n" \
"v_mfma_f32_16x16x32_bf16 v[60:63], a[252:255], a[188:191], v[60:63]\n" \
"s_barrier\n" \
"ds_read_b128 a[248:251], v247 offset:23552\n" \
"v_mfma_f32_16x16x32_bf16 a[32:35], a[192:195], v[72:75], a[32:35]\n" \
"v_exp_f32 v0, v0\n" \
"v_mfma_f32_16x16x32_bf16 a[32:35], a[196:199], v[104:107], a[32:35]\n" \
"v_cvt_pk_bf16_f32 v123, v126, v127\n" \
"v_add_f32 v244, v244, v0\n" \
"v_exp_f32 v1, v1\n" \
"v_mfma_f32_16x16x32_bf16 v[128:131], a[192:195], v[80:83], v[128:131]\n" \
"s_xor_b32 m0, s73, 0x8000\n" \
"v_add_f32 v244, v244, v1\n" \
"v_exp_f32 v2, v2\n" \
"v_mfma_f32_16x16x32_bf16 v[128:131], a[196:199], v[112:115], v[128:131]\n" \
"ds_read_b128 a[252:255], v247 offset:31744\n" \
"global_load_lds_dwordx4 v250, s[36:37] offset:-4096 sc1\n" \
"v_add_f32 v244, v244, v2\n" \
"v_cvt_pk_bf16_f32 v0, v0, v1\n" \
"v_exp_f32 v3, v3\n" \
"v_mfma_f32_16x16x32_bf16 v[160:163], a[192:195], v[88:91], v[160:163]\n" \
"s_add_u32 s70, s82, 0x10000\n" \
"v_add_f32 v244, v244, v3\n" \
"v_exp_f32 v4, v4\n" \
"v_add_u32 v247, s70, v248\n" \
"v_mfma_f32_16x16x32_bf16 v[160:163], a[196:199], v[120:123], v[160:163]\n" \
"ds_read_b128 a[192:195], v252 offset:0\n" \
"v_add_f32 v244, v244, v4\n" \
"v_cvt_pk_bf16_f32 v1, v2, v3\n" \
"v_exp_f32 v5, v5\n" \
"v_mfma_f32_16x16x32_bf16 a[36:39], a[200:203], v[72:75], a[36:39]\n" \
"v_add_f32 v244, v244, v5\n" \
"v_exp_f32 v6, v6\n" \
"v_mfma_f32_16x16x32_bf16 a[36:39], a[204:207], v[104:107], a[36:39]\n" \
"global_load_lds_dwordx4 v250, s[36:37] offset:-3072 sc1\n" \
"v_add_f32 v244, v244, v6\n" \
"v_cvt_pk_bf16_f32 v2, v4, v5\n" \
"v_exp_f32 v7, v7\n" \
"v_mfma_f32_16x16x32_bf16 v[132:135], a[200:203], v[80:83], v[132:135]\n" \
"ds_read_b128 a[196:199], v252 offset:2048\n" \
"v_add_f32 v244, v244, v7\n" \
"v_exp_f32 v8, v8\n" \
"v_mfma_f32_16x16x32_bf16 v[132:135], a[204:207], v[112:115], v[132:135]\n" \
"v_add_f32 v245, v245, v8\n" \
"v_cvt_pk_bf16_f32 v3, v6, v7\n" \
"v_exp_f32 v9, v9\n" \
"v_mfma_f32_16x16x32_bf16 v[164:167], a[200:203], v[88:91], v[164:167]\n" \
"v_add_f32 v245, v245, v9\n" \
"v_exp_f32 v10, v10\n" \
"v_mfma_f32_16x16x32_bf16 v[164:167], a[204:207], v[120:123], v[164:167]\n" \
"ds_read_b128 a[200:203], v252 offset:256\n" \
"v_add_f32 v245, v245, v10\n" \
"v_cvt_pk_bf16_f32 v8, v8, v9\n" \
"v_exp_f32 v11, v11\n" \
"v_mfma_f32_16x16x32_bf16 a[40:43], a[208:211], v[72:75], a[40:43]\n" \
"global_load_lds_dwordx4 v250, s[36:37] offset:-2048 sc1\n" \
"v_add_f32 v245, v245, v11\n" \
"v_exp_f32 v12, v12\n" \
"v_mfma_f32_16x16x32_bf16 a[40:43], a[212:215], v[104:107], a[40:43]\n" \
"v_add_f32 v245, v245, v12\n" \
"v_cvt_pk_bf16_f32 v9, v10, v11\n" \
"v_exp_f32 v13, v13\n" \
"v_mfma_f32_16x16x32_bf16 v[136:139], a[208:211], v[80:83], v[136:139]\n" \
"ds_read_b128 a[204:207], v252 offset:2304\n" \
"v_add_f32 v245, v245, v13\n" \
"v_mfma_f32_16x16x32_bf16 v[136:139], a[212:215], v[112:115], v[136:139]\n" \
"v_exp_f32 v14, v14\n" \
"v_mfma_f32_16x16x32_bf16 v[168:171], a[208:211], v[88:91], v[168:171]\n" \
"global_load_lds_dwordx4 v250, s[36:37] offset:-1024 sc1\n" \
"v_add_f32 v245, v245, v14\n" \
"v_mfma_f32_16x16x32_bf16 v[168:171], a[212:215], v[120:123], v[168:171]\n" \
"ds_read_b128 a[208:211], v252 offset:8192\n" \
"v_cvt_pk_bf16_f32 v10, v12, v13\n" \
"v_mfma_f32_16x16x32_bf16 a[44:47], a[216:219], v[72:75], a[44:47]\n" \
"v_exp_f32 v15, v15\n" \
"v_mfma_f32_16x16x32_bf16 a[44:47], a[220:223], v[104:107], a[44:47]\n" \
"v_add_f32 v245, v245, v15\n" \
"v_exp_f32 v16, v16\n" \
"v_mfma_f32_16x16x32_bf16 v[140:143], a[216:219], v[80:83], v[140:143]\n" \
"ds_read_b128 a[212:215], v252 offset:10240\n" \
"v_add_f32 v242, v242, v16\n" \
"v_mfma_f32_16x16x32_bf16 v[140:143], a[220:223], v[112:115], v[140:143]\n" \
"global_load_lds_dwordx4 v250, s[36:37] offset:0 sc1\n" \
"v_cvt_pk_bf16_f32 v11, v14, v15\n" \
"v_mfma_f32_16x16x32_bf16 v[172:175], a[216:219], v[88:91], v[172:175]\n" \
"s_waitcnt lgkmcnt(6)\n" \
"v_exp_f32 v17, v17\n" \
"v_mfma_f32_16x16x32_bf16 v[172:175], a[220:223], v[120:123], v[172:175]\n" \
"ds_read_b128 a[216:219], v252 offset:8448\n" \
"v_add_f32 v242, v242, v17\n" \
"v_exp_f32 v18, v18\n" \
"v_mfma_f32_16x16x32_bf16 a[48:51], a[224:227], v[72:75], a[48:51]\n" \
"v_add_f32 v242, v242, v18\n" \
"v_cvt_pk_bf16_f32 v16, v16, v17\n" \
"v_mfma_f32_16x16x32_bf16 a[48:51], a[228:231], v[104:107], a[48:51]\n" \
"global_load_lds_dwordx4 v250, s[36:37] offset:1024 sc1\n" \
"v_exp_f32 v19, v19\n" \
"v_mfma_f32_16x16x32_bf16 v[144:147], a[224:227], v[80:83], v[144:147]\n" \
"ds_read_b128 a[220:223], v252 offset:10496\n" \
"v_add_f32 v242, v242, v19\n" \
"v_mfma_f32_16x16x32_bf16 v[144:147], a[228:231], v[112:115], v[144:147]\n" \
"v_exp_f32 v20, v20\n" \
"v_mfma_f32_16x16x32_bf16 v[176:179], a[224:227], v[88:91], v[176:179]\n" \
"v_add_f32 v242, v242, v20\n" \
"v_cvt_pk_bf16_f32 v17, v18, v19\n" \
"v_mfma_f32_16x16x32_bf16 v[176:179], a[228:231], v[120:123], v[176:179]\n" \
"ds_read_b128 a[224:227], v252 offset:4096\n" \
"v_exp_f32 v21, v21\n" \
"v_mfma_f32_16x16x32_bf16 a[52:55], a[232:235], v[72:75], a[52:55]\n" \
"global_load_lds_dwordx4 v250, s[36:37] offset:2048 sc1\n" \
"v_add_f32 v242, v242, v21\n" \
"v_mfma_f32_16x16x32_bf16 a[52:55], a[236:239], v[104:107], a[52:55]\n" \
"v_exp_f32 v22, v22\n" \
"v_mfma_f32_16x16x32_bf16 v[148:151], a[232:235], v[80:83], v[148:151]\n" \
"ds_read_b128 a[228:231], v252 offset:6144\n" \
"v_add_f32 v242, v242, v22\n" \
"v_cvt_pk_bf16_f32 v18, v20, v21\n" \
"v_mfma_f32_16x16x32_bf16 v[148:151], a[236:239], v[112:115], v[148:151]\n" \
"v_exp_f32 v23, v23\n" \
"v_mfma_f32_16x16x32_bf16 v[180:183], a[232:235], v[88:91], v[180:183]\n" \
"global_load_lds_dwordx4 v250, s[36:37] offset:3072 sc1\n" \
"v_add_f32 v242, v242, v23\n" \
"v_mfma_f32_16x16x32_bf16 v[180:183], a[236:239], v[120:123], v[180:183]\n" \
"s_add_u32 s36, s36, 34816\n" \
"ds_read_b128 a[232:235], v252 offset:4352\n" \
"v_exp_f32 v24, v24\n" \
"v_mfma_f32_16x16x32_bf16 a[56:59], a[240:243], v[72:75], a[56:59]\n" \
"v_add_f32 v243, v243, v24\n" \
"v_cvt_pk_bf16_f32 v19, v22, v23\n" \
"v_mfma_f32_16x16x32_bf16 a[56:59], a[244:247], v[104:107], a[56:59]\n" \
"v_exp_f32 v25, v25\n" \
"v_mfma_f32_16x16x32_bf16 v[152:155], a[240:243], v[80:83], v[152:155]\n" \
"ds_read_b128 a[236:239], v252 offset:6400\n" \
"v_add_f32 v243, v243, v25\n" \
"v_exp_f32 v26, v26\n" \
"v_mfma_f32_16x16x32_bf16 v[152:155], a[244:247], v[112:115], v[152:155]\n" \
"v_add_f32 v243, v243, v26\n" \
"v_cvt_pk_bf16_f32 v24, v24, v25\n" \
"v_mfma_f32_16x16x32_bf16 v[184:187], a[240:243], v[88:91], v[184:187]\n" \
"s_load_dwordx8 s[44:51], s[56:57], s79 glc\n" \
"v_exp_f32 v27, v27\n" \
"v_mfma_f32_16x16x32_bf16 v[184:187], a[244:247], v[120:123], v[184:187]\n" \
"ds_read_b128 a[240:243], v252 offset:12288\n" \
"v_add_f32 v243, v243, v27\n" \
"v_exp_f32 v28, v28\n" \
"v_mfma_f32_16x16x32_bf16 a[60:63], a[248:251], v[72:75], a[60:63]\n" \
"v_add_f32 v243, v243, v28\n" \
"v_cvt_pk_bf16_f32 v25, v26, v27\n" \
"v_mfma_f32_16x16x32_bf16 a[60:63], a[252:255], v[104:107], a[60:63]\n" \
"v_exp_f32 v29, v29\n" \
"v_mfma_f32_16x16x32_bf16 v[156:159], a[248:251], v[80:83], v[156:159]\n" \
"ds_read_b128 a[244:247], v252 offset:14336\n" \
"v_add_f32 v243, v243, v29\n" \
"v_exp_f32 v30, v30\n" \
"v_mfma_f32_16x16x32_bf16 v[156:159], a[252:255], v[112:115], v[156:159]\n" \
"v_add_f32 v243, v243, v30\n" \
"v_cvt_pk_bf16_f32 v26, v28, v29\n" \
"v_mfma_f32_16x16x32_bf16 v[188:191], a[248:251], v[88:91], v[188:191]\n" \
"s_waitcnt vmcnt(12) lgkmcnt(6)\n" \
"v_exp_f32 v31, v31\n" \
"v_mfma_f32_16x16x32_bf16 v[188:191], a[252:255], v[120:123], v[188:191]\n" \
"s_barrier\n" \
"ds_read_b128 a[248:251], v252 offset:12544\n" \
"v_add_f32 v243, v243, v31\n" \
"s_mov_b64 exec, 0\n" \
"s_cmp_eq_u32 s61, 0\n" \
"s_cbranch_scc1 Lst1x0_%=\n" \
"s_cmp_eq_u32 s61, 1\n" \
"s_cbranch_scc1 Lst1x1_%=\n" \
"s_cmp_eq_u32 s61, 2\n" \
"s_cbranch_scc1 Lst1x2_%=\n" \
"s_branch Lst1x3_%=\n" \
"Lcb1_%=:\n" \
"s_mov_b64 exec, -1\n" \
"s_waitcnt lgkmcnt(0)\n" \
"s_cmp_lg_u32 s63, 0\n" \
"s_cbranch_scc1 LA1g_%=\n" \
"s_xor_b64 s[52:53], s[44:45], s[42:43]\n" \
"s_xor_b64 s[54:55], s[46:47], s[42:43]\n" \
"s_or_b64 s[52:53], s[52:53], s[54:55]\n" \
"s_xor_b64 s[54:55], s[48:49], s[42:43]\n" \
"s_or_b64 s[52:53], s[52:53], s[54:55]\n" \
"s_xor_b64 s[54:55], s[50:51], s[42:43]\n" \
"s_or_b64 s[52:53], s[52:53], s[54:55]\n" \
"s_and_b64 s[52:53], s[52:53], s[8:9]\n" \
"s_cbranch_scc1 Lrs3_%=\n" \
"Lrr3_%=:\n" \
"s_branch LA1g_%=\n" \
"Lpg7140_%=:\n" \
"s_cbranch_execz Lcb0_%=\n" \
"Lloop2_%=:\n" \
"v_mfma_f32_16x16x32_bf16 a[64:67], a[192:195], v[0:3], a[64:67]\n" \
"v_exp_f32 v64, v64\n" \
"v_mfma_f32_16x16x32_bf16 a[64:67], a[196:199], v[32:35], a[64:67]\n" \
"v_cvt_pk_bf16_f32 v59, v62, v63\n" \
"v_add_f32 v244, v244, v64\n" \
"v_mfma_f32_16x16x32_bf16 a[96:99], a[192:195], v[8:11], a[96:99]\n" \
"s_mov_b32 m0, s73\n" \
"v_exp_f32 v65, v65\n" \
"v_mfma_f32_16x16x32_bf16 a[96:99], a[196:199], v[40:43], a[96:99]\n" \
"ds_read_b128 a[252:255], v247 offset:15360\n" \
"global_load_lds_dwordx4 v250, s[36:37] offset:-4096 sc1\n" \
"v_add_f32 v244, v244, v65\n" \
"v_mfma_f32_16x16x32_bf16 v[128:131], a[192:195], v[16:19], v[128:131]\n" \
"v_exp_f32 v66, v66\n" \
"v_mfma_f32_16x16x32_bf16 v[128:131], a[196:199], v[48:51], v[128:131]\n" \
"v_add_f32 v244, v244, v66\n" \
"v_cvt_pk_bf16_f32 v64, v64, v65\n" \
"v_mfma_f32_16x16x32_bf16 v[160:163], a[192:195], v[24:27], v[160:163]\n" \
"v_exp_f32 v67, v67\n" \
"v_mfma_f32_16x16x32_bf16 v[160:163], a[196:199], v[56:59], v[160:163]\n" \
"ds_read_b128 a[192:195], v246 offset:0\n" \
"global_load_lds_dwordx4 v250, s[36:37] offset:-3072 sc1\n" \
"v_add_f32 v244, v244, v67\n" \
"v_mfma_f32_16x16x32_bf16 a[68:71], a[200:203], v[0:3], a[68:71]\n" \
"v_exp_f32 v68, v68\n" \
"v_mfma_f32_16x16x32_bf16 a[68:71], a[204:207], v[32:35], a[68:71]\n" \
"v_add_f32 v244, v244, v68\n" \
"v_cvt_pk_bf16_f32 v65, v66, v67\n" \
"v_mfma_f32_16x16x32_bf16 a[100:103], a[200:203], v[8:11], a[100:103]\n" \
"v_exp_f32 v69, v69\n" \
"v_mfma_f32_16x16x32_bf16 a[100:103], a[204:207], v[40:43], a[100:103]\n" \
"ds_read_b128 a[196:199], v246 offset:2048\n" \
"v_add_f32 v244, v244, v69\n" \
"v_mfma_f32_16x16x32_bf16 v[132:135], a[200:203], v[16:19], v[132:135]\n" \
"v_exp_f32 v70, v70\n" \
"v_mfma_f32_16x16x32_bf16 v[132:135], a[204:207], v[48:51], v[132:135]\n" \
"v_add_f32 v244, v244, v70\n" \
"v_cvt_pk_bf16_f32 v66, v68, v69\n" \
"v_mfma_f32_16x16x32_bf16 v[164:167], a[200:203], v[24:27], v[164:167]\n" \
"v_exp_f32 v71, v71\n" \
"v_mfma_f32_16x16x32_bf16 v[164:167], a[204:207], v[56:59], v[164:167]\n" \
"ds_read_b128 a[200:203], v246 offset:256\n" \
"global_load_lds_dwordx4 v250, s[36:37] offset:-2048 sc1\n" \
"v_add_f32 v244, v244, v71\n" \
"v_mfma_f32_16x16x32_bf16 a[72:75], a[208:211], v[0:3], a[72:75]\n" \
"v_exp_f32 v72, v72\n" \
"v_mfma_f32_16x16x32_bf16 a[72:75], a[212:215], v[32:35], a[72:75]\n" \
"v_add_f32 v245, v245, v72\n" \
"v_cvt_pk_bf16_f32 v67, v70, v71\n" \
"v_mfma_f32_16x16x32_bf16 a[104:107], a[208:211], v[8:11], a[104:107]\n" \
"v_exp_f32 v73, v73\n" \
"v_mfma_f32_16x16x32_bf16 a[104:107], a[212:215], v[40:43], a[104:107]\n" \
"ds_read_b128 a[204:207], v246 offset:2304\n" \
"global_load_lds_dwordx4 v250, s[36:37] offset:-1024 sc1\n" \
"v_add_f32 v245, v245, v73\n" \
"v_mfma_f32_16x16x32_bf16 v[136:139], a[208:211], v[16:19], v[136:139]\n" \
"v_exp_f32 v74, v74\n" \
"v_mfma_f32_16x16x32_bf16 v[136:139], a[212:215], v[48:51], v[136:139]\n" \
"v_add_f32 v245, v245, v74\n" \
"v_cvt_pk_bf16_f32 v72, v72, v73\n" \
"v_mfma_f32_16x16x32_bf16 v[168:171], a[208:211], v[24:27], v[168:171]\n" \
"v_exp_f32 v75, v75\n" \
"v_mfma_f32_16x16x32_bf16 v[168:171], a[212:215], v[56:59], v[168:171]\n" \
"ds_read_b128 a[208:211], v246 offset:8192\n" \
"v_add_f32 v245, v245, v75\n" \
"v_mfma_f32_16x16x32_bf16 a[76:79], a[216:219], v[0:3], a[76:79]\n" \
"v_exp_f32 v76, v76\n" \
"v_mfma_f32_16x16x32_bf16 a[76:79], a[220:223], v[32:35], a[76:79]\n" \
"v_add_f32 v245, v245, v76\n" \
"v_cvt_pk_bf16_f32 v73, v74, v75\n" \
"v_mfma_f32_16x16x32_bf16 a[108:111], a[216:219], v[8:11], a[108:111]\n" \
"v_exp_f32 v77, v77\n" \
"v_mfma_f32_16x16x32_bf16 a[108:111], a[220:223], v[40:43], a[108:111]\n" \
"ds_read_b128 a[212:215], v246 offset:10240\n" \
"global_load_lds_dwordx4 v250, s[36:37] offset:0 sc1\n" \
"v_add_f32 v245, v245, v77\n" \
"v_mfma_f32_16x16x32_bf16 v[140:143], a[216:219], v[16:19], v[140:143]\n" \
"v_exp_f32 v78, v78\n" \
"v_mfma_f32_16x16x32_bf16 v[140:143], a[220:223], v[48:51], v[140:143]\n" \
"v_add_f32 v245, v245, v78\n" \
"v_cvt_pk_bf16_f32 v74, v76, v77\n" \
"v_mfma_f32_16x16x32_bf16 v[172:175], a[216:219], v[24:27], v[172:175]\n" \
"s_waitcnt lgkmcnt(6)\n" \
"v_exp_f32 v79, v79\n" \
"v_mfma_f32_16x16x32_bf16 v[172:175], a[220:223], v[56:59], v[172:175]\n" \
"ds_read_b128 a[216:219], v246 offset:8448\n" \
"global_load_lds_dwordx4 v250, s[36:37] offset:1024 sc1\n" \
"v_add_f32 v245, v245, v79\n" \
"v_mfma_f32_16x16x32_bf16 a[80:83], a[224:227], v[0:3], a[80:83]\n" \
"v_exp_f32 v80, v80\n" \
"v_mfma_f32_16x16x32_bf16 a[80:83], a[228:231], v[32:35], a[80:83]\n" \
"v_add_f32 v242, v242, v80\n" \
"v_cvt_pk_bf16_f32 v75, v78, v79\n" \
"v_mfma_f32_16x16x32_bf16 a[112:115], a[224:227], v[8:11], a[112:115]\n" \
"v_exp_f32 v81, v81\n" \
"v_mfma_f32_16x16x32_bf16 a[112:115], a[228:231], v[40:43], a[112:115]\n" \
"ds_read_b128 a[220:223], v246 offset:10496\n" \
"v_add_f32 v242, v242, v81\n" \
"v_mfma_f32_16x16x32_bf16 v[144:147], a[224:227], v[16:19], v[144:147]\n" \
"v_exp_f32 v82, v82\n" \
"v_mfma_f32_16x16x32_bf16 v[144:147], a[228:231], v[48:51], v[144:147]\n" \
"v_add_f32 v242, v242, v82\n" \
"v_cvt_pk_bf16_f32 v80, v80, v81\n" \
"v_mfma_f32_16x16x32_bf16 v[176:179], a[224:227], v[24:27], v[176:179]\n" \
"v_exp_f32 v83, v83\n" \
"v_mfma_f32_16x16x32_bf16 v[176:179], a[228:231], v[56:59], v[176:179]\n" \
"ds_read_b128 a[224:227], v246 offset:4096\n" \
"global_load_lds_dwordx4 v250, s[36:37] offset:2048 sc1\n" \
"v_add_f32 v242, v242, v83\n" \
"v_mfma_f32_16x16x32_bf16 a[84:87], a[232:235], v[0:3], a[84:87]\n" \
"v_exp_f32 v84, v84\n" \
"v_mfma_f32_16x16x32_bf16 a[84:87], a[236:239], v[32:35], a[84:87]\n" \
"v_add_f32 v242, v242, v84\n" \
"v_cvt_pk_bf16_f32 v81, v82, v83\n" \
"v_mfma_f32_16x16x32_bf16 a[116:119], a[232:235], v[8:11], a[116:119]\n" \
"v_exp_f32 v85, v85\n" \
"v_mfma_f32_16x16x32_bf16 a[116:119], a[236:239], v[40:43], a[116:119]\n" \
"ds_read_b128 a[228:231], v246 offset:6144\n" \
"global_load_lds_dwordx4 v250, s[36:37] offset:3072 sc1\n" \
"v_add_f32 v242, v242, v85\n" \
"v_mfma_f32_16x16x32_bf16 v[148:151], a[232:235], v[16:19], v[148:151]\n" \
"s_add_u32 s36, s36, 34816\n" \
"v_exp_f32 v86, v86\n" \
"v_mfma_f32_16x16x32_bf16 v[148:151], a[236:239], v[48:51], v[148:151]\n" \
"v_add_f32 v242, v242, v86\n" \
"v_cvt_pk_bf16_f32 v82, v84, v85\n" \
"v_mfma_f32_16x16x32_bf16 v[180:183], a[232:235], v[24:27], v[180:183]\n" \
"v_exp_f32 v87, v87\n" \
"v_mfma_f32_16x16x32_bf16 v[180:183], a[236:239], v[56:59], v[180:183]\n" \
"ds_read_b128 a[232:235], v246 offset:4352\n" \
"v_add_f32 v242, v242, v87\n" \
"v_mfma_f32_16x16x32_bf16 a[88:91], a[240:243], v[0:3], a[88:91]\n" \
"v_exp_f32 v88, v88\n" \
"v_mfma_f32_16x16x32_bf16 a[88:91], a[244:247], v[32:35], a[88:91]\n" \
"v_add_f32 v243, v243, v88\n" \
"v_cvt_pk_bf16_f32 v83, v86, v87\n" \
"v_mfma_f32_16x16x32_bf16 a[120:123], a[240:243], v[8:11], a[120:123]\n" \
"v_exp_f32 v89, v89\n" \
"v_mfma_f32_16x16x32_bf16 a[120:123], a[244:247], v[40:43], a[120:123]\n" \
"ds_read_b128 a[236:239], v246 offset:6400\n" \
"v_add_f32 v243, v243, v89\n" \
"v_mfma_f32_16x16x32_bf16 v[152:155], a[240:243], v[16:19], v[152:155]\n" \
"v_exp_f32 v90, v90\n" \
"v_mfma_f32_16x16x32_bf16 v[152:155], a[244:247], v[48:51], v[152:155]\n" \
"v_add_f32 v243, v243, v90\n" \
"v_cvt_pk_bf16_f32 v88, v88, v89\n" \
"v_mfma_f32_16x16x32_bf16 v[184:187], a[240:243], v[24:27], v[184:187]\n" \
"v_exp_f32 v91, v91\n" \
"v_mfma_f32_16x16x32_bf16 v[184:187], a[244:247], v[56:59], v[184:187]\n" \
"ds_read_b128 a[240:243], v246 offset:12288\n" \
"v_add_f32 v243, v243, v91\n" \
"v_mfma_f32_16x16x32_bf16 a[92:95], a[248:251], v[0:3], a[92:95]\n" \
"v_exp_f32 v92, v92\n" \
"v_mfma_f32_16x16x32_bf16 a[92:95], a[252:255], v[32:35], a[92:95]\n" \
"v_add_f32 v243, v243, v92\n" \
"v_cvt_pk_bf16_f32 v89, v90, v91\n" \
"v_mfma_f32_16x16x32_bf16 a[124:127], a[248:251], v[8:11], a[124:127]\n" \
"v_exp_f32 v93, v93\n" \
"v_mfma_f32_16x16x32_bf16 a[124:127], a[252:255], v[40:43], a[124:127]\n" \
"ds_read_b128 a[244:247], v246 offset:14336\n" \
"v_add_f32 v243, v243, v93\n" \
"v_mfma_f32_16x16x32_bf16 v[156:159], a[248:251], v[16:19], v[156:159]\n" \
"v_exp_f32 v94, v94\n" \
"v_mfma_f32_16x16x32_bf16 v[156:159], a[252:255], v[48:51], v[156:159]\n" \
"v_add_f32 v243, v243, v94\n" \
"v_cvt_pk_bf16_f32 v90, v92, v93\n" \
"v_mfma_f32_16x16x32_bf16 v[188:191], a[248:251], v[24:27], v[188:191]\n" \
"s_waitcnt vmcnt(8) lgkmcnt(6)\n" \
"v_exp_f32 v95, v95\n" \
"v_mfma_f32_16x16x32_bf16 v[188:191], a[252:255], v[56:59], v[188:191]\n" \
"s_barrier\n" \
"ds_read_b128 a[248:251], v246 offset:12544\n" \
"v_add_f32 v243, v243, v95\n" \
"v_mfma_f32_16x16x32_bf16 v[0:3], a[192:195], v[192:195], v[224:227]\n" \
"v_exp_f32 v96, v96\n" \
"v_mfma_f32_16x16x32_bf16 v[0:3], a[196:199], v[196:199], v[0:3]\n" \
"v_cvt_pk_bf16_f32 v91, v94, v95\n" \
"v_add_f32 v244, v244, v96\n" \
"v_mfma_f32_16x16x32_bf16 v[8:11], a[192:195], v[208:211], v[228:231]\n" \
"s_mov_b32 m0, s74\n" \
"v_exp_f32 v97, v97\n" \
"v_mfma_f32_16x16x32_bf16 v[8:11], a[196:199], v[212:215], v[8:11]\n" \
"ds_read_b128 a[252:255], v246 offset:14592\n" \
"global_load_lds_dwordx4 v250, s[38:39] offset:-4096 sc1\n" \
"v_add_f32 v244, v244, v97\n" \
"v_mfma_f32_16x16x32_bf16 v[16:19], a[192:195], a[160:163], v[232:235]\n" \
"v_exp_f32 v98, v98\n" \
"v_mfma_f32_16x16x32_bf16 v[16:19], a[196:199], a[164:167], v[16:19]\n" \
"v_add_f32 v244, v244, v98\n" \
"v_cvt_pk_bf16_f32 v96, v96, v97\n" \
"v_mfma_f32_16x16x32_bf16 v[24:27], a[192:195], a[176:179], v[236:239]\n" \
"v_exp_f32 v99, v99\n" \
"v_mfma_f32_16x16x32_bf16 v[24:27], a[196:199], a[180:183], v[24:27]\n" \
"ds_read_b128 a[192:195], v247 offset:32768\n" \
"global_load_lds_dwordx4 v250, s[38:39] offset:-3072 sc1\n" \
"v_add_f32 v244, v244, v99\n" \
"v_mfma_f32_16x16x32_bf16 v[4:7], a[200:203], v[192:195], v[224:227]\n" \
"v_exp_f32 v100, v100\n" \
"v_mfma_f32_16x16x32_bf16 v[4:7], a[204:207], v[196:199], v[4:7]\n" \
"v_add_f32 v244, v244, v100\n" \
"v_cvt_pk_bf16_f32 v97, v98, v99\n" \
"v_mfma_f32_16x16x32_bf16 v[12:15], a[200:203], v[208:211], v[228:231]\n" \
"v_exp_f32 v101, v101\n" \
"v_mfma_f32_16x16x32_bf16 v[12:15], a[204:207], v[212:215], v[12:15]\n" \
"ds_read_b128 a[196:199], v247 offset:40960\n" \
"v_add_f32 v244, v244, v101\n" \
"v_mfma_f32_16x16x32_bf16 v[20:23], a[200:203], a[160:163], v[232:235]\n" \
"v_exp_f32 v102, v102\n" \
"v_mfma_f32_16x16x32_bf16 v[20:23], a[204:207], a[164:167], v[20:23]\n" \
"v_add_f32 v244, v244, v102\n" \
"v_cvt_pk_bf16_f32 v98, v100, v101\n" \
"v_mfma_f32_16x16x32_bf16 v[28:31], a[200:203], a[176:179], v[236:239]\n" \
"v_exp_f32 v103, v103\n" \
"v_mfma_f32_16x16x32_bf16 v[28:31], a[204:207], a[180:183], v[28:31]\n" \
"ds_read_b128 a[200:203], v247 offset:33792\n" \
"global_load_lds_dwordx4 v250, s[38:39] offset:-2048 sc1\n" \
"v_add_f32 v244, v244, v103\n" \
"v_mfma_f32_16x16x32_bf16 v[32:35], a[208:211], v[192:195], v[224:227]\n" \
"v_exp_f32 v104, v104\n" \
"v_mfma_f32_16x16x32_bf16 v[32:35], a[212:215], v[196:199], v[32:35]\n" \
"v_add_f32 v245, v245, v104\n" \
"v_cvt_pk_bf16_f32 v99, v102, v103\n" \
"v_mfma_f32_16x16x32_bf16 v[40:43], a[208:211], v[208:211], v[228:231]\n" \
"v_exp_f32 v105, v105\n" \
"v_mfma_f32_16x16x32_bf16 v[40:43], a[212:215], v[212:215], v[40:43]\n" \
"ds_read_b128 a[204:207], v247 offset:41984\n" \
"global_load_lds_dwordx4 v250, s[38:39] offset:-1024 sc1\n" \
"v_add_f32 v245, v245, v105\n" \
"v_mfma_f32_16x16x32_bf16 v[48:51], a[208:211], a[160:163], v[232:235]\n" \
"v_exp_f32 v106, v106\n" \
"v_mfma_f32_16x16x32_bf16 v[48:51], a[212:215], a[164:167], v[48:51]\n" \
"v_add_f32 v245, v245, v106\n" \
"v_cvt_pk_bf16_f32 v104, v104, v105\n" \
"v_mfma_f32_16x16x32_bf16 v[56:59], a[208:211], a[176:179], v[236:239]\n" \
"v_exp_f32 v107, v107\n" \
"v_mfma_f32_16x16x32_bf16 v[56:59], a[212:215], a[180:183], v[56:59]\n" \
"ds_read_b128 a[208:211], v247 offset:34816\n" \
"v_add_f32 v245, v245, v107\n" \
"v_mfma_f32_16x16x32_bf16 v[36:39], a[216:219], v[192:195], v[224:227]\n" \
"v_exp_f32 v108, v108\n" \
"v_mfma_f32_16x16x32_bf16 v[36:39], a[220:223], v[196:199], v[36:39]\n" \
"v_add_f32 v245, v245, v108\n" \
"v_cvt_pk_bf16_f32 v105, v106, v107\n" \
"v_mfma_f32_16x16x32_bf16 v[44:47], a[216:219], v[208:211], v[228:231]\n" \
"v_exp_f32 v109, v109\n" \
"v_mfma_f32_16x16x32_bf16 v[44:47], a[220:223], v[212:215], v[44:47]\n" \
"ds_read_b128 a[212:215], v247 offset:43008\n" \
"global_load_lds_dwordx4 v250, s[38:39] offset:0 sc1\n" \
"v_add_f32 v245, v245, v109\n" \
"v_mfma_f32_16x16x32_bf16 v[52:55], a[216:219], a[160:163], v[232:235]\n" \
"v_exp_f32 v110, v110\n" \
"v_mfma_f32_16x16x32_bf16 v[52:55], a[220:223], a[164:167], v[52:55]\n" \
"v_add_f32 v245, v245, v110\n" \
"v_cvt_pk_bf16_f32 v106, v108, v109\n" \
"v_mfma_f32_16x16x32_bf16 v[60:63], a[216:219], a[176:179], v[236:239]\n" \
"s_waitcnt lgkmcnt(6)\n" \
"v_exp_f32 v111, v111\n" \
"v_mfma_f32_16x16x32_bf16 v[60:63], a[220:223], a[180:183], v[60:63]\n" \
"ds_read_b128 a[216:219], v247 offset:35840\n" \
"global_load_lds_dwordx4 v250, s[38:39] offset:1024 sc1\n" \
"v_add_f32 v245, v245, v111\n" \
"v_mfma_f32_16x16x32_bf16 v[0:3], a[224:227], v[200:203], v[0:3]\n" \
"v_exp_f32 v112, v112\n" \
"v_mfma_f32_16x16x32_bf16 v[0:3], a[228:231], v[204:207], v[0:3]\n" \
"v_add_f32 v242, v242, v112\n" \
"v_cvt_pk_bf16_f32 v107, v110, v111\n" \
"v_mfma_f32_16x16x32_bf16 v[8:11], a[224:227], v[216:219], v[8:11]\n" \
"v_exp_f32 v113, v113\n" \
"v_mfma_f32_16x16x32_bf16 v[8:11], a[228:231], v[220:223], v[8:11]\n" \
"ds_read_b128 a[220:223], v247 offset:44032\n" \
"v_add_f32 v242, v242, v113\n" \
"v_mfma_f32_16x16x32_bf16 v[16:19], a[224:227], a[168:171], v[16:19]\n" \
"v_exp_f32 v114, v114\n" \
"v_mfma_f32_16x16x32_bf16 v[16:19], a[228:231], a[172:175], v[16:19]\n" \
"v_add_f32 v242, v242, v114\n" \
"v_cvt_pk_bf16_f32 v112, v112, v113\n" \
"v_mfma_f32_16x16x32_bf16 v[24:27], a[224:227], a[184:187], v[24:27]\n" \
"v_exp_f32 v115, v115\n" \
"v_mfma_f32_16x16x32_bf16 v[24:27], a[228:231], a[188:191], v[24:27]\n" \
"ds_read_b128 a[224:227], v247 offset:36864\n" \
"global_load_lds_dwordx4 v250, s[38:39] offset:2048 sc1\n" \
"v_add_f32 v242, v242, v115\n" \
"v_mfma_f32_16x16x32_bf16 v[4:7], a[232:235], v[200:203], v[4:7]\n" \
"v_exp_f32 v116, v116\n" \
"v_mfma_f32_16x16x32_bf16 v[4:7], a[236:239], v[204:207], v[4:7]\n" \
"v_add_f32 v242, v242, v116\n" \
"v_cvt_pk_bf16_f32 v113, v114, v115\n" \
"v_mfma_f32_16x16x32_bf16 v[12:15], a[232:235], v[216:219], v[12:15]\n" \
"v_exp_f32 v117, v117\n" \
"v_mfma_f32_16x16x32_bf16 v[12:15], a[236:239], v[220:223], v[12:15]\n" \
"ds_read_b128 a[228:231], v247 offset:45056\n" \
"global_load_lds_dwordx4 v250, s[38:39] offset:3072 sc1\n" \
"v_add_f32 v242, v242, v117\n" \
"v_mfma_f32_16x16x32_bf16 v[20:23], a[232:235], a[168:171], v[20:23]\n" \
"s_add_u32 s38, s38, 34816\n" \
"v_exp_f32 v118, v118\n" \
"v_mfma_f32_16x16x32_bf16 v[20:23], a[236:239], a[172:175], v[20:23]\n" \
"v_add_f32 v242, v242, v118\n" \
"v_cvt_pk_bf16_f32 v114, v116, v117\n" \
"v_mfma_f32_16x16x32_bf16 v[28:31], a[232:235], a[184:187], v[28:31]\n" \
"v_exp_f32 v119, v119\n" \
"v_mfma_f32_16x16x32_bf16 v[28:31], a[236:239], a[188:191], v[28:31]\n" \
"ds_read_b128 a[232:235], v247 offset:37888\n" \
"v_add_f32 v242, v242, v119\n" \
"v_mfma_f32_16x16x32_bf16 v[32:35], a[240:243], v[200:203], v[32:35]\n" \
"v_exp_f32 v120, v120\n" \
"v_mfma_f32_16x16x32_bf16 v[32:35], a[244:247], v[204:207], v[32:35]\n" \
"v_add_f32 v243, v243, v120\n" \
"v_cvt_pk_bf16_f32 v115, v118, v119\n" \
"v_mfma_f32_16x16x32_bf16 v[40:43], a[240:243], v[216:219], v[40:43]\n" \
"v_exp_f32 v121, v121\n" \
"v_mfma_f32_16x16x32_bf16 v[40:43], a[244:247], v[220:223], v[40:43]\n" \
"ds_read_b128 a[236:239], v247 offset:46080\n" \
"v_add_f32 v243, v243, v121\n" \
"v_mfma_f32_16x16x32_bf16 v[48:51], a[240:243], a[168:171], v[48:51]\n" \
"v_exp_f32 v122, v122\n" \
"v_mfma_f32_16x16x32_bf16 v[48:51], a[244:247], a[172:175], v[48:51]\n" \
"v_add_f32 v243, v243, v122\n" \
"v_cvt_pk_bf16_f32 v120, v120, v121\n" \
"v_mfma_f32_16x16x32_bf16 v[56:59], a[240:243], a[184:187], v[56:59]\n" \
"v_exp_f32 v123, v123\n" \
"v_mfma_f32_16x16x32_bf16 v[56:59], a[244:247], a[188:191], v[56:59]\n" \
"ds_read_b128 a[240:243], v247 offset:38912\n" \
"v_add_f32 v243, v243, v123\n" \
"v_mfma_f32_16x16x32_bf16 v[36:39], a[248:251], v[200:203], v[36:39]\n" \
"v_exp_f32 v124, v124\n" \
"v_mfma_f32_16x16x32_bf16 v[36:39], a[252:255], v[204:207], v[36:39]\n" \
"v_add_f32 v243, v243, v124\n" \
"Lpg7470_%=:\n" \
"Lst1x0_%=:\n" \
"s_cbranch_execz Lpg9871_%=\n" \
"v_cvt_pk_bf16_f32 v121, v122, v123\n" \
"v_mfma_f32_16x16x32_bf16 v[44:47], a[248:251], v[216:219], v[44:47]\n" \
"v_exp_f32 v125, v125\n" \
"v_mfma_f32_16x16x32_bf16 v[44:47], a[252:255], v[220:223], v[44:47]\n" \
"ds_read_b128 a[244:247], v247 offset:47104\n" \
"v_add_f32 v243, v243, v125\n" \
"v_mfma_f32_16x16x32_bf16 v[52:55], a[248:251], a[168:171], v[52:55]\n" \
"v_exp_f32 v126, v126\n" \
"v_mfma_f32_16x16x32_bf16 v[52:55], a[252:255], a[172:175], v[52:55]\n" \
"v_add_f32 v243, v243, v126\n" \
"v_cvt_pk_bf16_f32 v122, v124, v125\n" \
"v_mfma_f32_16x16x32_bf16 v[60:63], a[248:251], a[184:187], v[60:63]\n" \
"s_waitcnt vmcnt(8) lgkmcnt(6)\n" \
"v_exp_f32 v127, v127\n" \
"v_mfma_f32_16x16x32_bf16 v[60:63], a[252:255], a[188:191], v[60:63]\n" \
"s_barrier\n" \
"ds_read_b128 a[248:251], v247 offset:39936\n" \
"v_add_f32 v243, v243, v127\n" \
"v_mfma_f32_16x16x32_bf16 a[64:67], a[192:195], v[64:67], a[64:67]\n" \
"v_exp_f32 v0, v0\n" \
"v_mfma_f32_16x16x32_bf16 a[64:67], a[196:199], v[96:99], a[64:67]\n" \
"v_cvt_pk_bf16_f32 v123, v126, v127\n" \
"v_add_f32 v244, v244, v0\n" \
"v_mfma_f32_16x16x32_bf16 a[96:99], a[192:195], v[72:75], a[96:99]\n" \
"s_xor_b32 m0, s73, 0x8000\n" \
"v_exp_f32 v1, v1\n" \
"v_mfma_f32_16x16x32_bf16 a[96:99], a[196:199], v[104:107], a[96:99]\n" \
"ds_read_b128 a[252:255], v247 offset:48128\n" \
"global_load_lds_dwordx4 v250, s[36:37] offset:-4096 sc1\n" \
"v_add_f32 v244, v244, v1\n" \
"v_mfma_f32_16x16x32_bf16 v[128:131], a[192:195], v[80:83], v[128:131]\n" \
"v_exp_f32 v2, v2\n" \
"v_mfma_f32_16x16x32_bf16 v[128:131], a[196:199], v[112:115], v[128:131]\n" \
"v_add_f32 v244, v244, v2\n" \
"v_cvt_pk_bf16_f32 v0, v0, v1\n" \
"v_mfma_f32_16x16x32_bf16 v[160:163], a[192:195], v[88:91], v[160:163]\n" \
"v_exp_f32 v3, v3\n" \
"v_mfma_f32_16x16x32_bf16 v[160:163], a[196:199], v[120:123], v[160:163]\n" \
"ds_read_b128 a[192:195], v252 offset:0\n" \
"global_load_lds_dwordx4 v250, s[36:37] offset:-3072 sc1\n" \
"v_add_f32 v244, v244, v3\n" \
"v_mfma_f32_16x16x32_bf16 a[68:71], a[200:203], v[64:67], a[68:71]\n" \
"v_exp_f32 v4, v4\n" \
"v_mfma_f32_16x16x32_bf16 a[68:71], a[204:207], v[96:99], a[68:71]\n" \
"v_add_f32 v244, v244, v4\n" \
"v_cvt_pk_bf16_f32 v1, v2, v3\n" \
"v_mfma_f32_16x16x32_bf16 a[100:103], a[200:203], v[72:75], a[100:103]\n" \
"v_exp_f32 v5, v5\n" \
"v_mfma_f32_16x16x32_bf16 a[100:103], a[204:207], v[104:107], a[100:103]\n" \
"ds_read_b128 a[196:199], v252 offset:2048\n" \
"v_add_f32 v244, v244, v5\n" \
"v_mfma_f32_16x16x32_bf16 v[132:135], a[200:203], v[80:83], v[132:135]\n" \
"v_exp_f32 v6, v6\n" \
"v_mfma_f32_16x16x32_bf16 v[132:135], a[204:207], v[112:115], v[132:135]\n" \
"v_add_f32 v244, v244, v6\n" \
"v_cvt_pk_bf16_f32 v2, v4, v5\n" \
"v_mfma_f32_16x16x32_bf16 v[164:167], a[200:203], v[88:91], v[164:167]\n" \
"v_exp_f32 v7, v7\n" \
"v_mfma_f32_16x16x32_bf16 v[164:167], a[204:207], v[120:123], v[164:167]\n" \
"ds_read_b128 a[200:203], v252 offset:256\n" \
"global_load_lds_dwordx4 v250, s[36:37] offset:-2048 sc1\n" \
"v_add_f32 v244, v244, v7\n" \
"v_mfma_f32_16x16x32_bf16 a[72:75], a[208:211], v[64:67], a[72:75]\n" \
"v_exp_f32 v8, v8\n" \
"v_mfma_f32_16x16x32_bf16 a[72:75], a[212:215], v[96:99], a[72:75]\n" \
"v_add_f32 v245, v245, v8\n" \
"v_cvt_pk_bf16_f32 v3, v6, v7\n" \
"v_mfma_f32_16x16x32_bf16 a[104:107], a[208:211], v[72:75], a[104:107]\n" \
"v_exp_f32 v9, v9\n" \
"v_mfma_f32_16x16x32_bf16 a[104:107], a[212:215], v[104:107], a[104:107]\n" \
"ds_read_b128 a[204:207], v252 offset:2304\n" \
"global_load_lds_dwordx4 v250, s[36:37] offset:-1024 sc1\n" \
"v_add_f32 v245, v245, v9\n" \
"v_mfma_f32_16x16x32_bf16 v[136:139], a[208:211], v[80:83], v[136:139]\n" \
"v_exp_f32 v10, v10\n" \
"v_mfma_f32_16x16x32_bf16 v[136:139], a[212:215], v[112:115], v[136:139]\n" \
"v_add_f32 v245, v245, v10\n" \
"v_cvt_pk_bf16_f32 v8, v8, v9\n" \
"v_mfma_f32_16x16x32_bf16 v[168:171], a[208:211], v[88:91], v[168:171]\n" \
"v_exp_f32 v11, v11\n" \
"v_mfma_f32_16x16x32_bf16 v[168:171], a[212:215], v[120:123], v[168:171]\n" \
"ds_read_b128 a[208:211], v252 offset:8192\n" \
"v_add_f32 v245, v245, v11\n" \
"v_mfma_f32_16x16x32_bf16 a[76:79], a[216:219], v[64:67], a[76:79]\n" \
"v_exp_f32 v12, v12\n" \
"v_mfma_f32_16x16x32_bf16 a[76:79], a[220:223], v[96:99], a[76:79]\n" \
"v_add_f32 v245, v245, v12\n" \
"v_cvt_pk_bf16_f32 v9, v10, v11\n" \
"v_mfma_f32_16x16x32_bf16 a[108:111], a[216:219], v[72:75], a[108:111]\n" \
"v_exp_f32 v13, v13\n" \
"v_mfma_f32_16x16x32_bf16 a[108:111], a[220:223], v[104:107], a[108:111]\n" \
"ds_read_b128 a[212:215], v252 offset:10240\n" \
"global_load_lds_dwordx4 v250, s[36:37] offset:0 sc1\n" \
"v_add_f32 v245, v245, v13\n" \
"v_mfma_f32_16x16x32_bf16 v[140:143], a[216:219], v[80:83], v[140:143]\n" \
"v_exp_f32 v14, v14\n" \
"v_mfma_f32_16x16x32_bf16 v[140:143], a[220:223], v[112:115], v[140:143]\n" \
"v_add_f32 v245, v245, v14\n" \
"v_cvt_pk_bf16_f32 v10, v12, v13\n" \
"v_mfma_f32_16x16x32_bf16 v[172:175], a[216:219], v[88:91], v[172:175]\n" \
"s_waitcnt lgkmcnt(6)\n" \
"v_exp_f32 v15, v15\n" \
"v_mfma_f32_16x16x32_bf16 v[172:175], a[220:223], v[120:123], v[172:175]\n" \
"ds_read_b128 a[216:219], v252 offset:8448\n" \
"global_load_lds_dwordx4 v250, s[36:37] offset:1024 sc1\n" \
"v_add_f32 v245, v245, v15\n" \
"v_mfma_f32_16x16x32_bf16 a[80:83], a[224:227], v[64:67], a[80:83]\n" \
"v_exp_f32 v16, v16\n" \
"v_mfma_f32_16x16x32_bf16 a[80:83], a[228:231], v[96:99], a[80:83]\n" \
"v_add_f32 v242, v242, v16\n" \
"v_cvt_pk_bf16_f32 v11, v14, v15\n" \
"v_mfma_f32_16x16x32_bf16 a[112:115], a[224:227], v[72:75], a[112:115]\n" \
"v_exp_f32 v17, v17\n" \
"v_mfma_f32_16x16x32_bf16 a[112:115], a[228:231], v[104:107], a[112:115]\n" \
"ds_read_b128 a[220:223], v252 offset:10496\n" \
"v_add_f32 v242, v242, v17\n" \
"v_mfma_f32_16x16x32_bf16 v[144:147], a[224:227], v[80:83], v[144:147]\n" \
"v_exp_f32 v18, v18\n" \
"v_mfma_f32_16x16x32_bf16 v[144:147], a[228:231], v[112:115], v[144:147]\n" \
"v_add_f32 v242, v242, v18\n" \
"v_cvt_pk_bf16_f32 v16, v16, v17\n" \
"v_mfma_f32_16x16x32_bf16 v[176:179], a[224:227], v[88:91], v[176:179]\n" \
"v_exp_f32 v19, v19\n" \
"v_mfma_f32_16x16x32_bf16 v[176:179], a[228:231], v[120:123], v[176:179]\n" \
"ds_read_b128 a[224:227], v252 offset:4096\n" \
"global_load_lds_dwordx4 v250, s[36:37] offset:2048 sc1\n" \
"v_add_f32 v242, v242, v19\n" \
"v_mfma_f32_16x16x32_bf16 a[84:87], a[232:235], v[64:67], a[84:87]\n" \
"v_exp_f32 v20, v20\n" \
"v_mfma_f32_16x16x32_bf16 a[84:87], a[236:239], v[96:99], a[84:87]\n" \
"v_add_f32 v242, v242, v20\n" \
"v_cvt_pk_bf16_f32 v17, v18, v19\n" \
"v_mfma_f32_16x16x32_bf16 a[116:119], a[232:235], v[72:75], a[116:119]\n" \
"v_exp_f32 v21, v21\n" \
"v_mfma_f32_16x16x32_bf16 a[116:119], a[236:239], v[104:107], a[116:119]\n" \
"ds_read_b128 a[228:231], v252 offset:6144\n" \
"global_load_lds_dwordx4 v250, s[36:37] offset:3072 sc1\n" \
"v_add_f32 v242, v242, v21\n" \
"v_mfma_f32_16x16x32_bf16 v[148:151], a[232:235], v[80:83], v[148:151]\n" \
"s_add_u32 s36, s36, 34816\n" \
"v_exp_f32 v22, v22\n" \
"v_mfma_f32_16x16x32_bf16 v[148:151], a[236:239], v[112:115], v[148:151]\n" \
"v_add_f32 v242, v242, v22\n" \
"v_cvt_pk_bf16_f32 v18, v20, v21\n" \
"v_mfma_f32_16x16x32_bf16 v[180:183], a[232:235], v[88:91], v[180:183]\n" \
"v_exp_f32 v23, v23\n" \
"v_mfma_f32_16x16x32_bf16 v[180:183], a[236:239], v[120:123], v[180:183]\n" \
"ds_read_b128 a[232:235], v252 offset:4352\n" \
"v_add_f32 v242, v242, v23\n" \
"v_mfma_f32_16x16x32_bf16 a[88:91], a[240:243], v[64:67], a[88:91]\n" \
"v_exp_f32 v24, v24\n" \
"v_mfma_f32_16x16x32_bf16 a[88:91], a[244:247], v[96:99], a[88:91]\n" \
"v_add_f32 v243, v243, v24\n" \
"v_cvt_pk_bf16_f32 v19, v22, v23\n" \
"v_mfma_f32_16x16x32_bf16 a[120:123], a[240:243], v[72:75], a[120:123]\n" \
"v_exp_f32 v25, v25\n" \
"v_mfma_f32_16x16x32_bf16 a[120:123], a[244:247], v[104:107], a[120:123]\n" \
"ds_read_b128 a[236:239], v252 offset:6400\n" \
"v_add_f32 v243, v243, v25\n" \
"v_mfma_f32_16x16x32_bf16 v[152:155], a[240:243], v[80:83], v[152:155]\n" \
"v_exp_f32 v26, v26\n" \
"v_mfma_f32_16x16x32_bf16 v[152:155], a[244:247], v[112:115], v[152:155]\n" \
"v_add_f32 v243, v243, v26\n" \
"v_cvt_pk_bf16_f32 v24, v24, v25\n" \
"v_mfma_f32_16x16x32_bf16 v[184:187], a[240:243], v[88:91], v[184:187]\n" \
"v_exp_f32 v27, v27\n" \
"v_mfma_f32_16x16x32_bf16 v[184:187], a[244:247], v[120:123], v[184:187]\n" \
"ds_read_b128 a[240:243], v252 offset:12288\n" \
"v_add_f32 v243, v243, v27\n" \
"v_mfma_f32_16x16x32_bf16 a[92:95], a[248:251], v[64:67], a[92:95]\n" \
"v_exp_f32 v28, v28\n" \
"v_mfma_f32_16x16x32_bf16 a[92:95], a[252:255], v[96:99], a[92:95]\n" \
"v_add_f32 v243, v243, v28\n" \
"v_cvt_pk_bf16_f32 v25, v26, v27\n" \
"v_mfma_f32_16x16x32_bf16 a[124:127], a[248:251], v[72:75], a[124:127]\n" \
"v_exp_f32 v29, v29\n" \
"v_mfma_f32_16x16x32_bf16 a[124:127], a[252:255], v[104:107], a[124:127]\n" \
"ds_read_b128 a[244:247], v252 offset:14336\n" \
"v_add_f32 v243, v243, v29\n" \
"v_mfma_f32_16x16x32_bf16 v[156:159], a[248:251], v[80:83], v[156:159]\n" \
"v_exp_f32 v30, v30\n" \
"v_mfma_f32_16x16x32_bf16 v[156:159], a[252:255], v[112:115], v[156:159]\n" \
"v_add_f32 v243, v243, v30\n" \
"v_cvt_pk_bf16_f32 v26, v28, v29\n" \
"v_mfma_f32_16x16x32_bf16 v[188:191], a[248:251], v[88:91], v[188:191]\n" \
"s_waitcnt vmcnt(8) lgkmcnt(6)\n" \
"v_exp_f32 v31, v31\n" \
"v_mfma_f32_16x16x32_bf16 v[188:191], a[252:255], v[120:123], v[188:191]\n" \
"s_barrier\n" \
"ds_read_b128 a[248:251], v252 offset:12544\n" \
"v_add_f32 v243, v243, v31\n" \
"LA1g_%=:\n" \
"Lpg7663_%=:\n" \
"s_cbranch_execz Lcb0_%=\n" \
"v_mfma_f32_16x16x32_bf16 v[64:67], a[192:195], v[192:195], v[224:227]\n" \
"v_exp_f32 v32, v32\n" \
"v_mfma_f32_16x16x32_bf16 v[64:67], a[196:199], v[196:199], v[64:67]\n" \
"v_cvt_pk_bf16_f32 v27, v30, v31\n" \
"v_add_f32 v244, v244, v32\n" \
"v_mfma_f32_16x16x32_bf16 v[72:75], a[192:195], v[208:211], v[228:231]\n" \
"s_add_u32 m0, s74, 0x8000\n" \
"v_exp_f32 v33, v33\n" \
"v_mfma_f32_16x16x32_bf16 v[72:75], a[196:199], v[212:215], v[72:75]\n" \
"ds_read_b128 a[252:255], v252 offset:14592\n" \
"global_load_lds_dwordx4 v250, s[38:39] offset:-4096 sc1\n" \
"v_add_f32 v244, v244, v33\n" \
"v_mfma_f32_16x16x32_bf16 v[80:83], a[192:195], a[160:163], v[232:235]\n" \
"v_exp_f32 v34, v34\n" \
"v_mfma_f32_16x16x32_bf16 v[80:83], a[196:199], a[164:167], v[80:83]\n" \
"v_add_f32 v244, v244, v34\n" \
"v_cvt_pk_bf16_f32 v32, v32, v33\n" \
"v_mfma_f32_16x16x32_bf16 v[88:91], a[192:195], a[176:179], v[236:239]\n" \
"v_exp_f32 v35, v35\n" \
"v_mfma_f32_16x16x32_bf16 v[88:91], a[196:199], a[180:183], v[88:91]\n" \
"ds_read_b128 a[192:195], v247 offset:0\n" \
"global_load_lds_dwordx4 v250, s[38:39] offset:-3072 sc1\n" \
"v_add_f32 v244, v244, v35\n" \
"v_mfma_f32_16x16x32_bf16 v[68:71], a[200:203], v[192:195], v[224:227]\n" \
"v_exp_f32 v36, v36\n" \
"v_mfma_f32_16x16x32_bf16 v[68:71], a[204:207], v[196:199], v[68:71]\n" \
"v_add_f32 v244, v244, v36\n" \
"v_cvt_pk_bf16_f32 v33, v34, v35\n" \
"v_mfma_f32_16x16x32_bf16 v[76:79], a[200:203], v[208:211], v[228:231]\n" \
"v_exp_f32 v37, v37\n" \
"v_mfma_f32_16x16x32_bf16 v[76:79], a[204:207], v[212:215], v[76:79]\n" \
"ds_read_b128 a[196:199], v247 offset:8192\n" \
"v_add_f32 v244, v244, v37\n" \
"v_mfma_f32_16x16x32_bf16 v[84:87], a[200:203], a[160:163], v[232:235]\n" \
"v_exp_f32 v38, v38\n" \
"v_mfma_f32_16x16x32_bf16 v[84:87], a[204:207], a[164:167], v[84:87]\n" \
"v_add_f32 v244, v244, v38\n" \
"v_cvt_pk_bf16_f32 v34, v36, v37\n" \
"v_mfma_f32_16x16x32_bf16 v[92:95], a[200:203], a[176:179], v[236:239]\n" \
"v_exp_f32 v39, v39\n" \
"v_mfma_f32_16x16x32_bf16 v[92:95], a[204:207], a[180:183], v[92:95]\n" \
"ds_read_b128 a[200:203], v247 offset:1024\n" \
"global_load_lds_dwordx4 v250, s[38:39] offset:-2048 sc1\n" \
"v_add_f32 v244, v244, v39\n" \
"v_mfma_f32_16x16x32_bf16 v[96:99], a[208:211], v[192:195], v[224:227]\n" \
"v_exp_f32 v40, v40\n" \
"v_mfma_f32_16x16x32_bf16 v[96:99], a[212:215], v[196:199], v[96:99]\n" \
"v_add_f32 v245, v245, v40\n" \
"v_cvt_pk_bf16_f32 v35, v38, v39\n" \
"v_mfma_f32_16x16x32_bf16 v[104:107], a[208:211], v[208:211], v[228:231]\n" \
"v_exp_f32 v41, v41\n" \
"v_mfma_f32_16x16x32_bf16 v[104:107], a[212:215], v[212:215], v[104:107]\n" \
"ds_read_b128 a[204:207], v247 offset:9216\n" \
"global_load_lds_dwordx4 v250, s[38:39] offset:-1024 sc1\n" \
"v_add_f32 v245, v245, v41\n" \
"v_mfma_f32_16x16x32_bf16 v[112:115], a[208:211], a[160:163], v[232:235]\n" \
"v_exp_f32 v42, v42\n" \
"v_mfma_f32_16x16x32_bf16 v[112:115], a[212:215], a[164:167], v[112:115]\n" \
"v_add_f32 v245, v245, v42\n" \
"v_cvt_pk_bf16_f32 v40, v40, v41\n" \
"v_mfma_f32_16x16x32_bf16 v[120:123], a[208:211], a[176:179], v[236:239]\n" \
"v_exp_f32 v43, v43\n" \
"v_mfma_f32_16x16x32_bf16 v[120:123], a[212:215], a[180:183], v[120:123]\n" \
"ds_read_b128 a[208:211], v247 offset:2048\n" \
"v_add_f32 v245, v245, v43\n" \
"v_mfma_f32_16x16x32_bf16 v[100:103], a[216:219], v[192:195], v[224:227]\n" \
"v_exp_f32 v44, v44\n" \
"v_mfma_f32_16x16x32_bf16 v[100:103], a[220:223], v[196:199], v[100:103]\n" \
"v_add_f32 v245, v245, v44\n" \
"v_cvt_pk_bf16_f32 v41, v42, v43\n" \
"v_mfma_f32_16x16x32_bf16 v[108:111], a[216:219], v[208:211], v[228:231]\n" \
"v_exp_f32 v45, v45\n" \
"v_mfma_f32_16x16x32_bf16 v[108:111], a[220:223], v[212:215], v[108:111]\n" \
"ds_read_b128 a[212:215], v247 offset:10240\n" \
"global_load_lds_dwordx4 v250, s[38:39] offset:0 sc1\n" \
"v_add_f32 v245, v245, v45\n" \
"v_mfma_f32_16x16x32_bf16 v[116:119], a[216:219], a[160:163], v[232:235]\n" \
"v_exp_f32 v46, v46\n" \
"v_mfma_f32_16x16x32_bf16 v[116:119], a[220:223], a[164:167], v[116:119]\n" \
"v_add_f32 v245, v245, v46\n" \
"v_cvt_pk_bf16_f32 v42, v44, v45\n" \
"v_mfma_f32_16x16x32_bf16 v[124:127], a[216:219], a[176:179], v[236:239]\n" \
"s_waitcnt lgkmcnt(6)\n" \
"v_exp_f32 v47, v47\n" \
"v_mfma_f32_16x16x32_bf16 v[124:127], a[220:223], a[180:183], v[124:127]\n" \
"ds_read_b128 a[216:219], v247 offset:3072\n" \
"global_load_lds_dwordx4 v250, s[38:39] offset:1024 sc1\n" \
"v_add_f32 v245, v245, v47\n" \
"v_mfma_f32_16x16x32_bf16 v[64:67], a[224:227], v[200:203], v[64:67]\n" \
"v_exp_f32 v48, v48\n" \
"v_mfma_f32_16x16x32_bf16 v[64:67], a[228:231], v[204:207], v[64:67]\n" \
"v_add_f32 v242, v242, v48\n" \
"v_cvt_pk_bf16_f32 v43, v46, v47\n" \
"v_mfma_f32_16x16x32_bf16 v[72:75], a[224:227], v[216:219], v[72:75]\n" \
"v_exp_f32 v49, v49\n" \
"v_mfma_f32_16x16x32_bf16 v[72:75], a[228:231], v[220:223], v[72:75]\n" \
"ds_read_b128 a[220:223], v247 offset:11264\n" \
"v_add_f32 v242, v242, v49\n" \
"v_exp_f32 v50, v50\n" \
"v_mfma_f32_16x16x32_bf16 v[80:83], a[224:227], a[168:171], v[80:83]\n" \
"v_add_f32 v242, v242, v50\n" \
"v_cvt_pk_bf16_f32 v48, v48, v49\n" \
"v_mfma_f32_16x16x32_bf16 v[80:83], a[228:231], a[172:175], v[80:83]\n" \
"v_exp_f32 v51, v51\n" \
"v_mfma_f32_16x16x32_bf16 v[88:91], a[224:227], a[184:187], v[88:91]\n" \
"v_add_f32 v242, v242, v51\n" \
"v_exp_f32 v52, v52\n" \
"v_mfma_f32_16x16x32_bf16 v[88:91], a[228:231], a[188:191], v[88:91]\n" \
"ds_read_b128 a[224:227], v247 offset:4096\n" \
"global_load_lds_dwordx4 v250, s[38:39] offset:2048 sc1\n" \
"v_mfma_f32_16x16x32_bf16 v[68:71], a[232:235], v[200:203], v[68:71]\n" \
"v_add_f32 v242, v242, v52\n" \
"v_cvt_pk_bf16_f32 v49, v50, v51\n" \
"v_mfma_f32_16x16x32_bf16 v[68:71], a[236:239], v[204:207], v[68:71]\n" \
"v_exp_f32 v53, v53\n" \
"v_mfma_f32_16x16x32_bf16 v[76:79], a[232:235], v[216:219], v[76:79]\n" \
"v_add_f32 v242, v242, v53\n" \
"v_exp_f32 v54, v54\n" \
"v_mfma_f32_16x16x32_bf16 v[76:79], a[236:239], v[220:223], v[76:79]\n" \
"ds_read_b128 a[228:231], v247 offset:12288\n" \
"global_load_lds_dwordx4 v250, s[38:39] offset:3072 sc1\n" \
"v_mfma_f32_16x16x32_bf16 v[84:87], a[232:235], a[168:171], v[84:87]\n" \
"s_add_u32 s38, s38, 34816\n" \
"s_sub_u32 s76, s76, 1\n" \
"s_cmp_lg_u32 s76, 0\n" \
"v_mfma_f32_16x16x32_bf16 v[84:87], a[236:239], a[172:175], v[84:87]\n" \
"v_add_f32 v242, v242, v54\n" \
"v_cvt_pk_bf16_f32 v50, v52, v53\n" \
"v_mfma_f32_16x16x32_bf16 v[92:95], a[232:235], a[184:187], v[92:95]\n" \
"v_exp_f32 v55, v55\n" \
"v_mfma_f32_16x16x32_bf16 v[92:95], a[236:239], a[188:191], v[92:95]\n" \
"ds_read_b128 a[232:235], v247 offset:5120\n" \
"v_add_f32 v242, v242, v55\n" \
"v_mfma_f32_16x16x32_bf16 v[96:99], a[240:243], v[200:203], v[96:99]\n" \
"v_exp_f32 v56, v56\n" \
"v_mfma_f32_16x16x32_bf16 v[96:99], a[244:247], v[204:207], v[96:99]\n" \
"v_add_f32 v243, v243, v56\n" \
"v_cvt_pk_bf16_f32 v51, v54, v55\n" \
"v_mfma_f32_16x16x32_bf16 v[104:107], a[240:243], v[216:219], v[104:107]\n" \
"v_exp_f32 v57, v57\n" \
"v_mfma_f32_16x16x32_bf16 v[104:107], a[244:247], v[220:223], v[104:107]\n" \
"ds_read_b128 a[236:239], v247 offset:13312\n" \
"v_add_f32 v243, v243, v57\n" \
"v_mfma_f32_16x16x32_bf16 v[112:115], a[240:243], a[168:171], v[112:115]\n" \
"v_exp_f32 v58, v58\n" \
"v_mfma_f32_16x16x32_bf16 v[112:115], a[244:247], a[172:175], v[112:115]\n" \
"v_add_f32 v243, v243, v58\n" \
"v_cvt_pk_bf16_f32 v56, v56, v57\n" \
"v_mfma_f32_16x16x32_bf16 v[120:123], a[240:243], a[184:187], v[120:123]\n" \
"v_exp_f32 v59, v59\n" \
"v_mfma_f32_16x16x32_bf16 v[120:123], a[244:247], a[188:191], v[120:123]\n" \
"ds_read_b128 a[240:243], v247 offset:6144\n" \
"v_add_f32 v243, v243, v59\n" \
"v_mfma_f32_16x16x32_bf16 v[100:103], a[248:251], v[200:203], v[100:103]\n" \
"v_exp_f32 v60, v60\n" \
"v_mfma_f32_16x16x32_bf16 v[100:103], a[252:255], v[204:207], v[100:103]\n" \
"v_add_f32 v243, v243, v60\n" \
"v_cvt_pk_bf16_f32 v57, v58, v59\n" \
"v_mfma_f32_16x16x32_bf16 v[108:111], a[248:251], v[216:219], v[108:111]\n" \
"v_exp_f32 v61, v61\n" \
"v_mfma_f32_16x16x32_bf16 v[108:111], a[252:255], v[220:223], v[108:111]\n" \
"ds_read_b128 a[244:247], v247 offset:14336\n" \
"v_add_f32 v243, v243, v61\n" \
"v_mfma_f32_16x16x32_bf16 v[116:119], a[248:251], a[168:171], v[116:119]\n" \
"v_exp_f32 v62, v62\n" \
"v_mfma_f32_16x16x32_bf16 v[116:119], a[252:255], a[172:175], v[116:119]\n" \
"v_add_f32 v243, v243, v62\n" \
"v_cvt_pk_bf16_f32 v58, v60, v61\n" \
"v_mfma_f32_16x16x32_bf16 v[124:127], a[248:251], a[184:187], v[124:127]\n" \
"s_waitcnt vmcnt(8) lgkmcnt(6)\n" \
"v_exp_f32 v63, v63\n" \
"v_mfma_f32_16x16x32_bf16 v[124:127], a[252:255], a[188:191], v[124:127]\n" \
"s_barrier\n" \
"ds_read_b128 a[248:251], v247 offset:7168\n" \
"v_add_f32 v243, v243, v63\n" \
"s_cbranch_scc1 Lloop2_%=\n" \
"v_mfma_f32_16x16x32_bf16 a[64:67], a[192:195], v[0:3], a[64:67]\n" \
"v_subrev_u32 v246, s82, v246\n" \
"v_exp_f32 v64, v64\n" \
"v_mfma_f32_16x16x32_bf16 a[64:67], a[196:199], v[32:35], a[64:67]\n" \
"s_memrealtime s[44:45]\n" \
"v_cvt_pk_bf16_f32 v59, v62, v63\n" \
"v_add_f32 v244, v244, v64\n" \
"v_exp_f32 v65, v65\n" \
"v_mfma_f32_16x16x32_bf16 a[96:99], a[192:195], v[8:11], a[96:99]\n" \
"v_mov_b32 v208, v241\n" \
"v_add_f32 v244, v244, v65\n" \
"v_exp_f32 v66, v66\n" \
"v_mfma_f32_16x16x32_bf16 a[96:99], a[196:199], v[40:43], a[96:99]\n" \
"ds_read_b128 a[252:255], v247 offset:15360\n" \
"v_add_f32 v244, v244, v66\n" \
"v_cvt_pk_bf16_f32 v64, v64, v65\n" \
"v_exp_f32 v67, v67\n" \
"v_mfma_f32_16x16x32_bf16 v[128:131], a[192:195], v[16:19], v[128:131]\n" \
"v_add_f32 v244, v244, v67\n" \
"v_exp_f32 v68, v68\n" \
"v_permlane16_swap_b32 v208, v241\n" \
"v_add_f32 v241, v241, v208\n" \
"v_mov_b32 v208, v241\n" \
"v_mfma_f32_16x16x32_bf16 v[128:131], a[196:199], v[48:51], v[128:131]\n" \
"v_add_f32 v244, v244, v68\n" \
"v_cvt_pk_bf16_f32 v65, v66, v67\n" \
"v_exp_f32 v69, v69\n" \
"v_mfma_f32_16x16x32_bf16 v[160:163], a[192:195], v[24:27], v[160:163]\n" \
"v_add_f32 v244, v244, v69\n" \
"v_exp_f32 v70, v70\n" \
"v_permlane32_swap_b32 v208, v241\n" \
"v_add_f32 v241, v241, v208\n" \
"v_rcp_f32 v208, v241\n" \
"v_mul_f32 v241, 0x4d800000, v241\n" \
"v_add_u32 v209, 0x1c90080, v249\n" \
"v_cndmask_b32 v209, v249, v209, s[16:17]\n" \
"v_accvgpr_read_b32 v192, a32\n" \
"v_accvgpr_read_b32 v193, a33\n" \
"v_accvgpr_read_b32 v194, a34\n" \
"v_accvgpr_read_b32 v195, a35\n" \
"v_accvgpr_read_b32 v196, a36\n" \
"v_accvgpr_read_b32 v197, a37\n" \
"v_accvgpr_read_b32 v198, a38\n" \
"v_accvgpr_read_b32 v199, a39\n" \
"v_pk_mul_f32 v[192:193], v[192:193], v[208:209] op_sel_hi:[1,0]\n" \
"v_pk_mul_f32 v[194:195], v[194:195], v[208:209] op_sel_hi:[1,0]\n" \
"v_pk_mul_f32 v[196:197], v[196:197], v[208:209] op_sel_hi:[1,0]\n" \
"v_pk_mul_f32 v[198:199], v[198:199], v[208:209] op_sel_hi:[1,0]\n" \
"v_cvt_pk_bf16_f32 v200, v192, v193\n" \
"v_cvt_pk_bf16_f32 v201, v194, v195\n" \
"v_mfma_f32_16x16x32_bf16 v[160:163], a[196:199], v[56:59], v[160:163]\n" \
"ds_read_b128 a[192:195], v246 offset:0\n" \
"v_add_f32 v244, v244, v70\n" \
"v_cvt_pk_bf16_f32 v66, v68, v69\n" \
"v_cvt_pk_bf16_f32 v202, v196, v197\n" \
"v_cvt_pk_bf16_f32 v203, v198, v199\n" \
"v_pk_mul_f32 v[192:193], v[192:193], v[194:195]\n" \
"v_pk_fma_f32 v[192:193], v[196:197], v[198:199], v[192:193]\n" \
"v_fma_f32 v241, v192, v193, v241\n" \
"v_mfma_f32_16x16x32_bf16 a[68:71], a[200:203], v[0:3], a[68:71]\n" \
"v_exp_f32 v71, v71\n" \
"v_mfma_f32_16x16x32_bf16 a[68:71], a[204:207], v[32:35], a[68:71]\n" \
"s_waitcnt lgkmcnt(0)\n" \
"v_add_f32 v244, v244, v71\n" \
"v_exp_f32 v72, v72\n" \
"v_mfma_f32_16x16x32_bf16 a[100:103], a[200:203], v[8:11], a[100:103]\n" \
"s_barrier\n" \
"v_add_f32 v245, v245, v72\n" \
"v_cvt_pk_bf16_f32 v67, v70, v71\n" \
"v_mfma_f32_16x16x32_bf16 a[100:103], a[204:207], v[40:43], a[100:103]\n" \
"s_and_b32 s46, %1, 7\n" \
"ds_read_b128 a[196:199], v246 offset:2048\n" \
"s_lshl_b32 s46, s46, 2\n" \
"v_mfma_f32_16x16x32_bf16 v[132:135], a[200:203], v[16:19], v[132:135]\n" \
"s_lshr_b32 s47, %1, 6\n" \
"s_add_u32 s46, s46, s47\n" \
"s_lshl_b32 s46, s46, 3\n" \
"v_mfma_f32_16x16x32_bf16 v[132:135], a[204:207], v[48:51], v[132:135]\n" \
"s_add_u32 s46, s46, 65593856\n" \
"s_sub_u32 s45, s44, s12\n" \
"s_mul_i32 s45, s45, 3\n" \
"v_mfma_f32_16x16x32_bf16 v[164:167], a[200:203], v[24:27], v[164:167]\n" \
"s_lshl_b32 s44, s44, 1\n" \
"s_add_u32 s44, s44, s45\n" \
"v_mov_b32 v211, s44\n" \
"v_mfma_f32_16x16x32_bf16 v[164:167], a[204:207], v[56:59], v[164:167]\n" \
"s_mov_b32 m0, s74\n" \
"ds_read_b128 a[200:203], v246 offset:256\n" \
"v_xor_b32 v210, s60, v211\n" \
"v_mfma_f32_16x16x32_bf16 a[72:75], a[208:211], v[0:3], a[72:75]\n" \
"global_load_lds_dwordx4 v250, s[38:39] offset:-4096 sc1\n" \
"v_lshlrev_b32 v212, 28, s61\n" \
"v_mfma_f32_16x16x32_bf16 a[72:75], a[212:215], v[32:35], a[72:75]\n" \
"s_cmp_eq_u32 s63, 3\n" \
"s_cselect_b32 s46, s46, 0x20000000\n" \
"v_exp_f32 v73, v73\n" \
"v_mfma_f32_16x16x32_bf16 a[104:107], a[208:211], v[8:11], a[104:107]\n" \
"global_load_lds_dwordx4 v250, s[38:39] offset:-3072 sc1\n" \
"global_load_lds_dwordx4 v250, s[38:39] offset:-2048 sc1\n" \
"v_mfma_f32_16x16x32_bf16 a[104:107], a[212:215], v[40:43], a[104:107]\n" \
"ds_read_b128 a[204:207], v246 offset:2304\n" \
"v_add_f32 v245, v245, v73\n" \
"v_mfma_f32_16x16x32_bf16 v[136:139], a[208:211], v[16:19], v[136:139]\n" \
"v_exp_f32 v74, v74\n" \
"v_mfma_f32_16x16x32_bf16 v[136:139], a[212:215], v[48:51], v[136:139]\n" \
"global_load_lds_dwordx4 v250, s[38:39] offset:-1024 sc1\n" \
"v_add_f32 v245, v245, v74\n" \
"v_cvt_pk_bf16_f32 v72, v72, v73\n" \
"v_mfma_f32_16x16x32_bf16 v[168:171], a[208:211], v[24:27], v[168:171]\n" \
"v_exp_f32 v75, v75\n" \
"v_mfma_f32_16x16x32_bf16 v[168:171], a[212:215], v[56:59], v[168:171]\n" \
"ds_read_b128 a[208:211], v246 offset:8192\n" \
"v_add_f32 v245, v245, v75\n" \
"v_mfma_f32_16x16x32_bf16 a[76:79], a[216:219], v[0:3], a[76:79]\n" \
"global_load_lds_dwordx4 v250, s[38:39] offset:0 sc1\n" \
"v_exp_f32 v76, v76\n" \
"v_mfma_f32_16x16x32_bf16 a[76:79], a[220:223], v[32:35], a[76:79]\n" \
"v_add_f32 v245, v245, v76\n" \
"v_cvt_pk_bf16_f32 v73, v74, v75\n" \
"v_mfma_f32_16x16x32_bf16 a[108:111], a[216:219], v[8:11], a[108:111]\n" \
"v_exp_f32 v77, v77\n" \
"v_mfma_f32_16x16x32_bf16 a[108:111], a[220:223], v[40:43], a[108:111]\n" \
"ds_read_b128 a[212:215], v246 offset:10240\n" \
"global_load_lds_dwordx4 v250, s[38:39] offset:1024 sc1\n" \
"v_add_f32 v245, v245, v77\n" \
"v_mfma_f32_16x16x32_bf16 v[140:143], a[216:219], v[16:19], v[140:143]\n" \
"v_exp_f32 v78, v78\n" \
"v_mfma_f32_16x16x32_bf16 v[140:143], a[220:223], v[48:51], v[140:143]\n" \
"v_add_f32 v245, v245, v78\n" \
"v_cvt_pk_bf16_f32 v74, v76, v77\n" \
"v_mfma_f32_16x16x32_bf16 v[172:175], a[216:219], v[24:27], v[172:175]\n" \
"s_waitcnt lgkmcnt(6)\n" \
"global_load_lds_dwordx4 v250, s[38:39] offset:2048 sc1\n" \
"v_exp_f32 v79, v79\n" \
"v_mfma_f32_16x16x32_bf16 v[172:175], a[220:223], v[56:59], v[172:175]\n" \
"ds_read_b128 a[216:219], v246 offset:8448\n" \
"v_add_f32 v245, v245, v79\n" \
"v_mfma_f32_16x16x32_bf16 a[80:83], a[224:227], v[0:3], a[80:83]\n" \
"v_exp_f32 v80, v80\n" \
"v_mfma_f32_16x16x32_bf16 a[80:83], a[228:231], v[32:35], a[80:83]\n" \
"global_load_lds_dwordx4 v250, s[38:39] offset:3072 sc1\n" \
"v_add_f32 v242, v242, v80\n" \
"v_cvt_pk_bf16_f32 v75, v78, v79\n" \
"v_mfma_f32_16x16x32_bf16 a[112:115], a[224:227], v[8:11], a[112:115]\n" \
"v_exp_f32 v81, v81\n" \
"v_mfma_f32_16x16x32_bf16 a[112:115], a[228:231], v[40:43], a[112:115]\n" \
"ds_read_b128 a[220:223], v246 offset:10496\n" \
"v_add_f32 v242, v242, v81\n" \
"v_mfma_f32_16x16x32_bf16 v[144:147], a[224:227], v[16:19], v[144:147]\n" \
"v_exp_f32 v82, v82\n" \
"v_mfma_f32_16x16x32_bf16 v[144:147], a[228:231], v[48:51], v[144:147]\n" \
"v_add_f32 v242, v242, v82\n" \
"v_cvt_pk_bf16_f32 v80, v80, v81\n" \
"v_mfma_f32_16x16x32_bf16 v[176:179], a[224:227], v[24:27], v[176:179]\n" \
"v_exp_f32 v83, v83\n" \
"v_mfma_f32_16x16x32_bf16 v[176:179], a[228:231], v[56:59], v[176:179]\n" \
"ds_read_b128 a[224:227], v246 offset:4096\n" \
"v_add_f32 v242, v242, v83\n" \
"v_exp_f32 v84, v84\n" \
"v_mfma_f32_16x16x32_bf16 a[84:87], a[232:235], v[0:3], a[84:87]\n" \
"buffer_store_dwordx4 v[200:203], v209, s[56:59], s77 offen offset:0 sc1\n" \
"v_add_f32 v242, v242, v84\n" \
"v_cvt_pk_bf16_f32 v81, v82, v83\n" \
"v_accvgpr_read_b32 v192, a40\n" \
"v_accvgpr_read_b32 v193, a41\n" \
"v_accvgpr_read_b32 v194, a42\n" \
"v_accvgpr_read_b32 v195, a43\n" \
"v_accvgpr_read_b32 v196, a44\n" \
"v_accvgpr_read_b32 v197, a45\n" \
"v_accvgpr_read_b32 v198, a46\n" \
"v_accvgpr_read_b32 v199, a47\n" \
"v_pk_mul_f32 v[192:193], v[192:193], v[208:209] op_sel_hi:[1,0]\n" \
"v_pk_mul_f32 v[194:195], v[194:195], v[208:209] op_sel_hi:[1,0]\n" \
"v_pk_mul_f32 v[196:197], v[196:197], v[208:209] op_sel_hi:[1,0]\n" \
"v_pk_mul_f32 v[198:199], v[198:199], v[208:209] op_sel_hi:[1,0]\n" \
"v_cvt_pk_bf16_f32 v204, v192, v193\n" \
"v_cvt_pk_bf16_f32 v205, v194, v195\n" \
"v_cvt_pk_bf16_f32 v206, v196, v197\n" \
"v_cvt_pk_bf16_f32 v207, v198, v199\n" \
"v_pk_mul_f32 v[192:193], v[192:193], v[194:195]\n" \
"v_pk_fma_f32 v[192:193], v[196:197], v[198:199], v[192:193]\n" \
"v_fma_f32 v241, v192, v193, v241\n" \
"v_mfma_f32_16x16x32_bf16 a[84:87], a[236:239], v[32:35], a[84:87]\n" \
"buffer_store_dwordx2 v[210:211], v212, s[56:59], s46 offen sc1\n" \
"buffer_store_dwordx4 v[204:207], v209, s[56:59], s77 offen offset:2048 sc1\n" \
"s_add_u32 s77, s77, 0x1000\n" \
"v_accvgpr_read_b32 v192, a48\n" \
"v_accvgpr_read_b32 v193, a49\n" \
"v_accvgpr_read_b32 v194, a50\n" \
"v_accvgpr_read_b32 v195, a51\n" \
"v_mfma_f32_16x16x32_bf16 a[116:119], a[232:235], v[8:11], a[116:119]\n" \
"v_accvgpr_read_b32 v196, a52\n" \
"v_accvgpr_read_b32 v197, a53\n" \
"v_accvgpr_read_b32 v198, a54\n" \
"v_accvgpr_read_b32 v199, a55\n" \
"v_exp_f32 v85, v85\n" \
"v_pk_mul_f32 v[192:193], v[192:193], v[208:209] op_sel_hi:[1,0]\n" \
"v_pk_mul_f32 v[194:195], v[194:195], v[208:209] op_sel_hi:[1,0]\n" \
"v_pk_mul_f32 v[196:197], v[196:197], v[208:209] op_sel_hi:[1,0]\n" \
"v_pk_mul_f32 v[198:199], v[198:199], v[208:209] op_sel_hi:[1,0]\n" \
"v_cvt_pk_bf16_f32 v200, v192, v193\n" \
"v_cvt_pk_bf16_f32 v201, v194, v195\n" \
"v_cvt_pk_bf16_f32 v202, v196, v197\n" \
"v_cvt_pk_bf16_f32 v203, v198, v199\n" \
"v_pk_mul_f32 v[192:193], v[192:193], v[194:195]\n" \
"v_pk_fma_f32 v[192:193], v[196:197], v[198:199], v[192:193]\n" \
"v_fma_f32 v241, v192, v193, v241\n" \
"v_mfma_f32_16x16x32_bf16 a[116:119], a[236:239], v[40:43], a[116:119]\n" \
"buffer_store_dwordx4 v[200:203], v209, s[56:59], s77 offen offset:0 sc1\n" \
"ds_read_b128 a[228:231], v246 offset:6144\n" \
"v_add_f32 v242, v242, v85\n" \
"v_accvgpr_read_b32 v192, a56\n" \
"v_accvgpr_read_b32 v193, a57\n" \
"v_accvgpr_read_b32 v194, a58\n" \
"v_accvgpr_read_b32 v195, a59\n" \
"v_accvgpr_read_b32 v196, a60\n" \
"v_accvgpr_read_b32 v197, a61\n" \
"v_accvgpr_read_b32 v198, a62\n" \
"v_accvgpr_read_b32 v199, a63\n" \
"v_pk_mul_f32 v[192:193], v[192:193], v[208:209] op_sel_hi:[1,0]\n" \
"v_pk_mul_f32 v[194:195], v[194:195], v[208:209] op_sel_hi:[1,0]\n" \
"v_pk_mul_f32 v[196:197], v[196:197], v[208:209] op_sel_hi:[1,0]\n" \
"v_pk_mul_f32 v[198:199], v[198:199], v[208:209] op_sel_hi:[1,0]\n" \
"v_cvt_pk_bf16_f32 v204, v192, v193\n" \
"v_cvt_pk_bf16_f32 v205, v194, v195\n" \
"v_cvt_pk_bf16_f32 v206, v196, v197\n" \
"v_cvt_pk_bf16_f32 v207, v198, v199\n" \
"v_pk_mul_f32 v[192:193], v[192:193], v[194:195]\n" \
"v_pk_fma_f32 v[192:193], v[196:197], v[198:199], v[192:193]\n" \
"v_fma_f32 v241, v192, v193, v241\n" \
"v_mfma_f32_16x16x32_bf16 v[148:151], a[232:235], v[16:19], v[148:151]\n" \
"buffer_store_dwordx4 v[204:207], v209, s[56:59], s77 offen offset:2048 sc1\n" \
"v_exp_f32 v86, v86\n" \
"s_add_u32 s77, s77, 0xfff7d000\n" \
"v_mfma_f32_16x16x32_bf16 v[148:151], a[236:239], v[48:51], v[148:151]\n" \
"v_add_f32 v242, v242, v86\n" \
"v_cvt_pk_bf16_f32 v82, v84, v85\n" \
"v_mfma_f32_16x16x32_bf16 v[180:183], a[232:235], v[24:27], v[180:183]\n" \
"v_exp_f32 v87, v87\n" \
"v_mfma_f32_16x16x32_bf16 v[180:183], a[236:239], v[56:59], v[180:183]\n" \
"ds_read_b128 a[232:235], v246 offset:4352\n" \
"v_add_f32 v242, v242, v87\n" \
"v_mfma_f32_16x16x32_bf16 a[88:91], a[240:243], v[0:3], a[88:91]\n" \
"v_exp_f32 v88, v88\n" \
"v_mfma_f32_16x16x32_bf16 a[88:91], a[244:247], v[32:35], a[88:91]\n" \
"v_add_f32 v243, v243, v88\n" \
"v_cvt_pk_bf16_f32 v83, v86, v87\n" \
"v_mfma_f32_16x16x32_bf16 a[120:123], a[240:243], v[8:11], a[120:123]\n" \
"v_exp_f32 v89, v89\n" \
"v_mfma_f32_16x16x32_bf16 a[120:123], a[244:247], v[40:43], a[120:123]\n" \
"ds_read_b128 a[236:239], v246 offset:6400\n" \
"v_add_f32 v243, v243, v89\n" \
"v_mfma_f32_16x16x32_bf16 v[152:155], a[240:243], v[16:19], v[152:155]\n" \
"v_exp_f32 v90, v90\n" \
"v_mfma_f32_16x16x32_bf16 v[152:155], a[244:247], v[48:51], v[152:155]\n" \
"Lpg8095_%=:\n" \
"Lst1x1_%=:\n" \
"s_cbranch_execz Lpg10433_%=\n" \
"v_add_f32 v243, v243, v90\n" \
"v_cvt_pk_bf16_f32 v88, v88, v89\n" \
"v_mfma_f32_16x16x32_bf16 v[184:187], a[240:243], v[24:27], v[184:187]\n" \
"v_exp_f32 v91, v91\n" \
"v_mfma_f32_16x16x32_bf16 v[184:187], a[244:247], v[56:59], v[184:187]\n" \
"ds_read_b128 a[240:243], v246 offset:12288\n" \
"v_add_f32 v243, v243, v91\n" \
"v_mfma_f32_16x16x32_bf16 a[92:95], a[248:251], v[0:3], a[92:95]\n" \
"v_exp_f32 v92, v92\n" \
"v_mfma_f32_16x16x32_bf16 a[92:95], a[252:255], v[32:35], a[92:95]\n" \
"v_add_f32 v243, v243, v92\n" \
"v_cvt_pk_bf16_f32 v89, v90, v91\n" \
"v_mfma_f32_16x16x32_bf16 a[124:127], a[248:251], v[8:11], a[124:127]\n" \
"v_exp_f32 v93, v93\n" \
"v_mfma_f32_16x16x32_bf16 a[124:127], a[252:255], v[40:43], a[124:127]\n" \
"ds_read_b128 a[244:247], v246 offset:14336\n" \
"v_add_f32 v243, v243, v93\n" \
"v_mfma_f32_16x16x32_bf16 v[156:159], a[248:251], v[16:19], v[156:159]\n" \
"v_exp_f32 v94, v94\n" \
"v_mfma_f32_16x16x32_bf16 v[156:159], a[252:255], v[48:51], v[156:159]\n" \
"v_add_f32 v243, v243, v94\n" \
"v_cvt_pk_bf16_f32 v90, v92, v93\n" \
"v_mfma_f32_16x16x32_bf16 v[188:191], a[248:251], v[24:27], v[188:191]\n" \
"s_waitcnt vmcnt(13) lgkmcnt(6)\n" \
"v_exp_f32 v95, v95\n" \
"v_mfma_f32_16x16x32_bf16 v[188:191], a[252:255], v[56:59], v[188:191]\n" \
"s_barrier\n" \
"ds_read_b128 a[248:251], v246 offset:12544\n" \
"v_add_f32 v243, v243, v95\n" \
"v_mfma_f32_16x16x32_bf16 v[16:19], a[192:195], a[160:163], v[232:235]\n" \
"s_bfe_u32 s88, %1, 0x20001\n" \
"ds_read_b128 v[192:195], v246 offset:16384\n" \
"v_exp_f32 v96, v96\n" \
"s_lshl_b32 s88, s88, 2\n" \
"v_mfma_f32_16x16x32_bf16 v[16:19], a[196:199], a[164:167], v[16:19]\n" \
"s_lshr_b32 s70, %1, 6\n" \
"ds_read_b128 v[196:199], v246 offset:18432\n" \
"v_cvt_pk_bf16_f32 v91, v94, v95\n" \
"v_add_f32 v244, v244, v96\n" \
"v_exp_f32 v97, v97\n" \
"v_mfma_f32_16x16x32_bf16 v[24:27], a[192:195], a[176:179], v[236:239]\n" \
"s_add_u32 s88, s88, s70\n" \
"ds_read_b128 v[200:203], v246 offset:16640\n" \
"v_add_f32 v244, v244, v97\n" \
"v_exp_f32 v98, v98\n" \
"v_bfe_u32 v69, v248, 8, 1\n" \
"v_bfe_u32 v70, v248, 6, 2\n" \
"s_lshr_b32 s71, s22, 7\n" \
"v_mfma_f32_16x16x32_bf16 v[24:27], a[196:199], a[180:183], v[24:27]\n" \
"s_sub_u32 s71, s63, s71\n" \
"ds_read_b128 a[252:255], v246 offset:14592\n" \
"v_add_f32 v244, v244, v98\n" \
"v_cvt_pk_bf16_f32 v96, v96, v97\n" \
"v_exp_f32 v99, v99\n" \
"s_add_u32 s71, s71, 6\n" \
"v_mfma_f32_16x16x32_bf16 v[20:23], a[200:203], a[160:163], v[232:235]\n" \
"s_and_b32 s71, s71, 7\n" \
"ds_read_b128 v[204:207], v246 offset:18688\n" \
"v_add_f32 v244, v244, v99\n" \
"v_exp_f32 v100, v100\n" \
"v_lshl_add_u32 v70, v69, 3, v70\n" \
"v_mul_u32_u24 v70, 6144, v70\n" \
"v_mfma_f32_16x16x32_bf16 v[20:23], a[204:207], a[164:167], v[20:23]\n" \
"s_and_b32 s70, %1, 1\n" \
"ds_read_b128 v[208:211], v246 offset:24576\n" \
"v_add_f32 v244, v244, v100\n" \
"v_cvt_pk_bf16_f32 v97, v98, v99\n" \
"v_exp_f32 v101, v101\n" \
"s_lshl_b32 s70, s70, 3\n" \
"v_mfma_f32_16x16x32_bf16 v[28:31], a[200:203], a[176:179], v[236:239]\n" \
"s_add_u32 s70, s70, s71\n" \
"ds_read_b128 v[212:215], v246 offset:26624\n" \
"v_add_f32 v244, v244, v101\n" \
"v_exp_f32 v102, v102\n" \
"v_lshlrev_b32 v69, 5, v69\n" \
"v_and_b32 v71, 0x30, v248\n" \
"s_lshl_b32 s87, s70, 12\n" \
"v_mfma_f32_16x16x32_bf16 v[28:31], a[204:207], a[180:183], v[28:31]\n" \
"s_lshl_b32 s70, s70, 7\n" \
"ds_read_b128 v[216:219], v246 offset:24832\n" \
"v_add_f32 v244, v244, v102\n" \
"v_cvt_pk_bf16_f32 v98, v100, v101\n" \
"v_exp_f32 v103, v103\n" \
"s_mul_i32 s84, s61, 6144\n" \
"v_mfma_f32_16x16x32_bf16 v[48:51], a[208:211], a[160:163], v[232:235]\n" \
"s_lshl_b32 s71, s84, 4\n" \
"ds_read_b128 v[220:223], v246 offset:26880\n" \
"v_add_f32 v244, v244, v103\n" \
"v_exp_f32 v104, v104\n" \
"v_xor_b32 v69, v69, v71\n" \
"v_mfma_f32_16x16x32_bf16 v[48:51], a[212:215], a[164:167], v[48:51]\n" \
"ds_read_b128 a[192:195], v247 offset:32768\n" \
"v_lshrrev_b32 v71, 9, v248\n" \
"v_add_f32 v245, v245, v104\n" \
"v_cvt_pk_bf16_f32 v99, v102, v103\n" \
"v_exp_f32 v105, v105\n" \
"v_mfma_f32_16x16x32_bf16 v[56:59], a[208:211], a[176:179], v[236:239]\n" \
"s_add_u32 s70, s70, s71\n" \
"ds_read_b128 a[196:199], v247 offset:40960\n" \
"v_add_f32 v245, v245, v105\n" \
"v_exp_f32 v106, v106\n" \
"s_add_u32 s70, s70, 4096\n" \
"v_mfma_f32_16x16x32_bf16 v[56:59], a[212:215], a[180:183], v[56:59]\n" \
"s_waitcnt lgkmcnt(4)\n" \
"ds_read_b128 a[200:203], v247 offset:33792\n" \
"v_lshl_add_u32 v70, v71, 6, v70\n" \
"v_add_f32 v245, v245, v106\n" \
"v_cvt_pk_bf16_f32 v104, v104, v105\n" \
"v_exp_f32 v107, v107\n" \
"v_mfma_f32_16x16x32_bf16 v[8:11], v[192:195], a[176:179], v[236:239]\n" \
"ds_read_b128 a[204:207], v247 offset:41984\n" \
"v_add_u32 v253, v69, v70\n" \
"v_add_f32 v245, v245, v107\n" \
"v_exp_f32 v108, v108\n" \
"v_mfma_f32_16x16x32_bf16 v[8:11], v[196:199], a[180:183], v[8:11]\n" \
"s_add_u32 s48, s22, s70\n" \
"s_addc_u32 s49, s23, 0\n" \
"ds_read_b128 a[208:211], v247 offset:34816\n" \
"v_add_f32 v245, v245, v108\n" \
"v_cvt_pk_bf16_f32 v105, v106, v107\n" \
"v_exp_f32 v109, v109\n" \
"v_mfma_f32_16x16x32_bf16 v[52:55], a[216:219], a[160:163], v[232:235]\n" \
"s_add_u32 s50, s48, 23552\n" \
"s_addc_u32 s51, s49, 0\n" \
"ds_read_b128 a[212:215], v247 offset:43008\n" \
"v_add_f32 v245, v245, v109\n" \
"v_exp_f32 v110, v110\n" \
"v_mfma_f32_16x16x32_bf16 v[52:55], a[220:223], a[164:167], v[52:55]\n" \
"s_lshr_b32 s89, s22, 7\n" \
"ds_read_b128 v[192:195], v246 offset:20480\n" \
"v_add_f32 v245, v245, v110\n" \
"v_cvt_pk_bf16_f32 v106, v108, v109\n" \
"v_exp_f32 v111, v111\n" \
"s_sub_u32 s89, 3, s89\n" \
"v_mfma_f32_16x16x32_bf16 v[60:63], a[216:219], a[176:179], v[236:239]\n" \
"ds_read_b128 v[196:199], v246 offset:22528\n" \
"v_bfe_u32 v84, v248, 7, 2\n" \
"v_add_f32 v245, v245, v111\n" \
"v_exp_f32 v112, v112\n" \
"v_mfma_f32_16x16x32_bf16 v[60:63], a[220:223], a[180:183], v[60:63]\n" \
"s_and_b32 s89, s89, 7\n" \
"ds_read_b128 a[216:219], v247 offset:35840\n" \
"v_add_f32 v242, v242, v112\n" \
"v_cvt_pk_bf16_f32 v107, v110, v111\n" \
"v_exp_f32 v113, v113\n" \
"s_and_b32 s90, %1, 1\n" \
"v_mfma_f32_16x16x32_bf16 v[12:15], v[200:203], a[176:179], v[236:239]\n" \
"ds_read_b128 a[220:223], v247 offset:44032\n" \
"v_bfe_u32 v85, v248, 5, 2\n" \
"v_add_f32 v242, v242, v113\n" \
"v_exp_f32 v114, v114\n" \
"v_mfma_f32_16x16x32_bf16 v[12:15], v[204:207], a[180:183], v[12:15]\n" \
"s_lshl_b32 s90, s90, 3\n" \
"ds_read_b128 v[200:203], v246 offset:20736\n" \
"v_add_f32 v242, v242, v114\n" \
"v_cvt_pk_bf16_f32 v112, v112, v113\n" \
"v_exp_f32 v115, v115\n" \
"s_add_u32 s89, s89, s90\n" \
"v_mfma_f32_16x16x32_bf16 v[16:19], a[224:227], a[168:171], v[16:19]\n" \
"ds_read_b128 v[204:207], v246 offset:22784\n" \
"v_min_u32 v85, 2, v85\n" \
"v_add_f32 v242, v242, v115\n" \
"v_exp_f32 v116, v116\n" \
"v_mfma_f32_16x16x32_bf16 v[16:19], a[228:231], a[172:175], v[16:19]\n" \
"s_lshl_b32 s89, s89, 7\n" \
"v_add_f32 v242, v242, v116\n" \
"v_cvt_pk_bf16_f32 v113, v114, v115\n" \
"v_exp_f32 v117, v117\n" \
"s_lshr_b32 s90, %1, 3\n" \
"v_mad_u32_u24 v84, v84, 3, v85\n" \
"v_mfma_f32_16x16x32_bf16 v[24:27], a[224:227], a[184:187], v[24:27]\n" \
"s_lshl_b32 s90, s90, 2\n" \
"v_add_f32 v242, v242, v117\n" \
"v_exp_f32 v118, v118\n" \
"s_add_u32 s90, s90, s61\n" \
"v_and_b32 v85, 16, v248\n" \
"v_mfma_f32_16x16x32_bf16 v[24:27], a[228:231], a[188:191], v[24:27]\n" \
"s_mul_i32 s90, s90, 24576\n" \
"ds_read_b128 a[224:227], v247 offset:36864\n" \
"v_add_f32 v242, v242, v118\n" \
"v_cvt_pk_bf16_f32 v114, v116, v117\n" \
"s_add_u32 s89, s89, s90\n" \
"v_mfma_f32_16x16x32_bf16 v[40:43], v[208:211], a[176:179], v[236:239]\n" \
"ds_read_b128 a[228:231], v247 offset:45056\n" \
"v_lshl_add_u32 v85, v85, 2, s89\n" \
"v_mfma_f32_16x16x32_bf16 v[40:43], v[212:215], a[180:183], v[40:43]\n" \
"ds_read_b128 v[208:211], v246 offset:28672\n" \
"v_lshl_add_u32 v84, v84, 11, v85\n" \
"v_mfma_f32_16x16x32_bf16 v[20:23], a[232:235], a[168:171], v[20:23]\n" \
"s_add_u32 m0, s84, 143360\n" \
"ds_read_b128 v[212:215], v246 offset:30720\n" \
"v_exp_f32 v119, v119\n" \
"v_mfma_f32_16x16x32_bf16 v[20:23], a[236:239], a[172:175], v[20:23]\n" \
"global_load_lds_dwordx4 v253, s[48:49] offset:-4096\n" \
"v_add_f32 v242, v242, v119\n" \
"v_mfma_f32_16x16x32_bf16 v[28:31], a[232:235], a[184:187], v[28:31]\n" \
"v_exp_f32 v120, v120\n" \
"v_mfma_f32_16x16x32_bf16 v[28:31], a[236:239], a[188:191], v[28:31]\n" \
"s_waitcnt lgkmcnt(2)\n" \
"ds_read_b128 a[232:235], v247 offset:37888\n" \
"v_add_f32 v243, v243, v120\n" \
"v_cvt_pk_bf16_f32 v115, v118, v119\n" \
"v_mfma_f32_16x16x32_bf16 v[44:47], v[216:219], a[176:179], v[236:239]\n" \
"ds_read_b128 a[236:239], v247 offset:46080\n" \
"global_load_lds_dwordx4 v253, s[48:49] offset:-2048\n" \
"v_mfma_f32_16x16x32_bf16 v[44:47], v[220:223], a[180:183], v[44:47]\n" \
"ds_read_b128 v[216:219], v246 offset:28928\n" \
"v_exp_f32 v121, v121\n" \
"v_mfma_f32_16x16x32_bf16 v[48:51], a[240:243], a[168:171], v[48:51]\n" \
"ds_read_b128 v[220:223], v246 offset:30976\n" \
"v_add_f32 v243, v243, v121\n" \
"v_mfma_f32_16x16x32_bf16 v[48:51], a[244:247], a[172:175], v[48:51]\n" \
"global_load_lds_dwordx4 v253, s[48:49] offset:0\n" \
"v_exp_f32 v122, v122\n" \
"v_mfma_f32_16x16x32_bf16 v[56:59], a[240:243], a[184:187], v[56:59]\n" \
"s_add_u32 s48, s48, 0x120000\n" \
"s_addc_u32 s49, s49, 0\n" \
"v_add_f32 v243, v243, v122\n" \
"v_mfma_f32_16x16x32_bf16 v[56:59], a[244:247], a[188:191], v[56:59]\n" \
"ds_read_b128 a[240:243], v247 offset:38912\n" \
"v_cvt_pk_bf16_f32 v120, v120, v121\n" \
"v_mfma_f32_16x16x32_bf16 v[8:11], v[192:195], a[184:187], v[8:11]\n" \
"ds_read_b128 a[244:247], v247 offset:47104\n" \
"global_load_lds_dwordx4 v253, s[50:51] offset:-3072\n" \
"v_mfma_f32_16x16x32_bf16 v[8:11], v[196:199], a[188:191], v[8:11]\n" \
"v_exp_f32 v123, v123\n" \
"v_mfma_f32_16x16x32_bf16 v[52:55], a[248:251], a[168:171], v[52:55]\n" \
"v_add_f32 v243, v243, v123\n" \
"v_exp_f32 v124, v124\n" \
"v_mfma_f32_16x16x32_bf16 v[52:55], a[252:255], a[172:175], v[52:55]\n" \
"global_load_lds_dwordx4 v253, s[50:51] offset:-1024\n" \
"v_add_f32 v243, v243, v124\n" \
"v_cvt_pk_bf16_f32 v121, v122, v123\n" \
"v_mfma_f32_16x16x32_bf16 v[60:63], a[248:251], a[184:187], v[60:63]\n" \
"v_exp_f32 v125, v125\n" \
"v_mfma_f32_16x16x32_bf16 v[60:63], a[252:255], a[188:191], v[60:63]\n" \
"ds_read_b128 a[248:251], v247 offset:39936\n" \
"v_add_f32 v243, v243, v125\n" \
"v_mfma_f32_16x16x32_bf16 v[12:15], v[200:203], a[184:187], v[12:15]\n" \
"ds_read_b128 a[252:255], v247 offset:48128\n" \
"global_load_lds_dwordx4 v253, s[50:51] offset:1024\n" \
"v_mfma_f32_16x16x32_bf16 v[12:15], v[204:207], a[188:191], v[12:15]\n" \
"s_waitcnt lgkmcnt(2)\n" \
"s_add_u32 s50, s50, 0x120000\n" \
"s_addc_u32 s51, s51, 0\n" \
"v_mfma_f32_16x16x32_bf16 v[40:43], v[208:211], a[184:187], v[40:43]\n" \
"v_exp_f32 v126, v126\n" \
"v_mfma_f32_16x16x32_bf16 v[40:43], v[212:215], a[188:191], v[40:43]\n" \
"global_load_dword v85, v84, s[22:23]\n" \
"v_mfma_f32_16x16x32_bf16 v[44:47], v[216:219], a[184:187], v[44:47]\n" \
"s_waitcnt vmcnt(12) lgkmcnt(0)\n" \
"v_add_u32 v247, 0x10000, v248\n" \
"v_add_f32 v243, v243, v126\n" \
"v_mfma_f32_16x16x32_bf16 v[44:47], v[220:223], a[188:191], v[44:47]\n" \
"s_barrier\n" \
"v_cvt_pk_bf16_f32 v122, v124, v125\n" \
"v_mfma_f32_16x16x32_bf16 a[64:67], a[192:195], v[64:67], a[64:67]\n" \
"v_exp_f32 v127, v127\n" \
"v_mfma_f32_16x16x32_bf16 a[64:67], a[196:199], v[96:99], a[64:67]\n" \
"v_add_f32 v243, v243, v127\n" \
"v_cvt_pk_bf16_f32 v123, v126, v127\n" \
"v_mfma_f32_16x16x32_bf16 a[96:99], a[192:195], v[72:75], a[96:99]\n" \
"s_lshl_b32 s70, s61, 14\n" \
"ds_read_b128 v[192:195], v247 offset:16384\n" \
"v_cmp_gt_i32 vcc, 0, v251\n" \
"v_cmp_gt_i32 s[64:65], 1, v251\n" \
"v_cmp_gt_i32 s[66:67], 2, v251\n" \
"v_cndmask_b32 v8, v8, v254, vcc\n" \
"v_cndmask_b32 v16, v16, v254, vcc\n" \
"v_cmp_gt_i32 vcc, 3, v251\n" \
"v_add_u32 v124, s70, v248\n" \
"v_xor_b32 v125, 0x4000, v124\n" \
"v_mfma_f32_16x16x32_bf16 a[96:99], a[196:199], v[104:107], a[96:99]\n" \
"s_lshl_b32 s70, s61, 9\n" \
"s_add_u32 s70, s70, 110592\n" \
"v_cndmask_b32 v9, v9, v254, s[64:65]\n" \
"v_cndmask_b32 v17, v17, v254, s[64:65]\n" \
"v_cmp_gt_i32 s[64:65], 16, v251\n" \
"v_cndmask_b32 v10, v10, v254, s[66:67]\n" \
"v_cndmask_b32 v18, v18, v254, s[66:67]\n" \
"v_cmp_gt_i32 s[66:67], 17, v251\n" \
"v_lshrrev_b32 v126, 1, v248\n" \
"v_mfma_f32_16x16x32_bf16 v[128:131], a[192:195], v[80:83], v[128:131]\n" \
"v_add_u32 v126, s70, v126\n" \
"v_xor_b32 v127, 0x200, v126\n" \
"v_cndmask_b32 v11, v11, v254, vcc\n" \
"v_cndmask_b32 v19, v19, v254, vcc\n" \
"v_cmp_gt_i32 vcc, 18, v251\n" \
"v_cndmask_b32 v12, v12, v254, s[64:65]\n" \
"v_cndmask_b32 v20, v20, v254, s[64:65]\n" \
"v_cmp_gt_i32 s[64:65], 19, v251\n" \
"v_mfma_f32_16x16x32_bf16 v[128:131], a[196:199], v[112:115], v[128:131]\n" \
"v_lshrrev_b32 v71, 7, v248\n" \
"v_and_b32 v76, 0x70, v248\n" \
"v_cndmask_b32 v13, v13, v254, s[66:67]\n" \
"v_cndmask_b32 v21, v21, v254, s[66:67]\n" \
"v_cmp_gt_i32 s[66:67], 32, v251\n" \
"v_cndmask_b32 v14, v14, v254, vcc\n" \
"v_cndmask_b32 v22, v22, v254, vcc\n" \
"v_cmp_gt_i32 vcc, 33, v251\n" \
"v_mfma_f32_16x16x32_bf16 v[160:163], a[192:195], v[88:91], v[160:163]\n" \
"s_lshl_b32 s70, s88, 18\n" \
"s_lshl_b32 s71, s61, 16\n" \
"ds_read_b128 v[196:199], v247 offset:24576\n" \
"v_cndmask_b32 v15, v15, v254, s[64:65]\n" \
"v_cndmask_b32 v23, v23, v254, s[64:65]\n" \
"v_cmp_gt_i32 s[64:65], 34, v251\n" \
"v_cndmask_b32 v40, v40, v254, s[66:67]\n" \
"v_cndmask_b32 v48, v48, v254, s[66:67]\n" \
"v_cmp_gt_i32 s[66:67], 35, v251\n" \
"v_mfma_f32_16x16x32_bf16 v[160:163], a[196:199], v[120:123], v[160:163]\n" \
"s_add_u32 s71, s71, s70\n" \
"ds_read_b128 a[192:195], v247 offset:0\n" \
"v_cndmask_b32 v41, v41, v254, vcc\n" \
"v_cndmask_b32 v49, v49, v254, vcc\n" \
"v_cmp_gt_i32 vcc, 48, v251\n" \
"v_cndmask_b32 v42, v42, v254, s[64:65]\n" \
"v_cndmask_b32 v50, v50, v254, s[64:65]\n" \
"v_cmp_gt_i32 s[64:65], 49, v251\n" \
"v_lshlrev_b32 v77, 4, v71\n" \
"v_xor_b32 v77, v77, v76\n" \
"v_mfma_f32_16x16x32_bf16 a[68:71], a[200:203], v[64:67], a[68:71]\n" \
"s_add_u32 s42, s20, s71\n" \
"s_addc_u32 s43, s21, 0\n" \
"ds_write_b128 v124, a[96:99] offset:8192\n" \
"v_cndmask_b32 v43, v43, v254, s[66:67]\n" \
"v_cndmask_b32 v51, v51, v254, s[66:67]\n" \
"v_cmp_gt_i32 s[66:67], 50, v251\n" \
"v_cndmask_b32 v44, v44, v254, vcc\n" \
"v_cndmask_b32 v52, v52, v254, vcc\n" \
"v_cmp_gt_i32 vcc, 51, v251\n" \
"v_mfma_f32_16x16x32_bf16 a[68:71], a[204:207], v[96:99], a[68:71]\n" \
"ds_write_b128 v124, a[64:67] offset:0\n" \
"v_cndmask_b32 v45, v45, v254, s[64:65]\n" \
"v_cndmask_b32 v53, v53, v254, s[64:65]\n" \
"v_cndmask_b32 v46, v46, v254, s[66:67]\n" \
"v_cndmask_b32 v54, v54, v254, s[66:67]\n" \
"v_cndmask_b32 v47, v47, v254, vcc\n" \
"v_cndmask_b32 v55, v55, v254, vcc\n" \
"v_mfma_f32_16x16x32_bf16 a[100:103], a[200:203], v[72:75], a[100:103]\n" \
"ds_read_b128 v[200:203], v247 offset:17408\n" \
"v_lshl_add_u32 v252, v71, 12, v77\n" \
"v_exp_f32 v8, v8\n" \
"v_mfma_f32_16x16x32_bf16 a[100:103], a[204:207], v[104:107], a[100:103]\n" \
"s_add_u32 s44, s42, 31744\n" \
"s_addc_u32 s45, s43, 0\n" \
"ds_read_b128 a[196:199], v247 offset:8192\n" \
"v_add_f32 v243, v243, v8\n" \
"v_exp_f32 v9, v9\n" \
"v_mfma_f32_16x16x32_bf16 v[132:135], a[200:203], v[80:83], v[132:135]\n" \
"s_max_u32 s70, s70, 32768\n" \
"ds_write_b128 v124, a[68:71] offset:1024\n" \
"v_add_f32 v243, v243, v9\n" \
"v_exp_f32 v10, v10\n" \
"v_mfma_f32_16x16x32_bf16 v[132:135], a[204:207], v[112:115], v[132:135]\n" \
"s_sub_u32 s70, s70, 32768\n" \
"ds_write_b64 v126, v[244:245]\n" \
"v_add_f32 v243, v243, v10\n" \
"v_cvt_pk_bf16_f32 v8, v8, v9\n" \
"v_exp_f32 v11, v11\n" \
"v_mfma_f32_16x16x32_bf16 v[164:167], a[200:203], v[88:91], v[164:167]\n" \
"s_and_b32 s71, s61, 1\n" \
"ds_read_b128 v[204:207], v247 offset:25600\n" \
"v_add_f32 v243, v243, v11\n" \
"v_exp_f32 v12, v12\n" \
"s_lshl_b32 s71, s71, 2\n" \
"v_mfma_f32_16x16x32_bf16 v[164:167], a[204:207], v[120:123], v[164:167]\n" \
"ds_read_b128 a[200:203], v247 offset:1024\n" \
"v_and_or_b32 v71, v71, 3, s71\n" \
"v_add_f32 v243, v243, v12\n" \
"v_cvt_pk_bf16_f32 v9, v10, v11\n" \
"v_exp_f32 v13, v13\n" \
"v_mfma_f32_16x16x32_bf16 a[72:75], a[208:211], v[64:67], a[72:75]\n" \
"s_lshr_b32 s71, s61, 1\n" \
"ds_write_b128 v124, a[100:103] offset:9216\n" \
"v_add_f32 v243, v243, v13\n" \
"v_exp_f32 v14, v14\n" \
"v_mfma_f32_16x16x32_bf16 a[72:75], a[212:215], v[96:99], a[72:75]\n" \
"s_lshl_b32 s71, s71, 11\n" \
"v_add_f32 v243, v243, v14\n" \
"v_cvt_pk_bf16_f32 v10, v12, v13\n" \
"v_exp_f32 v15, v15\n" \
"v_lshlrev_b32 v77, 4, v71\n" \
"s_add_u32 s70, s70, s71\n" \
"v_xor_b32 v77, v77, v76\n" \
"v_mfma_f32_16x16x32_bf16 a[104:107], a[208:211], v[72:75], a[104:107]\n" \
"s_add_u32 s46, s20, s70\n" \
"s_addc_u32 s47, s21, 0\n" \
"ds_read_b128 v[208:211], v247 offset:18432\n" \
"v_add_f32 v243, v243, v15\n" \
"v_exp_f32 v40, v40\n" \
"v_lshl_add_u32 v246, v71, 12, v77\n" \
"v_mfma_f32_16x16x32_bf16 a[104:107], a[212:215], v[104:107], a[104:107]\n" \
"s_lshl_b32 s85, s61, 10\n" \
"ds_read_b128 a[204:207], v247 offset:9216\n" \
"v_add_f32 v243, v243, v40\n" \
"v_cvt_pk_bf16_f32 v11, v14, v15\n" \
"v_exp_f32 v41, v41\n" \
"s_mov_b64 s[12:13], s[20:21]\n" \
"v_mfma_f32_16x16x32_bf16 v[136:139], a[208:211], v[80:83], v[136:139]\n" \
"s_and_b32 s13, s13, 0xffff\n" \
"ds_write_b128 v124, a[72:75] offset:2048\n" \
"v_add_f32 v243, v243, v41\n" \
"v_exp_f32 v42, v42\n" \
"v_mfma_f32_16x16x32_bf16 v[136:139], a[212:215], v[112:115], v[136:139]\n" \
"s_mov_b32 s14, 4194304\n" \
"v_add_f32 v243, v243, v42\n" \
"v_cvt_pk_bf16_f32 v40, v40, v41\n" \
"v_exp_f32 v43, v43\n" \
"s_mov_b32 s15, 0x20000\n" \
"s_mov_b32 s46, s70\n" \
"v_mfma_f32_16x16x32_bf16 v[168:171], a[208:211], v[88:91], v[168:171]\n" \
"ds_read_b128 v[212:215], v247 offset:26624\n" \
"v_lshrrev_b32 v71, 9, v248\n" \
"v_add_f32 v243, v243, v43\n" \
"v_exp_f32 v44, v44\n" \
"v_mfma_f32_16x16x32_bf16 v[168:171], a[212:215], v[120:123], v[168:171]\n" \
"ds_read_b128 a[208:211], v247 offset:2048\n" \
"v_lshl_add_u32 v246, v71, 30, v246\n" \
"v_add_f32 v243, v243, v44\n" \
"v_cvt_pk_bf16_f32 v41, v42, v43\n" \
"v_exp_f32 v45, v45\n" \
"v_mfma_f32_16x16x32_bf16 a[76:79], a[216:219], v[64:67], a[76:79]\n" \
"s_add_u32 m0, s95, 118784\n" \
"ds_write_b128 v124, a[104:107] offset:10240\n" \
"v_add_f32 v243, v243, v45\n" \
"v_exp_f32 v46, v46\n" \
"v_mfma_f32_16x16x32_bf16 a[76:79], a[220:223], v[96:99], a[76:79]\n" \
"global_load_lds_dwordx4 v252, s[42:43] offset:0\n" \
"v_add_f32 v243, v243, v46\n" \
"v_cvt_pk_bf16_f32 v42, v44, v45\n" \
"v_exp_f32 v47, v47\n" \
"v_mfma_f32_16x16x32_bf16 a[108:111], a[216:219], v[72:75], a[108:111]\n" \
"ds_read_b128 v[216:219], v247 offset:19456\n" \
"v_add_f32 v243, v243, v47\n" \
"v_cvt_pk_bf16_f32 v43, v46, v47\n" \
"v_exp_f32 v24, v24\n" \
"v_mfma_f32_16x16x32_bf16 a[108:111], a[220:223], v[104:107], a[108:111]\n" \
"ds_read_b128 a[212:215], v247 offset:10240\n" \
"global_load_lds_dwordx4 v252, s[42:43] offset:2048\n" \
"v_add_f32 v243, v243, v24\n" \
"v_exp_f32 v25, v25\n" \
"v_mfma_f32_16x16x32_bf16 v[140:143], a[216:219], v[80:83], v[140:143]\n" \
"ds_write_b128 v124, a[76:79] offset:3072\n" \
"v_add_f32 v243, v243, v25\n" \
"v_exp_f32 v26, v26\n" \
"v_mfma_f32_16x16x32_bf16 v[140:143], a[220:223], v[112:115], v[140:143]\n" \
"s_add_u32 s42, s42, 0x180\n" \
"s_addc_u32 s43, s43, 0\n" \
"v_add_f32 v243, v243, v26\n" \
"v_cvt_pk_bf16_f32 v24, v24, v25\n" \
"v_exp_f32 v27, v27\n" \
"v_mfma_f32_16x16x32_bf16 v[172:175], a[216:219], v[88:91], v[172:175]\n" \
"ds_read_b128 v[220:223], v247 offset:27648\n" \
"v_add_f32 v243, v243, v27\n" \
"v_exp_f32 v28, v28\n" \
"v_mfma_f32_16x16x32_bf16 v[172:175], a[220:223], v[120:123], v[172:175]\n" \
"ds_read_b128 a[216:219], v247 offset:3072\n" \
"global_load_lds_dwordx4 v252, s[44:45] offset:1024\n" \
"v_add_f32 v243, v243, v28\n" \
"v_cvt_pk_bf16_f32 v25, v26, v27\n" \
"v_exp_f32 v29, v29\n" \
"v_mfma_f32_16x16x32_bf16 a[80:83], a[224:227], v[64:67], a[80:83]\n" \
"ds_write_b128 v124, a[108:111] offset:11264\n" \
"v_add_f32 v243, v243, v29\n" \
"v_exp_f32 v30, v30\n" \
"v_mfma_f32_16x16x32_bf16 a[80:83], a[228:231], v[96:99], a[80:83]\n" \
"v_add_f32 v243, v243, v30\n" \
"v_cvt_pk_bf16_f32 v26, v28, v29\n" \
"v_exp_f32 v31, v31\n" \
"v_mfma_f32_16x16x32_bf16 a[112:115], a[224:227], v[72:75], a[112:115]\n" \
"ds_read_b128 v[224:227], v247 offset:20480\n" \
"global_load_lds_dwordx4 v252, s[44:45] offset:3072\n" \
"v_add_f32 v243, v243, v31\n" \
"v_exp_f32 v56, v56\n" \
"v_mfma_f32_16x16x32_bf16 a[112:115], a[228:231], v[104:107], a[112:115]\n" \
"s_add_u32 s44, s44, 0x180\n" \
"s_addc_u32 s45, s45, 0\n" \
"ds_read_b128 a[220:223], v247 offset:11264\n" \
"v_add_f32 v243, v243, v56\n" \
"v_cvt_pk_bf16_f32 v27, v30, v31\n" \
"v_exp_f32 v57, v57\n" \
"v_mfma_f32_16x16x32_bf16 v[144:147], a[224:227], v[80:83], v[144:147]\n" \
"s_add_u32 m0, s85, 135168\n" \
"ds_write_b128 v124, a[80:83] offset:4096\n" \
"v_add_f32 v243, v243, v57\n" \
"v_exp_f32 v58, v58\n" \
"v_mfma_f32_16x16x32_bf16 v[144:147], a[228:231], v[112:115], v[144:147]\n" \
"buffer_load_dwordx4 v246, s[12:15], s46 offen lds\n" \
"v_add_f32 v243, v243, v58\n" \
"v_cvt_pk_bf16_f32 v56, v56, v57\n" \
"v_exp_f32 v59, v59\n" \
"v_mfma_f32_16x16x32_bf16 v[176:179], a[224:227], v[88:91], v[176:179]\n" \
"s_add_u32 s46, s46, 0x180\n" \
"ds_read_b128 v[228:231], v247 offset:28672\n" \
"v_add_f32 v243, v243, v59\n" \
"v_exp_f32 v60, v60\n" \
"v_mfma_f32_16x16x32_bf16 v[176:179], a[228:231], v[120:123], v[176:179]\n" \
"ds_read_b128 a[224:227], v247 offset:4096\n" \
"v_add_f32 v243, v243, v60\n" \
"v_cvt_pk_bf16_f32 v57, v58, v59\n" \
"v_exp_f32 v61, v61\n" \
"v_mfma_f32_16x16x32_bf16 a[84:87], a[232:235], v[64:67], a[84:87]\n" \
"ds_write_b128 v124, a[112:115] offset:12288\n" \
"v_add_f32 v243, v243, v61\n" \
"v_exp_f32 v62, v62\n" \
"v_mfma_f32_16x16x32_bf16 a[84:87], a[236:239], v[96:99], a[84:87]\n" \
"v_add_f32 v243, v243, v62\n" \
"v_cvt_pk_bf16_f32 v58, v60, v61\n" \
"v_exp_f32 v63, v63\n" \
"v_mfma_f32_16x16x32_bf16 a[116:119], a[232:235], v[72:75], a[116:119]\n" \
"ds_read_b128 v[232:235], v247 offset:21504\n" \
"v_add_f32 v243, v243, v63\n" \
"v_cvt_pk_bf16_f32 v59, v62, v63\n" \
"v_exp_f32 v16, v16\n" \
"v_mfma_f32_16x16x32_bf16 a[116:119], a[236:239], v[104:107], a[116:119]\n" \
"ds_read_b128 a[228:231], v247 offset:12288\n" \
"v_add_f32 v242, v242, v16\n" \
"v_exp_f32 v17, v17\n" \
"v_mfma_f32_16x16x32_bf16 v[148:151], a[232:235], v[80:83], v[148:151]\n" \
"ds_write_b128 v124, a[84:87] offset:5120\n" \
"v_add_f32 v242, v242, v17\n" \
"v_exp_f32 v18, v18\n" \
"v_mfma_f32_16x16x32_bf16 v[148:151], a[236:239], v[112:115], v[148:151]\n" \
"v_add_f32 v242, v242, v18\n" \
"v_cvt_pk_bf16_f32 v16, v16, v17\n" \
"v_exp_f32 v19, v19\n" \
"v_mfma_f32_16x16x32_bf16 v[180:183], a[232:235], v[88:91], v[180:183]\n" \
"ds_read_b128 v[236:239], v247 offset:29696\n" \
"v_add_f32 v242, v242, v19\n" \
"v_exp_f32 v20, v20\n" \
"v_mfma_f32_16x16x32_bf16 v[180:183], a[236:239], v[120:123], v[180:183]\n" \
"ds_read_b128 a[232:235], v247 offset:5120\n" \
"v_add_f32 v242, v242, v20\n" \
"v_cvt_pk_bf16_f32 v17, v18, v19\n" \
"v_exp_f32 v21, v21\n" \
"v_mfma_f32_16x16x32_bf16 a[88:91], a[240:243], v[64:67], a[88:91]\n" \
"ds_write_b128 v124, a[116:119] offset:13312\n" \
"v_add_f32 v242, v242, v21\n" \
"v_mfma_f32_16x16x32_bf16 a[88:91], a[244:247], v[96:99], a[88:91]\n" \
"v_exp_f32 v22, v22\n" \
"v_mfma_f32_16x16x32_bf16 a[120:123], a[240:243], v[72:75], a[120:123]\n" \
"ds_read_b128 v[0:3], v247 offset:22528\n" \
"v_add_f32 v242, v242, v22\n" \
"v_mfma_f32_16x16x32_bf16 a[120:123], a[244:247], v[104:107], a[120:123]\n" \
"ds_read_b128 a[236:239], v247 offset:13312\n" \
"v_cvt_pk_bf16_f32 v18, v20, v21\n" \
"v_mfma_f32_16x16x32_bf16 v[152:155], a[240:243], v[80:83], v[152:155]\n" \
"ds_write_b128 v124, a[88:91] offset:6144\n" \
"v_mfma_f32_16x16x32_bf16 v[152:155], a[244:247], v[112:115], v[152:155]\n" \
"v_exp_f32 v23, v23\n" \
"v_mfma_f32_16x16x32_bf16 v[184:187], a[240:243], v[88:91], v[184:187]\n" \
"ds_read_b128 v[4:7], v247 offset:30720\n" \
"v_add_f32 v242, v242, v23\n" \
"v_mfma_f32_16x16x32_bf16 v[184:187], a[244:247], v[120:123], v[184:187]\n" \
"ds_read_b128 a[240:243], v247 offset:6144\n" \
"v_exp_f32 v48, v48\n" \
"v_mfma_f32_16x16x32_bf16 a[92:95], a[248:251], v[64:67], a[92:95]\n" \
"ds_write_b128 v124, a[120:123] offset:14336\n" \
"v_mfma_f32_16x16x32_bf16 a[92:95], a[252:255], v[96:99], a[92:95]\n" \
"v_add_f32 v242, v242, v48\n" \
"v_cvt_pk_bf16_f32 v19, v22, v23\n" \
"v_mfma_f32_16x16x32_bf16 a[124:127], a[248:251], v[72:75], a[124:127]\n" \
"ds_read_b128 v[32:35], v247 offset:23552\n" \
"v_exp_f32 v49, v49\n" \
"v_mfma_f32_16x16x32_bf16 a[124:127], a[252:255], v[104:107], a[124:127]\n" \
"ds_read_b128 a[244:247], v247 offset:14336\n" \
"v_add_f32 v242, v242, v49\n" \
"v_mfma_f32_16x16x32_bf16 v[156:159], a[248:251], v[80:83], v[156:159]\n" \
"ds_write_b128 v124, a[92:95] offset:7168\n" \
"v_mfma_f32_16x16x32_bf16 v[156:159], a[252:255], v[112:115], v[156:159]\n" \
"v_exp_f32 v50, v50\n" \
"v_mfma_f32_16x16x32_bf16 v[188:191], a[248:251], v[88:91], v[188:191]\n" \
"ds_read_b128 v[36:39], v247 offset:31744\n" \
"v_add_f32 v242, v242, v50\n" \
"v_mfma_f32_16x16x32_bf16 v[188:191], a[252:255], v[120:123], v[188:191]\n" \
"s_waitcnt lgkmcnt(3)\n" \
"ds_read_b128 a[248:251], v247 offset:7168\n" \
"v_cvt_pk_bf16_f32 v48, v48, v49\n" \
"v_mfma_f32_16x16x32_bf16 v[160:163], v[192:195], v[8:11], v[160:163]\n" \
"v_exp_f32 v51, v51\n" \
"v_mfma_f32_16x16x32_bf16 v[160:163], v[196:199], v[40:43], v[160:163]\n" \
"ds_read_b128 a[252:255], v247 offset:15360\n" \
"v_add_f32 v242, v242, v51\n" \
"v_exp_f32 v52, v52\n" \
"v_mfma_f32_16x16x32_bf16 v[164:167], v[200:203], v[8:11], v[164:167]\n" \
"ds_write_b128 v124, a[124:127] offset:15360\n" \
"v_mfma_f32_16x16x32_bf16 v[164:167], v[204:207], v[40:43], v[164:167]\n" \
"v_add_f32 v242, v242, v52\n" \
"v_cvt_pk_bf16_f32 v49, v50, v51\n" \
"v_mfma_f32_16x16x32_bf16 v[168:171], v[208:211], v[8:11], v[168:171]\n" \
"v_exp_f32 v53, v53\n" \
"v_mfma_f32_16x16x32_bf16 v[168:171], v[212:215], v[40:43], v[168:171]\n" \
"s_waitcnt vmcnt(0) lgkmcnt(0)\n" \
"v_add_f32 v242, v242, v53\n" \
"v_exp_f32 v54, v54\n" \
"v_mfma_f32_16x16x32_bf16 v[172:175], v[216:219], v[8:11], v[172:175]\n" \
"s_barrier\n" \
"v_add_f32 v242, v242, v54\n" \
"v_cvt_pk_bf16_f32 v50, v52, v53\n" \
"v_mfma_f32_16x16x32_bf16 v[172:175], v[220:223], v[40:43], v[172:175]\n" \
"s_add_u32 m0, s84, 90112\n" \
"ds_read_b64 v[244:245], v127\n" \
"v_exp_f32 v55, v55\n" \
"v_mfma_f32_16x16x32_bf16 v[176:179], v[224:227], v[8:11], v[176:179]\n" \
"ds_read_b128 v[64:67], v125 offset:0\n" \
"global_load_lds_dwordx4 v253, s[48:49] offset:-4096\n" \
"v_mfma_f32_16x16x32_bf16 v[176:179], v[228:231], v[40:43], v[176:179]\n" \
"ds_read_b128 v[68:71], v125 offset:1024\n" \
"global_load_lds_dwordx4 v253, s[48:49] offset:-2048\n" \
"v_mfma_f32_16x16x32_bf16 v[180:183], v[232:235], v[8:11], v[180:183]\n" \
"ds_read_b128 v[72:75], v125 offset:2048\n" \
"v_add_f32 v242, v242, v55\n" \
"v_mfma_f32_16x16x32_bf16 v[180:183], v[236:239], v[40:43], v[180:183]\n" \
"ds_read_b128 v[76:79], v125 offset:3072\n" \
"global_load_lds_dwordx4 v253, s[48:49] offset:0\n" \
"v_mfma_f32_16x16x32_bf16 v[184:187], v[0:3], v[8:11], v[184:187]\n" \
"s_add_u32 s48, s48, 0x300000\n" \
"s_addc_u32 s49, s49, 0\n" \
"ds_read_b128 v[80:83], v125 offset:4096\n" \
"v_mfma_f32_16x16x32_bf16 v[184:187], v[4:7], v[40:43], v[184:187]\n" \
"ds_read_b128 v[84:87], v125 offset:5120\n" \
"global_load_lds_dwordx4 v253, s[50:51] offset:-3072\n" \
"v_mfma_f32_16x16x32_bf16 v[188:191], v[32:35], v[8:11], v[188:191]\n" \
"s_waitcnt lgkmcnt(5)\n" \
"ds_read_b128 v[88:91], v125 offset:6144\n" \
"v_cvt_pk_bf16_f32 v51, v54, v55\n" \
"v_mfma_f32_16x16x32_bf16 v[188:191], v[36:39], v[40:43], v[188:191]\n" \
"ds_read_b128 v[92:95], v125 offset:7168\n" \
"global_load_lds_dwordx4 v253, s[50:51] offset:-1024\n" \
"v_mfma_f32_16x16x32_bf16 v[160:163], a[192:195], v[24:27], v[160:163]\n" \
"ds_read_b128 v[96:99], v125 offset:8192\n" \
"global_load_lds_dwordx4 v253, s[50:51] offset:1024\n" \
"v_mfma_f32_16x16x32_bf16 v[160:163], a[196:199], v[56:59], v[160:163]\n" \
"s_add_u32 s50, s50, 0x300000\n" \
"s_addc_u32 s51, s51, 0\n" \
"ds_read_b128 v[100:103], v125 offset:9216\n" \
"v_mfma_f32_16x16x32_bf16 v[164:167], a[200:203], v[24:27], v[164:167]\n" \
"s_add_u32 m0, s95, 65536\n" \
"ds_read_b128 v[104:107], v125 offset:10240\n" \
"s_movk_i32 s89, 0xc0\n" \
"v_mfma_f32_16x16x32_bf16 v[164:167], a[204:207], v[56:59], v[164:167]\n" \
"s_movk_i32 s90, 288\n" \
"ds_read_b128 v[108:111], v125 offset:11264\n" \
"global_load_lds_dwordx4 v252, s[42:43] offset:0\n" \
"v_lshrrev_b32 v211, 4, v248\n" \
"v_lshrrev_b32 v214, 8, v248\n" \
"v_and_b32 v215, 7, v211\n" \
"Lpg8744_%=:\n" \
"Lst1x2_%=:\n" \
"s_cbranch_execz Lpg11020_%=\n" \
"v_xor_b32 v214, v214, v215\n" \
"v_pk_add_f32 v[128:129], v[128:129], v[64:65]\n" \
"v_pk_add_f32 v[130:131], v[130:131], v[66:67]\n" \
"s_waitcnt lgkmcnt(10)\n" \
"v_pk_add_f32 v[132:133], v[132:133], v[68:69]\n" \
"v_pk_add_f32 v[134:135], v[134:135], v[70:71]\n" \
"s_waitcnt lgkmcnt(9)\n" \
"v_pk_add_f32 v[136:137], v[136:137], v[72:73]\n" \
"v_pk_add_f32 v[138:139], v[138:139], v[74:75]\n" \
"s_waitcnt lgkmcnt(8)\n" \
"v_pk_add_f32 v[140:141], v[140:141], v[76:77]\n" \
"v_pk_add_f32 v[142:143], v[142:143], v[78:79]\n" \
"s_waitcnt lgkmcnt(7)\n" \
"v_pk_add_f32 v[144:145], v[144:145], v[80:81]\n" \
"v_pk_add_f32 v[146:147], v[146:147], v[82:83]\n" \
"s_waitcnt lgkmcnt(6)\n" \
"v_pk_add_f32 v[148:149], v[148:149], v[84:85]\n" \
"v_pk_add_f32 v[150:151], v[150:151], v[86:87]\n" \
"s_waitcnt lgkmcnt(5)\n" \
"v_pk_add_f32 v[152:153], v[152:153], v[88:89]\n" \
"v_pk_add_f32 v[154:155], v[154:155], v[90:91]\n" \
"s_waitcnt lgkmcnt(4)\n" \
"v_pk_add_f32 v[156:157], v[156:157], v[92:93]\n" \
"v_pk_add_f32 v[158:159], v[158:159], v[94:95]\n" \
"v_mfma_f32_16x16x32_bf16 v[168:171], a[208:211], v[24:27], v[168:171]\n" \
"s_lshr_b32 s71, s61, 1\n" \
"ds_read_b128 v[112:115], v125 offset:12288\n" \
"s_lshl_b32 s70, s71, 11\n" \
"s_waitcnt lgkmcnt(4)\n" \
"v_mfma_f32_16x16x32_bf16 v[168:171], a[212:215], v[56:59], v[168:171]\n" \
"s_add_u32 s70, s70, 118784\n" \
"ds_read_b128 v[116:119], v125 offset:13312\n" \
"global_load_lds_dwordx4 v252, s[42:43] offset:2048\n" \
"v_and_b32 v216, 15, v211\n" \
"v_lshlrev_b32 v213, 7, v216\n" \
"v_lshl_add_u32 v213, v214, 4, v213\n" \
"v_pk_add_f32 v[160:161], v[160:161], v[96:97]\n" \
"v_pk_add_f32 v[162:163], v[162:163], v[98:99]\n" \
"v_mfma_f32_16x16x32_bf16 v[172:175], a[216:219], v[24:27], v[172:175]\n" \
"s_waitcnt lgkmcnt(4)\n" \
"s_add_u32 s42, s42, 0x400\n" \
"s_addc_u32 s43, s43, 0\n" \
"ds_read_b128 v[120:123], v125 offset:14336\n" \
"v_add_u32 v205, s70, v213\n" \
"v_xor_b32 v206, 64, v205\n" \
"v_lshrrev_b32 v216, 2, v216\n" \
"v_lshl_add_u32 v216, v216, 9, v213\n" \
"v_add_u32 v207, 135168, v216\n" \
"v_xor_b32 v208, 64, v207\n" \
"v_pk_add_f32 v[164:165], v[164:165], v[100:101]\n" \
"v_pk_add_f32 v[166:167], v[166:167], v[102:103]\n" \
"v_mfma_f32_16x16x32_bf16 v[172:175], a[220:223], v[56:59], v[172:175]\n" \
"s_and_b32 s70, s61, 1\n" \
"ds_read_b128 v[124:127], v125 offset:15360\n" \
"global_load_lds_dwordx4 v252, s[44:45] offset:1024\n" \
"v_mfma_f32_16x16x32_bf16 v[176:179], a[224:227], v[24:27], v[176:179]\n" \
"s_lshl_b32 s70, s70, 9\n" \
"s_add_u32 s70, s70, 139264\n" \
"s_lshl_b32 s71, s71, 5\n" \
"v_mfma_f32_16x16x32_bf16 v[176:179], a[228:231], v[56:59], v[176:179]\n" \
"s_waitcnt lgkmcnt(5)\n" \
"global_load_lds_dwordx4 v252, s[44:45] offset:3072\n" \
"v_and_b32 v213, 3, v211\n" \
"v_mfma_f32_16x16x32_bf16 v[180:183], a[232:235], v[24:27], v[180:183]\n" \
"s_add_u32 s44, s44, 0x400\n" \
"s_addc_u32 s45, s45, 0\n" \
"s_add_u32 m0, s85, 81920\n" \
"v_mfma_f32_16x16x32_bf16 v[180:183], a[236:239], v[56:59], v[180:183]\n" \
"s_waitcnt lgkmcnt(0)\n" \
"buffer_load_dwordx4 v246, s[12:15], s46 offen lds\n" \
"v_mfma_f32_16x16x32_bf16 v[184:187], a[240:243], v[24:27], v[184:187]\n" \
"s_barrier\n" \
"v_lshlrev_b32 v213, 3, v213\n" \
"v_mfma_f32_16x16x32_bf16 v[184:187], a[244:247], v[56:59], v[184:187]\n" \
"s_add_u32 s46, s46, 0x400\n" \
"v_and_or_b32 v213, v248, s89, v213\n" \
"v_bfe_u32 v214, v211, 4, 1\n" \
"v_mfma_f32_16x16x32_bf16 v[188:191], a[248:251], v[24:27], v[188:191]\n" \
"s_add_u32 m0, s95, 0\n" \
"v_mad_u32_u24 v213, v214, s90, v213\n" \
"v_lshrrev_b32 v214, 5, v211\n" \
"v_mul_u32_u24 v214, 6144, v214\n" \
"v_add3_u32 v213, v213, v214, s70\n" \
"v_xor_b32 v209, s71, v213\n" \
"v_xor_b32 v210, 32, v209\n" \
"v_pk_add_f32 v[168:169], v[168:169], v[104:105]\n" \
"v_pk_add_f32 v[170:171], v[170:171], v[106:107]\n" \
"v_pk_add_f32 v[172:173], v[172:173], v[108:109]\n" \
"v_pk_add_f32 v[174:175], v[174:175], v[110:111]\n" \
"v_pk_add_f32 v[176:177], v[176:177], v[112:113]\n" \
"v_pk_add_f32 v[178:179], v[178:179], v[114:115]\n" \
"v_pk_add_f32 v[180:181], v[180:181], v[116:117]\n" \
"v_pk_add_f32 v[182:183], v[182:183], v[118:119]\n" \
"v_mfma_f32_16x16x32_bf16 v[188:191], a[252:255], v[56:59], v[188:191]\n" \
"ds_read_b128 a[64:67], v207 offset:0\n" \
"ds_read_b64_tr_b16 a[96:97], v209 offset:2048\n" \
"v_mfma_f32_16x16x32_bf16 v[128:131], a[192:195], v[16:19], v[128:131]\n" \
"ds_read_b64_tr_b16 a[98:99], v209 offset:3072\n" \
"global_load_lds_dwordx4 v252, s[42:43] offset:0\n" \
"v_mfma_f32_16x16x32_bf16 v[128:131], a[196:199], v[48:51], v[128:131]\n" \
"ds_read_b128 a[68:71], v208 offset:0\n" \
"v_mfma_f32_16x16x32_bf16 v[132:135], a[200:203], v[16:19], v[132:135]\n" \
"ds_read_b64_tr_b16 a[100:101], v209 offset:14336\n" \
"ds_read_b64_tr_b16 a[102:103], v209 offset:15360\n" \
"global_load_lds_dwordx4 v252, s[42:43] offset:2048\n" \
"v_pk_add_f32 v[184:185], v[184:185], v[120:121]\n" \
"v_pk_add_f32 v[186:187], v[186:187], v[122:123]\n" \
"v_pk_add_f32 v[188:189], v[188:189], v[124:125]\n" \
"v_pk_add_f32 v[190:191], v[190:191], v[126:127]\n" \
"v_mfma_f32_16x16x32_bf16 v[132:135], a[204:207], v[48:51], v[132:135]\n" \
"s_add_u32 s42, s42, 0xfffffb00\n" \
"s_addc_u32 s43, s43, -1\n" \
"ds_read_b128 a[72:75], v205 offset:0\n" \
"v_mfma_f32_16x16x32_bf16 v[136:139], a[208:211], v[16:19], v[136:139]\n" \
"ds_read_b64_tr_b16 a[104:105], v209 offset:0\n" \
"global_load_lds_dwordx4 v252, s[44:45] offset:1024\n" \
"v_mfma_f32_16x16x32_bf16 v[136:139], a[212:215], v[48:51], v[136:139]\n" \
"ds_read_b128 a[76:79], v206 offset:0\n" \
"ds_read_b64_tr_b16 a[106:107], v209 offset:1024\n" \
"v_mfma_f32_16x16x32_bf16 v[140:143], a[216:219], v[16:19], v[140:143]\n" \
"ds_read_b64_tr_b16 a[108:109], v209 offset:12288\n" \
"v_add_f32 v242, v242, v244\n" \
"v_mfma_f32_16x16x32_bf16 v[140:143], a[220:223], v[48:51], v[140:143]\n" \
"ds_read_b64_tr_b16 a[110:111], v209 offset:13312\n" \
"global_load_lds_dwordx4 v252, s[44:45] offset:3072\n" \
"v_mfma_f32_16x16x32_bf16 v[144:147], a[224:227], v[16:19], v[144:147]\n" \
"ds_read_b128 a[80:83], v205 offset:4096\n" \
"ds_read_b64_tr_b16 a[112:113], v210 offset:0\n" \
"v_mfma_f32_16x16x32_bf16 v[144:147], a[228:231], v[48:51], v[144:147]\n" \
"s_add_u32 s44, s44, 0xfffffb00\n" \
"s_addc_u32 s45, s45, -1\n" \
"ds_read_b64_tr_b16 a[114:115], v210 offset:1024\n" \
"v_mfma_f32_16x16x32_bf16 v[148:151], a[232:235], v[16:19], v[148:151]\n" \
"s_add_u32 m0, s85, 16384\n" \
"ds_read_b64_tr_b16 a[116:117], v210 offset:12288\n" \
"v_add_f32 v243, v243, v245\n" \
"v_mfma_f32_16x16x32_bf16 v[148:151], a[236:239], v[48:51], v[148:151]\n" \
"ds_read_b128 a[84:87], v206 offset:4096\n" \
"ds_read_b64_tr_b16 a[118:119], v210 offset:13312\n" \
"v_mfma_f32_16x16x32_bf16 v[152:155], a[240:243], v[16:19], v[152:155]\n" \
"ds_read_b128 a[88:91], v205 offset:8192\n" \
"v_mfma_f32_16x16x32_bf16 v[152:155], a[244:247], v[48:51], v[152:155]\n" \
"ds_read_b128 a[92:95], v206 offset:8192\n" \
"v_mfma_f32_16x16x32_bf16 v[156:159], a[248:251], v[16:19], v[156:159]\n" \
"buffer_load_dwordx4 v246, s[12:15], s46 offen lds\n" \
"v_mfma_f32_16x16x32_bf16 v[156:159], a[252:255], v[48:51], v[156:159]\n" \
"s_waitcnt lgkmcnt(4)\n" \
"Ljoin_%=:\n" \
"v_mfma_f32_16x16x32_bf16 v[32:35], a[96:99], a[64:67], 0\n" \
"s_add_u32 s46, s46, 0xfffffb00\n" \
"ds_read_b128 a[192:195], v205 offset:12288\n" \
"s_add_u32 m0, s84, 24576\n" \
"v_mfma_f32_16x16x32_bf16 v[32:35], a[100:103], a[68:71], v[32:35]\n" \
"global_load_lds_dwordx4 v253, s[48:49] offset:-4096\n" \
"v_mfma_f32_16x16x32_bf16 v[40:43], a[96:99], a[72:75], 0\n" \
"ds_read_b128 a[196:199], v206 offset:12288\n" \
"v_mfma_f32_16x16x32_bf16 v[40:43], a[100:103], a[76:79], v[40:43]\n" \
"ds_read_b64_tr_b16 a[200:201], v210 offset:2048\n" \
"v_mov_b32 v232, v242\n" \
"v_mfma_f32_16x16x32_bf16 v[0:3], a[104:107], a[72:75], 0\n" \
"ds_read_b64_tr_b16 a[202:203], v210 offset:3072\n" \
"global_load_lds_dwordx4 v253, s[48:49] offset:-2048\n" \
"v_mfma_f32_16x16x32_bf16 v[0:3], a[108:111], a[76:79], v[0:3]\n" \
"s_waitcnt lgkmcnt(3)\n" \
"v_permlane16_swap_b32 v232, v242\n" \
"v_mfma_f32_16x16x32_bf16 v[4:7], a[112:115], a[72:75], 0\n" \
"ds_read_b64_tr_b16 a[204:205], v210 offset:14336\n" \
"v_add_f32 v242, v242, v232\n" \
"v_mfma_f32_16x16x32_bf16 v[4:7], a[116:119], a[76:79], v[4:7]\n" \
"ds_read_b64_tr_b16 a[206:207], v210 offset:15360\n" \
"global_load_lds_dwordx4 v253, s[48:49] offset:0\n" \
"v_mfma_f32_16x16x32_bf16 v[48:51], a[96:99], a[80:83], 0\n" \
"s_add_u32 s48, s48, 0xffc40000\n" \
"s_addc_u32 s49, s49, -1\n" \
"ds_read_b64_tr_b16 a[208:209], v209 offset:4096\n" \
"v_mfma_f32_16x16x32_bf16 v[48:51], a[100:103], a[84:87], v[48:51]\n" \
"v_mov_b32 v232, v242\n" \
"v_mfma_f32_16x16x32_bf16 v[8:11], a[104:107], a[80:83], 0\n" \
"ds_read_b64_tr_b16 a[210:211], v209 offset:5120\n" \
"global_load_lds_dwordx4 v253, s[50:51] offset:-3072\n" \
"v_mfma_f32_16x16x32_bf16 v[8:11], a[108:111], a[84:87], v[8:11]\n" \
"ds_read_b64_tr_b16 a[212:213], v209 offset:16384\n" \
"v_mfma_f32_16x16x32_bf16 v[12:15], a[112:115], a[80:83], 0\n" \
"ds_read_b64_tr_b16 a[214:215], v209 offset:17408\n" \
"v_mfma_f32_16x16x32_bf16 v[12:15], a[116:119], a[84:87], v[12:15]\n" \
"global_load_lds_dwordx4 v253, s[50:51] offset:-1024\n" \
"v_mfma_f32_16x16x32_bf16 v[56:59], a[96:99], a[88:91], 0\n" \
"ds_read_b64_tr_b16 a[216:217], v210 offset:4096\n" \
"v_mfma_f32_16x16x32_bf16 v[56:59], a[100:103], a[92:95], v[56:59]\n" \
"ds_read_b64_tr_b16 a[218:219], v210 offset:5120\n" \
"v_mfma_f32_16x16x32_bf16 v[16:19], a[104:107], a[88:91], 0\n" \
"ds_read_b64_tr_b16 a[220:221], v210 offset:16384\n" \
"global_load_lds_dwordx4 v253, s[50:51] offset:1024\n" \
"v_mfma_f32_16x16x32_bf16 v[16:19], a[108:111], a[92:95], v[16:19]\n" \
"s_add_u32 s50, s50, 0xffc40000\n" \
"s_addc_u32 s51, s51, -1\n" \
"v_mfma_f32_16x16x32_bf16 v[20:23], a[112:115], a[88:91], 0\n" \
"ds_read_b64_tr_b16 a[222:223], v210 offset:17408\n" \
"v_mfma_f32_16x16x32_bf16 v[20:23], a[116:119], a[92:95], v[20:23]\n" \
"s_waitcnt lgkmcnt(3)\n" \
"v_add_u32 v207, 0xffff3000, v207\n" \
"v_add_u32 v208, 0xffff3000, v208\n" \
"v_mfma_f32_16x16x32_bf16 v[64:67], a[96:99], a[192:195], 0\n" \
"v_add_u32 v205, 0xffff3000, v205\n" \
"v_add_u32 v206, 0xffff3000, v206\n" \
"v_mfma_f32_16x16x32_bf16 v[64:67], a[100:103], a[196:199], v[64:67]\n" \
"v_add_u32 v209, 0xffff3000, v209\n" \
"v_add_u32 v210, 0xffff3000, v210\n" \
"v_mfma_f32_16x16x32_bf16 v[24:27], a[104:107], a[192:195], 0\n" \
"v_permlane32_swap_b32 v232, v242\n" \
"v_mfma_f32_16x16x32_bf16 v[24:27], a[108:111], a[196:199], v[24:27]\n" \
"v_add_f32 v242, v242, v232\n" \
"v_mfma_f32_16x16x32_bf16 v[28:31], a[112:115], a[192:195], 0\n" \
"s_waitcnt vmcnt(11) lgkmcnt(0)\n" \
"v_rcp_f32 v232, v242\n" \
"v_mfma_f32_16x16x32_bf16 v[28:31], a[116:119], a[196:199], v[28:31]\n" \
"s_barrier\n" \
"v_mul_f32 v242, 0x4d800000, v242\n" \
"v_mfma_f32_16x16x32_bf16 v[72:75], a[64:67], a[208:211], 0\n" \
"ds_read_b64_tr_b16 a[96:97], v209 offset:2048\n" \
"v_add_u32 v233, 0x1c90080, v249\n" \
"v_mfma_f32_16x16x32_bf16 v[72:75], a[68:71], a[212:215], v[72:75]\n" \
"ds_read_b64_tr_b16 a[98:99], v209 offset:3072\n" \
"v_cndmask_b32 v233, v249, v233, s[16:17]\n" \
"v_mfma_f32_16x16x32_bf16 v[44:47], a[200:203], a[72:75], 0\n" \
"s_add_u32 m0, s95, 118784\n" \
"ds_read_b128 a[64:67], v207 offset:0\n" \
"ds_read_b64_tr_b16 a[100:101], v209 offset:14336\n" \
"v_mfma_f32_16x16x32_bf16 v[44:47], a[204:207], a[76:79], v[44:47]\n" \
"global_load_lds_dwordx4 v252, s[42:43] offset:0\n" \
"v_mul_f32 v128, v128, v232\n" \
"v_mfma_f32_16x16x32_bf16 v[80:83], a[72:75], a[208:211], 0\n" \
"ds_read_b128 a[68:71], v208 offset:0\n" \
"ds_read_b64_tr_b16 a[102:103], v209 offset:15360\n" \
"v_mfma_f32_16x16x32_bf16 v[80:83], a[76:79], a[212:215], v[80:83]\n" \
"ds_read_b64_tr_b16 a[104:105], v209 offset:0\n" \
"v_mul_f32 v129, v129, v232\n" \
"v_mfma_f32_16x16x32_bf16 v[84:87], a[72:75], a[216:219], 0\n" \
"ds_read_b64_tr_b16 a[106:107], v209 offset:1024\n" \
"v_mul_f32 v130, v130, v232\n" \
"v_mfma_f32_16x16x32_bf16 v[84:87], a[76:79], a[220:223], v[84:87]\n" \
"global_load_lds_dwordx4 v252, s[42:43] offset:2048\n" \
"v_mul_f32 v131, v131, v232\n" \
"v_mfma_f32_16x16x32_bf16 v[52:55], a[200:203], a[80:83], 0\n" \
"s_add_u32 s42, s42, 0x80\n" \
"s_addc_u32 s43, s43, 0\n" \
"ds_read_b128 a[72:75], v205 offset:0\n" \
"v_mfma_f32_16x16x32_bf16 v[52:55], a[204:207], a[84:87], v[52:55]\n" \
"ds_read_b128 a[76:79], v206 offset:0\n" \
"v_mul_f32 v132, v132, v232\n" \
"v_mfma_f32_16x16x32_bf16 v[88:91], a[80:83], a[208:211], 0\n" \
"ds_read_b64_tr_b16 a[108:109], v209 offset:12288\n" \
"v_mul_f32 v133, v133, v232\n" \
"v_mfma_f32_16x16x32_bf16 v[88:91], a[84:87], a[212:215], v[88:91]\n" \
"global_load_lds_dwordx4 v252, s[44:45] offset:1024\n" \
"v_mul_f32 v134, v134, v232\n" \
"v_mfma_f32_16x16x32_bf16 v[92:95], a[80:83], a[216:219], 0\n" \
"ds_read_b64_tr_b16 a[110:111], v209 offset:13312\n" \
"v_mul_f32 v135, v135, v232\n" \
"v_mfma_f32_16x16x32_bf16 v[92:95], a[84:87], a[220:223], v[92:95]\n" \
"ds_read_b64_tr_b16 a[112:113], v210 offset:0\n" \
"v_fma_f32 v242, v128, v129, v242\n" \
"v_mfma_f32_16x16x32_bf16 v[60:63], a[200:203], a[88:91], 0\n" \
"ds_read_b128 a[80:83], v205 offset:4096\n" \
"v_cvt_pk_bf16_f32 v128, v128, v129\n" \
"v_mfma_f32_16x16x32_bf16 v[60:63], a[204:207], a[92:95], v[60:63]\n" \
"global_load_lds_dwordx4 v252, s[44:45] offset:3072\n" \
"v_fma_f32 v242, v130, v131, v242\n" \
"v_mfma_f32_16x16x32_bf16 v[96:99], a[88:91], a[208:211], 0\n" \
"s_add_u32 s44, s44, 0x80\n" \
"s_addc_u32 s45, s45, 0\n" \
"ds_read_b128 a[84:87], v206 offset:4096\n" \
"v_mfma_f32_16x16x32_bf16 v[96:99], a[92:95], a[212:215], v[96:99]\n" \
"ds_read_b64_tr_b16 a[114:115], v210 offset:1024\n" \
"v_cvt_pk_bf16_f32 v129, v130, v131\n" \
"v_mfma_f32_16x16x32_bf16 v[100:103], a[88:91], a[216:219], 0\n" \
"s_add_u32 m0, s85, 135168\n" \
"ds_read_b64_tr_b16 a[116:117], v210 offset:12288\n" \
"v_fma_f32 v242, v132, v133, v242\n" \
"v_mfma_f32_16x16x32_bf16 v[100:103], a[92:95], a[220:223], v[100:103]\n" \
"buffer_load_dwordx4 v246, s[12:15], s46 offen lds\n" \
"v_mfma_f32_16x16x32_bf16 v[68:71], a[200:203], a[192:195], 0\n" \
"s_add_u32 s46, s46, 0x80\n" \
"ds_read_b128 a[88:91], v205 offset:8192\n" \
"v_cvt_pk_bf16_f32 v130, v132, v133\n" \
"v_mfma_f32_16x16x32_bf16 v[68:71], a[204:207], a[196:199], v[68:71]\n" \
"ds_read_b128 a[92:95], v206 offset:8192\n" \
"v_fma_f32 v242, v134, v135, v242\n" \
"v_mfma_f32_16x16x32_bf16 v[104:107], a[192:195], a[208:211], 0\n" \
"s_add_u32 m0, s84, 143360\n" \
"ds_read_b64_tr_b16 a[118:119], v210 offset:13312\n" \
"v_cvt_pk_bf16_f32 v131, v134, v135\n" \
"v_mfma_f32_16x16x32_bf16 v[104:107], a[196:199], a[212:215], v[104:107]\n" \
"buffer_store_dwordx4 v[128:131], v233, s[56:59], s78 offen offset:0 sc1\n" \
"global_load_lds_dwordx4 v253, s[48:49] offset:-4096\n" \
"v_mul_f32 v136, v136, v232\n" \
"v_mul_f32 v137, v137, v232\n" \
"v_mul_f32 v138, v138, v232\n" \
"v_mfma_f32_16x16x32_bf16 v[108:111], a[192:195], a[216:219], 0\n" \
"v_mul_f32 v139, v139, v232\n" \
"v_mul_f32 v140, v140, v232\n" \
"v_mfma_f32_16x16x32_bf16 v[108:111], a[196:199], a[220:223], v[108:111]\n" \
"s_waitcnt lgkmcnt(2)\n" \
"v_mul_f32 v141, v141, v232\n" \
"v_mul_f32 v142, v142, v232\n" \
"v_mfma_f32_16x16x32_bf16 v[32:35], a[96:99], a[64:67], v[32:35]\n" \
"ds_read_b128 a[192:195], v205 offset:12288\n" \
"v_mul_f32 v143, v143, v232\n" \
"v_mfma_f32_16x16x32_bf16 v[32:35], a[100:103], a[68:71], v[32:35]\n" \
"global_load_lds_dwordx4 v253, s[48:49] offset:-2048\n" \
"v_fma_f32 v242, v136, v137, v242\n" \
"v_mfma_f32_16x16x32_bf16 v[40:43], a[96:99], a[72:75], v[40:43]\n" \
"ds_read_b128 a[196:199], v206 offset:12288\n" \
"v_cvt_pk_bf16_f32 v136, v136, v137\n" \
"v_mfma_f32_16x16x32_bf16 v[40:43], a[100:103], a[76:79], v[40:43]\n" \
"ds_read_b64_tr_b16 a[200:201], v210 offset:2048\n" \
"v_fma_f32 v242, v138, v139, v242\n" \
"v_mfma_f32_16x16x32_bf16 v[0:3], a[104:107], a[72:75], v[0:3]\n" \
"ds_read_b64_tr_b16 a[202:203], v210 offset:3072\n" \
"v_cvt_pk_bf16_f32 v137, v138, v139\n" \
"v_mfma_f32_16x16x32_bf16 v[0:3], a[108:111], a[76:79], v[0:3]\n" \
"s_waitcnt lgkmcnt(3)\n" \
"global_load_lds_dwordx4 v253, s[48:49] offset:0\n" \
"v_fma_f32 v242, v140, v141, v242\n" \
"v_mfma_f32_16x16x32_bf16 v[4:7], a[112:115], a[72:75], v[4:7]\n" \
"s_add_u32 s48, s48, 0x60000\n" \
"s_addc_u32 s49, s49, 0\n" \
"ds_read_b64_tr_b16 a[204:205], v210 offset:14336\n" \
"v_mfma_f32_16x16x32_bf16 v[4:7], a[116:119], a[76:79], v[4:7]\n" \
"ds_read_b64_tr_b16 a[206:207], v210 offset:15360\n" \
"v_cvt_pk_bf16_f32 v138, v140, v141\n" \
"v_mfma_f32_16x16x32_bf16 v[48:51], a[96:99], a[80:83], v[48:51]\n" \
"ds_read_b64_tr_b16 a[208:209], v209 offset:4096\n" \
"v_fma_f32 v242, v142, v143, v242\n" \
"v_mfma_f32_16x16x32_bf16 v[48:51], a[100:103], a[84:87], v[48:51]\n" \
"global_load_lds_dwordx4 v253, s[50:51] offset:-3072\n" \
"v_cvt_pk_bf16_f32 v139, v142, v143\n" \
"v_mfma_f32_16x16x32_bf16 v[8:11], a[104:107], a[80:83], v[8:11]\n" \
"buffer_store_dwordx4 v[136:139], v233, s[56:59], s78 offen offset:2048 sc1\n" \
"ds_read_b64_tr_b16 a[210:211], v209 offset:5120\n" \
"s_add_u32 s78, s78, 0x1000\n" \
"v_mul_f32 v144, v144, v232\n" \
"v_mul_f32 v145, v145, v232\n" \
"v_mfma_f32_16x16x32_bf16 v[8:11], a[108:111], a[84:87], v[8:11]\n" \
"ds_read_b64_tr_b16 a[212:213], v209 offset:16384\n" \
"v_mul_f32 v146, v146, v232\n" \
"v_mfma_f32_16x16x32_bf16 v[12:15], a[112:115], a[80:83], v[12:15]\n" \
"ds_read_b64_tr_b16 a[214:215], v209 offset:17408\n" \
"v_mul_f32 v147, v147, v232\n" \
"v_mfma_f32_16x16x32_bf16 v[12:15], a[116:119], a[84:87], v[12:15]\n" \
"global_load_lds_dwordx4 v253, s[50:51] offset:-1024\n" \
"v_mul_f32 v148, v148, v232\n" \
"v_mfma_f32_16x16x32_bf16 v[56:59], a[96:99], a[88:91], v[56:59]\n" \
"ds_read_b64_tr_b16 a[216:217], v210 offset:4096\n" \
"v_mul_f32 v149, v149, v232\n" \
"v_mfma_f32_16x16x32_bf16 v[56:59], a[100:103], a[92:95], v[56:59]\n" \
"ds_read_b64_tr_b16 a[218:219], v210 offset:5120\n" \
"v_mul_f32 v150, v150, v232\n" \
"v_mfma_f32_16x16x32_bf16 v[16:19], a[104:107], a[88:91], v[16:19]\n" \
"ds_read_b64_tr_b16 a[220:221], v210 offset:16384\n" \
"v_mul_f32 v151, v151, v232\n" \
"v_mfma_f32_16x16x32_bf16 v[16:19], a[108:111], a[92:95], v[16:19]\n" \
"global_load_lds_dwordx4 v253, s[50:51] offset:1024\n" \
"v_fma_f32 v242, v144, v145, v242\n" \
"v_mfma_f32_16x16x32_bf16 v[20:23], a[112:115], a[88:91], v[20:23]\n" \
"s_add_u32 s50, s50, 0x60000\n" \
"s_addc_u32 s51, s51, 0\n" \
"ds_read_b64_tr_b16 a[222:223], v210 offset:17408\n" \
"v_mfma_f32_16x16x32_bf16 v[20:23], a[116:119], a[92:95], v[20:23]\n" \
"s_waitcnt lgkmcnt(3)\n" \
"v_add_u32 v207, 0xffff0000, v207\n" \
"v_add_u32 v208, 0xffff0000, v208\n" \
"v_mfma_f32_16x16x32_bf16 v[64:67], a[96:99], a[192:195], v[64:67]\n" \
"v_add_u32 v205, 0xffff0000, v205\n" \
"v_add_u32 v206, 0xffff0000, v206\n" \
"v_mfma_f32_16x16x32_bf16 v[64:67], a[100:103], a[196:199], v[64:67]\n" \
"v_add_u32 v209, 0xffff0000, v209\n" \
"v_add_u32 v210, 0xffff0000, v210\n" \
"v_mfma_f32_16x16x32_bf16 v[24:27], a[104:107], a[192:195], v[24:27]\n" \
"v_cvt_pk_bf16_f32 v144, v144, v145\n" \
"v_fma_f32 v242, v146, v147, v242\n" \
"v_mfma_f32_16x16x32_bf16 v[24:27], a[108:111], a[196:199], v[24:27]\n" \
"v_cvt_pk_bf16_f32 v145, v146, v147\n" \
"v_fma_f32 v242, v148, v149, v242\n" \
"v_mfma_f32_16x16x32_bf16 v[28:31], a[112:115], a[192:195], v[28:31]\n" \
"s_waitcnt vmcnt(13) lgkmcnt(0)\n" \
"v_cvt_pk_bf16_f32 v146, v148, v149\n" \
"v_fma_f32 v242, v150, v151, v242\n" \
"v_mfma_f32_16x16x32_bf16 v[28:31], a[116:119], a[196:199], v[28:31]\n" \
"s_barrier\n" \
"v_cvt_pk_bf16_f32 v147, v150, v151\n" \
"v_mfma_f32_16x16x32_bf16 v[72:75], a[64:67], a[208:211], v[72:75]\n" \
"buffer_store_dwordx4 v[144:147], v233, s[56:59], s78 offen offset:0 sc1\n" \
"ds_read_b64_tr_b16 a[96:97], v209 offset:2048\n" \
"v_mul_f32 v152, v152, v232\n" \
"v_mul_f32 v153, v153, v232\n" \
"v_mul_f32 v154, v154, v232\n" \
"v_mfma_f32_16x16x32_bf16 v[72:75], a[68:71], a[212:215], v[72:75]\n" \
"ds_read_b64_tr_b16 a[98:99], v209 offset:3072\n" \
"v_mul_f32 v155, v155, v232\n" \
"v_mfma_f32_16x16x32_bf16 v[44:47], a[200:203], a[72:75], v[44:47]\n" \
"s_add_u32 m0, s95, 65536\n" \
"ds_read_b128 a[64:67], v207 offset:0\n" \
"ds_read_b64_tr_b16 a[100:101], v209 offset:14336\n" \
"v_mfma_f32_16x16x32_bf16 v[44:47], a[204:207], a[76:79], v[44:47]\n" \
"global_load_lds_dwordx4 v252, s[42:43] offset:0\n" \
"v_mul_f32 v156, v156, v232\n" \
"v_mfma_f32_16x16x32_bf16 v[80:83], a[72:75], a[208:211], v[80:83]\n" \
"ds_read_b128 a[68:71], v208 offset:0\n" \
"ds_read_b64_tr_b16 a[102:103], v209 offset:15360\n" \
"v_mfma_f32_16x16x32_bf16 v[80:83], a[76:79], a[212:215], v[80:83]\n" \
"ds_read_b64_tr_b16 a[104:105], v209 offset:0\n" \
"v_mul_f32 v157, v157, v232\n" \
"v_mfma_f32_16x16x32_bf16 v[84:87], a[72:75], a[216:219], v[84:87]\n" \
"ds_read_b64_tr_b16 a[106:107], v209 offset:1024\n" \
"v_mul_f32 v158, v158, v232\n" \
"v_mfma_f32_16x16x32_bf16 v[84:87], a[76:79], a[220:223], v[84:87]\n" \
"global_load_lds_dwordx4 v252, s[42:43] offset:2048\n" \
"v_mul_f32 v159, v159, v232\n" \
"v_mfma_f32_16x16x32_bf16 v[52:55], a[200:203], a[80:83], v[52:55]\n" \
"s_add_u32 s42, s42, 0x100\n" \
"s_addc_u32 s43, s43, 0\n" \
"ds_read_b128 a[72:75], v205 offset:0\n" \
"v_mfma_f32_16x16x32_bf16 v[52:55], a[204:207], a[84:87], v[52:55]\n" \
"ds_read_b128 a[76:79], v206 offset:0\n" \
"v_fma_f32 v242, v152, v153, v242\n" \
"v_mfma_f32_16x16x32_bf16 v[88:91], a[80:83], a[208:211], v[88:91]\n" \
"ds_read_b64_tr_b16 a[108:109], v209 offset:12288\n" \
"v_cvt_pk_bf16_f32 v152, v152, v153\n" \
"v_mfma_f32_16x16x32_bf16 v[88:91], a[84:87], a[212:215], v[88:91]\n" \
"global_load_lds_dwordx4 v252, s[44:45] offset:1024\n" \
"v_fma_f32 v242, v154, v155, v242\n" \
"v_mfma_f32_16x16x32_bf16 v[92:95], a[80:83], a[216:219], v[92:95]\n" \
"ds_read_b64_tr_b16 a[110:111], v209 offset:13312\n" \
"v_cvt_pk_bf16_f32 v153, v154, v155\n" \
"v_mfma_f32_16x16x32_bf16 v[92:95], a[84:87], a[220:223], v[92:95]\n" \
"ds_read_b64_tr_b16 a[112:113], v210 offset:0\n" \
"v_fma_f32 v242, v156, v157, v242\n" \
"v_mfma_f32_16x16x32_bf16 v[60:63], a[200:203], a[88:91], v[60:63]\n" \
"ds_read_b128 a[80:83], v205 offset:4096\n" \
"v_cvt_pk_bf16_f32 v154, v156, v157\n" \
"v_mfma_f32_16x16x32_bf16 v[60:63], a[204:207], a[92:95], v[60:63]\n" \
"global_load_lds_dwordx4 v252, s[44:45] offset:3072\n" \
"v_fma_f32 v242, v158, v159, v242\n" \
"v_mfma_f32_16x16x32_bf16 v[96:99], a[88:91], a[208:211], v[96:99]\n" \
"s_add_u32 s44, s44, 0x100\n" \
"s_addc_u32 s45, s45, 0\n" \
"ds_read_b128 a[84:87], v206 offset:4096\n" \
"v_mfma_f32_16x16x32_bf16 v[96:99], a[92:95], a[212:215], v[96:99]\n" \
"ds_read_b64_tr_b16 a[114:115], v210 offset:1024\n" \
"v_cvt_pk_bf16_f32 v155, v158, v159\n" \
"v_mfma_f32_16x16x32_bf16 v[100:103], a[88:91], a[216:219], v[100:103]\n" \
"buffer_store_dwordx4 v[152:155], v233, s[56:59], s78 offen offset:2048 sc1\n" \
"s_add_u32 m0, s85, 81920\n" \
"ds_read_b64_tr_b16 a[116:117], v210 offset:12288\n" \
"s_add_u32 s78, s78, 0x81000\n" \
"v_mov_b32 v234, v243\n" \
"v_mfma_f32_16x16x32_bf16 v[100:103], a[92:95], a[220:223], v[100:103]\n" \
"buffer_load_dwordx4 v246, s[12:15], s46 offen lds\n" \
"v_mfma_f32_16x16x32_bf16 v[68:71], a[200:203], a[192:195], v[68:71]\n" \
"s_add_u32 s46, s46, 0x100\n" \
"ds_read_b128 a[88:91], v205 offset:8192\n" \
"v_permlane16_swap_b32 v234, v243\n" \
"v_mfma_f32_16x16x32_bf16 v[68:71], a[204:207], a[196:199], v[68:71]\n" \
"ds_read_b128 a[92:95], v206 offset:8192\n" \
"v_add_f32 v243, v243, v234\n" \
"v_mfma_f32_16x16x32_bf16 v[104:107], a[192:195], a[208:211], v[104:107]\n" \
"s_add_u32 m0, s84, 90112\n" \
"ds_read_b64_tr_b16 a[118:119], v210 offset:13312\n" \
"v_mov_b32 v234, v243\n" \
"v_mfma_f32_16x16x32_bf16 v[104:107], a[196:199], a[212:215], v[104:107]\n" \
"global_load_lds_dwordx4 v253, s[48:49] offset:-4096\n" \
"v_mfma_f32_16x16x32_bf16 v[108:111], a[192:195], a[216:219], v[108:111]\n" \
"v_permlane32_swap_b32 v234, v243\n" \
"v_add_f32 v243, v243, v234\n" \
"v_mfma_f32_16x16x32_bf16 v[108:111], a[196:199], a[220:223], v[108:111]\n" \
"s_waitcnt lgkmcnt(2)\n" \
"v_rcp_f32 v234, v243\n" \
"v_mfma_f32_16x16x32_bf16 v[32:35], a[96:99], a[64:67], v[32:35]\n" \
"ds_read_b128 a[192:195], v205 offset:12288\n" \
"v_mul_f32 v243, 0x4d800000, v243\n" \
"v_mfma_f32_16x16x32_bf16 v[32:35], a[100:103], a[68:71], v[32:35]\n" \
"global_load_lds_dwordx4 v253, s[48:49] offset:-2048\n" \
"v_add_u32 v235, 0x1c90080, v249\n" \
"v_mfma_f32_16x16x32_bf16 v[40:43], a[96:99], a[72:75], v[40:43]\n" \
"ds_read_b128 a[196:199], v206 offset:12288\n" \
"v_cndmask_b32 v235, v249, v235, s[16:17]\n" \
"v_mfma_f32_16x16x32_bf16 v[40:43], a[100:103], a[76:79], v[40:43]\n" \
"ds_read_b64_tr_b16 a[200:201], v210 offset:2048\n" \
"v_mul_f32 v160, v160, v234\n" \
"v_mfma_f32_16x16x32_bf16 v[0:3], a[104:107], a[72:75], v[0:3]\n" \
"ds_read_b64_tr_b16 a[202:203], v210 offset:3072\n" \
"v_mul_f32 v161, v161, v234\n" \
"v_mfma_f32_16x16x32_bf16 v[0:3], a[108:111], a[76:79], v[0:3]\n" \
"s_waitcnt lgkmcnt(3)\n" \
"global_load_lds_dwordx4 v253, s[48:49] offset:0\n" \
"v_mul_f32 v162, v162, v234\n" \
"v_mfma_f32_16x16x32_bf16 v[4:7], a[112:115], a[72:75], v[4:7]\n" \
"s_add_u32 s48, s48, 0xc0000\n" \
"s_addc_u32 s49, s49, 0\n" \
"ds_read_b64_tr_b16 a[204:205], v210 offset:14336\n" \
"v_mfma_f32_16x16x32_bf16 v[4:7], a[116:119], a[76:79], v[4:7]\n" \
"ds_read_b64_tr_b16 a[206:207], v210 offset:15360\n" \
"v_mul_f32 v163, v163, v234\n" \
"v_mfma_f32_16x16x32_bf16 v[48:51], a[96:99], a[80:83], v[48:51]\n" \
"ds_read_b64_tr_b16 a[208:209], v209 offset:4096\n" \
"v_mul_f32 v164, v164, v234\n" \
"v_mfma_f32_16x16x32_bf16 v[48:51], a[100:103], a[84:87], v[48:51]\n" \
"global_load_lds_dwordx4 v253, s[50:51] offset:-3072\n" \
"v_mul_f32 v165, v165, v234\n" \
"v_mfma_f32_16x16x32_bf16 v[8:11], a[104:107], a[80:83], v[8:11]\n" \
"ds_read_b64_tr_b16 a[210:211], v209 offset:5120\n" \
"v_mul_f32 v166, v166, v234\n" \
"v_mfma_f32_16x16x32_bf16 v[8:11], a[108:111], a[84:87], v[8:11]\n" \
"ds_read_b64_tr_b16 a[212:213], v209 offset:16384\n" \
"v_mul_f32 v167, v167, v234\n" \
"v_mfma_f32_16x16x32_bf16 v[12:15], a[112:115], a[80:83], v[12:15]\n" \
"ds_read_b64_tr_b16 a[214:215], v209 offset:17408\n" \
"v_fma_f32 v243, v160, v161, v243\n" \
"v_mfma_f32_16x16x32_bf16 v[12:15], a[116:119], a[84:87], v[12:15]\n" \
"global_load_lds_dwordx4 v253, s[50:51] offset:-1024\n" \
"v_cvt_pk_bf16_f32 v160, v160, v161\n" \
"v_mfma_f32_16x16x32_bf16 v[56:59], a[96:99], a[88:91], v[56:59]\n" \
"ds_read_b64_tr_b16 a[216:217], v210 offset:4096\n" \
"v_fma_f32 v243, v162, v163, v243\n" \
"v_mfma_f32_16x16x32_bf16 v[56:59], a[100:103], a[92:95], v[56:59]\n" \
"ds_read_b64_tr_b16 a[218:219], v210 offset:5120\n" \
"v_cvt_pk_bf16_f32 v161, v162, v163\n" \
"v_mfma_f32_16x16x32_bf16 v[16:19], a[104:107], a[88:91], v[16:19]\n" \
"ds_read_b64_tr_b16 a[220:221], v210 offset:16384\n" \
"v_fma_f32 v243, v164, v165, v243\n" \
"v_mfma_f32_16x16x32_bf16 v[16:19], a[108:111], a[92:95], v[16:19]\n" \
"global_load_lds_dwordx4 v253, s[50:51] offset:1024\n" \
"v_cvt_pk_bf16_f32 v162, v164, v165\n" \
"v_mfma_f32_16x16x32_bf16 v[20:23], a[112:115], a[88:91], v[20:23]\n" \
"s_add_u32 s50, s50, 0xc0000\n" \
"s_addc_u32 s51, s51, 0\n" \
"ds_read_b64_tr_b16 a[222:223], v210 offset:17408\n" \
"v_mfma_f32_16x16x32_bf16 v[20:23], a[116:119], a[92:95], v[20:23]\n" \
"s_waitcnt lgkmcnt(3)\n" \
"v_add_u32 v207, 0x1d000, v207\n" \
"v_add_u32 v208, 0x1d000, v208\n" \
"v_mfma_f32_16x16x32_bf16 v[64:67], a[96:99], a[192:195], v[64:67]\n" \
"v_add_u32 v205, 0x1d000, v205\n" \
"v_add_u32 v206, 0x1d000, v206\n" \
"v_mfma_f32_16x16x32_bf16 v[64:67], a[100:103], a[196:199], v[64:67]\n" \
"v_add_u32 v209, 0x1d000, v209\n" \
"v_add_u32 v210, 0x1d000, v210\n" \
"v_mfma_f32_16x16x32_bf16 v[24:27], a[104:107], a[192:195], v[24:27]\n" \
"v_fma_f32 v243, v166, v167, v243\n" \
"v_cvt_pk_bf16_f32 v163, v166, v167\n" \
"v_mfma_f32_16x16x32_bf16 v[24:27], a[108:111], a[196:199], v[24:27]\n" \
"buffer_store_dwordx4 v[160:163], v235, s[56:59], s78 offen offset:0 sc1\n" \
"v_mul_f32 v168, v168, v234\n" \
"v_mul_f32 v169, v169, v234\n" \
"v_mul_f32 v170, v170, v234\n" \
"v_mul_f32 v171, v171, v234\n" \
"v_mfma_f32_16x16x32_bf16 v[28:31], a[112:115], a[192:195], v[28:31]\n" \
"s_waitcnt vmcnt(14) lgkmcnt(0)\n" \
"v_mul_f32 v172, v172, v234\n" \
"v_mul_f32 v173, v173, v234\n" \
"v_mfma_f32_16x16x32_bf16 v[28:31], a[116:119], a[196:199], v[28:31]\n" \
"s_barrier\n" \
"v_mul_f32 v174, v174, v234\n" \
"v_mfma_f32_16x16x32_bf16 v[72:75], a[64:67], a[208:211], v[72:75]\n" \
"ds_read_b64_tr_b16 a[96:97], v209 offset:2048\n" \
"v_mul_f32 v175, v175, v234\n" \
"v_mfma_f32_16x16x32_bf16 v[72:75], a[68:71], a[212:215], v[72:75]\n" \
"ds_read_b64_tr_b16 a[98:99], v209 offset:3072\n" \
"v_fma_f32 v243, v168, v169, v243\n" \
"v_mfma_f32_16x16x32_bf16 v[44:47], a[200:203], a[72:75], v[44:47]\n" \
"s_add_u32 m0, s95, 0\n" \
"ds_read_b128 a[64:67], v207 offset:0\n" \
"ds_read_b64_tr_b16 a[100:101], v209 offset:14336\n" \
"v_mfma_f32_16x16x32_bf16 v[44:47], a[204:207], a[76:79], v[44:47]\n" \
"Lpg9320_%=:\n" \
"Lst1x3_%=:\n" \
"s_cbranch_execz Lpg11579_%=\n" \
"global_load_lds_dwordx4 v252, s[42:43] offset:0\n" \
"v_cvt_pk_bf16_f32 v168, v168, v169\n" \
"v_mfma_f32_16x16x32_bf16 v[80:83], a[72:75], a[208:211], v[80:83]\n" \
"ds_read_b128 a[68:71], v208 offset:0\n" \
"ds_read_b64_tr_b16 a[102:103], v209 offset:15360\n" \
"v_mfma_f32_16x16x32_bf16 v[80:83], a[76:79], a[212:215], v[80:83]\n" \
"ds_read_b64_tr_b16 a[104:105], v209 offset:0\n" \
"v_fma_f32 v243, v170, v171, v243\n" \
"v_mfma_f32_16x16x32_bf16 v[84:87], a[72:75], a[216:219], v[84:87]\n" \
"ds_read_b64_tr_b16 a[106:107], v209 offset:1024\n" \
"v_cvt_pk_bf16_f32 v169, v170, v171\n" \
"v_mfma_f32_16x16x32_bf16 v[84:87], a[76:79], a[220:223], v[84:87]\n" \
"global_load_lds_dwordx4 v252, s[42:43] offset:2048\n" \
"v_fma_f32 v243, v172, v173, v243\n" \
"v_mfma_f32_16x16x32_bf16 v[52:55], a[200:203], a[80:83], v[52:55]\n" \
"s_add_u32 s42, s42, 0x80\n" \
"s_addc_u32 s43, s43, 0\n" \
"ds_read_b128 a[72:75], v205 offset:0\n" \
"v_mfma_f32_16x16x32_bf16 v[52:55], a[204:207], a[84:87], v[52:55]\n" \
"ds_read_b128 a[76:79], v206 offset:0\n" \
"v_cvt_pk_bf16_f32 v170, v172, v173\n" \
"v_mfma_f32_16x16x32_bf16 v[88:91], a[80:83], a[208:211], v[88:91]\n" \
"ds_read_b64_tr_b16 a[108:109], v209 offset:12288\n" \
"v_fma_f32 v243, v174, v175, v243\n" \
"v_mfma_f32_16x16x32_bf16 v[88:91], a[84:87], a[212:215], v[88:91]\n" \
"global_load_lds_dwordx4 v252, s[44:45] offset:1024\n" \
"v_cvt_pk_bf16_f32 v171, v174, v175\n" \
"v_mfma_f32_16x16x32_bf16 v[92:95], a[80:83], a[216:219], v[92:95]\n" \
"buffer_store_dwordx4 v[168:171], v235, s[56:59], s78 offen offset:2048 sc1\n" \
"ds_read_b64_tr_b16 a[110:111], v209 offset:13312\n" \
"s_add_u32 s78, s78, 0x1000\n" \
"v_mul_f32 v176, v176, v234\n" \
"v_mul_f32 v177, v177, v234\n" \
"v_mfma_f32_16x16x32_bf16 v[92:95], a[84:87], a[220:223], v[92:95]\n" \
"ds_read_b64_tr_b16 a[112:113], v210 offset:0\n" \
"v_mul_f32 v178, v178, v234\n" \
"v_mfma_f32_16x16x32_bf16 v[60:63], a[200:203], a[88:91], v[60:63]\n" \
"ds_read_b128 a[80:83], v205 offset:4096\n" \
"v_mul_f32 v179, v179, v234\n" \
"v_mfma_f32_16x16x32_bf16 v[60:63], a[204:207], a[92:95], v[60:63]\n" \
"global_load_lds_dwordx4 v252, s[44:45] offset:3072\n" \
"v_mul_f32 v180, v180, v234\n" \
"v_mfma_f32_16x16x32_bf16 v[96:99], a[88:91], a[208:211], v[96:99]\n" \
"s_add_u32 s44, s44, 0x80\n" \
"s_addc_u32 s45, s45, 0\n" \
"ds_read_b128 a[84:87], v206 offset:4096\n" \
"v_mfma_f32_16x16x32_bf16 v[96:99], a[92:95], a[212:215], v[96:99]\n" \
"ds_read_b64_tr_b16 a[114:115], v210 offset:1024\n" \
"v_mul_f32 v181, v181, v234\n" \
"v_mfma_f32_16x16x32_bf16 v[100:103], a[88:91], a[216:219], v[100:103]\n" \
"s_add_u32 m0, s85, 16384\n" \
"ds_read_b64_tr_b16 a[116:117], v210 offset:12288\n" \
"v_mul_f32 v182, v182, v234\n" \
"v_mfma_f32_16x16x32_bf16 v[100:103], a[92:95], a[220:223], v[100:103]\n" \
"buffer_load_dwordx4 v246, s[12:15], s46 offen lds\n" \
"v_mfma_f32_16x16x32_bf16 v[68:71], a[200:203], a[192:195], v[68:71]\n" \
"s_add_u32 s46, s46, 0x80\n" \
"ds_read_b128 a[88:91], v205 offset:8192\n" \
"v_mul_f32 v183, v183, v234\n" \
"v_mfma_f32_16x16x32_bf16 v[68:71], a[204:207], a[196:199], v[68:71]\n" \
"ds_read_b128 a[92:95], v206 offset:8192\n" \
"v_fma_f32 v243, v176, v177, v243\n" \
"v_mfma_f32_16x16x32_bf16 v[104:107], a[192:195], a[208:211], v[104:107]\n" \
"s_add_u32 m0, s84, 24576\n" \
"ds_read_b64_tr_b16 a[118:119], v210 offset:13312\n" \
"v_cvt_pk_bf16_f32 v176, v176, v177\n" \
"v_mfma_f32_16x16x32_bf16 v[104:107], a[196:199], a[212:215], v[104:107]\n" \
"global_load_lds_dwordx4 v253, s[48:49] offset:-4096\n" \
"v_fma_f32 v243, v178, v179, v243\n" \
"v_mfma_f32_16x16x32_bf16 v[108:111], a[192:195], a[216:219], v[108:111]\n" \
"v_cvt_pk_bf16_f32 v177, v178, v179\n" \
"v_fma_f32 v243, v180, v181, v243\n" \
"v_mfma_f32_16x16x32_bf16 v[108:111], a[196:199], a[220:223], v[108:111]\n" \
"s_waitcnt lgkmcnt(2)\n" \
"v_cvt_pk_bf16_f32 v178, v180, v181\n" \
"v_fma_f32 v243, v182, v183, v243\n" \
"v_mfma_f32_16x16x32_bf16 v[32:35], a[96:99], a[64:67], v[32:35]\n" \
"ds_read_b128 a[192:195], v205 offset:12288\n" \
"v_cvt_pk_bf16_f32 v179, v182, v183\n" \
"v_mfma_f32_16x16x32_bf16 v[32:35], a[100:103], a[68:71], v[32:35]\n" \
"buffer_store_dwordx4 v[176:179], v235, s[56:59], s78 offen offset:0 sc1\n" \
"global_load_lds_dwordx4 v253, s[48:49] offset:-2048\n" \
"v_mul_f32 v184, v184, v234\n" \
"v_mul_f32 v185, v185, v234\n" \
"v_mul_f32 v186, v186, v234\n" \
"v_mfma_f32_16x16x32_bf16 v[40:43], a[96:99], a[72:75], v[40:43]\n" \
"ds_read_b128 a[196:199], v206 offset:12288\n" \
"v_mul_f32 v187, v187, v234\n" \
"v_mfma_f32_16x16x32_bf16 v[40:43], a[100:103], a[76:79], v[40:43]\n" \
"ds_read_b64_tr_b16 a[200:201], v210 offset:2048\n" \
"v_mul_f32 v188, v188, v234\n" \
"v_mfma_f32_16x16x32_bf16 v[0:3], a[104:107], a[72:75], v[0:3]\n" \
"ds_read_b64_tr_b16 a[202:203], v210 offset:3072\n" \
"v_mul_f32 v189, v189, v234\n" \
"v_mfma_f32_16x16x32_bf16 v[0:3], a[108:111], a[76:79], v[0:3]\n" \
"s_waitcnt lgkmcnt(3)\n" \
"global_load_lds_dwordx4 v253, s[48:49] offset:0\n" \
"v_mul_f32 v190, v190, v234\n" \
"v_mfma_f32_16x16x32_bf16 v[4:7], a[112:115], a[72:75], v[4:7]\n" \
"s_add_u32 s48, s48, 0x60000\n" \
"s_addc_u32 s49, s49, 0\n" \
"ds_read_b64_tr_b16 a[204:205], v210 offset:14336\n" \
"v_mfma_f32_16x16x32_bf16 v[4:7], a[116:119], a[76:79], v[4:7]\n" \
"ds_read_b64_tr_b16 a[206:207], v210 offset:15360\n" \
"v_mul_f32 v191, v191, v234\n" \
"v_mfma_f32_16x16x32_bf16 v[48:51], a[96:99], a[80:83], v[48:51]\n" \
"ds_read_b64_tr_b16 a[208:209], v209 offset:4096\n" \
"v_fma_f32 v243, v184, v185, v243\n" \
"v_mfma_f32_16x16x32_bf16 v[48:51], a[100:103], a[84:87], v[48:51]\n" \
"global_load_lds_dwordx4 v253, s[50:51] offset:-3072\n" \
"v_cvt_pk_bf16_f32 v184, v184, v185\n" \
"v_mfma_f32_16x16x32_bf16 v[8:11], a[104:107], a[80:83], v[8:11]\n" \
"ds_read_b64_tr_b16 a[210:211], v209 offset:5120\n" \
"v_fma_f32 v243, v186, v187, v243\n" \
"v_mfma_f32_16x16x32_bf16 v[8:11], a[108:111], a[84:87], v[8:11]\n" \
"ds_read_b64_tr_b16 a[212:213], v209 offset:16384\n" \
"v_cvt_pk_bf16_f32 v185, v186, v187\n" \
"v_mfma_f32_16x16x32_bf16 v[12:15], a[112:115], a[80:83], v[12:15]\n" \
"ds_read_b64_tr_b16 a[214:215], v209 offset:17408\n" \
"v_fma_f32 v243, v188, v189, v243\n" \
"v_mfma_f32_16x16x32_bf16 v[12:15], a[116:119], a[84:87], v[12:15]\n" \
"global_load_lds_dwordx4 v253, s[50:51] offset:-1024\n" \
"v_cvt_pk_bf16_f32 v186, v188, v189\n" \
"v_mfma_f32_16x16x32_bf16 v[56:59], a[96:99], a[88:91], v[56:59]\n" \
"ds_read_b64_tr_b16 a[216:217], v210 offset:4096\n" \
"v_fma_f32 v243, v190, v191, v243\n" \
"v_mfma_f32_16x16x32_bf16 v[56:59], a[100:103], a[92:95], v[56:59]\n" \
"ds_read_b64_tr_b16 a[218:219], v210 offset:5120\n" \
"v_cvt_pk_bf16_f32 v187, v190, v191\n" \
"v_mfma_f32_16x16x32_bf16 v[16:19], a[104:107], a[88:91], v[16:19]\n" \
"buffer_store_dwordx4 v[184:187], v235, s[56:59], s78 offen offset:2048 sc1\n" \
"ds_read_b64_tr_b16 a[220:221], v210 offset:16384\n" \
"s_add_u32 s78, s78, 0xfff7d000\n" \
"v_add_f32 v240, v240, v241\n" \
"v_add_f32 v242, v242, v243\n" \
"v_mfma_f32_16x16x32_bf16 v[16:19], a[108:111], a[92:95], v[16:19]\n" \
"global_load_lds_dwordx4 v253, s[50:51] offset:1024\n" \
"v_mov_b32 v241, 0x207\n" \
"v_mfma_f32_16x16x32_bf16 v[20:23], a[112:115], a[88:91], v[20:23]\n" \
"s_add_u32 s50, s50, 0x60000\n" \
"s_addc_u32 s51, s51, 0\n" \
"ds_read_b64_tr_b16 a[222:223], v210 offset:17408\n" \
"v_mfma_f32_16x16x32_bf16 v[20:23], a[116:119], a[92:95], v[20:23]\n" \
"s_waitcnt lgkmcnt(3)\n" \
"v_add_u32 v207, 0xffff3000, v207\n" \
"v_add_u32 v208, 0xffff3000, v208\n" \
"v_mfma_f32_16x16x32_bf16 v[64:67], a[96:99], a[192:195], v[64:67]\n" \
"v_add_u32 v205, 0xffff3000, v205\n" \
"v_add_u32 v206, 0xffff3000, v206\n" \
"v_mfma_f32_16x16x32_bf16 v[64:67], a[100:103], a[196:199], v[64:67]\n" \
"v_add_u32 v209, 0xffff3000, v209\n" \
"v_add_u32 v210, 0xffff3000, v210\n" \
"v_mfma_f32_16x16x32_bf16 v[24:27], a[104:107], a[192:195], v[24:27]\n" \
"v_add_f32 v240, v240, v242\n" \
"v_cmp_class_f32 vcc, v240, v241\n" \
"v_mfma_f32_16x16x32_bf16 v[24:27], a[108:111], a[196:199], v[24:27]\n" \
"v_mfma_f32_16x16x32_bf16 v[28:31], a[112:115], a[192:195], v[28:31]\n" \
"s_waitcnt vmcnt(15) lgkmcnt(0)\n" \
"s_cbranch_vccnz Lexact_%=\n" \
"Lexr_%=:\n" \
"v_mfma_f32_16x16x32_bf16 v[28:31], a[116:119], a[196:199], v[28:31]\n" \
"s_barrier\n" \
"v_mfma_f32_16x16x32_bf16 v[72:75], a[64:67], a[208:211], v[72:75]\n" \
"ds_read_b64_tr_b16 a[96:97], v209 offset:2048\n" \
"v_mfma_f32_16x16x32_bf16 v[72:75], a[68:71], a[212:215], v[72:75]\n" \
"ds_read_b64_tr_b16 a[98:99], v209 offset:3072\n" \
"v_mfma_f32_16x16x32_bf16 v[44:47], a[200:203], a[72:75], v[44:47]\n" \
"s_add_u32 m0, s95, 118784\n" \
"ds_read_b128 a[64:67], v207 offset:0\n" \
"ds_read_b64_tr_b16 a[100:101], v209 offset:14336\n" \
"v_mfma_f32_16x16x32_bf16 v[44:47], a[204:207], a[76:79], v[44:47]\n" \
"global_load_lds_dwordx4 v252, s[42:43] offset:0\n" \
"v_mfma_f32_16x16x32_bf16 v[80:83], a[72:75], a[208:211], v[80:83]\n" \
"ds_read_b128 a[68:71], v208 offset:0\n" \
"ds_read_b64_tr_b16 a[102:103], v209 offset:15360\n" \
"v_mfma_f32_16x16x32_bf16 v[80:83], a[76:79], a[212:215], v[80:83]\n" \
"ds_read_b64_tr_b16 a[104:105], v209 offset:0\n" \
"v_mfma_f32_16x16x32_bf16 v[84:87], a[72:75], a[216:219], v[84:87]\n" \
"ds_read_b64_tr_b16 a[106:107], v209 offset:1024\n" \
"v_mfma_f32_16x16x32_bf16 v[84:87], a[76:79], a[220:223], v[84:87]\n" \
"global_load_lds_dwordx4 v252, s[42:43] offset:2048\n" \
"v_mfma_f32_16x16x32_bf16 v[52:55], a[200:203], a[80:83], v[52:55]\n" \
"s_add_u32 s42, s42, 0x80\n" \
"s_addc_u32 s43, s43, 0\n" \
"ds_read_b128 a[72:75], v205 offset:0\n" \
"v_mfma_f32_16x16x32_bf16 v[52:55], a[204:207], a[84:87], v[52:55]\n" \
"ds_read_b128 a[76:79], v206 offset:0\n" \
"v_mfma_f32_16x16x32_bf16 v[88:91], a[80:83], a[208:211], v[88:91]\n" \
"ds_read_b64_tr_b16 a[108:109], v209 offset:12288\n" \
"v_mfma_f32_16x16x32_bf16 v[88:91], a[84:87], a[212:215], v[88:91]\n" \
"global_load_lds_dwordx4 v252, s[44:45] offset:1024\n" \
"v_mfma_f32_16x16x32_bf16 v[92:95], a[80:83], a[216:219], v[92:95]\n" \
"ds_read_b64_tr_b16 a[110:111], v209 offset:13312\n" \
"v_mfma_f32_16x16x32_bf16 v[92:95], a[84:87], a[220:223], v[92:95]\n" \
"ds_read_b64_tr_b16 a[112:113], v210 offset:0\n" \
"v_mfma_f32_16x16x32_bf16 v[60:63], a[200:203], a[88:91], v[60:63]\n" \
"ds_read_b128 a[80:83], v205 offset:4096\n" \
"v_mfma_f32_16x16x32_bf16 v[60:63], a[204:207], a[92:95], v[60:63]\n" \
"global_load_lds_dwordx4 v252, s[44:45] offset:3072\n" \
"v_mfma_f32_16x16x32_bf16 v[96:99], a[88:91], a[208:211], v[96:99]\n" \
"s_add_u32 s44, s44, 0x80\n" \
"s_addc_u32 s45, s45, 0\n" \
"ds_read_b128 a[84:87], v206 offset:4096\n" \
"v_mfma_f32_16x16x32_bf16 v[96:99], a[92:95], a[212:215], v[96:99]\n" \
"ds_read_b64_tr_b16 a[114:115], v210 offset:1024\n" \
"v_mfma_f32_16x16x32_bf16 v[100:103], a[88:91], a[216:219], v[100:103]\n" \
"s_add_u32 m0, s85, 135168\n" \
"ds_read_b64_tr_b16 a[116:117], v210 offset:12288\n" \
"v_mfma_f32_16x16x32_bf16 v[100:103], a[92:95], a[220:223], v[100:103]\n" \
"buffer_load_dwordx4 v246, s[12:15], s46 offen lds\n" \
"v_mfma_f32_16x16x32_bf16 v[68:71], a[200:203], a[192:195], v[68:71]\n" \
"ds_read_b128 a[88:91], v205 offset:8192\n" \
"s_add_u32 s46, s46, 0x80\n" \
"v_mfma_f32_16x16x32_bf16 v[68:71], a[204:207], a[196:199], v[68:71]\n" \
"ds_read_b128 a[92:95], v206 offset:8192\n" \
"v_mfma_f32_16x16x32_bf16 v[104:107], a[192:195], a[208:211], v[104:107]\n" \
"s_add_u32 m0, s84, 143360\n" \
"ds_read_b64_tr_b16 a[118:119], v210 offset:13312\n" \
"v_mfma_f32_16x16x32_bf16 v[104:107], a[196:199], a[212:215], v[104:107]\n" \
"global_load_lds_dwordx4 v253, s[48:49] offset:-4096\n" \
"v_mfma_f32_16x16x32_bf16 v[108:111], a[192:195], a[216:219], v[108:111]\n" \
"v_mfma_f32_16x16x32_bf16 v[108:111], a[196:199], a[220:223], v[108:111]\n" \
"s_waitcnt lgkmcnt(2)\n" \
"v_mfma_f32_16x16x32_bf16 v[32:35], a[96:99], a[64:67], v[32:35]\n" \
"ds_read_b128 a[192:195], v205 offset:12288\n" \
"v_mfma_f32_16x16x32_bf16 v[32:35], a[100:103], a[68:71], v[32:35]\n" \
"global_load_lds_dwordx4 v253, s[48:49] offset:-2048\n" \
"v_mfma_f32_16x16x32_bf16 v[40:43], a[96:99], a[72:75], v[40:43]\n" \
"ds_read_b128 a[196:199], v206 offset:12288\n" \
"v_mfma_f32_16x16x32_bf16 v[40:43], a[100:103], a[76:79], v[40:43]\n" \
"ds_read_b64_tr_b16 a[200:201], v210 offset:2048\n" \
"v_mfma_f32_16x16x32_bf16 v[0:3], a[104:107], a[72:75], v[0:3]\n" \
"ds_read_b64_tr_b16 a[202:203], v210 offset:3072\n" \
"v_mfma_f32_16x16x32_bf16 v[0:3], a[108:111], a[76:79], v[0:3]\n" \
"s_waitcnt lgkmcnt(3)\n" \
"global_load_lds_dwordx4 v253, s[48:49] offset:0\n" \
"v_mfma_f32_16x16x32_bf16 v[4:7], a[112:115], a[72:75], v[4:7]\n" \
"s_add_u32 s48, s48, 0x60000\n" \
"s_addc_u32 s49, s49, 0\n" \
"ds_read_b64_tr_b16 a[204:205], v210 offset:14336\n" \
"v_mfma_f32_16x16x32_bf16 v[4:7], a[116:119], a[76:79], v[4:7]\n" \
"ds_read_b64_tr_b16 a[206:207], v210 offset:15360\n" \
"v_mfma_f32_16x16x32_bf16 v[48:51], a[96:99], a[80:83], v[48:51]\n" \
"ds_read_b64_tr_b16 a[208:209], v209 offset:4096\n" \
"v_mfma_f32_16x16x32_bf16 v[48:51], a[100:103], a[84:87], v[48:51]\n" \
"global_load_lds_dwordx4 v253, s[50:51] offset:-3072\n" \
"v_mfma_f32_16x16x32_bf16 v[8:11], a[104:107], a[80:83], v[8:11]\n" \
"ds_read_b64_tr_b16 a[210:211], v209 offset:5120\n" \
"v_mfma_f32_16x16x32_bf16 v[8:11], a[108:111], a[84:87], v[8:11]\n" \
"ds_read_b64_tr_b16 a[212:213], v209 offset:16384\n" \
"v_mfma_f32_16x16x32_bf16 v[12:15], a[112:115], a[80:83], v[12:15]\n" \
"ds_read_b64_tr_b16 a[214:215], v209 offset:17408\n" \
"v_mfma_f32_16x16x32_bf16 v[12:15], a[116:119], a[84:87], v[12:15]\n" \
"global_load_lds_dwordx4 v253, s[50:51] offset:-1024\n" \
"v_mfma_f32_16x16x32_bf16 v[56:59], a[96:99], a[88:91], v[56:59]\n" \
"ds_read_b64_tr_b16 a[216:217], v210 offset:4096\n" \
"v_mfma_f32_16x16x32_bf16 v[56:59], a[100:103], a[92:95], v[56:59]\n" \
"ds_read_b64_tr_b16 a[218:219], v210 offset:5120\n" \
"v_mfma_f32_16x16x32_bf16 v[16:19], a[104:107], a[88:91], v[16:19]\n" \
"ds_read_b64_tr_b16 a[220:221], v210 offset:16384\n" \
"v_mfma_f32_16x16x32_bf16 v[16:19], a[108:111], a[92:95], v[16:19]\n" \
"global_load_lds_dwordx4 v253, s[50:51] offset:1024\n" \
"v_mfma_f32_16x16x32_bf16 v[20:23], a[112:115], a[88:91], v[20:23]\n" \
"s_add_u32 s50, s50, 0x60000\n" \
"s_addc_u32 s51, s51, 0\n" \
"ds_read_b64_tr_b16 a[222:223], v210 offset:17408\n" \
"v_mfma_f32_16x16x32_bf16 v[20:23], a[116:119], a[92:95], v[20:23]\n" \
"s_waitcnt lgkmcnt(3)\n" \
"v_add_u32 v207, 0xffff0000, v207\n" \
"v_add_u32 v208, 0xffff0000, v208\n" \
"v_mfma_f32_16x16x32_bf16 v[64:67], a[96:99], a[192:195], v[64:67]\n" \
"v_add_u32 v205, 0xffff0000, v205\n" \
"v_add_u32 v206, 0xffff0000, v206\n" \
"v_mfma_f32_16x16x32_bf16 v[64:67], a[100:103], a[196:199], v[64:67]\n" \
"v_add_u32 v209, 0xffff0000, v209\n" \
"v_add_u32 v210, 0xffff0000, v210\n" \
"v_mfma_f32_16x16x32_bf16 v[24:27], a[104:107], a[192:195], v[24:27]\n" \
"v_mfma_f32_16x16x32_bf16 v[24:27], a[108:111], a[196:199], v[24:27]\n" \
"s_lshl_b32 s70, s63, 7\n" \
"s_lshl_b32 s71, s62, 2\n" \
"s_add_u32 s71, s71, 65589248\n" \
"v_mfma_f32_16x16x32_bf16 v[28:31], a[112:115], a[192:195], v[28:31]\n" \
"s_waitcnt vmcnt(11) lgkmcnt(0)\n" \
"s_add_u32 s72, s71, s70\n" \
"s_sub_u32 s71, s71, s70\n" \
"v_mfma_f32_16x16x32_bf16 v[28:31], a[116:119], a[196:199], v[28:31]\n" \
"s_barrier\n" \
"v_mfma_f32_16x16x32_bf16 v[72:75], a[64:67], a[208:211], v[72:75]\n" \
"s_add_u32 s71, s71, 1920\n" \
"ds_read_b64_tr_b16 a[96:97], v209 offset:2048\n" \
"v_mov_b32 v236, s60\n" \
"v_mfma_f32_16x16x32_bf16 v[72:75], a[68:71], a[212:215], v[72:75]\n" \
"ds_read_b64_tr_b16 a[98:99], v209 offset:3072\n" \
"v_lshlrev_b32 v237, 28, s61\n" \
"v_mfma_f32_16x16x32_bf16 v[44:47], a[200:203], a[72:75], v[44:47]\n" \
"s_add_u32 m0, s95, 65536\n" \
"ds_read_b128 a[64:67], v207 offset:0\n" \
"ds_read_b64_tr_b16 a[100:101], v209 offset:14336\n" \
"v_mfma_f32_16x16x32_bf16 v[44:47], a[204:207], a[76:79], v[44:47]\n" \
"buffer_store_dword v236, v237, s[56:59], s72 offen sc1\n" \
"global_load_lds_dwordx4 v252, s[42:43] offset:0\n" \
"v_mfma_f32_16x16x32_bf16 v[80:83], a[72:75], a[208:211], v[80:83]\n" \
"ds_read_b128 a[68:71], v208 offset:0\n" \
"ds_read_b64_tr_b16 a[102:103], v209 offset:15360\n" \
"v_mfma_f32_16x16x32_bf16 v[80:83], a[76:79], a[212:215], v[80:83]\n" \
"buffer_store_dword v236, v237, s[56:59], s71 offen sc1\n" \
"ds_read_b64_tr_b16 a[104:105], v209 offset:0\n" \
"v_mfma_f32_16x16x32_bf16 v[84:87], a[72:75], a[216:219], v[84:87]\n" \
"ds_read_b64_tr_b16 a[106:107], v209 offset:1024\n" \
"v_mfma_f32_16x16x32_bf16 v[84:87], a[76:79], a[220:223], v[84:87]\n" \
"global_load_lds_dwordx4 v252, s[42:43] offset:2048\n" \
"v_mfma_f32_16x16x32_bf16 v[52:55], a[200:203], a[80:83], v[52:55]\n" \
"s_add_u32 s42, s42, 0x80\n" \
"s_addc_u32 s43, s43, 0\n" \
"ds_read_b128 a[72:75], v205 offset:0\n" \
"v_mfma_f32_16x16x32_bf16 v[52:55], a[204:207], a[84:87], v[52:55]\n" \
"ds_read_b128 a[76:79], v206 offset:0\n" \
"v_mfma_f32_16x16x32_bf16 v[88:91], a[80:83], a[208:211], v[88:91]\n" \
"ds_read_b64_tr_b16 a[108:109], v209 offset:12288\n" \
"v_mfma_f32_16x16x32_bf16 v[88:91], a[84:87], a[212:215], v[88:91]\n" \
"global_load_lds_dwordx4 v252, s[44:45] offset:1024\n" \
"v_mfma_f32_16x16x32_bf16 v[92:95], a[80:83], a[216:219], v[92:95]\n" \
"ds_read_b64_tr_b16 a[110:111], v209 offset:13312\n" \
"v_mfma_f32_16x16x32_bf16 v[92:95], a[84:87], a[220:223], v[92:95]\n" \
"ds_read_b64_tr_b16 a[112:113], v210 offset:0\n" \
"v_mfma_f32_16x16x32_bf16 v[60:63], a[200:203], a[88:91], v[60:63]\n" \
"ds_read_b128 a[80:83], v205 offset:4096\n" \
"v_mfma_f32_16x16x32_bf16 v[60:63], a[204:207], a[92:95], v[60:63]\n" \
"global_load_lds_dwordx4 v252, s[44:45] offset:3072\n" \
"v_mfma_f32_16x16x32_bf16 v[96:99], a[88:91], a[208:211], v[96:99]\n" \
"s_add_u32 s44, s44, 0x80\n" \
"s_addc_u32 s45, s45, 0\n" \
"ds_read_b128 a[84:87], v206 offset:4096\n" \
"v_mfma_f32_16x16x32_bf16 v[96:99], a[92:95], a[212:215], v[96:99]\n" \
"ds_read_b64_tr_b16 a[114:115], v210 offset:1024\n" \
"v_mfma_f32_16x16x32_bf16 v[100:103], a[88:91], a[216:219], v[100:103]\n" \
"s_add_u32 m0, s85, 81920\n" \
"ds_read_b64_tr_b16 a[116:117], v210 offset:12288\n" \
"v_mfma_f32_16x16x32_bf16 v[100:103], a[92:95], a[220:223], v[100:103]\n" \
"buffer_load_dwordx4 v246, s[12:15], s46 offen lds\n" \
"v_mfma_f32_16x16x32_bf16 v[68:71], a[200:203], a[192:195], v[68:71]\n" \
"ds_read_b128 a[88:91], v205 offset:8192\n" \
"s_add_u32 s46, s46, 0x80\n" \
"v_mfma_f32_16x16x32_bf16 v[68:71], a[204:207], a[196:199], v[68:71]\n" \
"ds_read_b128 a[92:95], v206 offset:8192\n" \
"v_mfma_f32_16x16x32_bf16 v[104:107], a[192:195], a[208:211], v[104:107]\n" \
"s_add_u32 m0, s84, 90112\n" \
"ds_read_b64_tr_b16 a[118:119], v210 offset:13312\n" \
"v_mfma_f32_16x16x32_bf16 v[104:107], a[196:199], a[212:215], v[104:107]\n" \
"global_load_lds_dwordx4 v253, s[48:49] offset:-4096\n" \
"v_mfma_f32_16x16x32_bf16 v[108:111], a[192:195], a[216:219], v[108:111]\n" \
"v_mfma_f32_16x16x32_bf16 v[108:111], a[196:199], a[220:223], v[108:111]\n" \
"s_waitcnt lgkmcnt(2)\n" \
"v_mfma_f32_16x16x32_bf16 v[32:35], a[96:99], a[64:67], v[32:35]\n" \
"s_lshr_b32 s89, s22, 7\n" \
"ds_read_b128 a[192:195], v205 offset:12288\n" \
"s_sub_u32 s89, 3, s89\n" \
"v_mfma_f32_16x16x32_bf16 v[32:35], a[100:103], a[68:71], v[32:35]\n" \
"global_load_lds_dwordx4 v253, s[48:49] offset:-2048\n" \
"v_bfe_u32 v203, v248, 7, 2\n" \
"v_mfma_f32_16x16x32_bf16 v[40:43], a[96:99], a[72:75], v[40:43]\n" \
"s_and_b32 s89, s89, 7\n" \
"ds_read_b128 a[196:199], v206 offset:12288\n" \
"s_and_b32 s90, %1, 1\n" \
"v_mfma_f32_16x16x32_bf16 v[40:43], a[100:103], a[76:79], v[40:43]\n" \
"ds_read_b64_tr_b16 a[200:201], v210 offset:2048\n" \
"v_bfe_u32 v204, v248, 5, 2\n" \
"v_mfma_f32_16x16x32_bf16 v[0:3], a[104:107], a[72:75], v[0:3]\n" \
"s_lshl_b32 s90, s90, 3\n" \
"s_add_u32 s89, s89, s90\n" \
"ds_read_b64_tr_b16 a[202:203], v210 offset:3072\n" \
"v_mfma_f32_16x16x32_bf16 v[0:3], a[108:111], a[76:79], v[0:3]\n" \
"s_waitcnt lgkmcnt(3)\n" \
"global_load_lds_dwordx4 v253, s[48:49] offset:0\n" \
"v_min_u32 v204, 2, v204\n" \
"v_mfma_f32_16x16x32_bf16 v[4:7], a[112:115], a[72:75], v[4:7]\n" \
"s_add_u32 s48, s48, 0x60000\n" \
"s_addc_u32 s49, s49, 0\n" \
"ds_read_b64_tr_b16 a[204:205], v210 offset:14336\n" \
"v_mfma_f32_16x16x32_bf16 v[4:7], a[116:119], a[76:79], v[4:7]\n" \
"s_lshl_b32 s89, s89, 7\n" \
"s_lshr_b32 s90, %1, 3\n" \
"ds_read_b64_tr_b16 a[206:207], v210 offset:15360\n" \
"v_mfma_f32_16x16x32_bf16 v[48:51], a[96:99], a[80:83], v[48:51]\n" \
"ds_read_b64_tr_b16 a[208:209], v209 offset:4096\n" \
"v_mad_u32_u24 v203, v203, 3, v204\n" \
"v_mfma_f32_16x16x32_bf16 v[48:51], a[100:103], a[84:87], v[48:51]\n" \
"s_lshl_b32 s90, s90, 2\n" \
"global_load_lds_dwordx4 v253, s[50:51] offset:-3072\n" \
"v_mfma_f32_16x16x32_bf16 v[8:11], a[104:107], a[80:83], v[8:11]\n" \
"s_add_u32 s90, s90, s61\n" \
"ds_read_b64_tr_b16 a[210:211], v209 offset:5120\n" \
"v_and_b32 v204, 16, v248\n" \
"v_mfma_f32_16x16x32_bf16 v[8:11], a[108:111], a[84:87], v[8:11]\n" \
"s_add_u32 s90, s90, 128\n" \
"s_mul_i32 s90, s90, 24576\n" \
"ds_read_b64_tr_b16 a[212:213], v209 offset:16384\n" \
"v_mfma_f32_16x16x32_bf16 v[12:15], a[112:115], a[80:83], v[12:15]\n" \
"s_add_u32 s89, s89, s90\n" \
"ds_read_b64_tr_b16 a[214:215], v209 offset:17408\n" \
"v_lshl_add_u32 v204, v204, 2, s89\n" \
"v_mfma_f32_16x16x32_bf16 v[12:15], a[116:119], a[84:87], v[12:15]\n" \
"global_load_lds_dwordx4 v253, s[50:51] offset:-1024\n" \
"v_lshl_add_u32 v203, v203, 11, v204\n" \
"v_mfma_f32_16x16x32_bf16 v[56:59], a[96:99], a[88:91], v[56:59]\n" \
"ds_read_b64_tr_b16 a[216:217], v210 offset:4096\n" \
"v_mfma_f32_16x16x32_bf16 v[56:59], a[100:103], a[92:95], v[56:59]\n" \
"ds_read_b64_tr_b16 a[218:219], v210 offset:5120\n" \
"v_mfma_f32_16x16x32_bf16 v[16:19], a[104:107], a[88:91], v[16:19]\n" \
"ds_read_b64_tr_b16 a[220:221], v210 offset:16384\n" \
"v_mfma_f32_16x16x32_bf16 v[16:19], a[108:111], a[92:95], v[16:19]\n" \
"global_load_lds_dwordx4 v253, s[50:51] offset:1024\n" \
"v_mfma_f32_16x16x32_bf16 v[20:23], a[112:115], a[88:91], v[20:23]\n" \
"s_add_u32 s50, s50, 0x60000\n" \
"s_addc_u32 s51, s51, 0\n" \
"ds_read_b64_tr_b16 a[222:223], v210 offset:17408\n" \
"v_mfma_f32_16x16x32_bf16 v[20:23], a[116:119], a[92:95], v[20:23]\n" \
"s_waitcnt lgkmcnt(3)\n" \
"v_add_u32 v207, 0x1d000, v207\n" \
"v_add_u32 v208, 0x1d000, v208\n" \
"v_mfma_f32_16x16x32_bf16 v[64:67], a[96:99], a[192:195], v[64:67]\n" \
"v_add_u32 v205, 0x1d000, v205\n" \
"v_add_u32 v206, 0x1d000, v206\n" \
"v_mfma_f32_16x16x32_bf16 v[64:67], a[100:103], a[196:199], v[64:67]\n" \
"v_add_u32 v209, 0x1d000, v209\n" \
"v_add_u32 v210, 0x1d000, v210\n" \
"v_mfma_f32_16x16x32_bf16 v[24:27], a[104:107], a[192:195], v[24:27]\n" \
"v_mfma_f32_16x16x32_bf16 v[24:27], a[108:111], a[196:199], v[24:27]\n" \
"v_mfma_f32_16x16x32_bf16 v[28:31], a[112:115], a[192:195], v[28:31]\n" \
"s_waitcnt vmcnt(13) lgkmcnt(0)\n" \
"v_mfma_f32_16x16x32_bf16 v[28:31], a[116:119], a[196:199], v[28:31]\n" \
"s_barrier\n" \
"v_mfma_f32_16x16x32_bf16 v[72:75], a[64:67], a[208:211], v[72:75]\n" \
"ds_read_b64_tr_b16 a[96:97], v209 offset:2048\n" \
"v_mfma_f32_16x16x32_bf16 v[72:75], a[68:71], a[212:215], v[72:75]\n" \
"ds_read_b64_tr_b16 a[98:99], v209 offset:3072\n" \
"v_mfma_f32_16x16x32_bf16 v[44:47], a[200:203], a[72:75], v[44:47]\n" \
"s_add_u32 m0, s95, 0\n" \
"ds_read_b128 a[64:67], v207 offset:0\n" \
"ds_read_b64_tr_b16 a[100:101], v209 offset:14336\n" \
"v_mfma_f32_16x16x32_bf16 v[44:47], a[204:207], a[76:79], v[44:47]\n" \
"global_load_lds_dwordx4 v252, s[42:43] offset:0\n" \
"v_mfma_f32_16x16x32_bf16 v[80:83], a[72:75], a[208:211], v[80:83]\n" \
"ds_read_b128 a[68:71], v208 offset:0\n" \
"ds_read_b64_tr_b16 a[102:103], v209 offset:15360\n" \
"v_mfma_f32_16x16x32_bf16 v[80:83], a[76:79], a[212:215], v[80:83]\n" \
"ds_read_b64_tr_b16 a[104:105], v209 offset:0\n" \
"v_mfma_f32_16x16x32_bf16 v[84:87], a[72:75], a[216:219], v[84:87]\n" \
"ds_read_b64_tr_b16 a[106:107], v209 offset:1024\n" \
"v_mfma_f32_16x16x32_bf16 v[84:87], a[76:79], a[220:223], v[84:87]\n" \
"global_load_lds_dwordx4 v252, s[42:43] offset:2048\n" \
"v_mfma_f32_16x16x32_bf16 v[52:55], a[200:203], a[80:83], v[52:55]\n" \
"s_add_u32 s42, s42, 0x80\n" \
"s_addc_u32 s43, s43, 0\n" \
"ds_read_b128 a[72:75], v205 offset:0\n" \
"v_mfma_f32_16x16x32_bf16 v[52:55], a[204:207], a[84:87], v[52:55]\n" \
"ds_read_b128 a[76:79], v206 offset:0\n" \
"v_mfma_f32_16x16x32_bf16 v[88:91], a[80:83], a[208:211], v[88:91]\n" \
"ds_read_b64_tr_b16 a[108:109], v209 offset:12288\n" \
"v_mfma_f32_16x16x32_bf16 v[88:91], a[84:87], a[212:215], v[88:91]\n" \
"global_load_lds_dwordx4 v252, s[44:45] offset:1024\n" \
"v_mfma_f32_16x16x32_bf16 v[92:95], a[80:83], a[216:219], v[92:95]\n" \
"ds_read_b64_tr_b16 a[110:111], v209 offset:13312\n" \
"v_mfma_f32_16x16x32_bf16 v[92:95], a[84:87], a[220:223], v[92:95]\n" \
"ds_read_b64_tr_b16 a[112:113], v210 offset:0\n" \
"v_mfma_f32_16x16x32_bf16 v[60:63], a[200:203], a[88:91], v[60:63]\n" \
"ds_read_b128 a[80:83], v205 offset:4096\n" \
"v_mfma_f32_16x16x32_bf16 v[60:63], a[204:207], a[92:95], v[60:63]\n" \
"global_load_lds_dwordx4 v252, s[44:45] offset:3072\n" \
"v_mfma_f32_16x16x32_bf16 v[96:99], a[88:91], a[208:211], v[96:99]\n" \
"s_add_u32 s44, s44, 0x80\n" \
"s_addc_u32 s45, s45, 0\n" \
"ds_read_b128 a[84:87], v206 offset:4096\n" \
"v_mfma_f32_16x16x32_bf16 v[96:99], a[92:95], a[212:215], v[96:99]\n" \
"ds_read_b64_tr_b16 a[114:115], v210 offset:1024\n" \
"v_mfma_f32_16x16x32_bf16 v[100:103], a[88:91], a[216:219], v[100:103]\n" \
"s_add_u32 m0, s85, 16384\n" \
"ds_read_b64_tr_b16 a[116:117], v210 offset:12288\n" \
"v_mfma_f32_16x16x32_bf16 v[100:103], a[92:95], a[220:223], v[100:103]\n" \
"buffer_load_dwordx4 v246, s[12:15], s46 offen lds\n" \
"v_mfma_f32_16x16x32_bf16 v[68:71], a[200:203], a[192:195], v[68:71]\n" \
"ds_read_b128 a[88:91], v205 offset:8192\n" \
"s_add_u32 s46, s46, 0x80\n" \
"v_mfma_f32_16x16x32_bf16 v[68:71], a[204:207], a[196:199], v[68:71]\n" \
"ds_read_b128 a[92:95], v206 offset:8192\n" \
"v_mfma_f32_16x16x32_bf16 v[104:107], a[192:195], a[208:211], v[104:107]\n" \
"s_add_u32 m0, s84, 24576\n" \
"ds_read_b64_tr_b16 a[118:119], v210 offset:13312\n" \
"v_mfma_f32_16x16x32_bf16 v[104:107], a[196:199], a[212:215], v[104:107]\n" \
"global_load_lds_dwordx4 v253, s[48:49] offset:-4096\n" \
"v_mfma_f32_16x16x32_bf16 v[108:111], a[192:195], a[216:219], v[108:111]\n" \
"v_mfma_f32_16x16x32_bf16 v[108:111], a[196:199], a[220:223], v[108:111]\n" \
"s_waitcnt lgkmcnt(2)\n" \
"v_mfma_f32_16x16x32_bf16 v[32:35], a[96:99], a[64:67], v[32:35]\n" \
"s_mov_b32 s30, 65593856\n" \
"ds_read_b128 a[192:195], v205 offset:12288\n" \
"v_lshrrev_b32 v228, 1, v248\n" \
"v_mfma_f32_16x16x32_bf16 v[32:35], a[100:103], a[68:71], v[32:35]\n" \
"global_load_lds_dwordx4 v253, s[48:49] offset:-2048\n" \
"v_mfma_f32_16x16x32_bf16 v[40:43], a[96:99], a[72:75], v[40:43]\n" \
"ds_read_b128 a[196:199], v206 offset:12288\n" \
"v_mfma_f32_16x16x32_bf16 v[40:43], a[100:103], a[76:79], v[40:43]\n" \
"ds_read_b64_tr_b16 a[200:201], v210 offset:2048\n" \
"v_mfma_f32_16x16x32_bf16 v[0:3], a[104:107], a[72:75], v[0:3]\n" \
"ds_read_b64_tr_b16 a[202:203], v210 offset:3072\n" \
"v_mfma_f32_16x16x32_bf16 v[0:3], a[108:111], a[76:79], v[0:3]\n" \
"s_waitcnt lgkmcnt(3)\n" \
"global_load_lds_dwordx4 v253, s[48:49] offset:0\n" \
"v_mfma_f32_16x16x32_bf16 v[4:7], a[112:115], a[72:75], v[4:7]\n" \
"s_add_u32 s48, s48, 0x60000\n" \
"s_addc_u32 s49, s49, 0\n" \
"ds_read_b64_tr_b16 a[204:205], v210 offset:14336\n" \
"v_mfma_f32_16x16x32_bf16 v[4:7], a[116:119], a[76:79], v[4:7]\n" \
"ds_read_b64_tr_b16 a[206:207], v210 offset:15360\n" \
"v_mfma_f32_16x16x32_bf16 v[48:51], a[96:99], a[80:83], v[48:51]\n" \
"ds_read_b64_tr_b16 a[208:209], v209 offset:4096\n" \
"v_mfma_f32_16x16x32_bf16 v[48:51], a[100:103], a[84:87], v[48:51]\n" \
"global_load_lds_dwordx4 v253, s[50:51] offset:-3072\n" \
"v_mfma_f32_16x16x32_bf16 v[8:11], a[104:107], a[80:83], v[8:11]\n" \
"ds_read_b64_tr_b16 a[210:211], v209 offset:5120\n" \
"v_mfma_f32_16x16x32_bf16 v[8:11], a[108:111], a[84:87], v[8:11]\n" \
"ds_read_b64_tr_b16 a[212:213], v209 offset:16384\n" \
"v_mfma_f32_16x16x32_bf16 v[12:15], a[112:115], a[80:83], v[12:15]\n" \
"ds_read_b64_tr_b16 a[214:215], v209 offset:17408\n" \
"v_mfma_f32_16x16x32_bf16 v[12:15], a[116:119], a[84:87], v[12:15]\n" \
"global_load_lds_dwordx4 v253, s[50:51] offset:-1024\n" \
"v_mfma_f32_16x16x32_bf16 v[56:59], a[96:99], a[88:91], v[56:59]\n" \
"ds_read_b64_tr_b16 a[216:217], v210 offset:4096\n" \
"v_mfma_f32_16x16x32_bf16 v[56:59], a[100:103], a[92:95], v[56:59]\n" \
"ds_read_b64_tr_b16 a[218:219], v210 offset:5120\n" \
"v_mfma_f32_16x16x32_bf16 v[16:19], a[104:107], a[88:91], v[16:19]\n" \
"ds_read_b64_tr_b16 a[220:221], v210 offset:16384\n" \
"v_mfma_f32_16x16x32_bf16 v[16:19], a[108:111], a[92:95], v[16:19]\n" \
"global_load_lds_dwordx4 v253, s[50:51] offset:1024\n" \
"v_mfma_f32_16x16x32_bf16 v[20:23], a[112:115], a[88:91], v[20:23]\n" \
"s_add_u32 s50, s50, 0x60000\n" \
"s_addc_u32 s51, s51, 0\n" \
"ds_read_b64_tr_b16 a[222:223], v210 offset:17408\n" \
"v_mfma_f32_16x16x32_bf16 v[20:23], a[116:119], a[92:95], v[20:23]\n" \
"s_waitcnt lgkmcnt(3)\n" \
"v_add_u32 v207, 0xffff3000, v207\n" \
"v_add_u32 v208, 0xffff3000, v208\n" \
"v_mfma_f32_16x16x32_bf16 v[64:67], a[96:99], a[192:195], v[64:67]\n" \
"v_add_u32 v205, 0xffff3000, v205\n" \
"v_add_u32 v206, 0xffff3000, v206\n" \
"v_mfma_f32_16x16x32_bf16 v[64:67], a[100:103], a[196:199], v[64:67]\n" \
"global_load_dword v204, v203, s[22:23]\n" \
"v_mfma_f32_16x16x32_bf16 v[24:27], a[104:107], a[192:195], v[24:27]\n" \
"v_add_u32 v209, 0xffff3000, v209\n" \
"v_add_u32 v210, 0xffff3000, v210\n" \
"Lpg9871_%=:\n" \
"s_cbranch_execz Lpg12167_%=\n" \
"v_mfma_f32_16x16x32_bf16 v[24:27], a[108:111], a[196:199], v[24:27]\n" \
"v_mfma_f32_16x16x32_bf16 v[28:31], a[112:115], a[192:195], v[28:31]\n" \
"s_waitcnt vmcnt(12) lgkmcnt(0)\n" \
"v_mfma_f32_16x16x32_bf16 v[28:31], a[116:119], a[196:199], v[28:31]\n" \
"s_barrier\n" \
"v_mfma_f32_16x16x32_bf16 v[72:75], a[64:67], a[208:211], v[72:75]\n" \
"ds_read_b64_tr_b16 a[96:97], v209 offset:2048\n" \
"v_mfma_f32_16x16x32_bf16 v[72:75], a[68:71], a[212:215], v[72:75]\n" \
"ds_read_b64_tr_b16 a[98:99], v209 offset:3072\n" \
"v_mfma_f32_16x16x32_bf16 v[44:47], a[200:203], a[72:75], v[44:47]\n" \
"s_add_u32 m0, s95, 118784\n" \
"ds_read_b128 a[64:67], v207 offset:0\n" \
"ds_read_b64_tr_b16 a[100:101], v209 offset:14336\n" \
"v_mfma_f32_16x16x32_bf16 v[44:47], a[204:207], a[76:79], v[44:47]\n" \
"global_load_lds_dwordx4 v252, s[42:43] offset:0\n" \
"v_mfma_f32_16x16x32_bf16 v[80:83], a[72:75], a[208:211], v[80:83]\n" \
"ds_read_b128 a[68:71], v208 offset:0\n" \
"ds_read_b64_tr_b16 a[102:103], v209 offset:15360\n" \
"v_mfma_f32_16x16x32_bf16 v[80:83], a[76:79], a[212:215], v[80:83]\n" \
"ds_read_b64_tr_b16 a[104:105], v209 offset:0\n" \
"v_mfma_f32_16x16x32_bf16 v[84:87], a[72:75], a[216:219], v[84:87]\n" \
"ds_read_b64_tr_b16 a[106:107], v209 offset:1024\n" \
"v_mfma_f32_16x16x32_bf16 v[84:87], a[76:79], a[220:223], v[84:87]\n" \
"global_load_lds_dwordx4 v252, s[42:43] offset:2048\n" \
"v_mfma_f32_16x16x32_bf16 v[52:55], a[200:203], a[80:83], v[52:55]\n" \
"s_add_u32 s42, s42, 0x80\n" \
"s_addc_u32 s43, s43, 0\n" \
"ds_read_b128 a[72:75], v205 offset:0\n" \
"v_mfma_f32_16x16x32_bf16 v[52:55], a[204:207], a[84:87], v[52:55]\n" \
"ds_read_b128 a[76:79], v206 offset:0\n" \
"v_mfma_f32_16x16x32_bf16 v[88:91], a[80:83], a[208:211], v[88:91]\n" \
"ds_read_b64_tr_b16 a[108:109], v209 offset:12288\n" \
"v_mfma_f32_16x16x32_bf16 v[88:91], a[84:87], a[212:215], v[88:91]\n" \
"global_load_lds_dwordx4 v252, s[44:45] offset:1024\n" \
"v_mfma_f32_16x16x32_bf16 v[92:95], a[80:83], a[216:219], v[92:95]\n" \
"ds_read_b64_tr_b16 a[110:111], v209 offset:13312\n" \
"v_mfma_f32_16x16x32_bf16 v[92:95], a[84:87], a[220:223], v[92:95]\n" \
"ds_read_b64_tr_b16 a[112:113], v210 offset:0\n" \
"v_mfma_f32_16x16x32_bf16 v[60:63], a[200:203], a[88:91], v[60:63]\n" \
"ds_read_b128 a[80:83], v205 offset:4096\n" \
"v_mfma_f32_16x16x32_bf16 v[60:63], a[204:207], a[92:95], v[60:63]\n" \
"global_load_lds_dwordx4 v252, s[44:45] offset:3072\n" \
"v_mfma_f32_16x16x32_bf16 v[96:99], a[88:91], a[208:211], v[96:99]\n" \
"s_add_u32 s44, s44, 0x80\n" \
"s_addc_u32 s45, s45, 0\n" \
"ds_read_b128 a[84:87], v206 offset:4096\n" \
"v_mfma_f32_16x16x32_bf16 v[96:99], a[92:95], a[212:215], v[96:99]\n" \
"ds_read_b64_tr_b16 a[114:115], v210 offset:1024\n" \
"v_mfma_f32_16x16x32_bf16 v[100:103], a[88:91], a[216:219], v[100:103]\n" \
"s_add_u32 m0, s85, 135168\n" \
"ds_read_b64_tr_b16 a[116:117], v210 offset:12288\n" \
"v_mfma_f32_16x16x32_bf16 v[100:103], a[92:95], a[220:223], v[100:103]\n" \
"buffer_load_dwordx4 v246, s[12:15], s46 offen lds\n" \
"v_mfma_f32_16x16x32_bf16 v[68:71], a[200:203], a[192:195], v[68:71]\n" \
"ds_read_b128 a[88:91], v205 offset:8192\n" \
"s_add_u32 s46, s46, 0x80\n" \
"v_mfma_f32_16x16x32_bf16 v[68:71], a[204:207], a[196:199], v[68:71]\n" \
"ds_read_b128 a[92:95], v206 offset:8192\n" \
"v_mfma_f32_16x16x32_bf16 v[104:107], a[192:195], a[208:211], v[104:107]\n" \
"s_add_u32 m0, s84, 143360\n" \
"ds_read_b64_tr_b16 a[118:119], v210 offset:13312\n" \
"v_mfma_f32_16x16x32_bf16 v[104:107], a[196:199], a[212:215], v[104:107]\n" \
"global_load_lds_dwordx4 v253, s[48:49] offset:-4096\n" \
"v_mfma_f32_16x16x32_bf16 v[108:111], a[192:195], a[216:219], v[108:111]\n" \
"v_mfma_f32_16x16x32_bf16 v[108:111], a[196:199], a[220:223], v[108:111]\n" \
"s_waitcnt lgkmcnt(2)\n" \
"v_mfma_f32_16x16x32_bf16 v[32:35], a[96:99], a[64:67], v[32:35]\n" \
"ds_read_b128 a[192:195], v205 offset:12288\n" \
"v_mfma_f32_16x16x32_bf16 v[32:35], a[100:103], a[68:71], v[32:35]\n" \
"global_load_lds_dwordx4 v253, s[48:49] offset:-2048\n" \
"v_mfma_f32_16x16x32_bf16 v[40:43], a[96:99], a[72:75], v[40:43]\n" \
"ds_read_b128 a[196:199], v206 offset:12288\n" \
"v_mfma_f32_16x16x32_bf16 v[40:43], a[100:103], a[76:79], v[40:43]\n" \
"ds_read_b64_tr_b16 a[200:201], v210 offset:2048\n" \
"v_mfma_f32_16x16x32_bf16 v[0:3], a[104:107], a[72:75], v[0:3]\n" \
"ds_read_b64_tr_b16 a[202:203], v210 offset:3072\n" \
"v_mfma_f32_16x16x32_bf16 v[0:3], a[108:111], a[76:79], v[0:3]\n" \
"s_waitcnt lgkmcnt(3)\n" \
"global_load_lds_dwordx4 v253, s[48:49] offset:0\n" \
"v_mfma_f32_16x16x32_bf16 v[4:7], a[112:115], a[72:75], v[4:7]\n" \
"s_add_u32 s48, s48, 0x60000\n" \
"s_addc_u32 s49, s49, 0\n" \
"ds_read_b64_tr_b16 a[204:205], v210 offset:14336\n" \
"v_mfma_f32_16x16x32_bf16 v[4:7], a[116:119], a[76:79], v[4:7]\n" \
"ds_read_b64_tr_b16 a[206:207], v210 offset:15360\n" \
"v_mfma_f32_16x16x32_bf16 v[48:51], a[96:99], a[80:83], v[48:51]\n" \
"ds_read_b64_tr_b16 a[208:209], v209 offset:4096\n" \
"v_mfma_f32_16x16x32_bf16 v[48:51], a[100:103], a[84:87], v[48:51]\n" \
"global_load_lds_dwordx4 v253, s[50:51] offset:-3072\n" \
"v_mfma_f32_16x16x32_bf16 v[8:11], a[104:107], a[80:83], v[8:11]\n" \
"ds_read_b64_tr_b16 a[210:211], v209 offset:5120\n" \
"v_mfma_f32_16x16x32_bf16 v[8:11], a[108:111], a[84:87], v[8:11]\n" \
"ds_read_b64_tr_b16 a[212:213], v209 offset:16384\n" \
"v_mfma_f32_16x16x32_bf16 v[12:15], a[112:115], a[80:83], v[12:15]\n" \
"ds_read_b64_tr_b16 a[214:215], v209 offset:17408\n" \
"v_mfma_f32_16x16x32_bf16 v[12:15], a[116:119], a[84:87], v[12:15]\n" \
"global_load_lds_dwordx4 v253, s[50:51] offset:-1024\n" \
"v_mfma_f32_16x16x32_bf16 v[56:59], a[96:99], a[88:91], v[56:59]\n" \
"ds_read_b64_tr_b16 a[216:217], v210 offset:4096\n" \
"v_mfma_f32_16x16x32_bf16 v[56:59], a[100:103], a[92:95], v[56:59]\n" \
"ds_read_b64_tr_b16 a[218:219], v210 offset:5120\n" \
"v_mfma_f32_16x16x32_bf16 v[16:19], a[104:107], a[88:91], v[16:19]\n" \
"ds_read_b64_tr_b16 a[220:221], v210 offset:16384\n" \
"v_mfma_f32_16x16x32_bf16 v[16:19], a[108:111], a[92:95], v[16:19]\n" \
"global_load_lds_dwordx4 v253, s[50:51] offset:1024\n" \
"v_mfma_f32_16x16x32_bf16 v[20:23], a[112:115], a[88:91], v[20:23]\n" \
"s_add_u32 s50, s50, 0x60000\n" \
"s_addc_u32 s51, s51, 0\n" \
"ds_read_b64_tr_b16 a[222:223], v210 offset:17408\n" \
"v_mfma_f32_16x16x32_bf16 v[20:23], a[116:119], a[92:95], v[20:23]\n" \
"s_waitcnt lgkmcnt(3)\n" \
"v_add_u32 v207, 0xffff0000, v207\n" \
"v_add_u32 v208, 0xffff0000, v208\n" \
"v_mfma_f32_16x16x32_bf16 v[64:67], a[96:99], a[192:195], v[64:67]\n" \
"v_add_u32 v205, 0xffff0000, v205\n" \
"v_add_u32 v206, 0xffff0000, v206\n" \
"v_mfma_f32_16x16x32_bf16 v[64:67], a[100:103], a[196:199], v[64:67]\n" \
"buffer_load_dwordx2 v[226:227], v228, s[56:59], s30 offen sc1\n" \
"v_mfma_f32_16x16x32_bf16 v[24:27], a[104:107], a[192:195], v[24:27]\n" \
"v_add_u32 v209, 0xffff0000, v209\n" \
"v_add_u32 v210, 0xffff0000, v210\n" \
"v_mfma_f32_16x16x32_bf16 v[24:27], a[108:111], a[196:199], v[24:27]\n" \
"v_mfma_f32_16x16x32_bf16 v[28:31], a[112:115], a[192:195], v[28:31]\n" \
"s_waitcnt vmcnt(13) lgkmcnt(0)\n" \
"v_mfma_f32_16x16x32_bf16 v[28:31], a[116:119], a[196:199], v[28:31]\n" \
"s_barrier\n" \
"v_mfma_f32_16x16x32_bf16 v[72:75], a[64:67], a[208:211], v[72:75]\n" \
"ds_read_b64_tr_b16 a[96:97], v209 offset:2048\n" \
"v_mfma_f32_16x16x32_bf16 v[72:75], a[68:71], a[212:215], v[72:75]\n" \
"ds_read_b64_tr_b16 a[98:99], v209 offset:3072\n" \
"v_mfma_f32_16x16x32_bf16 v[44:47], a[200:203], a[72:75], v[44:47]\n" \
"s_add_u32 m0, s95, 65536\n" \
"ds_read_b128 a[64:67], v207 offset:0\n" \
"ds_read_b64_tr_b16 a[100:101], v209 offset:14336\n" \
"v_mfma_f32_16x16x32_bf16 v[44:47], a[204:207], a[76:79], v[44:47]\n" \
"global_load_lds_dwordx4 v252, s[42:43] offset:0\n" \
"v_mfma_f32_16x16x32_bf16 v[80:83], a[72:75], a[208:211], v[80:83]\n" \
"ds_read_b128 a[68:71], v208 offset:0\n" \
"ds_read_b64_tr_b16 a[102:103], v209 offset:15360\n" \
"v_mfma_f32_16x16x32_bf16 v[80:83], a[76:79], a[212:215], v[80:83]\n" \
"ds_read_b64_tr_b16 a[104:105], v209 offset:0\n" \
"v_mfma_f32_16x16x32_bf16 v[84:87], a[72:75], a[216:219], v[84:87]\n" \
"ds_read_b64_tr_b16 a[106:107], v209 offset:1024\n" \
"v_mfma_f32_16x16x32_bf16 v[84:87], a[76:79], a[220:223], v[84:87]\n" \
"global_load_lds_dwordx4 v252, s[42:43] offset:2048\n" \
"v_mfma_f32_16x16x32_bf16 v[52:55], a[200:203], a[80:83], v[52:55]\n" \
"s_add_u32 s42, s42, 0x80\n" \
"s_addc_u32 s43, s43, 0\n" \
"ds_read_b128 a[72:75], v205 offset:0\n" \
"v_mfma_f32_16x16x32_bf16 v[52:55], a[204:207], a[84:87], v[52:55]\n" \
"ds_read_b128 a[76:79], v206 offset:0\n" \
"v_mfma_f32_16x16x32_bf16 v[88:91], a[80:83], a[208:211], v[88:91]\n" \
"ds_read_b64_tr_b16 a[108:109], v209 offset:12288\n" \
"v_mfma_f32_16x16x32_bf16 v[88:91], a[84:87], a[212:215], v[88:91]\n" \
"global_load_lds_dwordx4 v252, s[44:45] offset:1024\n" \
"v_mfma_f32_16x16x32_bf16 v[92:95], a[80:83], a[216:219], v[92:95]\n" \
"ds_read_b64_tr_b16 a[110:111], v209 offset:13312\n" \
"v_mfma_f32_16x16x32_bf16 v[92:95], a[84:87], a[220:223], v[92:95]\n" \
"ds_read_b64_tr_b16 a[112:113], v210 offset:0\n" \
"v_mfma_f32_16x16x32_bf16 v[60:63], a[200:203], a[88:91], v[60:63]\n" \
"ds_read_b128 a[80:83], v205 offset:4096\n" \
"v_mfma_f32_16x16x32_bf16 v[60:63], a[204:207], a[92:95], v[60:63]\n" \
"global_load_lds_dwordx4 v252, s[44:45] offset:3072\n" \
"v_mfma_f32_16x16x32_bf16 v[96:99], a[88:91], a[208:211], v[96:99]\n" \
"s_add_u32 s44, s44, 0x80\n" \
"s_addc_u32 s45, s45, 0\n" \
"ds_read_b128 a[84:87], v206 offset:4096\n" \
"v_mfma_f32_16x16x32_bf16 v[96:99], a[92:95], a[212:215], v[96:99]\n" \
"ds_read_b64_tr_b16 a[114:115], v210 offset:1024\n" \
"v_mfma_f32_16x16x32_bf16 v[100:103], a[88:91], a[216:219], v[100:103]\n" \
"s_add_u32 m0, s85, 81920\n" \
"ds_read_b64_tr_b16 a[116:117], v210 offset:12288\n" \
"v_mfma_f32_16x16x32_bf16 v[100:103], a[92:95], a[220:223], v[100:103]\n" \
"buffer_load_dwordx4 v246, s[12:15], s46 offen lds\n" \
"v_mfma_f32_16x16x32_bf16 v[68:71], a[200:203], a[192:195], v[68:71]\n" \
"ds_read_b128 a[88:91], v205 offset:8192\n" \
"s_add_u32 s46, s46, 0x80\n" \
"v_mfma_f32_16x16x32_bf16 v[68:71], a[204:207], a[196:199], v[68:71]\n" \
"ds_read_b128 a[92:95], v206 offset:8192\n" \
"v_mfma_f32_16x16x32_bf16 v[104:107], a[192:195], a[208:211], v[104:107]\n" \
"s_add_u32 m0, s84, 90112\n" \
"ds_read_b64_tr_b16 a[118:119], v210 offset:13312\n" \
"v_mfma_f32_16x16x32_bf16 v[104:107], a[196:199], a[212:215], v[104:107]\n" \
"global_load_lds_dwordx4 v253, s[48:49] offset:-4096\n" \
"v_mfma_f32_16x16x32_bf16 v[108:111], a[192:195], a[216:219], v[108:111]\n" \
"v_mfma_f32_16x16x32_bf16 v[108:111], a[196:199], a[220:223], v[108:111]\n" \
"s_waitcnt lgkmcnt(2)\n" \
"v_mfma_f32_16x16x32_bf16 v[32:35], a[96:99], a[64:67], v[32:35]\n" \
"ds_read_b128 a[192:195], v205 offset:12288\n" \
"v_mfma_f32_16x16x32_bf16 v[32:35], a[100:103], a[68:71], v[32:35]\n" \
"global_load_lds_dwordx4 v253, s[48:49] offset:-2048\n" \
"v_mfma_f32_16x16x32_bf16 v[40:43], a[96:99], a[72:75], v[40:43]\n" \
"ds_read_b128 a[196:199], v206 offset:12288\n" \
"v_mfma_f32_16x16x32_bf16 v[40:43], a[100:103], a[76:79], v[40:43]\n" \
"ds_read_b64_tr_b16 a[200:201], v210 offset:2048\n" \
"v_mfma_f32_16x16x32_bf16 v[0:3], a[104:107], a[72:75], v[0:3]\n" \
"ds_read_b64_tr_b16 a[202:203], v210 offset:3072\n" \
"v_mfma_f32_16x16x32_bf16 v[0:3], a[108:111], a[76:79], v[0:3]\n" \
"s_waitcnt lgkmcnt(3)\n" \
"global_load_lds_dwordx4 v253, s[48:49] offset:0\n" \
"v_mfma_f32_16x16x32_bf16 v[4:7], a[112:115], a[72:75], v[4:7]\n" \
"s_add_u32 s48, s48, 0x60000\n" \
"s_addc_u32 s49, s49, 0\n" \
"ds_read_b64_tr_b16 a[204:205], v210 offset:14336\n" \
"v_mfma_f32_16x16x32_bf16 v[4:7], a[116:119], a[76:79], v[4:7]\n" \
"ds_read_b64_tr_b16 a[206:207], v210 offset:15360\n" \
"v_mfma_f32_16x16x32_bf16 v[48:51], a[96:99], a[80:83], v[48:51]\n" \
"ds_read_b64_tr_b16 a[208:209], v209 offset:4096\n" \
"v_mfma_f32_16x16x32_bf16 v[48:51], a[100:103], a[84:87], v[48:51]\n" \
"global_load_lds_dwordx4 v253, s[50:51] offset:-3072\n" \
"v_mfma_f32_16x16x32_bf16 v[8:11], a[104:107], a[80:83], v[8:11]\n" \
"ds_read_b64_tr_b16 a[210:211], v209 offset:5120\n" \
"v_mfma_f32_16x16x32_bf16 v[8:11], a[108:111], a[84:87], v[8:11]\n" \
"ds_read_b64_tr_b16 a[212:213], v209 offset:16384\n" \
"v_mfma_f32_16x16x32_bf16 v[12:15], a[112:115], a[80:83], v[12:15]\n" \
"ds_read_b64_tr_b16 a[214:215], v209 offset:17408\n" \
"v_mfma_f32_16x16x32_bf16 v[12:15], a[116:119], a[84:87], v[12:15]\n" \
"global_load_lds_dwordx4 v253, s[50:51] offset:-1024\n" \
"v_mfma_f32_16x16x32_bf16 v[56:59], a[96:99], a[88:91], v[56:59]\n" \
"ds_read_b64_tr_b16 a[216:217], v210 offset:4096\n" \
"v_mfma_f32_16x16x32_bf16 v[56:59], a[100:103], a[92:95], v[56:59]\n" \
"ds_read_b64_tr_b16 a[218:219], v210 offset:5120\n" \
"v_mfma_f32_16x16x32_bf16 v[16:19], a[104:107], a[88:91], v[16:19]\n" \
"ds_read_b64_tr_b16 a[220:221], v210 offset:16384\n" \
"v_mfma_f32_16x16x32_bf16 v[16:19], a[108:111], a[92:95], v[16:19]\n" \
"global_load_lds_dwordx4 v253, s[50:51] offset:1024\n" \
"v_mfma_f32_16x16x32_bf16 v[20:23], a[112:115], a[88:91], v[20:23]\n" \
"s_add_u32 s50, s50, 0x60000\n" \
"s_addc_u32 s51, s51, 0\n" \
"ds_read_b64_tr_b16 a[222:223], v210 offset:17408\n" \
"v_mfma_f32_16x16x32_bf16 v[20:23], a[116:119], a[92:95], v[20:23]\n" \
"s_waitcnt lgkmcnt(3)\n" \
"v_add_u32 v207, 0x1d000, v207\n" \
"v_add_u32 v208, 0x1d000, v208\n" \
"v_mfma_f32_16x16x32_bf16 v[64:67], a[96:99], a[192:195], v[64:67]\n" \
"v_add_u32 v205, 0x1d000, v205\n" \
"v_add_u32 v206, 0x1d000, v206\n" \
"v_mfma_f32_16x16x32_bf16 v[64:67], a[100:103], a[196:199], v[64:67]\n" \
"v_add_u32 v209, 0x1d000, v209\n" \
"v_add_u32 v210, 0x1d000, v210\n" \
"v_mfma_f32_16x16x32_bf16 v[24:27], a[104:107], a[192:195], v[24:27]\n" \
"v_mfma_f32_16x16x32_bf16 v[24:27], a[108:111], a[196:199], v[24:27]\n" \
"v_mfma_f32_16x16x32_bf16 v[28:31], a[112:115], a[192:195], v[28:31]\n" \
"s_waitcnt vmcnt(12) lgkmcnt(0)\n" \
"v_mfma_f32_16x16x32_bf16 v[28:31], a[116:119], a[196:199], v[28:31]\n" \
"s_barrier\n" \
"v_mfma_f32_16x16x32_bf16 v[72:75], a[64:67], a[208:211], v[72:75]\n" \
"ds_read_b64_tr_b16 a[96:97], v209 offset:2048\n" \
"s_waitcnt vmcnt(11)\n" \
"v_mfma_f32_16x16x32_bf16 v[72:75], a[68:71], a[212:215], v[72:75]\n" \
"ds_read_b64_tr_b16 a[98:99], v209 offset:3072\n" \
"v_mfma_f32_16x16x32_bf16 v[44:47], a[200:203], a[72:75], v[44:47]\n" \
"s_add_u32 m0, s95, 0\n" \
"ds_read_b128 a[64:67], v207 offset:0\n" \
"ds_read_b64_tr_b16 a[100:101], v209 offset:14336\n" \
"v_mfma_f32_16x16x32_bf16 v[44:47], a[204:207], a[76:79], v[44:47]\n" \
"global_load_lds_dwordx4 v252, s[42:43] offset:0\n" \
"v_mfma_f32_16x16x32_bf16 v[80:83], a[72:75], a[208:211], v[80:83]\n" \
"ds_read_b128 a[68:71], v208 offset:0\n" \
"ds_read_b64_tr_b16 a[102:103], v209 offset:15360\n" \
"v_mfma_f32_16x16x32_bf16 v[80:83], a[76:79], a[212:215], v[80:83]\n" \
"ds_read_b64_tr_b16 a[104:105], v209 offset:0\n" \
"v_mfma_f32_16x16x32_bf16 v[84:87], a[72:75], a[216:219], v[84:87]\n" \
"ds_read_b64_tr_b16 a[106:107], v209 offset:1024\n" \
"v_mfma_f32_16x16x32_bf16 v[84:87], a[76:79], a[220:223], v[84:87]\n" \
"global_load_lds_dwordx4 v252, s[42:43] offset:2048\n" \
"v_mfma_f32_16x16x32_bf16 v[52:55], a[200:203], a[80:83], v[52:55]\n" \
"s_add_u32 s42, s42, 0x100\n" \
"s_addc_u32 s43, s43, 0\n" \
"ds_read_b128 a[72:75], v205 offset:0\n" \
"v_mfma_f32_16x16x32_bf16 v[52:55], a[204:207], a[84:87], v[52:55]\n" \
"ds_read_b128 a[76:79], v206 offset:0\n" \
"v_mfma_f32_16x16x32_bf16 v[88:91], a[80:83], a[208:211], v[88:91]\n" \
"ds_read_b64_tr_b16 a[108:109], v209 offset:12288\n" \
"v_mfma_f32_16x16x32_bf16 v[88:91], a[84:87], a[212:215], v[88:91]\n" \
"global_load_lds_dwordx4 v252, s[44:45] offset:1024\n" \
"v_mfma_f32_16x16x32_bf16 v[92:95], a[80:83], a[216:219], v[92:95]\n" \
"ds_read_b64_tr_b16 a[110:111], v209 offset:13312\n" \
"v_mfma_f32_16x16x32_bf16 v[92:95], a[84:87], a[220:223], v[92:95]\n" \
"ds_read_b64_tr_b16 a[112:113], v210 offset:0\n" \
"v_mfma_f32_16x16x32_bf16 v[60:63], a[200:203], a[88:91], v[60:63]\n" \
"ds_read_b128 a[80:83], v205 offset:4096\n" \
"v_mfma_f32_16x16x32_bf16 v[60:63], a[204:207], a[92:95], v[60:63]\n" \
"global_load_lds_dwordx4 v252, s[44:45] offset:3072\n" \
"v_mfma_f32_16x16x32_bf16 v[96:99], a[88:91], a[208:211], v[96:99]\n" \
"s_add_u32 s44, s44, 0x100\n" \
"s_addc_u32 s45, s45, 0\n" \
"ds_read_b128 a[84:87], v206 offset:4096\n" \
"v_mfma_f32_16x16x32_bf16 v[96:99], a[92:95], a[212:215], v[96:99]\n" \
"ds_read_b64_tr_b16 a[114:115], v210 offset:1024\n" \
"v_mfma_f32_16x16x32_bf16 v[100:103], a[88:91], a[216:219], v[100:103]\n" \
"s_add_u32 m0, s85, 16384\n" \
"ds_read_b64_tr_b16 a[116:117], v210 offset:12288\n" \
"v_mfma_f32_16x16x32_bf16 v[100:103], a[92:95], a[220:223], v[100:103]\n" \
"buffer_load_dwordx4 v246, s[12:15], s46 offen lds\n" \
"v_mfma_f32_16x16x32_bf16 v[68:71], a[200:203], a[192:195], v[68:71]\n" \
"ds_read_b128 a[88:91], v205 offset:8192\n" \
"s_add_u32 s46, s46, 0x100\n" \
"v_mfma_f32_16x16x32_bf16 v[68:71], a[204:207], a[196:199], v[68:71]\n" \
"ds_read_b128 a[92:95], v206 offset:8192\n" \
"v_mfma_f32_16x16x32_bf16 v[104:107], a[192:195], a[208:211], v[104:107]\n" \
"s_add_u32 m0, s84, 24576\n" \
"ds_read_b64_tr_b16 a[118:119], v210 offset:13312\n" \
"v_mfma_f32_16x16x32_bf16 v[104:107], a[196:199], a[212:215], v[104:107]\n" \
"global_load_lds_dwordx4 v253, s[48:49] offset:-4096\n" \
"v_mfma_f32_16x16x32_bf16 v[108:111], a[192:195], a[216:219], v[108:111]\n" \
"v_xor_b32 v226, v226, v227\n" \
"v_cmp_ne_u32 vcc, s60, v226\n" \
"v_mfma_f32_16x16x32_bf16 v[108:111], a[196:199], a[220:223], v[108:111]\n" \
"s_waitcnt lgkmcnt(2)\n" \
"v_mfma_f32_16x16x32_bf16 v[32:35], a[96:99], a[64:67], v[32:35]\n" \
"ds_read_b128 a[192:195], v205 offset:12288\n" \
"v_mfma_f32_16x16x32_bf16 v[32:35], a[100:103], a[68:71], v[32:35]\n" \
"global_load_lds_dwordx4 v253, s[48:49] offset:-2048\n" \
"v_mfma_f32_16x16x32_bf16 v[40:43], a[96:99], a[72:75], v[40:43]\n" \
"ds_read_b128 a[196:199], v206 offset:12288\n" \
"v_mfma_f32_16x16x32_bf16 v[40:43], a[100:103], a[76:79], v[40:43]\n" \
"ds_read_b64_tr_b16 a[200:201], v210 offset:2048\n" \
"v_mfma_f32_16x16x32_bf16 v[0:3], a[104:107], a[72:75], v[0:3]\n" \
"ds_read_b64_tr_b16 a[202:203], v210 offset:3072\n" \
"v_mfma_f32_16x16x32_bf16 v[0:3], a[108:111], a[76:79], v[0:3]\n" \
"s_waitcnt lgkmcnt(3)\n" \
"global_load_lds_dwordx4 v253, s[48:49] offset:0\n" \
"v_mfma_f32_16x16x32_bf16 v[4:7], a[112:115], a[72:75], v[4:7]\n" \
"s_add_u32 s48, s48, 0xc0000\n" \
"s_addc_u32 s49, s49, 0\n" \
"ds_read_b64_tr_b16 a[204:205], v210 offset:14336\n" \
"v_mfma_f32_16x16x32_bf16 v[4:7], a[116:119], a[76:79], v[4:7]\n" \
"ds_read_b64_tr_b16 a[206:207], v210 offset:15360\n" \
"v_mfma_f32_16x16x32_bf16 v[48:51], a[96:99], a[80:83], v[48:51]\n" \
"ds_read_b64_tr_b16 a[208:209], v209 offset:4096\n" \
"v_mfma_f32_16x16x32_bf16 v[48:51], a[100:103], a[84:87], v[48:51]\n" \
"global_load_lds_dwordx4 v253, s[50:51] offset:-3072\n" \
"v_mfma_f32_16x16x32_bf16 v[8:11], a[104:107], a[80:83], v[8:11]\n" \
"ds_read_b64_tr_b16 a[210:211], v209 offset:5120\n" \
"v_mfma_f32_16x16x32_bf16 v[8:11], a[108:111], a[84:87], v[8:11]\n" \
"ds_read_b64_tr_b16 a[212:213], v209 offset:16384\n" \
"v_mfma_f32_16x16x32_bf16 v[12:15], a[112:115], a[80:83], v[12:15]\n" \
"ds_read_b64_tr_b16 a[214:215], v209 offset:17408\n" \
"v_mfma_f32_16x16x32_bf16 v[12:15], a[116:119], a[84:87], v[12:15]\n" \
"global_load_lds_dwordx4 v253, s[50:51] offset:-1024\n" \
"v_mfma_f32_16x16x32_bf16 v[56:59], a[96:99], a[88:91], v[56:59]\n" \
"ds_read_b64_tr_b16 a[216:217], v210 offset:4096\n" \
"v_mfma_f32_16x16x32_bf16 v[56:59], a[100:103], a[92:95], v[56:59]\n" \
"ds_read_b64_tr_b16 a[218:219], v210 offset:5120\n" \
"v_mfma_f32_16x16x32_bf16 v[16:19], a[104:107], a[88:91], v[16:19]\n" \
"ds_read_b64_tr_b16 a[220:221], v210 offset:16384\n" \
"v_mfma_f32_16x16x32_bf16 v[16:19], a[108:111], a[92:95], v[16:19]\n" \
"global_load_lds_dwordx4 v253, s[50:51] offset:1024\n" \
"v_mfma_f32_16x16x32_bf16 v[20:23], a[112:115], a[88:91], v[20:23]\n" \
"s_add_u32 s50, s50, 0xc0000\n" \
"s_addc_u32 s51, s51, 0\n" \
"ds_read_b64_tr_b16 a[222:223], v210 offset:17408\n" \
"v_mfma_f32_16x16x32_bf16 v[20:23], a[116:119], a[92:95], v[20:23]\n" \
"s_waitcnt lgkmcnt(3)\n" \
"v_add_u32 v207, 0xffff3000, v207\n" \
"v_add_u32 v208, 0xffff3000, v208\n" \
"v_mfma_f32_16x16x32_bf16 v[64:67], a[96:99], a[192:195], v[64:67]\n" \
"v_add_u32 v205, 0xffff3000, v205\n" \
"v_add_u32 v206, 0xffff3000, v206\n" \
"v_mfma_f32_16x16x32_bf16 v[64:67], a[100:103], a[196:199], v[64:67]\n" \
"v_add_u32 v209, 0xffff3000, v209\n" \
"v_add_u32 v210, 0xffff3000, v210\n" \
"v_mfma_f32_16x16x32_bf16 v[24:27], a[104:107], a[192:195], v[24:27]\n" \
"s_and_b32 vcc_hi, vcc_lo, -1\n" \
"s_cbranch_scc1 Lpl3_%=\n" \
"Lpr3_%=:\n" \
"v_mfma_f32_16x16x32_bf16 v[24:27], a[108:111], a[196:199], v[24:27]\n" \
"v_readfirstlane_b32 s71, v227\n" \
"s_and_b32 s70, %1, 7\n" \
"v_mfma_f32_16x16x32_bf16 v[28:31], a[112:115], a[192:195], v[28:31]\n" \
"s_waitcnt vmcnt(11) lgkmcnt(0)\n" \
"s_lshl_b32 s78, s70, 2\n" \
"v_mfma_f32_16x16x32_bf16 v[28:31], a[116:119], a[196:199], v[28:31]\n" \
"s_barrier\n" \
"v_mfma_f32_16x16x32_bf16 v[72:75], a[64:67], a[208:211], v[72:75]\n" \
"ds_read_b64_tr_b16 a[96:97], v209 offset:2048\n" \
"v_subrev_u32 v227, s71, v227\n" \
"v_mfma_f32_16x16x32_bf16 v[72:75], a[68:71], a[212:215], v[72:75]\n" \
"ds_read_b64_tr_b16 a[98:99], v209 offset:3072\n" \
"v_mfma_f32_16x16x32_bf16 v[44:47], a[200:203], a[72:75], v[44:47]\n" \
"s_add_u32 m0, s95, 118784\n" \
"ds_read_b128 a[64:67], v207 offset:0\n" \
"ds_read_b64_tr_b16 a[100:101], v209 offset:14336\n" \
"v_mfma_f32_16x16x32_bf16 v[44:47], a[204:207], a[76:79], v[44:47]\n" \
"global_load_lds_dwordx4 v252, s[42:43] offset:0\n" \
"v_add_u32_dpp v227, v227, v227 quad_perm:[1,0,3,2] row_mask:0xf bank_mask:0xf\n" \
"v_mfma_f32_16x16x32_bf16 v[80:83], a[72:75], a[208:211], v[80:83]\n" \
"ds_read_b128 a[68:71], v208 offset:0\n" \
"ds_read_b64_tr_b16 a[102:103], v209 offset:15360\n" \
"v_mfma_f32_16x16x32_bf16 v[80:83], a[76:79], a[212:215], v[80:83]\n" \
"ds_read_b64_tr_b16 a[104:105], v209 offset:0\n" \
"v_add_u32_dpp v227, v227, v227 quad_perm:[2,3,0,1] row_mask:0xf bank_mask:0xf\n" \
"v_mfma_f32_16x16x32_bf16 v[84:87], a[72:75], a[216:219], v[84:87]\n" \
"ds_read_b64_tr_b16 a[106:107], v209 offset:1024\n" \
"v_readlane_b32 s71, v227, s78\n" \
"v_mfma_f32_16x16x32_bf16 v[84:87], a[76:79], a[220:223], v[84:87]\n" \
"global_load_lds_dwordx4 v252, s[42:43] offset:2048\n" \
"v_lshrrev_b32 v226, 6, v248\n" \
"v_mfma_f32_16x16x32_bf16 v[52:55], a[200:203], a[80:83], v[52:55]\n" \
"s_add_u32 s42, s42, 0x80\n" \
"s_addc_u32 s43, s43, 0\n" \
"ds_read_b128 a[72:75], v205 offset:0\n" \
"v_mfma_f32_16x16x32_bf16 v[52:55], a[204:207], a[84:87], v[52:55]\n" \
"ds_read_b128 a[76:79], v206 offset:0\n" \
"v_subrev_u32 v227, s71, v227\n" \
"v_mfma_f32_16x16x32_bf16 v[88:91], a[80:83], a[208:211], v[88:91]\n" \
"ds_read_b64_tr_b16 a[108:109], v209 offset:12288\n" \
"v_lshl_add_u32 v227, v227, 3, v226\n" \
"v_mfma_f32_16x16x32_bf16 v[88:91], a[84:87], a[212:215], v[88:91]\n" \
"global_load_lds_dwordx4 v252, s[44:45] offset:1024\n" \
"v_cmp_gt_i32 vcc, s70, v227\n" \
"v_mfma_f32_16x16x32_bf16 v[92:95], a[80:83], a[216:219], v[92:95]\n" \
"ds_read_b64_tr_b16 a[110:111], v209 offset:13312\n" \
"v_mfma_f32_16x16x32_bf16 v[92:95], a[84:87], a[220:223], v[92:95]\n" \
"s_bcnt1_i32_b32 s70, vcc_lo\n" \
"s_lshr_b32 s70, s70, 2\n" \
"ds_read_b64_tr_b16 a[112:113], v210 offset:0\n" \
"v_mfma_f32_16x16x32_bf16 v[60:63], a[200:203], a[88:91], v[60:63]\n" \
"s_lshr_b32 s78, s70, 2\n" \
"ds_read_b128 a[80:83], v205 offset:4096\n" \
"s_mul_i32 s71, s78, 7\n" \
"v_mfma_f32_16x16x32_bf16 v[60:63], a[204:207], a[92:95], v[60:63]\n" \
"s_xor_b32 s70, s70, s71\n" \
"global_load_lds_dwordx4 v252, s[44:45] offset:3072\n" \
"v_mfma_f32_16x16x32_bf16 v[96:99], a[88:91], a[208:211], v[96:99]\n" \
"s_add_u32 s44, s44, 0x80\n" \
"s_addc_u32 s45, s45, 0\n" \
"ds_read_b128 a[84:87], v206 offset:4096\n" \
"v_mfma_f32_16x16x32_bf16 v[96:99], a[92:95], a[212:215], v[96:99]\n" \
"s_xor_b32 s78, s78, 1\n" \
"s_lshl_b32 s65, s70, 2\n" \
"ds_read_b64_tr_b16 a[114:115], v210 offset:1024\n" \
"v_mfma_f32_16x16x32_bf16 v[100:103], a[88:91], a[216:219], v[100:103]\n" \
"s_add_u32 m0, s85, 135168\n" \
"s_lshr_b32 s64, 0x8654, s65\n" \
"ds_read_b64_tr_b16 a[116:117], v210 offset:12288\n" \
"v_mfma_f32_16x16x32_bf16 v[100:103], a[92:95], a[220:223], v[100:103]\n" \
"buffer_load_dwordx4 v246, s[12:15], s46 offen lds\n" \
"v_mfma_f32_16x16x32_bf16 v[68:71], a[200:203], a[192:195], v[68:71]\n" \
"s_add_u32 s46, s46, 0x80\n" \
"ds_read_b128 a[88:91], v205 offset:8192\n" \
"s_and_b32 s64, s64, 15\n" \
"v_mfma_f32_16x16x32_bf16 v[68:71], a[204:207], a[196:199], v[68:71]\n" \
"s_add_u32 s64, s64, 32\n" \
"ds_read_b128 a[92:95], v206 offset:8192\n" \
"s_lshr_b32 s70, %1, 6\n" \
"v_mfma_f32_16x16x32_bf16 v[104:107], a[192:195], a[208:211], v[104:107]\n" \
"s_add_u32 m0, s84, 143360\n" \
"s_add_u32 s65, s65, s70\n" \
"ds_read_b64_tr_b16 a[118:119], v210 offset:13312\n" \
"v_mfma_f32_16x16x32_bf16 v[104:107], a[196:199], a[212:215], v[104:107]\n" \
"s_and_b32 s30, %1, 1\n" \
"global_load_lds_dwordx4 v253, s[48:49] offset:-4096\n" \
"v_mfma_f32_16x16x32_bf16 v[108:111], a[192:195], a[216:219], v[108:111]\n" \
"s_lshl_b32 s30, s30, 3\n" \
"s_add_u32 s30, s30, s63\n" \
"s_lshl_b32 s30, s30, 2\n" \
"v_mfma_f32_16x16x32_bf16 v[108:111], a[196:199], a[220:223], v[108:111]\n" \
"s_waitcnt lgkmcnt(2)\n" \
"s_lshl_b32 s70, s88, 7\n" \
"s_add_u32 s30, s30, s70\n" \
"v_mfma_f32_16x16x32_bf16 v[32:35], a[96:99], a[64:67], v[32:35]\n" \
"s_add_u32 s30, s30, 65591296\n" \
"ds_read_b128 a[192:195], v205 offset:12288\n" \
"v_lshrrev_b32 v221, 2, v248\n" \
"v_mfma_f32_16x16x32_bf16 v[32:35], a[100:103], a[68:71], v[32:35]\n" \
"s_lshl_b32 s81, s65, 7\n" \
"global_load_lds_dwordx4 v253, s[48:49] offset:-2048\n" \
"v_mfma_f32_16x16x32_bf16 v[40:43], a[96:99], a[72:75], v[40:43]\n" \
"s_add_u32 s82, s81, 65591296\n" \
"ds_read_b128 a[196:199], v206 offset:12288\n" \
"s_add_u32 s81, s81, 65589248\n" \
"v_mfma_f32_16x16x32_bf16 v[40:43], a[100:103], a[76:79], v[40:43]\n" \
"s_mov_b64 s[52:53], s[26:27]\n" \
"s_and_b32 s53, s53, 0xffff\n" \
"ds_read_b64_tr_b16 a[200:201], v210 offset:2048\n" \
"v_mfma_f32_16x16x32_bf16 v[0:3], a[104:107], a[72:75], v[0:3]\n" \
"s_mov_b32 s54, 8388608\n" \
"s_mov_b32 s55, 0x20000\n" \
"ds_read_b64_tr_b16 a[202:203], v210 offset:3072\n" \
"v_mfma_f32_16x16x32_bf16 v[0:3], a[108:111], a[76:79], v[0:3]\n" \
"s_waitcnt lgkmcnt(3)\n" \
"global_load_lds_dwordx4 v253, s[48:49] offset:0\n" \
"s_lshr_b32 s83, s63, 1\n" \
"v_mfma_f32_16x16x32_bf16 v[4:7], a[112:115], a[72:75], v[4:7]\n" \
"s_add_u32 s48, s48, 0x60000\n" \
"s_addc_u32 s49, s49, 0\n" \
"ds_read_b64_tr_b16 a[204:205], v210 offset:14336\n" \
"v_mfma_f32_16x16x32_bf16 v[4:7], a[116:119], a[76:79], v[4:7]\n" \
"s_add_u32 s83, s83, 2\n" \
"s_lshr_b32 s70, s18, 8\n" \
"ds_read_b64_tr_b16 a[206:207], v210 offset:15360\n" \
"v_mfma_f32_16x16x32_bf16 v[48:51], a[96:99], a[80:83], v[48:51]\n" \
"s_sub_u32 s83, s83, s70\n" \
"s_and_b32 s83, s83, 3\n" \
"ds_read_b64_tr_b16 a[208:209], v209 offset:4096\n" \
"v_mfma_f32_16x16x32_bf16 v[48:51], a[100:103], a[84:87], v[48:51]\n" \
"s_and_b32 s70, s63, 1\n" \
"global_load_lds_dwordx4 v253, s[50:51] offset:-3072\n" \
"v_mfma_f32_16x16x32_bf16 v[8:11], a[104:107], a[80:83], v[8:11]\n" \
"s_lshl_b32 s70, s70, 2\n" \
"s_or_b32 s83, s83, s70\n" \
"ds_read_b64_tr_b16 a[210:211], v209 offset:5120\n" \
"v_mfma_f32_16x16x32_bf16 v[8:11], a[108:111], a[84:87], v[8:11]\n" \
"s_mul_i32 s75, s78, s64\n" \
"ds_read_b64_tr_b16 a[212:213], v209 offset:16384\n" \
"v_lshrrev_b32 v228, 4, v248\n" \
"v_mfma_f32_16x16x32_bf16 v[12:15], a[112:115], a[80:83], v[12:15]\n" \
"s_lshl_b32 s70, s65, 2\n" \
"s_add_u32 s70, s70, s61\n" \
"ds_read_b64_tr_b16 a[214:215], v209 offset:17408\n" \
"v_mfma_f32_16x16x32_bf16 v[12:15], a[116:119], a[84:87], v[12:15]\n" \
"s_mul_i32 s73, s70, 69632\n" \
"global_load_lds_dwordx4 v253, s[50:51] offset:-1024\n" \
"v_mfma_f32_16x16x32_bf16 v[56:59], a[96:99], a[88:91], v[56:59]\n" \
"s_add_u32 s73, s73, 52740096\n" \
"s_add_u32 s73, s73, s28\n" \
"ds_read_b64_tr_b16 a[216:217], v210 offset:4096\n" \
"v_mfma_f32_16x16x32_bf16 v[56:59], a[100:103], a[92:95], v[56:59]\n" \
"s_mul_i32 s70, s70, 266240\n" \
"s_lshl_b32 s71, s75, 12\n" \
"ds_read_b64_tr_b16 a[218:219], v210 offset:5120\n" \
"v_mfma_f32_16x16x32_bf16 v[16:19], a[104:107], a[88:91], v[16:19]\n" \
"s_add_u32 s70, s70, s71\n" \
"s_add_u32 s70, s70, 35700736\n" \
"ds_read_b64_tr_b16 a[220:221], v210 offset:16384\n" \
"v_mfma_f32_16x16x32_bf16 v[16:19], a[108:111], a[92:95], v[16:19]\n" \
"s_add_u32 s36, s28, s70\n" \
"global_load_lds_dwordx4 v253, s[50:51] offset:1024\n" \
"v_mfma_f32_16x16x32_bf16 v[20:23], a[112:115], a[88:91], v[20:23]\n" \
"s_add_u32 s50, s50, 0x60000\n" \
"s_addc_u32 s51, s51, 0\n" \
"ds_read_b64_tr_b16 a[222:223], v210 offset:17408\n" \
"v_mfma_f32_16x16x32_bf16 v[20:23], a[116:119], a[92:95], v[20:23]\n" \
"s_waitcnt lgkmcnt(3)\n" \
"v_add_u32 v207, 0xffff0000, v207\n" \
"v_add_u32 v208, 0xffff0000, v208\n" \
"v_mfma_f32_16x16x32_bf16 v[64:67], a[96:99], a[192:195], v[64:67]\n" \
"s_mov_b32 s37, s29\n" \
"v_add_u32 v205, 0xffff0000, v205\n" \
"v_add_u32 v206, 0xffff0000, v206\n" \
"v_mfma_f32_16x16x32_bf16 v[64:67], a[100:103], a[196:199], v[64:67]\n" \
"s_lshr_b32 s74, s61, 1\n" \
"v_add_u32 v209, 0xffff0000, v209\n" \
"v_add_u32 v210, 0xffff0000, v210\n" \
"v_mfma_f32_16x16x32_bf16 v[24:27], a[104:107], a[192:195], v[24:27]\n" \
"s_lshl_b32 s74, s74, 16\n" \
"s_and_b32 s70, s61, 1\n" \
"s_lshl_b32 s70, s70, 14\n" \
"v_mfma_f32_16x16x32_bf16 v[24:27], a[108:111], a[196:199], v[24:27]\n" \
"s_add_u32 s74, s74, s70\n" \
"s_lshl_b32 s71, s83, 8\n" \
"s_add_u32 s74, s74, s71\n" \
"v_mfma_f32_16x16x32_bf16 v[28:31], a[112:115], a[192:195], v[28:31]\n" \
"s_waitcnt vmcnt(11) lgkmcnt(0)\n" \
"s_add_u32 s40, s24, s74\n" \
"s_addc_u32 s41, s25, 0\n" \
"v_mfma_f32_16x16x32_bf16 v[28:31], a[116:119], a[196:199], v[28:31]\n" \
"s_barrier\n" \
"v_mfma_f32_16x16x32_bf16 v[72:75], a[64:67], a[208:211], v[72:75]\n" \
"Lpg10433_%=:\n" \
"s_cbranch_execz Lpg12620_%=\n" \
"s_lshl_b32 s70, s75, 17\n" \
"s_add_u32 s70, s70, s74\n" \
"ds_read_b64_tr_b16 a[96:97], v209 offset:2048\n" \
"v_mfma_f32_16x16x32_bf16 v[72:75], a[68:71], a[212:215], v[72:75]\n" \
"s_add_u32 s38, s18, s70\n" \
"s_addc_u32 s39, s19, 0\n" \
"ds_read_b64_tr_b16 a[98:99], v209 offset:3072\n" \
"v_mfma_f32_16x16x32_bf16 v[44:47], a[200:203], a[72:75], v[44:47]\n" \
"s_add_u32 m0, s95, 65536\n" \
"ds_read_b128 a[64:67], v207 offset:0\n" \
"ds_read_b64_tr_b16 a[100:101], v209 offset:14336\n" \
"v_mfma_f32_16x16x32_bf16 v[44:47], a[204:207], a[76:79], v[44:47]\n" \
"global_load_lds_dwordx4 v252, s[42:43] offset:0\n" \
"v_bfe_u32 v229, v228, 2, 3\n" \
"v_mfma_f32_16x16x32_bf16 v[80:83], a[72:75], a[208:211], v[80:83]\n" \
"ds_read_b128 a[68:71], v208 offset:0\n" \
"ds_read_b64_tr_b16 a[102:103], v209 offset:15360\n" \
"v_mfma_f32_16x16x32_bf16 v[80:83], a[76:79], a[212:215], v[80:83]\n" \
"ds_read_b64_tr_b16 a[104:105], v209 offset:0\n" \
"v_lshlrev_b32 v229, 11, v229\n" \
"v_mfma_f32_16x16x32_bf16 v[84:87], a[72:75], a[216:219], v[84:87]\n" \
"ds_read_b64_tr_b16 a[106:107], v209 offset:1024\n" \
"v_lshrrev_b32 v230, 5, v228\n" \
"v_mfma_f32_16x16x32_bf16 v[84:87], a[76:79], a[220:223], v[84:87]\n" \
"global_load_lds_dwordx4 v252, s[42:43] offset:2048\n" \
"v_lshl_add_u32 v229, v230, 6, v229\n" \
"v_mfma_f32_16x16x32_bf16 v[52:55], a[200:203], a[80:83], v[52:55]\n" \
"s_add_u32 s42, s42, 0x80\n" \
"s_addc_u32 s43, s43, 0\n" \
"ds_read_b128 a[72:75], v205 offset:0\n" \
"v_mfma_f32_16x16x32_bf16 v[52:55], a[204:207], a[84:87], v[52:55]\n" \
"ds_read_b128 a[76:79], v206 offset:0\n" \
"v_and_b32 v230, 3, v228\n" \
"v_mfma_f32_16x16x32_bf16 v[88:91], a[80:83], a[208:211], v[88:91]\n" \
"ds_read_b64_tr_b16 a[108:109], v209 offset:12288\n" \
"v_bfe_u32 v231, v228, 4, 1\n" \
"v_mfma_f32_16x16x32_bf16 v[88:91], a[84:87], a[212:215], v[88:91]\n" \
"global_load_lds_dwordx4 v252, s[44:45] offset:1024\n" \
"v_lshlrev_b32 v231, 1, v231\n" \
"v_mfma_f32_16x16x32_bf16 v[92:95], a[80:83], a[216:219], v[92:95]\n" \
"ds_read_b64_tr_b16 a[110:111], v209 offset:13312\n" \
"v_xor_b32 v230, v230, v231\n" \
"v_mfma_f32_16x16x32_bf16 v[92:95], a[84:87], a[220:223], v[92:95]\n" \
"ds_read_b64_tr_b16 a[112:113], v210 offset:0\n" \
"v_lshl_add_u32 v229, v230, 4, v229\n" \
"v_mfma_f32_16x16x32_bf16 v[60:63], a[200:203], a[88:91], v[60:63]\n" \
"ds_read_b128 a[80:83], v205 offset:4096\n" \
"v_add_u32 v216, 4096, v229\n" \
"v_mfma_f32_16x16x32_bf16 v[60:63], a[204:207], a[92:95], v[60:63]\n" \
"global_load_lds_dwordx4 v252, s[44:45] offset:3072\n" \
"v_add_u32 v217, 3200, v229\n" \
"v_mfma_f32_16x16x32_bf16 v[96:99], a[88:91], a[208:211], v[96:99]\n" \
"s_add_u32 s44, s44, 0x80\n" \
"s_addc_u32 s45, s45, 0\n" \
"ds_read_b128 a[84:87], v206 offset:4096\n" \
"v_mfma_f32_16x16x32_bf16 v[96:99], a[92:95], a[212:215], v[96:99]\n" \
"ds_read_b64_tr_b16 a[114:115], v210 offset:1024\n" \
"v_add_u32 v218, 34816, v229\n" \
"v_mfma_f32_16x16x32_bf16 v[100:103], a[88:91], a[216:219], v[100:103]\n" \
"s_add_u32 m0, s85, 81920\n" \
"ds_read_b64_tr_b16 a[116:117], v210 offset:12288\n" \
"v_add_u32 v219, 33920, v229\n" \
"v_mfma_f32_16x16x32_bf16 v[100:103], a[92:95], a[220:223], v[100:103]\n" \
"buffer_load_dwordx4 v246, s[12:15], s46 offen lds\n" \
"v_mfma_f32_16x16x32_bf16 v[68:71], a[200:203], a[192:195], v[68:71]\n" \
"s_add_u32 s46, s46, 0x80\n" \
"ds_read_b128 a[88:91], v205 offset:8192\n" \
"s_mov_b32 s75, 65536\n" \
"v_mfma_f32_16x16x32_bf16 v[68:71], a[204:207], a[196:199], v[68:71]\n" \
"s_lshr_b32 s70, s61, 1\n" \
"ds_read_b128 a[92:95], v206 offset:8192\n" \
"s_lshl_b32 s71, s70, 13\n" \
"v_mfma_f32_16x16x32_bf16 v[104:107], a[192:195], a[208:211], v[104:107]\n" \
"s_add_u32 m0, s84, 90112\n" \
"ds_read_b64_tr_b16 a[118:119], v210 offset:13312\n" \
"v_add_u32 v211, s71, v249\n" \
"v_mfma_f32_16x16x32_bf16 v[104:107], a[196:199], a[212:215], v[104:107]\n" \
"s_lshl_b32 s71, s65, 1\n" \
"global_load_lds_dwordx4 v253, s[48:49] offset:-4096\n" \
"v_mfma_f32_16x16x32_bf16 v[108:111], a[192:195], a[216:219], v[108:111]\n" \
"s_add_u32 s70, s70, s71\n" \
"s_lshl_b32 s77, s70, 18\n" \
"s_lshl_b32 s71, s83, 8\n" \
"v_mfma_f32_16x16x32_bf16 v[108:111], a[196:199], a[220:223], v[108:111]\n" \
"s_waitcnt lgkmcnt(2)\n" \
"s_add_u32 s77, s77, s71\n" \
"s_lshl_b32 s71, s78, 11\n" \
"v_mfma_f32_16x16x32_bf16 v[32:35], a[96:99], a[64:67], v[32:35]\n" \
"s_add_u32 s77, s77, s71\n" \
"ds_read_b128 a[192:195], v205 offset:12288\n" \
"s_and_b32 s70, s61, 1\n" \
"v_mfma_f32_16x16x32_bf16 v[32:35], a[100:103], a[68:71], v[32:35]\n" \
"s_lshl_b32 s71, s70, 7\n" \
"global_load_lds_dwordx4 v253, s[48:49] offset:-2048\n" \
"v_mfma_f32_16x16x32_bf16 v[40:43], a[96:99], a[72:75], v[40:43]\n" \
"s_add_u32 s77, s77, s71\n" \
"ds_read_b128 a[196:199], v206 offset:12288\n" \
"v_and_b32 v229, 3, v228\n" \
"v_mfma_f32_16x16x32_bf16 v[40:43], a[100:103], a[76:79], v[40:43]\n" \
"ds_read_b64_tr_b16 a[200:201], v210 offset:2048\n" \
"v_lshlrev_b32 v229, 3, v229\n" \
"v_mfma_f32_16x16x32_bf16 v[0:3], a[104:107], a[72:75], v[0:3]\n" \
"ds_read_b64_tr_b16 a[202:203], v210 offset:3072\n" \
"v_bfe_u32 v230, v228, 2, 2\n" \
"v_mfma_f32_16x16x32_bf16 v[0:3], a[108:111], a[76:79], v[0:3]\n" \
"s_waitcnt lgkmcnt(3)\n" \
"global_load_lds_dwordx4 v253, s[48:49] offset:0\n" \
"v_lshl_add_u32 v229, v230, 6, v229\n" \
"v_mfma_f32_16x16x32_bf16 v[4:7], a[112:115], a[72:75], v[4:7]\n" \
"s_add_u32 s48, s48, 0x60000\n" \
"s_addc_u32 s49, s49, 0\n" \
"ds_read_b64_tr_b16 a[204:205], v210 offset:14336\n" \
"v_mfma_f32_16x16x32_bf16 v[4:7], a[116:119], a[76:79], v[4:7]\n" \
"ds_read_b64_tr_b16 a[206:207], v210 offset:15360\n" \
"v_bfe_u32 v230, v228, 4, 1\n" \
"v_mfma_f32_16x16x32_bf16 v[48:51], a[96:99], a[80:83], v[48:51]\n" \
"ds_read_b64_tr_b16 a[208:209], v209 offset:4096\n" \
"v_lshl_add_u32 v229, v230, 5, v229\n" \
"v_mfma_f32_16x16x32_bf16 v[48:51], a[100:103], a[84:87], v[48:51]\n" \
"global_load_lds_dwordx4 v253, s[50:51] offset:-3072\n" \
"v_lshl_add_u32 v229, v230, 8, v229\n" \
"v_mfma_f32_16x16x32_bf16 v[8:11], a[104:107], a[80:83], v[8:11]\n" \
"ds_read_b64_tr_b16 a[210:211], v209 offset:5120\n" \
"v_lshrrev_b32 v230, 5, v228\n" \
"v_mfma_f32_16x16x32_bf16 v[8:11], a[108:111], a[84:87], v[8:11]\n" \
"ds_read_b64_tr_b16 a[212:213], v209 offset:16384\n" \
"v_lshl_add_u32 v229, v230, 12, v229\n" \
"v_mfma_f32_16x16x32_bf16 v[12:15], a[112:115], a[80:83], v[12:15]\n" \
"s_lshl_b32 s71, s70, 10\n" \
"ds_read_b64_tr_b16 a[214:215], v209 offset:17408\n" \
"v_add_u32 v212, s71, v229\n" \
"v_mfma_f32_16x16x32_bf16 v[12:15], a[116:119], a[84:87], v[12:15]\n" \
"s_lshl_b32 s70, s65, 3\n" \
"global_load_lds_dwordx4 v253, s[50:51] offset:-1024\n" \
"v_mfma_f32_16x16x32_bf16 v[56:59], a[96:99], a[88:91], v[56:59]\n" \
"s_add_u32 s70, s70, s83\n" \
"s_lshl_b32 s76, s70, 16\n" \
"ds_read_b64_tr_b16 a[216:217], v210 offset:4096\n" \
"v_mfma_f32_16x16x32_bf16 v[56:59], a[100:103], a[92:95], v[56:59]\n" \
"s_lshl_b32 s71, s61, 14\n" \
"s_add_u32 s76, s76, s71\n" \
"ds_read_b64_tr_b16 a[218:219], v210 offset:5120\n" \
"v_mfma_f32_16x16x32_bf16 v[16:19], a[104:107], a[88:91], v[16:19]\n" \
"s_add_u32 s76, s76, 57196544\n" \
"s_lshl_b32 s79, s70, 7\n" \
"ds_read_b64_tr_b16 a[220:221], v210 offset:16384\n" \
"v_mfma_f32_16x16x32_bf16 v[16:19], a[108:111], a[92:95], v[16:19]\n" \
"s_add_u32 s79, s79, 65601536\n" \
"global_load_lds_dwordx4 v253, s[50:51] offset:1024\n" \
"v_mfma_f32_16x16x32_bf16 v[20:23], a[112:115], a[88:91], v[20:23]\n" \
"s_add_u32 s50, s50, 0x60000\n" \
"s_addc_u32 s51, s51, 0\n" \
"ds_read_b64_tr_b16 a[222:223], v210 offset:17408\n" \
"v_mfma_f32_16x16x32_bf16 v[20:23], a[116:119], a[92:95], v[20:23]\n" \
"s_waitcnt lgkmcnt(3)\n" \
"v_add_u32 v207, 0x1d000, v207\n" \
"v_add_u32 v208, 0x1d000, v208\n" \
"v_mfma_f32_16x16x32_bf16 v[64:67], a[96:99], a[192:195], v[64:67]\n" \
"s_lshr_b32 s70, s64, 1\n" \
"v_add_u32 v205, 0x1d000, v205\n" \
"v_add_u32 v206, 0x1d000, v206\n" \
"v_mfma_f32_16x16x32_bf16 v[64:67], a[100:103], a[196:199], v[64:67]\n" \
"s_lshl_b32 s70, -1, s70\n" \
"v_add_u32 v209, 0x1d000, v209\n" \
"v_add_u32 v210, 0x1d000, v210\n" \
"v_mfma_f32_16x16x32_bf16 v[24:27], a[104:107], a[192:195], v[24:27]\n" \
"s_add_u32 s71, s64, 1\n" \
"s_lshr_b32 s71, s71, 1\n" \
"s_bfm_b32 s71, s71, 0\n" \
"v_mfma_f32_16x16x32_bf16 v[24:27], a[108:111], a[196:199], v[24:27]\n" \
"s_cmp_eq_u32 s78, 0\n" \
"s_cselect_b32 s80, s71, s70\n" \
"s_mov_b32 s16, 0xff000000\n" \
"v_mfma_f32_16x16x32_bf16 v[28:31], a[112:115], a[192:195], v[28:31]\n" \
"s_waitcnt vmcnt(11) lgkmcnt(0)\n" \
"s_mov_b32 s17, 0\n" \
"v_add_u32 v222, 0x1c90080, v250\n" \
"v_mfma_f32_16x16x32_bf16 v[28:31], a[116:119], a[196:199], v[28:31]\n" \
"s_barrier\n" \
"v_mfma_f32_16x16x32_bf16 v[72:75], a[64:67], a[208:211], v[72:75]\n" \
"ds_read_b64_tr_b16 a[96:97], v209 offset:2048\n" \
"v_cndmask_b32 v222, v250, v222, s[16:17]\n" \
"v_mfma_f32_16x16x32_bf16 v[72:75], a[68:71], a[212:215], v[72:75]\n" \
"s_mov_b32 s74, 98304\n" \
"ds_read_b64_tr_b16 a[98:99], v209 offset:3072\n" \
"v_mfma_f32_16x16x32_bf16 v[44:47], a[200:203], a[72:75], v[44:47]\n" \
"s_add_u32 m0, s95, 0\n" \
"ds_read_b128 a[64:67], v207 offset:0\n" \
"ds_read_b64_tr_b16 a[100:101], v209 offset:14336\n" \
"v_mfma_f32_16x16x32_bf16 v[44:47], a[204:207], a[76:79], v[44:47]\n" \
"global_load_lds_dwordx4 v252, s[42:43] offset:0\n" \
"v_mfma_f32_16x16x32_bf16 v[80:83], a[72:75], a[208:211], v[80:83]\n" \
"ds_read_b128 a[68:71], v208 offset:0\n" \
"ds_read_b64_tr_b16 a[102:103], v209 offset:15360\n" \
"v_mfma_f32_16x16x32_bf16 v[80:83], a[76:79], a[212:215], v[80:83]\n" \
"s_sub_u32 s74, s74, 32768\n" \
"s_cselect_b32 s74, 131072, s74\n" \
"ds_read_b64_tr_b16 a[104:105], v209 offset:0\n" \
"v_mfma_f32_16x16x32_bf16 v[84:87], a[72:75], a[216:219], v[84:87]\n" \
"ds_read_b64_tr_b16 a[106:107], v209 offset:1024\n" \
"v_add_u32 v213, s74, v211\n" \
"v_mfma_f32_16x16x32_bf16 v[84:87], a[76:79], a[220:223], v[84:87]\n" \
"global_load_lds_dwordx4 v252, s[42:43] offset:2048\n" \
"v_add_u32 v214, s74, v212\n" \
"v_mfma_f32_16x16x32_bf16 v[52:55], a[200:203], a[80:83], v[52:55]\n" \
"s_add_u32 s42, s42, 0x80\n" \
"s_addc_u32 s43, s43, 0\n" \
"ds_read_b128 a[72:75], v205 offset:0\n" \
"v_mfma_f32_16x16x32_bf16 v[52:55], a[204:207], a[84:87], v[52:55]\n" \
"ds_read_b128 a[76:79], v206 offset:0\n" \
"v_xor_b32 v215, 32, v214\n" \
"v_mfma_f32_16x16x32_bf16 v[88:91], a[80:83], a[208:211], v[88:91]\n" \
"s_and_b32 s89, s61, 1\n" \
"s_lshl_b32 s89, s89, 11\n" \
"ds_read_b64_tr_b16 a[108:109], v209 offset:12288\n" \
"v_mfma_f32_16x16x32_bf16 v[88:91], a[84:87], a[212:215], v[88:91]\n" \
"s_add_u32 s87, s87, s89\n" \
"global_load_lds_dwordx4 v252, s[44:45] offset:1024\n" \
"v_mfma_f32_16x16x32_bf16 v[92:95], a[80:83], a[216:219], v[92:95]\n" \
"s_add_u32 s87, s87, 52740096\n" \
"ds_read_b64_tr_b16 a[110:111], v209 offset:13312\n" \
"v_mfma_f32_16x16x32_bf16 v[92:95], a[84:87], a[220:223], v[92:95]\n" \
"s_cmp_eq_u32 s88, 0\n" \
"s_cselect_b32 s86, -1, 0\n" \
"ds_read_b64_tr_b16 a[112:113], v210 offset:0\n" \
"v_mfma_f32_16x16x32_bf16 v[60:63], a[200:203], a[88:91], v[60:63]\n" \
"s_mul_i32 s89, s88, 278528\n" \
"ds_read_b128 a[80:83], v205 offset:4096\n" \
"s_add_u32 s87, s87, s89\n" \
"v_mfma_f32_16x16x32_bf16 v[60:63], a[204:207], a[92:95], v[60:63]\n" \
"s_lshr_b32 s89, s61, 1\n" \
"global_load_lds_dwordx4 v252, s[44:45] offset:3072\n" \
"v_mfma_f32_16x16x32_bf16 v[96:99], a[88:91], a[208:211], v[96:99]\n" \
"s_add_u32 s44, s44, 0x80\n" \
"s_addc_u32 s45, s45, 0\n" \
"ds_read_b128 a[84:87], v206 offset:4096\n" \
"v_mfma_f32_16x16x32_bf16 v[96:99], a[92:95], a[212:215], v[96:99]\n" \
"s_lshl_b32 s89, s89, 8\n" \
"s_add_u32 s87, s87, s89\n" \
"ds_read_b64_tr_b16 a[114:115], v210 offset:1024\n" \
"v_mfma_f32_16x16x32_bf16 v[100:103], a[88:91], a[216:219], v[100:103]\n" \
"s_add_u32 m0, s85, 16384\n" \
"s_lshl_b32 s89, s61, 4\n" \
"ds_read_b64_tr_b16 a[116:117], v210 offset:12288\n" \
"v_mfma_f32_16x16x32_bf16 v[100:103], a[92:95], a[220:223], v[100:103]\n" \
"buffer_load_dwordx4 v246, s[12:15], s46 offen lds\n" \
"v_mfma_f32_16x16x32_bf16 v[68:71], a[200:203], a[192:195], v[68:71]\n" \
"s_add_u32 s46, s46, 0x80\n" \
"ds_read_b128 a[88:91], v205 offset:8192\n" \
"v_subrev_u32 v147, s89, v251\n" \
"v_mfma_f32_16x16x32_bf16 v[68:71], a[204:207], a[196:199], v[68:71]\n" \
"s_lshr_b32 s89, s61, 1\n" \
"ds_read_b128 a[92:95], v206 offset:8192\n" \
"s_lshl_b32 s90, s89, 9\n" \
"v_mfma_f32_16x16x32_bf16 v[104:107], a[192:195], a[208:211], v[104:107]\n" \
"s_add_u32 m0, s84, 24576\n" \
"s_add_u32 s90, s90, 511\n" \
"ds_read_b64_tr_b16 a[118:119], v210 offset:13312\n" \
"v_mfma_f32_16x16x32_bf16 v[104:107], a[196:199], a[212:215], v[104:107]\n" \
"global_load_lds_dwordx4 v253, s[48:49] offset:-4096\n" \
"v_sub_u32 v145, s90, v248\n" \
"v_mfma_f32_16x16x32_bf16 v[108:111], a[192:195], a[216:219], v[108:111]\n" \
"s_lshl_b32 s90, s89, 3\n" \
"s_not_b32 s90, s90\n" \
"v_sub_u32 v146, s90, v147\n" \
"v_mfma_f32_16x16x32_bf16 v[108:111], a[196:199], a[220:223], v[108:111]\n" \
"s_waitcnt lgkmcnt(2)\n" \
"v_subrev_u32 v152, 0, v147\n" \
"v_sub_u32 v144, 7, v147\n" \
"v_mfma_f32_16x16x32_bf16 v[32:35], a[96:99], a[64:67], v[32:35]\n" \
"ds_read_b128 a[192:195], v205 offset:12288\n" \
"v_or_b32 v152, v152, v144\n" \
"v_mfma_f32_16x16x32_bf16 v[32:35], a[100:103], a[68:71], v[32:35]\n" \
"global_load_lds_dwordx4 v253, s[48:49] offset:-2048\n" \
"v_ashrrev_i32 v152, 31, v152\n" \
"v_mfma_f32_16x16x32_bf16 v[40:43], a[96:99], a[72:75], v[40:43]\n" \
"ds_read_b128 a[196:199], v206 offset:12288\n" \
"v_and_b32 v152, 0xff800000, v152\n" \
"v_mfma_f32_16x16x32_bf16 v[40:43], a[100:103], a[76:79], v[40:43]\n" \
"ds_read_b64_tr_b16 a[200:201], v210 offset:2048\n" \
"v_mov_b32 v160, v152\n" \
"v_mfma_f32_16x16x32_bf16 v[0:3], a[104:107], a[72:75], v[0:3]\n" \
"ds_read_b64_tr_b16 a[202:203], v210 offset:3072\n" \
"v_mov_b32 v168, v152\n" \
"v_mfma_f32_16x16x32_bf16 v[0:3], a[108:111], a[76:79], v[0:3]\n" \
"s_waitcnt lgkmcnt(3)\n" \
"global_load_lds_dwordx4 v253, s[48:49] offset:0\n" \
"v_mov_b32 v176, v152\n" \
"v_mfma_f32_16x16x32_bf16 v[4:7], a[112:115], a[72:75], v[4:7]\n" \
"s_add_u32 s48, s48, 0x60000\n" \
"s_addc_u32 s49, s49, 0\n" \
"ds_read_b64_tr_b16 a[204:205], v210 offset:14336\n" \
"v_mfma_f32_16x16x32_bf16 v[4:7], a[116:119], a[76:79], v[4:7]\n" \
"ds_read_b64_tr_b16 a[206:207], v210 offset:15360\n" \
"v_sub_u32 v156, -9, v147\n" \
"v_mfma_f32_16x16x32_bf16 v[48:51], a[96:99], a[80:83], v[48:51]\n" \
"ds_read_b64_tr_b16 a[208:209], v209 offset:4096\n" \
"v_ashrrev_i32 v156, 31, v156\n" \
"v_mfma_f32_16x16x32_bf16 v[48:51], a[100:103], a[84:87], v[48:51]\n" \
"global_load_lds_dwordx4 v253, s[50:51] offset:-3072\n" \
"v_and_b32 v156, 0xff800000, v156\n" \
"v_mfma_f32_16x16x32_bf16 v[8:11], a[104:107], a[80:83], v[8:11]\n" \
"ds_read_b64_tr_b16 a[210:211], v209 offset:5120\n" \
"v_mov_b32 v164, v156\n" \
"v_mfma_f32_16x16x32_bf16 v[8:11], a[108:111], a[84:87], v[8:11]\n" \
"ds_read_b64_tr_b16 a[212:213], v209 offset:16384\n" \
"v_mov_b32 v172, v156\n" \
"v_mfma_f32_16x16x32_bf16 v[12:15], a[112:115], a[80:83], v[12:15]\n" \
"ds_read_b64_tr_b16 a[214:215], v209 offset:17408\n" \
"v_add_u32 v148, 0, v146\n" \
"v_mfma_f32_16x16x32_bf16 v[12:15], a[116:119], a[84:87], v[12:15]\n" \
"global_load_lds_dwordx4 v253, s[50:51] offset:-1024\n" \
"v_or3_b32 v148, v148, v145, s86\n" \
"v_mfma_f32_16x16x32_bf16 v[56:59], a[96:99], a[88:91], v[56:59]\n" \
"ds_read_b64_tr_b16 a[216:217], v210 offset:4096\n" \
"v_ashrrev_i32 v148, 31, v148\n" \
"v_mfma_f32_16x16x32_bf16 v[56:59], a[100:103], a[92:95], v[56:59]\n" \
"ds_read_b64_tr_b16 a[218:219], v210 offset:5120\n" \
"v_and_b32 v148, 0xff800000, v148\n" \
"v_mfma_f32_16x16x32_bf16 v[16:19], a[104:107], a[88:91], v[16:19]\n" \
"ds_read_b64_tr_b16 a[220:221], v210 offset:16384\n" \
"v_subrev_u32 v153, 1, v147\n" \
"v_mfma_f32_16x16x32_bf16 v[16:19], a[108:111], a[92:95], v[16:19]\n" \
"global_load_lds_dwordx4 v253, s[50:51] offset:1024\n" \
"v_sub_u32 v144, 8, v147\n" \
"v_mfma_f32_16x16x32_bf16 v[20:23], a[112:115], a[88:91], v[20:23]\n" \
"s_add_u32 s50, s50, 0x60000\n" \
"s_addc_u32 s51, s51, 0\n" \
"ds_read_b64_tr_b16 a[222:223], v210 offset:17408\n" \
"v_mfma_f32_16x16x32_bf16 v[20:23], a[116:119], a[92:95], v[20:23]\n" \
"s_waitcnt lgkmcnt(3)\n" \
"v_add_u32 v207, 0xffff3000, v207\n" \
"v_add_u32 v208, 0xffff3000, v208\n" \
"v_mfma_f32_16x16x32_bf16 v[64:67], a[96:99], a[192:195], v[64:67]\n" \
"v_add_u32 v205, 0xffff3000, v205\n" \
"v_add_u32 v206, 0xffff3000, v206\n" \
"v_mfma_f32_16x16x32_bf16 v[64:67], a[100:103], a[196:199], v[64:67]\n" \
"buffer_load_dword v220, v221, s[56:59], s81 offen sc1\n" \
"v_mfma_f32_16x16x32_bf16 v[24:27], a[104:107], a[192:195], v[24:27]\n" \
"v_add_u32 v209, 0xffff3000, v209\n" \
"v_add_u32 v210, 0xffff3000, v210\n" \
"v_mfma_f32_16x16x32_bf16 v[24:27], a[108:111], a[196:199], v[24:27]\n" \
"v_or_b32 v153, v153, v144\n" \
"v_ashrrev_i32 v153, 31, v153\n" \
"v_mfma_f32_16x16x32_bf16 v[28:31], a[112:115], a[192:195], v[28:31]\n" \
"s_waitcnt vmcnt(12) lgkmcnt(0)\n" \
"v_and_b32 v153, 0xff800000, v153\n" \
"v_mov_b32 v161, v153\n" \
"v_mfma_f32_16x16x32_bf16 v[28:31], a[116:119], a[196:199], v[28:31]\n" \
"s_barrier\n" \
"v_mfma_f32_16x16x32_bf16 v[72:75], a[64:67], a[208:211], v[72:75]\n" \
"ds_read_b64_tr_b16 a[96:97], v209 offset:2048\n" \
"v_mov_b32 v169, v153\n" \
"v_mfma_f32_16x16x32_bf16 v[72:75], a[68:71], a[212:215], v[72:75]\n" \
"ds_read_b64_tr_b16 a[98:99], v209 offset:3072\n" \
"v_mov_b32 v177, v153\n" \
"v_mfma_f32_16x16x32_bf16 v[44:47], a[200:203], a[72:75], v[44:47]\n" \
"s_add_u32 m0, s95, 118784\n" \
"ds_read_b128 a[64:67], v207 offset:0\n" \
"ds_read_b64_tr_b16 a[100:101], v209 offset:14336\n" \
"v_mfma_f32_16x16x32_bf16 v[44:47], a[204:207], a[76:79], v[44:47]\n" \
"global_load_lds_dwordx4 v252, s[42:43] offset:0\n" \
"v_sub_u32 v157, -8, v147\n" \
"v_mfma_f32_16x16x32_bf16 v[80:83], a[72:75], a[208:211], v[80:83]\n" \
"ds_read_b128 a[68:71], v208 offset:0\n" \
"ds_read_b64_tr_b16 a[102:103], v209 offset:15360\n" \
"v_mfma_f32_16x16x32_bf16 v[80:83], a[76:79], a[212:215], v[80:83]\n" \
"ds_read_b64_tr_b16 a[104:105], v209 offset:0\n" \
"v_ashrrev_i32 v157, 31, v157\n" \
"v_mfma_f32_16x16x32_bf16 v[84:87], a[72:75], a[216:219], v[84:87]\n" \
"ds_read_b64_tr_b16 a[106:107], v209 offset:1024\n" \
"v_and_b32 v157, 0xff800000, v157\n" \
"v_mfma_f32_16x16x32_bf16 v[84:87], a[76:79], a[220:223], v[84:87]\n" \
"global_load_lds_dwordx4 v252, s[42:43] offset:2048\n" \
"v_mov_b32 v165, v157\n" \
"v_mfma_f32_16x16x32_bf16 v[52:55], a[200:203], a[80:83], v[52:55]\n" \
"ds_read_b128 a[72:75], v205 offset:0\n" \
"v_mov_b32 v173, v157\n" \
"v_mfma_f32_16x16x32_bf16 v[52:55], a[204:207], a[84:87], v[52:55]\n" \
"ds_read_b128 a[76:79], v206 offset:0\n" \
"v_add_u32 v149, 1, v146\n" \
"v_mfma_f32_16x16x32_bf16 v[88:91], a[80:83], a[208:211], v[88:91]\n" \
"ds_read_b64_tr_b16 a[108:109], v209 offset:12288\n" \
"v_or3_b32 v149, v149, v145, s86\n" \
"v_mfma_f32_16x16x32_bf16 v[88:91], a[84:87], a[212:215], v[88:91]\n" \
"global_load_lds_dwordx4 v252, s[44:45] offset:1024\n" \
"v_ashrrev_i32 v149, 31, v149\n" \
"v_mfma_f32_16x16x32_bf16 v[92:95], a[80:83], a[216:219], v[92:95]\n" \
"ds_read_b64_tr_b16 a[110:111], v209 offset:13312\n" \
"v_and_b32 v149, 0xff800000, v149\n" \
"v_mfma_f32_16x16x32_bf16 v[92:95], a[84:87], a[220:223], v[92:95]\n" \
"ds_read_b64_tr_b16 a[112:113], v210 offset:0\n" \
"v_subrev_u32 v154, 2, v147\n" \
"v_mfma_f32_16x16x32_bf16 v[60:63], a[200:203], a[88:91], v[60:63]\n" \
"ds_read_b128 a[80:83], v205 offset:4096\n" \
"v_sub_u32 v144, 9, v147\n" \
"v_mfma_f32_16x16x32_bf16 v[60:63], a[204:207], a[92:95], v[60:63]\n" \
"global_load_lds_dwordx4 v252, s[44:45] offset:3072\n" \
"v_or_b32 v154, v154, v144\n" \
"v_mfma_f32_16x16x32_bf16 v[96:99], a[88:91], a[208:211], v[96:99]\n" \
"ds_read_b128 a[84:87], v206 offset:4096\n" \
"v_ashrrev_i32 v154, 31, v154\n" \
"v_mfma_f32_16x16x32_bf16 v[96:99], a[92:95], a[212:215], v[96:99]\n" \
"ds_read_b64_tr_b16 a[114:115], v210 offset:1024\n" \
"v_and_b32 v154, 0xff800000, v154\n" \
"v_mfma_f32_16x16x32_bf16 v[100:103], a[88:91], a[216:219], v[100:103]\n" \
"s_add_u32 m0, s85, 135168\n" \
"ds_read_b64_tr_b16 a[116:117], v210 offset:12288\n" \
"v_mov_b32 v162, v154\n" \
"v_mfma_f32_16x16x32_bf16 v[100:103], a[92:95], a[220:223], v[100:103]\n" \
"buffer_load_dwordx4 v246, s[12:15], s46 offen lds\n" \
"v_mfma_f32_16x16x32_bf16 v[68:71], a[200:203], a[192:195], v[68:71]\n" \
"ds_read_b128 a[88:91], v205 offset:8192\n" \
"v_mov_b32 v170, v154\n" \
"v_mfma_f32_16x16x32_bf16 v[68:71], a[204:207], a[196:199], v[68:71]\n" \
"ds_read_b128 a[92:95], v206 offset:8192\n" \
"v_mov_b32 v178, v154\n" \
"v_mfma_f32_16x16x32_bf16 v[104:107], a[192:195], a[208:211], v[104:107]\n" \
"s_add_u32 m0, s84, 143360\n" \
"ds_read_b64_tr_b16 a[118:119], v210 offset:13312\n" \
"v_sub_u32 v158, -7, v147\n" \
"v_mfma_f32_16x16x32_bf16 v[104:107], a[196:199], a[212:215], v[104:107]\n" \
"global_load_lds_dwordx4 v253, s[48:49] offset:-4096\n" \
"v_ashrrev_i32 v158, 31, v158\n" \
"v_mfma_f32_16x16x32_bf16 v[108:111], a[192:195], a[216:219], v[108:111]\n" \
"v_and_b32 v158, 0xff800000, v158\n" \
"v_mov_b32 v166, v158\n" \
"v_mfma_f32_16x16x32_bf16 v[108:111], a[196:199], a[220:223], v[108:111]\n" \
"s_waitcnt lgkmcnt(2)\n" \
"v_mov_b32 v174, v158\n" \
"v_add_u32 v150, 2, v146\n" \
"v_mfma_f32_16x16x32_bf16 v[32:35], a[96:99], a[64:67], v[32:35]\n" \
"ds_read_b128 a[192:195], v205 offset:12288\n" \
"v_or3_b32 v150, v150, v145, s86\n" \
"v_mfma_f32_16x16x32_bf16 v[32:35], a[100:103], a[68:71], v[32:35]\n" \
"global_load_lds_dwordx4 v253, s[48:49] offset:-2048\n" \
"v_ashrrev_i32 v150, 31, v150\n" \
"v_mfma_f32_16x16x32_bf16 v[40:43], a[96:99], a[72:75], v[40:43]\n" \
"ds_read_b128 a[196:199], v206 offset:12288\n" \
"v_and_b32 v150, 0xff800000, v150\n" \
"v_mfma_f32_16x16x32_bf16 v[40:43], a[100:103], a[76:79], v[40:43]\n" \
"ds_read_b64_tr_b16 a[200:201], v210 offset:2048\n" \
"v_subrev_u32 v155, 3, v147\n" \
"v_mfma_f32_16x16x32_bf16 v[0:3], a[104:107], a[72:75], v[0:3]\n" \
"ds_read_b64_tr_b16 a[202:203], v210 offset:3072\n" \
"v_sub_u32 v144, 10, v147\n" \
"v_mfma_f32_16x16x32_bf16 v[0:3], a[108:111], a[76:79], v[0:3]\n" \
"s_waitcnt lgkmcnt(3)\n" \
"global_load_lds_dwordx4 v253, s[48:49] offset:0\n" \
"v_or_b32 v155, v155, v144\n" \
"v_mfma_f32_16x16x32_bf16 v[4:7], a[112:115], a[72:75], v[4:7]\n" \
"ds_read_b64_tr_b16 a[204:205], v210 offset:14336\n" \
"v_ashrrev_i32 v155, 31, v155\n" \
"v_mfma_f32_16x16x32_bf16 v[4:7], a[116:119], a[76:79], v[4:7]\n" \
"ds_read_b64_tr_b16 a[206:207], v210 offset:15360\n" \
"v_and_b32 v155, 0xff800000, v155\n" \
"v_mfma_f32_16x16x32_bf16 v[48:51], a[96:99], a[80:83], v[48:51]\n" \
"ds_read_b64_tr_b16 a[208:209], v209 offset:4096\n" \
"v_mov_b32 v163, v155\n" \
"v_mfma_f32_16x16x32_bf16 v[48:51], a[100:103], a[84:87], v[48:51]\n" \
"global_load_lds_dwordx4 v253, s[50:51] offset:-3072\n" \
"v_mov_b32 v171, v155\n" \
"v_mfma_f32_16x16x32_bf16 v[8:11], a[104:107], a[80:83], v[8:11]\n" \
"ds_read_b64_tr_b16 a[210:211], v209 offset:5120\n" \
"v_mov_b32 v179, v155\n" \
"v_mfma_f32_16x16x32_bf16 v[8:11], a[108:111], a[84:87], v[8:11]\n" \
"ds_read_b64_tr_b16 a[212:213], v209 offset:16384\n" \
"v_sub_u32 v159, -6, v147\n" \
"v_mfma_f32_16x16x32_bf16 v[12:15], a[112:115], a[80:83], v[12:15]\n" \
"ds_read_b64_tr_b16 a[214:215], v209 offset:17408\n" \
"v_ashrrev_i32 v159, 31, v159\n" \
"v_mfma_f32_16x16x32_bf16 v[12:15], a[116:119], a[84:87], v[12:15]\n" \
"global_load_lds_dwordx4 v253, s[50:51] offset:-1024\n" \
"v_and_b32 v159, 0xff800000, v159\n" \
"v_mfma_f32_16x16x32_bf16 v[56:59], a[96:99], a[88:91], v[56:59]\n" \
"ds_read_b64_tr_b16 a[216:217], v210 offset:4096\n" \
"v_mov_b32 v167, v159\n" \
"v_mfma_f32_16x16x32_bf16 v[56:59], a[100:103], a[92:95], v[56:59]\n" \
"ds_read_b64_tr_b16 a[218:219], v210 offset:5120\n" \
"v_mov_b32 v175, v159\n" \
"v_mfma_f32_16x16x32_bf16 v[16:19], a[104:107], a[88:91], v[16:19]\n" \
"ds_read_b64_tr_b16 a[220:221], v210 offset:16384\n" \
"v_add_u32 v151, 3, v146\n" \
"v_mfma_f32_16x16x32_bf16 v[16:19], a[108:111], a[92:95], v[16:19]\n" \
"global_load_lds_dwordx4 v253, s[50:51] offset:1024\n" \
"v_or3_b32 v151, v151, v145, s86\n" \
"v_mfma_f32_16x16x32_bf16 v[20:23], a[112:115], a[88:91], v[20:23]\n" \
"ds_read_b64_tr_b16 a[222:223], v210 offset:17408\n" \
"v_ashrrev_i32 v151, 31, v151\n" \
"v_mfma_f32_16x16x32_bf16 v[20:23], a[116:119], a[92:95], v[20:23]\n" \
"s_waitcnt lgkmcnt(3)\n" \
"v_add_u32 v207, 0xffff0000, v207\n" \
"v_add_u32 v208, 0xffff0000, v208\n" \
"v_mfma_f32_16x16x32_bf16 v[64:67], a[96:99], a[192:195], v[64:67]\n" \
"v_add_u32 v205, 0xffff0000, v205\n" \
"v_add_u32 v206, 0xffff0000, v206\n" \
"v_mfma_f32_16x16x32_bf16 v[64:67], a[100:103], a[196:199], v[64:67]\n" \
"v_add_u32 v209, 0xffff0000, v209\n" \
"v_add_u32 v210, 0xffff0000, v210\n" \
"v_mfma_f32_16x16x32_bf16 v[24:27], a[104:107], a[192:195], v[24:27]\n" \
"v_and_b32 v151, 0xff800000, v151\n" \
"v_mov_b32 v144, 0x3e8293ee\n" \
"v_mfma_f32_16x16x32_bf16 v[24:27], a[108:111], a[196:199], v[24:27]\n" \
"s_add_u32 s90, s95, 117760\n" \
"v_add_u32 v223, s90, v248\n" \
"v_mfma_f32_16x16x32_bf16 v[28:31], a[112:115], a[192:195], v[28:31]\n" \
"s_waitcnt vmcnt(12) lgkmcnt(0)\n" \
"s_cmp_lg_u32 s89, 0\n" \
"s_cselect_b64 s[92:93], -1, 0\n" \
"v_mfma_f32_16x16x32_bf16 v[28:31], a[116:119], a[196:199], v[28:31]\n" \
"s_barrier\n" \
"v_mfma_f32_16x16x32_bf16 v[72:75], a[64:67], a[208:211], v[72:75]\n" \
"s_waitcnt vmcnt(11)\n" \
"ds_read_b64_tr_b16 a[96:97], v209 offset:2048\n" \
"v_cmp_ne_u32 vcc, s60, v220\n" \
"v_mfma_f32_16x16x32_bf16 v[72:75], a[68:71], a[212:215], v[72:75]\n" \
"ds_read_b64_tr_b16 a[98:99], v209 offset:3072\n" \
"v_xor_b32 v145, 0x2000, v223\n" \
"v_mfma_f32_16x16x32_bf16 v[44:47], a[200:203], a[72:75], v[44:47]\n" \
"ds_read_b128 a[64:67], v207 offset:0\n" \
"ds_read_b64_tr_b16 a[100:101], v209 offset:14336\n" \
"v_mfma_f32_16x16x32_bf16 v[44:47], a[204:207], a[76:79], v[44:47]\n" \
"s_and_b32 vcc_hi, vcc_lo, s80\n" \
"s_cbranch_scc1 Lpl2_%=\n" \
"Lpr2_%=:\n" \
"v_mfma_f32_16x16x32_bf16 v[80:83], a[72:75], a[208:211], v[80:83]\n" \
"s_add_u32 m0, s75, s95\n" \
"ds_read_b128 a[68:71], v208 offset:0\n" \
"ds_read_b64_tr_b16 a[102:103], v209 offset:15360\n" \
"v_mfma_f32_16x16x32_bf16 v[80:83], a[76:79], a[212:215], v[80:83]\n" \
"ds_read_b64_tr_b16 a[104:105], v209 offset:0\n" \
"global_load_lds_dwordx4 v222, s[36:37] offset:0 sc1\n" \
"v_mfma_f32_16x16x32_bf16 v[84:87], a[72:75], a[216:219], v[84:87]\n" \
"s_cmp_eq_u32 s89, 0\n" \
"s_cselect_b32 s16, 0, 0xff00\n" \
"ds_read_b64_tr_b16 a[106:107], v209 offset:1024\n" \
"v_mfma_f32_16x16x32_bf16 v[84:87], a[76:79], a[220:223], v[84:87]\n" \
"s_mov_b32 s17, s16\n" \
"v_add_u32 v146, 0x1c90080, v249\n" \
"v_cndmask_b32 v146, v249, v146, s[16:17]\n" \
"v_mfma_f32_16x16x32_bf16 v[52:55], a[200:203], a[80:83], v[52:55]\n" \
"ds_read_b128 a[72:75], v205 offset:0\n" \
"v_mfma_f32_16x16x32_bf16 v[52:55], a[204:207], a[84:87], v[52:55]\n" \
"ds_read_b128 a[76:79], v206 offset:0\n" \
"global_load_lds_dwordx4 v222, s[36:37] offset:1024 sc1\n" \
"v_mfma_f32_16x16x32_bf16 v[88:91], a[80:83], a[208:211], v[88:91]\n" \
"ds_read_b64_tr_b16 a[108:109], v209 offset:12288\n" \
"v_mfma_f32_16x16x32_bf16 v[88:91], a[84:87], a[212:215], v[88:91]\n" \
"v_mfma_f32_16x16x32_bf16 v[92:95], a[80:83], a[216:219], v[92:95]\n" \
"ds_read_b64_tr_b16 a[110:111], v209 offset:13312\n" \
"v_mfma_f32_16x16x32_bf16 v[92:95], a[84:87], a[220:223], v[92:95]\n" \
"ds_read_b64_tr_b16 a[112:113], v210 offset:0\n" \
"global_load_lds_dwordx4 v222, s[36:37] offset:2048 sc1\n" \
"v_mfma_f32_16x16x32_bf16 v[60:63], a[200:203], a[88:91], v[60:63]\n" \
"ds_read_b128 a[80:83], v205 offset:4096\n" \
"v_mfma_f32_16x16x32_bf16 v[60:63], a[204:207], a[92:95], v[60:63]\n" \
"v_mfma_f32_16x16x32_bf16 v[96:99], a[88:91], a[208:211], v[96:99]\n" \
"ds_read_b128 a[84:87], v206 offset:4096\n" \
"v_mfma_f32_16x16x32_bf16 v[96:99], a[92:95], a[212:215], v[96:99]\n" \
"ds_read_b64_tr_b16 a[114:115], v210 offset:1024\n" \
"global_load_lds_dwordx4 v222, s[36:37] offset:3072 sc1\n" \
"v_mfma_f32_16x16x32_bf16 v[100:103], a[88:91], a[216:219], v[100:103]\n" \
"s_add_u32 s36, s36, 0x1000\n" \
"ds_read_b64_tr_b16 a[116:117], v210 offset:12288\n" \
"v_mfma_f32_16x16x32_bf16 v[100:103], a[92:95], a[220:223], v[100:103]\n" \
"v_mfma_f32_16x16x32_bf16 v[68:71], a[200:203], a[192:195], v[68:71]\n" \
"ds_read_b128 a[88:91], v205 offset:8192\n" \
"s_add_u32 m0, m0, 0x5000\n" \
"v_mfma_f32_16x16x32_bf16 v[68:71], a[204:207], a[196:199], v[68:71]\n" \
"ds_read_b128 a[92:95], v206 offset:8192\n" \
"global_load_lds_dwordx4 v216, s[38:39] offset:-4096\n" \
"v_mfma_f32_16x16x32_bf16 v[104:107], a[192:195], a[208:211], v[104:107]\n" \
"ds_read_b64_tr_b16 a[118:119], v210 offset:13312\n" \
"v_mfma_f32_16x16x32_bf16 v[104:107], a[196:199], a[212:215], v[104:107]\n" \
"v_mfma_f32_16x16x32_bf16 v[108:111], a[192:195], a[216:219], v[108:111]\n" \
"v_mfma_f32_16x16x32_bf16 v[108:111], a[196:199], a[220:223], v[108:111]\n" \
"s_waitcnt lgkmcnt(2)\n" \
"global_load_lds_dwordx4 v217, s[38:39] offset:-3072\n" \
"v_mfma_f32_16x16x32_bf16 v[32:35], a[96:99], a[64:67], v[32:35]\n" \
"ds_read_b128 a[192:195], v205 offset:12288\n" \
"v_mfma_f32_16x16x32_bf16 v[32:35], a[100:103], a[68:71], v[32:35]\n" \
"v_mfma_f32_16x16x32_bf16 v[40:43], a[96:99], a[72:75], v[40:43]\n" \
"ds_read_b128 a[196:199], v206 offset:12288\n" \
"v_mfma_f32_16x16x32_bf16 v[40:43], a[100:103], a[76:79], v[40:43]\n" \
"ds_read_b64_tr_b16 a[200:201], v210 offset:2048\n" \
"global_load_lds_dwordx4 v218, s[38:39] offset:-2048\n" \
"Lpg11020_%=:\n" \
"s_cbranch_execz Lpg12708_%=\n" \
"v_mfma_f32_16x16x32_bf16 v[0:3], a[104:107], a[72:75], v[0:3]\n" \
"ds_read_b64_tr_b16 a[202:203], v210 offset:3072\n" \
"v_mfma_f32_16x16x32_bf16 v[0:3], a[108:111], a[76:79], v[0:3]\n" \
"s_waitcnt lgkmcnt(3)\n" \
"v_mfma_f32_16x16x32_bf16 v[4:7], a[112:115], a[72:75], v[4:7]\n" \
"ds_read_b64_tr_b16 a[204:205], v210 offset:14336\n" \
"v_mfma_f32_16x16x32_bf16 v[4:7], a[116:119], a[76:79], v[4:7]\n" \
"ds_read_b64_tr_b16 a[206:207], v210 offset:15360\n" \
"global_load_lds_dwordx4 v219, s[38:39] offset:-1024\n" \
"v_mfma_f32_16x16x32_bf16 v[48:51], a[96:99], a[80:83], v[48:51]\n" \
"s_add_u32 s38, s38, 0x20000\n" \
"s_addc_u32 s39, s39, 0\n" \
"ds_read_b64_tr_b16 a[208:209], v209 offset:4096\n" \
"v_mfma_f32_16x16x32_bf16 v[48:51], a[100:103], a[84:87], v[48:51]\n" \
"s_sub_u32 s75, s75, 32768\n" \
"s_cselect_b32 s75, 131072, s75\n" \
"v_mfma_f32_16x16x32_bf16 v[8:11], a[104:107], a[80:83], v[8:11]\n" \
"ds_read_b64_tr_b16 a[210:211], v209 offset:5120\n" \
"v_mfma_f32_16x16x32_bf16 v[8:11], a[108:111], a[84:87], v[8:11]\n" \
"ds_read_b64_tr_b16 a[212:213], v209 offset:16384\n" \
"v_mfma_f32_16x16x32_bf16 v[12:15], a[112:115], a[80:83], v[12:15]\n" \
"ds_read_b64_tr_b16 a[214:215], v209 offset:17408\n" \
"v_mfma_f32_16x16x32_bf16 v[12:15], a[116:119], a[84:87], v[12:15]\n" \
"v_mfma_f32_16x16x32_bf16 v[56:59], a[96:99], a[88:91], v[56:59]\n" \
"ds_read_b64_tr_b16 a[216:217], v210 offset:4096\n" \
"v_mfma_f32_16x16x32_bf16 v[56:59], a[100:103], a[92:95], v[56:59]\n" \
"ds_read_b64_tr_b16 a[218:219], v210 offset:5120\n" \
"v_mfma_f32_16x16x32_bf16 v[16:19], a[104:107], a[88:91], v[16:19]\n" \
"ds_read_b64_tr_b16 a[220:221], v210 offset:16384\n" \
"v_mfma_f32_16x16x32_bf16 v[16:19], a[108:111], a[92:95], v[16:19]\n" \
"v_mfma_f32_16x16x32_bf16 v[20:23], a[112:115], a[88:91], v[20:23]\n" \
"ds_read_b64_tr_b16 a[222:223], v210 offset:17408\n" \
"v_mfma_f32_16x16x32_bf16 v[20:23], a[116:119], a[92:95], v[20:23]\n" \
"s_waitcnt lgkmcnt(3)\n" \
"v_add_u32 v207, 0x1d000, v207\n" \
"v_add_u32 v208, 0x1d000, v208\n" \
"v_mfma_f32_16x16x32_bf16 v[64:67], a[96:99], a[192:195], v[64:67]\n" \
"v_add_u32 v205, 0x1d000, v205\n" \
"v_add_u32 v206, 0x1d000, v206\n" \
"v_mfma_f32_16x16x32_bf16 v[64:67], a[100:103], a[196:199], v[64:67]\n" \
"v_add_u32 v209, 0x1d000, v209\n" \
"v_add_u32 v210, 0x1d000, v210\n" \
"v_mfma_f32_16x16x32_bf16 v[24:27], a[104:107], a[192:195], v[24:27]\n" \
"v_mfma_f32_16x16x32_bf16 v[24:27], a[108:111], a[196:199], v[24:27]\n" \
"v_mfma_f32_16x16x32_bf16 v[28:31], a[112:115], a[192:195], v[28:31]\n" \
"s_waitcnt vmcnt(8) lgkmcnt(0)\n" \
"v_mfma_f32_16x16x32_bf16 v[28:31], a[116:119], a[196:199], v[28:31]\n" \
"s_barrier\n" \
"v_mfma_f32_16x16x32_bf16 v[72:75], a[64:67], a[208:211], v[72:75]\n" \
"ds_read_b64_tr_b16 a[96:97], v209 offset:2048\n" \
"v_mfma_f32_16x16x32_bf16 v[72:75], a[68:71], a[212:215], v[72:75]\n" \
"ds_read_b64_tr_b16 a[98:99], v209 offset:3072\n" \
"v_mfma_f32_16x16x32_bf16 v[44:47], a[200:203], a[72:75], v[44:47]\n" \
"s_add_u32 m0, s75, s95\n" \
"ds_read_b128 a[64:67], v207 offset:0\n" \
"ds_read_b64_tr_b16 a[100:101], v209 offset:14336\n" \
"v_mfma_f32_16x16x32_bf16 v[44:47], a[204:207], a[76:79], v[44:47]\n" \
"global_load_lds_dwordx4 v222, s[36:37] offset:0 sc1\n" \
"v_mfma_f32_16x16x32_bf16 v[80:83], a[72:75], a[208:211], v[80:83]\n" \
"ds_read_b128 a[68:71], v208 offset:0\n" \
"ds_read_b64_tr_b16 a[102:103], v209 offset:15360\n" \
"v_mfma_f32_16x16x32_bf16 v[80:83], a[76:79], a[212:215], v[80:83]\n" \
"ds_read_b64_tr_b16 a[104:105], v209 offset:0\n" \
"v_mfma_f32_16x16x32_bf16 v[84:87], a[72:75], a[216:219], v[84:87]\n" \
"ds_read_b64_tr_b16 a[106:107], v209 offset:1024\n" \
"v_mfma_f32_16x16x32_bf16 v[84:87], a[76:79], a[220:223], v[84:87]\n" \
"global_load_lds_dwordx4 v222, s[36:37] offset:1024 sc1\n" \
"v_mfma_f32_16x16x32_bf16 v[52:55], a[200:203], a[80:83], v[52:55]\n" \
"ds_read_b128 a[72:75], v205 offset:0\n" \
"v_mfma_f32_16x16x32_bf16 v[52:55], a[204:207], a[84:87], v[52:55]\n" \
"ds_read_b128 a[76:79], v206 offset:0\n" \
"v_mfma_f32_16x16x32_bf16 v[88:91], a[80:83], a[208:211], v[88:91]\n" \
"ds_read_b64_tr_b16 a[108:109], v209 offset:12288\n" \
"v_mfma_f32_16x16x32_bf16 v[88:91], a[84:87], a[212:215], v[88:91]\n" \
"global_load_lds_dwordx4 v222, s[36:37] offset:2048 sc1\n" \
"v_mfma_f32_16x16x32_bf16 v[92:95], a[80:83], a[216:219], v[92:95]\n" \
"ds_read_b64_tr_b16 a[110:111], v209 offset:13312\n" \
"v_mfma_f32_16x16x32_bf16 v[92:95], a[84:87], a[220:223], v[92:95]\n" \
"ds_read_b64_tr_b16 a[112:113], v210 offset:0\n" \
"v_mfma_f32_16x16x32_bf16 v[60:63], a[200:203], a[88:91], v[60:63]\n" \
"ds_read_b128 a[80:83], v205 offset:4096\n" \
"v_mfma_f32_16x16x32_bf16 v[60:63], a[204:207], a[92:95], v[60:63]\n" \
"global_load_lds_dwordx4 v222, s[36:37] offset:3072 sc1\n" \
"v_mfma_f32_16x16x32_bf16 v[96:99], a[88:91], a[208:211], v[96:99]\n" \
"ds_read_b128 a[84:87], v206 offset:4096\n" \
"s_add_u32 s36, s36, 0x1000\n" \
"v_mfma_f32_16x16x32_bf16 v[96:99], a[92:95], a[212:215], v[96:99]\n" \
"ds_read_b64_tr_b16 a[114:115], v210 offset:1024\n" \
"v_mfma_f32_16x16x32_bf16 v[100:103], a[88:91], a[216:219], v[100:103]\n" \
"s_add_u32 m0, m0, 0x5000\n" \
"ds_read_b64_tr_b16 a[116:117], v210 offset:12288\n" \
"v_mfma_f32_16x16x32_bf16 v[100:103], a[92:95], a[220:223], v[100:103]\n" \
"global_load_lds_dwordx4 v216, s[38:39] offset:-4096\n" \
"v_mfma_f32_16x16x32_bf16 v[68:71], a[200:203], a[192:195], v[68:71]\n" \
"ds_read_b128 a[88:91], v205 offset:8192\n" \
"v_mfma_f32_16x16x32_bf16 v[68:71], a[204:207], a[196:199], v[68:71]\n" \
"ds_read_b128 a[92:95], v206 offset:8192\n" \
"v_mfma_f32_16x16x32_bf16 v[104:107], a[192:195], a[208:211], v[104:107]\n" \
"ds_read_b64_tr_b16 a[118:119], v210 offset:13312\n" \
"v_mfma_f32_16x16x32_bf16 v[104:107], a[196:199], a[212:215], v[104:107]\n" \
"global_load_lds_dwordx4 v217, s[38:39] offset:-3072\n" \
"v_mfma_f32_16x16x32_bf16 v[108:111], a[192:195], a[216:219], v[108:111]\n" \
"v_mfma_f32_16x16x32_bf16 v[108:111], a[196:199], a[220:223], v[108:111]\n" \
"s_waitcnt lgkmcnt(2)\n" \
"v_mfma_f32_16x16x32_bf16 v[32:35], a[96:99], a[64:67], v[32:35]\n" \
"ds_read_b128 a[192:195], v205 offset:12288\n" \
"v_mfma_f32_16x16x32_bf16 v[32:35], a[100:103], a[68:71], v[32:35]\n" \
"global_load_lds_dwordx4 v218, s[38:39] offset:-2048\n" \
"v_mfma_f32_16x16x32_bf16 v[40:43], a[96:99], a[72:75], v[40:43]\n" \
"ds_read_b128 a[196:199], v206 offset:12288\n" \
"v_mfma_f32_16x16x32_bf16 v[40:43], a[100:103], a[76:79], v[40:43]\n" \
"ds_read_b64_tr_b16 a[200:201], v210 offset:2048\n" \
"v_mfma_f32_16x16x32_bf16 v[0:3], a[104:107], a[72:75], v[0:3]\n" \
"ds_read_b64_tr_b16 a[202:203], v210 offset:3072\n" \
"v_mfma_f32_16x16x32_bf16 v[0:3], a[108:111], a[76:79], v[0:3]\n" \
"s_waitcnt lgkmcnt(3)\n" \
"global_load_lds_dwordx4 v219, s[38:39] offset:-1024\n" \
"v_mfma_f32_16x16x32_bf16 v[4:7], a[112:115], a[72:75], v[4:7]\n" \
"s_add_u32 s38, s38, 0x20000\n" \
"s_addc_u32 s39, s39, 0\n" \
"ds_read_b64_tr_b16 a[204:205], v210 offset:14336\n" \
"v_mfma_f32_16x16x32_bf16 v[4:7], a[116:119], a[76:79], v[4:7]\n" \
"s_sub_u32 s75, s75, 32768\n" \
"s_cselect_b32 s75, 131072, s75\n" \
"ds_read_b64_tr_b16 a[206:207], v210 offset:15360\n" \
"v_mfma_f32_16x16x32_bf16 v[48:51], a[96:99], a[80:83], v[48:51]\n" \
"s_add_u32 m0, s75, s95\n" \
"ds_read_b64_tr_b16 a[208:209], v209 offset:4096\n" \
"v_mfma_f32_16x16x32_bf16 v[48:51], a[100:103], a[84:87], v[48:51]\n" \
"global_load_lds_dwordx4 v222, s[36:37] offset:0 sc1\n" \
"v_mfma_f32_16x16x32_bf16 v[8:11], a[104:107], a[80:83], v[8:11]\n" \
"ds_read_b64_tr_b16 a[210:211], v209 offset:5120\n" \
"v_cvt_pk_bf16_f32 v112, v0, v1\n" \
"v_mfma_f32_16x16x32_bf16 v[8:11], a[108:111], a[84:87], v[8:11]\n" \
"ds_read_b64_tr_b16 a[212:213], v209 offset:16384\n" \
"v_cvt_pk_bf16_f32 v113, v2, v3\n" \
"v_mfma_f32_16x16x32_bf16 v[12:15], a[112:115], a[80:83], v[12:15]\n" \
"ds_read_b64_tr_b16 a[214:215], v209 offset:17408\n" \
"v_cvt_pk_bf16_f32 v114, v4, v5\n" \
"v_mfma_f32_16x16x32_bf16 v[12:15], a[116:119], a[84:87], v[12:15]\n" \
"global_load_lds_dwordx4 v222, s[36:37] offset:1024 sc1\n" \
"v_cvt_pk_bf16_f32 v115, v6, v7\n" \
"v_mfma_f32_16x16x32_bf16 v[56:59], a[96:99], a[88:91], v[56:59]\n" \
"ds_read_b64_tr_b16 a[216:217], v210 offset:4096\n" \
"v_mfma_f32_16x16x32_bf16 v[56:59], a[100:103], a[92:95], v[56:59]\n" \
"ds_read_b64_tr_b16 a[218:219], v210 offset:5120\n" \
"v_mfma_f32_16x16x32_bf16 v[16:19], a[104:107], a[88:91], v[16:19]\n" \
"ds_read_b64_tr_b16 a[220:221], v210 offset:16384\n" \
"v_cvt_pk_bf16_f32 v116, v8, v9\n" \
"v_mfma_f32_16x16x32_bf16 v[16:19], a[108:111], a[92:95], v[16:19]\n" \
"global_load_lds_dwordx4 v222, s[36:37] offset:2048 sc1\n" \
"v_cvt_pk_bf16_f32 v117, v10, v11\n" \
"v_mfma_f32_16x16x32_bf16 v[20:23], a[112:115], a[88:91], v[20:23]\n" \
"ds_read_b64_tr_b16 a[222:223], v210 offset:17408\n" \
"v_cvt_pk_bf16_f32 v118, v12, v13\n" \
"v_mfma_f32_16x16x32_bf16 v[20:23], a[116:119], a[92:95], v[20:23]\n" \
"s_waitcnt lgkmcnt(3)\n" \
"v_cvt_pk_bf16_f32 v119, v14, v15\n" \
"v_mfma_f32_16x16x32_bf16 v[64:67], a[96:99], a[192:195], v[64:67]\n" \
"v_mfma_f32_16x16x32_bf16 v[64:67], a[100:103], a[196:199], v[64:67]\n" \
"global_load_lds_dwordx4 v222, s[36:37] offset:3072 sc1\n" \
"v_mfma_f32_16x16x32_bf16 v[24:27], a[104:107], a[192:195], v[24:27]\n" \
"v_cvt_pk_bf16_f32 v120, v16, v17\n" \
"v_cvt_pk_bf16_f32 v121, v18, v19\n" \
"v_mfma_f32_16x16x32_bf16 v[24:27], a[108:111], a[196:199], v[24:27]\n" \
"v_cvt_pk_bf16_f32 v122, v20, v21\n" \
"v_cvt_pk_bf16_f32 v123, v22, v23\n" \
"v_mfma_f32_16x16x32_bf16 v[28:31], a[112:115], a[192:195], v[28:31]\n" \
"s_waitcnt vmcnt(12) lgkmcnt(0)\n" \
"v_mfma_f32_16x16x32_bf16 v[28:31], a[116:119], a[196:199], v[28:31]\n" \
"s_barrier\n" \
"v_mfma_f32_16x16x32_bf16 v[72:75], a[64:67], a[208:211], v[72:75]\n" \
"ds_read_b64_tr_b16 a[32:33], v214 offset:16384\n" \
"v_mfma_f32_16x16x32_bf16 v[72:75], a[68:71], a[212:215], v[72:75]\n" \
"v_mfma_f32_16x16x32_bf16 v[44:47], a[200:203], a[72:75], v[44:47]\n" \
"s_add_u32 s36, s36, 0x1000\n" \
"s_add_u32 m0, m0, 0x5000\n" \
"ds_read_b64_tr_b16 a[34:35], v214 offset:18432\n" \
"v_mfma_f32_16x16x32_bf16 v[44:47], a[204:207], a[76:79], v[44:47]\n" \
"ds_read_b64_tr_b16 a[36:37], v214 offset:24576\n" \
"global_load_lds_dwordx4 v216, s[38:39] offset:-4096\n" \
"v_mfma_f32_16x16x32_bf16 v[80:83], a[72:75], a[208:211], v[80:83]\n" \
"v_cvt_pk_bf16_f32 v124, v24, v25\n" \
"v_cvt_pk_bf16_f32 v125, v26, v27\n" \
"v_mfma_f32_16x16x32_bf16 v[80:83], a[76:79], a[212:215], v[80:83]\n" \
"ds_read_b64_tr_b16 a[38:39], v214 offset:26624\n" \
"v_cvt_pk_bf16_f32 v126, v28, v29\n" \
"v_mfma_f32_16x16x32_bf16 v[84:87], a[72:75], a[216:219], v[84:87]\n" \
"ds_read_b128 a[0:3], v213 offset:0\n" \
"v_cvt_pk_bf16_f32 v127, v30, v31\n" \
"v_mfma_f32_16x16x32_bf16 v[84:87], a[76:79], a[220:223], v[84:87]\n" \
"global_load_lds_dwordx4 v217, s[38:39] offset:-3072\n" \
"v_cvt_pk_bf16_f32 v128, v32, v33\n" \
"v_mfma_f32_16x16x32_bf16 v[52:55], a[200:203], a[80:83], v[52:55]\n" \
"ds_read_b128 a[4:7], v213 offset:2048\n" \
"v_cvt_pk_bf16_f32 v129, v34, v35\n" \
"v_mfma_f32_16x16x32_bf16 v[52:55], a[204:207], a[84:87], v[52:55]\n" \
"ds_read_b64_tr_b16 a[40:41], v215 offset:16384\n" \
"v_cvt_pk_bf16_f32 v130, v72, v73\n" \
"v_mfma_f32_16x16x32_bf16 v[88:91], a[80:83], a[208:211], v[88:91]\n" \
"v_cvt_pk_bf16_f32 v131, v74, v75\n" \
"v_mfma_f32_16x16x32_bf16 v[88:91], a[84:87], a[212:215], v[88:91]\n" \
"ds_read_b64_tr_b16 a[42:43], v215 offset:18432\n" \
"global_load_lds_dwordx4 v218, s[38:39] offset:-2048\n" \
"v_mfma_f32_16x16x32_bf16 v[92:95], a[80:83], a[216:219], v[92:95]\n" \
"ds_read_b64_tr_b16 a[44:45], v215 offset:24576\n" \
"v_mfma_f32_16x16x32_bf16 v[92:95], a[84:87], a[220:223], v[92:95]\n" \
"ds_write_b128 v145, v[128:131]\n" \
"v_mfma_f32_16x16x32_bf16 v[60:63], a[200:203], a[88:91], v[60:63]\n" \
"ds_read_b64_tr_b16 a[46:47], v215 offset:26624\n" \
"v_cvt_pk_bf16_f32 v132, v40, v41\n" \
"v_mfma_f32_16x16x32_bf16 v[60:63], a[204:207], a[92:95], v[60:63]\n" \
"ds_read_b128 a[8:11], v213 offset:256\n" \
"global_load_lds_dwordx4 v219, s[38:39] offset:-1024\n" \
"v_mfma_f32_16x16x32_bf16 v[96:99], a[88:91], a[208:211], v[96:99]\n" \
"s_add_u32 s38, s38, 0x20000\n" \
"s_addc_u32 s39, s39, 0\n" \
"v_cvt_pk_bf16_f32 v133, v42, v43\n" \
"v_mfma_f32_16x16x32_bf16 v[96:99], a[92:95], a[212:215], v[96:99]\n" \
"s_sub_u32 s75, s75, 32768\n" \
"s_cselect_b32 s75, 131072, s75\n" \
"ds_read_b128 a[12:15], v213 offset:2304\n" \
"v_mfma_f32_16x16x32_bf16 v[100:103], a[88:91], a[216:219], v[100:103]\n" \
"s_add_u32 m0, s75, s95\n" \
"ds_read_b128 a[16:19], v213 offset:4096\n" \
"v_cvt_pk_bf16_f32 v134, v44, v45\n" \
"v_mfma_f32_16x16x32_bf16 v[100:103], a[92:95], a[220:223], v[100:103]\n" \
"global_load_lds_dwordx4 v222, s[36:37] offset:0 sc1\n" \
"v_cvt_pk_bf16_f32 v135, v46, v47\n" \
"v_mfma_f32_16x16x32_bf16 v[68:71], a[200:203], a[192:195], v[68:71]\n" \
"ds_read_b128 a[20:23], v213 offset:6144\n" \
"v_cvt_pk_bf16_f32 v136, v48, v49\n" \
"v_mfma_f32_16x16x32_bf16 v[68:71], a[204:207], a[196:199], v[68:71]\n" \
"v_cvt_pk_bf16_f32 v137, v50, v51\n" \
"v_cvt_pk_bf16_f32 v138, v52, v53\n" \
"v_mfma_f32_16x16x32_bf16 v[104:107], a[192:195], a[208:211], v[104:107]\n" \
"v_cvt_pk_bf16_f32 v139, v54, v55\n" \
"v_cvt_pk_bf16_f32 v140, v56, v57\n" \
"v_mfma_f32_16x16x32_bf16 v[104:107], a[196:199], a[212:215], v[104:107]\n" \
"global_load_lds_dwordx4 v222, s[36:37] offset:1024 sc1\n" \
"v_cvt_pk_bf16_f32 v141, v58, v59\n" \
"v_mfma_f32_16x16x32_bf16 v[108:111], a[192:195], a[216:219], v[108:111]\n" \
"v_cvt_pk_bf16_f32 v142, v60, v61\n" \
"v_cvt_pk_bf16_f32 v143, v62, v63\n" \
"v_mfma_f32_16x16x32_bf16 v[108:111], a[196:199], a[220:223], v[108:111]\n" \
"s_waitcnt lgkmcnt(0)\n" \
"v_mfma_f32_16x16x32_bf16 v[0:3], a[32:35], a[0:3], 0\n" \
"ds_read_b128 a[24:27], v213 offset:4352\n" \
"v_mfma_f32_16x16x32_bf16 v[0:3], a[36:39], a[4:7], v[0:3]\n" \
"ds_read_b128 a[28:31], v213 offset:6400\n" \
"global_load_lds_dwordx4 v222, s[36:37] offset:2048 sc1\n" \
"v_mfma_f32_16x16x32_bf16 v[4:7], a[40:43], a[0:3], 0\n" \
"s_sub_u32 s74, s74, 32768\n" \
"s_cselect_b32 s74, 131072, s74\n" \
"ds_read_b64_tr_b16 a[48:49], v214 offset:16896\n" \
"v_mfma_f32_16x16x32_bf16 v[4:7], a[44:47], a[4:7], v[4:7]\n" \
"ds_read_b64_tr_b16 a[50:51], v214 offset:18944\n" \
"v_cvt_pk_bf16_f32 v64, v64, v65\n" \
"v_mfma_f32_16x16x32_bf16 v[16:19], a[32:35], a[8:11], 0\n" \
"ds_read_b64_tr_b16 a[52:53], v214 offset:25088\n" \
"global_load_lds_dwordx4 v222, s[36:37] offset:3072 sc1\n" \
"v_mfma_f32_16x16x32_bf16 v[16:19], a[36:39], a[12:15], v[16:19]\n" \
"s_add_u32 s36, s36, 0x1000\n" \
"ds_read_b64_tr_b16 a[54:55], v214 offset:27136\n" \
"v_cvt_pk_bf16_f32 v65, v66, v67\n" \
"v_mfma_f32_16x16x32_bf16 v[20:23], a[40:43], a[8:11], 0\n" \
"s_add_u32 m0, m0, 0x5000\n" \
"ds_read_b64_tr_b16 a[56:57], v215 offset:16896\n" \
"v_cvt_pk_bf16_f32 v66, v68, v69\n" \
"v_mfma_f32_16x16x32_bf16 v[20:23], a[44:47], a[12:15], v[20:23]\n" \
"ds_read_b64_tr_b16 a[58:59], v215 offset:18944\n" \
"global_load_lds_dwordx4 v216, s[38:39] offset:-4096\n" \
"v_mfma_f32_16x16x32_bf16 v[32:35], a[32:35], a[16:19], 0\n" \
"ds_read_b64_tr_b16 a[60:61], v215 offset:25088\n" \
"v_cvt_pk_bf16_f32 v67, v70, v71\n" \
"v_mfma_f32_16x16x32_bf16 v[32:35], a[36:39], a[20:23], v[32:35]\n" \
"ds_read_b64_tr_b16 a[62:63], v215 offset:27136\n" \
"v_cvt_pk_bf16_f32 v68, v72, v73\n" \
"v_mfma_f32_16x16x32_bf16 v[36:39], a[40:43], a[16:19], 0\n" \
"global_load_lds_dwordx4 v217, s[38:39] offset:-3072\n" \
"v_add_u32 v213, s74, v211\n" \
"v_mfma_f32_16x16x32_bf16 v[36:39], a[44:47], a[20:23], v[36:39]\n" \
"s_waitcnt lgkmcnt(3)\n" \
"v_add_u32 v214, s74, v212\n" \
"v_xor_b32 v215, 32, v214\n" \
"v_mfma_f32_16x16x32_bf16 v[48:51], a[32:35], a[24:27], 0\n" \
"v_cvt_pk_bf16_f32 v69, v74, v75\n" \
"v_cvt_pk_bf16_f32 v70, v80, v81\n" \
"v_mfma_f32_16x16x32_bf16 v[48:51], a[36:39], a[28:31], v[48:51]\n" \
"global_load_lds_dwordx4 v218, s[38:39] offset:-2048\n" \
"v_cvt_pk_bf16_f32 v71, v82, v83\n" \
"v_mfma_f32_16x16x32_bf16 v[52:55], a[40:43], a[24:27], 0\n" \
"s_waitcnt vmcnt(15) lgkmcnt(0)\n" \
"global_load_lds_dwordx4 v219, s[38:39] offset:-1024\n" \
"v_cvt_pk_bf16_f32 v194, v84, v85\n" \
"v_mfma_f32_16x16x32_bf16 v[52:55], a[44:47], a[28:31], v[52:55]\n" \
"s_barrier\n" \
"v_cvt_pk_bf16_f32 v195, v86, v87\n" \
"v_mfma_f32_16x16x32_bf16 v[8:11], a[48:51], a[0:3], 0\n" \
"ds_read_b64 v[130:131], v223\n" \
"ds_read_b64_tr_b16 a[32:33], v214 offset:16384\n" \
"v_mfma_f32_16x16x32_bf16 v[8:11], a[52:55], a[4:7], v[8:11]\n" \
"ds_read_b64 v[192:193], v223 offset:8\n" \
"ds_read_b64_tr_b16 a[34:35], v214 offset:18432\n" \
"v_mfma_f32_16x16x32_bf16 v[12:15], a[56:59], a[0:3], 0\n" \
"s_add_u32 s38, s38, 0x20000\n" \
"s_addc_u32 s39, s39, 0\n" \
"ds_read_b64_tr_b16 a[36:37], v214 offset:24576\n" \
"v_mfma_f32_16x16x32_bf16 v[12:15], a[60:63], a[4:7], v[12:15]\n" \
"s_sub_u32 s75, s75, 32768\n" \
"s_cselect_b32 s75, 131072, s75\n" \
"ds_read_b128 a[0:3], v213 offset:0\n" \
"v_mfma_f32_16x16x32_bf16 v[24:27], a[48:51], a[8:11], 0\n" \
"s_add_u32 m0, s75, s95\n" \
"ds_read_b128 a[4:7], v213 offset:2048\n" \
"v_cvt_pk_bf16_f32 v72, v88, v89\n" \
"v_mfma_f32_16x16x32_bf16 v[24:27], a[52:55], a[12:15], v[24:27]\n" \
"ds_read_b64_tr_b16 a[38:39], v214 offset:26624\n" \
"global_load_lds_dwordx4 v222, s[36:37] offset:0 sc1\n" \
"v_mfma_f32_16x16x32_bf16 v[28:31], a[56:59], a[8:11], 0\n" \
"ds_read_b64_tr_b16 a[40:41], v215 offset:16384\n" \
"v_cvt_pk_bf16_f32 v73, v90, v91\n" \
"v_mfma_f32_16x16x32_bf16 v[28:31], a[60:63], a[12:15], v[28:31]\n" \
"s_waitcnt lgkmcnt(4)\n" \
"ds_read_b128 a[8:11], v213 offset:256\n" \
"global_load_lds_dwordx4 v222, s[36:37] offset:1024 sc1\n" \
"v_mfma_f32_16x16x32_bf16 v[40:43], a[48:51], a[16:19], 0\n" \
"ds_read_b128 a[12:15], v213 offset:2304\n" \
"v_cvt_pk_bf16_f32 v196, v92, v93\n" \
"v_mfma_f32_16x16x32_bf16 v[40:43], a[52:55], a[20:23], v[40:43]\n" \
"ds_read_b64_tr_b16 a[42:43], v215 offset:18432\n" \
"v_cvt_pk_bf16_f32 v197, v94, v95\n" \
"v_mfma_f32_16x16x32_bf16 v[44:47], a[56:59], a[16:19], 0\n" \
"ds_read_b64_tr_b16 a[44:45], v215 offset:24576\n" \
"global_load_lds_dwordx4 v222, s[36:37] offset:2048 sc1\n" \
"v_mfma_f32_16x16x32_bf16 v[44:47], a[60:63], a[20:23], v[44:47]\n" \
"ds_read_b128 a[16:19], v213 offset:4096\n" \
"v_cvt_pk_bf16_f32 v74, v96, v97\n" \
"v_mfma_f32_16x16x32_bf16 v[56:59], a[48:51], a[24:27], 0\n" \
"ds_read_b128 a[20:23], v213 offset:6144\n" \
"global_load_lds_dwordx4 v222, s[36:37] offset:3072 sc1\n" \
"v_mfma_f32_16x16x32_bf16 v[56:59], a[52:55], a[28:31], v[56:59]\n" \
"ds_read_b64_tr_b16 a[46:47], v215 offset:26624\n" \
"v_cvt_pk_bf16_f32 v75, v98, v99\n" \
"v_mfma_f32_16x16x32_bf16 v[60:63], a[56:59], a[24:27], 0\n" \
"v_cvt_pk_bf16_f32 v198, v100, v101\n" \
"v_cvt_pk_bf16_f32 v199, v102, v103\n" \
"v_mfma_f32_16x16x32_bf16 v[60:63], a[60:63], a[28:31], v[60:63]\n" \
"s_waitcnt lgkmcnt(3)\n" \
"v_cvt_pk_bf16_f32 v76, v104, v105\n" \
"v_cvt_pk_bf16_f32 v77, v106, v107\n" \
"v_mfma_f32_16x16x32_bf16 v[0:3], a[32:35], a[0:3], v[0:3]\n" \
"v_cvt_pk_bf16_f32 v200, v108, v109\n" \
"v_cvt_pk_bf16_f32 v201, v110, v111\n" \
"v_mfma_f32_16x16x32_bf16 v[176:179], v[64:67], v[124:127], v[176:179]\n" \
"v_mfma_f32_16x16x32_bf16 v[172:175], v[140:143], v[124:127], v[172:175]\n" \
"s_add_u32 s36, s36, 0x1000\n" \
"ds_read_b128 a[24:27], v213 offset:4352\n" \
"s_add_u32 m0, m0, 0x5000\n" \
"v_mfma_f32_16x16x32_bf16 v[0:3], a[36:39], a[4:7], v[0:3]\n" \
"s_waitcnt lgkmcnt(1)\n" \
"ds_read_b128 a[28:31], v213 offset:6400\n" \
"global_load_lds_dwordx4 v216, s[38:39] offset:-4096\n" \
"v_mfma_f32_16x16x32_bf16 v[168:171], v[140:143], v[120:123], v[168:171]\n" \
"v_mfma_f32_16x16x32_bf16 v[164:167], v[136:139], v[120:123], v[164:167]\n" \
"v_mfma_f32_16x16x32_bf16 v[160:163], v[136:139], v[116:119], v[160:163]\n" \
"v_mfma_f32_16x16x32_bf16 v[156:159], v[132:135], v[116:119], v[156:159]\n" \
"v_mfma_f32_16x16x32_bf16 v[152:155], v[132:135], v[112:115], v[152:155]\n" \
"v_mfma_f32_16x16x32_bf16 v[148:151], v[128:131], v[112:115], v[148:151]\n" \
"v_mfma_f32_16x16x32_bf16 v[4:7], a[40:43], a[0:3], v[4:7]\n" \
"s_sub_u32 s74, s74, 32768\n" \
"s_cselect_b32 s74, 131072, s74\n" \
"ds_read_b64_tr_b16 a[48:49], v214 offset:16896\n" \
"v_mfma_f32_16x16x32_bf16 v[4:7], a[44:47], a[4:7], v[4:7]\n" \
"ds_read_b64_tr_b16 a[50:51], v214 offset:18944\n" \
"v_max3_f32 v183, v172, v173, v174\n" \
"v_max3_f32 v182, v164, v165, v166\n" \
"v_max3_f32 v181, v156, v157, v158\n" \
"v_max3_f32 v180, v148, v149, v150\n" \
"v_max3_f32 v183, v183, v175, v176\n" \
"v_max3_f32 v182, v182, v167, v168\n" \
"v_mfma_f32_16x16x32_bf16 v[16:19], a[32:35], a[8:11], v[16:19]\n" \
"ds_read_b64_tr_b16 a[52:53], v214 offset:25088\n" \
"global_load_lds_dwordx4 v217, s[38:39] offset:-3072\n" \
"v_max3_f32 v181, v181, v159, v160\n" \
"v_max3_f32 v180, v180, v151, v152\n" \
"v_max3_f32 v183, v183, v177, v178\n" \
"v_max3_f32 v182, v182, v169, v170\n" \
"v_max3_f32 v181, v181, v161, v162\n" \
"v_max3_f32 v180, v180, v153, v154\n" \
"v_mfma_f32_16x16x32_bf16 v[16:19], a[36:39], a[12:15], v[16:19]\n" \
"ds_read_b64_tr_b16 a[54:55], v214 offset:27136\n" \
"v_max_f32 v183, v183, v179\n" \
"v_max_f32 v182, v182, v171\n" \
"v_max_f32 v181, v181, v163\n" \
"v_max_f32 v180, v180, v155\n" \
"v_mov_b32 v187, v183\n" \
"v_mov_b32 v186, v182\n" \
"v_mfma_f32_16x16x32_bf16 v[20:23], a[40:43], a[8:11], v[20:23]\n" \
"ds_read_b64_tr_b16 a[56:57], v215 offset:16896\n" \
"v_mov_b32 v185, v181\n" \
"v_mov_b32 v184, v180\n" \
"v_permlane16_swap_b32 v187, v183\n" \
"v_permlane16_swap_b32 v186, v182\n" \
"v_permlane16_swap_b32 v185, v181\n" \
"v_permlane16_swap_b32 v184, v180\n" \
"v_mfma_f32_16x16x32_bf16 v[20:23], a[44:47], a[12:15], v[20:23]\n" \
"ds_read_b64_tr_b16 a[58:59], v215 offset:18944\n" \
"global_load_lds_dwordx4 v218, s[38:39] offset:-2048\n" \
"v_max_f32 v183, v183, v187\n" \
"v_max_f32 v182, v182, v186\n" \
"v_max_f32 v181, v181, v185\n" \
"v_max_f32 v180, v180, v184\n" \
"v_mov_b32 v187, v183\n" \
"v_mov_b32 v186, v182\n" \
"v_mfma_f32_16x16x32_bf16 v[32:35], a[32:35], a[16:19], v[32:35]\n" \
"ds_read_b64_tr_b16 a[60:61], v215 offset:25088\n" \
"v_mov_b32 v185, v181\n" \
"v_mov_b32 v184, v180\n" \
"v_permlane32_swap_b32 v187, v183\n" \
"v_permlane32_swap_b32 v186, v182\n" \
"v_permlane32_swap_b32 v185, v181\n" \
"v_permlane32_swap_b32 v184, v180\n" \
"v_mfma_f32_16x16x32_bf16 v[32:35], a[36:39], a[20:23], v[32:35]\n" \
"ds_read_b64_tr_b16 a[62:63], v215 offset:27136\n" \
"v_max_f32 v183, v183, v187\n" \
"v_max_f32 v182, v182, v186\n" \
"v_max_f32 v181, v181, v185\n" \
"v_max_f32 v180, v180, v184\n" \
"v_mul_f32 v183, v183, v144\n" \
"v_mul_f32 v182, v182, v144\n" \
"v_mfma_f32_16x16x32_bf16 v[36:39], a[40:43], a[16:19], v[36:39]\n" \
"global_load_lds_dwordx4 v219, s[38:39] offset:-1024\n" \
"v_add_u32 v213, s74, v211\n" \
"v_mul_f32 v181, v181, v144\n" \
"v_mul_f32 v180, v180, v144\n" \
"v_fma_f32 v172, v172, v144, -v183\n" \
"v_fma_f32 v164, v164, v144, -v182\n" \
"v_fma_f32 v156, v156, v144, -v181\n" \
"v_mfma_f32_16x16x32_bf16 v[36:39], a[44:47], a[20:23], v[36:39]\n" \
"s_waitcnt lgkmcnt(3)\n" \
"v_exp_f32 v172, v172\n" \
"v_fma_f32 v148, v148, v144, -v180\n" \
"s_add_u32 s38, s38, 0x20000\n" \
"s_addc_u32 s39, s39, 0\n" \
"v_mfma_f32_16x16x32_bf16 v[48:51], a[32:35], a[24:27], v[48:51]\n" \
"s_sub_u32 s75, s75, 32768\n" \
"s_cselect_b32 s75, 131072, s75\n" \
"v_exp_f32 v164, v164\n" \
"s_add_u32 m0, s75, s95\n" \
"v_mfma_f32_16x16x32_bf16 v[48:51], a[36:39], a[28:31], v[48:51]\n" \
"global_load_lds_dwordx4 v222, s[36:37] offset:0 sc1\n" \
"v_add_u32 v214, s74, v212\n" \
"v_fma_f32 v173, v173, v144, -v183\n" \
"v_mfma_f32_16x16x32_bf16 v[52:55], a[40:43], a[24:27], v[52:55]\n" \
"s_waitcnt vmcnt(17) lgkmcnt(0)\n" \
"v_xor_b32 v215, 32, v214\n" \
"v_exp_f32 v156, v156\n" \
"v_mfma_f32_16x16x32_bf16 v[52:55], a[44:47], a[28:31], v[52:55]\n" \
"s_barrier\n" \
"v_fma_f32 v165, v165, v144, -v182\n" \
"v_mfma_f32_16x16x32_bf16 v[8:11], a[48:51], a[0:3], v[8:11]\n" \
"ds_read_b64_tr_b16 a[32:33], v214 offset:16384\n" \
"v_exp_f32 v148, v148\n" \
"v_fma_f32 v157, v157, v144, -v181\n" \
"v_mfma_f32_16x16x32_bf16 v[8:11], a[52:55], a[4:7], v[8:11]\n" \
"ds_read_b64_tr_b16 a[34:35], v214 offset:18432\n" \
"global_load_lds_dwordx4 v222, s[36:37] offset:1024 sc1\n" \
"v_mfma_f32_16x16x32_bf16 v[12:15], a[56:59], a[0:3], v[12:15]\n" \
"ds_read_b64_tr_b16 a[36:37], v214 offset:24576\n" \
"v_exp_f32 v173, v173\n" \
"v_fma_f32 v149, v149, v144, -v180\n" \
"v_mfma_f32_16x16x32_bf16 v[12:15], a[60:63], a[4:7], v[12:15]\n" \
"ds_read_b128 a[0:3], v213 offset:0\n" \
"v_exp_f32 v165, v165\n" \
"v_mfma_f32_16x16x32_bf16 v[24:27], a[48:51], a[8:11], v[24:27]\n" \
"ds_read_b128 a[4:7], v213 offset:2048\n" \
"global_load_lds_dwordx4 v222, s[36:37] offset:2048 sc1\n" \
"v_fma_f32 v174, v174, v144, -v183\n" \
"v_mfma_f32_16x16x32_bf16 v[24:27], a[52:55], a[12:15], v[24:27]\n" \
"ds_read_b64_tr_b16 a[38:39], v214 offset:26624\n" \
"v_exp_f32 v157, v157\n" \
"v_mfma_f32_16x16x32_bf16 v[28:31], a[56:59], a[8:11], v[28:31]\n" \
"ds_read_b64_tr_b16 a[40:41], v215 offset:16384\n" \
"v_fma_f32 v166, v166, v144, -v182\n" \
"v_mfma_f32_16x16x32_bf16 v[28:31], a[60:63], a[12:15], v[28:31]\n" \
"ds_read_b128 a[8:11], v213 offset:256\n" \
"global_load_lds_dwordx4 v222, s[36:37] offset:3072 sc1\n" \
"v_exp_f32 v149, v149\n" \
"v_fma_f32 v158, v158, v144, -v181\n" \
"v_mfma_f32_16x16x32_bf16 v[40:43], a[48:51], a[16:19], v[40:43]\n" \
"s_add_u32 s36, s36, 0x1000\n" \
"ds_read_b128 a[12:15], v213 offset:2304\n" \
"v_exp_f32 v174, v174\n" \
"v_mfma_f32_16x16x32_bf16 v[40:43], a[52:55], a[20:23], v[40:43]\n" \
"s_add_u32 m0, m0, 0x5000\n" \
"ds_read_b64_tr_b16 a[42:43], v215 offset:18432\n" \
"v_add_f32 v191, v172, v173\n" \
"v_fma_f32 v150, v150, v144, -v180\n" \
"v_mfma_f32_16x16x32_bf16 v[44:47], a[56:59], a[16:19], v[44:47]\n" \
"ds_read_b64_tr_b16 a[44:45], v215 offset:24576\n" \
"global_load_lds_dwordx4 v216, s[38:39] offset:-4096\n" \
"v_mfma_f32_16x16x32_bf16 v[44:47], a[60:63], a[20:23], v[44:47]\n" \
"ds_read_b128 a[16:19], v213 offset:4096\n" \
"v_exp_f32 v166, v166\n" \
"v_add_f32 v190, v164, v165\n" \
"v_mfma_f32_16x16x32_bf16 v[56:59], a[48:51], a[24:27], v[56:59]\n" \
"ds_read_b128 a[20:23], v213 offset:6144\n" \
"global_load_lds_dwordx4 v217, s[38:39] offset:-3072\n" \
"v_fma_f32 v175, v175, v144, -v183\n" \
"v_mfma_f32_16x16x32_bf16 v[56:59], a[52:55], a[28:31], v[56:59]\n" \
"ds_read_b64_tr_b16 a[46:47], v215 offset:26624\n" \
"v_exp_f32 v158, v158\n" \
"v_mfma_f32_16x16x32_bf16 v[60:63], a[56:59], a[24:27], v[60:63]\n" \
"v_add_f32 v189, v156, v157\n" \
"v_fma_f32 v167, v167, v144, -v182\n" \
"v_mfma_f32_16x16x32_bf16 v[60:63], a[60:63], a[28:31], v[60:63]\n" \
"s_waitcnt lgkmcnt(3)\n" \
"v_exp_f32 v150, v150\n" \
"v_mfma_f32_16x16x32_bf16 v[0:3], a[32:35], a[0:3], v[0:3]\n" \
"ds_read_b128 a[24:27], v213 offset:4352\n" \
"v_add_f32 v188, v148, v149\n" \
"v_fma_f32 v159, v159, v144, -v181\n" \
"v_mfma_f32_16x16x32_bf16 v[0:3], a[36:39], a[4:7], v[0:3]\n" \
"s_waitcnt lgkmcnt(1)\n" \
"ds_read_b128 a[28:31], v213 offset:6400\n" \
"global_load_lds_dwordx4 v218, s[38:39] offset:-2048\n" \
"v_exp_f32 v175, v175\n" \
"v_mfma_f32_16x16x32_bf16 v[4:7], a[40:43], a[0:3], v[4:7]\n" \
"s_sub_u32 s74, s74, 32768\n" \
"s_cselect_b32 s74, 131072, s74\n" \
"ds_read_b64_tr_b16 a[48:49], v214 offset:16896\n" \
"v_add_f32 v191, v191, v174\n" \
"v_fma_f32 v151, v151, v144, -v180\n" \
"v_mfma_f32_16x16x32_bf16 v[4:7], a[44:47], a[4:7], v[4:7]\n" \
"ds_read_b64_tr_b16 a[50:51], v214 offset:18944\n" \
"global_load_lds_dwordx4 v219, s[38:39] offset:-1024\n" \
"v_mfma_f32_16x16x32_bf16 v[16:19], a[32:35], a[8:11], v[16:19]\n" \
"s_add_u32 s38, s38, 0x20000\n" \
"s_addc_u32 s39, s39, 0\n" \
"ds_read_b64_tr_b16 a[52:53], v214 offset:25088\n" \
"v_exp_f32 v167, v167\n" \
"v_add_f32 v190, v190, v166\n" \
"v_mfma_f32_16x16x32_bf16 v[16:19], a[36:39], a[12:15], v[16:19]\n" \
"s_sub_u32 s75, s75, 32768\n" \
"s_cselect_b32 s75, 131072, s75\n" \
"ds_read_b64_tr_b16 a[54:55], v214 offset:27136\n" \
"v_fma_f32 v176, v176, v144, -v183\n" \
"v_mfma_f32_16x16x32_bf16 v[20:23], a[40:43], a[8:11], v[20:23]\n" \
"s_add_u32 m0, s75, s95\n" \
"ds_read_b64_tr_b16 a[56:57], v215 offset:16896\n" \
"v_exp_f32 v159, v159\n" \
"v_add_f32 v189, v189, v158\n" \
"v_mfma_f32_16x16x32_bf16 v[20:23], a[44:47], a[12:15], v[20:23]\n" \
"ds_read_b64_tr_b16 a[58:59], v215 offset:18944\n" \
"global_load_lds_dwordx4 v222, s[36:37] offset:0 sc1\n" \
"v_fma_f32 v168, v168, v144, -v182\n" \
"v_mfma_f32_16x16x32_bf16 v[32:35], a[32:35], a[16:19], v[32:35]\n" \
"Lpg11579_%=:\n" \
"s_cbranch_execz Lpg12736_%=\n" \
"ds_read_b64_tr_b16 a[60:61], v215 offset:25088\n" \
"v_exp_f32 v151, v151\n" \
"v_mfma_f32_16x16x32_bf16 v[32:35], a[36:39], a[20:23], v[32:35]\n" \
"ds_read_b64_tr_b16 a[62:63], v215 offset:27136\n" \
"v_add_f32 v188, v188, v150\n" \
"v_fma_f32 v160, v160, v144, -v181\n" \
"v_mfma_f32_16x16x32_bf16 v[36:39], a[40:43], a[16:19], v[36:39]\n" \
"global_load_lds_dwordx4 v222, s[36:37] offset:1024 sc1\n" \
"v_add_u32 v213, s74, v211\n" \
"v_exp_f32 v176, v176\n" \
"v_mfma_f32_16x16x32_bf16 v[36:39], a[44:47], a[20:23], v[36:39]\n" \
"s_waitcnt lgkmcnt(3)\n" \
"v_add_u32 v214, s74, v212\n" \
"v_xor_b32 v215, 32, v214\n" \
"v_mfma_f32_16x16x32_bf16 v[48:51], a[32:35], a[24:27], v[48:51]\n" \
"v_add_f32 v191, v191, v175\n" \
"v_fma_f32 v152, v152, v144, -v180\n" \
"v_mfma_f32_16x16x32_bf16 v[48:51], a[36:39], a[28:31], v[48:51]\n" \
"global_load_lds_dwordx4 v222, s[36:37] offset:2048 sc1\n" \
"v_exp_f32 v168, v168\n" \
"v_mfma_f32_16x16x32_bf16 v[52:55], a[40:43], a[24:27], v[52:55]\n" \
"s_waitcnt vmcnt(19) lgkmcnt(0)\n" \
"v_add_f32 v190, v190, v167\n" \
"v_fma_f32 v177, v177, v144, -v183\n" \
"v_mfma_f32_16x16x32_bf16 v[52:55], a[44:47], a[28:31], v[52:55]\n" \
"s_barrier\n" \
"v_exp_f32 v160, v160\n" \
"v_mfma_f32_16x16x32_bf16 v[8:11], a[48:51], a[0:3], v[8:11]\n" \
"ds_read_b64_tr_b16 a[32:33], v214 offset:16384\n" \
"v_add_f32 v189, v189, v159\n" \
"v_fma_f32 v169, v169, v144, -v182\n" \
"v_mfma_f32_16x16x32_bf16 v[8:11], a[52:55], a[4:7], v[8:11]\n" \
"ds_read_b64_tr_b16 a[34:35], v214 offset:18432\n" \
"global_load_lds_dwordx4 v222, s[36:37] offset:3072 sc1\n" \
"v_exp_f32 v152, v152\n" \
"v_mfma_f32_16x16x32_bf16 v[12:15], a[56:59], a[0:3], v[12:15]\n" \
"s_add_u32 s36, s36, 0x1000\n" \
"ds_read_b64_tr_b16 a[36:37], v214 offset:24576\n" \
"v_add_f32 v188, v188, v151\n" \
"v_fma_f32 v161, v161, v144, -v181\n" \
"v_mfma_f32_16x16x32_bf16 v[12:15], a[60:63], a[4:7], v[12:15]\n" \
"s_add_u32 m0, m0, 0x5000\n" \
"ds_read_b128 a[0:3], v213 offset:0\n" \
"v_exp_f32 v177, v177\n" \
"v_mfma_f32_16x16x32_bf16 v[24:27], a[48:51], a[8:11], v[24:27]\n" \
"ds_read_b128 a[4:7], v213 offset:2048\n" \
"global_load_lds_dwordx4 v216, s[38:39] offset:-4096\n" \
"v_add_f32 v191, v191, v176\n" \
"v_mfma_f32_16x16x32_bf16 v[24:27], a[52:55], a[12:15], v[24:27]\n" \
"ds_read_b64_tr_b16 a[38:39], v214 offset:26624\n" \
"v_fma_f32 v153, v153, v144, -v180\n" \
"v_mfma_f32_16x16x32_bf16 v[28:31], a[56:59], a[8:11], v[28:31]\n" \
"ds_read_b64_tr_b16 a[40:41], v215 offset:16384\n" \
"v_exp_f32 v169, v169\n" \
"v_add_f32 v190, v190, v168\n" \
"v_mfma_f32_16x16x32_bf16 v[28:31], a[60:63], a[12:15], v[28:31]\n" \
"ds_read_b128 a[8:11], v213 offset:256\n" \
"global_load_lds_dwordx4 v217, s[38:39] offset:-3072\n" \
"v_fma_f32 v178, v178, v144, -v183\n" \
"v_mfma_f32_16x16x32_bf16 v[40:43], a[48:51], a[16:19], v[40:43]\n" \
"ds_read_b128 a[12:15], v213 offset:2304\n" \
"v_exp_f32 v161, v161\n" \
"v_mfma_f32_16x16x32_bf16 v[40:43], a[52:55], a[20:23], v[40:43]\n" \
"ds_read_b64_tr_b16 a[42:43], v215 offset:18432\n" \
"v_add_f32 v189, v189, v160\n" \
"v_fma_f32 v170, v170, v144, -v182\n" \
"v_mfma_f32_16x16x32_bf16 v[44:47], a[56:59], a[16:19], v[44:47]\n" \
"ds_read_b64_tr_b16 a[44:45], v215 offset:24576\n" \
"global_load_lds_dwordx4 v218, s[38:39] offset:-2048\n" \
"v_exp_f32 v153, v153\n" \
"v_mfma_f32_16x16x32_bf16 v[44:47], a[60:63], a[20:23], v[44:47]\n" \
"ds_read_b128 a[16:19], v213 offset:4096\n" \
"v_add_f32 v188, v188, v152\n" \
"v_fma_f32 v162, v162, v144, -v181\n" \
"v_mfma_f32_16x16x32_bf16 v[56:59], a[48:51], a[24:27], v[56:59]\n" \
"ds_read_b128 a[20:23], v213 offset:6144\n" \
"global_load_lds_dwordx4 v219, s[38:39] offset:-1024\n" \
"v_mfma_f32_16x16x32_bf16 v[56:59], a[52:55], a[28:31], v[56:59]\n" \
"s_add_u32 s38, s38, 0x20000\n" \
"s_addc_u32 s39, s39, 0\n" \
"ds_read_b64_tr_b16 a[46:47], v215 offset:26624\n" \
"v_exp_f32 v178, v178\n" \
"v_add_f32 v191, v191, v177\n" \
"v_mfma_f32_16x16x32_bf16 v[60:63], a[56:59], a[24:27], v[60:63]\n" \
"s_sub_u32 s75, s75, 32768\n" \
"s_cselect_b32 s75, 131072, s75\n" \
"v_fma_f32 v154, v154, v144, -v180\n" \
"v_mfma_f32_16x16x32_bf16 v[60:63], a[60:63], a[28:31], v[60:63]\n" \
"s_waitcnt lgkmcnt(3)\n" \
"v_exp_f32 v170, v170\n" \
"v_add_f32 v190, v190, v169\n" \
"v_mfma_f32_16x16x32_bf16 v[0:3], a[32:35], a[0:3], v[0:3]\n" \
"s_add_u32 m0, s75, s95\n" \
"ds_read_b128 a[24:27], v213 offset:4352\n" \
"v_fma_f32 v179, v179, v144, -v183\n" \
"v_mfma_f32_16x16x32_bf16 v[0:3], a[36:39], a[4:7], v[0:3]\n" \
"s_waitcnt lgkmcnt(1)\n" \
"ds_read_b128 a[28:31], v213 offset:6400\n" \
"global_load_lds_dwordx4 v222, s[36:37] offset:0 sc1\n" \
"v_exp_f32 v162, v162\n" \
"v_mfma_f32_16x16x32_bf16 v[4:7], a[40:43], a[0:3], v[4:7]\n" \
"s_sub_u32 s74, s74, 32768\n" \
"s_cselect_b32 s74, 131072, s74\n" \
"ds_read_b64_tr_b16 a[48:49], v214 offset:16896\n" \
"v_add_f32 v189, v189, v161\n" \
"v_fma_f32 v171, v171, v144, -v182\n" \
"v_mfma_f32_16x16x32_bf16 v[4:7], a[44:47], a[4:7], v[4:7]\n" \
"ds_read_b64_tr_b16 a[50:51], v214 offset:18944\n" \
"v_exp_f32 v154, v154\n" \
"v_mfma_f32_16x16x32_bf16 v[16:19], a[32:35], a[8:11], v[16:19]\n" \
"ds_read_b64_tr_b16 a[52:53], v214 offset:25088\n" \
"v_add_f32 v188, v188, v153\n" \
"v_fma_f32 v163, v163, v144, -v181\n" \
"v_mfma_f32_16x16x32_bf16 v[16:19], a[36:39], a[12:15], v[16:19]\n" \
"ds_read_b64_tr_b16 a[54:55], v214 offset:27136\n" \
"global_load_lds_dwordx4 v222, s[36:37] offset:1024 sc1\n" \
"v_mfma_f32_16x16x32_bf16 v[20:23], a[40:43], a[8:11], v[20:23]\n" \
"ds_read_b64_tr_b16 a[56:57], v215 offset:16896\n" \
"v_exp_f32 v179, v179\n" \
"v_add_f32 v191, v191, v178\n" \
"v_mfma_f32_16x16x32_bf16 v[20:23], a[44:47], a[12:15], v[20:23]\n" \
"ds_read_b64_tr_b16 a[58:59], v215 offset:18944\n" \
"v_fma_f32 v155, v155, v144, -v180\n" \
"v_mfma_f32_16x16x32_bf16 v[32:35], a[32:35], a[16:19], v[32:35]\n" \
"ds_read_b64_tr_b16 a[60:61], v215 offset:25088\n" \
"v_exp_f32 v171, v171\n" \
"v_add_f32 v190, v190, v170\n" \
"v_mfma_f32_16x16x32_bf16 v[32:35], a[36:39], a[20:23], v[32:35]\n" \
"ds_read_b64_tr_b16 a[62:63], v215 offset:27136\n" \
"global_load_lds_dwordx4 v222, s[36:37] offset:2048 sc1\n" \
"v_mfma_f32_16x16x32_bf16 v[36:39], a[40:43], a[16:19], v[36:39]\n" \
"v_add_u32 v213, s74, v211\n" \
"v_add_u32 v214, s74, v212\n" \
"v_exp_f32 v163, v163\n" \
"v_add_f32 v189, v189, v162\n" \
"v_mfma_f32_16x16x32_bf16 v[36:39], a[44:47], a[20:23], v[36:39]\n" \
"s_waitcnt lgkmcnt(3)\n" \
"v_xor_b32 v215, 32, v214\n" \
"v_exp_f32 v155, v155\n" \
"v_mfma_f32_16x16x32_bf16 v[48:51], a[32:35], a[24:27], v[48:51]\n" \
"v_add_f32 v188, v188, v154\n" \
"v_add_f32 v191, v191, v179\n" \
"v_mfma_f32_16x16x32_bf16 v[48:51], a[36:39], a[28:31], v[48:51]\n" \
"global_load_lds_dwordx4 v222, s[36:37] offset:3072 sc1\n" \
"v_add_f32 v190, v190, v171\n" \
"v_mfma_f32_16x16x32_bf16 v[52:55], a[40:43], a[24:27], v[52:55]\n" \
"s_waitcnt vmcnt(20) lgkmcnt(0)\n" \
"v_add_f32 v189, v189, v163\n" \
"v_add_f32 v188, v188, v155\n" \
"s_add_u32 s36, s36, 0x1000\n" \
"v_mfma_f32_16x16x32_bf16 v[52:55], a[44:47], a[28:31], v[52:55]\n" \
"s_barrier\n" \
"v_mov_b32 v187, v191\n" \
"v_mov_b32 v186, v190\n" \
"v_mfma_f32_16x16x32_bf16 v[8:11], a[48:51], a[0:3], v[8:11]\n" \
"s_add_u32 m0, m0, 0x5000\n" \
"ds_read_b64_tr_b16 a[32:33], v214 offset:16384\n" \
"v_mov_b32 v185, v189\n" \
"v_mov_b32 v184, v188\n" \
"v_mfma_f32_16x16x32_bf16 v[8:11], a[52:55], a[4:7], v[8:11]\n" \
"ds_read_b64_tr_b16 a[34:35], v214 offset:18432\n" \
"global_load_lds_dwordx4 v216, s[38:39] offset:-4096\n" \
"v_mfma_f32_16x16x32_bf16 v[12:15], a[56:59], a[0:3], v[12:15]\n" \
"ds_read_b64_tr_b16 a[36:37], v214 offset:24576\n" \
"v_permlane16_swap_b32 v187, v191\n" \
"v_mfma_f32_16x16x32_bf16 v[12:15], a[60:63], a[4:7], v[12:15]\n" \
"ds_read_b128 a[0:3], v213 offset:0\n" \
"v_permlane16_swap_b32 v186, v190\n" \
"v_mfma_f32_16x16x32_bf16 v[24:27], a[48:51], a[8:11], v[24:27]\n" \
"ds_read_b128 a[4:7], v213 offset:2048\n" \
"v_permlane16_swap_b32 v185, v189\n" \
"v_mfma_f32_16x16x32_bf16 v[24:27], a[52:55], a[12:15], v[24:27]\n" \
"ds_read_b64_tr_b16 a[38:39], v214 offset:26624\n" \
"global_load_lds_dwordx4 v217, s[38:39] offset:-3072\n" \
"v_permlane16_swap_b32 v184, v188\n" \
"v_add_f32 v191, v191, v187\n" \
"v_mfma_f32_16x16x32_bf16 v[28:31], a[56:59], a[8:11], v[28:31]\n" \
"ds_read_b64_tr_b16 a[40:41], v215 offset:16384\n" \
"v_add_f32 v190, v190, v186\n" \
"v_mfma_f32_16x16x32_bf16 v[28:31], a[60:63], a[12:15], v[28:31]\n" \
"ds_read_b128 a[8:11], v213 offset:256\n" \
"v_add_f32 v189, v189, v185\n" \
"v_add_f32 v188, v188, v184\n" \
"v_mfma_f32_16x16x32_bf16 v[40:43], a[48:51], a[16:19], v[40:43]\n" \
"ds_read_b128 a[12:15], v213 offset:2304\n" \
"v_mov_b32 v187, v191\n" \
"v_mov_b32 v186, v190\n" \
"v_mfma_f32_16x16x32_bf16 v[40:43], a[52:55], a[20:23], v[40:43]\n" \
"ds_read_b64_tr_b16 a[42:43], v215 offset:18432\n" \
"global_load_lds_dwordx4 v218, s[38:39] offset:-2048\n" \
"v_mov_b32 v185, v189\n" \
"v_mfma_f32_16x16x32_bf16 v[44:47], a[56:59], a[16:19], v[44:47]\n" \
"ds_read_b64_tr_b16 a[44:45], v215 offset:24576\n" \
"v_mov_b32 v184, v188\n" \
"v_mfma_f32_16x16x32_bf16 v[44:47], a[60:63], a[20:23], v[44:47]\n" \
"ds_read_b128 a[16:19], v213 offset:4096\n" \
"v_permlane32_swap_b32 v187, v191\n" \
"v_mfma_f32_16x16x32_bf16 v[56:59], a[48:51], a[24:27], v[56:59]\n" \
"ds_read_b128 a[20:23], v213 offset:6144\n" \
"global_load_lds_dwordx4 v219, s[38:39] offset:-1024\n" \
"v_permlane32_swap_b32 v186, v190\n" \
"v_mfma_f32_16x16x32_bf16 v[56:59], a[52:55], a[28:31], v[56:59]\n" \
"s_add_u32 s38, s38, 0x20000\n" \
"s_addc_u32 s39, s39, 0\n" \
"ds_read_b64_tr_b16 a[46:47], v215 offset:26624\n" \
"v_permlane32_swap_b32 v185, v189\n" \
"v_mfma_f32_16x16x32_bf16 v[60:63], a[56:59], a[24:27], v[60:63]\n" \
"s_sub_u32 s75, s75, 32768\n" \
"s_cselect_b32 s75, 131072, s75\n" \
"v_permlane32_swap_b32 v184, v188\n" \
"v_mfma_f32_16x16x32_bf16 v[60:63], a[60:63], a[28:31], v[60:63]\n" \
"s_waitcnt lgkmcnt(3)\n" \
"v_add_f32 v191, v191, v187\n" \
"v_add_f32 v190, v190, v186\n" \
"v_mfma_f32_16x16x32_bf16 v[0:3], a[32:35], a[0:3], v[0:3]\n" \
"s_add_u32 m0, s75, s95\n" \
"ds_read_b128 a[24:27], v213 offset:4352\n" \
"v_add_f32 v189, v189, v185\n" \
"v_add_f32 v188, v188, v184\n" \
"v_mfma_f32_16x16x32_bf16 v[0:3], a[36:39], a[4:7], v[0:3]\n" \
"s_waitcnt lgkmcnt(1)\n" \
"ds_read_b128 a[28:31], v213 offset:6400\n" \
"global_load_lds_dwordx4 v222, s[36:37] offset:0 sc1\n" \
"v_rcp_f32 v191, v191\n" \
"v_mfma_f32_16x16x32_bf16 v[4:7], a[40:43], a[0:3], v[4:7]\n" \
"s_sub_u32 s74, s74, 32768\n" \
"s_cselect_b32 s74, 131072, s74\n" \
"ds_read_b64_tr_b16 a[48:49], v214 offset:16896\n" \
"v_mfma_f32_16x16x32_bf16 v[4:7], a[44:47], a[4:7], v[4:7]\n" \
"ds_read_b64_tr_b16 a[50:51], v214 offset:18944\n" \
"v_rcp_f32 v190, v190\n" \
"v_mfma_f32_16x16x32_bf16 v[16:19], a[32:35], a[8:11], v[16:19]\n" \
"ds_read_b64_tr_b16 a[52:53], v214 offset:25088\n" \
"v_rcp_f32 v189, v189\n" \
"v_mfma_f32_16x16x32_bf16 v[16:19], a[36:39], a[12:15], v[16:19]\n" \
"ds_read_b64_tr_b16 a[54:55], v214 offset:27136\n" \
"global_load_lds_dwordx4 v222, s[36:37] offset:1024 sc1\n" \
"v_rcp_f32 v188, v188\n" \
"v_mul_f32 v172, v172, v191\n" \
"v_mfma_f32_16x16x32_bf16 v[20:23], a[40:43], a[8:11], v[20:23]\n" \
"ds_read_b64_tr_b16 a[56:57], v215 offset:16896\n" \
"v_mul_f32 v164, v164, v190\n" \
"v_mul_f32 v156, v156, v189\n" \
"v_mfma_f32_16x16x32_bf16 v[20:23], a[44:47], a[12:15], v[20:23]\n" \
"ds_read_b64_tr_b16 a[58:59], v215 offset:18944\n" \
"v_mul_f32 v148, v148, v188\n" \
"v_mfma_f32_16x16x32_bf16 v[32:35], a[32:35], a[16:19], v[32:35]\n" \
"ds_read_b64_tr_b16 a[60:61], v215 offset:25088\n" \
"v_mul_f32 v173, v173, v191\n" \
"v_mul_f32 v165, v165, v190\n" \
"v_mfma_f32_16x16x32_bf16 v[32:35], a[36:39], a[20:23], v[32:35]\n" \
"ds_read_b64_tr_b16 a[62:63], v215 offset:27136\n" \
"global_load_lds_dwordx4 v222, s[36:37] offset:2048 sc1\n" \
"v_mul_f32 v157, v157, v189\n" \
"v_mul_f32 v149, v149, v188\n" \
"v_mfma_f32_16x16x32_bf16 v[36:39], a[40:43], a[16:19], v[36:39]\n" \
"v_add_u32 v213, s74, v211\n" \
"v_add_u32 v214, s74, v212\n" \
"v_mul_f32 v174, v174, v191\n" \
"v_mul_f32 v166, v166, v190\n" \
"v_mfma_f32_16x16x32_bf16 v[36:39], a[44:47], a[20:23], v[36:39]\n" \
"s_waitcnt lgkmcnt(3)\n" \
"v_xor_b32 v215, 32, v214\n" \
"v_mul_f32 v158, v158, v189\n" \
"v_mfma_f32_16x16x32_bf16 v[48:51], a[32:35], a[24:27], v[48:51]\n" \
"v_mul_f32 v150, v150, v188\n" \
"v_mul_f32 v175, v175, v191\n" \
"v_mfma_f32_16x16x32_bf16 v[48:51], a[36:39], a[28:31], v[48:51]\n" \
"global_load_lds_dwordx4 v222, s[36:37] offset:3072 sc1\n" \
"v_mul_f32 v167, v167, v190\n" \
"v_mul_f32 v159, v159, v189\n" \
"v_mfma_f32_16x16x32_bf16 v[52:55], a[40:43], a[24:27], v[52:55]\n" \
"s_waitcnt vmcnt(20) lgkmcnt(0)\n" \
"v_mul_f32 v151, v151, v188\n" \
"v_mul_f32 v176, v176, v191\n" \
"s_add_u32 s36, s36, 0x1000\n" \
"v_mfma_f32_16x16x32_bf16 v[52:55], a[44:47], a[28:31], v[52:55]\n" \
"s_barrier\n" \
"v_mul_f32 v168, v168, v190\n" \
"v_mul_f32 v160, v160, v189\n" \
"v_mfma_f32_16x16x32_bf16 v[8:11], a[48:51], a[0:3], v[8:11]\n" \
"s_add_u32 m0, m0, 0x5000\n" \
"ds_read_b64_tr_b16 a[32:33], v214 offset:16384\n" \
"v_mul_f32 v152, v152, v188\n" \
"v_mfma_f32_16x16x32_bf16 v[8:11], a[52:55], a[4:7], v[8:11]\n" \
"ds_read_b64_tr_b16 a[34:35], v214 offset:18432\n" \
"global_load_lds_dwordx4 v216, s[38:39] offset:-4096\n" \
"v_mul_f32 v177, v177, v191\n" \
"v_mul_f32 v169, v169, v190\n" \
"v_mfma_f32_16x16x32_bf16 v[12:15], a[56:59], a[0:3], v[12:15]\n" \
"ds_read_b64_tr_b16 a[36:37], v214 offset:24576\n" \
"v_mul_f32 v161, v161, v189\n" \
"v_mul_f32 v153, v153, v188\n" \
"v_mfma_f32_16x16x32_bf16 v[12:15], a[60:63], a[4:7], v[12:15]\n" \
"ds_read_b128 a[0:3], v213 offset:0\n" \
"v_mul_f32 v178, v178, v191\n" \
"v_mul_f32 v170, v170, v190\n" \
"v_mfma_f32_16x16x32_bf16 v[24:27], a[48:51], a[8:11], v[24:27]\n" \
"ds_read_b128 a[4:7], v213 offset:2048\n" \
"v_mul_f32 v162, v162, v189\n" \
"v_mfma_f32_16x16x32_bf16 v[24:27], a[52:55], a[12:15], v[24:27]\n" \
"ds_read_b64_tr_b16 a[38:39], v214 offset:26624\n" \
"global_load_lds_dwordx4 v217, s[38:39] offset:-3072\n" \
"v_mul_f32 v154, v154, v188\n" \
"v_mul_f32 v179, v179, v191\n" \
"v_mfma_f32_16x16x32_bf16 v[28:31], a[56:59], a[8:11], v[28:31]\n" \
"ds_read_b64_tr_b16 a[40:41], v215 offset:16384\n" \
"v_mul_f32 v171, v171, v190\n" \
"v_mul_f32 v163, v163, v189\n" \
"v_mfma_f32_16x16x32_bf16 v[28:31], a[60:63], a[12:15], v[28:31]\n" \
"ds_read_b128 a[8:11], v213 offset:256\n" \
"v_mul_f32 v155, v155, v188\n" \
"v_cvt_pk_bf16_f32 v172, v172, v173\n" \
"v_mfma_f32_16x16x32_bf16 v[40:43], a[48:51], a[16:19], v[40:43]\n" \
"ds_read_b128 a[12:15], v213 offset:2304\n" \
"v_cvt_pk_bf16_f32 v173, v174, v175\n" \
"v_mfma_f32_16x16x32_bf16 v[40:43], a[52:55], a[20:23], v[40:43]\n" \
"ds_read_b64_tr_b16 a[42:43], v215 offset:18432\n" \
"global_load_lds_dwordx4 v218, s[38:39] offset:-2048\n" \
"v_cvt_pk_bf16_f32 v174, v176, v177\n" \
"v_cvt_pk_bf16_f32 v175, v178, v179\n" \
"v_mfma_f32_16x16x32_bf16 v[44:47], a[56:59], a[16:19], v[44:47]\n" \
"ds_read_b64_tr_b16 a[44:45], v215 offset:24576\n" \
"v_cvt_pk_bf16_f32 v164, v164, v165\n" \
"v_cvt_pk_bf16_f32 v165, v166, v167\n" \
"v_mfma_f32_16x16x32_bf16 v[44:47], a[60:63], a[20:23], v[44:47]\n" \
"ds_read_b128 a[16:19], v213 offset:4096\n" \
"v_cvt_pk_bf16_f32 v166, v168, v169\n" \
"v_cvt_pk_bf16_f32 v167, v170, v171\n" \
"v_mfma_f32_16x16x32_bf16 v[56:59], a[48:51], a[24:27], v[56:59]\n" \
"ds_read_b128 a[20:23], v213 offset:6144\n" \
"global_load_lds_dwordx4 v219, s[38:39] offset:-1024\n" \
"v_mfma_f32_16x16x32_bf16 v[56:59], a[52:55], a[28:31], v[56:59]\n" \
"s_add_u32 s38, s38, 0x20000\n" \
"s_addc_u32 s39, s39, 0\n" \
"ds_read_b64_tr_b16 a[46:47], v215 offset:26624\n" \
"v_mfma_f32_16x16x32_bf16 v[60:63], a[56:59], a[24:27], v[60:63]\n" \
"s_sub_u32 s75, s75, 32768\n" \
"s_cselect_b32 s75, 131072, s75\n" \
"v_cvt_pk_bf16_f32 v156, v156, v157\n" \
"v_mfma_f32_16x16x32_bf16 v[60:63], a[60:63], a[28:31], v[60:63]\n" \
"s_waitcnt lgkmcnt(3)\n" \
"v_cvt_pk_bf16_f32 v157, v158, v159\n" \
"v_cvt_pk_bf16_f32 v158, v160, v161\n" \
"v_mfma_f32_16x16x32_bf16 v[0:3], a[32:35], a[0:3], v[0:3]\n" \
"s_add_u32 m0, s75, s95\n" \
"ds_read_b128 a[24:27], v213 offset:4352\n" \
"v_cvt_pk_bf16_f32 v159, v162, v163\n" \
"v_mfma_f32_16x16x32_bf16 v[0:3], a[36:39], a[4:7], v[0:3]\n" \
"s_waitcnt lgkmcnt(1)\n" \
"ds_read_b128 a[28:31], v213 offset:6400\n" \
"global_load_lds_dwordx4 v222, s[36:37] offset:0 sc1\n" \
"v_mfma_f32_16x16x32_bf16 v[4:7], a[40:43], a[0:3], v[4:7]\n" \
"s_sub_u32 s74, s74, 32768\n" \
"s_cselect_b32 s74, 131072, s74\n" \
"ds_read_b64_tr_b16 a[48:49], v214 offset:16896\n" \
"v_mfma_f32_16x16x32_bf16 v[4:7], a[44:47], a[4:7], v[4:7]\n" \
"ds_read_b64_tr_b16 a[50:51], v214 offset:18944\n" \
"v_cvt_pk_bf16_f32 v148, v148, v149\n" \
"v_mfma_f32_16x16x32_bf16 v[16:19], a[32:35], a[8:11], v[16:19]\n" \
"ds_read_b64_tr_b16 a[52:53], v214 offset:25088\n" \
"v_cvt_pk_bf16_f32 v149, v150, v151\n" \
"v_mfma_f32_16x16x32_bf16 v[16:19], a[36:39], a[12:15], v[16:19]\n" \
"ds_read_b64_tr_b16 a[54:55], v214 offset:27136\n" \
"global_load_lds_dwordx4 v222, s[36:37] offset:1024 sc1\n" \
"v_mfma_f32_16x16x32_bf16 v[20:23], a[40:43], a[8:11], v[20:23]\n" \
"ds_read_b64_tr_b16 a[56:57], v215 offset:16896\n" \
"v_cvt_pk_bf16_f32 v150, v152, v153\n" \
"v_mfma_f32_16x16x32_bf16 v[20:23], a[44:47], a[12:15], v[20:23]\n" \
"ds_read_b64_tr_b16 a[58:59], v215 offset:18944\n" \
"v_cvt_pk_bf16_f32 v151, v154, v155\n" \
"v_mfma_f32_16x16x32_bf16 v[32:35], a[32:35], a[16:19], v[32:35]\n" \
"v_mfma_f32_16x16x32_bf16 v[136:139], v[74:77], v[172:175], 0\n" \
"v_mfma_f32_16x16x32_bf16 v[140:143], v[198:201], v[172:175], 0\n" \
"v_mfma_f32_16x16x32_bf16 v[128:131], v[72:75], v[164:167], 0\n" \
"v_mfma_f32_16x16x32_bf16 v[132:135], v[196:199], v[164:167], 0\n" \
"v_mfma_f32_16x16x32_bf16 v[120:123], v[70:73], v[156:159], 0\n" \
"v_mfma_f32_16x16x32_bf16 v[124:127], v[194:197], v[156:159], 0\n" \
"ds_read_b64_tr_b16 a[60:61], v215 offset:25088\n" \
"v_mfma_f32_16x16x32_bf16 v[32:35], a[36:39], a[20:23], v[32:35]\n" \
"v_mfma_f32_16x16x32_bf16 v[112:115], v[68:71], v[148:151], 0\n" \
"v_mfma_f32_16x16x32_bf16 v[116:119], v[192:195], v[148:151], 0\n" \
"ds_read_b64_tr_b16 a[62:63], v215 offset:27136\n" \
"global_load_lds_dwordx4 v222, s[36:37] offset:2048 sc1\n" \
"v_mfma_f32_16x16x32_bf16 v[36:39], a[40:43], a[16:19], v[36:39]\n" \
"v_add_u32 v213, s74, v211\n" \
"v_add_u32 v214, s74, v212\n" \
"v_mfma_f32_16x16x32_bf16 v[36:39], a[44:47], a[20:23], v[36:39]\n" \
"s_waitcnt lgkmcnt(3)\n" \
"v_xor_b32 v215, 32, v214\n" \
"v_cvt_pk_bf16_f32 v136, v136, v137\n" \
"v_cvt_pk_bf16_f32 v137, v138, v139\n" \
"v_mfma_f32_16x16x32_bf16 v[48:51], a[32:35], a[24:27], v[48:51]\n" \
"v_cvt_pk_bf16_f32 v138, v140, v141\n" \
"v_cvt_pk_bf16_f32 v139, v142, v143\n" \
"v_mfma_f32_16x16x32_bf16 v[48:51], a[36:39], a[28:31], v[48:51]\n" \
"global_load_lds_dwordx4 v222, s[36:37] offset:3072 sc1\n" \
"v_cndmask_b32 v140, v136, v138, s[92:93]\n" \
"v_mfma_f32_16x16x32_bf16 v[52:55], a[40:43], a[24:27], v[52:55]\n" \
"s_waitcnt vmcnt(20) lgkmcnt(0)\n" \
"v_cndmask_b32 v141, v137, v139, s[92:93]\n" \
"s_add_u32 s36, s36, 0x1000\n" \
"v_mfma_f32_16x16x32_bf16 v[52:55], a[44:47], a[28:31], v[52:55]\n" \
"s_barrier\n" \
"v_cndmask_b32 v142, v138, v136, s[92:93]\n" \
"v_mfma_f32_16x16x32_bf16 v[8:11], a[48:51], a[0:3], v[8:11]\n" \
"s_add_u32 m0, m0, 0x5000\n" \
"ds_read_b64_tr_b16 a[32:33], v214 offset:16384\n" \
"v_cndmask_b32 v143, v139, v137, s[92:93]\n" \
"v_mfma_f32_16x16x32_bf16 v[8:11], a[52:55], a[4:7], v[8:11]\n" \
"s_add_u32 s71, s87, 208896\n" \
"ds_read_b64_tr_b16 a[34:35], v214 offset:18432\n" \
"global_load_lds_dwordx4 v216, s[38:39] offset:-4096\n" \
"v_mfma_f32_16x16x32_bf16 v[12:15], a[56:59], a[0:3], v[12:15]\n" \
"ds_read_b64_tr_b16 a[36:37], v214 offset:24576\n" \
"buffer_store_dwordx4 v[140:143], v146, s[56:59], s71 offen sc1\n" \
"v_cvt_pk_bf16_f32 v128, v128, v129\n" \
"v_cvt_pk_bf16_f32 v129, v130, v131\n" \
"v_cvt_pk_bf16_f32 v130, v132, v133\n" \
"v_cvt_pk_bf16_f32 v131, v134, v135\n" \
"v_cndmask_b32 v132, v128, v130, s[92:93]\n" \
"v_mfma_f32_16x16x32_bf16 v[12:15], a[60:63], a[4:7], v[12:15]\n" \
"ds_read_b128 a[0:3], v213 offset:0\n" \
"v_cndmask_b32 v133, v129, v131, s[92:93]\n" \
"v_cndmask_b32 v134, v130, v128, s[92:93]\n" \
"v_mfma_f32_16x16x32_bf16 v[24:27], a[48:51], a[8:11], v[24:27]\n" \
"ds_read_b128 a[4:7], v213 offset:2048\n" \
"v_cndmask_b32 v135, v131, v129, s[92:93]\n" \
"v_mfma_f32_16x16x32_bf16 v[24:27], a[52:55], a[12:15], v[24:27]\n" \
"s_add_u32 s71, s87, 139264\n" \
"ds_read_b64_tr_b16 a[38:39], v214 offset:26624\n" \
"global_load_lds_dwordx4 v217, s[38:39] offset:-3072\n" \
"v_mfma_f32_16x16x32_bf16 v[28:31], a[56:59], a[8:11], v[28:31]\n" \
"ds_read_b64_tr_b16 a[40:41], v215 offset:16384\n" \
"buffer_store_dwordx4 v[132:135], v146, s[56:59], s71 offen sc1\n" \
"v_cvt_pk_bf16_f32 v120, v120, v121\n" \
"v_cvt_pk_bf16_f32 v121, v122, v123\n" \
"v_cvt_pk_bf16_f32 v122, v124, v125\n" \
"v_cvt_pk_bf16_f32 v123, v126, v127\n" \
"v_cndmask_b32 v124, v120, v122, s[92:93]\n" \
"v_mfma_f32_16x16x32_bf16 v[28:31], a[60:63], a[12:15], v[28:31]\n" \
"ds_read_b128 a[8:11], v213 offset:256\n" \
"v_cndmask_b32 v125, v121, v123, s[92:93]\n" \
"v_cndmask_b32 v126, v122, v120, s[92:93]\n" \
"v_mfma_f32_16x16x32_bf16 v[40:43], a[48:51], a[16:19], v[40:43]\n" \
"ds_read_b128 a[12:15], v213 offset:2304\n" \
"v_cndmask_b32 v127, v123, v121, s[92:93]\n" \
"v_mfma_f32_16x16x32_bf16 v[40:43], a[52:55], a[20:23], v[40:43]\n" \
"s_add_u32 s71, s87, 69632\n" \
"ds_read_b64_tr_b16 a[42:43], v215 offset:18432\n" \
"global_load_lds_dwordx4 v218, s[38:39] offset:-2048\n" \
"v_mfma_f32_16x16x32_bf16 v[44:47], a[56:59], a[16:19], v[44:47]\n" \
"ds_read_b64_tr_b16 a[44:45], v215 offset:24576\n" \
"buffer_store_dwordx4 v[124:127], v146, s[56:59], s71 offen sc1\n" \
"v_cvt_pk_bf16_f32 v112, v112, v113\n" \
"v_cvt_pk_bf16_f32 v113, v114, v115\n" \
"v_cvt_pk_bf16_f32 v114, v116, v117\n" \
"v_cvt_pk_bf16_f32 v115, v118, v119\n" \
"v_cndmask_b32 v116, v112, v114, s[92:93]\n" \
"v_mfma_f32_16x16x32_bf16 v[44:47], a[60:63], a[20:23], v[44:47]\n" \
"ds_read_b128 a[16:19], v213 offset:4096\n" \
"v_cndmask_b32 v117, v113, v115, s[92:93]\n" \
"v_cndmask_b32 v118, v114, v112, s[92:93]\n" \
"v_cndmask_b32 v119, v115, v113, s[92:93]\n" \
"v_mfma_f32_16x16x32_bf16 v[56:59], a[48:51], a[24:27], v[56:59]\n" \
"s_add_u32 s71, s87, 0\n" \
"ds_read_b128 a[20:23], v213 offset:6144\n" \
"global_load_lds_dwordx4 v219, s[38:39] offset:-1024\n" \
"v_mfma_f32_16x16x32_bf16 v[56:59], a[52:55], a[28:31], v[56:59]\n" \
"buffer_store_dwordx4 v[116:119], v146, s[56:59], s71 offen sc1\n" \
"s_add_u32 s38, s38, 0x20000\n" \
"s_addc_u32 s39, s39, 0\n" \
"ds_read_b64_tr_b16 a[46:47], v215 offset:26624\n" \
"v_mfma_f32_16x16x32_bf16 v[60:63], a[56:59], a[24:27], v[60:63]\n" \
"s_sub_u32 s75, s75, 32768\n" \
"s_cselect_b32 s75, 131072, s75\n" \
"v_mfma_f32_16x16x32_bf16 v[60:63], a[60:63], a[28:31], v[60:63]\n" \
"s_waitcnt lgkmcnt(3)\n" \
"v_mfma_f32_16x16x32_bf16 v[0:3], a[32:35], a[0:3], v[0:3]\n" \
"s_add_u32 m0, s75, s95\n" \
"ds_read_b128 a[24:27], v213 offset:4352\n" \
"v_mov_b32 v224, s60\n" \
"v_mfma_f32_16x16x32_bf16 v[0:3], a[36:39], a[4:7], v[0:3]\n" \
"s_waitcnt lgkmcnt(1)\n" \
"ds_read_b128 a[28:31], v213 offset:6400\n" \
"global_load_lds_dwordx4 v222, s[36:37] offset:0 sc1\n" \
"v_mfma_f32_16x16x32_bf16 v[4:7], a[40:43], a[0:3], v[4:7]\n" \
"s_sub_u32 s74, s74, 32768\n" \
"s_cselect_b32 s74, 131072, s74\n" \
"ds_read_b64_tr_b16 a[48:49], v214 offset:16896\n" \
"v_mfma_f32_16x16x32_bf16 v[4:7], a[44:47], a[4:7], v[4:7]\n" \
"ds_read_b64_tr_b16 a[50:51], v214 offset:18944\n" \
"v_lshlrev_b32 v225, 28, s61\n" \
"v_mfma_f32_16x16x32_bf16 v[16:19], a[32:35], a[8:11], v[16:19]\n" \
"ds_read_b64_tr_b16 a[52:53], v214 offset:25088\n" \
"v_mfma_f32_16x16x32_bf16 v[16:19], a[36:39], a[12:15], v[16:19]\n" \
"ds_read_b64_tr_b16 a[54:55], v214 offset:27136\n" \
"global_load_lds_dwordx4 v222, s[36:37] offset:1024 sc1\n" \
"v_mfma_f32_16x16x32_bf16 v[20:23], a[40:43], a[8:11], v[20:23]\n" \
"ds_read_b64_tr_b16 a[56:57], v215 offset:16896\n" \
"v_mfma_f32_16x16x32_bf16 v[20:23], a[44:47], a[12:15], v[20:23]\n" \
"ds_read_b64_tr_b16 a[58:59], v215 offset:18944\n" \
"v_mfma_f32_16x16x32_bf16 v[32:35], a[32:35], a[16:19], v[32:35]\n" \
"ds_read_b64_tr_b16 a[60:61], v215 offset:25088\n" \
"v_mfma_f32_16x16x32_bf16 v[32:35], a[36:39], a[20:23], v[32:35]\n" \
"ds_read_b64_tr_b16 a[62:63], v215 offset:27136\n" \
"global_load_lds_dwordx4 v222, s[36:37] offset:2048 sc1\n" \
"v_mfma_f32_16x16x32_bf16 v[36:39], a[40:43], a[16:19], v[36:39]\n" \
"v_add_u32 v213, s74, v211\n" \
"v_add_u32 v214, s74, v212\n" \
"v_mfma_f32_16x16x32_bf16 v[36:39], a[44:47], a[20:23], v[36:39]\n" \
"s_waitcnt lgkmcnt(3)\n" \
"v_xor_b32 v215, 32, v214\n" \
"v_mfma_f32_16x16x32_bf16 v[48:51], a[32:35], a[24:27], v[48:51]\n" \
"v_mfma_f32_16x16x32_bf16 v[48:51], a[36:39], a[28:31], v[48:51]\n" \
"global_load_lds_dwordx4 v222, s[36:37] offset:3072 sc1\n" \
"v_mfma_f32_16x16x32_bf16 v[52:55], a[40:43], a[24:27], v[52:55]\n" \
"s_waitcnt vmcnt(24) lgkmcnt(0)\n" \
"s_add_u32 s36, s36, 0x1000\n" \
"v_mfma_f32_16x16x32_bf16 v[52:55], a[44:47], a[28:31], v[52:55]\n" \
"s_barrier\n" \
"v_mfma_f32_16x16x32_bf16 v[8:11], a[48:51], a[0:3], v[8:11]\n" \
"s_add_u32 m0, m0, 0x5000\n" \
"ds_read_b64_tr_b16 a[32:33], v214 offset:16384\n" \
"v_mfma_f32_16x16x32_bf16 v[8:11], a[52:55], a[4:7], v[8:11]\n" \
"ds_read_b64_tr_b16 a[34:35], v214 offset:18432\n" \
"global_load_lds_dwordx4 v216, s[38:39] offset:-4096\n" \
"v_mfma_f32_16x16x32_bf16 v[12:15], a[56:59], a[0:3], v[12:15]\n" \
"ds_read_b64_tr_b16 a[36:37], v214 offset:24576\n" \
"v_mfma_f32_16x16x32_bf16 v[12:15], a[60:63], a[4:7], v[12:15]\n" \
"ds_read_b128 a[0:3], v213 offset:0\n" \
"v_mfma_f32_16x16x32_bf16 v[24:27], a[48:51], a[8:11], v[24:27]\n" \
"ds_read_b128 a[4:7], v213 offset:2048\n" \
"v_mfma_f32_16x16x32_bf16 v[24:27], a[52:55], a[12:15], v[24:27]\n" \
"ds_read_b64_tr_b16 a[38:39], v214 offset:26624\n" \
"global_load_lds_dwordx4 v217, s[38:39] offset:-3072\n" \
"v_mfma_f32_16x16x32_bf16 v[28:31], a[56:59], a[8:11], v[28:31]\n" \
"ds_read_b64_tr_b16 a[40:41], v215 offset:16384\n" \
"v_mfma_f32_16x16x32_bf16 v[28:31], a[60:63], a[12:15], v[28:31]\n" \
"ds_read_b128 a[8:11], v213 offset:256\n" \
"v_mfma_f32_16x16x32_bf16 v[40:43], a[48:51], a[16:19], v[40:43]\n" \
"ds_read_b128 a[12:15], v213 offset:2304\n" \
"v_mfma_f32_16x16x32_bf16 v[40:43], a[52:55], a[20:23], v[40:43]\n" \
"ds_read_b64_tr_b16 a[42:43], v215 offset:18432\n" \
"global_load_lds_dwordx4 v218, s[38:39] offset:-2048\n" \
"v_mfma_f32_16x16x32_bf16 v[44:47], a[56:59], a[16:19], v[44:47]\n" \
"ds_read_b64_tr_b16 a[44:45], v215 offset:24576\n" \
"v_mfma_f32_16x16x32_bf16 v[44:47], a[60:63], a[20:23], v[44:47]\n" \
"ds_read_b128 a[16:19], v213 offset:4096\n" \
"v_mfma_f32_16x16x32_bf16 v[56:59], a[48:51], a[24:27], v[56:59]\n" \
"ds_read_b128 a[20:23], v213 offset:6144\n" \
"global_load_lds_dwordx4 v219, s[38:39] offset:-1024\n" \
"v_mfma_f32_16x16x32_bf16 v[56:59], a[52:55], a[28:31], v[56:59]\n" \
"s_add_u32 s38, s38, 0x20000\n" \
"s_addc_u32 s39, s39, 0\n" \
"ds_read_b64_tr_b16 a[46:47], v215 offset:26624\n" \
"v_mfma_f32_16x16x32_bf16 v[60:63], a[56:59], a[24:27], v[60:63]\n" \
"s_sub_u32 s75, s75, 32768\n" \
"s_cselect_b32 s75, 131072, s75\n" \
"v_mfma_f32_16x16x32_bf16 v[60:63], a[60:63], a[28:31], v[60:63]\n" \
"s_waitcnt lgkmcnt(3)\n" \
"v_mfma_f32_16x16x32_bf16 v[0:3], a[32:35], a[0:3], v[0:3]\n" \
"ds_read_b128 a[24:27], v213 offset:4352\n" \
"s_add_u32 m0, s75, s95\n" \
"v_mfma_f32_16x16x32_bf16 v[0:3], a[36:39], a[4:7], v[0:3]\n" \
"s_waitcnt lgkmcnt(1)\n" \
"ds_read_b128 a[28:31], v213 offset:6400\n" \
"global_load_lds_dwordx4 v222, s[36:37] offset:0 sc1\n" \
"v_mfma_f32_16x16x32_bf16 v[4:7], a[40:43], a[0:3], v[4:7]\n" \
"s_sub_u32 s74, s74, 32768\n" \
"s_cselect_b32 s74, 131072, s74\n" \
"ds_read_b64_tr_b16 a[48:49], v214 offset:16896\n" \
"v_mfma_f32_16x16x32_bf16 v[4:7], a[44:47], a[4:7], v[4:7]\n" \
"ds_read_b64_tr_b16 a[50:51], v214 offset:18944\n" \
"v_mfma_f32_16x16x32_bf16 v[16:19], a[32:35], a[8:11], v[16:19]\n" \
"ds_read_b64_tr_b16 a[52:53], v214 offset:25088\n" \
"v_mfma_f32_16x16x32_bf16 v[16:19], a[36:39], a[12:15], v[16:19]\n" \
"ds_read_b64_tr_b16 a[54:55], v214 offset:27136\n" \
"global_load_lds_dwordx4 v222, s[36:37] offset:1024 sc1\n" \
"v_mfma_f32_16x16x32_bf16 v[20:23], a[40:43], a[8:11], v[20:23]\n" \
"ds_read_b64_tr_b16 a[56:57], v215 offset:16896\n" \
"v_mfma_f32_16x16x32_bf16 v[20:23], a[44:47], a[12:15], v[20:23]\n" \
"ds_read_b64_tr_b16 a[58:59], v215 offset:18944\n" \
"v_mfma_f32_16x16x32_bf16 v[32:35], a[32:35], a[16:19], v[32:35]\n" \
"ds_read_b64_tr_b16 a[60:61], v215 offset:25088\n" \
"v_mfma_f32_16x16x32_bf16 v[32:35], a[36:39], a[20:23], v[32:35]\n" \
"ds_read_b64_tr_b16 a[62:63], v215 offset:27136\n" \
"Lpg12167_%=:\n" \
"s_cbranch_execz Lpg13280_%=\n" \
"global_load_lds_dwordx4 v222, s[36:37] offset:2048 sc1\n" \
"v_mfma_f32_16x16x32_bf16 v[36:39], a[40:43], a[16:19], v[36:39]\n" \
"v_add_u32 v213, s74, v211\n" \
"v_add_u32 v214, s74, v212\n" \
"v_mfma_f32_16x16x32_bf16 v[36:39], a[44:47], a[20:23], v[36:39]\n" \
"s_waitcnt lgkmcnt(3)\n" \
"v_xor_b32 v215, 32, v214\n" \
"v_mfma_f32_16x16x32_bf16 v[48:51], a[32:35], a[24:27], v[48:51]\n" \
"v_mfma_f32_16x16x32_bf16 v[48:51], a[36:39], a[28:31], v[48:51]\n" \
"global_load_lds_dwordx4 v222, s[36:37] offset:3072 sc1\n" \
"v_mfma_f32_16x16x32_bf16 v[52:55], a[40:43], a[24:27], v[52:55]\n" \
"s_waitcnt vmcnt(12) lgkmcnt(0)\n" \
"s_add_u32 s36, s36, 0x1000\n" \
"v_mfma_f32_16x16x32_bf16 v[52:55], a[44:47], a[28:31], v[52:55]\n" \
"s_barrier\n" \
"v_mfma_f32_16x16x32_bf16 v[8:11], a[48:51], a[0:3], v[8:11]\n" \
"s_add_u32 m0, m0, 0x5000\n" \
"ds_read_b64_tr_b16 a[32:33], v214 offset:16384\n" \
"buffer_store_dword v224, v225, s[56:59], s30 offen sc1\n" \
"v_mfma_f32_16x16x32_bf16 v[8:11], a[52:55], a[4:7], v[8:11]\n" \
"ds_read_b64_tr_b16 a[34:35], v214 offset:18432\n" \
"global_load_lds_dwordx4 v216, s[38:39] offset:-4096\n" \
"v_mfma_f32_16x16x32_bf16 v[12:15], a[56:59], a[0:3], v[12:15]\n" \
"ds_read_b64_tr_b16 a[36:37], v214 offset:24576\n" \
"v_mfma_f32_16x16x32_bf16 v[12:15], a[60:63], a[4:7], v[12:15]\n" \
"ds_read_b128 a[0:3], v213 offset:0\n" \
"v_mfma_f32_16x16x32_bf16 v[24:27], a[48:51], a[8:11], v[24:27]\n" \
"ds_read_b128 a[4:7], v213 offset:2048\n" \
"v_mfma_f32_16x16x32_bf16 v[24:27], a[52:55], a[12:15], v[24:27]\n" \
"ds_read_b64_tr_b16 a[38:39], v214 offset:26624\n" \
"global_load_lds_dwordx4 v217, s[38:39] offset:-3072\n" \
"v_mfma_f32_16x16x32_bf16 v[28:31], a[56:59], a[8:11], v[28:31]\n" \
"ds_read_b64_tr_b16 a[40:41], v215 offset:16384\n" \
"v_mfma_f32_16x16x32_bf16 v[28:31], a[60:63], a[12:15], v[28:31]\n" \
"ds_read_b128 a[8:11], v213 offset:256\n" \
"v_mfma_f32_16x16x32_bf16 v[40:43], a[48:51], a[16:19], v[40:43]\n" \
"ds_read_b128 a[12:15], v213 offset:2304\n" \
"v_mfma_f32_16x16x32_bf16 v[40:43], a[52:55], a[20:23], v[40:43]\n" \
"ds_read_b64_tr_b16 a[42:43], v215 offset:18432\n" \
"global_load_lds_dwordx4 v218, s[38:39] offset:-2048\n" \
"v_mfma_f32_16x16x32_bf16 v[44:47], a[56:59], a[16:19], v[44:47]\n" \
"ds_read_b64_tr_b16 a[44:45], v215 offset:24576\n" \
"v_mfma_f32_16x16x32_bf16 v[44:47], a[60:63], a[20:23], v[44:47]\n" \
"ds_read_b128 a[16:19], v213 offset:4096\n" \
"v_mfma_f32_16x16x32_bf16 v[56:59], a[48:51], a[24:27], v[56:59]\n" \
"ds_read_b128 a[20:23], v213 offset:6144\n" \
"global_load_lds_dwordx4 v219, s[38:39] offset:-1024\n" \
"v_mfma_f32_16x16x32_bf16 v[56:59], a[52:55], a[28:31], v[56:59]\n" \
"s_add_u32 s38, s38, 0x20000\n" \
"s_addc_u32 s39, s39, 0\n" \
"ds_read_b64_tr_b16 a[46:47], v215 offset:26624\n" \
"v_mfma_f32_16x16x32_bf16 v[60:63], a[56:59], a[24:27], v[60:63]\n" \
"s_sub_u32 s75, s75, 32768\n" \
"s_cselect_b32 s75, 131072, s75\n" \
"v_mfma_f32_16x16x32_bf16 v[60:63], a[60:63], a[28:31], v[60:63]\n" \
"s_waitcnt lgkmcnt(3)\n" \
"s_cmp_eq_u32 s78, 0\n" \
"s_cbranch_scc0 Lrole1_%=\n" \
"s_sub_u32 s72, s64, 14\n" \
"s_call_b64 s[68:69], Lo0_%=\n" \
"buffer_load_dword v220, off, s[56:59], s79 sc1\n" \
"s_mov_b32 s72, 2\n" \
"s_call_b64 s[68:69], Lo0_%=\n" \
"v_mfma_f32_16x16x32_bf16 v[0:3], a[32:35], a[0:3], v[0:3]\n" \
"s_lshl_b32 s71, s61, 4\n" \
"ds_read_b128 a[24:27], v213 offset:4352\n" \
"v_subrev_u32 v72, s71, v251\n" \
"v_mfma_f32_16x16x32_bf16 v[0:3], a[36:39], a[4:7], v[0:3]\n" \
"s_waitcnt lgkmcnt(1)\n" \
"ds_read_b128 a[28:31], v213 offset:6400\n" \
"v_mov_b32 v73, 0x3f80\n" \
"v_mfma_f32_16x16x32_bf16 v[4:7], a[40:43], a[0:3], v[4:7]\n" \
"s_sub_u32 s74, s74, 32768\n" \
"s_cselect_b32 s74, 131072, s74\n" \
"ds_read_b64_tr_b16 a[48:49], v214 offset:16896\n" \
"v_mfma_f32_16x16x32_bf16 v[4:7], a[44:47], a[4:7], v[4:7]\n" \
"ds_read_b64_tr_b16 a[50:51], v214 offset:18944\n" \
"v_mov_b32 v74, 0x3f800000\n" \
"v_mfma_f32_16x16x32_bf16 v[16:19], a[32:35], a[8:11], v[16:19]\n" \
"ds_read_b64_tr_b16 a[52:53], v214 offset:25088\n" \
"v_cmp_eq_u32 vcc, 0, v72\n" \
"v_mfma_f32_16x16x32_bf16 v[16:19], a[36:39], a[12:15], v[16:19]\n" \
"ds_read_b64_tr_b16 a[54:55], v214 offset:27136\n" \
"v_cmp_eq_u32 s[66:67], 1, v72\n" \
"v_mfma_f32_16x16x32_bf16 v[20:23], a[40:43], a[8:11], v[20:23]\n" \
"ds_read_b64_tr_b16 a[56:57], v215 offset:16896\n" \
"v_mov_b32 v66, 0\n" \
"v_mfma_f32_16x16x32_bf16 v[20:23], a[44:47], a[12:15], v[20:23]\n" \
"ds_read_b64_tr_b16 a[58:59], v215 offset:18944\n" \
"v_mov_b32 v68, 0\n" \
"v_mfma_f32_16x16x32_bf16 v[32:35], a[32:35], a[16:19], v[32:35]\n" \
"ds_read_b64_tr_b16 a[60:61], v215 offset:25088\n" \
"v_cndmask_b32 v64, 0, v73, vcc\n" \
"v_mfma_f32_16x16x32_bf16 v[32:35], a[36:39], a[20:23], v[32:35]\n" \
"ds_read_b64_tr_b16 a[62:63], v215 offset:27136\n" \
"v_cndmask_b32 v64, v64, v74, s[66:67]\n" \
"v_mfma_f32_16x16x32_bf16 v[36:39], a[40:43], a[16:19], v[36:39]\n" \
"v_add_u32 v213, s74, v211\n" \
"v_add_u32 v214, s74, v212\n" \
"v_mfma_f32_16x16x32_bf16 v[36:39], a[44:47], a[20:23], v[36:39]\n" \
"s_waitcnt lgkmcnt(3)\n" \
"v_xor_b32 v215, 32, v214\n" \
"v_mov_b32 v70, v64\n" \
"v_mfma_f32_16x16x32_bf16 v[48:51], a[32:35], a[24:27], v[48:51]\n" \
"v_cmp_eq_u32 vcc, 2, v72\n" \
"v_cmp_eq_u32 s[66:67], 3, v72\n" \
"v_mfma_f32_16x16x32_bf16 v[48:51], a[36:39], a[28:31], v[48:51]\n" \
"v_mov_b32 v67, 0\n" \
"v_mov_b32 v69, 0\n" \
"v_mfma_f32_16x16x32_bf16 v[52:55], a[40:43], a[24:27], v[52:55]\n" \
"s_waitcnt vmcnt(17) lgkmcnt(0)\n" \
"v_cndmask_b32 v65, 0, v73, vcc\n" \
"v_cndmask_b32 v65, v65, v74, s[66:67]\n" \
"v_mfma_f32_16x16x32_bf16 v[52:55], a[44:47], a[28:31], v[52:55]\n" \
"s_barrier\n" \
"v_mov_b32 v71, v65\n" \
"v_mfma_f32_16x16x32_bf16 v[8:11], a[48:51], a[0:3], v[8:11]\n" \
"s_lshl_b32 s83, s95, 1\n" \
"s_add_u32 s83, s83, s74\n" \
"ds_read_b64_tr_b16 a[32:33], v214 offset:16384\n" \
"v_mfma_f32_16x16x32_bf16 v[8:11], a[52:55], a[4:7], v[8:11]\n" \
"s_waitcnt vmcnt(16)\n" \
"ds_read_b64_tr_b16 a[34:35], v214 offset:18432\n" \
"v_cmp_ne_u32 vcc, s60, v220\n" \
"v_mfma_f32_16x16x32_bf16 v[12:15], a[56:59], a[0:3], v[12:15]\n" \
"ds_read_b64_tr_b16 a[36:37], v214 offset:24576\n" \
"v_mfma_f32_16x16x32_bf16 v[12:15], a[60:63], a[4:7], v[12:15]\n" \
"s_and_b32 vcc_hi, vcc_lo, 1\n" \
"s_cbranch_scc1 Lpl4_%=\n" \
"Lpr4_%=:\n" \
"ds_read_b128 a[0:3], v213 offset:0\n" \
"v_mfma_f32_16x16x32_bf16 v[24:27], a[48:51], a[8:11], v[24:27]\n" \
"ds_read_b128 a[4:7], v213 offset:2048\n" \
"v_mfma_f32_16x16x32_bf16 v[24:27], a[52:55], a[12:15], v[24:27]\n" \
"s_add_u32 s71, s76, 0\n" \
"ds_read_b64_tr_b16 a[38:39], v214 offset:26624\n" \
"buffer_load_dwordx4 v[176:179], v248, s[56:59], s71 offen offset:0 sc1\n" \
"v_mfma_f32_16x16x32_bf16 v[28:31], a[56:59], a[8:11], v[28:31]\n" \
"ds_read_b64_tr_b16 a[40:41], v215 offset:16384\n" \
"v_mfma_f32_16x16x32_bf16 v[28:31], a[60:63], a[12:15], v[28:31]\n" \
"buffer_load_dwordx4 v[180:183], v248, s[56:59], s71 offen offset:1024 sc1\n" \
"ds_read_b128 a[8:11], v213 offset:256\n" \
"v_mfma_f32_16x16x32_bf16 v[40:43], a[48:51], a[16:19], v[40:43]\n" \
"ds_read_b128 a[12:15], v213 offset:2304\n" \
"v_mfma_f32_16x16x32_bf16 v[40:43], a[52:55], a[20:23], v[40:43]\n" \
"ds_read_b64_tr_b16 a[42:43], v215 offset:18432\n" \
"v_mfma_f32_16x16x32_bf16 v[44:47], a[56:59], a[16:19], v[44:47]\n" \
"ds_read_b64_tr_b16 a[44:45], v215 offset:24576\n" \
"v_mfma_f32_16x16x32_bf16 v[44:47], a[60:63], a[20:23], v[44:47]\n" \
"ds_read_b128 a[16:19], v213 offset:4096\n" \
"v_mfma_f32_16x16x32_bf16 v[56:59], a[48:51], a[24:27], v[56:59]\n" \
"ds_read_b128 a[20:23], v213 offset:6144\n" \
"v_mfma_f32_16x16x32_bf16 v[56:59], a[52:55], a[28:31], v[56:59]\n" \
"ds_read_b64_tr_b16 a[46:47], v215 offset:26624\n" \
"v_mfma_f32_16x16x32_bf16 v[60:63], a[56:59], a[24:27], v[60:63]\n" \
"buffer_load_dwordx4 v[184:187], v248, s[56:59], s71 offen offset:2048 sc1\n" \
"v_mfma_f32_16x16x32_bf16 v[60:63], a[60:63], a[28:31], v[60:63]\n" \
"s_waitcnt lgkmcnt(3)\n" \
"buffer_load_dwordx4 v[188:191], v248, s[56:59], s71 offen offset:3072 sc1\n" \
"v_mfma_f32_16x16x32_bf16 v[0:3], a[32:35], a[0:3], v[0:3]\n" \
"s_add_u32 s71, s76, 4096\n" \
"ds_read_b128 a[24:27], v213 offset:4352\n" \
"buffer_load_dwordx4 v[192:195], v248, s[56:59], s71 offen offset:0 sc1\n" \
"v_mfma_f32_16x16x32_bf16 v[0:3], a[36:39], a[4:7], v[0:3]\n" \
"s_waitcnt lgkmcnt(1)\n" \
"ds_read_b128 a[28:31], v213 offset:6400\n" \
"v_lshrrev_b32 v132, 4, v248\n" \
"v_mfma_f32_16x16x32_bf16 v[4:7], a[40:43], a[0:3], v[4:7]\n" \
"s_sub_u32 s74, s74, 32768\n" \
"s_cselect_b32 s74, 131072, s74\n" \
"ds_read_b64_tr_b16 a[48:49], v214 offset:16896\n" \
"v_mfma_f32_16x16x32_bf16 v[4:7], a[44:47], a[4:7], v[4:7]\n" \
"ds_read_b64_tr_b16 a[50:51], v214 offset:18944\n" \
"v_and_b32 v133, 15, v132\n" \
"v_mfma_f32_16x16x32_bf16 v[16:19], a[32:35], a[8:11], v[16:19]\n" \
"ds_read_b64_tr_b16 a[52:53], v214 offset:25088\n" \
"v_mul_u32_u24 v128, 144, v133\n" \
"v_mfma_f32_16x16x32_bf16 v[16:19], a[36:39], a[12:15], v[16:19]\n" \
"ds_read_b64_tr_b16 a[54:55], v214 offset:27136\n" \
"v_lshrrev_b32 v133, 4, v132\n" \
"v_mfma_f32_16x16x32_bf16 v[20:23], a[40:43], a[8:11], v[20:23]\n" \
"ds_read_b64_tr_b16 a[56:57], v215 offset:16896\n" \
"v_lshl_add_u32 v128, v133, 3, v128\n" \
"v_mfma_f32_16x16x32_bf16 v[20:23], a[44:47], a[12:15], v[20:23]\n" \
"ds_read_b64_tr_b16 a[58:59], v215 offset:18944\n" \
"v_add_u32 v128, s83, v128\n" \
"v_mfma_f32_16x16x32_bf16 v[32:35], a[32:35], a[16:19], v[32:35]\n" \
"ds_read_b64_tr_b16 a[60:61], v215 offset:25088\n" \
"v_lshrrev_b32 v133, 3, v132\n" \
"v_mfma_f32_16x16x32_bf16 v[32:35], a[36:39], a[20:23], v[32:35]\n" \
"ds_read_b64_tr_b16 a[62:63], v215 offset:27136\n" \
"v_and_b32 v134, 7, v132\n" \
"v_mfma_f32_16x16x32_bf16 v[36:39], a[40:43], a[16:19], v[36:39]\n" \
"v_add_u32 v213, s74, v211\n" \
"v_add_u32 v214, s74, v212\n" \
"v_mfma_f32_16x16x32_bf16 v[36:39], a[44:47], a[20:23], v[36:39]\n" \
"s_waitcnt lgkmcnt(3)\n" \
"v_xor_b32 v215, 32, v214\n" \
"v_mul_u32_u24 v129, 144, v133\n" \
"v_mfma_f32_16x16x32_bf16 v[48:51], a[32:35], a[24:27], v[48:51]\n" \
"buffer_load_dwordx4 v[196:199], v248, s[56:59], s71 offen offset:1024 sc1\n" \
"v_mfma_f32_16x16x32_bf16 v[48:51], a[36:39], a[28:31], v[48:51]\n" \
"buffer_load_dwordx4 v[200:203], v248, s[56:59], s71 offen offset:2048 sc1\n" \
"v_mfma_f32_16x16x32_bf16 v[52:55], a[40:43], a[24:27], v[52:55]\n" \
"s_waitcnt vmcnt(7) lgkmcnt(0)\n" \
"buffer_load_dwordx4 v[204:207], v248, s[56:59], s71 offen offset:3072 sc1\n" \
"v_mfma_f32_16x16x32_bf16 v[52:55], a[44:47], a[28:31], v[52:55]\n" \
"s_barrier\n" \
"v_mfma_f32_16x16x32_bf16 v[8:11], a[48:51], a[0:3], v[8:11]\n" \
"s_mov_b32 s70, 0x20000000\n" \
"ds_read_b128 a[192:195], v213 offset:0\n" \
"ds_read_b64_tr_b16 a[128:129], v214 offset:16384\n" \
"v_mfma_f32_16x16x32_bf16 v[8:11], a[52:55], a[4:7], v[8:11]\n" \
"s_sub_u32 s74, s74, 32768\n" \
"s_cselect_b32 s74, 131072, s74\n" \
"ds_read_b64_tr_b16 a[130:131], v214 offset:18432\n" \
"v_mfma_f32_16x16x32_bf16 v[12:15], a[56:59], a[0:3], v[12:15]\n" \
"ds_read_b128 a[196:199], v213 offset:2048\n" \
"ds_read_b64_tr_b16 a[132:133], v214 offset:24576\n" \
"v_mfma_f32_16x16x32_bf16 v[12:15], a[60:63], a[4:7], v[12:15]\n" \
"ds_read_b64_tr_b16 a[134:135], v214 offset:26624\n" \
"v_add_u32 v226, s74, v211\n" \
"v_mfma_f32_16x16x32_bf16 v[24:27], a[48:51], a[8:11], v[24:27]\n" \
"ds_read_b128 a[200:203], v213 offset:256\n" \
"ds_read_b64_tr_b16 a[136:137], v215 offset:16384\n" \
"v_mfma_f32_16x16x32_bf16 v[24:27], a[52:55], a[12:15], v[24:27]\n" \
"ds_read_b64_tr_b16 a[138:139], v215 offset:18432\n" \
"v_add_u32 v227, s74, v212\n" \
"v_mfma_f32_16x16x32_bf16 v[28:31], a[56:59], a[8:11], v[28:31]\n" \
"ds_read_b128 a[204:207], v213 offset:2304\n" \
"ds_read_b64_tr_b16 a[140:141], v215 offset:24576\n" \
"v_mfma_f32_16x16x32_bf16 v[28:31], a[60:63], a[12:15], v[28:31]\n" \
"ds_read_b64_tr_b16 a[142:143], v215 offset:26624\n" \
"v_xor_b32 v228, 32, v227\n" \
"v_mfma_f32_16x16x32_bf16 v[40:43], a[48:51], a[16:19], v[40:43]\n" \
"s_cmp_eq_u32 s79, 65601536\n" \
"s_cselect_b32 s70, 65593344, s70\n" \
"ds_read_b64_tr_b16 a[144:145], v214 offset:16896\n" \
"v_mfma_f32_16x16x32_bf16 v[40:43], a[52:55], a[20:23], v[40:43]\n" \
"buffer_store_dword v224, v225, s[56:59], s70 offen sc1\n" \
"ds_read_b64_tr_b16 a[146:147], v214 offset:18944\n" \
"v_mfma_f32_16x16x32_bf16 v[44:47], a[56:59], a[16:19], v[44:47]\n" \
"ds_read_b64_tr_b16 a[148:149], v214 offset:25088\n" \
"v_lshl_add_u32 v129, v134, 4, v129\n" \
"v_mfma_f32_16x16x32_bf16 v[44:47], a[60:63], a[20:23], v[44:47]\n" \
"ds_read_b64_tr_b16 a[150:151], v214 offset:27136\n" \
"v_add_u32 v129, s83, v129\n" \
"v_mfma_f32_16x16x32_bf16 v[56:59], a[48:51], a[24:27], v[56:59]\n" \
"ds_read_b64_tr_b16 a[152:153], v215 offset:16896\n" \
"v_lshlrev_b32 v134, 4, v134\n" \
"v_mfma_f32_16x16x32_bf16 v[56:59], a[52:55], a[28:31], v[56:59]\n" \
"ds_read_b64_tr_b16 a[154:155], v215 offset:18944\n" \
"v_lshl_add_u32 v130, v133, 12, v134\n" \
"v_mfma_f32_16x16x32_bf16 v[60:63], a[56:59], a[24:27], v[60:63]\n" \
"ds_read_b64_tr_b16 a[156:157], v215 offset:25088\n" \
"v_mfma_f32_16x16x32_bf16 v[60:63], a[60:63], a[28:31], v[60:63]\n" \
"s_waitcnt lgkmcnt(4)\n" \
"ds_read_b64_tr_b16 a[158:159], v215 offset:27136\n" \
"v_mfma_f32_16x16x32_bf16 v[0:3], a[128:131], a[192:195], v[0:3]\n" \
"ds_read_b128 a[224:227], v226 offset:0\n" \
"ds_read_b64_tr_b16 a[160:161], v227 offset:16384\n" \
"v_mfma_f32_16x16x32_bf16 v[0:3], a[132:135], a[196:199], v[0:3]\n" \
"ds_read_b64_tr_b16 a[162:163], v227 offset:18432\n" \
"v_mfma_f32_16x16x32_bf16 v[16:19], a[128:131], a[200:203], v[16:19]\n" \
"ds_read_b64_tr_b16 a[164:165], v227 offset:24576\n" \
"v_mfma_f32_16x16x32_bf16 v[16:19], a[132:135], a[204:207], v[16:19]\n" \
"ds_read_b128 a[228:231], v226 offset:2048\n" \
"ds_read_b64_tr_b16 a[166:167], v227 offset:26624\n" \
"v_mfma_f32_16x16x32_bf16 v[4:7], a[136:139], a[192:195], v[4:7]\n" \
"ds_read_b64_tr_b16 a[168:169], v228 offset:16384\n" \
"v_mfma_f32_16x16x32_bf16 v[4:7], a[140:143], a[196:199], v[4:7]\n" \
"ds_read_b64_tr_b16 a[170:171], v228 offset:18432\n" \
"v_mfma_f32_16x16x32_bf16 v[20:23], a[136:139], a[200:203], v[20:23]\n" \
"ds_read_b128 a[232:235], v226 offset:256\n" \
"ds_read_b64_tr_b16 a[172:173], v228 offset:24576\n" \
"v_mfma_f32_16x16x32_bf16 v[20:23], a[140:143], a[204:207], v[20:23]\n" \
"s_waitcnt lgkmcnt(6)\n" \
"ds_read_b64_tr_b16 a[174:175], v228 offset:26624\n" \
"v_mfma_f32_16x16x32_bf16 v[8:11], a[144:147], a[192:195], v[8:11]\n" \
"ds_read_b64_tr_b16 a[176:177], v227 offset:16896\n" \
"v_mfma_f32_16x16x32_bf16 v[8:11], a[148:151], a[196:199], v[8:11]\n" \
"ds_read_b128 a[236:239], v226 offset:2304\n" \
"ds_read_b64_tr_b16 a[178:179], v227 offset:18944\n" \
"v_mfma_f32_16x16x32_bf16 v[24:27], a[144:147], a[200:203], v[24:27]\n" \
"ds_read_b64_tr_b16 a[180:181], v227 offset:25088\n" \
"v_mfma_f32_16x16x32_bf16 v[24:27], a[148:151], a[204:207], v[24:27]\n" \
"ds_read_b64_tr_b16 a[182:183], v227 offset:27136\n" \
"v_mfma_f32_16x16x32_bf16 v[12:15], a[152:155], a[192:195], v[12:15]\n" \
"ds_read_b64_tr_b16 a[184:185], v228 offset:16896\n" \
"v_mfma_f32_16x16x32_bf16 v[12:15], a[156:159], a[196:199], v[12:15]\n" \
"ds_read_b64_tr_b16 a[186:187], v228 offset:18944\n" \
"v_mfma_f32_16x16x32_bf16 v[28:31], a[152:155], a[200:203], v[28:31]\n" \
"ds_read_b64_tr_b16 a[188:189], v228 offset:25088\n" \
"v_mfma_f32_16x16x32_bf16 v[28:31], a[156:159], a[204:207], v[28:31]\n" \
"s_waitcnt lgkmcnt(4)\n" \
"ds_read_b64_tr_b16 a[190:191], v228 offset:27136\n" \
"v_mfma_f32_16x16x32_bf16 v[0:3], a[160:163], a[224:227], v[0:3]\n" \
"s_waitcnt vmcnt(1)\n" \
"ds_read_b128 a[208:211], v213 offset:4096\n" \
"v_mfma_f32_16x16x32_bf16 v[0:3], a[164:167], a[228:231], v[0:3]\n" \
"ds_read_b128 a[212:215], v213 offset:6144\n" \
"v_mfma_f32_16x16x32_bf16 v[0:3], v[64:67], v[176:179], v[0:3]\n" \
"ds_read_b128 a[240:243], v226 offset:4096\n" \
"v_mfma_f32_16x16x32_bf16 v[16:19], a[160:163], a[232:235], v[16:19]\n" \
"ds_read_b128 a[244:247], v226 offset:6144\n" \
"v_mfma_f32_16x16x32_bf16 v[16:19], a[164:167], a[236:239], v[16:19]\n" \
"ds_read_b128 a[216:219], v213 offset:4352\n" \
"v_mfma_f32_16x16x32_bf16 v[16:19], v[64:67], v[184:187], v[16:19]\n" \
"ds_read_b128 a[220:223], v213 offset:6400\n" \
"v_cvt_pk_bf16_f32 v0, v0, v1\n" \
"v_mfma_f32_16x16x32_bf16 v[4:7], a[168:171], a[224:227], v[4:7]\n" \
"ds_read_b128 a[248:251], v226 offset:4352\n" \
"v_cvt_pk_bf16_f32 v1, v2, v3\n" \
"v_mfma_f32_16x16x32_bf16 v[4:7], a[172:175], a[228:231], v[4:7]\n" \
"ds_read_b128 a[252:255], v226 offset:6400\n" \
"v_mfma_f32_16x16x32_bf16 v[4:7], v[68:71], v[176:179], v[4:7]\n" \
"ds_write_b64 v128, v[0:1] offset:0\n" \
"v_mfma_f32_16x16x32_bf16 v[20:23], a[168:171], a[232:235], v[20:23]\n" \
"v_cvt_pk_bf16_f32 v16, v16, v17\n" \
"v_cvt_pk_bf16_f32 v17, v18, v19\n" \
"v_mfma_f32_16x16x32_bf16 v[20:23], a[172:175], a[236:239], v[20:23]\n" \
"ds_write_b64 v128, v[16:17] offset:2304\n" \
"v_mfma_f32_16x16x32_bf16 v[20:23], v[68:71], v[184:187], v[20:23]\n" \
"s_waitcnt lgkmcnt(3)\n" \
"v_cvt_pk_bf16_f32 v4, v4, v5\n" \
"v_cvt_pk_bf16_f32 v5, v6, v7\n" \
"v_mfma_f32_16x16x32_bf16 v[8:11], a[176:179], a[224:227], v[8:11]\n" \
"ds_write_b64 v128, v[4:5] offset:32\n" \
"v_mfma_f32_16x16x32_bf16 v[8:11], a[180:183], a[228:231], v[8:11]\n" \
"v_mfma_f32_16x16x32_bf16 v[8:11], v[64:67], v[180:183], v[8:11]\n" \
"v_cvt_pk_bf16_f32 v20, v20, v21\n" \
"v_cvt_pk_bf16_f32 v21, v22, v23\n" \
"v_mfma_f32_16x16x32_bf16 v[24:27], a[176:179], a[232:235], v[24:27]\n" \
"ds_write_b64 v128, v[20:21] offset:2336\n" \
"v_mfma_f32_16x16x32_bf16 v[24:27], a[180:183], a[236:239], v[24:27]\n" \
"v_mfma_f32_16x16x32_bf16 v[24:27], v[64:67], v[188:191], v[24:27]\n" \
"v_cvt_pk_bf16_f32 v8, v8, v9\n" \
"v_cvt_pk_bf16_f32 v9, v10, v11\n" \
"v_mfma_f32_16x16x32_bf16 v[12:15], a[184:187], a[224:227], v[12:15]\n" \
"ds_write_b64 v128, v[8:9] offset:64\n" \
"v_mfma_f32_16x16x32_bf16 v[12:15], a[188:191], a[228:231], v[12:15]\n" \
"v_mfma_f32_16x16x32_bf16 v[12:15], v[68:71], v[180:183], v[12:15]\n" \
"v_cvt_pk_bf16_f32 v24, v24, v25\n" \
"v_cvt_pk_bf16_f32 v25, v26, v27\n" \
"v_mfma_f32_16x16x32_bf16 v[28:31], a[184:187], a[232:235], v[28:31]\n" \
"ds_write_b64 v128, v[24:25] offset:2368\n" \
"v_mfma_f32_16x16x32_bf16 v[28:31], a[188:191], a[236:239], v[28:31]\n" \
"v_mfma_f32_16x16x32_bf16 v[28:31], v[68:71], v[188:191], v[28:31]\n" \
"v_cvt_pk_bf16_f32 v12, v12, v13\n" \
"v_cvt_pk_bf16_f32 v13, v14, v15\n" \
"v_mfma_f32_16x16x32_bf16 v[32:35], a[128:131], a[208:211], v[32:35]\n" \
"ds_write_b64 v128, v[12:13] offset:96\n" \
"v_mfma_f32_16x16x32_bf16 v[32:35], a[132:135], a[212:215], v[32:35]\n" \
"v_mfma_f32_16x16x32_bf16 v[32:35], a[160:163], a[240:243], v[32:35]\n" \
"v_cvt_pk_bf16_f32 v28, v28, v29\n" \
"v_cvt_pk_bf16_f32 v29, v30, v31\n" \
"v_mfma_f32_16x16x32_bf16 v[32:35], a[164:167], a[244:247], v[32:35]\n" \
"ds_write_b64 v128, v[28:29] offset:2400\n" \
"v_mfma_f32_16x16x32_bf16 v[32:35], v[64:67], v[192:195], v[32:35]\n" \
"ds_read_b128 v[140:143], v129 offset:0\n" \
"v_mfma_f32_16x16x32_bf16 v[36:39], a[136:139], a[208:211], v[36:39]\n" \
"ds_read_b128 v[144:147], v129 offset:1152\n" \
"v_mfma_f32_16x16x32_bf16 v[36:39], a[140:143], a[212:215], v[36:39]\n" \
"ds_read_b128 v[148:151], v129 offset:2304\n" \
"v_mfma_f32_16x16x32_bf16 v[36:39], a[168:171], a[240:243], v[36:39]\n" \
"ds_read_b128 v[152:155], v129 offset:3456\n" \
"v_mfma_f32_16x16x32_bf16 v[36:39], a[172:175], a[244:247], v[36:39]\n" \
"v_cvt_pk_bf16_f32 v32, v32, v33\n" \
"v_cvt_pk_bf16_f32 v33, v34, v35\n" \
"v_mfma_f32_16x16x32_bf16 v[36:39], v[68:71], v[192:195], v[36:39]\n" \
"ds_write_b64 v128, v[32:33] offset:0\n" \
"v_mfma_f32_16x16x32_bf16 v[40:43], a[144:147], a[208:211], v[40:43]\n" \
"v_mfma_f32_16x16x32_bf16 v[40:43], a[148:151], a[212:215], v[40:43]\n" \
"s_add_u32 s70, s77, 0\n" \
"s_waitcnt lgkmcnt(4)\n" \
"v_mfma_f32_16x16x32_bf16 v[40:43], a[176:179], a[240:243], v[40:43]\n" \
"v_cvt_pk_bf16_f32 v36, v36, v37\n" \
"v_cvt_pk_bf16_f32 v37, v38, v39\n" \
"v_mfma_f32_16x16x32_bf16 v[40:43], a[180:183], a[244:247], v[40:43]\n" \
"buffer_store_dwordx4 v[140:143], v130, s[52:55], s70 offen sc1\n" \
"ds_write_b64 v128, v[36:37] offset:32\n" \
"v_mfma_f32_16x16x32_bf16 v[40:43], v[64:67], v[196:199], v[40:43]\n" \
"v_mfma_f32_16x16x32_bf16 v[44:47], a[152:155], a[208:211], v[44:47]\n" \
"v_mfma_f32_16x16x32_bf16 v[44:47], a[156:159], a[212:215], v[44:47]\n" \
"v_mfma_f32_16x16x32_bf16 v[44:47], a[184:187], a[240:243], v[44:47]\n" \
"s_add_u32 s70, s77, 32768\n" \
"v_cvt_pk_bf16_f32 v40, v40, v41\n" \
"v_cvt_pk_bf16_f32 v41, v42, v43\n" \
"v_mfma_f32_16x16x32_bf16 v[44:47], a[188:191], a[244:247], v[44:47]\n" \
"s_waitcnt lgkmcnt(4)\n" \
"buffer_store_dwordx4 v[144:147], v130, s[52:55], s70 offen sc1\n" \
"ds_write_b64 v128, v[40:41] offset:64\n" \
"v_mfma_f32_16x16x32_bf16 v[44:47], v[68:71], v[196:199], v[44:47]\n" \
"v_mfma_f32_16x16x32_bf16 v[48:51], a[128:131], a[216:219], v[48:51]\n" \
"v_mfma_f32_16x16x32_bf16 v[48:51], a[132:135], a[220:223], v[48:51]\n" \
"v_mfma_f32_16x16x32_bf16 v[48:51], a[160:163], a[248:251], v[48:51]\n" \
"s_add_u32 s70, s77, 65536\n" \
"v_cvt_pk_bf16_f32 v44, v44, v45\n" \
"v_cvt_pk_bf16_f32 v45, v46, v47\n" \
"v_mfma_f32_16x16x32_bf16 v[48:51], a[164:167], a[252:255], v[48:51]\n" \
"s_waitcnt lgkmcnt(4)\n" \
"buffer_store_dwordx4 v[148:151], v130, s[52:55], s70 offen sc1\n" \
"ds_write_b64 v128, v[44:45] offset:96\n" \
"v_mfma_f32_16x16x32_bf16 v[48:51], v[64:67], v[200:203], v[48:51]\n" \
"ds_read_b128 v[156:159], v129 offset:0\n" \
"v_mfma_f32_16x16x32_bf16 v[52:55], a[136:139], a[216:219], v[52:55]\n" \
"ds_read_b128 v[160:163], v129 offset:1152\n" \
"v_mfma_f32_16x16x32_bf16 v[52:55], a[140:143], a[220:223], v[52:55]\n" \
"v_mfma_f32_16x16x32_bf16 v[52:55], a[168:171], a[248:251], v[52:55]\n" \
"s_add_u32 s70, s77, 98304\n" \
"v_cvt_pk_bf16_f32 v48, v48, v49\n" \
"v_cvt_pk_bf16_f32 v49, v50, v51\n" \
"v_mfma_f32_16x16x32_bf16 v[52:55], a[172:175], a[252:255], v[52:55]\n" \
"s_waitcnt lgkmcnt(6)\n" \
"buffer_store_dwordx4 v[152:155], v130, s[52:55], s70 offen sc1\n" \
"ds_write_b64 v128, v[48:49] offset:2304\n" \
"v_mfma_f32_16x16x32_bf16 v[52:55], v[68:71], v[200:203], v[52:55]\n" \
"v_mfma_f32_16x16x32_bf16 v[56:59], a[144:147], a[216:219], v[56:59]\n" \
"v_mfma_f32_16x16x32_bf16 v[56:59], a[148:151], a[220:223], v[56:59]\n" \
"v_mfma_f32_16x16x32_bf16 v[56:59], a[176:179], a[248:251], v[56:59]\n" \
"s_add_u32 s70, s77, 131072\n" \
"v_cvt_pk_bf16_f32 v52, v52, v53\n" \
"v_cvt_pk_bf16_f32 v53, v54, v55\n" \
"v_mfma_f32_16x16x32_bf16 v[56:59], a[180:183], a[252:255], v[56:59]\n" \
"s_waitcnt lgkmcnt(2)\n" \
"buffer_store_dwordx4 v[156:159], v130, s[52:55], s70 offen sc1\n" \
"ds_write_b64 v128, v[52:53] offset:2336\n" \
"v_mfma_f32_16x16x32_bf16 v[56:59], v[64:67], v[204:207], v[56:59]\n" \
"v_mfma_f32_16x16x32_bf16 v[60:63], a[152:155], a[216:219], v[60:63]\n" \
"v_mfma_f32_16x16x32_bf16 v[60:63], a[156:159], a[220:223], v[60:63]\n" \
"v_mfma_f32_16x16x32_bf16 v[60:63], a[184:187], a[248:251], v[60:63]\n" \
"s_add_u32 s70, s77, 163840\n" \
"v_cvt_pk_bf16_f32 v56, v56, v57\n" \
"v_cvt_pk_bf16_f32 v57, v58, v59\n" \
"v_mfma_f32_16x16x32_bf16 v[60:63], a[188:191], a[252:255], v[60:63]\n" \
"s_waitcnt lgkmcnt(2)\n" \
"buffer_store_dwordx4 v[160:163], v130, s[52:55], s70 offen sc1\n" \
"ds_write_b64 v128, v[56:57] offset:2368\n" \
"v_mfma_f32_16x16x32_bf16 v[60:63], v[68:71], v[204:207], v[60:63]\n" \
"s_nop 7\n" \
"v_cvt_pk_bf16_f32 v60, v60, v61\n" \
"v_cvt_pk_bf16_f32 v61, v62, v63\n" \
"ds_write_b64 v128, v[60:61] offset:2400\n" \
"ds_read_b128 v[164:167], v129 offset:2304\n" \
"ds_read_b128 v[168:171], v129 offset:3456\n" \
"s_add_u32 s70, s77, 196608\n" \
"s_waitcnt lgkmcnt(1)\n" \
"buffer_store_dwordx4 v[164:167], v130, s[52:55], s70 offen sc1\n" \
"s_add_u32 s70, s77, 229376\n" \
"s_waitcnt lgkmcnt(0)\n" \
"buffer_store_dwordx4 v[168:171], v130, s[52:55], s70 offen sc1\n" \
"s_endpgm\n" \
"Lpg12620_%=:\n" \
"s_cbranch_execz Lpg13593_%=\n" \
"Lo0_%=:\n" \
"v_mfma_f32_16x16x32_bf16 v[0:3], a[32:35], a[0:3], v[0:3]\n" \
"ds_read_b128 a[24:27], v213 offset:4352\n" \
"s_add_u32 m0, s75, s95\n" \
"v_mfma_f32_16x16x32_bf16 v[0:3], a[36:39], a[4:7], v[0:3]\n" \
"s_waitcnt lgkmcnt(1)\n" \
"ds_read_b128 a[28:31], v213 offset:6400\n" \
"global_load_lds_dwordx4 v222, s[36:37] offset:0 sc1\n" \
"v_mfma_f32_16x16x32_bf16 v[4:7], a[40:43], a[0:3], v[4:7]\n" \
"s_sub_u32 s74, s74, 32768\n" \
"s_cselect_b32 s74, 131072, s74\n" \
"ds_read_b64_tr_b16 a[48:49], v214 offset:16896\n" \
"v_mfma_f32_16x16x32_bf16 v[4:7], a[44:47], a[4:7], v[4:7]\n" \
"ds_read_b64_tr_b16 a[50:51], v214 offset:18944\n" \
"v_mfma_f32_16x16x32_bf16 v[16:19], a[32:35], a[8:11], v[16:19]\n" \
"ds_read_b64_tr_b16 a[52:53], v214 offset:25088\n" \
"v_mfma_f32_16x16x32_bf16 v[16:19], a[36:39], a[12:15], v[16:19]\n" \
"ds_read_b64_tr_b16 a[54:55], v214 offset:27136\n" \
"global_load_lds_dwordx4 v222, s[36:37] offset:1024 sc1\n" \
"v_mfma_f32_16x16x32_bf16 v[20:23], a[40:43], a[8:11], v[20:23]\n" \
"ds_read_b64_tr_b16 a[56:57], v215 offset:16896\n" \
"v_mfma_f32_16x16x32_bf16 v[20:23], a[44:47], a[12:15], v[20:23]\n" \
"ds_read_b64_tr_b16 a[58:59], v215 offset:18944\n" \
"v_mfma_f32_16x16x32_bf16 v[32:35], a[32:35], a[16:19], v[32:35]\n" \
"ds_read_b64_tr_b16 a[60:61], v215 offset:25088\n" \
"v_mfma_f32_16x16x32_bf16 v[32:35], a[36:39], a[20:23], v[32:35]\n" \
"ds_read_b64_tr_b16 a[62:63], v215 offset:27136\n" \
"global_load_lds_dwordx4 v222, s[36:37] offset:2048 sc1\n" \
"v_mfma_f32_16x16x32_bf16 v[36:39], a[40:43], a[16:19], v[36:39]\n" \
"v_add_u32 v213, s74, v211\n" \
"v_add_u32 v214, s74, v212\n" \
"v_mfma_f32_16x16x32_bf16 v[36:39], a[44:47], a[20:23], v[36:39]\n" \
"s_waitcnt lgkmcnt(3)\n" \
"v_xor_b32 v215, 32, v214\n" \
"v_mfma_f32_16x16x32_bf16 v[48:51], a[32:35], a[24:27], v[48:51]\n" \
"v_mfma_f32_16x16x32_bf16 v[48:51], a[36:39], a[28:31], v[48:51]\n" \
"global_load_lds_dwordx4 v222, s[36:37] offset:3072 sc1\n" \
"v_mfma_f32_16x16x32_bf16 v[52:55], a[40:43], a[24:27], v[52:55]\n" \
"s_waitcnt vmcnt(20) lgkmcnt(0)\n" \
"s_add_u32 s36, s36, 0x1000\n" \
"v_mfma_f32_16x16x32_bf16 v[52:55], a[44:47], a[28:31], v[52:55]\n" \
"s_barrier\n" \
"v_mfma_f32_16x16x32_bf16 v[8:11], a[48:51], a[0:3], v[8:11]\n" \
"s_add_u32 m0, m0, 0x5000\n" \
"ds_read_b64_tr_b16 a[32:33], v214 offset:16384\n" \
"v_mfma_f32_16x16x32_bf16 v[8:11], a[52:55], a[4:7], v[8:11]\n" \
"ds_read_b64_tr_b16 a[34:35], v214 offset:18432\n" \
"global_load_lds_dwordx4 v216, s[38:39] offset:-4096\n" \
"v_mfma_f32_16x16x32_bf16 v[12:15], a[56:59], a[0:3], v[12:15]\n" \
"ds_read_b64_tr_b16 a[36:37], v214 offset:24576\n" \
"v_mfma_f32_16x16x32_bf16 v[12:15], a[60:63], a[4:7], v[12:15]\n" \
"ds_read_b128 a[0:3], v213 offset:0\n" \
"v_mfma_f32_16x16x32_bf16 v[24:27], a[48:51], a[8:11], v[24:27]\n" \
"ds_read_b128 a[4:7], v213 offset:2048\n" \
"v_mfma_f32_16x16x32_bf16 v[24:27], a[52:55], a[12:15], v[24:27]\n" \
"ds_read_b64_tr_b16 a[38:39], v214 offset:26624\n" \
"global_load_lds_dwordx4 v217, s[38:39] offset:-3072\n" \
"v_mfma_f32_16x16x32_bf16 v[28:31], a[56:59], a[8:11], v[28:31]\n" \
"ds_read_b64_tr_b16 a[40:41], v215 offset:16384\n" \
"v_mfma_f32_16x16x32_bf16 v[28:31], a[60:63], a[12:15], v[28:31]\n" \
"ds_read_b128 a[8:11], v213 offset:256\n" \
"v_mfma_f32_16x16x32_bf16 v[40:43], a[48:51], a[16:19], v[40:43]\n" \
"ds_read_b128 a[12:15], v213 offset:2304\n" \
"v_mfma_f32_16x16x32_bf16 v[40:43], a[52:55], a[20:23], v[40:43]\n" \
"ds_read_b64_tr_b16 a[42:43], v215 offset:18432\n" \
"global_load_lds_dwordx4 v218, s[38:39] offset:-2048\n" \
"v_mfma_f32_16x16x32_bf16 v[44:47], a[56:59], a[16:19], v[44:47]\n" \
"ds_read_b64_tr_b16 a[44:45], v215 offset:24576\n" \
"v_mfma_f32_16x16x32_bf16 v[44:47], a[60:63], a[20:23], v[44:47]\n" \
"ds_read_b128 a[16:19], v213 offset:4096\n" \
"global_load_lds_dwordx4 v219, s[38:39] offset:-1024\n" \
"v_mfma_f32_16x16x32_bf16 v[56:59], a[48:51], a[24:27], v[56:59]\n" \
"s_add_u32 s38, s38, 0x20000\n" \
"s_addc_u32 s39, s39, 0\n" \
"ds_read_b128 a[20:23], v213 offset:6144\n" \
"v_mfma_f32_16x16x32_bf16 v[56:59], a[52:55], a[28:31], v[56:59]\n" \
"s_sub_u32 s75, s75, 32768\n" \
"s_cselect_b32 s75, 131072, s75\n" \
"ds_read_b64_tr_b16 a[46:47], v215 offset:26624\n" \
"v_mfma_f32_16x16x32_bf16 v[60:63], a[56:59], a[24:27], v[60:63]\n" \
"s_sub_u32 s72, s72, 1\n" \
"s_cmp_lg_u32 s72, 0\n" \
"v_mfma_f32_16x16x32_bf16 v[60:63], a[60:63], a[28:31], v[60:63]\n" \
"s_waitcnt lgkmcnt(3)\n" \
"s_cbranch_scc1 Lo0_%=\n" \
"s_setpc_b64 s[68:69]\n" \
"Lrole1_%=:\n" \
"Lpg12708_%=:\n" \
"s_cbranch_execz Lcb1_%=\n" \
"s_sub_u32 s72, 50, s64\n" \
"s_call_b64 s[68:69], Lo0_%=\n" \
"buffer_load_dword v220, v221, s[56:59], s82 offen sc1\n" \
"s_mov_b32 s72, 2\n" \
"s_call_b64 s[68:69], Lo0_%=\n" \
"s_waitcnt vmcnt(16)\n" \
"v_cmp_ne_u32 vcc, s60, v220\n" \
"s_nop 1\n" \
"s_and_b32 vcc_hi, vcc_lo, 65535\n" \
"s_cbranch_scc1 Lpl5_%=\n" \
"Lpr5_%=:\n" \
"s_mov_b32 s36, s73\n" \
"s_mov_b64 s[38:39], s[40:41]\n" \
"s_mov_b32 s72, 4\n" \
"s_call_b64 s[68:69], Lo0_%=\n" \
"v_mfma_f32_16x16x32_bf16 v[64:67], a[32:35], a[0:3], 0\n" \
"ds_read_b128 a[24:27], v213 offset:4352\n" \
"s_add_u32 m0, s75, s95\n" \
"v_mfma_f32_16x16x32_bf16 v[64:67], a[36:39], a[4:7], v[64:67]\n" \
"s_waitcnt lgkmcnt(1)\n" \
"ds_read_b128 a[28:31], v213 offset:6400\n" \
"global_load_lds_dwordx4 v222, s[36:37] offset:0 sc1\n" \
"v_mfma_f32_16x16x32_bf16 v[68:71], a[40:43], a[0:3], 0\n" \
"s_sub_u32 s74, s74, 32768\n" \
"s_cselect_b32 s74, 131072, s74\n" \
"ds_read_b64_tr_b16 a[48:49], v214 offset:16896\n" \
"v_mfma_f32_16x16x32_bf16 v[68:71], a[44:47], a[4:7], v[68:71]\n" \
"Lpg12736_%=:\n" \
"s_cbranch_execz Lcb1_%=\n" \
"ds_read_b64_tr_b16 a[50:51], v214 offset:18944\n" \
"v_cvt_pk_bf16_f32 v0, v0, v1\n" \
"v_mfma_f32_16x16x32_bf16 v[80:83], a[32:35], a[8:11], 0\n" \
"ds_read_b64_tr_b16 a[52:53], v214 offset:25088\n" \
"v_cvt_pk_bf16_f32 v1, v2, v3\n" \
"v_mfma_f32_16x16x32_bf16 v[80:83], a[36:39], a[12:15], v[80:83]\n" \
"ds_read_b64_tr_b16 a[54:55], v214 offset:27136\n" \
"global_load_lds_dwordx4 v222, s[36:37] offset:1024 sc1\n" \
"v_mfma_f32_16x16x32_bf16 v[84:87], a[40:43], a[8:11], 0\n" \
"ds_read_b64_tr_b16 a[56:57], v215 offset:16896\n" \
"v_cvt_pk_bf16_f32 v2, v4, v5\n" \
"v_mfma_f32_16x16x32_bf16 v[84:87], a[44:47], a[12:15], v[84:87]\n" \
"ds_read_b64_tr_b16 a[58:59], v215 offset:18944\n" \
"v_cvt_pk_bf16_f32 v3, v6, v7\n" \
"v_mfma_f32_16x16x32_bf16 v[96:99], a[32:35], a[16:19], 0\n" \
"s_add_u32 s71, s76, 0\n" \
"ds_read_b64_tr_b16 a[60:61], v215 offset:25088\n" \
"v_mfma_f32_16x16x32_bf16 v[96:99], a[36:39], a[20:23], v[96:99]\n" \
"ds_read_b64_tr_b16 a[62:63], v215 offset:27136\n" \
"global_load_lds_dwordx4 v222, s[36:37] offset:2048 sc1\n" \
"v_mfma_f32_16x16x32_bf16 v[100:103], a[40:43], a[16:19], 0\n" \
"v_add_u32 v213, s74, v211\n" \
"v_add_u32 v214, s74, v212\n" \
"v_mfma_f32_16x16x32_bf16 v[100:103], a[44:47], a[20:23], v[100:103]\n" \
"s_waitcnt lgkmcnt(3)\n" \
"v_xor_b32 v215, 32, v214\n" \
"v_mfma_f32_16x16x32_bf16 v[112:115], a[32:35], a[24:27], 0\n" \
"v_mfma_f32_16x16x32_bf16 v[112:115], a[36:39], a[28:31], v[112:115]\n" \
"global_load_lds_dwordx4 v222, s[36:37] offset:3072 sc1\n" \
"v_mfma_f32_16x16x32_bf16 v[116:119], a[40:43], a[24:27], 0\n" \
"s_waitcnt vmcnt(20) lgkmcnt(0)\n" \
"s_add_u32 s36, s36, 0x1000\n" \
"v_mfma_f32_16x16x32_bf16 v[116:119], a[44:47], a[28:31], v[116:119]\n" \
"s_barrier\n" \
"v_mfma_f32_16x16x32_bf16 v[72:75], a[48:51], a[0:3], 0\n" \
"s_add_u32 m0, m0, 0x5000\n" \
"ds_read_b64_tr_b16 a[32:33], v214 offset:16384\n" \
"v_mfma_f32_16x16x32_bf16 v[72:75], a[52:55], a[4:7], v[72:75]\n" \
"ds_read_b64_tr_b16 a[34:35], v214 offset:18432\n" \
"global_load_lds_dwordx4 v216, s[38:39] offset:-4096\n" \
"v_mfma_f32_16x16x32_bf16 v[76:79], a[56:59], a[0:3], 0\n" \
"ds_read_b64_tr_b16 a[36:37], v214 offset:24576\n" \
"v_mfma_f32_16x16x32_bf16 v[76:79], a[60:63], a[4:7], v[76:79]\n" \
"ds_read_b128 a[0:3], v213 offset:0\n" \
"v_mfma_f32_16x16x32_bf16 v[88:91], a[48:51], a[8:11], 0\n" \
"ds_read_b128 a[4:7], v213 offset:2048\n" \
"v_mfma_f32_16x16x32_bf16 v[88:91], a[52:55], a[12:15], v[88:91]\n" \
"ds_read_b64_tr_b16 a[38:39], v214 offset:26624\n" \
"global_load_lds_dwordx4 v217, s[38:39] offset:-3072\n" \
"v_mfma_f32_16x16x32_bf16 v[92:95], a[56:59], a[8:11], 0\n" \
"buffer_store_dwordx4 v[0:3], v248, s[56:59], s71 offen offset:0 sc1\n" \
"ds_read_b64_tr_b16 a[40:41], v215 offset:16384\n" \
"v_cvt_pk_bf16_f32 v4, v8, v9\n" \
"v_cvt_pk_bf16_f32 v5, v10, v11\n" \
"v_cvt_pk_bf16_f32 v6, v12, v13\n" \
"v_mfma_f32_16x16x32_bf16 v[92:95], a[60:63], a[12:15], v[92:95]\n" \
"ds_read_b128 a[8:11], v213 offset:256\n" \
"v_cvt_pk_bf16_f32 v7, v14, v15\n" \
"v_mfma_f32_16x16x32_bf16 v[104:107], a[48:51], a[16:19], 0\n" \
"ds_read_b128 a[12:15], v213 offset:2304\n" \
"v_mfma_f32_16x16x32_bf16 v[104:107], a[52:55], a[20:23], v[104:107]\n" \
"ds_read_b64_tr_b16 a[42:43], v215 offset:18432\n" \
"global_load_lds_dwordx4 v218, s[38:39] offset:-2048\n" \
"v_mfma_f32_16x16x32_bf16 v[108:111], a[56:59], a[16:19], 0\n" \
"buffer_store_dwordx4 v[4:7], v248, s[56:59], s71 offen offset:1024 sc1\n" \
"ds_read_b64_tr_b16 a[44:45], v215 offset:24576\n" \
"v_cvt_pk_bf16_f32 v8, v16, v17\n" \
"v_cvt_pk_bf16_f32 v9, v18, v19\n" \
"v_cvt_pk_bf16_f32 v10, v20, v21\n" \
"v_mfma_f32_16x16x32_bf16 v[108:111], a[60:63], a[20:23], v[108:111]\n" \
"ds_read_b128 a[16:19], v213 offset:4096\n" \
"v_cvt_pk_bf16_f32 v11, v22, v23\n" \
"v_mfma_f32_16x16x32_bf16 v[120:123], a[48:51], a[24:27], 0\n" \
"ds_read_b128 a[20:23], v213 offset:6144\n" \
"global_load_lds_dwordx4 v219, s[38:39] offset:-1024\n" \
"v_mfma_f32_16x16x32_bf16 v[120:123], a[52:55], a[28:31], v[120:123]\n" \
"s_add_u32 s38, s38, 0x20000\n" \
"s_addc_u32 s39, s39, 0\n" \
"ds_read_b64_tr_b16 a[46:47], v215 offset:26624\n" \
"v_mfma_f32_16x16x32_bf16 v[124:127], a[56:59], a[24:27], 0\n" \
"buffer_store_dwordx4 v[8:11], v248, s[56:59], s71 offen offset:2048 sc1\n" \
"s_sub_u32 s75, s75, 32768\n" \
"s_cselect_b32 s75, 131072, s75\n" \
"v_cvt_pk_bf16_f32 v12, v24, v25\n" \
"v_cvt_pk_bf16_f32 v13, v26, v27\n" \
"v_mfma_f32_16x16x32_bf16 v[124:127], a[60:63], a[28:31], v[124:127]\n" \
"s_waitcnt lgkmcnt(3)\n" \
"v_cvt_pk_bf16_f32 v14, v28, v29\n" \
"v_cvt_pk_bf16_f32 v15, v30, v31\n" \
"v_mfma_f32_16x16x32_bf16 v[64:67], a[32:35], a[0:3], v[64:67]\n" \
"ds_read_b128 a[24:27], v213 offset:4352\n" \
"s_add_u32 m0, s75, s95\n" \
"v_mfma_f32_16x16x32_bf16 v[64:67], a[36:39], a[4:7], v[64:67]\n" \
"s_waitcnt lgkmcnt(1)\n" \
"ds_read_b128 a[28:31], v213 offset:6400\n" \
"global_load_lds_dwordx4 v222, s[36:37] offset:0 sc1\n" \
"v_mfma_f32_16x16x32_bf16 v[68:71], a[40:43], a[0:3], v[68:71]\n" \
"s_sub_u32 s74, s74, 32768\n" \
"s_cselect_b32 s74, 131072, s74\n" \
"ds_read_b64_tr_b16 a[48:49], v214 offset:16896\n" \
"v_mfma_f32_16x16x32_bf16 v[68:71], a[44:47], a[4:7], v[68:71]\n" \
"ds_read_b64_tr_b16 a[50:51], v214 offset:18944\n" \
"v_mfma_f32_16x16x32_bf16 v[80:83], a[32:35], a[8:11], v[80:83]\n" \
"ds_read_b64_tr_b16 a[52:53], v214 offset:25088\n" \
"v_mfma_f32_16x16x32_bf16 v[80:83], a[36:39], a[12:15], v[80:83]\n" \
"ds_read_b64_tr_b16 a[54:55], v214 offset:27136\n" \
"global_load_lds_dwordx4 v222, s[36:37] offset:1024 sc1\n" \
"v_mfma_f32_16x16x32_bf16 v[84:87], a[40:43], a[8:11], v[84:87]\n" \
"ds_read_b64_tr_b16 a[56:57], v215 offset:16896\n" \
"v_mfma_f32_16x16x32_bf16 v[84:87], a[44:47], a[12:15], v[84:87]\n" \
"ds_read_b64_tr_b16 a[58:59], v215 offset:18944\n" \
"v_mfma_f32_16x16x32_bf16 v[96:99], a[32:35], a[16:19], v[96:99]\n" \
"ds_read_b64_tr_b16 a[60:61], v215 offset:25088\n" \
"v_mfma_f32_16x16x32_bf16 v[96:99], a[36:39], a[20:23], v[96:99]\n" \
"ds_read_b64_tr_b16 a[62:63], v215 offset:27136\n" \
"global_load_lds_dwordx4 v222, s[36:37] offset:2048 sc1\n" \
"v_mfma_f32_16x16x32_bf16 v[100:103], a[40:43], a[16:19], v[100:103]\n" \
"v_add_u32 v213, s74, v211\n" \
"v_add_u32 v214, s74, v212\n" \
"v_mfma_f32_16x16x32_bf16 v[100:103], a[44:47], a[20:23], v[100:103]\n" \
"s_waitcnt lgkmcnt(3)\n" \
"v_xor_b32 v215, 32, v214\n" \
"v_mfma_f32_16x16x32_bf16 v[112:115], a[32:35], a[24:27], v[112:115]\n" \
"v_mfma_f32_16x16x32_bf16 v[112:115], a[36:39], a[28:31], v[112:115]\n" \
"global_load_lds_dwordx4 v222, s[36:37] offset:3072 sc1\n" \
"v_mfma_f32_16x16x32_bf16 v[116:119], a[40:43], a[24:27], v[116:119]\n" \
"s_waitcnt vmcnt(23) lgkmcnt(0)\n" \
"s_add_u32 s36, s36, 0x1000\n" \
"v_mfma_f32_16x16x32_bf16 v[116:119], a[44:47], a[28:31], v[116:119]\n" \
"s_barrier\n" \
"v_mfma_f32_16x16x32_bf16 v[72:75], a[48:51], a[0:3], v[72:75]\n" \
"s_add_u32 m0, m0, 0x5000\n" \
"ds_read_b64_tr_b16 a[32:33], v214 offset:16384\n" \
"v_mfma_f32_16x16x32_bf16 v[72:75], a[52:55], a[4:7], v[72:75]\n" \
"ds_read_b64_tr_b16 a[34:35], v214 offset:18432\n" \
"global_load_lds_dwordx4 v216, s[38:39] offset:-4096\n" \
"v_mfma_f32_16x16x32_bf16 v[76:79], a[56:59], a[0:3], v[76:79]\n" \
"ds_read_b64_tr_b16 a[36:37], v214 offset:24576\n" \
"v_mfma_f32_16x16x32_bf16 v[76:79], a[60:63], a[4:7], v[76:79]\n" \
"ds_read_b128 a[0:3], v213 offset:0\n" \
"v_mfma_f32_16x16x32_bf16 v[88:91], a[48:51], a[8:11], v[88:91]\n" \
"ds_read_b128 a[4:7], v213 offset:2048\n" \
"v_mfma_f32_16x16x32_bf16 v[88:91], a[52:55], a[12:15], v[88:91]\n" \
"ds_read_b64_tr_b16 a[38:39], v214 offset:26624\n" \
"global_load_lds_dwordx4 v217, s[38:39] offset:-3072\n" \
"v_mfma_f32_16x16x32_bf16 v[92:95], a[56:59], a[8:11], v[92:95]\n" \
"buffer_store_dwordx4 v[12:15], v248, s[56:59], s71 offen offset:3072 sc1\n" \
"ds_read_b64_tr_b16 a[40:41], v215 offset:16384\n" \
"v_cvt_pk_bf16_f32 v16, v32, v33\n" \
"v_cvt_pk_bf16_f32 v17, v34, v35\n" \
"v_cvt_pk_bf16_f32 v18, v36, v37\n" \
"v_mfma_f32_16x16x32_bf16 v[92:95], a[60:63], a[12:15], v[92:95]\n" \
"ds_read_b128 a[8:11], v213 offset:256\n" \
"v_cvt_pk_bf16_f32 v19, v38, v39\n" \
"v_mfma_f32_16x16x32_bf16 v[104:107], a[48:51], a[16:19], v[104:107]\n" \
"ds_read_b128 a[12:15], v213 offset:2304\n" \
"s_add_u32 s71, s76, 4096\n" \
"v_mfma_f32_16x16x32_bf16 v[104:107], a[52:55], a[20:23], v[104:107]\n" \
"ds_read_b64_tr_b16 a[42:43], v215 offset:18432\n" \
"global_load_lds_dwordx4 v218, s[38:39] offset:-2048\n" \
"v_mfma_f32_16x16x32_bf16 v[108:111], a[56:59], a[16:19], v[108:111]\n" \
"buffer_store_dwordx4 v[16:19], v248, s[56:59], s71 offen offset:0 sc1\n" \
"ds_read_b64_tr_b16 a[44:45], v215 offset:24576\n" \
"v_cvt_pk_bf16_f32 v20, v40, v41\n" \
"v_cvt_pk_bf16_f32 v21, v42, v43\n" \
"v_cvt_pk_bf16_f32 v22, v44, v45\n" \
"v_mfma_f32_16x16x32_bf16 v[108:111], a[60:63], a[20:23], v[108:111]\n" \
"ds_read_b128 a[16:19], v213 offset:4096\n" \
"v_cvt_pk_bf16_f32 v23, v46, v47\n" \
"v_mfma_f32_16x16x32_bf16 v[120:123], a[48:51], a[24:27], v[120:123]\n" \
"ds_read_b128 a[20:23], v213 offset:6144\n" \
"global_load_lds_dwordx4 v219, s[38:39] offset:-1024\n" \
"v_mfma_f32_16x16x32_bf16 v[120:123], a[52:55], a[28:31], v[120:123]\n" \
"s_add_u32 s38, s38, 0x20000\n" \
"s_addc_u32 s39, s39, 0\n" \
"ds_read_b64_tr_b16 a[46:47], v215 offset:26624\n" \
"v_mfma_f32_16x16x32_bf16 v[124:127], a[56:59], a[24:27], v[124:127]\n" \
"buffer_store_dwordx4 v[20:23], v248, s[56:59], s71 offen offset:1024 sc1\n" \
"s_sub_u32 s75, s75, 32768\n" \
"s_cselect_b32 s75, 131072, s75\n" \
"v_cvt_pk_bf16_f32 v24, v48, v49\n" \
"v_cvt_pk_bf16_f32 v25, v50, v51\n" \
"v_mfma_f32_16x16x32_bf16 v[124:127], a[60:63], a[28:31], v[124:127]\n" \
"s_waitcnt lgkmcnt(3)\n" \
"v_cvt_pk_bf16_f32 v26, v52, v53\n" \
"v_cvt_pk_bf16_f32 v27, v54, v55\n" \
"v_mfma_f32_16x16x32_bf16 v[64:67], a[32:35], a[0:3], v[64:67]\n" \
"ds_read_b128 a[24:27], v213 offset:4352\n" \
"s_add_u32 m0, s75, s95\n" \
"v_mfma_f32_16x16x32_bf16 v[64:67], a[36:39], a[4:7], v[64:67]\n" \
"s_waitcnt lgkmcnt(1)\n" \
"ds_read_b128 a[28:31], v213 offset:6400\n" \
"global_load_lds_dwordx4 v222, s[36:37] offset:0 sc1\n" \
"v_mfma_f32_16x16x32_bf16 v[68:71], a[40:43], a[0:3], v[68:71]\n" \
"s_sub_u32 s74, s74, 32768\n" \
"s_cselect_b32 s74, 131072, s74\n" \
"ds_read_b64_tr_b16 a[48:49], v214 offset:16896\n" \
"v_mfma_f32_16x16x32_bf16 v[68:71], a[44:47], a[4:7], v[68:71]\n" \
"ds_read_b64_tr_b16 a[50:51], v214 offset:18944\n" \
"v_mfma_f32_16x16x32_bf16 v[80:83], a[32:35], a[8:11], v[80:83]\n" \
"ds_read_b64_tr_b16 a[52:53], v214 offset:25088\n" \
"v_mfma_f32_16x16x32_bf16 v[80:83], a[36:39], a[12:15], v[80:83]\n" \
"ds_read_b64_tr_b16 a[54:55], v214 offset:27136\n" \
"global_load_lds_dwordx4 v222, s[36:37] offset:1024 sc1\n" \
"v_mfma_f32_16x16x32_bf16 v[84:87], a[40:43], a[8:11], v[84:87]\n" \
"ds_read_b64_tr_b16 a[56:57], v215 offset:16896\n" \
"v_mfma_f32_16x16x32_bf16 v[84:87], a[44:47], a[12:15], v[84:87]\n" \
"ds_read_b64_tr_b16 a[58:59], v215 offset:18944\n" \
"v_mfma_f32_16x16x32_bf16 v[96:99], a[32:35], a[16:19], v[96:99]\n" \
"ds_read_b64_tr_b16 a[60:61], v215 offset:25088\n" \
"v_mfma_f32_16x16x32_bf16 v[96:99], a[36:39], a[20:23], v[96:99]\n" \
"ds_read_b64_tr_b16 a[62:63], v215 offset:27136\n" \
"global_load_lds_dwordx4 v222, s[36:37] offset:2048 sc1\n" \
"v_mfma_f32_16x16x32_bf16 v[100:103], a[40:43], a[16:19], v[100:103]\n" \
"v_add_u32 v213, s74, v211\n" \
"v_add_u32 v214, s74, v212\n" \
"v_mfma_f32_16x16x32_bf16 v[100:103], a[44:47], a[20:23], v[100:103]\n" \
"s_waitcnt lgkmcnt(3)\n" \
"v_xor_b32 v215, 32, v214\n" \
"v_mfma_f32_16x16x32_bf16 v[112:115], a[32:35], a[24:27], v[112:115]\n" \
"v_mfma_f32_16x16x32_bf16 v[112:115], a[36:39], a[28:31], v[112:115]\n" \
"global_load_lds_dwordx4 v222, s[36:37] offset:3072 sc1\n" \
"v_mfma_f32_16x16x32_bf16 v[116:119], a[40:43], a[24:27], v[116:119]\n" \
"s_waitcnt vmcnt(26) lgkmcnt(0)\n" \
"s_add_u32 s36, s36, 0x1000\n" \
"v_mfma_f32_16x16x32_bf16 v[116:119], a[44:47], a[28:31], v[116:119]\n" \
"s_barrier\n" \
"v_mfma_f32_16x16x32_bf16 v[72:75], a[48:51], a[0:3], v[72:75]\n" \
"s_add_u32 m0, m0, 0x5000\n" \
"ds_read_b64_tr_b16 a[32:33], v214 offset:16384\n" \
"v_mfma_f32_16x16x32_bf16 v[72:75], a[52:55], a[4:7], v[72:75]\n" \
"ds_read_b64_tr_b16 a[34:35], v214 offset:18432\n" \
"global_load_lds_dwordx4 v216, s[38:39] offset:-4096\n" \
"v_mfma_f32_16x16x32_bf16 v[76:79], a[56:59], a[0:3], v[76:79]\n" \
"ds_read_b64_tr_b16 a[36:37], v214 offset:24576\n" \
"v_mfma_f32_16x16x32_bf16 v[76:79], a[60:63], a[4:7], v[76:79]\n" \
"ds_read_b128 a[0:3], v213 offset:0\n" \
"v_mfma_f32_16x16x32_bf16 v[88:91], a[48:51], a[8:11], v[88:91]\n" \
"ds_read_b128 a[4:7], v213 offset:2048\n" \
"v_mfma_f32_16x16x32_bf16 v[88:91], a[52:55], a[12:15], v[88:91]\n" \
"ds_read_b64_tr_b16 a[38:39], v214 offset:26624\n" \
"global_load_lds_dwordx4 v217, s[38:39] offset:-3072\n" \
"v_mfma_f32_16x16x32_bf16 v[92:95], a[56:59], a[8:11], v[92:95]\n" \
"buffer_store_dwordx4 v[24:27], v248, s[56:59], s71 offen offset:2048 sc1\n" \
"ds_read_b64_tr_b16 a[40:41], v215 offset:16384\n" \
"v_cvt_pk_bf16_f32 v28, v56, v57\n" \
"v_cvt_pk_bf16_f32 v29, v58, v59\n" \
"v_cvt_pk_bf16_f32 v30, v60, v61\n" \
"v_mfma_f32_16x16x32_bf16 v[92:95], a[60:63], a[12:15], v[92:95]\n" \
"ds_read_b128 a[8:11], v213 offset:256\n" \
"v_cvt_pk_bf16_f32 v31, v62, v63\n" \
"v_mfma_f32_16x16x32_bf16 v[104:107], a[48:51], a[16:19], v[104:107]\n" \
"ds_read_b128 a[12:15], v213 offset:2304\n" \
"v_mfma_f32_16x16x32_bf16 v[104:107], a[52:55], a[20:23], v[104:107]\n" \
"ds_read_b64_tr_b16 a[42:43], v215 offset:18432\n" \
"global_load_lds_dwordx4 v218, s[38:39] offset:-2048\n" \
"v_mfma_f32_16x16x32_bf16 v[108:111], a[56:59], a[16:19], v[108:111]\n" \
"buffer_store_dwordx4 v[28:31], v248, s[56:59], s71 offen offset:3072 sc1\n" \
"ds_read_b64_tr_b16 a[44:45], v215 offset:24576\n" \
"v_mfma_f32_16x16x32_bf16 v[108:111], a[60:63], a[20:23], v[108:111]\n" \
"ds_read_b128 a[16:19], v213 offset:4096\n" \
"v_mfma_f32_16x16x32_bf16 v[120:123], a[48:51], a[24:27], v[120:123]\n" \
"ds_read_b128 a[20:23], v213 offset:6144\n" \
"global_load_lds_dwordx4 v219, s[38:39] offset:-1024\n" \
"v_mfma_f32_16x16x32_bf16 v[120:123], a[52:55], a[28:31], v[120:123]\n" \
"s_add_u32 s38, s38, 0x20000\n" \
"s_addc_u32 s39, s39, 0\n" \
"ds_read_b64_tr_b16 a[46:47], v215 offset:26624\n" \
"v_mfma_f32_16x16x32_bf16 v[124:127], a[56:59], a[24:27], v[124:127]\n" \
"s_sub_u32 s75, s75, 32768\n" \
"s_cselect_b32 s75, 131072, s75\n" \
"v_mfma_f32_16x16x32_bf16 v[124:127], a[60:63], a[28:31], v[124:127]\n" \
"s_waitcnt lgkmcnt(3)\n" \
"v_mfma_f32_16x16x32_bf16 v[64:67], a[32:35], a[0:3], v[64:67]\n" \
"ds_read_b128 a[24:27], v213 offset:4352\n" \
"s_add_u32 m0, s75, s95\n" \
"v_mfma_f32_16x16x32_bf16 v[64:67], a[36:39], a[4:7], v[64:67]\n" \
"s_waitcnt lgkmcnt(1)\n" \
"ds_read_b128 a[28:31], v213 offset:6400\n" \
"global_load_lds_dwordx4 v222, s[36:37] offset:0 sc1\n" \
"v_mfma_f32_16x16x32_bf16 v[68:71], a[40:43], a[0:3], v[68:71]\n" \
"s_sub_u32 s74, s74, 32768\n" \
"s_cselect_b32 s74, 131072, s74\n" \
"ds_read_b64_tr_b16 a[48:49], v214 offset:16896\n" \
"v_mfma_f32_16x16x32_bf16 v[68:71], a[44:47], a[4:7], v[68:71]\n" \
"ds_read_b64_tr_b16 a[50:51], v214 offset:18944\n" \
"v_mfma_f32_16x16x32_bf16 v[80:83], a[32:35], a[8:11], v[80:83]\n" \
"ds_read_b64_tr_b16 a[52:53], v214 offset:25088\n" \
"v_mfma_f32_16x16x32_bf16 v[80:83], a[36:39], a[12:15], v[80:83]\n" \
"ds_read_b64_tr_b16 a[54:55], v214 offset:27136\n" \
"global_load_lds_dwordx4 v222, s[36:37] offset:1024 sc1\n" \
"v_mfma_f32_16x16x32_bf16 v[84:87], a[40:43], a[8:11], v[84:87]\n" \
"ds_read_b64_tr_b16 a[56:57], v215 offset:16896\n" \
"v_mfma_f32_16x16x32_bf16 v[84:87], a[44:47], a[12:15], v[84:87]\n" \
"ds_read_b64_tr_b16 a[58:59], v215 offset:18944\n" \
"v_mfma_f32_16x16x32_bf16 v[96:99], a[32:35], a[16:19], v[96:99]\n" \
"ds_read_b64_tr_b16 a[60:61], v215 offset:25088\n" \
"v_mfma_f32_16x16x32_bf16 v[96:99], a[36:39], a[20:23], v[96:99]\n" \
"ds_read_b64_tr_b16 a[62:63], v215 offset:27136\n" \
"global_load_lds_dwordx4 v222, s[36:37] offset:2048 sc1\n" \
"v_mfma_f32_16x16x32_bf16 v[100:103], a[40:43], a[16:19], v[100:103]\n" \
"v_add_u32 v213, s74, v211\n" \
"v_add_u32 v214, s74, v212\n" \
"v_mfma_f32_16x16x32_bf16 v[100:103], a[44:47], a[20:23], v[100:103]\n" \
"s_waitcnt lgkmcnt(3)\n" \
"v_xor_b32 v215, 32, v214\n" \
"v_mfma_f32_16x16x32_bf16 v[112:115], a[32:35], a[24:27], v[112:115]\n" \
"v_mfma_f32_16x16x32_bf16 v[112:115], a[36:39], a[28:31], v[112:115]\n" \
"global_load_lds_dwordx4 v222, s[36:37] offset:3072 sc1\n" \
"v_mfma_f32_16x16x32_bf16 v[116:119], a[40:43], a[24:27], v[116:119]\n" \
"s_waitcnt vmcnt(26) lgkmcnt(0)\n" \
"s_add_u32 s36, s36, 0x1000\n" \
"v_mfma_f32_16x16x32_bf16 v[116:119], a[44:47], a[28:31], v[116:119]\n" \
"s_barrier\n" \
"v_mfma_f32_16x16x32_bf16 v[72:75], a[48:51], a[0:3], v[72:75]\n" \
"s_add_u32 m0, m0, 0x5000\n" \
"ds_read_b64_tr_b16 a[32:33], v214 offset:16384\n" \
"v_mfma_f32_16x16x32_bf16 v[72:75], a[52:55], a[4:7], v[72:75]\n" \
"ds_read_b64_tr_b16 a[34:35], v214 offset:18432\n" \
"global_load_lds_dwordx4 v216, s[38:39] offset:-4096\n" \
"v_mfma_f32_16x16x32_bf16 v[76:79], a[56:59], a[0:3], v[76:79]\n" \
"ds_read_b64_tr_b16 a[36:37], v214 offset:24576\n" \
"v_mfma_f32_16x16x32_bf16 v[76:79], a[60:63], a[4:7], v[76:79]\n" \
"ds_read_b128 a[0:3], v213 offset:0\n" \
"v_mfma_f32_16x16x32_bf16 v[88:91], a[48:51], a[8:11], v[88:91]\n" \
"ds_read_b128 a[4:7], v213 offset:2048\n" \
"v_mfma_f32_16x16x32_bf16 v[88:91], a[52:55], a[12:15], v[88:91]\n" \
"ds_read_b64_tr_b16 a[38:39], v214 offset:26624\n" \
"global_load_lds_dwordx4 v217, s[38:39] offset:-3072\n" \
"v_mfma_f32_16x16x32_bf16 v[92:95], a[56:59], a[8:11], v[92:95]\n" \
"ds_read_b64_tr_b16 a[40:41], v215 offset:16384\n" \
"v_mfma_f32_16x16x32_bf16 v[92:95], a[60:63], a[12:15], v[92:95]\n" \
"ds_read_b128 a[8:11], v213 offset:256\n" \
"v_mfma_f32_16x16x32_bf16 v[104:107], a[48:51], a[16:19], v[104:107]\n" \
"ds_read_b128 a[12:15], v213 offset:2304\n" \
"v_mfma_f32_16x16x32_bf16 v[104:107], a[52:55], a[20:23], v[104:107]\n" \
"ds_read_b64_tr_b16 a[42:43], v215 offset:18432\n" \
"global_load_lds_dwordx4 v218, s[38:39] offset:-2048\n" \
"v_mfma_f32_16x16x32_bf16 v[108:111], a[56:59], a[16:19], v[108:111]\n" \
"ds_read_b64_tr_b16 a[44:45], v215 offset:24576\n" \
"v_mfma_f32_16x16x32_bf16 v[108:111], a[60:63], a[20:23], v[108:111]\n" \
"ds_read_b128 a[16:19], v213 offset:4096\n" \
"v_mfma_f32_16x16x32_bf16 v[120:123], a[48:51], a[24:27], v[120:123]\n" \
"ds_read_b128 a[20:23], v213 offset:6144\n" \
"global_load_lds_dwordx4 v219, s[38:39] offset:-1024\n" \
"v_mfma_f32_16x16x32_bf16 v[120:123], a[52:55], a[28:31], v[120:123]\n" \
"s_add_u32 s38, s38, 0x20000\n" \
"s_addc_u32 s39, s39, 0\n" \
"ds_read_b64_tr_b16 a[46:47], v215 offset:26624\n" \
"v_mfma_f32_16x16x32_bf16 v[124:127], a[56:59], a[24:27], v[124:127]\n" \
"s_sub_u32 s75, s75, 32768\n" \
"s_cselect_b32 s75, 131072, s75\n" \
"v_mfma_f32_16x16x32_bf16 v[124:127], a[60:63], a[28:31], v[124:127]\n" \
"s_waitcnt lgkmcnt(3)\n" \
"v_mfma_f32_16x16x32_bf16 v[64:67], a[32:35], a[0:3], v[64:67]\n" \
"ds_read_b128 a[24:27], v213 offset:4352\n" \
"s_add_u32 m0, s75, s95\n" \
"v_mfma_f32_16x16x32_bf16 v[64:67], a[36:39], a[4:7], v[64:67]\n" \
"s_waitcnt lgkmcnt(1)\n" \
"ds_read_b128 a[28:31], v213 offset:6400\n" \
"global_load_lds_dwordx4 v222, s[36:37] offset:0 sc1\n" \
"v_mfma_f32_16x16x32_bf16 v[68:71], a[40:43], a[0:3], v[68:71]\n" \
"s_sub_u32 s74, s74, 32768\n" \
"s_cselect_b32 s74, 131072, s74\n" \
"ds_read_b64_tr_b16 a[48:49], v214 offset:16896\n" \
"v_mfma_f32_16x16x32_bf16 v[68:71], a[44:47], a[4:7], v[68:71]\n" \
"ds_read_b64_tr_b16 a[50:51], v214 offset:18944\n" \
"v_mfma_f32_16x16x32_bf16 v[80:83], a[32:35], a[8:11], v[80:83]\n" \
"ds_read_b64_tr_b16 a[52:53], v214 offset:25088\n" \
"v_mfma_f32_16x16x32_bf16 v[80:83], a[36:39], a[12:15], v[80:83]\n" \
"ds_read_b64_tr_b16 a[54:55], v214 offset:27136\n" \
"global_load_lds_dwordx4 v222, s[36:37] offset:1024 sc1\n" \
"v_mfma_f32_16x16x32_bf16 v[84:87], a[40:43], a[8:11], v[84:87]\n" \
"ds_read_b64_tr_b16 a[56:57], v215 offset:16896\n" \
"v_mfma_f32_16x16x32_bf16 v[84:87], a[44:47], a[12:15], v[84:87]\n" \
"ds_read_b64_tr_b16 a[58:59], v215 offset:18944\n" \
"v_mfma_f32_16x16x32_bf16 v[96:99], a[32:35], a[16:19], v[96:99]\n" \
"ds_read_b64_tr_b16 a[60:61], v215 offset:25088\n" \
"v_mfma_f32_16x16x32_bf16 v[96:99], a[36:39], a[20:23], v[96:99]\n" \
"ds_read_b64_tr_b16 a[62:63], v215 offset:27136\n" \
"global_load_lds_dwordx4 v222, s[36:37] offset:2048 sc1\n" \
"v_mfma_f32_16x16x32_bf16 v[100:103], a[40:43], a[16:19], v[100:103]\n" \
"v_add_u32 v213, s74, v211\n" \
"v_add_u32 v214, s74, v212\n" \
"v_mfma_f32_16x16x32_bf16 v[100:103], a[44:47], a[20:23], v[100:103]\n" \
"s_waitcnt lgkmcnt(3)\n" \
"v_xor_b32 v215, 32, v214\n" \
"v_mfma_f32_16x16x32_bf16 v[112:115], a[32:35], a[24:27], v[112:115]\n" \
"v_mfma_f32_16x16x32_bf16 v[112:115], a[36:39], a[28:31], v[112:115]\n" \
"global_load_lds_dwordx4 v222, s[36:37] offset:3072 sc1\n" \
"v_mfma_f32_16x16x32_bf16 v[116:119], a[40:43], a[24:27], v[116:119]\n" \
"s_waitcnt vmcnt(23) lgkmcnt(0)\n" \
"s_add_u32 s36, s36, 0x1000\n" \
"v_mfma_f32_16x16x32_bf16 v[116:119], a[44:47], a[28:31], v[116:119]\n" \
"s_barrier\n" \
"v_mfma_f32_16x16x32_bf16 v[72:75], a[48:51], a[0:3], v[72:75]\n" \
"s_add_u32 m0, m0, 0x5000\n" \
"ds_read_b64_tr_b16 a[32:33], v214 offset:16384\n" \
"v_mfma_f32_16x16x32_bf16 v[72:75], a[52:55], a[4:7], v[72:75]\n" \
"ds_read_b64_tr_b16 a[34:35], v214 offset:18432\n" \
"global_load_lds_dwordx4 v216, s[38:39] offset:-4096\n" \
"v_mfma_f32_16x16x32_bf16 v[76:79], a[56:59], a[0:3], v[76:79]\n" \
"ds_read_b64_tr_b16 a[36:37], v214 offset:24576\n" \
"v_mfma_f32_16x16x32_bf16 v[76:79], a[60:63], a[4:7], v[76:79]\n" \
"ds_read_b128 a[0:3], v213 offset:0\n" \
"v_mfma_f32_16x16x32_bf16 v[88:91], a[48:51], a[8:11], v[88:91]\n" \
"ds_read_b128 a[4:7], v213 offset:2048\n" \
"v_mfma_f32_16x16x32_bf16 v[88:91], a[52:55], a[12:15], v[88:91]\n" \
"ds_read_b64_tr_b16 a[38:39], v214 offset:26624\n" \
"global_load_lds_dwordx4 v217, s[38:39] offset:-3072\n" \
"v_mfma_f32_16x16x32_bf16 v[92:95], a[56:59], a[8:11], v[92:95]\n" \
"ds_read_b64_tr_b16 a[40:41], v215 offset:16384\n" \
"v_mfma_f32_16x16x32_bf16 v[92:95], a[60:63], a[12:15], v[92:95]\n" \
"ds_read_b128 a[8:11], v213 offset:256\n" \
"v_mfma_f32_16x16x32_bf16 v[104:107], a[48:51], a[16:19], v[104:107]\n" \
"ds_read_b128 a[12:15], v213 offset:2304\n" \
"v_mfma_f32_16x16x32_bf16 v[104:107], a[52:55], a[20:23], v[104:107]\n" \
"ds_read_b64_tr_b16 a[42:43], v215 offset:18432\n" \
"global_load_lds_dwordx4 v218, s[38:39] offset:-2048\n" \
"v_mfma_f32_16x16x32_bf16 v[108:111], a[56:59], a[16:19], v[108:111]\n" \
"ds_read_b64_tr_b16 a[44:45], v215 offset:24576\n" \
"v_mfma_f32_16x16x32_bf16 v[108:111], a[60:63], a[20:23], v[108:111]\n" \
"ds_read_b128 a[16:19], v213 offset:4096\n" \
"v_mfma_f32_16x16x32_bf16 v[120:123], a[48:51], a[24:27], v[120:123]\n" \
"ds_read_b128 a[20:23], v213 offset:6144\n" \
"global_load_lds_dwordx4 v219, s[38:39] offset:-1024\n" \
"v_mfma_f32_16x16x32_bf16 v[120:123], a[52:55], a[28:31], v[120:123]\n" \
"s_add_u32 s38, s38, 0x20000\n" \
"s_addc_u32 s39, s39, 0\n" \
"ds_read_b64_tr_b16 a[46:47], v215 offset:26624\n" \
"v_mfma_f32_16x16x32_bf16 v[124:127], a[56:59], a[24:27], v[124:127]\n" \
"s_sub_u32 s75, s75, 32768\n" \
"s_cselect_b32 s75, 131072, s75\n" \
"v_mfma_f32_16x16x32_bf16 v[124:127], a[60:63], a[28:31], v[124:127]\n" \
"s_waitcnt lgkmcnt(3)\n" \
"v_mfma_f32_16x16x32_bf16 v[64:67], a[32:35], a[0:3], v[64:67]\n" \
"ds_read_b128 a[24:27], v213 offset:4352\n" \
"s_add_u32 m0, s75, s95\n" \
"v_mfma_f32_16x16x32_bf16 v[64:67], a[36:39], a[4:7], v[64:67]\n" \
"s_waitcnt lgkmcnt(1)\n" \
"ds_read_b128 a[28:31], v213 offset:6400\n" \
"global_load_lds_dwordx4 v222, s[36:37] offset:0 sc1\n" \
"v_mfma_f32_16x16x32_bf16 v[68:71], a[40:43], a[0:3], v[68:71]\n" \
"s_sub_u32 s74, s74, 32768\n" \
"s_cselect_b32 s74, 131072, s74\n" \
"ds_read_b64_tr_b16 a[48:49], v214 offset:16896\n" \
"v_mfma_f32_16x16x32_bf16 v[68:71], a[44:47], a[4:7], v[68:71]\n" \
"ds_read_b64_tr_b16 a[50:51], v214 offset:18944\n" \
"v_mfma_f32_16x16x32_bf16 v[80:83], a[32:35], a[8:11], v[80:83]\n" \
"ds_read_b64_tr_b16 a[52:53], v214 offset:25088\n" \
"v_mfma_f32_16x16x32_bf16 v[80:83], a[36:39], a[12:15], v[80:83]\n" \
"ds_read_b64_tr_b16 a[54:55], v214 offset:27136\n" \
"global_load_lds_dwordx4 v222, s[36:37] offset:1024 sc1\n" \
"v_mfma_f32_16x16x32_bf16 v[84:87], a[40:43], a[8:11], v[84:87]\n" \
"ds_read_b64_tr_b16 a[56:57], v215 offset:16896\n" \
"v_mfma_f32_16x16x32_bf16 v[84:87], a[44:47], a[12:15], v[84:87]\n" \
"ds_read_b64_tr_b16 a[58:59], v215 offset:18944\n" \
"v_mfma_f32_16x16x32_bf16 v[96:99], a[32:35], a[16:19], v[96:99]\n" \
"ds_read_b64_tr_b16 a[60:61], v215 offset:25088\n" \
"v_mfma_f32_16x16x32_bf16 v[96:99], a[36:39], a[20:23], v[96:99]\n" \
"ds_read_b64_tr_b16 a[62:63], v215 offset:27136\n" \
"global_load_lds_dwordx4 v222, s[36:37] offset:2048 sc1\n" \
"v_mfma_f32_16x16x32_bf16 v[100:103], a[40:43], a[16:19], v[100:103]\n" \
"v_add_u32 v213, s74, v211\n" \
"v_add_u32 v214, s74, v212\n" \
"v_mfma_f32_16x16x32_bf16 v[100:103], a[44:47], a[20:23], v[100:103]\n" \
"s_waitcnt lgkmcnt(3)\n" \
"v_xor_b32 v215, 32, v214\n" \
"v_mfma_f32_16x16x32_bf16 v[112:115], a[32:35], a[24:27], v[112:115]\n" \
"v_mfma_f32_16x16x32_bf16 v[112:115], a[36:39], a[28:31], v[112:115]\n" \
"global_load_lds_dwordx4 v222, s[36:37] offset:3072 sc1\n" \
"v_mfma_f32_16x16x32_bf16 v[116:119], a[40:43], a[24:27], v[116:119]\n" \
"s_waitcnt vmcnt(20) lgkmcnt(0)\n" \
"s_add_u32 s36, s36, 0x1000\n" \
"v_mfma_f32_16x16x32_bf16 v[116:119], a[44:47], a[28:31], v[116:119]\n" \
"s_barrier\n" \
"v_mfma_f32_16x16x32_bf16 v[72:75], a[48:51], a[0:3], v[72:75]\n" \
"s_add_u32 m0, m0, 0x5000\n" \
"ds_read_b64_tr_b16 a[32:33], v214 offset:16384\n" \
"buffer_store_dword v224, v225, s[56:59], s79 offen sc1\n" \
"v_mfma_f32_16x16x32_bf16 v[72:75], a[52:55], a[4:7], v[72:75]\n" \
"ds_read_b64_tr_b16 a[34:35], v214 offset:18432\n" \
"global_load_lds_dwordx4 v216, s[38:39] offset:-4096\n" \
"v_mfma_f32_16x16x32_bf16 v[76:79], a[56:59], a[0:3], v[76:79]\n" \
"ds_read_b64_tr_b16 a[36:37], v214 offset:24576\n" \
"v_mfma_f32_16x16x32_bf16 v[76:79], a[60:63], a[4:7], v[76:79]\n" \
"ds_read_b128 a[0:3], v213 offset:0\n" \
"v_mfma_f32_16x16x32_bf16 v[88:91], a[48:51], a[8:11], v[88:91]\n" \
"ds_read_b128 a[4:7], v213 offset:2048\n" \
"v_mfma_f32_16x16x32_bf16 v[88:91], a[52:55], a[12:15], v[88:91]\n" \
"ds_read_b64_tr_b16 a[38:39], v214 offset:26624\n" \
"global_load_lds_dwordx4 v217, s[38:39] offset:-3072\n" \
"v_mfma_f32_16x16x32_bf16 v[92:95], a[56:59], a[8:11], v[92:95]\n" \
"ds_read_b64_tr_b16 a[40:41], v215 offset:16384\n" \
"v_mfma_f32_16x16x32_bf16 v[92:95], a[60:63], a[12:15], v[92:95]\n" \
"ds_read_b128 a[8:11], v213 offset:256\n" \
"v_mfma_f32_16x16x32_bf16 v[104:107], a[48:51], a[16:19], v[104:107]\n" \
"ds_read_b128 a[12:15], v213 offset:2304\n" \
"v_mfma_f32_16x16x32_bf16 v[104:107], a[52:55], a[20:23], v[104:107]\n" \
"ds_read_b64_tr_b16 a[42:43], v215 offset:18432\n" \
"global_load_lds_dwordx4 v218, s[38:39] offset:-2048\n" \
"v_mfma_f32_16x16x32_bf16 v[108:111], a[56:59], a[16:19], v[108:111]\n" \
"ds_read_b64_tr_b16 a[44:45], v215 offset:24576\n" \
"v_mfma_f32_16x16x32_bf16 v[108:111], a[60:63], a[20:23], v[108:111]\n" \
"ds_read_b128 a[16:19], v213 offset:4096\n" \
"v_mfma_f32_16x16x32_bf16 v[120:123], a[48:51], a[24:27], v[120:123]\n" \
"ds_read_b128 a[20:23], v213 offset:6144\n" \
"global_load_lds_dwordx4 v219, s[38:39] offset:-1024\n" \
"v_mfma_f32_16x16x32_bf16 v[120:123], a[52:55], a[28:31], v[120:123]\n" \
"s_add_u32 s38, s38, 0x20000\n" \
"s_addc_u32 s39, s39, 0\n" \
"ds_read_b64_tr_b16 a[46:47], v215 offset:26624\n" \
"v_mfma_f32_16x16x32_bf16 v[124:127], a[56:59], a[24:27], v[124:127]\n" \
"s_sub_u32 s75, s75, 32768\n" \
"s_cselect_b32 s75, 131072, s75\n" \
"v_mfma_f32_16x16x32_bf16 v[124:127], a[60:63], a[28:31], v[124:127]\n" \
"s_waitcnt lgkmcnt(3)\n" \
"s_mov_b32 s72, 6\n" \
"s_call_b64 s[68:69], Lo64_%=\n" \
"v_mfma_f32_16x16x32_bf16 v[64:67], a[32:35], a[0:3], v[64:67]\n" \
"ds_read_b128 a[24:27], v213 offset:4352\n" \
"v_mfma_f32_16x16x32_bf16 v[64:67], a[36:39], a[4:7], v[64:67]\n" \
"s_waitcnt lgkmcnt(1)\n" \
"ds_read_b128 a[28:31], v213 offset:6400\n" \
"v_mfma_f32_16x16x32_bf16 v[68:71], a[40:43], a[0:3], v[68:71]\n" \
"s_sub_u32 s74, s74, 32768\n" \
"s_cselect_b32 s74, 131072, s74\n" \
"ds_read_b64_tr_b16 a[48:49], v214 offset:16896\n" \
"v_mfma_f32_16x16x32_bf16 v[68:71], a[44:47], a[4:7], v[68:71]\n" \
"ds_read_b64_tr_b16 a[50:51], v214 offset:18944\n" \
"v_mfma_f32_16x16x32_bf16 v[80:83], a[32:35], a[8:11], v[80:83]\n" \
"ds_read_b64_tr_b16 a[52:53], v214 offset:25088\n" \
"v_mfma_f32_16x16x32_bf16 v[80:83], a[36:39], a[12:15], v[80:83]\n" \
"ds_read_b64_tr_b16 a[54:55], v214 offset:27136\n" \
"v_mfma_f32_16x16x32_bf16 v[84:87], a[40:43], a[8:11], v[84:87]\n" \
"ds_read_b64_tr_b16 a[56:57], v215 offset:16896\n" \
"v_mfma_f32_16x16x32_bf16 v[84:87], a[44:47], a[12:15], v[84:87]\n" \
"ds_read_b64_tr_b16 a[58:59], v215 offset:18944\n" \
"v_mfma_f32_16x16x32_bf16 v[96:99], a[32:35], a[16:19], v[96:99]\n" \
"ds_read_b64_tr_b16 a[60:61], v215 offset:25088\n" \
"v_mfma_f32_16x16x32_bf16 v[96:99], a[36:39], a[20:23], v[96:99]\n" \
"ds_read_b64_tr_b16 a[62:63], v215 offset:27136\n" \
"v_mfma_f32_16x16x32_bf16 v[100:103], a[40:43], a[16:19], v[100:103]\n" \
"Lpg13280_%=:\n" \
"s_cbranch_execz Lcb1_%=\n" \
"v_add_u32 v213, s74, v211\n" \
"v_add_u32 v214, s74, v212\n" \
"v_mfma_f32_16x16x32_bf16 v[100:103], a[44:47], a[20:23], v[100:103]\n" \
"s_waitcnt lgkmcnt(3)\n" \
"v_xor_b32 v215, 32, v214\n" \
"v_mfma_f32_16x16x32_bf16 v[112:115], a[32:35], a[24:27], v[112:115]\n" \
"v_mfma_f32_16x16x32_bf16 v[112:115], a[36:39], a[28:31], v[112:115]\n" \
"v_mfma_f32_16x16x32_bf16 v[116:119], a[40:43], a[24:27], v[116:119]\n" \
"s_waitcnt vmcnt(16) lgkmcnt(0)\n" \
"v_mfma_f32_16x16x32_bf16 v[116:119], a[44:47], a[28:31], v[116:119]\n" \
"s_barrier\n" \
"v_mfma_f32_16x16x32_bf16 v[72:75], a[48:51], a[0:3], v[72:75]\n" \
"s_lshl_b32 s83, s95, 1\n" \
"s_add_u32 s83, s83, s74\n" \
"ds_read_b64_tr_b16 a[32:33], v214 offset:16384\n" \
"v_mfma_f32_16x16x32_bf16 v[72:75], a[52:55], a[4:7], v[72:75]\n" \
"ds_read_b64_tr_b16 a[34:35], v214 offset:18432\n" \
"v_mfma_f32_16x16x32_bf16 v[76:79], a[56:59], a[0:3], v[76:79]\n" \
"ds_read_b64_tr_b16 a[36:37], v214 offset:24576\n" \
"v_mfma_f32_16x16x32_bf16 v[76:79], a[60:63], a[4:7], v[76:79]\n" \
"ds_read_b128 a[0:3], v213 offset:0\n" \
"v_mfma_f32_16x16x32_bf16 v[88:91], a[48:51], a[8:11], v[88:91]\n" \
"ds_read_b128 a[4:7], v213 offset:2048\n" \
"v_mfma_f32_16x16x32_bf16 v[88:91], a[52:55], a[12:15], v[88:91]\n" \
"ds_read_b64_tr_b16 a[38:39], v214 offset:26624\n" \
"v_mfma_f32_16x16x32_bf16 v[92:95], a[56:59], a[8:11], v[92:95]\n" \
"ds_read_b64_tr_b16 a[40:41], v215 offset:16384\n" \
"v_mfma_f32_16x16x32_bf16 v[92:95], a[60:63], a[12:15], v[92:95]\n" \
"ds_read_b128 a[8:11], v213 offset:256\n" \
"v_mfma_f32_16x16x32_bf16 v[104:107], a[48:51], a[16:19], v[104:107]\n" \
"ds_read_b128 a[12:15], v213 offset:2304\n" \
"v_mfma_f32_16x16x32_bf16 v[104:107], a[52:55], a[20:23], v[104:107]\n" \
"ds_read_b64_tr_b16 a[42:43], v215 offset:18432\n" \
"v_mfma_f32_16x16x32_bf16 v[108:111], a[56:59], a[16:19], v[108:111]\n" \
"ds_read_b64_tr_b16 a[44:45], v215 offset:24576\n" \
"v_mfma_f32_16x16x32_bf16 v[108:111], a[60:63], a[20:23], v[108:111]\n" \
"ds_read_b128 a[16:19], v213 offset:4096\n" \
"v_mfma_f32_16x16x32_bf16 v[120:123], a[48:51], a[24:27], v[120:123]\n" \
"ds_read_b128 a[20:23], v213 offset:6144\n" \
"v_mfma_f32_16x16x32_bf16 v[120:123], a[52:55], a[28:31], v[120:123]\n" \
"ds_read_b64_tr_b16 a[46:47], v215 offset:26624\n" \
"v_mfma_f32_16x16x32_bf16 v[124:127], a[56:59], a[24:27], v[124:127]\n" \
"v_mfma_f32_16x16x32_bf16 v[124:127], a[60:63], a[28:31], v[124:127]\n" \
"s_waitcnt lgkmcnt(3)\n" \
"v_mfma_f32_16x16x32_bf16 v[64:67], a[32:35], a[0:3], v[64:67]\n" \
"ds_read_b128 a[24:27], v213 offset:4352\n" \
"v_mfma_f32_16x16x32_bf16 v[64:67], a[36:39], a[4:7], v[64:67]\n" \
"s_waitcnt lgkmcnt(1)\n" \
"ds_read_b128 a[28:31], v213 offset:6400\n" \
"v_lshrrev_b32 v132, 4, v248\n" \
"v_mfma_f32_16x16x32_bf16 v[68:71], a[40:43], a[0:3], v[68:71]\n" \
"s_sub_u32 s74, s74, 32768\n" \
"s_cselect_b32 s74, 131072, s74\n" \
"ds_read_b64_tr_b16 a[48:49], v214 offset:16896\n" \
"v_mfma_f32_16x16x32_bf16 v[68:71], a[44:47], a[4:7], v[68:71]\n" \
"ds_read_b64_tr_b16 a[50:51], v214 offset:18944\n" \
"v_and_b32 v133, 15, v132\n" \
"v_mfma_f32_16x16x32_bf16 v[80:83], a[32:35], a[8:11], v[80:83]\n" \
"ds_read_b64_tr_b16 a[52:53], v214 offset:25088\n" \
"v_mul_u32_u24 v128, 144, v133\n" \
"v_mfma_f32_16x16x32_bf16 v[80:83], a[36:39], a[12:15], v[80:83]\n" \
"ds_read_b64_tr_b16 a[54:55], v214 offset:27136\n" \
"v_lshrrev_b32 v133, 4, v132\n" \
"v_mfma_f32_16x16x32_bf16 v[84:87], a[40:43], a[8:11], v[84:87]\n" \
"ds_read_b64_tr_b16 a[56:57], v215 offset:16896\n" \
"v_lshl_add_u32 v128, v133, 3, v128\n" \
"v_mfma_f32_16x16x32_bf16 v[84:87], a[44:47], a[12:15], v[84:87]\n" \
"ds_read_b64_tr_b16 a[58:59], v215 offset:18944\n" \
"v_add_u32 v128, s83, v128\n" \
"v_mfma_f32_16x16x32_bf16 v[96:99], a[32:35], a[16:19], v[96:99]\n" \
"ds_read_b64_tr_b16 a[60:61], v215 offset:25088\n" \
"v_lshrrev_b32 v133, 3, v132\n" \
"v_mfma_f32_16x16x32_bf16 v[96:99], a[36:39], a[20:23], v[96:99]\n" \
"ds_read_b64_tr_b16 a[62:63], v215 offset:27136\n" \
"v_and_b32 v134, 7, v132\n" \
"v_mfma_f32_16x16x32_bf16 v[100:103], a[40:43], a[16:19], v[100:103]\n" \
"v_add_u32 v213, s74, v211\n" \
"v_add_u32 v214, s74, v212\n" \
"v_mfma_f32_16x16x32_bf16 v[100:103], a[44:47], a[20:23], v[100:103]\n" \
"s_waitcnt lgkmcnt(3)\n" \
"v_xor_b32 v215, 32, v214\n" \
"v_mul_u32_u24 v129, 144, v133\n" \
"v_mfma_f32_16x16x32_bf16 v[112:115], a[32:35], a[24:27], v[112:115]\n" \
"v_lshl_add_u32 v129, v134, 4, v129\n" \
"v_add_u32 v129, s83, v129\n" \
"v_mfma_f32_16x16x32_bf16 v[112:115], a[36:39], a[28:31], v[112:115]\n" \
"v_lshlrev_b32 v134, 4, v134\n" \
"v_lshl_add_u32 v130, v133, 13, v134\n" \
"v_mfma_f32_16x16x32_bf16 v[116:119], a[40:43], a[24:27], v[116:119]\n" \
"s_waitcnt vmcnt(0) lgkmcnt(0)\n" \
"v_mfma_f32_16x16x32_bf16 v[116:119], a[44:47], a[28:31], v[116:119]\n" \
"s_barrier\n" \
"v_mfma_f32_16x16x32_bf16 v[72:75], a[48:51], a[0:3], v[72:75]\n" \
"ds_read_b128 a[192:195], v213 offset:0\n" \
"ds_read_b64_tr_b16 a[128:129], v214 offset:16384\n" \
"v_mfma_f32_16x16x32_bf16 v[72:75], a[52:55], a[4:7], v[72:75]\n" \
"s_sub_u32 s74, s74, 32768\n" \
"s_cselect_b32 s74, 131072, s74\n" \
"ds_read_b64_tr_b16 a[130:131], v214 offset:18432\n" \
"v_mfma_f32_16x16x32_bf16 v[76:79], a[56:59], a[0:3], v[76:79]\n" \
"ds_read_b128 a[196:199], v213 offset:2048\n" \
"ds_read_b64_tr_b16 a[132:133], v214 offset:24576\n" \
"v_mfma_f32_16x16x32_bf16 v[76:79], a[60:63], a[4:7], v[76:79]\n" \
"ds_read_b64_tr_b16 a[134:135], v214 offset:26624\n" \
"v_add_u32 v226, s74, v211\n" \
"v_mfma_f32_16x16x32_bf16 v[88:91], a[48:51], a[8:11], v[88:91]\n" \
"ds_read_b128 a[200:203], v213 offset:256\n" \
"ds_read_b64_tr_b16 a[136:137], v215 offset:16384\n" \
"v_mfma_f32_16x16x32_bf16 v[88:91], a[52:55], a[12:15], v[88:91]\n" \
"ds_read_b64_tr_b16 a[138:139], v215 offset:18432\n" \
"v_add_u32 v227, s74, v212\n" \
"v_mfma_f32_16x16x32_bf16 v[92:95], a[56:59], a[8:11], v[92:95]\n" \
"ds_read_b128 a[204:207], v213 offset:2304\n" \
"ds_read_b64_tr_b16 a[140:141], v215 offset:24576\n" \
"v_mfma_f32_16x16x32_bf16 v[92:95], a[60:63], a[12:15], v[92:95]\n" \
"ds_read_b64_tr_b16 a[142:143], v215 offset:26624\n" \
"v_xor_b32 v228, 32, v227\n" \
"v_mfma_f32_16x16x32_bf16 v[104:107], a[48:51], a[16:19], v[104:107]\n" \
"ds_read_b64_tr_b16 a[144:145], v214 offset:16896\n" \
"v_mfma_f32_16x16x32_bf16 v[104:107], a[52:55], a[20:23], v[104:107]\n" \
"ds_read_b64_tr_b16 a[146:147], v214 offset:18944\n" \
"v_mfma_f32_16x16x32_bf16 v[108:111], a[56:59], a[16:19], v[108:111]\n" \
"ds_read_b64_tr_b16 a[148:149], v214 offset:25088\n" \
"v_mfma_f32_16x16x32_bf16 v[108:111], a[60:63], a[20:23], v[108:111]\n" \
"ds_read_b64_tr_b16 a[150:151], v214 offset:27136\n" \
"v_mfma_f32_16x16x32_bf16 v[120:123], a[48:51], a[24:27], v[120:123]\n" \
"ds_read_b64_tr_b16 a[152:153], v215 offset:16896\n" \
"v_mfma_f32_16x16x32_bf16 v[120:123], a[52:55], a[28:31], v[120:123]\n" \
"ds_read_b64_tr_b16 a[154:155], v215 offset:18944\n" \
"v_mfma_f32_16x16x32_bf16 v[124:127], a[56:59], a[24:27], v[124:127]\n" \
"ds_read_b64_tr_b16 a[156:157], v215 offset:25088\n" \
"v_mfma_f32_16x16x32_bf16 v[124:127], a[60:63], a[28:31], v[124:127]\n" \
"s_waitcnt lgkmcnt(4)\n" \
"ds_read_b64_tr_b16 a[158:159], v215 offset:27136\n" \
"v_mfma_f32_16x16x32_bf16 v[64:67], a[128:131], a[192:195], v[64:67]\n" \
"ds_read_b128 a[224:227], v226 offset:0\n" \
"ds_read_b64_tr_b16 a[160:161], v227 offset:16384\n" \
"v_mfma_f32_16x16x32_bf16 v[64:67], a[132:135], a[196:199], v[64:67]\n" \
"ds_read_b64_tr_b16 a[162:163], v227 offset:18432\n" \
"v_mfma_f32_16x16x32_bf16 v[80:83], a[128:131], a[200:203], v[80:83]\n" \
"ds_read_b64_tr_b16 a[164:165], v227 offset:24576\n" \
"v_mfma_f32_16x16x32_bf16 v[80:83], a[132:135], a[204:207], v[80:83]\n" \
"ds_read_b128 a[228:231], v226 offset:2048\n" \
"ds_read_b64_tr_b16 a[166:167], v227 offset:26624\n" \
"v_mfma_f32_16x16x32_bf16 v[68:71], a[136:139], a[192:195], v[68:71]\n" \
"ds_read_b64_tr_b16 a[168:169], v228 offset:16384\n" \
"v_mfma_f32_16x16x32_bf16 v[68:71], a[140:143], a[196:199], v[68:71]\n" \
"ds_read_b64_tr_b16 a[170:171], v228 offset:18432\n" \
"v_mfma_f32_16x16x32_bf16 v[84:87], a[136:139], a[200:203], v[84:87]\n" \
"ds_read_b128 a[232:235], v226 offset:256\n" \
"ds_read_b64_tr_b16 a[172:173], v228 offset:24576\n" \
"v_mfma_f32_16x16x32_bf16 v[84:87], a[140:143], a[204:207], v[84:87]\n" \
"s_waitcnt lgkmcnt(6)\n" \
"ds_read_b64_tr_b16 a[174:175], v228 offset:26624\n" \
"v_mfma_f32_16x16x32_bf16 v[72:75], a[144:147], a[192:195], v[72:75]\n" \
"ds_read_b64_tr_b16 a[176:177], v227 offset:16896\n" \
"v_mfma_f32_16x16x32_bf16 v[72:75], a[148:151], a[196:199], v[72:75]\n" \
"ds_read_b128 a[236:239], v226 offset:2304\n" \
"ds_read_b64_tr_b16 a[178:179], v227 offset:18944\n" \
"v_mfma_f32_16x16x32_bf16 v[88:91], a[144:147], a[200:203], v[88:91]\n" \
"ds_read_b64_tr_b16 a[180:181], v227 offset:25088\n" \
"v_mfma_f32_16x16x32_bf16 v[88:91], a[148:151], a[204:207], v[88:91]\n" \
"ds_read_b64_tr_b16 a[182:183], v227 offset:27136\n" \
"v_mfma_f32_16x16x32_bf16 v[76:79], a[152:155], a[192:195], v[76:79]\n" \
"ds_read_b64_tr_b16 a[184:185], v228 offset:16896\n" \
"v_mfma_f32_16x16x32_bf16 v[76:79], a[156:159], a[196:199], v[76:79]\n" \
"ds_read_b64_tr_b16 a[186:187], v228 offset:18944\n" \
"v_mfma_f32_16x16x32_bf16 v[92:95], a[152:155], a[200:203], v[92:95]\n" \
"ds_read_b64_tr_b16 a[188:189], v228 offset:25088\n" \
"v_mfma_f32_16x16x32_bf16 v[92:95], a[156:159], a[204:207], v[92:95]\n" \
"s_waitcnt lgkmcnt(4)\n" \
"ds_read_b64_tr_b16 a[190:191], v228 offset:27136\n" \
"v_mfma_f32_16x16x32_bf16 v[64:67], a[160:163], a[224:227], v[64:67]\n" \
"ds_read_b128 a[208:211], v213 offset:4096\n" \
"v_mfma_f32_16x16x32_bf16 v[64:67], a[164:167], a[228:231], v[64:67]\n" \
"ds_read_b128 a[212:215], v213 offset:6144\n" \
"v_mfma_f32_16x16x32_bf16 v[80:83], a[160:163], a[232:235], v[80:83]\n" \
"ds_read_b128 a[240:243], v226 offset:4096\n" \
"v_mfma_f32_16x16x32_bf16 v[80:83], a[164:167], a[236:239], v[80:83]\n" \
"ds_read_b128 a[244:247], v226 offset:6144\n" \
"v_mfma_f32_16x16x32_bf16 v[68:71], a[168:171], a[224:227], v[68:71]\n" \
"ds_read_b128 a[216:219], v213 offset:4352\n" \
"v_cvt_pk_bf16_f32 v64, v64, v65\n" \
"v_mfma_f32_16x16x32_bf16 v[68:71], a[172:175], a[228:231], v[68:71]\n" \
"ds_read_b128 a[220:223], v213 offset:6400\n" \
"v_cvt_pk_bf16_f32 v65, v66, v67\n" \
"v_mfma_f32_16x16x32_bf16 v[84:87], a[168:171], a[232:235], v[84:87]\n" \
"ds_read_b128 a[248:251], v226 offset:4352\n" \
"v_mfma_f32_16x16x32_bf16 v[84:87], a[172:175], a[236:239], v[84:87]\n" \
"s_waitcnt lgkmcnt(4)\n" \
"ds_read_b128 a[252:255], v226 offset:6400\n" \
"v_mfma_f32_16x16x32_bf16 v[72:75], a[176:179], a[224:227], v[72:75]\n" \
"ds_write_b64 v128, v[64:65] offset:0\n" \
"v_mfma_f32_16x16x32_bf16 v[72:75], a[180:183], a[228:231], v[72:75]\n" \
"v_cvt_pk_bf16_f32 v80, v80, v81\n" \
"v_cvt_pk_bf16_f32 v81, v82, v83\n" \
"v_mfma_f32_16x16x32_bf16 v[88:91], a[176:179], a[232:235], v[88:91]\n" \
"ds_write_b64 v128, v[80:81] offset:2304\n" \
"v_mfma_f32_16x16x32_bf16 v[88:91], a[180:183], a[236:239], v[88:91]\n" \
"v_cvt_pk_bf16_f32 v68, v68, v69\n" \
"v_cvt_pk_bf16_f32 v69, v70, v71\n" \
"v_mfma_f32_16x16x32_bf16 v[76:79], a[184:187], a[224:227], v[76:79]\n" \
"ds_write_b64 v128, v[68:69] offset:32\n" \
"v_mfma_f32_16x16x32_bf16 v[76:79], a[188:191], a[228:231], v[76:79]\n" \
"v_cvt_pk_bf16_f32 v84, v84, v85\n" \
"v_cvt_pk_bf16_f32 v85, v86, v87\n" \
"v_mfma_f32_16x16x32_bf16 v[92:95], a[184:187], a[232:235], v[92:95]\n" \
"ds_write_b64 v128, v[84:85] offset:2336\n" \
"v_mfma_f32_16x16x32_bf16 v[92:95], a[188:191], a[236:239], v[92:95]\n" \
"v_cvt_pk_bf16_f32 v72, v72, v73\n" \
"v_cvt_pk_bf16_f32 v73, v74, v75\n" \
"v_mfma_f32_16x16x32_bf16 v[96:99], a[128:131], a[208:211], v[96:99]\n" \
"ds_write_b64 v128, v[72:73] offset:64\n" \
"v_mfma_f32_16x16x32_bf16 v[96:99], a[132:135], a[212:215], v[96:99]\n" \
"s_waitcnt lgkmcnt(2)\n" \
"v_cvt_pk_bf16_f32 v88, v88, v89\n" \
"v_cvt_pk_bf16_f32 v89, v90, v91\n" \
"v_mfma_f32_16x16x32_bf16 v[96:99], a[160:163], a[240:243], v[96:99]\n" \
"ds_write_b64 v128, v[88:89] offset:2368\n" \
"v_mfma_f32_16x16x32_bf16 v[96:99], a[164:167], a[244:247], v[96:99]\n" \
"v_cvt_pk_bf16_f32 v76, v76, v77\n" \
"v_cvt_pk_bf16_f32 v77, v78, v79\n" \
"v_mfma_f32_16x16x32_bf16 v[100:103], a[136:139], a[208:211], v[100:103]\n" \
"ds_write_b64 v128, v[76:77] offset:96\n" \
"v_mfma_f32_16x16x32_bf16 v[100:103], a[140:143], a[212:215], v[100:103]\n" \
"v_cvt_pk_bf16_f32 v92, v92, v93\n" \
"v_cvt_pk_bf16_f32 v93, v94, v95\n" \
"v_mfma_f32_16x16x32_bf16 v[100:103], a[168:171], a[240:243], v[100:103]\n" \
"ds_write_b64 v128, v[92:93] offset:2400\n" \
"v_mfma_f32_16x16x32_bf16 v[100:103], a[172:175], a[244:247], v[100:103]\n" \
"ds_read_b128 v[140:143], v129 offset:0\n" \
"v_mfma_f32_16x16x32_bf16 v[104:107], a[144:147], a[208:211], v[104:107]\n" \
"ds_read_b128 v[144:147], v129 offset:1152\n" \
"v_mfma_f32_16x16x32_bf16 v[104:107], a[148:151], a[212:215], v[104:107]\n" \
"ds_read_b128 v[148:151], v129 offset:2304\n" \
"v_mfma_f32_16x16x32_bf16 v[104:107], a[176:179], a[240:243], v[104:107]\n" \
"ds_read_b128 v[152:155], v129 offset:3456\n" \
"v_mfma_f32_16x16x32_bf16 v[104:107], a[180:183], a[244:247], v[104:107]\n" \
"v_cvt_pk_bf16_f32 v96, v96, v97\n" \
"v_cvt_pk_bf16_f32 v97, v98, v99\n" \
"v_mfma_f32_16x16x32_bf16 v[108:111], a[152:155], a[208:211], v[108:111]\n" \
"ds_write_b64 v128, v[96:97] offset:0\n" \
"v_mfma_f32_16x16x32_bf16 v[108:111], a[156:159], a[212:215], v[108:111]\n" \
"v_cvt_pk_bf16_f32 v100, v100, v101\n" \
"v_cvt_pk_bf16_f32 v101, v102, v103\n" \
"v_mfma_f32_16x16x32_bf16 v[108:111], a[184:187], a[240:243], v[108:111]\n" \
"s_add_u32 s70, s77, 0\n" \
"ds_write_b64 v128, v[100:101] offset:32\n" \
"v_mfma_f32_16x16x32_bf16 v[108:111], a[188:191], a[244:247], v[108:111]\n" \
"s_waitcnt lgkmcnt(5)\n" \
"v_cvt_pk_bf16_f32 v104, v104, v105\n" \
"v_cvt_pk_bf16_f32 v105, v106, v107\n" \
"v_mfma_f32_16x16x32_bf16 v[112:115], a[128:131], a[216:219], v[112:115]\n" \
"buffer_store_dwordx4 v[140:143], v130, s[52:55], s70 offen sc1\n" \
"ds_write_b64 v128, v[104:105] offset:64\n" \
"v_mfma_f32_16x16x32_bf16 v[112:115], a[132:135], a[220:223], v[112:115]\n" \
"v_mfma_f32_16x16x32_bf16 v[112:115], a[160:163], a[248:251], v[112:115]\n" \
"v_cvt_pk_bf16_f32 v108, v108, v109\n" \
"v_cvt_pk_bf16_f32 v109, v110, v111\n" \
"v_mfma_f32_16x16x32_bf16 v[112:115], a[164:167], a[252:255], v[112:115]\n" \
"ds_write_b64 v128, v[108:109] offset:96\n" \
"v_mfma_f32_16x16x32_bf16 v[116:119], a[136:139], a[216:219], v[116:119]\n" \
"s_add_u32 s70, s77, 65536\n" \
"ds_read_b128 v[156:159], v129 offset:0\n" \
"s_waitcnt lgkmcnt(7)\n" \
"v_mfma_f32_16x16x32_bf16 v[116:119], a[140:143], a[220:223], v[116:119]\n" \
"buffer_store_dwordx4 v[144:147], v130, s[52:55], s70 offen sc1\n" \
"ds_read_b128 v[160:163], v129 offset:1152\n" \
"v_mfma_f32_16x16x32_bf16 v[116:119], a[168:171], a[248:251], v[116:119]\n" \
"v_cvt_pk_bf16_f32 v112, v112, v113\n" \
"v_cvt_pk_bf16_f32 v113, v114, v115\n" \
"v_mfma_f32_16x16x32_bf16 v[116:119], a[172:175], a[252:255], v[116:119]\n" \
"ds_write_b64 v128, v[112:113] offset:2304\n" \
"v_mfma_f32_16x16x32_bf16 v[120:123], a[144:147], a[216:219], v[120:123]\n" \
"v_mfma_f32_16x16x32_bf16 v[120:123], a[148:151], a[220:223], v[120:123]\n" \
"s_add_u32 s70, s77, 4096\n" \
"s_waitcnt lgkmcnt(8)\n" \
"v_mfma_f32_16x16x32_bf16 v[120:123], a[176:179], a[248:251], v[120:123]\n" \
"buffer_store_dwordx4 v[148:151], v130, s[52:55], s70 offen sc1\n" \
"v_cvt_pk_bf16_f32 v116, v116, v117\n" \
"v_cvt_pk_bf16_f32 v117, v118, v119\n" \
"v_mfma_f32_16x16x32_bf16 v[120:123], a[180:183], a[252:255], v[120:123]\n" \
"ds_write_b64 v128, v[116:117] offset:2336\n" \
"v_mfma_f32_16x16x32_bf16 v[124:127], a[152:155], a[216:219], v[124:127]\n" \
"v_mfma_f32_16x16x32_bf16 v[124:127], a[156:159], a[220:223], v[124:127]\n" \
"v_mfma_f32_16x16x32_bf16 v[124:127], a[184:187], a[248:251], v[124:127]\n" \
"s_add_u32 s70, s77, 69632\n" \
"v_cvt_pk_bf16_f32 v120, v120, v121\n" \
"v_cvt_pk_bf16_f32 v121, v122, v123\n" \
"v_mfma_f32_16x16x32_bf16 v[124:127], a[188:191], a[252:255], v[124:127]\n" \
"s_waitcnt lgkmcnt(8)\n" \
"ds_write_b64 v128, v[120:121] offset:2368\n" \
"buffer_store_dwordx4 v[152:155], v130, s[52:55], s70 offen sc1\n" \
"s_add_u32 s70, s77, 131072\n" \
"s_waitcnt lgkmcnt(4)\n" \
"s_nop 2\n" \
"v_cvt_pk_bf16_f32 v124, v124, v125\n" \
"v_cvt_pk_bf16_f32 v125, v126, v127\n" \
"ds_write_b64 v128, v[124:125] offset:2400\n" \
"ds_read_b128 v[164:167], v129 offset:2304\n" \
"ds_read_b128 v[168:171], v129 offset:3456\n" \
"buffer_store_dwordx4 v[156:159], v130, s[52:55], s70 offen sc1\n" \
"s_add_u32 s70, s77, 196608\n" \
"s_waitcnt lgkmcnt(6)\n" \
"buffer_store_dwordx4 v[160:163], v130, s[52:55], s70 offen sc1\n" \
"s_add_u32 s70, s77, 135168\n" \
"s_waitcnt lgkmcnt(1)\n" \
"buffer_store_dwordx4 v[164:167], v130, s[52:55], s70 offen sc1\n" \
"s_add_u32 s70, s77, 200704\n" \
"s_waitcnt lgkmcnt(0)\n" \
"buffer_store_dwordx4 v[168:171], v130, s[52:55], s70 offen sc1\n" \
"s_endpgm\n" \
"Lpg13593_%=:\n" \
"s_cbranch_execz Lcb1_%=\n" \
"Lo64_%=:\n" \
"v_mfma_f32_16x16x32_bf16 v[64:67], a[32:35], a[0:3], v[64:67]\n" \
"ds_read_b128 a[24:27], v213 offset:4352\n" \
"s_add_u32 m0, s75, s95\n" \
"v_mfma_f32_16x16x32_bf16 v[64:67], a[36:39], a[4:7], v[64:67]\n" \
"s_waitcnt lgkmcnt(1)\n" \
"ds_read_b128 a[28:31], v213 offset:6400\n" \
"global_load_lds_dwordx4 v222, s[36:37] offset:0 sc1\n" \
"v_mfma_f32_16x16x32_bf16 v[68:71], a[40:43], a[0:3], v[68:71]\n" \
"s_sub_u32 s74, s74, 32768\n" \
"s_cselect_b32 s74, 131072, s74\n" \
"ds_read_b64_tr_b16 a[48:49], v214 offset:16896\n" \
"v_mfma_f32_16x16x32_bf16 v[68:71], a[44:47], a[4:7], v[68:71]\n" \
"ds_read_b64_tr_b16 a[50:51], v214 offset:18944\n" \
"v_mfma_f32_16x16x32_bf16 v[80:83], a[32:35], a[8:11], v[80:83]\n" \
"ds_read_b64_tr_b16 a[52:53], v214 offset:25088\n" \
"v_mfma_f32_16x16x32_bf16 v[80:83], a[36:39], a[12:15], v[80:83]\n" \
"ds_read_b64_tr_b16 a[54:55], v214 offset:27136\n" \
"global_load_lds_dwordx4 v222, s[36:37] offset:1024 sc1\n" \
"v_mfma_f32_16x16x32_bf16 v[84:87], a[40:43], a[8:11], v[84:87]\n" \
"ds_read_b64_tr_b16 a[56:57], v215 offset:16896\n" \
"v_mfma_f32_16x16x32_bf16 v[84:87], a[44:47], a[12:15], v[84:87]\n" \
"ds_read_b64_tr_b16 a[58:59], v215 offset:18944\n" \
"v_mfma_f32_16x16x32_bf16 v[96:99], a[32:35], a[16:19], v[96:99]\n" \
"ds_read_b64_tr_b16 a[60:61], v215 offset:25088\n" \
"v_mfma_f32_16x16x32_bf16 v[96:99], a[36:39], a[20:23], v[96:99]\n" \
"ds_read_b64_tr_b16 a[62:63], v215 offset:27136\n" \
"global_load_lds_dwordx4 v222, s[36:37] offset:2048 sc1\n" \
"v_mfma_f32_16x16x32_bf16 v[100:103], a[40:43], a[16:19], v[100:103]\n" \
"v_add_u32 v213, s74, v211\n" \
"v_add_u32 v214, s74, v212\n" \
"v_mfma_f32_16x16x32_bf16 v[100:103], a[44:47], a[20:23], v[100:103]\n" \
"s_waitcnt lgkmcnt(3)\n" \
"v_xor_b32 v215, 32, v214\n" \
"v_mfma_f32_16x16x32_bf16 v[112:115], a[32:35], a[24:27], v[112:115]\n" \
"v_mfma_f32_16x16x32_bf16 v[112:115], a[36:39], a[28:31], v[112:115]\n" \
"global_load_lds_dwordx4 v222, s[36:37] offset:3072 sc1\n" \
"v_mfma_f32_16x16x32_bf16 v[116:119], a[40:43], a[24:27], v[116:119]\n" \
"s_waitcnt vmcnt(20) lgkmcnt(0)\n" \
"s_add_u32 s36, s36, 0x1000\n" \
"v_mfma_f32_16x16x32_bf16 v[116:119], a[44:47], a[28:31], v[116:119]\n" \
"s_barrier\n" \
"v_mfma_f32_16x16x32_bf16 v[72:75], a[48:51], a[0:3], v[72:75]\n" \
"s_add_u32 m0, m0, 0x5000\n" \
"ds_read_b64_tr_b16 a[32:33], v214 offset:16384\n" \
"v_mfma_f32_16x16x32_bf16 v[72:75], a[52:55], a[4:7], v[72:75]\n" \
"ds_read_b64_tr_b16 a[34:35], v214 offset:18432\n" \
"global_load_lds_dwordx4 v216, s[38:39] offset:-4096\n" \
"v_mfma_f32_16x16x32_bf16 v[76:79], a[56:59], a[0:3], v[76:79]\n" \
"ds_read_b64_tr_b16 a[36:37], v214 offset:24576\n" \
"v_mfma_f32_16x16x32_bf16 v[76:79], a[60:63], a[4:7], v[76:79]\n" \
"ds_read_b128 a[0:3], v213 offset:0\n" \
"v_mfma_f32_16x16x32_bf16 v[88:91], a[48:51], a[8:11], v[88:91]\n" \
"ds_read_b128 a[4:7], v213 offset:2048\n" \
"v_mfma_f32_16x16x32_bf16 v[88:91], a[52:55], a[12:15], v[88:91]\n" \
"ds_read_b64_tr_b16 a[38:39], v214 offset:26624\n" \
"global_load_lds_dwordx4 v217, s[38:39] offset:-3072\n" \
"v_mfma_f32_16x16x32_bf16 v[92:95], a[56:59], a[8:11], v[92:95]\n" \
"ds_read_b64_tr_b16 a[40:41], v215 offset:16384\n" \
"v_mfma_f32_16x16x32_bf16 v[92:95], a[60:63], a[12:15], v[92:95]\n" \
"ds_read_b128 a[8:11], v213 offset:256\n" \
"v_mfma_f32_16x16x32_bf16 v[104:107], a[48:51], a[16:19], v[104:107]\n" \
"ds_read_b128 a[12:15], v213 offset:2304\n" \
"v_mfma_f32_16x16x32_bf16 v[104:107], a[52:55], a[20:23], v[104:107]\n" \
"ds_read_b64_tr_b16 a[42:43], v215 offset:18432\n" \
"global_load_lds_dwordx4 v218, s[38:39] offset:-2048\n" \
"v_mfma_f32_16x16x32_bf16 v[108:111], a[56:59], a[16:19], v[108:111]\n" \
"ds_read_b64_tr_b16 a[44:45], v215 offset:24576\n" \
"v_mfma_f32_16x16x32_bf16 v[108:111], a[60:63], a[20:23], v[108:111]\n" \
"ds_read_b128 a[16:19], v213 offset:4096\n" \
"global_load_lds_dwordx4 v219, s[38:39] offset:-1024\n" \
"v_mfma_f32_16x16x32_bf16 v[120:123], a[48:51], a[24:27], v[120:123]\n" \
"s_add_u32 s38, s38, 0x20000\n" \
"s_addc_u32 s39, s39, 0\n" \
"ds_read_b128 a[20:23], v213 offset:6144\n" \
"v_mfma_f32_16x16x32_bf16 v[120:123], a[52:55], a[28:31], v[120:123]\n" \
"s_sub_u32 s75, s75, 32768\n" \
"s_cselect_b32 s75, 131072, s75\n" \
"ds_read_b64_tr_b16 a[46:47], v215 offset:26624\n" \
"v_mfma_f32_16x16x32_bf16 v[124:127], a[56:59], a[24:27], v[124:127]\n" \
"s_sub_u32 s72, s72, 1\n" \
"s_cmp_lg_u32 s72, 0\n" \
"v_mfma_f32_16x16x32_bf16 v[124:127], a[60:63], a[28:31], v[124:127]\n" \
"s_waitcnt lgkmcnt(3)\n" \
"s_cbranch_scc1 Lo64_%=\n" \
"s_setpc_b64 s[68:69]\n" \
"Lhot_end_%=:\n" \
"Lmask0_%=:\n" \
"Lpg13682_%=:\n" \
"s_cbranch_execz Lcb0_%=\n" \
"s_waitcnt lgkmcnt(0)\n" \
"v_subrev_u32 v0, 0, v251\n" \
"v_subrev_u32 v1, 1, v251\n" \
"v_subrev_u32 v2, 2, v251\n" \
"v_subrev_u32 v3, 3, v251\n" \
"v_subrev_u32 v4, 16, v251\n" \
"v_subrev_u32 v5, 17, v251\n" \
"v_subrev_u32 v6, 18, v251\n" \
"v_subrev_u32 v7, 19, v251\n" \
"v_subrev_u32 v32, 32, v251\n" \
"v_subrev_u32 v33, 33, v251\n" \
"v_subrev_u32 v34, 34, v251\n" \
"v_subrev_u32 v35, 35, v251\n" \
"v_subrev_u32 v36, 48, v251\n" \
"v_subrev_u32 v37, 49, v251\n" \
"v_subrev_u32 v38, 50, v251\n" \
"v_subrev_u32 v39, 51, v251\n" \
"v_ashrrev_i32 v0, 31, v0\n" \
"v_ashrrev_i32 v1, 31, v1\n" \
"v_ashrrev_i32 v2, 31, v2\n" \
"v_ashrrev_i32 v3, 31, v3\n" \
"v_ashrrev_i32 v4, 31, v4\n" \
"v_ashrrev_i32 v5, 31, v5\n" \
"v_ashrrev_i32 v6, 31, v6\n" \
"v_ashrrev_i32 v7, 31, v7\n" \
"v_ashrrev_i32 v32, 31, v32\n" \
"v_ashrrev_i32 v33, 31, v33\n" \
"v_ashrrev_i32 v34, 31, v34\n" \
"v_ashrrev_i32 v35, 31, v35\n" \
"v_ashrrev_i32 v36, 31, v36\n" \
"v_ashrrev_i32 v37, 31, v37\n" \
"v_ashrrev_i32 v38, 31, v38\n" \
"v_ashrrev_i32 v39, 31, v39\n" \
"v_and_b32 v0, 0xff800000, v0\n" \
"v_and_b32 v1, 0xff800000, v1\n" \
"v_and_b32 v2, 0xff800000, v2\n" \
"v_and_b32 v3, 0xff800000, v3\n" \
"v_and_b32 v4, 0xff800000, v4\n" \
"v_and_b32 v5, 0xff800000, v5\n" \
"v_and_b32 v6, 0xff800000, v6\n" \
"v_and_b32 v7, 0xff800000, v7\n" \
"v_and_b32 v32, 0xff800000, v32\n" \
"v_and_b32 v33, 0xff800000, v33\n" \
"v_and_b32 v34, 0xff800000, v34\n" \
"v_and_b32 v35, 0xff800000, v35\n" \
"v_and_b32 v36, 0xff800000, v36\n" \
"v_and_b32 v37, 0xff800000, v37\n" \
"v_and_b32 v38, 0xff800000, v38\n" \
"v_and_b32 v39, 0xff800000, v39\n" \
"s_branch Lmask0r_%=\n" \
"Lpl1_%=:\n" \
"s_mov_b32 s31, 0\n" \
"Lpk1_%=:\n" \
"s_sleep 2\n" \
"buffer_load_dword v244, v245, s[56:59], s91 offen sc1\n" \
"s_waitcnt vmcnt(0)\n" \
"v_cmp_ne_u32 vcc, s60, v244\n" \
"s_nop 1\n" \
"s_and_b32 vcc_hi, vcc_lo, 255\n" \
"s_cbranch_scc0 Lpr1_%=\n" \
"s_add_u32 s31, s31, 1\n" \
"s_cmp_lt_u32 s31, 0x40000\n" \
"s_cbranch_scc1 Lpk1_%=\n" \
"s_branch Lpr1_%=\n" \
"Lrs0_%=:\n" \
"s_call_b64 s[68:69], Lrest_%=\n" \
"s_branch Lrr0_%=\n" \
"Lrest_%=:\n" \
"s_mov_b32 s31, 0\n" \
"s_waitcnt vmcnt(0)\n" \
"Lrk_%=:\n" \
"v_lshrrev_b32 v253, 2, v248\n" \
"s_sleep 2\n" \
"buffer_load_dword v253, v253, s[56:59], s79 offen sc1\n" \
"s_waitcnt vmcnt(0)\n" \
"v_cmp_ne_u32 s[52:53], s60, v253\n" \
"s_add_u32 s31, s31, 1\n" \
"s_cmp_lt_u32 s31, 0x40000\n" \
"s_cselect_b32 s54, 0xff, 0\n" \
"s_and_b32 s52, s52, s54\n" \
"s_cmp_lg_u32 s52, 0\n" \
"s_cbranch_scc1 Lrk_%=\n" \
"s_mov_b64 s[10:11], 0\n" \
"s_setpc_b64 s[68:69]\n" \
"Lrs1_%=:\n" \
"s_call_b64 s[68:69], Lrest_%=\n" \
"s_branch Lrr1_%=\n" \
"Lrs2_%=:\n" \
"s_call_b64 s[68:69], Lrest_%=\n" \
"s_branch Lrr2_%=\n" \
"Lrs3_%=:\n" \
"s_call_b64 s[68:69], Lrest_%=\n" \
"s_branch Lrr3_%=\n" \
"LF7_%=:\n" \
"v_mfma_f32_16x16x32_bf16 v[16:19], a[192:195], a[160:163], v[232:235]\n" \
"s_waitcnt vmcnt(12)\n" \
"v_exp_f32 v104, v104\n" \
"s_bfe_u32 s88, %1, 0x20001\n" \
"s_lshl_b32 s88, s88, 2\n" \
"ds_read_b128 v[192:195], v246 offset:16384\n" \
"v_mfma_f32_16x16x32_bf16 v[16:19], a[196:199], a[164:167], v[16:19]\n" \
"s_lshr_b32 s70, %1, 6\n" \
"ds_read_b128 v[196:199], v246 offset:18432\n" \
"v_cvt_pk_bf16_f32 v91, v94, v95\n" \
"v_add_f32 v241, v241, v104\n" \
"v_exp_f32 v105, v105\n" \
"v_mfma_f32_16x16x32_bf16 v[24:27], a[192:195], a[176:179], v[236:239]\n" \
"s_add_u32 s88, s88, s70\n" \
"ds_read_b128 v[200:203], v246 offset:16640\n" \
"v_mov_b32 v78, v240\n" \
"v_add_f32 v241, v241, v105\n" \
"v_bfe_u32 v69, v248, 8, 1\n" \
"v_bfe_u32 v70, v248, 6, 2\n" \
"s_lshr_b32 s71, s22, 7\n" \
"v_exp_f32 v106, v106\n" \
"v_mfma_f32_16x16x32_bf16 v[24:27], a[196:199], a[180:183], v[24:27]\n" \
"s_sub_u32 s71, s63, s71\n" \
"ds_read_b128 a[252:255], v246 offset:14592\n" \
"v_add_f32 v241, v241, v106\n" \
"v_cvt_pk_bf16_f32 v104, v104, v105\n" \
"v_exp_f32 v107, v107\n" \
"s_add_u32 s71, s71, 6\n" \
"v_mfma_f32_16x16x32_bf16 v[20:23], a[200:203], a[160:163], v[232:235]\n" \
"s_and_b32 s71, s71, 7\n" \
"ds_read_b128 v[204:207], v246 offset:18688\n" \
"v_add_f32 v241, v241, v107\n" \
"v_lshl_add_u32 v70, v69, 3, v70\n" \
"v_mul_u32_u24 v70, 6144, v70\n" \
"v_exp_f32 v108, v108\n" \
"v_permlane16_swap_b32 v78, v240\n" \
"v_add_f32 v240, v240, v78\n" \
"v_mov_b32 v78, v240\n" \
"v_mfma_f32_16x16x32_bf16 v[20:23], a[204:207], a[164:167], v[20:23]\n" \
"s_and_b32 s70, %1, 1\n" \
"ds_read_b128 v[208:211], v246 offset:24576\n" \
"v_add_f32 v241, v241, v108\n" \
"v_cvt_pk_bf16_f32 v105, v106, v107\n" \
"v_exp_f32 v109, v109\n" \
"s_lshl_b32 s70, s70, 3\n" \
"v_mfma_f32_16x16x32_bf16 v[28:31], a[200:203], a[176:179], v[236:239]\n" \
"s_add_u32 s70, s70, s71\n" \
"ds_read_b128 v[212:215], v246 offset:26624\n" \
"v_add_f32 v241, v241, v109\n" \
"v_lshlrev_b32 v69, 5, v69\n" \
"v_and_b32 v71, 0x30, v248\n" \
"s_lshl_b32 s87, s70, 12\n" \
"v_exp_f32 v110, v110\n" \
"v_permlane32_swap_b32 v78, v240\n" \
"v_add_f32 v240, v240, v78\n" \
"v_rcp_f32 v78, v240\n" \
"v_mul_f32 v240, 0x4d800000, v240\n" \
"v_add_u32 v79, 0x1c90080, v249\n" \
"v_cndmask_b32 v79, v249, v79, s[16:17]\n" \
"v_accvgpr_read_b32 v96, a0\n" \
"v_accvgpr_read_b32 v97, a1\n" \
"v_accvgpr_read_b32 v98, a2\n" \
"v_accvgpr_read_b32 v99, a3\n" \
"v_accvgpr_read_b32 v100, a4\n" \
"v_accvgpr_read_b32 v101, a5\n" \
"v_accvgpr_read_b32 v102, a6\n" \
"v_accvgpr_read_b32 v103, a7\n" \
"v_pk_mul_f32 v[96:97], v[96:97], v[78:79] op_sel_hi:[1,0]\n" \
"v_pk_mul_f32 v[98:99], v[98:99], v[78:79] op_sel_hi:[1,0]\n" \
"v_pk_mul_f32 v[100:101], v[100:101], v[78:79] op_sel_hi:[1,0]\n" \
"v_pk_mul_f32 v[102:103], v[102:103], v[78:79] op_sel_hi:[1,0]\n" \
"v_cvt_pk_bf16_f32 v224, v96, v97\n" \
"v_cvt_pk_bf16_f32 v225, v98, v99\n" \
"v_mfma_f32_16x16x32_bf16 v[28:31], a[204:207], a[180:183], v[28:31]\n" \
"s_lshl_b32 s70, s70, 7\n" \
"s_mul_i32 s84, s61, 6144\n" \
"ds_read_b128 v[216:219], v246 offset:24832\n" \
"v_add_f32 v241, v241, v110\n" \
"v_cvt_pk_bf16_f32 v106, v108, v109\n" \
"v_cvt_pk_bf16_f32 v226, v100, v101\n" \
"v_cvt_pk_bf16_f32 v227, v102, v103\n" \
"v_exp_f32 v111, v111\n" \
"v_pk_mul_f32 v[96:97], v[96:97], v[98:99]\n" \
"v_pk_fma_f32 v[96:97], v[100:101], v[102:103], v[96:97]\n" \
"v_fma_f32 v240, v96, v97, v240\n" \
"v_mfma_f32_16x16x32_bf16 v[48:51], a[208:211], a[160:163], v[232:235]\n" \
"s_lshl_b32 s71, s84, 4\n" \
"ds_read_b128 v[220:223], v246 offset:26880\n" \
"buffer_store_dwordx4 v[224:227], v79, s[56:59], s77 offen offset:0 sc1\n" \
"v_add_f32 v241, v241, v111\n" \
"v_exp_f32 v112, v112\n" \
"v_xor_b32 v69, v69, v71\n" \
"v_accvgpr_read_b32 v96, a8\n" \
"v_mfma_f32_16x16x32_bf16 v[48:51], a[212:215], a[164:167], v[48:51]\n" \
"ds_read_b128 a[192:195], v247 offset:16384\n" \
"v_lshrrev_b32 v71, 9, v248\n" \
"v_accvgpr_read_b32 v97, a9\n" \
"v_accvgpr_read_b32 v98, a10\n" \
"v_accvgpr_read_b32 v99, a11\n" \
"v_accvgpr_read_b32 v100, a12\n" \
"v_accvgpr_read_b32 v101, a13\n" \
"v_accvgpr_read_b32 v102, a14\n" \
"v_accvgpr_read_b32 v103, a15\n" \
"v_add_f32 v242, v242, v112\n" \
"v_cvt_pk_bf16_f32 v107, v110, v111\n" \
"v_exp_f32 v113, v113\n" \
"v_pk_mul_f32 v[96:97], v[96:97], v[78:79] op_sel_hi:[1,0]\n" \
"v_pk_mul_f32 v[98:99], v[98:99], v[78:79] op_sel_hi:[1,0]\n" \
"v_pk_mul_f32 v[100:101], v[100:101], v[78:79] op_sel_hi:[1,0]\n" \
"v_pk_mul_f32 v[102:103], v[102:103], v[78:79] op_sel_hi:[1,0]\n" \
"v_cvt_pk_bf16_f32 v228, v96, v97\n" \
"v_cvt_pk_bf16_f32 v229, v98, v99\n" \
"v_cvt_pk_bf16_f32 v230, v100, v101\n" \
"v_cvt_pk_bf16_f32 v231, v102, v103\n" \
"v_pk_mul_f32 v[96:97], v[96:97], v[98:99]\n" \
"v_pk_fma_f32 v[96:97], v[100:101], v[102:103], v[96:97]\n" \
"v_fma_f32 v240, v96, v97, v240\n" \
"v_mfma_f32_16x16x32_bf16 v[56:59], a[208:211], a[176:179], v[236:239]\n" \
"s_add_u32 s70, s70, s71\n" \
"ds_read_b128 a[196:199], v247 offset:24576\n" \
"buffer_store_dwordx4 v[228:231], v79, s[56:59], s77 offen offset:2048 sc1\n" \
"v_add_f32 v242, v242, v113\n" \
"v_exp_f32 v114, v114\n" \
"s_add_u32 s70, s70, 4096\n" \
"v_mfma_f32_16x16x32_bf16 v[56:59], a[212:215], a[180:183], v[56:59]\n" \
"s_waitcnt lgkmcnt(4)\n" \
"ds_read_b128 a[200:203], v247 offset:17408\n" \
"v_lshl_add_u32 v70, v71, 6, v70\n" \
"v_add_f32 v242, v242, v114\n" \
"v_cvt_pk_bf16_f32 v112, v112, v113\n" \
"v_exp_f32 v115, v115\n" \
"v_mfma_f32_16x16x32_bf16 v[8:11], v[192:195], a[176:179], v[236:239]\n" \
"s_add_u32 s77, s77, 0x1000\n" \
"ds_read_b128 a[204:207], v247 offset:25600\n" \
"v_add_u32 v253, v69, v70\n" \
"v_add_f32 v242, v242, v115\n" \
"v_accvgpr_read_b32 v96, a16\n" \
"v_accvgpr_read_b32 v97, a17\n" \
"v_accvgpr_read_b32 v98, a18\n" \
"v_accvgpr_read_b32 v99, a19\n" \
"v_accvgpr_read_b32 v100, a20\n" \
"v_accvgpr_read_b32 v101, a21\n" \
"v_accvgpr_read_b32 v102, a22\n" \
"v_accvgpr_read_b32 v103, a23\n" \
"v_exp_f32 v116, v116\n" \
"v_pk_mul_f32 v[96:97], v[96:97], v[78:79] op_sel_hi:[1,0]\n" \
"v_pk_mul_f32 v[98:99], v[98:99], v[78:79] op_sel_hi:[1,0]\n" \
"v_pk_mul_f32 v[100:101], v[100:101], v[78:79] op_sel_hi:[1,0]\n" \
"v_pk_mul_f32 v[102:103], v[102:103], v[78:79] op_sel_hi:[1,0]\n" \
"v_cvt_pk_bf16_f32 v224, v96, v97\n" \
"v_cvt_pk_bf16_f32 v225, v98, v99\n" \
"v_cvt_pk_bf16_f32 v226, v100, v101\n" \
"v_cvt_pk_bf16_f32 v227, v102, v103\n" \
"v_pk_mul_f32 v[96:97], v[96:97], v[98:99]\n" \
"v_pk_fma_f32 v[96:97], v[100:101], v[102:103], v[96:97]\n" \
"v_fma_f32 v240, v96, v97, v240\n" \
"v_mfma_f32_16x16x32_bf16 v[8:11], v[196:199], a[180:183], v[8:11]\n" \
"s_add_u32 s48, s22, s70\n" \
"s_addc_u32 s49, s23, 0\n" \
"ds_read_b128 a[208:211], v247 offset:18432\n" \
"buffer_store_dwordx4 v[224:227], v79, s[56:59], s77 offen offset:0 sc1\n" \
"v_add_f32 v242, v242, v116\n" \
"v_cvt_pk_bf16_f32 v113, v114, v115\n" \
"v_exp_f32 v117, v117\n" \
"v_accvgpr_read_b32 v96, a24\n" \
"v_mfma_f32_16x16x32_bf16 v[52:55], a[216:219], a[160:163], v[232:235]\n" \
"s_add_u32 s50, s48, 23552\n" \
"s_addc_u32 s51, s49, 0\n" \
"ds_read_b128 a[212:215], v247 offset:26624\n" \
"v_accvgpr_read_b32 v97, a25\n" \
"v_accvgpr_read_b32 v98, a26\n" \
"v_accvgpr_read_b32 v99, a27\n" \
"v_accvgpr_read_b32 v100, a28\n" \
"v_accvgpr_read_b32 v101, a29\n" \
"v_accvgpr_read_b32 v102, a30\n" \
"v_accvgpr_read_b32 v103, a31\n" \
"v_add_f32 v242, v242, v117\n" \
"v_exp_f32 v118, v118\n" \
"v_pk_mul_f32 v[96:97], v[96:97], v[78:79] op_sel_hi:[1,0]\n" \
"v_pk_mul_f32 v[98:99], v[98:99], v[78:79] op_sel_hi:[1,0]\n" \
"v_pk_mul_f32 v[100:101], v[100:101], v[78:79] op_sel_hi:[1,0]\n" \
"v_pk_mul_f32 v[102:103], v[102:103], v[78:79] op_sel_hi:[1,0]\n" \
"v_cvt_pk_bf16_f32 v228, v96, v97\n" \
"v_cvt_pk_bf16_f32 v229, v98, v99\n" \
"v_cvt_pk_bf16_f32 v230, v100, v101\n" \
"v_cvt_pk_bf16_f32 v231, v102, v103\n" \
"v_pk_mul_f32 v[96:97], v[96:97], v[98:99]\n" \
"v_pk_fma_f32 v[96:97], v[100:101], v[102:103], v[96:97]\n" \
"v_fma_f32 v240, v96, v97, v240\n" \
"v_mfma_f32_16x16x32_bf16 v[52:55], a[220:223], a[164:167], v[52:55]\n" \
"s_lshr_b32 s89, s22, 7\n" \
"ds_read_b128 v[192:195], v246 offset:20480\n" \
"v_add_f32 v242, v242, v118\n" \
"v_cvt_pk_bf16_f32 v114, v116, v117\n" \
"s_sub_u32 s89, 3, s89\n" \
"v_mfma_f32_16x16x32_bf16 v[60:63], a[216:219], a[176:179], v[236:239]\n" \
"ds_read_b128 v[196:199], v246 offset:22528\n" \
"v_bfe_u32 v84, v248, 7, 2\n" \
"v_mfma_f32_16x16x32_bf16 v[60:63], a[220:223], a[180:183], v[60:63]\n" \
"s_and_b32 s89, s89, 7\n" \
"ds_read_b128 a[216:219], v247 offset:19456\n" \
"s_and_b32 s90, %1, 1\n" \
"v_mfma_f32_16x16x32_bf16 v[12:15], v[200:203], a[176:179], v[236:239]\n" \
"ds_read_b128 a[220:223], v247 offset:27648\n" \
"v_bfe_u32 v85, v248, 5, 2\n" \
"v_mfma_f32_16x16x32_bf16 v[12:15], v[204:207], a[180:183], v[12:15]\n" \
"s_lshl_b32 s90, s90, 3\n" \
"ds_read_b128 v[200:203], v246 offset:20736\n" \
"s_add_u32 s89, s89, s90\n" \
"v_mfma_f32_16x16x32_bf16 v[16:19], a[224:227], a[168:171], v[16:19]\n" \
"ds_read_b128 v[204:207], v246 offset:22784\n" \
"v_min_u32 v85, 2, v85\n" \
"v_mfma_f32_16x16x32_bf16 v[16:19], a[228:231], a[172:175], v[16:19]\n" \
"s_lshl_b32 s89, s89, 7\n" \
"s_lshr_b32 s90, %1, 3\n" \
"v_mad_u32_u24 v84, v84, 3, v85\n" \
"v_mfma_f32_16x16x32_bf16 v[24:27], a[224:227], a[184:187], v[24:27]\n" \
"s_lshl_b32 s90, s90, 2\n" \
"s_add_u32 s90, s90, s61\n" \
"v_and_b32 v85, 16, v248\n" \
"v_mfma_f32_16x16x32_bf16 v[24:27], a[228:231], a[188:191], v[24:27]\n" \
"s_mul_i32 s90, s90, 24576\n" \
"ds_read_b128 a[224:227], v247 offset:20480\n" \
"s_add_u32 s89, s89, s90\n" \
"v_mfma_f32_16x16x32_bf16 v[40:43], v[208:211], a[176:179], v[236:239]\n" \
"ds_read_b128 a[228:231], v247 offset:28672\n" \
"v_lshl_add_u32 v85, v85, 2, s89\n" \
"v_mfma_f32_16x16x32_bf16 v[40:43], v[212:215], a[180:183], v[40:43]\n" \
"ds_read_b128 v[208:211], v246 offset:28672\n" \
"v_lshl_add_u32 v84, v84, 11, v85\n" \
"v_mfma_f32_16x16x32_bf16 v[20:23], a[232:235], a[168:171], v[20:23]\n" \
"s_add_u32 m0, s84, 143360\n" \
"ds_read_b128 v[212:215], v246 offset:30720\n" \
"v_exp_f32 v119, v119\n" \
"v_mfma_f32_16x16x32_bf16 v[20:23], a[236:239], a[172:175], v[20:23]\n" \
"global_load_lds_dwordx4 v253, s[48:49] offset:-4096\n" \
"v_add_f32 v242, v242, v119\n" \
"v_mfma_f32_16x16x32_bf16 v[28:31], a[232:235], a[184:187], v[28:31]\n" \
"v_exp_f32 v120, v120\n" \
"v_mfma_f32_16x16x32_bf16 v[28:31], a[236:239], a[188:191], v[28:31]\n" \
"s_waitcnt lgkmcnt(2)\n" \
"ds_read_b128 a[232:235], v247 offset:21504\n" \
"v_add_f32 v243, v243, v120\n" \
"v_cvt_pk_bf16_f32 v115, v118, v119\n" \
"v_mfma_f32_16x16x32_bf16 v[44:47], v[216:219], a[176:179], v[236:239]\n" \
"ds_read_b128 a[236:239], v247 offset:29696\n" \
"global_load_lds_dwordx4 v253, s[48:49] offset:-2048\n" \
"v_mfma_f32_16x16x32_bf16 v[44:47], v[220:223], a[180:183], v[44:47]\n" \
"ds_read_b128 v[216:219], v246 offset:28928\n" \
"v_exp_f32 v121, v121\n" \
"v_mfma_f32_16x16x32_bf16 v[48:51], a[240:243], a[168:171], v[48:51]\n" \
"ds_read_b128 v[220:223], v246 offset:30976\n" \
"v_add_f32 v243, v243, v121\n" \
"v_mfma_f32_16x16x32_bf16 v[48:51], a[244:247], a[172:175], v[48:51]\n" \
"global_load_lds_dwordx4 v253, s[48:49] offset:0\n" \
"v_exp_f32 v122, v122\n" \
"v_mfma_f32_16x16x32_bf16 v[56:59], a[240:243], a[184:187], v[56:59]\n" \
"s_add_u32 s48, s48, 0x120000\n" \
"s_addc_u32 s49, s49, 0\n" \
"v_add_f32 v243, v243, v122\n" \
"v_mfma_f32_16x16x32_bf16 v[56:59], a[244:247], a[188:191], v[56:59]\n" \
"ds_read_b128 a[240:243], v247 offset:22528\n" \
"v_cvt_pk_bf16_f32 v120, v120, v121\n" \
"v_mfma_f32_16x16x32_bf16 v[8:11], v[192:195], a[184:187], v[8:11]\n" \
"ds_read_b128 a[244:247], v247 offset:30720\n" \
"global_load_lds_dwordx4 v253, s[50:51] offset:-3072\n" \
"v_mfma_f32_16x16x32_bf16 v[8:11], v[196:199], a[188:191], v[8:11]\n" \
"v_exp_f32 v123, v123\n" \
"v_mfma_f32_16x16x32_bf16 v[52:55], a[248:251], a[168:171], v[52:55]\n" \
"v_add_f32 v243, v243, v123\n" \
"v_exp_f32 v124, v124\n" \
"v_mfma_f32_16x16x32_bf16 v[52:55], a[252:255], a[172:175], v[52:55]\n" \
"global_load_lds_dwordx4 v253, s[50:51] offset:-1024\n" \
"v_add_f32 v243, v243, v124\n" \
"v_cvt_pk_bf16_f32 v121, v122, v123\n" \
"v_mfma_f32_16x16x32_bf16 v[60:63], a[248:251], a[184:187], v[60:63]\n" \
"v_exp_f32 v125, v125\n" \
"v_mfma_f32_16x16x32_bf16 v[60:63], a[252:255], a[188:191], v[60:63]\n" \
"ds_read_b128 a[248:251], v247 offset:23552\n" \
"v_add_f32 v243, v243, v125\n" \
"v_mfma_f32_16x16x32_bf16 v[12:15], v[200:203], a[184:187], v[12:15]\n" \
"ds_read_b128 a[252:255], v247 offset:31744\n" \
"global_load_lds_dwordx4 v253, s[50:51] offset:1024\n" \
"v_mfma_f32_16x16x32_bf16 v[12:15], v[204:207], a[188:191], v[12:15]\n" \
"s_waitcnt lgkmcnt(2)\n" \
"s_add_u32 s50, s50, 0x120000\n" \
"s_addc_u32 s51, s51, 0\n" \
"v_mfma_f32_16x16x32_bf16 v[40:43], v[208:211], a[184:187], v[40:43]\n" \
"v_exp_f32 v126, v126\n" \
"v_mfma_f32_16x16x32_bf16 v[40:43], v[212:215], a[188:191], v[40:43]\n" \
"global_load_dword v85, v84, s[22:23]\n" \
"v_mfma_f32_16x16x32_bf16 v[44:47], v[216:219], a[184:187], v[44:47]\n" \
"s_waitcnt vmcnt(10) lgkmcnt(0)\n" \
"v_add_u32 v247, 0x10000, v248\n" \
"v_add_f32 v243, v243, v126\n" \
"v_mfma_f32_16x16x32_bf16 v[44:47], v[220:223], a[188:191], v[44:47]\n" \
"s_barrier\n" \
"v_cvt_pk_bf16_f32 v122, v124, v125\n" \
"v_mfma_f32_16x16x32_bf16 a[32:35], a[192:195], v[72:75], a[32:35]\n" \
"v_exp_f32 v127, v127\n" \
"v_mfma_f32_16x16x32_bf16 a[32:35], a[196:199], v[104:107], a[32:35]\n" \
"v_add_f32 v243, v243, v127\n" \
"v_cvt_pk_bf16_f32 v123, v126, v127\n" \
"v_mfma_f32_16x16x32_bf16 v[128:131], a[192:195], v[80:83], v[128:131]\n" \
"ds_read_b128 v[192:195], v247 offset:16384\n" \
"v_lshrrev_b32 v71, 7, v248\n" \
"v_cmp_gt_i32 vcc, 0, v251\n" \
"v_cmp_gt_i32 s[64:65], 1, v251\n" \
"v_cmp_gt_i32 s[66:67], 2, v251\n" \
"v_cndmask_b32 v8, v8, v254, vcc\n" \
"v_cndmask_b32 v16, v16, v254, vcc\n" \
"v_cmp_gt_i32 vcc, 3, v251\n" \
"v_mfma_f32_16x16x32_bf16 v[128:131], a[196:199], v[112:115], v[128:131]\n" \
"v_cndmask_b32 v9, v9, v254, s[64:65]\n" \
"v_cndmask_b32 v17, v17, v254, s[64:65]\n" \
"v_cmp_gt_i32 s[64:65], 16, v251\n" \
"v_cndmask_b32 v10, v10, v254, s[66:67]\n" \
"v_cndmask_b32 v18, v18, v254, s[66:67]\n" \
"v_cmp_gt_i32 s[66:67], 17, v251\n" \
"v_and_b32 v76, 0x70, v248\n" \
"s_lshl_b32 s70, s88, 18\n" \
"v_mfma_f32_16x16x32_bf16 v[160:163], a[192:195], v[88:91], v[160:163]\n" \
"s_lshl_b32 s71, s61, 16\n" \
"s_add_u32 s71, s71, s70\n" \
"v_cndmask_b32 v11, v11, v254, vcc\n" \
"v_cndmask_b32 v19, v19, v254, vcc\n" \
"v_cmp_gt_i32 vcc, 18, v251\n" \
"v_cndmask_b32 v12, v12, v254, s[64:65]\n" \
"v_cndmask_b32 v20, v20, v254, s[64:65]\n" \
"v_cmp_gt_i32 s[64:65], 19, v251\n" \
"v_lshlrev_b32 v77, 4, v71\n" \
"v_mfma_f32_16x16x32_bf16 v[160:163], a[196:199], v[120:123], v[160:163]\n" \
"ds_read_b128 a[192:195], v247 offset:0\n" \
"ds_read_b128 v[196:199], v247 offset:24576\n" \
"v_cndmask_b32 v13, v13, v254, s[66:67]\n" \
"v_cndmask_b32 v21, v21, v254, s[66:67]\n" \
"v_cmp_gt_i32 s[66:67], 32, v251\n" \
"v_cndmask_b32 v14, v14, v254, vcc\n" \
"v_cndmask_b32 v22, v22, v254, vcc\n" \
"v_cmp_gt_i32 vcc, 33, v251\n" \
"v_mfma_f32_16x16x32_bf16 a[36:39], a[200:203], v[72:75], a[36:39]\n" \
"v_xor_b32 v77, v77, v76\n" \
"v_cndmask_b32 v15, v15, v254, s[64:65]\n" \
"v_cndmask_b32 v23, v23, v254, s[64:65]\n" \
"v_cmp_gt_i32 s[64:65], 34, v251\n" \
"v_cndmask_b32 v40, v40, v254, s[66:67]\n" \
"v_cndmask_b32 v48, v48, v254, s[66:67]\n" \
"v_cmp_gt_i32 s[66:67], 35, v251\n" \
"v_mfma_f32_16x16x32_bf16 a[36:39], a[204:207], v[104:107], a[36:39]\n" \
"s_add_u32 s42, s20, s71\n" \
"s_addc_u32 s43, s21, 0\n" \
"ds_read_b128 a[196:199], v247 offset:8192\n" \
"v_cndmask_b32 v41, v41, v254, vcc\n" \
"v_cndmask_b32 v49, v49, v254, vcc\n" \
"v_cmp_gt_i32 vcc, 48, v251\n" \
"v_cndmask_b32 v42, v42, v254, s[64:65]\n" \
"v_cndmask_b32 v50, v50, v254, s[64:65]\n" \
"v_cmp_gt_i32 s[64:65], 49, v251\n" \
"v_mfma_f32_16x16x32_bf16 v[132:135], a[200:203], v[80:83], v[132:135]\n" \
"ds_read_b128 v[200:203], v247 offset:17408\n" \
"v_lshl_add_u32 v252, v71, 12, v77\n" \
"v_cndmask_b32 v43, v43, v254, s[66:67]\n" \
"v_cndmask_b32 v51, v51, v254, s[66:67]\n" \
"v_cmp_gt_i32 s[66:67], 50, v251\n" \
"v_cndmask_b32 v44, v44, v254, vcc\n" \
"v_cndmask_b32 v52, v52, v254, vcc\n" \
"v_cmp_gt_i32 vcc, 51, v251\n" \
"v_mfma_f32_16x16x32_bf16 v[132:135], a[204:207], v[112:115], v[132:135]\n" \
"s_add_u32 s44, s42, 31744\n" \
"s_addc_u32 s45, s43, 0\n" \
"s_max_u32 s70, s70, 32768\n" \
"v_cndmask_b32 v45, v45, v254, s[64:65]\n" \
"v_cndmask_b32 v53, v53, v254, s[64:65]\n" \
"v_cndmask_b32 v46, v46, v254, s[66:67]\n" \
"v_cndmask_b32 v54, v54, v254, s[66:67]\n" \
"v_cndmask_b32 v47, v47, v254, vcc\n" \
"v_cndmask_b32 v55, v55, v254, vcc\n" \
"v_mfma_f32_16x16x32_bf16 v[164:167], a[200:203], v[88:91], v[164:167]\n" \
"s_sub_u32 s70, s70, 32768\n" \
"v_exp_f32 v8, v8\n" \
"s_and_b32 s71, s61, 1\n" \
"s_lshl_b32 s71, s71, 2\n" \
"v_mfma_f32_16x16x32_bf16 v[164:167], a[204:207], v[120:123], v[164:167]\n" \
"ds_read_b128 a[200:203], v247 offset:1024\n" \
"buffer_store_dwordx4 v[228:231], v79, s[56:59], s77 offen offset:2048 sc1\n" \
"ds_read_b128 v[204:207], v247 offset:25600\n" \
"v_add_f32 v243, v243, v8\n" \
"v_exp_f32 v9, v9\n" \
"v_mfma_f32_16x16x32_bf16 a[40:43], a[208:211], v[72:75], a[40:43]\n" \
"s_add_u32 s77, s77, 0x81000\n" \
"v_add_f32 v243, v243, v9\n" \
"v_exp_f32 v10, v10\n" \
"v_and_or_b32 v71, v71, 3, s71\n" \
"s_lshr_b32 s71, s61, 1\n" \
"v_mfma_f32_16x16x32_bf16 a[40:43], a[212:215], v[104:107], a[40:43]\n" \
"s_lshl_b32 s71, s71, 11\n" \
"ds_read_b128 a[204:207], v247 offset:9216\n" \
"v_add_f32 v243, v243, v10\n" \
"v_cvt_pk_bf16_f32 v8, v8, v9\n" \
"v_exp_f32 v11, v11\n" \
"v_lshlrev_b32 v77, 4, v71\n" \
"v_mfma_f32_16x16x32_bf16 v[136:139], a[208:211], v[80:83], v[136:139]\n" \
"s_add_u32 s70, s70, s71\n" \
"ds_read_b128 v[208:211], v247 offset:18432\n" \
"v_add_f32 v243, v243, v11\n" \
"v_exp_f32 v12, v12\n" \
"v_xor_b32 v77, v77, v76\n" \
"v_mfma_f32_16x16x32_bf16 v[136:139], a[212:215], v[112:115], v[136:139]\n" \
"s_add_u32 s46, s20, s70\n" \
"s_addc_u32 s47, s21, 0\n" \
"v_add_f32 v243, v243, v12\n" \
"v_cvt_pk_bf16_f32 v9, v10, v11\n" \
"v_exp_f32 v13, v13\n" \
"v_lshl_add_u32 v246, v71, 12, v77\n" \
"v_mfma_f32_16x16x32_bf16 v[168:171], a[208:211], v[88:91], v[168:171]\n" \
"s_lshl_b32 s85, s61, 10\n" \
"v_add_f32 v243, v243, v13\n" \
"v_exp_f32 v14, v14\n" \
"s_mov_b64 s[12:13], s[20:21]\n" \
"s_and_b32 s13, s13, 0xffff\n" \
"v_mfma_f32_16x16x32_bf16 v[168:171], a[212:215], v[120:123], v[168:171]\n" \
"s_mov_b32 s14, 4194304\n" \
"ds_read_b128 a[208:211], v247 offset:2048\n" \
"s_mov_b32 s15, 0x20000\n" \
"ds_read_b128 v[212:215], v247 offset:26624\n" \
"v_add_f32 v243, v243, v14\n" \
"v_cvt_pk_bf16_f32 v10, v12, v13\n" \
"v_exp_f32 v15, v15\n" \
"v_mfma_f32_16x16x32_bf16 a[44:47], a[216:219], v[72:75], a[44:47]\n" \
"s_mov_b32 s46, s70\n" \
"v_add_f32 v243, v243, v15\n" \
"v_exp_f32 v40, v40\n" \
"v_lshrrev_b32 v71, 9, v248\n" \
"v_lshl_add_u32 v246, v71, 30, v246\n" \
"v_mfma_f32_16x16x32_bf16 a[44:47], a[220:223], v[104:107], a[44:47]\n" \
"s_add_u32 m0, s95, 118784\n" \
"ds_read_b128 a[212:215], v247 offset:10240\n" \
"v_add_f32 v243, v243, v40\n" \
"v_cvt_pk_bf16_f32 v11, v14, v15\n" \
"v_exp_f32 v41, v41\n" \
"v_mfma_f32_16x16x32_bf16 v[140:143], a[216:219], v[80:83], v[140:143]\n" \
"ds_read_b128 v[216:219], v247 offset:19456\n" \
"global_load_lds_dwordx4 v252, s[42:43] offset:0\n" \
"v_add_f32 v243, v243, v41\n" \
"v_exp_f32 v42, v42\n" \
"v_mfma_f32_16x16x32_bf16 v[140:143], a[220:223], v[112:115], v[140:143]\n" \
"v_add_f32 v243, v243, v42\n" \
"v_cvt_pk_bf16_f32 v40, v40, v41\n" \
"v_exp_f32 v43, v43\n" \
"v_mfma_f32_16x16x32_bf16 v[172:175], a[216:219], v[88:91], v[172:175]\n" \
"v_add_f32 v243, v243, v43\n" \
"v_exp_f32 v44, v44\n" \
"v_mfma_f32_16x16x32_bf16 v[172:175], a[220:223], v[120:123], v[172:175]\n" \
"ds_read_b128 a[216:219], v247 offset:3072\n" \
"global_load_lds_dwordx4 v252, s[42:43] offset:2048\n" \
"ds_read_b128 v[220:223], v247 offset:27648\n" \
"v_add_f32 v243, v243, v44\n" \
"v_cvt_pk_bf16_f32 v41, v42, v43\n" \
"v_exp_f32 v45, v45\n" \
"v_mfma_f32_16x16x32_bf16 a[48:51], a[224:227], v[72:75], a[48:51]\n" \
"s_add_u32 s42, s42, 0x180\n" \
"s_addc_u32 s43, s43, 0\n" \
"v_add_f32 v243, v243, v45\n" \
"v_exp_f32 v46, v46\n" \
"v_mfma_f32_16x16x32_bf16 a[48:51], a[228:231], v[104:107], a[48:51]\n" \
"ds_read_b128 a[220:223], v247 offset:11264\n" \
"v_add_f32 v243, v243, v46\n" \
"v_cvt_pk_bf16_f32 v42, v44, v45\n" \
"v_exp_f32 v47, v47\n" \
"v_mfma_f32_16x16x32_bf16 v[144:147], a[224:227], v[80:83], v[144:147]\n" \
"ds_read_b128 v[224:227], v247 offset:20480\n" \
"global_load_lds_dwordx4 v252, s[44:45] offset:1024\n" \
"v_add_f32 v243, v243, v47\n" \
"v_cvt_pk_bf16_f32 v43, v46, v47\n" \
"v_exp_f32 v24, v24\n" \
"v_mfma_f32_16x16x32_bf16 v[144:147], a[228:231], v[112:115], v[144:147]\n" \
"v_add_f32 v243, v243, v24\n" \
"v_exp_f32 v25, v25\n" \
"v_mfma_f32_16x16x32_bf16 v[176:179], a[224:227], v[88:91], v[176:179]\n" \
"v_add_f32 v243, v243, v25\n" \
"v_exp_f32 v26, v26\n" \
"v_mfma_f32_16x16x32_bf16 v[176:179], a[228:231], v[120:123], v[176:179]\n" \
"ds_read_b128 a[224:227], v247 offset:4096\n" \
"global_load_lds_dwordx4 v252, s[44:45] offset:3072\n" \
"ds_read_b128 v[228:231], v247 offset:28672\n" \
"v_add_f32 v243, v243, v26\n" \
"v_cvt_pk_bf16_f32 v24, v24, v25\n" \
"v_exp_f32 v27, v27\n" \
"v_mfma_f32_16x16x32_bf16 a[52:55], a[232:235], v[72:75], a[52:55]\n" \
"s_add_u32 s44, s44, 0x180\n" \
"s_addc_u32 s45, s45, 0\n" \
"v_add_f32 v243, v243, v27\n" \
"v_exp_f32 v28, v28\n" \
"v_mfma_f32_16x16x32_bf16 a[52:55], a[236:239], v[104:107], a[52:55]\n" \
"s_add_u32 m0, s85, 135168\n" \
"ds_read_b128 a[228:231], v247 offset:12288\n" \
"v_add_f32 v243, v243, v28\n" \
"v_cvt_pk_bf16_f32 v25, v26, v27\n" \
"v_exp_f32 v29, v29\n" \
"v_mfma_f32_16x16x32_bf16 v[148:151], a[232:235], v[80:83], v[148:151]\n" \
"ds_read_b128 v[232:235], v247 offset:21504\n" \
"v_add_f32 v243, v243, v29\n" \
"v_exp_f32 v30, v30\n" \
"v_mfma_f32_16x16x32_bf16 v[148:151], a[236:239], v[112:115], v[148:151]\n" \
"buffer_load_dwordx4 v246, s[12:15], s46 offen lds\n" \
"v_add_f32 v243, v243, v30\n" \
"v_cvt_pk_bf16_f32 v26, v28, v29\n" \
"v_exp_f32 v31, v31\n" \
"v_mfma_f32_16x16x32_bf16 v[180:183], a[232:235], v[88:91], v[180:183]\n" \
"s_add_u32 s46, s46, 0x180\n" \
"v_add_f32 v243, v243, v31\n" \
"v_exp_f32 v56, v56\n" \
"v_mfma_f32_16x16x32_bf16 v[180:183], a[236:239], v[120:123], v[180:183]\n" \
"ds_read_b128 a[232:235], v247 offset:5120\n" \
"ds_read_b128 v[236:239], v247 offset:29696\n" \
"v_add_f32 v243, v243, v56\n" \
"v_cvt_pk_bf16_f32 v27, v30, v31\n" \
"v_exp_f32 v57, v57\n" \
"v_mfma_f32_16x16x32_bf16 a[56:59], a[240:243], v[72:75], a[56:59]\n" \
"v_add_f32 v243, v243, v57\n" \
"v_exp_f32 v58, v58\n" \
"v_mfma_f32_16x16x32_bf16 a[56:59], a[244:247], v[104:107], a[56:59]\n" \
"ds_read_b128 a[236:239], v247 offset:13312\n" \
"v_add_f32 v243, v243, v58\n" \
"v_cvt_pk_bf16_f32 v56, v56, v57\n" \
"v_exp_f32 v59, v59\n" \
"v_mfma_f32_16x16x32_bf16 v[152:155], a[240:243], v[80:83], v[152:155]\n" \
"ds_read_b128 v[0:3], v247 offset:22528\n" \
"v_add_f32 v243, v243, v59\n" \
"v_exp_f32 v60, v60\n" \
"v_mfma_f32_16x16x32_bf16 v[152:155], a[244:247], v[112:115], v[152:155]\n" \
"v_add_f32 v243, v243, v60\n" \
"v_cvt_pk_bf16_f32 v57, v58, v59\n" \
"v_exp_f32 v61, v61\n" \
"v_mfma_f32_16x16x32_bf16 v[184:187], a[240:243], v[88:91], v[184:187]\n" \
"v_add_f32 v243, v243, v61\n" \
"v_exp_f32 v62, v62\n" \
"v_mfma_f32_16x16x32_bf16 v[184:187], a[244:247], v[120:123], v[184:187]\n" \
"ds_read_b128 a[240:243], v247 offset:6144\n" \
"ds_read_b128 v[4:7], v247 offset:30720\n" \
"v_add_f32 v243, v243, v62\n" \
"v_cvt_pk_bf16_f32 v58, v60, v61\n" \
"v_exp_f32 v63, v63\n" \
"v_mfma_f32_16x16x32_bf16 a[60:63], a[248:251], v[72:75], a[60:63]\n" \
"v_add_f32 v243, v243, v63\n" \
"v_cvt_pk_bf16_f32 v59, v62, v63\n" \
"v_exp_f32 v16, v16\n" \
"v_mfma_f32_16x16x32_bf16 a[60:63], a[252:255], v[104:107], a[60:63]\n" \
"ds_read_b128 a[244:247], v247 offset:14336\n" \
"v_add_f32 v242, v242, v16\n" \
"v_exp_f32 v17, v17\n" \
"v_mfma_f32_16x16x32_bf16 v[156:159], a[248:251], v[80:83], v[156:159]\n" \
"ds_read_b128 v[32:35], v247 offset:23552\n" \
"v_add_f32 v242, v242, v17\n" \
"v_exp_f32 v18, v18\n" \
"v_mfma_f32_16x16x32_bf16 v[156:159], a[252:255], v[112:115], v[156:159]\n" \
"v_add_f32 v242, v242, v18\n" \
"v_cvt_pk_bf16_f32 v16, v16, v17\n" \
"v_exp_f32 v19, v19\n" \
"v_mfma_f32_16x16x32_bf16 v[188:191], a[248:251], v[88:91], v[188:191]\n" \
"v_add_f32 v242, v242, v19\n" \
"v_exp_f32 v20, v20\n" \
"v_mfma_f32_16x16x32_bf16 v[188:191], a[252:255], v[120:123], v[188:191]\n" \
"s_waitcnt lgkmcnt(2)\n" \
"ds_read_b128 a[248:251], v247 offset:7168\n" \
"ds_read_b128 v[36:39], v247 offset:31744\n" \
"v_add_f32 v242, v242, v20\n" \
"v_cvt_pk_bf16_f32 v17, v18, v19\n" \
"v_exp_f32 v21, v21\n" \
"v_mfma_f32_16x16x32_bf16 v[160:163], v[192:195], v[8:11], v[160:163]\n" \
"v_add_f32 v242, v242, v21\n" \
"v_exp_f32 v22, v22\n" \
"v_mfma_f32_16x16x32_bf16 v[160:163], v[196:199], v[40:43], v[160:163]\n" \
"ds_read_b128 a[252:255], v247 offset:15360\n" \
"v_add_f32 v242, v242, v22\n" \
"v_cvt_pk_bf16_f32 v18, v20, v21\n" \
"v_exp_f32 v23, v23\n" \
"v_mfma_f32_16x16x32_bf16 v[164:167], v[200:203], v[8:11], v[164:167]\n" \
"v_mov_b32 v112, v241\n" \
"v_add_f32 v242, v242, v23\n" \
"v_exp_f32 v48, v48\n" \
"v_mfma_f32_16x16x32_bf16 v[164:167], v[204:207], v[40:43], v[164:167]\n" \
"v_add_f32 v242, v242, v48\n" \
"v_cvt_pk_bf16_f32 v19, v22, v23\n" \
"v_mfma_f32_16x16x32_bf16 v[168:171], v[208:211], v[8:11], v[168:171]\n" \
"v_exp_f32 v49, v49\n" \
"v_permlane16_swap_b32 v112, v241\n" \
"v_add_f32 v241, v241, v112\n" \
"v_mov_b32 v112, v241\n" \
"v_mfma_f32_16x16x32_bf16 v[168:171], v[212:215], v[40:43], v[168:171]\n" \
"s_waitcnt vmcnt(0) lgkmcnt(0)\n" \
"v_add_f32 v242, v242, v49\n" \
"v_exp_f32 v50, v50\n" \
"v_mfma_f32_16x16x32_bf16 v[172:175], v[216:219], v[8:11], v[172:175]\n" \
"s_barrier\n" \
"v_add_f32 v242, v242, v50\n" \
"v_cvt_pk_bf16_f32 v48, v48, v49\n" \
"v_permlane32_swap_b32 v112, v241\n" \
"v_add_f32 v241, v241, v112\n" \
"v_rcp_f32 v112, v241\n" \
"v_mul_f32 v241, 0x4d800000, v241\n" \
"v_add_u32 v113, 0x1c90080, v249\n" \
"v_cndmask_b32 v113, v249, v113, s[16:17]\n" \
"v_accvgpr_read_b32 v96, a32\n" \
"v_accvgpr_read_b32 v97, a33\n" \
"v_accvgpr_read_b32 v98, a34\n" \
"v_accvgpr_read_b32 v99, a35\n" \
"v_accvgpr_read_b32 v100, a36\n" \
"v_accvgpr_read_b32 v101, a37\n" \
"v_accvgpr_read_b32 v102, a38\n" \
"v_accvgpr_read_b32 v103, a39\n" \
"v_pk_mul_f32 v[96:97], v[96:97], v[112:113] op_sel_hi:[1,0]\n" \
"v_pk_mul_f32 v[98:99], v[98:99], v[112:113] op_sel_hi:[1,0]\n" \
"v_pk_mul_f32 v[100:101], v[100:101], v[112:113] op_sel_hi:[1,0]\n" \
"v_pk_mul_f32 v[102:103], v[102:103], v[112:113] op_sel_hi:[1,0]\n" \
"v_cvt_pk_bf16_f32 v104, v96, v97\n" \
"v_mfma_f32_16x16x32_bf16 v[172:175], v[220:223], v[40:43], v[172:175]\n" \
"s_add_u32 m0, s84, 90112\n" \
"v_cvt_pk_bf16_f32 v105, v98, v99\n" \
"v_cvt_pk_bf16_f32 v106, v100, v101\n" \
"v_cvt_pk_bf16_f32 v107, v102, v103\n" \
"v_exp_f32 v51, v51\n" \
"v_pk_mul_f32 v[96:97], v[96:97], v[98:99]\n" \
"v_pk_fma_f32 v[96:97], v[100:101], v[102:103], v[96:97]\n" \
"v_fma_f32 v241, v96, v97, v241\n" \
"v_mfma_f32_16x16x32_bf16 v[176:179], v[224:227], v[8:11], v[176:179]\n" \
"buffer_store_dwordx4 v[104:107], v113, s[56:59], s77 offen offset:0 sc1\n" \
"global_load_lds_dwordx4 v253, s[48:49] offset:-4096\n" \
"v_add_f32 v242, v242, v51\n" \
"v_accvgpr_read_b32 v96, a40\n" \
"v_accvgpr_read_b32 v97, a41\n" \
"v_accvgpr_read_b32 v98, a42\n" \
"v_accvgpr_read_b32 v99, a43\n" \
"v_accvgpr_read_b32 v100, a44\n" \
"v_accvgpr_read_b32 v101, a45\n" \
"v_accvgpr_read_b32 v102, a46\n" \
"v_accvgpr_read_b32 v103, a47\n" \
"v_pk_mul_f32 v[96:97], v[96:97], v[112:113] op_sel_hi:[1,0]\n" \
"v_pk_mul_f32 v[98:99], v[98:99], v[112:113] op_sel_hi:[1,0]\n" \
"v_pk_mul_f32 v[100:101], v[100:101], v[112:113] op_sel_hi:[1,0]\n" \
"v_pk_mul_f32 v[102:103], v[102:103], v[112:113] op_sel_hi:[1,0]\n" \
"v_cvt_pk_bf16_f32 v108, v96, v97\n" \
"v_cvt_pk_bf16_f32 v109, v98, v99\n" \
"v_cvt_pk_bf16_f32 v110, v100, v101\n" \
"v_cvt_pk_bf16_f32 v111, v102, v103\n" \
"v_pk_mul_f32 v[96:97], v[96:97], v[98:99]\n" \
"v_pk_fma_f32 v[96:97], v[100:101], v[102:103], v[96:97]\n" \
"v_fma_f32 v241, v96, v97, v241\n" \
"v_mfma_f32_16x16x32_bf16 v[176:179], v[228:231], v[40:43], v[176:179]\n" \
"global_load_lds_dwordx4 v253, s[48:49] offset:-2048\n" \
"v_exp_f32 v52, v52\n" \
"v_mfma_f32_16x16x32_bf16 v[180:183], v[232:235], v[8:11], v[180:183]\n" \
"v_add_f32 v242, v242, v52\n" \
"v_cvt_pk_bf16_f32 v49, v50, v51\n" \
"v_mfma_f32_16x16x32_bf16 v[180:183], v[236:239], v[40:43], v[180:183]\n" \
"global_load_lds_dwordx4 v253, s[48:49] offset:0\n" \
"v_exp_f32 v53, v53\n" \
"v_mfma_f32_16x16x32_bf16 v[184:187], v[0:3], v[8:11], v[184:187]\n" \
"s_add_u32 s48, s48, 0x300000\n" \
"s_addc_u32 s49, s49, 0\n" \
"v_add_f32 v242, v242, v53\n" \
"v_mfma_f32_16x16x32_bf16 v[184:187], v[4:7], v[40:43], v[184:187]\n" \
"global_load_lds_dwordx4 v253, s[50:51] offset:-3072\n" \
"v_exp_f32 v54, v54\n" \
"v_mfma_f32_16x16x32_bf16 v[188:191], v[32:35], v[8:11], v[188:191]\n" \
"v_add_f32 v242, v242, v54\n" \
"v_cvt_pk_bf16_f32 v50, v52, v53\n" \
"v_mfma_f32_16x16x32_bf16 v[188:191], v[36:39], v[40:43], v[188:191]\n" \
"global_load_lds_dwordx4 v253, s[50:51] offset:-1024\n" \
"v_exp_f32 v55, v55\n" \
"v_mfma_f32_16x16x32_bf16 v[160:163], a[192:195], v[24:27], v[160:163]\n" \
"v_add_f32 v242, v242, v55\n" \
"v_cvt_pk_bf16_f32 v51, v54, v55\n" \
"v_mfma_f32_16x16x32_bf16 v[160:163], a[196:199], v[56:59], v[160:163]\n" \
"s_movk_i32 s89, 0xc0\n" \
"global_load_lds_dwordx4 v253, s[50:51] offset:1024\n" \
"v_mfma_f32_16x16x32_bf16 v[164:167], a[200:203], v[24:27], v[164:167]\n" \
"s_add_u32 s50, s50, 0x300000\n" \
"s_addc_u32 s51, s51, 0\n" \
"s_add_u32 m0, s95, 65536\n" \
"v_mfma_f32_16x16x32_bf16 v[164:167], a[204:207], v[56:59], v[164:167]\n" \
"s_movk_i32 s90, 288\n" \
"global_load_lds_dwordx4 v252, s[42:43] offset:0\n" \
"buffer_store_dwordx4 v[108:111], v113, s[56:59], s77 offen offset:2048 sc1\n" \
"v_lshrrev_b32 v211, 4, v248\n" \
"v_lshrrev_b32 v214, 8, v248\n" \
"v_and_b32 v215, 7, v211\n" \
"v_xor_b32 v214, v214, v215\n" \
"v_mfma_f32_16x16x32_bf16 v[168:171], a[208:211], v[24:27], v[168:171]\n" \
"s_lshr_b32 s71, s61, 1\n" \
"s_lshl_b32 s70, s71, 11\n" \
"s_add_u32 s70, s70, 118784\n" \
"v_mfma_f32_16x16x32_bf16 v[168:171], a[212:215], v[56:59], v[168:171]\n" \
"global_load_lds_dwordx4 v252, s[42:43] offset:2048\n" \
"v_and_b32 v216, 15, v211\n" \
"v_mfma_f32_16x16x32_bf16 v[172:175], a[216:219], v[24:27], v[172:175]\n" \
"s_add_u32 s42, s42, 0x400\n" \
"s_addc_u32 s43, s43, 0\n" \
"v_lshlrev_b32 v213, 7, v216\n" \
"v_lshl_add_u32 v213, v214, 4, v213\n" \
"v_mfma_f32_16x16x32_bf16 v[172:175], a[220:223], v[56:59], v[172:175]\n" \
"global_load_lds_dwordx4 v252, s[44:45] offset:1024\n" \
"v_add_u32 v205, s70, v213\n" \
"v_xor_b32 v206, 64, v205\n" \
"v_lshrrev_b32 v216, 2, v216\n" \
"v_lshl_add_u32 v216, v216, 9, v213\n" \
"v_add_u32 v207, 135168, v216\n" \
"v_xor_b32 v208, 64, v207\n" \
"v_mfma_f32_16x16x32_bf16 v[176:179], a[224:227], v[24:27], v[176:179]\n" \
"s_and_b32 s70, s61, 1\n" \
"s_lshl_b32 s70, s70, 9\n" \
"s_add_u32 s70, s70, 139264\n" \
"v_mfma_f32_16x16x32_bf16 v[176:179], a[228:231], v[56:59], v[176:179]\n" \
"s_lshl_b32 s71, s71, 5\n" \
"global_load_lds_dwordx4 v252, s[44:45] offset:3072\n" \
"v_mfma_f32_16x16x32_bf16 v[180:183], a[232:235], v[24:27], v[180:183]\n" \
"s_add_u32 s44, s44, 0x400\n" \
"s_addc_u32 s45, s45, 0\n" \
"s_add_u32 m0, s85, 81920\n" \
"v_mfma_f32_16x16x32_bf16 v[180:183], a[236:239], v[56:59], v[180:183]\n" \
"buffer_load_dwordx4 v246, s[12:15], s46 offen lds\n" \
"v_mfma_f32_16x16x32_bf16 v[184:187], a[240:243], v[24:27], v[184:187]\n" \
"s_add_u32 s46, s46, 0x400\n" \
"v_and_b32 v213, 3, v211\n" \
"v_lshlrev_b32 v213, 3, v213\n" \
"v_mfma_f32_16x16x32_bf16 v[184:187], a[244:247], v[56:59], v[184:187]\n" \
"s_add_u32 m0, s95, 0\n" \
"v_and_or_b32 v213, v248, s89, v213\n" \
"v_bfe_u32 v214, v211, 4, 1\n" \
"v_mfma_f32_16x16x32_bf16 v[188:191], a[248:251], v[24:27], v[188:191]\n" \
"global_load_lds_dwordx4 v252, s[42:43] offset:0\n" \
"v_mad_u32_u24 v213, v214, s90, v213\n" \
"v_lshrrev_b32 v214, 5, v211\n" \
"v_mul_u32_u24 v214, 6144, v214\n" \
"v_add3_u32 v213, v213, v214, s70\n" \
"v_xor_b32 v209, s71, v213\n" \
"v_xor_b32 v210, 32, v209\n" \
"v_mfma_f32_16x16x32_bf16 v[188:191], a[252:255], v[56:59], v[188:191]\n" \
"s_add_u32 s77, s77, 0x1000\n" \
"ds_read_b128 a[64:67], v207 offset:0\n" \
"ds_read_b64_tr_b16 a[96:97], v209 offset:2048\n" \
"v_accvgpr_read_b32 v96, a48\n" \
"v_accvgpr_read_b32 v97, a49\n" \
"v_accvgpr_read_b32 v98, a50\n" \
"v_accvgpr_read_b32 v99, a51\n" \
"v_accvgpr_read_b32 v100, a52\n" \
"v_accvgpr_read_b32 v101, a53\n" \
"v_accvgpr_read_b32 v102, a54\n" \
"v_accvgpr_read_b32 v103, a55\n" \
"v_pk_mul_f32 v[96:97], v[96:97], v[112:113] op_sel_hi:[1,0]\n" \
"v_pk_mul_f32 v[98:99], v[98:99], v[112:113] op_sel_hi:[1,0]\n" \
"v_pk_mul_f32 v[100:101], v[100:101], v[112:113] op_sel_hi:[1,0]\n" \
"v_pk_mul_f32 v[102:103], v[102:103], v[112:113] op_sel_hi:[1,0]\n" \
"v_cvt_pk_bf16_f32 v104, v96, v97\n" \
"v_cvt_pk_bf16_f32 v105, v98, v99\n" \
"v_cvt_pk_bf16_f32 v106, v100, v101\n" \
"v_cvt_pk_bf16_f32 v107, v102, v103\n" \
"v_pk_mul_f32 v[96:97], v[96:97], v[98:99]\n" \
"v_pk_fma_f32 v[96:97], v[100:101], v[102:103], v[96:97]\n" \
"v_mfma_f32_16x16x32_bf16 v[128:131], a[192:195], v[16:19], v[128:131]\n" \
"ds_read_b64_tr_b16 a[98:99], v209 offset:3072\n" \
"v_fma_f32 v241, v96, v97, v241\n" \
"v_mfma_f32_16x16x32_bf16 v[128:131], a[196:199], v[48:51], v[128:131]\n" \
"ds_read_b128 a[68:71], v208 offset:0\n" \
"global_load_lds_dwordx4 v252, s[42:43] offset:2048\n" \
"v_mfma_f32_16x16x32_bf16 v[132:135], a[200:203], v[16:19], v[132:135]\n" \
"ds_read_b64_tr_b16 a[100:101], v209 offset:14336\n" \
"buffer_store_dwordx4 v[104:107], v113, s[56:59], s77 offen offset:0 sc1\n" \
"ds_read_b64_tr_b16 a[102:103], v209 offset:15360\n" \
"v_accvgpr_read_b32 v96, a56\n" \
"v_accvgpr_read_b32 v97, a57\n" \
"v_accvgpr_read_b32 v98, a58\n" \
"v_mfma_f32_16x16x32_bf16 v[132:135], a[204:207], v[48:51], v[132:135]\n" \
"s_add_u32 s42, s42, 0xfffffb00\n" \
"s_addc_u32 s43, s43, -1\n" \
"ds_read_b128 a[72:75], v205 offset:0\n" \
"v_mfma_f32_16x16x32_bf16 v[136:139], a[208:211], v[16:19], v[136:139]\n" \
"ds_read_b64_tr_b16 a[104:105], v209 offset:0\n" \
"global_load_lds_dwordx4 v252, s[44:45] offset:1024\n" \
"v_mfma_f32_16x16x32_bf16 v[136:139], a[212:215], v[48:51], v[136:139]\n" \
"ds_read_b128 a[76:79], v206 offset:0\n" \
"ds_read_b64_tr_b16 a[106:107], v209 offset:1024\n" \
"v_mfma_f32_16x16x32_bf16 v[140:143], a[216:219], v[16:19], v[140:143]\n" \
"ds_read_b64_tr_b16 a[108:109], v209 offset:12288\n" \
"global_load_lds_dwordx4 v252, s[44:45] offset:3072\n" \
"v_mfma_f32_16x16x32_bf16 v[140:143], a[220:223], v[48:51], v[140:143]\n" \
"s_add_u32 s44, s44, 0xfffffb00\n" \
"s_addc_u32 s45, s45, -1\n" \
"ds_read_b64_tr_b16 a[110:111], v209 offset:13312\n" \
"v_mfma_f32_16x16x32_bf16 v[144:147], a[224:227], v[16:19], v[144:147]\n" \
"ds_read_b128 a[80:83], v205 offset:4096\n" \
"ds_read_b64_tr_b16 a[112:113], v210 offset:0\n" \
"v_mfma_f32_16x16x32_bf16 v[144:147], a[228:231], v[48:51], v[144:147]\n" \
"s_add_u32 m0, s85, 16384\n" \
"ds_read_b64_tr_b16 a[114:115], v210 offset:1024\n" \
"v_accvgpr_read_b32 v99, a59\n" \
"v_mfma_f32_16x16x32_bf16 v[148:151], a[232:235], v[16:19], v[148:151]\n" \
"ds_read_b64_tr_b16 a[116:117], v210 offset:12288\n" \
"v_accvgpr_read_b32 v100, a60\n" \
"v_accvgpr_read_b32 v101, a61\n" \
"v_accvgpr_read_b32 v102, a62\n" \
"v_accvgpr_read_b32 v103, a63\n" \
"v_pk_mul_f32 v[96:97], v[96:97], v[112:113] op_sel_hi:[1,0]\n" \
"v_pk_mul_f32 v[98:99], v[98:99], v[112:113] op_sel_hi:[1,0]\n" \
"v_pk_mul_f32 v[100:101], v[100:101], v[112:113] op_sel_hi:[1,0]\n" \
"v_pk_mul_f32 v[102:103], v[102:103], v[112:113] op_sel_hi:[1,0]\n" \
"v_mfma_f32_16x16x32_bf16 v[148:151], a[236:239], v[48:51], v[148:151]\n" \
"ds_read_b128 a[84:87], v206 offset:4096\n" \
"ds_read_b64_tr_b16 a[118:119], v210 offset:13312\n" \
"v_mfma_f32_16x16x32_bf16 v[152:155], a[240:243], v[16:19], v[152:155]\n" \
"ds_read_b128 a[88:91], v205 offset:8192\n" \
"v_cvt_pk_bf16_f32 v108, v96, v97\n" \
"v_cvt_pk_bf16_f32 v109, v98, v99\n" \
"v_cvt_pk_bf16_f32 v110, v100, v101\n" \
"v_cvt_pk_bf16_f32 v111, v102, v103\n" \
"v_pk_mul_f32 v[96:97], v[96:97], v[98:99]\n" \
"v_pk_fma_f32 v[96:97], v[100:101], v[102:103], v[96:97]\n" \
"v_mfma_f32_16x16x32_bf16 v[152:155], a[244:247], v[48:51], v[152:155]\n" \
"ds_read_b128 a[92:95], v206 offset:8192\n" \
"v_fma_f32 v241, v96, v97, v241\n" \
"v_mfma_f32_16x16x32_bf16 v[156:159], a[248:251], v[16:19], v[156:159]\n" \
"buffer_store_dwordx4 v[108:111], v113, s[56:59], s77 offen offset:2048 sc1\n" \
"buffer_load_dwordx4 v246, s[12:15], s46 offen lds\n" \
"s_add_u32 s77, s77, 0xfff7d000\n" \
"v_mfma_f32_16x16x32_bf16 v[156:159], a[252:255], v[48:51], v[156:159]\n" \
"s_waitcnt lgkmcnt(4)\n" \
"s_branch Ljoin_%=\n" \
"Lexact_%=:\n" \
"v_mov_b32 v190, 0xff800000\n" \
"v_add_u32 v191, 0x1c90080, v249\n" \
"v_cndmask_b32 v191, v249, v191, s[16:17]\n" \
"s_and_b32 s80, s61, 2\n" \
"s_lshl_b32 s80, s80, 4\n" \
"v_subrev_u32 v186, s80, v251\n" \
"v_accvgpr_read_b32 v160, a128\n" \
"v_accvgpr_read_b32 v161, a129\n" \
"v_accvgpr_read_b32 v162, a130\n" \
"v_accvgpr_read_b32 v163, a131\n" \
"v_accvgpr_read_b32 v164, a132\n" \
"v_accvgpr_read_b32 v165, a133\n" \
"v_accvgpr_read_b32 v166, a134\n" \
"v_accvgpr_read_b32 v167, a135\n" \
"v_accvgpr_read_b32 v168, a136\n" \
"v_accvgpr_read_b32 v169, a137\n" \
"v_accvgpr_read_b32 v170, a138\n" \
"v_accvgpr_read_b32 v171, a139\n" \
"v_accvgpr_read_b32 v172, a140\n" \
"v_accvgpr_read_b32 v173, a141\n" \
"v_accvgpr_read_b32 v174, a142\n" \
"v_accvgpr_read_b32 v175, a143\n" \
"s_lshl_b32 s75, s63, 2\n" \
"s_lshr_b32 s80, s61, 1\n" \
"s_add_u32 s75, s75, s80\n" \
"s_add_u32 s75, s75, 0\n" \
"s_add_u32 s76, s77, 0\n" \
"s_call_b64 s[68:69], Lex1_%=\n" \
"v_accvgpr_read_b32 v160, a144\n" \
"v_accvgpr_read_b32 v161, a145\n" \
"v_accvgpr_read_b32 v162, a146\n" \
"v_accvgpr_read_b32 v163, a147\n" \
"v_accvgpr_read_b32 v164, a148\n" \
"v_accvgpr_read_b32 v165, a149\n" \
"v_accvgpr_read_b32 v166, a150\n" \
"v_accvgpr_read_b32 v167, a151\n" \
"v_accvgpr_read_b32 v168, a152\n" \
"v_accvgpr_read_b32 v169, a153\n" \
"v_accvgpr_read_b32 v170, a154\n" \
"v_accvgpr_read_b32 v171, a155\n" \
"v_accvgpr_read_b32 v172, a156\n" \
"v_accvgpr_read_b32 v173, a157\n" \
"v_accvgpr_read_b32 v174, a158\n" \
"v_accvgpr_read_b32 v175, a159\n" \
"s_lshl_b32 s75, s63, 2\n" \
"s_lshr_b32 s80, s61, 1\n" \
"s_add_u32 s75, s75, s80\n" \
"s_add_u32 s75, s75, 2\n" \
"s_add_u32 s76, s77, 532480\n" \
"s_call_b64 s[68:69], Lex1_%=\n" \
"v_accvgpr_read_b32 v160, a160\n" \
"v_accvgpr_read_b32 v161, a161\n" \
"v_accvgpr_read_b32 v162, a162\n" \
"v_accvgpr_read_b32 v163, a163\n" \
"v_accvgpr_read_b32 v164, a164\n" \
"v_accvgpr_read_b32 v165, a165\n" \
"v_accvgpr_read_b32 v166, a166\n" \
"v_accvgpr_read_b32 v167, a167\n" \
"v_accvgpr_read_b32 v168, a168\n" \
"v_accvgpr_read_b32 v169, a169\n" \
"v_accvgpr_read_b32 v170, a170\n" \
"v_accvgpr_read_b32 v171, a171\n" \
"v_accvgpr_read_b32 v172, a172\n" \
"v_accvgpr_read_b32 v173, a173\n" \
"v_accvgpr_read_b32 v174, a174\n" \
"v_accvgpr_read_b32 v175, a175\n" \
"s_lshl_b32 s75, s63, 2\n" \
"s_sub_u32 s75, 60, s75\n" \
"s_lshr_b32 s80, s61, 1\n" \
"s_add_u32 s75, s75, s80\n" \
"s_add_u32 s75, s75, 0\n" \
"s_add_u32 s76, s78, 0\n" \
"s_call_b64 s[68:69], Lex1_%=\n" \
"v_accvgpr_read_b32 v160, a176\n" \
"v_accvgpr_read_b32 v161, a177\n" \
"v_accvgpr_read_b32 v162, a178\n" \
"v_accvgpr_read_b32 v163, a179\n" \
"v_accvgpr_read_b32 v164, a180\n" \
"v_accvgpr_read_b32 v165, a181\n" \
"v_accvgpr_read_b32 v166, a182\n" \
"v_accvgpr_read_b32 v167, a183\n" \
"v_accvgpr_read_b32 v168, a184\n" \
"v_accvgpr_read_b32 v169, a185\n" \
"v_accvgpr_read_b32 v170, a186\n" \
"v_accvgpr_read_b32 v171, a187\n" \
"v_accvgpr_read_b32 v172, a188\n" \
"v_accvgpr_read_b32 v173, a189\n" \
"v_accvgpr_read_b32 v174, a190\n" \
"v_accvgpr_read_b32 v175, a191\n" \
"s_lshl_b32 s75, s63, 2\n" \
"s_sub_u32 s75, 60, s75\n" \
"s_lshr_b32 s80, s61, 1\n" \
"s_add_u32 s75, s75, s80\n" \
"s_add_u32 s75, s75, 2\n" \
"s_add_u32 s76, s78, 532480\n" \
"s_call_b64 s[68:69], Lex1_%=\n" \
"s_branch Lexr_%=\n" \
"Lex1_%=:\n" \
"v_mov_b32 v128, 0\n" \
"v_mov_b32 v129, 0\n" \
"v_mov_b32 v130, 0\n" \
"v_mov_b32 v131, 0\n" \
"v_mov_b32 v132, 0\n" \
"v_mov_b32 v133, 0\n" \
"v_mov_b32 v134, 0\n" \
"v_mov_b32 v135, 0\n" \
"v_mov_b32 v136, 0\n" \
"v_mov_b32 v137, 0\n" \
"v_mov_b32 v138, 0\n" \
"v_mov_b32 v139, 0\n" \
"v_mov_b32 v140, 0\n" \
"v_mov_b32 v141, 0\n" \
"v_mov_b32 v142, 0\n" \
"v_mov_b32 v143, 0\n" \
"v_mov_b32 v144, 0\n" \
"v_mov_b32 v145, 0\n" \
"v_mov_b32 v146, 0\n" \
"v_mov_b32 v147, 0\n" \
"v_mov_b32 v148, 0\n" \
"v_mov_b32 v149, 0\n" \
"v_mov_b32 v150, 0\n" \
"v_mov_b32 v151, 0\n" \
"v_mov_b32 v152, 0\n" \
"v_mov_b32 v153, 0\n" \
"v_mov_b32 v154, 0\n" \
"v_mov_b32 v155, 0\n" \
"v_mov_b32 v156, 0\n" \
"v_mov_b32 v157, 0\n" \
"v_mov_b32 v158, 0\n" \
"v_mov_b32 v159, 0\n" \
"v_mov_b32 v184, 0xf149f2ca\n" \
"v_mov_b32 v185, 0\n" \
"s_mov_b32 s73, 0\n" \
"s_mul_i32 s74, s62, 557824\n" \
"Lex2_%=:\n" \
"s_add_u32 s80, s74, 4096\n" \
"buffer_load_dwordx4 a[0:3], v249, s[56:59], s74 offen offset:0 sc1\n" \
"buffer_load_dwordx4 a[4:7], v249, s[56:59], s74 offen offset:2048 sc1\n" \
"buffer_load_dwordx4 a[8:11], v249, s[56:59], s80 offen offset:0 sc1\n" \
"buffer_load_dwordx4 a[12:15], v249, s[56:59], s80 offen offset:2048 sc1\n" \
"buffer_load_dwordx4 a[16:19], v249, s[56:59], s74 offen offset:256 sc1\n" \
"buffer_load_dwordx4 a[20:23], v249, s[56:59], s74 offen offset:2304 sc1\n" \
"buffer_load_dwordx4 a[24:27], v249, s[56:59], s80 offen offset:256 sc1\n" \
"buffer_load_dwordx4 a[28:31], v249, s[56:59], s80 offen offset:2304 sc1\n" \
"s_add_u32 s80, s74, 17850368\n" \
"s_add_u32 s81, s80, 4096\n" \
"buffer_load_dwordx4 a[32:35], v248, s[56:59], s80 offen offset:0 sc1\n" \
"buffer_load_dwordx4 a[36:39], v248, s[56:59], s80 offen offset:1024 sc1\n" \
"buffer_load_dwordx4 a[40:43], v248, s[56:59], s80 offen offset:2048 sc1\n" \
"buffer_load_dwordx4 a[44:47], v248, s[56:59], s80 offen offset:3072 sc1\n" \
"buffer_load_dwordx4 a[48:51], v248, s[56:59], s81 offen offset:0 sc1\n" \
"buffer_load_dwordx4 a[52:55], v248, s[56:59], s81 offen offset:1024 sc1\n" \
"buffer_load_dwordx4 a[56:59], v248, s[56:59], s81 offen offset:2048 sc1\n" \
"buffer_load_dwordx4 a[60:63], v248, s[56:59], s81 offen offset:3072 sc1\n" \
"s_waitcnt vmcnt(0)\n" \
"v_mfma_f32_16x16x32_bf16 v[176:179], a[0:3], v[160:163], 0\n" \
"v_mfma_f32_16x16x32_bf16 v[176:179], a[4:7], v[164:167], v[176:179]\n" \
"v_mfma_f32_16x16x32_bf16 v[176:179], a[8:11], v[168:171], v[176:179]\n" \
"v_mfma_f32_16x16x32_bf16 v[176:179], a[12:15], v[172:175], v[176:179]\n" \
"v_mfma_f32_16x16x32_bf16 v[180:183], a[16:19], v[160:163], 0\n" \
"v_mfma_f32_16x16x32_bf16 v[180:183], a[20:23], v[164:167], v[180:183]\n" \
"v_mfma_f32_16x16x32_bf16 v[180:183], a[24:27], v[168:171], v[180:183]\n" \
"v_mfma_f32_16x16x32_bf16 v[180:183], a[28:31], v[172:175], v[180:183]\n" \
"s_nop 7\n" \
"s_nop 1\n" \
"s_cmp_eq_u32 s73, s75\n" \
"s_cbranch_scc0 Lex3_%=\n" \
"v_cmp_gt_i32 vcc, 0, v186\n" \
"s_nop 1\n" \
"v_cndmask_b32 v176, v176, v190, vcc\n" \
"v_cmp_gt_i32 vcc, 1, v186\n" \
"s_nop 1\n" \
"v_cndmask_b32 v177, v177, v190, vcc\n" \
"v_cmp_gt_i32 vcc, 2, v186\n" \
"s_nop 1\n" \
"v_cndmask_b32 v178, v178, v190, vcc\n" \
"v_cmp_gt_i32 vcc, 3, v186\n" \
"s_nop 1\n" \
"v_cndmask_b32 v179, v179, v190, vcc\n" \
"v_cmp_gt_i32 vcc, 16, v186\n" \
"s_nop 1\n" \
"v_cndmask_b32 v180, v180, v190, vcc\n" \
"v_cmp_gt_i32 vcc, 17, v186\n" \
"s_nop 1\n" \
"v_cndmask_b32 v181, v181, v190, vcc\n" \
"v_cmp_gt_i32 vcc, 18, v186\n" \
"s_nop 1\n" \
"v_cndmask_b32 v182, v182, v190, vcc\n" \
"v_cmp_gt_i32 vcc, 19, v186\n" \
"s_nop 1\n" \
"v_cndmask_b32 v183, v183, v190, vcc\n" \
"Lex3_%=:\n" \
"v_max3_f32 v187, v176, v177, v178\n" \
"v_max3_f32 v187, v187, v179, v180\n" \
"v_max3_f32 v187, v187, v181, v182\n" \
"v_max3_f32 v187, v187, v183, v184\n" \
"v_mov_b32 v188, v187\n" \
"s_nop 1\n" \
"v_permlane16_swap_b32 v188, v187\n" \
"v_max_f32 v187, v187, v188\n" \
"v_mov_b32 v188, v187\n" \
"s_nop 1\n" \
"v_permlane32_swap_b32 v188, v187\n" \
"v_max_f32 v187, v187, v188\n" \
"v_sub_f32 v189, v184, v187\n" \
"v_exp_f32 v189, v189\n" \
"v_mov_b32 v184, v187\n" \
"v_sub_f32 v176, v176, v187\n" \
"v_sub_f32 v177, v177, v187\n" \
"v_sub_f32 v178, v178, v187\n" \
"v_sub_f32 v179, v179, v187\n" \
"v_sub_f32 v180, v180, v187\n" \
"v_sub_f32 v181, v181, v187\n" \
"v_sub_f32 v182, v182, v187\n" \
"v_sub_f32 v183, v183, v187\n" \
"v_exp_f32 v176, v176\n" \
"v_exp_f32 v177, v177\n" \
"v_exp_f32 v178, v178\n" \
"v_exp_f32 v179, v179\n" \
"v_exp_f32 v180, v180\n" \
"v_exp_f32 v181, v181\n" \
"v_exp_f32 v182, v182\n" \
"v_exp_f32 v183, v183\n" \
"v_mul_f32 v185, v185, v189\n" \
"v_add_f32 v185, v185, v176\n" \
"v_add_f32 v185, v185, v177\n" \
"v_add_f32 v185, v185, v178\n" \
"v_add_f32 v185, v185, v179\n" \
"v_add_f32 v185, v185, v180\n" \
"v_add_f32 v185, v185, v181\n" \
"v_add_f32 v185, v185, v182\n" \
"v_add_f32 v185, v185, v183\n" \
"v_cvt_pk_bf16_f32 v112, v176, v177\n" \
"v_cvt_pk_bf16_f32 v113, v178, v179\n" \
"v_cvt_pk_bf16_f32 v114, v180, v181\n" \
"v_cvt_pk_bf16_f32 v115, v182, v183\n" \
"v_mul_f32 v128, v128, v189\n" \
"v_mul_f32 v129, v129, v189\n" \
"v_mul_f32 v130, v130, v189\n" \
"v_mul_f32 v131, v131, v189\n" \
"v_mul_f32 v132, v132, v189\n" \
"v_mul_f32 v133, v133, v189\n" \
"v_mul_f32 v134, v134, v189\n" \
"v_mul_f32 v135, v135, v189\n" \
"v_mul_f32 v136, v136, v189\n" \
"v_mul_f32 v137, v137, v189\n" \
"v_mul_f32 v138, v138, v189\n" \
"v_mul_f32 v139, v139, v189\n" \
"v_mul_f32 v140, v140, v189\n" \
"v_mul_f32 v141, v141, v189\n" \
"v_mul_f32 v142, v142, v189\n" \
"v_mul_f32 v143, v143, v189\n" \
"v_mul_f32 v144, v144, v189\n" \
"v_mul_f32 v145, v145, v189\n" \
"v_mul_f32 v146, v146, v189\n" \
"v_mul_f32 v147, v147, v189\n" \
"v_mul_f32 v148, v148, v189\n" \
"v_mul_f32 v149, v149, v189\n" \
"v_mul_f32 v150, v150, v189\n" \
"v_mul_f32 v151, v151, v189\n" \
"v_mul_f32 v152, v152, v189\n" \
"v_mul_f32 v153, v153, v189\n" \
"v_mul_f32 v154, v154, v189\n" \
"v_mul_f32 v155, v155, v189\n" \
"v_mul_f32 v156, v156, v189\n" \
"v_mul_f32 v157, v157, v189\n" \
"v_mul_f32 v158, v158, v189\n" \
"v_mul_f32 v159, v159, v189\n" \
"s_nop 1\n" \
"v_mfma_f32_16x16x32_bf16 v[128:131], a[32:35], v[112:115], v[128:131]\n" \
"v_mfma_f32_16x16x32_bf16 v[132:135], a[36:39], v[112:115], v[132:135]\n" \
"v_mfma_f32_16x16x32_bf16 v[136:139], a[40:43], v[112:115], v[136:139]\n" \
"v_mfma_f32_16x16x32_bf16 v[140:143], a[44:47], v[112:115], v[140:143]\n" \
"v_mfma_f32_16x16x32_bf16 v[144:147], a[48:51], v[112:115], v[144:147]\n" \
"v_mfma_f32_16x16x32_bf16 v[148:151], a[52:55], v[112:115], v[148:151]\n" \
"v_mfma_f32_16x16x32_bf16 v[152:155], a[56:59], v[112:115], v[152:155]\n" \
"v_mfma_f32_16x16x32_bf16 v[156:159], a[60:63], v[112:115], v[156:159]\n" \
"s_nop 7\n" \
"s_nop 1\n" \
"s_and_b32 s80, s73, 1\n" \
"s_mul_i32 s80, s80, 1024\n" \
"s_add_u32 s74, s74, s80\n" \
"s_add_u32 s74, s74, 8192\n" \
"s_add_u32 s73, s73, 1\n" \
"s_cmp_le_u32 s73, s75\n" \
"s_cbranch_scc1 Lex2_%=\n" \
"v_mov_b32 v187, v185\n" \
"s_nop 1\n" \
"v_permlane16_swap_b32 v187, v185\n" \
"v_add_f32 v185, v185, v187\n" \
"v_mov_b32 v187, v185\n" \
"s_nop 1\n" \
"v_permlane32_swap_b32 v187, v185\n" \
"v_add_f32 v185, v185, v187\n" \
"v_rcp_f32 v185, v185\n" \
"s_nop 0\n" \
"v_mul_f32 v128, v128, v185\n" \
"v_mul_f32 v129, v129, v185\n" \
"v_mul_f32 v130, v130, v185\n" \
"v_mul_f32 v131, v131, v185\n" \
"v_mul_f32 v132, v132, v185\n" \
"v_mul_f32 v133, v133, v185\n" \
"v_mul_f32 v134, v134, v185\n" \
"v_mul_f32 v135, v135, v185\n" \
"v_mul_f32 v136, v136, v185\n" \
"v_mul_f32 v137, v137, v185\n" \
"v_mul_f32 v138, v138, v185\n" \
"v_mul_f32 v139, v139, v185\n" \
"v_mul_f32 v140, v140, v185\n" \
"v_mul_f32 v141, v141, v185\n" \
"v_mul_f32 v142, v142, v185\n" \
"v_mul_f32 v143, v143, v185\n" \
"v_mul_f32 v144, v144, v185\n" \
"v_mul_f32 v145, v145, v185\n" \
"v_mul_f32 v146, v146, v185\n" \
"v_mul_f32 v147, v147, v185\n" \
"v_mul_f32 v148, v148, v185\n" \
"v_mul_f32 v149, v149, v185\n" \
"v_mul_f32 v150, v150, v185\n" \
"v_mul_f32 v151, v151, v185\n" \
"v_mul_f32 v152, v152, v185\n" \
"v_mul_f32 v153, v153, v185\n" \
"v_mul_f32 v154, v154, v185\n" \
"v_mul_f32 v155, v155, v185\n" \
"v_mul_f32 v156, v156, v185\n" \
"v_mul_f32 v157, v157, v185\n" \
"v_mul_f32 v158, v158, v185\n" \
"v_mul_f32 v159, v159, v185\n" \
"s_add_u32 s80, s76, 4096\n" \
"v_cvt_pk_bf16_f32 v112, v128, v129\n" \
"v_cvt_pk_bf16_f32 v113, v130, v131\n" \
"v_cvt_pk_bf16_f32 v114, v132, v133\n" \
"v_cvt_pk_bf16_f32 v115, v134, v135\n" \
"buffer_store_dwordx4 v[112:115], v191, s[56:59], s76 offen offset:0 sc1\n" \
"v_cvt_pk_bf16_f32 v116, v136, v137\n" \
"v_cvt_pk_bf16_f32 v117, v138, v139\n" \
"v_cvt_pk_bf16_f32 v118, v140, v141\n" \
"v_cvt_pk_bf16_f32 v119, v142, v143\n" \
"buffer_store_dwordx4 v[116:119], v191, s[56:59], s76 offen offset:2048 sc1\n" \
"v_cvt_pk_bf16_f32 v120, v144, v145\n" \
"v_cvt_pk_bf16_f32 v121, v146, v147\n" \
"v_cvt_pk_bf16_f32 v122, v148, v149\n" \
"v_cvt_pk_bf16_f32 v123, v150, v151\n" \
"buffer_store_dwordx4 v[120:123], v191, s[56:59], s80 offen offset:0 sc1\n" \
"v_cvt_pk_bf16_f32 v124, v152, v153\n" \
"v_cvt_pk_bf16_f32 v125, v154, v155\n" \
"v_cvt_pk_bf16_f32 v126, v156, v157\n" \
"v_cvt_pk_bf16_f32 v127, v158, v159\n" \
"buffer_store_dwordx4 v[124:127], v191, s[56:59], s80 offen offset:2048 sc1\n" \
"s_waitcnt vmcnt(0)\n" \
"s_setpc_b64 s[68:69]\n" \
"Lpl2_%=:\n" \
"s_mov_b32 s31, 0\n" \
"Lpk2_%=:\n" \
"s_sleep 2\n" \
"buffer_load_dword v220, v221, s[56:59], s81 offen sc1\n" \
"s_waitcnt vmcnt(0)\n" \
"v_cmp_ne_u32 vcc, s60, v220\n" \
"s_nop 1\n" \
"s_and_b32 vcc_hi, vcc_lo, s80\n" \
"s_cbranch_scc0 Lpr2_%=\n" \
"s_add_u32 s31, s31, 1\n" \
"s_cmp_lt_u32 s31, 0x40000\n" \
"s_cbranch_scc1 Lpk2_%=\n" \
"s_branch Lpr2_%=\n" \
"Lpl3_%=:\n" \
"s_mov_b32 s31, 0\n" \
"Lpk3_%=:\n" \
"s_sleep 2\n" \
"buffer_load_dwordx2 v[226:227], v228, s[56:59], s30 offen sc1\n" \
"s_waitcnt vmcnt(0)\n" \
"v_xor_b32 v226, v226, v227\n" \
"v_cmp_ne_u32 vcc, s60, v226\n" \
"s_nop 1\n" \
"s_and_b32 vcc_hi, vcc_lo, -1\n" \
"s_cbranch_scc0 Lpr3_%=\n" \
"s_add_u32 s31, s31, 1\n" \
"s_cmp_lt_u32 s31, 0x40000\n" \
"s_cbranch_scc1 Lpk3_%=\n" \
"s_branch Lpr3_%=\n" \
"Lpl4_%=:\n" \
"s_mov_b32 s31, 0\n" \
"Lpk4_%=:\n" \
"s_sleep 2\n" \
"buffer_load_dword v220, off, s[56:59], s79 sc1\n" \
"s_waitcnt vmcnt(0)\n" \
"v_cmp_ne_u32 vcc, s60, v220\n" \
"s_nop 1\n" \
"s_and_b32 vcc_hi, vcc_lo, 1\n" \
"s_cbranch_scc0 Lpr4_%=\n" \
"s_add_u32 s31, s31, 1\n" \
"s_cmp_lt_u32 s31, 0x40000\n" \
"s_cbranch_scc1 Lpk4_%=\n" \
"s_branch Lpr4_%=\n" \
"Lpl5_%=:\n" \
"s_mov_b32 s31, 0\n" \
"Lpk5_%=:\n" \
"s_sleep 2\n" \
"buffer_load_dword v220, v221, s[56:59], s82 offen sc1\n" \
"s_waitcnt vmcnt(0)\n" \
"v_cmp_ne_u32 vcc, s60, v220\n" \
"s_nop 1\n" \
"s_and_b32 vcc_hi, vcc_lo, 65535\n" \
"s_cbranch_scc0 Lpr5_%=\n" \
"s_add_u32 s31, s31, 1\n" \
"s_cmp_lt_u32 s31, 0x40000\n" \
"s_cbranch_scc1 Lpk5_%=\n" \
"s_branch Lpr5_%=\n" \
"Lcode_end_%=:\n" \
".p2align 12\n" \
""
#define MK_ASM_CLOBBERS "v1", "v2", "v3", "v4", "v5", "v6", "v7", "v8", "v9", "v10", "v11", "v12", "v13", "v14", "v15", "v16", "v17", "v18", "v19", "v20", "v21", "v22", "v23", "v24", "v25", "v26", "v27", "v28", "v29", "v30", "v31", "v32", "v33", "v34", "v35", "v36", "v37", "v38", "v39", "v40", "v41", "v42", "v43", "v44", "v45", "v46", "v47", "v48", "v49", "v50", "v51", "v52", "v53", "v54", "v55", "v56", "v57", "v58", "v59", "v60", "v61", "v62", "v63", "v64", "v65", "v66", "v67", "v68", "v69", "v70", "v71", "v72", "v73", "v74", "v75", "v76", "v77", "v78", "v79", "v80", "v81", "v82", "v83", "v84", "v85", "v86", "v87", "v88", "v89", "v90", "v91", "v92", "v93", "v94", "v95", "v96", "v97", "v98", "v99", "v100", "v101", "v102", "v103", "v104", "v105", "v106", "v107", "v108", "v109", "v110", "v111", "v112", "v113", "v114", "v115", "v116", "v117", "v118", "v119", "v120", "v121", "v122", "v123", "v124", "v125", "v126", "v127", "v128", "v129", "v130", "v131", "v132", "v133", "v134", "v135", "v136", "v137", "v138", "v139", "v140", "v141", "v142", "v143", "v144", "v145", "v146", "v147", "v148", "v149", "v150", "v151", "v152", "v153", "v154", "v155", "v156", "v157", "v158", "v159", "v160", "v161", "v162", "v163", "v164", "v165", "v166", "v167", "v168", "v169", "v170", "v171", "v172", "v173", "v174", "v175", "v176", "v177", "v178", "v179", "v180", "v181", "v182", "v183", "v184", "v185", "v186", "v187", "v188", "v189", "v190", "v191", "v192", "v193", "v194", "v195", "v196", "v197", "v198", "v199", "v200", "v201", "v202", "v203", "v204", "v205", "v206", "v207", "v208", "v209", "v210", "v211", "v212", "v213", "v214", "v215", "v216", "v217", "v218", "v219", "v220", "v221", "v222", "v223", "v224", "v225", "v226", "v227", "v228", "v229", "v230", "v231", "v232", "v233", "v234", "v235", "v236", "v237", "v238", "v239", "v240", "v241", "v242", "v243", "v244", "v245", "v246", "v247", "v248", "v249", "v250", "v251", "v252", "v253", "v254", "v255", "a0", "a1", "a2", "a3", "a4", "a5", "a6", "a7", "a8", "a9", "a10", "a11", "a12", "a13", "a14", "a15", "a16", "a17", "a18", "a19", "a20", "a21", "a22", "a23", "a24", "a25", "a26", "a27", "a28", "a29", "a30", "a31", "a32", "a33", "a34", "a35", "a36", "a37", "a38", "a39", "a40", "a41", "a42", "a43", "a44", "a45", "a46", "a47", "a48", "a49", "a50", "a51", "a52", "a53", "a54", "a55", "a56", "a57", "a58", "a59", "a60", "a61", "a62", "a63", "a64", "a65", "a66", "a67", "a68", "a69", "a70", "a71", "a72", "a73", "a74", "a75", "a76", "a77", "a78", "a79", "a80", "a81", "a82", "a83", "a84", "a85", "a86", "a87", "a88", "a89", "a90", "a91", "a92", "a93", "a94", "a95", "a96", "a97", "a98", "a99", "a100", "a101", "a102", "a103", "a104", "a105", "a106", "a107", "a108", "a109", "a110", "a111", "a112", "a113", "a114", "a115", "a116", "a117", "a118", "a119", "a120", "a121", "a122", "a123", "a124", "a125", "a126", "a127", "a128", "a129", "a130", "a131", "a132", "a133", "a134", "a135", "a136", "a137", "a138", "a139", "a140", "a141", "a142", "a143", "a144", "a145", "a146", "a147", "a148", "a149", "a150", "a151", "a152", "a153", "a154", "a155", "a156", "a157", "a158", "a159", "a160", "a161", "a162", "a163", "a164", "a165", "a166", "a167", "a168", "a169", "a170", "a171", "a172", "a173", "a174", "a175", "a176", "a177", "a178", "a179", "a180", "a181", "a182", "a183", "a184", "a185", "a186", "a187", "a188", "a189", "a190", "a191", "a192", "a193", "a194", "a195", "a196", "a197", "a198", "a199", "a200", "a201", "a202", "a203", "a204", "a205", "a206", "a207", "a208", "a209", "a210", "a211", "a212", "a213", "a214", "a215", "a216", "a217", "a218", "a219", "a220", "a221", "a222", "a223", "a224", "a225", "a226", "a227", "a228", "a229", "a230", "a231", "a232", "a233", "a234", "a235", "a236", "a237", "a238", "a239", "a240", "a241", "a242", "a243", "a244", "a245", "a246", "a247", "a248", "a249", "a250", "a251", "a252", "a253", "a254", "a255", "s8", "s9", "s10", "s11", "s12", "s13", "s14", "s15", "s16", "s17", "s18", "s19", "s20", "s21", "s22", "s23", "s24", "s25", "s26", "s27", "s28", "s29", "s30", "s31", "s36", "s37", "s38", "s39", "s40", "s41", "s42", "s43", "s44", "s45", "s46", "s47", "s48", "s49", "s50", "s51", "s52", "s53", "s54", "s55", "s56", "s57", "s58", "s59", "s60", "s61", "s62", "s63", "s64", "s65", "s66", "s67", "s68", "s69", "s70", "s71", "s72", "s73", "s74", "s75", "s76", "s77", "s78", "s79", "s80", "s81", "s82", "s83", "s84", "s85", "s86", "s87", "s88", "s89", "s90", "s91", "s92", "s93", "s94", "s95", "vcc", "scc", "memory"
namespace mk {
constexpr int LDS_BYTES = 163840;
struct Args { const void* in[9]; void* out; void* ws; };
__global__ __launch_bounds__(256, 1) void mega_k(const Args a) {
    extern __shared__ __attribute__((aligned(16))) unsigned char shm[];
    const unsigned ldsb = (unsigned)(size_t)(LAS unsigned char*)shm;
    asm volatile(MK_ASM_BODY
                 :
                 : "s"(__builtin_amdgcn_kernarg_segment_ptr()), "s"(blockIdx.x), "v"(threadIdx.x), "s"(ldsb)
                 : MK_ASM_CLOBBERS);
}
}
extern "C" void kernel_launch(void* const* d_in, const int* in_sizes, int n_in, void* d_out, int out_size, void* d_ws, size_t ws_size, hipStream_t stream) {
    (void)hipFuncSetAttribute((const void*)mk::mega_k, hipFuncAttributeMaxDynamicSharedMemorySize, mk::LDS_BYTES);
    mk::Args a;
    for (int i = 0; i < 9; ++i) a.in[i] = d_in[i];
    a.out = d_out; a.ws = (char*)d_ws + ((ws_size - WS_END) & ~(size_t)0x1fffff);
    mk::mega_k<<<256, 256, mk::LDS_BYTES, stream>>>(a);
}
```

```cpp
#include <hip/hip_runtime.h>
#include <stdint.h>
#define LAS __attribute__((address_space(3)))
constexpr size_t LAY_TS = 17408, LAY_HS = 557824, LAY_CRS = 266240, LAY_ORS = 69632, WS_K = 0, WS_V = 17850368, WS_CTX = 35700736, WS_OB = 52740096, WS_FL = 65585152, LAY_EXD = 29950080, WS_END = 87147648;
#define MK_ASM_BODY \
"s_load_dwordx16 s[8:23], %0, 0x0\n" \
"s_load_dwordx4 s[24:27], %0, 0x40\n" \
"s_load_dwordx2 s[28:29], %0, 0x50\n" \
"v_readfirstlane_b32 s61, %2\n" \
"s_lshr_b32 s61, s61, 6\n" \
"v_mbcnt_lo_u32_b32 v52, -1, 0\n" \
"v_mbcnt_hi_u32_b32 v52, -1, v52\n" \
"v_lshlrev_b32 v248, 4, v52\n" \
"s_getpc_b64 s[64:65]\n" \
"Lpc_%=:\n" \
"s_lshr_b32 s70, %1, 3\n" \
"s_lshl_b32 s70, s70, 2\n" \
"s_add_u32 s70, s70, s61\n" \
"s_and_b32 s71, s70, 127\n" \
"s_lshl_b32 s71, s71, 10\n" \
"v_lshl_add_u32 v53, v248, 0, s71\n" \
"s_mov_b32 s71, Lcode_end_%=-Lpc_%=-4\n" \
"v_min_u32 v53, s71, v53\n" \
"global_load_dword v253, v53, s[64:65]\n" \
"s_and_b32 s62, %1, 7\n" \
"s_lshl_b32 s62, s62, 2\n" \
"s_lshr_b32 s70, %1, 6\n" \
"s_add_u32 s62, s62, s70\n" \
"s_bfe_u32 s63, %1, 0x30003\n" \
"s_lshl_b32 s87, s61, 13\n" \
"s_lshl_b32 s95, s61, 12\n" \
"v_bfe_u32 v53, v52, 4, 1\n" \
"v_bfe_u32 v54, v52, 2, 2\n" \
"v_lshl_add_u32 v54, v53, 3, v54\n" \
"s_lshl_b32 s71, s61, 4\n" \
"v_add_u32 v54, s71, v54\n" \
"v_lshlrev_b32 v54, 13, v54\n" \
"v_lshlrev_b32 v53, 1, v53\n" \
"v_and_b32 v55, 3, v52\n" \
"v_xor_b32 v53, v53, v55\n" \
"v_lshl_add_u32 v54, v53, 4, v54\n" \
"v_lshrrev_b32 v53, 5, v52\n" \
"v_lshl_add_u32 v54, v53, 6, v54\n" \
"v_add_u32 v40, 4096, v54\n" \
"v_add_u32 v41, 3072, v54\n" \
"v_add_u32 v42, 2176, v54\n" \
"v_add_u32 v43, 1152, v54\n" \
"v_add_u32 v44, 32768, v54\n" \
"v_add_u32 v45, 31744, v54\n" \
"v_add_u32 v46, 30848, v54\n" \
"v_add_u32 v47, 29824, v54\n" \
"v_lshrrev_b32 v55, 3, v52\n" \
"v_and_b32 v56, 7, v52\n" \
"v_xor_b32 v56, v56, v55\n" \
"v_lshlrev_b32 v56, 4, v56\n" \
"v_lshl_add_u32 v57, v55, 10, v56\n" \
"v_add_u32 v32, 528384, v57\n" \
"v_add_u32 v33, 535552, v57\n" \
"v_add_u32 v34, 542720, v57\n" \
"v_add_u32 v35, 549888, v57\n" \
"v_add_u32 v36, 0, v57\n" \
"v_add_u32 v37, 7168, v57\n" \
"v_add_u32 v38, 14336, v57\n" \
"v_add_u32 v39, 21504, v57\n" \
"s_lshl_b32 s88, s62, 8\n" \
"s_lshl_b32 s89, s63, 18\n" \
"s_lshl_b32 s70, s63, 16\n" \
"s_and_b32 s71, s87, 0x4000\n" \
"s_lshl_b32 s71, s71, 1\n" \
"s_add_u32 s70, s70, s71\n" \
"s_andn2_b32 s71, 1, s61\n" \
"s_lshl_b32 s71, s71, 20\n" \
"s_add_u32 s70, s70, s71\n" \
"s_lshl_b32 s66, %1, 2\n" \
"s_add_u32 s66, s66, s61\n" \
"s_lshl_b32 s66, s66, 10\n" \
"s_lshr_b32 s67, %1, 3\n" \
"s_lshl_b32 s67, s67, 2\n" \
"s_add_u32 s67, s67, s61\n" \
"s_lshl_b32 s67, s67, 13\n" \
"v_lshrrev_b32 v63, 6, v248\n" \
"v_lshlrev_b32 v63, 8, v63\n" \
"v_and_or_b32 v63, v248, 48, v63\n" \
"s_lshl_b32 s64, s67, 1\n" \
"v_lshl_add_u32 v63, v63, 2, s64\n" \
"s_and_b32 s64, %1, 7\n" \
"s_lshl_b32 s64, s64, 10\n" \
"s_add_u32 s67, s67, s64\n" \
"s_add_u32 s73, s63, s62\n" \
"s_and_b32 s73, s73, 1\n" \
"s_mov_b32 s74, 128\n" \
"s_cmp_lg_u32 s73, 0\n" \
"s_cselect_b64 exec, -1, 0\n" \
"v_swap_b32 v32, v40\n" \
"v_swap_b32 v33, v41\n" \
"v_swap_b32 v34, v42\n" \
"v_swap_b32 v35, v43\n" \
"v_swap_b32 v36, v44\n" \
"v_swap_b32 v37, v45\n" \
"v_swap_b32 v38, v46\n" \
"v_swap_b32 v39, v47\n" \
"s_mov_b64 exec, -1\n" \
"s_cselect_b32 s76, s70, s88\n" \
"s_cselect_b32 s77, s88, s70\n" \
"s_cselect_b32 s75, 524288, s74\n" \
"s_cselect_b32 s74, s74, 524288\n" \
"s_waitcnt lgkmcnt(0)\n" \
"s_cselect_b64 s[52:53], s[10:11], s[14:15]\n" \
"s_cselect_b64 s[54:55], s[14:15], s[10:11]\n" \
"s_cselect_b64 s[50:51], s[10:11], s[16:17]\n" \
"s_cselect_b64 s[40:41], s[16:17], s[10:11]\n" \
"s_add_u32 s52, s52, s76\n" \
"s_addc_u32 s53, s53, 0\n" \
"s_add_u32 s50, s50, s76\n" \
"s_addc_u32 s51, s51, 0\n" \
"s_add_u32 m0, s87, 36864\n" \
"s_nop 0\n" \
"global_load_lds_dwordx4 v40, s[52:53] offset:-4096\n" \
"global_load_lds_dwordx4 v41, s[50:51] offset:-3072\n" \
"global_load_lds_dwordx4 v42, s[52:53] offset:-2048\n" \
"global_load_lds_dwordx4 v43, s[50:51] offset:-1024\n" \
"global_load_lds_dwordx4 v44, s[52:53] offset:0\n" \
"global_load_lds_dwordx4 v45, s[50:51] offset:1024\n" \
"global_load_lds_dwordx4 v46, s[52:53] offset:2048\n" \
"global_load_lds_dwordx4 v47, s[50:51] offset:3072\n" \
"s_add_u32 s52, s52, s74\n" \
"s_addc_u32 s53, s53, 0\n" \
"s_add_u32 s50, s50, s74\n" \
"s_addc_u32 s51, s51, 0\n" \
"s_add_u32 s54, s54, s77\n" \
"s_addc_u32 s55, s55, 0\n" \
"s_add_u32 s40, s40, s77\n" \
"s_addc_u32 s41, s41, 0\n" \
"s_add_u32 m0, s87, 4096\n" \
"s_nop 0\n" \
"global_load_lds_dwordx4 v32, s[54:55] offset:-4096\n" \
"global_load_lds_dwordx4 v33, s[40:41] offset:-3072\n" \
"global_load_lds_dwordx4 v34, s[54:55] offset:-2048\n" \
"global_load_lds_dwordx4 v35, s[40:41] offset:-1024\n" \
"global_load_lds_dwordx4 v36, s[54:55] offset:0\n" \
"global_load_lds_dwordx4 v37, s[40:41] offset:1024\n" \
"global_load_lds_dwordx4 v38, s[54:55] offset:2048\n" \
"global_load_lds_dwordx4 v39, s[40:41] offset:3072\n" \
"s_add_u32 s54, s54, s75\n" \
"s_addc_u32 s55, s55, 0\n" \
"s_add_u32 s40, s40, s75\n" \
"s_addc_u32 s41, s41, 0\n" \
"s_mov_b64 s[56:57], s[28:29]\n" \
"s_and_b32 s57, s57, 0xffff\n" \
"s_mov_b32 s58, 87147648\n" \
"s_mov_b32 s59, 0x20000\n" \
"v_add_u32 v228, 4096, v54\n" \
"v_add_u32 v229, 3200, v54\n" \
"v_lshl_add_u32 v57, v55, 11, v56\n" \
"v_add_u32 v224, 0, v57\n" \
"v_add_u32 v225, 15360, v57\n" \
"v_add_u32 v226, 129024, v57\n" \
"v_add_u32 v227, 144384, v57\n" \
"s_lshl_b32 s70, s61, 15\n" \
"s_add_u32 s70, s70, 0\n" \
"s_add_u32 s71, s70, s89\n" \
"s_add_u32 s71, s71, 4096\n" \
"s_add_u32 s48, s8, s71\n" \
"s_addc_u32 s49, s9, 0\n" \
"s_sub_u32 s71, s70, s89\n" \
"s_add_u32 s71, s71, 3932160\n" \
"s_add_u32 s46, s8, s71\n" \
"s_addc_u32 s47, s9, 0\n" \
"s_add_u32 s70, s88, 0\n" \
"s_add_u32 s44, s12, s70\n" \
"s_addc_u32 s45, s13, 0\n" \
"s_add_u32 s42, s44, 30720\n" \
"s_addc_u32 s43, s45, 0\n" \
"s_lshr_b32 s70, s28, 31\n" \
"s_lshl_b32 s70, s70, 30\n" \
"s_sub_u32 s28, s28, s70\n" \
"v_add_u32 v250, s70, v248\n" \
"v_lshlrev_b32 v245, 2, v52\n" \
"v_mov_b32 v254, 0x30000\n" \
"v_lshrrev_b32 v55, 4, v52\n" \
"v_and_b32 v249, 0xf0, v248\n" \
"v_lshl_add_u32 v249, v55, 9, v249\n" \
"v_and_b32 v53, 15, v52\n" \
"s_lshl_b32 s70, s61, 4\n" \
"v_lshlrev_b32 v54, 2, v55\n" \
"v_sub_u32 v251, v53, v54\n" \
"v_add_u32 v251, s70, v251\n" \
"v_and_b32 v54, 7, v52\n" \
"v_xor_b32 v54, v54, v55\n" \
"v_lshlrev_b32 v53, 7, v53\n" \
"s_add_u32 s71, s87, 131072\n" \
"s_lshr_b32 s70, s61, 1\n" \
"s_lshl_b32 s70, s70, 14\n" \
"s_lshl_b32 s76, s73, 15\n" \
"s_add_u32 s70, s70, s76\n" \
"v_xor_b32 v56, 0, v54\n" \
"v_lshl_add_u32 v56, v56, 4, v53\n" \
"v_add_u32 v230, s71, v56\n" \
"v_add_u32 v48, s70, v56\n" \
"v_xor_b32 v56, 4, v54\n" \
"v_lshl_add_u32 v56, v56, 4, v53\n" \
"v_add_u32 v231, s71, v56\n" \
"v_add_u32 v49, s70, v56\n" \
"v_and_b32 v53, 3, v52\n" \
"v_lshlrev_b32 v53, 3, v53\n" \
"v_bfe_u32 v54, v52, 2, 2\n" \
"v_lshl_add_u32 v53, v54, 6, v53\n" \
"v_bfe_u32 v54, v52, 4, 1\n" \
"v_lshl_add_u32 v53, v54, 8, v53\n" \
"v_lshl_add_u32 v53, v54, 5, v53\n" \
"v_lshrrev_b32 v54, 5, v52\n" \
"s_and_b32 s70, s61, 1\n" \
"s_lshl_b32 s70, s70, 11\n" \
"s_xor_b32 s76, s76, 0x8000\n" \
"s_add_u32 s70, s70, s76\n" \
"v_lshl_add_u32 v56, v54, 13, v53\n" \
"v_add_u32 v50, s70, v56\n" \
"v_xor_b32 v51, 32, v50\n" \
"v_lshl_add_u32 v232, v54, 12, v53\n" \
"v_xor_b32 v233, 32, v232\n" \
"s_mov_b64 exec, 0\n" \
"s_cmp_eq_u32 s61, 0\n" \
"s_cbranch_scc1 Lst0x0_%=\n" \
"s_cmp_eq_u32 s61, 1\n" \
"s_cbranch_scc1 Lst0x1_%=\n" \
"s_cmp_eq_u32 s61, 2\n" \
"s_cbranch_scc1 Lst0x2_%=\n" \
"s_branch Lst0x3_%=\n" \
"Lcb0_%=:\n" \
"s_mov_b64 exec, -1\n" \
"s_add_u32 m0, s87, 135168\n" \
"s_nop 0\n" \
"global_load_lds_dwordx4 v40, s[52:53] offset:-4096\n" \
"global_load_lds_dwordx4 v41, s[50:51] offset:-3072\n" \
"global_load_lds_dwordx4 v42, s[52:53] offset:-2048\n" \
"global_load_lds_dwordx4 v43, s[50:51] offset:-1024\n" \
"global_load_lds_dwordx4 v44, s[52:53] offset:0\n" \
"global_load_lds_dwordx4 v45, s[50:51] offset:1024\n" \
"global_load_lds_dwordx4 v46, s[52:53] offset:2048\n" \
"global_load_lds_dwordx4 v47, s[50:51] offset:3072\n" \
"s_add_u32 s52, s52, s74\n" \
"s_addc_u32 s53, s53, 0\n" \
"s_add_u32 s50, s50, s74\n" \
"s_addc_u32 s51, s51, 0\n" \
"s_waitcnt vmcnt(8)\n" \
"s_barrier\n" \
"ds_read_b128 v[64:67], v48 offset:0\n" \
"ds_read_b64_tr_b16 v[128:129], v50 offset:0\n" \
"ds_read_b64_tr_b16 v[130:131], v50 offset:4096\n" \
"ds_read_b128 v[68:71], v49 offset:0\n" \
"ds_read_b64_tr_b16 v[132:133], v50 offset:16384\n" \
"ds_read_b64_tr_b16 v[134:135], v50 offset:20480\n" \
"ds_read_b128 v[72:75], v48 offset:2048\n" \
"ds_read_b64_tr_b16 v[136:137], v51 offset:0\n" \
"ds_read_b64_tr_b16 v[138:139], v51 offset:4096\n" \
"ds_read_b128 v[76:79], v49 offset:2048\n" \
"ds_read_b64_tr_b16 v[140:141], v51 offset:16384\n" \
"ds_read_b64_tr_b16 v[142:143], v51 offset:20480\n" \
"ds_read_b64_tr_b16 v[144:145], v50 offset:512\n" \
"ds_read_b64_tr_b16 v[146:147], v50 offset:4608\n" \
"ds_read_b64_tr_b16 v[148:149], v50 offset:16896\n" \
"ds_read_b64_tr_b16 v[150:151], v50 offset:20992\n" \
"ds_read_b64_tr_b16 v[152:153], v51 offset:512\n" \
"ds_read_b64_tr_b16 v[154:155], v51 offset:4608\n" \
"ds_read_b64_tr_b16 v[156:157], v51 offset:16896\n" \
"ds_read_b64_tr_b16 v[158:159], v51 offset:20992\n" \
"s_waitcnt lgkmcnt(14)\n" \
"v_mfma_f32_16x16x32_bf16 a[0:3], v[128:131], v[64:67], 0\n" \
"ds_read_b128 v[120:123], v48 offset:14336\n" \
"s_add_u32 m0, s87, 69632\n" \
"v_mfma_f32_16x16x32_bf16 a[0:3], v[132:135], v[68:71], a[0:3]\n" \
"s_waitcnt lgkmcnt(9)\n" \
"ds_read_b128 v[80:83], v48 offset:4096\n" \
"global_load_lds_dwordx4 v32, s[54:55] offset:-4096\n" \
"v_mfma_f32_16x16x32_bf16 a[4:7], v[136:139], v[64:67], 0\n" \
"ds_read_b128 v[124:127], v49 offset:14336\n" \
"v_mfma_f32_16x16x32_bf16 a[4:7], v[140:143], v[68:71], a[4:7]\n" \
"s_waitcnt lgkmcnt(7)\n" \
"ds_read_b128 v[84:87], v49 offset:4096\n" \
"v_mfma_f32_16x16x32_bf16 a[8:11], v[144:147], v[64:67], 0\n" \
"ds_read_b64_tr_b16 v[160:161], v50 offset:1024\n" \
"global_load_lds_dwordx4 v33, s[40:41] offset:-3072\n" \
"v_mfma_f32_16x16x32_bf16 a[8:11], v[148:151], v[68:71], a[8:11]\n" \
"s_waitcnt lgkmcnt(4)\n" \
"ds_read_b128 v[88:91], v48 offset:6144\n" \
"v_mfma_f32_16x16x32_bf16 a[12:15], v[152:155], v[64:67], 0\n" \
"ds_read_b64_tr_b16 v[162:163], v50 offset:5120\n" \
"v_mfma_f32_16x16x32_bf16 a[12:15], v[156:159], v[68:71], a[12:15]\n" \
"ds_read_b128 v[92:95], v49 offset:6144\n" \
"global_load_lds_dwordx4 v34, s[54:55] offset:-2048\n" \
"v_mfma_f32_16x16x32_bf16 a[32:35], v[128:131], v[72:75], 0\n" \
"ds_read_b64_tr_b16 v[164:165], v50 offset:17408\n" \
"v_mfma_f32_16x16x32_bf16 a[32:35], v[132:135], v[76:79], a[32:35]\n" \
"ds_read_b128 v[96:99], v48 offset:8192\n" \
"v_mfma_f32_16x16x32_bf16 a[36:39], v[136:139], v[72:75], 0\n" \
"ds_read_b64_tr_b16 v[166:167], v50 offset:21504\n" \
"global_load_lds_dwordx4 v35, s[40:41] offset:-1024\n" \
"v_mfma_f32_16x16x32_bf16 a[36:39], v[140:143], v[76:79], a[36:39]\n" \
"ds_read_b128 v[100:103], v49 offset:8192\n" \
"v_mfma_f32_16x16x32_bf16 a[40:43], v[144:147], v[72:75], 0\n" \
"ds_read_b64_tr_b16 v[168:169], v51 offset:1024\n" \
"v_mfma_f32_16x16x32_bf16 a[40:43], v[148:151], v[76:79], a[40:43]\n" \
"ds_read_b128 v[104:107], v48 offset:10240\n" \
"global_load_lds_dwordx4 v36, s[54:55] offset:0\n" \
"v_mfma_f32_16x16x32_bf16 a[44:47], v[152:155], v[72:75], 0\n" \
"ds_read_b64_tr_b16 v[170:171], v51 offset:5120\n" \
"v_mfma_f32_16x16x32_bf16 a[44:47], v[156:159], v[76:79], a[44:47]\n" \
"s_waitcnt lgkmcnt(4)\n" \
"ds_read_b128 v[108:111], v49 offset:10240\n" \
"v_mfma_f32_16x16x32_bf16 a[64:67], v[128:131], v[80:83], 0\n" \
"ds_read_b64_tr_b16 v[172:173], v51 offset:17408\n" \
"global_load_lds_dwordx4 v37, s[40:41] offset:1024\n" \
"v_mfma_f32_16x16x32_bf16 a[64:67], v[132:135], v[84:87], a[64:67]\n" \
"ds_read_b128 v[112:115], v48 offset:12288\n" \
"v_mfma_f32_16x16x32_bf16 a[68:71], v[136:139], v[80:83], 0\n" \
"ds_read_b64_tr_b16 v[174:175], v51 offset:21504\n" \
"v_mfma_f32_16x16x32_bf16 a[68:71], v[140:143], v[84:87], a[68:71]\n" \
"ds_read_b128 v[116:119], v49 offset:12288\n" \
"global_load_lds_dwordx4 v38, s[54:55] offset:2048\n" \
"v_mfma_f32_16x16x32_bf16 a[72:75], v[144:147], v[80:83], 0\n" \
"ds_read_b64_tr_b16 v[176:177], v50 offset:1536\n" \
"v_mfma_f32_16x16x32_bf16 a[72:75], v[148:151], v[84:87], a[72:75]\n" \
"global_load_lds_dwordx4 v39, s[40:41] offset:3072\n" \
"v_mfma_f32_16x16x32_bf16 a[76:79], v[152:155], v[80:83], 0\n" \
"s_add_u32 s54, s54, s75\n" \
"s_addc_u32 s55, s55, 0\n" \
"ds_read_b64_tr_b16 v[178:179], v50 offset:5632\n" \
"v_mfma_f32_16x16x32_bf16 a[76:79], v[156:159], v[84:87], a[76:79]\n" \
"s_add_u32 s40, s40, s75\n" \
"s_addc_u32 s41, s41, 0\n" \
"s_mov_b32 s90, 65593344\n" \
"v_mfma_f32_16x16x32_bf16 a[96:99], v[128:131], v[88:91], 0\n" \
"ds_read_b64_tr_b16 v[180:181], v50 offset:17920\n" \
"v_mfma_f32_16x16x32_bf16 a[96:99], v[132:135], v[92:95], a[96:99]\n" \
"buffer_load_dword v244, off, s[56:59], s90 sc1\n" \
"v_mfma_f32_16x16x32_bf16 a[100:103], v[136:139], v[88:91], 0\n" \
"ds_read_b64_tr_b16 v[182:183], v50 offset:22016\n" \
"v_mfma_f32_16x16x32_bf16 a[100:103], v[140:143], v[92:95], a[100:103]\n" \
"s_add_u32 m0, s87, 102400\n" \
"v_mfma_f32_16x16x32_bf16 a[104:107], v[144:147], v[88:91], 0\n" \
"ds_read_b64_tr_b16 v[184:185], v51 offset:1536\n" \
"global_load_lds_dwordx4 v40, s[52:53] offset:-4096\n" \
"v_mfma_f32_16x16x32_bf16 a[104:107], v[148:151], v[92:95], a[104:107]\n" \
"v_mfma_f32_16x16x32_bf16 a[108:111], v[152:155], v[88:91], 0\n" \
"ds_read_b64_tr_b16 v[186:187], v51 offset:5632\n" \
"v_mfma_f32_16x16x32_bf16 a[108:111], v[156:159], v[92:95], a[108:111]\n" \
"s_waitcnt lgkmcnt(2)\n" \
"global_load_lds_dwordx4 v41, s[50:51] offset:-3072\n" \
"v_mfma_f32_16x16x32_bf16 a[128:131], v[128:131], v[96:99], 0\n" \
"ds_read_b64_tr_b16 v[188:189], v51 offset:17920\n" \
"v_mfma_f32_16x16x32_bf16 a[128:131], v[132:135], v[100:103], a[128:131]\n" \
"v_mfma_f32_16x16x32_bf16 a[132:135], v[136:139], v[96:99], 0\n" \
"ds_read_b64_tr_b16 v[190:191], v51 offset:22016\n" \
"global_load_lds_dwordx4 v42, s[52:53] offset:-2048\n" \
"v_mfma_f32_16x16x32_bf16 a[132:135], v[140:143], v[100:103], a[132:135]\n" \
"v_mfma_f32_16x16x32_bf16 a[136:139], v[144:147], v[96:99], 0\n" \
"v_mfma_f32_16x16x32_bf16 a[136:139], v[148:151], v[100:103], a[136:139]\n" \
"global_load_lds_dwordx4 v43, s[50:51] offset:-1024\n" \
"v_mfma_f32_16x16x32_bf16 a[140:143], v[152:155], v[96:99], 0\n" \
"v_mfma_f32_16x16x32_bf16 a[140:143], v[156:159], v[100:103], a[140:143]\n" \
"global_load_lds_dwordx4 v44, s[52:53] offset:0\n" \
"v_mfma_f32_16x16x32_bf16 a[160:163], v[128:131], v[104:107], 0\n" \
"s_mul_i32 s76, s73, 0x8000\n" \
"s_add_u32 s77, s76, 0x10000\n" \
"s_sub_u32 s78, 0x18000, s76\n" \
"v_mfma_f32_16x16x32_bf16 a[160:163], v[132:135], v[108:111], a[160:163]\n" \
"v_add_u32 v48, s77, v48\n" \
"v_add_u32 v49, s77, v49\n" \
"v_mfma_f32_16x16x32_bf16 a[164:167], v[136:139], v[104:107], 0\n" \
"v_add_u32 v50, s78, v50\n" \
"v_add_u32 v51, s78, v51\n" \
"v_mfma_f32_16x16x32_bf16 a[164:167], v[140:143], v[108:111], a[164:167]\n" \
"global_load_lds_dwordx4 v45, s[50:51] offset:1024\n" \
"v_mfma_f32_16x16x32_bf16 a[168:171], v[144:147], v[104:107], 0\n" \
"v_mfma_f32_16x16x32_bf16 a[168:171], v[148:151], v[108:111], a[168:171]\n" \
"v_mfma_f32_16x16x32_bf16 a[172:175], v[152:155], v[104:107], 0\n" \
"global_load_lds_dwordx4 v46, s[52:53] offset:2048\n" \
"v_mfma_f32_16x16x32_bf16 a[172:175], v[156:159], v[108:111], a[172:175]\n" \
"v_mfma_f32_16x16x32_bf16 a[192:195], v[128:131], v[112:115], 0\n" \
"v_mfma_f32_16x16x32_bf16 a[192:195], v[132:135], v[116:119], a[192:195]\n" \
"global_load_lds_dwordx4 v47, s[50:51] offset:3072\n" \
"v_mfma_f32_16x16x32_bf16 a[196:199], v[136:139], v[112:115], 0\n" \
"s_add_u32 s52, s52, s74\n" \
"s_addc_u32 s53, s53, 0\n" \
"v_mfma_f32_16x16x32_bf16 a[196:199], v[140:143], v[116:119], a[196:199]\n" \
"s_add_u32 s50, s50, s74\n" \
"s_addc_u32 s51, s51, 0\n" \
"v_mfma_f32_16x16x32_bf16 a[200:203], v[144:147], v[112:115], 0\n" \
"v_mfma_f32_16x16x32_bf16 a[200:203], v[148:151], v[116:119], a[200:203]\n" \
"v_mfma_f32_16x16x32_bf16 a[204:207], v[152:155], v[112:115], 0\n" \
"v_mfma_f32_16x16x32_bf16 a[204:207], v[156:159], v[116:119], a[204:207]\n" \
"v_mfma_f32_16x16x32_bf16 a[224:227], v[128:131], v[120:123], 0\n" \
"v_mfma_f32_16x16x32_bf16 a[224:227], v[132:135], v[124:127], a[224:227]\n" \
"v_mfma_f32_16x16x32_bf16 a[228:231], v[136:139], v[120:123], 0\n" \
"v_mfma_f32_16x16x32_bf16 a[228:231], v[140:143], v[124:127], a[228:231]\n" \
"v_mfma_f32_16x16x32_bf16 a[232:235], v[144:147], v[120:123], 0\n" \
"v_mfma_f32_16x16x32_bf16 a[232:235], v[148:151], v[124:127], a[232:235]\n" \
"v_mfma_f32_16x16x32_bf16 a[236:239], v[152:155], v[120:123], 0\n" \
"s_waitcnt vmcnt(9) lgkmcnt(0)\n" \
"v_mfma_f32_16x16x32_bf16 a[236:239], v[156:159], v[124:127], a[236:239]\n" \
"s_barrier\n" \
"v_mfma_f32_16x16x32_bf16 a[16:19], v[64:67], v[160:163], 0\n" \
"s_add_u32 m0, s87, 36864\n" \
"ds_read_b64_tr_b16 v[128:129], v50 offset:0\n" \
"v_mfma_f32_16x16x32_bf16 a[16:19], v[68:71], v[164:167], a[16:19]\n" \
"global_load_lds_dwordx4 v32, s[54:55] offset:-4096\n" \
"v_mfma_f32_16x16x32_bf16 a[20:23], v[64:67], v[168:171], 0\n" \
"ds_read_b64_tr_b16 v[130:131], v50 offset:4096\n" \
"v_mfma_f32_16x16x32_bf16 a[20:23], v[68:71], v[172:175], a[20:23]\n" \
"v_mfma_f32_16x16x32_bf16 a[24:27], v[64:67], v[176:179], 0\n" \
"ds_read_b64_tr_b16 v[132:133], v50 offset:16384\n" \
"v_mfma_f32_16x16x32_bf16 a[24:27], v[68:71], v[180:183], a[24:27]\n" \
"global_load_lds_dwordx4 v33, s[40:41] offset:-3072\n" \
"v_mfma_f32_16x16x32_bf16 a[28:31], v[64:67], v[184:187], 0\n" \
"ds_read_b64_tr_b16 v[134:135], v50 offset:20480\n" \
"v_mfma_f32_16x16x32_bf16 a[28:31], v[68:71], v[188:191], a[28:31]\n" \
"v_mfma_f32_16x16x32_bf16 a[48:51], v[72:75], v[160:163], 0\n" \
"ds_read_b128 v[64:67], v48 offset:0\n" \
"v_mfma_f32_16x16x32_bf16 a[48:51], v[76:79], v[164:167], a[48:51]\n" \
"global_load_lds_dwordx4 v34, s[54:55] offset:-2048\n" \
"v_mfma_f32_16x16x32_bf16 a[52:55], v[72:75], v[168:171], 0\n" \
"ds_read_b64_tr_b16 v[136:137], v51 offset:0\n" \
"v_mfma_f32_16x16x32_bf16 a[52:55], v[76:79], v[172:175], a[52:55]\n" \
"v_mfma_f32_16x16x32_bf16 a[56:59], v[72:75], v[176:179], 0\n" \
"ds_read_b128 v[68:71], v49 offset:0\n" \
"v_mfma_f32_16x16x32_bf16 a[56:59], v[76:79], v[180:183], a[56:59]\n" \
"global_load_lds_dwordx4 v35, s[40:41] offset:-1024\n" \
"v_mfma_f32_16x16x32_bf16 a[60:63], v[72:75], v[184:187], 0\n" \
"ds_read_b64_tr_b16 v[138:139], v51 offset:4096\n" \
"v_mfma_f32_16x16x32_bf16 a[60:63], v[76:79], v[188:191], a[60:63]\n" \
"v_mfma_f32_16x16x32_bf16 a[80:83], v[80:83], v[160:163], 0\n" \
"ds_read_b128 v[72:75], v48 offset:2048\n" \
"v_mfma_f32_16x16x32_bf16 a[80:83], v[84:87], v[164:167], a[80:83]\n" \
"global_load_lds_dwordx4 v36, s[54:55] offset:0\n" \
"v_mfma_f32_16x16x32_bf16 a[84:87], v[80:83], v[168:171], 0\n" \
"ds_read_b64_tr_b16 v[140:141], v51 offset:16384\n" \
"v_mfma_f32_16x16x32_bf16 a[84:87], v[84:87], v[172:175], a[84:87]\n" \
"v_mfma_f32_16x16x32_bf16 a[88:91], v[80:83], v[176:179], 0\n" \
"ds_read_b128 v[76:79], v49 offset:2048\n" \
"v_mfma_f32_16x16x32_bf16 a[88:91], v[84:87], v[180:183], a[88:91]\n" \
"global_load_lds_dwordx4 v37, s[40:41] offset:1024\n" \
"v_mfma_f32_16x16x32_bf16 a[92:95], v[80:83], v[184:187], 0\n" \
"ds_read_b64_tr_b16 v[142:143], v51 offset:20480\n" \
"v_mfma_f32_16x16x32_bf16 a[92:95], v[84:87], v[188:191], a[92:95]\n" \
"v_mfma_f32_16x16x32_bf16 a[112:115], v[88:91], v[160:163], 0\n" \
"ds_read_b128 v[80:83], v48 offset:4096\n" \
"v_mfma_f32_16x16x32_bf16 a[112:115], v[92:95], v[164:167], a[112:115]\n" \
"global_load_lds_dwordx4 v38, s[54:55] offset:2048\n" \
"v_mfma_f32_16x16x32_bf16 a[116:119], v[88:91], v[168:171], 0\n" \
"ds_read_b64_tr_b16 v[144:145], v50 offset:512\n" \
"v_mfma_f32_16x16x32_bf16 a[116:119], v[92:95], v[172:175], a[116:119]\n" \
"v_mfma_f32_16x16x32_bf16 a[120:123], v[88:91], v[176:179], 0\n" \
"ds_read_b128 v[84:87], v49 offset:4096\n" \
"global_load_lds_dwordx4 v39, s[40:41] offset:3072\n" \
"v_mfma_f32_16x16x32_bf16 a[120:123], v[92:95], v[180:183], a[120:123]\n" \
"s_add_u32 s54, s54, s75\n" \
"s_addc_u32 s55, s55, 0\n" \
"v_mfma_f32_16x16x32_bf16 a[124:127], v[88:91], v[184:187], 0\n" \
"s_add_u32 s40, s40, s75\n" \
"s_addc_u32 s41, s41, 0\n" \
"ds_read_b64_tr_b16 v[146:147], v50 offset:4608\n" \
"v_mfma_f32_16x16x32_bf16 a[124:127], v[92:95], v[188:191], a[124:127]\n" \
"s_add_u32 s68, s66, 0x0\n" \
"s_add_u32 s64, s10, s68\n" \
"s_addc_u32 s65, s11, 0\n" \
"v_mfma_f32_16x16x32_bf16 a[144:147], v[96:99], v[160:163], 0\n" \
"ds_read_b128 v[88:91], v48 offset:6144\n" \
"v_mfma_f32_16x16x32_bf16 a[144:147], v[100:103], v[164:167], a[144:147]\n" \
"global_load_dword v253, v248, s[64:65]\n" \
"v_mfma_f32_16x16x32_bf16 a[148:151], v[96:99], v[168:171], 0\n" \
"ds_read_b64_tr_b16 v[148:149], v50 offset:16896\n" \
"v_mfma_f32_16x16x32_bf16 a[148:151], v[100:103], v[172:175], a[148:151]\n" \
"v_mfma_f32_16x16x32_bf16 a[152:155], v[96:99], v[176:179], 0\n" \
"ds_read_b128 v[92:95], v49 offset:6144\n" \
"v_mfma_f32_16x16x32_bf16 a[152:155], v[100:103], v[180:183], a[152:155]\n" \
"v_mfma_f32_16x16x32_bf16 a[156:159], v[96:99], v[184:187], 0\n" \
"ds_read_b64_tr_b16 v[150:151], v50 offset:20992\n" \
"v_mfma_f32_16x16x32_bf16 a[156:159], v[100:103], v[188:191], a[156:159]\n" \
"v_mfma_f32_16x16x32_bf16 a[176:179], v[104:107], v[160:163], 0\n" \
"ds_read_b128 v[96:99], v48 offset:8192\n" \
"v_mfma_f32_16x16x32_bf16 a[176:179], v[108:111], v[164:167], a[176:179]\n" \
"v_mfma_f32_16x16x32_bf16 a[180:183], v[104:107], v[168:171], 0\n" \
"ds_read_b64_tr_b16 v[152:153], v51 offset:512\n" \
"v_mfma_f32_16x16x32_bf16 a[180:183], v[108:111], v[172:175], a[180:183]\n" \
"v_mfma_f32_16x16x32_bf16 a[184:187], v[104:107], v[176:179], 0\n" \
"ds_read_b128 v[100:103], v49 offset:8192\n" \
"v_mfma_f32_16x16x32_bf16 a[184:187], v[108:111], v[180:183], a[184:187]\n" \
"v_mfma_f32_16x16x32_bf16 a[188:191], v[104:107], v[184:187], 0\n" \
"ds_read_b64_tr_b16 v[154:155], v51 offset:4608\n" \
"v_mfma_f32_16x16x32_bf16 a[188:191], v[108:111], v[188:191], a[188:191]\n" \
"v_mfma_f32_16x16x32_bf16 a[208:211], v[112:115], v[160:163], 0\n" \
"ds_read_b128 v[104:107], v48 offset:10240\n" \
"v_mfma_f32_16x16x32_bf16 a[208:211], v[116:119], v[164:167], a[208:211]\n" \
"v_mfma_f32_16x16x32_bf16 a[212:215], v[112:115], v[168:171], 0\n" \
"ds_read_b64_tr_b16 v[156:157], v51 offset:16896\n" \
"v_mfma_f32_16x16x32_bf16 a[212:215], v[116:119], v[172:175], a[212:215]\n" \
"v_mfma_f32_16x16x32_bf16 a[216:219], v[112:115], v[176:179], 0\n" \
"ds_read_b128 v[108:111], v49 offset:10240\n" \
"v_mfma_f32_16x16x32_bf16 a[216:219], v[116:119], v[180:183], a[216:219]\n" \
"v_mfma_f32_16x16x32_bf16 a[220:223], v[112:115], v[184:187], 0\n" \
"ds_read_b64_tr_b16 v[158:159], v51 offset:20992\n" \
"v_mfma_f32_16x16x32_bf16 a[220:223], v[116:119], v[188:191], a[220:223]\n" \
"v_mfma_f32_16x16x32_bf16 a[240:243], v[120:123], v[160:163], 0\n" \
"ds_read_b128 v[112:115], v48 offset:12288\n" \
"v_mfma_f32_16x16x32_bf16 a[240:243], v[124:127], v[164:167], a[240:243]\n" \
"v_mfma_f32_16x16x32_bf16 a[244:247], v[120:123], v[168:171], 0\n" \
"v_mfma_f32_16x16x32_bf16 a[244:247], v[124:127], v[172:175], a[244:247]\n" \
"v_mfma_f32_16x16x32_bf16 a[248:251], v[120:123], v[176:179], 0\n" \
"ds_read_b128 v[116:119], v49 offset:12288\n" \
"v_mfma_f32_16x16x32_bf16 a[248:251], v[124:127], v[180:183], a[248:251]\n" \
"v_mfma_f32_16x16x32_bf16 a[252:255], v[120:123], v[184:187], 0\n" \
"v_mfma_f32_16x16x32_bf16 a[252:255], v[124:127], v[188:191], a[252:255]\n" \
"s_waitcnt lgkmcnt(1)\n" \
"v_mfma_f32_16x16x32_bf16 a[0:3], v[128:131], v[64:67], a[0:3]\n" \
"ds_read_b128 v[120:123], v48 offset:14336\n" \
"v_mfma_f32_16x16x32_bf16 a[0:3], v[132:135], v[68:71], a[0:3]\n" \
"v_mfma_f32_16x16x32_bf16 a[4:7], v[136:139], v[64:67], a[4:7]\n" \
"ds_read_b128 v[124:127], v49 offset:14336\n" \
"v_mfma_f32_16x16x32_bf16 a[4:7], v[140:143], v[68:71], a[4:7]\n" \
"s_add_u32 s68, s66, 0x100000\n" \
"v_mfma_f32_16x16x32_bf16 a[8:11], v[144:147], v[64:67], a[8:11]\n" \
"s_add_u32 s64, s10, s68\n" \
"s_addc_u32 s65, s11, 0\n" \
"ds_read_b64_tr_b16 v[160:161], v50 offset:1024\n" \
"v_mfma_f32_16x16x32_bf16 a[8:11], v[148:151], v[68:71], a[8:11]\n" \
"global_load_dword v253, v248, s[64:65]\n" \
"v_mfma_f32_16x16x32_bf16 a[12:15], v[152:155], v[64:67], a[12:15]\n" \
"s_add_u32 m0, s87, 4096\n" \
"ds_read_b64_tr_b16 v[162:163], v50 offset:5120\n" \
"v_mfma_f32_16x16x32_bf16 a[12:15], v[156:159], v[68:71], a[12:15]\n" \
"global_load_lds_dwordx4 v40, s[52:53] offset:-4096\n" \
"v_mfma_f32_16x16x32_bf16 a[32:35], v[128:131], v[72:75], a[32:35]\n" \
"ds_read_b64_tr_b16 v[164:165], v50 offset:17408\n" \
"v_mfma_f32_16x16x32_bf16 a[32:35], v[132:135], v[76:79], a[32:35]\n" \
"v_mfma_f32_16x16x32_bf16 a[36:39], v[136:139], v[72:75], a[36:39]\n" \
"ds_read_b64_tr_b16 v[166:167], v50 offset:21504\n" \
"v_mfma_f32_16x16x32_bf16 a[36:39], v[140:143], v[76:79], a[36:39]\n" \
"global_load_lds_dwordx4 v41, s[50:51] offset:-3072\n" \
"v_mfma_f32_16x16x32_bf16 a[40:43], v[144:147], v[72:75], a[40:43]\n" \
"ds_read_b64_tr_b16 v[168:169], v51 offset:1024\n" \
"v_mfma_f32_16x16x32_bf16 a[40:43], v[148:151], v[76:79], a[40:43]\n" \
"v_mfma_f32_16x16x32_bf16 a[44:47], v[152:155], v[72:75], a[44:47]\n" \
"ds_read_b64_tr_b16 v[170:171], v51 offset:5120\n" \
"v_mfma_f32_16x16x32_bf16 a[44:47], v[156:159], v[76:79], a[44:47]\n" \
"global_load_lds_dwordx4 v42, s[52:53] offset:-2048\n" \
"v_mfma_f32_16x16x32_bf16 a[64:67], v[128:131], v[80:83], a[64:67]\n" \
"ds_read_b64_tr_b16 v[172:173], v51 offset:17408\n" \
"v_mfma_f32_16x16x32_bf16 a[64:67], v[132:135], v[84:87], a[64:67]\n" \
"v_mfma_f32_16x16x32_bf16 a[68:71], v[136:139], v[80:83], a[68:71]\n" \
"ds_read_b64_tr_b16 v[174:175], v51 offset:21504\n" \
"v_mfma_f32_16x16x32_bf16 a[68:71], v[140:143], v[84:87], a[68:71]\n" \
"global_load_lds_dwordx4 v43, s[50:51] offset:-1024\n" \
"v_mfma_f32_16x16x32_bf16 a[72:75], v[144:147], v[80:83], a[72:75]\n" \
"ds_read_b64_tr_b16 v[176:177], v50 offset:1536\n" \
"v_mfma_f32_16x16x32_bf16 a[72:75], v[148:151], v[84:87], a[72:75]\n" \
"v_mfma_f32_16x16x32_bf16 a[76:79], v[152:155], v[80:83], a[76:79]\n" \
"ds_read_b64_tr_b16 v[178:179], v50 offset:5632\n" \
"v_mfma_f32_16x16x32_bf16 a[76:79], v[156:159], v[84:87], a[76:79]\n" \
"global_load_lds_dwordx4 v44, s[52:53] offset:0\n" \
"v_mfma_f32_16x16x32_bf16 a[96:99], v[128:131], v[88:91], a[96:99]\n" \
"ds_read_b64_tr_b16 v[180:181], v50 offset:17920\n" \
"v_mfma_f32_16x16x32_bf16 a[96:99], v[132:135], v[92:95], a[96:99]\n" \
"v_mfma_f32_16x16x32_bf16 a[100:103], v[136:139], v[88:91], a[100:103]\n" \
"ds_read_b64_tr_b16 v[182:183], v50 offset:22016\n" \
"v_mfma_f32_16x16x32_bf16 a[100:103], v[140:143], v[92:95], a[100:103]\n" \
"global_load_lds_dwordx4 v45, s[50:51] offset:1024\n" \
"v_mfma_f32_16x16x32_bf16 a[104:107], v[144:147], v[88:91], a[104:107]\n" \
"ds_read_b64_tr_b16 v[184:185], v51 offset:1536\n" \
"v_mfma_f32_16x16x32_bf16 a[104:107], v[148:151], v[92:95], a[104:107]\n" \
"v_mfma_f32_16x16x32_bf16 a[108:111], v[152:155], v[88:91], a[108:111]\n" \
"ds_read_b64_tr_b16 v[186:187], v51 offset:5632\n" \
"v_mfma_f32_16x16x32_bf16 a[108:111], v[156:159], v[92:95], a[108:111]\n" \
"global_load_lds_dwordx4 v46, s[52:53] offset:2048\n" \
"v_mfma_f32_16x16x32_bf16 a[128:131], v[128:131], v[96:99], a[128:131]\n" \
"ds_read_b64_tr_b16 v[188:189], v51 offset:17920\n" \
"v_mfma_f32_16x16x32_bf16 a[128:131], v[132:135], v[100:103], a[128:131]\n" \
"v_mfma_f32_16x16x32_bf16 a[132:135], v[136:139], v[96:99], a[132:135]\n" \
"ds_read_b64_tr_b16 v[190:191], v51 offset:22016\n" \
"v_mfma_f32_16x16x32_bf16 a[132:135], v[140:143], v[100:103], a[132:135]\n" \
"global_load_lds_dwordx4 v47, s[50:51] offset:3072\n" \
"v_mfma_f32_16x16x32_bf16 a[136:139], v[144:147], v[96:99], a[136:139]\n" \
"s_add_u32 s52, s52, s74\n" \
"s_addc_u32 s53, s53, 0\n" \
"v_mfma_f32_16x16x32_bf16 a[136:139], v[148:151], v[100:103], a[136:139]\n" \
"s_add_u32 s50, s50, s74\n" \
"s_addc_u32 s51, s51, 0\n" \
"v_mfma_f32_16x16x32_bf16 a[140:143], v[152:155], v[96:99], a[140:143]\n" \
"v_mfma_f32_16x16x32_bf16 a[140:143], v[156:159], v[100:103], a[140:143]\n" \
"v_mfma_f32_16x16x32_bf16 a[160:163], v[128:131], v[104:107], a[160:163]\n" \
"s_mul_i32 s76, s73, 0x0\n" \
"s_add_u32 s77, s76, 0xffff8000\n" \
"s_sub_u32 s78, 0xffff8000, s76\n" \
"v_mfma_f32_16x16x32_bf16 a[160:163], v[132:135], v[108:111], a[160:163]\n" \
"v_add_u32 v48, s77, v48\n" \
"v_add_u32 v49, s77, v49\n" \
"v_mfma_f32_16x16x32_bf16 a[164:167], v[136:139], v[104:107], a[164:167]\n" \
"v_add_u32 v50, s78, v50\n" \
"v_add_u32 v51, s78, v51\n" \
"v_mfma_f32_16x16x32_bf16 a[164:167], v[140:143], v[108:111], a[164:167]\n" \
"v_mfma_f32_16x16x32_bf16 a[168:171], v[144:147], v[104:107], a[168:171]\n" \
"v_mfma_f32_16x16x32_bf16 a[168:171], v[148:151], v[108:111], a[168:171]\n" \
"v_mfma_f32_16x16x32_bf16 a[172:175], v[152:155], v[104:107], a[172:175]\n" \
"v_mfma_f32_16x16x32_bf16 a[172:175], v[156:159], v[108:111], a[172:175]\n" \
"s_waitcnt lgkmcnt(0)\n" \
"v_mfma_f32_16x16x32_bf16 a[192:195], v[128:131], v[112:115], a[192:195]\n" \
"v_mfma_f32_16x16x32_bf16 a[192:195], v[132:135], v[116:119], a[192:195]\n" \
"v_mfma_f32_16x16x32_bf16 a[196:199], v[136:139], v[112:115], a[196:199]\n" \
"v_mfma_f32_16x16x32_bf16 a[196:199], v[140:143], v[116:119], a[196:199]\n" \
"v_mfma_f32_16x16x32_bf16 a[200:203], v[144:147], v[112:115], a[200:203]\n" \
"v_mfma_f32_16x16x32_bf16 a[200:203], v[148:151], v[116:119], a[200:203]\n" \
"v_mfma_f32_16x16x32_bf16 a[204:207], v[152:155], v[112:115], a[204:207]\n" \
"v_mfma_f32_16x16x32_bf16 a[204:207], v[156:159], v[116:119], a[204:207]\n" \
"v_mfma_f32_16x16x32_bf16 a[224:227], v[128:131], v[120:123], a[224:227]\n" \
"v_mfma_f32_16x16x32_bf16 a[224:227], v[132:135], v[124:127], a[224:227]\n" \
"Lpg609_%=:\n" \
"Lst0x0_%=:\n" \
"s_cbranch_execz Lpg2768_%=\n" \
"v_mfma_f32_16x16x32_bf16 a[228:231], v[136:139], v[120:123], a[228:231]\n" \
"v_mfma_f32_16x16x32_bf16 a[228:231], v[140:143], v[124:127], a[228:231]\n" \
"v_mfma_f32_16x16x32_bf16 a[232:235], v[144:147], v[120:123], a[232:235]\n" \
"v_mfma_f32_16x16x32_bf16 a[232:235], v[148:151], v[124:127], a[232:235]\n" \
"v_mfma_f32_16x16x32_bf16 a[236:239], v[152:155], v[120:123], a[236:239]\n" \
"s_waitcnt vmcnt(10) lgkmcnt(0)\n" \
"v_mfma_f32_16x16x32_bf16 a[236:239], v[156:159], v[124:127], a[236:239]\n" \
"s_barrier\n" \
"v_mfma_f32_16x16x32_bf16 a[16:19], v[64:67], v[160:163], a[16:19]\n" \
"s_add_u32 m0, s87, 135168\n" \
"ds_read_b64_tr_b16 v[128:129], v50 offset:0\n" \
"v_readfirstlane_b32 s60, v244\n" \
"v_mfma_f32_16x16x32_bf16 a[16:19], v[68:71], v[164:167], a[16:19]\n" \
"s_add_u32 s60, s60, 0x9e3779b9\n" \
"global_load_lds_dwordx4 v32, s[54:55] offset:-4096\n" \
"v_mfma_f32_16x16x32_bf16 a[20:23], v[64:67], v[168:171], a[20:23]\n" \
"s_mul_i32 s88, s62, 557824\n" \
"s_mul_i32 s89, s63, 17408\n" \
"ds_read_b64_tr_b16 v[130:131], v50 offset:4096\n" \
"v_mfma_f32_16x16x32_bf16 a[20:23], v[68:71], v[172:175], a[20:23]\n" \
"s_add_u32 s88, s88, s89\n" \
"s_add_u32 s88, s88, s95\n" \
"s_add_u32 s89, s88, 17850368\n" \
"v_mfma_f32_16x16x32_bf16 a[24:27], v[64:67], v[176:179], a[24:27]\n" \
"s_lshl_b32 s91, s62, 7\n" \
"s_add_u32 s91, s91, 65585152\n" \
"ds_read_b64_tr_b16 v[132:133], v50 offset:16384\n" \
"v_mfma_f32_16x16x32_bf16 a[24:27], v[68:71], v[180:183], a[24:27]\n" \
"s_mov_b32 s92, 0x3e0293ee\n" \
"global_load_lds_dwordx4 v33, s[40:41] offset:-3072\n" \
"v_mfma_f32_16x16x32_bf16 a[28:31], v[64:67], v[184:187], a[28:31]\n" \
"ds_read_b64_tr_b16 v[134:135], v50 offset:20480\n" \
"v_mfma_f32_16x16x32_bf16 a[28:31], v[68:71], v[188:191], a[28:31]\n" \
"v_mfma_f32_16x16x32_bf16 a[48:51], v[72:75], v[160:163], a[48:51]\n" \
"ds_read_b128 v[64:67], v48 offset:0\n" \
"v_mfma_f32_16x16x32_bf16 a[48:51], v[76:79], v[164:167], a[48:51]\n" \
"global_load_lds_dwordx4 v34, s[54:55] offset:-2048\n" \
"v_mfma_f32_16x16x32_bf16 a[52:55], v[72:75], v[168:171], a[52:55]\n" \
"ds_read_b64_tr_b16 v[136:137], v51 offset:0\n" \
"v_mfma_f32_16x16x32_bf16 a[52:55], v[76:79], v[172:175], a[52:55]\n" \
"v_mfma_f32_16x16x32_bf16 a[56:59], v[72:75], v[176:179], a[56:59]\n" \
"ds_read_b128 v[68:71], v49 offset:0\n" \
"v_mfma_f32_16x16x32_bf16 a[56:59], v[76:79], v[180:183], a[56:59]\n" \
"global_load_lds_dwordx4 v35, s[40:41] offset:-1024\n" \
"v_mfma_f32_16x16x32_bf16 a[60:63], v[72:75], v[184:187], a[60:63]\n" \
"ds_read_b64_tr_b16 v[138:139], v51 offset:4096\n" \
"v_mfma_f32_16x16x32_bf16 a[60:63], v[76:79], v[188:191], a[60:63]\n" \
"v_mfma_f32_16x16x32_bf16 a[80:83], v[80:83], v[160:163], a[80:83]\n" \
"ds_read_b128 v[72:75], v48 offset:2048\n" \
"v_mfma_f32_16x16x32_bf16 a[80:83], v[84:87], v[164:167], a[80:83]\n" \
"global_load_lds_dwordx4 v36, s[54:55] offset:0\n" \
"v_mfma_f32_16x16x32_bf16 a[84:87], v[80:83], v[168:171], a[84:87]\n" \
"ds_read_b64_tr_b16 v[140:141], v51 offset:16384\n" \
"v_mfma_f32_16x16x32_bf16 a[84:87], v[84:87], v[172:175], a[84:87]\n" \
"v_mfma_f32_16x16x32_bf16 a[88:91], v[80:83], v[176:179], a[88:91]\n" \
"ds_read_b128 v[76:79], v49 offset:2048\n" \
"v_mfma_f32_16x16x32_bf16 a[88:91], v[84:87], v[180:183], a[88:91]\n" \
"global_load_lds_dwordx4 v37, s[40:41] offset:1024\n" \
"v_mfma_f32_16x16x32_bf16 a[92:95], v[80:83], v[184:187], a[92:95]\n" \
"ds_read_b64_tr_b16 v[142:143], v51 offset:20480\n" \
"v_mfma_f32_16x16x32_bf16 a[92:95], v[84:87], v[188:191], a[92:95]\n" \
"v_mfma_f32_16x16x32_bf16 a[112:115], v[88:91], v[160:163], a[112:115]\n" \
"ds_read_b128 v[80:83], v48 offset:4096\n" \
"v_mfma_f32_16x16x32_bf16 a[112:115], v[92:95], v[164:167], a[112:115]\n" \
"global_load_lds_dwordx4 v38, s[54:55] offset:2048\n" \
"v_mfma_f32_16x16x32_bf16 a[116:119], v[88:91], v[168:171], a[116:119]\n" \
"ds_read_b64_tr_b16 v[144:145], v50 offset:512\n" \
"v_mfma_f32_16x16x32_bf16 a[116:119], v[92:95], v[172:175], a[116:119]\n" \
"v_mfma_f32_16x16x32_bf16 a[120:123], v[88:91], v[176:179], a[120:123]\n" \
"ds_read_b128 v[84:87], v49 offset:4096\n" \
"global_load_lds_dwordx4 v39, s[40:41] offset:3072\n" \
"v_mfma_f32_16x16x32_bf16 a[120:123], v[92:95], v[180:183], a[120:123]\n" \
"s_add_u32 s54, s54, s75\n" \
"s_addc_u32 s55, s55, 0\n" \
"v_mfma_f32_16x16x32_bf16 a[124:127], v[88:91], v[184:187], a[124:127]\n" \
"s_add_u32 s40, s40, s75\n" \
"s_addc_u32 s41, s41, 0\n" \
"ds_read_b64_tr_b16 v[146:147], v50 offset:4608\n" \
"v_mfma_f32_16x16x32_bf16 a[124:127], v[92:95], v[188:191], a[124:127]\n" \
"s_add_u32 s68, s67, 0x200000\n" \
"s_add_u32 s64, s14, s68\n" \
"s_addc_u32 s65, s15, 0\n" \
"v_mfma_f32_16x16x32_bf16 a[144:147], v[96:99], v[160:163], a[144:147]\n" \
"ds_read_b128 v[88:91], v48 offset:6144\n" \
"v_mfma_f32_16x16x32_bf16 a[144:147], v[100:103], v[164:167], a[144:147]\n" \
"global_load_dword v253, v248, s[64:65]\n" \
"v_mfma_f32_16x16x32_bf16 a[148:151], v[96:99], v[168:171], a[148:151]\n" \
"s_add_u32 s68, s67, 0x200000\n" \
"ds_read_b64_tr_b16 v[148:149], v50 offset:16896\n" \
"v_mfma_f32_16x16x32_bf16 a[148:151], v[100:103], v[172:175], a[148:151]\n" \
"s_add_u32 s64, s16, s68\n" \
"s_addc_u32 s65, s17, 0\n" \
"v_mfma_f32_16x16x32_bf16 a[152:155], v[96:99], v[176:179], a[152:155]\n" \
"ds_read_b128 v[92:95], v49 offset:6144\n" \
"v_mfma_f32_16x16x32_bf16 a[152:155], v[100:103], v[180:183], a[152:155]\n" \
"global_load_dword v253, v248, s[64:65]\n" \
"v_mfma_f32_16x16x32_bf16 a[156:159], v[96:99], v[184:187], a[156:159]\n" \
"ds_read_b64_tr_b16 v[150:151], v50 offset:20992\n" \
"v_mfma_f32_16x16x32_bf16 a[156:159], v[100:103], v[188:191], a[156:159]\n" \
"v_mfma_f32_16x16x32_bf16 a[176:179], v[104:107], v[160:163], a[176:179]\n" \
"ds_read_b128 v[96:99], v48 offset:8192\n" \
"v_mfma_f32_16x16x32_bf16 a[176:179], v[108:111], v[164:167], a[176:179]\n" \
"v_mfma_f32_16x16x32_bf16 a[180:183], v[104:107], v[168:171], a[180:183]\n" \
"ds_read_b64_tr_b16 v[152:153], v51 offset:512\n" \
"v_mfma_f32_16x16x32_bf16 a[180:183], v[108:111], v[172:175], a[180:183]\n" \
"v_mfma_f32_16x16x32_bf16 a[184:187], v[104:107], v[176:179], a[184:187]\n" \
"ds_read_b128 v[100:103], v49 offset:8192\n" \
"v_mfma_f32_16x16x32_bf16 a[184:187], v[108:111], v[180:183], a[184:187]\n" \
"v_mfma_f32_16x16x32_bf16 a[188:191], v[104:107], v[184:187], a[188:191]\n" \
"ds_read_b64_tr_b16 v[154:155], v51 offset:4608\n" \
"v_mfma_f32_16x16x32_bf16 a[188:191], v[108:111], v[188:191], a[188:191]\n" \
"v_mfma_f32_16x16x32_bf16 a[208:211], v[112:115], v[160:163], a[208:211]\n" \
"ds_read_b128 v[104:107], v48 offset:10240\n" \
"v_mfma_f32_16x16x32_bf16 a[208:211], v[116:119], v[164:167], a[208:211]\n" \
"v_mfma_f32_16x16x32_bf16 a[212:215], v[112:115], v[168:171], a[212:215]\n" \
"ds_read_b64_tr_b16 v[156:157], v51 offset:16896\n" \
"v_mfma_f32_16x16x32_bf16 a[212:215], v[116:119], v[172:175], a[212:215]\n" \
"v_mfma_f32_16x16x32_bf16 a[216:219], v[112:115], v[176:179], a[216:219]\n" \
"ds_read_b128 v[108:111], v49 offset:10240\n" \
"v_mfma_f32_16x16x32_bf16 a[216:219], v[116:119], v[180:183], a[216:219]\n" \
"v_mfma_f32_16x16x32_bf16 a[220:223], v[112:115], v[184:187], a[220:223]\n" \
"ds_read_b64_tr_b16 v[158:159], v51 offset:20992\n" \
"v_mfma_f32_16x16x32_bf16 a[220:223], v[116:119], v[188:191], a[220:223]\n" \
"v_mfma_f32_16x16x32_bf16 a[240:243], v[120:123], v[160:163], a[240:243]\n" \
"ds_read_b128 v[112:115], v48 offset:12288\n" \
"v_mfma_f32_16x16x32_bf16 a[240:243], v[124:127], v[164:167], a[240:243]\n" \
"v_mfma_f32_16x16x32_bf16 a[244:247], v[120:123], v[168:171], a[244:247]\n" \
"v_mfma_f32_16x16x32_bf16 a[244:247], v[124:127], v[172:175], a[244:247]\n" \
"v_mfma_f32_16x16x32_bf16 a[248:251], v[120:123], v[176:179], a[248:251]\n" \
"ds_read_b128 v[116:119], v49 offset:12288\n" \
"v_mfma_f32_16x16x32_bf16 a[248:251], v[124:127], v[180:183], a[248:251]\n" \
"v_mfma_f32_16x16x32_bf16 a[252:255], v[120:123], v[184:187], a[252:255]\n" \
"v_mfma_f32_16x16x32_bf16 a[252:255], v[124:127], v[188:191], a[252:255]\n" \
"s_waitcnt lgkmcnt(1)\n" \
"v_mfma_f32_16x16x32_bf16 a[0:3], v[128:131], v[64:67], a[0:3]\n" \
"ds_read_b128 v[120:123], v48 offset:14336\n" \
"v_mfma_f32_16x16x32_bf16 a[0:3], v[132:135], v[68:71], a[0:3]\n" \
"v_mfma_f32_16x16x32_bf16 a[4:7], v[136:139], v[64:67], a[4:7]\n" \
"ds_read_b128 v[124:127], v49 offset:14336\n" \
"v_mfma_f32_16x16x32_bf16 a[4:7], v[140:143], v[68:71], a[4:7]\n" \
"s_add_u32 s68, s67, 0x300000\n" \
"v_mfma_f32_16x16x32_bf16 a[8:11], v[144:147], v[64:67], a[8:11]\n" \
"s_add_u32 s64, s14, s68\n" \
"s_addc_u32 s65, s15, 0\n" \
"ds_read_b64_tr_b16 v[160:161], v50 offset:1024\n" \
"v_mfma_f32_16x16x32_bf16 a[8:11], v[148:151], v[68:71], a[8:11]\n" \
"global_load_dword v253, v248, s[64:65]\n" \
"v_mfma_f32_16x16x32_bf16 a[12:15], v[152:155], v[64:67], a[12:15]\n" \
"s_add_u32 m0, s87, 69632\n" \
"ds_read_b64_tr_b16 v[162:163], v50 offset:5120\n" \
"v_mfma_f32_16x16x32_bf16 a[12:15], v[156:159], v[68:71], a[12:15]\n" \
"global_load_lds_dwordx4 v40, s[52:53] offset:-4096\n" \
"v_mfma_f32_16x16x32_bf16 a[32:35], v[128:131], v[72:75], a[32:35]\n" \
"ds_read_b64_tr_b16 v[164:165], v50 offset:17408\n" \
"v_mfma_f32_16x16x32_bf16 a[32:35], v[132:135], v[76:79], a[32:35]\n" \
"v_mfma_f32_16x16x32_bf16 a[36:39], v[136:139], v[72:75], a[36:39]\n" \
"ds_read_b64_tr_b16 v[166:167], v50 offset:21504\n" \
"v_mfma_f32_16x16x32_bf16 a[36:39], v[140:143], v[76:79], a[36:39]\n" \
"global_load_lds_dwordx4 v41, s[50:51] offset:-3072\n" \
"v_mfma_f32_16x16x32_bf16 a[40:43], v[144:147], v[72:75], a[40:43]\n" \
"ds_read_b64_tr_b16 v[168:169], v51 offset:1024\n" \
"v_mfma_f32_16x16x32_bf16 a[40:43], v[148:151], v[76:79], a[40:43]\n" \
"v_mfma_f32_16x16x32_bf16 a[44:47], v[152:155], v[72:75], a[44:47]\n" \
"ds_read_b64_tr_b16 v[170:171], v51 offset:5120\n" \
"v_mfma_f32_16x16x32_bf16 a[44:47], v[156:159], v[76:79], a[44:47]\n" \
"global_load_lds_dwordx4 v42, s[52:53] offset:-2048\n" \
"v_mfma_f32_16x16x32_bf16 a[64:67], v[128:131], v[80:83], a[64:67]\n" \
"ds_read_b64_tr_b16 v[172:173], v51 offset:17408\n" \
"v_mfma_f32_16x16x32_bf16 a[64:67], v[132:135], v[84:87], a[64:67]\n" \
"v_mfma_f32_16x16x32_bf16 a[68:71], v[136:139], v[80:83], a[68:71]\n" \
"ds_read_b64_tr_b16 v[174:175], v51 offset:21504\n" \
"v_mfma_f32_16x16x32_bf16 a[68:71], v[140:143], v[84:87], a[68:71]\n" \
"global_load_lds_dwordx4 v43, s[50:51] offset:-1024\n" \
"v_mfma_f32_16x16x32_bf16 a[72:75], v[144:147], v[80:83], a[72:75]\n" \
"ds_read_b64_tr_b16 v[176:177], v50 offset:1536\n" \
"v_mfma_f32_16x16x32_bf16 a[72:75], v[148:151], v[84:87], a[72:75]\n" \
"v_mfma_f32_16x16x32_bf16 a[76:79], v[152:155], v[80:83], a[76:79]\n" \
"ds_read_b64_tr_b16 v[178:179], v50 offset:5632\n" \
"v_mfma_f32_16x16x32_bf16 a[76:79], v[156:159], v[84:87], a[76:79]\n" \
"global_load_lds_dwordx4 v44, s[52:53] offset:0\n" \
"v_mfma_f32_16x16x32_bf16 a[96:99], v[128:131], v[88:91], a[96:99]\n" \
"ds_read_b64_tr_b16 v[180:181], v50 offset:17920\n" \
"v_mfma_f32_16x16x32_bf16 a[96:99], v[132:135], v[92:95], a[96:99]\n" \
"v_mfma_f32_16x16x32_bf16 a[100:103], v[136:139], v[88:91], a[100:103]\n" \
"ds_read_b64_tr_b16 v[182:183], v50 offset:22016\n" \
"v_mfma_f32_16x16x32_bf16 a[100:103], v[140:143], v[92:95], a[100:103]\n" \
"global_load_lds_dwordx4 v45, s[50:51] offset:1024\n" \
"v_mfma_f32_16x16x32_bf16 a[104:107], v[144:147], v[88:91], a[104:107]\n" \
"ds_read_b64_tr_b16 v[184:185], v51 offset:1536\n" \
"v_mfma_f32_16x16x32_bf16 a[104:107], v[148:151], v[92:95], a[104:107]\n" \
"v_mfma_f32_16x16x32_bf16 a[108:111], v[152:155], v[88:91], a[108:111]\n" \
"ds_read_b64_tr_b16 v[186:187], v51 offset:5632\n" \
"v_mfma_f32_16x16x32_bf16 a[108:111], v[156:159], v[92:95], a[108:111]\n" \
"global_load_lds_dwordx4 v46, s[52:53] offset:2048\n" \
"v_mfma_f32_16x16x32_bf16 a[128:131], v[128:131], v[96:99], a[128:131]\n" \
"ds_read_b64_tr_b16 v[188:189], v51 offset:17920\n" \
"v_mfma_f32_16x16x32_bf16 a[128:131], v[132:135], v[100:103], a[128:131]\n" \
"v_mfma_f32_16x16x32_bf16 a[132:135], v[136:139], v[96:99], a[132:135]\n" \
"ds_read_b64_tr_b16 v[190:191], v51 offset:22016\n" \
"v_mfma_f32_16x16x32_bf16 a[132:135], v[140:143], v[100:103], a[132:135]\n" \
"global_load_lds_dwordx4 v47, s[50:51] offset:3072\n" \
"v_mfma_f32_16x16x32_bf16 a[136:139], v[144:147], v[96:99], a[136:139]\n" \
"s_add_u32 s52, s52, s74\n" \
"s_addc_u32 s53, s53, 0\n" \
"v_mfma_f32_16x16x32_bf16 a[136:139], v[148:151], v[100:103], a[136:139]\n" \
"s_add_u32 s50, s50, s74\n" \
"s_addc_u32 s51, s51, 0\n" \
"v_mfma_f32_16x16x32_bf16 a[140:143], v[152:155], v[96:99], a[140:143]\n" \
"v_mfma_f32_16x16x32_bf16 a[140:143], v[156:159], v[100:103], a[140:143]\n" \
"v_mfma_f32_16x16x32_bf16 a[160:163], v[128:131], v[104:107], a[160:163]\n" \
"s_mul_i32 s76, s73, 0xfffd0000\n" \
"s_add_u32 s77, s76, 0x18000\n" \
"s_sub_u32 s78, 0xfffe8000, s76\n" \
"v_mfma_f32_16x16x32_bf16 a[160:163], v[132:135], v[108:111], a[160:163]\n" \
"v_add_u32 v48, s77, v48\n" \
"v_add_u32 v49, s77, v49\n" \
"v_mfma_f32_16x16x32_bf16 a[164:167], v[136:139], v[104:107], a[164:167]\n" \
"v_add_u32 v50, s78, v50\n" \
"v_add_u32 v51, s78, v51\n" \
"v_mfma_f32_16x16x32_bf16 a[164:167], v[140:143], v[108:111], a[164:167]\n" \
"v_mfma_f32_16x16x32_bf16 a[168:171], v[144:147], v[104:107], a[168:171]\n" \
"v_mfma_f32_16x16x32_bf16 a[168:171], v[148:151], v[108:111], a[168:171]\n" \
"v_mfma_f32_16x16x32_bf16 a[172:175], v[152:155], v[104:107], a[172:175]\n" \
"v_mfma_f32_16x16x32_bf16 a[172:175], v[156:159], v[108:111], a[172:175]\n" \
"s_waitcnt lgkmcnt(0)\n" \
"v_mfma_f32_16x16x32_bf16 a[192:195], v[128:131], v[112:115], a[192:195]\n" \
"v_mfma_f32_16x16x32_bf16 a[192:195], v[132:135], v[116:119], a[192:195]\n" \
"v_mfma_f32_16x16x32_bf16 a[196:199], v[136:139], v[112:115], a[196:199]\n" \
"v_mfma_f32_16x16x32_bf16 a[196:199], v[140:143], v[116:119], a[196:199]\n" \
"v_mfma_f32_16x16x32_bf16 a[200:203], v[144:147], v[112:115], a[200:203]\n" \
"v_mfma_f32_16x16x32_bf16 a[200:203], v[148:151], v[116:119], a[200:203]\n" \
"v_mfma_f32_16x16x32_bf16 a[204:207], v[152:155], v[112:115], a[204:207]\n" \
"v_mfma_f32_16x16x32_bf16 a[204:207], v[156:159], v[116:119], a[204:207]\n" \
"v_mfma_f32_16x16x32_bf16 a[224:227], v[128:131], v[120:123], a[224:227]\n" \
"v_mfma_f32_16x16x32_bf16 a[224:227], v[132:135], v[124:127], a[224:227]\n" \
"v_mfma_f32_16x16x32_bf16 a[228:231], v[136:139], v[120:123], a[228:231]\n" \
"v_mfma_f32_16x16x32_bf16 a[228:231], v[140:143], v[124:127], a[228:231]\n" \
"v_mfma_f32_16x16x32_bf16 a[232:235], v[144:147], v[120:123], a[232:235]\n" \
"v_mfma_f32_16x16x32_bf16 a[232:235], v[148:151], v[124:127], a[232:235]\n" \
"v_mfma_f32_16x16x32_bf16 a[236:239], v[152:155], v[120:123], a[236:239]\n" \
"s_waitcnt vmcnt(11) lgkmcnt(0)\n" \
"v_mfma_f32_16x16x32_bf16 a[236:239], v[156:159], v[124:127], a[236:239]\n" \
"s_barrier\n" \
"v_mfma_f32_16x16x32_bf16 a[16:19], v[64:67], v[160:163], a[16:19]\n" \
"s_add_u32 m0, s87, 102400\n" \
"ds_read_b64_tr_b16 v[128:129], v50 offset:0\n" \
"v_mfma_f32_16x16x32_bf16 a[16:19], v[68:71], v[164:167], a[16:19]\n" \
"global_load_lds_dwordx4 v32, s[54:55] offset:-4096\n" \
"v_mfma_f32_16x16x32_bf16 a[20:23], v[64:67], v[168:171], a[20:23]\n" \
"ds_read_b64_tr_b16 v[130:131], v50 offset:4096\n" \
"v_mfma_f32_16x16x32_bf16 a[20:23], v[68:71], v[172:175], a[20:23]\n" \
"v_mfma_f32_16x16x32_bf16 a[24:27], v[64:67], v[176:179], a[24:27]\n" \
"ds_read_b64_tr_b16 v[132:133], v50 offset:16384\n" \
"v_mfma_f32_16x16x32_bf16 a[24:27], v[68:71], v[180:183], a[24:27]\n" \
"global_load_lds_dwordx4 v33, s[40:41] offset:-3072\n" \
"v_mfma_f32_16x16x32_bf16 a[28:31], v[64:67], v[184:187], a[28:31]\n" \
"ds_read_b64_tr_b16 v[134:135], v50 offset:20480\n" \
"v_mfma_f32_16x16x32_bf16 a[28:31], v[68:71], v[188:191], a[28:31]\n" \
"v_mfma_f32_16x16x32_bf16 a[48:51], v[72:75], v[160:163], a[48:51]\n" \
"ds_read_b128 v[64:67], v48 offset:0\n" \
"v_mfma_f32_16x16x32_bf16 a[48:51], v[76:79], v[164:167], a[48:51]\n" \
"global_load_lds_dwordx4 v34, s[54:55] offset:-2048\n" \
"v_mfma_f32_16x16x32_bf16 a[52:55], v[72:75], v[168:171], a[52:55]\n" \
"ds_read_b64_tr_b16 v[136:137], v51 offset:0\n" \
"v_mfma_f32_16x16x32_bf16 a[52:55], v[76:79], v[172:175], a[52:55]\n" \
"v_mfma_f32_16x16x32_bf16 a[56:59], v[72:75], v[176:179], a[56:59]\n" \
"ds_read_b128 v[68:71], v49 offset:0\n" \
"v_mfma_f32_16x16x32_bf16 a[56:59], v[76:79], v[180:183], a[56:59]\n" \
"global_load_lds_dwordx4 v35, s[40:41] offset:-1024\n" \
"v_mfma_f32_16x16x32_bf16 a[60:63], v[72:75], v[184:187], a[60:63]\n" \
"ds_read_b64_tr_b16 v[138:139], v51 offset:4096\n" \
"v_mfma_f32_16x16x32_bf16 a[60:63], v[76:79], v[188:191], a[60:63]\n" \
"v_mfma_f32_16x16x32_bf16 a[80:83], v[80:83], v[160:163], a[80:83]\n" \
"ds_read_b128 v[72:75], v48 offset:2048\n" \
"v_mfma_f32_16x16x32_bf16 a[80:83], v[84:87], v[164:167], a[80:83]\n" \
"global_load_lds_dwordx4 v36, s[54:55] offset:0\n" \
"v_mfma_f32_16x16x32_bf16 a[84:87], v[80:83], v[168:171], a[84:87]\n" \
"ds_read_b64_tr_b16 v[140:141], v51 offset:16384\n" \
"v_mfma_f32_16x16x32_bf16 a[84:87], v[84:87], v[172:175], a[84:87]\n" \
"v_mfma_f32_16x16x32_bf16 a[88:91], v[80:83], v[176:179], a[88:91]\n" \
"ds_read_b128 v[76:79], v49 offset:2048\n" \
"v_mfma_f32_16x16x32_bf16 a[88:91], v[84:87], v[180:183], a[88:91]\n" \
"global_load_lds_dwordx4 v37, s[40:41] offset:1024\n" \
"v_mfma_f32_16x16x32_bf16 a[92:95], v[80:83], v[184:187], a[92:95]\n" \
"ds_read_b64_tr_b16 v[142:143], v51 offset:20480\n" \
"v_mfma_f32_16x16x32_bf16 a[92:95], v[84:87], v[188:191], a[92:95]\n" \
"v_mfma_f32_16x16x32_bf16 a[112:115], v[88:91], v[160:163], a[112:115]\n" \
"ds_read_b128 v[80:83], v48 offset:4096\n" \
"v_mfma_f32_16x16x32_bf16 a[112:115], v[92:95], v[164:167], a[112:115]\n" \
"global_load_lds_dwordx4 v38, s[54:55] offset:2048\n" \
"v_mfma_f32_16x16x32_bf16 a[116:119], v[88:91], v[168:171], a[116:119]\n" \
"ds_read_b64_tr_b16 v[144:145], v50 offset:512\n" \
"v_mfma_f32_16x16x32_bf16 a[116:119], v[92:95], v[172:175], a[116:119]\n" \
"v_mfma_f32_16x16x32_bf16 a[120:123], v[88:91], v[176:179], a[120:123]\n" \
"ds_read_b128 v[84:87], v49 offset:4096\n" \
"global_load_lds_dwordx4 v39, s[40:41] offset:3072\n" \
"v_mfma_f32_16x16x32_bf16 a[120:123], v[92:95], v[180:183], a[120:123]\n" \
"s_add_u32 s54, s54, s75\n" \
"s_addc_u32 s55, s55, 0\n" \
"v_mfma_f32_16x16x32_bf16 a[124:127], v[88:91], v[184:187], a[124:127]\n" \
"s_add_u32 s40, s40, s75\n" \
"s_addc_u32 s41, s41, 0\n" \
"ds_read_b64_tr_b16 v[146:147], v50 offset:4608\n" \
"v_mfma_f32_16x16x32_bf16 a[124:127], v[92:95], v[188:191], a[124:127]\n" \
"s_add_u32 s68, s67, 0x300000\n" \
"s_add_u32 s64, s16, s68\n" \
"s_addc_u32 s65, s17, 0\n" \
"v_mfma_f32_16x16x32_bf16 a[144:147], v[96:99], v[160:163], a[144:147]\n" \
"ds_read_b128 v[88:91], v48 offset:6144\n" \
"v_mfma_f32_16x16x32_bf16 a[144:147], v[100:103], v[164:167], a[144:147]\n" \
"global_load_dword v253, v248, s[64:65]\n" \
"v_mfma_f32_16x16x32_bf16 a[148:151], v[96:99], v[168:171], a[148:151]\n" \
"s_add_u32 s68, s66, 0x0\n" \
"ds_read_b64_tr_b16 v[148:149], v50 offset:16896\n" \
"v_mfma_f32_16x16x32_bf16 a[148:151], v[100:103], v[172:175], a[148:151]\n" \
"s_add_u32 s64, s8, s68\n" \
"s_addc_u32 s65, s9, 0\n" \
"v_mfma_f32_16x16x32_bf16 a[152:155], v[96:99], v[176:179], a[152:155]\n" \
"ds_read_b128 v[92:95], v49 offset:6144\n" \
"v_mfma_f32_16x16x32_bf16 a[152:155], v[100:103], v[180:183], a[152:155]\n" \
"global_load_dword v253, v248, s[64:65]\n" \
"v_mfma_f32_16x16x32_bf16 a[156:159], v[96:99], v[184:187], a[156:159]\n" \
"ds_read_b64_tr_b16 v[150:151], v50 offset:20992\n" \
"v_mfma_f32_16x16x32_bf16 a[156:159], v[100:103], v[188:191], a[156:159]\n" \
"v_mfma_f32_16x16x32_bf16 a[176:179], v[104:107], v[160:163], a[176:179]\n" \
"ds_read_b128 v[96:99], v48 offset:8192\n" \
"v_mfma_f32_16x16x32_bf16 a[176:179], v[108:111], v[164:167], a[176:179]\n" \
"v_mfma_f32_16x16x32_bf16 a[180:183], v[104:107], v[168:171], a[180:183]\n" \
"ds_read_b64_tr_b16 v[152:153], v51 offset:512\n" \
"v_mfma_f32_16x16x32_bf16 a[180:183], v[108:111], v[172:175], a[180:183]\n" \
"v_mfma_f32_16x16x32_bf16 a[184:187], v[104:107], v[176:179], a[184:187]\n" \
"ds_read_b128 v[100:103], v49 offset:8192\n" \
"v_mfma_f32_16x16x32_bf16 a[184:187], v[108:111], v[180:183], a[184:187]\n" \
"v_mfma_f32_16x16x32_bf16 a[188:191], v[104:107], v[184:187], a[188:191]\n" \
"ds_read_b64_tr_b16 v[154:155], v51 offset:4608\n" \
"v_mfma_f32_16x16x32_bf16 a[188:191], v[108:111], v[188:191], a[188:191]\n" \
"v_mfma_f32_16x16x32_bf16 a[208:211], v[112:115], v[160:163], a[208:211]\n" \
"ds_read_b128 v[104:107], v48 offset:10240\n" \
"v_mfma_f32_16x16x32_bf16 a[208:211], v[116:119], v[164:167], a[208:211]\n" \
"v_mfma_f32_16x16x32_bf16 a[212:215], v[112:115], v[168:171], a[212:215]\n" \
"ds_read_b64_tr_b16 v[156:157], v51 offset:16896\n" \
"v_mfma_f32_16x16x32_bf16 a[212:215], v[116:119], v[172:175], a[212:215]\n" \
"v_mfma_f32_16x16x32_bf16 a[216:219], v[112:115], v[176:179], a[216:219]\n" \
"ds_read_b128 v[108:111], v49 offset:10240\n" \
"v_mfma_f32_16x16x32_bf16 a[216:219], v[116:119], v[180:183], a[216:219]\n" \
"v_mfma_f32_16x16x32_bf16 a[220:223], v[112:115], v[184:187], a[220:223]\n" \
"ds_read_b64_tr_b16 v[158:159], v51 offset:20992\n" \
"v_mfma_f32_16x16x32_bf16 a[220:223], v[116:119], v[188:191], a[220:223]\n" \
"v_mfma_f32_16x16x32_bf16 a[240:243], v[120:123], v[160:163], a[240:243]\n" \
"ds_read_b128 v[112:115], v48 offset:12288\n" \
"v_mfma_f32_16x16x32_bf16 a[240:243], v[124:127], v[164:167], a[240:243]\n" \
"v_mfma_f32_16x16x32_bf16 a[244:247], v[120:123], v[168:171], a[244:247]\n" \
"v_mfma_f32_16x16x32_bf16 a[244:247], v[124:127], v[172:175], a[244:247]\n" \
"v_mfma_f32_16x16x32_bf16 a[248:251], v[120:123], v[176:179], a[248:251]\n" \
"ds_read_b128 v[116:119], v49 offset:12288\n" \
"v_mfma_f32_16x16x32_bf16 a[248:251], v[124:127], v[180:183], a[248:251]\n" \
"v_mfma_f32_16x16x32_bf16 a[252:255], v[120:123], v[184:187], a[252:255]\n" \
"v_mfma_f32_16x16x32_bf16 a[252:255], v[124:127], v[188:191], a[252:255]\n" \
"s_waitcnt lgkmcnt(1)\n" \
"v_mfma_f32_16x16x32_bf16 a[0:3], v[128:131], v[64:67], a[0:3]\n" \
"ds_read_b128 v[120:123], v48 offset:14336\n" \
"v_mfma_f32_16x16x32_bf16 a[0:3], v[132:135], v[68:71], a[0:3]\n" \
"v_mfma_f32_16x16x32_bf16 a[4:7], v[136:139], v[64:67], a[4:7]\n" \
"ds_read_b128 v[124:127], v49 offset:14336\n" \
"v_mfma_f32_16x16x32_bf16 a[4:7], v[140:143], v[68:71], a[4:7]\n" \
"s_add_u32 s68, s66, 0x100000\n" \
"v_mfma_f32_16x16x32_bf16 a[8:11], v[144:147], v[64:67], a[8:11]\n" \
"s_add_u32 s64, s8, s68\n" \
"s_addc_u32 s65, s9, 0\n" \
"ds_read_b64_tr_b16 v[160:161], v50 offset:1024\n" \
"v_mfma_f32_16x16x32_bf16 a[8:11], v[148:151], v[68:71], a[8:11]\n" \
"global_load_dword v253, v248, s[64:65]\n" \
"v_mfma_f32_16x16x32_bf16 a[12:15], v[152:155], v[64:67], a[12:15]\n" \
"s_add_u32 m0, s87, 36864\n" \
"ds_read_b64_tr_b16 v[162:163], v50 offset:5120\n" \
"v_mfma_f32_16x16x32_bf16 a[12:15], v[156:159], v[68:71], a[12:15]\n" \
"global_load_lds_dwordx4 v40, s[52:53] offset:-4096\n" \
"v_mfma_f32_16x16x32_bf16 a[32:35], v[128:131], v[72:75], a[32:35]\n" \
"ds_read_b64_tr_b16 v[164:165], v50 offset:17408\n" \
"v_mfma_f32_16x16x32_bf16 a[32:35], v[132:135], v[76:79], a[32:35]\n" \
"v_mfma_f32_16x16x32_bf16 a[36:39], v[136:139], v[72:75], a[36:39]\n" \
"ds_read_b64_tr_b16 v[166:167], v50 offset:21504\n" \
"v_mfma_f32_16x16x32_bf16 a[36:39], v[140:143], v[76:79], a[36:39]\n" \
"global_load_lds_dwordx4 v41, s[50:51] offset:-3072\n" \
"v_mfma_f32_16x16x32_bf16 a[40:43], v[144:147], v[72:75], a[40:43]\n" \
"ds_read_b64_tr_b16 v[168:169], v51 offset:1024\n" \
"v_mfma_f32_16x16x32_bf16 a[40:43], v[148:151], v[76:79], a[40:43]\n" \
"v_mfma_f32_16x16x32_bf16 a[44:47], v[152:155], v[72:75], a[44:47]\n" \
"ds_read_b64_tr_b16 v[170:171], v51 offset:5120\n" \
"v_mfma_f32_16x16x32_bf16 a[44:47], v[156:159], v[76:79], a[44:47]\n" \
"global_load_lds_dwordx4 v42, s[52:53] offset:-2048\n" \
"v_mfma_f32_16x16x32_bf16 a[64:67], v[128:131], v[80:83], a[64:67]\n" \
"ds_read_b64_tr_b16 v[172:173], v51 offset:17408\n" \
"v_mfma_f32_16x16x32_bf16 a[64:67], v[132:135], v[84:87], a[64:67]\n" \
"v_mfma_f32_16x16x32_bf16 a[68:71], v[136:139], v[80:83], a[68:71]\n" \
"ds_read_b64_tr_b16 v[174:175], v51 offset:21504\n" \
"v_mfma_f32_16x16x32_bf16 a[68:71], v[140:143], v[84:87], a[68:71]\n" \
"global_load_lds_dwordx4 v43, s[50:51] offset:-1024\n" \
"v_mfma_f32_16x16x32_bf16 a[72:75], v[144:147], v[80:83], a[72:75]\n" \
"ds_read_b64_tr_b16 v[176:177], v50 offset:1536\n" \
"v_mfma_f32_16x16x32_bf16 a[72:75], v[148:151], v[84:87], a[72:75]\n" \
"v_mfma_f32_16x16x32_bf16 a[76:79], v[152:155], v[80:83], a[76:79]\n" \
"ds_read_b64_tr_b16 v[178:179], v50 offset:5632\n" \
"v_mfma_f32_16x16x32_bf16 a[76:79], v[156:159], v[84:87], a[76:79]\n" \
"global_load_lds_dwordx4 v44, s[52:53] offset:0\n" \
"v_mfma_f32_16x16x32_bf16 a[96:99], v[128:131], v[88:91], a[96:99]\n" \
"ds_read_b64_tr_b16 v[180:181], v50 offset:17920\n" \
"v_mfma_f32_16x16x32_bf16 a[96:99], v[132:135], v[92:95], a[96:99]\n" \
"v_mfma_f32_16x16x32_bf16 a[100:103], v[136:139], v[88:91], a[100:103]\n" \
"ds_read_b64_tr_b16 v[182:183], v50 offset:22016\n" \
"v_mfma_f32_16x16x32_bf16 a[100:103], v[140:143], v[92:95], a[100:103]\n" \
"global_load_lds_dwordx4 v45, s[50:51] offset:1024\n" \
"v_mfma_f32_16x16x32_bf16 a[104:107], v[144:147], v[88:91], a[104:107]\n" \
"ds_read_b64_tr_b16 v[184:185], v51 offset:1536\n" \
"v_mfma_f32_16x16x32_bf16 a[104:107], v[148:151], v[92:95], a[104:107]\n" \
"v_mfma_f32_16x16x32_bf16 a[108:111], v[152:155], v[88:91], a[108:111]\n" \
"ds_read_b64_tr_b16 v[186:187], v51 offset:5632\n" \
"v_mfma_f32_16x16x32_bf16 a[108:111], v[156:159], v[92:95], a[108:111]\n" \
"global_load_lds_dwordx4 v46, s[52:53] offset:2048\n" \
"v_mfma_f32_16x16x32_bf16 a[128:131], v[128:131], v[96:99], a[128:131]\n" \
"ds_read_b64_tr_b16 v[188:189], v51 offset:17920\n" \
"v_mfma_f32_16x16x32_bf16 a[128:131], v[132:135], v[100:103], a[128:131]\n" \
"v_mfma_f32_16x16x32_bf16 a[132:135], v[136:139], v[96:99], a[132:135]\n" \
"ds_read_b64_tr_b16 v[190:191], v51 offset:22016\n" \
"v_mfma_f32_16x16x32_bf16 a[132:135], v[140:143], v[100:103], a[132:135]\n" \
"global_load_lds_dwordx4 v47, s[50:51] offset:3072\n" \
"v_mfma_f32_16x16x32_bf16 a[136:139], v[144:147], v[96:99], a[136:139]\n" \
"s_add_u32 s52, s52, s74\n" \
"s_addc_u32 s53, s53, 0\n" \
"v_mfma_f32_16x16x32_bf16 a[136:139], v[148:151], v[100:103], a[136:139]\n" \
"s_add_u32 s50, s50, s74\n" \
"s_addc_u32 s51, s51, 0\n" \
"v_mfma_f32_16x16x32_bf16 a[140:143], v[152:155], v[96:99], a[140:143]\n" \
"v_mfma_f32_16x16x32_bf16 a[140:143], v[156:159], v[100:103], a[140:143]\n" \
"v_mfma_f32_16x16x32_bf16 a[160:163], v[128:131], v[104:107], a[160:163]\n" \
"s_mul_i32 s76, s73, 0x18000\n" \
"s_add_u32 s77, s76, 0xffff8000\n" \
"s_sub_u32 s78, 0x10000, s76\n" \
"v_mfma_f32_16x16x32_bf16 a[160:163], v[132:135], v[108:111], a[160:163]\n" \
"v_add_u32 v48, s77, v48\n" \
"v_add_u32 v49, s77, v49\n" \
"v_mfma_f32_16x16x32_bf16 a[164:167], v[136:139], v[104:107], a[164:167]\n" \
"v_add_u32 v50, s78, v50\n" \
"v_add_u32 v51, s78, v51\n" \
"v_mfma_f32_16x16x32_bf16 a[164:167], v[140:143], v[108:111], a[164:167]\n" \
"v_mfma_f32_16x16x32_bf16 a[168:171], v[144:147], v[104:107], a[168:171]\n" \
"v_mfma_f32_16x16x32_bf16 a[168:171], v[148:151], v[108:111], a[168:171]\n" \
"v_mfma_f32_16x16x32_bf16 a[172:175], v[152:155], v[104:107], a[172:175]\n" \
"v_mfma_f32_16x16x32_bf16 a[172:175], v[156:159], v[108:111], a[172:175]\n" \
"s_waitcnt lgkmcnt(0)\n" \
"v_mfma_f32_16x16x32_bf16 a[192:195], v[128:131], v[112:115], a[192:195]\n" \
"v_mfma_f32_16x16x32_bf16 a[192:195], v[132:135], v[116:119], a[192:195]\n" \
"v_mfma_f32_16x16x32_bf16 a[196:199], v[136:139], v[112:115], a[196:199]\n" \
"v_mfma_f32_16x16x32_bf16 a[196:199], v[140:143], v[116:119], a[196:199]\n" \
"v_mfma_f32_16x16x32_bf16 a[200:203], v[144:147], v[112:115], a[200:203]\n" \
"v_mfma_f32_16x16x32_bf16 a[200:203], v[148:151], v[116:119], a[200:203]\n" \
"v_mfma_f32_16x16x32_bf16 a[204:207], v[152:155], v[112:115], a[204:207]\n" \
"v_mfma_f32_16x16x32_bf16 a[204:207], v[156:159], v[116:119], a[204:207]\n" \
"v_mfma_f32_16x16x32_bf16 a[224:227], v[128:131], v[120:123], a[224:227]\n" \
"v_mfma_f32_16x16x32_bf16 a[224:227], v[132:135], v[124:127], a[224:227]\n" \
"v_mfma_f32_16x16x32_bf16 a[228:231], v[136:139], v[120:123], a[228:231]\n" \
"v_mfma_f32_16x16x32_bf16 a[228:231], v[140:143], v[124:127], a[228:231]\n" \
"v_mfma_f32_16x16x32_bf16 a[232:235], v[144:147], v[120:123], a[232:235]\n" \
"v_mfma_f32_16x16x32_bf16 a[232:235], v[148:151], v[124:127], a[232:235]\n" \
"v_mfma_f32_16x16x32_bf16 a[236:239], v[152:155], v[120:123], a[236:239]\n" \
"s_waitcnt vmcnt(11) lgkmcnt(0)\n" \
"v_mfma_f32_16x16x32_bf16 a[236:239], v[156:159], v[124:127], a[236:239]\n" \
"s_barrier\n" \
"v_mfma_f32_16x16x32_bf16 a[16:19], v[64:67], v[160:163], a[16:19]\n" \
"s_add_u32 m0, s87, 4096\n" \
"ds_read_b64_tr_b16 v[128:129], v50 offset:0\n" \
"v_mfma_f32_16x16x32_bf16 a[16:19], v[68:71], v[164:167], a[16:19]\n" \
"global_load_lds_dwordx4 v32, s[54:55] offset:-4096\n" \
"v_mfma_f32_16x16x32_bf16 a[20:23], v[64:67], v[168:171], a[20:23]\n" \
"ds_read_b64_tr_b16 v[130:131], v50 offset:4096\n" \
"v_mfma_f32_16x16x32_bf16 a[20:23], v[68:71], v[172:175], a[20:23]\n" \
"v_mfma_f32_16x16x32_bf16 a[24:27], v[64:67], v[176:179], a[24:27]\n" \
"ds_read_b64_tr_b16 v[132:133], v50 offset:16384\n" \
"v_mfma_f32_16x16x32_bf16 a[24:27], v[68:71], v[180:183], a[24:27]\n" \
"global_load_lds_dwordx4 v33, s[40:41] offset:-3072\n" \
"v_mfma_f32_16x16x32_bf16 a[28:31], v[64:67], v[184:187], a[28:31]\n" \
"ds_read_b64_tr_b16 v[134:135], v50 offset:20480\n" \
"v_mfma_f32_16x16x32_bf16 a[28:31], v[68:71], v[188:191], a[28:31]\n" \
"v_mfma_f32_16x16x32_bf16 a[48:51], v[72:75], v[160:163], a[48:51]\n" \
"ds_read_b128 v[64:67], v48 offset:0\n" \
"v_mfma_f32_16x16x32_bf16 a[48:51], v[76:79], v[164:167], a[48:51]\n" \
"global_load_lds_dwordx4 v34, s[54:55] offset:-2048\n" \
"v_mfma_f32_16x16x32_bf16 a[52:55], v[72:75], v[168:171], a[52:55]\n" \
"ds_read_b64_tr_b16 v[136:137], v51 offset:0\n" \
"v_mfma_f32_16x16x32_bf16 a[52:55], v[76:79], v[172:175], a[52:55]\n" \
"v_mfma_f32_16x16x32_bf16 a[56:59], v[72:75], v[176:179], a[56:59]\n" \
"ds_read_b128 v[68:71], v49 offset:0\n" \
"v_mfma_f32_16x16x32_bf16 a[56:59], v[76:79], v[180:183], a[56:59]\n" \
"global_load_lds_dwordx4 v35, s[40:41] offset:-1024\n" \
"v_mfma_f32_16x16x32_bf16 a[60:63], v[72:75], v[184:187], a[60:63]\n" \
"ds_read_b64_tr_b16 v[138:139], v51 offset:4096\n" \
"v_mfma_f32_16x16x32_bf16 a[60:63], v[76:79], v[188:191], a[60:63]\n" \
"v_mfma_f32_16x16x32_bf16 a[80:83], v[80:83], v[160:163], a[80:83]\n" \
"ds_read_b128 v[72:75], v48 offset:2048\n" \
"v_mfma_f32_16x16x32_bf16 a[80:83], v[84:87], v[164:167], a[80:83]\n" \
"global_load_lds_dwordx4 v36, s[54:55] offset:0\n" \
"v_mfma_f32_16x16x32_bf16 a[84:87], v[80:83], v[168:171], a[84:87]\n" \
"ds_read_b64_tr_b16 v[140:141], v51 offset:16384\n" \
"v_mfma_f32_16x16x32_bf16 a[84:87], v[84:87], v[172:175], a[84:87]\n" \
"v_mfma_f32_16x16x32_bf16 a[88:91], v[80:83], v[176:179], a[88:91]\n" \
"ds_read_b128 v[76:79], v49 offset:2048\n" \
"v_mfma_f32_16x16x32_bf16 a[88:91], v[84:87], v[180:183], a[88:91]\n" \
"global_load_lds_dwordx4 v37, s[40:41] offset:1024\n" \
"v_mfma_f32_16x16x32_bf16 a[92:95], v[80:83], v[184:187], a[92:95]\n" \
"ds_read_b64_tr_b16 v[142:143], v51 offset:20480\n" \
"v_mfma_f32_16x16x32_bf16 a[92:95], v[84:87], v[188:191], a[92:95]\n" \
"v_mfma_f32_16x16x32_bf16 a[112:115], v[88:91], v[160:163], a[112:115]\n" \
"ds_read_b128 v[80:83], v48 offset:4096\n" \
"v_mfma_f32_16x16x32_bf16 a[112:115], v[92:95], v[164:167], a[112:115]\n" \
"global_load_lds_dwordx4 v38, s[54:55] offset:2048\n" \
"v_mfma_f32_16x16x32_bf16 a[116:119], v[88:91], v[168:171], a[116:119]\n" \
"ds_read_b64_tr_b16 v[144:145], v50 offset:512\n" \
"v_mfma_f32_16x16x32_bf16 a[116:119], v[92:95], v[172:175], a[116:119]\n" \
"v_mfma_f32_16x16x32_bf16 a[120:123], v[88:91], v[176:179], a[120:123]\n" \
"ds_read_b128 v[84:87], v49 offset:4096\n" \
"global_load_lds_dwordx4 v39, s[40:41] offset:3072\n" \
"v_mfma_f32_16x16x32_bf16 a[120:123], v[92:95], v[180:183], a[120:123]\n" \
"s_add_u32 s54, s54, s75\n" \
"s_addc_u32 s55, s55, 0\n" \
"v_mfma_f32_16x16x32_bf16 a[124:127], v[88:91], v[184:187], a[124:127]\n" \
"s_add_u32 s40, s40, s75\n" \
"s_addc_u32 s41, s41, 0\n" \
"ds_read_b64_tr_b16 v[146:147], v50 offset:4608\n" \
"v_mfma_f32_16x16x32_bf16 a[124:127], v[92:95], v[188:191], a[124:127]\n" \
"s_add_u32 s68, s66, 0x200000\n" \
"s_add_u32 s64, s8, s68\n" \
"s_addc_u32 s65, s9, 0\n" \
"v_mfma_f32_16x16x32_bf16 a[144:147], v[96:99], v[160:163], a[144:147]\n" \
"ds_read_b128 v[88:91], v48 offset:6144\n" \
"v_mfma_f32_16x16x32_bf16 a[144:147], v[100:103], v[164:167], a[144:147]\n" \
"global_load_dword v253, v248, s[64:65]\n" \
"v_mfma_f32_16x16x32_bf16 a[148:151], v[96:99], v[168:171], a[148:151]\n" \
"s_add_u32 s68, s67, 0x0\n" \
"ds_read_b64_tr_b16 v[148:149], v50 offset:16896\n" \
"v_mfma_f32_16x16x32_bf16 a[148:151], v[100:103], v[172:175], a[148:151]\n" \
"s_add_u32 s64, s12, s68\n" \
"Lpg1151_%=:\n" \
"Lst0x1_%=:\n" \
"s_cbranch_execz Lpg3309_%=\n" \
"s_addc_u32 s65, s13, 0\n" \
"v_mfma_f32_16x16x32_bf16 a[152:155], v[96:99], v[176:179], a[152:155]\n" \
"ds_read_b128 v[92:95], v49 offset:6144\n" \
"v_mfma_f32_16x16x32_bf16 a[152:155], v[100:103], v[180:183], a[152:155]\n" \
"global_load_dword v253, v248, s[64:65]\n" \
"v_mfma_f32_16x16x32_bf16 a[156:159], v[96:99], v[184:187], a[156:159]\n" \
"ds_read_b64_tr_b16 v[150:151], v50 offset:20992\n" \
"v_mfma_f32_16x16x32_bf16 a[156:159], v[100:103], v[188:191], a[156:159]\n" \
"v_mfma_f32_16x16x32_bf16 a[176:179], v[104:107], v[160:163], a[176:179]\n" \
"ds_read_b128 v[96:99], v48 offset:8192\n" \
"v_mfma_f32_16x16x32_bf16 a[176:179], v[108:111], v[164:167], a[176:179]\n" \
"v_mfma_f32_16x16x32_bf16 a[180:183], v[104:107], v[168:171], a[180:183]\n" \
"ds_read_b64_tr_b16 v[152:153], v51 offset:512\n" \
"v_mfma_f32_16x16x32_bf16 a[180:183], v[108:111], v[172:175], a[180:183]\n" \
"v_mfma_f32_16x16x32_bf16 a[184:187], v[104:107], v[176:179], a[184:187]\n" \
"ds_read_b128 v[100:103], v49 offset:8192\n" \
"v_mfma_f32_16x16x32_bf16 a[184:187], v[108:111], v[180:183], a[184:187]\n" \
"v_mfma_f32_16x16x32_bf16 a[188:191], v[104:107], v[184:187], a[188:191]\n" \
"ds_read_b64_tr_b16 v[154:155], v51 offset:4608\n" \
"v_mfma_f32_16x16x32_bf16 a[188:191], v[108:111], v[188:191], a[188:191]\n" \
"v_mfma_f32_16x16x32_bf16 a[208:211], v[112:115], v[160:163], a[208:211]\n" \
"ds_read_b128 v[104:107], v48 offset:10240\n" \
"v_mfma_f32_16x16x32_bf16 a[208:211], v[116:119], v[164:167], a[208:211]\n" \
"v_mfma_f32_16x16x32_bf16 a[212:215], v[112:115], v[168:171], a[212:215]\n" \
"ds_read_b64_tr_b16 v[156:157], v51 offset:16896\n" \
"v_mfma_f32_16x16x32_bf16 a[212:215], v[116:119], v[172:175], a[212:215]\n" \
"v_mfma_f32_16x16x32_bf16 a[216:219], v[112:115], v[176:179], a[216:219]\n" \
"ds_read_b128 v[108:111], v49 offset:10240\n" \
"v_mfma_f32_16x16x32_bf16 a[216:219], v[116:119], v[180:183], a[216:219]\n" \
"v_mfma_f32_16x16x32_bf16 a[220:223], v[112:115], v[184:187], a[220:223]\n" \
"ds_read_b64_tr_b16 v[158:159], v51 offset:20992\n" \
"v_mfma_f32_16x16x32_bf16 a[220:223], v[116:119], v[188:191], a[220:223]\n" \
"v_mfma_f32_16x16x32_bf16 a[240:243], v[120:123], v[160:163], a[240:243]\n" \
"ds_read_b128 v[112:115], v48 offset:12288\n" \
"v_mfma_f32_16x16x32_bf16 a[240:243], v[124:127], v[164:167], a[240:243]\n" \
"v_mfma_f32_16x16x32_bf16 a[244:247], v[120:123], v[168:171], a[244:247]\n" \
"v_mfma_f32_16x16x32_bf16 a[244:247], v[124:127], v[172:175], a[244:247]\n" \
"v_mfma_f32_16x16x32_bf16 a[248:251], v[120:123], v[176:179], a[248:251]\n" \
"ds_read_b128 v[116:119], v49 offset:12288\n" \
"v_mfma_f32_16x16x32_bf16 a[248:251], v[124:127], v[180:183], a[248:251]\n" \
"v_mfma_f32_16x16x32_bf16 a[252:255], v[120:123], v[184:187], a[252:255]\n" \
"v_mfma_f32_16x16x32_bf16 a[252:255], v[124:127], v[188:191], a[252:255]\n" \
"s_waitcnt lgkmcnt(1)\n" \
"v_mfma_f32_16x16x32_bf16 a[0:3], v[128:131], v[64:67], a[0:3]\n" \
"ds_read_b128 v[120:123], v48 offset:14336\n" \
"v_mfma_f32_16x16x32_bf16 a[0:3], v[132:135], v[68:71], a[0:3]\n" \
"v_mfma_f32_16x16x32_bf16 a[4:7], v[136:139], v[64:67], a[4:7]\n" \
"ds_read_b128 v[124:127], v49 offset:14336\n" \
"v_mfma_f32_16x16x32_bf16 a[4:7], v[140:143], v[68:71], a[4:7]\n" \
"s_add_u32 s68, s66, 0x300000\n" \
"v_mfma_f32_16x16x32_bf16 a[8:11], v[144:147], v[64:67], a[8:11]\n" \
"s_add_u32 s64, s8, s68\n" \
"s_addc_u32 s65, s9, 0\n" \
"ds_read_b64_tr_b16 v[160:161], v50 offset:1024\n" \
"v_mfma_f32_16x16x32_bf16 a[8:11], v[148:151], v[68:71], a[8:11]\n" \
"global_load_dword v253, v248, s[64:65]\n" \
"v_mfma_f32_16x16x32_bf16 a[12:15], v[152:155], v[64:67], a[12:15]\n" \
"s_add_u32 m0, s87, 135168\n" \
"ds_read_b64_tr_b16 v[162:163], v50 offset:5120\n" \
"v_mfma_f32_16x16x32_bf16 a[12:15], v[156:159], v[68:71], a[12:15]\n" \
"global_load_lds_dwordx4 v40, s[52:53] offset:-4096\n" \
"v_mfma_f32_16x16x32_bf16 a[32:35], v[128:131], v[72:75], a[32:35]\n" \
"ds_read_b64_tr_b16 v[164:165], v50 offset:17408\n" \
"v_mfma_f32_16x16x32_bf16 a[32:35], v[132:135], v[76:79], a[32:35]\n" \
"v_mfma_f32_16x16x32_bf16 a[36:39], v[136:139], v[72:75], a[36:39]\n" \
"ds_read_b64_tr_b16 v[166:167], v50 offset:21504\n" \
"v_mfma_f32_16x16x32_bf16 a[36:39], v[140:143], v[76:79], a[36:39]\n" \
"global_load_lds_dwordx4 v41, s[50:51] offset:-3072\n" \
"v_mfma_f32_16x16x32_bf16 a[40:43], v[144:147], v[72:75], a[40:43]\n" \
"ds_read_b64_tr_b16 v[168:169], v51 offset:1024\n" \
"v_mfma_f32_16x16x32_bf16 a[40:43], v[148:151], v[76:79], a[40:43]\n" \
"v_mfma_f32_16x16x32_bf16 a[44:47], v[152:155], v[72:75], a[44:47]\n" \
"ds_read_b64_tr_b16 v[170:171], v51 offset:5120\n" \
"v_mfma_f32_16x16x32_bf16 a[44:47], v[156:159], v[76:79], a[44:47]\n" \
"global_load_lds_dwordx4 v42, s[52:53] offset:-2048\n" \
"v_mfma_f32_16x16x32_bf16 a[64:67], v[128:131], v[80:83], a[64:67]\n" \
"ds_read_b64_tr_b16 v[172:173], v51 offset:17408\n" \
"v_mfma_f32_16x16x32_bf16 a[64:67], v[132:135], v[84:87], a[64:67]\n" \
"v_mfma_f32_16x16x32_bf16 a[68:71], v[136:139], v[80:83], a[68:71]\n" \
"ds_read_b64_tr_b16 v[174:175], v51 offset:21504\n" \
"v_mfma_f32_16x16x32_bf16 a[68:71], v[140:143], v[84:87], a[68:71]\n" \
"global_load_lds_dwordx4 v43, s[50:51] offset:-1024\n" \
"v_mfma_f32_16x16x32_bf16 a[72:75], v[144:147], v[80:83], a[72:75]\n" \
"ds_read_b64_tr_b16 v[176:177], v50 offset:1536\n" \
"v_mfma_f32_16x16x32_bf16 a[72:75], v[148:151], v[84:87], a[72:75]\n" \
"v_mfma_f32_16x16x32_bf16 a[76:79], v[152:155], v[80:83], a[76:79]\n" \
"ds_read_b64_tr_b16 v[178:179], v50 offset:5632\n" \
"v_mfma_f32_16x16x32_bf16 a[76:79], v[156:159], v[84:87], a[76:79]\n" \
"global_load_lds_dwordx4 v44, s[52:53] offset:0\n" \
"v_mfma_f32_16x16x32_bf16 a[96:99], v[128:131], v[88:91], a[96:99]\n" \
"ds_read_b64_tr_b16 v[180:181], v50 offset:17920\n" \
"v_mfma_f32_16x16x32_bf16 a[96:99], v[132:135], v[92:95], a[96:99]\n" \
"v_mfma_f32_16x16x32_bf16 a[100:103], v[136:139], v[88:91], a[100:103]\n" \
"ds_read_b64_tr_b16 v[182:183], v50 offset:22016\n" \
"v_mfma_f32_16x16x32_bf16 a[100:103], v[140:143], v[92:95], a[100:103]\n" \
"global_load_lds_dwordx4 v45, s[50:51] offset:1024\n" \
"v_mfma_f32_16x16x32_bf16 a[104:107], v[144:147], v[88:91], a[104:107]\n" \
"ds_read_b64_tr_b16 v[184:185], v51 offset:1536\n" \
"v_mfma_f32_16x16x32_bf16 a[104:107], v[148:151], v[92:95], a[104:107]\n" \
"v_mfma_f32_16x16x32_bf16 a[108:111], v[152:155], v[88:91], a[108:111]\n" \
"ds_read_b64_tr_b16 v[186:187], v51 offset:5632\n" \
"v_mfma_f32_16x16x32_bf16 a[108:111], v[156:159], v[92:95], a[108:111]\n" \
"global_load_lds_dwordx4 v46, s[52:53] offset:2048\n" \
"v_mfma_f32_16x16x32_bf16 a[128:131], v[128:131], v[96:99], a[128:131]\n" \
"ds_read_b64_tr_b16 v[188:189], v51 offset:17920\n" \
"v_mfma_f32_16x16x32_bf16 a[128:131], v[132:135], v[100:103], a[128:131]\n" \
"v_mfma_f32_16x16x32_bf16 a[132:135], v[136:139], v[96:99], a[132:135]\n" \
"ds_read_b64_tr_b16 v[190:191], v51 offset:22016\n" \
"v_mfma_f32_16x16x32_bf16 a[132:135], v[140:143], v[100:103], a[132:135]\n" \
"global_load_lds_dwordx4 v47, s[50:51] offset:3072\n" \
"v_mfma_f32_16x16x32_bf16 a[136:139], v[144:147], v[96:99], a[136:139]\n" \
"s_add_u32 s52, s52, s74\n" \
"s_addc_u32 s53, s53, 0\n" \
"v_mfma_f32_16x16x32_bf16 a[136:139], v[148:151], v[100:103], a[136:139]\n" \
"s_add_u32 s50, s50, s74\n" \
"s_addc_u32 s51, s51, 0\n" \
"v_mfma_f32_16x16x32_bf16 a[140:143], v[152:155], v[96:99], a[140:143]\n" \
"v_mfma_f32_16x16x32_bf16 a[140:143], v[156:159], v[100:103], a[140:143]\n" \
"v_mfma_f32_16x16x32_bf16 a[160:163], v[128:131], v[104:107], a[160:163]\n" \
"s_mul_i32 s76, s73, 0x10000\n" \
"s_add_u32 s77, s76, 0xfffe8000\n" \
"s_sub_u32 s78, 0xffff8000, s76\n" \
"v_mfma_f32_16x16x32_bf16 a[160:163], v[132:135], v[108:111], a[160:163]\n" \
"v_add_u32 v48, s77, v48\n" \
"v_add_u32 v49, s77, v49\n" \
"v_mfma_f32_16x16x32_bf16 a[164:167], v[136:139], v[104:107], a[164:167]\n" \
"v_add_u32 v50, s78, v50\n" \
"v_add_u32 v51, s78, v51\n" \
"v_mfma_f32_16x16x32_bf16 a[164:167], v[140:143], v[108:111], a[164:167]\n" \
"v_mfma_f32_16x16x32_bf16 a[168:171], v[144:147], v[104:107], a[168:171]\n" \
"v_mfma_f32_16x16x32_bf16 a[168:171], v[148:151], v[108:111], a[168:171]\n" \
"v_mfma_f32_16x16x32_bf16 a[172:175], v[152:155], v[104:107], a[172:175]\n" \
"v_mfma_f32_16x16x32_bf16 a[172:175], v[156:159], v[108:111], a[172:175]\n" \
"s_waitcnt lgkmcnt(0)\n" \
"v_mfma_f32_16x16x32_bf16 a[192:195], v[128:131], v[112:115], a[192:195]\n" \
"v_mfma_f32_16x16x32_bf16 a[192:195], v[132:135], v[116:119], a[192:195]\n" \
"v_mfma_f32_16x16x32_bf16 a[196:199], v[136:139], v[112:115], a[196:199]\n" \
"v_mfma_f32_16x16x32_bf16 a[196:199], v[140:143], v[116:119], a[196:199]\n" \
"v_mfma_f32_16x16x32_bf16 a[200:203], v[144:147], v[112:115], a[200:203]\n" \
"v_mfma_f32_16x16x32_bf16 a[200:203], v[148:151], v[116:119], a[200:203]\n" \
"v_mfma_f32_16x16x32_bf16 a[204:207], v[152:155], v[112:115], a[204:207]\n" \
"v_mfma_f32_16x16x32_bf16 a[204:207], v[156:159], v[116:119], a[204:207]\n" \
"v_mfma_f32_16x16x32_bf16 a[224:227], v[128:131], v[120:123], a[224:227]\n" \
"v_mfma_f32_16x16x32_bf16 a[224:227], v[132:135], v[124:127], a[224:227]\n" \
"v_mfma_f32_16x16x32_bf16 a[228:231], v[136:139], v[120:123], a[228:231]\n" \
"v_mfma_f32_16x16x32_bf16 a[228:231], v[140:143], v[124:127], a[228:231]\n" \
"v_mfma_f32_16x16x32_bf16 a[232:235], v[144:147], v[120:123], a[232:235]\n" \
"v_mfma_f32_16x16x32_bf16 a[232:235], v[148:151], v[124:127], a[232:235]\n" \
"v_mfma_f32_16x16x32_bf16 a[236:239], v[152:155], v[120:123], a[236:239]\n" \
"s_waitcnt vmcnt(11) lgkmcnt(0)\n" \
"v_mfma_f32_16x16x32_bf16 a[236:239], v[156:159], v[124:127], a[236:239]\n" \
"s_barrier\n" \
"v_mfma_f32_16x16x32_bf16 a[16:19], v[64:67], v[160:163], a[16:19]\n" \
"s_add_u32 m0, s87, 69632\n" \
"ds_read_b64_tr_b16 v[128:129], v50 offset:0\n" \
"v_mfma_f32_16x16x32_bf16 a[16:19], v[68:71], v[164:167], a[16:19]\n" \
"global_load_lds_dwordx4 v32, s[54:55] offset:-4096\n" \
"v_mfma_f32_16x16x32_bf16 a[20:23], v[64:67], v[168:171], a[20:23]\n" \
"ds_read_b64_tr_b16 v[130:131], v50 offset:4096\n" \
"v_mfma_f32_16x16x32_bf16 a[20:23], v[68:71], v[172:175], a[20:23]\n" \
"v_mfma_f32_16x16x32_bf16 a[24:27], v[64:67], v[176:179], a[24:27]\n" \
"ds_read_b64_tr_b16 v[132:133], v50 offset:16384\n" \
"v_mfma_f32_16x16x32_bf16 a[24:27], v[68:71], v[180:183], a[24:27]\n" \
"global_load_lds_dwordx4 v33, s[40:41] offset:-3072\n" \
"v_mfma_f32_16x16x32_bf16 a[28:31], v[64:67], v[184:187], a[28:31]\n" \
"ds_read_b64_tr_b16 v[134:135], v50 offset:20480\n" \
"v_mfma_f32_16x16x32_bf16 a[28:31], v[68:71], v[188:191], a[28:31]\n" \
"v_mfma_f32_16x16x32_bf16 a[48:51], v[72:75], v[160:163], a[48:51]\n" \
"ds_read_b128 v[64:67], v48 offset:0\n" \
"v_mfma_f32_16x16x32_bf16 a[48:51], v[76:79], v[164:167], a[48:51]\n" \
"global_load_lds_dwordx4 v34, s[54:55] offset:-2048\n" \
"v_mfma_f32_16x16x32_bf16 a[52:55], v[72:75], v[168:171], a[52:55]\n" \
"ds_read_b64_tr_b16 v[136:137], v51 offset:0\n" \
"v_mfma_f32_16x16x32_bf16 a[52:55], v[76:79], v[172:175], a[52:55]\n" \
"v_mfma_f32_16x16x32_bf16 a[56:59], v[72:75], v[176:179], a[56:59]\n" \
"ds_read_b128 v[68:71], v49 offset:0\n" \
"v_mfma_f32_16x16x32_bf16 a[56:59], v[76:79], v[180:183], a[56:59]\n" \
"global_load_lds_dwordx4 v35, s[40:41] offset:-1024\n" \
"v_mfma_f32_16x16x32_bf16 a[60:63], v[72:75], v[184:187], a[60:63]\n" \
"ds_read_b64_tr_b16 v[138:139], v51 offset:4096\n" \
"v_mfma_f32_16x16x32_bf16 a[60:63], v[76:79], v[188:191], a[60:63]\n" \
"v_mfma_f32_16x16x32_bf16 a[80:83], v[80:83], v[160:163], a[80:83]\n" \
"ds_read_b128 v[72:75], v48 offset:2048\n" \
"v_mfma_f32_16x16x32_bf16 a[80:83], v[84:87], v[164:167], a[80:83]\n" \
"global_load_lds_dwordx4 v36, s[54:55] offset:0\n" \
"v_mfma_f32_16x16x32_bf16 a[84:87], v[80:83], v[168:171], a[84:87]\n" \
"ds_read_b64_tr_b16 v[140:141], v51 offset:16384\n" \
"v_mfma_f32_16x16x32_bf16 a[84:87], v[84:87], v[172:175], a[84:87]\n" \
"v_mfma_f32_16x16x32_bf16 a[88:91], v[80:83], v[176:179], a[88:91]\n" \
"ds_read_b128 v[76:79], v49 offset:2048\n" \
"v_mfma_f32_16x16x32_bf16 a[88:91], v[84:87], v[180:183], a[88:91]\n" \
"global_load_lds_dwordx4 v37, s[40:41] offset:1024\n" \
"v_mfma_f32_16x16x32_bf16 a[92:95], v[80:83], v[184:187], a[92:95]\n" \
"ds_read_b64_tr_b16 v[142:143], v51 offset:20480\n" \
"v_mfma_f32_16x16x32_bf16 a[92:95], v[84:87], v[188:191], a[92:95]\n" \
"v_mfma_f32_16x16x32_bf16 a[112:115], v[88:91], v[160:163], a[112:115]\n" \
"ds_read_b128 v[80:83], v48 offset:4096\n" \
"v_mfma_f32_16x16x32_bf16 a[112:115], v[92:95], v[164:167], a[112:115]\n" \
"global_load_lds_dwordx4 v38, s[54:55] offset:2048\n" \
"v_mfma_f32_16x16x32_bf16 a[116:119], v[88:91], v[168:171], a[116:119]\n" \
"ds_read_b64_tr_b16 v[144:145], v50 offset:512\n" \
"v_mfma_f32_16x16x32_bf16 a[116:119], v[92:95], v[172:175], a[116:119]\n" \
"v_mfma_f32_16x16x32_bf16 a[120:123], v[88:91], v[176:179], a[120:123]\n" \
"ds_read_b128 v[84:87], v49 offset:4096\n" \
"global_load_lds_dwordx4 v39, s[40:41] offset:3072\n" \
"v_mfma_f32_16x16x32_bf16 a[120:123], v[92:95], v[180:183], a[120:123]\n" \
"s_add_u32 s54, s54, s75\n" \
"s_addc_u32 s55, s55, 0\n" \
"v_mfma_f32_16x16x32_bf16 a[124:127], v[88:91], v[184:187], a[124:127]\n" \
"s_add_u32 s40, s40, s75\n" \
"s_addc_u32 s41, s41, 0\n" \
"ds_read_b64_tr_b16 v[146:147], v50 offset:4608\n" \
"v_mfma_f32_16x16x32_bf16 a[124:127], v[92:95], v[188:191], a[124:127]\n" \
"s_add_u32 s68, s67, 0x100000\n" \
"s_add_u32 s64, s12, s68\n" \
"s_addc_u32 s65, s13, 0\n" \
"v_mfma_f32_16x16x32_bf16 a[144:147], v[96:99], v[160:163], a[144:147]\n" \
"ds_read_b128 v[88:91], v48 offset:6144\n" \
"v_mfma_f32_16x16x32_bf16 a[144:147], v[100:103], v[164:167], a[144:147]\n" \
"global_load_dword v253, v248, s[64:65]\n" \
"v_mfma_f32_16x16x32_bf16 a[148:151], v[96:99], v[168:171], a[148:151]\n" \
"s_add_u32 s68, s67, 0x500000\n" \
"ds_read_b64_tr_b16 v[148:149], v50 offset:16896\n" \
"v_mfma_f32_16x16x32_bf16 a[148:151], v[100:103], v[172:175], a[148:151]\n" \
"s_add_u32 s64, s12, s68\n" \
"s_addc_u32 s65, s13, 0\n" \
"v_mfma_f32_16x16x32_bf16 a[152:155], v[96:99], v[176:179], a[152:155]\n" \
"ds_read_b128 v[92:95], v49 offset:6144\n" \
"v_mfma_f32_16x16x32_bf16 a[152:155], v[100:103], v[180:183], a[152:155]\n" \
"global_load_dword v253, v248, s[64:65]\n" \
"v_mfma_f32_16x16x32_bf16 a[156:159], v[96:99], v[184:187], a[156:159]\n" \
"ds_read_b64_tr_b16 v[150:151], v50 offset:20992\n" \
"v_mfma_f32_16x16x32_bf16 a[156:159], v[100:103], v[188:191], a[156:159]\n" \
"v_mfma_f32_16x16x32_bf16 a[176:179], v[104:107], v[160:163], a[176:179]\n" \
"ds_read_b128 v[96:99], v48 offset:8192\n" \
"v_mfma_f32_16x16x32_bf16 a[176:179], v[108:111], v[164:167], a[176:179]\n" \
"v_mfma_f32_16x16x32_bf16 a[180:183], v[104:107], v[168:171], a[180:183]\n" \
"ds_read_b64_tr_b16 v[152:153], v51 offset:512\n" \
"v_mfma_f32_16x16x32_bf16 a[180:183], v[108:111], v[172:175], a[180:183]\n" \
"v_mfma_f32_16x16x32_bf16 a[184:187], v[104:107], v[176:179], a[184:187]\n" \
"ds_read_b128 v[100:103], v49 offset:8192\n" \
"v_mfma_f32_16x16x32_bf16 a[184:187], v[108:111], v[180:183], a[184:187]\n" \
"v_mfma_f32_16x16x32_bf16 a[188:191], v[104:107], v[184:187], a[188:191]\n" \
"ds_read_b64_tr_b16 v[154:155], v51 offset:4608\n" \
"v_mfma_f32_16x16x32_bf16 a[188:191], v[108:111], v[188:191], a[188:191]\n" \
"v_mfma_f32_16x16x32_bf16 a[208:211], v[112:115], v[160:163], a[208:211]\n" \
"ds_read_b128 v[104:107], v48 offset:10240\n" \
"v_mfma_f32_16x16x32_bf16 a[208:211], v[116:119], v[164:167], a[208:211]\n" \
"v_mfma_f32_16x16x32_bf16 a[212:215], v[112:115], v[168:171], a[212:215]\n" \
"ds_read_b64_tr_b16 v[156:157], v51 offset:16896\n" \
"v_mfma_f32_16x16x32_bf16 a[212:215], v[116:119], v[172:175], a[212:215]\n" \
"v_mfma_f32_16x16x32_bf16 a[216:219], v[112:115], v[176:179], a[216:219]\n" \
"ds_read_b128 v[108:111], v49 offset:10240\n" \
"v_mfma_f32_16x16x32_bf16 a[216:219], v[116:119], v[180:183], a[216:219]\n" \
"v_mfma_f32_16x16x32_bf16 a[220:223], v[112:115], v[184:187], a[220:223]\n" \
"ds_read_b64_tr_b16 v[158:159], v51 offset:20992\n" \
"v_mfma_f32_16x16x32_bf16 a[220:223], v[116:119], v[188:191], a[220:223]\n" \
"v_mfma_f32_16x16x32_bf16 a[240:243], v[120:123], v[160:163], a[240:243]\n" \
"ds_read_b128 v[112:115], v48 offset:12288\n" \
"v_mfma_f32_16x16x32_bf16 a[240:243], v[124:127], v[164:167], a[240:243]\n" \
"v_mfma_f32_16x16x32_bf16 a[244:247], v[120:123], v[168:171], a[244:247]\n" \
"v_mfma_f32_16x16x32_bf16 a[244:247], v[124:127], v[172:175], a[244:247]\n" \
"v_mfma_f32_16x16x32_bf16 a[248:251], v[120:123], v[176:179], a[248:251]\n" \
"ds_read_b128 v[116:119], v49 offset:12288\n" \
"v_mfma_f32_16x16x32_bf16 a[248:251], v[124:127], v[180:183], a[248:251]\n" \
"v_mfma_f32_16x16x32_bf16 a[252:255], v[120:123], v[184:187], a[252:255]\n" \
"v_mfma_f32_16x16x32_bf16 a[252:255], v[124:127], v[188:191], a[252:255]\n" \
"s_waitcnt lgkmcnt(1)\n" \
"v_mfma_f32_16x16x32_bf16 a[0:3], v[128:131], v[64:67], a[0:3]\n" \
"ds_read_b128 v[120:123], v48 offset:14336\n" \
"v_mfma_f32_16x16x32_bf16 a[0:3], v[132:135], v[68:71], a[0:3]\n" \
"v_mfma_f32_16x16x32_bf16 a[4:7], v[136:139], v[64:67], a[4:7]\n" \
"ds_read_b128 v[124:127], v49 offset:14336\n" \
"v_mfma_f32_16x16x32_bf16 a[4:7], v[140:143], v[68:71], a[4:7]\n" \
"s_add_u32 s68, s67, 0x200000\n" \
"v_mfma_f32_16x16x32_bf16 a[8:11], v[144:147], v[64:67], a[8:11]\n" \
"s_add_u32 s64, s12, s68\n" \
"s_addc_u32 s65, s13, 0\n" \
"ds_read_b64_tr_b16 v[160:161], v50 offset:1024\n" \
"v_mfma_f32_16x16x32_bf16 a[8:11], v[148:151], v[68:71], a[8:11]\n" \
"global_load_dword v253, v248, s[64:65]\n" \
"v_mfma_f32_16x16x32_bf16 a[12:15], v[152:155], v[64:67], a[12:15]\n" \
"s_add_u32 m0, s87, 102400\n" \
"ds_read_b64_tr_b16 v[162:163], v50 offset:5120\n" \
"v_mfma_f32_16x16x32_bf16 a[12:15], v[156:159], v[68:71], a[12:15]\n" \
"global_load_lds_dwordx4 v40, s[52:53] offset:-4096\n" \
"v_mfma_f32_16x16x32_bf16 a[32:35], v[128:131], v[72:75], a[32:35]\n" \
"ds_read_b64_tr_b16 v[164:165], v50 offset:17408\n" \
"v_mfma_f32_16x16x32_bf16 a[32:35], v[132:135], v[76:79], a[32:35]\n" \
"v_mfma_f32_16x16x32_bf16 a[36:39], v[136:139], v[72:75], a[36:39]\n" \
"ds_read_b64_tr_b16 v[166:167], v50 offset:21504\n" \
"v_mfma_f32_16x16x32_bf16 a[36:39], v[140:143], v[76:79], a[36:39]\n" \
"global_load_lds_dwordx4 v41, s[50:51] offset:-3072\n" \
"v_mfma_f32_16x16x32_bf16 a[40:43], v[144:147], v[72:75], a[40:43]\n" \
"ds_read_b64_tr_b16 v[168:169], v51 offset:1024\n" \
"v_mfma_f32_16x16x32_bf16 a[40:43], v[148:151], v[76:79], a[40:43]\n" \
"v_mfma_f32_16x16x32_bf16 a[44:47], v[152:155], v[72:75], a[44:47]\n" \
"ds_read_b64_tr_b16 v[170:171], v51 offset:5120\n" \
"v_mfma_f32_16x16x32_bf16 a[44:47], v[156:159], v[76:79], a[44:47]\n" \
"global_load_lds_dwordx4 v42, s[52:53] offset:-2048\n" \
"v_mfma_f32_16x16x32_bf16 a[64:67], v[128:131], v[80:83], a[64:67]\n" \
"ds_read_b64_tr_b16 v[172:173], v51 offset:17408\n" \
"v_mfma_f32_16x16x32_bf16 a[64:67], v[132:135], v[84:87], a[64:67]\n" \
"v_mfma_f32_16x16x32_bf16 a[68:71], v[136:139], v[80:83], a[68:71]\n" \
"ds_read_b64_tr_b16 v[174:175], v51 offset:21504\n" \
"v_mfma_f32_16x16x32_bf16 a[68:71], v[140:143], v[84:87], a[68:71]\n" \
"global_load_lds_dwordx4 v43, s[50:51] offset:-1024\n" \
"v_mfma_f32_16x16x32_bf16 a[72:75], v[144:147], v[80:83], a[72:75]\n" \
"ds_read_b64_tr_b16 v[176:177], v50 offset:1536\n" \
"v_mfma_f32_16x16x32_bf16 a[72:75], v[148:151], v[84:87], a[72:75]\n" \
"v_mfma_f32_16x16x32_bf16 a[76:79], v[152:155], v[80:83], a[76:79]\n" \
"ds_read_b64_tr_b16 v[178:179], v50 offset:5632\n" \
"v_mfma_f32_16x16x32_bf16 a[76:79], v[156:159], v[84:87], a[76:79]\n" \
"global_load_lds_dwordx4 v44, s[52:53] offset:0\n" \
"v_mfma_f32_16x16x32_bf16 a[96:99], v[128:131], v[88:91], a[96:99]\n" \
"ds_read_b64_tr_b16 v[180:181], v50 offset:17920\n" \
"v_mfma_f32_16x16x32_bf16 a[96:99], v[132:135], v[92:95], a[96:99]\n" \
"v_mfma_f32_16x16x32_bf16 a[100:103], v[136:139], v[88:91], a[100:103]\n" \
"ds_read_b64_tr_b16 v[182:183], v50 offset:22016\n" \
"v_mfma_f32_16x16x32_bf16 a[100:103], v[140:143], v[92:95], a[100:103]\n" \
"global_load_lds_dwordx4 v45, s[50:51] offset:1024\n" \
"v_mfma_f32_16x16x32_bf16 a[104:107], v[144:147], v[88:91], a[104:107]\n" \
"ds_read_b64_tr_b16 v[184:185], v51 offset:1536\n" \
"v_mfma_f32_16x16x32_bf16 a[104:107], v[148:151], v[92:95], a[104:107]\n" \
"v_mfma_f32_16x16x32_bf16 a[108:111], v[152:155], v[88:91], a[108:111]\n" \
"ds_read_b64_tr_b16 v[186:187], v51 offset:5632\n" \
"v_mfma_f32_16x16x32_bf16 a[108:111], v[156:159], v[92:95], a[108:111]\n" \
"global_load_lds_dwordx4 v46, s[52:53] offset:2048\n" \
"v_mfma_f32_16x16x32_bf16 a[128:131], v[128:131], v[96:99], a[128:131]\n" \
"ds_read_b64_tr_b16 v[188:189], v51 offset:17920\n" \
"v_mfma_f32_16x16x32_bf16 a[128:131], v[132:135], v[100:103], a[128:131]\n" \
"v_mfma_f32_16x16x32_bf16 a[132:135], v[136:139], v[96:99], a[132:135]\n" \
"ds_read_b64_tr_b16 v[190:191], v51 offset:22016\n" \
"v_mfma_f32_16x16x32_bf16 a[132:135], v[140:143], v[100:103], a[132:135]\n" \
"global_load_lds_dwordx4 v47, s[50:51] offset:3072\n" \
"v_mfma_f32_16x16x32_bf16 a[136:139], v[144:147], v[96:99], a[136:139]\n" \
"v_mfma_f32_16x16x32_bf16 a[136:139], v[148:151], v[100:103], a[136:139]\n" \
"v_mfma_f32_16x16x32_bf16 a[140:143], v[152:155], v[96:99], a[140:143]\n" \
"v_mfma_f32_16x16x32_bf16 a[140:143], v[156:159], v[100:103], a[140:143]\n" \
"v_mfma_f32_16x16x32_bf16 a[160:163], v[128:131], v[104:107], a[160:163]\n" \
"s_mul_i32 s76, s73, 0x8000\n" \
"s_add_u32 s77, s76, 0x10000\n" \
"s_sub_u32 s78, 0x18000, s76\n" \
"v_mfma_f32_16x16x32_bf16 a[160:163], v[132:135], v[108:111], a[160:163]\n" \
"v_add_u32 v48, s77, v48\n" \
"v_add_u32 v49, s77, v49\n" \
"v_mfma_f32_16x16x32_bf16 a[164:167], v[136:139], v[104:107], a[164:167]\n" \
"v_add_u32 v50, s78, v50\n" \
"v_add_u32 v51, s78, v51\n" \
"v_mfma_f32_16x16x32_bf16 a[164:167], v[140:143], v[108:111], a[164:167]\n" \
"v_mfma_f32_16x16x32_bf16 a[168:171], v[144:147], v[104:107], a[168:171]\n" \
"v_mfma_f32_16x16x32_bf16 a[168:171], v[148:151], v[108:111], a[168:171]\n" \
"v_mfma_f32_16x16x32_bf16 a[172:175], v[152:155], v[104:107], a[172:175]\n" \
"v_mfma_f32_16x16x32_bf16 a[172:175], v[156:159], v[108:111], a[172:175]\n" \
"s_waitcnt lgkmcnt(0)\n" \
"v_mfma_f32_16x16x32_bf16 a[192:195], v[128:131], v[112:115], a[192:195]\n" \
"v_mfma_f32_16x16x32_bf16 a[192:195], v[132:135], v[116:119], a[192:195]\n" \
"v_mfma_f32_16x16x32_bf16 a[196:199], v[136:139], v[112:115], a[196:199]\n" \
"v_mfma_f32_16x16x32_bf16 a[196:199], v[140:143], v[116:119], a[196:199]\n" \
"v_mfma_f32_16x16x32_bf16 a[200:203], v[144:147], v[112:115], a[200:203]\n" \
"v_mfma_f32_16x16x32_bf16 a[200:203], v[148:151], v[116:119], a[200:203]\n" \
"v_mfma_f32_16x16x32_bf16 a[204:207], v[152:155], v[112:115], a[204:207]\n" \
"v_mfma_f32_16x16x32_bf16 a[204:207], v[156:159], v[116:119], a[204:207]\n" \
"v_mfma_f32_16x16x32_bf16 a[224:227], v[128:131], v[120:123], a[224:227]\n" \
"v_mfma_f32_16x16x32_bf16 a[224:227], v[132:135], v[124:127], a[224:227]\n" \
"v_mfma_f32_16x16x32_bf16 a[228:231], v[136:139], v[120:123], a[228:231]\n" \
"v_mfma_f32_16x16x32_bf16 a[228:231], v[140:143], v[124:127], a[228:231]\n" \
"v_mfma_f32_16x16x32_bf16 a[232:235], v[144:147], v[120:123], a[232:235]\n" \
"v_mfma_f32_16x16x32_bf16 a[232:235], v[148:151], v[124:127], a[232:235]\n" \
"v_mfma_f32_16x16x32_bf16 a[236:239], v[152:155], v[120:123], a[236:239]\n" \
"s_waitcnt vmcnt(11) lgkmcnt(0)\n" \
"v_mfma_f32_16x16x32_bf16 a[236:239], v[156:159], v[124:127], a[236:239]\n" \
"s_barrier\n" \
"v_mfma_f32_16x16x32_bf16 a[16:19], v[64:67], v[160:163], a[16:19]\n" \
"s_add_u32 m0, s87, 36864\n" \
"ds_read_b64_tr_b16 v[128:129], v50 offset:0\n" \
"v_mfma_f32_16x16x32_bf16 a[16:19], v[68:71], v[164:167], a[16:19]\n" \
"global_load_lds_dwordx4 v32, s[54:55] offset:-4096\n" \
"v_mfma_f32_16x16x32_bf16 a[20:23], v[64:67], v[168:171], a[20:23]\n" \
"ds_read_b64_tr_b16 v[130:131], v50 offset:4096\n" \
"v_mfma_f32_16x16x32_bf16 a[20:23], v[68:71], v[172:175], a[20:23]\n" \
"v_mfma_f32_16x16x32_bf16 a[24:27], v[64:67], v[176:179], a[24:27]\n" \
"ds_read_b64_tr_b16 v[132:133], v50 offset:16384\n" \
"v_mfma_f32_16x16x32_bf16 a[24:27], v[68:71], v[180:183], a[24:27]\n" \
"global_load_lds_dwordx4 v33, s[40:41] offset:-3072\n" \
"v_mfma_f32_16x16x32_bf16 a[28:31], v[64:67], v[184:187], a[28:31]\n" \
"ds_read_b64_tr_b16 v[134:135], v50 offset:20480\n" \
"v_mfma_f32_16x16x32_bf16 a[28:31], v[68:71], v[188:191], a[28:31]\n" \
"v_mfma_f32_16x16x32_bf16 a[48:51], v[72:75], v[160:163], a[48:51]\n" \
"ds_read_b128 v[64:67], v48 offset:0\n" \
"v_mfma_f32_16x16x32_bf16 a[48:51], v[76:79], v[164:167], a[48:51]\n" \
"global_load_lds_dwordx4 v34, s[54:55] offset:-2048\n" \
"v_mfma_f32_16x16x32_bf16 a[52:55], v[72:75], v[168:171], a[52:55]\n" \
"ds_read_b64_tr_b16 v[136:137], v51 offset:0\n" \
"v_mfma_f32_16x16x32_bf16 a[52:55], v[76:79], v[172:175], a[52:55]\n" \
"v_mfma_f32_16x16x32_bf16 a[56:59], v[72:75], v[176:179], a[56:59]\n" \
"ds_read_b128 v[68:71], v49 offset:0\n" \
"v_mfma_f32_16x16x32_bf16 a[56:59], v[76:79], v[180:183], a[56:59]\n" \
"global_load_lds_dwordx4 v35, s[40:41] offset:-1024\n" \
"v_mfma_f32_16x16x32_bf16 a[60:63], v[72:75], v[184:187], a[60:63]\n" \
"ds_read_b64_tr_b16 v[138:139], v51 offset:4096\n" \
"v_mfma_f32_16x16x32_bf16 a[60:63], v[76:79], v[188:191], a[60:63]\n" \
"v_mfma_f32_16x16x32_bf16 a[80:83], v[80:83], v[160:163], a[80:83]\n" \
"ds_read_b128 v[72:75], v48 offset:2048\n" \
"v_mfma_f32_16x16x32_bf16 a[80:83], v[84:87], v[164:167], a[80:83]\n" \
"global_load_lds_dwordx4 v36, s[54:55] offset:0\n" \
"v_mfma_f32_16x16x32_bf16 a[84:87], v[80:83], v[168:171], a[84:87]\n" \
"ds_read_b64_tr_b16 v[140:141], v51 offset:16384\n" \
"v_mfma_f32_16x16x32_bf16 a[84:87], v[84:87], v[172:175], a[84:87]\n" \
"v_mfma_f32_16x16x32_bf16 a[88:91], v[80:83], v[176:179], a[88:91]\n" \
"ds_read_b128 v[76:79], v49 offset:2048\n" \
"v_mfma_f32_16x16x32_bf16 a[88:91], v[84:87], v[180:183], a[88:91]\n" \
"global_load_lds_dwordx4 v37, s[40:41] offset:1024\n" \
"v_mfma_f32_16x16x32_bf16 a[92:95], v[80:83], v[184:187], a[92:95]\n" \
"ds_read_b64_tr_b16 v[142:143], v51 offset:20480\n" \
"v_mfma_f32_16x16x32_bf16 a[92:95], v[84:87], v[188:191], a[92:95]\n" \
"v_mfma_f32_16x16x32_bf16 a[112:115], v[88:91], v[160:163], a[112:115]\n" \
"ds_read_b128 v[80:83], v48 offset:4096\n" \
"v_mfma_f32_16x16x32_bf16 a[112:115], v[92:95], v[164:167], a[112:115]\n" \
"global_load_lds_dwordx4 v38, s[54:55] offset:2048\n" \
"v_mfma_f32_16x16x32_bf16 a[116:119], v[88:91], v[168:171], a[116:119]\n" \
"ds_read_b64_tr_b16 v[144:145], v50 offset:512\n" \
"v_mfma_f32_16x16x32_bf16 a[116:119], v[92:95], v[172:175], a[116:119]\n" \
"v_mfma_f32_16x16x32_bf16 a[120:123], v[88:91], v[176:179], a[120:123]\n" \
"ds_read_b128 v[84:87], v49 offset:4096\n" \
"v_mfma_f32_16x16x32_bf16 a[120:123], v[92:95], v[180:183], a[120:123]\n" \
"global_load_lds_dwordx4 v39, s[40:41] offset:3072\n" \
"s_add_u32 s68, s67, 0x300000\n" \
"v_mfma_f32_16x16x32_bf16 a[124:127], v[88:91], v[184:187], a[124:127]\n" \
"s_add_u32 s64, s12, s68\n" \
"s_addc_u32 s65, s13, 0\n" \
"ds_read_b64_tr_b16 v[146:147], v50 offset:4608\n" \
"v_mfma_f32_16x16x32_bf16 a[124:127], v[92:95], v[188:191], a[124:127]\n" \
"v_mfma_f32_16x16x32_bf16 a[144:147], v[96:99], v[160:163], a[144:147]\n" \
"ds_read_b128 v[88:91], v48 offset:6144\n" \
"v_mfma_f32_16x16x32_bf16 a[144:147], v[100:103], v[164:167], a[144:147]\n" \
"global_load_dword v253, v248, s[64:65]\n" \
"v_mfma_f32_16x16x32_bf16 a[148:151], v[96:99], v[168:171], a[148:151]\n" \
"s_add_u32 s68, s67, 0x400000\n" \
"ds_read_b64_tr_b16 v[148:149], v50 offset:16896\n" \
"v_mfma_f32_16x16x32_bf16 a[148:151], v[100:103], v[172:175], a[148:151]\n" \
"s_add_u32 s64, s12, s68\n" \
"s_addc_u32 s65, s13, 0\n" \
"v_mfma_f32_16x16x32_bf16 a[152:155], v[96:99], v[176:179], a[152:155]\n" \
"ds_read_b128 v[92:95], v49 offset:6144\n" \
"v_mfma_f32_16x16x32_bf16 a[152:155], v[100:103], v[180:183], a[152:155]\n" \
"global_load_dword v253, v248, s[64:65]\n" \
"v_mfma_f32_16x16x32_bf16 a[156:159], v[96:99], v[184:187], a[156:159]\n" \
"ds_read_b64_tr_b16 v[150:151], v50 offset:20992\n" \
"v_mfma_f32_16x16x32_bf16 a[156:159], v[100:103], v[188:191], a[156:159]\n" \
"v_mfma_f32_16x16x32_bf16 a[176:179], v[104:107], v[160:163], a[176:179]\n" \
"ds_read_b128 v[96:99], v48 offset:8192\n" \
"v_mfma_f32_16x16x32_bf16 a[176:179], v[108:111], v[164:167], a[176:179]\n" \
"v_mfma_f32_16x16x32_bf16 a[180:183], v[104:107], v[168:171], a[180:183]\n" \
"ds_read_b64_tr_b16 v[152:153], v51 offset:512\n" \
"v_mfma_f32_16x16x32_bf16 a[180:183], v[108:111], v[172:175], a[180:183]\n" \
"v_mfma_f32_16x16x32_bf16 a[184:187], v[104:107], v[176:179], a[184:187]\n" \
"ds_read_b128 v[100:103], v49 offset:8192\n" \
"v_mfma_f32_16x16x32_bf16 a[184:187], v[108:111], v[180:183], a[184:187]\n" \
"v_mfma_f32_16x16x32_bf16 a[188:191], v[104:107], v[184:187], a[188:191]\n" \
"ds_read_b64_tr_b16 v[154:155], v51 offset:4608\n" \
"v_mfma_f32_16x16x32_bf16 a[188:191], v[108:111], v[188:191], a[188:191]\n" \
"v_mfma_f32_16x16x32_bf16 a[208:211], v[112:115], v[160:163], a[208:211]\n" \
"ds_read_b128 v[104:107], v48 offset:10240\n" \
"v_mfma_f32_16x16x32_bf16 a[208:211], v[116:119], v[164:167], a[208:211]\n" \
"v_mfma_f32_16x16x32_bf16 a[212:215], v[112:115], v[168:171], a[212:215]\n" \
"ds_read_b64_tr_b16 v[156:157], v51 offset:16896\n" \
"v_mfma_f32_16x16x32_bf16 a[212:215], v[116:119], v[172:175], a[212:215]\n" \
"v_mfma_f32_16x16x32_bf16 a[216:219], v[112:115], v[176:179], a[216:219]\n" \
"ds_read_b128 v[108:111], v49 offset:10240\n" \
"v_mfma_f32_16x16x32_bf16 a[216:219], v[116:119], v[180:183], a[216:219]\n" \
"v_mfma_f32_16x16x32_bf16 a[220:223], v[112:115], v[184:187], a[220:223]\n" \
"ds_read_b64_tr_b16 v[158:159], v51 offset:20992\n" \
"v_mfma_f32_16x16x32_bf16 a[220:223], v[116:119], v[188:191], a[220:223]\n" \
"v_mfma_f32_16x16x32_bf16 a[240:243], v[120:123], v[160:163], a[240:243]\n" \
"ds_read_b128 v[112:115], v48 offset:12288\n" \
"v_mfma_f32_16x16x32_bf16 a[240:243], v[124:127], v[164:167], a[240:243]\n" \
"v_mfma_f32_16x16x32_bf16 a[244:247], v[120:123], v[168:171], a[244:247]\n" \
"v_mfma_f32_16x16x32_bf16 a[244:247], v[124:127], v[172:175], a[244:247]\n" \
"v_mfma_f32_16x16x32_bf16 a[248:251], v[120:123], v[176:179], a[248:251]\n" \
"ds_read_b128 v[116:119], v49 offset:12288\n" \
"v_mfma_f32_16x16x32_bf16 a[248:251], v[124:127], v[180:183], a[248:251]\n" \
"v_mfma_f32_16x16x32_bf16 a[252:255], v[120:123], v[184:187], a[252:255]\n" \
"v_mfma_f32_16x16x32_bf16 a[252:255], v[124:127], v[188:191], a[252:255]\n" \
"s_waitcnt lgkmcnt(1)\n" \
"v_mfma_f32_16x16x32_bf16 a[0:3], v[128:131], v[64:67], a[0:3]\n" \
"ds_read_b128 v[120:123], v48 offset:14336\n" \
"v_mfma_f32_16x16x32_bf16 a[0:3], v[132:135], v[68:71], a[0:3]\n" \
"v_mfma_f32_16x16x32_bf16 a[4:7], v[136:139], v[64:67], a[4:7]\n" \
"ds_read_b128 v[124:127], v49 offset:14336\n" \
"s_add_u32 m0, s95, 4096\n" \
"v_mfma_f32_16x16x32_bf16 a[4:7], v[140:143], v[68:71], a[4:7]\n" \
"global_load_lds_dwordx4 v228, s[44:45] offset:-4096\n" \
"v_mfma_f32_16x16x32_bf16 a[8:11], v[144:147], v[64:67], a[8:11]\n" \
"ds_read_b64_tr_b16 v[160:161], v50 offset:1024\n" \
"v_mfma_f32_16x16x32_bf16 a[8:11], v[148:151], v[68:71], a[8:11]\n" \
"v_mfma_f32_16x16x32_bf16 a[12:15], v[152:155], v[64:67], a[12:15]\n" \
"ds_read_b64_tr_b16 v[162:163], v50 offset:5120\n" \
"v_mfma_f32_16x16x32_bf16 a[12:15], v[156:159], v[68:71], a[12:15]\n" \
"global_load_lds_dwordx4 v229, s[44:45] offset:-3072\n" \
"v_mfma_f32_16x16x32_bf16 a[32:35], v[128:131], v[72:75], a[32:35]\n" \
"s_add_u32 s44, s44, 0x180000\n" \
"s_addc_u32 s45, s45, 0\n" \
"ds_read_b64_tr_b16 v[164:165], v50 offset:17408\n" \
"v_mfma_f32_16x16x32_bf16 a[32:35], v[132:135], v[76:79], a[32:35]\n" \
"v_mfma_f32_16x16x32_bf16 a[36:39], v[136:139], v[72:75], a[36:39]\n" \
"ds_read_b64_tr_b16 v[166:167], v50 offset:21504\n" \
"v_mfma_f32_16x16x32_bf16 a[36:39], v[140:143], v[76:79], a[36:39]\n" \
"global_load_lds_dwordx4 v228, s[42:43] offset:-2048\n" \
"v_mfma_f32_16x16x32_bf16 a[40:43], v[144:147], v[72:75], a[40:43]\n" \
"ds_read_b64_tr_b16 v[168:169], v51 offset:1024\n" \
"v_mfma_f32_16x16x32_bf16 a[40:43], v[148:151], v[76:79], a[40:43]\n" \
"v_mfma_f32_16x16x32_bf16 a[44:47], v[152:155], v[72:75], a[44:47]\n" \
"ds_read_b64_tr_b16 v[170:171], v51 offset:5120\n" \
"v_mfma_f32_16x16x32_bf16 a[44:47], v[156:159], v[76:79], a[44:47]\n" \
"global_load_lds_dwordx4 v229, s[42:43] offset:-1024\n" \
"v_mfma_f32_16x16x32_bf16 a[64:67], v[128:131], v[80:83], a[64:67]\n" \
"s_add_u32 s42, s42, 0x180000\n" \
"s_addc_u32 s43, s43, 0\n" \
"ds_read_b64_tr_b16 v[172:173], v51 offset:17408\n" \
"v_mfma_f32_16x16x32_bf16 a[64:67], v[132:135], v[84:87], a[64:67]\n" \
"v_mfma_f32_16x16x32_bf16 a[68:71], v[136:139], v[80:83], a[68:71]\n" \
"s_add_u32 m0, s95, 20480\n" \
"ds_read_b64_tr_b16 v[174:175], v51 offset:21504\n" \
"v_mfma_f32_16x16x32_bf16 a[68:71], v[140:143], v[84:87], a[68:71]\n" \
"global_load_lds_dwordx4 v228, s[44:45] offset:-4096\n" \
"v_mfma_f32_16x16x32_bf16 a[72:75], v[144:147], v[80:83], a[72:75]\n" \
"ds_read_b64_tr_b16 v[176:177], v50 offset:1536\n" \
"v_mfma_f32_16x16x32_bf16 a[72:75], v[148:151], v[84:87], a[72:75]\n" \
"v_mfma_f32_16x16x32_bf16 a[76:79], v[152:155], v[80:83], a[76:79]\n" \
"ds_read_b64_tr_b16 v[178:179], v50 offset:5632\n" \
"v_mfma_f32_16x16x32_bf16 a[76:79], v[156:159], v[84:87], a[76:79]\n" \
"Lpg1684_%=:\n" \
"Lst0x2_%=:\n" \
"s_cbranch_execz Lpg3850_%=\n" \
"global_load_lds_dwordx4 v229, s[44:45] offset:-3072\n" \
"v_mfma_f32_16x16x32_bf16 a[96:99], v[128:131], v[88:91], a[96:99]\n" \
"s_add_u32 s44, s44, 0x400000\n" \
"s_addc_u32 s45, s45, 0\n" \
"ds_read_b64_tr_b16 v[180:181], v50 offset:17920\n" \
"v_mfma_f32_16x16x32_bf16 a[96:99], v[132:135], v[92:95], a[96:99]\n" \
"v_mfma_f32_16x16x32_bf16 a[100:103], v[136:139], v[88:91], a[100:103]\n" \
"ds_read_b64_tr_b16 v[182:183], v50 offset:22016\n" \
"v_mfma_f32_16x16x32_bf16 a[100:103], v[140:143], v[92:95], a[100:103]\n" \
"global_load_lds_dwordx4 v228, s[42:43] offset:-2048\n" \
"v_mfma_f32_16x16x32_bf16 a[104:107], v[144:147], v[88:91], a[104:107]\n" \
"ds_read_b64_tr_b16 v[184:185], v51 offset:1536\n" \
"v_mfma_f32_16x16x32_bf16 a[104:107], v[148:151], v[92:95], a[104:107]\n" \
"v_mfma_f32_16x16x32_bf16 a[108:111], v[152:155], v[88:91], a[108:111]\n" \
"ds_read_b64_tr_b16 v[186:187], v51 offset:5632\n" \
"v_mfma_f32_16x16x32_bf16 a[108:111], v[156:159], v[92:95], a[108:111]\n" \
"global_load_lds_dwordx4 v229, s[42:43] offset:-1024\n" \
"v_mfma_f32_16x16x32_bf16 a[128:131], v[128:131], v[96:99], a[128:131]\n" \
"s_add_u32 s42, s42, 0x400000\n" \
"s_addc_u32 s43, s43, 0\n" \
"ds_read_b64_tr_b16 v[188:189], v51 offset:17920\n" \
"v_mfma_f32_16x16x32_bf16 a[128:131], v[132:135], v[100:103], a[128:131]\n" \
"v_mfma_f32_16x16x32_bf16 a[132:135], v[136:139], v[96:99], a[132:135]\n" \
"ds_read_b64_tr_b16 v[190:191], v51 offset:22016\n" \
"v_mfma_f32_16x16x32_bf16 a[132:135], v[140:143], v[100:103], a[132:135]\n" \
"v_mfma_f32_16x16x32_bf16 a[136:139], v[144:147], v[96:99], a[136:139]\n" \
"v_mfma_f32_16x16x32_bf16 a[136:139], v[148:151], v[100:103], a[136:139]\n" \
"v_mfma_f32_16x16x32_bf16 a[140:143], v[152:155], v[96:99], a[140:143]\n" \
"v_mfma_f32_16x16x32_bf16 a[140:143], v[156:159], v[100:103], a[140:143]\n" \
"v_mfma_f32_16x16x32_bf16 a[160:163], v[128:131], v[104:107], a[160:163]\n" \
"s_mul_i32 s76, s73, 0x0\n" \
"s_add_u32 s77, s76, 0xffff8000\n" \
"s_sub_u32 s78, 0xffff8000, s76\n" \
"v_mfma_f32_16x16x32_bf16 a[160:163], v[132:135], v[108:111], a[160:163]\n" \
"v_add_u32 v48, s77, v48\n" \
"v_add_u32 v49, s77, v49\n" \
"v_mfma_f32_16x16x32_bf16 a[164:167], v[136:139], v[104:107], a[164:167]\n" \
"v_add_u32 v50, s78, v50\n" \
"v_add_u32 v51, s78, v51\n" \
"v_mfma_f32_16x16x32_bf16 a[164:167], v[140:143], v[108:111], a[164:167]\n" \
"v_mfma_f32_16x16x32_bf16 a[168:171], v[144:147], v[104:107], a[168:171]\n" \
"v_mfma_f32_16x16x32_bf16 a[168:171], v[148:151], v[108:111], a[168:171]\n" \
"v_mfma_f32_16x16x32_bf16 a[172:175], v[152:155], v[104:107], a[172:175]\n" \
"v_mfma_f32_16x16x32_bf16 a[172:175], v[156:159], v[108:111], a[172:175]\n" \
"s_waitcnt lgkmcnt(0)\n" \
"v_mfma_f32_16x16x32_bf16 a[192:195], v[128:131], v[112:115], a[192:195]\n" \
"v_mfma_f32_16x16x32_bf16 a[192:195], v[132:135], v[116:119], a[192:195]\n" \
"v_mfma_f32_16x16x32_bf16 a[196:199], v[136:139], v[112:115], a[196:199]\n" \
"v_mfma_f32_16x16x32_bf16 a[196:199], v[140:143], v[116:119], a[196:199]\n" \
"v_mfma_f32_16x16x32_bf16 a[200:203], v[144:147], v[112:115], a[200:203]\n" \
"v_mfma_f32_16x16x32_bf16 a[200:203], v[148:151], v[116:119], a[200:203]\n" \
"v_mfma_f32_16x16x32_bf16 a[204:207], v[152:155], v[112:115], a[204:207]\n" \
"v_mfma_f32_16x16x32_bf16 a[204:207], v[156:159], v[116:119], a[204:207]\n" \
"v_mfma_f32_16x16x32_bf16 a[224:227], v[128:131], v[120:123], a[224:227]\n" \
"v_mfma_f32_16x16x32_bf16 a[224:227], v[132:135], v[124:127], a[224:227]\n" \
"v_mfma_f32_16x16x32_bf16 a[228:231], v[136:139], v[120:123], a[228:231]\n" \
"v_mfma_f32_16x16x32_bf16 a[228:231], v[140:143], v[124:127], a[228:231]\n" \
"v_mfma_f32_16x16x32_bf16 a[232:235], v[144:147], v[120:123], a[232:235]\n" \
"v_mfma_f32_16x16x32_bf16 a[232:235], v[148:151], v[124:127], a[232:235]\n" \
"v_mfma_f32_16x16x32_bf16 a[236:239], v[152:155], v[120:123], a[236:239]\n" \
"s_waitcnt vmcnt(10) lgkmcnt(0)\n" \
"v_mfma_f32_16x16x32_bf16 a[236:239], v[156:159], v[124:127], a[236:239]\n" \
"s_barrier\n" \
"v_mfma_f32_16x16x32_bf16 a[16:19], v[64:67], v[160:163], a[16:19]\n" \
"s_add_u32 m0, s87, 135168\n" \
"ds_read_b64_tr_b16 v[128:129], v50 offset:0\n" \
"v_mfma_f32_16x16x32_bf16 a[16:19], v[68:71], v[164:167], a[16:19]\n" \
"global_load_lds_dwordx4 v224, s[48:49] offset:-4096\n" \
"v_mfma_f32_16x16x32_bf16 a[20:23], v[64:67], v[168:171], a[20:23]\n" \
"ds_read_b64_tr_b16 v[130:131], v50 offset:4096\n" \
"v_mfma_f32_16x16x32_bf16 a[20:23], v[68:71], v[172:175], a[20:23]\n" \
"v_mfma_f32_16x16x32_bf16 a[24:27], v[64:67], v[176:179], a[24:27]\n" \
"ds_read_b64_tr_b16 v[132:133], v50 offset:16384\n" \
"v_mfma_f32_16x16x32_bf16 a[24:27], v[68:71], v[180:183], a[24:27]\n" \
"global_load_lds_dwordx4 v225, s[48:49] offset:-3072\n" \
"v_mfma_f32_16x16x32_bf16 a[28:31], v[64:67], v[184:187], a[28:31]\n" \
"ds_read_b64_tr_b16 v[134:135], v50 offset:20480\n" \
"v_mfma_f32_16x16x32_bf16 a[28:31], v[68:71], v[188:191], a[28:31]\n" \
"v_mfma_f32_16x16x32_bf16 a[48:51], v[72:75], v[160:163], a[48:51]\n" \
"ds_read_b128 v[64:67], v48 offset:0\n" \
"v_mfma_f32_16x16x32_bf16 a[48:51], v[76:79], v[164:167], a[48:51]\n" \
"global_load_lds_dwordx4 v226, s[48:49] offset:-2048\n" \
"v_mfma_f32_16x16x32_bf16 a[52:55], v[72:75], v[168:171], a[52:55]\n" \
"ds_read_b64_tr_b16 v[136:137], v51 offset:0\n" \
"v_mfma_f32_16x16x32_bf16 a[52:55], v[76:79], v[172:175], a[52:55]\n" \
"v_mfma_f32_16x16x32_bf16 a[56:59], v[72:75], v[176:179], a[56:59]\n" \
"ds_read_b128 v[68:71], v49 offset:0\n" \
"v_mfma_f32_16x16x32_bf16 a[56:59], v[76:79], v[180:183], a[56:59]\n" \
"global_load_lds_dwordx4 v227, s[48:49] offset:-1024\n" \
"v_mfma_f32_16x16x32_bf16 a[60:63], v[72:75], v[184:187], a[60:63]\n" \
"s_add_u32 s48, s48, 0x180\n" \
"s_addc_u32 s49, s49, 0\n" \
"ds_read_b64_tr_b16 v[138:139], v51 offset:4096\n" \
"v_mfma_f32_16x16x32_bf16 a[60:63], v[76:79], v[188:191], a[60:63]\n" \
"v_mfma_f32_16x16x32_bf16 a[80:83], v[80:83], v[160:163], a[80:83]\n" \
"ds_read_b128 v[72:75], v48 offset:2048\n" \
"v_mfma_f32_16x16x32_bf16 a[80:83], v[84:87], v[164:167], a[80:83]\n" \
"global_load_lds_dwordx4 v224, s[46:47] offset:0\n" \
"v_mfma_f32_16x16x32_bf16 a[84:87], v[80:83], v[168:171], a[84:87]\n" \
"ds_read_b64_tr_b16 v[140:141], v51 offset:16384\n" \
"v_mfma_f32_16x16x32_bf16 a[84:87], v[84:87], v[172:175], a[84:87]\n" \
"v_mfma_f32_16x16x32_bf16 a[88:91], v[80:83], v[176:179], a[88:91]\n" \
"ds_read_b128 v[76:79], v49 offset:2048\n" \
"v_mfma_f32_16x16x32_bf16 a[88:91], v[84:87], v[180:183], a[88:91]\n" \
"global_load_lds_dwordx4 v225, s[46:47] offset:1024\n" \
"v_mfma_f32_16x16x32_bf16 a[92:95], v[80:83], v[184:187], a[92:95]\n" \
"ds_read_b64_tr_b16 v[142:143], v51 offset:20480\n" \
"v_mfma_f32_16x16x32_bf16 a[92:95], v[84:87], v[188:191], a[92:95]\n" \
"v_mfma_f32_16x16x32_bf16 a[112:115], v[88:91], v[160:163], a[112:115]\n" \
"ds_read_b128 v[80:83], v48 offset:4096\n" \
"v_mfma_f32_16x16x32_bf16 a[112:115], v[92:95], v[164:167], a[112:115]\n" \
"global_load_lds_dwordx4 v226, s[46:47] offset:2048\n" \
"v_mfma_f32_16x16x32_bf16 a[116:119], v[88:91], v[168:171], a[116:119]\n" \
"ds_read_b64_tr_b16 v[144:145], v50 offset:512\n" \
"v_mfma_f32_16x16x32_bf16 a[116:119], v[92:95], v[172:175], a[116:119]\n" \
"v_mfma_f32_16x16x32_bf16 a[120:123], v[88:91], v[176:179], a[120:123]\n" \
"ds_read_b128 v[84:87], v49 offset:4096\n" \
"v_mfma_f32_16x16x32_bf16 a[120:123], v[92:95], v[180:183], a[120:123]\n" \
"global_load_lds_dwordx4 v227, s[46:47] offset:3072\n" \
"v_mfma_f32_16x16x32_bf16 a[124:127], v[88:91], v[184:187], a[124:127]\n" \
"s_add_u32 s46, s46, 0x180\n" \
"s_addc_u32 s47, s47, 0\n" \
"ds_read_b64_tr_b16 v[146:147], v50 offset:4608\n" \
"v_mfma_f32_16x16x32_bf16 a[124:127], v[92:95], v[188:191], a[124:127]\n" \
"s_add_u32 s68, s67, 0x600000\n" \
"s_add_u32 s64, s12, s68\n" \
"s_addc_u32 s65, s13, 0\n" \
"v_mfma_f32_16x16x32_bf16 a[144:147], v[96:99], v[160:163], a[144:147]\n" \
"ds_read_b128 v[88:91], v48 offset:6144\n" \
"v_mfma_f32_16x16x32_bf16 a[144:147], v[100:103], v[164:167], a[144:147]\n" \
"global_load_dword v253, v248, s[64:65]\n" \
"v_mfma_f32_16x16x32_bf16 a[148:151], v[96:99], v[168:171], a[148:151]\n" \
"s_add_u32 s68, s67, 0x700000\n" \
"ds_read_b64_tr_b16 v[148:149], v50 offset:16896\n" \
"v_mfma_f32_16x16x32_bf16 a[148:151], v[100:103], v[172:175], a[148:151]\n" \
"s_add_u32 s64, s12, s68\n" \
"s_addc_u32 s65, s13, 0\n" \
"v_mfma_f32_16x16x32_bf16 a[152:155], v[96:99], v[176:179], a[152:155]\n" \
"ds_read_b128 v[92:95], v49 offset:6144\n" \
"v_mfma_f32_16x16x32_bf16 a[152:155], v[100:103], v[180:183], a[152:155]\n" \
"global_load_dword v253, v248, s[64:65]\n" \
"v_mfma_f32_16x16x32_bf16 a[156:159], v[96:99], v[184:187], a[156:159]\n" \
"s_lshr_b32 s68, s66, 2\n" \
"s_and_b32 s68, s68, 0xffff\n" \
"ds_read_b64_tr_b16 v[150:151], v50 offset:20992\n" \
"v_mfma_f32_16x16x32_bf16 a[156:159], v[100:103], v[188:191], a[156:159]\n" \
"s_add_u32 s68, s68, 65585152\n" \
"v_mfma_f32_16x16x32_bf16 a[176:179], v[104:107], v[160:163], a[176:179]\n" \
"ds_read_b128 v[96:99], v48 offset:8192\n" \
"v_mfma_f32_16x16x32_bf16 a[176:179], v[108:111], v[164:167], a[176:179]\n" \
"buffer_load_dword v253, v245, s[56:59], s68 offen sc1\n" \
"v_mfma_f32_16x16x32_bf16 a[180:183], v[104:107], v[168:171], a[180:183]\n" \
"ds_read_b64_tr_b16 v[152:153], v51 offset:512\n" \
"v_mfma_f32_16x16x32_bf16 a[180:183], v[108:111], v[172:175], a[180:183]\n" \
"v_mfma_f32_16x16x32_bf16 a[184:187], v[104:107], v[176:179], a[184:187]\n" \
"ds_read_b128 v[100:103], v49 offset:8192\n" \
"v_mfma_f32_16x16x32_bf16 a[184:187], v[108:111], v[180:183], a[184:187]\n" \
"v_mfma_f32_16x16x32_bf16 a[188:191], v[104:107], v[184:187], a[188:191]\n" \
"ds_read_b64_tr_b16 v[154:155], v51 offset:4608\n" \
"v_mfma_f32_16x16x32_bf16 a[188:191], v[108:111], v[188:191], a[188:191]\n" \
"v_mfma_f32_16x16x32_bf16 a[208:211], v[112:115], v[160:163], a[208:211]\n" \
"ds_read_b128 v[104:107], v48 offset:10240\n" \
"v_mfma_f32_16x16x32_bf16 a[208:211], v[116:119], v[164:167], a[208:211]\n" \
"v_mfma_f32_16x16x32_bf16 a[212:215], v[112:115], v[168:171], a[212:215]\n" \
"ds_read_b64_tr_b16 v[156:157], v51 offset:16896\n" \
"v_mfma_f32_16x16x32_bf16 a[212:215], v[116:119], v[172:175], a[212:215]\n" \
"v_mfma_f32_16x16x32_bf16 a[216:219], v[112:115], v[176:179], a[216:219]\n" \
"ds_read_b128 v[108:111], v49 offset:10240\n" \
"v_mfma_f32_16x16x32_bf16 a[216:219], v[116:119], v[180:183], a[216:219]\n" \
"v_mfma_f32_16x16x32_bf16 a[220:223], v[112:115], v[184:187], a[220:223]\n" \
"ds_read_b64_tr_b16 v[158:159], v51 offset:20992\n" \
"v_mfma_f32_16x16x32_bf16 a[220:223], v[116:119], v[188:191], a[220:223]\n" \
"v_mfma_f32_16x16x32_bf16 a[240:243], v[120:123], v[160:163], a[240:243]\n" \
"ds_read_b128 v[112:115], v48 offset:12288\n" \
"v_mfma_f32_16x16x32_bf16 a[240:243], v[124:127], v[164:167], a[240:243]\n" \
"v_mfma_f32_16x16x32_bf16 a[244:247], v[120:123], v[168:171], a[244:247]\n" \
"v_mfma_f32_16x16x32_bf16 a[244:247], v[124:127], v[172:175], a[244:247]\n" \
"v_mfma_f32_16x16x32_bf16 a[248:251], v[120:123], v[176:179], a[248:251]\n" \
"ds_read_b128 v[116:119], v49 offset:12288\n" \
"v_mfma_f32_16x16x32_bf16 a[248:251], v[124:127], v[180:183], a[248:251]\n" \
"v_mfma_f32_16x16x32_bf16 a[252:255], v[120:123], v[184:187], a[252:255]\n" \
"v_mfma_f32_16x16x32_bf16 a[252:255], v[124:127], v[188:191], a[252:255]\n" \
"s_waitcnt lgkmcnt(1)\n" \
"v_mfma_f32_16x16x32_bf16 a[0:3], v[128:131], v[64:67], a[0:3]\n" \
"ds_read_b128 v[120:123], v48 offset:14336\n" \
"v_mfma_f32_16x16x32_bf16 a[0:3], v[132:135], v[68:71], a[0:3]\n" \
"v_mfma_f32_16x16x32_bf16 a[4:7], v[136:139], v[64:67], a[4:7]\n" \
"ds_read_b128 v[124:127], v49 offset:14336\n" \
"s_add_u32 m0, s87, 69632\n" \
"v_mfma_f32_16x16x32_bf16 a[4:7], v[140:143], v[68:71], a[4:7]\n" \
"global_load_lds_dwordx4 v224, s[48:49] offset:-4096\n" \
"v_mfma_f32_16x16x32_bf16 a[8:11], v[144:147], v[64:67], a[8:11]\n" \
"ds_read_b64_tr_b16 v[160:161], v50 offset:1024\n" \
"v_mfma_f32_16x16x32_bf16 a[8:11], v[148:151], v[68:71], a[8:11]\n" \
"v_mfma_f32_16x16x32_bf16 a[12:15], v[152:155], v[64:67], a[12:15]\n" \
"ds_read_b64_tr_b16 v[162:163], v50 offset:5120\n" \
"v_mfma_f32_16x16x32_bf16 a[12:15], v[156:159], v[68:71], a[12:15]\n" \
"global_load_lds_dwordx4 v225, s[48:49] offset:-3072\n" \
"v_mfma_f32_16x16x32_bf16 a[32:35], v[128:131], v[72:75], a[32:35]\n" \
"ds_read_b64_tr_b16 v[164:165], v50 offset:17408\n" \
"v_mfma_f32_16x16x32_bf16 a[32:35], v[132:135], v[76:79], a[32:35]\n" \
"v_mfma_f32_16x16x32_bf16 a[36:39], v[136:139], v[72:75], a[36:39]\n" \
"ds_read_b64_tr_b16 v[166:167], v50 offset:21504\n" \
"v_mfma_f32_16x16x32_bf16 a[36:39], v[140:143], v[76:79], a[36:39]\n" \
"global_load_lds_dwordx4 v226, s[48:49] offset:-2048\n" \
"v_mfma_f32_16x16x32_bf16 a[40:43], v[144:147], v[72:75], a[40:43]\n" \
"ds_read_b64_tr_b16 v[168:169], v51 offset:1024\n" \
"v_mfma_f32_16x16x32_bf16 a[40:43], v[148:151], v[76:79], a[40:43]\n" \
"v_mfma_f32_16x16x32_bf16 a[44:47], v[152:155], v[72:75], a[44:47]\n" \
"ds_read_b64_tr_b16 v[170:171], v51 offset:5120\n" \
"v_mfma_f32_16x16x32_bf16 a[44:47], v[156:159], v[76:79], a[44:47]\n" \
"global_load_lds_dwordx4 v227, s[48:49] offset:-1024\n" \
"v_mfma_f32_16x16x32_bf16 a[64:67], v[128:131], v[80:83], a[64:67]\n" \
"s_add_u32 s48, s48, 0x400\n" \
"s_addc_u32 s49, s49, 0\n" \
"ds_read_b64_tr_b16 v[172:173], v51 offset:17408\n" \
"v_mfma_f32_16x16x32_bf16 a[64:67], v[132:135], v[84:87], a[64:67]\n" \
"v_mfma_f32_16x16x32_bf16 a[68:71], v[136:139], v[80:83], a[68:71]\n" \
"ds_read_b64_tr_b16 v[174:175], v51 offset:21504\n" \
"v_mfma_f32_16x16x32_bf16 a[68:71], v[140:143], v[84:87], a[68:71]\n" \
"global_load_lds_dwordx4 v224, s[46:47] offset:0\n" \
"v_mfma_f32_16x16x32_bf16 a[72:75], v[144:147], v[80:83], a[72:75]\n" \
"ds_read_b64_tr_b16 v[176:177], v50 offset:1536\n" \
"v_mfma_f32_16x16x32_bf16 a[72:75], v[148:151], v[84:87], a[72:75]\n" \
"v_mfma_f32_16x16x32_bf16 a[76:79], v[152:155], v[80:83], a[76:79]\n" \
"ds_read_b64_tr_b16 v[178:179], v50 offset:5632\n" \
"v_mfma_f32_16x16x32_bf16 a[76:79], v[156:159], v[84:87], a[76:79]\n" \
"global_load_lds_dwordx4 v225, s[46:47] offset:1024\n" \
"v_mfma_f32_16x16x32_bf16 a[96:99], v[128:131], v[88:91], a[96:99]\n" \
"ds_read_b64_tr_b16 v[180:181], v50 offset:17920\n" \
"v_mfma_f32_16x16x32_bf16 a[96:99], v[132:135], v[92:95], a[96:99]\n" \
"v_mfma_f32_16x16x32_bf16 a[100:103], v[136:139], v[88:91], a[100:103]\n" \
"ds_read_b64_tr_b16 v[182:183], v50 offset:22016\n" \
"v_mfma_f32_16x16x32_bf16 a[100:103], v[140:143], v[92:95], a[100:103]\n" \
"global_load_lds_dwordx4 v226, s[46:47] offset:2048\n" \
"v_mfma_f32_16x16x32_bf16 a[104:107], v[144:147], v[88:91], a[104:107]\n" \
"ds_read_b64_tr_b16 v[184:185], v51 offset:1536\n" \
"v_mfma_f32_16x16x32_bf16 a[104:107], v[148:151], v[92:95], a[104:107]\n" \
"v_mfma_f32_16x16x32_bf16 a[108:111], v[152:155], v[88:91], a[108:111]\n" \
"ds_read_b64_tr_b16 v[186:187], v51 offset:5632\n" \
"v_mfma_f32_16x16x32_bf16 a[108:111], v[156:159], v[92:95], a[108:111]\n" \
"global_load_lds_dwordx4 v227, s[46:47] offset:3072\n" \
"v_mfma_f32_16x16x32_bf16 a[128:131], v[128:131], v[96:99], a[128:131]\n" \
"s_add_u32 s46, s46, 0x400\n" \
"s_addc_u32 s47, s47, 0\n" \
"ds_read_b64_tr_b16 v[188:189], v51 offset:17920\n" \
"v_mfma_f32_16x16x32_bf16 a[128:131], v[132:135], v[100:103], a[128:131]\n" \
"v_mfma_f32_16x16x32_bf16 a[132:135], v[136:139], v[96:99], a[132:135]\n" \
"ds_read_b64_tr_b16 v[190:191], v51 offset:22016\n" \
"v_mfma_f32_16x16x32_bf16 a[132:135], v[140:143], v[100:103], a[132:135]\n" \
"v_mfma_f32_16x16x32_bf16 a[136:139], v[144:147], v[96:99], a[136:139]\n" \
"v_mfma_f32_16x16x32_bf16 a[136:139], v[148:151], v[100:103], a[136:139]\n" \
"v_mfma_f32_16x16x32_bf16 a[140:143], v[152:155], v[96:99], a[140:143]\n" \
"v_mfma_f32_16x16x32_bf16 a[140:143], v[156:159], v[100:103], a[140:143]\n" \
"v_mfma_f32_16x16x32_bf16 a[160:163], v[128:131], v[104:107], a[160:163]\n" \
"v_mfma_f32_16x16x32_bf16 a[160:163], v[132:135], v[108:111], a[160:163]\n" \
"v_mfma_f32_16x16x32_bf16 a[164:167], v[136:139], v[104:107], a[164:167]\n" \
"v_mfma_f32_16x16x32_bf16 a[164:167], v[140:143], v[108:111], a[164:167]\n" \
"v_mfma_f32_16x16x32_bf16 a[168:171], v[144:147], v[104:107], a[168:171]\n" \
"v_mfma_f32_16x16x32_bf16 a[168:171], v[148:151], v[108:111], a[168:171]\n" \
"v_mfma_f32_16x16x32_bf16 a[172:175], v[152:155], v[104:107], a[172:175]\n" \
"v_mfma_f32_16x16x32_bf16 a[172:175], v[156:159], v[108:111], a[172:175]\n" \
"s_waitcnt lgkmcnt(0)\n" \
"v_mfma_f32_16x16x32_bf16 a[192:195], v[128:131], v[112:115], a[192:195]\n" \
"v_mfma_f32_16x16x32_bf16 a[192:195], v[132:135], v[116:119], a[192:195]\n" \
"v_mfma_f32_16x16x32_bf16 a[196:199], v[136:139], v[112:115], a[196:199]\n" \
"v_mfma_f32_16x16x32_bf16 a[196:199], v[140:143], v[116:119], a[196:199]\n" \
"v_mfma_f32_16x16x32_bf16 a[200:203], v[144:147], v[112:115], a[200:203]\n" \
"v_mfma_f32_16x16x32_bf16 a[200:203], v[148:151], v[116:119], a[200:203]\n" \
"v_mfma_f32_16x16x32_bf16 a[204:207], v[152:155], v[112:115], a[204:207]\n" \
"v_mfma_f32_16x16x32_bf16 a[204:207], v[156:159], v[116:119], a[204:207]\n" \
"v_mfma_f32_16x16x32_bf16 a[224:227], v[128:131], v[120:123], a[224:227]\n" \
"v_mfma_f32_16x16x32_bf16 a[224:227], v[132:135], v[124:127], a[224:227]\n" \
"v_mfma_f32_16x16x32_bf16 a[228:231], v[136:139], v[120:123], a[228:231]\n" \
"v_mfma_f32_16x16x32_bf16 a[228:231], v[140:143], v[124:127], a[228:231]\n" \
"v_mfma_f32_16x16x32_bf16 a[232:235], v[144:147], v[120:123], a[232:235]\n" \
"v_mfma_f32_16x16x32_bf16 a[232:235], v[148:151], v[124:127], a[232:235]\n" \
"v_mfma_f32_16x16x32_bf16 a[236:239], v[152:155], v[120:123], a[236:239]\n" \
"s_waitcnt vmcnt(11) lgkmcnt(0)\n" \
"v_mfma_f32_16x16x32_bf16 a[236:239], v[156:159], v[124:127], a[236:239]\n" \
"s_barrier\n" \
"v_mfma_f32_16x16x32_bf16 a[16:19], v[64:67], v[160:163], a[16:19]\n" \
"s_add_u32 m0, s87, 102400\n" \
"v_mfma_f32_16x16x32_bf16 a[16:19], v[68:71], v[164:167], a[16:19]\n" \
"global_load_lds_dwordx4 v224, s[48:49] offset:-4096\n" \
"v_mfma_f32_16x16x32_bf16 a[20:23], v[64:67], v[168:171], a[20:23]\n" \
"ds_read_b64_tr_b16 v[0:1], v232 offset:0\n" \
"v_mfma_f32_16x16x32_bf16 a[20:23], v[68:71], v[172:175], a[20:23]\n" \
"v_accvgpr_read_b32 v234, a192\n" \
"v_accvgpr_read_b32 v235, a193\n" \
"v_mfma_f32_16x16x32_bf16 a[24:27], v[64:67], v[176:179], a[24:27]\n" \
"ds_read_b64_tr_b16 v[2:3], v232 offset:2048\n" \
"v_accvgpr_read_b32 v236, a194\n" \
"v_mfma_f32_16x16x32_bf16 a[24:27], v[68:71], v[180:183], a[24:27]\n" \
"global_load_lds_dwordx4 v225, s[48:49] offset:-3072\n" \
"v_accvgpr_read_b32 v237, a195\n" \
"v_mfma_f32_16x16x32_bf16 a[28:31], v[64:67], v[184:187], a[28:31]\n" \
"ds_read_b64_tr_b16 v[4:5], v232 offset:8192\n" \
"v_mul_f32 v234, s92, v234\n" \
"v_mfma_f32_16x16x32_bf16 a[28:31], v[68:71], v[188:191], a[28:31]\n" \
"v_mul_f32 v235, s92, v235\n" \
"v_mul_f32 v236, s92, v236\n" \
"v_mfma_f32_16x16x32_bf16 a[48:51], v[72:75], v[160:163], a[48:51]\n" \
"ds_read_b64_tr_b16 v[6:7], v232 offset:10240\n" \
"v_mul_f32 v237, s92, v237\n" \
"v_mfma_f32_16x16x32_bf16 a[48:51], v[76:79], v[164:167], a[48:51]\n" \
"global_load_lds_dwordx4 v226, s[48:49] offset:-2048\n" \
"v_cvt_pk_bf16_f32 v234, v234, v235\n" \
"v_mfma_f32_16x16x32_bf16 a[52:55], v[72:75], v[168:171], a[52:55]\n" \
"ds_read_b64_tr_b16 v[8:9], v233 offset:0\n" \
"v_cvt_pk_bf16_f32 v236, v236, v237\n" \
"v_mfma_f32_16x16x32_bf16 a[52:55], v[76:79], v[172:175], a[52:55]\n" \
"v_accvgpr_write_b32 a192, v234\n" \
"v_accvgpr_write_b32 a193, v236\n" \
"v_mfma_f32_16x16x32_bf16 a[56:59], v[72:75], v[176:179], a[56:59]\n" \
"ds_read_b64_tr_b16 v[10:11], v233 offset:2048\n" \
"v_accvgpr_read_b32 v234, a196\n" \
"v_mfma_f32_16x16x32_bf16 a[56:59], v[76:79], v[180:183], a[56:59]\n" \
"global_load_lds_dwordx4 v227, s[48:49] offset:-1024\n" \
"v_accvgpr_read_b32 v235, a197\n" \
"v_mfma_f32_16x16x32_bf16 a[60:63], v[72:75], v[184:187], a[60:63]\n" \
"s_add_u32 s48, s48, 0xfffffb00\n" \
"s_addc_u32 s49, s49, -1\n" \
"ds_read_b64_tr_b16 v[12:13], v233 offset:8192\n" \
"v_mfma_f32_16x16x32_bf16 a[60:63], v[76:79], v[188:191], a[60:63]\n" \
"v_accvgpr_read_b32 v236, a198\n" \
"v_accvgpr_read_b32 v237, a199\n" \
"v_mfma_f32_16x16x32_bf16 a[80:83], v[80:83], v[160:163], a[80:83]\n" \
"ds_read_b64_tr_b16 v[14:15], v233 offset:10240\n" \
"v_mul_f32 v234, s92, v234\n" \
"v_mfma_f32_16x16x32_bf16 a[80:83], v[84:87], v[164:167], a[80:83]\n" \
"global_load_lds_dwordx4 v224, s[46:47] offset:0\n" \
"v_mul_f32 v235, s92, v235\n" \
"v_mfma_f32_16x16x32_bf16 a[84:87], v[80:83], v[168:171], a[84:87]\n" \
"ds_read_b64_tr_b16 v[16:17], v232 offset:512\n" \
"v_mul_f32 v236, s92, v236\n" \
"v_mfma_f32_16x16x32_bf16 a[84:87], v[84:87], v[172:175], a[84:87]\n" \
"v_mul_f32 v237, s92, v237\n" \
"v_cvt_pk_bf16_f32 v234, v234, v235\n" \
"v_mfma_f32_16x16x32_bf16 a[88:91], v[80:83], v[176:179], a[88:91]\n" \
"ds_read_b64_tr_b16 v[18:19], v232 offset:2560\n" \
"v_cvt_pk_bf16_f32 v236, v236, v237\n" \
"v_mfma_f32_16x16x32_bf16 a[88:91], v[84:87], v[180:183], a[88:91]\n" \
"global_load_lds_dwordx4 v225, s[46:47] offset:1024\n" \
"v_accvgpr_write_b32 a194, v234\n" \
"v_mfma_f32_16x16x32_bf16 a[92:95], v[80:83], v[184:187], a[92:95]\n" \
"ds_read_b64_tr_b16 v[20:21], v232 offset:8704\n" \
"v_accvgpr_write_b32 a195, v236\n" \
"v_mfma_f32_16x16x32_bf16 a[92:95], v[84:87], v[188:191], a[92:95]\n" \
"v_accvgpr_read_b32 v234, a224\n" \
"v_accvgpr_read_b32 v235, a225\n" \
"v_mfma_f32_16x16x32_bf16 a[112:115], v[88:91], v[160:163], a[112:115]\n" \
"ds_read_b64_tr_b16 v[22:23], v232 offset:10752\n" \
"v_accvgpr_read_b32 v236, a226\n" \
"v_mfma_f32_16x16x32_bf16 a[112:115], v[92:95], v[164:167], a[112:115]\n" \
"global_load_lds_dwordx4 v226, s[46:47] offset:2048\n" \
"v_accvgpr_read_b32 v237, a227\n" \
"v_mfma_f32_16x16x32_bf16 a[116:119], v[88:91], v[168:171], a[116:119]\n" \
"ds_read_b64_tr_b16 v[24:25], v233 offset:512\n" \
"v_mul_f32 v234, s92, v234\n" \
"v_mfma_f32_16x16x32_bf16 a[116:119], v[92:95], v[172:175], a[116:119]\n" \
"v_mul_f32 v235, s92, v235\n" \
"v_mul_f32 v236, s92, v236\n" \
"v_mfma_f32_16x16x32_bf16 a[120:123], v[88:91], v[176:179], a[120:123]\n" \
"ds_read_b64_tr_b16 v[26:27], v233 offset:2560\n" \
"v_mul_f32 v237, s92, v237\n" \
"v_mfma_f32_16x16x32_bf16 a[120:123], v[92:95], v[180:183], a[120:123]\n" \
"global_load_lds_dwordx4 v227, s[46:47] offset:3072\n" \
"v_cvt_pk_bf16_f32 v234, v234, v235\n" \
"v_mfma_f32_16x16x32_bf16 a[124:127], v[88:91], v[184:187], a[124:127]\n" \
"s_add_u32 s46, s46, 0xfffffb00\n" \
"s_addc_u32 s47, s47, -1\n" \
"ds_read_b64_tr_b16 v[28:29], v233 offset:8704\n" \
"v_mfma_f32_16x16x32_bf16 a[124:127], v[92:95], v[188:191], a[124:127]\n" \
"v_cvt_pk_bf16_f32 v236, v236, v237\n" \
"v_accvgpr_write_b32 a224, v234\n" \
"v_mfma_f32_16x16x32_bf16 a[144:147], v[96:99], v[160:163], a[144:147]\n" \
"s_add_u32 m0, s95, 36864\n" \
"ds_read_b64_tr_b16 v[30:31], v233 offset:10752\n" \
"v_accvgpr_write_b32 a225, v236\n" \
"v_mfma_f32_16x16x32_bf16 a[144:147], v[100:103], v[164:167], a[144:147]\n" \
"global_load_lds_dwordx4 v228, s[44:45] offset:-4096\n" \
"v_accvgpr_read_b32 v234, a228\n" \
"v_mfma_f32_16x16x32_bf16 a[148:151], v[96:99], v[168:171], a[148:151]\n" \
"ds_read_b128 v[192:195], v230 offset:0\n" \
"v_accvgpr_read_b32 v235, a229\n" \
"v_mfma_f32_16x16x32_bf16 a[148:151], v[100:103], v[172:175], a[148:151]\n" \
"v_accvgpr_read_b32 v236, a230\n" \
"v_accvgpr_read_b32 v237, a231\n" \
"v_mfma_f32_16x16x32_bf16 a[152:155], v[96:99], v[176:179], a[152:155]\n" \
"ds_read_b128 v[196:199], v231 offset:0\n" \
"v_mul_f32 v234, s92, v234\n" \
"v_mfma_f32_16x16x32_bf16 a[152:155], v[100:103], v[180:183], a[152:155]\n" \
"global_load_lds_dwordx4 v229, s[44:45] offset:-3072\n" \
"v_mul_f32 v235, s92, v235\n" \
"v_mfma_f32_16x16x32_bf16 a[156:159], v[96:99], v[184:187], a[156:159]\n" \
"s_add_u32 s44, s44, 0xffb00000\n" \
"s_addc_u32 s45, s45, -1\n" \
"ds_read_b128 v[200:203], v230 offset:2048\n" \
"v_mfma_f32_16x16x32_bf16 a[156:159], v[100:103], v[188:191], a[156:159]\n" \
"v_mul_f32 v236, s92, v236\n" \
"v_mul_f32 v237, s92, v237\n" \
"v_mfma_f32_16x16x32_bf16 a[176:179], v[104:107], v[160:163], a[176:179]\n" \
"ds_read_b128 v[204:207], v231 offset:2048\n" \
"v_cvt_pk_bf16_f32 v234, v234, v235\n" \
"v_mfma_f32_16x16x32_bf16 a[176:179], v[108:111], v[164:167], a[176:179]\n" \
"global_load_lds_dwordx4 v228, s[42:43] offset:-2048\n" \
"v_cvt_pk_bf16_f32 v236, v236, v237\n" \
"v_mfma_f32_16x16x32_bf16 a[180:183], v[104:107], v[168:171], a[180:183]\n" \
"ds_read_b128 v[208:211], v230 offset:4096\n" \
"v_accvgpr_write_b32 a226, v234\n" \
"v_mfma_f32_16x16x32_bf16 a[180:183], v[108:111], v[172:175], a[180:183]\n" \
"v_accvgpr_write_b32 a227, v236\n" \
"v_accvgpr_read_b32 v234, a200\n" \
"v_mfma_f32_16x16x32_bf16 a[184:187], v[104:107], v[176:179], a[184:187]\n" \
"ds_read_b128 v[212:215], v231 offset:4096\n" \
"v_accvgpr_read_b32 v235, a201\n" \
"v_mfma_f32_16x16x32_bf16 a[184:187], v[108:111], v[180:183], a[184:187]\n" \
"global_load_lds_dwordx4 v229, s[42:43] offset:-1024\n" \
"v_accvgpr_read_b32 v236, a202\n" \
"v_mfma_f32_16x16x32_bf16 a[188:191], v[104:107], v[184:187], a[188:191]\n" \
"s_add_u32 s42, s42, 0xffb00000\n" \
"s_addc_u32 s43, s43, -1\n" \
"v_accvgpr_read_b32 v237, a203\n" \
"v_mfma_f32_16x16x32_bf16 a[188:191], v[108:111], v[188:191], a[188:191]\n" \
"v_mul_f32 v234, s92, v234\n" \
"v_mul_f32 v235, s92, v235\n" \
"v_mfma_f32_16x16x32_bf16 a[208:211], v[112:115], v[160:163], a[208:211]\n" \
"s_add_u32 m0, s95, 53248\n" \
"v_mul_f32 v236, s92, v236\n" \
"v_mul_f32 v237, s92, v237\n" \
"v_mfma_f32_16x16x32_bf16 a[208:211], v[116:119], v[164:167], a[208:211]\n" \
"global_load_lds_dwordx4 v228, s[44:45] offset:-4096\n" \
"v_cvt_pk_bf16_f32 v234, v234, v235\n" \
"v_mfma_f32_16x16x32_bf16 a[212:215], v[112:115], v[168:171], a[212:215]\n" \
"v_cvt_pk_bf16_f32 v236, v236, v237\n" \
"v_accvgpr_write_b32 a200, v234\n" \
"v_mfma_f32_16x16x32_bf16 a[212:215], v[116:119], v[172:175], a[212:215]\n" \
"v_accvgpr_write_b32 a201, v236\n" \
"v_accvgpr_read_b32 v234, a204\n" \
"v_mfma_f32_16x16x32_bf16 a[216:219], v[112:115], v[176:179], a[216:219]\n" \
"v_accvgpr_read_b32 v235, a205\n" \
"v_accvgpr_read_b32 v236, a206\n" \
"v_mfma_f32_16x16x32_bf16 a[216:219], v[116:119], v[180:183], a[216:219]\n" \
"global_load_lds_dwordx4 v229, s[44:45] offset:-3072\n" \
"v_accvgpr_read_b32 v237, a207\n" \
"v_mfma_f32_16x16x32_bf16 a[220:223], v[112:115], v[184:187], a[220:223]\n" \
"s_add_u32 s44, s44, 0x80000\n" \
"s_addc_u32 s45, s45, 0\n" \
"v_mul_f32 v234, s92, v234\n" \
"v_mfma_f32_16x16x32_bf16 a[220:223], v[116:119], v[188:191], a[220:223]\n" \
"v_mul_f32 v235, s92, v235\n" \
"v_mul_f32 v236, s92, v236\n" \
"v_mfma_f32_16x16x32_bf16 a[240:243], v[120:123], v[160:163], a[240:243]\n" \
"v_mul_f32 v237, s92, v237\n" \
"v_cvt_pk_bf16_f32 v234, v234, v235\n" \
"v_mfma_f32_16x16x32_bf16 a[240:243], v[124:127], v[164:167], a[240:243]\n" \
"global_load_lds_dwordx4 v228, s[42:43] offset:-2048\n" \
"v_cvt_pk_bf16_f32 v236, v236, v237\n" \
"v_mfma_f32_16x16x32_bf16 a[244:247], v[120:123], v[168:171], a[244:247]\n" \
"v_accvgpr_write_b32 a202, v234\n" \
"v_accvgpr_write_b32 a203, v236\n" \
"v_mfma_f32_16x16x32_bf16 a[244:247], v[124:127], v[172:175], a[244:247]\n" \
"v_accvgpr_read_b32 v234, a232\n" \
"v_accvgpr_read_b32 v235, a233\n" \
"v_mfma_f32_16x16x32_bf16 a[248:251], v[120:123], v[176:179], a[248:251]\n" \
"v_accvgpr_read_b32 v236, a234\n" \
"v_accvgpr_read_b32 v237, a235\n" \
"v_mfma_f32_16x16x32_bf16 a[248:251], v[124:127], v[180:183], a[248:251]\n" \
"global_load_lds_dwordx4 v229, s[42:43] offset:-1024\n" \
"v_mul_f32 v234, s92, v234\n" \
"v_mfma_f32_16x16x32_bf16 a[252:255], v[120:123], v[184:187], a[252:255]\n" \
"s_add_u32 s42, s42, 0x80000\n" \
"s_addc_u32 s43, s43, 0\n" \
"v_mul_f32 v235, s92, v235\n" \
"v_mfma_f32_16x16x32_bf16 a[252:255], v[124:127], v[188:191], a[252:255]\n" \
"s_waitcnt lgkmcnt(0)\n" \
"v_mul_f32 v236, s92, v236\n" \
"v_mul_f32 v237, s92, v237\n" \
"v_mfma_f32_16x16x32_bf16 v[64:67], v[0:3], v[192:195], 0\n" \
"ds_read_b128 v[216:219], v230 offset:6144\n" \
"v_cvt_pk_bf16_f32 v234, v234, v235\n" \
"v_mfma_f32_16x16x32_bf16 v[64:67], v[4:7], v[196:199], v[64:67]\n" \
"ds_read_b128 v[220:223], v231 offset:6144\n" \
"v_cvt_pk_bf16_f32 v236, v236, v237\n" \
"v_mfma_f32_16x16x32_bf16 v[68:71], v[8:11], v[192:195], 0\n" \
"s_add_u32 s90, s88, 0\n" \
"ds_read_b64_tr_b16 v[32:33], v232 offset:1024\n" \
"v_accvgpr_write_b32 a232, v234\n" \
"v_mfma_f32_16x16x32_bf16 v[68:71], v[12:15], v[196:199], v[68:71]\n" \
"buffer_store_dwordx4 a[192:195], v249, s[56:59], s90 offen offset:0 sc1\n" \
"v_accvgpr_write_b32 a233, v236\n" \
"v_accvgpr_read_b32 v234, a236\n" \
"v_accvgpr_read_b32 v235, a237\n" \
"v_accvgpr_read_b32 v236, a238\n" \
"v_mfma_f32_16x16x32_bf16 v[72:75], v[16:19], v[192:195], 0\n" \
"ds_read_b64_tr_b16 v[34:35], v232 offset:3072\n" \
"v_accvgpr_read_b32 v237, a239\n" \
"v_mfma_f32_16x16x32_bf16 v[72:75], v[20:23], v[196:199], v[72:75]\n" \
"ds_read_b64_tr_b16 v[36:37], v232 offset:9216\n" \
"v_mul_f32 v234, s92, v234\n" \
"v_mfma_f32_16x16x32_bf16 v[76:79], v[24:27], v[192:195], 0\n" \
"ds_read_b64_tr_b16 v[38:39], v232 offset:11264\n" \
"v_mul_f32 v235, s92, v235\n" \
"v_mfma_f32_16x16x32_bf16 v[76:79], v[28:31], v[196:199], v[76:79]\n" \
"buffer_store_dwordx4 a[224:227], v249, s[56:59], s90 offen offset:256 sc1\n" \
"v_mul_f32 v236, s92, v236\n" \
"v_mul_f32 v237, s92, v237\n" \
"v_cvt_pk_bf16_f32 v234, v234, v235\n" \
"v_cvt_pk_bf16_f32 v236, v236, v237\n" \
"v_mfma_f32_16x16x32_bf16 v[96:99], v[0:3], v[200:203], 0\n" \
"ds_read_b64_tr_b16 v[40:41], v233 offset:1024\n" \
"v_accvgpr_write_b32 a234, v234\n" \
"v_mfma_f32_16x16x32_bf16 v[96:99], v[4:7], v[204:207], v[96:99]\n" \
"ds_read_b64_tr_b16 v[42:43], v233 offset:3072\n" \
"v_accvgpr_write_b32 a235, v236\n" \
"v_mfma_f32_16x16x32_bf16 v[100:103], v[8:11], v[200:203], 0\n" \
"ds_read_b64_tr_b16 v[44:45], v233 offset:9216\n" \
"v_accvgpr_read_b32 v234, a208\n" \
"v_mfma_f32_16x16x32_bf16 v[100:103], v[12:15], v[204:207], v[100:103]\n" \
"v_accvgpr_read_b32 v235, a209\n" \
"v_accvgpr_read_b32 v236, a210\n" \
"v_mfma_f32_16x16x32_bf16 v[104:107], v[16:19], v[200:203], 0\n" \
"ds_read_b64_tr_b16 v[46:47], v233 offset:11264\n" \
"v_accvgpr_read_b32 v237, a211\n" \
"v_mfma_f32_16x16x32_bf16 v[104:107], v[20:23], v[204:207], v[104:107]\n" \
"ds_read_b64_tr_b16 v[48:49], v232 offset:1536\n" \
"v_cvt_pk_bf16_f32 v234, v234, v235\n" \
"v_mfma_f32_16x16x32_bf16 v[108:111], v[24:27], v[200:203], 0\n" \
"ds_read_b64_tr_b16 v[50:51], v232 offset:3584\n" \
"v_cvt_pk_bf16_f32 v236, v236, v237\n" \
"v_mfma_f32_16x16x32_bf16 v[108:111], v[28:31], v[204:207], v[108:111]\n" \
"v_accvgpr_write_b32 a208, v234\n" \
"v_accvgpr_write_b32 a209, v236\n" \
"v_mfma_f32_16x16x32_bf16 v[128:131], v[0:3], v[208:211], 0\n" \
"ds_read_b64_tr_b16 v[52:53], v232 offset:9728\n" \
"v_accvgpr_read_b32 v234, a240\n" \
"v_mfma_f32_16x16x32_bf16 v[128:131], v[4:7], v[212:215], v[128:131]\n" \
"ds_read_b64_tr_b16 v[54:55], v232 offset:11776\n" \
"v_accvgpr_read_b32 v235, a241\n" \
"v_mfma_f32_16x16x32_bf16 v[132:135], v[8:11], v[208:211], 0\n" \
"ds_read_b64_tr_b16 v[56:57], v233 offset:1536\n" \
"v_accvgpr_read_b32 v236, a242\n" \
"Lpg2228_%=:\n" \
"Lst0x3_%=:\n" \
"s_cbranch_execz Lpg4399_%=\n" \
"v_mfma_f32_16x16x32_bf16 v[132:135], v[12:15], v[212:215], v[132:135]\n" \
"v_accvgpr_read_b32 v237, a243\n" \
"v_cvt_pk_bf16_f32 v234, v234, v235\n" \
"v_mfma_f32_16x16x32_bf16 v[136:139], v[16:19], v[208:211], 0\n" \
"ds_read_b64_tr_b16 v[58:59], v233 offset:3584\n" \
"v_cvt_pk_bf16_f32 v236, v236, v237\n" \
"v_mfma_f32_16x16x32_bf16 v[136:139], v[20:23], v[212:215], v[136:139]\n" \
"ds_read_b64_tr_b16 v[60:61], v233 offset:9728\n" \
"v_accvgpr_write_b32 a210, v234\n" \
"v_mfma_f32_16x16x32_bf16 v[140:143], v[24:27], v[208:211], 0\n" \
"ds_read_b64_tr_b16 v[62:63], v233 offset:11776\n" \
"v_accvgpr_write_b32 a211, v236\n" \
"v_mfma_f32_16x16x32_bf16 v[140:143], v[28:31], v[212:215], v[140:143]\n" \
"s_waitcnt lgkmcnt(3)\n" \
"v_accvgpr_read_b32 v234, a212\n" \
"v_accvgpr_read_b32 v235, a213\n" \
"v_mfma_f32_16x16x32_bf16 v[160:163], v[0:3], v[216:219], 0\n" \
"v_add_u32 v230, 0xffff0000, v230\n" \
"v_add_u32 v231, 0xffff0000, v231\n" \
"v_mfma_f32_16x16x32_bf16 v[160:163], v[4:7], v[220:223], v[160:163]\n" \
"v_add_u32 v232, 0x4000, v232\n" \
"v_add_u32 v233, 0x4000, v233\n" \
"v_mfma_f32_16x16x32_bf16 v[164:167], v[8:11], v[216:219], 0\n" \
"v_accvgpr_read_b32 v236, a214\n" \
"v_accvgpr_read_b32 v237, a215\n" \
"v_mfma_f32_16x16x32_bf16 v[164:167], v[12:15], v[220:223], v[164:167]\n" \
"v_cvt_pk_bf16_f32 v234, v234, v235\n" \
"v_cvt_pk_bf16_f32 v236, v236, v237\n" \
"v_mfma_f32_16x16x32_bf16 v[168:171], v[16:19], v[216:219], 0\n" \
"v_accvgpr_write_b32 a212, v234\n" \
"v_accvgpr_write_b32 a213, v236\n" \
"v_mfma_f32_16x16x32_bf16 v[168:171], v[20:23], v[220:223], v[168:171]\n" \
"v_accvgpr_read_b32 v234, a244\n" \
"v_accvgpr_read_b32 v235, a245\n" \
"v_mfma_f32_16x16x32_bf16 v[172:175], v[24:27], v[216:219], 0\n" \
"s_waitcnt vmcnt(18) lgkmcnt(0)\n" \
"v_accvgpr_read_b32 v236, a246\n" \
"v_accvgpr_read_b32 v237, a247\n" \
"v_mfma_f32_16x16x32_bf16 v[172:175], v[28:31], v[220:223], v[172:175]\n" \
"s_barrier\n" \
"v_mfma_f32_16x16x32_bf16 v[80:83], v[32:35], v[192:195], 0\n" \
"ds_read_b64_tr_b16 v[0:1], v232 offset:0\n" \
"v_cvt_pk_bf16_f32 v234, v234, v235\n" \
"v_mfma_f32_16x16x32_bf16 v[80:83], v[36:39], v[196:199], v[80:83]\n" \
"ds_read_b64_tr_b16 v[2:3], v232 offset:2048\n" \
"v_cvt_pk_bf16_f32 v236, v236, v237\n" \
"v_mfma_f32_16x16x32_bf16 v[84:87], v[40:43], v[192:195], 0\n" \
"s_add_u32 m0, s87, 135168\n" \
"ds_read_b64_tr_b16 v[4:5], v232 offset:8192\n" \
"v_accvgpr_write_b32 a214, v234\n" \
"v_mfma_f32_16x16x32_bf16 v[84:87], v[44:47], v[196:199], v[84:87]\n" \
"global_load_lds_dwordx4 v224, s[48:49] offset:-4096\n" \
"v_accvgpr_write_b32 a215, v236\n" \
"v_mfma_f32_16x16x32_bf16 v[88:91], v[48:51], v[192:195], 0\n" \
"ds_read_b64_tr_b16 v[6:7], v232 offset:10240\n" \
"v_accvgpr_read_b32 v234, a216\n" \
"v_mfma_f32_16x16x32_bf16 v[88:91], v[52:55], v[196:199], v[88:91]\n" \
"ds_read_b64_tr_b16 v[8:9], v233 offset:0\n" \
"v_accvgpr_read_b32 v235, a217\n" \
"v_mfma_f32_16x16x32_bf16 v[92:95], v[56:59], v[192:195], 0\n" \
"ds_read_b64_tr_b16 v[10:11], v233 offset:2048\n" \
"v_accvgpr_read_b32 v236, a218\n" \
"v_mfma_f32_16x16x32_bf16 v[92:95], v[60:63], v[196:199], v[92:95]\n" \
"global_load_lds_dwordx4 v225, s[48:49] offset:-3072\n" \
"v_accvgpr_read_b32 v237, a219\n" \
"v_mfma_f32_16x16x32_bf16 v[112:115], v[32:35], v[200:203], 0\n" \
"ds_read_b128 v[192:195], v230 offset:0\n" \
"v_cvt_pk_bf16_f32 v234, v234, v235\n" \
"v_mfma_f32_16x16x32_bf16 v[112:115], v[36:39], v[204:207], v[112:115]\n" \
"ds_read_b128 v[196:199], v231 offset:0\n" \
"v_cvt_pk_bf16_f32 v236, v236, v237\n" \
"v_mfma_f32_16x16x32_bf16 v[116:119], v[40:43], v[200:203], 0\n" \
"ds_read_b64_tr_b16 v[12:13], v233 offset:8192\n" \
"v_accvgpr_write_b32 a216, v234\n" \
"v_mfma_f32_16x16x32_bf16 v[116:119], v[44:47], v[204:207], v[116:119]\n" \
"global_load_lds_dwordx4 v226, s[48:49] offset:-2048\n" \
"v_accvgpr_write_b32 a217, v236\n" \
"v_mfma_f32_16x16x32_bf16 v[120:123], v[48:51], v[200:203], 0\n" \
"ds_read_b64_tr_b16 v[14:15], v233 offset:10240\n" \
"v_accvgpr_read_b32 v234, a248\n" \
"v_mfma_f32_16x16x32_bf16 v[120:123], v[52:55], v[204:207], v[120:123]\n" \
"ds_read_b64_tr_b16 v[16:17], v232 offset:512\n" \
"v_accvgpr_read_b32 v235, a249\n" \
"v_mfma_f32_16x16x32_bf16 v[124:127], v[56:59], v[200:203], 0\n" \
"ds_read_b64_tr_b16 v[18:19], v232 offset:2560\n" \
"v_accvgpr_read_b32 v236, a250\n" \
"v_mfma_f32_16x16x32_bf16 v[124:127], v[60:63], v[204:207], v[124:127]\n" \
"global_load_lds_dwordx4 v227, s[48:49] offset:-1024\n" \
"v_accvgpr_read_b32 v237, a251\n" \
"v_mfma_f32_16x16x32_bf16 v[144:147], v[32:35], v[208:211], 0\n" \
"s_add_u32 s48, s48, 0x80\n" \
"s_addc_u32 s49, s49, 0\n" \
"ds_read_b128 v[200:203], v230 offset:2048\n" \
"v_mfma_f32_16x16x32_bf16 v[144:147], v[36:39], v[212:215], v[144:147]\n" \
"ds_read_b128 v[204:207], v231 offset:2048\n" \
"v_cvt_pk_bf16_f32 v234, v234, v235\n" \
"v_mfma_f32_16x16x32_bf16 v[148:151], v[40:43], v[208:211], 0\n" \
"ds_read_b64_tr_b16 v[20:21], v232 offset:8704\n" \
"v_cvt_pk_bf16_f32 v236, v236, v237\n" \
"v_mfma_f32_16x16x32_bf16 v[148:151], v[44:47], v[212:215], v[148:151]\n" \
"global_load_lds_dwordx4 v224, s[46:47] offset:0\n" \
"v_accvgpr_write_b32 a218, v234\n" \
"v_mfma_f32_16x16x32_bf16 v[152:155], v[48:51], v[208:211], 0\n" \
"ds_read_b64_tr_b16 v[22:23], v232 offset:10752\n" \
"v_accvgpr_write_b32 a219, v236\n" \
"v_mfma_f32_16x16x32_bf16 v[152:155], v[52:55], v[212:215], v[152:155]\n" \
"ds_read_b64_tr_b16 v[24:25], v233 offset:512\n" \
"v_accvgpr_read_b32 v234, a220\n" \
"v_mfma_f32_16x16x32_bf16 v[156:159], v[56:59], v[208:211], 0\n" \
"ds_read_b64_tr_b16 v[26:27], v233 offset:2560\n" \
"v_accvgpr_read_b32 v235, a221\n" \
"v_mfma_f32_16x16x32_bf16 v[156:159], v[60:63], v[212:215], v[156:159]\n" \
"global_load_lds_dwordx4 v225, s[46:47] offset:1024\n" \
"v_accvgpr_read_b32 v236, a222\n" \
"v_mfma_f32_16x16x32_bf16 v[176:179], v[32:35], v[216:219], 0\n" \
"ds_read_b128 v[208:211], v230 offset:4096\n" \
"v_accvgpr_read_b32 v237, a223\n" \
"v_mfma_f32_16x16x32_bf16 v[176:179], v[36:39], v[220:223], v[176:179]\n" \
"ds_read_b128 v[212:215], v231 offset:4096\n" \
"v_cvt_pk_bf16_f32 v234, v234, v235\n" \
"v_mfma_f32_16x16x32_bf16 v[180:183], v[40:43], v[216:219], 0\n" \
"ds_read_b64_tr_b16 v[28:29], v233 offset:8704\n" \
"v_cvt_pk_bf16_f32 v236, v236, v237\n" \
"v_mfma_f32_16x16x32_bf16 v[180:183], v[44:47], v[220:223], v[180:183]\n" \
"global_load_lds_dwordx4 v226, s[46:47] offset:2048\n" \
"v_accvgpr_write_b32 a220, v234\n" \
"v_mfma_f32_16x16x32_bf16 v[184:187], v[48:51], v[216:219], 0\n" \
"ds_read_b64_tr_b16 v[30:31], v233 offset:10752\n" \
"v_accvgpr_write_b32 a221, v236\n" \
"v_mfma_f32_16x16x32_bf16 v[184:187], v[52:55], v[220:223], v[184:187]\n" \
"v_accvgpr_read_b32 v234, a252\n" \
"v_accvgpr_read_b32 v235, a253\n" \
"v_mfma_f32_16x16x32_bf16 v[188:191], v[56:59], v[216:219], 0\n" \
"v_accvgpr_read_b32 v236, a254\n" \
"v_accvgpr_read_b32 v237, a255\n" \
"v_mfma_f32_16x16x32_bf16 v[188:191], v[60:63], v[220:223], v[188:191]\n" \
"s_waitcnt lgkmcnt(1)\n" \
"global_load_lds_dwordx4 v227, s[46:47] offset:3072\n" \
"v_cvt_pk_bf16_f32 v234, v234, v235\n" \
"v_mfma_f32_16x16x32_bf16 v[64:67], v[0:3], v[192:195], v[64:67]\n" \
"s_add_u32 s46, s46, 0x80\n" \
"s_addc_u32 s47, s47, 0\n" \
"ds_read_b128 v[216:219], v230 offset:6144\n" \
"v_mfma_f32_16x16x32_bf16 v[64:67], v[4:7], v[196:199], v[64:67]\n" \
"ds_read_b128 v[220:223], v231 offset:6144\n" \
"v_cvt_pk_bf16_f32 v236, v236, v237\n" \
"v_mfma_f32_16x16x32_bf16 v[68:71], v[8:11], v[192:195], v[68:71]\n" \
"ds_read_b64_tr_b16 v[32:33], v232 offset:1024\n" \
"v_accvgpr_write_b32 a222, v234\n" \
"v_mfma_f32_16x16x32_bf16 v[68:71], v[12:15], v[196:199], v[68:71]\n" \
"buffer_store_dwordx4 a[200:203], v249, s[56:59], s90 offen offset:2048 sc1\n" \
"v_accvgpr_write_b32 a223, v236\n" \
"v_accvgpr_read_b32 v234, a128\n" \
"v_accvgpr_read_b32 v235, a129\n" \
"v_accvgpr_read_b32 v236, a130\n" \
"v_mfma_f32_16x16x32_bf16 v[72:75], v[16:19], v[192:195], v[72:75]\n" \
"ds_read_b64_tr_b16 v[34:35], v232 offset:3072\n" \
"v_accvgpr_read_b32 v237, a131\n" \
"v_mfma_f32_16x16x32_bf16 v[72:75], v[20:23], v[196:199], v[72:75]\n" \
"s_waitcnt lgkmcnt(3)\n" \
"ds_read_b64_tr_b16 v[36:37], v232 offset:9216\n" \
"v_mul_f32 v234, s92, v234\n" \
"v_mfma_f32_16x16x32_bf16 v[76:79], v[24:27], v[192:195], v[76:79]\n" \
"ds_read_b64_tr_b16 v[38:39], v232 offset:11264\n" \
"v_mul_f32 v235, s92, v235\n" \
"v_mfma_f32_16x16x32_bf16 v[76:79], v[28:31], v[196:199], v[76:79]\n" \
"v_mul_f32 v236, s92, v236\n" \
"v_mul_f32 v237, s92, v237\n" \
"v_mfma_f32_16x16x32_bf16 v[96:99], v[0:3], v[200:203], v[96:99]\n" \
"ds_read_b64_tr_b16 v[40:41], v233 offset:1024\n" \
"v_cvt_pk_bf16_f32 v234, v234, v235\n" \
"v_mfma_f32_16x16x32_bf16 v[96:99], v[4:7], v[204:207], v[96:99]\n" \
"ds_read_b64_tr_b16 v[42:43], v233 offset:3072\n" \
"v_cvt_pk_bf16_f32 v236, v236, v237\n" \
"v_mfma_f32_16x16x32_bf16 v[100:103], v[8:11], v[200:203], v[100:103]\n" \
"ds_read_b64_tr_b16 v[44:45], v233 offset:9216\n" \
"v_accvgpr_write_b32 a128, v234\n" \
"v_mfma_f32_16x16x32_bf16 v[100:103], v[12:15], v[204:207], v[100:103]\n" \
"v_accvgpr_write_b32 a129, v236\n" \
"v_accvgpr_read_b32 v234, a132\n" \
"v_mfma_f32_16x16x32_bf16 v[104:107], v[16:19], v[200:203], v[104:107]\n" \
"ds_read_b64_tr_b16 v[46:47], v233 offset:11264\n" \
"v_accvgpr_read_b32 v235, a133\n" \
"v_mfma_f32_16x16x32_bf16 v[104:107], v[20:23], v[204:207], v[104:107]\n" \
"ds_read_b64_tr_b16 v[48:49], v232 offset:1536\n" \
"v_accvgpr_read_b32 v236, a134\n" \
"v_mfma_f32_16x16x32_bf16 v[108:111], v[24:27], v[200:203], v[108:111]\n" \
"s_add_u32 m0, s95, 4096\n" \
"ds_read_b64_tr_b16 v[50:51], v232 offset:3584\n" \
"v_accvgpr_read_b32 v237, a135\n" \
"v_mfma_f32_16x16x32_bf16 v[108:111], v[28:31], v[204:207], v[108:111]\n" \
"global_load_lds_dwordx4 v228, s[44:45] offset:-4096\n" \
"v_mul_f32 v234, s92, v234\n" \
"v_mfma_f32_16x16x32_bf16 v[128:131], v[0:3], v[208:211], v[128:131]\n" \
"ds_read_b64_tr_b16 v[52:53], v232 offset:9728\n" \
"v_mul_f32 v235, s92, v235\n" \
"v_mfma_f32_16x16x32_bf16 v[128:131], v[4:7], v[212:215], v[128:131]\n" \
"ds_read_b64_tr_b16 v[54:55], v232 offset:11776\n" \
"v_mul_f32 v236, s92, v236\n" \
"v_mfma_f32_16x16x32_bf16 v[132:135], v[8:11], v[208:211], v[132:135]\n" \
"ds_read_b64_tr_b16 v[56:57], v233 offset:1536\n" \
"v_mul_f32 v237, s92, v237\n" \
"v_mfma_f32_16x16x32_bf16 v[132:135], v[12:15], v[212:215], v[132:135]\n" \
"global_load_lds_dwordx4 v229, s[44:45] offset:-3072\n" \
"v_cvt_pk_bf16_f32 v234, v234, v235\n" \
"v_mfma_f32_16x16x32_bf16 v[136:139], v[16:19], v[208:211], v[136:139]\n" \
"s_add_u32 s44, s44, 0x100000\n" \
"s_addc_u32 s45, s45, 0\n" \
"ds_read_b64_tr_b16 v[58:59], v233 offset:3584\n" \
"v_mfma_f32_16x16x32_bf16 v[136:139], v[20:23], v[212:215], v[136:139]\n" \
"ds_read_b64_tr_b16 v[60:61], v233 offset:9728\n" \
"v_cvt_pk_bf16_f32 v236, v236, v237\n" \
"v_mfma_f32_16x16x32_bf16 v[140:143], v[24:27], v[208:211], v[140:143]\n" \
"ds_read_b64_tr_b16 v[62:63], v233 offset:11776\n" \
"v_accvgpr_write_b32 a130, v234\n" \
"v_mfma_f32_16x16x32_bf16 v[140:143], v[28:31], v[212:215], v[140:143]\n" \
"s_waitcnt lgkmcnt(3)\n" \
"global_load_lds_dwordx4 v228, s[42:43] offset:-2048\n" \
"v_accvgpr_write_b32 a131, v236\n" \
"v_mfma_f32_16x16x32_bf16 v[160:163], v[0:3], v[216:219], v[160:163]\n" \
"v_add_u32 v230, 0x8000, v230\n" \
"v_add_u32 v231, 0x8000, v231\n" \
"v_mfma_f32_16x16x32_bf16 v[160:163], v[4:7], v[220:223], v[160:163]\n" \
"v_add_u32 v232, 0x4000, v232\n" \
"v_add_u32 v233, 0x4000, v233\n" \
"v_mfma_f32_16x16x32_bf16 v[164:167], v[8:11], v[216:219], v[164:167]\n" \
"v_accvgpr_read_b32 v234, a160\n" \
"v_accvgpr_read_b32 v235, a161\n" \
"v_mfma_f32_16x16x32_bf16 v[164:167], v[12:15], v[220:223], v[164:167]\n" \
"global_load_lds_dwordx4 v229, s[42:43] offset:-1024\n" \
"v_accvgpr_read_b32 v236, a162\n" \
"v_mfma_f32_16x16x32_bf16 v[168:171], v[16:19], v[216:219], v[168:171]\n" \
"s_add_u32 s42, s42, 0x100000\n" \
"s_addc_u32 s43, s43, 0\n" \
"v_accvgpr_read_b32 v237, a163\n" \
"v_mfma_f32_16x16x32_bf16 v[168:171], v[20:23], v[220:223], v[168:171]\n" \
"v_mul_f32 v234, s92, v234\n" \
"v_mul_f32 v235, s92, v235\n" \
"v_mfma_f32_16x16x32_bf16 v[172:175], v[24:27], v[216:219], v[172:175]\n" \
"s_waitcnt vmcnt(19) lgkmcnt(0)\n" \
"v_mul_f32 v236, s92, v236\n" \
"v_mul_f32 v237, s92, v237\n" \
"v_mfma_f32_16x16x32_bf16 v[172:175], v[28:31], v[220:223], v[172:175]\n" \
"s_barrier\n" \
"v_mfma_f32_16x16x32_bf16 v[80:83], v[32:35], v[192:195], v[80:83]\n" \
"ds_read_b64_tr_b16 v[0:1], v232 offset:0\n" \
"v_cvt_pk_bf16_f32 v234, v234, v235\n" \
"v_mfma_f32_16x16x32_bf16 v[80:83], v[36:39], v[196:199], v[80:83]\n" \
"ds_read_b64_tr_b16 v[2:3], v232 offset:2048\n" \
"v_cvt_pk_bf16_f32 v236, v236, v237\n" \
"v_mfma_f32_16x16x32_bf16 v[84:87], v[40:43], v[192:195], v[84:87]\n" \
"s_add_u32 m0, s87, 69632\n" \
"ds_read_b64_tr_b16 v[4:5], v232 offset:8192\n" \
"v_accvgpr_write_b32 a160, v234\n" \
"v_mfma_f32_16x16x32_bf16 v[84:87], v[44:47], v[196:199], v[84:87]\n" \
"global_load_lds_dwordx4 v224, s[48:49] offset:-4096\n" \
"v_accvgpr_write_b32 a161, v236\n" \
"v_mfma_f32_16x16x32_bf16 v[88:91], v[48:51], v[192:195], v[88:91]\n" \
"ds_read_b64_tr_b16 v[6:7], v232 offset:10240\n" \
"v_accvgpr_read_b32 v234, a164\n" \
"v_mfma_f32_16x16x32_bf16 v[88:91], v[52:55], v[196:199], v[88:91]\n" \
"ds_read_b64_tr_b16 v[8:9], v233 offset:0\n" \
"v_accvgpr_read_b32 v235, a165\n" \
"v_mfma_f32_16x16x32_bf16 v[92:95], v[56:59], v[192:195], v[92:95]\n" \
"ds_read_b64_tr_b16 v[10:11], v233 offset:2048\n" \
"v_accvgpr_read_b32 v236, a166\n" \
"v_mfma_f32_16x16x32_bf16 v[92:95], v[60:63], v[196:199], v[92:95]\n" \
"global_load_lds_dwordx4 v225, s[48:49] offset:-3072\n" \
"v_accvgpr_read_b32 v237, a167\n" \
"v_mfma_f32_16x16x32_bf16 v[112:115], v[32:35], v[200:203], v[112:115]\n" \
"ds_read_b128 v[192:195], v230 offset:0\n" \
"v_mul_f32 v234, s92, v234\n" \
"v_mfma_f32_16x16x32_bf16 v[112:115], v[36:39], v[204:207], v[112:115]\n" \
"ds_read_b128 v[196:199], v231 offset:0\n" \
"v_mul_f32 v235, s92, v235\n" \
"v_mfma_f32_16x16x32_bf16 v[116:119], v[40:43], v[200:203], v[116:119]\n" \
"ds_read_b64_tr_b16 v[12:13], v233 offset:8192\n" \
"v_mul_f32 v236, s92, v236\n" \
"v_mfma_f32_16x16x32_bf16 v[116:119], v[44:47], v[204:207], v[116:119]\n" \
"global_load_lds_dwordx4 v226, s[48:49] offset:-2048\n" \
"v_mul_f32 v237, s92, v237\n" \
"v_mfma_f32_16x16x32_bf16 v[120:123], v[48:51], v[200:203], v[120:123]\n" \
"ds_read_b64_tr_b16 v[14:15], v233 offset:10240\n" \
"v_cvt_pk_bf16_f32 v234, v234, v235\n" \
"v_mfma_f32_16x16x32_bf16 v[120:123], v[52:55], v[204:207], v[120:123]\n" \
"ds_read_b64_tr_b16 v[16:17], v232 offset:512\n" \
"v_cvt_pk_bf16_f32 v236, v236, v237\n" \
"v_mfma_f32_16x16x32_bf16 v[124:127], v[56:59], v[200:203], v[124:127]\n" \
"ds_read_b64_tr_b16 v[18:19], v232 offset:2560\n" \
"v_accvgpr_write_b32 a162, v234\n" \
"v_mfma_f32_16x16x32_bf16 v[124:127], v[60:63], v[204:207], v[124:127]\n" \
"global_load_lds_dwordx4 v227, s[48:49] offset:-1024\n" \
"v_accvgpr_write_b32 a163, v236\n" \
"v_mfma_f32_16x16x32_bf16 v[144:147], v[32:35], v[208:211], v[144:147]\n" \
"s_add_u32 s48, s48, 0x100\n" \
"s_addc_u32 s49, s49, 0\n" \
"ds_read_b128 v[200:203], v230 offset:2048\n" \
"v_mfma_f32_16x16x32_bf16 v[144:147], v[36:39], v[212:215], v[144:147]\n" \
"ds_read_b128 v[204:207], v231 offset:2048\n" \
"v_accvgpr_read_b32 v234, a136\n" \
"v_mfma_f32_16x16x32_bf16 v[148:151], v[40:43], v[208:211], v[148:151]\n" \
"ds_read_b64_tr_b16 v[20:21], v232 offset:8704\n" \
"v_accvgpr_read_b32 v235, a137\n" \
"v_mfma_f32_16x16x32_bf16 v[148:151], v[44:47], v[212:215], v[148:151]\n" \
"global_load_lds_dwordx4 v224, s[46:47] offset:0\n" \
"v_accvgpr_read_b32 v236, a138\n" \
"v_mfma_f32_16x16x32_bf16 v[152:155], v[48:51], v[208:211], v[152:155]\n" \
"ds_read_b64_tr_b16 v[22:23], v232 offset:10752\n" \
"v_accvgpr_read_b32 v237, a139\n" \
"v_mfma_f32_16x16x32_bf16 v[152:155], v[52:55], v[212:215], v[152:155]\n" \
"ds_read_b64_tr_b16 v[24:25], v233 offset:512\n" \
"v_mul_f32 v234, s92, v234\n" \
"v_mfma_f32_16x16x32_bf16 v[156:159], v[56:59], v[208:211], v[156:159]\n" \
"ds_read_b64_tr_b16 v[26:27], v233 offset:2560\n" \
"v_mul_f32 v235, s92, v235\n" \
"v_mfma_f32_16x16x32_bf16 v[156:159], v[60:63], v[212:215], v[156:159]\n" \
"global_load_lds_dwordx4 v225, s[46:47] offset:1024\n" \
"v_mul_f32 v236, s92, v236\n" \
"v_mfma_f32_16x16x32_bf16 v[176:179], v[32:35], v[216:219], v[176:179]\n" \
"ds_read_b128 v[208:211], v230 offset:4096\n" \
"v_mul_f32 v237, s92, v237\n" \
"v_mfma_f32_16x16x32_bf16 v[176:179], v[36:39], v[220:223], v[176:179]\n" \
"ds_read_b128 v[212:215], v231 offset:4096\n" \
"v_cvt_pk_bf16_f32 v234, v234, v235\n" \
"v_mfma_f32_16x16x32_bf16 v[180:183], v[40:43], v[216:219], v[180:183]\n" \
"ds_read_b64_tr_b16 v[28:29], v233 offset:8704\n" \
"v_cvt_pk_bf16_f32 v236, v236, v237\n" \
"v_mfma_f32_16x16x32_bf16 v[180:183], v[44:47], v[220:223], v[180:183]\n" \
"global_load_lds_dwordx4 v226, s[46:47] offset:2048\n" \
"v_accvgpr_write_b32 a136, v234\n" \
"v_mfma_f32_16x16x32_bf16 v[184:187], v[48:51], v[216:219], v[184:187]\n" \
"ds_read_b64_tr_b16 v[30:31], v233 offset:10752\n" \
"v_accvgpr_write_b32 a137, v236\n" \
"v_mfma_f32_16x16x32_bf16 v[184:187], v[52:55], v[220:223], v[184:187]\n" \
"v_accvgpr_read_b32 v234, a140\n" \
"v_accvgpr_read_b32 v235, a141\n" \
"v_mfma_f32_16x16x32_bf16 v[188:191], v[56:59], v[216:219], v[188:191]\n" \
"v_accvgpr_read_b32 v236, a142\n" \
"v_accvgpr_read_b32 v237, a143\n" \
"v_mfma_f32_16x16x32_bf16 v[188:191], v[60:63], v[220:223], v[188:191]\n" \
"s_waitcnt lgkmcnt(1)\n" \
"global_load_lds_dwordx4 v227, s[46:47] offset:3072\n" \
"v_mul_f32 v234, s92, v234\n" \
"v_mfma_f32_16x16x32_bf16 v[64:67], v[0:3], v[192:195], v[64:67]\n" \
"s_add_u32 s46, s46, 0x100\n" \
"s_addc_u32 s47, s47, 0\n" \
"ds_read_b128 v[216:219], v230 offset:6144\n" \
"v_mfma_f32_16x16x32_bf16 v[64:67], v[4:7], v[196:199], v[64:67]\n" \
"ds_read_b128 v[220:223], v231 offset:6144\n" \
"v_mul_f32 v235, s92, v235\n" \
"v_mfma_f32_16x16x32_bf16 v[68:71], v[8:11], v[192:195], v[68:71]\n" \
"ds_read_b64_tr_b16 v[32:33], v232 offset:1024\n" \
"v_mul_f32 v236, s92, v236\n" \
"v_mfma_f32_16x16x32_bf16 v[68:71], v[12:15], v[196:199], v[68:71]\n" \
"buffer_store_dwordx4 a[232:235], v249, s[56:59], s90 offen offset:2304 sc1\n" \
"v_mul_f32 v237, s92, v237\n" \
"v_cvt_pk_bf16_f32 v234, v234, v235\n" \
"v_cvt_pk_bf16_f32 v236, v236, v237\n" \
"v_accvgpr_write_b32 a138, v234\n" \
"v_mfma_f32_16x16x32_bf16 v[72:75], v[16:19], v[192:195], v[72:75]\n" \
"s_add_u32 s90, s89, 0\n" \
"ds_read_b64_tr_b16 v[34:35], v232 offset:3072\n" \
"v_accvgpr_write_b32 a139, v236\n" \
"v_mfma_f32_16x16x32_bf16 v[72:75], v[20:23], v[196:199], v[72:75]\n" \
"s_waitcnt lgkmcnt(3)\n" \
"ds_read_b64_tr_b16 v[36:37], v232 offset:9216\n" \
"v_accvgpr_read_b32 v234, a168\n" \
"v_mfma_f32_16x16x32_bf16 v[76:79], v[24:27], v[192:195], v[76:79]\n" \
"ds_read_b64_tr_b16 v[38:39], v232 offset:11264\n" \
"v_accvgpr_read_b32 v235, a169\n" \
"v_mfma_f32_16x16x32_bf16 v[76:79], v[28:31], v[196:199], v[76:79]\n" \
"buffer_store_dwordx4 a[208:211], v248, s[56:59], s90 offen offset:0 sc1\n" \
"v_accvgpr_read_b32 v236, a170\n" \
"v_accvgpr_read_b32 v237, a171\n" \
"v_mul_f32 v234, s92, v234\n" \
"v_mul_f32 v235, s92, v235\n" \
"v_mfma_f32_16x16x32_bf16 v[96:99], v[0:3], v[200:203], v[96:99]\n" \
"ds_read_b64_tr_b16 v[40:41], v233 offset:1024\n" \
"v_mul_f32 v236, s92, v236\n" \
"v_mfma_f32_16x16x32_bf16 v[96:99], v[4:7], v[204:207], v[96:99]\n" \
"ds_read_b64_tr_b16 v[42:43], v233 offset:3072\n" \
"v_mul_f32 v237, s92, v237\n" \
"v_mfma_f32_16x16x32_bf16 v[100:103], v[8:11], v[200:203], v[100:103]\n" \
"ds_read_b64_tr_b16 v[44:45], v233 offset:9216\n" \
"v_cvt_pk_bf16_f32 v234, v234, v235\n" \
"v_mfma_f32_16x16x32_bf16 v[100:103], v[12:15], v[204:207], v[100:103]\n" \
"v_cvt_pk_bf16_f32 v236, v236, v237\n" \
"v_accvgpr_write_b32 a168, v234\n" \
"v_mfma_f32_16x16x32_bf16 v[104:107], v[16:19], v[200:203], v[104:107]\n" \
"ds_read_b64_tr_b16 v[46:47], v233 offset:11264\n" \
"v_accvgpr_write_b32 a169, v236\n" \
"v_mfma_f32_16x16x32_bf16 v[104:107], v[20:23], v[204:207], v[104:107]\n" \
"ds_read_b64_tr_b16 v[48:49], v232 offset:1536\n" \
"v_accvgpr_read_b32 v234, a172\n" \
"v_mfma_f32_16x16x32_bf16 v[108:111], v[24:27], v[200:203], v[108:111]\n" \
"s_add_u32 m0, s95, 20480\n" \
"ds_read_b64_tr_b16 v[50:51], v232 offset:3584\n" \
"v_accvgpr_read_b32 v235, a173\n" \
"v_mfma_f32_16x16x32_bf16 v[108:111], v[28:31], v[204:207], v[108:111]\n" \
"global_load_lds_dwordx4 v228, s[44:45] offset:-4096\n" \
"v_accvgpr_read_b32 v236, a174\n" \
"v_mfma_f32_16x16x32_bf16 v[128:131], v[0:3], v[208:211], v[128:131]\n" \
"ds_read_b64_tr_b16 v[52:53], v232 offset:9728\n" \
"v_accvgpr_read_b32 v237, a175\n" \
"v_mfma_f32_16x16x32_bf16 v[128:131], v[4:7], v[212:215], v[128:131]\n" \
"ds_read_b64_tr_b16 v[54:55], v232 offset:11776\n" \
"v_mul_f32 v234, s92, v234\n" \
"v_mfma_f32_16x16x32_bf16 v[132:135], v[8:11], v[208:211], v[132:135]\n" \
"ds_read_b64_tr_b16 v[56:57], v233 offset:1536\n" \
"v_mul_f32 v235, s92, v235\n" \
"v_mfma_f32_16x16x32_bf16 v[132:135], v[12:15], v[212:215], v[132:135]\n" \
"global_load_lds_dwordx4 v229, s[44:45] offset:-3072\n" \
"v_mul_f32 v236, s92, v236\n" \
"v_mfma_f32_16x16x32_bf16 v[136:139], v[16:19], v[208:211], v[136:139]\n" \
"s_add_u32 s44, s44, 0x80000\n" \
"s_addc_u32 s45, s45, 0\n" \
"ds_read_b64_tr_b16 v[58:59], v233 offset:3584\n" \
"v_mfma_f32_16x16x32_bf16 v[136:139], v[20:23], v[212:215], v[136:139]\n" \
"ds_read_b64_tr_b16 v[60:61], v233 offset:9728\n" \
"v_mul_f32 v237, s92, v237\n" \
"v_mfma_f32_16x16x32_bf16 v[140:143], v[24:27], v[208:211], v[140:143]\n" \
"ds_read_b64_tr_b16 v[62:63], v233 offset:11776\n" \
"v_cvt_pk_bf16_f32 v234, v234, v235\n" \
"v_mfma_f32_16x16x32_bf16 v[140:143], v[28:31], v[212:215], v[140:143]\n" \
"s_waitcnt lgkmcnt(3)\n" \
"global_load_lds_dwordx4 v228, s[42:43] offset:-2048\n" \
"v_cvt_pk_bf16_f32 v236, v236, v237\n" \
"v_mfma_f32_16x16x32_bf16 v[160:163], v[0:3], v[216:219], v[160:163]\n" \
"v_add_u32 v230, 0x8000, v230\n" \
"v_add_u32 v231, 0x8000, v231\n" \
"v_mfma_f32_16x16x32_bf16 v[160:163], v[4:7], v[220:223], v[160:163]\n" \
"v_add_u32 v232, 0x4000, v232\n" \
"v_add_u32 v233, 0x4000, v233\n" \
"v_mfma_f32_16x16x32_bf16 v[164:167], v[8:11], v[216:219], v[164:167]\n" \
"v_accvgpr_write_b32 a170, v234\n" \
"v_accvgpr_write_b32 a171, v236\n" \
"v_mfma_f32_16x16x32_bf16 v[164:167], v[12:15], v[220:223], v[164:167]\n" \
"global_load_lds_dwordx4 v229, s[42:43] offset:-1024\n" \
"v_accvgpr_read_b32 v234, a144\n" \
"v_mfma_f32_16x16x32_bf16 v[168:171], v[16:19], v[216:219], v[168:171]\n" \
"s_add_u32 s42, s42, 0x80000\n" \
"s_addc_u32 s43, s43, 0\n" \
"v_accvgpr_read_b32 v235, a145\n" \
"v_mfma_f32_16x16x32_bf16 v[168:171], v[20:23], v[220:223], v[168:171]\n" \
"v_accvgpr_read_b32 v236, a146\n" \
"v_accvgpr_read_b32 v237, a147\n" \
"v_mfma_f32_16x16x32_bf16 v[172:175], v[24:27], v[216:219], v[172:175]\n" \
"s_waitcnt vmcnt(19) lgkmcnt(0)\n" \
"v_cvt_pk_bf16_f32 v234, v234, v235\n" \
"v_cvt_pk_bf16_f32 v236, v236, v237\n" \
"v_mfma_f32_16x16x32_bf16 v[172:175], v[28:31], v[220:223], v[172:175]\n" \
"s_barrier\n" \
"v_mfma_f32_16x16x32_bf16 v[80:83], v[32:35], v[192:195], v[80:83]\n" \
"ds_read_b64_tr_b16 v[0:1], v232 offset:0\n" \
"v_accvgpr_write_b32 a144, v234\n" \
"v_mfma_f32_16x16x32_bf16 v[80:83], v[36:39], v[196:199], v[80:83]\n" \
"ds_read_b64_tr_b16 v[2:3], v232 offset:2048\n" \
"v_accvgpr_write_b32 a145, v236\n" \
"v_mfma_f32_16x16x32_bf16 v[84:87], v[40:43], v[192:195], v[84:87]\n" \
"s_add_u32 m0, s87, 102400\n" \
"ds_read_b64_tr_b16 v[4:5], v232 offset:8192\n" \
"v_accvgpr_read_b32 v234, a176\n" \
"v_mfma_f32_16x16x32_bf16 v[84:87], v[44:47], v[196:199], v[84:87]\n" \
"global_load_lds_dwordx4 v224, s[48:49] offset:-4096\n" \
"v_accvgpr_read_b32 v235, a177\n" \
"v_mfma_f32_16x16x32_bf16 v[88:91], v[48:51], v[192:195], v[88:91]\n" \
"ds_read_b64_tr_b16 v[6:7], v232 offset:10240\n" \
"v_accvgpr_read_b32 v236, a178\n" \
"v_mfma_f32_16x16x32_bf16 v[88:91], v[52:55], v[196:199], v[88:91]\n" \
"ds_read_b64_tr_b16 v[8:9], v233 offset:0\n" \
"v_accvgpr_read_b32 v237, a179\n" \
"v_mfma_f32_16x16x32_bf16 v[92:95], v[56:59], v[192:195], v[92:95]\n" \
"ds_read_b64_tr_b16 v[10:11], v233 offset:2048\n" \
"v_cvt_pk_bf16_f32 v234, v234, v235\n" \
"v_mfma_f32_16x16x32_bf16 v[92:95], v[60:63], v[196:199], v[92:95]\n" \
"global_load_lds_dwordx4 v225, s[48:49] offset:-3072\n" \
"v_cvt_pk_bf16_f32 v236, v236, v237\n" \
"v_mfma_f32_16x16x32_bf16 v[112:115], v[32:35], v[200:203], v[112:115]\n" \
"ds_read_b128 v[192:195], v230 offset:0\n" \
"v_accvgpr_write_b32 a146, v234\n" \
"v_mfma_f32_16x16x32_bf16 v[112:115], v[36:39], v[204:207], v[112:115]\n" \
"ds_read_b128 v[196:199], v231 offset:0\n" \
"v_accvgpr_write_b32 a147, v236\n" \
"v_mfma_f32_16x16x32_bf16 v[116:119], v[40:43], v[200:203], v[116:119]\n" \
"ds_read_b64_tr_b16 v[12:13], v233 offset:8192\n" \
"v_accvgpr_read_b32 v234, a148\n" \
"v_mfma_f32_16x16x32_bf16 v[116:119], v[44:47], v[204:207], v[116:119]\n" \
"global_load_lds_dwordx4 v226, s[48:49] offset:-2048\n" \
"v_accvgpr_read_b32 v235, a149\n" \
"v_mfma_f32_16x16x32_bf16 v[120:123], v[48:51], v[200:203], v[120:123]\n" \
"ds_read_b64_tr_b16 v[14:15], v233 offset:10240\n" \
"v_accvgpr_read_b32 v236, a150\n" \
"v_mfma_f32_16x16x32_bf16 v[120:123], v[52:55], v[204:207], v[120:123]\n" \
"ds_read_b64_tr_b16 v[16:17], v232 offset:512\n" \
"v_accvgpr_read_b32 v237, a151\n" \
"v_mfma_f32_16x16x32_bf16 v[124:127], v[56:59], v[200:203], v[124:127]\n" \
"ds_read_b64_tr_b16 v[18:19], v232 offset:2560\n" \
"v_cvt_pk_bf16_f32 v234, v234, v235\n" \
"v_mfma_f32_16x16x32_bf16 v[124:127], v[60:63], v[204:207], v[124:127]\n" \
"global_load_lds_dwordx4 v227, s[48:49] offset:-1024\n" \
"v_cvt_pk_bf16_f32 v236, v236, v237\n" \
"v_mfma_f32_16x16x32_bf16 v[144:147], v[32:35], v[208:211], v[144:147]\n" \
"s_add_u32 s48, s48, 0x80\n" \
"s_addc_u32 s49, s49, 0\n" \
"ds_read_b128 v[200:203], v230 offset:2048\n" \
"v_mfma_f32_16x16x32_bf16 v[144:147], v[36:39], v[212:215], v[144:147]\n" \
"ds_read_b128 v[204:207], v231 offset:2048\n" \
"v_accvgpr_write_b32 a148, v234\n" \
"v_mfma_f32_16x16x32_bf16 v[148:151], v[40:43], v[208:211], v[148:151]\n" \
"ds_read_b64_tr_b16 v[20:21], v232 offset:8704\n" \
"v_accvgpr_write_b32 a149, v236\n" \
"v_mfma_f32_16x16x32_bf16 v[148:151], v[44:47], v[212:215], v[148:151]\n" \
"global_load_lds_dwordx4 v224, s[46:47] offset:0\n" \
"v_accvgpr_read_b32 v234, a180\n" \
"v_mfma_f32_16x16x32_bf16 v[152:155], v[48:51], v[208:211], v[152:155]\n" \
"ds_read_b64_tr_b16 v[22:23], v232 offset:10752\n" \
"v_accvgpr_read_b32 v235, a181\n" \
"v_mfma_f32_16x16x32_bf16 v[152:155], v[52:55], v[212:215], v[152:155]\n" \
"ds_read_b64_tr_b16 v[24:25], v233 offset:512\n" \
"v_accvgpr_read_b32 v236, a182\n" \
"v_mfma_f32_16x16x32_bf16 v[156:159], v[56:59], v[208:211], v[156:159]\n" \
"ds_read_b64_tr_b16 v[26:27], v233 offset:2560\n" \
"v_accvgpr_read_b32 v237, a183\n" \
"v_mfma_f32_16x16x32_bf16 v[156:159], v[60:63], v[212:215], v[156:159]\n" \
"global_load_lds_dwordx4 v225, s[46:47] offset:1024\n" \
"v_cvt_pk_bf16_f32 v234, v234, v235\n" \
"v_mfma_f32_16x16x32_bf16 v[176:179], v[32:35], v[216:219], v[176:179]\n" \
"ds_read_b128 v[208:211], v230 offset:4096\n" \
"v_cvt_pk_bf16_f32 v236, v236, v237\n" \
"v_mfma_f32_16x16x32_bf16 v[176:179], v[36:39], v[220:223], v[176:179]\n" \
"ds_read_b128 v[212:215], v231 offset:4096\n" \
"v_accvgpr_write_b32 a150, v234\n" \
"v_mfma_f32_16x16x32_bf16 v[180:183], v[40:43], v[216:219], v[180:183]\n" \
"ds_read_b64_tr_b16 v[28:29], v233 offset:8704\n" \
"v_accvgpr_write_b32 a151, v236\n" \
"v_mfma_f32_16x16x32_bf16 v[180:183], v[44:47], v[220:223], v[180:183]\n" \
"global_load_lds_dwordx4 v226, s[46:47] offset:2048\n" \
"v_accvgpr_read_b32 v234, a152\n" \
"v_mfma_f32_16x16x32_bf16 v[184:187], v[48:51], v[216:219], v[184:187]\n" \
"Lpg2768_%=:\n" \
"s_cbranch_execz Lpg4933_%=\n" \
"ds_read_b64_tr_b16 v[30:31], v233 offset:10752\n" \
"v_accvgpr_read_b32 v235, a153\n" \
"v_mfma_f32_16x16x32_bf16 v[184:187], v[52:55], v[220:223], v[184:187]\n" \
"v_accvgpr_read_b32 v236, a154\n" \
"v_accvgpr_read_b32 v237, a155\n" \
"v_mfma_f32_16x16x32_bf16 v[188:191], v[56:59], v[216:219], v[188:191]\n" \
"v_cvt_pk_bf16_f32 v234, v234, v235\n" \
"v_cvt_pk_bf16_f32 v236, v236, v237\n" \
"v_mfma_f32_16x16x32_bf16 v[188:191], v[60:63], v[220:223], v[188:191]\n" \
"s_waitcnt lgkmcnt(1)\n" \
"global_load_lds_dwordx4 v227, s[46:47] offset:3072\n" \
"v_accvgpr_write_b32 a152, v234\n" \
"v_mfma_f32_16x16x32_bf16 v[64:67], v[0:3], v[192:195], v[64:67]\n" \
"s_add_u32 s46, s46, 0x80\n" \
"s_addc_u32 s47, s47, 0\n" \
"ds_read_b128 v[216:219], v230 offset:6144\n" \
"v_mfma_f32_16x16x32_bf16 v[64:67], v[4:7], v[196:199], v[64:67]\n" \
"ds_read_b128 v[220:223], v231 offset:6144\n" \
"v_accvgpr_write_b32 a153, v236\n" \
"v_mfma_f32_16x16x32_bf16 v[68:71], v[8:11], v[192:195], v[68:71]\n" \
"ds_read_b64_tr_b16 v[32:33], v232 offset:1024\n" \
"v_accvgpr_read_b32 v234, a184\n" \
"v_mfma_f32_16x16x32_bf16 v[68:71], v[12:15], v[196:199], v[68:71]\n" \
"buffer_store_dwordx4 a[212:215], v248, s[56:59], s90 offen offset:1024 sc1\n" \
"v_accvgpr_read_b32 v235, a185\n" \
"v_accvgpr_read_b32 v236, a186\n" \
"v_accvgpr_read_b32 v237, a187\n" \
"v_cvt_pk_bf16_f32 v234, v234, v235\n" \
"v_mfma_f32_16x16x32_bf16 v[72:75], v[16:19], v[192:195], v[72:75]\n" \
"ds_read_b64_tr_b16 v[34:35], v232 offset:3072\n" \
"v_cvt_pk_bf16_f32 v236, v236, v237\n" \
"v_mfma_f32_16x16x32_bf16 v[72:75], v[20:23], v[196:199], v[72:75]\n" \
"s_waitcnt lgkmcnt(3)\n" \
"ds_read_b64_tr_b16 v[36:37], v232 offset:9216\n" \
"v_accvgpr_write_b32 a154, v234\n" \
"v_mfma_f32_16x16x32_bf16 v[76:79], v[24:27], v[192:195], v[76:79]\n" \
"ds_read_b64_tr_b16 v[38:39], v232 offset:11264\n" \
"v_accvgpr_write_b32 a155, v236\n" \
"v_mfma_f32_16x16x32_bf16 v[76:79], v[28:31], v[196:199], v[76:79]\n" \
"v_accvgpr_read_b32 v234, a156\n" \
"v_accvgpr_read_b32 v235, a157\n" \
"v_mfma_f32_16x16x32_bf16 v[96:99], v[0:3], v[200:203], v[96:99]\n" \
"ds_read_b64_tr_b16 v[40:41], v233 offset:1024\n" \
"v_accvgpr_read_b32 v236, a158\n" \
"v_mfma_f32_16x16x32_bf16 v[96:99], v[4:7], v[204:207], v[96:99]\n" \
"ds_read_b64_tr_b16 v[42:43], v233 offset:3072\n" \
"v_accvgpr_read_b32 v237, a159\n" \
"v_mfma_f32_16x16x32_bf16 v[100:103], v[8:11], v[200:203], v[100:103]\n" \
"ds_read_b64_tr_b16 v[44:45], v233 offset:9216\n" \
"v_cvt_pk_bf16_f32 v234, v234, v235\n" \
"v_mfma_f32_16x16x32_bf16 v[100:103], v[12:15], v[204:207], v[100:103]\n" \
"v_cvt_pk_bf16_f32 v236, v236, v237\n" \
"v_accvgpr_write_b32 a156, v234\n" \
"v_mfma_f32_16x16x32_bf16 v[104:107], v[16:19], v[200:203], v[104:107]\n" \
"ds_read_b64_tr_b16 v[46:47], v233 offset:11264\n" \
"v_accvgpr_write_b32 a157, v236\n" \
"v_mfma_f32_16x16x32_bf16 v[104:107], v[20:23], v[204:207], v[104:107]\n" \
"ds_read_b64_tr_b16 v[48:49], v232 offset:1536\n" \
"v_accvgpr_read_b32 v234, a188\n" \
"v_mfma_f32_16x16x32_bf16 v[108:111], v[24:27], v[200:203], v[108:111]\n" \
"s_add_u32 m0, s95, 36864\n" \
"ds_read_b64_tr_b16 v[50:51], v232 offset:3584\n" \
"v_accvgpr_read_b32 v235, a189\n" \
"v_mfma_f32_16x16x32_bf16 v[108:111], v[28:31], v[204:207], v[108:111]\n" \
"global_load_lds_dwordx4 v228, s[44:45] offset:-4096\n" \
"v_accvgpr_read_b32 v236, a190\n" \
"v_mfma_f32_16x16x32_bf16 v[128:131], v[0:3], v[208:211], v[128:131]\n" \
"ds_read_b64_tr_b16 v[52:53], v232 offset:9728\n" \
"v_accvgpr_read_b32 v237, a191\n" \
"v_mfma_f32_16x16x32_bf16 v[128:131], v[4:7], v[212:215], v[128:131]\n" \
"ds_read_b64_tr_b16 v[54:55], v232 offset:11776\n" \
"v_cvt_pk_bf16_f32 v234, v234, v235\n" \
"v_mfma_f32_16x16x32_bf16 v[132:135], v[8:11], v[208:211], v[132:135]\n" \
"ds_read_b64_tr_b16 v[56:57], v233 offset:1536\n" \
"v_cvt_pk_bf16_f32 v236, v236, v237\n" \
"v_mfma_f32_16x16x32_bf16 v[132:135], v[12:15], v[212:215], v[132:135]\n" \
"global_load_lds_dwordx4 v229, s[44:45] offset:-3072\n" \
"v_accvgpr_write_b32 a158, v234\n" \
"v_mfma_f32_16x16x32_bf16 v[136:139], v[16:19], v[208:211], v[136:139]\n" \
"s_add_u32 s44, s44, 0x80000\n" \
"s_addc_u32 s45, s45, 0\n" \
"ds_read_b64_tr_b16 v[58:59], v233 offset:3584\n" \
"v_mfma_f32_16x16x32_bf16 v[136:139], v[20:23], v[212:215], v[136:139]\n" \
"ds_read_b64_tr_b16 v[60:61], v233 offset:9728\n" \
"v_accvgpr_write_b32 a159, v236\n" \
"v_mfma_f32_16x16x32_bf16 v[140:143], v[24:27], v[208:211], v[140:143]\n" \
"ds_read_b64_tr_b16 v[62:63], v233 offset:11776\n" \
"v_accvgpr_read_b32 v234, a64\n" \
"v_mfma_f32_16x16x32_bf16 v[140:143], v[28:31], v[212:215], v[140:143]\n" \
"s_waitcnt lgkmcnt(3)\n" \
"global_load_lds_dwordx4 v228, s[42:43] offset:-2048\n" \
"v_accvgpr_read_b32 v235, a65\n" \
"v_mfma_f32_16x16x32_bf16 v[160:163], v[0:3], v[216:219], v[160:163]\n" \
"v_add_u32 v230, 0xffff0000, v230\n" \
"v_add_u32 v231, 0xffff0000, v231\n" \
"v_mfma_f32_16x16x32_bf16 v[160:163], v[4:7], v[220:223], v[160:163]\n" \
"v_add_u32 v232, 0xffff4000, v232\n" \
"v_add_u32 v233, 0xffff4000, v233\n" \
"v_mfma_f32_16x16x32_bf16 v[164:167], v[8:11], v[216:219], v[164:167]\n" \
"v_accvgpr_read_b32 v236, a66\n" \
"v_accvgpr_read_b32 v237, a67\n" \
"v_mfma_f32_16x16x32_bf16 v[164:167], v[12:15], v[220:223], v[164:167]\n" \
"global_load_lds_dwordx4 v229, s[42:43] offset:-1024\n" \
"v_mul_f32 v234, s92, v234\n" \
"v_mfma_f32_16x16x32_bf16 v[168:171], v[16:19], v[216:219], v[168:171]\n" \
"s_add_u32 s42, s42, 0x80000\n" \
"s_addc_u32 s43, s43, 0\n" \
"v_mul_f32 v235, s92, v235\n" \
"v_mfma_f32_16x16x32_bf16 v[168:171], v[20:23], v[220:223], v[168:171]\n" \
"v_mul_f32 v236, s92, v236\n" \
"v_mul_f32 v237, s92, v237\n" \
"v_mfma_f32_16x16x32_bf16 v[172:175], v[24:27], v[216:219], v[172:175]\n" \
"s_waitcnt vmcnt(19) lgkmcnt(0)\n" \
"v_cvt_pk_bf16_f32 v234, v234, v235\n" \
"v_cvt_pk_bf16_f32 v236, v236, v237\n" \
"v_mfma_f32_16x16x32_bf16 v[172:175], v[28:31], v[220:223], v[172:175]\n" \
"s_barrier\n" \
"v_mfma_f32_16x16x32_bf16 v[80:83], v[32:35], v[192:195], v[80:83]\n" \
"ds_read_b64_tr_b16 v[0:1], v232 offset:0\n" \
"v_accvgpr_write_b32 a64, v234\n" \
"v_mfma_f32_16x16x32_bf16 v[80:83], v[36:39], v[196:199], v[80:83]\n" \
"ds_read_b64_tr_b16 v[2:3], v232 offset:2048\n" \
"v_accvgpr_write_b32 a65, v236\n" \
"v_mfma_f32_16x16x32_bf16 v[84:87], v[40:43], v[192:195], v[84:87]\n" \
"s_add_u32 m0, s87, 135168\n" \
"ds_read_b64_tr_b16 v[4:5], v232 offset:8192\n" \
"v_accvgpr_read_b32 v234, a68\n" \
"v_mfma_f32_16x16x32_bf16 v[84:87], v[44:47], v[196:199], v[84:87]\n" \
"global_load_lds_dwordx4 v224, s[48:49] offset:-4096\n" \
"v_accvgpr_read_b32 v235, a69\n" \
"v_mfma_f32_16x16x32_bf16 v[88:91], v[48:51], v[192:195], v[88:91]\n" \
"ds_read_b64_tr_b16 v[6:7], v232 offset:10240\n" \
"v_accvgpr_read_b32 v236, a70\n" \
"v_mfma_f32_16x16x32_bf16 v[88:91], v[52:55], v[196:199], v[88:91]\n" \
"ds_read_b64_tr_b16 v[8:9], v233 offset:0\n" \
"v_accvgpr_read_b32 v237, a71\n" \
"v_mfma_f32_16x16x32_bf16 v[92:95], v[56:59], v[192:195], v[92:95]\n" \
"ds_read_b64_tr_b16 v[10:11], v233 offset:2048\n" \
"v_mul_f32 v234, s92, v234\n" \
"v_mfma_f32_16x16x32_bf16 v[92:95], v[60:63], v[196:199], v[92:95]\n" \
"global_load_lds_dwordx4 v225, s[48:49] offset:-3072\n" \
"v_mul_f32 v235, s92, v235\n" \
"v_mfma_f32_16x16x32_bf16 v[112:115], v[32:35], v[200:203], v[112:115]\n" \
"ds_read_b128 v[192:195], v230 offset:0\n" \
"v_mul_f32 v236, s92, v236\n" \
"v_mfma_f32_16x16x32_bf16 v[112:115], v[36:39], v[204:207], v[112:115]\n" \
"ds_read_b128 v[196:199], v231 offset:0\n" \
"v_mul_f32 v237, s92, v237\n" \
"v_mfma_f32_16x16x32_bf16 v[116:119], v[40:43], v[200:203], v[116:119]\n" \
"ds_read_b64_tr_b16 v[12:13], v233 offset:8192\n" \
"v_cvt_pk_bf16_f32 v234, v234, v235\n" \
"v_mfma_f32_16x16x32_bf16 v[116:119], v[44:47], v[204:207], v[116:119]\n" \
"global_load_lds_dwordx4 v226, s[48:49] offset:-2048\n" \
"v_cvt_pk_bf16_f32 v236, v236, v237\n" \
"v_mfma_f32_16x16x32_bf16 v[120:123], v[48:51], v[200:203], v[120:123]\n" \
"ds_read_b64_tr_b16 v[14:15], v233 offset:10240\n" \
"v_accvgpr_write_b32 a66, v234\n" \
"v_mfma_f32_16x16x32_bf16 v[120:123], v[52:55], v[204:207], v[120:123]\n" \
"ds_read_b64_tr_b16 v[16:17], v232 offset:512\n" \
"v_accvgpr_write_b32 a67, v236\n" \
"v_mfma_f32_16x16x32_bf16 v[124:127], v[56:59], v[200:203], v[124:127]\n" \
"ds_read_b64_tr_b16 v[18:19], v232 offset:2560\n" \
"v_accvgpr_read_b32 v234, a96\n" \
"v_mfma_f32_16x16x32_bf16 v[124:127], v[60:63], v[204:207], v[124:127]\n" \
"global_load_lds_dwordx4 v227, s[48:49] offset:-1024\n" \
"v_accvgpr_read_b32 v235, a97\n" \
"v_mfma_f32_16x16x32_bf16 v[144:147], v[32:35], v[208:211], v[144:147]\n" \
"s_add_u32 s48, s48, 0x80\n" \
"s_addc_u32 s49, s49, 0\n" \
"ds_read_b128 v[200:203], v230 offset:2048\n" \
"v_mfma_f32_16x16x32_bf16 v[144:147], v[36:39], v[212:215], v[144:147]\n" \
"ds_read_b128 v[204:207], v231 offset:2048\n" \
"v_accvgpr_read_b32 v236, a98\n" \
"v_mfma_f32_16x16x32_bf16 v[148:151], v[40:43], v[208:211], v[148:151]\n" \
"ds_read_b64_tr_b16 v[20:21], v232 offset:8704\n" \
"v_accvgpr_read_b32 v237, a99\n" \
"v_mfma_f32_16x16x32_bf16 v[148:151], v[44:47], v[212:215], v[148:151]\n" \
"global_load_lds_dwordx4 v224, s[46:47] offset:0\n" \
"v_mul_f32 v234, s92, v234\n" \
"v_mfma_f32_16x16x32_bf16 v[152:155], v[48:51], v[208:211], v[152:155]\n" \
"ds_read_b64_tr_b16 v[22:23], v232 offset:10752\n" \
"v_mul_f32 v235, s92, v235\n" \
"v_mfma_f32_16x16x32_bf16 v[152:155], v[52:55], v[212:215], v[152:155]\n" \
"ds_read_b64_tr_b16 v[24:25], v233 offset:512\n" \
"v_mul_f32 v236, s92, v236\n" \
"v_mfma_f32_16x16x32_bf16 v[156:159], v[56:59], v[208:211], v[156:159]\n" \
"ds_read_b64_tr_b16 v[26:27], v233 offset:2560\n" \
"v_mul_f32 v237, s92, v237\n" \
"v_mfma_f32_16x16x32_bf16 v[156:159], v[60:63], v[212:215], v[156:159]\n" \
"global_load_lds_dwordx4 v225, s[46:47] offset:1024\n" \
"v_cvt_pk_bf16_f32 v234, v234, v235\n" \
"v_mfma_f32_16x16x32_bf16 v[176:179], v[32:35], v[216:219], v[176:179]\n" \
"ds_read_b128 v[208:211], v230 offset:4096\n" \
"v_cvt_pk_bf16_f32 v236, v236, v237\n" \
"v_mfma_f32_16x16x32_bf16 v[176:179], v[36:39], v[220:223], v[176:179]\n" \
"ds_read_b128 v[212:215], v231 offset:4096\n" \
"v_accvgpr_write_b32 a96, v234\n" \
"v_mfma_f32_16x16x32_bf16 v[180:183], v[40:43], v[216:219], v[180:183]\n" \
"ds_read_b64_tr_b16 v[28:29], v233 offset:8704\n" \
"v_accvgpr_write_b32 a97, v236\n" \
"v_mfma_f32_16x16x32_bf16 v[180:183], v[44:47], v[220:223], v[180:183]\n" \
"global_load_lds_dwordx4 v226, s[46:47] offset:2048\n" \
"v_accvgpr_read_b32 v234, a100\n" \
"v_mfma_f32_16x16x32_bf16 v[184:187], v[48:51], v[216:219], v[184:187]\n" \
"ds_read_b64_tr_b16 v[30:31], v233 offset:10752\n" \
"v_accvgpr_read_b32 v235, a101\n" \
"v_mfma_f32_16x16x32_bf16 v[184:187], v[52:55], v[220:223], v[184:187]\n" \
"v_accvgpr_read_b32 v236, a102\n" \
"v_accvgpr_read_b32 v237, a103\n" \
"v_mfma_f32_16x16x32_bf16 v[188:191], v[56:59], v[216:219], v[188:191]\n" \
"v_mul_f32 v234, s92, v234\n" \
"v_mul_f32 v235, s92, v235\n" \
"v_mfma_f32_16x16x32_bf16 v[188:191], v[60:63], v[220:223], v[188:191]\n" \
"s_waitcnt lgkmcnt(1)\n" \
"global_load_lds_dwordx4 v227, s[46:47] offset:3072\n" \
"v_mul_f32 v236, s92, v236\n" \
"v_mfma_f32_16x16x32_bf16 v[64:67], v[0:3], v[192:195], v[64:67]\n" \
"s_add_u32 s46, s46, 0x80\n" \
"s_addc_u32 s47, s47, 0\n" \
"ds_read_b128 v[216:219], v230 offset:6144\n" \
"v_mfma_f32_16x16x32_bf16 v[64:67], v[4:7], v[196:199], v[64:67]\n" \
"ds_read_b128 v[220:223], v231 offset:6144\n" \
"v_mul_f32 v237, s92, v237\n" \
"v_mfma_f32_16x16x32_bf16 v[68:71], v[8:11], v[192:195], v[68:71]\n" \
"ds_read_b64_tr_b16 v[32:33], v232 offset:1024\n" \
"v_cvt_pk_bf16_f32 v234, v234, v235\n" \
"v_mfma_f32_16x16x32_bf16 v[68:71], v[12:15], v[196:199], v[68:71]\n" \
"buffer_store_dwordx4 a[216:219], v248, s[56:59], s90 offen offset:2048 sc1\n" \
"v_cvt_pk_bf16_f32 v236, v236, v237\n" \
"v_accvgpr_write_b32 a98, v234\n" \
"v_accvgpr_write_b32 a99, v236\n" \
"v_accvgpr_read_b32 v234, a72\n" \
"v_mfma_f32_16x16x32_bf16 v[72:75], v[16:19], v[192:195], v[72:75]\n" \
"ds_read_b64_tr_b16 v[34:35], v232 offset:3072\n" \
"v_accvgpr_read_b32 v235, a73\n" \
"v_mfma_f32_16x16x32_bf16 v[72:75], v[20:23], v[196:199], v[72:75]\n" \
"s_waitcnt lgkmcnt(3)\n" \
"ds_read_b64_tr_b16 v[36:37], v232 offset:9216\n" \
"v_accvgpr_read_b32 v236, a74\n" \
"v_mfma_f32_16x16x32_bf16 v[76:79], v[24:27], v[192:195], v[76:79]\n" \
"ds_read_b64_tr_b16 v[38:39], v232 offset:11264\n" \
"v_accvgpr_read_b32 v237, a75\n" \
"v_mfma_f32_16x16x32_bf16 v[76:79], v[28:31], v[196:199], v[76:79]\n" \
"v_mul_f32 v234, s92, v234\n" \
"v_mul_f32 v235, s92, v235\n" \
"v_mfma_f32_16x16x32_bf16 v[96:99], v[0:3], v[200:203], v[96:99]\n" \
"ds_read_b64_tr_b16 v[40:41], v233 offset:1024\n" \
"v_mul_f32 v236, s92, v236\n" \
"v_mfma_f32_16x16x32_bf16 v[96:99], v[4:7], v[204:207], v[96:99]\n" \
"ds_read_b64_tr_b16 v[42:43], v233 offset:3072\n" \
"v_mul_f32 v237, s92, v237\n" \
"v_mfma_f32_16x16x32_bf16 v[100:103], v[8:11], v[200:203], v[100:103]\n" \
"ds_read_b64_tr_b16 v[44:45], v233 offset:9216\n" \
"v_cvt_pk_bf16_f32 v234, v234, v235\n" \
"v_mfma_f32_16x16x32_bf16 v[100:103], v[12:15], v[204:207], v[100:103]\n" \
"v_cvt_pk_bf16_f32 v236, v236, v237\n" \
"v_accvgpr_write_b32 a72, v234\n" \
"v_mfma_f32_16x16x32_bf16 v[104:107], v[16:19], v[200:203], v[104:107]\n" \
"ds_read_b64_tr_b16 v[46:47], v233 offset:11264\n" \
"v_accvgpr_write_b32 a73, v236\n" \
"v_mfma_f32_16x16x32_bf16 v[104:107], v[20:23], v[204:207], v[104:107]\n" \
"ds_read_b64_tr_b16 v[48:49], v232 offset:1536\n" \
"v_accvgpr_read_b32 v234, a76\n" \
"v_mfma_f32_16x16x32_bf16 v[108:111], v[24:27], v[200:203], v[108:111]\n" \
"s_add_u32 m0, s95, 53248\n" \
"ds_read_b64_tr_b16 v[50:51], v232 offset:3584\n" \
"v_accvgpr_read_b32 v235, a77\n" \
"v_mfma_f32_16x16x32_bf16 v[108:111], v[28:31], v[204:207], v[108:111]\n" \
"global_load_lds_dwordx4 v228, s[44:45] offset:-4096\n" \
"v_accvgpr_read_b32 v236, a78\n" \
"v_mfma_f32_16x16x32_bf16 v[128:131], v[0:3], v[208:211], v[128:131]\n" \
"ds_read_b64_tr_b16 v[52:53], v232 offset:9728\n" \
"v_accvgpr_read_b32 v237, a79\n" \
"v_mfma_f32_16x16x32_bf16 v[128:131], v[4:7], v[212:215], v[128:131]\n" \
"ds_read_b64_tr_b16 v[54:55], v232 offset:11776\n" \
"v_mul_f32 v234, s92, v234\n" \
"v_mfma_f32_16x16x32_bf16 v[132:135], v[8:11], v[208:211], v[132:135]\n" \
"ds_read_b64_tr_b16 v[56:57], v233 offset:1536\n" \
"v_mul_f32 v235, s92, v235\n" \
"v_mfma_f32_16x16x32_bf16 v[132:135], v[12:15], v[212:215], v[132:135]\n" \
"global_load_lds_dwordx4 v229, s[44:45] offset:-3072\n" \
"v_mul_f32 v236, s92, v236\n" \
"v_mfma_f32_16x16x32_bf16 v[136:139], v[16:19], v[208:211], v[136:139]\n" \
"s_add_u32 s44, s44, 0x80000\n" \
"s_addc_u32 s45, s45, 0\n" \
"ds_read_b64_tr_b16 v[58:59], v233 offset:3584\n" \
"v_mfma_f32_16x16x32_bf16 v[136:139], v[20:23], v[212:215], v[136:139]\n" \
"ds_read_b64_tr_b16 v[60:61], v233 offset:9728\n" \
"v_mul_f32 v237, s92, v237\n" \
"v_mfma_f32_16x16x32_bf16 v[140:143], v[24:27], v[208:211], v[140:143]\n" \
"ds_read_b64_tr_b16 v[62:63], v233 offset:11776\n" \
"v_cvt_pk_bf16_f32 v234, v234, v235\n" \
"v_mfma_f32_16x16x32_bf16 v[140:143], v[28:31], v[212:215], v[140:143]\n" \
"s_waitcnt lgkmcnt(3)\n" \
"global_load_lds_dwordx4 v228, s[42:43] offset:-2048\n" \
"v_cvt_pk_bf16_f32 v236, v236, v237\n" \
"v_mfma_f32_16x16x32_bf16 v[160:163], v[0:3], v[216:219], v[160:163]\n" \
"v_add_u32 v230, 0x8000, v230\n" \
"v_add_u32 v231, 0x8000, v231\n" \
"v_mfma_f32_16x16x32_bf16 v[160:163], v[4:7], v[220:223], v[160:163]\n" \
"v_add_u32 v232, 0x4000, v232\n" \
"v_add_u32 v233, 0x4000, v233\n" \
"v_mfma_f32_16x16x32_bf16 v[164:167], v[8:11], v[216:219], v[164:167]\n" \
"v_accvgpr_write_b32 a74, v234\n" \
"v_accvgpr_write_b32 a75, v236\n" \
"v_mfma_f32_16x16x32_bf16 v[164:167], v[12:15], v[220:223], v[164:167]\n" \
"global_load_lds_dwordx4 v229, s[42:43] offset:-1024\n" \
"v_accvgpr_read_b32 v234, a104\n" \
"v_mfma_f32_16x16x32_bf16 v[168:171], v[16:19], v[216:219], v[168:171]\n" \
"s_add_u32 s42, s42, 0x80000\n" \
"s_addc_u32 s43, s43, 0\n" \
"v_accvgpr_read_b32 v235, a105\n" \
"v_mfma_f32_16x16x32_bf16 v[168:171], v[20:23], v[220:223], v[168:171]\n" \
"v_accvgpr_read_b32 v236, a106\n" \
"v_accvgpr_read_b32 v237, a107\n" \
"v_mfma_f32_16x16x32_bf16 v[172:175], v[24:27], v[216:219], v[172:175]\n" \
"s_waitcnt vmcnt(18) lgkmcnt(0)\n" \
"v_mul_f32 v234, s92, v234\n" \
"v_mul_f32 v235, s92, v235\n" \
"v_mfma_f32_16x16x32_bf16 v[172:175], v[28:31], v[220:223], v[172:175]\n" \
"s_barrier\n" \
"v_mfma_f32_16x16x32_bf16 v[80:83], v[32:35], v[192:195], v[80:83]\n" \
"ds_read_b64_tr_b16 v[0:1], v232 offset:0\n" \
"v_mul_f32 v236, s92, v236\n" \
"v_mfma_f32_16x16x32_bf16 v[80:83], v[36:39], v[196:199], v[80:83]\n" \
"ds_read_b64_tr_b16 v[2:3], v232 offset:2048\n" \
"v_mul_f32 v237, s92, v237\n" \
"v_mfma_f32_16x16x32_bf16 v[84:87], v[40:43], v[192:195], v[84:87]\n" \
"s_add_u32 m0, s87, 69632\n" \
"ds_read_b64_tr_b16 v[4:5], v232 offset:8192\n" \
"v_cvt_pk_bf16_f32 v234, v234, v235\n" \
"v_mfma_f32_16x16x32_bf16 v[84:87], v[44:47], v[196:199], v[84:87]\n" \
"global_load_lds_dwordx4 v224, s[48:49] offset:-4096\n" \
"v_cvt_pk_bf16_f32 v236, v236, v237\n" \
"v_mfma_f32_16x16x32_bf16 v[88:91], v[48:51], v[192:195], v[88:91]\n" \
"ds_read_b64_tr_b16 v[6:7], v232 offset:10240\n" \
"v_accvgpr_write_b32 a104, v234\n" \
"v_mfma_f32_16x16x32_bf16 v[88:91], v[52:55], v[196:199], v[88:91]\n" \
"ds_read_b64_tr_b16 v[8:9], v233 offset:0\n" \
"v_accvgpr_write_b32 a105, v236\n" \
"v_mfma_f32_16x16x32_bf16 v[92:95], v[56:59], v[192:195], v[92:95]\n" \
"ds_read_b64_tr_b16 v[10:11], v233 offset:2048\n" \
"v_accvgpr_read_b32 v234, a108\n" \
"v_mfma_f32_16x16x32_bf16 v[92:95], v[60:63], v[196:199], v[92:95]\n" \
"global_load_lds_dwordx4 v225, s[48:49] offset:-3072\n" \
"v_accvgpr_read_b32 v235, a109\n" \
"v_mfma_f32_16x16x32_bf16 v[112:115], v[32:35], v[200:203], v[112:115]\n" \
"ds_read_b128 v[192:195], v230 offset:0\n" \
"v_accvgpr_read_b32 v236, a110\n" \
"v_mfma_f32_16x16x32_bf16 v[112:115], v[36:39], v[204:207], v[112:115]\n" \
"ds_read_b128 v[196:199], v231 offset:0\n" \
"v_accvgpr_read_b32 v237, a111\n" \
"v_mfma_f32_16x16x32_bf16 v[116:119], v[40:43], v[200:203], v[116:119]\n" \
"ds_read_b64_tr_b16 v[12:13], v233 offset:8192\n" \
"v_mul_f32 v234, s92, v234\n" \
"v_mfma_f32_16x16x32_bf16 v[116:119], v[44:47], v[204:207], v[116:119]\n" \
"global_load_lds_dwordx4 v226, s[48:49] offset:-2048\n" \
"v_mul_f32 v235, s92, v235\n" \
"v_mfma_f32_16x16x32_bf16 v[120:123], v[48:51], v[200:203], v[120:123]\n" \
"ds_read_b64_tr_b16 v[14:15], v233 offset:10240\n" \
"v_mul_f32 v236, s92, v236\n" \
"v_mfma_f32_16x16x32_bf16 v[120:123], v[52:55], v[204:207], v[120:123]\n" \
"ds_read_b64_tr_b16 v[16:17], v232 offset:512\n" \
"v_mul_f32 v237, s92, v237\n" \
"v_mfma_f32_16x16x32_bf16 v[124:127], v[56:59], v[200:203], v[124:127]\n" \
"ds_read_b64_tr_b16 v[18:19], v232 offset:2560\n" \
"v_cvt_pk_bf16_f32 v234, v234, v235\n" \
"v_mfma_f32_16x16x32_bf16 v[124:127], v[60:63], v[204:207], v[124:127]\n" \
"global_load_lds_dwordx4 v227, s[48:49] offset:-1024\n" \
"v_cvt_pk_bf16_f32 v236, v236, v237\n" \
"v_mfma_f32_16x16x32_bf16 v[144:147], v[32:35], v[208:211], v[144:147]\n" \
"s_add_u32 s48, s48, 0x80\n" \
"s_addc_u32 s49, s49, 0\n" \
"ds_read_b128 v[200:203], v230 offset:2048\n" \
"v_mfma_f32_16x16x32_bf16 v[144:147], v[36:39], v[212:215], v[144:147]\n" \
"ds_read_b128 v[204:207], v231 offset:2048\n" \
"v_accvgpr_write_b32 a106, v234\n" \
"v_mfma_f32_16x16x32_bf16 v[148:151], v[40:43], v[208:211], v[148:151]\n" \
"ds_read_b64_tr_b16 v[20:21], v232 offset:8704\n" \
"v_accvgpr_write_b32 a107, v236\n" \
"v_mfma_f32_16x16x32_bf16 v[148:151], v[44:47], v[212:215], v[148:151]\n" \
"global_load_lds_dwordx4 v224, s[46:47] offset:0\n" \
"v_accvgpr_read_b32 v234, a80\n" \
"v_mfma_f32_16x16x32_bf16 v[152:155], v[48:51], v[208:211], v[152:155]\n" \
"ds_read_b64_tr_b16 v[22:23], v232 offset:10752\n" \
"v_accvgpr_read_b32 v235, a81\n" \
"v_mfma_f32_16x16x32_bf16 v[152:155], v[52:55], v[212:215], v[152:155]\n" \
"ds_read_b64_tr_b16 v[24:25], v233 offset:512\n" \
"v_accvgpr_read_b32 v236, a82\n" \
"v_mfma_f32_16x16x32_bf16 v[156:159], v[56:59], v[208:211], v[156:159]\n" \
"ds_read_b64_tr_b16 v[26:27], v233 offset:2560\n" \
"v_accvgpr_read_b32 v237, a83\n" \
"v_mfma_f32_16x16x32_bf16 v[156:159], v[60:63], v[212:215], v[156:159]\n" \
"global_load_lds_dwordx4 v225, s[46:47] offset:1024\n" \
"v_cvt_pk_bf16_f32 v234, v234, v235\n" \
"v_mfma_f32_16x16x32_bf16 v[176:179], v[32:35], v[216:219], v[176:179]\n" \
"ds_read_b128 v[208:211], v230 offset:4096\n" \
"v_cvt_pk_bf16_f32 v236, v236, v237\n" \
"v_mfma_f32_16x16x32_bf16 v[176:179], v[36:39], v[220:223], v[176:179]\n" \
"ds_read_b128 v[212:215], v231 offset:4096\n" \
"v_accvgpr_write_b32 a80, v234\n" \
"v_mfma_f32_16x16x32_bf16 v[180:183], v[40:43], v[216:219], v[180:183]\n" \
"ds_read_b64_tr_b16 v[28:29], v233 offset:8704\n" \
"v_accvgpr_write_b32 a81, v236\n" \
"v_mfma_f32_16x16x32_bf16 v[180:183], v[44:47], v[220:223], v[180:183]\n" \
"global_load_lds_dwordx4 v226, s[46:47] offset:2048\n" \
"v_accvgpr_read_b32 v234, a112\n" \
"v_mfma_f32_16x16x32_bf16 v[184:187], v[48:51], v[216:219], v[184:187]\n" \
"ds_read_b64_tr_b16 v[30:31], v233 offset:10752\n" \
"v_accvgpr_read_b32 v235, a113\n" \
"v_mfma_f32_16x16x32_bf16 v[184:187], v[52:55], v[220:223], v[184:187]\n" \
"v_accvgpr_read_b32 v236, a114\n" \
"v_accvgpr_read_b32 v237, a115\n" \
"v_mfma_f32_16x16x32_bf16 v[188:191], v[56:59], v[216:219], v[188:191]\n" \
"v_cvt_pk_bf16_f32 v234, v234, v235\n" \
"v_cvt_pk_bf16_f32 v236, v236, v237\n" \
"v_mfma_f32_16x16x32_bf16 v[188:191], v[60:63], v[220:223], v[188:191]\n" \
"s_waitcnt lgkmcnt(1)\n" \
"global_load_lds_dwordx4 v227, s[46:47] offset:3072\n" \
"v_accvgpr_write_b32 a82, v234\n" \
"v_mfma_f32_16x16x32_bf16 v[64:67], v[0:3], v[192:195], v[64:67]\n" \
"s_add_u32 s46, s46, 0x80\n" \
"s_addc_u32 s47, s47, 0\n" \
"ds_read_b128 v[216:219], v230 offset:6144\n" \
"v_mfma_f32_16x16x32_bf16 v[64:67], v[4:7], v[196:199], v[64:67]\n" \
"ds_read_b128 v[220:223], v231 offset:6144\n" \
"v_accvgpr_write_b32 a83, v236\n" \
"v_mfma_f32_16x16x32_bf16 v[68:71], v[8:11], v[192:195], v[68:71]\n" \
"ds_read_b64_tr_b16 v[32:33], v232 offset:1024\n" \
"v_accvgpr_read_b32 v234, a84\n" \
"v_mfma_f32_16x16x32_bf16 v[68:71], v[12:15], v[196:199], v[68:71]\n" \
"buffer_store_dwordx4 a[220:223], v248, s[56:59], s90 offen offset:3072 sc1\n" \
"v_accvgpr_read_b32 v235, a85\n" \
"v_accvgpr_read_b32 v236, a86\n" \
"v_accvgpr_read_b32 v237, a87\n" \
"v_cvt_pk_bf16_f32 v234, v234, v235\n" \
"v_mfma_f32_16x16x32_bf16 v[72:75], v[16:19], v[192:195], v[72:75]\n" \
"s_add_u32 s90, s88, 139264\n" \
"ds_read_b64_tr_b16 v[34:35], v232 offset:3072\n" \
"v_cvt_pk_bf16_f32 v236, v236, v237\n" \
"v_mfma_f32_16x16x32_bf16 v[72:75], v[20:23], v[196:199], v[72:75]\n" \
"s_waitcnt lgkmcnt(3)\n" \
"ds_read_b64_tr_b16 v[36:37], v232 offset:9216\n" \
"v_accvgpr_write_b32 a84, v234\n" \
"v_mfma_f32_16x16x32_bf16 v[76:79], v[24:27], v[192:195], v[76:79]\n" \
"ds_read_b64_tr_b16 v[38:39], v232 offset:11264\n" \
"v_accvgpr_write_b32 a85, v236\n" \
"v_mfma_f32_16x16x32_bf16 v[76:79], v[28:31], v[196:199], v[76:79]\n" \
"buffer_store_dwordx4 a[128:131], v249, s[56:59], s90 offen offset:0 sc1\n" \
"v_accvgpr_read_b32 v234, a116\n" \
"v_accvgpr_read_b32 v235, a117\n" \
"v_accvgpr_read_b32 v236, a118\n" \
"v_accvgpr_read_b32 v237, a119\n" \
"v_mfma_f32_16x16x32_bf16 v[96:99], v[0:3], v[200:203], v[96:99]\n" \
"ds_read_b64_tr_b16 v[40:41], v233 offset:1024\n" \
"v_cvt_pk_bf16_f32 v234, v234, v235\n" \
"v_mfma_f32_16x16x32_bf16 v[96:99], v[4:7], v[204:207], v[96:99]\n" \
"ds_read_b64_tr_b16 v[42:43], v233 offset:3072\n" \
"v_cvt_pk_bf16_f32 v236, v236, v237\n" \
"v_mfma_f32_16x16x32_bf16 v[100:103], v[8:11], v[200:203], v[100:103]\n" \
"ds_read_b64_tr_b16 v[44:45], v233 offset:9216\n" \
"v_accvgpr_write_b32 a86, v234\n" \
"v_mfma_f32_16x16x32_bf16 v[100:103], v[12:15], v[204:207], v[100:103]\n" \
"v_accvgpr_write_b32 a87, v236\n" \
"v_accvgpr_read_b32 v234, a88\n" \
"v_mfma_f32_16x16x32_bf16 v[104:107], v[16:19], v[200:203], v[104:107]\n" \
"ds_read_b64_tr_b16 v[46:47], v233 offset:11264\n" \
"v_accvgpr_read_b32 v235, a89\n" \
"v_mfma_f32_16x16x32_bf16 v[104:107], v[20:23], v[204:207], v[104:107]\n" \
"ds_read_b64_tr_b16 v[48:49], v232 offset:1536\n" \
"v_accvgpr_read_b32 v236, a90\n" \
"v_mfma_f32_16x16x32_bf16 v[108:111], v[24:27], v[200:203], v[108:111]\n" \
"s_add_u32 m0, s95, 4096\n" \
"ds_read_b64_tr_b16 v[50:51], v232 offset:3584\n" \
"v_accvgpr_read_b32 v237, a91\n" \
"v_mfma_f32_16x16x32_bf16 v[108:111], v[28:31], v[204:207], v[108:111]\n" \
"global_load_lds_dwordx4 v228, s[44:45] offset:-4096\n" \
"v_cvt_pk_bf16_f32 v234, v234, v235\n" \
"v_mfma_f32_16x16x32_bf16 v[128:131], v[0:3], v[208:211], v[128:131]\n" \
"ds_read_b64_tr_b16 v[52:53], v232 offset:9728\n" \
"v_cvt_pk_bf16_f32 v236, v236, v237\n" \
"v_mfma_f32_16x16x32_bf16 v[128:131], v[4:7], v[212:215], v[128:131]\n" \
"ds_read_b64_tr_b16 v[54:55], v232 offset:11776\n" \
"v_accvgpr_write_b32 a88, v234\n" \
"v_mfma_f32_16x16x32_bf16 v[132:135], v[8:11], v[208:211], v[132:135]\n" \
"ds_read_b64_tr_b16 v[56:57], v233 offset:1536\n" \
"v_accvgpr_write_b32 a89, v236\n" \
"v_mfma_f32_16x16x32_bf16 v[132:135], v[12:15], v[212:215], v[132:135]\n" \
"global_load_lds_dwordx4 v229, s[44:45] offset:-3072\n" \
"v_accvgpr_read_b32 v234, a120\n" \
"v_mfma_f32_16x16x32_bf16 v[136:139], v[16:19], v[208:211], v[136:139]\n" \
"s_add_u32 s44, s44, 0x80000\n" \
"s_addc_u32 s45, s45, 0\n" \
"ds_read_b64_tr_b16 v[58:59], v233 offset:3584\n" \
"v_mfma_f32_16x16x32_bf16 v[136:139], v[20:23], v[212:215], v[136:139]\n" \
"ds_read_b64_tr_b16 v[60:61], v233 offset:9728\n" \
"v_accvgpr_read_b32 v235, a121\n" \
"v_mfma_f32_16x16x32_bf16 v[140:143], v[24:27], v[208:211], v[140:143]\n" \
"ds_read_b64_tr_b16 v[62:63], v233 offset:11776\n" \
"v_accvgpr_read_b32 v236, a122\n" \
"v_mfma_f32_16x16x32_bf16 v[140:143], v[28:31], v[212:215], v[140:143]\n" \
"s_waitcnt lgkmcnt(3)\n" \
"global_load_lds_dwordx4 v228, s[42:43] offset:-2048\n" \
"v_accvgpr_read_b32 v237, a123\n" \
"v_mfma_f32_16x16x32_bf16 v[160:163], v[0:3], v[216:219], v[160:163]\n" \
"v_add_u32 v230, 0x8000, v230\n" \
"v_add_u32 v231, 0x8000, v231\n" \
"v_mfma_f32_16x16x32_bf16 v[160:163], v[4:7], v[220:223], v[160:163]\n" \
"v_add_u32 v232, 0x4000, v232\n" \
"v_add_u32 v233, 0x4000, v233\n" \
"v_mfma_f32_16x16x32_bf16 v[164:167], v[8:11], v[216:219], v[164:167]\n" \
"v_cvt_pk_bf16_f32 v234, v234, v235\n" \
"v_cvt_pk_bf16_f32 v236, v236, v237\n" \
"v_mfma_f32_16x16x32_bf16 v[164:167], v[12:15], v[220:223], v[164:167]\n" \
"global_load_lds_dwordx4 v229, s[42:43] offset:-1024\n" \
"v_accvgpr_write_b32 a90, v234\n" \
"v_mfma_f32_16x16x32_bf16 v[168:171], v[16:19], v[216:219], v[168:171]\n" \
"s_add_u32 s42, s42, 0x80000\n" \
"s_addc_u32 s43, s43, 0\n" \
"v_accvgpr_write_b32 a91, v236\n" \
"v_mfma_f32_16x16x32_bf16 v[168:171], v[20:23], v[220:223], v[168:171]\n" \
"v_accvgpr_read_b32 v234, a92\n" \
"v_accvgpr_read_b32 v235, a93\n" \
"v_mfma_f32_16x16x32_bf16 v[172:175], v[24:27], v[216:219], v[172:175]\n" \
"s_waitcnt vmcnt(19) lgkmcnt(0)\n" \
"v_accvgpr_read_b32 v236, a94\n" \
"v_accvgpr_read_b32 v237, a95\n" \
"v_mfma_f32_16x16x32_bf16 v[172:175], v[28:31], v[220:223], v[172:175]\n" \
"s_barrier\n" \
"v_mfma_f32_16x16x32_bf16 v[80:83], v[32:35], v[192:195], v[80:83]\n" \
"ds_read_b64_tr_b16 v[0:1], v232 offset:0\n" \
"v_cvt_pk_bf16_f32 v234, v234, v235\n" \
"v_mfma_f32_16x16x32_bf16 v[80:83], v[36:39], v[196:199], v[80:83]\n" \
"ds_read_b64_tr_b16 v[2:3], v232 offset:2048\n" \
"v_cvt_pk_bf16_f32 v236, v236, v237\n" \
"v_mfma_f32_16x16x32_bf16 v[84:87], v[40:43], v[192:195], v[84:87]\n" \
"s_add_u32 m0, s87, 102400\n" \
"ds_read_b64_tr_b16 v[4:5], v232 offset:8192\n" \
"v_accvgpr_write_b32 a92, v234\n" \
"v_mfma_f32_16x16x32_bf16 v[84:87], v[44:47], v[196:199], v[84:87]\n" \
"Lpg3309_%=:\n" \
"s_cbranch_execz Lpg5398_%=\n" \
"global_load_lds_dwordx4 v224, s[48:49] offset:-4096\n" \
"v_accvgpr_write_b32 a93, v236\n" \
"v_mfma_f32_16x16x32_bf16 v[88:91], v[48:51], v[192:195], v[88:91]\n" \
"ds_read_b64_tr_b16 v[6:7], v232 offset:10240\n" \
"v_accvgpr_read_b32 v234, a124\n" \
"v_mfma_f32_16x16x32_bf16 v[88:91], v[52:55], v[196:199], v[88:91]\n" \
"ds_read_b64_tr_b16 v[8:9], v233 offset:0\n" \
"v_accvgpr_read_b32 v235, a125\n" \
"v_mfma_f32_16x16x32_bf16 v[92:95], v[56:59], v[192:195], v[92:95]\n" \
"ds_read_b64_tr_b16 v[10:11], v233 offset:2048\n" \
"v_accvgpr_read_b32 v236, a126\n" \
"v_mfma_f32_16x16x32_bf16 v[92:95], v[60:63], v[196:199], v[92:95]\n" \
"global_load_lds_dwordx4 v225, s[48:49] offset:-3072\n" \
"v_accvgpr_read_b32 v237, a127\n" \
"v_mfma_f32_16x16x32_bf16 v[112:115], v[32:35], v[200:203], v[112:115]\n" \
"ds_read_b128 v[192:195], v230 offset:0\n" \
"v_cvt_pk_bf16_f32 v234, v234, v235\n" \
"v_mfma_f32_16x16x32_bf16 v[112:115], v[36:39], v[204:207], v[112:115]\n" \
"ds_read_b128 v[196:199], v231 offset:0\n" \
"v_cvt_pk_bf16_f32 v236, v236, v237\n" \
"v_mfma_f32_16x16x32_bf16 v[116:119], v[40:43], v[200:203], v[116:119]\n" \
"ds_read_b64_tr_b16 v[12:13], v233 offset:8192\n" \
"v_accvgpr_write_b32 a94, v234\n" \
"v_mfma_f32_16x16x32_bf16 v[116:119], v[44:47], v[204:207], v[116:119]\n" \
"global_load_lds_dwordx4 v226, s[48:49] offset:-2048\n" \
"v_accvgpr_write_b32 a95, v236\n" \
"v_mfma_f32_16x16x32_bf16 v[120:123], v[48:51], v[200:203], v[120:123]\n" \
"ds_read_b64_tr_b16 v[14:15], v233 offset:10240\n" \
"v_accvgpr_read_b32 v234, a0\n" \
"v_mfma_f32_16x16x32_bf16 v[120:123], v[52:55], v[204:207], v[120:123]\n" \
"ds_read_b64_tr_b16 v[16:17], v232 offset:512\n" \
"v_accvgpr_read_b32 v235, a1\n" \
"v_mfma_f32_16x16x32_bf16 v[124:127], v[56:59], v[200:203], v[124:127]\n" \
"ds_read_b64_tr_b16 v[18:19], v232 offset:2560\n" \
"v_accvgpr_read_b32 v236, a2\n" \
"v_mfma_f32_16x16x32_bf16 v[124:127], v[60:63], v[204:207], v[124:127]\n" \
"global_load_lds_dwordx4 v227, s[48:49] offset:-1024\n" \
"v_accvgpr_read_b32 v237, a3\n" \
"v_mfma_f32_16x16x32_bf16 v[144:147], v[32:35], v[208:211], v[144:147]\n" \
"s_add_u32 s48, s48, 0x80\n" \
"s_addc_u32 s49, s49, 0\n" \
"ds_read_b128 v[200:203], v230 offset:2048\n" \
"v_mfma_f32_16x16x32_bf16 v[144:147], v[36:39], v[212:215], v[144:147]\n" \
"ds_read_b128 v[204:207], v231 offset:2048\n" \
"v_mul_f32 v234, s92, v234\n" \
"v_mfma_f32_16x16x32_bf16 v[148:151], v[40:43], v[208:211], v[148:151]\n" \
"ds_read_b64_tr_b16 v[20:21], v232 offset:8704\n" \
"v_mul_f32 v235, s92, v235\n" \
"v_mfma_f32_16x16x32_bf16 v[148:151], v[44:47], v[212:215], v[148:151]\n" \
"global_load_lds_dwordx4 v224, s[46:47] offset:0\n" \
"v_mul_f32 v236, s92, v236\n" \
"v_mfma_f32_16x16x32_bf16 v[152:155], v[48:51], v[208:211], v[152:155]\n" \
"ds_read_b64_tr_b16 v[22:23], v232 offset:10752\n" \
"v_mul_f32 v237, s92, v237\n" \
"v_mfma_f32_16x16x32_bf16 v[152:155], v[52:55], v[212:215], v[152:155]\n" \
"ds_read_b64_tr_b16 v[24:25], v233 offset:512\n" \
"v_cvt_pk_bf16_f32 v234, v234, v235\n" \
"v_mfma_f32_16x16x32_bf16 v[156:159], v[56:59], v[208:211], v[156:159]\n" \
"ds_read_b64_tr_b16 v[26:27], v233 offset:2560\n" \
"v_cvt_pk_bf16_f32 v236, v236, v237\n" \
"v_mfma_f32_16x16x32_bf16 v[156:159], v[60:63], v[212:215], v[156:159]\n" \
"global_load_lds_dwordx4 v225, s[46:47] offset:1024\n" \
"v_accvgpr_write_b32 a0, v234\n" \
"v_mfma_f32_16x16x32_bf16 v[176:179], v[32:35], v[216:219], v[176:179]\n" \
"ds_read_b128 v[208:211], v230 offset:4096\n" \
"v_accvgpr_write_b32 a1, v236\n" \
"v_mfma_f32_16x16x32_bf16 v[176:179], v[36:39], v[220:223], v[176:179]\n" \
"ds_read_b128 v[212:215], v231 offset:4096\n" \
"v_accvgpr_read_b32 v234, a4\n" \
"v_mfma_f32_16x16x32_bf16 v[180:183], v[40:43], v[216:219], v[180:183]\n" \
"ds_read_b64_tr_b16 v[28:29], v233 offset:8704\n" \
"v_accvgpr_read_b32 v235, a5\n" \
"v_mfma_f32_16x16x32_bf16 v[180:183], v[44:47], v[220:223], v[180:183]\n" \
"global_load_lds_dwordx4 v226, s[46:47] offset:2048\n" \
"v_accvgpr_read_b32 v236, a6\n" \
"v_mfma_f32_16x16x32_bf16 v[184:187], v[48:51], v[216:219], v[184:187]\n" \
"ds_read_b64_tr_b16 v[30:31], v233 offset:10752\n" \
"v_accvgpr_read_b32 v237, a7\n" \
"v_mfma_f32_16x16x32_bf16 v[184:187], v[52:55], v[220:223], v[184:187]\n" \
"v_mul_f32 v234, s92, v234\n" \
"v_mul_f32 v235, s92, v235\n" \
"v_mfma_f32_16x16x32_bf16 v[188:191], v[56:59], v[216:219], v[188:191]\n" \
"v_mul_f32 v236, s92, v236\n" \
"v_mul_f32 v237, s92, v237\n" \
"v_mfma_f32_16x16x32_bf16 v[188:191], v[60:63], v[220:223], v[188:191]\n" \
"s_waitcnt lgkmcnt(1)\n" \
"global_load_lds_dwordx4 v227, s[46:47] offset:3072\n" \
"v_cvt_pk_bf16_f32 v234, v234, v235\n" \
"v_mfma_f32_16x16x32_bf16 v[64:67], v[0:3], v[192:195], v[64:67]\n" \
"s_add_u32 s46, s46, 0x80\n" \
"s_addc_u32 s47, s47, 0\n" \
"ds_read_b128 v[216:219], v230 offset:6144\n" \
"v_mfma_f32_16x16x32_bf16 v[64:67], v[4:7], v[196:199], v[64:67]\n" \
"ds_read_b128 v[220:223], v231 offset:6144\n" \
"v_cvt_pk_bf16_f32 v236, v236, v237\n" \
"v_mfma_f32_16x16x32_bf16 v[68:71], v[8:11], v[192:195], v[68:71]\n" \
"ds_read_b64_tr_b16 v[32:33], v232 offset:1024\n" \
"v_accvgpr_write_b32 a2, v234\n" \
"v_mfma_f32_16x16x32_bf16 v[68:71], v[12:15], v[196:199], v[68:71]\n" \
"buffer_store_dwordx4 a[160:163], v249, s[56:59], s90 offen offset:256 sc1\n" \
"v_accvgpr_write_b32 a3, v236\n" \
"v_accvgpr_read_b32 v234, a32\n" \
"v_accvgpr_read_b32 v235, a33\n" \
"v_accvgpr_read_b32 v236, a34\n" \
"v_mfma_f32_16x16x32_bf16 v[72:75], v[16:19], v[192:195], v[72:75]\n" \
"ds_read_b64_tr_b16 v[34:35], v232 offset:3072\n" \
"v_accvgpr_read_b32 v237, a35\n" \
"v_mfma_f32_16x16x32_bf16 v[72:75], v[20:23], v[196:199], v[72:75]\n" \
"s_waitcnt lgkmcnt(3)\n" \
"ds_read_b64_tr_b16 v[36:37], v232 offset:9216\n" \
"v_mul_f32 v234, s92, v234\n" \
"v_mfma_f32_16x16x32_bf16 v[76:79], v[24:27], v[192:195], v[76:79]\n" \
"ds_read_b64_tr_b16 v[38:39], v232 offset:11264\n" \
"v_mul_f32 v235, s92, v235\n" \
"v_mfma_f32_16x16x32_bf16 v[76:79], v[28:31], v[196:199], v[76:79]\n" \
"v_mul_f32 v236, s92, v236\n" \
"v_mul_f32 v237, s92, v237\n" \
"v_mfma_f32_16x16x32_bf16 v[96:99], v[0:3], v[200:203], v[96:99]\n" \
"ds_read_b64_tr_b16 v[40:41], v233 offset:1024\n" \
"v_cvt_pk_bf16_f32 v234, v234, v235\n" \
"v_mfma_f32_16x16x32_bf16 v[96:99], v[4:7], v[204:207], v[96:99]\n" \
"ds_read_b64_tr_b16 v[42:43], v233 offset:3072\n" \
"v_cvt_pk_bf16_f32 v236, v236, v237\n" \
"v_mfma_f32_16x16x32_bf16 v[100:103], v[8:11], v[200:203], v[100:103]\n" \
"ds_read_b64_tr_b16 v[44:45], v233 offset:9216\n" \
"v_accvgpr_write_b32 a32, v234\n" \
"v_mfma_f32_16x16x32_bf16 v[100:103], v[12:15], v[204:207], v[100:103]\n" \
"v_accvgpr_write_b32 a33, v236\n" \
"v_accvgpr_read_b32 v234, a36\n" \
"v_mfma_f32_16x16x32_bf16 v[104:107], v[16:19], v[200:203], v[104:107]\n" \
"ds_read_b64_tr_b16 v[46:47], v233 offset:11264\n" \
"v_accvgpr_read_b32 v235, a37\n" \
"v_mfma_f32_16x16x32_bf16 v[104:107], v[20:23], v[204:207], v[104:107]\n" \
"ds_read_b64_tr_b16 v[48:49], v232 offset:1536\n" \
"v_accvgpr_read_b32 v236, a38\n" \
"v_mfma_f32_16x16x32_bf16 v[108:111], v[24:27], v[200:203], v[108:111]\n" \
"s_add_u32 m0, s95, 20480\n" \
"ds_read_b64_tr_b16 v[50:51], v232 offset:3584\n" \
"v_accvgpr_read_b32 v237, a39\n" \
"v_mfma_f32_16x16x32_bf16 v[108:111], v[28:31], v[204:207], v[108:111]\n" \
"global_load_lds_dwordx4 v228, s[44:45] offset:-4096\n" \
"v_mul_f32 v234, s92, v234\n" \
"v_mfma_f32_16x16x32_bf16 v[128:131], v[0:3], v[208:211], v[128:131]\n" \
"ds_read_b64_tr_b16 v[52:53], v232 offset:9728\n" \
"v_mul_f32 v235, s92, v235\n" \
"v_mfma_f32_16x16x32_bf16 v[128:131], v[4:7], v[212:215], v[128:131]\n" \
"ds_read_b64_tr_b16 v[54:55], v232 offset:11776\n" \
"v_mul_f32 v236, s92, v236\n" \
"v_mfma_f32_16x16x32_bf16 v[132:135], v[8:11], v[208:211], v[132:135]\n" \
"ds_read_b64_tr_b16 v[56:57], v233 offset:1536\n" \
"v_mul_f32 v237, s92, v237\n" \
"v_mfma_f32_16x16x32_bf16 v[132:135], v[12:15], v[212:215], v[132:135]\n" \
"global_load_lds_dwordx4 v229, s[44:45] offset:-3072\n" \
"v_cvt_pk_bf16_f32 v234, v234, v235\n" \
"v_mfma_f32_16x16x32_bf16 v[136:139], v[16:19], v[208:211], v[136:139]\n" \
"s_add_u32 s44, s44, 0x80000\n" \
"s_addc_u32 s45, s45, 0\n" \
"ds_read_b64_tr_b16 v[58:59], v233 offset:3584\n" \
"v_mfma_f32_16x16x32_bf16 v[136:139], v[20:23], v[212:215], v[136:139]\n" \
"ds_read_b64_tr_b16 v[60:61], v233 offset:9728\n" \
"v_cvt_pk_bf16_f32 v236, v236, v237\n" \
"v_mfma_f32_16x16x32_bf16 v[140:143], v[24:27], v[208:211], v[140:143]\n" \
"ds_read_b64_tr_b16 v[62:63], v233 offset:11776\n" \
"v_accvgpr_write_b32 a34, v234\n" \
"v_mfma_f32_16x16x32_bf16 v[140:143], v[28:31], v[212:215], v[140:143]\n" \
"s_waitcnt lgkmcnt(3)\n" \
"global_load_lds_dwordx4 v228, s[42:43] offset:-2048\n" \
"v_accvgpr_write_b32 a35, v236\n" \
"v_mfma_f32_16x16x32_bf16 v[160:163], v[0:3], v[216:219], v[160:163]\n" \
"v_add_u32 v230, 0xffff0000, v230\n" \
"v_add_u32 v231, 0xffff0000, v231\n" \
"v_mfma_f32_16x16x32_bf16 v[160:163], v[4:7], v[220:223], v[160:163]\n" \
"v_add_u32 v232, 0x4000, v232\n" \
"v_add_u32 v233, 0x4000, v233\n" \
"v_mfma_f32_16x16x32_bf16 v[164:167], v[8:11], v[216:219], v[164:167]\n" \
"v_accvgpr_read_b32 v234, a8\n" \
"v_accvgpr_read_b32 v235, a9\n" \
"v_mfma_f32_16x16x32_bf16 v[164:167], v[12:15], v[220:223], v[164:167]\n" \
"global_load_lds_dwordx4 v229, s[42:43] offset:-1024\n" \
"v_accvgpr_read_b32 v236, a10\n" \
"v_mfma_f32_16x16x32_bf16 v[168:171], v[16:19], v[216:219], v[168:171]\n" \
"s_add_u32 s42, s42, 0x80000\n" \
"s_addc_u32 s43, s43, 0\n" \
"v_accvgpr_read_b32 v237, a11\n" \
"v_mfma_f32_16x16x32_bf16 v[168:171], v[20:23], v[220:223], v[168:171]\n" \
"v_mul_f32 v234, s92, v234\n" \
"v_mul_f32 v235, s92, v235\n" \
"v_mfma_f32_16x16x32_bf16 v[172:175], v[24:27], v[216:219], v[172:175]\n" \
"s_waitcnt vmcnt(19) lgkmcnt(0)\n" \
"v_mul_f32 v236, s92, v236\n" \
"v_mul_f32 v237, s92, v237\n" \
"v_mfma_f32_16x16x32_bf16 v[172:175], v[28:31], v[220:223], v[172:175]\n" \
"s_barrier\n" \
"v_mfma_f32_16x16x32_bf16 v[80:83], v[32:35], v[192:195], v[80:83]\n" \
"ds_read_b64_tr_b16 v[0:1], v232 offset:0\n" \
"v_cvt_pk_bf16_f32 v234, v234, v235\n" \
"v_mfma_f32_16x16x32_bf16 v[80:83], v[36:39], v[196:199], v[80:83]\n" \
"ds_read_b64_tr_b16 v[2:3], v232 offset:2048\n" \
"v_cvt_pk_bf16_f32 v236, v236, v237\n" \
"v_mfma_f32_16x16x32_bf16 v[84:87], v[40:43], v[192:195], v[84:87]\n" \
"s_add_u32 m0, s87, 135168\n" \
"ds_read_b64_tr_b16 v[4:5], v232 offset:8192\n" \
"v_accvgpr_write_b32 a8, v234\n" \
"v_mfma_f32_16x16x32_bf16 v[84:87], v[44:47], v[196:199], v[84:87]\n" \
"global_load_lds_dwordx4 v224, s[48:49] offset:-4096\n" \
"v_accvgpr_write_b32 a9, v236\n" \
"v_mfma_f32_16x16x32_bf16 v[88:91], v[48:51], v[192:195], v[88:91]\n" \
"ds_read_b64_tr_b16 v[6:7], v232 offset:10240\n" \
"v_accvgpr_read_b32 v234, a12\n" \
"v_mfma_f32_16x16x32_bf16 v[88:91], v[52:55], v[196:199], v[88:91]\n" \
"ds_read_b64_tr_b16 v[8:9], v233 offset:0\n" \
"v_accvgpr_read_b32 v235, a13\n" \
"v_mfma_f32_16x16x32_bf16 v[92:95], v[56:59], v[192:195], v[92:95]\n" \
"ds_read_b64_tr_b16 v[10:11], v233 offset:2048\n" \
"v_accvgpr_read_b32 v236, a14\n" \
"v_mfma_f32_16x16x32_bf16 v[92:95], v[60:63], v[196:199], v[92:95]\n" \
"global_load_lds_dwordx4 v225, s[48:49] offset:-3072\n" \
"v_accvgpr_read_b32 v237, a15\n" \
"v_mfma_f32_16x16x32_bf16 v[112:115], v[32:35], v[200:203], v[112:115]\n" \
"ds_read_b128 v[192:195], v230 offset:0\n" \
"v_mul_f32 v234, s92, v234\n" \
"v_mfma_f32_16x16x32_bf16 v[112:115], v[36:39], v[204:207], v[112:115]\n" \
"ds_read_b128 v[196:199], v231 offset:0\n" \
"v_mul_f32 v235, s92, v235\n" \
"v_mfma_f32_16x16x32_bf16 v[116:119], v[40:43], v[200:203], v[116:119]\n" \
"ds_read_b64_tr_b16 v[12:13], v233 offset:8192\n" \
"v_mul_f32 v236, s92, v236\n" \
"v_mfma_f32_16x16x32_bf16 v[116:119], v[44:47], v[204:207], v[116:119]\n" \
"global_load_lds_dwordx4 v226, s[48:49] offset:-2048\n" \
"v_mul_f32 v237, s92, v237\n" \
"v_mfma_f32_16x16x32_bf16 v[120:123], v[48:51], v[200:203], v[120:123]\n" \
"ds_read_b64_tr_b16 v[14:15], v233 offset:10240\n" \
"v_cvt_pk_bf16_f32 v234, v234, v235\n" \
"v_mfma_f32_16x16x32_bf16 v[120:123], v[52:55], v[204:207], v[120:123]\n" \
"ds_read_b64_tr_b16 v[16:17], v232 offset:512\n" \
"v_cvt_pk_bf16_f32 v236, v236, v237\n" \
"v_mfma_f32_16x16x32_bf16 v[124:127], v[56:59], v[200:203], v[124:127]\n" \
"ds_read_b64_tr_b16 v[18:19], v232 offset:2560\n" \
"v_accvgpr_write_b32 a10, v234\n" \
"v_mfma_f32_16x16x32_bf16 v[124:127], v[60:63], v[204:207], v[124:127]\n" \
"global_load_lds_dwordx4 v227, s[48:49] offset:-1024\n" \
"v_accvgpr_write_b32 a11, v236\n" \
"v_mfma_f32_16x16x32_bf16 v[144:147], v[32:35], v[208:211], v[144:147]\n" \
"s_add_u32 s48, s48, 0x80\n" \
"s_addc_u32 s49, s49, 0\n" \
"ds_read_b128 v[200:203], v230 offset:2048\n" \
"v_mfma_f32_16x16x32_bf16 v[144:147], v[36:39], v[212:215], v[144:147]\n" \
"ds_read_b128 v[204:207], v231 offset:2048\n" \
"v_accvgpr_read_b32 v234, a40\n" \
"v_mfma_f32_16x16x32_bf16 v[148:151], v[40:43], v[208:211], v[148:151]\n" \
"ds_read_b64_tr_b16 v[20:21], v232 offset:8704\n" \
"v_accvgpr_read_b32 v235, a41\n" \
"v_mfma_f32_16x16x32_bf16 v[148:151], v[44:47], v[212:215], v[148:151]\n" \
"global_load_lds_dwordx4 v224, s[46:47] offset:0\n" \
"v_accvgpr_read_b32 v236, a42\n" \
"v_mfma_f32_16x16x32_bf16 v[152:155], v[48:51], v[208:211], v[152:155]\n" \
"ds_read_b64_tr_b16 v[22:23], v232 offset:10752\n" \
"v_accvgpr_read_b32 v237, a43\n" \
"v_mfma_f32_16x16x32_bf16 v[152:155], v[52:55], v[212:215], v[152:155]\n" \
"ds_read_b64_tr_b16 v[24:25], v233 offset:512\n" \
"v_mul_f32 v234, s92, v234\n" \
"v_mfma_f32_16x16x32_bf16 v[156:159], v[56:59], v[208:211], v[156:159]\n" \
"ds_read_b64_tr_b16 v[26:27], v233 offset:2560\n" \
"v_mul_f32 v235, s92, v235\n" \
"v_mfma_f32_16x16x32_bf16 v[156:159], v[60:63], v[212:215], v[156:159]\n" \
"global_load_lds_dwordx4 v225, s[46:47] offset:1024\n" \
"v_mul_f32 v236, s92, v236\n" \
"v_mfma_f32_16x16x32_bf16 v[176:179], v[32:35], v[216:219], v[176:179]\n" \
"ds_read_b128 v[208:211], v230 offset:4096\n" \
"v_mul_f32 v237, s92, v237\n" \
"v_mfma_f32_16x16x32_bf16 v[176:179], v[36:39], v[220:223], v[176:179]\n" \
"ds_read_b128 v[212:215], v231 offset:4096\n" \
"v_cvt_pk_bf16_f32 v234, v234, v235\n" \
"v_mfma_f32_16x16x32_bf16 v[180:183], v[40:43], v[216:219], v[180:183]\n" \
"ds_read_b64_tr_b16 v[28:29], v233 offset:8704\n" \
"v_cvt_pk_bf16_f32 v236, v236, v237\n" \
"v_mfma_f32_16x16x32_bf16 v[180:183], v[44:47], v[220:223], v[180:183]\n" \
"global_load_lds_dwordx4 v226, s[46:47] offset:2048\n" \
"v_accvgpr_write_b32 a40, v234\n" \
"v_mfma_f32_16x16x32_bf16 v[184:187], v[48:51], v[216:219], v[184:187]\n" \
"ds_read_b64_tr_b16 v[30:31], v233 offset:10752\n" \
"v_accvgpr_write_b32 a41, v236\n" \
"v_mfma_f32_16x16x32_bf16 v[184:187], v[52:55], v[220:223], v[184:187]\n" \
"v_accvgpr_read_b32 v234, a44\n" \
"v_accvgpr_read_b32 v235, a45\n" \
"v_mfma_f32_16x16x32_bf16 v[188:191], v[56:59], v[216:219], v[188:191]\n" \
"v_accvgpr_read_b32 v236, a46\n" \
"v_accvgpr_read_b32 v237, a47\n" \
"v_mfma_f32_16x16x32_bf16 v[188:191], v[60:63], v[220:223], v[188:191]\n" \
"s_waitcnt lgkmcnt(1)\n" \
"global_load_lds_dwordx4 v227, s[46:47] offset:3072\n" \
"v_mul_f32 v234, s92, v234\n" \
"v_mfma_f32_16x16x32_bf16 v[64:67], v[0:3], v[192:195], v[64:67]\n" \
"s_add_u32 s46, s46, 0x80\n" \
"s_addc_u32 s47, s47, 0\n" \
"ds_read_b128 v[216:219], v230 offset:6144\n" \
"v_mfma_f32_16x16x32_bf16 v[64:67], v[4:7], v[196:199], v[64:67]\n" \
"ds_read_b128 v[220:223], v231 offset:6144\n" \
"v_mul_f32 v235, s92, v235\n" \
"v_mfma_f32_16x16x32_bf16 v[68:71], v[8:11], v[192:195], v[68:71]\n" \
"ds_read_b64_tr_b16 v[32:33], v232 offset:1024\n" \
"v_mul_f32 v236, s92, v236\n" \
"v_mfma_f32_16x16x32_bf16 v[68:71], v[12:15], v[196:199], v[68:71]\n" \
"buffer_store_dwordx4 a[136:139], v249, s[56:59], s90 offen offset:2048 sc1\n" \
"v_mul_f32 v237, s92, v237\n" \
"v_cvt_pk_bf16_f32 v234, v234, v235\n" \
"v_cvt_pk_bf16_f32 v236, v236, v237\n" \
"v_accvgpr_write_b32 a42, v234\n" \
"v_mfma_f32_16x16x32_bf16 v[72:75], v[16:19], v[192:195], v[72:75]\n" \
"ds_read_b64_tr_b16 v[34:35], v232 offset:3072\n" \
"v_accvgpr_write_b32 a43, v236\n" \
"v_mfma_f32_16x16x32_bf16 v[72:75], v[20:23], v[196:199], v[72:75]\n" \
"s_waitcnt lgkmcnt(3)\n" \
"ds_read_b64_tr_b16 v[36:37], v232 offset:9216\n" \
"v_accvgpr_read_b32 v234, a16\n" \
"v_mfma_f32_16x16x32_bf16 v[76:79], v[24:27], v[192:195], v[76:79]\n" \
"ds_read_b64_tr_b16 v[38:39], v232 offset:11264\n" \
"v_accvgpr_read_b32 v235, a17\n" \
"v_mfma_f32_16x16x32_bf16 v[76:79], v[28:31], v[196:199], v[76:79]\n" \
"v_accvgpr_read_b32 v236, a18\n" \
"v_accvgpr_read_b32 v237, a19\n" \
"v_mfma_f32_16x16x32_bf16 v[96:99], v[0:3], v[200:203], v[96:99]\n" \
"ds_read_b64_tr_b16 v[40:41], v233 offset:1024\n" \
"v_cvt_pk_bf16_f32 v234, v234, v235\n" \
"v_mfma_f32_16x16x32_bf16 v[96:99], v[4:7], v[204:207], v[96:99]\n" \
"ds_read_b64_tr_b16 v[42:43], v233 offset:3072\n" \
"v_cvt_pk_bf16_f32 v236, v236, v237\n" \
"v_mfma_f32_16x16x32_bf16 v[100:103], v[8:11], v[200:203], v[100:103]\n" \
"ds_read_b64_tr_b16 v[44:45], v233 offset:9216\n" \
"v_accvgpr_write_b32 a16, v234\n" \
"v_mfma_f32_16x16x32_bf16 v[100:103], v[12:15], v[204:207], v[100:103]\n" \
"v_accvgpr_write_b32 a17, v236\n" \
"v_accvgpr_read_b32 v234, a48\n" \
"v_mfma_f32_16x16x32_bf16 v[104:107], v[16:19], v[200:203], v[104:107]\n" \
"ds_read_b64_tr_b16 v[46:47], v233 offset:11264\n" \
"v_accvgpr_read_b32 v235, a49\n" \
"v_mfma_f32_16x16x32_bf16 v[104:107], v[20:23], v[204:207], v[104:107]\n" \
"ds_read_b64_tr_b16 v[48:49], v232 offset:1536\n" \
"v_accvgpr_read_b32 v236, a50\n" \
"v_mfma_f32_16x16x32_bf16 v[108:111], v[24:27], v[200:203], v[108:111]\n" \
"s_add_u32 m0, s95, 36864\n" \
"ds_read_b64_tr_b16 v[50:51], v232 offset:3584\n" \
"v_accvgpr_read_b32 v237, a51\n" \
"v_mfma_f32_16x16x32_bf16 v[108:111], v[28:31], v[204:207], v[108:111]\n" \
"global_load_lds_dwordx4 v228, s[44:45] offset:-4096\n" \
"v_cvt_pk_bf16_f32 v234, v234, v235\n" \
"v_mfma_f32_16x16x32_bf16 v[128:131], v[0:3], v[208:211], v[128:131]\n" \
"ds_read_b64_tr_b16 v[52:53], v232 offset:9728\n" \
"v_cvt_pk_bf16_f32 v236, v236, v237\n" \
"v_mfma_f32_16x16x32_bf16 v[128:131], v[4:7], v[212:215], v[128:131]\n" \
"ds_read_b64_tr_b16 v[54:55], v232 offset:11776\n" \
"v_accvgpr_write_b32 a18, v234\n" \
"v_mfma_f32_16x16x32_bf16 v[132:135], v[8:11], v[208:211], v[132:135]\n" \
"ds_read_b64_tr_b16 v[56:57], v233 offset:1536\n" \
"v_accvgpr_write_b32 a19, v236\n" \
"v_mfma_f32_16x16x32_bf16 v[132:135], v[12:15], v[212:215], v[132:135]\n" \
"global_load_lds_dwordx4 v229, s[44:45] offset:-3072\n" \
"v_accvgpr_read_b32 v234, a20\n" \
"v_mfma_f32_16x16x32_bf16 v[136:139], v[16:19], v[208:211], v[136:139]\n" \
"s_add_u32 s44, s44, 0x80000\n" \
"s_addc_u32 s45, s45, 0\n" \
"ds_read_b64_tr_b16 v[58:59], v233 offset:3584\n" \
"v_mfma_f32_16x16x32_bf16 v[136:139], v[20:23], v[212:215], v[136:139]\n" \
"ds_read_b64_tr_b16 v[60:61], v233 offset:9728\n" \
"v_accvgpr_read_b32 v235, a21\n" \
"v_mfma_f32_16x16x32_bf16 v[140:143], v[24:27], v[208:211], v[140:143]\n" \
"ds_read_b64_tr_b16 v[62:63], v233 offset:11776\n" \
"v_accvgpr_read_b32 v236, a22\n" \
"v_mfma_f32_16x16x32_bf16 v[140:143], v[28:31], v[212:215], v[140:143]\n" \
"s_waitcnt lgkmcnt(3)\n" \
"global_load_lds_dwordx4 v228, s[42:43] offset:-2048\n" \
"v_accvgpr_read_b32 v237, a23\n" \
"v_mfma_f32_16x16x32_bf16 v[160:163], v[0:3], v[216:219], v[160:163]\n" \
"v_add_u32 v230, 0x8000, v230\n" \
"v_add_u32 v231, 0x8000, v231\n" \
"v_mfma_f32_16x16x32_bf16 v[160:163], v[4:7], v[220:223], v[160:163]\n" \
"v_add_u32 v232, 0xffff4000, v232\n" \
"v_add_u32 v233, 0xffff4000, v233\n" \
"v_mfma_f32_16x16x32_bf16 v[164:167], v[8:11], v[216:219], v[164:167]\n" \
"v_cvt_pk_bf16_f32 v234, v234, v235\n" \
"v_cvt_pk_bf16_f32 v236, v236, v237\n" \
"v_mfma_f32_16x16x32_bf16 v[164:167], v[12:15], v[220:223], v[164:167]\n" \
"global_load_lds_dwordx4 v229, s[42:43] offset:-1024\n" \
"v_accvgpr_write_b32 a20, v234\n" \
"v_mfma_f32_16x16x32_bf16 v[168:171], v[16:19], v[216:219], v[168:171]\n" \
"s_add_u32 s42, s42, 0x80000\n" \
"s_addc_u32 s43, s43, 0\n" \
"v_accvgpr_write_b32 a21, v236\n" \
"v_mfma_f32_16x16x32_bf16 v[168:171], v[20:23], v[220:223], v[168:171]\n" \
"v_accvgpr_read_b32 v234, a52\n" \
"v_accvgpr_read_b32 v235, a53\n" \
"v_mfma_f32_16x16x32_bf16 v[172:175], v[24:27], v[216:219], v[172:175]\n" \
"s_waitcnt vmcnt(18) lgkmcnt(0)\n" \
"v_accvgpr_read_b32 v236, a54\n" \
"v_accvgpr_read_b32 v237, a55\n" \
"v_mfma_f32_16x16x32_bf16 v[172:175], v[28:31], v[220:223], v[172:175]\n" \
"s_barrier\n" \
"v_mfma_f32_16x16x32_bf16 v[80:83], v[32:35], v[192:195], v[80:83]\n" \
"ds_read_b64_tr_b16 v[0:1], v232 offset:0\n" \
"v_cvt_pk_bf16_f32 v234, v234, v235\n" \
"v_mfma_f32_16x16x32_bf16 v[80:83], v[36:39], v[196:199], v[80:83]\n" \
"ds_read_b64_tr_b16 v[2:3], v232 offset:2048\n" \
"v_cvt_pk_bf16_f32 v236, v236, v237\n" \
"v_mfma_f32_16x16x32_bf16 v[84:87], v[40:43], v[192:195], v[84:87]\n" \
"s_add_u32 m0, s87, 69632\n" \
"ds_read_b64_tr_b16 v[4:5], v232 offset:8192\n" \
"v_accvgpr_write_b32 a22, v234\n" \
"v_mfma_f32_16x16x32_bf16 v[84:87], v[44:47], v[196:199], v[84:87]\n" \
"global_load_lds_dwordx4 v224, s[48:49] offset:-4096\n" \
"v_accvgpr_write_b32 a23, v236\n" \
"v_mfma_f32_16x16x32_bf16 v[88:91], v[48:51], v[192:195], v[88:91]\n" \
"ds_read_b64_tr_b16 v[6:7], v232 offset:10240\n" \
"v_accvgpr_read_b32 v234, a24\n" \
"v_mfma_f32_16x16x32_bf16 v[88:91], v[52:55], v[196:199], v[88:91]\n" \
"ds_read_b64_tr_b16 v[8:9], v233 offset:0\n" \
"v_accvgpr_read_b32 v235, a25\n" \
"v_mfma_f32_16x16x32_bf16 v[92:95], v[56:59], v[192:195], v[92:95]\n" \
"ds_read_b64_tr_b16 v[10:11], v233 offset:2048\n" \
"v_accvgpr_read_b32 v236, a26\n" \
"v_mfma_f32_16x16x32_bf16 v[92:95], v[60:63], v[196:199], v[92:95]\n" \
"global_load_lds_dwordx4 v225, s[48:49] offset:-3072\n" \
"v_accvgpr_read_b32 v237, a27\n" \
"v_mfma_f32_16x16x32_bf16 v[112:115], v[32:35], v[200:203], v[112:115]\n" \
"ds_read_b128 v[192:195], v230 offset:0\n" \
"v_cvt_pk_bf16_f32 v234, v234, v235\n" \
"v_mfma_f32_16x16x32_bf16 v[112:115], v[36:39], v[204:207], v[112:115]\n" \
"ds_read_b128 v[196:199], v231 offset:0\n" \
"v_cvt_pk_bf16_f32 v236, v236, v237\n" \
"v_mfma_f32_16x16x32_bf16 v[116:119], v[40:43], v[200:203], v[116:119]\n" \
"ds_read_b64_tr_b16 v[12:13], v233 offset:8192\n" \
"v_accvgpr_write_b32 a24, v234\n" \
"v_mfma_f32_16x16x32_bf16 v[116:119], v[44:47], v[204:207], v[116:119]\n" \
"global_load_lds_dwordx4 v226, s[48:49] offset:-2048\n" \
"v_accvgpr_write_b32 a25, v236\n" \
"v_mfma_f32_16x16x32_bf16 v[120:123], v[48:51], v[200:203], v[120:123]\n" \
"ds_read_b64_tr_b16 v[14:15], v233 offset:10240\n" \
"v_accvgpr_read_b32 v234, a56\n" \
"v_mfma_f32_16x16x32_bf16 v[120:123], v[52:55], v[204:207], v[120:123]\n" \
"ds_read_b64_tr_b16 v[16:17], v232 offset:512\n" \
"v_accvgpr_read_b32 v235, a57\n" \
"v_mfma_f32_16x16x32_bf16 v[124:127], v[56:59], v[200:203], v[124:127]\n" \
"ds_read_b64_tr_b16 v[18:19], v232 offset:2560\n" \
"v_accvgpr_read_b32 v236, a58\n" \
"v_mfma_f32_16x16x32_bf16 v[124:127], v[60:63], v[204:207], v[124:127]\n" \
"global_load_lds_dwordx4 v227, s[48:49] offset:-1024\n" \
"v_accvgpr_read_b32 v237, a59\n" \
"v_mfma_f32_16x16x32_bf16 v[144:147], v[32:35], v[208:211], v[144:147]\n" \
"s_add_u32 s48, s48, 0x80\n" \
"s_addc_u32 s49, s49, 0\n" \
"ds_read_b128 v[200:203], v230 offset:2048\n" \
"v_mfma_f32_16x16x32_bf16 v[144:147], v[36:39], v[212:215], v[144:147]\n" \
"ds_read_b128 v[204:207], v231 offset:2048\n" \
"v_cvt_pk_bf16_f32 v234, v234, v235\n" \
"v_mfma_f32_16x16x32_bf16 v[148:151], v[40:43], v[208:211], v[148:151]\n" \
"ds_read_b64_tr_b16 v[20:21], v232 offset:8704\n" \
"v_cvt_pk_bf16_f32 v236, v236, v237\n" \
"v_mfma_f32_16x16x32_bf16 v[148:151], v[44:47], v[212:215], v[148:151]\n" \
"global_load_lds_dwordx4 v224, s[46:47] offset:0\n" \
"v_accvgpr_write_b32 a26, v234\n" \
"v_mfma_f32_16x16x32_bf16 v[152:155], v[48:51], v[208:211], v[152:155]\n" \
"ds_read_b64_tr_b16 v[22:23], v232 offset:10752\n" \
"v_accvgpr_write_b32 a27, v236\n" \
"v_mfma_f32_16x16x32_bf16 v[152:155], v[52:55], v[212:215], v[152:155]\n" \
"ds_read_b64_tr_b16 v[24:25], v233 offset:512\n" \
"v_accvgpr_read_b32 v234, a28\n" \
"v_mfma_f32_16x16x32_bf16 v[156:159], v[56:59], v[208:211], v[156:159]\n" \
"ds_read_b64_tr_b16 v[26:27], v233 offset:2560\n" \
"v_accvgpr_read_b32 v235, a29\n" \
"v_mfma_f32_16x16x32_bf16 v[156:159], v[60:63], v[212:215], v[156:159]\n" \
"global_load_lds_dwordx4 v225, s[46:47] offset:1024\n" \
"v_accvgpr_read_b32 v236, a30\n" \
"v_mfma_f32_16x16x32_bf16 v[176:179], v[32:35], v[216:219], v[176:179]\n" \
"ds_read_b128 v[208:211], v230 offset:4096\n" \
"v_accvgpr_read_b32 v237, a31\n" \
"v_mfma_f32_16x16x32_bf16 v[176:179], v[36:39], v[220:223], v[176:179]\n" \
"ds_read_b128 v[212:215], v231 offset:4096\n" \
"v_cvt_pk_bf16_f32 v234, v234, v235\n" \
"v_mfma_f32_16x16x32_bf16 v[180:183], v[40:43], v[216:219], v[180:183]\n" \
"ds_read_b64_tr_b16 v[28:29], v233 offset:8704\n" \
"v_cvt_pk_bf16_f32 v236, v236, v237\n" \
"v_mfma_f32_16x16x32_bf16 v[180:183], v[44:47], v[220:223], v[180:183]\n" \
"global_load_lds_dwordx4 v226, s[46:47] offset:2048\n" \
"v_accvgpr_write_b32 a28, v234\n" \
"v_mfma_f32_16x16x32_bf16 v[184:187], v[48:51], v[216:219], v[184:187]\n" \
"ds_read_b64_tr_b16 v[30:31], v233 offset:10752\n" \
"v_accvgpr_write_b32 a29, v236\n" \
"v_mfma_f32_16x16x32_bf16 v[184:187], v[52:55], v[220:223], v[184:187]\n" \
"v_accvgpr_read_b32 v234, a60\n" \
"v_accvgpr_read_b32 v235, a61\n" \
"v_mfma_f32_16x16x32_bf16 v[188:191], v[56:59], v[216:219], v[188:191]\n" \
"v_accvgpr_read_b32 v236, a62\n" \
"v_accvgpr_read_b32 v237, a63\n" \
"v_mfma_f32_16x16x32_bf16 v[188:191], v[60:63], v[220:223], v[188:191]\n" \
"s_waitcnt lgkmcnt(1)\n" \
"global_load_lds_dwordx4 v227, s[46:47] offset:3072\n" \
"v_cvt_pk_bf16_f32 v234, v234, v235\n" \
"v_mfma_f32_16x16x32_bf16 v[64:67], v[0:3], v[192:195], v[64:67]\n" \
"s_add_u32 s46, s46, 0x80\n" \
"s_addc_u32 s47, s47, 0\n" \
"ds_read_b128 v[216:219], v230 offset:6144\n" \
"v_mfma_f32_16x16x32_bf16 v[64:67], v[4:7], v[196:199], v[64:67]\n" \
"ds_read_b128 v[220:223], v231 offset:6144\n" \
"v_cvt_pk_bf16_f32 v236, v236, v237\n" \
"v_mfma_f32_16x16x32_bf16 v[68:71], v[8:11], v[192:195], v[68:71]\n" \
"ds_read_b64_tr_b16 v[32:33], v232 offset:1024\n" \
"v_accvgpr_write_b32 a30, v234\n" \
"v_mfma_f32_16x16x32_bf16 v[68:71], v[12:15], v[196:199], v[68:71]\n" \
"buffer_store_dwordx4 a[168:171], v249, s[56:59], s90 offen offset:2304 sc1\n" \
"v_accvgpr_write_b32 a31, v236\n" \
"v_mfma_f32_16x16x32_bf16 v[72:75], v[16:19], v[192:195], v[72:75]\n" \
"s_add_u32 s90, s89, 139264\n" \
"ds_read_b64_tr_b16 v[34:35], v232 offset:3072\n" \
"v_mfma_f32_16x16x32_bf16 v[72:75], v[20:23], v[196:199], v[72:75]\n" \
"s_waitcnt lgkmcnt(3)\n" \
"ds_read_b64_tr_b16 v[36:37], v232 offset:9216\n" \
"v_mfma_f32_16x16x32_bf16 v[76:79], v[24:27], v[192:195], v[76:79]\n" \
"ds_read_b64_tr_b16 v[38:39], v232 offset:11264\n" \
"v_mfma_f32_16x16x32_bf16 v[76:79], v[28:31], v[196:199], v[76:79]\n" \
"buffer_store_dwordx4 a[144:147], v248, s[56:59], s90 offen offset:0 sc1\n" \
"v_mfma_f32_16x16x32_bf16 v[96:99], v[0:3], v[200:203], v[96:99]\n" \
"s_lshl_b32 s70, s63, 2\n" \
"ds_read_b64_tr_b16 v[40:41], v233 offset:1024\n" \
"v_mfma_f32_16x16x32_bf16 v[96:99], v[4:7], v[204:207], v[96:99]\n" \
"s_add_u32 s70, s70, s91\n" \
"ds_read_b64_tr_b16 v[42:43], v233 offset:3072\n" \
"v_mov_b32 v238, s60\n" \
"v_mfma_f32_16x16x32_bf16 v[100:103], v[8:11], v[200:203], v[100:103]\n" \
"ds_read_b64_tr_b16 v[44:45], v233 offset:9216\n" \
"v_lshlrev_b32 v239, 28, s61\n" \
"v_mfma_f32_16x16x32_bf16 v[100:103], v[12:15], v[204:207], v[100:103]\n" \
"buffer_store_dword v238, v239, s[56:59], s70 offen sc1\n" \
"v_mfma_f32_16x16x32_bf16 v[104:107], v[16:19], v[200:203], v[104:107]\n" \
"ds_read_b64_tr_b16 v[46:47], v233 offset:11264\n" \
"v_mfma_f32_16x16x32_bf16 v[104:107], v[20:23], v[204:207], v[104:107]\n" \
"ds_read_b64_tr_b16 v[48:49], v232 offset:1536\n" \
"v_mfma_f32_16x16x32_bf16 v[108:111], v[24:27], v[200:203], v[108:111]\n" \
"s_add_u32 m0, s95, 53248\n" \
"ds_read_b64_tr_b16 v[50:51], v232 offset:3584\n" \
"v_mfma_f32_16x16x32_bf16 v[108:111], v[28:31], v[204:207], v[108:111]\n" \
"global_load_lds_dwordx4 v228, s[44:45] offset:-4096\n" \
"Lpg3850_%=:\n" \
"s_cbranch_execz Lpg5547_%=\n" \
"v_mfma_f32_16x16x32_bf16 v[128:131], v[0:3], v[208:211], v[128:131]\n" \
"ds_read_b64_tr_b16 v[52:53], v232 offset:9728\n" \
"v_mfma_f32_16x16x32_bf16 v[128:131], v[4:7], v[212:215], v[128:131]\n" \
"ds_read_b64_tr_b16 v[54:55], v232 offset:11776\n" \
"v_mfma_f32_16x16x32_bf16 v[132:135], v[8:11], v[208:211], v[132:135]\n" \
"ds_read_b64_tr_b16 v[56:57], v233 offset:1536\n" \
"v_mfma_f32_16x16x32_bf16 v[132:135], v[12:15], v[212:215], v[132:135]\n" \
"global_load_lds_dwordx4 v229, s[44:45] offset:-3072\n" \
"v_mfma_f32_16x16x32_bf16 v[136:139], v[16:19], v[208:211], v[136:139]\n" \
"s_add_u32 s44, s44, 0x100000\n" \
"s_addc_u32 s45, s45, 0\n" \
"ds_read_b64_tr_b16 v[58:59], v233 offset:3584\n" \
"v_mfma_f32_16x16x32_bf16 v[136:139], v[20:23], v[212:215], v[136:139]\n" \
"ds_read_b64_tr_b16 v[60:61], v233 offset:9728\n" \
"v_mfma_f32_16x16x32_bf16 v[140:143], v[24:27], v[208:211], v[140:143]\n" \
"ds_read_b64_tr_b16 v[62:63], v233 offset:11776\n" \
"v_mfma_f32_16x16x32_bf16 v[140:143], v[28:31], v[212:215], v[140:143]\n" \
"s_waitcnt lgkmcnt(3)\n" \
"global_load_lds_dwordx4 v228, s[42:43] offset:-2048\n" \
"v_mfma_f32_16x16x32_bf16 v[160:163], v[0:3], v[216:219], v[160:163]\n" \
"v_add_u32 v230, 0x8000, v230\n" \
"v_add_u32 v231, 0x8000, v231\n" \
"v_mfma_f32_16x16x32_bf16 v[160:163], v[4:7], v[220:223], v[160:163]\n" \
"v_add_u32 v232, 0x4000, v232\n" \
"v_add_u32 v233, 0x4000, v233\n" \
"v_mfma_f32_16x16x32_bf16 v[164:167], v[8:11], v[216:219], v[164:167]\n" \
"v_mfma_f32_16x16x32_bf16 v[164:167], v[12:15], v[220:223], v[164:167]\n" \
"global_load_lds_dwordx4 v229, s[42:43] offset:-1024\n" \
"v_mfma_f32_16x16x32_bf16 v[168:171], v[16:19], v[216:219], v[168:171]\n" \
"s_add_u32 s42, s42, 0x100000\n" \
"s_addc_u32 s43, s43, 0\n" \
"v_mfma_f32_16x16x32_bf16 v[168:171], v[20:23], v[220:223], v[168:171]\n" \
"v_mfma_f32_16x16x32_bf16 v[172:175], v[24:27], v[216:219], v[172:175]\n" \
"s_waitcnt vmcnt(20) lgkmcnt(0)\n" \
"v_mfma_f32_16x16x32_bf16 v[172:175], v[28:31], v[220:223], v[172:175]\n" \
"s_barrier\n" \
"v_mfma_f32_16x16x32_bf16 v[80:83], v[32:35], v[192:195], v[80:83]\n" \
"ds_read_b64_tr_b16 v[0:1], v232 offset:0\n" \
"v_mfma_f32_16x16x32_bf16 v[80:83], v[36:39], v[196:199], v[80:83]\n" \
"ds_read_b64_tr_b16 v[2:3], v232 offset:2048\n" \
"v_mfma_f32_16x16x32_bf16 v[84:87], v[40:43], v[192:195], v[84:87]\n" \
"s_add_u32 m0, s87, 102400\n" \
"ds_read_b64_tr_b16 v[4:5], v232 offset:8192\n" \
"v_mfma_f32_16x16x32_bf16 v[84:87], v[44:47], v[196:199], v[84:87]\n" \
"global_load_lds_dwordx4 v224, s[48:49] offset:-4096\n" \
"v_mfma_f32_16x16x32_bf16 v[88:91], v[48:51], v[192:195], v[88:91]\n" \
"ds_read_b64_tr_b16 v[6:7], v232 offset:10240\n" \
"v_mfma_f32_16x16x32_bf16 v[88:91], v[52:55], v[196:199], v[88:91]\n" \
"ds_read_b64_tr_b16 v[8:9], v233 offset:0\n" \
"v_mfma_f32_16x16x32_bf16 v[92:95], v[56:59], v[192:195], v[92:95]\n" \
"ds_read_b64_tr_b16 v[10:11], v233 offset:2048\n" \
"v_mfma_f32_16x16x32_bf16 v[92:95], v[60:63], v[196:199], v[92:95]\n" \
"global_load_lds_dwordx4 v225, s[48:49] offset:-3072\n" \
"v_mfma_f32_16x16x32_bf16 v[112:115], v[32:35], v[200:203], v[112:115]\n" \
"ds_read_b128 v[192:195], v230 offset:0\n" \
"v_mfma_f32_16x16x32_bf16 v[112:115], v[36:39], v[204:207], v[112:115]\n" \
"ds_read_b128 v[196:199], v231 offset:0\n" \
"v_mfma_f32_16x16x32_bf16 v[116:119], v[40:43], v[200:203], v[116:119]\n" \
"ds_read_b64_tr_b16 v[12:13], v233 offset:8192\n" \
"v_mfma_f32_16x16x32_bf16 v[116:119], v[44:47], v[204:207], v[116:119]\n" \
"global_load_lds_dwordx4 v226, s[48:49] offset:-2048\n" \
"v_mfma_f32_16x16x32_bf16 v[120:123], v[48:51], v[200:203], v[120:123]\n" \
"ds_read_b64_tr_b16 v[14:15], v233 offset:10240\n" \
"v_mfma_f32_16x16x32_bf16 v[120:123], v[52:55], v[204:207], v[120:123]\n" \
"ds_read_b64_tr_b16 v[16:17], v232 offset:512\n" \
"v_mfma_f32_16x16x32_bf16 v[124:127], v[56:59], v[200:203], v[124:127]\n" \
"ds_read_b64_tr_b16 v[18:19], v232 offset:2560\n" \
"v_mfma_f32_16x16x32_bf16 v[124:127], v[60:63], v[204:207], v[124:127]\n" \
"global_load_lds_dwordx4 v227, s[48:49] offset:-1024\n" \
"v_mfma_f32_16x16x32_bf16 v[144:147], v[32:35], v[208:211], v[144:147]\n" \
"s_add_u32 s48, s48, 0x100\n" \
"s_addc_u32 s49, s49, 0\n" \
"ds_read_b128 v[200:203], v230 offset:2048\n" \
"v_mfma_f32_16x16x32_bf16 v[144:147], v[36:39], v[212:215], v[144:147]\n" \
"ds_read_b128 v[204:207], v231 offset:2048\n" \
"v_mfma_f32_16x16x32_bf16 v[148:151], v[40:43], v[208:211], v[148:151]\n" \
"ds_read_b64_tr_b16 v[20:21], v232 offset:8704\n" \
"v_mfma_f32_16x16x32_bf16 v[148:151], v[44:47], v[212:215], v[148:151]\n" \
"global_load_lds_dwordx4 v224, s[46:47] offset:0\n" \
"v_mfma_f32_16x16x32_bf16 v[152:155], v[48:51], v[208:211], v[152:155]\n" \
"ds_read_b64_tr_b16 v[22:23], v232 offset:10752\n" \
"v_mfma_f32_16x16x32_bf16 v[152:155], v[52:55], v[212:215], v[152:155]\n" \
"ds_read_b64_tr_b16 v[24:25], v233 offset:512\n" \
"v_mfma_f32_16x16x32_bf16 v[156:159], v[56:59], v[208:211], v[156:159]\n" \
"ds_read_b64_tr_b16 v[26:27], v233 offset:2560\n" \
"v_mfma_f32_16x16x32_bf16 v[156:159], v[60:63], v[212:215], v[156:159]\n" \
"global_load_lds_dwordx4 v225, s[46:47] offset:1024\n" \
"v_mfma_f32_16x16x32_bf16 v[176:179], v[32:35], v[216:219], v[176:179]\n" \
"ds_read_b128 v[208:211], v230 offset:4096\n" \
"v_mfma_f32_16x16x32_bf16 v[176:179], v[36:39], v[220:223], v[176:179]\n" \
"ds_read_b128 v[212:215], v231 offset:4096\n" \
"v_mfma_f32_16x16x32_bf16 v[180:183], v[40:43], v[216:219], v[180:183]\n" \
"ds_read_b64_tr_b16 v[28:29], v233 offset:8704\n" \
"v_mfma_f32_16x16x32_bf16 v[180:183], v[44:47], v[220:223], v[180:183]\n" \
"global_load_lds_dwordx4 v226, s[46:47] offset:2048\n" \
"v_mfma_f32_16x16x32_bf16 v[184:187], v[48:51], v[216:219], v[184:187]\n" \
"ds_read_b64_tr_b16 v[30:31], v233 offset:10752\n" \
"v_mfma_f32_16x16x32_bf16 v[184:187], v[52:55], v[220:223], v[184:187]\n" \
"v_mfma_f32_16x16x32_bf16 v[188:191], v[56:59], v[216:219], v[188:191]\n" \
"v_mfma_f32_16x16x32_bf16 v[188:191], v[60:63], v[220:223], v[188:191]\n" \
"s_waitcnt lgkmcnt(1)\n" \
"global_load_lds_dwordx4 v227, s[46:47] offset:3072\n" \
"v_mfma_f32_16x16x32_bf16 v[64:67], v[0:3], v[192:195], v[64:67]\n" \
"s_add_u32 s46, s46, 0x100\n" \
"s_addc_u32 s47, s47, 0\n" \
"ds_read_b128 v[216:219], v230 offset:6144\n" \
"v_mfma_f32_16x16x32_bf16 v[64:67], v[4:7], v[196:199], v[64:67]\n" \
"ds_read_b128 v[220:223], v231 offset:6144\n" \
"v_mfma_f32_16x16x32_bf16 v[68:71], v[8:11], v[192:195], v[68:71]\n" \
"ds_read_b64_tr_b16 v[32:33], v232 offset:1024\n" \
"v_mfma_f32_16x16x32_bf16 v[68:71], v[12:15], v[196:199], v[68:71]\n" \
"buffer_store_dwordx4 a[148:151], v248, s[56:59], s90 offen offset:1024 sc1\n" \
"v_mfma_f32_16x16x32_bf16 v[72:75], v[16:19], v[192:195], v[72:75]\n" \
"ds_read_b64_tr_b16 v[34:35], v232 offset:3072\n" \
"v_mfma_f32_16x16x32_bf16 v[72:75], v[20:23], v[196:199], v[72:75]\n" \
"s_waitcnt lgkmcnt(3)\n" \
"ds_read_b64_tr_b16 v[36:37], v232 offset:9216\n" \
"v_mfma_f32_16x16x32_bf16 v[76:79], v[24:27], v[192:195], v[76:79]\n" \
"ds_read_b64_tr_b16 v[38:39], v232 offset:11264\n" \
"v_mfma_f32_16x16x32_bf16 v[76:79], v[28:31], v[196:199], v[76:79]\n" \
"v_mfma_f32_16x16x32_bf16 v[96:99], v[0:3], v[200:203], v[96:99]\n" \
"ds_read_b64_tr_b16 v[40:41], v233 offset:1024\n" \
"v_mfma_f32_16x16x32_bf16 v[96:99], v[4:7], v[204:207], v[96:99]\n" \
"ds_read_b64_tr_b16 v[42:43], v233 offset:3072\n" \
"v_mfma_f32_16x16x32_bf16 v[100:103], v[8:11], v[200:203], v[100:103]\n" \
"ds_read_b64_tr_b16 v[44:45], v233 offset:9216\n" \
"v_mfma_f32_16x16x32_bf16 v[100:103], v[12:15], v[204:207], v[100:103]\n" \
"v_mfma_f32_16x16x32_bf16 v[104:107], v[16:19], v[200:203], v[104:107]\n" \
"ds_read_b64_tr_b16 v[46:47], v233 offset:11264\n" \
"v_mfma_f32_16x16x32_bf16 v[104:107], v[20:23], v[204:207], v[104:107]\n" \
"ds_read_b64_tr_b16 v[48:49], v232 offset:1536\n" \
"v_mfma_f32_16x16x32_bf16 v[108:111], v[24:27], v[200:203], v[108:111]\n" \
"s_add_u32 m0, s95, 4096\n" \
"ds_read_b64_tr_b16 v[50:51], v232 offset:3584\n" \
"v_mfma_f32_16x16x32_bf16 v[108:111], v[28:31], v[204:207], v[108:111]\n" \
"global_load_lds_dwordx4 v228, s[44:45] offset:-4096\n" \
"v_mfma_f32_16x16x32_bf16 v[128:131], v[0:3], v[208:211], v[128:131]\n" \
"ds_read_b64_tr_b16 v[52:53], v232 offset:9728\n" \
"v_mfma_f32_16x16x32_bf16 v[128:131], v[4:7], v[212:215], v[128:131]\n" \
"ds_read_b64_tr_b16 v[54:55], v232 offset:11776\n" \
"v_mfma_f32_16x16x32_bf16 v[132:135], v[8:11], v[208:211], v[132:135]\n" \
"ds_read_b64_tr_b16 v[56:57], v233 offset:1536\n" \
"v_mfma_f32_16x16x32_bf16 v[132:135], v[12:15], v[212:215], v[132:135]\n" \
"global_load_lds_dwordx4 v229, s[44:45] offset:-3072\n" \
"v_mfma_f32_16x16x32_bf16 v[136:139], v[16:19], v[208:211], v[136:139]\n" \
"s_add_u32 s44, s44, 0x80000\n" \
"s_addc_u32 s45, s45, 0\n" \
"ds_read_b64_tr_b16 v[58:59], v233 offset:3584\n" \
"v_mfma_f32_16x16x32_bf16 v[136:139], v[20:23], v[212:215], v[136:139]\n" \
"ds_read_b64_tr_b16 v[60:61], v233 offset:9728\n" \
"v_mfma_f32_16x16x32_bf16 v[140:143], v[24:27], v[208:211], v[140:143]\n" \
"ds_read_b64_tr_b16 v[62:63], v233 offset:11776\n" \
"v_mfma_f32_16x16x32_bf16 v[140:143], v[28:31], v[212:215], v[140:143]\n" \
"s_waitcnt lgkmcnt(3)\n" \
"global_load_lds_dwordx4 v228, s[42:43] offset:-2048\n" \
"v_mfma_f32_16x16x32_bf16 v[160:163], v[0:3], v[216:219], v[160:163]\n" \
"v_add_u32 v230, 0xffff0000, v230\n" \
"v_add_u32 v231, 0xffff0000, v231\n" \
"v_mfma_f32_16x16x32_bf16 v[160:163], v[4:7], v[220:223], v[160:163]\n" \
"v_add_u32 v232, 0x4000, v232\n" \
"v_add_u32 v233, 0x4000, v233\n" \
"v_mfma_f32_16x16x32_bf16 v[164:167], v[8:11], v[216:219], v[164:167]\n" \
"v_mfma_f32_16x16x32_bf16 v[164:167], v[12:15], v[220:223], v[164:167]\n" \
"global_load_lds_dwordx4 v229, s[42:43] offset:-1024\n" \
"v_mfma_f32_16x16x32_bf16 v[168:171], v[16:19], v[216:219], v[168:171]\n" \
"s_add_u32 s42, s42, 0x80000\n" \
"s_addc_u32 s43, s43, 0\n" \
"v_mfma_f32_16x16x32_bf16 v[168:171], v[20:23], v[220:223], v[168:171]\n" \
"v_mfma_f32_16x16x32_bf16 v[172:175], v[24:27], v[216:219], v[172:175]\n" \
"s_waitcnt vmcnt(20) lgkmcnt(0)\n" \
"v_mfma_f32_16x16x32_bf16 v[172:175], v[28:31], v[220:223], v[172:175]\n" \
"s_barrier\n" \
"v_mfma_f32_16x16x32_bf16 v[80:83], v[32:35], v[192:195], v[80:83]\n" \
"ds_read_b64_tr_b16 v[0:1], v232 offset:0\n" \
"v_mfma_f32_16x16x32_bf16 v[80:83], v[36:39], v[196:199], v[80:83]\n" \
"ds_read_b64_tr_b16 v[2:3], v232 offset:2048\n" \
"v_mfma_f32_16x16x32_bf16 v[84:87], v[40:43], v[192:195], v[84:87]\n" \
"s_add_u32 m0, s87, 135168\n" \
"ds_read_b64_tr_b16 v[4:5], v232 offset:8192\n" \
"v_mfma_f32_16x16x32_bf16 v[84:87], v[44:47], v[196:199], v[84:87]\n" \
"global_load_lds_dwordx4 v224, s[48:49] offset:-4096\n" \
"v_mfma_f32_16x16x32_bf16 v[88:91], v[48:51], v[192:195], v[88:91]\n" \
"ds_read_b64_tr_b16 v[6:7], v232 offset:10240\n" \
"v_mfma_f32_16x16x32_bf16 v[88:91], v[52:55], v[196:199], v[88:91]\n" \
"ds_read_b64_tr_b16 v[8:9], v233 offset:0\n" \
"v_mfma_f32_16x16x32_bf16 v[92:95], v[56:59], v[192:195], v[92:95]\n" \
"ds_read_b64_tr_b16 v[10:11], v233 offset:2048\n" \
"v_mfma_f32_16x16x32_bf16 v[92:95], v[60:63], v[196:199], v[92:95]\n" \
"global_load_lds_dwordx4 v225, s[48:49] offset:-3072\n" \
"v_mfma_f32_16x16x32_bf16 v[112:115], v[32:35], v[200:203], v[112:115]\n" \
"ds_read_b128 v[192:195], v230 offset:0\n" \
"v_mfma_f32_16x16x32_bf16 v[112:115], v[36:39], v[204:207], v[112:115]\n" \
"ds_read_b128 v[196:199], v231 offset:0\n" \
"v_mfma_f32_16x16x32_bf16 v[116:119], v[40:43], v[200:203], v[116:119]\n" \
"ds_read_b64_tr_b16 v[12:13], v233 offset:8192\n" \
"v_mfma_f32_16x16x32_bf16 v[116:119], v[44:47], v[204:207], v[116:119]\n" \
"global_load_lds_dwordx4 v226, s[48:49] offset:-2048\n" \
"v_mfma_f32_16x16x32_bf16 v[120:123], v[48:51], v[200:203], v[120:123]\n" \
"ds_read_b64_tr_b16 v[14:15], v233 offset:10240\n" \
"v_mfma_f32_16x16x32_bf16 v[120:123], v[52:55], v[204:207], v[120:123]\n" \
"ds_read_b64_tr_b16 v[16:17], v232 offset:512\n" \
"v_mfma_f32_16x16x32_bf16 v[124:127], v[56:59], v[200:203], v[124:127]\n" \
"ds_read_b64_tr_b16 v[18:19], v232 offset:2560\n" \
"v_mfma_f32_16x16x32_bf16 v[124:127], v[60:63], v[204:207], v[124:127]\n" \
"global_load_lds_dwordx4 v227, s[48:49] offset:-1024\n" \
"v_mfma_f32_16x16x32_bf16 v[144:147], v[32:35], v[208:211], v[144:147]\n" \
"s_add_u32 s48, s48, 0x80\n" \
"s_addc_u32 s49, s49, 0\n" \
"ds_read_b128 v[200:203], v230 offset:2048\n" \
"v_mfma_f32_16x16x32_bf16 v[144:147], v[36:39], v[212:215], v[144:147]\n" \
"ds_read_b128 v[204:207], v231 offset:2048\n" \
"v_mfma_f32_16x16x32_bf16 v[148:151], v[40:43], v[208:211], v[148:151]\n" \
"ds_read_b64_tr_b16 v[20:21], v232 offset:8704\n" \
"v_mfma_f32_16x16x32_bf16 v[148:151], v[44:47], v[212:215], v[148:151]\n" \
"global_load_lds_dwordx4 v224, s[46:47] offset:0\n" \
"v_mfma_f32_16x16x32_bf16 v[152:155], v[48:51], v[208:211], v[152:155]\n" \
"ds_read_b64_tr_b16 v[22:23], v232 offset:10752\n" \
"v_mfma_f32_16x16x32_bf16 v[152:155], v[52:55], v[212:215], v[152:155]\n" \
"ds_read_b64_tr_b16 v[24:25], v233 offset:512\n" \
"v_mfma_f32_16x16x32_bf16 v[156:159], v[56:59], v[208:211], v[156:159]\n" \
"ds_read_b64_tr_b16 v[26:27], v233 offset:2560\n" \
"v_mfma_f32_16x16x32_bf16 v[156:159], v[60:63], v[212:215], v[156:159]\n" \
"global_load_lds_dwordx4 v225, s[46:47] offset:1024\n" \
"v_mfma_f32_16x16x32_bf16 v[176:179], v[32:35], v[216:219], v[176:179]\n" \
"ds_read_b128 v[208:211], v230 offset:4096\n" \
"v_mfma_f32_16x16x32_bf16 v[176:179], v[36:39], v[220:223], v[176:179]\n" \
"ds_read_b128 v[212:215], v231 offset:4096\n" \
"v_mfma_f32_16x16x32_bf16 v[180:183], v[40:43], v[216:219], v[180:183]\n" \
"ds_read_b64_tr_b16 v[28:29], v233 offset:8704\n" \
"v_mfma_f32_16x16x32_bf16 v[180:183], v[44:47], v[220:223], v[180:183]\n" \
"global_load_lds_dwordx4 v226, s[46:47] offset:2048\n" \
"v_mfma_f32_16x16x32_bf16 v[184:187], v[48:51], v[216:219], v[184:187]\n" \
"ds_read_b64_tr_b16 v[30:31], v233 offset:10752\n" \
"v_mfma_f32_16x16x32_bf16 v[184:187], v[52:55], v[220:223], v[184:187]\n" \
"v_mfma_f32_16x16x32_bf16 v[188:191], v[56:59], v[216:219], v[188:191]\n" \
"v_mfma_f32_16x16x32_bf16 v[188:191], v[60:63], v[220:223], v[188:191]\n" \
"s_waitcnt lgkmcnt(1)\n" \
"global_load_lds_dwordx4 v227, s[46:47] offset:3072\n" \
"v_mfma_f32_16x16x32_bf16 v[64:67], v[0:3], v[192:195], v[64:67]\n" \
"s_add_u32 s46, s46, 0x80\n" \
"s_addc_u32 s47, s47, 0\n" \
"ds_read_b128 v[216:219], v230 offset:6144\n" \
"v_mfma_f32_16x16x32_bf16 v[64:67], v[4:7], v[196:199], v[64:67]\n" \
"ds_read_b128 v[220:223], v231 offset:6144\n" \
"v_mfma_f32_16x16x32_bf16 v[68:71], v[8:11], v[192:195], v[68:71]\n" \
"ds_read_b64_tr_b16 v[32:33], v232 offset:1024\n" \
"v_mfma_f32_16x16x32_bf16 v[68:71], v[12:15], v[196:199], v[68:71]\n" \
"buffer_store_dwordx4 a[152:155], v248, s[56:59], s90 offen offset:2048 sc1\n" \
"v_mfma_f32_16x16x32_bf16 v[72:75], v[16:19], v[192:195], v[72:75]\n" \
"ds_read_b64_tr_b16 v[34:35], v232 offset:3072\n" \
"v_mfma_f32_16x16x32_bf16 v[72:75], v[20:23], v[196:199], v[72:75]\n" \
"s_waitcnt lgkmcnt(3)\n" \
"ds_read_b64_tr_b16 v[36:37], v232 offset:9216\n" \
"v_mfma_f32_16x16x32_bf16 v[76:79], v[24:27], v[192:195], v[76:79]\n" \
"ds_read_b64_tr_b16 v[38:39], v232 offset:11264\n" \
"v_mfma_f32_16x16x32_bf16 v[76:79], v[28:31], v[196:199], v[76:79]\n" \
"v_mfma_f32_16x16x32_bf16 v[96:99], v[0:3], v[200:203], v[96:99]\n" \
"ds_read_b64_tr_b16 v[40:41], v233 offset:1024\n" \
"v_mfma_f32_16x16x32_bf16 v[96:99], v[4:7], v[204:207], v[96:99]\n" \
"ds_read_b64_tr_b16 v[42:43], v233 offset:3072\n" \
"v_mfma_f32_16x16x32_bf16 v[100:103], v[8:11], v[200:203], v[100:103]\n" \
"ds_read_b64_tr_b16 v[44:45], v233 offset:9216\n" \
"v_mfma_f32_16x16x32_bf16 v[100:103], v[12:15], v[204:207], v[100:103]\n" \
"v_mfma_f32_16x16x32_bf16 v[104:107], v[16:19], v[200:203], v[104:107]\n" \
"ds_read_b64_tr_b16 v[46:47], v233 offset:11264\n" \
"v_mfma_f32_16x16x32_bf16 v[104:107], v[20:23], v[204:207], v[104:107]\n" \
"ds_read_b64_tr_b16 v[48:49], v232 offset:1536\n" \
"v_mfma_f32_16x16x32_bf16 v[108:111], v[24:27], v[200:203], v[108:111]\n" \
"s_add_u32 m0, s95, 20480\n" \
"ds_read_b64_tr_b16 v[50:51], v232 offset:3584\n" \
"v_mfma_f32_16x16x32_bf16 v[108:111], v[28:31], v[204:207], v[108:111]\n" \
"global_load_lds_dwordx4 v228, s[44:45] offset:-4096\n" \
"v_mfma_f32_16x16x32_bf16 v[128:131], v[0:3], v[208:211], v[128:131]\n" \
"ds_read_b64_tr_b16 v[52:53], v232 offset:9728\n" \
"v_mfma_f32_16x16x32_bf16 v[128:131], v[4:7], v[212:215], v[128:131]\n" \
"ds_read_b64_tr_b16 v[54:55], v232 offset:11776\n" \
"v_mfma_f32_16x16x32_bf16 v[132:135], v[8:11], v[208:211], v[132:135]\n" \
"ds_read_b64_tr_b16 v[56:57], v233 offset:1536\n" \
"v_mfma_f32_16x16x32_bf16 v[132:135], v[12:15], v[212:215], v[132:135]\n" \
"global_load_lds_dwordx4 v229, s[44:45] offset:-3072\n" \
"v_mfma_f32_16x16x32_bf16 v[136:139], v[16:19], v[208:211], v[136:139]\n" \
"s_add_u32 s44, s44, 0x80000\n" \
"s_addc_u32 s45, s45, 0\n" \
"ds_read_b64_tr_b16 v[58:59], v233 offset:3584\n" \
"v_mfma_f32_16x16x32_bf16 v[136:139], v[20:23], v[212:215], v[136:139]\n" \
"ds_read_b64_tr_b16 v[60:61], v233 offset:9728\n" \
"v_mfma_f32_16x16x32_bf16 v[140:143], v[24:27], v[208:211], v[140:143]\n" \
"ds_read_b64_tr_b16 v[62:63], v233 offset:11776\n" \
"v_mfma_f32_16x16x32_bf16 v[140:143], v[28:31], v[212:215], v[140:143]\n" \
"s_waitcnt lgkmcnt(3)\n" \
"global_load_lds_dwordx4 v228, s[42:43] offset:-2048\n" \
"v_mfma_f32_16x16x32_bf16 v[160:163], v[0:3], v[216:219], v[160:163]\n" \
"v_add_u32 v230, 0x8000, v230\n" \
"v_add_u32 v231, 0x8000, v231\n" \
"v_mfma_f32_16x16x32_bf16 v[160:163], v[4:7], v[220:223], v[160:163]\n" \
"v_add_u32 v232, 0x4000, v232\n" \
"v_add_u32 v233, 0x4000, v233\n" \
"v_mfma_f32_16x16x32_bf16 v[164:167], v[8:11], v[216:219], v[164:167]\n" \
"v_mfma_f32_16x16x32_bf16 v[164:167], v[12:15], v[220:223], v[164:167]\n" \
"global_load_lds_dwordx4 v229, s[42:43] offset:-1024\n" \
"v_mfma_f32_16x16x32_bf16 v[168:171], v[16:19], v[216:219], v[168:171]\n" \
"s_add_u32 s42, s42, 0x80000\n" \
"s_addc_u32 s43, s43, 0\n" \
"v_mfma_f32_16x16x32_bf16 v[168:171], v[20:23], v[220:223], v[168:171]\n" \
"v_mfma_f32_16x16x32_bf16 v[172:175], v[24:27], v[216:219], v[172:175]\n" \
"s_waitcnt vmcnt(18) lgkmcnt(0)\n" \
"v_mfma_f32_16x16x32_bf16 v[172:175], v[28:31], v[220:223], v[172:175]\n" \
"s_barrier\n" \
"v_mfma_f32_16x16x32_bf16 v[80:83], v[32:35], v[192:195], v[80:83]\n" \
"ds_read_b64_tr_b16 v[0:1], v232 offset:0\n" \
"v_mfma_f32_16x16x32_bf16 v[80:83], v[36:39], v[196:199], v[80:83]\n" \
"ds_read_b64_tr_b16 v[2:3], v232 offset:2048\n" \
"v_mfma_f32_16x16x32_bf16 v[84:87], v[40:43], v[192:195], v[84:87]\n" \
"s_add_u32 m0, s87, 69632\n" \
"ds_read_b64_tr_b16 v[4:5], v232 offset:8192\n" \
"v_mfma_f32_16x16x32_bf16 v[84:87], v[44:47], v[196:199], v[84:87]\n" \
"global_load_lds_dwordx4 v224, s[48:49] offset:-4096\n" \
"v_mfma_f32_16x16x32_bf16 v[88:91], v[48:51], v[192:195], v[88:91]\n" \
"ds_read_b64_tr_b16 v[6:7], v232 offset:10240\n" \
"v_mfma_f32_16x16x32_bf16 v[88:91], v[52:55], v[196:199], v[88:91]\n" \
"ds_read_b64_tr_b16 v[8:9], v233 offset:0\n" \
"v_mfma_f32_16x16x32_bf16 v[92:95], v[56:59], v[192:195], v[92:95]\n" \
"ds_read_b64_tr_b16 v[10:11], v233 offset:2048\n" \
"v_mfma_f32_16x16x32_bf16 v[92:95], v[60:63], v[196:199], v[92:95]\n" \
"global_load_lds_dwordx4 v225, s[48:49] offset:-3072\n" \
"v_mfma_f32_16x16x32_bf16 v[112:115], v[32:35], v[200:203], v[112:115]\n" \
"ds_read_b128 v[192:195], v230 offset:0\n" \
"v_mfma_f32_16x16x32_bf16 v[112:115], v[36:39], v[204:207], v[112:115]\n" \
"ds_read_b128 v[196:199], v231 offset:0\n" \
"v_mfma_f32_16x16x32_bf16 v[116:119], v[40:43], v[200:203], v[116:119]\n" \
"ds_read_b64_tr_b16 v[12:13], v233 offset:8192\n" \
"v_mfma_f32_16x16x32_bf16 v[116:119], v[44:47], v[204:207], v[116:119]\n" \
"global_load_lds_dwordx4 v226, s[48:49] offset:-2048\n" \
"v_mfma_f32_16x16x32_bf16 v[120:123], v[48:51], v[200:203], v[120:123]\n" \
"ds_read_b64_tr_b16 v[14:15], v233 offset:10240\n" \
"v_mfma_f32_16x16x32_bf16 v[120:123], v[52:55], v[204:207], v[120:123]\n" \
"ds_read_b64_tr_b16 v[16:17], v232 offset:512\n" \
"v_mfma_f32_16x16x32_bf16 v[124:127], v[56:59], v[200:203], v[124:127]\n" \
"ds_read_b64_tr_b16 v[18:19], v232 offset:2560\n" \
"v_mfma_f32_16x16x32_bf16 v[124:127], v[60:63], v[204:207], v[124:127]\n" \
"global_load_lds_dwordx4 v227, s[48:49] offset:-1024\n" \
"v_mfma_f32_16x16x32_bf16 v[144:147], v[32:35], v[208:211], v[144:147]\n" \
"s_add_u32 s48, s48, 0x80\n" \
"s_addc_u32 s49, s49, 0\n" \
"ds_read_b128 v[200:203], v230 offset:2048\n" \
"v_mfma_f32_16x16x32_bf16 v[144:147], v[36:39], v[212:215], v[144:147]\n" \
"ds_read_b128 v[204:207], v231 offset:2048\n" \
"v_mfma_f32_16x16x32_bf16 v[148:151], v[40:43], v[208:211], v[148:151]\n" \
"ds_read_b64_tr_b16 v[20:21], v232 offset:8704\n" \
"v_mfma_f32_16x16x32_bf16 v[148:151], v[44:47], v[212:215], v[148:151]\n" \
"global_load_lds_dwordx4 v224, s[46:47] offset:0\n" \
"v_mfma_f32_16x16x32_bf16 v[152:155], v[48:51], v[208:211], v[152:155]\n" \
"ds_read_b64_tr_b16 v[22:23], v232 offset:10752\n" \
"v_mfma_f32_16x16x32_bf16 v[152:155], v[52:55], v[212:215], v[152:155]\n" \
"ds_read_b64_tr_b16 v[24:25], v233 offset:512\n" \
"v_mfma_f32_16x16x32_bf16 v[156:159], v[56:59], v[208:211], v[156:159]\n" \
"ds_read_b64_tr_b16 v[26:27], v233 offset:2560\n" \
"v_mfma_f32_16x16x32_bf16 v[156:159], v[60:63], v[212:215], v[156:159]\n" \
"global_load_lds_dwordx4 v225, s[46:47] offset:1024\n" \
"v_mfma_f32_16x16x32_bf16 v[176:179], v[32:35], v[216:219], v[176:179]\n" \
"ds_read_b128 v[208:211], v230 offset:4096\n" \
"v_mfma_f32_16x16x32_bf16 v[176:179], v[36:39], v[220:223], v[176:179]\n" \
"ds_read_b128 v[212:215], v231 offset:4096\n" \
"v_mfma_f32_16x16x32_bf16 v[180:183], v[40:43], v[216:219], v[180:183]\n" \
"ds_read_b64_tr_b16 v[28:29], v233 offset:8704\n" \
"v_mfma_f32_16x16x32_bf16 v[180:183], v[44:47], v[220:223], v[180:183]\n" \
"global_load_lds_dwordx4 v226, s[46:47] offset:2048\n" \
"v_mfma_f32_16x16x32_bf16 v[184:187], v[48:51], v[216:219], v[184:187]\n" \
"ds_read_b64_tr_b16 v[30:31], v233 offset:10752\n" \
"v_mfma_f32_16x16x32_bf16 v[184:187], v[52:55], v[220:223], v[184:187]\n" \
"v_mfma_f32_16x16x32_bf16 v[188:191], v[56:59], v[216:219], v[188:191]\n" \
"v_mfma_f32_16x16x32_bf16 v[188:191], v[60:63], v[220:223], v[188:191]\n" \
"s_waitcnt lgkmcnt(1)\n" \
"global_load_lds_dwordx4 v227, s[46:47] offset:3072\n" \
"v_mfma_f32_16x16x32_bf16 v[64:67], v[0:3], v[192:195], v[64:67]\n" \
"s_add_u32 s46, s46, 0x80\n" \
"s_addc_u32 s47, s47, 0\n" \
"ds_read_b128 v[216:219], v230 offset:6144\n" \
"v_mfma_f32_16x16x32_bf16 v[64:67], v[4:7], v[196:199], v[64:67]\n" \
"s_mul_i32 s77, s62, 557824\n" \
"ds_read_b128 v[220:223], v231 offset:6144\n" \
"s_add_u32 s77, s77, s95\n" \
"v_mfma_f32_16x16x32_bf16 v[68:71], v[8:11], v[192:195], v[68:71]\n" \
"s_add_u32 s36, s28, s77\n" \
"s_mov_b32 s37, s29\n" \
"ds_read_b64_tr_b16 v[32:33], v232 offset:1024\n" \
"v_mfma_f32_16x16x32_bf16 v[68:71], v[12:15], v[196:199], v[68:71]\n" \
"buffer_store_dwordx4 a[156:159], v248, s[56:59], s90 offen offset:3072 sc1\n" \
"s_add_u32 s77, s77, 17850368\n" \
"s_add_u32 s38, s28, s77\n" \
"s_mov_b32 s39, s29\n" \
"s_and_b32 s74, s63, 1\n" \
"v_mfma_f32_16x16x32_bf16 v[72:75], v[16:19], v[192:195], v[72:75]\n" \
"s_add_u32 s90, s88, 278528\n" \
"s_lshl_b32 s74, s74, 15\n" \
"ds_read_b64_tr_b16 v[34:35], v232 offset:3072\n" \
"v_mfma_f32_16x16x32_bf16 v[72:75], v[20:23], v[196:199], v[72:75]\n" \
"s_waitcnt lgkmcnt(3)\n" \
"s_xor_b32 s74, s74, 0x18000\n" \
"ds_read_b64_tr_b16 v[36:37], v232 offset:9216\n" \
"v_mfma_f32_16x16x32_bf16 v[76:79], v[24:27], v[192:195], v[76:79]\n" \
"ds_read_b64_tr_b16 v[38:39], v232 offset:11264\n" \
"v_add_u32 v247, s74, v248\n" \
"v_mfma_f32_16x16x32_bf16 v[76:79], v[28:31], v[196:199], v[76:79]\n" \
"buffer_store_dwordx4 a[64:67], v249, s[56:59], s90 offen offset:0 sc1\n" \
"s_add_u32 s74, s74, s95\n" \
"s_lshr_b32 s83, s61, 1\n" \
"s_lshl_b32 s75, s63, 2\n" \
"s_add_u32 s77, s83, s75\n" \
"v_mfma_f32_16x16x32_bf16 v[96:99], v[0:3], v[200:203], v[96:99]\n" \
"s_mul_i32 s77, s77, 266240\n" \
"s_sub_u32 s78, s83, s75\n" \
"ds_read_b64_tr_b16 v[40:41], v233 offset:1024\n" \
"v_mfma_f32_16x16x32_bf16 v[96:99], v[4:7], v[204:207], v[96:99]\n" \
"s_add_u32 s78, s78, 60\n" \
"s_mul_i32 s78, s78, 266240\n" \
"ds_read_b64_tr_b16 v[42:43], v233 offset:3072\n" \
"v_mfma_f32_16x16x32_bf16 v[100:103], v[8:11], v[200:203], v[100:103]\n" \
"s_and_b32 s82, s61, 1\n" \
"ds_read_b128 a[64:67], v254\n" \
"ds_read_b64_tr_b16 v[44:45], v233 offset:9216\n" \
"v_mfma_f32_16x16x32_bf16 v[100:103], v[12:15], v[204:207], v[100:103]\n" \
"buffer_load_dword v244, v245, s[56:59], s91 offen sc1\n" \
"v_mfma_f32_16x16x32_bf16 v[104:107], v[16:19], v[200:203], v[104:107]\n" \
"s_cmp_eq_u32 s82, 0\n" \
"s_cselect_b32 s16, 0, 0xff00\n" \
"ds_read_b64_tr_b16 v[46:47], v233 offset:11264\n" \
"v_mfma_f32_16x16x32_bf16 v[104:107], v[20:23], v[204:207], v[104:107]\n" \
"s_mov_b32 s17, s16\n" \
"s_lshl_b32 s75, s82, 8\n" \
"ds_read_b64_tr_b16 v[48:49], v232 offset:1536\n" \
"v_mfma_f32_16x16x32_bf16 v[108:111], v[24:27], v[200:203], v[108:111]\n" \
"s_add_u32 m0, s95, 36864\n" \
"s_lshl_b32 s76, s62, 13\n" \
"ds_read_b64_tr_b16 v[50:51], v232 offset:3584\n" \
"v_mfma_f32_16x16x32_bf16 v[108:111], v[28:31], v[204:207], v[108:111]\n" \
"s_add_u32 s75, s75, s76\n" \
"ds_read_b128 a[68:71], v254\n" \
"global_load_lds_dwordx4 v228, s[44:45] offset:-4096\n" \
"v_mfma_f32_16x16x32_bf16 v[128:131], v[0:3], v[208:211], v[128:131]\n" \
"s_add_u32 s75, s75, 35700736\n" \
"s_add_u32 s77, s77, s75\n" \
"ds_read_b64_tr_b16 v[52:53], v232 offset:9728\n" \
"v_mfma_f32_16x16x32_bf16 v[128:131], v[4:7], v[212:215], v[128:131]\n" \
"s_add_u32 s78, s78, s75\n" \
"s_mul_i32 s83, s83, 9216\n" \
"ds_read_b64_tr_b16 v[54:55], v232 offset:11776\n" \
"v_mfma_f32_16x16x32_bf16 v[132:135], v[8:11], v[208:211], v[132:135]\n" \
"s_lshl_b32 s75, s82, 12\n" \
"s_add_u32 s83, s83, s75\n" \
"ds_read_b64_tr_b16 v[56:57], v233 offset:1536\n" \
"v_mfma_f32_16x16x32_bf16 v[132:135], v[12:15], v[212:215], v[132:135]\n" \
"s_add_u32 s83, s83, 4096\n" \
"global_load_lds_dwordx4 v229, s[44:45] offset:-3072\n" \
"v_mfma_f32_16x16x32_bf16 v[136:139], v[16:19], v[208:211], v[136:139]\n" \
"s_add_u32 s44, s44, 0x80000\n" \
"s_addc_u32 s45, s45, 0\n" \
"ds_read_b64_tr_b16 v[58:59], v233 offset:3584\n" \
"v_mfma_f32_16x16x32_bf16 v[136:139], v[20:23], v[212:215], v[136:139]\n" \
"s_lshl_b32 s82, s82, 14\n" \
"ds_read_b64_tr_b16 v[60:61], v233 offset:9728\n" \
"v_mfma_f32_16x16x32_bf16 v[140:143], v[24:27], v[208:211], v[140:143]\n" \
"s_cmp_eq_u32 s63, 7\n" \
"s_cselect_b32 s82, 0, s82\n" \
"ds_read_b64_tr_b16 v[62:63], v233 offset:11776\n" \
"v_mfma_f32_16x16x32_bf16 v[140:143], v[28:31], v[212:215], v[140:143]\n" \
"s_waitcnt lgkmcnt(3)\n" \
"global_load_lds_dwordx4 v228, s[42:43] offset:-2048\n" \
"s_mov_b32 s75, s63\n" \
"v_mfma_f32_16x16x32_bf16 v[160:163], v[0:3], v[216:219], v[160:163]\n" \
"s_sub_u32 s76, 7, s63\n" \
"s_mov_b32 s73, s95\n" \
"s_mov_b64 s[40:41], s[14:15]\n" \
"v_mfma_f32_16x16x32_bf16 v[160:163], v[4:7], v[220:223], v[160:163]\n" \
"s_mov_b32 s80, 0\n" \
"v_add_u32 v230, 0x8000, v230\n" \
"s_mov_b32 s81, 0\n" \
"v_mfma_f32_16x16x32_bf16 v[164:167], v[8:11], v[216:219], v[164:167]\n" \
"s_cmp_eq_u32 s63, 7\n" \
"s_cselect_b64 s[40:41], s[18:19], s[40:41]\n" \
"s_cselect_b32 s80, 12, s80\n" \
"v_mfma_f32_16x16x32_bf16 v[164:167], v[12:15], v[220:223], v[164:167]\n" \
"global_load_lds_dwordx4 v229, s[42:43] offset:-1024\n" \
"v_add_u32 v231, 0x8000, v231\n" \
"v_mfma_f32_16x16x32_bf16 v[168:171], v[16:19], v[216:219], v[168:171]\n" \
"s_add_u32 s42, s42, 0x80000\n" \
"s_addc_u32 s43, s43, 0\n" \
"v_add_u32 v232, 0xffff4000, v232\n" \
"v_mfma_f32_16x16x32_bf16 v[168:171], v[20:23], v[220:223], v[168:171]\n" \
"s_cmp_eq_u32 s63, 6\n" \
"s_cselect_b64 s[40:41], s[22:23], s[40:41]\n" \
"s_cselect_b32 s80, 12, s80\n" \
"v_mfma_f32_16x16x32_bf16 v[172:175], v[24:27], v[216:219], v[172:175]\n" \
"s_waitcnt vmcnt(20) lgkmcnt(0)\n" \
"v_add_u32 v233, 0xffff4000, v233\n" \
"v_mfma_f32_16x16x32_bf16 v[172:175], v[28:31], v[220:223], v[172:175]\n" \
"s_barrier\n" \
"v_mfma_f32_16x16x32_bf16 v[80:83], v[32:35], v[192:195], v[80:83]\n" \
"ds_read_b64_tr_b16 v[0:1], v232 offset:0\n" \
"v_mfma_f32_16x16x32_bf16 v[80:83], v[36:39], v[196:199], v[80:83]\n" \
"ds_read_b64_tr_b16 v[2:3], v232 offset:2048\n" \
"v_mfma_f32_16x16x32_bf16 v[84:87], v[40:43], v[192:195], v[84:87]\n" \
"s_add_u32 m0, s87, 102400\n" \
"ds_read_b64_tr_b16 v[4:5], v232 offset:8192\n" \
"v_mfma_f32_16x16x32_bf16 v[84:87], v[44:47], v[196:199], v[84:87]\n" \
"global_load_lds_dwordx4 v224, s[48:49] offset:-4096\n" \
"v_mfma_f32_16x16x32_bf16 v[88:91], v[48:51], v[192:195], v[88:91]\n" \
"ds_read_b64_tr_b16 v[6:7], v232 offset:10240\n" \
"v_mfma_f32_16x16x32_bf16 v[88:91], v[52:55], v[196:199], v[88:91]\n" \
"ds_read_b64_tr_b16 v[8:9], v233 offset:0\n" \
"v_mfma_f32_16x16x32_bf16 v[92:95], v[56:59], v[192:195], v[92:95]\n" \
"ds_read_b64_tr_b16 v[10:11], v233 offset:2048\n" \
"v_mfma_f32_16x16x32_bf16 v[92:95], v[60:63], v[196:199], v[92:95]\n" \
"global_load_lds_dwordx4 v225, s[48:49] offset:-3072\n" \
"v_mfma_f32_16x16x32_bf16 v[112:115], v[32:35], v[200:203], v[112:115]\n" \
"ds_read_b128 v[192:195], v230 offset:0\n" \
"v_mfma_f32_16x16x32_bf16 v[112:115], v[36:39], v[204:207], v[112:115]\n" \
"ds_read_b128 v[196:199], v231 offset:0\n" \
"v_mfma_f32_16x16x32_bf16 v[116:119], v[40:43], v[200:203], v[116:119]\n" \
"ds_read_b64_tr_b16 v[12:13], v233 offset:8192\n" \
"v_mfma_f32_16x16x32_bf16 v[116:119], v[44:47], v[204:207], v[116:119]\n" \
"global_load_lds_dwordx4 v226, s[48:49] offset:-2048\n" \
"v_mfma_f32_16x16x32_bf16 v[120:123], v[48:51], v[200:203], v[120:123]\n" \
"ds_read_b64_tr_b16 v[14:15], v233 offset:10240\n" \
"v_mfma_f32_16x16x32_bf16 v[120:123], v[52:55], v[204:207], v[120:123]\n" \
"ds_read_b64_tr_b16 v[16:17], v232 offset:512\n" \
"v_mfma_f32_16x16x32_bf16 v[124:127], v[56:59], v[200:203], v[124:127]\n" \
"ds_read_b64_tr_b16 v[18:19], v232 offset:2560\n" \
"v_mfma_f32_16x16x32_bf16 v[124:127], v[60:63], v[204:207], v[124:127]\n" \
"global_load_lds_dwordx4 v227, s[48:49] offset:-1024\n" \
"v_mfma_f32_16x16x32_bf16 v[144:147], v[32:35], v[208:211], v[144:147]\n" \
"s_add_u32 s48, s48, 0x80\n" \
"s_addc_u32 s49, s49, 0\n" \
"ds_read_b128 v[200:203], v230 offset:2048\n" \
"v_mfma_f32_16x16x32_bf16 v[144:147], v[36:39], v[212:215], v[144:147]\n" \
"ds_read_b128 v[204:207], v231 offset:2048\n" \
"v_mfma_f32_16x16x32_bf16 v[148:151], v[40:43], v[208:211], v[148:151]\n" \
"ds_read_b64_tr_b16 v[20:21], v232 offset:8704\n" \
"v_mfma_f32_16x16x32_bf16 v[148:151], v[44:47], v[212:215], v[148:151]\n" \
"global_load_lds_dwordx4 v224, s[46:47] offset:0\n" \
"v_mfma_f32_16x16x32_bf16 v[152:155], v[48:51], v[208:211], v[152:155]\n" \
"ds_read_b64_tr_b16 v[22:23], v232 offset:10752\n" \
"v_mfma_f32_16x16x32_bf16 v[152:155], v[52:55], v[212:215], v[152:155]\n" \
"ds_read_b64_tr_b16 v[24:25], v233 offset:512\n" \
"v_mfma_f32_16x16x32_bf16 v[156:159], v[56:59], v[208:211], v[156:159]\n" \
"ds_read_b64_tr_b16 v[26:27], v233 offset:2560\n" \
"v_mfma_f32_16x16x32_bf16 v[156:159], v[60:63], v[212:215], v[156:159]\n" \
"global_load_lds_dwordx4 v225, s[46:47] offset:1024\n" \
"Lpg4399_%=:\n" \
"s_cbranch_execz Lpg5583_%=\n" \
"v_mfma_f32_16x16x32_bf16 v[176:179], v[32:35], v[216:219], v[176:179]\n" \
"ds_read_b128 v[208:211], v230 offset:4096\n" \
"v_mfma_f32_16x16x32_bf16 v[176:179], v[36:39], v[220:223], v[176:179]\n" \
"ds_read_b128 v[212:215], v231 offset:4096\n" \
"v_mfma_f32_16x16x32_bf16 v[180:183], v[40:43], v[216:219], v[180:183]\n" \
"ds_read_b64_tr_b16 v[28:29], v233 offset:8704\n" \
"v_mfma_f32_16x16x32_bf16 v[180:183], v[44:47], v[220:223], v[180:183]\n" \
"global_load_lds_dwordx4 v226, s[46:47] offset:2048\n" \
"v_mfma_f32_16x16x32_bf16 v[184:187], v[48:51], v[216:219], v[184:187]\n" \
"ds_read_b64_tr_b16 v[30:31], v233 offset:10752\n" \
"v_mfma_f32_16x16x32_bf16 v[184:187], v[52:55], v[220:223], v[184:187]\n" \
"s_cmp_eq_u32 s63, 5\n" \
"s_cselect_b64 s[40:41], s[18:19], s[40:41]\n" \
"s_cselect_b32 s80, 4, s80\n" \
"v_mfma_f32_16x16x32_bf16 v[188:191], v[56:59], v[216:219], v[188:191]\n" \
"s_cmp_eq_u32 s63, 5\n" \
"s_cselect_b32 s81, 384, s81\n" \
"v_mfma_f32_16x16x32_bf16 v[188:191], v[60:63], v[220:223], v[188:191]\n" \
"s_waitcnt lgkmcnt(1)\n" \
"global_load_lds_dwordx4 v227, s[46:47] offset:3072\n" \
"v_mfma_f32_16x16x32_bf16 v[64:67], v[0:3], v[192:195], v[64:67]\n" \
"s_add_u32 s46, s46, 0x80\n" \
"s_addc_u32 s47, s47, 0\n" \
"ds_read_b128 v[216:219], v230 offset:6144\n" \
"v_mfma_f32_16x16x32_bf16 v[64:67], v[4:7], v[196:199], v[64:67]\n" \
"ds_read_b128 v[220:223], v231 offset:6144\n" \
"v_mfma_f32_16x16x32_bf16 v[68:71], v[8:11], v[192:195], v[68:71]\n" \
"ds_read_b64_tr_b16 v[32:33], v232 offset:1024\n" \
"v_mfma_f32_16x16x32_bf16 v[68:71], v[12:15], v[196:199], v[68:71]\n" \
"buffer_store_dwordx4 a[96:99], v249, s[56:59], s90 offen offset:256 sc1\n" \
"s_cmp_eq_u32 s63, 4\n" \
"s_cselect_b64 s[40:41], s[20:21], s[40:41]\n" \
"s_cselect_b32 s80, 8, s80\n" \
"v_mfma_f32_16x16x32_bf16 v[72:75], v[16:19], v[192:195], v[72:75]\n" \
"ds_read_b64_tr_b16 v[34:35], v232 offset:3072\n" \
"v_mfma_f32_16x16x32_bf16 v[72:75], v[20:23], v[196:199], v[72:75]\n" \
"s_waitcnt lgkmcnt(3)\n" \
"ds_read_b64_tr_b16 v[36:37], v232 offset:9216\n" \
"v_mfma_f32_16x16x32_bf16 v[76:79], v[24:27], v[192:195], v[76:79]\n" \
"ds_read_b128 a[96:99], v254\n" \
"ds_read_b64_tr_b16 v[38:39], v232 offset:11264\n" \
"v_mfma_f32_16x16x32_bf16 v[76:79], v[28:31], v[196:199], v[76:79]\n" \
"buffer_store_dwordx4 a[72:75], v249, s[56:59], s90 offen offset:2048 sc1\n" \
"s_cmp_eq_u32 s63, 2\n" \
"s_cselect_b64 s[40:41], s[24:25], s[40:41]\n" \
"s_cselect_b32 s80, 4, s80\n" \
"s_add_u32 s81, s81, s62\n" \
"v_mfma_f32_16x16x32_bf16 v[96:99], v[0:3], v[200:203], v[96:99]\n" \
"s_lshl_b32 s81, s81, 2\n" \
"s_add_u32 s81, s81, s61\n" \
"ds_read_b64_tr_b16 v[40:41], v233 offset:1024\n" \
"v_mfma_f32_16x16x32_bf16 v[96:99], v[4:7], v[204:207], v[96:99]\n" \
"s_lshl_b32 s81, s81, 12\n" \
"ds_read_b128 a[100:103], v254\n" \
"ds_read_b64_tr_b16 v[42:43], v233 offset:3072\n" \
"v_mfma_f32_16x16x32_bf16 v[100:103], v[8:11], v[200:203], v[100:103]\n" \
"s_add_u32 s40, s40, s81\n" \
"s_addc_u32 s41, s41, 0\n" \
"ds_read_b64_tr_b16 v[44:45], v233 offset:9216\n" \
"v_mfma_f32_16x16x32_bf16 v[100:103], v[12:15], v[204:207], v[100:103]\n" \
"s_min_u32 s81, s80, 1\n" \
"s_lshl_b32 s81, s81, 2\n" \
"v_mul_u32_u24 v252, s81, v248\n" \
"v_mfma_f32_16x16x32_bf16 v[104:107], v[16:19], v[200:203], v[104:107]\n" \
"ds_read_b128 a[72:75], v254\n" \
"ds_read_b64_tr_b16 v[46:47], v233 offset:11264\n" \
"v_mfma_f32_16x16x32_bf16 v[104:107], v[20:23], v[204:207], v[104:107]\n" \
"ds_read_b128 a[76:79], v254\n" \
"ds_read_b64_tr_b16 v[48:49], v232 offset:1536\n" \
"v_mfma_f32_16x16x32_bf16 v[108:111], v[24:27], v[200:203], v[108:111]\n" \
"s_add_u32 m0, s95, 53248\n" \
"ds_read_b64_tr_b16 v[50:51], v232 offset:3584\n" \
"v_mfma_f32_16x16x32_bf16 v[108:111], v[28:31], v[204:207], v[108:111]\n" \
"global_load_lds_dwordx4 v228, s[44:45] offset:-4096\n" \
"v_mfma_f32_16x16x32_bf16 v[128:131], v[0:3], v[208:211], v[128:131]\n" \
"ds_read_b64_tr_b16 v[52:53], v232 offset:9728\n" \
"v_mfma_f32_16x16x32_bf16 v[128:131], v[4:7], v[212:215], v[128:131]\n" \
"ds_read_b64_tr_b16 v[54:55], v232 offset:11776\n" \
"v_mfma_f32_16x16x32_bf16 v[132:135], v[8:11], v[208:211], v[132:135]\n" \
"ds_read_b64_tr_b16 v[56:57], v233 offset:1536\n" \
"v_mfma_f32_16x16x32_bf16 v[132:135], v[12:15], v[212:215], v[132:135]\n" \
"global_load_lds_dwordx4 v229, s[44:45] offset:-3072\n" \
"v_mfma_f32_16x16x32_bf16 v[136:139], v[16:19], v[208:211], v[136:139]\n" \
"ds_read_b64_tr_b16 v[58:59], v233 offset:3584\n" \
"v_mfma_f32_16x16x32_bf16 v[136:139], v[20:23], v[212:215], v[136:139]\n" \
"ds_read_b64_tr_b16 v[60:61], v233 offset:9728\n" \
"v_mfma_f32_16x16x32_bf16 v[140:143], v[24:27], v[208:211], v[140:143]\n" \
"ds_read_b64_tr_b16 v[62:63], v233 offset:11776\n" \
"v_mfma_f32_16x16x32_bf16 v[140:143], v[28:31], v[212:215], v[140:143]\n" \
"s_waitcnt lgkmcnt(3)\n" \
"global_load_lds_dwordx4 v228, s[42:43] offset:-2048\n" \
"v_mfma_f32_16x16x32_bf16 v[160:163], v[0:3], v[216:219], v[160:163]\n" \
"v_add_u32 v230, 0xffff0000, v230\n" \
"v_add_u32 v231, 0xffff0000, v231\n" \
"v_mfma_f32_16x16x32_bf16 v[160:163], v[4:7], v[220:223], v[160:163]\n" \
"v_add_u32 v232, 0x4000, v232\n" \
"v_add_u32 v233, 0x4000, v233\n" \
"v_mfma_f32_16x16x32_bf16 v[164:167], v[8:11], v[216:219], v[164:167]\n" \
"v_mfma_f32_16x16x32_bf16 v[164:167], v[12:15], v[220:223], v[164:167]\n" \
"global_load_lds_dwordx4 v229, s[42:43] offset:-1024\n" \
"v_mfma_f32_16x16x32_bf16 v[168:171], v[16:19], v[216:219], v[168:171]\n" \
"v_mfma_f32_16x16x32_bf16 v[168:171], v[20:23], v[220:223], v[168:171]\n" \
"v_mfma_f32_16x16x32_bf16 v[172:175], v[24:27], v[216:219], v[172:175]\n" \
"s_waitcnt vmcnt(21) lgkmcnt(0)\n" \
"v_mfma_f32_16x16x32_bf16 v[172:175], v[28:31], v[220:223], v[172:175]\n" \
"s_barrier\n" \
"v_mfma_f32_16x16x32_bf16 v[80:83], v[32:35], v[192:195], v[80:83]\n" \
"s_waitcnt vmcnt(18)\n" \
"ds_read_b64_tr_b16 v[0:1], v232 offset:0\n" \
"v_cmp_ne_u32 vcc, s60, v244\n" \
"v_mfma_f32_16x16x32_bf16 v[80:83], v[36:39], v[196:199], v[80:83]\n" \
"ds_read_b64_tr_b16 v[2:3], v232 offset:2048\n" \
"v_mfma_f32_16x16x32_bf16 v[84:87], v[40:43], v[192:195], v[84:87]\n" \
"s_add_u32 m0, s87, 135168\n" \
"ds_read_b64_tr_b16 v[4:5], v232 offset:8192\n" \
"v_mfma_f32_16x16x32_bf16 v[84:87], v[44:47], v[196:199], v[84:87]\n" \
"s_and_b32 vcc_hi, vcc_lo, 255\n" \
"s_cbranch_scc1 Lpl1_%=\n" \
"Lpr1_%=:\n" \
"global_load_lds_dwordx4 v224, s[48:49] offset:-4096\n" \
"v_mfma_f32_16x16x32_bf16 v[88:91], v[48:51], v[192:195], v[88:91]\n" \
"s_add_u32 s79, s91, 12288\n" \
"ds_read_b64_tr_b16 v[6:7], v232 offset:10240\n" \
"v_mfma_f32_16x16x32_bf16 v[88:91], v[52:55], v[196:199], v[88:91]\n" \
"ds_read_b64_tr_b16 v[8:9], v233 offset:0\n" \
"v_mfma_f32_16x16x32_bf16 v[92:95], v[56:59], v[192:195], v[92:95]\n" \
"ds_read_b64_tr_b16 v[10:11], v233 offset:2048\n" \
"v_mfma_f32_16x16x32_bf16 v[92:95], v[60:63], v[196:199], v[92:95]\n" \
"global_load_lds_dwordx4 v225, s[48:49] offset:-3072\n" \
"v_mfma_f32_16x16x32_bf16 v[112:115], v[32:35], v[200:203], v[112:115]\n" \
"ds_read_b128 v[192:195], v230 offset:0\n" \
"v_mfma_f32_16x16x32_bf16 v[112:115], v[36:39], v[204:207], v[112:115]\n" \
"ds_read_b128 v[196:199], v231 offset:0\n" \
"v_mfma_f32_16x16x32_bf16 v[116:119], v[40:43], v[200:203], v[116:119]\n" \
"ds_read_b64_tr_b16 v[12:13], v233 offset:8192\n" \
"v_mfma_f32_16x16x32_bf16 v[116:119], v[44:47], v[204:207], v[116:119]\n" \
"global_load_lds_dwordx4 v226, s[48:49] offset:-2048\n" \
"v_mfma_f32_16x16x32_bf16 v[120:123], v[48:51], v[200:203], v[120:123]\n" \
"ds_read_b64_tr_b16 v[14:15], v233 offset:10240\n" \
"v_mfma_f32_16x16x32_bf16 v[120:123], v[52:55], v[204:207], v[120:123]\n" \
"ds_read_b64_tr_b16 v[16:17], v232 offset:512\n" \
"v_mfma_f32_16x16x32_bf16 v[124:127], v[56:59], v[200:203], v[124:127]\n" \
"ds_read_b64_tr_b16 v[18:19], v232 offset:2560\n" \
"v_mfma_f32_16x16x32_bf16 v[124:127], v[60:63], v[204:207], v[124:127]\n" \
"global_load_lds_dwordx4 v227, s[48:49] offset:-1024\n" \
"v_mfma_f32_16x16x32_bf16 v[144:147], v[32:35], v[208:211], v[144:147]\n" \
"ds_read_b128 v[200:203], v230 offset:2048\n" \
"v_mfma_f32_16x16x32_bf16 v[144:147], v[36:39], v[212:215], v[144:147]\n" \
"ds_read_b128 v[204:207], v231 offset:2048\n" \
"v_mfma_f32_16x16x32_bf16 v[148:151], v[40:43], v[208:211], v[148:151]\n" \
"ds_read_b64_tr_b16 v[20:21], v232 offset:8704\n" \
"v_mfma_f32_16x16x32_bf16 v[148:151], v[44:47], v[212:215], v[148:151]\n" \
"global_load_lds_dwordx4 v224, s[46:47] offset:0\n" \
"v_mfma_f32_16x16x32_bf16 v[152:155], v[48:51], v[208:211], v[152:155]\n" \
"ds_read_b64_tr_b16 v[22:23], v232 offset:10752\n" \
"v_mfma_f32_16x16x32_bf16 v[152:155], v[52:55], v[212:215], v[152:155]\n" \
"ds_read_b64_tr_b16 v[24:25], v233 offset:512\n" \
"v_mfma_f32_16x16x32_bf16 v[156:159], v[56:59], v[208:211], v[156:159]\n" \
"ds_read_b64_tr_b16 v[26:27], v233 offset:2560\n" \
"v_mfma_f32_16x16x32_bf16 v[156:159], v[60:63], v[212:215], v[156:159]\n" \
"global_load_lds_dwordx4 v225, s[46:47] offset:1024\n" \
"v_mfma_f32_16x16x32_bf16 v[176:179], v[32:35], v[216:219], v[176:179]\n" \
"ds_read_b128 v[208:211], v230 offset:4096\n" \
"v_mfma_f32_16x16x32_bf16 v[176:179], v[36:39], v[220:223], v[176:179]\n" \
"ds_read_b128 v[212:215], v231 offset:4096\n" \
"v_mfma_f32_16x16x32_bf16 v[180:183], v[40:43], v[216:219], v[180:183]\n" \
"ds_read_b64_tr_b16 v[28:29], v233 offset:8704\n" \
"v_mfma_f32_16x16x32_bf16 v[180:183], v[44:47], v[220:223], v[180:183]\n" \
"global_load_lds_dwordx4 v226, s[46:47] offset:2048\n" \
"v_mfma_f32_16x16x32_bf16 v[184:187], v[48:51], v[216:219], v[184:187]\n" \
"ds_read_b64_tr_b16 v[30:31], v233 offset:10752\n" \
"v_mfma_f32_16x16x32_bf16 v[184:187], v[52:55], v[220:223], v[184:187]\n" \
"v_mfma_f32_16x16x32_bf16 v[188:191], v[56:59], v[216:219], v[188:191]\n" \
"v_mfma_f32_16x16x32_bf16 v[188:191], v[60:63], v[220:223], v[188:191]\n" \
"s_waitcnt lgkmcnt(1)\n" \
"global_load_lds_dwordx4 v227, s[46:47] offset:3072\n" \
"v_mfma_f32_16x16x32_bf16 v[64:67], v[0:3], v[192:195], v[64:67]\n" \
"ds_read_b128 v[216:219], v230 offset:6144\n" \
"v_mfma_f32_16x16x32_bf16 v[64:67], v[4:7], v[196:199], v[64:67]\n" \
"ds_read_b128 v[220:223], v231 offset:6144\n" \
"v_mfma_f32_16x16x32_bf16 v[68:71], v[8:11], v[192:195], v[68:71]\n" \
"ds_read_b64_tr_b16 v[32:33], v232 offset:1024\n" \
"v_mfma_f32_16x16x32_bf16 v[68:71], v[12:15], v[196:199], v[68:71]\n" \
"buffer_store_dwordx4 a[104:107], v249, s[56:59], s90 offen offset:2304 sc1\n" \
"v_mfma_f32_16x16x32_bf16 v[72:75], v[16:19], v[192:195], v[72:75]\n" \
"s_add_u32 s90, s89, 278528\n" \
"ds_read_b64_tr_b16 v[34:35], v232 offset:3072\n" \
"v_mfma_f32_16x16x32_bf16 v[72:75], v[20:23], v[196:199], v[72:75]\n" \
"s_waitcnt lgkmcnt(3)\n" \
"ds_read_b64_tr_b16 v[36:37], v232 offset:9216\n" \
"v_mfma_f32_16x16x32_bf16 v[76:79], v[24:27], v[192:195], v[76:79]\n" \
"ds_read_b128 a[104:107], v254\n" \
"ds_read_b64_tr_b16 v[38:39], v232 offset:11264\n" \
"v_mfma_f32_16x16x32_bf16 v[76:79], v[28:31], v[196:199], v[76:79]\n" \
"buffer_store_dwordx4 a[80:83], v248, s[56:59], s90 offen offset:0 sc1\n" \
"ds_read_b128 a[108:111], v254\n" \
"v_mfma_f32_16x16x32_bf16 v[96:99], v[0:3], v[200:203], v[96:99]\n" \
"ds_read_b64_tr_b16 v[40:41], v233 offset:1024\n" \
"v_mfma_f32_16x16x32_bf16 v[96:99], v[4:7], v[204:207], v[96:99]\n" \
"ds_read_b64_tr_b16 v[42:43], v233 offset:3072\n" \
"v_mfma_f32_16x16x32_bf16 v[100:103], v[8:11], v[200:203], v[100:103]\n" \
"ds_read_b128 a[80:83], v254\n" \
"ds_read_b64_tr_b16 v[44:45], v233 offset:9216\n" \
"v_mfma_f32_16x16x32_bf16 v[100:103], v[12:15], v[204:207], v[100:103]\n" \
"buffer_store_dwordx4 a[84:87], v248, s[56:59], s90 offen offset:1024 sc1\n" \
"ds_read_b128 a[112:115], v254\n" \
"v_mfma_f32_16x16x32_bf16 v[104:107], v[16:19], v[200:203], v[104:107]\n" \
"ds_read_b64_tr_b16 v[46:47], v233 offset:11264\n" \
"v_mfma_f32_16x16x32_bf16 v[104:107], v[20:23], v[204:207], v[104:107]\n" \
"ds_read_b64_tr_b16 v[48:49], v232 offset:1536\n" \
"v_mfma_f32_16x16x32_bf16 v[108:111], v[24:27], v[200:203], v[108:111]\n" \
"s_add_u32 m0, s95, 0\n" \
"ds_read_b128 a[84:87], v254\n" \
"ds_read_b64_tr_b16 v[50:51], v232 offset:3584\n" \
"v_mfma_f32_16x16x32_bf16 v[108:111], v[28:31], v[204:207], v[108:111]\n" \
"ds_read_b128 a[116:119], v254\n" \
"global_load_lds_dwordx4 v250, s[36:37] offset:0 sc1\n" \
"v_mfma_f32_16x16x32_bf16 v[128:131], v[0:3], v[208:211], v[128:131]\n" \
"ds_read_b64_tr_b16 v[52:53], v232 offset:9728\n" \
"v_mfma_f32_16x16x32_bf16 v[128:131], v[4:7], v[212:215], v[128:131]\n" \
"ds_read_b64_tr_b16 v[54:55], v232 offset:11776\n" \
"v_mfma_f32_16x16x32_bf16 v[132:135], v[8:11], v[208:211], v[132:135]\n" \
"ds_read_b64_tr_b16 v[56:57], v233 offset:1536\n" \
"v_mfma_f32_16x16x32_bf16 v[132:135], v[12:15], v[212:215], v[132:135]\n" \
"global_load_lds_dwordx4 v250, s[36:37] offset:1024 sc1\n" \
"v_mfma_f32_16x16x32_bf16 v[136:139], v[16:19], v[208:211], v[136:139]\n" \
"ds_read_b64_tr_b16 v[58:59], v233 offset:3584\n" \
"v_mfma_f32_16x16x32_bf16 v[136:139], v[20:23], v[212:215], v[136:139]\n" \
"ds_read_b64_tr_b16 v[60:61], v233 offset:9728\n" \
"v_mfma_f32_16x16x32_bf16 v[140:143], v[24:27], v[208:211], v[140:143]\n" \
"ds_read_b64_tr_b16 v[62:63], v233 offset:11776\n" \
"v_mfma_f32_16x16x32_bf16 v[140:143], v[28:31], v[212:215], v[140:143]\n" \
"s_waitcnt lgkmcnt(3)\n" \
"global_load_lds_dwordx4 v250, s[36:37] offset:2048 sc1\n" \
"v_mfma_f32_16x16x32_bf16 v[160:163], v[0:3], v[216:219], v[160:163]\n" \
"v_add_u32 v230, 0x8000, v230\n" \
"v_add_u32 v231, 0x8000, v231\n" \
"v_mfma_f32_16x16x32_bf16 v[160:163], v[4:7], v[220:223], v[160:163]\n" \
"v_add_u32 v232, 0x4000, v232\n" \
"v_add_u32 v233, 0x4000, v233\n" \
"v_mfma_f32_16x16x32_bf16 v[164:167], v[8:11], v[216:219], v[164:167]\n" \
"v_mfma_f32_16x16x32_bf16 v[164:167], v[12:15], v[220:223], v[164:167]\n" \
"global_load_lds_dwordx4 v250, s[36:37] offset:3072 sc1\n" \
"v_mfma_f32_16x16x32_bf16 v[168:171], v[16:19], v[216:219], v[168:171]\n" \
"s_add_u32 s36, s36, 17408\n" \
"v_mfma_f32_16x16x32_bf16 v[168:171], v[20:23], v[220:223], v[168:171]\n" \
"v_mfma_f32_16x16x32_bf16 v[172:175], v[24:27], v[216:219], v[172:175]\n" \
"s_waitcnt vmcnt(21) lgkmcnt(0)\n" \
"v_mfma_f32_16x16x32_bf16 v[172:175], v[28:31], v[220:223], v[172:175]\n" \
"s_barrier\n" \
"v_mfma_f32_16x16x32_bf16 v[80:83], v[32:35], v[192:195], v[80:83]\n" \
"ds_read_b64_tr_b16 v[0:1], v232 offset:0\n" \
"v_mfma_f32_16x16x32_bf16 v[80:83], v[36:39], v[196:199], v[80:83]\n" \
"ds_read_b64_tr_b16 v[2:3], v232 offset:2048\n" \
"v_mfma_f32_16x16x32_bf16 v[84:87], v[40:43], v[192:195], v[84:87]\n" \
"s_add_u32 m0, s95, 16384\n" \
"ds_read_b64_tr_b16 v[4:5], v232 offset:8192\n" \
"v_mfma_f32_16x16x32_bf16 v[84:87], v[44:47], v[196:199], v[84:87]\n" \
"global_load_lds_dwordx4 v250, s[36:37] offset:0 sc1\n" \
"v_mfma_f32_16x16x32_bf16 v[88:91], v[48:51], v[192:195], v[88:91]\n" \
"ds_read_b64_tr_b16 v[6:7], v232 offset:10240\n" \
"v_mfma_f32_16x16x32_bf16 v[88:91], v[52:55], v[196:199], v[88:91]\n" \
"ds_read_b64_tr_b16 v[8:9], v233 offset:0\n" \
"v_mfma_f32_16x16x32_bf16 v[92:95], v[56:59], v[192:195], v[92:95]\n" \
"ds_read_b64_tr_b16 v[10:11], v233 offset:2048\n" \
"v_mfma_f32_16x16x32_bf16 v[92:95], v[60:63], v[196:199], v[92:95]\n" \
"global_load_lds_dwordx4 v250, s[36:37] offset:1024 sc1\n" \
"v_mfma_f32_16x16x32_bf16 v[112:115], v[32:35], v[200:203], v[112:115]\n" \
"ds_read_b128 v[192:195], v230 offset:0\n" \
"v_mfma_f32_16x16x32_bf16 v[112:115], v[36:39], v[204:207], v[112:115]\n" \
"ds_read_b128 v[196:199], v231 offset:0\n" \
"v_mfma_f32_16x16x32_bf16 v[116:119], v[40:43], v[200:203], v[116:119]\n" \
"ds_read_b64_tr_b16 v[12:13], v233 offset:8192\n" \
"v_mfma_f32_16x16x32_bf16 v[116:119], v[44:47], v[204:207], v[116:119]\n" \
"global_load_lds_dwordx4 v250, s[36:37] offset:2048 sc1\n" \
"v_mfma_f32_16x16x32_bf16 v[120:123], v[48:51], v[200:203], v[120:123]\n" \
"ds_read_b64_tr_b16 v[14:15], v233 offset:10240\n" \
"v_mfma_f32_16x16x32_bf16 v[120:123], v[52:55], v[204:207], v[120:123]\n" \
"ds_read_b64_tr_b16 v[16:17], v232 offset:512\n" \
"v_mfma_f32_16x16x32_bf16 v[124:127], v[56:59], v[200:203], v[124:127]\n" \
"ds_read_b64_tr_b16 v[18:19], v232 offset:2560\n" \
"v_mfma_f32_16x16x32_bf16 v[124:127], v[60:63], v[204:207], v[124:127]\n" \
"global_load_lds_dwordx4 v250, s[36:37] offset:3072 sc1\n" \
"v_mfma_f32_16x16x32_bf16 v[144:147], v[32:35], v[208:211], v[144:147]\n" \
"ds_read_b128 v[200:203], v230 offset:2048\n" \
"s_add_u32 s36, s36, 17408\n" \
"v_mfma_f32_16x16x32_bf16 v[144:147], v[36:39], v[212:215], v[144:147]\n" \
"ds_read_b128 v[204:207], v231 offset:2048\n" \
"v_mfma_f32_16x16x32_bf16 v[148:151], v[40:43], v[208:211], v[148:151]\n" \
"ds_read_b64_tr_b16 v[20:21], v232 offset:8704\n" \
"v_mfma_f32_16x16x32_bf16 v[148:151], v[44:47], v[212:215], v[148:151]\n" \
"buffer_store_dwordx4 a[88:91], v248, s[56:59], s90 offen offset:2048 sc1\n" \
"v_mfma_f32_16x16x32_bf16 v[152:155], v[48:51], v[208:211], v[152:155]\n" \
"ds_read_b64_tr_b16 v[22:23], v232 offset:10752\n" \
"v_mfma_f32_16x16x32_bf16 v[152:155], v[52:55], v[212:215], v[152:155]\n" \
"ds_read_b64_tr_b16 v[24:25], v233 offset:512\n" \
"v_mfma_f32_16x16x32_bf16 v[156:159], v[56:59], v[208:211], v[156:159]\n" \
"ds_read_b128 a[88:91], v254\n" \
"ds_read_b64_tr_b16 v[26:27], v233 offset:2560\n" \
"v_mfma_f32_16x16x32_bf16 v[156:159], v[60:63], v[212:215], v[156:159]\n" \
"buffer_store_dwordx4 a[92:95], v248, s[56:59], s90 offen offset:3072 sc1\n" \
"ds_read_b128 a[120:123], v254\n" \
"v_mfma_f32_16x16x32_bf16 v[176:179], v[32:35], v[216:219], v[176:179]\n" \
"ds_read_b128 v[208:211], v230 offset:4096\n" \
"s_add_u32 s90, s88, 417792\n" \
"v_mfma_f32_16x16x32_bf16 v[176:179], v[36:39], v[220:223], v[176:179]\n" \
"ds_read_b128 v[212:215], v231 offset:4096\n" \
"v_mfma_f32_16x16x32_bf16 v[180:183], v[40:43], v[216:219], v[180:183]\n" \
"ds_read_b128 a[92:95], v254\n" \
"ds_read_b64_tr_b16 v[28:29], v233 offset:8704\n" \
"v_mfma_f32_16x16x32_bf16 v[180:183], v[44:47], v[220:223], v[180:183]\n" \
"buffer_store_dwordx4 a[0:3], v249, s[56:59], s90 offen offset:0 sc1\n" \
"ds_read_b128 a[124:127], v254\n" \
"v_mfma_f32_16x16x32_bf16 v[184:187], v[48:51], v[216:219], v[184:187]\n" \
"ds_read_b64_tr_b16 v[30:31], v233 offset:10752\n" \
"v_mfma_f32_16x16x32_bf16 v[184:187], v[52:55], v[220:223], v[184:187]\n" \
"v_mfma_f32_16x16x32_bf16 v[188:191], v[56:59], v[216:219], v[188:191]\n" \
"ds_read_b128 a[0:3], v254\n" \
"v_mfma_f32_16x16x32_bf16 v[188:191], v[60:63], v[220:223], v[188:191]\n" \
"s_waitcnt lgkmcnt(3)\n" \
"ds_read_b128 a[4:7], v254\n" \
"v_mfma_f32_16x16x32_bf16 v[64:67], v[0:3], v[192:195], v[64:67]\n" \
"ds_read_b128 v[216:219], v230 offset:6144\n" \
"v_mfma_f32_16x16x32_bf16 v[64:67], v[4:7], v[196:199], v[64:67]\n" \
"ds_read_b128 v[220:223], v231 offset:6144\n" \
"v_mfma_f32_16x16x32_bf16 v[68:71], v[8:11], v[192:195], v[68:71]\n" \
"ds_read_b64_tr_b16 v[32:33], v232 offset:1024\n" \
"v_mfma_f32_16x16x32_bf16 v[68:71], v[12:15], v[196:199], v[68:71]\n" \
"v_mfma_f32_16x16x32_bf16 v[72:75], v[16:19], v[192:195], v[72:75]\n" \
"ds_read_b64_tr_b16 v[34:35], v232 offset:3072\n" \
"v_mfma_f32_16x16x32_bf16 v[72:75], v[20:23], v[196:199], v[72:75]\n" \
"s_waitcnt lgkmcnt(3)\n" \
"ds_read_b64_tr_b16 v[36:37], v232 offset:9216\n" \
"v_mfma_f32_16x16x32_bf16 v[76:79], v[24:27], v[192:195], v[76:79]\n" \
"ds_read_b64_tr_b16 v[38:39], v232 offset:11264\n" \
"v_mfma_f32_16x16x32_bf16 v[76:79], v[28:31], v[196:199], v[76:79]\n" \
"v_mfma_f32_16x16x32_bf16 v[96:99], v[0:3], v[200:203], v[96:99]\n" \
"ds_read_b64_tr_b16 v[40:41], v233 offset:1024\n" \
"v_mfma_f32_16x16x32_bf16 v[96:99], v[4:7], v[204:207], v[96:99]\n" \
"ds_read_b64_tr_b16 v[42:43], v233 offset:3072\n" \
"v_mfma_f32_16x16x32_bf16 v[100:103], v[8:11], v[200:203], v[100:103]\n" \
"ds_read_b64_tr_b16 v[44:45], v233 offset:9216\n" \
"v_mfma_f32_16x16x32_bf16 v[100:103], v[12:15], v[204:207], v[100:103]\n" \
"v_mfma_f32_16x16x32_bf16 v[104:107], v[16:19], v[200:203], v[104:107]\n" \
"ds_read_b64_tr_b16 v[46:47], v233 offset:11264\n" \
"v_mfma_f32_16x16x32_bf16 v[104:107], v[20:23], v[204:207], v[104:107]\n" \
"ds_read_b64_tr_b16 v[48:49], v232 offset:1536\n" \
"v_mfma_f32_16x16x32_bf16 v[108:111], v[24:27], v[200:203], v[108:111]\n" \
"ds_read_b64_tr_b16 v[50:51], v232 offset:3584\n" \
"v_mfma_f32_16x16x32_bf16 v[108:111], v[28:31], v[204:207], v[108:111]\n" \
"v_mfma_f32_16x16x32_bf16 v[128:131], v[0:3], v[208:211], v[128:131]\n" \
"ds_read_b64_tr_b16 v[52:53], v232 offset:9728\n" \
"v_mfma_f32_16x16x32_bf16 v[128:131], v[4:7], v[212:215], v[128:131]\n" \
"ds_read_b64_tr_b16 v[54:55], v232 offset:11776\n" \
"v_mfma_f32_16x16x32_bf16 v[132:135], v[8:11], v[208:211], v[132:135]\n" \
"ds_read_b64_tr_b16 v[56:57], v233 offset:1536\n" \
"v_mfma_f32_16x16x32_bf16 v[132:135], v[12:15], v[212:215], v[132:135]\n" \
"v_mfma_f32_16x16x32_bf16 v[136:139], v[16:19], v[208:211], v[136:139]\n" \
"ds_read_b64_tr_b16 v[58:59], v233 offset:3584\n" \
"v_mfma_f32_16x16x32_bf16 v[136:139], v[20:23], v[212:215], v[136:139]\n" \
"ds_read_b64_tr_b16 v[60:61], v233 offset:9728\n" \
"v_mfma_f32_16x16x32_bf16 v[140:143], v[24:27], v[208:211], v[140:143]\n" \
"ds_read_b64_tr_b16 v[62:63], v233 offset:11776\n" \
"v_mfma_f32_16x16x32_bf16 v[140:143], v[28:31], v[212:215], v[140:143]\n" \
"s_waitcnt lgkmcnt(3)\n" \
"v_mfma_f32_16x16x32_bf16 v[160:163], v[0:3], v[216:219], v[160:163]\n" \
"v_add_u32 v230, 0x8000, v230\n" \
"v_add_u32 v231, 0x8000, v231\n" \
"v_mfma_f32_16x16x32_bf16 v[160:163], v[4:7], v[220:223], v[160:163]\n" \
"v_add_u32 v232, 0x4000, v232\n" \
"v_add_u32 v233, 0x4000, v233\n" \
"v_mfma_f32_16x16x32_bf16 v[164:167], v[8:11], v[216:219], v[164:167]\n" \
"v_mfma_f32_16x16x32_bf16 v[164:167], v[12:15], v[220:223], v[164:167]\n" \
"v_mfma_f32_16x16x32_bf16 v[168:171], v[16:19], v[216:219], v[168:171]\n" \
"v_mfma_f32_16x16x32_bf16 v[168:171], v[20:23], v[220:223], v[168:171]\n" \
"v_mfma_f32_16x16x32_bf16 v[172:175], v[24:27], v[216:219], v[172:175]\n" \
"s_waitcnt vmcnt(14) lgkmcnt(0)\n" \
"v_mfma_f32_16x16x32_bf16 v[172:175], v[28:31], v[220:223], v[172:175]\n" \
"s_barrier\n" \
"v_mfma_f32_16x16x32_bf16 v[80:83], v[32:35], v[192:195], v[80:83]\n" \
"ds_read_b64_tr_b16 v[0:1], v232 offset:0\n" \
"v_mfma_f32_16x16x32_bf16 v[80:83], v[36:39], v[196:199], v[80:83]\n" \
"ds_read_b64_tr_b16 v[2:3], v232 offset:2048\n" \
"v_mfma_f32_16x16x32_bf16 v[84:87], v[40:43], v[192:195], v[84:87]\n" \
"s_add_u32 m0, s95, 32768\n" \
"ds_read_b64_tr_b16 v[4:5], v232 offset:8192\n" \
"v_mfma_f32_16x16x32_bf16 v[84:87], v[44:47], v[196:199], v[84:87]\n" \
"global_load_lds_dwordx4 v250, s[36:37] offset:0 sc1\n" \
"v_mfma_f32_16x16x32_bf16 v[88:91], v[48:51], v[192:195], v[88:91]\n" \
"ds_read_b64_tr_b16 v[6:7], v232 offset:10240\n" \
"v_mfma_f32_16x16x32_bf16 v[88:91], v[52:55], v[196:199], v[88:91]\n" \
"ds_read_b64_tr_b16 v[8:9], v233 offset:0\n" \
"v_mfma_f32_16x16x32_bf16 v[92:95], v[56:59], v[192:195], v[92:95]\n" \
"ds_read_b64_tr_b16 v[10:11], v233 offset:2048\n" \
"v_mfma_f32_16x16x32_bf16 v[92:95], v[60:63], v[196:199], v[92:95]\n" \
"global_load_lds_dwordx4 v250, s[36:37] offset:1024 sc1\n" \
"v_mfma_f32_16x16x32_bf16 v[112:115], v[32:35], v[200:203], v[112:115]\n" \
"ds_read_b128 v[192:195], v230 offset:0\n" \
"v_mfma_f32_16x16x32_bf16 v[112:115], v[36:39], v[204:207], v[112:115]\n" \
"ds_read_b128 v[196:199], v231 offset:0\n" \
"v_mfma_f32_16x16x32_bf16 v[116:119], v[40:43], v[200:203], v[116:119]\n" \
"ds_read_b64_tr_b16 v[12:13], v233 offset:8192\n" \
"v_mfma_f32_16x16x32_bf16 v[116:119], v[44:47], v[204:207], v[116:119]\n" \
"global_load_lds_dwordx4 v250, s[36:37] offset:2048 sc1\n" \
"v_mfma_f32_16x16x32_bf16 v[120:123], v[48:51], v[200:203], v[120:123]\n" \
"ds_read_b64_tr_b16 v[14:15], v233 offset:10240\n" \
"v_mfma_f32_16x16x32_bf16 v[120:123], v[52:55], v[204:207], v[120:123]\n" \
"ds_read_b64_tr_b16 v[16:17], v232 offset:512\n" \
"v_mfma_f32_16x16x32_bf16 v[124:127], v[56:59], v[200:203], v[124:127]\n" \
"ds_read_b64_tr_b16 v[18:19], v232 offset:2560\n" \
"v_mfma_f32_16x16x32_bf16 v[124:127], v[60:63], v[204:207], v[124:127]\n" \
"global_load_lds_dwordx4 v250, s[36:37] offset:3072 sc1\n" \
"v_mfma_f32_16x16x32_bf16 v[144:147], v[32:35], v[208:211], v[144:147]\n" \
"ds_read_b128 v[200:203], v230 offset:2048\n" \
"s_add_u32 s36, s36, 17408\n" \
"v_mfma_f32_16x16x32_bf16 v[144:147], v[36:39], v[212:215], v[144:147]\n" \
"ds_read_b128 v[204:207], v231 offset:2048\n" \
"v_mfma_f32_16x16x32_bf16 v[148:151], v[40:43], v[208:211], v[148:151]\n" \
"ds_read_b64_tr_b16 v[20:21], v232 offset:8704\n" \
"v_mfma_f32_16x16x32_bf16 v[148:151], v[44:47], v[212:215], v[148:151]\n" \
"buffer_store_dwordx4 a[32:35], v249, s[56:59], s90 offen offset:256 sc1\n" \
"v_mfma_f32_16x16x32_bf16 v[152:155], v[48:51], v[208:211], v[152:155]\n" \
"ds_read_b64_tr_b16 v[22:23], v232 offset:10752\n" \
"v_mfma_f32_16x16x32_bf16 v[152:155], v[52:55], v[212:215], v[152:155]\n" \
"ds_read_b64_tr_b16 v[24:25], v233 offset:512\n" \
"v_mfma_f32_16x16x32_bf16 v[156:159], v[56:59], v[208:211], v[156:159]\n" \
"ds_read_b128 a[32:35], v254\n" \
"ds_read_b64_tr_b16 v[26:27], v233 offset:2560\n" \
"v_mfma_f32_16x16x32_bf16 v[156:159], v[60:63], v[212:215], v[156:159]\n" \
"buffer_store_dwordx4 a[8:11], v249, s[56:59], s90 offen offset:2048 sc1\n" \
"ds_read_b128 a[36:39], v254\n" \
"v_mfma_f32_16x16x32_bf16 v[176:179], v[32:35], v[216:219], v[176:179]\n" \
"ds_read_b128 v[208:211], v230 offset:4096\n" \
"v_mfma_f32_16x16x32_bf16 v[176:179], v[36:39], v[220:223], v[176:179]\n" \
"ds_read_b128 v[212:215], v231 offset:4096\n" \
"v_mfma_f32_16x16x32_bf16 v[180:183], v[40:43], v[216:219], v[180:183]\n" \
"ds_read_b128 a[8:11], v254\n" \
"ds_read_b64_tr_b16 v[28:29], v233 offset:8704\n" \
"v_mfma_f32_16x16x32_bf16 v[180:183], v[44:47], v[220:223], v[180:183]\n" \
"buffer_store_dwordx4 a[40:43], v249, s[56:59], s90 offen offset:2304 sc1\n" \
"ds_read_b128 a[12:15], v254\n" \
"v_mfma_f32_16x16x32_bf16 v[184:187], v[48:51], v[216:219], v[184:187]\n" \
"ds_read_b64_tr_b16 v[30:31], v233 offset:10752\n" \
"v_mfma_f32_16x16x32_bf16 v[184:187], v[52:55], v[220:223], v[184:187]\n" \
"v_mfma_f32_16x16x32_bf16 v[188:191], v[56:59], v[216:219], v[188:191]\n" \
"ds_read_b128 a[40:43], v254\n" \
"v_mfma_f32_16x16x32_bf16 v[188:191], v[60:63], v[220:223], v[188:191]\n" \
"s_waitcnt lgkmcnt(3)\n" \
"ds_read_b128 a[44:47], v254\n" \
"v_mfma_f32_16x16x32_bf16 v[64:67], v[0:3], v[192:195], v[64:67]\n" \
"ds_read_b128 v[216:219], v230 offset:6144\n" \
"v_mfma_f32_16x16x32_bf16 v[64:67], v[4:7], v[196:199], v[64:67]\n" \
"ds_read_b128 v[220:223], v231 offset:6144\n" \
"v_mfma_f32_16x16x32_bf16 v[68:71], v[8:11], v[192:195], v[68:71]\n" \
"ds_read_b64_tr_b16 v[32:33], v232 offset:1024\n" \
"v_mfma_f32_16x16x32_bf16 v[68:71], v[12:15], v[196:199], v[68:71]\n" \
"v_mfma_f32_16x16x32_bf16 v[72:75], v[16:19], v[192:195], v[72:75]\n" \
"ds_read_b64_tr_b16 v[34:35], v232 offset:3072\n" \
"v_mfma_f32_16x16x32_bf16 v[72:75], v[20:23], v[196:199], v[72:75]\n" \
"s_waitcnt lgkmcnt(3)\n" \
"ds_read_b64_tr_b16 v[36:37], v232 offset:9216\n" \
"v_mfma_f32_16x16x32_bf16 v[76:79], v[24:27], v[192:195], v[76:79]\n" \
"ds_read_b64_tr_b16 v[38:39], v232 offset:11264\n" \
"v_cvt_pk_bf16_f32 v64, v64, v65\n" \
"v_cvt_pk_bf16_f32 v65, v66, v67\n" \
"v_cvt_pk_bf16_f32 v66, v68, v69\n" \
"v_cvt_pk_bf16_f32 v67, v70, v71\n" \
"v_mfma_f32_16x16x32_bf16 v[76:79], v[28:31], v[196:199], v[76:79]\n" \
"v_accvgpr_write_b32 a128, v64\n" \
"v_accvgpr_write_b32 a129, v65\n" \
"v_mfma_f32_16x16x32_bf16 v[96:99], v[0:3], v[200:203], v[96:99]\n" \
"ds_read_b64_tr_b16 v[40:41], v233 offset:1024\n" \
"v_accvgpr_write_b32 a130, v66\n" \
"v_mfma_f32_16x16x32_bf16 v[96:99], v[4:7], v[204:207], v[96:99]\n" \
"ds_read_b64_tr_b16 v[42:43], v233 offset:3072\n" \
"v_accvgpr_write_b32 a131, v67\n" \
"v_mfma_f32_16x16x32_bf16 v[100:103], v[8:11], v[200:203], v[100:103]\n" \
"ds_read_b64_tr_b16 v[44:45], v233 offset:9216\n" \
"v_cvt_pk_bf16_f32 v72, v72, v73\n" \
"v_cvt_pk_bf16_f32 v73, v74, v75\n" \
"v_cvt_pk_bf16_f32 v74, v76, v77\n" \
"v_cvt_pk_bf16_f32 v75, v78, v79\n" \
"v_mfma_f32_16x16x32_bf16 v[100:103], v[12:15], v[204:207], v[100:103]\n" \
"v_accvgpr_write_b32 a132, v72\n" \
"v_accvgpr_write_b32 a133, v73\n" \
"v_mfma_f32_16x16x32_bf16 v[104:107], v[16:19], v[200:203], v[104:107]\n" \
"ds_read_b64_tr_b16 v[46:47], v233 offset:11264\n" \
"v_accvgpr_write_b32 a134, v74\n" \
"v_mfma_f32_16x16x32_bf16 v[104:107], v[20:23], v[204:207], v[104:107]\n" \
"ds_read_b64_tr_b16 v[48:49], v232 offset:1536\n" \
"v_accvgpr_write_b32 a135, v75\n" \
"v_mfma_f32_16x16x32_bf16 v[108:111], v[24:27], v[200:203], v[108:111]\n" \
"ds_read_b64_tr_b16 v[50:51], v232 offset:3584\n" \
"v_cvt_pk_bf16_f32 v96, v96, v97\n" \
"v_cvt_pk_bf16_f32 v97, v98, v99\n" \
"v_cvt_pk_bf16_f32 v98, v100, v101\n" \
"v_cvt_pk_bf16_f32 v99, v102, v103\n" \
"v_mfma_f32_16x16x32_bf16 v[108:111], v[28:31], v[204:207], v[108:111]\n" \
"v_accvgpr_write_b32 a144, v96\n" \
"v_accvgpr_write_b32 a145, v97\n" \
"v_mfma_f32_16x16x32_bf16 v[128:131], v[0:3], v[208:211], v[128:131]\n" \
"ds_read_b64_tr_b16 v[52:53], v232 offset:9728\n" \
"v_accvgpr_write_b32 a146, v98\n" \
"v_mfma_f32_16x16x32_bf16 v[128:131], v[4:7], v[212:215], v[128:131]\n" \
"ds_read_b64_tr_b16 v[54:55], v232 offset:11776\n" \
"v_accvgpr_write_b32 a147, v99\n" \
"v_mfma_f32_16x16x32_bf16 v[132:135], v[8:11], v[208:211], v[132:135]\n" \
"ds_read_b64_tr_b16 v[56:57], v233 offset:1536\n" \
"v_cvt_pk_bf16_f32 v104, v104, v105\n" \
"v_cvt_pk_bf16_f32 v105, v106, v107\n" \
"v_cvt_pk_bf16_f32 v106, v108, v109\n" \
"v_cvt_pk_bf16_f32 v107, v110, v111\n" \
"v_mfma_f32_16x16x32_bf16 v[132:135], v[12:15], v[212:215], v[132:135]\n" \
"v_accvgpr_write_b32 a148, v104\n" \
"v_accvgpr_write_b32 a149, v105\n" \
"v_mfma_f32_16x16x32_bf16 v[136:139], v[16:19], v[208:211], v[136:139]\n" \
"ds_read_b64_tr_b16 v[58:59], v233 offset:3584\n" \
"v_accvgpr_write_b32 a150, v106\n" \
"v_mfma_f32_16x16x32_bf16 v[136:139], v[20:23], v[212:215], v[136:139]\n" \
"ds_read_b64_tr_b16 v[60:61], v233 offset:9728\n" \
"v_accvgpr_write_b32 a151, v107\n" \
"v_mfma_f32_16x16x32_bf16 v[140:143], v[24:27], v[208:211], v[140:143]\n" \
"ds_read_b64_tr_b16 v[62:63], v233 offset:11776\n" \
"v_cvt_pk_bf16_f32 v128, v128, v129\n" \
"v_cvt_pk_bf16_f32 v129, v130, v131\n" \
"v_mfma_f32_16x16x32_bf16 v[140:143], v[28:31], v[212:215], v[140:143]\n" \
"s_waitcnt lgkmcnt(3)\n" \
"v_cvt_pk_bf16_f32 v130, v132, v133\n" \
"v_cvt_pk_bf16_f32 v131, v134, v135\n" \
"v_mfma_f32_16x16x32_bf16 v[160:163], v[0:3], v[216:219], v[160:163]\n" \
"v_accvgpr_write_b32 a160, v128\n" \
"v_accvgpr_write_b32 a161, v129\n" \
"v_mfma_f32_16x16x32_bf16 v[160:163], v[4:7], v[220:223], v[160:163]\n" \
"Lpg4933_%=:\n" \
"s_cbranch_execz Lpg6194_%=\n" \
"v_accvgpr_write_b32 a162, v130\n" \
"v_accvgpr_write_b32 a163, v131\n" \
"v_mfma_f32_16x16x32_bf16 v[164:167], v[8:11], v[216:219], v[164:167]\n" \
"v_cvt_pk_bf16_f32 v136, v136, v137\n" \
"v_cvt_pk_bf16_f32 v137, v138, v139\n" \
"v_mfma_f32_16x16x32_bf16 v[164:167], v[12:15], v[220:223], v[164:167]\n" \
"v_cvt_pk_bf16_f32 v138, v140, v141\n" \
"v_cvt_pk_bf16_f32 v139, v142, v143\n" \
"v_mfma_f32_16x16x32_bf16 v[168:171], v[16:19], v[216:219], v[168:171]\n" \
"v_accvgpr_write_b32 a164, v136\n" \
"v_accvgpr_write_b32 a165, v137\n" \
"v_mfma_f32_16x16x32_bf16 v[168:171], v[20:23], v[220:223], v[168:171]\n" \
"v_accvgpr_write_b32 a166, v138\n" \
"v_accvgpr_write_b32 a167, v139\n" \
"v_mfma_f32_16x16x32_bf16 v[172:175], v[24:27], v[216:219], v[172:175]\n" \
"s_waitcnt vmcnt(10) lgkmcnt(0)\n" \
"v_cvt_pk_bf16_f32 v160, v160, v161\n" \
"v_cvt_pk_bf16_f32 v161, v162, v163\n" \
"v_cvt_pk_bf16_f32 v162, v164, v165\n" \
"v_cvt_pk_bf16_f32 v163, v166, v167\n" \
"v_accvgpr_write_b32 a176, v160\n" \
"v_mfma_f32_16x16x32_bf16 v[172:175], v[28:31], v[220:223], v[172:175]\n" \
"s_barrier\n" \
"v_accvgpr_write_b32 a177, v161\n" \
"v_mfma_f32_16x16x32_bf16 v[80:83], v[32:35], v[192:195], v[80:83]\n" \
"s_mov_b32 m0, s74\n" \
"v_accvgpr_write_b32 a178, v162\n" \
"v_accvgpr_write_b32 a179, v163\n" \
"v_mfma_f32_16x16x32_bf16 v[80:83], v[36:39], v[196:199], v[80:83]\n" \
"ds_read_b128 a[192:195], v249 offset:0\n" \
"global_load_lds_dwordx4 v250, s[38:39] offset:0 sc1\n" \
"v_mfma_f32_16x16x32_bf16 v[84:87], v[40:43], v[192:195], v[84:87]\n" \
"ds_read_b128 v[0:3], v254\n" \
"v_cvt_pk_bf16_f32 v168, v168, v169\n" \
"v_cvt_pk_bf16_f32 v169, v170, v171\n" \
"v_cvt_pk_bf16_f32 v170, v172, v173\n" \
"v_cvt_pk_bf16_f32 v171, v174, v175\n" \
"v_accvgpr_write_b32 a180, v168\n" \
"v_accvgpr_write_b32 a181, v169\n" \
"v_mfma_f32_16x16x32_bf16 v[84:87], v[44:47], v[196:199], v[84:87]\n" \
"ds_read_b128 a[196:199], v249 offset:2048\n" \
"global_load_lds_dwordx4 v250, s[38:39] offset:1024 sc1\n" \
"v_accvgpr_write_b32 a182, v170\n" \
"v_mfma_f32_16x16x32_bf16 v[88:91], v[48:51], v[192:195], v[88:91]\n" \
"ds_read_b128 v[4:7], v254\n" \
"v_accvgpr_write_b32 a183, v171\n" \
"v_mfma_f32_16x16x32_bf16 v[88:91], v[52:55], v[196:199], v[88:91]\n" \
"ds_read_b128 a[224:227], v249 offset:4096\n" \
"global_load_lds_dwordx4 v250, s[38:39] offset:2048 sc1\n" \
"v_mfma_f32_16x16x32_bf16 v[92:95], v[56:59], v[192:195], v[92:95]\n" \
"v_cvt_pk_bf16_f32 v80, v80, v81\n" \
"v_cvt_pk_bf16_f32 v81, v82, v83\n" \
"v_cvt_pk_bf16_f32 v82, v84, v85\n" \
"v_cvt_pk_bf16_f32 v83, v86, v87\n" \
"v_accvgpr_write_b32 a136, v80\n" \
"v_accvgpr_write_b32 a137, v81\n" \
"v_mfma_f32_16x16x32_bf16 v[92:95], v[60:63], v[196:199], v[92:95]\n" \
"ds_read_b128 a[228:231], v249 offset:6144\n" \
"global_load_lds_dwordx4 v250, s[38:39] offset:3072 sc1\n" \
"v_accvgpr_write_b32 a138, v82\n" \
"v_mfma_f32_16x16x32_bf16 v[112:115], v[32:35], v[200:203], v[112:115]\n" \
"s_add_u32 s38, s38, 17408\n" \
"v_accvgpr_write_b32 a139, v83\n" \
"s_add_u32 m0, m0, 0x4000\n" \
"v_mfma_f32_16x16x32_bf16 v[112:115], v[36:39], v[204:207], v[112:115]\n" \
"ds_read_b128 a[200:203], v249 offset:256\n" \
"global_load_lds_dwordx4 v250, s[38:39] offset:0 sc1\n" \
"v_mfma_f32_16x16x32_bf16 v[116:119], v[40:43], v[200:203], v[116:119]\n" \
"v_cvt_pk_bf16_f32 v88, v88, v89\n" \
"v_cvt_pk_bf16_f32 v89, v90, v91\n" \
"v_cvt_pk_bf16_f32 v90, v92, v93\n" \
"v_cvt_pk_bf16_f32 v91, v94, v95\n" \
"v_accvgpr_write_b32 a140, v88\n" \
"v_accvgpr_write_b32 a141, v89\n" \
"v_mfma_f32_16x16x32_bf16 v[116:119], v[44:47], v[204:207], v[116:119]\n" \
"ds_read_b128 a[204:207], v249 offset:2304\n" \
"global_load_lds_dwordx4 v250, s[38:39] offset:1024 sc1\n" \
"v_mfma_f32_16x16x32_bf16 v[120:123], v[48:51], v[200:203], v[120:123]\n" \
"v_accvgpr_write_b32 a142, v90\n" \
"v_accvgpr_write_b32 a143, v91\n" \
"v_mfma_f32_16x16x32_bf16 v[120:123], v[52:55], v[204:207], v[120:123]\n" \
"ds_read_b128 a[232:235], v249 offset:4352\n" \
"global_load_lds_dwordx4 v250, s[38:39] offset:2048 sc1\n" \
"v_mfma_f32_16x16x32_bf16 v[124:127], v[56:59], v[200:203], v[124:127]\n" \
"v_cvt_pk_bf16_f32 v112, v112, v113\n" \
"v_cvt_pk_bf16_f32 v113, v114, v115\n" \
"v_cvt_pk_bf16_f32 v114, v116, v117\n" \
"v_cvt_pk_bf16_f32 v115, v118, v119\n" \
"v_accvgpr_write_b32 a152, v112\n" \
"v_mfma_f32_16x16x32_bf16 v[124:127], v[60:63], v[204:207], v[124:127]\n" \
"ds_read_b128 a[236:239], v249 offset:6400\n" \
"global_load_lds_dwordx4 v250, s[38:39] offset:3072 sc1\n" \
"v_mfma_f32_16x16x32_bf16 v[144:147], v[32:35], v[208:211], v[144:147]\n" \
"s_add_u32 s38, s38, 17408\n" \
"s_xor_b32 s74, s74, 0x8000\n" \
"s_add_u32 s90, s89, 417792\n" \
"v_mfma_f32_16x16x32_bf16 v[144:147], v[36:39], v[212:215], v[144:147]\n" \
"ds_read_b128 a[208:211], v249 offset:8192\n" \
"v_accvgpr_write_b32 a153, v113\n" \
"v_mfma_f32_16x16x32_bf16 v[148:151], v[40:43], v[208:211], v[148:151]\n" \
"v_accvgpr_write_b32 a154, v114\n" \
"v_accvgpr_write_b32 a155, v115\n" \
"v_mfma_f32_16x16x32_bf16 v[148:151], v[44:47], v[212:215], v[148:151]\n" \
"buffer_store_dwordx4 a[16:19], v248, s[56:59], s90 offen offset:0 sc1\n" \
"ds_read_b128 a[212:215], v249 offset:10240\n" \
"v_cvt_pk_bf16_f32 v120, v120, v121\n" \
"v_cvt_pk_bf16_f32 v121, v122, v123\n" \
"v_cvt_pk_bf16_f32 v122, v124, v125\n" \
"v_mfma_f32_16x16x32_bf16 v[152:155], v[48:51], v[208:211], v[152:155]\n" \
"v_cvt_pk_bf16_f32 v123, v126, v127\n" \
"v_accvgpr_write_b32 a156, v120\n" \
"v_mfma_f32_16x16x32_bf16 v[152:155], v[52:55], v[212:215], v[152:155]\n" \
"ds_read_b128 a[240:243], v249 offset:12288\n" \
"v_accvgpr_write_b32 a157, v121\n" \
"v_mfma_f32_16x16x32_bf16 v[156:159], v[56:59], v[208:211], v[156:159]\n" \
"ds_read_b128 a[16:19], v254\n" \
"v_accvgpr_write_b32 a158, v122\n" \
"v_mfma_f32_16x16x32_bf16 v[156:159], v[60:63], v[212:215], v[156:159]\n" \
"ds_read_b128 a[244:247], v249 offset:14336\n" \
"v_accvgpr_write_b32 a159, v123\n" \
"v_mfma_f32_16x16x32_bf16 v[176:179], v[32:35], v[216:219], v[176:179]\n" \
"ds_read_b128 a[216:219], v249 offset:8448\n" \
"v_cvt_pk_bf16_f32 v144, v144, v145\n" \
"v_mfma_f32_16x16x32_bf16 v[176:179], v[36:39], v[220:223], v[176:179]\n" \
"ds_read_b128 a[220:223], v249 offset:10496\n" \
"v_cvt_pk_bf16_f32 v145, v146, v147\n" \
"v_mfma_f32_16x16x32_bf16 v[180:183], v[40:43], v[216:219], v[180:183]\n" \
"ds_read_b128 a[248:251], v249 offset:12544\n" \
"v_cvt_pk_bf16_f32 v146, v148, v149\n" \
"v_mfma_f32_16x16x32_bf16 v[180:183], v[44:47], v[220:223], v[180:183]\n" \
"ds_read_b128 a[252:255], v249 offset:14592\n" \
"v_cvt_pk_bf16_f32 v147, v150, v151\n" \
"v_mfma_f32_16x16x32_bf16 v[184:187], v[48:51], v[216:219], v[184:187]\n" \
"ds_read_b128 a[48:51], v254\n" \
"v_accvgpr_write_b32 a168, v144\n" \
"v_mfma_f32_16x16x32_bf16 v[184:187], v[52:55], v[220:223], v[184:187]\n" \
"ds_read_b128 v[32:35], v254\n" \
"v_accvgpr_write_b32 a169, v145\n" \
"v_mfma_f32_16x16x32_bf16 v[188:191], v[56:59], v[216:219], v[188:191]\n" \
"ds_read_b128 v[36:39], v254\n" \
"v_accvgpr_write_b32 a170, v146\n" \
"v_mfma_f32_16x16x32_bf16 v[188:191], v[60:63], v[220:223], v[188:191]\n" \
"s_waitcnt lgkmcnt(4)\n" \
"buffer_store_dwordx4 a[20:23], v248, s[56:59], s90 offen offset:1024 sc1\n" \
"v_accvgpr_write_b32 a171, v147\n" \
"s_cmp_eq_u32 s63, 0\n" \
"s_cbranch_scc1 Lmask0_%=\n" \
"Lmask0r_%=:\n" \
"v_mfma_f32_16x16x32_bf16 v[0:3], a[192:195], a[128:131], v[0:3]\n" \
"s_lshl_b32 s70, s95, 1\n" \
"v_mov_b32 v201, 0xf149f2ca\n" \
"v_cvt_pk_bf16_f32 v152, v152, v153\n" \
"v_cvt_pk_bf16_f32 v153, v154, v155\n" \
"v_mfma_f32_16x16x32_bf16 v[0:3], a[196:199], a[132:135], v[0:3]\n" \
"s_add_u32 s70, s70, 131072\n" \
"v_cvt_pk_bf16_f32 v154, v156, v157\n" \
"v_cvt_pk_bf16_f32 v155, v158, v159\n" \
"v_mfma_f32_16x16x32_bf16 v[0:3], a[224:227], a[136:139], v[0:3]\n" \
"ds_read_b128 a[20:23], v254\n" \
"v_accvgpr_write_b32 a172, v152\n" \
"v_mfma_f32_16x16x32_bf16 v[0:3], a[228:231], a[140:143], v[0:3]\n" \
"ds_read_b128 a[52:55], v254\n" \
"v_accvgpr_write_b32 a173, v153\n" \
"v_mfma_f32_16x16x32_bf16 v[8:11], a[192:195], a[144:147], 0\n" \
"v_accvgpr_write_b32 a174, v154\n" \
"v_accvgpr_write_b32 a175, v155\n" \
"v_mfma_f32_16x16x32_bf16 v[8:11], a[196:199], a[148:151], v[8:11]\n" \
"v_cvt_pk_bf16_f32 v176, v176, v177\n" \
"v_cvt_pk_bf16_f32 v177, v178, v179\n" \
"v_mfma_f32_16x16x32_bf16 v[8:11], a[224:227], a[152:155], v[8:11]\n" \
"v_cvt_pk_bf16_f32 v178, v180, v181\n" \
"v_cvt_pk_bf16_f32 v179, v182, v183\n" \
"v_max3_f32 v192, v0, v1, v2\n" \
"v_max3_f32 v192, v192, v3, v201\n" \
"v_mfma_f32_16x16x32_bf16 v[8:11], a[228:231], a[156:159], v[8:11]\n" \
"v_accvgpr_write_b32 a184, v176\n" \
"v_accvgpr_write_b32 a185, v177\n" \
"v_mfma_f32_16x16x32_bf16 v[16:19], a[192:195], a[160:163], 0\n" \
"v_accvgpr_write_b32 a186, v178\n" \
"v_accvgpr_write_b32 a187, v179\n" \
"v_mfma_f32_16x16x32_bf16 v[16:19], a[196:199], a[164:167], v[16:19]\n" \
"v_cvt_pk_bf16_f32 v184, v184, v185\n" \
"v_cvt_pk_bf16_f32 v185, v186, v187\n" \
"v_mfma_f32_16x16x32_bf16 v[16:19], a[224:227], a[168:171], v[16:19]\n" \
"v_max3_f32 v193, v8, v9, v10\n" \
"v_cvt_pk_bf16_f32 v186, v188, v189\n" \
"v_cvt_pk_bf16_f32 v187, v190, v191\n" \
"v_mfma_f32_16x16x32_bf16 v[16:19], a[228:231], a[172:175], v[16:19]\n" \
"v_accvgpr_write_b32 a188, v184\n" \
"v_accvgpr_write_b32 a189, v185\n" \
"v_mfma_f32_16x16x32_bf16 v[24:27], a[192:195], a[176:179], 0\n" \
"v_accvgpr_write_b32 a190, v186\n" \
"v_accvgpr_write_b32 a191, v187\n" \
"v_mfma_f32_16x16x32_bf16 v[24:27], a[196:199], a[180:183], v[24:27]\n" \
"v_add_u32 v200, s70, v248\n" \
"v_max3_f32 v193, v193, v11, v201\n" \
"v_mfma_f32_16x16x32_bf16 v[24:27], a[224:227], a[184:187], v[24:27]\n" \
"ds_read_b128 v[128:131], v254\n" \
"ds_read_b128 v[132:135], v254\n" \
"v_max3_f32 v194, v16, v17, v18\n" \
"v_max3_f32 v194, v194, v19, v201\n" \
"v_mfma_f32_16x16x32_bf16 v[24:27], a[228:231], a[188:191], v[24:27]\n" \
"s_waitcnt lgkmcnt(2)\n" \
"ds_read_b128 v[136:139], v254\n" \
"ds_read_b128 v[140:143], v254\n" \
"v_mfma_f32_16x16x32_bf16 v[4:7], a[200:203], a[128:131], v[4:7]\n" \
"ds_read_b128 v[144:147], v254\n" \
"ds_read_b128 v[148:151], v254\n" \
"v_mfma_f32_16x16x32_bf16 v[4:7], a[204:207], a[132:135], v[4:7]\n" \
"ds_read_b128 a[192:195], v249 offset:16384\n" \
"ds_read_b128 v[152:155], v254\n" \
"v_mfma_f32_16x16x32_bf16 v[4:7], a[232:235], a[136:139], v[4:7]\n" \
"ds_read_b128 v[156:159], v254\n" \
"ds_read_b128 v[160:163], v254\n" \
"v_max3_f32 v195, v24, v25, v26\n" \
"v_max3_f32 v195, v195, v27, v201\n" \
"v_mov_b32 v196, v192\n" \
"v_mov_b32 v197, v193\n" \
"v_mov_b32 v198, v194\n" \
"v_mov_b32 v199, v195\n" \
"v_permlane16_swap_b32 v196, v192\n" \
"v_permlane16_swap_b32 v197, v193\n" \
"v_permlane16_swap_b32 v198, v194\n" \
"v_permlane16_swap_b32 v199, v195\n" \
"v_max_f32 v192, v192, v196\n" \
"v_max_f32 v193, v193, v197\n" \
"v_mfma_f32_16x16x32_bf16 v[4:7], a[236:239], a[140:143], v[4:7]\n" \
"ds_read_b128 a[196:199], v249 offset:18432\n" \
"ds_read_b128 v[164:167], v254\n" \
"v_max_f32 v194, v194, v198\n" \
"v_max_f32 v195, v195, v199\n" \
"v_mov_b32 v196, v192\n" \
"v_mov_b32 v197, v193\n" \
"v_mov_b32 v198, v194\n" \
"v_mov_b32 v199, v195\n" \
"v_permlane32_swap_b32 v196, v192\n" \
"v_permlane32_swap_b32 v197, v193\n" \
"v_permlane32_swap_b32 v198, v194\n" \
"v_permlane32_swap_b32 v199, v195\n" \
"v_max_f32 v192, v192, v196\n" \
"v_max_f32 v193, v193, v197\n" \
"v_mfma_f32_16x16x32_bf16 v[12:15], a[200:203], a[144:147], 0\n" \
"ds_read_b128 v[168:171], v254\n" \
"ds_read_b128 v[172:175], v254\n" \
"v_max_f32 v194, v194, v198\n" \
"v_max_f32 v195, v195, v199\n" \
"v_sub_f32 v224, 0, v192\n" \
"v_sub_f32 v225, 0, v192\n" \
"v_sub_f32 v226, 0, v192\n" \
"v_sub_f32 v227, 0, v192\n" \
"v_add_f32 v32, v32, v224\n" \
"v_add_f32 v33, v33, v224\n" \
"v_add_f32 v34, v34, v224\n" \
"v_add_f32 v35, v35, v224\n" \
"v_sub_f32 v228, 0, v193\n" \
"v_sub_f32 v229, 0, v193\n" \
"v_mfma_f32_16x16x32_bf16 v[12:15], a[204:207], a[148:151], v[12:15]\n" \
"ds_read_b128 a[224:227], v249 offset:20480\n" \
"ds_read_b128 v[176:179], v254\n" \
"v_sub_f32 v230, 0, v193\n" \
"v_sub_f32 v231, 0, v193\n" \
"v_sub_f32 v232, 0, v194\n" \
"v_sub_f32 v233, 0, v194\n" \
"v_sub_f32 v234, 0, v194\n" \
"v_sub_f32 v235, 0, v194\n" \
"v_sub_f32 v236, 0, v195\n" \
"v_sub_f32 v237, 0, v195\n" \
"v_sub_f32 v238, 0, v195\n" \
"v_sub_f32 v239, 0, v195\n" \
"v_mfma_f32_16x16x32_bf16 v[12:15], a[232:235], a[152:155], v[12:15]\n" \
"ds_read_b128 v[180:183], v254\n" \
"v_add_f32 v36, v36, v224\n" \
"v_add_f32 v37, v37, v224\n" \
"v_add_f32 v38, v38, v224\n" \
"v_add_f32 v39, v39, v224\n" \
"v_pk_add_f32 v[0:1], v[0:1], v[224:225]\n" \
"v_pk_add_f32 v[2:3], v[2:3], v[224:225]\n" \
"v_pk_add_f32 v[4:5], v[4:5], v[224:225]\n" \
"v_pk_add_f32 v[6:7], v[6:7], v[224:225]\n" \
"v_exp_f32 v0, v0\n" \
"v_exp_f32 v1, v1\n" \
"v_exp_f32 v2, v2\n" \
"v_add_f32 v240, v0, v1\n" \
"v_mfma_f32_16x16x32_bf16 v[12:15], a[236:239], a[156:159], v[12:15]\n" \
"ds_read_b128 a[228:231], v249 offset:22528\n" \
"v_exp_f32 v3, v3\n" \
"v_add_f32 v240, v240, v2\n" \
"v_cvt_pk_bf16_f32 v0, v0, v1\n" \
"v_exp_f32 v4, v4\n" \
"v_add_f32 v240, v240, v3\n" \
"v_exp_f32 v5, v5\n" \
"v_add_f32 v240, v240, v4\n" \
"v_cvt_pk_bf16_f32 v1, v2, v3\n" \
"v_mfma_f32_16x16x32_bf16 v[20:23], a[200:203], a[160:163], 0\n" \
"buffer_store_dwordx4 a[24:27], v248, s[56:59], s90 offen offset:2048 sc1\n" \
"ds_write_b128 v200, a[160:163] offset:0\n" \
"v_mfma_f32_16x16x32_bf16 v[20:23], a[204:207], a[164:167], v[20:23]\n" \
"v_exp_f32 v6, v6\n" \
"v_add_f32 v240, v240, v5\n" \
"v_mfma_f32_16x16x32_bf16 v[20:23], a[232:235], a[168:171], v[20:23]\n" \
"v_exp_f32 v7, v7\n" \
"v_add_f32 v240, v240, v6\n" \
"v_cvt_pk_bf16_f32 v2, v4, v5\n" \
"v_add_f32 v240, v240, v7\n" \
"v_cvt_pk_bf16_f32 v3, v6, v7\n" \
"v_pk_add_f32 v[8:9], v[8:9], v[228:229]\n" \
"v_pk_add_f32 v[10:11], v[10:11], v[228:229]\n" \
"v_pk_add_f32 v[12:13], v[12:13], v[228:229]\n" \
"v_pk_add_f32 v[14:15], v[14:15], v[228:229]\n" \
"v_exp_f32 v8, v8\n" \
"v_exp_f32 v9, v9\n" \
"v_exp_f32 v10, v10\n" \
"v_mfma_f32_16x16x32_bf16 v[20:23], a[236:239], a[172:175], v[20:23]\n" \
"buffer_store_dwordx4 a[28:31], v248, s[56:59], s90 offen offset:3072 sc1\n" \
"ds_write_b128 v200, a[164:167] offset:1024\n" \
"v_add_f32 v241, v8, v9\n" \
"v_exp_f32 v11, v11\n" \
"v_add_f32 v241, v241, v10\n" \
"v_cvt_pk_bf16_f32 v8, v8, v9\n" \
"v_exp_f32 v12, v12\n" \
"v_add_f32 v241, v241, v11\n" \
"v_exp_f32 v13, v13\n" \
"v_add_f32 v241, v241, v12\n" \
"v_cvt_pk_bf16_f32 v9, v10, v11\n" \
"v_exp_f32 v14, v14\n" \
"v_add_f32 v241, v241, v13\n" \
"v_exp_f32 v15, v15\n" \
"v_mfma_f32_16x16x32_bf16 v[28:31], a[200:203], a[176:179], 0\n" \
"v_add_f32 v241, v241, v14\n" \
"v_cvt_pk_bf16_f32 v10, v12, v13\n" \
"v_add_f32 v241, v241, v15\n" \
"v_cvt_pk_bf16_f32 v11, v14, v15\n" \
"v_mfma_f32_16x16x32_bf16 v[28:31], a[204:207], a[180:183], v[28:31]\n" \
"global_load_dword v253, v252, s[40:41]\n" \
"v_mfma_f32_16x16x32_bf16 v[28:31], a[232:235], a[184:187], v[28:31]\n" \
"s_sub_u32 s80, s80, 1\n" \
"ds_write_b128 v200, a[168:171] offset:2048\n" \
"v_pk_add_f32 v[16:17], v[16:17], v[232:233]\n" \
"v_pk_add_f32 v[18:19], v[18:19], v[232:233]\n" \
"v_pk_add_f32 v[20:21], v[20:21], v[232:233]\n" \
"v_pk_add_f32 v[22:23], v[22:23], v[232:233]\n" \
"v_exp_f32 v16, v16\n" \
"v_exp_f32 v17, v17\n" \
"v_exp_f32 v18, v18\n" \
"v_add_f32 v242, v16, v17\n" \
"v_exp_f32 v19, v19\n" \
"v_add_f32 v242, v242, v18\n" \
"v_cvt_pk_bf16_f32 v16, v16, v17\n" \
"v_exp_f32 v20, v20\n" \
"v_mfma_f32_16x16x32_bf16 v[28:31], a[236:239], a[188:191], v[28:31]\n" \
"s_cmp_gt_i32 s80, 0\n" \
"v_add_f32 v242, v242, v19\n" \
"s_cselect_b32 s81, 0x80000, 0\n" \
"s_add_u32 s40, s40, s81\n" \
"v_mfma_f32_16x16x32_bf16 v[32:35], a[208:211], a[128:131], v[32:35]\n" \
"s_addc_u32 s41, s41, 0\n" \
"v_exp_f32 v21, v21\n" \
"s_add_u32 m0, s95, 49152\n" \
"v_mfma_f32_16x16x32_bf16 v[32:35], a[212:215], a[132:135], v[32:35]\n" \
"ds_read_b128 a[200:203], v249 offset:16640\n" \
"global_load_lds_dwordx4 v250, s[36:37] offset:0 sc1\n" \
"v_mfma_f32_16x16x32_bf16 v[32:35], a[240:243], a[136:139], v[32:35]\n" \
"ds_read_b128 a[24:27], v254\n" \
"v_add_f32 v242, v242, v20\n" \
"v_mfma_f32_16x16x32_bf16 v[32:35], a[244:247], a[140:143], v[32:35]\n" \
"ds_read_b128 a[204:207], v249 offset:18688\n" \
"v_cvt_pk_bf16_f32 v17, v18, v19\n" \
"v_mfma_f32_16x16x32_bf16 v[40:43], a[208:211], a[144:147], v[228:231]\n" \
"ds_read_b128 a[56:59], v254\n" \
"v_exp_f32 v22, v22\n" \
"v_mfma_f32_16x16x32_bf16 v[40:43], a[212:215], a[148:151], v[40:43]\n" \
"ds_read_b128 a[232:235], v249 offset:20736\n" \
"global_load_lds_dwordx4 v250, s[36:37] offset:1024 sc1\n" \
"v_mfma_f32_16x16x32_bf16 v[40:43], a[240:243], a[152:155], v[40:43]\n" \
"ds_read_b128 a[28:31], v254\n" \
"v_add_f32 v242, v242, v21\n" \
"v_mfma_f32_16x16x32_bf16 v[40:43], a[244:247], a[156:159], v[40:43]\n" \
"ds_read_b128 a[236:239], v249 offset:22784\n" \
"v_exp_f32 v23, v23\n" \
"v_mfma_f32_16x16x32_bf16 v[48:51], a[208:211], a[160:163], v[232:235]\n" \
"ds_write_b128 v200, a[172:175] offset:3072\n" \
"v_mfma_f32_16x16x32_bf16 v[48:51], a[212:215], a[164:167], v[48:51]\n" \
"global_load_lds_dwordx4 v250, s[36:37] offset:2048 sc1\n" \
"v_add_f32 v242, v242, v22\n" \
"v_cvt_pk_bf16_f32 v18, v20, v21\n" \
"v_add_f32 v242, v242, v23\n" \
"v_cvt_pk_bf16_f32 v19, v22, v23\n" \
"v_pk_add_f32 v[24:25], v[24:25], v[236:237]\n" \
"v_pk_add_f32 v[26:27], v[26:27], v[236:237]\n" \
"v_pk_add_f32 v[28:29], v[28:29], v[236:237]\n" \
"v_pk_add_f32 v[30:31], v[30:31], v[236:237]\n" \
"v_mfma_f32_16x16x32_bf16 v[48:51], a[240:243], a[168:171], v[48:51]\n" \
"v_exp_f32 v24, v24\n" \
"v_mfma_f32_16x16x32_bf16 v[48:51], a[244:247], a[172:175], v[48:51]\n" \
"ds_write_b128 v200, a[176:179] offset:4096\n" \
"v_mfma_f32_16x16x32_bf16 v[56:59], a[208:211], a[176:179], v[236:239]\n" \
"global_load_lds_dwordx4 v250, s[36:37] offset:3072 sc1\n" \
"v_exp_f32 v25, v25\n" \
"v_mfma_f32_16x16x32_bf16 v[56:59], a[212:215], a[180:183], v[56:59]\n" \
"s_add_u32 s36, s36, 17408\n" \
"s_mov_b32 s42, s60\n" \
"s_mov_b32 s43, s60\n" \
"v_mfma_f32_16x16x32_bf16 v[56:59], a[240:243], a[184:187], v[56:59]\n" \
"s_mov_b64 s[8:9], 0\n" \
"ds_write_b128 v200, a[180:183] offset:5120\n" \
"v_mfma_f32_16x16x32_bf16 v[56:59], a[244:247], a[188:191], v[56:59]\n" \
"s_mov_b64 s[10:11], -1\n" \
"s_lshl_b32 s71, s63, 2\n" \
"s_add_u32 s71, s71, s79\n" \
"v_mfma_f32_16x16x32_bf16 v[36:39], a[216:219], a[128:131], v[36:39]\n" \
"v_exp_f32 v26, v26\n" \
"v_mfma_f32_16x16x32_bf16 v[36:39], a[220:223], a[132:135], v[36:39]\n" \
"ds_read_b128 a[208:211], v249 offset:24576\n" \
"v_add_f32 v243, v24, v25\n" \
"v_mfma_f32_16x16x32_bf16 v[36:39], a[248:251], a[136:139], v[36:39]\n" \
"ds_read_b128 a[60:63], v254\n" \
"v_exp_f32 v27, v27\n" \
"v_mfma_f32_16x16x32_bf16 v[36:39], a[252:255], a[140:143], v[36:39]\n" \
"ds_read_b128 a[212:215], v249 offset:26624\n" \
"v_add_f32 v243, v243, v26\n" \
"v_mfma_f32_16x16x32_bf16 v[44:47], a[216:219], a[144:147], v[228:231]\n" \
"ds_read_b128 v[184:187], v254\n" \
"v_cvt_pk_bf16_f32 v24, v24, v25\n" \
"v_mfma_f32_16x16x32_bf16 v[44:47], a[220:223], a[148:151], v[44:47]\n" \
"ds_read_b128 a[240:243], v249 offset:28672\n" \
"v_exp_f32 v28, v28\n" \
"v_mfma_f32_16x16x32_bf16 v[44:47], a[248:251], a[152:155], v[44:47]\n" \
"ds_read_b128 v[188:191], v254\n" \
"v_add_f32 v243, v243, v27\n" \
"v_mfma_f32_16x16x32_bf16 v[44:47], a[252:255], a[156:159], v[44:47]\n" \
"ds_read_b128 a[244:247], v249 offset:30720\n" \
"v_exp_f32 v29, v29\n" \
"v_mfma_f32_16x16x32_bf16 v[52:55], a[216:219], a[160:163], v[232:235]\n" \
"ds_write_b128 v200, a[184:187] offset:6144\n" \
"v_mfma_f32_16x16x32_bf16 v[52:55], a[220:223], a[164:167], v[52:55]\n" \
"v_add_f32 v243, v243, v28\n" \
"v_cvt_pk_bf16_f32 v25, v26, v27\n" \
"v_mfma_f32_16x16x32_bf16 v[52:55], a[248:251], a[168:171], v[52:55]\n" \
"v_exp_f32 v30, v30\n" \
"v_add_f32 v243, v243, v29\n" \
"v_mfma_f32_16x16x32_bf16 v[52:55], a[252:255], a[172:175], v[52:55]\n" \
"ds_write_b128 v200, a[188:191] offset:7168\n" \
"v_mfma_f32_16x16x32_bf16 v[60:63], a[216:219], a[176:179], v[236:239]\n" \
"v_exp_f32 v31, v31\n" \
"v_add_f32 v243, v243, v30\n" \
"v_mfma_f32_16x16x32_bf16 v[60:63], a[220:223], a[180:183], v[60:63]\n" \
"v_cvt_pk_bf16_f32 v26, v28, v29\n" \
"v_add_f32 v243, v243, v31\n" \
"v_mfma_f32_16x16x32_bf16 v[60:63], a[248:251], a[184:187], v[60:63]\n" \
"v_mov_b32 v202, s60\n" \
"v_lshlrev_b32 v203, 28, s61\n" \
"v_mfma_f32_16x16x32_bf16 v[60:63], a[252:255], a[188:191], v[60:63]\n" \
"v_mov_b32 v246, v249\n" \
"v_mov_b32 v254, 0xff800000\n" \
"ds_read_b128 a[216:219], v249 offset:24832\n" \
"ds_read_b128 a[220:223], v249 offset:26880\n" \
"ds_read_b128 a[248:251], v249 offset:28928\n" \
"s_cmp_eq_u32 s63, 0\n" \
"s_waitcnt vmcnt(5) lgkmcnt(1)\n" \
"s_barrier\n" \
"buffer_store_dword v202, v203, s[56:59], s71 offen sc1\n" \
"s_memrealtime s[12:13]\n" \
"s_cbranch_scc1 LA1l_%=\n" \
"s_branch LA1_%=\n" \
"Lpg5398_%=:\n" \
"s_cbranch_execz Lpg6323_%=\n" \
"Lloop_%=:\n" \
"v_mfma_f32_16x16x32_bf16 a[0:3], a[192:195], v[64:67], a[0:3]\n" \
"v_exp_f32 v0, v0\n" \
"v_mfma_f32_16x16x32_bf16 a[0:3], a[196:199], v[96:99], a[0:3]\n" \
"v_cvt_pk_bf16_f32 v123, v126, v127\n" \
"v_add_f32 v240, v240, v0\n" \
"v_exp_f32 v1, v1\n" \
"v_mfma_f32_16x16x32_bf16 a[32:35], a[192:195], v[72:75], a[32:35]\n" \
"s_add_u32 m0, s73, 0x4000\n" \
"v_add_f32 v240, v240, v1\n" \
"v_exp_f32 v2, v2\n" \
"v_mfma_f32_16x16x32_bf16 a[32:35], a[196:199], v[104:107], a[32:35]\n" \
"ds_read_b128 a[252:255], v247 offset:31744\n" \
"global_load_lds_dwordx4 v250, s[36:37] offset:0 sc1\n" \
"v_add_f32 v240, v240, v2\n" \
"v_cvt_pk_bf16_f32 v0, v0, v1\n" \
"v_exp_f32 v3, v3\n" \
"v_mfma_f32_16x16x32_bf16 v[128:131], a[192:195], v[80:83], v[128:131]\n" \
"v_xor_b32 v247, 0x8000, v247\n" \
"v_add_f32 v240, v240, v3\n" \
"v_exp_f32 v4, v4\n" \
"v_mfma_f32_16x16x32_bf16 v[128:131], a[196:199], v[112:115], v[128:131]\n" \
"v_add_f32 v240, v240, v4\n" \
"v_cvt_pk_bf16_f32 v1, v2, v3\n" \
"v_mfma_f32_16x16x32_bf16 v[160:163], a[192:195], v[88:91], v[160:163]\n" \
"v_exp_f32 v5, v5\n" \
"v_mfma_f32_16x16x32_bf16 v[160:163], a[196:199], v[120:123], v[160:163]\n" \
"ds_read_b128 a[192:195], v246 offset:16384\n" \
"v_add_f32 v240, v240, v5\n" \
"v_mfma_f32_16x16x32_bf16 a[4:7], a[200:203], v[64:67], a[4:7]\n" \
"v_exp_f32 v6, v6\n" \
"v_mfma_f32_16x16x32_bf16 a[4:7], a[204:207], v[96:99], a[4:7]\n" \
"v_add_f32 v240, v240, v6\n" \
"v_cvt_pk_bf16_f32 v2, v4, v5\n" \
"v_mfma_f32_16x16x32_bf16 a[36:39], a[200:203], v[72:75], a[36:39]\n" \
"v_exp_f32 v7, v7\n" \
"v_mfma_f32_16x16x32_bf16 a[36:39], a[204:207], v[104:107], a[36:39]\n" \
"ds_read_b128 a[196:199], v246 offset:18432\n" \
"v_add_f32 v240, v240, v7\n" \
"v_mfma_f32_16x16x32_bf16 v[132:135], a[200:203], v[80:83], v[132:135]\n" \
"v_exp_f32 v8, v8\n" \
"v_mfma_f32_16x16x32_bf16 v[132:135], a[204:207], v[112:115], v[132:135]\n" \
"v_add_f32 v241, v241, v8\n" \
"v_cvt_pk_bf16_f32 v3, v6, v7\n" \
"v_mfma_f32_16x16x32_bf16 v[164:167], a[200:203], v[88:91], v[164:167]\n" \
"v_exp_f32 v9, v9\n" \
"v_mfma_f32_16x16x32_bf16 v[164:167], a[204:207], v[120:123], v[164:167]\n" \
"ds_read_b128 a[200:203], v246 offset:16640\n" \
"global_load_lds_dwordx4 v250, s[36:37] offset:1024 sc1\n" \
"v_add_f32 v241, v241, v9\n" \
"v_mfma_f32_16x16x32_bf16 a[8:11], a[208:211], v[64:67], a[8:11]\n" \
"v_exp_f32 v10, v10\n" \
"v_mfma_f32_16x16x32_bf16 a[8:11], a[212:215], v[96:99], a[8:11]\n" \
"v_add_f32 v241, v241, v10\n" \
"v_cvt_pk_bf16_f32 v8, v8, v9\n" \
"v_mfma_f32_16x16x32_bf16 a[40:43], a[208:211], v[72:75], a[40:43]\n" \
"v_exp_f32 v11, v11\n" \
"v_mfma_f32_16x16x32_bf16 a[40:43], a[212:215], v[104:107], a[40:43]\n" \
"ds_read_b128 a[204:207], v246 offset:18688\n" \
"v_add_f32 v241, v241, v11\n" \
"v_mfma_f32_16x16x32_bf16 v[136:139], a[208:211], v[80:83], v[136:139]\n" \
"v_exp_f32 v12, v12\n" \
"v_mfma_f32_16x16x32_bf16 v[136:139], a[212:215], v[112:115], v[136:139]\n" \
"v_add_f32 v241, v241, v12\n" \
"v_cvt_pk_bf16_f32 v9, v10, v11\n" \
"v_mfma_f32_16x16x32_bf16 v[168:171], a[208:211], v[88:91], v[168:171]\n" \
"v_exp_f32 v13, v13\n" \
"v_mfma_f32_16x16x32_bf16 v[168:171], a[212:215], v[120:123], v[168:171]\n" \
"ds_read_b128 a[208:211], v246 offset:24576\n" \
"v_add_f32 v241, v241, v13\n" \
"v_mfma_f32_16x16x32_bf16 a[12:15], a[216:219], v[64:67], a[12:15]\n" \
"v_exp_f32 v14, v14\n" \
"v_mfma_f32_16x16x32_bf16 a[12:15], a[220:223], v[96:99], a[12:15]\n" \
"v_add_f32 v241, v241, v14\n" \
"v_cvt_pk_bf16_f32 v10, v12, v13\n" \
"v_mfma_f32_16x16x32_bf16 a[44:47], a[216:219], v[72:75], a[44:47]\n" \
"v_exp_f32 v15, v15\n" \
"v_mfma_f32_16x16x32_bf16 a[44:47], a[220:223], v[104:107], a[44:47]\n" \
"ds_read_b128 a[212:215], v246 offset:26624\n" \
"global_load_lds_dwordx4 v250, s[36:37] offset:2048 sc1\n" \
"v_add_f32 v241, v241, v15\n" \
"v_mfma_f32_16x16x32_bf16 v[140:143], a[216:219], v[80:83], v[140:143]\n" \
"v_exp_f32 v16, v16\n" \
"v_mfma_f32_16x16x32_bf16 v[140:143], a[220:223], v[112:115], v[140:143]\n" \
"v_add_f32 v242, v242, v16\n" \
"v_cvt_pk_bf16_f32 v11, v14, v15\n" \
"v_mfma_f32_16x16x32_bf16 v[172:175], a[216:219], v[88:91], v[172:175]\n" \
"s_waitcnt lgkmcnt(6)\n" \
"v_exp_f32 v17, v17\n" \
"v_mfma_f32_16x16x32_bf16 v[172:175], a[220:223], v[120:123], v[172:175]\n" \
"ds_read_b128 a[216:219], v246 offset:24832\n" \
"v_add_f32 v242, v242, v17\n" \
"v_mfma_f32_16x16x32_bf16 a[16:19], a[224:227], v[64:67], a[16:19]\n" \
"v_exp_f32 v18, v18\n" \
"v_mfma_f32_16x16x32_bf16 a[16:19], a[228:231], v[96:99], a[16:19]\n" \
"v_add_f32 v242, v242, v18\n" \
"v_cvt_pk_bf16_f32 v16, v16, v17\n" \
"v_mfma_f32_16x16x32_bf16 a[48:51], a[224:227], v[72:75], a[48:51]\n" \
"v_exp_f32 v19, v19\n" \
"v_mfma_f32_16x16x32_bf16 a[48:51], a[228:231], v[104:107], a[48:51]\n" \
"ds_read_b128 a[220:223], v246 offset:26880\n" \
"v_add_f32 v242, v242, v19\n" \
"v_exp_f32 v20, v20\n" \
"v_mfma_f32_16x16x32_bf16 v[144:147], a[224:227], v[80:83], v[144:147]\n" \
"v_add_f32 v242, v242, v20\n" \
"v_cvt_pk_bf16_f32 v17, v18, v19\n" \
"v_mfma_f32_16x16x32_bf16 v[144:147], a[228:231], v[112:115], v[144:147]\n" \
"v_exp_f32 v21, v21\n" \
"v_mfma_f32_16x16x32_bf16 v[176:179], a[224:227], v[88:91], v[176:179]\n" \
"global_load_lds_dwordx4 v250, s[36:37] offset:3072 sc1\n" \
"s_add_u32 s36, s36, 17408\n" \
"v_mfma_f32_16x16x32_bf16 v[176:179], a[228:231], v[120:123], v[176:179]\n" \
"s_xor_b32 s73, s73, 0x8000\n" \
"ds_read_b128 a[224:227], v246 offset:20480\n" \
"s_xor_b64 s[52:53], s[44:45], s[42:43]\n" \
"v_mfma_f32_16x16x32_bf16 a[20:23], a[232:235], v[64:67], a[20:23]\n" \
"s_xor_b64 s[54:55], s[46:47], s[42:43]\n" \
"s_or_b64 s[52:53], s[52:53], s[54:55]\n" \
"s_xor_b64 s[54:55], s[48:49], s[42:43]\n" \
"v_mfma_f32_16x16x32_bf16 a[20:23], a[236:239], v[96:99], a[20:23]\n" \
"s_or_b64 s[52:53], s[52:53], s[54:55]\n" \
"s_xor_b64 s[54:55], s[50:51], s[42:43]\n" \
"s_or_b64 s[52:53], s[52:53], s[54:55]\n" \
"v_mfma_f32_16x16x32_bf16 a[52:55], a[232:235], v[72:75], a[52:55]\n" \
"s_and_b64 s[52:53], s[52:53], s[8:9]\n" \
"s_cbranch_scc1 Lrs0_%=\n" \
"Lrr0_%=:\n" \
"v_mfma_f32_16x16x32_bf16 a[52:55], a[236:239], v[104:107], a[52:55]\n" \
"s_mov_b64 s[8:9], s[10:11]\n" \
"ds_read_b128 a[228:231], v246 offset:22528\n" \
"v_add_f32 v242, v242, v21\n" \
"v_mfma_f32_16x16x32_bf16 v[148:151], a[232:235], v[80:83], v[148:151]\n" \
"v_exp_f32 v22, v22\n" \
"v_mfma_f32_16x16x32_bf16 v[148:151], a[236:239], v[112:115], v[148:151]\n" \
"v_add_f32 v242, v242, v22\n" \
"v_cvt_pk_bf16_f32 v18, v20, v21\n" \
"v_mfma_f32_16x16x32_bf16 v[180:183], a[232:235], v[88:91], v[180:183]\n" \
"v_exp_f32 v23, v23\n" \
"v_mfma_f32_16x16x32_bf16 v[180:183], a[236:239], v[120:123], v[180:183]\n" \
"ds_read_b128 a[232:235], v246 offset:20736\n" \
"v_add_f32 v242, v242, v23\n" \
"v_mfma_f32_16x16x32_bf16 a[24:27], a[240:243], v[64:67], a[24:27]\n" \
"v_exp_f32 v24, v24\n" \
"v_mfma_f32_16x16x32_bf16 a[24:27], a[244:247], v[96:99], a[24:27]\n" \
"v_add_f32 v243, v243, v24\n" \
"v_cvt_pk_bf16_f32 v19, v22, v23\n" \
"v_mfma_f32_16x16x32_bf16 a[56:59], a[240:243], v[72:75], a[56:59]\n" \
"v_exp_f32 v25, v25\n" \
"Lpg5547_%=:\n" \
"s_cbranch_execz Lpg6839_%=\n" \
"v_mfma_f32_16x16x32_bf16 a[56:59], a[244:247], v[104:107], a[56:59]\n" \
"ds_read_b128 a[236:239], v246 offset:22784\n" \
"v_add_f32 v243, v243, v25\n" \
"v_mfma_f32_16x16x32_bf16 v[152:155], a[240:243], v[80:83], v[152:155]\n" \
"v_exp_f32 v26, v26\n" \
"v_mfma_f32_16x16x32_bf16 v[152:155], a[244:247], v[112:115], v[152:155]\n" \
"v_add_f32 v243, v243, v26\n" \
"v_cvt_pk_bf16_f32 v24, v24, v25\n" \
"v_mfma_f32_16x16x32_bf16 v[184:187], a[240:243], v[88:91], v[184:187]\n" \
"v_exp_f32 v27, v27\n" \
"v_mfma_f32_16x16x32_bf16 v[184:187], a[244:247], v[120:123], v[184:187]\n" \
"ds_read_b128 a[240:243], v246 offset:28672\n" \
"v_add_f32 v243, v243, v27\n" \
"v_mfma_f32_16x16x32_bf16 a[28:31], a[248:251], v[64:67], a[28:31]\n" \
"v_exp_f32 v28, v28\n" \
"v_mfma_f32_16x16x32_bf16 a[28:31], a[252:255], v[96:99], a[28:31]\n" \
"v_add_f32 v243, v243, v28\n" \
"v_cvt_pk_bf16_f32 v25, v26, v27\n" \
"v_mfma_f32_16x16x32_bf16 a[60:63], a[248:251], v[72:75], a[60:63]\n" \
"v_exp_f32 v29, v29\n" \
"v_mfma_f32_16x16x32_bf16 a[60:63], a[252:255], v[104:107], a[60:63]\n" \
"ds_read_b128 a[244:247], v246 offset:30720\n" \
"v_add_f32 v243, v243, v29\n" \
"v_mfma_f32_16x16x32_bf16 v[156:159], a[248:251], v[80:83], v[156:159]\n" \
"v_exp_f32 v30, v30\n" \
"v_mfma_f32_16x16x32_bf16 v[156:159], a[252:255], v[112:115], v[156:159]\n" \
"v_add_f32 v243, v243, v30\n" \
"v_cvt_pk_bf16_f32 v26, v28, v29\n" \
"v_mfma_f32_16x16x32_bf16 v[188:191], a[248:251], v[88:91], v[188:191]\n" \
"s_waitcnt lgkmcnt(6)\n" \
"v_exp_f32 v31, v31\n" \
"v_mfma_f32_16x16x32_bf16 v[188:191], a[252:255], v[120:123], v[188:191]\n" \
"ds_read_b128 a[248:251], v246 offset:28928\n" \
"v_add_f32 v243, v243, v31\n" \
"LA1_%=:\n" \
"Lpg5583_%=:\n" \
"s_cbranch_execz Lpg7139_%=\n" \
"v_mfma_f32_16x16x32_bf16 v[64:67], a[192:195], a[128:131], v[224:227]\n" \
"v_exp_f32 v32, v32\n" \
"v_mfma_f32_16x16x32_bf16 v[64:67], a[196:199], a[132:135], v[64:67]\n" \
"v_cvt_pk_bf16_f32 v27, v30, v31\n" \
"v_add_f32 v240, v240, v32\n" \
"v_mfma_f32_16x16x32_bf16 v[72:75], a[192:195], a[144:147], v[228:231]\n" \
"s_mov_b32 m0, s74\n" \
"v_exp_f32 v33, v33\n" \
"v_mfma_f32_16x16x32_bf16 v[72:75], a[196:199], a[148:151], v[72:75]\n" \
"ds_read_b128 a[252:255], v246 offset:30976\n" \
"global_load_lds_dwordx4 v250, s[38:39] offset:0 sc1\n" \
"v_add_f32 v240, v240, v33\n" \
"v_mfma_f32_16x16x32_bf16 v[80:83], a[192:195], a[160:163], v[232:235]\n" \
"v_xor_b32 v246, 0x8000, v246\n" \
"v_exp_f32 v34, v34\n" \
"v_mfma_f32_16x16x32_bf16 v[80:83], a[196:199], a[164:167], v[80:83]\n" \
"v_add_f32 v240, v240, v34\n" \
"v_cvt_pk_bf16_f32 v32, v32, v33\n" \
"v_mfma_f32_16x16x32_bf16 v[88:91], a[192:195], a[176:179], v[236:239]\n" \
"v_exp_f32 v35, v35\n" \
"v_mfma_f32_16x16x32_bf16 v[88:91], a[196:199], a[180:183], v[88:91]\n" \
"ds_read_b128 a[192:195], v247 offset:0\n" \
"v_add_f32 v240, v240, v35\n" \
"v_mfma_f32_16x16x32_bf16 v[68:71], a[200:203], a[128:131], v[224:227]\n" \
"v_exp_f32 v36, v36\n" \
"v_mfma_f32_16x16x32_bf16 v[68:71], a[204:207], a[132:135], v[68:71]\n" \
"v_add_f32 v240, v240, v36\n" \
"v_cvt_pk_bf16_f32 v33, v34, v35\n" \
"v_mfma_f32_16x16x32_bf16 v[76:79], a[200:203], a[144:147], v[228:231]\n" \
"v_exp_f32 v37, v37\n" \
"v_mfma_f32_16x16x32_bf16 v[76:79], a[204:207], a[148:151], v[76:79]\n" \
"ds_read_b128 a[196:199], v247 offset:8192\n" \
"v_add_f32 v240, v240, v37\n" \
"v_mfma_f32_16x16x32_bf16 v[84:87], a[200:203], a[160:163], v[232:235]\n" \
"v_exp_f32 v38, v38\n" \
"v_mfma_f32_16x16x32_bf16 v[84:87], a[204:207], a[164:167], v[84:87]\n" \
"v_add_f32 v240, v240, v38\n" \
"v_cvt_pk_bf16_f32 v34, v36, v37\n" \
"v_mfma_f32_16x16x32_bf16 v[92:95], a[200:203], a[176:179], v[236:239]\n" \
"v_exp_f32 v39, v39\n" \
"v_mfma_f32_16x16x32_bf16 v[92:95], a[204:207], a[180:183], v[92:95]\n" \
"ds_read_b128 a[200:203], v247 offset:1024\n" \
"global_load_lds_dwordx4 v250, s[38:39] offset:1024 sc1\n" \
"v_add_f32 v240, v240, v39\n" \
"v_mfma_f32_16x16x32_bf16 v[96:99], a[208:211], a[128:131], v[224:227]\n" \
"v_exp_f32 v40, v40\n" \
"v_mfma_f32_16x16x32_bf16 v[96:99], a[212:215], a[132:135], v[96:99]\n" \
"v_add_f32 v241, v241, v40\n" \
"v_cvt_pk_bf16_f32 v35, v38, v39\n" \
"v_mfma_f32_16x16x32_bf16 v[104:107], a[208:211], a[144:147], v[228:231]\n" \
"v_exp_f32 v41, v41\n" \
"v_mfma_f32_16x16x32_bf16 v[104:107], a[212:215], a[148:151], v[104:107]\n" \
"ds_read_b128 a[204:207], v247 offset:9216\n" \
"v_add_f32 v241, v241, v41\n" \
"v_mfma_f32_16x16x32_bf16 v[112:115], a[208:211], a[160:163], v[232:235]\n" \
"v_exp_f32 v42, v42\n" \
"v_mfma_f32_16x16x32_bf16 v[112:115], a[212:215], a[164:167], v[112:115]\n" \
"v_add_f32 v241, v241, v42\n" \
"v_cvt_pk_bf16_f32 v40, v40, v41\n" \
"v_mfma_f32_16x16x32_bf16 v[120:123], a[208:211], a[176:179], v[236:239]\n" \
"v_exp_f32 v43, v43\n" \
"v_mfma_f32_16x16x32_bf16 v[120:123], a[212:215], a[180:183], v[120:123]\n" \
"ds_read_b128 a[208:211], v247 offset:2048\n" \
"v_add_f32 v241, v241, v43\n" \
"v_mfma_f32_16x16x32_bf16 v[100:103], a[216:219], a[128:131], v[224:227]\n" \
"v_exp_f32 v44, v44\n" \
"v_mfma_f32_16x16x32_bf16 v[100:103], a[220:223], a[132:135], v[100:103]\n" \
"v_add_f32 v241, v241, v44\n" \
"v_cvt_pk_bf16_f32 v41, v42, v43\n" \
"v_mfma_f32_16x16x32_bf16 v[108:111], a[216:219], a[144:147], v[228:231]\n" \
"v_exp_f32 v45, v45\n" \
"v_mfma_f32_16x16x32_bf16 v[108:111], a[220:223], a[148:151], v[108:111]\n" \
"ds_read_b128 a[212:215], v247 offset:10240\n" \
"global_load_lds_dwordx4 v250, s[38:39] offset:2048 sc1\n" \
"v_add_f32 v241, v241, v45\n" \
"v_mfma_f32_16x16x32_bf16 v[116:119], a[216:219], a[160:163], v[232:235]\n" \
"v_exp_f32 v46, v46\n" \
"v_mfma_f32_16x16x32_bf16 v[116:119], a[220:223], a[164:167], v[116:119]\n" \
"v_add_f32 v241, v241, v46\n" \
"v_cvt_pk_bf16_f32 v42, v44, v45\n" \
"v_mfma_f32_16x16x32_bf16 v[124:127], a[216:219], a[176:179], v[236:239]\n" \
"s_waitcnt lgkmcnt(6)\n" \
"v_exp_f32 v47, v47\n" \
"v_mfma_f32_16x16x32_bf16 v[124:127], a[220:223], a[180:183], v[124:127]\n" \
"ds_read_b128 a[216:219], v247 offset:3072\n" \
"v_add_f32 v241, v241, v47\n" \
"v_mfma_f32_16x16x32_bf16 v[64:67], a[224:227], a[136:139], v[64:67]\n" \
"v_exp_f32 v48, v48\n" \
"v_mfma_f32_16x16x32_bf16 v[64:67], a[228:231], a[140:143], v[64:67]\n" \
"v_add_f32 v242, v242, v48\n" \
"v_cvt_pk_bf16_f32 v43, v46, v47\n" \
"v_mfma_f32_16x16x32_bf16 v[72:75], a[224:227], a[152:155], v[72:75]\n" \
"v_exp_f32 v49, v49\n" \
"v_mfma_f32_16x16x32_bf16 v[72:75], a[228:231], a[156:159], v[72:75]\n" \
"ds_read_b128 a[220:223], v247 offset:11264\n" \
"v_add_f32 v242, v242, v49\n" \
"v_exp_f32 v50, v50\n" \
"v_mfma_f32_16x16x32_bf16 v[80:83], a[224:227], a[168:171], v[80:83]\n" \
"v_add_f32 v242, v242, v50\n" \
"v_cvt_pk_bf16_f32 v48, v48, v49\n" \
"v_mfma_f32_16x16x32_bf16 v[80:83], a[228:231], a[172:175], v[80:83]\n" \
"v_exp_f32 v51, v51\n" \
"v_mfma_f32_16x16x32_bf16 v[88:91], a[224:227], a[184:187], v[88:91]\n" \
"v_add_f32 v242, v242, v51\n" \
"v_exp_f32 v52, v52\n" \
"v_mfma_f32_16x16x32_bf16 v[88:91], a[228:231], a[188:191], v[88:91]\n" \
"ds_read_b128 a[224:227], v247 offset:4096\n" \
"global_load_lds_dwordx4 v250, s[38:39] offset:3072 sc1\n" \
"v_mfma_f32_16x16x32_bf16 v[68:71], a[232:235], a[136:139], v[68:71]\n" \
"s_add_u32 s38, s38, 17408\n" \
"v_add_f32 v242, v242, v52\n" \
"v_cvt_pk_bf16_f32 v49, v50, v51\n" \
"v_mfma_f32_16x16x32_bf16 v[68:71], a[236:239], a[140:143], v[68:71]\n" \
"v_exp_f32 v53, v53\n" \
"v_mfma_f32_16x16x32_bf16 v[76:79], a[232:235], a[152:155], v[76:79]\n" \
"v_add_f32 v242, v242, v53\n" \
"v_exp_f32 v54, v54\n" \
"v_mfma_f32_16x16x32_bf16 v[76:79], a[236:239], a[156:159], v[76:79]\n" \
"ds_read_b128 a[228:231], v247 offset:12288\n" \
"v_add_f32 v242, v242, v54\n" \
"v_cvt_pk_bf16_f32 v50, v52, v53\n" \
"v_mfma_f32_16x16x32_bf16 v[84:87], a[232:235], a[168:171], v[84:87]\n" \
"v_exp_f32 v55, v55\n" \
"v_mfma_f32_16x16x32_bf16 v[84:87], a[236:239], a[172:175], v[84:87]\n" \
"global_load_dword v253, v252, s[40:41]\n" \
"v_add_f32 v242, v242, v55\n" \
"v_mfma_f32_16x16x32_bf16 v[92:95], a[232:235], a[184:187], v[92:95]\n" \
"s_sub_u32 s80, s80, 1\n" \
"s_cmp_gt_i32 s80, 0\n" \
"s_cselect_b32 s81, 0x80000, 0\n" \
"v_mfma_f32_16x16x32_bf16 v[92:95], a[236:239], a[188:191], v[92:95]\n" \
"s_add_u32 s40, s40, s81\n" \
"ds_read_b128 a[232:235], v247 offset:5120\n" \
"s_addc_u32 s41, s41, 0\n" \
"v_mfma_f32_16x16x32_bf16 v[96:99], a[240:243], a[136:139], v[96:99]\n" \
"v_exp_f32 v56, v56\n" \
"v_mfma_f32_16x16x32_bf16 v[96:99], a[244:247], a[140:143], v[96:99]\n" \
"v_add_f32 v243, v243, v56\n" \
"v_cvt_pk_bf16_f32 v51, v54, v55\n" \
"v_mfma_f32_16x16x32_bf16 v[104:107], a[240:243], a[152:155], v[104:107]\n" \
"v_exp_f32 v57, v57\n" \
"v_mfma_f32_16x16x32_bf16 v[104:107], a[244:247], a[156:159], v[104:107]\n" \
"ds_read_b128 a[236:239], v247 offset:13312\n" \
"v_add_f32 v243, v243, v57\n" \
"v_mfma_f32_16x16x32_bf16 v[112:115], a[240:243], a[168:171], v[112:115]\n" \
"v_exp_f32 v58, v58\n" \
"v_mfma_f32_16x16x32_bf16 v[112:115], a[244:247], a[172:175], v[112:115]\n" \
"v_add_f32 v243, v243, v58\n" \
"v_cvt_pk_bf16_f32 v56, v56, v57\n" \
"v_mfma_f32_16x16x32_bf16 v[120:123], a[240:243], a[184:187], v[120:123]\n" \
"v_exp_f32 v59, v59\n" \
"v_mfma_f32_16x16x32_bf16 v[120:123], a[244:247], a[188:191], v[120:123]\n" \
"ds_read_b128 a[240:243], v247 offset:6144\n" \
"v_add_f32 v243, v243, v59\n" \
"v_mfma_f32_16x16x32_bf16 v[100:103], a[248:251], a[136:139], v[100:103]\n" \
"v_exp_f32 v60, v60\n" \
"v_mfma_f32_16x16x32_bf16 v[100:103], a[252:255], a[140:143], v[100:103]\n" \
"v_add_f32 v243, v243, v60\n" \
"v_cvt_pk_bf16_f32 v57, v58, v59\n" \
"v_mfma_f32_16x16x32_bf16 v[108:111], a[248:251], a[152:155], v[108:111]\n" \
"v_exp_f32 v61, v61\n" \
"v_mfma_f32_16x16x32_bf16 v[108:111], a[252:255], a[156:159], v[108:111]\n" \
"ds_read_b128 a[244:247], v247 offset:14336\n" \
"v_add_f32 v243, v243, v61\n" \
"v_mfma_f32_16x16x32_bf16 v[116:119], a[248:251], a[168:171], v[116:119]\n" \
"v_exp_f32 v62, v62\n" \
"v_mfma_f32_16x16x32_bf16 v[116:119], a[252:255], a[172:175], v[116:119]\n" \
"v_add_f32 v243, v243, v62\n" \
"v_cvt_pk_bf16_f32 v58, v60, v61\n" \
"v_mfma_f32_16x16x32_bf16 v[124:127], a[248:251], a[184:187], v[124:127]\n" \
"s_waitcnt vmcnt(10) lgkmcnt(6)\n" \
"v_exp_f32 v63, v63\n" \
"v_mfma_f32_16x16x32_bf16 v[124:127], a[252:255], a[188:191], v[124:127]\n" \
"s_barrier\n" \
"ds_read_b128 a[248:251], v247 offset:7168\n" \
"v_add_f32 v243, v243, v63\n" \
"v_mfma_f32_16x16x32_bf16 a[0:3], a[192:195], v[0:3], a[0:3]\n" \
"v_exp_f32 v64, v64\n" \
"v_mfma_f32_16x16x32_bf16 a[0:3], a[196:199], v[32:35], a[0:3]\n" \
"v_cvt_pk_bf16_f32 v59, v62, v63\n" \
"v_add_f32 v240, v240, v64\n" \
"v_mfma_f32_16x16x32_bf16 a[32:35], a[192:195], v[8:11], a[32:35]\n" \
"s_mov_b32 m0, s73\n" \
"v_exp_f32 v65, v65\n" \
"v_mfma_f32_16x16x32_bf16 a[32:35], a[196:199], v[40:43], a[32:35]\n" \
"ds_read_b128 a[252:255], v247 offset:15360\n" \
"global_load_lds_dwordx4 v250, s[36:37] offset:0 sc1\n" \
"v_add_f32 v240, v240, v65\n" \
"v_mfma_f32_16x16x32_bf16 v[128:131], a[192:195], v[16:19], v[128:131]\n" \
"v_exp_f32 v66, v66\n" \
"v_mfma_f32_16x16x32_bf16 v[128:131], a[196:199], v[48:51], v[128:131]\n" \
"v_add_f32 v240, v240, v66\n" \
"v_cvt_pk_bf16_f32 v64, v64, v65\n" \
"v_mfma_f32_16x16x32_bf16 v[160:163], a[192:195], v[24:27], v[160:163]\n" \
"v_exp_f32 v67, v67\n" \
"v_mfma_f32_16x16x32_bf16 v[160:163], a[196:199], v[56:59], v[160:163]\n" \
"ds_read_b128 a[192:195], v246 offset:0\n" \
"v_add_f32 v240, v240, v67\n" \
"v_mfma_f32_16x16x32_bf16 a[4:7], a[200:203], v[0:3], a[4:7]\n" \
"v_exp_f32 v68, v68\n" \
"v_mfma_f32_16x16x32_bf16 a[4:7], a[204:207], v[32:35], a[4:7]\n" \
"v_add_f32 v240, v240, v68\n" \
"v_cvt_pk_bf16_f32 v65, v66, v67\n" \
"v_mfma_f32_16x16x32_bf16 a[36:39], a[200:203], v[8:11], a[36:39]\n" \
"v_exp_f32 v69, v69\n" \
"v_mfma_f32_16x16x32_bf16 a[36:39], a[204:207], v[40:43], a[36:39]\n" \
"ds_read_b128 a[196:199], v246 offset:2048\n" \
"v_add_f32 v240, v240, v69\n" \
"v_mfma_f32_16x16x32_bf16 v[132:135], a[200:203], v[16:19], v[132:135]\n" \
"v_exp_f32 v70, v70\n" \
"v_mfma_f32_16x16x32_bf16 v[132:135], a[204:207], v[48:51], v[132:135]\n" \
"v_add_f32 v240, v240, v70\n" \
"v_cvt_pk_bf16_f32 v66, v68, v69\n" \
"v_mfma_f32_16x16x32_bf16 v[164:167], a[200:203], v[24:27], v[164:167]\n" \
"v_exp_f32 v71, v71\n" \
"v_mfma_f32_16x16x32_bf16 v[164:167], a[204:207], v[56:59], v[164:167]\n" \
"ds_read_b128 a[200:203], v246 offset:256\n" \
"global_load_lds_dwordx4 v250, s[36:37] offset:1024 sc1\n" \
"v_add_f32 v240, v240, v71\n" \
"v_mfma_f32_16x16x32_bf16 a[8:11], a[208:211], v[0:3], a[8:11]\n" \
"v_exp_f32 v72, v72\n" \
"v_mfma_f32_16x16x32_bf16 a[8:11], a[212:215], v[32:35], a[8:11]\n" \
"v_add_f32 v241, v241, v72\n" \
"v_cvt_pk_bf16_f32 v67, v70, v71\n" \
"v_mfma_f32_16x16x32_bf16 a[40:43], a[208:211], v[8:11], a[40:43]\n" \
"v_exp_f32 v73, v73\n" \
"v_mfma_f32_16x16x32_bf16 a[40:43], a[212:215], v[40:43], a[40:43]\n" \
"ds_read_b128 a[204:207], v246 offset:2304\n" \
"v_add_f32 v241, v241, v73\n" \
"v_mfma_f32_16x16x32_bf16 v[136:139], a[208:211], v[16:19], v[136:139]\n" \
"v_exp_f32 v74, v74\n" \
"v_mfma_f32_16x16x32_bf16 v[136:139], a[212:215], v[48:51], v[136:139]\n" \
"v_add_f32 v241, v241, v74\n" \
"v_cvt_pk_bf16_f32 v72, v72, v73\n" \
"v_mfma_f32_16x16x32_bf16 v[168:171], a[208:211], v[24:27], v[168:171]\n" \
"v_exp_f32 v75, v75\n" \
"v_mfma_f32_16x16x32_bf16 v[168:171], a[212:215], v[56:59], v[168:171]\n" \
"ds_read_b128 a[208:211], v246 offset:8192\n" \
"v_add_f32 v241, v241, v75\n" \
"v_mfma_f32_16x16x32_bf16 a[12:15], a[216:219], v[0:3], a[12:15]\n" \
"v_exp_f32 v76, v76\n" \
"v_mfma_f32_16x16x32_bf16 a[12:15], a[220:223], v[32:35], a[12:15]\n" \
"v_add_f32 v241, v241, v76\n" \
"v_cvt_pk_bf16_f32 v73, v74, v75\n" \
"v_mfma_f32_16x16x32_bf16 a[44:47], a[216:219], v[8:11], a[44:47]\n" \
"v_exp_f32 v77, v77\n" \
"v_mfma_f32_16x16x32_bf16 a[44:47], a[220:223], v[40:43], a[44:47]\n" \
"ds_read_b128 a[212:215], v246 offset:10240\n" \
"global_load_lds_dwordx4 v250, s[36:37] offset:2048 sc1\n" \
"v_add_f32 v241, v241, v77\n" \
"v_mfma_f32_16x16x32_bf16 v[140:143], a[216:219], v[16:19], v[140:143]\n" \
"v_exp_f32 v78, v78\n" \
"v_mfma_f32_16x16x32_bf16 v[140:143], a[220:223], v[48:51], v[140:143]\n" \
"v_add_f32 v241, v241, v78\n" \
"v_cvt_pk_bf16_f32 v74, v76, v77\n" \
"v_mfma_f32_16x16x32_bf16 v[172:175], a[216:219], v[24:27], v[172:175]\n" \
"s_waitcnt lgkmcnt(6)\n" \
"v_exp_f32 v79, v79\n" \
"v_mfma_f32_16x16x32_bf16 v[172:175], a[220:223], v[56:59], v[172:175]\n" \
"ds_read_b128 a[216:219], v246 offset:8448\n" \
"v_add_f32 v241, v241, v79\n" \
"v_mfma_f32_16x16x32_bf16 a[16:19], a[224:227], v[0:3], a[16:19]\n" \
"v_exp_f32 v80, v80\n" \
"v_mfma_f32_16x16x32_bf16 a[16:19], a[228:231], v[32:35], a[16:19]\n" \
"v_add_f32 v242, v242, v80\n" \
"v_cvt_pk_bf16_f32 v75, v78, v79\n" \
"v_mfma_f32_16x16x32_bf16 a[48:51], a[224:227], v[8:11], a[48:51]\n" \
"v_exp_f32 v81, v81\n" \
"v_mfma_f32_16x16x32_bf16 a[48:51], a[228:231], v[40:43], a[48:51]\n" \
"ds_read_b128 a[220:223], v246 offset:10496\n" \
"v_add_f32 v242, v242, v81\n" \
"v_mfma_f32_16x16x32_bf16 v[144:147], a[224:227], v[16:19], v[144:147]\n" \
"v_exp_f32 v82, v82\n" \
"v_mfma_f32_16x16x32_bf16 v[144:147], a[228:231], v[48:51], v[144:147]\n" \
"v_add_f32 v242, v242, v82\n" \
"v_cvt_pk_bf16_f32 v80, v80, v81\n" \
"v_mfma_f32_16x16x32_bf16 v[176:179], a[224:227], v[24:27], v[176:179]\n" \
"v_exp_f32 v83, v83\n" \
"v_mfma_f32_16x16x32_bf16 v[176:179], a[228:231], v[56:59], v[176:179]\n" \
"ds_read_b128 a[224:227], v246 offset:4096\n" \
"global_load_lds_dwordx4 v250, s[36:37] offset:3072 sc1\n" \
"v_add_f32 v242, v242, v83\n" \
"v_mfma_f32_16x16x32_bf16 a[20:23], a[232:235], v[0:3], a[20:23]\n" \
"s_add_u32 s36, s36, 17408\n" \
"v_exp_f32 v84, v84\n" \
"v_mfma_f32_16x16x32_bf16 a[20:23], a[236:239], v[32:35], a[20:23]\n" \
"v_add_f32 v242, v242, v84\n" \
"v_cvt_pk_bf16_f32 v81, v82, v83\n" \
"v_mfma_f32_16x16x32_bf16 a[52:55], a[232:235], v[8:11], a[52:55]\n" \
"v_exp_f32 v85, v85\n" \
"v_mfma_f32_16x16x32_bf16 a[52:55], a[236:239], v[40:43], a[52:55]\n" \
"ds_read_b128 a[228:231], v246 offset:6144\n" \
"v_add_f32 v242, v242, v85\n" \
"v_mfma_f32_16x16x32_bf16 v[148:151], a[232:235], v[16:19], v[148:151]\n" \
"v_exp_f32 v86, v86\n" \
"v_mfma_f32_16x16x32_bf16 v[148:151], a[236:239], v[48:51], v[148:151]\n" \
"v_add_f32 v242, v242, v86\n" \
"v_cvt_pk_bf16_f32 v82, v84, v85\n" \
"v_mfma_f32_16x16x32_bf16 v[180:183], a[232:235], v[24:27], v[180:183]\n" \
"v_exp_f32 v87, v87\n" \
"v_mfma_f32_16x16x32_bf16 v[180:183], a[236:239], v[56:59], v[180:183]\n" \
"ds_read_b128 a[232:235], v246 offset:4352\n" \
"v_add_f32 v242, v242, v87\n" \
"v_mfma_f32_16x16x32_bf16 a[24:27], a[240:243], v[0:3], a[24:27]\n" \
"v_exp_f32 v88, v88\n" \
"v_mfma_f32_16x16x32_bf16 a[24:27], a[244:247], v[32:35], a[24:27]\n" \
"v_add_f32 v243, v243, v88\n" \
"v_cvt_pk_bf16_f32 v83, v86, v87\n" \
"v_mfma_f32_16x16x32_bf16 a[56:59], a[240:243], v[8:11], a[56:59]\n" \
"v_exp_f32 v89, v89\n" \
"v_mfma_f32_16x16x32_bf16 a[56:59], a[244:247], v[40:43], a[56:59]\n" \
"ds_read_b128 a[236:239], v246 offset:6400\n" \
"v_add_f32 v243, v243, v89\n" \
"v_mfma_f32_16x16x32_bf16 v[152:155], a[240:243], v[16:19], v[152:155]\n" \
"v_exp_f32 v90, v90\n" \
"v_mfma_f32_16x16x32_bf16 v[152:155], a[244:247], v[48:51], v[152:155]\n" \
"v_add_f32 v243, v243, v90\n" \
"v_cvt_pk_bf16_f32 v88, v88, v89\n" \
"v_mfma_f32_16x16x32_bf16 v[184:187], a[240:243], v[24:27], v[184:187]\n" \
"v_exp_f32 v91, v91\n" \
"v_mfma_f32_16x16x32_bf16 v[184:187], a[244:247], v[56:59], v[184:187]\n" \
"ds_read_b128 a[240:243], v246 offset:12288\n" \
"v_add_f32 v243, v243, v91\n" \
"v_mfma_f32_16x16x32_bf16 a[28:31], a[248:251], v[0:3], a[28:31]\n" \
"v_exp_f32 v92, v92\n" \
"v_mfma_f32_16x16x32_bf16 a[28:31], a[252:255], v[32:35], a[28:31]\n" \
"v_add_f32 v243, v243, v92\n" \
"v_cvt_pk_bf16_f32 v89, v90, v91\n" \
"v_mfma_f32_16x16x32_bf16 a[60:63], a[248:251], v[8:11], a[60:63]\n" \
"v_exp_f32 v93, v93\n" \
"v_mfma_f32_16x16x32_bf16 a[60:63], a[252:255], v[40:43], a[60:63]\n" \
"ds_read_b128 a[244:247], v246 offset:14336\n" \
"v_add_f32 v243, v243, v93\n" \
"v_mfma_f32_16x16x32_bf16 v[156:159], a[248:251], v[16:19], v[156:159]\n" \
"v_exp_f32 v94, v94\n" \
"v_mfma_f32_16x16x32_bf16 v[156:159], a[252:255], v[48:51], v[156:159]\n" \
"v_add_f32 v243, v243, v94\n" \
"v_cvt_pk_bf16_f32 v90, v92, v93\n" \
"v_mfma_f32_16x16x32_bf16 v[188:191], a[248:251], v[24:27], v[188:191]\n" \
"s_waitcnt lgkmcnt(6)\n" \
"v_exp_f32 v95, v95\n" \
"v_mfma_f32_16x16x32_bf16 v[188:191], a[252:255], v[56:59], v[188:191]\n" \
"ds_read_b128 a[248:251], v246 offset:12544\n" \
"v_add_f32 v243, v243, v95\n" \
"v_mfma_f32_16x16x32_bf16 v[0:3], a[192:195], a[128:131], v[224:227]\n" \
"v_exp_f32 v96, v96\n" \
"v_mfma_f32_16x16x32_bf16 v[0:3], a[196:199], a[132:135], v[0:3]\n" \
"v_cvt_pk_bf16_f32 v91, v94, v95\n" \
"v_add_f32 v240, v240, v96\n" \
"v_exp_f32 v97, v97\n" \
"v_mfma_f32_16x16x32_bf16 v[8:11], a[192:195], a[144:147], v[228:231]\n" \
"s_add_u32 m0, s74, 0x4000\n" \
"v_add_f32 v240, v240, v97\n" \
"v_exp_f32 v98, v98\n" \
"v_mfma_f32_16x16x32_bf16 v[8:11], a[196:199], a[148:151], v[8:11]\n" \
"ds_read_b128 a[252:255], v246 offset:14592\n" \
"global_load_lds_dwordx4 v250, s[38:39] offset:0 sc1\n" \
"v_add_f32 v240, v240, v98\n" \
"v_cvt_pk_bf16_f32 v96, v96, v97\n" \
"v_exp_f32 v99, v99\n" \
"v_mfma_f32_16x16x32_bf16 v[16:19], a[192:195], a[160:163], v[232:235]\n" \
"v_add_f32 v240, v240, v99\n" \
"v_exp_f32 v100, v100\n" \
"v_mfma_f32_16x16x32_bf16 v[16:19], a[196:199], a[164:167], v[16:19]\n" \
"v_add_f32 v240, v240, v100\n" \
"v_cvt_pk_bf16_f32 v97, v98, v99\n" \
"v_mfma_f32_16x16x32_bf16 v[24:27], a[192:195], a[176:179], v[236:239]\n" \
"v_exp_f32 v101, v101\n" \
"v_mfma_f32_16x16x32_bf16 v[24:27], a[196:199], a[180:183], v[24:27]\n" \
"ds_read_b128 a[192:195], v247 offset:16384\n" \
"v_add_f32 v240, v240, v101\n" \
"v_mfma_f32_16x16x32_bf16 v[4:7], a[200:203], a[128:131], v[224:227]\n" \
"v_exp_f32 v102, v102\n" \
"v_mfma_f32_16x16x32_bf16 v[4:7], a[204:207], a[132:135], v[4:7]\n" \
"v_add_f32 v240, v240, v102\n" \
"v_cvt_pk_bf16_f32 v98, v100, v101\n" \
"v_mfma_f32_16x16x32_bf16 v[12:15], a[200:203], a[144:147], v[228:231]\n" \
"v_exp_f32 v103, v103\n" \
"v_mfma_f32_16x16x32_bf16 v[12:15], a[204:207], a[148:151], v[12:15]\n" \
"ds_read_b128 a[196:199], v247 offset:24576\n" \
"v_add_f32 v240, v240, v103\n" \
"v_mfma_f32_16x16x32_bf16 v[20:23], a[200:203], a[160:163], v[232:235]\n" \
"v_exp_f32 v104, v104\n" \
"v_mfma_f32_16x16x32_bf16 v[20:23], a[204:207], a[164:167], v[20:23]\n" \
"v_add_f32 v241, v241, v104\n" \
"v_cvt_pk_bf16_f32 v99, v102, v103\n" \
"v_mfma_f32_16x16x32_bf16 v[28:31], a[200:203], a[176:179], v[236:239]\n" \
"v_exp_f32 v105, v105\n" \
"v_mfma_f32_16x16x32_bf16 v[28:31], a[204:207], a[180:183], v[28:31]\n" \
"ds_read_b128 a[200:203], v247 offset:17408\n" \
"global_load_lds_dwordx4 v250, s[38:39] offset:1024 sc1\n" \
"v_add_f32 v241, v241, v105\n" \
"v_mfma_f32_16x16x32_bf16 v[32:35], a[208:211], a[128:131], v[224:227]\n" \
"v_exp_f32 v106, v106\n" \
"v_mfma_f32_16x16x32_bf16 v[32:35], a[212:215], a[132:135], v[32:35]\n" \
"v_add_f32 v241, v241, v106\n" \
"v_cvt_pk_bf16_f32 v104, v104, v105\n" \
"v_mfma_f32_16x16x32_bf16 v[40:43], a[208:211], a[144:147], v[228:231]\n" \
"v_exp_f32 v107, v107\n" \
"v_mfma_f32_16x16x32_bf16 v[40:43], a[212:215], a[148:151], v[40:43]\n" \
"ds_read_b128 a[204:207], v247 offset:25600\n" \
"v_add_f32 v241, v241, v107\n" \
"v_mfma_f32_16x16x32_bf16 v[48:51], a[208:211], a[160:163], v[232:235]\n" \
"v_exp_f32 v108, v108\n" \
"v_mfma_f32_16x16x32_bf16 v[48:51], a[212:215], a[164:167], v[48:51]\n" \
"v_add_f32 v241, v241, v108\n" \
"v_cvt_pk_bf16_f32 v105, v106, v107\n" \
"v_mfma_f32_16x16x32_bf16 v[56:59], a[208:211], a[176:179], v[236:239]\n" \
"v_exp_f32 v109, v109\n" \
"v_mfma_f32_16x16x32_bf16 v[56:59], a[212:215], a[180:183], v[56:59]\n" \
"ds_read_b128 a[208:211], v247 offset:18432\n" \
"v_add_f32 v241, v241, v109\n" \
"v_mfma_f32_16x16x32_bf16 v[36:39], a[216:219], a[128:131], v[224:227]\n" \
"v_exp_f32 v110, v110\n" \
"v_mfma_f32_16x16x32_bf16 v[36:39], a[220:223], a[132:135], v[36:39]\n" \
"v_add_f32 v241, v241, v110\n" \
"v_cvt_pk_bf16_f32 v106, v108, v109\n" \
"v_mfma_f32_16x16x32_bf16 v[44:47], a[216:219], a[144:147], v[228:231]\n" \
"v_exp_f32 v111, v111\n" \
"v_mfma_f32_16x16x32_bf16 v[44:47], a[220:223], a[148:151], v[44:47]\n" \
"ds_read_b128 a[212:215], v247 offset:26624\n" \
"global_load_lds_dwordx4 v250, s[38:39] offset:2048 sc1\n" \
"v_add_f32 v241, v241, v111\n" \
"v_mfma_f32_16x16x32_bf16 v[52:55], a[216:219], a[160:163], v[232:235]\n" \
"v_exp_f32 v112, v112\n" \
"v_mfma_f32_16x16x32_bf16 v[52:55], a[220:223], a[164:167], v[52:55]\n" \
"v_add_f32 v242, v242, v112\n" \
"v_cvt_pk_bf16_f32 v107, v110, v111\n" \
"v_mfma_f32_16x16x32_bf16 v[60:63], a[216:219], a[176:179], v[236:239]\n" \
"s_waitcnt lgkmcnt(6)\n" \
"v_exp_f32 v113, v113\n" \
"v_mfma_f32_16x16x32_bf16 v[60:63], a[220:223], a[180:183], v[60:63]\n" \
"ds_read_b128 a[216:219], v247 offset:19456\n" \
"v_add_f32 v242, v242, v113\n" \
"v_mfma_f32_16x16x32_bf16 v[0:3], a[224:227], a[136:139], v[0:3]\n" \
"v_exp_f32 v114, v114\n" \
"v_mfma_f32_16x16x32_bf16 v[0:3], a[228:231], a[140:143], v[0:3]\n" \
"v_add_f32 v242, v242, v114\n" \
"v_cvt_pk_bf16_f32 v112, v112, v113\n" \
"v_mfma_f32_16x16x32_bf16 v[8:11], a[224:227], a[152:155], v[8:11]\n" \
"v_exp_f32 v115, v115\n" \
"v_mfma_f32_16x16x32_bf16 v[8:11], a[228:231], a[156:159], v[8:11]\n" \
"ds_read_b128 a[220:223], v247 offset:27648\n" \
"v_add_f32 v242, v242, v115\n" \
"v_mfma_f32_16x16x32_bf16 v[16:19], a[224:227], a[168:171], v[16:19]\n" \
"v_exp_f32 v116, v116\n" \
"v_mfma_f32_16x16x32_bf16 v[16:19], a[228:231], a[172:175], v[16:19]\n" \
"v_add_f32 v242, v242, v116\n" \
"v_cvt_pk_bf16_f32 v113, v114, v115\n" \
"v_mfma_f32_16x16x32_bf16 v[24:27], a[224:227], a[184:187], v[24:27]\n" \
"v_exp_f32 v117, v117\n" \
"v_mfma_f32_16x16x32_bf16 v[24:27], a[228:231], a[188:191], v[24:27]\n" \
"ds_read_b128 a[224:227], v247 offset:20480\n" \
"global_load_lds_dwordx4 v250, s[38:39] offset:3072 sc1\n" \
"v_add_f32 v242, v242, v117\n" \
"v_mfma_f32_16x16x32_bf16 v[4:7], a[232:235], a[136:139], v[4:7]\n" \
"s_add_u32 s38, s38, 17408\n" \
"v_exp_f32 v118, v118\n" \
"v_mfma_f32_16x16x32_bf16 v[4:7], a[236:239], a[140:143], v[4:7]\n" \
"v_add_f32 v242, v242, v118\n" \
"v_cvt_pk_bf16_f32 v114, v116, v117\n" \
"v_mfma_f32_16x16x32_bf16 v[12:15], a[232:235], a[152:155], v[12:15]\n" \
"v_exp_f32 v119, v119\n" \
"v_mfma_f32_16x16x32_bf16 v[12:15], a[236:239], a[156:159], v[12:15]\n" \
"ds_read_b128 a[228:231], v247 offset:28672\n" \
"v_add_f32 v242, v242, v119\n" \
"v_mfma_f32_16x16x32_bf16 v[20:23], a[232:235], a[168:171], v[20:23]\n" \
"global_load_dword v253, v252, s[40:41]\n" \
"v_mfma_f32_16x16x32_bf16 v[20:23], a[236:239], a[172:175], v[20:23]\n" \
"s_sub_u32 s80, s80, 1\n" \
"s_cmp_gt_i32 s80, 0\n" \
"s_cselect_b32 s81, 0x80000, 0\n" \
"v_mfma_f32_16x16x32_bf16 v[28:31], a[232:235], a[184:187], v[28:31]\n" \
"s_add_u32 s40, s40, s81\n" \
"s_addc_u32 s41, s41, 0\n" \
"s_load_dwordx8 s[44:51], s[56:57], s79 glc\n" \
"v_mfma_f32_16x16x32_bf16 v[28:31], a[236:239], a[188:191], v[28:31]\n" \
"s_xor_b32 s74, s74, 0x8000\n" \
"ds_read_b128 a[232:235], v247 offset:21504\n" \
"s_sub_u32 s75, s75, 1\n" \
"v_mfma_f32_16x16x32_bf16 v[32:35], a[240:243], a[136:139], v[32:35]\n" \
"s_cmp_lg_u32 s75, 0\n" \
"v_exp_f32 v120, v120\n" \
"v_mfma_f32_16x16x32_bf16 v[32:35], a[244:247], a[140:143], v[32:35]\n" \
"v_add_f32 v243, v243, v120\n" \
"v_cvt_pk_bf16_f32 v115, v118, v119\n" \
"v_mfma_f32_16x16x32_bf16 v[40:43], a[240:243], a[152:155], v[40:43]\n" \
"v_exp_f32 v121, v121\n" \
"v_mfma_f32_16x16x32_bf16 v[40:43], a[244:247], a[156:159], v[40:43]\n" \
"ds_read_b128 a[236:239], v247 offset:29696\n" \
"v_add_f32 v243, v243, v121\n" \
"v_mfma_f32_16x16x32_bf16 v[48:51], a[240:243], a[168:171], v[48:51]\n" \
"v_exp_f32 v122, v122\n" \
"v_mfma_f32_16x16x32_bf16 v[48:51], a[244:247], a[172:175], v[48:51]\n" \
"v_add_f32 v243, v243, v122\n" \
"v_cvt_pk_bf16_f32 v120, v120, v121\n" \
"v_mfma_f32_16x16x32_bf16 v[56:59], a[240:243], a[184:187], v[56:59]\n" \
"v_exp_f32 v123, v123\n" \
"v_mfma_f32_16x16x32_bf16 v[56:59], a[244:247], a[188:191], v[56:59]\n" \
"ds_read_b128 a[240:243], v247 offset:22528\n" \
"v_add_f32 v243, v243, v123\n" \
"v_mfma_f32_16x16x32_bf16 v[36:39], a[248:251], a[136:139], v[36:39]\n" \
"v_exp_f32 v124, v124\n" \
"v_mfma_f32_16x16x32_bf16 v[36:39], a[252:255], a[140:143], v[36:39]\n" \
"v_add_f32 v243, v243, v124\n" \
"v_cvt_pk_bf16_f32 v121, v122, v123\n" \
"v_mfma_f32_16x16x32_bf16 v[44:47], a[248:251], a[152:155], v[44:47]\n" \
"v_exp_f32 v125, v125\n" \
"v_mfma_f32_16x16x32_bf16 v[44:47], a[252:255], a[156:159], v[44:47]\n" \
"ds_read_b128 a[244:247], v247 offset:30720\n" \
"v_add_f32 v243, v243, v125\n" \
"v_mfma_f32_16x16x32_bf16 v[52:55], a[248:251], a[168:171], v[52:55]\n" \
"v_exp_f32 v126, v126\n" \
"v_mfma_f32_16x16x32_bf16 v[52:55], a[252:255], a[172:175], v[52:55]\n" \
"v_add_f32 v243, v243, v126\n" \
"v_cvt_pk_bf16_f32 v122, v124, v125\n" \
"v_mfma_f32_16x16x32_bf16 v[60:63], a[248:251], a[184:187], v[60:63]\n" \
"s_waitcnt vmcnt(10) lgkmcnt(6)\n" \
"v_exp_f32 v127, v127\n" \
"v_mfma_f32_16x16x32_bf16 v[60:63], a[252:255], a[188:191], v[60:63]\n" \
"s_barrier\n" \
"ds_read_b128 a[248:251], v247 offset:23552\n" \
"v_add_f32 v243, v243, v127\n" \
"s_cbranch_scc1 Lloop_%=\n" \
"v_mfma_f32_16x16x32_bf16 a[0:3], a[192:195], v[64:67], a[0:3]\n" \
"v_exp_f32 v8, v8\n" \
"v_mfma_f32_16x16x32_bf16 a[0:3], a[196:199], v[96:99], a[0:3]\n" \
"v_cvt_pk_bf16_f32 v123, v126, v127\n" \
"v_add_f32 v241, v241, v8\n" \
"v_exp_f32 v9, v9\n" \
"v_mfma_f32_16x16x32_bf16 a[32:35], a[192:195], v[72:75], a[32:35]\n" \
"s_add_u32 m0, s73, 0x4000\n" \
"v_add_f32 v241, v241, v9\n" \
"v_exp_f32 v10, v10\n" \
"v_mfma_f32_16x16x32_bf16 a[32:35], a[196:199], v[104:107], a[32:35]\n" \
"ds_read_b128 a[252:255], v247 offset:31744\n" \
"global_load_lds_dwordx4 v250, s[36:37] offset:0 sc1\n" \
"v_add_f32 v241, v241, v10\n" \
"v_cvt_pk_bf16_f32 v8, v8, v9\n" \
"v_exp_f32 v11, v11\n" \
"v_mfma_f32_16x16x32_bf16 v[128:131], a[192:195], v[80:83], v[128:131]\n" \
"v_xor_b32 v247, 0x8000, v247\n" \
"v_add_f32 v241, v241, v11\n" \
"v_exp_f32 v12, v12\n" \
"v_mfma_f32_16x16x32_bf16 v[128:131], a[196:199], v[112:115], v[128:131]\n" \
"v_add_f32 v241, v241, v12\n" \
"v_cvt_pk_bf16_f32 v9, v10, v11\n" \
"v_exp_f32 v13, v13\n" \
"v_mfma_f32_16x16x32_bf16 v[160:163], a[192:195], v[88:91], v[160:163]\n" \
"v_add_f32 v241, v241, v13\n" \
"v_exp_f32 v14, v14\n" \
"v_mfma_f32_16x16x32_bf16 v[160:163], a[196:199], v[120:123], v[160:163]\n" \
"ds_read_b128 a[192:195], v246 offset:16384\n" \
"v_add_f32 v241, v241, v14\n" \
"v_cvt_pk_bf16_f32 v10, v12, v13\n" \
"v_exp_f32 v15, v15\n" \
"v_mfma_f32_16x16x32_bf16 a[4:7], a[200:203], v[64:67], a[4:7]\n" \
"v_add_f32 v241, v241, v15\n" \
"v_exp_f32 v16, v16\n" \
"v_mfma_f32_16x16x32_bf16 a[4:7], a[204:207], v[96:99], a[4:7]\n" \
"v_add_f32 v242, v242, v16\n" \
"v_cvt_pk_bf16_f32 v11, v14, v15\n" \
"v_exp_f32 v17, v17\n" \
"v_mfma_f32_16x16x32_bf16 a[36:39], a[200:203], v[72:75], a[36:39]\n" \
"v_add_f32 v242, v242, v17\n" \
"v_exp_f32 v18, v18\n" \
"v_mfma_f32_16x16x32_bf16 a[36:39], a[204:207], v[104:107], a[36:39]\n" \
"ds_read_b128 a[196:199], v246 offset:18432\n" \
"v_add_f32 v242, v242, v18\n" \
"v_cvt_pk_bf16_f32 v16, v16, v17\n" \
"v_exp_f32 v19, v19\n" \
"v_mfma_f32_16x16x32_bf16 v[132:135], a[200:203], v[80:83], v[132:135]\n" \
"v_add_f32 v242, v242, v19\n" \
"v_exp_f32 v20, v20\n" \
"v_mfma_f32_16x16x32_bf16 v[132:135], a[204:207], v[112:115], v[132:135]\n" \
"v_add_f32 v242, v242, v20\n" \
"v_cvt_pk_bf16_f32 v17, v18, v19\n" \
"v_exp_f32 v21, v21\n" \
"v_mfma_f32_16x16x32_bf16 v[164:167], a[200:203], v[88:91], v[164:167]\n" \
"v_add_f32 v242, v242, v21\n" \
"v_exp_f32 v22, v22\n" \
"v_mfma_f32_16x16x32_bf16 v[164:167], a[204:207], v[120:123], v[164:167]\n" \
"ds_read_b128 a[200:203], v246 offset:16640\n" \
"global_load_lds_dwordx4 v250, s[36:37] offset:1024 sc1\n" \
"v_add_f32 v242, v242, v22\n" \
"v_cvt_pk_bf16_f32 v18, v20, v21\n" \
"v_exp_f32 v23, v23\n" \
"v_mfma_f32_16x16x32_bf16 a[8:11], a[208:211], v[64:67], a[8:11]\n" \
"v_add_f32 v242, v242, v23\n" \
"v_cvt_pk_bf16_f32 v19, v22, v23\n" \
"v_cmp_gt_i32 vcc, 0, v251\n" \
"v_cmp_gt_i32 s[64:65], 1, v251\n" \
"v_cmp_gt_i32 s[66:67], 2, v251\n" \
"v_cndmask_b32 v0, v0, v254, vcc\n" \
"v_mfma_f32_16x16x32_bf16 a[8:11], a[212:215], v[96:99], a[8:11]\n" \
"v_cmp_gt_i32 vcc, 3, v251\n" \
"v_cndmask_b32 v1, v1, v254, s[64:65]\n" \
"v_cmp_gt_i32 s[64:65], 16, v251\n" \
"v_cndmask_b32 v2, v2, v254, s[66:67]\n" \
"v_cmp_gt_i32 s[66:67], 17, v251\n" \
"v_cndmask_b32 v3, v3, v254, vcc\n" \
"v_mfma_f32_16x16x32_bf16 a[40:43], a[208:211], v[72:75], a[40:43]\n" \
"v_cmp_gt_i32 vcc, 18, v251\n" \
"v_cndmask_b32 v4, v4, v254, s[64:65]\n" \
"v_cmp_gt_i32 s[64:65], 19, v251\n" \
"v_cndmask_b32 v5, v5, v254, s[66:67]\n" \
"v_cmp_gt_i32 s[66:67], 32, v251\n" \
"v_cndmask_b32 v6, v6, v254, vcc\n" \
"v_mfma_f32_16x16x32_bf16 a[40:43], a[212:215], v[104:107], a[40:43]\n" \
"ds_read_b128 a[204:207], v246 offset:18688\n" \
"Lpg6194_%=:\n" \
"s_cbranch_execz Lpg7662_%=\n" \
"v_cmp_gt_i32 vcc, 33, v251\n" \
"v_mfma_f32_16x16x32_bf16 v[136:139], a[208:211], v[80:83], v[136:139]\n" \
"v_cndmask_b32 v7, v7, v254, s[64:65]\n" \
"v_cmp_gt_i32 s[64:65], 34, v251\n" \
"v_mfma_f32_16x16x32_bf16 v[136:139], a[212:215], v[112:115], v[136:139]\n" \
"v_cndmask_b32 v32, v32, v254, s[66:67]\n" \
"v_cmp_gt_i32 s[66:67], 35, v251\n" \
"v_mfma_f32_16x16x32_bf16 v[168:171], a[208:211], v[88:91], v[168:171]\n" \
"v_cndmask_b32 v33, v33, v254, vcc\n" \
"v_cmp_gt_i32 vcc, 48, v251\n" \
"v_mfma_f32_16x16x32_bf16 v[168:171], a[212:215], v[120:123], v[168:171]\n" \
"ds_read_b128 a[208:211], v246 offset:24576\n" \
"v_cndmask_b32 v34, v34, v254, s[64:65]\n" \
"v_mfma_f32_16x16x32_bf16 a[12:15], a[216:219], v[64:67], a[12:15]\n" \
"v_cmp_gt_i32 s[64:65], 49, v251\n" \
"v_cndmask_b32 v35, v35, v254, s[66:67]\n" \
"v_mfma_f32_16x16x32_bf16 a[12:15], a[220:223], v[96:99], a[12:15]\n" \
"v_cmp_gt_i32 s[66:67], 50, v251\n" \
"v_cndmask_b32 v36, v36, v254, vcc\n" \
"v_mfma_f32_16x16x32_bf16 a[44:47], a[216:219], v[72:75], a[44:47]\n" \
"v_cmp_gt_i32 vcc, 51, v251\n" \
"v_cndmask_b32 v37, v37, v254, s[64:65]\n" \
"v_mfma_f32_16x16x32_bf16 a[44:47], a[220:223], v[104:107], a[44:47]\n" \
"ds_read_b128 a[212:215], v246 offset:26624\n" \
"global_load_lds_dwordx4 v250, s[36:37] offset:2048 sc1\n" \
"v_mfma_f32_16x16x32_bf16 v[140:143], a[216:219], v[80:83], v[140:143]\n" \
"v_cndmask_b32 v38, v38, v254, s[66:67]\n" \
"v_cndmask_b32 v39, v39, v254, vcc\n" \
"v_mfma_f32_16x16x32_bf16 v[140:143], a[220:223], v[112:115], v[140:143]\n" \
"v_exp_f32 v0, v0\n" \
"v_mfma_f32_16x16x32_bf16 v[172:175], a[216:219], v[88:91], v[172:175]\n" \
"s_waitcnt lgkmcnt(6)\n" \
"v_add_f32 v240, v240, v0\n" \
"v_exp_f32 v1, v1\n" \
"v_mfma_f32_16x16x32_bf16 v[172:175], a[220:223], v[120:123], v[172:175]\n" \
"ds_read_b128 a[216:219], v246 offset:24832\n" \
"v_add_f32 v240, v240, v1\n" \
"v_mfma_f32_16x16x32_bf16 a[16:19], a[224:227], v[64:67], a[16:19]\n" \
"v_exp_f32 v2, v2\n" \
"v_mfma_f32_16x16x32_bf16 a[16:19], a[228:231], v[96:99], a[16:19]\n" \
"v_add_f32 v240, v240, v2\n" \
"v_cvt_pk_bf16_f32 v0, v0, v1\n" \
"v_mfma_f32_16x16x32_bf16 a[48:51], a[224:227], v[72:75], a[48:51]\n" \
"v_exp_f32 v3, v3\n" \
"v_mfma_f32_16x16x32_bf16 a[48:51], a[228:231], v[104:107], a[48:51]\n" \
"ds_read_b128 a[220:223], v246 offset:26880\n" \
"v_add_f32 v240, v240, v3\n" \
"v_exp_f32 v4, v4\n" \
"v_mfma_f32_16x16x32_bf16 v[144:147], a[224:227], v[80:83], v[144:147]\n" \
"v_add_f32 v240, v240, v4\n" \
"v_cvt_pk_bf16_f32 v1, v2, v3\n" \
"v_mfma_f32_16x16x32_bf16 v[144:147], a[228:231], v[112:115], v[144:147]\n" \
"v_exp_f32 v5, v5\n" \
"v_mfma_f32_16x16x32_bf16 v[176:179], a[224:227], v[88:91], v[176:179]\n" \
"global_load_lds_dwordx4 v250, s[36:37] offset:3072 sc1\n" \
"s_add_u32 s36, s36, 17408\n" \
"v_mfma_f32_16x16x32_bf16 v[176:179], a[228:231], v[120:123], v[176:179]\n" \
"s_xor_b32 s73, s73, 0x8000\n" \
"ds_read_b128 a[224:227], v246 offset:20480\n" \
"s_xor_b64 s[52:53], s[44:45], s[42:43]\n" \
"v_mfma_f32_16x16x32_bf16 a[20:23], a[232:235], v[64:67], a[20:23]\n" \
"s_xor_b64 s[54:55], s[46:47], s[42:43]\n" \
"s_or_b64 s[52:53], s[52:53], s[54:55]\n" \
"s_xor_b64 s[54:55], s[48:49], s[42:43]\n" \
"v_mfma_f32_16x16x32_bf16 a[20:23], a[236:239], v[96:99], a[20:23]\n" \
"s_or_b64 s[52:53], s[52:53], s[54:55]\n" \
"s_xor_b64 s[54:55], s[50:51], s[42:43]\n" \
"s_or_b64 s[52:53], s[52:53], s[54:55]\n" \
"v_mfma_f32_16x16x32_bf16 a[52:55], a[232:235], v[72:75], a[52:55]\n" \
"s_and_b64 s[52:53], s[52:53], s[8:9]\n" \
"s_cbranch_scc1 Lrs1_%=\n" \
"Lrr1_%=:\n" \
"v_mfma_f32_16x16x32_bf16 a[52:55], a[236:239], v[104:107], a[52:55]\n" \
"s_mov_b64 s[8:9], s[10:11]\n" \
"ds_read_b128 a[228:231], v246 offset:22528\n" \
"v_add_f32 v240, v240, v5\n" \
"v_mfma_f32_16x16x32_bf16 v[148:151], a[232:235], v[80:83], v[148:151]\n" \
"v_exp_f32 v6, v6\n" \
"v_mfma_f32_16x16x32_bf16 v[148:151], a[236:239], v[112:115], v[148:151]\n" \
"v_add_f32 v240, v240, v6\n" \
"v_cvt_pk_bf16_f32 v2, v4, v5\n" \
"v_mfma_f32_16x16x32_bf16 v[180:183], a[232:235], v[88:91], v[180:183]\n" \
"v_exp_f32 v7, v7\n" \
"v_mfma_f32_16x16x32_bf16 v[180:183], a[236:239], v[120:123], v[180:183]\n" \
"ds_read_b128 a[232:235], v246 offset:20736\n" \
"v_add_f32 v240, v240, v7\n" \
"v_mfma_f32_16x16x32_bf16 a[24:27], a[240:243], v[64:67], a[24:27]\n" \
"v_exp_f32 v24, v24\n" \
"v_mfma_f32_16x16x32_bf16 a[24:27], a[244:247], v[96:99], a[24:27]\n" \
"v_add_f32 v243, v243, v24\n" \
"v_cvt_pk_bf16_f32 v3, v6, v7\n" \
"v_mfma_f32_16x16x32_bf16 a[56:59], a[240:243], v[72:75], a[56:59]\n" \
"v_exp_f32 v25, v25\n" \
"v_mfma_f32_16x16x32_bf16 a[56:59], a[244:247], v[104:107], a[56:59]\n" \
"ds_read_b128 a[236:239], v246 offset:22784\n" \
"v_add_f32 v243, v243, v25\n" \
"v_mfma_f32_16x16x32_bf16 v[152:155], a[240:243], v[80:83], v[152:155]\n" \
"v_exp_f32 v26, v26\n" \
"v_mfma_f32_16x16x32_bf16 v[152:155], a[244:247], v[112:115], v[152:155]\n" \
"v_add_f32 v243, v243, v26\n" \
"v_cvt_pk_bf16_f32 v24, v24, v25\n" \
"v_mfma_f32_16x16x32_bf16 v[184:187], a[240:243], v[88:91], v[184:187]\n" \
"v_exp_f32 v27, v27\n" \
"v_mfma_f32_16x16x32_bf16 v[184:187], a[244:247], v[120:123], v[184:187]\n" \
"ds_read_b128 a[240:243], v246 offset:28672\n" \
"v_add_f32 v243, v243, v27\n" \
"v_mfma_f32_16x16x32_bf16 a[28:31], a[248:251], v[64:67], a[28:31]\n" \
"v_exp_f32 v28, v28\n" \
"v_mfma_f32_16x16x32_bf16 a[28:31], a[252:255], v[96:99], a[28:31]\n" \
"v_add_f32 v243, v243, v28\n" \
"v_cvt_pk_bf16_f32 v25, v26, v27\n" \
"v_mfma_f32_16x16x32_bf16 a[60:63], a[248:251], v[72:75], a[60:63]\n" \
"v_exp_f32 v29, v29\n" \
"v_mfma_f32_16x16x32_bf16 a[60:63], a[252:255], v[104:107], a[60:63]\n" \
"ds_read_b128 a[244:247], v246 offset:30720\n" \
"v_add_f32 v243, v243, v29\n" \
"v_mfma_f32_16x16x32_bf16 v[156:159], a[248:251], v[80:83], v[156:159]\n" \
"v_exp_f32 v30, v30\n" \
"v_mfma_f32_16x16x32_bf16 v[156:159], a[252:255], v[112:115], v[156:159]\n" \
"v_add_f32 v243, v243, v30\n" \
"v_cvt_pk_bf16_f32 v26, v28, v29\n" \
"v_mfma_f32_16x16x32_bf16 v[188:191], a[248:251], v[88:91], v[188:191]\n" \
"s_waitcnt lgkmcnt(6)\n" \
"v_exp_f32 v31, v31\n" \
"v_mfma_f32_16x16x32_bf16 v[188:191], a[252:255], v[120:123], v[188:191]\n" \
"ds_read_b128 a[248:251], v246 offset:28928\n" \
"v_add_f32 v243, v243, v31\n" \
"LA1l_%=:\n" \
"Lpg6323_%=:\n" \
"s_cbranch_execz Lpg13681_%=\n" \
"s_getpc_b64 s[40:41]\n" \
"Lpd_%=:\n" \
"s_lshl_b32 s80, s62, 2\n" \
"s_add_u32 s80, s80, s61\n" \
"s_and_b32 s80, s80, 15\n" \
"s_lshl_b32 s80, s80, 12\n" \
"s_mov_b32 s81, Lcode_end_%=-Lpd_%=-4\n" \
"v_lshl_add_u32 v252, v248, 2, s80\n" \
"v_min_u32 v252, s81, v252\n" \
"s_waitcnt lgkmcnt(0)\n" \
"v_mfma_f32_16x16x32_bf16 v[72:75], a[192:195], a[144:147], v[228:231]\n" \
"v_exp_f32 v32, v32\n" \
"v_mfma_f32_16x16x32_bf16 v[72:75], a[196:199], a[148:151], v[72:75]\n" \
"v_cvt_pk_bf16_f32 v27, v30, v31\n" \
"v_add_f32 v240, v240, v32\n" \
"v_exp_f32 v33, v33\n" \
"v_mfma_f32_16x16x32_bf16 v[80:83], a[192:195], a[160:163], v[232:235]\n" \
"s_mov_b32 m0, s74\n" \
"v_add_f32 v240, v240, v33\n" \
"v_exp_f32 v34, v34\n" \
"v_mfma_f32_16x16x32_bf16 v[80:83], a[196:199], a[164:167], v[80:83]\n" \
"ds_read_b128 a[252:255], v246 offset:30976\n" \
"global_load_lds_dwordx4 v250, s[38:39] offset:0 sc1\n" \
"v_add_f32 v240, v240, v34\n" \
"v_cvt_pk_bf16_f32 v32, v32, v33\n" \
"v_exp_f32 v35, v35\n" \
"v_mfma_f32_16x16x32_bf16 v[88:91], a[192:195], a[176:179], v[236:239]\n" \
"s_lshl_b32 s70, s95, 1\n" \
"v_add_f32 v240, v240, v35\n" \
"v_exp_f32 v36, v36\n" \
"s_xor_b32 s70, s70, 139264\n" \
"v_add_u32 v244, s70, v248\n" \
"v_mfma_f32_16x16x32_bf16 v[88:91], a[196:199], a[180:183], v[88:91]\n" \
"ds_read_b128 a[192:195], v247 offset:0\n" \
"v_add_f32 v240, v240, v36\n" \
"v_cvt_pk_bf16_f32 v33, v34, v35\n" \
"v_exp_f32 v37, v37\n" \
"v_mfma_f32_16x16x32_bf16 v[76:79], a[200:203], a[144:147], v[228:231]\n" \
"ds_read_b128 v[192:195], v244 offset:0\n" \
"v_add_f32 v240, v240, v37\n" \
"v_exp_f32 v38, v38\n" \
"v_mfma_f32_16x16x32_bf16 v[76:79], a[204:207], a[148:151], v[76:79]\n" \
"ds_read_b128 v[196:199], v244 offset:1024\n" \
"v_add_f32 v240, v240, v38\n" \
"v_cvt_pk_bf16_f32 v34, v36, v37\n" \
"v_exp_f32 v39, v39\n" \
"v_mfma_f32_16x16x32_bf16 v[84:87], a[200:203], a[160:163], v[232:235]\n" \
"ds_read_b128 a[196:199], v247 offset:8192\n" \
"v_add_f32 v240, v240, v39\n" \
"v_exp_f32 v40, v40\n" \
"v_mfma_f32_16x16x32_bf16 v[84:87], a[204:207], a[164:167], v[84:87]\n" \
"ds_read_b128 v[200:203], v244 offset:2048\n" \
"v_add_f32 v241, v241, v40\n" \
"v_cvt_pk_bf16_f32 v35, v38, v39\n" \
"v_exp_f32 v41, v41\n" \
"v_mfma_f32_16x16x32_bf16 v[92:95], a[200:203], a[176:179], v[236:239]\n" \
"ds_read_b128 v[204:207], v244 offset:3072\n" \
"v_add_f32 v241, v241, v41\n" \
"v_exp_f32 v42, v42\n" \
"v_mfma_f32_16x16x32_bf16 v[92:95], a[204:207], a[180:183], v[92:95]\n" \
"ds_read_b128 a[200:203], v247 offset:1024\n" \
"v_add_f32 v241, v241, v42\n" \
"v_cvt_pk_bf16_f32 v40, v40, v41\n" \
"v_exp_f32 v43, v43\n" \
"v_mfma_f32_16x16x32_bf16 v[104:107], a[208:211], a[144:147], v[228:231]\n" \
"ds_read_b128 v[208:211], v244 offset:4096\n" \
"global_load_lds_dwordx4 v250, s[38:39] offset:1024 sc1\n" \
"v_add_f32 v241, v241, v43\n" \
"v_exp_f32 v44, v44\n" \
"v_mfma_f32_16x16x32_bf16 v[104:107], a[212:215], a[148:151], v[104:107]\n" \
"ds_read_b128 v[212:215], v244 offset:5120\n" \
"v_add_f32 v241, v241, v44\n" \
"v_cvt_pk_bf16_f32 v41, v42, v43\n" \
"v_exp_f32 v45, v45\n" \
"v_mfma_f32_16x16x32_bf16 v[112:115], a[208:211], a[160:163], v[232:235]\n" \
"ds_read_b128 a[204:207], v247 offset:9216\n" \
"v_add_f32 v241, v241, v45\n" \
"v_exp_f32 v46, v46\n" \
"v_mfma_f32_16x16x32_bf16 v[112:115], a[212:215], a[164:167], v[112:115]\n" \
"ds_read_b128 v[216:219], v244 offset:6144\n" \
"v_add_f32 v241, v241, v46\n" \
"v_cvt_pk_bf16_f32 v42, v44, v45\n" \
"v_exp_f32 v47, v47\n" \
"v_mfma_f32_16x16x32_bf16 v[120:123], a[208:211], a[176:179], v[236:239]\n" \
"ds_read_b128 v[220:223], v244 offset:7168\n" \
"v_add_f32 v241, v241, v47\n" \
"v_exp_f32 v48, v48\n" \
"v_mfma_f32_16x16x32_bf16 v[120:123], a[212:215], a[180:183], v[120:123]\n" \
"ds_read_b128 a[208:211], v247 offset:2048\n" \
"v_add_f32 v242, v242, v48\n" \
"v_cvt_pk_bf16_f32 v43, v46, v47\n" \
"v_exp_f32 v49, v49\n" \
"v_mfma_f32_16x16x32_bf16 v[108:111], a[216:219], a[144:147], v[228:231]\n" \
"ds_write_b128 v244, v[232:235]\n" \
"v_mfma_f32_16x16x32_bf16 v[108:111], a[220:223], a[148:151], v[108:111]\n" \
"ds_write_b128 v244, v[236:239] offset:1024\n" \
"v_mfma_f32_16x16x32_bf16 v[116:119], a[216:219], a[160:163], v[232:235]\n" \
"ds_read_b128 a[212:215], v247 offset:10240\n" \
"v_xor_b32 v245, 0x2000, v244\n" \
"v_mfma_f32_16x16x32_bf16 v[116:119], a[220:223], a[164:167], v[116:119]\n" \
"v_xor_b32 v246, 0x8000, v246\n" \
"v_add_u32 v246, s82, v246\n" \
"v_mfma_f32_16x16x32_bf16 v[124:127], a[216:219], a[176:179], v[236:239]\n" \
"s_waitcnt lgkmcnt(6)\n" \
"global_load_lds_dwordx4 v250, s[38:39] offset:2048 sc1\n" \
"v_add_f32 v242, v242, v49\n" \
"v_mfma_f32_16x16x32_bf16 v[124:127], a[220:223], a[180:183], v[124:127]\n" \
"ds_read_b128 a[216:219], v247 offset:3072\n" \
"v_exp_f32 v50, v50\n" \
"v_mfma_f32_16x16x32_bf16 v[72:75], a[224:227], a[152:155], v[72:75]\n" \
"v_add_f32 v242, v242, v50\n" \
"v_cvt_pk_bf16_f32 v48, v48, v49\n" \
"v_mfma_f32_16x16x32_bf16 v[72:75], a[228:231], a[156:159], v[72:75]\n" \
"v_exp_f32 v51, v51\n" \
"v_mfma_f32_16x16x32_bf16 v[80:83], a[224:227], a[168:171], v[80:83]\n" \
"ds_read_b128 a[220:223], v247 offset:11264\n" \
"v_add_f32 v242, v242, v51\n" \
"v_mfma_f32_16x16x32_bf16 v[80:83], a[228:231], a[172:175], v[80:83]\n" \
"v_exp_f32 v52, v52\n" \
"v_mfma_f32_16x16x32_bf16 v[88:91], a[224:227], a[184:187], v[88:91]\n" \
"v_add_f32 v242, v242, v52\n" \
"v_cvt_pk_bf16_f32 v49, v50, v51\n" \
"v_mfma_f32_16x16x32_bf16 v[88:91], a[228:231], a[188:191], v[88:91]\n" \
"ds_read_b128 a[224:227], v247 offset:4096\n" \
"v_exp_f32 v53, v53\n" \
"v_mfma_f32_16x16x32_bf16 v[76:79], a[232:235], a[152:155], v[76:79]\n" \
"global_load_lds_dwordx4 v250, s[38:39] offset:3072 sc1\n" \
"v_add_f32 v242, v242, v53\n" \
"v_mfma_f32_16x16x32_bf16 v[76:79], a[236:239], a[156:159], v[76:79]\n" \
"s_add_u32 s38, s38, 17408\n" \
"v_exp_f32 v54, v54\n" \
"v_mfma_f32_16x16x32_bf16 v[84:87], a[232:235], a[168:171], v[84:87]\n" \
"ds_read_b128 a[228:231], v247 offset:12288\n" \
"v_add_f32 v242, v242, v54\n" \
"v_cvt_pk_bf16_f32 v50, v52, v53\n" \
"v_mfma_f32_16x16x32_bf16 v[84:87], a[236:239], a[172:175], v[84:87]\n" \
"v_exp_f32 v55, v55\n" \
"v_mfma_f32_16x16x32_bf16 v[92:95], a[232:235], a[184:187], v[92:95]\n" \
"v_add_f32 v242, v242, v55\n" \
"v_exp_f32 v56, v56\n" \
"v_mfma_f32_16x16x32_bf16 v[92:95], a[236:239], a[188:191], v[92:95]\n" \
"ds_read_b128 a[232:235], v247 offset:5120\n" \
"v_add_f32 v243, v243, v56\n" \
"v_cvt_pk_bf16_f32 v51, v54, v55\n" \
"v_mfma_f32_16x16x32_bf16 v[104:107], a[240:243], a[152:155], v[104:107]\n" \
"global_load_dword v253, v252, s[40:41]\n" \
"v_mfma_f32_16x16x32_bf16 v[104:107], a[244:247], a[156:159], v[104:107]\n" \
"v_exp_f32 v57, v57\n" \
"v_mfma_f32_16x16x32_bf16 v[112:115], a[240:243], a[168:171], v[112:115]\n" \
"ds_read_b128 a[236:239], v247 offset:13312\n" \
"v_add_f32 v243, v243, v57\n" \
"v_exp_f32 v58, v58\n" \
"v_mfma_f32_16x16x32_bf16 v[112:115], a[244:247], a[172:175], v[112:115]\n" \
"v_add_f32 v243, v243, v58\n" \
"v_cvt_pk_bf16_f32 v56, v56, v57\n" \
"v_mfma_f32_16x16x32_bf16 v[120:123], a[240:243], a[184:187], v[120:123]\n" \
"v_exp_f32 v59, v59\n" \
"v_mfma_f32_16x16x32_bf16 v[120:123], a[244:247], a[188:191], v[120:123]\n" \
"ds_read_b128 a[240:243], v247 offset:6144\n" \
"v_add_f32 v243, v243, v59\n" \
"v_exp_f32 v60, v60\n" \
"v_mfma_f32_16x16x32_bf16 v[108:111], a[248:251], a[152:155], v[108:111]\n" \
"v_add_f32 v243, v243, v60\n" \
"v_cvt_pk_bf16_f32 v57, v58, v59\n" \
"v_mfma_f32_16x16x32_bf16 v[108:111], a[252:255], a[156:159], v[108:111]\n" \
"v_exp_f32 v61, v61\n" \
"v_mfma_f32_16x16x32_bf16 v[116:119], a[248:251], a[168:171], v[116:119]\n" \
"ds_read_b128 a[244:247], v247 offset:14336\n" \
"v_add_f32 v243, v243, v61\n" \
"v_exp_f32 v62, v62\n" \
"v_mfma_f32_16x16x32_bf16 v[116:119], a[252:255], a[172:175], v[116:119]\n" \
"v_add_f32 v243, v243, v62\n" \
"v_cvt_pk_bf16_f32 v58, v60, v61\n" \
"v_mfma_f32_16x16x32_bf16 v[124:127], a[248:251], a[184:187], v[124:127]\n" \
"s_waitcnt vmcnt(5) lgkmcnt(6)\n" \
"v_exp_f32 v63, v63\n" \
"v_mfma_f32_16x16x32_bf16 v[124:127], a[252:255], a[188:191], v[124:127]\n" \
"s_barrier\n" \
"ds_read_b128 a[248:251], v247 offset:7168\n" \
"v_add_f32 v243, v243, v63\n" \
"v_mfma_f32_16x16x32_bf16 a[0:3], a[192:195], v[0:3], a[0:3]\n" \
"v_exp_f32 v80, v80\n" \
"v_mfma_f32_16x16x32_bf16 a[0:3], a[196:199], v[32:35], a[0:3]\n" \
"v_cvt_pk_bf16_f32 v59, v62, v63\n" \
"v_add_f32 v242, v242, v80\n" \
"v_exp_f32 v81, v81\n" \
"v_mfma_f32_16x16x32_bf16 a[32:35], a[192:195], v[8:11], a[32:35]\n" \
"v_add_f32 v242, v242, v81\n" \
"v_exp_f32 v82, v82\n" \
"v_mfma_f32_16x16x32_bf16 a[32:35], a[196:199], v[40:43], a[32:35]\n" \
"ds_read_b128 a[252:255], v247 offset:15360\n" \
"v_add_f32 v242, v242, v82\n" \
"v_cvt_pk_bf16_f32 v80, v80, v81\n" \
"v_exp_f32 v83, v83\n" \
"v_mfma_f32_16x16x32_bf16 v[128:131], a[192:195], v[16:19], v[128:131]\n" \
"ds_read_b128 v[224:227], v245\n" \
"v_add_f32 v242, v242, v83\n" \
"v_exp_f32 v84, v84\n" \
"v_mfma_f32_16x16x32_bf16 v[128:131], a[196:199], v[48:51], v[128:131]\n" \
"ds_read_b128 v[228:231], v245 offset:1024\n" \
"v_add_f32 v242, v242, v84\n" \
"v_cvt_pk_bf16_f32 v81, v82, v83\n" \
"v_exp_f32 v85, v85\n" \
"v_mov_b32 v244, 0\n" \
"v_mfma_f32_16x16x32_bf16 v[160:163], a[192:195], v[24:27], v[160:163]\n" \
"v_mov_b32 v245, 0\n" \
"v_xor_b32 v252, 0x8000, v246\n" \
"v_add_f32 v242, v242, v85\n" \
"v_exp_f32 v86, v86\n" \
"v_mfma_f32_16x16x32_bf16 v[160:163], a[196:199], v[56:59], v[160:163]\n" \
"s_add_u32 s73, s73, s95\n" \
"ds_read_b128 a[192:195], v246 offset:0\n" \
"v_add_f32 v242, v242, v86\n" \
"v_cvt_pk_bf16_f32 v82, v84, v85\n" \
"v_exp_f32 v87, v87\n" \
"v_mfma_f32_16x16x32_bf16 a[4:7], a[200:203], v[0:3], a[4:7]\n" \
"s_add_u32 s36, s36, s83\n" \
"v_add_f32 v242, v242, v87\n" \
"s_add_u32 s73, s73, 4096\n" \
"s_mov_b32 m0, s73\n" \
"v_mfma_f32_16x16x32_bf16 a[4:7], a[204:207], v[32:35], a[4:7]\n" \
"global_load_lds_dwordx4 v250, s[36:37] offset:-4096 sc1\n" \
"v_cvt_pk_bf16_f32 v83, v86, v87\n" \
"v_mfma_f32_16x16x32_bf16 a[36:39], a[200:203], v[8:11], a[36:39]\n" \
"v_cmp_gt_i32 vcc, 0, v251\n" \
"v_cmp_gt_i32 s[64:65], 1, v251\n" \
"v_mfma_f32_16x16x32_bf16 a[36:39], a[204:207], v[40:43], a[36:39]\n" \
"ds_read_b128 a[196:199], v246 offset:2048\n" \
"global_load_lds_dwordx4 v250, s[36:37] offset:-3072 sc1\n" \
"v_mfma_f32_16x16x32_bf16 v[132:135], a[200:203], v[16:19], v[132:135]\n" \
"v_cmp_gt_i32 s[66:67], 2, v251\n" \
"v_cndmask_b32 v72, v72, v254, vcc\n" \
"v_mfma_f32_16x16x32_bf16 v[132:135], a[204:207], v[48:51], v[132:135]\n" \
"v_cmp_gt_i32 vcc, 3, v251\n" \
"v_cndmask_b32 v73, v73, v254, s[64:65]\n" \
"v_mfma_f32_16x16x32_bf16 v[164:167], a[200:203], v[24:27], v[164:167]\n" \
"v_cmp_gt_i32 s[64:65], 16, v251\n" \
"v_cndmask_b32 v74, v74, v254, s[66:67]\n" \
"v_mfma_f32_16x16x32_bf16 v[164:167], a[204:207], v[56:59], v[164:167]\n" \
"ds_read_b128 a[200:203], v246 offset:256\n" \
"global_load_lds_dwordx4 v250, s[36:37] offset:-2048 sc1\n" \
"v_mfma_f32_16x16x32_bf16 a[8:11], a[208:211], v[0:3], a[8:11]\n" \
"v_cmp_gt_i32 s[66:67], 17, v251\n" \
"v_cndmask_b32 v75, v75, v254, vcc\n" \
"v_mfma_f32_16x16x32_bf16 a[8:11], a[212:215], v[32:35], a[8:11]\n" \
"v_cmp_gt_i32 vcc, 18, v251\n" \
"v_cndmask_b32 v76, v76, v254, s[64:65]\n" \
"v_mfma_f32_16x16x32_bf16 a[40:43], a[208:211], v[8:11], a[40:43]\n" \
"v_cmp_gt_i32 s[64:65], 19, v251\n" \
"v_cndmask_b32 v77, v77, v254, s[66:67]\n" \
"v_mfma_f32_16x16x32_bf16 a[40:43], a[212:215], v[40:43], a[40:43]\n" \
"ds_read_b128 a[204:207], v246 offset:2304\n" \
"global_load_lds_dwordx4 v250, s[36:37] offset:-1024 sc1\n" \
"v_mfma_f32_16x16x32_bf16 v[136:139], a[208:211], v[16:19], v[136:139]\n" \
"v_cmp_gt_i32 s[66:67], 32, v251\n" \
"v_cndmask_b32 v78, v78, v254, vcc\n" \
"v_mfma_f32_16x16x32_bf16 v[136:139], a[212:215], v[48:51], v[136:139]\n" \
"v_cmp_gt_i32 vcc, 33, v251\n" \
"v_cndmask_b32 v79, v79, v254, s[64:65]\n" \
"v_mfma_f32_16x16x32_bf16 v[168:171], a[208:211], v[24:27], v[168:171]\n" \
"v_cmp_gt_i32 s[64:65], 34, v251\n" \
"v_cndmask_b32 v104, v104, v254, s[66:67]\n" \
"v_mfma_f32_16x16x32_bf16 v[168:171], a[212:215], v[56:59], v[168:171]\n" \
"ds_read_b128 a[208:211], v246 offset:8192\n" \
"global_load_lds_dwordx4 v250, s[36:37] offset:0 sc1\n" \
"v_mfma_f32_16x16x32_bf16 a[12:15], a[216:219], v[0:3], a[12:15]\n" \
"v_cmp_gt_i32 s[66:67], 35, v251\n" \
"v_cndmask_b32 v105, v105, v254, vcc\n" \
"v_mfma_f32_16x16x32_bf16 a[12:15], a[220:223], v[32:35], a[12:15]\n" \
"v_cmp_gt_i32 vcc, 48, v251\n" \
"v_cndmask_b32 v106, v106, v254, s[64:65]\n" \
"v_mfma_f32_16x16x32_bf16 a[44:47], a[216:219], v[8:11], a[44:47]\n" \
"v_cmp_gt_i32 s[64:65], 49, v251\n" \
"v_cndmask_b32 v107, v107, v254, s[66:67]\n" \
"v_mfma_f32_16x16x32_bf16 a[44:47], a[220:223], v[40:43], a[44:47]\n" \
"ds_read_b128 a[212:215], v246 offset:10240\n" \
"global_load_lds_dwordx4 v250, s[36:37] offset:1024 sc1\n" \
"v_mfma_f32_16x16x32_bf16 v[140:143], a[216:219], v[16:19], v[140:143]\n" \
"v_cmp_gt_i32 s[66:67], 50, v251\n" \
"v_cndmask_b32 v108, v108, v254, vcc\n" \
"v_mfma_f32_16x16x32_bf16 v[140:143], a[220:223], v[48:51], v[140:143]\n" \
"v_cmp_gt_i32 vcc, 51, v251\n" \
"v_cndmask_b32 v109, v109, v254, s[64:65]\n" \
"v_mfma_f32_16x16x32_bf16 v[172:175], a[216:219], v[24:27], v[172:175]\n" \
"s_waitcnt lgkmcnt(6)\n" \
"v_cndmask_b32 v110, v110, v254, s[66:67]\n" \
"v_cndmask_b32 v111, v111, v254, vcc\n" \
"v_mfma_f32_16x16x32_bf16 v[172:175], a[220:223], v[56:59], v[172:175]\n" \
"ds_read_b128 a[216:219], v246 offset:8448\n" \
"global_load_lds_dwordx4 v250, s[36:37] offset:2048 sc1\n" \
"v_mfma_f32_16x16x32_bf16 a[16:19], a[224:227], v[0:3], a[16:19]\n" \
"v_exp_f32 v72, v72\n" \
"v_mfma_f32_16x16x32_bf16 a[16:19], a[228:231], v[32:35], a[16:19]\n" \
"v_add_f32 v241, v241, v72\n" \
"v_exp_f32 v73, v73\n" \
"v_mfma_f32_16x16x32_bf16 a[48:51], a[224:227], v[8:11], a[48:51]\n" \
"v_add_f32 v241, v241, v73\n" \
"v_exp_f32 v74, v74\n" \
"v_mfma_f32_16x16x32_bf16 a[48:51], a[228:231], v[40:43], a[48:51]\n" \
"ds_read_b128 a[220:223], v246 offset:10496\n" \
"global_load_lds_dwordx4 v250, s[36:37] offset:3072 sc1\n" \
"v_mfma_f32_16x16x32_bf16 v[144:147], a[224:227], v[16:19], v[144:147]\n" \
"s_add_u32 s36, s36, 34816\n" \
"v_add_f32 v241, v241, v74\n" \
"v_cvt_pk_bf16_f32 v72, v72, v73\n" \
"v_mfma_f32_16x16x32_bf16 v[144:147], a[228:231], v[48:51], v[144:147]\n" \
"v_exp_f32 v75, v75\n" \
"v_mfma_f32_16x16x32_bf16 v[176:179], a[224:227], v[24:27], v[176:179]\n" \
"s_add_u32 m0, s74, 0x4000\n" \
"v_add_f32 v241, v241, v75\n" \
"v_exp_f32 v76, v76\n" \
"v_mfma_f32_16x16x32_bf16 v[176:179], a[228:231], v[56:59], v[176:179]\n" \
"ds_read_b128 a[224:227], v246 offset:4096\n" \
"global_load_lds_dwordx4 v250, s[38:39] offset:0 sc1\n" \
"v_mfma_f32_16x16x32_bf16 a[20:23], a[232:235], v[0:3], a[20:23]\n" \
"v_add_f32 v241, v241, v76\n" \
"v_cvt_pk_bf16_f32 v73, v74, v75\n" \
"v_mfma_f32_16x16x32_bf16 a[20:23], a[236:239], v[32:35], a[20:23]\n" \
"v_exp_f32 v77, v77\n" \
"v_mfma_f32_16x16x32_bf16 a[52:55], a[232:235], v[8:11], a[52:55]\n" \
"v_add_f32 v241, v241, v77\n" \
"v_exp_f32 v78, v78\n" \
"v_mfma_f32_16x16x32_bf16 a[52:55], a[236:239], v[40:43], a[52:55]\n" \
"ds_read_b128 a[228:231], v246 offset:6144\n" \
"global_load_lds_dwordx4 v250, s[38:39] offset:1024 sc1\n" \
"v_mfma_f32_16x16x32_bf16 v[148:151], a[232:235], v[16:19], v[148:151]\n" \
"v_add_f32 v241, v241, v78\n" \
"v_cvt_pk_bf16_f32 v74, v76, v77\n" \
"v_mfma_f32_16x16x32_bf16 v[148:151], a[236:239], v[48:51], v[148:151]\n" \
"v_exp_f32 v79, v79\n" \
"v_mfma_f32_16x16x32_bf16 v[180:183], a[232:235], v[24:27], v[180:183]\n" \
"v_add_f32 v241, v241, v79\n" \
"v_exp_f32 v88, v88\n" \
"v_mfma_f32_16x16x32_bf16 v[180:183], a[236:239], v[56:59], v[180:183]\n" \
"ds_read_b128 a[232:235], v246 offset:4352\n" \
"global_load_lds_dwordx4 v250, s[38:39] offset:2048 sc1\n" \
"v_mfma_f32_16x16x32_bf16 a[24:27], a[240:243], v[0:3], a[24:27]\n" \
"v_add_f32 v243, v243, v88\n" \
"v_cvt_pk_bf16_f32 v75, v78, v79\n" \
"v_mfma_f32_16x16x32_bf16 a[24:27], a[244:247], v[32:35], a[24:27]\n" \
"v_exp_f32 v89, v89\n" \
"v_mfma_f32_16x16x32_bf16 a[56:59], a[240:243], v[8:11], a[56:59]\n" \
"v_add_f32 v243, v243, v89\n" \
"v_exp_f32 v90, v90\n" \
"v_mfma_f32_16x16x32_bf16 a[56:59], a[244:247], v[40:43], a[56:59]\n" \
"ds_read_b128 a[236:239], v246 offset:6400\n" \
"global_load_lds_dwordx4 v250, s[38:39] offset:3072 sc1\n" \
"v_add_f32 v243, v243, v90\n" \
"v_mfma_f32_16x16x32_bf16 v[152:155], a[240:243], v[16:19], v[152:155]\n" \
"s_add_u32 s38, s38, 17408\n" \
"s_add_u32 s38, s38, s83\n" \
"s_add_u32 s74, s74, s95\n" \
"v_mfma_f32_16x16x32_bf16 v[152:155], a[244:247], v[48:51], v[152:155]\n" \
"s_add_u32 s74, s74, 4096\n" \
"v_cvt_pk_bf16_f32 v88, v88, v89\n" \
"s_cmp_eq_u32 s63, 7\n" \
"v_mfma_f32_16x16x32_bf16 v[184:187], a[240:243], v[24:27], v[184:187]\n" \
"v_exp_f32 v91, v91\n" \
"v_mfma_f32_16x16x32_bf16 v[184:187], a[244:247], v[56:59], v[184:187]\n" \
"ds_read_b128 a[240:243], v246 offset:12288\n" \
"v_add_f32 v243, v243, v91\n" \
"v_mfma_f32_16x16x32_bf16 a[28:31], a[248:251], v[0:3], a[28:31]\n" \
"v_exp_f32 v92, v92\n" \
"v_mfma_f32_16x16x32_bf16 a[28:31], a[252:255], v[32:35], a[28:31]\n" \
"v_add_f32 v243, v243, v92\n" \
"v_cvt_pk_bf16_f32 v89, v90, v91\n" \
"v_mfma_f32_16x16x32_bf16 a[60:63], a[248:251], v[8:11], a[60:63]\n" \
"v_exp_f32 v93, v93\n" \
"v_mfma_f32_16x16x32_bf16 a[60:63], a[252:255], v[40:43], a[60:63]\n" \
"ds_read_b128 a[244:247], v246 offset:14336\n" \
"v_add_f32 v243, v243, v93\n" \
"v_mfma_f32_16x16x32_bf16 v[156:159], a[248:251], v[16:19], v[156:159]\n" \
"v_exp_f32 v94, v94\n" \
"v_mfma_f32_16x16x32_bf16 v[156:159], a[252:255], v[48:51], v[156:159]\n" \
"v_add_f32 v243, v243, v94\n" \
"v_cvt_pk_bf16_f32 v90, v92, v93\n" \
"v_mfma_f32_16x16x32_bf16 v[188:191], a[248:251], v[24:27], v[188:191]\n" \
"s_waitcnt lgkmcnt(6)\n" \
"v_exp_f32 v95, v95\n" \
"v_mfma_f32_16x16x32_bf16 v[188:191], a[252:255], v[56:59], v[188:191]\n" \
"ds_read_b128 a[248:251], v246 offset:12544\n" \
"v_add_f32 v243, v243, v95\n" \
"s_cbranch_scc1 LF7_%=\n" \
"v_mfma_f32_16x16x32_bf16 v[0:3], a[192:195], v[192:195], v[224:227]\n" \
"s_load_dwordx8 s[44:51], s[56:57], s79 glc\n" \
"v_exp_f32 v104, v104\n" \
"v_mfma_f32_16x16x32_bf16 v[0:3], a[196:199], v[196:199], v[0:3]\n" \
"v_cvt_pk_bf16_f32 v91, v94, v95\n" \
"v_add_f32 v241, v241, v104\n" \
"v_mfma_f32_16x16x32_bf16 v[8:11], a[192:195], v[208:211], v[228:231]\n" \
"v_mov_b32 v76, v240\n" \
"v_exp_f32 v105, v105\n" \
"v_mfma_f32_16x16x32_bf16 v[8:11], a[196:199], v[212:215], v[8:11]\n" \
"ds_read_b128 a[252:255], v246 offset:14592\n" \
"v_add_f32 v241, v241, v105\n" \
"v_mfma_f32_16x16x32_bf16 v[16:19], a[192:195], a[160:163], v[232:235]\n" \
"v_exp_f32 v106, v106\n" \
"v_permlane16_swap_b32 v76, v240\n" \
"v_add_f32 v240, v240, v76\n" \
"v_mov_b32 v76, v240\n" \
"v_mfma_f32_16x16x32_bf16 v[16:19], a[196:199], a[164:167], v[16:19]\n" \
"v_add_f32 v241, v241, v106\n" \
"v_cvt_pk_bf16_f32 v104, v104, v105\n" \
"v_mfma_f32_16x16x32_bf16 v[24:27], a[192:195], a[176:179], v[236:239]\n" \
"v_exp_f32 v107, v107\n" \
"v_permlane32_swap_b32 v76, v240\n" \
"v_add_f32 v240, v240, v76\n" \
"v_rcp_f32 v76, v240\n" \
"v_mul_f32 v240, 0x4d800000, v240\n" \
"v_mfma_f32_16x16x32_bf16 v[24:27], a[196:199], a[180:183], v[24:27]\n" \
"ds_read_b128 a[192:195], v247 offset:16384\n" \
"v_add_f32 v241, v241, v107\n" \
"v_mfma_f32_16x16x32_bf16 v[4:7], a[200:203], v[192:195], v[224:227]\n" \
"v_exp_f32 v108, v108\n" \
"v_mfma_f32_16x16x32_bf16 v[4:7], a[204:207], v[196:199], v[4:7]\n" \
"v_add_f32 v241, v241, v108\n" \
"v_cvt_pk_bf16_f32 v105, v106, v107\n" \
"v_mfma_f32_16x16x32_bf16 v[12:15], a[200:203], v[208:211], v[228:231]\n" \
"v_exp_f32 v109, v109\n" \
"v_mfma_f32_16x16x32_bf16 v[12:15], a[204:207], v[212:215], v[12:15]\n" \
"ds_read_b128 a[196:199], v247 offset:24576\n" \
"v_add_f32 v241, v241, v109\n" \
"v_mfma_f32_16x16x32_bf16 v[20:23], a[200:203], a[160:163], v[232:235]\n" \
"v_exp_f32 v110, v110\n" \
"v_mfma_f32_16x16x32_bf16 v[20:23], a[204:207], a[164:167], v[20:23]\n" \
"v_add_f32 v241, v241, v110\n" \
"v_cvt_pk_bf16_f32 v106, v108, v109\n" \
"v_mfma_f32_16x16x32_bf16 v[28:31], a[200:203], a[176:179], v[236:239]\n" \
"v_exp_f32 v111, v111\n" \
"v_mfma_f32_16x16x32_bf16 v[28:31], a[204:207], a[180:183], v[28:31]\n" \
"ds_read_b128 a[200:203], v247 offset:17408\n" \
"v_add_f32 v241, v241, v111\n" \
"v_mfma_f32_16x16x32_bf16 v[32:35], a[208:211], v[192:195], v[224:227]\n" \
"v_exp_f32 v112, v112\n" \
"v_mfma_f32_16x16x32_bf16 v[32:35], a[212:215], v[196:199], v[32:35]\n" \
"v_add_f32 v242, v242, v112\n" \
"v_cvt_pk_bf16_f32 v107, v110, v111\n" \
"v_mfma_f32_16x16x32_bf16 v[40:43], a[208:211], v[208:211], v[228:231]\n" \
"v_exp_f32 v113, v113\n" \
"v_mfma_f32_16x16x32_bf16 v[40:43], a[212:215], v[212:215], v[40:43]\n" \
"ds_read_b128 a[204:207], v247 offset:25600\n" \
"v_add_f32 v242, v242, v113\n" \
"v_mfma_f32_16x16x32_bf16 v[48:51], a[208:211], a[160:163], v[232:235]\n" \
"v_exp_f32 v114, v114\n" \
"v_mfma_f32_16x16x32_bf16 v[48:51], a[212:215], a[164:167], v[48:51]\n" \
"v_add_f32 v242, v242, v114\n" \
"v_cvt_pk_bf16_f32 v112, v112, v113\n" \
"v_mfma_f32_16x16x32_bf16 v[56:59], a[208:211], a[176:179], v[236:239]\n" \
"v_exp_f32 v115, v115\n" \
"v_mfma_f32_16x16x32_bf16 v[56:59], a[212:215], a[180:183], v[56:59]\n" \
"ds_read_b128 a[208:211], v247 offset:18432\n" \
"v_add_f32 v242, v242, v115\n" \
"v_mfma_f32_16x16x32_bf16 v[36:39], a[216:219], v[192:195], v[224:227]\n" \
"v_exp_f32 v116, v116\n" \
"v_mfma_f32_16x16x32_bf16 v[36:39], a[220:223], v[196:199], v[36:39]\n" \
"v_add_f32 v242, v242, v116\n" \
"v_cvt_pk_bf16_f32 v113, v114, v115\n" \
"v_mfma_f32_16x16x32_bf16 v[44:47], a[216:219], v[208:211], v[228:231]\n" \
"v_exp_f32 v117, v117\n" \
"v_mfma_f32_16x16x32_bf16 v[44:47], a[220:223], v[212:215], v[44:47]\n" \
"ds_read_b128 a[212:215], v247 offset:26624\n" \
"v_add_f32 v242, v242, v117\n" \
"v_mfma_f32_16x16x32_bf16 v[52:55], a[216:219], a[160:163], v[232:235]\n" \
"v_exp_f32 v118, v118\n" \
"v_mfma_f32_16x16x32_bf16 v[52:55], a[220:223], a[164:167], v[52:55]\n" \
"v_add_f32 v242, v242, v118\n" \
"v_cvt_pk_bf16_f32 v114, v116, v117\n" \
"v_mfma_f32_16x16x32_bf16 v[60:63], a[216:219], a[176:179], v[236:239]\n" \
"s_waitcnt lgkmcnt(6)\n" \
"v_exp_f32 v119, v119\n" \
"v_mfma_f32_16x16x32_bf16 v[60:63], a[220:223], a[180:183], v[60:63]\n" \
"ds_read_b128 a[216:219], v247 offset:19456\n" \
"v_add_f32 v242, v242, v119\n" \
"v_mfma_f32_16x16x32_bf16 v[0:3], a[224:227], v[200:203], v[0:3]\n" \
"v_exp_f32 v120, v120\n" \
"v_mfma_f32_16x16x32_bf16 v[0:3], a[228:231], v[204:207], v[0:3]\n" \
"v_add_f32 v243, v243, v120\n" \
"v_cvt_pk_bf16_f32 v115, v118, v119\n" \
"v_mfma_f32_16x16x32_bf16 v[8:11], a[224:227], v[216:219], v[8:11]\n" \
"v_exp_f32 v121, v121\n" \
"v_mfma_f32_16x16x32_bf16 v[8:11], a[228:231], v[220:223], v[8:11]\n" \
"ds_read_b128 a[220:223], v247 offset:27648\n" \
"v_add_f32 v243, v243, v121\n" \
"v_mfma_f32_16x16x32_bf16 v[16:19], a[224:227], a[168:171], v[16:19]\n" \
"v_exp_f32 v122, v122\n" \
"v_mfma_f32_16x16x32_bf16 v[16:19], a[228:231], a[172:175], v[16:19]\n" \
"v_add_f32 v243, v243, v122\n" \
"v_cvt_pk_bf16_f32 v120, v120, v121\n" \
"v_mfma_f32_16x16x32_bf16 v[24:27], a[224:227], a[184:187], v[24:27]\n" \
"v_exp_f32 v123, v123\n" \
"v_mfma_f32_16x16x32_bf16 v[24:27], a[228:231], a[188:191], v[24:27]\n" \
"ds_read_b128 a[224:227], v247 offset:20480\n" \
"v_add_f32 v243, v243, v123\n" \
"v_mfma_f32_16x16x32_bf16 v[4:7], a[232:235], v[200:203], v[4:7]\n" \
"v_exp_f32 v124, v124\n" \
"v_mfma_f32_16x16x32_bf16 v[4:7], a[236:239], v[204:207], v[4:7]\n" \
"v_add_f32 v243, v243, v124\n" \
"v_cvt_pk_bf16_f32 v121, v122, v123\n" \
"v_mfma_f32_16x16x32_bf16 v[12:15], a[232:235], v[216:219], v[12:15]\n" \
"v_exp_f32 v125, v125\n" \
"v_mfma_f32_16x16x32_bf16 v[12:15], a[236:239], v[220:223], v[12:15]\n" \
"ds_read_b128 a[228:231], v247 offset:28672\n" \
"v_add_f32 v243, v243, v125\n" \
"v_mfma_f32_16x16x32_bf16 v[20:23], a[232:235], a[168:171], v[20:23]\n" \
"s_xor_b64 s[52:53], s[44:45], s[42:43]\n" \
"s_xor_b64 s[54:55], s[46:47], s[42:43]\n" \
"s_or_b64 s[52:53], s[52:53], s[54:55]\n" \
"v_mfma_f32_16x16x32_bf16 v[20:23], a[236:239], a[172:175], v[20:23]\n" \
"s_xor_b64 s[54:55], s[48:49], s[42:43]\n" \
"s_or_b64 s[52:53], s[52:53], s[54:55]\n" \
"s_xor_b64 s[54:55], s[50:51], s[42:43]\n" \
"v_mfma_f32_16x16x32_bf16 v[28:31], a[232:235], a[184:187], v[28:31]\n" \
"s_or_b64 s[52:53], s[52:53], s[54:55]\n" \
"v_exp_f32 v126, v126\n" \
"v_mfma_f32_16x16x32_bf16 v[28:31], a[236:239], a[188:191], v[28:31]\n" \
"Lpg6839_%=:\n" \
"s_cbranch_execz Lcb0_%=\n" \
"ds_read_b128 a[232:235], v247 offset:21504\n" \
"v_add_f32 v243, v243, v126\n" \
"v_mfma_f32_16x16x32_bf16 v[32:35], a[240:243], v[200:203], v[32:35]\n" \
"s_and_b64 s[52:53], s[52:53], s[8:9]\n" \
"s_cbranch_scc1 Lrs2_%=\n" \
"Lrr2_%=:\n" \
"v_mfma_f32_16x16x32_bf16 v[32:35], a[244:247], v[204:207], v[32:35]\n" \
"s_mov_b64 s[8:9], s[10:11]\n" \
"v_cvt_pk_bf16_f32 v122, v124, v125\n" \
"v_add_u32 v77, 0x1c90080, v249\n" \
"v_mfma_f32_16x16x32_bf16 v[40:43], a[240:243], v[216:219], v[40:43]\n" \
"v_exp_f32 v127, v127\n" \
"v_mfma_f32_16x16x32_bf16 v[40:43], a[244:247], v[220:223], v[40:43]\n" \
"ds_read_b128 a[236:239], v247 offset:29696\n" \
"v_add_f32 v243, v243, v127\n" \
"v_mfma_f32_16x16x32_bf16 v[48:51], a[240:243], a[168:171], v[48:51]\n" \
"v_cndmask_b32 v77, v249, v77, s[16:17]\n" \
"v_accvgpr_read_b32 v64, a0\n" \
"v_mfma_f32_16x16x32_bf16 v[48:51], a[244:247], a[172:175], v[48:51]\n" \
"v_accvgpr_read_b32 v65, a1\n" \
"v_accvgpr_read_b32 v66, a2\n" \
"v_mfma_f32_16x16x32_bf16 v[56:59], a[240:243], a[184:187], v[56:59]\n" \
"v_accvgpr_read_b32 v67, a3\n" \
"v_accvgpr_read_b32 v68, a4\n" \
"v_accvgpr_read_b32 v69, a5\n" \
"v_accvgpr_read_b32 v70, a6\n" \
"v_accvgpr_read_b32 v71, a7\n" \
"v_pk_mul_f32 v[64:65], v[64:65], v[76:77] op_sel_hi:[1,0]\n" \
"v_pk_mul_f32 v[66:67], v[66:67], v[76:77] op_sel_hi:[1,0]\n" \
"v_pk_mul_f32 v[68:69], v[68:69], v[76:77] op_sel_hi:[1,0]\n" \
"v_pk_mul_f32 v[70:71], v[70:71], v[76:77] op_sel_hi:[1,0]\n" \
"v_cvt_pk_bf16_f32 v96, v64, v65\n" \
"v_cvt_pk_bf16_f32 v97, v66, v67\n" \
"v_cvt_pk_bf16_f32 v98, v68, v69\n" \
"v_cvt_pk_bf16_f32 v99, v70, v71\n" \
"v_pk_mul_f32 v[64:65], v[64:65], v[66:67]\n" \
"v_pk_fma_f32 v[64:65], v[68:69], v[70:71], v[64:65]\n" \
"v_fma_f32 v240, v64, v65, v240\n" \
"v_mfma_f32_16x16x32_bf16 v[56:59], a[244:247], a[188:191], v[56:59]\n" \
"buffer_store_dwordx4 v[96:99], v77, s[56:59], s77 offen offset:0 sc1\n" \
"ds_read_b128 a[240:243], v247 offset:22528\n" \
"v_accvgpr_read_b32 v64, a8\n" \
"v_accvgpr_read_b32 v65, a9\n" \
"v_accvgpr_read_b32 v66, a10\n" \
"v_mfma_f32_16x16x32_bf16 v[36:39], a[248:251], v[200:203], v[36:39]\n" \
"v_accvgpr_read_b32 v67, a11\n" \
"v_accvgpr_read_b32 v68, a12\n" \
"v_accvgpr_read_b32 v69, a13\n" \
"v_accvgpr_read_b32 v70, a14\n" \
"v_accvgpr_read_b32 v71, a15\n" \
"v_pk_mul_f32 v[64:65], v[64:65], v[76:77] op_sel_hi:[1,0]\n" \
"v_pk_mul_f32 v[66:67], v[66:67], v[76:77] op_sel_hi:[1,0]\n" \
"v_pk_mul_f32 v[68:69], v[68:69], v[76:77] op_sel_hi:[1,0]\n" \
"v_pk_mul_f32 v[70:71], v[70:71], v[76:77] op_sel_hi:[1,0]\n" \
"v_cvt_pk_bf16_f32 v100, v64, v65\n" \
"v_cvt_pk_bf16_f32 v101, v66, v67\n" \
"v_cvt_pk_bf16_f32 v102, v68, v69\n" \
"v_cvt_pk_bf16_f32 v103, v70, v71\n" \
"v_pk_mul_f32 v[64:65], v[64:65], v[66:67]\n" \
"v_pk_fma_f32 v[64:65], v[68:69], v[70:71], v[64:65]\n" \
"v_fma_f32 v240, v64, v65, v240\n" \
"v_mfma_f32_16x16x32_bf16 v[36:39], a[252:255], v[204:207], v[36:39]\n" \
"buffer_store_dwordx4 v[100:103], v77, s[56:59], s77 offen offset:2048 sc1\n" \
"s_add_u32 s77, s77, 0x1000\n" \
"v_accvgpr_read_b32 v64, a16\n" \
"v_accvgpr_read_b32 v65, a17\n" \
"v_accvgpr_read_b32 v66, a18\n" \
"v_mfma_f32_16x16x32_bf16 v[44:47], a[248:251], v[216:219], v[44:47]\n" \
"v_accvgpr_read_b32 v67, a19\n" \
"v_accvgpr_read_b32 v68, a20\n" \
"v_accvgpr_read_b32 v69, a21\n" \
"v_accvgpr_read_b32 v70, a22\n" \
"v_accvgpr_read_b32 v71, a23\n" \
"v_pk_mul_f32 v[64:65], v[64:65], v[76:77] op_sel_hi:[1,0]\n" \
"v_pk_mul_f32 v[66:67], v[66:67], v[76:77] op_sel_hi:[1,0]\n" \
"v_pk_mul_f32 v[68:69], v[68:69], v[76:77] op_sel_hi:[1,0]\n" \
"v_pk_mul_f32 v[70:71], v[70:71], v[76:77] op_sel_hi:[1,0]\n" \
"v_cvt_pk_bf16_f32 v96, v64, v65\n" \
"v_cvt_pk_bf16_f32 v97, v66, v67\n" \
"v_cvt_pk_bf16_f32 v98, v68, v69\n" \
"v_cvt_pk_bf16_f32 v99, v70, v71\n" \
"v_pk_mul_f32 v[64:65], v[64:65], v[66:67]\n" \
"v_pk_fma_f32 v[64:65], v[68:69], v[70:71], v[64:65]\n" \
"v_fma_f32 v240, v64, v65, v240\n" \
"v_mfma_f32_16x16x32_bf16 v[44:47], a[252:255], v[220:223], v[44:47]\n" \
"buffer_store_dwordx4 v[96:99], v77, s[56:59], s77 offen offset:0 sc1\n" \
"ds_read_b128 a[244:247], v247 offset:30720\n" \
"v_accvgpr_read_b32 v64, a24\n" \
"v_accvgpr_read_b32 v65, a25\n" \
"v_accvgpr_read_b32 v66, a26\n" \
"v_mfma_f32_16x16x32_bf16 v[52:55], a[248:251], a[168:171], v[52:55]\n" \
"v_accvgpr_read_b32 v67, a27\n" \
"v_accvgpr_read_b32 v68, a28\n" \
"v_accvgpr_read_b32 v69, a29\n" \
"v_accvgpr_read_b32 v70, a30\n" \
"v_accvgpr_read_b32 v71, a31\n" \
"v_pk_mul_f32 v[64:65], v[64:65], v[76:77] op_sel_hi:[1,0]\n" \
"v_pk_mul_f32 v[66:67], v[66:67], v[76:77] op_sel_hi:[1,0]\n" \
"v_pk_mul_f32 v[68:69], v[68:69], v[76:77] op_sel_hi:[1,0]\n" \
"v_pk_mul_f32 v[70:71], v[70:71], v[76:77] op_sel_hi:[1,0]\n" \
"v_cvt_pk_bf16_f32 v100, v64, v65\n" \
"v_cvt_pk_bf16_f32 v101, v66, v67\n" \
"v_cvt_pk_bf16_f32 v102, v68, v69\n" \
"v_cvt_pk_bf16_f32 v103, v70, v71\n" \
"v_pk_mul_f32 v[64:65], v[64:65], v[66:67]\n" \
"v_pk_fma_f32 v[64:65], v[68:69], v[70:71], v[64:65]\n" \
"v_fma_f32 v240, v64, v65, v240\n" \
"v_mfma_f32_16x16x32_bf16 v[52:55], a[252:255], a[172:175], v[52:55]\n" \
"buffer_store_dwordx4 v[100:103], v77, s[56:59], s77 offen offset:2048 sc1\n" \
"s_add_u32 s77, s77, 0x81000\n" \
"v_mfma_f32_16x16x32_bf16 v[60:63], a[248:251], a[184:187], v[60:63]\n" \
"s_waitcnt vmcnt(8) lgkmcnt(6)\n" \
"v_mfma_f32_16x16x32_bf16 v[60:63], a[252:255], a[188:191], v[60:63]\n" \
"s_barrier\n" \
"ds_read_b128 a[248:251], v247 offset:23552\n" \
"v_mfma_f32_16x16x32_bf16 a[32:35], a[192:195], v[72:75], a[32:35]\n" \
"v_exp_f32 v0, v0\n" \
"v_mfma_f32_16x16x32_bf16 a[32:35], a[196:199], v[104:107], a[32:35]\n" \
"v_cvt_pk_bf16_f32 v123, v126, v127\n" \
"v_add_f32 v244, v244, v0\n" \
"v_exp_f32 v1, v1\n" \
"v_mfma_f32_16x16x32_bf16 v[128:131], a[192:195], v[80:83], v[128:131]\n" \
"s_xor_b32 m0, s73, 0x8000\n" \
"v_add_f32 v244, v244, v1\n" \
"v_exp_f32 v2, v2\n" \
"v_mfma_f32_16x16x32_bf16 v[128:131], a[196:199], v[112:115], v[128:131]\n" \
"ds_read_b128 a[252:255], v247 offset:31744\n" \
"global_load_lds_dwordx4 v250, s[36:37] offset:-4096 sc1\n" \
"v_add_f32 v244, v244, v2\n" \
"v_cvt_pk_bf16_f32 v0, v0, v1\n" \
"v_exp_f32 v3, v3\n" \
"v_mfma_f32_16x16x32_bf16 v[160:163], a[192:195], v[88:91], v[160:163]\n" \
"s_add_u32 s70, s82, 0x10000\n" \
"v_add_f32 v244, v244, v3\n" \
"v_exp_f32 v4, v4\n" \
"v_add_u32 v247, s70, v248\n" \
"v_mfma_f32_16x16x32_bf16 v[160:163], a[196:199], v[120:123], v[160:163]\n" \
"ds_read_b128 a[192:195], v252 offset:0\n" \
"v_add_f32 v244, v244, v4\n" \
"v_cvt_pk_bf16_f32 v1, v2, v3\n" \
"v_exp_f32 v5, v5\n" \
"v_mfma_f32_16x16x32_bf16 a[36:39], a[200:203], v[72:75], a[36:39]\n" \
"v_add_f32 v244, v244, v5\n" \
"v_exp_f32 v6, v6\n" \
"v_mfma_f32_16x16x32_bf16 a[36:39], a[204:207], v[104:107], a[36:39]\n" \
"global_load_lds_dwordx4 v250, s[36:37] offset:-3072 sc1\n" \
"v_add_f32 v244, v244, v6\n" \
"v_cvt_pk_bf16_f32 v2, v4, v5\n" \
"v_exp_f32 v7, v7\n" \
"v_mfma_f32_16x16x32_bf16 v[132:135], a[200:203], v[80:83], v[132:135]\n" \
"ds_read_b128 a[196:199], v252 offset:2048\n" \
"v_add_f32 v244, v244, v7\n" \
"v_exp_f32 v8, v8\n" \
"v_mfma_f32_16x16x32_bf16 v[132:135], a[204:207], v[112:115], v[132:135]\n" \
"v_add_f32 v245, v245, v8\n" \
"v_cvt_pk_bf16_f32 v3, v6, v7\n" \
"v_exp_f32 v9, v9\n" \
"v_mfma_f32_16x16x32_bf16 v[164:167], a[200:203], v[88:91], v[164:167]\n" \
"v_add_f32 v245, v245, v9\n" \
"v_exp_f32 v10, v10\n" \
"v_mfma_f32_16x16x32_bf16 v[164:167], a[204:207], v[120:123], v[164:167]\n" \
"ds_read_b128 a[200:203], v252 offset:256\n" \
"v_add_f32 v245, v245, v10\n" \
"v_cvt_pk_bf16_f32 v8, v8, v9\n" \
"v_exp_f32 v11, v11\n" \
"v_mfma_f32_16x16x32_bf16 a[40:43], a[208:211], v[72:75], a[40:43]\n" \
"global_load_lds_dwordx4 v250, s[36:37] offset:-2048 sc1\n" \
"v_add_f32 v245, v245, v11\n" \
"v_exp_f32 v12, v12\n" \
"v_mfma_f32_16x16x32_bf16 a[40:43], a[212:215], v[104:107], a[40:43]\n" \
"v_add_f32 v245, v245, v12\n" \
"v_cvt_pk_bf16_f32 v9, v10, v11\n" \
"v_exp_f32 v13, v13\n" \
"v_mfma_f32_16x16x32_bf16 v[136:139], a[208:211], v[80:83], v[136:139]\n" \
"ds_read_b128 a[204:207], v252 offset:2304\n" \
"v_add_f32 v245, v245, v13\n" \
"v_mfma_f32_16x16x32_bf16 v[136:139], a[212:215], v[112:115], v[136:139]\n" \
"v_exp_f32 v14, v14\n" \
"v_mfma_f32_16x16x32_bf16 v[168:171], a[208:211], v[88:91], v[168:171]\n" \
"global_load_lds_dwordx4 v250, s[36:37] offset:-1024 sc1\n" \
"v_add_f32 v245, v245, v14\n" \
"v_mfma_f32_16x16x32_bf16 v[168:171], a[212:215], v[120:123], v[168:171]\n" \
"ds_read_b128 a[208:211], v252 offset:8192\n" \
"v_cvt_pk_bf16_f32 v10, v12, v13\n" \
"v_mfma_f32_16x16x32_bf16 a[44:47], a[216:219], v[72:75], a[44:47]\n" \
"v_exp_f32 v15, v15\n" \
"v_mfma_f32_16x16x32_bf16 a[44:47], a[220:223], v[104:107], a[44:47]\n" \
"v_add_f32 v245, v245, v15\n" \
"v_exp_f32 v16, v16\n" \
"v_mfma_f32_16x16x32_bf16 v[140:143], a[216:219], v[80:83], v[140:143]\n" \
"ds_read_b128 a[212:215], v252 offset:10240\n" \
"v_add_f32 v242, v242, v16\n" \
"v_mfma_f32_16x16x32_bf16 v[140:143], a[220:223], v[112:115], v[140:143]\n" \
"global_load_lds_dwordx4 v250, s[36:37] offset:0 sc1\n" \
"v_cvt_pk_bf16_f32 v11, v14, v15\n" \
"v_mfma_f32_16x16x32_bf16 v[172:175], a[216:219], v[88:91], v[172:175]\n" \
"s_waitcnt lgkmcnt(6)\n" \
"v_exp_f32 v17, v17\n" \
"v_mfma_f32_16x16x32_bf16 v[172:175], a[220:223], v[120:123], v[172:175]\n" \
"ds_read_b128 a[216:219], v252 offset:8448\n" \
"v_add_f32 v242, v242, v17\n" \
"v_exp_f32 v18, v18\n" \
"v_mfma_f32_16x16x32_bf16 a[48:51], a[224:227], v[72:75], a[48:51]\n" \
"v_add_f32 v242, v242, v18\n" \
"v_cvt_pk_bf16_f32 v16, v16, v17\n" \
"v_mfma_f32_16x16x32_bf16 a[48:51], a[228:231], v[104:107], a[48:51]\n" \
"global_load_lds_dwordx4 v250, s[36:37] offset:1024 sc1\n" \
"v_exp_f32 v19, v19\n" \
"v_mfma_f32_16x16x32_bf16 v[144:147], a[224:227], v[80:83], v[144:147]\n" \
"ds_read_b128 a[220:223], v252 offset:10496\n" \
"v_add_f32 v242, v242, v19\n" \
"v_mfma_f32_16x16x32_bf16 v[144:147], a[228:231], v[112:115], v[144:147]\n" \
"v_exp_f32 v20, v20\n" \
"v_mfma_f32_16x16x32_bf16 v[176:179], a[224:227], v[88:91], v[176:179]\n" \
"v_add_f32 v242, v242, v20\n" \
"v_cvt_pk_bf16_f32 v17, v18, v19\n" \
"v_mfma_f32_16x16x32_bf16 v[176:179], a[228:231], v[120:123], v[176:179]\n" \
"ds_read_b128 a[224:227], v252 offset:4096\n" \
"v_exp_f32 v21, v21\n" \
"v_mfma_f32_16x16x32_bf16 a[52:55], a[232:235], v[72:75], a[52:55]\n" \
"global_load_lds_dwordx4 v250, s[36:37] offset:2048 sc1\n" \
"v_add_f32 v242, v242, v21\n" \
"v_mfma_f32_16x16x32_bf16 a[52:55], a[236:239], v[104:107], a[52:55]\n" \
"v_exp_f32 v22, v22\n" \
"v_mfma_f32_16x16x32_bf16 v[148:151], a[232:235], v[80:83], v[148:151]\n" \
"ds_read_b128 a[228:231], v252 offset:6144\n" \
"v_add_f32 v242, v242, v22\n" \
"v_cvt_pk_bf16_f32 v18, v20, v21\n" \
"v_mfma_f32_16x16x32_bf16 v[148:151], a[236:239], v[112:115], v[148:151]\n" \
"v_exp_f32 v23, v23\n" \
"v_mfma_f32_16x16x32_bf16 v[180:183], a[232:235], v[88:91], v[180:183]\n" \
"global_load_lds_dwordx4 v250, s[36:37] offset:3072 sc1\n" \
"v_add_f32 v242, v242, v23\n" \
"v_mfma_f32_16x16x32_bf16 v[180:183], a[236:239], v[120:123], v[180:183]\n" \
"s_add_u32 s36, s36, 34816\n" \
"ds_read_b128 a[232:235], v252 offset:4352\n" \
"v_exp_f32 v24, v24\n" \
"v_mfma_f32_16x16x32_bf16 a[56:59], a[240:243], v[72:75], a[56:59]\n" \
"v_add_f32 v243, v243, v24\n" \
"v_cvt_pk_bf16_f32 v19, v22, v23\n" \
"v_mfma_f32_16x16x32_bf16 a[56:59], a[244:247], v[104:107], a[56:59]\n" \
"v_exp_f32 v25, v25\n" \
"v_mfma_f32_16x16x32_bf16 v[152:155], a[240:243], v[80:83], v[152:155]\n" \
"ds_read_b128 a[236:239], v252 offset:6400\n" \
"v_add_f32 v243, v243, v25\n" \
"v_exp_f32 v26, v26\n" \
"v_mfma_f32_16x16x32_bf16 v[152:155], a[244:247], v[112:115], v[152:155]\n" \
"v_add_f32 v243, v243, v26\n" \
"v_cvt_pk_bf16_f32 v24, v24, v25\n" \
"v_mfma_f32_16x16x32_bf16 v[184:187], a[240:243], v[88:91], v[184:187]\n" \
"s_load_dwordx8 s[44:51], s[56:57], s79 glc\n" \
"v_exp_f32 v27, v27\n" \
"v_mfma_f32_16x16x32_bf16 v[184:187], a[244:247], v[120:123], v[184:187]\n" \
"ds_read_b128 a[240:243], v252 offset:12288\n" \
"v_add_f32 v243, v243, v27\n" \
"v_exp_f32 v28, v28\n" \
"v_mfma_f32_16x16x32_bf16 a[60:63], a[248:251], v[72:75], a[60:63]\n" \
"v_add_f32 v243, v243, v28\n" \
"v_cvt_pk_bf16_f32 v25, v26, v27\n" \
"v_mfma_f32_16x16x32_bf16 a[60:63], a[252:255], v[104:107], a[60:63]\n" \
"v_exp_f32 v29, v29\n" \
"v_mfma_f32_16x16x32_bf16 v[156:159], a[248:251], v[80:83], v[156:159]\n" \
"ds_read_b128 a[244:247], v252 offset:14336\n" \
"v_add_f32 v243, v243, v29\n" \
"v_exp_f32 v30, v30\n" \
"v_mfma_f32_16x16x32_bf16 v[156:159], a[252:255], v[112:115], v[156:159]\n" \
"v_add_f32 v243, v243, v30\n" \
"v_cvt_pk_bf16_f32 v26, v28, v29\n" \
"v_mfma_f32_16x16x32_bf16 v[188:191], a[248:251], v[88:91], v[188:191]\n" \
"s_waitcnt vmcnt(12) lgkmcnt(6)\n" \
"v_exp_f32 v31, v31\n" \
"v_mfma_f32_16x16x32_bf16 v[188:191], a[252:255], v[120:123], v[188:191]\n" \
"s_barrier\n" \
"ds_read_b128 a[248:251], v252 offset:12544\n" \
"v_add_f32 v243, v243, v31\n" \
"s_mov_b64 exec, 0\n" \
"s_cmp_eq_u32 s61, 0\n" \
"s_cbranch_scc1 Lst1x0_%=\n" \
"s_cmp_eq_u32 s61, 1\n" \
"s_cbranch_scc1 Lst1x1_%=\n" \
"s_cmp_eq_u32 s61, 2\n" \
"s_cbranch_scc1 Lst1x2_%=\n" \
"s_branch Lst1x3_%=\n" \
"Lcb1_%=:\n" \
"s_mov_b64 exec, -1\n" \
"s_waitcnt lgkmcnt(0)\n" \
"s_cmp_lg_u32 s63, 0\n" \
"s_cbranch_scc1 LA1g_%=\n" \
"s_xor_b64 s[52:53], s[44:45], s[42:43]\n" \
"s_xor_b64 s[54:55], s[46:47], s[42:43]\n" \
"s_or_b64 s[52:53], s[52:53], s[54:55]\n" \
"s_xor_b64 s[54:55], s[48:49], s[42:43]\n" \
"s_or_b64 s[52:53], s[52:53], s[54:55]\n" \
"s_xor_b64 s[54:55], s[50:51], s[42:43]\n" \
"s_or_b64 s[52:53], s[52:53], s[54:55]\n" \
"s_and_b64 s[52:53], s[52:53], s[8:9]\n" \
"s_cbranch_scc1 Lrs3_%=\n" \
"Lrr3_%=:\n" \
"s_branch LA1g_%=\n" \
"Lpg7139_%=:\n" \
"s_cbranch_execz Lcb0_%=\n" \
"Lloop2_%=:\n" \
"v_mfma_f32_16x16x32_bf16 a[64:67], a[192:195], v[0:3], a[64:67]\n" \
"v_exp_f32 v64, v64\n" \
"v_mfma_f32_16x16x32_bf16 a[64:67], a[196:199], v[32:35], a[64:67]\n" \
"v_cvt_pk_bf16_f32 v59, v62, v63\n" \
"v_add_f32 v244, v244, v64\n" \
"v_mfma_f32_16x16x32_bf16 a[96:99], a[192:195], v[8:11], a[96:99]\n" \
"s_mov_b32 m0, s73\n" \
"v_exp_f32 v65, v65\n" \
"v_mfma_f32_16x16x32_bf16 a[96:99], a[196:199], v[40:43], a[96:99]\n" \
"ds_read_b128 a[252:255], v247 offset:15360\n" \
"global_load_lds_dwordx4 v250, s[36:37] offset:-4096 sc1\n" \
"v_add_f32 v244, v244, v65\n" \
"v_mfma_f32_16x16x32_bf16 v[128:131], a[192:195], v[16:19], v[128:131]\n" \
"v_exp_f32 v66, v66\n" \
"v_mfma_f32_16x16x32_bf16 v[128:131], a[196:199], v[48:51], v[128:131]\n" \
"v_add_f32 v244, v244, v66\n" \
"v_cvt_pk_bf16_f32 v64, v64, v65\n" \
"v_mfma_f32_16x16x32_bf16 v[160:163], a[192:195], v[24:27], v[160:163]\n" \
"v_exp_f32 v67, v67\n" \
"v_mfma_f32_16x16x32_bf16 v[160:163], a[196:199], v[56:59], v[160:163]\n" \
"ds_read_b128 a[192:195], v246 offset:0\n" \
"global_load_lds_dwordx4 v250, s[36:37] offset:-3072 sc1\n" \
"v_add_f32 v244, v244, v67\n" \
"v_mfma_f32_16x16x32_bf16 a[68:71], a[200:203], v[0:3], a[68:71]\n" \
"v_exp_f32 v68, v68\n" \
"v_mfma_f32_16x16x32_bf16 a[68:71], a[204:207], v[32:35], a[68:71]\n" \
"v_add_f32 v244, v244, v68\n" \
"v_cvt_pk_bf16_f32 v65, v66, v67\n" \
"v_mfma_f32_16x16x32_bf16 a[100:103], a[200:203], v[8:11], a[100:103]\n" \
"v_exp_f32 v69, v69\n" \
"v_mfma_f32_16x16x32_bf16 a[100:103], a[204:207], v[40:43], a[100:103]\n" \
"ds_read_b128 a[196:199], v246 offset:2048\n" \
"v_add_f32 v244, v244, v69\n" \
"v_mfma_f32_16x16x32_bf16 v[132:135], a[200:203], v[16:19], v[132:135]\n" \
"v_exp_f32 v70, v70\n" \
"v_mfma_f32_16x16x32_bf16 v[132:135], a[204:207], v[48:51], v[132:135]\n" \
"v_add_f32 v244, v244, v70\n" \
"v_cvt_pk_bf16_f32 v66, v68, v69\n" \
"v_mfma_f32_16x16x32_bf16 v[164:167], a[200:203], v[24:27], v[164:167]\n" \
"v_exp_f32 v71, v71\n" \
"v_mfma_f32_16x16x32_bf16 v[164:167], a[204:207], v[56:59], v[164:167]\n" \
"ds_read_b128 a[200:203], v246 offset:256\n" \
"global_load_lds_dwordx4 v250, s[36:37] offset:-2048 sc1\n" \
"v_add_f32 v244, v244, v71\n" \
"v_mfma_f32_16x16x32_bf16 a[72:75], a[208:211], v[0:3], a[72:75]\n" \
"v_exp_f32 v72, v72\n" \
"v_mfma_f32_16x16x32_bf16 a[72:75], a[212:215], v[32:35], a[72:75]\n" \
"v_add_f32 v245, v245, v72\n" \
"v_cvt_pk_bf16_f32 v67, v70, v71\n" \
"v_mfma_f32_16x16x32_bf16 a[104:107], a[208:211], v[8:11], a[104:107]\n" \
"v_exp_f32 v73, v73\n" \
"v_mfma_f32_16x16x32_bf16 a[104:107], a[212:215], v[40:43], a[104:107]\n" \
"ds_read_b128 a[204:207], v246 offset:2304\n" \
"global_load_lds_dwordx4 v250, s[36:37] offset:-1024 sc1\n" \
"v_add_f32 v245, v245, v73\n" \
"v_mfma_f32_16x16x32_bf16 v[136:139], a[208:211], v[16:19], v[136:139]\n" \
"v_exp_f32 v74, v74\n" \
"v_mfma_f32_16x16x32_bf16 v[136:139], a[212:215], v[48:51], v[136:139]\n" \
"v_add_f32 v245, v245, v74\n" \
"v_cvt_pk_bf16_f32 v72, v72, v73\n" \
"v_mfma_f32_16x16x32_bf16 v[168:171], a[208:211], v[24:27], v[168:171]\n" \
"v_exp_f32 v75, v75\n" \
"v_mfma_f32_16x16x32_bf16 v[168:171], a[212:215], v[56:59], v[168:171]\n" \
"ds_read_b128 a[208:211], v246 offset:8192\n" \
"v_add_f32 v245, v245, v75\n" \
"v_mfma_f32_16x16x32_bf16 a[76:79], a[216:219], v[0:3], a[76:79]\n" \
"v_exp_f32 v76, v76\n" \
"v_mfma_f32_16x16x32_bf16 a[76:79], a[220:223], v[32:35], a[76:79]\n" \
"v_add_f32 v245, v245, v76\n" \
"v_cvt_pk_bf16_f32 v73, v74, v75\n" \
"v_mfma_f32_16x16x32_bf16 a[108:111], a[216:219], v[8:11], a[108:111]\n" \
"v_exp_f32 v77, v77\n" \
"v_mfma_f32_16x16x32_bf16 a[108:111], a[220:223], v[40:43], a[108:111]\n" \
"ds_read_b128 a[212:215], v246 offset:10240\n" \
"global_load_lds_dwordx4 v250, s[36:37] offset:0 sc1\n" \
"v_add_f32 v245, v245, v77\n" \
"v_mfma_f32_16x16x32_bf16 v[140:143], a[216:219], v[16:19], v[140:143]\n" \
"v_exp_f32 v78, v78\n" \
"v_mfma_f32_16x16x32_bf16 v[140:143], a[220:223], v[48:51], v[140:143]\n" \
"v_add_f32 v245, v245, v78\n" \
"v_cvt_pk_bf16_f32 v74, v76, v77\n" \
"v_mfma_f32_16x16x32_bf16 v[172:175], a[216:219], v[24:27], v[172:175]\n" \
"s_waitcnt lgkmcnt(6)\n" \
"v_exp_f32 v79, v79\n" \
"v_mfma_f32_16x16x32_bf16 v[172:175], a[220:223], v[56:59], v[172:175]\n" \
"ds_read_b128 a[216:219], v246 offset:8448\n" \
"global_load_lds_dwordx4 v250, s[36:37] offset:1024 sc1\n" \
"v_add_f32 v245, v245, v79\n" \
"v_mfma_f32_16x16x32_bf16 a[80:83], a[224:227], v[0:3], a[80:83]\n" \
"v_exp_f32 v80, v80\n" \
"v_mfma_f32_16x16x32_bf16 a[80:83], a[228:231], v[32:35], a[80:83]\n" \
"v_add_f32 v242, v242, v80\n" \
"v_cvt_pk_bf16_f32 v75, v78, v79\n" \
"v_mfma_f32_16x16x32_bf16 a[112:115], a[224:227], v[8:11], a[112:115]\n" \
"v_exp_f32 v81, v81\n" \
"v_mfma_f32_16x16x32_bf16 a[112:115], a[228:231], v[40:43], a[112:115]\n" \
"ds_read_b128 a[220:223], v246 offset:10496\n" \
"v_add_f32 v242, v242, v81\n" \
"v_mfma_f32_16x16x32_bf16 v[144:147], a[224:227], v[16:19], v[144:147]\n" \
"v_exp_f32 v82, v82\n" \
"v_mfma_f32_16x16x32_bf16 v[144:147], a[228:231], v[48:51], v[144:147]\n" \
"v_add_f32 v242, v242, v82\n" \
"v_cvt_pk_bf16_f32 v80, v80, v81\n" \
"v_mfma_f32_16x16x32_bf16 v[176:179], a[224:227], v[24:27], v[176:179]\n" \
"v_exp_f32 v83, v83\n" \
"v_mfma_f32_16x16x32_bf16 v[176:179], a[228:231], v[56:59], v[176:179]\n" \
"ds_read_b128 a[224:227], v246 offset:4096\n" \
"global_load_lds_dwordx4 v250, s[36:37] offset:2048 sc1\n" \
"v_add_f32 v242, v242, v83\n" \
"v_mfma_f32_16x16x32_bf16 a[84:87], a[232:235], v[0:3], a[84:87]\n" \
"v_exp_f32 v84, v84\n" \
"v_mfma_f32_16x16x32_bf16 a[84:87], a[236:239], v[32:35], a[84:87]\n" \
"v_add_f32 v242, v242, v84\n" \
"v_cvt_pk_bf16_f32 v81, v82, v83\n" \
"v_mfma_f32_16x16x32_bf16 a[116:119], a[232:235], v[8:11], a[116:119]\n" \
"v_exp_f32 v85, v85\n" \
"v_mfma_f32_16x16x32_bf16 a[116:119], a[236:239], v[40:43], a[116:119]\n" \
"ds_read_b128 a[228:231], v246 offset:6144\n" \
"global_load_lds_dwordx4 v250, s[36:37] offset:3072 sc1\n" \
"v_add_f32 v242, v242, v85\n" \
"v_mfma_f32_16x16x32_bf16 v[148:151], a[232:235], v[16:19], v[148:151]\n" \
"s_add_u32 s36, s36, 34816\n" \
"v_exp_f32 v86, v86\n" \
"v_mfma_f32_16x16x32_bf16 v[148:151], a[236:239], v[48:51], v[148:151]\n" \
"v_add_f32 v242, v242, v86\n" \
"v_cvt_pk_bf16_f32 v82, v84, v85\n" \
"v_mfma_f32_16x16x32_bf16 v[180:183], a[232:235], v[24:27], v[180:183]\n" \
"v_exp_f32 v87, v87\n" \
"v_mfma_f32_16x16x32_bf16 v[180:183], a[236:239], v[56:59], v[180:183]\n" \
"ds_read_b128 a[232:235], v246 offset:4352\n" \
"v_add_f32 v242, v242, v87\n" \
"v_mfma_f32_16x16x32_bf16 a[88:91], a[240:243], v[0:3], a[88:91]\n" \
"v_exp_f32 v88, v88\n" \
"v_mfma_f32_16x16x32_bf16 a[88:91], a[244:247], v[32:35], a[88:91]\n" \
"v_add_f32 v243, v243, v88\n" \
"v_cvt_pk_bf16_f32 v83, v86, v87\n" \
"v_mfma_f32_16x16x32_bf16 a[120:123], a[240:243], v[8:11], a[120:123]\n" \
"v_exp_f32 v89, v89\n" \
"v_mfma_f32_16x16x32_bf16 a[120:123], a[244:247], v[40:43], a[120:123]\n" \
"ds_read_b128 a[236:239], v246 offset:6400\n" \
"v_add_f32 v243, v243, v89\n" \
"v_mfma_f32_16x16x32_bf16 v[152:155], a[240:243], v[16:19], v[152:155]\n" \
"v_exp_f32 v90, v90\n" \
"v_mfma_f32_16x16x32_bf16 v[152:155], a[244:247], v[48:51], v[152:155]\n" \
"v_add_f32 v243, v243, v90\n" \
"v_cvt_pk_bf16_f32 v88, v88, v89\n" \
"v_mfma_f32_16x16x32_bf16 v[184:187], a[240:243], v[24:27], v[184:187]\n" \
"v_exp_f32 v91, v91\n" \
"v_mfma_f32_16x16x32_bf16 v[184:187], a[244:247], v[56:59], v[184:187]\n" \
"ds_read_b128 a[240:243], v246 offset:12288\n" \
"v_add_f32 v243, v243, v91\n" \
"v_mfma_f32_16x16x32_bf16 a[92:95], a[248:251], v[0:3], a[92:95]\n" \
"v_exp_f32 v92, v92\n" \
"v_mfma_f32_16x16x32_bf16 a[92:95], a[252:255], v[32:35], a[92:95]\n" \
"v_add_f32 v243, v243, v92\n" \
"v_cvt_pk_bf16_f32 v89, v90, v91\n" \
"v_mfma_f32_16x16x32_bf16 a[124:127], a[248:251], v[8:11], a[124:127]\n" \
"v_exp_f32 v93, v93\n" \
"v_mfma_f32_16x16x32_bf16 a[124:127], a[252:255], v[40:43], a[124:127]\n" \
"ds_read_b128 a[244:247], v246 offset:14336\n" \
"v_add_f32 v243, v243, v93\n" \
"v_mfma_f32_16x16x32_bf16 v[156:159], a[248:251], v[16:19], v[156:159]\n" \
"v_exp_f32 v94, v94\n" \
"v_mfma_f32_16x16x32_bf16 v[156:159], a[252:255], v[48:51], v[156:159]\n" \
"v_add_f32 v243, v243, v94\n" \
"v_cvt_pk_bf16_f32 v90, v92, v93\n" \
"v_mfma_f32_16x16x32_bf16 v[188:191], a[248:251], v[24:27], v[188:191]\n" \
"s_waitcnt vmcnt(8) lgkmcnt(6)\n" \
"v_exp_f32 v95, v95\n" \
"v_mfma_f32_16x16x32_bf16 v[188:191], a[252:255], v[56:59], v[188:191]\n" \
"s_barrier\n" \
"ds_read_b128 a[248:251], v246 offset:12544\n" \
"v_add_f32 v243, v243, v95\n" \
"v_mfma_f32_16x16x32_bf16 v[0:3], a[192:195], v[192:195], v[224:227]\n" \
"v_exp_f32 v96, v96\n" \
"v_mfma_f32_16x16x32_bf16 v[0:3], a[196:199], v[196:199], v[0:3]\n" \
"v_cvt_pk_bf16_f32 v91, v94, v95\n" \
"v_add_f32 v244, v244, v96\n" \
"v_mfma_f32_16x16x32_bf16 v[8:11], a[192:195], v[208:211], v[228:231]\n" \
"s_mov_b32 m0, s74\n" \
"v_exp_f32 v97, v97\n" \
"v_mfma_f32_16x16x32_bf16 v[8:11], a[196:199], v[212:215], v[8:11]\n" \
"ds_read_b128 a[252:255], v246 offset:14592\n" \
"global_load_lds_dwordx4 v250, s[38:39] offset:-4096 sc1\n" \
"v_add_f32 v244, v244, v97\n" \
"v_mfma_f32_16x16x32_bf16 v[16:19], a[192:195], a[160:163], v[232:235]\n" \
"v_exp_f32 v98, v98\n" \
"v_mfma_f32_16x16x32_bf16 v[16:19], a[196:199], a[164:167], v[16:19]\n" \
"v_add_f32 v244, v244, v98\n" \
"v_cvt_pk_bf16_f32 v96, v96, v97\n" \
"v_mfma_f32_16x16x32_bf16 v[24:27], a[192:195], a[176:179], v[236:239]\n" \
"v_exp_f32 v99, v99\n" \
"v_mfma_f32_16x16x32_bf16 v[24:27], a[196:199], a[180:183], v[24:27]\n" \
"ds_read_b128 a[192:195], v247 offset:32768\n" \
"global_load_lds_dwordx4 v250, s[38:39] offset:-3072 sc1\n" \
"v_add_f32 v244, v244, v99\n" \
"v_mfma_f32_16x16x32_bf16 v[4:7], a[200:203], v[192:195], v[224:227]\n" \
"v_exp_f32 v100, v100\n" \
"v_mfma_f32_16x16x32_bf16 v[4:7], a[204:207], v[196:199], v[4:7]\n" \
"v_add_f32 v244, v244, v100\n" \
"v_cvt_pk_bf16_f32 v97, v98, v99\n" \
"v_mfma_f32_16x16x32_bf16 v[12:15], a[200:203], v[208:211], v[228:231]\n" \
"v_exp_f32 v101, v101\n" \
"v_mfma_f32_16x16x32_bf16 v[12:15], a[204:207], v[212:215], v[12:15]\n" \
"ds_read_b128 a[196:199], v247 offset:40960\n" \
"v_add_f32 v244, v244, v101\n" \
"v_mfma_f32_16x16x32_bf16 v[20:23], a[200:203], a[160:163], v[232:235]\n" \
"v_exp_f32 v102, v102\n" \
"v_mfma_f32_16x16x32_bf16 v[20:23], a[204:207], a[164:167], v[20:23]\n" \
"v_add_f32 v244, v244, v102\n" \
"v_cvt_pk_bf16_f32 v98, v100, v101\n" \
"v_mfma_f32_16x16x32_bf16 v[28:31], a[200:203], a[176:179], v[236:239]\n" \
"v_exp_f32 v103, v103\n" \
"v_mfma_f32_16x16x32_bf16 v[28:31], a[204:207], a[180:183], v[28:31]\n" \
"ds_read_b128 a[200:203], v247 offset:33792\n" \
"global_load_lds_dwordx4 v250, s[38:39] offset:-2048 sc1\n" \
"v_add_f32 v244, v244, v103\n" \
"v_mfma_f32_16x16x32_bf16 v[32:35], a[208:211], v[192:195], v[224:227]\n" \
"v_exp_f32 v104, v104\n" \
"v_mfma_f32_16x16x32_bf16 v[32:35], a[212:215], v[196:199], v[32:35]\n" \
"v_add_f32 v245, v245, v104\n" \
"v_cvt_pk_bf16_f32 v99, v102, v103\n" \
"v_mfma_f32_16x16x32_bf16 v[40:43], a[208:211], v[208:211], v[228:231]\n" \
"v_exp_f32 v105, v105\n" \
"v_mfma_f32_16x16x32_bf16 v[40:43], a[212:215], v[212:215], v[40:43]\n" \
"ds_read_b128 a[204:207], v247 offset:41984\n" \
"global_load_lds_dwordx4 v250, s[38:39] offset:-1024 sc1\n" \
"v_add_f32 v245, v245, v105\n" \
"v_mfma_f32_16x16x32_bf16 v[48:51], a[208:211], a[160:163], v[232:235]\n" \
"v_exp_f32 v106, v106\n" \
"v_mfma_f32_16x16x32_bf16 v[48:51], a[212:215], a[164:167], v[48:51]\n" \
"v_add_f32 v245, v245, v106\n" \
"v_cvt_pk_bf16_f32 v104, v104, v105\n" \
"v_mfma_f32_16x16x32_bf16 v[56:59], a[208:211], a[176:179], v[236:239]\n" \
"v_exp_f32 v107, v107\n" \
"v_mfma_f32_16x16x32_bf16 v[56:59], a[212:215], a[180:183], v[56:59]\n" \
"ds_read_b128 a[208:211], v247 offset:34816\n" \
"v_add_f32 v245, v245, v107\n" \
"v_mfma_f32_16x16x32_bf16 v[36:39], a[216:219], v[192:195], v[224:227]\n" \
"v_exp_f32 v108, v108\n" \
"v_mfma_f32_16x16x32_bf16 v[36:39], a[220:223], v[196:199], v[36:39]\n" \
"v_add_f32 v245, v245, v108\n" \
"v_cvt_pk_bf16_f32 v105, v106, v107\n" \
"v_mfma_f32_16x16x32_bf16 v[44:47], a[216:219], v[208:211], v[228:231]\n" \
"v_exp_f32 v109, v109\n" \
"v_mfma_f32_16x16x32_bf16 v[44:47], a[220:223], v[212:215], v[44:47]\n" \
"ds_read_b128 a[212:215], v247 offset:43008\n" \
"global_load_lds_dwordx4 v250, s[38:39] offset:0 sc1\n" \
"v_add_f32 v245, v245, v109\n" \
"v_mfma_f32_16x16x32_bf16 v[52:55], a[216:219], a[160:163], v[232:235]\n" \
"v_exp_f32 v110, v110\n" \
"v_mfma_f32_16x16x32_bf16 v[52:55], a[220:223], a[164:167], v[52:55]\n" \
"v_add_f32 v245, v245, v110\n" \
"v_cvt_pk_bf16_f32 v106, v108, v109\n" \
"v_mfma_f32_16x16x32_bf16 v[60:63], a[216:219], a[176:179], v[236:239]\n" \
"s_waitcnt lgkmcnt(6)\n" \
"v_exp_f32 v111, v111\n" \
"v_mfma_f32_16x16x32_bf16 v[60:63], a[220:223], a[180:183], v[60:63]\n" \
"ds_read_b128 a[216:219], v247 offset:35840\n" \
"global_load_lds_dwordx4 v250, s[38:39] offset:1024 sc1\n" \
"v_add_f32 v245, v245, v111\n" \
"v_mfma_f32_16x16x32_bf16 v[0:3], a[224:227], v[200:203], v[0:3]\n" \
"v_exp_f32 v112, v112\n" \
"v_mfma_f32_16x16x32_bf16 v[0:3], a[228:231], v[204:207], v[0:3]\n" \
"v_add_f32 v242, v242, v112\n" \
"v_cvt_pk_bf16_f32 v107, v110, v111\n" \
"v_mfma_f32_16x16x32_bf16 v[8:11], a[224:227], v[216:219], v[8:11]\n" \
"v_exp_f32 v113, v113\n" \
"v_mfma_f32_16x16x32_bf16 v[8:11], a[228:231], v[220:223], v[8:11]\n" \
"ds_read_b128 a[220:223], v247 offset:44032\n" \
"v_add_f32 v242, v242, v113\n" \
"v_mfma_f32_16x16x32_bf16 v[16:19], a[224:227], a[168:171], v[16:19]\n" \
"v_exp_f32 v114, v114\n" \
"v_mfma_f32_16x16x32_bf16 v[16:19], a[228:231], a[172:175], v[16:19]\n" \
"v_add_f32 v242, v242, v114\n" \
"v_cvt_pk_bf16_f32 v112, v112, v113\n" \
"v_mfma_f32_16x16x32_bf16 v[24:27], a[224:227], a[184:187], v[24:27]\n" \
"v_exp_f32 v115, v115\n" \
"v_mfma_f32_16x16x32_bf16 v[24:27], a[228:231], a[188:191], v[24:27]\n" \
"ds_read_b128 a[224:227], v247 offset:36864\n" \
"global_load_lds_dwordx4 v250, s[38:39] offset:2048 sc1\n" \
"v_add_f32 v242, v242, v115\n" \
"v_mfma_f32_16x16x32_bf16 v[4:7], a[232:235], v[200:203], v[4:7]\n" \
"v_exp_f32 v116, v116\n" \
"v_mfma_f32_16x16x32_bf16 v[4:7], a[236:239], v[204:207], v[4:7]\n" \
"v_add_f32 v242, v242, v116\n" \
"v_cvt_pk_bf16_f32 v113, v114, v115\n" \
"v_mfma_f32_16x16x32_bf16 v[12:15], a[232:235], v[216:219], v[12:15]\n" \
"v_exp_f32 v117, v117\n" \
"v_mfma_f32_16x16x32_bf16 v[12:15], a[236:239], v[220:223], v[12:15]\n" \
"ds_read_b128 a[228:231], v247 offset:45056\n" \
"global_load_lds_dwordx4 v250, s[38:39] offset:3072 sc1\n" \
"v_add_f32 v242, v242, v117\n" \
"v_mfma_f32_16x16x32_bf16 v[20:23], a[232:235], a[168:171], v[20:23]\n" \
"s_add_u32 s38, s38, 34816\n" \
"v_exp_f32 v118, v118\n" \
"v_mfma_f32_16x16x32_bf16 v[20:23], a[236:239], a[172:175], v[20:23]\n" \
"v_add_f32 v242, v242, v118\n" \
"v_cvt_pk_bf16_f32 v114, v116, v117\n" \
"v_mfma_f32_16x16x32_bf16 v[28:31], a[232:235], a[184:187], v[28:31]\n" \
"v_exp_f32 v119, v119\n" \
"v_mfma_f32_16x16x32_bf16 v[28:31], a[236:239], a[188:191], v[28:31]\n" \
"ds_read_b128 a[232:235], v247 offset:37888\n" \
"v_add_f32 v242, v242, v119\n" \
"v_mfma_f32_16x16x32_bf16 v[32:35], a[240:243], v[200:203], v[32:35]\n" \
"v_exp_f32 v120, v120\n" \
"v_mfma_f32_16x16x32_bf16 v[32:35], a[244:247], v[204:207], v[32:35]\n" \
"v_add_f32 v243, v243, v120\n" \
"v_cvt_pk_bf16_f32 v115, v118, v119\n" \
"v_mfma_f32_16x16x32_bf16 v[40:43], a[240:243], v[216:219], v[40:43]\n" \
"v_exp_f32 v121, v121\n" \
"v_mfma_f32_16x16x32_bf16 v[40:43], a[244:247], v[220:223], v[40:43]\n" \
"ds_read_b128 a[236:239], v247 offset:46080\n" \
"v_add_f32 v243, v243, v121\n" \
"v_mfma_f32_16x16x32_bf16 v[48:51], a[240:243], a[168:171], v[48:51]\n" \
"v_exp_f32 v122, v122\n" \
"v_mfma_f32_16x16x32_bf16 v[48:51], a[244:247], a[172:175], v[48:51]\n" \
"v_add_f32 v243, v243, v122\n" \
"v_cvt_pk_bf16_f32 v120, v120, v121\n" \
"v_mfma_f32_16x16x32_bf16 v[56:59], a[240:243], a[184:187], v[56:59]\n" \
"v_exp_f32 v123, v123\n" \
"v_mfma_f32_16x16x32_bf16 v[56:59], a[244:247], a[188:191], v[56:59]\n" \
"ds_read_b128 a[240:243], v247 offset:38912\n" \
"v_add_f32 v243, v243, v123\n" \
"v_mfma_f32_16x16x32_bf16 v[36:39], a[248:251], v[200:203], v[36:39]\n" \
"v_exp_f32 v124, v124\n" \
"v_mfma_f32_16x16x32_bf16 v[36:39], a[252:255], v[204:207], v[36:39]\n" \
"v_add_f32 v243, v243, v124\n" \
"Lpg7469_%=:\n" \
"Lst1x0_%=:\n" \
"s_cbranch_execz Lpg9870_%=\n" \
"v_cvt_pk_bf16_f32 v121, v122, v123\n" \
"v_mfma_f32_16x16x32_bf16 v[44:47], a[248:251], v[216:219], v[44:47]\n" \
"v_exp_f32 v125, v125\n" \
"v_mfma_f32_16x16x32_bf16 v[44:47], a[252:255], v[220:223], v[44:47]\n" \
"ds_read_b128 a[244:247], v247 offset:47104\n" \
"v_add_f32 v243, v243, v125\n" \
"v_mfma_f32_16x16x32_bf16 v[52:55], a[248:251], a[168:171], v[52:55]\n" \
"v_exp_f32 v126, v126\n" \
"v_mfma_f32_16x16x32_bf16 v[52:55], a[252:255], a[172:175], v[52:55]\n" \
"v_add_f32 v243, v243, v126\n" \
"v_cvt_pk_bf16_f32 v122, v124, v125\n" \
"v_mfma_f32_16x16x32_bf16 v[60:63], a[248:251], a[184:187], v[60:63]\n" \
"s_waitcnt vmcnt(8) lgkmcnt(6)\n" \
"v_exp_f32 v127, v127\n" \
"v_mfma_f32_16x16x32_bf16 v[60:63], a[252:255], a[188:191], v[60:63]\n" \
"s_barrier\n" \
"ds_read_b128 a[248:251], v247 offset:39936\n" \
"v_add_f32 v243, v243, v127\n" \
"v_mfma_f32_16x16x32_bf16 a[64:67], a[192:195], v[64:67], a[64:67]\n" \
"v_exp_f32 v0, v0\n" \
"v_mfma_f32_16x16x32_bf16 a[64:67], a[196:199], v[96:99], a[64:67]\n" \
"v_cvt_pk_bf16_f32 v123, v126, v127\n" \
"v_add_f32 v244, v244, v0\n" \
"v_mfma_f32_16x16x32_bf16 a[96:99], a[192:195], v[72:75], a[96:99]\n" \
"s_xor_b32 m0, s73, 0x8000\n" \
"v_exp_f32 v1, v1\n" \
"v_mfma_f32_16x16x32_bf16 a[96:99], a[196:199], v[104:107], a[96:99]\n" \
"ds_read_b128 a[252:255], v247 offset:48128\n" \
"global_load_lds_dwordx4 v250, s[36:37] offset:-4096 sc1\n" \
"v_add_f32 v244, v244, v1\n" \
"v_mfma_f32_16x16x32_bf16 v[128:131], a[192:195], v[80:83], v[128:131]\n" \
"v_exp_f32 v2, v2\n" \
"v_mfma_f32_16x16x32_bf16 v[128:131], a[196:199], v[112:115], v[128:131]\n" \
"v_add_f32 v244, v244, v2\n" \
"v_cvt_pk_bf16_f32 v0, v0, v1\n" \
"v_mfma_f32_16x16x32_bf16 v[160:163], a[192:195], v[88:91], v[160:163]\n" \
"v_exp_f32 v3, v3\n" \
"v_mfma_f32_16x16x32_bf16 v[160:163], a[196:199], v[120:123], v[160:163]\n" \
"ds_read_b128 a[192:195], v252 offset:0\n" \
"global_load_lds_dwordx4 v250, s[36:37] offset:-3072 sc1\n" \
"v_add_f32 v244, v244, v3\n" \
"v_mfma_f32_16x16x32_bf16 a[68:71], a[200:203], v[64:67], a[68:71]\n" \
"v_exp_f32 v4, v4\n" \
"v_mfma_f32_16x16x32_bf16 a[68:71], a[204:207], v[96:99], a[68:71]\n" \
"v_add_f32 v244, v244, v4\n" \
"v_cvt_pk_bf16_f32 v1, v2, v3\n" \
"v_mfma_f32_16x16x32_bf16 a[100:103], a[200:203], v[72:75], a[100:103]\n" \
"v_exp_f32 v5, v5\n" \
"v_mfma_f32_16x16x32_bf16 a[100:103], a[204:207], v[104:107], a[100:103]\n" \
"ds_read_b128 a[196:199], v252 offset:2048\n" \
"v_add_f32 v244, v244, v5\n" \
"v_mfma_f32_16x16x32_bf16 v[132:135], a[200:203], v[80:83], v[132:135]\n" \
"v_exp_f32 v6, v6\n" \
"v_mfma_f32_16x16x32_bf16 v[132:135], a[204:207], v[112:115], v[132:135]\n" \
"v_add_f32 v244, v244, v6\n" \
"v_cvt_pk_bf16_f32 v2, v4, v5\n" \
"v_mfma_f32_16x16x32_bf16 v[164:167], a[200:203], v[88:91], v[164:167]\n" \
"v_exp_f32 v7, v7\n" \
"v_mfma_f32_16x16x32_bf16 v[164:167], a[204:207], v[120:123], v[164:167]\n" \
"ds_read_b128 a[200:203], v252 offset:256\n" \
"global_load_lds_dwordx4 v250, s[36:37] offset:-2048 sc1\n" \
"v_add_f32 v244, v244, v7\n" \
"v_mfma_f32_16x16x32_bf16 a[72:75], a[208:211], v[64:67], a[72:75]\n" \
"v_exp_f32 v8, v8\n" \
"v_mfma_f32_16x16x32_bf16 a[72:75], a[212:215], v[96:99], a[72:75]\n" \
"v_add_f32 v245, v245, v8\n" \
"v_cvt_pk_bf16_f32 v3, v6, v7\n" \
"v_mfma_f32_16x16x32_bf16 a[104:107], a[208:211], v[72:75], a[104:107]\n" \
"v_exp_f32 v9, v9\n" \
"v_mfma_f32_16x16x32_bf16 a[104:107], a[212:215], v[104:107], a[104:107]\n" \
"ds_read_b128 a[204:207], v252 offset:2304\n" \
"global_load_lds_dwordx4 v250, s[36:37] offset:-1024 sc1\n" \
"v_add_f32 v245, v245, v9\n" \
"v_mfma_f32_16x16x32_bf16 v[136:139], a[208:211], v[80:83], v[136:139]\n" \
"v_exp_f32 v10, v10\n" \
"v_mfma_f32_16x16x32_bf16 v[136:139], a[212:215], v[112:115], v[136:139]\n" \
"v_add_f32 v245, v245, v10\n" \
"v_cvt_pk_bf16_f32 v8, v8, v9\n" \
"v_mfma_f32_16x16x32_bf16 v[168:171], a[208:211], v[88:91], v[168:171]\n" \
"v_exp_f32 v11, v11\n" \
"v_mfma_f32_16x16x32_bf16 v[168:171], a[212:215], v[120:123], v[168:171]\n" \
"ds_read_b128 a[208:211], v252 offset:8192\n" \
"v_add_f32 v245, v245, v11\n" \
"v_mfma_f32_16x16x32_bf16 a[76:79], a[216:219], v[64:67], a[76:79]\n" \
"v_exp_f32 v12, v12\n" \
"v_mfma_f32_16x16x32_bf16 a[76:79], a[220:223], v[96:99], a[76:79]\n" \
"v_add_f32 v245, v245, v12\n" \
"v_cvt_pk_bf16_f32 v9, v10, v11\n" \
"v_mfma_f32_16x16x32_bf16 a[108:111], a[216:219], v[72:75], a[108:111]\n" \
"v_exp_f32 v13, v13\n" \
"v_mfma_f32_16x16x32_bf16 a[108:111], a[220:223], v[104:107], a[108:111]\n" \
"ds_read_b128 a[212:215], v252 offset:10240\n" \
"global_load_lds_dwordx4 v250, s[36:37] offset:0 sc1\n" \
"v_add_f32 v245, v245, v13\n" \
"v_mfma_f32_16x16x32_bf16 v[140:143], a[216:219], v[80:83], v[140:143]\n" \
"v_exp_f32 v14, v14\n" \
"v_mfma_f32_16x16x32_bf16 v[140:143], a[220:223], v[112:115], v[140:143]\n" \
"v_add_f32 v245, v245, v14\n" \
"v_cvt_pk_bf16_f32 v10, v12, v13\n" \
"v_mfma_f32_16x16x32_bf16 v[172:175], a[216:219], v[88:91], v[172:175]\n" \
"s_waitcnt lgkmcnt(6)\n" \
"v_exp_f32 v15, v15\n" \
"v_mfma_f32_16x16x32_bf16 v[172:175], a[220:223], v[120:123], v[172:175]\n" \
"ds_read_b128 a[216:219], v252 offset:8448\n" \
"global_load_lds_dwordx4 v250, s[36:37] offset:1024 sc1\n" \
"v_add_f32 v245, v245, v15\n" \
"v_mfma_f32_16x16x32_bf16 a[80:83], a[224:227], v[64:67], a[80:83]\n" \
"v_exp_f32 v16, v16\n" \
"v_mfma_f32_16x16x32_bf16 a[80:83], a[228:231], v[96:99], a[80:83]\n" \
"v_add_f32 v242, v242, v16\n" \
"v_cvt_pk_bf16_f32 v11, v14, v15\n" \
"v_mfma_f32_16x16x32_bf16 a[112:115], a[224:227], v[72:75], a[112:115]\n" \
"v_exp_f32 v17, v17\n" \
"v_mfma_f32_16x16x32_bf16 a[112:115], a[228:231], v[104:107], a[112:115]\n" \
"ds_read_b128 a[220:223], v252 offset:10496\n" \
"v_add_f32 v242, v242, v17\n" \
"v_mfma_f32_16x16x32_bf16 v[144:147], a[224:227], v[80:83], v[144:147]\n" \
"v_exp_f32 v18, v18\n" \
"v_mfma_f32_16x16x32_bf16 v[144:147], a[228:231], v[112:115], v[144:147]\n" \
"v_add_f32 v242, v242, v18\n" \
"v_cvt_pk_bf16_f32 v16, v16, v17\n" \
"v_mfma_f32_16x16x32_bf16 v[176:179], a[224:227], v[88:91], v[176:179]\n" \
"v_exp_f32 v19, v19\n" \
"v_mfma_f32_16x16x32_bf16 v[176:179], a[228:231], v[120:123], v[176:179]\n" \
"ds_read_b128 a[224:227], v252 offset:4096\n" \
"global_load_lds_dwordx4 v250, s[36:37] offset:2048 sc1\n" \
"v_add_f32 v242, v242, v19\n" \
"v_mfma_f32_16x16x32_bf16 a[84:87], a[232:235], v[64:67], a[84:87]\n" \
"v_exp_f32 v20, v20\n" \
"v_mfma_f32_16x16x32_bf16 a[84:87], a[236:239], v[96:99], a[84:87]\n" \
"v_add_f32 v242, v242, v20\n" \
"v_cvt_pk_bf16_f32 v17, v18, v19\n" \
"v_mfma_f32_16x16x32_bf16 a[116:119], a[232:235], v[72:75], a[116:119]\n" \
"v_exp_f32 v21, v21\n" \
"v_mfma_f32_16x16x32_bf16 a[116:119], a[236:239], v[104:107], a[116:119]\n" \
"ds_read_b128 a[228:231], v252 offset:6144\n" \
"global_load_lds_dwordx4 v250, s[36:37] offset:3072 sc1\n" \
"v_add_f32 v242, v242, v21\n" \
"v_mfma_f32_16x16x32_bf16 v[148:151], a[232:235], v[80:83], v[148:151]\n" \
"s_add_u32 s36, s36, 34816\n" \
"v_exp_f32 v22, v22\n" \
"v_mfma_f32_16x16x32_bf16 v[148:151], a[236:239], v[112:115], v[148:151]\n" \
"v_add_f32 v242, v242, v22\n" \
"v_cvt_pk_bf16_f32 v18, v20, v21\n" \
"v_mfma_f32_16x16x32_bf16 v[180:183], a[232:235], v[88:91], v[180:183]\n" \
"v_exp_f32 v23, v23\n" \
"v_mfma_f32_16x16x32_bf16 v[180:183], a[236:239], v[120:123], v[180:183]\n" \
"ds_read_b128 a[232:235], v252 offset:4352\n" \
"v_add_f32 v242, v242, v23\n" \
"v_mfma_f32_16x16x32_bf16 a[88:91], a[240:243], v[64:67], a[88:91]\n" \
"v_exp_f32 v24, v24\n" \
"v_mfma_f32_16x16x32_bf16 a[88:91], a[244:247], v[96:99], a[88:91]\n" \
"v_add_f32 v243, v243, v24\n" \
"v_cvt_pk_bf16_f32 v19, v22, v23\n" \
"v_mfma_f32_16x16x32_bf16 a[120:123], a[240:243], v[72:75], a[120:123]\n" \
"v_exp_f32 v25, v25\n" \
"v_mfma_f32_16x16x32_bf16 a[120:123], a[244:247], v[104:107], a[120:123]\n" \
"ds_read_b128 a[236:239], v252 offset:6400\n" \
"v_add_f32 v243, v243, v25\n" \
"v_mfma_f32_16x16x32_bf16 v[152:155], a[240:243], v[80:83], v[152:155]\n" \
"v_exp_f32 v26, v26\n" \
"v_mfma_f32_16x16x32_bf16 v[152:155], a[244:247], v[112:115], v[152:155]\n" \
"v_add_f32 v243, v243, v26\n" \
"v_cvt_pk_bf16_f32 v24, v24, v25\n" \
"v_mfma_f32_16x16x32_bf16 v[184:187], a[240:243], v[88:91], v[184:187]\n" \
"v_exp_f32 v27, v27\n" \
"v_mfma_f32_16x16x32_bf16 v[184:187], a[244:247], v[120:123], v[184:187]\n" \
"ds_read_b128 a[240:243], v252 offset:12288\n" \
"v_add_f32 v243, v243, v27\n" \
"v_mfma_f32_16x16x32_bf16 a[92:95], a[248:251], v[64:67], a[92:95]\n" \
"v_exp_f32 v28, v28\n" \
"v_mfma_f32_16x16x32_bf16 a[92:95], a[252:255], v[96:99], a[92:95]\n" \
"v_add_f32 v243, v243, v28\n" \
"v_cvt_pk_bf16_f32 v25, v26, v27\n" \
"v_mfma_f32_16x16x32_bf16 a[124:127], a[248:251], v[72:75], a[124:127]\n" \
"v_exp_f32 v29, v29\n" \
"v_mfma_f32_16x16x32_bf16 a[124:127], a[252:255], v[104:107], a[124:127]\n" \
"ds_read_b128 a[244:247], v252 offset:14336\n" \
"v_add_f32 v243, v243, v29\n" \
"v_mfma_f32_16x16x32_bf16 v[156:159], a[248:251], v[80:83], v[156:159]\n" \
"v_exp_f32 v30, v30\n" \
"v_mfma_f32_16x16x32_bf16 v[156:159], a[252:255], v[112:115], v[156:159]\n" \
"v_add_f32 v243, v243, v30\n" \
"v_cvt_pk_bf16_f32 v26, v28, v29\n" \
"v_mfma_f32_16x16x32_bf16 v[188:191], a[248:251], v[88:91], v[188:191]\n" \
"s_waitcnt vmcnt(8) lgkmcnt(6)\n" \
"v_exp_f32 v31, v31\n" \
"v_mfma_f32_16x16x32_bf16 v[188:191], a[252:255], v[120:123], v[188:191]\n" \
"s_barrier\n" \
"ds_read_b128 a[248:251], v252 offset:12544\n" \
"v_add_f32 v243, v243, v31\n" \
"LA1g_%=:\n" \
"Lpg7662_%=:\n" \
"s_cbranch_execz Lcb0_%=\n" \
"v_mfma_f32_16x16x32_bf16 v[64:67], a[192:195], v[192:195], v[224:227]\n" \
"v_exp_f32 v32, v32\n" \
"v_mfma_f32_16x16x32_bf16 v[64:67], a[196:199], v[196:199], v[64:67]\n" \
"v_cvt_pk_bf16_f32 v27, v30, v31\n" \
"v_add_f32 v244, v244, v32\n" \
"v_mfma_f32_16x16x32_bf16 v[72:75], a[192:195], v[208:211], v[228:231]\n" \
"s_add_u32 m0, s74, 0x8000\n" \
"v_exp_f32 v33, v33\n" \
"v_mfma_f32_16x16x32_bf16 v[72:75], a[196:199], v[212:215], v[72:75]\n" \
"ds_read_b128 a[252:255], v252 offset:14592\n" \
"global_load_lds_dwordx4 v250, s[38:39] offset:-4096 sc1\n" \
"v_add_f32 v244, v244, v33\n" \
"v_mfma_f32_16x16x32_bf16 v[80:83], a[192:195], a[160:163], v[232:235]\n" \
"v_exp_f32 v34, v34\n" \
"v_mfma_f32_16x16x32_bf16 v[80:83], a[196:199], a[164:167], v[80:83]\n" \
"v_add_f32 v244, v244, v34\n" \
"v_cvt_pk_bf16_f32 v32, v32, v33\n" \
"v_mfma_f32_16x16x32_bf16 v[88:91], a[192:195], a[176:179], v[236:239]\n" \
"v_exp_f32 v35, v35\n" \
"v_mfma_f32_16x16x32_bf16 v[88:91], a[196:199], a[180:183], v[88:91]\n" \
"ds_read_b128 a[192:195], v247 offset:0\n" \
"global_load_lds_dwordx4 v250, s[38:39] offset:-3072 sc1\n" \
"v_add_f32 v244, v244, v35\n" \
"v_mfma_f32_16x16x32_bf16 v[68:71], a[200:203], v[192:195], v[224:227]\n" \
"v_exp_f32 v36, v36\n" \
"v_mfma_f32_16x16x32_bf16 v[68:71], a[204:207], v[196:199], v[68:71]\n" \
"v_add_f32 v244, v244, v36\n" \
"v_cvt_pk_bf16_f32 v33, v34, v35\n" \
"v_mfma_f32_16x16x32_bf16 v[76:79], a[200:203], v[208:211], v[228:231]\n" \
"v_exp_f32 v37, v37\n" \
"v_mfma_f32_16x16x32_bf16 v[76:79], a[204:207], v[212:215], v[76:79]\n" \
"ds_read_b128 a[196:199], v247 offset:8192\n" \
"v_add_f32 v244, v244, v37\n" \
"v_mfma_f32_16x16x32_bf16 v[84:87], a[200:203], a[160:163], v[232:235]\n" \
"v_exp_f32 v38, v38\n" \
"v_mfma_f32_16x16x32_bf16 v[84:87], a[204:207], a[164:167], v[84:87]\n" \
"v_add_f32 v244, v244, v38\n" \
"v_cvt_pk_bf16_f32 v34, v36, v37\n" \
"v_mfma_f32_16x16x32_bf16 v[92:95], a[200:203], a[176:179], v[236:239]\n" \
"v_exp_f32 v39, v39\n" \
"v_mfma_f32_16x16x32_bf16 v[92:95], a[204:207], a[180:183], v[92:95]\n" \
"ds_read_b128 a[200:203], v247 offset:1024\n" \
"global_load_lds_dwordx4 v250, s[38:39] offset:-2048 sc1\n" \
"v_add_f32 v244, v244, v39\n" \
"v_mfma_f32_16x16x32_bf16 v[96:99], a[208:211], v[192:195], v[224:227]\n" \
"v_exp_f32 v40, v40\n" \
"v_mfma_f32_16x16x32_bf16 v[96:99], a[212:215], v[196:199], v[96:99]\n" \
"v_add_f32 v245, v245, v40\n" \
"v_cvt_pk_bf16_f32 v35, v38, v39\n" \
"v_mfma_f32_16x16x32_bf16 v[104:107], a[208:211], v[208:211], v[228:231]\n" \
"v_exp_f32 v41, v41\n" \
"v_mfma_f32_16x16x32_bf16 v[104:107], a[212:215], v[212:215], v[104:107]\n" \
"ds_read_b128 a[204:207], v247 offset:9216\n" \
"global_load_lds_dwordx4 v250, s[38:39] offset:-1024 sc1\n" \
"v_add_f32 v245, v245, v41\n" \
"v_mfma_f32_16x16x32_bf16 v[112:115], a[208:211], a[160:163], v[232:235]\n" \
"v_exp_f32 v42, v42\n" \
"v_mfma_f32_16x16x32_bf16 v[112:115], a[212:215], a[164:167], v[112:115]\n" \
"v_add_f32 v245, v245, v42\n" \
"v_cvt_pk_bf16_f32 v40, v40, v41\n" \
"v_mfma_f32_16x16x32_bf16 v[120:123], a[208:211], a[176:179], v[236:239]\n" \
"v_exp_f32 v43, v43\n" \
"v_mfma_f32_16x16x32_bf16 v[120:123], a[212:215], a[180:183], v[120:123]\n" \
"ds_read_b128 a[208:211], v247 offset:2048\n" \
"v_add_f32 v245, v245, v43\n" \
"v_mfma_f32_16x16x32_bf16 v[100:103], a[216:219], v[192:195], v[224:227]\n" \
"v_exp_f32 v44, v44\n" \
"v_mfma_f32_16x16x32_bf16 v[100:103], a[220:223], v[196:199], v[100:103]\n" \
"v_add_f32 v245, v245, v44\n" \
"v_cvt_pk_bf16_f32 v41, v42, v43\n" \
"v_mfma_f32_16x16x32_bf16 v[108:111], a[216:219], v[208:211], v[228:231]\n" \
"v_exp_f32 v45, v45\n" \
"v_mfma_f32_16x16x32_bf16 v[108:111], a[220:223], v[212:215], v[108:111]\n" \
"ds_read_b128 a[212:215], v247 offset:10240\n" \
"global_load_lds_dwordx4 v250, s[38:39] offset:0 sc1\n" \
"v_add_f32 v245, v245, v45\n" \
"v_mfma_f32_16x16x32_bf16 v[116:119], a[216:219], a[160:163], v[232:235]\n" \
"v_exp_f32 v46, v46\n" \
"v_mfma_f32_16x16x32_bf16 v[116:119], a[220:223], a[164:167], v[116:119]\n" \
"v_add_f32 v245, v245, v46\n" \
"v_cvt_pk_bf16_f32 v42, v44, v45\n" \
"v_mfma_f32_16x16x32_bf16 v[124:127], a[216:219], a[176:179], v[236:239]\n" \
"s_waitcnt lgkmcnt(6)\n" \
"v_exp_f32 v47, v47\n" \
"v_mfma_f32_16x16x32_bf16 v[124:127], a[220:223], a[180:183], v[124:127]\n" \
"ds_read_b128 a[216:219], v247 offset:3072\n" \
"global_load_lds_dwordx4 v250, s[38:39] offset:1024 sc1\n" \
"v_add_f32 v245, v245, v47\n" \
"v_mfma_f32_16x16x32_bf16 v[64:67], a[224:227], v[200:203], v[64:67]\n" \
"v_exp_f32 v48, v48\n" \
"v_mfma_f32_16x16x32_bf16 v[64:67], a[228:231], v[204:207], v[64:67]\n" \
"v_add_f32 v242, v242, v48\n" \
"v_cvt_pk_bf16_f32 v43, v46, v47\n" \
"v_mfma_f32_16x16x32_bf16 v[72:75], a[224:227], v[216:219], v[72:75]\n" \
"v_exp_f32 v49, v49\n" \
"v_mfma_f32_16x16x32_bf16 v[72:75], a[228:231], v[220:223], v[72:75]\n" \
"ds_read_b128 a[220:223], v247 offset:11264\n" \
"v_add_f32 v242, v242, v49\n" \
"v_exp_f32 v50, v50\n" \
"v_mfma_f32_16x16x32_bf16 v[80:83], a[224:227], a[168:171], v[80:83]\n" \
"v_add_f32 v242, v242, v50\n" \
"v_cvt_pk_bf16_f32 v48, v48, v49\n" \
"v_mfma_f32_16x16x32_bf16 v[80:83], a[228:231], a[172:175], v[80:83]\n" \
"v_exp_f32 v51, v51\n" \
"v_mfma_f32_16x16x32_bf16 v[88:91], a[224:227], a[184:187], v[88:91]\n" \
"v_add_f32 v242, v242, v51\n" \
"v_exp_f32 v52, v52\n" \
"v_mfma_f32_16x16x32_bf16 v[88:91], a[228:231], a[188:191], v[88:91]\n" \
"ds_read_b128 a[224:227], v247 offset:4096\n" \
"global_load_lds_dwordx4 v250, s[38:39] offset:2048 sc1\n" \
"v_mfma_f32_16x16x32_bf16 v[68:71], a[232:235], v[200:203], v[68:71]\n" \
"v_add_f32 v242, v242, v52\n" \
"v_cvt_pk_bf16_f32 v49, v50, v51\n" \
"v_mfma_f32_16x16x32_bf16 v[68:71], a[236:239], v[204:207], v[68:71]\n" \
"v_exp_f32 v53, v53\n" \
"v_mfma_f32_16x16x32_bf16 v[76:79], a[232:235], v[216:219], v[76:79]\n" \
"v_add_f32 v242, v242, v53\n" \
"v_exp_f32 v54, v54\n" \
"v_mfma_f32_16x16x32_bf16 v[76:79], a[236:239], v[220:223], v[76:79]\n" \
"ds_read_b128 a[228:231], v247 offset:12288\n" \
"global_load_lds_dwordx4 v250, s[38:39] offset:3072 sc1\n" \
"v_mfma_f32_16x16x32_bf16 v[84:87], a[232:235], a[168:171], v[84:87]\n" \
"s_add_u32 s38, s38, 34816\n" \
"s_sub_u32 s76, s76, 1\n" \
"s_cmp_lg_u32 s76, 0\n" \
"v_mfma_f32_16x16x32_bf16 v[84:87], a[236:239], a[172:175], v[84:87]\n" \
"v_add_f32 v242, v242, v54\n" \
"v_cvt_pk_bf16_f32 v50, v52, v53\n" \
"v_mfma_f32_16x16x32_bf16 v[92:95], a[232:235], a[184:187], v[92:95]\n" \
"v_exp_f32 v55, v55\n" \
"v_mfma_f32_16x16x32_bf16 v[92:95], a[236:239], a[188:191], v[92:95]\n" \
"ds_read_b128 a[232:235], v247 offset:5120\n" \
"v_add_f32 v242, v242, v55\n" \
"v_mfma_f32_16x16x32_bf16 v[96:99], a[240:243], v[200:203], v[96:99]\n" \
"v_exp_f32 v56, v56\n" \
"v_mfma_f32_16x16x32_bf16 v[96:99], a[244:247], v[204:207], v[96:99]\n" \
"v_add_f32 v243, v243, v56\n" \
"v_cvt_pk_bf16_f32 v51, v54, v55\n" \
"v_mfma_f32_16x16x32_bf16 v[104:107], a[240:243], v[216:219], v[104:107]\n" \
"v_exp_f32 v57, v57\n" \
"v_mfma_f32_16x16x32_bf16 v[104:107], a[244:247], v[220:223], v[104:107]\n" \
"ds_read_b128 a[236:239], v247 offset:13312\n" \
"v_add_f32 v243, v243, v57\n" \
"v_mfma_f32_16x16x32_bf16 v[112:115], a[240:243], a[168:171], v[112:115]\n" \
"v_exp_f32 v58, v58\n" \
"v_mfma_f32_16x16x32_bf16 v[112:115], a[244:247], a[172:175], v[112:115]\n" \
"v_add_f32 v243, v243, v58\n" \
"v_cvt_pk_bf16_f32 v56, v56, v57\n" \
"v_mfma_f32_16x16x32_bf16 v[120:123], a[240:243], a[184:187], v[120:123]\n" \
"v_exp_f32 v59, v59\n" \
"v_mfma_f32_16x16x32_bf16 v[120:123], a[244:247], a[188:191], v[120:123]\n" \
"ds_read_b128 a[240:243], v247 offset:6144\n" \
"v_add_f32 v243, v243, v59\n" \
"v_mfma_f32_16x16x32_bf16 v[100:103], a[248:251], v[200:203], v[100:103]\n" \
"v_exp_f32 v60, v60\n" \
"v_mfma_f32_16x16x32_bf16 v[100:103], a[252:255], v[204:207], v[100:103]\n" \
"v_add_f32 v243, v243, v60\n" \
"v_cvt_pk_bf16_f32 v57, v58, v59\n" \
"v_mfma_f32_16x16x32_bf16 v[108:111], a[248:251], v[216:219], v[108:111]\n" \
"v_exp_f32 v61, v61\n" \
"v_mfma_f32_16x16x32_bf16 v[108:111], a[252:255], v[220:223], v[108:111]\n" \
"ds_read_b128 a[244:247], v247 offset:14336\n" \
"v_add_f32 v243, v243, v61\n" \
"v_mfma_f32_16x16x32_bf16 v[116:119], a[248:251], a[168:171], v[116:119]\n" \
"v_exp_f32 v62, v62\n" \
"v_mfma_f32_16x16x32_bf16 v[116:119], a[252:255], a[172:175], v[116:119]\n" \
"v_add_f32 v243, v243, v62\n" \
"v_cvt_pk_bf16_f32 v58, v60, v61\n" \
"v_mfma_f32_16x16x32_bf16 v[124:127], a[248:251], a[184:187], v[124:127]\n" \
"s_waitcnt vmcnt(8) lgkmcnt(6)\n" \
"v_exp_f32 v63, v63\n" \
"v_mfma_f32_16x16x32_bf16 v[124:127], a[252:255], a[188:191], v[124:127]\n" \
"s_barrier\n" \
"ds_read_b128 a[248:251], v247 offset:7168\n" \
"v_add_f32 v243, v243, v63\n" \
"s_cbranch_scc1 Lloop2_%=\n" \
"v_mfma_f32_16x16x32_bf16 a[64:67], a[192:195], v[0:3], a[64:67]\n" \
"v_subrev_u32 v246, s82, v246\n" \
"v_exp_f32 v64, v64\n" \
"v_mfma_f32_16x16x32_bf16 a[64:67], a[196:199], v[32:35], a[64:67]\n" \
"s_memrealtime s[44:45]\n" \
"v_cvt_pk_bf16_f32 v59, v62, v63\n" \
"v_add_f32 v244, v244, v64\n" \
"v_exp_f32 v65, v65\n" \
"v_mfma_f32_16x16x32_bf16 a[96:99], a[192:195], v[8:11], a[96:99]\n" \
"v_mov_b32 v208, v241\n" \
"v_add_f32 v244, v244, v65\n" \
"v_exp_f32 v66, v66\n" \
"v_mfma_f32_16x16x32_bf16 a[96:99], a[196:199], v[40:43], a[96:99]\n" \
"ds_read_b128 a[252:255], v247 offset:15360\n" \
"v_add_f32 v244, v244, v66\n" \
"v_cvt_pk_bf16_f32 v64, v64, v65\n" \
"v_exp_f32 v67, v67\n" \
"v_mfma_f32_16x16x32_bf16 v[128:131], a[192:195], v[16:19], v[128:131]\n" \
"v_add_f32 v244, v244, v67\n" \
"v_exp_f32 v68, v68\n" \
"v_permlane16_swap_b32 v208, v241\n" \
"v_add_f32 v241, v241, v208\n" \
"v_mov_b32 v208, v241\n" \
"v_mfma_f32_16x16x32_bf16 v[128:131], a[196:199], v[48:51], v[128:131]\n" \
"v_add_f32 v244, v244, v68\n" \
"v_cvt_pk_bf16_f32 v65, v66, v67\n" \
"v_exp_f32 v69, v69\n" \
"v_mfma_f32_16x16x32_bf16 v[160:163], a[192:195], v[24:27], v[160:163]\n" \
"v_add_f32 v244, v244, v69\n" \
"v_exp_f32 v70, v70\n" \
"v_permlane32_swap_b32 v208, v241\n" \
"v_add_f32 v241, v241, v208\n" \
"v_rcp_f32 v208, v241\n" \
"v_mul_f32 v241, 0x4d800000, v241\n" \
"v_add_u32 v209, 0x1c90080, v249\n" \
"v_cndmask_b32 v209, v249, v209, s[16:17]\n" \
"v_accvgpr_read_b32 v192, a32\n" \
"v_accvgpr_read_b32 v193, a33\n" \
"v_accvgpr_read_b32 v194, a34\n" \
"v_accvgpr_read_b32 v195, a35\n" \
"v_accvgpr_read_b32 v196, a36\n" \
"v_accvgpr_read_b32 v197, a37\n" \
"v_accvgpr_read_b32 v198, a38\n" \
"v_accvgpr_read_b32 v199, a39\n" \
"v_pk_mul_f32 v[192:193], v[192:193], v[208:209] op_sel_hi:[1,0]\n" \
"v_pk_mul_f32 v[194:195], v[194:195], v[208:209] op_sel_hi:[1,0]\n" \
"v_pk_mul_f32 v[196:197], v[196:197], v[208:209] op_sel_hi:[1,0]\n" \
"v_pk_mul_f32 v[198:199], v[198:199], v[208:209] op_sel_hi:[1,0]\n" \
"v_cvt_pk_bf16_f32 v200, v192, v193\n" \
"v_cvt_pk_bf16_f32 v201, v194, v195\n" \
"v_mfma_f32_16x16x32_bf16 v[160:163], a[196:199], v[56:59], v[160:163]\n" \
"ds_read_b128 a[192:195], v246 offset:0\n" \
"v_add_f32 v244, v244, v70\n" \
"v_cvt_pk_bf16_f32 v66, v68, v69\n" \
"v_cvt_pk_bf16_f32 v202, v196, v197\n" \
"v_cvt_pk_bf16_f32 v203, v198, v199\n" \
"v_pk_mul_f32 v[192:193], v[192:193], v[194:195]\n" \
"v_pk_fma_f32 v[192:193], v[196:197], v[198:199], v[192:193]\n" \
"v_fma_f32 v241, v192, v193, v241\n" \
"v_mfma_f32_16x16x32_bf16 a[68:71], a[200:203], v[0:3], a[68:71]\n" \
"v_exp_f32 v71, v71\n" \
"v_mfma_f32_16x16x32_bf16 a[68:71], a[204:207], v[32:35], a[68:71]\n" \
"s_waitcnt lgkmcnt(0)\n" \
"v_add_f32 v244, v244, v71\n" \
"v_exp_f32 v72, v72\n" \
"v_mfma_f32_16x16x32_bf16 a[100:103], a[200:203], v[8:11], a[100:103]\n" \
"s_barrier\n" \
"v_add_f32 v245, v245, v72\n" \
"v_cvt_pk_bf16_f32 v67, v70, v71\n" \
"v_mfma_f32_16x16x32_bf16 a[100:103], a[204:207], v[40:43], a[100:103]\n" \
"s_and_b32 s46, %1, 7\n" \
"ds_read_b128 a[196:199], v246 offset:2048\n" \
"s_lshl_b32 s46, s46, 2\n" \
"v_mfma_f32_16x16x32_bf16 v[132:135], a[200:203], v[16:19], v[132:135]\n" \
"s_lshr_b32 s47, %1, 6\n" \
"s_add_u32 s46, s46, s47\n" \
"s_lshl_b32 s46, s46, 3\n" \
"v_mfma_f32_16x16x32_bf16 v[132:135], a[204:207], v[48:51], v[132:135]\n" \
"s_add_u32 s46, s46, 65593856\n" \
"s_sub_u32 s45, s44, s12\n" \
"s_mul_i32 s45, s45, 3\n" \
"v_mfma_f32_16x16x32_bf16 v[164:167], a[200:203], v[24:27], v[164:167]\n" \
"s_lshl_b32 s44, s44, 1\n" \
"s_add_u32 s44, s44, s45\n" \
"v_mov_b32 v211, s44\n" \
"v_mfma_f32_16x16x32_bf16 v[164:167], a[204:207], v[56:59], v[164:167]\n" \
"s_mov_b32 m0, s74\n" \
"ds_read_b128 a[200:203], v246 offset:256\n" \
"v_xor_b32 v210, s60, v211\n" \
"v_mfma_f32_16x16x32_bf16 a[72:75], a[208:211], v[0:3], a[72:75]\n" \
"global_load_lds_dwordx4 v250, s[38:39] offset:-4096 sc1\n" \
"v_lshlrev_b32 v212, 28, s61\n" \
"v_mfma_f32_16x16x32_bf16 a[72:75], a[212:215], v[32:35], a[72:75]\n" \
"s_cmp_eq_u32 s63, 3\n" \
"s_cselect_b32 s46, s46, 0x20000000\n" \
"v_exp_f32 v73, v73\n" \
"v_mfma_f32_16x16x32_bf16 a[104:107], a[208:211], v[8:11], a[104:107]\n" \
"global_load_lds_dwordx4 v250, s[38:39] offset:-3072 sc1\n" \
"global_load_lds_dwordx4 v250, s[38:39] offset:-2048 sc1\n" \
"v_mfma_f32_16x16x32_bf16 a[104:107], a[212:215], v[40:43], a[104:107]\n" \
"ds_read_b128 a[204:207], v246 offset:2304\n" \
"v_add_f32 v245, v245, v73\n" \
"v_mfma_f32_16x16x32_bf16 v[136:139], a[208:211], v[16:19], v[136:139]\n" \
"v_exp_f32 v74, v74\n" \
"v_mfma_f32_16x16x32_bf16 v[136:139], a[212:215], v[48:51], v[136:139]\n" \
"global_load_lds_dwordx4 v250, s[38:39] offset:-1024 sc1\n" \
"v_add_f32 v245, v245, v74\n" \
"v_cvt_pk_bf16_f32 v72, v72, v73\n" \
"v_mfma_f32_16x16x32_bf16 v[168:171], a[208:211], v[24:27], v[168:171]\n" \
"v_exp_f32 v75, v75\n" \
"v_mfma_f32_16x16x32_bf16 v[168:171], a[212:215], v[56:59], v[168:171]\n" \
"ds_read_b128 a[208:211], v246 offset:8192\n" \
"v_add_f32 v245, v245, v75\n" \
"v_mfma_f32_16x16x32_bf16 a[76:79], a[216:219], v[0:3], a[76:79]\n" \
"global_load_lds_dwordx4 v250, s[38:39] offset:0 sc1\n" \
"v_exp_f32 v76, v76\n" \
"v_mfma_f32_16x16x32_bf16 a[76:79], a[220:223], v[32:35], a[76:79]\n" \
"v_add_f32 v245, v245, v76\n" \
"v_cvt_pk_bf16_f32 v73, v74, v75\n" \
"v_mfma_f32_16x16x32_bf16 a[108:111], a[216:219], v[8:11], a[108:111]\n" \
"v_exp_f32 v77, v77\n" \
"v_mfma_f32_16x16x32_bf16 a[108:111], a[220:223], v[40:43], a[108:111]\n" \
"ds_read_b128 a[212:215], v246 offset:10240\n" \
"global_load_lds_dwordx4 v250, s[38:39] offset:1024 sc1\n" \
"v_add_f32 v245, v245, v77\n" \
"v_mfma_f32_16x16x32_bf16 v[140:143], a[216:219], v[16:19], v[140:143]\n" \
"v_exp_f32 v78, v78\n" \
"v_mfma_f32_16x16x32_bf16 v[140:143], a[220:223], v[48:51], v[140:143]\n" \
"v_add_f32 v245, v245, v78\n" \
"v_cvt_pk_bf16_f32 v74, v76, v77\n" \
"v_mfma_f32_16x16x32_bf16 v[172:175], a[216:219], v[24:27], v[172:175]\n" \
"s_waitcnt lgkmcnt(6)\n" \
"global_load_lds_dwordx4 v250, s[38:39] offset:2048 sc1\n" \
"v_exp_f32 v79, v79\n" \
"v_mfma_f32_16x16x32_bf16 v[172:175], a[220:223], v[56:59], v[172:175]\n" \
"ds_read_b128 a[216:219], v246 offset:8448\n" \
"v_add_f32 v245, v245, v79\n" \
"v_mfma_f32_16x16x32_bf16 a[80:83], a[224:227], v[0:3], a[80:83]\n" \
"v_exp_f32 v80, v80\n" \
"v_mfma_f32_16x16x32_bf16 a[80:83], a[228:231], v[32:35], a[80:83]\n" \
"global_load_lds_dwordx4 v250, s[38:39] offset:3072 sc1\n" \
"v_add_f32 v242, v242, v80\n" \
"v_cvt_pk_bf16_f32 v75, v78, v79\n" \
"v_mfma_f32_16x16x32_bf16 a[112:115], a[224:227], v[8:11], a[112:115]\n" \
"v_exp_f32 v81, v81\n" \
"v_mfma_f32_16x16x32_bf16 a[112:115], a[228:231], v[40:43], a[112:115]\n" \
"ds_read_b128 a[220:223], v246 offset:10496\n" \
"v_add_f32 v242, v242, v81\n" \
"v_mfma_f32_16x16x32_bf16 v[144:147], a[224:227], v[16:19], v[144:147]\n" \
"v_exp_f32 v82, v82\n" \
"v_mfma_f32_16x16x32_bf16 v[144:147], a[228:231], v[48:51], v[144:147]\n" \
"v_add_f32 v242, v242, v82\n" \
"v_cvt_pk_bf16_f32 v80, v80, v81\n" \
"v_mfma_f32_16x16x32_bf16 v[176:179], a[224:227], v[24:27], v[176:179]\n" \
"v_exp_f32 v83, v83\n" \
"v_mfma_f32_16x16x32_bf16 v[176:179], a[228:231], v[56:59], v[176:179]\n" \
"ds_read_b128 a[224:227], v246 offset:4096\n" \
"v_add_f32 v242, v242, v83\n" \
"v_exp_f32 v84, v84\n" \
"v_mfma_f32_16x16x32_bf16 a[84:87], a[232:235], v[0:3], a[84:87]\n" \
"buffer_store_dwordx4 v[200:203], v209, s[56:59], s77 offen offset:0 sc1\n" \
"v_add_f32 v242, v242, v84\n" \
"v_cvt_pk_bf16_f32 v81, v82, v83\n" \
"v_accvgpr_read_b32 v192, a40\n" \
"v_accvgpr_read_b32 v193, a41\n" \
"v_accvgpr_read_b32 v194, a42\n" \
"v_accvgpr_read_b32 v195, a43\n" \
"v_accvgpr_read_b32 v196, a44\n" \
"v_accvgpr_read_b32 v197, a45\n" \
"v_accvgpr_read_b32 v198, a46\n" \
"v_accvgpr_read_b32 v199, a47\n" \
"v_pk_mul_f32 v[192:193], v[192:193], v[208:209] op_sel_hi:[1,0]\n" \
"v_pk_mul_f32 v[194:195], v[194:195], v[208:209] op_sel_hi:[1,0]\n" \
"v_pk_mul_f32 v[196:197], v[196:197], v[208:209] op_sel_hi:[1,0]\n" \
"v_pk_mul_f32 v[198:199], v[198:199], v[208:209] op_sel_hi:[1,0]\n" \
"v_cvt_pk_bf16_f32 v204, v192, v193\n" \
"v_cvt_pk_bf16_f32 v205, v194, v195\n" \
"v_cvt_pk_bf16_f32 v206, v196, v197\n" \
"v_cvt_pk_bf16_f32 v207, v198, v199\n" \
"v_pk_mul_f32 v[192:193], v[192:193], v[194:195]\n" \
"v_pk_fma_f32 v[192:193], v[196:197], v[198:199], v[192:193]\n" \
"v_fma_f32 v241, v192, v193, v241\n" \
"v_mfma_f32_16x16x32_bf16 a[84:87], a[236:239], v[32:35], a[84:87]\n" \
"buffer_store_dwordx2 v[210:211], v212, s[56:59], s46 offen sc1\n" \
"buffer_store_dwordx4 v[204:207], v209, s[56:59], s77 offen offset:2048 sc1\n" \
"s_add_u32 s77, s77, 0x1000\n" \
"v_accvgpr_read_b32 v192, a48\n" \
"v_accvgpr_read_b32 v193, a49\n" \
"v_accvgpr_read_b32 v194, a50\n" \
"v_accvgpr_read_b32 v195, a51\n" \
"v_mfma_f32_16x16x32_bf16 a[116:119], a[232:235], v[8:11], a[116:119]\n" \
"v_accvgpr_read_b32 v196, a52\n" \
"v_accvgpr_read_b32 v197, a53\n" \
"v_accvgpr_read_b32 v198, a54\n" \
"v_accvgpr_read_b32 v199, a55\n" \
"v_exp_f32 v85, v85\n" \
"v_pk_mul_f32 v[192:193], v[192:193], v[208:209] op_sel_hi:[1,0]\n" \
"v_pk_mul_f32 v[194:195], v[194:195], v[208:209] op_sel_hi:[1,0]\n" \
"v_pk_mul_f32 v[196:197], v[196:197], v[208:209] op_sel_hi:[1,0]\n" \
"v_pk_mul_f32 v[198:199], v[198:199], v[208:209] op_sel_hi:[1,0]\n" \
"v_cvt_pk_bf16_f32 v200, v192, v193\n" \
"v_cvt_pk_bf16_f32 v201, v194, v195\n" \
"v_cvt_pk_bf16_f32 v202, v196, v197\n" \
"v_cvt_pk_bf16_f32 v203, v198, v199\n" \
"v_pk_mul_f32 v[192:193], v[192:193], v[194:195]\n" \
"v_pk_fma_f32 v[192:193], v[196:197], v[198:199], v[192:193]\n" \
"v_fma_f32 v241, v192, v193, v241\n" \
"v_mfma_f32_16x16x32_bf16 a[116:119], a[236:239], v[40:43], a[116:119]\n" \
"buffer_store_dwordx4 v[200:203], v209, s[56:59], s77 offen offset:0 sc1\n" \
"ds_read_b128 a[228:231], v246 offset:6144\n" \
"v_add_f32 v242, v242, v85\n" \
"v_accvgpr_read_b32 v192, a56\n" \
"v_accvgpr_read_b32 v193, a57\n" \
"v_accvgpr_read_b32 v194, a58\n" \
"v_accvgpr_read_b32 v195, a59\n" \
"v_accvgpr_read_b32 v196, a60\n" \
"v_accvgpr_read_b32 v197, a61\n" \
"v_accvgpr_read_b32 v198, a62\n" \
"v_accvgpr_read_b32 v199, a63\n" \
"v_pk_mul_f32 v[192:193], v[192:193], v[208:209] op_sel_hi:[1,0]\n" \
"v_pk_mul_f32 v[194:195], v[194:195], v[208:209] op_sel_hi:[1,0]\n" \
"v_pk_mul_f32 v[196:197], v[196:197], v[208:209] op_sel_hi:[1,0]\n" \
"v_pk_mul_f32 v[198:199], v[198:199], v[208:209] op_sel_hi:[1,0]\n" \
"v_cvt_pk_bf16_f32 v204, v192, v193\n" \
"v_cvt_pk_bf16_f32 v205, v194, v195\n" \
"v_cvt_pk_bf16_f32 v206, v196, v197\n" \
"v_cvt_pk_bf16_f32 v207, v198, v199\n" \
"v_pk_mul_f32 v[192:193], v[192:193], v[194:195]\n" \
"v_pk_fma_f32 v[192:193], v[196:197], v[198:199], v[192:193]\n" \
"v_fma_f32 v241, v192, v193, v241\n" \
"v_mfma_f32_16x16x32_bf16 v[148:151], a[232:235], v[16:19], v[148:151]\n" \
"buffer_store_dwordx4 v[204:207], v209, s[56:59], s77 offen offset:2048 sc1\n" \
"v_exp_f32 v86, v86\n" \
"s_add_u32 s77, s77, 0xfff7d000\n" \
"v_mfma_f32_16x16x32_bf16 v[148:151], a[236:239], v[48:51], v[148:151]\n" \
"v_add_f32 v242, v242, v86\n" \
"v_cvt_pk_bf16_f32 v82, v84, v85\n" \
"v_mfma_f32_16x16x32_bf16 v[180:183], a[232:235], v[24:27], v[180:183]\n" \
"v_exp_f32 v87, v87\n" \
"v_mfma_f32_16x16x32_bf16 v[180:183], a[236:239], v[56:59], v[180:183]\n" \
"ds_read_b128 a[232:235], v246 offset:4352\n" \
"v_add_f32 v242, v242, v87\n" \
"v_mfma_f32_16x16x32_bf16 a[88:91], a[240:243], v[0:3], a[88:91]\n" \
"v_exp_f32 v88, v88\n" \
"v_mfma_f32_16x16x32_bf16 a[88:91], a[244:247], v[32:35], a[88:91]\n" \
"v_add_f32 v243, v243, v88\n" \
"v_cvt_pk_bf16_f32 v83, v86, v87\n" \
"v_mfma_f32_16x16x32_bf16 a[120:123], a[240:243], v[8:11], a[120:123]\n" \
"v_exp_f32 v89, v89\n" \
"v_mfma_f32_16x16x32_bf16 a[120:123], a[244:247], v[40:43], a[120:123]\n" \
"ds_read_b128 a[236:239], v246 offset:6400\n" \
"v_add_f32 v243, v243, v89\n" \
"v_mfma_f32_16x16x32_bf16 v[152:155], a[240:243], v[16:19], v[152:155]\n" \
"v_exp_f32 v90, v90\n" \
"v_mfma_f32_16x16x32_bf16 v[152:155], a[244:247], v[48:51], v[152:155]\n" \
"Lpg8094_%=:\n" \
"Lst1x1_%=:\n" \
"s_cbranch_execz Lpg10432_%=\n" \
"v_add_f32 v243, v243, v90\n" \
"v_cvt_pk_bf16_f32 v88, v88, v89\n" \
"v_mfma_f32_16x16x32_bf16 v[184:187], a[240:243], v[24:27], v[184:187]\n" \
"v_exp_f32 v91, v91\n" \
"v_mfma_f32_16x16x32_bf16 v[184:187], a[244:247], v[56:59], v[184:187]\n" \
"ds_read_b128 a[240:243], v246 offset:12288\n" \
"v_add_f32 v243, v243, v91\n" \
"v_mfma_f32_16x16x32_bf16 a[92:95], a[248:251], v[0:3], a[92:95]\n" \
"v_exp_f32 v92, v92\n" \
"v_mfma_f32_16x16x32_bf16 a[92:95], a[252:255], v[32:35], a[92:95]\n" \
"v_add_f32 v243, v243, v92\n" \
"v_cvt_pk_bf16_f32 v89, v90, v91\n" \
"v_mfma_f32_16x16x32_bf16 a[124:127], a[248:251], v[8:11], a[124:127]\n" \
"v_exp_f32 v93, v93\n" \
"v_mfma_f32_16x16x32_bf16 a[124:127], a[252:255], v[40:43], a[124:127]\n" \
"ds_read_b128 a[244:247], v246 offset:14336\n" \
"v_add_f32 v243, v243, v93\n" \
"v_mfma_f32_16x16x32_bf16 v[156:159], a[248:251], v[16:19], v[156:159]\n" \
"v_exp_f32 v94, v94\n" \
"v_mfma_f32_16x16x32_bf16 v[156:159], a[252:255], v[48:51], v[156:159]\n" \
"v_add_f32 v243, v243, v94\n" \
"v_cvt_pk_bf16_f32 v90, v92, v93\n" \
"v_mfma_f32_16x16x32_bf16 v[188:191], a[248:251], v[24:27], v[188:191]\n" \
"s_waitcnt vmcnt(13) lgkmcnt(6)\n" \
"v_exp_f32 v95, v95\n" \
"v_mfma_f32_16x16x32_bf16 v[188:191], a[252:255], v[56:59], v[188:191]\n" \
"s_barrier\n" \
"ds_read_b128 a[248:251], v246 offset:12544\n" \
"v_add_f32 v243, v243, v95\n" \
"v_mfma_f32_16x16x32_bf16 v[16:19], a[192:195], a[160:163], v[232:235]\n" \
"s_bfe_u32 s88, %1, 0x20001\n" \
"ds_read_b128 v[192:195], v246 offset:16384\n" \
"v_exp_f32 v96, v96\n" \
"s_lshl_b32 s88, s88, 2\n" \
"v_mfma_f32_16x16x32_bf16 v[16:19], a[196:199], a[164:167], v[16:19]\n" \
"s_lshr_b32 s70, %1, 6\n" \
"ds_read_b128 v[196:199], v246 offset:18432\n" \
"v_cvt_pk_bf16_f32 v91, v94, v95\n" \
"v_add_f32 v244, v244, v96\n" \
"v_exp_f32 v97, v97\n" \
"v_mfma_f32_16x16x32_bf16 v[24:27], a[192:195], a[176:179], v[236:239]\n" \
"s_add_u32 s88, s88, s70\n" \
"ds_read_b128 v[200:203], v246 offset:16640\n" \
"v_add_f32 v244, v244, v97\n" \
"v_exp_f32 v98, v98\n" \
"v_bfe_u32 v69, v248, 8, 1\n" \
"v_bfe_u32 v70, v248, 6, 2\n" \
"s_lshr_b32 s71, s22, 7\n" \
"v_mfma_f32_16x16x32_bf16 v[24:27], a[196:199], a[180:183], v[24:27]\n" \
"s_sub_u32 s71, s63, s71\n" \
"ds_read_b128 a[252:255], v246 offset:14592\n" \
"v_add_f32 v244, v244, v98\n" \
"v_cvt_pk_bf16_f32 v96, v96, v97\n" \
"v_exp_f32 v99, v99\n" \
"s_add_u32 s71, s71, 6\n" \
"v_mfma_f32_16x16x32_bf16 v[20:23], a[200:203], a[160:163], v[232:235]\n" \
"s_and_b32 s71, s71, 7\n" \
"ds_read_b128 v[204:207], v246 offset:18688\n" \
"v_add_f32 v244, v244, v99\n" \
"v_exp_f32 v100, v100\n" \
"v_lshl_add_u32 v70, v69, 3, v70\n" \
"v_mul_u32_u24 v70, 6144, v70\n" \
"v_mfma_f32_16x16x32_bf16 v[20:23], a[204:207], a[164:167], v[20:23]\n" \
"s_and_b32 s70, %1, 1\n" \
"ds_read_b128 v[208:211], v246 offset:24576\n" \
"v_add_f32 v244, v244, v100\n" \
"v_cvt_pk_bf16_f32 v97, v98, v99\n" \
"v_exp_f32 v101, v101\n" \
"s_lshl_b32 s70, s70, 3\n" \
"v_mfma_f32_16x16x32_bf16 v[28:31], a[200:203], a[176:179], v[236:239]\n" \
"s_add_u32 s70, s70, s71\n" \
"ds_read_b128 v[212:215], v246 offset:26624\n" \
"v_add_f32 v244, v244, v101\n" \
"v_exp_f32 v102, v102\n" \
"v_lshlrev_b32 v69, 5, v69\n" \
"v_and_b32 v71, 0x30, v248\n" \
"s_lshl_b32 s87, s70, 12\n" \
"v_mfma_f32_16x16x32_bf16 v[28:31], a[204:207], a[180:183], v[28:31]\n" \
"s_lshl_b32 s70, s70, 7\n" \
"ds_read_b128 v[216:219], v246 offset:24832\n" \
"v_add_f32 v244, v244, v102\n" \
"v_cvt_pk_bf16_f32 v98, v100, v101\n" \
"v_exp_f32 v103, v103\n" \
"s_mul_i32 s84, s61, 6144\n" \
"v_mfma_f32_16x16x32_bf16 v[48:51], a[208:211], a[160:163], v[232:235]\n" \
"s_lshl_b32 s71, s84, 4\n" \
"ds_read_b128 v[220:223], v246 offset:26880\n" \
"v_add_f32 v244, v244, v103\n" \
"v_exp_f32 v104, v104\n" \
"v_xor_b32 v69, v69, v71\n" \
"v_mfma_f32_16x16x32_bf16 v[48:51], a[212:215], a[164:167], v[48:51]\n" \
"ds_read_b128 a[192:195], v247 offset:32768\n" \
"v_lshrrev_b32 v71, 9, v248\n" \
"v_add_f32 v245, v245, v104\n" \
"v_cvt_pk_bf16_f32 v99, v102, v103\n" \
"v_exp_f32 v105, v105\n" \
"v_mfma_f32_16x16x32_bf16 v[56:59], a[208:211], a[176:179], v[236:239]\n" \
"s_add_u32 s70, s70, s71\n" \
"ds_read_b128 a[196:199], v247 offset:40960\n" \
"v_add_f32 v245, v245, v105\n" \
"v_exp_f32 v106, v106\n" \
"s_add_u32 s70, s70, 4096\n" \
"v_mfma_f32_16x16x32_bf16 v[56:59], a[212:215], a[180:183], v[56:59]\n" \
"s_waitcnt lgkmcnt(4)\n" \
"ds_read_b128 a[200:203], v247 offset:33792\n" \
"v_lshl_add_u32 v70, v71, 6, v70\n" \
"v_add_f32 v245, v245, v106\n" \
"v_cvt_pk_bf16_f32 v104, v104, v105\n" \
"v_exp_f32 v107, v107\n" \
"v_mfma_f32_16x16x32_bf16 v[8:11], v[192:195], a[176:179], v[236:239]\n" \
"ds_read_b128 a[204:207], v247 offset:41984\n" \
"v_add_u32 v253, v69, v70\n" \
"v_add_f32 v245, v245, v107\n" \
"v_exp_f32 v108, v108\n" \
"v_mfma_f32_16x16x32_bf16 v[8:11], v[196:199], a[180:183], v[8:11]\n" \
"s_add_u32 s48, s22, s70\n" \
"s_addc_u32 s49, s23, 0\n" \
"ds_read_b128 a[208:211], v247 offset:34816\n" \
"v_add_f32 v245, v245, v108\n" \
"v_cvt_pk_bf16_f32 v105, v106, v107\n" \
"v_exp_f32 v109, v109\n" \
"v_mfma_f32_16x16x32_bf16 v[52:55], a[216:219], a[160:163], v[232:235]\n" \
"s_add_u32 s50, s48, 23552\n" \
"s_addc_u32 s51, s49, 0\n" \
"ds_read_b128 a[212:215], v247 offset:43008\n" \
"v_add_f32 v245, v245, v109\n" \
"v_exp_f32 v110, v110\n" \
"v_mfma_f32_16x16x32_bf16 v[52:55], a[220:223], a[164:167], v[52:55]\n" \
"s_lshr_b32 s89, s22, 7\n" \
"ds_read_b128 v[192:195], v246 offset:20480\n" \
"v_add_f32 v245, v245, v110\n" \
"v_cvt_pk_bf16_f32 v106, v108, v109\n" \
"v_exp_f32 v111, v111\n" \
"s_sub_u32 s89, 3, s89\n" \
"v_mfma_f32_16x16x32_bf16 v[60:63], a[216:219], a[176:179], v[236:239]\n" \
"ds_read_b128 v[196:199], v246 offset:22528\n" \
"v_bfe_u32 v84, v248, 7, 2\n" \
"v_add_f32 v245, v245, v111\n" \
"v_exp_f32 v112, v112\n" \
"v_mfma_f32_16x16x32_bf16 v[60:63], a[220:223], a[180:183], v[60:63]\n" \
"s_and_b32 s89, s89, 7\n" \
"ds_read_b128 a[216:219], v247 offset:35840\n" \
"v_add_f32 v242, v242, v112\n" \
"v_cvt_pk_bf16_f32 v107, v110, v111\n" \
"v_exp_f32 v113, v113\n" \
"s_and_b32 s90, %1, 1\n" \
"v_mfma_f32_16x16x32_bf16 v[12:15], v[200:203], a[176:179], v[236:239]\n" \
"ds_read_b128 a[220:223], v247 offset:44032\n" \
"v_bfe_u32 v85, v248, 5, 2\n" \
"v_add_f32 v242, v242, v113\n" \
"v_exp_f32 v114, v114\n" \
"v_mfma_f32_16x16x32_bf16 v[12:15], v[204:207], a[180:183], v[12:15]\n" \
"s_lshl_b32 s90, s90, 3\n" \
"ds_read_b128 v[200:203], v246 offset:20736\n" \
"v_add_f32 v242, v242, v114\n" \
"v_cvt_pk_bf16_f32 v112, v112, v113\n" \
"v_exp_f32 v115, v115\n" \
"s_add_u32 s89, s89, s90\n" \
"v_mfma_f32_16x16x32_bf16 v[16:19], a[224:227], a[168:171], v[16:19]\n" \
"ds_read_b128 v[204:207], v246 offset:22784\n" \
"v_min_u32 v85, 2, v85\n" \
"v_add_f32 v242, v242, v115\n" \
"v_exp_f32 v116, v116\n" \
"v_mfma_f32_16x16x32_bf16 v[16:19], a[228:231], a[172:175], v[16:19]\n" \
"s_lshl_b32 s89, s89, 7\n" \
"v_add_f32 v242, v242, v116\n" \
"v_cvt_pk_bf16_f32 v113, v114, v115\n" \
"v_exp_f32 v117, v117\n" \
"s_lshr_b32 s90, %1, 3\n" \
"v_mad_u32_u24 v84, v84, 3, v85\n" \
"v_mfma_f32_16x16x32_bf16 v[24:27], a[224:227], a[184:187], v[24:27]\n" \
"s_lshl_b32 s90, s90, 2\n" \
"v_add_f32 v242, v242, v117\n" \
"v_exp_f32 v118, v118\n" \
"s_add_u32 s90, s90, s61\n" \
"v_and_b32 v85, 16, v248\n" \
"v_mfma_f32_16x16x32_bf16 v[24:27], a[228:231], a[188:191], v[24:27]\n" \
"s_mul_i32 s90, s90, 24576\n" \
"ds_read_b128 a[224:227], v247 offset:36864\n" \
"v_add_f32 v242, v242, v118\n" \
"v_cvt_pk_bf16_f32 v114, v116, v117\n" \
"s_add_u32 s89, s89, s90\n" \
"v_mfma_f32_16x16x32_bf16 v[40:43], v[208:211], a[176:179], v[236:239]\n" \
"ds_read_b128 a[228:231], v247 offset:45056\n" \
"v_lshl_add_u32 v85, v85, 2, s89\n" \
"v_mfma_f32_16x16x32_bf16 v[40:43], v[212:215], a[180:183], v[40:43]\n" \
"ds_read_b128 v[208:211], v246 offset:28672\n" \
"v_lshl_add_u32 v84, v84, 11, v85\n" \
"v_mfma_f32_16x16x32_bf16 v[20:23], a[232:235], a[168:171], v[20:23]\n" \
"s_add_u32 m0, s84, 143360\n" \
"ds_read_b128 v[212:215], v246 offset:30720\n" \
"v_exp_f32 v119, v119\n" \
"v_mfma_f32_16x16x32_bf16 v[20:23], a[236:239], a[172:175], v[20:23]\n" \
"global_load_lds_dwordx4 v253, s[48:49] offset:-4096\n" \
"v_add_f32 v242, v242, v119\n" \
"v_mfma_f32_16x16x32_bf16 v[28:31], a[232:235], a[184:187], v[28:31]\n" \
"v_exp_f32 v120, v120\n" \
"v_mfma_f32_16x16x32_bf16 v[28:31], a[236:239], a[188:191], v[28:31]\n" \
"s_waitcnt lgkmcnt(2)\n" \
"ds_read_b128 a[232:235], v247 offset:37888\n" \
"v_add_f32 v243, v243, v120\n" \
"v_cvt_pk_bf16_f32 v115, v118, v119\n" \
"v_mfma_f32_16x16x32_bf16 v[44:47], v[216:219], a[176:179], v[236:239]\n" \
"ds_read_b128 a[236:239], v247 offset:46080\n" \
"global_load_lds_dwordx4 v253, s[48:49] offset:-2048\n" \
"v_mfma_f32_16x16x32_bf16 v[44:47], v[220:223], a[180:183], v[44:47]\n" \
"ds_read_b128 v[216:219], v246 offset:28928\n" \
"v_exp_f32 v121, v121\n" \
"v_mfma_f32_16x16x32_bf16 v[48:51], a[240:243], a[168:171], v[48:51]\n" \
"ds_read_b128 v[220:223], v246 offset:30976\n" \
"v_add_f32 v243, v243, v121\n" \
"v_mfma_f32_16x16x32_bf16 v[48:51], a[244:247], a[172:175], v[48:51]\n" \
"global_load_lds_dwordx4 v253, s[48:49] offset:0\n" \
"v_exp_f32 v122, v122\n" \
"v_mfma_f32_16x16x32_bf16 v[56:59], a[240:243], a[184:187], v[56:59]\n" \
"s_add_u32 s48, s48, 0x120000\n" \
"s_addc_u32 s49, s49, 0\n" \
"v_add_f32 v243, v243, v122\n" \
"v_mfma_f32_16x16x32_bf16 v[56:59], a[244:247], a[188:191], v[56:59]\n" \
"ds_read_b128 a[240:243], v247 offset:38912\n" \
"v_cvt_pk_bf16_f32 v120, v120, v121\n" \
"v_mfma_f32_16x16x32_bf16 v[8:11], v[192:195], a[184:187], v[8:11]\n" \
"ds_read_b128 a[244:247], v247 offset:47104\n" \
"global_load_lds_dwordx4 v253, s[50:51] offset:-3072\n" \
"v_mfma_f32_16x16x32_bf16 v[8:11], v[196:199], a[188:191], v[8:11]\n" \
"v_exp_f32 v123, v123\n" \
"v_mfma_f32_16x16x32_bf16 v[52:55], a[248:251], a[168:171], v[52:55]\n" \
"v_add_f32 v243, v243, v123\n" \
"v_exp_f32 v124, v124\n" \
"v_mfma_f32_16x16x32_bf16 v[52:55], a[252:255], a[172:175], v[52:55]\n" \
"global_load_lds_dwordx4 v253, s[50:51] offset:-1024\n" \
"v_add_f32 v243, v243, v124\n" \
"v_cvt_pk_bf16_f32 v121, v122, v123\n" \
"v_mfma_f32_16x16x32_bf16 v[60:63], a[248:251], a[184:187], v[60:63]\n" \
"v_exp_f32 v125, v125\n" \
"v_mfma_f32_16x16x32_bf16 v[60:63], a[252:255], a[188:191], v[60:63]\n" \
"ds_read_b128 a[248:251], v247 offset:39936\n" \
"v_add_f32 v243, v243, v125\n" \
"v_mfma_f32_16x16x32_bf16 v[12:15], v[200:203], a[184:187], v[12:15]\n" \
"ds_read_b128 a[252:255], v247 offset:48128\n" \
"global_load_lds_dwordx4 v253, s[50:51] offset:1024\n" \
"v_mfma_f32_16x16x32_bf16 v[12:15], v[204:207], a[188:191], v[12:15]\n" \
"s_waitcnt lgkmcnt(2)\n" \
"s_add_u32 s50, s50, 0x120000\n" \
"s_addc_u32 s51, s51, 0\n" \
"v_mfma_f32_16x16x32_bf16 v[40:43], v[208:211], a[184:187], v[40:43]\n" \
"v_exp_f32 v126, v126\n" \
"v_mfma_f32_16x16x32_bf16 v[40:43], v[212:215], a[188:191], v[40:43]\n" \
"global_load_dword v85, v84, s[22:23]\n" \
"v_mfma_f32_16x16x32_bf16 v[44:47], v[216:219], a[184:187], v[44:47]\n" \
"s_waitcnt vmcnt(12) lgkmcnt(0)\n" \
"v_add_u32 v247, 0x10000, v248\n" \
"v_add_f32 v243, v243, v126\n" \
"v_mfma_f32_16x16x32_bf16 v[44:47], v[220:223], a[188:191], v[44:47]\n" \
"s_barrier\n" \
"v_cvt_pk_bf16_f32 v122, v124, v125\n" \
"v_mfma_f32_16x16x32_bf16 a[64:67], a[192:195], v[64:67], a[64:67]\n" \
"v_exp_f32 v127, v127\n" \
"v_mfma_f32_16x16x32_bf16 a[64:67], a[196:199], v[96:99], a[64:67]\n" \
"v_add_f32 v243, v243, v127\n" \
"v_cvt_pk_bf16_f32 v123, v126, v127\n" \
"v_mfma_f32_16x16x32_bf16 a[96:99], a[192:195], v[72:75], a[96:99]\n" \
"s_lshl_b32 s70, s61, 14\n" \
"ds_read_b128 v[192:195], v247 offset:16384\n" \
"v_cmp_gt_i32 vcc, 0, v251\n" \
"v_cmp_gt_i32 s[64:65], 1, v251\n" \
"v_cmp_gt_i32 s[66:67], 2, v251\n" \
"v_cndmask_b32 v8, v8, v254, vcc\n" \
"v_cndmask_b32 v16, v16, v254, vcc\n" \
"v_cmp_gt_i32 vcc, 3, v251\n" \
"v_add_u32 v124, s70, v248\n" \
"v_xor_b32 v125, 0x4000, v124\n" \
"v_mfma_f32_16x16x32_bf16 a[96:99], a[196:199], v[104:107], a[96:99]\n" \
"s_lshl_b32 s70, s61, 9\n" \
"s_add_u32 s70, s70, 110592\n" \
"v_cndmask_b32 v9, v9, v254, s[64:65]\n" \
"v_cndmask_b32 v17, v17, v254, s[64:65]\n" \
"v_cmp_gt_i32 s[64:65], 16, v251\n" \
"v_cndmask_b32 v10, v10, v254, s[66:67]\n" \
"v_cndmask_b32 v18, v18, v254, s[66:67]\n" \
"v_cmp_gt_i32 s[66:67], 17, v251\n" \
"v_lshrrev_b32 v126, 1, v248\n" \
"v_mfma_f32_16x16x32_bf16 v[128:131], a[192:195], v[80:83], v[128:131]\n" \
"v_add_u32 v126, s70, v126\n" \
"v_xor_b32 v127, 0x200, v126\n" \
"v_cndmask_b32 v11, v11, v254, vcc\n" \
"v_cndmask_b32 v19, v19, v254, vcc\n" \
"v_cmp_gt_i32 vcc, 18, v251\n" \
"v_cndmask_b32 v12, v12, v254, s[64:65]\n" \
"v_cndmask_b32 v20, v20, v254, s[64:65]\n" \
"v_cmp_gt_i32 s[64:65], 19, v251\n" \
"v_mfma_f32_16x16x32_bf16 v[128:131], a[196:199], v[112:115], v[128:131]\n" \
"v_lshrrev_b32 v71, 7, v248\n" \
"v_and_b32 v76, 0x70, v248\n" \
"v_cndmask_b32 v13, v13, v254, s[66:67]\n" \
"v_cndmask_b32 v21, v21, v254, s[66:67]\n" \
"v_cmp_gt_i32 s[66:67], 32, v251\n" \
"v_cndmask_b32 v14, v14, v254, vcc\n" \
"v_cndmask_b32 v22, v22, v254, vcc\n" \
"v_cmp_gt_i32 vcc, 33, v251\n" \
"v_mfma_f32_16x16x32_bf16 v[160:163], a[192:195], v[88:91], v[160:163]\n" \
"s_lshl_b32 s70, s88, 18\n" \
"s_lshl_b32 s71, s61, 16\n" \
"ds_read_b128 v[196:199], v247 offset:24576\n" \
"v_cndmask_b32 v15, v15, v254, s[64:65]\n" \
"v_cndmask_b32 v23, v23, v254, s[64:65]\n" \
"v_cmp_gt_i32 s[64:65], 34, v251\n" \
"v_cndmask_b32 v40, v40, v254, s[66:67]\n" \
"v_cndmask_b32 v48, v48, v254, s[66:67]\n" \
"v_cmp_gt_i32 s[66:67], 35, v251\n" \
"v_mfma_f32_16x16x32_bf16 v[160:163], a[196:199], v[120:123], v[160:163]\n" \
"s_add_u32 s71, s71, s70\n" \
"ds_read_b128 a[192:195], v247 offset:0\n" \
"v_cndmask_b32 v41, v41, v254, vcc\n" \
"v_cndmask_b32 v49, v49, v254, vcc\n" \
"v_cmp_gt_i32 vcc, 48, v251\n" \
"v_cndmask_b32 v42, v42, v254, s[64:65]\n" \
"v_cndmask_b32 v50, v50, v254, s[64:65]\n" \
"v_cmp_gt_i32 s[64:65], 49, v251\n" \
"v_lshlrev_b32 v77, 4, v71\n" \
"v_xor_b32 v77, v77, v76\n" \
"v_mfma_f32_16x16x32_bf16 a[68:71], a[200:203], v[64:67], a[68:71]\n" \
"s_add_u32 s42, s20, s71\n" \
"s_addc_u32 s43, s21, 0\n" \
"ds_write_b128 v124, a[96:99] offset:8192\n" \
"v_cndmask_b32 v43, v43, v254, s[66:67]\n" \
"v_cndmask_b32 v51, v51, v254, s[66:67]\n" \
"v_cmp_gt_i32 s[66:67], 50, v251\n" \
"v_cndmask_b32 v44, v44, v254, vcc\n" \
"v_cndmask_b32 v52, v52, v254, vcc\n" \
"v_cmp_gt_i32 vcc, 51, v251\n" \
"v_mfma_f32_16x16x32_bf16 a[68:71], a[204:207], v[96:99], a[68:71]\n" \
"ds_write_b128 v124, a[64:67] offset:0\n" \
"v_cndmask_b32 v45, v45, v254, s[64:65]\n" \
"v_cndmask_b32 v53, v53, v254, s[64:65]\n" \
"v_cndmask_b32 v46, v46, v254, s[66:67]\n" \
"v_cndmask_b32 v54, v54, v254, s[66:67]\n" \
"v_cndmask_b32 v47, v47, v254, vcc\n" \
"v_cndmask_b32 v55, v55, v254, vcc\n" \
"v_mfma_f32_16x16x32_bf16 a[100:103], a[200:203], v[72:75], a[100:103]\n" \
"ds_read_b128 v[200:203], v247 offset:17408\n" \
"v_lshl_add_u32 v252, v71, 12, v77\n" \
"v_exp_f32 v8, v8\n" \
"v_mfma_f32_16x16x32_bf16 a[100:103], a[204:207], v[104:107], a[100:103]\n" \
"s_add_u32 s44, s42, 31744\n" \
"s_addc_u32 s45, s43, 0\n" \
"ds_read_b128 a[196:199], v247 offset:8192\n" \
"v_add_f32 v243, v243, v8\n" \
"v_exp_f32 v9, v9\n" \
"v_mfma_f32_16x16x32_bf16 v[132:135], a[200:203], v[80:83], v[132:135]\n" \
"s_max_u32 s70, s70, 32768\n" \
"ds_write_b128 v124, a[68:71] offset:1024\n" \
"v_add_f32 v243, v243, v9\n" \
"v_exp_f32 v10, v10\n" \
"v_mfma_f32_16x16x32_bf16 v[132:135], a[204:207], v[112:115], v[132:135]\n" \
"s_sub_u32 s70, s70, 32768\n" \
"ds_write_b64 v126, v[244:245]\n" \
"v_add_f32 v243, v243, v10\n" \
"v_cvt_pk_bf16_f32 v8, v8, v9\n" \
"v_exp_f32 v11, v11\n" \
"v_mfma_f32_16x16x32_bf16 v[164:167], a[200:203], v[88:91], v[164:167]\n" \
"s_and_b32 s71, s61, 1\n" \
"ds_read_b128 v[204:207], v247 offset:25600\n" \
"v_add_f32 v243, v243, v11\n" \
"v_exp_f32 v12, v12\n" \
"s_lshl_b32 s71, s71, 2\n" \
"v_mfma_f32_16x16x32_bf16 v[164:167], a[204:207], v[120:123], v[164:167]\n" \
"ds_read_b128 a[200:203], v247 offset:1024\n" \
"v_and_or_b32 v71, v71, 3, s71\n" \
"v_add_f32 v243, v243, v12\n" \
"v_cvt_pk_bf16_f32 v9, v10, v11\n" \
"v_exp_f32 v13, v13\n" \
"v_mfma_f32_16x16x32_bf16 a[72:75], a[208:211], v[64:67], a[72:75]\n" \
"s_lshr_b32 s71, s61, 1\n" \
"ds_write_b128 v124, a[100:103] offset:9216\n" \
"v_add_f32 v243, v243, v13\n" \
"v_exp_f32 v14, v14\n" \
"v_mfma_f32_16x16x32_bf16 a[72:75], a[212:215], v[96:99], a[72:75]\n" \
"s_lshl_b32 s71, s71, 11\n" \
"v_add_f32 v243, v243, v14\n" \
"v_cvt_pk_bf16_f32 v10, v12, v13\n" \
"v_exp_f32 v15, v15\n" \
"v_lshlrev_b32 v77, 4, v71\n" \
"s_add_u32 s70, s70, s71\n" \
"v_xor_b32 v77, v77, v76\n" \
"v_mfma_f32_16x16x32_bf16 a[104:107], a[208:211], v[72:75], a[104:107]\n" \
"s_add_u32 s46, s20, s70\n" \
"s_addc_u32 s47, s21, 0\n" \
"ds_read_b128 v[208:211], v247 offset:18432\n" \
"v_add_f32 v243, v243, v15\n" \
"v_exp_f32 v40, v40\n" \
"v_lshl_add_u32 v246, v71, 12, v77\n" \
"v_mfma_f32_16x16x32_bf16 a[104:107], a[212:215], v[104:107], a[104:107]\n" \
"s_lshl_b32 s85, s61, 10\n" \
"ds_read_b128 a[204:207], v247 offset:9216\n" \
"v_add_f32 v243, v243, v40\n" \
"v_cvt_pk_bf16_f32 v11, v14, v15\n" \
"v_exp_f32 v41, v41\n" \
"s_mov_b64 s[12:13], s[20:21]\n" \
"v_mfma_f32_16x16x32_bf16 v[136:139], a[208:211], v[80:83], v[136:139]\n" \
"s_and_b32 s13, s13, 0xffff\n" \
"ds_write_b128 v124, a[72:75] offset:2048\n" \
"v_add_f32 v243, v243, v41\n" \
"v_exp_f32 v42, v42\n" \
"v_mfma_f32_16x16x32_bf16 v[136:139], a[212:215], v[112:115], v[136:139]\n" \
"s_mov_b32 s14, 4194304\n" \
"v_add_f32 v243, v243, v42\n" \
"v_cvt_pk_bf16_f32 v40, v40, v41\n" \
"v_exp_f32 v43, v43\n" \
"s_mov_b32 s15, 0x20000\n" \
"s_mov_b32 s46, s70\n" \
"v_mfma_f32_16x16x32_bf16 v[168:171], a[208:211], v[88:91], v[168:171]\n" \
"ds_read_b128 v[212:215], v247 offset:26624\n" \
"v_lshrrev_b32 v71, 9, v248\n" \
"v_add_f32 v243, v243, v43\n" \
"v_exp_f32 v44, v44\n" \
"v_mfma_f32_16x16x32_bf16 v[168:171], a[212:215], v[120:123], v[168:171]\n" \
"ds_read_b128 a[208:211], v247 offset:2048\n" \
"v_lshl_add_u32 v246, v71, 30, v246\n" \
"v_add_f32 v243, v243, v44\n" \
"v_cvt_pk_bf16_f32 v41, v42, v43\n" \
"v_exp_f32 v45, v45\n" \
"v_mfma_f32_16x16x32_bf16 a[76:79], a[216:219], v[64:67], a[76:79]\n" \
"s_add_u32 m0, s95, 118784\n" \
"ds_write_b128 v124, a[104:107] offset:10240\n" \
"v_add_f32 v243, v243, v45\n" \
"v_exp_f32 v46, v46\n" \
"v_mfma_f32_16x16x32_bf16 a[76:79], a[220:223], v[96:99], a[76:79]\n" \
"global_load_lds_dwordx4 v252, s[42:43] offset:0\n" \
"v_add_f32 v243, v243, v46\n" \
"v_cvt_pk_bf16_f32 v42, v44, v45\n" \
"v_exp_f32 v47, v47\n" \
"v_mfma_f32_16x16x32_bf16 a[108:111], a[216:219], v[72:75], a[108:111]\n" \
"ds_read_b128 v[216:219], v247 offset:19456\n" \
"v_add_f32 v243, v243, v47\n" \
"v_cvt_pk_bf16_f32 v43, v46, v47\n" \
"v_exp_f32 v24, v24\n" \
"v_mfma_f32_16x16x32_bf16 a[108:111], a[220:223], v[104:107], a[108:111]\n" \
"ds_read_b128 a[212:215], v247 offset:10240\n" \
"global_load_lds_dwordx4 v252, s[42:43] offset:2048\n" \
"v_add_f32 v243, v243, v24\n" \
"v_exp_f32 v25, v25\n" \
"v_mfma_f32_16x16x32_bf16 v[140:143], a[216:219], v[80:83], v[140:143]\n" \
"ds_write_b128 v124, a[76:79] offset:3072\n" \
"v_add_f32 v243, v243, v25\n" \
"v_exp_f32 v26, v26\n" \
"v_mfma_f32_16x16x32_bf16 v[140:143], a[220:223], v[112:115], v[140:143]\n" \
"s_add_u32 s42, s42, 0x180\n" \
"s_addc_u32 s43, s43, 0\n" \
"v_add_f32 v243, v243, v26\n" \
"v_cvt_pk_bf16_f32 v24, v24, v25\n" \
"v_exp_f32 v27, v27\n" \
"v_mfma_f32_16x16x32_bf16 v[172:175], a[216:219], v[88:91], v[172:175]\n" \
"ds_read_b128 v[220:223], v247 offset:27648\n" \
"v_add_f32 v243, v243, v27\n" \
"v_exp_f32 v28, v28\n" \
"v_mfma_f32_16x16x32_bf16 v[172:175], a[220:223], v[120:123], v[172:175]\n" \
"ds_read_b128 a[216:219], v247 offset:3072\n" \
"global_load_lds_dwordx4 v252, s[44:45] offset:1024\n" \
"v_add_f32 v243, v243, v28\n" \
"v_cvt_pk_bf16_f32 v25, v26, v27\n" \
"v_exp_f32 v29, v29\n" \
"v_mfma_f32_16x16x32_bf16 a[80:83], a[224:227], v[64:67], a[80:83]\n" \
"ds_write_b128 v124, a[108:111] offset:11264\n" \
"v_add_f32 v243, v243, v29\n" \
"v_exp_f32 v30, v30\n" \
"v_mfma_f32_16x16x32_bf16 a[80:83], a[228:231], v[96:99], a[80:83]\n" \
"v_add_f32 v243, v243, v30\n" \
"v_cvt_pk_bf16_f32 v26, v28, v29\n" \
"v_exp_f32 v31, v31\n" \
"v_mfma_f32_16x16x32_bf16 a[112:115], a[224:227], v[72:75], a[112:115]\n" \
"ds_read_b128 v[224:227], v247 offset:20480\n" \
"global_load_lds_dwordx4 v252, s[44:45] offset:3072\n" \
"v_add_f32 v243, v243, v31\n" \
"v_exp_f32 v56, v56\n" \
"v_mfma_f32_16x16x32_bf16 a[112:115], a[228:231], v[104:107], a[112:115]\n" \
"s_add_u32 s44, s44, 0x180\n" \
"s_addc_u32 s45, s45, 0\n" \
"ds_read_b128 a[220:223], v247 offset:11264\n" \
"v_add_f32 v243, v243, v56\n" \
"v_cvt_pk_bf16_f32 v27, v30, v31\n" \
"v_exp_f32 v57, v57\n" \
"v_mfma_f32_16x16x32_bf16 v[144:147], a[224:227], v[80:83], v[144:147]\n" \
"s_add_u32 m0, s85, 135168\n" \
"ds_write_b128 v124, a[80:83] offset:4096\n" \
"v_add_f32 v243, v243, v57\n" \
"v_exp_f32 v58, v58\n" \
"v_mfma_f32_16x16x32_bf16 v[144:147], a[228:231], v[112:115], v[144:147]\n" \
"buffer_load_dwordx4 v246, s[12:15], s46 offen lds\n" \
"v_add_f32 v243, v243, v58\n" \
"v_cvt_pk_bf16_f32 v56, v56, v57\n" \
"v_exp_f32 v59, v59\n" \
"v_mfma_f32_16x16x32_bf16 v[176:179], a[224:227], v[88:91], v[176:179]\n" \
"s_add_u32 s46, s46, 0x180\n" \
"ds_read_b128 v[228:231], v247 offset:28672\n" \
"v_add_f32 v243, v243, v59\n" \
"v_exp_f32 v60, v60\n" \
"v_mfma_f32_16x16x32_bf16 v[176:179], a[228:231], v[120:123], v[176:179]\n" \
"ds_read_b128 a[224:227], v247 offset:4096\n" \
"v_add_f32 v243, v243, v60\n" \
"v_cvt_pk_bf16_f32 v57, v58, v59\n" \
"v_exp_f32 v61, v61\n" \
"v_mfma_f32_16x16x32_bf16 a[84:87], a[232:235], v[64:67], a[84:87]\n" \
"ds_write_b128 v124, a[112:115] offset:12288\n" \
"v_add_f32 v243, v243, v61\n" \
"v_exp_f32 v62, v62\n" \
"v_mfma_f32_16x16x32_bf16 a[84:87], a[236:239], v[96:99], a[84:87]\n" \
"v_add_f32 v243, v243, v62\n" \
"v_cvt_pk_bf16_f32 v58, v60, v61\n" \
"v_exp_f32 v63, v63\n" \
"v_mfma_f32_16x16x32_bf16 a[116:119], a[232:235], v[72:75], a[116:119]\n" \
"ds_read_b128 v[232:235], v247 offset:21504\n" \
"v_add_f32 v243, v243, v63\n" \
"v_cvt_pk_bf16_f32 v59, v62, v63\n" \
"v_exp_f32 v16, v16\n" \
"v_mfma_f32_16x16x32_bf16 a[116:119], a[236:239], v[104:107], a[116:119]\n" \
"ds_read_b128 a[228:231], v247 offset:12288\n" \
"v_add_f32 v242, v242, v16\n" \
"v_exp_f32 v17, v17\n" \
"v_mfma_f32_16x16x32_bf16 v[148:151], a[232:235], v[80:83], v[148:151]\n" \
"ds_write_b128 v124, a[84:87] offset:5120\n" \
"v_add_f32 v242, v242, v17\n" \
"v_exp_f32 v18, v18\n" \
"v_mfma_f32_16x16x32_bf16 v[148:151], a[236:239], v[112:115], v[148:151]\n" \
"v_add_f32 v242, v242, v18\n" \
"v_cvt_pk_bf16_f32 v16, v16, v17\n" \
"v_exp_f32 v19, v19\n" \
"v_mfma_f32_16x16x32_bf16 v[180:183], a[232:235], v[88:91], v[180:183]\n" \
"ds_read_b128 v[236:239], v247 offset:29696\n" \
"v_add_f32 v242, v242, v19\n" \
"v_exp_f32 v20, v20\n" \
"v_mfma_f32_16x16x32_bf16 v[180:183], a[236:239], v[120:123], v[180:183]\n" \
"ds_read_b128 a[232:235], v247 offset:5120\n" \
"v_add_f32 v242, v242, v20\n" \
"v_cvt_pk_bf16_f32 v17, v18, v19\n" \
"v_exp_f32 v21, v21\n" \
"v_mfma_f32_16x16x32_bf16 a[88:91], a[240:243], v[64:67], a[88:91]\n" \
"ds_write_b128 v124, a[116:119] offset:13312\n" \
"v_add_f32 v242, v242, v21\n" \
"v_mfma_f32_16x16x32_bf16 a[88:91], a[244:247], v[96:99], a[88:91]\n" \
"v_exp_f32 v22, v22\n" \
"v_mfma_f32_16x16x32_bf16 a[120:123], a[240:243], v[72:75], a[120:123]\n" \
"ds_read_b128 v[0:3], v247 offset:22528\n" \
"v_add_f32 v242, v242, v22\n" \
"v_mfma_f32_16x16x32_bf16 a[120:123], a[244:247], v[104:107], a[120:123]\n" \
"ds_read_b128 a[236:239], v247 offset:13312\n" \
"v_cvt_pk_bf16_f32 v18, v20, v21\n" \
"v_mfma_f32_16x16x32_bf16 v[152:155], a[240:243], v[80:83], v[152:155]\n" \
"ds_write_b128 v124, a[88:91] offset:6144\n" \
"v_mfma_f32_16x16x32_bf16 v[152:155], a[244:247], v[112:115], v[152:155]\n" \
"v_exp_f32 v23, v23\n" \
"v_mfma_f32_16x16x32_bf16 v[184:187], a[240:243], v[88:91], v[184:187]\n" \
"ds_read_b128 v[4:7], v247 offset:30720\n" \
"v_add_f32 v242, v242, v23\n" \
"v_mfma_f32_16x16x32_bf16 v[184:187], a[244:247], v[120:123], v[184:187]\n" \
"ds_read_b128 a[240:243], v247 offset:6144\n" \
"v_exp_f32 v48, v48\n" \
"v_mfma_f32_16x16x32_bf16 a[92:95], a[248:251], v[64:67], a[92:95]\n" \
"ds_write_b128 v124, a[120:123] offset:14336\n" \
"v_mfma_f32_16x16x32_bf16 a[92:95], a[252:255], v[96:99], a[92:95]\n" \
"v_add_f32 v242, v242, v48\n" \
"v_cvt_pk_bf16_f32 v19, v22, v23\n" \
"v_mfma_f32_16x16x32_bf16 a[124:127], a[248:251], v[72:75], a[124:127]\n" \
"ds_read_b128 v[32:35], v247 offset:23552\n" \
"v_exp_f32 v49, v49\n" \
"v_mfma_f32_16x16x32_bf16 a[124:127], a[252:255], v[104:107], a[124:127]\n" \
"ds_read_b128 a[244:247], v247 offset:14336\n" \
"v_add_f32 v242, v242, v49\n" \
"v_mfma_f32_16x16x32_bf16 v[156:159], a[248:251], v[80:83], v[156:159]\n" \
"ds_write_b128 v124, a[92:95] offset:7168\n" \
"v_mfma_f32_16x16x32_bf16 v[156:159], a[252:255], v[112:115], v[156:159]\n" \
"v_exp_f32 v50, v50\n" \
"v_mfma_f32_16x16x32_bf16 v[188:191], a[248:251], v[88:91], v[188:191]\n" \
"ds_read_b128 v[36:39], v247 offset:31744\n" \
"v_add_f32 v242, v242, v50\n" \
"v_mfma_f32_16x16x32_bf16 v[188:191], a[252:255], v[120:123], v[188:191]\n" \
"s_waitcnt lgkmcnt(3)\n" \
"ds_read_b128 a[248:251], v247 offset:7168\n" \
"v_cvt_pk_bf16_f32 v48, v48, v49\n" \
"v_mfma_f32_16x16x32_bf16 v[160:163], v[192:195], v[8:11], v[160:163]\n" \
"v_exp_f32 v51, v51\n" \
"v_mfma_f32_16x16x32_bf16 v[160:163], v[196:199], v[40:43], v[160:163]\n" \
"ds_read_b128 a[252:255], v247 offset:15360\n" \
"v_add_f32 v242, v242, v51\n" \
"v_exp_f32 v52, v52\n" \
"v_mfma_f32_16x16x32_bf16 v[164:167], v[200:203], v[8:11], v[164:167]\n" \
"ds_write_b128 v124, a[124:127] offset:15360\n" \
"v_mfma_f32_16x16x32_bf16 v[164:167], v[204:207], v[40:43], v[164:167]\n" \
"v_add_f32 v242, v242, v52\n" \
"v_cvt_pk_bf16_f32 v49, v50, v51\n" \
"v_mfma_f32_16x16x32_bf16 v[168:171], v[208:211], v[8:11], v[168:171]\n" \
"v_exp_f32 v53, v53\n" \
"v_mfma_f32_16x16x32_bf16 v[168:171], v[212:215], v[40:43], v[168:171]\n" \
"s_waitcnt vmcnt(0) lgkmcnt(0)\n" \
"v_add_f32 v242, v242, v53\n" \
"v_exp_f32 v54, v54\n" \
"v_mfma_f32_16x16x32_bf16 v[172:175], v[216:219], v[8:11], v[172:175]\n" \
"s_barrier\n" \
"v_add_f32 v242, v242, v54\n" \
"v_cvt_pk_bf16_f32 v50, v52, v53\n" \
"v_mfma_f32_16x16x32_bf16 v[172:175], v[220:223], v[40:43], v[172:175]\n" \
"s_add_u32 m0, s84, 90112\n" \
"ds_read_b64 v[244:245], v127\n" \
"v_exp_f32 v55, v55\n" \
"v_mfma_f32_16x16x32_bf16 v[176:179], v[224:227], v[8:11], v[176:179]\n" \
"ds_read_b128 v[64:67], v125 offset:0\n" \
"global_load_lds_dwordx4 v253, s[48:49] offset:-4096\n" \
"v_mfma_f32_16x16x32_bf16 v[176:179], v[228:231], v[40:43], v[176:179]\n" \
"ds_read_b128 v[68:71], v125 offset:1024\n" \
"global_load_lds_dwordx4 v253, s[48:49] offset:-2048\n" \
"v_mfma_f32_16x16x32_bf16 v[180:183], v[232:235], v[8:11], v[180:183]\n" \
"ds_read_b128 v[72:75], v125 offset:2048\n" \
"v_add_f32 v242, v242, v55\n" \
"v_mfma_f32_16x16x32_bf16 v[180:183], v[236:239], v[40:43], v[180:183]\n" \
"ds_read_b128 v[76:79], v125 offset:3072\n" \
"global_load_lds_dwordx4 v253, s[48:49] offset:0\n" \
"v_mfma_f32_16x16x32_bf16 v[184:187], v[0:3], v[8:11], v[184:187]\n" \
"s_add_u32 s48, s48, 0x300000\n" \
"s_addc_u32 s49, s49, 0\n" \
"ds_read_b128 v[80:83], v125 offset:4096\n" \
"v_mfma_f32_16x16x32_bf16 v[184:187], v[4:7], v[40:43], v[184:187]\n" \
"ds_read_b128 v[84:87], v125 offset:5120\n" \
"global_load_lds_dwordx4 v253, s[50:51] offset:-3072\n" \
"v_mfma_f32_16x16x32_bf16 v[188:191], v[32:35], v[8:11], v[188:191]\n" \
"s_waitcnt lgkmcnt(5)\n" \
"ds_read_b128 v[88:91], v125 offset:6144\n" \
"v_cvt_pk_bf16_f32 v51, v54, v55\n" \
"v_mfma_f32_16x16x32_bf16 v[188:191], v[36:39], v[40:43], v[188:191]\n" \
"ds_read_b128 v[92:95], v125 offset:7168\n" \
"global_load_lds_dwordx4 v253, s[50:51] offset:-1024\n" \
"v_mfma_f32_16x16x32_bf16 v[160:163], a[192:195], v[24:27], v[160:163]\n" \
"ds_read_b128 v[96:99], v125 offset:8192\n" \
"global_load_lds_dwordx4 v253, s[50:51] offset:1024\n" \
"v_mfma_f32_16x16x32_bf16 v[160:163], a[196:199], v[56:59], v[160:163]\n" \
"s_add_u32 s50, s50, 0x300000\n" \
"s_addc_u32 s51, s51, 0\n" \
"ds_read_b128 v[100:103], v125 offset:9216\n" \
"v_mfma_f32_16x16x32_bf16 v[164:167], a[200:203], v[24:27], v[164:167]\n" \
"s_add_u32 m0, s95, 65536\n" \
"ds_read_b128 v[104:107], v125 offset:10240\n" \
"s_movk_i32 s89, 0xc0\n" \
"v_mfma_f32_16x16x32_bf16 v[164:167], a[204:207], v[56:59], v[164:167]\n" \
"s_movk_i32 s90, 288\n" \
"ds_read_b128 v[108:111], v125 offset:11264\n" \
"global_load_lds_dwordx4 v252, s[42:43] offset:0\n" \
"v_lshrrev_b32 v211, 4, v248\n" \
"v_lshrrev_b32 v214, 8, v248\n" \
"v_and_b32 v215, 7, v211\n" \
"Lpg8743_%=:\n" \
"Lst1x2_%=:\n" \
"s_cbranch_execz Lpg11019_%=\n" \
"v_xor_b32 v214, v214, v215\n" \
"v_pk_add_f32 v[128:129], v[128:129], v[64:65]\n" \
"v_pk_add_f32 v[130:131], v[130:131], v[66:67]\n" \
"s_waitcnt lgkmcnt(10)\n" \
"v_pk_add_f32 v[132:133], v[132:133], v[68:69]\n" \
"v_pk_add_f32 v[134:135], v[134:135], v[70:71]\n" \
"s_waitcnt lgkmcnt(9)\n" \
"v_pk_add_f32 v[136:137], v[136:137], v[72:73]\n" \
"v_pk_add_f32 v[138:139], v[138:139], v[74:75]\n" \
"s_waitcnt lgkmcnt(8)\n" \
"v_pk_add_f32 v[140:141], v[140:141], v[76:77]\n" \
"v_pk_add_f32 v[142:143], v[142:143], v[78:79]\n" \
"s_waitcnt lgkmcnt(7)\n" \
"v_pk_add_f32 v[144:145], v[144:145], v[80:81]\n" \
"v_pk_add_f32 v[146:147], v[146:147], v[82:83]\n" \
"s_waitcnt lgkmcnt(6)\n" \
"v_pk_add_f32 v[148:149], v[148:149], v[84:85]\n" \
"v_pk_add_f32 v[150:151], v[150:151], v[86:87]\n" \
"s_waitcnt lgkmcnt(5)\n" \
"v_pk_add_f32 v[152:153], v[152:153], v[88:89]\n" \
"v_pk_add_f32 v[154:155], v[154:155], v[90:91]\n" \
"s_waitcnt lgkmcnt(4)\n" \
"v_pk_add_f32 v[156:157], v[156:157], v[92:93]\n" \
"v_pk_add_f32 v[158:159], v[158:159], v[94:95]\n" \
"v_mfma_f32_16x16x32_bf16 v[168:171], a[208:211], v[24:27], v[168:171]\n" \
"s_lshr_b32 s71, s61, 1\n" \
"ds_read_b128 v[112:115], v125 offset:12288\n" \
"s_lshl_b32 s70, s71, 11\n" \
"s_waitcnt lgkmcnt(4)\n" \
"v_mfma_f32_16x16x32_bf16 v[168:171], a[212:215], v[56:59], v[168:171]\n" \
"s_add_u32 s70, s70, 118784\n" \
"ds_read_b128 v[116:119], v125 offset:13312\n" \
"global_load_lds_dwordx4 v252, s[42:43] offset:2048\n" \
"v_and_b32 v216, 15, v211\n" \
"v_lshlrev_b32 v213, 7, v216\n" \
"v_lshl_add_u32 v213, v214, 4, v213\n" \
"v_pk_add_f32 v[160:161], v[160:161], v[96:97]\n" \
"v_pk_add_f32 v[162:163], v[162:163], v[98:99]\n" \
"v_mfma_f32_16x16x32_bf16 v[172:175], a[216:219], v[24:27], v[172:175]\n" \
"s_waitcnt lgkmcnt(4)\n" \
"s_add_u32 s42, s42, 0x400\n" \
"s_addc_u32 s43, s43, 0\n" \
"ds_read_b128 v[120:123], v125 offset:14336\n" \
"v_add_u32 v205, s70, v213\n" \
"v_xor_b32 v206, 64, v205\n" \
"v_lshrrev_b32 v216, 2, v216\n" \
"v_lshl_add_u32 v216, v216, 9, v213\n" \
"v_add_u32 v207, 135168, v216\n" \
"v_xor_b32 v208, 64, v207\n" \
"v_pk_add_f32 v[164:165], v[164:165], v[100:101]\n" \
"v_pk_add_f32 v[166:167], v[166:167], v[102:103]\n" \
"v_mfma_f32_16x16x32_bf16 v[172:175], a[220:223], v[56:59], v[172:175]\n" \
"s_and_b32 s70, s61, 1\n" \
"ds_read_b128 v[124:127], v125 offset:15360\n" \
"global_load_lds_dwordx4 v252, s[44:45] offset:1024\n" \
"v_mfma_f32_16x16x32_bf16 v[176:179], a[224:227], v[24:27], v[176:179]\n" \
"s_lshl_b32 s70, s70, 9\n" \
"s_add_u32 s70, s70, 139264\n" \
"s_lshl_b32 s71, s71, 5\n" \
"v_mfma_f32_16x16x32_bf16 v[176:179], a[228:231], v[56:59], v[176:179]\n" \
"s_waitcnt lgkmcnt(5)\n" \
"global_load_lds_dwordx4 v252, s[44:45] offset:3072\n" \
"v_and_b32 v213, 3, v211\n" \
"v_mfma_f32_16x16x32_bf16 v[180:183], a[232:235], v[24:27], v[180:183]\n" \
"s_add_u32 s44, s44, 0x400\n" \
"s_addc_u32 s45, s45, 0\n" \
"s_add_u32 m0, s85, 81920\n" \
"v_mfma_f32_16x16x32_bf16 v[180:183], a[236:239], v[56:59], v[180:183]\n" \
"s_waitcnt lgkmcnt(0)\n" \
"buffer_load_dwordx4 v246, s[12:15], s46 offen lds\n" \
"v_mfma_f32_16x16x32_bf16 v[184:187], a[240:243], v[24:27], v[184:187]\n" \
"s_barrier\n" \
"v_lshlrev_b32 v213, 3, v213\n" \
"v_mfma_f32_16x16x32_bf16 v[184:187], a[244:247], v[56:59], v[184:187]\n" \
"s_add_u32 s46, s46, 0x400\n" \
"v_and_or_b32 v213, v248, s89, v213\n" \
"v_bfe_u32 v214, v211, 4, 1\n" \
"v_mfma_f32_16x16x32_bf16 v[188:191], a[248:251], v[24:27], v[188:191]\n" \
"s_add_u32 m0, s95, 0\n" \
"v_mad_u32_u24 v213, v214, s90, v213\n" \
"v_lshrrev_b32 v214, 5, v211\n" \
"v_mul_u32_u24 v214, 6144, v214\n" \
"v_add3_u32 v213, v213, v214, s70\n" \
"v_xor_b32 v209, s71, v213\n" \
"v_xor_b32 v210, 32, v209\n" \
"v_pk_add_f32 v[168:169], v[168:169], v[104:105]\n" \
"v_pk_add_f32 v[170:171], v[170:171], v[106:107]\n" \
"v_pk_add_f32 v[172:173], v[172:173], v[108:109]\n" \
"v_pk_add_f32 v[174:175], v[174:175], v[110:111]\n" \
"v_pk_add_f32 v[176:177], v[176:177], v[112:113]\n" \
"v_pk_add_f32 v[178:179], v[178:179], v[114:115]\n" \
"v_pk_add_f32 v[180:181], v[180:181], v[116:117]\n" \
"v_pk_add_f32 v[182:183], v[182:183], v[118:119]\n" \
"v_mfma_f32_16x16x32_bf16 v[188:191], a[252:255], v[56:59], v[188:191]\n" \
"ds_read_b128 a[64:67], v207 offset:0\n" \
"ds_read_b64_tr_b16 a[96:97], v209 offset:2048\n" \
"v_mfma_f32_16x16x32_bf16 v[128:131], a[192:195], v[16:19], v[128:131]\n" \
"ds_read_b64_tr_b16 a[98:99], v209 offset:3072\n" \
"global_load_lds_dwordx4 v252, s[42:43] offset:0\n" \
"v_mfma_f32_16x16x32_bf16 v[128:131], a[196:199], v[48:51], v[128:131]\n" \
"ds_read_b128 a[68:71], v208 offset:0\n" \
"v_mfma_f32_16x16x32_bf16 v[132:135], a[200:203], v[16:19], v[132:135]\n" \
"ds_read_b64_tr_b16 a[100:101], v209 offset:14336\n" \
"ds_read_b64_tr_b16 a[102:103], v209 offset:15360\n" \
"global_load_lds_dwordx4 v252, s[42:43] offset:2048\n" \
"v_pk_add_f32 v[184:185], v[184:185], v[120:121]\n" \
"v_pk_add_f32 v[186:187], v[186:187], v[122:123]\n" \
"v_pk_add_f32 v[188:189], v[188:189], v[124:125]\n" \
"v_pk_add_f32 v[190:191], v[190:191], v[126:127]\n" \
"v_mfma_f32_16x16x32_bf16 v[132:135], a[204:207], v[48:51], v[132:135]\n" \
"s_add_u32 s42, s42, 0xfffffb00\n" \
"s_addc_u32 s43, s43, -1\n" \
"ds_read_b128 a[72:75], v205 offset:0\n" \
"v_mfma_f32_16x16x32_bf16 v[136:139], a[208:211], v[16:19], v[136:139]\n" \
"ds_read_b64_tr_b16 a[104:105], v209 offset:0\n" \
"global_load_lds_dwordx4 v252, s[44:45] offset:1024\n" \
"v_mfma_f32_16x16x32_bf16 v[136:139], a[212:215], v[48:51], v[136:139]\n" \
"ds_read_b128 a[76:79], v206 offset:0\n" \
"ds_read_b64_tr_b16 a[106:107], v209 offset:1024\n" \
"v_mfma_f32_16x16x32_bf16 v[140:143], a[216:219], v[16:19], v[140:143]\n" \
"ds_read_b64_tr_b16 a[108:109], v209 offset:12288\n" \
"v_add_f32 v242, v242, v244\n" \
"v_mfma_f32_16x16x32_bf16 v[140:143], a[220:223], v[48:51], v[140:143]\n" \
"ds_read_b64_tr_b16 a[110:111], v209 offset:13312\n" \
"global_load_lds_dwordx4 v252, s[44:45] offset:3072\n" \
"v_mfma_f32_16x16x32_bf16 v[144:147], a[224:227], v[16:19], v[144:147]\n" \
"ds_read_b128 a[80:83], v205 offset:4096\n" \
"ds_read_b64_tr_b16 a[112:113], v210 offset:0\n" \
"v_mfma_f32_16x16x32_bf16 v[144:147], a[228:231], v[48:51], v[144:147]\n" \
"s_add_u32 s44, s44, 0xfffffb00\n" \
"s_addc_u32 s45, s45, -1\n" \
"ds_read_b64_tr_b16 a[114:115], v210 offset:1024\n" \
"v_mfma_f32_16x16x32_bf16 v[148:151], a[232:235], v[16:19], v[148:151]\n" \
"s_add_u32 m0, s85, 16384\n" \
"ds_read_b64_tr_b16 a[116:117], v210 offset:12288\n" \
"v_add_f32 v243, v243, v245\n" \
"v_mfma_f32_16x16x32_bf16 v[148:151], a[236:239], v[48:51], v[148:151]\n" \
"ds_read_b128 a[84:87], v206 offset:4096\n" \
"ds_read_b64_tr_b16 a[118:119], v210 offset:13312\n" \
"v_mfma_f32_16x16x32_bf16 v[152:155], a[240:243], v[16:19], v[152:155]\n" \
"ds_read_b128 a[88:91], v205 offset:8192\n" \
"v_mfma_f32_16x16x32_bf16 v[152:155], a[244:247], v[48:51], v[152:155]\n" \
"ds_read_b128 a[92:95], v206 offset:8192\n" \
"v_mfma_f32_16x16x32_bf16 v[156:159], a[248:251], v[16:19], v[156:159]\n" \
"buffer_load_dwordx4 v246, s[12:15], s46 offen lds\n" \
"v_mfma_f32_16x16x32_bf16 v[156:159], a[252:255], v[48:51], v[156:159]\n" \
"s_waitcnt lgkmcnt(4)\n" \
"Ljoin_%=:\n" \
"v_mfma_f32_16x16x32_bf16 v[32:35], a[96:99], a[64:67], 0\n" \
"s_add_u32 s46, s46, 0xfffffb00\n" \
"ds_read_b128 a[192:195], v205 offset:12288\n" \
"s_add_u32 m0, s84, 24576\n" \
"v_mfma_f32_16x16x32_bf16 v[32:35], a[100:103], a[68:71], v[32:35]\n" \
"global_load_lds_dwordx4 v253, s[48:49] offset:-4096\n" \
"v_mfma_f32_16x16x32_bf16 v[40:43], a[96:99], a[72:75], 0\n" \
"ds_read_b128 a[196:199], v206 offset:12288\n" \
"v_mfma_f32_16x16x32_bf16 v[40:43], a[100:103], a[76:79], v[40:43]\n" \
"ds_read_b64_tr_b16 a[200:201], v210 offset:2048\n" \
"v_mov_b32 v232, v242\n" \
"v_mfma_f32_16x16x32_bf16 v[0:3], a[104:107], a[72:75], 0\n" \
"ds_read_b64_tr_b16 a[202:203], v210 offset:3072\n" \
"global_load_lds_dwordx4 v253, s[48:49] offset:-2048\n" \
"v_mfma_f32_16x16x32_bf16 v[0:3], a[108:111], a[76:79], v[0:3]\n" \
"s_waitcnt lgkmcnt(3)\n" \
"v_permlane16_swap_b32 v232, v242\n" \
"v_mfma_f32_16x16x32_bf16 v[4:7], a[112:115], a[72:75], 0\n" \
"ds_read_b64_tr_b16 a[204:205], v210 offset:14336\n" \
"v_add_f32 v242, v242, v232\n" \
"v_mfma_f32_16x16x32_bf16 v[4:7], a[116:119], a[76:79], v[4:7]\n" \
"ds_read_b64_tr_b16 a[206:207], v210 offset:15360\n" \
"global_load_lds_dwordx4 v253, s[48:49] offset:0\n" \
"v_mfma_f32_16x16x32_bf16 v[48:51], a[96:99], a[80:83], 0\n" \
"s_add_u32 s48, s48, 0xffc40000\n" \
"s_addc_u32 s49, s49, -1\n" \
"ds_read_b64_tr_b16 a[208:209], v209 offset:4096\n" \
"v_mfma_f32_16x16x32_bf16 v[48:51], a[100:103], a[84:87], v[48:51]\n" \
"v_mov_b32 v232, v242\n" \
"v_mfma_f32_16x16x32_bf16 v[8:11], a[104:107], a[80:83], 0\n" \
"ds_read_b64_tr_b16 a[210:211], v209 offset:5120\n" \
"global_load_lds_dwordx4 v253, s[50:51] offset:-3072\n" \
"v_mfma_f32_16x16x32_bf16 v[8:11], a[108:111], a[84:87], v[8:11]\n" \
"ds_read_b64_tr_b16 a[212:213], v209 offset:16384\n" \
"v_mfma_f32_16x16x32_bf16 v[12:15], a[112:115], a[80:83], 0\n" \
"ds_read_b64_tr_b16 a[214:215], v209 offset:17408\n" \
"v_mfma_f32_16x16x32_bf16 v[12:15], a[116:119], a[84:87], v[12:15]\n" \
"global_load_lds_dwordx4 v253, s[50:51] offset:-1024\n" \
"v_mfma_f32_16x16x32_bf16 v[56:59], a[96:99], a[88:91], 0\n" \
"ds_read_b64_tr_b16 a[216:217], v210 offset:4096\n" \
"v_mfma_f32_16x16x32_bf16 v[56:59], a[100:103], a[92:95], v[56:59]\n" \
"ds_read_b64_tr_b16 a[218:219], v210 offset:5120\n" \
"v_mfma_f32_16x16x32_bf16 v[16:19], a[104:107], a[88:91], 0\n" \
"ds_read_b64_tr_b16 a[220:221], v210 offset:16384\n" \
"global_load_lds_dwordx4 v253, s[50:51] offset:1024\n" \
"v_mfma_f32_16x16x32_bf16 v[16:19], a[108:111], a[92:95], v[16:19]\n" \
"s_add_u32 s50, s50, 0xffc40000\n" \
"s_addc_u32 s51, s51, -1\n" \
"v_mfma_f32_16x16x32_bf16 v[20:23], a[112:115], a[88:91], 0\n" \
"ds_read_b64_tr_b16 a[222:223], v210 offset:17408\n" \
"v_mfma_f32_16x16x32_bf16 v[20:23], a[116:119], a[92:95], v[20:23]\n" \
"s_waitcnt lgkmcnt(3)\n" \
"v_add_u32 v207, 0xffff3000, v207\n" \
"v_add_u32 v208, 0xffff3000, v208\n" \
"v_mfma_f32_16x16x32_bf16 v[64:67], a[96:99], a[192:195], 0\n" \
"v_add_u32 v205, 0xffff3000, v205\n" \
"v_add_u32 v206, 0xffff3000, v206\n" \
"v_mfma_f32_16x16x32_bf16 v[64:67], a[100:103], a[196:199], v[64:67]\n" \
"v_add_u32 v209, 0xffff3000, v209\n" \
"v_add_u32 v210, 0xffff3000, v210\n" \
"v_mfma_f32_16x16x32_bf16 v[24:27], a[104:107], a[192:195], 0\n" \
"v_permlane32_swap_b32 v232, v242\n" \
"v_mfma_f32_16x16x32_bf16 v[24:27], a[108:111], a[196:199], v[24:27]\n" \
"v_add_f32 v242, v242, v232\n" \
"v_mfma_f32_16x16x32_bf16 v[28:31], a[112:115], a[192:195], 0\n" \
"s_waitcnt vmcnt(11) lgkmcnt(0)\n" \
"v_rcp_f32 v232, v242\n" \
"v_mfma_f32_16x16x32_bf16 v[28:31], a[116:119], a[196:199], v[28:31]\n" \
"s_barrier\n" \
"v_mul_f32 v242, 0x4d800000, v242\n" \
"v_mfma_f32_16x16x32_bf16 v[72:75], a[64:67], a[208:211], 0\n" \
"ds_read_b64_tr_b16 a[96:97], v209 offset:2048\n" \
"v_add_u32 v233, 0x1c90080, v249\n" \
"v_mfma_f32_16x16x32_bf16 v[72:75], a[68:71], a[212:215], v[72:75]\n" \
"ds_read_b64_tr_b16 a[98:99], v209 offset:3072\n" \
"v_cndmask_b32 v233, v249, v233, s[16:17]\n" \
"v_mfma_f32_16x16x32_bf16 v[44:47], a[200:203], a[72:75], 0\n" \
"s_add_u32 m0, s95, 118784\n" \
"ds_read_b128 a[64:67], v207 offset:0\n" \
"ds_read_b64_tr_b16 a[100:101], v209 offset:14336\n" \
"v_mfma_f32_16x16x32_bf16 v[44:47], a[204:207], a[76:79], v[44:47]\n" \
"global_load_lds_dwordx4 v252, s[42:43] offset:0\n" \
"v_mul_f32 v128, v128, v232\n" \
"v_mfma_f32_16x16x32_bf16 v[80:83], a[72:75], a[208:211], 0\n" \
"ds_read_b128 a[68:71], v208 offset:0\n" \
"ds_read_b64_tr_b16 a[102:103], v209 offset:15360\n" \
"v_mfma_f32_16x16x32_bf16 v[80:83], a[76:79], a[212:215], v[80:83]\n" \
"ds_read_b64_tr_b16 a[104:105], v209 offset:0\n" \
"v_mul_f32 v129, v129, v232\n" \
"v_mfma_f32_16x16x32_bf16 v[84:87], a[72:75], a[216:219], 0\n" \
"ds_read_b64_tr_b16 a[106:107], v209 offset:1024\n" \
"v_mul_f32 v130, v130, v232\n" \
"v_mfma_f32_16x16x32_bf16 v[84:87], a[76:79], a[220:223], v[84:87]\n" \
"global_load_lds_dwordx4 v252, s[42:43] offset:2048\n" \
"v_mul_f32 v131, v131, v232\n" \
"v_mfma_f32_16x16x32_bf16 v[52:55], a[200:203], a[80:83], 0\n" \
"s_add_u32 s42, s42, 0x80\n" \
"s_addc_u32 s43, s43, 0\n" \
"ds_read_b128 a[72:75], v205 offset:0\n" \
"v_mfma_f32_16x16x32_bf16 v[52:55], a[204:207], a[84:87], v[52:55]\n" \
"ds_read_b128 a[76:79], v206 offset:0\n" \
"v_mul_f32 v132, v132, v232\n" \
"v_mfma_f32_16x16x32_bf16 v[88:91], a[80:83], a[208:211], 0\n" \
"ds_read_b64_tr_b16 a[108:109], v209 offset:12288\n" \
"v_mul_f32 v133, v133, v232\n" \
"v_mfma_f32_16x16x32_bf16 v[88:91], a[84:87], a[212:215], v[88:91]\n" \
"global_load_lds_dwordx4 v252, s[44:45] offset:1024\n" \
"v_mul_f32 v134, v134, v232\n" \
"v_mfma_f32_16x16x32_bf16 v[92:95], a[80:83], a[216:219], 0\n" \
"ds_read_b64_tr_b16 a[110:111], v209 offset:13312\n" \
"v_mul_f32 v135, v135, v232\n" \
"v_mfma_f32_16x16x32_bf16 v[92:95], a[84:87], a[220:223], v[92:95]\n" \
"ds_read_b64_tr_b16 a[112:113], v210 offset:0\n" \
"v_fma_f32 v242, v128, v129, v242\n" \
"v_mfma_f32_16x16x32_bf16 v[60:63], a[200:203], a[88:91], 0\n" \
"ds_read_b128 a[80:83], v205 offset:4096\n" \
"v_cvt_pk_bf16_f32 v128, v128, v129\n" \
"v_mfma_f32_16x16x32_bf16 v[60:63], a[204:207], a[92:95], v[60:63]\n" \
"global_load_lds_dwordx4 v252, s[44:45] offset:3072\n" \
"v_fma_f32 v242, v130, v131, v242\n" \
"v_mfma_f32_16x16x32_bf16 v[96:99], a[88:91], a[208:211], 0\n" \
"s_add_u32 s44, s44, 0x80\n" \
"s_addc_u32 s45, s45, 0\n" \
"ds_read_b128 a[84:87], v206 offset:4096\n" \
"v_mfma_f32_16x16x32_bf16 v[96:99], a[92:95], a[212:215], v[96:99]\n" \
"ds_read_b64_tr_b16 a[114:115], v210 offset:1024\n" \
"v_cvt_pk_bf16_f32 v129, v130, v131\n" \
"v_mfma_f32_16x16x32_bf16 v[100:103], a[88:91], a[216:219], 0\n" \
"s_add_u32 m0, s85, 135168\n" \
"ds_read_b64_tr_b16 a[116:117], v210 offset:12288\n" \
"v_fma_f32 v242, v132, v133, v242\n" \
"v_mfma_f32_16x16x32_bf16 v[100:103], a[92:95], a[220:223], v[100:103]\n" \
"buffer_load_dwordx4 v246, s[12:15], s46 offen lds\n" \
"v_mfma_f32_16x16x32_bf16 v[68:71], a[200:203], a[192:195], 0\n" \
"s_add_u32 s46, s46, 0x80\n" \
"ds_read_b128 a[88:91], v205 offset:8192\n" \
"v_cvt_pk_bf16_f32 v130, v132, v133\n" \
"v_mfma_f32_16x16x32_bf16 v[68:71], a[204:207], a[196:199], v[68:71]\n" \
"ds_read_b128 a[92:95], v206 offset:8192\n" \
"v_fma_f32 v242, v134, v135, v242\n" \
"v_mfma_f32_16x16x32_bf16 v[104:107], a[192:195], a[208:211], 0\n" \
"s_add_u32 m0, s84, 143360\n" \
"ds_read_b64_tr_b16 a[118:119], v210 offset:13312\n" \
"v_cvt_pk_bf16_f32 v131, v134, v135\n" \
"v_mfma_f32_16x16x32_bf16 v[104:107], a[196:199], a[212:215], v[104:107]\n" \
"buffer_store_dwordx4 v[128:131], v233, s[56:59], s78 offen offset:0 sc1\n" \
"global_load_lds_dwordx4 v253, s[48:49] offset:-4096\n" \
"v_mul_f32 v136, v136, v232\n" \
"v_mul_f32 v137, v137, v232\n" \
"v_mul_f32 v138, v138, v232\n" \
"v_mfma_f32_16x16x32_bf16 v[108:111], a[192:195], a[216:219], 0\n" \
"v_mul_f32 v139, v139, v232\n" \
"v_mul_f32 v140, v140, v232\n" \
"v_mfma_f32_16x16x32_bf16 v[108:111], a[196:199], a[220:223], v[108:111]\n" \
"s_waitcnt lgkmcnt(2)\n" \
"v_mul_f32 v141, v141, v232\n" \
"v_mul_f32 v142, v142, v232\n" \
"v_mfma_f32_16x16x32_bf16 v[32:35], a[96:99], a[64:67], v[32:35]\n" \
"ds_read_b128 a[192:195], v205 offset:12288\n" \
"v_mul_f32 v143, v143, v232\n" \
"v_mfma_f32_16x16x32_bf16 v[32:35], a[100:103], a[68:71], v[32:35]\n" \
"global_load_lds_dwordx4 v253, s[48:49] offset:-2048\n" \
"v_fma_f32 v242, v136, v137, v242\n" \
"v_mfma_f32_16x16x32_bf16 v[40:43], a[96:99], a[72:75], v[40:43]\n" \
"ds_read_b128 a[196:199], v206 offset:12288\n" \
"v_cvt_pk_bf16_f32 v136, v136, v137\n" \
"v_mfma_f32_16x16x32_bf16 v[40:43], a[100:103], a[76:79], v[40:43]\n" \
"ds_read_b64_tr_b16 a[200:201], v210 offset:2048\n" \
"v_fma_f32 v242, v138, v139, v242\n" \
"v_mfma_f32_16x16x32_bf16 v[0:3], a[104:107], a[72:75], v[0:3]\n" \
"ds_read_b64_tr_b16 a[202:203], v210 offset:3072\n" \
"v_cvt_pk_bf16_f32 v137, v138, v139\n" \
"v_mfma_f32_16x16x32_bf16 v[0:3], a[108:111], a[76:79], v[0:3]\n" \
"s_waitcnt lgkmcnt(3)\n" \
"global_load_lds_dwordx4 v253, s[48:49] offset:0\n" \
"v_fma_f32 v242, v140, v141, v242\n" \
"v_mfma_f32_16x16x32_bf16 v[4:7], a[112:115], a[72:75], v[4:7]\n" \
"s_add_u32 s48, s48, 0x60000\n" \
"s_addc_u32 s49, s49, 0\n" \
"ds_read_b64_tr_b16 a[204:205], v210 offset:14336\n" \
"v_mfma_f32_16x16x32_bf16 v[4:7], a[116:119], a[76:79], v[4:7]\n" \
"ds_read_b64_tr_b16 a[206:207], v210 offset:15360\n" \
"v_cvt_pk_bf16_f32 v138, v140, v141\n" \
"v_mfma_f32_16x16x32_bf16 v[48:51], a[96:99], a[80:83], v[48:51]\n" \
"ds_read_b64_tr_b16 a[208:209], v209 offset:4096\n" \
"v_fma_f32 v242, v142, v143, v242\n" \
"v_mfma_f32_16x16x32_bf16 v[48:51], a[100:103], a[84:87], v[48:51]\n" \
"global_load_lds_dwordx4 v253, s[50:51] offset:-3072\n" \
"v_cvt_pk_bf16_f32 v139, v142, v143\n" \
"v_mfma_f32_16x16x32_bf16 v[8:11], a[104:107], a[80:83], v[8:11]\n" \
"buffer_store_dwordx4 v[136:139], v233, s[56:59], s78 offen offset:2048 sc1\n" \
"ds_read_b64_tr_b16 a[210:211], v209 offset:5120\n" \
"s_add_u32 s78, s78, 0x1000\n" \
"v_mul_f32 v144, v144, v232\n" \
"v_mul_f32 v145, v145, v232\n" \
"v_mfma_f32_16x16x32_bf16 v[8:11], a[108:111], a[84:87], v[8:11]\n" \
"ds_read_b64_tr_b16 a[212:213], v209 offset:16384\n" \
"v_mul_f32 v146, v146, v232\n" \
"v_mfma_f32_16x16x32_bf16 v[12:15], a[112:115], a[80:83], v[12:15]\n" \
"ds_read_b64_tr_b16 a[214:215], v209 offset:17408\n" \
"v_mul_f32 v147, v147, v232\n" \
"v_mfma_f32_16x16x32_bf16 v[12:15], a[116:119], a[84:87], v[12:15]\n" \
"global_load_lds_dwordx4 v253, s[50:51] offset:-1024\n" \
"v_mul_f32 v148, v148, v232\n" \
"v_mfma_f32_16x16x32_bf16 v[56:59], a[96:99], a[88:91], v[56:59]\n" \
"ds_read_b64_tr_b16 a[216:217], v210 offset:4096\n" \
"v_mul_f32 v149, v149, v232\n" \
"v_mfma_f32_16x16x32_bf16 v[56:59], a[100:103], a[92:95], v[56:59]\n" \
"ds_read_b64_tr_b16 a[218:219], v210 offset:5120\n" \
"v_mul_f32 v150, v150, v232\n" \
"v_mfma_f32_16x16x32_bf16 v[16:19], a[104:107], a[88:91], v[16:19]\n" \
"ds_read_b64_tr_b16 a[220:221], v210 offset:16384\n" \
"v_mul_f32 v151, v151, v232\n" \
"v_mfma_f32_16x16x32_bf16 v[16:19], a[108:111], a[92:95], v[16:19]\n" \
"global_load_lds_dwordx4 v253, s[50:51] offset:1024\n" \
"v_fma_f32 v242, v144, v145, v242\n" \
"v_mfma_f32_16x16x32_bf16 v[20:23], a[112:115], a[88:91], v[20:23]\n" \
"s_add_u32 s50, s50, 0x60000\n" \
"s_addc_u32 s51, s51, 0\n" \
"ds_read_b64_tr_b16 a[222:223], v210 offset:17408\n" \
"v_mfma_f32_16x16x32_bf16 v[20:23], a[116:119], a[92:95], v[20:23]\n" \
"s_waitcnt lgkmcnt(3)\n" \
"v_add_u32 v207, 0xffff0000, v207\n" \
"v_add_u32 v208, 0xffff0000, v208\n" \
"v_mfma_f32_16x16x32_bf16 v[64:67], a[96:99], a[192:195], v[64:67]\n" \
"v_add_u32 v205, 0xffff0000, v205\n" \
"v_add_u32 v206, 0xffff0000, v206\n" \
"v_mfma_f32_16x16x32_bf16 v[64:67], a[100:103], a[196:199], v[64:67]\n" \
"v_add_u32 v209, 0xffff0000, v209\n" \
"v_add_u32 v210, 0xffff0000, v210\n" \
"v_mfma_f32_16x16x32_bf16 v[24:27], a[104:107], a[192:195], v[24:27]\n" \
"v_cvt_pk_bf16_f32 v144, v144, v145\n" \
"v_fma_f32 v242, v146, v147, v242\n" \
"v_mfma_f32_16x16x32_bf16 v[24:27], a[108:111], a[196:199], v[24:27]\n" \
"v_cvt_pk_bf16_f32 v145, v146, v147\n" \
"v_fma_f32 v242, v148, v149, v242\n" \
"v_mfma_f32_16x16x32_bf16 v[28:31], a[112:115], a[192:195], v[28:31]\n" \
"s_waitcnt vmcnt(13) lgkmcnt(0)\n" \
"v_cvt_pk_bf16_f32 v146, v148, v149\n" \
"v_fma_f32 v242, v150, v151, v242\n" \
"v_mfma_f32_16x16x32_bf16 v[28:31], a[116:119], a[196:199], v[28:31]\n" \
"s_barrier\n" \
"v_cvt_pk_bf16_f32 v147, v150, v151\n" \
"v_mfma_f32_16x16x32_bf16 v[72:75], a[64:67], a[208:211], v[72:75]\n" \
"buffer_store_dwordx4 v[144:147], v233, s[56:59], s78 offen offset:0 sc1\n" \
"ds_read_b64_tr_b16 a[96:97], v209 offset:2048\n" \
"v_mul_f32 v152, v152, v232\n" \
"v_mul_f32 v153, v153, v232\n" \
"v_mul_f32 v154, v154, v232\n" \
"v_mfma_f32_16x16x32_bf16 v[72:75], a[68:71], a[212:215], v[72:75]\n" \
"ds_read_b64_tr_b16 a[98:99], v209 offset:3072\n" \
"v_mul_f32 v155, v155, v232\n" \
"v_mfma_f32_16x16x32_bf16 v[44:47], a[200:203], a[72:75], v[44:47]\n" \
"s_add_u32 m0, s95, 65536\n" \
"ds_read_b128 a[64:67], v207 offset:0\n" \
"ds_read_b64_tr_b16 a[100:101], v209 offset:14336\n" \
"v_mfma_f32_16x16x32_bf16 v[44:47], a[204:207], a[76:79], v[44:47]\n" \
"global_load_lds_dwordx4 v252, s[42:43] offset:0\n" \
"v_mul_f32 v156, v156, v232\n" \
"v_mfma_f32_16x16x32_bf16 v[80:83], a[72:75], a[208:211], v[80:83]\n" \
"ds_read_b128 a[68:71], v208 offset:0\n" \
"ds_read_b64_tr_b16 a[102:103], v209 offset:15360\n" \
"v_mfma_f32_16x16x32_bf16 v[80:83], a[76:79], a[212:215], v[80:83]\n" \
"ds_read_b64_tr_b16 a[104:105], v209 offset:0\n" \
"v_mul_f32 v157, v157, v232\n" \
"v_mfma_f32_16x16x32_bf16 v[84:87], a[72:75], a[216:219], v[84:87]\n" \
"ds_read_b64_tr_b16 a[106:107], v209 offset:1024\n" \
"v_mul_f32 v158, v158, v232\n" \
"v_mfma_f32_16x16x32_bf16 v[84:87], a[76:79], a[220:223], v[84:87]\n" \
"global_load_lds_dwordx4 v252, s[42:43] offset:2048\n" \
"v_mul_f32 v159, v159, v232\n" \
"v_mfma_f32_16x16x32_bf16 v[52:55], a[200:203], a[80:83], v[52:55]\n" \
"s_add_u32 s42, s42, 0x100\n" \
"s_addc_u32 s43, s43, 0\n" \
"ds_read_b128 a[72:75], v205 offset:0\n" \
"v_mfma_f32_16x16x32_bf16 v[52:55], a[204:207], a[84:87], v[52:55]\n" \
"ds_read_b128 a[76:79], v206 offset:0\n" \
"v_fma_f32 v242, v152, v153, v242\n" \
"v_mfma_f32_16x16x32_bf16 v[88:91], a[80:83], a[208:211], v[88:91]\n" \
"ds_read_b64_tr_b16 a[108:109], v209 offset:12288\n" \
"v_cvt_pk_bf16_f32 v152, v152, v153\n" \
"v_mfma_f32_16x16x32_bf16 v[88:91], a[84:87], a[212:215], v[88:91]\n" \
"global_load_lds_dwordx4 v252, s[44:45] offset:1024\n" \
"v_fma_f32 v242, v154, v155, v242\n" \
"v_mfma_f32_16x16x32_bf16 v[92:95], a[80:83], a[216:219], v[92:95]\n" \
"ds_read_b64_tr_b16 a[110:111], v209 offset:13312\n" \
"v_cvt_pk_bf16_f32 v153, v154, v155\n" \
"v_mfma_f32_16x16x32_bf16 v[92:95], a[84:87], a[220:223], v[92:95]\n" \
"ds_read_b64_tr_b16 a[112:113], v210 offset:0\n" \
"v_fma_f32 v242, v156, v157, v242\n" \
"v_mfma_f32_16x16x32_bf16 v[60:63], a[200:203], a[88:91], v[60:63]\n" \
"ds_read_b128 a[80:83], v205 offset:4096\n" \
"v_cvt_pk_bf16_f32 v154, v156, v157\n" \
"v_mfma_f32_16x16x32_bf16 v[60:63], a[204:207], a[92:95], v[60:63]\n" \
"global_load_lds_dwordx4 v252, s[44:45] offset:3072\n" \
"v_fma_f32 v242, v158, v159, v242\n" \
"v_mfma_f32_16x16x32_bf16 v[96:99], a[88:91], a[208:211], v[96:99]\n" \
"s_add_u32 s44, s44, 0x100\n" \
"s_addc_u32 s45, s45, 0\n" \
"ds_read_b128 a[84:87], v206 offset:4096\n" \
"v_mfma_f32_16x16x32_bf16 v[96:99], a[92:95], a[212:215], v[96:99]\n" \
"ds_read_b64_tr_b16 a[114:115], v210 offset:1024\n" \
"v_cvt_pk_bf16_f32 v155, v158, v159\n" \
"v_mfma_f32_16x16x32_bf16 v[100:103], a[88:91], a[216:219], v[100:103]\n" \
"buffer_store_dwordx4 v[152:155], v233, s[56:59], s78 offen offset:2048 sc1\n" \
"s_add_u32 m0, s85, 81920\n" \
"ds_read_b64_tr_b16 a[116:117], v210 offset:12288\n" \
"s_add_u32 s78, s78, 0x81000\n" \
"v_mov_b32 v234, v243\n" \
"v_mfma_f32_16x16x32_bf16 v[100:103], a[92:95], a[220:223], v[100:103]\n" \
"buffer_load_dwordx4 v246, s[12:15], s46 offen lds\n" \
"v_mfma_f32_16x16x32_bf16 v[68:71], a[200:203], a[192:195], v[68:71]\n" \
"s_add_u32 s46, s46, 0x100\n" \
"ds_read_b128 a[88:91], v205 offset:8192\n" \
"v_permlane16_swap_b32 v234, v243\n" \
"v_mfma_f32_16x16x32_bf16 v[68:71], a[204:207], a[196:199], v[68:71]\n" \
"ds_read_b128 a[92:95], v206 offset:8192\n" \
"v_add_f32 v243, v243, v234\n" \
"v_mfma_f32_16x16x32_bf16 v[104:107], a[192:195], a[208:211], v[104:107]\n" \
"s_add_u32 m0, s84, 90112\n" \
"ds_read_b64_tr_b16 a[118:119], v210 offset:13312\n" \
"v_mov_b32 v234, v243\n" \
"v_mfma_f32_16x16x32_bf16 v[104:107], a[196:199], a[212:215], v[104:107]\n" \
"global_load_lds_dwordx4 v253, s[48:49] offset:-4096\n" \
"v_mfma_f32_16x16x32_bf16 v[108:111], a[192:195], a[216:219], v[108:111]\n" \
"v_permlane32_swap_b32 v234, v243\n" \
"v_add_f32 v243, v243, v234\n" \
"v_mfma_f32_16x16x32_bf16 v[108:111], a[196:199], a[220:223], v[108:111]\n" \
"s_waitcnt lgkmcnt(2)\n" \
"v_rcp_f32 v234, v243\n" \
"v_mfma_f32_16x16x32_bf16 v[32:35], a[96:99], a[64:67], v[32:35]\n" \
"ds_read_b128 a[192:195], v205 offset:12288\n" \
"v_mul_f32 v243, 0x4d800000, v243\n" \
"v_mfma_f32_16x16x32_bf16 v[32:35], a[100:103], a[68:71], v[32:35]\n" \
"global_load_lds_dwordx4 v253, s[48:49] offset:-2048\n" \
"v_add_u32 v235, 0x1c90080, v249\n" \
"v_mfma_f32_16x16x32_bf16 v[40:43], a[96:99], a[72:75], v[40:43]\n" \
"ds_read_b128 a[196:199], v206 offset:12288\n" \
"v_cndmask_b32 v235, v249, v235, s[16:17]\n" \
"v_mfma_f32_16x16x32_bf16 v[40:43], a[100:103], a[76:79], v[40:43]\n" \
"ds_read_b64_tr_b16 a[200:201], v210 offset:2048\n" \
"v_mul_f32 v160, v160, v234\n" \
"v_mfma_f32_16x16x32_bf16 v[0:3], a[104:107], a[72:75], v[0:3]\n" \
"ds_read_b64_tr_b16 a[202:203], v210 offset:3072\n" \
"v_mul_f32 v161, v161, v234\n" \
"v_mfma_f32_16x16x32_bf16 v[0:3], a[108:111], a[76:79], v[0:3]\n" \
"s_waitcnt lgkmcnt(3)\n" \
"global_load_lds_dwordx4 v253, s[48:49] offset:0\n" \
"v_mul_f32 v162, v162, v234\n" \
"v_mfma_f32_16x16x32_bf16 v[4:7], a[112:115], a[72:75], v[4:7]\n" \
"s_add_u32 s48, s48, 0xc0000\n" \
"s_addc_u32 s49, s49, 0\n" \
"ds_read_b64_tr_b16 a[204:205], v210 offset:14336\n" \
"v_mfma_f32_16x16x32_bf16 v[4:7], a[116:119], a[76:79], v[4:7]\n" \
"ds_read_b64_tr_b16 a[206:207], v210 offset:15360\n" \
"v_mul_f32 v163, v163, v234\n" \
"v_mfma_f32_16x16x32_bf16 v[48:51], a[96:99], a[80:83], v[48:51]\n" \
"ds_read_b64_tr_b16 a[208:209], v209 offset:4096\n" \
"v_mul_f32 v164, v164, v234\n" \
"v_mfma_f32_16x16x32_bf16 v[48:51], a[100:103], a[84:87], v[48:51]\n" \
"global_load_lds_dwordx4 v253, s[50:51] offset:-3072\n" \
"v_mul_f32 v165, v165, v234\n" \
"v_mfma_f32_16x16x32_bf16 v[8:11], a[104:107], a[80:83], v[8:11]\n" \
"ds_read_b64_tr_b16 a[210:211], v209 offset:5120\n" \
"v_mul_f32 v166, v166, v234\n" \
"v_mfma_f32_16x16x32_bf16 v[8:11], a[108:111], a[84:87], v[8:11]\n" \
"ds_read_b64_tr_b16 a[212:213], v209 offset:16384\n" \
"v_mul_f32 v167, v167, v234\n" \
"v_mfma_f32_16x16x32_bf16 v[12:15], a[112:115], a[80:83], v[12:15]\n" \
"ds_read_b64_tr_b16 a[214:215], v209 offset:17408\n" \
"v_fma_f32 v243, v160, v161, v243\n" \
"v_mfma_f32_16x16x32_bf16 v[12:15], a[116:119], a[84:87], v[12:15]\n" \
"global_load_lds_dwordx4 v253, s[50:51] offset:-1024\n" \
"v_cvt_pk_bf16_f32 v160, v160, v161\n" \
"v_mfma_f32_16x16x32_bf16 v[56:59], a[96:99], a[88:91], v[56:59]\n" \
"ds_read_b64_tr_b16 a[216:217], v210 offset:4096\n" \
"v_fma_f32 v243, v162, v163, v243\n" \
"v_mfma_f32_16x16x32_bf16 v[56:59], a[100:103], a[92:95], v[56:59]\n" \
"ds_read_b64_tr_b16 a[218:219], v210 offset:5120\n" \
"v_cvt_pk_bf16_f32 v161, v162, v163\n" \
"v_mfma_f32_16x16x32_bf16 v[16:19], a[104:107], a[88:91], v[16:19]\n" \
"ds_read_b64_tr_b16 a[220:221], v210 offset:16384\n" \
"v_fma_f32 v243, v164, v165, v243\n" \
"v_mfma_f32_16x16x32_bf16 v[16:19], a[108:111], a[92:95], v[16:19]\n" \
"global_load_lds_dwordx4 v253, s[50:51] offset:1024\n" \
"v_cvt_pk_bf16_f32 v162, v164, v165\n" \
"v_mfma_f32_16x16x32_bf16 v[20:23], a[112:115], a[88:91], v[20:23]\n" \
"s_add_u32 s50, s50, 0xc0000\n" \
"s_addc_u32 s51, s51, 0\n" \
"ds_read_b64_tr_b16 a[222:223], v210 offset:17408\n" \
"v_mfma_f32_16x16x32_bf16 v[20:23], a[116:119], a[92:95], v[20:23]\n" \
"s_waitcnt lgkmcnt(3)\n" \
"v_add_u32 v207, 0x1d000, v207\n" \
"v_add_u32 v208, 0x1d000, v208\n" \
"v_mfma_f32_16x16x32_bf16 v[64:67], a[96:99], a[192:195], v[64:67]\n" \
"v_add_u32 v205, 0x1d000, v205\n" \
"v_add_u32 v206, 0x1d000, v206\n" \
"v_mfma_f32_16x16x32_bf16 v[64:67], a[100:103], a[196:199], v[64:67]\n" \
"v_add_u32 v209, 0x1d000, v209\n" \
"v_add_u32 v210, 0x1d000, v210\n" \
"v_mfma_f32_16x16x32_bf16 v[24:27], a[104:107], a[192:195], v[24:27]\n" \
"v_fma_f32 v243, v166, v167, v243\n" \
"v_cvt_pk_bf16_f32 v163, v166, v167\n" \
"v_mfma_f32_16x16x32_bf16 v[24:27], a[108:111], a[196:199], v[24:27]\n" \
"buffer_store_dwordx4 v[160:163], v235, s[56:59], s78 offen offset:0 sc1\n" \
"v_mul_f32 v168, v168, v234\n" \
"v_mul_f32 v169, v169, v234\n" \
"v_mul_f32 v170, v170, v234\n" \
"v_mul_f32 v171, v171, v234\n" \
"v_mfma_f32_16x16x32_bf16 v[28:31], a[112:115], a[192:195], v[28:31]\n" \
"s_waitcnt vmcnt(14) lgkmcnt(0)\n" \
"v_mul_f32 v172, v172, v234\n" \
"v_mul_f32 v173, v173, v234\n" \
"v_mfma_f32_16x16x32_bf16 v[28:31], a[116:119], a[196:199], v[28:31]\n" \
"s_barrier\n" \
"v_mul_f32 v174, v174, v234\n" \
"v_mfma_f32_16x16x32_bf16 v[72:75], a[64:67], a[208:211], v[72:75]\n" \
"ds_read_b64_tr_b16 a[96:97], v209 offset:2048\n" \
"v_mul_f32 v175, v175, v234\n" \
"v_mfma_f32_16x16x32_bf16 v[72:75], a[68:71], a[212:215], v[72:75]\n" \
"ds_read_b64_tr_b16 a[98:99], v209 offset:3072\n" \
"v_fma_f32 v243, v168, v169, v243\n" \
"v_mfma_f32_16x16x32_bf16 v[44:47], a[200:203], a[72:75], v[44:47]\n" \
"s_add_u32 m0, s95, 0\n" \
"ds_read_b128 a[64:67], v207 offset:0\n" \
"ds_read_b64_tr_b16 a[100:101], v209 offset:14336\n" \
"v_mfma_f32_16x16x32_bf16 v[44:47], a[204:207], a[76:79], v[44:47]\n" \
"Lpg9319_%=:\n" \
"Lst1x3_%=:\n" \
"s_cbranch_execz Lpg11578_%=\n" \
"global_load_lds_dwordx4 v252, s[42:43] offset:0\n" \
"v_cvt_pk_bf16_f32 v168, v168, v169\n" \
"v_mfma_f32_16x16x32_bf16 v[80:83], a[72:75], a[208:211], v[80:83]\n" \
"ds_read_b128 a[68:71], v208 offset:0\n" \
"ds_read_b64_tr_b16 a[102:103], v209 offset:15360\n" \
"v_mfma_f32_16x16x32_bf16 v[80:83], a[76:79], a[212:215], v[80:83]\n" \
"ds_read_b64_tr_b16 a[104:105], v209 offset:0\n" \
"v_fma_f32 v243, v170, v171, v243\n" \
"v_mfma_f32_16x16x32_bf16 v[84:87], a[72:75], a[216:219], v[84:87]\n" \
"ds_read_b64_tr_b16 a[106:107], v209 offset:1024\n" \
"v_cvt_pk_bf16_f32 v169, v170, v171\n" \
"v_mfma_f32_16x16x32_bf16 v[84:87], a[76:79], a[220:223], v[84:87]\n" \
"global_load_lds_dwordx4 v252, s[42:43] offset:2048\n" \
"v_fma_f32 v243, v172, v173, v243\n" \
"v_mfma_f32_16x16x32_bf16 v[52:55], a[200:203], a[80:83], v[52:55]\n" \
"s_add_u32 s42, s42, 0x80\n" \
"s_addc_u32 s43, s43, 0\n" \
"ds_read_b128 a[72:75], v205 offset:0\n" \
"v_mfma_f32_16x16x32_bf16 v[52:55], a[204:207], a[84:87], v[52:55]\n" \
"ds_read_b128 a[76:79], v206 offset:0\n" \
"v_cvt_pk_bf16_f32 v170, v172, v173\n" \
"v_mfma_f32_16x16x32_bf16 v[88:91], a[80:83], a[208:211], v[88:91]\n" \
"ds_read_b64_tr_b16 a[108:109], v209 offset:12288\n" \
"v_fma_f32 v243, v174, v175, v243\n" \
"v_mfma_f32_16x16x32_bf16 v[88:91], a[84:87], a[212:215], v[88:91]\n" \
"global_load_lds_dwordx4 v252, s[44:45] offset:1024\n" \
"v_cvt_pk_bf16_f32 v171, v174, v175\n" \
"v_mfma_f32_16x16x32_bf16 v[92:95], a[80:83], a[216:219], v[92:95]\n" \
"buffer_store_dwordx4 v[168:171], v235, s[56:59], s78 offen offset:2048 sc1\n" \
"ds_read_b64_tr_b16 a[110:111], v209 offset:13312\n" \
"s_add_u32 s78, s78, 0x1000\n" \
"v_mul_f32 v176, v176, v234\n" \
"v_mul_f32 v177, v177, v234\n" \
"v_mfma_f32_16x16x32_bf16 v[92:95], a[84:87], a[220:223], v[92:95]\n" \
"ds_read_b64_tr_b16 a[112:113], v210 offset:0\n" \
"v_mul_f32 v178, v178, v234\n" \
"v_mfma_f32_16x16x32_bf16 v[60:63], a[200:203], a[88:91], v[60:63]\n" \
"ds_read_b128 a[80:83], v205 offset:4096\n" \
"v_mul_f32 v179, v179, v234\n" \
"v_mfma_f32_16x16x32_bf16 v[60:63], a[204:207], a[92:95], v[60:63]\n" \
"global_load_lds_dwordx4 v252, s[44:45] offset:3072\n" \
"v_mul_f32 v180, v180, v234\n" \
"v_mfma_f32_16x16x32_bf16 v[96:99], a[88:91], a[208:211], v[96:99]\n" \
"s_add_u32 s44, s44, 0x80\n" \
"s_addc_u32 s45, s45, 0\n" \
"ds_read_b128 a[84:87], v206 offset:4096\n" \
"v_mfma_f32_16x16x32_bf16 v[96:99], a[92:95], a[212:215], v[96:99]\n" \
"ds_read_b64_tr_b16 a[114:115], v210 offset:1024\n" \
"v_mul_f32 v181, v181, v234\n" \
"v_mfma_f32_16x16x32_bf16 v[100:103], a[88:91], a[216:219], v[100:103]\n" \
"s_add_u32 m0, s85, 16384\n" \
"ds_read_b64_tr_b16 a[116:117], v210 offset:12288\n" \
"v_mul_f32 v182, v182, v234\n" \
"v_mfma_f32_16x16x32_bf16 v[100:103], a[92:95], a[220:223], v[100:103]\n" \
"buffer_load_dwordx4 v246, s[12:15], s46 offen lds\n" \
"v_mfma_f32_16x16x32_bf16 v[68:71], a[200:203], a[192:195], v[68:71]\n" \
"s_add_u32 s46, s46, 0x80\n" \
"ds_read_b128 a[88:91], v205 offset:8192\n" \
"v_mul_f32 v183, v183, v234\n" \
"v_mfma_f32_16x16x32_bf16 v[68:71], a[204:207], a[196:199], v[68:71]\n" \
"ds_read_b128 a[92:95], v206 offset:8192\n" \
"v_fma_f32 v243, v176, v177, v243\n" \
"v_mfma_f32_16x16x32_bf16 v[104:107], a[192:195], a[208:211], v[104:107]\n" \
"s_add_u32 m0, s84, 24576\n" \
"ds_read_b64_tr_b16 a[118:119], v210 offset:13312\n" \
"v_cvt_pk_bf16_f32 v176, v176, v177\n" \
"v_mfma_f32_16x16x32_bf16 v[104:107], a[196:199], a[212:215], v[104:107]\n" \
"global_load_lds_dwordx4 v253, s[48:49] offset:-4096\n" \
"v_fma_f32 v243, v178, v179, v243\n" \
"v_mfma_f32_16x16x32_bf16 v[108:111], a[192:195], a[216:219], v[108:111]\n" \
"v_cvt_pk_bf16_f32 v177, v178, v179\n" \
"v_fma_f32 v243, v180, v181, v243\n" \
"v_mfma_f32_16x16x32_bf16 v[108:111], a[196:199], a[220:223], v[108:111]\n" \
"s_waitcnt lgkmcnt(2)\n" \
"v_cvt_pk_bf16_f32 v178, v180, v181\n" \
"v_fma_f32 v243, v182, v183, v243\n" \
"v_mfma_f32_16x16x32_bf16 v[32:35], a[96:99], a[64:67], v[32:35]\n" \
"ds_read_b128 a[192:195], v205 offset:12288\n" \
"v_cvt_pk_bf16_f32 v179, v182, v183\n" \
"v_mfma_f32_16x16x32_bf16 v[32:35], a[100:103], a[68:71], v[32:35]\n" \
"buffer_store_dwordx4 v[176:179], v235, s[56:59], s78 offen offset:0 sc1\n" \
"global_load_lds_dwordx4 v253, s[48:49] offset:-2048\n" \
"v_mul_f32 v184, v184, v234\n" \
"v_mul_f32 v185, v185, v234\n" \
"v_mul_f32 v186, v186, v234\n" \
"v_mfma_f32_16x16x32_bf16 v[40:43], a[96:99], a[72:75], v[40:43]\n" \
"ds_read_b128 a[196:199], v206 offset:12288\n" \
"v_mul_f32 v187, v187, v234\n" \
"v_mfma_f32_16x16x32_bf16 v[40:43], a[100:103], a[76:79], v[40:43]\n" \
"ds_read_b64_tr_b16 a[200:201], v210 offset:2048\n" \
"v_mul_f32 v188, v188, v234\n" \
"v_mfma_f32_16x16x32_bf16 v[0:3], a[104:107], a[72:75], v[0:3]\n" \
"ds_read_b64_tr_b16 a[202:203], v210 offset:3072\n" \
"v_mul_f32 v189, v189, v234\n" \
"v_mfma_f32_16x16x32_bf16 v[0:3], a[108:111], a[76:79], v[0:3]\n" \
"s_waitcnt lgkmcnt(3)\n" \
"global_load_lds_dwordx4 v253, s[48:49] offset:0\n" \
"v_mul_f32 v190, v190, v234\n" \
"v_mfma_f32_16x16x32_bf16 v[4:7], a[112:115], a[72:75], v[4:7]\n" \
"s_add_u32 s48, s48, 0x60000\n" \
"s_addc_u32 s49, s49, 0\n" \
"ds_read_b64_tr_b16 a[204:205], v210 offset:14336\n" \
"v_mfma_f32_16x16x32_bf16 v[4:7], a[116:119], a[76:79], v[4:7]\n" \
"ds_read_b64_tr_b16 a[206:207], v210 offset:15360\n" \
"v_mul_f32 v191, v191, v234\n" \
"v_mfma_f32_16x16x32_bf16 v[48:51], a[96:99], a[80:83], v[48:51]\n" \
"ds_read_b64_tr_b16 a[208:209], v209 offset:4096\n" \
"v_fma_f32 v243, v184, v185, v243\n" \
"v_mfma_f32_16x16x32_bf16 v[48:51], a[100:103], a[84:87], v[48:51]\n" \
"global_load_lds_dwordx4 v253, s[50:51] offset:-3072\n" \
"v_cvt_pk_bf16_f32 v184, v184, v185\n" \
"v_mfma_f32_16x16x32_bf16 v[8:11], a[104:107], a[80:83], v[8:11]\n" \
"ds_read_b64_tr_b16 a[210:211], v209 offset:5120\n" \
"v_fma_f32 v243, v186, v187, v243\n" \
"v_mfma_f32_16x16x32_bf16 v[8:11], a[108:111], a[84:87], v[8:11]\n" \
"ds_read_b64_tr_b16 a[212:213], v209 offset:16384\n" \
"v_cvt_pk_bf16_f32 v185, v186, v187\n" \
"v_mfma_f32_16x16x32_bf16 v[12:15], a[112:115], a[80:83], v[12:15]\n" \
"ds_read_b64_tr_b16 a[214:215], v209 offset:17408\n" \
"v_fma_f32 v243, v188, v189, v243\n" \
"v_mfma_f32_16x16x32_bf16 v[12:15], a[116:119], a[84:87], v[12:15]\n" \
"global_load_lds_dwordx4 v253, s[50:51] offset:-1024\n" \
"v_cvt_pk_bf16_f32 v186, v188, v189\n" \
"v_mfma_f32_16x16x32_bf16 v[56:59], a[96:99], a[88:91], v[56:59]\n" \
"ds_read_b64_tr_b16 a[216:217], v210 offset:4096\n" \
"v_fma_f32 v243, v190, v191, v243\n" \
"v_mfma_f32_16x16x32_bf16 v[56:59], a[100:103], a[92:95], v[56:59]\n" \
"ds_read_b64_tr_b16 a[218:219], v210 offset:5120\n" \
"v_cvt_pk_bf16_f32 v187, v190, v191\n" \
"v_mfma_f32_16x16x32_bf16 v[16:19], a[104:107], a[88:91], v[16:19]\n" \
"buffer_store_dwordx4 v[184:187], v235, s[56:59], s78 offen offset:2048 sc1\n" \
"ds_read_b64_tr_b16 a[220:221], v210 offset:16384\n" \
"s_add_u32 s78, s78, 0xfff7d000\n" \
"v_add_f32 v240, v240, v241\n" \
"v_add_f32 v242, v242, v243\n" \
"v_mfma_f32_16x16x32_bf16 v[16:19], a[108:111], a[92:95], v[16:19]\n" \
"global_load_lds_dwordx4 v253, s[50:51] offset:1024\n" \
"v_mov_b32 v241, 0x207\n" \
"v_mfma_f32_16x16x32_bf16 v[20:23], a[112:115], a[88:91], v[20:23]\n" \
"s_add_u32 s50, s50, 0x60000\n" \
"s_addc_u32 s51, s51, 0\n" \
"ds_read_b64_tr_b16 a[222:223], v210 offset:17408\n" \
"v_mfma_f32_16x16x32_bf16 v[20:23], a[116:119], a[92:95], v[20:23]\n" \
"s_waitcnt lgkmcnt(3)\n" \
"v_add_u32 v207, 0xffff3000, v207\n" \
"v_add_u32 v208, 0xffff3000, v208\n" \
"v_mfma_f32_16x16x32_bf16 v[64:67], a[96:99], a[192:195], v[64:67]\n" \
"v_add_u32 v205, 0xffff3000, v205\n" \
"v_add_u32 v206, 0xffff3000, v206\n" \
"v_mfma_f32_16x16x32_bf16 v[64:67], a[100:103], a[196:199], v[64:67]\n" \
"v_add_u32 v209, 0xffff3000, v209\n" \
"v_add_u32 v210, 0xffff3000, v210\n" \
"v_mfma_f32_16x16x32_bf16 v[24:27], a[104:107], a[192:195], v[24:27]\n" \
"v_add_f32 v240, v240, v242\n" \
"v_cmp_class_f32 vcc, v240, v241\n" \
"v_mfma_f32_16x16x32_bf16 v[24:27], a[108:111], a[196:199], v[24:27]\n" \
"v_mfma_f32_16x16x32_bf16 v[28:31], a[112:115], a[192:195], v[28:31]\n" \
"s_waitcnt vmcnt(15) lgkmcnt(0)\n" \
"s_cbranch_vccnz Lexact_%=\n" \
"Lexr_%=:\n" \
"v_mfma_f32_16x16x32_bf16 v[28:31], a[116:119], a[196:199], v[28:31]\n" \
"s_barrier\n" \
"v_mfma_f32_16x16x32_bf16 v[72:75], a[64:67], a[208:211], v[72:75]\n" \
"ds_read_b64_tr_b16 a[96:97], v209 offset:2048\n" \
"v_mfma_f32_16x16x32_bf16 v[72:75], a[68:71], a[212:215], v[72:75]\n" \
"ds_read_b64_tr_b16 a[98:99], v209 offset:3072\n" \
"v_mfma_f32_16x16x32_bf16 v[44:47], a[200:203], a[72:75], v[44:47]\n" \
"s_add_u32 m0, s95, 118784\n" \
"ds_read_b128 a[64:67], v207 offset:0\n" \
"ds_read_b64_tr_b16 a[100:101], v209 offset:14336\n" \
"v_mfma_f32_16x16x32_bf16 v[44:47], a[204:207], a[76:79], v[44:47]\n" \
"global_load_lds_dwordx4 v252, s[42:43] offset:0\n" \
"v_mfma_f32_16x16x32_bf16 v[80:83], a[72:75], a[208:211], v[80:83]\n" \
"ds_read_b128 a[68:71], v208 offset:0\n" \
"ds_read_b64_tr_b16 a[102:103], v209 offset:15360\n" \
"v_mfma_f32_16x16x32_bf16 v[80:83], a[76:79], a[212:215], v[80:83]\n" \
"ds_read_b64_tr_b16 a[104:105], v209 offset:0\n" \
"v_mfma_f32_16x16x32_bf16 v[84:87], a[72:75], a[216:219], v[84:87]\n" \
"ds_read_b64_tr_b16 a[106:107], v209 offset:1024\n" \
"v_mfma_f32_16x16x32_bf16 v[84:87], a[76:79], a[220:223], v[84:87]\n" \
"global_load_lds_dwordx4 v252, s[42:43] offset:2048\n" \
"v_mfma_f32_16x16x32_bf16 v[52:55], a[200:203], a[80:83], v[52:55]\n" \
"s_add_u32 s42, s42, 0x80\n" \
"s_addc_u32 s43, s43, 0\n" \
"ds_read_b128 a[72:75], v205 offset:0\n" \
"v_mfma_f32_16x16x32_bf16 v[52:55], a[204:207], a[84:87], v[52:55]\n" \
"ds_read_b128 a[76:79], v206 offset:0\n" \
"v_mfma_f32_16x16x32_bf16 v[88:91], a[80:83], a[208:211], v[88:91]\n" \
"ds_read_b64_tr_b16 a[108:109], v209 offset:12288\n" \
"v_mfma_f32_16x16x32_bf16 v[88:91], a[84:87], a[212:215], v[88:91]\n" \
"global_load_lds_dwordx4 v252, s[44:45] offset:1024\n" \
"v_mfma_f32_16x16x32_bf16 v[92:95], a[80:83], a[216:219], v[92:95]\n" \
"ds_read_b64_tr_b16 a[110:111], v209 offset:13312\n" \
"v_mfma_f32_16x16x32_bf16 v[92:95], a[84:87], a[220:223], v[92:95]\n" \
"ds_read_b64_tr_b16 a[112:113], v210 offset:0\n" \
"v_mfma_f32_16x16x32_bf16 v[60:63], a[200:203], a[88:91], v[60:63]\n" \
"ds_read_b128 a[80:83], v205 offset:4096\n" \
"v_mfma_f32_16x16x32_bf16 v[60:63], a[204:207], a[92:95], v[60:63]\n" \
"global_load_lds_dwordx4 v252, s[44:45] offset:3072\n" \
"v_mfma_f32_16x16x32_bf16 v[96:99], a[88:91], a[208:211], v[96:99]\n" \
"s_add_u32 s44, s44, 0x80\n" \
"s_addc_u32 s45, s45, 0\n" \
"ds_read_b128 a[84:87], v206 offset:4096\n" \
"v_mfma_f32_16x16x32_bf16 v[96:99], a[92:95], a[212:215], v[96:99]\n" \
"ds_read_b64_tr_b16 a[114:115], v210 offset:1024\n" \
"v_mfma_f32_16x16x32_bf16 v[100:103], a[88:91], a[216:219], v[100:103]\n" \
"s_add_u32 m0, s85, 135168\n" \
"ds_read_b64_tr_b16 a[116:117], v210 offset:12288\n" \
"v_mfma_f32_16x16x32_bf16 v[100:103], a[92:95], a[220:223], v[100:103]\n" \
"buffer_load_dwordx4 v246, s[12:15], s46 offen lds\n" \
"v_mfma_f32_16x16x32_bf16 v[68:71], a[200:203], a[192:195], v[68:71]\n" \
"ds_read_b128 a[88:91], v205 offset:8192\n" \
"s_add_u32 s46, s46, 0x80\n" \
"v_mfma_f32_16x16x32_bf16 v[68:71], a[204:207], a[196:199], v[68:71]\n" \
"ds_read_b128 a[92:95], v206 offset:8192\n" \
"v_mfma_f32_16x16x32_bf16 v[104:107], a[192:195], a[208:211], v[104:107]\n" \
"s_add_u32 m0, s84, 143360\n" \
"ds_read_b64_tr_b16 a[118:119], v210 offset:13312\n" \
"v_mfma_f32_16x16x32_bf16 v[104:107], a[196:199], a[212:215], v[104:107]\n" \
"global_load_lds_dwordx4 v253, s[48:49] offset:-4096\n" \
"v_mfma_f32_16x16x32_bf16 v[108:111], a[192:195], a[216:219], v[108:111]\n" \
"v_mfma_f32_16x16x32_bf16 v[108:111], a[196:199], a[220:223], v[108:111]\n" \
"s_waitcnt lgkmcnt(2)\n" \
"v_mfma_f32_16x16x32_bf16 v[32:35], a[96:99], a[64:67], v[32:35]\n" \
"ds_read_b128 a[192:195], v205 offset:12288\n" \
"v_mfma_f32_16x16x32_bf16 v[32:35], a[100:103], a[68:71], v[32:35]\n" \
"global_load_lds_dwordx4 v253, s[48:49] offset:-2048\n" \
"v_mfma_f32_16x16x32_bf16 v[40:43], a[96:99], a[72:75], v[40:43]\n" \
"ds_read_b128 a[196:199], v206 offset:12288\n" \
"v_mfma_f32_16x16x32_bf16 v[40:43], a[100:103], a[76:79], v[40:43]\n" \
"ds_read_b64_tr_b16 a[200:201], v210 offset:2048\n" \
"v_mfma_f32_16x16x32_bf16 v[0:3], a[104:107], a[72:75], v[0:3]\n" \
"ds_read_b64_tr_b16 a[202:203], v210 offset:3072\n" \
"v_mfma_f32_16x16x32_bf16 v[0:3], a[108:111], a[76:79], v[0:3]\n" \
"s_waitcnt lgkmcnt(3)\n" \
"global_load_lds_dwordx4 v253, s[48:49] offset:0\n" \
"v_mfma_f32_16x16x32_bf16 v[4:7], a[112:115], a[72:75], v[4:7]\n" \
"s_add_u32 s48, s48, 0x60000\n" \
"s_addc_u32 s49, s49, 0\n" \
"ds_read_b64_tr_b16 a[204:205], v210 offset:14336\n" \
"v_mfma_f32_16x16x32_bf16 v[4:7], a[116:119], a[76:79], v[4:7]\n" \
"ds_read_b64_tr_b16 a[206:207], v210 offset:15360\n" \
"v_mfma_f32_16x16x32_bf16 v[48:51], a[96:99], a[80:83], v[48:51]\n" \
"ds_read_b64_tr_b16 a[208:209], v209 offset:4096\n" \
"v_mfma_f32_16x16x32_bf16 v[48:51], a[100:103], a[84:87], v[48:51]\n" \
"global_load_lds_dwordx4 v253, s[50:51] offset:-3072\n" \
"v_mfma_f32_16x16x32_bf16 v[8:11], a[104:107], a[80:83], v[8:11]\n" \
"ds_read_b64_tr_b16 a[210:211], v209 offset:5120\n" \
"v_mfma_f32_16x16x32_bf16 v[8:11], a[108:111], a[84:87], v[8:11]\n" \
"ds_read_b64_tr_b16 a[212:213], v209 offset:16384\n" \
"v_mfma_f32_16x16x32_bf16 v[12:15], a[112:115], a[80:83], v[12:15]\n" \
"ds_read_b64_tr_b16 a[214:215], v209 offset:17408\n" \
"v_mfma_f32_16x16x32_bf16 v[12:15], a[116:119], a[84:87], v[12:15]\n" \
"global_load_lds_dwordx4 v253, s[50:51] offset:-1024\n" \
"v_mfma_f32_16x16x32_bf16 v[56:59], a[96:99], a[88:91], v[56:59]\n" \
"ds_read_b64_tr_b16 a[216:217], v210 offset:4096\n" \
"v_mfma_f32_16x16x32_bf16 v[56:59], a[100:103], a[92:95], v[56:59]\n" \
"ds_read_b64_tr_b16 a[218:219], v210 offset:5120\n" \
"v_mfma_f32_16x16x32_bf16 v[16:19], a[104:107], a[88:91], v[16:19]\n" \
"ds_read_b64_tr_b16 a[220:221], v210 offset:16384\n" \
"v_mfma_f32_16x16x32_bf16 v[16:19], a[108:111], a[92:95], v[16:19]\n" \
"global_load_lds_dwordx4 v253, s[50:51] offset:1024\n" \
"v_mfma_f32_16x16x32_bf16 v[20:23], a[112:115], a[88:91], v[20:23]\n" \
"s_add_u32 s50, s50, 0x60000\n" \
"s_addc_u32 s51, s51, 0\n" \
"ds_read_b64_tr_b16 a[222:223], v210 offset:17408\n" \
"v_mfma_f32_16x16x32_bf16 v[20:23], a[116:119], a[92:95], v[20:23]\n" \
"s_waitcnt lgkmcnt(3)\n" \
"v_add_u32 v207, 0xffff0000, v207\n" \
"v_add_u32 v208, 0xffff0000, v208\n" \
"v_mfma_f32_16x16x32_bf16 v[64:67], a[96:99], a[192:195], v[64:67]\n" \
"v_add_u32 v205, 0xffff0000, v205\n" \
"v_add_u32 v206, 0xffff0000, v206\n" \
"v_mfma_f32_16x16x32_bf16 v[64:67], a[100:103], a[196:199], v[64:67]\n" \
"v_add_u32 v209, 0xffff0000, v209\n" \
"v_add_u32 v210, 0xffff0000, v210\n" \
"v_mfma_f32_16x16x32_bf16 v[24:27], a[104:107], a[192:195], v[24:27]\n" \
"v_mfma_f32_16x16x32_bf16 v[24:27], a[108:111], a[196:199], v[24:27]\n" \
"s_lshl_b32 s70, s63, 7\n" \
"s_lshl_b32 s71, s62, 2\n" \
"s_add_u32 s71, s71, 65589248\n" \
"v_mfma_f32_16x16x32_bf16 v[28:31], a[112:115], a[192:195], v[28:31]\n" \
"s_waitcnt vmcnt(11) lgkmcnt(0)\n" \
"s_add_u32 s72, s71, s70\n" \
"s_sub_u32 s71, s71, s70\n" \
"v_mfma_f32_16x16x32_bf16 v[28:31], a[116:119], a[196:199], v[28:31]\n" \
"s_barrier\n" \
"v_mfma_f32_16x16x32_bf16 v[72:75], a[64:67], a[208:211], v[72:75]\n" \
"s_add_u32 s71, s71, 1920\n" \
"ds_read_b64_tr_b16 a[96:97], v209 offset:2048\n" \
"v_mov_b32 v236, s60\n" \
"v_mfma_f32_16x16x32_bf16 v[72:75], a[68:71], a[212:215], v[72:75]\n" \
"ds_read_b64_tr_b16 a[98:99], v209 offset:3072\n" \
"v_lshlrev_b32 v237, 28, s61\n" \
"v_mfma_f32_16x16x32_bf16 v[44:47], a[200:203], a[72:75], v[44:47]\n" \
"s_add_u32 m0, s95, 65536\n" \
"ds_read_b128 a[64:67], v207 offset:0\n" \
"ds_read_b64_tr_b16 a[100:101], v209 offset:14336\n" \
"v_mfma_f32_16x16x32_bf16 v[44:47], a[204:207], a[76:79], v[44:47]\n" \
"buffer_store_dword v236, v237, s[56:59], s72 offen sc1\n" \
"global_load_lds_dwordx4 v252, s[42:43] offset:0\n" \
"v_mfma_f32_16x16x32_bf16 v[80:83], a[72:75], a[208:211], v[80:83]\n" \
"ds_read_b128 a[68:71], v208 offset:0\n" \
"ds_read_b64_tr_b16 a[102:103], v209 offset:15360\n" \
"v_mfma_f32_16x16x32_bf16 v[80:83], a[76:79], a[212:215], v[80:83]\n" \
"buffer_store_dword v236, v237, s[56:59], s71 offen sc1\n" \
"ds_read_b64_tr_b16 a[104:105], v209 offset:0\n" \
"v_mfma_f32_16x16x32_bf16 v[84:87], a[72:75], a[216:219], v[84:87]\n" \
"ds_read_b64_tr_b16 a[106:107], v209 offset:1024\n" \
"v_mfma_f32_16x16x32_bf16 v[84:87], a[76:79], a[220:223], v[84:87]\n" \
"global_load_lds_dwordx4 v252, s[42:43] offset:2048\n" \
"v_mfma_f32_16x16x32_bf16 v[52:55], a[200:203], a[80:83], v[52:55]\n" \
"s_add_u32 s42, s42, 0x80\n" \
"s_addc_u32 s43, s43, 0\n" \
"ds_read_b128 a[72:75], v205 offset:0\n" \
"v_mfma_f32_16x16x32_bf16 v[52:55], a[204:207], a[84:87], v[52:55]\n" \
"ds_read_b128 a[76:79], v206 offset:0\n" \
"v_mfma_f32_16x16x32_bf16 v[88:91], a[80:83], a[208:211], v[88:91]\n" \
"ds_read_b64_tr_b16 a[108:109], v209 offset:12288\n" \
"v_mfma_f32_16x16x32_bf16 v[88:91], a[84:87], a[212:215], v[88:91]\n" \
"global_load_lds_dwordx4 v252, s[44:45] offset:1024\n" \
"v_mfma_f32_16x16x32_bf16 v[92:95], a[80:83], a[216:219], v[92:95]\n" \
"ds_read_b64_tr_b16 a[110:111], v209 offset:13312\n" \
"v_mfma_f32_16x16x32_bf16 v[92:95], a[84:87], a[220:223], v[92:95]\n" \
"ds_read_b64_tr_b16 a[112:113], v210 offset:0\n" \
"v_mfma_f32_16x16x32_bf16 v[60:63], a[200:203], a[88:91], v[60:63]\n" \
"ds_read_b128 a[80:83], v205 offset:4096\n" \
"v_mfma_f32_16x16x32_bf16 v[60:63], a[204:207], a[92:95], v[60:63]\n" \
"global_load_lds_dwordx4 v252, s[44:45] offset:3072\n" \
"v_mfma_f32_16x16x32_bf16 v[96:99], a[88:91], a[208:211], v[96:99]\n" \
"s_add_u32 s44, s44, 0x80\n" \
"s_addc_u32 s45, s45, 0\n" \
"ds_read_b128 a[84:87], v206 offset:4096\n" \
"v_mfma_f32_16x16x32_bf16 v[96:99], a[92:95], a[212:215], v[96:99]\n" \
"ds_read_b64_tr_b16 a[114:115], v210 offset:1024\n" \
"v_mfma_f32_16x16x32_bf16 v[100:103], a[88:91], a[216:219], v[100:103]\n" \
"s_add_u32 m0, s85, 81920\n" \
"ds_read_b64_tr_b16 a[116:117], v210 offset:12288\n" \
"v_mfma_f32_16x16x32_bf16 v[100:103], a[92:95], a[220:223], v[100:103]\n" \
"buffer_load_dwordx4 v246, s[12:15], s46 offen lds\n" \
"v_mfma_f32_16x16x32_bf16 v[68:71], a[200:203], a[192:195], v[68:71]\n" \
"ds_read_b128 a[88:91], v205 offset:8192\n" \
"s_add_u32 s46, s46, 0x80\n" \
"v_mfma_f32_16x16x32_bf16 v[68:71], a[204:207], a[196:199], v[68:71]\n" \
"ds_read_b128 a[92:95], v206 offset:8192\n" \
"v_mfma_f32_16x16x32_bf16 v[104:107], a[192:195], a[208:211], v[104:107]\n" \
"s_add_u32 m0, s84, 90112\n" \
"ds_read_b64_tr_b16 a[118:119], v210 offset:13312\n" \
"v_mfma_f32_16x16x32_bf16 v[104:107], a[196:199], a[212:215], v[104:107]\n" \
"global_load_lds_dwordx4 v253, s[48:49] offset:-4096\n" \
"v_mfma_f32_16x16x32_bf16 v[108:111], a[192:195], a[216:219], v[108:111]\n" \
"v_mfma_f32_16x16x32_bf16 v[108:111], a[196:199], a[220:223], v[108:111]\n" \
"s_waitcnt lgkmcnt(2)\n" \
"v_mfma_f32_16x16x32_bf16 v[32:35], a[96:99], a[64:67], v[32:35]\n" \
"s_lshr_b32 s89, s22, 7\n" \
"ds_read_b128 a[192:195], v205 offset:12288\n" \
"s_sub_u32 s89, 3, s89\n" \
"v_mfma_f32_16x16x32_bf16 v[32:35], a[100:103], a[68:71], v[32:35]\n" \
"global_load_lds_dwordx4 v253, s[48:49] offset:-2048\n" \
"v_bfe_u32 v203, v248, 7, 2\n" \
"v_mfma_f32_16x16x32_bf16 v[40:43], a[96:99], a[72:75], v[40:43]\n" \
"s_and_b32 s89, s89, 7\n" \
"ds_read_b128 a[196:199], v206 offset:12288\n" \
"s_and_b32 s90, %1, 1\n" \
"v_mfma_f32_16x16x32_bf16 v[40:43], a[100:103], a[76:79], v[40:43]\n" \
"ds_read_b64_tr_b16 a[200:201], v210 offset:2048\n" \
"v_bfe_u32 v204, v248, 5, 2\n" \
"v_mfma_f32_16x16x32_bf16 v[0:3], a[104:107], a[72:75], v[0:3]\n" \
"s_lshl_b32 s90, s90, 3\n" \
"s_add_u32 s89, s89, s90\n" \
"ds_read_b64_tr_b16 a[202:203], v210 offset:3072\n" \
"v_mfma_f32_16x16x32_bf16 v[0:3], a[108:111], a[76:79], v[0:3]\n" \
"s_waitcnt lgkmcnt(3)\n" \
"global_load_lds_dwordx4 v253, s[48:49] offset:0\n" \
"v_min_u32 v204, 2, v204\n" \
"v_mfma_f32_16x16x32_bf16 v[4:7], a[112:115], a[72:75], v[4:7]\n" \
"s_add_u32 s48, s48, 0x60000\n" \
"s_addc_u32 s49, s49, 0\n" \
"ds_read_b64_tr_b16 a[204:205], v210 offset:14336\n" \
"v_mfma_f32_16x16x32_bf16 v[4:7], a[116:119], a[76:79], v[4:7]\n" \
"s_lshl_b32 s89, s89, 7\n" \
"s_lshr_b32 s90, %1, 3\n" \
"ds_read_b64_tr_b16 a[206:207], v210 offset:15360\n" \
"v_mfma_f32_16x16x32_bf16 v[48:51], a[96:99], a[80:83], v[48:51]\n" \
"ds_read_b64_tr_b16 a[208:209], v209 offset:4096\n" \
"v_mad_u32_u24 v203, v203, 3, v204\n" \
"v_mfma_f32_16x16x32_bf16 v[48:51], a[100:103], a[84:87], v[48:51]\n" \
"s_lshl_b32 s90, s90, 2\n" \
"global_load_lds_dwordx4 v253, s[50:51] offset:-3072\n" \
"v_mfma_f32_16x16x32_bf16 v[8:11], a[104:107], a[80:83], v[8:11]\n" \
"s_add_u32 s90, s90, s61\n" \
"ds_read_b64_tr_b16 a[210:211], v209 offset:5120\n" \
"v_and_b32 v204, 16, v248\n" \
"v_mfma_f32_16x16x32_bf16 v[8:11], a[108:111], a[84:87], v[8:11]\n" \
"s_add_u32 s90, s90, 128\n" \
"s_mul_i32 s90, s90, 24576\n" \
"ds_read_b64_tr_b16 a[212:213], v209 offset:16384\n" \
"v_mfma_f32_16x16x32_bf16 v[12:15], a[112:115], a[80:83], v[12:15]\n" \
"s_add_u32 s89, s89, s90\n" \
"ds_read_b64_tr_b16 a[214:215], v209 offset:17408\n" \
"v_lshl_add_u32 v204, v204, 2, s89\n" \
"v_mfma_f32_16x16x32_bf16 v[12:15], a[116:119], a[84:87], v[12:15]\n" \
"global_load_lds_dwordx4 v253, s[50:51] offset:-1024\n" \
"v_lshl_add_u32 v203, v203, 11, v204\n" \
"v_mfma_f32_16x16x32_bf16 v[56:59], a[96:99], a[88:91], v[56:59]\n" \
"ds_read_b64_tr_b16 a[216:217], v210 offset:4096\n" \
"v_mfma_f32_16x16x32_bf16 v[56:59], a[100:103], a[92:95], v[56:59]\n" \
"ds_read_b64_tr_b16 a[218:219], v210 offset:5120\n" \
"v_mfma_f32_16x16x32_bf16 v[16:19], a[104:107], a[88:91], v[16:19]\n" \
"ds_read_b64_tr_b16 a[220:221], v210 offset:16384\n" \
"v_mfma_f32_16x16x32_bf16 v[16:19], a[108:111], a[92:95], v[16:19]\n" \
"global_load_lds_dwordx4 v253, s[50:51] offset:1024\n" \
"v_mfma_f32_16x16x32_bf16 v[20:23], a[112:115], a[88:91], v[20:23]\n" \
"s_add_u32 s50, s50, 0x60000\n" \
"s_addc_u32 s51, s51, 0\n" \
"ds_read_b64_tr_b16 a[222:223], v210 offset:17408\n" \
"v_mfma_f32_16x16x32_bf16 v[20:23], a[116:119], a[92:95], v[20:23]\n" \
"s_waitcnt lgkmcnt(3)\n" \
"v_add_u32 v207, 0x1d000, v207\n" \
"v_add_u32 v208, 0x1d000, v208\n" \
"v_mfma_f32_16x16x32_bf16 v[64:67], a[96:99], a[192:195], v[64:67]\n" \
"v_add_u32 v205, 0x1d000, v205\n" \
"v_add_u32 v206, 0x1d000, v206\n" \
"v_mfma_f32_16x16x32_bf16 v[64:67], a[100:103], a[196:199], v[64:67]\n" \
"v_add_u32 v209, 0x1d000, v209\n" \
"v_add_u32 v210, 0x1d000, v210\n" \
"v_mfma_f32_16x16x32_bf16 v[24:27], a[104:107], a[192:195], v[24:27]\n" \
"v_mfma_f32_16x16x32_bf16 v[24:27], a[108:111], a[196:199], v[24:27]\n" \
"v_mfma_f32_16x16x32_bf16 v[28:31], a[112:115], a[192:195], v[28:31]\n" \
"s_waitcnt vmcnt(13) lgkmcnt(0)\n" \
"v_mfma_f32_16x16x32_bf16 v[28:31], a[116:119], a[196:199], v[28:31]\n" \
"s_barrier\n" \
"v_mfma_f32_16x16x32_bf16 v[72:75], a[64:67], a[208:211], v[72:75]\n" \
"ds_read_b64_tr_b16 a[96:97], v209 offset:2048\n" \
"v_mfma_f32_16x16x32_bf16 v[72:75], a[68:71], a[212:215], v[72:75]\n" \
"ds_read_b64_tr_b16 a[98:99], v209 offset:3072\n" \
"v_mfma_f32_16x16x32_bf16 v[44:47], a[200:203], a[72:75], v[44:47]\n" \
"s_add_u32 m0, s95, 0\n" \
"ds_read_b128 a[64:67], v207 offset:0\n" \
"ds_read_b64_tr_b16 a[100:101], v209 offset:14336\n" \
"v_mfma_f32_16x16x32_bf16 v[44:47], a[204:207], a[76:79], v[44:47]\n" \
"global_load_lds_dwordx4 v252, s[42:43] offset:0\n" \
"v_mfma_f32_16x16x32_bf16 v[80:83], a[72:75], a[208:211], v[80:83]\n" \
"ds_read_b128 a[68:71], v208 offset:0\n" \
"ds_read_b64_tr_b16 a[102:103], v209 offset:15360\n" \
"v_mfma_f32_16x16x32_bf16 v[80:83], a[76:79], a[212:215], v[80:83]\n" \
"ds_read_b64_tr_b16 a[104:105], v209 offset:0\n" \
"v_mfma_f32_16x16x32_bf16 v[84:87], a[72:75], a[216:219], v[84:87]\n" \
"ds_read_b64_tr_b16 a[106:107], v209 offset:1024\n" \
"v_mfma_f32_16x16x32_bf16 v[84:87], a[76:79], a[220:223], v[84:87]\n" \
"global_load_lds_dwordx4 v252, s[42:43] offset:2048\n" \
"v_mfma_f32_16x16x32_bf16 v[52:55], a[200:203], a[80:83], v[52:55]\n" \
"s_add_u32 s42, s42, 0x80\n" \
"s_addc_u32 s43, s43, 0\n" \
"ds_read_b128 a[72:75], v205 offset:0\n" \
"v_mfma_f32_16x16x32_bf16 v[52:55], a[204:207], a[84:87], v[52:55]\n" \
"ds_read_b128 a[76:79], v206 offset:0\n" \
"v_mfma_f32_16x16x32_bf16 v[88:91], a[80:83], a[208:211], v[88:91]\n" \
"ds_read_b64_tr_b16 a[108:109], v209 offset:12288\n" \
"v_mfma_f32_16x16x32_bf16 v[88:91], a[84:87], a[212:215], v[88:91]\n" \
"global_load_lds_dwordx4 v252, s[44:45] offset:1024\n" \
"v_mfma_f32_16x16x32_bf16 v[92:95], a[80:83], a[216:219], v[92:95]\n" \
"ds_read_b64_tr_b16 a[110:111], v209 offset:13312\n" \
"v_mfma_f32_16x16x32_bf16 v[92:95], a[84:87], a[220:223], v[92:95]\n" \
"ds_read_b64_tr_b16 a[112:113], v210 offset:0\n" \
"v_mfma_f32_16x16x32_bf16 v[60:63], a[200:203], a[88:91], v[60:63]\n" \
"ds_read_b128 a[80:83], v205 offset:4096\n" \
"v_mfma_f32_16x16x32_bf16 v[60:63], a[204:207], a[92:95], v[60:63]\n" \
"global_load_lds_dwordx4 v252, s[44:45] offset:3072\n" \
"v_mfma_f32_16x16x32_bf16 v[96:99], a[88:91], a[208:211], v[96:99]\n" \
"s_add_u32 s44, s44, 0x80\n" \
"s_addc_u32 s45, s45, 0\n" \
"ds_read_b128 a[84:87], v206 offset:4096\n" \
"v_mfma_f32_16x16x32_bf16 v[96:99], a[92:95], a[212:215], v[96:99]\n" \
"ds_read_b64_tr_b16 a[114:115], v210 offset:1024\n" \
"v_mfma_f32_16x16x32_bf16 v[100:103], a[88:91], a[216:219], v[100:103]\n" \
"s_add_u32 m0, s85, 16384\n" \
"ds_read_b64_tr_b16 a[116:117], v210 offset:12288\n" \
"v_mfma_f32_16x16x32_bf16 v[100:103], a[92:95], a[220:223], v[100:103]\n" \
"buffer_load_dwordx4 v246, s[12:15], s46 offen lds\n" \
"v_mfma_f32_16x16x32_bf16 v[68:71], a[200:203], a[192:195], v[68:71]\n" \
"ds_read_b128 a[88:91], v205 offset:8192\n" \
"s_add_u32 s46, s46, 0x80\n" \
"v_mfma_f32_16x16x32_bf16 v[68:71], a[204:207], a[196:199], v[68:71]\n" \
"ds_read_b128 a[92:95], v206 offset:8192\n" \
"v_mfma_f32_16x16x32_bf16 v[104:107], a[192:195], a[208:211], v[104:107]\n" \
"s_add_u32 m0, s84, 24576\n" \
"ds_read_b64_tr_b16 a[118:119], v210 offset:13312\n" \
"v_mfma_f32_16x16x32_bf16 v[104:107], a[196:199], a[212:215], v[104:107]\n" \
"global_load_lds_dwordx4 v253, s[48:49] offset:-4096\n" \
"v_mfma_f32_16x16x32_bf16 v[108:111], a[192:195], a[216:219], v[108:111]\n" \
"v_mfma_f32_16x16x32_bf16 v[108:111], a[196:199], a[220:223], v[108:111]\n" \
"s_waitcnt lgkmcnt(2)\n" \
"v_mfma_f32_16x16x32_bf16 v[32:35], a[96:99], a[64:67], v[32:35]\n" \
"s_mov_b32 s30, 65593856\n" \
"ds_read_b128 a[192:195], v205 offset:12288\n" \
"v_lshrrev_b32 v228, 1, v248\n" \
"v_mfma_f32_16x16x32_bf16 v[32:35], a[100:103], a[68:71], v[32:35]\n" \
"global_load_lds_dwordx4 v253, s[48:49] offset:-2048\n" \
"v_mfma_f32_16x16x32_bf16 v[40:43], a[96:99], a[72:75], v[40:43]\n" \
"ds_read_b128 a[196:199], v206 offset:12288\n" \
"v_mfma_f32_16x16x32_bf16 v[40:43], a[100:103], a[76:79], v[40:43]\n" \
"ds_read_b64_tr_b16 a[200:201], v210 offset:2048\n" \
"v_mfma_f32_16x16x32_bf16 v[0:3], a[104:107], a[72:75], v[0:3]\n" \
"ds_read_b64_tr_b16 a[202:203], v210 offset:3072\n" \
"v_mfma_f32_16x16x32_bf16 v[0:3], a[108:111], a[76:79], v[0:3]\n" \
"s_waitcnt lgkmcnt(3)\n" \
"global_load_lds_dwordx4 v253, s[48:49] offset:0\n" \
"v_mfma_f32_16x16x32_bf16 v[4:7], a[112:115], a[72:75], v[4:7]\n" \
"s_add_u32 s48, s48, 0x60000\n" \
"s_addc_u32 s49, s49, 0\n" \
"ds_read_b64_tr_b16 a[204:205], v210 offset:14336\n" \
"v_mfma_f32_16x16x32_bf16 v[4:7], a[116:119], a[76:79], v[4:7]\n" \
"ds_read_b64_tr_b16 a[206:207], v210 offset:15360\n" \
"v_mfma_f32_16x16x32_bf16 v[48:51], a[96:99], a[80:83], v[48:51]\n" \
"ds_read_b64_tr_b16 a[208:209], v209 offset:4096\n" \
"v_mfma_f32_16x16x32_bf16 v[48:51], a[100:103], a[84:87], v[48:51]\n" \
"global_load_lds_dwordx4 v253, s[50:51] offset:-3072\n" \
"v_mfma_f32_16x16x32_bf16 v[8:11], a[104:107], a[80:83], v[8:11]\n" \
"ds_read_b64_tr_b16 a[210:211], v209 offset:5120\n" \
"v_mfma_f32_16x16x32_bf16 v[8:11], a[108:111], a[84:87], v[8:11]\n" \
"ds_read_b64_tr_b16 a[212:213], v209 offset:16384\n" \
"v_mfma_f32_16x16x32_bf16 v[12:15], a[112:115], a[80:83], v[12:15]\n" \
"ds_read_b64_tr_b16 a[214:215], v209 offset:17408\n" \
"v_mfma_f32_16x16x32_bf16 v[12:15], a[116:119], a[84:87], v[12:15]\n" \
"global_load_lds_dwordx4 v253, s[50:51] offset:-1024\n" \
"v_mfma_f32_16x16x32_bf16 v[56:59], a[96:99], a[88:91], v[56:59]\n" \
"ds_read_b64_tr_b16 a[216:217], v210 offset:4096\n" \
"v_mfma_f32_16x16x32_bf16 v[56:59], a[100:103], a[92:95], v[56:59]\n" \
"ds_read_b64_tr_b16 a[218:219], v210 offset:5120\n" \
"v_mfma_f32_16x16x32_bf16 v[16:19], a[104:107], a[88:91], v[16:19]\n" \
"ds_read_b64_tr_b16 a[220:221], v210 offset:16384\n" \
"v_mfma_f32_16x16x32_bf16 v[16:19], a[108:111], a[92:95], v[16:19]\n" \
"global_load_lds_dwordx4 v253, s[50:51] offset:1024\n" \
"v_mfma_f32_16x16x32_bf16 v[20:23], a[112:115], a[88:91], v[20:23]\n" \
"s_add_u32 s50, s50, 0x60000\n" \
"s_addc_u32 s51, s51, 0\n" \
"ds_read_b64_tr_b16 a[222:223], v210 offset:17408\n" \
"v_mfma_f32_16x16x32_bf16 v[20:23], a[116:119], a[92:95], v[20:23]\n" \
"s_waitcnt lgkmcnt(3)\n" \
"v_add_u32 v207, 0xffff3000, v207\n" \
"v_add_u32 v208, 0xffff3000, v208\n" \
"v_mfma_f32_16x16x32_bf16 v[64:67], a[96:99], a[192:195], v[64:67]\n" \
"v_add_u32 v205, 0xffff3000, v205\n" \
"v_add_u32 v206, 0xffff3000, v206\n" \
"v_mfma_f32_16x16x32_bf16 v[64:67], a[100:103], a[196:199], v[64:67]\n" \
"global_load_dword v204, v203, s[22:23]\n" \
"v_mfma_f32_16x16x32_bf16 v[24:27], a[104:107], a[192:195], v[24:27]\n" \
"v_add_u32 v209, 0xffff3000, v209\n" \
"v_add_u32 v210, 0xffff3000, v210\n" \
"Lpg9870_%=:\n" \
"s_cbranch_execz Lpg12166_%=\n" \
"v_mfma_f32_16x16x32_bf16 v[24:27], a[108:111], a[196:199], v[24:27]\n" \
"v_mfma_f32_16x16x32_bf16 v[28:31], a[112:115], a[192:195], v[28:31]\n" \
"s_waitcnt vmcnt(12) lgkmcnt(0)\n" \
"v_mfma_f32_16x16x32_bf16 v[28:31], a[116:119], a[196:199], v[28:31]\n" \
"s_barrier\n" \
"v_mfma_f32_16x16x32_bf16 v[72:75], a[64:67], a[208:211], v[72:75]\n" \
"ds_read_b64_tr_b16 a[96:97], v209 offset:2048\n" \
"v_mfma_f32_16x16x32_bf16 v[72:75], a[68:71], a[212:215], v[72:75]\n" \
"ds_read_b64_tr_b16 a[98:99], v209 offset:3072\n" \
"v_mfma_f32_16x16x32_bf16 v[44:47], a[200:203], a[72:75], v[44:47]\n" \
"s_add_u32 m0, s95, 118784\n" \
"ds_read_b128 a[64:67], v207 offset:0\n" \
"ds_read_b64_tr_b16 a[100:101], v209 offset:14336\n" \
"v_mfma_f32_16x16x32_bf16 v[44:47], a[204:207], a[76:79], v[44:47]\n" \
"global_load_lds_dwordx4 v252, s[42:43] offset:0\n" \
"v_mfma_f32_16x16x32_bf16 v[80:83], a[72:75], a[208:211], v[80:83]\n" \
"ds_read_b128 a[68:71], v208 offset:0\n" \
"ds_read_b64_tr_b16 a[102:103], v209 offset:15360\n" \
"v_mfma_f32_16x16x32_bf16 v[80:83], a[76:79], a[212:215], v[80:83]\n" \
"ds_read_b64_tr_b16 a[104:105], v209 offset:0\n" \
"v_mfma_f32_16x16x32_bf16 v[84:87], a[72:75], a[216:219], v[84:87]\n" \
"ds_read_b64_tr_b16 a[106:107], v209 offset:1024\n" \
"v_mfma_f32_16x16x32_bf16 v[84:87], a[76:79], a[220:223], v[84:87]\n" \
"global_load_lds_dwordx4 v252, s[42:43] offset:2048\n" \
"v_mfma_f32_16x16x32_bf16 v[52:55], a[200:203], a[80:83], v[52:55]\n" \
"s_add_u32 s42, s42, 0x80\n" \
"s_addc_u32 s43, s43, 0\n" \
"ds_read_b128 a[72:75], v205 offset:0\n" \
"v_mfma_f32_16x16x32_bf16 v[52:55], a[204:207], a[84:87], v[52:55]\n" \
"ds_read_b128 a[76:79], v206 offset:0\n" \
"v_mfma_f32_16x16x32_bf16 v[88:91], a[80:83], a[208:211], v[88:91]\n" \
"ds_read_b64_tr_b16 a[108:109], v209 offset:12288\n" \
"v_mfma_f32_16x16x32_bf16 v[88:91], a[84:87], a[212:215], v[88:91]\n" \
"global_load_lds_dwordx4 v252, s[44:45] offset:1024\n" \
"v_mfma_f32_16x16x32_bf16 v[92:95], a[80:83], a[216:219], v[92:95]\n" \
"ds_read_b64_tr_b16 a[110:111], v209 offset:13312\n" \
"v_mfma_f32_16x16x32_bf16 v[92:95], a[84:87], a[220:223], v[92:95]\n" \
"ds_read_b64_tr_b16 a[112:113], v210 offset:0\n" \
"v_mfma_f32_16x16x32_bf16 v[60:63], a[200:203], a[88:91], v[60:63]\n" \
"ds_read_b128 a[80:83], v205 offset:4096\n" \
"v_mfma_f32_16x16x32_bf16 v[60:63], a[204:207], a[92:95], v[60:63]\n" \
"global_load_lds_dwordx4 v252, s[44:45] offset:3072\n" \
"v_mfma_f32_16x16x32_bf16 v[96:99], a[88:91], a[208:211], v[96:99]\n" \
"s_add_u32 s44, s44, 0x80\n" \
"s_addc_u32 s45, s45, 0\n" \
"ds_read_b128 a[84:87], v206 offset:4096\n" \
"v_mfma_f32_16x16x32_bf16 v[96:99], a[92:95], a[212:215], v[96:99]\n" \
"ds_read_b64_tr_b16 a[114:115], v210 offset:1024\n" \
"v_mfma_f32_16x16x32_bf16 v[100:103], a[88:91], a[216:219], v[100:103]\n" \
"s_add_u32 m0, s85, 135168\n" \
"ds_read_b64_tr_b16 a[116:117], v210 offset:12288\n" \
"v_mfma_f32_16x16x32_bf16 v[100:103], a[92:95], a[220:223], v[100:103]\n" \
"buffer_load_dwordx4 v246, s[12:15], s46 offen lds\n" \
"v_mfma_f32_16x16x32_bf16 v[68:71], a[200:203], a[192:195], v[68:71]\n" \
"ds_read_b128 a[88:91], v205 offset:8192\n" \
"s_add_u32 s46, s46, 0x80\n" \
"v_mfma_f32_16x16x32_bf16 v[68:71], a[204:207], a[196:199], v[68:71]\n" \
"ds_read_b128 a[92:95], v206 offset:8192\n" \
"v_mfma_f32_16x16x32_bf16 v[104:107], a[192:195], a[208:211], v[104:107]\n" \
"s_add_u32 m0, s84, 143360\n" \
"ds_read_b64_tr_b16 a[118:119], v210 offset:13312\n" \
"v_mfma_f32_16x16x32_bf16 v[104:107], a[196:199], a[212:215], v[104:107]\n" \
"global_load_lds_dwordx4 v253, s[48:49] offset:-4096\n" \
"v_mfma_f32_16x16x32_bf16 v[108:111], a[192:195], a[216:219], v[108:111]\n" \
"v_mfma_f32_16x16x32_bf16 v[108:111], a[196:199], a[220:223], v[108:111]\n" \
"s_waitcnt lgkmcnt(2)\n" \
"v_mfma_f32_16x16x32_bf16 v[32:35], a[96:99], a[64:67], v[32:35]\n" \
"ds_read_b128 a[192:195], v205 offset:12288\n" \
"v_mfma_f32_16x16x32_bf16 v[32:35], a[100:103], a[68:71], v[32:35]\n" \
"global_load_lds_dwordx4 v253, s[48:49] offset:-2048\n" \
"v_mfma_f32_16x16x32_bf16 v[40:43], a[96:99], a[72:75], v[40:43]\n" \
"ds_read_b128 a[196:199], v206 offset:12288\n" \
"v_mfma_f32_16x16x32_bf16 v[40:43], a[100:103], a[76:79], v[40:43]\n" \
"ds_read_b64_tr_b16 a[200:201], v210 offset:2048\n" \
"v_mfma_f32_16x16x32_bf16 v[0:3], a[104:107], a[72:75], v[0:3]\n" \
"ds_read_b64_tr_b16 a[202:203], v210 offset:3072\n" \
"v_mfma_f32_16x16x32_bf16 v[0:3], a[108:111], a[76:79], v[0:3]\n" \
"s_waitcnt lgkmcnt(3)\n" \
"global_load_lds_dwordx4 v253, s[48:49] offset:0\n" \
"v_mfma_f32_16x16x32_bf16 v[4:7], a[112:115], a[72:75], v[4:7]\n" \
"s_add_u32 s48, s48, 0x60000\n" \
"s_addc_u32 s49, s49, 0\n" \
"ds_read_b64_tr_b16 a[204:205], v210 offset:14336\n" \
"v_mfma_f32_16x16x32_bf16 v[4:7], a[116:119], a[76:79], v[4:7]\n" \
"ds_read_b64_tr_b16 a[206:207], v210 offset:15360\n" \
"v_mfma_f32_16x16x32_bf16 v[48:51], a[96:99], a[80:83], v[48:51]\n" \
"ds_read_b64_tr_b16 a[208:209], v209 offset:4096\n" \
"v_mfma_f32_16x16x32_bf16 v[48:51], a[100:103], a[84:87], v[48:51]\n" \
"global_load_lds_dwordx4 v253, s[50:51] offset:-3072\n" \
"v_mfma_f32_16x16x32_bf16 v[8:11], a[104:107], a[80:83], v[8:11]\n" \
"ds_read_b64_tr_b16 a[210:211], v209 offset:5120\n" \
"v_mfma_f32_16x16x32_bf16 v[8:11], a[108:111], a[84:87], v[8:11]\n" \
"ds_read_b64_tr_b16 a[212:213], v209 offset:16384\n" \
"v_mfma_f32_16x16x32_bf16 v[12:15], a[112:115], a[80:83], v[12:15]\n" \
"ds_read_b64_tr_b16 a[214:215], v209 offset:17408\n" \
"v_mfma_f32_16x16x32_bf16 v[12:15], a[116:119], a[84:87], v[12:15]\n" \
"global_load_lds_dwordx4 v253, s[50:51] offset:-1024\n" \
"v_mfma_f32_16x16x32_bf16 v[56:59], a[96:99], a[88:91], v[56:59]\n" \
"ds_read_b64_tr_b16 a[216:217], v210 offset:4096\n" \
"v_mfma_f32_16x16x32_bf16 v[56:59], a[100:103], a[92:95], v[56:59]\n" \
"ds_read_b64_tr_b16 a[218:219], v210 offset:5120\n" \
"v_mfma_f32_16x16x32_bf16 v[16:19], a[104:107], a[88:91], v[16:19]\n" \
"ds_read_b64_tr_b16 a[220:221], v210 offset:16384\n" \
"v_mfma_f32_16x16x32_bf16 v[16:19], a[108:111], a[92:95], v[16:19]\n" \
"global_load_lds_dwordx4 v253, s[50:51] offset:1024\n" \
"v_mfma_f32_16x16x32_bf16 v[20:23], a[112:115], a[88:91], v[20:23]\n" \
"s_add_u32 s50, s50, 0x60000\n" \
"s_addc_u32 s51, s51, 0\n" \
"ds_read_b64_tr_b16 a[222:223], v210 offset:17408\n" \
"v_mfma_f32_16x16x32_bf16 v[20:23], a[116:119], a[92:95], v[20:23]\n" \
"s_waitcnt lgkmcnt(3)\n" \
"v_add_u32 v207, 0xffff0000, v207\n" \
"v_add_u32 v208, 0xffff0000, v208\n" \
"v_mfma_f32_16x16x32_bf16 v[64:67], a[96:99], a[192:195], v[64:67]\n" \
"v_add_u32 v205, 0xffff0000, v205\n" \
"v_add_u32 v206, 0xffff0000, v206\n" \
"v_mfma_f32_16x16x32_bf16 v[64:67], a[100:103], a[196:199], v[64:67]\n" \
"buffer_load_dwordx2 v[226:227], v228, s[56:59], s30 offen sc1\n" \
"v_mfma_f32_16x16x32_bf16 v[24:27], a[104:107], a[192:195], v[24:27]\n" \
"v_add_u32 v209, 0xffff0000, v209\n" \
"v_add_u32 v210, 0xffff0000, v210\n" \
"v_mfma_f32_16x16x32_bf16 v[24:27], a[108:111], a[196:199], v[24:27]\n" \
"v_mfma_f32_16x16x32_bf16 v[28:31], a[112:115], a[192:195], v[28:31]\n" \
"s_waitcnt vmcnt(13) lgkmcnt(0)\n" \
"v_mfma_f32_16x16x32_bf16 v[28:31], a[116:119], a[196:199], v[28:31]\n" \
"s_barrier\n" \
"v_mfma_f32_16x16x32_bf16 v[72:75], a[64:67], a[208:211], v[72:75]\n" \
"ds_read_b64_tr_b16 a[96:97], v209 offset:2048\n" \
"v_mfma_f32_16x16x32_bf16 v[72:75], a[68:71], a[212:215], v[72:75]\n" \
"ds_read_b64_tr_b16 a[98:99], v209 offset:3072\n" \
"v_mfma_f32_16x16x32_bf16 v[44:47], a[200:203], a[72:75], v[44:47]\n" \
"s_add_u32 m0, s95, 65536\n" \
"ds_read_b128 a[64:67], v207 offset:0\n" \
"ds_read_b64_tr_b16 a[100:101], v209 offset:14336\n" \
"v_mfma_f32_16x16x32_bf16 v[44:47], a[204:207], a[76:79], v[44:47]\n" \
"global_load_lds_dwordx4 v252, s[42:43] offset:0\n" \
"v_mfma_f32_16x16x32_bf16 v[80:83], a[72:75], a[208:211], v[80:83]\n" \
"ds_read_b128 a[68:71], v208 offset:0\n" \
"ds_read_b64_tr_b16 a[102:103], v209 offset:15360\n" \
"v_mfma_f32_16x16x32_bf16 v[80:83], a[76:79], a[212:215], v[80:83]\n" \
"ds_read_b64_tr_b16 a[104:105], v209 offset:0\n" \
"v_mfma_f32_16x16x32_bf16 v[84:87], a[72:75], a[216:219], v[84:87]\n" \
"ds_read_b64_tr_b16 a[106:107], v209 offset:1024\n" \
"v_mfma_f32_16x16x32_bf16 v[84:87], a[76:79], a[220:223], v[84:87]\n" \
"global_load_lds_dwordx4 v252, s[42:43] offset:2048\n" \
"v_mfma_f32_16x16x32_bf16 v[52:55], a[200:203], a[80:83], v[52:55]\n" \
"s_add_u32 s42, s42, 0x80\n" \
"s_addc_u32 s43, s43, 0\n" \
"ds_read_b128 a[72:75], v205 offset:0\n" \
"v_mfma_f32_16x16x32_bf16 v[52:55], a[204:207], a[84:87], v[52:55]\n" \
"ds_read_b128 a[76:79], v206 offset:0\n" \
"v_mfma_f32_16x16x32_bf16 v[88:91], a[80:83], a[208:211], v[88:91]\n" \
"ds_read_b64_tr_b16 a[108:109], v209 offset:12288\n" \
"v_mfma_f32_16x16x32_bf16 v[88:91], a[84:87], a[212:215], v[88:91]\n" \
"global_load_lds_dwordx4 v252, s[44:45] offset:1024\n" \
"v_mfma_f32_16x16x32_bf16 v[92:95], a[80:83], a[216:219], v[92:95]\n" \
"ds_read_b64_tr_b16 a[110:111], v209 offset:13312\n" \
"v_mfma_f32_16x16x32_bf16 v[92:95], a[84:87], a[220:223], v[92:95]\n" \
"ds_read_b64_tr_b16 a[112:113], v210 offset:0\n" \
"v_mfma_f32_16x16x32_bf16 v[60:63], a[200:203], a[88:91], v[60:63]\n" \
"ds_read_b128 a[80:83], v205 offset:4096\n" \
"v_mfma_f32_16x16x32_bf16 v[60:63], a[204:207], a[92:95], v[60:63]\n" \
"global_load_lds_dwordx4 v252, s[44:45] offset:3072\n" \
"v_mfma_f32_16x16x32_bf16 v[96:99], a[88:91], a[208:211], v[96:99]\n" \
"s_add_u32 s44, s44, 0x80\n" \
"s_addc_u32 s45, s45, 0\n" \
"ds_read_b128 a[84:87], v206 offset:4096\n" \
"v_mfma_f32_16x16x32_bf16 v[96:99], a[92:95], a[212:215], v[96:99]\n" \
"ds_read_b64_tr_b16 a[114:115], v210 offset:1024\n" \
"v_mfma_f32_16x16x32_bf16 v[100:103], a[88:91], a[216:219], v[100:103]\n" \
"s_add_u32 m0, s85, 81920\n" \
"ds_read_b64_tr_b16 a[116:117], v210 offset:12288\n" \
"v_mfma_f32_16x16x32_bf16 v[100:103], a[92:95], a[220:223], v[100:103]\n" \
"buffer_load_dwordx4 v246, s[12:15], s46 offen lds\n" \
"v_mfma_f32_16x16x32_bf16 v[68:71], a[200:203], a[192:195], v[68:71]\n" \
"ds_read_b128 a[88:91], v205 offset:8192\n" \
"s_add_u32 s46, s46, 0x80\n" \
"v_mfma_f32_16x16x32_bf16 v[68:71], a[204:207], a[196:199], v[68:71]\n" \
"ds_read_b128 a[92:95], v206 offset:8192\n" \
"v_mfma_f32_16x16x32_bf16 v[104:107], a[192:195], a[208:211], v[104:107]\n" \
"s_add_u32 m0, s84, 90112\n" \
"ds_read_b64_tr_b16 a[118:119], v210 offset:13312\n" \
"v_mfma_f32_16x16x32_bf16 v[104:107], a[196:199], a[212:215], v[104:107]\n" \
"global_load_lds_dwordx4 v253, s[48:49] offset:-4096\n" \
"v_mfma_f32_16x16x32_bf16 v[108:111], a[192:195], a[216:219], v[108:111]\n" \
"v_mfma_f32_16x16x32_bf16 v[108:111], a[196:199], a[220:223], v[108:111]\n" \
"s_waitcnt lgkmcnt(2)\n" \
"v_mfma_f32_16x16x32_bf16 v[32:35], a[96:99], a[64:67], v[32:35]\n" \
"ds_read_b128 a[192:195], v205 offset:12288\n" \
"v_mfma_f32_16x16x32_bf16 v[32:35], a[100:103], a[68:71], v[32:35]\n" \
"global_load_lds_dwordx4 v253, s[48:49] offset:-2048\n" \
"v_mfma_f32_16x16x32_bf16 v[40:43], a[96:99], a[72:75], v[40:43]\n" \
"ds_read_b128 a[196:199], v206 offset:12288\n" \
"v_mfma_f32_16x16x32_bf16 v[40:43], a[100:103], a[76:79], v[40:43]\n" \
"ds_read_b64_tr_b16 a[200:201], v210 offset:2048\n" \
"v_mfma_f32_16x16x32_bf16 v[0:3], a[104:107], a[72:75], v[0:3]\n" \
"ds_read_b64_tr_b16 a[202:203], v210 offset:3072\n" \
"v_mfma_f32_16x16x32_bf16 v[0:3], a[108:111], a[76:79], v[0:3]\n" \
"s_waitcnt lgkmcnt(3)\n" \
"global_load_lds_dwordx4 v253, s[48:49] offset:0\n" \
"v_mfma_f32_16x16x32_bf16 v[4:7], a[112:115], a[72:75], v[4:7]\n" \
"s_add_u32 s48, s48, 0x60000\n" \
"s_addc_u32 s49, s49, 0\n" \
"ds_read_b64_tr_b16 a[204:205], v210 offset:14336\n" \
"v_mfma_f32_16x16x32_bf16 v[4:7], a[116:119], a[76:79], v[4:7]\n" \
"ds_read_b64_tr_b16 a[206:207], v210 offset:15360\n" \
"v_mfma_f32_16x16x32_bf16 v[48:51], a[96:99], a[80:83], v[48:51]\n" \
"ds_read_b64_tr_b16 a[208:209], v209 offset:4096\n" \
"v_mfma_f32_16x16x32_bf16 v[48:51], a[100:103], a[84:87], v[48:51]\n" \
"global_load_lds_dwordx4 v253, s[50:51] offset:-3072\n" \
"v_mfma_f32_16x16x32_bf16 v[8:11], a[104:107], a[80:83], v[8:11]\n" \
"ds_read_b64_tr_b16 a[210:211], v209 offset:5120\n" \
"v_mfma_f32_16x16x32_bf16 v[8:11], a[108:111], a[84:87], v[8:11]\n" \
"ds_read_b64_tr_b16 a[212:213], v209 offset:16384\n" \
"v_mfma_f32_16x16x32_bf16 v[12:15], a[112:115], a[80:83], v[12:15]\n" \
"ds_read_b64_tr_b16 a[214:215], v209 offset:17408\n" \
"v_mfma_f32_16x16x32_bf16 v[12:15], a[116:119], a[84:87], v[12:15]\n" \
"global_load_lds_dwordx4 v253, s[50:51] offset:-1024\n" \
"v_mfma_f32_16x16x32_bf16 v[56:59], a[96:99], a[88:91], v[56:59]\n" \
"ds_read_b64_tr_b16 a[216:217], v210 offset:4096\n" \
"v_mfma_f32_16x16x32_bf16 v[56:59], a[100:103], a[92:95], v[56:59]\n" \
"ds_read_b64_tr_b16 a[218:219], v210 offset:5120\n" \
"v_mfma_f32_16x16x32_bf16 v[16:19], a[104:107], a[88:91], v[16:19]\n" \
"ds_read_b64_tr_b16 a[220:221], v210 offset:16384\n" \
"v_mfma_f32_16x16x32_bf16 v[16:19], a[108:111], a[92:95], v[16:19]\n" \
"global_load_lds_dwordx4 v253, s[50:51] offset:1024\n" \
"v_mfma_f32_16x16x32_bf16 v[20:23], a[112:115], a[88:91], v[20:23]\n" \
"s_add_u32 s50, s50, 0x60000\n" \
"s_addc_u32 s51, s51, 0\n" \
"ds_read_b64_tr_b16 a[222:223], v210 offset:17408\n" \
"v_mfma_f32_16x16x32_bf16 v[20:23], a[116:119], a[92:95], v[20:23]\n" \
"s_waitcnt lgkmcnt(3)\n" \
"v_add_u32 v207, 0x1d000, v207\n" \
"v_add_u32 v208, 0x1d000, v208\n" \
"v_mfma_f32_16x16x32_bf16 v[64:67], a[96:99], a[192:195], v[64:67]\n" \
"v_add_u32 v205, 0x1d000, v205\n" \
"v_add_u32 v206, 0x1d000, v206\n" \
"v_mfma_f32_16x16x32_bf16 v[64:67], a[100:103], a[196:199], v[64:67]\n" \
"v_add_u32 v209, 0x1d000, v209\n" \
"v_add_u32 v210, 0x1d000, v210\n" \
"v_mfma_f32_16x16x32_bf16 v[24:27], a[104:107], a[192:195], v[24:27]\n" \
"v_mfma_f32_16x16x32_bf16 v[24:27], a[108:111], a[196:199], v[24:27]\n" \
"v_mfma_f32_16x16x32_bf16 v[28:31], a[112:115], a[192:195], v[28:31]\n" \
"s_waitcnt vmcnt(12) lgkmcnt(0)\n" \
"v_mfma_f32_16x16x32_bf16 v[28:31], a[116:119], a[196:199], v[28:31]\n" \
"s_barrier\n" \
"v_mfma_f32_16x16x32_bf16 v[72:75], a[64:67], a[208:211], v[72:75]\n" \
"ds_read_b64_tr_b16 a[96:97], v209 offset:2048\n" \
"s_waitcnt vmcnt(11)\n" \
"v_mfma_f32_16x16x32_bf16 v[72:75], a[68:71], a[212:215], v[72:75]\n" \
"ds_read_b64_tr_b16 a[98:99], v209 offset:3072\n" \
"v_mfma_f32_16x16x32_bf16 v[44:47], a[200:203], a[72:75], v[44:47]\n" \
"s_add_u32 m0, s95, 0\n" \
"ds_read_b128 a[64:67], v207 offset:0\n" \
"ds_read_b64_tr_b16 a[100:101], v209 offset:14336\n" \
"v_mfma_f32_16x16x32_bf16 v[44:47], a[204:207], a[76:79], v[44:47]\n" \
"global_load_lds_dwordx4 v252, s[42:43] offset:0\n" \
"v_mfma_f32_16x16x32_bf16 v[80:83], a[72:75], a[208:211], v[80:83]\n" \
"ds_read_b128 a[68:71], v208 offset:0\n" \
"ds_read_b64_tr_b16 a[102:103], v209 offset:15360\n" \
"v_mfma_f32_16x16x32_bf16 v[80:83], a[76:79], a[212:215], v[80:83]\n" \
"ds_read_b64_tr_b16 a[104:105], v209 offset:0\n" \
"v_mfma_f32_16x16x32_bf16 v[84:87], a[72:75], a[216:219], v[84:87]\n" \
"ds_read_b64_tr_b16 a[106:107], v209 offset:1024\n" \
"v_mfma_f32_16x16x32_bf16 v[84:87], a[76:79], a[220:223], v[84:87]\n" \
"global_load_lds_dwordx4 v252, s[42:43] offset:2048\n" \
"v_mfma_f32_16x16x32_bf16 v[52:55], a[200:203], a[80:83], v[52:55]\n" \
"s_add_u32 s42, s42, 0x100\n" \
"s_addc_u32 s43, s43, 0\n" \
"ds_read_b128 a[72:75], v205 offset:0\n" \
"v_mfma_f32_16x16x32_bf16 v[52:55], a[204:207], a[84:87], v[52:55]\n" \
"ds_read_b128 a[76:79], v206 offset:0\n" \
"v_mfma_f32_16x16x32_bf16 v[88:91], a[80:83], a[208:211], v[88:91]\n" \
"ds_read_b64_tr_b16 a[108:109], v209 offset:12288\n" \
"v_mfma_f32_16x16x32_bf16 v[88:91], a[84:87], a[212:215], v[88:91]\n" \
"global_load_lds_dwordx4 v252, s[44:45] offset:1024\n" \
"v_mfma_f32_16x16x32_bf16 v[92:95], a[80:83], a[216:219], v[92:95]\n" \
"ds_read_b64_tr_b16 a[110:111], v209 offset:13312\n" \
"v_mfma_f32_16x16x32_bf16 v[92:95], a[84:87], a[220:223], v[92:95]\n" \
"ds_read_b64_tr_b16 a[112:113], v210 offset:0\n" \
"v_mfma_f32_16x16x32_bf16 v[60:63], a[200:203], a[88:91], v[60:63]\n" \
"ds_read_b128 a[80:83], v205 offset:4096\n" \
"v_mfma_f32_16x16x32_bf16 v[60:63], a[204:207], a[92:95], v[60:63]\n" \
"global_load_lds_dwordx4 v252, s[44:45] offset:3072\n" \
"v_mfma_f32_16x16x32_bf16 v[96:99], a[88:91], a[208:211], v[96:99]\n" \
"s_add_u32 s44, s44, 0x100\n" \
"s_addc_u32 s45, s45, 0\n" \
"ds_read_b128 a[84:87], v206 offset:4096\n" \
"v_mfma_f32_16x16x32_bf16 v[96:99], a[92:95], a[212:215], v[96:99]\n" \
"ds_read_b64_tr_b16 a[114:115], v210 offset:1024\n" \
"v_mfma_f32_16x16x32_bf16 v[100:103], a[88:91], a[216:219], v[100:103]\n" \
"s_add_u32 m0, s85, 16384\n" \
"ds_read_b64_tr_b16 a[116:117], v210 offset:12288\n" \
"v_mfma_f32_16x16x32_bf16 v[100:103], a[92:95], a[220:223], v[100:103]\n" \
"buffer_load_dwordx4 v246, s[12:15], s46 offen lds\n" \
"v_mfma_f32_16x16x32_bf16 v[68:71], a[200:203], a[192:195], v[68:71]\n" \
"ds_read_b128 a[88:91], v205 offset:8192\n" \
"s_add_u32 s46, s46, 0x100\n" \
"v_mfma_f32_16x16x32_bf16 v[68:71], a[204:207], a[196:199], v[68:71]\n" \
"ds_read_b128 a[92:95], v206 offset:8192\n" \
"v_mfma_f32_16x16x32_bf16 v[104:107], a[192:195], a[208:211], v[104:107]\n" \
"s_add_u32 m0, s84, 24576\n" \
"ds_read_b64_tr_b16 a[118:119], v210 offset:13312\n" \
"v_mfma_f32_16x16x32_bf16 v[104:107], a[196:199], a[212:215], v[104:107]\n" \
"global_load_lds_dwordx4 v253, s[48:49] offset:-4096\n" \
"v_mfma_f32_16x16x32_bf16 v[108:111], a[192:195], a[216:219], v[108:111]\n" \
"v_xor_b32 v226, v226, v227\n" \
"v_cmp_ne_u32 vcc, s60, v226\n" \
"v_mfma_f32_16x16x32_bf16 v[108:111], a[196:199], a[220:223], v[108:111]\n" \
"s_waitcnt lgkmcnt(2)\n" \
"v_mfma_f32_16x16x32_bf16 v[32:35], a[96:99], a[64:67], v[32:35]\n" \
"ds_read_b128 a[192:195], v205 offset:12288\n" \
"v_mfma_f32_16x16x32_bf16 v[32:35], a[100:103], a[68:71], v[32:35]\n" \
"global_load_lds_dwordx4 v253, s[48:49] offset:-2048\n" \
"v_mfma_f32_16x16x32_bf16 v[40:43], a[96:99], a[72:75], v[40:43]\n" \
"ds_read_b128 a[196:199], v206 offset:12288\n" \
"v_mfma_f32_16x16x32_bf16 v[40:43], a[100:103], a[76:79], v[40:43]\n" \
"ds_read_b64_tr_b16 a[200:201], v210 offset:2048\n" \
"v_mfma_f32_16x16x32_bf16 v[0:3], a[104:107], a[72:75], v[0:3]\n" \
"ds_read_b64_tr_b16 a[202:203], v210 offset:3072\n" \
"v_mfma_f32_16x16x32_bf16 v[0:3], a[108:111], a[76:79], v[0:3]\n" \
"s_waitcnt lgkmcnt(3)\n" \
"global_load_lds_dwordx4 v253, s[48:49] offset:0\n" \
"v_mfma_f32_16x16x32_bf16 v[4:7], a[112:115], a[72:75], v[4:7]\n" \
"s_add_u32 s48, s48, 0xc0000\n" \
"s_addc_u32 s49, s49, 0\n" \
"ds_read_b64_tr_b16 a[204:205], v210 offset:14336\n" \
"v_mfma_f32_16x16x32_bf16 v[4:7], a[116:119], a[76:79], v[4:7]\n" \
"ds_read_b64_tr_b16 a[206:207], v210 offset:15360\n" \
"v_mfma_f32_16x16x32_bf16 v[48:51], a[96:99], a[80:83], v[48:51]\n" \
"ds_read_b64_tr_b16 a[208:209], v209 offset:4096\n" \
"v_mfma_f32_16x16x32_bf16 v[48:51], a[100:103], a[84:87], v[48:51]\n" \
"global_load_lds_dwordx4 v253, s[50:51] offset:-3072\n" \
"v_mfma_f32_16x16x32_bf16 v[8:11], a[104:107], a[80:83], v[8:11]\n" \
"ds_read_b64_tr_b16 a[210:211], v209 offset:5120\n" \
"v_mfma_f32_16x16x32_bf16 v[8:11], a[108:111], a[84:87], v[8:11]\n" \
"ds_read_b64_tr_b16 a[212:213], v209 offset:16384\n" \
"v_mfma_f32_16x16x32_bf16 v[12:15], a[112:115], a[80:83], v[12:15]\n" \
"ds_read_b64_tr_b16 a[214:215], v209 offset:17408\n" \
"v_mfma_f32_16x16x32_bf16 v[12:15], a[116:119], a[84:87], v[12:15]\n" \
"global_load_lds_dwordx4 v253, s[50:51] offset:-1024\n" \
"v_mfma_f32_16x16x32_bf16 v[56:59], a[96:99], a[88:91], v[56:59]\n" \
"ds_read_b64_tr_b16 a[216:217], v210 offset:4096\n" \
"v_mfma_f32_16x16x32_bf16 v[56:59], a[100:103], a[92:95], v[56:59]\n" \
"ds_read_b64_tr_b16 a[218:219], v210 offset:5120\n" \
"v_mfma_f32_16x16x32_bf16 v[16:19], a[104:107], a[88:91], v[16:19]\n" \
"ds_read_b64_tr_b16 a[220:221], v210 offset:16384\n" \
"v_mfma_f32_16x16x32_bf16 v[16:19], a[108:111], a[92:95], v[16:19]\n" \
"global_load_lds_dwordx4 v253, s[50:51] offset:1024\n" \
"v_mfma_f32_16x16x32_bf16 v[20:23], a[112:115], a[88:91], v[20:23]\n" \
"s_add_u32 s50, s50, 0xc0000\n" \
"s_addc_u32 s51, s51, 0\n" \
"ds_read_b64_tr_b16 a[222:223], v210 offset:17408\n" \
"v_mfma_f32_16x16x32_bf16 v[20:23], a[116:119], a[92:95], v[20:23]\n" \
"s_waitcnt lgkmcnt(3)\n" \
"v_add_u32 v207, 0xffff3000, v207\n" \
"v_add_u32 v208, 0xffff3000, v208\n" \
"v_mfma_f32_16x16x32_bf16 v[64:67], a[96:99], a[192:195], v[64:67]\n" \
"v_add_u32 v205, 0xffff3000, v205\n" \
"v_add_u32 v206, 0xffff3000, v206\n" \
"v_mfma_f32_16x16x32_bf16 v[64:67], a[100:103], a[196:199], v[64:67]\n" \
"v_add_u32 v209, 0xffff3000, v209\n" \
"v_add_u32 v210, 0xffff3000, v210\n" \
"v_mfma_f32_16x16x32_bf16 v[24:27], a[104:107], a[192:195], v[24:27]\n" \
"s_and_b32 vcc_hi, vcc_lo, -1\n" \
"s_cbranch_scc1 Lpl3_%=\n" \
"Lpr3_%=:\n" \
"v_mfma_f32_16x16x32_bf16 v[24:27], a[108:111], a[196:199], v[24:27]\n" \
"v_readfirstlane_b32 s71, v227\n" \
"s_and_b32 s70, %1, 7\n" \
"v_mfma_f32_16x16x32_bf16 v[28:31], a[112:115], a[192:195], v[28:31]\n" \
"s_waitcnt vmcnt(11) lgkmcnt(0)\n" \
"s_lshl_b32 s78, s70, 2\n" \
"v_mfma_f32_16x16x32_bf16 v[28:31], a[116:119], a[196:199], v[28:31]\n" \
"s_barrier\n" \
"v_mfma_f32_16x16x32_bf16 v[72:75], a[64:67], a[208:211], v[72:75]\n" \
"ds_read_b64_tr_b16 a[96:97], v209 offset:2048\n" \
"v_subrev_u32 v227, s71, v227\n" \
"v_mfma_f32_16x16x32_bf16 v[72:75], a[68:71], a[212:215], v[72:75]\n" \
"ds_read_b64_tr_b16 a[98:99], v209 offset:3072\n" \
"v_mfma_f32_16x16x32_bf16 v[44:47], a[200:203], a[72:75], v[44:47]\n" \
"s_add_u32 m0, s95, 118784\n" \
"ds_read_b128 a[64:67], v207 offset:0\n" \
"ds_read_b64_tr_b16 a[100:101], v209 offset:14336\n" \
"v_mfma_f32_16x16x32_bf16 v[44:47], a[204:207], a[76:79], v[44:47]\n" \
"global_load_lds_dwordx4 v252, s[42:43] offset:0\n" \
"v_add_u32_dpp v227, v227, v227 quad_perm:[1,0,3,2] row_mask:0xf bank_mask:0xf\n" \
"v_mfma_f32_16x16x32_bf16 v[80:83], a[72:75], a[208:211], v[80:83]\n" \
"ds_read_b128 a[68:71], v208 offset:0\n" \
"ds_read_b64_tr_b16 a[102:103], v209 offset:15360\n" \
"v_mfma_f32_16x16x32_bf16 v[80:83], a[76:79], a[212:215], v[80:83]\n" \
"ds_read_b64_tr_b16 a[104:105], v209 offset:0\n" \
"v_add_u32_dpp v227, v227, v227 quad_perm:[2,3,0,1] row_mask:0xf bank_mask:0xf\n" \
"v_mfma_f32_16x16x32_bf16 v[84:87], a[72:75], a[216:219], v[84:87]\n" \
"ds_read_b64_tr_b16 a[106:107], v209 offset:1024\n" \
"v_readlane_b32 s71, v227, s78\n" \
"v_mfma_f32_16x16x32_bf16 v[84:87], a[76:79], a[220:223], v[84:87]\n" \
"global_load_lds_dwordx4 v252, s[42:43] offset:2048\n" \
"v_lshrrev_b32 v226, 6, v248\n" \
"v_mfma_f32_16x16x32_bf16 v[52:55], a[200:203], a[80:83], v[52:55]\n" \
"s_add_u32 s42, s42, 0x80\n" \
"s_addc_u32 s43, s43, 0\n" \
"ds_read_b128 a[72:75], v205 offset:0\n" \
"v_mfma_f32_16x16x32_bf16 v[52:55], a[204:207], a[84:87], v[52:55]\n" \
"ds_read_b128 a[76:79], v206 offset:0\n" \
"v_subrev_u32 v227, s71, v227\n" \
"v_mfma_f32_16x16x32_bf16 v[88:91], a[80:83], a[208:211], v[88:91]\n" \
"ds_read_b64_tr_b16 a[108:109], v209 offset:12288\n" \
"v_lshl_add_u32 v227, v227, 3, v226\n" \
"v_mfma_f32_16x16x32_bf16 v[88:91], a[84:87], a[212:215], v[88:91]\n" \
"global_load_lds_dwordx4 v252, s[44:45] offset:1024\n" \
"v_cmp_gt_i32 vcc, s70, v227\n" \
"v_mfma_f32_16x16x32_bf16 v[92:95], a[80:83], a[216:219], v[92:95]\n" \
"ds_read_b64_tr_b16 a[110:111], v209 offset:13312\n" \
"v_mfma_f32_16x16x32_bf16 v[92:95], a[84:87], a[220:223], v[92:95]\n" \
"s_bcnt1_i32_b32 s70, vcc_lo\n" \
"s_lshr_b32 s70, s70, 2\n" \
"ds_read_b64_tr_b16 a[112:113], v210 offset:0\n" \
"v_mfma_f32_16x16x32_bf16 v[60:63], a[200:203], a[88:91], v[60:63]\n" \
"s_lshr_b32 s78, s70, 2\n" \
"ds_read_b128 a[80:83], v205 offset:4096\n" \
"s_mul_i32 s71, s78, 7\n" \
"v_mfma_f32_16x16x32_bf16 v[60:63], a[204:207], a[92:95], v[60:63]\n" \
"s_xor_b32 s70, s70, s71\n" \
"global_load_lds_dwordx4 v252, s[44:45] offset:3072\n" \
"v_mfma_f32_16x16x32_bf16 v[96:99], a[88:91], a[208:211], v[96:99]\n" \
"s_add_u32 s44, s44, 0x80\n" \
"s_addc_u32 s45, s45, 0\n" \
"ds_read_b128 a[84:87], v206 offset:4096\n" \
"v_mfma_f32_16x16x32_bf16 v[96:99], a[92:95], a[212:215], v[96:99]\n" \
"s_xor_b32 s78, s78, 1\n" \
"s_lshl_b32 s65, s70, 2\n" \
"ds_read_b64_tr_b16 a[114:115], v210 offset:1024\n" \
"v_mfma_f32_16x16x32_bf16 v[100:103], a[88:91], a[216:219], v[100:103]\n" \
"s_add_u32 m0, s85, 135168\n" \
"s_lshr_b32 s64, 0x8654, s65\n" \
"ds_read_b64_tr_b16 a[116:117], v210 offset:12288\n" \
"v_mfma_f32_16x16x32_bf16 v[100:103], a[92:95], a[220:223], v[100:103]\n" \
"buffer_load_dwordx4 v246, s[12:15], s46 offen lds\n" \
"v_mfma_f32_16x16x32_bf16 v[68:71], a[200:203], a[192:195], v[68:71]\n" \
"s_add_u32 s46, s46, 0x80\n" \
"ds_read_b128 a[88:91], v205 offset:8192\n" \
"s_and_b32 s64, s64, 15\n" \
"v_mfma_f32_16x16x32_bf16 v[68:71], a[204:207], a[196:199], v[68:71]\n" \
"s_add_u32 s64, s64, 32\n" \
"ds_read_b128 a[92:95], v206 offset:8192\n" \
"s_lshr_b32 s70, %1, 6\n" \
"v_mfma_f32_16x16x32_bf16 v[104:107], a[192:195], a[208:211], v[104:107]\n" \
"s_add_u32 m0, s84, 143360\n" \
"s_add_u32 s65, s65, s70\n" \
"ds_read_b64_tr_b16 a[118:119], v210 offset:13312\n" \
"v_mfma_f32_16x16x32_bf16 v[104:107], a[196:199], a[212:215], v[104:107]\n" \
"s_and_b32 s30, %1, 1\n" \
"global_load_lds_dwordx4 v253, s[48:49] offset:-4096\n" \
"v_mfma_f32_16x16x32_bf16 v[108:111], a[192:195], a[216:219], v[108:111]\n" \
"s_lshl_b32 s30, s30, 3\n" \
"s_add_u32 s30, s30, s63\n" \
"s_lshl_b32 s30, s30, 2\n" \
"v_mfma_f32_16x16x32_bf16 v[108:111], a[196:199], a[220:223], v[108:111]\n" \
"s_waitcnt lgkmcnt(2)\n" \
"s_lshl_b32 s70, s88, 7\n" \
"s_add_u32 s30, s30, s70\n" \
"v_mfma_f32_16x16x32_bf16 v[32:35], a[96:99], a[64:67], v[32:35]\n" \
"s_add_u32 s30, s30, 65591296\n" \
"ds_read_b128 a[192:195], v205 offset:12288\n" \
"v_lshrrev_b32 v221, 2, v248\n" \
"v_mfma_f32_16x16x32_bf16 v[32:35], a[100:103], a[68:71], v[32:35]\n" \
"s_lshl_b32 s81, s65, 7\n" \
"global_load_lds_dwordx4 v253, s[48:49] offset:-2048\n" \
"v_mfma_f32_16x16x32_bf16 v[40:43], a[96:99], a[72:75], v[40:43]\n" \
"s_add_u32 s82, s81, 65591296\n" \
"ds_read_b128 a[196:199], v206 offset:12288\n" \
"s_add_u32 s81, s81, 65589248\n" \
"v_mfma_f32_16x16x32_bf16 v[40:43], a[100:103], a[76:79], v[40:43]\n" \
"s_mov_b64 s[52:53], s[26:27]\n" \
"s_and_b32 s53, s53, 0xffff\n" \
"ds_read_b64_tr_b16 a[200:201], v210 offset:2048\n" \
"v_mfma_f32_16x16x32_bf16 v[0:3], a[104:107], a[72:75], v[0:3]\n" \
"s_mov_b32 s54, 8388608\n" \
"s_mov_b32 s55, 0x20000\n" \
"ds_read_b64_tr_b16 a[202:203], v210 offset:3072\n" \
"v_mfma_f32_16x16x32_bf16 v[0:3], a[108:111], a[76:79], v[0:3]\n" \
"s_waitcnt lgkmcnt(3)\n" \
"global_load_lds_dwordx4 v253, s[48:49] offset:0\n" \
"s_lshr_b32 s83, s63, 1\n" \
"v_mfma_f32_16x16x32_bf16 v[4:7], a[112:115], a[72:75], v[4:7]\n" \
"s_add_u32 s48, s48, 0x60000\n" \
"s_addc_u32 s49, s49, 0\n" \
"ds_read_b64_tr_b16 a[204:205], v210 offset:14336\n" \
"v_mfma_f32_16x16x32_bf16 v[4:7], a[116:119], a[76:79], v[4:7]\n" \
"s_add_u32 s83, s83, 2\n" \
"s_lshr_b32 s70, s18, 8\n" \
"ds_read_b64_tr_b16 a[206:207], v210 offset:15360\n" \
"v_mfma_f32_16x16x32_bf16 v[48:51], a[96:99], a[80:83], v[48:51]\n" \
"s_sub_u32 s83, s83, s70\n" \
"s_and_b32 s83, s83, 3\n" \
"ds_read_b64_tr_b16 a[208:209], v209 offset:4096\n" \
"v_mfma_f32_16x16x32_bf16 v[48:51], a[100:103], a[84:87], v[48:51]\n" \
"s_and_b32 s70, s63, 1\n" \
"global_load_lds_dwordx4 v253, s[50:51] offset:-3072\n" \
"v_mfma_f32_16x16x32_bf16 v[8:11], a[104:107], a[80:83], v[8:11]\n" \
"s_lshl_b32 s70, s70, 2\n" \
"s_or_b32 s83, s83, s70\n" \
"ds_read_b64_tr_b16 a[210:211], v209 offset:5120\n" \
"v_mfma_f32_16x16x32_bf16 v[8:11], a[108:111], a[84:87], v[8:11]\n" \
"s_mul_i32 s75, s78, s64\n" \
"ds_read_b64_tr_b16 a[212:213], v209 offset:16384\n" \
"v_lshrrev_b32 v228, 4, v248\n" \
"v_mfma_f32_16x16x32_bf16 v[12:15], a[112:115], a[80:83], v[12:15]\n" \
"s_lshl_b32 s70, s65, 2\n" \
"s_add_u32 s70, s70, s61\n" \
"ds_read_b64_tr_b16 a[214:215], v209 offset:17408\n" \
"v_mfma_f32_16x16x32_bf16 v[12:15], a[116:119], a[84:87], v[12:15]\n" \
"s_mul_i32 s73, s70, 69632\n" \
"global_load_lds_dwordx4 v253, s[50:51] offset:-1024\n" \
"v_mfma_f32_16x16x32_bf16 v[56:59], a[96:99], a[88:91], v[56:59]\n" \
"s_add_u32 s73, s73, 52740096\n" \
"s_add_u32 s73, s73, s28\n" \
"ds_read_b64_tr_b16 a[216:217], v210 offset:4096\n" \
"v_mfma_f32_16x16x32_bf16 v[56:59], a[100:103], a[92:95], v[56:59]\n" \
"s_mul_i32 s70, s70, 266240\n" \
"s_lshl_b32 s71, s75, 12\n" \
"ds_read_b64_tr_b16 a[218:219], v210 offset:5120\n" \
"v_mfma_f32_16x16x32_bf16 v[16:19], a[104:107], a[88:91], v[16:19]\n" \
"s_add_u32 s70, s70, s71\n" \
"s_add_u32 s70, s70, 35700736\n" \
"ds_read_b64_tr_b16 a[220:221], v210 offset:16384\n" \
"v_mfma_f32_16x16x32_bf16 v[16:19], a[108:111], a[92:95], v[16:19]\n" \
"s_add_u32 s36, s28, s70\n" \
"global_load_lds_dwordx4 v253, s[50:51] offset:1024\n" \
"v_mfma_f32_16x16x32_bf16 v[20:23], a[112:115], a[88:91], v[20:23]\n" \
"s_add_u32 s50, s50, 0x60000\n" \
"s_addc_u32 s51, s51, 0\n" \
"ds_read_b64_tr_b16 a[222:223], v210 offset:17408\n" \
"v_mfma_f32_16x16x32_bf16 v[20:23], a[116:119], a[92:95], v[20:23]\n" \
"s_waitcnt lgkmcnt(3)\n" \
"v_add_u32 v207, 0xffff0000, v207\n" \
"v_add_u32 v208, 0xffff0000, v208\n" \
"v_mfma_f32_16x16x32_bf16 v[64:67], a[96:99], a[192:195], v[64:67]\n" \
"s_mov_b32 s37, s29\n" \
"v_add_u32 v205, 0xffff0000, v205\n" \
"v_add_u32 v206, 0xffff0000, v206\n" \
"v_mfma_f32_16x16x32_bf16 v[64:67], a[100:103], a[196:199], v[64:67]\n" \
"s_lshr_b32 s74, s61, 1\n" \
"v_add_u32 v209, 0xffff0000, v209\n" \
"v_add_u32 v210, 0xffff0000, v210\n" \
"v_mfma_f32_16x16x32_bf16 v[24:27], a[104:107], a[192:195], v[24:27]\n" \
"s_lshl_b32 s74, s74, 16\n" \
"s_and_b32 s70, s61, 1\n" \
"s_lshl_b32 s70, s70, 14\n" \
"v_mfma_f32_16x16x32_bf16 v[24:27], a[108:111], a[196:199], v[24:27]\n" \
"s_add_u32 s74, s74, s70\n" \
"s_lshl_b32 s71, s83, 8\n" \
"s_add_u32 s74, s74, s71\n" \
"v_mfma_f32_16x16x32_bf16 v[28:31], a[112:115], a[192:195], v[28:31]\n" \
"s_waitcnt vmcnt(11) lgkmcnt(0)\n" \
"s_add_u32 s40, s24, s74\n" \
"s_addc_u32 s41, s25, 0\n" \
"v_mfma_f32_16x16x32_bf16 v[28:31], a[116:119], a[196:199], v[28:31]\n" \
"s_barrier\n" \
"v_mfma_f32_16x16x32_bf16 v[72:75], a[64:67], a[208:211], v[72:75]\n" \
"Lpg10432_%=:\n" \
"s_cbranch_execz Lpg12619_%=\n" \
"s_lshl_b32 s70, s75, 17\n" \
"s_add_u32 s70, s70, s74\n" \
"ds_read_b64_tr_b16 a[96:97], v209 offset:2048\n" \
"v_mfma_f32_16x16x32_bf16 v[72:75], a[68:71], a[212:215], v[72:75]\n" \
"s_add_u32 s38, s18, s70\n" \
"s_addc_u32 s39, s19, 0\n" \
"ds_read_b64_tr_b16 a[98:99], v209 offset:3072\n" \
"v_mfma_f32_16x16x32_bf16 v[44:47], a[200:203], a[72:75], v[44:47]\n" \
"s_add_u32 m0, s95, 65536\n" \
"ds_read_b128 a[64:67], v207 offset:0\n" \
"ds_read_b64_tr_b16 a[100:101], v209 offset:14336\n" \
"v_mfma_f32_16x16x32_bf16 v[44:47], a[204:207], a[76:79], v[44:47]\n" \
"global_load_lds_dwordx4 v252, s[42:43] offset:0\n" \
"v_bfe_u32 v229, v228, 2, 3\n" \
"v_mfma_f32_16x16x32_bf16 v[80:83], a[72:75], a[208:211], v[80:83]\n" \
"ds_read_b128 a[68:71], v208 offset:0\n" \
"ds_read_b64_tr_b16 a[102:103], v209 offset:15360\n" \
"v_mfma_f32_16x16x32_bf16 v[80:83], a[76:79], a[212:215], v[80:83]\n" \
"ds_read_b64_tr_b16 a[104:105], v209 offset:0\n" \
"v_lshlrev_b32 v229, 11, v229\n" \
"v_mfma_f32_16x16x32_bf16 v[84:87], a[72:75], a[216:219], v[84:87]\n" \
"ds_read_b64_tr_b16 a[106:107], v209 offset:1024\n" \
"v_lshrrev_b32 v230, 5, v228\n" \
"v_mfma_f32_16x16x32_bf16 v[84:87], a[76:79], a[220:223], v[84:87]\n" \
"global_load_lds_dwordx4 v252, s[42:43] offset:2048\n" \
"v_lshl_add_u32 v229, v230, 6, v229\n" \
"v_mfma_f32_16x16x32_bf16 v[52:55], a[200:203], a[80:83], v[52:55]\n" \
"s_add_u32 s42, s42, 0x80\n" \
"s_addc_u32 s43, s43, 0\n" \
"ds_read_b128 a[72:75], v205 offset:0\n" \
"v_mfma_f32_16x16x32_bf16 v[52:55], a[204:207], a[84:87], v[52:55]\n" \
"ds_read_b128 a[76:79], v206 offset:0\n" \
"v_and_b32 v230, 3, v228\n" \
"v_mfma_f32_16x16x32_bf16 v[88:91], a[80:83], a[208:211], v[88:91]\n" \
"ds_read_b64_tr_b16 a[108:109], v209 offset:12288\n" \
"v_bfe_u32 v231, v228, 4, 1\n" \
"v_mfma_f32_16x16x32_bf16 v[88:91], a[84:87], a[212:215], v[88:91]\n" \
"global_load_lds_dwordx4 v252, s[44:45] offset:1024\n" \
"v_lshlrev_b32 v231, 1, v231\n" \
"v_mfma_f32_16x16x32_bf16 v[92:95], a[80:83], a[216:219], v[92:95]\n" \
"ds_read_b64_tr_b16 a[110:111], v209 offset:13312\n" \
"v_xor_b32 v230, v230, v231\n" \
"v_mfma_f32_16x16x32_bf16 v[92:95], a[84:87], a[220:223], v[92:95]\n" \
"ds_read_b64_tr_b16 a[112:113], v210 offset:0\n" \
"v_lshl_add_u32 v229, v230, 4, v229\n" \
"v_mfma_f32_16x16x32_bf16 v[60:63], a[200:203], a[88:91], v[60:63]\n" \
"ds_read_b128 a[80:83], v205 offset:4096\n" \
"v_add_u32 v216, 4096, v229\n" \
"v_mfma_f32_16x16x32_bf16 v[60:63], a[204:207], a[92:95], v[60:63]\n" \
"global_load_lds_dwordx4 v252, s[44:45] offset:3072\n" \
"v_add_u32 v217, 3200, v229\n" \
"v_mfma_f32_16x16x32_bf16 v[96:99], a[88:91], a[208:211], v[96:99]\n" \
"s_add_u32 s44, s44, 0x80\n" \
"s_addc_u32 s45, s45, 0\n" \
"ds_read_b128 a[84:87], v206 offset:4096\n" \
"v_mfma_f32_16x16x32_bf16 v[96:99], a[92:95], a[212:215], v[96:99]\n" \
"ds_read_b64_tr_b16 a[114:115], v210 offset:1024\n" \
"v_add_u32 v218, 34816, v229\n" \
"v_mfma_f32_16x16x32_bf16 v[100:103], a[88:91], a[216:219], v[100:103]\n" \
"s_add_u32 m0, s85, 81920\n" \
"ds_read_b64_tr_b16 a[116:117], v210 offset:12288\n" \
"v_add_u32 v219, 33920, v229\n" \
"v_mfma_f32_16x16x32_bf16 v[100:103], a[92:95], a[220:223], v[100:103]\n" \
"buffer_load_dwordx4 v246, s[12:15], s46 offen lds\n" \
"v_mfma_f32_16x16x32_bf16 v[68:71], a[200:203], a[192:195], v[68:71]\n" \
"s_add_u32 s46, s46, 0x80\n" \
"ds_read_b128 a[88:91], v205 offset:8192\n" \
"s_mov_b32 s75, 65536\n" \
"v_mfma_f32_16x16x32_bf16 v[68:71], a[204:207], a[196:199], v[68:71]\n" \
"s_lshr_b32 s70, s61, 1\n" \
"ds_read_b128 a[92:95], v206 offset:8192\n" \
"s_lshl_b32 s71, s70, 13\n" \
"v_mfma_f32_16x16x32_bf16 v[104:107], a[192:195], a[208:211], v[104:107]\n" \
"s_add_u32 m0, s84, 90112\n" \
"ds_read_b64_tr_b16 a[118:119], v210 offset:13312\n" \
"v_add_u32 v211, s71, v249\n" \
"v_mfma_f32_16x16x32_bf16 v[104:107], a[196:199], a[212:215], v[104:107]\n" \
"s_lshl_b32 s71, s65, 1\n" \
"global_load_lds_dwordx4 v253, s[48:49] offset:-4096\n" \
"v_mfma_f32_16x16x32_bf16 v[108:111], a[192:195], a[216:219], v[108:111]\n" \
"s_add_u32 s70, s70, s71\n" \
"s_lshl_b32 s77, s70, 18\n" \
"s_lshl_b32 s71, s83, 8\n" \
"v_mfma_f32_16x16x32_bf16 v[108:111], a[196:199], a[220:223], v[108:111]\n" \
"s_waitcnt lgkmcnt(2)\n" \
"s_add_u32 s77, s77, s71\n" \
"s_lshl_b32 s71, s78, 11\n" \
"v_mfma_f32_16x16x32_bf16 v[32:35], a[96:99], a[64:67], v[32:35]\n" \
"s_add_u32 s77, s77, s71\n" \
"ds_read_b128 a[192:195], v205 offset:12288\n" \
"s_and_b32 s70, s61, 1\n" \
"v_mfma_f32_16x16x32_bf16 v[32:35], a[100:103], a[68:71], v[32:35]\n" \
"s_lshl_b32 s71, s70, 7\n" \
"global_load_lds_dwordx4 v253, s[48:49] offset:-2048\n" \
"v_mfma_f32_16x16x32_bf16 v[40:43], a[96:99], a[72:75], v[40:43]\n" \
"s_add_u32 s77, s77, s71\n" \
"ds_read_b128 a[196:199], v206 offset:12288\n" \
"v_and_b32 v229, 3, v228\n" \
"v_mfma_f32_16x16x32_bf16 v[40:43], a[100:103], a[76:79], v[40:43]\n" \
"ds_read_b64_tr_b16 a[200:201], v210 offset:2048\n" \
"v_lshlrev_b32 v229, 3, v229\n" \
"v_mfma_f32_16x16x32_bf16 v[0:3], a[104:107], a[72:75], v[0:3]\n" \
"ds_read_b64_tr_b16 a[202:203], v210 offset:3072\n" \
"v_bfe_u32 v230, v228, 2, 2\n" \
"v_mfma_f32_16x16x32_bf16 v[0:3], a[108:111], a[76:79], v[0:3]\n" \
"s_waitcnt lgkmcnt(3)\n" \
"global_load_lds_dwordx4 v253, s[48:49] offset:0\n" \
"v_lshl_add_u32 v229, v230, 6, v229\n" \
"v_mfma_f32_16x16x32_bf16 v[4:7], a[112:115], a[72:75], v[4:7]\n" \
"s_add_u32 s48, s48, 0x60000\n" \
"s_addc_u32 s49, s49, 0\n" \
"ds_read_b64_tr_b16 a[204:205], v210 offset:14336\n" \
"v_mfma_f32_16x16x32_bf16 v[4:7], a[116:119], a[76:79], v[4:7]\n" \
"ds_read_b64_tr_b16 a[206:207], v210 offset:15360\n" \
"v_bfe_u32 v230, v228, 4, 1\n" \
"v_mfma_f32_16x16x32_bf16 v[48:51], a[96:99], a[80:83], v[48:51]\n" \
"ds_read_b64_tr_b16 a[208:209], v209 offset:4096\n" \
"v_lshl_add_u32 v229, v230, 5, v229\n" \
"v_mfma_f32_16x16x32_bf16 v[48:51], a[100:103], a[84:87], v[48:51]\n" \
"global_load_lds_dwordx4 v253, s[50:51] offset:-3072\n" \
"v_lshl_add_u32 v229, v230, 8, v229\n" \
"v_mfma_f32_16x16x32_bf16 v[8:11], a[104:107], a[80:83], v[8:11]\n" \
"ds_read_b64_tr_b16 a[210:211], v209 offset:5120\n" \
"v_lshrrev_b32 v230, 5, v228\n" \
"v_mfma_f32_16x16x32_bf16 v[8:11], a[108:111], a[84:87], v[8:11]\n" \
"ds_read_b64_tr_b16 a[212:213], v209 offset:16384\n" \
"v_lshl_add_u32 v229, v230, 12, v229\n" \
"v_mfma_f32_16x16x32_bf16 v[12:15], a[112:115], a[80:83], v[12:15]\n" \
"s_lshl_b32 s71, s70, 10\n" \
"ds_read_b64_tr_b16 a[214:215], v209 offset:17408\n" \
"v_add_u32 v212, s71, v229\n" \
"v_mfma_f32_16x16x32_bf16 v[12:15], a[116:119], a[84:87], v[12:15]\n" \
"s_lshl_b32 s70, s65, 3\n" \
"global_load_lds_dwordx4 v253, s[50:51] offset:-1024\n" \
"v_mfma_f32_16x16x32_bf16 v[56:59], a[96:99], a[88:91], v[56:59]\n" \
"s_add_u32 s70, s70, s83\n" \
"s_lshl_b32 s76, s70, 16\n" \
"ds_read_b64_tr_b16 a[216:217], v210 offset:4096\n" \
"v_mfma_f32_16x16x32_bf16 v[56:59], a[100:103], a[92:95], v[56:59]\n" \
"s_lshl_b32 s71, s61, 14\n" \
"s_add_u32 s76, s76, s71\n" \
"ds_read_b64_tr_b16 a[218:219], v210 offset:5120\n" \
"v_mfma_f32_16x16x32_bf16 v[16:19], a[104:107], a[88:91], v[16:19]\n" \
"s_add_u32 s76, s76, 57196544\n" \
"s_lshl_b32 s79, s70, 7\n" \
"ds_read_b64_tr_b16 a[220:221], v210 offset:16384\n" \
"v_mfma_f32_16x16x32_bf16 v[16:19], a[108:111], a[92:95], v[16:19]\n" \
"s_add_u32 s79, s79, 65601536\n" \
"global_load_lds_dwordx4 v253, s[50:51] offset:1024\n" \
"v_mfma_f32_16x16x32_bf16 v[20:23], a[112:115], a[88:91], v[20:23]\n" \
"s_add_u32 s50, s50, 0x60000\n" \
"s_addc_u32 s51, s51, 0\n" \
"ds_read_b64_tr_b16 a[222:223], v210 offset:17408\n" \
"v_mfma_f32_16x16x32_bf16 v[20:23], a[116:119], a[92:95], v[20:23]\n" \
"s_waitcnt lgkmcnt(3)\n" \
"v_add_u32 v207, 0x1d000, v207\n" \
"v_add_u32 v208, 0x1d000, v208\n" \
"v_mfma_f32_16x16x32_bf16 v[64:67], a[96:99], a[192:195], v[64:67]\n" \
"s_lshr_b32 s70, s64, 1\n" \
"v_add_u32 v205, 0x1d000, v205\n" \
"v_add_u32 v206, 0x1d000, v206\n" \
"v_mfma_f32_16x16x32_bf16 v[64:67], a[100:103], a[196:199], v[64:67]\n" \
"s_lshl_b32 s70, -1, s70\n" \
"v_add_u32 v209, 0x1d000, v209\n" \
"v_add_u32 v210, 0x1d000, v210\n" \
"v_mfma_f32_16x16x32_bf16 v[24:27], a[104:107], a[192:195], v[24:27]\n" \
"s_add_u32 s71, s64, 1\n" \
"s_lshr_b32 s71, s71, 1\n" \
"s_bfm_b32 s71, s71, 0\n" \
"v_mfma_f32_16x16x32_bf16 v[24:27], a[108:111], a[196:199], v[24:27]\n" \
"s_cmp_eq_u32 s78, 0\n" \
"s_cselect_b32 s80, s71, s70\n" \
"s_mov_b32 s16, 0xff000000\n" \
"v_mfma_f32_16x16x32_bf16 v[28:31], a[112:115], a[192:195], v[28:31]\n" \
"s_waitcnt vmcnt(11) lgkmcnt(0)\n" \
"s_mov_b32 s17, 0\n" \
"v_add_u32 v222, 0x1c90080, v250\n" \
"v_mfma_f32_16x16x32_bf16 v[28:31], a[116:119], a[196:199], v[28:31]\n" \
"s_barrier\n" \
"v_mfma_f32_16x16x32_bf16 v[72:75], a[64:67], a[208:211], v[72:75]\n" \
"ds_read_b64_tr_b16 a[96:97], v209 offset:2048\n" \
"v_cndmask_b32 v222, v250, v222, s[16:17]\n" \
"v_mfma_f32_16x16x32_bf16 v[72:75], a[68:71], a[212:215], v[72:75]\n" \
"s_mov_b32 s74, 98304\n" \
"ds_read_b64_tr_b16 a[98:99], v209 offset:3072\n" \
"v_mfma_f32_16x16x32_bf16 v[44:47], a[200:203], a[72:75], v[44:47]\n" \
"s_add_u32 m0, s95, 0\n" \
"ds_read_b128 a[64:67], v207 offset:0\n" \
"ds_read_b64_tr_b16 a[100:101], v209 offset:14336\n" \
"v_mfma_f32_16x16x32_bf16 v[44:47], a[204:207], a[76:79], v[44:47]\n" \
"global_load_lds_dwordx4 v252, s[42:43] offset:0\n" \
"v_mfma_f32_16x16x32_bf16 v[80:83], a[72:75], a[208:211], v[80:83]\n" \
"ds_read_b128 a[68:71], v208 offset:0\n" \
"ds_read_b64_tr_b16 a[102:103], v209 offset:15360\n" \
"v_mfma_f32_16x16x32_bf16 v[80:83], a[76:79], a[212:215], v[80:83]\n" \
"s_sub_u32 s74, s74, 32768\n" \
"s_cselect_b32 s74, 131072, s74\n" \
"ds_read_b64_tr_b16 a[104:105], v209 offset:0\n" \
"v_mfma_f32_16x16x32_bf16 v[84:87], a[72:75], a[216:219], v[84:87]\n" \
"ds_read_b64_tr_b16 a[106:107], v209 offset:1024\n" \
"v_add_u32 v213, s74, v211\n" \
"v_mfma_f32_16x16x32_bf16 v[84:87], a[76:79], a[220:223], v[84:87]\n" \
"global_load_lds_dwordx4 v252, s[42:43] offset:2048\n" \
"v_add_u32 v214, s74, v212\n" \
"v_mfma_f32_16x16x32_bf16 v[52:55], a[200:203], a[80:83], v[52:55]\n" \
"s_add_u32 s42, s42, 0x80\n" \
"s_addc_u32 s43, s43, 0\n" \
"ds_read_b128 a[72:75], v205 offset:0\n" \
"v_mfma_f32_16x16x32_bf16 v[52:55], a[204:207], a[84:87], v[52:55]\n" \
"ds_read_b128 a[76:79], v206 offset:0\n" \
"v_xor_b32 v215, 32, v214\n" \
"v_mfma_f32_16x16x32_bf16 v[88:91], a[80:83], a[208:211], v[88:91]\n" \
"s_and_b32 s89, s61, 1\n" \
"s_lshl_b32 s89, s89, 11\n" \
"ds_read_b64_tr_b16 a[108:109], v209 offset:12288\n" \
"v_mfma_f32_16x16x32_bf16 v[88:91], a[84:87], a[212:215], v[88:91]\n" \
"s_add_u32 s87, s87, s89\n" \
"global_load_lds_dwordx4 v252, s[44:45] offset:1024\n" \
"v_mfma_f32_16x16x32_bf16 v[92:95], a[80:83], a[216:219], v[92:95]\n" \
"s_add_u32 s87, s87, 52740096\n" \
"ds_read_b64_tr_b16 a[110:111], v209 offset:13312\n" \
"v_mfma_f32_16x16x32_bf16 v[92:95], a[84:87], a[220:223], v[92:95]\n" \
"s_cmp_eq_u32 s88, 0\n" \
"s_cselect_b32 s86, -1, 0\n" \
"ds_read_b64_tr_b16 a[112:113], v210 offset:0\n" \
"v_mfma_f32_16x16x32_bf16 v[60:63], a[200:203], a[88:91], v[60:63]\n" \
"s_mul_i32 s89, s88, 278528\n" \
"ds_read_b128 a[80:83], v205 offset:4096\n" \
"s_add_u32 s87, s87, s89\n" \
"v_mfma_f32_16x16x32_bf16 v[60:63], a[204:207], a[92:95], v[60:63]\n" \
"s_lshr_b32 s89, s61, 1\n" \
"global_load_lds_dwordx4 v252, s[44:45] offset:3072\n" \
"v_mfma_f32_16x16x32_bf16 v[96:99], a[88:91], a[208:211], v[96:99]\n" \
"s_add_u32 s44, s44, 0x80\n" \
"s_addc_u32 s45, s45, 0\n" \
"ds_read_b128 a[84:87], v206 offset:4096\n" \
"v_mfma_f32_16x16x32_bf16 v[96:99], a[92:95], a[212:215], v[96:99]\n" \
"s_lshl_b32 s89, s89, 8\n" \
"s_add_u32 s87, s87, s89\n" \
"ds_read_b64_tr_b16 a[114:115], v210 offset:1024\n" \
"v_mfma_f32_16x16x32_bf16 v[100:103], a[88:91], a[216:219], v[100:103]\n" \
"s_add_u32 m0, s85, 16384\n" \
"s_lshl_b32 s89, s61, 4\n" \
"ds_read_b64_tr_b16 a[116:117], v210 offset:12288\n" \
"v_mfma_f32_16x16x32_bf16 v[100:103], a[92:95], a[220:223], v[100:103]\n" \
"buffer_load_dwordx4 v246, s[12:15], s46 offen lds\n" \
"v_mfma_f32_16x16x32_bf16 v[68:71], a[200:203], a[192:195], v[68:71]\n" \
"s_add_u32 s46, s46, 0x80\n" \
"ds_read_b128 a[88:91], v205 offset:8192\n" \
"v_subrev_u32 v147, s89, v251\n" \
"v_mfma_f32_16x16x32_bf16 v[68:71], a[204:207], a[196:199], v[68:71]\n" \
"s_lshr_b32 s89, s61, 1\n" \
"ds_read_b128 a[92:95], v206 offset:8192\n" \
"s_lshl_b32 s90, s89, 9\n" \
"v_mfma_f32_16x16x32_bf16 v[104:107], a[192:195], a[208:211], v[104:107]\n" \
"s_add_u32 m0, s84, 24576\n" \
"s_add_u32 s90, s90, 511\n" \
"ds_read_b64_tr_b16 a[118:119], v210 offset:13312\n" \
"v_mfma_f32_16x16x32_bf16 v[104:107], a[196:199], a[212:215], v[104:107]\n" \
"global_load_lds_dwordx4 v253, s[48:49] offset:-4096\n" \
"v_sub_u32 v145, s90, v248\n" \
"v_mfma_f32_16x16x32_bf16 v[108:111], a[192:195], a[216:219], v[108:111]\n" \
"s_lshl_b32 s90, s89, 3\n" \
"s_not_b32 s90, s90\n" \
"v_sub_u32 v146, s90, v147\n" \
"v_mfma_f32_16x16x32_bf16 v[108:111], a[196:199], a[220:223], v[108:111]\n" \
"s_waitcnt lgkmcnt(2)\n" \
"v_subrev_u32 v152, 0, v147\n" \
"v_sub_u32 v144, 7, v147\n" \
"v_mfma_f32_16x16x32_bf16 v[32:35], a[96:99], a[64:67], v[32:35]\n" \
"ds_read_b128 a[192:195], v205 offset:12288\n" \
"v_or_b32 v152, v152, v144\n" \
"v_mfma_f32_16x16x32_bf16 v[32:35], a[100:103], a[68:71], v[32:35]\n" \
"global_load_lds_dwordx4 v253, s[48:49] offset:-2048\n" \
"v_ashrrev_i32 v152, 31, v152\n" \
"v_mfma_f32_16x16x32_bf16 v[40:43], a[96:99], a[72:75], v[40:43]\n" \
"ds_read_b128 a[196:199], v206 offset:12288\n" \
"v_and_b32 v152, 0xff800000, v152\n" \
"v_mfma_f32_16x16x32_bf16 v[40:43], a[100:103], a[76:79], v[40:43]\n" \
"ds_read_b64_tr_b16 a[200:201], v210 offset:2048\n" \
"v_mov_b32 v160, v152\n" \
"v_mfma_f32_16x16x32_bf16 v[0:3], a[104:107], a[72:75], v[0:3]\n" \
"ds_read_b64_tr_b16 a[202:203], v210 offset:3072\n" \
"v_mov_b32 v168, v152\n" \
"v_mfma_f32_16x16x32_bf16 v[0:3], a[108:111], a[76:79], v[0:3]\n" \
"s_waitcnt lgkmcnt(3)\n" \
"global_load_lds_dwordx4 v253, s[48:49] offset:0\n" \
"v_mov_b32 v176, v152\n" \
"v_mfma_f32_16x16x32_bf16 v[4:7], a[112:115], a[72:75], v[4:7]\n" \
"s_add_u32 s48, s48, 0x60000\n" \
"s_addc_u32 s49, s49, 0\n" \
"ds_read_b64_tr_b16 a[204:205], v210 offset:14336\n" \
"v_mfma_f32_16x16x32_bf16 v[4:7], a[116:119], a[76:79], v[4:7]\n" \
"ds_read_b64_tr_b16 a[206:207], v210 offset:15360\n" \
"v_sub_u32 v156, -9, v147\n" \
"v_mfma_f32_16x16x32_bf16 v[48:51], a[96:99], a[80:83], v[48:51]\n" \
"ds_read_b64_tr_b16 a[208:209], v209 offset:4096\n" \
"v_ashrrev_i32 v156, 31, v156\n" \
"v_mfma_f32_16x16x32_bf16 v[48:51], a[100:103], a[84:87], v[48:51]\n" \
"global_load_lds_dwordx4 v253, s[50:51] offset:-3072\n" \
"v_and_b32 v156, 0xff800000, v156\n" \
"v_mfma_f32_16x16x32_bf16 v[8:11], a[104:107], a[80:83], v[8:11]\n" \
"ds_read_b64_tr_b16 a[210:211], v209 offset:5120\n" \
"v_mov_b32 v164, v156\n" \
"v_mfma_f32_16x16x32_bf16 v[8:11], a[108:111], a[84:87], v[8:11]\n" \
"ds_read_b64_tr_b16 a[212:213], v209 offset:16384\n" \
"v_mov_b32 v172, v156\n" \
"v_mfma_f32_16x16x32_bf16 v[12:15], a[112:115], a[80:83], v[12:15]\n" \
"ds_read_b64_tr_b16 a[214:215], v209 offset:17408\n" \
"v_add_u32 v148, 0, v146\n" \
"v_mfma_f32_16x16x32_bf16 v[12:15], a[116:119], a[84:87], v[12:15]\n" \
"global_load_lds_dwordx4 v253, s[50:51] offset:-1024\n" \
"v_or3_b32 v148, v148, v145, s86\n" \
"v_mfma_f32_16x16x32_bf16 v[56:59], a[96:99], a[88:91], v[56:59]\n" \
"ds_read_b64_tr_b16 a[216:217], v210 offset:4096\n" \
"v_ashrrev_i32 v148, 31, v148\n" \
"v_mfma_f32_16x16x32_bf16 v[56:59], a[100:103], a[92:95], v[56:59]\n" \
"ds_read_b64_tr_b16 a[218:219], v210 offset:5120\n" \
"v_and_b32 v148, 0xff800000, v148\n" \
"v_mfma_f32_16x16x32_bf16 v[16:19], a[104:107], a[88:91], v[16:19]\n" \
"ds_read_b64_tr_b16 a[220:221], v210 offset:16384\n" \
"v_subrev_u32 v153, 1, v147\n" \
"v_mfma_f32_16x16x32_bf16 v[16:19], a[108:111], a[92:95], v[16:19]\n" \
"global_load_lds_dwordx4 v253, s[50:51] offset:1024\n" \
"v_sub_u32 v144, 8, v147\n" \
"v_mfma_f32_16x16x32_bf16 v[20:23], a[112:115], a[88:91], v[20:23]\n" \
"s_add_u32 s50, s50, 0x60000\n" \
"s_addc_u32 s51, s51, 0\n" \
"ds_read_b64_tr_b16 a[222:223], v210 offset:17408\n" \
"v_mfma_f32_16x16x32_bf16 v[20:23], a[116:119], a[92:95], v[20:23]\n" \
"s_waitcnt lgkmcnt(3)\n" \
"v_add_u32 v207, 0xffff3000, v207\n" \
"v_add_u32 v208, 0xffff3000, v208\n" \
"v_mfma_f32_16x16x32_bf16 v[64:67], a[96:99], a[192:195], v[64:67]\n" \
"v_add_u32 v205, 0xffff3000, v205\n" \
"v_add_u32 v206, 0xffff3000, v206\n" \
"v_mfma_f32_16x16x32_bf16 v[64:67], a[100:103], a[196:199], v[64:67]\n" \
"buffer_load_dword v220, v221, s[56:59], s81 offen sc1\n" \
"v_mfma_f32_16x16x32_bf16 v[24:27], a[104:107], a[192:195], v[24:27]\n" \
"v_add_u32 v209, 0xffff3000, v209\n" \
"v_add_u32 v210, 0xffff3000, v210\n" \
"v_mfma_f32_16x16x32_bf16 v[24:27], a[108:111], a[196:199], v[24:27]\n" \
"v_or_b32 v153, v153, v144\n" \
"v_ashrrev_i32 v153, 31, v153\n" \
"v_mfma_f32_16x16x32_bf16 v[28:31], a[112:115], a[192:195], v[28:31]\n" \
"s_waitcnt vmcnt(12) lgkmcnt(0)\n" \
"v_and_b32 v153, 0xff800000, v153\n" \
"v_mov_b32 v161, v153\n" \
"v_mfma_f32_16x16x32_bf16 v[28:31], a[116:119], a[196:199], v[28:31]\n" \
"s_barrier\n" \
"v_mfma_f32_16x16x32_bf16 v[72:75], a[64:67], a[208:211], v[72:75]\n" \
"ds_read_b64_tr_b16 a[96:97], v209 offset:2048\n" \
"v_mov_b32 v169, v153\n" \
"v_mfma_f32_16x16x32_bf16 v[72:75], a[68:71], a[212:215], v[72:75]\n" \
"ds_read_b64_tr_b16 a[98:99], v209 offset:3072\n" \
"v_mov_b32 v177, v153\n" \
"v_mfma_f32_16x16x32_bf16 v[44:47], a[200:203], a[72:75], v[44:47]\n" \
"s_add_u32 m0, s95, 118784\n" \
"ds_read_b128 a[64:67], v207 offset:0\n" \
"ds_read_b64_tr_b16 a[100:101], v209 offset:14336\n" \
"v_mfma_f32_16x16x32_bf16 v[44:47], a[204:207], a[76:79], v[44:47]\n" \
"global_load_lds_dwordx4 v252, s[42:43] offset:0\n" \
"v_sub_u32 v157, -8, v147\n" \
"v_mfma_f32_16x16x32_bf16 v[80:83], a[72:75], a[208:211], v[80:83]\n" \
"ds_read_b128 a[68:71], v208 offset:0\n" \
"ds_read_b64_tr_b16 a[102:103], v209 offset:15360\n" \
"v_mfma_f32_16x16x32_bf16 v[80:83], a[76:79], a[212:215], v[80:83]\n" \
"ds_read_b64_tr_b16 a[104:105], v209 offset:0\n" \
"v_ashrrev_i32 v157, 31, v157\n" \
"v_mfma_f32_16x16x32_bf16 v[84:87], a[72:75], a[216:219], v[84:87]\n" \
"ds_read_b64_tr_b16 a[106:107], v209 offset:1024\n" \
"v_and_b32 v157, 0xff800000, v157\n" \
"v_mfma_f32_16x16x32_bf16 v[84:87], a[76:79], a[220:223], v[84:87]\n" \
"global_load_lds_dwordx4 v252, s[42:43] offset:2048\n" \
"v_mov_b32 v165, v157\n" \
"v_mfma_f32_16x16x32_bf16 v[52:55], a[200:203], a[80:83], v[52:55]\n" \
"ds_read_b128 a[72:75], v205 offset:0\n" \
"v_mov_b32 v173, v157\n" \
"v_mfma_f32_16x16x32_bf16 v[52:55], a[204:207], a[84:87], v[52:55]\n" \
"ds_read_b128 a[76:79], v206 offset:0\n" \
"v_add_u32 v149, 1, v146\n" \
"v_mfma_f32_16x16x32_bf16 v[88:91], a[80:83], a[208:211], v[88:91]\n" \
"ds_read_b64_tr_b16 a[108:109], v209 offset:12288\n" \
"v_or3_b32 v149, v149, v145, s86\n" \
"v_mfma_f32_16x16x32_bf16 v[88:91], a[84:87], a[212:215], v[88:91]\n" \
"global_load_lds_dwordx4 v252, s[44:45] offset:1024\n" \
"v_ashrrev_i32 v149, 31, v149\n" \
"v_mfma_f32_16x16x32_bf16 v[92:95], a[80:83], a[216:219], v[92:95]\n" \
"ds_read_b64_tr_b16 a[110:111], v209 offset:13312\n" \
"v_and_b32 v149, 0xff800000, v149\n" \
"v_mfma_f32_16x16x32_bf16 v[92:95], a[84:87], a[220:223], v[92:95]\n" \
"ds_read_b64_tr_b16 a[112:113], v210 offset:0\n" \
"v_subrev_u32 v154, 2, v147\n" \
"v_mfma_f32_16x16x32_bf16 v[60:63], a[200:203], a[88:91], v[60:63]\n" \
"ds_read_b128 a[80:83], v205 offset:4096\n" \
"v_sub_u32 v144, 9, v147\n" \
"v_mfma_f32_16x16x32_bf16 v[60:63], a[204:207], a[92:95], v[60:63]\n" \
"global_load_lds_dwordx4 v252, s[44:45] offset:3072\n" \
"v_or_b32 v154, v154, v144\n" \
"v_mfma_f32_16x16x32_bf16 v[96:99], a[88:91], a[208:211], v[96:99]\n" \
"ds_read_b128 a[84:87], v206 offset:4096\n" \
"v_ashrrev_i32 v154, 31, v154\n" \
"v_mfma_f32_16x16x32_bf16 v[96:99], a[92:95], a[212:215], v[96:99]\n" \
"ds_read_b64_tr_b16 a[114:115], v210 offset:1024\n" \
"v_and_b32 v154, 0xff800000, v154\n" \
"v_mfma_f32_16x16x32_bf16 v[100:103], a[88:91], a[216:219], v[100:103]\n" \
"s_add_u32 m0, s85, 135168\n" \
"ds_read_b64_tr_b16 a[116:117], v210 offset:12288\n" \
"v_mov_b32 v162, v154\n" \
"v_mfma_f32_16x16x32_bf16 v[100:103], a[92:95], a[220:223], v[100:103]\n" \
"buffer_load_dwordx4 v246, s[12:15], s46 offen lds\n" \
"v_mfma_f32_16x16x32_bf16 v[68:71], a[200:203], a[192:195], v[68:71]\n" \
"ds_read_b128 a[88:91], v205 offset:8192\n" \
"v_mov_b32 v170, v154\n" \
"v_mfma_f32_16x16x32_bf16 v[68:71], a[204:207], a[196:199], v[68:71]\n" \
"ds_read_b128 a[92:95], v206 offset:8192\n" \
"v_mov_b32 v178, v154\n" \
"v_mfma_f32_16x16x32_bf16 v[104:107], a[192:195], a[208:211], v[104:107]\n" \
"s_add_u32 m0, s84, 143360\n" \
"ds_read_b64_tr_b16 a[118:119], v210 offset:13312\n" \
"v_sub_u32 v158, -7, v147\n" \
"v_mfma_f32_16x16x32_bf16 v[104:107], a[196:199], a[212:215], v[104:107]\n" \
"global_load_lds_dwordx4 v253, s[48:49] offset:-4096\n" \
"v_ashrrev_i32 v158, 31, v158\n" \
"v_mfma_f32_16x16x32_bf16 v[108:111], a[192:195], a[216:219], v[108:111]\n" \
"v_and_b32 v158, 0xff800000, v158\n" \
"v_mov_b32 v166, v158\n" \
"v_mfma_f32_16x16x32_bf16 v[108:111], a[196:199], a[220:223], v[108:111]\n" \
"s_waitcnt lgkmcnt(2)\n" \
"v_mov_b32 v174, v158\n" \
"v_add_u32 v150, 2, v146\n" \
"v_mfma_f32_16x16x32_bf16 v[32:35], a[96:99], a[64:67], v[32:35]\n" \
"ds_read_b128 a[192:195], v205 offset:12288\n" \
"v_or3_b32 v150, v150, v145, s86\n" \
"v_mfma_f32_16x16x32_bf16 v[32:35], a[100:103], a[68:71], v[32:35]\n" \
"global_load_lds_dwordx4 v253, s[48:49] offset:-2048\n" \
"v_ashrrev_i32 v150, 31, v150\n" \
"v_mfma_f32_16x16x32_bf16 v[40:43], a[96:99], a[72:75], v[40:43]\n" \
"ds_read_b128 a[196:199], v206 offset:12288\n" \
"v_and_b32 v150, 0xff800000, v150\n" \
"v_mfma_f32_16x16x32_bf16 v[40:43], a[100:103], a[76:79], v[40:43]\n" \
"ds_read_b64_tr_b16 a[200:201], v210 offset:2048\n" \
"v_subrev_u32 v155, 3, v147\n" \
"v_mfma_f32_16x16x32_bf16 v[0:3], a[104:107], a[72:75], v[0:3]\n" \
"ds_read_b64_tr_b16 a[202:203], v210 offset:3072\n" \
"v_sub_u32 v144, 10, v147\n" \
"v_mfma_f32_16x16x32_bf16 v[0:3], a[108:111], a[76:79], v[0:3]\n" \
"s_waitcnt lgkmcnt(3)\n" \
"global_load_lds_dwordx4 v253, s[48:49] offset:0\n" \
"v_or_b32 v155, v155, v144\n" \
"v_mfma_f32_16x16x32_bf16 v[4:7], a[112:115], a[72:75], v[4:7]\n" \
"ds_read_b64_tr_b16 a[204:205], v210 offset:14336\n" \
"v_ashrrev_i32 v155, 31, v155\n" \
"v_mfma_f32_16x16x32_bf16 v[4:7], a[116:119], a[76:79], v[4:7]\n" \
"ds_read_b64_tr_b16 a[206:207], v210 offset:15360\n" \
"v_and_b32 v155, 0xff800000, v155\n" \
"v_mfma_f32_16x16x32_bf16 v[48:51], a[96:99], a[80:83], v[48:51]\n" \
"ds_read_b64_tr_b16 a[208:209], v209 offset:4096\n" \
"v_mov_b32 v163, v155\n" \
"v_mfma_f32_16x16x32_bf16 v[48:51], a[100:103], a[84:87], v[48:51]\n" \
"global_load_lds_dwordx4 v253, s[50:51] offset:-3072\n" \
"v_mov_b32 v171, v155\n" \
"v_mfma_f32_16x16x32_bf16 v[8:11], a[104:107], a[80:83], v[8:11]\n" \
"ds_read_b64_tr_b16 a[210:211], v209 offset:5120\n" \
"v_mov_b32 v179, v155\n" \
"v_mfma_f32_16x16x32_bf16 v[8:11], a[108:111], a[84:87], v[8:11]\n" \
"ds_read_b64_tr_b16 a[212:213], v209 offset:16384\n" \
"v_sub_u32 v159, -6, v147\n" \
"v_mfma_f32_16x16x32_bf16 v[12:15], a[112:115], a[80:83], v[12:15]\n" \
"ds_read_b64_tr_b16 a[214:215], v209 offset:17408\n" \
"v_ashrrev_i32 v159, 31, v159\n" \
"v_mfma_f32_16x16x32_bf16 v[12:15], a[116:119], a[84:87], v[12:15]\n" \
"global_load_lds_dwordx4 v253, s[50:51] offset:-1024\n" \
"v_and_b32 v159, 0xff800000, v159\n" \
"v_mfma_f32_16x16x32_bf16 v[56:59], a[96:99], a[88:91], v[56:59]\n" \
"ds_read_b64_tr_b16 a[216:217], v210 offset:4096\n" \
"v_mov_b32 v167, v159\n" \
"v_mfma_f32_16x16x32_bf16 v[56:59], a[100:103], a[92:95], v[56:59]\n" \
"ds_read_b64_tr_b16 a[218:219], v210 offset:5120\n" \
"v_mov_b32 v175, v159\n" \
"v_mfma_f32_16x16x32_bf16 v[16:19], a[104:107], a[88:91], v[16:19]\n" \
"ds_read_b64_tr_b16 a[220:221], v210 offset:16384\n" \
"v_add_u32 v151, 3, v146\n" \
"v_mfma_f32_16x16x32_bf16 v[16:19], a[108:111], a[92:95], v[16:19]\n" \
"global_load_lds_dwordx4 v253, s[50:51] offset:1024\n" \
"v_or3_b32 v151, v151, v145, s86\n" \
"v_mfma_f32_16x16x32_bf16 v[20:23], a[112:115], a[88:91], v[20:23]\n" \
"ds_read_b64_tr_b16 a[222:223], v210 offset:17408\n" \
"v_ashrrev_i32 v151, 31, v151\n" \
"v_mfma_f32_16x16x32_bf16 v[20:23], a[116:119], a[92:95], v[20:23]\n" \
"s_waitcnt lgkmcnt(3)\n" \
"v_add_u32 v207, 0xffff0000, v207\n" \
"v_add_u32 v208, 0xffff0000, v208\n" \
"v_mfma_f32_16x16x32_bf16 v[64:67], a[96:99], a[192:195], v[64:67]\n" \
"v_add_u32 v205, 0xffff0000, v205\n" \
"v_add_u32 v206, 0xffff0000, v206\n" \
"v_mfma_f32_16x16x32_bf16 v[64:67], a[100:103], a[196:199], v[64:67]\n" \
"v_add_u32 v209, 0xffff0000, v209\n" \
"v_add_u32 v210, 0xffff0000, v210\n" \
"v_mfma_f32_16x16x32_bf16 v[24:27], a[104:107], a[192:195], v[24:27]\n" \
"v_and_b32 v151, 0xff800000, v151\n" \
"v_mov_b32 v144, 0x3e8293ee\n" \
"v_mfma_f32_16x16x32_bf16 v[24:27], a[108:111], a[196:199], v[24:27]\n" \
"s_add_u32 s90, s95, 117760\n" \
"v_add_u32 v223, s90, v248\n" \
"v_mfma_f32_16x16x32_bf16 v[28:31], a[112:115], a[192:195], v[28:31]\n" \
"s_waitcnt vmcnt(12) lgkmcnt(0)\n" \
"s_cmp_lg_u32 s89, 0\n" \
"s_cselect_b64 s[92:93], -1, 0\n" \
"v_mfma_f32_16x16x32_bf16 v[28:31], a[116:119], a[196:199], v[28:31]\n" \
"s_barrier\n" \
"v_mfma_f32_16x16x32_bf16 v[72:75], a[64:67], a[208:211], v[72:75]\n" \
"s_waitcnt vmcnt(11)\n" \
"ds_read_b64_tr_b16 a[96:97], v209 offset:2048\n" \
"v_cmp_ne_u32 vcc, s60, v220\n" \
"v_mfma_f32_16x16x32_bf16 v[72:75], a[68:71], a[212:215], v[72:75]\n" \
"ds_read_b64_tr_b16 a[98:99], v209 offset:3072\n" \
"v_xor_b32 v145, 0x2000, v223\n" \
"v_mfma_f32_16x16x32_bf16 v[44:47], a[200:203], a[72:75], v[44:47]\n" \
"ds_read_b128 a[64:67], v207 offset:0\n" \
"ds_read_b64_tr_b16 a[100:101], v209 offset:14336\n" \
"v_mfma_f32_16x16x32_bf16 v[44:47], a[204:207], a[76:79], v[44:47]\n" \
"s_and_b32 vcc_hi, vcc_lo, s80\n" \
"s_cbranch_scc1 Lpl2_%=\n" \
"Lpr2_%=:\n" \
"v_mfma_f32_16x16x32_bf16 v[80:83], a[72:75], a[208:211], v[80:83]\n" \
"s_add_u32 m0, s75, s95\n" \
"ds_read_b128 a[68:71], v208 offset:0\n" \
"ds_read_b64_tr_b16 a[102:103], v209 offset:15360\n" \
"v_mfma_f32_16x16x32_bf16 v[80:83], a[76:79], a[212:215], v[80:83]\n" \
"ds_read_b64_tr_b16 a[104:105], v209 offset:0\n" \
"global_load_lds_dwordx4 v222, s[36:37] offset:0 sc1\n" \
"v_mfma_f32_16x16x32_bf16 v[84:87], a[72:75], a[216:219], v[84:87]\n" \
"s_cmp_eq_u32 s89, 0\n" \
"s_cselect_b32 s16, 0, 0xff00\n" \
"ds_read_b64_tr_b16 a[106:107], v209 offset:1024\n" \
"v_mfma_f32_16x16x32_bf16 v[84:87], a[76:79], a[220:223], v[84:87]\n" \
"s_mov_b32 s17, s16\n" \
"v_add_u32 v146, 0x1c90080, v249\n" \
"v_cndmask_b32 v146, v249, v146, s[16:17]\n" \
"v_mfma_f32_16x16x32_bf16 v[52:55], a[200:203], a[80:83], v[52:55]\n" \
"ds_read_b128 a[72:75], v205 offset:0\n" \
"v_mfma_f32_16x16x32_bf16 v[52:55], a[204:207], a[84:87], v[52:55]\n" \
"ds_read_b128 a[76:79], v206 offset:0\n" \
"global_load_lds_dwordx4 v222, s[36:37] offset:1024 sc1\n" \
"v_mfma_f32_16x16x32_bf16 v[88:91], a[80:83], a[208:211], v[88:91]\n" \
"ds_read_b64_tr_b16 a[108:109], v209 offset:12288\n" \
"v_mfma_f32_16x16x32_bf16 v[88:91], a[84:87], a[212:215], v[88:91]\n" \
"v_mfma_f32_16x16x32_bf16 v[92:95], a[80:83], a[216:219], v[92:95]\n" \
"ds_read_b64_tr_b16 a[110:111], v209 offset:13312\n" \
"v_mfma_f32_16x16x32_bf16 v[92:95], a[84:87], a[220:223], v[92:95]\n" \
"ds_read_b64_tr_b16 a[112:113], v210 offset:0\n" \
"global_load_lds_dwordx4 v222, s[36:37] offset:2048 sc1\n" \
"v_mfma_f32_16x16x32_bf16 v[60:63], a[200:203], a[88:91], v[60:63]\n" \
"ds_read_b128 a[80:83], v205 offset:4096\n" \
"v_mfma_f32_16x16x32_bf16 v[60:63], a[204:207], a[92:95], v[60:63]\n" \
"v_mfma_f32_16x16x32_bf16 v[96:99], a[88:91], a[208:211], v[96:99]\n" \
"ds_read_b128 a[84:87], v206 offset:4096\n" \
"v_mfma_f32_16x16x32_bf16 v[96:99], a[92:95], a[212:215], v[96:99]\n" \
"ds_read_b64_tr_b16 a[114:115], v210 offset:1024\n" \
"global_load_lds_dwordx4 v222, s[36:37] offset:3072 sc1\n" \
"v_mfma_f32_16x16x32_bf16 v[100:103], a[88:91], a[216:219], v[100:103]\n" \
"s_add_u32 s36, s36, 0x1000\n" \
"ds_read_b64_tr_b16 a[116:117], v210 offset:12288\n" \
"v_mfma_f32_16x16x32_bf16 v[100:103], a[92:95], a[220:223], v[100:103]\n" \
"v_mfma_f32_16x16x32_bf16 v[68:71], a[200:203], a[192:195], v[68:71]\n" \
"ds_read_b128 a[88:91], v205 offset:8192\n" \
"s_add_u32 m0, m0, 0x5000\n" \
"v_mfma_f32_16x16x32_bf16 v[68:71], a[204:207], a[196:199], v[68:71]\n" \
"ds_read_b128 a[92:95], v206 offset:8192\n" \
"global_load_lds_dwordx4 v216, s[38:39] offset:-4096\n" \
"v_mfma_f32_16x16x32_bf16 v[104:107], a[192:195], a[208:211], v[104:107]\n" \
"ds_read_b64_tr_b16 a[118:119], v210 offset:13312\n" \
"v_mfma_f32_16x16x32_bf16 v[104:107], a[196:199], a[212:215], v[104:107]\n" \
"v_mfma_f32_16x16x32_bf16 v[108:111], a[192:195], a[216:219], v[108:111]\n" \
"v_mfma_f32_16x16x32_bf16 v[108:111], a[196:199], a[220:223], v[108:111]\n" \
"s_waitcnt lgkmcnt(2)\n" \
"global_load_lds_dwordx4 v217, s[38:39] offset:-3072\n" \
"v_mfma_f32_16x16x32_bf16 v[32:35], a[96:99], a[64:67], v[32:35]\n" \
"ds_read_b128 a[192:195], v205 offset:12288\n" \
"v_mfma_f32_16x16x32_bf16 v[32:35], a[100:103], a[68:71], v[32:35]\n" \
"v_mfma_f32_16x16x32_bf16 v[40:43], a[96:99], a[72:75], v[40:43]\n" \
"ds_read_b128 a[196:199], v206 offset:12288\n" \
"v_mfma_f32_16x16x32_bf16 v[40:43], a[100:103], a[76:79], v[40:43]\n" \
"ds_read_b64_tr_b16 a[200:201], v210 offset:2048\n" \
"global_load_lds_dwordx4 v218, s[38:39] offset:-2048\n" \
"Lpg11019_%=:\n" \
"s_cbranch_execz Lpg12707_%=\n" \
"v_mfma_f32_16x16x32_bf16 v[0:3], a[104:107], a[72:75], v[0:3]\n" \
"ds_read_b64_tr_b16 a[202:203], v210 offset:3072\n" \
"v_mfma_f32_16x16x32_bf16 v[0:3], a[108:111], a[76:79], v[0:3]\n" \
"s_waitcnt lgkmcnt(3)\n" \
"v_mfma_f32_16x16x32_bf16 v[4:7], a[112:115], a[72:75], v[4:7]\n" \
"ds_read_b64_tr_b16 a[204:205], v210 offset:14336\n" \
"v_mfma_f32_16x16x32_bf16 v[4:7], a[116:119], a[76:79], v[4:7]\n" \
"ds_read_b64_tr_b16 a[206:207], v210 offset:15360\n" \
"global_load_lds_dwordx4 v219, s[38:39] offset:-1024\n" \
"v_mfma_f32_16x16x32_bf16 v[48:51], a[96:99], a[80:83], v[48:51]\n" \
"s_add_u32 s38, s38, 0x20000\n" \
"s_addc_u32 s39, s39, 0\n" \
"ds_read_b64_tr_b16 a[208:209], v209 offset:4096\n" \
"v_mfma_f32_16x16x32_bf16 v[48:51], a[100:103], a[84:87], v[48:51]\n" \
"s_sub_u32 s75, s75, 32768\n" \
"s_cselect_b32 s75, 131072, s75\n" \
"v_mfma_f32_16x16x32_bf16 v[8:11], a[104:107], a[80:83], v[8:11]\n" \
"ds_read_b64_tr_b16 a[210:211], v209 offset:5120\n" \
"v_mfma_f32_16x16x32_bf16 v[8:11], a[108:111], a[84:87], v[8:11]\n" \
"ds_read_b64_tr_b16 a[212:213], v209 offset:16384\n" \
"v_mfma_f32_16x16x32_bf16 v[12:15], a[112:115], a[80:83], v[12:15]\n" \
"ds_read_b64_tr_b16 a[214:215], v209 offset:17408\n" \
"v_mfma_f32_16x16x32_bf16 v[12:15], a[116:119], a[84:87], v[12:15]\n" \
"v_mfma_f32_16x16x32_bf16 v[56:59], a[96:99], a[88:91], v[56:59]\n" \
"ds_read_b64_tr_b16 a[216:217], v210 offset:4096\n" \
"v_mfma_f32_16x16x32_bf16 v[56:59], a[100:103], a[92:95], v[56:59]\n" \
"ds_read_b64_tr_b16 a[218:219], v210 offset:5120\n" \
"v_mfma_f32_16x16x32_bf16 v[16:19], a[104:107], a[88:91], v[16:19]\n" \
"ds_read_b64_tr_b16 a[220:221], v210 offset:16384\n" \
"v_mfma_f32_16x16x32_bf16 v[16:19], a[108:111], a[92:95], v[16:19]\n" \
"v_mfma_f32_16x16x32_bf16 v[20:23], a[112:115], a[88:91], v[20:23]\n" \
"ds_read_b64_tr_b16 a[222:223], v210 offset:17408\n" \
"v_mfma_f32_16x16x32_bf16 v[20:23], a[116:119], a[92:95], v[20:23]\n" \
"s_waitcnt lgkmcnt(3)\n" \
"v_add_u32 v207, 0x1d000, v207\n" \
"v_add_u32 v208, 0x1d000, v208\n" \
"v_mfma_f32_16x16x32_bf16 v[64:67], a[96:99], a[192:195], v[64:67]\n" \
"v_add_u32 v205, 0x1d000, v205\n" \
"v_add_u32 v206, 0x1d000, v206\n" \
"v_mfma_f32_16x16x32_bf16 v[64:67], a[100:103], a[196:199], v[64:67]\n" \
"v_add_u32 v209, 0x1d000, v209\n" \
"v_add_u32 v210, 0x1d000, v210\n" \
"v_mfma_f32_16x16x32_bf16 v[24:27], a[104:107], a[192:195], v[24:27]\n" \
"v_mfma_f32_16x16x32_bf16 v[24:27], a[108:111], a[196:199], v[24:27]\n" \
"v_mfma_f32_16x16x32_bf16 v[28:31], a[112:115], a[192:195], v[28:31]\n" \
"s_waitcnt vmcnt(8) lgkmcnt(0)\n" \
"v_mfma_f32_16x16x32_bf16 v[28:31], a[116:119], a[196:199], v[28:31]\n" \
"s_barrier\n" \
"v_mfma_f32_16x16x32_bf16 v[72:75], a[64:67], a[208:211], v[72:75]\n" \
"ds_read_b64_tr_b16 a[96:97], v209 offset:2048\n" \
"v_mfma_f32_16x16x32_bf16 v[72:75], a[68:71], a[212:215], v[72:75]\n" \
"ds_read_b64_tr_b16 a[98:99], v209 offset:3072\n" \
"v_mfma_f32_16x16x32_bf16 v[44:47], a[200:203], a[72:75], v[44:47]\n" \
"s_add_u32 m0, s75, s95\n" \
"ds_read_b128 a[64:67], v207 offset:0\n" \
"ds_read_b64_tr_b16 a[100:101], v209 offset:14336\n" \
"v_mfma_f32_16x16x32_bf16 v[44:47], a[204:207], a[76:79], v[44:47]\n" \
"global_load_lds_dwordx4 v222, s[36:37] offset:0 sc1\n" \
"v_mfma_f32_16x16x32_bf16 v[80:83], a[72:75], a[208:211], v[80:83]\n" \
"ds_read_b128 a[68:71], v208 offset:0\n" \
"ds_read_b64_tr_b16 a[102:103], v209 offset:15360\n" \
"v_mfma_f32_16x16x32_bf16 v[80:83], a[76:79], a[212:215], v[80:83]\n" \
"ds_read_b64_tr_b16 a[104:105], v209 offset:0\n" \
"v_mfma_f32_16x16x32_bf16 v[84:87], a[72:75], a[216:219], v[84:87]\n" \
"ds_read_b64_tr_b16 a[106:107], v209 offset:1024\n" \
"v_mfma_f32_16x16x32_bf16 v[84:87], a[76:79], a[220:223], v[84:87]\n" \
"global_load_lds_dwordx4 v222, s[36:37] offset:1024 sc1\n" \
"v_mfma_f32_16x16x32_bf16 v[52:55], a[200:203], a[80:83], v[52:55]\n" \
"ds_read_b128 a[72:75], v205 offset:0\n" \
"v_mfma_f32_16x16x32_bf16 v[52:55], a[204:207], a[84:87], v[52:55]\n" \
"ds_read_b128 a[76:79], v206 offset:0\n" \
"v_mfma_f32_16x16x32_bf16 v[88:91], a[80:83], a[208:211], v[88:91]\n" \
"ds_read_b64_tr_b16 a[108:109], v209 offset:12288\n" \
"v_mfma_f32_16x16x32_bf16 v[88:91], a[84:87], a[212:215], v[88:91]\n" \
"global_load_lds_dwordx4 v222, s[36:37] offset:2048 sc1\n" \
"v_mfma_f32_16x16x32_bf16 v[92:95], a[80:83], a[216:219], v[92:95]\n" \
"ds_read_b64_tr_b16 a[110:111], v209 offset:13312\n" \
"v_mfma_f32_16x16x32_bf16 v[92:95], a[84:87], a[220:223], v[92:95]\n" \
"ds_read_b64_tr_b16 a[112:113], v210 offset:0\n" \
"v_mfma_f32_16x16x32_bf16 v[60:63], a[200:203], a[88:91], v[60:63]\n" \
"ds_read_b128 a[80:83], v205 offset:4096\n" \
"v_mfma_f32_16x16x32_bf16 v[60:63], a[204:207], a[92:95], v[60:63]\n" \
"global_load_lds_dwordx4 v222, s[36:37] offset:3072 sc1\n" \
"v_mfma_f32_16x16x32_bf16 v[96:99], a[88:91], a[208:211], v[96:99]\n" \
"ds_read_b128 a[84:87], v206 offset:4096\n" \
"s_add_u32 s36, s36, 0x1000\n" \
"v_mfma_f32_16x16x32_bf16 v[96:99], a[92:95], a[212:215], v[96:99]\n" \
"ds_read_b64_tr_b16 a[114:115], v210 offset:1024\n" \
"v_mfma_f32_16x16x32_bf16 v[100:103], a[88:91], a[216:219], v[100:103]\n" \
"s_add_u32 m0, m0, 0x5000\n" \
"ds_read_b64_tr_b16 a[116:117], v210 offset:12288\n" \
"v_mfma_f32_16x16x32_bf16 v[100:103], a[92:95], a[220:223], v[100:103]\n" \
"global_load_lds_dwordx4 v216, s[38:39] offset:-4096\n" \
"v_mfma_f32_16x16x32_bf16 v[68:71], a[200:203], a[192:195], v[68:71]\n" \
"ds_read_b128 a[88:91], v205 offset:8192\n" \
"v_mfma_f32_16x16x32_bf16 v[68:71], a[204:207], a[196:199], v[68:71]\n" \
"ds_read_b128 a[92:95], v206 offset:8192\n" \
"v_mfma_f32_16x16x32_bf16 v[104:107], a[192:195], a[208:211], v[104:107]\n" \
"ds_read_b64_tr_b16 a[118:119], v210 offset:13312\n" \
"v_mfma_f32_16x16x32_bf16 v[104:107], a[196:199], a[212:215], v[104:107]\n" \
"global_load_lds_dwordx4 v217, s[38:39] offset:-3072\n" \
"v_mfma_f32_16x16x32_bf16 v[108:111], a[192:195], a[216:219], v[108:111]\n" \
"v_mfma_f32_16x16x32_bf16 v[108:111], a[196:199], a[220:223], v[108:111]\n" \
"s_waitcnt lgkmcnt(2)\n" \
"v_mfma_f32_16x16x32_bf16 v[32:35], a[96:99], a[64:67], v[32:35]\n" \
"ds_read_b128 a[192:195], v205 offset:12288\n" \
"v_mfma_f32_16x16x32_bf16 v[32:35], a[100:103], a[68:71], v[32:35]\n" \
"global_load_lds_dwordx4 v218, s[38:39] offset:-2048\n" \
"v_mfma_f32_16x16x32_bf16 v[40:43], a[96:99], a[72:75], v[40:43]\n" \
"ds_read_b128 a[196:199], v206 offset:12288\n" \
"v_mfma_f32_16x16x32_bf16 v[40:43], a[100:103], a[76:79], v[40:43]\n" \
"ds_read_b64_tr_b16 a[200:201], v210 offset:2048\n" \
"v_mfma_f32_16x16x32_bf16 v[0:3], a[104:107], a[72:75], v[0:3]\n" \
"ds_read_b64_tr_b16 a[202:203], v210 offset:3072\n" \
"v_mfma_f32_16x16x32_bf16 v[0:3], a[108:111], a[76:79], v[0:3]\n" \
"s_waitcnt lgkmcnt(3)\n" \
"global_load_lds_dwordx4 v219, s[38:39] offset:-1024\n" \
"v_mfma_f32_16x16x32_bf16 v[4:7], a[112:115], a[72:75], v[4:7]\n" \
"s_add_u32 s38, s38, 0x20000\n" \
"s_addc_u32 s39, s39, 0\n" \
"ds_read_b64_tr_b16 a[204:205], v210 offset:14336\n" \
"v_mfma_f32_16x16x32_bf16 v[4:7], a[116:119], a[76:79], v[4:7]\n" \
"s_sub_u32 s75, s75, 32768\n" \
"s_cselect_b32 s75, 131072, s75\n" \
"ds_read_b64_tr_b16 a[206:207], v210 offset:15360\n" \
"v_mfma_f32_16x16x32_bf16 v[48:51], a[96:99], a[80:83], v[48:51]\n" \
"s_add_u32 m0, s75, s95\n" \
"ds_read_b64_tr_b16 a[208:209], v209 offset:4096\n" \
"v_mfma_f32_16x16x32_bf16 v[48:51], a[100:103], a[84:87], v[48:51]\n" \
"global_load_lds_dwordx4 v222, s[36:37] offset:0 sc1\n" \
"v_mfma_f32_16x16x32_bf16 v[8:11], a[104:107], a[80:83], v[8:11]\n" \
"ds_read_b64_tr_b16 a[210:211], v209 offset:5120\n" \
"v_cvt_pk_bf16_f32 v112, v0, v1\n" \
"v_mfma_f32_16x16x32_bf16 v[8:11], a[108:111], a[84:87], v[8:11]\n" \
"ds_read_b64_tr_b16 a[212:213], v209 offset:16384\n" \
"v_cvt_pk_bf16_f32 v113, v2, v3\n" \
"v_mfma_f32_16x16x32_bf16 v[12:15], a[112:115], a[80:83], v[12:15]\n" \
"ds_read_b64_tr_b16 a[214:215], v209 offset:17408\n" \
"v_cvt_pk_bf16_f32 v114, v4, v5\n" \
"v_mfma_f32_16x16x32_bf16 v[12:15], a[116:119], a[84:87], v[12:15]\n" \
"global_load_lds_dwordx4 v222, s[36:37] offset:1024 sc1\n" \
"v_cvt_pk_bf16_f32 v115, v6, v7\n" \
"v_mfma_f32_16x16x32_bf16 v[56:59], a[96:99], a[88:91], v[56:59]\n" \
"ds_read_b64_tr_b16 a[216:217], v210 offset:4096\n" \
"v_mfma_f32_16x16x32_bf16 v[56:59], a[100:103], a[92:95], v[56:59]\n" \
"ds_read_b64_tr_b16 a[218:219], v210 offset:5120\n" \
"v_mfma_f32_16x16x32_bf16 v[16:19], a[104:107], a[88:91], v[16:19]\n" \
"ds_read_b64_tr_b16 a[220:221], v210 offset:16384\n" \
"v_cvt_pk_bf16_f32 v116, v8, v9\n" \
"v_mfma_f32_16x16x32_bf16 v[16:19], a[108:111], a[92:95], v[16:19]\n" \
"global_load_lds_dwordx4 v222, s[36:37] offset:2048 sc1\n" \
"v_cvt_pk_bf16_f32 v117, v10, v11\n" \
"v_mfma_f32_16x16x32_bf16 v[20:23], a[112:115], a[88:91], v[20:23]\n" \
"ds_read_b64_tr_b16 a[222:223], v210 offset:17408\n" \
"v_cvt_pk_bf16_f32 v118, v12, v13\n" \
"v_mfma_f32_16x16x32_bf16 v[20:23], a[116:119], a[92:95], v[20:23]\n" \
"s_waitcnt lgkmcnt(3)\n" \
"v_cvt_pk_bf16_f32 v119, v14, v15\n" \
"v_mfma_f32_16x16x32_bf16 v[64:67], a[96:99], a[192:195], v[64:67]\n" \
"v_mfma_f32_16x16x32_bf16 v[64:67], a[100:103], a[196:199], v[64:67]\n" \
"global_load_lds_dwordx4 v222, s[36:37] offset:3072 sc1\n" \
"v_mfma_f32_16x16x32_bf16 v[24:27], a[104:107], a[192:195], v[24:27]\n" \
"v_cvt_pk_bf16_f32 v120, v16, v17\n" \
"v_cvt_pk_bf16_f32 v121, v18, v19\n" \
"v_mfma_f32_16x16x32_bf16 v[24:27], a[108:111], a[196:199], v[24:27]\n" \
"v_cvt_pk_bf16_f32 v122, v20, v21\n" \
"v_cvt_pk_bf16_f32 v123, v22, v23\n" \
"v_mfma_f32_16x16x32_bf16 v[28:31], a[112:115], a[192:195], v[28:31]\n" \
"s_waitcnt vmcnt(12) lgkmcnt(0)\n" \
"v_mfma_f32_16x16x32_bf16 v[28:31], a[116:119], a[196:199], v[28:31]\n" \
"s_barrier\n" \
"v_mfma_f32_16x16x32_bf16 v[72:75], a[64:67], a[208:211], v[72:75]\n" \
"ds_read_b64_tr_b16 a[32:33], v214 offset:16384\n" \
"v_mfma_f32_16x16x32_bf16 v[72:75], a[68:71], a[212:215], v[72:75]\n" \
"v_mfma_f32_16x16x32_bf16 v[44:47], a[200:203], a[72:75], v[44:47]\n" \
"s_add_u32 s36, s36, 0x1000\n" \
"s_add_u32 m0, m0, 0x5000\n" \
"ds_read_b64_tr_b16 a[34:35], v214 offset:18432\n" \
"v_mfma_f32_16x16x32_bf16 v[44:47], a[204:207], a[76:79], v[44:47]\n" \
"ds_read_b64_tr_b16 a[36:37], v214 offset:24576\n" \
"global_load_lds_dwordx4 v216, s[38:39] offset:-4096\n" \
"v_mfma_f32_16x16x32_bf16 v[80:83], a[72:75], a[208:211], v[80:83]\n" \
"v_cvt_pk_bf16_f32 v124, v24, v25\n" \
"v_cvt_pk_bf16_f32 v125, v26, v27\n" \
"v_mfma_f32_16x16x32_bf16 v[80:83], a[76:79], a[212:215], v[80:83]\n" \
"ds_read_b64_tr_b16 a[38:39], v214 offset:26624\n" \
"v_cvt_pk_bf16_f32 v126, v28, v29\n" \
"v_mfma_f32_16x16x32_bf16 v[84:87], a[72:75], a[216:219], v[84:87]\n" \
"ds_read_b128 a[0:3], v213 offset:0\n" \
"v_cvt_pk_bf16_f32 v127, v30, v31\n" \
"v_mfma_f32_16x16x32_bf16 v[84:87], a[76:79], a[220:223], v[84:87]\n" \
"global_load_lds_dwordx4 v217, s[38:39] offset:-3072\n" \
"v_cvt_pk_bf16_f32 v128, v32, v33\n" \
"v_mfma_f32_16x16x32_bf16 v[52:55], a[200:203], a[80:83], v[52:55]\n" \
"ds_read_b128 a[4:7], v213 offset:2048\n" \
"v_cvt_pk_bf16_f32 v129, v34, v35\n" \
"v_mfma_f32_16x16x32_bf16 v[52:55], a[204:207], a[84:87], v[52:55]\n" \
"ds_read_b64_tr_b16 a[40:41], v215 offset:16384\n" \
"v_cvt_pk_bf16_f32 v130, v72, v73\n" \
"v_mfma_f32_16x16x32_bf16 v[88:91], a[80:83], a[208:211], v[88:91]\n" \
"v_cvt_pk_bf16_f32 v131, v74, v75\n" \
"v_mfma_f32_16x16x32_bf16 v[88:91], a[84:87], a[212:215], v[88:91]\n" \
"ds_read_b64_tr_b16 a[42:43], v215 offset:18432\n" \
"global_load_lds_dwordx4 v218, s[38:39] offset:-2048\n" \
"v_mfma_f32_16x16x32_bf16 v[92:95], a[80:83], a[216:219], v[92:95]\n" \
"ds_read_b64_tr_b16 a[44:45], v215 offset:24576\n" \
"v_mfma_f32_16x16x32_bf16 v[92:95], a[84:87], a[220:223], v[92:95]\n" \
"ds_write_b128 v145, v[128:131]\n" \
"v_mfma_f32_16x16x32_bf16 v[60:63], a[200:203], a[88:91], v[60:63]\n" \
"ds_read_b64_tr_b16 a[46:47], v215 offset:26624\n" \
"v_cvt_pk_bf16_f32 v132, v40, v41\n" \
"v_mfma_f32_16x16x32_bf16 v[60:63], a[204:207], a[92:95], v[60:63]\n" \
"ds_read_b128 a[8:11], v213 offset:256\n" \
"global_load_lds_dwordx4 v219, s[38:39] offset:-1024\n" \
"v_mfma_f32_16x16x32_bf16 v[96:99], a[88:91], a[208:211], v[96:99]\n" \
"s_add_u32 s38, s38, 0x20000\n" \
"s_addc_u32 s39, s39, 0\n" \
"v_cvt_pk_bf16_f32 v133, v42, v43\n" \
"v_mfma_f32_16x16x32_bf16 v[96:99], a[92:95], a[212:215], v[96:99]\n" \
"s_sub_u32 s75, s75, 32768\n" \
"s_cselect_b32 s75, 131072, s75\n" \
"ds_read_b128 a[12:15], v213 offset:2304\n" \
"v_mfma_f32_16x16x32_bf16 v[100:103], a[88:91], a[216:219], v[100:103]\n" \
"s_add_u32 m0, s75, s95\n" \
"ds_read_b128 a[16:19], v213 offset:4096\n" \
"v_cvt_pk_bf16_f32 v134, v44, v45\n" \
"v_mfma_f32_16x16x32_bf16 v[100:103], a[92:95], a[220:223], v[100:103]\n" \
"global_load_lds_dwordx4 v222, s[36:37] offset:0 sc1\n" \
"v_cvt_pk_bf16_f32 v135, v46, v47\n" \
"v_mfma_f32_16x16x32_bf16 v[68:71], a[200:203], a[192:195], v[68:71]\n" \
"ds_read_b128 a[20:23], v213 offset:6144\n" \
"v_cvt_pk_bf16_f32 v136, v48, v49\n" \
"v_mfma_f32_16x16x32_bf16 v[68:71], a[204:207], a[196:199], v[68:71]\n" \
"v_cvt_pk_bf16_f32 v137, v50, v51\n" \
"v_cvt_pk_bf16_f32 v138, v52, v53\n" \
"v_mfma_f32_16x16x32_bf16 v[104:107], a[192:195], a[208:211], v[104:107]\n" \
"v_cvt_pk_bf16_f32 v139, v54, v55\n" \
"v_cvt_pk_bf16_f32 v140, v56, v57\n" \
"v_mfma_f32_16x16x32_bf16 v[104:107], a[196:199], a[212:215], v[104:107]\n" \
"global_load_lds_dwordx4 v222, s[36:37] offset:1024 sc1\n" \
"v_cvt_pk_bf16_f32 v141, v58, v59\n" \
"v_mfma_f32_16x16x32_bf16 v[108:111], a[192:195], a[216:219], v[108:111]\n" \
"v_cvt_pk_bf16_f32 v142, v60, v61\n" \
"v_cvt_pk_bf16_f32 v143, v62, v63\n" \
"v_mfma_f32_16x16x32_bf16 v[108:111], a[196:199], a[220:223], v[108:111]\n" \
"s_waitcnt lgkmcnt(0)\n" \
"v_mfma_f32_16x16x32_bf16 v[0:3], a[32:35], a[0:3], 0\n" \
"ds_read_b128 a[24:27], v213 offset:4352\n" \
"v_mfma_f32_16x16x32_bf16 v[0:3], a[36:39], a[4:7], v[0:3]\n" \
"ds_read_b128 a[28:31], v213 offset:6400\n" \
"global_load_lds_dwordx4 v222, s[36:37] offset:2048 sc1\n" \
"v_mfma_f32_16x16x32_bf16 v[4:7], a[40:43], a[0:3], 0\n" \
"s_sub_u32 s74, s74, 32768\n" \
"s_cselect_b32 s74, 131072, s74\n" \
"ds_read_b64_tr_b16 a[48:49], v214 offset:16896\n" \
"v_mfma_f32_16x16x32_bf16 v[4:7], a[44:47], a[4:7], v[4:7]\n" \
"ds_read_b64_tr_b16 a[50:51], v214 offset:18944\n" \
"v_cvt_pk_bf16_f32 v64, v64, v65\n" \
"v_mfma_f32_16x16x32_bf16 v[16:19], a[32:35], a[8:11], 0\n" \
"ds_read_b64_tr_b16 a[52:53], v214 offset:25088\n" \
"global_load_lds_dwordx4 v222, s[36:37] offset:3072 sc1\n" \
"v_mfma_f32_16x16x32_bf16 v[16:19], a[36:39], a[12:15], v[16:19]\n" \
"s_add_u32 s36, s36, 0x1000\n" \
"ds_read_b64_tr_b16 a[54:55], v214 offset:27136\n" \
"v_cvt_pk_bf16_f32 v65, v66, v67\n" \
"v_mfma_f32_16x16x32_bf16 v[20:23], a[40:43], a[8:11], 0\n" \
"s_add_u32 m0, m0, 0x5000\n" \
"ds_read_b64_tr_b16 a[56:57], v215 offset:16896\n" \
"v_cvt_pk_bf16_f32 v66, v68, v69\n" \
"v_mfma_f32_16x16x32_bf16 v[20:23], a[44:47], a[12:15], v[20:23]\n" \
"ds_read_b64_tr_b16 a[58:59], v215 offset:18944\n" \
"global_load_lds_dwordx4 v216, s[38:39] offset:-4096\n" \
"v_mfma_f32_16x16x32_bf16 v[32:35], a[32:35], a[16:19], 0\n" \
"ds_read_b64_tr_b16 a[60:61], v215 offset:25088\n" \
"v_cvt_pk_bf16_f32 v67, v70, v71\n" \
"v_mfma_f32_16x16x32_bf16 v[32:35], a[36:39], a[20:23], v[32:35]\n" \
"ds_read_b64_tr_b16 a[62:63], v215 offset:27136\n" \
"v_cvt_pk_bf16_f32 v68, v72, v73\n" \
"v_mfma_f32_16x16x32_bf16 v[36:39], a[40:43], a[16:19], 0\n" \
"global_load_lds_dwordx4 v217, s[38:39] offset:-3072\n" \
"v_add_u32 v213, s74, v211\n" \
"v_mfma_f32_16x16x32_bf16 v[36:39], a[44:47], a[20:23], v[36:39]\n" \
"s_waitcnt lgkmcnt(3)\n" \
"v_add_u32 v214, s74, v212\n" \
"v_xor_b32 v215, 32, v214\n" \
"v_mfma_f32_16x16x32_bf16 v[48:51], a[32:35], a[24:27], 0\n" \
"v_cvt_pk_bf16_f32 v69, v74, v75\n" \
"v_cvt_pk_bf16_f32 v70, v80, v81\n" \
"v_mfma_f32_16x16x32_bf16 v[48:51], a[36:39], a[28:31], v[48:51]\n" \
"global_load_lds_dwordx4 v218, s[38:39] offset:-2048\n" \
"v_cvt_pk_bf16_f32 v71, v82, v83\n" \
"v_mfma_f32_16x16x32_bf16 v[52:55], a[40:43], a[24:27], 0\n" \
"s_waitcnt vmcnt(15) lgkmcnt(0)\n" \
"global_load_lds_dwordx4 v219, s[38:39] offset:-1024\n" \
"v_cvt_pk_bf16_f32 v194, v84, v85\n" \
"v_mfma_f32_16x16x32_bf16 v[52:55], a[44:47], a[28:31], v[52:55]\n" \
"s_barrier\n" \
"v_cvt_pk_bf16_f32 v195, v86, v87\n" \
"v_mfma_f32_16x16x32_bf16 v[8:11], a[48:51], a[0:3], 0\n" \
"ds_read_b64 v[130:131], v223\n" \
"ds_read_b64_tr_b16 a[32:33], v214 offset:16384\n" \
"v_mfma_f32_16x16x32_bf16 v[8:11], a[52:55], a[4:7], v[8:11]\n" \
"ds_read_b64 v[192:193], v223 offset:8\n" \
"ds_read_b64_tr_b16 a[34:35], v214 offset:18432\n" \
"v_mfma_f32_16x16x32_bf16 v[12:15], a[56:59], a[0:3], 0\n" \
"s_add_u32 s38, s38, 0x20000\n" \
"s_addc_u32 s39, s39, 0\n" \
"ds_read_b64_tr_b16 a[36:37], v214 offset:24576\n" \
"v_mfma_f32_16x16x32_bf16 v[12:15], a[60:63], a[4:7], v[12:15]\n" \
"s_sub_u32 s75, s75, 32768\n" \
"s_cselect_b32 s75, 131072, s75\n" \
"ds_read_b128 a[0:3], v213 offset:0\n" \
"v_mfma_f32_16x16x32_bf16 v[24:27], a[48:51], a[8:11], 0\n" \
"s_add_u32 m0, s75, s95\n" \
"ds_read_b128 a[4:7], v213 offset:2048\n" \
"v_cvt_pk_bf16_f32 v72, v88, v89\n" \
"v_mfma_f32_16x16x32_bf16 v[24:27], a[52:55], a[12:15], v[24:27]\n" \
"ds_read_b64_tr_b16 a[38:39], v214 offset:26624\n" \
"global_load_lds_dwordx4 v222, s[36:37] offset:0 sc1\n" \
"v_mfma_f32_16x16x32_bf16 v[28:31], a[56:59], a[8:11], 0\n" \
"ds_read_b64_tr_b16 a[40:41], v215 offset:16384\n" \
"v_cvt_pk_bf16_f32 v73, v90, v91\n" \
"v_mfma_f32_16x16x32_bf16 v[28:31], a[60:63], a[12:15], v[28:31]\n" \
"s_waitcnt lgkmcnt(4)\n" \
"ds_read_b128 a[8:11], v213 offset:256\n" \
"global_load_lds_dwordx4 v222, s[36:37] offset:1024 sc1\n" \
"v_mfma_f32_16x16x32_bf16 v[40:43], a[48:51], a[16:19], 0\n" \
"ds_read_b128 a[12:15], v213 offset:2304\n" \
"v_cvt_pk_bf16_f32 v196, v92, v93\n" \
"v_mfma_f32_16x16x32_bf16 v[40:43], a[52:55], a[20:23], v[40:43]\n" \
"ds_read_b64_tr_b16 a[42:43], v215 offset:18432\n" \
"v_cvt_pk_bf16_f32 v197, v94, v95\n" \
"v_mfma_f32_16x16x32_bf16 v[44:47], a[56:59], a[16:19], 0\n" \
"ds_read_b64_tr_b16 a[44:45], v215 offset:24576\n" \
"global_load_lds_dwordx4 v222, s[36:37] offset:2048 sc1\n" \
"v_mfma_f32_16x16x32_bf16 v[44:47], a[60:63], a[20:23], v[44:47]\n" \
"ds_read_b128 a[16:19], v213 offset:4096\n" \
"v_cvt_pk_bf16_f32 v74, v96, v97\n" \
"v_mfma_f32_16x16x32_bf16 v[56:59], a[48:51], a[24:27], 0\n" \
"ds_read_b128 a[20:23], v213 offset:6144\n" \
"global_load_lds_dwordx4 v222, s[36:37] offset:3072 sc1\n" \
"v_mfma_f32_16x16x32_bf16 v[56:59], a[52:55], a[28:31], v[56:59]\n" \
"ds_read_b64_tr_b16 a[46:47], v215 offset:26624\n" \
"v_cvt_pk_bf16_f32 v75, v98, v99\n" \
"v_mfma_f32_16x16x32_bf16 v[60:63], a[56:59], a[24:27], 0\n" \
"v_cvt_pk_bf16_f32 v198, v100, v101\n" \
"v_cvt_pk_bf16_f32 v199, v102, v103\n" \
"v_mfma_f32_16x16x32_bf16 v[60:63], a[60:63], a[28:31], v[60:63]\n" \
"s_waitcnt lgkmcnt(3)\n" \
"v_cvt_pk_bf16_f32 v76, v104, v105\n" \
"v_cvt_pk_bf16_f32 v77, v106, v107\n" \
"v_mfma_f32_16x16x32_bf16 v[0:3], a[32:35], a[0:3], v[0:3]\n" \
"v_cvt_pk_bf16_f32 v200, v108, v109\n" \
"v_cvt_pk_bf16_f32 v201, v110, v111\n" \
"v_mfma_f32_16x16x32_bf16 v[176:179], v[64:67], v[124:127], v[176:179]\n" \
"v_mfma_f32_16x16x32_bf16 v[172:175], v[140:143], v[124:127], v[172:175]\n" \
"s_add_u32 s36, s36, 0x1000\n" \
"ds_read_b128 a[24:27], v213 offset:4352\n" \
"s_add_u32 m0, m0, 0x5000\n" \
"v_mfma_f32_16x16x32_bf16 v[0:3], a[36:39], a[4:7], v[0:3]\n" \
"s_waitcnt lgkmcnt(1)\n" \
"ds_read_b128 a[28:31], v213 offset:6400\n" \
"global_load_lds_dwordx4 v216, s[38:39] offset:-4096\n" \
"v_mfma_f32_16x16x32_bf16 v[168:171], v[140:143], v[120:123], v[168:171]\n" \
"v_mfma_f32_16x16x32_bf16 v[164:167], v[136:139], v[120:123], v[164:167]\n" \
"v_mfma_f32_16x16x32_bf16 v[160:163], v[136:139], v[116:119], v[160:163]\n" \
"v_mfma_f32_16x16x32_bf16 v[156:159], v[132:135], v[116:119], v[156:159]\n" \
"v_mfma_f32_16x16x32_bf16 v[152:155], v[132:135], v[112:115], v[152:155]\n" \
"v_mfma_f32_16x16x32_bf16 v[148:151], v[128:131], v[112:115], v[148:151]\n" \
"v_mfma_f32_16x16x32_bf16 v[4:7], a[40:43], a[0:3], v[4:7]\n" \
"s_sub_u32 s74, s74, 32768\n" \
"s_cselect_b32 s74, 131072, s74\n" \
"ds_read_b64_tr_b16 a[48:49], v214 offset:16896\n" \
"v_mfma_f32_16x16x32_bf16 v[4:7], a[44:47], a[4:7], v[4:7]\n" \
"ds_read_b64_tr_b16 a[50:51], v214 offset:18944\n" \
"v_max3_f32 v183, v172, v173, v174\n" \
"v_max3_f32 v182, v164, v165, v166\n" \
"v_max3_f32 v181, v156, v157, v158\n" \
"v_max3_f32 v180, v148, v149, v150\n" \
"v_max3_f32 v183, v183, v175, v176\n" \
"v_max3_f32 v182, v182, v167, v168\n" \
"v_mfma_f32_16x16x32_bf16 v[16:19], a[32:35], a[8:11], v[16:19]\n" \
"ds_read_b64_tr_b16 a[52:53], v214 offset:25088\n" \
"global_load_lds_dwordx4 v217, s[38:39] offset:-3072\n" \
"v_max3_f32 v181, v181, v159, v160\n" \
"v_max3_f32 v180, v180, v151, v152\n" \
"v_max3_f32 v183, v183, v177, v178\n" \
"v_max3_f32 v182, v182, v169, v170\n" \
"v_max3_f32 v181, v181, v161, v162\n" \
"v_max3_f32 v180, v180, v153, v154\n" \
"v_mfma_f32_16x16x32_bf16 v[16:19], a[36:39], a[12:15], v[16:19]\n" \
"ds_read_b64_tr_b16 a[54:55], v214 offset:27136\n" \
"v_max_f32 v183, v183, v179\n" \
"v_max_f32 v182, v182, v171\n" \
"v_max_f32 v181, v181, v163\n" \
"v_max_f32 v180, v180, v155\n" \
"v_mov_b32 v187, v183\n" \
"v_mov_b32 v186, v182\n" \
"v_mfma_f32_16x16x32_bf16 v[20:23], a[40:43], a[8:11], v[20:23]\n" \
"ds_read_b64_tr_b16 a[56:57], v215 offset:16896\n" \
"v_mov_b32 v185, v181\n" \
"v_mov_b32 v184, v180\n" \
"v_permlane16_swap_b32 v187, v183\n" \
"v_permlane16_swap_b32 v186, v182\n" \
"v_permlane16_swap_b32 v185, v181\n" \
"v_permlane16_swap_b32 v184, v180\n" \
"v_mfma_f32_16x16x32_bf16 v[20:23], a[44:47], a[12:15], v[20:23]\n" \
"ds_read_b64_tr_b16 a[58:59], v215 offset:18944\n" \
"global_load_lds_dwordx4 v218, s[38:39] offset:-2048\n" \
"v_max_f32 v183, v183, v187\n" \
"v_max_f32 v182, v182, v186\n" \
"v_max_f32 v181, v181, v185\n" \
"v_max_f32 v180, v180, v184\n" \
"v_mov_b32 v187, v183\n" \
"v_mov_b32 v186, v182\n" \
"v_mfma_f32_16x16x32_bf16 v[32:35], a[32:35], a[16:19], v[32:35]\n" \
"ds_read_b64_tr_b16 a[60:61], v215 offset:25088\n" \
"v_mov_b32 v185, v181\n" \
"v_mov_b32 v184, v180\n" \
"v_permlane32_swap_b32 v187, v183\n" \
"v_permlane32_swap_b32 v186, v182\n" \
"v_permlane32_swap_b32 v185, v181\n" \
"v_permlane32_swap_b32 v184, v180\n" \
"v_mfma_f32_16x16x32_bf16 v[32:35], a[36:39], a[20:23], v[32:35]\n" \
"ds_read_b64_tr_b16 a[62:63], v215 offset:27136\n" \
"v_max_f32 v183, v183, v187\n" \
"v_max_f32 v182, v182, v186\n" \
"v_max_f32 v181, v181, v185\n" \
"v_max_f32 v180, v180, v184\n" \
"v_mul_f32 v183, v183, v144\n" \
"v_mul_f32 v182, v182, v144\n" \
"v_mfma_f32_16x16x32_bf16 v[36:39], a[40:43], a[16:19], v[36:39]\n" \
"global_load_lds_dwordx4 v219, s[38:39] offset:-1024\n" \
"v_add_u32 v213, s74, v211\n" \
"v_mul_f32 v181, v181, v144\n" \
"v_mul_f32 v180, v180, v144\n" \
"v_fma_f32 v172, v172, v144, -v183\n" \
"v_fma_f32 v164, v164, v144, -v182\n" \
"v_fma_f32 v156, v156, v144, -v181\n" \
"v_mfma_f32_16x16x32_bf16 v[36:39], a[44:47], a[20:23], v[36:39]\n" \
"s_waitcnt lgkmcnt(3)\n" \
"v_exp_f32 v172, v172\n" \
"v_fma_f32 v148, v148, v144, -v180\n" \
"s_add_u32 s38, s38, 0x20000\n" \
"s_addc_u32 s39, s39, 0\n" \
"v_mfma_f32_16x16x32_bf16 v[48:51], a[32:35], a[24:27], v[48:51]\n" \
"s_sub_u32 s75, s75, 32768\n" \
"s_cselect_b32 s75, 131072, s75\n" \
"v_exp_f32 v164, v164\n" \
"s_add_u32 m0, s75, s95\n" \
"v_mfma_f32_16x16x32_bf16 v[48:51], a[36:39], a[28:31], v[48:51]\n" \
"global_load_lds_dwordx4 v222, s[36:37] offset:0 sc1\n" \
"v_add_u32 v214, s74, v212\n" \
"v_fma_f32 v173, v173, v144, -v183\n" \
"v_mfma_f32_16x16x32_bf16 v[52:55], a[40:43], a[24:27], v[52:55]\n" \
"s_waitcnt vmcnt(17) lgkmcnt(0)\n" \
"v_xor_b32 v215, 32, v214\n" \
"v_exp_f32 v156, v156\n" \
"v_mfma_f32_16x16x32_bf16 v[52:55], a[44:47], a[28:31], v[52:55]\n" \
"s_barrier\n" \
"v_fma_f32 v165, v165, v144, -v182\n" \
"v_mfma_f32_16x16x32_bf16 v[8:11], a[48:51], a[0:3], v[8:11]\n" \
"ds_read_b64_tr_b16 a[32:33], v214 offset:16384\n" \
"v_exp_f32 v148, v148\n" \
"v_fma_f32 v157, v157, v144, -v181\n" \
"v_mfma_f32_16x16x32_bf16 v[8:11], a[52:55], a[4:7], v[8:11]\n" \
"ds_read_b64_tr_b16 a[34:35], v214 offset:18432\n" \
"global_load_lds_dwordx4 v222, s[36:37] offset:1024 sc1\n" \
"v_mfma_f32_16x16x32_bf16 v[12:15], a[56:59], a[0:3], v[12:15]\n" \
"ds_read_b64_tr_b16 a[36:37], v214 offset:24576\n" \
"v_exp_f32 v173, v173\n" \
"v_fma_f32 v149, v149, v144, -v180\n" \
"v_mfma_f32_16x16x32_bf16 v[12:15], a[60:63], a[4:7], v[12:15]\n" \
"ds_read_b128 a[0:3], v213 offset:0\n" \
"v_exp_f32 v165, v165\n" \
"v_mfma_f32_16x16x32_bf16 v[24:27], a[48:51], a[8:11], v[24:27]\n" \
"ds_read_b128 a[4:7], v213 offset:2048\n" \
"global_load_lds_dwordx4 v222, s[36:37] offset:2048 sc1\n" \
"v_fma_f32 v174, v174, v144, -v183\n" \
"v_mfma_f32_16x16x32_bf16 v[24:27], a[52:55], a[12:15], v[24:27]\n" \
"ds_read_b64_tr_b16 a[38:39], v214 offset:26624\n" \
"v_exp_f32 v157, v157\n" \
"v_mfma_f32_16x16x32_bf16 v[28:31], a[56:59], a[8:11], v[28:31]\n" \
"ds_read_b64_tr_b16 a[40:41], v215 offset:16384\n" \
"v_fma_f32 v166, v166, v144, -v182\n" \
"v_mfma_f32_16x16x32_bf16 v[28:31], a[60:63], a[12:15], v[28:31]\n" \
"ds_read_b128 a[8:11], v213 offset:256\n" \
"global_load_lds_dwordx4 v222, s[36:37] offset:3072 sc1\n" \
"v_exp_f32 v149, v149\n" \
"v_fma_f32 v158, v158, v144, -v181\n" \
"v_mfma_f32_16x16x32_bf16 v[40:43], a[48:51], a[16:19], v[40:43]\n" \
"s_add_u32 s36, s36, 0x1000\n" \
"ds_read_b128 a[12:15], v213 offset:2304\n" \
"v_exp_f32 v174, v174\n" \
"v_mfma_f32_16x16x32_bf16 v[40:43], a[52:55], a[20:23], v[40:43]\n" \
"s_add_u32 m0, m0, 0x5000\n" \
"ds_read_b64_tr_b16 a[42:43], v215 offset:18432\n" \
"v_add_f32 v191, v172, v173\n" \
"v_fma_f32 v150, v150, v144, -v180\n" \
"v_mfma_f32_16x16x32_bf16 v[44:47], a[56:59], a[16:19], v[44:47]\n" \
"ds_read_b64_tr_b16 a[44:45], v215 offset:24576\n" \
"global_load_lds_dwordx4 v216, s[38:39] offset:-4096\n" \
"v_mfma_f32_16x16x32_bf16 v[44:47], a[60:63], a[20:23], v[44:47]\n" \
"ds_read_b128 a[16:19], v213 offset:4096\n" \
"v_exp_f32 v166, v166\n" \
"v_add_f32 v190, v164, v165\n" \
"v_mfma_f32_16x16x32_bf16 v[56:59], a[48:51], a[24:27], v[56:59]\n" \
"ds_read_b128 a[20:23], v213 offset:6144\n" \
"global_load_lds_dwordx4 v217, s[38:39] offset:-3072\n" \
"v_fma_f32 v175, v175, v144, -v183\n" \
"v_mfma_f32_16x16x32_bf16 v[56:59], a[52:55], a[28:31], v[56:59]\n" \
"ds_read_b64_tr_b16 a[46:47], v215 offset:26624\n" \
"v_exp_f32 v158, v158\n" \
"v_mfma_f32_16x16x32_bf16 v[60:63], a[56:59], a[24:27], v[60:63]\n" \
"v_add_f32 v189, v156, v157\n" \
"v_fma_f32 v167, v167, v144, -v182\n" \
"v_mfma_f32_16x16x32_bf16 v[60:63], a[60:63], a[28:31], v[60:63]\n" \
"s_waitcnt lgkmcnt(3)\n" \
"v_exp_f32 v150, v150\n" \
"v_mfma_f32_16x16x32_bf16 v[0:3], a[32:35], a[0:3], v[0:3]\n" \
"ds_read_b128 a[24:27], v213 offset:4352\n" \
"v_add_f32 v188, v148, v149\n" \
"v_fma_f32 v159, v159, v144, -v181\n" \
"v_mfma_f32_16x16x32_bf16 v[0:3], a[36:39], a[4:7], v[0:3]\n" \
"s_waitcnt lgkmcnt(1)\n" \
"ds_read_b128 a[28:31], v213 offset:6400\n" \
"global_load_lds_dwordx4 v218, s[38:39] offset:-2048\n" \
"v_exp_f32 v175, v175\n" \
"v_mfma_f32_16x16x32_bf16 v[4:7], a[40:43], a[0:3], v[4:7]\n" \
"s_sub_u32 s74, s74, 32768\n" \
"s_cselect_b32 s74, 131072, s74\n" \
"ds_read_b64_tr_b16 a[48:49], v214 offset:16896\n" \
"v_add_f32 v191, v191, v174\n" \
"v_fma_f32 v151, v151, v144, -v180\n" \
"v_mfma_f32_16x16x32_bf16 v[4:7], a[44:47], a[4:7], v[4:7]\n" \
"ds_read_b64_tr_b16 a[50:51], v214 offset:18944\n" \
"global_load_lds_dwordx4 v219, s[38:39] offset:-1024\n" \
"v_mfma_f32_16x16x32_bf16 v[16:19], a[32:35], a[8:11], v[16:19]\n" \
"s_add_u32 s38, s38, 0x20000\n" \
"s_addc_u32 s39, s39, 0\n" \
"ds_read_b64_tr_b16 a[52:53], v214 offset:25088\n" \
"v_exp_f32 v167, v167\n" \
"v_add_f32 v190, v190, v166\n" \
"v_mfma_f32_16x16x32_bf16 v[16:19], a[36:39], a[12:15], v[16:19]\n" \
"s_sub_u32 s75, s75, 32768\n" \
"s_cselect_b32 s75, 131072, s75\n" \
"ds_read_b64_tr_b16 a[54:55], v214 offset:27136\n" \
"v_fma_f32 v176, v176, v144, -v183\n" \
"v_mfma_f32_16x16x32_bf16 v[20:23], a[40:43], a[8:11], v[20:23]\n" \
"s_add_u32 m0, s75, s95\n" \
"ds_read_b64_tr_b16 a[56:57], v215 offset:16896\n" \
"v_exp_f32 v159, v159\n" \
"v_add_f32 v189, v189, v158\n" \
"v_mfma_f32_16x16x32_bf16 v[20:23], a[44:47], a[12:15], v[20:23]\n" \
"ds_read_b64_tr_b16 a[58:59], v215 offset:18944\n" \
"global_load_lds_dwordx4 v222, s[36:37] offset:0 sc1\n" \
"v_fma_f32 v168, v168, v144, -v182\n" \
"v_mfma_f32_16x16x32_bf16 v[32:35], a[32:35], a[16:19], v[32:35]\n" \
"Lpg11578_%=:\n" \
"s_cbranch_execz Lpg12735_%=\n" \
"ds_read_b64_tr_b16 a[60:61], v215 offset:25088\n" \
"v_exp_f32 v151, v151\n" \
"v_mfma_f32_16x16x32_bf16 v[32:35], a[36:39], a[20:23], v[32:35]\n" \
"ds_read_b64_tr_b16 a[62:63], v215 offset:27136\n" \
"v_add_f32 v188, v188, v150\n" \
"v_fma_f32 v160, v160, v144, -v181\n" \
"v_mfma_f32_16x16x32_bf16 v[36:39], a[40:43], a[16:19], v[36:39]\n" \
"global_load_lds_dwordx4 v222, s[36:37] offset:1024 sc1\n" \
"v_add_u32 v213, s74, v211\n" \
"v_exp_f32 v176, v176\n" \
"v_mfma_f32_16x16x32_bf16 v[36:39], a[44:47], a[20:23], v[36:39]\n" \
"s_waitcnt lgkmcnt(3)\n" \
"v_add_u32 v214, s74, v212\n" \
"v_xor_b32 v215, 32, v214\n" \
"v_mfma_f32_16x16x32_bf16 v[48:51], a[32:35], a[24:27], v[48:51]\n" \
"v_add_f32 v191, v191, v175\n" \
"v_fma_f32 v152, v152, v144, -v180\n" \
"v_mfma_f32_16x16x32_bf16 v[48:51], a[36:39], a[28:31], v[48:51]\n" \
"global_load_lds_dwordx4 v222, s[36:37] offset:2048 sc1\n" \
"v_exp_f32 v168, v168\n" \
"v_mfma_f32_16x16x32_bf16 v[52:55], a[40:43], a[24:27], v[52:55]\n" \
"s_waitcnt vmcnt(19) lgkmcnt(0)\n" \
"v_add_f32 v190, v190, v167\n" \
"v_fma_f32 v177, v177, v144, -v183\n" \
"v_mfma_f32_16x16x32_bf16 v[52:55], a[44:47], a[28:31], v[52:55]\n" \
"s_barrier\n" \
"v_exp_f32 v160, v160\n" \
"v_mfma_f32_16x16x32_bf16 v[8:11], a[48:51], a[0:3], v[8:11]\n" \
"ds_read_b64_tr_b16 a[32:33], v214 offset:16384\n" \
"v_add_f32 v189, v189, v159\n" \
"v_fma_f32 v169, v169, v144, -v182\n" \
"v_mfma_f32_16x16x32_bf16 v[8:11], a[52:55], a[4:7], v[8:11]\n" \
"ds_read_b64_tr_b16 a[34:35], v214 offset:18432\n" \
"global_load_lds_dwordx4 v222, s[36:37] offset:3072 sc1\n" \
"v_exp_f32 v152, v152\n" \
"v_mfma_f32_16x16x32_bf16 v[12:15], a[56:59], a[0:3], v[12:15]\n" \
"s_add_u32 s36, s36, 0x1000\n" \
"ds_read_b64_tr_b16 a[36:37], v214 offset:24576\n" \
"v_add_f32 v188, v188, v151\n" \
"v_fma_f32 v161, v161, v144, -v181\n" \
"v_mfma_f32_16x16x32_bf16 v[12:15], a[60:63], a[4:7], v[12:15]\n" \
"s_add_u32 m0, m0, 0x5000\n" \
"ds_read_b128 a[0:3], v213 offset:0\n" \
"v_exp_f32 v177, v177\n" \
"v_mfma_f32_16x16x32_bf16 v[24:27], a[48:51], a[8:11], v[24:27]\n" \
"ds_read_b128 a[4:7], v213 offset:2048\n" \
"global_load_lds_dwordx4 v216, s[38:39] offset:-4096\n" \
"v_add_f32 v191, v191, v176\n" \
"v_mfma_f32_16x16x32_bf16 v[24:27], a[52:55], a[12:15], v[24:27]\n" \
"ds_read_b64_tr_b16 a[38:39], v214 offset:26624\n" \
"v_fma_f32 v153, v153, v144, -v180\n" \
"v_mfma_f32_16x16x32_bf16 v[28:31], a[56:59], a[8:11], v[28:31]\n" \
"ds_read_b64_tr_b16 a[40:41], v215 offset:16384\n" \
"v_exp_f32 v169, v169\n" \
"v_add_f32 v190, v190, v168\n" \
"v_mfma_f32_16x16x32_bf16 v[28:31], a[60:63], a[12:15], v[28:31]\n" \
"ds_read_b128 a[8:11], v213 offset:256\n" \
"global_load_lds_dwordx4 v217, s[38:39] offset:-3072\n" \
"v_fma_f32 v178, v178, v144, -v183\n" \
"v_mfma_f32_16x16x32_bf16 v[40:43], a[48:51], a[16:19], v[40:43]\n" \
"ds_read_b128 a[12:15], v213 offset:2304\n" \
"v_exp_f32 v161, v161\n" \
"v_mfma_f32_16x16x32_bf16 v[40:43], a[52:55], a[20:23], v[40:43]\n" \
"ds_read_b64_tr_b16 a[42:43], v215 offset:18432\n" \
"v_add_f32 v189, v189, v160\n" \
"v_fma_f32 v170, v170, v144, -v182\n" \
"v_mfma_f32_16x16x32_bf16 v[44:47], a[56:59], a[16:19], v[44:47]\n" \
"ds_read_b64_tr_b16 a[44:45], v215 offset:24576\n" \
"global_load_lds_dwordx4 v218, s[38:39] offset:-2048\n" \
"v_exp_f32 v153, v153\n" \
"v_mfma_f32_16x16x32_bf16 v[44:47], a[60:63], a[20:23], v[44:47]\n" \
"ds_read_b128 a[16:19], v213 offset:4096\n" \
"v_add_f32 v188, v188, v152\n" \
"v_fma_f32 v162, v162, v144, -v181\n" \
"v_mfma_f32_16x16x32_bf16 v[56:59], a[48:51], a[24:27], v[56:59]\n" \
"ds_read_b128 a[20:23], v213 offset:6144\n" \
"global_load_lds_dwordx4 v219, s[38:39] offset:-1024\n" \
"v_mfma_f32_16x16x32_bf16 v[56:59], a[52:55], a[28:31], v[56:59]\n" \
"s_add_u32 s38, s38, 0x20000\n" \
"s_addc_u32 s39, s39, 0\n" \
"ds_read_b64_tr_b16 a[46:47], v215 offset:26624\n" \
"v_exp_f32 v178, v178\n" \
"v_add_f32 v191, v191, v177\n" \
"v_mfma_f32_16x16x32_bf16 v[60:63], a[56:59], a[24:27], v[60:63]\n" \
"s_sub_u32 s75, s75, 32768\n" \
"s_cselect_b32 s75, 131072, s75\n" \
"v_fma_f32 v154, v154, v144, -v180\n" \
"v_mfma_f32_16x16x32_bf16 v[60:63], a[60:63], a[28:31], v[60:63]\n" \
"s_waitcnt lgkmcnt(3)\n" \
"v_exp_f32 v170, v170\n" \
"v_add_f32 v190, v190, v169\n" \
"v_mfma_f32_16x16x32_bf16 v[0:3], a[32:35], a[0:3], v[0:3]\n" \
"s_add_u32 m0, s75, s95\n" \
"ds_read_b128 a[24:27], v213 offset:4352\n" \
"v_fma_f32 v179, v179, v144, -v183\n" \
"v_mfma_f32_16x16x32_bf16 v[0:3], a[36:39], a[4:7], v[0:3]\n" \
"s_waitcnt lgkmcnt(1)\n" \
"ds_read_b128 a[28:31], v213 offset:6400\n" \
"global_load_lds_dwordx4 v222, s[36:37] offset:0 sc1\n" \
"v_exp_f32 v162, v162\n" \
"v_mfma_f32_16x16x32_bf16 v[4:7], a[40:43], a[0:3], v[4:7]\n" \
"s_sub_u32 s74, s74, 32768\n" \
"s_cselect_b32 s74, 131072, s74\n" \
"ds_read_b64_tr_b16 a[48:49], v214 offset:16896\n" \
"v_add_f32 v189, v189, v161\n" \
"v_fma_f32 v171, v171, v144, -v182\n" \
"v_mfma_f32_16x16x32_bf16 v[4:7], a[44:47], a[4:7], v[4:7]\n" \
"ds_read_b64_tr_b16 a[50:51], v214 offset:18944\n" \
"v_exp_f32 v154, v154\n" \
"v_mfma_f32_16x16x32_bf16 v[16:19], a[32:35], a[8:11], v[16:19]\n" \
"ds_read_b64_tr_b16 a[52:53], v214 offset:25088\n" \
"v_add_f32 v188, v188, v153\n" \
"v_fma_f32 v163, v163, v144, -v181\n" \
"v_mfma_f32_16x16x32_bf16 v[16:19], a[36:39], a[12:15], v[16:19]\n" \
"ds_read_b64_tr_b16 a[54:55], v214 offset:27136\n" \
"global_load_lds_dwordx4 v222, s[36:37] offset:1024 sc1\n" \
"v_mfma_f32_16x16x32_bf16 v[20:23], a[40:43], a[8:11], v[20:23]\n" \
"ds_read_b64_tr_b16 a[56:57], v215 offset:16896\n" \
"v_exp_f32 v179, v179\n" \
"v_add_f32 v191, v191, v178\n" \
"v_mfma_f32_16x16x32_bf16 v[20:23], a[44:47], a[12:15], v[20:23]\n" \
"ds_read_b64_tr_b16 a[58:59], v215 offset:18944\n" \
"v_fma_f32 v155, v155, v144, -v180\n" \
"v_mfma_f32_16x16x32_bf16 v[32:35], a[32:35], a[16:19], v[32:35]\n" \
"ds_read_b64_tr_b16 a[60:61], v215 offset:25088\n" \
"v_exp_f32 v171, v171\n" \
"v_add_f32 v190, v190, v170\n" \
"v_mfma_f32_16x16x32_bf16 v[32:35], a[36:39], a[20:23], v[32:35]\n" \
"ds_read_b64_tr_b16 a[62:63], v215 offset:27136\n" \
"global_load_lds_dwordx4 v222, s[36:37] offset:2048 sc1\n" \
"v_mfma_f32_16x16x32_bf16 v[36:39], a[40:43], a[16:19], v[36:39]\n" \
"v_add_u32 v213, s74, v211\n" \
"v_add_u32 v214, s74, v212\n" \
"v_exp_f32 v163, v163\n" \
"v_add_f32 v189, v189, v162\n" \
"v_mfma_f32_16x16x32_bf16 v[36:39], a[44:47], a[20:23], v[36:39]\n" \
"s_waitcnt lgkmcnt(3)\n" \
"v_xor_b32 v215, 32, v214\n" \
"v_exp_f32 v155, v155\n" \
"v_mfma_f32_16x16x32_bf16 v[48:51], a[32:35], a[24:27], v[48:51]\n" \
"v_add_f32 v188, v188, v154\n" \
"v_add_f32 v191, v191, v179\n" \
"v_mfma_f32_16x16x32_bf16 v[48:51], a[36:39], a[28:31], v[48:51]\n" \
"global_load_lds_dwordx4 v222, s[36:37] offset:3072 sc1\n" \
"v_add_f32 v190, v190, v171\n" \
"v_mfma_f32_16x16x32_bf16 v[52:55], a[40:43], a[24:27], v[52:55]\n" \
"s_waitcnt vmcnt(20) lgkmcnt(0)\n" \
"v_add_f32 v189, v189, v163\n" \
"v_add_f32 v188, v188, v155\n" \
"s_add_u32 s36, s36, 0x1000\n" \
"v_mfma_f32_16x16x32_bf16 v[52:55], a[44:47], a[28:31], v[52:55]\n" \
"s_barrier\n" \
"v_mov_b32 v187, v191\n" \
"v_mov_b32 v186, v190\n" \
"v_mfma_f32_16x16x32_bf16 v[8:11], a[48:51], a[0:3], v[8:11]\n" \
"s_add_u32 m0, m0, 0x5000\n" \
"ds_read_b64_tr_b16 a[32:33], v214 offset:16384\n" \
"v_mov_b32 v185, v189\n" \
"v_mov_b32 v184, v188\n" \
"v_mfma_f32_16x16x32_bf16 v[8:11], a[52:55], a[4:7], v[8:11]\n" \
"ds_read_b64_tr_b16 a[34:35], v214 offset:18432\n" \
"global_load_lds_dwordx4 v216, s[38:39] offset:-4096\n" \
"v_mfma_f32_16x16x32_bf16 v[12:15], a[56:59], a[0:3], v[12:15]\n" \
"ds_read_b64_tr_b16 a[36:37], v214 offset:24576\n" \
"v_permlane16_swap_b32 v187, v191\n" \
"v_mfma_f32_16x16x32_bf16 v[12:15], a[60:63], a[4:7], v[12:15]\n" \
"ds_read_b128 a[0:3], v213 offset:0\n" \
"v_permlane16_swap_b32 v186, v190\n" \
"v_mfma_f32_16x16x32_bf16 v[24:27], a[48:51], a[8:11], v[24:27]\n" \
"ds_read_b128 a[4:7], v213 offset:2048\n" \
"v_permlane16_swap_b32 v185, v189\n" \
"v_mfma_f32_16x16x32_bf16 v[24:27], a[52:55], a[12:15], v[24:27]\n" \
"ds_read_b64_tr_b16 a[38:39], v214 offset:26624\n" \
"global_load_lds_dwordx4 v217, s[38:39] offset:-3072\n" \
"v_permlane16_swap_b32 v184, v188\n" \
"v_add_f32 v191, v191, v187\n" \
"v_mfma_f32_16x16x32_bf16 v[28:31], a[56:59], a[8:11], v[28:31]\n" \
"ds_read_b64_tr_b16 a[40:41], v215 offset:16384\n" \
"v_add_f32 v190, v190, v186\n" \
"v_mfma_f32_16x16x32_bf16 v[28:31], a[60:63], a[12:15], v[28:31]\n" \
"ds_read_b128 a[8:11], v213 offset:256\n" \
"v_add_f32 v189, v189, v185\n" \
"v_add_f32 v188, v188, v184\n" \
"v_mfma_f32_16x16x32_bf16 v[40:43], a[48:51], a[16:19], v[40:43]\n" \
"ds_read_b128 a[12:15], v213 offset:2304\n" \
"v_mov_b32 v187, v191\n" \
"v_mov_b32 v186, v190\n" \
"v_mfma_f32_16x16x32_bf16 v[40:43], a[52:55], a[20:23], v[40:43]\n" \
"ds_read_b64_tr_b16 a[42:43], v215 offset:18432\n" \
"global_load_lds_dwordx4 v218, s[38:39] offset:-2048\n" \
"v_mov_b32 v185, v189\n" \
"v_mfma_f32_16x16x32_bf16 v[44:47], a[56:59], a[16:19], v[44:47]\n" \
"ds_read_b64_tr_b16 a[44:45], v215 offset:24576\n" \
"v_mov_b32 v184, v188\n" \
"v_mfma_f32_16x16x32_bf16 v[44:47], a[60:63], a[20:23], v[44:47]\n" \
"ds_read_b128 a[16:19], v213 offset:4096\n" \
"v_permlane32_swap_b32 v187, v191\n" \
"v_mfma_f32_16x16x32_bf16 v[56:59], a[48:51], a[24:27], v[56:59]\n" \
"ds_read_b128 a[20:23], v213 offset:6144\n" \
"global_load_lds_dwordx4 v219, s[38:39] offset:-1024\n" \
"v_permlane32_swap_b32 v186, v190\n" \
"v_mfma_f32_16x16x32_bf16 v[56:59], a[52:55], a[28:31], v[56:59]\n" \
"s_add_u32 s38, s38, 0x20000\n" \
"s_addc_u32 s39, s39, 0\n" \
"ds_read_b64_tr_b16 a[46:47], v215 offset:26624\n" \
"v_permlane32_swap_b32 v185, v189\n" \
"v_mfma_f32_16x16x32_bf16 v[60:63], a[56:59], a[24:27], v[60:63]\n" \
"s_sub_u32 s75, s75, 32768\n" \
"s_cselect_b32 s75, 131072, s75\n" \
"v_permlane32_swap_b32 v184, v188\n" \
"v_mfma_f32_16x16x32_bf16 v[60:63], a[60:63], a[28:31], v[60:63]\n" \
"s_waitcnt lgkmcnt(3)\n" \
"v_add_f32 v191, v191, v187\n" \
"v_add_f32 v190, v190, v186\n" \
"v_mfma_f32_16x16x32_bf16 v[0:3], a[32:35], a[0:3], v[0:3]\n" \
"s_add_u32 m0, s75, s95\n" \
"ds_read_b128 a[24:27], v213 offset:4352\n" \
"v_add_f32 v189, v189, v185\n" \
"v_add_f32 v188, v188, v184\n" \
"v_mfma_f32_16x16x32_bf16 v[0:3], a[36:39], a[4:7], v[0:3]\n" \
"s_waitcnt lgkmcnt(1)\n" \
"ds_read_b128 a[28:31], v213 offset:6400\n" \
"global_load_lds_dwordx4 v222, s[36:37] offset:0 sc1\n" \
"v_rcp_f32 v191, v191\n" \
"v_mfma_f32_16x16x32_bf16 v[4:7], a[40:43], a[0:3], v[4:7]\n" \
"s_sub_u32 s74, s74, 32768\n" \
"s_cselect_b32 s74, 131072, s74\n" \
"ds_read_b64_tr_b16 a[48:49], v214 offset:16896\n" \
"v_mfma_f32_16x16x32_bf16 v[4:7], a[44:47], a[4:7], v[4:7]\n" \
"ds_read_b64_tr_b16 a[50:51], v214 offset:18944\n" \
"v_rcp_f32 v190, v190\n" \
"v_mfma_f32_16x16x32_bf16 v[16:19], a[32:35], a[8:11], v[16:19]\n" \
"ds_read_b64_tr_b16 a[52:53], v214 offset:25088\n" \
"v_rcp_f32 v189, v189\n" \
"v_mfma_f32_16x16x32_bf16 v[16:19], a[36:39], a[12:15], v[16:19]\n" \
"ds_read_b64_tr_b16 a[54:55], v214 offset:27136\n" \
"global_load_lds_dwordx4 v222, s[36:37] offset:1024 sc1\n" \
"v_rcp_f32 v188, v188\n" \
"v_mul_f32 v172, v172, v191\n" \
"v_mfma_f32_16x16x32_bf16 v[20:23], a[40:43], a[8:11], v[20:23]\n" \
"ds_read_b64_tr_b16 a[56:57], v215 offset:16896\n" \
"v_mul_f32 v164, v164, v190\n" \
"v_mul_f32 v156, v156, v189\n" \
"v_mfma_f32_16x16x32_bf16 v[20:23], a[44:47], a[12:15], v[20:23]\n" \
"ds_read_b64_tr_b16 a[58:59], v215 offset:18944\n" \
"v_mul_f32 v148, v148, v188\n" \
"v_mfma_f32_16x16x32_bf16 v[32:35], a[32:35], a[16:19], v[32:35]\n" \
"ds_read_b64_tr_b16 a[60:61], v215 offset:25088\n" \
"v_mul_f32 v173, v173, v191\n" \
"v_mul_f32 v165, v165, v190\n" \
"v_mfma_f32_16x16x32_bf16 v[32:35], a[36:39], a[20:23], v[32:35]\n" \
"ds_read_b64_tr_b16 a[62:63], v215 offset:27136\n" \
"global_load_lds_dwordx4 v222, s[36:37] offset:2048 sc1\n" \
"v_mul_f32 v157, v157, v189\n" \
"v_mul_f32 v149, v149, v188\n" \
"v_mfma_f32_16x16x32_bf16 v[36:39], a[40:43], a[16:19], v[36:39]\n" \
"v_add_u32 v213, s74, v211\n" \
"v_add_u32 v214, s74, v212\n" \
"v_mul_f32 v174, v174, v191\n" \
"v_mul_f32 v166, v166, v190\n" \
"v_mfma_f32_16x16x32_bf16 v[36:39], a[44:47], a[20:23], v[36:39]\n" \
"s_waitcnt lgkmcnt(3)\n" \
"v_xor_b32 v215, 32, v214\n" \
"v_mul_f32 v158, v158, v189\n" \
"v_mfma_f32_16x16x32_bf16 v[48:51], a[32:35], a[24:27], v[48:51]\n" \
"v_mul_f32 v150, v150, v188\n" \
"v_mul_f32 v175, v175, v191\n" \
"v_mfma_f32_16x16x32_bf16 v[48:51], a[36:39], a[28:31], v[48:51]\n" \
"global_load_lds_dwordx4 v222, s[36:37] offset:3072 sc1\n" \
"v_mul_f32 v167, v167, v190\n" \
"v_mul_f32 v159, v159, v189\n" \
"v_mfma_f32_16x16x32_bf16 v[52:55], a[40:43], a[24:27], v[52:55]\n" \
"s_waitcnt vmcnt(20) lgkmcnt(0)\n" \
"v_mul_f32 v151, v151, v188\n" \
"v_mul_f32 v176, v176, v191\n" \
"s_add_u32 s36, s36, 0x1000\n" \
"v_mfma_f32_16x16x32_bf16 v[52:55], a[44:47], a[28:31], v[52:55]\n" \
"s_barrier\n" \
"v_mul_f32 v168, v168, v190\n" \
"v_mul_f32 v160, v160, v189\n" \
"v_mfma_f32_16x16x32_bf16 v[8:11], a[48:51], a[0:3], v[8:11]\n" \
"s_add_u32 m0, m0, 0x5000\n" \
"ds_read_b64_tr_b16 a[32:33], v214 offset:16384\n" \
"v_mul_f32 v152, v152, v188\n" \
"v_mfma_f32_16x16x32_bf16 v[8:11], a[52:55], a[4:7], v[8:11]\n" \
"ds_read_b64_tr_b16 a[34:35], v214 offset:18432\n" \
"global_load_lds_dwordx4 v216, s[38:39] offset:-4096\n" \
"v_mul_f32 v177, v177, v191\n" \
"v_mul_f32 v169, v169, v190\n" \
"v_mfma_f32_16x16x32_bf16 v[12:15], a[56:59], a[0:3], v[12:15]\n" \
"ds_read_b64_tr_b16 a[36:37], v214 offset:24576\n" \
"v_mul_f32 v161, v161, v189\n" \
"v_mul_f32 v153, v153, v188\n" \
"v_mfma_f32_16x16x32_bf16 v[12:15], a[60:63], a[4:7], v[12:15]\n" \
"ds_read_b128 a[0:3], v213 offset:0\n" \
"v_mul_f32 v178, v178, v191\n" \
"v_mul_f32 v170, v170, v190\n" \
"v_mfma_f32_16x16x32_bf16 v[24:27], a[48:51], a[8:11], v[24:27]\n" \
"ds_read_b128 a[4:7], v213 offset:2048\n" \
"v_mul_f32 v162, v162, v189\n" \
"v_mfma_f32_16x16x32_bf16 v[24:27], a[52:55], a[12:15], v[24:27]\n" \
"ds_read_b64_tr_b16 a[38:39], v214 offset:26624\n" \
"global_load_lds_dwordx4 v217, s[38:39] offset:-3072\n" \
"v_mul_f32 v154, v154, v188\n" \
"v_mul_f32 v179, v179, v191\n" \
"v_mfma_f32_16x16x32_bf16 v[28:31], a[56:59], a[8:11], v[28:31]\n" \
"ds_read_b64_tr_b16 a[40:41], v215 offset:16384\n" \
"v_mul_f32 v171, v171, v190\n" \
"v_mul_f32 v163, v163, v189\n" \
"v_mfma_f32_16x16x32_bf16 v[28:31], a[60:63], a[12:15], v[28:31]\n" \
"ds_read_b128 a[8:11], v213 offset:256\n" \
"v_mul_f32 v155, v155, v188\n" \
"v_cvt_pk_bf16_f32 v172, v172, v173\n" \
"v_mfma_f32_16x16x32_bf16 v[40:43], a[48:51], a[16:19], v[40:43]\n" \
"ds_read_b128 a[12:15], v213 offset:2304\n" \
"v_cvt_pk_bf16_f32 v173, v174, v175\n" \
"v_mfma_f32_16x16x32_bf16 v[40:43], a[52:55], a[20:23], v[40:43]\n" \
"ds_read_b64_tr_b16 a[42:43], v215 offset:18432\n" \
"global_load_lds_dwordx4 v218, s[38:39] offset:-2048\n" \
"v_cvt_pk_bf16_f32 v174, v176, v177\n" \
"v_cvt_pk_bf16_f32 v175, v178, v179\n" \
"v_mfma_f32_16x16x32_bf16 v[44:47], a[56:59], a[16:19], v[44:47]\n" \
"ds_read_b64_tr_b16 a[44:45], v215 offset:24576\n" \
"v_cvt_pk_bf16_f32 v164, v164, v165\n" \
"v_cvt_pk_bf16_f32 v165, v166, v167\n" \
"v_mfma_f32_16x16x32_bf16 v[44:47], a[60:63], a[20:23], v[44:47]\n" \
"ds_read_b128 a[16:19], v213 offset:4096\n" \
"v_cvt_pk_bf16_f32 v166, v168, v169\n" \
"v_cvt_pk_bf16_f32 v167, v170, v171\n" \
"v_mfma_f32_16x16x32_bf16 v[56:59], a[48:51], a[24:27], v[56:59]\n" \
"ds_read_b128 a[20:23], v213 offset:6144\n" \
"global_load_lds_dwordx4 v219, s[38:39] offset:-1024\n" \
"v_mfma_f32_16x16x32_bf16 v[56:59], a[52:55], a[28:31], v[56:59]\n" \
"s_add_u32 s38, s38, 0x20000\n" \
"s_addc_u32 s39, s39, 0\n" \
"ds_read_b64_tr_b16 a[46:47], v215 offset:26624\n" \
"v_mfma_f32_16x16x32_bf16 v[60:63], a[56:59], a[24:27], v[60:63]\n" \
"s_sub_u32 s75, s75, 32768\n" \
"s_cselect_b32 s75, 131072, s75\n" \
"v_cvt_pk_bf16_f32 v156, v156, v157\n" \
"v_mfma_f32_16x16x32_bf16 v[60:63], a[60:63], a[28:31], v[60:63]\n" \
"s_waitcnt lgkmcnt(3)\n" \
"v_cvt_pk_bf16_f32 v157, v158, v159\n" \
"v_cvt_pk_bf16_f32 v158, v160, v161\n" \
"v_mfma_f32_16x16x32_bf16 v[0:3], a[32:35], a[0:3], v[0:3]\n" \
"s_add_u32 m0, s75, s95\n" \
"ds_read_b128 a[24:27], v213 offset:4352\n" \
"v_cvt_pk_bf16_f32 v159, v162, v163\n" \
"v_mfma_f32_16x16x32_bf16 v[0:3], a[36:39], a[4:7], v[0:3]\n" \
"s_waitcnt lgkmcnt(1)\n" \
"ds_read_b128 a[28:31], v213 offset:6400\n" \
"global_load_lds_dwordx4 v222, s[36:37] offset:0 sc1\n" \
"v_mfma_f32_16x16x32_bf16 v[4:7], a[40:43], a[0:3], v[4:7]\n" \
"s_sub_u32 s74, s74, 32768\n" \
"s_cselect_b32 s74, 131072, s74\n" \
"ds_read_b64_tr_b16 a[48:49], v214 offset:16896\n" \
"v_mfma_f32_16x16x32_bf16 v[4:7], a[44:47], a[4:7], v[4:7]\n" \
"ds_read_b64_tr_b16 a[50:51], v214 offset:18944\n" \
"v_cvt_pk_bf16_f32 v148, v148, v149\n" \
"v_mfma_f32_16x16x32_bf16 v[16:19], a[32:35], a[8:11], v[16:19]\n" \
"ds_read_b64_tr_b16 a[52:53], v214 offset:25088\n" \
"v_cvt_pk_bf16_f32 v149, v150, v151\n" \
"v_mfma_f32_16x16x32_bf16 v[16:19], a[36:39], a[12:15], v[16:19]\n" \
"ds_read_b64_tr_b16 a[54:55], v214 offset:27136\n" \
"global_load_lds_dwordx4 v222, s[36:37] offset:1024 sc1\n" \
"v_mfma_f32_16x16x32_bf16 v[20:23], a[40:43], a[8:11], v[20:23]\n" \
"ds_read_b64_tr_b16 a[56:57], v215 offset:16896\n" \
"v_cvt_pk_bf16_f32 v150, v152, v153\n" \
"v_mfma_f32_16x16x32_bf16 v[20:23], a[44:47], a[12:15], v[20:23]\n" \
"ds_read_b64_tr_b16 a[58:59], v215 offset:18944\n" \
"v_cvt_pk_bf16_f32 v151, v154, v155\n" \
"v_mfma_f32_16x16x32_bf16 v[32:35], a[32:35], a[16:19], v[32:35]\n" \
"v_mfma_f32_16x16x32_bf16 v[136:139], v[74:77], v[172:175], 0\n" \
"v_mfma_f32_16x16x32_bf16 v[140:143], v[198:201], v[172:175], 0\n" \
"v_mfma_f32_16x16x32_bf16 v[128:131], v[72:75], v[164:167], 0\n" \
"v_mfma_f32_16x16x32_bf16 v[132:135], v[196:199], v[164:167], 0\n" \
"v_mfma_f32_16x16x32_bf16 v[120:123], v[70:73], v[156:159], 0\n" \
"v_mfma_f32_16x16x32_bf16 v[124:127], v[194:197], v[156:159], 0\n" \
"ds_read_b64_tr_b16 a[60:61], v215 offset:25088\n" \
"v_mfma_f32_16x16x32_bf16 v[32:35], a[36:39], a[20:23], v[32:35]\n" \
"v_mfma_f32_16x16x32_bf16 v[112:115], v[68:71], v[148:151], 0\n" \
"v_mfma_f32_16x16x32_bf16 v[116:119], v[192:195], v[148:151], 0\n" \
"ds_read_b64_tr_b16 a[62:63], v215 offset:27136\n" \
"global_load_lds_dwordx4 v222, s[36:37] offset:2048 sc1\n" \
"v_mfma_f32_16x16x32_bf16 v[36:39], a[40:43], a[16:19], v[36:39]\n" \
"v_add_u32 v213, s74, v211\n" \
"v_add_u32 v214, s74, v212\n" \
"v_mfma_f32_16x16x32_bf16 v[36:39], a[44:47], a[20:23], v[36:39]\n" \
"s_waitcnt lgkmcnt(3)\n" \
"v_xor_b32 v215, 32, v214\n" \
"v_cvt_pk_bf16_f32 v136, v136, v137\n" \
"v_cvt_pk_bf16_f32 v137, v138, v139\n" \
"v_mfma_f32_16x16x32_bf16 v[48:51], a[32:35], a[24:27], v[48:51]\n" \
"v_cvt_pk_bf16_f32 v138, v140, v141\n" \
"v_cvt_pk_bf16_f32 v139, v142, v143\n" \
"v_mfma_f32_16x16x32_bf16 v[48:51], a[36:39], a[28:31], v[48:51]\n" \
"global_load_lds_dwordx4 v222, s[36:37] offset:3072 sc1\n" \
"v_cndmask_b32 v140, v136, v138, s[92:93]\n" \
"v_mfma_f32_16x16x32_bf16 v[52:55], a[40:43], a[24:27], v[52:55]\n" \
"s_waitcnt vmcnt(20) lgkmcnt(0)\n" \
"v_cndmask_b32 v141, v137, v139, s[92:93]\n" \
"s_add_u32 s36, s36, 0x1000\n" \
"v_mfma_f32_16x16x32_bf16 v[52:55], a[44:47], a[28:31], v[52:55]\n" \
"s_barrier\n" \
"v_cndmask_b32 v142, v138, v136, s[92:93]\n" \
"v_mfma_f32_16x16x32_bf16 v[8:11], a[48:51], a[0:3], v[8:11]\n" \
"s_add_u32 m0, m0, 0x5000\n" \
"ds_read_b64_tr_b16 a[32:33], v214 offset:16384\n" \
"v_cndmask_b32 v143, v139, v137, s[92:93]\n" \
"v_mfma_f32_16x16x32_bf16 v[8:11], a[52:55], a[4:7], v[8:11]\n" \
"s_add_u32 s71, s87, 208896\n" \
"ds_read_b64_tr_b16 a[34:35], v214 offset:18432\n" \
"global_load_lds_dwordx4 v216, s[38:39] offset:-4096\n" \
"v_mfma_f32_16x16x32_bf16 v[12:15], a[56:59], a[0:3], v[12:15]\n" \
"ds_read_b64_tr_b16 a[36:37], v214 offset:24576\n" \
"buffer_store_dwordx4 v[140:143], v146, s[56:59], s71 offen sc1\n" \
"v_cvt_pk_bf16_f32 v128, v128, v129\n" \
"v_cvt_pk_bf16_f32 v129, v130, v131\n" \
"v_cvt_pk_bf16_f32 v130, v132, v133\n" \
"v_cvt_pk_bf16_f32 v131, v134, v135\n" \
"v_cndmask_b32 v132, v128, v130, s[92:93]\n" \
"v_mfma_f32_16x16x32_bf16 v[12:15], a[60:63], a[4:7], v[12:15]\n" \
"ds_read_b128 a[0:3], v213 offset:0\n" \
"v_cndmask_b32 v133, v129, v131, s[92:93]\n" \
"v_cndmask_b32 v134, v130, v128, s[92:93]\n" \
"v_mfma_f32_16x16x32_bf16 v[24:27], a[48:51], a[8:11], v[24:27]\n" \
"ds_read_b128 a[4:7], v213 offset:2048\n" \
"v_cndmask_b32 v135, v131, v129, s[92:93]\n" \
"v_mfma_f32_16x16x32_bf16 v[24:27], a[52:55], a[12:15], v[24:27]\n" \
"s_add_u32 s71, s87, 139264\n" \
"ds_read_b64_tr_b16 a[38:39], v214 offset:26624\n" \
"global_load_lds_dwordx4 v217, s[38:39] offset:-3072\n" \
"v_mfma_f32_16x16x32_bf16 v[28:31], a[56:59], a[8:11], v[28:31]\n" \
"ds_read_b64_tr_b16 a[40:41], v215 offset:16384\n" \
"buffer_store_dwordx4 v[132:135], v146, s[56:59], s71 offen sc1\n" \
"v_cvt_pk_bf16_f32 v120, v120, v121\n" \
"v_cvt_pk_bf16_f32 v121, v122, v123\n" \
"v_cvt_pk_bf16_f32 v122, v124, v125\n" \
"v_cvt_pk_bf16_f32 v123, v126, v127\n" \
"v_cndmask_b32 v124, v120, v122, s[92:93]\n" \
"v_mfma_f32_16x16x32_bf16 v[28:31], a[60:63], a[12:15], v[28:31]\n" \
"ds_read_b128 a[8:11], v213 offset:256\n" \
"v_cndmask_b32 v125, v121, v123, s[92:93]\n" \
"v_cndmask_b32 v126, v122, v120, s[92:93]\n" \
"v_mfma_f32_16x16x32_bf16 v[40:43], a[48:51], a[16:19], v[40:43]\n" \
"ds_read_b128 a[12:15], v213 offset:2304\n" \
"v_cndmask_b32 v127, v123, v121, s[92:93]\n" \
"v_mfma_f32_16x16x32_bf16 v[40:43], a[52:55], a[20:23], v[40:43]\n" \
"s_add_u32 s71, s87, 69632\n" \
"ds_read_b64_tr_b16 a[42:43], v215 offset:18432\n" \
"global_load_lds_dwordx4 v218, s[38:39] offset:-2048\n" \
"v_mfma_f32_16x16x32_bf16 v[44:47], a[56:59], a[16:19], v[44:47]\n" \
"ds_read_b64_tr_b16 a[44:45], v215 offset:24576\n" \
"buffer_store_dwordx4 v[124:127], v146, s[56:59], s71 offen sc1\n" \
"v_cvt_pk_bf16_f32 v112, v112, v113\n" \
"v_cvt_pk_bf16_f32 v113, v114, v115\n" \
"v_cvt_pk_bf16_f32 v114, v116, v117\n" \
"v_cvt_pk_bf16_f32 v115, v118, v119\n" \
"v_cndmask_b32 v116, v112, v114, s[92:93]\n" \
"v_mfma_f32_16x16x32_bf16 v[44:47], a[60:63], a[20:23], v[44:47]\n" \
"ds_read_b128 a[16:19], v213 offset:4096\n" \
"v_cndmask_b32 v117, v113, v115, s[92:93]\n" \
"v_cndmask_b32 v118, v114, v112, s[92:93]\n" \
"v_cndmask_b32 v119, v115, v113, s[92:93]\n" \
"v_mfma_f32_16x16x32_bf16 v[56:59], a[48:51], a[24:27], v[56:59]\n" \
"s_add_u32 s71, s87, 0\n" \
"ds_read_b128 a[20:23], v213 offset:6144\n" \
"global_load_lds_dwordx4 v219, s[38:39] offset:-1024\n" \
"v_mfma_f32_16x16x32_bf16 v[56:59], a[52:55], a[28:31], v[56:59]\n" \
"buffer_store_dwordx4 v[116:119], v146, s[56:59], s71 offen sc1\n" \
"s_add_u32 s38, s38, 0x20000\n" \
"s_addc_u32 s39, s39, 0\n" \
"ds_read_b64_tr_b16 a[46:47], v215 offset:26624\n" \
"v_mfma_f32_16x16x32_bf16 v[60:63], a[56:59], a[24:27], v[60:63]\n" \
"s_sub_u32 s75, s75, 32768\n" \
"s_cselect_b32 s75, 131072, s75\n" \
"v_mfma_f32_16x16x32_bf16 v[60:63], a[60:63], a[28:31], v[60:63]\n" \
"s_waitcnt lgkmcnt(3)\n" \
"v_mfma_f32_16x16x32_bf16 v[0:3], a[32:35], a[0:3], v[0:3]\n" \
"s_add_u32 m0, s75, s95\n" \
"ds_read_b128 a[24:27], v213 offset:4352\n" \
"v_mov_b32 v224, s60\n" \
"v_mfma_f32_16x16x32_bf16 v[0:3], a[36:39], a[4:7], v[0:3]\n" \
"s_waitcnt lgkmcnt(1)\n" \
"ds_read_b128 a[28:31], v213 offset:6400\n" \
"global_load_lds_dwordx4 v222, s[36:37] offset:0 sc1\n" \
"v_mfma_f32_16x16x32_bf16 v[4:7], a[40:43], a[0:3], v[4:7]\n" \
"s_sub_u32 s74, s74, 32768\n" \
"s_cselect_b32 s74, 131072, s74\n" \
"ds_read_b64_tr_b16 a[48:49], v214 offset:16896\n" \
"v_mfma_f32_16x16x32_bf16 v[4:7], a[44:47], a[4:7], v[4:7]\n" \
"ds_read_b64_tr_b16 a[50:51], v214 offset:18944\n" \
"v_lshlrev_b32 v225, 28, s61\n" \
"v_mfma_f32_16x16x32_bf16 v[16:19], a[32:35], a[8:11], v[16:19]\n" \
"ds_read_b64_tr_b16 a[52:53], v214 offset:25088\n" \
"v_mfma_f32_16x16x32_bf16 v[16:19], a[36:39], a[12:15], v[16:19]\n" \
"ds_read_b64_tr_b16 a[54:55], v214 offset:27136\n" \
"global_load_lds_dwordx4 v222, s[36:37] offset:1024 sc1\n" \
"v_mfma_f32_16x16x32_bf16 v[20:23], a[40:43], a[8:11], v[20:23]\n" \
"ds_read_b64_tr_b16 a[56:57], v215 offset:16896\n" \
"v_mfma_f32_16x16x32_bf16 v[20:23], a[44:47], a[12:15], v[20:23]\n" \
"ds_read_b64_tr_b16 a[58:59], v215 offset:18944\n" \
"v_mfma_f32_16x16x32_bf16 v[32:35], a[32:35], a[16:19], v[32:35]\n" \
"ds_read_b64_tr_b16 a[60:61], v215 offset:25088\n" \
"v_mfma_f32_16x16x32_bf16 v[32:35], a[36:39], a[20:23], v[32:35]\n" \
"ds_read_b64_tr_b16 a[62:63], v215 offset:27136\n" \
"global_load_lds_dwordx4 v222, s[36:37] offset:2048 sc1\n" \
"v_mfma_f32_16x16x32_bf16 v[36:39], a[40:43], a[16:19], v[36:39]\n" \
"v_add_u32 v213, s74, v211\n" \
"v_add_u32 v214, s74, v212\n" \
"v_mfma_f32_16x16x32_bf16 v[36:39], a[44:47], a[20:23], v[36:39]\n" \
"s_waitcnt lgkmcnt(3)\n" \
"v_xor_b32 v215, 32, v214\n" \
"v_mfma_f32_16x16x32_bf16 v[48:51], a[32:35], a[24:27], v[48:51]\n" \
"v_mfma_f32_16x16x32_bf16 v[48:51], a[36:39], a[28:31], v[48:51]\n" \
"global_load_lds_dwordx4 v222, s[36:37] offset:3072 sc1\n" \
"v_mfma_f32_16x16x32_bf16 v[52:55], a[40:43], a[24:27], v[52:55]\n" \
"s_waitcnt vmcnt(24) lgkmcnt(0)\n" \
"s_add_u32 s36, s36, 0x1000\n" \
"v_mfma_f32_16x16x32_bf16 v[52:55], a[44:47], a[28:31], v[52:55]\n" \
"s_barrier\n" \
"v_mfma_f32_16x16x32_bf16 v[8:11], a[48:51], a[0:3], v[8:11]\n" \
"s_add_u32 m0, m0, 0x5000\n" \
"ds_read_b64_tr_b16 a[32:33], v214 offset:16384\n" \
"v_mfma_f32_16x16x32_bf16 v[8:11], a[52:55], a[4:7], v[8:11]\n" \
"ds_read_b64_tr_b16 a[34:35], v214 offset:18432\n" \
"global_load_lds_dwordx4 v216, s[38:39] offset:-4096\n" \
"v_mfma_f32_16x16x32_bf16 v[12:15], a[56:59], a[0:3], v[12:15]\n" \
"ds_read_b64_tr_b16 a[36:37], v214 offset:24576\n" \
"v_mfma_f32_16x16x32_bf16 v[12:15], a[60:63], a[4:7], v[12:15]\n" \
"ds_read_b128 a[0:3], v213 offset:0\n" \
"v_mfma_f32_16x16x32_bf16 v[24:27], a[48:51], a[8:11], v[24:27]\n" \
"ds_read_b128 a[4:7], v213 offset:2048\n" \
"v_mfma_f32_16x16x32_bf16 v[24:27], a[52:55], a[12:15], v[24:27]\n" \
"ds_read_b64_tr_b16 a[38:39], v214 offset:26624\n" \
"global_load_lds_dwordx4 v217, s[38:39] offset:-3072\n" \
"v_mfma_f32_16x16x32_bf16 v[28:31], a[56:59], a[8:11], v[28:31]\n" \
"ds_read_b64_tr_b16 a[40:41], v215 offset:16384\n" \
"v_mfma_f32_16x16x32_bf16 v[28:31], a[60:63], a[12:15], v[28:31]\n" \
"ds_read_b128 a[8:11], v213 offset:256\n" \
"v_mfma_f32_16x16x32_bf16 v[40:43], a[48:51], a[16:19], v[40:43]\n" \
"ds_read_b128 a[12:15], v213 offset:2304\n" \
"v_mfma_f32_16x16x32_bf16 v[40:43], a[52:55], a[20:23], v[40:43]\n" \
"ds_read_b64_tr_b16 a[42:43], v215 offset:18432\n" \
"global_load_lds_dwordx4 v218, s[38:39] offset:-2048\n" \
"v_mfma_f32_16x16x32_bf16 v[44:47], a[56:59], a[16:19], v[44:47]\n" \
"ds_read_b64_tr_b16 a[44:45], v215 offset:24576\n" \
"v_mfma_f32_16x16x32_bf16 v[44:47], a[60:63], a[20:23], v[44:47]\n" \
"ds_read_b128 a[16:19], v213 offset:4096\n" \
"v_mfma_f32_16x16x32_bf16 v[56:59], a[48:51], a[24:27], v[56:59]\n" \
"ds_read_b128 a[20:23], v213 offset:6144\n" \
"global_load_lds_dwordx4 v219, s[38:39] offset:-1024\n" \
"v_mfma_f32_16x16x32_bf16 v[56:59], a[52:55], a[28:31], v[56:59]\n" \
"s_add_u32 s38, s38, 0x20000\n" \
"s_addc_u32 s39, s39, 0\n" \
"ds_read_b64_tr_b16 a[46:47], v215 offset:26624\n" \
"v_mfma_f32_16x16x32_bf16 v[60:63], a[56:59], a[24:27], v[60:63]\n" \
"s_sub_u32 s75, s75, 32768\n" \
"s_cselect_b32 s75, 131072, s75\n" \
"v_mfma_f32_16x16x32_bf16 v[60:63], a[60:63], a[28:31], v[60:63]\n" \
"s_waitcnt lgkmcnt(3)\n" \
"v_mfma_f32_16x16x32_bf16 v[0:3], a[32:35], a[0:3], v[0:3]\n" \
"ds_read_b128 a[24:27], v213 offset:4352\n" \
"s_add_u32 m0, s75, s95\n" \
"v_mfma_f32_16x16x32_bf16 v[0:3], a[36:39], a[4:7], v[0:3]\n" \
"s_waitcnt lgkmcnt(1)\n" \
"ds_read_b128 a[28:31], v213 offset:6400\n" \
"global_load_lds_dwordx4 v222, s[36:37] offset:0 sc1\n" \
"v_mfma_f32_16x16x32_bf16 v[4:7], a[40:43], a[0:3], v[4:7]\n" \
"s_sub_u32 s74, s74, 32768\n" \
"s_cselect_b32 s74, 131072, s74\n" \
"ds_read_b64_tr_b16 a[48:49], v214 offset:16896\n" \
"v_mfma_f32_16x16x32_bf16 v[4:7], a[44:47], a[4:7], v[4:7]\n" \
"ds_read_b64_tr_b16 a[50:51], v214 offset:18944\n" \
"v_mfma_f32_16x16x32_bf16 v[16:19], a[32:35], a[8:11], v[16:19]\n" \
"ds_read_b64_tr_b16 a[52:53], v214 offset:25088\n" \
"v_mfma_f32_16x16x32_bf16 v[16:19], a[36:39], a[12:15], v[16:19]\n" \
"ds_read_b64_tr_b16 a[54:55], v214 offset:27136\n" \
"global_load_lds_dwordx4 v222, s[36:37] offset:1024 sc1\n" \
"v_mfma_f32_16x16x32_bf16 v[20:23], a[40:43], a[8:11], v[20:23]\n" \
"ds_read_b64_tr_b16 a[56:57], v215 offset:16896\n" \
"v_mfma_f32_16x16x32_bf16 v[20:23], a[44:47], a[12:15], v[20:23]\n" \
"ds_read_b64_tr_b16 a[58:59], v215 offset:18944\n" \
"v_mfma_f32_16x16x32_bf16 v[32:35], a[32:35], a[16:19], v[32:35]\n" \
"ds_read_b64_tr_b16 a[60:61], v215 offset:25088\n" \
"v_mfma_f32_16x16x32_bf16 v[32:35], a[36:39], a[20:23], v[32:35]\n" \
"ds_read_b64_tr_b16 a[62:63], v215 offset:27136\n" \
"Lpg12166_%=:\n" \
"s_cbranch_execz Lpg13279_%=\n" \
"global_load_lds_dwordx4 v222, s[36:37] offset:2048 sc1\n" \
"v_mfma_f32_16x16x32_bf16 v[36:39], a[40:43], a[16:19], v[36:39]\n" \
"v_add_u32 v213, s74, v211\n" \
"v_add_u32 v214, s74, v212\n" \
"v_mfma_f32_16x16x32_bf16 v[36:39], a[44:47], a[20:23], v[36:39]\n" \
"s_waitcnt lgkmcnt(3)\n" \
"v_xor_b32 v215, 32, v214\n" \
"v_mfma_f32_16x16x32_bf16 v[48:51], a[32:35], a[24:27], v[48:51]\n" \
"v_mfma_f32_16x16x32_bf16 v[48:51], a[36:39], a[28:31], v[48:51]\n" \
"global_load_lds_dwordx4 v222, s[36:37] offset:3072 sc1\n" \
"v_mfma_f32_16x16x32_bf16 v[52:55], a[40:43], a[24:27], v[52:55]\n" \
"s_waitcnt vmcnt(12) lgkmcnt(0)\n" \
"s_add_u32 s36, s36, 0x1000\n" \
"v_mfma_f32_16x16x32_bf16 v[52:55], a[44:47], a[28:31], v[52:55]\n" \
"s_barrier\n" \
"v_mfma_f32_16x16x32_bf16 v[8:11], a[48:51], a[0:3], v[8:11]\n" \
"s_add_u32 m0, m0, 0x5000\n" \
"ds_read_b64_tr_b16 a[32:33], v214 offset:16384\n" \
"buffer_store_dword v224, v225, s[56:59], s30 offen sc1\n" \
"v_mfma_f32_16x16x32_bf16 v[8:11], a[52:55], a[4:7], v[8:11]\n" \
"ds_read_b64_tr_b16 a[34:35], v214 offset:18432\n" \
"global_load_lds_dwordx4 v216, s[38:39] offset:-4096\n" \
"v_mfma_f32_16x16x32_bf16 v[12:15], a[56:59], a[0:3], v[12:15]\n" \
"ds_read_b64_tr_b16 a[36:37], v214 offset:24576\n" \
"v_mfma_f32_16x16x32_bf16 v[12:15], a[60:63], a[4:7], v[12:15]\n" \
"ds_read_b128 a[0:3], v213 offset:0\n" \
"v_mfma_f32_16x16x32_bf16 v[24:27], a[48:51], a[8:11], v[24:27]\n" \
"ds_read_b128 a[4:7], v213 offset:2048\n" \
"v_mfma_f32_16x16x32_bf16 v[24:27], a[52:55], a[12:15], v[24:27]\n" \
"ds_read_b64_tr_b16 a[38:39], v214 offset:26624\n" \
"global_load_lds_dwordx4 v217, s[38:39] offset:-3072\n" \
"v_mfma_f32_16x16x32_bf16 v[28:31], a[56:59], a[8:11], v[28:31]\n" \
"ds_read_b64_tr_b16 a[40:41], v215 offset:16384\n" \
"v_mfma_f32_16x16x32_bf16 v[28:31], a[60:63], a[12:15], v[28:31]\n" \
"ds_read_b128 a[8:11], v213 offset:256\n" \
"v_mfma_f32_16x16x32_bf16 v[40:43], a[48:51], a[16:19], v[40:43]\n" \
"ds_read_b128 a[12:15], v213 offset:2304\n" \
"v_mfma_f32_16x16x32_bf16 v[40:43], a[52:55], a[20:23], v[40:43]\n" \
"ds_read_b64_tr_b16 a[42:43], v215 offset:18432\n" \
"global_load_lds_dwordx4 v218, s[38:39] offset:-2048\n" \
"v_mfma_f32_16x16x32_bf16 v[44:47], a[56:59], a[16:19], v[44:47]\n" \
"ds_read_b64_tr_b16 a[44:45], v215 offset:24576\n" \
"v_mfma_f32_16x16x32_bf16 v[44:47], a[60:63], a[20:23], v[44:47]\n" \
"ds_read_b128 a[16:19], v213 offset:4096\n" \
"v_mfma_f32_16x16x32_bf16 v[56:59], a[48:51], a[24:27], v[56:59]\n" \
"ds_read_b128 a[20:23], v213 offset:6144\n" \
"global_load_lds_dwordx4 v219, s[38:39] offset:-1024\n" \
"v_mfma_f32_16x16x32_bf16 v[56:59], a[52:55], a[28:31], v[56:59]\n" \
"s_add_u32 s38, s38, 0x20000\n" \
"s_addc_u32 s39, s39, 0\n" \
"ds_read_b64_tr_b16 a[46:47], v215 offset:26624\n" \
"v_mfma_f32_16x16x32_bf16 v[60:63], a[56:59], a[24:27], v[60:63]\n" \
"s_sub_u32 s75, s75, 32768\n" \
"s_cselect_b32 s75, 131072, s75\n" \
"v_mfma_f32_16x16x32_bf16 v[60:63], a[60:63], a[28:31], v[60:63]\n" \
"s_waitcnt lgkmcnt(3)\n" \
"s_cmp_eq_u32 s78, 0\n" \
"s_cbranch_scc0 Lrole1_%=\n" \
"s_sub_u32 s72, s64, 14\n" \
"s_call_b64 s[68:69], Lo0_%=\n" \
"buffer_load_dword v220, off, s[56:59], s79 sc1\n" \
"s_mov_b32 s72, 2\n" \
"s_call_b64 s[68:69], Lo0_%=\n" \
"v_mfma_f32_16x16x32_bf16 v[0:3], a[32:35], a[0:3], v[0:3]\n" \
"s_lshl_b32 s71, s61, 4\n" \
"ds_read_b128 a[24:27], v213 offset:4352\n" \
"v_subrev_u32 v72, s71, v251\n" \
"v_mfma_f32_16x16x32_bf16 v[0:3], a[36:39], a[4:7], v[0:3]\n" \
"s_waitcnt lgkmcnt(1)\n" \
"ds_read_b128 a[28:31], v213 offset:6400\n" \
"v_mov_b32 v73, 0x3f80\n" \
"v_mfma_f32_16x16x32_bf16 v[4:7], a[40:43], a[0:3], v[4:7]\n" \
"s_sub_u32 s74, s74, 32768\n" \
"s_cselect_b32 s74, 131072, s74\n" \
"ds_read_b64_tr_b16 a[48:49], v214 offset:16896\n" \
"v_mfma_f32_16x16x32_bf16 v[4:7], a[44:47], a[4:7], v[4:7]\n" \
"ds_read_b64_tr_b16 a[50:51], v214 offset:18944\n" \
"v_mov_b32 v74, 0x3f800000\n" \
"v_mfma_f32_16x16x32_bf16 v[16:19], a[32:35], a[8:11], v[16:19]\n" \
"ds_read_b64_tr_b16 a[52:53], v214 offset:25088\n" \
"v_cmp_eq_u32 vcc, 0, v72\n" \
"v_mfma_f32_16x16x32_bf16 v[16:19], a[36:39], a[12:15], v[16:19]\n" \
"ds_read_b64_tr_b16 a[54:55], v214 offset:27136\n" \
"v_cmp_eq_u32 s[66:67], 1, v72\n" \
"v_mfma_f32_16x16x32_bf16 v[20:23], a[40:43], a[8:11], v[20:23]\n" \
"ds_read_b64_tr_b16 a[56:57], v215 offset:16896\n" \
"v_mov_b32 v66, 0\n" \
"v_mfma_f32_16x16x32_bf16 v[20:23], a[44:47], a[12:15], v[20:23]\n" \
"ds_read_b64_tr_b16 a[58:59], v215 offset:18944\n" \
"v_mov_b32 v68, 0\n" \
"v_mfma_f32_16x16x32_bf16 v[32:35], a[32:35], a[16:19], v[32:35]\n" \
"ds_read_b64_tr_b16 a[60:61], v215 offset:25088\n" \
"v_cndmask_b32 v64, 0, v73, vcc\n" \
"v_mfma_f32_16x16x32_bf16 v[32:35], a[36:39], a[20:23], v[32:35]\n" \
"ds_read_b64_tr_b16 a[62:63], v215 offset:27136\n" \
"v_cndmask_b32 v64, v64, v74, s[66:67]\n" \
"v_mfma_f32_16x16x32_bf16 v[36:39], a[40:43], a[16:19], v[36:39]\n" \
"v_add_u32 v213, s74, v211\n" \
"v_add_u32 v214, s74, v212\n" \
"v_mfma_f32_16x16x32_bf16 v[36:39], a[44:47], a[20:23], v[36:39]\n" \
"s_waitcnt lgkmcnt(3)\n" \
"v_xor_b32 v215, 32, v214\n" \
"v_mov_b32 v70, v64\n" \
"v_mfma_f32_16x16x32_bf16 v[48:51], a[32:35], a[24:27], v[48:51]\n" \
"v_cmp_eq_u32 vcc, 2, v72\n" \
"v_cmp_eq_u32 s[66:67], 3, v72\n" \
"v_mfma_f32_16x16x32_bf16 v[48:51], a[36:39], a[28:31], v[48:51]\n" \
"v_mov_b32 v67, 0\n" \
"v_mov_b32 v69, 0\n" \
"v_mfma_f32_16x16x32_bf16 v[52:55], a[40:43], a[24:27], v[52:55]\n" \
"s_waitcnt vmcnt(17) lgkmcnt(0)\n" \
"v_cndmask_b32 v65, 0, v73, vcc\n" \
"v_cndmask_b32 v65, v65, v74, s[66:67]\n" \
"v_mfma_f32_16x16x32_bf16 v[52:55], a[44:47], a[28:31], v[52:55]\n" \
"s_barrier\n" \
"v_mov_b32 v71, v65\n" \
"v_mfma_f32_16x16x32_bf16 v[8:11], a[48:51], a[0:3], v[8:11]\n" \
"s_lshl_b32 s83, s95, 1\n" \
"s_add_u32 s83, s83, s74\n" \
"ds_read_b64_tr_b16 a[32:33], v214 offset:16384\n" \
"v_mfma_f32_16x16x32_bf16 v[8:11], a[52:55], a[4:7], v[8:11]\n" \
"s_waitcnt vmcnt(16)\n" \
"ds_read_b64_tr_b16 a[34:35], v214 offset:18432\n" \
"v_cmp_ne_u32 vcc, s60, v220\n" \
"v_mfma_f32_16x16x32_bf16 v[12:15], a[56:59], a[0:3], v[12:15]\n" \
"ds_read_b64_tr_b16 a[36:37], v214 offset:24576\n" \
"v_mfma_f32_16x16x32_bf16 v[12:15], a[60:63], a[4:7], v[12:15]\n" \
"s_and_b32 vcc_hi, vcc_lo, 1\n" \
"s_cbranch_scc1 Lpl4_%=\n" \
"Lpr4_%=:\n" \
"ds_read_b128 a[0:3], v213 offset:0\n" \
"v_mfma_f32_16x16x32_bf16 v[24:27], a[48:51], a[8:11], v[24:27]\n" \
"ds_read_b128 a[4:7], v213 offset:2048\n" \
"v_mfma_f32_16x16x32_bf16 v[24:27], a[52:55], a[12:15], v[24:27]\n" \
"s_add_u32 s71, s76, 0\n" \
"ds_read_b64_tr_b16 a[38:39], v214 offset:26624\n" \
"buffer_load_dwordx4 v[176:179], v248, s[56:59], s71 offen offset:0 sc1\n" \
"v_mfma_f32_16x16x32_bf16 v[28:31], a[56:59], a[8:11], v[28:31]\n" \
"ds_read_b64_tr_b16 a[40:41], v215 offset:16384\n" \
"v_mfma_f32_16x16x32_bf16 v[28:31], a[60:63], a[12:15], v[28:31]\n" \
"buffer_load_dwordx4 v[180:183], v248, s[56:59], s71 offen offset:1024 sc1\n" \
"ds_read_b128 a[8:11], v213 offset:256\n" \
"v_mfma_f32_16x16x32_bf16 v[40:43], a[48:51], a[16:19], v[40:43]\n" \
"ds_read_b128 a[12:15], v213 offset:2304\n" \
"v_mfma_f32_16x16x32_bf16 v[40:43], a[52:55], a[20:23], v[40:43]\n" \
"ds_read_b64_tr_b16 a[42:43], v215 offset:18432\n" \
"v_mfma_f32_16x16x32_bf16 v[44:47], a[56:59], a[16:19], v[44:47]\n" \
"ds_read_b64_tr_b16 a[44:45], v215 offset:24576\n" \
"v_mfma_f32_16x16x32_bf16 v[44:47], a[60:63], a[20:23], v[44:47]\n" \
"ds_read_b128 a[16:19], v213 offset:4096\n" \
"v_mfma_f32_16x16x32_bf16 v[56:59], a[48:51], a[24:27], v[56:59]\n" \
"ds_read_b128 a[20:23], v213 offset:6144\n" \
"v_mfma_f32_16x16x32_bf16 v[56:59], a[52:55], a[28:31], v[56:59]\n" \
"ds_read_b64_tr_b16 a[46:47], v215 offset:26624\n" \
"v_mfma_f32_16x16x32_bf16 v[60:63], a[56:59], a[24:27], v[60:63]\n" \
"buffer_load_dwordx4 v[184:187], v248, s[56:59], s71 offen offset:2048 sc1\n" \
"v_mfma_f32_16x16x32_bf16 v[60:63], a[60:63], a[28:31], v[60:63]\n" \
"s_waitcnt lgkmcnt(3)\n" \
"buffer_load_dwordx4 v[188:191], v248, s[56:59], s71 offen offset:3072 sc1\n" \
"v_mfma_f32_16x16x32_bf16 v[0:3], a[32:35], a[0:3], v[0:3]\n" \
"s_add_u32 s71, s76, 4096\n" \
"ds_read_b128 a[24:27], v213 offset:4352\n" \
"buffer_load_dwordx4 v[192:195], v248, s[56:59], s71 offen offset:0 sc1\n" \
"v_mfma_f32_16x16x32_bf16 v[0:3], a[36:39], a[4:7], v[0:3]\n" \
"s_waitcnt lgkmcnt(1)\n" \
"ds_read_b128 a[28:31], v213 offset:6400\n" \
"v_lshrrev_b32 v132, 4, v248\n" \
"v_mfma_f32_16x16x32_bf16 v[4:7], a[40:43], a[0:3], v[4:7]\n" \
"s_sub_u32 s74, s74, 32768\n" \
"s_cselect_b32 s74, 131072, s74\n" \
"ds_read_b64_tr_b16 a[48:49], v214 offset:16896\n" \
"v_mfma_f32_16x16x32_bf16 v[4:7], a[44:47], a[4:7], v[4:7]\n" \
"ds_read_b64_tr_b16 a[50:51], v214 offset:18944\n" \
"v_and_b32 v133, 15, v132\n" \
"v_mfma_f32_16x16x32_bf16 v[16:19], a[32:35], a[8:11], v[16:19]\n" \
"ds_read_b64_tr_b16 a[52:53], v214 offset:25088\n" \
"v_mul_u32_u24 v128, 144, v133\n" \
"v_mfma_f32_16x16x32_bf16 v[16:19], a[36:39], a[12:15], v[16:19]\n" \
"ds_read_b64_tr_b16 a[54:55], v214 offset:27136\n" \
"v_lshrrev_b32 v133, 4, v132\n" \
"v_mfma_f32_16x16x32_bf16 v[20:23], a[40:43], a[8:11], v[20:23]\n" \
"ds_read_b64_tr_b16 a[56:57], v215 offset:16896\n" \
"v_lshl_add_u32 v128, v133, 3, v128\n" \
"v_mfma_f32_16x16x32_bf16 v[20:23], a[44:47], a[12:15], v[20:23]\n" \
"ds_read_b64_tr_b16 a[58:59], v215 offset:18944\n" \
"v_add_u32 v128, s83, v128\n" \
"v_mfma_f32_16x16x32_bf16 v[32:35], a[32:35], a[16:19], v[32:35]\n" \
"ds_read_b64_tr_b16 a[60:61], v215 offset:25088\n" \
"v_lshrrev_b32 v133, 3, v132\n" \
"v_mfma_f32_16x16x32_bf16 v[32:35], a[36:39], a[20:23], v[32:35]\n" \
"ds_read_b64_tr_b16 a[62:63], v215 offset:27136\n" \
"v_and_b32 v134, 7, v132\n" \
"v_mfma_f32_16x16x32_bf16 v[36:39], a[40:43], a[16:19], v[36:39]\n" \
"v_add_u32 v213, s74, v211\n" \
"v_add_u32 v214, s74, v212\n" \
"v_mfma_f32_16x16x32_bf16 v[36:39], a[44:47], a[20:23], v[36:39]\n" \
"s_waitcnt lgkmcnt(3)\n" \
"v_xor_b32 v215, 32, v214\n" \
"v_mul_u32_u24 v129, 144, v133\n" \
"v_mfma_f32_16x16x32_bf16 v[48:51], a[32:35], a[24:27], v[48:51]\n" \
"buffer_load_dwordx4 v[196:199], v248, s[56:59], s71 offen offset:1024 sc1\n" \
"v_mfma_f32_16x16x32_bf16 v[48:51], a[36:39], a[28:31], v[48:51]\n" \
"buffer_load_dwordx4 v[200:203], v248, s[56:59], s71 offen offset:2048 sc1\n" \
"v_mfma_f32_16x16x32_bf16 v[52:55], a[40:43], a[24:27], v[52:55]\n" \
"s_waitcnt vmcnt(7) lgkmcnt(0)\n" \
"buffer_load_dwordx4 v[204:207], v248, s[56:59], s71 offen offset:3072 sc1\n" \
"v_mfma_f32_16x16x32_bf16 v[52:55], a[44:47], a[28:31], v[52:55]\n" \
"s_barrier\n" \
"v_mfma_f32_16x16x32_bf16 v[8:11], a[48:51], a[0:3], v[8:11]\n" \
"s_mov_b32 s70, 0x20000000\n" \
"ds_read_b128 a[192:195], v213 offset:0\n" \
"ds_read_b64_tr_b16 a[128:129], v214 offset:16384\n" \
"v_mfma_f32_16x16x32_bf16 v[8:11], a[52:55], a[4:7], v[8:11]\n" \
"s_sub_u32 s74, s74, 32768\n" \
"s_cselect_b32 s74, 131072, s74\n" \
"ds_read_b64_tr_b16 a[130:131], v214 offset:18432\n" \
"v_mfma_f32_16x16x32_bf16 v[12:15], a[56:59], a[0:3], v[12:15]\n" \
"ds_read_b128 a[196:199], v213 offset:2048\n" \
"ds_read_b64_tr_b16 a[132:133], v214 offset:24576\n" \
"v_mfma_f32_16x16x32_bf16 v[12:15], a[60:63], a[4:7], v[12:15]\n" \
"ds_read_b64_tr_b16 a[134:135], v214 offset:26624\n" \
"v_add_u32 v226, s74, v211\n" \
"v_mfma_f32_16x16x32_bf16 v[24:27], a[48:51], a[8:11], v[24:27]\n" \
"ds_read_b128 a[200:203], v213 offset:256\n" \
"ds_read_b64_tr_b16 a[136:137], v215 offset:16384\n" \
"v_mfma_f32_16x16x32_bf16 v[24:27], a[52:55], a[12:15], v[24:27]\n" \
"ds_read_b64_tr_b16 a[138:139], v215 offset:18432\n" \
"v_add_u32 v227, s74, v212\n" \
"v_mfma_f32_16x16x32_bf16 v[28:31], a[56:59], a[8:11], v[28:31]\n" \
"ds_read_b128 a[204:207], v213 offset:2304\n" \
"ds_read_b64_tr_b16 a[140:141], v215 offset:24576\n" \
"v_mfma_f32_16x16x32_bf16 v[28:31], a[60:63], a[12:15], v[28:31]\n" \
"ds_read_b64_tr_b16 a[142:143], v215 offset:26624\n" \
"v_xor_b32 v228, 32, v227\n" \
"v_mfma_f32_16x16x32_bf16 v[40:43], a[48:51], a[16:19], v[40:43]\n" \
"s_cmp_eq_u32 s79, 65601536\n" \
"s_cselect_b32 s70, 65593344, s70\n" \
"ds_read_b64_tr_b16 a[144:145], v214 offset:16896\n" \
"v_mfma_f32_16x16x32_bf16 v[40:43], a[52:55], a[20:23], v[40:43]\n" \
"buffer_store_dword v224, v225, s[56:59], s70 offen sc1\n" \
"ds_read_b64_tr_b16 a[146:147], v214 offset:18944\n" \
"v_mfma_f32_16x16x32_bf16 v[44:47], a[56:59], a[16:19], v[44:47]\n" \
"ds_read_b64_tr_b16 a[148:149], v214 offset:25088\n" \
"v_lshl_add_u32 v129, v134, 4, v129\n" \
"v_mfma_f32_16x16x32_bf16 v[44:47], a[60:63], a[20:23], v[44:47]\n" \
"ds_read_b64_tr_b16 a[150:151], v214 offset:27136\n" \
"v_add_u32 v129, s83, v129\n" \
"v_mfma_f32_16x16x32_bf16 v[56:59], a[48:51], a[24:27], v[56:59]\n" \
"ds_read_b64_tr_b16 a[152:153], v215 offset:16896\n" \
"v_lshlrev_b32 v134, 4, v134\n" \
"v_mfma_f32_16x16x32_bf16 v[56:59], a[52:55], a[28:31], v[56:59]\n" \
"ds_read_b64_tr_b16 a[154:155], v215 offset:18944\n" \
"v_lshl_add_u32 v130, v133, 12, v134\n" \
"v_mfma_f32_16x16x32_bf16 v[60:63], a[56:59], a[24:27], v[60:63]\n" \
"ds_read_b64_tr_b16 a[156:157], v215 offset:25088\n" \
"v_mfma_f32_16x16x32_bf16 v[60:63], a[60:63], a[28:31], v[60:63]\n" \
"s_waitcnt lgkmcnt(4)\n" \
"ds_read_b64_tr_b16 a[158:159], v215 offset:27136\n" \
"v_mfma_f32_16x16x32_bf16 v[0:3], a[128:131], a[192:195], v[0:3]\n" \
"ds_read_b128 a[224:227], v226 offset:0\n" \
"ds_read_b64_tr_b16 a[160:161], v227 offset:16384\n" \
"v_mfma_f32_16x16x32_bf16 v[0:3], a[132:135], a[196:199], v[0:3]\n" \
"ds_read_b64_tr_b16 a[162:163], v227 offset:18432\n" \
"v_mfma_f32_16x16x32_bf16 v[16:19], a[128:131], a[200:203], v[16:19]\n" \
"ds_read_b64_tr_b16 a[164:165], v227 offset:24576\n" \
"v_mfma_f32_16x16x32_bf16 v[16:19], a[132:135], a[204:207], v[16:19]\n" \
"ds_read_b128 a[228:231], v226 offset:2048\n" \
"ds_read_b64_tr_b16 a[166:167], v227 offset:26624\n" \
"v_mfma_f32_16x16x32_bf16 v[4:7], a[136:139], a[192:195], v[4:7]\n" \
"ds_read_b64_tr_b16 a[168:169], v228 offset:16384\n" \
"v_mfma_f32_16x16x32_bf16 v[4:7], a[140:143], a[196:199], v[4:7]\n" \
"ds_read_b64_tr_b16 a[170:171], v228 offset:18432\n" \
"v_mfma_f32_16x16x32_bf16 v[20:23], a[136:139], a[200:203], v[20:23]\n" \
"ds_read_b128 a[232:235], v226 offset:256\n" \
"ds_read_b64_tr_b16 a[172:173], v228 offset:24576\n" \
"v_mfma_f32_16x16x32_bf16 v[20:23], a[140:143], a[204:207], v[20:23]\n" \
"s_waitcnt lgkmcnt(6)\n" \
"ds_read_b64_tr_b16 a[174:175], v228 offset:26624\n" \
"v_mfma_f32_16x16x32_bf16 v[8:11], a[144:147], a[192:195], v[8:11]\n" \
"ds_read_b64_tr_b16 a[176:177], v227 offset:16896\n" \
"v_mfma_f32_16x16x32_bf16 v[8:11], a[148:151], a[196:199], v[8:11]\n" \
"ds_read_b128 a[236:239], v226 offset:2304\n" \
"ds_read_b64_tr_b16 a[178:179], v227 offset:18944\n" \
"v_mfma_f32_16x16x32_bf16 v[24:27], a[144:147], a[200:203], v[24:27]\n" \
"ds_read_b64_tr_b16 a[180:181], v227 offset:25088\n" \
"v_mfma_f32_16x16x32_bf16 v[24:27], a[148:151], a[204:207], v[24:27]\n" \
"ds_read_b64_tr_b16 a[182:183], v227 offset:27136\n" \
"v_mfma_f32_16x16x32_bf16 v[12:15], a[152:155], a[192:195], v[12:15]\n" \
"ds_read_b64_tr_b16 a[184:185], v228 offset:16896\n" \
"v_mfma_f32_16x16x32_bf16 v[12:15], a[156:159], a[196:199], v[12:15]\n" \
"ds_read_b64_tr_b16 a[186:187], v228 offset:18944\n" \
"v_mfma_f32_16x16x32_bf16 v[28:31], a[152:155], a[200:203], v[28:31]\n" \
"ds_read_b64_tr_b16 a[188:189], v228 offset:25088\n" \
"v_mfma_f32_16x16x32_bf16 v[28:31], a[156:159], a[204:207], v[28:31]\n" \
"s_waitcnt lgkmcnt(4)\n" \
"ds_read_b64_tr_b16 a[190:191], v228 offset:27136\n" \
"v_mfma_f32_16x16x32_bf16 v[0:3], a[160:163], a[224:227], v[0:3]\n" \
"s_waitcnt vmcnt(1)\n" \
"ds_read_b128 a[208:211], v213 offset:4096\n" \
"v_mfma_f32_16x16x32_bf16 v[0:3], a[164:167], a[228:231], v[0:3]\n" \
"ds_read_b128 a[212:215], v213 offset:6144\n" \
"v_mfma_f32_16x16x32_bf16 v[0:3], v[64:67], v[176:179], v[0:3]\n" \
"ds_read_b128 a[240:243], v226 offset:4096\n" \
"v_mfma_f32_16x16x32_bf16 v[16:19], a[160:163], a[232:235], v[16:19]\n" \
"ds_read_b128 a[244:247], v226 offset:6144\n" \
"v_mfma_f32_16x16x32_bf16 v[16:19], a[164:167], a[236:239], v[16:19]\n" \
"ds_read_b128 a[216:219], v213 offset:4352\n" \
"v_mfma_f32_16x16x32_bf16 v[16:19], v[64:67], v[184:187], v[16:19]\n" \
"ds_read_b128 a[220:223], v213 offset:6400\n" \
"v_cvt_pk_bf16_f32 v0, v0, v1\n" \
"v_mfma_f32_16x16x32_bf16 v[4:7], a[168:171], a[224:227], v[4:7]\n" \
"ds_read_b128 a[248:251], v226 offset:4352\n" \
"v_cvt_pk_bf16_f32 v1, v2, v3\n" \
"v_mfma_f32_16x16x32_bf16 v[4:7], a[172:175], a[228:231], v[4:7]\n" \
"ds_read_b128 a[252:255], v226 offset:6400\n" \
"v_mfma_f32_16x16x32_bf16 v[4:7], v[68:71], v[176:179], v[4:7]\n" \
"ds_write_b64 v128, v[0:1] offset:0\n" \
"v_mfma_f32_16x16x32_bf16 v[20:23], a[168:171], a[232:235], v[20:23]\n" \
"v_cvt_pk_bf16_f32 v16, v16, v17\n" \
"v_cvt_pk_bf16_f32 v17, v18, v19\n" \
"v_mfma_f32_16x16x32_bf16 v[20:23], a[172:175], a[236:239], v[20:23]\n" \
"ds_write_b64 v128, v[16:17] offset:2304\n" \
"v_mfma_f32_16x16x32_bf16 v[20:23], v[68:71], v[184:187], v[20:23]\n" \
"s_waitcnt lgkmcnt(3)\n" \
"v_cvt_pk_bf16_f32 v4, v4, v5\n" \
"v_cvt_pk_bf16_f32 v5, v6, v7\n" \
"v_mfma_f32_16x16x32_bf16 v[8:11], a[176:179], a[224:227], v[8:11]\n" \
"ds_write_b64 v128, v[4:5] offset:32\n" \
"v_mfma_f32_16x16x32_bf16 v[8:11], a[180:183], a[228:231], v[8:11]\n" \
"v_mfma_f32_16x16x32_bf16 v[8:11], v[64:67], v[180:183], v[8:11]\n" \
"v_cvt_pk_bf16_f32 v20, v20, v21\n" \
"v_cvt_pk_bf16_f32 v21, v22, v23\n" \
"v_mfma_f32_16x16x32_bf16 v[24:27], a[176:179], a[232:235], v[24:27]\n" \
"ds_write_b64 v128, v[20:21] offset:2336\n" \
"v_mfma_f32_16x16x32_bf16 v[24:27], a[180:183], a[236:239], v[24:27]\n" \
"v_mfma_f32_16x16x32_bf16 v[24:27], v[64:67], v[188:191], v[24:27]\n" \
"v_cvt_pk_bf16_f32 v8, v8, v9\n" \
"v_cvt_pk_bf16_f32 v9, v10, v11\n" \
"v_mfma_f32_16x16x32_bf16 v[12:15], a[184:187], a[224:227], v[12:15]\n" \
"ds_write_b64 v128, v[8:9] offset:64\n" \
"v_mfma_f32_16x16x32_bf16 v[12:15], a[188:191], a[228:231], v[12:15]\n" \
"v_mfma_f32_16x16x32_bf16 v[12:15], v[68:71], v[180:183], v[12:15]\n" \
"v_cvt_pk_bf16_f32 v24, v24, v25\n" \
"v_cvt_pk_bf16_f32 v25, v26, v27\n" \
"v_mfma_f32_16x16x32_bf16 v[28:31], a[184:187], a[232:235], v[28:31]\n" \
"ds_write_b64 v128, v[24:25] offset:2368\n" \
"v_mfma_f32_16x16x32_bf16 v[28:31], a[188:191], a[236:239], v[28:31]\n" \
"v_mfma_f32_16x16x32_bf16 v[28:31], v[68:71], v[188:191], v[28:31]\n" \
"v_cvt_pk_bf16_f32 v12, v12, v13\n" \
"v_cvt_pk_bf16_f32 v13, v14, v15\n" \
"v_mfma_f32_16x16x32_bf16 v[32:35], a[128:131], a[208:211], v[32:35]\n" \
"ds_write_b64 v128, v[12:13] offset:96\n" \
"v_mfma_f32_16x16x32_bf16 v[32:35], a[132:135], a[212:215], v[32:35]\n" \
"v_mfma_f32_16x16x32_bf16 v[32:35], a[160:163], a[240:243], v[32:35]\n" \
"v_cvt_pk_bf16_f32 v28, v28, v29\n" \
"v_cvt_pk_bf16_f32 v29, v30, v31\n" \
"v_mfma_f32_16x16x32_bf16 v[32:35], a[164:167], a[244:247], v[32:35]\n" \
"ds_write_b64 v128, v[28:29] offset:2400\n" \
"v_mfma_f32_16x16x32_bf16 v[32:35], v[64:67], v[192:195], v[32:35]\n" \
"ds_read_b128 v[140:143], v129 offset:0\n" \
"v_mfma_f32_16x16x32_bf16 v[36:39], a[136:139], a[208:211], v[36:39]\n" \
"ds_read_b128 v[144:147], v129 offset:1152\n" \
"v_mfma_f32_16x16x32_bf16 v[36:39], a[140:143], a[212:215], v[36:39]\n" \
"ds_read_b128 v[148:151], v129 offset:2304\n" \
"v_mfma_f32_16x16x32_bf16 v[36:39], a[168:171], a[240:243], v[36:39]\n" \
"ds_read_b128 v[152:155], v129 offset:3456\n" \
"v_mfma_f32_16x16x32_bf16 v[36:39], a[172:175], a[244:247], v[36:39]\n" \
"v_cvt_pk_bf16_f32 v32, v32, v33\n" \
"v_cvt_pk_bf16_f32 v33, v34, v35\n" \
"v_mfma_f32_16x16x32_bf16 v[36:39], v[68:71], v[192:195], v[36:39]\n" \
"ds_write_b64 v128, v[32:33] offset:0\n" \
"v_mfma_f32_16x16x32_bf16 v[40:43], a[144:147], a[208:211], v[40:43]\n" \
"v_mfma_f32_16x16x32_bf16 v[40:43], a[148:151], a[212:215], v[40:43]\n" \
"s_add_u32 s70, s77, 0\n" \
"s_waitcnt lgkmcnt(4)\n" \
"v_mfma_f32_16x16x32_bf16 v[40:43], a[176:179], a[240:243], v[40:43]\n" \
"v_cvt_pk_bf16_f32 v36, v36, v37\n" \
"v_cvt_pk_bf16_f32 v37, v38, v39\n" \
"v_mfma_f32_16x16x32_bf16 v[40:43], a[180:183], a[244:247], v[40:43]\n" \
"buffer_store_dwordx4 v[140:143], v130, s[52:55], s70 offen sc1\n" \
"ds_write_b64 v128, v[36:37] offset:32\n" \
"v_mfma_f32_16x16x32_bf16 v[40:43], v[64:67], v[196:199], v[40:43]\n" \
"v_mfma_f32_16x16x32_bf16 v[44:47], a[152:155], a[208:211], v[44:47]\n" \
"v_mfma_f32_16x16x32_bf16 v[44:47], a[156:159], a[212:215], v[44:47]\n" \
"v_mfma_f32_16x16x32_bf16 v[44:47], a[184:187], a[240:243], v[44:47]\n" \
"s_add_u32 s70, s77, 32768\n" \
"v_cvt_pk_bf16_f32 v40, v40, v41\n" \
"v_cvt_pk_bf16_f32 v41, v42, v43\n" \
"v_mfma_f32_16x16x32_bf16 v[44:47], a[188:191], a[244:247], v[44:47]\n" \
"s_waitcnt lgkmcnt(4)\n" \
"buffer_store_dwordx4 v[144:147], v130, s[52:55], s70 offen sc1\n" \
"ds_write_b64 v128, v[40:41] offset:64\n" \
"v_mfma_f32_16x16x32_bf16 v[44:47], v[68:71], v[196:199], v[44:47]\n" \
"v_mfma_f32_16x16x32_bf16 v[48:51], a[128:131], a[216:219], v[48:51]\n" \
"v_mfma_f32_16x16x32_bf16 v[48:51], a[132:135], a[220:223], v[48:51]\n" \
"v_mfma_f32_16x16x32_bf16 v[48:51], a[160:163], a[248:251], v[48:51]\n" \
"s_add_u32 s70, s77, 65536\n" \
"v_cvt_pk_bf16_f32 v44, v44, v45\n" \
"v_cvt_pk_bf16_f32 v45, v46, v47\n" \
"v_mfma_f32_16x16x32_bf16 v[48:51], a[164:167], a[252:255], v[48:51]\n" \
"s_waitcnt lgkmcnt(4)\n" \
"buffer_store_dwordx4 v[148:151], v130, s[52:55], s70 offen sc1\n" \
"ds_write_b64 v128, v[44:45] offset:96\n" \
"v_mfma_f32_16x16x32_bf16 v[48:51], v[64:67], v[200:203], v[48:51]\n" \
"ds_read_b128 v[156:159], v129 offset:0\n" \
"v_mfma_f32_16x16x32_bf16 v[52:55], a[136:139], a[216:219], v[52:55]\n" \
"ds_read_b128 v[160:163], v129 offset:1152\n" \
"v_mfma_f32_16x16x32_bf16 v[52:55], a[140:143], a[220:223], v[52:55]\n" \
"v_mfma_f32_16x16x32_bf16 v[52:55], a[168:171], a[248:251], v[52:55]\n" \
"s_add_u32 s70, s77, 98304\n" \
"v_cvt_pk_bf16_f32 v48, v48, v49\n" \
"v_cvt_pk_bf16_f32 v49, v50, v51\n" \
"v_mfma_f32_16x16x32_bf16 v[52:55], a[172:175], a[252:255], v[52:55]\n" \
"s_waitcnt lgkmcnt(6)\n" \
"buffer_store_dwordx4 v[152:155], v130, s[52:55], s70 offen sc1\n" \
"ds_write_b64 v128, v[48:49] offset:2304\n" \
"v_mfma_f32_16x16x32_bf16 v[52:55], v[68:71], v[200:203], v[52:55]\n" \
"v_mfma_f32_16x16x32_bf16 v[56:59], a[144:147], a[216:219], v[56:59]\n" \
"v_mfma_f32_16x16x32_bf16 v[56:59], a[148:151], a[220:223], v[56:59]\n" \
"v_mfma_f32_16x16x32_bf16 v[56:59], a[176:179], a[248:251], v[56:59]\n" \
"s_add_u32 s70, s77, 131072\n" \
"v_cvt_pk_bf16_f32 v52, v52, v53\n" \
"v_cvt_pk_bf16_f32 v53, v54, v55\n" \
"v_mfma_f32_16x16x32_bf16 v[56:59], a[180:183], a[252:255], v[56:59]\n" \
"s_waitcnt lgkmcnt(2)\n" \
"buffer_store_dwordx4 v[156:159], v130, s[52:55], s70 offen sc1\n" \
"ds_write_b64 v128, v[52:53] offset:2336\n" \
"v_mfma_f32_16x16x32_bf16 v[56:59], v[64:67], v[204:207], v[56:59]\n" \
"v_mfma_f32_16x16x32_bf16 v[60:63], a[152:155], a[216:219], v[60:63]\n" \
"v_mfma_f32_16x16x32_bf16 v[60:63], a[156:159], a[220:223], v[60:63]\n" \
"v_mfma_f32_16x16x32_bf16 v[60:63], a[184:187], a[248:251], v[60:63]\n" \
"s_add_u32 s70, s77, 163840\n" \
"v_cvt_pk_bf16_f32 v56, v56, v57\n" \
"v_cvt_pk_bf16_f32 v57, v58, v59\n" \
"v_mfma_f32_16x16x32_bf16 v[60:63], a[188:191], a[252:255], v[60:63]\n" \
"s_waitcnt lgkmcnt(2)\n" \
"buffer_store_dwordx4 v[160:163], v130, s[52:55], s70 offen sc1\n" \
"ds_write_b64 v128, v[56:57] offset:2368\n" \
"v_mfma_f32_16x16x32_bf16 v[60:63], v[68:71], v[204:207], v[60:63]\n" \
"s_nop 7\n" \
"v_cvt_pk_bf16_f32 v60, v60, v61\n" \
"v_cvt_pk_bf16_f32 v61, v62, v63\n" \
"ds_write_b64 v128, v[60:61] offset:2400\n" \
"ds_read_b128 v[164:167], v129 offset:2304\n" \
"ds_read_b128 v[168:171], v129 offset:3456\n" \
"s_add_u32 s70, s77, 196608\n" \
"s_waitcnt lgkmcnt(1)\n" \
"buffer_store_dwordx4 v[164:167], v130, s[52:55], s70 offen sc1\n" \
"s_add_u32 s70, s77, 229376\n" \
"s_waitcnt lgkmcnt(0)\n" \
"buffer_store_dwordx4 v[168:171], v130, s[52:55], s70 offen sc1\n" \
"s_endpgm\n" \
"Lpg12619_%=:\n" \
"s_cbranch_execz Lpg13592_%=\n" \
"Lo0_%=:\n" \
"v_mfma_f32_16x16x32_bf16 v[0:3], a[32:35], a[0:3], v[0:3]\n" \
"ds_read_b128 a[24:27], v213 offset:4352\n" \
"s_add_u32 m0, s75, s95\n" \
"v_mfma_f32_16x16x32_bf16 v[0:3], a[36:39], a[4:7], v[0:3]\n" \
"s_waitcnt lgkmcnt(1)\n" \
"ds_read_b128 a[28:31], v213 offset:6400\n" \
"global_load_lds_dwordx4 v222, s[36:37] offset:0 sc1\n" \
"v_mfma_f32_16x16x32_bf16 v[4:7], a[40:43], a[0:3], v[4:7]\n" \
"s_sub_u32 s74, s74, 32768\n" \
"s_cselect_b32 s74, 131072, s74\n" \
"ds_read_b64_tr_b16 a[48:49], v214 offset:16896\n" \
"v_mfma_f32_16x16x32_bf16 v[4:7], a[44:47], a[4:7], v[4:7]\n" \
"ds_read_b64_tr_b16 a[50:51], v214 offset:18944\n" \
"v_mfma_f32_16x16x32_bf16 v[16:19], a[32:35], a[8:11], v[16:19]\n" \
"ds_read_b64_tr_b16 a[52:53], v214 offset:25088\n" \
"v_mfma_f32_16x16x32_bf16 v[16:19], a[36:39], a[12:15], v[16:19]\n" \
"ds_read_b64_tr_b16 a[54:55], v214 offset:27136\n" \
"global_load_lds_dwordx4 v222, s[36:37] offset:1024 sc1\n" \
"v_mfma_f32_16x16x32_bf16 v[20:23], a[40:43], a[8:11], v[20:23]\n" \
"ds_read_b64_tr_b16 a[56:57], v215 offset:16896\n" \
"v_mfma_f32_16x16x32_bf16 v[20:23], a[44:47], a[12:15], v[20:23]\n" \
"ds_read_b64_tr_b16 a[58:59], v215 offset:18944\n" \
"v_mfma_f32_16x16x32_bf16 v[32:35], a[32:35], a[16:19], v[32:35]\n" \
"ds_read_b64_tr_b16 a[60:61], v215 offset:25088\n" \
"v_mfma_f32_16x16x32_bf16 v[32:35], a[36:39], a[20:23], v[32:35]\n" \
"ds_read_b64_tr_b16 a[62:63], v215 offset:27136\n" \
"global_load_lds_dwordx4 v222, s[36:37] offset:2048 sc1\n" \
"v_mfma_f32_16x16x32_bf16 v[36:39], a[40:43], a[16:19], v[36:39]\n" \
"v_add_u32 v213, s74, v211\n" \
"v_add_u32 v214, s74, v212\n" \
"v_mfma_f32_16x16x32_bf16 v[36:39], a[44:47], a[20:23], v[36:39]\n" \
"s_waitcnt lgkmcnt(3)\n" \
"v_xor_b32 v215, 32, v214\n" \
"v_mfma_f32_16x16x32_bf16 v[48:51], a[32:35], a[24:27], v[48:51]\n" \
"v_mfma_f32_16x16x32_bf16 v[48:51], a[36:39], a[28:31], v[48:51]\n" \
"global_load_lds_dwordx4 v222, s[36:37] offset:3072 sc1\n" \
"v_mfma_f32_16x16x32_bf16 v[52:55], a[40:43], a[24:27], v[52:55]\n" \
"s_waitcnt vmcnt(20) lgkmcnt(0)\n" \
"s_add_u32 s36, s36, 0x1000\n" \
"v_mfma_f32_16x16x32_bf16 v[52:55], a[44:47], a[28:31], v[52:55]\n" \
"s_barrier\n" \
"v_mfma_f32_16x16x32_bf16 v[8:11], a[48:51], a[0:3], v[8:11]\n" \
"s_add_u32 m0, m0, 0x5000\n" \
"ds_read_b64_tr_b16 a[32:33], v214 offset:16384\n" \
"v_mfma_f32_16x16x32_bf16 v[8:11], a[52:55], a[4:7], v[8:11]\n" \
"ds_read_b64_tr_b16 a[34:35], v214 offset:18432\n" \
"global_load_lds_dwordx4 v216, s[38:39] offset:-4096\n" \
"v_mfma_f32_16x16x32_bf16 v[12:15], a[56:59], a[0:3], v[12:15]\n" \
"ds_read_b64_tr_b16 a[36:37], v214 offset:24576\n" \
"v_mfma_f32_16x16x32_bf16 v[12:15], a[60:63], a[4:7], v[12:15]\n" \
"ds_read_b128 a[0:3], v213 offset:0\n" \
"v_mfma_f32_16x16x32_bf16 v[24:27], a[48:51], a[8:11], v[24:27]\n" \
"ds_read_b128 a[4:7], v213 offset:2048\n" \
"v_mfma_f32_16x16x32_bf16 v[24:27], a[52:55], a[12:15], v[24:27]\n" \
"ds_read_b64_tr_b16 a[38:39], v214 offset:26624\n" \
"global_load_lds_dwordx4 v217, s[38:39] offset:-3072\n" \
"v_mfma_f32_16x16x32_bf16 v[28:31], a[56:59], a[8:11], v[28:31]\n" \
"ds_read_b64_tr_b16 a[40:41], v215 offset:16384\n" \
"v_mfma_f32_16x16x32_bf16 v[28:31], a[60:63], a[12:15], v[28:31]\n" \
"ds_read_b128 a[8:11], v213 offset:256\n" \
"v_mfma_f32_16x16x32_bf16 v[40:43], a[48:51], a[16:19], v[40:43]\n" \
"ds_read_b128 a[12:15], v213 offset:2304\n" \
"v_mfma_f32_16x16x32_bf16 v[40:43], a[52:55], a[20:23], v[40:43]\n" \
"ds_read_b64_tr_b16 a[42:43], v215 offset:18432\n" \
"global_load_lds_dwordx4 v218, s[38:39] offset:-2048\n" \
"v_mfma_f32_16x16x32_bf16 v[44:47], a[56:59], a[16:19], v[44:47]\n" \
"ds_read_b64_tr_b16 a[44:45], v215 offset:24576\n" \
"v_mfma_f32_16x16x32_bf16 v[44:47], a[60:63], a[20:23], v[44:47]\n" \
"ds_read_b128 a[16:19], v213 offset:4096\n" \
"global_load_lds_dwordx4 v219, s[38:39] offset:-1024\n" \
"v_mfma_f32_16x16x32_bf16 v[56:59], a[48:51], a[24:27], v[56:59]\n" \
"s_add_u32 s38, s38, 0x20000\n" \
"s_addc_u32 s39, s39, 0\n" \
"ds_read_b128 a[20:23], v213 offset:6144\n" \
"v_mfma_f32_16x16x32_bf16 v[56:59], a[52:55], a[28:31], v[56:59]\n" \
"s_sub_u32 s75, s75, 32768\n" \
"s_cselect_b32 s75, 131072, s75\n" \
"ds_read_b64_tr_b16 a[46:47], v215 offset:26624\n" \
"v_mfma_f32_16x16x32_bf16 v[60:63], a[56:59], a[24:27], v[60:63]\n" \
"s_sub_u32 s72, s72, 1\n" \
"s_cmp_lg_u32 s72, 0\n" \
"v_mfma_f32_16x16x32_bf16 v[60:63], a[60:63], a[28:31], v[60:63]\n" \
"s_waitcnt lgkmcnt(3)\n" \
"s_cbranch_scc1 Lo0_%=\n" \
"s_setpc_b64 s[68:69]\n" \
"Lrole1_%=:\n" \
"Lpg12707_%=:\n" \
"s_cbranch_execz Lcb1_%=\n" \
"s_sub_u32 s72, 50, s64\n" \
"s_call_b64 s[68:69], Lo0_%=\n" \
"buffer_load_dword v220, v221, s[56:59], s82 offen sc1\n" \
"s_mov_b32 s72, 2\n" \
"s_call_b64 s[68:69], Lo0_%=\n" \
"s_waitcnt vmcnt(16)\n" \
"v_cmp_ne_u32 vcc, s60, v220\n" \
"s_nop 1\n" \
"s_and_b32 vcc_hi, vcc_lo, 65535\n" \
"s_cbranch_scc1 Lpl5_%=\n" \
"Lpr5_%=:\n" \
"s_mov_b32 s36, s73\n" \
"s_mov_b64 s[38:39], s[40:41]\n" \
"s_mov_b32 s72, 4\n" \
"s_call_b64 s[68:69], Lo0_%=\n" \
"v_mfma_f32_16x16x32_bf16 v[64:67], a[32:35], a[0:3], 0\n" \
"ds_read_b128 a[24:27], v213 offset:4352\n" \
"s_add_u32 m0, s75, s95\n" \
"v_mfma_f32_16x16x32_bf16 v[64:67], a[36:39], a[4:7], v[64:67]\n" \
"s_waitcnt lgkmcnt(1)\n" \
"ds_read_b128 a[28:31], v213 offset:6400\n" \
"global_load_lds_dwordx4 v222, s[36:37] offset:0 sc1\n" \
"v_mfma_f32_16x16x32_bf16 v[68:71], a[40:43], a[0:3], 0\n" \
"s_sub_u32 s74, s74, 32768\n" \
"s_cselect_b32 s74, 131072, s74\n" \
"ds_read_b64_tr_b16 a[48:49], v214 offset:16896\n" \
"v_mfma_f32_16x16x32_bf16 v[68:71], a[44:47], a[4:7], v[68:71]\n" \
"Lpg12735_%=:\n" \
"s_cbranch_execz Lcb1_%=\n" \
"ds_read_b64_tr_b16 a[50:51], v214 offset:18944\n" \
"v_cvt_pk_bf16_f32 v0, v0, v1\n" \
"v_mfma_f32_16x16x32_bf16 v[80:83], a[32:35], a[8:11], 0\n" \
"ds_read_b64_tr_b16 a[52:53], v214 offset:25088\n" \
"v_cvt_pk_bf16_f32 v1, v2, v3\n" \
"v_mfma_f32_16x16x32_bf16 v[80:83], a[36:39], a[12:15], v[80:83]\n" \
"ds_read_b64_tr_b16 a[54:55], v214 offset:27136\n" \
"global_load_lds_dwordx4 v222, s[36:37] offset:1024 sc1\n" \
"v_mfma_f32_16x16x32_bf16 v[84:87], a[40:43], a[8:11], 0\n" \
"ds_read_b64_tr_b16 a[56:57], v215 offset:16896\n" \
"v_cvt_pk_bf16_f32 v2, v4, v5\n" \
"v_mfma_f32_16x16x32_bf16 v[84:87], a[44:47], a[12:15], v[84:87]\n" \
"ds_read_b64_tr_b16 a[58:59], v215 offset:18944\n" \
"v_cvt_pk_bf16_f32 v3, v6, v7\n" \
"v_mfma_f32_16x16x32_bf16 v[96:99], a[32:35], a[16:19], 0\n" \
"s_add_u32 s71, s76, 0\n" \
"ds_read_b64_tr_b16 a[60:61], v215 offset:25088\n" \
"v_mfma_f32_16x16x32_bf16 v[96:99], a[36:39], a[20:23], v[96:99]\n" \
"ds_read_b64_tr_b16 a[62:63], v215 offset:27136\n" \
"global_load_lds_dwordx4 v222, s[36:37] offset:2048 sc1\n" \
"v_mfma_f32_16x16x32_bf16 v[100:103], a[40:43], a[16:19], 0\n" \
"v_add_u32 v213, s74, v211\n" \
"v_add_u32 v214, s74, v212\n" \
"v_mfma_f32_16x16x32_bf16 v[100:103], a[44:47], a[20:23], v[100:103]\n" \
"s_waitcnt lgkmcnt(3)\n" \
"v_xor_b32 v215, 32, v214\n" \
"v_mfma_f32_16x16x32_bf16 v[112:115], a[32:35], a[24:27], 0\n" \
"v_mfma_f32_16x16x32_bf16 v[112:115], a[36:39], a[28:31], v[112:115]\n" \
"global_load_lds_dwordx4 v222, s[36:37] offset:3072 sc1\n" \
"v_mfma_f32_16x16x32_bf16 v[116:119], a[40:43], a[24:27], 0\n" \
"s_waitcnt vmcnt(20) lgkmcnt(0)\n" \
"s_add_u32 s36, s36, 0x1000\n" \
"v_mfma_f32_16x16x32_bf16 v[116:119], a[44:47], a[28:31], v[116:119]\n" \
"s_barrier\n" \
"v_mfma_f32_16x16x32_bf16 v[72:75], a[48:51], a[0:3], 0\n" \
"s_add_u32 m0, m0, 0x5000\n" \
"ds_read_b64_tr_b16 a[32:33], v214 offset:16384\n" \
"v_mfma_f32_16x16x32_bf16 v[72:75], a[52:55], a[4:7], v[72:75]\n" \
"ds_read_b64_tr_b16 a[34:35], v214 offset:18432\n" \
"global_load_lds_dwordx4 v216, s[38:39] offset:-4096\n" \
"v_mfma_f32_16x16x32_bf16 v[76:79], a[56:59], a[0:3], 0\n" \
"ds_read_b64_tr_b16 a[36:37], v214 offset:24576\n" \
"v_mfma_f32_16x16x32_bf16 v[76:79], a[60:63], a[4:7], v[76:79]\n" \
"ds_read_b128 a[0:3], v213 offset:0\n" \
"v_mfma_f32_16x16x32_bf16 v[88:91], a[48:51], a[8:11], 0\n" \
"ds_read_b128 a[4:7], v213 offset:2048\n" \
"v_mfma_f32_16x16x32_bf16 v[88:91], a[52:55], a[12:15], v[88:91]\n" \
"ds_read_b64_tr_b16 a[38:39], v214 offset:26624\n" \
"global_load_lds_dwordx4 v217, s[38:39] offset:-3072\n" \
"v_mfma_f32_16x16x32_bf16 v[92:95], a[56:59], a[8:11], 0\n" \
"buffer_store_dwordx4 v[0:3], v248, s[56:59], s71 offen offset:0 sc1\n" \
"ds_read_b64_tr_b16 a[40:41], v215 offset:16384\n" \
"v_cvt_pk_bf16_f32 v4, v8, v9\n" \
"v_cvt_pk_bf16_f32 v5, v10, v11\n" \
"v_cvt_pk_bf16_f32 v6, v12, v13\n" \
"v_mfma_f32_16x16x32_bf16 v[92:95], a[60:63], a[12:15], v[92:95]\n" \
"ds_read_b128 a[8:11], v213 offset:256\n" \
"v_cvt_pk_bf16_f32 v7, v14, v15\n" \
"v_mfma_f32_16x16x32_bf16 v[104:107], a[48:51], a[16:19], 0\n" \
"ds_read_b128 a[12:15], v213 offset:2304\n" \
"v_mfma_f32_16x16x32_bf16 v[104:107], a[52:55], a[20:23], v[104:107]\n" \
"ds_read_b64_tr_b16 a[42:43], v215 offset:18432\n" \
"global_load_lds_dwordx4 v218, s[38:39] offset:-2048\n" \
"v_mfma_f32_16x16x32_bf16 v[108:111], a[56:59], a[16:19], 0\n" \
"buffer_store_dwordx4 v[4:7], v248, s[56:59], s71 offen offset:1024 sc1\n" \
"ds_read_b64_tr_b16 a[44:45], v215 offset:24576\n" \
"v_cvt_pk_bf16_f32 v8, v16, v17\n" \
"v_cvt_pk_bf16_f32 v9, v18, v19\n" \
"v_cvt_pk_bf16_f32 v10, v20, v21\n" \
"v_mfma_f32_16x16x32_bf16 v[108:111], a[60:63], a[20:23], v[108:111]\n" \
"ds_read_b128 a[16:19], v213 offset:4096\n" \
"v_cvt_pk_bf16_f32 v11, v22, v23\n" \
"v_mfma_f32_16x16x32_bf16 v[120:123], a[48:51], a[24:27], 0\n" \
"ds_read_b128 a[20:23], v213 offset:6144\n" \
"global_load_lds_dwordx4 v219, s[38:39] offset:-1024\n" \
"v_mfma_f32_16x16x32_bf16 v[120:123], a[52:55], a[28:31], v[120:123]\n" \
"s_add_u32 s38, s38, 0x20000\n" \
"s_addc_u32 s39, s39, 0\n" \
"ds_read_b64_tr_b16 a[46:47], v215 offset:26624\n" \
"v_mfma_f32_16x16x32_bf16 v[124:127], a[56:59], a[24:27], 0\n" \
"buffer_store_dwordx4 v[8:11], v248, s[56:59], s71 offen offset:2048 sc1\n" \
"s_sub_u32 s75, s75, 32768\n" \
"s_cselect_b32 s75, 131072, s75\n" \
"v_cvt_pk_bf16_f32 v12, v24, v25\n" \
"v_cvt_pk_bf16_f32 v13, v26, v27\n" \
"v_mfma_f32_16x16x32_bf16 v[124:127], a[60:63], a[28:31], v[124:127]\n" \
"s_waitcnt lgkmcnt(3)\n" \
"v_cvt_pk_bf16_f32 v14, v28, v29\n" \
"v_cvt_pk_bf16_f32 v15, v30, v31\n" \
"v_mfma_f32_16x16x32_bf16 v[64:67], a[32:35], a[0:3], v[64:67]\n" \
"ds_read_b128 a[24:27], v213 offset:4352\n" \
"s_add_u32 m0, s75, s95\n" \
"v_mfma_f32_16x16x32_bf16 v[64:67], a[36:39], a[4:7], v[64:67]\n" \
"s_waitcnt lgkmcnt(1)\n" \
"ds_read_b128 a[28:31], v213 offset:6400\n" \
"global_load_lds_dwordx4 v222, s[36:37] offset:0 sc1\n" \
"v_mfma_f32_16x16x32_bf16 v[68:71], a[40:43], a[0:3], v[68:71]\n" \
"s_sub_u32 s74, s74, 32768\n" \
"s_cselect_b32 s74, 131072, s74\n" \
"ds_read_b64_tr_b16 a[48:49], v214 offset:16896\n" \
"v_mfma_f32_16x16x32_bf16 v[68:71], a[44:47], a[4:7], v[68:71]\n" \
"ds_read_b64_tr_b16 a[50:51], v214 offset:18944\n" \
"v_mfma_f32_16x16x32_bf16 v[80:83], a[32:35], a[8:11], v[80:83]\n" \
"ds_read_b64_tr_b16 a[52:53], v214 offset:25088\n" \
"v_mfma_f32_16x16x32_bf16 v[80:83], a[36:39], a[12:15], v[80:83]\n" \
"ds_read_b64_tr_b16 a[54:55], v214 offset:27136\n" \
"global_load_lds_dwordx4 v222, s[36:37] offset:1024 sc1\n" \
"v_mfma_f32_16x16x32_bf16 v[84:87], a[40:43], a[8:11], v[84:87]\n" \
"ds_read_b64_tr_b16 a[56:57], v215 offset:16896\n" \
"v_mfma_f32_16x16x32_bf16 v[84:87], a[44:47], a[12:15], v[84:87]\n" \
"ds_read_b64_tr_b16 a[58:59], v215 offset:18944\n" \
"v_mfma_f32_16x16x32_bf16 v[96:99], a[32:35], a[16:19], v[96:99]\n" \
"ds_read_b64_tr_b16 a[60:61], v215 offset:25088\n" \
"v_mfma_f32_16x16x32_bf16 v[96:99], a[36:39], a[20:23], v[96:99]\n" \
"ds_read_b64_tr_b16 a[62:63], v215 offset:27136\n" \
"global_load_lds_dwordx4 v222, s[36:37] offset:2048 sc1\n" \
"v_mfma_f32_16x16x32_bf16 v[100:103], a[40:43], a[16:19], v[100:103]\n" \
"v_add_u32 v213, s74, v211\n" \
"v_add_u32 v214, s74, v212\n" \
"v_mfma_f32_16x16x32_bf16 v[100:103], a[44:47], a[20:23], v[100:103]\n" \
"s_waitcnt lgkmcnt(3)\n" \
"v_xor_b32 v215, 32, v214\n" \
"v_mfma_f32_16x16x32_bf16 v[112:115], a[32:35], a[24:27], v[112:115]\n" \
"v_mfma_f32_16x16x32_bf16 v[112:115], a[36:39], a[28:31], v[112:115]\n" \
"global_load_lds_dwordx4 v222, s[36:37] offset:3072 sc1\n" \
"v_mfma_f32_16x16x32_bf16 v[116:119], a[40:43], a[24:27], v[116:119]\n" \
"s_waitcnt vmcnt(23) lgkmcnt(0)\n" \
"s_add_u32 s36, s36, 0x1000\n" \
"v_mfma_f32_16x16x32_bf16 v[116:119], a[44:47], a[28:31], v[116:119]\n" \
"s_barrier\n" \
"v_mfma_f32_16x16x32_bf16 v[72:75], a[48:51], a[0:3], v[72:75]\n" \
"s_add_u32 m0, m0, 0x5000\n" \
"ds_read_b64_tr_b16 a[32:33], v214 offset:16384\n" \
"v_mfma_f32_16x16x32_bf16 v[72:75], a[52:55], a[4:7], v[72:75]\n" \
"ds_read_b64_tr_b16 a[34:35], v214 offset:18432\n" \
"global_load_lds_dwordx4 v216, s[38:39] offset:-4096\n" \
"v_mfma_f32_16x16x32_bf16 v[76:79], a[56:59], a[0:3], v[76:79]\n" \
"ds_read_b64_tr_b16 a[36:37], v214 offset:24576\n" \
"v_mfma_f32_16x16x32_bf16 v[76:79], a[60:63], a[4:7], v[76:79]\n" \
"ds_read_b128 a[0:3], v213 offset:0\n" \
"v_mfma_f32_16x16x32_bf16 v[88:91], a[48:51], a[8:11], v[88:91]\n" \
"ds_read_b128 a[4:7], v213 offset:2048\n" \
"v_mfma_f32_16x16x32_bf16 v[88:91], a[52:55], a[12:15], v[88:91]\n" \
"ds_read_b64_tr_b16 a[38:39], v214 offset:26624\n" \
"global_load_lds_dwordx4 v217, s[38:39] offset:-3072\n" \
"v_mfma_f32_16x16x32_bf16 v[92:95], a[56:59], a[8:11], v[92:95]\n" \
"buffer_store_dwordx4 v[12:15], v248, s[56:59], s71 offen offset:3072 sc1\n" \
"ds_read_b64_tr_b16 a[40:41], v215 offset:16384\n" \
"v_cvt_pk_bf16_f32 v16, v32, v33\n" \
"v_cvt_pk_bf16_f32 v17, v34, v35\n" \
"v_cvt_pk_bf16_f32 v18, v36, v37\n" \
"v_mfma_f32_16x16x32_bf16 v[92:95], a[60:63], a[12:15], v[92:95]\n" \
"ds_read_b128 a[8:11], v213 offset:256\n" \
"v_cvt_pk_bf16_f32 v19, v38, v39\n" \
"v_mfma_f32_16x16x32_bf16 v[104:107], a[48:51], a[16:19], v[104:107]\n" \
"ds_read_b128 a[12:15], v213 offset:2304\n" \
"s_add_u32 s71, s76, 4096\n" \
"v_mfma_f32_16x16x32_bf16 v[104:107], a[52:55], a[20:23], v[104:107]\n" \
"ds_read_b64_tr_b16 a[42:43], v215 offset:18432\n" \
"global_load_lds_dwordx4 v218, s[38:39] offset:-2048\n" \
"v_mfma_f32_16x16x32_bf16 v[108:111], a[56:59], a[16:19], v[108:111]\n" \
"buffer_store_dwordx4 v[16:19], v248, s[56:59], s71 offen offset:0 sc1\n" \
"ds_read_b64_tr_b16 a[44:45], v215 offset:24576\n" \
"v_cvt_pk_bf16_f32 v20, v40, v41\n" \
"v_cvt_pk_bf16_f32 v21, v42, v43\n" \
"v_cvt_pk_bf16_f32 v22, v44, v45\n" \
"v_mfma_f32_16x16x32_bf16 v[108:111], a[60:63], a[20:23], v[108:111]\n" \
"ds_read_b128 a[16:19], v213 offset:4096\n" \
"v_cvt_pk_bf16_f32 v23, v46, v47\n" \
"v_mfma_f32_16x16x32_bf16 v[120:123], a[48:51], a[24:27], v[120:123]\n" \
"ds_read_b128 a[20:23], v213 offset:6144\n" \
"global_load_lds_dwordx4 v219, s[38:39] offset:-1024\n" \
"v_mfma_f32_16x16x32_bf16 v[120:123], a[52:55], a[28:31], v[120:123]\n" \
"s_add_u32 s38, s38, 0x20000\n" \
"s_addc_u32 s39, s39, 0\n" \
"ds_read_b64_tr_b16 a[46:47], v215 offset:26624\n" \
"v_mfma_f32_16x16x32_bf16 v[124:127], a[56:59], a[24:27], v[124:127]\n" \
"buffer_store_dwordx4 v[20:23], v248, s[56:59], s71 offen offset:1024 sc1\n" \
"s_sub_u32 s75, s75, 32768\n" \
"s_cselect_b32 s75, 131072, s75\n" \
"v_cvt_pk_bf16_f32 v24, v48, v49\n" \
"v_cvt_pk_bf16_f32 v25, v50, v51\n" \
"v_mfma_f32_16x16x32_bf16 v[124:127], a[60:63], a[28:31], v[124:127]\n" \
"s_waitcnt lgkmcnt(3)\n" \
"v_cvt_pk_bf16_f32 v26, v52, v53\n" \
"v_cvt_pk_bf16_f32 v27, v54, v55\n" \
"v_mfma_f32_16x16x32_bf16 v[64:67], a[32:35], a[0:3], v[64:67]\n" \
"ds_read_b128 a[24:27], v213 offset:4352\n" \
"s_add_u32 m0, s75, s95\n" \
"v_mfma_f32_16x16x32_bf16 v[64:67], a[36:39], a[4:7], v[64:67]\n" \
"s_waitcnt lgkmcnt(1)\n" \
"ds_read_b128 a[28:31], v213 offset:6400\n" \
"global_load_lds_dwordx4 v222, s[36:37] offset:0 sc1\n" \
"v_mfma_f32_16x16x32_bf16 v[68:71], a[40:43], a[0:3], v[68:71]\n" \
"s_sub_u32 s74, s74, 32768\n" \
"s_cselect_b32 s74, 131072, s74\n" \
"ds_read_b64_tr_b16 a[48:49], v214 offset:16896\n" \
"v_mfma_f32_16x16x32_bf16 v[68:71], a[44:47], a[4:7], v[68:71]\n" \
"ds_read_b64_tr_b16 a[50:51], v214 offset:18944\n" \
"v_mfma_f32_16x16x32_bf16 v[80:83], a[32:35], a[8:11], v[80:83]\n" \
"ds_read_b64_tr_b16 a[52:53], v214 offset:25088\n" \
"v_mfma_f32_16x16x32_bf16 v[80:83], a[36:39], a[12:15], v[80:83]\n" \
"ds_read_b64_tr_b16 a[54:55], v214 offset:27136\n" \
"global_load_lds_dwordx4 v222, s[36:37] offset:1024 sc1\n" \
"v_mfma_f32_16x16x32_bf16 v[84:87], a[40:43], a[8:11], v[84:87]\n" \
"ds_read_b64_tr_b16 a[56:57], v215 offset:16896\n" \
"v_mfma_f32_16x16x32_bf16 v[84:87], a[44:47], a[12:15], v[84:87]\n" \
"ds_read_b64_tr_b16 a[58:59], v215 offset:18944\n" \
"v_mfma_f32_16x16x32_bf16 v[96:99], a[32:35], a[16:19], v[96:99]\n" \
"ds_read_b64_tr_b16 a[60:61], v215 offset:25088\n" \
"v_mfma_f32_16x16x32_bf16 v[96:99], a[36:39], a[20:23], v[96:99]\n" \
"ds_read_b64_tr_b16 a[62:63], v215 offset:27136\n" \
"global_load_lds_dwordx4 v222, s[36:37] offset:2048 sc1\n" \
"v_mfma_f32_16x16x32_bf16 v[100:103], a[40:43], a[16:19], v[100:103]\n" \
"v_add_u32 v213, s74, v211\n" \
"v_add_u32 v214, s74, v212\n" \
"v_mfma_f32_16x16x32_bf16 v[100:103], a[44:47], a[20:23], v[100:103]\n" \
"s_waitcnt lgkmcnt(3)\n" \
"v_xor_b32 v215, 32, v214\n" \
"v_mfma_f32_16x16x32_bf16 v[112:115], a[32:35], a[24:27], v[112:115]\n" \
"v_mfma_f32_16x16x32_bf16 v[112:115], a[36:39], a[28:31], v[112:115]\n" \
"global_load_lds_dwordx4 v222, s[36:37] offset:3072 sc1\n" \
"v_mfma_f32_16x16x32_bf16 v[116:119], a[40:43], a[24:27], v[116:119]\n" \
"s_waitcnt vmcnt(26) lgkmcnt(0)\n" \
"s_add_u32 s36, s36, 0x1000\n" \
"v_mfma_f32_16x16x32_bf16 v[116:119], a[44:47], a[28:31], v[116:119]\n" \
"s_barrier\n" \
"v_mfma_f32_16x16x32_bf16 v[72:75], a[48:51], a[0:3], v[72:75]\n" \
"s_add_u32 m0, m0, 0x5000\n" \
"ds_read_b64_tr_b16 a[32:33], v214 offset:16384\n" \
"v_mfma_f32_16x16x32_bf16 v[72:75], a[52:55], a[4:7], v[72:75]\n" \
"ds_read_b64_tr_b16 a[34:35], v214 offset:18432\n" \
"global_load_lds_dwordx4 v216, s[38:39] offset:-4096\n" \
"v_mfma_f32_16x16x32_bf16 v[76:79], a[56:59], a[0:3], v[76:79]\n" \
"ds_read_b64_tr_b16 a[36:37], v214 offset:24576\n" \
"v_mfma_f32_16x16x32_bf16 v[76:79], a[60:63], a[4:7], v[76:79]\n" \
"ds_read_b128 a[0:3], v213 offset:0\n" \
"v_mfma_f32_16x16x32_bf16 v[88:91], a[48:51], a[8:11], v[88:91]\n" \
"ds_read_b128 a[4:7], v213 offset:2048\n" \
"v_mfma_f32_16x16x32_bf16 v[88:91], a[52:55], a[12:15], v[88:91]\n" \
"ds_read_b64_tr_b16 a[38:39], v214 offset:26624\n" \
"global_load_lds_dwordx4 v217, s[38:39] offset:-3072\n" \
"v_mfma_f32_16x16x32_bf16 v[92:95], a[56:59], a[8:11], v[92:95]\n" \
"buffer_store_dwordx4 v[24:27], v248, s[56:59], s71 offen offset:2048 sc1\n" \
"ds_read_b64_tr_b16 a[40:41], v215 offset:16384\n" \
"v_cvt_pk_bf16_f32 v28, v56, v57\n" \
"v_cvt_pk_bf16_f32 v29, v58, v59\n" \
"v_cvt_pk_bf16_f32 v30, v60, v61\n" \
"v_mfma_f32_16x16x32_bf16 v[92:95], a[60:63], a[12:15], v[92:95]\n" \
"ds_read_b128 a[8:11], v213 offset:256\n" \
"v_cvt_pk_bf16_f32 v31, v62, v63\n" \
"v_mfma_f32_16x16x32_bf16 v[104:107], a[48:51], a[16:19], v[104:107]\n" \
"ds_read_b128 a[12:15], v213 offset:2304\n" \
"v_mfma_f32_16x16x32_bf16 v[104:107], a[52:55], a[20:23], v[104:107]\n" \
"ds_read_b64_tr_b16 a[42:43], v215 offset:18432\n" \
"global_load_lds_dwordx4 v218, s[38:39] offset:-2048\n" \
"v_mfma_f32_16x16x32_bf16 v[108:111], a[56:59], a[16:19], v[108:111]\n" \
"buffer_store_dwordx4 v[28:31], v248, s[56:59], s71 offen offset:3072 sc1\n" \
"ds_read_b64_tr_b16 a[44:45], v215 offset:24576\n" \
"v_mfma_f32_16x16x32_bf16 v[108:111], a[60:63], a[20:23], v[108:111]\n" \
"ds_read_b128 a[16:19], v213 offset:4096\n" \
"v_mfma_f32_16x16x32_bf16 v[120:123], a[48:51], a[24:27], v[120:123]\n" \
"ds_read_b128 a[20:23], v213 offset:6144\n" \
"global_load_lds_dwordx4 v219, s[38:39] offset:-1024\n" \
"v_mfma_f32_16x16x32_bf16 v[120:123], a[52:55], a[28:31], v[120:123]\n" \
"s_add_u32 s38, s38, 0x20000\n" \
"s_addc_u32 s39, s39, 0\n" \
"ds_read_b64_tr_b16 a[46:47], v215 offset:26624\n" \
"v_mfma_f32_16x16x32_bf16 v[124:127], a[56:59], a[24:27], v[124:127]\n" \
"s_sub_u32 s75, s75, 32768\n" \
"s_cselect_b32 s75, 131072, s75\n" \
"v_mfma_f32_16x16x32_bf16 v[124:127], a[60:63], a[28:31], v[124:127]\n" \
"s_waitcnt lgkmcnt(3)\n" \
"v_mfma_f32_16x16x32_bf16 v[64:67], a[32:35], a[0:3], v[64:67]\n" \
"ds_read_b128 a[24:27], v213 offset:4352\n" \
"s_add_u32 m0, s75, s95\n" \
"v_mfma_f32_16x16x32_bf16 v[64:67], a[36:39], a[4:7], v[64:67]\n" \
"s_waitcnt lgkmcnt(1)\n" \
"ds_read_b128 a[28:31], v213 offset:6400\n" \
"global_load_lds_dwordx4 v222, s[36:37] offset:0 sc1\n" \
"v_mfma_f32_16x16x32_bf16 v[68:71], a[40:43], a[0:3], v[68:71]\n" \
"s_sub_u32 s74, s74, 32768\n" \
"s_cselect_b32 s74, 131072, s74\n" \
"ds_read_b64_tr_b16 a[48:49], v214 offset:16896\n" \
"v_mfma_f32_16x16x32_bf16 v[68:71], a[44:47], a[4:7], v[68:71]\n" \
"ds_read_b64_tr_b16 a[50:51], v214 offset:18944\n" \
"v_mfma_f32_16x16x32_bf16 v[80:83], a[32:35], a[8:11], v[80:83]\n" \
"ds_read_b64_tr_b16 a[52:53], v214 offset:25088\n" \
"v_mfma_f32_16x16x32_bf16 v[80:83], a[36:39], a[12:15], v[80:83]\n" \
"ds_read_b64_tr_b16 a[54:55], v214 offset:27136\n" \
"global_load_lds_dwordx4 v222, s[36:37] offset:1024 sc1\n" \
"v_mfma_f32_16x16x32_bf16 v[84:87], a[40:43], a[8:11], v[84:87]\n" \
"ds_read_b64_tr_b16 a[56:57], v215 offset:16896\n" \
"v_mfma_f32_16x16x32_bf16 v[84:87], a[44:47], a[12:15], v[84:87]\n" \
"ds_read_b64_tr_b16 a[58:59], v215 offset:18944\n" \
"v_mfma_f32_16x16x32_bf16 v[96:99], a[32:35], a[16:19], v[96:99]\n" \
"ds_read_b64_tr_b16 a[60:61], v215 offset:25088\n" \
"v_mfma_f32_16x16x32_bf16 v[96:99], a[36:39], a[20:23], v[96:99]\n" \
"ds_read_b64_tr_b16 a[62:63], v215 offset:27136\n" \
"global_load_lds_dwordx4 v222, s[36:37] offset:2048 sc1\n" \
"v_mfma_f32_16x16x32_bf16 v[100:103], a[40:43], a[16:19], v[100:103]\n" \
"v_add_u32 v213, s74, v211\n" \
"v_add_u32 v214, s74, v212\n" \
"v_mfma_f32_16x16x32_bf16 v[100:103], a[44:47], a[20:23], v[100:103]\n" \
"s_waitcnt lgkmcnt(3)\n" \
"v_xor_b32 v215, 32, v214\n" \
"v_mfma_f32_16x16x32_bf16 v[112:115], a[32:35], a[24:27], v[112:115]\n" \
"v_mfma_f32_16x16x32_bf16 v[112:115], a[36:39], a[28:31], v[112:115]\n" \
"global_load_lds_dwordx4 v222, s[36:37] offset:3072 sc1\n" \
"v_mfma_f32_16x16x32_bf16 v[116:119], a[40:43], a[24:27], v[116:119]\n" \
"s_waitcnt vmcnt(26) lgkmcnt(0)\n" \
"s_add_u32 s36, s36, 0x1000\n" \
"v_mfma_f32_16x16x32_bf16 v[116:119], a[44:47], a[28:31], v[116:119]\n" \
"s_barrier\n" \
"v_mfma_f32_16x16x32_bf16 v[72:75], a[48:51], a[0:3], v[72:75]\n" \
"s_add_u32 m0, m0, 0x5000\n" \
"ds_read_b64_tr_b16 a[32:33], v214 offset:16384\n" \
"v_mfma_f32_16x16x32_bf16 v[72:75], a[52:55], a[4:7], v[72:75]\n" \
"ds_read_b64_tr_b16 a[34:35], v214 offset:18432\n" \
"global_load_lds_dwordx4 v216, s[38:39] offset:-4096\n" \
"v_mfma_f32_16x16x32_bf16 v[76:79], a[56:59], a[0:3], v[76:79]\n" \
"ds_read_b64_tr_b16 a[36:37], v214 offset:24576\n" \
"v_mfma_f32_16x16x32_bf16 v[76:79], a[60:63], a[4:7], v[76:79]\n" \
"ds_read_b128 a[0:3], v213 offset:0\n" \
"v_mfma_f32_16x16x32_bf16 v[88:91], a[48:51], a[8:11], v[88:91]\n" \
"ds_read_b128 a[4:7], v213 offset:2048\n" \
"v_mfma_f32_16x16x32_bf16 v[88:91], a[52:55], a[12:15], v[88:91]\n" \
"ds_read_b64_tr_b16 a[38:39], v214 offset:26624\n" \
"global_load_lds_dwordx4 v217, s[38:39] offset:-3072\n" \
"v_mfma_f32_16x16x32_bf16 v[92:95], a[56:59], a[8:11], v[92:95]\n" \
"ds_read_b64_tr_b16 a[40:41], v215 offset:16384\n" \
"v_mfma_f32_16x16x32_bf16 v[92:95], a[60:63], a[12:15], v[92:95]\n" \
"ds_read_b128 a[8:11], v213 offset:256\n" \
"v_mfma_f32_16x16x32_bf16 v[104:107], a[48:51], a[16:19], v[104:107]\n" \
"ds_read_b128 a[12:15], v213 offset:2304\n" \
"v_mfma_f32_16x16x32_bf16 v[104:107], a[52:55], a[20:23], v[104:107]\n" \
"ds_read_b64_tr_b16 a[42:43], v215 offset:18432\n" \
"global_load_lds_dwordx4 v218, s[38:39] offset:-2048\n" \
"v_mfma_f32_16x16x32_bf16 v[108:111], a[56:59], a[16:19], v[108:111]\n" \
"ds_read_b64_tr_b16 a[44:45], v215 offset:24576\n" \
"v_mfma_f32_16x16x32_bf16 v[108:111], a[60:63], a[20:23], v[108:111]\n" \
"ds_read_b128 a[16:19], v213 offset:4096\n" \
"v_mfma_f32_16x16x32_bf16 v[120:123], a[48:51], a[24:27], v[120:123]\n" \
"ds_read_b128 a[20:23], v213 offset:6144\n" \
"global_load_lds_dwordx4 v219, s[38:39] offset:-1024\n" \
"v_mfma_f32_16x16x32_bf16 v[120:123], a[52:55], a[28:31], v[120:123]\n" \
"s_add_u32 s38, s38, 0x20000\n" \
"s_addc_u32 s39, s39, 0\n" \
"ds_read_b64_tr_b16 a[46:47], v215 offset:26624\n" \
"v_mfma_f32_16x16x32_bf16 v[124:127], a[56:59], a[24:27], v[124:127]\n" \
"s_sub_u32 s75, s75, 32768\n" \
"s_cselect_b32 s75, 131072, s75\n" \
"v_mfma_f32_16x16x32_bf16 v[124:127], a[60:63], a[28:31], v[124:127]\n" \
"s_waitcnt lgkmcnt(3)\n" \
"v_mfma_f32_16x16x32_bf16 v[64:67], a[32:35], a[0:3], v[64:67]\n" \
"ds_read_b128 a[24:27], v213 offset:4352\n" \
"s_add_u32 m0, s75, s95\n" \
"v_mfma_f32_16x16x32_bf16 v[64:67], a[36:39], a[4:7], v[64:67]\n" \
"s_waitcnt lgkmcnt(1)\n" \
"ds_read_b128 a[28:31], v213 offset:6400\n" \
"global_load_lds_dwordx4 v222, s[36:37] offset:0 sc1\n" \
"v_mfma_f32_16x16x32_bf16 v[68:71], a[40:43], a[0:3], v[68:71]\n" \
"s_sub_u32 s74, s74, 32768\n" \
"s_cselect_b32 s74, 131072, s74\n" \
"ds_read_b64_tr_b16 a[48:49], v214 offset:16896\n" \
"v_mfma_f32_16x16x32_bf16 v[68:71], a[44:47], a[4:7], v[68:71]\n" \
"ds_read_b64_tr_b16 a[50:51], v214 offset:18944\n" \
"v_mfma_f32_16x16x32_bf16 v[80:83], a[32:35], a[8:11], v[80:83]\n" \
"ds_read_b64_tr_b16 a[52:53], v214 offset:25088\n" \
"v_mfma_f32_16x16x32_bf16 v[80:83], a[36:39], a[12:15], v[80:83]\n" \
"ds_read_b64_tr_b16 a[54:55], v214 offset:27136\n" \
"global_load_lds_dwordx4 v222, s[36:37] offset:1024 sc1\n" \
"v_mfma_f32_16x16x32_bf16 v[84:87], a[40:43], a[8:11], v[84:87]\n" \
"ds_read_b64_tr_b16 a[56:57], v215 offset:16896\n" \
"v_mfma_f32_16x16x32_bf16 v[84:87], a[44:47], a[12:15], v[84:87]\n" \
"ds_read_b64_tr_b16 a[58:59], v215 offset:18944\n" \
"v_mfma_f32_16x16x32_bf16 v[96:99], a[32:35], a[16:19], v[96:99]\n" \
"ds_read_b64_tr_b16 a[60:61], v215 offset:25088\n" \
"v_mfma_f32_16x16x32_bf16 v[96:99], a[36:39], a[20:23], v[96:99]\n" \
"ds_read_b64_tr_b16 a[62:63], v215 offset:27136\n" \
"global_load_lds_dwordx4 v222, s[36:37] offset:2048 sc1\n" \
"v_mfma_f32_16x16x32_bf16 v[100:103], a[40:43], a[16:19], v[100:103]\n" \
"v_add_u32 v213, s74, v211\n" \
"v_add_u32 v214, s74, v212\n" \
"v_mfma_f32_16x16x32_bf16 v[100:103], a[44:47], a[20:23], v[100:103]\n" \
"s_waitcnt lgkmcnt(3)\n" \
"v_xor_b32 v215, 32, v214\n" \
"v_mfma_f32_16x16x32_bf16 v[112:115], a[32:35], a[24:27], v[112:115]\n" \
"v_mfma_f32_16x16x32_bf16 v[112:115], a[36:39], a[28:31], v[112:115]\n" \
"global_load_lds_dwordx4 v222, s[36:37] offset:3072 sc1\n" \
"v_mfma_f32_16x16x32_bf16 v[116:119], a[40:43], a[24:27], v[116:119]\n" \
"s_waitcnt vmcnt(23) lgkmcnt(0)\n" \
"s_add_u32 s36, s36, 0x1000\n" \
"v_mfma_f32_16x16x32_bf16 v[116:119], a[44:47], a[28:31], v[116:119]\n" \
"s_barrier\n" \
"v_mfma_f32_16x16x32_bf16 v[72:75], a[48:51], a[0:3], v[72:75]\n" \
"s_add_u32 m0, m0, 0x5000\n" \
"ds_read_b64_tr_b16 a[32:33], v214 offset:16384\n" \
"v_mfma_f32_16x16x32_bf16 v[72:75], a[52:55], a[4:7], v[72:75]\n" \
"ds_read_b64_tr_b16 a[34:35], v214 offset:18432\n" \
"global_load_lds_dwordx4 v216, s[38:39] offset:-4096\n" \
"v_mfma_f32_16x16x32_bf16 v[76:79], a[56:59], a[0:3], v[76:79]\n" \
"ds_read_b64_tr_b16 a[36:37], v214 offset:24576\n" \
"v_mfma_f32_16x16x32_bf16 v[76:79], a[60:63], a[4:7], v[76:79]\n" \
"ds_read_b128 a[0:3], v213 offset:0\n" \
"v_mfma_f32_16x16x32_bf16 v[88:91], a[48:51], a[8:11], v[88:91]\n" \
"ds_read_b128 a[4:7], v213 offset:2048\n" \
"v_mfma_f32_16x16x32_bf16 v[88:91], a[52:55], a[12:15], v[88:91]\n" \
"ds_read_b64_tr_b16 a[38:39], v214 offset:26624\n" \
"global_load_lds_dwordx4 v217, s[38:39] offset:-3072\n" \
"v_mfma_f32_16x16x32_bf16 v[92:95], a[56:59], a[8:11], v[92:95]\n" \
"ds_read_b64_tr_b16 a[40:41], v215 offset:16384\n" \
"v_mfma_f32_16x16x32_bf16 v[92:95], a[60:63], a[12:15], v[92:95]\n" \
"ds_read_b128 a[8:11], v213 offset:256\n" \
"v_mfma_f32_16x16x32_bf16 v[104:107], a[48:51], a[16:19], v[104:107]\n" \
"ds_read_b128 a[12:15], v213 offset:2304\n" \
"v_mfma_f32_16x16x32_bf16 v[104:107], a[52:55], a[20:23], v[104:107]\n" \
"ds_read_b64_tr_b16 a[42:43], v215 offset:18432\n" \
"global_load_lds_dwordx4 v218, s[38:39] offset:-2048\n" \
"v_mfma_f32_16x16x32_bf16 v[108:111], a[56:59], a[16:19], v[108:111]\n" \
"ds_read_b64_tr_b16 a[44:45], v215 offset:24576\n" \
"v_mfma_f32_16x16x32_bf16 v[108:111], a[60:63], a[20:23], v[108:111]\n" \
"ds_read_b128 a[16:19], v213 offset:4096\n" \
"v_mfma_f32_16x16x32_bf16 v[120:123], a[48:51], a[24:27], v[120:123]\n" \
"ds_read_b128 a[20:23], v213 offset:6144\n" \
"global_load_lds_dwordx4 v219, s[38:39] offset:-1024\n" \
"v_mfma_f32_16x16x32_bf16 v[120:123], a[52:55], a[28:31], v[120:123]\n" \
"s_add_u32 s38, s38, 0x20000\n" \
"s_addc_u32 s39, s39, 0\n" \
"ds_read_b64_tr_b16 a[46:47], v215 offset:26624\n" \
"v_mfma_f32_16x16x32_bf16 v[124:127], a[56:59], a[24:27], v[124:127]\n" \
"s_sub_u32 s75, s75, 32768\n" \
"s_cselect_b32 s75, 131072, s75\n" \
"v_mfma_f32_16x16x32_bf16 v[124:127], a[60:63], a[28:31], v[124:127]\n" \
"s_waitcnt lgkmcnt(3)\n" \
"v_mfma_f32_16x16x32_bf16 v[64:67], a[32:35], a[0:3], v[64:67]\n" \
"ds_read_b128 a[24:27], v213 offset:4352\n" \
"s_add_u32 m0, s75, s95\n" \
"v_mfma_f32_16x16x32_bf16 v[64:67], a[36:39], a[4:7], v[64:67]\n" \
"s_waitcnt lgkmcnt(1)\n" \
"ds_read_b128 a[28:31], v213 offset:6400\n" \
"global_load_lds_dwordx4 v222, s[36:37] offset:0 sc1\n" \
"v_mfma_f32_16x16x32_bf16 v[68:71], a[40:43], a[0:3], v[68:71]\n" \
"s_sub_u32 s74, s74, 32768\n" \
"s_cselect_b32 s74, 131072, s74\n" \
"ds_read_b64_tr_b16 a[48:49], v214 offset:16896\n" \
"v_mfma_f32_16x16x32_bf16 v[68:71], a[44:47], a[4:7], v[68:71]\n" \
"ds_read_b64_tr_b16 a[50:51], v214 offset:18944\n" \
"v_mfma_f32_16x16x32_bf16 v[80:83], a[32:35], a[8:11], v[80:83]\n" \
"ds_read_b64_tr_b16 a[52:53], v214 offset:25088\n" \
"v_mfma_f32_16x16x32_bf16 v[80:83], a[36:39], a[12:15], v[80:83]\n" \
"ds_read_b64_tr_b16 a[54:55], v214 offset:27136\n" \
"global_load_lds_dwordx4 v222, s[36:37] offset:1024 sc1\n" \
"v_mfma_f32_16x16x32_bf16 v[84:87], a[40:43], a[8:11], v[84:87]\n" \
"ds_read_b64_tr_b16 a[56:57], v215 offset:16896\n" \
"v_mfma_f32_16x16x32_bf16 v[84:87], a[44:47], a[12:15], v[84:87]\n" \
"ds_read_b64_tr_b16 a[58:59], v215 offset:18944\n" \
"v_mfma_f32_16x16x32_bf16 v[96:99], a[32:35], a[16:19], v[96:99]\n" \
"ds_read_b64_tr_b16 a[60:61], v215 offset:25088\n" \
"v_mfma_f32_16x16x32_bf16 v[96:99], a[36:39], a[20:23], v[96:99]\n" \
"ds_read_b64_tr_b16 a[62:63], v215 offset:27136\n" \
"global_load_lds_dwordx4 v222, s[36:37] offset:2048 sc1\n" \
"v_mfma_f32_16x16x32_bf16 v[100:103], a[40:43], a[16:19], v[100:103]\n" \
"v_add_u32 v213, s74, v211\n" \
"v_add_u32 v214, s74, v212\n" \
"v_mfma_f32_16x16x32_bf16 v[100:103], a[44:47], a[20:23], v[100:103]\n" \
"s_waitcnt lgkmcnt(3)\n" \
"v_xor_b32 v215, 32, v214\n" \
"v_mfma_f32_16x16x32_bf16 v[112:115], a[32:35], a[24:27], v[112:115]\n" \
"v_mfma_f32_16x16x32_bf16 v[112:115], a[36:39], a[28:31], v[112:115]\n" \
"global_load_lds_dwordx4 v222, s[36:37] offset:3072 sc1\n" \
"v_mfma_f32_16x16x32_bf16 v[116:119], a[40:43], a[24:27], v[116:119]\n" \
"s_waitcnt vmcnt(20) lgkmcnt(0)\n" \
"s_add_u32 s36, s36, 0x1000\n" \
"v_mfma_f32_16x16x32_bf16 v[116:119], a[44:47], a[28:31], v[116:119]\n" \
"s_barrier\n" \
"v_mfma_f32_16x16x32_bf16 v[72:75], a[48:51], a[0:3], v[72:75]\n" \
"s_add_u32 m0, m0, 0x5000\n" \
"ds_read_b64_tr_b16 a[32:33], v214 offset:16384\n" \
"buffer_store_dword v224, v225, s[56:59], s79 offen sc1\n" \
"v_mfma_f32_16x16x32_bf16 v[72:75], a[52:55], a[4:7], v[72:75]\n" \
"ds_read_b64_tr_b16 a[34:35], v214 offset:18432\n" \
"global_load_lds_dwordx4 v216, s[38:39] offset:-4096\n" \
"v_mfma_f32_16x16x32_bf16 v[76:79], a[56:59], a[0:3], v[76:79]\n" \
"ds_read_b64_tr_b16 a[36:37], v214 offset:24576\n" \
"v_mfma_f32_16x16x32_bf16 v[76:79], a[60:63], a[4:7], v[76:79]\n" \
"ds_read_b128 a[0:3], v213 offset:0\n" \
"v_mfma_f32_16x16x32_bf16 v[88:91], a[48:51], a[8:11], v[88:91]\n" \
"ds_read_b128 a[4:7], v213 offset:2048\n" \
"v_mfma_f32_16x16x32_bf16 v[88:91], a[52:55], a[12:15], v[88:91]\n" \
"ds_read_b64_tr_b16 a[38:39], v214 offset:26624\n" \
"global_load_lds_dwordx4 v217, s[38:39] offset:-3072\n" \
"v_mfma_f32_16x16x32_bf16 v[92:95], a[56:59], a[8:11], v[92:95]\n" \
"ds_read_b64_tr_b16 a[40:41], v215 offset:16384\n" \
"v_mfma_f32_16x16x32_bf16 v[92:95], a[60:63], a[12:15], v[92:95]\n" \
"ds_read_b128 a[8:11], v213 offset:256\n" \
"v_mfma_f32_16x16x32_bf16 v[104:107], a[48:51], a[16:19], v[104:107]\n" \
"ds_read_b128 a[12:15], v213 offset:2304\n" \
"v_mfma_f32_16x16x32_bf16 v[104:107], a[52:55], a[20:23], v[104:107]\n" \
"ds_read_b64_tr_b16 a[42:43], v215 offset:18432\n" \
"global_load_lds_dwordx4 v218, s[38:39] offset:-2048\n" \
"v_mfma_f32_16x16x32_bf16 v[108:111], a[56:59], a[16:19], v[108:111]\n" \
"ds_read_b64_tr_b16 a[44:45], v215 offset:24576\n" \
"v_mfma_f32_16x16x32_bf16 v[108:111], a[60:63], a[20:23], v[108:111]\n" \
"ds_read_b128 a[16:19], v213 offset:4096\n" \
"v_mfma_f32_16x16x32_bf16 v[120:123], a[48:51], a[24:27], v[120:123]\n" \
"ds_read_b128 a[20:23], v213 offset:6144\n" \
"global_load_lds_dwordx4 v219, s[38:39] offset:-1024\n" \
"v_mfma_f32_16x16x32_bf16 v[120:123], a[52:55], a[28:31], v[120:123]\n" \
"s_add_u32 s38, s38, 0x20000\n" \
"s_addc_u32 s39, s39, 0\n" \
"ds_read_b64_tr_b16 a[46:47], v215 offset:26624\n" \
"v_mfma_f32_16x16x32_bf16 v[124:127], a[56:59], a[24:27], v[124:127]\n" \
"s_sub_u32 s75, s75, 32768\n" \
"s_cselect_b32 s75, 131072, s75\n" \
"v_mfma_f32_16x16x32_bf16 v[124:127], a[60:63], a[28:31], v[124:127]\n" \
"s_waitcnt lgkmcnt(3)\n" \
"s_mov_b32 s72, 6\n" \
"s_call_b64 s[68:69], Lo64_%=\n" \
"v_mfma_f32_16x16x32_bf16 v[64:67], a[32:35], a[0:3], v[64:67]\n" \
"ds_read_b128 a[24:27], v213 offset:4352\n" \
"v_mfma_f32_16x16x32_bf16 v[64:67], a[36:39], a[4:7], v[64:67]\n" \
"s_waitcnt lgkmcnt(1)\n" \
"ds_read_b128 a[28:31], v213 offset:6400\n" \
"v_mfma_f32_16x16x32_bf16 v[68:71], a[40:43], a[0:3], v[68:71]\n" \
"s_sub_u32 s74, s74, 32768\n" \
"s_cselect_b32 s74, 131072, s74\n" \
"ds_read_b64_tr_b16 a[48:49], v214 offset:16896\n" \
"v_mfma_f32_16x16x32_bf16 v[68:71], a[44:47], a[4:7], v[68:71]\n" \
"ds_read_b64_tr_b16 a[50:51], v214 offset:18944\n" \
"v_mfma_f32_16x16x32_bf16 v[80:83], a[32:35], a[8:11], v[80:83]\n" \
"ds_read_b64_tr_b16 a[52:53], v214 offset:25088\n" \
"v_mfma_f32_16x16x32_bf16 v[80:83], a[36:39], a[12:15], v[80:83]\n" \
"ds_read_b64_tr_b16 a[54:55], v214 offset:27136\n" \
"v_mfma_f32_16x16x32_bf16 v[84:87], a[40:43], a[8:11], v[84:87]\n" \
"ds_read_b64_tr_b16 a[56:57], v215 offset:16896\n" \
"v_mfma_f32_16x16x32_bf16 v[84:87], a[44:47], a[12:15], v[84:87]\n" \
"ds_read_b64_tr_b16 a[58:59], v215 offset:18944\n" \
"v_mfma_f32_16x16x32_bf16 v[96:99], a[32:35], a[16:19], v[96:99]\n" \
"ds_read_b64_tr_b16 a[60:61], v215 offset:25088\n" \
"v_mfma_f32_16x16x32_bf16 v[96:99], a[36:39], a[20:23], v[96:99]\n" \
"ds_read_b64_tr_b16 a[62:63], v215 offset:27136\n" \
"v_mfma_f32_16x16x32_bf16 v[100:103], a[40:43], a[16:19], v[100:103]\n" \
"Lpg13279_%=:\n" \
"s_cbranch_execz Lcb1_%=\n" \
"v_add_u32 v213, s74, v211\n" \
"v_add_u32 v214, s74, v212\n" \
"v_mfma_f32_16x16x32_bf16 v[100:103], a[44:47], a[20:23], v[100:103]\n" \
"s_waitcnt lgkmcnt(3)\n" \
"v_xor_b32 v215, 32, v214\n" \
"v_mfma_f32_16x16x32_bf16 v[112:115], a[32:35], a[24:27], v[112:115]\n" \
"v_mfma_f32_16x16x32_bf16 v[112:115], a[36:39], a[28:31], v[112:115]\n" \
"v_mfma_f32_16x16x32_bf16 v[116:119], a[40:43], a[24:27], v[116:119]\n" \
"s_waitcnt vmcnt(16) lgkmcnt(0)\n" \
"v_mfma_f32_16x16x32_bf16 v[116:119], a[44:47], a[28:31], v[116:119]\n" \
"s_barrier\n" \
"v_mfma_f32_16x16x32_bf16 v[72:75], a[48:51], a[0:3], v[72:75]\n" \
"s_lshl_b32 s83, s95, 1\n" \
"s_add_u32 s83, s83, s74\n" \
"ds_read_b64_tr_b16 a[32:33], v214 offset:16384\n" \
"v_mfma_f32_16x16x32_bf16 v[72:75], a[52:55], a[4:7], v[72:75]\n" \
"ds_read_b64_tr_b16 a[34:35], v214 offset:18432\n" \
"v_mfma_f32_16x16x32_bf16 v[76:79], a[56:59], a[0:3], v[76:79]\n" \
"ds_read_b64_tr_b16 a[36:37], v214 offset:24576\n" \
"v_mfma_f32_16x16x32_bf16 v[76:79], a[60:63], a[4:7], v[76:79]\n" \
"ds_read_b128 a[0:3], v213 offset:0\n" \
"v_mfma_f32_16x16x32_bf16 v[88:91], a[48:51], a[8:11], v[88:91]\n" \
"ds_read_b128 a[4:7], v213 offset:2048\n" \
"v_mfma_f32_16x16x32_bf16 v[88:91], a[52:55], a[12:15], v[88:91]\n" \
"ds_read_b64_tr_b16 a[38:39], v214 offset:26624\n" \
"v_mfma_f32_16x16x32_bf16 v[92:95], a[56:59], a[8:11], v[92:95]\n" \
"ds_read_b64_tr_b16 a[40:41], v215 offset:16384\n" \
"v_mfma_f32_16x16x32_bf16 v[92:95], a[60:63], a[12:15], v[92:95]\n" \
"ds_read_b128 a[8:11], v213 offset:256\n" \
"v_mfma_f32_16x16x32_bf16 v[104:107], a[48:51], a[16:19], v[104:107]\n" \
"ds_read_b128 a[12:15], v213 offset:2304\n" \
"v_mfma_f32_16x16x32_bf16 v[104:107], a[52:55], a[20:23], v[104:107]\n" \
"ds_read_b64_tr_b16 a[42:43], v215 offset:18432\n" \
"v_mfma_f32_16x16x32_bf16 v[108:111], a[56:59], a[16:19], v[108:111]\n" \
"ds_read_b64_tr_b16 a[44:45], v215 offset:24576\n" \
"v_mfma_f32_16x16x32_bf16 v[108:111], a[60:63], a[20:23], v[108:111]\n" \
"ds_read_b128 a[16:19], v213 offset:4096\n" \
"v_mfma_f32_16x16x32_bf16 v[120:123], a[48:51], a[24:27], v[120:123]\n" \
"ds_read_b128 a[20:23], v213 offset:6144\n" \
"v_mfma_f32_16x16x32_bf16 v[120:123], a[52:55], a[28:31], v[120:123]\n" \
"ds_read_b64_tr_b16 a[46:47], v215 offset:26624\n" \
"v_mfma_f32_16x16x32_bf16 v[124:127], a[56:59], a[24:27], v[124:127]\n" \
"v_mfma_f32_16x16x32_bf16 v[124:127], a[60:63], a[28:31], v[124:127]\n" \
"s_waitcnt lgkmcnt(3)\n" \
"v_mfma_f32_16x16x32_bf16 v[64:67], a[32:35], a[0:3], v[64:67]\n" \
"ds_read_b128 a[24:27], v213 offset:4352\n" \
"v_mfma_f32_16x16x32_bf16 v[64:67], a[36:39], a[4:7], v[64:67]\n" \
"s_waitcnt lgkmcnt(1)\n" \
"ds_read_b128 a[28:31], v213 offset:6400\n" \
"v_lshrrev_b32 v132, 4, v248\n" \
"v_mfma_f32_16x16x32_bf16 v[68:71], a[40:43], a[0:3], v[68:71]\n" \
"s_sub_u32 s74, s74, 32768\n" \
"s_cselect_b32 s74, 131072, s74\n" \
"ds_read_b64_tr_b16 a[48:49], v214 offset:16896\n" \
"v_mfma_f32_16x16x32_bf16 v[68:71], a[44:47], a[4:7], v[68:71]\n" \
"ds_read_b64_tr_b16 a[50:51], v214 offset:18944\n" \
"v_and_b32 v133, 15, v132\n" \
"v_mfma_f32_16x16x32_bf16 v[80:83], a[32:35], a[8:11], v[80:83]\n" \
"ds_read_b64_tr_b16 a[52:53], v214 offset:25088\n" \
"v_mul_u32_u24 v128, 144, v133\n" \
"v_mfma_f32_16x16x32_bf16 v[80:83], a[36:39], a[12:15], v[80:83]\n" \
"ds_read_b64_tr_b16 a[54:55], v214 offset:27136\n" \
"v_lshrrev_b32 v133, 4, v132\n" \
"v_mfma_f32_16x16x32_bf16 v[84:87], a[40:43], a[8:11], v[84:87]\n" \
"ds_read_b64_tr_b16 a[56:57], v215 offset:16896\n" \
"v_lshl_add_u32 v128, v133, 3, v128\n" \
"v_mfma_f32_16x16x32_bf16 v[84:87], a[44:47], a[12:15], v[84:87]\n" \
"ds_read_b64_tr_b16 a[58:59], v215 offset:18944\n" \
"v_add_u32 v128, s83, v128\n" \
"v_mfma_f32_16x16x32_bf16 v[96:99], a[32:35], a[16:19], v[96:99]\n" \
"ds_read_b64_tr_b16 a[60:61], v215 offset:25088\n" \
"v_lshrrev_b32 v133, 3, v132\n" \
"v_mfma_f32_16x16x32_bf16 v[96:99], a[36:39], a[20:23], v[96:99]\n" \
"ds_read_b64_tr_b16 a[62:63], v215 offset:27136\n" \
"v_and_b32 v134, 7, v132\n" \
"v_mfma_f32_16x16x32_bf16 v[100:103], a[40:43], a[16:19], v[100:103]\n" \
"v_add_u32 v213, s74, v211\n" \
"v_add_u32 v214, s74, v212\n" \
"v_mfma_f32_16x16x32_bf16 v[100:103], a[44:47], a[20:23], v[100:103]\n" \
"s_waitcnt lgkmcnt(3)\n" \
"v_xor_b32 v215, 32, v214\n" \
"v_mul_u32_u24 v129, 144, v133\n" \
"v_mfma_f32_16x16x32_bf16 v[112:115], a[32:35], a[24:27], v[112:115]\n" \
"v_lshl_add_u32 v129, v134, 4, v129\n" \
"v_add_u32 v129, s83, v129\n" \
"v_mfma_f32_16x16x32_bf16 v[112:115], a[36:39], a[28:31], v[112:115]\n" \
"v_lshlrev_b32 v134, 4, v134\n" \
"v_lshl_add_u32 v130, v133, 13, v134\n" \
"v_mfma_f32_16x16x32_bf16 v[116:119], a[40:43], a[24:27], v[116:119]\n" \
"s_waitcnt vmcnt(0) lgkmcnt(0)\n" \
"v_mfma_f32_16x16x32_bf16 v[116:119], a[44:47], a[28:31], v[116:119]\n" \
"s_barrier\n" \
"v_mfma_f32_16x16x32_bf16 v[72:75], a[48:51], a[0:3], v[72:75]\n" \
"ds_read_b128 a[192:195], v213 offset:0\n" \
"ds_read_b64_tr_b16 a[128:129], v214 offset:16384\n" \
"v_mfma_f32_16x16x32_bf16 v[72:75], a[52:55], a[4:7], v[72:75]\n" \
"s_sub_u32 s74, s74, 32768\n" \
"s_cselect_b32 s74, 131072, s74\n" \
"ds_read_b64_tr_b16 a[130:131], v214 offset:18432\n" \
"v_mfma_f32_16x16x32_bf16 v[76:79], a[56:59], a[0:3], v[76:79]\n" \
"ds_read_b128 a[196:199], v213 offset:2048\n" \
"ds_read_b64_tr_b16 a[132:133], v214 offset:24576\n" \
"v_mfma_f32_16x16x32_bf16 v[76:79], a[60:63], a[4:7], v[76:79]\n" \
"ds_read_b64_tr_b16 a[134:135], v214 offset:26624\n" \
"v_add_u32 v226, s74, v211\n" \
"v_mfma_f32_16x16x32_bf16 v[88:91], a[48:51], a[8:11], v[88:91]\n" \
"ds_read_b128 a[200:203], v213 offset:256\n" \
"ds_read_b64_tr_b16 a[136:137], v215 offset:16384\n" \
"v_mfma_f32_16x16x32_bf16 v[88:91], a[52:55], a[12:15], v[88:91]\n" \
"ds_read_b64_tr_b16 a[138:139], v215 offset:18432\n" \
"v_add_u32 v227, s74, v212\n" \
"v_mfma_f32_16x16x32_bf16 v[92:95], a[56:59], a[8:11], v[92:95]\n" \
"ds_read_b128 a[204:207], v213 offset:2304\n" \
"ds_read_b64_tr_b16 a[140:141], v215 offset:24576\n" \
"v_mfma_f32_16x16x32_bf16 v[92:95], a[60:63], a[12:15], v[92:95]\n" \
"ds_read_b64_tr_b16 a[142:143], v215 offset:26624\n" \
"v_xor_b32 v228, 32, v227\n" \
"v_mfma_f32_16x16x32_bf16 v[104:107], a[48:51], a[16:19], v[104:107]\n" \
"ds_read_b64_tr_b16 a[144:145], v214 offset:16896\n" \
"v_mfma_f32_16x16x32_bf16 v[104:107], a[52:55], a[20:23], v[104:107]\n" \
"ds_read_b64_tr_b16 a[146:147], v214 offset:18944\n" \
"v_mfma_f32_16x16x32_bf16 v[108:111], a[56:59], a[16:19], v[108:111]\n" \
"ds_read_b64_tr_b16 a[148:149], v214 offset:25088\n" \
"v_mfma_f32_16x16x32_bf16 v[108:111], a[60:63], a[20:23], v[108:111]\n" \
"ds_read_b64_tr_b16 a[150:151], v214 offset:27136\n" \
"v_mfma_f32_16x16x32_bf16 v[120:123], a[48:51], a[24:27], v[120:123]\n" \
"ds_read_b64_tr_b16 a[152:153], v215 offset:16896\n" \
"v_mfma_f32_16x16x32_bf16 v[120:123], a[52:55], a[28:31], v[120:123]\n" \
"ds_read_b64_tr_b16 a[154:155], v215 offset:18944\n" \
"v_mfma_f32_16x16x32_bf16 v[124:127], a[56:59], a[24:27], v[124:127]\n" \
"ds_read_b64_tr_b16 a[156:157], v215 offset:25088\n" \
"v_mfma_f32_16x16x32_bf16 v[124:127], a[60:63], a[28:31], v[124:127]\n" \
"s_waitcnt lgkmcnt(4)\n" \
"ds_read_b64_tr_b16 a[158:159], v215 offset:27136\n" \
"v_mfma_f32_16x16x32_bf16 v[64:67], a[128:131], a[192:195], v[64:67]\n" \
"ds_read_b128 a[224:227], v226 offset:0\n" \
"ds_read_b64_tr_b16 a[160:161], v227 offset:16384\n" \
"v_mfma_f32_16x16x32_bf16 v[64:67], a[132:135], a[196:199], v[64:67]\n" \
"ds_read_b64_tr_b16 a[162:163], v227 offset:18432\n" \
"v_mfma_f32_16x16x32_bf16 v[80:83], a[128:131], a[200:203], v[80:83]\n" \
"ds_read_b64_tr_b16 a[164:165], v227 offset:24576\n" \
"v_mfma_f32_16x16x32_bf16 v[80:83], a[132:135], a[204:207], v[80:83]\n" \
"ds_read_b128 a[228:231], v226 offset:2048\n" \
"ds_read_b64_tr_b16 a[166:167], v227 offset:26624\n" \
"v_mfma_f32_16x16x32_bf16 v[68:71], a[136:139], a[192:195], v[68:71]\n" \
"ds_read_b64_tr_b16 a[168:169], v228 offset:16384\n" \
"v_mfma_f32_16x16x32_bf16 v[68:71], a[140:143], a[196:199], v[68:71]\n" \
"ds_read_b64_tr_b16 a[170:171], v228 offset:18432\n" \
"v_mfma_f32_16x16x32_bf16 v[84:87], a[136:139], a[200:203], v[84:87]\n" \
"ds_read_b128 a[232:235], v226 offset:256\n" \
"ds_read_b64_tr_b16 a[172:173], v228 offset:24576\n" \
"v_mfma_f32_16x16x32_bf16 v[84:87], a[140:143], a[204:207], v[84:87]\n" \
"s_waitcnt lgkmcnt(6)\n" \
"ds_read_b64_tr_b16 a[174:175], v228 offset:26624\n" \
"v_mfma_f32_16x16x32_bf16 v[72:75], a[144:147], a[192:195], v[72:75]\n" \
"ds_read_b64_tr_b16 a[176:177], v227 offset:16896\n" \
"v_mfma_f32_16x16x32_bf16 v[72:75], a[148:151], a[196:199], v[72:75]\n" \
"ds_read_b128 a[236:239], v226 offset:2304\n" \
"ds_read_b64_tr_b16 a[178:179], v227 offset:18944\n" \
"v_mfma_f32_16x16x32_bf16 v[88:91], a[144:147], a[200:203], v[88:91]\n" \
"ds_read_b64_tr_b16 a[180:181], v227 offset:25088\n" \
"v_mfma_f32_16x16x32_bf16 v[88:91], a[148:151], a[204:207], v[88:91]\n" \
"ds_read_b64_tr_b16 a[182:183], v227 offset:27136\n" \
"v_mfma_f32_16x16x32_bf16 v[76:79], a[152:155], a[192:195], v[76:79]\n" \
"ds_read_b64_tr_b16 a[184:185], v228 offset:16896\n" \
"v_mfma_f32_16x16x32_bf16 v[76:79], a[156:159], a[196:199], v[76:79]\n" \
"ds_read_b64_tr_b16 a[186:187], v228 offset:18944\n" \
"v_mfma_f32_16x16x32_bf16 v[92:95], a[152:155], a[200:203], v[92:95]\n" \
"ds_read_b64_tr_b16 a[188:189], v228 offset:25088\n" \
"v_mfma_f32_16x16x32_bf16 v[92:95], a[156:159], a[204:207], v[92:95]\n" \
"s_waitcnt lgkmcnt(4)\n" \
"ds_read_b64_tr_b16 a[190:191], v228 offset:27136\n" \
"v_mfma_f32_16x16x32_bf16 v[64:67], a[160:163], a[224:227], v[64:67]\n" \
"ds_read_b128 a[208:211], v213 offset:4096\n" \
"v_mfma_f32_16x16x32_bf16 v[64:67], a[164:167], a[228:231], v[64:67]\n" \
"ds_read_b128 a[212:215], v213 offset:6144\n" \
"v_mfma_f32_16x16x32_bf16 v[80:83], a[160:163], a[232:235], v[80:83]\n" \
"ds_read_b128 a[240:243], v226 offset:4096\n" \
"v_mfma_f32_16x16x32_bf16 v[80:83], a[164:167], a[236:239], v[80:83]\n" \
"ds_read_b128 a[244:247], v226 offset:6144\n" \
"v_mfma_f32_16x16x32_bf16 v[68:71], a[168:171], a[224:227], v[68:71]\n" \
"ds_read_b128 a[216:219], v213 offset:4352\n" \
"v_cvt_pk_bf16_f32 v64, v64, v65\n" \
"v_mfma_f32_16x16x32_bf16 v[68:71], a[172:175], a[228:231], v[68:71]\n" \
"ds_read_b128 a[220:223], v213 offset:6400\n" \
"v_cvt_pk_bf16_f32 v65, v66, v67\n" \
"v_mfma_f32_16x16x32_bf16 v[84:87], a[168:171], a[232:235], v[84:87]\n" \
"ds_read_b128 a[248:251], v226 offset:4352\n" \
"v_mfma_f32_16x16x32_bf16 v[84:87], a[172:175], a[236:239], v[84:87]\n" \
"s_waitcnt lgkmcnt(4)\n" \
"ds_read_b128 a[252:255], v226 offset:6400\n" \
"v_mfma_f32_16x16x32_bf16 v[72:75], a[176:179], a[224:227], v[72:75]\n" \
"ds_write_b64 v128, v[64:65] offset:0\n" \
"v_mfma_f32_16x16x32_bf16 v[72:75], a[180:183], a[228:231], v[72:75]\n" \
"v_cvt_pk_bf16_f32 v80, v80, v81\n" \
"v_cvt_pk_bf16_f32 v81, v82, v83\n" \
"v_mfma_f32_16x16x32_bf16 v[88:91], a[176:179], a[232:235], v[88:91]\n" \
"ds_write_b64 v128, v[80:81] offset:2304\n" \
"v_mfma_f32_16x16x32_bf16 v[88:91], a[180:183], a[236:239], v[88:91]\n" \
"v_cvt_pk_bf16_f32 v68, v68, v69\n" \
"v_cvt_pk_bf16_f32 v69, v70, v71\n" \
"v_mfma_f32_16x16x32_bf16 v[76:79], a[184:187], a[224:227], v[76:79]\n" \
"ds_write_b64 v128, v[68:69] offset:32\n" \
"v_mfma_f32_16x16x32_bf16 v[76:79], a[188:191], a[228:231], v[76:79]\n" \
"v_cvt_pk_bf16_f32 v84, v84, v85\n" \
"v_cvt_pk_bf16_f32 v85, v86, v87\n" \
"v_mfma_f32_16x16x32_bf16 v[92:95], a[184:187], a[232:235], v[92:95]\n" \
"ds_write_b64 v128, v[84:85] offset:2336\n" \
"v_mfma_f32_16x16x32_bf16 v[92:95], a[188:191], a[236:239], v[92:95]\n" \
"v_cvt_pk_bf16_f32 v72, v72, v73\n" \
"v_cvt_pk_bf16_f32 v73, v74, v75\n" \
"v_mfma_f32_16x16x32_bf16 v[96:99], a[128:131], a[208:211], v[96:99]\n" \
"ds_write_b64 v128, v[72:73] offset:64\n" \
"v_mfma_f32_16x16x32_bf16 v[96:99], a[132:135], a[212:215], v[96:99]\n" \
"s_waitcnt lgkmcnt(2)\n" \
"v_cvt_pk_bf16_f32 v88, v88, v89\n" \
"v_cvt_pk_bf16_f32 v89, v90, v91\n" \
"v_mfma_f32_16x16x32_bf16 v[96:99], a[160:163], a[240:243], v[96:99]\n" \
"ds_write_b64 v128, v[88:89] offset:2368\n" \
"v_mfma_f32_16x16x32_bf16 v[96:99], a[164:167], a[244:247], v[96:99]\n" \
"v_cvt_pk_bf16_f32 v76, v76, v77\n" \
"v_cvt_pk_bf16_f32 v77, v78, v79\n" \
"v_mfma_f32_16x16x32_bf16 v[100:103], a[136:139], a[208:211], v[100:103]\n" \
"ds_write_b64 v128, v[76:77] offset:96\n" \
"v_mfma_f32_16x16x32_bf16 v[100:103], a[140:143], a[212:215], v[100:103]\n" \
"v_cvt_pk_bf16_f32 v92, v92, v93\n" \
"v_cvt_pk_bf16_f32 v93, v94, v95\n" \
"v_mfma_f32_16x16x32_bf16 v[100:103], a[168:171], a[240:243], v[100:103]\n" \
"ds_write_b64 v128, v[92:93] offset:2400\n" \
"v_mfma_f32_16x16x32_bf16 v[100:103], a[172:175], a[244:247], v[100:103]\n" \
"ds_read_b128 v[140:143], v129 offset:0\n" \
"v_mfma_f32_16x16x32_bf16 v[104:107], a[144:147], a[208:211], v[104:107]\n" \
"ds_read_b128 v[144:147], v129 offset:1152\n" \
"v_mfma_f32_16x16x32_bf16 v[104:107], a[148:151], a[212:215], v[104:107]\n" \
"ds_read_b128 v[148:151], v129 offset:2304\n" \
"v_mfma_f32_16x16x32_bf16 v[104:107], a[176:179], a[240:243], v[104:107]\n" \
"ds_read_b128 v[152:155], v129 offset:3456\n" \
"v_mfma_f32_16x16x32_bf16 v[104:107], a[180:183], a[244:247], v[104:107]\n" \
"v_cvt_pk_bf16_f32 v96, v96, v97\n" \
"v_cvt_pk_bf16_f32 v97, v98, v99\n" \
"v_mfma_f32_16x16x32_bf16 v[108:111], a[152:155], a[208:211], v[108:111]\n" \
"ds_write_b64 v128, v[96:97] offset:0\n" \
"v_mfma_f32_16x16x32_bf16 v[108:111], a[156:159], a[212:215], v[108:111]\n" \
"v_cvt_pk_bf16_f32 v100, v100, v101\n" \
"v_cvt_pk_bf16_f32 v101, v102, v103\n" \
"v_mfma_f32_16x16x32_bf16 v[108:111], a[184:187], a[240:243], v[108:111]\n" \
"s_add_u32 s70, s77, 0\n" \
"ds_write_b64 v128, v[100:101] offset:32\n" \
"v_mfma_f32_16x16x32_bf16 v[108:111], a[188:191], a[244:247], v[108:111]\n" \
"s_waitcnt lgkmcnt(5)\n" \
"v_cvt_pk_bf16_f32 v104, v104, v105\n" \
"v_cvt_pk_bf16_f32 v105, v106, v107\n" \
"v_mfma_f32_16x16x32_bf16 v[112:115], a[128:131], a[216:219], v[112:115]\n" \
"buffer_store_dwordx4 v[140:143], v130, s[52:55], s70 offen sc1\n" \
"ds_write_b64 v128, v[104:105] offset:64\n" \
"v_mfma_f32_16x16x32_bf16 v[112:115], a[132:135], a[220:223], v[112:115]\n" \
"v_mfma_f32_16x16x32_bf16 v[112:115], a[160:163], a[248:251], v[112:115]\n" \
"v_cvt_pk_bf16_f32 v108, v108, v109\n" \
"v_cvt_pk_bf16_f32 v109, v110, v111\n" \
"v_mfma_f32_16x16x32_bf16 v[112:115], a[164:167], a[252:255], v[112:115]\n" \
"ds_write_b64 v128, v[108:109] offset:96\n" \
"v_mfma_f32_16x16x32_bf16 v[116:119], a[136:139], a[216:219], v[116:119]\n" \
"s_add_u32 s70, s77, 65536\n" \
"ds_read_b128 v[156:159], v129 offset:0\n" \
"s_waitcnt lgkmcnt(7)\n" \
"v_mfma_f32_16x16x32_bf16 v[116:119], a[140:143], a[220:223], v[116:119]\n" \
"buffer_store_dwordx4 v[144:147], v130, s[52:55], s70 offen sc1\n" \
"ds_read_b128 v[160:163], v129 offset:1152\n" \
"v_mfma_f32_16x16x32_bf16 v[116:119], a[168:171], a[248:251], v[116:119]\n" \
"v_cvt_pk_bf16_f32 v112, v112, v113\n" \
"v_cvt_pk_bf16_f32 v113, v114, v115\n" \
"v_mfma_f32_16x16x32_bf16 v[116:119], a[172:175], a[252:255], v[116:119]\n" \
"ds_write_b64 v128, v[112:113] offset:2304\n" \
"v_mfma_f32_16x16x32_bf16 v[120:123], a[144:147], a[216:219], v[120:123]\n" \
"v_mfma_f32_16x16x32_bf16 v[120:123], a[148:151], a[220:223], v[120:123]\n" \
"s_add_u32 s70, s77, 4096\n" \
"s_waitcnt lgkmcnt(8)\n" \
"v_mfma_f32_16x16x32_bf16 v[120:123], a[176:179], a[248:251], v[120:123]\n" \
"buffer_store_dwordx4 v[148:151], v130, s[52:55], s70 offen sc1\n" \
"v_cvt_pk_bf16_f32 v116, v116, v117\n" \
"v_cvt_pk_bf16_f32 v117, v118, v119\n" \
"v_mfma_f32_16x16x32_bf16 v[120:123], a[180:183], a[252:255], v[120:123]\n" \
"ds_write_b64 v128, v[116:117] offset:2336\n" \
"v_mfma_f32_16x16x32_bf16 v[124:127], a[152:155], a[216:219], v[124:127]\n" \
"v_mfma_f32_16x16x32_bf16 v[124:127], a[156:159], a[220:223], v[124:127]\n" \
"v_mfma_f32_16x16x32_bf16 v[124:127], a[184:187], a[248:251], v[124:127]\n" \
"s_add_u32 s70, s77, 69632\n" \
"v_cvt_pk_bf16_f32 v120, v120, v121\n" \
"v_cvt_pk_bf16_f32 v121, v122, v123\n" \
"v_mfma_f32_16x16x32_bf16 v[124:127], a[188:191], a[252:255], v[124:127]\n" \
"s_waitcnt lgkmcnt(8)\n" \
"ds_write_b64 v128, v[120:121] offset:2368\n" \
"buffer_store_dwordx4 v[152:155], v130, s[52:55], s70 offen sc1\n" \
"s_add_u32 s70, s77, 131072\n" \
"s_waitcnt lgkmcnt(4)\n" \
"s_nop 2\n" \
"v_cvt_pk_bf16_f32 v124, v124, v125\n" \
"v_cvt_pk_bf16_f32 v125, v126, v127\n" \
"ds_write_b64 v128, v[124:125] offset:2400\n" \
"ds_read_b128 v[164:167], v129 offset:2304\n" \
"ds_read_b128 v[168:171], v129 offset:3456\n" \
"buffer_store_dwordx4 v[156:159], v130, s[52:55], s70 offen sc1\n" \
"s_add_u32 s70, s77, 196608\n" \
"s_waitcnt lgkmcnt(6)\n" \
"buffer_store_dwordx4 v[160:163], v130, s[52:55], s70 offen sc1\n" \
"s_add_u32 s70, s77, 135168\n" \
"s_waitcnt lgkmcnt(1)\n" \
"buffer_store_dwordx4 v[164:167], v130, s[52:55], s70 offen sc1\n" \
"s_add_u32 s70, s77, 200704\n" \
"s_waitcnt lgkmcnt(0)\n" \
"buffer_store_dwordx4 v[168:171], v130, s[52:55], s70 offen sc1\n" \
"s_endpgm\n" \
"Lpg13592_%=:\n" \
"s_cbranch_execz Lcb1_%=\n" \
"Lo64_%=:\n" \
"v_mfma_f32_16x16x32_bf16 v[64:67], a[32:35], a[0:3], v[64:67]\n" \
"ds_read_b128 a[24:27], v213 offset:4352\n" \
"s_add_u32 m0, s75, s95\n" \
"v_mfma_f32_16x16x32_bf16 v[64:67], a[36:39], a[4:7], v[64:67]\n" \
"s_waitcnt lgkmcnt(1)\n" \
"ds_read_b128 a[28:31], v213 offset:6400\n" \
"global_load_lds_dwordx4 v222, s[36:37] offset:0 sc1\n" \
"v_mfma_f32_16x16x32_bf16 v[68:71], a[40:43], a[0:3], v[68:71]\n" \
"s_sub_u32 s74, s74, 32768\n" \
"s_cselect_b32 s74, 131072, s74\n" \
"ds_read_b64_tr_b16 a[48:49], v214 offset:16896\n" \
"v_mfma_f32_16x16x32_bf16 v[68:71], a[44:47], a[4:7], v[68:71]\n" \
"ds_read_b64_tr_b16 a[50:51], v214 offset:18944\n" \
"v_mfma_f32_16x16x32_bf16 v[80:83], a[32:35], a[8:11], v[80:83]\n" \
"ds_read_b64_tr_b16 a[52:53], v214 offset:25088\n" \
"v_mfma_f32_16x16x32_bf16 v[80:83], a[36:39], a[12:15], v[80:83]\n" \
"ds_read_b64_tr_b16 a[54:55], v214 offset:27136\n" \
"global_load_lds_dwordx4 v222, s[36:37] offset:1024 sc1\n" \
"v_mfma_f32_16x16x32_bf16 v[84:87], a[40:43], a[8:11], v[84:87]\n" \
"ds_read_b64_tr_b16 a[56:57], v215 offset:16896\n" \
"v_mfma_f32_16x16x32_bf16 v[84:87], a[44:47], a[12:15], v[84:87]\n" \
"ds_read_b64_tr_b16 a[58:59], v215 offset:18944\n" \
"v_mfma_f32_16x16x32_bf16 v[96:99], a[32:35], a[16:19], v[96:99]\n" \
"ds_read_b64_tr_b16 a[60:61], v215 offset:25088\n" \
"v_mfma_f32_16x16x32_bf16 v[96:99], a[36:39], a[20:23], v[96:99]\n" \
"ds_read_b64_tr_b16 a[62:63], v215 offset:27136\n" \
"global_load_lds_dwordx4 v222, s[36:37] offset:2048 sc1\n" \
"v_mfma_f32_16x16x32_bf16 v[100:103], a[40:43], a[16:19], v[100:103]\n" \
"v_add_u32 v213, s74, v211\n" \
"v_add_u32 v214, s74, v212\n" \
"v_mfma_f32_16x16x32_bf16 v[100:103], a[44:47], a[20:23], v[100:103]\n" \
"s_waitcnt lgkmcnt(3)\n" \
"v_xor_b32 v215, 32, v214\n" \
"v_mfma_f32_16x16x32_bf16 v[112:115], a[32:35], a[24:27], v[112:115]\n" \
"v_mfma_f32_16x16x32_bf16 v[112:115], a[36:39], a[28:31], v[112:115]\n" \
"global_load_lds_dwordx4 v222, s[36:37] offset:3072 sc1\n" \
"v_mfma_f32_16x16x32_bf16 v[116:119], a[40:43], a[24:27], v[116:119]\n" \
"s_waitcnt vmcnt(20) lgkmcnt(0)\n" \
"s_add_u32 s36, s36, 0x1000\n" \
"v_mfma_f32_16x16x32_bf16 v[116:119], a[44:47], a[28:31], v[116:119]\n" \
"s_barrier\n" \
"v_mfma_f32_16x16x32_bf16 v[72:75], a[48:51], a[0:3], v[72:75]\n" \
"s_add_u32 m0, m0, 0x5000\n" \
"ds_read_b64_tr_b16 a[32:33], v214 offset:16384\n" \
"v_mfma_f32_16x16x32_bf16 v[72:75], a[52:55], a[4:7], v[72:75]\n" \
"ds_read_b64_tr_b16 a[34:35], v214 offset:18432\n" \
"global_load_lds_dwordx4 v216, s[38:39] offset:-4096\n" \
"v_mfma_f32_16x16x32_bf16 v[76:79], a[56:59], a[0:3], v[76:79]\n" \
"ds_read_b64_tr_b16 a[36:37], v214 offset:24576\n" \
"v_mfma_f32_16x16x32_bf16 v[76:79], a[60:63], a[4:7], v[76:79]\n" \
"ds_read_b128 a[0:3], v213 offset:0\n" \
"v_mfma_f32_16x16x32_bf16 v[88:91], a[48:51], a[8:11], v[88:91]\n" \
"ds_read_b128 a[4:7], v213 offset:2048\n" \
"v_mfma_f32_16x16x32_bf16 v[88:91], a[52:55], a[12:15], v[88:91]\n" \
"ds_read_b64_tr_b16 a[38:39], v214 offset:26624\n" \
"global_load_lds_dwordx4 v217, s[38:39] offset:-3072\n" \
"v_mfma_f32_16x16x32_bf16 v[92:95], a[56:59], a[8:11], v[92:95]\n" \
"ds_read_b64_tr_b16 a[40:41], v215 offset:16384\n" \
"v_mfma_f32_16x16x32_bf16 v[92:95], a[60:63], a[12:15], v[92:95]\n" \
"ds_read_b128 a[8:11], v213 offset:256\n" \
"v_mfma_f32_16x16x32_bf16 v[104:107], a[48:51], a[16:19], v[104:107]\n" \
"ds_read_b128 a[12:15], v213 offset:2304\n" \
"v_mfma_f32_16x16x32_bf16 v[104:107], a[52:55], a[20:23], v[104:107]\n" \
"ds_read_b64_tr_b16 a[42:43], v215 offset:18432\n" \
"global_load_lds_dwordx4 v218, s[38:39] offset:-2048\n" \
"v_mfma_f32_16x16x32_bf16 v[108:111], a[56:59], a[16:19], v[108:111]\n" \
"ds_read_b64_tr_b16 a[44:45], v215 offset:24576\n" \
"v_mfma_f32_16x16x32_bf16 v[108:111], a[60:63], a[20:23], v[108:111]\n" \
"ds_read_b128 a[16:19], v213 offset:4096\n" \
"global_load_lds_dwordx4 v219, s[38:39] offset:-1024\n" \
"v_mfma_f32_16x16x32_bf16 v[120:123], a[48:51], a[24:27], v[120:123]\n" \
"s_add_u32 s38, s38, 0x20000\n" \
"s_addc_u32 s39, s39, 0\n" \
"ds_read_b128 a[20:23], v213 offset:6144\n" \
"v_mfma_f32_16x16x32_bf16 v[120:123], a[52:55], a[28:31], v[120:123]\n" \
"s_sub_u32 s75, s75, 32768\n" \
"s_cselect_b32 s75, 131072, s75\n" \
"ds_read_b64_tr_b16 a[46:47], v215 offset:26624\n" \
"v_mfma_f32_16x16x32_bf16 v[124:127], a[56:59], a[24:27], v[124:127]\n" \
"s_sub_u32 s72, s72, 1\n" \
"s_cmp_lg_u32 s72, 0\n" \
"v_mfma_f32_16x16x32_bf16 v[124:127], a[60:63], a[28:31], v[124:127]\n" \
"s_waitcnt lgkmcnt(3)\n" \
"s_cbranch_scc1 Lo64_%=\n" \
"s_setpc_b64 s[68:69]\n" \
"Lhot_end_%=:\n" \
"Lmask0_%=:\n" \
"Lpg13681_%=:\n" \
"s_cbranch_execz Lcb0_%=\n" \
"s_waitcnt lgkmcnt(0)\n" \
"v_subrev_u32 v0, 0, v251\n" \
"v_subrev_u32 v1, 1, v251\n" \
"v_subrev_u32 v2, 2, v251\n" \
"v_subrev_u32 v3, 3, v251\n" \
"v_subrev_u32 v4, 16, v251\n" \
"v_subrev_u32 v5, 17, v251\n" \
"v_subrev_u32 v6, 18, v251\n" \
"v_subrev_u32 v7, 19, v251\n" \
"v_subrev_u32 v32, 32, v251\n" \
"v_subrev_u32 v33, 33, v251\n" \
"v_subrev_u32 v34, 34, v251\n" \
"v_subrev_u32 v35, 35, v251\n" \
"v_subrev_u32 v36, 48, v251\n" \
"v_subrev_u32 v37, 49, v251\n" \
"v_subrev_u32 v38, 50, v251\n" \
"v_subrev_u32 v39, 51, v251\n" \
"v_ashrrev_i32 v0, 31, v0\n" \
"v_ashrrev_i32 v1, 31, v1\n" \
"v_ashrrev_i32 v2, 31, v2\n" \
"v_ashrrev_i32 v3, 31, v3\n" \
"v_ashrrev_i32 v4, 31, v4\n" \
"v_ashrrev_i32 v5, 31, v5\n" \
"v_ashrrev_i32 v6, 31, v6\n" \
"v_ashrrev_i32 v7, 31, v7\n" \
"v_ashrrev_i32 v32, 31, v32\n" \
"v_ashrrev_i32 v33, 31, v33\n" \
"v_ashrrev_i32 v34, 31, v34\n" \
"v_ashrrev_i32 v35, 31, v35\n" \
"v_ashrrev_i32 v36, 31, v36\n" \
"v_ashrrev_i32 v37, 31, v37\n" \
"v_ashrrev_i32 v38, 31, v38\n" \
"v_ashrrev_i32 v39, 31, v39\n" \
"v_and_b32 v0, 0xff800000, v0\n" \
"v_and_b32 v1, 0xff800000, v1\n" \
"v_and_b32 v2, 0xff800000, v2\n" \
"v_and_b32 v3, 0xff800000, v3\n" \
"v_and_b32 v4, 0xff800000, v4\n" \
"v_and_b32 v5, 0xff800000, v5\n" \
"v_and_b32 v6, 0xff800000, v6\n" \
"v_and_b32 v7, 0xff800000, v7\n" \
"v_and_b32 v32, 0xff800000, v32\n" \
"v_and_b32 v33, 0xff800000, v33\n" \
"v_and_b32 v34, 0xff800000, v34\n" \
"v_and_b32 v35, 0xff800000, v35\n" \
"v_and_b32 v36, 0xff800000, v36\n" \
"v_and_b32 v37, 0xff800000, v37\n" \
"v_and_b32 v38, 0xff800000, v38\n" \
"v_and_b32 v39, 0xff800000, v39\n" \
"s_branch Lmask0r_%=\n" \
"Lpl1_%=:\n" \
"s_mov_b32 s31, 0\n" \
"Lpk1_%=:\n" \
"s_sleep 2\n" \
"buffer_load_dword v244, v245, s[56:59], s91 offen sc1\n" \
"s_waitcnt vmcnt(0)\n" \
"v_cmp_ne_u32 vcc, s60, v244\n" \
"s_nop 1\n" \
"s_and_b32 vcc_hi, vcc_lo, 255\n" \
"s_cbranch_scc0 Lpr1_%=\n" \
"s_add_u32 s31, s31, 1\n" \
"s_cmp_lt_u32 s31, 0x40000\n" \
"s_cbranch_scc1 Lpk1_%=\n" \
"s_branch Lpr1_%=\n" \
"Lrs0_%=:\n" \
"s_call_b64 s[68:69], Lrest_%=\n" \
"s_branch Lrr0_%=\n" \
"Lrest_%=:\n" \
"s_mov_b32 s31, 0\n" \
"s_waitcnt vmcnt(0)\n" \
"Lrk_%=:\n" \
"v_lshrrev_b32 v253, 2, v248\n" \
"s_sleep 2\n" \
"buffer_load_dword v253, v253, s[56:59], s79 offen sc1\n" \
"s_waitcnt vmcnt(0)\n" \
"v_cmp_ne_u32 s[52:53], s60, v253\n" \
"s_add_u32 s31, s31, 1\n" \
"s_cmp_lt_u32 s31, 0x40000\n" \
"s_cselect_b32 s54, 0xff, 0\n" \
"s_and_b32 s52, s52, s54\n" \
"s_cmp_lg_u32 s52, 0\n" \
"s_cbranch_scc1 Lrk_%=\n" \
"s_mov_b64 s[10:11], 0\n" \
"s_setpc_b64 s[68:69]\n" \
"Lrs1_%=:\n" \
"s_call_b64 s[68:69], Lrest_%=\n" \
"s_branch Lrr1_%=\n" \
"Lrs2_%=:\n" \
"s_call_b64 s[68:69], Lrest_%=\n" \
"s_branch Lrr2_%=\n" \
"Lrs3_%=:\n" \
"s_call_b64 s[68:69], Lrest_%=\n" \
"s_branch Lrr3_%=\n" \
"LF7_%=:\n" \
"v_mfma_f32_16x16x32_bf16 v[16:19], a[192:195], a[160:163], v[232:235]\n" \
"s_waitcnt vmcnt(12)\n" \
"v_exp_f32 v104, v104\n" \
"s_bfe_u32 s88, %1, 0x20001\n" \
"s_lshl_b32 s88, s88, 2\n" \
"ds_read_b128 v[192:195], v246 offset:16384\n" \
"v_mfma_f32_16x16x32_bf16 v[16:19], a[196:199], a[164:167], v[16:19]\n" \
"s_lshr_b32 s70, %1, 6\n" \
"ds_read_b128 v[196:199], v246 offset:18432\n" \
"v_cvt_pk_bf16_f32 v91, v94, v95\n" \
"v_add_f32 v241, v241, v104\n" \
"v_exp_f32 v105, v105\n" \
"v_mfma_f32_16x16x32_bf16 v[24:27], a[192:195], a[176:179], v[236:239]\n" \
"s_add_u32 s88, s88, s70\n" \
"ds_read_b128 v[200:203], v246 offset:16640\n" \
"v_mov_b32 v78, v240\n" \
"v_add_f32 v241, v241, v105\n" \
"v_bfe_u32 v69, v248, 8, 1\n" \
"v_bfe_u32 v70, v248, 6, 2\n" \
"s_lshr_b32 s71, s22, 7\n" \
"v_exp_f32 v106, v106\n" \
"v_mfma_f32_16x16x32_bf16 v[24:27], a[196:199], a[180:183], v[24:27]\n" \
"s_sub_u32 s71, s63, s71\n" \
"ds_read_b128 a[252:255], v246 offset:14592\n" \
"v_add_f32 v241, v241, v106\n" \
"v_cvt_pk_bf16_f32 v104, v104, v105\n" \
"v_exp_f32 v107, v107\n" \
"s_add_u32 s71, s71, 6\n" \
"v_mfma_f32_16x16x32_bf16 v[20:23], a[200:203], a[160:163], v[232:235]\n" \
"s_and_b32 s71, s71, 7\n" \
"ds_read_b128 v[204:207], v246 offset:18688\n" \
"v_add_f32 v241, v241, v107\n" \
"v_lshl_add_u32 v70, v69, 3, v70\n" \
"v_mul_u32_u24 v70, 6144, v70\n" \
"v_exp_f32 v108, v108\n" \
"v_permlane16_swap_b32 v78, v240\n" \
"v_add_f32 v240, v240, v78\n" \
"v_mov_b32 v78, v240\n" \
"v_mfma_f32_16x16x32_bf16 v[20:23], a[204:207], a[164:167], v[20:23]\n" \
"s_and_b32 s70, %1, 1\n" \
"ds_read_b128 v[208:211], v246 offset:24576\n" \
"v_add_f32 v241, v241, v108\n" \
"v_cvt_pk_bf16_f32 v105, v106, v107\n" \
"v_exp_f32 v109, v109\n" \
"s_lshl_b32 s70, s70, 3\n" \
"v_mfma_f32_16x16x32_bf16 v[28:31], a[200:203], a[176:179], v[236:239]\n" \
"s_add_u32 s70, s70, s71\n" \
"ds_read_b128 v[212:215], v246 offset:26624\n" \
"v_add_f32 v241, v241, v109\n" \
"v_lshlrev_b32 v69, 5, v69\n" \
"v_and_b32 v71, 0x30, v248\n" \
"s_lshl_b32 s87, s70, 12\n" \
"v_exp_f32 v110, v110\n" \
"v_permlane32_swap_b32 v78, v240\n" \
"v_add_f32 v240, v240, v78\n" \
"v_rcp_f32 v78, v240\n" \
"v_mul_f32 v240, 0x4d800000, v240\n" \
"v_add_u32 v79, 0x1c90080, v249\n" \
"v_cndmask_b32 v79, v249, v79, s[16:17]\n" \
"v_accvgpr_read_b32 v96, a0\n" \
"v_accvgpr_read_b32 v97, a1\n" \
"v_accvgpr_read_b32 v98, a2\n" \
"v_accvgpr_read_b32 v99, a3\n" \
"v_accvgpr_read_b32 v100, a4\n" \
"v_accvgpr_read_b32 v101, a5\n" \
"v_accvgpr_read_b32 v102, a6\n" \
"v_accvgpr_read_b32 v103, a7\n" \
"v_pk_mul_f32 v[96:97], v[96:97], v[78:79] op_sel_hi:[1,0]\n" \
"v_pk_mul_f32 v[98:99], v[98:99], v[78:79] op_sel_hi:[1,0]\n" \
"v_pk_mul_f32 v[100:101], v[100:101], v[78:79] op_sel_hi:[1,0]\n" \
"v_pk_mul_f32 v[102:103], v[102:103], v[78:79] op_sel_hi:[1,0]\n" \
"v_cvt_pk_bf16_f32 v224, v96, v97\n" \
"v_cvt_pk_bf16_f32 v225, v98, v99\n" \
"v_mfma_f32_16x16x32_bf16 v[28:31], a[204:207], a[180:183], v[28:31]\n" \
"s_lshl_b32 s70, s70, 7\n" \
"s_mul_i32 s84, s61, 6144\n" \
"ds_read_b128 v[216:219], v246 offset:24832\n" \
"v_add_f32 v241, v241, v110\n" \
"v_cvt_pk_bf16_f32 v106, v108, v109\n" \
"v_cvt_pk_bf16_f32 v226, v100, v101\n" \
"v_cvt_pk_bf16_f32 v227, v102, v103\n" \
"v_exp_f32 v111, v111\n" \
"v_pk_mul_f32 v[96:97], v[96:97], v[98:99]\n" \
"v_pk_fma_f32 v[96:97], v[100:101], v[102:103], v[96:97]\n" \
"v_fma_f32 v240, v96, v97, v240\n" \
"v_mfma_f32_16x16x32_bf16 v[48:51], a[208:211], a[160:163], v[232:235]\n" \
"s_lshl_b32 s71, s84, 4\n" \
"ds_read_b128 v[220:223], v246 offset:26880\n" \
"buffer_store_dwordx4 v[224:227], v79, s[56:59], s77 offen offset:0 sc1\n" \
"v_add_f32 v241, v241, v111\n" \
"v_exp_f32 v112, v112\n" \
"v_xor_b32 v69, v69, v71\n" \
"v_accvgpr_read_b32 v96, a8\n" \
"v_mfma_f32_16x16x32_bf16 v[48:51], a[212:215], a[164:167], v[48:51]\n" \
"ds_read_b128 a[192:195], v247 offset:16384\n" \
"v_lshrrev_b32 v71, 9, v248\n" \
"v_accvgpr_read_b32 v97, a9\n" \
"v_accvgpr_read_b32 v98, a10\n" \
"v_accvgpr_read_b32 v99, a11\n" \
"v_accvgpr_read_b32 v100, a12\n" \
"v_accvgpr_read_b32 v101, a13\n" \
"v_accvgpr_read_b32 v102, a14\n" \
"v_accvgpr_read_b32 v103, a15\n" \
"v_add_f32 v242, v242, v112\n" \
"v_cvt_pk_bf16_f32 v107, v110, v111\n" \
"v_exp_f32 v113, v113\n" \
"v_pk_mul_f32 v[96:97], v[96:97], v[78:79] op_sel_hi:[1,0]\n" \
"v_pk_mul_f32 v[98:99], v[98:99], v[78:79] op_sel_hi:[1,0]\n" \
"v_pk_mul_f32 v[100:101], v[100:101], v[78:79] op_sel_hi:[1,0]\n" \
"v_pk_mul_f32 v[102:103], v[102:103], v[78:79] op_sel_hi:[1,0]\n" \
"v_cvt_pk_bf16_f32 v228, v96, v97\n" \
"v_cvt_pk_bf16_f32 v229, v98, v99\n" \
"v_cvt_pk_bf16_f32 v230, v100, v101\n" \
"v_cvt_pk_bf16_f32 v231, v102, v103\n" \
"v_pk_mul_f32 v[96:97], v[96:97], v[98:99]\n" \
"v_pk_fma_f32 v[96:97], v[100:101], v[102:103], v[96:97]\n" \
"v_fma_f32 v240, v96, v97, v240\n" \
"v_mfma_f32_16x16x32_bf16 v[56:59], a[208:211], a[176:179], v[236:239]\n" \
"s_add_u32 s70, s70, s71\n" \
"ds_read_b128 a[196:199], v247 offset:24576\n" \
"buffer_store_dwordx4 v[228:231], v79, s[56:59], s77 offen offset:2048 sc1\n" \
"v_add_f32 v242, v242, v113\n" \
"v_exp_f32 v114, v114\n" \
"s_add_u32 s70, s70, 4096\n" \
"v_mfma_f32_16x16x32_bf16 v[56:59], a[212:215], a[180:183], v[56:59]\n" \
"s_waitcnt lgkmcnt(4)\n" \
"ds_read_b128 a[200:203], v247 offset:17408\n" \
"v_lshl_add_u32 v70, v71, 6, v70\n" \
"v_add_f32 v242, v242, v114\n" \
"v_cvt_pk_bf16_f32 v112, v112, v113\n" \
"v_exp_f32 v115, v115\n" \
"v_mfma_f32_16x16x32_bf16 v[8:11], v[192:195], a[176:179], v[236:239]\n" \
"s_add_u32 s77, s77, 0x1000\n" \
"ds_read_b128 a[204:207], v247 offset:25600\n" \
"v_add_u32 v253, v69, v70\n" \
"v_add_f32 v242, v242, v115\n" \
"v_accvgpr_read_b32 v96, a16\n" \
"v_accvgpr_read_b32 v97, a17\n" \
"v_accvgpr_read_b32 v98, a18\n" \
"v_accvgpr_read_b32 v99, a19\n" \
"v_accvgpr_read_b32 v100, a20\n" \
"v_accvgpr_read_b32 v101, a21\n" \
"v_accvgpr_read_b32 v102, a22\n" \
"v_accvgpr_read_b32 v103, a23\n" \
"v_exp_f32 v116, v116\n" \
"v_pk_mul_f32 v[96:97], v[96:97], v[78:79] op_sel_hi:[1,0]\n" \
"v_pk_mul_f32 v[98:99], v[98:99], v[78:79] op_sel_hi:[1,0]\n" \
"v_pk_mul_f32 v[100:101], v[100:101], v[78:79] op_sel_hi:[1,0]\n" \
"v_pk_mul_f32 v[102:103], v[102:103], v[78:79] op_sel_hi:[1,0]\n" \
"v_cvt_pk_bf16_f32 v224, v96, v97\n" \
"v_cvt_pk_bf16_f32 v225, v98, v99\n" \
"v_cvt_pk_bf16_f32 v226, v100, v101\n" \
"v_cvt_pk_bf16_f32 v227, v102, v103\n" \
"v_pk_mul_f32 v[96:97], v[96:97], v[98:99]\n" \
"v_pk_fma_f32 v[96:97], v[100:101], v[102:103], v[96:97]\n" \
"v_fma_f32 v240, v96, v97, v240\n" \
"v_mfma_f32_16x16x32_bf16 v[8:11], v[196:199], a[180:183], v[8:11]\n" \
"s_add_u32 s48, s22, s70\n" \
"s_addc_u32 s49, s23, 0\n" \
"ds_read_b128 a[208:211], v247 offset:18432\n" \
"buffer_store_dwordx4 v[224:227], v79, s[56:59], s77 offen offset:0 sc1\n" \
"v_add_f32 v242, v242, v116\n" \
"v_cvt_pk_bf16_f32 v113, v114, v115\n" \
"v_exp_f32 v117, v117\n" \
"v_accvgpr_read_b32 v96, a24\n" \
"v_mfma_f32_16x16x32_bf16 v[52:55], a[216:219], a[160:163], v[232:235]\n" \
"s_add_u32 s50, s48, 23552\n" \
"s_addc_u32 s51, s49, 0\n" \
"ds_read_b128 a[212:215], v247 offset:26624\n" \
"v_accvgpr_read_b32 v97, a25\n" \
"v_accvgpr_read_b32 v98, a26\n" \
"v_accvgpr_read_b32 v99, a27\n" \
"v_accvgpr_read_b32 v100, a28\n" \
"v_accvgpr_read_b32 v101, a29\n" \
"v_accvgpr_read_b32 v102, a30\n" \
"v_accvgpr_read_b32 v103, a31\n" \
"v_add_f32 v242, v242, v117\n" \
"v_exp_f32 v118, v118\n" \
"v_pk_mul_f32 v[96:97], v[96:97], v[78:79] op_sel_hi:[1,0]\n" \
"v_pk_mul_f32 v[98:99], v[98:99], v[78:79] op_sel_hi:[1,0]\n" \
"v_pk_mul_f32 v[100:101], v[100:101], v[78:79] op_sel_hi:[1,0]\n" \
"v_pk_mul_f32 v[102:103], v[102:103], v[78:79] op_sel_hi:[1,0]\n" \
"v_cvt_pk_bf16_f32 v228, v96, v97\n" \
"v_cvt_pk_bf16_f32 v229, v98, v99\n" \
"v_cvt_pk_bf16_f32 v230, v100, v101\n" \
"v_cvt_pk_bf16_f32 v231, v102, v103\n" \
"v_pk_mul_f32 v[96:97], v[96:97], v[98:99]\n" \
"v_pk_fma_f32 v[96:97], v[100:101], v[102:103], v[96:97]\n" \
"v_fma_f32 v240, v96, v97, v240\n" \
"v_mfma_f32_16x16x32_bf16 v[52:55], a[220:223], a[164:167], v[52:55]\n" \
"s_lshr_b32 s89, s22, 7\n" \
"ds_read_b128 v[192:195], v246 offset:20480\n" \
"v_add_f32 v242, v242, v118\n" \
"v_cvt_pk_bf16_f32 v114, v116, v117\n" \
"s_sub_u32 s89, 3, s89\n" \
"v_mfma_f32_16x16x32_bf16 v[60:63], a[216:219], a[176:179], v[236:239]\n" \
"ds_read_b128 v[196:199], v246 offset:22528\n" \
"v_bfe_u32 v84, v248, 7, 2\n" \
"v_mfma_f32_16x16x32_bf16 v[60:63], a[220:223], a[180:183], v[60:63]\n" \
"s_and_b32 s89, s89, 7\n" \
"ds_read_b128 a[216:219], v247 offset:19456\n" \
"s_and_b32 s90, %1, 1\n" \
"v_mfma_f32_16x16x32_bf16 v[12:15], v[200:203], a[176:179], v[236:239]\n" \
"ds_read_b128 a[220:223], v247 offset:27648\n" \
"v_bfe_u32 v85, v248, 5, 2\n" \
"v_mfma_f32_16x16x32_bf16 v[12:15], v[204:207], a[180:183], v[12:15]\n" \
"s_lshl_b32 s90, s90, 3\n" \
"ds_read_b128 v[200:203], v246 offset:20736\n" \
"s_add_u32 s89, s89, s90\n" \
"v_mfma_f32_16x16x32_bf16 v[16:19], a[224:227], a[168:171], v[16:19]\n" \
"ds_read_b128 v[204:207], v246 offset:22784\n" \
"v_min_u32 v85, 2, v85\n" \
"v_mfma_f32_16x16x32_bf16 v[16:19], a[228:231], a[172:175], v[16:19]\n" \
"s_lshl_b32 s89, s89, 7\n" \
"s_lshr_b32 s90, %1, 3\n" \
"v_mad_u32_u24 v84, v84, 3, v85\n" \
"v_mfma_f32_16x16x32_bf16 v[24:27], a[224:227], a[184:187], v[24:27]\n" \
"s_lshl_b32 s90, s90, 2\n" \
"s_add_u32 s90, s90, s61\n" \
"v_and_b32 v85, 16, v248\n" \
"v_mfma_f32_16x16x32_bf16 v[24:27], a[228:231], a[188:191], v[24:27]\n" \
"s_mul_i32 s90, s90, 24576\n" \
"ds_read_b128 a[224:227], v247 offset:20480\n" \
"s_add_u32 s89, s89, s90\n" \
"v_mfma_f32_16x16x32_bf16 v[40:43], v[208:211], a[176:179], v[236:239]\n" \
"ds_read_b128 a[228:231], v247 offset:28672\n" \
"v_lshl_add_u32 v85, v85, 2, s89\n" \
"v_mfma_f32_16x16x32_bf16 v[40:43], v[212:215], a[180:183], v[40:43]\n" \
"ds_read_b128 v[208:211], v246 offset:28672\n" \
"v_lshl_add_u32 v84, v84, 11, v85\n" \
"v_mfma_f32_16x16x32_bf16 v[20:23], a[232:235], a[168:171], v[20:23]\n" \
"s_add_u32 m0, s84, 143360\n" \
"ds_read_b128 v[212:215], v246 offset:30720\n" \
"v_exp_f32 v119, v119\n" \
"v_mfma_f32_16x16x32_bf16 v[20:23], a[236:239], a[172:175], v[20:23]\n" \
"global_load_lds_dwordx4 v253, s[48:49] offset:-4096\n" \
"v_add_f32 v242, v242, v119\n" \
"v_mfma_f32_16x16x32_bf16 v[28:31], a[232:235], a[184:187], v[28:31]\n" \
"v_exp_f32 v120, v120\n" \
"v_mfma_f32_16x16x32_bf16 v[28:31], a[236:239], a[188:191], v[28:31]\n" \
"s_waitcnt lgkmcnt(2)\n" \
"ds_read_b128 a[232:235], v247 offset:21504\n" \
"v_add_f32 v243, v243, v120\n" \
"v_cvt_pk_bf16_f32 v115, v118, v119\n" \
"v_mfma_f32_16x16x32_bf16 v[44:47], v[216:219], a[176:179], v[236:239]\n" \
"ds_read_b128 a[236:239], v247 offset:29696\n" \
"global_load_lds_dwordx4 v253, s[48:49] offset:-2048\n" \
"v_mfma_f32_16x16x32_bf16 v[44:47], v[220:223], a[180:183], v[44:47]\n" \
"ds_read_b128 v[216:219], v246 offset:28928\n" \
"v_exp_f32 v121, v121\n" \
"v_mfma_f32_16x16x32_bf16 v[48:51], a[240:243], a[168:171], v[48:51]\n" \
"ds_read_b128 v[220:223], v246 offset:30976\n" \
"v_add_f32 v243, v243, v121\n" \
"v_mfma_f32_16x16x32_bf16 v[48:51], a[244:247], a[172:175], v[48:51]\n" \
"global_load_lds_dwordx4 v253, s[48:49] offset:0\n" \
"v_exp_f32 v122, v122\n" \
"v_mfma_f32_16x16x32_bf16 v[56:59], a[240:243], a[184:187], v[56:59]\n" \
"s_add_u32 s48, s48, 0x120000\n" \
"s_addc_u32 s49, s49, 0\n" \
"v_add_f32 v243, v243, v122\n" \
"v_mfma_f32_16x16x32_bf16 v[56:59], a[244:247], a[188:191], v[56:59]\n" \
"ds_read_b128 a[240:243], v247 offset:22528\n" \
"v_cvt_pk_bf16_f32 v120, v120, v121\n" \
"v_mfma_f32_16x16x32_bf16 v[8:11], v[192:195], a[184:187], v[8:11]\n" \
"ds_read_b128 a[244:247], v247 offset:30720\n" \
"global_load_lds_dwordx4 v253, s[50:51] offset:-3072\n" \
"v_mfma_f32_16x16x32_bf16 v[8:11], v[196:199], a[188:191], v[8:11]\n" \
"v_exp_f32 v123, v123\n" \
"v_mfma_f32_16x16x32_bf16 v[52:55], a[248:251], a[168:171], v[52:55]\n" \
"v_add_f32 v243, v243, v123\n" \
"v_exp_f32 v124, v124\n" \
"v_mfma_f32_16x16x32_bf16 v[52:55], a[252:255], a[172:175], v[52:55]\n" \
"global_load_lds_dwordx4 v253, s[50:51] offset:-1024\n" \
"v_add_f32 v243, v243, v124\n" \
"v_cvt_pk_bf16_f32 v121, v122, v123\n" \
"v_mfma_f32_16x16x32_bf16 v[60:63], a[248:251], a[184:187], v[60:63]\n" \
"v_exp_f32 v125, v125\n" \
"v_mfma_f32_16x16x32_bf16 v[60:63], a[252:255], a[188:191], v[60:63]\n" \
"ds_read_b128 a[248:251], v247 offset:23552\n" \
"v_add_f32 v243, v243, v125\n" \
"v_mfma_f32_16x16x32_bf16 v[12:15], v[200:203], a[184:187], v[12:15]\n" \
"ds_read_b128 a[252:255], v247 offset:31744\n" \
"global_load_lds_dwordx4 v253, s[50:51] offset:1024\n" \
"v_mfma_f32_16x16x32_bf16 v[12:15], v[204:207], a[188:191], v[12:15]\n" \
"s_waitcnt lgkmcnt(2)\n" \
"s_add_u32 s50, s50, 0x120000\n" \
"s_addc_u32 s51, s51, 0\n" \
"v_mfma_f32_16x16x32_bf16 v[40:43], v[208:211], a[184:187], v[40:43]\n" \
"v_exp_f32 v126, v126\n" \
"v_mfma_f32_16x16x32_bf16 v[40:43], v[212:215], a[188:191], v[40:43]\n" \
"global_load_dword v85, v84, s[22:23]\n" \
"v_mfma_f32_16x16x32_bf16 v[44:47], v[216:219], a[184:187], v[44:47]\n" \
"s_waitcnt vmcnt(10) lgkmcnt(0)\n" \
"v_add_u32 v247, 0x10000, v248\n" \
"v_add_f32 v243, v243, v126\n" \
"v_mfma_f32_16x16x32_bf16 v[44:47], v[220:223], a[188:191], v[44:47]\n" \
"s_barrier\n" \
"v_cvt_pk_bf16_f32 v122, v124, v125\n" \
"v_mfma_f32_16x16x32_bf16 a[32:35], a[192:195], v[72:75], a[32:35]\n" \
"v_exp_f32 v127, v127\n" \
"v_mfma_f32_16x16x32_bf16 a[32:35], a[196:199], v[104:107], a[32:35]\n" \
"v_add_f32 v243, v243, v127\n" \
"v_cvt_pk_bf16_f32 v123, v126, v127\n" \
"v_mfma_f32_16x16x32_bf16 v[128:131], a[192:195], v[80:83], v[128:131]\n" \
"ds_read_b128 v[192:195], v247 offset:16384\n" \
"v_lshrrev_b32 v71, 7, v248\n" \
"v_cmp_gt_i32 vcc, 0, v251\n" \
"v_cmp_gt_i32 s[64:65], 1, v251\n" \
"v_cmp_gt_i32 s[66:67], 2, v251\n" \
"v_cndmask_b32 v8, v8, v254, vcc\n" \
"v_cndmask_b32 v16, v16, v254, vcc\n" \
"v_cmp_gt_i32 vcc, 3, v251\n" \
"v_mfma_f32_16x16x32_bf16 v[128:131], a[196:199], v[112:115], v[128:131]\n" \
"v_cndmask_b32 v9, v9, v254, s[64:65]\n" \
"v_cndmask_b32 v17, v17, v254, s[64:65]\n" \
"v_cmp_gt_i32 s[64:65], 16, v251\n" \
"v_cndmask_b32 v10, v10, v254, s[66:67]\n" \
"v_cndmask_b32 v18, v18, v254, s[66:67]\n" \
"v_cmp_gt_i32 s[66:67], 17, v251\n" \
"v_and_b32 v76, 0x70, v248\n" \
"s_lshl_b32 s70, s88, 18\n" \
"v_mfma_f32_16x16x32_bf16 v[160:163], a[192:195], v[88:91], v[160:163]\n" \
"s_lshl_b32 s71, s61, 16\n" \
"s_add_u32 s71, s71, s70\n" \
"v_cndmask_b32 v11, v11, v254, vcc\n" \
"v_cndmask_b32 v19, v19, v254, vcc\n" \
"v_cmp_gt_i32 vcc, 18, v251\n" \
"v_cndmask_b32 v12, v12, v254, s[64:65]\n" \
"v_cndmask_b32 v20, v20, v254, s[64:65]\n" \
"v_cmp_gt_i32 s[64:65], 19, v251\n" \
"v_lshlrev_b32 v77, 4, v71\n" \
"v_mfma_f32_16x16x32_bf16 v[160:163], a[196:199], v[120:123], v[160:163]\n" \
"ds_read_b128 a[192:195], v247 offset:0\n" \
"ds_read_b128 v[196:199], v247 offset:24576\n" \
"v_cndmask_b32 v13, v13, v254, s[66:67]\n" \
"v_cndmask_b32 v21, v21, v254, s[66:67]\n" \
"v_cmp_gt_i32 s[66:67], 32, v251\n" \
"v_cndmask_b32 v14, v14, v254, vcc\n" \
"v_cndmask_b32 v22, v22, v254, vcc\n" \
"v_cmp_gt_i32 vcc, 33, v251\n" \
"v_mfma_f32_16x16x32_bf16 a[36:39], a[200:203], v[72:75], a[36:39]\n" \
"v_xor_b32 v77, v77, v76\n" \
"v_cndmask_b32 v15, v15, v254, s[64:65]\n" \
"v_cndmask_b32 v23, v23, v254, s[64:65]\n" \
"v_cmp_gt_i32 s[64:65], 34, v251\n" \
"v_cndmask_b32 v40, v40, v254, s[66:67]\n" \
"v_cndmask_b32 v48, v48, v254, s[66:67]\n" \
"v_cmp_gt_i32 s[66:67], 35, v251\n" \
"v_mfma_f32_16x16x32_bf16 a[36:39], a[204:207], v[104:107], a[36:39]\n" \
"s_add_u32 s42, s20, s71\n" \
"s_addc_u32 s43, s21, 0\n" \
"ds_read_b128 a[196:199], v247 offset:8192\n" \
"v_cndmask_b32 v41, v41, v254, vcc\n" \
"v_cndmask_b32 v49, v49, v254, vcc\n" \
"v_cmp_gt_i32 vcc, 48, v251\n" \
"v_cndmask_b32 v42, v42, v254, s[64:65]\n" \
"v_cndmask_b32 v50, v50, v254, s[64:65]\n" \
"v_cmp_gt_i32 s[64:65], 49, v251\n" \
"v_mfma_f32_16x16x32_bf16 v[132:135], a[200:203], v[80:83], v[132:135]\n" \
"ds_read_b128 v[200:203], v247 offset:17408\n" \
"v_lshl_add_u32 v252, v71, 12, v77\n" \
"v_cndmask_b32 v43, v43, v254, s[66:67]\n" \
"v_cndmask_b32 v51, v51, v254, s[66:67]\n" \
"v_cmp_gt_i32 s[66:67], 50, v251\n" \
"v_cndmask_b32 v44, v44, v254, vcc\n" \
"v_cndmask_b32 v52, v52, v254, vcc\n" \
"v_cmp_gt_i32 vcc, 51, v251\n" \
"v_mfma_f32_16x16x32_bf16 v[132:135], a[204:207], v[112:115], v[132:135]\n" \
"s_add_u32 s44, s42, 31744\n" \
"s_addc_u32 s45, s43, 0\n" \
"s_max_u32 s70, s70, 32768\n" \
"v_cndmask_b32 v45, v45, v254, s[64:65]\n" \
"v_cndmask_b32 v53, v53, v254, s[64:65]\n" \
"v_cndmask_b32 v46, v46, v254, s[66:67]\n" \
"v_cndmask_b32 v54, v54, v254, s[66:67]\n" \
"v_cndmask_b32 v47, v47, v254, vcc\n" \
"v_cndmask_b32 v55, v55, v254, vcc\n" \
"v_mfma_f32_16x16x32_bf16 v[164:167], a[200:203], v[88:91], v[164:167]\n" \
"s_sub_u32 s70, s70, 32768\n" \
"v_exp_f32 v8, v8\n" \
"s_and_b32 s71, s61, 1\n" \
"s_lshl_b32 s71, s71, 2\n" \
"v_mfma_f32_16x16x32_bf16 v[164:167], a[204:207], v[120:123], v[164:167]\n" \
"ds_read_b128 a[200:203], v247 offset:1024\n" \
"buffer_store_dwordx4 v[228:231], v79, s[56:59], s77 offen offset:2048 sc1\n" \
"ds_read_b128 v[204:207], v247 offset:25600\n" \
"v_add_f32 v243, v243, v8\n" \
"v_exp_f32 v9, v9\n" \
"v_mfma_f32_16x16x32_bf16 a[40:43], a[208:211], v[72:75], a[40:43]\n" \
"s_add_u32 s77, s77, 0x81000\n" \
"v_add_f32 v243, v243, v9\n" \
"v_exp_f32 v10, v10\n" \
"v_and_or_b32 v71, v71, 3, s71\n" \
"s_lshr_b32 s71, s61, 1\n" \
"v_mfma_f32_16x16x32_bf16 a[40:43], a[212:215], v[104:107], a[40:43]\n" \
"s_lshl_b32 s71, s71, 11\n" \
"ds_read_b128 a[204:207], v247 offset:9216\n" \
"v_add_f32 v243, v243, v10\n" \
"v_cvt_pk_bf16_f32 v8, v8, v9\n" \
"v_exp_f32 v11, v11\n" \
"v_lshlrev_b32 v77, 4, v71\n" \
"v_mfma_f32_16x16x32_bf16 v[136:139], a[208:211], v[80:83], v[136:139]\n" \
"s_add_u32 s70, s70, s71\n" \
"ds_read_b128 v[208:211], v247 offset:18432\n" \
"v_add_f32 v243, v243, v11\n" \
"v_exp_f32 v12, v12\n" \
"v_xor_b32 v77, v77, v76\n" \
"v_mfma_f32_16x16x32_bf16 v[136:139], a[212:215], v[112:115], v[136:139]\n" \
"s_add_u32 s46, s20, s70\n" \
"s_addc_u32 s47, s21, 0\n" \
"v_add_f32 v243, v243, v12\n" \
"v_cvt_pk_bf16_f32 v9, v10, v11\n" \
"v_exp_f32 v13, v13\n" \
"v_lshl_add_u32 v246, v71, 12, v77\n" \
"v_mfma_f32_16x16x32_bf16 v[168:171], a[208:211], v[88:91], v[168:171]\n" \
"s_lshl_b32 s85, s61, 10\n" \
"v_add_f32 v243, v243, v13\n" \
"v_exp_f32 v14, v14\n" \
"s_mov_b64 s[12:13], s[20:21]\n" \
"s_and_b32 s13, s13, 0xffff\n" \
"v_mfma_f32_16x16x32_bf16 v[168:171], a[212:215], v[120:123], v[168:171]\n" \
"s_mov_b32 s14, 4194304\n" \
"ds_read_b128 a[208:211], v247 offset:2048\n" \
"s_mov_b32 s15, 0x20000\n" \
"ds_read_b128 v[212:215], v247 offset:26624\n" \
"v_add_f32 v243, v243, v14\n" \
"v_cvt_pk_bf16_f32 v10, v12, v13\n" \
"v_exp_f32 v15, v15\n" \
"v_mfma_f32_16x16x32_bf16 a[44:47], a[216:219], v[72:75], a[44:47]\n" \
"s_mov_b32 s46, s70\n" \
"v_add_f32 v243, v243, v15\n" \
"v_exp_f32 v40, v40\n" \
"v_lshrrev_b32 v71, 9, v248\n" \
"v_lshl_add_u32 v246, v71, 30, v246\n" \
"v_mfma_f32_16x16x32_bf16 a[44:47], a[220:223], v[104:107], a[44:47]\n" \
"s_add_u32 m0, s95, 118784\n" \
"ds_read_b128 a[212:215], v247 offset:10240\n" \
"v_add_f32 v243, v243, v40\n" \
"v_cvt_pk_bf16_f32 v11, v14, v15\n" \
"v_exp_f32 v41, v41\n" \
"v_mfma_f32_16x16x32_bf16 v[140:143], a[216:219], v[80:83], v[140:143]\n" \
"ds_read_b128 v[216:219], v247 offset:19456\n" \
"global_load_lds_dwordx4 v252, s[42:43] offset:0\n" \
"v_add_f32 v243, v243, v41\n" \
"v_exp_f32 v42, v42\n" \
"v_mfma_f32_16x16x32_bf16 v[140:143], a[220:223], v[112:115], v[140:143]\n" \
"v_add_f32 v243, v243, v42\n" \
"v_cvt_pk_bf16_f32 v40, v40, v41\n" \
"v_exp_f32 v43, v43\n" \
"v_mfma_f32_16x16x32_bf16 v[172:175], a[216:219], v[88:91], v[172:175]\n" \
"v_add_f32 v243, v243, v43\n" \
"v_exp_f32 v44, v44\n" \
"v_mfma_f32_16x16x32_bf16 v[172:175], a[220:223], v[120:123], v[172:175]\n" \
"ds_read_b128 a[216:219], v247 offset:3072\n" \
"global_load_lds_dwordx4 v252, s[42:43] offset:2048\n" \
"ds_read_b128 v[220:223], v247 offset:27648\n" \
"v_add_f32 v243, v243, v44\n" \
"v_cvt_pk_bf16_f32 v41, v42, v43\n" \
"v_exp_f32 v45, v45\n" \
"v_mfma_f32_16x16x32_bf16 a[48:51], a[224:227], v[72:75], a[48:51]\n" \
"s_add_u32 s42, s42, 0x180\n" \
"s_addc_u32 s43, s43, 0\n" \
"v_add_f32 v243, v243, v45\n" \
"v_exp_f32 v46, v46\n" \
"v_mfma_f32_16x16x32_bf16 a[48:51], a[228:231], v[104:107], a[48:51]\n" \
"ds_read_b128 a[220:223], v247 offset:11264\n" \
"v_add_f32 v243, v243, v46\n" \
"v_cvt_pk_bf16_f32 v42, v44, v45\n" \
"v_exp_f32 v47, v47\n" \
"v_mfma_f32_16x16x32_bf16 v[144:147], a[224:227], v[80:83], v[144:147]\n" \
"ds_read_b128 v[224:227], v247 offset:20480\n" \
"global_load_lds_dwordx4 v252, s[44:45] offset:1024\n" \
"v_add_f32 v243, v243, v47\n" \
"v_cvt_pk_bf16_f32 v43, v46, v47\n" \
"v_exp_f32 v24, v24\n" \
"v_mfma_f32_16x16x32_bf16 v[144:147], a[228:231], v[112:115], v[144:147]\n" \
"v_add_f32 v243, v243, v24\n" \
"v_exp_f32 v25, v25\n" \
"v_mfma_f32_16x16x32_bf16 v[176:179], a[224:227], v[88:91], v[176:179]\n" \
"v_add_f32 v243, v243, v25\n" \
"v_exp_f32 v26, v26\n" \
"v_mfma_f32_16x16x32_bf16 v[176:179], a[228:231], v[120:123], v[176:179]\n" \
"ds_read_b128 a[224:227], v247 offset:4096\n" \
"global_load_lds_dwordx4 v252, s[44:45] offset:3072\n" \
"ds_read_b128 v[228:231], v247 offset:28672\n" \
"v_add_f32 v243, v243, v26\n" \
"v_cvt_pk_bf16_f32 v24, v24, v25\n" \
"v_exp_f32 v27, v27\n" \
"v_mfma_f32_16x16x32_bf16 a[52:55], a[232:235], v[72:75], a[52:55]\n" \
"s_add_u32 s44, s44, 0x180\n" \
"s_addc_u32 s45, s45, 0\n" \
"v_add_f32 v243, v243, v27\n" \
"v_exp_f32 v28, v28\n" \
"v_mfma_f32_16x16x32_bf16 a[52:55], a[236:239], v[104:107], a[52:55]\n" \
"s_add_u32 m0, s85, 135168\n" \
"ds_read_b128 a[228:231], v247 offset:12288\n" \
"v_add_f32 v243, v243, v28\n" \
"v_cvt_pk_bf16_f32 v25, v26, v27\n" \
"v_exp_f32 v29, v29\n" \
"v_mfma_f32_16x16x32_bf16 v[148:151], a[232:235], v[80:83], v[148:151]\n" \
"ds_read_b128 v[232:235], v247 offset:21504\n" \
"v_add_f32 v243, v243, v29\n" \
"v_exp_f32 v30, v30\n" \
"v_mfma_f32_16x16x32_bf16 v[148:151], a[236:239], v[112:115], v[148:151]\n" \
"buffer_load_dwordx4 v246, s[12:15], s46 offen lds\n" \
"v_add_f32 v243, v243, v30\n" \
"v_cvt_pk_bf16_f32 v26, v28, v29\n" \
"v_exp_f32 v31, v31\n" \
"v_mfma_f32_16x16x32_bf16 v[180:183], a[232:235], v[88:91], v[180:183]\n" \
"s_add_u32 s46, s46, 0x180\n" \
"v_add_f32 v243, v243, v31\n" \
"v_exp_f32 v56, v56\n" \
"v_mfma_f32_16x16x32_bf16 v[180:183], a[236:239], v[120:123], v[180:183]\n" \
"ds_read_b128 a[232:235], v247 offset:5120\n" \
"ds_read_b128 v[236:239], v247 offset:29696\n" \
"v_add_f32 v243, v243, v56\n" \
"v_cvt_pk_bf16_f32 v27, v30, v31\n" \
"v_exp_f32 v57, v57\n" \
"v_mfma_f32_16x16x32_bf16 a[56:59], a[240:243], v[72:75], a[56:59]\n" \
"v_add_f32 v243, v243, v57\n" \
"v_exp_f32 v58, v58\n" \
"v_mfma_f32_16x16x32_bf16 a[56:59], a[244:247], v[104:107], a[56:59]\n" \
"ds_read_b128 a[236:239], v247 offset:13312\n" \
"v_add_f32 v243, v243, v58\n" \
"v_cvt_pk_bf16_f32 v56, v56, v57\n" \
"v_exp_f32 v59, v59\n" \
"v_mfma_f32_16x16x32_bf16 v[152:155], a[240:243], v[80:83], v[152:155]\n" \
"ds_read_b128 v[0:3], v247 offset:22528\n" \
"v_add_f32 v243, v243, v59\n" \
"v_exp_f32 v60, v60\n" \
"v_mfma_f32_16x16x32_bf16 v[152:155], a[244:247], v[112:115], v[152:155]\n" \
"v_add_f32 v243, v243, v60\n" \
"v_cvt_pk_bf16_f32 v57, v58, v59\n" \
"v_exp_f32 v61, v61\n" \
"v_mfma_f32_16x16x32_bf16 v[184:187], a[240:243], v[88:91], v[184:187]\n" \
"v_add_f32 v243, v243, v61\n" \
"v_exp_f32 v62, v62\n" \
"v_mfma_f32_16x16x32_bf16 v[184:187], a[244:247], v[120:123], v[184:187]\n" \
"ds_read_b128 a[240:243], v247 offset:6144\n" \
"ds_read_b128 v[4:7], v247 offset:30720\n" \
"v_add_f32 v243, v243, v62\n" \
"v_cvt_pk_bf16_f32 v58, v60, v61\n" \
"v_exp_f32 v63, v63\n" \
"v_mfma_f32_16x16x32_bf16 a[60:63], a[248:251], v[72:75], a[60:63]\n" \
"v_add_f32 v243, v243, v63\n" \
"v_cvt_pk_bf16_f32 v59, v62, v63\n" \
"v_exp_f32 v16, v16\n" \
"v_mfma_f32_16x16x32_bf16 a[60:63], a[252:255], v[104:107], a[60:63]\n" \
"ds_read_b128 a[244:247], v247 offset:14336\n" \
"v_add_f32 v242, v242, v16\n" \
"v_exp_f32 v17, v17\n" \
"v_mfma_f32_16x16x32_bf16 v[156:159], a[248:251], v[80:83], v[156:159]\n" \
"ds_read_b128 v[32:35], v247 offset:23552\n" \
"v_add_f32 v242, v242, v17\n" \
"v_exp_f32 v18, v18\n" \
"v_mfma_f32_16x16x32_bf16 v[156:159], a[252:255], v[112:115], v[156:159]\n" \
"v_add_f32 v242, v242, v18\n" \
"v_cvt_pk_bf16_f32 v16, v16, v17\n" \
"v_exp_f32 v19, v19\n" \
"v_mfma_f32_16x16x32_bf16 v[188:191], a[248:251], v[88:91], v[188:191]\n" \
"v_add_f32 v242, v242, v19\n" \
"v_exp_f32 v20, v20\n" \
"v_mfma_f32_16x16x32_bf16 v[188:191], a[252:255], v[120:123], v[188:191]\n" \
"s_waitcnt lgkmcnt(2)\n" \
"ds_read_b128 a[248:251], v247 offset:7168\n" \
"ds_read_b128 v[36:39], v247 offset:31744\n" \
"v_add_f32 v242, v242, v20\n" \
"v_cvt_pk_bf16_f32 v17, v18, v19\n" \
"v_exp_f32 v21, v21\n" \
"v_mfma_f32_16x16x32_bf16 v[160:163], v[192:195], v[8:11], v[160:163]\n" \
"v_add_f32 v242, v242, v21\n" \
"v_exp_f32 v22, v22\n" \
"v_mfma_f32_16x16x32_bf16 v[160:163], v[196:199], v[40:43], v[160:163]\n" \
"ds_read_b128 a[252:255], v247 offset:15360\n" \
"v_add_f32 v242, v242, v22\n" \
"v_cvt_pk_bf16_f32 v18, v20, v21\n" \
"v_exp_f32 v23, v23\n" \
"v_mfma_f32_16x16x32_bf16 v[164:167], v[200:203], v[8:11], v[164:167]\n" \
"v_mov_b32 v112, v241\n" \
"v_add_f32 v242, v242, v23\n" \
"v_exp_f32 v48, v48\n" \
"v_mfma_f32_16x16x32_bf16 v[164:167], v[204:207], v[40:43], v[164:167]\n" \
"v_add_f32 v242, v242, v48\n" \
"v_cvt_pk_bf16_f32 v19, v22, v23\n" \
"v_mfma_f32_16x16x32_bf16 v[168:171], v[208:211], v[8:11], v[168:171]\n" \
"v_exp_f32 v49, v49\n" \
"v_permlane16_swap_b32 v112, v241\n" \
"v_add_f32 v241, v241, v112\n" \
"v_mov_b32 v112, v241\n" \
"v_mfma_f32_16x16x32_bf16 v[168:171], v[212:215], v[40:43], v[168:171]\n" \
"s_waitcnt vmcnt(0) lgkmcnt(0)\n" \
"v_add_f32 v242, v242, v49\n" \
"v_exp_f32 v50, v50\n" \
"v_mfma_f32_16x16x32_bf16 v[172:175], v[216:219], v[8:11], v[172:175]\n" \
"s_barrier\n" \
"v_add_f32 v242, v242, v50\n" \
"v_cvt_pk_bf16_f32 v48, v48, v49\n" \
"v_permlane32_swap_b32 v112, v241\n" \
"v_add_f32 v241, v241, v112\n" \
"v_rcp_f32 v112, v241\n" \
"v_mul_f32 v241, 0x4d800000, v241\n" \
"v_add_u32 v113, 0x1c90080, v249\n" \
"v_cndmask_b32 v113, v249, v113, s[16:17]\n" \
"v_accvgpr_read_b32 v96, a32\n" \
"v_accvgpr_read_b32 v97, a33\n" \
"v_accvgpr_read_b32 v98, a34\n" \
"v_accvgpr_read_b32 v99, a35\n" \
"v_accvgpr_read_b32 v100, a36\n" \
"v_accvgpr_read_b32 v101, a37\n" \
"v_accvgpr_read_b32 v102, a38\n" \
"v_accvgpr_read_b32 v103, a39\n" \
"v_pk_mul_f32 v[96:97], v[96:97], v[112:113] op_sel_hi:[1,0]\n" \
"v_pk_mul_f32 v[98:99], v[98:99], v[112:113] op_sel_hi:[1,0]\n" \
"v_pk_mul_f32 v[100:101], v[100:101], v[112:113] op_sel_hi:[1,0]\n" \
"v_pk_mul_f32 v[102:103], v[102:103], v[112:113] op_sel_hi:[1,0]\n" \
"v_cvt_pk_bf16_f32 v104, v96, v97\n" \
"v_mfma_f32_16x16x32_bf16 v[172:175], v[220:223], v[40:43], v[172:175]\n" \
"s_add_u32 m0, s84, 90112\n" \
"v_cvt_pk_bf16_f32 v105, v98, v99\n" \
"v_cvt_pk_bf16_f32 v106, v100, v101\n" \
"v_cvt_pk_bf16_f32 v107, v102, v103\n" \
"v_exp_f32 v51, v51\n" \
"v_pk_mul_f32 v[96:97], v[96:97], v[98:99]\n" \
"v_pk_fma_f32 v[96:97], v[100:101], v[102:103], v[96:97]\n" \
"v_fma_f32 v241, v96, v97, v241\n" \
"v_mfma_f32_16x16x32_bf16 v[176:179], v[224:227], v[8:11], v[176:179]\n" \
"buffer_store_dwordx4 v[104:107], v113, s[56:59], s77 offen offset:0 sc1\n" \
"global_load_lds_dwordx4 v253, s[48:49] offset:-4096\n" \
"v_add_f32 v242, v242, v51\n" \
"v_accvgpr_read_b32 v96, a40\n" \
"v_accvgpr_read_b32 v97, a41\n" \
"v_accvgpr_read_b32 v98, a42\n" \
"v_accvgpr_read_b32 v99, a43\n" \
"v_accvgpr_read_b32 v100, a44\n" \
"v_accvgpr_read_b32 v101, a45\n" \
"v_accvgpr_read_b32 v102, a46\n" \
"v_accvgpr_read_b32 v103, a47\n" \
"v_pk_mul_f32 v[96:97], v[96:97], v[112:113] op_sel_hi:[1,0]\n" \
"v_pk_mul_f32 v[98:99], v[98:99], v[112:113] op_sel_hi:[1,0]\n" \
"v_pk_mul_f32 v[100:101], v[100:101], v[112:113] op_sel_hi:[1,0]\n" \
"v_pk_mul_f32 v[102:103], v[102:103], v[112:113] op_sel_hi:[1,0]\n" \
"v_cvt_pk_bf16_f32 v108, v96, v97\n" \
"v_cvt_pk_bf16_f32 v109, v98, v99\n" \
"v_cvt_pk_bf16_f32 v110, v100, v101\n" \
"v_cvt_pk_bf16_f32 v111, v102, v103\n" \
"v_pk_mul_f32 v[96:97], v[96:97], v[98:99]\n" \
"v_pk_fma_f32 v[96:97], v[100:101], v[102:103], v[96:97]\n" \
"v_fma_f32 v241, v96, v97, v241\n" \
"v_mfma_f32_16x16x32_bf16 v[176:179], v[228:231], v[40:43], v[176:179]\n" \
"global_load_lds_dwordx4 v253, s[48:49] offset:-2048\n" \
"v_exp_f32 v52, v52\n" \
"v_mfma_f32_16x16x32_bf16 v[180:183], v[232:235], v[8:11], v[180:183]\n" \
"v_add_f32 v242, v242, v52\n" \
"v_cvt_pk_bf16_f32 v49, v50, v51\n" \
"v_mfma_f32_16x16x32_bf16 v[180:183], v[236:239], v[40:43], v[180:183]\n" \
"global_load_lds_dwordx4 v253, s[48:49] offset:0\n" \
"v_exp_f32 v53, v53\n" \
"v_mfma_f32_16x16x32_bf16 v[184:187], v[0:3], v[8:11], v[184:187]\n" \
"s_add_u32 s48, s48, 0x300000\n" \
"s_addc_u32 s49, s49, 0\n" \
"v_add_f32 v242, v242, v53\n" \
"v_mfma_f32_16x16x32_bf16 v[184:187], v[4:7], v[40:43], v[184:187]\n" \
"global_load_lds_dwordx4 v253, s[50:51] offset:-3072\n" \
"v_exp_f32 v54, v54\n" \
"v_mfma_f32_16x16x32_bf16 v[188:191], v[32:35], v[8:11], v[188:191]\n" \
"v_add_f32 v242, v242, v54\n" \
"v_cvt_pk_bf16_f32 v50, v52, v53\n" \
"v_mfma_f32_16x16x32_bf16 v[188:191], v[36:39], v[40:43], v[188:191]\n" \
"global_load_lds_dwordx4 v253, s[50:51] offset:-1024\n" \
"v_exp_f32 v55, v55\n" \
"v_mfma_f32_16x16x32_bf16 v[160:163], a[192:195], v[24:27], v[160:163]\n" \
"v_add_f32 v242, v242, v55\n" \
"v_cvt_pk_bf16_f32 v51, v54, v55\n" \
"v_mfma_f32_16x16x32_bf16 v[160:163], a[196:199], v[56:59], v[160:163]\n" \
"s_movk_i32 s89, 0xc0\n" \
"global_load_lds_dwordx4 v253, s[50:51] offset:1024\n" \
"v_mfma_f32_16x16x32_bf16 v[164:167], a[200:203], v[24:27], v[164:167]\n" \
"s_add_u32 s50, s50, 0x300000\n" \
"s_addc_u32 s51, s51, 0\n" \
"s_add_u32 m0, s95, 65536\n" \
"v_mfma_f32_16x16x32_bf16 v[164:167], a[204:207], v[56:59], v[164:167]\n" \
"s_movk_i32 s90, 288\n" \
"global_load_lds_dwordx4 v252, s[42:43] offset:0\n" \
"buffer_store_dwordx4 v[108:111], v113, s[56:59], s77 offen offset:2048 sc1\n" \
"v_lshrrev_b32 v211, 4, v248\n" \
"v_lshrrev_b32 v214, 8, v248\n" \
"v_and_b32 v215, 7, v211\n" \
"v_xor_b32 v214, v214, v215\n" \
"v_mfma_f32_16x16x32_bf16 v[168:171], a[208:211], v[24:27], v[168:171]\n" \
"s_lshr_b32 s71, s61, 1\n" \
"s_lshl_b32 s70, s71, 11\n" \
"s_add_u32 s70, s70, 118784\n" \
"v_mfma_f32_16x16x32_bf16 v[168:171], a[212:215], v[56:59], v[168:171]\n" \
"global_load_lds_dwordx4 v252, s[42:43] offset:2048\n" \
"v_and_b32 v216, 15, v211\n" \
"v_mfma_f32_16x16x32_bf16 v[172:175], a[216:219], v[24:27], v[172:175]\n" \
"s_add_u32 s42, s42, 0x400\n" \
"s_addc_u32 s43, s43, 0\n" \
"v_lshlrev_b32 v213, 7, v216\n" \
"v_lshl_add_u32 v213, v214, 4, v213\n" \
"v_mfma_f32_16x16x32_bf16 v[172:175], a[220:223], v[56:59], v[172:175]\n" \
"global_load_lds_dwordx4 v252, s[44:45] offset:1024\n" \
"v_add_u32 v205, s70, v213\n" \
"v_xor_b32 v206, 64, v205\n" \
"v_lshrrev_b32 v216, 2, v216\n" \
"v_lshl_add_u32 v216, v216, 9, v213\n" \
"v_add_u32 v207, 135168, v216\n" \
"v_xor_b32 v208, 64, v207\n" \
"v_mfma_f32_16x16x32_bf16 v[176:179], a[224:227], v[24:27], v[176:179]\n" \
"s_and_b32 s70, s61, 1\n" \
"s_lshl_b32 s70, s70, 9\n" \
"s_add_u32 s70, s70, 139264\n" \
"v_mfma_f32_16x16x32_bf16 v[176:179], a[228:231], v[56:59], v[176:179]\n" \
"s_lshl_b32 s71, s71, 5\n" \
"global_load_lds_dwordx4 v252, s[44:45] offset:3072\n" \
"v_mfma_f32_16x16x32_bf16 v[180:183], a[232:235], v[24:27], v[180:183]\n" \
"s_add_u32 s44, s44, 0x400\n" \
"s_addc_u32 s45, s45, 0\n" \
"s_add_u32 m0, s85, 81920\n" \
"v_mfma_f32_16x16x32_bf16 v[180:183], a[236:239], v[56:59], v[180:183]\n" \
"buffer_load_dwordx4 v246, s[12:15], s46 offen lds\n" \
"v_mfma_f32_16x16x32_bf16 v[184:187], a[240:243], v[24:27], v[184:187]\n" \
"s_add_u32 s46, s46, 0x400\n" \
"v_and_b32 v213, 3, v211\n" \
"v_lshlrev_b32 v213, 3, v213\n" \
"v_mfma_f32_16x16x32_bf16 v[184:187], a[244:247], v[56:59], v[184:187]\n" \
"s_add_u32 m0, s95, 0\n" \
"v_and_or_b32 v213, v248, s89, v213\n" \
"v_bfe_u32 v214, v211, 4, 1\n" \
"v_mfma_f32_16x16x32_bf16 v[188:191], a[248:251], v[24:27], v[188:191]\n" \
"global_load_lds_dwordx4 v252, s[42:43] offset:0\n" \
"v_mad_u32_u24 v213, v214, s90, v213\n" \
"v_lshrrev_b32 v214, 5, v211\n" \
"v_mul_u32_u24 v214, 6144, v214\n" \
"v_add3_u32 v213, v213, v214, s70\n" \
"v_xor_b32 v209, s71, v213\n" \
"v_xor_b32 v210, 32, v209\n" \
"v_mfma_f32_16x16x32_bf16 v[188:191], a[252:255], v[56:59], v[188:191]\n" \
"s_add_u32 s77, s77, 0x1000\n" \
"ds_read_b128 a[64:67], v207 offset:0\n" \
"ds_read_b64_tr_b16 a[96:97], v209 offset:2048\n" \
"v_accvgpr_read_b32 v96, a48\n" \
"v_accvgpr_read_b32 v97, a49\n" \
"v_accvgpr_read_b32 v98, a50\n" \
"v_accvgpr_read_b32 v99, a51\n" \
"v_accvgpr_read_b32 v100, a52\n" \
"v_accvgpr_read_b32 v101, a53\n" \
"v_accvgpr_read_b32 v102, a54\n" \
"v_accvgpr_read_b32 v103, a55\n" \
"v_pk_mul_f32 v[96:97], v[96:97], v[112:113] op_sel_hi:[1,0]\n" \
"v_pk_mul_f32 v[98:99], v[98:99], v[112:113] op_sel_hi:[1,0]\n" \
"v_pk_mul_f32 v[100:101], v[100:101], v[112:113] op_sel_hi:[1,0]\n" \
"v_pk_mul_f32 v[102:103], v[102:103], v[112:113] op_sel_hi:[1,0]\n" \
"v_cvt_pk_bf16_f32 v104, v96, v97\n" \
"v_cvt_pk_bf16_f32 v105, v98, v99\n" \
"v_cvt_pk_bf16_f32 v106, v100, v101\n" \
"v_cvt_pk_bf16_f32 v107, v102, v103\n" \
"v_pk_mul_f32 v[96:97], v[96:97], v[98:99]\n" \
"v_pk_fma_f32 v[96:97], v[100:101], v[102:103], v[96:97]\n" \
"v_mfma_f32_16x16x32_bf16 v[128:131], a[192:195], v[16:19], v[128:131]\n" \
"ds_read_b64_tr_b16 a[98:99], v209 offset:3072\n" \
"v_fma_f32 v241, v96, v97, v241\n" \
"v_mfma_f32_16x16x32_bf16 v[128:131], a[196:199], v[48:51], v[128:131]\n" \
"ds_read_b128 a[68:71], v208 offset:0\n" \
"global_load_lds_dwordx4 v252, s[42:43] offset:2048\n" \
"v_mfma_f32_16x16x32_bf16 v[132:135], a[200:203], v[16:19], v[132:135]\n" \
"ds_read_b64_tr_b16 a[100:101], v209 offset:14336\n" \
"buffer_store_dwordx4 v[104:107], v113, s[56:59], s77 offen offset:0 sc1\n" \
"ds_read_b64_tr_b16 a[102:103], v209 offset:15360\n" \
"v_accvgpr_read_b32 v96, a56\n" \
"v_accvgpr_read_b32 v97, a57\n" \
"v_accvgpr_read_b32 v98, a58\n" \
"v_mfma_f32_16x16x32_bf16 v[132:135], a[204:207], v[48:51], v[132:135]\n" \
"s_add_u32 s42, s42, 0xfffffb00\n" \
"s_addc_u32 s43, s43, -1\n" \
"ds_read_b128 a[72:75], v205 offset:0\n" \
"v_mfma_f32_16x16x32_bf16 v[136:139], a[208:211], v[16:19], v[136:139]\n" \
"ds_read_b64_tr_b16 a[104:105], v209 offset:0\n" \
"global_load_lds_dwordx4 v252, s[44:45] offset:1024\n" \
"v_mfma_f32_16x16x32_bf16 v[136:139], a[212:215], v[48:51], v[136:139]\n" \
"ds_read_b128 a[76:79], v206 offset:0\n" \
"ds_read_b64_tr_b16 a[106:107], v209 offset:1024\n" \
"v_mfma_f32_16x16x32_bf16 v[140:143], a[216:219], v[16:19], v[140:143]\n" \
"ds_read_b64_tr_b16 a[108:109], v209 offset:12288\n" \
"global_load_lds_dwordx4 v252, s[44:45] offset:3072\n" \
"v_mfma_f32_16x16x32_bf16 v[140:143], a[220:223], v[48:51], v[140:143]\n" \
"s_add_u32 s44, s44, 0xfffffb00\n" \
"s_addc_u32 s45, s45, -1\n" \
"ds_read_b64_tr_b16 a[110:111], v209 offset:13312\n" \
"v_mfma_f32_16x16x32_bf16 v[144:147], a[224:227], v[16:19], v[144:147]\n" \
"ds_read_b128 a[80:83], v205 offset:4096\n" \
"ds_read_b64_tr_b16 a[112:113], v210 offset:0\n" \
"v_mfma_f32_16x16x32_bf16 v[144:147], a[228:231], v[48:51], v[144:147]\n" \
"s_add_u32 m0, s85, 16384\n" \
"ds_read_b64_tr_b16 a[114:115], v210 offset:1024\n" \
"v_accvgpr_read_b32 v99, a59\n" \
"v_mfma_f32_16x16x32_bf16 v[148:151], a[232:235], v[16:19], v[148:151]\n" \
"ds_read_b64_tr_b16 a[116:117], v210 offset:12288\n" \
"v_accvgpr_read_b32 v100, a60\n" \
"v_accvgpr_read_b32 v101, a61\n" \
"v_accvgpr_read_b32 v102, a62\n" \
"v_accvgpr_read_b32 v103, a63\n" \
"v_pk_mul_f32 v[96:97], v[96:97], v[112:113] op_sel_hi:[1,0]\n" \
"v_pk_mul_f32 v[98:99], v[98:99], v[112:113] op_sel_hi:[1,0]\n" \
"v_pk_mul_f32 v[100:101], v[100:101], v[112:113] op_sel_hi:[1,0]\n" \
"v_pk_mul_f32 v[102:103], v[102:103], v[112:113] op_sel_hi:[1,0]\n" \
"v_mfma_f32_16x16x32_bf16 v[148:151], a[236:239], v[48:51], v[148:151]\n" \
"ds_read_b128 a[84:87], v206 offset:4096\n" \
"ds_read_b64_tr_b16 a[118:119], v210 offset:13312\n" \
"v_mfma_f32_16x16x32_bf16 v[152:155], a[240:243], v[16:19], v[152:155]\n" \
"ds_read_b128 a[88:91], v205 offset:8192\n" \
"v_cvt_pk_bf16_f32 v108, v96, v97\n" \
"v_cvt_pk_bf16_f32 v109, v98, v99\n" \
"v_cvt_pk_bf16_f32 v110, v100, v101\n" \
"v_cvt_pk_bf16_f32 v111, v102, v103\n" \
"v_pk_mul_f32 v[96:97], v[96:97], v[98:99]\n" \
"v_pk_fma_f32 v[96:97], v[100:101], v[102:103], v[96:97]\n" \
"v_mfma_f32_16x16x32_bf16 v[152:155], a[244:247], v[48:51], v[152:155]\n" \
"ds_read_b128 a[92:95], v206 offset:8192\n" \
"v_fma_f32 v241, v96, v97, v241\n" \
"v_mfma_f32_16x16x32_bf16 v[156:159], a[248:251], v[16:19], v[156:159]\n" \
"buffer_store_dwordx4 v[108:111], v113, s[56:59], s77 offen offset:2048 sc1\n" \
"buffer_load_dwordx4 v246, s[12:15], s46 offen lds\n" \
"s_add_u32 s77, s77, 0xfff7d000\n" \
"v_mfma_f32_16x16x32_bf16 v[156:159], a[252:255], v[48:51], v[156:159]\n" \
"s_waitcnt lgkmcnt(4)\n" \
"s_branch Ljoin_%=\n" \
"Lexact_%=:\n" \
"v_mov_b32 v190, 0xff800000\n" \
"v_add_u32 v191, 0x1c90080, v249\n" \
"v_cndmask_b32 v191, v249, v191, s[16:17]\n" \
"s_and_b32 s80, s61, 2\n" \
"s_lshl_b32 s80, s80, 4\n" \
"v_subrev_u32 v186, s80, v251\n" \
"v_accvgpr_read_b32 v160, a128\n" \
"v_accvgpr_read_b32 v161, a129\n" \
"v_accvgpr_read_b32 v162, a130\n" \
"v_accvgpr_read_b32 v163, a131\n" \
"v_accvgpr_read_b32 v164, a132\n" \
"v_accvgpr_read_b32 v165, a133\n" \
"v_accvgpr_read_b32 v166, a134\n" \
"v_accvgpr_read_b32 v167, a135\n" \
"v_accvgpr_read_b32 v168, a136\n" \
"v_accvgpr_read_b32 v169, a137\n" \
"v_accvgpr_read_b32 v170, a138\n" \
"v_accvgpr_read_b32 v171, a139\n" \
"v_accvgpr_read_b32 v172, a140\n" \
"v_accvgpr_read_b32 v173, a141\n" \
"v_accvgpr_read_b32 v174, a142\n" \
"v_accvgpr_read_b32 v175, a143\n" \
"s_lshl_b32 s75, s63, 2\n" \
"s_lshr_b32 s80, s61, 1\n" \
"s_add_u32 s75, s75, s80\n" \
"s_add_u32 s75, s75, 0\n" \
"s_add_u32 s76, s77, 0\n" \
"s_call_b64 s[68:69], Lex1_%=\n" \
"v_accvgpr_read_b32 v160, a144\n" \
"v_accvgpr_read_b32 v161, a145\n" \
"v_accvgpr_read_b32 v162, a146\n" \
"v_accvgpr_read_b32 v163, a147\n" \
"v_accvgpr_read_b32 v164, a148\n" \
"v_accvgpr_read_b32 v165, a149\n" \
"v_accvgpr_read_b32 v166, a150\n" \
"v_accvgpr_read_b32 v167, a151\n" \
"v_accvgpr_read_b32 v168, a152\n" \
"v_accvgpr_read_b32 v169, a153\n" \
"v_accvgpr_read_b32 v170, a154\n" \
"v_accvgpr_read_b32 v171, a155\n" \
"v_accvgpr_read_b32 v172, a156\n" \
"v_accvgpr_read_b32 v173, a157\n" \
"v_accvgpr_read_b32 v174, a158\n" \
"v_accvgpr_read_b32 v175, a159\n" \
"s_lshl_b32 s75, s63, 2\n" \
"s_lshr_b32 s80, s61, 1\n" \
"s_add_u32 s75, s75, s80\n" \
"s_add_u32 s75, s75, 2\n" \
"s_add_u32 s76, s77, 532480\n" \
"s_call_b64 s[68:69], Lex1_%=\n" \
"v_accvgpr_read_b32 v160, a160\n" \
"v_accvgpr_read_b32 v161, a161\n" \
"v_accvgpr_read_b32 v162, a162\n" \
"v_accvgpr_read_b32 v163, a163\n" \
"v_accvgpr_read_b32 v164, a164\n" \
"v_accvgpr_read_b32 v165, a165\n" \
"v_accvgpr_read_b32 v166, a166\n" \
"v_accvgpr_read_b32 v167, a167\n" \
"v_accvgpr_read_b32 v168, a168\n" \
"v_accvgpr_read_b32 v169, a169\n" \
"v_accvgpr_read_b32 v170, a170\n" \
"v_accvgpr_read_b32 v171, a171\n" \
"v_accvgpr_read_b32 v172, a172\n" \
"v_accvgpr_read_b32 v173, a173\n" \
"v_accvgpr_read_b32 v174, a174\n" \
"v_accvgpr_read_b32 v175, a175\n" \
"s_lshl_b32 s75, s63, 2\n" \
"s_sub_u32 s75, 60, s75\n" \
"s_lshr_b32 s80, s61, 1\n" \
"s_add_u32 s75, s75, s80\n" \
"s_add_u32 s75, s75, 0\n" \
"s_add_u32 s76, s78, 0\n" \
"s_call_b64 s[68:69], Lex1_%=\n" \
"v_accvgpr_read_b32 v160, a176\n" \
"v_accvgpr_read_b32 v161, a177\n" \
"v_accvgpr_read_b32 v162, a178\n" \
"v_accvgpr_read_b32 v163, a179\n" \
"v_accvgpr_read_b32 v164, a180\n" \
"v_accvgpr_read_b32 v165, a181\n" \
"v_accvgpr_read_b32 v166, a182\n" \
"v_accvgpr_read_b32 v167, a183\n" \
"v_accvgpr_read_b32 v168, a184\n" \
"v_accvgpr_read_b32 v169, a185\n" \
"v_accvgpr_read_b32 v170, a186\n" \
"v_accvgpr_read_b32 v171, a187\n" \
"v_accvgpr_read_b32 v172, a188\n" \
"v_accvgpr_read_b32 v173, a189\n" \
"v_accvgpr_read_b32 v174, a190\n" \
"v_accvgpr_read_b32 v175, a191\n" \
"s_lshl_b32 s75, s63, 2\n" \
"s_sub_u32 s75, 60, s75\n" \
"s_lshr_b32 s80, s61, 1\n" \
"s_add_u32 s75, s75, s80\n" \
"s_add_u32 s75, s75, 2\n" \
"s_add_u32 s76, s78, 532480\n" \
"s_call_b64 s[68:69], Lex1_%=\n" \
"s_branch Lexr_%=\n" \
"Lex1_%=:\n" \
"v_mov_b32 v128, 0\n" \
"v_mov_b32 v129, 0\n" \
"v_mov_b32 v130, 0\n" \
"v_mov_b32 v131, 0\n" \
"v_mov_b32 v132, 0\n" \
"v_mov_b32 v133, 0\n" \
"v_mov_b32 v134, 0\n" \
"v_mov_b32 v135, 0\n" \
"v_mov_b32 v136, 0\n" \
"v_mov_b32 v137, 0\n" \
"v_mov_b32 v138, 0\n" \
"v_mov_b32 v139, 0\n" \
"v_mov_b32 v140, 0\n" \
"v_mov_b32 v141, 0\n" \
"v_mov_b32 v142, 0\n" \
"v_mov_b32 v143, 0\n" \
"v_mov_b32 v144, 0\n" \
"v_mov_b32 v145, 0\n" \
"v_mov_b32 v146, 0\n" \
"v_mov_b32 v147, 0\n" \
"v_mov_b32 v148, 0\n" \
"v_mov_b32 v149, 0\n" \
"v_mov_b32 v150, 0\n" \
"v_mov_b32 v151, 0\n" \
"v_mov_b32 v152, 0\n" \
"v_mov_b32 v153, 0\n" \
"v_mov_b32 v154, 0\n" \
"v_mov_b32 v155, 0\n" \
"v_mov_b32 v156, 0\n" \
"v_mov_b32 v157, 0\n" \
"v_mov_b32 v158, 0\n" \
"v_mov_b32 v159, 0\n" \
"v_mov_b32 v184, 0xf149f2ca\n" \
"v_mov_b32 v185, 0\n" \
"s_mov_b32 s73, 0\n" \
"s_mul_i32 s74, s62, 557824\n" \
"Lex2_%=:\n" \
"s_add_u32 s80, s74, 4096\n" \
"buffer_load_dwordx4 a[0:3], v249, s[56:59], s74 offen offset:0 sc1\n" \
"buffer_load_dwordx4 a[4:7], v249, s[56:59], s74 offen offset:2048 sc1\n" \
"buffer_load_dwordx4 a[8:11], v249, s[56:59], s80 offen offset:0 sc1\n" \
"buffer_load_dwordx4 a[12:15], v249, s[56:59], s80 offen offset:2048 sc1\n" \
"buffer_load_dwordx4 a[16:19], v249, s[56:59], s74 offen offset:256 sc1\n" \
"buffer_load_dwordx4 a[20:23], v249, s[56:59], s74 offen offset:2304 sc1\n" \
"buffer_load_dwordx4 a[24:27], v249, s[56:59], s80 offen offset:256 sc1\n" \
"buffer_load_dwordx4 a[28:31], v249, s[56:59], s80 offen offset:2304 sc1\n" \
"s_add_u32 s80, s74, 17850368\n" \
"s_add_u32 s81, s80, 4096\n" \
"buffer_load_dwordx4 a[32:35], v248, s[56:59], s80 offen offset:0 sc1\n" \
"buffer_load_dwordx4 a[36:39], v248, s[56:59], s80 offen offset:1024 sc1\n" \
"buffer_load_dwordx4 a[40:43], v248, s[56:59], s80 offen offset:2048 sc1\n" \
"buffer_load_dwordx4 a[44:47], v248, s[56:59], s80 offen offset:3072 sc1\n" \
"buffer_load_dwordx4 a[48:51], v248, s[56:59], s81 offen offset:0 sc1\n" \
"buffer_load_dwordx4 a[52:55], v248, s[56:59], s81 offen offset:1024 sc1\n" \
"buffer_load_dwordx4 a[56:59], v248, s[56:59], s81 offen offset:2048 sc1\n" \
"buffer_load_dwordx4 a[60:63], v248, s[56:59], s81 offen offset:3072 sc1\n" \
"s_waitcnt vmcnt(0)\n" \
"v_mfma_f32_16x16x32_bf16 v[176:179], a[0:3], v[160:163], 0\n" \
"v_mfma_f32_16x16x32_bf16 v[176:179], a[4:7], v[164:167], v[176:179]\n" \
"v_mfma_f32_16x16x32_bf16 v[176:179], a[8:11], v[168:171], v[176:179]\n" \
"v_mfma_f32_16x16x32_bf16 v[176:179], a[12:15], v[172:175], v[176:179]\n" \
"v_mfma_f32_16x16x32_bf16 v[180:183], a[16:19], v[160:163], 0\n" \
"v_mfma_f32_16x16x32_bf16 v[180:183], a[20:23], v[164:167], v[180:183]\n" \
"v_mfma_f32_16x16x32_bf16 v[180:183], a[24:27], v[168:171], v[180:183]\n" \
"v_mfma_f32_16x16x32_bf16 v[180:183], a[28:31], v[172:175], v[180:183]\n" \
"s_nop 7\n" \
"s_nop 1\n" \
"s_cmp_eq_u32 s73, s75\n" \
"s_cbranch_scc0 Lex3_%=\n" \
"v_cmp_gt_i32 vcc, 0, v186\n" \
"s_nop 1\n" \
"v_cndmask_b32 v176, v176, v190, vcc\n" \
"v_cmp_gt_i32 vcc, 1, v186\n" \
"s_nop 1\n" \
"v_cndmask_b32 v177, v177, v190, vcc\n" \
"v_cmp_gt_i32 vcc, 2, v186\n" \
"s_nop 1\n" \
"v_cndmask_b32 v178, v178, v190, vcc\n" \
"v_cmp_gt_i32 vcc, 3, v186\n" \
"s_nop 1\n" \
"v_cndmask_b32 v179, v179, v190, vcc\n" \
"v_cmp_gt_i32 vcc, 16, v186\n" \
"s_nop 1\n" \
"v_cndmask_b32 v180, v180, v190, vcc\n" \
"v_cmp_gt_i32 vcc, 17, v186\n" \
"s_nop 1\n" \
"v_cndmask_b32 v181, v181, v190, vcc\n" \
"v_cmp_gt_i32 vcc, 18, v186\n" \
"s_nop 1\n" \
"v_cndmask_b32 v182, v182, v190, vcc\n" \
"v_cmp_gt_i32 vcc, 19, v186\n" \
"s_nop 1\n" \
"v_cndmask_b32 v183, v183, v190, vcc\n" \
"Lex3_%=:\n" \
"v_max3_f32 v187, v176, v177, v178\n" \
"v_max3_f32 v187, v187, v179, v180\n" \
"v_max3_f32 v187, v187, v181, v182\n" \
"v_max3_f32 v187, v187, v183, v184\n" \
"v_mov_b32 v188, v187\n" \
"s_nop 1\n" \
"v_permlane16_swap_b32 v188, v187\n" \
"v_max_f32 v187, v187, v188\n" \
"v_mov_b32 v188, v187\n" \
"s_nop 1\n" \
"v_permlane32_swap_b32 v188, v187\n" \
"v_max_f32 v187, v187, v188\n" \
"v_sub_f32 v189, v184, v187\n" \
"v_exp_f32 v189, v189\n" \
"v_mov_b32 v184, v187\n" \
"v_sub_f32 v176, v176, v187\n" \
"v_sub_f32 v177, v177, v187\n" \
"v_sub_f32 v178, v178, v187\n" \
"v_sub_f32 v179, v179, v187\n" \
"v_sub_f32 v180, v180, v187\n" \
"v_sub_f32 v181, v181, v187\n" \
"v_sub_f32 v182, v182, v187\n" \
"v_sub_f32 v183, v183, v187\n" \
"v_exp_f32 v176, v176\n" \
"v_exp_f32 v177, v177\n" \
"v_exp_f32 v178, v178\n" \
"v_exp_f32 v179, v179\n" \
"v_exp_f32 v180, v180\n" \
"v_exp_f32 v181, v181\n" \
"v_exp_f32 v182, v182\n" \
"v_exp_f32 v183, v183\n" \
"v_mul_f32 v185, v185, v189\n" \
"v_add_f32 v185, v185, v176\n" \
"v_add_f32 v185, v185, v177\n" \
"v_add_f32 v185, v185, v178\n" \
"v_add_f32 v185, v185, v179\n" \
"v_add_f32 v185, v185, v180\n" \
"v_add_f32 v185, v185, v181\n" \
"v_add_f32 v185, v185, v182\n" \
"v_add_f32 v185, v185, v183\n" \
"v_cvt_pk_bf16_f32 v112, v176, v177\n" \
"v_cvt_pk_bf16_f32 v113, v178, v179\n" \
"v_cvt_pk_bf16_f32 v114, v180, v181\n" \
"v_cvt_pk_bf16_f32 v115, v182, v183\n" \
"v_mul_f32 v128, v128, v189\n" \
"v_mul_f32 v129, v129, v189\n" \
"v_mul_f32 v130, v130, v189\n" \
"v_mul_f32 v131, v131, v189\n" \
"v_mul_f32 v132, v132, v189\n" \
"v_mul_f32 v133, v133, v189\n" \
"v_mul_f32 v134, v134, v189\n" \
"v_mul_f32 v135, v135, v189\n" \
"v_mul_f32 v136, v136, v189\n" \
"v_mul_f32 v137, v137, v189\n" \
"v_mul_f32 v138, v138, v189\n" \
"v_mul_f32 v139, v139, v189\n" \
"v_mul_f32 v140, v140, v189\n" \
"v_mul_f32 v141, v141, v189\n" \
"v_mul_f32 v142, v142, v189\n" \
"v_mul_f32 v143, v143, v189\n" \
"v_mul_f32 v144, v144, v189\n" \
"v_mul_f32 v145, v145, v189\n" \
"v_mul_f32 v146, v146, v189\n" \
"v_mul_f32 v147, v147, v189\n" \
"v_mul_f32 v148, v148, v189\n" \
"v_mul_f32 v149, v149, v189\n" \
"v_mul_f32 v150, v150, v189\n" \
"v_mul_f32 v151, v151, v189\n" \
"v_mul_f32 v152, v152, v189\n" \
"v_mul_f32 v153, v153, v189\n" \
"v_mul_f32 v154, v154, v189\n" \
"v_mul_f32 v155, v155, v189\n" \
"v_mul_f32 v156, v156, v189\n" \
"v_mul_f32 v157, v157, v189\n" \
"v_mul_f32 v158, v158, v189\n" \
"v_mul_f32 v159, v159, v189\n" \
"s_nop 1\n" \
"v_mfma_f32_16x16x32_bf16 v[128:131], a[32:35], v[112:115], v[128:131]\n" \
"v_mfma_f32_16x16x32_bf16 v[132:135], a[36:39], v[112:115], v[132:135]\n" \
"v_mfma_f32_16x16x32_bf16 v[136:139], a[40:43], v[112:115], v[136:139]\n" \
"v_mfma_f32_16x16x32_bf16 v[140:143], a[44:47], v[112:115], v[140:143]\n" \
"v_mfma_f32_16x16x32_bf16 v[144:147], a[48:51], v[112:115], v[144:147]\n" \
"v_mfma_f32_16x16x32_bf16 v[148:151], a[52:55], v[112:115], v[148:151]\n" \
"v_mfma_f32_16x16x32_bf16 v[152:155], a[56:59], v[112:115], v[152:155]\n" \
"v_mfma_f32_16x16x32_bf16 v[156:159], a[60:63], v[112:115], v[156:159]\n" \
"s_nop 7\n" \
"s_nop 1\n" \
"s_and_b32 s80, s73, 1\n" \
"s_mul_i32 s80, s80, 1024\n" \
"s_add_u32 s74, s74, s80\n" \
"s_add_u32 s74, s74, 8192\n" \
"s_add_u32 s73, s73, 1\n" \
"s_cmp_le_u32 s73, s75\n" \
"s_cbranch_scc1 Lex2_%=\n" \
"v_mov_b32 v187, v185\n" \
"s_nop 1\n" \
"v_permlane16_swap_b32 v187, v185\n" \
"v_add_f32 v185, v185, v187\n" \
"v_mov_b32 v187, v185\n" \
"s_nop 1\n" \
"v_permlane32_swap_b32 v187, v185\n" \
"v_add_f32 v185, v185, v187\n" \
"v_rcp_f32 v185, v185\n" \
"s_nop 0\n" \
"v_mul_f32 v128, v128, v185\n" \
"v_mul_f32 v129, v129, v185\n" \
"v_mul_f32 v130, v130, v185\n" \
"v_mul_f32 v131, v131, v185\n" \
"v_mul_f32 v132, v132, v185\n" \
"v_mul_f32 v133, v133, v185\n" \
"v_mul_f32 v134, v134, v185\n" \
"v_mul_f32 v135, v135, v185\n" \
"v_mul_f32 v136, v136, v185\n" \
"v_mul_f32 v137, v137, v185\n" \
"v_mul_f32 v138, v138, v185\n" \
"v_mul_f32 v139, v139, v185\n" \
"v_mul_f32 v140, v140, v185\n" \
"v_mul_f32 v141, v141, v185\n" \
"v_mul_f32 v142, v142, v185\n" \
"v_mul_f32 v143, v143, v185\n" \
"v_mul_f32 v144, v144, v185\n" \
"v_mul_f32 v145, v145, v185\n" \
"v_mul_f32 v146, v146, v185\n" \
"v_mul_f32 v147, v147, v185\n" \
"v_mul_f32 v148, v148, v185\n" \
"v_mul_f32 v149, v149, v185\n" \
"v_mul_f32 v150, v150, v185\n" \
"v_mul_f32 v151, v151, v185\n" \
"v_mul_f32 v152, v152, v185\n" \
"v_mul_f32 v153, v153, v185\n" \
"v_mul_f32 v154, v154, v185\n" \
"v_mul_f32 v155, v155, v185\n" \
"v_mul_f32 v156, v156, v185\n" \
"v_mul_f32 v157, v157, v185\n" \
"v_mul_f32 v158, v158, v185\n" \
"v_mul_f32 v159, v159, v185\n" \
"s_add_u32 s80, s76, 4096\n" \
"v_cvt_pk_bf16_f32 v112, v128, v129\n" \
"v_cvt_pk_bf16_f32 v113, v130, v131\n" \
"v_cvt_pk_bf16_f32 v114, v132, v133\n" \
"v_cvt_pk_bf16_f32 v115, v134, v135\n" \
"buffer_store_dwordx4 v[112:115], v191, s[56:59], s76 offen offset:0 sc1\n" \
"v_cvt_pk_bf16_f32 v116, v136, v137\n" \
"v_cvt_pk_bf16_f32 v117, v138, v139\n" \
"v_cvt_pk_bf16_f32 v118, v140, v141\n" \
"v_cvt_pk_bf16_f32 v119, v142, v143\n" \
"buffer_store_dwordx4 v[116:119], v191, s[56:59], s76 offen offset:2048 sc1\n" \
"v_cvt_pk_bf16_f32 v120, v144, v145\n" \
"v_cvt_pk_bf16_f32 v121, v146, v147\n" \
"v_cvt_pk_bf16_f32 v122, v148, v149\n" \
"v_cvt_pk_bf16_f32 v123, v150, v151\n" \
"buffer_store_dwordx4 v[120:123], v191, s[56:59], s80 offen offset:0 sc1\n" \
"v_cvt_pk_bf16_f32 v124, v152, v153\n" \
"v_cvt_pk_bf16_f32 v125, v154, v155\n" \
"v_cvt_pk_bf16_f32 v126, v156, v157\n" \
"v_cvt_pk_bf16_f32 v127, v158, v159\n" \
"buffer_store_dwordx4 v[124:127], v191, s[56:59], s80 offen offset:2048 sc1\n" \
"s_waitcnt vmcnt(0)\n" \
"s_setpc_b64 s[68:69]\n" \
"Lpl2_%=:\n" \
"s_mov_b32 s31, 0\n" \
"Lpk2_%=:\n" \
"s_sleep 2\n" \
"buffer_load_dword v220, v221, s[56:59], s81 offen sc1\n" \
"s_waitcnt vmcnt(0)\n" \
"v_cmp_ne_u32 vcc, s60, v220\n" \
"s_nop 1\n" \
"s_and_b32 vcc_hi, vcc_lo, s80\n" \
"s_cbranch_scc0 Lpr2_%=\n" \
"s_add_u32 s31, s31, 1\n" \
"s_cmp_lt_u32 s31, 0x40000\n" \
"s_cbranch_scc1 Lpk2_%=\n" \
"s_branch Lpr2_%=\n" \
"Lpl3_%=:\n" \
"s_mov_b32 s31, 0\n" \
"Lpk3_%=:\n" \
"s_sleep 2\n" \
"buffer_load_dwordx2 v[226:227], v228, s[56:59], s30 offen sc1\n" \
"s_waitcnt vmcnt(0)\n" \
"v_xor_b32 v226, v226, v227\n" \
"v_cmp_ne_u32 vcc, s60, v226\n" \
"s_nop 1\n" \
"s_and_b32 vcc_hi, vcc_lo, -1\n" \
"s_cbranch_scc0 Lpr3_%=\n" \
"s_add_u32 s31, s31, 1\n" \
"s_cmp_lt_u32 s31, 0x40000\n" \
"s_cbranch_scc1 Lpk3_%=\n" \
"s_branch Lpr3_%=\n" \
"Lpl4_%=:\n" \
"s_mov_b32 s31, 0\n" \
"Lpk4_%=:\n" \
"s_sleep 2\n" \
"buffer_load_dword v220, off, s[56:59], s79 sc1\n" \
"s_waitcnt vmcnt(0)\n" \
"v_cmp_ne_u32 vcc, s60, v220\n" \
"s_nop 1\n" \
"s_and_b32 vcc_hi, vcc_lo, 1\n" \
"s_cbranch_scc0 Lpr4_%=\n" \
"s_add_u32 s31, s31, 1\n" \
"s_cmp_lt_u32 s31, 0x40000\n" \
"s_cbranch_scc1 Lpk4_%=\n" \
"s_branch Lpr4_%=\n" \
"Lpl5_%=:\n" \
"s_mov_b32 s31, 0\n" \
"Lpk5_%=:\n" \
"s_sleep 2\n" \
"buffer_load_dword v220, v221, s[56:59], s82 offen sc1\n" \
"s_waitcnt vmcnt(0)\n" \
"v_cmp_ne_u32 vcc, s60, v220\n" \
"s_nop 1\n" \
"s_and_b32 vcc_hi, vcc_lo, 65535\n" \
"s_cbranch_scc0 Lpr5_%=\n" \
"s_add_u32 s31, s31, 1\n" \
"s_cmp_lt_u32 s31, 0x40000\n" \
"s_cbranch_scc1 Lpk5_%=\n" \
"s_branch Lpr5_%=\n" \
"Lcode_end_%=:\n" \
".p2align 12\n" \
""
#define MK_ASM_CLOBBERS "v1", "v2", "v3", "v4", "v5", "v6", "v7", "v8", "v9", "v10", "v11", "v12", "v13", "v14", "v15", "v16", "v17", "v18", "v19", "v20", "v21", "v22", "v23", "v24", "v25", "v26", "v27", "v28", "v29", "v30", "v31", "v32", "v33", "v34", "v35", "v36", "v37", "v38", "v39", "v40", "v41", "v42", "v43", "v44", "v45", "v46", "v47", "v48", "v49", "v50", "v51", "v52", "v53", "v54", "v55", "v56", "v57", "v58", "v59", "v60", "v61", "v62", "v63", "v64", "v65", "v66", "v67", "v68", "v69", "v70", "v71", "v72", "v73", "v74", "v75", "v76", "v77", "v78", "v79", "v80", "v81", "v82", "v83", "v84", "v85", "v86", "v87", "v88", "v89", "v90", "v91", "v92", "v93", "v94", "v95", "v96", "v97", "v98", "v99", "v100", "v101", "v102", "v103", "v104", "v105", "v106", "v107", "v108", "v109", "v110", "v111", "v112", "v113", "v114", "v115", "v116", "v117", "v118", "v119", "v120", "v121", "v122", "v123", "v124", "v125", "v126", "v127", "v128", "v129", "v130", "v131", "v132", "v133", "v134", "v135", "v136", "v137", "v138", "v139", "v140", "v141", "v142", "v143", "v144", "v145", "v146", "v147", "v148", "v149", "v150", "v151", "v152", "v153", "v154", "v155", "v156", "v157", "v158", "v159", "v160", "v161", "v162", "v163", "v164", "v165", "v166", "v167", "v168", "v169", "v170", "v171", "v172", "v173", "v174", "v175", "v176", "v177", "v178", "v179", "v180", "v181", "v182", "v183", "v184", "v185", "v186", "v187", "v188", "v189", "v190", "v191", "v192", "v193", "v194", "v195", "v196", "v197", "v198", "v199", "v200", "v201", "v202", "v203", "v204", "v205", "v206", "v207", "v208", "v209", "v210", "v211", "v212", "v213", "v214", "v215", "v216", "v217", "v218", "v219", "v220", "v221", "v222", "v223", "v224", "v225", "v226", "v227", "v228", "v229", "v230", "v231", "v232", "v233", "v234", "v235", "v236", "v237", "v238", "v239", "v240", "v241", "v242", "v243", "v244", "v245", "v246", "v247", "v248", "v249", "v250", "v251", "v252", "v253", "v254", "v255", "a0", "a1", "a2", "a3", "a4", "a5", "a6", "a7", "a8", "a9", "a10", "a11", "a12", "a13", "a14", "a15", "a16", "a17", "a18", "a19", "a20", "a21", "a22", "a23", "a24", "a25", "a26", "a27", "a28", "a29", "a30", "a31", "a32", "a33", "a34", "a35", "a36", "a37", "a38", "a39", "a40", "a41", "a42", "a43", "a44", "a45", "a46", "a47", "a48", "a49", "a50", "a51", "a52", "a53", "a54", "a55", "a56", "a57", "a58", "a59", "a60", "a61", "a62", "a63", "a64", "a65", "a66", "a67", "a68", "a69", "a70", "a71", "a72", "a73", "a74", "a75", "a76", "a77", "a78", "a79", "a80", "a81", "a82", "a83", "a84", "a85", "a86", "a87", "a88", "a89", "a90", "a91", "a92", "a93", "a94", "a95", "a96", "a97", "a98", "a99", "a100", "a101", "a102", "a103", "a104", "a105", "a106", "a107", "a108", "a109", "a110", "a111", "a112", "a113", "a114", "a115", "a116", "a117", "a118", "a119", "a120", "a121", "a122", "a123", "a124", "a125", "a126", "a127", "a128", "a129", "a130", "a131", "a132", "a133", "a134", "a135", "a136", "a137", "a138", "a139", "a140", "a141", "a142", "a143", "a144", "a145", "a146", "a147", "a148", "a149", "a150", "a151", "a152", "a153", "a154", "a155", "a156", "a157", "a158", "a159", "a160", "a161", "a162", "a163", "a164", "a165", "a166", "a167", "a168", "a169", "a170", "a171", "a172", "a173", "a174", "a175", "a176", "a177", "a178", "a179", "a180", "a181", "a182", "a183", "a184", "a185", "a186", "a187", "a188", "a189", "a190", "a191", "a192", "a193", "a194", "a195", "a196", "a197", "a198", "a199", "a200", "a201", "a202", "a203", "a204", "a205", "a206", "a207", "a208", "a209", "a210", "a211", "a212", "a213", "a214", "a215", "a216", "a217", "a218", "a219", "a220", "a221", "a222", "a223", "a224", "a225", "a226", "a227", "a228", "a229", "a230", "a231", "a232", "a233", "a234", "a235", "a236", "a237", "a238", "a239", "a240", "a241", "a242", "a243", "a244", "a245", "a246", "a247", "a248", "a249", "a250", "a251", "a252", "a253", "a254", "a255", "s8", "s9", "s10", "s11", "s12", "s13", "s14", "s15", "s16", "s17", "s18", "s19", "s20", "s21", "s22", "s23", "s24", "s25", "s26", "s27", "s28", "s29", "s30", "s31", "s36", "s37", "s38", "s39", "s40", "s41", "s42", "s43", "s44", "s45", "s46", "s47", "s48", "s49", "s50", "s51", "s52", "s53", "s54", "s55", "s56", "s57", "s58", "s59", "s60", "s61", "s62", "s63", "s64", "s65", "s66", "s67", "s68", "s69", "s70", "s71", "s72", "s73", "s74", "s75", "s76", "s77", "s78", "s79", "s80", "s81", "s82", "s83", "s84", "s85", "s86", "s87", "s88", "s89", "s90", "s91", "s92", "s93", "s94", "s95", "vcc", "scc", "memory"
namespace mk {
constexpr int LDS_BYTES = 163840;
struct Args { const void* in[9]; void* out; void* ws; };
__global__ __launch_bounds__(256, 1) void mega_k(const Args a) {
    extern __shared__ __attribute__((aligned(16))) unsigned char shm[];
    const unsigned ldsb = (unsigned)(size_t)(LAS unsigned char*)shm;
    asm volatile(MK_ASM_BODY
                 :
                 : "s"(__builtin_amdgcn_kernarg_segment_ptr()), "s"(blockIdx.x), "v"(threadIdx.x), "s"(ldsb)
                 : MK_ASM_CLOBBERS);
}
}
extern "C" void kernel_launch(void* const* d_in, const int* in_sizes, int n_in, void* d_out, int out_size, void* d_ws, size_t ws_size, hipStream_t stream) {
    (void)hipFuncSetAttribute((const void*)mk::mega_k, hipFuncAttributeMaxDynamicSharedMemorySize, mk::LDS_BYTES);
    mk::Args a;
    for (int i = 0; i < 9; ++i) a.in[i] = d_in[i];
    a.out = d_out; a.ws = (char*)d_ws + ((ws_size - WS_END) & ~(size_t)0x1fffff);
    mk::mega_k<<<256, 256, mk::LDS_BYTES, stream>>>(a);
}
```

```cpp
#include <hip/hip_runtime.h>
#include <stdint.h>
#define LAS __attribute__((address_space(3)))
constexpr size_t LAY_TS = 17408, LAY_HS = 557824, LAY_CRS = 266240, LAY_ORS = 69632, WS_K = 0, WS_V = 17850368, WS_CTX = 35700736, WS_OB = 52740096, WS_FL = 65585152, LAY_EXD = 29950080, WS_END = 87147648;
#define MK_ASM_BODY \
"s_load_dwordx16 s[8:23], %0, 0x0\n" \
"s_load_dwordx4 s[24:27], %0, 0x40\n" \
"s_load_dwordx2 s[28:29], %0, 0x50\n" \
"v_readfirstlane_b32 s61, %2\n" \
"s_lshr_b32 s61, s61, 6\n" \
"v_mbcnt_lo_u32_b32 v52, -1, 0\n" \
"v_mbcnt_hi_u32_b32 v52, -1, v52\n" \
"v_lshlrev_b32 v248, 4, v52\n" \
"s_getpc_b64 s[64:65]\n" \
"Lpc_%=:\n" \
"s_lshr_b32 s70, %1, 3\n" \
"s_lshl_b32 s70, s70, 2\n" \
"s_add_u32 s70, s70, s61\n" \
"s_and_b32 s71, s70, 127\n" \
"s_lshl_b32 s71, s71, 10\n" \
"v_lshl_add_u32 v53, v248, 0, s71\n" \
"s_mov_b32 s71, Lcode_end_%=-Lpc_%=-4\n" \
"v_min_u32 v53, s71, v53\n" \
"global_load_dword v253, v53, s[64:65]\n" \
"s_and_b32 s62, %1, 7\n" \
"s_lshl_b32 s62, s62, 2\n" \
"s_lshr_b32 s70, %1, 6\n" \
"s_add_u32 s62, s62, s70\n" \
"s_bfe_u32 s63, %1, 0x30003\n" \
"s_lshl_b32 s87, s61, 13\n" \
"s_lshl_b32 s95, s61, 12\n" \
"v_bfe_u32 v53, v52, 4, 1\n" \
"v_bfe_u32 v54, v52, 2, 2\n" \
"v_lshl_add_u32 v54, v53, 3, v54\n" \
"s_lshl_b32 s71, s61, 4\n" \
"v_add_u32 v54, s71, v54\n" \
"v_lshlrev_b32 v54, 13, v54\n" \
"v_lshlrev_b32 v53, 1, v53\n" \
"v_and_b32 v55, 3, v52\n" \
"v_xor_b32 v53, v53, v55\n" \
"v_lshl_add_u32 v54, v53, 4, v54\n" \
"v_lshrrev_b32 v53, 5, v52\n" \
"v_lshl_add_u32 v54, v53, 6, v54\n" \
"v_add_u32 v40, 4096, v54\n" \
"v_add_u32 v41, 3072, v54\n" \
"v_add_u32 v42, 2176, v54\n" \
"v_add_u32 v43, 1152, v54\n" \
"v_add_u32 v44, 32768, v54\n" \
"v_add_u32 v45, 31744, v54\n" \
"v_add_u32 v46, 30848, v54\n" \
"v_add_u32 v47, 29824, v54\n" \
"v_lshrrev_b32 v55, 3, v52\n" \
"v_and_b32 v56, 7, v52\n" \
"v_xor_b32 v56, v56, v55\n" \
"v_lshlrev_b32 v56, 4, v56\n" \
"v_lshl_add_u32 v57, v55, 10, v56\n" \
"v_add_u32 v32, 528384, v57\n" \
"v_add_u32 v33, 535552, v57\n" \
"v_add_u32 v34, 542720, v57\n" \
"v_add_u32 v35, 549888, v57\n" \
"v_add_u32 v36, 0, v57\n" \
"v_add_u32 v37, 7168, v57\n" \
"v_add_u32 v38, 14336, v57\n" \
"v_add_u32 v39, 21504, v57\n" \
"s_lshl_b32 s88, s62, 8\n" \
"s_lshl_b32 s89, s63, 18\n" \
"s_lshl_b32 s70, s63, 16\n" \
"s_and_b32 s71, s87, 0x4000\n" \
"s_lshl_b32 s71, s71, 1\n" \
"s_add_u32 s70, s70, s71\n" \
"s_andn2_b32 s71, 1, s61\n" \
"s_lshl_b32 s71, s71, 20\n" \
"s_add_u32 s70, s70, s71\n" \
"s_lshl_b32 s66, %1, 2\n" \
"s_add_u32 s66, s66, s61\n" \
"s_lshl_b32 s66, s66, 10\n" \
"s_lshr_b32 s67, %1, 3\n" \
"s_lshl_b32 s67, s67, 2\n" \
"s_add_u32 s67, s67, s61\n" \
"s_lshl_b32 s67, s67, 13\n" \
"v_lshrrev_b32 v63, 6, v248\n" \
"v_lshlrev_b32 v63, 8, v63\n" \
"v_and_or_b32 v63, v248, 48, v63\n" \
"s_lshl_b32 s64, s67, 1\n" \
"v_lshl_add_u32 v63, v63, 2, s64\n" \
"s_and_b32 s64, %1, 7\n" \
"s_lshl_b32 s64, s64, 10\n" \
"s_add_u32 s67, s67, s64\n" \
"s_add_u32 s73, s63, s62\n" \
"s_and_b32 s73, s73, 1\n" \
"s_mov_b32 s74, 128\n" \
"s_cmp_lg_u32 s73, 0\n" \
"s_cselect_b64 exec, -1, 0\n" \
"v_swap_b32 v32, v40\n" \
"v_swap_b32 v33, v41\n" \
"v_swap_b32 v34, v42\n" \
"v_swap_b32 v35, v43\n" \
"v_swap_b32 v36, v44\n" \
"v_swap_b32 v37, v45\n" \
"v_swap_b32 v38, v46\n" \
"v_swap_b32 v39, v47\n" \
"s_mov_b64 exec, -1\n" \
"s_cselect_b32 s76, s70, s88\n" \
"s_cselect_b32 s77, s88, s70\n" \
"s_cselect_b32 s75, 524288, s74\n" \
"s_cselect_b32 s74, s74, 524288\n" \
"s_waitcnt lgkmcnt(0)\n" \
"s_cselect_b64 s[52:53], s[10:11], s[14:15]\n" \
"s_cselect_b64 s[54:55], s[14:15], s[10:11]\n" \
"s_cselect_b64 s[50:51], s[10:11], s[16:17]\n" \
"s_cselect_b64 s[40:41], s[16:17], s[10:11]\n" \
"s_add_u32 s52, s52, s76\n" \
"s_addc_u32 s53, s53, 0\n" \
"s_add_u32 s50, s50, s76\n" \
"s_addc_u32 s51, s51, 0\n" \
"s_add_u32 m0, s87, 36864\n" \
"s_nop 0\n" \
"global_load_lds_dwordx4 v40, s[52:53] offset:-4096\n" \
"global_load_lds_dwordx4 v41, s[50:51] offset:-3072\n" \
"global_load_lds_dwordx4 v42, s[52:53] offset:-2048\n" \
"global_load_lds_dwordx4 v43, s[50:51] offset:-1024\n" \
"global_load_lds_dwordx4 v44, s[52:53] offset:0\n" \
"global_load_lds_dwordx4 v45, s[50:51] offset:1024\n" \
"global_load_lds_dwordx4 v46, s[52:53] offset:2048\n" \
"global_load_lds_dwordx4 v47, s[50:51] offset:3072\n" \
"s_add_u32 s52, s52, s74\n" \
"s_addc_u32 s53, s53, 0\n" \
"s_add_u32 s50, s50, s74\n" \
"s_addc_u32 s51, s51, 0\n" \
"s_add_u32 s54, s54, s77\n" \
"s_addc_u32 s55, s55, 0\n" \
"s_add_u32 s40, s40, s77\n" \
"s_addc_u32 s41, s41, 0\n" \
"s_add_u32 m0, s87, 4096\n" \
"s_nop 0\n" \
"global_load_lds_dwordx4 v32, s[54:55] offset:-4096\n" \
"global_load_lds_dwordx4 v33, s[40:41] offset:-3072\n" \
"global_load_lds_dwordx4 v34, s[54:55] offset:-2048\n" \
"global_load_lds_dwordx4 v35, s[40:41] offset:-1024\n" \
"global_load_lds_dwordx4 v36, s[54:55] offset:0\n" \
"global_load_lds_dwordx4 v37, s[40:41] offset:1024\n" \
"global_load_lds_dwordx4 v38, s[54:55] offset:2048\n" \
"global_load_lds_dwordx4 v39, s[40:41] offset:3072\n" \
"s_add_u32 s54, s54, s75\n" \
"s_addc_u32 s55, s55, 0\n" \
"s_add_u32 s40, s40, s75\n" \
"s_addc_u32 s41, s41, 0\n" \
"s_mov_b64 s[56:57], s[28:29]\n" \
"s_and_b32 s57, s57, 0xffff\n" \
"s_mov_b32 s58, 87147648\n" \
"s_mov_b32 s59, 0x20000\n" \
"v_add_u32 v228, 4096, v54\n" \
"v_add_u32 v229, 3200, v54\n" \
"v_lshl_add_u32 v57, v55, 11, v56\n" \
"v_add_u32 v224, 0, v57\n" \
"v_add_u32 v225, 15360, v57\n" \
"v_add_u32 v226, 129024, v57\n" \
"v_add_u32 v227, 144384, v57\n" \
"s_lshl_b32 s70, s61, 15\n" \
"s_add_u32 s70, s70, 0\n" \
"s_add_u32 s71, s70, s89\n" \
"s_add_u32 s71, s71, 4096\n" \
"s_add_u32 s48, s8, s71\n" \
"s_addc_u32 s49, s9, 0\n" \
"s_sub_u32 s71, s70, s89\n" \
"s_add_u32 s71, s71, 3932160\n" \
"s_add_u32 s46, s8, s71\n" \
"s_addc_u32 s47, s9, 0\n" \
"s_add_u32 s70, s88, 0\n" \
"s_add_u32 s44, s12, s70\n" \
"s_addc_u32 s45, s13, 0\n" \
"s_add_u32 s42, s44, 30720\n" \
"s_addc_u32 s43, s45, 0\n" \
"s_lshr_b32 s70, s28, 31\n" \
"s_lshl_b32 s70, s70, 30\n" \
"s_sub_u32 s28, s28, s70\n" \
"v_add_u32 v250, s70, v248\n" \
"v_lshlrev_b32 v245, 2, v52\n" \
"v_mov_b32 v254, 0x30000\n" \
"v_lshrrev_b32 v55, 4, v52\n" \
"v_and_b32 v249, 0xf0, v248\n" \
"v_lshl_add_u32 v249, v55, 9, v249\n" \
"v_and_b32 v53, 15, v52\n" \
"s_lshl_b32 s70, s61, 4\n" \
"v_lshlrev_b32 v54, 2, v55\n" \
"v_sub_u32 v251, v53, v54\n" \
"v_add_u32 v251, s70, v251\n" \
"v_and_b32 v54, 7, v52\n" \
"v_xor_b32 v54, v54, v55\n" \
"v_lshlrev_b32 v53, 7, v53\n" \
"s_add_u32 s71, s87, 131072\n" \
"s_lshr_b32 s70, s61, 1\n" \
"s_lshl_b32 s70, s70, 14\n" \
"s_lshl_b32 s76, s73, 15\n" \
"s_add_u32 s70, s70, s76\n" \
"v_xor_b32 v56, 0, v54\n" \
"v_lshl_add_u32 v56, v56, 4, v53\n" \
"v_add_u32 v230, s71, v56\n" \
"v_add_u32 v48, s70, v56\n" \
"v_xor_b32 v56, 4, v54\n" \
"v_lshl_add_u32 v56, v56, 4, v53\n" \
"v_add_u32 v231, s71, v56\n" \
"v_add_u32 v49, s70, v56\n" \
"v_and_b32 v53, 3, v52\n" \
"v_lshlrev_b32 v53, 3, v53\n" \
"v_bfe_u32 v54, v52, 2, 2\n" \
"v_lshl_add_u32 v53, v54, 6, v53\n" \
"v_bfe_u32 v54, v52, 4, 1\n" \
"v_lshl_add_u32 v53, v54, 8, v53\n" \
"v_lshl_add_u32 v53, v54, 5, v53\n" \
"v_lshrrev_b32 v54, 5, v52\n" \
"s_and_b32 s70, s61, 1\n" \
"s_lshl_b32 s70, s70, 11\n" \
"s_xor_b32 s76, s76, 0x8000\n" \
"s_add_u32 s70, s70, s76\n" \
"v_lshl_add_u32 v56, v54, 13, v53\n" \
"v_add_u32 v50, s70, v56\n" \
"v_xor_b32 v51, 32, v50\n" \
"v_lshl_add_u32 v232, v54, 12, v53\n" \
"v_xor_b32 v233, 32, v232\n" \
"s_mov_b64 exec, 0\n" \
"s_cmp_eq_u32 s61, 0\n" \
"s_cbranch_scc1 Lst0x0_%=\n" \
"s_cmp_eq_u32 s61, 1\n" \
"s_cbranch_scc1 Lst0x1_%=\n" \
"s_cmp_eq_u32 s61, 2\n" \
"s_cbranch_scc1 Lst0x2_%=\n" \
"s_branch Lst0x3_%=\n" \
"Lcb0_%=:\n" \
"s_mov_b64 exec, -1\n" \
"s_add_u32 m0, s87, 135168\n" \
"s_nop 0\n" \
"global_load_lds_dwordx4 v40, s[52:53] offset:-4096\n" \
"global_load_lds_dwordx4 v41, s[50:51] offset:-3072\n" \
"global_load_lds_dwordx4 v42, s[52:53] offset:-2048\n" \
"global_load_lds_dwordx4 v43, s[50:51] offset:-1024\n" \
"global_load_lds_dwordx4 v44, s[52:53] offset:0\n" \
"global_load_lds_dwordx4 v45, s[50:51] offset:1024\n" \
"global_load_lds_dwordx4 v46, s[52:53] offset:2048\n" \
"global_load_lds_dwordx4 v47, s[50:51] offset:3072\n" \
"s_add_u32 s52, s52, s74\n" \
"s_addc_u32 s53, s53, 0\n" \
"s_add_u32 s50, s50, s74\n" \
"s_addc_u32 s51, s51, 0\n" \
"s_waitcnt vmcnt(8)\n" \
"s_barrier\n" \
"ds_read_b128 v[64:67], v48 offset:0\n" \
"ds_read_b64_tr_b16 v[128:129], v50 offset:0\n" \
"ds_read_b64_tr_b16 v[130:131], v50 offset:4096\n" \
"ds_read_b128 v[68:71], v49 offset:0\n" \
"ds_read_b64_tr_b16 v[132:133], v50 offset:16384\n" \
"ds_read_b64_tr_b16 v[134:135], v50 offset:20480\n" \
"ds_read_b128 v[72:75], v48 offset:2048\n" \
"ds_read_b64_tr_b16 v[136:137], v51 offset:0\n" \
"ds_read_b64_tr_b16 v[138:139], v51 offset:4096\n" \
"ds_read_b128 v[76:79], v49 offset:2048\n" \
"ds_read_b64_tr_b16 v[140:141], v51 offset:16384\n" \
"ds_read_b64_tr_b16 v[142:143], v51 offset:20480\n" \
"ds_read_b64_tr_b16 v[144:145], v50 offset:512\n" \
"ds_read_b64_tr_b16 v[146:147], v50 offset:4608\n" \
"ds_read_b64_tr_b16 v[148:149], v50 offset:16896\n" \
"ds_read_b64_tr_b16 v[150:151], v50 offset:20992\n" \
"ds_read_b64_tr_b16 v[152:153], v51 offset:512\n" \
"ds_read_b64_tr_b16 v[154:155], v51 offset:4608\n" \
"ds_read_b64_tr_b16 v[156:157], v51 offset:16896\n" \
"ds_read_b64_tr_b16 v[158:159], v51 offset:20992\n" \
"s_waitcnt lgkmcnt(14)\n" \
"v_mfma_f32_16x16x32_bf16 a[0:3], v[128:131], v[64:67], 0\n" \
"ds_read_b128 v[120:123], v48 offset:14336\n" \
"s_add_u32 m0, s87, 69632\n" \
"v_mfma_f32_16x16x32_bf16 a[0:3], v[132:135], v[68:71], a[0:3]\n" \
"s_waitcnt lgkmcnt(9)\n" \
"ds_read_b128 v[80:83], v48 offset:4096\n" \
"global_load_lds_dwordx4 v32, s[54:55] offset:-4096\n" \
"v_mfma_f32_16x16x32_bf16 a[4:7], v[136:139], v[64:67], 0\n" \
"ds_read_b128 v[124:127], v49 offset:14336\n" \
"v_mfma_f32_16x16x32_bf16 a[4:7], v[140:143], v[68:71], a[4:7]\n" \
"s_waitcnt lgkmcnt(7)\n" \
"ds_read_b128 v[84:87], v49 offset:4096\n" \
"v_mfma_f32_16x16x32_bf16 a[8:11], v[144:147], v[64:67], 0\n" \
"ds_read_b64_tr_b16 v[160:161], v50 offset:1024\n" \
"global_load_lds_dwordx4 v33, s[40:41] offset:-3072\n" \
"v_mfma_f32_16x16x32_bf16 a[8:11], v[148:151], v[68:71], a[8:11]\n" \
"s_waitcnt lgkmcnt(4)\n" \
"ds_read_b128 v[88:91], v48 offset:6144\n" \
"v_mfma_f32_16x16x32_bf16 a[12:15], v[152:155], v[64:67], 0\n" \
"ds_read_b64_tr_b16 v[162:163], v50 offset:5120\n" \
"v_mfma_f32_16x16x32_bf16 a[12:15], v[156:159], v[68:71], a[12:15]\n" \
"ds_read_b128 v[92:95], v49 offset:6144\n" \
"global_load_lds_dwordx4 v34, s[54:55] offset:-2048\n" \
"v_mfma_f32_16x16x32_bf16 a[44:47], v[152:155], v[72:75], 0\n" \
"ds_read_b64_tr_b16 v[164:165], v50 offset:17408\n" \
"v_mfma_f32_16x16x32_bf16 a[44:47], v[156:159], v[76:79], a[44:47]\n" \
"ds_read_b128 v[96:99], v48 offset:8192\n" \
"v_mfma_f32_16x16x32_bf16 a[40:43], v[144:147], v[72:75], 0\n" \
"ds_read_b64_tr_b16 v[166:167], v50 offset:21504\n" \
"global_load_lds_dwordx4 v35, s[40:41] offset:-1024\n" \
"v_mfma_f32_16x16x32_bf16 a[40:43], v[148:151], v[76:79], a[40:43]\n" \
"ds_read_b128 v[100:103], v49 offset:8192\n" \
"v_mfma_f32_16x16x32_bf16 a[36:39], v[136:139], v[72:75], 0\n" \
"ds_read_b64_tr_b16 v[168:169], v51 offset:1024\n" \
"v_mfma_f32_16x16x32_bf16 a[36:39], v[140:143], v[76:79], a[36:39]\n" \
"ds_read_b128 v[104:107], v48 offset:10240\n" \
"global_load_lds_dwordx4 v36, s[54:55] offset:0\n" \
"v_mfma_f32_16x16x32_bf16 a[32:35], v[128:131], v[72:75], 0\n" \
"ds_read_b64_tr_b16 v[170:171], v51 offset:5120\n" \
"v_mfma_f32_16x16x32_bf16 a[32:35], v[132:135], v[76:79], a[32:35]\n" \
"s_waitcnt lgkmcnt(4)\n" \
"ds_read_b128 v[108:111], v49 offset:10240\n" \
"v_mfma_f32_16x16x32_bf16 a[64:67], v[128:131], v[80:83], 0\n" \
"ds_read_b64_tr_b16 v[172:173], v51 offset:17408\n" \
"global_load_lds_dwordx4 v37, s[40:41] offset:1024\n" \
"v_mfma_f32_16x16x32_bf16 a[64:67], v[132:135], v[84:87], a[64:67]\n" \
"ds_read_b128 v[112:115], v48 offset:12288\n" \
"v_mfma_f32_16x16x32_bf16 a[68:71], v[136:139], v[80:83], 0\n" \
"ds_read_b64_tr_b16 v[174:175], v51 offset:21504\n" \
"v_mfma_f32_16x16x32_bf16 a[68:71], v[140:143], v[84:87], a[68:71]\n" \
"ds_read_b128 v[116:119], v49 offset:12288\n" \
"global_load_lds_dwordx4 v38, s[54:55] offset:2048\n" \
"v_mfma_f32_16x16x32_bf16 a[72:75], v[144:147], v[80:83], 0\n" \
"ds_read_b64_tr_b16 v[176:177], v50 offset:1536\n" \
"v_mfma_f32_16x16x32_bf16 a[72:75], v[148:151], v[84:87], a[72:75]\n" \
"global_load_lds_dwordx4 v39, s[40:41] offset:3072\n" \
"v_mfma_f32_16x16x32_bf16 a[76:79], v[152:155], v[80:83], 0\n" \
"s_add_u32 s54, s54, s75\n" \
"s_addc_u32 s55, s55, 0\n" \
"ds_read_b64_tr_b16 v[178:179], v50 offset:5632\n" \
"v_mfma_f32_16x16x32_bf16 a[76:79], v[156:159], v[84:87], a[76:79]\n" \
"s_add_u32 s40, s40, s75\n" \
"s_addc_u32 s41, s41, 0\n" \
"s_mov_b32 s90, 65593344\n" \
"v_mfma_f32_16x16x32_bf16 a[108:111], v[152:155], v[88:91], 0\n" \
"ds_read_b64_tr_b16 v[180:181], v50 offset:17920\n" \
"v_mfma_f32_16x16x32_bf16 a[108:111], v[156:159], v[92:95], a[108:111]\n" \
"buffer_load_dword v244, off, s[56:59], s90 sc1\n" \
"v_mfma_f32_16x16x32_bf16 a[104:107], v[144:147], v[88:91], 0\n" \
"ds_read_b64_tr_b16 v[182:183], v50 offset:22016\n" \
"v_mfma_f32_16x16x32_bf16 a[104:107], v[148:151], v[92:95], a[104:107]\n" \
"s_add_u32 m0, s87, 102400\n" \
"v_mfma_f32_16x16x32_bf16 a[100:103], v[136:139], v[88:91], 0\n" \
"ds_read_b64_tr_b16 v[184:185], v51 offset:1536\n" \
"global_load_lds_dwordx4 v40, s[52:53] offset:-4096\n" \
"v_mfma_f32_16x16x32_bf16 a[100:103], v[140:143], v[92:95], a[100:103]\n" \
"v_mfma_f32_16x16x32_bf16 a[96:99], v[128:131], v[88:91], 0\n" \
"ds_read_b64_tr_b16 v[186:187], v51 offset:5632\n" \
"v_mfma_f32_16x16x32_bf16 a[96:99], v[132:135], v[92:95], a[96:99]\n" \
"s_waitcnt lgkmcnt(2)\n" \
"global_load_lds_dwordx4 v41, s[50:51] offset:-3072\n" \
"v_mfma_f32_16x16x32_bf16 a[128:131], v[128:131], v[96:99], 0\n" \
"ds_read_b64_tr_b16 v[188:189], v51 offset:17920\n" \
"v_mfma_f32_16x16x32_bf16 a[128:131], v[132:135], v[100:103], a[128:131]\n" \
"v_mfma_f32_16x16x32_bf16 a[132:135], v[136:139], v[96:99], 0\n" \
"ds_read_b64_tr_b16 v[190:191], v51 offset:22016\n" \
"global_load_lds_dwordx4 v42, s[52:53] offset:-2048\n" \
"v_mfma_f32_16x16x32_bf16 a[132:135], v[140:143], v[100:103], a[132:135]\n" \
"v_mfma_f32_16x16x32_bf16 a[136:139], v[144:147], v[96:99], 0\n" \
"v_mfma_f32_16x16x32_bf16 a[136:139], v[148:151], v[100:103], a[136:139]\n" \
"global_load_lds_dwordx4 v43, s[50:51] offset:-1024\n" \
"v_mfma_f32_16x16x32_bf16 a[140:143], v[152:155], v[96:99], 0\n" \
"v_mfma_f32_16x16x32_bf16 a[140:143], v[156:159], v[100:103], a[140:143]\n" \
"global_load_lds_dwordx4 v44, s[52:53] offset:0\n" \
"v_mfma_f32_16x16x32_bf16 a[172:175], v[152:155], v[104:107], 0\n" \
"s_mul_i32 s76, s73, 0x8000\n" \
"s_add_u32 s77, s76, 0x10000\n" \
"s_sub_u32 s78, 0x18000, s76\n" \
"v_mfma_f32_16x16x32_bf16 a[172:175], v[156:159], v[108:111], a[172:175]\n" \
"v_add_u32 v48, s77, v48\n" \
"v_add_u32 v49, s77, v49\n" \
"v_mfma_f32_16x16x32_bf16 a[168:171], v[144:147], v[104:107], 0\n" \
"v_add_u32 v50, s78, v50\n" \
"v_add_u32 v51, s78, v51\n" \
"v_mfma_f32_16x16x32_bf16 a[168:171], v[148:151], v[108:111], a[168:171]\n" \
"global_load_lds_dwordx4 v45, s[50:51] offset:1024\n" \
"v_mfma_f32_16x16x32_bf16 a[164:167], v[136:139], v[104:107], 0\n" \
"v_mfma_f32_16x16x32_bf16 a[164:167], v[140:143], v[108:111], a[164:167]\n" \
"v_mfma_f32_16x16x32_bf16 a[160:163], v[128:131], v[104:107], 0\n" \
"global_load_lds_dwordx4 v46, s[52:53] offset:2048\n" \
"v_mfma_f32_16x16x32_bf16 a[160:163], v[132:135], v[108:111], a[160:163]\n" \
"v_mfma_f32_16x16x32_bf16 a[192:195], v[128:131], v[112:115], 0\n" \
"v_mfma_f32_16x16x32_bf16 a[192:195], v[132:135], v[116:119], a[192:195]\n" \
"global_load_lds_dwordx4 v47, s[50:51] offset:3072\n" \
"v_mfma_f32_16x16x32_bf16 a[196:199], v[136:139], v[112:115], 0\n" \
"s_add_u32 s52, s52, s74\n" \
"s_addc_u32 s53, s53, 0\n" \
"v_mfma_f32_16x16x32_bf16 a[196:199], v[140:143], v[116:119], a[196:199]\n" \
"s_add_u32 s50, s50, s74\n" \
"s_addc_u32 s51, s51, 0\n" \
"v_mfma_f32_16x16x32_bf16 a[200:203], v[144:147], v[112:115], 0\n" \
"v_mfma_f32_16x16x32_bf16 a[200:203], v[148:151], v[116:119], a[200:203]\n" \
"v_mfma_f32_16x16x32_bf16 a[204:207], v[152:155], v[112:115], 0\n" \
"v_mfma_f32_16x16x32_bf16 a[204:207], v[156:159], v[116:119], a[204:207]\n" \
"v_mfma_f32_16x16x32_bf16 a[236:239], v[152:155], v[120:123], 0\n" \
"v_mfma_f32_16x16x32_bf16 a[236:239], v[156:159], v[124:127], a[236:239]\n" \
"v_mfma_f32_16x16x32_bf16 a[232:235], v[144:147], v[120:123], 0\n" \
"v_mfma_f32_16x16x32_bf16 a[232:235], v[148:151], v[124:127], a[232:235]\n" \
"v_mfma_f32_16x16x32_bf16 a[228:231], v[136:139], v[120:123], 0\n" \
"v_mfma_f32_16x16x32_bf16 a[228:231], v[140:143], v[124:127], a[228:231]\n" \
"v_mfma_f32_16x16x32_bf16 a[224:227], v[128:131], v[120:123], 0\n" \
"s_waitcnt vmcnt(9) lgkmcnt(0)\n" \
"v_mfma_f32_16x16x32_bf16 a[224:227], v[132:135], v[124:127], a[224:227]\n" \
"s_barrier\n" \
"v_mfma_f32_16x16x32_bf16 a[16:19], v[64:67], v[160:163], 0\n" \
"s_add_u32 m0, s87, 36864\n" \
"ds_read_b64_tr_b16 v[128:129], v50 offset:0\n" \
"v_mfma_f32_16x16x32_bf16 a[16:19], v[68:71], v[164:167], a[16:19]\n" \
"global_load_lds_dwordx4 v32, s[54:55] offset:-4096\n" \
"v_mfma_f32_16x16x32_bf16 a[20:23], v[64:67], v[168:171], 0\n" \
"ds_read_b64_tr_b16 v[130:131], v50 offset:4096\n" \
"v_mfma_f32_16x16x32_bf16 a[20:23], v[68:71], v[172:175], a[20:23]\n" \
"v_mfma_f32_16x16x32_bf16 a[24:27], v[64:67], v[176:179], 0\n" \
"ds_read_b64_tr_b16 v[132:133], v50 offset:16384\n" \
"v_mfma_f32_16x16x32_bf16 a[24:27], v[68:71], v[180:183], a[24:27]\n" \
"global_load_lds_dwordx4 v33, s[40:41] offset:-3072\n" \
"v_mfma_f32_16x16x32_bf16 a[28:31], v[64:67], v[184:187], 0\n" \
"ds_read_b64_tr_b16 v[134:135], v50 offset:20480\n" \
"v_mfma_f32_16x16x32_bf16 a[28:31], v[68:71], v[188:191], a[28:31]\n" \
"v_mfma_f32_16x16x32_bf16 a[60:63], v[72:75], v[184:187], 0\n" \
"ds_read_b128 v[64:67], v48 offset:0\n" \
"v_mfma_f32_16x16x32_bf16 a[60:63], v[76:79], v[188:191], a[60:63]\n" \
"global_load_lds_dwordx4 v34, s[54:55] offset:-2048\n" \
"v_mfma_f32_16x16x32_bf16 a[56:59], v[72:75], v[176:179], 0\n" \
"ds_read_b64_tr_b16 v[136:137], v51 offset:0\n" \
"v_mfma_f32_16x16x32_bf16 a[56:59], v[76:79], v[180:183], a[56:59]\n" \
"v_mfma_f32_16x16x32_bf16 a[52:55], v[72:75], v[168:171], 0\n" \
"ds_read_b128 v[68:71], v49 offset:0\n" \
"v_mfma_f32_16x16x32_bf16 a[52:55], v[76:79], v[172:175], a[52:55]\n" \
"global_load_lds_dwordx4 v35, s[40:41] offset:-1024\n" \
"v_mfma_f32_16x16x32_bf16 a[48:51], v[72:75], v[160:163], 0\n" \
"ds_read_b64_tr_b16 v[138:139], v51 offset:4096\n" \
"v_mfma_f32_16x16x32_bf16 a[48:51], v[76:79], v[164:167], a[48:51]\n" \
"v_mfma_f32_16x16x32_bf16 a[80:83], v[80:83], v[160:163], 0\n" \
"ds_read_b128 v[72:75], v48 offset:2048\n" \
"v_mfma_f32_16x16x32_bf16 a[80:83], v[84:87], v[164:167], a[80:83]\n" \
"global_load_lds_dwordx4 v36, s[54:55] offset:0\n" \
"v_mfma_f32_16x16x32_bf16 a[84:87], v[80:83], v[168:171], 0\n" \
"ds_read_b64_tr_b16 v[140:141], v51 offset:16384\n" \
"v_mfma_f32_16x16x32_bf16 a[84:87], v[84:87], v[172:175], a[84:87]\n" \
"v_mfma_f32_16x16x32_bf16 a[88:91], v[80:83], v[176:179], 0\n" \
"ds_read_b128 v[76:79], v49 offset:2048\n" \
"v_mfma_f32_16x16x32_bf16 a[88:91], v[84:87], v[180:183], a[88:91]\n" \
"global_load_lds_dwordx4 v37, s[40:41] offset:1024\n" \
"v_mfma_f32_16x16x32_bf16 a[92:95], v[80:83], v[184:187], 0\n" \
"ds_read_b64_tr_b16 v[142:143], v51 offset:20480\n" \
"v_mfma_f32_16x16x32_bf16 a[92:95], v[84:87], v[188:191], a[92:95]\n" \
"v_mfma_f32_16x16x32_bf16 a[124:127], v[88:91], v[184:187], 0\n" \
"ds_read_b128 v[80:83], v48 offset:4096\n" \
"v_mfma_f32_16x16x32_bf16 a[124:127], v[92:95], v[188:191], a[124:127]\n" \
"global_load_lds_dwordx4 v38, s[54:55] offset:2048\n" \
"v_mfma_f32_16x16x32_bf16 a[120:123], v[88:91], v[176:179], 0\n" \
"ds_read_b64_tr_b16 v[144:145], v50 offset:512\n" \
"v_mfma_f32_16x16x32_bf16 a[120:123], v[92:95], v[180:183], a[120:123]\n" \
"v_mfma_f32_16x16x32_bf16 a[116:119], v[88:91], v[168:171], 0\n" \
"ds_read_b128 v[84:87], v49 offset:4096\n" \
"global_load_lds_dwordx4 v39, s[40:41] offset:3072\n" \
"v_mfma_f32_16x16x32_bf16 a[116:119], v[92:95], v[172:175], a[116:119]\n" \
"s_add_u32 s54, s54, s75\n" \
"s_addc_u32 s55, s55, 0\n" \
"v_mfma_f32_16x16x32_bf16 a[112:115], v[88:91], v[160:163], 0\n" \
"s_add_u32 s40, s40, s75\n" \
"s_addc_u32 s41, s41, 0\n" \
"ds_read_b64_tr_b16 v[146:147], v50 offset:4608\n" \
"v_mfma_f32_16x16x32_bf16 a[112:115], v[92:95], v[164:167], a[112:115]\n" \
"s_add_u32 s68, s66, 0x0\n" \
"s_add_u32 s64, s10, s68\n" \
"s_addc_u32 s65, s11, 0\n" \
"v_mfma_f32_16x16x32_bf16 a[144:147], v[96:99], v[160:163], 0\n" \
"ds_read_b128 v[88:91], v48 offset:6144\n" \
"v_mfma_f32_16x16x32_bf16 a[144:147], v[100:103], v[164:167], a[144:147]\n" \
"global_load_dword v253, v248, s[64:65]\n" \
"v_mfma_f32_16x16x32_bf16 a[148:151], v[96:99], v[168:171], 0\n" \
"ds_read_b64_tr_b16 v[148:149], v50 offset:16896\n" \
"v_mfma_f32_16x16x32_bf16 a[148:151], v[100:103], v[172:175], a[148:151]\n" \
"v_mfma_f32_16x16x32_bf16 a[152:155], v[96:99], v[176:179], 0\n" \
"ds_read_b128 v[92:95], v49 offset:6144\n" \
"v_mfma_f32_16x16x32_bf16 a[152:155], v[100:103], v[180:183], a[152:155]\n" \
"v_mfma_f32_16x16x32_bf16 a[156:159], v[96:99], v[184:187], 0\n" \
"ds_read_b64_tr_b16 v[150:151], v50 offset:20992\n" \
"v_mfma_f32_16x16x32_bf16 a[156:159], v[100:103], v[188:191], a[156:159]\n" \
"v_mfma_f32_16x16x32_bf16 a[188:191], v[104:107], v[184:187], 0\n" \
"ds_read_b128 v[96:99], v48 offset:8192\n" \
"v_mfma_f32_16x16x32_bf16 a[188:191], v[108:111], v[188:191], a[188:191]\n" \
"v_mfma_f32_16x16x32_bf16 a[184:187], v[104:107], v[176:179], 0\n" \
"ds_read_b64_tr_b16 v[152:153], v51 offset:512\n" \
"v_mfma_f32_16x16x32_bf16 a[184:187], v[108:111], v[180:183], a[184:187]\n" \
"v_mfma_f32_16x16x32_bf16 a[180:183], v[104:107], v[168:171], 0\n" \
"ds_read_b128 v[100:103], v49 offset:8192\n" \
"v_mfma_f32_16x16x32_bf16 a[180:183], v[108:111], v[172:175], a[180:183]\n" \
"v_mfma_f32_16x16x32_bf16 a[176:179], v[104:107], v[160:163], 0\n" \
"ds_read_b64_tr_b16 v[154:155], v51 offset:4608\n" \
"v_mfma_f32_16x16x32_bf16 a[176:179], v[108:111], v[164:167], a[176:179]\n" \
"v_mfma_f32_16x16x32_bf16 a[208:211], v[112:115], v[160:163], 0\n" \
"ds_read_b128 v[104:107], v48 offset:10240\n" \
"v_mfma_f32_16x16x32_bf16 a[208:211], v[116:119], v[164:167], a[208:211]\n" \
"v_mfma_f32_16x16x32_bf16 a[212:215], v[112:115], v[168:171], 0\n" \
"ds_read_b64_tr_b16 v[156:157], v51 offset:16896\n" \
"v_mfma_f32_16x16x32_bf16 a[212:215], v[116:119], v[172:175], a[212:215]\n" \
"v_mfma_f32_16x16x32_bf16 a[216:219], v[112:115], v[176:179], 0\n" \
"ds_read_b128 v[108:111], v49 offset:10240\n" \
"v_mfma_f32_16x16x32_bf16 a[216:219], v[116:119], v[180:183], a[216:219]\n" \
"v_mfma_f32_16x16x32_bf16 a[220:223], v[112:115], v[184:187], 0\n" \
"ds_read_b64_tr_b16 v[158:159], v51 offset:20992\n" \
"v_mfma_f32_16x16x32_bf16 a[220:223], v[116:119], v[188:191], a[220:223]\n" \
"v_mfma_f32_16x16x32_bf16 a[252:255], v[120:123], v[184:187], 0\n" \
"ds_read_b128 v[112:115], v48 offset:12288\n" \
"v_mfma_f32_16x16x32_bf16 a[252:255], v[124:127], v[188:191], a[252:255]\n" \
"v_mfma_f32_16x16x32_bf16 a[248:251], v[120:123], v[176:179], 0\n" \
"v_mfma_f32_16x16x32_bf16 a[248:251], v[124:127], v[180:183], a[248:251]\n" \
"v_mfma_f32_16x16x32_bf16 a[244:247], v[120:123], v[168:171], 0\n" \
"ds_read_b128 v[116:119], v49 offset:12288\n" \
"v_mfma_f32_16x16x32_bf16 a[244:247], v[124:127], v[172:175], a[244:247]\n" \
"v_mfma_f32_16x16x32_bf16 a[240:243], v[120:123], v[160:163], 0\n" \
"v_mfma_f32_16x16x32_bf16 a[240:243], v[124:127], v[164:167], a[240:243]\n" \
"s_waitcnt lgkmcnt(1)\n" \
"v_mfma_f32_16x16x32_bf16 a[0:3], v[128:131], v[64:67], a[0:3]\n" \
"ds_read_b128 v[120:123], v48 offset:14336\n" \
"v_mfma_f32_16x16x32_bf16 a[0:3], v[132:135], v[68:71], a[0:3]\n" \
"v_mfma_f32_16x16x32_bf16 a[4:7], v[136:139], v[64:67], a[4:7]\n" \
"ds_read_b128 v[124:127], v49 offset:14336\n" \
"v_mfma_f32_16x16x32_bf16 a[4:7], v[140:143], v[68:71], a[4:7]\n" \
"s_add_u32 s68, s66, 0x100000\n" \
"v_mfma_f32_16x16x32_bf16 a[8:11], v[144:147], v[64:67], a[8:11]\n" \
"s_add_u32 s64, s10, s68\n" \
"s_addc_u32 s65, s11, 0\n" \
"ds_read_b64_tr_b16 v[160:161], v50 offset:1024\n" \
"v_mfma_f32_16x16x32_bf16 a[8:11], v[148:151], v[68:71], a[8:11]\n" \
"global_load_dword v253, v248, s[64:65]\n" \
"v_mfma_f32_16x16x32_bf16 a[12:15], v[152:155], v[64:67], a[12:15]\n" \
"s_add_u32 m0, s87, 4096\n" \
"ds_read_b64_tr_b16 v[162:163], v50 offset:5120\n" \
"v_mfma_f32_16x16x32_bf16 a[12:15], v[156:159], v[68:71], a[12:15]\n" \
"global_load_lds_dwordx4 v40, s[52:53] offset:-4096\n" \
"v_mfma_f32_16x16x32_bf16 a[44:47], v[152:155], v[72:75], a[44:47]\n" \
"ds_read_b64_tr_b16 v[164:165], v50 offset:17408\n" \
"v_mfma_f32_16x16x32_bf16 a[44:47], v[156:159], v[76:79], a[44:47]\n" \
"v_mfma_f32_16x16x32_bf16 a[40:43], v[144:147], v[72:75], a[40:43]\n" \
"ds_read_b64_tr_b16 v[166:167], v50 offset:21504\n" \
"v_mfma_f32_16x16x32_bf16 a[40:43], v[148:151], v[76:79], a[40:43]\n" \
"global_load_lds_dwordx4 v41, s[50:51] offset:-3072\n" \
"v_mfma_f32_16x16x32_bf16 a[36:39], v[136:139], v[72:75], a[36:39]\n" \
"ds_read_b64_tr_b16 v[168:169], v51 offset:1024\n" \
"v_mfma_f32_16x16x32_bf16 a[36:39], v[140:143], v[76:79], a[36:39]\n" \
"v_mfma_f32_16x16x32_bf16 a[32:35], v[128:131], v[72:75], a[32:35]\n" \
"ds_read_b64_tr_b16 v[170:171], v51 offset:5120\n" \
"v_mfma_f32_16x16x32_bf16 a[32:35], v[132:135], v[76:79], a[32:35]\n" \
"global_load_lds_dwordx4 v42, s[52:53] offset:-2048\n" \
"v_mfma_f32_16x16x32_bf16 a[64:67], v[128:131], v[80:83], a[64:67]\n" \
"ds_read_b64_tr_b16 v[172:173], v51 offset:17408\n" \
"v_mfma_f32_16x16x32_bf16 a[64:67], v[132:135], v[84:87], a[64:67]\n" \
"v_mfma_f32_16x16x32_bf16 a[68:71], v[136:139], v[80:83], a[68:71]\n" \
"ds_read_b64_tr_b16 v[174:175], v51 offset:21504\n" \
"v_mfma_f32_16x16x32_bf16 a[68:71], v[140:143], v[84:87], a[68:71]\n" \
"global_load_lds_dwordx4 v43, s[50:51] offset:-1024\n" \
"v_mfma_f32_16x16x32_bf16 a[72:75], v[144:147], v[80:83], a[72:75]\n" \
"ds_read_b64_tr_b16 v[176:177], v50 offset:1536\n" \
"v_mfma_f32_16x16x32_bf16 a[72:75], v[148:151], v[84:87], a[72:75]\n" \
"v_mfma_f32_16x16x32_bf16 a[76:79], v[152:155], v[80:83], a[76:79]\n" \
"ds_read_b64_tr_b16 v[178:179], v50 offset:5632\n" \
"v_mfma_f32_16x16x32_bf16 a[76:79], v[156:159], v[84:87], a[76:79]\n" \
"global_load_lds_dwordx4 v44, s[52:53] offset:0\n" \
"v_mfma_f32_16x16x32_bf16 a[108:111], v[152:155], v[88:91], a[108:111]\n" \
"ds_read_b64_tr_b16 v[180:181], v50 offset:17920\n" \
"v_mfma_f32_16x16x32_bf16 a[108:111], v[156:159], v[92:95], a[108:111]\n" \
"v_mfma_f32_16x16x32_bf16 a[104:107], v[144:147], v[88:91], a[104:107]\n" \
"ds_read_b64_tr_b16 v[182:183], v50 offset:22016\n" \
"v_mfma_f32_16x16x32_bf16 a[104:107], v[148:151], v[92:95], a[104:107]\n" \
"global_load_lds_dwordx4 v45, s[50:51] offset:1024\n" \
"v_mfma_f32_16x16x32_bf16 a[100:103], v[136:139], v[88:91], a[100:103]\n" \
"ds_read_b64_tr_b16 v[184:185], v51 offset:1536\n" \
"v_mfma_f32_16x16x32_bf16 a[100:103], v[140:143], v[92:95], a[100:103]\n" \
"v_mfma_f32_16x16x32_bf16 a[96:99], v[128:131], v[88:91], a[96:99]\n" \
"ds_read_b64_tr_b16 v[186:187], v51 offset:5632\n" \
"v_mfma_f32_16x16x32_bf16 a[96:99], v[132:135], v[92:95], a[96:99]\n" \
"global_load_lds_dwordx4 v46, s[52:53] offset:2048\n" \
"v_mfma_f32_16x16x32_bf16 a[128:131], v[128:131], v[96:99], a[128:131]\n" \
"ds_read_b64_tr_b16 v[188:189], v51 offset:17920\n" \
"v_mfma_f32_16x16x32_bf16 a[128:131], v[132:135], v[100:103], a[128:131]\n" \
"v_mfma_f32_16x16x32_bf16 a[132:135], v[136:139], v[96:99], a[132:135]\n" \
"ds_read_b64_tr_b16 v[190:191], v51 offset:22016\n" \
"v_mfma_f32_16x16x32_bf16 a[132:135], v[140:143], v[100:103], a[132:135]\n" \
"global_load_lds_dwordx4 v47, s[50:51] offset:3072\n" \
"v_mfma_f32_16x16x32_bf16 a[136:139], v[144:147], v[96:99], a[136:139]\n" \
"s_add_u32 s52, s52, s74\n" \
"s_addc_u32 s53, s53, 0\n" \
"v_mfma_f32_16x16x32_bf16 a[136:139], v[148:151], v[100:103], a[136:139]\n" \
"s_add_u32 s50, s50, s74\n" \
"s_addc_u32 s51, s51, 0\n" \
"v_mfma_f32_16x16x32_bf16 a[140:143], v[152:155], v[96:99], a[140:143]\n" \
"v_mfma_f32_16x16x32_bf16 a[140:143], v[156:159], v[100:103], a[140:143]\n" \
"v_mfma_f32_16x16x32_bf16 a[172:175], v[152:155], v[104:107], a[172:175]\n" \
"s_mul_i32 s76, s73, 0x0\n" \
"s_add_u32 s77, s76, 0xffff8000\n" \
"s_sub_u32 s78, 0xffff8000, s76\n" \
"v_mfma_f32_16x16x32_bf16 a[172:175], v[156:159], v[108:111], a[172:175]\n" \
"v_add_u32 v48, s77, v48\n" \
"v_add_u32 v49, s77, v49\n" \
"v_mfma_f32_16x16x32_bf16 a[168:171], v[144:147], v[104:107], a[168:171]\n" \
"v_add_u32 v50, s78, v50\n" \
"v_add_u32 v51, s78, v51\n" \
"v_mfma_f32_16x16x32_bf16 a[168:171], v[148:151], v[108:111], a[168:171]\n" \
"v_mfma_f32_16x16x32_bf16 a[164:167], v[136:139], v[104:107], a[164:167]\n" \
"v_mfma_f32_16x16x32_bf16 a[164:167], v[140:143], v[108:111], a[164:167]\n" \
"v_mfma_f32_16x16x32_bf16 a[160:163], v[128:131], v[104:107], a[160:163]\n" \
"v_mfma_f32_16x16x32_bf16 a[160:163], v[132:135], v[108:111], a[160:163]\n" \
"s_waitcnt lgkmcnt(0)\n" \
"v_mfma_f32_16x16x32_bf16 a[192:195], v[128:131], v[112:115], a[192:195]\n" \
"v_mfma_f32_16x16x32_bf16 a[192:195], v[132:135], v[116:119], a[192:195]\n" \
"v_mfma_f32_16x16x32_bf16 a[196:199], v[136:139], v[112:115], a[196:199]\n" \
"v_mfma_f32_16x16x32_bf16 a[196:199], v[140:143], v[116:119], a[196:199]\n" \
"v_mfma_f32_16x16x32_bf16 a[200:203], v[144:147], v[112:115], a[200:203]\n" \
"v_mfma_f32_16x16x32_bf16 a[200:203], v[148:151], v[116:119], a[200:203]\n" \
"v_mfma_f32_16x16x32_bf16 a[204:207], v[152:155], v[112:115], a[204:207]\n" \
"v_mfma_f32_16x16x32_bf16 a[204:207], v[156:159], v[116:119], a[204:207]\n" \
"v_mfma_f32_16x16x32_bf16 a[236:239], v[152:155], v[120:123], a[236:239]\n" \
"v_mfma_f32_16x16x32_bf16 a[236:239], v[156:159], v[124:127], a[236:239]\n" \
"Lpg609_%=:\n" \
"Lst0x0_%=:\n" \
"s_cbranch_execz Lpg2768_%=\n" \
"v_mfma_f32_16x16x32_bf16 a[232:235], v[144:147], v[120:123], a[232:235]\n" \
"v_mfma_f32_16x16x32_bf16 a[232:235], v[148:151], v[124:127], a[232:235]\n" \
"v_mfma_f32_16x16x32_bf16 a[228:231], v[136:139], v[120:123], a[228:231]\n" \
"v_mfma_f32_16x16x32_bf16 a[228:231], v[140:143], v[124:127], a[228:231]\n" \
"v_mfma_f32_16x16x32_bf16 a[224:227], v[128:131], v[120:123], a[224:227]\n" \
"s_waitcnt vmcnt(10) lgkmcnt(0)\n" \
"v_mfma_f32_16x16x32_bf16 a[224:227], v[132:135], v[124:127], a[224:227]\n" \
"s_barrier\n" \
"v_mfma_f32_16x16x32_bf16 a[16:19], v[64:67], v[160:163], a[16:19]\n" \
"s_add_u32 m0, s87, 135168\n" \
"ds_read_b64_tr_b16 v[128:129], v50 offset:0\n" \
"v_readfirstlane_b32 s60, v244\n" \
"v_mfma_f32_16x16x32_bf16 a[16:19], v[68:71], v[164:167], a[16:19]\n" \
"s_add_u32 s60, s60, 0x9e3779b9\n" \
"global_load_lds_dwordx4 v32, s[54:55] offset:-4096\n" \
"v_mfma_f32_16x16x32_bf16 a[20:23], v[64:67], v[168:171], a[20:23]\n" \
"s_mul_i32 s88, s62, 557824\n" \
"s_mul_i32 s89, s63, 17408\n" \
"ds_read_b64_tr_b16 v[130:131], v50 offset:4096\n" \
"v_mfma_f32_16x16x32_bf16 a[20:23], v[68:71], v[172:175], a[20:23]\n" \
"s_add_u32 s88, s88, s89\n" \
"s_add_u32 s88, s88, s95\n" \
"s_add_u32 s89, s88, 17850368\n" \
"v_mfma_f32_16x16x32_bf16 a[24:27], v[64:67], v[176:179], a[24:27]\n" \
"s_lshl_b32 s91, s62, 7\n" \
"s_add_u32 s91, s91, 65585152\n" \
"ds_read_b64_tr_b16 v[132:133], v50 offset:16384\n" \
"v_mfma_f32_16x16x32_bf16 a[24:27], v[68:71], v[180:183], a[24:27]\n" \
"s_mov_b32 s92, 0x3e0293ee\n" \
"global_load_lds_dwordx4 v33, s[40:41] offset:-3072\n" \
"v_mfma_f32_16x16x32_bf16 a[28:31], v[64:67], v[184:187], a[28:31]\n" \
"ds_read_b64_tr_b16 v[134:135], v50 offset:20480\n" \
"v_mfma_f32_16x16x32_bf16 a[28:31], v[68:71], v[188:191], a[28:31]\n" \
"v_mfma_f32_16x16x32_bf16 a[60:63], v[72:75], v[184:187], a[60:63]\n" \
"ds_read_b128 v[64:67], v48 offset:0\n" \
"v_mfma_f32_16x16x32_bf16 a[60:63], v[76:79], v[188:191], a[60:63]\n" \
"global_load_lds_dwordx4 v34, s[54:55] offset:-2048\n" \
"v_mfma_f32_16x16x32_bf16 a[56:59], v[72:75], v[176:179], a[56:59]\n" \
"ds_read_b64_tr_b16 v[136:137], v51 offset:0\n" \
"v_mfma_f32_16x16x32_bf16 a[56:59], v[76:79], v[180:183], a[56:59]\n" \
"v_mfma_f32_16x16x32_bf16 a[52:55], v[72:75], v[168:171], a[52:55]\n" \
"ds_read_b128 v[68:71], v49 offset:0\n" \
"v_mfma_f32_16x16x32_bf16 a[52:55], v[76:79], v[172:175], a[52:55]\n" \
"global_load_lds_dwordx4 v35, s[40:41] offset:-1024\n" \
"v_mfma_f32_16x16x32_bf16 a[48:51], v[72:75], v[160:163], a[48:51]\n" \
"ds_read_b64_tr_b16 v[138:139], v51 offset:4096\n" \
"v_mfma_f32_16x16x32_bf16 a[48:51], v[76:79], v[164:167], a[48:51]\n" \
"v_mfma_f32_16x16x32_bf16 a[80:83], v[80:83], v[160:163], a[80:83]\n" \
"ds_read_b128 v[72:75], v48 offset:2048\n" \
"v_mfma_f32_16x16x32_bf16 a[80:83], v[84:87], v[164:167], a[80:83]\n" \
"global_load_lds_dwordx4 v36, s[54:55] offset:0\n" \
"v_mfma_f32_16x16x32_bf16 a[84:87], v[80:83], v[168:171], a[84:87]\n" \
"ds_read_b64_tr_b16 v[140:141], v51 offset:16384\n" \
"v_mfma_f32_16x16x32_bf16 a[84:87], v[84:87], v[172:175], a[84:87]\n" \
"v_mfma_f32_16x16x32_bf16 a[88:91], v[80:83], v[176:179], a[88:91]\n" \
"ds_read_b128 v[76:79], v49 offset:2048\n" \
"v_mfma_f32_16x16x32_bf16 a[88:91], v[84:87], v[180:183], a[88:91]\n" \
"global_load_lds_dwordx4 v37, s[40:41] offset:1024\n" \
"v_mfma_f32_16x16x32_bf16 a[92:95], v[80:83], v[184:187], a[92:95]\n" \
"ds_read_b64_tr_b16 v[142:143], v51 offset:20480\n" \
"v_mfma_f32_16x16x32_bf16 a[92:95], v[84:87], v[188:191], a[92:95]\n" \
"v_mfma_f32_16x16x32_bf16 a[124:127], v[88:91], v[184:187], a[124:127]\n" \
"ds_read_b128 v[80:83], v48 offset:4096\n" \
"v_mfma_f32_16x16x32_bf16 a[124:127], v[92:95], v[188:191], a[124:127]\n" \
"global_load_lds_dwordx4 v38, s[54:55] offset:2048\n" \
"v_mfma_f32_16x16x32_bf16 a[120:123], v[88:91], v[176:179], a[120:123]\n" \
"ds_read_b64_tr_b16 v[144:145], v50 offset:512\n" \
"v_mfma_f32_16x16x32_bf16 a[120:123], v[92:95], v[180:183], a[120:123]\n" \
"v_mfma_f32_16x16x32_bf16 a[116:119], v[88:91], v[168:171], a[116:119]\n" \
"ds_read_b128 v[84:87], v49 offset:4096\n" \
"global_load_lds_dwordx4 v39, s[40:41] offset:3072\n" \
"v_mfma_f32_16x16x32_bf16 a[116:119], v[92:95], v[172:175], a[116:119]\n" \
"s_add_u32 s54, s54, s75\n" \
"s_addc_u32 s55, s55, 0\n" \
"v_mfma_f32_16x16x32_bf16 a[112:115], v[88:91], v[160:163], a[112:115]\n" \
"s_add_u32 s40, s40, s75\n" \
"s_addc_u32 s41, s41, 0\n" \
"ds_read_b64_tr_b16 v[146:147], v50 offset:4608\n" \
"v_mfma_f32_16x16x32_bf16 a[112:115], v[92:95], v[164:167], a[112:115]\n" \
"s_add_u32 s68, s67, 0x200000\n" \
"s_add_u32 s64, s14, s68\n" \
"s_addc_u32 s65, s15, 0\n" \
"v_mfma_f32_16x16x32_bf16 a[144:147], v[96:99], v[160:163], a[144:147]\n" \
"ds_read_b128 v[88:91], v48 offset:6144\n" \
"v_mfma_f32_16x16x32_bf16 a[144:147], v[100:103], v[164:167], a[144:147]\n" \
"global_load_dword v253, v248, s[64:65]\n" \
"v_mfma_f32_16x16x32_bf16 a[148:151], v[96:99], v[168:171], a[148:151]\n" \
"s_add_u32 s68, s67, 0x200000\n" \
"ds_read_b64_tr_b16 v[148:149], v50 offset:16896\n" \
"v_mfma_f32_16x16x32_bf16 a[148:151], v[100:103], v[172:175], a[148:151]\n" \
"s_add_u32 s64, s16, s68\n" \
"s_addc_u32 s65, s17, 0\n" \
"v_mfma_f32_16x16x32_bf16 a[152:155], v[96:99], v[176:179], a[152:155]\n" \
"ds_read_b128 v[92:95], v49 offset:6144\n" \
"v_mfma_f32_16x16x32_bf16 a[152:155], v[100:103], v[180:183], a[152:155]\n" \
"global_load_dword v253, v248, s[64:65]\n" \
"v_mfma_f32_16x16x32_bf16 a[156:159], v[96:99], v[184:187], a[156:159]\n" \
"ds_read_b64_tr_b16 v[150:151], v50 offset:20992\n" \
"v_mfma_f32_16x16x32_bf16 a[156:159], v[100:103], v[188:191], a[156:159]\n" \
"v_mfma_f32_16x16x32_bf16 a[188:191], v[104:107], v[184:187], a[188:191]\n" \
"ds_read_b128 v[96:99], v48 offset:8192\n" \
"v_mfma_f32_16x16x32_bf16 a[188:191], v[108:111], v[188:191], a[188:191]\n" \
"v_mfma_f32_16x16x32_bf16 a[184:187], v[104:107], v[176:179], a[184:187]\n" \
"ds_read_b64_tr_b16 v[152:153], v51 offset:512\n" \
"v_mfma_f32_16x16x32_bf16 a[184:187], v[108:111], v[180:183], a[184:187]\n" \
"v_mfma_f32_16x16x32_bf16 a[180:183], v[104:107], v[168:171], a[180:183]\n" \
"ds_read_b128 v[100:103], v49 offset:8192\n" \
"v_mfma_f32_16x16x32_bf16 a[180:183], v[108:111], v[172:175], a[180:183]\n" \
"v_mfma_f32_16x16x32_bf16 a[176:179], v[104:107], v[160:163], a[176:179]\n" \
"ds_read_b64_tr_b16 v[154:155], v51 offset:4608\n" \
"v_mfma_f32_16x16x32_bf16 a[176:179], v[108:111], v[164:167], a[176:179]\n" \
"v_mfma_f32_16x16x32_bf16 a[208:211], v[112:115], v[160:163], a[208:211]\n" \
"ds_read_b128 v[104:107], v48 offset:10240\n" \
"v_mfma_f32_16x16x32_bf16 a[208:211], v[116:119], v[164:167], a[208:211]\n" \
"v_mfma_f32_16x16x32_bf16 a[212:215], v[112:115], v[168:171], a[212:215]\n" \
"ds_read_b64_tr_b16 v[156:157], v51 offset:16896\n" \
"v_mfma_f32_16x16x32_bf16 a[212:215], v[116:119], v[172:175], a[212:215]\n" \
"v_mfma_f32_16x16x32_bf16 a[216:219], v[112:115], v[176:179], a[216:219]\n" \
"ds_read_b128 v[108:111], v49 offset:10240\n" \
"v_mfma_f32_16x16x32_bf16 a[216:219], v[116:119], v[180:183], a[216:219]\n" \
"v_mfma_f32_16x16x32_bf16 a[220:223], v[112:115], v[184:187], a[220:223]\n" \
"ds_read_b64_tr_b16 v[158:159], v51 offset:20992\n" \
"v_mfma_f32_16x16x32_bf16 a[220:223], v[116:119], v[188:191], a[220:223]\n" \
"v_mfma_f32_16x16x32_bf16 a[252:255], v[120:123], v[184:187], a[252:255]\n" \
"ds_read_b128 v[112:115], v48 offset:12288\n" \
"v_mfma_f32_16x16x32_bf16 a[252:255], v[124:127], v[188:191], a[252:255]\n" \
"v_mfma_f32_16x16x32_bf16 a[248:251], v[120:123], v[176:179], a[248:251]\n" \
"v_mfma_f32_16x16x32_bf16 a[248:251], v[124:127], v[180:183], a[248:251]\n" \
"v_mfma_f32_16x16x32_bf16 a[244:247], v[120:123], v[168:171], a[244:247]\n" \
"ds_read_b128 v[116:119], v49 offset:12288\n" \
"v_mfma_f32_16x16x32_bf16 a[244:247], v[124:127], v[172:175], a[244:247]\n" \
"v_mfma_f32_16x16x32_bf16 a[240:243], v[120:123], v[160:163], a[240:243]\n" \
"v_mfma_f32_16x16x32_bf16 a[240:243], v[124:127], v[164:167], a[240:243]\n" \
"s_waitcnt lgkmcnt(1)\n" \
"v_mfma_f32_16x16x32_bf16 a[0:3], v[128:131], v[64:67], a[0:3]\n" \
"ds_read_b128 v[120:123], v48 offset:14336\n" \
"v_mfma_f32_16x16x32_bf16 a[0:3], v[132:135], v[68:71], a[0:3]\n" \
"v_mfma_f32_16x16x32_bf16 a[4:7], v[136:139], v[64:67], a[4:7]\n" \
"ds_read_b128 v[124:127], v49 offset:14336\n" \
"v_mfma_f32_16x16x32_bf16 a[4:7], v[140:143], v[68:71], a[4:7]\n" \
"s_add_u32 s68, s67, 0x300000\n" \
"v_mfma_f32_16x16x32_bf16 a[8:11], v[144:147], v[64:67], a[8:11]\n" \
"s_add_u32 s64, s14, s68\n" \
"s_addc_u32 s65, s15, 0\n" \
"ds_read_b64_tr_b16 v[160:161], v50 offset:1024\n" \
"v_mfma_f32_16x16x32_bf16 a[8:11], v[148:151], v[68:71], a[8:11]\n" \
"global_load_dword v253, v248, s[64:65]\n" \
"v_mfma_f32_16x16x32_bf16 a[12:15], v[152:155], v[64:67], a[12:15]\n" \
"s_add_u32 m0, s87, 69632\n" \
"ds_read_b64_tr_b16 v[162:163], v50 offset:5120\n" \
"v_mfma_f32_16x16x32_bf16 a[12:15], v[156:159], v[68:71], a[12:15]\n" \
"global_load_lds_dwordx4 v40, s[52:53] offset:-4096\n" \
"v_mfma_f32_16x16x32_bf16 a[44:47], v[152:155], v[72:75], a[44:47]\n" \
"ds_read_b64_tr_b16 v[164:165], v50 offset:17408\n" \
"v_mfma_f32_16x16x32_bf16 a[44:47], v[156:159], v[76:79], a[44:47]\n" \
"v_mfma_f32_16x16x32_bf16 a[40:43], v[144:147], v[72:75], a[40:43]\n" \
"ds_read_b64_tr_b16 v[166:167], v50 offset:21504\n" \
"v_mfma_f32_16x16x32_bf16 a[40:43], v[148:151], v[76:79], a[40:43]\n" \
"global_load_lds_dwordx4 v41, s[50:51] offset:-3072\n" \
"v_mfma_f32_16x16x32_bf16 a[36:39], v[136:139], v[72:75], a[36:39]\n" \
"ds_read_b64_tr_b16 v[168:169], v51 offset:1024\n" \
"v_mfma_f32_16x16x32_bf16 a[36:39], v[140:143], v[76:79], a[36:39]\n" \
"v_mfma_f32_16x16x32_bf16 a[32:35], v[128:131], v[72:75], a[32:35]\n" \
"ds_read_b64_tr_b16 v[170:171], v51 offset:5120\n" \
"v_mfma_f32_16x16x32_bf16 a[32:35], v[132:135], v[76:79], a[32:35]\n" \
"global_load_lds_dwordx4 v42, s[52:53] offset:-2048\n" \
"v_mfma_f32_16x16x32_bf16 a[64:67], v[128:131], v[80:83], a[64:67]\n" \
"ds_read_b64_tr_b16 v[172:173], v51 offset:17408\n" \
"v_mfma_f32_16x16x32_bf16 a[64:67], v[132:135], v[84:87], a[64:67]\n" \
"v_mfma_f32_16x16x32_bf16 a[68:71], v[136:139], v[80:83], a[68:71]\n" \
"ds_read_b64_tr_b16 v[174:175], v51 offset:21504\n" \
"v_mfma_f32_16x16x32_bf16 a[68:71], v[140:143], v[84:87], a[68:71]\n" \
"global_load_lds_dwordx4 v43, s[50:51] offset:-1024\n" \
"v_mfma_f32_16x16x32_bf16 a[72:75], v[144:147], v[80:83], a[72:75]\n" \
"ds_read_b64_tr_b16 v[176:177], v50 offset:1536\n" \
"v_mfma_f32_16x16x32_bf16 a[72:75], v[148:151], v[84:87], a[72:75]\n" \
"v_mfma_f32_16x16x32_bf16 a[76:79], v[152:155], v[80:83], a[76:79]\n" \
"ds_read_b64_tr_b16 v[178:179], v50 offset:5632\n" \
"v_mfma_f32_16x16x32_bf16 a[76:79], v[156:159], v[84:87], a[76:79]\n" \
"global_load_lds_dwordx4 v44, s[52:53] offset:0\n" \
"v_mfma_f32_16x16x32_bf16 a[108:111], v[152:155], v[88:91], a[108:111]\n" \
"ds_read_b64_tr_b16 v[180:181], v50 offset:17920\n" \
"v_mfma_f32_16x16x32_bf16 a[108:111], v[156:159], v[92:95], a[108:111]\n" \
"v_mfma_f32_16x16x32_bf16 a[104:107], v[144:147], v[88:91], a[104:107]\n" \
"ds_read_b64_tr_b16 v[182:183], v50 offset:22016\n" \
"v_mfma_f32_16x16x32_bf16 a[104:107], v[148:151], v[92:95], a[104:107]\n" \
"global_load_lds_dwordx4 v45, s[50:51] offset:1024\n" \
"v_mfma_f32_16x16x32_bf16 a[100:103], v[136:139], v[88:91], a[100:103]\n" \
"ds_read_b64_tr_b16 v[184:185], v51 offset:1536\n" \
"v_mfma_f32_16x16x32_bf16 a[100:103], v[140:143], v[92:95], a[100:103]\n" \
"v_mfma_f32_16x16x32_bf16 a[96:99], v[128:131], v[88:91], a[96:99]\n" \
"ds_read_b64_tr_b16 v[186:187], v51 offset:5632\n" \
"v_mfma_f32_16x16x32_bf16 a[96:99], v[132:135], v[92:95], a[96:99]\n" \
"global_load_lds_dwordx4 v46, s[52:53] offset:2048\n" \
"v_mfma_f32_16x16x32_bf16 a[128:131], v[128:131], v[96:99], a[128:131]\n" \
"ds_read_b64_tr_b16 v[188:189], v51 offset:17920\n" \
"v_mfma_f32_16x16x32_bf16 a[128:131], v[132:135], v[100:103], a[128:131]\n" \
"v_mfma_f32_16x16x32_bf16 a[132:135], v[136:139], v[96:99], a[132:135]\n" \
"ds_read_b64_tr_b16 v[190:191], v51 offset:22016\n" \
"v_mfma_f32_16x16x32_bf16 a[132:135], v[140:143], v[100:103], a[132:135]\n" \
"global_load_lds_dwordx4 v47, s[50:51] offset:3072\n" \
"v_mfma_f32_16x16x32_bf16 a[136:139], v[144:147], v[96:99], a[136:139]\n" \
"s_add_u32 s52, s52, s74\n" \
"s_addc_u32 s53, s53, 0\n" \
"v_mfma_f32_16x16x32_bf16 a[136:139], v[148:151], v[100:103], a[136:139]\n" \
"s_add_u32 s50, s50, s74\n" \
"s_addc_u32 s51, s51, 0\n" \
"v_mfma_f32_16x16x32_bf16 a[140:143], v[152:155], v[96:99], a[140:143]\n" \
"v_mfma_f32_16x16x32_bf16 a[140:143], v[156:159], v[100:103], a[140:143]\n" \
"v_mfma_f32_16x16x32_bf16 a[172:175], v[152:155], v[104:107], a[172:175]\n" \
"s_mul_i32 s76, s73, 0xfffd0000\n" \
"s_add_u32 s77, s76, 0x18000\n" \
"s_sub_u32 s78, 0xfffe8000, s76\n" \
"v_mfma_f32_16x16x32_bf16 a[172:175], v[156:159], v[108:111], a[172:175]\n" \
"v_add_u32 v48, s77, v48\n" \
"v_add_u32 v49, s77, v49\n" \
"v_mfma_f32_16x16x32_bf16 a[168:171], v[144:147], v[104:107], a[168:171]\n" \
"v_add_u32 v50, s78, v50\n" \
"v_add_u32 v51, s78, v51\n" \
"v_mfma_f32_16x16x32_bf16 a[168:171], v[148:151], v[108:111], a[168:171]\n" \
"v_mfma_f32_16x16x32_bf16 a[164:167], v[136:139], v[104:107], a[164:167]\n" \
"v_mfma_f32_16x16x32_bf16 a[164:167], v[140:143], v[108:111], a[164:167]\n" \
"v_mfma_f32_16x16x32_bf16 a[160:163], v[128:131], v[104:107], a[160:163]\n" \
"v_mfma_f32_16x16x32_bf16 a[160:163], v[132:135], v[108:111], a[160:163]\n" \
"s_waitcnt lgkmcnt(0)\n" \
"v_mfma_f32_16x16x32_bf16 a[192:195], v[128:131], v[112:115], a[192:195]\n" \
"v_mfma_f32_16x16x32_bf16 a[192:195], v[132:135], v[116:119], a[192:195]\n" \
"v_mfma_f32_16x16x32_bf16 a[196:199], v[136:139], v[112:115], a[196:199]\n" \
"v_mfma_f32_16x16x32_bf16 a[196:199], v[140:143], v[116:119], a[196:199]\n" \
"v_mfma_f32_16x16x32_bf16 a[200:203], v[144:147], v[112:115], a[200:203]\n" \
"v_mfma_f32_16x16x32_bf16 a[200:203], v[148:151], v[116:119], a[200:203]\n" \
"v_mfma_f32_16x16x32_bf16 a[204:207], v[152:155], v[112:115], a[204:207]\n" \
"v_mfma_f32_16x16x32_bf16 a[204:207], v[156:159], v[116:119], a[204:207]\n" \
"v_mfma_f32_16x16x32_bf16 a[236:239], v[152:155], v[120:123], a[236:239]\n" \
"v_mfma_f32_16x16x32_bf16 a[236:239], v[156:159], v[124:127], a[236:239]\n" \
"v_mfma_f32_16x16x32_bf16 a[232:235], v[144:147], v[120:123], a[232:235]\n" \
"v_mfma_f32_16x16x32_bf16 a[232:235], v[148:151], v[124:127], a[232:235]\n" \
"v_mfma_f32_16x16x32_bf16 a[228:231], v[136:139], v[120:123], a[228:231]\n" \
"v_mfma_f32_16x16x32_bf16 a[228:231], v[140:143], v[124:127], a[228:231]\n" \
"v_mfma_f32_16x16x32_bf16 a[224:227], v[128:131], v[120:123], a[224:227]\n" \
"s_waitcnt vmcnt(11) lgkmcnt(0)\n" \
"v_mfma_f32_16x16x32_bf16 a[224:227], v[132:135], v[124:127], a[224:227]\n" \
"s_barrier\n" \
"v_mfma_f32_16x16x32_bf16 a[16:19], v[64:67], v[160:163], a[16:19]\n" \
"s_add_u32 m0, s87, 102400\n" \
"ds_read_b64_tr_b16 v[128:129], v50 offset:0\n" \
"v_mfma_f32_16x16x32_bf16 a[16:19], v[68:71], v[164:167], a[16:19]\n" \
"global_load_lds_dwordx4 v32, s[54:55] offset:-4096\n" \
"v_mfma_f32_16x16x32_bf16 a[20:23], v[64:67], v[168:171], a[20:23]\n" \
"ds_read_b64_tr_b16 v[130:131], v50 offset:4096\n" \
"v_mfma_f32_16x16x32_bf16 a[20:23], v[68:71], v[172:175], a[20:23]\n" \
"v_mfma_f32_16x16x32_bf16 a[24:27], v[64:67], v[176:179], a[24:27]\n" \
"ds_read_b64_tr_b16 v[132:133], v50 offset:16384\n" \
"v_mfma_f32_16x16x32_bf16 a[24:27], v[68:71], v[180:183], a[24:27]\n" \
"global_load_lds_dwordx4 v33, s[40:41] offset:-3072\n" \
"v_mfma_f32_16x16x32_bf16 a[28:31], v[64:67], v[184:187], a[28:31]\n" \
"ds_read_b64_tr_b16 v[134:135], v50 offset:20480\n" \
"v_mfma_f32_16x16x32_bf16 a[28:31], v[68:71], v[188:191], a[28:31]\n" \
"v_mfma_f32_16x16x32_bf16 a[60:63], v[72:75], v[184:187], a[60:63]\n" \
"ds_read_b128 v[64:67], v48 offset:0\n" \
"v_mfma_f32_16x16x32_bf16 a[60:63], v[76:79], v[188:191], a[60:63]\n" \
"global_load_lds_dwordx4 v34, s[54:55] offset:-2048\n" \
"v_mfma_f32_16x16x32_bf16 a[56:59], v[72:75], v[176:179], a[56:59]\n" \
"ds_read_b64_tr_b16 v[136:137], v51 offset:0\n" \
"v_mfma_f32_16x16x32_bf16 a[56:59], v[76:79], v[180:183], a[56:59]\n" \
"v_mfma_f32_16x16x32_bf16 a[52:55], v[72:75], v[168:171], a[52:55]\n" \
"ds_read_b128 v[68:71], v49 offset:0\n" \
"v_mfma_f32_16x16x32_bf16 a[52:55], v[76:79], v[172:175], a[52:55]\n" \
"global_load_lds_dwordx4 v35, s[40:41] offset:-1024\n" \
"v_mfma_f32_16x16x32_bf16 a[48:51], v[72:75], v[160:163], a[48:51]\n" \
"ds_read_b64_tr_b16 v[138:139], v51 offset:4096\n" \
"v_mfma_f32_16x16x32_bf16 a[48:51], v[76:79], v[164:167], a[48:51]\n" \
"v_mfma_f32_16x16x32_bf16 a[80:83], v[80:83], v[160:163], a[80:83]\n" \
"ds_read_b128 v[72:75], v48 offset:2048\n" \
"v_mfma_f32_16x16x32_bf16 a[80:83], v[84:87], v[164:167], a[80:83]\n" \
"global_load_lds_dwordx4 v36, s[54:55] offset:0\n" \
"v_mfma_f32_16x16x32_bf16 a[84:87], v[80:83], v[168:171], a[84:87]\n" \
"ds_read_b64_tr_b16 v[140:141], v51 offset:16384\n" \
"v_mfma_f32_16x16x32_bf16 a[84:87], v[84:87], v[172:175], a[84:87]\n" \
"v_mfma_f32_16x16x32_bf16 a[88:91], v[80:83], v[176:179], a[88:91]\n" \
"ds_read_b128 v[76:79], v49 offset:2048\n" \
"v_mfma_f32_16x16x32_bf16 a[88:91], v[84:87], v[180:183], a[88:91]\n" \
"global_load_lds_dwordx4 v37, s[40:41] offset:1024\n" \
"v_mfma_f32_16x16x32_bf16 a[92:95], v[80:83], v[184:187], a[92:95]\n" \
"ds_read_b64_tr_b16 v[142:143], v51 offset:20480\n" \
"v_mfma_f32_16x16x32_bf16 a[92:95], v[84:87], v[188:191], a[92:95]\n" \
"v_mfma_f32_16x16x32_bf16 a[124:127], v[88:91], v[184:187], a[124:127]\n" \
"ds_read_b128 v[80:83], v48 offset:4096\n" \
"v_mfma_f32_16x16x32_bf16 a[124:127], v[92:95], v[188:191], a[124:127]\n" \
"global_load_lds_dwordx4 v38, s[54:55] offset:2048\n" \
"v_mfma_f32_16x16x32_bf16 a[120:123], v[88:91], v[176:179], a[120:123]\n" \
"ds_read_b64_tr_b16 v[144:145], v50 offset:512\n" \
"v_mfma_f32_16x16x32_bf16 a[120:123], v[92:95], v[180:183], a[120:123]\n" \
"v_mfma_f32_16x16x32_bf16 a[116:119], v[88:91], v[168:171], a[116:119]\n" \
"ds_read_b128 v[84:87], v49 offset:4096\n" \
"global_load_lds_dwordx4 v39, s[40:41] offset:3072\n" \
"v_mfma_f32_16x16x32_bf16 a[116:119], v[92:95], v[172:175], a[116:119]\n" \
"s_add_u32 s54, s54, s75\n" \
"s_addc_u32 s55, s55, 0\n" \
"v_mfma_f32_16x16x32_bf16 a[112:115], v[88:91], v[160:163], a[112:115]\n" \
"s_add_u32 s40, s40, s75\n" \
"s_addc_u32 s41, s41, 0\n" \
"ds_read_b64_tr_b16 v[146:147], v50 offset:4608\n" \
"v_mfma_f32_16x16x32_bf16 a[112:115], v[92:95], v[164:167], a[112:115]\n" \
"s_add_u32 s68, s67, 0x300000\n" \
"s_add_u32 s64, s16, s68\n" \
"s_addc_u32 s65, s17, 0\n" \
"v_mfma_f32_16x16x32_bf16 a[144:147], v[96:99], v[160:163], a[144:147]\n" \
"ds_read_b128 v[88:91], v48 offset:6144\n" \
"v_mfma_f32_16x16x32_bf16 a[144:147], v[100:103], v[164:167], a[144:147]\n" \
"global_load_dword v253, v248, s[64:65]\n" \
"v_mfma_f32_16x16x32_bf16 a[148:151], v[96:99], v[168:171], a[148:151]\n" \
"s_add_u32 s68, s66, 0x0\n" \
"ds_read_b64_tr_b16 v[148:149], v50 offset:16896\n" \
"v_mfma_f32_16x16x32_bf16 a[148:151], v[100:103], v[172:175], a[148:151]\n" \
"s_add_u32 s64, s8, s68\n" \
"s_addc_u32 s65, s9, 0\n" \
"v_mfma_f32_16x16x32_bf16 a[152:155], v[96:99], v[176:179], a[152:155]\n" \
"ds_read_b128 v[92:95], v49 offset:6144\n" \
"v_mfma_f32_16x16x32_bf16 a[152:155], v[100:103], v[180:183], a[152:155]\n" \
"global_load_dword v253, v248, s[64:65]\n" \
"v_mfma_f32_16x16x32_bf16 a[156:159], v[96:99], v[184:187], a[156:159]\n" \
"ds_read_b64_tr_b16 v[150:151], v50 offset:20992\n" \
"v_mfma_f32_16x16x32_bf16 a[156:159], v[100:103], v[188:191], a[156:159]\n" \
"v_mfma_f32_16x16x32_bf16 a[188:191], v[104:107], v[184:187], a[188:191]\n" \
"ds_read_b128 v[96:99], v48 offset:8192\n" \
"v_mfma_f32_16x16x32_bf16 a[188:191], v[108:111], v[188:191], a[188:191]\n" \
"v_mfma_f32_16x16x32_bf16 a[184:187], v[104:107], v[176:179], a[184:187]\n" \
"ds_read_b64_tr_b16 v[152:153], v51 offset:512\n" \
"v_mfma_f32_16x16x32_bf16 a[184:187], v[108:111], v[180:183], a[184:187]\n" \
"v_mfma_f32_16x16x32_bf16 a[180:183], v[104:107], v[168:171], a[180:183]\n" \
"ds_read_b128 v[100:103], v49 offset:8192\n" \
"v_mfma_f32_16x16x32_bf16 a[180:183], v[108:111], v[172:175], a[180:183]\n" \
"v_mfma_f32_16x16x32_bf16 a[176:179], v[104:107], v[160:163], a[176:179]\n" \
"ds_read_b64_tr_b16 v[154:155], v51 offset:4608\n" \
"v_mfma_f32_16x16x32_bf16 a[176:179], v[108:111], v[164:167], a[176:179]\n" \
"v_mfma_f32_16x16x32_bf16 a[208:211], v[112:115], v[160:163], a[208:211]\n" \
"ds_read_b128 v[104:107], v48 offset:10240\n" \
"v_mfma_f32_16x16x32_bf16 a[208:211], v[116:119], v[164:167], a[208:211]\n" \
"v_mfma_f32_16x16x32_bf16 a[212:215], v[112:115], v[168:171], a[212:215]\n" \
"ds_read_b64_tr_b16 v[156:157], v51 offset:16896\n" \
"v_mfma_f32_16x16x32_bf16 a[212:215], v[116:119], v[172:175], a[212:215]\n" \
"v_mfma_f32_16x16x32_bf16 a[216:219], v[112:115], v[176:179], a[216:219]\n" \
"ds_read_b128 v[108:111], v49 offset:10240\n" \
"v_mfma_f32_16x16x32_bf16 a[216:219], v[116:119], v[180:183], a[216:219]\n" \
"v_mfma_f32_16x16x32_bf16 a[220:223], v[112:115], v[184:187], a[220:223]\n" \
"ds_read_b64_tr_b16 v[158:159], v51 offset:20992\n" \
"v_mfma_f32_16x16x32_bf16 a[220:223], v[116:119], v[188:191], a[220:223]\n" \
"v_mfma_f32_16x16x32_bf16 a[252:255], v[120:123], v[184:187], a[252:255]\n" \
"ds_read_b128 v[112:115], v48 offset:12288\n" \
"v_mfma_f32_16x16x32_bf16 a[252:255], v[124:127], v[188:191], a[252:255]\n" \
"v_mfma_f32_16x16x32_bf16 a[248:251], v[120:123], v[176:179], a[248:251]\n" \
"v_mfma_f32_16x16x32_bf16 a[248:251], v[124:127], v[180:183], a[248:251]\n" \
"v_mfma_f32_16x16x32_bf16 a[244:247], v[120:123], v[168:171], a[244:247]\n" \
"ds_read_b128 v[116:119], v49 offset:12288\n" \
"v_mfma_f32_16x16x32_bf16 a[244:247], v[124:127], v[172:175], a[244:247]\n" \
"v_mfma_f32_16x16x32_bf16 a[240:243], v[120:123], v[160:163], a[240:243]\n" \
"v_mfma_f32_16x16x32_bf16 a[240:243], v[124:127], v[164:167], a[240:243]\n" \
"s_waitcnt lgkmcnt(1)\n" \
"v_mfma_f32_16x16x32_bf16 a[0:3], v[128:131], v[64:67], a[0:3]\n" \
"ds_read_b128 v[120:123], v48 offset:14336\n" \
"v_mfma_f32_16x16x32_bf16 a[0:3], v[132:135], v[68:71], a[0:3]\n" \
"v_mfma_f32_16x16x32_bf16 a[4:7], v[136:139], v[64:67], a[4:7]\n" \
"ds_read_b128 v[124:127], v49 offset:14336\n" \
"v_mfma_f32_16x16x32_bf16 a[4:7], v[140:143], v[68:71], a[4:7]\n" \
"s_add_u32 s68, s66, 0x100000\n" \
"v_mfma_f32_16x16x32_bf16 a[8:11], v[144:147], v[64:67], a[8:11]\n" \
"s_add_u32 s64, s8, s68\n" \
"s_addc_u32 s65, s9, 0\n" \
"ds_read_b64_tr_b16 v[160:161], v50 offset:1024\n" \
"v_mfma_f32_16x16x32_bf16 a[8:11], v[148:151], v[68:71], a[8:11]\n" \
"global_load_dword v253, v248, s[64:65]\n" \
"v_mfma_f32_16x16x32_bf16 a[12:15], v[152:155], v[64:67], a[12:15]\n" \
"s_add_u32 m0, s87, 36864\n" \
"ds_read_b64_tr_b16 v[162:163], v50 offset:5120\n" \
"v_mfma_f32_16x16x32_bf16 a[12:15], v[156:159], v[68:71], a[12:15]\n" \
"global_load_lds_dwordx4 v40, s[52:53] offset:-4096\n" \
"v_mfma_f32_16x16x32_bf16 a[44:47], v[152:155], v[72:75], a[44:47]\n" \
"ds_read_b64_tr_b16 v[164:165], v50 offset:17408\n" \
"v_mfma_f32_16x16x32_bf16 a[44:47], v[156:159], v[76:79], a[44:47]\n" \
"v_mfma_f32_16x16x32_bf16 a[40:43], v[144:147], v[72:75], a[40:43]\n" \
"ds_read_b64_tr_b16 v[166:167], v50 offset:21504\n" \
"v_mfma_f32_16x16x32_bf16 a[40:43], v[148:151], v[76:79], a[40:43]\n" \
"global_load_lds_dwordx4 v41, s[50:51] offset:-3072\n" \
"v_mfma_f32_16x16x32_bf16 a[36:39], v[136:139], v[72:75], a[36:39]\n" \
"ds_read_b64_tr_b16 v[168:169], v51 offset:1024\n" \
"v_mfma_f32_16x16x32_bf16 a[36:39], v[140:143], v[76:79], a[36:39]\n" \
"v_mfma_f32_16x16x32_bf16 a[32:35], v[128:131], v[72:75], a[32:35]\n" \
"ds_read_b64_tr_b16 v[170:171], v51 offset:5120\n" \
"v_mfma_f32_16x16x32_bf16 a[32:35], v[132:135], v[76:79], a[32:35]\n" \
"global_load_lds_dwordx4 v42, s[52:53] offset:-2048\n" \
"v_mfma_f32_16x16x32_bf16 a[64:67], v[128:131], v[80:83], a[64:67]\n" \
"ds_read_b64_tr_b16 v[172:173], v51 offset:17408\n" \
"v_mfma_f32_16x16x32_bf16 a[64:67], v[132:135], v[84:87], a[64:67]\n" \
"v_mfma_f32_16x16x32_bf16 a[68:71], v[136:139], v[80:83], a[68:71]\n" \
"ds_read_b64_tr_b16 v[174:175], v51 offset:21504\n" \
"v_mfma_f32_16x16x32_bf16 a[68:71], v[140:143], v[84:87], a[68:71]\n" \
"global_load_lds_dwordx4 v43, s[50:51] offset:-1024\n" \
"v_mfma_f32_16x16x32_bf16 a[72:75], v[144:147], v[80:83], a[72:75]\n" \
"ds_read_b64_tr_b16 v[176:177], v50 offset:1536\n" \
"v_mfma_f32_16x16x32_bf16 a[72:75], v[148:151], v[84:87], a[72:75]\n" \
"v_mfma_f32_16x16x32_bf16 a[76:79], v[152:155], v[80:83], a[76:79]\n" \
"ds_read_b64_tr_b16 v[178:179], v50 offset:5632\n" \
"v_mfma_f32_16x16x32_bf16 a[76:79], v[156:159], v[84:87], a[76:79]\n" \
"global_load_lds_dwordx4 v44, s[52:53] offset:0\n" \
"v_mfma_f32_16x16x32_bf16 a[108:111], v[152:155], v[88:91], a[108:111]\n" \
"ds_read_b64_tr_b16 v[180:181], v50 offset:17920\n" \
"v_mfma_f32_16x16x32_bf16 a[108:111], v[156:159], v[92:95], a[108:111]\n" \
"v_mfma_f32_16x16x32_bf16 a[104:107], v[144:147], v[88:91], a[104:107]\n" \
"ds_read_b64_tr_b16 v[182:183], v50 offset:22016\n" \
"v_mfma_f32_16x16x32_bf16 a[104:107], v[148:151], v[92:95], a[104:107]\n" \
"global_load_lds_dwordx4 v45, s[50:51] offset:1024\n" \
"v_mfma_f32_16x16x32_bf16 a[100:103], v[136:139], v[88:91], a[100:103]\n" \
"ds_read_b64_tr_b16 v[184:185], v51 offset:1536\n" \
"v_mfma_f32_16x16x32_bf16 a[100:103], v[140:143], v[92:95], a[100:103]\n" \
"v_mfma_f32_16x16x32_bf16 a[96:99], v[128:131], v[88:91], a[96:99]\n" \
"ds_read_b64_tr_b16 v[186:187], v51 offset:5632\n" \
"v_mfma_f32_16x16x32_bf16 a[96:99], v[132:135], v[92:95], a[96:99]\n" \
"global_load_lds_dwordx4 v46, s[52:53] offset:2048\n" \
"v_mfma_f32_16x16x32_bf16 a[128:131], v[128:131], v[96:99], a[128:131]\n" \
"ds_read_b64_tr_b16 v[188:189], v51 offset:17920\n" \
"v_mfma_f32_16x16x32_bf16 a[128:131], v[132:135], v[100:103], a[128:131]\n" \
"v_mfma_f32_16x16x32_bf16 a[132:135], v[136:139], v[96:99], a[132:135]\n" \
"ds_read_b64_tr_b16 v[190:191], v51 offset:22016\n" \
"v_mfma_f32_16x16x32_bf16 a[132:135], v[140:143], v[100:103], a[132:135]\n" \
"global_load_lds_dwordx4 v47, s[50:51] offset:3072\n" \
"v_mfma_f32_16x16x32_bf16 a[136:139], v[144:147], v[96:99], a[136:139]\n" \
"s_add_u32 s52, s52, s74\n" \
"s_addc_u32 s53, s53, 0\n" \
"v_mfma_f32_16x16x32_bf16 a[136:139], v[148:151], v[100:103], a[136:139]\n" \
"s_add_u32 s50, s50, s74\n" \
"s_addc_u32 s51, s51, 0\n" \
"v_mfma_f32_16x16x32_bf16 a[140:143], v[152:155], v[96:99], a[140:143]\n" \
"v_mfma_f32_16x16x32_bf16 a[140:143], v[156:159], v[100:103], a[140:143]\n" \
"v_mfma_f32_16x16x32_bf16 a[172:175], v[152:155], v[104:107], a[172:175]\n" \
"s_mul_i32 s76, s73, 0x18000\n" \
"s_add_u32 s77, s76, 0xffff8000\n" \
"s_sub_u32 s78, 0x10000, s76\n" \
"v_mfma_f32_16x16x32_bf16 a[172:175], v[156:159], v[108:111], a[172:175]\n" \
"v_add_u32 v48, s77, v48\n" \
"v_add_u32 v49, s77, v49\n" \
"v_mfma_f32_16x16x32_bf16 a[168:171], v[144:147], v[104:107], a[168:171]\n" \
"v_add_u32 v50, s78, v50\n" \
"v_add_u32 v51, s78, v51\n" \
"v_mfma_f32_16x16x32_bf16 a[168:171], v[148:151], v[108:111], a[168:171]\n" \
"v_mfma_f32_16x16x32_bf16 a[164:167], v[136:139], v[104:107], a[164:167]\n" \
"v_mfma_f32_16x16x32_bf16 a[164:167], v[140:143], v[108:111], a[164:167]\n" \
"v_mfma_f32_16x16x32_bf16 a[160:163], v[128:131], v[104:107], a[160:163]\n" \
"v_mfma_f32_16x16x32_bf16 a[160:163], v[132:135], v[108:111], a[160:163]\n" \
"s_waitcnt lgkmcnt(0)\n" \
"v_mfma_f32_16x16x32_bf16 a[192:195], v[128:131], v[112:115], a[192:195]\n" \
"v_mfma_f32_16x16x32_bf16 a[192:195], v[132:135], v[116:119], a[192:195]\n" \
"v_mfma_f32_16x16x32_bf16 a[196:199], v[136:139], v[112:115], a[196:199]\n" \
"v_mfma_f32_16x16x32_bf16 a[196:199], v[140:143], v[116:119], a[196:199]\n" \
"v_mfma_f32_16x16x32_bf16 a[200:203], v[144:147], v[112:115], a[200:203]\n" \
"v_mfma_f32_16x16x32_bf16 a[200:203], v[148:151], v[116:119], a[200:203]\n" \
"v_mfma_f32_16x16x32_bf16 a[204:207], v[152:155], v[112:115], a[204:207]\n" \
"v_mfma_f32_16x16x32_bf16 a[204:207], v[156:159], v[116:119], a[204:207]\n" \
"v_mfma_f32_16x16x32_bf16 a[236:239], v[152:155], v[120:123], a[236:239]\n" \
"v_mfma_f32_16x16x32_bf16 a[236:239], v[156:159], v[124:127], a[236:239]\n" \
"v_mfma_f32_16x16x32_bf16 a[232:235], v[144:147], v[120:123], a[232:235]\n" \
"v_mfma_f32_16x16x32_bf16 a[232:235], v[148:151], v[124:127], a[232:235]\n" \
"v_mfma_f32_16x16x32_bf16 a[228:231], v[136:139], v[120:123], a[228:231]\n" \
"v_mfma_f32_16x16x32_bf16 a[228:231], v[140:143], v[124:127], a[228:231]\n" \
"v_mfma_f32_16x16x32_bf16 a[224:227], v[128:131], v[120:123], a[224:227]\n" \
"s_waitcnt vmcnt(11) lgkmcnt(0)\n" \
"v_mfma_f32_16x16x32_bf16 a[224:227], v[132:135], v[124:127], a[224:227]\n" \
"s_barrier\n" \
"v_mfma_f32_16x16x32_bf16 a[16:19], v[64:67], v[160:163], a[16:19]\n" \
"s_add_u32 m0, s87, 4096\n" \
"ds_read_b64_tr_b16 v[128:129], v50 offset:0\n" \
"v_mfma_f32_16x16x32_bf16 a[16:19], v[68:71], v[164:167], a[16:19]\n" \
"global_load_lds_dwordx4 v32, s[54:55] offset:-4096\n" \
"v_mfma_f32_16x16x32_bf16 a[20:23], v[64:67], v[168:171], a[20:23]\n" \
"ds_read_b64_tr_b16 v[130:131], v50 offset:4096\n" \
"v_mfma_f32_16x16x32_bf16 a[20:23], v[68:71], v[172:175], a[20:23]\n" \
"v_mfma_f32_16x16x32_bf16 a[24:27], v[64:67], v[176:179], a[24:27]\n" \
"ds_read_b64_tr_b16 v[132:133], v50 offset:16384\n" \
"v_mfma_f32_16x16x32_bf16 a[24:27], v[68:71], v[180:183], a[24:27]\n" \
"global_load_lds_dwordx4 v33, s[40:41] offset:-3072\n" \
"v_mfma_f32_16x16x32_bf16 a[28:31], v[64:67], v[184:187], a[28:31]\n" \
"ds_read_b64_tr_b16 v[134:135], v50 offset:20480\n" \
"v_mfma_f32_16x16x32_bf16 a[28:31], v[68:71], v[188:191], a[28:31]\n" \
"v_mfma_f32_16x16x32_bf16 a[60:63], v[72:75], v[184:187], a[60:63]\n" \
"ds_read_b128 v[64:67], v48 offset:0\n" \
"v_mfma_f32_16x16x32_bf16 a[60:63], v[76:79], v[188:191], a[60:63]\n" \
"global_load_lds_dwordx4 v34, s[54:55] offset:-2048\n" \
"v_mfma_f32_16x16x32_bf16 a[56:59], v[72:75], v[176:179], a[56:59]\n" \
"ds_read_b64_tr_b16 v[136:137], v51 offset:0\n" \
"v_mfma_f32_16x16x32_bf16 a[56:59], v[76:79], v[180:183], a[56:59]\n" \
"v_mfma_f32_16x16x32_bf16 a[52:55], v[72:75], v[168:171], a[52:55]\n" \
"ds_read_b128 v[68:71], v49 offset:0\n" \
"v_mfma_f32_16x16x32_bf16 a[52:55], v[76:79], v[172:175], a[52:55]\n" \
"global_load_lds_dwordx4 v35, s[40:41] offset:-1024\n" \
"v_mfma_f32_16x16x32_bf16 a[48:51], v[72:75], v[160:163], a[48:51]\n" \
"ds_read_b64_tr_b16 v[138:139], v51 offset:4096\n" \
"v_mfma_f32_16x16x32_bf16 a[48:51], v[76:79], v[164:167], a[48:51]\n" \
"v_mfma_f32_16x16x32_bf16 a[80:83], v[80:83], v[160:163], a[80:83]\n" \
"ds_read_b128 v[72:75], v48 offset:2048\n" \
"v_mfma_f32_16x16x32_bf16 a[80:83], v[84:87], v[164:167], a[80:83]\n" \
"global_load_lds_dwordx4 v36, s[54:55] offset:0\n" \
"v_mfma_f32_16x16x32_bf16 a[84:87], v[80:83], v[168:171], a[84:87]\n" \
"ds_read_b64_tr_b16 v[140:141], v51 offset:16384\n" \
"v_mfma_f32_16x16x32_bf16 a[84:87], v[84:87], v[172:175], a[84:87]\n" \
"v_mfma_f32_16x16x32_bf16 a[88:91], v[80:83], v[176:179], a[88:91]\n" \
"ds_read_b128 v[76:79], v49 offset:2048\n" \
"v_mfma_f32_16x16x32_bf16 a[88:91], v[84:87], v[180:183], a[88:91]\n" \
"global_load_lds_dwordx4 v37, s[40:41] offset:1024\n" \
"v_mfma_f32_16x16x32_bf16 a[92:95], v[80:83], v[184:187], a[92:95]\n" \
"ds_read_b64_tr_b16 v[142:143], v51 offset:20480\n" \
"v_mfma_f32_16x16x32_bf16 a[92:95], v[84:87], v[188:191], a[92:95]\n" \
"v_mfma_f32_16x16x32_bf16 a[124:127], v[88:91], v[184:187], a[124:127]\n" \
"ds_read_b128 v[80:83], v48 offset:4096\n" \
"v_mfma_f32_16x16x32_bf16 a[124:127], v[92:95], v[188:191], a[124:127]\n" \
"global_load_lds_dwordx4 v38, s[54:55] offset:2048\n" \
"v_mfma_f32_16x16x32_bf16 a[120:123], v[88:91], v[176:179], a[120:123]\n" \
"ds_read_b64_tr_b16 v[144:145], v50 offset:512\n" \
"v_mfma_f32_16x16x32_bf16 a[120:123], v[92:95], v[180:183], a[120:123]\n" \
"v_mfma_f32_16x16x32_bf16 a[116:119], v[88:91], v[168:171], a[116:119]\n" \
"ds_read_b128 v[84:87], v49 offset:4096\n" \
"global_load_lds_dwordx4 v39, s[40:41] offset:3072\n" \
"v_mfma_f32_16x16x32_bf16 a[116:119], v[92:95], v[172:175], a[116:119]\n" \
"s_add_u32 s54, s54, s75\n" \
"s_addc_u32 s55, s55, 0\n" \
"v_mfma_f32_16x16x32_bf16 a[112:115], v[88:91], v[160:163], a[112:115]\n" \
"s_add_u32 s40, s40, s75\n" \
"s_addc_u32 s41, s41, 0\n" \
"ds_read_b64_tr_b16 v[146:147], v50 offset:4608\n" \
"v_mfma_f32_16x16x32_bf16 a[112:115], v[92:95], v[164:167], a[112:115]\n" \
"s_add_u32 s68, s66, 0x200000\n" \
"s_add_u32 s64, s8, s68\n" \
"s_addc_u32 s65, s9, 0\n" \
"v_mfma_f32_16x16x32_bf16 a[144:147], v[96:99], v[160:163], a[144:147]\n" \
"ds_read_b128 v[88:91], v48 offset:6144\n" \
"v_mfma_f32_16x16x32_bf16 a[144:147], v[100:103], v[164:167], a[144:147]\n" \
"global_load_dword v253, v248, s[64:65]\n" \
"v_mfma_f32_16x16x32_bf16 a[148:151], v[96:99], v[168:171], a[148:151]\n" \
"s_add_u32 s68, s67, 0x0\n" \
"ds_read_b64_tr_b16 v[148:149], v50 offset:16896\n" \
"v_mfma_f32_16x16x32_bf16 a[148:151], v[100:103], v[172:175], a[148:151]\n" \
"s_add_u32 s64, s12, s68\n" \
"Lpg1151_%=:\n" \
"Lst0x1_%=:\n" \
"s_cbranch_execz Lpg3309_%=\n" \
"s_addc_u32 s65, s13, 0\n" \
"v_mfma_f32_16x16x32_bf16 a[152:155], v[96:99], v[176:179], a[152:155]\n" \
"ds_read_b128 v[92:95], v49 offset:6144\n" \
"v_mfma_f32_16x16x32_bf16 a[152:155], v[100:103], v[180:183], a[152:155]\n" \
"global_load_dword v253, v248, s[64:65]\n" \
"v_mfma_f32_16x16x32_bf16 a[156:159], v[96:99], v[184:187], a[156:159]\n" \
"ds_read_b64_tr_b16 v[150:151], v50 offset:20992\n" \
"v_mfma_f32_16x16x32_bf16 a[156:159], v[100:103], v[188:191], a[156:159]\n" \
"v_mfma_f32_16x16x32_bf16 a[188:191], v[104:107], v[184:187], a[188:191]\n" \
"ds_read_b128 v[96:99], v48 offset:8192\n" \
"v_mfma_f32_16x16x32_bf16 a[188:191], v[108:111], v[188:191], a[188:191]\n" \
"v_mfma_f32_16x16x32_bf16 a[184:187], v[104:107], v[176:179], a[184:187]\n" \
"ds_read_b64_tr_b16 v[152:153], v51 offset:512\n" \
"v_mfma_f32_16x16x32_bf16 a[184:187], v[108:111], v[180:183], a[184:187]\n" \
"v_mfma_f32_16x16x32_bf16 a[180:183], v[104:107], v[168:171], a[180:183]\n" \
"ds_read_b128 v[100:103], v49 offset:8192\n" \
"v_mfma_f32_16x16x32_bf16 a[180:183], v[108:111], v[172:175], a[180:183]\n" \
"v_mfma_f32_16x16x32_bf16 a[176:179], v[104:107], v[160:163], a[176:179]\n" \
"ds_read_b64_tr_b16 v[154:155], v51 offset:4608\n" \
"v_mfma_f32_16x16x32_bf16 a[176:179], v[108:111], v[164:167], a[176:179]\n" \
"v_mfma_f32_16x16x32_bf16 a[208:211], v[112:115], v[160:163], a[208:211]\n" \
"ds_read_b128 v[104:107], v48 offset:10240\n" \
"v_mfma_f32_16x16x32_bf16 a[208:211], v[116:119], v[164:167], a[208:211]\n" \
"v_mfma_f32_16x16x32_bf16 a[212:215], v[112:115], v[168:171], a[212:215]\n" \
"ds_read_b64_tr_b16 v[156:157], v51 offset:16896\n" \
"v_mfma_f32_16x16x32_bf16 a[212:215], v[116:119], v[172:175], a[212:215]\n" \
"v_mfma_f32_16x16x32_bf16 a[216:219], v[112:115], v[176:179], a[216:219]\n" \
"ds_read_b128 v[108:111], v49 offset:10240\n" \
"v_mfma_f32_16x16x32_bf16 a[216:219], v[116:119], v[180:183], a[216:219]\n" \
"v_mfma_f32_16x16x32_bf16 a[220:223], v[112:115], v[184:187], a[220:223]\n" \
"ds_read_b64_tr_b16 v[158:159], v51 offset:20992\n" \
"v_mfma_f32_16x16x32_bf16 a[220:223], v[116:119], v[188:191], a[220:223]\n" \
"v_mfma_f32_16x16x32_bf16 a[252:255], v[120:123], v[184:187], a[252:255]\n" \
"ds_read_b128 v[112:115], v48 offset:12288\n" \
"v_mfma_f32_16x16x32_bf16 a[252:255], v[124:127], v[188:191], a[252:255]\n" \
"v_mfma_f32_16x16x32_bf16 a[248:251], v[120:123], v[176:179], a[248:251]\n" \
"v_mfma_f32_16x16x32_bf16 a[248:251], v[124:127], v[180:183], a[248:251]\n" \
"v_mfma_f32_16x16x32_bf16 a[244:247], v[120:123], v[168:171], a[244:247]\n" \
"ds_read_b128 v[116:119], v49 offset:12288\n" \
"v_mfma_f32_16x16x32_bf16 a[244:247], v[124:127], v[172:175], a[244:247]\n" \
"v_mfma_f32_16x16x32_bf16 a[240:243], v[120:123], v[160:163], a[240:243]\n" \
"v_mfma_f32_16x16x32_bf16 a[240:243], v[124:127], v[164:167], a[240:243]\n" \
"s_waitcnt lgkmcnt(1)\n" \
"v_mfma_f32_16x16x32_bf16 a[0:3], v[128:131], v[64:67], a[0:3]\n" \
"ds_read_b128 v[120:123], v48 offset:14336\n" \
"v_mfma_f32_16x16x32_bf16 a[0:3], v[132:135], v[68:71], a[0:3]\n" \
"v_mfma_f32_16x16x32_bf16 a[4:7], v[136:139], v[64:67], a[4:7]\n" \
"ds_read_b128 v[124:127], v49 offset:14336\n" \
"v_mfma_f32_16x16x32_bf16 a[4:7], v[140:143], v[68:71], a[4:7]\n" \
"s_add_u32 s68, s66, 0x300000\n" \
"v_mfma_f32_16x16x32_bf16 a[8:11], v[144:147], v[64:67], a[8:11]\n" \
"s_add_u32 s64, s8, s68\n" \
"s_addc_u32 s65, s9, 0\n" \
"ds_read_b64_tr_b16 v[160:161], v50 offset:1024\n" \
"v_mfma_f32_16x16x32_bf16 a[8:11], v[148:151], v[68:71], a[8:11]\n" \
"global_load_dword v253, v248, s[64:65]\n" \
"v_mfma_f32_16x16x32_bf16 a[12:15], v[152:155], v[64:67], a[12:15]\n" \
"s_add_u32 m0, s87, 135168\n" \
"ds_read_b64_tr_b16 v[162:163], v50 offset:5120\n" \
"v_mfma_f32_16x16x32_bf16 a[12:15], v[156:159], v[68:71], a[12:15]\n" \
"global_load_lds_dwordx4 v40, s[52:53] offset:-4096\n" \
"v_mfma_f32_16x16x32_bf16 a[44:47], v[152:155], v[72:75], a[44:47]\n" \
"ds_read_b64_tr_b16 v[164:165], v50 offset:17408\n" \
"v_mfma_f32_16x16x32_bf16 a[44:47], v[156:159], v[76:79], a[44:47]\n" \
"v_mfma_f32_16x16x32_bf16 a[40:43], v[144:147], v[72:75], a[40:43]\n" \
"ds_read_b64_tr_b16 v[166:167], v50 offset:21504\n" \
"v_mfma_f32_16x16x32_bf16 a[40:43], v[148:151], v[76:79], a[40:43]\n" \
"global_load_lds_dwordx4 v41, s[50:51] offset:-3072\n" \
"v_mfma_f32_16x16x32_bf16 a[36:39], v[136:139], v[72:75], a[36:39]\n" \
"ds_read_b64_tr_b16 v[168:169], v51 offset:1024\n" \
"v_mfma_f32_16x16x32_bf16 a[36:39], v[140:143], v[76:79], a[36:39]\n" \
"v_mfma_f32_16x16x32_bf16 a[32:35], v[128:131], v[72:75], a[32:35]\n" \
"ds_read_b64_tr_b16 v[170:171], v51 offset:5120\n" \
"v_mfma_f32_16x16x32_bf16 a[32:35], v[132:135], v[76:79], a[32:35]\n" \
"global_load_lds_dwordx4 v42, s[52:53] offset:-2048\n" \
"v_mfma_f32_16x16x32_bf16 a[64:67], v[128:131], v[80:83], a[64:67]\n" \
"ds_read_b64_tr_b16 v[172:173], v51 offset:17408\n" \
"v_mfma_f32_16x16x32_bf16 a[64:67], v[132:135], v[84:87], a[64:67]\n" \
"v_mfma_f32_16x16x32_bf16 a[68:71], v[136:139], v[80:83], a[68:71]\n" \
"ds_read_b64_tr_b16 v[174:175], v51 offset:21504\n" \
"v_mfma_f32_16x16x32_bf16 a[68:71], v[140:143], v[84:87], a[68:71]\n" \
"global_load_lds_dwordx4 v43, s[50:51] offset:-1024\n" \
"v_mfma_f32_16x16x32_bf16 a[72:75], v[144:147], v[80:83], a[72:75]\n" \
"ds_read_b64_tr_b16 v[176:177], v50 offset:1536\n" \
"v_mfma_f32_16x16x32_bf16 a[72:75], v[148:151], v[84:87], a[72:75]\n" \
"v_mfma_f32_16x16x32_bf16 a[76:79], v[152:155], v[80:83], a[76:79]\n" \
"ds_read_b64_tr_b16 v[178:179], v50 offset:5632\n" \
"v_mfma_f32_16x16x32_bf16 a[76:79], v[156:159], v[84:87], a[76:79]\n" \
"global_load_lds_dwordx4 v44, s[52:53] offset:0\n" \
"v_mfma_f32_16x16x32_bf16 a[108:111], v[152:155], v[88:91], a[108:111]\n" \
"ds_read_b64_tr_b16 v[180:181], v50 offset:17920\n" \
"v_mfma_f32_16x16x32_bf16 a[108:111], v[156:159], v[92:95], a[108:111]\n" \
"v_mfma_f32_16x16x32_bf16 a[104:107], v[144:147], v[88:91], a[104:107]\n" \
"ds_read_b64_tr_b16 v[182:183], v50 offset:22016\n" \
"v_mfma_f32_16x16x32_bf16 a[104:107], v[148:151], v[92:95], a[104:107]\n" \
"global_load_lds_dwordx4 v45, s[50:51] offset:1024\n" \
"v_mfma_f32_16x16x32_bf16 a[100:103], v[136:139], v[88:91], a[100:103]\n" \
"ds_read_b64_tr_b16 v[184:185], v51 offset:1536\n" \
"v_mfma_f32_16x16x32_bf16 a[100:103], v[140:143], v[92:95], a[100:103]\n" \
"v_mfma_f32_16x16x32_bf16 a[96:99], v[128:131], v[88:91], a[96:99]\n" \
"ds_read_b64_tr_b16 v[186:187], v51 offset:5632\n" \
"v_mfma_f32_16x16x32_bf16 a[96:99], v[132:135], v[92:95], a[96:99]\n" \
"global_load_lds_dwordx4 v46, s[52:53] offset:2048\n" \
"v_mfma_f32_16x16x32_bf16 a[128:131], v[128:131], v[96:99], a[128:131]\n" \
"ds_read_b64_tr_b16 v[188:189], v51 offset:17920\n" \
"v_mfma_f32_16x16x32_bf16 a[128:131], v[132:135], v[100:103], a[128:131]\n" \
"v_mfma_f32_16x16x32_bf16 a[132:135], v[136:139], v[96:99], a[132:135]\n" \
"ds_read_b64_tr_b16 v[190:191], v51 offset:22016\n" \
"v_mfma_f32_16x16x32_bf16 a[132:135], v[140:143], v[100:103], a[132:135]\n" \
"global_load_lds_dwordx4 v47, s[50:51] offset:3072\n" \
"v_mfma_f32_16x16x32_bf16 a[136:139], v[144:147], v[96:99], a[136:139]\n" \
"s_add_u32 s52, s52, s74\n" \
"s_addc_u32 s53, s53, 0\n" \
"v_mfma_f32_16x16x32_bf16 a[136:139], v[148:151], v[100:103], a[136:139]\n" \
"s_add_u32 s50, s50, s74\n" \
"s_addc_u32 s51, s51, 0\n" \
"v_mfma_f32_16x16x32_bf16 a[140:143], v[152:155], v[96:99], a[140:143]\n" \
"v_mfma_f32_16x16x32_bf16 a[140:143], v[156:159], v[100:103], a[140:143]\n" \
"v_mfma_f32_16x16x32_bf16 a[172:175], v[152:155], v[104:107], a[172:175]\n" \
"s_mul_i32 s76, s73, 0x10000\n" \
"s_add_u32 s77, s76, 0xfffe8000\n" \
"s_sub_u32 s78, 0xffff8000, s76\n" \
"v_mfma_f32_16x16x32_bf16 a[172:175], v[156:159], v[108:111], a[172:175]\n" \
"v_add_u32 v48, s77, v48\n" \
"v_add_u32 v49, s77, v49\n" \
"v_mfma_f32_16x16x32_bf16 a[168:171], v[144:147], v[104:107], a[168:171]\n" \
"v_add_u32 v50, s78, v50\n" \
"v_add_u32 v51, s78, v51\n" \
"v_mfma_f32_16x16x32_bf16 a[168:171], v[148:151], v[108:111], a[168:171]\n" \
"v_mfma_f32_16x16x32_bf16 a[164:167], v[136:139], v[104:107], a[164:167]\n" \
"v_mfma_f32_16x16x32_bf16 a[164:167], v[140:143], v[108:111], a[164:167]\n" \
"v_mfma_f32_16x16x32_bf16 a[160:163], v[128:131], v[104:107], a[160:163]\n" \
"v_mfma_f32_16x16x32_bf16 a[160:163], v[132:135], v[108:111], a[160:163]\n" \
"s_waitcnt lgkmcnt(0)\n" \
"v_mfma_f32_16x16x32_bf16 a[192:195], v[128:131], v[112:115], a[192:195]\n" \
"v_mfma_f32_16x16x32_bf16 a[192:195], v[132:135], v[116:119], a[192:195]\n" \
"v_mfma_f32_16x16x32_bf16 a[196:199], v[136:139], v[112:115], a[196:199]\n" \
"v_mfma_f32_16x16x32_bf16 a[196:199], v[140:143], v[116:119], a[196:199]\n" \
"v_mfma_f32_16x16x32_bf16 a[200:203], v[144:147], v[112:115], a[200:203]\n" \
"v_mfma_f32_16x16x32_bf16 a[200:203], v[148:151], v[116:119], a[200:203]\n" \
"v_mfma_f32_16x16x32_bf16 a[204:207], v[152:155], v[112:115], a[204:207]\n" \
"v_mfma_f32_16x16x32_bf16 a[204:207], v[156:159], v[116:119], a[204:207]\n" \
"v_mfma_f32_16x16x32_bf16 a[236:239], v[152:155], v[120:123], a[236:239]\n" \
"v_mfma_f32_16x16x32_bf16 a[236:239], v[156:159], v[124:127], a[236:239]\n" \
"v_mfma_f32_16x16x32_bf16 a[232:235], v[144:147], v[120:123], a[232:235]\n" \
"v_mfma_f32_16x16x32_bf16 a[232:235], v[148:151], v[124:127], a[232:235]\n" \
"v_mfma_f32_16x16x32_bf16 a[228:231], v[136:139], v[120:123], a[228:231]\n" \
"v_mfma_f32_16x16x32_bf16 a[228:231], v[140:143], v[124:127], a[228:231]\n" \
"v_mfma_f32_16x16x32_bf16 a[224:227], v[128:131], v[120:123], a[224:227]\n" \
"s_waitcnt vmcnt(11) lgkmcnt(0)\n" \
"v_mfma_f32_16x16x32_bf16 a[224:227], v[132:135], v[124:127], a[224:227]\n" \
"s_barrier\n" \
"v_mfma_f32_16x16x32_bf16 a[16:19], v[64:67], v[160:163], a[16:19]\n" \
"s_add_u32 m0, s87, 69632\n" \
"ds_read_b64_tr_b16 v[128:129], v50 offset:0\n" \
"v_mfma_f32_16x16x32_bf16 a[16:19], v[68:71], v[164:167], a[16:19]\n" \
"global_load_lds_dwordx4 v32, s[54:55] offset:-4096\n" \
"v_mfma_f32_16x16x32_bf16 a[20:23], v[64:67], v[168:171], a[20:23]\n" \
"ds_read_b64_tr_b16 v[130:131], v50 offset:4096\n" \
"v_mfma_f32_16x16x32_bf16 a[20:23], v[68:71], v[172:175], a[20:23]\n" \
"v_mfma_f32_16x16x32_bf16 a[24:27], v[64:67], v[176:179], a[24:27]\n" \
"ds_read_b64_tr_b16 v[132:133], v50 offset:16384\n" \
"v_mfma_f32_16x16x32_bf16 a[24:27], v[68:71], v[180:183], a[24:27]\n" \
"global_load_lds_dwordx4 v33, s[40:41] offset:-3072\n" \
"v_mfma_f32_16x16x32_bf16 a[28:31], v[64:67], v[184:187], a[28:31]\n" \
"ds_read_b64_tr_b16 v[134:135], v50 offset:20480\n" \
"v_mfma_f32_16x16x32_bf16 a[28:31], v[68:71], v[188:191], a[28:31]\n" \
"v_mfma_f32_16x16x32_bf16 a[60:63], v[72:75], v[184:187], a[60:63]\n" \
"ds_read_b128 v[64:67], v48 offset:0\n" \
"v_mfma_f32_16x16x32_bf16 a[60:63], v[76:79], v[188:191], a[60:63]\n" \
"global_load_lds_dwordx4 v34, s[54:55] offset:-2048\n" \
"v_mfma_f32_16x16x32_bf16 a[56:59], v[72:75], v[176:179], a[56:59]\n" \
"ds_read_b64_tr_b16 v[136:137], v51 offset:0\n" \
"v_mfma_f32_16x16x32_bf16 a[56:59], v[76:79], v[180:183], a[56:59]\n" \
"v_mfma_f32_16x16x32_bf16 a[52:55], v[72:75], v[168:171], a[52:55]\n" \
"ds_read_b128 v[68:71], v49 offset:0\n" \
"v_mfma_f32_16x16x32_bf16 a[52:55], v[76:79], v[172:175], a[52:55]\n" \
"global_load_lds_dwordx4 v35, s[40:41] offset:-1024\n" \
"v_mfma_f32_16x16x32_bf16 a[48:51], v[72:75], v[160:163], a[48:51]\n" \
"ds_read_b64_tr_b16 v[138:139], v51 offset:4096\n" \
"v_mfma_f32_16x16x32_bf16 a[48:51], v[76:79], v[164:167], a[48:51]\n" \
"v_mfma_f32_16x16x32_bf16 a[80:83], v[80:83], v[160:163], a[80:83]\n" \
"ds_read_b128 v[72:75], v48 offset:2048\n" \
"v_mfma_f32_16x16x32_bf16 a[80:83], v[84:87], v[164:167], a[80:83]\n" \
"global_load_lds_dwordx4 v36, s[54:55] offset:0\n" \
"v_mfma_f32_16x16x32_bf16 a[84:87], v[80:83], v[168:171], a[84:87]\n" \
"ds_read_b64_tr_b16 v[140:141], v51 offset:16384\n" \
"v_mfma_f32_16x16x32_bf16 a[84:87], v[84:87], v[172:175], a[84:87]\n" \
"v_mfma_f32_16x16x32_bf16 a[88:91], v[80:83], v[176:179], a[88:91]\n" \
"ds_read_b128 v[76:79], v49 offset:2048\n" \
"v_mfma_f32_16x16x32_bf16 a[88:91], v[84:87], v[180:183], a[88:91]\n" \
"global_load_lds_dwordx4 v37, s[40:41] offset:1024\n" \
"v_mfma_f32_16x16x32_bf16 a[92:95], v[80:83], v[184:187], a[92:95]\n" \
"ds_read_b64_tr_b16 v[142:143], v51 offset:20480\n" \
"v_mfma_f32_16x16x32_bf16 a[92:95], v[84:87], v[188:191], a[92:95]\n" \
"v_mfma_f32_16x16x32_bf16 a[124:127], v[88:91], v[184:187], a[124:127]\n" \
"ds_read_b128 v[80:83], v48 offset:4096\n" \
"v_mfma_f32_16x16x32_bf16 a[124:127], v[92:95], v[188:191], a[124:127]\n" \
"global_load_lds_dwordx4 v38, s[54:55] offset:2048\n" \
"v_mfma_f32_16x16x32_bf16 a[120:123], v[88:91], v[176:179], a[120:123]\n" \
"ds_read_b64_tr_b16 v[144:145], v50 offset:512\n" \
"v_mfma_f32_16x16x32_bf16 a[120:123], v[92:95], v[180:183], a[120:123]\n" \
"v_mfma_f32_16x16x32_bf16 a[116:119], v[88:91], v[168:171], a[116:119]\n" \
"ds_read_b128 v[84:87], v49 offset:4096\n" \
"global_load_lds_dwordx4 v39, s[40:41] offset:3072\n" \
"v_mfma_f32_16x16x32_bf16 a[116:119], v[92:95], v[172:175], a[116:119]\n" \
"s_add_u32 s54, s54, s75\n" \
"s_addc_u32 s55, s55, 0\n" \
"v_mfma_f32_16x16x32_bf16 a[112:115], v[88:91], v[160:163], a[112:115]\n" \
"s_add_u32 s40, s40, s75\n" \
"s_addc_u32 s41, s41, 0\n" \
"ds_read_b64_tr_b16 v[146:147], v50 offset:4608\n" \
"v_mfma_f32_16x16x32_bf16 a[112:115], v[92:95], v[164:167], a[112:115]\n" \
"s_add_u32 s68, s67, 0x100000\n" \
"s_add_u32 s64, s12, s68\n" \
"s_addc_u32 s65, s13, 0\n" \
"v_mfma_f32_16x16x32_bf16 a[144:147], v[96:99], v[160:163], a[144:147]\n" \
"ds_read_b128 v[88:91], v48 offset:6144\n" \
"v_mfma_f32_16x16x32_bf16 a[144:147], v[100:103], v[164:167], a[144:147]\n" \
"global_load_dword v253, v248, s[64:65]\n" \
"v_mfma_f32_16x16x32_bf16 a[148:151], v[96:99], v[168:171], a[148:151]\n" \
"s_add_u32 s68, s67, 0x500000\n" \
"ds_read_b64_tr_b16 v[148:149], v50 offset:16896\n" \
"v_mfma_f32_16x16x32_bf16 a[148:151], v[100:103], v[172:175], a[148:151]\n" \
"s_add_u32 s64, s12, s68\n" \
"s_addc_u32 s65, s13, 0\n" \
"v_mfma_f32_16x16x32_bf16 a[152:155], v[96:99], v[176:179], a[152:155]\n" \
"ds_read_b128 v[92:95], v49 offset:6144\n" \
"v_mfma_f32_16x16x32_bf16 a[152:155], v[100:103], v[180:183], a[152:155]\n" \
"global_load_dword v253, v248, s[64:65]\n" \
"v_mfma_f32_16x16x32_bf16 a[156:159], v[96:99], v[184:187], a[156:159]\n" \
"ds_read_b64_tr_b16 v[150:151], v50 offset:20992\n" \
"v_mfma_f32_16x16x32_bf16 a[156:159], v[100:103], v[188:191], a[156:159]\n" \
"v_mfma_f32_16x16x32_bf16 a[188:191], v[104:107], v[184:187], a[188:191]\n" \
"ds_read_b128 v[96:99], v48 offset:8192\n" \
"v_mfma_f32_16x16x32_bf16 a[188:191], v[108:111], v[188:191], a[188:191]\n" \
"v_mfma_f32_16x16x32_bf16 a[184:187], v[104:107], v[176:179], a[184:187]\n" \
"ds_read_b64_tr_b16 v[152:153], v51 offset:512\n" \
"v_mfma_f32_16x16x32_bf16 a[184:187], v[108:111], v[180:183], a[184:187]\n" \
"v_mfma_f32_16x16x32_bf16 a[180:183], v[104:107], v[168:171], a[180:183]\n" \
"ds_read_b128 v[100:103], v49 offset:8192\n" \
"v_mfma_f32_16x16x32_bf16 a[180:183], v[108:111], v[172:175], a[180:183]\n" \
"v_mfma_f32_16x16x32_bf16 a[176:179], v[104:107], v[160:163], a[176:179]\n" \
"ds_read_b64_tr_b16 v[154:155], v51 offset:4608\n" \
"v_mfma_f32_16x16x32_bf16 a[176:179], v[108:111], v[164:167], a[176:179]\n" \
"v_mfma_f32_16x16x32_bf16 a[208:211], v[112:115], v[160:163], a[208:211]\n" \
"ds_read_b128 v[104:107], v48 offset:10240\n" \
"v_mfma_f32_16x16x32_bf16 a[208:211], v[116:119], v[164:167], a[208:211]\n" \
"v_mfma_f32_16x16x32_bf16 a[212:215], v[112:115], v[168:171], a[212:215]\n" \
"ds_read_b64_tr_b16 v[156:157], v51 offset:16896\n" \
"v_mfma_f32_16x16x32_bf16 a[212:215], v[116:119], v[172:175], a[212:215]\n" \
"v_mfma_f32_16x16x32_bf16 a[216:219], v[112:115], v[176:179], a[216:219]\n" \
"ds_read_b128 v[108:111], v49 offset:10240\n" \
"v_mfma_f32_16x16x32_bf16 a[216:219], v[116:119], v[180:183], a[216:219]\n" \
"v_mfma_f32_16x16x32_bf16 a[220:223], v[112:115], v[184:187], a[220:223]\n" \
"ds_read_b64_tr_b16 v[158:159], v51 offset:20992\n" \
"v_mfma_f32_16x16x32_bf16 a[220:223], v[116:119], v[188:191], a[220:223]\n" \
"v_mfma_f32_16x16x32_bf16 a[252:255], v[120:123], v[184:187], a[252:255]\n" \
"ds_read_b128 v[112:115], v48 offset:12288\n" \
"v_mfma_f32_16x16x32_bf16 a[252:255], v[124:127], v[188:191], a[252:255]\n" \
"v_mfma_f32_16x16x32_bf16 a[248:251], v[120:123], v[176:179], a[248:251]\n" \
"v_mfma_f32_16x16x32_bf16 a[248:251], v[124:127], v[180:183], a[248:251]\n" \
"v_mfma_f32_16x16x32_bf16 a[244:247], v[120:123], v[168:171], a[244:247]\n" \
"ds_read_b128 v[116:119], v49 offset:12288\n" \
"v_mfma_f32_16x16x32_bf16 a[244:247], v[124:127], v[172:175], a[244:247]\n" \
"v_mfma_f32_16x16x32_bf16 a[240:243], v[120:123], v[160:163], a[240:243]\n" \
"v_mfma_f32_16x16x32_bf16 a[240:243], v[124:127], v[164:167], a[240:243]\n" \
"s_waitcnt lgkmcnt(1)\n" \
"v_mfma_f32_16x16x32_bf16 a[0:3], v[128:131], v[64:67], a[0:3]\n" \
"ds_read_b128 v[120:123], v48 offset:14336\n" \
"v_mfma_f32_16x16x32_bf16 a[0:3], v[132:135], v[68:71], a[0:3]\n" \
"v_mfma_f32_16x16x32_bf16 a[4:7], v[136:139], v[64:67], a[4:7]\n" \
"ds_read_b128 v[124:127], v49 offset:14336\n" \
"v_mfma_f32_16x16x32_bf16 a[4:7], v[140:143], v[68:71], a[4:7]\n" \
"s_add_u32 s68, s67, 0x200000\n" \
"v_mfma_f32_16x16x32_bf16 a[8:11], v[144:147], v[64:67], a[8:11]\n" \
"s_add_u32 s64, s12, s68\n" \
"s_addc_u32 s65, s13, 0\n" \
"ds_read_b64_tr_b16 v[160:161], v50 offset:1024\n" \
"v_mfma_f32_16x16x32_bf16 a[8:11], v[148:151], v[68:71], a[8:11]\n" \
"global_load_dword v253, v248, s[64:65]\n" \
"v_mfma_f32_16x16x32_bf16 a[12:15], v[152:155], v[64:67], a[12:15]\n" \
"s_add_u32 m0, s87, 102400\n" \
"ds_read_b64_tr_b16 v[162:163], v50 offset:5120\n" \
"v_mfma_f32_16x16x32_bf16 a[12:15], v[156:159], v[68:71], a[12:15]\n" \
"global_load_lds_dwordx4 v40, s[52:53] offset:-4096\n" \
"v_mfma_f32_16x16x32_bf16 a[44:47], v[152:155], v[72:75], a[44:47]\n" \
"ds_read_b64_tr_b16 v[164:165], v50 offset:17408\n" \
"v_mfma_f32_16x16x32_bf16 a[44:47], v[156:159], v[76:79], a[44:47]\n" \
"v_mfma_f32_16x16x32_bf16 a[40:43], v[144:147], v[72:75], a[40:43]\n" \
"ds_read_b64_tr_b16 v[166:167], v50 offset:21504\n" \
"v_mfma_f32_16x16x32_bf16 a[40:43], v[148:151], v[76:79], a[40:43]\n" \
"global_load_lds_dwordx4 v41, s[50:51] offset:-3072\n" \
"v_mfma_f32_16x16x32_bf16 a[36:39], v[136:139], v[72:75], a[36:39]\n" \
"ds_read_b64_tr_b16 v[168:169], v51 offset:1024\n" \
"v_mfma_f32_16x16x32_bf16 a[36:39], v[140:143], v[76:79], a[36:39]\n" \
"v_mfma_f32_16x16x32_bf16 a[32:35], v[128:131], v[72:75], a[32:35]\n" \
"ds_read_b64_tr_b16 v[170:171], v51 offset:5120\n" \
"v_mfma_f32_16x16x32_bf16 a[32:35], v[132:135], v[76:79], a[32:35]\n" \
"global_load_lds_dwordx4 v42, s[52:53] offset:-2048\n" \
"v_mfma_f32_16x16x32_bf16 a[64:67], v[128:131], v[80:83], a[64:67]\n" \
"ds_read_b64_tr_b16 v[172:173], v51 offset:17408\n" \
"v_mfma_f32_16x16x32_bf16 a[64:67], v[132:135], v[84:87], a[64:67]\n" \
"v_mfma_f32_16x16x32_bf16 a[68:71], v[136:139], v[80:83], a[68:71]\n" \
"ds_read_b64_tr_b16 v[174:175], v51 offset:21504\n" \
"v_mfma_f32_16x16x32_bf16 a[68:71], v[140:143], v[84:87], a[68:71]\n" \
"global_load_lds_dwordx4 v43, s[50:51] offset:-1024\n" \
"v_mfma_f32_16x16x32_bf16 a[72:75], v[144:147], v[80:83], a[72:75]\n" \
"ds_read_b64_tr_b16 v[176:177], v50 offset:1536\n" \
"v_mfma_f32_16x16x32_bf16 a[72:75], v[148:151], v[84:87], a[72:75]\n" \
"v_mfma_f32_16x16x32_bf16 a[76:79], v[152:155], v[80:83], a[76:79]\n" \
"ds_read_b64_tr_b16 v[178:179], v50 offset:5632\n" \
"v_mfma_f32_16x16x32_bf16 a[76:79], v[156:159], v[84:87], a[76:79]\n" \
"global_load_lds_dwordx4 v44, s[52:53] offset:0\n" \
"v_mfma_f32_16x16x32_bf16 a[108:111], v[152:155], v[88:91], a[108:111]\n" \
"ds_read_b64_tr_b16 v[180:181], v50 offset:17920\n" \
"v_mfma_f32_16x16x32_bf16 a[108:111], v[156:159], v[92:95], a[108:111]\n" \
"v_mfma_f32_16x16x32_bf16 a[104:107], v[144:147], v[88:91], a[104:107]\n" \
"ds_read_b64_tr_b16 v[182:183], v50 offset:22016\n" \
"v_mfma_f32_16x16x32_bf16 a[104:107], v[148:151], v[92:95], a[104:107]\n" \
"global_load_lds_dwordx4 v45, s[50:51] offset:1024\n" \
"v_mfma_f32_16x16x32_bf16 a[100:103], v[136:139], v[88:91], a[100:103]\n" \
"ds_read_b64_tr_b16 v[184:185], v51 offset:1536\n" \
"v_mfma_f32_16x16x32_bf16 a[100:103], v[140:143], v[92:95], a[100:103]\n" \
"v_mfma_f32_16x16x32_bf16 a[96:99], v[128:131], v[88:91], a[96:99]\n" \
"ds_read_b64_tr_b16 v[186:187], v51 offset:5632\n" \
"v_mfma_f32_16x16x32_bf16 a[96:99], v[132:135], v[92:95], a[96:99]\n" \
"global_load_lds_dwordx4 v46, s[52:53] offset:2048\n" \
"v_mfma_f32_16x16x32_bf16 a[128:131], v[128:131], v[96:99], a[128:131]\n" \
"ds_read_b64_tr_b16 v[188:189], v51 offset:17920\n" \
"v_mfma_f32_16x16x32_bf16 a[128:131], v[132:135], v[100:103], a[128:131]\n" \
"v_mfma_f32_16x16x32_bf16 a[132:135], v[136:139], v[96:99], a[132:135]\n" \
"ds_read_b64_tr_b16 v[190:191], v51 offset:22016\n" \
"v_mfma_f32_16x16x32_bf16 a[132:135], v[140:143], v[100:103], a[132:135]\n" \
"global_load_lds_dwordx4 v47, s[50:51] offset:3072\n" \
"v_mfma_f32_16x16x32_bf16 a[136:139], v[144:147], v[96:99], a[136:139]\n" \
"v_mfma_f32_16x16x32_bf16 a[136:139], v[148:151], v[100:103], a[136:139]\n" \
"v_mfma_f32_16x16x32_bf16 a[140:143], v[152:155], v[96:99], a[140:143]\n" \
"v_mfma_f32_16x16x32_bf16 a[140:143], v[156:159], v[100:103], a[140:143]\n" \
"v_mfma_f32_16x16x32_bf16 a[172:175], v[152:155], v[104:107], a[172:175]\n" \
"s_mul_i32 s76, s73, 0x8000\n" \
"s_add_u32 s77, s76, 0x10000\n" \
"s_sub_u32 s78, 0x18000, s76\n" \
"v_mfma_f32_16x16x32_bf16 a[172:175], v[156:159], v[108:111], a[172:175]\n" \
"v_add_u32 v48, s77, v48\n" \
"v_add_u32 v49, s77, v49\n" \
"v_mfma_f32_16x16x32_bf16 a[168:171], v[144:147], v[104:107], a[168:171]\n" \
"v_add_u32 v50, s78, v50\n" \
"v_add_u32 v51, s78, v51\n" \
"v_mfma_f32_16x16x32_bf16 a[168:171], v[148:151], v[108:111], a[168:171]\n" \
"v_mfma_f32_16x16x32_bf16 a[164:167], v[136:139], v[104:107], a[164:167]\n" \
"v_mfma_f32_16x16x32_bf16 a[164:167], v[140:143], v[108:111], a[164:167]\n" \
"v_mfma_f32_16x16x32_bf16 a[160:163], v[128:131], v[104:107], a[160:163]\n" \
"v_mfma_f32_16x16x32_bf16 a[160:163], v[132:135], v[108:111], a[160:163]\n" \
"s_waitcnt lgkmcnt(0)\n" \
"v_mfma_f32_16x16x32_bf16 a[192:195], v[128:131], v[112:115], a[192:195]\n" \
"v_mfma_f32_16x16x32_bf16 a[192:195], v[132:135], v[116:119], a[192:195]\n" \
"v_mfma_f32_16x16x32_bf16 a[196:199], v[136:139], v[112:115], a[196:199]\n" \
"v_mfma_f32_16x16x32_bf16 a[196:199], v[140:143], v[116:119], a[196:199]\n" \
"v_mfma_f32_16x16x32_bf16 a[200:203], v[144:147], v[112:115], a[200:203]\n" \
"v_mfma_f32_16x16x32_bf16 a[200:203], v[148:151], v[116:119], a[200:203]\n" \
"v_mfma_f32_16x16x32_bf16 a[204:207], v[152:155], v[112:115], a[204:207]\n" \
"v_mfma_f32_16x16x32_bf16 a[204:207], v[156:159], v[116:119], a[204:207]\n" \
"v_mfma_f32_16x16x32_bf16 a[236:239], v[152:155], v[120:123], a[236:239]\n" \
"v_mfma_f32_16x16x32_bf16 a[236:239], v[156:159], v[124:127], a[236:239]\n" \
"v_mfma_f32_16x16x32_bf16 a[232:235], v[144:147], v[120:123], a[232:235]\n" \
"v_mfma_f32_16x16x32_bf16 a[232:235], v[148:151], v[124:127], a[232:235]\n" \
"v_mfma_f32_16x16x32_bf16 a[228:231], v[136:139], v[120:123], a[228:231]\n" \
"v_mfma_f32_16x16x32_bf16 a[228:231], v[140:143], v[124:127], a[228:231]\n" \
"v_mfma_f32_16x16x32_bf16 a[224:227], v[128:131], v[120:123], a[224:227]\n" \
"s_waitcnt vmcnt(11) lgkmcnt(0)\n" \
"v_mfma_f32_16x16x32_bf16 a[224:227], v[132:135], v[124:127], a[224:227]\n" \
"s_barrier\n" \
"v_mfma_f32_16x16x32_bf16 a[16:19], v[64:67], v[160:163], a[16:19]\n" \
"s_add_u32 m0, s87, 36864\n" \
"ds_read_b64_tr_b16 v[128:129], v50 offset:0\n" \
"v_mfma_f32_16x16x32_bf16 a[16:19], v[68:71], v[164:167], a[16:19]\n" \
"global_load_lds_dwordx4 v32, s[54:55] offset:-4096\n" \
"v_mfma_f32_16x16x32_bf16 a[20:23], v[64:67], v[168:171], a[20:23]\n" \
"ds_read_b64_tr_b16 v[130:131], v50 offset:4096\n" \
"v_mfma_f32_16x16x32_bf16 a[20:23], v[68:71], v[172:175], a[20:23]\n" \
"v_mfma_f32_16x16x32_bf16 a[24:27], v[64:67], v[176:179], a[24:27]\n" \
"ds_read_b64_tr_b16 v[132:133], v50 offset:16384\n" \
"v_mfma_f32_16x16x32_bf16 a[24:27], v[68:71], v[180:183], a[24:27]\n" \
"global_load_lds_dwordx4 v33, s[40:41] offset:-3072\n" \
"v_mfma_f32_16x16x32_bf16 a[28:31], v[64:67], v[184:187], a[28:31]\n" \
"ds_read_b64_tr_b16 v[134:135], v50 offset:20480\n" \
"v_mfma_f32_16x16x32_bf16 a[28:31], v[68:71], v[188:191], a[28:31]\n" \
"v_mfma_f32_16x16x32_bf16 a[60:63], v[72:75], v[184:187], a[60:63]\n" \
"ds_read_b128 v[64:67], v48 offset:0\n" \
"v_mfma_f32_16x16x32_bf16 a[60:63], v[76:79], v[188:191], a[60:63]\n" \
"global_load_lds_dwordx4 v34, s[54:55] offset:-2048\n" \
"v_mfma_f32_16x16x32_bf16 a[56:59], v[72:75], v[176:179], a[56:59]\n" \
"ds_read_b64_tr_b16 v[136:137], v51 offset:0\n" \
"v_mfma_f32_16x16x32_bf16 a[56:59], v[76:79], v[180:183], a[56:59]\n" \
"v_mfma_f32_16x16x32_bf16 a[52:55], v[72:75], v[168:171], a[52:55]\n" \
"ds_read_b128 v[68:71], v49 offset:0\n" \
"v_mfma_f32_16x16x32_bf16 a[52:55], v[76:79], v[172:175], a[52:55]\n" \
"global_load_lds_dwordx4 v35, s[40:41] offset:-1024\n" \
"v_mfma_f32_16x16x32_bf16 a[48:51], v[72:75], v[160:163], a[48:51]\n" \
"ds_read_b64_tr_b16 v[138:139], v51 offset:4096\n" \
"v_mfma_f32_16x16x32_bf16 a[48:51], v[76:79], v[164:167], a[48:51]\n" \
"v_mfma_f32_16x16x32_bf16 a[80:83], v[80:83], v[160:163], a[80:83]\n" \
"ds_read_b128 v[72:75], v48 offset:2048\n" \
"v_mfma_f32_16x16x32_bf16 a[80:83], v[84:87], v[164:167], a[80:83]\n" \
"global_load_lds_dwordx4 v36, s[54:55] offset:0\n" \
"v_mfma_f32_16x16x32_bf16 a[84:87], v[80:83], v[168:171], a[84:87]\n" \
"ds_read_b64_tr_b16 v[140:141], v51 offset:16384\n" \
"v_mfma_f32_16x16x32_bf16 a[84:87], v[84:87], v[172:175], a[84:87]\n" \
"v_mfma_f32_16x16x32_bf16 a[88:91], v[80:83], v[176:179], a[88:91]\n" \
"ds_read_b128 v[76:79], v49 offset:2048\n" \
"v_mfma_f32_16x16x32_bf16 a[88:91], v[84:87], v[180:183], a[88:91]\n" \
"global_load_lds_dwordx4 v37, s[40:41] offset:1024\n" \
"v_mfma_f32_16x16x32_bf16 a[92:95], v[80:83], v[184:187], a[92:95]\n" \
"ds_read_b64_tr_b16 v[142:143], v51 offset:20480\n" \
"v_mfma_f32_16x16x32_bf16 a[92:95], v[84:87], v[188:191], a[92:95]\n" \
"v_mfma_f32_16x16x32_bf16 a[124:127], v[88:91], v[184:187], a[124:127]\n" \
"ds_read_b128 v[80:83], v48 offset:4096\n" \
"v_mfma_f32_16x16x32_bf16 a[124:127], v[92:95], v[188:191], a[124:127]\n" \
"global_load_lds_dwordx4 v38, s[54:55] offset:2048\n" \
"v_mfma_f32_16x16x32_bf16 a[120:123], v[88:91], v[176:179], a[120:123]\n" \
"ds_read_b64_tr_b16 v[144:145], v50 offset:512\n" \
"v_mfma_f32_16x16x32_bf16 a[120:123], v[92:95], v[180:183], a[120:123]\n" \
"v_mfma_f32_16x16x32_bf16 a[116:119], v[88:91], v[168:171], a[116:119]\n" \
"ds_read_b128 v[84:87], v49 offset:4096\n" \
"v_mfma_f32_16x16x32_bf16 a[116:119], v[92:95], v[172:175], a[116:119]\n" \
"global_load_lds_dwordx4 v39, s[40:41] offset:3072\n" \
"s_add_u32 s68, s67, 0x300000\n" \
"v_mfma_f32_16x16x32_bf16 a[112:115], v[88:91], v[160:163], a[112:115]\n" \
"s_add_u32 s64, s12, s68\n" \
"s_addc_u32 s65, s13, 0\n" \
"ds_read_b64_tr_b16 v[146:147], v50 offset:4608\n" \
"v_mfma_f32_16x16x32_bf16 a[112:115], v[92:95], v[164:167], a[112:115]\n" \
"v_mfma_f32_16x16x32_bf16 a[144:147], v[96:99], v[160:163], a[144:147]\n" \
"ds_read_b128 v[88:91], v48 offset:6144\n" \
"v_mfma_f32_16x16x32_bf16 a[144:147], v[100:103], v[164:167], a[144:147]\n" \
"global_load_dword v253, v248, s[64:65]\n" \
"v_mfma_f32_16x16x32_bf16 a[148:151], v[96:99], v[168:171], a[148:151]\n" \
"s_add_u32 s68, s67, 0x400000\n" \
"ds_read_b64_tr_b16 v[148:149], v50 offset:16896\n" \
"v_mfma_f32_16x16x32_bf16 a[148:151], v[100:103], v[172:175], a[148:151]\n" \
"s_add_u32 s64, s12, s68\n" \
"s_addc_u32 s65, s13, 0\n" \
"v_mfma_f32_16x16x32_bf16 a[152:155], v[96:99], v[176:179], a[152:155]\n" \
"ds_read_b128 v[92:95], v49 offset:6144\n" \
"v_mfma_f32_16x16x32_bf16 a[152:155], v[100:103], v[180:183], a[152:155]\n" \
"global_load_dword v253, v248, s[64:65]\n" \
"v_mfma_f32_16x16x32_bf16 a[156:159], v[96:99], v[184:187], a[156:159]\n" \
"ds_read_b64_tr_b16 v[150:151], v50 offset:20992\n" \
"v_mfma_f32_16x16x32_bf16 a[156:159], v[100:103], v[188:191], a[156:159]\n" \
"v_mfma_f32_16x16x32_bf16 a[188:191], v[104:107], v[184:187], a[188:191]\n" \
"ds_read_b128 v[96:99], v48 offset:8192\n" \
"v_mfma_f32_16x16x32_bf16 a[188:191], v[108:111], v[188:191], a[188:191]\n" \
"v_mfma_f32_16x16x32_bf16 a[184:187], v[104:107], v[176:179], a[184:187]\n" \
"ds_read_b64_tr_b16 v[152:153], v51 offset:512\n" \
"v_mfma_f32_16x16x32_bf16 a[184:187], v[108:111], v[180:183], a[184:187]\n" \
"v_mfma_f32_16x16x32_bf16 a[180:183], v[104:107], v[168:171], a[180:183]\n" \
"ds_read_b128 v[100:103], v49 offset:8192\n" \
"v_mfma_f32_16x16x32_bf16 a[180:183], v[108:111], v[172:175], a[180:183]\n" \
"v_mfma_f32_16x16x32_bf16 a[176:179], v[104:107], v[160:163], a[176:179]\n" \
"ds_read_b64_tr_b16 v[154:155], v51 offset:4608\n" \
"v_mfma_f32_16x16x32_bf16 a[176:179], v[108:111], v[164:167], a[176:179]\n" \
"v_mfma_f32_16x16x32_bf16 a[208:211], v[112:115], v[160:163], a[208:211]\n" \
"ds_read_b128 v[104:107], v48 offset:10240\n" \
"v_mfma_f32_16x16x32_bf16 a[208:211], v[116:119], v[164:167], a[208:211]\n" \
"v_mfma_f32_16x16x32_bf16 a[212:215], v[112:115], v[168:171], a[212:215]\n" \
"ds_read_b64_tr_b16 v[156:157], v51 offset:16896\n" \
"v_mfma_f32_16x16x32_bf16 a[212:215], v[116:119], v[172:175], a[212:215]\n" \
"v_mfma_f32_16x16x32_bf16 a[216:219], v[112:115], v[176:179], a[216:219]\n" \
"ds_read_b128 v[108:111], v49 offset:10240\n" \
"v_mfma_f32_16x16x32_bf16 a[216:219], v[116:119], v[180:183], a[216:219]\n" \
"v_mfma_f32_16x16x32_bf16 a[220:223], v[112:115], v[184:187], a[220:223]\n" \
"ds_read_b64_tr_b16 v[158:159], v51 offset:20992\n" \
"v_mfma_f32_16x16x32_bf16 a[220:223], v[116:119], v[188:191], a[220:223]\n" \
"v_mfma_f32_16x16x32_bf16 a[252:255], v[120:123], v[184:187], a[252:255]\n" \
"ds_read_b128 v[112:115], v48 offset:12288\n" \
"v_mfma_f32_16x16x32_bf16 a[252:255], v[124:127], v[188:191], a[252:255]\n" \
"v_mfma_f32_16x16x32_bf16 a[248:251], v[120:123], v[176:179], a[248:251]\n" \
"v_mfma_f32_16x16x32_bf16 a[248:251], v[124:127], v[180:183], a[248:251]\n" \
"v_mfma_f32_16x16x32_bf16 a[244:247], v[120:123], v[168:171], a[244:247]\n" \
"ds_read_b128 v[116:119], v49 offset:12288\n" \
"v_mfma_f32_16x16x32_bf16 a[244:247], v[124:127], v[172:175], a[244:247]\n" \
"v_mfma_f32_16x16x32_bf16 a[240:243], v[120:123], v[160:163], a[240:243]\n" \
"v_mfma_f32_16x16x32_bf16 a[240:243], v[124:127], v[164:167], a[240:243]\n" \
"s_waitcnt lgkmcnt(1)\n" \
"v_mfma_f32_16x16x32_bf16 a[0:3], v[128:131], v[64:67], a[0:3]\n" \
"ds_read_b128 v[120:123], v48 offset:14336\n" \
"v_mfma_f32_16x16x32_bf16 a[0:3], v[132:135], v[68:71], a[0:3]\n" \
"v_mfma_f32_16x16x32_bf16 a[4:7], v[136:139], v[64:67], a[4:7]\n" \
"ds_read_b128 v[124:127], v49 offset:14336\n" \
"s_add_u32 m0, s95, 4096\n" \
"v_mfma_f32_16x16x32_bf16 a[4:7], v[140:143], v[68:71], a[4:7]\n" \
"global_load_lds_dwordx4 v228, s[44:45] offset:-4096\n" \
"v_mfma_f32_16x16x32_bf16 a[8:11], v[144:147], v[64:67], a[8:11]\n" \
"ds_read_b64_tr_b16 v[160:161], v50 offset:1024\n" \
"v_mfma_f32_16x16x32_bf16 a[8:11], v[148:151], v[68:71], a[8:11]\n" \
"v_mfma_f32_16x16x32_bf16 a[12:15], v[152:155], v[64:67], a[12:15]\n" \
"ds_read_b64_tr_b16 v[162:163], v50 offset:5120\n" \
"v_mfma_f32_16x16x32_bf16 a[12:15], v[156:159], v[68:71], a[12:15]\n" \
"global_load_lds_dwordx4 v229, s[44:45] offset:-3072\n" \
"v_mfma_f32_16x16x32_bf16 a[44:47], v[152:155], v[72:75], a[44:47]\n" \
"s_add_u32 s44, s44, 0x180000\n" \
"s_addc_u32 s45, s45, 0\n" \
"ds_read_b64_tr_b16 v[164:165], v50 offset:17408\n" \
"v_mfma_f32_16x16x32_bf16 a[44:47], v[156:159], v[76:79], a[44:47]\n" \
"v_mfma_f32_16x16x32_bf16 a[40:43], v[144:147], v[72:75], a[40:43]\n" \
"ds_read_b64_tr_b16 v[166:167], v50 offset:21504\n" \
"v_mfma_f32_16x16x32_bf16 a[40:43], v[148:151], v[76:79], a[40:43]\n" \
"global_load_lds_dwordx4 v228, s[42:43] offset:-2048\n" \
"v_mfma_f32_16x16x32_bf16 a[36:39], v[136:139], v[72:75], a[36:39]\n" \
"ds_read_b64_tr_b16 v[168:169], v51 offset:1024\n" \
"v_mfma_f32_16x16x32_bf16 a[36:39], v[140:143], v[76:79], a[36:39]\n" \
"v_mfma_f32_16x16x32_bf16 a[32:35], v[128:131], v[72:75], a[32:35]\n" \
"ds_read_b64_tr_b16 v[170:171], v51 offset:5120\n" \
"v_mfma_f32_16x16x32_bf16 a[32:35], v[132:135], v[76:79], a[32:35]\n" \
"global_load_lds_dwordx4 v229, s[42:43] offset:-1024\n" \
"v_mfma_f32_16x16x32_bf16 a[64:67], v[128:131], v[80:83], a[64:67]\n" \
"s_add_u32 s42, s42, 0x180000\n" \
"s_addc_u32 s43, s43, 0\n" \
"ds_read_b64_tr_b16 v[172:173], v51 offset:17408\n" \
"v_mfma_f32_16x16x32_bf16 a[64:67], v[132:135], v[84:87], a[64:67]\n" \
"v_mfma_f32_16x16x32_bf16 a[68:71], v[136:139], v[80:83], a[68:71]\n" \
"s_add_u32 m0, s95, 20480\n" \
"ds_read_b64_tr_b16 v[174:175], v51 offset:21504\n" \
"v_mfma_f32_16x16x32_bf16 a[68:71], v[140:143], v[84:87], a[68:71]\n" \
"global_load_lds_dwordx4 v228, s[44:45] offset:-4096\n" \
"v_mfma_f32_16x16x32_bf16 a[72:75], v[144:147], v[80:83], a[72:75]\n" \
"ds_read_b64_tr_b16 v[176:177], v50 offset:1536\n" \
"v_mfma_f32_16x16x32_bf16 a[72:75], v[148:151], v[84:87], a[72:75]\n" \
"v_mfma_f32_16x16x32_bf16 a[76:79], v[152:155], v[80:83], a[76:79]\n" \
"ds_read_b64_tr_b16 v[178:179], v50 offset:5632\n" \
"v_mfma_f32_16x16x32_bf16 a[76:79], v[156:159], v[84:87], a[76:79]\n" \
"Lpg1684_%=:\n" \
"Lst0x2_%=:\n" \
"s_cbranch_execz Lpg3850_%=\n" \
"global_load_lds_dwordx4 v229, s[44:45] offset:-3072\n" \
"v_mfma_f32_16x16x32_bf16 a[108:111], v[152:155], v[88:91], a[108:111]\n" \
"s_add_u32 s44, s44, 0x400000\n" \
"s_addc_u32 s45, s45, 0\n" \
"ds_read_b64_tr_b16 v[180:181], v50 offset:17920\n" \
"v_mfma_f32_16x16x32_bf16 a[108:111], v[156:159], v[92:95], a[108:111]\n" \
"v_mfma_f32_16x16x32_bf16 a[104:107], v[144:147], v[88:91], a[104:107]\n" \
"ds_read_b64_tr_b16 v[182:183], v50 offset:22016\n" \
"v_mfma_f32_16x16x32_bf16 a[104:107], v[148:151], v[92:95], a[104:107]\n" \
"global_load_lds_dwordx4 v228, s[42:43] offset:-2048\n" \
"v_mfma_f32_16x16x32_bf16 a[100:103], v[136:139], v[88:91], a[100:103]\n" \
"ds_read_b64_tr_b16 v[184:185], v51 offset:1536\n" \
"v_mfma_f32_16x16x32_bf16 a[100:103], v[140:143], v[92:95], a[100:103]\n" \
"v_mfma_f32_16x16x32_bf16 a[96:99], v[128:131], v[88:91], a[96:99]\n" \
"ds_read_b64_tr_b16 v[186:187], v51 offset:5632\n" \
"v_mfma_f32_16x16x32_bf16 a[96:99], v[132:135], v[92:95], a[96:99]\n" \
"global_load_lds_dwordx4 v229, s[42:43] offset:-1024\n" \
"v_mfma_f32_16x16x32_bf16 a[128:131], v[128:131], v[96:99], a[128:131]\n" \
"s_add_u32 s42, s42, 0x400000\n" \
"s_addc_u32 s43, s43, 0\n" \
"ds_read_b64_tr_b16 v[188:189], v51 offset:17920\n" \
"v_mfma_f32_16x16x32_bf16 a[128:131], v[132:135], v[100:103], a[128:131]\n" \
"v_mfma_f32_16x16x32_bf16 a[132:135], v[136:139], v[96:99], a[132:135]\n" \
"ds_read_b64_tr_b16 v[190:191], v51 offset:22016\n" \
"v_mfma_f32_16x16x32_bf16 a[132:135], v[140:143], v[100:103], a[132:135]\n" \
"v_mfma_f32_16x16x32_bf16 a[136:139], v[144:147], v[96:99], a[136:139]\n" \
"v_mfma_f32_16x16x32_bf16 a[136:139], v[148:151], v[100:103], a[136:139]\n" \
"v_mfma_f32_16x16x32_bf16 a[140:143], v[152:155], v[96:99], a[140:143]\n" \
"v_mfma_f32_16x16x32_bf16 a[140:143], v[156:159], v[100:103], a[140:143]\n" \
"v_mfma_f32_16x16x32_bf16 a[172:175], v[152:155], v[104:107], a[172:175]\n" \
"s_mul_i32 s76, s73, 0x0\n" \
"s_add_u32 s77, s76, 0xffff8000\n" \
"s_sub_u32 s78, 0xffff8000, s76\n" \
"v_mfma_f32_16x16x32_bf16 a[172:175], v[156:159], v[108:111], a[172:175]\n" \
"v_add_u32 v48, s77, v48\n" \
"v_add_u32 v49, s77, v49\n" \
"v_mfma_f32_16x16x32_bf16 a[168:171], v[144:147], v[104:107], a[168:171]\n" \
"v_add_u32 v50, s78, v50\n" \
"v_add_u32 v51, s78, v51\n" \
"v_mfma_f32_16x16x32_bf16 a[168:171], v[148:151], v[108:111], a[168:171]\n" \
"v_mfma_f32_16x16x32_bf16 a[164:167], v[136:139], v[104:107], a[164:167]\n" \
"v_mfma_f32_16x16x32_bf16 a[164:167], v[140:143], v[108:111], a[164:167]\n" \
"v_mfma_f32_16x16x32_bf16 a[160:163], v[128:131], v[104:107], a[160:163]\n" \
"v_mfma_f32_16x16x32_bf16 a[160:163], v[132:135], v[108:111], a[160:163]\n" \
"s_waitcnt lgkmcnt(0)\n" \
"v_mfma_f32_16x16x32_bf16 a[192:195], v[128:131], v[112:115], a[192:195]\n" \
"v_mfma_f32_16x16x32_bf16 a[192:195], v[132:135], v[116:119], a[192:195]\n" \
"v_mfma_f32_16x16x32_bf16 a[196:199], v[136:139], v[112:115], a[196:199]\n" \
"v_mfma_f32_16x16x32_bf16 a[196:199], v[140:143], v[116:119], a[196:199]\n" \
"v_mfma_f32_16x16x32_bf16 a[200:203], v[144:147], v[112:115], a[200:203]\n" \
"v_mfma_f32_16x16x32_bf16 a[200:203], v[148:151], v[116:119], a[200:203]\n" \
"v_mfma_f32_16x16x32_bf16 a[204:207], v[152:155], v[112:115], a[204:207]\n" \
"v_mfma_f32_16x16x32_bf16 a[204:207], v[156:159], v[116:119], a[204:207]\n" \
"v_mfma_f32_16x16x32_bf16 a[236:239], v[152:155], v[120:123], a[236:239]\n" \
"v_mfma_f32_16x16x32_bf16 a[236:239], v[156:159], v[124:127], a[236:239]\n" \
"v_mfma_f32_16x16x32_bf16 a[232:235], v[144:147], v[120:123], a[232:235]\n" \
"v_mfma_f32_16x16x32_bf16 a[232:235], v[148:151], v[124:127], a[232:235]\n" \
"v_mfma_f32_16x16x32_bf16 a[228:231], v[136:139], v[120:123], a[228:231]\n" \
"v_mfma_f32_16x16x32_bf16 a[228:231], v[140:143], v[124:127], a[228:231]\n" \
"v_mfma_f32_16x16x32_bf16 a[224:227], v[128:131], v[120:123], a[224:227]\n" \
"s_waitcnt vmcnt(10) lgkmcnt(0)\n" \
"v_mfma_f32_16x16x32_bf16 a[224:227], v[132:135], v[124:127], a[224:227]\n" \
"s_barrier\n" \
"v_mfma_f32_16x16x32_bf16 a[16:19], v[64:67], v[160:163], a[16:19]\n" \
"s_add_u32 m0, s87, 135168\n" \
"ds_read_b64_tr_b16 v[128:129], v50 offset:0\n" \
"v_mfma_f32_16x16x32_bf16 a[16:19], v[68:71], v[164:167], a[16:19]\n" \
"global_load_lds_dwordx4 v224, s[48:49] offset:-4096\n" \
"v_mfma_f32_16x16x32_bf16 a[20:23], v[64:67], v[168:171], a[20:23]\n" \
"ds_read_b64_tr_b16 v[130:131], v50 offset:4096\n" \
"v_mfma_f32_16x16x32_bf16 a[20:23], v[68:71], v[172:175], a[20:23]\n" \
"v_mfma_f32_16x16x32_bf16 a[24:27], v[64:67], v[176:179], a[24:27]\n" \
"ds_read_b64_tr_b16 v[132:133], v50 offset:16384\n" \
"v_mfma_f32_16x16x32_bf16 a[24:27], v[68:71], v[180:183], a[24:27]\n" \
"global_load_lds_dwordx4 v225, s[48:49] offset:-3072\n" \
"v_mfma_f32_16x16x32_bf16 a[28:31], v[64:67], v[184:187], a[28:31]\n" \
"ds_read_b64_tr_b16 v[134:135], v50 offset:20480\n" \
"v_mfma_f32_16x16x32_bf16 a[28:31], v[68:71], v[188:191], a[28:31]\n" \
"v_mfma_f32_16x16x32_bf16 a[60:63], v[72:75], v[184:187], a[60:63]\n" \
"ds_read_b128 v[64:67], v48 offset:0\n" \
"v_mfma_f32_16x16x32_bf16 a[60:63], v[76:79], v[188:191], a[60:63]\n" \
"global_load_lds_dwordx4 v226, s[48:49] offset:-2048\n" \
"v_mfma_f32_16x16x32_bf16 a[56:59], v[72:75], v[176:179], a[56:59]\n" \
"ds_read_b64_tr_b16 v[136:137], v51 offset:0\n" \
"v_mfma_f32_16x16x32_bf16 a[56:59], v[76:79], v[180:183], a[56:59]\n" \
"v_mfma_f32_16x16x32_bf16 a[52:55], v[72:75], v[168:171], a[52:55]\n" \
"ds_read_b128 v[68:71], v49 offset:0\n" \
"v_mfma_f32_16x16x32_bf16 a[52:55], v[76:79], v[172:175], a[52:55]\n" \
"global_load_lds_dwordx4 v227, s[48:49] offset:-1024\n" \
"v_mfma_f32_16x16x32_bf16 a[48:51], v[72:75], v[160:163], a[48:51]\n" \
"s_add_u32 s48, s48, 0x180\n" \
"s_addc_u32 s49, s49, 0\n" \
"ds_read_b64_tr_b16 v[138:139], v51 offset:4096\n" \
"v_mfma_f32_16x16x32_bf16 a[48:51], v[76:79], v[164:167], a[48:51]\n" \
"v_mfma_f32_16x16x32_bf16 a[80:83], v[80:83], v[160:163], a[80:83]\n" \
"ds_read_b128 v[72:75], v48 offset:2048\n" \
"v_mfma_f32_16x16x32_bf16 a[80:83], v[84:87], v[164:167], a[80:83]\n" \
"global_load_lds_dwordx4 v224, s[46:47] offset:0\n" \
"v_mfma_f32_16x16x32_bf16 a[84:87], v[80:83], v[168:171], a[84:87]\n" \
"ds_read_b64_tr_b16 v[140:141], v51 offset:16384\n" \
"v_mfma_f32_16x16x32_bf16 a[84:87], v[84:87], v[172:175], a[84:87]\n" \
"v_mfma_f32_16x16x32_bf16 a[88:91], v[80:83], v[176:179], a[88:91]\n" \
"ds_read_b128 v[76:79], v49 offset:2048\n" \
"v_mfma_f32_16x16x32_bf16 a[88:91], v[84:87], v[180:183], a[88:91]\n" \
"global_load_lds_dwordx4 v225, s[46:47] offset:1024\n" \
"v_mfma_f32_16x16x32_bf16 a[92:95], v[80:83], v[184:187], a[92:95]\n" \
"ds_read_b64_tr_b16 v[142:143], v51 offset:20480\n" \
"v_mfma_f32_16x16x32_bf16 a[92:95], v[84:87], v[188:191], a[92:95]\n" \
"v_mfma_f32_16x16x32_bf16 a[124:127], v[88:91], v[184:187], a[124:127]\n" \
"ds_read_b128 v[80:83], v48 offset:4096\n" \
"v_mfma_f32_16x16x32_bf16 a[124:127], v[92:95], v[188:191], a[124:127]\n" \
"global_load_lds_dwordx4 v226, s[46:47] offset:2048\n" \
"v_mfma_f32_16x16x32_bf16 a[120:123], v[88:91], v[176:179], a[120:123]\n" \
"ds_read_b64_tr_b16 v[144:145], v50 offset:512\n" \
"v_mfma_f32_16x16x32_bf16 a[120:123], v[92:95], v[180:183], a[120:123]\n" \
"v_mfma_f32_16x16x32_bf16 a[116:119], v[88:91], v[168:171], a[116:119]\n" \
"ds_read_b128 v[84:87], v49 offset:4096\n" \
"v_mfma_f32_16x16x32_bf16 a[116:119], v[92:95], v[172:175], a[116:119]\n" \
"global_load_lds_dwordx4 v227, s[46:47] offset:3072\n" \
"v_mfma_f32_16x16x32_bf16 a[112:115], v[88:91], v[160:163], a[112:115]\n" \
"s_add_u32 s46, s46, 0x180\n" \
"s_addc_u32 s47, s47, 0\n" \
"ds_read_b64_tr_b16 v[146:147], v50 offset:4608\n" \
"v_mfma_f32_16x16x32_bf16 a[112:115], v[92:95], v[164:167], a[112:115]\n" \
"s_add_u32 s68, s67, 0x600000\n" \
"s_add_u32 s64, s12, s68\n" \
"s_addc_u32 s65, s13, 0\n" \
"v_mfma_f32_16x16x32_bf16 a[144:147], v[96:99], v[160:163], a[144:147]\n" \
"ds_read_b128 v[88:91], v48 offset:6144\n" \
"v_mfma_f32_16x16x32_bf16 a[144:147], v[100:103], v[164:167], a[144:147]\n" \
"global_load_dword v253, v248, s[64:65]\n" \
"v_mfma_f32_16x16x32_bf16 a[148:151], v[96:99], v[168:171], a[148:151]\n" \
"s_add_u32 s68, s67, 0x700000\n" \
"ds_read_b64_tr_b16 v[148:149], v50 offset:16896\n" \
"v_mfma_f32_16x16x32_bf16 a[148:151], v[100:103], v[172:175], a[148:151]\n" \
"s_add_u32 s64, s12, s68\n" \
"s_addc_u32 s65, s13, 0\n" \
"v_mfma_f32_16x16x32_bf16 a[152:155], v[96:99], v[176:179], a[152:155]\n" \
"ds_read_b128 v[92:95], v49 offset:6144\n" \
"v_mfma_f32_16x16x32_bf16 a[152:155], v[100:103], v[180:183], a[152:155]\n" \
"global_load_dword v253, v248, s[64:65]\n" \
"v_mfma_f32_16x16x32_bf16 a[156:159], v[96:99], v[184:187], a[156:159]\n" \
"s_lshr_b32 s68, s66, 2\n" \
"s_and_b32 s68, s68, 0xffff\n" \
"ds_read_b64_tr_b16 v[150:151], v50 offset:20992\n" \
"v_mfma_f32_16x16x32_bf16 a[156:159], v[100:103], v[188:191], a[156:159]\n" \
"s_add_u32 s68, s68, 65585152\n" \
"v_mfma_f32_16x16x32_bf16 a[188:191], v[104:107], v[184:187], a[188:191]\n" \
"ds_read_b128 v[96:99], v48 offset:8192\n" \
"v_mfma_f32_16x16x32_bf16 a[188:191], v[108:111], v[188:191], a[188:191]\n" \
"buffer_load_dword v253, v245, s[56:59], s68 offen sc1\n" \
"v_mfma_f32_16x16x32_bf16 a[184:187], v[104:107], v[176:179], a[184:187]\n" \
"ds_read_b64_tr_b16 v[152:153], v51 offset:512\n" \
"v_mfma_f32_16x16x32_bf16 a[184:187], v[108:111], v[180:183], a[184:187]\n" \
"v_mfma_f32_16x16x32_bf16 a[180:183], v[104:107], v[168:171], a[180:183]\n" \
"ds_read_b128 v[100:103], v49 offset:8192\n" \
"v_mfma_f32_16x16x32_bf16 a[180:183], v[108:111], v[172:175], a[180:183]\n" \
"v_mfma_f32_16x16x32_bf16 a[176:179], v[104:107], v[160:163], a[176:179]\n" \
"ds_read_b64_tr_b16 v[154:155], v51 offset:4608\n" \
"v_mfma_f32_16x16x32_bf16 a[176:179], v[108:111], v[164:167], a[176:179]\n" \
"v_mfma_f32_16x16x32_bf16 a[208:211], v[112:115], v[160:163], a[208:211]\n" \
"ds_read_b128 v[104:107], v48 offset:10240\n" \
"v_mfma_f32_16x16x32_bf16 a[208:211], v[116:119], v[164:167], a[208:211]\n" \
"v_mfma_f32_16x16x32_bf16 a[212:215], v[112:115], v[168:171], a[212:215]\n" \
"ds_read_b64_tr_b16 v[156:157], v51 offset:16896\n" \
"v_mfma_f32_16x16x32_bf16 a[212:215], v[116:119], v[172:175], a[212:215]\n" \
"v_mfma_f32_16x16x32_bf16 a[216:219], v[112:115], v[176:179], a[216:219]\n" \
"ds_read_b128 v[108:111], v49 offset:10240\n" \
"v_mfma_f32_16x16x32_bf16 a[216:219], v[116:119], v[180:183], a[216:219]\n" \
"v_mfma_f32_16x16x32_bf16 a[220:223], v[112:115], v[184:187], a[220:223]\n" \
"ds_read_b64_tr_b16 v[158:159], v51 offset:20992\n" \
"v_mfma_f32_16x16x32_bf16 a[220:223], v[116:119], v[188:191], a[220:223]\n" \
"v_mfma_f32_16x16x32_bf16 a[252:255], v[120:123], v[184:187], a[252:255]\n" \
"ds_read_b128 v[112:115], v48 offset:12288\n" \
"v_mfma_f32_16x16x32_bf16 a[252:255], v[124:127], v[188:191], a[252:255]\n" \
"v_mfma_f32_16x16x32_bf16 a[248:251], v[120:123], v[176:179], a[248:251]\n" \
"v_mfma_f32_16x16x32_bf16 a[248:251], v[124:127], v[180:183], a[248:251]\n" \
"v_mfma_f32_16x16x32_bf16 a[244:247], v[120:123], v[168:171], a[244:247]\n" \
"ds_read_b128 v[116:119], v49 offset:12288\n" \
"v_mfma_f32_16x16x32_bf16 a[244:247], v[124:127], v[172:175], a[244:247]\n" \
"v_mfma_f32_16x16x32_bf16 a[240:243], v[120:123], v[160:163], a[240:243]\n" \
"v_mfma_f32_16x16x32_bf16 a[240:243], v[124:127], v[164:167], a[240:243]\n" \
"s_waitcnt lgkmcnt(1)\n" \
"v_mfma_f32_16x16x32_bf16 a[0:3], v[128:131], v[64:67], a[0:3]\n" \
"ds_read_b128 v[120:123], v48 offset:14336\n" \
"v_mfma_f32_16x16x32_bf16 a[0:3], v[132:135], v[68:71], a[0:3]\n" \
"v_mfma_f32_16x16x32_bf16 a[4:7], v[136:139], v[64:67], a[4:7]\n" \
"ds_read_b128 v[124:127], v49 offset:14336\n" \
"s_add_u32 m0, s87, 69632\n" \
"v_mfma_f32_16x16x32_bf16 a[4:7], v[140:143], v[68:71], a[4:7]\n" \
"global_load_lds_dwordx4 v224, s[48:49] offset:-4096\n" \
"v_mfma_f32_16x16x32_bf16 a[8:11], v[144:147], v[64:67], a[8:11]\n" \
"ds_read_b64_tr_b16 v[160:161], v50 offset:1024\n" \
"v_mfma_f32_16x16x32_bf16 a[8:11], v[148:151], v[68:71], a[8:11]\n" \
"v_mfma_f32_16x16x32_bf16 a[12:15], v[152:155], v[64:67], a[12:15]\n" \
"ds_read_b64_tr_b16 v[162:163], v50 offset:5120\n" \
"v_mfma_f32_16x16x32_bf16 a[12:15], v[156:159], v[68:71], a[12:15]\n" \
"global_load_lds_dwordx4 v225, s[48:49] offset:-3072\n" \
"v_mfma_f32_16x16x32_bf16 a[44:47], v[152:155], v[72:75], a[44:47]\n" \
"ds_read_b64_tr_b16 v[164:165], v50 offset:17408\n" \
"v_mfma_f32_16x16x32_bf16 a[44:47], v[156:159], v[76:79], a[44:47]\n" \
"v_mfma_f32_16x16x32_bf16 a[40:43], v[144:147], v[72:75], a[40:43]\n" \
"ds_read_b64_tr_b16 v[166:167], v50 offset:21504\n" \
"v_mfma_f32_16x16x32_bf16 a[40:43], v[148:151], v[76:79], a[40:43]\n" \
"global_load_lds_dwordx4 v226, s[48:49] offset:-2048\n" \
"v_mfma_f32_16x16x32_bf16 a[36:39], v[136:139], v[72:75], a[36:39]\n" \
"ds_read_b64_tr_b16 v[168:169], v51 offset:1024\n" \
"v_mfma_f32_16x16x32_bf16 a[36:39], v[140:143], v[76:79], a[36:39]\n" \
"v_mfma_f32_16x16x32_bf16 a[32:35], v[128:131], v[72:75], a[32:35]\n" \
"ds_read_b64_tr_b16 v[170:171], v51 offset:5120\n" \
"v_mfma_f32_16x16x32_bf16 a[32:35], v[132:135], v[76:79], a[32:35]\n" \
"global_load_lds_dwordx4 v227, s[48:49] offset:-1024\n" \
"v_mfma_f32_16x16x32_bf16 a[64:67], v[128:131], v[80:83], a[64:67]\n" \
"s_add_u32 s48, s48, 0x400\n" \
"s_addc_u32 s49, s49, 0\n" \
"ds_read_b64_tr_b16 v[172:173], v51 offset:17408\n" \
"v_mfma_f32_16x16x32_bf16 a[64:67], v[132:135], v[84:87], a[64:67]\n" \
"v_mfma_f32_16x16x32_bf16 a[68:71], v[136:139], v[80:83], a[68:71]\n" \
"ds_read_b64_tr_b16 v[174:175], v51 offset:21504\n" \
"v_mfma_f32_16x16x32_bf16 a[68:71], v[140:143], v[84:87], a[68:71]\n" \
"global_load_lds_dwordx4 v224, s[46:47] offset:0\n" \
"v_mfma_f32_16x16x32_bf16 a[72:75], v[144:147], v[80:83], a[72:75]\n" \
"ds_read_b64_tr_b16 v[176:177], v50 offset:1536\n" \
"v_mfma_f32_16x16x32_bf16 a[72:75], v[148:151], v[84:87], a[72:75]\n" \
"v_mfma_f32_16x16x32_bf16 a[76:79], v[152:155], v[80:83], a[76:79]\n" \
"ds_read_b64_tr_b16 v[178:179], v50 offset:5632\n" \
"v_mfma_f32_16x16x32_bf16 a[76:79], v[156:159], v[84:87], a[76:79]\n" \
"global_load_lds_dwordx4 v225, s[46:47] offset:1024\n" \
"v_mfma_f32_16x16x32_bf16 a[108:111], v[152:155], v[88:91], a[108:111]\n" \
"ds_read_b64_tr_b16 v[180:181], v50 offset:17920\n" \
"v_mfma_f32_16x16x32_bf16 a[108:111], v[156:159], v[92:95], a[108:111]\n" \
"v_mfma_f32_16x16x32_bf16 a[104:107], v[144:147], v[88:91], a[104:107]\n" \
"ds_read_b64_tr_b16 v[182:183], v50 offset:22016\n" \
"v_mfma_f32_16x16x32_bf16 a[104:107], v[148:151], v[92:95], a[104:107]\n" \
"global_load_lds_dwordx4 v226, s[46:47] offset:2048\n" \
"v_mfma_f32_16x16x32_bf16 a[100:103], v[136:139], v[88:91], a[100:103]\n" \
"ds_read_b64_tr_b16 v[184:185], v51 offset:1536\n" \
"v_mfma_f32_16x16x32_bf16 a[100:103], v[140:143], v[92:95], a[100:103]\n" \
"v_mfma_f32_16x16x32_bf16 a[96:99], v[128:131], v[88:91], a[96:99]\n" \
"ds_read_b64_tr_b16 v[186:187], v51 offset:5632\n" \
"v_mfma_f32_16x16x32_bf16 a[96:99], v[132:135], v[92:95], a[96:99]\n" \
"global_load_lds_dwordx4 v227, s[46:47] offset:3072\n" \
"v_mfma_f32_16x16x32_bf16 a[128:131], v[128:131], v[96:99], a[128:131]\n" \
"s_add_u32 s46, s46, 0x400\n" \
"s_addc_u32 s47, s47, 0\n" \
"ds_read_b64_tr_b16 v[188:189], v51 offset:17920\n" \
"v_mfma_f32_16x16x32_bf16 a[128:131], v[132:135], v[100:103], a[128:131]\n" \
"v_mfma_f32_16x16x32_bf16 a[132:135], v[136:139], v[96:99], a[132:135]\n" \
"ds_read_b64_tr_b16 v[190:191], v51 offset:22016\n" \
"v_mfma_f32_16x16x32_bf16 a[132:135], v[140:143], v[100:103], a[132:135]\n" \
"v_mfma_f32_16x16x32_bf16 a[136:139], v[144:147], v[96:99], a[136:139]\n" \
"v_mfma_f32_16x16x32_bf16 a[136:139], v[148:151], v[100:103], a[136:139]\n" \
"v_mfma_f32_16x16x32_bf16 a[140:143], v[152:155], v[96:99], a[140:143]\n" \
"v_mfma_f32_16x16x32_bf16 a[140:143], v[156:159], v[100:103], a[140:143]\n" \
"v_mfma_f32_16x16x32_bf16 a[172:175], v[152:155], v[104:107], a[172:175]\n" \
"v_mfma_f32_16x16x32_bf16 a[172:175], v[156:159], v[108:111], a[172:175]\n" \
"v_mfma_f32_16x16x32_bf16 a[168:171], v[144:147], v[104:107], a[168:171]\n" \
"v_mfma_f32_16x16x32_bf16 a[168:171], v[148:151], v[108:111], a[168:171]\n" \
"v_mfma_f32_16x16x32_bf16 a[164:167], v[136:139], v[104:107], a[164:167]\n" \
"v_mfma_f32_16x16x32_bf16 a[164:167], v[140:143], v[108:111], a[164:167]\n" \
"v_mfma_f32_16x16x32_bf16 a[160:163], v[128:131], v[104:107], a[160:163]\n" \
"v_mfma_f32_16x16x32_bf16 a[160:163], v[132:135], v[108:111], a[160:163]\n" \
"s_waitcnt lgkmcnt(0)\n" \
"v_mfma_f32_16x16x32_bf16 a[192:195], v[128:131], v[112:115], a[192:195]\n" \
"v_mfma_f32_16x16x32_bf16 a[192:195], v[132:135], v[116:119], a[192:195]\n" \
"v_mfma_f32_16x16x32_bf16 a[196:199], v[136:139], v[112:115], a[196:199]\n" \
"v_mfma_f32_16x16x32_bf16 a[196:199], v[140:143], v[116:119], a[196:199]\n" \
"v_mfma_f32_16x16x32_bf16 a[200:203], v[144:147], v[112:115], a[200:203]\n" \
"v_mfma_f32_16x16x32_bf16 a[200:203], v[148:151], v[116:119], a[200:203]\n" \
"v_mfma_f32_16x16x32_bf16 a[204:207], v[152:155], v[112:115], a[204:207]\n" \
"v_mfma_f32_16x16x32_bf16 a[204:207], v[156:159], v[116:119], a[204:207]\n" \
"v_mfma_f32_16x16x32_bf16 a[236:239], v[152:155], v[120:123], a[236:239]\n" \
"v_mfma_f32_16x16x32_bf16 a[236:239], v[156:159], v[124:127], a[236:239]\n" \
"v_mfma_f32_16x16x32_bf16 a[232:235], v[144:147], v[120:123], a[232:235]\n" \
"v_mfma_f32_16x16x32_bf16 a[232:235], v[148:151], v[124:127], a[232:235]\n" \
"v_mfma_f32_16x16x32_bf16 a[228:231], v[136:139], v[120:123], a[228:231]\n" \
"v_mfma_f32_16x16x32_bf16 a[228:231], v[140:143], v[124:127], a[228:231]\n" \
"v_mfma_f32_16x16x32_bf16 a[224:227], v[128:131], v[120:123], a[224:227]\n" \
"s_waitcnt vmcnt(11) lgkmcnt(0)\n" \
"v_mfma_f32_16x16x32_bf16 a[224:227], v[132:135], v[124:127], a[224:227]\n" \
"s_barrier\n" \
"v_mfma_f32_16x16x32_bf16 a[16:19], v[64:67], v[160:163], a[16:19]\n" \
"s_add_u32 m0, s87, 102400\n" \
"v_mfma_f32_16x16x32_bf16 a[16:19], v[68:71], v[164:167], a[16:19]\n" \
"global_load_lds_dwordx4 v224, s[48:49] offset:-4096\n" \
"v_mfma_f32_16x16x32_bf16 a[20:23], v[64:67], v[168:171], a[20:23]\n" \
"ds_read_b64_tr_b16 v[0:1], v232 offset:0\n" \
"v_mfma_f32_16x16x32_bf16 a[20:23], v[68:71], v[172:175], a[20:23]\n" \
"v_accvgpr_read_b32 v234, a192\n" \
"v_accvgpr_read_b32 v235, a193\n" \
"v_mfma_f32_16x16x32_bf16 a[24:27], v[64:67], v[176:179], a[24:27]\n" \
"ds_read_b64_tr_b16 v[2:3], v232 offset:2048\n" \
"v_accvgpr_read_b32 v236, a194\n" \
"v_mfma_f32_16x16x32_bf16 a[24:27], v[68:71], v[180:183], a[24:27]\n" \
"global_load_lds_dwordx4 v225, s[48:49] offset:-3072\n" \
"v_accvgpr_read_b32 v237, a195\n" \
"v_mfma_f32_16x16x32_bf16 a[28:31], v[64:67], v[184:187], a[28:31]\n" \
"ds_read_b64_tr_b16 v[4:5], v232 offset:8192\n" \
"v_mul_f32 v234, s92, v234\n" \
"v_mfma_f32_16x16x32_bf16 a[28:31], v[68:71], v[188:191], a[28:31]\n" \
"v_mul_f32 v235, s92, v235\n" \
"v_mul_f32 v236, s92, v236\n" \
"v_mfma_f32_16x16x32_bf16 a[60:63], v[72:75], v[184:187], a[60:63]\n" \
"ds_read_b64_tr_b16 v[6:7], v232 offset:10240\n" \
"v_mul_f32 v237, s92, v237\n" \
"v_mfma_f32_16x16x32_bf16 a[60:63], v[76:79], v[188:191], a[60:63]\n" \
"global_load_lds_dwordx4 v226, s[48:49] offset:-2048\n" \
"v_cvt_pk_bf16_f32 v234, v234, v235\n" \
"v_mfma_f32_16x16x32_bf16 a[56:59], v[72:75], v[176:179], a[56:59]\n" \
"ds_read_b64_tr_b16 v[8:9], v233 offset:0\n" \
"v_cvt_pk_bf16_f32 v236, v236, v237\n" \
"v_mfma_f32_16x16x32_bf16 a[56:59], v[76:79], v[180:183], a[56:59]\n" \
"v_accvgpr_write_b32 a192, v234\n" \
"v_accvgpr_write_b32 a193, v236\n" \
"v_mfma_f32_16x16x32_bf16 a[52:55], v[72:75], v[168:171], a[52:55]\n" \
"ds_read_b64_tr_b16 v[10:11], v233 offset:2048\n" \
"v_accvgpr_read_b32 v234, a196\n" \
"v_mfma_f32_16x16x32_bf16 a[52:55], v[76:79], v[172:175], a[52:55]\n" \
"global_load_lds_dwordx4 v227, s[48:49] offset:-1024\n" \
"v_accvgpr_read_b32 v235, a197\n" \
"v_mfma_f32_16x16x32_bf16 a[48:51], v[72:75], v[160:163], a[48:51]\n" \
"s_add_u32 s48, s48, 0xfffffb00\n" \
"s_addc_u32 s49, s49, -1\n" \
"ds_read_b64_tr_b16 v[12:13], v233 offset:8192\n" \
"v_mfma_f32_16x16x32_bf16 a[48:51], v[76:79], v[164:167], a[48:51]\n" \
"v_accvgpr_read_b32 v236, a198\n" \
"v_accvgpr_read_b32 v237, a199\n" \
"v_mfma_f32_16x16x32_bf16 a[80:83], v[80:83], v[160:163], a[80:83]\n" \
"ds_read_b64_tr_b16 v[14:15], v233 offset:10240\n" \
"v_mul_f32 v234, s92, v234\n" \
"v_mfma_f32_16x16x32_bf16 a[80:83], v[84:87], v[164:167], a[80:83]\n" \
"global_load_lds_dwordx4 v224, s[46:47] offset:0\n" \
"v_mul_f32 v235, s92, v235\n" \
"v_mfma_f32_16x16x32_bf16 a[84:87], v[80:83], v[168:171], a[84:87]\n" \
"ds_read_b64_tr_b16 v[16:17], v232 offset:512\n" \
"v_mul_f32 v236, s92, v236\n" \
"v_mfma_f32_16x16x32_bf16 a[84:87], v[84:87], v[172:175], a[84:87]\n" \
"v_mul_f32 v237, s92, v237\n" \
"v_cvt_pk_bf16_f32 v234, v234, v235\n" \
"v_mfma_f32_16x16x32_bf16 a[88:91], v[80:83], v[176:179], a[88:91]\n" \
"ds_read_b64_tr_b16 v[18:19], v232 offset:2560\n" \
"v_cvt_pk_bf16_f32 v236, v236, v237\n" \
"v_mfma_f32_16x16x32_bf16 a[88:91], v[84:87], v[180:183], a[88:91]\n" \
"global_load_lds_dwordx4 v225, s[46:47] offset:1024\n" \
"v_accvgpr_write_b32 a194, v234\n" \
"v_mfma_f32_16x16x32_bf16 a[92:95], v[80:83], v[184:187], a[92:95]\n" \
"ds_read_b64_tr_b16 v[20:21], v232 offset:8704\n" \
"v_accvgpr_write_b32 a195, v236\n" \
"v_mfma_f32_16x16x32_bf16 a[92:95], v[84:87], v[188:191], a[92:95]\n" \
"v_accvgpr_read_b32 v234, a224\n" \
"v_accvgpr_read_b32 v235, a225\n" \
"v_mfma_f32_16x16x32_bf16 a[124:127], v[88:91], v[184:187], a[124:127]\n" \
"ds_read_b64_tr_b16 v[22:23], v232 offset:10752\n" \
"v_accvgpr_read_b32 v236, a226\n" \
"v_mfma_f32_16x16x32_bf16 a[124:127], v[92:95], v[188:191], a[124:127]\n" \
"global_load_lds_dwordx4 v226, s[46:47] offset:2048\n" \
"v_accvgpr_read_b32 v237, a227\n" \
"v_mfma_f32_16x16x32_bf16 a[120:123], v[88:91], v[176:179], a[120:123]\n" \
"ds_read_b64_tr_b16 v[24:25], v233 offset:512\n" \
"v_mul_f32 v234, s92, v234\n" \
"v_mfma_f32_16x16x32_bf16 a[120:123], v[92:95], v[180:183], a[120:123]\n" \
"v_mul_f32 v235, s92, v235\n" \
"v_mul_f32 v236, s92, v236\n" \
"v_mfma_f32_16x16x32_bf16 a[116:119], v[88:91], v[168:171], a[116:119]\n" \
"ds_read_b64_tr_b16 v[26:27], v233 offset:2560\n" \
"v_mul_f32 v237, s92, v237\n" \
"v_mfma_f32_16x16x32_bf16 a[116:119], v[92:95], v[172:175], a[116:119]\n" \
"global_load_lds_dwordx4 v227, s[46:47] offset:3072\n" \
"v_cvt_pk_bf16_f32 v234, v234, v235\n" \
"v_mfma_f32_16x16x32_bf16 a[112:115], v[88:91], v[160:163], a[112:115]\n" \
"s_add_u32 s46, s46, 0xfffffb00\n" \
"s_addc_u32 s47, s47, -1\n" \
"ds_read_b64_tr_b16 v[28:29], v233 offset:8704\n" \
"v_mfma_f32_16x16x32_bf16 a[112:115], v[92:95], v[164:167], a[112:115]\n" \
"v_cvt_pk_bf16_f32 v236, v236, v237\n" \
"v_accvgpr_write_b32 a224, v234\n" \
"v_mfma_f32_16x16x32_bf16 a[144:147], v[96:99], v[160:163], a[144:147]\n" \
"s_add_u32 m0, s95, 36864\n" \
"ds_read_b64_tr_b16 v[30:31], v233 offset:10752\n" \
"v_accvgpr_write_b32 a225, v236\n" \
"v_mfma_f32_16x16x32_bf16 a[144:147], v[100:103], v[164:167], a[144:147]\n" \
"global_load_lds_dwordx4 v228, s[44:45] offset:-4096\n" \
"v_accvgpr_read_b32 v234, a228\n" \
"v_mfma_f32_16x16x32_bf16 a[148:151], v[96:99], v[168:171], a[148:151]\n" \
"ds_read_b128 v[192:195], v230 offset:0\n" \
"v_accvgpr_read_b32 v235, a229\n" \
"v_mfma_f32_16x16x32_bf16 a[148:151], v[100:103], v[172:175], a[148:151]\n" \
"v_accvgpr_read_b32 v236, a230\n" \
"v_accvgpr_read_b32 v237, a231\n" \
"v_mfma_f32_16x16x32_bf16 a[152:155], v[96:99], v[176:179], a[152:155]\n" \
"ds_read_b128 v[196:199], v231 offset:0\n" \
"v_mul_f32 v234, s92, v234\n" \
"v_mfma_f32_16x16x32_bf16 a[152:155], v[100:103], v[180:183], a[152:155]\n" \
"global_load_lds_dwordx4 v229, s[44:45] offset:-3072\n" \
"v_mul_f32 v235, s92, v235\n" \
"v_mfma_f32_16x16x32_bf16 a[156:159], v[96:99], v[184:187], a[156:159]\n" \
"s_add_u32 s44, s44, 0xffb00000\n" \
"s_addc_u32 s45, s45, -1\n" \
"ds_read_b128 v[200:203], v230 offset:2048\n" \
"v_mfma_f32_16x16x32_bf16 a[156:159], v[100:103], v[188:191], a[156:159]\n" \
"v_mul_f32 v236, s92, v236\n" \
"v_mul_f32 v237, s92, v237\n" \
"v_mfma_f32_16x16x32_bf16 a[188:191], v[104:107], v[184:187], a[188:191]\n" \
"ds_read_b128 v[204:207], v231 offset:2048\n" \
"v_cvt_pk_bf16_f32 v234, v234, v235\n" \
"v_mfma_f32_16x16x32_bf16 a[188:191], v[108:111], v[188:191], a[188:191]\n" \
"global_load_lds_dwordx4 v228, s[42:43] offset:-2048\n" \
"v_cvt_pk_bf16_f32 v236, v236, v237\n" \
"v_mfma_f32_16x16x32_bf16 a[184:187], v[104:107], v[176:179], a[184:187]\n" \
"ds_read_b128 v[208:211], v230 offset:4096\n" \
"v_accvgpr_write_b32 a226, v234\n" \
"v_mfma_f32_16x16x32_bf16 a[184:187], v[108:111], v[180:183], a[184:187]\n" \
"v_accvgpr_write_b32 a227, v236\n" \
"v_accvgpr_read_b32 v234, a200\n" \
"v_mfma_f32_16x16x32_bf16 a[180:183], v[104:107], v[168:171], a[180:183]\n" \
"ds_read_b128 v[212:215], v231 offset:4096\n" \
"v_accvgpr_read_b32 v235, a201\n" \
"v_mfma_f32_16x16x32_bf16 a[180:183], v[108:111], v[172:175], a[180:183]\n" \
"global_load_lds_dwordx4 v229, s[42:43] offset:-1024\n" \
"v_accvgpr_read_b32 v236, a202\n" \
"v_mfma_f32_16x16x32_bf16 a[176:179], v[104:107], v[160:163], a[176:179]\n" \
"s_add_u32 s42, s42, 0xffb00000\n" \
"s_addc_u32 s43, s43, -1\n" \
"v_accvgpr_read_b32 v237, a203\n" \
"v_mfma_f32_16x16x32_bf16 a[176:179], v[108:111], v[164:167], a[176:179]\n" \
"v_mul_f32 v234, s92, v234\n" \
"v_mul_f32 v235, s92, v235\n" \
"v_mfma_f32_16x16x32_bf16 a[208:211], v[112:115], v[160:163], a[208:211]\n" \
"s_add_u32 m0, s95, 53248\n" \
"v_mul_f32 v236, s92, v236\n" \
"v_mul_f32 v237, s92, v237\n" \
"v_mfma_f32_16x16x32_bf16 a[208:211], v[116:119], v[164:167], a[208:211]\n" \
"global_load_lds_dwordx4 v228, s[44:45] offset:-4096\n" \
"v_cvt_pk_bf16_f32 v234, v234, v235\n" \
"v_mfma_f32_16x16x32_bf16 a[212:215], v[112:115], v[168:171], a[212:215]\n" \
"v_cvt_pk_bf16_f32 v236, v236, v237\n" \
"v_accvgpr_write_b32 a200, v234\n" \
"v_mfma_f32_16x16x32_bf16 a[212:215], v[116:119], v[172:175], a[212:215]\n" \
"v_accvgpr_write_b32 a201, v236\n" \
"v_accvgpr_read_b32 v234, a204\n" \
"v_mfma_f32_16x16x32_bf16 a[216:219], v[112:115], v[176:179], a[216:219]\n" \
"v_accvgpr_read_b32 v235, a205\n" \
"v_accvgpr_read_b32 v236, a206\n" \
"v_mfma_f32_16x16x32_bf16 a[216:219], v[116:119], v[180:183], a[216:219]\n" \
"global_load_lds_dwordx4 v229, s[44:45] offset:-3072\n" \
"v_accvgpr_read_b32 v237, a207\n" \
"v_mfma_f32_16x16x32_bf16 a[220:223], v[112:115], v[184:187], a[220:223]\n" \
"s_add_u32 s44, s44, 0x80000\n" \
"s_addc_u32 s45, s45, 0\n" \
"v_mul_f32 v234, s92, v234\n" \
"v_mfma_f32_16x16x32_bf16 a[220:223], v[116:119], v[188:191], a[220:223]\n" \
"v_mul_f32 v235, s92, v235\n" \
"v_mul_f32 v236, s92, v236\n" \
"v_mfma_f32_16x16x32_bf16 a[252:255], v[120:123], v[184:187], a[252:255]\n" \
"v_mul_f32 v237, s92, v237\n" \
"v_cvt_pk_bf16_f32 v234, v234, v235\n" \
"v_mfma_f32_16x16x32_bf16 a[252:255], v[124:127], v[188:191], a[252:255]\n" \
"global_load_lds_dwordx4 v228, s[42:43] offset:-2048\n" \
"v_cvt_pk_bf16_f32 v236, v236, v237\n" \
"v_mfma_f32_16x16x32_bf16 a[248:251], v[120:123], v[176:179], a[248:251]\n" \
"v_accvgpr_write_b32 a202, v234\n" \
"v_accvgpr_write_b32 a203, v236\n" \
"v_mfma_f32_16x16x32_bf16 a[248:251], v[124:127], v[180:183], a[248:251]\n" \
"v_accvgpr_read_b32 v234, a232\n" \
"v_accvgpr_read_b32 v235, a233\n" \
"v_mfma_f32_16x16x32_bf16 a[244:247], v[120:123], v[168:171], a[244:247]\n" \
"v_accvgpr_read_b32 v236, a234\n" \
"v_accvgpr_read_b32 v237, a235\n" \
"v_mfma_f32_16x16x32_bf16 a[244:247], v[124:127], v[172:175], a[244:247]\n" \
"global_load_lds_dwordx4 v229, s[42:43] offset:-1024\n" \
"v_mul_f32 v234, s92, v234\n" \
"v_mfma_f32_16x16x32_bf16 a[240:243], v[120:123], v[160:163], a[240:243]\n" \
"s_add_u32 s42, s42, 0x80000\n" \
"s_addc_u32 s43, s43, 0\n" \
"v_mul_f32 v235, s92, v235\n" \
"v_mfma_f32_16x16x32_bf16 a[240:243], v[124:127], v[164:167], a[240:243]\n" \
"s_waitcnt lgkmcnt(0)\n" \
"v_mul_f32 v236, s92, v236\n" \
"v_mul_f32 v237, s92, v237\n" \
"v_mfma_f32_16x16x32_bf16 v[64:67], v[0:3], v[192:195], 0\n" \
"ds_read_b128 v[216:219], v230 offset:6144\n" \
"v_cvt_pk_bf16_f32 v234, v234, v235\n" \
"v_mfma_f32_16x16x32_bf16 v[64:67], v[4:7], v[196:199], v[64:67]\n" \
"ds_read_b128 v[220:223], v231 offset:6144\n" \
"v_cvt_pk_bf16_f32 v236, v236, v237\n" \
"v_mfma_f32_16x16x32_bf16 v[68:71], v[8:11], v[192:195], 0\n" \
"s_add_u32 s90, s88, 0\n" \
"ds_read_b64_tr_b16 v[32:33], v232 offset:1024\n" \
"v_accvgpr_write_b32 a232, v234\n" \
"v_mfma_f32_16x16x32_bf16 v[68:71], v[12:15], v[196:199], v[68:71]\n" \
"buffer_store_dwordx4 a[192:195], v249, s[56:59], s90 offen offset:0 sc1\n" \
"v_accvgpr_write_b32 a233, v236\n" \
"v_accvgpr_read_b32 v234, a236\n" \
"v_accvgpr_read_b32 v235, a237\n" \
"v_accvgpr_read_b32 v236, a238\n" \
"v_mfma_f32_16x16x32_bf16 v[72:75], v[16:19], v[192:195], 0\n" \
"ds_read_b64_tr_b16 v[34:35], v232 offset:3072\n" \
"v_accvgpr_read_b32 v237, a239\n" \
"v_mfma_f32_16x16x32_bf16 v[72:75], v[20:23], v[196:199], v[72:75]\n" \
"ds_read_b64_tr_b16 v[36:37], v232 offset:9216\n" \
"v_mul_f32 v234, s92, v234\n" \
"v_mfma_f32_16x16x32_bf16 v[76:79], v[24:27], v[192:195], 0\n" \
"ds_read_b64_tr_b16 v[38:39], v232 offset:11264\n" \
"v_mul_f32 v235, s92, v235\n" \
"v_mfma_f32_16x16x32_bf16 v[76:79], v[28:31], v[196:199], v[76:79]\n" \
"buffer_store_dwordx4 a[224:227], v249, s[56:59], s90 offen offset:256 sc1\n" \
"v_mul_f32 v236, s92, v236\n" \
"v_mul_f32 v237, s92, v237\n" \
"v_cvt_pk_bf16_f32 v234, v234, v235\n" \
"v_cvt_pk_bf16_f32 v236, v236, v237\n" \
"v_mfma_f32_16x16x32_bf16 v[108:111], v[24:27], v[200:203], 0\n" \
"ds_read_b64_tr_b16 v[40:41], v233 offset:1024\n" \
"v_accvgpr_write_b32 a234, v234\n" \
"v_mfma_f32_16x16x32_bf16 v[108:111], v[28:31], v[204:207], v[108:111]\n" \
"ds_read_b64_tr_b16 v[42:43], v233 offset:3072\n" \
"v_accvgpr_write_b32 a235, v236\n" \
"v_mfma_f32_16x16x32_bf16 v[104:107], v[16:19], v[200:203], 0\n" \
"ds_read_b64_tr_b16 v[44:45], v233 offset:9216\n" \
"v_accvgpr_read_b32 v234, a208\n" \
"v_mfma_f32_16x16x32_bf16 v[104:107], v[20:23], v[204:207], v[104:107]\n" \
"v_accvgpr_read_b32 v235, a209\n" \
"v_accvgpr_read_b32 v236, a210\n" \
"v_mfma_f32_16x16x32_bf16 v[100:103], v[8:11], v[200:203], 0\n" \
"ds_read_b64_tr_b16 v[46:47], v233 offset:11264\n" \
"v_accvgpr_read_b32 v237, a211\n" \
"v_mfma_f32_16x16x32_bf16 v[100:103], v[12:15], v[204:207], v[100:103]\n" \
"ds_read_b64_tr_b16 v[48:49], v232 offset:1536\n" \
"v_cvt_pk_bf16_f32 v234, v234, v235\n" \
"v_mfma_f32_16x16x32_bf16 v[96:99], v[0:3], v[200:203], 0\n" \
"ds_read_b64_tr_b16 v[50:51], v232 offset:3584\n" \
"v_cvt_pk_bf16_f32 v236, v236, v237\n" \
"v_mfma_f32_16x16x32_bf16 v[96:99], v[4:7], v[204:207], v[96:99]\n" \
"v_accvgpr_write_b32 a208, v234\n" \
"v_accvgpr_write_b32 a209, v236\n" \
"v_mfma_f32_16x16x32_bf16 v[128:131], v[0:3], v[208:211], 0\n" \
"ds_read_b64_tr_b16 v[52:53], v232 offset:9728\n" \
"v_accvgpr_read_b32 v234, a240\n" \
"v_mfma_f32_16x16x32_bf16 v[128:131], v[4:7], v[212:215], v[128:131]\n" \
"ds_read_b64_tr_b16 v[54:55], v232 offset:11776\n" \
"v_accvgpr_read_b32 v235, a241\n" \
"v_mfma_f32_16x16x32_bf16 v[132:135], v[8:11], v[208:211], 0\n" \
"ds_read_b64_tr_b16 v[56:57], v233 offset:1536\n" \
"v_accvgpr_read_b32 v236, a242\n" \
"Lpg2228_%=:\n" \
"Lst0x3_%=:\n" \
"s_cbranch_execz Lpg4399_%=\n" \
"v_mfma_f32_16x16x32_bf16 v[132:135], v[12:15], v[212:215], v[132:135]\n" \
"v_accvgpr_read_b32 v237, a243\n" \
"v_cvt_pk_bf16_f32 v234, v234, v235\n" \
"v_mfma_f32_16x16x32_bf16 v[136:139], v[16:19], v[208:211], 0\n" \
"ds_read_b64_tr_b16 v[58:59], v233 offset:3584\n" \
"v_cvt_pk_bf16_f32 v236, v236, v237\n" \
"v_mfma_f32_16x16x32_bf16 v[136:139], v[20:23], v[212:215], v[136:139]\n" \
"ds_read_b64_tr_b16 v[60:61], v233 offset:9728\n" \
"v_accvgpr_write_b32 a210, v234\n" \
"v_mfma_f32_16x16x32_bf16 v[140:143], v[24:27], v[208:211], 0\n" \
"ds_read_b64_tr_b16 v[62:63], v233 offset:11776\n" \
"v_accvgpr_write_b32 a211, v236\n" \
"v_mfma_f32_16x16x32_bf16 v[140:143], v[28:31], v[212:215], v[140:143]\n" \
"s_waitcnt lgkmcnt(3)\n" \
"v_accvgpr_read_b32 v234, a212\n" \
"v_accvgpr_read_b32 v235, a213\n" \
"v_mfma_f32_16x16x32_bf16 v[172:175], v[24:27], v[216:219], 0\n" \
"v_add_u32 v230, 0xffff0000, v230\n" \
"v_add_u32 v231, 0xffff0000, v231\n" \
"v_mfma_f32_16x16x32_bf16 v[172:175], v[28:31], v[220:223], v[172:175]\n" \
"v_add_u32 v232, 0x4000, v232\n" \
"v_add_u32 v233, 0x4000, v233\n" \
"v_mfma_f32_16x16x32_bf16 v[168:171], v[16:19], v[216:219], 0\n" \
"v_accvgpr_read_b32 v236, a214\n" \
"v_accvgpr_read_b32 v237, a215\n" \
"v_mfma_f32_16x16x32_bf16 v[168:171], v[20:23], v[220:223], v[168:171]\n" \
"v_cvt_pk_bf16_f32 v234, v234, v235\n" \
"v_cvt_pk_bf16_f32 v236, v236, v237\n" \
"v_mfma_f32_16x16x32_bf16 v[164:167], v[8:11], v[216:219], 0\n" \
"v_accvgpr_write_b32 a212, v234\n" \
"v_accvgpr_write_b32 a213, v236\n" \
"v_mfma_f32_16x16x32_bf16 v[164:167], v[12:15], v[220:223], v[164:167]\n" \
"v_accvgpr_read_b32 v234, a244\n" \
"v_accvgpr_read_b32 v235, a245\n" \
"v_mfma_f32_16x16x32_bf16 v[160:163], v[0:3], v[216:219], 0\n" \
"s_waitcnt vmcnt(18) lgkmcnt(0)\n" \
"v_accvgpr_read_b32 v236, a246\n" \
"v_accvgpr_read_b32 v237, a247\n" \
"v_mfma_f32_16x16x32_bf16 v[160:163], v[4:7], v[220:223], v[160:163]\n" \
"s_barrier\n" \
"v_mfma_f32_16x16x32_bf16 v[80:83], v[32:35], v[192:195], 0\n" \
"ds_read_b64_tr_b16 v[0:1], v232 offset:0\n" \
"v_cvt_pk_bf16_f32 v234, v234, v235\n" \
"v_mfma_f32_16x16x32_bf16 v[80:83], v[36:39], v[196:199], v[80:83]\n" \
"ds_read_b64_tr_b16 v[2:3], v232 offset:2048\n" \
"v_cvt_pk_bf16_f32 v236, v236, v237\n" \
"v_mfma_f32_16x16x32_bf16 v[84:87], v[40:43], v[192:195], 0\n" \
"s_add_u32 m0, s87, 135168\n" \
"ds_read_b64_tr_b16 v[4:5], v232 offset:8192\n" \
"v_accvgpr_write_b32 a214, v234\n" \
"v_mfma_f32_16x16x32_bf16 v[84:87], v[44:47], v[196:199], v[84:87]\n" \
"global_load_lds_dwordx4 v224, s[48:49] offset:-4096\n" \
"v_accvgpr_write_b32 a215, v236\n" \
"v_mfma_f32_16x16x32_bf16 v[88:91], v[48:51], v[192:195], 0\n" \
"ds_read_b64_tr_b16 v[6:7], v232 offset:10240\n" \
"v_accvgpr_read_b32 v234, a216\n" \
"v_mfma_f32_16x16x32_bf16 v[88:91], v[52:55], v[196:199], v[88:91]\n" \
"ds_read_b64_tr_b16 v[8:9], v233 offset:0\n" \
"v_accvgpr_read_b32 v235, a217\n" \
"v_mfma_f32_16x16x32_bf16 v[92:95], v[56:59], v[192:195], 0\n" \
"ds_read_b64_tr_b16 v[10:11], v233 offset:2048\n" \
"v_accvgpr_read_b32 v236, a218\n" \
"v_mfma_f32_16x16x32_bf16 v[92:95], v[60:63], v[196:199], v[92:95]\n" \
"global_load_lds_dwordx4 v225, s[48:49] offset:-3072\n" \
"v_accvgpr_read_b32 v237, a219\n" \
"v_mfma_f32_16x16x32_bf16 v[124:127], v[56:59], v[200:203], 0\n" \
"ds_read_b128 v[192:195], v230 offset:0\n" \
"v_cvt_pk_bf16_f32 v234, v234, v235\n" \
"v_mfma_f32_16x16x32_bf16 v[124:127], v[60:63], v[204:207], v[124:127]\n" \
"ds_read_b128 v[196:199], v231 offset:0\n" \
"v_cvt_pk_bf16_f32 v236, v236, v237\n" \
"v_mfma_f32_16x16x32_bf16 v[120:123], v[48:51], v[200:203], 0\n" \
"ds_read_b64_tr_b16 v[12:13], v233 offset:8192\n" \
"v_accvgpr_write_b32 a216, v234\n" \
"v_mfma_f32_16x16x32_bf16 v[120:123], v[52:55], v[204:207], v[120:123]\n" \
"global_load_lds_dwordx4 v226, s[48:49] offset:-2048\n" \
"v_accvgpr_write_b32 a217, v236\n" \
"v_mfma_f32_16x16x32_bf16 v[116:119], v[40:43], v[200:203], 0\n" \
"ds_read_b64_tr_b16 v[14:15], v233 offset:10240\n" \
"v_accvgpr_read_b32 v234, a248\n" \
"v_mfma_f32_16x16x32_bf16 v[116:119], v[44:47], v[204:207], v[116:119]\n" \
"ds_read_b64_tr_b16 v[16:17], v232 offset:512\n" \
"v_accvgpr_read_b32 v235, a249\n" \
"v_mfma_f32_16x16x32_bf16 v[112:115], v[32:35], v[200:203], 0\n" \
"ds_read_b64_tr_b16 v[18:19], v232 offset:2560\n" \
"v_accvgpr_read_b32 v236, a250\n" \
"v_mfma_f32_16x16x32_bf16 v[112:115], v[36:39], v[204:207], v[112:115]\n" \
"global_load_lds_dwordx4 v227, s[48:49] offset:-1024\n" \
"v_accvgpr_read_b32 v237, a251\n" \
"v_mfma_f32_16x16x32_bf16 v[144:147], v[32:35], v[208:211], 0\n" \
"s_add_u32 s48, s48, 0x80\n" \
"s_addc_u32 s49, s49, 0\n" \
"ds_read_b128 v[200:203], v230 offset:2048\n" \
"v_mfma_f32_16x16x32_bf16 v[144:147], v[36:39], v[212:215], v[144:147]\n" \
"ds_read_b128 v[204:207], v231 offset:2048\n" \
"v_cvt_pk_bf16_f32 v234, v234, v235\n" \
"v_mfma_f32_16x16x32_bf16 v[148:151], v[40:43], v[208:211], 0\n" \
"ds_read_b64_tr_b16 v[20:21], v232 offset:8704\n" \
"v_cvt_pk_bf16_f32 v236, v236, v237\n" \
"v_mfma_f32_16x16x32_bf16 v[148:151], v[44:47], v[212:215], v[148:151]\n" \
"global_load_lds_dwordx4 v224, s[46:47] offset:0\n" \
"v_accvgpr_write_b32 a218, v234\n" \
"v_mfma_f32_16x16x32_bf16 v[152:155], v[48:51], v[208:211], 0\n" \
"ds_read_b64_tr_b16 v[22:23], v232 offset:10752\n" \
"v_accvgpr_write_b32 a219, v236\n" \
"v_mfma_f32_16x16x32_bf16 v[152:155], v[52:55], v[212:215], v[152:155]\n" \
"ds_read_b64_tr_b16 v[24:25], v233 offset:512\n" \
"v_accvgpr_read_b32 v234, a220\n" \
"v_mfma_f32_16x16x32_bf16 v[156:159], v[56:59], v[208:211], 0\n" \
"ds_read_b64_tr_b16 v[26:27], v233 offset:2560\n" \
"v_accvgpr_read_b32 v235, a221\n" \
"v_mfma_f32_16x16x32_bf16 v[156:159], v[60:63], v[212:215], v[156:159]\n" \
"global_load_lds_dwordx4 v225, s[46:47] offset:1024\n" \
"v_accvgpr_read_b32 v236, a222\n" \
"v_mfma_f32_16x16x32_bf16 v[188:191], v[56:59], v[216:219], 0\n" \
"ds_read_b128 v[208:211], v230 offset:4096\n" \
"v_accvgpr_read_b32 v237, a223\n" \
"v_mfma_f32_16x16x32_bf16 v[188:191], v[60:63], v[220:223], v[188:191]\n" \
"ds_read_b128 v[212:215], v231 offset:4096\n" \
"v_cvt_pk_bf16_f32 v234, v234, v235\n" \
"v_mfma_f32_16x16x32_bf16 v[184:187], v[48:51], v[216:219], 0\n" \
"ds_read_b64_tr_b16 v[28:29], v233 offset:8704\n" \
"v_cvt_pk_bf16_f32 v236, v236, v237\n" \
"v_mfma_f32_16x16x32_bf16 v[184:187], v[52:55], v[220:223], v[184:187]\n" \
"global_load_lds_dwordx4 v226, s[46:47] offset:2048\n" \
"v_accvgpr_write_b32 a220, v234\n" \
"v_mfma_f32_16x16x32_bf16 v[180:183], v[40:43], v[216:219], 0\n" \
"ds_read_b64_tr_b16 v[30:31], v233 offset:10752\n" \
"v_accvgpr_write_b32 a221, v236\n" \
"v_mfma_f32_16x16x32_bf16 v[180:183], v[44:47], v[220:223], v[180:183]\n" \
"v_accvgpr_read_b32 v234, a252\n" \
"v_accvgpr_read_b32 v235, a253\n" \
"v_mfma_f32_16x16x32_bf16 v[176:179], v[32:35], v[216:219], 0\n" \
"v_accvgpr_read_b32 v236, a254\n" \
"v_accvgpr_read_b32 v237, a255\n" \
"v_mfma_f32_16x16x32_bf16 v[176:179], v[36:39], v[220:223], v[176:179]\n" \
"s_waitcnt lgkmcnt(1)\n" \
"global_load_lds_dwordx4 v227, s[46:47] offset:3072\n" \
"v_cvt_pk_bf16_f32 v234, v234, v235\n" \
"v_mfma_f32_16x16x32_bf16 v[64:67], v[0:3], v[192:195], v[64:67]\n" \
"s_add_u32 s46, s46, 0x80\n" \
"s_addc_u32 s47, s47, 0\n" \
"ds_read_b128 v[216:219], v230 offset:6144\n" \
"v_mfma_f32_16x16x32_bf16 v[64:67], v[4:7], v[196:199], v[64:67]\n" \
"ds_read_b128 v[220:223], v231 offset:6144\n" \
"v_cvt_pk_bf16_f32 v236, v236, v237\n" \
"v_mfma_f32_16x16x32_bf16 v[68:71], v[8:11], v[192:195], v[68:71]\n" \
"ds_read_b64_tr_b16 v[32:33], v232 offset:1024\n" \
"v_accvgpr_write_b32 a222, v234\n" \
"v_mfma_f32_16x16x32_bf16 v[68:71], v[12:15], v[196:199], v[68:71]\n" \
"buffer_store_dwordx4 a[200:203], v249, s[56:59], s90 offen offset:2048 sc1\n" \
"v_accvgpr_write_b32 a223, v236\n" \
"v_accvgpr_read_b32 v234, a128\n" \
"v_accvgpr_read_b32 v235, a129\n" \
"v_accvgpr_read_b32 v236, a130\n" \
"v_mfma_f32_16x16x32_bf16 v[72:75], v[16:19], v[192:195], v[72:75]\n" \
"ds_read_b64_tr_b16 v[34:35], v232 offset:3072\n" \
"v_accvgpr_read_b32 v237, a131\n" \
"v_mfma_f32_16x16x32_bf16 v[72:75], v[20:23], v[196:199], v[72:75]\n" \
"s_waitcnt lgkmcnt(3)\n" \
"ds_read_b64_tr_b16 v[36:37], v232 offset:9216\n" \
"v_mul_f32 v234, s92, v234\n" \
"v_mfma_f32_16x16x32_bf16 v[76:79], v[24:27], v[192:195], v[76:79]\n" \
"ds_read_b64_tr_b16 v[38:39], v232 offset:11264\n" \
"v_mul_f32 v235, s92, v235\n" \
"v_mfma_f32_16x16x32_bf16 v[76:79], v[28:31], v[196:199], v[76:79]\n" \
"v_mul_f32 v236, s92, v236\n" \
"v_mul_f32 v237, s92, v237\n" \
"v_mfma_f32_16x16x32_bf16 v[108:111], v[24:27], v[200:203], v[108:111]\n" \
"ds_read_b64_tr_b16 v[40:41], v233 offset:1024\n" \
"v_cvt_pk_bf16_f32 v234, v234, v235\n" \
"v_mfma_f32_16x16x32_bf16 v[108:111], v[28:31], v[204:207], v[108:111]\n" \
"ds_read_b64_tr_b16 v[42:43], v233 offset:3072\n" \
"v_cvt_pk_bf16_f32 v236, v236, v237\n" \
"v_mfma_f32_16x16x32_bf16 v[104:107], v[16:19], v[200:203], v[104:107]\n" \
"ds_read_b64_tr_b16 v[44:45], v233 offset:9216\n" \
"v_accvgpr_write_b32 a128, v234\n" \
"v_mfma_f32_16x16x32_bf16 v[104:107], v[20:23], v[204:207], v[104:107]\n" \
"v_accvgpr_write_b32 a129, v236\n" \
"v_accvgpr_read_b32 v234, a132\n" \
"v_mfma_f32_16x16x32_bf16 v[100:103], v[8:11], v[200:203], v[100:103]\n" \
"ds_read_b64_tr_b16 v[46:47], v233 offset:11264\n" \
"v_accvgpr_read_b32 v235, a133\n" \
"v_mfma_f32_16x16x32_bf16 v[100:103], v[12:15], v[204:207], v[100:103]\n" \
"ds_read_b64_tr_b16 v[48:49], v232 offset:1536\n" \
"v_accvgpr_read_b32 v236, a134\n" \
"v_mfma_f32_16x16x32_bf16 v[96:99], v[0:3], v[200:203], v[96:99]\n" \
"s_add_u32 m0, s95, 4096\n" \
"ds_read_b64_tr_b16 v[50:51], v232 offset:3584\n" \
"v_accvgpr_read_b32 v237, a135\n" \
"v_mfma_f32_16x16x32_bf16 v[96:99], v[4:7], v[204:207], v[96:99]\n" \
"global_load_lds_dwordx4 v228, s[44:45] offset:-4096\n" \
"v_mul_f32 v234, s92, v234\n" \
"v_mfma_f32_16x16x32_bf16 v[128:131], v[0:3], v[208:211], v[128:131]\n" \
"ds_read_b64_tr_b16 v[52:53], v232 offset:9728\n" \
"v_mul_f32 v235, s92, v235\n" \
"v_mfma_f32_16x16x32_bf16 v[128:131], v[4:7], v[212:215], v[128:131]\n" \
"ds_read_b64_tr_b16 v[54:55], v232 offset:11776\n" \
"v_mul_f32 v236, s92, v236\n" \
"v_mfma_f32_16x16x32_bf16 v[132:135], v[8:11], v[208:211], v[132:135]\n" \
"ds_read_b64_tr_b16 v[56:57], v233 offset:1536\n" \
"v_mul_f32 v237, s92, v237\n" \
"v_mfma_f32_16x16x32_bf16 v[132:135], v[12:15], v[212:215], v[132:135]\n" \
"global_load_lds_dwordx4 v229, s[44:45] offset:-3072\n" \
"v_cvt_pk_bf16_f32 v234, v234, v235\n" \
"v_mfma_f32_16x16x32_bf16 v[136:139], v[16:19], v[208:211], v[136:139]\n" \
"s_add_u32 s44, s44, 0x100000\n" \
"s_addc_u32 s45, s45, 0\n" \
"ds_read_b64_tr_b16 v[58:59], v233 offset:3584\n" \
"v_mfma_f32_16x16x32_bf16 v[136:139], v[20:23], v[212:215], v[136:139]\n" \
"ds_read_b64_tr_b16 v[60:61], v233 offset:9728\n" \
"v_cvt_pk_bf16_f32 v236, v236, v237\n" \
"v_mfma_f32_16x16x32_bf16 v[140:143], v[24:27], v[208:211], v[140:143]\n" \
"ds_read_b64_tr_b16 v[62:63], v233 offset:11776\n" \
"v_accvgpr_write_b32 a130, v234\n" \
"v_mfma_f32_16x16x32_bf16 v[140:143], v[28:31], v[212:215], v[140:143]\n" \
"s_waitcnt lgkmcnt(3)\n" \
"global_load_lds_dwordx4 v228, s[42:43] offset:-2048\n" \
"v_accvgpr_write_b32 a131, v236\n" \
"v_mfma_f32_16x16x32_bf16 v[172:175], v[24:27], v[216:219], v[172:175]\n" \
"v_add_u32 v230, 0x8000, v230\n" \
"v_add_u32 v231, 0x8000, v231\n" \
"v_mfma_f32_16x16x32_bf16 v[172:175], v[28:31], v[220:223], v[172:175]\n" \
"v_add_u32 v232, 0x4000, v232\n" \
"v_add_u32 v233, 0x4000, v233\n" \
"v_mfma_f32_16x16x32_bf16 v[168:171], v[16:19], v[216:219], v[168:171]\n" \
"v_accvgpr_read_b32 v234, a160\n" \
"v_accvgpr_read_b32 v235, a161\n" \
"v_mfma_f32_16x16x32_bf16 v[168:171], v[20:23], v[220:223], v[168:171]\n" \
"global_load_lds_dwordx4 v229, s[42:43] offset:-1024\n" \
"v_accvgpr_read_b32 v236, a162\n" \
"v_mfma_f32_16x16x32_bf16 v[164:167], v[8:11], v[216:219], v[164:167]\n" \
"s_add_u32 s42, s42, 0x100000\n" \
"s_addc_u32 s43, s43, 0\n" \
"v_accvgpr_read_b32 v237, a163\n" \
"v_mfma_f32_16x16x32_bf16 v[164:167], v[12:15], v[220:223], v[164:167]\n" \
"v_mul_f32 v234, s92, v234\n" \
"v_mul_f32 v235, s92, v235\n" \
"v_mfma_f32_16x16x32_bf16 v[160:163], v[0:3], v[216:219], v[160:163]\n" \
"s_waitcnt vmcnt(19) lgkmcnt(0)\n" \
"v_mul_f32 v236, s92, v236\n" \
"v_mul_f32 v237, s92, v237\n" \
"v_mfma_f32_16x16x32_bf16 v[160:163], v[4:7], v[220:223], v[160:163]\n" \
"s_barrier\n" \
"v_mfma_f32_16x16x32_bf16 v[80:83], v[32:35], v[192:195], v[80:83]\n" \
"ds_read_b64_tr_b16 v[0:1], v232 offset:0\n" \
"v_cvt_pk_bf16_f32 v234, v234, v235\n" \
"v_mfma_f32_16x16x32_bf16 v[80:83], v[36:39], v[196:199], v[80:83]\n" \
"ds_read_b64_tr_b16 v[2:3], v232 offset:2048\n" \
"v_cvt_pk_bf16_f32 v236, v236, v237\n" \
"v_mfma_f32_16x16x32_bf16 v[84:87], v[40:43], v[192:195], v[84:87]\n" \
"s_add_u32 m0, s87, 69632\n" \
"ds_read_b64_tr_b16 v[4:5], v232 offset:8192\n" \
"v_accvgpr_write_b32 a160, v234\n" \
"v_mfma_f32_16x16x32_bf16 v[84:87], v[44:47], v[196:199], v[84:87]\n" \
"global_load_lds_dwordx4 v224, s[48:49] offset:-4096\n" \
"v_accvgpr_write_b32 a161, v236\n" \
"v_mfma_f32_16x16x32_bf16 v[88:91], v[48:51], v[192:195], v[88:91]\n" \
"ds_read_b64_tr_b16 v[6:7], v232 offset:10240\n" \
"v_accvgpr_read_b32 v234, a164\n" \
"v_mfma_f32_16x16x32_bf16 v[88:91], v[52:55], v[196:199], v[88:91]\n" \
"ds_read_b64_tr_b16 v[8:9], v233 offset:0\n" \
"v_accvgpr_read_b32 v235, a165\n" \
"v_mfma_f32_16x16x32_bf16 v[92:95], v[56:59], v[192:195], v[92:95]\n" \
"ds_read_b64_tr_b16 v[10:11], v233 offset:2048\n" \
"v_accvgpr_read_b32 v236, a166\n" \
"v_mfma_f32_16x16x32_bf16 v[92:95], v[60:63], v[196:199], v[92:95]\n" \
"global_load_lds_dwordx4 v225, s[48:49] offset:-3072\n" \
"v_accvgpr_read_b32 v237, a167\n" \
"v_mfma_f32_16x16x32_bf16 v[124:127], v[56:59], v[200:203], v[124:127]\n" \
"ds_read_b128 v[192:195], v230 offset:0\n" \
"v_mul_f32 v234, s92, v234\n" \
"v_mfma_f32_16x16x32_bf16 v[124:127], v[60:63], v[204:207], v[124:127]\n" \
"ds_read_b128 v[196:199], v231 offset:0\n" \
"v_mul_f32 v235, s92, v235\n" \
"v_mfma_f32_16x16x32_bf16 v[120:123], v[48:51], v[200:203], v[120:123]\n" \
"ds_read_b64_tr_b16 v[12:13], v233 offset:8192\n" \
"v_mul_f32 v236, s92, v236\n" \
"v_mfma_f32_16x16x32_bf16 v[120:123], v[52:55], v[204:207], v[120:123]\n" \
"global_load_lds_dwordx4 v226, s[48:49] offset:-2048\n" \
"v_mul_f32 v237, s92, v237\n" \
"v_mfma_f32_16x16x32_bf16 v[116:119], v[40:43], v[200:203], v[116:119]\n" \
"ds_read_b64_tr_b16 v[14:15], v233 offset:10240\n" \
"v_cvt_pk_bf16_f32 v234, v234, v235\n" \
"v_mfma_f32_16x16x32_bf16 v[116:119], v[44:47], v[204:207], v[116:119]\n" \
"ds_read_b64_tr_b16 v[16:17], v232 offset:512\n" \
"v_cvt_pk_bf16_f32 v236, v236, v237\n" \
"v_mfma_f32_16x16x32_bf16 v[112:115], v[32:35], v[200:203], v[112:115]\n" \
"ds_read_b64_tr_b16 v[18:19], v232 offset:2560\n" \
"v_accvgpr_write_b32 a162, v234\n" \
"v_mfma_f32_16x16x32_bf16 v[112:115], v[36:39], v[204:207], v[112:115]\n" \
"global_load_lds_dwordx4 v227, s[48:49] offset:-1024\n" \
"v_accvgpr_write_b32 a163, v236\n" \
"v_mfma_f32_16x16x32_bf16 v[144:147], v[32:35], v[208:211], v[144:147]\n" \
"s_add_u32 s48, s48, 0x100\n" \
"s_addc_u32 s49, s49, 0\n" \
"ds_read_b128 v[200:203], v230 offset:2048\n" \
"v_mfma_f32_16x16x32_bf16 v[144:147], v[36:39], v[212:215], v[144:147]\n" \
"ds_read_b128 v[204:207], v231 offset:2048\n" \
"v_accvgpr_read_b32 v234, a136\n" \
"v_mfma_f32_16x16x32_bf16 v[148:151], v[40:43], v[208:211], v[148:151]\n" \
"ds_read_b64_tr_b16 v[20:21], v232 offset:8704\n" \
"v_accvgpr_read_b32 v235, a137\n" \
"v_mfma_f32_16x16x32_bf16 v[148:151], v[44:47], v[212:215], v[148:151]\n" \
"global_load_lds_dwordx4 v224, s[46:47] offset:0\n" \
"v_accvgpr_read_b32 v236, a138\n" \
"v_mfma_f32_16x16x32_bf16 v[152:155], v[48:51], v[208:211], v[152:155]\n" \
"ds_read_b64_tr_b16 v[22:23], v232 offset:10752\n" \
"v_accvgpr_read_b32 v237, a139\n" \
"v_mfma_f32_16x16x32_bf16 v[152:155], v[52:55], v[212:215], v[152:155]\n" \
"ds_read_b64_tr_b16 v[24:25], v233 offset:512\n" \
"v_mul_f32 v234, s92, v234\n" \
"v_mfma_f32_16x16x32_bf16 v[156:159], v[56:59], v[208:211], v[156:159]\n" \
"ds_read_b64_tr_b16 v[26:27], v233 offset:2560\n" \
"v_mul_f32 v235, s92, v235\n" \
"v_mfma_f32_16x16x32_bf16 v[156:159], v[60:63], v[212:215], v[156:159]\n" \
"global_load_lds_dwordx4 v225, s[46:47] offset:1024\n" \
"v_mul_f32 v236, s92, v236\n" \
"v_mfma_f32_16x16x32_bf16 v[188:191], v[56:59], v[216:219], v[188:191]\n" \
"ds_read_b128 v[208:211], v230 offset:4096\n" \
"v_mul_f32 v237, s92, v237\n" \
"v_mfma_f32_16x16x32_bf16 v[188:191], v[60:63], v[220:223], v[188:191]\n" \
"ds_read_b128 v[212:215], v231 offset:4096\n" \
"v_cvt_pk_bf16_f32 v234, v234, v235\n" \
"v_mfma_f32_16x16x32_bf16 v[184:187], v[48:51], v[216:219], v[184:187]\n" \
"ds_read_b64_tr_b16 v[28:29], v233 offset:8704\n" \
"v_cvt_pk_bf16_f32 v236, v236, v237\n" \
"v_mfma_f32_16x16x32_bf16 v[184:187], v[52:55], v[220:223], v[184:187]\n" \
"global_load_lds_dwordx4 v226, s[46:47] offset:2048\n" \
"v_accvgpr_write_b32 a136, v234\n" \
"v_mfma_f32_16x16x32_bf16 v[180:183], v[40:43], v[216:219], v[180:183]\n" \
"ds_read_b64_tr_b16 v[30:31], v233 offset:10752\n" \
"v_accvgpr_write_b32 a137, v236\n" \
"v_mfma_f32_16x16x32_bf16 v[180:183], v[44:47], v[220:223], v[180:183]\n" \
"v_accvgpr_read_b32 v234, a140\n" \
"v_accvgpr_read_b32 v235, a141\n" \
"v_mfma_f32_16x16x32_bf16 v[176:179], v[32:35], v[216:219], v[176:179]\n" \
"v_accvgpr_read_b32 v236, a142\n" \
"v_accvgpr_read_b32 v237, a143\n" \
"v_mfma_f32_16x16x32_bf16 v[176:179], v[36:39], v[220:223], v[176:179]\n" \
"s_waitcnt lgkmcnt(1)\n" \
"global_load_lds_dwordx4 v227, s[46:47] offset:3072\n" \
"v_mul_f32 v234, s92, v234\n" \
"v_mfma_f32_16x16x32_bf16 v[64:67], v[0:3], v[192:195], v[64:67]\n" \
"s_add_u32 s46, s46, 0x100\n" \
"s_addc_u32 s47, s47, 0\n" \
"ds_read_b128 v[216:219], v230 offset:6144\n" \
"v_mfma_f32_16x16x32_bf16 v[64:67], v[4:7], v[196:199], v[64:67]\n" \
"ds_read_b128 v[220:223], v231 offset:6144\n" \
"v_mul_f32 v235, s92, v235\n" \
"v_mfma_f32_16x16x32_bf16 v[68:71], v[8:11], v[192:195], v[68:71]\n" \
"ds_read_b64_tr_b16 v[32:33], v232 offset:1024\n" \
"v_mul_f32 v236, s92, v236\n" \
"v_mfma_f32_16x16x32_bf16 v[68:71], v[12:15], v[196:199], v[68:71]\n" \
"buffer_store_dwordx4 a[232:235], v249, s[56:59], s90 offen offset:2304 sc1\n" \
"v_mul_f32 v237, s92, v237\n" \
"v_cvt_pk_bf16_f32 v234, v234, v235\n" \
"v_cvt_pk_bf16_f32 v236, v236, v237\n" \
"v_accvgpr_write_b32 a138, v234\n" \
"v_mfma_f32_16x16x32_bf16 v[72:75], v[16:19], v[192:195], v[72:75]\n" \
"s_add_u32 s90, s89, 0\n" \
"ds_read_b64_tr_b16 v[34:35], v232 offset:3072\n" \
"v_accvgpr_write_b32 a139, v236\n" \
"v_mfma_f32_16x16x32_bf16 v[72:75], v[20:23], v[196:199], v[72:75]\n" \
"s_waitcnt lgkmcnt(3)\n" \
"ds_read_b64_tr_b16 v[36:37], v232 offset:9216\n" \
"v_accvgpr_read_b32 v234, a168\n" \
"v_mfma_f32_16x16x32_bf16 v[76:79], v[24:27], v[192:195], v[76:79]\n" \
"ds_read_b64_tr_b16 v[38:39], v232 offset:11264\n" \
"v_accvgpr_read_b32 v235, a169\n" \
"v_mfma_f32_16x16x32_bf16 v[76:79], v[28:31], v[196:199], v[76:79]\n" \
"buffer_store_dwordx4 a[208:211], v248, s[56:59], s90 offen offset:0 sc1\n" \
"v_accvgpr_read_b32 v236, a170\n" \
"v_accvgpr_read_b32 v237, a171\n" \
"v_mul_f32 v234, s92, v234\n" \
"v_mul_f32 v235, s92, v235\n" \
"v_mfma_f32_16x16x32_bf16 v[108:111], v[24:27], v[200:203], v[108:111]\n" \
"ds_read_b64_tr_b16 v[40:41], v233 offset:1024\n" \
"v_mul_f32 v236, s92, v236\n" \
"v_mfma_f32_16x16x32_bf16 v[108:111], v[28:31], v[204:207], v[108:111]\n" \
"ds_read_b64_tr_b16 v[42:43], v233 offset:3072\n" \
"v_mul_f32 v237, s92, v237\n" \
"v_mfma_f32_16x16x32_bf16 v[104:107], v[16:19], v[200:203], v[104:107]\n" \
"ds_read_b64_tr_b16 v[44:45], v233 offset:9216\n" \
"v_cvt_pk_bf16_f32 v234, v234, v235\n" \
"v_mfma_f32_16x16x32_bf16 v[104:107], v[20:23], v[204:207], v[104:107]\n" \
"v_cvt_pk_bf16_f32 v236, v236, v237\n" \
"v_accvgpr_write_b32 a168, v234\n" \
"v_mfma_f32_16x16x32_bf16 v[100:103], v[8:11], v[200:203], v[100:103]\n" \
"ds_read_b64_tr_b16 v[46:47], v233 offset:11264\n" \
"v_accvgpr_write_b32 a169, v236\n" \
"v_mfma_f32_16x16x32_bf16 v[100:103], v[12:15], v[204:207], v[100:103]\n" \
"ds_read_b64_tr_b16 v[48:49], v232 offset:1536\n" \
"v_accvgpr_read_b32 v234, a172\n" \
"v_mfma_f32_16x16x32_bf16 v[96:99], v[0:3], v[200:203], v[96:99]\n" \
"s_add_u32 m0, s95, 20480\n" \
"ds_read_b64_tr_b16 v[50:51], v232 offset:3584\n" \
"v_accvgpr_read_b32 v235, a173\n" \
"v_mfma_f32_16x16x32_bf16 v[96:99], v[4:7], v[204:207], v[96:99]\n" \
"global_load_lds_dwordx4 v228, s[44:45] offset:-4096\n" \
"v_accvgpr_read_b32 v236, a174\n" \
"v_mfma_f32_16x16x32_bf16 v[128:131], v[0:3], v[208:211], v[128:131]\n" \
"ds_read_b64_tr_b16 v[52:53], v232 offset:9728\n" \
"v_accvgpr_read_b32 v237, a175\n" \
"v_mfma_f32_16x16x32_bf16 v[128:131], v[4:7], v[212:215], v[128:131]\n" \
"ds_read_b64_tr_b16 v[54:55], v232 offset:11776\n" \
"v_mul_f32 v234, s92, v234\n" \
"v_mfma_f32_16x16x32_bf16 v[132:135], v[8:11], v[208:211], v[132:135]\n" \
"ds_read_b64_tr_b16 v[56:57], v233 offset:1536\n" \
"v_mul_f32 v235, s92, v235\n" \
"v_mfma_f32_16x16x32_bf16 v[132:135], v[12:15], v[212:215], v[132:135]\n" \
"global_load_lds_dwordx4 v229, s[44:45] offset:-3072\n" \
"v_mul_f32 v236, s92, v236\n" \
"v_mfma_f32_16x16x32_bf16 v[136:139], v[16:19], v[208:211], v[136:139]\n" \
"s_add_u32 s44, s44, 0x80000\n" \
"s_addc_u32 s45, s45, 0\n" \
"ds_read_b64_tr_b16 v[58:59], v233 offset:3584\n" \
"v_mfma_f32_16x16x32_bf16 v[136:139], v[20:23], v[212:215], v[136:139]\n" \
"ds_read_b64_tr_b16 v[60:61], v233 offset:9728\n" \
"v_mul_f32 v237, s92, v237\n" \
"v_mfma_f32_16x16x32_bf16 v[140:143], v[24:27], v[208:211], v[140:143]\n" \
"ds_read_b64_tr_b16 v[62:63], v233 offset:11776\n" \
"v_cvt_pk_bf16_f32 v234, v234, v235\n" \
"v_mfma_f32_16x16x32_bf16 v[140:143], v[28:31], v[212:215], v[140:143]\n" \
"s_waitcnt lgkmcnt(3)\n" \
"global_load_lds_dwordx4 v228, s[42:43] offset:-2048\n" \
"v_cvt_pk_bf16_f32 v236, v236, v237\n" \
"v_mfma_f32_16x16x32_bf16 v[172:175], v[24:27], v[216:219], v[172:175]\n" \
"v_add_u32 v230, 0x8000, v230\n" \
"v_add_u32 v231, 0x8000, v231\n" \
"v_mfma_f32_16x16x32_bf16 v[172:175], v[28:31], v[220:223], v[172:175]\n" \
"v_add_u32 v232, 0x4000, v232\n" \
"v_add_u32 v233, 0x4000, v233\n" \
"v_mfma_f32_16x16x32_bf16 v[168:171], v[16:19], v[216:219], v[168:171]\n" \
"v_accvgpr_write_b32 a170, v234\n" \
"v_accvgpr_write_b32 a171, v236\n" \
"v_mfma_f32_16x16x32_bf16 v[168:171], v[20:23], v[220:223], v[168:171]\n" \
"global_load_lds_dwordx4 v229, s[42:43] offset:-1024\n" \
"v_accvgpr_read_b32 v234, a144\n" \
"v_mfma_f32_16x16x32_bf16 v[164:167], v[8:11], v[216:219], v[164:167]\n" \
"s_add_u32 s42, s42, 0x80000\n" \
"s_addc_u32 s43, s43, 0\n" \
"v_accvgpr_read_b32 v235, a145\n" \
"v_mfma_f32_16x16x32_bf16 v[164:167], v[12:15], v[220:223], v[164:167]\n" \
"v_accvgpr_read_b32 v236, a146\n" \
"v_accvgpr_read_b32 v237, a147\n" \
"v_mfma_f32_16x16x32_bf16 v[160:163], v[0:3], v[216:219], v[160:163]\n" \
"s_waitcnt vmcnt(19) lgkmcnt(0)\n" \
"v_cvt_pk_bf16_f32 v234, v234, v235\n" \
"v_cvt_pk_bf16_f32 v236, v236, v237\n" \
"v_mfma_f32_16x16x32_bf16 v[160:163], v[4:7], v[220:223], v[160:163]\n" \
"s_barrier\n" \
"v_mfma_f32_16x16x32_bf16 v[80:83], v[32:35], v[192:195], v[80:83]\n" \
"ds_read_b64_tr_b16 v[0:1], v232 offset:0\n" \
"v_accvgpr_write_b32 a144, v234\n" \
"v_mfma_f32_16x16x32_bf16 v[80:83], v[36:39], v[196:199], v[80:83]\n" \
"ds_read_b64_tr_b16 v[2:3], v232 offset:2048\n" \
"v_accvgpr_write_b32 a145, v236\n" \
"v_mfma_f32_16x16x32_bf16 v[84:87], v[40:43], v[192:195], v[84:87]\n" \
"s_add_u32 m0, s87, 102400\n" \
"ds_read_b64_tr_b16 v[4:5], v232 offset:8192\n" \
"v_accvgpr_read_b32 v234, a176\n" \
"v_mfma_f32_16x16x32_bf16 v[84:87], v[44:47], v[196:199], v[84:87]\n" \
"global_load_lds_dwordx4 v224, s[48:49] offset:-4096\n" \
"v_accvgpr_read_b32 v235, a177\n" \
"v_mfma_f32_16x16x32_bf16 v[88:91], v[48:51], v[192:195], v[88:91]\n" \
"ds_read_b64_tr_b16 v[6:7], v232 offset:10240\n" \
"v_accvgpr_read_b32 v236, a178\n" \
"v_mfma_f32_16x16x32_bf16 v[88:91], v[52:55], v[196:199], v[88:91]\n" \
"ds_read_b64_tr_b16 v[8:9], v233 offset:0\n" \
"v_accvgpr_read_b32 v237, a179\n" \
"v_mfma_f32_16x16x32_bf16 v[92:95], v[56:59], v[192:195], v[92:95]\n" \
"ds_read_b64_tr_b16 v[10:11], v233 offset:2048\n" \
"v_cvt_pk_bf16_f32 v234, v234, v235\n" \
"v_mfma_f32_16x16x32_bf16 v[92:95], v[60:63], v[196:199], v[92:95]\n" \
"global_load_lds_dwordx4 v225, s[48:49] offset:-3072\n" \
"v_cvt_pk_bf16_f32 v236, v236, v237\n" \
"v_mfma_f32_16x16x32_bf16 v[124:127], v[56:59], v[200:203], v[124:127]\n" \
"ds_read_b128 v[192:195], v230 offset:0\n" \
"v_accvgpr_write_b32 a146, v234\n" \
"v_mfma_f32_16x16x32_bf16 v[124:127], v[60:63], v[204:207], v[124:127]\n" \
"ds_read_b128 v[196:199], v231 offset:0\n" \
"v_accvgpr_write_b32 a147, v236\n" \
"v_mfma_f32_16x16x32_bf16 v[120:123], v[48:51], v[200:203], v[120:123]\n" \
"ds_read_b64_tr_b16 v[12:13], v233 offset:8192\n" \
"v_accvgpr_read_b32 v234, a148\n" \
"v_mfma_f32_16x16x32_bf16 v[120:123], v[52:55], v[204:207], v[120:123]\n" \
"global_load_lds_dwordx4 v226, s[48:49] offset:-2048\n" \
"v_accvgpr_read_b32 v235, a149\n" \
"v_mfma_f32_16x16x32_bf16 v[116:119], v[40:43], v[200:203], v[116:119]\n" \
"ds_read_b64_tr_b16 v[14:15], v233 offset:10240\n" \
"v_accvgpr_read_b32 v236, a150\n" \
"v_mfma_f32_16x16x32_bf16 v[116:119], v[44:47], v[204:207], v[116:119]\n" \
"ds_read_b64_tr_b16 v[16:17], v232 offset:512\n" \
"v_accvgpr_read_b32 v237, a151\n" \
"v_mfma_f32_16x16x32_bf16 v[112:115], v[32:35], v[200:203], v[112:115]\n" \
"ds_read_b64_tr_b16 v[18:19], v232 offset:2560\n" \
"v_cvt_pk_bf16_f32 v234, v234, v235\n" \
"v_mfma_f32_16x16x32_bf16 v[112:115], v[36:39], v[204:207], v[112:115]\n" \
"global_load_lds_dwordx4 v227, s[48:49] offset:-1024\n" \
"v_cvt_pk_bf16_f32 v236, v236, v237\n" \
"v_mfma_f32_16x16x32_bf16 v[144:147], v[32:35], v[208:211], v[144:147]\n" \
"s_add_u32 s48, s48, 0x80\n" \
"s_addc_u32 s49, s49, 0\n" \
"ds_read_b128 v[200:203], v230 offset:2048\n" \
"v_mfma_f32_16x16x32_bf16 v[144:147], v[36:39], v[212:215], v[144:147]\n" \
"ds_read_b128 v[204:207], v231 offset:2048\n" \
"v_accvgpr_write_b32 a148, v234\n" \
"v_mfma_f32_16x16x32_bf16 v[148:151], v[40:43], v[208:211], v[148:151]\n" \
"ds_read_b64_tr_b16 v[20:21], v232 offset:8704\n" \
"v_accvgpr_write_b32 a149, v236\n" \
"v_mfma_f32_16x16x32_bf16 v[148:151], v[44:47], v[212:215], v[148:151]\n" \
"global_load_lds_dwordx4 v224, s[46:47] offset:0\n" \
"v_accvgpr_read_b32 v234, a180\n" \
"v_mfma_f32_16x16x32_bf16 v[152:155], v[48:51], v[208:211], v[152:155]\n" \
"ds_read_b64_tr_b16 v[22:23], v232 offset:10752\n" \
"v_accvgpr_read_b32 v235, a181\n" \
"v_mfma_f32_16x16x32_bf16 v[152:155], v[52:55], v[212:215], v[152:155]\n" \
"ds_read_b64_tr_b16 v[24:25], v233 offset:512\n" \
"v_accvgpr_read_b32 v236, a182\n" \
"v_mfma_f32_16x16x32_bf16 v[156:159], v[56:59], v[208:211], v[156:159]\n" \
"ds_read_b64_tr_b16 v[26:27], v233 offset:2560\n" \
"v_accvgpr_read_b32 v237, a183\n" \
"v_mfma_f32_16x16x32_bf16 v[156:159], v[60:63], v[212:215], v[156:159]\n" \
"global_load_lds_dwordx4 v225, s[46:47] offset:1024\n" \
"v_cvt_pk_bf16_f32 v234, v234, v235\n" \
"v_mfma_f32_16x16x32_bf16 v[188:191], v[56:59], v[216:219], v[188:191]\n" \
"ds_read_b128 v[208:211], v230 offset:4096\n" \
"v_cvt_pk_bf16_f32 v236, v236, v237\n" \
"v_mfma_f32_16x16x32_bf16 v[188:191], v[60:63], v[220:223], v[188:191]\n" \
"ds_read_b128 v[212:215], v231 offset:4096\n" \
"v_accvgpr_write_b32 a150, v234\n" \
"v_mfma_f32_16x16x32_bf16 v[184:187], v[48:51], v[216:219], v[184:187]\n" \
"ds_read_b64_tr_b16 v[28:29], v233 offset:8704\n" \
"v_accvgpr_write_b32 a151, v236\n" \
"v_mfma_f32_16x16x32_bf16 v[184:187], v[52:55], v[220:223], v[184:187]\n" \
"global_load_lds_dwordx4 v226, s[46:47] offset:2048\n" \
"v_accvgpr_read_b32 v234, a152\n" \
"v_mfma_f32_16x16x32_bf16 v[180:183], v[40:43], v[216:219], v[180:183]\n" \
"Lpg2768_%=:\n" \
"s_cbranch_execz Lpg4933_%=\n" \
"ds_read_b64_tr_b16 v[30:31], v233 offset:10752\n" \
"v_accvgpr_read_b32 v235, a153\n" \
"v_mfma_f32_16x16x32_bf16 v[180:183], v[44:47], v[220:223], v[180:183]\n" \
"v_accvgpr_read_b32 v236, a154\n" \
"v_accvgpr_read_b32 v237, a155\n" \
"v_mfma_f32_16x16x32_bf16 v[176:179], v[32:35], v[216:219], v[176:179]\n" \
"v_cvt_pk_bf16_f32 v234, v234, v235\n" \
"v_cvt_pk_bf16_f32 v236, v236, v237\n" \
"v_mfma_f32_16x16x32_bf16 v[176:179], v[36:39], v[220:223], v[176:179]\n" \
"s_waitcnt lgkmcnt(1)\n" \
"global_load_lds_dwordx4 v227, s[46:47] offset:3072\n" \
"v_accvgpr_write_b32 a152, v234\n" \
"v_mfma_f32_16x16x32_bf16 v[64:67], v[0:3], v[192:195], v[64:67]\n" \
"s_add_u32 s46, s46, 0x80\n" \
"s_addc_u32 s47, s47, 0\n" \
"ds_read_b128 v[216:219], v230 offset:6144\n" \
"v_mfma_f32_16x16x32_bf16 v[64:67], v[4:7], v[196:199], v[64:67]\n" \
"ds_read_b128 v[220:223], v231 offset:6144\n" \
"v_accvgpr_write_b32 a153, v236\n" \
"v_mfma_f32_16x16x32_bf16 v[68:71], v[8:11], v[192:195], v[68:71]\n" \
"ds_read_b64_tr_b16 v[32:33], v232 offset:1024\n" \
"v_accvgpr_read_b32 v234, a184\n" \
"v_mfma_f32_16x16x32_bf16 v[68:71], v[12:15], v[196:199], v[68:71]\n" \
"buffer_store_dwordx4 a[212:215], v248, s[56:59], s90 offen offset:1024 sc1\n" \
"v_accvgpr_read_b32 v235, a185\n" \
"v_accvgpr_read_b32 v236, a186\n" \
"v_accvgpr_read_b32 v237, a187\n" \
"v_cvt_pk_bf16_f32 v234, v234, v235\n" \
"v_mfma_f32_16x16x32_bf16 v[72:75], v[16:19], v[192:195], v[72:75]\n" \
"ds_read_b64_tr_b16 v[34:35], v232 offset:3072\n" \
"v_cvt_pk_bf16_f32 v236, v236, v237\n" \
"v_mfma_f32_16x16x32_bf16 v[72:75], v[20:23], v[196:199], v[72:75]\n" \
"s_waitcnt lgkmcnt(3)\n" \
"ds_read_b64_tr_b16 v[36:37], v232 offset:9216\n" \
"v_accvgpr_write_b32 a154, v234\n" \
"v_mfma_f32_16x16x32_bf16 v[76:79], v[24:27], v[192:195], v[76:79]\n" \
"ds_read_b64_tr_b16 v[38:39], v232 offset:11264\n" \
"v_accvgpr_write_b32 a155, v236\n" \
"v_mfma_f32_16x16x32_bf16 v[76:79], v[28:31], v[196:199], v[76:79]\n" \
"v_accvgpr_read_b32 v234, a156\n" \
"v_accvgpr_read_b32 v235, a157\n" \
"v_mfma_f32_16x16x32_bf16 v[108:111], v[24:27], v[200:203], v[108:111]\n" \
"ds_read_b64_tr_b16 v[40:41], v233 offset:1024\n" \
"v_accvgpr_read_b32 v236, a158\n" \
"v_mfma_f32_16x16x32_bf16 v[108:111], v[28:31], v[204:207], v[108:111]\n" \
"ds_read_b64_tr_b16 v[42:43], v233 offset:3072\n" \
"v_accvgpr_read_b32 v237, a159\n" \
"v_mfma_f32_16x16x32_bf16 v[104:107], v[16:19], v[200:203], v[104:107]\n" \
"ds_read_b64_tr_b16 v[44:45], v233 offset:9216\n" \
"v_cvt_pk_bf16_f32 v234, v234, v235\n" \
"v_mfma_f32_16x16x32_bf16 v[104:107], v[20:23], v[204:207], v[104:107]\n" \
"v_cvt_pk_bf16_f32 v236, v236, v237\n" \
"v_accvgpr_write_b32 a156, v234\n" \
"v_mfma_f32_16x16x32_bf16 v[100:103], v[8:11], v[200:203], v[100:103]\n" \
"ds_read_b64_tr_b16 v[46:47], v233 offset:11264\n" \
"v_accvgpr_write_b32 a157, v236\n" \
"v_mfma_f32_16x16x32_bf16 v[100:103], v[12:15], v[204:207], v[100:103]\n" \
"ds_read_b64_tr_b16 v[48:49], v232 offset:1536\n" \
"v_accvgpr_read_b32 v234, a188\n" \
"v_mfma_f32_16x16x32_bf16 v[96:99], v[0:3], v[200:203], v[96:99]\n" \
"s_add_u32 m0, s95, 36864\n" \
"ds_read_b64_tr_b16 v[50:51], v232 offset:3584\n" \
"v_accvgpr_read_b32 v235, a189\n" \
"v_mfma_f32_16x16x32_bf16 v[96:99], v[4:7], v[204:207], v[96:99]\n" \
"global_load_lds_dwordx4 v228, s[44:45] offset:-4096\n" \
"v_accvgpr_read_b32 v236, a190\n" \
"v_mfma_f32_16x16x32_bf16 v[128:131], v[0:3], v[208:211], v[128:131]\n" \
"ds_read_b64_tr_b16 v[52:53], v232 offset:9728\n" \
"v_accvgpr_read_b32 v237, a191\n" \
"v_mfma_f32_16x16x32_bf16 v[128:131], v[4:7], v[212:215], v[128:131]\n" \
"ds_read_b64_tr_b16 v[54:55], v232 offset:11776\n" \
"v_cvt_pk_bf16_f32 v234, v234, v235\n" \
"v_mfma_f32_16x16x32_bf16 v[132:135], v[8:11], v[208:211], v[132:135]\n" \
"ds_read_b64_tr_b16 v[56:57], v233 offset:1536\n" \
"v_cvt_pk_bf16_f32 v236, v236, v237\n" \
"v_mfma_f32_16x16x32_bf16 v[132:135], v[12:15], v[212:215], v[132:135]\n" \
"global_load_lds_dwordx4 v229, s[44:45] offset:-3072\n" \
"v_accvgpr_write_b32 a158, v234\n" \
"v_mfma_f32_16x16x32_bf16 v[136:139], v[16:19], v[208:211], v[136:139]\n" \
"s_add_u32 s44, s44, 0x80000\n" \
"s_addc_u32 s45, s45, 0\n" \
"ds_read_b64_tr_b16 v[58:59], v233 offset:3584\n" \
"v_mfma_f32_16x16x32_bf16 v[136:139], v[20:23], v[212:215], v[136:139]\n" \
"ds_read_b64_tr_b16 v[60:61], v233 offset:9728\n" \
"v_accvgpr_write_b32 a159, v236\n" \
"v_mfma_f32_16x16x32_bf16 v[140:143], v[24:27], v[208:211], v[140:143]\n" \
"ds_read_b64_tr_b16 v[62:63], v233 offset:11776\n" \
"v_accvgpr_read_b32 v234, a64\n" \
"v_mfma_f32_16x16x32_bf16 v[140:143], v[28:31], v[212:215], v[140:143]\n" \
"s_waitcnt lgkmcnt(3)\n" \
"global_load_lds_dwordx4 v228, s[42:43] offset:-2048\n" \
"v_accvgpr_read_b32 v235, a65\n" \
"v_mfma_f32_16x16x32_bf16 v[172:175], v[24:27], v[216:219], v[172:175]\n" \
"v_add_u32 v230, 0xffff0000, v230\n" \
"v_add_u32 v231, 0xffff0000, v231\n" \
"v_mfma_f32_16x16x32_bf16 v[172:175], v[28:31], v[220:223], v[172:175]\n" \
"v_add_u32 v232, 0xffff4000, v232\n" \
"v_add_u32 v233, 0xffff4000, v233\n" \
"v_mfma_f32_16x16x32_bf16 v[168:171], v[16:19], v[216:219], v[168:171]\n" \
"v_accvgpr_read_b32 v236, a66\n" \
"v_accvgpr_read_b32 v237, a67\n" \
"v_mfma_f32_16x16x32_bf16 v[168:171], v[20:23], v[220:223], v[168:171]\n" \
"global_load_lds_dwordx4 v229, s[42:43] offset:-1024\n" \
"v_mul_f32 v234, s92, v234\n" \
"v_mfma_f32_16x16x32_bf16 v[164:167], v[8:11], v[216:219], v[164:167]\n" \
"s_add_u32 s42, s42, 0x80000\n" \
"s_addc_u32 s43, s43, 0\n" \
"v_mul_f32 v235, s92, v235\n" \
"v_mfma_f32_16x16x32_bf16 v[164:167], v[12:15], v[220:223], v[164:167]\n" \
"v_mul_f32 v236, s92, v236\n" \
"v_mul_f32 v237, s92, v237\n" \
"v_mfma_f32_16x16x32_bf16 v[160:163], v[0:3], v[216:219], v[160:163]\n" \
"s_waitcnt vmcnt(19) lgkmcnt(0)\n" \
"v_cvt_pk_bf16_f32 v234, v234, v235\n" \
"v_cvt_pk_bf16_f32 v236, v236, v237\n" \
"v_mfma_f32_16x16x32_bf16 v[160:163], v[4:7], v[220:223], v[160:163]\n" \
"s_barrier\n" \
"v_mfma_f32_16x16x32_bf16 v[80:83], v[32:35], v[192:195], v[80:83]\n" \
"ds_read_b64_tr_b16 v[0:1], v232 offset:0\n" \
"v_accvgpr_write_b32 a64, v234\n" \
"v_mfma_f32_16x16x32_bf16 v[80:83], v[36:39], v[196:199], v[80:83]\n" \
"ds_read_b64_tr_b16 v[2:3], v232 offset:2048\n" \
"v_accvgpr_write_b32 a65, v236\n" \
"v_mfma_f32_16x16x32_bf16 v[84:87], v[40:43], v[192:195], v[84:87]\n" \
"s_add_u32 m0, s87, 135168\n" \
"ds_read_b64_tr_b16 v[4:5], v232 offset:8192\n" \
"v_accvgpr_read_b32 v234, a68\n" \
"v_mfma_f32_16x16x32_bf16 v[84:87], v[44:47], v[196:199], v[84:87]\n" \
"global_load_lds_dwordx4 v224, s[48:49] offset:-4096\n" \
"v_accvgpr_read_b32 v235, a69\n" \
"v_mfma_f32_16x16x32_bf16 v[88:91], v[48:51], v[192:195], v[88:91]\n" \
"ds_read_b64_tr_b16 v[6:7], v232 offset:10240\n" \
"v_accvgpr_read_b32 v236, a70\n" \
"v_mfma_f32_16x16x32_bf16 v[88:91], v[52:55], v[196:199], v[88:91]\n" \
"ds_read_b64_tr_b16 v[8:9], v233 offset:0\n" \
"v_accvgpr_read_b32 v237, a71\n" \
"v_mfma_f32_16x16x32_bf16 v[92:95], v[56:59], v[192:195], v[92:95]\n" \
"ds_read_b64_tr_b16 v[10:11], v233 offset:2048\n" \
"v_mul_f32 v234, s92, v234\n" \
"v_mfma_f32_16x16x32_bf16 v[92:95], v[60:63], v[196:199], v[92:95]\n" \
"global_load_lds_dwordx4 v225, s[48:49] offset:-3072\n" \
"v_mul_f32 v235, s92, v235\n" \
"v_mfma_f32_16x16x32_bf16 v[124:127], v[56:59], v[200:203], v[124:127]\n" \
"ds_read_b128 v[192:195], v230 offset:0\n" \
"v_mul_f32 v236, s92, v236\n" \
"v_mfma_f32_16x16x32_bf16 v[124:127], v[60:63], v[204:207], v[124:127]\n" \
"ds_read_b128 v[196:199], v231 offset:0\n" \
"v_mul_f32 v237, s92, v237\n" \
"v_mfma_f32_16x16x32_bf16 v[120:123], v[48:51], v[200:203], v[120:123]\n" \
"ds_read_b64_tr_b16 v[12:13], v233 offset:8192\n" \
"v_cvt_pk_bf16_f32 v234, v234, v235\n" \
"v_mfma_f32_16x16x32_bf16 v[120:123], v[52:55], v[204:207], v[120:123]\n" \
"global_load_lds_dwordx4 v226, s[48:49] offset:-2048\n" \
"v_cvt_pk_bf16_f32 v236, v236, v237\n" \
"v_mfma_f32_16x16x32_bf16 v[116:119], v[40:43], v[200:203], v[116:119]\n" \
"ds_read_b64_tr_b16 v[14:15], v233 offset:10240\n" \
"v_accvgpr_write_b32 a66, v234\n" \
"v_mfma_f32_16x16x32_bf16 v[116:119], v[44:47], v[204:207], v[116:119]\n" \
"ds_read_b64_tr_b16 v[16:17], v232 offset:512\n" \
"v_accvgpr_write_b32 a67, v236\n" \
"v_mfma_f32_16x16x32_bf16 v[112:115], v[32:35], v[200:203], v[112:115]\n" \
"ds_read_b64_tr_b16 v[18:19], v232 offset:2560\n" \
"v_accvgpr_read_b32 v234, a96\n" \
"v_mfma_f32_16x16x32_bf16 v[112:115], v[36:39], v[204:207], v[112:115]\n" \
"global_load_lds_dwordx4 v227, s[48:49] offset:-1024\n" \
"v_accvgpr_read_b32 v235, a97\n" \
"v_mfma_f32_16x16x32_bf16 v[144:147], v[32:35], v[208:211], v[144:147]\n" \
"s_add_u32 s48, s48, 0x80\n" \
"s_addc_u32 s49, s49, 0\n" \
"ds_read_b128 v[200:203], v230 offset:2048\n" \
"v_mfma_f32_16x16x32_bf16 v[144:147], v[36:39], v[212:215], v[144:147]\n" \
"ds_read_b128 v[204:207], v231 offset:2048\n" \
"v_accvgpr_read_b32 v236, a98\n" \
"v_mfma_f32_16x16x32_bf16 v[148:151], v[40:43], v[208:211], v[148:151]\n" \
"ds_read_b64_tr_b16 v[20:21], v232 offset:8704\n" \
"v_accvgpr_read_b32 v237, a99\n" \
"v_mfma_f32_16x16x32_bf16 v[148:151], v[44:47], v[212:215], v[148:151]\n" \
"global_load_lds_dwordx4 v224, s[46:47] offset:0\n" \
"v_mul_f32 v234, s92, v234\n" \
"v_mfma_f32_16x16x32_bf16 v[152:155], v[48:51], v[208:211], v[152:155]\n" \
"ds_read_b64_tr_b16 v[22:23], v232 offset:10752\n" \
"v_mul_f32 v235, s92, v235\n" \
"v_mfma_f32_16x16x32_bf16 v[152:155], v[52:55], v[212:215], v[152:155]\n" \
"ds_read_b64_tr_b16 v[24:25], v233 offset:512\n" \
"v_mul_f32 v236, s92, v236\n" \
"v_mfma_f32_16x16x32_bf16 v[156:159], v[56:59], v[208:211], v[156:159]\n" \
"ds_read_b64_tr_b16 v[26:27], v233 offset:2560\n" \
"v_mul_f32 v237, s92, v237\n" \
"v_mfma_f32_16x16x32_bf16 v[156:159], v[60:63], v[212:215], v[156:159]\n" \
"global_load_lds_dwordx4 v225, s[46:47] offset:1024\n" \
"v_cvt_pk_bf16_f32 v234, v234, v235\n" \
"v_mfma_f32_16x16x32_bf16 v[188:191], v[56:59], v[216:219], v[188:191]\n" \
"ds_read_b128 v[208:211], v230 offset:4096\n" \
"v_cvt_pk_bf16_f32 v236, v236, v237\n" \
"v_mfma_f32_16x16x32_bf16 v[188:191], v[60:63], v[220:223], v[188:191]\n" \
"ds_read_b128 v[212:215], v231 offset:4096\n" \
"v_accvgpr_write_b32 a96, v234\n" \
"v_mfma_f32_16x16x32_bf16 v[184:187], v[48:51], v[216:219], v[184:187]\n" \
"ds_read_b64_tr_b16 v[28:29], v233 offset:8704\n" \
"v_accvgpr_write_b32 a97, v236\n" \
"v_mfma_f32_16x16x32_bf16 v[184:187], v[52:55], v[220:223], v[184:187]\n" \
"global_load_lds_dwordx4 v226, s[46:47] offset:2048\n" \
"v_accvgpr_read_b32 v234, a100\n" \
"v_mfma_f32_16x16x32_bf16 v[180:183], v[40:43], v[216:219], v[180:183]\n" \
"ds_read_b64_tr_b16 v[30:31], v233 offset:10752\n" \
"v_accvgpr_read_b32 v235, a101\n" \
"v_mfma_f32_16x16x32_bf16 v[180:183], v[44:47], v[220:223], v[180:183]\n" \
"v_accvgpr_read_b32 v236, a102\n" \
"v_accvgpr_read_b32 v237, a103\n" \
"v_mfma_f32_16x16x32_bf16 v[176:179], v[32:35], v[216:219], v[176:179]\n" \
"v_mul_f32 v234, s92, v234\n" \
"v_mul_f32 v235, s92, v235\n" \
"v_mfma_f32_16x16x32_bf16 v[176:179], v[36:39], v[220:223], v[176:179]\n" \
"s_waitcnt lgkmcnt(1)\n" \
"global_load_lds_dwordx4 v227, s[46:47] offset:3072\n" \
"v_mul_f32 v236, s92, v236\n" \
"v_mfma_f32_16x16x32_bf16 v[64:67], v[0:3], v[192:195], v[64:67]\n" \
"s_add_u32 s46, s46, 0x80\n" \
"s_addc_u32 s47, s47, 0\n" \
"ds_read_b128 v[216:219], v230 offset:6144\n" \
"v_mfma_f32_16x16x32_bf16 v[64:67], v[4:7], v[196:199], v[64:67]\n" \
"ds_read_b128 v[220:223], v231 offset:6144\n" \
"v_mul_f32 v237, s92, v237\n" \
"v_mfma_f32_16x16x32_bf16 v[68:71], v[8:11], v[192:195], v[68:71]\n" \
"ds_read_b64_tr_b16 v[32:33], v232 offset:1024\n" \
"v_cvt_pk_bf16_f32 v234, v234, v235\n" \
"v_mfma_f32_16x16x32_bf16 v[68:71], v[12:15], v[196:199], v[68:71]\n" \
"buffer_store_dwordx4 a[216:219], v248, s[56:59], s90 offen offset:2048 sc1\n" \
"v_cvt_pk_bf16_f32 v236, v236, v237\n" \
"v_accvgpr_write_b32 a98, v234\n" \
"v_accvgpr_write_b32 a99, v236\n" \
"v_accvgpr_read_b32 v234, a72\n" \
"v_mfma_f32_16x16x32_bf16 v[72:75], v[16:19], v[192:195], v[72:75]\n" \
"ds_read_b64_tr_b16 v[34:35], v232 offset:3072\n" \
"v_accvgpr_read_b32 v235, a73\n" \
"v_mfma_f32_16x16x32_bf16 v[72:75], v[20:23], v[196:199], v[72:75]\n" \
"s_waitcnt lgkmcnt(3)\n" \
"ds_read_b64_tr_b16 v[36:37], v232 offset:9216\n" \
"v_accvgpr_read_b32 v236, a74\n" \
"v_mfma_f32_16x16x32_bf16 v[76:79], v[24:27], v[192:195], v[76:79]\n" \
"ds_read_b64_tr_b16 v[38:39], v232 offset:11264\n" \
"v_accvgpr_read_b32 v237, a75\n" \
"v_mfma_f32_16x16x32_bf16 v[76:79], v[28:31], v[196:199], v[76:79]\n" \
"v_mul_f32 v234, s92, v234\n" \
"v_mul_f32 v235, s92, v235\n" \
"v_mfma_f32_16x16x32_bf16 v[108:111], v[24:27], v[200:203], v[108:111]\n" \
"ds_read_b64_tr_b16 v[40:41], v233 offset:1024\n" \
"v_mul_f32 v236, s92, v236\n" \
"v_mfma_f32_16x16x32_bf16 v[108:111], v[28:31], v[204:207], v[108:111]\n" \
"ds_read_b64_tr_b16 v[42:43], v233 offset:3072\n" \
"v_mul_f32 v237, s92, v237\n" \
"v_mfma_f32_16x16x32_bf16 v[104:107], v[16:19], v[200:203], v[104:107]\n" \
"ds_read_b64_tr_b16 v[44:45], v233 offset:9216\n" \
"v_cvt_pk_bf16_f32 v234, v234, v235\n" \
"v_mfma_f32_16x16x32_bf16 v[104:107], v[20:23], v[204:207], v[104:107]\n" \
"v_cvt_pk_bf16_f32 v236, v236, v237\n" \
"v_accvgpr_write_b32 a72, v234\n" \
"v_mfma_f32_16x16x32_bf16 v[100:103], v[8:11], v[200:203], v[100:103]\n" \
"ds_read_b64_tr_b16 v[46:47], v233 offset:11264\n" \
"v_accvgpr_write_b32 a73, v236\n" \
"v_mfma_f32_16x16x32_bf16 v[100:103], v[12:15], v[204:207], v[100:103]\n" \
"ds_read_b64_tr_b16 v[48:49], v232 offset:1536\n" \
"v_accvgpr_read_b32 v234, a76\n" \
"v_mfma_f32_16x16x32_bf16 v[96:99], v[0:3], v[200:203], v[96:99]\n" \
"s_add_u32 m0, s95, 53248\n" \
"ds_read_b64_tr_b16 v[50:51], v232 offset:3584\n" \
"v_accvgpr_read_b32 v235, a77\n" \
"v_mfma_f32_16x16x32_bf16 v[96:99], v[4:7], v[204:207], v[96:99]\n" \
"global_load_lds_dwordx4 v228, s[44:45] offset:-4096\n" \
"v_accvgpr_read_b32 v236, a78\n" \
"v_mfma_f32_16x16x32_bf16 v[128:131], v[0:3], v[208:211], v[128:131]\n" \
"ds_read_b64_tr_b16 v[52:53], v232 offset:9728\n" \
"v_accvgpr_read_b32 v237, a79\n" \
"v_mfma_f32_16x16x32_bf16 v[128:131], v[4:7], v[212:215], v[128:131]\n" \
"ds_read_b64_tr_b16 v[54:55], v232 offset:11776\n" \
"v_mul_f32 v234, s92, v234\n" \
"v_mfma_f32_16x16x32_bf16 v[132:135], v[8:11], v[208:211], v[132:135]\n" \
"ds_read_b64_tr_b16 v[56:57], v233 offset:1536\n" \
"v_mul_f32 v235, s92, v235\n" \
"v_mfma_f32_16x16x32_bf16 v[132:135], v[12:15], v[212:215], v[132:135]\n" \
"global_load_lds_dwordx4 v229, s[44:45] offset:-3072\n" \
"v_mul_f32 v236, s92, v236\n" \
"v_mfma_f32_16x16x32_bf16 v[136:139], v[16:19], v[208:211], v[136:139]\n" \
"s_add_u32 s44, s44, 0x80000\n" \
"s_addc_u32 s45, s45, 0\n" \
"ds_read_b64_tr_b16 v[58:59], v233 offset:3584\n" \
"v_mfma_f32_16x16x32_bf16 v[136:139], v[20:23], v[212:215], v[136:139]\n" \
"ds_read_b64_tr_b16 v[60:61], v233 offset:9728\n" \
"v_mul_f32 v237, s92, v237\n" \
"v_mfma_f32_16x16x32_bf16 v[140:143], v[24:27], v[208:211], v[140:143]\n" \
"ds_read_b64_tr_b16 v[62:63], v233 offset:11776\n" \
"v_cvt_pk_bf16_f32 v234, v234, v235\n" \
"v_mfma_f32_16x16x32_bf16 v[140:143], v[28:31], v[212:215], v[140:143]\n" \
"s_waitcnt lgkmcnt(3)\n" \
"global_load_lds_dwordx4 v228, s[42:43] offset:-2048\n" \
"v_cvt_pk_bf16_f32 v236, v236, v237\n" \
"v_mfma_f32_16x16x32_bf16 v[172:175], v[24:27], v[216:219], v[172:175]\n" \
"v_add_u32 v230, 0x8000, v230\n" \
"v_add_u32 v231, 0x8000, v231\n" \
"v_mfma_f32_16x16x32_bf16 v[172:175], v[28:31], v[220:223], v[172:175]\n" \
"v_add_u32 v232, 0x4000, v232\n" \
"v_add_u32 v233, 0x4000, v233\n" \
"v_mfma_f32_16x16x32_bf16 v[168:171], v[16:19], v[216:219], v[168:171]\n" \
"v_accvgpr_write_b32 a74, v234\n" \
"v_accvgpr_write_b32 a75, v236\n" \
"v_mfma_f32_16x16x32_bf16 v[168:171], v[20:23], v[220:223], v[168:171]\n" \
"global_load_lds_dwordx4 v229, s[42:43] offset:-1024\n" \
"v_accvgpr_read_b32 v234, a104\n" \
"v_mfma_f32_16x16x32_bf16 v[164:167], v[8:11], v[216:219], v[164:167]\n" \
"s_add_u32 s42, s42, 0x80000\n" \
"s_addc_u32 s43, s43, 0\n" \
"v_accvgpr_read_b32 v235, a105\n" \
"v_mfma_f32_16x16x32_bf16 v[164:167], v[12:15], v[220:223], v[164:167]\n" \
"v_accvgpr_read_b32 v236, a106\n" \
"v_accvgpr_read_b32 v237, a107\n" \
"v_mfma_f32_16x16x32_bf16 v[160:163], v[0:3], v[216:219], v[160:163]\n" \
"s_waitcnt vmcnt(18) lgkmcnt(0)\n" \
"v_mul_f32 v234, s92, v234\n" \
"v_mul_f32 v235, s92, v235\n" \
"v_mfma_f32_16x16x32_bf16 v[160:163], v[4:7], v[220:223], v[160:163]\n" \
"s_barrier\n" \
"v_mfma_f32_16x16x32_bf16 v[80:83], v[32:35], v[192:195], v[80:83]\n" \
"ds_read_b64_tr_b16 v[0:1], v232 offset:0\n" \
"v_mul_f32 v236, s92, v236\n" \
"v_mfma_f32_16x16x32_bf16 v[80:83], v[36:39], v[196:199], v[80:83]\n" \
"ds_read_b64_tr_b16 v[2:3], v232 offset:2048\n" \
"v_mul_f32 v237, s92, v237\n" \
"v_mfma_f32_16x16x32_bf16 v[84:87], v[40:43], v[192:195], v[84:87]\n" \
"s_add_u32 m0, s87, 69632\n" \
"ds_read_b64_tr_b16 v[4:5], v232 offset:8192\n" \
"v_cvt_pk_bf16_f32 v234, v234, v235\n" \
"v_mfma_f32_16x16x32_bf16 v[84:87], v[44:47], v[196:199], v[84:87]\n" \
"global_load_lds_dwordx4 v224, s[48:49] offset:-4096\n" \
"v_cvt_pk_bf16_f32 v236, v236, v237\n" \
"v_mfma_f32_16x16x32_bf16 v[88:91], v[48:51], v[192:195], v[88:91]\n" \
"ds_read_b64_tr_b16 v[6:7], v232 offset:10240\n" \
"v_accvgpr_write_b32 a104, v234\n" \
"v_mfma_f32_16x16x32_bf16 v[88:91], v[52:55], v[196:199], v[88:91]\n" \
"ds_read_b64_tr_b16 v[8:9], v233 offset:0\n" \
"v_accvgpr_write_b32 a105, v236\n" \
"v_mfma_f32_16x16x32_bf16 v[92:95], v[56:59], v[192:195], v[92:95]\n" \
"ds_read_b64_tr_b16 v[10:11], v233 offset:2048\n" \
"v_accvgpr_read_b32 v234, a108\n" \
"v_mfma_f32_16x16x32_bf16 v[92:95], v[60:63], v[196:199], v[92:95]\n" \
"global_load_lds_dwordx4 v225, s[48:49] offset:-3072\n" \
"v_accvgpr_read_b32 v235, a109\n" \
"v_mfma_f32_16x16x32_bf16 v[124:127], v[56:59], v[200:203], v[124:127]\n" \
"ds_read_b128 v[192:195], v230 offset:0\n" \
"v_accvgpr_read_b32 v236, a110\n" \
"v_mfma_f32_16x16x32_bf16 v[124:127], v[60:63], v[204:207], v[124:127]\n" \
"ds_read_b128 v[196:199], v231 offset:0\n" \
"v_accvgpr_read_b32 v237, a111\n" \
"v_mfma_f32_16x16x32_bf16 v[120:123], v[48:51], v[200:203], v[120:123]\n" \
"ds_read_b64_tr_b16 v[12:13], v233 offset:8192\n" \
"v_mul_f32 v234, s92, v234\n" \
"v_mfma_f32_16x16x32_bf16 v[120:123], v[52:55], v[204:207], v[120:123]\n" \
"global_load_lds_dwordx4 v226, s[48:49] offset:-2048\n" \
"v_mul_f32 v235, s92, v235\n" \
"v_mfma_f32_16x16x32_bf16 v[116:119], v[40:43], v[200:203], v[116:119]\n" \
"ds_read_b64_tr_b16 v[14:15], v233 offset:10240\n" \
"v_mul_f32 v236, s92, v236\n" \
"v_mfma_f32_16x16x32_bf16 v[116:119], v[44:47], v[204:207], v[116:119]\n" \
"ds_read_b64_tr_b16 v[16:17], v232 offset:512\n" \
"v_mul_f32 v237, s92, v237\n" \
"v_mfma_f32_16x16x32_bf16 v[112:115], v[32:35], v[200:203], v[112:115]\n" \
"ds_read_b64_tr_b16 v[18:19], v232 offset:2560\n" \
"v_cvt_pk_bf16_f32 v234, v234, v235\n" \
"v_mfma_f32_16x16x32_bf16 v[112:115], v[36:39], v[204:207], v[112:115]\n" \
"global_load_lds_dwordx4 v227, s[48:49] offset:-1024\n" \
"v_cvt_pk_bf16_f32 v236, v236, v237\n" \
"v_mfma_f32_16x16x32_bf16 v[144:147], v[32:35], v[208:211], v[144:147]\n" \
"s_add_u32 s48, s48, 0x80\n" \
"s_addc_u32 s49, s49, 0\n" \
"ds_read_b128 v[200:203], v230 offset:2048\n" \
"v_mfma_f32_16x16x32_bf16 v[144:147], v[36:39], v[212:215], v[144:147]\n" \
"ds_read_b128 v[204:207], v231 offset:2048\n" \
"v_accvgpr_write_b32 a106, v234\n" \
"v_mfma_f32_16x16x32_bf16 v[148:151], v[40:43], v[208:211], v[148:151]\n" \
"ds_read_b64_tr_b16 v[20:21], v232 offset:8704\n" \
"v_accvgpr_write_b32 a107, v236\n" \
"v_mfma_f32_16x16x32_bf16 v[148:151], v[44:47], v[212:215], v[148:151]\n" \
"global_load_lds_dwordx4 v224, s[46:47] offset:0\n" \
"v_accvgpr_read_b32 v234, a80\n" \
"v_mfma_f32_16x16x32_bf16 v[152:155], v[48:51], v[208:211], v[152:155]\n" \
"ds_read_b64_tr_b16 v[22:23], v232 offset:10752\n" \
"v_accvgpr_read_b32 v235, a81\n" \
"v_mfma_f32_16x16x32_bf16 v[152:155], v[52:55], v[212:215], v[152:155]\n" \
"ds_read_b64_tr_b16 v[24:25], v233 offset:512\n" \
"v_accvgpr_read_b32 v236, a82\n" \
"v_mfma_f32_16x16x32_bf16 v[156:159], v[56:59], v[208:211], v[156:159]\n" \
"ds_read_b64_tr_b16 v[26:27], v233 offset:2560\n" \
"v_accvgpr_read_b32 v237, a83\n" \
"v_mfma_f32_16x16x32_bf16 v[156:159], v[60:63], v[212:215], v[156:159]\n" \
"global_load_lds_dwordx4 v225, s[46:47] offset:1024\n" \
"v_cvt_pk_bf16_f32 v234, v234, v235\n" \
"v_mfma_f32_16x16x32_bf16 v[188:191], v[56:59], v[216:219], v[188:191]\n" \
"ds_read_b128 v[208:211], v230 offset:4096\n" \
"v_cvt_pk_bf16_f32 v236, v236, v237\n" \
"v_mfma_f32_16x16x32_bf16 v[188:191], v[60:63], v[220:223], v[188:191]\n" \
"ds_read_b128 v[212:215], v231 offset:4096\n" \
"v_accvgpr_write_b32 a80, v234\n" \
"v_mfma_f32_16x16x32_bf16 v[184:187], v[48:51], v[216:219], v[184:187]\n" \
"ds_read_b64_tr_b16 v[28:29], v233 offset:8704\n" \
"v_accvgpr_write_b32 a81, v236\n" \
"v_mfma_f32_16x16x32_bf16 v[184:187], v[52:55], v[220:223], v[184:187]\n" \
"global_load_lds_dwordx4 v226, s[46:47] offset:2048\n" \
"v_accvgpr_read_b32 v234, a112\n" \
"v_mfma_f32_16x16x32_bf16 v[180:183], v[40:43], v[216:219], v[180:183]\n" \
"ds_read_b64_tr_b16 v[30:31], v233 offset:10752\n" \
"v_accvgpr_read_b32 v235, a113\n" \
"v_mfma_f32_16x16x32_bf16 v[180:183], v[44:47], v[220:223], v[180:183]\n" \
"v_accvgpr_read_b32 v236, a114\n" \
"v_accvgpr_read_b32 v237, a115\n" \
"v_mfma_f32_16x16x32_bf16 v[176:179], v[32:35], v[216:219], v[176:179]\n" \
"v_cvt_pk_bf16_f32 v234, v234, v235\n" \
"v_cvt_pk_bf16_f32 v236, v236, v237\n" \
"v_mfma_f32_16x16x32_bf16 v[176:179], v[36:39], v[220:223], v[176:179]\n" \
"s_waitcnt lgkmcnt(1)\n" \
"global_load_lds_dwordx4 v227, s[46:47] offset:3072\n" \
"v_accvgpr_write_b32 a82, v234\n" \
"v_mfma_f32_16x16x32_bf16 v[64:67], v[0:3], v[192:195], v[64:67]\n" \
"s_add_u32 s46, s46, 0x80\n" \
"s_addc_u32 s47, s47, 0\n" \
"ds_read_b128 v[216:219], v230 offset:6144\n" \
"v_mfma_f32_16x16x32_bf16 v[64:67], v[4:7], v[196:199], v[64:67]\n" \
"ds_read_b128 v[220:223], v231 offset:6144\n" \
"v_accvgpr_write_b32 a83, v236\n" \
"v_mfma_f32_16x16x32_bf16 v[68:71], v[8:11], v[192:195], v[68:71]\n" \
"ds_read_b64_tr_b16 v[32:33], v232 offset:1024\n" \
"v_accvgpr_read_b32 v234, a84\n" \
"v_mfma_f32_16x16x32_bf16 v[68:71], v[12:15], v[196:199], v[68:71]\n" \
"buffer_store_dwordx4 a[220:223], v248, s[56:59], s90 offen offset:3072 sc1\n" \
"v_accvgpr_read_b32 v235, a85\n" \
"v_accvgpr_read_b32 v236, a86\n" \
"v_accvgpr_read_b32 v237, a87\n" \
"v_cvt_pk_bf16_f32 v234, v234, v235\n" \
"v_mfma_f32_16x16x32_bf16 v[72:75], v[16:19], v[192:195], v[72:75]\n" \
"s_add_u32 s90, s88, 139264\n" \
"ds_read_b64_tr_b16 v[34:35], v232 offset:3072\n" \
"v_cvt_pk_bf16_f32 v236, v236, v237\n" \
"v_mfma_f32_16x16x32_bf16 v[72:75], v[20:23], v[196:199], v[72:75]\n" \
"s_waitcnt lgkmcnt(3)\n" \
"ds_read_b64_tr_b16 v[36:37], v232 offset:9216\n" \
"v_accvgpr_write_b32 a84, v234\n" \
"v_mfma_f32_16x16x32_bf16 v[76:79], v[24:27], v[192:195], v[76:79]\n" \
"ds_read_b64_tr_b16 v[38:39], v232 offset:11264\n" \
"v_accvgpr_write_b32 a85, v236\n" \
"v_mfma_f32_16x16x32_bf16 v[76:79], v[28:31], v[196:199], v[76:79]\n" \
"buffer_store_dwordx4 a[128:131], v249, s[56:59], s90 offen offset:0 sc1\n" \
"v_accvgpr_read_b32 v234, a116\n" \
"v_accvgpr_read_b32 v235, a117\n" \
"v_accvgpr_read_b32 v236, a118\n" \
"v_accvgpr_read_b32 v237, a119\n" \
"v_mfma_f32_16x16x32_bf16 v[108:111], v[24:27], v[200:203], v[108:111]\n" \
"ds_read_b64_tr_b16 v[40:41], v233 offset:1024\n" \
"v_cvt_pk_bf16_f32 v234, v234, v235\n" \
"v_mfma_f32_16x16x32_bf16 v[108:111], v[28:31], v[204:207], v[108:111]\n" \
"ds_read_b64_tr_b16 v[42:43], v233 offset:3072\n" \
"v_cvt_pk_bf16_f32 v236, v236, v237\n" \
"v_mfma_f32_16x16x32_bf16 v[104:107], v[16:19], v[200:203], v[104:107]\n" \
"ds_read_b64_tr_b16 v[44:45], v233 offset:9216\n" \
"v_accvgpr_write_b32 a86, v234\n" \
"v_mfma_f32_16x16x32_bf16 v[104:107], v[20:23], v[204:207], v[104:107]\n" \
"v_accvgpr_write_b32 a87, v236\n" \
"v_accvgpr_read_b32 v234, a88\n" \
"v_mfma_f32_16x16x32_bf16 v[100:103], v[8:11], v[200:203], v[100:103]\n" \
"ds_read_b64_tr_b16 v[46:47], v233 offset:11264\n" \
"v_accvgpr_read_b32 v235, a89\n" \
"v_mfma_f32_16x16x32_bf16 v[100:103], v[12:15], v[204:207], v[100:103]\n" \
"ds_read_b64_tr_b16 v[48:49], v232 offset:1536\n" \
"v_accvgpr_read_b32 v236, a90\n" \
"v_mfma_f32_16x16x32_bf16 v[96:99], v[0:3], v[200:203], v[96:99]\n" \
"s_add_u32 m0, s95, 4096\n" \
"ds_read_b64_tr_b16 v[50:51], v232 offset:3584\n" \
"v_accvgpr_read_b32 v237, a91\n" \
"v_mfma_f32_16x16x32_bf16 v[96:99], v[4:7], v[204:207], v[96:99]\n" \
"global_load_lds_dwordx4 v228, s[44:45] offset:-4096\n" \
"v_cvt_pk_bf16_f32 v234, v234, v235\n" \
"v_mfma_f32_16x16x32_bf16 v[128:131], v[0:3], v[208:211], v[128:131]\n" \
"ds_read_b64_tr_b16 v[52:53], v232 offset:9728\n" \
"v_cvt_pk_bf16_f32 v236, v236, v237\n" \
"v_mfma_f32_16x16x32_bf16 v[128:131], v[4:7], v[212:215], v[128:131]\n" \
"ds_read_b64_tr_b16 v[54:55], v232 offset:11776\n" \
"v_accvgpr_write_b32 a88, v234\n" \
"v_mfma_f32_16x16x32_bf16 v[132:135], v[8:11], v[208:211], v[132:135]\n" \
"ds_read_b64_tr_b16 v[56:57], v233 offset:1536\n" \
"v_accvgpr_write_b32 a89, v236\n" \
"v_mfma_f32_16x16x32_bf16 v[132:135], v[12:15], v[212:215], v[132:135]\n" \
"global_load_lds_dwordx4 v229, s[44:45] offset:-3072\n" \
"v_accvgpr_read_b32 v234, a120\n" \
"v_mfma_f32_16x16x32_bf16 v[136:139], v[16:19], v[208:211], v[136:139]\n" \
"s_add_u32 s44, s44, 0x80000\n" \
"s_addc_u32 s45, s45, 0\n" \
"ds_read_b64_tr_b16 v[58:59], v233 offset:3584\n" \
"v_mfma_f32_16x16x32_bf16 v[136:139], v[20:23], v[212:215], v[136:139]\n" \
"ds_read_b64_tr_b16 v[60:61], v233 offset:9728\n" \
"v_accvgpr_read_b32 v235, a121\n" \
"v_mfma_f32_16x16x32_bf16 v[140:143], v[24:27], v[208:211], v[140:143]\n" \
"ds_read_b64_tr_b16 v[62:63], v233 offset:11776\n" \
"v_accvgpr_read_b32 v236, a122\n" \
"v_mfma_f32_16x16x32_bf16 v[140:143], v[28:31], v[212:215], v[140:143]\n" \
"s_waitcnt lgkmcnt(3)\n" \
"global_load_lds_dwordx4 v228, s[42:43] offset:-2048\n" \
"v_accvgpr_read_b32 v237, a123\n" \
"v_mfma_f32_16x16x32_bf16 v[172:175], v[24:27], v[216:219], v[172:175]\n" \
"v_add_u32 v230, 0x8000, v230\n" \
"v_add_u32 v231, 0x8000, v231\n" \
"v_mfma_f32_16x16x32_bf16 v[172:175], v[28:31], v[220:223], v[172:175]\n" \
"v_add_u32 v232, 0x4000, v232\n" \
"v_add_u32 v233, 0x4000, v233\n" \
"v_mfma_f32_16x16x32_bf16 v[168:171], v[16:19], v[216:219], v[168:171]\n" \
"v_cvt_pk_bf16_f32 v234, v234, v235\n" \
"v_cvt_pk_bf16_f32 v236, v236, v237\n" \
"v_mfma_f32_16x16x32_bf16 v[168:171], v[20:23], v[220:223], v[168:171]\n" \
"global_load_lds_dwordx4 v229, s[42:43] offset:-1024\n" \
"v_accvgpr_write_b32 a90, v234\n" \
"v_mfma_f32_16x16x32_bf16 v[164:167], v[8:11], v[216:219], v[164:167]\n" \
"s_add_u32 s42, s42, 0x80000\n" \
"s_addc_u32 s43, s43, 0\n" \
"v_accvgpr_write_b32 a91, v236\n" \
"v_mfma_f32_16x16x32_bf16 v[164:167], v[12:15], v[220:223], v[164:167]\n" \
"v_accvgpr_read_b32 v234, a92\n" \
"v_accvgpr_read_b32 v235, a93\n" \
"v_mfma_f32_16x16x32_bf16 v[160:163], v[0:3], v[216:219], v[160:163]\n" \
"s_waitcnt vmcnt(19) lgkmcnt(0)\n" \
"v_accvgpr_read_b32 v236, a94\n" \
"v_accvgpr_read_b32 v237, a95\n" \
"v_mfma_f32_16x16x32_bf16 v[160:163], v[4:7], v[220:223], v[160:163]\n" \
"s_barrier\n" \
"v_mfma_f32_16x16x32_bf16 v[80:83], v[32:35], v[192:195], v[80:83]\n" \
"ds_read_b64_tr_b16 v[0:1], v232 offset:0\n" \
"v_cvt_pk_bf16_f32 v234, v234, v235\n" \
"v_mfma_f32_16x16x32_bf16 v[80:83], v[36:39], v[196:199], v[80:83]\n" \
"ds_read_b64_tr_b16 v[2:3], v232 offset:2048\n" \
"v_cvt_pk_bf16_f32 v236, v236, v237\n" \
"v_mfma_f32_16x16x32_bf16 v[84:87], v[40:43], v[192:195], v[84:87]\n" \
"s_add_u32 m0, s87, 102400\n" \
"ds_read_b64_tr_b16 v[4:5], v232 offset:8192\n" \
"v_accvgpr_write_b32 a92, v234\n" \
"v_mfma_f32_16x16x32_bf16 v[84:87], v[44:47], v[196:199], v[84:87]\n" \
"Lpg3309_%=:\n" \
"s_cbranch_execz Lpg5398_%=\n" \
"global_load_lds_dwordx4 v224, s[48:49] offset:-4096\n" \
"v_accvgpr_write_b32 a93, v236\n" \
"v_mfma_f32_16x16x32_bf16 v[88:91], v[48:51], v[192:195], v[88:91]\n" \
"ds_read_b64_tr_b16 v[6:7], v232 offset:10240\n" \
"v_accvgpr_read_b32 v234, a124\n" \
"v_mfma_f32_16x16x32_bf16 v[88:91], v[52:55], v[196:199], v[88:91]\n" \
"ds_read_b64_tr_b16 v[8:9], v233 offset:0\n" \
"v_accvgpr_read_b32 v235, a125\n" \
"v_mfma_f32_16x16x32_bf16 v[92:95], v[56:59], v[192:195], v[92:95]\n" \
"ds_read_b64_tr_b16 v[10:11], v233 offset:2048\n" \
"v_accvgpr_read_b32 v236, a126\n" \
"v_mfma_f32_16x16x32_bf16 v[92:95], v[60:63], v[196:199], v[92:95]\n" \
"global_load_lds_dwordx4 v225, s[48:49] offset:-3072\n" \
"v_accvgpr_read_b32 v237, a127\n" \
"v_mfma_f32_16x16x32_bf16 v[124:127], v[56:59], v[200:203], v[124:127]\n" \
"ds_read_b128 v[192:195], v230 offset:0\n" \
"v_cvt_pk_bf16_f32 v234, v234, v235\n" \
"v_mfma_f32_16x16x32_bf16 v[124:127], v[60:63], v[204:207], v[124:127]\n" \
"ds_read_b128 v[196:199], v231 offset:0\n" \
"v_cvt_pk_bf16_f32 v236, v236, v237\n" \
"v_mfma_f32_16x16x32_bf16 v[120:123], v[48:51], v[200:203], v[120:123]\n" \
"ds_read_b64_tr_b16 v[12:13], v233 offset:8192\n" \
"v_accvgpr_write_b32 a94, v234\n" \
"v_mfma_f32_16x16x32_bf16 v[120:123], v[52:55], v[204:207], v[120:123]\n" \
"global_load_lds_dwordx4 v226, s[48:49] offset:-2048\n" \
"v_accvgpr_write_b32 a95, v236\n" \
"v_mfma_f32_16x16x32_bf16 v[116:119], v[40:43], v[200:203], v[116:119]\n" \
"ds_read_b64_tr_b16 v[14:15], v233 offset:10240\n" \
"v_accvgpr_read_b32 v234, a0\n" \
"v_mfma_f32_16x16x32_bf16 v[116:119], v[44:47], v[204:207], v[116:119]\n" \
"ds_read_b64_tr_b16 v[16:17], v232 offset:512\n" \
"v_accvgpr_read_b32 v235, a1\n" \
"v_mfma_f32_16x16x32_bf16 v[112:115], v[32:35], v[200:203], v[112:115]\n" \
"ds_read_b64_tr_b16 v[18:19], v232 offset:2560\n" \
"v_accvgpr_read_b32 v236, a2\n" \
"v_mfma_f32_16x16x32_bf16 v[112:115], v[36:39], v[204:207], v[112:115]\n" \
"global_load_lds_dwordx4 v227, s[48:49] offset:-1024\n" \
"v_accvgpr_read_b32 v237, a3\n" \
"v_mfma_f32_16x16x32_bf16 v[144:147], v[32:35], v[208:211], v[144:147]\n" \
"s_add_u32 s48, s48, 0x80\n" \
"s_addc_u32 s49, s49, 0\n" \
"ds_read_b128 v[200:203], v230 offset:2048\n" \
"v_mfma_f32_16x16x32_bf16 v[144:147], v[36:39], v[212:215], v[144:147]\n" \
"ds_read_b128 v[204:207], v231 offset:2048\n" \
"v_mul_f32 v234, s92, v234\n" \
"v_mfma_f32_16x16x32_bf16 v[148:151], v[40:43], v[208:211], v[148:151]\n" \
"ds_read_b64_tr_b16 v[20:21], v232 offset:8704\n" \
"v_mul_f32 v235, s92, v235\n" \
"v_mfma_f32_16x16x32_bf16 v[148:151], v[44:47], v[212:215], v[148:151]\n" \
"global_load_lds_dwordx4 v224, s[46:47] offset:0\n" \
"v_mul_f32 v236, s92, v236\n" \
"v_mfma_f32_16x16x32_bf16 v[152:155], v[48:51], v[208:211], v[152:155]\n" \
"ds_read_b64_tr_b16 v[22:23], v232 offset:10752\n" \
"v_mul_f32 v237, s92, v237\n" \
"v_mfma_f32_16x16x32_bf16 v[152:155], v[52:55], v[212:215], v[152:155]\n" \
"ds_read_b64_tr_b16 v[24:25], v233 offset:512\n" \
"v_cvt_pk_bf16_f32 v234, v234, v235\n" \
"v_mfma_f32_16x16x32_bf16 v[156:159], v[56:59], v[208:211], v[156:159]\n" \
"ds_read_b64_tr_b16 v[26:27], v233 offset:2560\n" \
"v_cvt_pk_bf16_f32 v236, v236, v237\n" \
"v_mfma_f32_16x16x32_bf16 v[156:159], v[60:63], v[212:215], v[156:159]\n" \
"global_load_lds_dwordx4 v225, s[46:47] offset:1024\n" \
"v_accvgpr_write_b32 a0, v234\n" \
"v_mfma_f32_16x16x32_bf16 v[188:191], v[56:59], v[216:219], v[188:191]\n" \
"ds_read_b128 v[208:211], v230 offset:4096\n" \
"v_accvgpr_write_b32 a1, v236\n" \
"v_mfma_f32_16x16x32_bf16 v[188:191], v[60:63], v[220:223], v[188:191]\n" \
"ds_read_b128 v[212:215], v231 offset:4096\n" \
"v_accvgpr_read_b32 v234, a4\n" \
"v_mfma_f32_16x16x32_bf16 v[184:187], v[48:51], v[216:219], v[184:187]\n" \
"ds_read_b64_tr_b16 v[28:29], v233 offset:8704\n" \
"v_accvgpr_read_b32 v235, a5\n" \
"v_mfma_f32_16x16x32_bf16 v[184:187], v[52:55], v[220:223], v[184:187]\n" \
"global_load_lds_dwordx4 v226, s[46:47] offset:2048\n" \
"v_accvgpr_read_b32 v236, a6\n" \
"v_mfma_f32_16x16x32_bf16 v[180:183], v[40:43], v[216:219], v[180:183]\n" \
"ds_read_b64_tr_b16 v[30:31], v233 offset:10752\n" \
"v_accvgpr_read_b32 v237, a7\n" \
"v_mfma_f32_16x16x32_bf16 v[180:183], v[44:47], v[220:223], v[180:183]\n" \
"v_mul_f32 v234, s92, v234\n" \
"v_mul_f32 v235, s92, v235\n" \
"v_mfma_f32_16x16x32_bf16 v[176:179], v[32:35], v[216:219], v[176:179]\n" \
"v_mul_f32 v236, s92, v236\n" \
"v_mul_f32 v237, s92, v237\n" \
"v_mfma_f32_16x16x32_bf16 v[176:179], v[36:39], v[220:223], v[176:179]\n" \
"s_waitcnt lgkmcnt(1)\n" \
"global_load_lds_dwordx4 v227, s[46:47] offset:3072\n" \
"v_cvt_pk_bf16_f32 v234, v234, v235\n" \
"v_mfma_f32_16x16x32_bf16 v[64:67], v[0:3], v[192:195], v[64:67]\n" \
"s_add_u32 s46, s46, 0x80\n" \
"s_addc_u32 s47, s47, 0\n" \
"ds_read_b128 v[216:219], v230 offset:6144\n" \
"v_mfma_f32_16x16x32_bf16 v[64:67], v[4:7], v[196:199], v[64:67]\n" \
"ds_read_b128 v[220:223], v231 offset:6144\n" \
"v_cvt_pk_bf16_f32 v236, v236, v237\n" \
"v_mfma_f32_16x16x32_bf16 v[68:71], v[8:11], v[192:195], v[68:71]\n" \
"ds_read_b64_tr_b16 v[32:33], v232 offset:1024\n" \
"v_accvgpr_write_b32 a2, v234\n" \
"v_mfma_f32_16x16x32_bf16 v[68:71], v[12:15], v[196:199], v[68:71]\n" \
"buffer_store_dwordx4 a[160:163], v249, s[56:59], s90 offen offset:256 sc1\n" \
"v_accvgpr_write_b32 a3, v236\n" \
"v_accvgpr_read_b32 v234, a32\n" \
"v_accvgpr_read_b32 v235, a33\n" \
"v_accvgpr_read_b32 v236, a34\n" \
"v_mfma_f32_16x16x32_bf16 v[72:75], v[16:19], v[192:195], v[72:75]\n" \
"ds_read_b64_tr_b16 v[34:35], v232 offset:3072\n" \
"v_accvgpr_read_b32 v237, a35\n" \
"v_mfma_f32_16x16x32_bf16 v[72:75], v[20:23], v[196:199], v[72:75]\n" \
"s_waitcnt lgkmcnt(3)\n" \
"ds_read_b64_tr_b16 v[36:37], v232 offset:9216\n" \
"v_mul_f32 v234, s92, v234\n" \
"v_mfma_f32_16x16x32_bf16 v[76:79], v[24:27], v[192:195], v[76:79]\n" \
"ds_read_b64_tr_b16 v[38:39], v232 offset:11264\n" \
"v_mul_f32 v235, s92, v235\n" \
"v_mfma_f32_16x16x32_bf16 v[76:79], v[28:31], v[196:199], v[76:79]\n" \
"v_mul_f32 v236, s92, v236\n" \
"v_mul_f32 v237, s92, v237\n" \
"v_mfma_f32_16x16x32_bf16 v[108:111], v[24:27], v[200:203], v[108:111]\n" \
"ds_read_b64_tr_b16 v[40:41], v233 offset:1024\n" \
"v_cvt_pk_bf16_f32 v234, v234, v235\n" \
"v_mfma_f32_16x16x32_bf16 v[108:111], v[28:31], v[204:207], v[108:111]\n" \
"ds_read_b64_tr_b16 v[42:43], v233 offset:3072\n" \
"v_cvt_pk_bf16_f32 v236, v236, v237\n" \
"v_mfma_f32_16x16x32_bf16 v[104:107], v[16:19], v[200:203], v[104:107]\n" \
"ds_read_b64_tr_b16 v[44:45], v233 offset:9216\n" \
"v_accvgpr_write_b32 a32, v234\n" \
"v_mfma_f32_16x16x32_bf16 v[104:107], v[20:23], v[204:207], v[104:107]\n" \
"v_accvgpr_write_b32 a33, v236\n" \
"v_accvgpr_read_b32 v234, a36\n" \
"v_mfma_f32_16x16x32_bf16 v[100:103], v[8:11], v[200:203], v[100:103]\n" \
"ds_read_b64_tr_b16 v[46:47], v233 offset:11264\n" \
"v_accvgpr_read_b32 v235, a37\n" \
"v_mfma_f32_16x16x32_bf16 v[100:103], v[12:15], v[204:207], v[100:103]\n" \
"ds_read_b64_tr_b16 v[48:49], v232 offset:1536\n" \
"v_accvgpr_read_b32 v236, a38\n" \
"v_mfma_f32_16x16x32_bf16 v[96:99], v[0:3], v[200:203], v[96:99]\n" \
"s_add_u32 m0, s95, 20480\n" \
"ds_read_b64_tr_b16 v[50:51], v232 offset:3584\n" \
"v_accvgpr_read_b32 v237, a39\n" \
"v_mfma_f32_16x16x32_bf16 v[96:99], v[4:7], v[204:207], v[96:99]\n" \
"global_load_lds_dwordx4 v228, s[44:45] offset:-4096\n" \
"v_mul_f32 v234, s92, v234\n" \
"v_mfma_f32_16x16x32_bf16 v[128:131], v[0:3], v[208:211], v[128:131]\n" \
"ds_read_b64_tr_b16 v[52:53], v232 offset:9728\n" \
"v_mul_f32 v235, s92, v235\n" \
"v_mfma_f32_16x16x32_bf16 v[128:131], v[4:7], v[212:215], v[128:131]\n" \
"ds_read_b64_tr_b16 v[54:55], v232 offset:11776\n" \
"v_mul_f32 v236, s92, v236\n" \
"v_mfma_f32_16x16x32_bf16 v[132:135], v[8:11], v[208:211], v[132:135]\n" \
"ds_read_b64_tr_b16 v[56:57], v233 offset:1536\n" \
"v_mul_f32 v237, s92, v237\n" \
"v_mfma_f32_16x16x32_bf16 v[132:135], v[12:15], v[212:215], v[132:135]\n" \
"global_load_lds_dwordx4 v229, s[44:45] offset:-3072\n" \
"v_cvt_pk_bf16_f32 v234, v234, v235\n" \
"v_mfma_f32_16x16x32_bf16 v[136:139], v[16:19], v[208:211], v[136:139]\n" \
"s_add_u32 s44, s44, 0x80000\n" \
"s_addc_u32 s45, s45, 0\n" \
"ds_read_b64_tr_b16 v[58:59], v233 offset:3584\n" \
"v_mfma_f32_16x16x32_bf16 v[136:139], v[20:23], v[212:215], v[136:139]\n" \
"ds_read_b64_tr_b16 v[60:61], v233 offset:9728\n" \
"v_cvt_pk_bf16_f32 v236, v236, v237\n" \
"v_mfma_f32_16x16x32_bf16 v[140:143], v[24:27], v[208:211], v[140:143]\n" \
"ds_read_b64_tr_b16 v[62:63], v233 offset:11776\n" \
"v_accvgpr_write_b32 a34, v234\n" \
"v_mfma_f32_16x16x32_bf16 v[140:143], v[28:31], v[212:215], v[140:143]\n" \
"s_waitcnt lgkmcnt(3)\n" \
"global_load_lds_dwordx4 v228, s[42:43] offset:-2048\n" \
"v_accvgpr_write_b32 a35, v236\n" \
"v_mfma_f32_16x16x32_bf16 v[172:175], v[24:27], v[216:219], v[172:175]\n" \
"v_add_u32 v230, 0xffff0000, v230\n" \
"v_add_u32 v231, 0xffff0000, v231\n" \
"v_mfma_f32_16x16x32_bf16 v[172:175], v[28:31], v[220:223], v[172:175]\n" \
"v_add_u32 v232, 0x4000, v232\n" \
"v_add_u32 v233, 0x4000, v233\n" \
"v_mfma_f32_16x16x32_bf16 v[168:171], v[16:19], v[216:219], v[168:171]\n" \
"v_accvgpr_read_b32 v234, a8\n" \
"v_accvgpr_read_b32 v235, a9\n" \
"v_mfma_f32_16x16x32_bf16 v[168:171], v[20:23], v[220:223], v[168:171]\n" \
"global_load_lds_dwordx4 v229, s[42:43] offset:-1024\n" \
"v_accvgpr_read_b32 v236, a10\n" \
"v_mfma_f32_16x16x32_bf16 v[164:167], v[8:11], v[216:219], v[164:167]\n" \
"s_add_u32 s42, s42, 0x80000\n" \
"s_addc_u32 s43, s43, 0\n" \
"v_accvgpr_read_b32 v237, a11\n" \
"v_mfma_f32_16x16x32_bf16 v[164:167], v[12:15], v[220:223], v[164:167]\n" \
"v_mul_f32 v234, s92, v234\n" \
"v_mul_f32 v235, s92, v235\n" \
"v_mfma_f32_16x16x32_bf16 v[160:163], v[0:3], v[216:219], v[160:163]\n" \
"s_waitcnt vmcnt(19) lgkmcnt(0)\n" \
"v_mul_f32 v236, s92, v236\n" \
"v_mul_f32 v237, s92, v237\n" \
"v_mfma_f32_16x16x32_bf16 v[160:163], v[4:7], v[220:223], v[160:163]\n" \
"s_barrier\n" \
"v_mfma_f32_16x16x32_bf16 v[80:83], v[32:35], v[192:195], v[80:83]\n" \
"ds_read_b64_tr_b16 v[0:1], v232 offset:0\n" \
"v_cvt_pk_bf16_f32 v234, v234, v235\n" \
"v_mfma_f32_16x16x32_bf16 v[80:83], v[36:39], v[196:199], v[80:83]\n" \
"ds_read_b64_tr_b16 v[2:3], v232 offset:2048\n" \
"v_cvt_pk_bf16_f32 v236, v236, v237\n" \
"v_mfma_f32_16x16x32_bf16 v[84:87], v[40:43], v[192:195], v[84:87]\n" \
"s_add_u32 m0, s87, 135168\n" \
"ds_read_b64_tr_b16 v[4:5], v232 offset:8192\n" \
"v_accvgpr_write_b32 a8, v234\n" \
"v_mfma_f32_16x16x32_bf16 v[84:87], v[44:47], v[196:199], v[84:87]\n" \
"global_load_lds_dwordx4 v224, s[48:49] offset:-4096\n" \
"v_accvgpr_write_b32 a9, v236\n" \
"v_mfma_f32_16x16x32_bf16 v[88:91], v[48:51], v[192:195], v[88:91]\n" \
"ds_read_b64_tr_b16 v[6:7], v232 offset:10240\n" \
"v_accvgpr_read_b32 v234, a12\n" \
"v_mfma_f32_16x16x32_bf16 v[88:91], v[52:55], v[196:199], v[88:91]\n" \
"ds_read_b64_tr_b16 v[8:9], v233 offset:0\n" \
"v_accvgpr_read_b32 v235, a13\n" \
"v_mfma_f32_16x16x32_bf16 v[92:95], v[56:59], v[192:195], v[92:95]\n" \
"ds_read_b64_tr_b16 v[10:11], v233 offset:2048\n" \
"v_accvgpr_read_b32 v236, a14\n" \
"v_mfma_f32_16x16x32_bf16 v[92:95], v[60:63], v[196:199], v[92:95]\n" \
"global_load_lds_dwordx4 v225, s[48:49] offset:-3072\n" \
"v_accvgpr_read_b32 v237, a15\n" \
"v_mfma_f32_16x16x32_bf16 v[124:127], v[56:59], v[200:203], v[124:127]\n" \
"ds_read_b128 v[192:195], v230 offset:0\n" \
"v_mul_f32 v234, s92, v234\n" \
"v_mfma_f32_16x16x32_bf16 v[124:127], v[60:63], v[204:207], v[124:127]\n" \
"ds_read_b128 v[196:199], v231 offset:0\n" \
"v_mul_f32 v235, s92, v235\n" \
"v_mfma_f32_16x16x32_bf16 v[120:123], v[48:51], v[200:203], v[120:123]\n" \
"ds_read_b64_tr_b16 v[12:13], v233 offset:8192\n" \
"v_mul_f32 v236, s92, v236\n" \
"v_mfma_f32_16x16x32_bf16 v[120:123], v[52:55], v[204:207], v[120:123]\n" \
"global_load_lds_dwordx4 v226, s[48:49] offset:-2048\n" \
"v_mul_f32 v237, s92, v237\n" \
"v_mfma_f32_16x16x32_bf16 v[116:119], v[40:43], v[200:203], v[116:119]\n" \
"ds_read_b64_tr_b16 v[14:15], v233 offset:10240\n" \
"v_cvt_pk_bf16_f32 v234, v234, v235\n" \
"v_mfma_f32_16x16x32_bf16 v[116:119], v[44:47], v[204:207], v[116:119]\n" \
"ds_read_b64_tr_b16 v[16:17], v232 offset:512\n" \
"v_cvt_pk_bf16_f32 v236, v236, v237\n" \
"v_mfma_f32_16x16x32_bf16 v[112:115], v[32:35], v[200:203], v[112:115]\n" \
"ds_read_b64_tr_b16 v[18:19], v232 offset:2560\n" \
"v_accvgpr_write_b32 a10, v234\n" \
"v_mfma_f32_16x16x32_bf16 v[112:115], v[36:39], v[204:207], v[112:115]\n" \
"global_load_lds_dwordx4 v227, s[48:49] offset:-1024\n" \
"v_accvgpr_write_b32 a11, v236\n" \
"v_mfma_f32_16x16x32_bf16 v[144:147], v[32:35], v[208:211], v[144:147]\n" \
"s_add_u32 s48, s48, 0x80\n" \
"s_addc_u32 s49, s49, 0\n" \
"ds_read_b128 v[200:203], v230 offset:2048\n" \
"v_mfma_f32_16x16x32_bf16 v[144:147], v[36:39], v[212:215], v[144:147]\n" \
"ds_read_b128 v[204:207], v231 offset:2048\n" \
"v_accvgpr_read_b32 v234, a40\n" \
"v_mfma_f32_16x16x32_bf16 v[148:151], v[40:43], v[208:211], v[148:151]\n" \
"ds_read_b64_tr_b16 v[20:21], v232 offset:8704\n" \
"v_accvgpr_read_b32 v235, a41\n" \
"v_mfma_f32_16x16x32_bf16 v[148:151], v[44:47], v[212:215], v[148:151]\n" \
"global_load_lds_dwordx4 v224, s[46:47] offset:0\n" \
"v_accvgpr_read_b32 v236, a42\n" \
"v_mfma_f32_16x16x32_bf16 v[152:155], v[48:51], v[208:211], v[152:155]\n" \
"ds_read_b64_tr_b16 v[22:23], v232 offset:10752\n" \
"v_accvgpr_read_b32 v237, a43\n" \
"v_mfma_f32_16x16x32_bf16 v[152:155], v[52:55], v[212:215], v[152:155]\n" \
"ds_read_b64_tr_b16 v[24:25], v233 offset:512\n" \
"v_mul_f32 v234, s92, v234\n" \
"v_mfma_f32_16x16x32_bf16 v[156:159], v[56:59], v[208:211], v[156:159]\n" \
"ds_read_b64_tr_b16 v[26:27], v233 offset:2560\n" \
"v_mul_f32 v235, s92, v235\n" \
"v_mfma_f32_16x16x32_bf16 v[156:159], v[60:63], v[212:215], v[156:159]\n" \
"global_load_lds_dwordx4 v225, s[46:47] offset:1024\n" \
"v_mul_f32 v236, s92, v236\n" \
"v_mfma_f32_16x16x32_bf16 v[188:191], v[56:59], v[216:219], v[188:191]\n" \
"ds_read_b128 v[208:211], v230 offset:4096\n" \
"v_mul_f32 v237, s92, v237\n" \
"v_mfma_f32_16x16x32_bf16 v[188:191], v[60:63], v[220:223], v[188:191]\n" \
"ds_read_b128 v[212:215], v231 offset:4096\n" \
"v_cvt_pk_bf16_f32 v234, v234, v235\n" \
"v_mfma_f32_16x16x32_bf16 v[184:187], v[48:51], v[216:219], v[184:187]\n" \
"ds_read_b64_tr_b16 v[28:29], v233 offset:8704\n" \
"v_cvt_pk_bf16_f32 v236, v236, v237\n" \
"v_mfma_f32_16x16x32_bf16 v[184:187], v[52:55], v[220:223], v[184:187]\n" \
"global_load_lds_dwordx4 v226, s[46:47] offset:2048\n" \
"v_accvgpr_write_b32 a40, v234\n" \
"v_mfma_f32_16x16x32_bf16 v[180:183], v[40:43], v[216:219], v[180:183]\n" \
"ds_read_b64_tr_b16 v[30:31], v233 offset:10752\n" \
"v_accvgpr_write_b32 a41, v236\n" \
"v_mfma_f32_16x16x32_bf16 v[180:183], v[44:47], v[220:223], v[180:183]\n" \
"v_accvgpr_read_b32 v234, a44\n" \
"v_accvgpr_read_b32 v235, a45\n" \
"v_mfma_f32_16x16x32_bf16 v[176:179], v[32:35], v[216:219], v[176:179]\n" \
"v_accvgpr_read_b32 v236, a46\n" \
"v_accvgpr_read_b32 v237, a47\n" \
"v_mfma_f32_16x16x32_bf16 v[176:179], v[36:39], v[220:223], v[176:179]\n" \
"s_waitcnt lgkmcnt(1)\n" \
"global_load_lds_dwordx4 v227, s[46:47] offset:3072\n" \
"v_mul_f32 v234, s92, v234\n" \
"v_mfma_f32_16x16x32_bf16 v[64:67], v[0:3], v[192:195], v[64:67]\n" \
"s_add_u32 s46, s46, 0x80\n" \
"s_addc_u32 s47, s47, 0\n" \
"ds_read_b128 v[216:219], v230 offset:6144\n" \
"v_mfma_f32_16x16x32_bf16 v[64:67], v[4:7], v[196:199], v[64:67]\n" \
"ds_read_b128 v[220:223], v231 offset:6144\n" \
"v_mul_f32 v235, s92, v235\n" \
"v_mfma_f32_16x16x32_bf16 v[68:71], v[8:11], v[192:195], v[68:71]\n" \
"ds_read_b64_tr_b16 v[32:33], v232 offset:1024\n" \
"v_mul_f32 v236, s92, v236\n" \
"v_mfma_f32_16x16x32_bf16 v[68:71], v[12:15], v[196:199], v[68:71]\n" \
"buffer_store_dwordx4 a[136:139], v249, s[56:59], s90 offen offset:2048 sc1\n" \
"v_mul_f32 v237, s92, v237\n" \
"v_cvt_pk_bf16_f32 v234, v234, v235\n" \
"v_cvt_pk_bf16_f32 v236, v236, v237\n" \
"v_accvgpr_write_b32 a42, v234\n" \
"v_mfma_f32_16x16x32_bf16 v[72:75], v[16:19], v[192:195], v[72:75]\n" \
"ds_read_b64_tr_b16 v[34:35], v232 offset:3072\n" \
"v_accvgpr_write_b32 a43, v236\n" \
"v_mfma_f32_16x16x32_bf16 v[72:75], v[20:23], v[196:199], v[72:75]\n" \
"s_waitcnt lgkmcnt(3)\n" \
"ds_read_b64_tr_b16 v[36:37], v232 offset:9216\n" \
"v_accvgpr_read_b32 v234, a16\n" \
"v_mfma_f32_16x16x32_bf16 v[76:79], v[24:27], v[192:195], v[76:79]\n" \
"ds_read_b64_tr_b16 v[38:39], v232 offset:11264\n" \
"v_accvgpr_read_b32 v235, a17\n" \
"v_mfma_f32_16x16x32_bf16 v[76:79], v[28:31], v[196:199], v[76:79]\n" \
"v_accvgpr_read_b32 v236, a18\n" \
"v_accvgpr_read_b32 v237, a19\n" \
"v_mfma_f32_16x16x32_bf16 v[108:111], v[24:27], v[200:203], v[108:111]\n" \
"ds_read_b64_tr_b16 v[40:41], v233 offset:1024\n" \
"v_cvt_pk_bf16_f32 v234, v234, v235\n" \
"v_mfma_f32_16x16x32_bf16 v[108:111], v[28:31], v[204:207], v[108:111]\n" \
"ds_read_b64_tr_b16 v[42:43], v233 offset:3072\n" \
"v_cvt_pk_bf16_f32 v236, v236, v237\n" \
"v_mfma_f32_16x16x32_bf16 v[104:107], v[16:19], v[200:203], v[104:107]\n" \
"ds_read_b64_tr_b16 v[44:45], v233 offset:9216\n" \
"v_accvgpr_write_b32 a16, v234\n" \
"v_mfma_f32_16x16x32_bf16 v[104:107], v[20:23], v[204:207], v[104:107]\n" \
"v_accvgpr_write_b32 a17, v236\n" \
"v_accvgpr_read_b32 v234, a48\n" \
"v_mfma_f32_16x16x32_bf16 v[100:103], v[8:11], v[200:203], v[100:103]\n" \
"ds_read_b64_tr_b16 v[46:47], v233 offset:11264\n" \
"v_accvgpr_read_b32 v235, a49\n" \
"v_mfma_f32_16x16x32_bf16 v[100:103], v[12:15], v[204:207], v[100:103]\n" \
"ds_read_b64_tr_b16 v[48:49], v232 offset:1536\n" \
"v_accvgpr_read_b32 v236, a50\n" \
"v_mfma_f32_16x16x32_bf16 v[96:99], v[0:3], v[200:203], v[96:99]\n" \
"s_add_u32 m0, s95, 36864\n" \
"ds_read_b64_tr_b16 v[50:51], v232 offset:3584\n" \
"v_accvgpr_read_b32 v237, a51\n" \
"v_mfma_f32_16x16x32_bf16 v[96:99], v[4:7], v[204:207], v[96:99]\n" \
"global_load_lds_dwordx4 v228, s[44:45] offset:-4096\n" \
"v_cvt_pk_bf16_f32 v234, v234, v235\n" \
"v_mfma_f32_16x16x32_bf16 v[128:131], v[0:3], v[208:211], v[128:131]\n" \
"ds_read_b64_tr_b16 v[52:53], v232 offset:9728\n" \
"v_cvt_pk_bf16_f32 v236, v236, v237\n" \
"v_mfma_f32_16x16x32_bf16 v[128:131], v[4:7], v[212:215], v[128:131]\n" \
"ds_read_b64_tr_b16 v[54:55], v232 offset:11776\n" \
"v_accvgpr_write_b32 a18, v234\n" \
"v_mfma_f32_16x16x32_bf16 v[132:135], v[8:11], v[208:211], v[132:135]\n" \
"ds_read_b64_tr_b16 v[56:57], v233 offset:1536\n" \
"v_accvgpr_write_b32 a19, v236\n" \
"v_mfma_f32_16x16x32_bf16 v[132:135], v[12:15], v[212:215], v[132:135]\n" \
"global_load_lds_dwordx4 v229, s[44:45] offset:-3072\n" \
"v_accvgpr_read_b32 v234, a20\n" \
"v_mfma_f32_16x16x32_bf16 v[136:139], v[16:19], v[208:211], v[136:139]\n" \
"s_add_u32 s44, s44, 0x80000\n" \
"s_addc_u32 s45, s45, 0\n" \
"ds_read_b64_tr_b16 v[58:59], v233 offset:3584\n" \
"v_mfma_f32_16x16x32_bf16 v[136:139], v[20:23], v[212:215], v[136:139]\n" \
"ds_read_b64_tr_b16 v[60:61], v233 offset:9728\n" \
"v_accvgpr_read_b32 v235, a21\n" \
"v_mfma_f32_16x16x32_bf16 v[140:143], v[24:27], v[208:211], v[140:143]\n" \
"ds_read_b64_tr_b16 v[62:63], v233 offset:11776\n" \
"v_accvgpr_read_b32 v236, a22\n" \
"v_mfma_f32_16x16x32_bf16 v[140:143], v[28:31], v[212:215], v[140:143]\n" \
"s_waitcnt lgkmcnt(3)\n" \
"global_load_lds_dwordx4 v228, s[42:43] offset:-2048\n" \
"v_accvgpr_read_b32 v237, a23\n" \
"v_mfma_f32_16x16x32_bf16 v[172:175], v[24:27], v[216:219], v[172:175]\n" \
"v_add_u32 v230, 0x8000, v230\n" \
"v_add_u32 v231, 0x8000, v231\n" \
"v_mfma_f32_16x16x32_bf16 v[172:175], v[28:31], v[220:223], v[172:175]\n" \
"v_add_u32 v232, 0xffff4000, v232\n" \
"v_add_u32 v233, 0xffff4000, v233\n" \
"v_mfma_f32_16x16x32_bf16 v[168:171], v[16:19], v[216:219], v[168:171]\n" \
"v_cvt_pk_bf16_f32 v234, v234, v235\n" \
"v_cvt_pk_bf16_f32 v236, v236, v237\n" \
"v_mfma_f32_16x16x32_bf16 v[168:171], v[20:23], v[220:223], v[168:171]\n" \
"global_load_lds_dwordx4 v229, s[42:43] offset:-1024\n" \
"v_accvgpr_write_b32 a20, v234\n" \
"v_mfma_f32_16x16x32_bf16 v[164:167], v[8:11], v[216:219], v[164:167]\n" \
"s_add_u32 s42, s42, 0x80000\n" \
"s_addc_u32 s43, s43, 0\n" \
"v_accvgpr_write_b32 a21, v236\n" \
"v_mfma_f32_16x16x32_bf16 v[164:167], v[12:15], v[220:223], v[164:167]\n" \
"v_accvgpr_read_b32 v234, a52\n" \
"v_accvgpr_read_b32 v235, a53\n" \
"v_mfma_f32_16x16x32_bf16 v[160:163], v[0:3], v[216:219], v[160:163]\n" \
"s_waitcnt vmcnt(18) lgkmcnt(0)\n" \
"v_accvgpr_read_b32 v236, a54\n" \
"v_accvgpr_read_b32 v237, a55\n" \
"v_mfma_f32_16x16x32_bf16 v[160:163], v[4:7], v[220:223], v[160:163]\n" \
"s_barrier\n" \
"v_mfma_f32_16x16x32_bf16 v[80:83], v[32:35], v[192:195], v[80:83]\n" \
"ds_read_b64_tr_b16 v[0:1], v232 offset:0\n" \
"v_cvt_pk_bf16_f32 v234, v234, v235\n" \
"v_mfma_f32_16x16x32_bf16 v[80:83], v[36:39], v[196:199], v[80:83]\n" \
"ds_read_b64_tr_b16 v[2:3], v232 offset:2048\n" \
"v_cvt_pk_bf16_f32 v236, v236, v237\n" \
"v_mfma_f32_16x16x32_bf16 v[84:87], v[40:43], v[192:195], v[84:87]\n" \
"s_add_u32 m0, s87, 69632\n" \
"ds_read_b64_tr_b16 v[4:5], v232 offset:8192\n" \
"v_accvgpr_write_b32 a22, v234\n" \
"v_mfma_f32_16x16x32_bf16 v[84:87], v[44:47], v[196:199], v[84:87]\n" \
"global_load_lds_dwordx4 v224, s[48:49] offset:-4096\n" \
"v_accvgpr_write_b32 a23, v236\n" \
"v_mfma_f32_16x16x32_bf16 v[88:91], v[48:51], v[192:195], v[88:91]\n" \
"ds_read_b64_tr_b16 v[6:7], v232 offset:10240\n" \
"v_accvgpr_read_b32 v234, a24\n" \
"v_mfma_f32_16x16x32_bf16 v[88:91], v[52:55], v[196:199], v[88:91]\n" \
"ds_read_b64_tr_b16 v[8:9], v233 offset:0\n" \
"v_accvgpr_read_b32 v235, a25\n" \
"v_mfma_f32_16x16x32_bf16 v[92:95], v[56:59], v[192:195], v[92:95]\n" \
"ds_read_b64_tr_b16 v[10:11], v233 offset:2048\n" \
"v_accvgpr_read_b32 v236, a26\n" \
"v_mfma_f32_16x16x32_bf16 v[92:95], v[60:63], v[196:199], v[92:95]\n" \
"global_load_lds_dwordx4 v225, s[48:49] offset:-3072\n" \
"v_accvgpr_read_b32 v237, a27\n" \
"v_mfma_f32_16x16x32_bf16 v[124:127], v[56:59], v[200:203], v[124:127]\n" \
"ds_read_b128 v[192:195], v230 offset:0\n" \
"v_cvt_pk_bf16_f32 v234, v234, v235\n" \
"v_mfma_f32_16x16x32_bf16 v[124:127], v[60:63], v[204:207], v[124:127]\n" \
"ds_read_b128 v[196:199], v231 offset:0\n" \
"v_cvt_pk_bf16_f32 v236, v236, v237\n" \
"v_mfma_f32_16x16x32_bf16 v[120:123], v[48:51], v[200:203], v[120:123]\n" \
"ds_read_b64_tr_b16 v[12:13], v233 offset:8192\n" \
"v_accvgpr_write_b32 a24, v234\n" \
"v_mfma_f32_16x16x32_bf16 v[120:123], v[52:55], v[204:207], v[120:123]\n" \
"global_load_lds_dwordx4 v226, s[48:49] offset:-2048\n" \
"v_accvgpr_write_b32 a25, v236\n" \
"v_mfma_f32_16x16x32_bf16 v[116:119], v[40:43], v[200:203], v[116:119]\n" \
"ds_read_b64_tr_b16 v[14:15], v233 offset:10240\n" \
"v_accvgpr_read_b32 v234, a56\n" \
"v_mfma_f32_16x16x32_bf16 v[116:119], v[44:47], v[204:207], v[116:119]\n" \
"ds_read_b64_tr_b16 v[16:17], v232 offset:512\n" \
"v_accvgpr_read_b32 v235, a57\n" \
"v_mfma_f32_16x16x32_bf16 v[112:115], v[32:35], v[200:203], v[112:115]\n" \
"ds_read_b64_tr_b16 v[18:19], v232 offset:2560\n" \
"v_accvgpr_read_b32 v236, a58\n" \
"v_mfma_f32_16x16x32_bf16 v[112:115], v[36:39], v[204:207], v[112:115]\n" \
"global_load_lds_dwordx4 v227, s[48:49] offset:-1024\n" \
"v_accvgpr_read_b32 v237, a59\n" \
"v_mfma_f32_16x16x32_bf16 v[144:147], v[32:35], v[208:211], v[144:147]\n" \
"s_add_u32 s48, s48, 0x80\n" \
"s_addc_u32 s49, s49, 0\n" \
"ds_read_b128 v[200:203], v230 offset:2048\n" \
"v_mfma_f32_16x16x32_bf16 v[144:147], v[36:39], v[212:215], v[144:147]\n" \
"ds_read_b128 v[204:207], v231 offset:2048\n" \
"v_cvt_pk_bf16_f32 v234, v234, v235\n" \
"v_mfma_f32_16x16x32_bf16 v[148:151], v[40:43], v[208:211], v[148:151]\n" \
"ds_read_b64_tr_b16 v[20:21], v232 offset:8704\n" \
"v_cvt_pk_bf16_f32 v236, v236, v237\n" \
"v_mfma_f32_16x16x32_bf16 v[148:151], v[44:47], v[212:215], v[148:151]\n" \
"global_load_lds_dwordx4 v224, s[46:47] offset:0\n" \
"v_accvgpr_write_b32 a26, v234\n" \
"v_mfma_f32_16x16x32_bf16 v[152:155], v[48:51], v[208:211], v[152:155]\n" \
"ds_read_b64_tr_b16 v[22:23], v232 offset:10752\n" \
"v_accvgpr_write_b32 a27, v236\n" \
"v_mfma_f32_16x16x32_bf16 v[152:155], v[52:55], v[212:215], v[152:155]\n" \
"ds_read_b64_tr_b16 v[24:25], v233 offset:512\n" \
"v_accvgpr_read_b32 v234, a28\n" \
"v_mfma_f32_16x16x32_bf16 v[156:159], v[56:59], v[208:211], v[156:159]\n" \
"ds_read_b64_tr_b16 v[26:27], v233 offset:2560\n" \
"v_accvgpr_read_b32 v235, a29\n" \
"v_mfma_f32_16x16x32_bf16 v[156:159], v[60:63], v[212:215], v[156:159]\n" \
"global_load_lds_dwordx4 v225, s[46:47] offset:1024\n" \
"v_accvgpr_read_b32 v236, a30\n" \
"v_mfma_f32_16x16x32_bf16 v[188:191], v[56:59], v[216:219], v[188:191]\n" \
"ds_read_b128 v[208:211], v230 offset:4096\n" \
"v_accvgpr_read_b32 v237, a31\n" \
"v_mfma_f32_16x16x32_bf16 v[188:191], v[60:63], v[220:223], v[188:191]\n" \
"ds_read_b128 v[212:215], v231 offset:4096\n" \
"v_cvt_pk_bf16_f32 v234, v234, v235\n" \
"v_mfma_f32_16x16x32_bf16 v[184:187], v[48:51], v[216:219], v[184:187]\n" \
"ds_read_b64_tr_b16 v[28:29], v233 offset:8704\n" \
"v_cvt_pk_bf16_f32 v236, v236, v237\n" \
"v_mfma_f32_16x16x32_bf16 v[184:187], v[52:55], v[220:223], v[184:187]\n" \
"global_load_lds_dwordx4 v226, s[46:47] offset:2048\n" \
"v_accvgpr_write_b32 a28, v234\n" \
"v_mfma_f32_16x16x32_bf16 v[180:183], v[40:43], v[216:219], v[180:183]\n" \
"ds_read_b64_tr_b16 v[30:31], v233 offset:10752\n" \
"v_accvgpr_write_b32 a29, v236\n" \
"v_mfma_f32_16x16x32_bf16 v[180:183], v[44:47], v[220:223], v[180:183]\n" \
"v_accvgpr_read_b32 v234, a60\n" \
"v_accvgpr_read_b32 v235, a61\n" \
"v_mfma_f32_16x16x32_bf16 v[176:179], v[32:35], v[216:219], v[176:179]\n" \
"v_accvgpr_read_b32 v236, a62\n" \
"v_accvgpr_read_b32 v237, a63\n" \
"v_mfma_f32_16x16x32_bf16 v[176:179], v[36:39], v[220:223], v[176:179]\n" \
"s_waitcnt lgkmcnt(1)\n" \
"global_load_lds_dwordx4 v227, s[46:47] offset:3072\n" \
"v_cvt_pk_bf16_f32 v234, v234, v235\n" \
"v_mfma_f32_16x16x32_bf16 v[64:67], v[0:3], v[192:195], v[64:67]\n" \
"s_add_u32 s46, s46, 0x80\n" \
"s_addc_u32 s47, s47, 0\n" \
"ds_read_b128 v[216:219], v230 offset:6144\n" \
"v_mfma_f32_16x16x32_bf16 v[64:67], v[4:7], v[196:199], v[64:67]\n" \
"ds_read_b128 v[220:223], v231 offset:6144\n" \
"v_cvt_pk_bf16_f32 v236, v236, v237\n" \
"v_mfma_f32_16x16x32_bf16 v[68:71], v[8:11], v[192:195], v[68:71]\n" \
"ds_read_b64_tr_b16 v[32:33], v232 offset:1024\n" \
"v_accvgpr_write_b32 a30, v234\n" \
"v_mfma_f32_16x16x32_bf16 v[68:71], v[12:15], v[196:199], v[68:71]\n" \
"buffer_store_dwordx4 a[168:171], v249, s[56:59], s90 offen offset:2304 sc1\n" \
"v_accvgpr_write_b32 a31, v236\n" \
"v_mfma_f32_16x16x32_bf16 v[72:75], v[16:19], v[192:195], v[72:75]\n" \
"s_add_u32 s90, s89, 139264\n" \
"ds_read_b64_tr_b16 v[34:35], v232 offset:3072\n" \
"v_mfma_f32_16x16x32_bf16 v[72:75], v[20:23], v[196:199], v[72:75]\n" \
"s_waitcnt lgkmcnt(3)\n" \
"ds_read_b64_tr_b16 v[36:37], v232 offset:9216\n" \
"v_mfma_f32_16x16x32_bf16 v[76:79], v[24:27], v[192:195], v[76:79]\n" \
"ds_read_b64_tr_b16 v[38:39], v232 offset:11264\n" \
"v_mfma_f32_16x16x32_bf16 v[76:79], v[28:31], v[196:199], v[76:79]\n" \
"buffer_store_dwordx4 a[144:147], v248, s[56:59], s90 offen offset:0 sc1\n" \
"v_mfma_f32_16x16x32_bf16 v[108:111], v[24:27], v[200:203], v[108:111]\n" \
"s_lshl_b32 s70, s63, 2\n" \
"ds_read_b64_tr_b16 v[40:41], v233 offset:1024\n" \
"v_mfma_f32_16x16x32_bf16 v[108:111], v[28:31], v[204:207], v[108:111]\n" \
"s_add_u32 s70, s70, s91\n" \
"ds_read_b64_tr_b16 v[42:43], v233 offset:3072\n" \
"v_mov_b32 v238, s60\n" \
"v_mfma_f32_16x16x32_bf16 v[104:107], v[16:19], v[200:203], v[104:107]\n" \
"ds_read_b64_tr_b16 v[44:45], v233 offset:9216\n" \
"v_lshlrev_b32 v239, 28, s61\n" \
"v_mfma_f32_16x16x32_bf16 v[104:107], v[20:23], v[204:207], v[104:107]\n" \
"buffer_store_dword v238, v239, s[56:59], s70 offen sc1\n" \
"v_mfma_f32_16x16x32_bf16 v[100:103], v[8:11], v[200:203], v[100:103]\n" \
"ds_read_b64_tr_b16 v[46:47], v233 offset:11264\n" \
"v_mfma_f32_16x16x32_bf16 v[100:103], v[12:15], v[204:207], v[100:103]\n" \
"ds_read_b64_tr_b16 v[48:49], v232 offset:1536\n" \
"v_mfma_f32_16x16x32_bf16 v[96:99], v[0:3], v[200:203], v[96:99]\n" \
"s_add_u32 m0, s95, 53248\n" \
"ds_read_b64_tr_b16 v[50:51], v232 offset:3584\n" \
"v_mfma_f32_16x16x32_bf16 v[96:99], v[4:7], v[204:207], v[96:99]\n" \
"global_load_lds_dwordx4 v228, s[44:45] offset:-4096\n" \
"Lpg3850_%=:\n" \
"s_cbranch_execz Lpg5547_%=\n" \
"v_mfma_f32_16x16x32_bf16 v[128:131], v[0:3], v[208:211], v[128:131]\n" \
"ds_read_b64_tr_b16 v[52:53], v232 offset:9728\n" \
"v_mfma_f32_16x16x32_bf16 v[128:131], v[4:7], v[212:215], v[128:131]\n" \
"ds_read_b64_tr_b16 v[54:55], v232 offset:11776\n" \
"v_mfma_f32_16x16x32_bf16 v[132:135], v[8:11], v[208:211], v[132:135]\n" \
"ds_read_b64_tr_b16 v[56:57], v233 offset:1536\n" \
"v_mfma_f32_16x16x32_bf16 v[132:135], v[12:15], v[212:215], v[132:135]\n" \
"global_load_lds_dwordx4 v229, s[44:45] offset:-3072\n" \
"v_mfma_f32_16x16x32_bf16 v[136:139], v[16:19], v[208:211], v[136:139]\n" \
"s_add_u32 s44, s44, 0x100000\n" \
"s_addc_u32 s45, s45, 0\n" \
"ds_read_b64_tr_b16 v[58:59], v233 offset:3584\n" \
"v_mfma_f32_16x16x32_bf16 v[136:139], v[20:23], v[212:215], v[136:139]\n" \
"ds_read_b64_tr_b16 v[60:61], v233 offset:9728\n" \
"v_mfma_f32_16x16x32_bf16 v[140:143], v[24:27], v[208:211], v[140:143]\n" \
"ds_read_b64_tr_b16 v[62:63], v233 offset:11776\n" \
"v_mfma_f32_16x16x32_bf16 v[140:143], v[28:31], v[212:215], v[140:143]\n" \
"s_waitcnt lgkmcnt(3)\n" \
"global_load_lds_dwordx4 v228, s[42:43] offset:-2048\n" \
"v_mfma_f32_16x16x32_bf16 v[172:175], v[24:27], v[216:219], v[172:175]\n" \
"v_add_u32 v230, 0x8000, v230\n" \
"v_add_u32 v231, 0x8000, v231\n" \
"v_mfma_f32_16x16x32_bf16 v[172:175], v[28:31], v[220:223], v[172:175]\n" \
"v_add_u32 v232, 0x4000, v232\n" \
"v_add_u32 v233, 0x4000, v233\n" \
"v_mfma_f32_16x16x32_bf16 v[168:171], v[16:19], v[216:219], v[168:171]\n" \
"v_mfma_f32_16x16x32_bf16 v[168:171], v[20:23], v[220:223], v[168:171]\n" \
"global_load_lds_dwordx4 v229, s[42:43] offset:-1024\n" \
"v_mfma_f32_16x16x32_bf16 v[164:167], v[8:11], v[216:219], v[164:167]\n" \
"s_add_u32 s42, s42, 0x100000\n" \
"s_addc_u32 s43, s43, 0\n" \
"v_mfma_f32_16x16x32_bf16 v[164:167], v[12:15], v[220:223], v[164:167]\n" \
"v_mfma_f32_16x16x32_bf16 v[160:163], v[0:3], v[216:219], v[160:163]\n" \
"s_waitcnt vmcnt(20) lgkmcnt(0)\n" \
"v_mfma_f32_16x16x32_bf16 v[160:163], v[4:7], v[220:223], v[160:163]\n" \
"s_barrier\n" \
"v_mfma_f32_16x16x32_bf16 v[80:83], v[32:35], v[192:195], v[80:83]\n" \
"ds_read_b64_tr_b16 v[0:1], v232 offset:0\n" \
"v_mfma_f32_16x16x32_bf16 v[80:83], v[36:39], v[196:199], v[80:83]\n" \
"ds_read_b64_tr_b16 v[2:3], v232 offset:2048\n" \
"v_mfma_f32_16x16x32_bf16 v[84:87], v[40:43], v[192:195], v[84:87]\n" \
"s_add_u32 m0, s87, 102400\n" \
"ds_read_b64_tr_b16 v[4:5], v232 offset:8192\n" \
"v_mfma_f32_16x16x32_bf16 v[84:87], v[44:47], v[196:199], v[84:87]\n" \
"global_load_lds_dwordx4 v224, s[48:49] offset:-4096\n" \
"v_mfma_f32_16x16x32_bf16 v[88:91], v[48:51], v[192:195], v[88:91]\n" \
"ds_read_b64_tr_b16 v[6:7], v232 offset:10240\n" \
"v_mfma_f32_16x16x32_bf16 v[88:91], v[52:55], v[196:199], v[88:91]\n" \
"ds_read_b64_tr_b16 v[8:9], v233 offset:0\n" \
"v_mfma_f32_16x16x32_bf16 v[92:95], v[56:59], v[192:195], v[92:95]\n" \
"ds_read_b64_tr_b16 v[10:11], v233 offset:2048\n" \
"v_mfma_f32_16x16x32_bf16 v[92:95], v[60:63], v[196:199], v[92:95]\n" \
"global_load_lds_dwordx4 v225, s[48:49] offset:-3072\n" \
"v_mfma_f32_16x16x32_bf16 v[124:127], v[56:59], v[200:203], v[124:127]\n" \
"ds_read_b128 v[192:195], v230 offset:0\n" \
"v_mfma_f32_16x16x32_bf16 v[124:127], v[60:63], v[204:207], v[124:127]\n" \
"ds_read_b128 v[196:199], v231 offset:0\n" \
"v_mfma_f32_16x16x32_bf16 v[120:123], v[48:51], v[200:203], v[120:123]\n" \
"ds_read_b64_tr_b16 v[12:13], v233 offset:8192\n" \
"v_mfma_f32_16x16x32_bf16 v[120:123], v[52:55], v[204:207], v[120:123]\n" \
"global_load_lds_dwordx4 v226, s[48:49] offset:-2048\n" \
"v_mfma_f32_16x16x32_bf16 v[116:119], v[40:43], v[200:203], v[116:119]\n" \
"ds_read_b64_tr_b16 v[14:15], v233 offset:10240\n" \
"v_mfma_f32_16x16x32_bf16 v[116:119], v[44:47], v[204:207], v[116:119]\n" \
"ds_read_b64_tr_b16 v[16:17], v232 offset:512\n" \
"v_mfma_f32_16x16x32_bf16 v[112:115], v[32:35], v[200:203], v[112:115]\n" \
"ds_read_b64_tr_b16 v[18:19], v232 offset:2560\n" \
"v_mfma_f32_16x16x32_bf16 v[112:115], v[36:39], v[204:207], v[112:115]\n" \
"global_load_lds_dwordx4 v227, s[48:49] offset:-1024\n" \
"v_mfma_f32_16x16x32_bf16 v[144:147], v[32:35], v[208:211], v[144:147]\n" \
"s_add_u32 s48, s48, 0x100\n" \
"s_addc_u32 s49, s49, 0\n" \
"ds_read_b128 v[200:203], v230 offset:2048\n" \
"v_mfma_f32_16x16x32_bf16 v[144:147], v[36:39], v[212:215], v[144:147]\n" \
"ds_read_b128 v[204:207], v231 offset:2048\n" \
"v_mfma_f32_16x16x32_bf16 v[148:151], v[40:43], v[208:211], v[148:151]\n" \
"ds_read_b64_tr_b16 v[20:21], v232 offset:8704\n" \
"v_mfma_f32_16x16x32_bf16 v[148:151], v[44:47], v[212:215], v[148:151]\n" \
"global_load_lds_dwordx4 v224, s[46:47] offset:0\n" \
"v_mfma_f32_16x16x32_bf16 v[152:155], v[48:51], v[208:211], v[152:155]\n" \
"ds_read_b64_tr_b16 v[22:23], v232 offset:10752\n" \
"v_mfma_f32_16x16x32_bf16 v[152:155], v[52:55], v[212:215], v[152:155]\n" \
"ds_read_b64_tr_b16 v[24:25], v233 offset:512\n" \
"v_mfma_f32_16x16x32_bf16 v[156:159], v[56:59], v[208:211], v[156:159]\n" \
"ds_read_b64_tr_b16 v[26:27], v233 offset:2560\n" \
"v_mfma_f32_16x16x32_bf16 v[156:159], v[60:63], v[212:215], v[156:159]\n" \
"global_load_lds_dwordx4 v225, s[46:47] offset:1024\n" \
"v_mfma_f32_16x16x32_bf16 v[188:191], v[56:59], v[216:219], v[188:191]\n" \
"ds_read_b128 v[208:211], v230 offset:4096\n" \
"v_mfma_f32_16x16x32_bf16 v[188:191], v[60:63], v[220:223], v[188:191]\n" \
"ds_read_b128 v[212:215], v231 offset:4096\n" \
"v_mfma_f32_16x16x32_bf16 v[184:187], v[48:51], v[216:219], v[184:187]\n" \
"ds_read_b64_tr_b16 v[28:29], v233 offset:8704\n" \
"v_mfma_f32_16x16x32_bf16 v[184:187], v[52:55], v[220:223], v[184:187]\n" \
"global_load_lds_dwordx4 v226, s[46:47] offset:2048\n" \
"v_mfma_f32_16x16x32_bf16 v[180:183], v[40:43], v[216:219], v[180:183]\n" \
"ds_read_b64_tr_b16 v[30:31], v233 offset:10752\n" \
"v_mfma_f32_16x16x32_bf16 v[180:183], v[44:47], v[220:223], v[180:183]\n" \
"v_mfma_f32_16x16x32_bf16 v[176:179], v[32:35], v[216:219], v[176:179]\n" \
"v_mfma_f32_16x16x32_bf16 v[176:179], v[36:39], v[220:223], v[176:179]\n" \
"s_waitcnt lgkmcnt(1)\n" \
"global_load_lds_dwordx4 v227, s[46:47] offset:3072\n" \
"v_mfma_f32_16x16x32_bf16 v[64:67], v[0:3], v[192:195], v[64:67]\n" \
"s_add_u32 s46, s46, 0x100\n" \
"s_addc_u32 s47, s47, 0\n" \
"ds_read_b128 v[216:219], v230 offset:6144\n" \
"v_mfma_f32_16x16x32_bf16 v[64:67], v[4:7], v[196:199], v[64:67]\n" \
"ds_read_b128 v[220:223], v231 offset:6144\n" \
"v_mfma_f32_16x16x32_bf16 v[68:71], v[8:11], v[192:195], v[68:71]\n" \
"ds_read_b64_tr_b16 v[32:33], v232 offset:1024\n" \
"v_mfma_f32_16x16x32_bf16 v[68:71], v[12:15], v[196:199], v[68:71]\n" \
"buffer_store_dwordx4 a[148:151], v248, s[56:59], s90 offen offset:1024 sc1\n" \
"v_mfma_f32_16x16x32_bf16 v[72:75], v[16:19], v[192:195], v[72:75]\n" \
"ds_read_b64_tr_b16 v[34:35], v232 offset:3072\n" \
"v_mfma_f32_16x16x32_bf16 v[72:75], v[20:23], v[196:199], v[72:75]\n" \
"s_waitcnt lgkmcnt(3)\n" \
"ds_read_b64_tr_b16 v[36:37], v232 offset:9216\n" \
"v_mfma_f32_16x16x32_bf16 v[76:79], v[24:27], v[192:195], v[76:79]\n" \
"ds_read_b64_tr_b16 v[38:39], v232 offset:11264\n" \
"v_mfma_f32_16x16x32_bf16 v[76:79], v[28:31], v[196:199], v[76:79]\n" \
"v_mfma_f32_16x16x32_bf16 v[108:111], v[24:27], v[200:203], v[108:111]\n" \
"ds_read_b64_tr_b16 v[40:41], v233 offset:1024\n" \
"v_mfma_f32_16x16x32_bf16 v[108:111], v[28:31], v[204:207], v[108:111]\n" \
"ds_read_b64_tr_b16 v[42:43], v233 offset:3072\n" \
"v_mfma_f32_16x16x32_bf16 v[104:107], v[16:19], v[200:203], v[104:107]\n" \
"ds_read_b64_tr_b16 v[44:45], v233 offset:9216\n" \
"v_mfma_f32_16x16x32_bf16 v[104:107], v[20:23], v[204:207], v[104:107]\n" \
"v_mfma_f32_16x16x32_bf16 v[100:103], v[8:11], v[200:203], v[100:103]\n" \
"ds_read_b64_tr_b16 v[46:47], v233 offset:11264\n" \
"v_mfma_f32_16x16x32_bf16 v[100:103], v[12:15], v[204:207], v[100:103]\n" \
"ds_read_b64_tr_b16 v[48:49], v232 offset:1536\n" \
"v_mfma_f32_16x16x32_bf16 v[96:99], v[0:3], v[200:203], v[96:99]\n" \
"s_add_u32 m0, s95, 4096\n" \
"ds_read_b64_tr_b16 v[50:51], v232 offset:3584\n" \
"v_mfma_f32_16x16x32_bf16 v[96:99], v[4:7], v[204:207], v[96:99]\n" \
"global_load_lds_dwordx4 v228, s[44:45] offset:-4096\n" \
"v_mfma_f32_16x16x32_bf16 v[128:131], v[0:3], v[208:211], v[128:131]\n" \
"ds_read_b64_tr_b16 v[52:53], v232 offset:9728\n" \
"v_mfma_f32_16x16x32_bf16 v[128:131], v[4:7], v[212:215], v[128:131]\n" \
"ds_read_b64_tr_b16 v[54:55], v232 offset:11776\n" \
"v_mfma_f32_16x16x32_bf16 v[132:135], v[8:11], v[208:211], v[132:135]\n" \
"ds_read_b64_tr_b16 v[56:57], v233 offset:1536\n" \
"v_mfma_f32_16x16x32_bf16 v[132:135], v[12:15], v[212:215], v[132:135]\n" \
"global_load_lds_dwordx4 v229, s[44:45] offset:-3072\n" \
"v_mfma_f32_16x16x32_bf16 v[136:139], v[16:19], v[208:211], v[136:139]\n" \
"s_add_u32 s44, s44, 0x80000\n" \
"s_addc_u32 s45, s45, 0\n" \
"ds_read_b64_tr_b16 v[58:59], v233 offset:3584\n" \
"v_mfma_f32_16x16x32_bf16 v[136:139], v[20:23], v[212:215], v[136:139]\n" \
"ds_read_b64_tr_b16 v[60:61], v233 offset:9728\n" \
"v_mfma_f32_16x16x32_bf16 v[140:143], v[24:27], v[208:211], v[140:143]\n" \
"ds_read_b64_tr_b16 v[62:63], v233 offset:11776\n" \
"v_mfma_f32_16x16x32_bf16 v[140:143], v[28:31], v[212:215], v[140:143]\n" \
"s_waitcnt lgkmcnt(3)\n" \
"global_load_lds_dwordx4 v228, s[42:43] offset:-2048\n" \
"v_mfma_f32_16x16x32_bf16 v[172:175], v[24:27], v[216:219], v[172:175]\n" \
"v_add_u32 v230, 0xffff0000, v230\n" \
"v_add_u32 v231, 0xffff0000, v231\n" \
"v_mfma_f32_16x16x32_bf16 v[172:175], v[28:31], v[220:223], v[172:175]\n" \
"v_add_u32 v232, 0x4000, v232\n" \
"v_add_u32 v233, 0x4000, v233\n" \
"v_mfma_f32_16x16x32_bf16 v[168:171], v[16:19], v[216:219], v[168:171]\n" \
"v_mfma_f32_16x16x32_bf16 v[168:171], v[20:23], v[220:223], v[168:171]\n" \
"global_load_lds_dwordx4 v229, s[42:43] offset:-1024\n" \
"v_mfma_f32_16x16x32_bf16 v[164:167], v[8:11], v[216:219], v[164:167]\n" \
"s_add_u32 s42, s42, 0x80000\n" \
"s_addc_u32 s43, s43, 0\n" \
"v_mfma_f32_16x16x32_bf16 v[164:167], v[12:15], v[220:223], v[164:167]\n" \
"v_mfma_f32_16x16x32_bf16 v[160:163], v[0:3], v[216:219], v[160:163]\n" \
"s_waitcnt vmcnt(20) lgkmcnt(0)\n" \
"v_mfma_f32_16x16x32_bf16 v[160:163], v[4:7], v[220:223], v[160:163]\n" \
"s_barrier\n" \
"v_mfma_f32_16x16x32_bf16 v[80:83], v[32:35], v[192:195], v[80:83]\n" \
"ds_read_b64_tr_b16 v[0:1], v232 offset:0\n" \
"v_mfma_f32_16x16x32_bf16 v[80:83], v[36:39], v[196:199], v[80:83]\n" \
"ds_read_b64_tr_b16 v[2:3], v232 offset:2048\n" \
"v_mfma_f32_16x16x32_bf16 v[84:87], v[40:43], v[192:195], v[84:87]\n" \
"s_add_u32 m0, s87, 135168\n" \
"ds_read_b64_tr_b16 v[4:5], v232 offset:8192\n" \
"v_mfma_f32_16x16x32_bf16 v[84:87], v[44:47], v[196:199], v[84:87]\n" \
"global_load_lds_dwordx4 v224, s[48:49] offset:-4096\n" \
"v_mfma_f32_16x16x32_bf16 v[88:91], v[48:51], v[192:195], v[88:91]\n" \
"ds_read_b64_tr_b16 v[6:7], v232 offset:10240\n" \
"v_mfma_f32_16x16x32_bf16 v[88:91], v[52:55], v[196:199], v[88:91]\n" \
"ds_read_b64_tr_b16 v[8:9], v233 offset:0\n" \
"v_mfma_f32_16x16x32_bf16 v[92:95], v[56:59], v[192:195], v[92:95]\n" \
"ds_read_b64_tr_b16 v[10:11], v233 offset:2048\n" \
"v_mfma_f32_16x16x32_bf16 v[92:95], v[60:63], v[196:199], v[92:95]\n" \
"global_load_lds_dwordx4 v225, s[48:49] offset:-3072\n" \
"v_mfma_f32_16x16x32_bf16 v[124:127], v[56:59], v[200:203], v[124:127]\n" \
"ds_read_b128 v[192:195], v230 offset:0\n" \
"v_mfma_f32_16x16x32_bf16 v[124:127], v[60:63], v[204:207], v[124:127]\n" \
"ds_read_b128 v[196:199], v231 offset:0\n" \
"v_mfma_f32_16x16x32_bf16 v[120:123], v[48:51], v[200:203], v[120:123]\n" \
"ds_read_b64_tr_b16 v[12:13], v233 offset:8192\n" \
"v_mfma_f32_16x16x32_bf16 v[120:123], v[52:55], v[204:207], v[120:123]\n" \
"global_load_lds_dwordx4 v226, s[48:49] offset:-2048\n" \
"v_mfma_f32_16x16x32_bf16 v[116:119], v[40:43], v[200:203], v[116:119]\n" \
"ds_read_b64_tr_b16 v[14:15], v233 offset:10240\n" \
"v_mfma_f32_16x16x32_bf16 v[116:119], v[44:47], v[204:207], v[116:119]\n" \
"ds_read_b64_tr_b16 v[16:17], v232 offset:512\n" \
"v_mfma_f32_16x16x32_bf16 v[112:115], v[32:35], v[200:203], v[112:115]\n" \
"ds_read_b64_tr_b16 v[18:19], v232 offset:2560\n" \
"v_mfma_f32_16x16x32_bf16 v[112:115], v[36:39], v[204:207], v[112:115]\n" \
"global_load_lds_dwordx4 v227, s[48:49] offset:-1024\n" \
"v_mfma_f32_16x16x32_bf16 v[144:147], v[32:35], v[208:211], v[144:147]\n" \
"s_add_u32 s48, s48, 0x80\n" \
"s_addc_u32 s49, s49, 0\n" \
"ds_read_b128 v[200:203], v230 offset:2048\n" \
"v_mfma_f32_16x16x32_bf16 v[144:147], v[36:39], v[212:215], v[144:147]\n" \
"ds_read_b128 v[204:207], v231 offset:2048\n" \
"v_mfma_f32_16x16x32_bf16 v[148:151], v[40:43], v[208:211], v[148:151]\n" \
"ds_read_b64_tr_b16 v[20:21], v232 offset:8704\n" \
"v_mfma_f32_16x16x32_bf16 v[148:151], v[44:47], v[212:215], v[148:151]\n" \
"global_load_lds_dwordx4 v224, s[46:47] offset:0\n" \
"v_mfma_f32_16x16x32_bf16 v[152:155], v[48:51], v[208:211], v[152:155]\n" \
"ds_read_b64_tr_b16 v[22:23], v232 offset:10752\n" \
"v_mfma_f32_16x16x32_bf16 v[152:155], v[52:55], v[212:215], v[152:155]\n" \
"ds_read_b64_tr_b16 v[24:25], v233 offset:512\n" \
"v_mfma_f32_16x16x32_bf16 v[156:159], v[56:59], v[208:211], v[156:159]\n" \
"ds_read_b64_tr_b16 v[26:27], v233 offset:2560\n" \
"v_mfma_f32_16x16x32_bf16 v[156:159], v[60:63], v[212:215], v[156:159]\n" \
"global_load_lds_dwordx4 v225, s[46:47] offset:1024\n" \
"v_mfma_f32_16x16x32_bf16 v[188:191], v[56:59], v[216:219], v[188:191]\n" \
"ds_read_b128 v[208:211], v230 offset:4096\n" \
"v_mfma_f32_16x16x32_bf16 v[188:191], v[60:63], v[220:223], v[188:191]\n" \
"ds_read_b128 v[212:215], v231 offset:4096\n" \
"v_mfma_f32_16x16x32_bf16 v[184:187], v[48:51], v[216:219], v[184:187]\n" \
"ds_read_b64_tr_b16 v[28:29], v233 offset:8704\n" \
"v_mfma_f32_16x16x32_bf16 v[184:187], v[52:55], v[220:223], v[184:187]\n" \
"global_load_lds_dwordx4 v226, s[46:47] offset:2048\n" \
"v_mfma_f32_16x16x32_bf16 v[180:183], v[40:43], v[216:219], v[180:183]\n" \
"ds_read_b64_tr_b16 v[30:31], v233 offset:10752\n" \
"v_mfma_f32_16x16x32_bf16 v[180:183], v[44:47], v[220:223], v[180:183]\n" \
"v_mfma_f32_16x16x32_bf16 v[176:179], v[32:35], v[216:219], v[176:179]\n" \
"v_mfma_f32_16x16x32_bf16 v[176:179], v[36:39], v[220:223], v[176:179]\n" \
"s_waitcnt lgkmcnt(1)\n" \
"global_load_lds_dwordx4 v227, s[46:47] offset:3072\n" \
"v_mfma_f32_16x16x32_bf16 v[64:67], v[0:3], v[192:195], v[64:67]\n" \
"s_add_u32 s46, s46, 0x80\n" \
"s_addc_u32 s47, s47, 0\n" \
"ds_read_b128 v[216:219], v230 offset:6144\n" \
"v_mfma_f32_16x16x32_bf16 v[64:67], v[4:7], v[196:199], v[64:67]\n" \
"ds_read_b128 v[220:223], v231 offset:6144\n" \
"v_mfma_f32_16x16x32_bf16 v[68:71], v[8:11], v[192:195], v[68:71]\n" \
"ds_read_b64_tr_b16 v[32:33], v232 offset:1024\n" \
"v_mfma_f32_16x16x32_bf16 v[68:71], v[12:15], v[196:199], v[68:71]\n" \
"buffer_store_dwordx4 a[152:155], v248, s[56:59], s90 offen offset:2048 sc1\n" \
"v_mfma_f32_16x16x32_bf16 v[72:75], v[16:19], v[192:195], v[72:75]\n" \
"ds_read_b64_tr_b16 v[34:35], v232 offset:3072\n" \
"v_mfma_f32_16x16x32_bf16 v[72:75], v[20:23], v[196:199], v[72:75]\n" \
"s_waitcnt lgkmcnt(3)\n" \
"ds_read_b64_tr_b16 v[36:37], v232 offset:9216\n" \
"v_mfma_f32_16x16x32_bf16 v[76:79], v[24:27], v[192:195], v[76:79]\n" \
"ds_read_b64_tr_b16 v[38:39], v232 offset:11264\n" \
"v_mfma_f32_16x16x32_bf16 v[76:79], v[28:31], v[196:199], v[76:79]\n" \
"v_mfma_f32_16x16x32_bf16 v[108:111], v[24:27], v[200:203], v[108:111]\n" \
"ds_read_b64_tr_b16 v[40:41], v233 offset:1024\n" \
"v_mfma_f32_16x16x32_bf16 v[108:111], v[28:31], v[204:207], v[108:111]\n" \
"ds_read_b64_tr_b16 v[42:43], v233 offset:3072\n" \
"v_mfma_f32_16x16x32_bf16 v[104:107], v[16:19], v[200:203], v[104:107]\n" \
"ds_read_b64_tr_b16 v[44:45], v233 offset:9216\n" \
"v_mfma_f32_16x16x32_bf16 v[104:107], v[20:23], v[204:207], v[104:107]\n" \
"v_mfma_f32_16x16x32_bf16 v[100:103], v[8:11], v[200:203], v[100:103]\n" \
"ds_read_b64_tr_b16 v[46:47], v233 offset:11264\n" \
"v_mfma_f32_16x16x32_bf16 v[100:103], v[12:15], v[204:207], v[100:103]\n" \
"ds_read_b64_tr_b16 v[48:49], v232 offset:1536\n" \
"v_mfma_f32_16x16x32_bf16 v[96:99], v[0:3], v[200:203], v[96:99]\n" \
"s_add_u32 m0, s95, 20480\n" \
"ds_read_b64_tr_b16 v[50:51], v232 offset:3584\n" \
"v_mfma_f32_16x16x32_bf16 v[96:99], v[4:7], v[204:207], v[96:99]\n" \
"global_load_lds_dwordx4 v228, s[44:45] offset:-4096\n" \
"v_mfma_f32_16x16x32_bf16 v[128:131], v[0:3], v[208:211], v[128:131]\n" \
"ds_read_b64_tr_b16 v[52:53], v232 offset:9728\n" \
"v_mfma_f32_16x16x32_bf16 v[128:131], v[4:7], v[212:215], v[128:131]\n" \
"ds_read_b64_tr_b16 v[54:55], v232 offset:11776\n" \
"v_mfma_f32_16x16x32_bf16 v[132:135], v[8:11], v[208:211], v[132:135]\n" \
"ds_read_b64_tr_b16 v[56:57], v233 offset:1536\n" \
"v_mfma_f32_16x16x32_bf16 v[132:135], v[12:15], v[212:215], v[132:135]\n" \
"global_load_lds_dwordx4 v229, s[44:45] offset:-3072\n" \
"v_mfma_f32_16x16x32_bf16 v[136:139], v[16:19], v[208:211], v[136:139]\n" \
"s_add_u32 s44, s44, 0x80000\n" \
"s_addc_u32 s45, s45, 0\n" \
"ds_read_b64_tr_b16 v[58:59], v233 offset:3584\n" \
"v_mfma_f32_16x16x32_bf16 v[136:139], v[20:23], v[212:215], v[136:139]\n" \
"ds_read_b64_tr_b16 v[60:61], v233 offset:9728\n" \
"v_mfma_f32_16x16x32_bf16 v[140:143], v[24:27], v[208:211], v[140:143]\n" \
"ds_read_b64_tr_b16 v[62:63], v233 offset:11776\n" \
"v_mfma_f32_16x16x32_bf16 v[140:143], v[28:31], v[212:215], v[140:143]\n" \
"s_waitcnt lgkmcnt(3)\n" \
"global_load_lds_dwordx4 v228, s[42:43] offset:-2048\n" \
"v_mfma_f32_16x16x32_bf16 v[172:175], v[24:27], v[216:219], v[172:175]\n" \
"v_add_u32 v230, 0x8000, v230\n" \
"v_add_u32 v231, 0x8000, v231\n" \
"v_mfma_f32_16x16x32_bf16 v[172:175], v[28:31], v[220:223], v[172:175]\n" \
"v_add_u32 v232, 0x4000, v232\n" \
"v_add_u32 v233, 0x4000, v233\n" \
"v_mfma_f32_16x16x32_bf16 v[168:171], v[16:19], v[216:219], v[168:171]\n" \
"v_mfma_f32_16x16x32_bf16 v[168:171], v[20:23], v[220:223], v[168:171]\n" \
"global_load_lds_dwordx4 v229, s[42:43] offset:-1024\n" \
"v_mfma_f32_16x16x32_bf16 v[164:167], v[8:11], v[216:219], v[164:167]\n" \
"s_add_u32 s42, s42, 0x80000\n" \
"s_addc_u32 s43, s43, 0\n" \
"v_mfma_f32_16x16x32_bf16 v[164:167], v[12:15], v[220:223], v[164:167]\n" \
"v_mfma_f32_16x16x32_bf16 v[160:163], v[0:3], v[216:219], v[160:163]\n" \
"s_waitcnt vmcnt(18) lgkmcnt(0)\n" \
"v_mfma_f32_16x16x32_bf16 v[160:163], v[4:7], v[220:223], v[160:163]\n" \
"s_barrier\n" \
"v_mfma_f32_16x16x32_bf16 v[80:83], v[32:35], v[192:195], v[80:83]\n" \
"ds_read_b64_tr_b16 v[0:1], v232 offset:0\n" \
"v_mfma_f32_16x16x32_bf16 v[80:83], v[36:39], v[196:199], v[80:83]\n" \
"ds_read_b64_tr_b16 v[2:3], v232 offset:2048\n" \
"v_mfma_f32_16x16x32_bf16 v[84:87], v[40:43], v[192:195], v[84:87]\n" \
"s_add_u32 m0, s87, 69632\n" \
"ds_read_b64_tr_b16 v[4:5], v232 offset:8192\n" \
"v_mfma_f32_16x16x32_bf16 v[84:87], v[44:47], v[196:199], v[84:87]\n" \
"global_load_lds_dwordx4 v224, s[48:49] offset:-4096\n" \
"v_mfma_f32_16x16x32_bf16 v[88:91], v[48:51], v[192:195], v[88:91]\n" \
"ds_read_b64_tr_b16 v[6:7], v232 offset:10240\n" \
"v_mfma_f32_16x16x32_bf16 v[88:91], v[52:55], v[196:199], v[88:91]\n" \
"ds_read_b64_tr_b16 v[8:9], v233 offset:0\n" \
"v_mfma_f32_16x16x32_bf16 v[92:95], v[56:59], v[192:195], v[92:95]\n" \
"ds_read_b64_tr_b16 v[10:11], v233 offset:2048\n" \
"v_mfma_f32_16x16x32_bf16 v[92:95], v[60:63], v[196:199], v[92:95]\n" \
"global_load_lds_dwordx4 v225, s[48:49] offset:-3072\n" \
"v_mfma_f32_16x16x32_bf16 v[124:127], v[56:59], v[200:203], v[124:127]\n" \
"ds_read_b128 v[192:195], v230 offset:0\n" \
"v_mfma_f32_16x16x32_bf16 v[124:127], v[60:63], v[204:207], v[124:127]\n" \
"ds_read_b128 v[196:199], v231 offset:0\n" \
"v_mfma_f32_16x16x32_bf16 v[120:123], v[48:51], v[200:203], v[120:123]\n" \
"ds_read_b64_tr_b16 v[12:13], v233 offset:8192\n" \
"v_mfma_f32_16x16x32_bf16 v[120:123], v[52:55], v[204:207], v[120:123]\n" \
"global_load_lds_dwordx4 v226, s[48:49] offset:-2048\n" \
"v_mfma_f32_16x16x32_bf16 v[116:119], v[40:43], v[200:203], v[116:119]\n" \
"ds_read_b64_tr_b16 v[14:15], v233 offset:10240\n" \
"v_mfma_f32_16x16x32_bf16 v[116:119], v[44:47], v[204:207], v[116:119]\n" \
"ds_read_b64_tr_b16 v[16:17], v232 offset:512\n" \
"v_mfma_f32_16x16x32_bf16 v[112:115], v[32:35], v[200:203], v[112:115]\n" \
"ds_read_b64_tr_b16 v[18:19], v232 offset:2560\n" \
"v_mfma_f32_16x16x32_bf16 v[112:115], v[36:39], v[204:207], v[112:115]\n" \
"global_load_lds_dwordx4 v227, s[48:49] offset:-1024\n" \
"v_mfma_f32_16x16x32_bf16 v[144:147], v[32:35], v[208:211], v[144:147]\n" \
"s_add_u32 s48, s48, 0x80\n" \
"s_addc_u32 s49, s49, 0\n" \
"ds_read_b128 v[200:203], v230 offset:2048\n" \
"v_mfma_f32_16x16x32_bf16 v[144:147], v[36:39], v[212:215], v[144:147]\n" \
"ds_read_b128 v[204:207], v231 offset:2048\n" \
"v_mfma_f32_16x16x32_bf16 v[148:151], v[40:43], v[208:211], v[148:151]\n" \
"ds_read_b64_tr_b16 v[20:21], v232 offset:8704\n" \
"v_mfma_f32_16x16x32_bf16 v[148:151], v[44:47], v[212:215], v[148:151]\n" \
"global_load_lds_dwordx4 v224, s[46:47] offset:0\n" \
"v_mfma_f32_16x16x32_bf16 v[152:155], v[48:51], v[208:211], v[152:155]\n" \
"ds_read_b64_tr_b16 v[22:23], v232 offset:10752\n" \
"v_mfma_f32_16x16x32_bf16 v[152:155], v[52:55], v[212:215], v[152:155]\n" \
"ds_read_b64_tr_b16 v[24:25], v233 offset:512\n" \
"v_mfma_f32_16x16x32_bf16 v[156:159], v[56:59], v[208:211], v[156:159]\n" \
"ds_read_b64_tr_b16 v[26:27], v233 offset:2560\n" \
"v_mfma_f32_16x16x32_bf16 v[156:159], v[60:63], v[212:215], v[156:159]\n" \
"global_load_lds_dwordx4 v225, s[46:47] offset:1024\n" \
"v_mfma_f32_16x16x32_bf16 v[188:191], v[56:59], v[216:219], v[188:191]\n" \
"ds_read_b128 v[208:211], v230 offset:4096\n" \
"v_mfma_f32_16x16x32_bf16 v[188:191], v[60:63], v[220:223], v[188:191]\n" \
"ds_read_b128 v[212:215], v231 offset:4096\n" \
"v_mfma_f32_16x16x32_bf16 v[184:187], v[48:51], v[216:219], v[184:187]\n" \
"ds_read_b64_tr_b16 v[28:29], v233 offset:8704\n" \
"v_mfma_f32_16x16x32_bf16 v[184:187], v[52:55], v[220:223], v[184:187]\n" \
"global_load_lds_dwordx4 v226, s[46:47] offset:2048\n" \
"v_mfma_f32_16x16x32_bf16 v[180:183], v[40:43], v[216:219], v[180:183]\n" \
"ds_read_b64_tr_b16 v[30:31], v233 offset:10752\n" \
"v_mfma_f32_16x16x32_bf16 v[180:183], v[44:47], v[220:223], v[180:183]\n" \
"v_mfma_f32_16x16x32_bf16 v[176:179], v[32:35], v[216:219], v[176:179]\n" \
"v_mfma_f32_16x16x32_bf16 v[176:179], v[36:39], v[220:223], v[176:179]\n" \
"s_waitcnt lgkmcnt(1)\n" \
"global_load_lds_dwordx4 v227, s[46:47] offset:3072\n" \
"v_mfma_f32_16x16x32_bf16 v[64:67], v[0:3], v[192:195], v[64:67]\n" \
"s_add_u32 s46, s46, 0x80\n" \
"s_addc_u32 s47, s47, 0\n" \
"ds_read_b128 v[216:219], v230 offset:6144\n" \
"v_mfma_f32_16x16x32_bf16 v[64:67], v[4:7], v[196:199], v[64:67]\n" \
"s_mul_i32 s77, s62, 557824\n" \
"ds_read_b128 v[220:223], v231 offset:6144\n" \
"s_add_u32 s77, s77, s95\n" \
"v_mfma_f32_16x16x32_bf16 v[68:71], v[8:11], v[192:195], v[68:71]\n" \
"s_add_u32 s36, s28, s77\n" \
"s_mov_b32 s37, s29\n" \
"ds_read_b64_tr_b16 v[32:33], v232 offset:1024\n" \
"v_mfma_f32_16x16x32_bf16 v[68:71], v[12:15], v[196:199], v[68:71]\n" \
"buffer_store_dwordx4 a[156:159], v248, s[56:59], s90 offen offset:3072 sc1\n" \
"s_add_u32 s77, s77, 17850368\n" \
"s_add_u32 s38, s28, s77\n" \
"s_mov_b32 s39, s29\n" \
"s_and_b32 s74, s63, 1\n" \
"v_mfma_f32_16x16x32_bf16 v[72:75], v[16:19], v[192:195], v[72:75]\n" \
"s_add_u32 s90, s88, 278528\n" \
"s_lshl_b32 s74, s74, 15\n" \
"ds_read_b64_tr_b16 v[34:35], v232 offset:3072\n" \
"v_mfma_f32_16x16x32_bf16 v[72:75], v[20:23], v[196:199], v[72:75]\n" \
"s_waitcnt lgkmcnt(3)\n" \
"s_xor_b32 s74, s74, 0x18000\n" \
"ds_read_b64_tr_b16 v[36:37], v232 offset:9216\n" \
"v_mfma_f32_16x16x32_bf16 v[76:79], v[24:27], v[192:195], v[76:79]\n" \
"ds_read_b64_tr_b16 v[38:39], v232 offset:11264\n" \
"v_add_u32 v247, s74, v248\n" \
"v_mfma_f32_16x16x32_bf16 v[76:79], v[28:31], v[196:199], v[76:79]\n" \
"buffer_store_dwordx4 a[64:67], v249, s[56:59], s90 offen offset:0 sc1\n" \
"s_add_u32 s74, s74, s95\n" \
"s_lshr_b32 s83, s61, 1\n" \
"s_lshl_b32 s75, s63, 2\n" \
"s_add_u32 s77, s83, s75\n" \
"v_mfma_f32_16x16x32_bf16 v[108:111], v[24:27], v[200:203], v[108:111]\n" \
"s_mul_i32 s77, s77, 266240\n" \
"s_sub_u32 s78, s83, s75\n" \
"ds_read_b64_tr_b16 v[40:41], v233 offset:1024\n" \
"v_mfma_f32_16x16x32_bf16 v[108:111], v[28:31], v[204:207], v[108:111]\n" \
"s_add_u32 s78, s78, 60\n" \
"s_mul_i32 s78, s78, 266240\n" \
"ds_read_b64_tr_b16 v[42:43], v233 offset:3072\n" \
"v_mfma_f32_16x16x32_bf16 v[104:107], v[16:19], v[200:203], v[104:107]\n" \
"s_and_b32 s82, s61, 1\n" \
"ds_read_b128 a[64:67], v254\n" \
"ds_read_b64_tr_b16 v[44:45], v233 offset:9216\n" \
"v_mfma_f32_16x16x32_bf16 v[104:107], v[20:23], v[204:207], v[104:107]\n" \
"buffer_load_dword v244, v245, s[56:59], s91 offen sc1\n" \
"v_mfma_f32_16x16x32_bf16 v[100:103], v[8:11], v[200:203], v[100:103]\n" \
"s_cmp_eq_u32 s82, 0\n" \
"s_cselect_b32 s16, 0, 0xff00\n" \
"ds_read_b64_tr_b16 v[46:47], v233 offset:11264\n" \
"v_mfma_f32_16x16x32_bf16 v[100:103], v[12:15], v[204:207], v[100:103]\n" \
"s_mov_b32 s17, s16\n" \
"s_lshl_b32 s75, s82, 8\n" \
"ds_read_b64_tr_b16 v[48:49], v232 offset:1536\n" \
"v_mfma_f32_16x16x32_bf16 v[96:99], v[0:3], v[200:203], v[96:99]\n" \
"s_add_u32 m0, s95, 36864\n" \
"s_lshl_b32 s76, s62, 13\n" \
"ds_read_b64_tr_b16 v[50:51], v232 offset:3584\n" \
"v_mfma_f32_16x16x32_bf16 v[96:99], v[4:7], v[204:207], v[96:99]\n" \
"s_add_u32 s75, s75, s76\n" \
"ds_read_b128 a[68:71], v254\n" \
"global_load_lds_dwordx4 v228, s[44:45] offset:-4096\n" \
"v_mfma_f32_16x16x32_bf16 v[128:131], v[0:3], v[208:211], v[128:131]\n" \
"s_add_u32 s75, s75, 35700736\n" \
"s_add_u32 s77, s77, s75\n" \
"ds_read_b64_tr_b16 v[52:53], v232 offset:9728\n" \
"v_mfma_f32_16x16x32_bf16 v[128:131], v[4:7], v[212:215], v[128:131]\n" \
"s_add_u32 s78, s78, s75\n" \
"s_mul_i32 s83, s83, 9216\n" \
"ds_read_b64_tr_b16 v[54:55], v232 offset:11776\n" \
"v_mfma_f32_16x16x32_bf16 v[132:135], v[8:11], v[208:211], v[132:135]\n" \
"s_lshl_b32 s75, s82, 12\n" \
"s_add_u32 s83, s83, s75\n" \
"ds_read_b64_tr_b16 v[56:57], v233 offset:1536\n" \
"v_mfma_f32_16x16x32_bf16 v[132:135], v[12:15], v[212:215], v[132:135]\n" \
"s_add_u32 s83, s83, 4096\n" \
"global_load_lds_dwordx4 v229, s[44:45] offset:-3072\n" \
"v_mfma_f32_16x16x32_bf16 v[136:139], v[16:19], v[208:211], v[136:139]\n" \
"s_add_u32 s44, s44, 0x80000\n" \
"s_addc_u32 s45, s45, 0\n" \
"ds_read_b64_tr_b16 v[58:59], v233 offset:3584\n" \
"v_mfma_f32_16x16x32_bf16 v[136:139], v[20:23], v[212:215], v[136:139]\n" \
"s_lshl_b32 s82, s82, 14\n" \
"ds_read_b64_tr_b16 v[60:61], v233 offset:9728\n" \
"v_mfma_f32_16x16x32_bf16 v[140:143], v[24:27], v[208:211], v[140:143]\n" \
"s_cmp_eq_u32 s63, 7\n" \
"s_cselect_b32 s82, 0, s82\n" \
"ds_read_b64_tr_b16 v[62:63], v233 offset:11776\n" \
"v_mfma_f32_16x16x32_bf16 v[140:143], v[28:31], v[212:215], v[140:143]\n" \
"s_waitcnt lgkmcnt(3)\n" \
"global_load_lds_dwordx4 v228, s[42:43] offset:-2048\n" \
"s_mov_b32 s75, s63\n" \
"v_mfma_f32_16x16x32_bf16 v[172:175], v[24:27], v[216:219], v[172:175]\n" \
"s_sub_u32 s76, 7, s63\n" \
"s_mov_b32 s73, s95\n" \
"s_mov_b64 s[40:41], s[14:15]\n" \
"v_mfma_f32_16x16x32_bf16 v[172:175], v[28:31], v[220:223], v[172:175]\n" \
"s_mov_b32 s80, 0\n" \
"v_add_u32 v230, 0x8000, v230\n" \
"s_mov_b32 s81, 0\n" \
"v_mfma_f32_16x16x32_bf16 v[168:171], v[16:19], v[216:219], v[168:171]\n" \
"s_cmp_eq_u32 s63, 7\n" \
"s_cselect_b64 s[40:41], s[18:19], s[40:41]\n" \
"s_cselect_b32 s80, 12, s80\n" \
"v_mfma_f32_16x16x32_bf16 v[168:171], v[20:23], v[220:223], v[168:171]\n" \
"global_load_lds_dwordx4 v229, s[42:43] offset:-1024\n" \
"v_add_u32 v231, 0x8000, v231\n" \
"v_mfma_f32_16x16x32_bf16 v[164:167], v[8:11], v[216:219], v[164:167]\n" \
"s_add_u32 s42, s42, 0x80000\n" \
"s_addc_u32 s43, s43, 0\n" \
"v_add_u32 v232, 0xffff4000, v232\n" \
"v_mfma_f32_16x16x32_bf16 v[164:167], v[12:15], v[220:223], v[164:167]\n" \
"s_cmp_eq_u32 s63, 6\n" \
"s_cselect_b64 s[40:41], s[22:23], s[40:41]\n" \
"s_cselect_b32 s80, 12, s80\n" \
"v_mfma_f32_16x16x32_bf16 v[160:163], v[0:3], v[216:219], v[160:163]\n" \
"s_waitcnt vmcnt(20) lgkmcnt(0)\n" \
"v_add_u32 v233, 0xffff4000, v233\n" \
"v_mfma_f32_16x16x32_bf16 v[160:163], v[4:7], v[220:223], v[160:163]\n" \
"s_barrier\n" \
"v_mfma_f32_16x16x32_bf16 v[80:83], v[32:35], v[192:195], v[80:83]\n" \
"ds_read_b64_tr_b16 v[0:1], v232 offset:0\n" \
"v_mfma_f32_16x16x32_bf16 v[80:83], v[36:39], v[196:199], v[80:83]\n" \
"ds_read_b64_tr_b16 v[2:3], v232 offset:2048\n" \
"v_mfma_f32_16x16x32_bf16 v[84:87], v[40:43], v[192:195], v[84:87]\n" \
"s_add_u32 m0, s87, 102400\n" \
"ds_read_b64_tr_b16 v[4:5], v232 offset:8192\n" \
"v_mfma_f32_16x16x32_bf16 v[84:87], v[44:47], v[196:199], v[84:87]\n" \
"global_load_lds_dwordx4 v224, s[48:49] offset:-4096\n" \
"v_mfma_f32_16x16x32_bf16 v[88:91], v[48:51], v[192:195], v[88:91]\n" \
"ds_read_b64_tr_b16 v[6:7], v232 offset:10240\n" \
"v_mfma_f32_16x16x32_bf16 v[88:91], v[52:55], v[196:199], v[88:91]\n" \
"ds_read_b64_tr_b16 v[8:9], v233 offset:0\n" \
"v_mfma_f32_16x16x32_bf16 v[92:95], v[56:59], v[192:195], v[92:95]\n" \
"ds_read_b64_tr_b16 v[10:11], v233 offset:2048\n" \
"v_mfma_f32_16x16x32_bf16 v[92:95], v[60:63], v[196:199], v[92:95]\n" \
"global_load_lds_dwordx4 v225, s[48:49] offset:-3072\n" \
"v_mfma_f32_16x16x32_bf16 v[124:127], v[56:59], v[200:203], v[124:127]\n" \
"ds_read_b128 v[192:195], v230 offset:0\n" \
"v_mfma_f32_16x16x32_bf16 v[124:127], v[60:63], v[204:207], v[124:127]\n" \
"ds_read_b128 v[196:199], v231 offset:0\n" \
"v_mfma_f32_16x16x32_bf16 v[120:123], v[48:51], v[200:203], v[120:123]\n" \
"ds_read_b64_tr_b16 v[12:13], v233 offset:8192\n" \
"v_mfma_f32_16x16x32_bf16 v[120:123], v[52:55], v[204:207], v[120:123]\n" \
"global_load_lds_dwordx4 v226, s[48:49] offset:-2048\n" \
"v_mfma_f32_16x16x32_bf16 v[116:119], v[40:43], v[200:203], v[116:119]\n" \
"ds_read_b64_tr_b16 v[14:15], v233 offset:10240\n" \
"v_mfma_f32_16x16x32_bf16 v[116:119], v[44:47], v[204:207], v[116:119]\n" \
"ds_read_b64_tr_b16 v[16:17], v232 offset:512\n" \
"v_mfma_f32_16x16x32_bf16 v[112:115], v[32:35], v[200:203], v[112:115]\n" \
"ds_read_b64_tr_b16 v[18:19], v232 offset:2560\n" \
"v_mfma_f32_16x16x32_bf16 v[112:115], v[36:39], v[204:207], v[112:115]\n" \
"global_load_lds_dwordx4 v227, s[48:49] offset:-1024\n" \
"v_mfma_f32_16x16x32_bf16 v[144:147], v[32:35], v[208:211], v[144:147]\n" \
"s_add_u32 s48, s48, 0x80\n" \
"s_addc_u32 s49, s49, 0\n" \
"ds_read_b128 v[200:203], v230 offset:2048\n" \
"v_mfma_f32_16x16x32_bf16 v[144:147], v[36:39], v[212:215], v[144:147]\n" \
"ds_read_b128 v[204:207], v231 offset:2048\n" \
"v_mfma_f32_16x16x32_bf16 v[148:151], v[40:43], v[208:211], v[148:151]\n" \
"ds_read_b64_tr_b16 v[20:21], v232 offset:8704\n" \
"v_mfma_f32_16x16x32_bf16 v[148:151], v[44:47], v[212:215], v[148:151]\n" \
"global_load_lds_dwordx4 v224, s[46:47] offset:0\n" \
"v_mfma_f32_16x16x32_bf16 v[152:155], v[48:51], v[208:211], v[152:155]\n" \
"ds_read_b64_tr_b16 v[22:23], v232 offset:10752\n" \
"v_mfma_f32_16x16x32_bf16 v[152:155], v[52:55], v[212:215], v[152:155]\n" \
"ds_read_b64_tr_b16 v[24:25], v233 offset:512\n" \
"v_mfma_f32_16x16x32_bf16 v[156:159], v[56:59], v[208:211], v[156:159]\n" \
"ds_read_b64_tr_b16 v[26:27], v233 offset:2560\n" \
"v_mfma_f32_16x16x32_bf16 v[156:159], v[60:63], v[212:215], v[156:159]\n" \
"global_load_lds_dwordx4 v225, s[46:47] offset:1024\n" \
"Lpg4399_%=:\n" \
"s_cbranch_execz Lpg5585_%=\n" \
"v_mfma_f32_16x16x32_bf16 v[188:191], v[56:59], v[216:219], v[188:191]\n" \
"ds_read_b128 v[208:211], v230 offset:4096\n" \
"v_mfma_f32_16x16x32_bf16 v[188:191], v[60:63], v[220:223], v[188:191]\n" \
"ds_read_b128 v[212:215], v231 offset:4096\n" \
"v_mfma_f32_16x16x32_bf16 v[184:187], v[48:51], v[216:219], v[184:187]\n" \
"ds_read_b64_tr_b16 v[28:29], v233 offset:8704\n" \
"v_mfma_f32_16x16x32_bf16 v[184:187], v[52:55], v[220:223], v[184:187]\n" \
"global_load_lds_dwordx4 v226, s[46:47] offset:2048\n" \
"v_mfma_f32_16x16x32_bf16 v[180:183], v[40:43], v[216:219], v[180:183]\n" \
"ds_read_b64_tr_b16 v[30:31], v233 offset:10752\n" \
"v_mfma_f32_16x16x32_bf16 v[180:183], v[44:47], v[220:223], v[180:183]\n" \
"s_cmp_eq_u32 s63, 5\n" \
"s_cselect_b64 s[40:41], s[18:19], s[40:41]\n" \
"s_cselect_b32 s80, 4, s80\n" \
"v_mfma_f32_16x16x32_bf16 v[176:179], v[32:35], v[216:219], v[176:179]\n" \
"s_cmp_eq_u32 s63, 5\n" \
"s_cselect_b32 s81, 384, s81\n" \
"v_mfma_f32_16x16x32_bf16 v[176:179], v[36:39], v[220:223], v[176:179]\n" \
"s_waitcnt lgkmcnt(1)\n" \
"global_load_lds_dwordx4 v227, s[46:47] offset:3072\n" \
"v_mfma_f32_16x16x32_bf16 v[64:67], v[0:3], v[192:195], v[64:67]\n" \
"s_add_u32 s46, s46, 0x80\n" \
"s_addc_u32 s47, s47, 0\n" \
"ds_read_b128 v[216:219], v230 offset:6144\n" \
"v_mfma_f32_16x16x32_bf16 v[64:67], v[4:7], v[196:199], v[64:67]\n" \
"ds_read_b128 v[220:223], v231 offset:6144\n" \
"v_mfma_f32_16x16x32_bf16 v[68:71], v[8:11], v[192:195], v[68:71]\n" \
"ds_read_b64_tr_b16 v[32:33], v232 offset:1024\n" \
"v_mfma_f32_16x16x32_bf16 v[68:71], v[12:15], v[196:199], v[68:71]\n" \
"buffer_store_dwordx4 a[96:99], v249, s[56:59], s90 offen offset:256 sc1\n" \
"s_cmp_eq_u32 s63, 4\n" \
"s_cselect_b64 s[40:41], s[20:21], s[40:41]\n" \
"s_cselect_b32 s80, 8, s80\n" \
"v_mfma_f32_16x16x32_bf16 v[72:75], v[16:19], v[192:195], v[72:75]\n" \
"ds_read_b64_tr_b16 v[34:35], v232 offset:3072\n" \
"v_mfma_f32_16x16x32_bf16 v[72:75], v[20:23], v[196:199], v[72:75]\n" \
"s_waitcnt lgkmcnt(3)\n" \
"ds_read_b64_tr_b16 v[36:37], v232 offset:9216\n" \
"v_mfma_f32_16x16x32_bf16 v[76:79], v[24:27], v[192:195], v[76:79]\n" \
"ds_read_b128 a[96:99], v254\n" \
"ds_read_b64_tr_b16 v[38:39], v232 offset:11264\n" \
"v_mfma_f32_16x16x32_bf16 v[76:79], v[28:31], v[196:199], v[76:79]\n" \
"buffer_store_dwordx4 a[72:75], v249, s[56:59], s90 offen offset:2048 sc1\n" \
"s_cmp_eq_u32 s63, 2\n" \
"s_cselect_b64 s[40:41], s[24:25], s[40:41]\n" \
"s_cselect_b32 s80, 4, s80\n" \
"s_add_u32 s81, s81, s62\n" \
"v_mfma_f32_16x16x32_bf16 v[108:111], v[24:27], v[200:203], v[108:111]\n" \
"s_lshl_b32 s81, s81, 2\n" \
"s_add_u32 s81, s81, s61\n" \
"ds_read_b64_tr_b16 v[40:41], v233 offset:1024\n" \
"v_mfma_f32_16x16x32_bf16 v[108:111], v[28:31], v[204:207], v[108:111]\n" \
"s_lshl_b32 s81, s81, 12\n" \
"ds_read_b128 a[100:103], v254\n" \
"ds_read_b64_tr_b16 v[42:43], v233 offset:3072\n" \
"v_mfma_f32_16x16x32_bf16 v[104:107], v[16:19], v[200:203], v[104:107]\n" \
"s_add_u32 s40, s40, s81\n" \
"s_addc_u32 s41, s41, 0\n" \
"ds_read_b64_tr_b16 v[44:45], v233 offset:9216\n" \
"v_mfma_f32_16x16x32_bf16 v[104:107], v[20:23], v[204:207], v[104:107]\n" \
"s_min_u32 s81, s80, 1\n" \
"s_lshl_b32 s81, s81, 2\n" \
"v_mul_u32_u24 v252, s81, v248\n" \
"v_mfma_f32_16x16x32_bf16 v[100:103], v[8:11], v[200:203], v[100:103]\n" \
"ds_read_b128 a[72:75], v254\n" \
"ds_read_b64_tr_b16 v[46:47], v233 offset:11264\n" \
"v_mfma_f32_16x16x32_bf16 v[100:103], v[12:15], v[204:207], v[100:103]\n" \
"ds_read_b128 a[76:79], v254\n" \
"ds_read_b64_tr_b16 v[48:49], v232 offset:1536\n" \
"v_mfma_f32_16x16x32_bf16 v[96:99], v[0:3], v[200:203], v[96:99]\n" \
"s_add_u32 m0, s95, 53248\n" \
"ds_read_b64_tr_b16 v[50:51], v232 offset:3584\n" \
"v_mfma_f32_16x16x32_bf16 v[96:99], v[4:7], v[204:207], v[96:99]\n" \
"global_load_lds_dwordx4 v228, s[44:45] offset:-4096\n" \
"v_mfma_f32_16x16x32_bf16 v[128:131], v[0:3], v[208:211], v[128:131]\n" \
"ds_read_b64_tr_b16 v[52:53], v232 offset:9728\n" \
"v_mfma_f32_16x16x32_bf16 v[128:131], v[4:7], v[212:215], v[128:131]\n" \
"ds_read_b64_tr_b16 v[54:55], v232 offset:11776\n" \
"v_mfma_f32_16x16x32_bf16 v[132:135], v[8:11], v[208:211], v[132:135]\n" \
"ds_read_b64_tr_b16 v[56:57], v233 offset:1536\n" \
"v_mfma_f32_16x16x32_bf16 v[132:135], v[12:15], v[212:215], v[132:135]\n" \
"global_load_lds_dwordx4 v229, s[44:45] offset:-3072\n" \
"v_mfma_f32_16x16x32_bf16 v[136:139], v[16:19], v[208:211], v[136:139]\n" \
"ds_read_b64_tr_b16 v[58:59], v233 offset:3584\n" \
"v_mfma_f32_16x16x32_bf16 v[136:139], v[20:23], v[212:215], v[136:139]\n" \
"ds_read_b64_tr_b16 v[60:61], v233 offset:9728\n" \
"v_mfma_f32_16x16x32_bf16 v[140:143], v[24:27], v[208:211], v[140:143]\n" \
"ds_read_b64_tr_b16 v[62:63], v233 offset:11776\n" \
"v_mfma_f32_16x16x32_bf16 v[140:143], v[28:31], v[212:215], v[140:143]\n" \
"s_waitcnt lgkmcnt(3)\n" \
"global_load_lds_dwordx4 v228, s[42:43] offset:-2048\n" \
"v_mfma_f32_16x16x32_bf16 v[172:175], v[24:27], v[216:219], v[172:175]\n" \
"v_add_u32 v230, 0xffff0000, v230\n" \
"v_add_u32 v231, 0xffff0000, v231\n" \
"v_mfma_f32_16x16x32_bf16 v[172:175], v[28:31], v[220:223], v[172:175]\n" \
"v_add_u32 v232, 0x4000, v232\n" \
"v_add_u32 v233, 0x4000, v233\n" \
"v_mfma_f32_16x16x32_bf16 v[168:171], v[16:19], v[216:219], v[168:171]\n" \
"v_mfma_f32_16x16x32_bf16 v[168:171], v[20:23], v[220:223], v[168:171]\n" \
"global_load_lds_dwordx4 v229, s[42:43] offset:-1024\n" \
"v_mfma_f32_16x16x32_bf16 v[164:167], v[8:11], v[216:219], v[164:167]\n" \
"v_mfma_f32_16x16x32_bf16 v[164:167], v[12:15], v[220:223], v[164:167]\n" \
"v_mfma_f32_16x16x32_bf16 v[160:163], v[0:3], v[216:219], v[160:163]\n" \
"s_waitcnt vmcnt(21) lgkmcnt(0)\n" \
"v_mfma_f32_16x16x32_bf16 v[160:163], v[4:7], v[220:223], v[160:163]\n" \
"s_barrier\n" \
"v_mfma_f32_16x16x32_bf16 v[80:83], v[32:35], v[192:195], v[80:83]\n" \
"s_waitcnt vmcnt(18)\n" \
"ds_read_b64_tr_b16 v[0:1], v232 offset:0\n" \
"v_cmp_ne_u32 vcc, s60, v244\n" \
"v_mfma_f32_16x16x32_bf16 v[80:83], v[36:39], v[196:199], v[80:83]\n" \
"ds_read_b64_tr_b16 v[2:3], v232 offset:2048\n" \
"v_mfma_f32_16x16x32_bf16 v[84:87], v[40:43], v[192:195], v[84:87]\n" \
"s_add_u32 m0, s87, 135168\n" \
"ds_read_b64_tr_b16 v[4:5], v232 offset:8192\n" \
"v_mfma_f32_16x16x32_bf16 v[84:87], v[44:47], v[196:199], v[84:87]\n" \
"s_and_b32 vcc_hi, vcc_lo, 255\n" \
"s_cbranch_scc1 Lpl1_%=\n" \
"Lpr1_%=:\n" \
"global_load_lds_dwordx4 v224, s[48:49] offset:-4096\n" \
"v_mfma_f32_16x16x32_bf16 v[88:91], v[48:51], v[192:195], v[88:91]\n" \
"s_add_u32 s79, s91, 12288\n" \
"ds_read_b64_tr_b16 v[6:7], v232 offset:10240\n" \
"v_mfma_f32_16x16x32_bf16 v[88:91], v[52:55], v[196:199], v[88:91]\n" \
"ds_read_b64_tr_b16 v[8:9], v233 offset:0\n" \
"v_mfma_f32_16x16x32_bf16 v[92:95], v[56:59], v[192:195], v[92:95]\n" \
"ds_read_b64_tr_b16 v[10:11], v233 offset:2048\n" \
"v_mfma_f32_16x16x32_bf16 v[92:95], v[60:63], v[196:199], v[92:95]\n" \
"global_load_lds_dwordx4 v225, s[48:49] offset:-3072\n" \
"v_mfma_f32_16x16x32_bf16 v[124:127], v[56:59], v[200:203], v[124:127]\n" \
"ds_read_b128 v[192:195], v230 offset:0\n" \
"v_mfma_f32_16x16x32_bf16 v[124:127], v[60:63], v[204:207], v[124:127]\n" \
"ds_read_b128 v[196:199], v231 offset:0\n" \
"v_mfma_f32_16x16x32_bf16 v[120:123], v[48:51], v[200:203], v[120:123]\n" \
"ds_read_b64_tr_b16 v[12:13], v233 offset:8192\n" \
"v_mfma_f32_16x16x32_bf16 v[120:123], v[52:55], v[204:207], v[120:123]\n" \
"global_load_lds_dwordx4 v226, s[48:49] offset:-2048\n" \
"v_mfma_f32_16x16x32_bf16 v[116:119], v[40:43], v[200:203], v[116:119]\n" \
"ds_read_b64_tr_b16 v[14:15], v233 offset:10240\n" \
"v_mfma_f32_16x16x32_bf16 v[116:119], v[44:47], v[204:207], v[116:119]\n" \
"ds_read_b64_tr_b16 v[16:17], v232 offset:512\n" \
"v_mfma_f32_16x16x32_bf16 v[112:115], v[32:35], v[200:203], v[112:115]\n" \
"ds_read_b64_tr_b16 v[18:19], v232 offset:2560\n" \
"v_mfma_f32_16x16x32_bf16 v[112:115], v[36:39], v[204:207], v[112:115]\n" \
"global_load_lds_dwordx4 v227, s[48:49] offset:-1024\n" \
"v_mfma_f32_16x16x32_bf16 v[144:147], v[32:35], v[208:211], v[144:147]\n" \
"ds_read_b128 v[200:203], v230 offset:2048\n" \
"v_mfma_f32_16x16x32_bf16 v[144:147], v[36:39], v[212:215], v[144:147]\n" \
"ds_read_b128 v[204:207], v231 offset:2048\n" \
"v_mfma_f32_16x16x32_bf16 v[148:151], v[40:43], v[208:211], v[148:151]\n" \
"ds_read_b64_tr_b16 v[20:21], v232 offset:8704\n" \
"v_mfma_f32_16x16x32_bf16 v[148:151], v[44:47], v[212:215], v[148:151]\n" \
"global_load_lds_dwordx4 v224, s[46:47] offset:0\n" \
"v_mfma_f32_16x16x32_bf16 v[152:155], v[48:51], v[208:211], v[152:155]\n" \
"ds_read_b64_tr_b16 v[22:23], v232 offset:10752\n" \
"v_mfma_f32_16x16x32_bf16 v[152:155], v[52:55], v[212:215], v[152:155]\n" \
"ds_read_b64_tr_b16 v[24:25], v233 offset:512\n" \
"v_mfma_f32_16x16x32_bf16 v[156:159], v[56:59], v[208:211], v[156:159]\n" \
"ds_read_b64_tr_b16 v[26:27], v233 offset:2560\n" \
"v_mfma_f32_16x16x32_bf16 v[156:159], v[60:63], v[212:215], v[156:159]\n" \
"global_load_lds_dwordx4 v225, s[46:47] offset:1024\n" \
"v_mfma_f32_16x16x32_bf16 v[188:191], v[56:59], v[216:219], v[188:191]\n" \
"ds_read_b128 v[208:211], v230 offset:4096\n" \
"v_mfma_f32_16x16x32_bf16 v[188:191], v[60:63], v[220:223], v[188:191]\n" \
"ds_read_b128 v[212:215], v231 offset:4096\n" \
"v_mfma_f32_16x16x32_bf16 v[184:187], v[48:51], v[216:219], v[184:187]\n" \
"ds_read_b64_tr_b16 v[28:29], v233 offset:8704\n" \
"v_mfma_f32_16x16x32_bf16 v[184:187], v[52:55], v[220:223], v[184:187]\n" \
"global_load_lds_dwordx4 v226, s[46:47] offset:2048\n" \
"v_mfma_f32_16x16x32_bf16 v[180:183], v[40:43], v[216:219], v[180:183]\n" \
"ds_read_b64_tr_b16 v[30:31], v233 offset:10752\n" \
"v_mfma_f32_16x16x32_bf16 v[180:183], v[44:47], v[220:223], v[180:183]\n" \
"v_mfma_f32_16x16x32_bf16 v[176:179], v[32:35], v[216:219], v[176:179]\n" \
"v_mfma_f32_16x16x32_bf16 v[176:179], v[36:39], v[220:223], v[176:179]\n" \
"s_waitcnt lgkmcnt(1)\n" \
"global_load_lds_dwordx4 v227, s[46:47] offset:3072\n" \
"v_mfma_f32_16x16x32_bf16 v[64:67], v[0:3], v[192:195], v[64:67]\n" \
"ds_read_b128 v[216:219], v230 offset:6144\n" \
"v_mfma_f32_16x16x32_bf16 v[64:67], v[4:7], v[196:199], v[64:67]\n" \
"ds_read_b128 v[220:223], v231 offset:6144\n" \
"v_mfma_f32_16x16x32_bf16 v[68:71], v[8:11], v[192:195], v[68:71]\n" \
"ds_read_b64_tr_b16 v[32:33], v232 offset:1024\n" \
"v_mfma_f32_16x16x32_bf16 v[68:71], v[12:15], v[196:199], v[68:71]\n" \
"buffer_store_dwordx4 a[104:107], v249, s[56:59], s90 offen offset:2304 sc1\n" \
"v_mfma_f32_16x16x32_bf16 v[72:75], v[16:19], v[192:195], v[72:75]\n" \
"s_add_u32 s90, s89, 278528\n" \
"ds_read_b64_tr_b16 v[34:35], v232 offset:3072\n" \
"v_mfma_f32_16x16x32_bf16 v[72:75], v[20:23], v[196:199], v[72:75]\n" \
"s_waitcnt lgkmcnt(3)\n" \
"ds_read_b64_tr_b16 v[36:37], v232 offset:9216\n" \
"v_mfma_f32_16x16x32_bf16 v[76:79], v[24:27], v[192:195], v[76:79]\n" \
"ds_read_b128 a[104:107], v254\n" \
"ds_read_b64_tr_b16 v[38:39], v232 offset:11264\n" \
"v_mfma_f32_16x16x32_bf16 v[76:79], v[28:31], v[196:199], v[76:79]\n" \
"buffer_store_dwordx4 a[80:83], v248, s[56:59], s90 offen offset:0 sc1\n" \
"ds_read_b128 a[108:111], v254\n" \
"v_mfma_f32_16x16x32_bf16 v[108:111], v[24:27], v[200:203], v[108:111]\n" \
"ds_read_b64_tr_b16 v[40:41], v233 offset:1024\n" \
"v_mfma_f32_16x16x32_bf16 v[108:111], v[28:31], v[204:207], v[108:111]\n" \
"ds_read_b64_tr_b16 v[42:43], v233 offset:3072\n" \
"v_mfma_f32_16x16x32_bf16 v[104:107], v[16:19], v[200:203], v[104:107]\n" \
"ds_read_b128 a[80:83], v254\n" \
"ds_read_b64_tr_b16 v[44:45], v233 offset:9216\n" \
"v_mfma_f32_16x16x32_bf16 v[104:107], v[20:23], v[204:207], v[104:107]\n" \
"buffer_store_dwordx4 a[84:87], v248, s[56:59], s90 offen offset:1024 sc1\n" \
"ds_read_b128 a[112:115], v254\n" \
"v_mfma_f32_16x16x32_bf16 v[100:103], v[8:11], v[200:203], v[100:103]\n" \
"ds_read_b64_tr_b16 v[46:47], v233 offset:11264\n" \
"v_mfma_f32_16x16x32_bf16 v[100:103], v[12:15], v[204:207], v[100:103]\n" \
"ds_read_b64_tr_b16 v[48:49], v232 offset:1536\n" \
"v_mfma_f32_16x16x32_bf16 v[96:99], v[0:3], v[200:203], v[96:99]\n" \
"s_add_u32 m0, s95, 0\n" \
"ds_read_b128 a[84:87], v254\n" \
"ds_read_b64_tr_b16 v[50:51], v232 offset:3584\n" \
"v_mfma_f32_16x16x32_bf16 v[96:99], v[4:7], v[204:207], v[96:99]\n" \
"ds_read_b128 a[116:119], v254\n" \
"global_load_lds_dwordx4 v250, s[36:37] offset:0 sc1\n" \
"v_mfma_f32_16x16x32_bf16 v[128:131], v[0:3], v[208:211], v[128:131]\n" \
"ds_read_b64_tr_b16 v[52:53], v232 offset:9728\n" \
"v_mfma_f32_16x16x32_bf16 v[128:131], v[4:7], v[212:215], v[128:131]\n" \
"ds_read_b64_tr_b16 v[54:55], v232 offset:11776\n" \
"v_mfma_f32_16x16x32_bf16 v[132:135], v[8:11], v[208:211], v[132:135]\n" \
"ds_read_b64_tr_b16 v[56:57], v233 offset:1536\n" \
"v_mfma_f32_16x16x32_bf16 v[132:135], v[12:15], v[212:215], v[132:135]\n" \
"global_load_lds_dwordx4 v250, s[36:37] offset:1024 sc1\n" \
"v_mfma_f32_16x16x32_bf16 v[136:139], v[16:19], v[208:211], v[136:139]\n" \
"ds_read_b64_tr_b16 v[58:59], v233 offset:3584\n" \
"v_mfma_f32_16x16x32_bf16 v[136:139], v[20:23], v[212:215], v[136:139]\n" \
"ds_read_b64_tr_b16 v[60:61], v233 offset:9728\n" \
"v_mfma_f32_16x16x32_bf16 v[140:143], v[24:27], v[208:211], v[140:143]\n" \
"ds_read_b64_tr_b16 v[62:63], v233 offset:11776\n" \
"v_mfma_f32_16x16x32_bf16 v[140:143], v[28:31], v[212:215], v[140:143]\n" \
"s_waitcnt lgkmcnt(3)\n" \
"global_load_lds_dwordx4 v250, s[36:37] offset:2048 sc1\n" \
"v_mfma_f32_16x16x32_bf16 v[172:175], v[24:27], v[216:219], v[172:175]\n" \
"v_add_u32 v230, 0x8000, v230\n" \
"v_add_u32 v231, 0x8000, v231\n" \
"v_mfma_f32_16x16x32_bf16 v[172:175], v[28:31], v[220:223], v[172:175]\n" \
"v_add_u32 v232, 0x4000, v232\n" \
"v_add_u32 v233, 0x4000, v233\n" \
"v_mfma_f32_16x16x32_bf16 v[168:171], v[16:19], v[216:219], v[168:171]\n" \
"v_mfma_f32_16x16x32_bf16 v[168:171], v[20:23], v[220:223], v[168:171]\n" \
"global_load_lds_dwordx4 v250, s[36:37] offset:3072 sc1\n" \
"v_mfma_f32_16x16x32_bf16 v[164:167], v[8:11], v[216:219], v[164:167]\n" \
"s_add_u32 s36, s36, 17408\n" \
"v_mfma_f32_16x16x32_bf16 v[164:167], v[12:15], v[220:223], v[164:167]\n" \
"v_mfma_f32_16x16x32_bf16 v[160:163], v[0:3], v[216:219], v[160:163]\n" \
"s_waitcnt vmcnt(21) lgkmcnt(0)\n" \
"v_mfma_f32_16x16x32_bf16 v[160:163], v[4:7], v[220:223], v[160:163]\n" \
"s_barrier\n" \
"v_mfma_f32_16x16x32_bf16 v[80:83], v[32:35], v[192:195], v[80:83]\n" \
"ds_read_b64_tr_b16 v[0:1], v232 offset:0\n" \
"v_mfma_f32_16x16x32_bf16 v[80:83], v[36:39], v[196:199], v[80:83]\n" \
"ds_read_b64_tr_b16 v[2:3], v232 offset:2048\n" \
"v_mfma_f32_16x16x32_bf16 v[84:87], v[40:43], v[192:195], v[84:87]\n" \
"s_add_u32 m0, s95, 16384\n" \
"ds_read_b64_tr_b16 v[4:5], v232 offset:8192\n" \
"v_mfma_f32_16x16x32_bf16 v[84:87], v[44:47], v[196:199], v[84:87]\n" \
"global_load_lds_dwordx4 v250, s[36:37] offset:0 sc1\n" \
"v_mfma_f32_16x16x32_bf16 v[88:91], v[48:51], v[192:195], v[88:91]\n" \
"ds_read_b64_tr_b16 v[6:7], v232 offset:10240\n" \
"v_mfma_f32_16x16x32_bf16 v[88:91], v[52:55], v[196:199], v[88:91]\n" \
"ds_read_b64_tr_b16 v[8:9], v233 offset:0\n" \
"v_mfma_f32_16x16x32_bf16 v[92:95], v[56:59], v[192:195], v[92:95]\n" \
"ds_read_b64_tr_b16 v[10:11], v233 offset:2048\n" \
"v_mfma_f32_16x16x32_bf16 v[92:95], v[60:63], v[196:199], v[92:95]\n" \
"global_load_lds_dwordx4 v250, s[36:37] offset:1024 sc1\n" \
"v_mfma_f32_16x16x32_bf16 v[124:127], v[56:59], v[200:203], v[124:127]\n" \
"ds_read_b128 v[192:195], v230 offset:0\n" \
"v_mfma_f32_16x16x32_bf16 v[124:127], v[60:63], v[204:207], v[124:127]\n" \
"ds_read_b128 v[196:199], v231 offset:0\n" \
"v_mfma_f32_16x16x32_bf16 v[120:123], v[48:51], v[200:203], v[120:123]\n" \
"ds_read_b64_tr_b16 v[12:13], v233 offset:8192\n" \
"v_mfma_f32_16x16x32_bf16 v[120:123], v[52:55], v[204:207], v[120:123]\n" \
"global_load_lds_dwordx4 v250, s[36:37] offset:2048 sc1\n" \
"v_mfma_f32_16x16x32_bf16 v[116:119], v[40:43], v[200:203], v[116:119]\n" \
"ds_read_b64_tr_b16 v[14:15], v233 offset:10240\n" \
"v_mfma_f32_16x16x32_bf16 v[116:119], v[44:47], v[204:207], v[116:119]\n" \
"ds_read_b64_tr_b16 v[16:17], v232 offset:512\n" \
"v_mfma_f32_16x16x32_bf16 v[112:115], v[32:35], v[200:203], v[112:115]\n" \
"ds_read_b64_tr_b16 v[18:19], v232 offset:2560\n" \
"v_mfma_f32_16x16x32_bf16 v[112:115], v[36:39], v[204:207], v[112:115]\n" \
"global_load_lds_dwordx4 v250, s[36:37] offset:3072 sc1\n" \
"v_mfma_f32_16x16x32_bf16 v[144:147], v[32:35], v[208:211], v[144:147]\n" \
"ds_read_b128 v[200:203], v230 offset:2048\n" \
"s_add_u32 s36, s36, 17408\n" \
"v_mfma_f32_16x16x32_bf16 v[144:147], v[36:39], v[212:215], v[144:147]\n" \
"ds_read_b128 v[204:207], v231 offset:2048\n" \
"v_mfma_f32_16x16x32_bf16 v[148:151], v[40:43], v[208:211], v[148:151]\n" \
"ds_read_b64_tr_b16 v[20:21], v232 offset:8704\n" \
"v_mfma_f32_16x16x32_bf16 v[148:151], v[44:47], v[212:215], v[148:151]\n" \
"buffer_store_dwordx4 a[88:91], v248, s[56:59], s90 offen offset:2048 sc1\n" \
"v_mfma_f32_16x16x32_bf16 v[152:155], v[48:51], v[208:211], v[152:155]\n" \
"ds_read_b64_tr_b16 v[22:23], v232 offset:10752\n" \
"v_mfma_f32_16x16x32_bf16 v[152:155], v[52:55], v[212:215], v[152:155]\n" \
"ds_read_b64_tr_b16 v[24:25], v233 offset:512\n" \
"v_mfma_f32_16x16x32_bf16 v[156:159], v[56:59], v[208:211], v[156:159]\n" \
"ds_read_b128 a[88:91], v254\n" \
"ds_read_b64_tr_b16 v[26:27], v233 offset:2560\n" \
"v_mfma_f32_16x16x32_bf16 v[156:159], v[60:63], v[212:215], v[156:159]\n" \
"buffer_store_dwordx4 a[92:95], v248, s[56:59], s90 offen offset:3072 sc1\n" \
"ds_read_b128 a[120:123], v254\n" \
"v_mfma_f32_16x16x32_bf16 v[188:191], v[56:59], v[216:219], v[188:191]\n" \
"ds_read_b128 v[208:211], v230 offset:4096\n" \
"s_add_u32 s90, s88, 417792\n" \
"v_mfma_f32_16x16x32_bf16 v[188:191], v[60:63], v[220:223], v[188:191]\n" \
"ds_read_b128 v[212:215], v231 offset:4096\n" \
"v_mfma_f32_16x16x32_bf16 v[184:187], v[48:51], v[216:219], v[184:187]\n" \
"ds_read_b128 a[92:95], v254\n" \
"ds_read_b64_tr_b16 v[28:29], v233 offset:8704\n" \
"v_mfma_f32_16x16x32_bf16 v[184:187], v[52:55], v[220:223], v[184:187]\n" \
"buffer_store_dwordx4 a[0:3], v249, s[56:59], s90 offen offset:0 sc1\n" \
"ds_read_b128 a[124:127], v254\n" \
"v_mfma_f32_16x16x32_bf16 v[180:183], v[40:43], v[216:219], v[180:183]\n" \
"ds_read_b64_tr_b16 v[30:31], v233 offset:10752\n" \
"v_mfma_f32_16x16x32_bf16 v[180:183], v[44:47], v[220:223], v[180:183]\n" \
"v_mfma_f32_16x16x32_bf16 v[176:179], v[32:35], v[216:219], v[176:179]\n" \
"ds_read_b128 a[0:3], v254\n" \
"v_mfma_f32_16x16x32_bf16 v[176:179], v[36:39], v[220:223], v[176:179]\n" \
"s_waitcnt lgkmcnt(3)\n" \
"ds_read_b128 a[4:7], v254\n" \
"v_mfma_f32_16x16x32_bf16 v[64:67], v[0:3], v[192:195], v[64:67]\n" \
"ds_read_b128 v[216:219], v230 offset:6144\n" \
"v_mfma_f32_16x16x32_bf16 v[64:67], v[4:7], v[196:199], v[64:67]\n" \
"ds_read_b128 v[220:223], v231 offset:6144\n" \
"v_mfma_f32_16x16x32_bf16 v[68:71], v[8:11], v[192:195], v[68:71]\n" \
"ds_read_b64_tr_b16 v[32:33], v232 offset:1024\n" \
"v_mfma_f32_16x16x32_bf16 v[68:71], v[12:15], v[196:199], v[68:71]\n" \
"v_mfma_f32_16x16x32_bf16 v[72:75], v[16:19], v[192:195], v[72:75]\n" \
"ds_read_b64_tr_b16 v[34:35], v232 offset:3072\n" \
"v_mfma_f32_16x16x32_bf16 v[72:75], v[20:23], v[196:199], v[72:75]\n" \
"s_waitcnt lgkmcnt(3)\n" \
"ds_read_b64_tr_b16 v[36:37], v232 offset:9216\n" \
"v_mfma_f32_16x16x32_bf16 v[76:79], v[24:27], v[192:195], v[76:79]\n" \
"ds_read_b64_tr_b16 v[38:39], v232 offset:11264\n" \
"v_mfma_f32_16x16x32_bf16 v[76:79], v[28:31], v[196:199], v[76:79]\n" \
"v_mfma_f32_16x16x32_bf16 v[108:111], v[24:27], v[200:203], v[108:111]\n" \
"ds_read_b64_tr_b16 v[40:41], v233 offset:1024\n" \
"v_mfma_f32_16x16x32_bf16 v[108:111], v[28:31], v[204:207], v[108:111]\n" \
"ds_read_b64_tr_b16 v[42:43], v233 offset:3072\n" \
"v_mfma_f32_16x16x32_bf16 v[104:107], v[16:19], v[200:203], v[104:107]\n" \
"ds_read_b64_tr_b16 v[44:45], v233 offset:9216\n" \
"v_mfma_f32_16x16x32_bf16 v[104:107], v[20:23], v[204:207], v[104:107]\n" \
"v_mfma_f32_16x16x32_bf16 v[100:103], v[8:11], v[200:203], v[100:103]\n" \
"ds_read_b64_tr_b16 v[46:47], v233 offset:11264\n" \
"v_mfma_f32_16x16x32_bf16 v[100:103], v[12:15], v[204:207], v[100:103]\n" \
"ds_read_b64_tr_b16 v[48:49], v232 offset:1536\n" \
"v_mfma_f32_16x16x32_bf16 v[96:99], v[0:3], v[200:203], v[96:99]\n" \
"ds_read_b64_tr_b16 v[50:51], v232 offset:3584\n" \
"v_mfma_f32_16x16x32_bf16 v[96:99], v[4:7], v[204:207], v[96:99]\n" \
"v_mfma_f32_16x16x32_bf16 v[128:131], v[0:3], v[208:211], v[128:131]\n" \
"ds_read_b64_tr_b16 v[52:53], v232 offset:9728\n" \
"v_mfma_f32_16x16x32_bf16 v[128:131], v[4:7], v[212:215], v[128:131]\n" \
"ds_read_b64_tr_b16 v[54:55], v232 offset:11776\n" \
"v_mfma_f32_16x16x32_bf16 v[132:135], v[8:11], v[208:211], v[132:135]\n" \
"ds_read_b64_tr_b16 v[56:57], v233 offset:1536\n" \
"v_mfma_f32_16x16x32_bf16 v[132:135], v[12:15], v[212:215], v[132:135]\n" \
"v_mfma_f32_16x16x32_bf16 v[136:139], v[16:19], v[208:211], v[136:139]\n" \
"ds_read_b64_tr_b16 v[58:59], v233 offset:3584\n" \
"v_mfma_f32_16x16x32_bf16 v[136:139], v[20:23], v[212:215], v[136:139]\n" \
"ds_read_b64_tr_b16 v[60:61], v233 offset:9728\n" \
"v_mfma_f32_16x16x32_bf16 v[140:143], v[24:27], v[208:211], v[140:143]\n" \
"ds_read_b64_tr_b16 v[62:63], v233 offset:11776\n" \
"v_mfma_f32_16x16x32_bf16 v[140:143], v[28:31], v[212:215], v[140:143]\n" \
"s_waitcnt lgkmcnt(3)\n" \
"v_mfma_f32_16x16x32_bf16 v[172:175], v[24:27], v[216:219], v[172:175]\n" \
"v_add_u32 v230, 0x8000, v230\n" \
"v_add_u32 v231, 0x8000, v231\n" \
"v_mfma_f32_16x16x32_bf16 v[172:175], v[28:31], v[220:223], v[172:175]\n" \
"v_add_u32 v232, 0x4000, v232\n" \
"v_add_u32 v233, 0x4000, v233\n" \
"v_mfma_f32_16x16x32_bf16 v[168:171], v[16:19], v[216:219], v[168:171]\n" \
"v_mfma_f32_16x16x32_bf16 v[168:171], v[20:23], v[220:223], v[168:171]\n" \
"v_mfma_f32_16x16x32_bf16 v[164:167], v[8:11], v[216:219], v[164:167]\n" \
"v_mfma_f32_16x16x32_bf16 v[164:167], v[12:15], v[220:223], v[164:167]\n" \
"v_mfma_f32_16x16x32_bf16 v[160:163], v[0:3], v[216:219], v[160:163]\n" \
"s_waitcnt vmcnt(14) lgkmcnt(0)\n" \
"v_mfma_f32_16x16x32_bf16 v[160:163], v[4:7], v[220:223], v[160:163]\n" \
"s_barrier\n" \
"v_mfma_f32_16x16x32_bf16 v[80:83], v[32:35], v[192:195], v[80:83]\n" \
"ds_read_b64_tr_b16 v[0:1], v232 offset:0\n" \
"v_mfma_f32_16x16x32_bf16 v[80:83], v[36:39], v[196:199], v[80:83]\n" \
"ds_read_b64_tr_b16 v[2:3], v232 offset:2048\n" \
"v_mfma_f32_16x16x32_bf16 v[84:87], v[40:43], v[192:195], v[84:87]\n" \
"s_add_u32 m0, s95, 32768\n" \
"ds_read_b64_tr_b16 v[4:5], v232 offset:8192\n" \
"v_mfma_f32_16x16x32_bf16 v[84:87], v[44:47], v[196:199], v[84:87]\n" \
"global_load_lds_dwordx4 v250, s[36:37] offset:0 sc1\n" \
"v_mfma_f32_16x16x32_bf16 v[88:91], v[48:51], v[192:195], v[88:91]\n" \
"ds_read_b64_tr_b16 v[6:7], v232 offset:10240\n" \
"v_mfma_f32_16x16x32_bf16 v[88:91], v[52:55], v[196:199], v[88:91]\n" \
"ds_read_b64_tr_b16 v[8:9], v233 offset:0\n" \
"v_mfma_f32_16x16x32_bf16 v[92:95], v[56:59], v[192:195], v[92:95]\n" \
"ds_read_b64_tr_b16 v[10:11], v233 offset:2048\n" \
"v_mfma_f32_16x16x32_bf16 v[92:95], v[60:63], v[196:199], v[92:95]\n" \
"global_load_lds_dwordx4 v250, s[36:37] offset:1024 sc1\n" \
"v_mfma_f32_16x16x32_bf16 v[124:127], v[56:59], v[200:203], v[124:127]\n" \
"ds_read_b128 v[192:195], v230 offset:0\n" \
"v_mfma_f32_16x16x32_bf16 v[124:127], v[60:63], v[204:207], v[124:127]\n" \
"ds_read_b128 v[196:199], v231 offset:0\n" \
"v_mfma_f32_16x16x32_bf16 v[120:123], v[48:51], v[200:203], v[120:123]\n" \
"ds_read_b64_tr_b16 v[12:13], v233 offset:8192\n" \
"v_mfma_f32_16x16x32_bf16 v[120:123], v[52:55], v[204:207], v[120:123]\n" \
"global_load_lds_dwordx4 v250, s[36:37] offset:2048 sc1\n" \
"v_mfma_f32_16x16x32_bf16 v[116:119], v[40:43], v[200:203], v[116:119]\n" \
"ds_read_b64_tr_b16 v[14:15], v233 offset:10240\n" \
"v_mfma_f32_16x16x32_bf16 v[116:119], v[44:47], v[204:207], v[116:119]\n" \
"ds_read_b64_tr_b16 v[16:17], v232 offset:512\n" \
"v_mfma_f32_16x16x32_bf16 v[112:115], v[32:35], v[200:203], v[112:115]\n" \
"ds_read_b64_tr_b16 v[18:19], v232 offset:2560\n" \
"v_mfma_f32_16x16x32_bf16 v[112:115], v[36:39], v[204:207], v[112:115]\n" \
"global_load_lds_dwordx4 v250, s[36:37] offset:3072 sc1\n" \
"v_mfma_f32_16x16x32_bf16 v[144:147], v[32:35], v[208:211], v[144:147]\n" \
"ds_read_b128 v[200:203], v230 offset:2048\n" \
"s_add_u32 s36, s36, 17408\n" \
"v_mfma_f32_16x16x32_bf16 v[144:147], v[36:39], v[212:215], v[144:147]\n" \
"ds_read_b128 v[204:207], v231 offset:2048\n" \
"v_mfma_f32_16x16x32_bf16 v[148:151], v[40:43], v[208:211], v[148:151]\n" \
"ds_read_b64_tr_b16 v[20:21], v232 offset:8704\n" \
"v_mfma_f32_16x16x32_bf16 v[148:151], v[44:47], v[212:215], v[148:151]\n" \
"buffer_store_dwordx4 a[32:35], v249, s[56:59], s90 offen offset:256 sc1\n" \
"v_mfma_f32_16x16x32_bf16 v[152:155], v[48:51], v[208:211], v[152:155]\n" \
"ds_read_b64_tr_b16 v[22:23], v232 offset:10752\n" \
"v_mfma_f32_16x16x32_bf16 v[152:155], v[52:55], v[212:215], v[152:155]\n" \
"ds_read_b64_tr_b16 v[24:25], v233 offset:512\n" \
"v_mfma_f32_16x16x32_bf16 v[156:159], v[56:59], v[208:211], v[156:159]\n" \
"ds_read_b128 a[32:35], v254\n" \
"ds_read_b64_tr_b16 v[26:27], v233 offset:2560\n" \
"v_mfma_f32_16x16x32_bf16 v[156:159], v[60:63], v[212:215], v[156:159]\n" \
"buffer_store_dwordx4 a[8:11], v249, s[56:59], s90 offen offset:2048 sc1\n" \
"ds_read_b128 a[36:39], v254\n" \
"v_mfma_f32_16x16x32_bf16 v[188:191], v[56:59], v[216:219], v[188:191]\n" \
"ds_read_b128 v[208:211], v230 offset:4096\n" \
"v_mfma_f32_16x16x32_bf16 v[188:191], v[60:63], v[220:223], v[188:191]\n" \
"ds_read_b128 v[212:215], v231 offset:4096\n" \
"v_mfma_f32_16x16x32_bf16 v[184:187], v[48:51], v[216:219], v[184:187]\n" \
"ds_read_b128 a[8:11], v254\n" \
"ds_read_b64_tr_b16 v[28:29], v233 offset:8704\n" \
"v_mfma_f32_16x16x32_bf16 v[184:187], v[52:55], v[220:223], v[184:187]\n" \
"buffer_store_dwordx4 a[40:43], v249, s[56:59], s90 offen offset:2304 sc1\n" \
"ds_read_b128 a[12:15], v254\n" \
"v_mfma_f32_16x16x32_bf16 v[180:183], v[40:43], v[216:219], v[180:183]\n" \
"ds_read_b64_tr_b16 v[30:31], v233 offset:10752\n" \
"v_mfma_f32_16x16x32_bf16 v[180:183], v[44:47], v[220:223], v[180:183]\n" \
"v_mfma_f32_16x16x32_bf16 v[176:179], v[32:35], v[216:219], v[176:179]\n" \
"ds_read_b128 a[40:43], v254\n" \
"v_mfma_f32_16x16x32_bf16 v[176:179], v[36:39], v[220:223], v[176:179]\n" \
"s_waitcnt lgkmcnt(3)\n" \
"ds_read_b128 a[44:47], v254\n" \
"v_mfma_f32_16x16x32_bf16 v[64:67], v[0:3], v[192:195], v[64:67]\n" \
"ds_read_b128 v[216:219], v230 offset:6144\n" \
"v_mfma_f32_16x16x32_bf16 v[64:67], v[4:7], v[196:199], v[64:67]\n" \
"ds_read_b128 v[220:223], v231 offset:6144\n" \
"v_mfma_f32_16x16x32_bf16 v[68:71], v[8:11], v[192:195], v[68:71]\n" \
"ds_read_b64_tr_b16 v[32:33], v232 offset:1024\n" \
"v_mfma_f32_16x16x32_bf16 v[68:71], v[12:15], v[196:199], v[68:71]\n" \
"v_mfma_f32_16x16x32_bf16 v[72:75], v[16:19], v[192:195], v[72:75]\n" \
"ds_read_b64_tr_b16 v[34:35], v232 offset:3072\n" \
"v_mfma_f32_16x16x32_bf16 v[72:75], v[20:23], v[196:199], v[72:75]\n" \
"s_waitcnt lgkmcnt(3)\n" \
"ds_read_b64_tr_b16 v[36:37], v232 offset:9216\n" \
"v_mfma_f32_16x16x32_bf16 v[76:79], v[24:27], v[192:195], v[76:79]\n" \
"ds_read_b64_tr_b16 v[38:39], v232 offset:11264\n" \
"v_cvt_pk_bf16_f32 v64, v64, v65\n" \
"v_mfma_f32_16x16x32_bf16 v[76:79], v[28:31], v[196:199], v[76:79]\n" \
"v_cvt_pk_bf16_f32 v65, v66, v67\n" \
"v_cvt_pk_bf16_f32 v66, v68, v69\n" \
"v_cvt_pk_bf16_f32 v67, v70, v71\n" \
"v_accvgpr_write_b32 a128, v64\n" \
"v_accvgpr_write_b32 a129, v65\n" \
"v_mfma_f32_16x16x32_bf16 v[96:99], v[0:3], v[200:203], v[96:99]\n" \
"ds_read_b64_tr_b16 v[40:41], v233 offset:1024\n" \
"v_accvgpr_write_b32 a130, v66\n" \
"v_mfma_f32_16x16x32_bf16 v[96:99], v[4:7], v[204:207], v[96:99]\n" \
"ds_read_b64_tr_b16 v[42:43], v233 offset:3072\n" \
"v_accvgpr_write_b32 a131, v67\n" \
"v_mfma_f32_16x16x32_bf16 v[100:103], v[8:11], v[200:203], v[100:103]\n" \
"ds_read_b64_tr_b16 v[44:45], v233 offset:9216\n" \
"v_cvt_pk_bf16_f32 v72, v72, v73\n" \
"v_mfma_f32_16x16x32_bf16 v[100:103], v[12:15], v[204:207], v[100:103]\n" \
"v_cvt_pk_bf16_f32 v73, v74, v75\n" \
"v_cvt_pk_bf16_f32 v74, v76, v77\n" \
"v_cvt_pk_bf16_f32 v75, v78, v79\n" \
"v_accvgpr_write_b32 a132, v72\n" \
"v_accvgpr_write_b32 a133, v73\n" \
"v_mfma_f32_16x16x32_bf16 v[104:107], v[16:19], v[200:203], v[104:107]\n" \
"ds_read_b64_tr_b16 v[46:47], v233 offset:11264\n" \
"v_accvgpr_write_b32 a134, v74\n" \
"v_mfma_f32_16x16x32_bf16 v[104:107], v[20:23], v[204:207], v[104:107]\n" \
"ds_read_b64_tr_b16 v[48:49], v232 offset:1536\n" \
"v_accvgpr_write_b32 a135, v75\n" \
"v_mfma_f32_16x16x32_bf16 v[108:111], v[24:27], v[200:203], v[108:111]\n" \
"ds_read_b64_tr_b16 v[50:51], v232 offset:3584\n" \
"v_cvt_pk_bf16_f32 v96, v96, v97\n" \
"v_mfma_f32_16x16x32_bf16 v[108:111], v[28:31], v[204:207], v[108:111]\n" \
"v_cvt_pk_bf16_f32 v97, v98, v99\n" \
"v_cvt_pk_bf16_f32 v98, v100, v101\n" \
"v_cvt_pk_bf16_f32 v99, v102, v103\n" \
"v_accvgpr_write_b32 a144, v96\n" \
"v_accvgpr_write_b32 a145, v97\n" \
"v_mfma_f32_16x16x32_bf16 v[128:131], v[0:3], v[208:211], v[128:131]\n" \
"ds_read_b64_tr_b16 v[52:53], v232 offset:9728\n" \
"v_accvgpr_write_b32 a146, v98\n" \
"v_mfma_f32_16x16x32_bf16 v[128:131], v[4:7], v[212:215], v[128:131]\n" \
"ds_read_b64_tr_b16 v[54:55], v232 offset:11776\n" \
"v_accvgpr_write_b32 a147, v99\n" \
"v_mfma_f32_16x16x32_bf16 v[132:135], v[8:11], v[208:211], v[132:135]\n" \
"ds_read_b64_tr_b16 v[56:57], v233 offset:1536\n" \
"v_cvt_pk_bf16_f32 v104, v104, v105\n" \
"v_mfma_f32_16x16x32_bf16 v[132:135], v[12:15], v[212:215], v[132:135]\n" \
"v_cvt_pk_bf16_f32 v105, v106, v107\n" \
"v_cvt_pk_bf16_f32 v106, v108, v109\n" \
"v_cvt_pk_bf16_f32 v107, v110, v111\n" \
"v_accvgpr_write_b32 a148, v104\n" \
"v_accvgpr_write_b32 a149, v105\n" \
"v_mfma_f32_16x16x32_bf16 v[136:139], v[16:19], v[208:211], v[136:139]\n" \
"ds_read_b64_tr_b16 v[58:59], v233 offset:3584\n" \
"v_accvgpr_write_b32 a150, v106\n" \
"v_mfma_f32_16x16x32_bf16 v[136:139], v[20:23], v[212:215], v[136:139]\n" \
"ds_read_b64_tr_b16 v[60:61], v233 offset:9728\n" \
"v_accvgpr_write_b32 a151, v107\n" \
"v_mfma_f32_16x16x32_bf16 v[140:143], v[24:27], v[208:211], v[140:143]\n" \
"ds_read_b64_tr_b16 v[62:63], v233 offset:11776\n" \
"v_cvt_pk_bf16_f32 v128, v128, v129\n" \
"v_mfma_f32_16x16x32_bf16 v[140:143], v[28:31], v[212:215], v[140:143]\n" \
"s_waitcnt lgkmcnt(3)\n" \
"v_cvt_pk_bf16_f32 v129, v130, v131\n" \
"v_cvt_pk_bf16_f32 v130, v132, v133\n" \
"v_cvt_pk_bf16_f32 v131, v134, v135\n" \
"v_mfma_f32_16x16x32_bf16 v[160:163], v[0:3], v[216:219], v[160:163]\n" \
"v_accvgpr_write_b32 a160, v128\n" \
"v_accvgpr_write_b32 a161, v129\n" \
"v_mfma_f32_16x16x32_bf16 v[160:163], v[4:7], v[220:223], v[160:163]\n" \
"Lpg4933_%=:\n" \
"s_cbranch_execz Lpg6193_%=\n" \
"v_accvgpr_write_b32 a162, v130\n" \
"v_accvgpr_write_b32 a163, v131\n" \
"v_mfma_f32_16x16x32_bf16 v[164:167], v[8:11], v[216:219], v[164:167]\n" \
"v_cvt_pk_bf16_f32 v136, v136, v137\n" \
"v_cvt_pk_bf16_f32 v137, v138, v139\n" \
"v_mfma_f32_16x16x32_bf16 v[164:167], v[12:15], v[220:223], v[164:167]\n" \
"v_cvt_pk_bf16_f32 v138, v140, v141\n" \
"v_cvt_pk_bf16_f32 v139, v142, v143\n" \
"v_mfma_f32_16x16x32_bf16 v[168:171], v[16:19], v[216:219], v[168:171]\n" \
"v_accvgpr_write_b32 a164, v136\n" \
"v_accvgpr_write_b32 a165, v137\n" \
"v_mfma_f32_16x16x32_bf16 v[168:171], v[20:23], v[220:223], v[168:171]\n" \
"v_accvgpr_write_b32 a166, v138\n" \
"v_accvgpr_write_b32 a167, v139\n" \
"v_mfma_f32_16x16x32_bf16 v[172:175], v[24:27], v[216:219], v[172:175]\n" \
"s_waitcnt vmcnt(10) lgkmcnt(0)\n" \
"v_cvt_pk_bf16_f32 v160, v160, v161\n" \
"v_cvt_pk_bf16_f32 v161, v162, v163\n" \
"v_mfma_f32_16x16x32_bf16 v[172:175], v[28:31], v[220:223], v[172:175]\n" \
"s_barrier\n" \
"v_cvt_pk_bf16_f32 v162, v164, v165\n" \
"v_cvt_pk_bf16_f32 v163, v166, v167\n" \
"v_accvgpr_write_b32 a176, v160\n" \
"v_accvgpr_write_b32 a177, v161\n" \
"v_mfma_f32_16x16x32_bf16 v[80:83], v[32:35], v[192:195], v[80:83]\n" \
"s_mov_b32 m0, s74\n" \
"v_accvgpr_write_b32 a178, v162\n" \
"v_accvgpr_write_b32 a179, v163\n" \
"v_mfma_f32_16x16x32_bf16 v[80:83], v[36:39], v[196:199], v[80:83]\n" \
"ds_read_b128 a[192:195], v249 offset:0\n" \
"global_load_lds_dwordx4 v250, s[38:39] offset:0 sc1\n" \
"v_mfma_f32_16x16x32_bf16 v[84:87], v[40:43], v[192:195], v[84:87]\n" \
"ds_read_b128 v[0:3], v254\n" \
"v_cvt_pk_bf16_f32 v168, v168, v169\n" \
"v_mfma_f32_16x16x32_bf16 v[84:87], v[44:47], v[196:199], v[84:87]\n" \
"ds_read_b128 a[196:199], v249 offset:2048\n" \
"global_load_lds_dwordx4 v250, s[38:39] offset:1024 sc1\n" \
"v_cvt_pk_bf16_f32 v169, v170, v171\n" \
"v_cvt_pk_bf16_f32 v170, v172, v173\n" \
"v_cvt_pk_bf16_f32 v171, v174, v175\n" \
"v_accvgpr_write_b32 a180, v168\n" \
"v_accvgpr_write_b32 a181, v169\n" \
"v_accvgpr_write_b32 a182, v170\n" \
"v_mfma_f32_16x16x32_bf16 v[88:91], v[48:51], v[192:195], v[88:91]\n" \
"ds_read_b128 v[4:7], v254\n" \
"v_accvgpr_write_b32 a183, v171\n" \
"v_mfma_f32_16x16x32_bf16 v[88:91], v[52:55], v[196:199], v[88:91]\n" \
"ds_read_b128 a[200:203], v249 offset:4096\n" \
"global_load_lds_dwordx4 v250, s[38:39] offset:2048 sc1\n" \
"v_mfma_f32_16x16x32_bf16 v[92:95], v[56:59], v[192:195], v[92:95]\n" \
"v_cvt_pk_bf16_f32 v80, v80, v81\n" \
"v_cvt_pk_bf16_f32 v81, v82, v83\n" \
"v_mfma_f32_16x16x32_bf16 v[92:95], v[60:63], v[196:199], v[92:95]\n" \
"ds_read_b128 a[204:207], v249 offset:6144\n" \
"global_load_lds_dwordx4 v250, s[38:39] offset:3072 sc1\n" \
"v_cvt_pk_bf16_f32 v82, v84, v85\n" \
"v_cvt_pk_bf16_f32 v83, v86, v87\n" \
"v_accvgpr_write_b32 a136, v80\n" \
"v_accvgpr_write_b32 a137, v81\n" \
"v_accvgpr_write_b32 a138, v82\n" \
"v_mfma_f32_16x16x32_bf16 v[112:115], v[32:35], v[200:203], v[112:115]\n" \
"s_add_u32 s38, s38, 17408\n" \
"v_accvgpr_write_b32 a139, v83\n" \
"s_add_u32 m0, m0, 0x4000\n" \
"v_mfma_f32_16x16x32_bf16 v[112:115], v[36:39], v[204:207], v[112:115]\n" \
"ds_read_b128 a[208:211], v249 offset:256\n" \
"global_load_lds_dwordx4 v250, s[38:39] offset:0 sc1\n" \
"v_mfma_f32_16x16x32_bf16 v[116:119], v[40:43], v[200:203], v[116:119]\n" \
"v_cvt_pk_bf16_f32 v88, v88, v89\n" \
"v_cvt_pk_bf16_f32 v89, v90, v91\n" \
"v_mfma_f32_16x16x32_bf16 v[116:119], v[44:47], v[204:207], v[116:119]\n" \
"ds_read_b128 a[212:215], v249 offset:2304\n" \
"global_load_lds_dwordx4 v250, s[38:39] offset:1024 sc1\n" \
"v_cvt_pk_bf16_f32 v90, v92, v93\n" \
"v_cvt_pk_bf16_f32 v91, v94, v95\n" \
"v_accvgpr_write_b32 a140, v88\n" \
"v_accvgpr_write_b32 a141, v89\n" \
"v_mfma_f32_16x16x32_bf16 v[120:123], v[48:51], v[200:203], v[120:123]\n" \
"v_accvgpr_write_b32 a142, v90\n" \
"v_accvgpr_write_b32 a143, v91\n" \
"v_mfma_f32_16x16x32_bf16 v[120:123], v[52:55], v[204:207], v[120:123]\n" \
"ds_read_b128 a[216:219], v249 offset:4352\n" \
"global_load_lds_dwordx4 v250, s[38:39] offset:2048 sc1\n" \
"v_mfma_f32_16x16x32_bf16 v[124:127], v[56:59], v[200:203], v[124:127]\n" \
"v_cvt_pk_bf16_f32 v112, v112, v113\n" \
"v_cvt_pk_bf16_f32 v113, v114, v115\n" \
"v_mfma_f32_16x16x32_bf16 v[124:127], v[60:63], v[204:207], v[124:127]\n" \
"ds_read_b128 a[220:223], v249 offset:6400\n" \
"global_load_lds_dwordx4 v250, s[38:39] offset:3072 sc1\n" \
"v_cvt_pk_bf16_f32 v114, v116, v117\n" \
"v_cvt_pk_bf16_f32 v115, v118, v119\n" \
"v_accvgpr_write_b32 a152, v112\n" \
"v_mfma_f32_16x16x32_bf16 v[144:147], v[32:35], v[208:211], v[144:147]\n" \
"s_add_u32 s38, s38, 17408\n" \
"s_xor_b32 s74, s74, 0x8000\n" \
"s_add_u32 s90, s89, 417792\n" \
"v_mfma_f32_16x16x32_bf16 v[144:147], v[36:39], v[212:215], v[144:147]\n" \
"ds_read_b128 a[224:227], v249 offset:8192\n" \
"v_accvgpr_write_b32 a153, v113\n" \
"v_mfma_f32_16x16x32_bf16 v[148:151], v[40:43], v[208:211], v[148:151]\n" \
"v_accvgpr_write_b32 a154, v114\n" \
"v_accvgpr_write_b32 a155, v115\n" \
"v_mfma_f32_16x16x32_bf16 v[148:151], v[44:47], v[212:215], v[148:151]\n" \
"buffer_store_dwordx4 a[16:19], v248, s[56:59], s90 offen offset:0 sc1\n" \
"ds_read_b128 a[228:231], v249 offset:10240\n" \
"v_cvt_pk_bf16_f32 v120, v120, v121\n" \
"v_cvt_pk_bf16_f32 v121, v122, v123\n" \
"v_cvt_pk_bf16_f32 v122, v124, v125\n" \
"v_mfma_f32_16x16x32_bf16 v[152:155], v[48:51], v[208:211], v[152:155]\n" \
"v_cvt_pk_bf16_f32 v123, v126, v127\n" \
"v_accvgpr_write_b32 a156, v120\n" \
"v_mfma_f32_16x16x32_bf16 v[152:155], v[52:55], v[212:215], v[152:155]\n" \
"ds_read_b128 a[232:235], v249 offset:12288\n" \
"v_accvgpr_write_b32 a157, v121\n" \
"v_mfma_f32_16x16x32_bf16 v[156:159], v[56:59], v[208:211], v[156:159]\n" \
"ds_read_b128 a[16:19], v254\n" \
"v_accvgpr_write_b32 a158, v122\n" \
"v_mfma_f32_16x16x32_bf16 v[156:159], v[60:63], v[212:215], v[156:159]\n" \
"ds_read_b128 a[236:239], v249 offset:14336\n" \
"v_accvgpr_write_b32 a159, v123\n" \
"v_mfma_f32_16x16x32_bf16 v[176:179], v[32:35], v[216:219], v[176:179]\n" \
"ds_read_b128 a[240:243], v249 offset:8448\n" \
"v_cvt_pk_bf16_f32 v144, v144, v145\n" \
"v_mfma_f32_16x16x32_bf16 v[176:179], v[36:39], v[220:223], v[176:179]\n" \
"ds_read_b128 a[244:247], v249 offset:10496\n" \
"v_cvt_pk_bf16_f32 v145, v146, v147\n" \
"v_mfma_f32_16x16x32_bf16 v[180:183], v[40:43], v[216:219], v[180:183]\n" \
"ds_read_b128 a[248:251], v249 offset:12544\n" \
"v_cvt_pk_bf16_f32 v146, v148, v149\n" \
"v_mfma_f32_16x16x32_bf16 v[180:183], v[44:47], v[220:223], v[180:183]\n" \
"ds_read_b128 a[252:255], v249 offset:14592\n" \
"v_cvt_pk_bf16_f32 v147, v150, v151\n" \
"v_mfma_f32_16x16x32_bf16 v[184:187], v[48:51], v[216:219], v[184:187]\n" \
"ds_read_b128 a[48:51], v254\n" \
"v_accvgpr_write_b32 a168, v144\n" \
"v_mfma_f32_16x16x32_bf16 v[184:187], v[52:55], v[220:223], v[184:187]\n" \
"ds_read_b128 v[32:35], v254\n" \
"v_accvgpr_write_b32 a169, v145\n" \
"v_mfma_f32_16x16x32_bf16 v[188:191], v[56:59], v[216:219], v[188:191]\n" \
"ds_read_b128 v[36:39], v254\n" \
"v_accvgpr_write_b32 a170, v146\n" \
"v_mfma_f32_16x16x32_bf16 v[188:191], v[60:63], v[220:223], v[188:191]\n" \
"s_waitcnt lgkmcnt(4)\n" \
"buffer_store_dwordx4 a[20:23], v248, s[56:59], s90 offen offset:1024 sc1\n" \
"v_accvgpr_write_b32 a171, v147\n" \
"s_cmp_eq_u32 s63, 0\n" \
"s_cbranch_scc1 Lmask0_%=\n" \
"Lmask0r_%=:\n" \
"v_mfma_f32_16x16x32_bf16 v[0:3], a[192:195], a[128:131], v[0:3]\n" \
"s_lshl_b32 s70, s95, 1\n" \
"v_cvt_pk_bf16_f32 v152, v152, v153\n" \
"v_cvt_pk_bf16_f32 v153, v154, v155\n" \
"v_mfma_f32_16x16x32_bf16 v[0:3], a[196:199], a[132:135], v[0:3]\n" \
"s_add_u32 s70, s70, 131072\n" \
"v_cvt_pk_bf16_f32 v154, v156, v157\n" \
"v_cvt_pk_bf16_f32 v155, v158, v159\n" \
"v_mfma_f32_16x16x32_bf16 v[0:3], a[200:203], a[136:139], v[0:3]\n" \
"ds_read_b128 a[20:23], v254\n" \
"v_accvgpr_write_b32 a172, v152\n" \
"v_mfma_f32_16x16x32_bf16 v[0:3], a[204:207], a[140:143], v[0:3]\n" \
"ds_read_b128 a[52:55], v254\n" \
"v_accvgpr_write_b32 a173, v153\n" \
"v_mov_b32 v201, 0xf149f2ca\n" \
"v_mfma_f32_16x16x32_bf16 v[8:11], a[192:195], a[144:147], 0\n" \
"v_accvgpr_write_b32 a174, v154\n" \
"v_accvgpr_write_b32 a175, v155\n" \
"v_mfma_f32_16x16x32_bf16 v[8:11], a[196:199], a[148:151], v[8:11]\n" \
"v_cvt_pk_bf16_f32 v176, v176, v177\n" \
"v_cvt_pk_bf16_f32 v177, v178, v179\n" \
"v_mfma_f32_16x16x32_bf16 v[8:11], a[200:203], a[152:155], v[8:11]\n" \
"v_cvt_pk_bf16_f32 v178, v180, v181\n" \
"v_cvt_pk_bf16_f32 v179, v182, v183\n" \
"v_mfma_f32_16x16x32_bf16 v[8:11], a[204:207], a[156:159], v[8:11]\n" \
"v_accvgpr_write_b32 a184, v176\n" \
"v_accvgpr_write_b32 a185, v177\n" \
"v_max3_f32 v192, v0, v1, v2\n" \
"v_max3_f32 v192, v192, v3, v201\n" \
"v_mfma_f32_16x16x32_bf16 v[16:19], a[192:195], a[160:163], 0\n" \
"v_accvgpr_write_b32 a186, v178\n" \
"v_accvgpr_write_b32 a187, v179\n" \
"v_mfma_f32_16x16x32_bf16 v[16:19], a[196:199], a[164:167], v[16:19]\n" \
"v_cvt_pk_bf16_f32 v184, v184, v185\n" \
"v_cvt_pk_bf16_f32 v185, v186, v187\n" \
"v_mfma_f32_16x16x32_bf16 v[16:19], a[200:203], a[168:171], v[16:19]\n" \
"v_cvt_pk_bf16_f32 v186, v188, v189\n" \
"v_cvt_pk_bf16_f32 v187, v190, v191\n" \
"v_mfma_f32_16x16x32_bf16 v[16:19], a[204:207], a[172:175], v[16:19]\n" \
"v_max3_f32 v193, v8, v9, v10\n" \
"v_accvgpr_write_b32 a188, v184\n" \
"v_accvgpr_write_b32 a189, v185\n" \
"v_mfma_f32_16x16x32_bf16 v[24:27], a[192:195], a[176:179], 0\n" \
"v_accvgpr_write_b32 a190, v186\n" \
"v_accvgpr_write_b32 a191, v187\n" \
"v_mfma_f32_16x16x32_bf16 v[24:27], a[196:199], a[180:183], v[24:27]\n" \
"v_add_u32 v200, s70, v248\n" \
"v_max3_f32 v193, v193, v11, v201\n" \
"v_mfma_f32_16x16x32_bf16 v[24:27], a[200:203], a[184:187], v[24:27]\n" \
"ds_read_b128 v[128:131], v254\n" \
"ds_read_b128 v[132:135], v254\n" \
"v_max3_f32 v194, v16, v17, v18\n" \
"v_mfma_f32_16x16x32_bf16 v[24:27], a[204:207], a[188:191], v[24:27]\n" \
"s_waitcnt lgkmcnt(2)\n" \
"ds_read_b128 v[136:139], v254\n" \
"ds_read_b128 v[140:143], v254\n" \
"v_mfma_f32_16x16x32_bf16 v[4:7], a[208:211], a[128:131], v[4:7]\n" \
"ds_read_b128 v[144:147], v254\n" \
"ds_read_b128 v[148:151], v254\n" \
"v_mfma_f32_16x16x32_bf16 v[4:7], a[212:215], a[132:135], v[4:7]\n" \
"ds_read_b128 a[192:195], v249 offset:16384\n" \
"v_max3_f32 v194, v194, v19, v201\n" \
"v_mfma_f32_16x16x32_bf16 v[4:7], a[216:219], a[136:139], v[4:7]\n" \
"ds_read_b128 v[152:155], v254\n" \
"v_max3_f32 v195, v24, v25, v26\n" \
"v_mfma_f32_16x16x32_bf16 v[4:7], a[220:223], a[140:143], v[4:7]\n" \
"ds_read_b128 a[196:199], v249 offset:18432\n" \
"ds_read_b128 v[156:159], v254\n" \
"v_max3_f32 v195, v195, v27, v201\n" \
"v_mov_b32 v196, v192\n" \
"v_mov_b32 v197, v193\n" \
"v_mov_b32 v198, v194\n" \
"v_mov_b32 v199, v195\n" \
"v_permlane16_swap_b32 v196, v192\n" \
"v_permlane16_swap_b32 v197, v193\n" \
"v_permlane16_swap_b32 v198, v194\n" \
"v_permlane16_swap_b32 v199, v195\n" \
"v_max_f32 v192, v192, v196\n" \
"v_max_f32 v193, v193, v197\n" \
"v_max_f32 v194, v194, v198\n" \
"v_mfma_f32_16x16x32_bf16 v[12:15], a[216:219], a[152:155], 0\n" \
"ds_read_b128 v[160:163], v254\n" \
"v_max_f32 v195, v195, v199\n" \
"v_mfma_f32_16x16x32_bf16 v[12:15], a[220:223], a[156:159], v[12:15]\n" \
"ds_read_b128 a[200:203], v249 offset:20480\n" \
"v_mov_b32 v196, v192\n" \
"v_mfma_f32_16x16x32_bf16 v[12:15], a[208:211], a[144:147], v[12:15]\n" \
"ds_read_b128 v[164:167], v254\n" \
"v_mov_b32 v197, v193\n" \
"v_mfma_f32_16x16x32_bf16 v[12:15], a[212:215], a[148:151], v[12:15]\n" \
"buffer_store_dwordx4 a[24:27], v248, s[56:59], s90 offen offset:2048 sc1\n" \
"ds_read_b128 a[204:207], v249 offset:22528\n" \
"ds_write_b128 v200, a[160:163] offset:0\n" \
"v_mov_b32 v198, v194\n" \
"v_mov_b32 v199, v195\n" \
"v_permlane32_swap_b32 v196, v192\n" \
"v_permlane32_swap_b32 v197, v193\n" \
"v_permlane32_swap_b32 v198, v194\n" \
"v_permlane32_swap_b32 v199, v195\n" \
"v_max_f32 v192, v192, v196\n" \
"v_max_f32 v193, v193, v197\n" \
"v_max_f32 v194, v194, v198\n" \
"v_max_f32 v195, v195, v199\n" \
"v_sub_f32 v224, 0, v192\n" \
"v_sub_f32 v225, 0, v192\n" \
"v_mfma_f32_16x16x32_bf16 v[20:23], a[208:211], a[160:163], 0\n" \
"v_sub_f32 v226, 0, v192\n" \
"v_sub_f32 v227, 0, v192\n" \
"v_mfma_f32_16x16x32_bf16 v[20:23], a[212:215], a[164:167], v[20:23]\n" \
"v_add_f32 v32, v32, v224\n" \
"v_add_f32 v33, v33, v224\n" \
"v_mfma_f32_16x16x32_bf16 v[20:23], a[216:219], a[168:171], v[20:23]\n" \
"ds_read_b128 a[24:27], v254\n" \
"v_add_f32 v34, v34, v224\n" \
"v_mfma_f32_16x16x32_bf16 v[20:23], a[220:223], a[172:175], v[20:23]\n" \
"buffer_store_dwordx4 a[28:31], v248, s[56:59], s90 offen offset:3072 sc1\n" \
"ds_write_b128 v200, a[164:167] offset:1024\n" \
"v_add_f32 v35, v35, v224\n" \
"v_sub_f32 v228, 0, v193\n" \
"v_sub_f32 v229, 0, v193\n" \
"v_sub_f32 v230, 0, v193\n" \
"v_sub_f32 v231, 0, v193\n" \
"v_sub_f32 v232, 0, v194\n" \
"v_sub_f32 v233, 0, v194\n" \
"v_sub_f32 v234, 0, v194\n" \
"v_sub_f32 v235, 0, v194\n" \
"v_sub_f32 v236, 0, v195\n" \
"v_sub_f32 v237, 0, v195\n" \
"v_sub_f32 v238, 0, v195\n" \
"v_mfma_f32_16x16x32_bf16 v[28:31], a[216:219], a[184:187], 0\n" \
"v_sub_f32 v239, 0, v195\n" \
"v_add_f32 v36, v36, v224\n" \
"v_mfma_f32_16x16x32_bf16 v[28:31], a[220:223], a[188:191], v[28:31]\n" \
"global_load_dword v253, v252, s[40:41]\n" \
"v_mfma_f32_16x16x32_bf16 v[28:31], a[208:211], a[176:179], v[28:31]\n" \
"s_sub_u32 s80, s80, 1\n" \
"s_cmp_gt_i32 s80, 0\n" \
"ds_write_b128 v200, a[168:171] offset:2048\n" \
"v_add_f32 v37, v37, v224\n" \
"v_add_f32 v38, v38, v224\n" \
"v_add_f32 v39, v39, v224\n" \
"v_pk_add_f32 v[0:1], v[0:1], v[224:225]\n" \
"v_pk_add_f32 v[2:3], v[2:3], v[224:225]\n" \
"v_pk_add_f32 v[4:5], v[4:5], v[224:225]\n" \
"v_pk_add_f32 v[6:7], v[6:7], v[224:225]\n" \
"v_exp_f32 v0, v0\n" \
"v_exp_f32 v1, v1\n" \
"v_exp_f32 v2, v2\n" \
"v_add_f32 v240, v0, v1\n" \
"v_exp_f32 v3, v3\n" \
"v_mfma_f32_16x16x32_bf16 v[28:31], a[212:215], a[180:183], v[28:31]\n" \
"s_cselect_b32 s81, 0x80000, 0\n" \
"s_add_u32 s40, s40, s81\n" \
"s_addc_u32 s41, s41, 0\n" \
"v_add_f32 v240, v240, v2\n" \
"v_cvt_pk_bf16_f32 v0, v0, v1\n" \
"v_exp_f32 v4, v4\n" \
"v_add_f32 v240, v240, v3\n" \
"v_exp_f32 v5, v5\n" \
"v_add_f32 v240, v240, v4\n" \
"v_cvt_pk_bf16_f32 v1, v2, v3\n" \
"v_exp_f32 v6, v6\n" \
"v_add_f32 v240, v240, v5\n" \
"v_mfma_f32_16x16x32_bf16 v[32:35], a[224:227], a[128:131], v[32:35]\n" \
"s_add_u32 m0, s95, 49152\n" \
"v_exp_f32 v7, v7\n" \
"v_mfma_f32_16x16x32_bf16 v[32:35], a[228:231], a[132:135], v[32:35]\n" \
"ds_read_b128 a[208:211], v249 offset:16640\n" \
"global_load_lds_dwordx4 v250, s[36:37] offset:0 sc1\n" \
"v_mfma_f32_16x16x32_bf16 v[32:35], a[232:235], a[136:139], v[32:35]\n" \
"ds_read_b128 a[56:59], v254\n" \
"v_add_f32 v240, v240, v6\n" \
"v_mfma_f32_16x16x32_bf16 v[32:35], a[236:239], a[140:143], v[32:35]\n" \
"ds_read_b128 a[212:215], v249 offset:18688\n" \
"ds_read_b128 v[168:171], v254\n" \
"v_cvt_pk_bf16_f32 v2, v4, v5\n" \
"v_add_f32 v240, v240, v7\n" \
"v_cvt_pk_bf16_f32 v3, v6, v7\n" \
"v_pk_add_f32 v[8:9], v[8:9], v[228:229]\n" \
"v_pk_add_f32 v[10:11], v[10:11], v[228:229]\n" \
"v_pk_add_f32 v[12:13], v[12:13], v[228:229]\n" \
"v_pk_add_f32 v[14:15], v[14:15], v[228:229]\n" \
"v_exp_f32 v8, v8\n" \
"v_exp_f32 v9, v9\n" \
"v_exp_f32 v10, v10\n" \
"v_add_f32 v241, v8, v9\n" \
"v_exp_f32 v11, v11\n" \
"v_mfma_f32_16x16x32_bf16 v[40:43], a[232:235], a[152:155], v[228:231]\n" \
"ds_read_b128 a[28:31], v254\n" \
"v_add_f32 v241, v241, v10\n" \
"v_mfma_f32_16x16x32_bf16 v[40:43], a[236:239], a[156:159], v[40:43]\n" \
"ds_read_b128 a[216:219], v249 offset:20736\n" \
"global_load_lds_dwordx4 v250, s[36:37] offset:1024 sc1\n" \
"v_mfma_f32_16x16x32_bf16 v[40:43], a[224:227], a[144:147], v[40:43]\n" \
"ds_read_b128 a[60:63], v254\n" \
"v_cvt_pk_bf16_f32 v8, v8, v9\n" \
"v_mfma_f32_16x16x32_bf16 v[40:43], a[228:231], a[148:151], v[40:43]\n" \
"ds_read_b128 a[220:223], v249 offset:22784\n" \
"ds_write_b128 v200, a[172:175] offset:3072\n" \
"v_exp_f32 v12, v12\n" \
"v_add_f32 v241, v241, v11\n" \
"v_exp_f32 v13, v13\n" \
"v_add_f32 v241, v241, v12\n" \
"v_cvt_pk_bf16_f32 v9, v10, v11\n" \
"v_exp_f32 v14, v14\n" \
"v_add_f32 v241, v241, v13\n" \
"v_exp_f32 v15, v15\n" \
"v_mfma_f32_16x16x32_bf16 v[48:51], a[224:227], a[160:163], v[232:235]\n" \
"v_add_f32 v241, v241, v14\n" \
"v_cvt_pk_bf16_f32 v10, v12, v13\n" \
"v_mfma_f32_16x16x32_bf16 v[48:51], a[228:231], a[164:167], v[48:51]\n" \
"global_load_lds_dwordx4 v250, s[36:37] offset:2048 sc1\n" \
"v_add_f32 v241, v241, v15\n" \
"v_mfma_f32_16x16x32_bf16 v[48:51], a[232:235], a[168:171], v[48:51]\n" \
"ds_read_b128 v[172:175], v254\n" \
"v_cvt_pk_bf16_f32 v11, v14, v15\n" \
"v_mfma_f32_16x16x32_bf16 v[48:51], a[236:239], a[172:175], v[48:51]\n" \
"ds_write_b128 v200, a[176:179] offset:4096\n" \
"v_pk_add_f32 v[16:17], v[16:17], v[232:233]\n" \
"v_pk_add_f32 v[18:19], v[18:19], v[232:233]\n" \
"v_pk_add_f32 v[20:21], v[20:21], v[232:233]\n" \
"v_pk_add_f32 v[22:23], v[22:23], v[232:233]\n" \
"v_exp_f32 v16, v16\n" \
"v_exp_f32 v17, v17\n" \
"v_exp_f32 v18, v18\n" \
"v_add_f32 v242, v16, v17\n" \
"v_exp_f32 v19, v19\n" \
"v_mfma_f32_16x16x32_bf16 v[56:59], a[232:235], a[184:187], v[236:239]\n" \
"global_load_lds_dwordx4 v250, s[36:37] offset:3072 sc1\n" \
"v_add_f32 v242, v242, v18\n" \
"v_mfma_f32_16x16x32_bf16 v[56:59], a[236:239], a[188:191], v[56:59]\n" \
"s_add_u32 s36, s36, 17408\n" \
"v_cvt_pk_bf16_f32 v16, v16, v17\n" \
"s_mov_b32 s42, s60\n" \
"v_mfma_f32_16x16x32_bf16 v[56:59], a[224:227], a[176:179], v[56:59]\n" \
"s_mov_b32 s43, s60\n" \
"s_mov_b64 s[8:9], 0\n" \
"ds_write_b128 v200, a[180:183] offset:5120\n" \
"v_exp_f32 v20, v20\n" \
"v_add_f32 v242, v242, v19\n" \
"v_mfma_f32_16x16x32_bf16 v[56:59], a[228:231], a[180:183], v[56:59]\n" \
"s_mov_b64 s[10:11], -1\n" \
"s_lshl_b32 s71, s63, 2\n" \
"s_add_u32 s71, s71, s79\n" \
"v_mfma_f32_16x16x32_bf16 v[36:39], a[240:243], a[128:131], v[36:39]\n" \
"v_exp_f32 v21, v21\n" \
"v_mfma_f32_16x16x32_bf16 v[36:39], a[244:247], a[132:135], v[36:39]\n" \
"ds_read_b128 a[224:227], v249 offset:24576\n" \
"v_add_f32 v242, v242, v20\n" \
"v_mfma_f32_16x16x32_bf16 v[36:39], a[248:251], a[136:139], v[36:39]\n" \
"ds_read_b128 v[176:179], v254\n" \
"v_cvt_pk_bf16_f32 v17, v18, v19\n" \
"v_mfma_f32_16x16x32_bf16 v[36:39], a[252:255], a[140:143], v[36:39]\n" \
"ds_read_b128 a[228:231], v249 offset:26624\n" \
"ds_read_b128 v[180:183], v254\n" \
"v_exp_f32 v22, v22\n" \
"v_add_f32 v242, v242, v21\n" \
"v_exp_f32 v23, v23\n" \
"v_add_f32 v242, v242, v22\n" \
"v_mfma_f32_16x16x32_bf16 v[44:47], a[248:251], a[152:155], v[228:231]\n" \
"ds_read_b128 v[184:187], v254\n" \
"v_cvt_pk_bf16_f32 v18, v20, v21\n" \
"v_mfma_f32_16x16x32_bf16 v[44:47], a[252:255], a[156:159], v[44:47]\n" \
"ds_read_b128 a[232:235], v249 offset:28672\n" \
"v_add_f32 v242, v242, v23\n" \
"v_mfma_f32_16x16x32_bf16 v[44:47], a[240:243], a[144:147], v[44:47]\n" \
"ds_read_b128 v[188:191], v254\n" \
"v_cvt_pk_bf16_f32 v19, v22, v23\n" \
"v_mfma_f32_16x16x32_bf16 v[44:47], a[244:247], a[148:151], v[44:47]\n" \
"ds_read_b128 a[236:239], v249 offset:30720\n" \
"ds_write_b128 v200, a[184:187] offset:6144\n" \
"v_pk_add_f32 v[24:25], v[24:25], v[236:237]\n" \
"v_pk_add_f32 v[26:27], v[26:27], v[236:237]\n" \
"v_pk_add_f32 v[28:29], v[28:29], v[236:237]\n" \
"v_pk_add_f32 v[30:31], v[30:31], v[236:237]\n" \
"v_exp_f32 v24, v24\n" \
"v_exp_f32 v25, v25\n" \
"v_exp_f32 v26, v26\n" \
"v_add_f32 v243, v24, v25\n" \
"v_mfma_f32_16x16x32_bf16 v[52:55], a[240:243], a[160:163], v[232:235]\n" \
"v_exp_f32 v27, v27\n" \
"v_mfma_f32_16x16x32_bf16 v[52:55], a[244:247], a[164:167], v[52:55]\n" \
"v_add_f32 v243, v243, v26\n" \
"v_cvt_pk_bf16_f32 v24, v24, v25\n" \
"v_mfma_f32_16x16x32_bf16 v[52:55], a[248:251], a[168:171], v[52:55]\n" \
"v_exp_f32 v28, v28\n" \
"v_mfma_f32_16x16x32_bf16 v[52:55], a[252:255], a[172:175], v[52:55]\n" \
"ds_write_b128 v200, a[188:191] offset:7168\n" \
"v_add_f32 v243, v243, v27\n" \
"v_exp_f32 v29, v29\n" \
"v_add_f32 v243, v243, v28\n" \
"v_cvt_pk_bf16_f32 v25, v26, v27\n" \
"v_exp_f32 v30, v30\n" \
"v_add_f32 v243, v243, v29\n" \
"v_mfma_f32_16x16x32_bf16 v[60:63], a[248:251], a[184:187], v[236:239]\n" \
"v_exp_f32 v31, v31\n" \
"v_mfma_f32_16x16x32_bf16 v[60:63], a[252:255], a[188:191], v[60:63]\n" \
"v_add_f32 v243, v243, v30\n" \
"v_cvt_pk_bf16_f32 v26, v28, v29\n" \
"v_mfma_f32_16x16x32_bf16 v[60:63], a[240:243], a[176:179], v[60:63]\n" \
"v_add_f32 v243, v243, v31\n" \
"v_mov_b32 v202, s60\n" \
"v_mfma_f32_16x16x32_bf16 v[60:63], a[244:247], a[180:183], v[60:63]\n" \
"v_lshlrev_b32 v203, 28, s61\n" \
"v_mov_b32 v246, v249\n" \
"v_mov_b32 v254, 0xff800000\n" \
"ds_read_b128 a[240:243], v249 offset:24832\n" \
"ds_read_b128 a[244:247], v249 offset:26880\n" \
"ds_read_b128 a[248:251], v249 offset:28928\n" \
"s_cmp_eq_u32 s63, 0\n" \
"s_waitcnt vmcnt(5) lgkmcnt(3)\n" \
"s_barrier\n" \
"buffer_store_dword v202, v203, s[56:59], s71 offen sc1\n" \
"s_memrealtime s[12:13]\n" \
"s_cbranch_scc1 LA1l_%=\n" \
"s_branch LA1_%=\n" \
"Lpg5398_%=:\n" \
"s_cbranch_execz Lpg6325_%=\n" \
"Lloop_%=:\n" \
"v_mfma_f32_16x16x32_bf16 a[0:3], a[192:195], v[64:67], a[0:3]\n" \
"v_exp_f32 v0, v0\n" \
"v_mfma_f32_16x16x32_bf16 a[0:3], a[196:199], v[96:99], a[0:3]\n" \
"v_cvt_pk_bf16_f32 v123, v126, v127\n" \
"v_add_f32 v240, v240, v0\n" \
"v_exp_f32 v1, v1\n" \
"v_mfma_f32_16x16x32_bf16 a[32:35], a[192:195], v[72:75], a[32:35]\n" \
"s_add_u32 m0, s73, 0x4000\n" \
"ds_read_b128 a[244:247], v247 offset:30720\n" \
"v_add_f32 v240, v240, v1\n" \
"v_mfma_f32_16x16x32_bf16 a[32:35], a[196:199], v[104:107], a[32:35]\n" \
"ds_read_b128 a[248:251], v247 offset:23552\n" \
"global_load_lds_dwordx4 v250, s[36:37] offset:0 sc1\n" \
"v_exp_f32 v2, v2\n" \
"v_mfma_f32_16x16x32_bf16 v[128:131], a[192:195], v[80:83], v[128:131]\n" \
"ds_read_b128 a[252:255], v247 offset:31744\n" \
"v_add_f32 v240, v240, v2\n" \
"v_mfma_f32_16x16x32_bf16 v[128:131], a[196:199], v[112:115], v[128:131]\n" \
"v_xor_b32 v247, 0x8000, v247\n" \
"v_cvt_pk_bf16_f32 v0, v0, v1\n" \
"v_mfma_f32_16x16x32_bf16 v[160:163], a[192:195], v[88:91], v[160:163]\n" \
"v_exp_f32 v3, v3\n" \
"v_mfma_f32_16x16x32_bf16 v[160:163], a[196:199], v[120:123], v[160:163]\n" \
"ds_read_b128 a[192:195], v246 offset:16384\n" \
"v_add_f32 v240, v240, v3\n" \
"v_mfma_f32_16x16x32_bf16 v[164:167], a[200:203], v[88:91], v[164:167]\n" \
"v_exp_f32 v4, v4\n" \
"v_mfma_f32_16x16x32_bf16 v[164:167], a[204:207], v[120:123], v[164:167]\n" \
"v_add_f32 v240, v240, v4\n" \
"v_cvt_pk_bf16_f32 v1, v2, v3\n" \
"v_mfma_f32_16x16x32_bf16 v[132:135], a[200:203], v[80:83], v[132:135]\n" \
"v_exp_f32 v5, v5\n" \
"v_mfma_f32_16x16x32_bf16 v[132:135], a[204:207], v[112:115], v[132:135]\n" \
"ds_read_b128 a[196:199], v246 offset:18432\n" \
"v_add_f32 v240, v240, v5\n" \
"v_mfma_f32_16x16x32_bf16 a[36:39], a[200:203], v[72:75], a[36:39]\n" \
"v_exp_f32 v6, v6\n" \
"v_mfma_f32_16x16x32_bf16 a[36:39], a[204:207], v[104:107], a[36:39]\n" \
"v_add_f32 v240, v240, v6\n" \
"v_cvt_pk_bf16_f32 v2, v4, v5\n" \
"v_mfma_f32_16x16x32_bf16 a[4:7], a[200:203], v[64:67], a[4:7]\n" \
"v_exp_f32 v7, v7\n" \
"v_mfma_f32_16x16x32_bf16 a[4:7], a[204:207], v[96:99], a[4:7]\n" \
"ds_read_b128 a[200:203], v246 offset:20480\n" \
"global_load_lds_dwordx4 v250, s[36:37] offset:1024 sc1\n" \
"v_add_f32 v240, v240, v7\n" \
"v_mfma_f32_16x16x32_bf16 a[8:11], a[208:211], v[64:67], a[8:11]\n" \
"v_exp_f32 v8, v8\n" \
"v_mfma_f32_16x16x32_bf16 a[8:11], a[212:215], v[96:99], a[8:11]\n" \
"v_add_f32 v241, v241, v8\n" \
"v_cvt_pk_bf16_f32 v3, v6, v7\n" \
"v_mfma_f32_16x16x32_bf16 a[40:43], a[208:211], v[72:75], a[40:43]\n" \
"v_exp_f32 v9, v9\n" \
"v_mfma_f32_16x16x32_bf16 a[40:43], a[212:215], v[104:107], a[40:43]\n" \
"ds_read_b128 a[204:207], v246 offset:22528\n" \
"v_add_f32 v241, v241, v9\n" \
"v_mfma_f32_16x16x32_bf16 v[136:139], a[208:211], v[80:83], v[136:139]\n" \
"v_exp_f32 v10, v10\n" \
"v_mfma_f32_16x16x32_bf16 v[136:139], a[212:215], v[112:115], v[136:139]\n" \
"v_add_f32 v241, v241, v10\n" \
"v_cvt_pk_bf16_f32 v8, v8, v9\n" \
"v_mfma_f32_16x16x32_bf16 v[168:171], a[208:211], v[88:91], v[168:171]\n" \
"v_exp_f32 v11, v11\n" \
"v_mfma_f32_16x16x32_bf16 v[168:171], a[212:215], v[120:123], v[168:171]\n" \
"ds_read_b128 a[208:211], v246 offset:16640\n" \
"v_add_f32 v241, v241, v11\n" \
"v_mfma_f32_16x16x32_bf16 v[172:175], a[216:219], v[88:91], v[172:175]\n" \
"v_exp_f32 v12, v12\n" \
"v_mfma_f32_16x16x32_bf16 v[172:175], a[220:223], v[120:123], v[172:175]\n" \
"v_add_f32 v241, v241, v12\n" \
"v_cvt_pk_bf16_f32 v9, v10, v11\n" \
"v_mfma_f32_16x16x32_bf16 v[140:143], a[216:219], v[80:83], v[140:143]\n" \
"v_exp_f32 v13, v13\n" \
"v_mfma_f32_16x16x32_bf16 v[140:143], a[220:223], v[112:115], v[140:143]\n" \
"ds_read_b128 a[212:215], v246 offset:18688\n" \
"global_load_lds_dwordx4 v250, s[36:37] offset:2048 sc1\n" \
"v_add_f32 v241, v241, v13\n" \
"v_mfma_f32_16x16x32_bf16 a[44:47], a[216:219], v[72:75], a[44:47]\n" \
"v_exp_f32 v14, v14\n" \
"v_mfma_f32_16x16x32_bf16 a[44:47], a[220:223], v[104:107], a[44:47]\n" \
"v_add_f32 v241, v241, v14\n" \
"v_cvt_pk_bf16_f32 v10, v12, v13\n" \
"v_mfma_f32_16x16x32_bf16 a[12:15], a[216:219], v[64:67], a[12:15]\n" \
"s_waitcnt lgkmcnt(6)\n" \
"v_exp_f32 v15, v15\n" \
"v_mfma_f32_16x16x32_bf16 a[12:15], a[220:223], v[96:99], a[12:15]\n" \
"ds_read_b128 a[216:219], v246 offset:20736\n" \
"v_add_f32 v241, v241, v15\n" \
"v_mfma_f32_16x16x32_bf16 a[16:19], a[224:227], v[64:67], a[16:19]\n" \
"v_exp_f32 v16, v16\n" \
"v_mfma_f32_16x16x32_bf16 a[16:19], a[228:231], v[96:99], a[16:19]\n" \
"v_add_f32 v242, v242, v16\n" \
"v_cvt_pk_bf16_f32 v11, v14, v15\n" \
"v_mfma_f32_16x16x32_bf16 a[48:51], a[224:227], v[72:75], a[48:51]\n" \
"v_exp_f32 v17, v17\n" \
"v_mfma_f32_16x16x32_bf16 a[48:51], a[228:231], v[104:107], a[48:51]\n" \
"ds_read_b128 a[220:223], v246 offset:22784\n" \
"v_add_f32 v242, v242, v17\n" \
"v_exp_f32 v18, v18\n" \
"v_mfma_f32_16x16x32_bf16 v[144:147], a[224:227], v[80:83], v[144:147]\n" \
"v_add_f32 v242, v242, v18\n" \
"v_cvt_pk_bf16_f32 v16, v16, v17\n" \
"v_mfma_f32_16x16x32_bf16 v[144:147], a[228:231], v[112:115], v[144:147]\n" \
"global_load_lds_dwordx4 v250, s[36:37] offset:3072 sc1\n" \
"v_exp_f32 v19, v19\n" \
"s_add_u32 s36, s36, 17408\n" \
"v_mfma_f32_16x16x32_bf16 v[176:179], a[224:227], v[88:91], v[176:179]\n" \
"s_xor_b32 s73, s73, 0x8000\n" \
"s_xor_b64 s[52:53], s[44:45], s[42:43]\n" \
"s_xor_b64 s[54:55], s[46:47], s[42:43]\n" \
"v_mfma_f32_16x16x32_bf16 v[176:179], a[228:231], v[120:123], v[176:179]\n" \
"s_or_b64 s[52:53], s[52:53], s[54:55]\n" \
"ds_read_b128 a[224:227], v246 offset:24576\n" \
"v_add_f32 v242, v242, v19\n" \
"v_exp_f32 v20, v20\n" \
"s_xor_b64 s[54:55], s[48:49], s[42:43]\n" \
"v_mfma_f32_16x16x32_bf16 v[180:183], a[232:235], v[88:91], v[180:183]\n" \
"s_or_b64 s[52:53], s[52:53], s[54:55]\n" \
"s_xor_b64 s[54:55], s[50:51], s[42:43]\n" \
"s_or_b64 s[52:53], s[52:53], s[54:55]\n" \
"v_mfma_f32_16x16x32_bf16 v[180:183], a[236:239], v[120:123], v[180:183]\n" \
"s_and_b64 s[52:53], s[52:53], s[8:9]\n" \
"s_cbranch_scc1 Lrs0_%=\n" \
"Lrr0_%=:\n" \
"v_add_f32 v242, v242, v20\n" \
"v_cvt_pk_bf16_f32 v17, v18, v19\n" \
"v_mfma_f32_16x16x32_bf16 v[148:151], a[232:235], v[80:83], v[148:151]\n" \
"s_mov_b64 s[8:9], s[10:11]\n" \
"v_exp_f32 v21, v21\n" \
"v_mfma_f32_16x16x32_bf16 v[148:151], a[236:239], v[112:115], v[148:151]\n" \
"ds_read_b128 a[228:231], v246 offset:26624\n" \
"v_add_f32 v242, v242, v21\n" \
"v_mfma_f32_16x16x32_bf16 a[52:55], a[232:235], v[72:75], a[52:55]\n" \
"v_exp_f32 v22, v22\n" \
"v_mfma_f32_16x16x32_bf16 a[52:55], a[236:239], v[104:107], a[52:55]\n" \
"v_add_f32 v242, v242, v22\n" \
"v_cvt_pk_bf16_f32 v18, v20, v21\n" \
"v_mfma_f32_16x16x32_bf16 a[20:23], a[232:235], v[64:67], a[20:23]\n" \
"v_exp_f32 v23, v23\n" \
"v_mfma_f32_16x16x32_bf16 a[20:23], a[236:239], v[96:99], a[20:23]\n" \
"ds_read_b128 a[232:235], v246 offset:28672\n" \
"v_add_f32 v242, v242, v23\n" \
"v_mfma_f32_16x16x32_bf16 a[24:27], a[240:243], v[64:67], a[24:27]\n" \
"v_exp_f32 v24, v24\n" \
"v_mfma_f32_16x16x32_bf16 a[24:27], a[244:247], v[96:99], a[24:27]\n" \
"v_add_f32 v243, v243, v24\n" \
"v_cvt_pk_bf16_f32 v19, v22, v23\n" \
"Lpg5547_%=:\n" \
"s_cbranch_execz Lpg6839_%=\n" \
"v_mfma_f32_16x16x32_bf16 a[56:59], a[240:243], v[72:75], a[56:59]\n" \
"v_exp_f32 v25, v25\n" \
"v_mfma_f32_16x16x32_bf16 a[56:59], a[244:247], v[104:107], a[56:59]\n" \
"ds_read_b128 a[236:239], v246 offset:30720\n" \
"v_add_f32 v243, v243, v25\n" \
"v_mfma_f32_16x16x32_bf16 v[152:155], a[240:243], v[80:83], v[152:155]\n" \
"v_exp_f32 v26, v26\n" \
"v_mfma_f32_16x16x32_bf16 v[152:155], a[244:247], v[112:115], v[152:155]\n" \
"v_add_f32 v243, v243, v26\n" \
"v_cvt_pk_bf16_f32 v24, v24, v25\n" \
"v_mfma_f32_16x16x32_bf16 v[184:187], a[240:243], v[88:91], v[184:187]\n" \
"v_exp_f32 v27, v27\n" \
"v_mfma_f32_16x16x32_bf16 v[184:187], a[244:247], v[120:123], v[184:187]\n" \
"ds_read_b128 a[240:243], v246 offset:24832\n" \
"v_add_f32 v243, v243, v27\n" \
"v_mfma_f32_16x16x32_bf16 v[188:191], a[248:251], v[88:91], v[188:191]\n" \
"v_exp_f32 v28, v28\n" \
"v_mfma_f32_16x16x32_bf16 v[188:191], a[252:255], v[120:123], v[188:191]\n" \
"v_add_f32 v243, v243, v28\n" \
"v_cvt_pk_bf16_f32 v25, v26, v27\n" \
"v_mfma_f32_16x16x32_bf16 v[156:159], a[248:251], v[80:83], v[156:159]\n" \
"v_exp_f32 v29, v29\n" \
"v_mfma_f32_16x16x32_bf16 v[156:159], a[252:255], v[112:115], v[156:159]\n" \
"ds_read_b128 a[244:247], v246 offset:26880\n" \
"v_add_f32 v243, v243, v29\n" \
"v_mfma_f32_16x16x32_bf16 a[60:63], a[248:251], v[72:75], a[60:63]\n" \
"v_exp_f32 v30, v30\n" \
"v_mfma_f32_16x16x32_bf16 a[60:63], a[252:255], v[104:107], a[60:63]\n" \
"v_add_f32 v243, v243, v30\n" \
"v_cvt_pk_bf16_f32 v26, v28, v29\n" \
"v_mfma_f32_16x16x32_bf16 a[28:31], a[248:251], v[64:67], a[28:31]\n" \
"s_waitcnt lgkmcnt(6)\n" \
"v_exp_f32 v31, v31\n" \
"v_mfma_f32_16x16x32_bf16 a[28:31], a[252:255], v[96:99], a[28:31]\n" \
"ds_read_b128 a[248:251], v246 offset:28928\n" \
"v_add_f32 v243, v243, v31\n" \
"LA1_%=:\n" \
"Lpg5585_%=:\n" \
"s_cbranch_execz Lpg7141_%=\n" \
"v_mfma_f32_16x16x32_bf16 v[64:67], a[192:195], a[128:131], v[224:227]\n" \
"v_exp_f32 v32, v32\n" \
"v_mfma_f32_16x16x32_bf16 v[64:67], a[196:199], a[132:135], v[64:67]\n" \
"v_cvt_pk_bf16_f32 v27, v30, v31\n" \
"v_add_f32 v240, v240, v32\n" \
"v_mfma_f32_16x16x32_bf16 v[64:67], a[200:203], a[136:139], v[64:67]\n" \
"s_mov_b32 m0, s74\n" \
"v_exp_f32 v33, v33\n" \
"v_mfma_f32_16x16x32_bf16 v[64:67], a[204:207], a[140:143], v[64:67]\n" \
"ds_read_b128 a[252:255], v246 offset:30976\n" \
"global_load_lds_dwordx4 v250, s[38:39] offset:0 sc1\n" \
"v_add_f32 v240, v240, v33\n" \
"v_mfma_f32_16x16x32_bf16 v[72:75], a[200:203], a[152:155], v[228:231]\n" \
"v_exp_f32 v34, v34\n" \
"v_mfma_f32_16x16x32_bf16 v[72:75], a[204:207], a[156:159], v[72:75]\n" \
"v_xor_b32 v246, 0x8000, v246\n" \
"v_add_f32 v240, v240, v34\n" \
"v_cvt_pk_bf16_f32 v32, v32, v33\n" \
"v_mfma_f32_16x16x32_bf16 v[72:75], a[192:195], a[144:147], v[72:75]\n" \
"v_exp_f32 v35, v35\n" \
"v_mfma_f32_16x16x32_bf16 v[72:75], a[196:199], a[148:151], v[72:75]\n" \
"v_add_f32 v240, v240, v35\n" \
"v_exp_f32 v36, v36\n" \
"v_mfma_f32_16x16x32_bf16 v[80:83], a[192:195], a[160:163], v[232:235]\n" \
"v_add_f32 v240, v240, v36\n" \
"v_cvt_pk_bf16_f32 v33, v34, v35\n" \
"v_mfma_f32_16x16x32_bf16 v[80:83], a[196:199], a[164:167], v[80:83]\n" \
"v_exp_f32 v37, v37\n" \
"v_mfma_f32_16x16x32_bf16 v[80:83], a[200:203], a[168:171], v[80:83]\n" \
"v_add_f32 v240, v240, v37\n" \
"v_mfma_f32_16x16x32_bf16 v[80:83], a[204:207], a[172:175], v[80:83]\n" \
"v_exp_f32 v38, v38\n" \
"v_mfma_f32_16x16x32_bf16 v[88:91], a[200:203], a[184:187], v[236:239]\n" \
"v_add_f32 v240, v240, v38\n" \
"v_cvt_pk_bf16_f32 v34, v36, v37\n" \
"v_mfma_f32_16x16x32_bf16 v[88:91], a[204:207], a[188:191], v[88:91]\n" \
"v_exp_f32 v39, v39\n" \
"v_mfma_f32_16x16x32_bf16 v[88:91], a[192:195], a[176:179], v[88:91]\n" \
"v_add_f32 v240, v240, v39\n" \
"v_mfma_f32_16x16x32_bf16 v[88:91], a[196:199], a[180:183], v[88:91]\n" \
"ds_read_b128 a[192:195], v247 offset:0\n" \
"global_load_lds_dwordx4 v250, s[38:39] offset:1024 sc1\n" \
"v_mfma_f32_16x16x32_bf16 v[92:95], a[208:211], a[176:179], v[236:239]\n" \
"v_exp_f32 v40, v40\n" \
"v_mfma_f32_16x16x32_bf16 v[92:95], a[212:215], a[180:183], v[92:95]\n" \
"v_add_f32 v241, v241, v40\n" \
"v_cvt_pk_bf16_f32 v35, v38, v39\n" \
"v_mfma_f32_16x16x32_bf16 v[92:95], a[216:219], a[184:187], v[92:95]\n" \
"v_exp_f32 v41, v41\n" \
"v_mfma_f32_16x16x32_bf16 v[92:95], a[220:223], a[188:191], v[92:95]\n" \
"ds_read_b128 a[196:199], v247 offset:8192\n" \
"v_add_f32 v241, v241, v41\n" \
"v_mfma_f32_16x16x32_bf16 v[84:87], a[216:219], a[168:171], v[232:235]\n" \
"v_exp_f32 v42, v42\n" \
"v_mfma_f32_16x16x32_bf16 v[84:87], a[220:223], a[172:175], v[84:87]\n" \
"v_add_f32 v241, v241, v42\n" \
"v_cvt_pk_bf16_f32 v40, v40, v41\n" \
"v_mfma_f32_16x16x32_bf16 v[84:87], a[208:211], a[160:163], v[84:87]\n" \
"v_exp_f32 v43, v43\n" \
"v_mfma_f32_16x16x32_bf16 v[84:87], a[212:215], a[164:167], v[84:87]\n" \
"ds_read_b128 a[200:203], v247 offset:1024\n" \
"v_add_f32 v241, v241, v43\n" \
"v_mfma_f32_16x16x32_bf16 v[76:79], a[208:211], a[144:147], v[228:231]\n" \
"v_exp_f32 v44, v44\n" \
"v_mfma_f32_16x16x32_bf16 v[76:79], a[212:215], a[148:151], v[76:79]\n" \
"v_add_f32 v241, v241, v44\n" \
"v_cvt_pk_bf16_f32 v41, v42, v43\n" \
"v_mfma_f32_16x16x32_bf16 v[76:79], a[216:219], a[152:155], v[76:79]\n" \
"v_exp_f32 v45, v45\n" \
"v_mfma_f32_16x16x32_bf16 v[76:79], a[220:223], a[156:159], v[76:79]\n" \
"ds_read_b128 a[204:207], v247 offset:9216\n" \
"global_load_lds_dwordx4 v250, s[38:39] offset:2048 sc1\n" \
"v_add_f32 v241, v241, v45\n" \
"v_mfma_f32_16x16x32_bf16 v[68:71], a[216:219], a[136:139], v[224:227]\n" \
"v_exp_f32 v46, v46\n" \
"v_mfma_f32_16x16x32_bf16 v[68:71], a[220:223], a[140:143], v[68:71]\n" \
"v_add_f32 v241, v241, v46\n" \
"v_cvt_pk_bf16_f32 v42, v44, v45\n" \
"v_mfma_f32_16x16x32_bf16 v[68:71], a[208:211], a[128:131], v[68:71]\n" \
"s_waitcnt lgkmcnt(4)\n" \
"v_exp_f32 v47, v47\n" \
"v_mfma_f32_16x16x32_bf16 v[68:71], a[212:215], a[132:135], v[68:71]\n" \
"ds_read_b128 a[208:211], v247 offset:2048\n" \
"v_add_f32 v241, v241, v47\n" \
"v_mfma_f32_16x16x32_bf16 v[96:99], a[224:227], a[128:131], v[224:227]\n" \
"v_exp_f32 v48, v48\n" \
"v_mfma_f32_16x16x32_bf16 v[96:99], a[228:231], a[132:135], v[96:99]\n" \
"v_add_f32 v242, v242, v48\n" \
"v_cvt_pk_bf16_f32 v43, v46, v47\n" \
"v_mfma_f32_16x16x32_bf16 v[96:99], a[232:235], a[136:139], v[96:99]\n" \
"v_exp_f32 v49, v49\n" \
"v_mfma_f32_16x16x32_bf16 v[96:99], a[236:239], a[140:143], v[96:99]\n" \
"ds_read_b128 a[212:215], v247 offset:10240\n" \
"v_add_f32 v242, v242, v49\n" \
"v_mfma_f32_16x16x32_bf16 v[104:107], a[232:235], a[152:155], v[228:231]\n" \
"v_exp_f32 v50, v50\n" \
"v_mfma_f32_16x16x32_bf16 v[104:107], a[236:239], a[156:159], v[104:107]\n" \
"v_add_f32 v242, v242, v50\n" \
"v_cvt_pk_bf16_f32 v48, v48, v49\n" \
"v_mfma_f32_16x16x32_bf16 v[104:107], a[224:227], a[144:147], v[104:107]\n" \
"v_exp_f32 v51, v51\n" \
"v_mfma_f32_16x16x32_bf16 v[104:107], a[228:231], a[148:151], v[104:107]\n" \
"ds_read_b128 a[216:219], v247 offset:3072\n" \
"global_load_lds_dwordx4 v250, s[38:39] offset:3072 sc1\n" \
"v_add_f32 v242, v242, v51\n" \
"v_exp_f32 v52, v52\n" \
"v_mfma_f32_16x16x32_bf16 v[112:115], a[224:227], a[160:163], v[232:235]\n" \
"s_add_u32 s38, s38, 17408\n" \
"v_add_f32 v242, v242, v52\n" \
"v_cvt_pk_bf16_f32 v49, v50, v51\n" \
"v_mfma_f32_16x16x32_bf16 v[112:115], a[228:231], a[164:167], v[112:115]\n" \
"v_exp_f32 v53, v53\n" \
"v_mfma_f32_16x16x32_bf16 v[112:115], a[232:235], a[168:171], v[112:115]\n" \
"v_add_f32 v242, v242, v53\n" \
"v_exp_f32 v54, v54\n" \
"v_mfma_f32_16x16x32_bf16 v[112:115], a[236:239], a[172:175], v[112:115]\n" \
"ds_read_b128 a[220:223], v247 offset:11264\n" \
"v_add_f32 v242, v242, v54\n" \
"v_cvt_pk_bf16_f32 v50, v52, v53\n" \
"v_mfma_f32_16x16x32_bf16 v[120:123], a[232:235], a[184:187], v[236:239]\n" \
"v_exp_f32 v55, v55\n" \
"v_mfma_f32_16x16x32_bf16 v[120:123], a[236:239], a[188:191], v[120:123]\n" \
"global_load_dword v253, v252, s[40:41]\n" \
"v_mfma_f32_16x16x32_bf16 v[120:123], a[224:227], a[176:179], v[120:123]\n" \
"s_sub_u32 s80, s80, 1\n" \
"s_cmp_gt_i32 s80, 0\n" \
"s_cselect_b32 s81, 0x80000, 0\n" \
"v_mfma_f32_16x16x32_bf16 v[120:123], a[228:231], a[180:183], v[120:123]\n" \
"s_add_u32 s40, s40, s81\n" \
"ds_read_b128 a[224:227], v247 offset:4096\n" \
"v_add_f32 v242, v242, v55\n" \
"s_addc_u32 s41, s41, 0\n" \
"v_mfma_f32_16x16x32_bf16 v[124:127], a[240:243], a[176:179], v[236:239]\n" \
"v_exp_f32 v56, v56\n" \
"v_mfma_f32_16x16x32_bf16 v[124:127], a[244:247], a[180:183], v[124:127]\n" \
"v_add_f32 v243, v243, v56\n" \
"v_cvt_pk_bf16_f32 v51, v54, v55\n" \
"v_mfma_f32_16x16x32_bf16 v[124:127], a[248:251], a[184:187], v[124:127]\n" \
"v_exp_f32 v57, v57\n" \
"v_mfma_f32_16x16x32_bf16 v[124:127], a[252:255], a[188:191], v[124:127]\n" \
"ds_read_b128 a[228:231], v247 offset:12288\n" \
"v_add_f32 v243, v243, v57\n" \
"v_mfma_f32_16x16x32_bf16 v[116:119], a[248:251], a[168:171], v[232:235]\n" \
"v_exp_f32 v58, v58\n" \
"v_mfma_f32_16x16x32_bf16 v[116:119], a[252:255], a[172:175], v[116:119]\n" \
"v_add_f32 v243, v243, v58\n" \
"v_cvt_pk_bf16_f32 v56, v56, v57\n" \
"v_mfma_f32_16x16x32_bf16 v[116:119], a[240:243], a[160:163], v[116:119]\n" \
"v_exp_f32 v59, v59\n" \
"v_mfma_f32_16x16x32_bf16 v[116:119], a[244:247], a[164:167], v[116:119]\n" \
"ds_read_b128 a[232:235], v247 offset:5120\n" \
"v_add_f32 v243, v243, v59\n" \
"v_mfma_f32_16x16x32_bf16 v[108:111], a[240:243], a[144:147], v[228:231]\n" \
"v_exp_f32 v60, v60\n" \
"v_mfma_f32_16x16x32_bf16 v[108:111], a[244:247], a[148:151], v[108:111]\n" \
"v_add_f32 v243, v243, v60\n" \
"v_cvt_pk_bf16_f32 v57, v58, v59\n" \
"v_mfma_f32_16x16x32_bf16 v[108:111], a[248:251], a[152:155], v[108:111]\n" \
"v_exp_f32 v61, v61\n" \
"v_mfma_f32_16x16x32_bf16 v[108:111], a[252:255], a[156:159], v[108:111]\n" \
"ds_read_b128 a[236:239], v247 offset:13312\n" \
"v_add_f32 v243, v243, v61\n" \
"v_mfma_f32_16x16x32_bf16 v[100:103], a[248:251], a[136:139], v[224:227]\n" \
"v_exp_f32 v62, v62\n" \
"v_mfma_f32_16x16x32_bf16 v[100:103], a[252:255], a[140:143], v[100:103]\n" \
"v_add_f32 v243, v243, v62\n" \
"v_cvt_pk_bf16_f32 v58, v60, v61\n" \
"v_mfma_f32_16x16x32_bf16 v[100:103], a[240:243], a[128:131], v[100:103]\n" \
"s_waitcnt vmcnt(10) lgkmcnt(4)\n" \
"v_exp_f32 v63, v63\n" \
"v_mfma_f32_16x16x32_bf16 v[100:103], a[244:247], a[132:135], v[100:103]\n" \
"s_barrier\n" \
"ds_read_b128 a[240:243], v247 offset:6144\n" \
"v_add_f32 v243, v243, v63\n" \
"v_mfma_f32_16x16x32_bf16 a[0:3], a[192:195], v[0:3], a[0:3]\n" \
"v_exp_f32 v64, v64\n" \
"v_mfma_f32_16x16x32_bf16 a[0:3], a[196:199], v[32:35], a[0:3]\n" \
"v_cvt_pk_bf16_f32 v59, v62, v63\n" \
"v_add_f32 v240, v240, v64\n" \
"v_exp_f32 v65, v65\n" \
"v_mfma_f32_16x16x32_bf16 a[32:35], a[192:195], v[8:11], a[32:35]\n" \
"s_mov_b32 m0, s73\n" \
"ds_read_b128 a[244:247], v247 offset:14336\n" \
"v_add_f32 v240, v240, v65\n" \
"v_mfma_f32_16x16x32_bf16 a[32:35], a[196:199], v[40:43], a[32:35]\n" \
"ds_read_b128 a[248:251], v247 offset:7168\n" \
"global_load_lds_dwordx4 v250, s[36:37] offset:0 sc1\n" \
"v_mfma_f32_16x16x32_bf16 v[128:131], a[192:195], v[16:19], v[128:131]\n" \
"ds_read_b128 a[252:255], v247 offset:15360\n" \
"v_exp_f32 v66, v66\n" \
"v_mfma_f32_16x16x32_bf16 v[128:131], a[196:199], v[48:51], v[128:131]\n" \
"v_add_f32 v240, v240, v66\n" \
"v_cvt_pk_bf16_f32 v64, v64, v65\n" \
"v_mfma_f32_16x16x32_bf16 v[160:163], a[192:195], v[24:27], v[160:163]\n" \
"v_exp_f32 v67, v67\n" \
"v_mfma_f32_16x16x32_bf16 v[160:163], a[196:199], v[56:59], v[160:163]\n" \
"ds_read_b128 a[192:195], v246 offset:0\n" \
"v_add_f32 v240, v240, v67\n" \
"v_mfma_f32_16x16x32_bf16 v[164:167], a[200:203], v[24:27], v[164:167]\n" \
"v_exp_f32 v68, v68\n" \
"v_mfma_f32_16x16x32_bf16 v[164:167], a[204:207], v[56:59], v[164:167]\n" \
"v_add_f32 v240, v240, v68\n" \
"v_cvt_pk_bf16_f32 v65, v66, v67\n" \
"v_mfma_f32_16x16x32_bf16 v[132:135], a[200:203], v[16:19], v[132:135]\n" \
"v_exp_f32 v69, v69\n" \
"v_mfma_f32_16x16x32_bf16 v[132:135], a[204:207], v[48:51], v[132:135]\n" \
"ds_read_b128 a[196:199], v246 offset:2048\n" \
"v_add_f32 v240, v240, v69\n" \
"v_mfma_f32_16x16x32_bf16 a[36:39], a[200:203], v[8:11], a[36:39]\n" \
"v_exp_f32 v70, v70\n" \
"v_mfma_f32_16x16x32_bf16 a[36:39], a[204:207], v[40:43], a[36:39]\n" \
"v_add_f32 v240, v240, v70\n" \
"v_cvt_pk_bf16_f32 v66, v68, v69\n" \
"v_mfma_f32_16x16x32_bf16 a[4:7], a[200:203], v[0:3], a[4:7]\n" \
"v_exp_f32 v71, v71\n" \
"v_mfma_f32_16x16x32_bf16 a[4:7], a[204:207], v[32:35], a[4:7]\n" \
"ds_read_b128 a[200:203], v246 offset:4096\n" \
"global_load_lds_dwordx4 v250, s[36:37] offset:1024 sc1\n" \
"v_add_f32 v240, v240, v71\n" \
"v_mfma_f32_16x16x32_bf16 a[8:11], a[208:211], v[0:3], a[8:11]\n" \
"v_exp_f32 v72, v72\n" \
"v_mfma_f32_16x16x32_bf16 a[8:11], a[212:215], v[32:35], a[8:11]\n" \
"v_add_f32 v241, v241, v72\n" \
"v_cvt_pk_bf16_f32 v67, v70, v71\n" \
"v_mfma_f32_16x16x32_bf16 a[40:43], a[208:211], v[8:11], a[40:43]\n" \
"v_exp_f32 v73, v73\n" \
"v_mfma_f32_16x16x32_bf16 a[40:43], a[212:215], v[40:43], a[40:43]\n" \
"ds_read_b128 a[204:207], v246 offset:6144\n" \
"v_add_f32 v241, v241, v73\n" \
"v_mfma_f32_16x16x32_bf16 v[136:139], a[208:211], v[16:19], v[136:139]\n" \
"v_exp_f32 v74, v74\n" \
"v_mfma_f32_16x16x32_bf16 v[136:139], a[212:215], v[48:51], v[136:139]\n" \
"v_add_f32 v241, v241, v74\n" \
"v_cvt_pk_bf16_f32 v72, v72, v73\n" \
"v_mfma_f32_16x16x32_bf16 v[168:171], a[208:211], v[24:27], v[168:171]\n" \
"v_exp_f32 v75, v75\n" \
"v_mfma_f32_16x16x32_bf16 v[168:171], a[212:215], v[56:59], v[168:171]\n" \
"ds_read_b128 a[208:211], v246 offset:256\n" \
"v_add_f32 v241, v241, v75\n" \
"v_mfma_f32_16x16x32_bf16 v[172:175], a[216:219], v[24:27], v[172:175]\n" \
"v_exp_f32 v76, v76\n" \
"v_mfma_f32_16x16x32_bf16 v[172:175], a[220:223], v[56:59], v[172:175]\n" \
"v_add_f32 v241, v241, v76\n" \
"v_cvt_pk_bf16_f32 v73, v74, v75\n" \
"v_mfma_f32_16x16x32_bf16 v[140:143], a[216:219], v[16:19], v[140:143]\n" \
"v_exp_f32 v77, v77\n" \
"v_mfma_f32_16x16x32_bf16 v[140:143], a[220:223], v[48:51], v[140:143]\n" \
"ds_read_b128 a[212:215], v246 offset:2304\n" \
"global_load_lds_dwordx4 v250, s[36:37] offset:2048 sc1\n" \
"v_add_f32 v241, v241, v77\n" \
"v_mfma_f32_16x16x32_bf16 a[44:47], a[216:219], v[8:11], a[44:47]\n" \
"v_exp_f32 v78, v78\n" \
"v_mfma_f32_16x16x32_bf16 a[44:47], a[220:223], v[40:43], a[44:47]\n" \
"v_add_f32 v241, v241, v78\n" \
"v_cvt_pk_bf16_f32 v74, v76, v77\n" \
"v_mfma_f32_16x16x32_bf16 a[12:15], a[216:219], v[0:3], a[12:15]\n" \
"s_waitcnt lgkmcnt(6)\n" \
"v_exp_f32 v79, v79\n" \
"v_mfma_f32_16x16x32_bf16 a[12:15], a[220:223], v[32:35], a[12:15]\n" \
"ds_read_b128 a[216:219], v246 offset:4352\n" \
"v_add_f32 v241, v241, v79\n" \
"v_mfma_f32_16x16x32_bf16 a[16:19], a[224:227], v[0:3], a[16:19]\n" \
"v_exp_f32 v80, v80\n" \
"v_mfma_f32_16x16x32_bf16 a[16:19], a[228:231], v[32:35], a[16:19]\n" \
"v_add_f32 v242, v242, v80\n" \
"v_cvt_pk_bf16_f32 v75, v78, v79\n" \
"v_mfma_f32_16x16x32_bf16 a[48:51], a[224:227], v[8:11], a[48:51]\n" \
"v_exp_f32 v81, v81\n" \
"v_mfma_f32_16x16x32_bf16 a[48:51], a[228:231], v[40:43], a[48:51]\n" \
"ds_read_b128 a[220:223], v246 offset:6400\n" \
"v_add_f32 v242, v242, v81\n" \
"v_mfma_f32_16x16x32_bf16 v[144:147], a[224:227], v[16:19], v[144:147]\n" \
"v_exp_f32 v82, v82\n" \
"v_mfma_f32_16x16x32_bf16 v[144:147], a[228:231], v[48:51], v[144:147]\n" \
"v_add_f32 v242, v242, v82\n" \
"v_cvt_pk_bf16_f32 v80, v80, v81\n" \
"v_mfma_f32_16x16x32_bf16 v[176:179], a[224:227], v[24:27], v[176:179]\n" \
"v_exp_f32 v83, v83\n" \
"v_mfma_f32_16x16x32_bf16 v[176:179], a[228:231], v[56:59], v[176:179]\n" \
"ds_read_b128 a[224:227], v246 offset:8192\n" \
"global_load_lds_dwordx4 v250, s[36:37] offset:3072 sc1\n" \
"v_add_f32 v242, v242, v83\n" \
"v_mfma_f32_16x16x32_bf16 v[180:183], a[232:235], v[24:27], v[180:183]\n" \
"s_add_u32 s36, s36, 17408\n" \
"v_exp_f32 v84, v84\n" \
"v_mfma_f32_16x16x32_bf16 v[180:183], a[236:239], v[56:59], v[180:183]\n" \
"v_add_f32 v242, v242, v84\n" \
"v_cvt_pk_bf16_f32 v81, v82, v83\n" \
"v_mfma_f32_16x16x32_bf16 v[148:151], a[232:235], v[16:19], v[148:151]\n" \
"v_exp_f32 v85, v85\n" \
"v_mfma_f32_16x16x32_bf16 v[148:151], a[236:239], v[48:51], v[148:151]\n" \
"ds_read_b128 a[228:231], v246 offset:10240\n" \
"v_add_f32 v242, v242, v85\n" \
"v_mfma_f32_16x16x32_bf16 a[52:55], a[232:235], v[8:11], a[52:55]\n" \
"v_exp_f32 v86, v86\n" \
"v_mfma_f32_16x16x32_bf16 a[52:55], a[236:239], v[40:43], a[52:55]\n" \
"v_add_f32 v242, v242, v86\n" \
"v_cvt_pk_bf16_f32 v82, v84, v85\n" \
"v_mfma_f32_16x16x32_bf16 a[20:23], a[232:235], v[0:3], a[20:23]\n" \
"v_exp_f32 v87, v87\n" \
"v_mfma_f32_16x16x32_bf16 a[20:23], a[236:239], v[32:35], a[20:23]\n" \
"ds_read_b128 a[232:235], v246 offset:12288\n" \
"v_add_f32 v242, v242, v87\n" \
"v_mfma_f32_16x16x32_bf16 a[24:27], a[240:243], v[0:3], a[24:27]\n" \
"v_exp_f32 v88, v88\n" \
"v_mfma_f32_16x16x32_bf16 a[24:27], a[244:247], v[32:35], a[24:27]\n" \
"v_add_f32 v243, v243, v88\n" \
"v_cvt_pk_bf16_f32 v83, v86, v87\n" \
"v_mfma_f32_16x16x32_bf16 a[56:59], a[240:243], v[8:11], a[56:59]\n" \
"v_exp_f32 v89, v89\n" \
"v_mfma_f32_16x16x32_bf16 a[56:59], a[244:247], v[40:43], a[56:59]\n" \
"ds_read_b128 a[236:239], v246 offset:14336\n" \
"v_add_f32 v243, v243, v89\n" \
"v_mfma_f32_16x16x32_bf16 v[152:155], a[240:243], v[16:19], v[152:155]\n" \
"v_exp_f32 v90, v90\n" \
"v_mfma_f32_16x16x32_bf16 v[152:155], a[244:247], v[48:51], v[152:155]\n" \
"v_add_f32 v243, v243, v90\n" \
"v_cvt_pk_bf16_f32 v88, v88, v89\n" \
"v_mfma_f32_16x16x32_bf16 v[184:187], a[240:243], v[24:27], v[184:187]\n" \
"v_exp_f32 v91, v91\n" \
"v_mfma_f32_16x16x32_bf16 v[184:187], a[244:247], v[56:59], v[184:187]\n" \
"ds_read_b128 a[240:243], v246 offset:8448\n" \
"v_add_f32 v243, v243, v91\n" \
"v_mfma_f32_16x16x32_bf16 v[188:191], a[248:251], v[24:27], v[188:191]\n" \
"v_exp_f32 v92, v92\n" \
"v_mfma_f32_16x16x32_bf16 v[188:191], a[252:255], v[56:59], v[188:191]\n" \
"v_add_f32 v243, v243, v92\n" \
"v_cvt_pk_bf16_f32 v89, v90, v91\n" \
"v_mfma_f32_16x16x32_bf16 v[156:159], a[248:251], v[16:19], v[156:159]\n" \
"v_exp_f32 v93, v93\n" \
"v_mfma_f32_16x16x32_bf16 v[156:159], a[252:255], v[48:51], v[156:159]\n" \
"ds_read_b128 a[244:247], v246 offset:10496\n" \
"v_add_f32 v243, v243, v93\n" \
"v_mfma_f32_16x16x32_bf16 a[60:63], a[248:251], v[8:11], a[60:63]\n" \
"v_exp_f32 v94, v94\n" \
"v_mfma_f32_16x16x32_bf16 a[60:63], a[252:255], v[40:43], a[60:63]\n" \
"v_add_f32 v243, v243, v94\n" \
"v_cvt_pk_bf16_f32 v90, v92, v93\n" \
"v_mfma_f32_16x16x32_bf16 a[28:31], a[248:251], v[0:3], a[28:31]\n" \
"s_waitcnt lgkmcnt(6)\n" \
"v_exp_f32 v95, v95\n" \
"v_mfma_f32_16x16x32_bf16 a[28:31], a[252:255], v[32:35], a[28:31]\n" \
"ds_read_b128 a[248:251], v246 offset:12544\n" \
"v_add_f32 v243, v243, v95\n" \
"v_mfma_f32_16x16x32_bf16 v[0:3], a[192:195], a[128:131], v[224:227]\n" \
"v_exp_f32 v96, v96\n" \
"v_mfma_f32_16x16x32_bf16 v[0:3], a[196:199], a[132:135], v[0:3]\n" \
"v_cvt_pk_bf16_f32 v91, v94, v95\n" \
"v_add_f32 v240, v240, v96\n" \
"v_mfma_f32_16x16x32_bf16 v[0:3], a[200:203], a[136:139], v[0:3]\n" \
"s_add_u32 m0, s74, 0x4000\n" \
"v_exp_f32 v97, v97\n" \
"v_mfma_f32_16x16x32_bf16 v[0:3], a[204:207], a[140:143], v[0:3]\n" \
"ds_read_b128 a[252:255], v246 offset:14592\n" \
"global_load_lds_dwordx4 v250, s[38:39] offset:0 sc1\n" \
"v_add_f32 v240, v240, v97\n" \
"v_mfma_f32_16x16x32_bf16 v[8:11], a[200:203], a[152:155], v[228:231]\n" \
"v_exp_f32 v98, v98\n" \
"v_mfma_f32_16x16x32_bf16 v[8:11], a[204:207], a[156:159], v[8:11]\n" \
"v_add_f32 v240, v240, v98\n" \
"v_cvt_pk_bf16_f32 v96, v96, v97\n" \
"v_mfma_f32_16x16x32_bf16 v[8:11], a[192:195], a[144:147], v[8:11]\n" \
"v_exp_f32 v99, v99\n" \
"v_mfma_f32_16x16x32_bf16 v[8:11], a[196:199], a[148:151], v[8:11]\n" \
"v_add_f32 v240, v240, v99\n" \
"v_exp_f32 v100, v100\n" \
"v_mfma_f32_16x16x32_bf16 v[16:19], a[192:195], a[160:163], v[232:235]\n" \
"v_add_f32 v240, v240, v100\n" \
"v_cvt_pk_bf16_f32 v97, v98, v99\n" \
"v_mfma_f32_16x16x32_bf16 v[16:19], a[196:199], a[164:167], v[16:19]\n" \
"v_exp_f32 v101, v101\n" \
"v_mfma_f32_16x16x32_bf16 v[16:19], a[200:203], a[168:171], v[16:19]\n" \
"v_add_f32 v240, v240, v101\n" \
"v_exp_f32 v102, v102\n" \
"v_mfma_f32_16x16x32_bf16 v[16:19], a[204:207], a[172:175], v[16:19]\n" \
"v_add_f32 v240, v240, v102\n" \
"v_cvt_pk_bf16_f32 v98, v100, v101\n" \
"v_exp_f32 v103, v103\n" \
"v_mfma_f32_16x16x32_bf16 v[24:27], a[200:203], a[184:187], v[236:239]\n" \
"v_add_f32 v240, v240, v103\n" \
"v_exp_f32 v104, v104\n" \
"v_mfma_f32_16x16x32_bf16 v[24:27], a[204:207], a[188:191], v[24:27]\n" \
"v_add_f32 v241, v241, v104\n" \
"v_cvt_pk_bf16_f32 v99, v102, v103\n" \
"v_mfma_f32_16x16x32_bf16 v[24:27], a[192:195], a[176:179], v[24:27]\n" \
"v_exp_f32 v105, v105\n" \
"v_mfma_f32_16x16x32_bf16 v[24:27], a[196:199], a[180:183], v[24:27]\n" \
"ds_read_b128 a[192:195], v247 offset:16384\n" \
"global_load_lds_dwordx4 v250, s[38:39] offset:1024 sc1\n" \
"v_add_f32 v241, v241, v105\n" \
"v_mfma_f32_16x16x32_bf16 v[28:31], a[208:211], a[176:179], v[236:239]\n" \
"v_exp_f32 v106, v106\n" \
"v_mfma_f32_16x16x32_bf16 v[28:31], a[212:215], a[180:183], v[28:31]\n" \
"v_add_f32 v241, v241, v106\n" \
"v_cvt_pk_bf16_f32 v104, v104, v105\n" \
"v_mfma_f32_16x16x32_bf16 v[28:31], a[216:219], a[184:187], v[28:31]\n" \
"v_exp_f32 v107, v107\n" \
"v_mfma_f32_16x16x32_bf16 v[28:31], a[220:223], a[188:191], v[28:31]\n" \
"ds_read_b128 a[196:199], v247 offset:24576\n" \
"v_add_f32 v241, v241, v107\n" \
"v_mfma_f32_16x16x32_bf16 v[20:23], a[216:219], a[168:171], v[232:235]\n" \
"v_exp_f32 v108, v108\n" \
"v_mfma_f32_16x16x32_bf16 v[20:23], a[220:223], a[172:175], v[20:23]\n" \
"v_add_f32 v241, v241, v108\n" \
"v_cvt_pk_bf16_f32 v105, v106, v107\n" \
"v_mfma_f32_16x16x32_bf16 v[20:23], a[208:211], a[160:163], v[20:23]\n" \
"v_exp_f32 v109, v109\n" \
"v_mfma_f32_16x16x32_bf16 v[20:23], a[212:215], a[164:167], v[20:23]\n" \
"ds_read_b128 a[200:203], v247 offset:17408\n" \
"v_add_f32 v241, v241, v109\n" \
"v_mfma_f32_16x16x32_bf16 v[12:15], a[208:211], a[144:147], v[228:231]\n" \
"v_exp_f32 v110, v110\n" \
"v_mfma_f32_16x16x32_bf16 v[12:15], a[212:215], a[148:151], v[12:15]\n" \
"v_add_f32 v241, v241, v110\n" \
"v_cvt_pk_bf16_f32 v106, v108, v109\n" \
"v_mfma_f32_16x16x32_bf16 v[12:15], a[216:219], a[152:155], v[12:15]\n" \
"v_exp_f32 v111, v111\n" \
"v_mfma_f32_16x16x32_bf16 v[12:15], a[220:223], a[156:159], v[12:15]\n" \
"ds_read_b128 a[204:207], v247 offset:25600\n" \
"global_load_lds_dwordx4 v250, s[38:39] offset:2048 sc1\n" \
"v_add_f32 v241, v241, v111\n" \
"v_mfma_f32_16x16x32_bf16 v[4:7], a[216:219], a[136:139], v[224:227]\n" \
"v_exp_f32 v112, v112\n" \
"v_mfma_f32_16x16x32_bf16 v[4:7], a[220:223], a[140:143], v[4:7]\n" \
"v_add_f32 v242, v242, v112\n" \
"v_cvt_pk_bf16_f32 v107, v110, v111\n" \
"v_mfma_f32_16x16x32_bf16 v[4:7], a[208:211], a[128:131], v[4:7]\n" \
"s_waitcnt lgkmcnt(4)\n" \
"v_exp_f32 v113, v113\n" \
"v_mfma_f32_16x16x32_bf16 v[4:7], a[212:215], a[132:135], v[4:7]\n" \
"ds_read_b128 a[208:211], v247 offset:18432\n" \
"v_add_f32 v242, v242, v113\n" \
"v_mfma_f32_16x16x32_bf16 v[32:35], a[224:227], a[128:131], v[224:227]\n" \
"v_exp_f32 v114, v114\n" \
"v_mfma_f32_16x16x32_bf16 v[32:35], a[228:231], a[132:135], v[32:35]\n" \
"v_add_f32 v242, v242, v114\n" \
"v_cvt_pk_bf16_f32 v112, v112, v113\n" \
"v_mfma_f32_16x16x32_bf16 v[32:35], a[232:235], a[136:139], v[32:35]\n" \
"v_exp_f32 v115, v115\n" \
"v_mfma_f32_16x16x32_bf16 v[32:35], a[236:239], a[140:143], v[32:35]\n" \
"ds_read_b128 a[212:215], v247 offset:26624\n" \
"v_add_f32 v242, v242, v115\n" \
"v_mfma_f32_16x16x32_bf16 v[40:43], a[232:235], a[152:155], v[228:231]\n" \
"v_exp_f32 v116, v116\n" \
"v_mfma_f32_16x16x32_bf16 v[40:43], a[236:239], a[156:159], v[40:43]\n" \
"v_add_f32 v242, v242, v116\n" \
"v_cvt_pk_bf16_f32 v113, v114, v115\n" \
"v_mfma_f32_16x16x32_bf16 v[40:43], a[224:227], a[144:147], v[40:43]\n" \
"v_exp_f32 v117, v117\n" \
"v_mfma_f32_16x16x32_bf16 v[40:43], a[228:231], a[148:151], v[40:43]\n" \
"ds_read_b128 a[216:219], v247 offset:19456\n" \
"global_load_lds_dwordx4 v250, s[38:39] offset:3072 sc1\n" \
"v_add_f32 v242, v242, v117\n" \
"v_mfma_f32_16x16x32_bf16 v[48:51], a[224:227], a[160:163], v[232:235]\n" \
"s_add_u32 s38, s38, 17408\n" \
"v_exp_f32 v118, v118\n" \
"v_mfma_f32_16x16x32_bf16 v[48:51], a[228:231], a[164:167], v[48:51]\n" \
"v_add_f32 v242, v242, v118\n" \
"v_cvt_pk_bf16_f32 v114, v116, v117\n" \
"v_mfma_f32_16x16x32_bf16 v[48:51], a[232:235], a[168:171], v[48:51]\n" \
"v_exp_f32 v119, v119\n" \
"v_mfma_f32_16x16x32_bf16 v[48:51], a[236:239], a[172:175], v[48:51]\n" \
"ds_read_b128 a[220:223], v247 offset:27648\n" \
"v_add_f32 v242, v242, v119\n" \
"v_mfma_f32_16x16x32_bf16 v[56:59], a[232:235], a[184:187], v[236:239]\n" \
"global_load_dword v253, v252, s[40:41]\n" \
"v_mfma_f32_16x16x32_bf16 v[56:59], a[236:239], a[188:191], v[56:59]\n" \
"s_sub_u32 s80, s80, 1\n" \
"s_cmp_gt_i32 s80, 0\n" \
"s_cselect_b32 s81, 0x80000, 0\n" \
"v_mfma_f32_16x16x32_bf16 v[56:59], a[224:227], a[176:179], v[56:59]\n" \
"s_add_u32 s40, s40, s81\n" \
"s_addc_u32 s41, s41, 0\n" \
"s_load_dwordx8 s[44:51], s[56:57], s79 glc\n" \
"v_mfma_f32_16x16x32_bf16 v[56:59], a[228:231], a[180:183], v[56:59]\n" \
"s_xor_b32 s74, s74, 0x8000\n" \
"ds_read_b128 a[224:227], v247 offset:20480\n" \
"s_sub_u32 s75, s75, 1\n" \
"v_mfma_f32_16x16x32_bf16 v[60:63], a[240:243], a[176:179], v[236:239]\n" \
"s_cmp_lg_u32 s75, 0\n" \
"v_exp_f32 v120, v120\n" \
"v_mfma_f32_16x16x32_bf16 v[60:63], a[244:247], a[180:183], v[60:63]\n" \
"v_add_f32 v243, v243, v120\n" \
"v_cvt_pk_bf16_f32 v115, v118, v119\n" \
"v_mfma_f32_16x16x32_bf16 v[60:63], a[248:251], a[184:187], v[60:63]\n" \
"v_exp_f32 v121, v121\n" \
"v_mfma_f32_16x16x32_bf16 v[60:63], a[252:255], a[188:191], v[60:63]\n" \
"ds_read_b128 a[228:231], v247 offset:28672\n" \
"v_add_f32 v243, v243, v121\n" \
"v_mfma_f32_16x16x32_bf16 v[52:55], a[248:251], a[168:171], v[232:235]\n" \
"v_exp_f32 v122, v122\n" \
"v_mfma_f32_16x16x32_bf16 v[52:55], a[252:255], a[172:175], v[52:55]\n" \
"v_add_f32 v243, v243, v122\n" \
"v_cvt_pk_bf16_f32 v120, v120, v121\n" \
"v_mfma_f32_16x16x32_bf16 v[52:55], a[240:243], a[160:163], v[52:55]\n" \
"v_exp_f32 v123, v123\n" \
"v_mfma_f32_16x16x32_bf16 v[52:55], a[244:247], a[164:167], v[52:55]\n" \
"ds_read_b128 a[232:235], v247 offset:21504\n" \
"v_add_f32 v243, v243, v123\n" \
"v_mfma_f32_16x16x32_bf16 v[44:47], a[240:243], a[144:147], v[228:231]\n" \
"v_exp_f32 v124, v124\n" \
"v_mfma_f32_16x16x32_bf16 v[44:47], a[244:247], a[148:151], v[44:47]\n" \
"v_add_f32 v243, v243, v124\n" \
"v_cvt_pk_bf16_f32 v121, v122, v123\n" \
"v_mfma_f32_16x16x32_bf16 v[44:47], a[248:251], a[152:155], v[44:47]\n" \
"v_exp_f32 v125, v125\n" \
"v_mfma_f32_16x16x32_bf16 v[44:47], a[252:255], a[156:159], v[44:47]\n" \
"ds_read_b128 a[236:239], v247 offset:29696\n" \
"v_add_f32 v243, v243, v125\n" \
"v_mfma_f32_16x16x32_bf16 v[36:39], a[248:251], a[136:139], v[224:227]\n" \
"v_exp_f32 v126, v126\n" \
"v_mfma_f32_16x16x32_bf16 v[36:39], a[252:255], a[140:143], v[36:39]\n" \
"v_add_f32 v243, v243, v126\n" \
"v_cvt_pk_bf16_f32 v122, v124, v125\n" \
"v_mfma_f32_16x16x32_bf16 v[36:39], a[240:243], a[128:131], v[36:39]\n" \
"s_waitcnt vmcnt(10) lgkmcnt(4)\n" \
"v_exp_f32 v127, v127\n" \
"v_mfma_f32_16x16x32_bf16 v[36:39], a[244:247], a[132:135], v[36:39]\n" \
"s_barrier\n" \
"ds_read_b128 a[240:243], v247 offset:22528\n" \
"v_add_f32 v243, v243, v127\n" \
"s_cbranch_scc1 Lloop_%=\n" \
"v_mfma_f32_16x16x32_bf16 a[0:3], a[192:195], v[64:67], a[0:3]\n" \
"v_exp_f32 v8, v8\n" \
"v_mfma_f32_16x16x32_bf16 a[0:3], a[196:199], v[96:99], a[0:3]\n" \
"v_cvt_pk_bf16_f32 v123, v126, v127\n" \
"v_exp_f32 v9, v9\n" \
"v_add_f32 v241, v241, v8\n" \
"v_exp_f32 v10, v10\n" \
"v_add_f32 v241, v241, v9\n" \
"v_cvt_pk_bf16_f32 v8, v8, v9\n" \
"v_exp_f32 v11, v11\n" \
"v_add_f32 v241, v241, v10\n" \
"v_exp_f32 v12, v12\n" \
"v_add_f32 v241, v241, v11\n" \
"v_mfma_f32_16x16x32_bf16 a[32:35], a[192:195], v[72:75], a[32:35]\n" \
"s_add_u32 m0, s73, 0x4000\n" \
"ds_read_b128 a[244:247], v247 offset:30720\n" \
"v_cvt_pk_bf16_f32 v9, v10, v11\n" \
"v_mfma_f32_16x16x32_bf16 a[32:35], a[196:199], v[104:107], a[32:35]\n" \
"ds_read_b128 a[248:251], v247 offset:23552\n" \
"global_load_lds_dwordx4 v250, s[36:37] offset:0 sc1\n" \
"v_exp_f32 v13, v13\n" \
"v_add_f32 v241, v241, v12\n" \
"v_exp_f32 v14, v14\n" \
"v_add_f32 v241, v241, v13\n" \
"v_cvt_pk_bf16_f32 v10, v12, v13\n" \
"v_exp_f32 v15, v15\n" \
"v_add_f32 v241, v241, v14\n" \
"v_exp_f32 v16, v16\n" \
"v_add_f32 v241, v241, v15\n" \
"v_mfma_f32_16x16x32_bf16 v[128:131], a[192:195], v[80:83], v[128:131]\n" \
"ds_read_b128 a[252:255], v247 offset:31744\n" \
"v_cvt_pk_bf16_f32 v11, v14, v15\n" \
"v_mfma_f32_16x16x32_bf16 v[128:131], a[196:199], v[112:115], v[128:131]\n" \
"v_xor_b32 v247, 0x8000, v247\n" \
"v_exp_f32 v17, v17\n" \
"v_add_f32 v242, v242, v16\n" \
"v_exp_f32 v18, v18\n" \
"v_add_f32 v242, v242, v17\n" \
"v_cvt_pk_bf16_f32 v16, v16, v17\n" \
"v_exp_f32 v19, v19\n" \
"v_add_f32 v242, v242, v18\n" \
"v_exp_f32 v20, v20\n" \
"v_add_f32 v242, v242, v19\n" \
"v_cvt_pk_bf16_f32 v17, v18, v19\n" \
"v_mfma_f32_16x16x32_bf16 v[160:163], a[192:195], v[88:91], v[160:163]\n" \
"v_exp_f32 v21, v21\n" \
"v_mfma_f32_16x16x32_bf16 v[160:163], a[196:199], v[120:123], v[160:163]\n" \
"ds_read_b128 a[192:195], v246 offset:16384\n" \
"v_add_f32 v242, v242, v20\n" \
"v_exp_f32 v22, v22\n" \
"v_mfma_f32_16x16x32_bf16 v[164:167], a[200:203], v[88:91], v[164:167]\n" \
"v_add_f32 v242, v242, v21\n" \
"v_cvt_pk_bf16_f32 v18, v20, v21\n" \
"v_mfma_f32_16x16x32_bf16 v[164:167], a[204:207], v[120:123], v[164:167]\n" \
"v_exp_f32 v23, v23\n" \
"v_add_f32 v242, v242, v22\n" \
"v_mfma_f32_16x16x32_bf16 v[132:135], a[200:203], v[80:83], v[132:135]\n" \
"v_add_f32 v242, v242, v23\n" \
"v_cvt_pk_bf16_f32 v19, v22, v23\n" \
"v_mfma_f32_16x16x32_bf16 v[132:135], a[204:207], v[112:115], v[132:135]\n" \
"ds_read_b128 a[196:199], v246 offset:18432\n" \
"v_cmp_gt_i32 vcc, 0, v251\n" \
"v_cmp_gt_i32 s[64:65], 1, v251\n" \
"v_cmp_gt_i32 s[66:67], 2, v251\n" \
"v_cndmask_b32 v0, v0, v254, vcc\n" \
"v_mfma_f32_16x16x32_bf16 a[36:39], a[200:203], v[72:75], a[36:39]\n" \
"v_cmp_gt_i32 vcc, 3, v251\n" \
"v_cndmask_b32 v1, v1, v254, s[64:65]\n" \
"v_mfma_f32_16x16x32_bf16 a[36:39], a[204:207], v[104:107], a[36:39]\n" \
"v_cmp_gt_i32 s[64:65], 16, v251\n" \
"v_cndmask_b32 v2, v2, v254, s[66:67]\n" \
"v_mfma_f32_16x16x32_bf16 a[4:7], a[200:203], v[64:67], a[4:7]\n" \
"v_cmp_gt_i32 s[66:67], 17, v251\n" \
"v_cndmask_b32 v3, v3, v254, vcc\n" \
"v_mfma_f32_16x16x32_bf16 a[4:7], a[204:207], v[96:99], a[4:7]\n" \
"ds_read_b128 a[200:203], v246 offset:20480\n" \
"global_load_lds_dwordx4 v250, s[36:37] offset:1024 sc1\n" \
"v_mfma_f32_16x16x32_bf16 a[8:11], a[208:211], v[64:67], a[8:11]\n" \
"v_cmp_gt_i32 vcc, 18, v251\n" \
"v_cndmask_b32 v4, v4, v254, s[64:65]\n" \
"v_mfma_f32_16x16x32_bf16 a[8:11], a[212:215], v[96:99], a[8:11]\n" \
"v_cmp_gt_i32 s[64:65], 19, v251\n" \
"v_cndmask_b32 v5, v5, v254, s[66:67]\n" \
"v_mfma_f32_16x16x32_bf16 a[40:43], a[208:211], v[72:75], a[40:43]\n" \
"v_cmp_gt_i32 s[66:67], 32, v251\n" \
"Lpg6193_%=:\n" \
"s_cbranch_execz Lpg7666_%=\n" \
"v_cndmask_b32 v6, v6, v254, vcc\n" \
"v_mfma_f32_16x16x32_bf16 a[40:43], a[212:215], v[104:107], a[40:43]\n" \
"ds_read_b128 a[204:207], v246 offset:22528\n" \
"v_cmp_gt_i32 vcc, 33, v251\n" \
"v_mfma_f32_16x16x32_bf16 v[136:139], a[208:211], v[80:83], v[136:139]\n" \
"v_cndmask_b32 v7, v7, v254, s[64:65]\n" \
"v_cmp_gt_i32 s[64:65], 34, v251\n" \
"v_mfma_f32_16x16x32_bf16 v[136:139], a[212:215], v[112:115], v[136:139]\n" \
"v_cndmask_b32 v32, v32, v254, s[66:67]\n" \
"v_cmp_gt_i32 s[66:67], 35, v251\n" \
"v_mfma_f32_16x16x32_bf16 v[168:171], a[208:211], v[88:91], v[168:171]\n" \
"v_cndmask_b32 v33, v33, v254, vcc\n" \
"v_cmp_gt_i32 vcc, 48, v251\n" \
"v_mfma_f32_16x16x32_bf16 v[168:171], a[212:215], v[120:123], v[168:171]\n" \
"ds_read_b128 a[208:211], v246 offset:16640\n" \
"v_cndmask_b32 v34, v34, v254, s[64:65]\n" \
"v_mfma_f32_16x16x32_bf16 v[172:175], a[216:219], v[88:91], v[172:175]\n" \
"v_cmp_gt_i32 s[64:65], 49, v251\n" \
"v_cndmask_b32 v35, v35, v254, s[66:67]\n" \
"v_mfma_f32_16x16x32_bf16 v[172:175], a[220:223], v[120:123], v[172:175]\n" \
"v_cmp_gt_i32 s[66:67], 50, v251\n" \
"v_cndmask_b32 v36, v36, v254, vcc\n" \
"v_mfma_f32_16x16x32_bf16 v[140:143], a[216:219], v[80:83], v[140:143]\n" \
"v_cmp_gt_i32 vcc, 51, v251\n" \
"v_cndmask_b32 v37, v37, v254, s[64:65]\n" \
"v_mfma_f32_16x16x32_bf16 v[140:143], a[220:223], v[112:115], v[140:143]\n" \
"ds_read_b128 a[212:215], v246 offset:18688\n" \
"global_load_lds_dwordx4 v250, s[36:37] offset:2048 sc1\n" \
"v_mfma_f32_16x16x32_bf16 a[44:47], a[216:219], v[72:75], a[44:47]\n" \
"v_cndmask_b32 v38, v38, v254, s[66:67]\n" \
"v_cndmask_b32 v39, v39, v254, vcc\n" \
"v_mfma_f32_16x16x32_bf16 a[44:47], a[220:223], v[104:107], a[44:47]\n" \
"v_exp_f32 v0, v0\n" \
"v_mfma_f32_16x16x32_bf16 a[12:15], a[216:219], v[64:67], a[12:15]\n" \
"s_waitcnt lgkmcnt(6)\n" \
"v_exp_f32 v1, v1\n" \
"v_mfma_f32_16x16x32_bf16 a[12:15], a[220:223], v[96:99], a[12:15]\n" \
"ds_read_b128 a[216:219], v246 offset:20736\n" \
"v_add_f32 v240, v240, v0\n" \
"v_mfma_f32_16x16x32_bf16 a[16:19], a[224:227], v[64:67], a[16:19]\n" \
"v_exp_f32 v2, v2\n" \
"v_mfma_f32_16x16x32_bf16 a[16:19], a[228:231], v[96:99], a[16:19]\n" \
"v_add_f32 v240, v240, v1\n" \
"v_cvt_pk_bf16_f32 v0, v0, v1\n" \
"v_mfma_f32_16x16x32_bf16 a[48:51], a[224:227], v[72:75], a[48:51]\n" \
"v_exp_f32 v3, v3\n" \
"v_mfma_f32_16x16x32_bf16 a[48:51], a[228:231], v[104:107], a[48:51]\n" \
"ds_read_b128 a[220:223], v246 offset:22784\n" \
"v_add_f32 v240, v240, v2\n" \
"v_exp_f32 v4, v4\n" \
"v_mfma_f32_16x16x32_bf16 v[144:147], a[224:227], v[80:83], v[144:147]\n" \
"v_add_f32 v240, v240, v3\n" \
"v_cvt_pk_bf16_f32 v1, v2, v3\n" \
"v_mfma_f32_16x16x32_bf16 v[144:147], a[228:231], v[112:115], v[144:147]\n" \
"global_load_lds_dwordx4 v250, s[36:37] offset:3072 sc1\n" \
"s_add_u32 s36, s36, 17408\n" \
"v_mfma_f32_16x16x32_bf16 v[176:179], a[224:227], v[88:91], v[176:179]\n" \
"s_xor_b32 s73, s73, 0x8000\n" \
"s_xor_b64 s[52:53], s[44:45], s[42:43]\n" \
"s_xor_b64 s[54:55], s[46:47], s[42:43]\n" \
"v_mfma_f32_16x16x32_bf16 v[176:179], a[228:231], v[120:123], v[176:179]\n" \
"s_or_b64 s[52:53], s[52:53], s[54:55]\n" \
"ds_read_b128 a[224:227], v246 offset:24576\n" \
"s_xor_b64 s[54:55], s[48:49], s[42:43]\n" \
"v_mfma_f32_16x16x32_bf16 v[180:183], a[232:235], v[88:91], v[180:183]\n" \
"s_or_b64 s[52:53], s[52:53], s[54:55]\n" \
"s_xor_b64 s[54:55], s[50:51], s[42:43]\n" \
"s_or_b64 s[52:53], s[52:53], s[54:55]\n" \
"v_mfma_f32_16x16x32_bf16 v[180:183], a[236:239], v[120:123], v[180:183]\n" \
"s_and_b64 s[52:53], s[52:53], s[8:9]\n" \
"s_cbranch_scc1 Lrs1_%=\n" \
"Lrr1_%=:\n" \
"v_mfma_f32_16x16x32_bf16 v[148:151], a[232:235], v[80:83], v[148:151]\n" \
"s_mov_b64 s[8:9], s[10:11]\n" \
"v_exp_f32 v5, v5\n" \
"v_mfma_f32_16x16x32_bf16 v[148:151], a[236:239], v[112:115], v[148:151]\n" \
"ds_read_b128 a[228:231], v246 offset:26624\n" \
"v_add_f32 v240, v240, v4\n" \
"v_mfma_f32_16x16x32_bf16 a[52:55], a[232:235], v[72:75], a[52:55]\n" \
"v_exp_f32 v6, v6\n" \
"v_mfma_f32_16x16x32_bf16 a[52:55], a[236:239], v[104:107], a[52:55]\n" \
"v_add_f32 v240, v240, v5\n" \
"v_cvt_pk_bf16_f32 v2, v4, v5\n" \
"v_mfma_f32_16x16x32_bf16 a[20:23], a[232:235], v[64:67], a[20:23]\n" \
"v_exp_f32 v7, v7\n" \
"v_mfma_f32_16x16x32_bf16 a[20:23], a[236:239], v[96:99], a[20:23]\n" \
"ds_read_b128 a[232:235], v246 offset:28672\n" \
"v_add_f32 v240, v240, v6\n" \
"v_mfma_f32_16x16x32_bf16 a[24:27], a[240:243], v[64:67], a[24:27]\n" \
"v_exp_f32 v24, v24\n" \
"v_mfma_f32_16x16x32_bf16 a[24:27], a[244:247], v[96:99], a[24:27]\n" \
"v_add_f32 v240, v240, v7\n" \
"v_cvt_pk_bf16_f32 v3, v6, v7\n" \
"v_mfma_f32_16x16x32_bf16 a[56:59], a[240:243], v[72:75], a[56:59]\n" \
"v_exp_f32 v25, v25\n" \
"v_mfma_f32_16x16x32_bf16 a[56:59], a[244:247], v[104:107], a[56:59]\n" \
"ds_read_b128 a[236:239], v246 offset:30720\n" \
"v_add_f32 v243, v243, v24\n" \
"v_mfma_f32_16x16x32_bf16 v[152:155], a[240:243], v[80:83], v[152:155]\n" \
"v_exp_f32 v26, v26\n" \
"v_mfma_f32_16x16x32_bf16 v[152:155], a[244:247], v[112:115], v[152:155]\n" \
"v_add_f32 v243, v243, v25\n" \
"v_cvt_pk_bf16_f32 v24, v24, v25\n" \
"v_mfma_f32_16x16x32_bf16 v[184:187], a[240:243], v[88:91], v[184:187]\n" \
"v_exp_f32 v27, v27\n" \
"v_mfma_f32_16x16x32_bf16 v[184:187], a[244:247], v[120:123], v[184:187]\n" \
"ds_read_b128 a[240:243], v246 offset:24832\n" \
"v_add_f32 v243, v243, v26\n" \
"v_mfma_f32_16x16x32_bf16 v[188:191], a[248:251], v[88:91], v[188:191]\n" \
"v_exp_f32 v28, v28\n" \
"v_mfma_f32_16x16x32_bf16 v[188:191], a[252:255], v[120:123], v[188:191]\n" \
"v_add_f32 v243, v243, v27\n" \
"v_cvt_pk_bf16_f32 v25, v26, v27\n" \
"v_mfma_f32_16x16x32_bf16 v[156:159], a[248:251], v[80:83], v[156:159]\n" \
"v_exp_f32 v29, v29\n" \
"v_mfma_f32_16x16x32_bf16 v[156:159], a[252:255], v[112:115], v[156:159]\n" \
"ds_read_b128 a[244:247], v246 offset:26880\n" \
"v_add_f32 v243, v243, v28\n" \
"v_mfma_f32_16x16x32_bf16 a[60:63], a[248:251], v[72:75], a[60:63]\n" \
"v_exp_f32 v30, v30\n" \
"v_mfma_f32_16x16x32_bf16 a[60:63], a[252:255], v[104:107], a[60:63]\n" \
"v_add_f32 v243, v243, v29\n" \
"v_cvt_pk_bf16_f32 v26, v28, v29\n" \
"v_mfma_f32_16x16x32_bf16 a[28:31], a[248:251], v[64:67], a[28:31]\n" \
"s_waitcnt lgkmcnt(6)\n" \
"v_exp_f32 v31, v31\n" \
"v_add_f32 v243, v243, v30\n" \
"v_mfma_f32_16x16x32_bf16 a[28:31], a[252:255], v[96:99], a[28:31]\n" \
"ds_read_b128 a[248:251], v246 offset:28928\n" \
"v_add_f32 v243, v243, v31\n" \
"LA1l_%=:\n" \
"Lpg6325_%=:\n" \
"s_cbranch_execz Lpg13686_%=\n" \
"s_getpc_b64 s[40:41]\n" \
"Lpd_%=:\n" \
"s_lshl_b32 s80, s62, 2\n" \
"s_add_u32 s80, s80, s61\n" \
"s_and_b32 s80, s80, 15\n" \
"s_lshl_b32 s80, s80, 12\n" \
"s_mov_b32 s81, Lcode_end_%=-Lpd_%=-4\n" \
"v_lshl_add_u32 v252, v248, 2, s80\n" \
"v_min_u32 v252, s81, v252\n" \
"s_waitcnt lgkmcnt(0)\n" \
"v_mfma_f32_16x16x32_bf16 v[72:75], a[192:195], a[144:147], v[228:231]\n" \
"v_exp_f32 v32, v32\n" \
"v_exp_f32 v33, v33\n" \
"v_add_f32 v240, v240, v32\n" \
"v_exp_f32 v34, v34\n" \
"v_add_f32 v240, v240, v33\n" \
"v_cvt_pk_bf16_f32 v32, v32, v33\n" \
"v_exp_f32 v35, v35\n" \
"v_add_f32 v240, v240, v34\n" \
"v_exp_f32 v36, v36\n" \
"v_add_f32 v240, v240, v35\n" \
"v_mfma_f32_16x16x32_bf16 v[72:75], a[196:199], a[148:151], v[72:75]\n" \
"v_cvt_pk_bf16_f32 v27, v30, v31\n" \
"v_cvt_pk_bf16_f32 v33, v34, v35\n" \
"v_mfma_f32_16x16x32_bf16 v[72:75], a[200:203], a[152:155], v[72:75]\n" \
"s_mov_b32 m0, s74\n" \
"v_exp_f32 v37, v37\n" \
"v_mfma_f32_16x16x32_bf16 v[72:75], a[204:207], a[156:159], v[72:75]\n" \
"ds_read_b128 a[252:255], v246 offset:30976\n" \
"global_load_lds_dwordx4 v250, s[38:39] offset:0 sc1\n" \
"v_add_f32 v240, v240, v36\n" \
"v_exp_f32 v38, v38\n" \
"v_mfma_f32_16x16x32_bf16 v[80:83], a[200:203], a[168:171], v[232:235]\n" \
"s_lshl_b32 s70, s95, 1\n" \
"s_xor_b32 s70, s70, 139264\n" \
"v_add_u32 v244, s70, v248\n" \
"v_mfma_f32_16x16x32_bf16 v[80:83], a[204:207], a[172:175], v[80:83]\n" \
"ds_read_b128 v[192:195], v244 offset:0\n" \
"v_add_f32 v240, v240, v37\n" \
"v_mfma_f32_16x16x32_bf16 v[80:83], a[192:195], a[160:163], v[80:83]\n" \
"ds_read_b128 v[196:199], v244 offset:1024\n" \
"v_cvt_pk_bf16_f32 v34, v36, v37\n" \
"v_mfma_f32_16x16x32_bf16 v[80:83], a[196:199], a[164:167], v[80:83]\n" \
"ds_read_b128 v[200:203], v244 offset:2048\n" \
"v_exp_f32 v39, v39\n" \
"v_add_f32 v240, v240, v38\n" \
"v_exp_f32 v40, v40\n" \
"v_add_f32 v240, v240, v39\n" \
"v_mfma_f32_16x16x32_bf16 v[88:91], a[192:195], a[176:179], v[236:239]\n" \
"ds_read_b128 v[204:207], v244 offset:3072\n" \
"v_cvt_pk_bf16_f32 v35, v38, v39\n" \
"v_mfma_f32_16x16x32_bf16 v[88:91], a[196:199], a[180:183], v[88:91]\n" \
"ds_read_b128 v[208:211], v244 offset:4096\n" \
"v_exp_f32 v41, v41\n" \
"v_mfma_f32_16x16x32_bf16 v[88:91], a[200:203], a[184:187], v[88:91]\n" \
"ds_read_b128 v[212:215], v244 offset:5120\n" \
"v_add_f32 v241, v241, v40\n" \
"v_mfma_f32_16x16x32_bf16 v[88:91], a[204:207], a[188:191], v[88:91]\n" \
"ds_read_b128 a[192:195], v247 offset:0\n" \
"v_exp_f32 v42, v42\n" \
"v_mfma_f32_16x16x32_bf16 v[92:95], a[216:219], a[184:187], v[236:239]\n" \
"ds_read_b128 v[216:219], v244 offset:6144\n" \
"global_load_lds_dwordx4 v250, s[38:39] offset:1024 sc1\n" \
"v_mfma_f32_16x16x32_bf16 v[92:95], a[220:223], a[188:191], v[92:95]\n" \
"ds_read_b128 v[220:223], v244 offset:7168\n" \
"v_add_f32 v241, v241, v41\n" \
"v_mfma_f32_16x16x32_bf16 v[92:95], a[208:211], a[176:179], v[92:95]\n" \
"ds_read_b128 a[196:199], v247 offset:8192\n" \
"v_cvt_pk_bf16_f32 v40, v40, v41\n" \
"v_mfma_f32_16x16x32_bf16 v[92:95], a[212:215], a[180:183], v[92:95]\n" \
"ds_write_b128 v244, v[232:235]\n" \
"v_exp_f32 v43, v43\n" \
"v_add_f32 v241, v241, v42\n" \
"v_exp_f32 v44, v44\n" \
"v_add_f32 v241, v241, v43\n" \
"v_cvt_pk_bf16_f32 v41, v42, v43\n" \
"v_mfma_f32_16x16x32_bf16 v[84:87], a[208:211], a[160:163], v[232:235]\n" \
"v_exp_f32 v45, v45\n" \
"v_mfma_f32_16x16x32_bf16 v[84:87], a[212:215], a[164:167], v[84:87]\n" \
"ds_read_b128 a[200:203], v247 offset:1024\n" \
"v_add_f32 v241, v241, v44\n" \
"v_mfma_f32_16x16x32_bf16 v[84:87], a[216:219], a[168:171], v[84:87]\n" \
"v_exp_f32 v46, v46\n" \
"v_mfma_f32_16x16x32_bf16 v[84:87], a[220:223], a[172:175], v[84:87]\n" \
"ds_write_b128 v244, v[236:239] offset:1024\n" \
"v_add_f32 v241, v241, v45\n" \
"v_cvt_pk_bf16_f32 v42, v44, v45\n" \
"v_xor_b32 v245, 0x2000, v244\n" \
"v_mfma_f32_16x16x32_bf16 v[76:79], a[216:219], a[152:155], v[228:231]\n" \
"ds_read_b128 a[204:207], v247 offset:9216\n" \
"v_xor_b32 v246, 0x8000, v246\n" \
"v_mfma_f32_16x16x32_bf16 v[76:79], a[220:223], a[156:159], v[76:79]\n" \
"global_load_lds_dwordx4 v250, s[38:39] offset:2048 sc1\n" \
"v_add_u32 v246, s82, v246\n" \
"v_mfma_f32_16x16x32_bf16 v[76:79], a[208:211], a[144:147], v[76:79]\n" \
"s_waitcnt lgkmcnt(4)\n" \
"v_exp_f32 v47, v47\n" \
"v_mfma_f32_16x16x32_bf16 v[76:79], a[212:215], a[148:151], v[76:79]\n" \
"ds_read_b128 a[208:211], v247 offset:2048\n" \
"v_add_f32 v241, v241, v46\n" \
"v_exp_f32 v48, v48\n" \
"v_mfma_f32_16x16x32_bf16 v[104:107], a[224:227], a[144:147], v[228:231]\n" \
"v_add_f32 v241, v241, v47\n" \
"v_cvt_pk_bf16_f32 v43, v46, v47\n" \
"v_mfma_f32_16x16x32_bf16 v[104:107], a[228:231], a[148:151], v[104:107]\n" \
"v_exp_f32 v49, v49\n" \
"v_mfma_f32_16x16x32_bf16 v[104:107], a[232:235], a[152:155], v[104:107]\n" \
"ds_read_b128 a[212:215], v247 offset:10240\n" \
"v_add_f32 v242, v242, v48\n" \
"v_mfma_f32_16x16x32_bf16 v[104:107], a[236:239], a[156:159], v[104:107]\n" \
"v_exp_f32 v50, v50\n" \
"v_add_f32 v242, v242, v49\n" \
"v_cvt_pk_bf16_f32 v48, v48, v49\n" \
"v_exp_f32 v51, v51\n" \
"v_add_f32 v242, v242, v50\n" \
"v_mfma_f32_16x16x32_bf16 v[112:115], a[232:235], a[168:171], v[232:235]\n" \
"v_exp_f32 v52, v52\n" \
"v_mfma_f32_16x16x32_bf16 v[112:115], a[236:239], a[172:175], v[112:115]\n" \
"ds_read_b128 a[216:219], v247 offset:3072\n" \
"v_add_f32 v242, v242, v51\n" \
"v_mfma_f32_16x16x32_bf16 v[112:115], a[224:227], a[160:163], v[112:115]\n" \
"global_load_lds_dwordx4 v250, s[38:39] offset:3072 sc1\n" \
"v_cvt_pk_bf16_f32 v49, v50, v51\n" \
"v_mfma_f32_16x16x32_bf16 v[112:115], a[228:231], a[164:167], v[112:115]\n" \
"s_add_u32 s38, s38, 17408\n" \
"v_exp_f32 v53, v53\n" \
"v_mfma_f32_16x16x32_bf16 v[120:123], a[224:227], a[176:179], v[236:239]\n" \
"ds_read_b128 a[220:223], v247 offset:11264\n" \
"v_add_f32 v242, v242, v52\n" \
"v_mfma_f32_16x16x32_bf16 v[120:123], a[228:231], a[180:183], v[120:123]\n" \
"v_exp_f32 v54, v54\n" \
"v_mfma_f32_16x16x32_bf16 v[120:123], a[232:235], a[184:187], v[120:123]\n" \
"v_add_f32 v242, v242, v53\n" \
"v_cvt_pk_bf16_f32 v50, v52, v53\n" \
"v_mfma_f32_16x16x32_bf16 v[120:123], a[236:239], a[188:191], v[120:123]\n" \
"ds_read_b128 a[224:227], v247 offset:4096\n" \
"v_exp_f32 v55, v55\n" \
"v_add_f32 v242, v242, v54\n" \
"v_exp_f32 v56, v56\n" \
"v_add_f32 v242, v242, v55\n" \
"v_cvt_pk_bf16_f32 v51, v54, v55\n" \
"v_mfma_f32_16x16x32_bf16 v[124:127], a[248:251], a[184:187], v[236:239]\n" \
"global_load_dword v253, v252, s[40:41]\n" \
"v_mfma_f32_16x16x32_bf16 v[124:127], a[252:255], a[188:191], v[124:127]\n" \
"v_exp_f32 v57, v57\n" \
"v_mfma_f32_16x16x32_bf16 v[124:127], a[240:243], a[176:179], v[124:127]\n" \
"ds_read_b128 a[228:231], v247 offset:12288\n" \
"v_add_f32 v243, v243, v56\n" \
"v_mfma_f32_16x16x32_bf16 v[124:127], a[244:247], a[180:183], v[124:127]\n" \
"v_exp_f32 v58, v58\n" \
"v_add_f32 v243, v243, v57\n" \
"v_cvt_pk_bf16_f32 v56, v56, v57\n" \
"v_mfma_f32_16x16x32_bf16 v[116:119], a[240:243], a[160:163], v[232:235]\n" \
"v_exp_f32 v59, v59\n" \
"v_mfma_f32_16x16x32_bf16 v[116:119], a[244:247], a[164:167], v[116:119]\n" \
"ds_read_b128 a[232:235], v247 offset:5120\n" \
"v_add_f32 v243, v243, v58\n" \
"v_mfma_f32_16x16x32_bf16 v[116:119], a[248:251], a[168:171], v[116:119]\n" \
"v_exp_f32 v60, v60\n" \
"v_mfma_f32_16x16x32_bf16 v[116:119], a[252:255], a[172:175], v[116:119]\n" \
"v_add_f32 v243, v243, v59\n" \
"v_cvt_pk_bf16_f32 v57, v58, v59\n" \
"v_exp_f32 v61, v61\n" \
"v_mfma_f32_16x16x32_bf16 v[108:111], a[248:251], a[152:155], v[228:231]\n" \
"ds_read_b128 a[236:239], v247 offset:13312\n" \
"v_add_f32 v243, v243, v60\n" \
"v_mfma_f32_16x16x32_bf16 v[108:111], a[252:255], a[156:159], v[108:111]\n" \
"v_exp_f32 v62, v62\n" \
"v_mfma_f32_16x16x32_bf16 v[108:111], a[240:243], a[144:147], v[108:111]\n" \
"s_waitcnt vmcnt(5) lgkmcnt(4)\n" \
"v_add_f32 v243, v243, v61\n" \
"v_cvt_pk_bf16_f32 v58, v60, v61\n" \
"v_exp_f32 v63, v63\n" \
"v_add_f32 v243, v243, v62\n" \
"v_mfma_f32_16x16x32_bf16 v[108:111], a[244:247], a[148:151], v[108:111]\n" \
"s_barrier\n" \
"ds_read_b128 a[240:243], v247 offset:6144\n" \
"v_add_f32 v243, v243, v63\n" \
"v_mfma_f32_16x16x32_bf16 a[0:3], a[192:195], v[0:3], a[0:3]\n" \
"v_exp_f32 v80, v80\n" \
"v_mfma_f32_16x16x32_bf16 a[0:3], a[196:199], v[32:35], a[0:3]\n" \
"v_cvt_pk_bf16_f32 v59, v62, v63\n" \
"v_exp_f32 v81, v81\n" \
"v_add_f32 v242, v242, v80\n" \
"v_exp_f32 v82, v82\n" \
"v_add_f32 v242, v242, v81\n" \
"v_cvt_pk_bf16_f32 v80, v80, v81\n" \
"v_exp_f32 v83, v83\n" \
"v_add_f32 v242, v242, v82\n" \
"v_exp_f32 v84, v84\n" \
"v_add_f32 v242, v242, v83\n" \
"v_mfma_f32_16x16x32_bf16 a[32:35], a[192:195], v[8:11], a[32:35]\n" \
"ds_read_b128 a[244:247], v247 offset:14336\n" \
"v_cvt_pk_bf16_f32 v81, v82, v83\n" \
"v_mfma_f32_16x16x32_bf16 a[32:35], a[196:199], v[40:43], a[32:35]\n" \
"ds_read_b128 a[248:251], v247 offset:7168\n" \
"v_exp_f32 v85, v85\n" \
"v_add_f32 v242, v242, v84\n" \
"v_exp_f32 v86, v86\n" \
"v_add_f32 v242, v242, v85\n" \
"v_cvt_pk_bf16_f32 v82, v84, v85\n" \
"v_exp_f32 v87, v87\n" \
"v_mfma_f32_16x16x32_bf16 v[128:131], a[192:195], v[16:19], v[128:131]\n" \
"ds_read_b128 a[252:255], v247 offset:15360\n" \
"v_add_f32 v242, v242, v86\n" \
"v_mfma_f32_16x16x32_bf16 v[128:131], a[196:199], v[48:51], v[128:131]\n" \
"ds_read_b128 v[224:227], v245\n" \
"v_add_f32 v242, v242, v87\n" \
"v_mfma_f32_16x16x32_bf16 v[160:163], a[192:195], v[24:27], v[160:163]\n" \
"s_add_u32 s73, s73, s95\n" \
"ds_read_b128 v[228:231], v245 offset:1024\n" \
"v_mov_b32 v244, 0\n" \
"v_mfma_f32_16x16x32_bf16 v[160:163], a[196:199], v[56:59], v[160:163]\n" \
"s_add_u32 s36, s36, s83\n" \
"ds_read_b128 a[192:195], v246 offset:0\n" \
"v_mov_b32 v245, 0\n" \
"v_mfma_f32_16x16x32_bf16 v[164:167], a[200:203], v[24:27], v[164:167]\n" \
"s_add_u32 s73, s73, 4096\n" \
"v_xor_b32 v252, 0x8000, v246\n" \
"s_mov_b32 m0, s73\n" \
"v_mfma_f32_16x16x32_bf16 v[164:167], a[204:207], v[56:59], v[164:167]\n" \
"global_load_lds_dwordx4 v250, s[36:37] offset:-4096 sc1\n" \
"v_cvt_pk_bf16_f32 v83, v86, v87\n" \
"v_mfma_f32_16x16x32_bf16 v[132:135], a[200:203], v[16:19], v[132:135]\n" \
"v_cmp_gt_i32 vcc, 0, v251\n" \
"v_cmp_gt_i32 s[64:65], 1, v251\n" \
"v_mfma_f32_16x16x32_bf16 v[132:135], a[204:207], v[48:51], v[132:135]\n" \
"ds_read_b128 a[196:199], v246 offset:2048\n" \
"global_load_lds_dwordx4 v250, s[36:37] offset:-3072 sc1\n" \
"v_mfma_f32_16x16x32_bf16 a[36:39], a[200:203], v[8:11], a[36:39]\n" \
"v_cmp_gt_i32 s[66:67], 2, v251\n" \
"v_cndmask_b32 v72, v72, v254, vcc\n" \
"v_mfma_f32_16x16x32_bf16 a[36:39], a[204:207], v[40:43], a[36:39]\n" \
"v_cmp_gt_i32 vcc, 3, v251\n" \
"v_cndmask_b32 v73, v73, v254, s[64:65]\n" \
"v_mfma_f32_16x16x32_bf16 a[4:7], a[200:203], v[0:3], a[4:7]\n" \
"v_cmp_gt_i32 s[64:65], 16, v251\n" \
"v_cndmask_b32 v74, v74, v254, s[66:67]\n" \
"v_mfma_f32_16x16x32_bf16 a[4:7], a[204:207], v[32:35], a[4:7]\n" \
"ds_read_b128 a[200:203], v246 offset:4096\n" \
"global_load_lds_dwordx4 v250, s[36:37] offset:-2048 sc1\n" \
"v_mfma_f32_16x16x32_bf16 a[8:11], a[208:211], v[0:3], a[8:11]\n" \
"v_cmp_gt_i32 s[66:67], 17, v251\n" \
"v_cndmask_b32 v75, v75, v254, vcc\n" \
"v_mfma_f32_16x16x32_bf16 a[8:11], a[212:215], v[32:35], a[8:11]\n" \
"v_cmp_gt_i32 vcc, 18, v251\n" \
"v_cndmask_b32 v76, v76, v254, s[64:65]\n" \
"v_mfma_f32_16x16x32_bf16 a[40:43], a[208:211], v[8:11], a[40:43]\n" \
"v_cmp_gt_i32 s[64:65], 19, v251\n" \
"v_cndmask_b32 v77, v77, v254, s[66:67]\n" \
"v_mfma_f32_16x16x32_bf16 a[40:43], a[212:215], v[40:43], a[40:43]\n" \
"ds_read_b128 a[204:207], v246 offset:6144\n" \
"global_load_lds_dwordx4 v250, s[36:37] offset:-1024 sc1\n" \
"v_mfma_f32_16x16x32_bf16 v[136:139], a[208:211], v[16:19], v[136:139]\n" \
"v_cmp_gt_i32 s[66:67], 32, v251\n" \
"v_cndmask_b32 v78, v78, v254, vcc\n" \
"v_mfma_f32_16x16x32_bf16 v[136:139], a[212:215], v[48:51], v[136:139]\n" \
"v_cmp_gt_i32 vcc, 33, v251\n" \
"v_cndmask_b32 v79, v79, v254, s[64:65]\n" \
"v_mfma_f32_16x16x32_bf16 v[168:171], a[208:211], v[24:27], v[168:171]\n" \
"v_cmp_gt_i32 s[64:65], 34, v251\n" \
"v_cndmask_b32 v104, v104, v254, s[66:67]\n" \
"v_mfma_f32_16x16x32_bf16 v[168:171], a[212:215], v[56:59], v[168:171]\n" \
"ds_read_b128 a[208:211], v246 offset:256\n" \
"global_load_lds_dwordx4 v250, s[36:37] offset:0 sc1\n" \
"v_mfma_f32_16x16x32_bf16 v[172:175], a[216:219], v[24:27], v[172:175]\n" \
"v_cmp_gt_i32 s[66:67], 35, v251\n" \
"v_cndmask_b32 v105, v105, v254, vcc\n" \
"v_mfma_f32_16x16x32_bf16 v[172:175], a[220:223], v[56:59], v[172:175]\n" \
"v_cmp_gt_i32 vcc, 48, v251\n" \
"v_cndmask_b32 v106, v106, v254, s[64:65]\n" \
"v_mfma_f32_16x16x32_bf16 v[140:143], a[216:219], v[16:19], v[140:143]\n" \
"v_cmp_gt_i32 s[64:65], 49, v251\n" \
"v_cndmask_b32 v107, v107, v254, s[66:67]\n" \
"v_mfma_f32_16x16x32_bf16 v[140:143], a[220:223], v[48:51], v[140:143]\n" \
"ds_read_b128 a[212:215], v246 offset:2304\n" \
"global_load_lds_dwordx4 v250, s[36:37] offset:1024 sc1\n" \
"v_mfma_f32_16x16x32_bf16 a[44:47], a[216:219], v[8:11], a[44:47]\n" \
"v_cmp_gt_i32 s[66:67], 50, v251\n" \
"v_cndmask_b32 v108, v108, v254, vcc\n" \
"v_mfma_f32_16x16x32_bf16 a[44:47], a[220:223], v[40:43], a[44:47]\n" \
"v_cmp_gt_i32 vcc, 51, v251\n" \
"v_cndmask_b32 v109, v109, v254, s[64:65]\n" \
"v_mfma_f32_16x16x32_bf16 a[12:15], a[216:219], v[0:3], a[12:15]\n" \
"s_waitcnt lgkmcnt(6)\n" \
"v_cndmask_b32 v110, v110, v254, s[66:67]\n" \
"v_cndmask_b32 v111, v111, v254, vcc\n" \
"v_mfma_f32_16x16x32_bf16 a[12:15], a[220:223], v[32:35], a[12:15]\n" \
"ds_read_b128 a[216:219], v246 offset:4352\n" \
"global_load_lds_dwordx4 v250, s[36:37] offset:2048 sc1\n" \
"v_mfma_f32_16x16x32_bf16 a[16:19], a[224:227], v[0:3], a[16:19]\n" \
"v_exp_f32 v72, v72\n" \
"v_mfma_f32_16x16x32_bf16 a[16:19], a[228:231], v[32:35], a[16:19]\n" \
"v_exp_f32 v73, v73\n" \
"v_add_f32 v241, v241, v72\n" \
"v_mfma_f32_16x16x32_bf16 a[48:51], a[224:227], v[8:11], a[48:51]\n" \
"v_exp_f32 v74, v74\n" \
"v_mfma_f32_16x16x32_bf16 a[48:51], a[228:231], v[40:43], a[48:51]\n" \
"ds_read_b128 a[220:223], v246 offset:6400\n" \
"global_load_lds_dwordx4 v250, s[36:37] offset:3072 sc1\n" \
"v_mfma_f32_16x16x32_bf16 v[144:147], a[224:227], v[16:19], v[144:147]\n" \
"s_add_u32 s36, s36, 34816\n" \
"v_add_f32 v241, v241, v73\n" \
"v_cvt_pk_bf16_f32 v72, v72, v73\n" \
"v_mfma_f32_16x16x32_bf16 v[144:147], a[228:231], v[48:51], v[144:147]\n" \
"v_exp_f32 v75, v75\n" \
"v_add_f32 v241, v241, v74\n" \
"v_mfma_f32_16x16x32_bf16 v[176:179], a[224:227], v[24:27], v[176:179]\n" \
"s_add_u32 m0, s74, 0x4000\n" \
"v_exp_f32 v76, v76\n" \
"v_mfma_f32_16x16x32_bf16 v[176:179], a[228:231], v[56:59], v[176:179]\n" \
"ds_read_b128 a[224:227], v246 offset:8192\n" \
"global_load_lds_dwordx4 v250, s[38:39] offset:0 sc1\n" \
"v_mfma_f32_16x16x32_bf16 v[180:183], a[232:235], v[24:27], v[180:183]\n" \
"v_add_f32 v241, v241, v75\n" \
"v_cvt_pk_bf16_f32 v73, v74, v75\n" \
"v_mfma_f32_16x16x32_bf16 v[180:183], a[236:239], v[56:59], v[180:183]\n" \
"v_exp_f32 v77, v77\n" \
"v_add_f32 v241, v241, v76\n" \
"v_mfma_f32_16x16x32_bf16 v[148:151], a[232:235], v[16:19], v[148:151]\n" \
"v_exp_f32 v78, v78\n" \
"v_mfma_f32_16x16x32_bf16 v[148:151], a[236:239], v[48:51], v[148:151]\n" \
"ds_read_b128 a[228:231], v246 offset:10240\n" \
"global_load_lds_dwordx4 v250, s[38:39] offset:1024 sc1\n" \
"v_mfma_f32_16x16x32_bf16 a[52:55], a[232:235], v[8:11], a[52:55]\n" \
"v_add_f32 v241, v241, v77\n" \
"v_cvt_pk_bf16_f32 v74, v76, v77\n" \
"v_mfma_f32_16x16x32_bf16 a[52:55], a[236:239], v[40:43], a[52:55]\n" \
"v_exp_f32 v79, v79\n" \
"v_add_f32 v241, v241, v78\n" \
"v_mfma_f32_16x16x32_bf16 a[20:23], a[232:235], v[0:3], a[20:23]\n" \
"v_exp_f32 v88, v88\n" \
"v_mfma_f32_16x16x32_bf16 a[20:23], a[236:239], v[32:35], a[20:23]\n" \
"ds_read_b128 a[232:235], v246 offset:12288\n" \
"global_load_lds_dwordx4 v250, s[38:39] offset:2048 sc1\n" \
"v_mfma_f32_16x16x32_bf16 a[24:27], a[240:243], v[0:3], a[24:27]\n" \
"v_add_f32 v241, v241, v79\n" \
"v_cvt_pk_bf16_f32 v75, v78, v79\n" \
"v_mfma_f32_16x16x32_bf16 a[24:27], a[244:247], v[32:35], a[24:27]\n" \
"v_exp_f32 v89, v89\n" \
"v_add_f32 v243, v243, v88\n" \
"v_mfma_f32_16x16x32_bf16 a[56:59], a[240:243], v[8:11], a[56:59]\n" \
"v_exp_f32 v90, v90\n" \
"v_mfma_f32_16x16x32_bf16 a[56:59], a[244:247], v[40:43], a[56:59]\n" \
"ds_read_b128 a[236:239], v246 offset:14336\n" \
"global_load_lds_dwordx4 v250, s[38:39] offset:3072 sc1\n" \
"v_add_f32 v243, v243, v89\n" \
"v_mfma_f32_16x16x32_bf16 v[152:155], a[240:243], v[16:19], v[152:155]\n" \
"s_add_u32 s38, s38, 17408\n" \
"s_add_u32 s38, s38, s83\n" \
"s_add_u32 s74, s74, s95\n" \
"v_mfma_f32_16x16x32_bf16 v[152:155], a[244:247], v[48:51], v[152:155]\n" \
"s_add_u32 s74, s74, 4096\n" \
"v_cvt_pk_bf16_f32 v88, v88, v89\n" \
"s_cmp_eq_u32 s63, 7\n" \
"v_mfma_f32_16x16x32_bf16 v[184:187], a[240:243], v[24:27], v[184:187]\n" \
"v_exp_f32 v91, v91\n" \
"v_mfma_f32_16x16x32_bf16 v[184:187], a[244:247], v[56:59], v[184:187]\n" \
"ds_read_b128 a[240:243], v246 offset:8448\n" \
"v_add_f32 v243, v243, v90\n" \
"v_mfma_f32_16x16x32_bf16 v[188:191], a[248:251], v[24:27], v[188:191]\n" \
"v_exp_f32 v92, v92\n" \
"v_mfma_f32_16x16x32_bf16 v[188:191], a[252:255], v[56:59], v[188:191]\n" \
"v_add_f32 v243, v243, v91\n" \
"v_cvt_pk_bf16_f32 v89, v90, v91\n" \
"v_mfma_f32_16x16x32_bf16 v[156:159], a[248:251], v[16:19], v[156:159]\n" \
"v_exp_f32 v93, v93\n" \
"v_mfma_f32_16x16x32_bf16 v[156:159], a[252:255], v[48:51], v[156:159]\n" \
"ds_read_b128 a[244:247], v246 offset:10496\n" \
"v_add_f32 v243, v243, v92\n" \
"v_mfma_f32_16x16x32_bf16 a[60:63], a[248:251], v[8:11], a[60:63]\n" \
"v_exp_f32 v94, v94\n" \
"v_mfma_f32_16x16x32_bf16 a[60:63], a[252:255], v[40:43], a[60:63]\n" \
"v_add_f32 v243, v243, v93\n" \
"v_cvt_pk_bf16_f32 v90, v92, v93\n" \
"v_mfma_f32_16x16x32_bf16 a[28:31], a[248:251], v[0:3], a[28:31]\n" \
"s_waitcnt lgkmcnt(6)\n" \
"v_exp_f32 v95, v95\n" \
"v_add_f32 v243, v243, v94\n" \
"v_mfma_f32_16x16x32_bf16 a[28:31], a[252:255], v[32:35], a[28:31]\n" \
"ds_read_b128 a[248:251], v246 offset:12544\n" \
"v_add_f32 v243, v243, v95\n" \
"s_cbranch_scc1 LF7_%=\n" \
"v_mfma_f32_16x16x32_bf16 v[0:3], a[192:195], v[192:195], v[224:227]\n" \
"s_load_dwordx8 s[44:51], s[56:57], s79 glc\n" \
"v_exp_f32 v104, v104\n" \
"v_mfma_f32_16x16x32_bf16 v[0:3], a[196:199], v[196:199], v[0:3]\n" \
"v_cvt_pk_bf16_f32 v91, v94, v95\n" \
"v_mfma_f32_16x16x32_bf16 v[0:3], a[200:203], v[200:203], v[0:3]\n" \
"v_exp_f32 v105, v105\n" \
"v_mfma_f32_16x16x32_bf16 v[0:3], a[204:207], v[204:207], v[0:3]\n" \
"ds_read_b128 a[252:255], v246 offset:14592\n" \
"v_add_f32 v241, v241, v104\n" \
"v_mov_b32 v76, v240\n" \
"v_mfma_f32_16x16x32_bf16 v[8:11], a[200:203], v[216:219], v[228:231]\n" \
"v_exp_f32 v106, v106\n" \
"v_mfma_f32_16x16x32_bf16 v[8:11], a[204:207], v[220:223], v[8:11]\n" \
"v_add_f32 v241, v241, v105\n" \
"v_cvt_pk_bf16_f32 v104, v104, v105\n" \
"v_mfma_f32_16x16x32_bf16 v[8:11], a[192:195], v[208:211], v[8:11]\n" \
"v_exp_f32 v107, v107\n" \
"v_mfma_f32_16x16x32_bf16 v[8:11], a[196:199], v[212:215], v[8:11]\n" \
"v_add_f32 v241, v241, v106\n" \
"v_permlane16_swap_b32 v76, v240\n" \
"v_add_f32 v240, v240, v76\n" \
"v_mov_b32 v76, v240\n" \
"v_mfma_f32_16x16x32_bf16 v[16:19], a[192:195], a[160:163], v[232:235]\n" \
"v_exp_f32 v108, v108\n" \
"v_mfma_f32_16x16x32_bf16 v[16:19], a[196:199], a[164:167], v[16:19]\n" \
"v_add_f32 v241, v241, v107\n" \
"v_cvt_pk_bf16_f32 v105, v106, v107\n" \
"v_mfma_f32_16x16x32_bf16 v[16:19], a[200:203], a[168:171], v[16:19]\n" \
"v_exp_f32 v109, v109\n" \
"v_mfma_f32_16x16x32_bf16 v[16:19], a[204:207], a[172:175], v[16:19]\n" \
"v_add_f32 v241, v241, v108\n" \
"v_permlane32_swap_b32 v76, v240\n" \
"v_add_f32 v240, v240, v76\n" \
"v_rcp_f32 v76, v240\n" \
"v_mul_f32 v240, 0x4d800000, v240\n" \
"v_mfma_f32_16x16x32_bf16 v[24:27], a[200:203], a[184:187], v[236:239]\n" \
"v_exp_f32 v110, v110\n" \
"v_mfma_f32_16x16x32_bf16 v[24:27], a[204:207], a[188:191], v[24:27]\n" \
"v_add_f32 v241, v241, v109\n" \
"v_cvt_pk_bf16_f32 v106, v108, v109\n" \
"v_mfma_f32_16x16x32_bf16 v[24:27], a[192:195], a[176:179], v[24:27]\n" \
"v_exp_f32 v111, v111\n" \
"v_mfma_f32_16x16x32_bf16 v[24:27], a[196:199], a[180:183], v[24:27]\n" \
"ds_read_b128 a[192:195], v247 offset:16384\n" \
"v_add_f32 v241, v241, v110\n" \
"v_mfma_f32_16x16x32_bf16 v[28:31], a[208:211], a[176:179], v[236:239]\n" \
"v_exp_f32 v112, v112\n" \
"v_mfma_f32_16x16x32_bf16 v[28:31], a[212:215], a[180:183], v[28:31]\n" \
"v_add_f32 v241, v241, v111\n" \
"v_cvt_pk_bf16_f32 v107, v110, v111\n" \
"v_mfma_f32_16x16x32_bf16 v[28:31], a[216:219], a[184:187], v[28:31]\n" \
"v_exp_f32 v113, v113\n" \
"v_mfma_f32_16x16x32_bf16 v[28:31], a[220:223], a[188:191], v[28:31]\n" \
"ds_read_b128 a[196:199], v247 offset:24576\n" \
"v_add_f32 v242, v242, v112\n" \
"v_mfma_f32_16x16x32_bf16 v[20:23], a[216:219], a[168:171], v[232:235]\n" \
"v_exp_f32 v114, v114\n" \
"v_mfma_f32_16x16x32_bf16 v[20:23], a[220:223], a[172:175], v[20:23]\n" \
"v_add_f32 v242, v242, v113\n" \
"v_cvt_pk_bf16_f32 v112, v112, v113\n" \
"v_mfma_f32_16x16x32_bf16 v[20:23], a[208:211], a[160:163], v[20:23]\n" \
"v_exp_f32 v115, v115\n" \
"v_mfma_f32_16x16x32_bf16 v[20:23], a[212:215], a[164:167], v[20:23]\n" \
"ds_read_b128 a[200:203], v247 offset:17408\n" \
"v_add_f32 v242, v242, v114\n" \
"v_mfma_f32_16x16x32_bf16 v[12:15], a[208:211], v[208:211], v[228:231]\n" \
"v_exp_f32 v116, v116\n" \
"v_mfma_f32_16x16x32_bf16 v[12:15], a[212:215], v[212:215], v[12:15]\n" \
"v_add_f32 v242, v242, v115\n" \
"v_cvt_pk_bf16_f32 v113, v114, v115\n" \
"v_mfma_f32_16x16x32_bf16 v[12:15], a[216:219], v[216:219], v[12:15]\n" \
"v_exp_f32 v117, v117\n" \
"v_mfma_f32_16x16x32_bf16 v[12:15], a[220:223], v[220:223], v[12:15]\n" \
"ds_read_b128 a[204:207], v247 offset:25600\n" \
"v_add_f32 v242, v242, v116\n" \
"v_mfma_f32_16x16x32_bf16 v[4:7], a[216:219], v[200:203], v[224:227]\n" \
"v_exp_f32 v118, v118\n" \
"v_mfma_f32_16x16x32_bf16 v[4:7], a[220:223], v[204:207], v[4:7]\n" \
"v_add_f32 v242, v242, v117\n" \
"v_cvt_pk_bf16_f32 v114, v116, v117\n" \
"v_mfma_f32_16x16x32_bf16 v[4:7], a[208:211], v[192:195], v[4:7]\n" \
"s_waitcnt lgkmcnt(4)\n" \
"v_exp_f32 v119, v119\n" \
"v_mfma_f32_16x16x32_bf16 v[4:7], a[212:215], v[196:199], v[4:7]\n" \
"ds_read_b128 a[208:211], v247 offset:18432\n" \
"v_add_f32 v242, v242, v118\n" \
"v_mfma_f32_16x16x32_bf16 v[32:35], a[224:227], v[192:195], v[224:227]\n" \
"v_exp_f32 v120, v120\n" \
"v_mfma_f32_16x16x32_bf16 v[32:35], a[228:231], v[196:199], v[32:35]\n" \
"v_add_f32 v242, v242, v119\n" \
"v_cvt_pk_bf16_f32 v115, v118, v119\n" \
"v_mfma_f32_16x16x32_bf16 v[32:35], a[232:235], v[200:203], v[32:35]\n" \
"v_exp_f32 v121, v121\n" \
"v_mfma_f32_16x16x32_bf16 v[32:35], a[236:239], v[204:207], v[32:35]\n" \
"ds_read_b128 a[212:215], v247 offset:26624\n" \
"v_add_f32 v243, v243, v120\n" \
"v_mfma_f32_16x16x32_bf16 v[40:43], a[232:235], v[216:219], v[228:231]\n" \
"v_exp_f32 v122, v122\n" \
"v_mfma_f32_16x16x32_bf16 v[40:43], a[236:239], v[220:223], v[40:43]\n" \
"v_add_f32 v243, v243, v121\n" \
"v_cvt_pk_bf16_f32 v120, v120, v121\n" \
"v_mfma_f32_16x16x32_bf16 v[40:43], a[224:227], v[208:211], v[40:43]\n" \
"v_exp_f32 v123, v123\n" \
"v_mfma_f32_16x16x32_bf16 v[40:43], a[228:231], v[212:215], v[40:43]\n" \
"ds_read_b128 a[216:219], v247 offset:19456\n" \
"v_add_f32 v243, v243, v122\n" \
"v_mfma_f32_16x16x32_bf16 v[48:51], a[224:227], a[160:163], v[232:235]\n" \
"v_exp_f32 v124, v124\n" \
"v_mfma_f32_16x16x32_bf16 v[48:51], a[228:231], a[164:167], v[48:51]\n" \
"v_add_f32 v243, v243, v123\n" \
"v_cvt_pk_bf16_f32 v121, v122, v123\n" \
"v_mfma_f32_16x16x32_bf16 v[48:51], a[232:235], a[168:171], v[48:51]\n" \
"v_exp_f32 v125, v125\n" \
"v_mfma_f32_16x16x32_bf16 v[48:51], a[236:239], a[172:175], v[48:51]\n" \
"ds_read_b128 a[220:223], v247 offset:27648\n" \
"v_add_f32 v243, v243, v124\n" \
"v_mfma_f32_16x16x32_bf16 v[56:59], a[232:235], a[184:187], v[236:239]\n" \
"s_xor_b64 s[52:53], s[44:45], s[42:43]\n" \
"s_xor_b64 s[54:55], s[46:47], s[42:43]\n" \
"s_or_b64 s[52:53], s[52:53], s[54:55]\n" \
"v_mfma_f32_16x16x32_bf16 v[56:59], a[236:239], a[188:191], v[56:59]\n" \
"s_xor_b64 s[54:55], s[48:49], s[42:43]\n" \
"s_or_b64 s[52:53], s[52:53], s[54:55]\n" \
"s_xor_b64 s[54:55], s[50:51], s[42:43]\n" \
"v_mfma_f32_16x16x32_bf16 v[56:59], a[224:227], a[176:179], v[56:59]\n" \
"s_or_b64 s[52:53], s[52:53], s[54:55]\n" \
"v_exp_f32 v126, v126\n" \
"v_mfma_f32_16x16x32_bf16 v[56:59], a[228:231], a[180:183], v[56:59]\n" \
"ds_read_b128 a[224:227], v247 offset:20480\n" \
"Lpg6839_%=:\n" \
"s_cbranch_execz Lcb0_%=\n" \
"v_add_f32 v243, v243, v125\n" \
"v_mfma_f32_16x16x32_bf16 v[60:63], a[240:243], a[176:179], v[236:239]\n" \
"s_and_b64 s[52:53], s[52:53], s[8:9]\n" \
"s_cbranch_scc1 Lrs2_%=\n" \
"Lrr2_%=:\n" \
"v_mfma_f32_16x16x32_bf16 v[60:63], a[244:247], a[180:183], v[60:63]\n" \
"s_mov_b64 s[8:9], s[10:11]\n" \
"v_cvt_pk_bf16_f32 v122, v124, v125\n" \
"v_add_u32 v77, 0x1c90080, v249\n" \
"v_mfma_f32_16x16x32_bf16 v[60:63], a[248:251], a[184:187], v[60:63]\n" \
"v_exp_f32 v127, v127\n" \
"v_mfma_f32_16x16x32_bf16 v[60:63], a[252:255], a[188:191], v[60:63]\n" \
"ds_read_b128 a[228:231], v247 offset:28672\n" \
"v_add_f32 v243, v243, v126\n" \
"v_mfma_f32_16x16x32_bf16 v[52:55], a[248:251], a[168:171], v[232:235]\n" \
"v_add_f32 v243, v243, v127\n" \
"v_cndmask_b32 v77, v249, v77, s[16:17]\n" \
"v_mfma_f32_16x16x32_bf16 v[52:55], a[252:255], a[172:175], v[52:55]\n" \
"v_accvgpr_read_b32 v64, a0\n" \
"v_accvgpr_read_b32 v65, a1\n" \
"v_mfma_f32_16x16x32_bf16 v[52:55], a[240:243], a[160:163], v[52:55]\n" \
"v_accvgpr_read_b32 v66, a2\n" \
"v_accvgpr_read_b32 v67, a3\n" \
"v_mfma_f32_16x16x32_bf16 v[52:55], a[244:247], a[164:167], v[52:55]\n" \
"ds_read_b128 a[232:235], v247 offset:21504\n" \
"v_accvgpr_read_b32 v68, a4\n" \
"v_accvgpr_read_b32 v69, a5\n" \
"v_accvgpr_read_b32 v70, a6\n" \
"v_accvgpr_read_b32 v71, a7\n" \
"v_pk_mul_f32 v[64:65], v[64:65], v[76:77] op_sel_hi:[1,0]\n" \
"v_pk_mul_f32 v[66:67], v[66:67], v[76:77] op_sel_hi:[1,0]\n" \
"v_pk_mul_f32 v[68:69], v[68:69], v[76:77] op_sel_hi:[1,0]\n" \
"v_pk_mul_f32 v[70:71], v[70:71], v[76:77] op_sel_hi:[1,0]\n" \
"v_cvt_pk_bf16_f32 v96, v64, v65\n" \
"v_cvt_pk_bf16_f32 v97, v66, v67\n" \
"v_cvt_pk_bf16_f32 v98, v68, v69\n" \
"v_cvt_pk_bf16_f32 v99, v70, v71\n" \
"v_pk_mul_f32 v[64:65], v[64:65], v[66:67]\n" \
"v_pk_fma_f32 v[64:65], v[68:69], v[70:71], v[64:65]\n" \
"v_fma_f32 v240, v64, v65, v240\n" \
"v_mfma_f32_16x16x32_bf16 v[44:47], a[240:243], v[208:211], v[228:231]\n" \
"buffer_store_dwordx4 v[96:99], v77, s[56:59], s77 offen offset:0 sc1\n" \
"v_accvgpr_read_b32 v64, a8\n" \
"v_accvgpr_read_b32 v65, a9\n" \
"v_accvgpr_read_b32 v66, a10\n" \
"v_accvgpr_read_b32 v67, a11\n" \
"v_accvgpr_read_b32 v68, a12\n" \
"v_accvgpr_read_b32 v69, a13\n" \
"v_accvgpr_read_b32 v70, a14\n" \
"v_accvgpr_read_b32 v71, a15\n" \
"v_pk_mul_f32 v[64:65], v[64:65], v[76:77] op_sel_hi:[1,0]\n" \
"v_pk_mul_f32 v[66:67], v[66:67], v[76:77] op_sel_hi:[1,0]\n" \
"v_pk_mul_f32 v[68:69], v[68:69], v[76:77] op_sel_hi:[1,0]\n" \
"v_pk_mul_f32 v[70:71], v[70:71], v[76:77] op_sel_hi:[1,0]\n" \
"v_cvt_pk_bf16_f32 v100, v64, v65\n" \
"v_cvt_pk_bf16_f32 v101, v66, v67\n" \
"v_cvt_pk_bf16_f32 v102, v68, v69\n" \
"v_cvt_pk_bf16_f32 v103, v70, v71\n" \
"v_pk_mul_f32 v[64:65], v[64:65], v[66:67]\n" \
"v_pk_fma_f32 v[64:65], v[68:69], v[70:71], v[64:65]\n" \
"v_fma_f32 v240, v64, v65, v240\n" \
"v_mfma_f32_16x16x32_bf16 v[44:47], a[244:247], v[212:215], v[44:47]\n" \
"buffer_store_dwordx4 v[100:103], v77, s[56:59], s77 offen offset:2048 sc1\n" \
"s_add_u32 s77, s77, 0x1000\n" \
"v_accvgpr_read_b32 v64, a16\n" \
"v_accvgpr_read_b32 v65, a17\n" \
"v_accvgpr_read_b32 v66, a18\n" \
"v_mfma_f32_16x16x32_bf16 v[44:47], a[248:251], v[216:219], v[44:47]\n" \
"v_accvgpr_read_b32 v67, a19\n" \
"v_accvgpr_read_b32 v68, a20\n" \
"v_accvgpr_read_b32 v69, a21\n" \
"v_accvgpr_read_b32 v70, a22\n" \
"v_accvgpr_read_b32 v71, a23\n" \
"v_pk_mul_f32 v[64:65], v[64:65], v[76:77] op_sel_hi:[1,0]\n" \
"v_pk_mul_f32 v[66:67], v[66:67], v[76:77] op_sel_hi:[1,0]\n" \
"v_pk_mul_f32 v[68:69], v[68:69], v[76:77] op_sel_hi:[1,0]\n" \
"v_pk_mul_f32 v[70:71], v[70:71], v[76:77] op_sel_hi:[1,0]\n" \
"v_cvt_pk_bf16_f32 v96, v64, v65\n" \
"v_cvt_pk_bf16_f32 v97, v66, v67\n" \
"v_cvt_pk_bf16_f32 v98, v68, v69\n" \
"v_cvt_pk_bf16_f32 v99, v70, v71\n" \
"v_pk_mul_f32 v[64:65], v[64:65], v[66:67]\n" \
"v_pk_fma_f32 v[64:65], v[68:69], v[70:71], v[64:65]\n" \
"v_fma_f32 v240, v64, v65, v240\n" \
"v_mfma_f32_16x16x32_bf16 v[44:47], a[252:255], v[220:223], v[44:47]\n" \
"buffer_store_dwordx4 v[96:99], v77, s[56:59], s77 offen offset:0 sc1\n" \
"ds_read_b128 a[236:239], v247 offset:29696\n" \
"v_accvgpr_read_b32 v64, a24\n" \
"v_accvgpr_read_b32 v65, a25\n" \
"v_accvgpr_read_b32 v66, a26\n" \
"v_mfma_f32_16x16x32_bf16 v[36:39], a[248:251], v[200:203], v[224:227]\n" \
"v_accvgpr_read_b32 v67, a27\n" \
"v_accvgpr_read_b32 v68, a28\n" \
"v_accvgpr_read_b32 v69, a29\n" \
"v_accvgpr_read_b32 v70, a30\n" \
"v_accvgpr_read_b32 v71, a31\n" \
"v_pk_mul_f32 v[64:65], v[64:65], v[76:77] op_sel_hi:[1,0]\n" \
"v_pk_mul_f32 v[66:67], v[66:67], v[76:77] op_sel_hi:[1,0]\n" \
"v_pk_mul_f32 v[68:69], v[68:69], v[76:77] op_sel_hi:[1,0]\n" \
"v_pk_mul_f32 v[70:71], v[70:71], v[76:77] op_sel_hi:[1,0]\n" \
"v_cvt_pk_bf16_f32 v100, v64, v65\n" \
"v_cvt_pk_bf16_f32 v101, v66, v67\n" \
"v_cvt_pk_bf16_f32 v102, v68, v69\n" \
"v_cvt_pk_bf16_f32 v103, v70, v71\n" \
"v_pk_mul_f32 v[64:65], v[64:65], v[66:67]\n" \
"v_pk_fma_f32 v[64:65], v[68:69], v[70:71], v[64:65]\n" \
"v_fma_f32 v240, v64, v65, v240\n" \
"v_mfma_f32_16x16x32_bf16 v[36:39], a[252:255], v[204:207], v[36:39]\n" \
"buffer_store_dwordx4 v[100:103], v77, s[56:59], s77 offen offset:2048 sc1\n" \
"s_sub_u32 s77, s77, 4096\n" \
"v_mfma_f32_16x16x32_bf16 v[36:39], a[240:243], v[192:195], v[36:39]\n" \
"s_waitcnt vmcnt(8) lgkmcnt(4)\n" \
"v_mfma_f32_16x16x32_bf16 v[36:39], a[244:247], v[196:199], v[36:39]\n" \
"s_barrier\n" \
"ds_read_b128 a[240:243], v247 offset:22528\n" \
"v_mfma_f32_16x16x32_bf16 a[32:35], a[192:195], v[72:75], a[32:35]\n" \
"v_exp_f32 v0, v0\n" \
"v_mfma_f32_16x16x32_bf16 a[32:35], a[196:199], v[104:107], a[32:35]\n" \
"v_cvt_pk_bf16_f32 v123, v126, v127\n" \
"v_exp_f32 v1, v1\n" \
"v_add_f32 v244, v244, v0\n" \
"v_exp_f32 v2, v2\n" \
"v_add_f32 v244, v244, v1\n" \
"v_cvt_pk_bf16_f32 v0, v0, v1\n" \
"v_exp_f32 v3, v3\n" \
"v_add_f32 v244, v244, v2\n" \
"v_exp_f32 v4, v4\n" \
"v_add_f32 v244, v244, v3\n" \
"v_mfma_f32_16x16x32_bf16 v[128:131], a[192:195], v[80:83], v[128:131]\n" \
"s_xor_b32 m0, s73, 0x8000\n" \
"ds_read_b128 a[244:247], v247 offset:30720\n" \
"v_cvt_pk_bf16_f32 v1, v2, v3\n" \
"v_mfma_f32_16x16x32_bf16 v[128:131], a[196:199], v[112:115], v[128:131]\n" \
"ds_read_b128 a[248:251], v247 offset:23552\n" \
"global_load_lds_dwordx4 v250, s[36:37] offset:-4096 sc1\n" \
"v_exp_f32 v5, v5\n" \
"v_add_f32 v244, v244, v4\n" \
"v_exp_f32 v6, v6\n" \
"v_add_f32 v244, v244, v5\n" \
"v_cvt_pk_bf16_f32 v2, v4, v5\n" \
"v_exp_f32 v7, v7\n" \
"v_add_f32 v244, v244, v6\n" \
"v_exp_f32 v8, v8\n" \
"v_add_f32 v244, v244, v7\n" \
"v_mfma_f32_16x16x32_bf16 v[160:163], a[192:195], v[88:91], v[160:163]\n" \
"ds_read_b128 a[252:255], v247 offset:31744\n" \
"v_cvt_pk_bf16_f32 v3, v6, v7\n" \
"v_mfma_f32_16x16x32_bf16 v[160:163], a[196:199], v[120:123], v[160:163]\n" \
"s_add_u32 s70, s82, 0x10000\n" \
"ds_read_b128 a[192:195], v252 offset:0\n" \
"v_exp_f32 v9, v9\n" \
"v_add_f32 v245, v245, v8\n" \
"v_add_u32 v247, s70, v248\n" \
"v_mfma_f32_16x16x32_bf16 v[164:167], a[200:203], v[88:91], v[164:167]\n" \
"v_exp_f32 v10, v10\n" \
"v_mfma_f32_16x16x32_bf16 v[164:167], a[204:207], v[120:123], v[164:167]\n" \
"global_load_lds_dwordx4 v250, s[36:37] offset:-3072 sc1\n" \
"v_add_f32 v245, v245, v9\n" \
"v_mfma_f32_16x16x32_bf16 v[132:135], a[200:203], v[80:83], v[132:135]\n" \
"ds_read_b128 a[196:199], v252 offset:2048\n" \
"v_cvt_pk_bf16_f32 v8, v8, v9\n" \
"v_mfma_f32_16x16x32_bf16 v[132:135], a[204:207], v[112:115], v[132:135]\n" \
"v_exp_f32 v11, v11\n" \
"v_add_f32 v245, v245, v10\n" \
"v_mfma_f32_16x16x32_bf16 a[36:39], a[200:203], v[72:75], a[36:39]\n" \
"v_exp_f32 v12, v12\n" \
"v_mfma_f32_16x16x32_bf16 a[36:39], a[204:207], v[104:107], a[36:39]\n" \
"ds_read_b128 a[200:203], v252 offset:4096\n" \
"v_add_f32 v245, v245, v11\n" \
"v_mfma_f32_16x16x32_bf16 a[40:43], a[208:211], v[72:75], a[40:43]\n" \
"global_load_lds_dwordx4 v250, s[36:37] offset:-2048 sc1\n" \
"v_cvt_pk_bf16_f32 v9, v10, v11\n" \
"v_mfma_f32_16x16x32_bf16 a[40:43], a[212:215], v[104:107], a[40:43]\n" \
"v_exp_f32 v13, v13\n" \
"v_mfma_f32_16x16x32_bf16 v[136:139], a[208:211], v[80:83], v[136:139]\n" \
"ds_read_b128 a[204:207], v252 offset:6144\n" \
"v_add_f32 v245, v245, v12\n" \
"v_mfma_f32_16x16x32_bf16 v[136:139], a[212:215], v[112:115], v[136:139]\n" \
"v_exp_f32 v14, v14\n" \
"v_mfma_f32_16x16x32_bf16 v[168:171], a[208:211], v[88:91], v[168:171]\n" \
"global_load_lds_dwordx4 v250, s[36:37] offset:-1024 sc1\n" \
"v_add_f32 v245, v245, v13\n" \
"v_mfma_f32_16x16x32_bf16 v[168:171], a[212:215], v[120:123], v[168:171]\n" \
"ds_read_b128 a[208:211], v252 offset:256\n" \
"v_cvt_pk_bf16_f32 v10, v12, v13\n" \
"v_mfma_f32_16x16x32_bf16 v[172:175], a[216:219], v[88:91], v[172:175]\n" \
"v_exp_f32 v15, v15\n" \
"v_mfma_f32_16x16x32_bf16 v[172:175], a[220:223], v[120:123], v[172:175]\n" \
"v_add_f32 v245, v245, v14\n" \
"v_exp_f32 v16, v16\n" \
"v_mfma_f32_16x16x32_bf16 v[140:143], a[216:219], v[80:83], v[140:143]\n" \
"ds_read_b128 a[212:215], v252 offset:2304\n" \
"v_add_f32 v245, v245, v15\n" \
"v_mfma_f32_16x16x32_bf16 v[140:143], a[220:223], v[112:115], v[140:143]\n" \
"global_load_lds_dwordx4 v250, s[36:37] offset:0 sc1\n" \
"v_cvt_pk_bf16_f32 v11, v14, v15\n" \
"v_mfma_f32_16x16x32_bf16 a[44:47], a[216:219], v[72:75], a[44:47]\n" \
"s_waitcnt lgkmcnt(6)\n" \
"v_exp_f32 v17, v17\n" \
"v_mfma_f32_16x16x32_bf16 a[44:47], a[220:223], v[104:107], a[44:47]\n" \
"ds_read_b128 a[216:219], v252 offset:4352\n" \
"v_add_f32 v242, v242, v16\n" \
"v_exp_f32 v18, v18\n" \
"v_mfma_f32_16x16x32_bf16 a[48:51], a[224:227], v[72:75], a[48:51]\n" \
"v_add_f32 v242, v242, v17\n" \
"v_cvt_pk_bf16_f32 v16, v16, v17\n" \
"v_mfma_f32_16x16x32_bf16 a[48:51], a[228:231], v[104:107], a[48:51]\n" \
"global_load_lds_dwordx4 v250, s[36:37] offset:1024 sc1\n" \
"v_exp_f32 v19, v19\n" \
"v_mfma_f32_16x16x32_bf16 v[144:147], a[224:227], v[80:83], v[144:147]\n" \
"ds_read_b128 a[220:223], v252 offset:6400\n" \
"v_add_f32 v242, v242, v18\n" \
"v_mfma_f32_16x16x32_bf16 v[144:147], a[228:231], v[112:115], v[144:147]\n" \
"v_exp_f32 v20, v20\n" \
"v_mfma_f32_16x16x32_bf16 v[176:179], a[224:227], v[88:91], v[176:179]\n" \
"v_add_f32 v242, v242, v19\n" \
"v_cvt_pk_bf16_f32 v17, v18, v19\n" \
"v_mfma_f32_16x16x32_bf16 v[176:179], a[228:231], v[120:123], v[176:179]\n" \
"ds_read_b128 a[224:227], v252 offset:8192\n" \
"v_exp_f32 v21, v21\n" \
"v_mfma_f32_16x16x32_bf16 v[180:183], a[232:235], v[88:91], v[180:183]\n" \
"global_load_lds_dwordx4 v250, s[36:37] offset:2048 sc1\n" \
"v_add_f32 v242, v242, v20\n" \
"v_mfma_f32_16x16x32_bf16 v[180:183], a[236:239], v[120:123], v[180:183]\n" \
"v_exp_f32 v22, v22\n" \
"v_add_f32 v242, v242, v21\n" \
"v_mfma_f32_16x16x32_bf16 v[148:151], a[232:235], v[80:83], v[148:151]\n" \
"ds_read_b128 a[228:231], v252 offset:10240\n" \
"v_cvt_pk_bf16_f32 v18, v20, v21\n" \
"v_mfma_f32_16x16x32_bf16 v[148:151], a[236:239], v[112:115], v[148:151]\n" \
"v_exp_f32 v23, v23\n" \
"v_mfma_f32_16x16x32_bf16 a[52:55], a[232:235], v[72:75], a[52:55]\n" \
"global_load_lds_dwordx4 v250, s[36:37] offset:3072 sc1\n" \
"v_add_f32 v242, v242, v22\n" \
"v_mfma_f32_16x16x32_bf16 a[52:55], a[236:239], v[104:107], a[52:55]\n" \
"s_add_u32 s36, s36, 34816\n" \
"ds_read_b128 a[232:235], v252 offset:12288\n" \
"v_exp_f32 v24, v24\n" \
"v_mfma_f32_16x16x32_bf16 a[56:59], a[240:243], v[72:75], a[56:59]\n" \
"v_add_f32 v242, v242, v23\n" \
"v_cvt_pk_bf16_f32 v19, v22, v23\n" \
"v_mfma_f32_16x16x32_bf16 a[56:59], a[244:247], v[104:107], a[56:59]\n" \
"v_exp_f32 v25, v25\n" \
"v_mfma_f32_16x16x32_bf16 v[152:155], a[240:243], v[80:83], v[152:155]\n" \
"ds_read_b128 a[236:239], v252 offset:14336\n" \
"v_add_f32 v243, v243, v24\n" \
"v_mfma_f32_16x16x32_bf16 v[152:155], a[244:247], v[112:115], v[152:155]\n" \
"v_exp_f32 v26, v26\n" \
"v_add_f32 v243, v243, v25\n" \
"v_cvt_pk_bf16_f32 v24, v24, v25\n" \
"v_mfma_f32_16x16x32_bf16 v[184:187], a[240:243], v[88:91], v[184:187]\n" \
"s_load_dwordx8 s[44:51], s[56:57], s79 glc\n" \
"v_exp_f32 v27, v27\n" \
"v_mfma_f32_16x16x32_bf16 v[184:187], a[244:247], v[120:123], v[184:187]\n" \
"ds_read_b128 a[240:243], v252 offset:8448\n" \
"v_add_f32 v243, v243, v26\n" \
"v_exp_f32 v28, v28\n" \
"v_mfma_f32_16x16x32_bf16 v[188:191], a[248:251], v[88:91], v[188:191]\n" \
"v_add_f32 v243, v243, v27\n" \
"v_cvt_pk_bf16_f32 v25, v26, v27\n" \
"v_mfma_f32_16x16x32_bf16 v[188:191], a[252:255], v[120:123], v[188:191]\n" \
"v_exp_f32 v29, v29\n" \
"v_mfma_f32_16x16x32_bf16 v[156:159], a[248:251], v[80:83], v[156:159]\n" \
"ds_read_b128 a[244:247], v252 offset:10496\n" \
"v_add_f32 v243, v243, v28\n" \
"v_mfma_f32_16x16x32_bf16 v[156:159], a[252:255], v[112:115], v[156:159]\n" \
"v_exp_f32 v30, v30\n" \
"v_add_f32 v243, v243, v29\n" \
"v_cvt_pk_bf16_f32 v26, v28, v29\n" \
"v_mfma_f32_16x16x32_bf16 a[60:63], a[248:251], v[72:75], a[60:63]\n" \
"s_waitcnt vmcnt(12) lgkmcnt(6)\n" \
"v_exp_f32 v31, v31\n" \
"v_add_f32 v243, v243, v30\n" \
"v_mfma_f32_16x16x32_bf16 a[60:63], a[252:255], v[104:107], a[60:63]\n" \
"s_barrier\n" \
"ds_read_b128 a[248:251], v252 offset:12544\n" \
"v_add_f32 v243, v243, v31\n" \
"s_mov_b64 exec, 0\n" \
"s_cmp_eq_u32 s61, 0\n" \
"s_cbranch_scc1 Lst1x0_%=\n" \
"s_cmp_eq_u32 s61, 1\n" \
"s_cbranch_scc1 Lst1x1_%=\n" \
"s_cmp_eq_u32 s61, 2\n" \
"s_cbranch_scc1 Lst1x2_%=\n" \
"s_branch Lst1x3_%=\n" \
"Lcb1_%=:\n" \
"s_mov_b64 exec, -1\n" \
"s_waitcnt lgkmcnt(0)\n" \
"s_cmp_lg_u32 s63, 0\n" \
"s_cbranch_scc1 LA1g_%=\n" \
"s_xor_b64 s[52:53], s[44:45], s[42:43]\n" \
"s_xor_b64 s[54:55], s[46:47], s[42:43]\n" \
"s_or_b64 s[52:53], s[52:53], s[54:55]\n" \
"s_xor_b64 s[54:55], s[48:49], s[42:43]\n" \
"s_or_b64 s[52:53], s[52:53], s[54:55]\n" \
"s_xor_b64 s[54:55], s[50:51], s[42:43]\n" \
"s_or_b64 s[52:53], s[52:53], s[54:55]\n" \
"s_and_b64 s[52:53], s[52:53], s[8:9]\n" \
"s_cbranch_scc1 Lrs3_%=\n" \
"Lrr3_%=:\n" \
"s_branch LA1g_%=\n" \
"Lpg7141_%=:\n" \
"s_cbranch_execz Lcb0_%=\n" \
"Lloop2_%=:\n" \
"v_mfma_f32_16x16x32_bf16 a[64:67], a[192:195], v[0:3], a[64:67]\n" \
"v_exp_f32 v64, v64\n" \
"v_mfma_f32_16x16x32_bf16 a[64:67], a[196:199], v[32:35], a[64:67]\n" \
"v_cvt_pk_bf16_f32 v59, v62, v63\n" \
"v_add_f32 v244, v244, v64\n" \
"v_exp_f32 v65, v65\n" \
"v_mfma_f32_16x16x32_bf16 a[96:99], a[192:195], v[8:11], a[96:99]\n" \
"s_mov_b32 m0, s73\n" \
"ds_read_b128 a[244:247], v247 offset:14336\n" \
"v_add_f32 v244, v244, v65\n" \
"v_mfma_f32_16x16x32_bf16 a[96:99], a[196:199], v[40:43], a[96:99]\n" \
"ds_read_b128 a[248:251], v247 offset:7168\n" \
"global_load_lds_dwordx4 v250, s[36:37] offset:-4096 sc1\n" \
"v_mfma_f32_16x16x32_bf16 v[128:131], a[192:195], v[16:19], v[128:131]\n" \
"ds_read_b128 a[252:255], v247 offset:15360\n" \
"v_exp_f32 v66, v66\n" \
"v_mfma_f32_16x16x32_bf16 v[128:131], a[196:199], v[48:51], v[128:131]\n" \
"v_add_f32 v244, v244, v66\n" \
"v_cvt_pk_bf16_f32 v64, v64, v65\n" \
"v_mfma_f32_16x16x32_bf16 v[160:163], a[192:195], v[24:27], v[160:163]\n" \
"v_exp_f32 v67, v67\n" \
"v_mfma_f32_16x16x32_bf16 v[160:163], a[196:199], v[56:59], v[160:163]\n" \
"ds_read_b128 a[192:195], v246 offset:0\n" \
"global_load_lds_dwordx4 v250, s[36:37] offset:-3072 sc1\n" \
"v_add_f32 v244, v244, v67\n" \
"v_mfma_f32_16x16x32_bf16 v[164:167], a[200:203], v[24:27], v[164:167]\n" \
"v_exp_f32 v68, v68\n" \
"v_mfma_f32_16x16x32_bf16 v[164:167], a[204:207], v[56:59], v[164:167]\n" \
"v_add_f32 v244, v244, v68\n" \
"v_cvt_pk_bf16_f32 v65, v66, v67\n" \
"v_mfma_f32_16x16x32_bf16 v[132:135], a[200:203], v[16:19], v[132:135]\n" \
"v_exp_f32 v69, v69\n" \
"v_mfma_f32_16x16x32_bf16 v[132:135], a[204:207], v[48:51], v[132:135]\n" \
"ds_read_b128 a[196:199], v246 offset:2048\n" \
"v_add_f32 v244, v244, v69\n" \
"v_mfma_f32_16x16x32_bf16 a[100:103], a[200:203], v[8:11], a[100:103]\n" \
"v_exp_f32 v70, v70\n" \
"v_mfma_f32_16x16x32_bf16 a[100:103], a[204:207], v[40:43], a[100:103]\n" \
"v_add_f32 v244, v244, v70\n" \
"v_cvt_pk_bf16_f32 v66, v68, v69\n" \
"v_mfma_f32_16x16x32_bf16 a[68:71], a[200:203], v[0:3], a[68:71]\n" \
"v_exp_f32 v71, v71\n" \
"v_mfma_f32_16x16x32_bf16 a[68:71], a[204:207], v[32:35], a[68:71]\n" \
"ds_read_b128 a[200:203], v246 offset:4096\n" \
"global_load_lds_dwordx4 v250, s[36:37] offset:-2048 sc1\n" \
"v_add_f32 v244, v244, v71\n" \
"v_mfma_f32_16x16x32_bf16 a[72:75], a[208:211], v[0:3], a[72:75]\n" \
"v_exp_f32 v72, v72\n" \
"v_mfma_f32_16x16x32_bf16 a[72:75], a[212:215], v[32:35], a[72:75]\n" \
"v_add_f32 v245, v245, v72\n" \
"v_cvt_pk_bf16_f32 v67, v70, v71\n" \
"v_mfma_f32_16x16x32_bf16 a[104:107], a[208:211], v[8:11], a[104:107]\n" \
"v_exp_f32 v73, v73\n" \
"v_mfma_f32_16x16x32_bf16 a[104:107], a[212:215], v[40:43], a[104:107]\n" \
"ds_read_b128 a[204:207], v246 offset:6144\n" \
"global_load_lds_dwordx4 v250, s[36:37] offset:-1024 sc1\n" \
"v_add_f32 v245, v245, v73\n" \
"v_mfma_f32_16x16x32_bf16 v[136:139], a[208:211], v[16:19], v[136:139]\n" \
"v_exp_f32 v74, v74\n" \
"v_mfma_f32_16x16x32_bf16 v[136:139], a[212:215], v[48:51], v[136:139]\n" \
"v_add_f32 v245, v245, v74\n" \
"v_cvt_pk_bf16_f32 v72, v72, v73\n" \
"v_mfma_f32_16x16x32_bf16 v[168:171], a[208:211], v[24:27], v[168:171]\n" \
"v_exp_f32 v75, v75\n" \
"v_mfma_f32_16x16x32_bf16 v[168:171], a[212:215], v[56:59], v[168:171]\n" \
"ds_read_b128 a[208:211], v246 offset:256\n" \
"v_add_f32 v245, v245, v75\n" \
"v_mfma_f32_16x16x32_bf16 v[172:175], a[216:219], v[24:27], v[172:175]\n" \
"v_exp_f32 v76, v76\n" \
"v_mfma_f32_16x16x32_bf16 v[172:175], a[220:223], v[56:59], v[172:175]\n" \
"v_add_f32 v245, v245, v76\n" \
"v_cvt_pk_bf16_f32 v73, v74, v75\n" \
"v_mfma_f32_16x16x32_bf16 v[140:143], a[216:219], v[16:19], v[140:143]\n" \
"v_exp_f32 v77, v77\n" \
"v_mfma_f32_16x16x32_bf16 v[140:143], a[220:223], v[48:51], v[140:143]\n" \
"ds_read_b128 a[212:215], v246 offset:2304\n" \
"global_load_lds_dwordx4 v250, s[36:37] offset:0 sc1\n" \
"v_add_f32 v245, v245, v77\n" \
"v_mfma_f32_16x16x32_bf16 a[108:111], a[216:219], v[8:11], a[108:111]\n" \
"v_exp_f32 v78, v78\n" \
"v_mfma_f32_16x16x32_bf16 a[108:111], a[220:223], v[40:43], a[108:111]\n" \
"v_add_f32 v245, v245, v78\n" \
"v_cvt_pk_bf16_f32 v74, v76, v77\n" \
"v_mfma_f32_16x16x32_bf16 a[76:79], a[216:219], v[0:3], a[76:79]\n" \
"s_waitcnt lgkmcnt(6)\n" \
"v_exp_f32 v79, v79\n" \
"v_mfma_f32_16x16x32_bf16 a[76:79], a[220:223], v[32:35], a[76:79]\n" \
"ds_read_b128 a[216:219], v246 offset:4352\n" \
"global_load_lds_dwordx4 v250, s[36:37] offset:1024 sc1\n" \
"v_add_f32 v245, v245, v79\n" \
"v_mfma_f32_16x16x32_bf16 a[80:83], a[224:227], v[0:3], a[80:83]\n" \
"v_exp_f32 v80, v80\n" \
"v_mfma_f32_16x16x32_bf16 a[80:83], a[228:231], v[32:35], a[80:83]\n" \
"v_add_f32 v242, v242, v80\n" \
"v_cvt_pk_bf16_f32 v75, v78, v79\n" \
"v_mfma_f32_16x16x32_bf16 a[112:115], a[224:227], v[8:11], a[112:115]\n" \
"v_exp_f32 v81, v81\n" \
"v_mfma_f32_16x16x32_bf16 a[112:115], a[228:231], v[40:43], a[112:115]\n" \
"ds_read_b128 a[220:223], v246 offset:6400\n" \
"v_add_f32 v242, v242, v81\n" \
"v_mfma_f32_16x16x32_bf16 v[144:147], a[224:227], v[16:19], v[144:147]\n" \
"v_exp_f32 v82, v82\n" \
"v_mfma_f32_16x16x32_bf16 v[144:147], a[228:231], v[48:51], v[144:147]\n" \
"v_add_f32 v242, v242, v82\n" \
"v_cvt_pk_bf16_f32 v80, v80, v81\n" \
"v_mfma_f32_16x16x32_bf16 v[176:179], a[224:227], v[24:27], v[176:179]\n" \
"v_exp_f32 v83, v83\n" \
"v_mfma_f32_16x16x32_bf16 v[176:179], a[228:231], v[56:59], v[176:179]\n" \
"ds_read_b128 a[224:227], v246 offset:8192\n" \
"global_load_lds_dwordx4 v250, s[36:37] offset:2048 sc1\n" \
"v_add_f32 v242, v242, v83\n" \
"v_mfma_f32_16x16x32_bf16 v[180:183], a[232:235], v[24:27], v[180:183]\n" \
"v_exp_f32 v84, v84\n" \
"v_mfma_f32_16x16x32_bf16 v[180:183], a[236:239], v[56:59], v[180:183]\n" \
"v_add_f32 v242, v242, v84\n" \
"v_cvt_pk_bf16_f32 v81, v82, v83\n" \
"v_mfma_f32_16x16x32_bf16 v[148:151], a[232:235], v[16:19], v[148:151]\n" \
"v_exp_f32 v85, v85\n" \
"v_mfma_f32_16x16x32_bf16 v[148:151], a[236:239], v[48:51], v[148:151]\n" \
"ds_read_b128 a[228:231], v246 offset:10240\n" \
"global_load_lds_dwordx4 v250, s[36:37] offset:3072 sc1\n" \
"v_add_f32 v242, v242, v85\n" \
"v_mfma_f32_16x16x32_bf16 a[116:119], a[232:235], v[8:11], a[116:119]\n" \
"s_add_u32 s36, s36, 34816\n" \
"v_exp_f32 v86, v86\n" \
"v_mfma_f32_16x16x32_bf16 a[116:119], a[236:239], v[40:43], a[116:119]\n" \
"v_add_f32 v242, v242, v86\n" \
"v_cvt_pk_bf16_f32 v82, v84, v85\n" \
"v_mfma_f32_16x16x32_bf16 a[84:87], a[232:235], v[0:3], a[84:87]\n" \
"v_exp_f32 v87, v87\n" \
"v_mfma_f32_16x16x32_bf16 a[84:87], a[236:239], v[32:35], a[84:87]\n" \
"ds_read_b128 a[232:235], v246 offset:12288\n" \
"v_add_f32 v242, v242, v87\n" \
"v_mfma_f32_16x16x32_bf16 a[88:91], a[240:243], v[0:3], a[88:91]\n" \
"v_exp_f32 v88, v88\n" \
"v_mfma_f32_16x16x32_bf16 a[88:91], a[244:247], v[32:35], a[88:91]\n" \
"v_add_f32 v243, v243, v88\n" \
"v_cvt_pk_bf16_f32 v83, v86, v87\n" \
"v_mfma_f32_16x16x32_bf16 a[120:123], a[240:243], v[8:11], a[120:123]\n" \
"v_exp_f32 v89, v89\n" \
"v_mfma_f32_16x16x32_bf16 a[120:123], a[244:247], v[40:43], a[120:123]\n" \
"ds_read_b128 a[236:239], v246 offset:14336\n" \
"v_add_f32 v243, v243, v89\n" \
"v_mfma_f32_16x16x32_bf16 v[152:155], a[240:243], v[16:19], v[152:155]\n" \
"v_exp_f32 v90, v90\n" \
"v_mfma_f32_16x16x32_bf16 v[152:155], a[244:247], v[48:51], v[152:155]\n" \
"v_add_f32 v243, v243, v90\n" \
"v_cvt_pk_bf16_f32 v88, v88, v89\n" \
"v_mfma_f32_16x16x32_bf16 v[184:187], a[240:243], v[24:27], v[184:187]\n" \
"v_exp_f32 v91, v91\n" \
"v_mfma_f32_16x16x32_bf16 v[184:187], a[244:247], v[56:59], v[184:187]\n" \
"ds_read_b128 a[240:243], v246 offset:8448\n" \
"v_add_f32 v243, v243, v91\n" \
"v_mfma_f32_16x16x32_bf16 v[188:191], a[248:251], v[24:27], v[188:191]\n" \
"v_exp_f32 v92, v92\n" \
"v_mfma_f32_16x16x32_bf16 v[188:191], a[252:255], v[56:59], v[188:191]\n" \
"v_add_f32 v243, v243, v92\n" \
"v_cvt_pk_bf16_f32 v89, v90, v91\n" \
"v_mfma_f32_16x16x32_bf16 v[156:159], a[248:251], v[16:19], v[156:159]\n" \
"v_exp_f32 v93, v93\n" \
"v_mfma_f32_16x16x32_bf16 v[156:159], a[252:255], v[48:51], v[156:159]\n" \
"ds_read_b128 a[244:247], v246 offset:10496\n" \
"v_add_f32 v243, v243, v93\n" \
"v_mfma_f32_16x16x32_bf16 a[124:127], a[248:251], v[8:11], a[124:127]\n" \
"v_exp_f32 v94, v94\n" \
"v_mfma_f32_16x16x32_bf16 a[124:127], a[252:255], v[40:43], a[124:127]\n" \
"v_add_f32 v243, v243, v94\n" \
"v_cvt_pk_bf16_f32 v90, v92, v93\n" \
"v_mfma_f32_16x16x32_bf16 a[92:95], a[248:251], v[0:3], a[92:95]\n" \
"s_waitcnt vmcnt(8) lgkmcnt(6)\n" \
"v_exp_f32 v95, v95\n" \
"v_mfma_f32_16x16x32_bf16 a[92:95], a[252:255], v[32:35], a[92:95]\n" \
"s_barrier\n" \
"ds_read_b128 a[248:251], v246 offset:12544\n" \
"v_add_f32 v243, v243, v95\n" \
"v_mfma_f32_16x16x32_bf16 v[0:3], a[192:195], v[192:195], v[224:227]\n" \
"v_exp_f32 v96, v96\n" \
"v_mfma_f32_16x16x32_bf16 v[0:3], a[196:199], v[196:199], v[0:3]\n" \
"v_cvt_pk_bf16_f32 v91, v94, v95\n" \
"v_add_f32 v244, v244, v96\n" \
"v_mfma_f32_16x16x32_bf16 v[0:3], a[200:203], v[200:203], v[0:3]\n" \
"s_mov_b32 m0, s74\n" \
"v_exp_f32 v97, v97\n" \
"v_mfma_f32_16x16x32_bf16 v[0:3], a[204:207], v[204:207], v[0:3]\n" \
"ds_read_b128 a[252:255], v246 offset:14592\n" \
"global_load_lds_dwordx4 v250, s[38:39] offset:-4096 sc1\n" \
"v_add_f32 v244, v244, v97\n" \
"v_mfma_f32_16x16x32_bf16 v[8:11], a[200:203], v[216:219], v[228:231]\n" \
"v_exp_f32 v98, v98\n" \
"v_mfma_f32_16x16x32_bf16 v[8:11], a[204:207], v[220:223], v[8:11]\n" \
"v_add_f32 v244, v244, v98\n" \
"v_cvt_pk_bf16_f32 v96, v96, v97\n" \
"v_mfma_f32_16x16x32_bf16 v[8:11], a[192:195], v[208:211], v[8:11]\n" \
"v_exp_f32 v99, v99\n" \
"v_mfma_f32_16x16x32_bf16 v[8:11], a[196:199], v[212:215], v[8:11]\n" \
"global_load_lds_dwordx4 v250, s[38:39] offset:-3072 sc1\n" \
"v_add_f32 v244, v244, v99\n" \
"v_mfma_f32_16x16x32_bf16 v[16:19], a[192:195], a[160:163], v[232:235]\n" \
"v_exp_f32 v100, v100\n" \
"v_mfma_f32_16x16x32_bf16 v[16:19], a[196:199], a[164:167], v[16:19]\n" \
"v_add_f32 v244, v244, v100\n" \
"v_cvt_pk_bf16_f32 v97, v98, v99\n" \
"v_mfma_f32_16x16x32_bf16 v[16:19], a[200:203], a[168:171], v[16:19]\n" \
"v_exp_f32 v101, v101\n" \
"v_mfma_f32_16x16x32_bf16 v[16:19], a[204:207], a[172:175], v[16:19]\n" \
"v_add_f32 v244, v244, v101\n" \
"v_exp_f32 v102, v102\n" \
"v_mfma_f32_16x16x32_bf16 v[24:27], a[200:203], a[184:187], v[236:239]\n" \
"v_add_f32 v244, v244, v102\n" \
"v_cvt_pk_bf16_f32 v98, v100, v101\n" \
"v_mfma_f32_16x16x32_bf16 v[24:27], a[204:207], a[188:191], v[24:27]\n" \
"v_exp_f32 v103, v103\n" \
"v_mfma_f32_16x16x32_bf16 v[24:27], a[192:195], a[176:179], v[24:27]\n" \
"v_add_f32 v244, v244, v103\n" \
"v_mfma_f32_16x16x32_bf16 v[24:27], a[196:199], a[180:183], v[24:27]\n" \
"ds_read_b128 a[192:195], v247 offset:32768\n" \
"global_load_lds_dwordx4 v250, s[38:39] offset:-2048 sc1\n" \
"v_mfma_f32_16x16x32_bf16 v[28:31], a[208:211], a[176:179], v[236:239]\n" \
"v_exp_f32 v104, v104\n" \
"v_mfma_f32_16x16x32_bf16 v[28:31], a[212:215], a[180:183], v[28:31]\n" \
"v_add_f32 v245, v245, v104\n" \
"v_cvt_pk_bf16_f32 v99, v102, v103\n" \
"v_mfma_f32_16x16x32_bf16 v[28:31], a[216:219], a[184:187], v[28:31]\n" \
"v_exp_f32 v105, v105\n" \
"v_mfma_f32_16x16x32_bf16 v[28:31], a[220:223], a[188:191], v[28:31]\n" \
"ds_read_b128 a[196:199], v247 offset:40960\n" \
"global_load_lds_dwordx4 v250, s[38:39] offset:-1024 sc1\n" \
"v_add_f32 v245, v245, v105\n" \
"v_mfma_f32_16x16x32_bf16 v[20:23], a[216:219], a[168:171], v[232:235]\n" \
"v_exp_f32 v106, v106\n" \
"v_mfma_f32_16x16x32_bf16 v[20:23], a[220:223], a[172:175], v[20:23]\n" \
"v_add_f32 v245, v245, v106\n" \
"v_cvt_pk_bf16_f32 v104, v104, v105\n" \
"v_mfma_f32_16x16x32_bf16 v[20:23], a[208:211], a[160:163], v[20:23]\n" \
"v_exp_f32 v107, v107\n" \
"v_mfma_f32_16x16x32_bf16 v[20:23], a[212:215], a[164:167], v[20:23]\n" \
"ds_read_b128 a[200:203], v247 offset:33792\n" \
"v_add_f32 v245, v245, v107\n" \
"v_mfma_f32_16x16x32_bf16 v[12:15], a[208:211], v[208:211], v[228:231]\n" \
"v_exp_f32 v108, v108\n" \
"v_mfma_f32_16x16x32_bf16 v[12:15], a[212:215], v[212:215], v[12:15]\n" \
"v_add_f32 v245, v245, v108\n" \
"v_cvt_pk_bf16_f32 v105, v106, v107\n" \
"v_mfma_f32_16x16x32_bf16 v[12:15], a[216:219], v[216:219], v[12:15]\n" \
"v_exp_f32 v109, v109\n" \
"v_mfma_f32_16x16x32_bf16 v[12:15], a[220:223], v[220:223], v[12:15]\n" \
"ds_read_b128 a[204:207], v247 offset:41984\n" \
"global_load_lds_dwordx4 v250, s[38:39] offset:0 sc1\n" \
"v_add_f32 v245, v245, v109\n" \
"v_mfma_f32_16x16x32_bf16 v[4:7], a[216:219], v[200:203], v[224:227]\n" \
"v_exp_f32 v110, v110\n" \
"v_mfma_f32_16x16x32_bf16 v[4:7], a[220:223], v[204:207], v[4:7]\n" \
"v_add_f32 v245, v245, v110\n" \
"v_cvt_pk_bf16_f32 v106, v108, v109\n" \
"v_mfma_f32_16x16x32_bf16 v[4:7], a[208:211], v[192:195], v[4:7]\n" \
"s_waitcnt lgkmcnt(4)\n" \
"v_exp_f32 v111, v111\n" \
"v_mfma_f32_16x16x32_bf16 v[4:7], a[212:215], v[196:199], v[4:7]\n" \
"ds_read_b128 a[208:211], v247 offset:34816\n" \
"global_load_lds_dwordx4 v250, s[38:39] offset:1024 sc1\n" \
"v_add_f32 v245, v245, v111\n" \
"v_mfma_f32_16x16x32_bf16 v[32:35], a[224:227], v[192:195], v[224:227]\n" \
"v_exp_f32 v112, v112\n" \
"v_mfma_f32_16x16x32_bf16 v[32:35], a[228:231], v[196:199], v[32:35]\n" \
"v_add_f32 v242, v242, v112\n" \
"v_cvt_pk_bf16_f32 v107, v110, v111\n" \
"v_mfma_f32_16x16x32_bf16 v[32:35], a[232:235], v[200:203], v[32:35]\n" \
"v_exp_f32 v113, v113\n" \
"v_mfma_f32_16x16x32_bf16 v[32:35], a[236:239], v[204:207], v[32:35]\n" \
"ds_read_b128 a[212:215], v247 offset:43008\n" \
"v_add_f32 v242, v242, v113\n" \
"v_mfma_f32_16x16x32_bf16 v[40:43], a[232:235], v[216:219], v[228:231]\n" \
"v_exp_f32 v114, v114\n" \
"v_mfma_f32_16x16x32_bf16 v[40:43], a[236:239], v[220:223], v[40:43]\n" \
"v_add_f32 v242, v242, v114\n" \
"v_cvt_pk_bf16_f32 v112, v112, v113\n" \
"v_mfma_f32_16x16x32_bf16 v[40:43], a[224:227], v[208:211], v[40:43]\n" \
"v_exp_f32 v115, v115\n" \
"v_mfma_f32_16x16x32_bf16 v[40:43], a[228:231], v[212:215], v[40:43]\n" \
"ds_read_b128 a[216:219], v247 offset:35840\n" \
"global_load_lds_dwordx4 v250, s[38:39] offset:2048 sc1\n" \
"v_add_f32 v242, v242, v115\n" \
"v_mfma_f32_16x16x32_bf16 v[48:51], a[224:227], a[160:163], v[232:235]\n" \
"v_exp_f32 v116, v116\n" \
"v_mfma_f32_16x16x32_bf16 v[48:51], a[228:231], a[164:167], v[48:51]\n" \
"v_add_f32 v242, v242, v116\n" \
"v_cvt_pk_bf16_f32 v113, v114, v115\n" \
"v_mfma_f32_16x16x32_bf16 v[48:51], a[232:235], a[168:171], v[48:51]\n" \
"v_exp_f32 v117, v117\n" \
"v_mfma_f32_16x16x32_bf16 v[48:51], a[236:239], a[172:175], v[48:51]\n" \
"ds_read_b128 a[220:223], v247 offset:44032\n" \
"global_load_lds_dwordx4 v250, s[38:39] offset:3072 sc1\n" \
"v_add_f32 v242, v242, v117\n" \
"v_mfma_f32_16x16x32_bf16 v[56:59], a[232:235], a[184:187], v[236:239]\n" \
"s_add_u32 s38, s38, 34816\n" \
"v_exp_f32 v118, v118\n" \
"v_mfma_f32_16x16x32_bf16 v[56:59], a[236:239], a[188:191], v[56:59]\n" \
"v_add_f32 v242, v242, v118\n" \
"v_cvt_pk_bf16_f32 v114, v116, v117\n" \
"v_mfma_f32_16x16x32_bf16 v[56:59], a[224:227], a[176:179], v[56:59]\n" \
"v_exp_f32 v119, v119\n" \
"v_mfma_f32_16x16x32_bf16 v[56:59], a[228:231], a[180:183], v[56:59]\n" \
"ds_read_b128 a[224:227], v247 offset:36864\n" \
"v_add_f32 v242, v242, v119\n" \
"v_mfma_f32_16x16x32_bf16 v[60:63], a[240:243], a[176:179], v[236:239]\n" \
"v_exp_f32 v120, v120\n" \
"v_mfma_f32_16x16x32_bf16 v[60:63], a[244:247], a[180:183], v[60:63]\n" \
"v_add_f32 v243, v243, v120\n" \
"v_cvt_pk_bf16_f32 v115, v118, v119\n" \
"v_mfma_f32_16x16x32_bf16 v[60:63], a[248:251], a[184:187], v[60:63]\n" \
"v_exp_f32 v121, v121\n" \
"v_mfma_f32_16x16x32_bf16 v[60:63], a[252:255], a[188:191], v[60:63]\n" \
"ds_read_b128 a[228:231], v247 offset:45056\n" \
"v_add_f32 v243, v243, v121\n" \
"v_mfma_f32_16x16x32_bf16 v[52:55], a[248:251], a[168:171], v[232:235]\n" \
"v_exp_f32 v122, v122\n" \
"v_mfma_f32_16x16x32_bf16 v[52:55], a[252:255], a[172:175], v[52:55]\n" \
"v_add_f32 v243, v243, v122\n" \
"v_cvt_pk_bf16_f32 v120, v120, v121\n" \
"v_mfma_f32_16x16x32_bf16 v[52:55], a[240:243], a[160:163], v[52:55]\n" \
"v_exp_f32 v123, v123\n" \
"v_mfma_f32_16x16x32_bf16 v[52:55], a[244:247], a[164:167], v[52:55]\n" \
"ds_read_b128 a[232:235], v247 offset:37888\n" \
"v_add_f32 v243, v243, v123\n" \
"v_mfma_f32_16x16x32_bf16 v[44:47], a[240:243], v[208:211], v[228:231]\n" \
"Lpg7468_%=:\n" \
"Lst1x0_%=:\n" \
"s_cbranch_execz Lpg9869_%=\n" \
"v_exp_f32 v124, v124\n" \
"v_mfma_f32_16x16x32_bf16 v[44:47], a[244:247], v[212:215], v[44:47]\n" \
"v_add_f32 v243, v243, v124\n" \
"v_cvt_pk_bf16_f32 v121, v122, v123\n" \
"v_mfma_f32_16x16x32_bf16 v[44:47], a[248:251], v[216:219], v[44:47]\n" \
"v_exp_f32 v125, v125\n" \
"v_mfma_f32_16x16x32_bf16 v[44:47], a[252:255], v[220:223], v[44:47]\n" \
"ds_read_b128 a[236:239], v247 offset:46080\n" \
"v_add_f32 v243, v243, v125\n" \
"v_mfma_f32_16x16x32_bf16 v[36:39], a[248:251], v[200:203], v[224:227]\n" \
"v_exp_f32 v126, v126\n" \
"v_mfma_f32_16x16x32_bf16 v[36:39], a[252:255], v[204:207], v[36:39]\n" \
"v_add_f32 v243, v243, v126\n" \
"v_cvt_pk_bf16_f32 v122, v124, v125\n" \
"v_mfma_f32_16x16x32_bf16 v[36:39], a[240:243], v[192:195], v[36:39]\n" \
"s_waitcnt vmcnt(8) lgkmcnt(4)\n" \
"v_exp_f32 v127, v127\n" \
"v_mfma_f32_16x16x32_bf16 v[36:39], a[244:247], v[196:199], v[36:39]\n" \
"s_barrier\n" \
"ds_read_b128 a[240:243], v247 offset:38912\n" \
"v_add_f32 v243, v243, v127\n" \
"v_mfma_f32_16x16x32_bf16 a[64:67], a[192:195], v[64:67], a[64:67]\n" \
"v_exp_f32 v0, v0\n" \
"v_mfma_f32_16x16x32_bf16 a[64:67], a[196:199], v[96:99], a[64:67]\n" \
"v_cvt_pk_bf16_f32 v123, v126, v127\n" \
"v_add_f32 v244, v244, v0\n" \
"v_exp_f32 v1, v1\n" \
"v_mfma_f32_16x16x32_bf16 a[96:99], a[192:195], v[72:75], a[96:99]\n" \
"s_xor_b32 m0, s73, 0x8000\n" \
"ds_read_b128 a[244:247], v247 offset:47104\n" \
"v_add_f32 v244, v244, v1\n" \
"v_mfma_f32_16x16x32_bf16 a[96:99], a[196:199], v[104:107], a[96:99]\n" \
"ds_read_b128 a[248:251], v247 offset:39936\n" \
"global_load_lds_dwordx4 v250, s[36:37] offset:-4096 sc1\n" \
"v_mfma_f32_16x16x32_bf16 v[128:131], a[192:195], v[80:83], v[128:131]\n" \
"ds_read_b128 a[252:255], v247 offset:48128\n" \
"v_exp_f32 v2, v2\n" \
"v_mfma_f32_16x16x32_bf16 v[128:131], a[196:199], v[112:115], v[128:131]\n" \
"v_add_f32 v244, v244, v2\n" \
"v_cvt_pk_bf16_f32 v0, v0, v1\n" \
"v_mfma_f32_16x16x32_bf16 v[160:163], a[192:195], v[88:91], v[160:163]\n" \
"v_exp_f32 v3, v3\n" \
"v_mfma_f32_16x16x32_bf16 v[160:163], a[196:199], v[120:123], v[160:163]\n" \
"ds_read_b128 a[192:195], v252 offset:0\n" \
"global_load_lds_dwordx4 v250, s[36:37] offset:-3072 sc1\n" \
"v_add_f32 v244, v244, v3\n" \
"v_mfma_f32_16x16x32_bf16 v[164:167], a[200:203], v[88:91], v[164:167]\n" \
"v_exp_f32 v4, v4\n" \
"v_mfma_f32_16x16x32_bf16 v[164:167], a[204:207], v[120:123], v[164:167]\n" \
"v_add_f32 v244, v244, v4\n" \
"v_cvt_pk_bf16_f32 v1, v2, v3\n" \
"v_mfma_f32_16x16x32_bf16 v[132:135], a[200:203], v[80:83], v[132:135]\n" \
"v_exp_f32 v5, v5\n" \
"v_mfma_f32_16x16x32_bf16 v[132:135], a[204:207], v[112:115], v[132:135]\n" \
"ds_read_b128 a[196:199], v252 offset:2048\n" \
"v_add_f32 v244, v244, v5\n" \
"v_mfma_f32_16x16x32_bf16 a[100:103], a[200:203], v[72:75], a[100:103]\n" \
"v_exp_f32 v6, v6\n" \
"v_mfma_f32_16x16x32_bf16 a[100:103], a[204:207], v[104:107], a[100:103]\n" \
"v_add_f32 v244, v244, v6\n" \
"v_cvt_pk_bf16_f32 v2, v4, v5\n" \
"v_mfma_f32_16x16x32_bf16 a[68:71], a[200:203], v[64:67], a[68:71]\n" \
"v_exp_f32 v7, v7\n" \
"v_mfma_f32_16x16x32_bf16 a[68:71], a[204:207], v[96:99], a[68:71]\n" \
"ds_read_b128 a[200:203], v252 offset:4096\n" \
"global_load_lds_dwordx4 v250, s[36:37] offset:-2048 sc1\n" \
"v_add_f32 v244, v244, v7\n" \
"v_mfma_f32_16x16x32_bf16 a[72:75], a[208:211], v[64:67], a[72:75]\n" \
"v_exp_f32 v8, v8\n" \
"v_mfma_f32_16x16x32_bf16 a[72:75], a[212:215], v[96:99], a[72:75]\n" \
"v_add_f32 v245, v245, v8\n" \
"v_cvt_pk_bf16_f32 v3, v6, v7\n" \
"v_mfma_f32_16x16x32_bf16 a[104:107], a[208:211], v[72:75], a[104:107]\n" \
"v_exp_f32 v9, v9\n" \
"v_mfma_f32_16x16x32_bf16 a[104:107], a[212:215], v[104:107], a[104:107]\n" \
"ds_read_b128 a[204:207], v252 offset:6144\n" \
"global_load_lds_dwordx4 v250, s[36:37] offset:-1024 sc1\n" \
"v_add_f32 v245, v245, v9\n" \
"v_mfma_f32_16x16x32_bf16 v[136:139], a[208:211], v[80:83], v[136:139]\n" \
"v_exp_f32 v10, v10\n" \
"v_mfma_f32_16x16x32_bf16 v[136:139], a[212:215], v[112:115], v[136:139]\n" \
"v_add_f32 v245, v245, v10\n" \
"v_cvt_pk_bf16_f32 v8, v8, v9\n" \
"v_mfma_f32_16x16x32_bf16 v[168:171], a[208:211], v[88:91], v[168:171]\n" \
"v_exp_f32 v11, v11\n" \
"v_mfma_f32_16x16x32_bf16 v[168:171], a[212:215], v[120:123], v[168:171]\n" \
"ds_read_b128 a[208:211], v252 offset:256\n" \
"v_add_f32 v245, v245, v11\n" \
"v_mfma_f32_16x16x32_bf16 v[172:175], a[216:219], v[88:91], v[172:175]\n" \
"v_exp_f32 v12, v12\n" \
"v_mfma_f32_16x16x32_bf16 v[172:175], a[220:223], v[120:123], v[172:175]\n" \
"v_add_f32 v245, v245, v12\n" \
"v_cvt_pk_bf16_f32 v9, v10, v11\n" \
"v_mfma_f32_16x16x32_bf16 v[140:143], a[216:219], v[80:83], v[140:143]\n" \
"v_exp_f32 v13, v13\n" \
"v_mfma_f32_16x16x32_bf16 v[140:143], a[220:223], v[112:115], v[140:143]\n" \
"ds_read_b128 a[212:215], v252 offset:2304\n" \
"global_load_lds_dwordx4 v250, s[36:37] offset:0 sc1\n" \
"v_add_f32 v245, v245, v13\n" \
"v_mfma_f32_16x16x32_bf16 a[108:111], a[216:219], v[72:75], a[108:111]\n" \
"v_exp_f32 v14, v14\n" \
"v_mfma_f32_16x16x32_bf16 a[108:111], a[220:223], v[104:107], a[108:111]\n" \
"v_add_f32 v245, v245, v14\n" \
"v_cvt_pk_bf16_f32 v10, v12, v13\n" \
"v_mfma_f32_16x16x32_bf16 a[76:79], a[216:219], v[64:67], a[76:79]\n" \
"s_waitcnt lgkmcnt(6)\n" \
"v_exp_f32 v15, v15\n" \
"v_mfma_f32_16x16x32_bf16 a[76:79], a[220:223], v[96:99], a[76:79]\n" \
"ds_read_b128 a[216:219], v252 offset:4352\n" \
"global_load_lds_dwordx4 v250, s[36:37] offset:1024 sc1\n" \
"v_add_f32 v245, v245, v15\n" \
"v_mfma_f32_16x16x32_bf16 a[80:83], a[224:227], v[64:67], a[80:83]\n" \
"v_exp_f32 v16, v16\n" \
"v_mfma_f32_16x16x32_bf16 a[80:83], a[228:231], v[96:99], a[80:83]\n" \
"v_add_f32 v242, v242, v16\n" \
"v_cvt_pk_bf16_f32 v11, v14, v15\n" \
"v_mfma_f32_16x16x32_bf16 a[112:115], a[224:227], v[72:75], a[112:115]\n" \
"v_exp_f32 v17, v17\n" \
"v_mfma_f32_16x16x32_bf16 a[112:115], a[228:231], v[104:107], a[112:115]\n" \
"ds_read_b128 a[220:223], v252 offset:6400\n" \
"v_add_f32 v242, v242, v17\n" \
"v_mfma_f32_16x16x32_bf16 v[144:147], a[224:227], v[80:83], v[144:147]\n" \
"v_exp_f32 v18, v18\n" \
"v_mfma_f32_16x16x32_bf16 v[144:147], a[228:231], v[112:115], v[144:147]\n" \
"v_add_f32 v242, v242, v18\n" \
"v_cvt_pk_bf16_f32 v16, v16, v17\n" \
"v_mfma_f32_16x16x32_bf16 v[176:179], a[224:227], v[88:91], v[176:179]\n" \
"v_exp_f32 v19, v19\n" \
"v_mfma_f32_16x16x32_bf16 v[176:179], a[228:231], v[120:123], v[176:179]\n" \
"ds_read_b128 a[224:227], v252 offset:8192\n" \
"global_load_lds_dwordx4 v250, s[36:37] offset:2048 sc1\n" \
"v_add_f32 v242, v242, v19\n" \
"v_mfma_f32_16x16x32_bf16 v[180:183], a[232:235], v[88:91], v[180:183]\n" \
"v_exp_f32 v20, v20\n" \
"v_mfma_f32_16x16x32_bf16 v[180:183], a[236:239], v[120:123], v[180:183]\n" \
"v_add_f32 v242, v242, v20\n" \
"v_cvt_pk_bf16_f32 v17, v18, v19\n" \
"v_mfma_f32_16x16x32_bf16 v[148:151], a[232:235], v[80:83], v[148:151]\n" \
"v_exp_f32 v21, v21\n" \
"v_mfma_f32_16x16x32_bf16 v[148:151], a[236:239], v[112:115], v[148:151]\n" \
"ds_read_b128 a[228:231], v252 offset:10240\n" \
"global_load_lds_dwordx4 v250, s[36:37] offset:3072 sc1\n" \
"v_add_f32 v242, v242, v21\n" \
"v_mfma_f32_16x16x32_bf16 a[116:119], a[232:235], v[72:75], a[116:119]\n" \
"s_add_u32 s36, s36, 34816\n" \
"v_exp_f32 v22, v22\n" \
"v_mfma_f32_16x16x32_bf16 a[116:119], a[236:239], v[104:107], a[116:119]\n" \
"v_add_f32 v242, v242, v22\n" \
"v_cvt_pk_bf16_f32 v18, v20, v21\n" \
"v_mfma_f32_16x16x32_bf16 a[84:87], a[232:235], v[64:67], a[84:87]\n" \
"v_exp_f32 v23, v23\n" \
"v_mfma_f32_16x16x32_bf16 a[84:87], a[236:239], v[96:99], a[84:87]\n" \
"ds_read_b128 a[232:235], v252 offset:12288\n" \
"v_add_f32 v242, v242, v23\n" \
"v_mfma_f32_16x16x32_bf16 a[88:91], a[240:243], v[64:67], a[88:91]\n" \
"v_exp_f32 v24, v24\n" \
"v_mfma_f32_16x16x32_bf16 a[88:91], a[244:247], v[96:99], a[88:91]\n" \
"v_add_f32 v243, v243, v24\n" \
"v_cvt_pk_bf16_f32 v19, v22, v23\n" \
"v_mfma_f32_16x16x32_bf16 a[120:123], a[240:243], v[72:75], a[120:123]\n" \
"v_exp_f32 v25, v25\n" \
"v_mfma_f32_16x16x32_bf16 a[120:123], a[244:247], v[104:107], a[120:123]\n" \
"ds_read_b128 a[236:239], v252 offset:14336\n" \
"v_add_f32 v243, v243, v25\n" \
"v_mfma_f32_16x16x32_bf16 v[152:155], a[240:243], v[80:83], v[152:155]\n" \
"v_exp_f32 v26, v26\n" \
"v_mfma_f32_16x16x32_bf16 v[152:155], a[244:247], v[112:115], v[152:155]\n" \
"v_add_f32 v243, v243, v26\n" \
"v_cvt_pk_bf16_f32 v24, v24, v25\n" \
"v_mfma_f32_16x16x32_bf16 v[184:187], a[240:243], v[88:91], v[184:187]\n" \
"v_exp_f32 v27, v27\n" \
"v_mfma_f32_16x16x32_bf16 v[184:187], a[244:247], v[120:123], v[184:187]\n" \
"ds_read_b128 a[240:243], v252 offset:8448\n" \
"v_add_f32 v243, v243, v27\n" \
"v_mfma_f32_16x16x32_bf16 v[188:191], a[248:251], v[88:91], v[188:191]\n" \
"v_exp_f32 v28, v28\n" \
"v_mfma_f32_16x16x32_bf16 v[188:191], a[252:255], v[120:123], v[188:191]\n" \
"v_add_f32 v243, v243, v28\n" \
"v_cvt_pk_bf16_f32 v25, v26, v27\n" \
"v_mfma_f32_16x16x32_bf16 v[156:159], a[248:251], v[80:83], v[156:159]\n" \
"v_exp_f32 v29, v29\n" \
"v_mfma_f32_16x16x32_bf16 v[156:159], a[252:255], v[112:115], v[156:159]\n" \
"ds_read_b128 a[244:247], v252 offset:10496\n" \
"v_add_f32 v243, v243, v29\n" \
"v_mfma_f32_16x16x32_bf16 a[124:127], a[248:251], v[72:75], a[124:127]\n" \
"v_exp_f32 v30, v30\n" \
"v_mfma_f32_16x16x32_bf16 a[124:127], a[252:255], v[104:107], a[124:127]\n" \
"v_add_f32 v243, v243, v30\n" \
"v_cvt_pk_bf16_f32 v26, v28, v29\n" \
"v_mfma_f32_16x16x32_bf16 a[92:95], a[248:251], v[64:67], a[92:95]\n" \
"s_waitcnt vmcnt(8) lgkmcnt(6)\n" \
"v_exp_f32 v31, v31\n" \
"v_mfma_f32_16x16x32_bf16 a[92:95], a[252:255], v[96:99], a[92:95]\n" \
"s_barrier\n" \
"ds_read_b128 a[248:251], v252 offset:12544\n" \
"v_add_f32 v243, v243, v31\n" \
"LA1g_%=:\n" \
"Lpg7666_%=:\n" \
"s_cbranch_execz Lcb0_%=\n" \
"v_mfma_f32_16x16x32_bf16 v[64:67], a[192:195], v[192:195], v[224:227]\n" \
"v_exp_f32 v32, v32\n" \
"v_mfma_f32_16x16x32_bf16 v[64:67], a[196:199], v[196:199], v[64:67]\n" \
"v_cvt_pk_bf16_f32 v27, v30, v31\n" \
"v_add_f32 v244, v244, v32\n" \
"v_mfma_f32_16x16x32_bf16 v[64:67], a[200:203], v[200:203], v[64:67]\n" \
"s_add_u32 m0, s74, 0x8000\n" \
"v_exp_f32 v33, v33\n" \
"v_mfma_f32_16x16x32_bf16 v[64:67], a[204:207], v[204:207], v[64:67]\n" \
"ds_read_b128 a[252:255], v252 offset:14592\n" \
"global_load_lds_dwordx4 v250, s[38:39] offset:-4096 sc1\n" \
"v_add_f32 v244, v244, v33\n" \
"v_mfma_f32_16x16x32_bf16 v[72:75], a[200:203], v[216:219], v[228:231]\n" \
"v_exp_f32 v34, v34\n" \
"v_mfma_f32_16x16x32_bf16 v[72:75], a[204:207], v[220:223], v[72:75]\n" \
"v_add_f32 v244, v244, v34\n" \
"v_cvt_pk_bf16_f32 v32, v32, v33\n" \
"v_mfma_f32_16x16x32_bf16 v[72:75], a[192:195], v[208:211], v[72:75]\n" \
"v_exp_f32 v35, v35\n" \
"v_mfma_f32_16x16x32_bf16 v[72:75], a[196:199], v[212:215], v[72:75]\n" \
"global_load_lds_dwordx4 v250, s[38:39] offset:-3072 sc1\n" \
"v_add_f32 v244, v244, v35\n" \
"v_mfma_f32_16x16x32_bf16 v[80:83], a[192:195], a[160:163], v[232:235]\n" \
"v_exp_f32 v36, v36\n" \
"v_mfma_f32_16x16x32_bf16 v[80:83], a[196:199], a[164:167], v[80:83]\n" \
"v_add_f32 v244, v244, v36\n" \
"v_cvt_pk_bf16_f32 v33, v34, v35\n" \
"v_mfma_f32_16x16x32_bf16 v[80:83], a[200:203], a[168:171], v[80:83]\n" \
"v_exp_f32 v37, v37\n" \
"v_mfma_f32_16x16x32_bf16 v[80:83], a[204:207], a[172:175], v[80:83]\n" \
"v_add_f32 v244, v244, v37\n" \
"v_exp_f32 v38, v38\n" \
"v_mfma_f32_16x16x32_bf16 v[88:91], a[200:203], a[184:187], v[236:239]\n" \
"v_add_f32 v244, v244, v38\n" \
"v_cvt_pk_bf16_f32 v34, v36, v37\n" \
"v_mfma_f32_16x16x32_bf16 v[88:91], a[204:207], a[188:191], v[88:91]\n" \
"v_exp_f32 v39, v39\n" \
"v_mfma_f32_16x16x32_bf16 v[88:91], a[192:195], a[176:179], v[88:91]\n" \
"v_add_f32 v244, v244, v39\n" \
"v_mfma_f32_16x16x32_bf16 v[88:91], a[196:199], a[180:183], v[88:91]\n" \
"ds_read_b128 a[192:195], v247 offset:0\n" \
"global_load_lds_dwordx4 v250, s[38:39] offset:-2048 sc1\n" \
"v_mfma_f32_16x16x32_bf16 v[92:95], a[208:211], a[176:179], v[236:239]\n" \
"v_exp_f32 v40, v40\n" \
"v_mfma_f32_16x16x32_bf16 v[92:95], a[212:215], a[180:183], v[92:95]\n" \
"v_add_f32 v245, v245, v40\n" \
"v_cvt_pk_bf16_f32 v35, v38, v39\n" \
"v_mfma_f32_16x16x32_bf16 v[92:95], a[216:219], a[184:187], v[92:95]\n" \
"v_exp_f32 v41, v41\n" \
"v_mfma_f32_16x16x32_bf16 v[92:95], a[220:223], a[188:191], v[92:95]\n" \
"ds_read_b128 a[196:199], v247 offset:8192\n" \
"global_load_lds_dwordx4 v250, s[38:39] offset:-1024 sc1\n" \
"v_add_f32 v245, v245, v41\n" \
"v_mfma_f32_16x16x32_bf16 v[84:87], a[216:219], a[168:171], v[232:235]\n" \
"v_exp_f32 v42, v42\n" \
"v_mfma_f32_16x16x32_bf16 v[84:87], a[220:223], a[172:175], v[84:87]\n" \
"v_add_f32 v245, v245, v42\n" \
"v_cvt_pk_bf16_f32 v40, v40, v41\n" \
"v_mfma_f32_16x16x32_bf16 v[84:87], a[208:211], a[160:163], v[84:87]\n" \
"v_exp_f32 v43, v43\n" \
"v_mfma_f32_16x16x32_bf16 v[84:87], a[212:215], a[164:167], v[84:87]\n" \
"ds_read_b128 a[200:203], v247 offset:1024\n" \
"v_add_f32 v245, v245, v43\n" \
"v_mfma_f32_16x16x32_bf16 v[76:79], a[208:211], v[208:211], v[228:231]\n" \
"v_exp_f32 v44, v44\n" \
"v_mfma_f32_16x16x32_bf16 v[76:79], a[212:215], v[212:215], v[76:79]\n" \
"v_add_f32 v245, v245, v44\n" \
"v_cvt_pk_bf16_f32 v41, v42, v43\n" \
"v_mfma_f32_16x16x32_bf16 v[76:79], a[216:219], v[216:219], v[76:79]\n" \
"v_exp_f32 v45, v45\n" \
"v_mfma_f32_16x16x32_bf16 v[76:79], a[220:223], v[220:223], v[76:79]\n" \
"ds_read_b128 a[204:207], v247 offset:9216\n" \
"global_load_lds_dwordx4 v250, s[38:39] offset:0 sc1\n" \
"v_add_f32 v245, v245, v45\n" \
"v_mfma_f32_16x16x32_bf16 v[68:71], a[216:219], v[200:203], v[224:227]\n" \
"v_exp_f32 v46, v46\n" \
"v_mfma_f32_16x16x32_bf16 v[68:71], a[220:223], v[204:207], v[68:71]\n" \
"v_add_f32 v245, v245, v46\n" \
"v_cvt_pk_bf16_f32 v42, v44, v45\n" \
"v_mfma_f32_16x16x32_bf16 v[68:71], a[208:211], v[192:195], v[68:71]\n" \
"s_waitcnt lgkmcnt(4)\n" \
"v_exp_f32 v47, v47\n" \
"v_mfma_f32_16x16x32_bf16 v[68:71], a[212:215], v[196:199], v[68:71]\n" \
"ds_read_b128 a[208:211], v247 offset:2048\n" \
"global_load_lds_dwordx4 v250, s[38:39] offset:1024 sc1\n" \
"v_add_f32 v245, v245, v47\n" \
"v_mfma_f32_16x16x32_bf16 v[96:99], a[224:227], v[192:195], v[224:227]\n" \
"v_exp_f32 v48, v48\n" \
"v_mfma_f32_16x16x32_bf16 v[96:99], a[228:231], v[196:199], v[96:99]\n" \
"v_add_f32 v242, v242, v48\n" \
"v_cvt_pk_bf16_f32 v43, v46, v47\n" \
"v_mfma_f32_16x16x32_bf16 v[96:99], a[232:235], v[200:203], v[96:99]\n" \
"v_exp_f32 v49, v49\n" \
"v_mfma_f32_16x16x32_bf16 v[96:99], a[236:239], v[204:207], v[96:99]\n" \
"ds_read_b128 a[212:215], v247 offset:10240\n" \
"v_add_f32 v242, v242, v49\n" \
"v_mfma_f32_16x16x32_bf16 v[104:107], a[232:235], v[216:219], v[228:231]\n" \
"v_exp_f32 v50, v50\n" \
"v_mfma_f32_16x16x32_bf16 v[104:107], a[236:239], v[220:223], v[104:107]\n" \
"v_add_f32 v242, v242, v50\n" \
"v_cvt_pk_bf16_f32 v48, v48, v49\n" \
"v_mfma_f32_16x16x32_bf16 v[104:107], a[224:227], v[208:211], v[104:107]\n" \
"v_exp_f32 v51, v51\n" \
"v_mfma_f32_16x16x32_bf16 v[104:107], a[228:231], v[212:215], v[104:107]\n" \
"ds_read_b128 a[216:219], v247 offset:3072\n" \
"global_load_lds_dwordx4 v250, s[38:39] offset:2048 sc1\n" \
"v_add_f32 v242, v242, v51\n" \
"v_mfma_f32_16x16x32_bf16 v[112:115], a[224:227], a[160:163], v[232:235]\n" \
"v_exp_f32 v52, v52\n" \
"v_mfma_f32_16x16x32_bf16 v[112:115], a[228:231], a[164:167], v[112:115]\n" \
"v_add_f32 v242, v242, v52\n" \
"v_cvt_pk_bf16_f32 v49, v50, v51\n" \
"v_mfma_f32_16x16x32_bf16 v[112:115], a[232:235], a[168:171], v[112:115]\n" \
"v_exp_f32 v53, v53\n" \
"v_mfma_f32_16x16x32_bf16 v[112:115], a[236:239], a[172:175], v[112:115]\n" \
"ds_read_b128 a[220:223], v247 offset:11264\n" \
"global_load_lds_dwordx4 v250, s[38:39] offset:3072 sc1\n" \
"v_add_f32 v242, v242, v53\n" \
"v_exp_f32 v54, v54\n" \
"v_mfma_f32_16x16x32_bf16 v[120:123], a[232:235], a[184:187], v[236:239]\n" \
"s_add_u32 s38, s38, 34816\n" \
"s_sub_u32 s76, s76, 1\n" \
"s_cmp_lg_u32 s76, 0\n" \
"v_mfma_f32_16x16x32_bf16 v[120:123], a[236:239], a[188:191], v[120:123]\n" \
"v_add_f32 v242, v242, v54\n" \
"v_cvt_pk_bf16_f32 v50, v52, v53\n" \
"v_mfma_f32_16x16x32_bf16 v[120:123], a[224:227], a[176:179], v[120:123]\n" \
"v_exp_f32 v55, v55\n" \
"v_mfma_f32_16x16x32_bf16 v[120:123], a[228:231], a[180:183], v[120:123]\n" \
"ds_read_b128 a[224:227], v247 offset:4096\n" \
"v_add_f32 v242, v242, v55\n" \
"v_mfma_f32_16x16x32_bf16 v[124:127], a[240:243], a[176:179], v[236:239]\n" \
"v_exp_f32 v56, v56\n" \
"v_mfma_f32_16x16x32_bf16 v[124:127], a[244:247], a[180:183], v[124:127]\n" \
"v_add_f32 v243, v243, v56\n" \
"v_cvt_pk_bf16_f32 v51, v54, v55\n" \
"v_mfma_f32_16x16x32_bf16 v[124:127], a[248:251], a[184:187], v[124:127]\n" \
"v_exp_f32 v57, v57\n" \
"v_mfma_f32_16x16x32_bf16 v[124:127], a[252:255], a[188:191], v[124:127]\n" \
"ds_read_b128 a[228:231], v247 offset:12288\n" \
"v_add_f32 v243, v243, v57\n" \
"v_mfma_f32_16x16x32_bf16 v[116:119], a[248:251], a[168:171], v[232:235]\n" \
"v_exp_f32 v58, v58\n" \
"v_mfma_f32_16x16x32_bf16 v[116:119], a[252:255], a[172:175], v[116:119]\n" \
"v_add_f32 v243, v243, v58\n" \
"v_cvt_pk_bf16_f32 v56, v56, v57\n" \
"v_mfma_f32_16x16x32_bf16 v[116:119], a[240:243], a[160:163], v[116:119]\n" \
"v_exp_f32 v59, v59\n" \
"v_mfma_f32_16x16x32_bf16 v[116:119], a[244:247], a[164:167], v[116:119]\n" \
"ds_read_b128 a[232:235], v247 offset:5120\n" \
"v_add_f32 v243, v243, v59\n" \
"v_mfma_f32_16x16x32_bf16 v[108:111], a[240:243], v[208:211], v[228:231]\n" \
"v_exp_f32 v60, v60\n" \
"v_mfma_f32_16x16x32_bf16 v[108:111], a[244:247], v[212:215], v[108:111]\n" \
"v_add_f32 v243, v243, v60\n" \
"v_cvt_pk_bf16_f32 v57, v58, v59\n" \
"v_mfma_f32_16x16x32_bf16 v[108:111], a[248:251], v[216:219], v[108:111]\n" \
"v_exp_f32 v61, v61\n" \
"v_mfma_f32_16x16x32_bf16 v[108:111], a[252:255], v[220:223], v[108:111]\n" \
"ds_read_b128 a[236:239], v247 offset:13312\n" \
"v_add_f32 v243, v243, v61\n" \
"v_mfma_f32_16x16x32_bf16 v[100:103], a[248:251], v[200:203], v[224:227]\n" \
"v_exp_f32 v62, v62\n" \
"v_mfma_f32_16x16x32_bf16 v[100:103], a[252:255], v[204:207], v[100:103]\n" \
"v_add_f32 v243, v243, v62\n" \
"v_cvt_pk_bf16_f32 v58, v60, v61\n" \
"v_mfma_f32_16x16x32_bf16 v[100:103], a[240:243], v[192:195], v[100:103]\n" \
"s_waitcnt vmcnt(8) lgkmcnt(4)\n" \
"v_exp_f32 v63, v63\n" \
"v_mfma_f32_16x16x32_bf16 v[100:103], a[244:247], v[196:199], v[100:103]\n" \
"s_barrier\n" \
"ds_read_b128 a[240:243], v247 offset:6144\n" \
"v_add_f32 v243, v243, v63\n" \
"s_cbranch_scc1 Lloop2_%=\n" \
"v_mfma_f32_16x16x32_bf16 a[64:67], a[192:195], v[0:3], a[64:67]\n" \
"v_subrev_u32 v246, s82, v246\n" \
"v_exp_f32 v64, v64\n" \
"v_exp_f32 v65, v65\n" \
"v_add_f32 v244, v244, v64\n" \
"v_exp_f32 v66, v66\n" \
"v_add_f32 v244, v244, v65\n" \
"v_cvt_pk_bf16_f32 v64, v64, v65\n" \
"v_mfma_f32_16x16x32_bf16 a[64:67], a[196:199], v[32:35], a[64:67]\n" \
"s_memrealtime s[44:45]\n" \
"v_cvt_pk_bf16_f32 v59, v62, v63\n" \
"v_exp_f32 v67, v67\n" \
"v_mfma_f32_16x16x32_bf16 a[96:99], a[192:195], v[8:11], a[96:99]\n" \
"ds_read_b128 a[244:247], v247 offset:14336\n" \
"v_add_f32 v244, v244, v66\n" \
"v_mfma_f32_16x16x32_bf16 a[96:99], a[196:199], v[40:43], a[96:99]\n" \
"ds_read_b128 a[248:251], v247 offset:7168\n" \
"v_mov_b32 v208, v241\n" \
"v_exp_f32 v68, v68\n" \
"v_add_f32 v244, v244, v67\n" \
"v_mfma_f32_16x16x32_bf16 v[128:131], a[192:195], v[16:19], v[128:131]\n" \
"ds_read_b128 a[252:255], v247 offset:15360\n" \
"v_cvt_pk_bf16_f32 v65, v66, v67\n" \
"v_mfma_f32_16x16x32_bf16 v[128:131], a[196:199], v[48:51], v[128:131]\n" \
"v_exp_f32 v69, v69\n" \
"v_add_f32 v244, v244, v68\n" \
"v_permlane16_swap_b32 v208, v241\n" \
"v_add_f32 v241, v241, v208\n" \
"v_mov_b32 v208, v241\n" \
"v_mfma_f32_16x16x32_bf16 v[160:163], a[192:195], v[24:27], v[160:163]\n" \
"v_exp_f32 v70, v70\n" \
"v_mfma_f32_16x16x32_bf16 v[160:163], a[196:199], v[56:59], v[160:163]\n" \
"ds_read_b128 a[192:195], v246 offset:0\n" \
"v_add_f32 v244, v244, v69\n" \
"v_cvt_pk_bf16_f32 v66, v68, v69\n" \
"v_permlane32_swap_b32 v208, v241\n" \
"v_add_f32 v241, v241, v208\n" \
"v_rcp_f32 v208, v241\n" \
"v_mul_f32 v241, 0x4d800000, v241\n" \
"v_add_u32 v209, 0x1c90080, v249\n" \
"v_cndmask_b32 v209, v249, v209, s[16:17]\n" \
"v_mfma_f32_16x16x32_bf16 v[164:167], a[200:203], v[24:27], v[164:167]\n" \
"s_add_u32 s77, s77, 532480\n" \
"v_exp_f32 v71, v71\n" \
"v_mfma_f32_16x16x32_bf16 v[164:167], a[204:207], v[56:59], v[164:167]\n" \
"s_waitcnt lgkmcnt(0)\n" \
"v_accvgpr_read_b32 v192, a32\n" \
"v_accvgpr_read_b32 v193, a33\n" \
"v_accvgpr_read_b32 v194, a34\n" \
"v_accvgpr_read_b32 v195, a35\n" \
"v_accvgpr_read_b32 v196, a36\n" \
"v_accvgpr_read_b32 v197, a37\n" \
"v_accvgpr_read_b32 v198, a38\n" \
"v_accvgpr_read_b32 v199, a39\n" \
"v_add_f32 v244, v244, v70\n" \
"v_exp_f32 v72, v72\n" \
"v_pk_mul_f32 v[192:193], v[192:193], v[208:209] op_sel_hi:[1,0]\n" \
"v_pk_mul_f32 v[194:195], v[194:195], v[208:209] op_sel_hi:[1,0]\n" \
"v_pk_mul_f32 v[196:197], v[196:197], v[208:209] op_sel_hi:[1,0]\n" \
"v_pk_mul_f32 v[198:199], v[198:199], v[208:209] op_sel_hi:[1,0]\n" \
"v_cvt_pk_bf16_f32 v200, v192, v193\n" \
"v_cvt_pk_bf16_f32 v201, v194, v195\n" \
"v_cvt_pk_bf16_f32 v202, v196, v197\n" \
"v_cvt_pk_bf16_f32 v203, v198, v199\n" \
"v_pk_mul_f32 v[192:193], v[192:193], v[194:195]\n" \
"v_pk_fma_f32 v[192:193], v[196:197], v[198:199], v[192:193]\n" \
"v_fma_f32 v241, v192, v193, v241\n" \
"v_mfma_f32_16x16x32_bf16 v[132:135], a[200:203], v[16:19], v[132:135]\n" \
"s_barrier\n" \
"v_mfma_f32_16x16x32_bf16 v[132:135], a[204:207], v[48:51], v[132:135]\n" \
"s_and_b32 s46, %1, 7\n" \
"ds_read_b128 a[196:199], v246 offset:2048\n" \
"s_lshl_b32 s46, s46, 2\n" \
"v_mfma_f32_16x16x32_bf16 a[100:103], a[200:203], v[8:11], a[100:103]\n" \
"s_lshr_b32 s47, %1, 6\n" \
"s_add_u32 s46, s46, s47\n" \
"s_lshl_b32 s46, s46, 3\n" \
"v_mfma_f32_16x16x32_bf16 a[100:103], a[204:207], v[40:43], a[100:103]\n" \
"s_add_u32 s46, s46, 65593856\n" \
"s_sub_u32 s45, s44, s12\n" \
"s_mul_i32 s45, s45, 3\n" \
"v_mfma_f32_16x16x32_bf16 a[68:71], a[200:203], v[0:3], a[68:71]\n" \
"s_lshl_b32 s44, s44, 1\n" \
"s_add_u32 s44, s44, s45\n" \
"v_mov_b32 v211, s44\n" \
"v_mfma_f32_16x16x32_bf16 a[68:71], a[204:207], v[32:35], a[68:71]\n" \
"s_mov_b32 m0, s74\n" \
"ds_read_b128 a[200:203], v246 offset:4096\n" \
"v_xor_b32 v210, s60, v211\n" \
"v_mfma_f32_16x16x32_bf16 a[72:75], a[208:211], v[0:3], a[72:75]\n" \
"global_load_lds_dwordx4 v250, s[38:39] offset:-4096 sc1\n" \
"v_lshlrev_b32 v212, 28, s61\n" \
"v_mfma_f32_16x16x32_bf16 a[72:75], a[212:215], v[32:35], a[72:75]\n" \
"s_cmp_eq_u32 s63, 3\n" \
"s_cselect_b32 s46, s46, 0x20000000\n" \
"v_add_f32 v244, v244, v71\n" \
"v_mfma_f32_16x16x32_bf16 a[104:107], a[208:211], v[8:11], a[104:107]\n" \
"global_load_lds_dwordx4 v250, s[38:39] offset:-3072 sc1\n" \
"global_load_lds_dwordx4 v250, s[38:39] offset:-2048 sc1\n" \
"v_mfma_f32_16x16x32_bf16 a[104:107], a[212:215], v[40:43], a[104:107]\n" \
"ds_read_b128 a[204:207], v246 offset:6144\n" \
"v_cvt_pk_bf16_f32 v67, v70, v71\n" \
"v_mfma_f32_16x16x32_bf16 v[136:139], a[208:211], v[16:19], v[136:139]\n" \
"v_exp_f32 v73, v73\n" \
"v_mfma_f32_16x16x32_bf16 v[136:139], a[212:215], v[48:51], v[136:139]\n" \
"global_load_lds_dwordx4 v250, s[38:39] offset:-1024 sc1\n" \
"v_add_f32 v245, v245, v72\n" \
"v_exp_f32 v74, v74\n" \
"v_mfma_f32_16x16x32_bf16 v[168:171], a[208:211], v[24:27], v[168:171]\n" \
"v_add_f32 v245, v245, v73\n" \
"v_cvt_pk_bf16_f32 v72, v72, v73\n" \
"v_mfma_f32_16x16x32_bf16 v[168:171], a[212:215], v[56:59], v[168:171]\n" \
"ds_read_b128 a[208:211], v246 offset:256\n" \
"v_exp_f32 v75, v75\n" \
"v_mfma_f32_16x16x32_bf16 v[172:175], a[216:219], v[24:27], v[172:175]\n" \
"global_load_lds_dwordx4 v250, s[38:39] offset:0 sc1\n" \
"v_add_f32 v245, v245, v74\n" \
"v_mfma_f32_16x16x32_bf16 v[172:175], a[220:223], v[56:59], v[172:175]\n" \
"v_exp_f32 v76, v76\n" \
"v_mfma_f32_16x16x32_bf16 v[140:143], a[216:219], v[16:19], v[140:143]\n" \
"v_add_f32 v245, v245, v75\n" \
"v_cvt_pk_bf16_f32 v73, v74, v75\n" \
"v_mfma_f32_16x16x32_bf16 v[140:143], a[220:223], v[48:51], v[140:143]\n" \
"ds_read_b128 a[212:215], v246 offset:2304\n" \
"global_load_lds_dwordx4 v250, s[38:39] offset:1024 sc1\n" \
"v_mfma_f32_16x16x32_bf16 a[108:111], a[216:219], v[8:11], a[108:111]\n" \
"v_exp_f32 v77, v77\n" \
"v_mfma_f32_16x16x32_bf16 a[108:111], a[220:223], v[40:43], a[108:111]\n" \
"v_add_f32 v245, v245, v76\n" \
"v_exp_f32 v78, v78\n" \
"v_add_f32 v245, v245, v77\n" \
"v_mfma_f32_16x16x32_bf16 a[76:79], a[216:219], v[0:3], a[76:79]\n" \
"s_waitcnt lgkmcnt(6)\n" \
"global_load_lds_dwordx4 v250, s[38:39] offset:2048 sc1\n" \
"v_cvt_pk_bf16_f32 v74, v76, v77\n" \
"v_mfma_f32_16x16x32_bf16 a[76:79], a[220:223], v[32:35], a[76:79]\n" \
"ds_read_b128 a[216:219], v246 offset:4352\n" \
"v_exp_f32 v79, v79\n" \
"v_add_f32 v245, v245, v78\n" \
"v_mfma_f32_16x16x32_bf16 a[80:83], a[224:227], v[0:3], a[80:83]\n" \
"v_exp_f32 v80, v80\n" \
"v_mfma_f32_16x16x32_bf16 a[80:83], a[228:231], v[32:35], a[80:83]\n" \
"global_load_lds_dwordx4 v250, s[38:39] offset:3072 sc1\n" \
"v_add_f32 v245, v245, v79\n" \
"v_cvt_pk_bf16_f32 v75, v78, v79\n" \
"v_mfma_f32_16x16x32_bf16 a[112:115], a[224:227], v[8:11], a[112:115]\n" \
"v_exp_f32 v81, v81\n" \
"v_mfma_f32_16x16x32_bf16 a[112:115], a[228:231], v[40:43], a[112:115]\n" \
"ds_read_b128 a[220:223], v246 offset:6400\n" \
"v_add_f32 v242, v242, v80\n" \
"v_mfma_f32_16x16x32_bf16 v[144:147], a[224:227], v[16:19], v[144:147]\n" \
"v_exp_f32 v82, v82\n" \
"v_mfma_f32_16x16x32_bf16 v[144:147], a[228:231], v[48:51], v[144:147]\n" \
"v_add_f32 v242, v242, v81\n" \
"v_cvt_pk_bf16_f32 v80, v80, v81\n" \
"v_mfma_f32_16x16x32_bf16 v[176:179], a[224:227], v[24:27], v[176:179]\n" \
"v_exp_f32 v83, v83\n" \
"v_mfma_f32_16x16x32_bf16 v[176:179], a[228:231], v[56:59], v[176:179]\n" \
"ds_read_b128 a[224:227], v246 offset:8192\n" \
"v_add_f32 v242, v242, v82\n" \
"v_exp_f32 v84, v84\n" \
"v_mfma_f32_16x16x32_bf16 v[180:183], a[232:235], v[24:27], v[180:183]\n" \
"v_add_f32 v242, v242, v83\n" \
"v_cvt_pk_bf16_f32 v81, v82, v83\n" \
"v_mfma_f32_16x16x32_bf16 v[180:183], a[236:239], v[56:59], v[180:183]\n" \
"buffer_store_dwordx4 v[200:203], v209, s[56:59], s77 offen offset:0 sc1\n" \
"buffer_store_dwordx2 v[210:211], v212, s[56:59], s46 offen sc1\n" \
"v_accvgpr_read_b32 v192, a40\n" \
"v_accvgpr_read_b32 v193, a41\n" \
"v_accvgpr_read_b32 v194, a42\n" \
"v_accvgpr_read_b32 v195, a43\n" \
"v_accvgpr_read_b32 v196, a44\n" \
"v_accvgpr_read_b32 v197, a45\n" \
"v_accvgpr_read_b32 v198, a46\n" \
"v_accvgpr_read_b32 v199, a47\n" \
"v_pk_mul_f32 v[192:193], v[192:193], v[208:209] op_sel_hi:[1,0]\n" \
"v_pk_mul_f32 v[194:195], v[194:195], v[208:209] op_sel_hi:[1,0]\n" \
"v_pk_mul_f32 v[196:197], v[196:197], v[208:209] op_sel_hi:[1,0]\n" \
"v_pk_mul_f32 v[198:199], v[198:199], v[208:209] op_sel_hi:[1,0]\n" \
"v_cvt_pk_bf16_f32 v204, v192, v193\n" \
"v_cvt_pk_bf16_f32 v205, v194, v195\n" \
"v_cvt_pk_bf16_f32 v206, v196, v197\n" \
"v_cvt_pk_bf16_f32 v207, v198, v199\n" \
"v_pk_mul_f32 v[192:193], v[192:193], v[194:195]\n" \
"v_pk_fma_f32 v[192:193], v[196:197], v[198:199], v[192:193]\n" \
"v_fma_f32 v241, v192, v193, v241\n" \
"v_mfma_f32_16x16x32_bf16 v[148:151], a[232:235], v[16:19], v[148:151]\n" \
"v_exp_f32 v85, v85\n" \
"v_mfma_f32_16x16x32_bf16 v[148:151], a[236:239], v[48:51], v[148:151]\n" \
"buffer_store_dwordx4 v[204:207], v209, s[56:59], s77 offen offset:2048 sc1\n" \
"s_add_u32 s77, s77, 0x1000\n" \
"ds_read_b128 a[228:231], v246 offset:10240\n" \
"v_add_f32 v242, v242, v84\n" \
"v_accvgpr_read_b32 v192, a48\n" \
"v_accvgpr_read_b32 v193, a49\n" \
"v_accvgpr_read_b32 v194, a50\n" \
"v_accvgpr_read_b32 v195, a51\n" \
"v_accvgpr_read_b32 v196, a52\n" \
"v_accvgpr_read_b32 v197, a53\n" \
"v_accvgpr_read_b32 v198, a54\n" \
"v_accvgpr_read_b32 v199, a55\n" \
"v_pk_mul_f32 v[192:193], v[192:193], v[208:209] op_sel_hi:[1,0]\n" \
"v_pk_mul_f32 v[194:195], v[194:195], v[208:209] op_sel_hi:[1,0]\n" \
"v_pk_mul_f32 v[196:197], v[196:197], v[208:209] op_sel_hi:[1,0]\n" \
"v_pk_mul_f32 v[198:199], v[198:199], v[208:209] op_sel_hi:[1,0]\n" \
"v_cvt_pk_bf16_f32 v200, v192, v193\n" \
"v_cvt_pk_bf16_f32 v201, v194, v195\n" \
"v_cvt_pk_bf16_f32 v202, v196, v197\n" \
"v_cvt_pk_bf16_f32 v203, v198, v199\n" \
"v_pk_mul_f32 v[192:193], v[192:193], v[194:195]\n" \
"v_pk_fma_f32 v[192:193], v[196:197], v[198:199], v[192:193]\n" \
"v_mfma_f32_16x16x32_bf16 a[116:119], a[232:235], v[8:11], a[116:119]\n" \
"v_exp_f32 v86, v86\n" \
"v_mfma_f32_16x16x32_bf16 a[116:119], a[236:239], v[40:43], a[116:119]\n" \
"v_fma_f32 v241, v192, v193, v241\n" \
"v_add_f32 v242, v242, v85\n" \
"v_cvt_pk_bf16_f32 v82, v84, v85\n" \
"v_mfma_f32_16x16x32_bf16 a[84:87], a[232:235], v[0:3], a[84:87]\n" \
"v_exp_f32 v87, v87\n" \
"v_mfma_f32_16x16x32_bf16 a[84:87], a[236:239], v[32:35], a[84:87]\n" \
"buffer_store_dwordx4 v[200:203], v209, s[56:59], s77 offen offset:0 sc1\n" \
"ds_read_b128 a[232:235], v246 offset:12288\n" \
"v_add_f32 v242, v242, v86\n" \
"v_accvgpr_read_b32 v192, a56\n" \
"v_accvgpr_read_b32 v193, a57\n" \
"v_accvgpr_read_b32 v194, a58\n" \
"v_accvgpr_read_b32 v195, a59\n" \
"v_accvgpr_read_b32 v196, a60\n" \
"v_accvgpr_read_b32 v197, a61\n" \
"v_accvgpr_read_b32 v198, a62\n" \
"v_accvgpr_read_b32 v199, a63\n" \
"v_pk_mul_f32 v[192:193], v[192:193], v[208:209] op_sel_hi:[1,0]\n" \
"v_pk_mul_f32 v[194:195], v[194:195], v[208:209] op_sel_hi:[1,0]\n" \
"v_pk_mul_f32 v[196:197], v[196:197], v[208:209] op_sel_hi:[1,0]\n" \
"v_pk_mul_f32 v[198:199], v[198:199], v[208:209] op_sel_hi:[1,0]\n" \
"v_cvt_pk_bf16_f32 v204, v192, v193\n" \
"v_cvt_pk_bf16_f32 v205, v194, v195\n" \
"v_cvt_pk_bf16_f32 v206, v196, v197\n" \
"v_cvt_pk_bf16_f32 v207, v198, v199\n" \
"v_pk_mul_f32 v[192:193], v[192:193], v[194:195]\n" \
"v_pk_fma_f32 v[192:193], v[196:197], v[198:199], v[192:193]\n" \
"v_fma_f32 v241, v192, v193, v241\n" \
"v_mfma_f32_16x16x32_bf16 a[88:91], a[240:243], v[0:3], a[88:91]\n" \
"v_exp_f32 v88, v88\n" \
"v_mfma_f32_16x16x32_bf16 a[88:91], a[244:247], v[32:35], a[88:91]\n" \
"buffer_store_dwordx4 v[204:207], v209, s[56:59], s77 offen offset:2048 sc1\n" \
"v_add_f32 v242, v242, v87\n" \
"v_cvt_pk_bf16_f32 v83, v86, v87\n" \
"s_sub_u32 s77, s77, 536576\n" \
"v_mfma_f32_16x16x32_bf16 a[120:123], a[240:243], v[8:11], a[120:123]\n" \
"v_exp_f32 v89, v89\n" \
"v_mfma_f32_16x16x32_bf16 a[120:123], a[244:247], v[40:43], a[120:123]\n" \
"Lpg8093_%=:\n" \
"Lst1x1_%=:\n" \
"s_cbranch_execz Lpg10428_%=\n" \
"ds_read_b128 a[236:239], v246 offset:14336\n" \
"v_add_f32 v243, v243, v88\n" \
"v_mfma_f32_16x16x32_bf16 v[152:155], a[240:243], v[16:19], v[152:155]\n" \
"v_exp_f32 v90, v90\n" \
"v_mfma_f32_16x16x32_bf16 v[152:155], a[244:247], v[48:51], v[152:155]\n" \
"v_add_f32 v243, v243, v89\n" \
"v_cvt_pk_bf16_f32 v88, v88, v89\n" \
"v_mfma_f32_16x16x32_bf16 v[184:187], a[240:243], v[24:27], v[184:187]\n" \
"v_exp_f32 v91, v91\n" \
"v_mfma_f32_16x16x32_bf16 v[184:187], a[244:247], v[56:59], v[184:187]\n" \
"ds_read_b128 a[240:243], v246 offset:8448\n" \
"v_add_f32 v243, v243, v90\n" \
"v_mfma_f32_16x16x32_bf16 v[188:191], a[248:251], v[24:27], v[188:191]\n" \
"v_exp_f32 v92, v92\n" \
"v_mfma_f32_16x16x32_bf16 v[188:191], a[252:255], v[56:59], v[188:191]\n" \
"v_add_f32 v243, v243, v91\n" \
"v_cvt_pk_bf16_f32 v89, v90, v91\n" \
"v_mfma_f32_16x16x32_bf16 v[156:159], a[248:251], v[16:19], v[156:159]\n" \
"v_exp_f32 v93, v93\n" \
"v_mfma_f32_16x16x32_bf16 v[156:159], a[252:255], v[48:51], v[156:159]\n" \
"ds_read_b128 a[244:247], v246 offset:10496\n" \
"v_add_f32 v243, v243, v92\n" \
"v_mfma_f32_16x16x32_bf16 a[124:127], a[248:251], v[8:11], a[124:127]\n" \
"v_exp_f32 v94, v94\n" \
"v_mfma_f32_16x16x32_bf16 a[124:127], a[252:255], v[40:43], a[124:127]\n" \
"v_add_f32 v243, v243, v93\n" \
"v_cvt_pk_bf16_f32 v90, v92, v93\n" \
"v_mfma_f32_16x16x32_bf16 a[92:95], a[248:251], v[0:3], a[92:95]\n" \
"s_waitcnt vmcnt(13) lgkmcnt(6)\n" \
"v_exp_f32 v95, v95\n" \
"v_add_f32 v243, v243, v94\n" \
"v_mfma_f32_16x16x32_bf16 a[92:95], a[252:255], v[32:35], a[92:95]\n" \
"s_barrier\n" \
"ds_read_b128 a[248:251], v246 offset:12544\n" \
"v_add_f32 v243, v243, v95\n" \
"v_mfma_f32_16x16x32_bf16 v[24:27], a[192:195], a[176:179], v[236:239]\n" \
"s_bfe_u32 s88, %1, 0x20001\n" \
"ds_read_b128 v[192:195], v246 offset:16384\n" \
"s_lshl_b32 s88, s88, 2\n" \
"v_mfma_f32_16x16x32_bf16 v[24:27], a[196:199], a[180:183], v[24:27]\n" \
"s_lshr_b32 s70, %1, 6\n" \
"ds_read_b128 v[196:199], v246 offset:18432\n" \
"v_cvt_pk_bf16_f32 v91, v94, v95\n" \
"v_mfma_f32_16x16x32_bf16 v[24:27], a[200:203], a[184:187], v[24:27]\n" \
"s_add_u32 s88, s88, s70\n" \
"ds_read_b128 v[200:203], v246 offset:20480\n" \
"v_bfe_u32 v69, v248, 8, 1\n" \
"v_mfma_f32_16x16x32_bf16 v[24:27], a[204:207], a[188:191], v[24:27]\n" \
"ds_read_b128 a[252:255], v246 offset:14592\n" \
"v_bfe_u32 v70, v248, 6, 2\n" \
"v_exp_f32 v96, v96\n" \
"v_exp_f32 v97, v97\n" \
"v_add_f32 v244, v244, v96\n" \
"v_exp_f32 v98, v98\n" \
"v_add_f32 v244, v244, v97\n" \
"v_cvt_pk_bf16_f32 v96, v96, v97\n" \
"v_exp_f32 v99, v99\n" \
"v_add_f32 v244, v244, v98\n" \
"v_exp_f32 v100, v100\n" \
"v_add_f32 v244, v244, v99\n" \
"v_mfma_f32_16x16x32_bf16 v[16:19], a[200:203], a[168:171], v[232:235]\n" \
"s_lshr_b32 s71, s22, 7\n" \
"ds_read_b128 v[204:207], v246 offset:22528\n" \
"s_sub_u32 s71, s63, s71\n" \
"v_mfma_f32_16x16x32_bf16 v[16:19], a[204:207], a[172:175], v[16:19]\n" \
"s_add_u32 s71, s71, 6\n" \
"ds_read_b128 v[208:211], v246 offset:16640\n" \
"s_and_b32 s71, s71, 7\n" \
"v_mfma_f32_16x16x32_bf16 v[16:19], a[192:195], a[160:163], v[16:19]\n" \
"ds_read_b128 v[212:215], v246 offset:18688\n" \
"v_lshl_add_u32 v70, v69, 3, v70\n" \
"v_mfma_f32_16x16x32_bf16 v[16:19], a[196:199], a[164:167], v[16:19]\n" \
"ds_read_b128 v[216:219], v246 offset:20736\n" \
"v_mul_u32_u24 v70, 6144, v70\n" \
"v_cvt_pk_bf16_f32 v97, v98, v99\n" \
"v_exp_f32 v101, v101\n" \
"v_add_f32 v244, v244, v100\n" \
"v_exp_f32 v102, v102\n" \
"v_add_f32 v244, v244, v101\n" \
"v_cvt_pk_bf16_f32 v98, v100, v101\n" \
"v_exp_f32 v103, v103\n" \
"v_add_f32 v244, v244, v102\n" \
"v_exp_f32 v104, v104\n" \
"v_add_f32 v244, v244, v103\n" \
"v_mfma_f32_16x16x32_bf16 v[20:23], a[208:211], a[160:163], v[232:235]\n" \
"s_and_b32 s70, %1, 1\n" \
"ds_read_b128 v[220:223], v246 offset:22784\n" \
"s_lshl_b32 s70, s70, 3\n" \
"v_mfma_f32_16x16x32_bf16 v[20:23], a[212:215], a[164:167], v[20:23]\n" \
"s_add_u32 s70, s70, s71\n" \
"ds_read_b128 a[192:195], v247 offset:32768\n" \
"v_lshlrev_b32 v69, 5, v69\n" \
"v_mfma_f32_16x16x32_bf16 v[20:23], a[216:219], a[168:171], v[20:23]\n" \
"ds_read_b128 a[196:199], v247 offset:40960\n" \
"v_and_b32 v71, 0x30, v248\n" \
"v_mfma_f32_16x16x32_bf16 v[20:23], a[220:223], a[172:175], v[20:23]\n" \
"s_lshl_b32 s87, s70, 12\n" \
"s_lshl_b32 s70, s70, 7\n" \
"ds_read_b128 a[200:203], v247 offset:33792\n" \
"v_cvt_pk_bf16_f32 v99, v102, v103\n" \
"v_exp_f32 v105, v105\n" \
"v_add_f32 v245, v245, v104\n" \
"v_exp_f32 v106, v106\n" \
"v_add_f32 v245, v245, v105\n" \
"v_cvt_pk_bf16_f32 v104, v104, v105\n" \
"v_exp_f32 v107, v107\n" \
"v_add_f32 v245, v245, v106\n" \
"v_exp_f32 v108, v108\n" \
"v_add_f32 v245, v245, v107\n" \
"v_mfma_f32_16x16x32_bf16 v[28:31], a[216:219], a[184:187], v[236:239]\n" \
"s_mul_i32 s84, s61, 6144\n" \
"ds_read_b128 a[204:207], v247 offset:41984\n" \
"s_lshl_b32 s71, s84, 4\n" \
"v_mfma_f32_16x16x32_bf16 v[28:31], a[220:223], a[188:191], v[28:31]\n" \
"v_xor_b32 v69, v69, v71\n" \
"v_lshrrev_b32 v71, 9, v248\n" \
"v_mfma_f32_16x16x32_bf16 v[28:31], a[208:211], a[176:179], v[28:31]\n" \
"s_add_u32 s70, s70, s71\n" \
"s_add_u32 s70, s70, 4096\n" \
"v_lshl_add_u32 v70, v71, 6, v70\n" \
"v_mfma_f32_16x16x32_bf16 v[28:31], a[212:215], a[180:183], v[28:31]\n" \
"s_waitcnt lgkmcnt(2)\n" \
"ds_read_b128 a[208:211], v247 offset:34816\n" \
"v_add_u32 v253, v69, v70\n" \
"v_cvt_pk_bf16_f32 v105, v106, v107\n" \
"v_exp_f32 v109, v109\n" \
"v_add_f32 v245, v245, v108\n" \
"v_exp_f32 v110, v110\n" \
"v_add_f32 v245, v245, v109\n" \
"v_cvt_pk_bf16_f32 v106, v108, v109\n" \
"v_exp_f32 v111, v111\n" \
"v_add_f32 v245, v245, v110\n" \
"v_exp_f32 v112, v112\n" \
"v_add_f32 v245, v245, v111\n" \
"v_mfma_f32_16x16x32_bf16 v[8:11], v[192:195], a[176:179], v[236:239]\n" \
"s_add_u32 s48, s22, s70\n" \
"s_addc_u32 s49, s23, 0\n" \
"ds_read_b128 a[212:215], v247 offset:43008\n" \
"v_mfma_f32_16x16x32_bf16 v[8:11], v[196:199], a[180:183], v[8:11]\n" \
"s_add_u32 s50, s48, 23552\n" \
"s_addc_u32 s51, s49, 0\n" \
"ds_read_b128 a[216:219], v247 offset:35840\n" \
"v_mfma_f32_16x16x32_bf16 v[8:11], v[200:203], a[184:187], v[8:11]\n" \
"s_lshr_b32 s89, s22, 7\n" \
"ds_read_b128 a[220:223], v247 offset:44032\n" \
"s_sub_u32 s89, 3, s89\n" \
"v_mfma_f32_16x16x32_bf16 v[8:11], v[204:207], a[188:191], v[8:11]\n" \
"ds_read_b128 v[192:195], v246 offset:24576\n" \
"v_cvt_pk_bf16_f32 v107, v110, v111\n" \
"v_bfe_u32 v84, v248, 7, 2\n" \
"s_and_b32 s89, s89, 7\n" \
"v_exp_f32 v113, v113\n" \
"v_add_f32 v242, v242, v112\n" \
"v_exp_f32 v114, v114\n" \
"v_add_f32 v242, v242, v113\n" \
"v_cvt_pk_bf16_f32 v112, v112, v113\n" \
"v_exp_f32 v115, v115\n" \
"v_add_f32 v242, v242, v114\n" \
"v_exp_f32 v116, v116\n" \
"v_add_f32 v242, v242, v115\n" \
"v_mfma_f32_16x16x32_bf16 v[56:59], a[232:235], a[184:187], v[236:239]\n" \
"s_and_b32 s90, %1, 1\n" \
"ds_read_b128 v[196:199], v246 offset:26624\n" \
"v_bfe_u32 v85, v248, 5, 2\n" \
"v_mfma_f32_16x16x32_bf16 v[56:59], a[236:239], a[188:191], v[56:59]\n" \
"s_lshl_b32 s90, s90, 3\n" \
"ds_read_b128 v[200:203], v246 offset:28672\n" \
"s_add_u32 s89, s89, s90\n" \
"v_mfma_f32_16x16x32_bf16 v[56:59], a[224:227], a[176:179], v[56:59]\n" \
"ds_read_b128 v[204:207], v246 offset:30720\n" \
"v_min_u32 v85, 2, v85\n" \
"v_mfma_f32_16x16x32_bf16 v[56:59], a[228:231], a[180:183], v[56:59]\n" \
"s_lshl_b32 s89, s89, 7\n" \
"v_cvt_pk_bf16_f32 v113, v114, v115\n" \
"v_exp_f32 v117, v117\n" \
"v_add_f32 v242, v242, v116\n" \
"s_lshr_b32 s90, %1, 3\n" \
"v_mad_u32_u24 v84, v84, 3, v85\n" \
"s_lshl_b32 s90, s90, 2\n" \
"v_mfma_f32_16x16x32_bf16 v[48:51], a[224:227], a[160:163], v[232:235]\n" \
"s_add_u32 s90, s90, s61\n" \
"v_and_b32 v85, 16, v248\n" \
"s_mul_i32 s90, s90, 24576\n" \
"v_mfma_f32_16x16x32_bf16 v[48:51], a[228:231], a[164:167], v[48:51]\n" \
"s_add_u32 s89, s89, s90\n" \
"v_lshl_add_u32 v85, v85, 2, s89\n" \
"v_lshl_add_u32 v84, v84, 11, v85\n" \
"v_mfma_f32_16x16x32_bf16 v[48:51], a[232:235], a[168:171], v[48:51]\n" \
"s_add_u32 m0, s84, 143360\n" \
"v_exp_f32 v118, v118\n" \
"v_mfma_f32_16x16x32_bf16 v[48:51], a[236:239], a[172:175], v[48:51]\n" \
"ds_read_b128 a[224:227], v247 offset:36864\n" \
"global_load_lds_dwordx4 v253, s[48:49] offset:-4096\n" \
"v_mfma_f32_16x16x32_bf16 v[12:15], v[216:219], a[184:187], v[236:239]\n" \
"ds_read_b128 a[228:231], v247 offset:45056\n" \
"v_add_f32 v242, v242, v117\n" \
"v_mfma_f32_16x16x32_bf16 v[12:15], v[220:223], a[188:191], v[12:15]\n" \
"ds_read_b128 a[232:235], v247 offset:37888\n" \
"v_cvt_pk_bf16_f32 v114, v116, v117\n" \
"v_mfma_f32_16x16x32_bf16 v[12:15], v[208:211], a[176:179], v[12:15]\n" \
"ds_read_b128 a[236:239], v247 offset:46080\n" \
"global_load_lds_dwordx4 v253, s[48:49] offset:-2048\n" \
"v_mfma_f32_16x16x32_bf16 v[12:15], v[212:215], a[180:183], v[12:15]\n" \
"ds_read_b128 v[208:211], v246 offset:24832\n" \
"v_exp_f32 v119, v119\n" \
"v_add_f32 v242, v242, v118\n" \
"v_exp_f32 v120, v120\n" \
"v_add_f32 v242, v242, v119\n" \
"v_mfma_f32_16x16x32_bf16 v[60:63], a[240:243], a[176:179], v[236:239]\n" \
"ds_read_b128 v[212:215], v246 offset:26880\n" \
"v_cvt_pk_bf16_f32 v115, v118, v119\n" \
"v_mfma_f32_16x16x32_bf16 v[60:63], a[244:247], a[180:183], v[60:63]\n" \
"ds_read_b128 v[216:219], v246 offset:28928\n" \
"global_load_lds_dwordx4 v253, s[48:49] offset:0\n" \
"v_mfma_f32_16x16x32_bf16 v[60:63], a[248:251], a[184:187], v[60:63]\n" \
"s_add_u32 s48, s48, 0x120000\n" \
"s_addc_u32 s49, s49, 0\n" \
"ds_read_b128 v[220:223], v246 offset:30976\n" \
"v_mfma_f32_16x16x32_bf16 v[60:63], a[252:255], a[188:191], v[60:63]\n" \
"v_exp_f32 v121, v121\n" \
"v_mfma_f32_16x16x32_bf16 v[52:55], a[248:251], a[168:171], v[232:235]\n" \
"global_load_lds_dwordx4 v253, s[50:51] offset:-3072\n" \
"v_add_f32 v243, v243, v120\n" \
"v_mfma_f32_16x16x32_bf16 v[52:55], a[252:255], a[172:175], v[52:55]\n" \
"v_exp_f32 v122, v122\n" \
"v_mfma_f32_16x16x32_bf16 v[52:55], a[240:243], a[160:163], v[52:55]\n" \
"v_add_f32 v243, v243, v121\n" \
"v_cvt_pk_bf16_f32 v120, v120, v121\n" \
"v_mfma_f32_16x16x32_bf16 v[52:55], a[244:247], a[164:167], v[52:55]\n" \
"s_waitcnt lgkmcnt(0)\n" \
"ds_read_b128 a[240:243], v247 offset:38912\n" \
"global_load_lds_dwordx4 v253, s[50:51] offset:-1024\n" \
"v_exp_f32 v123, v123\n" \
"v_add_f32 v243, v243, v122\n" \
"v_exp_f32 v124, v124\n" \
"v_mfma_f32_16x16x32_bf16 v[40:43], v[192:195], a[176:179], v[236:239]\n" \
"ds_read_b128 a[244:247], v247 offset:47104\n" \
"v_add_f32 v243, v243, v123\n" \
"v_mfma_f32_16x16x32_bf16 v[40:43], v[196:199], a[180:183], v[40:43]\n" \
"ds_read_b128 a[248:251], v247 offset:39936\n" \
"v_cvt_pk_bf16_f32 v121, v122, v123\n" \
"v_mfma_f32_16x16x32_bf16 v[40:43], v[200:203], a[184:187], v[40:43]\n" \
"ds_read_b128 a[252:255], v247 offset:48128\n" \
"global_load_lds_dwordx4 v253, s[50:51] offset:1024\n" \
"v_mfma_f32_16x16x32_bf16 v[40:43], v[204:207], a[188:191], v[40:43]\n" \
"s_add_u32 s50, s50, 0x120000\n" \
"s_addc_u32 s51, s51, 0\n" \
"v_exp_f32 v125, v125\n" \
"v_add_f32 v243, v243, v124\n" \
"v_mfma_f32_16x16x32_bf16 v[44:47], v[216:219], a[184:187], v[236:239]\n" \
"v_exp_f32 v126, v126\n" \
"v_mfma_f32_16x16x32_bf16 v[44:47], v[220:223], a[188:191], v[44:47]\n" \
"global_load_dword v85, v84, s[22:23]\n" \
"v_mfma_f32_16x16x32_bf16 v[44:47], v[208:211], a[176:179], v[44:47]\n" \
"s_waitcnt vmcnt(12) lgkmcnt(0)\n" \
"v_add_u32 v247, 0x10000, v248\n" \
"v_add_f32 v243, v243, v125\n" \
"v_mfma_f32_16x16x32_bf16 v[44:47], v[212:215], a[180:183], v[44:47]\n" \
"s_barrier\n" \
"v_cvt_pk_bf16_f32 v122, v124, v125\n" \
"v_exp_f32 v127, v127\n" \
"v_mfma_f32_16x16x32_bf16 a[64:67], a[192:195], v[64:67], a[64:67]\n" \
"ds_read_b128 v[192:195], v247 offset:16384\n" \
"v_add_f32 v243, v243, v126\n" \
"v_mfma_f32_16x16x32_bf16 a[64:67], a[196:199], v[96:99], a[64:67]\n" \
"v_add_f32 v243, v243, v127\n" \
"v_cvt_pk_bf16_f32 v123, v126, v127\n" \
"v_mfma_f32_16x16x32_bf16 a[96:99], a[192:195], v[72:75], a[96:99]\n" \
"s_lshl_b32 s70, s61, 14\n" \
"v_add_u32 v124, s70, v248\n" \
"v_xor_b32 v125, 0x4000, v124\n" \
"v_mfma_f32_16x16x32_bf16 a[96:99], a[196:199], v[104:107], a[96:99]\n" \
"s_lshl_b32 s70, s61, 9\n" \
"s_add_u32 s70, s70, 110592\n" \
"v_cmp_gt_i32 vcc, 0, v251\n" \
"v_cmp_gt_i32 s[64:65], 1, v251\n" \
"v_cmp_gt_i32 s[66:67], 2, v251\n" \
"v_cndmask_b32 v8, v8, v254, vcc\n" \
"v_cndmask_b32 v16, v16, v254, vcc\n" \
"v_cmp_gt_i32 vcc, 3, v251\n" \
"v_cndmask_b32 v9, v9, v254, s[64:65]\n" \
"v_cndmask_b32 v17, v17, v254, s[64:65]\n" \
"v_cmp_gt_i32 s[64:65], 16, v251\n" \
"v_cndmask_b32 v10, v10, v254, s[66:67]\n" \
"v_lshrrev_b32 v126, 1, v248\n" \
"v_mfma_f32_16x16x32_bf16 v[128:131], a[192:195], v[80:83], v[128:131]\n" \
"ds_read_b128 v[196:199], v247 offset:24576\n" \
"v_add_u32 v126, s70, v126\n" \
"v_mfma_f32_16x16x32_bf16 v[128:131], a[196:199], v[112:115], v[128:131]\n" \
"v_xor_b32 v127, 0x200, v126\n" \
"v_lshrrev_b32 v71, 7, v248\n" \
"v_cndmask_b32 v18, v18, v254, s[66:67]\n" \
"v_cmp_gt_i32 s[66:67], 17, v251\n" \
"v_cndmask_b32 v11, v11, v254, vcc\n" \
"v_cndmask_b32 v19, v19, v254, vcc\n" \
"v_cmp_gt_i32 vcc, 18, v251\n" \
"v_cndmask_b32 v12, v12, v254, s[64:65]\n" \
"v_cndmask_b32 v20, v20, v254, s[64:65]\n" \
"v_cmp_gt_i32 s[64:65], 19, v251\n" \
"v_cndmask_b32 v13, v13, v254, s[66:67]\n" \
"v_cndmask_b32 v21, v21, v254, s[66:67]\n" \
"v_mfma_f32_16x16x32_bf16 v[160:163], a[192:195], v[88:91], v[160:163]\n" \
"v_and_b32 v76, 0x70, v248\n" \
"s_lshl_b32 s70, s88, 18\n" \
"v_mfma_f32_16x16x32_bf16 v[160:163], a[196:199], v[120:123], v[160:163]\n" \
"s_lshl_b32 s71, s61, 16\n" \
"s_add_u32 s71, s71, s70\n" \
"ds_read_b128 a[192:195], v247 offset:0\n" \
"v_cmp_gt_i32 s[66:67], 32, v251\n" \
"v_cndmask_b32 v14, v14, v254, vcc\n" \
"v_cndmask_b32 v22, v22, v254, vcc\n" \
"v_cmp_gt_i32 vcc, 33, v251\n" \
"v_cndmask_b32 v15, v15, v254, s[64:65]\n" \
"v_cndmask_b32 v23, v23, v254, s[64:65]\n" \
"v_cmp_gt_i32 s[64:65], 34, v251\n" \
"v_cndmask_b32 v40, v40, v254, s[66:67]\n" \
"v_cndmask_b32 v48, v48, v254, s[66:67]\n" \
"v_cmp_gt_i32 s[66:67], 35, v251\n" \
"v_mfma_f32_16x16x32_bf16 v[164:167], a[200:203], v[88:91], v[164:167]\n" \
"ds_read_b128 v[200:203], v247 offset:17408\n" \
"v_lshlrev_b32 v77, 4, v71\n" \
"v_mfma_f32_16x16x32_bf16 v[164:167], a[204:207], v[120:123], v[164:167]\n" \
"ds_write_b128 v124, a[64:67] offset:0\n" \
"v_xor_b32 v77, v77, v76\n" \
"v_cndmask_b32 v41, v41, v254, vcc\n" \
"v_cndmask_b32 v49, v49, v254, vcc\n" \
"v_cmp_gt_i32 vcc, 48, v251\n" \
"v_cndmask_b32 v42, v42, v254, s[64:65]\n" \
"v_cndmask_b32 v50, v50, v254, s[64:65]\n" \
"v_cmp_gt_i32 s[64:65], 49, v251\n" \
"v_cndmask_b32 v43, v43, v254, s[66:67]\n" \
"v_cndmask_b32 v51, v51, v254, s[66:67]\n" \
"v_cmp_gt_i32 s[66:67], 50, v251\n" \
"v_cndmask_b32 v44, v44, v254, vcc\n" \
"v_mfma_f32_16x16x32_bf16 v[132:135], a[200:203], v[80:83], v[132:135]\n" \
"s_add_u32 s42, s20, s71\n" \
"s_addc_u32 s43, s21, 0\n" \
"v_lshl_add_u32 v252, v71, 12, v77\n" \
"v_mfma_f32_16x16x32_bf16 v[132:135], a[204:207], v[112:115], v[132:135]\n" \
"ds_read_b128 a[196:199], v247 offset:8192\n" \
"ds_write_b64 v126, v[244:245]\n" \
"v_cndmask_b32 v52, v52, v254, vcc\n" \
"v_cmp_gt_i32 vcc, 51, v251\n" \
"v_cndmask_b32 v45, v45, v254, s[64:65]\n" \
"v_cndmask_b32 v53, v53, v254, s[64:65]\n" \
"v_cndmask_b32 v46, v46, v254, s[66:67]\n" \
"v_cndmask_b32 v54, v54, v254, s[66:67]\n" \
"v_cndmask_b32 v47, v47, v254, vcc\n" \
"v_cndmask_b32 v55, v55, v254, vcc\n" \
"v_exp_f32 v8, v8\n" \
"v_exp_f32 v9, v9\n" \
"v_mfma_f32_16x16x32_bf16 a[100:103], a[200:203], v[72:75], a[100:103]\n" \
"s_add_u32 s44, s42, 31744\n" \
"s_addc_u32 s45, s43, 0\n" \
"ds_read_b128 v[204:207], v247 offset:25600\n" \
"v_mfma_f32_16x16x32_bf16 a[100:103], a[204:207], v[104:107], a[100:103]\n" \
"s_max_u32 s70, s70, 32768\n" \
"s_sub_u32 s70, s70, 32768\n" \
"ds_write_b128 v124, a[96:99] offset:8192\n" \
"v_add_f32 v243, v243, v8\n" \
"v_exp_f32 v10, v10\n" \
"v_add_f32 v243, v243, v9\n" \
"v_cvt_pk_bf16_f32 v8, v8, v9\n" \
"v_exp_f32 v11, v11\n" \
"v_add_f32 v243, v243, v10\n" \
"v_exp_f32 v12, v12\n" \
"v_add_f32 v243, v243, v11\n" \
"v_cvt_pk_bf16_f32 v9, v10, v11\n" \
"v_exp_f32 v13, v13\n" \
"v_mfma_f32_16x16x32_bf16 a[68:71], a[200:203], v[64:67], a[68:71]\n" \
"s_and_b32 s71, s61, 1\n" \
"s_lshl_b32 s71, s71, 2\n" \
"v_and_or_b32 v71, v71, 3, s71\n" \
"v_mfma_f32_16x16x32_bf16 a[68:71], a[204:207], v[96:99], a[68:71]\n" \
"s_lshr_b32 s71, s61, 1\n" \
"s_lshl_b32 s71, s71, 11\n" \
"ds_read_b128 a[200:203], v247 offset:1024\n" \
"v_add_f32 v243, v243, v12\n" \
"v_exp_f32 v14, v14\n" \
"v_add_f32 v243, v243, v13\n" \
"v_cvt_pk_bf16_f32 v10, v12, v13\n" \
"v_exp_f32 v15, v15\n" \
"v_add_f32 v243, v243, v14\n" \
"v_exp_f32 v40, v40\n" \
"v_add_f32 v243, v243, v15\n" \
"v_cvt_pk_bf16_f32 v11, v14, v15\n" \
"v_exp_f32 v41, v41\n" \
"v_mfma_f32_16x16x32_bf16 a[72:75], a[208:211], v[64:67], a[72:75]\n" \
"ds_read_b128 v[208:211], v247 offset:18432\n" \
"v_lshlrev_b32 v77, 4, v71\n" \
"v_mfma_f32_16x16x32_bf16 a[72:75], a[212:215], v[96:99], a[72:75]\n" \
"s_add_u32 s70, s70, s71\n" \
"ds_write_b128 v124, a[100:103] offset:9216\n" \
"v_add_f32 v243, v243, v40\n" \
"v_xor_b32 v77, v77, v76\n" \
"v_exp_f32 v42, v42\n" \
"v_add_f32 v243, v243, v41\n" \
"v_cvt_pk_bf16_f32 v40, v40, v41\n" \
"v_exp_f32 v43, v43\n" \
"v_add_f32 v243, v243, v42\n" \
"v_exp_f32 v44, v44\n" \
"v_add_f32 v243, v243, v43\n" \
"v_cvt_pk_bf16_f32 v41, v42, v43\n" \
"v_exp_f32 v45, v45\n" \
"v_mfma_f32_16x16x32_bf16 a[104:107], a[208:211], v[72:75], a[104:107]\n" \
"s_add_u32 s46, s20, s70\n" \
"s_addc_u32 s47, s21, 0\n" \
"v_lshl_add_u32 v246, v71, 12, v77\n" \
"v_mfma_f32_16x16x32_bf16 a[104:107], a[212:215], v[104:107], a[104:107]\n" \
"s_lshl_b32 s85, s61, 10\n" \
"s_mov_b64 s[12:13], s[20:21]\n" \
"ds_read_b128 a[204:207], v247 offset:9216\n" \
"v_add_f32 v243, v243, v44\n" \
"v_exp_f32 v46, v46\n" \
"v_add_f32 v243, v243, v45\n" \
"v_cvt_pk_bf16_f32 v42, v44, v45\n" \
"v_exp_f32 v47, v47\n" \
"v_add_f32 v243, v243, v46\n" \
"v_mfma_f32_16x16x32_bf16 v[136:139], a[208:211], v[80:83], v[136:139]\n" \
"s_and_b32 s13, s13, 0xffff\n" \
"ds_read_b128 v[212:215], v247 offset:26624\n" \
"s_mov_b32 s14, 4194304\n" \
"v_mfma_f32_16x16x32_bf16 v[136:139], a[212:215], v[112:115], v[136:139]\n" \
"s_mov_b32 s15, 0x20000\n" \
"s_mov_b32 s46, s70\n" \
"ds_write_b128 v124, a[68:71] offset:1024\n" \
"v_add_f32 v243, v243, v47\n" \
"v_cvt_pk_bf16_f32 v43, v46, v47\n" \
"v_exp_f32 v24, v24\n" \
"v_exp_f32 v25, v25\n" \
"v_add_f32 v243, v243, v24\n" \
"v_exp_f32 v26, v26\n" \
"v_add_f32 v243, v243, v25\n" \
"v_cvt_pk_bf16_f32 v24, v24, v25\n" \
"v_exp_f32 v27, v27\n" \
"v_add_f32 v243, v243, v26\n" \
"v_mfma_f32_16x16x32_bf16 v[168:171], a[208:211], v[88:91], v[168:171]\n" \
"v_lshrrev_b32 v71, 9, v248\n" \
"v_lshl_add_u32 v246, v71, 30, v246\n" \
"v_mfma_f32_16x16x32_bf16 v[168:171], a[212:215], v[120:123], v[168:171]\n" \
"s_add_u32 m0, s95, 118784\n" \
"ds_read_b128 a[208:211], v247 offset:2048\n" \
"v_exp_f32 v28, v28\n" \
"v_add_f32 v243, v243, v27\n" \
"v_cvt_pk_bf16_f32 v25, v26, v27\n" \
"v_exp_f32 v29, v29\n" \
"v_add_f32 v243, v243, v28\n" \
"v_exp_f32 v30, v30\n" \
"v_add_f32 v243, v243, v29\n" \
"v_cvt_pk_bf16_f32 v26, v28, v29\n" \
"v_exp_f32 v31, v31\n" \
"v_add_f32 v243, v243, v30\n" \
"v_mfma_f32_16x16x32_bf16 v[172:175], a[216:219], v[88:91], v[172:175]\n" \
"ds_read_b128 v[216:219], v247 offset:19456\n" \
"global_load_lds_dwordx4 v252, s[42:43] offset:0\n" \
"v_mfma_f32_16x16x32_bf16 v[172:175], a[220:223], v[120:123], v[172:175]\n" \
"ds_write_b128 v124, a[72:75] offset:2048\n" \
"v_exp_f32 v56, v56\n" \
"v_mfma_f32_16x16x32_bf16 v[140:143], a[216:219], v[80:83], v[140:143]\n" \
"v_add_f32 v243, v243, v31\n" \
"v_cvt_pk_bf16_f32 v27, v30, v31\n" \
"v_mfma_f32_16x16x32_bf16 v[140:143], a[220:223], v[112:115], v[140:143]\n" \
"ds_read_b128 a[212:215], v247 offset:10240\n" \
"global_load_lds_dwordx4 v252, s[42:43] offset:2048\n" \
"v_mfma_f32_16x16x32_bf16 a[108:111], a[216:219], v[72:75], a[108:111]\n" \
"s_add_u32 s42, s42, 0x180\n" \
"s_addc_u32 s43, s43, 0\n" \
"ds_read_b128 v[220:223], v247 offset:27648\n" \
"v_mfma_f32_16x16x32_bf16 a[108:111], a[220:223], v[104:107], a[108:111]\n" \
"ds_write_b128 v124, a[104:107] offset:10240\n" \
"v_exp_f32 v57, v57\n" \
"v_mfma_f32_16x16x32_bf16 a[76:79], a[216:219], v[64:67], a[76:79]\n" \
"global_load_lds_dwordx4 v252, s[44:45] offset:1024\n" \
"v_add_f32 v243, v243, v56\n" \
"v_mfma_f32_16x16x32_bf16 a[76:79], a[220:223], v[96:99], a[76:79]\n" \
"ds_read_b128 a[216:219], v247 offset:3072\n" \
"v_exp_f32 v58, v58\n" \
"v_add_f32 v243, v243, v57\n" \
"v_mfma_f32_16x16x32_bf16 a[80:83], a[224:227], v[64:67], a[80:83]\n" \
"ds_read_b128 v[224:227], v247 offset:20480\n" \
"v_cvt_pk_bf16_f32 v56, v56, v57\n" \
"v_mfma_f32_16x16x32_bf16 a[80:83], a[228:231], v[96:99], a[80:83]\n" \
"global_load_lds_dwordx4 v252, s[44:45] offset:3072\n" \
"ds_write_b128 v124, a[108:111] offset:11264\n" \
"v_exp_f32 v59, v59\n" \
"v_mfma_f32_16x16x32_bf16 a[112:115], a[224:227], v[72:75], a[112:115]\n" \
"s_add_u32 s44, s44, 0x180\n" \
"s_addc_u32 s45, s45, 0\n" \
"v_add_f32 v243, v243, v58\n" \
"v_mfma_f32_16x16x32_bf16 a[112:115], a[228:231], v[104:107], a[112:115]\n" \
"s_add_u32 m0, s85, 135168\n" \
"ds_read_b128 a[220:223], v247 offset:11264\n" \
"v_exp_f32 v60, v60\n" \
"v_add_f32 v243, v243, v59\n" \
"v_mfma_f32_16x16x32_bf16 v[144:147], a[224:227], v[80:83], v[144:147]\n" \
"ds_read_b128 v[228:231], v247 offset:28672\n" \
"v_cvt_pk_bf16_f32 v57, v58, v59\n" \
"v_mfma_f32_16x16x32_bf16 v[144:147], a[228:231], v[112:115], v[144:147]\n" \
"ds_write_b128 v124, a[76:79] offset:3072\n" \
"v_exp_f32 v61, v61\n" \
"v_add_f32 v243, v243, v60\n" \
"v_mfma_f32_16x16x32_bf16 v[176:179], a[224:227], v[88:91], v[176:179]\n" \
"buffer_load_dwordx4 v246, s[12:15], s46 offen lds\n" \
"v_mfma_f32_16x16x32_bf16 v[176:179], a[228:231], v[120:123], v[176:179]\n" \
"s_add_u32 s46, s46, 0x180\n" \
"ds_read_b128 a[224:227], v247 offset:4096\n" \
"v_exp_f32 v62, v62\n" \
"v_mfma_f32_16x16x32_bf16 v[180:183], a[232:235], v[88:91], v[180:183]\n" \
"ds_read_b128 v[232:235], v247 offset:21504\n" \
"v_add_f32 v243, v243, v61\n" \
"v_mfma_f32_16x16x32_bf16 v[180:183], a[236:239], v[120:123], v[180:183]\n" \
"ds_write_b128 v124, a[80:83] offset:4096\n" \
"v_cvt_pk_bf16_f32 v58, v60, v61\n" \
"v_mfma_f32_16x16x32_bf16 v[148:151], a[232:235], v[80:83], v[148:151]\n" \
"v_exp_f32 v63, v63\n" \
"v_mfma_f32_16x16x32_bf16 v[148:151], a[236:239], v[112:115], v[148:151]\n" \
"ds_read_b128 a[228:231], v247 offset:12288\n" \
"v_add_f32 v243, v243, v62\n" \
"v_mfma_f32_16x16x32_bf16 a[116:119], a[232:235], v[72:75], a[116:119]\n" \
"ds_read_b128 v[236:239], v247 offset:29696\n" \
"v_add_f32 v243, v243, v63\n" \
"v_mfma_f32_16x16x32_bf16 a[116:119], a[236:239], v[104:107], a[116:119]\n" \
"ds_write_b128 v124, a[112:115] offset:12288\n" \
"v_cvt_pk_bf16_f32 v59, v62, v63\n" \
"v_exp_f32 v16, v16\n" \
"v_exp_f32 v17, v17\n" \
"v_add_f32 v242, v242, v16\n" \
"v_mfma_f32_16x16x32_bf16 a[84:87], a[232:235], v[64:67], a[84:87]\n" \
"v_exp_f32 v18, v18\n" \
"v_mfma_f32_16x16x32_bf16 a[84:87], a[236:239], v[96:99], a[84:87]\n" \
"ds_read_b128 a[232:235], v247 offset:5120\n" \
"v_add_f32 v242, v242, v17\n" \
"v_mfma_f32_16x16x32_bf16 a[88:91], a[240:243], v[64:67], a[88:91]\n" \
"ds_read_b128 v[0:3], v247 offset:22528\n" \
"v_cvt_pk_bf16_f32 v16, v16, v17\n" \
"v_mfma_f32_16x16x32_bf16 a[88:91], a[244:247], v[96:99], a[88:91]\n" \
"ds_write_b128 v124, a[116:119] offset:13312\n" \
"v_exp_f32 v19, v19\n" \
"v_add_f32 v242, v242, v18\n" \
"v_mfma_f32_16x16x32_bf16 a[120:123], a[240:243], v[72:75], a[120:123]\n" \
"v_exp_f32 v20, v20\n" \
"v_mfma_f32_16x16x32_bf16 a[120:123], a[244:247], v[104:107], a[120:123]\n" \
"ds_read_b128 a[236:239], v247 offset:13312\n" \
"v_add_f32 v242, v242, v19\n" \
"v_mfma_f32_16x16x32_bf16 v[152:155], a[240:243], v[80:83], v[152:155]\n" \
"ds_read_b128 v[4:7], v247 offset:30720\n" \
"v_cvt_pk_bf16_f32 v17, v18, v19\n" \
"v_mfma_f32_16x16x32_bf16 v[152:155], a[244:247], v[112:115], v[152:155]\n" \
"ds_write_b128 v124, a[84:87] offset:5120\n" \
"v_exp_f32 v21, v21\n" \
"v_add_f32 v242, v242, v20\n" \
"v_mfma_f32_16x16x32_bf16 v[184:187], a[240:243], v[88:91], v[184:187]\n" \
"v_exp_f32 v22, v22\n" \
"v_mfma_f32_16x16x32_bf16 v[184:187], a[244:247], v[120:123], v[184:187]\n" \
"ds_read_b128 a[240:243], v247 offset:6144\n" \
"v_add_f32 v242, v242, v21\n" \
"v_mfma_f32_16x16x32_bf16 a[92:95], a[248:251], v[64:67], a[92:95]\n" \
"ds_read_b128 v[32:35], v247 offset:23552\n" \
"v_cvt_pk_bf16_f32 v18, v20, v21\n" \
"v_mfma_f32_16x16x32_bf16 a[92:95], a[252:255], v[96:99], a[92:95]\n" \
"ds_write_b128 v124, a[88:91] offset:6144\n" \
"v_exp_f32 v23, v23\n" \
"v_add_f32 v242, v242, v22\n" \
"v_mfma_f32_16x16x32_bf16 a[124:127], a[248:251], v[72:75], a[124:127]\n" \
"v_exp_f32 v48, v48\n" \
"v_mfma_f32_16x16x32_bf16 a[124:127], a[252:255], v[104:107], a[124:127]\n" \
"ds_read_b128 a[244:247], v247 offset:14336\n" \
"v_add_f32 v242, v242, v23\n" \
"v_mfma_f32_16x16x32_bf16 v[156:159], a[248:251], v[80:83], v[156:159]\n" \
"ds_read_b128 v[36:39], v247 offset:31744\n" \
"v_cvt_pk_bf16_f32 v19, v22, v23\n" \
"v_mfma_f32_16x16x32_bf16 v[156:159], a[252:255], v[112:115], v[156:159]\n" \
"ds_write_b128 v124, a[120:123] offset:14336\n" \
"v_exp_f32 v49, v49\n" \
"v_add_f32 v242, v242, v48\n" \
"v_mfma_f32_16x16x32_bf16 v[188:191], a[248:251], v[88:91], v[188:191]\n" \
"v_exp_f32 v50, v50\n" \
"v_mfma_f32_16x16x32_bf16 v[188:191], a[252:255], v[120:123], v[188:191]\n" \
"s_waitcnt lgkmcnt(3)\n" \
"ds_read_b128 a[248:251], v247 offset:7168\n" \
"ds_write_b128 v124, a[92:95] offset:7168\n" \
"v_add_f32 v242, v242, v49\n" \
"v_cvt_pk_bf16_f32 v48, v48, v49\n" \
"v_mfma_f32_16x16x32_bf16 v[160:163], v[192:195], v[8:11], v[160:163]\n" \
"v_exp_f32 v51, v51\n" \
"v_mfma_f32_16x16x32_bf16 v[160:163], v[196:199], v[40:43], v[160:163]\n" \
"ds_read_b128 a[252:255], v247 offset:15360\n" \
"v_add_f32 v242, v242, v50\n" \
"v_mfma_f32_16x16x32_bf16 v[164:167], v[200:203], v[8:11], v[164:167]\n" \
"v_exp_f32 v52, v52\n" \
"v_mfma_f32_16x16x32_bf16 v[164:167], v[204:207], v[40:43], v[164:167]\n" \
"ds_write_b128 v124, a[124:127] offset:15360\n" \
"v_add_f32 v242, v242, v51\n" \
"v_cvt_pk_bf16_f32 v49, v50, v51\n" \
"v_mfma_f32_16x16x32_bf16 v[168:171], v[208:211], v[8:11], v[168:171]\n" \
"v_exp_f32 v53, v53\n" \
"v_mfma_f32_16x16x32_bf16 v[168:171], v[212:215], v[40:43], v[168:171]\n" \
"v_add_f32 v242, v242, v52\n" \
"v_exp_f32 v54, v54\n" \
"v_mfma_f32_16x16x32_bf16 v[172:175], v[216:219], v[8:11], v[172:175]\n" \
"s_waitcnt vmcnt(0) lgkmcnt(0)\n" \
"v_add_f32 v242, v242, v53\n" \
"v_cvt_pk_bf16_f32 v50, v52, v53\n" \
"v_mfma_f32_16x16x32_bf16 v[172:175], v[220:223], v[40:43], v[172:175]\n" \
"s_barrier\n" \
"v_exp_f32 v55, v55\n" \
"v_mfma_f32_16x16x32_bf16 v[176:179], v[224:227], v[8:11], v[176:179]\n" \
"s_add_u32 m0, s84, 90112\n" \
"ds_read_b64 v[244:245], v127\n" \
"v_add_f32 v242, v242, v54\n" \
"v_mfma_f32_16x16x32_bf16 v[176:179], v[228:231], v[40:43], v[176:179]\n" \
"ds_read_b128 v[64:67], v125 offset:0\n" \
"global_load_lds_dwordx4 v253, s[48:49] offset:-4096\n" \
"v_mfma_f32_16x16x32_bf16 v[180:183], v[232:235], v[8:11], v[180:183]\n" \
"ds_read_b128 v[68:71], v125 offset:1024\n" \
"global_load_lds_dwordx4 v253, s[48:49] offset:-2048\n" \
"v_mfma_f32_16x16x32_bf16 v[180:183], v[236:239], v[40:43], v[180:183]\n" \
"ds_read_b128 v[72:75], v125 offset:2048\n" \
"v_add_f32 v242, v242, v55\n" \
"v_mfma_f32_16x16x32_bf16 v[184:187], v[0:3], v[8:11], v[184:187]\n" \
"ds_read_b128 v[76:79], v125 offset:3072\n" \
"global_load_lds_dwordx4 v253, s[48:49] offset:0\n" \
"v_mfma_f32_16x16x32_bf16 v[184:187], v[4:7], v[40:43], v[184:187]\n" \
"s_add_u32 s48, s48, 0x300000\n" \
"s_addc_u32 s49, s49, 0\n" \
"ds_read_b128 v[80:83], v125 offset:4096\n" \
"v_mfma_f32_16x16x32_bf16 v[188:191], v[32:35], v[8:11], v[188:191]\n" \
"ds_read_b128 v[84:87], v125 offset:5120\n" \
"global_load_lds_dwordx4 v253, s[50:51] offset:-3072\n" \
"v_mfma_f32_16x16x32_bf16 v[188:191], v[36:39], v[40:43], v[188:191]\n" \
"s_waitcnt lgkmcnt(5)\n" \
"ds_read_b128 v[88:91], v125 offset:6144\n" \
"v_cvt_pk_bf16_f32 v51, v54, v55\n" \
"v_mfma_f32_16x16x32_bf16 v[160:163], a[192:195], v[24:27], v[160:163]\n" \
"ds_read_b128 v[92:95], v125 offset:7168\n" \
"global_load_lds_dwordx4 v253, s[50:51] offset:-1024\n" \
"v_mfma_f32_16x16x32_bf16 v[160:163], a[196:199], v[56:59], v[160:163]\n" \
"s_movk_i32 s89, 0xc0\n" \
"ds_read_b128 v[96:99], v125 offset:8192\n" \
"global_load_lds_dwordx4 v253, s[50:51] offset:1024\n" \
"v_mfma_f32_16x16x32_bf16 v[164:167], a[200:203], v[24:27], v[164:167]\n" \
"s_add_u32 s50, s50, 0x300000\n" \
"s_addc_u32 s51, s51, 0\n" \
"ds_read_b128 v[100:103], v125 offset:9216\n" \
"v_mfma_f32_16x16x32_bf16 v[164:167], a[204:207], v[56:59], v[164:167]\n" \
"s_add_u32 m0, s95, 65536\n" \
"s_movk_i32 s90, 288\n" \
"Lpg8741_%=:\n" \
"Lst1x2_%=:\n" \
"s_cbranch_execz Lpg11019_%=\n" \
"ds_read_b128 v[104:107], v125 offset:10240\n" \
"v_lshrrev_b32 v211, 4, v248\n" \
"v_lshrrev_b32 v214, 8, v248\n" \
"v_and_b32 v215, 7, v211\n" \
"v_xor_b32 v214, v214, v215\n" \
"v_pk_add_f32 v[128:129], v[128:129], v[64:65]\n" \
"v_pk_add_f32 v[130:131], v[130:131], v[66:67]\n" \
"s_waitcnt lgkmcnt(9)\n" \
"v_pk_add_f32 v[132:133], v[132:133], v[68:69]\n" \
"v_pk_add_f32 v[134:135], v[134:135], v[70:71]\n" \
"s_waitcnt lgkmcnt(8)\n" \
"v_pk_add_f32 v[136:137], v[136:137], v[72:73]\n" \
"v_pk_add_f32 v[138:139], v[138:139], v[74:75]\n" \
"s_waitcnt lgkmcnt(7)\n" \
"v_pk_add_f32 v[140:141], v[140:141], v[76:77]\n" \
"v_pk_add_f32 v[142:143], v[142:143], v[78:79]\n" \
"s_waitcnt lgkmcnt(6)\n" \
"v_pk_add_f32 v[144:145], v[144:145], v[80:81]\n" \
"v_pk_add_f32 v[146:147], v[146:147], v[82:83]\n" \
"s_waitcnt lgkmcnt(5)\n" \
"v_pk_add_f32 v[148:149], v[148:149], v[84:85]\n" \
"v_pk_add_f32 v[150:151], v[150:151], v[86:87]\n" \
"s_waitcnt lgkmcnt(4)\n" \
"v_pk_add_f32 v[152:153], v[152:153], v[88:89]\n" \
"v_pk_add_f32 v[154:155], v[154:155], v[90:91]\n" \
"s_waitcnt lgkmcnt(3)\n" \
"v_pk_add_f32 v[156:157], v[156:157], v[92:93]\n" \
"v_pk_add_f32 v[158:159], v[158:159], v[94:95]\n" \
"v_mfma_f32_16x16x32_bf16 v[168:171], a[208:211], v[24:27], v[168:171]\n" \
"s_lshr_b32 s71, s61, 1\n" \
"ds_read_b128 v[108:111], v125 offset:11264\n" \
"global_load_lds_dwordx4 v252, s[42:43] offset:0\n" \
"v_mfma_f32_16x16x32_bf16 v[168:171], a[212:215], v[56:59], v[168:171]\n" \
"s_lshl_b32 s70, s71, 11\n" \
"ds_read_b128 v[112:115], v125 offset:12288\n" \
"s_add_u32 s70, s70, 118784\n" \
"v_and_b32 v216, 15, v211\n" \
"v_lshlrev_b32 v213, 7, v216\n" \
"v_lshl_add_u32 v213, v214, 4, v213\n" \
"v_mfma_f32_16x16x32_bf16 v[172:175], a[216:219], v[24:27], v[172:175]\n" \
"s_waitcnt lgkmcnt(4)\n" \
"ds_read_b128 v[116:119], v125 offset:13312\n" \
"global_load_lds_dwordx4 v252, s[42:43] offset:2048\n" \
"v_mfma_f32_16x16x32_bf16 v[172:175], a[220:223], v[56:59], v[172:175]\n" \
"s_add_u32 s42, s42, 0x400\n" \
"s_addc_u32 s43, s43, 0\n" \
"ds_read_b128 v[120:123], v125 offset:14336\n" \
"v_add_u32 v205, s70, v213\n" \
"v_xor_b32 v206, 64, v205\n" \
"v_lshrrev_b32 v216, 2, v216\n" \
"v_lshl_add_u32 v216, v216, 9, v213\n" \
"v_add_u32 v207, 135168, v216\n" \
"v_xor_b32 v208, 64, v207\n" \
"v_pk_add_f32 v[160:161], v[160:161], v[96:97]\n" \
"v_pk_add_f32 v[162:163], v[162:163], v[98:99]\n" \
"s_waitcnt lgkmcnt(5)\n" \
"v_pk_add_f32 v[164:165], v[164:165], v[100:101]\n" \
"v_pk_add_f32 v[166:167], v[166:167], v[102:103]\n" \
"v_mfma_f32_16x16x32_bf16 v[176:179], a[224:227], v[24:27], v[176:179]\n" \
"s_and_b32 s70, s61, 1\n" \
"ds_read_b128 v[124:127], v125 offset:15360\n" \
"global_load_lds_dwordx4 v252, s[44:45] offset:1024\n" \
"v_mfma_f32_16x16x32_bf16 v[176:179], a[228:231], v[56:59], v[176:179]\n" \
"s_lshl_b32 s70, s70, 9\n" \
"s_add_u32 s70, s70, 139264\n" \
"s_lshl_b32 s71, s71, 5\n" \
"v_mfma_f32_16x16x32_bf16 v[180:183], a[232:235], v[24:27], v[180:183]\n" \
"s_waitcnt lgkmcnt(5)\n" \
"global_load_lds_dwordx4 v252, s[44:45] offset:3072\n" \
"v_and_b32 v213, 3, v211\n" \
"v_mfma_f32_16x16x32_bf16 v[180:183], a[236:239], v[56:59], v[180:183]\n" \
"s_add_u32 s44, s44, 0x400\n" \
"s_addc_u32 s45, s45, 0\n" \
"v_lshlrev_b32 v213, 3, v213\n" \
"s_add_u32 m0, s85, 81920\n" \
"v_mfma_f32_16x16x32_bf16 v[184:187], a[240:243], v[24:27], v[184:187]\n" \
"s_waitcnt lgkmcnt(0)\n" \
"buffer_load_dwordx4 v246, s[12:15], s46 offen lds\n" \
"v_mfma_f32_16x16x32_bf16 v[184:187], a[244:247], v[56:59], v[184:187]\n" \
"s_barrier\n" \
"v_and_or_b32 v213, v248, s89, v213\n" \
"v_bfe_u32 v214, v211, 4, 1\n" \
"v_mad_u32_u24 v213, v214, s90, v213\n" \
"v_lshrrev_b32 v214, 5, v211\n" \
"v_mul_u32_u24 v214, 6144, v214\n" \
"v_add3_u32 v213, v213, v214, s70\n" \
"v_pk_add_f32 v[168:169], v[168:169], v[104:105]\n" \
"v_pk_add_f32 v[170:171], v[170:171], v[106:107]\n" \
"v_pk_add_f32 v[172:173], v[172:173], v[108:109]\n" \
"v_pk_add_f32 v[174:175], v[174:175], v[110:111]\n" \
"v_mfma_f32_16x16x32_bf16 v[188:191], a[248:251], v[24:27], v[188:191]\n" \
"s_add_u32 s46, s46, 0x400\n" \
"v_xor_b32 v209, s71, v213\n" \
"v_xor_b32 v210, 32, v209\n" \
"v_mfma_f32_16x16x32_bf16 v[188:191], a[252:255], v[56:59], v[188:191]\n" \
"s_add_u32 m0, s95, 0\n" \
"ds_read_b128 a[64:67], v207 offset:0\n" \
"ds_read_b64_tr_b16 a[96:97], v209 offset:2048\n" \
"v_mfma_f32_16x16x32_bf16 v[128:131], a[192:195], v[16:19], v[128:131]\n" \
"ds_read_b64_tr_b16 a[98:99], v209 offset:3072\n" \
"global_load_lds_dwordx4 v252, s[42:43] offset:0\n" \
"v_mfma_f32_16x16x32_bf16 v[128:131], a[196:199], v[48:51], v[128:131]\n" \
"ds_read_b128 a[68:71], v208 offset:0\n" \
"v_mfma_f32_16x16x32_bf16 v[132:135], a[200:203], v[16:19], v[132:135]\n" \
"ds_read_b64_tr_b16 a[100:101], v209 offset:14336\n" \
"ds_read_b64_tr_b16 a[102:103], v209 offset:15360\n" \
"v_pk_add_f32 v[176:177], v[176:177], v[112:113]\n" \
"v_pk_add_f32 v[178:179], v[178:179], v[114:115]\n" \
"v_pk_add_f32 v[180:181], v[180:181], v[116:117]\n" \
"v_pk_add_f32 v[182:183], v[182:183], v[118:119]\n" \
"v_pk_add_f32 v[184:185], v[184:185], v[120:121]\n" \
"v_pk_add_f32 v[186:187], v[186:187], v[122:123]\n" \
"v_pk_add_f32 v[188:189], v[188:189], v[124:125]\n" \
"v_pk_add_f32 v[190:191], v[190:191], v[126:127]\n" \
"v_mfma_f32_16x16x32_bf16 v[132:135], a[204:207], v[48:51], v[132:135]\n" \
"ds_read_b128 a[72:75], v205 offset:0\n" \
"global_load_lds_dwordx4 v252, s[42:43] offset:2048\n" \
"v_mfma_f32_16x16x32_bf16 v[136:139], a[208:211], v[16:19], v[136:139]\n" \
"s_add_u32 s42, s42, 0xfffffb00\n" \
"s_addc_u32 s43, s43, -1\n" \
"ds_read_b64_tr_b16 a[104:105], v209 offset:0\n" \
"v_mfma_f32_16x16x32_bf16 v[136:139], a[212:215], v[48:51], v[136:139]\n" \
"ds_read_b128 a[76:79], v206 offset:0\n" \
"ds_read_b64_tr_b16 a[106:107], v209 offset:1024\n" \
"v_mfma_f32_16x16x32_bf16 v[140:143], a[216:219], v[16:19], v[140:143]\n" \
"ds_read_b64_tr_b16 a[108:109], v209 offset:12288\n" \
"global_load_lds_dwordx4 v252, s[44:45] offset:1024\n" \
"v_mfma_f32_16x16x32_bf16 v[140:143], a[220:223], v[48:51], v[140:143]\n" \
"ds_read_b64_tr_b16 a[110:111], v209 offset:13312\n" \
"v_add_f32 v242, v242, v244\n" \
"v_mfma_f32_16x16x32_bf16 v[144:147], a[224:227], v[16:19], v[144:147]\n" \
"ds_read_b128 a[80:83], v205 offset:4096\n" \
"ds_read_b64_tr_b16 a[112:113], v210 offset:0\n" \
"v_mfma_f32_16x16x32_bf16 v[144:147], a[228:231], v[48:51], v[144:147]\n" \
"ds_read_b64_tr_b16 a[114:115], v210 offset:1024\n" \
"global_load_lds_dwordx4 v252, s[44:45] offset:3072\n" \
"v_mfma_f32_16x16x32_bf16 v[148:151], a[232:235], v[16:19], v[148:151]\n" \
"s_add_u32 s44, s44, 0xfffffb00\n" \
"s_addc_u32 s45, s45, -1\n" \
"ds_read_b64_tr_b16 a[116:117], v210 offset:12288\n" \
"v_mfma_f32_16x16x32_bf16 v[148:151], a[236:239], v[48:51], v[148:151]\n" \
"s_add_u32 m0, s85, 16384\n" \
"ds_read_b128 a[84:87], v206 offset:4096\n" \
"ds_read_b64_tr_b16 a[118:119], v210 offset:13312\n" \
"v_mfma_f32_16x16x32_bf16 v[152:155], a[240:243], v[16:19], v[152:155]\n" \
"ds_read_b128 a[88:91], v205 offset:8192\n" \
"v_add_f32 v243, v243, v245\n" \
"v_mfma_f32_16x16x32_bf16 v[152:155], a[244:247], v[48:51], v[152:155]\n" \
"ds_read_b128 a[92:95], v206 offset:8192\n" \
"v_mfma_f32_16x16x32_bf16 v[156:159], a[248:251], v[16:19], v[156:159]\n" \
"buffer_load_dwordx4 v246, s[12:15], s46 offen lds\n" \
"v_mfma_f32_16x16x32_bf16 v[156:159], a[252:255], v[48:51], v[156:159]\n" \
"s_waitcnt lgkmcnt(4)\n" \
"Ljoin_%=:\n" \
"v_mfma_f32_16x16x32_bf16 v[32:35], a[96:99], a[64:67], 0\n" \
"s_add_u32 s46, s46, 0xfffffb00\n" \
"ds_read_b128 a[192:195], v205 offset:12288\n" \
"s_add_u32 m0, s84, 24576\n" \
"v_mfma_f32_16x16x32_bf16 v[32:35], a[100:103], a[68:71], v[32:35]\n" \
"global_load_lds_dwordx4 v253, s[48:49] offset:-4096\n" \
"v_mfma_f32_16x16x32_bf16 v[40:43], a[96:99], a[72:75], 0\n" \
"ds_read_b128 a[196:199], v206 offset:12288\n" \
"v_mfma_f32_16x16x32_bf16 v[40:43], a[100:103], a[76:79], v[40:43]\n" \
"ds_read_b64_tr_b16 a[208:209], v209 offset:4096\n" \
"v_mov_b32 v232, v242\n" \
"v_mfma_f32_16x16x32_bf16 v[0:3], a[104:107], a[72:75], 0\n" \
"ds_read_b64_tr_b16 a[210:211], v209 offset:5120\n" \
"global_load_lds_dwordx4 v253, s[48:49] offset:-2048\n" \
"v_mfma_f32_16x16x32_bf16 v[0:3], a[108:111], a[76:79], v[0:3]\n" \
"s_waitcnt lgkmcnt(3)\n" \
"v_permlane16_swap_b32 v232, v242\n" \
"v_mfma_f32_16x16x32_bf16 v[4:7], a[112:115], a[72:75], 0\n" \
"ds_read_b64_tr_b16 a[212:213], v209 offset:16384\n" \
"v_add_f32 v242, v242, v232\n" \
"v_mfma_f32_16x16x32_bf16 v[4:7], a[116:119], a[76:79], v[4:7]\n" \
"ds_read_b64_tr_b16 a[214:215], v209 offset:17408\n" \
"global_load_lds_dwordx4 v253, s[48:49] offset:0\n" \
"v_mfma_f32_16x16x32_bf16 v[12:15], a[112:115], a[80:83], 0\n" \
"s_add_u32 s48, s48, 0xffc40000\n" \
"s_addc_u32 s49, s49, -1\n" \
"ds_read_b64_tr_b16 a[216:217], v210 offset:4096\n" \
"v_mfma_f32_16x16x32_bf16 v[12:15], a[116:119], a[84:87], v[12:15]\n" \
"v_mov_b32 v232, v242\n" \
"v_mfma_f32_16x16x32_bf16 v[8:11], a[104:107], a[80:83], 0\n" \
"ds_read_b64_tr_b16 a[218:219], v210 offset:5120\n" \
"global_load_lds_dwordx4 v253, s[50:51] offset:-3072\n" \
"v_mfma_f32_16x16x32_bf16 v[8:11], a[108:111], a[84:87], v[8:11]\n" \
"ds_read_b64_tr_b16 a[220:221], v210 offset:16384\n" \
"v_mfma_f32_16x16x32_bf16 v[48:51], a[96:99], a[80:83], 0\n" \
"ds_read_b64_tr_b16 a[222:223], v210 offset:17408\n" \
"v_mfma_f32_16x16x32_bf16 v[48:51], a[100:103], a[84:87], v[48:51]\n" \
"global_load_lds_dwordx4 v253, s[50:51] offset:-1024\n" \
"v_mfma_f32_16x16x32_bf16 v[56:59], a[96:99], a[88:91], 0\n" \
"ds_read_b64_tr_b16 a[200:201], v210 offset:2048\n" \
"v_mfma_f32_16x16x32_bf16 v[56:59], a[100:103], a[92:95], v[56:59]\n" \
"ds_read_b64_tr_b16 a[202:203], v210 offset:3072\n" \
"v_mfma_f32_16x16x32_bf16 v[16:19], a[104:107], a[88:91], 0\n" \
"ds_read_b64_tr_b16 a[204:205], v210 offset:14336\n" \
"global_load_lds_dwordx4 v253, s[50:51] offset:1024\n" \
"v_mfma_f32_16x16x32_bf16 v[16:19], a[108:111], a[92:95], v[16:19]\n" \
"s_add_u32 s50, s50, 0xffc40000\n" \
"s_addc_u32 s51, s51, -1\n" \
"v_mfma_f32_16x16x32_bf16 v[20:23], a[112:115], a[88:91], 0\n" \
"ds_read_b64_tr_b16 a[206:207], v210 offset:15360\n" \
"v_mfma_f32_16x16x32_bf16 v[20:23], a[116:119], a[92:95], v[20:23]\n" \
"s_waitcnt lgkmcnt(3)\n" \
"v_add_u32 v207, 0xffff3000, v207\n" \
"v_add_u32 v208, 0xffff3000, v208\n" \
"v_mfma_f32_16x16x32_bf16 v[28:31], a[112:115], a[192:195], 0\n" \
"v_add_u32 v205, 0xffff3000, v205\n" \
"v_add_u32 v206, 0xffff3000, v206\n" \
"v_mfma_f32_16x16x32_bf16 v[28:31], a[116:119], a[196:199], v[28:31]\n" \
"v_add_u32 v209, 0xffff3000, v209\n" \
"v_add_u32 v210, 0xffff3000, v210\n" \
"v_mfma_f32_16x16x32_bf16 v[24:27], a[104:107], a[192:195], 0\n" \
"v_permlane32_swap_b32 v232, v242\n" \
"v_mfma_f32_16x16x32_bf16 v[24:27], a[108:111], a[196:199], v[24:27]\n" \
"v_add_f32 v242, v242, v232\n" \
"v_mfma_f32_16x16x32_bf16 v[64:67], a[96:99], a[192:195], 0\n" \
"s_waitcnt vmcnt(11) lgkmcnt(0)\n" \
"v_rcp_f32 v232, v242\n" \
"v_mfma_f32_16x16x32_bf16 v[64:67], a[100:103], a[196:199], v[64:67]\n" \
"s_barrier\n" \
"v_mul_f32 v242, 0x4d800000, v242\n" \
"v_mfma_f32_16x16x32_bf16 v[72:75], a[64:67], a[208:211], 0\n" \
"ds_read_b64_tr_b16 a[96:97], v209 offset:2048\n" \
"v_add_u32 v233, 0x1c90080, v249\n" \
"v_mfma_f32_16x16x32_bf16 v[72:75], a[68:71], a[212:215], v[72:75]\n" \
"ds_read_b64_tr_b16 a[98:99], v209 offset:3072\n" \
"v_cndmask_b32 v233, v249, v233, s[16:17]\n" \
"v_mfma_f32_16x16x32_bf16 v[80:83], a[72:75], a[208:211], 0\n" \
"s_add_u32 m0, s95, 118784\n" \
"ds_read_b128 a[64:67], v207 offset:0\n" \
"ds_read_b64_tr_b16 a[100:101], v209 offset:14336\n" \
"v_mfma_f32_16x16x32_bf16 v[80:83], a[76:79], a[212:215], v[80:83]\n" \
"global_load_lds_dwordx4 v252, s[42:43] offset:0\n" \
"v_mul_f32 v128, v128, v232\n" \
"v_mfma_f32_16x16x32_bf16 v[84:87], a[72:75], a[216:219], 0\n" \
"ds_read_b128 a[68:71], v208 offset:0\n" \
"ds_read_b64_tr_b16 a[102:103], v209 offset:15360\n" \
"v_mfma_f32_16x16x32_bf16 v[84:87], a[76:79], a[220:223], v[84:87]\n" \
"ds_read_b64_tr_b16 a[104:105], v209 offset:0\n" \
"v_mul_f32 v129, v129, v232\n" \
"v_mfma_f32_16x16x32_bf16 v[44:47], a[200:203], a[72:75], 0\n" \
"ds_read_b64_tr_b16 a[106:107], v209 offset:1024\n" \
"v_mul_f32 v130, v130, v232\n" \
"v_mfma_f32_16x16x32_bf16 v[44:47], a[204:207], a[76:79], v[44:47]\n" \
"global_load_lds_dwordx4 v252, s[42:43] offset:2048\n" \
"v_mul_f32 v131, v131, v232\n" \
"v_mfma_f32_16x16x32_bf16 v[52:55], a[200:203], a[80:83], 0\n" \
"s_add_u32 s42, s42, 0x80\n" \
"s_addc_u32 s43, s43, 0\n" \
"ds_read_b128 a[72:75], v205 offset:0\n" \
"v_mfma_f32_16x16x32_bf16 v[52:55], a[204:207], a[84:87], v[52:55]\n" \
"ds_read_b128 a[76:79], v206 offset:0\n" \
"v_mul_f32 v132, v132, v232\n" \
"v_mfma_f32_16x16x32_bf16 v[92:95], a[80:83], a[216:219], 0\n" \
"ds_read_b64_tr_b16 a[108:109], v209 offset:12288\n" \
"v_mul_f32 v133, v133, v232\n" \
"v_mfma_f32_16x16x32_bf16 v[92:95], a[84:87], a[220:223], v[92:95]\n" \
"global_load_lds_dwordx4 v252, s[44:45] offset:1024\n" \
"v_mul_f32 v134, v134, v232\n" \
"v_mfma_f32_16x16x32_bf16 v[88:91], a[80:83], a[208:211], 0\n" \
"ds_read_b64_tr_b16 a[110:111], v209 offset:13312\n" \
"v_mul_f32 v135, v135, v232\n" \
"v_mfma_f32_16x16x32_bf16 v[88:91], a[84:87], a[212:215], v[88:91]\n" \
"ds_read_b64_tr_b16 a[112:113], v210 offset:0\n" \
"v_fma_f32 v242, v128, v129, v242\n" \
"v_mfma_f32_16x16x32_bf16 v[96:99], a[88:91], a[208:211], 0\n" \
"ds_read_b128 a[80:83], v205 offset:4096\n" \
"v_cvt_pk_bf16_f32 v128, v128, v129\n" \
"v_mfma_f32_16x16x32_bf16 v[96:99], a[92:95], a[212:215], v[96:99]\n" \
"global_load_lds_dwordx4 v252, s[44:45] offset:3072\n" \
"v_fma_f32 v242, v130, v131, v242\n" \
"v_mfma_f32_16x16x32_bf16 v[100:103], a[88:91], a[216:219], 0\n" \
"s_add_u32 s44, s44, 0x80\n" \
"s_addc_u32 s45, s45, 0\n" \
"ds_read_b128 a[84:87], v206 offset:4096\n" \
"v_mfma_f32_16x16x32_bf16 v[100:103], a[92:95], a[220:223], v[100:103]\n" \
"ds_read_b64_tr_b16 a[114:115], v210 offset:1024\n" \
"v_cvt_pk_bf16_f32 v129, v130, v131\n" \
"v_mfma_f32_16x16x32_bf16 v[60:63], a[200:203], a[88:91], 0\n" \
"s_add_u32 m0, s85, 135168\n" \
"ds_read_b64_tr_b16 a[116:117], v210 offset:12288\n" \
"v_fma_f32 v242, v132, v133, v242\n" \
"v_mfma_f32_16x16x32_bf16 v[60:63], a[204:207], a[92:95], v[60:63]\n" \
"buffer_load_dwordx4 v246, s[12:15], s46 offen lds\n" \
"v_mfma_f32_16x16x32_bf16 v[68:71], a[200:203], a[192:195], 0\n" \
"s_add_u32 s46, s46, 0x80\n" \
"ds_read_b128 a[88:91], v205 offset:8192\n" \
"v_cvt_pk_bf16_f32 v130, v132, v133\n" \
"v_mfma_f32_16x16x32_bf16 v[68:71], a[204:207], a[196:199], v[68:71]\n" \
"ds_read_b128 a[92:95], v206 offset:8192\n" \
"v_fma_f32 v242, v134, v135, v242\n" \
"v_mfma_f32_16x16x32_bf16 v[108:111], a[192:195], a[216:219], 0\n" \
"s_add_u32 m0, s84, 143360\n" \
"ds_read_b64_tr_b16 a[118:119], v210 offset:13312\n" \
"v_cvt_pk_bf16_f32 v131, v134, v135\n" \
"v_mfma_f32_16x16x32_bf16 v[108:111], a[196:199], a[220:223], v[108:111]\n" \
"buffer_store_dwordx4 v[128:131], v233, s[56:59], s78 offen offset:0 sc1\n" \
"global_load_lds_dwordx4 v253, s[48:49] offset:-4096\n" \
"v_mul_f32 v136, v136, v232\n" \
"v_mul_f32 v137, v137, v232\n" \
"v_mul_f32 v138, v138, v232\n" \
"v_mfma_f32_16x16x32_bf16 v[104:107], a[192:195], a[208:211], 0\n" \
"v_mul_f32 v139, v139, v232\n" \
"v_mul_f32 v140, v140, v232\n" \
"v_mfma_f32_16x16x32_bf16 v[104:107], a[196:199], a[212:215], v[104:107]\n" \
"s_waitcnt lgkmcnt(2)\n" \
"v_mul_f32 v141, v141, v232\n" \
"v_mul_f32 v142, v142, v232\n" \
"v_mfma_f32_16x16x32_bf16 v[32:35], a[96:99], a[64:67], v[32:35]\n" \
"ds_read_b128 a[192:195], v205 offset:12288\n" \
"v_mul_f32 v143, v143, v232\n" \
"v_mfma_f32_16x16x32_bf16 v[32:35], a[100:103], a[68:71], v[32:35]\n" \
"global_load_lds_dwordx4 v253, s[48:49] offset:-2048\n" \
"v_fma_f32 v242, v136, v137, v242\n" \
"v_mfma_f32_16x16x32_bf16 v[40:43], a[96:99], a[72:75], v[40:43]\n" \
"ds_read_b128 a[196:199], v206 offset:12288\n" \
"v_cvt_pk_bf16_f32 v136, v136, v137\n" \
"v_mfma_f32_16x16x32_bf16 v[40:43], a[100:103], a[76:79], v[40:43]\n" \
"ds_read_b64_tr_b16 a[208:209], v209 offset:4096\n" \
"v_fma_f32 v242, v138, v139, v242\n" \
"v_mfma_f32_16x16x32_bf16 v[0:3], a[104:107], a[72:75], v[0:3]\n" \
"ds_read_b64_tr_b16 a[210:211], v209 offset:5120\n" \
"v_cvt_pk_bf16_f32 v137, v138, v139\n" \
"v_mfma_f32_16x16x32_bf16 v[0:3], a[108:111], a[76:79], v[0:3]\n" \
"s_waitcnt lgkmcnt(3)\n" \
"global_load_lds_dwordx4 v253, s[48:49] offset:0\n" \
"v_fma_f32 v242, v140, v141, v242\n" \
"v_mfma_f32_16x16x32_bf16 v[4:7], a[112:115], a[72:75], v[4:7]\n" \
"s_add_u32 s48, s48, 0x60000\n" \
"s_addc_u32 s49, s49, 0\n" \
"ds_read_b64_tr_b16 a[212:213], v209 offset:16384\n" \
"v_mfma_f32_16x16x32_bf16 v[4:7], a[116:119], a[76:79], v[4:7]\n" \
"ds_read_b64_tr_b16 a[214:215], v209 offset:17408\n" \
"v_cvt_pk_bf16_f32 v138, v140, v141\n" \
"v_mfma_f32_16x16x32_bf16 v[12:15], a[112:115], a[80:83], v[12:15]\n" \
"ds_read_b64_tr_b16 a[216:217], v210 offset:4096\n" \
"v_fma_f32 v242, v142, v143, v242\n" \
"v_mfma_f32_16x16x32_bf16 v[12:15], a[116:119], a[84:87], v[12:15]\n" \
"global_load_lds_dwordx4 v253, s[50:51] offset:-3072\n" \
"v_cvt_pk_bf16_f32 v139, v142, v143\n" \
"v_mfma_f32_16x16x32_bf16 v[8:11], a[104:107], a[80:83], v[8:11]\n" \
"ds_read_b64_tr_b16 a[218:219], v210 offset:5120\n" \
"v_mfma_f32_16x16x32_bf16 v[8:11], a[108:111], a[84:87], v[8:11]\n" \
"buffer_store_dwordx4 v[136:139], v233, s[56:59], s78 offen offset:2048 sc1\n" \
"ds_read_b64_tr_b16 a[220:221], v210 offset:16384\n" \
"s_add_u32 s78, s78, 0x1000\n" \
"v_mul_f32 v144, v144, v232\n" \
"v_mul_f32 v145, v145, v232\n" \
"v_mfma_f32_16x16x32_bf16 v[48:51], a[96:99], a[80:83], v[48:51]\n" \
"ds_read_b64_tr_b16 a[222:223], v210 offset:17408\n" \
"v_mul_f32 v146, v146, v232\n" \
"v_mfma_f32_16x16x32_bf16 v[48:51], a[100:103], a[84:87], v[48:51]\n" \
"global_load_lds_dwordx4 v253, s[50:51] offset:-1024\n" \
"v_mul_f32 v147, v147, v232\n" \
"v_mfma_f32_16x16x32_bf16 v[56:59], a[96:99], a[88:91], v[56:59]\n" \
"ds_read_b64_tr_b16 a[200:201], v210 offset:2048\n" \
"v_mul_f32 v148, v148, v232\n" \
"v_mfma_f32_16x16x32_bf16 v[56:59], a[100:103], a[92:95], v[56:59]\n" \
"ds_read_b64_tr_b16 a[202:203], v210 offset:3072\n" \
"v_mul_f32 v149, v149, v232\n" \
"v_mfma_f32_16x16x32_bf16 v[16:19], a[104:107], a[88:91], v[16:19]\n" \
"ds_read_b64_tr_b16 a[204:205], v210 offset:14336\n" \
"v_mul_f32 v150, v150, v232\n" \
"v_mfma_f32_16x16x32_bf16 v[16:19], a[108:111], a[92:95], v[16:19]\n" \
"global_load_lds_dwordx4 v253, s[50:51] offset:1024\n" \
"v_mul_f32 v151, v151, v232\n" \
"v_mfma_f32_16x16x32_bf16 v[20:23], a[112:115], a[88:91], v[20:23]\n" \
"s_add_u32 s50, s50, 0x60000\n" \
"s_addc_u32 s51, s51, 0\n" \
"ds_read_b64_tr_b16 a[206:207], v210 offset:15360\n" \
"v_mfma_f32_16x16x32_bf16 v[20:23], a[116:119], a[92:95], v[20:23]\n" \
"s_waitcnt lgkmcnt(3)\n" \
"v_add_u32 v207, 0xffff0000, v207\n" \
"v_add_u32 v208, 0xffff0000, v208\n" \
"v_mfma_f32_16x16x32_bf16 v[28:31], a[112:115], a[192:195], v[28:31]\n" \
"v_add_u32 v205, 0xffff0000, v205\n" \
"v_add_u32 v206, 0xffff0000, v206\n" \
"v_mfma_f32_16x16x32_bf16 v[28:31], a[116:119], a[196:199], v[28:31]\n" \
"v_add_u32 v209, 0xffff0000, v209\n" \
"v_add_u32 v210, 0xffff0000, v210\n" \
"v_mfma_f32_16x16x32_bf16 v[24:27], a[104:107], a[192:195], v[24:27]\n" \
"v_fma_f32 v242, v144, v145, v242\n" \
"v_cvt_pk_bf16_f32 v144, v144, v145\n" \
"v_mfma_f32_16x16x32_bf16 v[24:27], a[108:111], a[196:199], v[24:27]\n" \
"v_fma_f32 v242, v146, v147, v242\n" \
"v_cvt_pk_bf16_f32 v145, v146, v147\n" \
"v_mfma_f32_16x16x32_bf16 v[64:67], a[96:99], a[192:195], v[64:67]\n" \
"s_waitcnt vmcnt(13) lgkmcnt(0)\n" \
"v_fma_f32 v242, v148, v149, v242\n" \
"v_cvt_pk_bf16_f32 v146, v148, v149\n" \
"v_mfma_f32_16x16x32_bf16 v[64:67], a[100:103], a[196:199], v[64:67]\n" \
"s_barrier\n" \
"v_fma_f32 v242, v150, v151, v242\n" \
"v_mfma_f32_16x16x32_bf16 v[72:75], a[64:67], a[208:211], v[72:75]\n" \
"ds_read_b64_tr_b16 a[96:97], v209 offset:2048\n" \
"v_cvt_pk_bf16_f32 v147, v150, v151\n" \
"v_mfma_f32_16x16x32_bf16 v[72:75], a[68:71], a[212:215], v[72:75]\n" \
"buffer_store_dwordx4 v[144:147], v233, s[56:59], s78 offen offset:0 sc1\n" \
"ds_read_b64_tr_b16 a[98:99], v209 offset:3072\n" \
"v_mul_f32 v152, v152, v232\n" \
"v_mul_f32 v153, v153, v232\n" \
"v_mul_f32 v154, v154, v232\n" \
"v_mfma_f32_16x16x32_bf16 v[80:83], a[72:75], a[208:211], v[80:83]\n" \
"s_add_u32 m0, s95, 65536\n" \
"ds_read_b128 a[64:67], v207 offset:0\n" \
"ds_read_b64_tr_b16 a[100:101], v209 offset:14336\n" \
"v_mfma_f32_16x16x32_bf16 v[80:83], a[76:79], a[212:215], v[80:83]\n" \
"global_load_lds_dwordx4 v252, s[42:43] offset:0\n" \
"v_mul_f32 v155, v155, v232\n" \
"v_mfma_f32_16x16x32_bf16 v[84:87], a[72:75], a[216:219], v[84:87]\n" \
"ds_read_b128 a[68:71], v208 offset:0\n" \
"ds_read_b64_tr_b16 a[102:103], v209 offset:15360\n" \
"v_mfma_f32_16x16x32_bf16 v[84:87], a[76:79], a[220:223], v[84:87]\n" \
"ds_read_b64_tr_b16 a[104:105], v209 offset:0\n" \
"v_mul_f32 v156, v156, v232\n" \
"v_mfma_f32_16x16x32_bf16 v[44:47], a[200:203], a[72:75], v[44:47]\n" \
"ds_read_b64_tr_b16 a[106:107], v209 offset:1024\n" \
"v_mul_f32 v157, v157, v232\n" \
"v_mfma_f32_16x16x32_bf16 v[44:47], a[204:207], a[76:79], v[44:47]\n" \
"global_load_lds_dwordx4 v252, s[42:43] offset:2048\n" \
"v_mul_f32 v158, v158, v232\n" \
"v_mfma_f32_16x16x32_bf16 v[52:55], a[200:203], a[80:83], v[52:55]\n" \
"s_add_u32 s42, s42, 0x100\n" \
"s_addc_u32 s43, s43, 0\n" \
"ds_read_b128 a[72:75], v205 offset:0\n" \
"v_mfma_f32_16x16x32_bf16 v[52:55], a[204:207], a[84:87], v[52:55]\n" \
"ds_read_b128 a[76:79], v206 offset:0\n" \
"v_mul_f32 v159, v159, v232\n" \
"v_mfma_f32_16x16x32_bf16 v[92:95], a[80:83], a[216:219], v[92:95]\n" \
"ds_read_b64_tr_b16 a[108:109], v209 offset:12288\n" \
"v_fma_f32 v242, v152, v153, v242\n" \
"v_mfma_f32_16x16x32_bf16 v[92:95], a[84:87], a[220:223], v[92:95]\n" \
"global_load_lds_dwordx4 v252, s[44:45] offset:1024\n" \
"v_cvt_pk_bf16_f32 v152, v152, v153\n" \
"v_mfma_f32_16x16x32_bf16 v[88:91], a[80:83], a[208:211], v[88:91]\n" \
"ds_read_b64_tr_b16 a[110:111], v209 offset:13312\n" \
"v_fma_f32 v242, v154, v155, v242\n" \
"v_mfma_f32_16x16x32_bf16 v[88:91], a[84:87], a[212:215], v[88:91]\n" \
"ds_read_b64_tr_b16 a[112:113], v210 offset:0\n" \
"v_cvt_pk_bf16_f32 v153, v154, v155\n" \
"v_mfma_f32_16x16x32_bf16 v[96:99], a[88:91], a[208:211], v[96:99]\n" \
"ds_read_b128 a[80:83], v205 offset:4096\n" \
"v_fma_f32 v242, v156, v157, v242\n" \
"v_mfma_f32_16x16x32_bf16 v[96:99], a[92:95], a[212:215], v[96:99]\n" \
"global_load_lds_dwordx4 v252, s[44:45] offset:3072\n" \
"v_cvt_pk_bf16_f32 v154, v156, v157\n" \
"v_mfma_f32_16x16x32_bf16 v[100:103], a[88:91], a[216:219], v[100:103]\n" \
"s_add_u32 s44, s44, 0x100\n" \
"s_addc_u32 s45, s45, 0\n" \
"ds_read_b128 a[84:87], v206 offset:4096\n" \
"v_mfma_f32_16x16x32_bf16 v[100:103], a[92:95], a[220:223], v[100:103]\n" \
"ds_read_b64_tr_b16 a[114:115], v210 offset:1024\n" \
"v_fma_f32 v242, v158, v159, v242\n" \
"v_mfma_f32_16x16x32_bf16 v[60:63], a[200:203], a[88:91], v[60:63]\n" \
"s_add_u32 m0, s85, 81920\n" \
"ds_read_b64_tr_b16 a[116:117], v210 offset:12288\n" \
"v_cvt_pk_bf16_f32 v155, v158, v159\n" \
"v_mfma_f32_16x16x32_bf16 v[60:63], a[204:207], a[92:95], v[60:63]\n" \
"buffer_store_dwordx4 v[152:155], v233, s[56:59], s78 offen offset:2048 sc1\n" \
"buffer_load_dwordx4 v246, s[12:15], s46 offen lds\n" \
"s_sub_u32 s78, s78, 4096\n" \
"v_mov_b32 v234, v243\n" \
"v_mfma_f32_16x16x32_bf16 v[68:71], a[200:203], a[192:195], v[68:71]\n" \
"ds_read_b128 a[88:91], v205 offset:8192\n" \
"s_add_u32 s46, s46, 0x100\n" \
"v_mfma_f32_16x16x32_bf16 v[68:71], a[204:207], a[196:199], v[68:71]\n" \
"ds_read_b128 a[92:95], v206 offset:8192\n" \
"v_permlane16_swap_b32 v234, v243\n" \
"v_add_f32 v243, v243, v234\n" \
"v_mfma_f32_16x16x32_bf16 v[108:111], a[192:195], a[216:219], v[108:111]\n" \
"s_add_u32 m0, s84, 90112\n" \
"ds_read_b64_tr_b16 a[118:119], v210 offset:13312\n" \
"v_mov_b32 v234, v243\n" \
"v_mfma_f32_16x16x32_bf16 v[108:111], a[196:199], a[220:223], v[108:111]\n" \
"global_load_lds_dwordx4 v253, s[48:49] offset:-4096\n" \
"v_mfma_f32_16x16x32_bf16 v[104:107], a[192:195], a[208:211], v[104:107]\n" \
"v_permlane32_swap_b32 v234, v243\n" \
"v_mfma_f32_16x16x32_bf16 v[104:107], a[196:199], a[212:215], v[104:107]\n" \
"s_waitcnt lgkmcnt(2)\n" \
"v_add_f32 v243, v243, v234\n" \
"v_rcp_f32 v234, v243\n" \
"v_mfma_f32_16x16x32_bf16 v[32:35], a[96:99], a[64:67], v[32:35]\n" \
"ds_read_b128 a[192:195], v205 offset:12288\n" \
"v_mul_f32 v243, 0x4d800000, v243\n" \
"v_mfma_f32_16x16x32_bf16 v[32:35], a[100:103], a[68:71], v[32:35]\n" \
"global_load_lds_dwordx4 v253, s[48:49] offset:-2048\n" \
"v_add_u32 v235, 0x1c90080, v249\n" \
"v_mfma_f32_16x16x32_bf16 v[40:43], a[96:99], a[72:75], v[40:43]\n" \
"ds_read_b128 a[196:199], v206 offset:12288\n" \
"v_cndmask_b32 v235, v249, v235, s[16:17]\n" \
"v_mfma_f32_16x16x32_bf16 v[40:43], a[100:103], a[76:79], v[40:43]\n" \
"s_add_u32 s78, s78, 532480\n" \
"ds_read_b64_tr_b16 a[208:209], v209 offset:4096\n" \
"v_mul_f32 v160, v160, v234\n" \
"v_mfma_f32_16x16x32_bf16 v[0:3], a[104:107], a[72:75], v[0:3]\n" \
"ds_read_b64_tr_b16 a[210:211], v209 offset:5120\n" \
"v_mul_f32 v161, v161, v234\n" \
"v_mfma_f32_16x16x32_bf16 v[0:3], a[108:111], a[76:79], v[0:3]\n" \
"s_waitcnt lgkmcnt(3)\n" \
"global_load_lds_dwordx4 v253, s[48:49] offset:0\n" \
"v_mul_f32 v162, v162, v234\n" \
"v_mfma_f32_16x16x32_bf16 v[4:7], a[112:115], a[72:75], v[4:7]\n" \
"s_add_u32 s48, s48, 0xc0000\n" \
"s_addc_u32 s49, s49, 0\n" \
"ds_read_b64_tr_b16 a[212:213], v209 offset:16384\n" \
"v_mfma_f32_16x16x32_bf16 v[4:7], a[116:119], a[76:79], v[4:7]\n" \
"ds_read_b64_tr_b16 a[214:215], v209 offset:17408\n" \
"v_mul_f32 v163, v163, v234\n" \
"v_mfma_f32_16x16x32_bf16 v[12:15], a[112:115], a[80:83], v[12:15]\n" \
"ds_read_b64_tr_b16 a[216:217], v210 offset:4096\n" \
"v_mul_f32 v164, v164, v234\n" \
"v_mfma_f32_16x16x32_bf16 v[12:15], a[116:119], a[84:87], v[12:15]\n" \
"global_load_lds_dwordx4 v253, s[50:51] offset:-3072\n" \
"v_mul_f32 v165, v165, v234\n" \
"v_mfma_f32_16x16x32_bf16 v[8:11], a[104:107], a[80:83], v[8:11]\n" \
"ds_read_b64_tr_b16 a[218:219], v210 offset:5120\n" \
"v_mul_f32 v166, v166, v234\n" \
"v_mfma_f32_16x16x32_bf16 v[8:11], a[108:111], a[84:87], v[8:11]\n" \
"ds_read_b64_tr_b16 a[220:221], v210 offset:16384\n" \
"v_mul_f32 v167, v167, v234\n" \
"v_mfma_f32_16x16x32_bf16 v[48:51], a[96:99], a[80:83], v[48:51]\n" \
"ds_read_b64_tr_b16 a[222:223], v210 offset:17408\n" \
"v_fma_f32 v243, v160, v161, v243\n" \
"v_mfma_f32_16x16x32_bf16 v[48:51], a[100:103], a[84:87], v[48:51]\n" \
"global_load_lds_dwordx4 v253, s[50:51] offset:-1024\n" \
"v_cvt_pk_bf16_f32 v160, v160, v161\n" \
"v_mfma_f32_16x16x32_bf16 v[56:59], a[96:99], a[88:91], v[56:59]\n" \
"ds_read_b64_tr_b16 a[200:201], v210 offset:2048\n" \
"v_fma_f32 v243, v162, v163, v243\n" \
"v_mfma_f32_16x16x32_bf16 v[56:59], a[100:103], a[92:95], v[56:59]\n" \
"ds_read_b64_tr_b16 a[202:203], v210 offset:3072\n" \
"v_cvt_pk_bf16_f32 v161, v162, v163\n" \
"v_mfma_f32_16x16x32_bf16 v[16:19], a[104:107], a[88:91], v[16:19]\n" \
"ds_read_b64_tr_b16 a[204:205], v210 offset:14336\n" \
"v_fma_f32 v243, v164, v165, v243\n" \
"v_mfma_f32_16x16x32_bf16 v[16:19], a[108:111], a[92:95], v[16:19]\n" \
"global_load_lds_dwordx4 v253, s[50:51] offset:1024\n" \
"v_cvt_pk_bf16_f32 v162, v164, v165\n" \
"v_mfma_f32_16x16x32_bf16 v[20:23], a[112:115], a[88:91], v[20:23]\n" \
"s_add_u32 s50, s50, 0xc0000\n" \
"s_addc_u32 s51, s51, 0\n" \
"ds_read_b64_tr_b16 a[206:207], v210 offset:15360\n" \
"v_mfma_f32_16x16x32_bf16 v[20:23], a[116:119], a[92:95], v[20:23]\n" \
"s_waitcnt lgkmcnt(3)\n" \
"v_add_u32 v207, 0x1d000, v207\n" \
"v_add_u32 v208, 0x1d000, v208\n" \
"v_mfma_f32_16x16x32_bf16 v[28:31], a[112:115], a[192:195], v[28:31]\n" \
"v_add_u32 v205, 0x1d000, v205\n" \
"v_add_u32 v206, 0x1d000, v206\n" \
"v_mfma_f32_16x16x32_bf16 v[28:31], a[116:119], a[196:199], v[28:31]\n" \
"v_add_u32 v209, 0x1d000, v209\n" \
"v_add_u32 v210, 0x1d000, v210\n" \
"v_mfma_f32_16x16x32_bf16 v[24:27], a[104:107], a[192:195], v[24:27]\n" \
"v_fma_f32 v243, v166, v167, v243\n" \
"v_cvt_pk_bf16_f32 v163, v166, v167\n" \
"v_mfma_f32_16x16x32_bf16 v[24:27], a[108:111], a[196:199], v[24:27]\n" \
"buffer_store_dwordx4 v[160:163], v235, s[56:59], s78 offen offset:0 sc1\n" \
"v_mul_f32 v168, v168, v234\n" \
"v_mul_f32 v169, v169, v234\n" \
"v_mul_f32 v170, v170, v234\n" \
"v_mul_f32 v171, v171, v234\n" \
"v_mfma_f32_16x16x32_bf16 v[64:67], a[96:99], a[192:195], v[64:67]\n" \
"s_waitcnt vmcnt(14) lgkmcnt(0)\n" \
"v_mul_f32 v172, v172, v234\n" \
"v_mul_f32 v173, v173, v234\n" \
"v_mfma_f32_16x16x32_bf16 v[64:67], a[100:103], a[196:199], v[64:67]\n" \
"s_barrier\n" \
"v_mfma_f32_16x16x32_bf16 v[72:75], a[64:67], a[208:211], v[72:75]\n" \
"ds_read_b64_tr_b16 a[96:97], v209 offset:2048\n" \
"v_mul_f32 v174, v174, v234\n" \
"v_mfma_f32_16x16x32_bf16 v[72:75], a[68:71], a[212:215], v[72:75]\n" \
"ds_read_b64_tr_b16 a[98:99], v209 offset:3072\n" \
"Lpg9317_%=:\n" \
"Lst1x3_%=:\n" \
"s_cbranch_execz Lpg11577_%=\n" \
"v_mul_f32 v175, v175, v234\n" \
"v_mfma_f32_16x16x32_bf16 v[80:83], a[72:75], a[208:211], v[80:83]\n" \
"s_add_u32 m0, s95, 0\n" \
"ds_read_b128 a[64:67], v207 offset:0\n" \
"ds_read_b64_tr_b16 a[100:101], v209 offset:14336\n" \
"v_mfma_f32_16x16x32_bf16 v[80:83], a[76:79], a[212:215], v[80:83]\n" \
"global_load_lds_dwordx4 v252, s[42:43] offset:0\n" \
"v_fma_f32 v243, v168, v169, v243\n" \
"v_mfma_f32_16x16x32_bf16 v[84:87], a[72:75], a[216:219], v[84:87]\n" \
"ds_read_b128 a[68:71], v208 offset:0\n" \
"ds_read_b64_tr_b16 a[102:103], v209 offset:15360\n" \
"v_mfma_f32_16x16x32_bf16 v[84:87], a[76:79], a[220:223], v[84:87]\n" \
"ds_read_b64_tr_b16 a[104:105], v209 offset:0\n" \
"v_cvt_pk_bf16_f32 v168, v168, v169\n" \
"v_mfma_f32_16x16x32_bf16 v[44:47], a[200:203], a[72:75], v[44:47]\n" \
"ds_read_b64_tr_b16 a[106:107], v209 offset:1024\n" \
"v_fma_f32 v243, v170, v171, v243\n" \
"v_mfma_f32_16x16x32_bf16 v[44:47], a[204:207], a[76:79], v[44:47]\n" \
"global_load_lds_dwordx4 v252, s[42:43] offset:2048\n" \
"v_cvt_pk_bf16_f32 v169, v170, v171\n" \
"v_mfma_f32_16x16x32_bf16 v[52:55], a[200:203], a[80:83], v[52:55]\n" \
"s_add_u32 s42, s42, 0x80\n" \
"s_addc_u32 s43, s43, 0\n" \
"ds_read_b128 a[72:75], v205 offset:0\n" \
"v_mfma_f32_16x16x32_bf16 v[52:55], a[204:207], a[84:87], v[52:55]\n" \
"ds_read_b128 a[76:79], v206 offset:0\n" \
"v_fma_f32 v243, v172, v173, v243\n" \
"v_mfma_f32_16x16x32_bf16 v[92:95], a[80:83], a[216:219], v[92:95]\n" \
"ds_read_b64_tr_b16 a[108:109], v209 offset:12288\n" \
"v_cvt_pk_bf16_f32 v170, v172, v173\n" \
"v_mfma_f32_16x16x32_bf16 v[92:95], a[84:87], a[220:223], v[92:95]\n" \
"global_load_lds_dwordx4 v252, s[44:45] offset:1024\n" \
"v_fma_f32 v243, v174, v175, v243\n" \
"v_mfma_f32_16x16x32_bf16 v[88:91], a[80:83], a[208:211], v[88:91]\n" \
"ds_read_b64_tr_b16 a[110:111], v209 offset:13312\n" \
"v_cvt_pk_bf16_f32 v171, v174, v175\n" \
"v_mfma_f32_16x16x32_bf16 v[88:91], a[84:87], a[212:215], v[88:91]\n" \
"buffer_store_dwordx4 v[168:171], v235, s[56:59], s78 offen offset:2048 sc1\n" \
"ds_read_b64_tr_b16 a[112:113], v210 offset:0\n" \
"s_add_u32 s78, s78, 0x1000\n" \
"v_mul_f32 v176, v176, v234\n" \
"v_mul_f32 v177, v177, v234\n" \
"v_mfma_f32_16x16x32_bf16 v[96:99], a[88:91], a[208:211], v[96:99]\n" \
"ds_read_b128 a[80:83], v205 offset:4096\n" \
"v_mul_f32 v178, v178, v234\n" \
"v_mfma_f32_16x16x32_bf16 v[96:99], a[92:95], a[212:215], v[96:99]\n" \
"global_load_lds_dwordx4 v252, s[44:45] offset:3072\n" \
"v_mul_f32 v179, v179, v234\n" \
"v_mfma_f32_16x16x32_bf16 v[100:103], a[88:91], a[216:219], v[100:103]\n" \
"s_add_u32 s44, s44, 0x80\n" \
"s_addc_u32 s45, s45, 0\n" \
"ds_read_b128 a[84:87], v206 offset:4096\n" \
"v_mfma_f32_16x16x32_bf16 v[100:103], a[92:95], a[220:223], v[100:103]\n" \
"ds_read_b64_tr_b16 a[114:115], v210 offset:1024\n" \
"v_mul_f32 v180, v180, v234\n" \
"v_mfma_f32_16x16x32_bf16 v[60:63], a[200:203], a[88:91], v[60:63]\n" \
"s_add_u32 m0, s85, 16384\n" \
"ds_read_b64_tr_b16 a[116:117], v210 offset:12288\n" \
"v_mul_f32 v181, v181, v234\n" \
"v_mfma_f32_16x16x32_bf16 v[60:63], a[204:207], a[92:95], v[60:63]\n" \
"buffer_load_dwordx4 v246, s[12:15], s46 offen lds\n" \
"v_mfma_f32_16x16x32_bf16 v[68:71], a[200:203], a[192:195], v[68:71]\n" \
"s_add_u32 s46, s46, 0x80\n" \
"ds_read_b128 a[88:91], v205 offset:8192\n" \
"v_mul_f32 v182, v182, v234\n" \
"v_mfma_f32_16x16x32_bf16 v[68:71], a[204:207], a[196:199], v[68:71]\n" \
"ds_read_b128 a[92:95], v206 offset:8192\n" \
"v_mul_f32 v183, v183, v234\n" \
"v_mfma_f32_16x16x32_bf16 v[108:111], a[192:195], a[216:219], v[108:111]\n" \
"s_add_u32 m0, s84, 24576\n" \
"ds_read_b64_tr_b16 a[118:119], v210 offset:13312\n" \
"v_fma_f32 v243, v176, v177, v243\n" \
"v_mfma_f32_16x16x32_bf16 v[108:111], a[196:199], a[220:223], v[108:111]\n" \
"global_load_lds_dwordx4 v253, s[48:49] offset:-4096\n" \
"v_cvt_pk_bf16_f32 v176, v176, v177\n" \
"v_mfma_f32_16x16x32_bf16 v[104:107], a[192:195], a[208:211], v[104:107]\n" \
"v_fma_f32 v243, v178, v179, v243\n" \
"v_cvt_pk_bf16_f32 v177, v178, v179\n" \
"v_mfma_f32_16x16x32_bf16 v[104:107], a[196:199], a[212:215], v[104:107]\n" \
"s_waitcnt lgkmcnt(2)\n" \
"v_fma_f32 v243, v180, v181, v243\n" \
"v_cvt_pk_bf16_f32 v178, v180, v181\n" \
"v_mfma_f32_16x16x32_bf16 v[32:35], a[96:99], a[64:67], v[32:35]\n" \
"ds_read_b128 a[192:195], v205 offset:12288\n" \
"v_fma_f32 v243, v182, v183, v243\n" \
"v_mfma_f32_16x16x32_bf16 v[32:35], a[100:103], a[68:71], v[32:35]\n" \
"global_load_lds_dwordx4 v253, s[48:49] offset:-2048\n" \
"v_cvt_pk_bf16_f32 v179, v182, v183\n" \
"v_mfma_f32_16x16x32_bf16 v[40:43], a[96:99], a[72:75], v[40:43]\n" \
"ds_read_b128 a[196:199], v206 offset:12288\n" \
"v_mfma_f32_16x16x32_bf16 v[40:43], a[100:103], a[76:79], v[40:43]\n" \
"buffer_store_dwordx4 v[176:179], v235, s[56:59], s78 offen offset:0 sc1\n" \
"ds_read_b64_tr_b16 a[208:209], v209 offset:4096\n" \
"v_mul_f32 v184, v184, v234\n" \
"v_mul_f32 v185, v185, v234\n" \
"v_mul_f32 v186, v186, v234\n" \
"v_mfma_f32_16x16x32_bf16 v[0:3], a[104:107], a[72:75], v[0:3]\n" \
"ds_read_b64_tr_b16 a[210:211], v209 offset:5120\n" \
"v_mul_f32 v187, v187, v234\n" \
"v_mfma_f32_16x16x32_bf16 v[0:3], a[108:111], a[76:79], v[0:3]\n" \
"s_waitcnt lgkmcnt(3)\n" \
"global_load_lds_dwordx4 v253, s[48:49] offset:0\n" \
"v_mul_f32 v188, v188, v234\n" \
"v_mfma_f32_16x16x32_bf16 v[4:7], a[112:115], a[72:75], v[4:7]\n" \
"s_add_u32 s48, s48, 0x60000\n" \
"s_addc_u32 s49, s49, 0\n" \
"ds_read_b64_tr_b16 a[212:213], v209 offset:16384\n" \
"v_mfma_f32_16x16x32_bf16 v[4:7], a[116:119], a[76:79], v[4:7]\n" \
"ds_read_b64_tr_b16 a[214:215], v209 offset:17408\n" \
"v_mul_f32 v189, v189, v234\n" \
"v_mfma_f32_16x16x32_bf16 v[12:15], a[112:115], a[80:83], v[12:15]\n" \
"ds_read_b64_tr_b16 a[216:217], v210 offset:4096\n" \
"v_mul_f32 v190, v190, v234\n" \
"v_mfma_f32_16x16x32_bf16 v[12:15], a[116:119], a[84:87], v[12:15]\n" \
"global_load_lds_dwordx4 v253, s[50:51] offset:-3072\n" \
"v_mul_f32 v191, v191, v234\n" \
"v_mfma_f32_16x16x32_bf16 v[8:11], a[104:107], a[80:83], v[8:11]\n" \
"ds_read_b64_tr_b16 a[218:219], v210 offset:5120\n" \
"v_fma_f32 v243, v184, v185, v243\n" \
"v_mfma_f32_16x16x32_bf16 v[8:11], a[108:111], a[84:87], v[8:11]\n" \
"ds_read_b64_tr_b16 a[220:221], v210 offset:16384\n" \
"v_cvt_pk_bf16_f32 v184, v184, v185\n" \
"v_mfma_f32_16x16x32_bf16 v[48:51], a[96:99], a[80:83], v[48:51]\n" \
"ds_read_b64_tr_b16 a[222:223], v210 offset:17408\n" \
"v_fma_f32 v243, v186, v187, v243\n" \
"v_mfma_f32_16x16x32_bf16 v[48:51], a[100:103], a[84:87], v[48:51]\n" \
"global_load_lds_dwordx4 v253, s[50:51] offset:-1024\n" \
"v_cvt_pk_bf16_f32 v185, v186, v187\n" \
"v_mfma_f32_16x16x32_bf16 v[56:59], a[96:99], a[88:91], v[56:59]\n" \
"ds_read_b64_tr_b16 a[200:201], v210 offset:2048\n" \
"v_fma_f32 v243, v188, v189, v243\n" \
"v_mfma_f32_16x16x32_bf16 v[56:59], a[100:103], a[92:95], v[56:59]\n" \
"ds_read_b64_tr_b16 a[202:203], v210 offset:3072\n" \
"v_cvt_pk_bf16_f32 v186, v188, v189\n" \
"v_mfma_f32_16x16x32_bf16 v[16:19], a[104:107], a[88:91], v[16:19]\n" \
"ds_read_b64_tr_b16 a[204:205], v210 offset:14336\n" \
"v_fma_f32 v243, v190, v191, v243\n" \
"v_mfma_f32_16x16x32_bf16 v[16:19], a[108:111], a[92:95], v[16:19]\n" \
"global_load_lds_dwordx4 v253, s[50:51] offset:1024\n" \
"v_cvt_pk_bf16_f32 v187, v190, v191\n" \
"v_mfma_f32_16x16x32_bf16 v[20:23], a[112:115], a[88:91], v[20:23]\n" \
"s_add_u32 s50, s50, 0x60000\n" \
"s_addc_u32 s51, s51, 0\n" \
"ds_read_b64_tr_b16 a[206:207], v210 offset:15360\n" \
"v_mfma_f32_16x16x32_bf16 v[20:23], a[116:119], a[92:95], v[20:23]\n" \
"s_waitcnt lgkmcnt(3)\n" \
"buffer_store_dwordx4 v[184:187], v235, s[56:59], s78 offen offset:2048 sc1\n" \
"v_add_u32 v207, 0xffff3000, v207\n" \
"v_add_u32 v208, 0xffff3000, v208\n" \
"s_sub_u32 s78, s78, 536576\n" \
"v_add_f32 v240, v240, v241\n" \
"v_mfma_f32_16x16x32_bf16 v[28:31], a[112:115], a[192:195], v[28:31]\n" \
"v_add_u32 v205, 0xffff3000, v205\n" \
"v_add_u32 v206, 0xffff3000, v206\n" \
"v_mfma_f32_16x16x32_bf16 v[28:31], a[116:119], a[196:199], v[28:31]\n" \
"v_add_u32 v209, 0xffff3000, v209\n" \
"v_add_u32 v210, 0xffff3000, v210\n" \
"v_mfma_f32_16x16x32_bf16 v[24:27], a[104:107], a[192:195], v[24:27]\n" \
"v_add_f32 v242, v242, v243\n" \
"v_mov_b32 v241, 0x207\n" \
"v_mfma_f32_16x16x32_bf16 v[24:27], a[108:111], a[196:199], v[24:27]\n" \
"v_add_f32 v240, v240, v242\n" \
"v_cmp_class_f32 vcc, v240, v241\n" \
"v_mfma_f32_16x16x32_bf16 v[64:67], a[96:99], a[192:195], v[64:67]\n" \
"s_waitcnt vmcnt(15) lgkmcnt(0)\n" \
"v_mfma_f32_16x16x32_bf16 v[64:67], a[100:103], a[196:199], v[64:67]\n" \
"s_barrier\n" \
"s_cbranch_vccnz Lexact_%=\n" \
"Lexr_%=:\n" \
"v_mfma_f32_16x16x32_bf16 v[72:75], a[64:67], a[208:211], v[72:75]\n" \
"ds_read_b64_tr_b16 a[96:97], v209 offset:2048\n" \
"v_mfma_f32_16x16x32_bf16 v[72:75], a[68:71], a[212:215], v[72:75]\n" \
"ds_read_b64_tr_b16 a[98:99], v209 offset:3072\n" \
"v_mfma_f32_16x16x32_bf16 v[80:83], a[72:75], a[208:211], v[80:83]\n" \
"s_add_u32 m0, s95, 118784\n" \
"ds_read_b128 a[64:67], v207 offset:0\n" \
"ds_read_b64_tr_b16 a[100:101], v209 offset:14336\n" \
"v_mfma_f32_16x16x32_bf16 v[80:83], a[76:79], a[212:215], v[80:83]\n" \
"global_load_lds_dwordx4 v252, s[42:43] offset:0\n" \
"v_mfma_f32_16x16x32_bf16 v[84:87], a[72:75], a[216:219], v[84:87]\n" \
"ds_read_b128 a[68:71], v208 offset:0\n" \
"ds_read_b64_tr_b16 a[102:103], v209 offset:15360\n" \
"v_mfma_f32_16x16x32_bf16 v[84:87], a[76:79], a[220:223], v[84:87]\n" \
"ds_read_b64_tr_b16 a[104:105], v209 offset:0\n" \
"v_mfma_f32_16x16x32_bf16 v[44:47], a[200:203], a[72:75], v[44:47]\n" \
"ds_read_b64_tr_b16 a[106:107], v209 offset:1024\n" \
"v_mfma_f32_16x16x32_bf16 v[44:47], a[204:207], a[76:79], v[44:47]\n" \
"global_load_lds_dwordx4 v252, s[42:43] offset:2048\n" \
"v_mfma_f32_16x16x32_bf16 v[52:55], a[200:203], a[80:83], v[52:55]\n" \
"s_add_u32 s42, s42, 0x80\n" \
"s_addc_u32 s43, s43, 0\n" \
"ds_read_b128 a[72:75], v205 offset:0\n" \
"v_mfma_f32_16x16x32_bf16 v[52:55], a[204:207], a[84:87], v[52:55]\n" \
"ds_read_b128 a[76:79], v206 offset:0\n" \
"v_mfma_f32_16x16x32_bf16 v[92:95], a[80:83], a[216:219], v[92:95]\n" \
"ds_read_b64_tr_b16 a[108:109], v209 offset:12288\n" \
"v_mfma_f32_16x16x32_bf16 v[92:95], a[84:87], a[220:223], v[92:95]\n" \
"global_load_lds_dwordx4 v252, s[44:45] offset:1024\n" \
"v_mfma_f32_16x16x32_bf16 v[88:91], a[80:83], a[208:211], v[88:91]\n" \
"ds_read_b64_tr_b16 a[110:111], v209 offset:13312\n" \
"v_mfma_f32_16x16x32_bf16 v[88:91], a[84:87], a[212:215], v[88:91]\n" \
"ds_read_b64_tr_b16 a[112:113], v210 offset:0\n" \
"v_mfma_f32_16x16x32_bf16 v[96:99], a[88:91], a[208:211], v[96:99]\n" \
"ds_read_b128 a[80:83], v205 offset:4096\n" \
"v_mfma_f32_16x16x32_bf16 v[96:99], a[92:95], a[212:215], v[96:99]\n" \
"global_load_lds_dwordx4 v252, s[44:45] offset:3072\n" \
"v_mfma_f32_16x16x32_bf16 v[100:103], a[88:91], a[216:219], v[100:103]\n" \
"s_add_u32 s44, s44, 0x80\n" \
"s_addc_u32 s45, s45, 0\n" \
"ds_read_b128 a[84:87], v206 offset:4096\n" \
"v_mfma_f32_16x16x32_bf16 v[100:103], a[92:95], a[220:223], v[100:103]\n" \
"ds_read_b64_tr_b16 a[114:115], v210 offset:1024\n" \
"v_mfma_f32_16x16x32_bf16 v[60:63], a[200:203], a[88:91], v[60:63]\n" \
"s_add_u32 m0, s85, 135168\n" \
"ds_read_b64_tr_b16 a[116:117], v210 offset:12288\n" \
"v_mfma_f32_16x16x32_bf16 v[60:63], a[204:207], a[92:95], v[60:63]\n" \
"buffer_load_dwordx4 v246, s[12:15], s46 offen lds\n" \
"v_mfma_f32_16x16x32_bf16 v[68:71], a[200:203], a[192:195], v[68:71]\n" \
"ds_read_b128 a[88:91], v205 offset:8192\n" \
"s_add_u32 s46, s46, 0x80\n" \
"v_mfma_f32_16x16x32_bf16 v[68:71], a[204:207], a[196:199], v[68:71]\n" \
"ds_read_b128 a[92:95], v206 offset:8192\n" \
"v_mfma_f32_16x16x32_bf16 v[108:111], a[192:195], a[216:219], v[108:111]\n" \
"s_add_u32 m0, s84, 143360\n" \
"ds_read_b64_tr_b16 a[118:119], v210 offset:13312\n" \
"v_mfma_f32_16x16x32_bf16 v[108:111], a[196:199], a[220:223], v[108:111]\n" \
"global_load_lds_dwordx4 v253, s[48:49] offset:-4096\n" \
"v_mfma_f32_16x16x32_bf16 v[104:107], a[192:195], a[208:211], v[104:107]\n" \
"v_mfma_f32_16x16x32_bf16 v[104:107], a[196:199], a[212:215], v[104:107]\n" \
"s_waitcnt lgkmcnt(2)\n" \
"v_mfma_f32_16x16x32_bf16 v[32:35], a[96:99], a[64:67], v[32:35]\n" \
"ds_read_b128 a[192:195], v205 offset:12288\n" \
"v_mfma_f32_16x16x32_bf16 v[32:35], a[100:103], a[68:71], v[32:35]\n" \
"global_load_lds_dwordx4 v253, s[48:49] offset:-2048\n" \
"v_mfma_f32_16x16x32_bf16 v[40:43], a[96:99], a[72:75], v[40:43]\n" \
"ds_read_b128 a[196:199], v206 offset:12288\n" \
"v_mfma_f32_16x16x32_bf16 v[40:43], a[100:103], a[76:79], v[40:43]\n" \
"ds_read_b64_tr_b16 a[208:209], v209 offset:4096\n" \
"v_mfma_f32_16x16x32_bf16 v[0:3], a[104:107], a[72:75], v[0:3]\n" \
"ds_read_b64_tr_b16 a[210:211], v209 offset:5120\n" \
"v_mfma_f32_16x16x32_bf16 v[0:3], a[108:111], a[76:79], v[0:3]\n" \
"s_waitcnt lgkmcnt(3)\n" \
"global_load_lds_dwordx4 v253, s[48:49] offset:0\n" \
"v_mfma_f32_16x16x32_bf16 v[4:7], a[112:115], a[72:75], v[4:7]\n" \
"s_add_u32 s48, s48, 0x60000\n" \
"s_addc_u32 s49, s49, 0\n" \
"ds_read_b64_tr_b16 a[212:213], v209 offset:16384\n" \
"v_mfma_f32_16x16x32_bf16 v[4:7], a[116:119], a[76:79], v[4:7]\n" \
"ds_read_b64_tr_b16 a[214:215], v209 offset:17408\n" \
"v_mfma_f32_16x16x32_bf16 v[12:15], a[112:115], a[80:83], v[12:15]\n" \
"ds_read_b64_tr_b16 a[216:217], v210 offset:4096\n" \
"v_mfma_f32_16x16x32_bf16 v[12:15], a[116:119], a[84:87], v[12:15]\n" \
"global_load_lds_dwordx4 v253, s[50:51] offset:-3072\n" \
"v_mfma_f32_16x16x32_bf16 v[8:11], a[104:107], a[80:83], v[8:11]\n" \
"ds_read_b64_tr_b16 a[218:219], v210 offset:5120\n" \
"v_mfma_f32_16x16x32_bf16 v[8:11], a[108:111], a[84:87], v[8:11]\n" \
"ds_read_b64_tr_b16 a[220:221], v210 offset:16384\n" \
"v_mfma_f32_16x16x32_bf16 v[48:51], a[96:99], a[80:83], v[48:51]\n" \
"ds_read_b64_tr_b16 a[222:223], v210 offset:17408\n" \
"v_mfma_f32_16x16x32_bf16 v[48:51], a[100:103], a[84:87], v[48:51]\n" \
"global_load_lds_dwordx4 v253, s[50:51] offset:-1024\n" \
"v_mfma_f32_16x16x32_bf16 v[56:59], a[96:99], a[88:91], v[56:59]\n" \
"ds_read_b64_tr_b16 a[200:201], v210 offset:2048\n" \
"v_mfma_f32_16x16x32_bf16 v[56:59], a[100:103], a[92:95], v[56:59]\n" \
"ds_read_b64_tr_b16 a[202:203], v210 offset:3072\n" \
"v_mfma_f32_16x16x32_bf16 v[16:19], a[104:107], a[88:91], v[16:19]\n" \
"ds_read_b64_tr_b16 a[204:205], v210 offset:14336\n" \
"v_mfma_f32_16x16x32_bf16 v[16:19], a[108:111], a[92:95], v[16:19]\n" \
"global_load_lds_dwordx4 v253, s[50:51] offset:1024\n" \
"v_mfma_f32_16x16x32_bf16 v[20:23], a[112:115], a[88:91], v[20:23]\n" \
"s_add_u32 s50, s50, 0x60000\n" \
"s_addc_u32 s51, s51, 0\n" \
"ds_read_b64_tr_b16 a[206:207], v210 offset:15360\n" \
"v_mfma_f32_16x16x32_bf16 v[20:23], a[116:119], a[92:95], v[20:23]\n" \
"s_waitcnt lgkmcnt(3)\n" \
"v_add_u32 v207, 0xffff0000, v207\n" \
"v_add_u32 v208, 0xffff0000, v208\n" \
"v_mfma_f32_16x16x32_bf16 v[28:31], a[112:115], a[192:195], v[28:31]\n" \
"v_add_u32 v205, 0xffff0000, v205\n" \
"v_add_u32 v206, 0xffff0000, v206\n" \
"v_mfma_f32_16x16x32_bf16 v[28:31], a[116:119], a[196:199], v[28:31]\n" \
"v_add_u32 v209, 0xffff0000, v209\n" \
"v_add_u32 v210, 0xffff0000, v210\n" \
"v_mfma_f32_16x16x32_bf16 v[24:27], a[104:107], a[192:195], v[24:27]\n" \
"v_mfma_f32_16x16x32_bf16 v[24:27], a[108:111], a[196:199], v[24:27]\n" \
"s_lshl_b32 s70, s63, 7\n" \
"s_lshl_b32 s71, s62, 2\n" \
"s_add_u32 s71, s71, 65589248\n" \
"v_mfma_f32_16x16x32_bf16 v[64:67], a[96:99], a[192:195], v[64:67]\n" \
"s_waitcnt vmcnt(11) lgkmcnt(0)\n" \
"s_add_u32 s72, s71, s70\n" \
"s_sub_u32 s71, s71, s70\n" \
"v_mfma_f32_16x16x32_bf16 v[64:67], a[100:103], a[196:199], v[64:67]\n" \
"s_barrier\n" \
"v_mfma_f32_16x16x32_bf16 v[72:75], a[64:67], a[208:211], v[72:75]\n" \
"s_add_u32 s71, s71, 1920\n" \
"ds_read_b64_tr_b16 a[96:97], v209 offset:2048\n" \
"v_mov_b32 v236, s60\n" \
"v_mfma_f32_16x16x32_bf16 v[72:75], a[68:71], a[212:215], v[72:75]\n" \
"ds_read_b64_tr_b16 a[98:99], v209 offset:3072\n" \
"v_lshlrev_b32 v237, 28, s61\n" \
"v_mfma_f32_16x16x32_bf16 v[80:83], a[72:75], a[208:211], v[80:83]\n" \
"s_add_u32 m0, s95, 65536\n" \
"ds_read_b128 a[64:67], v207 offset:0\n" \
"ds_read_b64_tr_b16 a[100:101], v209 offset:14336\n" \
"v_mfma_f32_16x16x32_bf16 v[80:83], a[76:79], a[212:215], v[80:83]\n" \
"buffer_store_dword v236, v237, s[56:59], s72 offen sc1\n" \
"global_load_lds_dwordx4 v252, s[42:43] offset:0\n" \
"v_mfma_f32_16x16x32_bf16 v[84:87], a[72:75], a[216:219], v[84:87]\n" \
"ds_read_b128 a[68:71], v208 offset:0\n" \
"ds_read_b64_tr_b16 a[102:103], v209 offset:15360\n" \
"v_mfma_f32_16x16x32_bf16 v[84:87], a[76:79], a[220:223], v[84:87]\n" \
"buffer_store_dword v236, v237, s[56:59], s71 offen sc1\n" \
"ds_read_b64_tr_b16 a[104:105], v209 offset:0\n" \
"v_mfma_f32_16x16x32_bf16 v[44:47], a[200:203], a[72:75], v[44:47]\n" \
"ds_read_b64_tr_b16 a[106:107], v209 offset:1024\n" \
"v_mfma_f32_16x16x32_bf16 v[44:47], a[204:207], a[76:79], v[44:47]\n" \
"global_load_lds_dwordx4 v252, s[42:43] offset:2048\n" \
"v_mfma_f32_16x16x32_bf16 v[52:55], a[200:203], a[80:83], v[52:55]\n" \
"s_add_u32 s42, s42, 0x80\n" \
"s_addc_u32 s43, s43, 0\n" \
"ds_read_b128 a[72:75], v205 offset:0\n" \
"v_mfma_f32_16x16x32_bf16 v[52:55], a[204:207], a[84:87], v[52:55]\n" \
"ds_read_b128 a[76:79], v206 offset:0\n" \
"v_mfma_f32_16x16x32_bf16 v[92:95], a[80:83], a[216:219], v[92:95]\n" \
"ds_read_b64_tr_b16 a[108:109], v209 offset:12288\n" \
"v_mfma_f32_16x16x32_bf16 v[92:95], a[84:87], a[220:223], v[92:95]\n" \
"global_load_lds_dwordx4 v252, s[44:45] offset:1024\n" \
"v_mfma_f32_16x16x32_bf16 v[88:91], a[80:83], a[208:211], v[88:91]\n" \
"ds_read_b64_tr_b16 a[110:111], v209 offset:13312\n" \
"v_mfma_f32_16x16x32_bf16 v[88:91], a[84:87], a[212:215], v[88:91]\n" \
"ds_read_b64_tr_b16 a[112:113], v210 offset:0\n" \
"v_mfma_f32_16x16x32_bf16 v[96:99], a[88:91], a[208:211], v[96:99]\n" \
"ds_read_b128 a[80:83], v205 offset:4096\n" \
"v_mfma_f32_16x16x32_bf16 v[96:99], a[92:95], a[212:215], v[96:99]\n" \
"global_load_lds_dwordx4 v252, s[44:45] offset:3072\n" \
"v_mfma_f32_16x16x32_bf16 v[100:103], a[88:91], a[216:219], v[100:103]\n" \
"s_add_u32 s44, s44, 0x80\n" \
"s_addc_u32 s45, s45, 0\n" \
"ds_read_b128 a[84:87], v206 offset:4096\n" \
"v_mfma_f32_16x16x32_bf16 v[100:103], a[92:95], a[220:223], v[100:103]\n" \
"ds_read_b64_tr_b16 a[114:115], v210 offset:1024\n" \
"v_mfma_f32_16x16x32_bf16 v[60:63], a[200:203], a[88:91], v[60:63]\n" \
"s_add_u32 m0, s85, 81920\n" \
"ds_read_b64_tr_b16 a[116:117], v210 offset:12288\n" \
"v_mfma_f32_16x16x32_bf16 v[60:63], a[204:207], a[92:95], v[60:63]\n" \
"buffer_load_dwordx4 v246, s[12:15], s46 offen lds\n" \
"v_mfma_f32_16x16x32_bf16 v[68:71], a[200:203], a[192:195], v[68:71]\n" \
"ds_read_b128 a[88:91], v205 offset:8192\n" \
"s_add_u32 s46, s46, 0x80\n" \
"v_mfma_f32_16x16x32_bf16 v[68:71], a[204:207], a[196:199], v[68:71]\n" \
"ds_read_b128 a[92:95], v206 offset:8192\n" \
"v_mfma_f32_16x16x32_bf16 v[108:111], a[192:195], a[216:219], v[108:111]\n" \
"s_add_u32 m0, s84, 90112\n" \
"ds_read_b64_tr_b16 a[118:119], v210 offset:13312\n" \
"v_mfma_f32_16x16x32_bf16 v[108:111], a[196:199], a[220:223], v[108:111]\n" \
"global_load_lds_dwordx4 v253, s[48:49] offset:-4096\n" \
"v_mfma_f32_16x16x32_bf16 v[104:107], a[192:195], a[208:211], v[104:107]\n" \
"v_mfma_f32_16x16x32_bf16 v[104:107], a[196:199], a[212:215], v[104:107]\n" \
"s_waitcnt lgkmcnt(2)\n" \
"v_mfma_f32_16x16x32_bf16 v[32:35], a[96:99], a[64:67], v[32:35]\n" \
"s_lshr_b32 s89, s22, 7\n" \
"ds_read_b128 a[192:195], v205 offset:12288\n" \
"s_sub_u32 s89, 3, s89\n" \
"v_mfma_f32_16x16x32_bf16 v[32:35], a[100:103], a[68:71], v[32:35]\n" \
"global_load_lds_dwordx4 v253, s[48:49] offset:-2048\n" \
"v_bfe_u32 v203, v248, 7, 2\n" \
"v_mfma_f32_16x16x32_bf16 v[40:43], a[96:99], a[72:75], v[40:43]\n" \
"s_and_b32 s89, s89, 7\n" \
"ds_read_b128 a[196:199], v206 offset:12288\n" \
"s_and_b32 s90, %1, 1\n" \
"v_mfma_f32_16x16x32_bf16 v[40:43], a[100:103], a[76:79], v[40:43]\n" \
"ds_read_b64_tr_b16 a[208:209], v209 offset:4096\n" \
"v_bfe_u32 v204, v248, 5, 2\n" \
"v_mfma_f32_16x16x32_bf16 v[0:3], a[104:107], a[72:75], v[0:3]\n" \
"s_lshl_b32 s90, s90, 3\n" \
"s_add_u32 s89, s89, s90\n" \
"ds_read_b64_tr_b16 a[210:211], v209 offset:5120\n" \
"v_mfma_f32_16x16x32_bf16 v[0:3], a[108:111], a[76:79], v[0:3]\n" \
"s_waitcnt lgkmcnt(3)\n" \
"global_load_lds_dwordx4 v253, s[48:49] offset:0\n" \
"v_min_u32 v204, 2, v204\n" \
"v_mfma_f32_16x16x32_bf16 v[4:7], a[112:115], a[72:75], v[4:7]\n" \
"s_add_u32 s48, s48, 0x60000\n" \
"s_addc_u32 s49, s49, 0\n" \
"ds_read_b64_tr_b16 a[212:213], v209 offset:16384\n" \
"v_mfma_f32_16x16x32_bf16 v[4:7], a[116:119], a[76:79], v[4:7]\n" \
"s_lshl_b32 s89, s89, 7\n" \
"s_lshr_b32 s90, %1, 3\n" \
"ds_read_b64_tr_b16 a[214:215], v209 offset:17408\n" \
"v_mfma_f32_16x16x32_bf16 v[12:15], a[112:115], a[80:83], v[12:15]\n" \
"ds_read_b64_tr_b16 a[216:217], v210 offset:4096\n" \
"v_mad_u32_u24 v203, v203, 3, v204\n" \
"v_mfma_f32_16x16x32_bf16 v[12:15], a[116:119], a[84:87], v[12:15]\n" \
"s_lshl_b32 s90, s90, 2\n" \
"global_load_lds_dwordx4 v253, s[50:51] offset:-3072\n" \
"v_mfma_f32_16x16x32_bf16 v[8:11], a[104:107], a[80:83], v[8:11]\n" \
"s_add_u32 s90, s90, s61\n" \
"ds_read_b64_tr_b16 a[218:219], v210 offset:5120\n" \
"v_and_b32 v204, 16, v248\n" \
"v_mfma_f32_16x16x32_bf16 v[8:11], a[108:111], a[84:87], v[8:11]\n" \
"s_add_u32 s90, s90, 128\n" \
"s_mul_i32 s90, s90, 24576\n" \
"ds_read_b64_tr_b16 a[220:221], v210 offset:16384\n" \
"v_mfma_f32_16x16x32_bf16 v[48:51], a[96:99], a[80:83], v[48:51]\n" \
"s_add_u32 s89, s89, s90\n" \
"ds_read_b64_tr_b16 a[222:223], v210 offset:17408\n" \
"v_lshl_add_u32 v204, v204, 2, s89\n" \
"v_mfma_f32_16x16x32_bf16 v[48:51], a[100:103], a[84:87], v[48:51]\n" \
"global_load_lds_dwordx4 v253, s[50:51] offset:-1024\n" \
"v_lshl_add_u32 v203, v203, 11, v204\n" \
"v_mfma_f32_16x16x32_bf16 v[56:59], a[96:99], a[88:91], v[56:59]\n" \
"ds_read_b64_tr_b16 a[200:201], v210 offset:2048\n" \
"v_mfma_f32_16x16x32_bf16 v[56:59], a[100:103], a[92:95], v[56:59]\n" \
"ds_read_b64_tr_b16 a[202:203], v210 offset:3072\n" \
"v_mfma_f32_16x16x32_bf16 v[16:19], a[104:107], a[88:91], v[16:19]\n" \
"ds_read_b64_tr_b16 a[204:205], v210 offset:14336\n" \
"v_mfma_f32_16x16x32_bf16 v[16:19], a[108:111], a[92:95], v[16:19]\n" \
"global_load_lds_dwordx4 v253, s[50:51] offset:1024\n" \
"v_mfma_f32_16x16x32_bf16 v[20:23], a[112:115], a[88:91], v[20:23]\n" \
"s_add_u32 s50, s50, 0x60000\n" \
"s_addc_u32 s51, s51, 0\n" \
"ds_read_b64_tr_b16 a[206:207], v210 offset:15360\n" \
"v_mfma_f32_16x16x32_bf16 v[20:23], a[116:119], a[92:95], v[20:23]\n" \
"s_waitcnt lgkmcnt(3)\n" \
"v_add_u32 v207, 0x1d000, v207\n" \
"v_add_u32 v208, 0x1d000, v208\n" \
"v_mfma_f32_16x16x32_bf16 v[28:31], a[112:115], a[192:195], v[28:31]\n" \
"v_add_u32 v205, 0x1d000, v205\n" \
"v_add_u32 v206, 0x1d000, v206\n" \
"v_mfma_f32_16x16x32_bf16 v[28:31], a[116:119], a[196:199], v[28:31]\n" \
"v_add_u32 v209, 0x1d000, v209\n" \
"v_add_u32 v210, 0x1d000, v210\n" \
"v_mfma_f32_16x16x32_bf16 v[24:27], a[104:107], a[192:195], v[24:27]\n" \
"v_mfma_f32_16x16x32_bf16 v[24:27], a[108:111], a[196:199], v[24:27]\n" \
"v_mfma_f32_16x16x32_bf16 v[64:67], a[96:99], a[192:195], v[64:67]\n" \
"s_waitcnt vmcnt(13) lgkmcnt(0)\n" \
"v_mfma_f32_16x16x32_bf16 v[64:67], a[100:103], a[196:199], v[64:67]\n" \
"s_barrier\n" \
"v_mfma_f32_16x16x32_bf16 v[72:75], a[64:67], a[208:211], v[72:75]\n" \
"ds_read_b64_tr_b16 a[96:97], v209 offset:2048\n" \
"v_mfma_f32_16x16x32_bf16 v[72:75], a[68:71], a[212:215], v[72:75]\n" \
"ds_read_b64_tr_b16 a[98:99], v209 offset:3072\n" \
"v_mfma_f32_16x16x32_bf16 v[80:83], a[72:75], a[208:211], v[80:83]\n" \
"s_add_u32 m0, s95, 0\n" \
"ds_read_b128 a[64:67], v207 offset:0\n" \
"ds_read_b64_tr_b16 a[100:101], v209 offset:14336\n" \
"v_mfma_f32_16x16x32_bf16 v[80:83], a[76:79], a[212:215], v[80:83]\n" \
"global_load_lds_dwordx4 v252, s[42:43] offset:0\n" \
"v_mfma_f32_16x16x32_bf16 v[84:87], a[72:75], a[216:219], v[84:87]\n" \
"ds_read_b128 a[68:71], v208 offset:0\n" \
"ds_read_b64_tr_b16 a[102:103], v209 offset:15360\n" \
"v_mfma_f32_16x16x32_bf16 v[84:87], a[76:79], a[220:223], v[84:87]\n" \
"ds_read_b64_tr_b16 a[104:105], v209 offset:0\n" \
"v_mfma_f32_16x16x32_bf16 v[44:47], a[200:203], a[72:75], v[44:47]\n" \
"ds_read_b64_tr_b16 a[106:107], v209 offset:1024\n" \
"v_mfma_f32_16x16x32_bf16 v[44:47], a[204:207], a[76:79], v[44:47]\n" \
"global_load_lds_dwordx4 v252, s[42:43] offset:2048\n" \
"v_mfma_f32_16x16x32_bf16 v[52:55], a[200:203], a[80:83], v[52:55]\n" \
"s_add_u32 s42, s42, 0x80\n" \
"s_addc_u32 s43, s43, 0\n" \
"ds_read_b128 a[72:75], v205 offset:0\n" \
"v_mfma_f32_16x16x32_bf16 v[52:55], a[204:207], a[84:87], v[52:55]\n" \
"ds_read_b128 a[76:79], v206 offset:0\n" \
"v_mfma_f32_16x16x32_bf16 v[92:95], a[80:83], a[216:219], v[92:95]\n" \
"ds_read_b64_tr_b16 a[108:109], v209 offset:12288\n" \
"v_mfma_f32_16x16x32_bf16 v[92:95], a[84:87], a[220:223], v[92:95]\n" \
"global_load_lds_dwordx4 v252, s[44:45] offset:1024\n" \
"v_mfma_f32_16x16x32_bf16 v[88:91], a[80:83], a[208:211], v[88:91]\n" \
"ds_read_b64_tr_b16 a[110:111], v209 offset:13312\n" \
"v_mfma_f32_16x16x32_bf16 v[88:91], a[84:87], a[212:215], v[88:91]\n" \
"ds_read_b64_tr_b16 a[112:113], v210 offset:0\n" \
"v_mfma_f32_16x16x32_bf16 v[96:99], a[88:91], a[208:211], v[96:99]\n" \
"ds_read_b128 a[80:83], v205 offset:4096\n" \
"v_mfma_f32_16x16x32_bf16 v[96:99], a[92:95], a[212:215], v[96:99]\n" \
"global_load_lds_dwordx4 v252, s[44:45] offset:3072\n" \
"v_mfma_f32_16x16x32_bf16 v[100:103], a[88:91], a[216:219], v[100:103]\n" \
"s_add_u32 s44, s44, 0x80\n" \
"s_addc_u32 s45, s45, 0\n" \
"ds_read_b128 a[84:87], v206 offset:4096\n" \
"v_mfma_f32_16x16x32_bf16 v[100:103], a[92:95], a[220:223], v[100:103]\n" \
"ds_read_b64_tr_b16 a[114:115], v210 offset:1024\n" \
"v_mfma_f32_16x16x32_bf16 v[60:63], a[200:203], a[88:91], v[60:63]\n" \
"s_add_u32 m0, s85, 16384\n" \
"ds_read_b64_tr_b16 a[116:117], v210 offset:12288\n" \
"v_mfma_f32_16x16x32_bf16 v[60:63], a[204:207], a[92:95], v[60:63]\n" \
"buffer_load_dwordx4 v246, s[12:15], s46 offen lds\n" \
"v_mfma_f32_16x16x32_bf16 v[68:71], a[200:203], a[192:195], v[68:71]\n" \
"ds_read_b128 a[88:91], v205 offset:8192\n" \
"s_add_u32 s46, s46, 0x80\n" \
"v_mfma_f32_16x16x32_bf16 v[68:71], a[204:207], a[196:199], v[68:71]\n" \
"ds_read_b128 a[92:95], v206 offset:8192\n" \
"v_mfma_f32_16x16x32_bf16 v[108:111], a[192:195], a[216:219], v[108:111]\n" \
"s_add_u32 m0, s84, 24576\n" \
"ds_read_b64_tr_b16 a[118:119], v210 offset:13312\n" \
"v_mfma_f32_16x16x32_bf16 v[108:111], a[196:199], a[220:223], v[108:111]\n" \
"global_load_lds_dwordx4 v253, s[48:49] offset:-4096\n" \
"v_mfma_f32_16x16x32_bf16 v[104:107], a[192:195], a[208:211], v[104:107]\n" \
"v_mfma_f32_16x16x32_bf16 v[104:107], a[196:199], a[212:215], v[104:107]\n" \
"s_waitcnt lgkmcnt(2)\n" \
"v_mfma_f32_16x16x32_bf16 v[32:35], a[96:99], a[64:67], v[32:35]\n" \
"s_mov_b32 s30, 65593856\n" \
"ds_read_b128 a[192:195], v205 offset:12288\n" \
"v_lshrrev_b32 v228, 1, v248\n" \
"v_mfma_f32_16x16x32_bf16 v[32:35], a[100:103], a[68:71], v[32:35]\n" \
"global_load_lds_dwordx4 v253, s[48:49] offset:-2048\n" \
"v_mfma_f32_16x16x32_bf16 v[40:43], a[96:99], a[72:75], v[40:43]\n" \
"ds_read_b128 a[196:199], v206 offset:12288\n" \
"v_mfma_f32_16x16x32_bf16 v[40:43], a[100:103], a[76:79], v[40:43]\n" \
"ds_read_b64_tr_b16 a[208:209], v209 offset:4096\n" \
"v_mfma_f32_16x16x32_bf16 v[0:3], a[104:107], a[72:75], v[0:3]\n" \
"ds_read_b64_tr_b16 a[210:211], v209 offset:5120\n" \
"v_mfma_f32_16x16x32_bf16 v[0:3], a[108:111], a[76:79], v[0:3]\n" \
"s_waitcnt lgkmcnt(3)\n" \
"global_load_lds_dwordx4 v253, s[48:49] offset:0\n" \
"v_mfma_f32_16x16x32_bf16 v[4:7], a[112:115], a[72:75], v[4:7]\n" \
"s_add_u32 s48, s48, 0x60000\n" \
"s_addc_u32 s49, s49, 0\n" \
"ds_read_b64_tr_b16 a[212:213], v209 offset:16384\n" \
"v_mfma_f32_16x16x32_bf16 v[4:7], a[116:119], a[76:79], v[4:7]\n" \
"ds_read_b64_tr_b16 a[214:215], v209 offset:17408\n" \
"v_mfma_f32_16x16x32_bf16 v[12:15], a[112:115], a[80:83], v[12:15]\n" \
"ds_read_b64_tr_b16 a[216:217], v210 offset:4096\n" \
"v_mfma_f32_16x16x32_bf16 v[12:15], a[116:119], a[84:87], v[12:15]\n" \
"global_load_lds_dwordx4 v253, s[50:51] offset:-3072\n" \
"v_mfma_f32_16x16x32_bf16 v[8:11], a[104:107], a[80:83], v[8:11]\n" \
"ds_read_b64_tr_b16 a[218:219], v210 offset:5120\n" \
"v_mfma_f32_16x16x32_bf16 v[8:11], a[108:111], a[84:87], v[8:11]\n" \
"ds_read_b64_tr_b16 a[220:221], v210 offset:16384\n" \
"v_mfma_f32_16x16x32_bf16 v[48:51], a[96:99], a[80:83], v[48:51]\n" \
"ds_read_b64_tr_b16 a[222:223], v210 offset:17408\n" \
"v_mfma_f32_16x16x32_bf16 v[48:51], a[100:103], a[84:87], v[48:51]\n" \
"global_load_lds_dwordx4 v253, s[50:51] offset:-1024\n" \
"v_mfma_f32_16x16x32_bf16 v[56:59], a[96:99], a[88:91], v[56:59]\n" \
"ds_read_b64_tr_b16 a[200:201], v210 offset:2048\n" \
"v_mfma_f32_16x16x32_bf16 v[56:59], a[100:103], a[92:95], v[56:59]\n" \
"ds_read_b64_tr_b16 a[202:203], v210 offset:3072\n" \
"v_mfma_f32_16x16x32_bf16 v[16:19], a[104:107], a[88:91], v[16:19]\n" \
"ds_read_b64_tr_b16 a[204:205], v210 offset:14336\n" \
"v_mfma_f32_16x16x32_bf16 v[16:19], a[108:111], a[92:95], v[16:19]\n" \
"global_load_lds_dwordx4 v253, s[50:51] offset:1024\n" \
"v_mfma_f32_16x16x32_bf16 v[20:23], a[112:115], a[88:91], v[20:23]\n" \
"s_add_u32 s50, s50, 0x60000\n" \
"s_addc_u32 s51, s51, 0\n" \
"ds_read_b64_tr_b16 a[206:207], v210 offset:15360\n" \
"v_mfma_f32_16x16x32_bf16 v[20:23], a[116:119], a[92:95], v[20:23]\n" \
"s_waitcnt lgkmcnt(3)\n" \
"v_add_u32 v207, 0xffff3000, v207\n" \
"v_add_u32 v208, 0xffff3000, v208\n" \
"v_mfma_f32_16x16x32_bf16 v[28:31], a[112:115], a[192:195], v[28:31]\n" \
"v_add_u32 v205, 0xffff3000, v205\n" \
"Lpg9869_%=:\n" \
"s_cbranch_execz Lpg12165_%=\n" \
"v_add_u32 v206, 0xffff3000, v206\n" \
"v_mfma_f32_16x16x32_bf16 v[28:31], a[116:119], a[196:199], v[28:31]\n" \
"global_load_dword v204, v203, s[22:23]\n" \
"v_mfma_f32_16x16x32_bf16 v[24:27], a[104:107], a[192:195], v[24:27]\n" \
"v_add_u32 v209, 0xffff3000, v209\n" \
"v_add_u32 v210, 0xffff3000, v210\n" \
"v_mfma_f32_16x16x32_bf16 v[24:27], a[108:111], a[196:199], v[24:27]\n" \
"v_mfma_f32_16x16x32_bf16 v[64:67], a[96:99], a[192:195], v[64:67]\n" \
"s_waitcnt vmcnt(12) lgkmcnt(0)\n" \
"v_mfma_f32_16x16x32_bf16 v[64:67], a[100:103], a[196:199], v[64:67]\n" \
"s_barrier\n" \
"v_mfma_f32_16x16x32_bf16 v[72:75], a[64:67], a[208:211], v[72:75]\n" \
"ds_read_b64_tr_b16 a[96:97], v209 offset:2048\n" \
"v_mfma_f32_16x16x32_bf16 v[72:75], a[68:71], a[212:215], v[72:75]\n" \
"ds_read_b64_tr_b16 a[98:99], v209 offset:3072\n" \
"v_mfma_f32_16x16x32_bf16 v[80:83], a[72:75], a[208:211], v[80:83]\n" \
"s_add_u32 m0, s95, 118784\n" \
"ds_read_b128 a[64:67], v207 offset:0\n" \
"ds_read_b64_tr_b16 a[100:101], v209 offset:14336\n" \
"v_mfma_f32_16x16x32_bf16 v[80:83], a[76:79], a[212:215], v[80:83]\n" \
"global_load_lds_dwordx4 v252, s[42:43] offset:0\n" \
"v_mfma_f32_16x16x32_bf16 v[84:87], a[72:75], a[216:219], v[84:87]\n" \
"ds_read_b128 a[68:71], v208 offset:0\n" \
"ds_read_b64_tr_b16 a[102:103], v209 offset:15360\n" \
"v_mfma_f32_16x16x32_bf16 v[84:87], a[76:79], a[220:223], v[84:87]\n" \
"ds_read_b64_tr_b16 a[104:105], v209 offset:0\n" \
"v_mfma_f32_16x16x32_bf16 v[44:47], a[200:203], a[72:75], v[44:47]\n" \
"ds_read_b64_tr_b16 a[106:107], v209 offset:1024\n" \
"v_mfma_f32_16x16x32_bf16 v[44:47], a[204:207], a[76:79], v[44:47]\n" \
"global_load_lds_dwordx4 v252, s[42:43] offset:2048\n" \
"v_mfma_f32_16x16x32_bf16 v[52:55], a[200:203], a[80:83], v[52:55]\n" \
"s_add_u32 s42, s42, 0x80\n" \
"s_addc_u32 s43, s43, 0\n" \
"ds_read_b128 a[72:75], v205 offset:0\n" \
"v_mfma_f32_16x16x32_bf16 v[52:55], a[204:207], a[84:87], v[52:55]\n" \
"ds_read_b128 a[76:79], v206 offset:0\n" \
"v_mfma_f32_16x16x32_bf16 v[92:95], a[80:83], a[216:219], v[92:95]\n" \
"ds_read_b64_tr_b16 a[108:109], v209 offset:12288\n" \
"v_mfma_f32_16x16x32_bf16 v[92:95], a[84:87], a[220:223], v[92:95]\n" \
"global_load_lds_dwordx4 v252, s[44:45] offset:1024\n" \
"v_mfma_f32_16x16x32_bf16 v[88:91], a[80:83], a[208:211], v[88:91]\n" \
"ds_read_b64_tr_b16 a[110:111], v209 offset:13312\n" \
"v_mfma_f32_16x16x32_bf16 v[88:91], a[84:87], a[212:215], v[88:91]\n" \
"ds_read_b64_tr_b16 a[112:113], v210 offset:0\n" \
"v_mfma_f32_16x16x32_bf16 v[96:99], a[88:91], a[208:211], v[96:99]\n" \
"ds_read_b128 a[80:83], v205 offset:4096\n" \
"v_mfma_f32_16x16x32_bf16 v[96:99], a[92:95], a[212:215], v[96:99]\n" \
"global_load_lds_dwordx4 v252, s[44:45] offset:3072\n" \
"v_mfma_f32_16x16x32_bf16 v[100:103], a[88:91], a[216:219], v[100:103]\n" \
"s_add_u32 s44, s44, 0x80\n" \
"s_addc_u32 s45, s45, 0\n" \
"ds_read_b128 a[84:87], v206 offset:4096\n" \
"v_mfma_f32_16x16x32_bf16 v[100:103], a[92:95], a[220:223], v[100:103]\n" \
"ds_read_b64_tr_b16 a[114:115], v210 offset:1024\n" \
"v_mfma_f32_16x16x32_bf16 v[60:63], a[200:203], a[88:91], v[60:63]\n" \
"s_add_u32 m0, s85, 135168\n" \
"ds_read_b64_tr_b16 a[116:117], v210 offset:12288\n" \
"v_mfma_f32_16x16x32_bf16 v[60:63], a[204:207], a[92:95], v[60:63]\n" \
"buffer_load_dwordx4 v246, s[12:15], s46 offen lds\n" \
"v_mfma_f32_16x16x32_bf16 v[68:71], a[200:203], a[192:195], v[68:71]\n" \
"ds_read_b128 a[88:91], v205 offset:8192\n" \
"s_add_u32 s46, s46, 0x80\n" \
"v_mfma_f32_16x16x32_bf16 v[68:71], a[204:207], a[196:199], v[68:71]\n" \
"ds_read_b128 a[92:95], v206 offset:8192\n" \
"v_mfma_f32_16x16x32_bf16 v[108:111], a[192:195], a[216:219], v[108:111]\n" \
"s_add_u32 m0, s84, 143360\n" \
"ds_read_b64_tr_b16 a[118:119], v210 offset:13312\n" \
"v_mfma_f32_16x16x32_bf16 v[108:111], a[196:199], a[220:223], v[108:111]\n" \
"global_load_lds_dwordx4 v253, s[48:49] offset:-4096\n" \
"v_mfma_f32_16x16x32_bf16 v[104:107], a[192:195], a[208:211], v[104:107]\n" \
"v_mfma_f32_16x16x32_bf16 v[104:107], a[196:199], a[212:215], v[104:107]\n" \
"s_waitcnt lgkmcnt(2)\n" \
"v_mfma_f32_16x16x32_bf16 v[32:35], a[96:99], a[64:67], v[32:35]\n" \
"ds_read_b128 a[192:195], v205 offset:12288\n" \
"v_mfma_f32_16x16x32_bf16 v[32:35], a[100:103], a[68:71], v[32:35]\n" \
"global_load_lds_dwordx4 v253, s[48:49] offset:-2048\n" \
"v_mfma_f32_16x16x32_bf16 v[40:43], a[96:99], a[72:75], v[40:43]\n" \
"ds_read_b128 a[196:199], v206 offset:12288\n" \
"v_mfma_f32_16x16x32_bf16 v[40:43], a[100:103], a[76:79], v[40:43]\n" \
"ds_read_b64_tr_b16 a[208:209], v209 offset:4096\n" \
"v_mfma_f32_16x16x32_bf16 v[0:3], a[104:107], a[72:75], v[0:3]\n" \
"ds_read_b64_tr_b16 a[210:211], v209 offset:5120\n" \
"v_mfma_f32_16x16x32_bf16 v[0:3], a[108:111], a[76:79], v[0:3]\n" \
"s_waitcnt lgkmcnt(3)\n" \
"global_load_lds_dwordx4 v253, s[48:49] offset:0\n" \
"v_mfma_f32_16x16x32_bf16 v[4:7], a[112:115], a[72:75], v[4:7]\n" \
"s_add_u32 s48, s48, 0x60000\n" \
"s_addc_u32 s49, s49, 0\n" \
"ds_read_b64_tr_b16 a[212:213], v209 offset:16384\n" \
"v_mfma_f32_16x16x32_bf16 v[4:7], a[116:119], a[76:79], v[4:7]\n" \
"ds_read_b64_tr_b16 a[214:215], v209 offset:17408\n" \
"v_mfma_f32_16x16x32_bf16 v[12:15], a[112:115], a[80:83], v[12:15]\n" \
"ds_read_b64_tr_b16 a[216:217], v210 offset:4096\n" \
"v_mfma_f32_16x16x32_bf16 v[12:15], a[116:119], a[84:87], v[12:15]\n" \
"global_load_lds_dwordx4 v253, s[50:51] offset:-3072\n" \
"v_mfma_f32_16x16x32_bf16 v[8:11], a[104:107], a[80:83], v[8:11]\n" \
"ds_read_b64_tr_b16 a[218:219], v210 offset:5120\n" \
"v_mfma_f32_16x16x32_bf16 v[8:11], a[108:111], a[84:87], v[8:11]\n" \
"ds_read_b64_tr_b16 a[220:221], v210 offset:16384\n" \
"v_mfma_f32_16x16x32_bf16 v[48:51], a[96:99], a[80:83], v[48:51]\n" \
"ds_read_b64_tr_b16 a[222:223], v210 offset:17408\n" \
"v_mfma_f32_16x16x32_bf16 v[48:51], a[100:103], a[84:87], v[48:51]\n" \
"global_load_lds_dwordx4 v253, s[50:51] offset:-1024\n" \
"v_mfma_f32_16x16x32_bf16 v[56:59], a[96:99], a[88:91], v[56:59]\n" \
"ds_read_b64_tr_b16 a[200:201], v210 offset:2048\n" \
"v_mfma_f32_16x16x32_bf16 v[56:59], a[100:103], a[92:95], v[56:59]\n" \
"ds_read_b64_tr_b16 a[202:203], v210 offset:3072\n" \
"v_mfma_f32_16x16x32_bf16 v[16:19], a[104:107], a[88:91], v[16:19]\n" \
"ds_read_b64_tr_b16 a[204:205], v210 offset:14336\n" \
"v_mfma_f32_16x16x32_bf16 v[16:19], a[108:111], a[92:95], v[16:19]\n" \
"global_load_lds_dwordx4 v253, s[50:51] offset:1024\n" \
"v_mfma_f32_16x16x32_bf16 v[20:23], a[112:115], a[88:91], v[20:23]\n" \
"s_add_u32 s50, s50, 0x60000\n" \
"s_addc_u32 s51, s51, 0\n" \
"ds_read_b64_tr_b16 a[206:207], v210 offset:15360\n" \
"v_mfma_f32_16x16x32_bf16 v[20:23], a[116:119], a[92:95], v[20:23]\n" \
"s_waitcnt lgkmcnt(3)\n" \
"v_add_u32 v207, 0xffff0000, v207\n" \
"v_add_u32 v208, 0xffff0000, v208\n" \
"v_mfma_f32_16x16x32_bf16 v[28:31], a[112:115], a[192:195], v[28:31]\n" \
"v_add_u32 v205, 0xffff0000, v205\n" \
"v_add_u32 v206, 0xffff0000, v206\n" \
"v_mfma_f32_16x16x32_bf16 v[28:31], a[116:119], a[196:199], v[28:31]\n" \
"buffer_load_dwordx2 v[226:227], v228, s[56:59], s30 offen sc1\n" \
"v_mfma_f32_16x16x32_bf16 v[24:27], a[104:107], a[192:195], v[24:27]\n" \
"v_add_u32 v209, 0xffff0000, v209\n" \
"v_add_u32 v210, 0xffff0000, v210\n" \
"v_mfma_f32_16x16x32_bf16 v[24:27], a[108:111], a[196:199], v[24:27]\n" \
"v_mfma_f32_16x16x32_bf16 v[64:67], a[96:99], a[192:195], v[64:67]\n" \
"s_waitcnt vmcnt(13) lgkmcnt(0)\n" \
"v_mfma_f32_16x16x32_bf16 v[64:67], a[100:103], a[196:199], v[64:67]\n" \
"s_barrier\n" \
"v_mfma_f32_16x16x32_bf16 v[72:75], a[64:67], a[208:211], v[72:75]\n" \
"ds_read_b64_tr_b16 a[96:97], v209 offset:2048\n" \
"v_mfma_f32_16x16x32_bf16 v[72:75], a[68:71], a[212:215], v[72:75]\n" \
"ds_read_b64_tr_b16 a[98:99], v209 offset:3072\n" \
"v_mfma_f32_16x16x32_bf16 v[80:83], a[72:75], a[208:211], v[80:83]\n" \
"s_add_u32 m0, s95, 65536\n" \
"ds_read_b128 a[64:67], v207 offset:0\n" \
"ds_read_b64_tr_b16 a[100:101], v209 offset:14336\n" \
"v_mfma_f32_16x16x32_bf16 v[80:83], a[76:79], a[212:215], v[80:83]\n" \
"global_load_lds_dwordx4 v252, s[42:43] offset:0\n" \
"v_mfma_f32_16x16x32_bf16 v[84:87], a[72:75], a[216:219], v[84:87]\n" \
"ds_read_b128 a[68:71], v208 offset:0\n" \
"ds_read_b64_tr_b16 a[102:103], v209 offset:15360\n" \
"v_mfma_f32_16x16x32_bf16 v[84:87], a[76:79], a[220:223], v[84:87]\n" \
"ds_read_b64_tr_b16 a[104:105], v209 offset:0\n" \
"v_mfma_f32_16x16x32_bf16 v[44:47], a[200:203], a[72:75], v[44:47]\n" \
"ds_read_b64_tr_b16 a[106:107], v209 offset:1024\n" \
"v_mfma_f32_16x16x32_bf16 v[44:47], a[204:207], a[76:79], v[44:47]\n" \
"global_load_lds_dwordx4 v252, s[42:43] offset:2048\n" \
"v_mfma_f32_16x16x32_bf16 v[52:55], a[200:203], a[80:83], v[52:55]\n" \
"s_add_u32 s42, s42, 0x80\n" \
"s_addc_u32 s43, s43, 0\n" \
"ds_read_b128 a[72:75], v205 offset:0\n" \
"v_mfma_f32_16x16x32_bf16 v[52:55], a[204:207], a[84:87], v[52:55]\n" \
"ds_read_b128 a[76:79], v206 offset:0\n" \
"v_mfma_f32_16x16x32_bf16 v[92:95], a[80:83], a[216:219], v[92:95]\n" \
"ds_read_b64_tr_b16 a[108:109], v209 offset:12288\n" \
"v_mfma_f32_16x16x32_bf16 v[92:95], a[84:87], a[220:223], v[92:95]\n" \
"global_load_lds_dwordx4 v252, s[44:45] offset:1024\n" \
"v_mfma_f32_16x16x32_bf16 v[88:91], a[80:83], a[208:211], v[88:91]\n" \
"ds_read_b64_tr_b16 a[110:111], v209 offset:13312\n" \
"v_mfma_f32_16x16x32_bf16 v[88:91], a[84:87], a[212:215], v[88:91]\n" \
"ds_read_b64_tr_b16 a[112:113], v210 offset:0\n" \
"v_mfma_f32_16x16x32_bf16 v[96:99], a[88:91], a[208:211], v[96:99]\n" \
"ds_read_b128 a[80:83], v205 offset:4096\n" \
"v_mfma_f32_16x16x32_bf16 v[96:99], a[92:95], a[212:215], v[96:99]\n" \
"global_load_lds_dwordx4 v252, s[44:45] offset:3072\n" \
"v_mfma_f32_16x16x32_bf16 v[100:103], a[88:91], a[216:219], v[100:103]\n" \
"s_add_u32 s44, s44, 0x80\n" \
"s_addc_u32 s45, s45, 0\n" \
"ds_read_b128 a[84:87], v206 offset:4096\n" \
"v_mfma_f32_16x16x32_bf16 v[100:103], a[92:95], a[220:223], v[100:103]\n" \
"ds_read_b64_tr_b16 a[114:115], v210 offset:1024\n" \
"v_mfma_f32_16x16x32_bf16 v[60:63], a[200:203], a[88:91], v[60:63]\n" \
"s_add_u32 m0, s85, 81920\n" \
"ds_read_b64_tr_b16 a[116:117], v210 offset:12288\n" \
"v_mfma_f32_16x16x32_bf16 v[60:63], a[204:207], a[92:95], v[60:63]\n" \
"buffer_load_dwordx4 v246, s[12:15], s46 offen lds\n" \
"v_mfma_f32_16x16x32_bf16 v[68:71], a[200:203], a[192:195], v[68:71]\n" \
"ds_read_b128 a[88:91], v205 offset:8192\n" \
"s_add_u32 s46, s46, 0x80\n" \
"v_mfma_f32_16x16x32_bf16 v[68:71], a[204:207], a[196:199], v[68:71]\n" \
"ds_read_b128 a[92:95], v206 offset:8192\n" \
"v_mfma_f32_16x16x32_bf16 v[108:111], a[192:195], a[216:219], v[108:111]\n" \
"s_add_u32 m0, s84, 90112\n" \
"ds_read_b64_tr_b16 a[118:119], v210 offset:13312\n" \
"v_mfma_f32_16x16x32_bf16 v[108:111], a[196:199], a[220:223], v[108:111]\n" \
"global_load_lds_dwordx4 v253, s[48:49] offset:-4096\n" \
"v_mfma_f32_16x16x32_bf16 v[104:107], a[192:195], a[208:211], v[104:107]\n" \
"v_mfma_f32_16x16x32_bf16 v[104:107], a[196:199], a[212:215], v[104:107]\n" \
"s_waitcnt lgkmcnt(2)\n" \
"v_mfma_f32_16x16x32_bf16 v[32:35], a[96:99], a[64:67], v[32:35]\n" \
"ds_read_b128 a[192:195], v205 offset:12288\n" \
"v_mfma_f32_16x16x32_bf16 v[32:35], a[100:103], a[68:71], v[32:35]\n" \
"global_load_lds_dwordx4 v253, s[48:49] offset:-2048\n" \
"v_mfma_f32_16x16x32_bf16 v[40:43], a[96:99], a[72:75], v[40:43]\n" \
"ds_read_b128 a[196:199], v206 offset:12288\n" \
"v_mfma_f32_16x16x32_bf16 v[40:43], a[100:103], a[76:79], v[40:43]\n" \
"ds_read_b64_tr_b16 a[208:209], v209 offset:4096\n" \
"v_mfma_f32_16x16x32_bf16 v[0:3], a[104:107], a[72:75], v[0:3]\n" \
"ds_read_b64_tr_b16 a[210:211], v209 offset:5120\n" \
"v_mfma_f32_16x16x32_bf16 v[0:3], a[108:111], a[76:79], v[0:3]\n" \
"s_waitcnt lgkmcnt(3)\n" \
"global_load_lds_dwordx4 v253, s[48:49] offset:0\n" \
"v_mfma_f32_16x16x32_bf16 v[4:7], a[112:115], a[72:75], v[4:7]\n" \
"s_add_u32 s48, s48, 0x60000\n" \
"s_addc_u32 s49, s49, 0\n" \
"ds_read_b64_tr_b16 a[212:213], v209 offset:16384\n" \
"v_mfma_f32_16x16x32_bf16 v[4:7], a[116:119], a[76:79], v[4:7]\n" \
"ds_read_b64_tr_b16 a[214:215], v209 offset:17408\n" \
"v_mfma_f32_16x16x32_bf16 v[12:15], a[112:115], a[80:83], v[12:15]\n" \
"ds_read_b64_tr_b16 a[216:217], v210 offset:4096\n" \
"v_mfma_f32_16x16x32_bf16 v[12:15], a[116:119], a[84:87], v[12:15]\n" \
"global_load_lds_dwordx4 v253, s[50:51] offset:-3072\n" \
"v_mfma_f32_16x16x32_bf16 v[8:11], a[104:107], a[80:83], v[8:11]\n" \
"ds_read_b64_tr_b16 a[218:219], v210 offset:5120\n" \
"v_mfma_f32_16x16x32_bf16 v[8:11], a[108:111], a[84:87], v[8:11]\n" \
"ds_read_b64_tr_b16 a[220:221], v210 offset:16384\n" \
"v_mfma_f32_16x16x32_bf16 v[48:51], a[96:99], a[80:83], v[48:51]\n" \
"ds_read_b64_tr_b16 a[222:223], v210 offset:17408\n" \
"v_mfma_f32_16x16x32_bf16 v[48:51], a[100:103], a[84:87], v[48:51]\n" \
"global_load_lds_dwordx4 v253, s[50:51] offset:-1024\n" \
"v_mfma_f32_16x16x32_bf16 v[56:59], a[96:99], a[88:91], v[56:59]\n" \
"ds_read_b64_tr_b16 a[200:201], v210 offset:2048\n" \
"v_mfma_f32_16x16x32_bf16 v[56:59], a[100:103], a[92:95], v[56:59]\n" \
"ds_read_b64_tr_b16 a[202:203], v210 offset:3072\n" \
"v_mfma_f32_16x16x32_bf16 v[16:19], a[104:107], a[88:91], v[16:19]\n" \
"ds_read_b64_tr_b16 a[204:205], v210 offset:14336\n" \
"v_mfma_f32_16x16x32_bf16 v[16:19], a[108:111], a[92:95], v[16:19]\n" \
"global_load_lds_dwordx4 v253, s[50:51] offset:1024\n" \
"v_mfma_f32_16x16x32_bf16 v[20:23], a[112:115], a[88:91], v[20:23]\n" \
"s_add_u32 s50, s50, 0x60000\n" \
"s_addc_u32 s51, s51, 0\n" \
"ds_read_b64_tr_b16 a[206:207], v210 offset:15360\n" \
"v_mfma_f32_16x16x32_bf16 v[20:23], a[116:119], a[92:95], v[20:23]\n" \
"s_waitcnt lgkmcnt(3)\n" \
"v_add_u32 v207, 0x1d000, v207\n" \
"v_add_u32 v208, 0x1d000, v208\n" \
"v_mfma_f32_16x16x32_bf16 v[28:31], a[112:115], a[192:195], v[28:31]\n" \
"v_add_u32 v205, 0x1d000, v205\n" \
"v_add_u32 v206, 0x1d000, v206\n" \
"v_mfma_f32_16x16x32_bf16 v[28:31], a[116:119], a[196:199], v[28:31]\n" \
"v_add_u32 v209, 0x1d000, v209\n" \
"v_add_u32 v210, 0x1d000, v210\n" \
"v_mfma_f32_16x16x32_bf16 v[24:27], a[104:107], a[192:195], v[24:27]\n" \
"v_mfma_f32_16x16x32_bf16 v[24:27], a[108:111], a[196:199], v[24:27]\n" \
"v_mfma_f32_16x16x32_bf16 v[64:67], a[96:99], a[192:195], v[64:67]\n" \
"s_waitcnt vmcnt(12) lgkmcnt(0)\n" \
"v_mfma_f32_16x16x32_bf16 v[64:67], a[100:103], a[196:199], v[64:67]\n" \
"s_barrier\n" \
"v_mfma_f32_16x16x32_bf16 v[72:75], a[64:67], a[208:211], v[72:75]\n" \
"ds_read_b64_tr_b16 a[96:97], v209 offset:2048\n" \
"s_waitcnt vmcnt(11)\n" \
"v_mfma_f32_16x16x32_bf16 v[72:75], a[68:71], a[212:215], v[72:75]\n" \
"ds_read_b64_tr_b16 a[98:99], v209 offset:3072\n" \
"v_mfma_f32_16x16x32_bf16 v[80:83], a[72:75], a[208:211], v[80:83]\n" \
"s_add_u32 m0, s95, 0\n" \
"ds_read_b128 a[64:67], v207 offset:0\n" \
"ds_read_b64_tr_b16 a[100:101], v209 offset:14336\n" \
"v_mfma_f32_16x16x32_bf16 v[80:83], a[76:79], a[212:215], v[80:83]\n" \
"global_load_lds_dwordx4 v252, s[42:43] offset:0\n" \
"v_mfma_f32_16x16x32_bf16 v[84:87], a[72:75], a[216:219], v[84:87]\n" \
"ds_read_b128 a[68:71], v208 offset:0\n" \
"ds_read_b64_tr_b16 a[102:103], v209 offset:15360\n" \
"v_mfma_f32_16x16x32_bf16 v[84:87], a[76:79], a[220:223], v[84:87]\n" \
"ds_read_b64_tr_b16 a[104:105], v209 offset:0\n" \
"v_mfma_f32_16x16x32_bf16 v[44:47], a[200:203], a[72:75], v[44:47]\n" \
"ds_read_b64_tr_b16 a[106:107], v209 offset:1024\n" \
"v_mfma_f32_16x16x32_bf16 v[44:47], a[204:207], a[76:79], v[44:47]\n" \
"global_load_lds_dwordx4 v252, s[42:43] offset:2048\n" \
"v_mfma_f32_16x16x32_bf16 v[52:55], a[200:203], a[80:83], v[52:55]\n" \
"s_add_u32 s42, s42, 0x100\n" \
"s_addc_u32 s43, s43, 0\n" \
"ds_read_b128 a[72:75], v205 offset:0\n" \
"v_mfma_f32_16x16x32_bf16 v[52:55], a[204:207], a[84:87], v[52:55]\n" \
"ds_read_b128 a[76:79], v206 offset:0\n" \
"v_mfma_f32_16x16x32_bf16 v[92:95], a[80:83], a[216:219], v[92:95]\n" \
"ds_read_b64_tr_b16 a[108:109], v209 offset:12288\n" \
"v_mfma_f32_16x16x32_bf16 v[92:95], a[84:87], a[220:223], v[92:95]\n" \
"global_load_lds_dwordx4 v252, s[44:45] offset:1024\n" \
"v_mfma_f32_16x16x32_bf16 v[88:91], a[80:83], a[208:211], v[88:91]\n" \
"ds_read_b64_tr_b16 a[110:111], v209 offset:13312\n" \
"v_mfma_f32_16x16x32_bf16 v[88:91], a[84:87], a[212:215], v[88:91]\n" \
"ds_read_b64_tr_b16 a[112:113], v210 offset:0\n" \
"v_mfma_f32_16x16x32_bf16 v[96:99], a[88:91], a[208:211], v[96:99]\n" \
"ds_read_b128 a[80:83], v205 offset:4096\n" \
"v_mfma_f32_16x16x32_bf16 v[96:99], a[92:95], a[212:215], v[96:99]\n" \
"global_load_lds_dwordx4 v252, s[44:45] offset:3072\n" \
"v_mfma_f32_16x16x32_bf16 v[100:103], a[88:91], a[216:219], v[100:103]\n" \
"s_add_u32 s44, s44, 0x100\n" \
"s_addc_u32 s45, s45, 0\n" \
"ds_read_b128 a[84:87], v206 offset:4096\n" \
"v_mfma_f32_16x16x32_bf16 v[100:103], a[92:95], a[220:223], v[100:103]\n" \
"ds_read_b64_tr_b16 a[114:115], v210 offset:1024\n" \
"v_mfma_f32_16x16x32_bf16 v[60:63], a[200:203], a[88:91], v[60:63]\n" \
"s_add_u32 m0, s85, 16384\n" \
"ds_read_b64_tr_b16 a[116:117], v210 offset:12288\n" \
"v_mfma_f32_16x16x32_bf16 v[60:63], a[204:207], a[92:95], v[60:63]\n" \
"buffer_load_dwordx4 v246, s[12:15], s46 offen lds\n" \
"v_mfma_f32_16x16x32_bf16 v[68:71], a[200:203], a[192:195], v[68:71]\n" \
"ds_read_b128 a[88:91], v205 offset:8192\n" \
"s_add_u32 s46, s46, 0x100\n" \
"v_mfma_f32_16x16x32_bf16 v[68:71], a[204:207], a[196:199], v[68:71]\n" \
"ds_read_b128 a[92:95], v206 offset:8192\n" \
"v_mfma_f32_16x16x32_bf16 v[108:111], a[192:195], a[216:219], v[108:111]\n" \
"s_add_u32 m0, s84, 24576\n" \
"ds_read_b64_tr_b16 a[118:119], v210 offset:13312\n" \
"v_mfma_f32_16x16x32_bf16 v[108:111], a[196:199], a[220:223], v[108:111]\n" \
"global_load_lds_dwordx4 v253, s[48:49] offset:-4096\n" \
"v_mfma_f32_16x16x32_bf16 v[104:107], a[192:195], a[208:211], v[104:107]\n" \
"v_xor_b32 v226, v226, v227\n" \
"v_cmp_ne_u32 vcc, s60, v226\n" \
"v_mfma_f32_16x16x32_bf16 v[104:107], a[196:199], a[212:215], v[104:107]\n" \
"s_waitcnt lgkmcnt(2)\n" \
"v_mfma_f32_16x16x32_bf16 v[32:35], a[96:99], a[64:67], v[32:35]\n" \
"ds_read_b128 a[192:195], v205 offset:12288\n" \
"v_mfma_f32_16x16x32_bf16 v[32:35], a[100:103], a[68:71], v[32:35]\n" \
"global_load_lds_dwordx4 v253, s[48:49] offset:-2048\n" \
"v_mfma_f32_16x16x32_bf16 v[40:43], a[96:99], a[72:75], v[40:43]\n" \
"ds_read_b128 a[196:199], v206 offset:12288\n" \
"v_mfma_f32_16x16x32_bf16 v[40:43], a[100:103], a[76:79], v[40:43]\n" \
"ds_read_b64_tr_b16 a[208:209], v209 offset:4096\n" \
"v_mfma_f32_16x16x32_bf16 v[0:3], a[104:107], a[72:75], v[0:3]\n" \
"ds_read_b64_tr_b16 a[210:211], v209 offset:5120\n" \
"v_mfma_f32_16x16x32_bf16 v[0:3], a[108:111], a[76:79], v[0:3]\n" \
"s_waitcnt lgkmcnt(3)\n" \
"global_load_lds_dwordx4 v253, s[48:49] offset:0\n" \
"v_mfma_f32_16x16x32_bf16 v[4:7], a[112:115], a[72:75], v[4:7]\n" \
"s_add_u32 s48, s48, 0xc0000\n" \
"s_addc_u32 s49, s49, 0\n" \
"ds_read_b64_tr_b16 a[212:213], v209 offset:16384\n" \
"v_mfma_f32_16x16x32_bf16 v[4:7], a[116:119], a[76:79], v[4:7]\n" \
"ds_read_b64_tr_b16 a[214:215], v209 offset:17408\n" \
"v_mfma_f32_16x16x32_bf16 v[12:15], a[112:115], a[80:83], v[12:15]\n" \
"ds_read_b64_tr_b16 a[216:217], v210 offset:4096\n" \
"v_mfma_f32_16x16x32_bf16 v[12:15], a[116:119], a[84:87], v[12:15]\n" \
"global_load_lds_dwordx4 v253, s[50:51] offset:-3072\n" \
"v_mfma_f32_16x16x32_bf16 v[8:11], a[104:107], a[80:83], v[8:11]\n" \
"ds_read_b64_tr_b16 a[218:219], v210 offset:5120\n" \
"v_mfma_f32_16x16x32_bf16 v[8:11], a[108:111], a[84:87], v[8:11]\n" \
"ds_read_b64_tr_b16 a[220:221], v210 offset:16384\n" \
"v_mfma_f32_16x16x32_bf16 v[48:51], a[96:99], a[80:83], v[48:51]\n" \
"ds_read_b64_tr_b16 a[222:223], v210 offset:17408\n" \
"v_mfma_f32_16x16x32_bf16 v[48:51], a[100:103], a[84:87], v[48:51]\n" \
"global_load_lds_dwordx4 v253, s[50:51] offset:-1024\n" \
"v_mfma_f32_16x16x32_bf16 v[56:59], a[96:99], a[88:91], v[56:59]\n" \
"ds_read_b64_tr_b16 a[200:201], v210 offset:2048\n" \
"v_mfma_f32_16x16x32_bf16 v[56:59], a[100:103], a[92:95], v[56:59]\n" \
"ds_read_b64_tr_b16 a[202:203], v210 offset:3072\n" \
"v_mfma_f32_16x16x32_bf16 v[16:19], a[104:107], a[88:91], v[16:19]\n" \
"ds_read_b64_tr_b16 a[204:205], v210 offset:14336\n" \
"v_mfma_f32_16x16x32_bf16 v[16:19], a[108:111], a[92:95], v[16:19]\n" \
"global_load_lds_dwordx4 v253, s[50:51] offset:1024\n" \
"v_mfma_f32_16x16x32_bf16 v[20:23], a[112:115], a[88:91], v[20:23]\n" \
"s_add_u32 s50, s50, 0xc0000\n" \
"s_addc_u32 s51, s51, 0\n" \
"ds_read_b64_tr_b16 a[206:207], v210 offset:15360\n" \
"v_mfma_f32_16x16x32_bf16 v[20:23], a[116:119], a[92:95], v[20:23]\n" \
"s_waitcnt lgkmcnt(3)\n" \
"v_add_u32 v207, 0xffff3000, v207\n" \
"v_add_u32 v208, 0xffff3000, v208\n" \
"v_mfma_f32_16x16x32_bf16 v[28:31], a[112:115], a[192:195], v[28:31]\n" \
"v_add_u32 v205, 0xffff3000, v205\n" \
"v_add_u32 v206, 0xffff3000, v206\n" \
"v_mfma_f32_16x16x32_bf16 v[28:31], a[116:119], a[196:199], v[28:31]\n" \
"v_add_u32 v209, 0xffff3000, v209\n" \
"v_add_u32 v210, 0xffff3000, v210\n" \
"v_mfma_f32_16x16x32_bf16 v[24:27], a[104:107], a[192:195], v[24:27]\n" \
"v_mfma_f32_16x16x32_bf16 v[24:27], a[108:111], a[196:199], v[24:27]\n" \
"s_and_b32 vcc_hi, vcc_lo, -1\n" \
"s_cbranch_scc1 Lpl3_%=\n" \
"Lpr3_%=:\n" \
"v_mfma_f32_16x16x32_bf16 v[64:67], a[96:99], a[192:195], v[64:67]\n" \
"s_waitcnt vmcnt(11) lgkmcnt(0)\n" \
"v_readfirstlane_b32 s71, v227\n" \
"s_and_b32 s70, %1, 7\n" \
"v_mfma_f32_16x16x32_bf16 v[64:67], a[100:103], a[196:199], v[64:67]\n" \
"s_barrier\n" \
"v_mfma_f32_16x16x32_bf16 v[72:75], a[64:67], a[208:211], v[72:75]\n" \
"s_lshl_b32 s78, s70, 2\n" \
"ds_read_b64_tr_b16 a[96:97], v209 offset:2048\n" \
"v_mfma_f32_16x16x32_bf16 v[72:75], a[68:71], a[212:215], v[72:75]\n" \
"ds_read_b64_tr_b16 a[98:99], v209 offset:3072\n" \
"v_subrev_u32 v227, s71, v227\n" \
"v_mfma_f32_16x16x32_bf16 v[80:83], a[72:75], a[208:211], v[80:83]\n" \
"s_add_u32 m0, s95, 118784\n" \
"ds_read_b128 a[64:67], v207 offset:0\n" \
"ds_read_b64_tr_b16 a[100:101], v209 offset:14336\n" \
"v_mfma_f32_16x16x32_bf16 v[80:83], a[76:79], a[212:215], v[80:83]\n" \
"global_load_lds_dwordx4 v252, s[42:43] offset:0\n" \
"v_add_u32_dpp v227, v227, v227 quad_perm:[1,0,3,2] row_mask:0xf bank_mask:0xf\n" \
"v_mfma_f32_16x16x32_bf16 v[84:87], a[72:75], a[216:219], v[84:87]\n" \
"ds_read_b128 a[68:71], v208 offset:0\n" \
"ds_read_b64_tr_b16 a[102:103], v209 offset:15360\n" \
"v_mfma_f32_16x16x32_bf16 v[84:87], a[76:79], a[220:223], v[84:87]\n" \
"ds_read_b64_tr_b16 a[104:105], v209 offset:0\n" \
"v_add_u32_dpp v227, v227, v227 quad_perm:[2,3,0,1] row_mask:0xf bank_mask:0xf\n" \
"v_mfma_f32_16x16x32_bf16 v[44:47], a[200:203], a[72:75], v[44:47]\n" \
"ds_read_b64_tr_b16 a[106:107], v209 offset:1024\n" \
"v_readlane_b32 s71, v227, s78\n" \
"v_mfma_f32_16x16x32_bf16 v[44:47], a[204:207], a[76:79], v[44:47]\n" \
"global_load_lds_dwordx4 v252, s[42:43] offset:2048\n" \
"v_lshrrev_b32 v226, 6, v248\n" \
"v_mfma_f32_16x16x32_bf16 v[52:55], a[200:203], a[80:83], v[52:55]\n" \
"s_add_u32 s42, s42, 0x80\n" \
"s_addc_u32 s43, s43, 0\n" \
"ds_read_b128 a[72:75], v205 offset:0\n" \
"v_mfma_f32_16x16x32_bf16 v[52:55], a[204:207], a[84:87], v[52:55]\n" \
"ds_read_b128 a[76:79], v206 offset:0\n" \
"v_subrev_u32 v227, s71, v227\n" \
"v_mfma_f32_16x16x32_bf16 v[92:95], a[80:83], a[216:219], v[92:95]\n" \
"ds_read_b64_tr_b16 a[108:109], v209 offset:12288\n" \
"v_lshl_add_u32 v227, v227, 3, v226\n" \
"v_mfma_f32_16x16x32_bf16 v[92:95], a[84:87], a[220:223], v[92:95]\n" \
"global_load_lds_dwordx4 v252, s[44:45] offset:1024\n" \
"v_cmp_gt_i32 vcc, s70, v227\n" \
"v_mfma_f32_16x16x32_bf16 v[88:91], a[80:83], a[208:211], v[88:91]\n" \
"ds_read_b64_tr_b16 a[110:111], v209 offset:13312\n" \
"v_mfma_f32_16x16x32_bf16 v[88:91], a[84:87], a[212:215], v[88:91]\n" \
"s_bcnt1_i32_b32 s70, vcc_lo\n" \
"s_lshr_b32 s70, s70, 2\n" \
"ds_read_b64_tr_b16 a[112:113], v210 offset:0\n" \
"v_mfma_f32_16x16x32_bf16 v[96:99], a[88:91], a[208:211], v[96:99]\n" \
"s_lshr_b32 s78, s70, 2\n" \
"ds_read_b128 a[80:83], v205 offset:4096\n" \
"s_mul_i32 s71, s78, 7\n" \
"v_mfma_f32_16x16x32_bf16 v[96:99], a[92:95], a[212:215], v[96:99]\n" \
"s_xor_b32 s70, s70, s71\n" \
"global_load_lds_dwordx4 v252, s[44:45] offset:3072\n" \
"v_mfma_f32_16x16x32_bf16 v[100:103], a[88:91], a[216:219], v[100:103]\n" \
"s_add_u32 s44, s44, 0x80\n" \
"s_addc_u32 s45, s45, 0\n" \
"ds_read_b128 a[84:87], v206 offset:4096\n" \
"v_mfma_f32_16x16x32_bf16 v[100:103], a[92:95], a[220:223], v[100:103]\n" \
"s_xor_b32 s78, s78, 1\n" \
"s_lshl_b32 s65, s70, 2\n" \
"ds_read_b64_tr_b16 a[114:115], v210 offset:1024\n" \
"v_mfma_f32_16x16x32_bf16 v[60:63], a[200:203], a[88:91], v[60:63]\n" \
"s_add_u32 m0, s85, 135168\n" \
"s_lshr_b32 s64, 0x8654, s65\n" \
"ds_read_b64_tr_b16 a[116:117], v210 offset:12288\n" \
"v_mfma_f32_16x16x32_bf16 v[60:63], a[204:207], a[92:95], v[60:63]\n" \
"buffer_load_dwordx4 v246, s[12:15], s46 offen lds\n" \
"v_mfma_f32_16x16x32_bf16 v[68:71], a[200:203], a[192:195], v[68:71]\n" \
"s_add_u32 s46, s46, 0x80\n" \
"ds_read_b128 a[88:91], v205 offset:8192\n" \
"s_and_b32 s64, s64, 15\n" \
"v_mfma_f32_16x16x32_bf16 v[68:71], a[204:207], a[196:199], v[68:71]\n" \
"s_add_u32 s64, s64, 32\n" \
"ds_read_b128 a[92:95], v206 offset:8192\n" \
"s_lshr_b32 s70, %1, 6\n" \
"v_mfma_f32_16x16x32_bf16 v[108:111], a[192:195], a[216:219], v[108:111]\n" \
"s_add_u32 m0, s84, 143360\n" \
"s_add_u32 s65, s65, s70\n" \
"ds_read_b64_tr_b16 a[118:119], v210 offset:13312\n" \
"v_mfma_f32_16x16x32_bf16 v[108:111], a[196:199], a[220:223], v[108:111]\n" \
"s_and_b32 s30, %1, 1\n" \
"global_load_lds_dwordx4 v253, s[48:49] offset:-4096\n" \
"v_mfma_f32_16x16x32_bf16 v[104:107], a[192:195], a[208:211], v[104:107]\n" \
"s_lshl_b32 s30, s30, 3\n" \
"s_add_u32 s30, s30, s63\n" \
"s_lshl_b32 s30, s30, 2\n" \
"v_mfma_f32_16x16x32_bf16 v[104:107], a[196:199], a[212:215], v[104:107]\n" \
"s_waitcnt lgkmcnt(2)\n" \
"s_lshl_b32 s70, s88, 7\n" \
"s_add_u32 s30, s30, s70\n" \
"v_mfma_f32_16x16x32_bf16 v[32:35], a[96:99], a[64:67], v[32:35]\n" \
"s_add_u32 s30, s30, 65591296\n" \
"ds_read_b128 a[192:195], v205 offset:12288\n" \
"v_lshrrev_b32 v221, 2, v248\n" \
"v_mfma_f32_16x16x32_bf16 v[32:35], a[100:103], a[68:71], v[32:35]\n" \
"s_lshl_b32 s81, s65, 7\n" \
"global_load_lds_dwordx4 v253, s[48:49] offset:-2048\n" \
"v_mfma_f32_16x16x32_bf16 v[40:43], a[96:99], a[72:75], v[40:43]\n" \
"s_add_u32 s82, s81, 65591296\n" \
"ds_read_b128 a[196:199], v206 offset:12288\n" \
"s_add_u32 s81, s81, 65589248\n" \
"v_mfma_f32_16x16x32_bf16 v[40:43], a[100:103], a[76:79], v[40:43]\n" \
"s_mov_b64 s[52:53], s[26:27]\n" \
"s_and_b32 s53, s53, 0xffff\n" \
"ds_read_b64_tr_b16 a[208:209], v209 offset:4096\n" \
"v_mfma_f32_16x16x32_bf16 v[0:3], a[104:107], a[72:75], v[0:3]\n" \
"s_mov_b32 s54, 8388608\n" \
"s_mov_b32 s55, 0x20000\n" \
"ds_read_b64_tr_b16 a[210:211], v209 offset:5120\n" \
"v_mfma_f32_16x16x32_bf16 v[0:3], a[108:111], a[76:79], v[0:3]\n" \
"s_waitcnt lgkmcnt(3)\n" \
"global_load_lds_dwordx4 v253, s[48:49] offset:0\n" \
"s_lshr_b32 s83, s63, 1\n" \
"v_mfma_f32_16x16x32_bf16 v[4:7], a[112:115], a[72:75], v[4:7]\n" \
"s_add_u32 s48, s48, 0x60000\n" \
"s_addc_u32 s49, s49, 0\n" \
"ds_read_b64_tr_b16 a[212:213], v209 offset:16384\n" \
"v_mfma_f32_16x16x32_bf16 v[4:7], a[116:119], a[76:79], v[4:7]\n" \
"s_add_u32 s83, s83, 2\n" \
"s_lshr_b32 s70, s18, 8\n" \
"ds_read_b64_tr_b16 a[214:215], v209 offset:17408\n" \
"v_mfma_f32_16x16x32_bf16 v[12:15], a[112:115], a[80:83], v[12:15]\n" \
"s_sub_u32 s83, s83, s70\n" \
"s_and_b32 s83, s83, 3\n" \
"ds_read_b64_tr_b16 a[216:217], v210 offset:4096\n" \
"v_mfma_f32_16x16x32_bf16 v[12:15], a[116:119], a[84:87], v[12:15]\n" \
"s_and_b32 s70, s63, 1\n" \
"global_load_lds_dwordx4 v253, s[50:51] offset:-3072\n" \
"v_mfma_f32_16x16x32_bf16 v[8:11], a[104:107], a[80:83], v[8:11]\n" \
"s_lshl_b32 s70, s70, 2\n" \
"s_or_b32 s83, s83, s70\n" \
"ds_read_b64_tr_b16 a[218:219], v210 offset:5120\n" \
"v_mfma_f32_16x16x32_bf16 v[8:11], a[108:111], a[84:87], v[8:11]\n" \
"s_mul_i32 s75, s78, s64\n" \
"ds_read_b64_tr_b16 a[220:221], v210 offset:16384\n" \
"v_lshrrev_b32 v228, 4, v248\n" \
"v_mfma_f32_16x16x32_bf16 v[48:51], a[96:99], a[80:83], v[48:51]\n" \
"s_lshl_b32 s70, s65, 2\n" \
"s_add_u32 s70, s70, s61\n" \
"ds_read_b64_tr_b16 a[222:223], v210 offset:17408\n" \
"v_mfma_f32_16x16x32_bf16 v[48:51], a[100:103], a[84:87], v[48:51]\n" \
"s_mul_i32 s73, s70, 69632\n" \
"global_load_lds_dwordx4 v253, s[50:51] offset:-1024\n" \
"v_mfma_f32_16x16x32_bf16 v[56:59], a[96:99], a[88:91], v[56:59]\n" \
"s_add_u32 s73, s73, 52740096\n" \
"s_add_u32 s73, s73, s28\n" \
"ds_read_b64_tr_b16 a[200:201], v210 offset:2048\n" \
"v_mfma_f32_16x16x32_bf16 v[56:59], a[100:103], a[92:95], v[56:59]\n" \
"s_mul_i32 s70, s70, 266240\n" \
"s_lshl_b32 s71, s75, 12\n" \
"ds_read_b64_tr_b16 a[202:203], v210 offset:3072\n" \
"v_mfma_f32_16x16x32_bf16 v[16:19], a[104:107], a[88:91], v[16:19]\n" \
"s_add_u32 s70, s70, s71\n" \
"s_add_u32 s70, s70, 35700736\n" \
"ds_read_b64_tr_b16 a[204:205], v210 offset:14336\n" \
"v_mfma_f32_16x16x32_bf16 v[16:19], a[108:111], a[92:95], v[16:19]\n" \
"s_add_u32 s36, s28, s70\n" \
"global_load_lds_dwordx4 v253, s[50:51] offset:1024\n" \
"v_mfma_f32_16x16x32_bf16 v[20:23], a[112:115], a[88:91], v[20:23]\n" \
"s_add_u32 s50, s50, 0x60000\n" \
"s_addc_u32 s51, s51, 0\n" \
"ds_read_b64_tr_b16 a[206:207], v210 offset:15360\n" \
"v_mfma_f32_16x16x32_bf16 v[20:23], a[116:119], a[92:95], v[20:23]\n" \
"s_waitcnt lgkmcnt(3)\n" \
"v_add_u32 v207, 0xffff0000, v207\n" \
"v_add_u32 v208, 0xffff0000, v208\n" \
"v_mfma_f32_16x16x32_bf16 v[28:31], a[112:115], a[192:195], v[28:31]\n" \
"s_mov_b32 s37, s29\n" \
"v_add_u32 v205, 0xffff0000, v205\n" \
"v_add_u32 v206, 0xffff0000, v206\n" \
"v_mfma_f32_16x16x32_bf16 v[28:31], a[116:119], a[196:199], v[28:31]\n" \
"s_lshr_b32 s74, s61, 1\n" \
"v_add_u32 v209, 0xffff0000, v209\n" \
"v_add_u32 v210, 0xffff0000, v210\n" \
"v_mfma_f32_16x16x32_bf16 v[24:27], a[104:107], a[192:195], v[24:27]\n" \
"s_lshl_b32 s74, s74, 16\n" \
"s_and_b32 s70, s61, 1\n" \
"s_lshl_b32 s70, s70, 14\n" \
"v_mfma_f32_16x16x32_bf16 v[24:27], a[108:111], a[196:199], v[24:27]\n" \
"s_add_u32 s74, s74, s70\n" \
"Lpg10428_%=:\n" \
"s_cbranch_execz Lpg12624_%=\n" \
"s_lshl_b32 s71, s83, 8\n" \
"s_add_u32 s74, s74, s71\n" \
"v_mfma_f32_16x16x32_bf16 v[64:67], a[96:99], a[192:195], v[64:67]\n" \
"s_waitcnt vmcnt(11) lgkmcnt(0)\n" \
"s_add_u32 s40, s24, s74\n" \
"s_addc_u32 s41, s25, 0\n" \
"v_mfma_f32_16x16x32_bf16 v[64:67], a[100:103], a[196:199], v[64:67]\n" \
"s_barrier\n" \
"v_mfma_f32_16x16x32_bf16 v[72:75], a[64:67], a[208:211], v[72:75]\n" \
"s_lshl_b32 s70, s75, 17\n" \
"s_add_u32 s70, s70, s74\n" \
"ds_read_b64_tr_b16 a[96:97], v209 offset:2048\n" \
"v_mfma_f32_16x16x32_bf16 v[72:75], a[68:71], a[212:215], v[72:75]\n" \
"s_add_u32 s38, s18, s70\n" \
"s_addc_u32 s39, s19, 0\n" \
"ds_read_b64_tr_b16 a[98:99], v209 offset:3072\n" \
"v_mfma_f32_16x16x32_bf16 v[80:83], a[72:75], a[208:211], v[80:83]\n" \
"s_add_u32 m0, s95, 65536\n" \
"ds_read_b128 a[64:67], v207 offset:0\n" \
"ds_read_b64_tr_b16 a[100:101], v209 offset:14336\n" \
"v_mfma_f32_16x16x32_bf16 v[80:83], a[76:79], a[212:215], v[80:83]\n" \
"global_load_lds_dwordx4 v252, s[42:43] offset:0\n" \
"v_bfe_u32 v229, v228, 2, 3\n" \
"v_mfma_f32_16x16x32_bf16 v[84:87], a[72:75], a[216:219], v[84:87]\n" \
"ds_read_b128 a[68:71], v208 offset:0\n" \
"ds_read_b64_tr_b16 a[102:103], v209 offset:15360\n" \
"v_mfma_f32_16x16x32_bf16 v[84:87], a[76:79], a[220:223], v[84:87]\n" \
"ds_read_b64_tr_b16 a[104:105], v209 offset:0\n" \
"v_lshlrev_b32 v229, 11, v229\n" \
"v_mfma_f32_16x16x32_bf16 v[44:47], a[200:203], a[72:75], v[44:47]\n" \
"ds_read_b64_tr_b16 a[106:107], v209 offset:1024\n" \
"v_lshrrev_b32 v230, 5, v228\n" \
"v_mfma_f32_16x16x32_bf16 v[44:47], a[204:207], a[76:79], v[44:47]\n" \
"global_load_lds_dwordx4 v252, s[42:43] offset:2048\n" \
"v_lshl_add_u32 v229, v230, 6, v229\n" \
"v_mfma_f32_16x16x32_bf16 v[52:55], a[200:203], a[80:83], v[52:55]\n" \
"s_add_u32 s42, s42, 0x80\n" \
"s_addc_u32 s43, s43, 0\n" \
"ds_read_b128 a[72:75], v205 offset:0\n" \
"v_mfma_f32_16x16x32_bf16 v[52:55], a[204:207], a[84:87], v[52:55]\n" \
"ds_read_b128 a[76:79], v206 offset:0\n" \
"v_and_b32 v230, 3, v228\n" \
"v_mfma_f32_16x16x32_bf16 v[92:95], a[80:83], a[216:219], v[92:95]\n" \
"ds_read_b64_tr_b16 a[108:109], v209 offset:12288\n" \
"v_bfe_u32 v231, v228, 4, 1\n" \
"v_mfma_f32_16x16x32_bf16 v[92:95], a[84:87], a[220:223], v[92:95]\n" \
"global_load_lds_dwordx4 v252, s[44:45] offset:1024\n" \
"v_lshlrev_b32 v231, 1, v231\n" \
"v_mfma_f32_16x16x32_bf16 v[88:91], a[80:83], a[208:211], v[88:91]\n" \
"ds_read_b64_tr_b16 a[110:111], v209 offset:13312\n" \
"v_xor_b32 v230, v230, v231\n" \
"v_mfma_f32_16x16x32_bf16 v[88:91], a[84:87], a[212:215], v[88:91]\n" \
"ds_read_b64_tr_b16 a[112:113], v210 offset:0\n" \
"v_lshl_add_u32 v229, v230, 4, v229\n" \
"v_mfma_f32_16x16x32_bf16 v[96:99], a[88:91], a[208:211], v[96:99]\n" \
"ds_read_b128 a[80:83], v205 offset:4096\n" \
"v_add_u32 v216, 4096, v229\n" \
"v_mfma_f32_16x16x32_bf16 v[96:99], a[92:95], a[212:215], v[96:99]\n" \
"global_load_lds_dwordx4 v252, s[44:45] offset:3072\n" \
"v_add_u32 v217, 3200, v229\n" \
"v_mfma_f32_16x16x32_bf16 v[100:103], a[88:91], a[216:219], v[100:103]\n" \
"s_add_u32 s44, s44, 0x80\n" \
"s_addc_u32 s45, s45, 0\n" \
"ds_read_b128 a[84:87], v206 offset:4096\n" \
"v_mfma_f32_16x16x32_bf16 v[100:103], a[92:95], a[220:223], v[100:103]\n" \
"ds_read_b64_tr_b16 a[114:115], v210 offset:1024\n" \
"v_add_u32 v218, 34816, v229\n" \
"v_mfma_f32_16x16x32_bf16 v[60:63], a[200:203], a[88:91], v[60:63]\n" \
"s_add_u32 m0, s85, 81920\n" \
"ds_read_b64_tr_b16 a[116:117], v210 offset:12288\n" \
"v_add_u32 v219, 33920, v229\n" \
"v_mfma_f32_16x16x32_bf16 v[60:63], a[204:207], a[92:95], v[60:63]\n" \
"buffer_load_dwordx4 v246, s[12:15], s46 offen lds\n" \
"v_mfma_f32_16x16x32_bf16 v[68:71], a[200:203], a[192:195], v[68:71]\n" \
"s_add_u32 s46, s46, 0x80\n" \
"ds_read_b128 a[88:91], v205 offset:8192\n" \
"s_mov_b32 s75, 65536\n" \
"v_mfma_f32_16x16x32_bf16 v[68:71], a[204:207], a[196:199], v[68:71]\n" \
"s_lshr_b32 s70, s61, 1\n" \
"ds_read_b128 a[92:95], v206 offset:8192\n" \
"s_lshl_b32 s71, s70, 13\n" \
"v_mfma_f32_16x16x32_bf16 v[108:111], a[192:195], a[216:219], v[108:111]\n" \
"s_add_u32 m0, s84, 90112\n" \
"ds_read_b64_tr_b16 a[118:119], v210 offset:13312\n" \
"v_add_u32 v211, s71, v249\n" \
"v_mfma_f32_16x16x32_bf16 v[108:111], a[196:199], a[220:223], v[108:111]\n" \
"s_lshl_b32 s71, s65, 1\n" \
"global_load_lds_dwordx4 v253, s[48:49] offset:-4096\n" \
"v_mfma_f32_16x16x32_bf16 v[104:107], a[192:195], a[208:211], v[104:107]\n" \
"s_add_u32 s70, s70, s71\n" \
"s_lshl_b32 s77, s70, 18\n" \
"s_lshl_b32 s71, s83, 8\n" \
"v_mfma_f32_16x16x32_bf16 v[104:107], a[196:199], a[212:215], v[104:107]\n" \
"s_waitcnt lgkmcnt(2)\n" \
"s_add_u32 s77, s77, s71\n" \
"s_lshl_b32 s71, s78, 11\n" \
"v_mfma_f32_16x16x32_bf16 v[32:35], a[96:99], a[64:67], v[32:35]\n" \
"s_add_u32 s77, s77, s71\n" \
"ds_read_b128 a[192:195], v205 offset:12288\n" \
"s_and_b32 s70, s61, 1\n" \
"v_mfma_f32_16x16x32_bf16 v[32:35], a[100:103], a[68:71], v[32:35]\n" \
"s_lshl_b32 s71, s70, 7\n" \
"global_load_lds_dwordx4 v253, s[48:49] offset:-2048\n" \
"v_mfma_f32_16x16x32_bf16 v[40:43], a[96:99], a[72:75], v[40:43]\n" \
"s_add_u32 s77, s77, s71\n" \
"ds_read_b128 a[196:199], v206 offset:12288\n" \
"v_and_b32 v229, 3, v228\n" \
"v_mfma_f32_16x16x32_bf16 v[40:43], a[100:103], a[76:79], v[40:43]\n" \
"ds_read_b64_tr_b16 a[208:209], v209 offset:4096\n" \
"v_lshlrev_b32 v229, 3, v229\n" \
"v_mfma_f32_16x16x32_bf16 v[0:3], a[104:107], a[72:75], v[0:3]\n" \
"ds_read_b64_tr_b16 a[210:211], v209 offset:5120\n" \
"v_bfe_u32 v230, v228, 2, 2\n" \
"v_mfma_f32_16x16x32_bf16 v[0:3], a[108:111], a[76:79], v[0:3]\n" \
"s_waitcnt lgkmcnt(3)\n" \
"global_load_lds_dwordx4 v253, s[48:49] offset:0\n" \
"v_lshl_add_u32 v229, v230, 6, v229\n" \
"v_mfma_f32_16x16x32_bf16 v[4:7], a[112:115], a[72:75], v[4:7]\n" \
"s_add_u32 s48, s48, 0x60000\n" \
"s_addc_u32 s49, s49, 0\n" \
"ds_read_b64_tr_b16 a[212:213], v209 offset:16384\n" \
"v_mfma_f32_16x16x32_bf16 v[4:7], a[116:119], a[76:79], v[4:7]\n" \
"ds_read_b64_tr_b16 a[214:215], v209 offset:17408\n" \
"v_bfe_u32 v230, v228, 4, 1\n" \
"v_mfma_f32_16x16x32_bf16 v[12:15], a[112:115], a[80:83], v[12:15]\n" \
"ds_read_b64_tr_b16 a[216:217], v210 offset:4096\n" \
"v_lshl_add_u32 v229, v230, 5, v229\n" \
"v_mfma_f32_16x16x32_bf16 v[12:15], a[116:119], a[84:87], v[12:15]\n" \
"global_load_lds_dwordx4 v253, s[50:51] offset:-3072\n" \
"v_lshl_add_u32 v229, v230, 8, v229\n" \
"v_mfma_f32_16x16x32_bf16 v[8:11], a[104:107], a[80:83], v[8:11]\n" \
"ds_read_b64_tr_b16 a[218:219], v210 offset:5120\n" \
"v_lshrrev_b32 v230, 5, v228\n" \
"v_mfma_f32_16x16x32_bf16 v[8:11], a[108:111], a[84:87], v[8:11]\n" \
"ds_read_b64_tr_b16 a[220:221], v210 offset:16384\n" \
"v_lshl_add_u32 v229, v230, 12, v229\n" \
"v_mfma_f32_16x16x32_bf16 v[48:51], a[96:99], a[80:83], v[48:51]\n" \
"s_lshl_b32 s71, s70, 10\n" \
"ds_read_b64_tr_b16 a[222:223], v210 offset:17408\n" \
"v_add_u32 v212, s71, v229\n" \
"v_mfma_f32_16x16x32_bf16 v[48:51], a[100:103], a[84:87], v[48:51]\n" \
"s_lshl_b32 s70, s65, 3\n" \
"global_load_lds_dwordx4 v253, s[50:51] offset:-1024\n" \
"v_mfma_f32_16x16x32_bf16 v[56:59], a[96:99], a[88:91], v[56:59]\n" \
"s_add_u32 s70, s70, s83\n" \
"s_lshl_b32 s76, s70, 16\n" \
"ds_read_b64_tr_b16 a[200:201], v210 offset:2048\n" \
"v_mfma_f32_16x16x32_bf16 v[56:59], a[100:103], a[92:95], v[56:59]\n" \
"s_lshl_b32 s71, s61, 14\n" \
"s_add_u32 s76, s76, s71\n" \
"ds_read_b64_tr_b16 a[202:203], v210 offset:3072\n" \
"v_mfma_f32_16x16x32_bf16 v[16:19], a[104:107], a[88:91], v[16:19]\n" \
"s_add_u32 s76, s76, 57196544\n" \
"s_lshl_b32 s79, s70, 7\n" \
"ds_read_b64_tr_b16 a[204:205], v210 offset:14336\n" \
"v_mfma_f32_16x16x32_bf16 v[16:19], a[108:111], a[92:95], v[16:19]\n" \
"s_add_u32 s79, s79, 65601536\n" \
"global_load_lds_dwordx4 v253, s[50:51] offset:1024\n" \
"v_mfma_f32_16x16x32_bf16 v[20:23], a[112:115], a[88:91], v[20:23]\n" \
"s_add_u32 s50, s50, 0x60000\n" \
"s_addc_u32 s51, s51, 0\n" \
"ds_read_b64_tr_b16 a[206:207], v210 offset:15360\n" \
"v_mfma_f32_16x16x32_bf16 v[20:23], a[116:119], a[92:95], v[20:23]\n" \
"s_waitcnt lgkmcnt(3)\n" \
"v_add_u32 v207, 0x1d000, v207\n" \
"v_add_u32 v208, 0x1d000, v208\n" \
"v_mfma_f32_16x16x32_bf16 v[28:31], a[112:115], a[192:195], v[28:31]\n" \
"s_lshr_b32 s70, s64, 1\n" \
"v_add_u32 v205, 0x1d000, v205\n" \
"v_add_u32 v206, 0x1d000, v206\n" \
"v_mfma_f32_16x16x32_bf16 v[28:31], a[116:119], a[196:199], v[28:31]\n" \
"s_lshl_b32 s70, -1, s70\n" \
"v_add_u32 v209, 0x1d000, v209\n" \
"v_add_u32 v210, 0x1d000, v210\n" \
"v_mfma_f32_16x16x32_bf16 v[24:27], a[104:107], a[192:195], v[24:27]\n" \
"s_add_u32 s71, s64, 1\n" \
"s_lshr_b32 s71, s71, 1\n" \
"s_bfm_b32 s71, s71, 0\n" \
"v_mfma_f32_16x16x32_bf16 v[24:27], a[108:111], a[196:199], v[24:27]\n" \
"s_cmp_eq_u32 s78, 0\n" \
"s_cselect_b32 s80, s71, s70\n" \
"s_mov_b32 s16, 0xff000000\n" \
"v_mfma_f32_16x16x32_bf16 v[64:67], a[96:99], a[192:195], v[64:67]\n" \
"s_waitcnt vmcnt(11) lgkmcnt(0)\n" \
"s_mov_b32 s17, 0\n" \
"v_add_u32 v222, 0x1c90080, v250\n" \
"v_mfma_f32_16x16x32_bf16 v[64:67], a[100:103], a[196:199], v[64:67]\n" \
"s_barrier\n" \
"v_mfma_f32_16x16x32_bf16 v[72:75], a[64:67], a[208:211], v[72:75]\n" \
"ds_read_b64_tr_b16 a[96:97], v209 offset:2048\n" \
"v_cndmask_b32 v222, v250, v222, s[16:17]\n" \
"v_mfma_f32_16x16x32_bf16 v[72:75], a[68:71], a[212:215], v[72:75]\n" \
"s_mov_b32 s74, 98304\n" \
"ds_read_b64_tr_b16 a[98:99], v209 offset:3072\n" \
"v_mfma_f32_16x16x32_bf16 v[80:83], a[72:75], a[208:211], v[80:83]\n" \
"s_add_u32 m0, s95, 0\n" \
"ds_read_b128 a[64:67], v207 offset:0\n" \
"ds_read_b64_tr_b16 a[100:101], v209 offset:14336\n" \
"v_mfma_f32_16x16x32_bf16 v[80:83], a[76:79], a[212:215], v[80:83]\n" \
"global_load_lds_dwordx4 v252, s[42:43] offset:0\n" \
"v_mfma_f32_16x16x32_bf16 v[84:87], a[72:75], a[216:219], v[84:87]\n" \
"ds_read_b128 a[68:71], v208 offset:0\n" \
"ds_read_b64_tr_b16 a[102:103], v209 offset:15360\n" \
"v_mfma_f32_16x16x32_bf16 v[84:87], a[76:79], a[220:223], v[84:87]\n" \
"s_sub_u32 s74, s74, 32768\n" \
"s_cselect_b32 s74, 131072, s74\n" \
"ds_read_b64_tr_b16 a[104:105], v209 offset:0\n" \
"v_mfma_f32_16x16x32_bf16 v[44:47], a[200:203], a[72:75], v[44:47]\n" \
"ds_read_b64_tr_b16 a[106:107], v209 offset:1024\n" \
"v_add_u32 v213, s74, v211\n" \
"v_mfma_f32_16x16x32_bf16 v[44:47], a[204:207], a[76:79], v[44:47]\n" \
"global_load_lds_dwordx4 v252, s[42:43] offset:2048\n" \
"v_add_u32 v214, s74, v212\n" \
"v_mfma_f32_16x16x32_bf16 v[52:55], a[200:203], a[80:83], v[52:55]\n" \
"s_add_u32 s42, s42, 0x80\n" \
"s_addc_u32 s43, s43, 0\n" \
"ds_read_b128 a[72:75], v205 offset:0\n" \
"v_mfma_f32_16x16x32_bf16 v[52:55], a[204:207], a[84:87], v[52:55]\n" \
"ds_read_b128 a[76:79], v206 offset:0\n" \
"v_xor_b32 v215, 32, v214\n" \
"v_mfma_f32_16x16x32_bf16 v[92:95], a[80:83], a[216:219], v[92:95]\n" \
"s_and_b32 s89, s61, 1\n" \
"s_lshl_b32 s89, s89, 11\n" \
"ds_read_b64_tr_b16 a[108:109], v209 offset:12288\n" \
"v_mfma_f32_16x16x32_bf16 v[92:95], a[84:87], a[220:223], v[92:95]\n" \
"s_add_u32 s87, s87, s89\n" \
"global_load_lds_dwordx4 v252, s[44:45] offset:1024\n" \
"v_mfma_f32_16x16x32_bf16 v[88:91], a[80:83], a[208:211], v[88:91]\n" \
"s_add_u32 s87, s87, 52740096\n" \
"ds_read_b64_tr_b16 a[110:111], v209 offset:13312\n" \
"v_mfma_f32_16x16x32_bf16 v[88:91], a[84:87], a[212:215], v[88:91]\n" \
"s_cmp_eq_u32 s88, 0\n" \
"s_cselect_b32 s86, -1, 0\n" \
"ds_read_b64_tr_b16 a[112:113], v210 offset:0\n" \
"v_mfma_f32_16x16x32_bf16 v[96:99], a[88:91], a[208:211], v[96:99]\n" \
"s_mul_i32 s89, s88, 278528\n" \
"ds_read_b128 a[80:83], v205 offset:4096\n" \
"s_add_u32 s87, s87, s89\n" \
"v_mfma_f32_16x16x32_bf16 v[96:99], a[92:95], a[212:215], v[96:99]\n" \
"s_lshr_b32 s89, s61, 1\n" \
"global_load_lds_dwordx4 v252, s[44:45] offset:3072\n" \
"v_mfma_f32_16x16x32_bf16 v[100:103], a[88:91], a[216:219], v[100:103]\n" \
"s_add_u32 s44, s44, 0x80\n" \
"s_addc_u32 s45, s45, 0\n" \
"ds_read_b128 a[84:87], v206 offset:4096\n" \
"v_mfma_f32_16x16x32_bf16 v[100:103], a[92:95], a[220:223], v[100:103]\n" \
"s_lshl_b32 s89, s89, 8\n" \
"s_add_u32 s87, s87, s89\n" \
"ds_read_b64_tr_b16 a[114:115], v210 offset:1024\n" \
"v_mfma_f32_16x16x32_bf16 v[60:63], a[200:203], a[88:91], v[60:63]\n" \
"s_add_u32 m0, s85, 16384\n" \
"s_lshl_b32 s89, s61, 4\n" \
"ds_read_b64_tr_b16 a[116:117], v210 offset:12288\n" \
"v_mfma_f32_16x16x32_bf16 v[60:63], a[204:207], a[92:95], v[60:63]\n" \
"buffer_load_dwordx4 v246, s[12:15], s46 offen lds\n" \
"v_mfma_f32_16x16x32_bf16 v[68:71], a[200:203], a[192:195], v[68:71]\n" \
"s_add_u32 s46, s46, 0x80\n" \
"ds_read_b128 a[88:91], v205 offset:8192\n" \
"v_subrev_u32 v147, s89, v251\n" \
"v_mfma_f32_16x16x32_bf16 v[68:71], a[204:207], a[196:199], v[68:71]\n" \
"s_lshr_b32 s89, s61, 1\n" \
"ds_read_b128 a[92:95], v206 offset:8192\n" \
"s_lshl_b32 s90, s89, 9\n" \
"v_mfma_f32_16x16x32_bf16 v[108:111], a[192:195], a[216:219], v[108:111]\n" \
"s_add_u32 m0, s84, 24576\n" \
"s_add_u32 s90, s90, 511\n" \
"ds_read_b64_tr_b16 a[118:119], v210 offset:13312\n" \
"v_mfma_f32_16x16x32_bf16 v[108:111], a[196:199], a[220:223], v[108:111]\n" \
"global_load_lds_dwordx4 v253, s[48:49] offset:-4096\n" \
"v_sub_u32 v145, s90, v248\n" \
"v_mfma_f32_16x16x32_bf16 v[104:107], a[192:195], a[208:211], v[104:107]\n" \
"s_lshl_b32 s90, s89, 3\n" \
"s_not_b32 s90, s90\n" \
"v_sub_u32 v146, s90, v147\n" \
"v_mfma_f32_16x16x32_bf16 v[104:107], a[196:199], a[212:215], v[104:107]\n" \
"s_waitcnt lgkmcnt(2)\n" \
"v_subrev_u32 v152, 0, v147\n" \
"v_sub_u32 v144, 7, v147\n" \
"v_mfma_f32_16x16x32_bf16 v[32:35], a[96:99], a[64:67], v[32:35]\n" \
"ds_read_b128 a[192:195], v205 offset:12288\n" \
"v_or_b32 v152, v152, v144\n" \
"v_mfma_f32_16x16x32_bf16 v[32:35], a[100:103], a[68:71], v[32:35]\n" \
"global_load_lds_dwordx4 v253, s[48:49] offset:-2048\n" \
"v_ashrrev_i32 v152, 31, v152\n" \
"v_mfma_f32_16x16x32_bf16 v[40:43], a[96:99], a[72:75], v[40:43]\n" \
"ds_read_b128 a[196:199], v206 offset:12288\n" \
"v_and_b32 v152, 0xff800000, v152\n" \
"v_mfma_f32_16x16x32_bf16 v[40:43], a[100:103], a[76:79], v[40:43]\n" \
"ds_read_b64_tr_b16 a[208:209], v209 offset:4096\n" \
"v_mov_b32 v160, v152\n" \
"v_mfma_f32_16x16x32_bf16 v[0:3], a[104:107], a[72:75], v[0:3]\n" \
"ds_read_b64_tr_b16 a[210:211], v209 offset:5120\n" \
"v_mov_b32 v168, v152\n" \
"v_mfma_f32_16x16x32_bf16 v[0:3], a[108:111], a[76:79], v[0:3]\n" \
"s_waitcnt lgkmcnt(3)\n" \
"global_load_lds_dwordx4 v253, s[48:49] offset:0\n" \
"v_mov_b32 v176, v152\n" \
"v_mfma_f32_16x16x32_bf16 v[4:7], a[112:115], a[72:75], v[4:7]\n" \
"s_add_u32 s48, s48, 0x60000\n" \
"s_addc_u32 s49, s49, 0\n" \
"ds_read_b64_tr_b16 a[212:213], v209 offset:16384\n" \
"v_mfma_f32_16x16x32_bf16 v[4:7], a[116:119], a[76:79], v[4:7]\n" \
"ds_read_b64_tr_b16 a[214:215], v209 offset:17408\n" \
"v_sub_u32 v156, -9, v147\n" \
"v_mfma_f32_16x16x32_bf16 v[12:15], a[112:115], a[80:83], v[12:15]\n" \
"ds_read_b64_tr_b16 a[216:217], v210 offset:4096\n" \
"v_ashrrev_i32 v156, 31, v156\n" \
"v_mfma_f32_16x16x32_bf16 v[12:15], a[116:119], a[84:87], v[12:15]\n" \
"global_load_lds_dwordx4 v253, s[50:51] offset:-3072\n" \
"v_and_b32 v156, 0xff800000, v156\n" \
"v_mfma_f32_16x16x32_bf16 v[8:11], a[104:107], a[80:83], v[8:11]\n" \
"ds_read_b64_tr_b16 a[218:219], v210 offset:5120\n" \
"v_mov_b32 v164, v156\n" \
"v_mfma_f32_16x16x32_bf16 v[8:11], a[108:111], a[84:87], v[8:11]\n" \
"ds_read_b64_tr_b16 a[220:221], v210 offset:16384\n" \
"v_mov_b32 v172, v156\n" \
"v_mfma_f32_16x16x32_bf16 v[48:51], a[96:99], a[80:83], v[48:51]\n" \
"ds_read_b64_tr_b16 a[222:223], v210 offset:17408\n" \
"v_add_u32 v148, 0, v146\n" \
"v_mfma_f32_16x16x32_bf16 v[48:51], a[100:103], a[84:87], v[48:51]\n" \
"global_load_lds_dwordx4 v253, s[50:51] offset:-1024\n" \
"v_or3_b32 v148, v148, v145, s86\n" \
"v_mfma_f32_16x16x32_bf16 v[56:59], a[96:99], a[88:91], v[56:59]\n" \
"ds_read_b64_tr_b16 a[200:201], v210 offset:2048\n" \
"v_ashrrev_i32 v148, 31, v148\n" \
"v_mfma_f32_16x16x32_bf16 v[56:59], a[100:103], a[92:95], v[56:59]\n" \
"ds_read_b64_tr_b16 a[202:203], v210 offset:3072\n" \
"v_and_b32 v148, 0xff800000, v148\n" \
"v_mfma_f32_16x16x32_bf16 v[16:19], a[104:107], a[88:91], v[16:19]\n" \
"ds_read_b64_tr_b16 a[204:205], v210 offset:14336\n" \
"v_subrev_u32 v153, 1, v147\n" \
"v_mfma_f32_16x16x32_bf16 v[16:19], a[108:111], a[92:95], v[16:19]\n" \
"global_load_lds_dwordx4 v253, s[50:51] offset:1024\n" \
"v_sub_u32 v144, 8, v147\n" \
"v_mfma_f32_16x16x32_bf16 v[20:23], a[112:115], a[88:91], v[20:23]\n" \
"s_add_u32 s50, s50, 0x60000\n" \
"s_addc_u32 s51, s51, 0\n" \
"ds_read_b64_tr_b16 a[206:207], v210 offset:15360\n" \
"v_mfma_f32_16x16x32_bf16 v[20:23], a[116:119], a[92:95], v[20:23]\n" \
"s_waitcnt lgkmcnt(3)\n" \
"v_add_u32 v207, 0xffff3000, v207\n" \
"v_add_u32 v208, 0xffff3000, v208\n" \
"v_mfma_f32_16x16x32_bf16 v[28:31], a[112:115], a[192:195], v[28:31]\n" \
"v_add_u32 v205, 0xffff3000, v205\n" \
"v_add_u32 v206, 0xffff3000, v206\n" \
"v_mfma_f32_16x16x32_bf16 v[28:31], a[116:119], a[196:199], v[28:31]\n" \
"buffer_load_dword v220, v221, s[56:59], s81 offen sc1\n" \
"v_mfma_f32_16x16x32_bf16 v[24:27], a[104:107], a[192:195], v[24:27]\n" \
"v_add_u32 v209, 0xffff3000, v209\n" \
"v_add_u32 v210, 0xffff3000, v210\n" \
"v_mfma_f32_16x16x32_bf16 v[24:27], a[108:111], a[196:199], v[24:27]\n" \
"v_or_b32 v153, v153, v144\n" \
"v_ashrrev_i32 v153, 31, v153\n" \
"v_mfma_f32_16x16x32_bf16 v[64:67], a[96:99], a[192:195], v[64:67]\n" \
"s_waitcnt vmcnt(12) lgkmcnt(0)\n" \
"v_and_b32 v153, 0xff800000, v153\n" \
"v_mov_b32 v161, v153\n" \
"v_mfma_f32_16x16x32_bf16 v[64:67], a[100:103], a[196:199], v[64:67]\n" \
"s_barrier\n" \
"v_mfma_f32_16x16x32_bf16 v[72:75], a[64:67], a[208:211], v[72:75]\n" \
"ds_read_b64_tr_b16 a[96:97], v209 offset:2048\n" \
"v_mov_b32 v169, v153\n" \
"v_mfma_f32_16x16x32_bf16 v[72:75], a[68:71], a[212:215], v[72:75]\n" \
"ds_read_b64_tr_b16 a[98:99], v209 offset:3072\n" \
"v_mov_b32 v177, v153\n" \
"v_mfma_f32_16x16x32_bf16 v[80:83], a[72:75], a[208:211], v[80:83]\n" \
"s_add_u32 m0, s95, 118784\n" \
"ds_read_b128 a[64:67], v207 offset:0\n" \
"ds_read_b64_tr_b16 a[100:101], v209 offset:14336\n" \
"v_mfma_f32_16x16x32_bf16 v[80:83], a[76:79], a[212:215], v[80:83]\n" \
"global_load_lds_dwordx4 v252, s[42:43] offset:0\n" \
"v_sub_u32 v157, -8, v147\n" \
"v_mfma_f32_16x16x32_bf16 v[84:87], a[72:75], a[216:219], v[84:87]\n" \
"ds_read_b128 a[68:71], v208 offset:0\n" \
"ds_read_b64_tr_b16 a[102:103], v209 offset:15360\n" \
"v_mfma_f32_16x16x32_bf16 v[84:87], a[76:79], a[220:223], v[84:87]\n" \
"ds_read_b64_tr_b16 a[104:105], v209 offset:0\n" \
"v_ashrrev_i32 v157, 31, v157\n" \
"v_mfma_f32_16x16x32_bf16 v[44:47], a[200:203], a[72:75], v[44:47]\n" \
"ds_read_b64_tr_b16 a[106:107], v209 offset:1024\n" \
"v_and_b32 v157, 0xff800000, v157\n" \
"v_mfma_f32_16x16x32_bf16 v[44:47], a[204:207], a[76:79], v[44:47]\n" \
"global_load_lds_dwordx4 v252, s[42:43] offset:2048\n" \
"v_mov_b32 v165, v157\n" \
"v_mfma_f32_16x16x32_bf16 v[52:55], a[200:203], a[80:83], v[52:55]\n" \
"ds_read_b128 a[72:75], v205 offset:0\n" \
"v_mov_b32 v173, v157\n" \
"v_mfma_f32_16x16x32_bf16 v[52:55], a[204:207], a[84:87], v[52:55]\n" \
"ds_read_b128 a[76:79], v206 offset:0\n" \
"v_add_u32 v149, 1, v146\n" \
"v_mfma_f32_16x16x32_bf16 v[92:95], a[80:83], a[216:219], v[92:95]\n" \
"ds_read_b64_tr_b16 a[108:109], v209 offset:12288\n" \
"v_or3_b32 v149, v149, v145, s86\n" \
"v_mfma_f32_16x16x32_bf16 v[92:95], a[84:87], a[220:223], v[92:95]\n" \
"global_load_lds_dwordx4 v252, s[44:45] offset:1024\n" \
"v_ashrrev_i32 v149, 31, v149\n" \
"v_mfma_f32_16x16x32_bf16 v[88:91], a[80:83], a[208:211], v[88:91]\n" \
"ds_read_b64_tr_b16 a[110:111], v209 offset:13312\n" \
"v_and_b32 v149, 0xff800000, v149\n" \
"v_mfma_f32_16x16x32_bf16 v[88:91], a[84:87], a[212:215], v[88:91]\n" \
"ds_read_b64_tr_b16 a[112:113], v210 offset:0\n" \
"v_subrev_u32 v154, 2, v147\n" \
"v_mfma_f32_16x16x32_bf16 v[96:99], a[88:91], a[208:211], v[96:99]\n" \
"ds_read_b128 a[80:83], v205 offset:4096\n" \
"v_sub_u32 v144, 9, v147\n" \
"v_mfma_f32_16x16x32_bf16 v[96:99], a[92:95], a[212:215], v[96:99]\n" \
"global_load_lds_dwordx4 v252, s[44:45] offset:3072\n" \
"v_or_b32 v154, v154, v144\n" \
"v_mfma_f32_16x16x32_bf16 v[100:103], a[88:91], a[216:219], v[100:103]\n" \
"ds_read_b128 a[84:87], v206 offset:4096\n" \
"v_ashrrev_i32 v154, 31, v154\n" \
"v_mfma_f32_16x16x32_bf16 v[100:103], a[92:95], a[220:223], v[100:103]\n" \
"ds_read_b64_tr_b16 a[114:115], v210 offset:1024\n" \
"v_and_b32 v154, 0xff800000, v154\n" \
"v_mfma_f32_16x16x32_bf16 v[60:63], a[200:203], a[88:91], v[60:63]\n" \
"s_add_u32 m0, s85, 135168\n" \
"ds_read_b64_tr_b16 a[116:117], v210 offset:12288\n" \
"v_mov_b32 v162, v154\n" \
"v_mfma_f32_16x16x32_bf16 v[60:63], a[204:207], a[92:95], v[60:63]\n" \
"buffer_load_dwordx4 v246, s[12:15], s46 offen lds\n" \
"v_mfma_f32_16x16x32_bf16 v[68:71], a[200:203], a[192:195], v[68:71]\n" \
"ds_read_b128 a[88:91], v205 offset:8192\n" \
"v_mov_b32 v170, v154\n" \
"v_mfma_f32_16x16x32_bf16 v[68:71], a[204:207], a[196:199], v[68:71]\n" \
"ds_read_b128 a[92:95], v206 offset:8192\n" \
"v_mov_b32 v178, v154\n" \
"v_mfma_f32_16x16x32_bf16 v[108:111], a[192:195], a[216:219], v[108:111]\n" \
"s_add_u32 m0, s84, 143360\n" \
"ds_read_b64_tr_b16 a[118:119], v210 offset:13312\n" \
"v_sub_u32 v158, -7, v147\n" \
"v_mfma_f32_16x16x32_bf16 v[108:111], a[196:199], a[220:223], v[108:111]\n" \
"global_load_lds_dwordx4 v253, s[48:49] offset:-4096\n" \
"v_ashrrev_i32 v158, 31, v158\n" \
"v_mfma_f32_16x16x32_bf16 v[104:107], a[192:195], a[208:211], v[104:107]\n" \
"v_and_b32 v158, 0xff800000, v158\n" \
"v_mov_b32 v166, v158\n" \
"v_mfma_f32_16x16x32_bf16 v[104:107], a[196:199], a[212:215], v[104:107]\n" \
"s_waitcnt lgkmcnt(2)\n" \
"v_mov_b32 v174, v158\n" \
"v_add_u32 v150, 2, v146\n" \
"v_mfma_f32_16x16x32_bf16 v[32:35], a[96:99], a[64:67], v[32:35]\n" \
"ds_read_b128 a[192:195], v205 offset:12288\n" \
"v_or3_b32 v150, v150, v145, s86\n" \
"v_mfma_f32_16x16x32_bf16 v[32:35], a[100:103], a[68:71], v[32:35]\n" \
"global_load_lds_dwordx4 v253, s[48:49] offset:-2048\n" \
"v_ashrrev_i32 v150, 31, v150\n" \
"v_mfma_f32_16x16x32_bf16 v[40:43], a[96:99], a[72:75], v[40:43]\n" \
"ds_read_b128 a[196:199], v206 offset:12288\n" \
"v_and_b32 v150, 0xff800000, v150\n" \
"v_mfma_f32_16x16x32_bf16 v[40:43], a[100:103], a[76:79], v[40:43]\n" \
"ds_read_b64_tr_b16 a[208:209], v209 offset:4096\n" \
"v_subrev_u32 v155, 3, v147\n" \
"v_mfma_f32_16x16x32_bf16 v[0:3], a[104:107], a[72:75], v[0:3]\n" \
"ds_read_b64_tr_b16 a[210:211], v209 offset:5120\n" \
"v_sub_u32 v144, 10, v147\n" \
"v_mfma_f32_16x16x32_bf16 v[0:3], a[108:111], a[76:79], v[0:3]\n" \
"s_waitcnt lgkmcnt(3)\n" \
"global_load_lds_dwordx4 v253, s[48:49] offset:0\n" \
"v_or_b32 v155, v155, v144\n" \
"v_mfma_f32_16x16x32_bf16 v[4:7], a[112:115], a[72:75], v[4:7]\n" \
"ds_read_b64_tr_b16 a[212:213], v209 offset:16384\n" \
"v_ashrrev_i32 v155, 31, v155\n" \
"v_mfma_f32_16x16x32_bf16 v[4:7], a[116:119], a[76:79], v[4:7]\n" \
"ds_read_b64_tr_b16 a[214:215], v209 offset:17408\n" \
"v_and_b32 v155, 0xff800000, v155\n" \
"v_mfma_f32_16x16x32_bf16 v[12:15], a[112:115], a[80:83], v[12:15]\n" \
"ds_read_b64_tr_b16 a[216:217], v210 offset:4096\n" \
"v_mov_b32 v163, v155\n" \
"v_mfma_f32_16x16x32_bf16 v[12:15], a[116:119], a[84:87], v[12:15]\n" \
"global_load_lds_dwordx4 v253, s[50:51] offset:-3072\n" \
"v_mov_b32 v171, v155\n" \
"v_mfma_f32_16x16x32_bf16 v[8:11], a[104:107], a[80:83], v[8:11]\n" \
"ds_read_b64_tr_b16 a[218:219], v210 offset:5120\n" \
"v_mov_b32 v179, v155\n" \
"v_mfma_f32_16x16x32_bf16 v[8:11], a[108:111], a[84:87], v[8:11]\n" \
"ds_read_b64_tr_b16 a[220:221], v210 offset:16384\n" \
"v_sub_u32 v159, -6, v147\n" \
"v_mfma_f32_16x16x32_bf16 v[48:51], a[96:99], a[80:83], v[48:51]\n" \
"ds_read_b64_tr_b16 a[222:223], v210 offset:17408\n" \
"v_ashrrev_i32 v159, 31, v159\n" \
"v_mfma_f32_16x16x32_bf16 v[48:51], a[100:103], a[84:87], v[48:51]\n" \
"global_load_lds_dwordx4 v253, s[50:51] offset:-1024\n" \
"v_and_b32 v159, 0xff800000, v159\n" \
"v_mfma_f32_16x16x32_bf16 v[56:59], a[96:99], a[88:91], v[56:59]\n" \
"ds_read_b64_tr_b16 a[200:201], v210 offset:2048\n" \
"v_mov_b32 v167, v159\n" \
"v_mfma_f32_16x16x32_bf16 v[56:59], a[100:103], a[92:95], v[56:59]\n" \
"ds_read_b64_tr_b16 a[202:203], v210 offset:3072\n" \
"v_mov_b32 v175, v159\n" \
"v_mfma_f32_16x16x32_bf16 v[16:19], a[104:107], a[88:91], v[16:19]\n" \
"ds_read_b64_tr_b16 a[204:205], v210 offset:14336\n" \
"v_add_u32 v151, 3, v146\n" \
"v_mfma_f32_16x16x32_bf16 v[16:19], a[108:111], a[92:95], v[16:19]\n" \
"global_load_lds_dwordx4 v253, s[50:51] offset:1024\n" \
"v_or3_b32 v151, v151, v145, s86\n" \
"v_mfma_f32_16x16x32_bf16 v[20:23], a[112:115], a[88:91], v[20:23]\n" \
"ds_read_b64_tr_b16 a[206:207], v210 offset:15360\n" \
"v_ashrrev_i32 v151, 31, v151\n" \
"v_mfma_f32_16x16x32_bf16 v[20:23], a[116:119], a[92:95], v[20:23]\n" \
"s_waitcnt lgkmcnt(3)\n" \
"v_add_u32 v207, 0xffff0000, v207\n" \
"v_add_u32 v208, 0xffff0000, v208\n" \
"v_mfma_f32_16x16x32_bf16 v[28:31], a[112:115], a[192:195], v[28:31]\n" \
"v_add_u32 v205, 0xffff0000, v205\n" \
"v_add_u32 v206, 0xffff0000, v206\n" \
"v_mfma_f32_16x16x32_bf16 v[28:31], a[116:119], a[196:199], v[28:31]\n" \
"v_add_u32 v209, 0xffff0000, v209\n" \
"v_add_u32 v210, 0xffff0000, v210\n" \
"v_mfma_f32_16x16x32_bf16 v[24:27], a[104:107], a[192:195], v[24:27]\n" \
"v_and_b32 v151, 0xff800000, v151\n" \
"v_mov_b32 v144, 0x3e8293ee\n" \
"v_mfma_f32_16x16x32_bf16 v[24:27], a[108:111], a[196:199], v[24:27]\n" \
"s_add_u32 s90, s95, 117760\n" \
"v_add_u32 v223, s90, v248\n" \
"v_mfma_f32_16x16x32_bf16 v[64:67], a[96:99], a[192:195], v[64:67]\n" \
"s_waitcnt vmcnt(12) lgkmcnt(0)\n" \
"s_cmp_lg_u32 s89, 0\n" \
"s_cselect_b64 s[92:93], -1, 0\n" \
"v_mfma_f32_16x16x32_bf16 v[64:67], a[100:103], a[196:199], v[64:67]\n" \
"s_barrier\n" \
"v_mfma_f32_16x16x32_bf16 v[72:75], a[64:67], a[208:211], v[72:75]\n" \
"s_waitcnt vmcnt(11)\n" \
"ds_read_b64_tr_b16 a[96:97], v209 offset:2048\n" \
"v_cmp_ne_u32 vcc, s60, v220\n" \
"v_mfma_f32_16x16x32_bf16 v[72:75], a[68:71], a[212:215], v[72:75]\n" \
"ds_read_b64_tr_b16 a[98:99], v209 offset:3072\n" \
"v_xor_b32 v145, 0x2000, v223\n" \
"v_mfma_f32_16x16x32_bf16 v[80:83], a[72:75], a[208:211], v[80:83]\n" \
"ds_read_b128 a[64:67], v207 offset:0\n" \
"ds_read_b64_tr_b16 a[100:101], v209 offset:14336\n" \
"v_mfma_f32_16x16x32_bf16 v[80:83], a[76:79], a[212:215], v[80:83]\n" \
"s_and_b32 vcc_hi, vcc_lo, s80\n" \
"s_cbranch_scc1 Lpl2_%=\n" \
"Lpr2_%=:\n" \
"v_mfma_f32_16x16x32_bf16 v[84:87], a[72:75], a[216:219], v[84:87]\n" \
"s_add_u32 m0, s75, s95\n" \
"ds_read_b128 a[68:71], v208 offset:0\n" \
"ds_read_b64_tr_b16 a[102:103], v209 offset:15360\n" \
"v_mfma_f32_16x16x32_bf16 v[84:87], a[76:79], a[220:223], v[84:87]\n" \
"ds_read_b64_tr_b16 a[104:105], v209 offset:0\n" \
"global_load_lds_dwordx4 v222, s[36:37] offset:0 sc1\n" \
"v_mfma_f32_16x16x32_bf16 v[44:47], a[200:203], a[72:75], v[44:47]\n" \
"s_cmp_eq_u32 s89, 0\n" \
"s_cselect_b32 s16, 0, 0xff00\n" \
"ds_read_b64_tr_b16 a[106:107], v209 offset:1024\n" \
"v_mfma_f32_16x16x32_bf16 v[44:47], a[204:207], a[76:79], v[44:47]\n" \
"s_mov_b32 s17, s16\n" \
"v_add_u32 v146, 0x1c90080, v249\n" \
"v_cndmask_b32 v146, v249, v146, s[16:17]\n" \
"v_mfma_f32_16x16x32_bf16 v[52:55], a[200:203], a[80:83], v[52:55]\n" \
"ds_read_b128 a[72:75], v205 offset:0\n" \
"v_mfma_f32_16x16x32_bf16 v[52:55], a[204:207], a[84:87], v[52:55]\n" \
"ds_read_b128 a[76:79], v206 offset:0\n" \
"global_load_lds_dwordx4 v222, s[36:37] offset:1024 sc1\n" \
"v_mfma_f32_16x16x32_bf16 v[92:95], a[80:83], a[216:219], v[92:95]\n" \
"ds_read_b64_tr_b16 a[108:109], v209 offset:12288\n" \
"v_mfma_f32_16x16x32_bf16 v[92:95], a[84:87], a[220:223], v[92:95]\n" \
"v_mfma_f32_16x16x32_bf16 v[88:91], a[80:83], a[208:211], v[88:91]\n" \
"ds_read_b64_tr_b16 a[110:111], v209 offset:13312\n" \
"v_mfma_f32_16x16x32_bf16 v[88:91], a[84:87], a[212:215], v[88:91]\n" \
"ds_read_b64_tr_b16 a[112:113], v210 offset:0\n" \
"global_load_lds_dwordx4 v222, s[36:37] offset:2048 sc1\n" \
"v_mfma_f32_16x16x32_bf16 v[96:99], a[88:91], a[208:211], v[96:99]\n" \
"ds_read_b128 a[80:83], v205 offset:4096\n" \
"v_mfma_f32_16x16x32_bf16 v[96:99], a[92:95], a[212:215], v[96:99]\n" \
"v_mfma_f32_16x16x32_bf16 v[100:103], a[88:91], a[216:219], v[100:103]\n" \
"ds_read_b128 a[84:87], v206 offset:4096\n" \
"v_mfma_f32_16x16x32_bf16 v[100:103], a[92:95], a[220:223], v[100:103]\n" \
"ds_read_b64_tr_b16 a[114:115], v210 offset:1024\n" \
"global_load_lds_dwordx4 v222, s[36:37] offset:3072 sc1\n" \
"v_mfma_f32_16x16x32_bf16 v[60:63], a[200:203], a[88:91], v[60:63]\n" \
"s_add_u32 s36, s36, 0x1000\n" \
"ds_read_b64_tr_b16 a[116:117], v210 offset:12288\n" \
"v_mfma_f32_16x16x32_bf16 v[60:63], a[204:207], a[92:95], v[60:63]\n" \
"v_mfma_f32_16x16x32_bf16 v[68:71], a[200:203], a[192:195], v[68:71]\n" \
"ds_read_b128 a[88:91], v205 offset:8192\n" \
"s_add_u32 m0, m0, 0x5000\n" \
"v_mfma_f32_16x16x32_bf16 v[68:71], a[204:207], a[196:199], v[68:71]\n" \
"ds_read_b128 a[92:95], v206 offset:8192\n" \
"global_load_lds_dwordx4 v216, s[38:39] offset:-4096\n" \
"v_mfma_f32_16x16x32_bf16 v[108:111], a[192:195], a[216:219], v[108:111]\n" \
"ds_read_b64_tr_b16 a[118:119], v210 offset:13312\n" \
"v_mfma_f32_16x16x32_bf16 v[108:111], a[196:199], a[220:223], v[108:111]\n" \
"v_mfma_f32_16x16x32_bf16 v[104:107], a[192:195], a[208:211], v[104:107]\n" \
"v_mfma_f32_16x16x32_bf16 v[104:107], a[196:199], a[212:215], v[104:107]\n" \
"s_waitcnt lgkmcnt(2)\n" \
"global_load_lds_dwordx4 v217, s[38:39] offset:-3072\n" \
"v_mfma_f32_16x16x32_bf16 v[32:35], a[96:99], a[64:67], v[32:35]\n" \
"ds_read_b128 a[192:195], v205 offset:12288\n" \
"v_mfma_f32_16x16x32_bf16 v[32:35], a[100:103], a[68:71], v[32:35]\n" \
"Lpg11019_%=:\n" \
"s_cbranch_execz Lpg12712_%=\n" \
"v_mfma_f32_16x16x32_bf16 v[40:43], a[96:99], a[72:75], v[40:43]\n" \
"ds_read_b128 a[196:199], v206 offset:12288\n" \
"v_mfma_f32_16x16x32_bf16 v[40:43], a[100:103], a[76:79], v[40:43]\n" \
"ds_read_b64_tr_b16 a[208:209], v209 offset:4096\n" \
"global_load_lds_dwordx4 v218, s[38:39] offset:-2048\n" \
"v_mfma_f32_16x16x32_bf16 v[0:3], a[104:107], a[72:75], v[0:3]\n" \
"ds_read_b64_tr_b16 a[210:211], v209 offset:5120\n" \
"v_mfma_f32_16x16x32_bf16 v[0:3], a[108:111], a[76:79], v[0:3]\n" \
"s_waitcnt lgkmcnt(3)\n" \
"v_mfma_f32_16x16x32_bf16 v[4:7], a[112:115], a[72:75], v[4:7]\n" \
"ds_read_b64_tr_b16 a[212:213], v209 offset:16384\n" \
"v_mfma_f32_16x16x32_bf16 v[4:7], a[116:119], a[76:79], v[4:7]\n" \
"ds_read_b64_tr_b16 a[214:215], v209 offset:17408\n" \
"global_load_lds_dwordx4 v219, s[38:39] offset:-1024\n" \
"v_mfma_f32_16x16x32_bf16 v[12:15], a[112:115], a[80:83], v[12:15]\n" \
"s_add_u32 s38, s38, 0x20000\n" \
"s_addc_u32 s39, s39, 0\n" \
"ds_read_b64_tr_b16 a[216:217], v210 offset:4096\n" \
"v_mfma_f32_16x16x32_bf16 v[12:15], a[116:119], a[84:87], v[12:15]\n" \
"s_sub_u32 s75, s75, 32768\n" \
"s_cselect_b32 s75, 131072, s75\n" \
"v_mfma_f32_16x16x32_bf16 v[8:11], a[104:107], a[80:83], v[8:11]\n" \
"ds_read_b64_tr_b16 a[218:219], v210 offset:5120\n" \
"v_mfma_f32_16x16x32_bf16 v[8:11], a[108:111], a[84:87], v[8:11]\n" \
"ds_read_b64_tr_b16 a[220:221], v210 offset:16384\n" \
"v_mfma_f32_16x16x32_bf16 v[48:51], a[96:99], a[80:83], v[48:51]\n" \
"ds_read_b64_tr_b16 a[222:223], v210 offset:17408\n" \
"v_mfma_f32_16x16x32_bf16 v[48:51], a[100:103], a[84:87], v[48:51]\n" \
"v_mfma_f32_16x16x32_bf16 v[56:59], a[96:99], a[88:91], v[56:59]\n" \
"ds_read_b64_tr_b16 a[200:201], v210 offset:2048\n" \
"v_mfma_f32_16x16x32_bf16 v[56:59], a[100:103], a[92:95], v[56:59]\n" \
"ds_read_b64_tr_b16 a[202:203], v210 offset:3072\n" \
"v_mfma_f32_16x16x32_bf16 v[16:19], a[104:107], a[88:91], v[16:19]\n" \
"ds_read_b64_tr_b16 a[204:205], v210 offset:14336\n" \
"v_mfma_f32_16x16x32_bf16 v[16:19], a[108:111], a[92:95], v[16:19]\n" \
"v_mfma_f32_16x16x32_bf16 v[20:23], a[112:115], a[88:91], v[20:23]\n" \
"ds_read_b64_tr_b16 a[206:207], v210 offset:15360\n" \
"v_mfma_f32_16x16x32_bf16 v[20:23], a[116:119], a[92:95], v[20:23]\n" \
"s_waitcnt lgkmcnt(3)\n" \
"v_add_u32 v207, 0x1d000, v207\n" \
"v_add_u32 v208, 0x1d000, v208\n" \
"v_mfma_f32_16x16x32_bf16 v[28:31], a[112:115], a[192:195], v[28:31]\n" \
"v_add_u32 v205, 0x1d000, v205\n" \
"v_add_u32 v206, 0x1d000, v206\n" \
"v_mfma_f32_16x16x32_bf16 v[28:31], a[116:119], a[196:199], v[28:31]\n" \
"v_add_u32 v209, 0x1d000, v209\n" \
"v_add_u32 v210, 0x1d000, v210\n" \
"v_mfma_f32_16x16x32_bf16 v[24:27], a[104:107], a[192:195], v[24:27]\n" \
"v_mfma_f32_16x16x32_bf16 v[24:27], a[108:111], a[196:199], v[24:27]\n" \
"v_mfma_f32_16x16x32_bf16 v[64:67], a[96:99], a[192:195], v[64:67]\n" \
"s_waitcnt vmcnt(8) lgkmcnt(0)\n" \
"v_mfma_f32_16x16x32_bf16 v[64:67], a[100:103], a[196:199], v[64:67]\n" \
"s_barrier\n" \
"v_mfma_f32_16x16x32_bf16 v[72:75], a[64:67], a[208:211], v[72:75]\n" \
"ds_read_b64_tr_b16 a[96:97], v209 offset:2048\n" \
"v_mfma_f32_16x16x32_bf16 v[72:75], a[68:71], a[212:215], v[72:75]\n" \
"ds_read_b64_tr_b16 a[98:99], v209 offset:3072\n" \
"v_mfma_f32_16x16x32_bf16 v[80:83], a[72:75], a[208:211], v[80:83]\n" \
"s_add_u32 m0, s75, s95\n" \
"ds_read_b128 a[64:67], v207 offset:0\n" \
"ds_read_b64_tr_b16 a[100:101], v209 offset:14336\n" \
"v_mfma_f32_16x16x32_bf16 v[80:83], a[76:79], a[212:215], v[80:83]\n" \
"global_load_lds_dwordx4 v222, s[36:37] offset:0 sc1\n" \
"v_mfma_f32_16x16x32_bf16 v[84:87], a[72:75], a[216:219], v[84:87]\n" \
"ds_read_b128 a[68:71], v208 offset:0\n" \
"ds_read_b64_tr_b16 a[102:103], v209 offset:15360\n" \
"v_mfma_f32_16x16x32_bf16 v[84:87], a[76:79], a[220:223], v[84:87]\n" \
"ds_read_b64_tr_b16 a[104:105], v209 offset:0\n" \
"v_mfma_f32_16x16x32_bf16 v[44:47], a[200:203], a[72:75], v[44:47]\n" \
"ds_read_b64_tr_b16 a[106:107], v209 offset:1024\n" \
"v_mfma_f32_16x16x32_bf16 v[44:47], a[204:207], a[76:79], v[44:47]\n" \
"global_load_lds_dwordx4 v222, s[36:37] offset:1024 sc1\n" \
"v_mfma_f32_16x16x32_bf16 v[52:55], a[200:203], a[80:83], v[52:55]\n" \
"ds_read_b128 a[72:75], v205 offset:0\n" \
"v_mfma_f32_16x16x32_bf16 v[52:55], a[204:207], a[84:87], v[52:55]\n" \
"ds_read_b128 a[76:79], v206 offset:0\n" \
"v_mfma_f32_16x16x32_bf16 v[92:95], a[80:83], a[216:219], v[92:95]\n" \
"ds_read_b64_tr_b16 a[108:109], v209 offset:12288\n" \
"v_mfma_f32_16x16x32_bf16 v[92:95], a[84:87], a[220:223], v[92:95]\n" \
"global_load_lds_dwordx4 v222, s[36:37] offset:2048 sc1\n" \
"v_mfma_f32_16x16x32_bf16 v[88:91], a[80:83], a[208:211], v[88:91]\n" \
"ds_read_b64_tr_b16 a[110:111], v209 offset:13312\n" \
"v_mfma_f32_16x16x32_bf16 v[88:91], a[84:87], a[212:215], v[88:91]\n" \
"ds_read_b64_tr_b16 a[112:113], v210 offset:0\n" \
"v_mfma_f32_16x16x32_bf16 v[96:99], a[88:91], a[208:211], v[96:99]\n" \
"ds_read_b128 a[80:83], v205 offset:4096\n" \
"v_mfma_f32_16x16x32_bf16 v[96:99], a[92:95], a[212:215], v[96:99]\n" \
"global_load_lds_dwordx4 v222, s[36:37] offset:3072 sc1\n" \
"v_mfma_f32_16x16x32_bf16 v[100:103], a[88:91], a[216:219], v[100:103]\n" \
"ds_read_b128 a[84:87], v206 offset:4096\n" \
"s_add_u32 s36, s36, 0x1000\n" \
"v_mfma_f32_16x16x32_bf16 v[100:103], a[92:95], a[220:223], v[100:103]\n" \
"ds_read_b64_tr_b16 a[114:115], v210 offset:1024\n" \
"v_mfma_f32_16x16x32_bf16 v[60:63], a[200:203], a[88:91], v[60:63]\n" \
"s_add_u32 m0, m0, 0x5000\n" \
"ds_read_b64_tr_b16 a[116:117], v210 offset:12288\n" \
"v_mfma_f32_16x16x32_bf16 v[60:63], a[204:207], a[92:95], v[60:63]\n" \
"global_load_lds_dwordx4 v216, s[38:39] offset:-4096\n" \
"v_mfma_f32_16x16x32_bf16 v[68:71], a[200:203], a[192:195], v[68:71]\n" \
"ds_read_b128 a[88:91], v205 offset:8192\n" \
"v_mfma_f32_16x16x32_bf16 v[68:71], a[204:207], a[196:199], v[68:71]\n" \
"ds_read_b128 a[92:95], v206 offset:8192\n" \
"v_mfma_f32_16x16x32_bf16 v[108:111], a[192:195], a[216:219], v[108:111]\n" \
"ds_read_b64_tr_b16 a[118:119], v210 offset:13312\n" \
"v_mfma_f32_16x16x32_bf16 v[108:111], a[196:199], a[220:223], v[108:111]\n" \
"global_load_lds_dwordx4 v217, s[38:39] offset:-3072\n" \
"v_mfma_f32_16x16x32_bf16 v[104:107], a[192:195], a[208:211], v[104:107]\n" \
"v_mfma_f32_16x16x32_bf16 v[104:107], a[196:199], a[212:215], v[104:107]\n" \
"s_waitcnt lgkmcnt(2)\n" \
"v_mfma_f32_16x16x32_bf16 v[32:35], a[96:99], a[64:67], v[32:35]\n" \
"ds_read_b128 a[192:195], v205 offset:12288\n" \
"v_mfma_f32_16x16x32_bf16 v[32:35], a[100:103], a[68:71], v[32:35]\n" \
"global_load_lds_dwordx4 v218, s[38:39] offset:-2048\n" \
"v_mfma_f32_16x16x32_bf16 v[40:43], a[96:99], a[72:75], v[40:43]\n" \
"ds_read_b128 a[196:199], v206 offset:12288\n" \
"v_mfma_f32_16x16x32_bf16 v[40:43], a[100:103], a[76:79], v[40:43]\n" \
"ds_read_b64_tr_b16 a[208:209], v209 offset:4096\n" \
"v_mfma_f32_16x16x32_bf16 v[0:3], a[104:107], a[72:75], v[0:3]\n" \
"ds_read_b64_tr_b16 a[210:211], v209 offset:5120\n" \
"v_mfma_f32_16x16x32_bf16 v[0:3], a[108:111], a[76:79], v[0:3]\n" \
"s_waitcnt lgkmcnt(3)\n" \
"global_load_lds_dwordx4 v219, s[38:39] offset:-1024\n" \
"v_mfma_f32_16x16x32_bf16 v[4:7], a[112:115], a[72:75], v[4:7]\n" \
"s_add_u32 s38, s38, 0x20000\n" \
"s_addc_u32 s39, s39, 0\n" \
"ds_read_b64_tr_b16 a[212:213], v209 offset:16384\n" \
"v_mfma_f32_16x16x32_bf16 v[4:7], a[116:119], a[76:79], v[4:7]\n" \
"s_sub_u32 s75, s75, 32768\n" \
"s_cselect_b32 s75, 131072, s75\n" \
"ds_read_b64_tr_b16 a[214:215], v209 offset:17408\n" \
"v_mfma_f32_16x16x32_bf16 v[48:51], a[96:99], a[80:83], v[48:51]\n" \
"s_add_u32 m0, s75, s95\n" \
"ds_read_b64_tr_b16 a[216:217], v210 offset:4096\n" \
"v_mfma_f32_16x16x32_bf16 v[48:51], a[100:103], a[84:87], v[48:51]\n" \
"global_load_lds_dwordx4 v222, s[36:37] offset:0 sc1\n" \
"v_mfma_f32_16x16x32_bf16 v[8:11], a[104:107], a[80:83], v[8:11]\n" \
"ds_read_b64_tr_b16 a[218:219], v210 offset:5120\n" \
"v_cvt_pk_bf16_f32 v112, v0, v1\n" \
"v_mfma_f32_16x16x32_bf16 v[8:11], a[108:111], a[84:87], v[8:11]\n" \
"ds_read_b64_tr_b16 a[220:221], v210 offset:16384\n" \
"v_cvt_pk_bf16_f32 v113, v2, v3\n" \
"v_mfma_f32_16x16x32_bf16 v[12:15], a[112:115], a[80:83], v[12:15]\n" \
"ds_read_b64_tr_b16 a[222:223], v210 offset:17408\n" \
"v_cvt_pk_bf16_f32 v114, v4, v5\n" \
"v_mfma_f32_16x16x32_bf16 v[12:15], a[116:119], a[84:87], v[12:15]\n" \
"global_load_lds_dwordx4 v222, s[36:37] offset:1024 sc1\n" \
"v_cvt_pk_bf16_f32 v115, v6, v7\n" \
"v_mfma_f32_16x16x32_bf16 v[56:59], a[96:99], a[88:91], v[56:59]\n" \
"ds_read_b64_tr_b16 a[200:201], v210 offset:2048\n" \
"v_mfma_f32_16x16x32_bf16 v[56:59], a[100:103], a[92:95], v[56:59]\n" \
"ds_read_b64_tr_b16 a[202:203], v210 offset:3072\n" \
"v_mfma_f32_16x16x32_bf16 v[16:19], a[104:107], a[88:91], v[16:19]\n" \
"ds_read_b64_tr_b16 a[204:205], v210 offset:14336\n" \
"v_cvt_pk_bf16_f32 v116, v8, v9\n" \
"v_mfma_f32_16x16x32_bf16 v[16:19], a[108:111], a[92:95], v[16:19]\n" \
"global_load_lds_dwordx4 v222, s[36:37] offset:2048 sc1\n" \
"v_cvt_pk_bf16_f32 v117, v10, v11\n" \
"v_mfma_f32_16x16x32_bf16 v[20:23], a[112:115], a[88:91], v[20:23]\n" \
"ds_read_b64_tr_b16 a[206:207], v210 offset:15360\n" \
"v_cvt_pk_bf16_f32 v118, v12, v13\n" \
"v_mfma_f32_16x16x32_bf16 v[20:23], a[116:119], a[92:95], v[20:23]\n" \
"s_waitcnt lgkmcnt(3)\n" \
"v_cvt_pk_bf16_f32 v119, v14, v15\n" \
"v_mfma_f32_16x16x32_bf16 v[64:67], a[96:99], a[192:195], v[64:67]\n" \
"v_mfma_f32_16x16x32_bf16 v[64:67], a[100:103], a[196:199], v[64:67]\n" \
"global_load_lds_dwordx4 v222, s[36:37] offset:3072 sc1\n" \
"v_mfma_f32_16x16x32_bf16 v[24:27], a[104:107], a[192:195], v[24:27]\n" \
"v_cvt_pk_bf16_f32 v120, v16, v17\n" \
"v_cvt_pk_bf16_f32 v121, v18, v19\n" \
"v_mfma_f32_16x16x32_bf16 v[24:27], a[108:111], a[196:199], v[24:27]\n" \
"v_cvt_pk_bf16_f32 v122, v20, v21\n" \
"v_cvt_pk_bf16_f32 v123, v22, v23\n" \
"v_mfma_f32_16x16x32_bf16 v[28:31], a[112:115], a[192:195], v[28:31]\n" \
"s_waitcnt vmcnt(12) lgkmcnt(0)\n" \
"v_mfma_f32_16x16x32_bf16 v[28:31], a[116:119], a[196:199], v[28:31]\n" \
"s_barrier\n" \
"v_mfma_f32_16x16x32_bf16 v[72:75], a[64:67], a[208:211], v[72:75]\n" \
"ds_read_b64_tr_b16 a[32:33], v214 offset:16384\n" \
"v_mfma_f32_16x16x32_bf16 v[72:75], a[68:71], a[212:215], v[72:75]\n" \
"v_mfma_f32_16x16x32_bf16 v[44:47], a[200:203], a[72:75], v[44:47]\n" \
"s_add_u32 s36, s36, 0x1000\n" \
"s_add_u32 m0, m0, 0x5000\n" \
"ds_read_b64_tr_b16 a[34:35], v214 offset:18432\n" \
"v_mfma_f32_16x16x32_bf16 v[44:47], a[204:207], a[76:79], v[44:47]\n" \
"ds_read_b64_tr_b16 a[36:37], v214 offset:24576\n" \
"global_load_lds_dwordx4 v216, s[38:39] offset:-4096\n" \
"v_mfma_f32_16x16x32_bf16 v[80:83], a[72:75], a[208:211], v[80:83]\n" \
"v_cvt_pk_bf16_f32 v124, v24, v25\n" \
"v_cvt_pk_bf16_f32 v125, v26, v27\n" \
"v_mfma_f32_16x16x32_bf16 v[80:83], a[76:79], a[212:215], v[80:83]\n" \
"ds_read_b64_tr_b16 a[38:39], v214 offset:26624\n" \
"v_cvt_pk_bf16_f32 v126, v28, v29\n" \
"v_mfma_f32_16x16x32_bf16 v[84:87], a[72:75], a[216:219], v[84:87]\n" \
"ds_read_b128 a[0:3], v213 offset:0\n" \
"v_cvt_pk_bf16_f32 v127, v30, v31\n" \
"v_mfma_f32_16x16x32_bf16 v[84:87], a[76:79], a[220:223], v[84:87]\n" \
"global_load_lds_dwordx4 v217, s[38:39] offset:-3072\n" \
"v_cvt_pk_bf16_f32 v128, v32, v33\n" \
"v_mfma_f32_16x16x32_bf16 v[52:55], a[200:203], a[80:83], v[52:55]\n" \
"ds_read_b128 a[4:7], v213 offset:2048\n" \
"v_cvt_pk_bf16_f32 v129, v34, v35\n" \
"v_mfma_f32_16x16x32_bf16 v[52:55], a[204:207], a[84:87], v[52:55]\n" \
"ds_read_b64_tr_b16 a[40:41], v215 offset:16384\n" \
"v_cvt_pk_bf16_f32 v130, v72, v73\n" \
"v_mfma_f32_16x16x32_bf16 v[88:91], a[80:83], a[208:211], v[88:91]\n" \
"v_cvt_pk_bf16_f32 v131, v74, v75\n" \
"v_mfma_f32_16x16x32_bf16 v[88:91], a[84:87], a[212:215], v[88:91]\n" \
"ds_read_b64_tr_b16 a[42:43], v215 offset:18432\n" \
"global_load_lds_dwordx4 v218, s[38:39] offset:-2048\n" \
"v_mfma_f32_16x16x32_bf16 v[92:95], a[80:83], a[216:219], v[92:95]\n" \
"ds_read_b64_tr_b16 a[44:45], v215 offset:24576\n" \
"v_mfma_f32_16x16x32_bf16 v[92:95], a[84:87], a[220:223], v[92:95]\n" \
"ds_write_b128 v145, v[128:131]\n" \
"v_cvt_pk_bf16_f32 v132, v40, v41\n" \
"v_mfma_f32_16x16x32_bf16 v[60:63], a[200:203], a[88:91], v[60:63]\n" \
"ds_read_b64_tr_b16 a[46:47], v215 offset:26624\n" \
"v_cvt_pk_bf16_f32 v133, v42, v43\n" \
"v_mfma_f32_16x16x32_bf16 v[60:63], a[204:207], a[92:95], v[60:63]\n" \
"ds_read_b128 a[8:11], v213 offset:256\n" \
"global_load_lds_dwordx4 v219, s[38:39] offset:-1024\n" \
"v_mfma_f32_16x16x32_bf16 v[96:99], a[88:91], a[208:211], v[96:99]\n" \
"s_add_u32 s38, s38, 0x20000\n" \
"s_addc_u32 s39, s39, 0\n" \
"v_cvt_pk_bf16_f32 v134, v44, v45\n" \
"v_mfma_f32_16x16x32_bf16 v[96:99], a[92:95], a[212:215], v[96:99]\n" \
"s_sub_u32 s75, s75, 32768\n" \
"s_cselect_b32 s75, 131072, s75\n" \
"ds_read_b128 a[12:15], v213 offset:2304\n" \
"v_mfma_f32_16x16x32_bf16 v[100:103], a[88:91], a[216:219], v[100:103]\n" \
"s_add_u32 m0, s75, s95\n" \
"ds_read_b128 a[16:19], v213 offset:4096\n" \
"v_cvt_pk_bf16_f32 v135, v46, v47\n" \
"v_mfma_f32_16x16x32_bf16 v[100:103], a[92:95], a[220:223], v[100:103]\n" \
"global_load_lds_dwordx4 v222, s[36:37] offset:0 sc1\n" \
"v_cvt_pk_bf16_f32 v136, v48, v49\n" \
"v_mfma_f32_16x16x32_bf16 v[68:71], a[200:203], a[192:195], v[68:71]\n" \
"ds_read_b128 a[20:23], v213 offset:6144\n" \
"v_cvt_pk_bf16_f32 v137, v50, v51\n" \
"v_mfma_f32_16x16x32_bf16 v[68:71], a[204:207], a[196:199], v[68:71]\n" \
"v_cvt_pk_bf16_f32 v138, v52, v53\n" \
"v_cvt_pk_bf16_f32 v139, v54, v55\n" \
"v_mfma_f32_16x16x32_bf16 v[104:107], a[192:195], a[208:211], v[104:107]\n" \
"v_cvt_pk_bf16_f32 v140, v56, v57\n" \
"v_cvt_pk_bf16_f32 v141, v58, v59\n" \
"v_mfma_f32_16x16x32_bf16 v[104:107], a[196:199], a[212:215], v[104:107]\n" \
"global_load_lds_dwordx4 v222, s[36:37] offset:1024 sc1\n" \
"v_cvt_pk_bf16_f32 v142, v60, v61\n" \
"v_mfma_f32_16x16x32_bf16 v[108:111], a[192:195], a[216:219], v[108:111]\n" \
"v_cvt_pk_bf16_f32 v143, v62, v63\n" \
"v_mfma_f32_16x16x32_bf16 v[108:111], a[196:199], a[220:223], v[108:111]\n" \
"s_waitcnt lgkmcnt(0)\n" \
"v_mfma_f32_16x16x32_bf16 v[0:3], a[32:35], a[0:3], 0\n" \
"ds_read_b128 a[24:27], v213 offset:4352\n" \
"v_mfma_f32_16x16x32_bf16 v[0:3], a[36:39], a[4:7], v[0:3]\n" \
"ds_read_b128 a[28:31], v213 offset:6400\n" \
"global_load_lds_dwordx4 v222, s[36:37] offset:2048 sc1\n" \
"v_mfma_f32_16x16x32_bf16 v[4:7], a[40:43], a[0:3], 0\n" \
"s_sub_u32 s74, s74, 32768\n" \
"s_cselect_b32 s74, 131072, s74\n" \
"ds_read_b64_tr_b16 a[48:49], v214 offset:16896\n" \
"v_mfma_f32_16x16x32_bf16 v[4:7], a[44:47], a[4:7], v[4:7]\n" \
"ds_read_b64_tr_b16 a[50:51], v214 offset:18944\n" \
"v_cvt_pk_bf16_f32 v64, v64, v65\n" \
"v_mfma_f32_16x16x32_bf16 v[20:23], a[40:43], a[8:11], 0\n" \
"ds_read_b64_tr_b16 a[52:53], v214 offset:25088\n" \
"global_load_lds_dwordx4 v222, s[36:37] offset:3072 sc1\n" \
"v_mfma_f32_16x16x32_bf16 v[20:23], a[44:47], a[12:15], v[20:23]\n" \
"s_add_u32 s36, s36, 0x1000\n" \
"ds_read_b64_tr_b16 a[54:55], v214 offset:27136\n" \
"v_cvt_pk_bf16_f32 v65, v66, v67\n" \
"v_mfma_f32_16x16x32_bf16 v[16:19], a[32:35], a[8:11], 0\n" \
"s_add_u32 m0, m0, 0x5000\n" \
"ds_read_b64_tr_b16 a[56:57], v215 offset:16896\n" \
"v_cvt_pk_bf16_f32 v66, v68, v69\n" \
"v_mfma_f32_16x16x32_bf16 v[16:19], a[36:39], a[12:15], v[16:19]\n" \
"ds_read_b64_tr_b16 a[58:59], v215 offset:18944\n" \
"global_load_lds_dwordx4 v216, s[38:39] offset:-4096\n" \
"v_mfma_f32_16x16x32_bf16 v[32:35], a[32:35], a[16:19], 0\n" \
"ds_read_b64_tr_b16 a[60:61], v215 offset:25088\n" \
"v_cvt_pk_bf16_f32 v67, v70, v71\n" \
"v_mfma_f32_16x16x32_bf16 v[32:35], a[36:39], a[20:23], v[32:35]\n" \
"ds_read_b64_tr_b16 a[62:63], v215 offset:27136\n" \
"v_cvt_pk_bf16_f32 v68, v72, v73\n" \
"v_mfma_f32_16x16x32_bf16 v[36:39], a[40:43], a[16:19], 0\n" \
"global_load_lds_dwordx4 v217, s[38:39] offset:-3072\n" \
"v_add_u32 v213, s74, v211\n" \
"v_mfma_f32_16x16x32_bf16 v[36:39], a[44:47], a[20:23], v[36:39]\n" \
"s_waitcnt lgkmcnt(3)\n" \
"v_add_u32 v214, s74, v212\n" \
"v_xor_b32 v215, 32, v214\n" \
"v_mfma_f32_16x16x32_bf16 v[52:55], a[40:43], a[24:27], 0\n" \
"v_cvt_pk_bf16_f32 v69, v74, v75\n" \
"v_cvt_pk_bf16_f32 v70, v80, v81\n" \
"v_mfma_f32_16x16x32_bf16 v[52:55], a[44:47], a[28:31], v[52:55]\n" \
"global_load_lds_dwordx4 v218, s[38:39] offset:-2048\n" \
"v_cvt_pk_bf16_f32 v71, v82, v83\n" \
"v_mfma_f32_16x16x32_bf16 v[48:51], a[32:35], a[24:27], 0\n" \
"s_waitcnt vmcnt(15) lgkmcnt(0)\n" \
"global_load_lds_dwordx4 v219, s[38:39] offset:-1024\n" \
"v_cvt_pk_bf16_f32 v194, v84, v85\n" \
"v_mfma_f32_16x16x32_bf16 v[48:51], a[36:39], a[28:31], v[48:51]\n" \
"s_barrier\n" \
"v_cvt_pk_bf16_f32 v195, v86, v87\n" \
"v_mfma_f32_16x16x32_bf16 v[8:11], a[48:51], a[0:3], 0\n" \
"ds_read_b64 v[130:131], v223\n" \
"ds_read_b64_tr_b16 a[32:33], v214 offset:16384\n" \
"v_mfma_f32_16x16x32_bf16 v[8:11], a[52:55], a[4:7], v[8:11]\n" \
"ds_read_b64 v[192:193], v223 offset:8\n" \
"ds_read_b64_tr_b16 a[34:35], v214 offset:18432\n" \
"v_mfma_f32_16x16x32_bf16 v[12:15], a[56:59], a[0:3], 0\n" \
"s_add_u32 s38, s38, 0x20000\n" \
"s_addc_u32 s39, s39, 0\n" \
"ds_read_b64_tr_b16 a[36:37], v214 offset:24576\n" \
"v_mfma_f32_16x16x32_bf16 v[12:15], a[60:63], a[4:7], v[12:15]\n" \
"s_sub_u32 s75, s75, 32768\n" \
"s_cselect_b32 s75, 131072, s75\n" \
"ds_read_b128 a[0:3], v213 offset:0\n" \
"v_mfma_f32_16x16x32_bf16 v[28:31], a[56:59], a[8:11], 0\n" \
"s_add_u32 m0, s75, s95\n" \
"ds_read_b128 a[4:7], v213 offset:2048\n" \
"v_cvt_pk_bf16_f32 v72, v88, v89\n" \
"v_mfma_f32_16x16x32_bf16 v[28:31], a[60:63], a[12:15], v[28:31]\n" \
"ds_read_b64_tr_b16 a[38:39], v214 offset:26624\n" \
"global_load_lds_dwordx4 v222, s[36:37] offset:0 sc1\n" \
"v_mfma_f32_16x16x32_bf16 v[24:27], a[48:51], a[8:11], 0\n" \
"ds_read_b64_tr_b16 a[40:41], v215 offset:16384\n" \
"v_cvt_pk_bf16_f32 v73, v90, v91\n" \
"v_mfma_f32_16x16x32_bf16 v[24:27], a[52:55], a[12:15], v[24:27]\n" \
"s_waitcnt lgkmcnt(4)\n" \
"ds_read_b128 a[8:11], v213 offset:256\n" \
"global_load_lds_dwordx4 v222, s[36:37] offset:1024 sc1\n" \
"v_mfma_f32_16x16x32_bf16 v[40:43], a[48:51], a[16:19], 0\n" \
"ds_read_b128 a[12:15], v213 offset:2304\n" \
"v_cvt_pk_bf16_f32 v196, v92, v93\n" \
"v_mfma_f32_16x16x32_bf16 v[40:43], a[52:55], a[20:23], v[40:43]\n" \
"ds_read_b64_tr_b16 a[42:43], v215 offset:18432\n" \
"v_cvt_pk_bf16_f32 v197, v94, v95\n" \
"v_mfma_f32_16x16x32_bf16 v[44:47], a[56:59], a[16:19], 0\n" \
"ds_read_b64_tr_b16 a[44:45], v215 offset:24576\n" \
"global_load_lds_dwordx4 v222, s[36:37] offset:2048 sc1\n" \
"v_mfma_f32_16x16x32_bf16 v[44:47], a[60:63], a[20:23], v[44:47]\n" \
"ds_read_b128 a[16:19], v213 offset:4096\n" \
"v_cvt_pk_bf16_f32 v74, v96, v97\n" \
"v_mfma_f32_16x16x32_bf16 v[60:63], a[56:59], a[24:27], 0\n" \
"ds_read_b128 a[20:23], v213 offset:6144\n" \
"global_load_lds_dwordx4 v222, s[36:37] offset:3072 sc1\n" \
"v_mfma_f32_16x16x32_bf16 v[60:63], a[60:63], a[28:31], v[60:63]\n" \
"ds_read_b64_tr_b16 a[46:47], v215 offset:26624\n" \
"v_cvt_pk_bf16_f32 v75, v98, v99\n" \
"v_mfma_f32_16x16x32_bf16 v[56:59], a[48:51], a[24:27], 0\n" \
"v_cvt_pk_bf16_f32 v198, v100, v101\n" \
"v_cvt_pk_bf16_f32 v199, v102, v103\n" \
"v_mfma_f32_16x16x32_bf16 v[56:59], a[52:55], a[28:31], v[56:59]\n" \
"s_waitcnt lgkmcnt(3)\n" \
"v_cvt_pk_bf16_f32 v76, v104, v105\n" \
"v_cvt_pk_bf16_f32 v77, v106, v107\n" \
"v_mfma_f32_16x16x32_bf16 v[0:3], a[32:35], a[0:3], v[0:3]\n" \
"v_cvt_pk_bf16_f32 v200, v108, v109\n" \
"v_cvt_pk_bf16_f32 v201, v110, v111\n" \
"v_mfma_f32_16x16x32_bf16 v[176:179], v[64:67], v[124:127], v[176:179]\n" \
"v_mfma_f32_16x16x32_bf16 v[172:175], v[140:143], v[124:127], v[172:175]\n" \
"s_add_u32 s36, s36, 0x1000\n" \
"ds_read_b128 a[24:27], v213 offset:4352\n" \
"s_add_u32 m0, m0, 0x5000\n" \
"v_mfma_f32_16x16x32_bf16 v[0:3], a[36:39], a[4:7], v[0:3]\n" \
"s_waitcnt lgkmcnt(1)\n" \
"ds_read_b128 a[28:31], v213 offset:6400\n" \
"global_load_lds_dwordx4 v216, s[38:39] offset:-4096\n" \
"v_mfma_f32_16x16x32_bf16 v[168:171], v[140:143], v[120:123], v[168:171]\n" \
"v_mfma_f32_16x16x32_bf16 v[164:167], v[136:139], v[120:123], v[164:167]\n" \
"v_mfma_f32_16x16x32_bf16 v[160:163], v[136:139], v[116:119], v[160:163]\n" \
"v_mfma_f32_16x16x32_bf16 v[156:159], v[132:135], v[116:119], v[156:159]\n" \
"v_mfma_f32_16x16x32_bf16 v[152:155], v[132:135], v[112:115], v[152:155]\n" \
"v_mfma_f32_16x16x32_bf16 v[148:151], v[128:131], v[112:115], v[148:151]\n" \
"v_mfma_f32_16x16x32_bf16 v[4:7], a[40:43], a[0:3], v[4:7]\n" \
"s_sub_u32 s74, s74, 32768\n" \
"s_cselect_b32 s74, 131072, s74\n" \
"ds_read_b64_tr_b16 a[48:49], v214 offset:16896\n" \
"v_mfma_f32_16x16x32_bf16 v[4:7], a[44:47], a[4:7], v[4:7]\n" \
"ds_read_b64_tr_b16 a[50:51], v214 offset:18944\n" \
"v_max3_f32 v183, v172, v173, v174\n" \
"v_max3_f32 v182, v164, v165, v166\n" \
"v_max3_f32 v181, v156, v157, v158\n" \
"v_max3_f32 v180, v148, v149, v150\n" \
"v_max3_f32 v183, v183, v175, v176\n" \
"v_max3_f32 v182, v182, v167, v168\n" \
"v_mfma_f32_16x16x32_bf16 v[20:23], a[40:43], a[8:11], v[20:23]\n" \
"ds_read_b64_tr_b16 a[52:53], v214 offset:25088\n" \
"global_load_lds_dwordx4 v217, s[38:39] offset:-3072\n" \
"v_mfma_f32_16x16x32_bf16 v[20:23], a[44:47], a[12:15], v[20:23]\n" \
"ds_read_b64_tr_b16 a[54:55], v214 offset:27136\n" \
"v_max3_f32 v181, v181, v159, v160\n" \
"v_max3_f32 v180, v180, v151, v152\n" \
"v_max3_f32 v183, v183, v177, v178\n" \
"v_max3_f32 v182, v182, v169, v170\n" \
"v_max3_f32 v181, v181, v161, v162\n" \
"v_max3_f32 v180, v180, v153, v154\n" \
"v_mfma_f32_16x16x32_bf16 v[16:19], a[32:35], a[8:11], v[16:19]\n" \
"ds_read_b64_tr_b16 a[56:57], v215 offset:16896\n" \
"v_max_f32 v183, v183, v179\n" \
"v_mfma_f32_16x16x32_bf16 v[16:19], a[36:39], a[12:15], v[16:19]\n" \
"ds_read_b64_tr_b16 a[58:59], v215 offset:18944\n" \
"global_load_lds_dwordx4 v218, s[38:39] offset:-2048\n" \
"v_max_f32 v182, v182, v171\n" \
"v_max_f32 v181, v181, v163\n" \
"v_max_f32 v180, v180, v155\n" \
"v_mov_b32 v187, v183\n" \
"v_mov_b32 v186, v182\n" \
"v_mov_b32 v185, v181\n" \
"v_mfma_f32_16x16x32_bf16 v[32:35], a[32:35], a[16:19], v[32:35]\n" \
"ds_read_b64_tr_b16 a[60:61], v215 offset:25088\n" \
"v_mov_b32 v184, v180\n" \
"v_mfma_f32_16x16x32_bf16 v[32:35], a[36:39], a[20:23], v[32:35]\n" \
"ds_read_b64_tr_b16 a[62:63], v215 offset:27136\n" \
"v_permlane16_swap_b32 v187, v183\n" \
"v_permlane16_swap_b32 v186, v182\n" \
"v_permlane16_swap_b32 v185, v181\n" \
"v_permlane16_swap_b32 v184, v180\n" \
"v_max_f32 v183, v183, v187\n" \
"v_max_f32 v182, v182, v186\n" \
"v_mfma_f32_16x16x32_bf16 v[36:39], a[40:43], a[16:19], v[36:39]\n" \
"global_load_lds_dwordx4 v219, s[38:39] offset:-1024\n" \
"v_add_u32 v213, s74, v211\n" \
"v_mfma_f32_16x16x32_bf16 v[36:39], a[44:47], a[20:23], v[36:39]\n" \
"s_waitcnt lgkmcnt(3)\n" \
"v_max_f32 v181, v181, v185\n" \
"v_max_f32 v180, v180, v184\n" \
"v_mov_b32 v187, v183\n" \
"v_mov_b32 v186, v182\n" \
"v_mov_b32 v185, v181\n" \
"v_mov_b32 v184, v180\n" \
"s_add_u32 s38, s38, 0x20000\n" \
"s_addc_u32 s39, s39, 0\n" \
"v_mfma_f32_16x16x32_bf16 v[52:55], a[40:43], a[24:27], v[52:55]\n" \
"s_sub_u32 s75, s75, 32768\n" \
"s_cselect_b32 s75, 131072, s75\n" \
"s_add_u32 m0, s75, s95\n" \
"v_mfma_f32_16x16x32_bf16 v[52:55], a[44:47], a[28:31], v[52:55]\n" \
"global_load_lds_dwordx4 v222, s[36:37] offset:0 sc1\n" \
"v_add_u32 v214, s74, v212\n" \
"v_permlane32_swap_b32 v187, v183\n" \
"v_permlane32_swap_b32 v186, v182\n" \
"v_permlane32_swap_b32 v185, v181\n" \
"v_permlane32_swap_b32 v184, v180\n" \
"v_max_f32 v183, v183, v187\n" \
"v_max_f32 v182, v182, v186\n" \
"v_mfma_f32_16x16x32_bf16 v[48:51], a[32:35], a[24:27], v[48:51]\n" \
"s_waitcnt vmcnt(17) lgkmcnt(0)\n" \
"v_xor_b32 v215, 32, v214\n" \
"v_max_f32 v181, v181, v185\n" \
"v_mfma_f32_16x16x32_bf16 v[48:51], a[36:39], a[28:31], v[48:51]\n" \
"s_barrier\n" \
"v_max_f32 v180, v180, v184\n" \
"v_mul_f32 v183, v183, v144\n" \
"v_mul_f32 v182, v182, v144\n" \
"v_mul_f32 v181, v181, v144\n" \
"v_mul_f32 v180, v180, v144\n" \
"v_fma_f32 v172, v172, v144, -v183\n" \
"v_mfma_f32_16x16x32_bf16 v[8:11], a[48:51], a[0:3], v[8:11]\n" \
"ds_read_b64_tr_b16 a[32:33], v214 offset:16384\n" \
"v_fma_f32 v164, v164, v144, -v182\n" \
"v_mfma_f32_16x16x32_bf16 v[8:11], a[52:55], a[4:7], v[8:11]\n" \
"ds_read_b64_tr_b16 a[34:35], v214 offset:18432\n" \
"global_load_lds_dwordx4 v222, s[36:37] offset:1024 sc1\n" \
"v_fma_f32 v156, v156, v144, -v181\n" \
"v_exp_f32 v172, v172\n" \
"v_fma_f32 v148, v148, v144, -v180\n" \
"v_exp_f32 v164, v164\n" \
"v_fma_f32 v173, v173, v144, -v183\n" \
"v_exp_f32 v156, v156\n" \
"v_mfma_f32_16x16x32_bf16 v[12:15], a[56:59], a[0:3], v[12:15]\n" \
"ds_read_b64_tr_b16 a[36:37], v214 offset:24576\n" \
"v_fma_f32 v165, v165, v144, -v182\n" \
"v_mfma_f32_16x16x32_bf16 v[12:15], a[60:63], a[4:7], v[12:15]\n" \
"ds_read_b128 a[0:3], v213 offset:0\n" \
"v_exp_f32 v148, v148\n" \
"v_fma_f32 v157, v157, v144, -v181\n" \
"v_exp_f32 v173, v173\n" \
"v_fma_f32 v149, v149, v144, -v180\n" \
"v_exp_f32 v165, v165\n" \
"v_mfma_f32_16x16x32_bf16 v[28:31], a[56:59], a[8:11], v[28:31]\n" \
"ds_read_b128 a[4:7], v213 offset:2048\n" \
"global_load_lds_dwordx4 v222, s[36:37] offset:2048 sc1\n" \
"v_mfma_f32_16x16x32_bf16 v[28:31], a[60:63], a[12:15], v[28:31]\n" \
"ds_read_b64_tr_b16 a[38:39], v214 offset:26624\n" \
"v_fma_f32 v174, v174, v144, -v183\n" \
"v_exp_f32 v157, v157\n" \
"v_mfma_f32_16x16x32_bf16 v[24:27], a[48:51], a[8:11], v[24:27]\n" \
"ds_read_b64_tr_b16 a[40:41], v215 offset:16384\n" \
"v_fma_f32 v166, v166, v144, -v182\n" \
"v_mfma_f32_16x16x32_bf16 v[24:27], a[52:55], a[12:15], v[24:27]\n" \
"ds_read_b128 a[8:11], v213 offset:256\n" \
"global_load_lds_dwordx4 v222, s[36:37] offset:3072 sc1\n" \
"v_exp_f32 v149, v149\n" \
"v_mfma_f32_16x16x32_bf16 v[40:43], a[48:51], a[16:19], v[40:43]\n" \
"s_add_u32 s36, s36, 0x1000\n" \
"ds_read_b128 a[12:15], v213 offset:2304\n" \
"v_fma_f32 v158, v158, v144, -v181\n" \
"v_mfma_f32_16x16x32_bf16 v[40:43], a[52:55], a[20:23], v[40:43]\n" \
"s_add_u32 m0, m0, 0x5000\n" \
"ds_read_b64_tr_b16 a[42:43], v215 offset:18432\n" \
"v_exp_f32 v174, v174\n" \
"v_add_f32 v191, v172, v173\n" \
"v_fma_f32 v150, v150, v144, -v180\n" \
"v_mfma_f32_16x16x32_bf16 v[44:47], a[56:59], a[16:19], v[44:47]\n" \
"ds_read_b64_tr_b16 a[44:45], v215 offset:24576\n" \
"global_load_lds_dwordx4 v216, s[38:39] offset:-4096\n" \
"v_mfma_f32_16x16x32_bf16 v[44:47], a[60:63], a[20:23], v[44:47]\n" \
"ds_read_b128 a[16:19], v213 offset:4096\n" \
"v_exp_f32 v166, v166\n" \
"v_add_f32 v190, v164, v165\n" \
"v_mfma_f32_16x16x32_bf16 v[60:63], a[56:59], a[24:27], v[60:63]\n" \
"ds_read_b128 a[20:23], v213 offset:6144\n" \
"global_load_lds_dwordx4 v217, s[38:39] offset:-3072\n" \
"v_mfma_f32_16x16x32_bf16 v[60:63], a[60:63], a[28:31], v[60:63]\n" \
"ds_read_b64_tr_b16 a[46:47], v215 offset:26624\n" \
"v_fma_f32 v175, v175, v144, -v183\n" \
"v_exp_f32 v158, v158\n" \
"v_mfma_f32_16x16x32_bf16 v[56:59], a[48:51], a[24:27], v[56:59]\n" \
"v_add_f32 v189, v156, v157\n" \
"v_fma_f32 v167, v167, v144, -v182\n" \
"v_mfma_f32_16x16x32_bf16 v[56:59], a[52:55], a[28:31], v[56:59]\n" \
"s_waitcnt lgkmcnt(3)\n" \
"v_exp_f32 v150, v150\n" \
"v_mfma_f32_16x16x32_bf16 v[0:3], a[32:35], a[0:3], v[0:3]\n" \
"ds_read_b128 a[24:27], v213 offset:4352\n" \
"v_add_f32 v188, v148, v149\n" \
"v_mfma_f32_16x16x32_bf16 v[0:3], a[36:39], a[4:7], v[0:3]\n" \
"s_waitcnt lgkmcnt(1)\n" \
"ds_read_b128 a[28:31], v213 offset:6400\n" \
"global_load_lds_dwordx4 v218, s[38:39] offset:-2048\n" \
"v_fma_f32 v159, v159, v144, -v181\n" \
"v_exp_f32 v175, v175\n" \
"v_mfma_f32_16x16x32_bf16 v[4:7], a[40:43], a[0:3], v[4:7]\n" \
"s_sub_u32 s74, s74, 32768\n" \
"s_cselect_b32 s74, 131072, s74\n" \
"ds_read_b64_tr_b16 a[48:49], v214 offset:16896\n" \
"v_mfma_f32_16x16x32_bf16 v[4:7], a[44:47], a[4:7], v[4:7]\n" \
"ds_read_b64_tr_b16 a[50:51], v214 offset:18944\n" \
"global_load_lds_dwordx4 v219, s[38:39] offset:-1024\n" \
"v_add_f32 v191, v191, v174\n" \
"v_fma_f32 v151, v151, v144, -v180\n" \
"v_mfma_f32_16x16x32_bf16 v[20:23], a[40:43], a[8:11], v[20:23]\n" \
"s_add_u32 s38, s38, 0x20000\n" \
"s_addc_u32 s39, s39, 0\n" \
"ds_read_b64_tr_b16 a[52:53], v214 offset:25088\n" \
"v_mfma_f32_16x16x32_bf16 v[20:23], a[44:47], a[12:15], v[20:23]\n" \
"s_sub_u32 s75, s75, 32768\n" \
"s_cselect_b32 s75, 131072, s75\n" \
"ds_read_b64_tr_b16 a[54:55], v214 offset:27136\n" \
"v_exp_f32 v167, v167\n" \
"v_add_f32 v190, v190, v166\n" \
"v_mfma_f32_16x16x32_bf16 v[16:19], a[32:35], a[8:11], v[16:19]\n" \
"s_add_u32 m0, s75, s95\n" \
"ds_read_b64_tr_b16 a[56:57], v215 offset:16896\n" \
"v_fma_f32 v176, v176, v144, -v183\n" \
"v_mfma_f32_16x16x32_bf16 v[16:19], a[36:39], a[12:15], v[16:19]\n" \
"Lpg11577_%=:\n" \
"s_cbranch_execz Lpg12735_%=\n" \
"ds_read_b64_tr_b16 a[58:59], v215 offset:18944\n" \
"global_load_lds_dwordx4 v222, s[36:37] offset:0 sc1\n" \
"v_exp_f32 v159, v159\n" \
"v_add_f32 v189, v189, v158\n" \
"v_mfma_f32_16x16x32_bf16 v[32:35], a[32:35], a[16:19], v[32:35]\n" \
"ds_read_b64_tr_b16 a[60:61], v215 offset:25088\n" \
"v_fma_f32 v168, v168, v144, -v182\n" \
"v_mfma_f32_16x16x32_bf16 v[32:35], a[36:39], a[20:23], v[32:35]\n" \
"ds_read_b64_tr_b16 a[62:63], v215 offset:27136\n" \
"v_exp_f32 v151, v151\n" \
"v_add_f32 v188, v188, v150\n" \
"v_fma_f32 v160, v160, v144, -v181\n" \
"v_mfma_f32_16x16x32_bf16 v[36:39], a[40:43], a[16:19], v[36:39]\n" \
"global_load_lds_dwordx4 v222, s[36:37] offset:1024 sc1\n" \
"v_add_u32 v213, s74, v211\n" \
"v_mfma_f32_16x16x32_bf16 v[36:39], a[44:47], a[20:23], v[36:39]\n" \
"s_waitcnt lgkmcnt(3)\n" \
"v_add_u32 v214, s74, v212\n" \
"v_xor_b32 v215, 32, v214\n" \
"v_exp_f32 v176, v176\n" \
"v_mfma_f32_16x16x32_bf16 v[52:55], a[40:43], a[24:27], v[52:55]\n" \
"v_add_f32 v191, v191, v175\n" \
"v_fma_f32 v152, v152, v144, -v180\n" \
"v_mfma_f32_16x16x32_bf16 v[52:55], a[44:47], a[28:31], v[52:55]\n" \
"global_load_lds_dwordx4 v222, s[36:37] offset:2048 sc1\n" \
"v_exp_f32 v168, v168\n" \
"v_mfma_f32_16x16x32_bf16 v[48:51], a[32:35], a[24:27], v[48:51]\n" \
"s_waitcnt vmcnt(19) lgkmcnt(0)\n" \
"v_add_f32 v190, v190, v167\n" \
"v_fma_f32 v177, v177, v144, -v183\n" \
"v_mfma_f32_16x16x32_bf16 v[48:51], a[36:39], a[28:31], v[48:51]\n" \
"s_barrier\n" \
"v_exp_f32 v160, v160\n" \
"v_mfma_f32_16x16x32_bf16 v[8:11], a[48:51], a[0:3], v[8:11]\n" \
"ds_read_b64_tr_b16 a[32:33], v214 offset:16384\n" \
"v_add_f32 v189, v189, v159\n" \
"v_mfma_f32_16x16x32_bf16 v[8:11], a[52:55], a[4:7], v[8:11]\n" \
"ds_read_b64_tr_b16 a[34:35], v214 offset:18432\n" \
"global_load_lds_dwordx4 v222, s[36:37] offset:3072 sc1\n" \
"v_fma_f32 v169, v169, v144, -v182\n" \
"v_exp_f32 v152, v152\n" \
"v_mfma_f32_16x16x32_bf16 v[12:15], a[56:59], a[0:3], v[12:15]\n" \
"s_add_u32 s36, s36, 0x1000\n" \
"ds_read_b64_tr_b16 a[36:37], v214 offset:24576\n" \
"v_add_f32 v188, v188, v151\n" \
"v_mfma_f32_16x16x32_bf16 v[12:15], a[60:63], a[4:7], v[12:15]\n" \
"s_add_u32 m0, m0, 0x5000\n" \
"ds_read_b128 a[0:3], v213 offset:0\n" \
"v_fma_f32 v161, v161, v144, -v181\n" \
"v_exp_f32 v177, v177\n" \
"v_mfma_f32_16x16x32_bf16 v[28:31], a[56:59], a[8:11], v[28:31]\n" \
"ds_read_b128 a[4:7], v213 offset:2048\n" \
"global_load_lds_dwordx4 v216, s[38:39] offset:-4096\n" \
"v_mfma_f32_16x16x32_bf16 v[28:31], a[60:63], a[12:15], v[28:31]\n" \
"ds_read_b64_tr_b16 a[38:39], v214 offset:26624\n" \
"v_add_f32 v191, v191, v176\n" \
"v_mfma_f32_16x16x32_bf16 v[24:27], a[48:51], a[8:11], v[24:27]\n" \
"ds_read_b64_tr_b16 a[40:41], v215 offset:16384\n" \
"v_fma_f32 v153, v153, v144, -v180\n" \
"v_mfma_f32_16x16x32_bf16 v[24:27], a[52:55], a[12:15], v[24:27]\n" \
"ds_read_b128 a[8:11], v213 offset:256\n" \
"global_load_lds_dwordx4 v217, s[38:39] offset:-3072\n" \
"v_exp_f32 v169, v169\n" \
"v_add_f32 v190, v190, v168\n" \
"v_mfma_f32_16x16x32_bf16 v[40:43], a[48:51], a[16:19], v[40:43]\n" \
"ds_read_b128 a[12:15], v213 offset:2304\n" \
"v_fma_f32 v178, v178, v144, -v183\n" \
"v_mfma_f32_16x16x32_bf16 v[40:43], a[52:55], a[20:23], v[40:43]\n" \
"ds_read_b64_tr_b16 a[42:43], v215 offset:18432\n" \
"v_exp_f32 v161, v161\n" \
"v_add_f32 v189, v189, v160\n" \
"v_fma_f32 v170, v170, v144, -v182\n" \
"v_mfma_f32_16x16x32_bf16 v[44:47], a[56:59], a[16:19], v[44:47]\n" \
"ds_read_b64_tr_b16 a[44:45], v215 offset:24576\n" \
"global_load_lds_dwordx4 v218, s[38:39] offset:-2048\n" \
"v_mfma_f32_16x16x32_bf16 v[44:47], a[60:63], a[20:23], v[44:47]\n" \
"ds_read_b128 a[16:19], v213 offset:4096\n" \
"v_exp_f32 v153, v153\n" \
"v_add_f32 v188, v188, v152\n" \
"v_fma_f32 v162, v162, v144, -v181\n" \
"v_mfma_f32_16x16x32_bf16 v[60:63], a[56:59], a[24:27], v[60:63]\n" \
"ds_read_b128 a[20:23], v213 offset:6144\n" \
"global_load_lds_dwordx4 v219, s[38:39] offset:-1024\n" \
"v_mfma_f32_16x16x32_bf16 v[60:63], a[60:63], a[28:31], v[60:63]\n" \
"s_add_u32 s38, s38, 0x20000\n" \
"s_addc_u32 s39, s39, 0\n" \
"ds_read_b64_tr_b16 a[46:47], v215 offset:26624\n" \
"v_exp_f32 v178, v178\n" \
"v_add_f32 v191, v191, v177\n" \
"v_mfma_f32_16x16x32_bf16 v[56:59], a[48:51], a[24:27], v[56:59]\n" \
"s_sub_u32 s75, s75, 32768\n" \
"s_cselect_b32 s75, 131072, s75\n" \
"v_fma_f32 v154, v154, v144, -v180\n" \
"v_mfma_f32_16x16x32_bf16 v[56:59], a[52:55], a[28:31], v[56:59]\n" \
"s_waitcnt lgkmcnt(3)\n" \
"v_exp_f32 v170, v170\n" \
"v_add_f32 v190, v190, v169\n" \
"v_mfma_f32_16x16x32_bf16 v[0:3], a[32:35], a[0:3], v[0:3]\n" \
"s_add_u32 m0, s75, s95\n" \
"ds_read_b128 a[24:27], v213 offset:4352\n" \
"v_fma_f32 v179, v179, v144, -v183\n" \
"v_mfma_f32_16x16x32_bf16 v[0:3], a[36:39], a[4:7], v[0:3]\n" \
"s_waitcnt lgkmcnt(1)\n" \
"ds_read_b128 a[28:31], v213 offset:6400\n" \
"global_load_lds_dwordx4 v222, s[36:37] offset:0 sc1\n" \
"v_exp_f32 v162, v162\n" \
"v_mfma_f32_16x16x32_bf16 v[4:7], a[40:43], a[0:3], v[4:7]\n" \
"s_sub_u32 s74, s74, 32768\n" \
"s_cselect_b32 s74, 131072, s74\n" \
"ds_read_b64_tr_b16 a[48:49], v214 offset:16896\n" \
"v_mfma_f32_16x16x32_bf16 v[4:7], a[44:47], a[4:7], v[4:7]\n" \
"ds_read_b64_tr_b16 a[50:51], v214 offset:18944\n" \
"v_add_f32 v189, v189, v161\n" \
"v_fma_f32 v171, v171, v144, -v182\n" \
"v_exp_f32 v154, v154\n" \
"v_mfma_f32_16x16x32_bf16 v[20:23], a[40:43], a[8:11], v[20:23]\n" \
"ds_read_b64_tr_b16 a[52:53], v214 offset:25088\n" \
"v_add_f32 v188, v188, v153\n" \
"v_mfma_f32_16x16x32_bf16 v[20:23], a[44:47], a[12:15], v[20:23]\n" \
"ds_read_b64_tr_b16 a[54:55], v214 offset:27136\n" \
"global_load_lds_dwordx4 v222, s[36:37] offset:1024 sc1\n" \
"v_mfma_f32_16x16x32_bf16 v[16:19], a[32:35], a[8:11], v[16:19]\n" \
"ds_read_b64_tr_b16 a[56:57], v215 offset:16896\n" \
"v_fma_f32 v163, v163, v144, -v181\n" \
"v_mfma_f32_16x16x32_bf16 v[16:19], a[36:39], a[12:15], v[16:19]\n" \
"ds_read_b64_tr_b16 a[58:59], v215 offset:18944\n" \
"v_exp_f32 v179, v179\n" \
"v_add_f32 v191, v191, v178\n" \
"v_mfma_f32_16x16x32_bf16 v[32:35], a[32:35], a[16:19], v[32:35]\n" \
"ds_read_b64_tr_b16 a[60:61], v215 offset:25088\n" \
"v_fma_f32 v155, v155, v144, -v180\n" \
"v_mfma_f32_16x16x32_bf16 v[32:35], a[36:39], a[20:23], v[32:35]\n" \
"ds_read_b64_tr_b16 a[62:63], v215 offset:27136\n" \
"global_load_lds_dwordx4 v222, s[36:37] offset:2048 sc1\n" \
"v_exp_f32 v171, v171\n" \
"v_add_f32 v190, v190, v170\n" \
"v_mfma_f32_16x16x32_bf16 v[36:39], a[40:43], a[16:19], v[36:39]\n" \
"v_add_u32 v213, s74, v211\n" \
"v_add_u32 v214, s74, v212\n" \
"v_mfma_f32_16x16x32_bf16 v[36:39], a[44:47], a[20:23], v[36:39]\n" \
"s_waitcnt lgkmcnt(3)\n" \
"v_xor_b32 v215, 32, v214\n" \
"v_exp_f32 v163, v163\n" \
"v_add_f32 v189, v189, v162\n" \
"v_exp_f32 v155, v155\n" \
"v_mfma_f32_16x16x32_bf16 v[52:55], a[40:43], a[24:27], v[52:55]\n" \
"v_add_f32 v188, v188, v154\n" \
"v_add_f32 v191, v191, v179\n" \
"v_mfma_f32_16x16x32_bf16 v[52:55], a[44:47], a[28:31], v[52:55]\n" \
"global_load_lds_dwordx4 v222, s[36:37] offset:3072 sc1\n" \
"v_add_f32 v190, v190, v171\n" \
"v_mfma_f32_16x16x32_bf16 v[48:51], a[32:35], a[24:27], v[48:51]\n" \
"s_waitcnt vmcnt(20) lgkmcnt(0)\n" \
"v_add_f32 v189, v189, v163\n" \
"s_add_u32 s36, s36, 0x1000\n" \
"v_mfma_f32_16x16x32_bf16 v[48:51], a[36:39], a[28:31], v[48:51]\n" \
"s_barrier\n" \
"v_add_f32 v188, v188, v155\n" \
"v_mfma_f32_16x16x32_bf16 v[8:11], a[48:51], a[0:3], v[8:11]\n" \
"s_add_u32 m0, m0, 0x5000\n" \
"ds_read_b64_tr_b16 a[32:33], v214 offset:16384\n" \
"v_mov_b32 v187, v191\n" \
"v_mfma_f32_16x16x32_bf16 v[8:11], a[52:55], a[4:7], v[8:11]\n" \
"ds_read_b64_tr_b16 a[34:35], v214 offset:18432\n" \
"global_load_lds_dwordx4 v216, s[38:39] offset:-4096\n" \
"v_mfma_f32_16x16x32_bf16 v[12:15], a[56:59], a[0:3], v[12:15]\n" \
"ds_read_b64_tr_b16 a[36:37], v214 offset:24576\n" \
"v_mov_b32 v186, v190\n" \
"v_mfma_f32_16x16x32_bf16 v[12:15], a[60:63], a[4:7], v[12:15]\n" \
"ds_read_b128 a[0:3], v213 offset:0\n" \
"v_mov_b32 v185, v189\n" \
"v_mfma_f32_16x16x32_bf16 v[28:31], a[56:59], a[8:11], v[28:31]\n" \
"ds_read_b128 a[4:7], v213 offset:2048\n" \
"v_mov_b32 v184, v188\n" \
"v_mfma_f32_16x16x32_bf16 v[28:31], a[60:63], a[12:15], v[28:31]\n" \
"ds_read_b64_tr_b16 a[38:39], v214 offset:26624\n" \
"global_load_lds_dwordx4 v217, s[38:39] offset:-3072\n" \
"v_permlane16_swap_b32 v187, v191\n" \
"v_permlane16_swap_b32 v186, v190\n" \
"v_permlane16_swap_b32 v185, v189\n" \
"v_permlane16_swap_b32 v184, v188\n" \
"v_add_f32 v191, v191, v187\n" \
"v_mfma_f32_16x16x32_bf16 v[24:27], a[48:51], a[8:11], v[24:27]\n" \
"ds_read_b64_tr_b16 a[40:41], v215 offset:16384\n" \
"v_add_f32 v190, v190, v186\n" \
"v_mfma_f32_16x16x32_bf16 v[24:27], a[52:55], a[12:15], v[24:27]\n" \
"ds_read_b128 a[8:11], v213 offset:256\n" \
"v_add_f32 v189, v189, v185\n" \
"v_add_f32 v188, v188, v184\n" \
"v_mfma_f32_16x16x32_bf16 v[40:43], a[48:51], a[16:19], v[40:43]\n" \
"ds_read_b128 a[12:15], v213 offset:2304\n" \
"v_mov_b32 v187, v191\n" \
"v_mfma_f32_16x16x32_bf16 v[40:43], a[52:55], a[20:23], v[40:43]\n" \
"ds_read_b64_tr_b16 a[42:43], v215 offset:18432\n" \
"global_load_lds_dwordx4 v218, s[38:39] offset:-2048\n" \
"v_mfma_f32_16x16x32_bf16 v[44:47], a[56:59], a[16:19], v[44:47]\n" \
"ds_read_b64_tr_b16 a[44:45], v215 offset:24576\n" \
"v_mov_b32 v186, v190\n" \
"v_mfma_f32_16x16x32_bf16 v[44:47], a[60:63], a[20:23], v[44:47]\n" \
"ds_read_b128 a[16:19], v213 offset:4096\n" \
"v_mov_b32 v185, v189\n" \
"v_mfma_f32_16x16x32_bf16 v[60:63], a[56:59], a[24:27], v[60:63]\n" \
"ds_read_b128 a[20:23], v213 offset:6144\n" \
"global_load_lds_dwordx4 v219, s[38:39] offset:-1024\n" \
"v_mfma_f32_16x16x32_bf16 v[60:63], a[60:63], a[28:31], v[60:63]\n" \
"s_add_u32 s38, s38, 0x20000\n" \
"s_addc_u32 s39, s39, 0\n" \
"ds_read_b64_tr_b16 a[46:47], v215 offset:26624\n" \
"v_mfma_f32_16x16x32_bf16 v[56:59], a[48:51], a[24:27], v[56:59]\n" \
"s_sub_u32 s75, s75, 32768\n" \
"s_cselect_b32 s75, 131072, s75\n" \
"v_mov_b32 v184, v188\n" \
"v_mfma_f32_16x16x32_bf16 v[56:59], a[52:55], a[28:31], v[56:59]\n" \
"s_waitcnt lgkmcnt(3)\n" \
"v_permlane32_swap_b32 v187, v191\n" \
"v_permlane32_swap_b32 v186, v190\n" \
"v_permlane32_swap_b32 v185, v189\n" \
"v_permlane32_swap_b32 v184, v188\n" \
"v_add_f32 v191, v191, v187\n" \
"v_mfma_f32_16x16x32_bf16 v[0:3], a[32:35], a[0:3], v[0:3]\n" \
"s_add_u32 m0, s75, s95\n" \
"ds_read_b128 a[24:27], v213 offset:4352\n" \
"v_add_f32 v190, v190, v186\n" \
"v_mfma_f32_16x16x32_bf16 v[0:3], a[36:39], a[4:7], v[0:3]\n" \
"s_waitcnt lgkmcnt(1)\n" \
"ds_read_b128 a[28:31], v213 offset:6400\n" \
"global_load_lds_dwordx4 v222, s[36:37] offset:0 sc1\n" \
"v_mfma_f32_16x16x32_bf16 v[4:7], a[40:43], a[0:3], v[4:7]\n" \
"s_sub_u32 s74, s74, 32768\n" \
"s_cselect_b32 s74, 131072, s74\n" \
"ds_read_b64_tr_b16 a[48:49], v214 offset:16896\n" \
"v_mfma_f32_16x16x32_bf16 v[4:7], a[44:47], a[4:7], v[4:7]\n" \
"ds_read_b64_tr_b16 a[50:51], v214 offset:18944\n" \
"v_add_f32 v189, v189, v185\n" \
"v_mfma_f32_16x16x32_bf16 v[20:23], a[40:43], a[8:11], v[20:23]\n" \
"ds_read_b64_tr_b16 a[52:53], v214 offset:25088\n" \
"v_add_f32 v188, v188, v184\n" \
"v_mfma_f32_16x16x32_bf16 v[20:23], a[44:47], a[12:15], v[20:23]\n" \
"ds_read_b64_tr_b16 a[54:55], v214 offset:27136\n" \
"global_load_lds_dwordx4 v222, s[36:37] offset:1024 sc1\n" \
"v_rcp_f32 v191, v191\n" \
"v_rcp_f32 v190, v190\n" \
"v_rcp_f32 v189, v189\n" \
"v_rcp_f32 v188, v188\n" \
"v_mul_f32 v172, v172, v191\n" \
"v_mfma_f32_16x16x32_bf16 v[16:19], a[32:35], a[8:11], v[16:19]\n" \
"ds_read_b64_tr_b16 a[56:57], v215 offset:16896\n" \
"v_mul_f32 v164, v164, v190\n" \
"v_mfma_f32_16x16x32_bf16 v[16:19], a[36:39], a[12:15], v[16:19]\n" \
"ds_read_b64_tr_b16 a[58:59], v215 offset:18944\n" \
"v_mul_f32 v156, v156, v189\n" \
"v_mul_f32 v148, v148, v188\n" \
"v_mfma_f32_16x16x32_bf16 v[32:35], a[32:35], a[16:19], v[32:35]\n" \
"ds_read_b64_tr_b16 a[60:61], v215 offset:25088\n" \
"v_mul_f32 v173, v173, v191\n" \
"v_mfma_f32_16x16x32_bf16 v[32:35], a[36:39], a[20:23], v[32:35]\n" \
"ds_read_b64_tr_b16 a[62:63], v215 offset:27136\n" \
"global_load_lds_dwordx4 v222, s[36:37] offset:2048 sc1\n" \
"v_mul_f32 v165, v165, v190\n" \
"v_mul_f32 v157, v157, v189\n" \
"v_mul_f32 v149, v149, v188\n" \
"v_mfma_f32_16x16x32_bf16 v[36:39], a[40:43], a[16:19], v[36:39]\n" \
"v_add_u32 v213, s74, v211\n" \
"v_add_u32 v214, s74, v212\n" \
"v_mfma_f32_16x16x32_bf16 v[36:39], a[44:47], a[20:23], v[36:39]\n" \
"s_waitcnt lgkmcnt(3)\n" \
"v_xor_b32 v215, 32, v214\n" \
"v_mul_f32 v174, v174, v191\n" \
"v_mul_f32 v166, v166, v190\n" \
"v_mul_f32 v158, v158, v189\n" \
"v_mfma_f32_16x16x32_bf16 v[52:55], a[40:43], a[24:27], v[52:55]\n" \
"v_mul_f32 v150, v150, v188\n" \
"v_mul_f32 v175, v175, v191\n" \
"v_mfma_f32_16x16x32_bf16 v[52:55], a[44:47], a[28:31], v[52:55]\n" \
"global_load_lds_dwordx4 v222, s[36:37] offset:3072 sc1\n" \
"v_mul_f32 v167, v167, v190\n" \
"v_mul_f32 v159, v159, v189\n" \
"v_mfma_f32_16x16x32_bf16 v[48:51], a[32:35], a[24:27], v[48:51]\n" \
"s_waitcnt vmcnt(20) lgkmcnt(0)\n" \
"v_mul_f32 v151, v151, v188\n" \
"s_add_u32 s36, s36, 0x1000\n" \
"v_mfma_f32_16x16x32_bf16 v[48:51], a[36:39], a[28:31], v[48:51]\n" \
"s_barrier\n" \
"v_mul_f32 v176, v176, v191\n" \
"v_mul_f32 v168, v168, v190\n" \
"v_mul_f32 v160, v160, v189\n" \
"v_mfma_f32_16x16x32_bf16 v[8:11], a[48:51], a[0:3], v[8:11]\n" \
"s_add_u32 m0, m0, 0x5000\n" \
"ds_read_b64_tr_b16 a[32:33], v214 offset:16384\n" \
"v_mul_f32 v152, v152, v188\n" \
"v_mfma_f32_16x16x32_bf16 v[8:11], a[52:55], a[4:7], v[8:11]\n" \
"ds_read_b64_tr_b16 a[34:35], v214 offset:18432\n" \
"global_load_lds_dwordx4 v216, s[38:39] offset:-4096\n" \
"v_mul_f32 v177, v177, v191\n" \
"v_mul_f32 v169, v169, v190\n" \
"v_mfma_f32_16x16x32_bf16 v[12:15], a[56:59], a[0:3], v[12:15]\n" \
"ds_read_b64_tr_b16 a[36:37], v214 offset:24576\n" \
"v_mul_f32 v161, v161, v189\n" \
"v_mfma_f32_16x16x32_bf16 v[12:15], a[60:63], a[4:7], v[12:15]\n" \
"ds_read_b128 a[0:3], v213 offset:0\n" \
"v_mul_f32 v153, v153, v188\n" \
"v_mul_f32 v178, v178, v191\n" \
"v_mul_f32 v170, v170, v190\n" \
"v_mfma_f32_16x16x32_bf16 v[28:31], a[56:59], a[8:11], v[28:31]\n" \
"ds_read_b128 a[4:7], v213 offset:2048\n" \
"v_mul_f32 v162, v162, v189\n" \
"v_mfma_f32_16x16x32_bf16 v[28:31], a[60:63], a[12:15], v[28:31]\n" \
"ds_read_b64_tr_b16 a[38:39], v214 offset:26624\n" \
"global_load_lds_dwordx4 v217, s[38:39] offset:-3072\n" \
"v_mul_f32 v154, v154, v188\n" \
"v_mul_f32 v179, v179, v191\n" \
"v_mfma_f32_16x16x32_bf16 v[24:27], a[48:51], a[8:11], v[24:27]\n" \
"ds_read_b64_tr_b16 a[40:41], v215 offset:16384\n" \
"v_mul_f32 v171, v171, v190\n" \
"v_mfma_f32_16x16x32_bf16 v[24:27], a[52:55], a[12:15], v[24:27]\n" \
"ds_read_b128 a[8:11], v213 offset:256\n" \
"v_mul_f32 v163, v163, v189\n" \
"v_mul_f32 v155, v155, v188\n" \
"v_cvt_pk_bf16_f32 v172, v172, v173\n" \
"v_mfma_f32_16x16x32_bf16 v[40:43], a[48:51], a[16:19], v[40:43]\n" \
"ds_read_b128 a[12:15], v213 offset:2304\n" \
"v_cvt_pk_bf16_f32 v173, v174, v175\n" \
"v_mfma_f32_16x16x32_bf16 v[40:43], a[52:55], a[20:23], v[40:43]\n" \
"ds_read_b64_tr_b16 a[42:43], v215 offset:18432\n" \
"global_load_lds_dwordx4 v218, s[38:39] offset:-2048\n" \
"v_cvt_pk_bf16_f32 v174, v176, v177\n" \
"v_cvt_pk_bf16_f32 v175, v178, v179\n" \
"v_mfma_f32_16x16x32_bf16 v[44:47], a[56:59], a[16:19], v[44:47]\n" \
"ds_read_b64_tr_b16 a[44:45], v215 offset:24576\n" \
"v_cvt_pk_bf16_f32 v164, v164, v165\n" \
"v_mfma_f32_16x16x32_bf16 v[44:47], a[60:63], a[20:23], v[44:47]\n" \
"ds_read_b128 a[16:19], v213 offset:4096\n" \
"v_cvt_pk_bf16_f32 v165, v166, v167\n" \
"v_cvt_pk_bf16_f32 v166, v168, v169\n" \
"v_cvt_pk_bf16_f32 v167, v170, v171\n" \
"v_mfma_f32_16x16x32_bf16 v[60:63], a[56:59], a[24:27], v[60:63]\n" \
"ds_read_b128 a[20:23], v213 offset:6144\n" \
"global_load_lds_dwordx4 v219, s[38:39] offset:-1024\n" \
"v_mfma_f32_16x16x32_bf16 v[60:63], a[60:63], a[28:31], v[60:63]\n" \
"s_add_u32 s38, s38, 0x20000\n" \
"s_addc_u32 s39, s39, 0\n" \
"ds_read_b64_tr_b16 a[46:47], v215 offset:26624\n" \
"v_mfma_f32_16x16x32_bf16 v[56:59], a[48:51], a[24:27], v[56:59]\n" \
"s_sub_u32 s75, s75, 32768\n" \
"s_cselect_b32 s75, 131072, s75\n" \
"v_cvt_pk_bf16_f32 v156, v156, v157\n" \
"v_mfma_f32_16x16x32_bf16 v[56:59], a[52:55], a[28:31], v[56:59]\n" \
"s_waitcnt lgkmcnt(3)\n" \
"v_cvt_pk_bf16_f32 v157, v158, v159\n" \
"v_cvt_pk_bf16_f32 v158, v160, v161\n" \
"v_mfma_f32_16x16x32_bf16 v[0:3], a[32:35], a[0:3], v[0:3]\n" \
"s_add_u32 m0, s75, s95\n" \
"ds_read_b128 a[24:27], v213 offset:4352\n" \
"v_cvt_pk_bf16_f32 v159, v162, v163\n" \
"v_mfma_f32_16x16x32_bf16 v[0:3], a[36:39], a[4:7], v[0:3]\n" \
"s_waitcnt lgkmcnt(1)\n" \
"ds_read_b128 a[28:31], v213 offset:6400\n" \
"global_load_lds_dwordx4 v222, s[36:37] offset:0 sc1\n" \
"v_mfma_f32_16x16x32_bf16 v[4:7], a[40:43], a[0:3], v[4:7]\n" \
"s_sub_u32 s74, s74, 32768\n" \
"s_cselect_b32 s74, 131072, s74\n" \
"ds_read_b64_tr_b16 a[48:49], v214 offset:16896\n" \
"v_mfma_f32_16x16x32_bf16 v[4:7], a[44:47], a[4:7], v[4:7]\n" \
"ds_read_b64_tr_b16 a[50:51], v214 offset:18944\n" \
"v_cvt_pk_bf16_f32 v148, v148, v149\n" \
"v_mfma_f32_16x16x32_bf16 v[20:23], a[40:43], a[8:11], v[20:23]\n" \
"ds_read_b64_tr_b16 a[52:53], v214 offset:25088\n" \
"v_cvt_pk_bf16_f32 v149, v150, v151\n" \
"v_mfma_f32_16x16x32_bf16 v[20:23], a[44:47], a[12:15], v[20:23]\n" \
"ds_read_b64_tr_b16 a[54:55], v214 offset:27136\n" \
"global_load_lds_dwordx4 v222, s[36:37] offset:1024 sc1\n" \
"v_mfma_f32_16x16x32_bf16 v[16:19], a[32:35], a[8:11], v[16:19]\n" \
"ds_read_b64_tr_b16 a[56:57], v215 offset:16896\n" \
"v_cvt_pk_bf16_f32 v150, v152, v153\n" \
"v_mfma_f32_16x16x32_bf16 v[16:19], a[36:39], a[12:15], v[16:19]\n" \
"ds_read_b64_tr_b16 a[58:59], v215 offset:18944\n" \
"v_cvt_pk_bf16_f32 v151, v154, v155\n" \
"v_mfma_f32_16x16x32_bf16 v[32:35], a[32:35], a[16:19], v[32:35]\n" \
"v_mfma_f32_16x16x32_bf16 v[136:139], v[74:77], v[172:175], 0\n" \
"v_mfma_f32_16x16x32_bf16 v[140:143], v[198:201], v[172:175], 0\n" \
"v_mfma_f32_16x16x32_bf16 v[128:131], v[72:75], v[164:167], 0\n" \
"v_mfma_f32_16x16x32_bf16 v[132:135], v[196:199], v[164:167], 0\n" \
"v_mfma_f32_16x16x32_bf16 v[120:123], v[70:73], v[156:159], 0\n" \
"v_mfma_f32_16x16x32_bf16 v[124:127], v[194:197], v[156:159], 0\n" \
"ds_read_b64_tr_b16 a[60:61], v215 offset:25088\n" \
"v_mfma_f32_16x16x32_bf16 v[32:35], a[36:39], a[20:23], v[32:35]\n" \
"v_mfma_f32_16x16x32_bf16 v[112:115], v[68:71], v[148:151], 0\n" \
"v_mfma_f32_16x16x32_bf16 v[116:119], v[192:195], v[148:151], 0\n" \
"ds_read_b64_tr_b16 a[62:63], v215 offset:27136\n" \
"global_load_lds_dwordx4 v222, s[36:37] offset:2048 sc1\n" \
"v_mfma_f32_16x16x32_bf16 v[36:39], a[40:43], a[16:19], v[36:39]\n" \
"v_add_u32 v213, s74, v211\n" \
"v_add_u32 v214, s74, v212\n" \
"v_mfma_f32_16x16x32_bf16 v[36:39], a[44:47], a[20:23], v[36:39]\n" \
"s_waitcnt lgkmcnt(3)\n" \
"v_xor_b32 v215, 32, v214\n" \
"v_cvt_pk_bf16_f32 v136, v136, v137\n" \
"v_cvt_pk_bf16_f32 v137, v138, v139\n" \
"v_mfma_f32_16x16x32_bf16 v[52:55], a[40:43], a[24:27], v[52:55]\n" \
"v_cvt_pk_bf16_f32 v138, v140, v141\n" \
"v_cvt_pk_bf16_f32 v139, v142, v143\n" \
"v_mfma_f32_16x16x32_bf16 v[52:55], a[44:47], a[28:31], v[52:55]\n" \
"global_load_lds_dwordx4 v222, s[36:37] offset:3072 sc1\n" \
"v_cndmask_b32 v140, v136, v138, s[92:93]\n" \
"v_mfma_f32_16x16x32_bf16 v[48:51], a[32:35], a[24:27], v[48:51]\n" \
"s_waitcnt vmcnt(20) lgkmcnt(0)\n" \
"v_cndmask_b32 v141, v137, v139, s[92:93]\n" \
"s_add_u32 s36, s36, 0x1000\n" \
"v_mfma_f32_16x16x32_bf16 v[48:51], a[36:39], a[28:31], v[48:51]\n" \
"s_barrier\n" \
"v_cndmask_b32 v142, v138, v136, s[92:93]\n" \
"v_mfma_f32_16x16x32_bf16 v[8:11], a[48:51], a[0:3], v[8:11]\n" \
"s_add_u32 m0, m0, 0x5000\n" \
"ds_read_b64_tr_b16 a[32:33], v214 offset:16384\n" \
"v_cndmask_b32 v143, v139, v137, s[92:93]\n" \
"v_mfma_f32_16x16x32_bf16 v[8:11], a[52:55], a[4:7], v[8:11]\n" \
"s_add_u32 s71, s87, 208896\n" \
"ds_read_b64_tr_b16 a[34:35], v214 offset:18432\n" \
"global_load_lds_dwordx4 v216, s[38:39] offset:-4096\n" \
"v_mfma_f32_16x16x32_bf16 v[12:15], a[56:59], a[0:3], v[12:15]\n" \
"ds_read_b64_tr_b16 a[36:37], v214 offset:24576\n" \
"buffer_store_dwordx4 v[140:143], v146, s[56:59], s71 offen sc1\n" \
"v_cvt_pk_bf16_f32 v128, v128, v129\n" \
"v_cvt_pk_bf16_f32 v129, v130, v131\n" \
"v_cvt_pk_bf16_f32 v130, v132, v133\n" \
"v_cvt_pk_bf16_f32 v131, v134, v135\n" \
"v_cndmask_b32 v132, v128, v130, s[92:93]\n" \
"v_mfma_f32_16x16x32_bf16 v[12:15], a[60:63], a[4:7], v[12:15]\n" \
"ds_read_b128 a[0:3], v213 offset:0\n" \
"v_cndmask_b32 v133, v129, v131, s[92:93]\n" \
"v_cndmask_b32 v134, v130, v128, s[92:93]\n" \
"v_mfma_f32_16x16x32_bf16 v[28:31], a[56:59], a[8:11], v[28:31]\n" \
"ds_read_b128 a[4:7], v213 offset:2048\n" \
"v_cndmask_b32 v135, v131, v129, s[92:93]\n" \
"v_mfma_f32_16x16x32_bf16 v[28:31], a[60:63], a[12:15], v[28:31]\n" \
"s_add_u32 s71, s87, 139264\n" \
"ds_read_b64_tr_b16 a[38:39], v214 offset:26624\n" \
"global_load_lds_dwordx4 v217, s[38:39] offset:-3072\n" \
"v_mfma_f32_16x16x32_bf16 v[24:27], a[48:51], a[8:11], v[24:27]\n" \
"ds_read_b64_tr_b16 a[40:41], v215 offset:16384\n" \
"buffer_store_dwordx4 v[132:135], v146, s[56:59], s71 offen sc1\n" \
"v_cvt_pk_bf16_f32 v120, v120, v121\n" \
"v_cvt_pk_bf16_f32 v121, v122, v123\n" \
"v_cvt_pk_bf16_f32 v122, v124, v125\n" \
"v_cvt_pk_bf16_f32 v123, v126, v127\n" \
"v_cndmask_b32 v124, v120, v122, s[92:93]\n" \
"v_mfma_f32_16x16x32_bf16 v[24:27], a[52:55], a[12:15], v[24:27]\n" \
"ds_read_b128 a[8:11], v213 offset:256\n" \
"v_cndmask_b32 v125, v121, v123, s[92:93]\n" \
"v_cndmask_b32 v126, v122, v120, s[92:93]\n" \
"v_mfma_f32_16x16x32_bf16 v[40:43], a[48:51], a[16:19], v[40:43]\n" \
"ds_read_b128 a[12:15], v213 offset:2304\n" \
"v_cndmask_b32 v127, v123, v121, s[92:93]\n" \
"v_mfma_f32_16x16x32_bf16 v[40:43], a[52:55], a[20:23], v[40:43]\n" \
"s_add_u32 s71, s87, 69632\n" \
"ds_read_b64_tr_b16 a[42:43], v215 offset:18432\n" \
"global_load_lds_dwordx4 v218, s[38:39] offset:-2048\n" \
"v_mfma_f32_16x16x32_bf16 v[44:47], a[56:59], a[16:19], v[44:47]\n" \
"ds_read_b64_tr_b16 a[44:45], v215 offset:24576\n" \
"buffer_store_dwordx4 v[124:127], v146, s[56:59], s71 offen sc1\n" \
"v_cvt_pk_bf16_f32 v112, v112, v113\n" \
"v_cvt_pk_bf16_f32 v113, v114, v115\n" \
"v_cvt_pk_bf16_f32 v114, v116, v117\n" \
"v_cvt_pk_bf16_f32 v115, v118, v119\n" \
"v_cndmask_b32 v116, v112, v114, s[92:93]\n" \
"v_mfma_f32_16x16x32_bf16 v[44:47], a[60:63], a[20:23], v[44:47]\n" \
"ds_read_b128 a[16:19], v213 offset:4096\n" \
"v_cndmask_b32 v117, v113, v115, s[92:93]\n" \
"v_cndmask_b32 v118, v114, v112, s[92:93]\n" \
"v_cndmask_b32 v119, v115, v113, s[92:93]\n" \
"v_mfma_f32_16x16x32_bf16 v[60:63], a[56:59], a[24:27], v[60:63]\n" \
"s_add_u32 s71, s87, 0\n" \
"ds_read_b128 a[20:23], v213 offset:6144\n" \
"global_load_lds_dwordx4 v219, s[38:39] offset:-1024\n" \
"v_mfma_f32_16x16x32_bf16 v[60:63], a[60:63], a[28:31], v[60:63]\n" \
"buffer_store_dwordx4 v[116:119], v146, s[56:59], s71 offen sc1\n" \
"s_add_u32 s38, s38, 0x20000\n" \
"s_addc_u32 s39, s39, 0\n" \
"ds_read_b64_tr_b16 a[46:47], v215 offset:26624\n" \
"v_mfma_f32_16x16x32_bf16 v[56:59], a[48:51], a[24:27], v[56:59]\n" \
"s_sub_u32 s75, s75, 32768\n" \
"s_cselect_b32 s75, 131072, s75\n" \
"v_mfma_f32_16x16x32_bf16 v[56:59], a[52:55], a[28:31], v[56:59]\n" \
"s_waitcnt lgkmcnt(3)\n" \
"v_mfma_f32_16x16x32_bf16 v[0:3], a[32:35], a[0:3], v[0:3]\n" \
"s_add_u32 m0, s75, s95\n" \
"ds_read_b128 a[24:27], v213 offset:4352\n" \
"v_mov_b32 v224, s60\n" \
"v_mfma_f32_16x16x32_bf16 v[0:3], a[36:39], a[4:7], v[0:3]\n" \
"s_waitcnt lgkmcnt(1)\n" \
"ds_read_b128 a[28:31], v213 offset:6400\n" \
"global_load_lds_dwordx4 v222, s[36:37] offset:0 sc1\n" \
"v_mfma_f32_16x16x32_bf16 v[4:7], a[40:43], a[0:3], v[4:7]\n" \
"s_sub_u32 s74, s74, 32768\n" \
"s_cselect_b32 s74, 131072, s74\n" \
"ds_read_b64_tr_b16 a[48:49], v214 offset:16896\n" \
"v_mfma_f32_16x16x32_bf16 v[4:7], a[44:47], a[4:7], v[4:7]\n" \
"ds_read_b64_tr_b16 a[50:51], v214 offset:18944\n" \
"v_lshlrev_b32 v225, 28, s61\n" \
"v_mfma_f32_16x16x32_bf16 v[20:23], a[40:43], a[8:11], v[20:23]\n" \
"ds_read_b64_tr_b16 a[52:53], v214 offset:25088\n" \
"v_mfma_f32_16x16x32_bf16 v[20:23], a[44:47], a[12:15], v[20:23]\n" \
"ds_read_b64_tr_b16 a[54:55], v214 offset:27136\n" \
"global_load_lds_dwordx4 v222, s[36:37] offset:1024 sc1\n" \
"v_mfma_f32_16x16x32_bf16 v[16:19], a[32:35], a[8:11], v[16:19]\n" \
"ds_read_b64_tr_b16 a[56:57], v215 offset:16896\n" \
"v_mfma_f32_16x16x32_bf16 v[16:19], a[36:39], a[12:15], v[16:19]\n" \
"ds_read_b64_tr_b16 a[58:59], v215 offset:18944\n" \
"v_mfma_f32_16x16x32_bf16 v[32:35], a[32:35], a[16:19], v[32:35]\n" \
"ds_read_b64_tr_b16 a[60:61], v215 offset:25088\n" \
"v_mfma_f32_16x16x32_bf16 v[32:35], a[36:39], a[20:23], v[32:35]\n" \
"ds_read_b64_tr_b16 a[62:63], v215 offset:27136\n" \
"global_load_lds_dwordx4 v222, s[36:37] offset:2048 sc1\n" \
"v_mfma_f32_16x16x32_bf16 v[36:39], a[40:43], a[16:19], v[36:39]\n" \
"v_add_u32 v213, s74, v211\n" \
"v_add_u32 v214, s74, v212\n" \
"v_mfma_f32_16x16x32_bf16 v[36:39], a[44:47], a[20:23], v[36:39]\n" \
"s_waitcnt lgkmcnt(3)\n" \
"v_xor_b32 v215, 32, v214\n" \
"v_mfma_f32_16x16x32_bf16 v[52:55], a[40:43], a[24:27], v[52:55]\n" \
"v_mfma_f32_16x16x32_bf16 v[52:55], a[44:47], a[28:31], v[52:55]\n" \
"global_load_lds_dwordx4 v222, s[36:37] offset:3072 sc1\n" \
"v_mfma_f32_16x16x32_bf16 v[48:51], a[32:35], a[24:27], v[48:51]\n" \
"s_waitcnt vmcnt(24) lgkmcnt(0)\n" \
"s_add_u32 s36, s36, 0x1000\n" \
"v_mfma_f32_16x16x32_bf16 v[48:51], a[36:39], a[28:31], v[48:51]\n" \
"s_barrier\n" \
"v_mfma_f32_16x16x32_bf16 v[8:11], a[48:51], a[0:3], v[8:11]\n" \
"s_add_u32 m0, m0, 0x5000\n" \
"ds_read_b64_tr_b16 a[32:33], v214 offset:16384\n" \
"v_mfma_f32_16x16x32_bf16 v[8:11], a[52:55], a[4:7], v[8:11]\n" \
"ds_read_b64_tr_b16 a[34:35], v214 offset:18432\n" \
"global_load_lds_dwordx4 v216, s[38:39] offset:-4096\n" \
"v_mfma_f32_16x16x32_bf16 v[12:15], a[56:59], a[0:3], v[12:15]\n" \
"ds_read_b64_tr_b16 a[36:37], v214 offset:24576\n" \
"v_mfma_f32_16x16x32_bf16 v[12:15], a[60:63], a[4:7], v[12:15]\n" \
"ds_read_b128 a[0:3], v213 offset:0\n" \
"v_mfma_f32_16x16x32_bf16 v[28:31], a[56:59], a[8:11], v[28:31]\n" \
"ds_read_b128 a[4:7], v213 offset:2048\n" \
"v_mfma_f32_16x16x32_bf16 v[28:31], a[60:63], a[12:15], v[28:31]\n" \
"ds_read_b64_tr_b16 a[38:39], v214 offset:26624\n" \
"global_load_lds_dwordx4 v217, s[38:39] offset:-3072\n" \
"v_mfma_f32_16x16x32_bf16 v[24:27], a[48:51], a[8:11], v[24:27]\n" \
"ds_read_b64_tr_b16 a[40:41], v215 offset:16384\n" \
"v_mfma_f32_16x16x32_bf16 v[24:27], a[52:55], a[12:15], v[24:27]\n" \
"ds_read_b128 a[8:11], v213 offset:256\n" \
"v_mfma_f32_16x16x32_bf16 v[40:43], a[48:51], a[16:19], v[40:43]\n" \
"ds_read_b128 a[12:15], v213 offset:2304\n" \
"v_mfma_f32_16x16x32_bf16 v[40:43], a[52:55], a[20:23], v[40:43]\n" \
"ds_read_b64_tr_b16 a[42:43], v215 offset:18432\n" \
"global_load_lds_dwordx4 v218, s[38:39] offset:-2048\n" \
"v_mfma_f32_16x16x32_bf16 v[44:47], a[56:59], a[16:19], v[44:47]\n" \
"ds_read_b64_tr_b16 a[44:45], v215 offset:24576\n" \
"v_mfma_f32_16x16x32_bf16 v[44:47], a[60:63], a[20:23], v[44:47]\n" \
"ds_read_b128 a[16:19], v213 offset:4096\n" \
"v_mfma_f32_16x16x32_bf16 v[60:63], a[56:59], a[24:27], v[60:63]\n" \
"ds_read_b128 a[20:23], v213 offset:6144\n" \
"global_load_lds_dwordx4 v219, s[38:39] offset:-1024\n" \
"v_mfma_f32_16x16x32_bf16 v[60:63], a[60:63], a[28:31], v[60:63]\n" \
"s_add_u32 s38, s38, 0x20000\n" \
"s_addc_u32 s39, s39, 0\n" \
"ds_read_b64_tr_b16 a[46:47], v215 offset:26624\n" \
"v_mfma_f32_16x16x32_bf16 v[56:59], a[48:51], a[24:27], v[56:59]\n" \
"s_sub_u32 s75, s75, 32768\n" \
"s_cselect_b32 s75, 131072, s75\n" \
"v_mfma_f32_16x16x32_bf16 v[56:59], a[52:55], a[28:31], v[56:59]\n" \
"s_waitcnt lgkmcnt(3)\n" \
"v_mfma_f32_16x16x32_bf16 v[0:3], a[32:35], a[0:3], v[0:3]\n" \
"ds_read_b128 a[24:27], v213 offset:4352\n" \
"s_add_u32 m0, s75, s95\n" \
"v_mfma_f32_16x16x32_bf16 v[0:3], a[36:39], a[4:7], v[0:3]\n" \
"s_waitcnt lgkmcnt(1)\n" \
"ds_read_b128 a[28:31], v213 offset:6400\n" \
"global_load_lds_dwordx4 v222, s[36:37] offset:0 sc1\n" \
"v_mfma_f32_16x16x32_bf16 v[4:7], a[40:43], a[0:3], v[4:7]\n" \
"s_sub_u32 s74, s74, 32768\n" \
"s_cselect_b32 s74, 131072, s74\n" \
"ds_read_b64_tr_b16 a[48:49], v214 offset:16896\n" \
"v_mfma_f32_16x16x32_bf16 v[4:7], a[44:47], a[4:7], v[4:7]\n" \
"ds_read_b64_tr_b16 a[50:51], v214 offset:18944\n" \
"v_mfma_f32_16x16x32_bf16 v[20:23], a[40:43], a[8:11], v[20:23]\n" \
"ds_read_b64_tr_b16 a[52:53], v214 offset:25088\n" \
"v_mfma_f32_16x16x32_bf16 v[20:23], a[44:47], a[12:15], v[20:23]\n" \
"ds_read_b64_tr_b16 a[54:55], v214 offset:27136\n" \
"global_load_lds_dwordx4 v222, s[36:37] offset:1024 sc1\n" \
"v_mfma_f32_16x16x32_bf16 v[16:19], a[32:35], a[8:11], v[16:19]\n" \
"ds_read_b64_tr_b16 a[56:57], v215 offset:16896\n" \
"Lpg12165_%=:\n" \
"s_cbranch_execz Lpg13278_%=\n" \
"v_mfma_f32_16x16x32_bf16 v[16:19], a[36:39], a[12:15], v[16:19]\n" \
"ds_read_b64_tr_b16 a[58:59], v215 offset:18944\n" \
"v_mfma_f32_16x16x32_bf16 v[32:35], a[32:35], a[16:19], v[32:35]\n" \
"ds_read_b64_tr_b16 a[60:61], v215 offset:25088\n" \
"v_mfma_f32_16x16x32_bf16 v[32:35], a[36:39], a[20:23], v[32:35]\n" \
"ds_read_b64_tr_b16 a[62:63], v215 offset:27136\n" \
"global_load_lds_dwordx4 v222, s[36:37] offset:2048 sc1\n" \
"v_mfma_f32_16x16x32_bf16 v[36:39], a[40:43], a[16:19], v[36:39]\n" \
"v_add_u32 v213, s74, v211\n" \
"v_add_u32 v214, s74, v212\n" \
"v_mfma_f32_16x16x32_bf16 v[36:39], a[44:47], a[20:23], v[36:39]\n" \
"s_waitcnt lgkmcnt(3)\n" \
"v_xor_b32 v215, 32, v214\n" \
"v_mfma_f32_16x16x32_bf16 v[52:55], a[40:43], a[24:27], v[52:55]\n" \
"v_mfma_f32_16x16x32_bf16 v[52:55], a[44:47], a[28:31], v[52:55]\n" \
"global_load_lds_dwordx4 v222, s[36:37] offset:3072 sc1\n" \
"v_mfma_f32_16x16x32_bf16 v[48:51], a[32:35], a[24:27], v[48:51]\n" \
"s_waitcnt vmcnt(12) lgkmcnt(0)\n" \
"s_add_u32 s36, s36, 0x1000\n" \
"v_mfma_f32_16x16x32_bf16 v[48:51], a[36:39], a[28:31], v[48:51]\n" \
"s_barrier\n" \
"v_mfma_f32_16x16x32_bf16 v[8:11], a[48:51], a[0:3], v[8:11]\n" \
"s_add_u32 m0, m0, 0x5000\n" \
"ds_read_b64_tr_b16 a[32:33], v214 offset:16384\n" \
"v_mfma_f32_16x16x32_bf16 v[8:11], a[52:55], a[4:7], v[8:11]\n" \
"ds_read_b64_tr_b16 a[34:35], v214 offset:18432\n" \
"global_load_lds_dwordx4 v216, s[38:39] offset:-4096\n" \
"v_mfma_f32_16x16x32_bf16 v[12:15], a[56:59], a[0:3], v[12:15]\n" \
"ds_read_b64_tr_b16 a[36:37], v214 offset:24576\n" \
"v_mfma_f32_16x16x32_bf16 v[12:15], a[60:63], a[4:7], v[12:15]\n" \
"buffer_store_dword v224, v225, s[56:59], s30 offen sc1\n" \
"ds_read_b128 a[0:3], v213 offset:0\n" \
"v_mfma_f32_16x16x32_bf16 v[28:31], a[56:59], a[8:11], v[28:31]\n" \
"ds_read_b128 a[4:7], v213 offset:2048\n" \
"v_mfma_f32_16x16x32_bf16 v[28:31], a[60:63], a[12:15], v[28:31]\n" \
"ds_read_b64_tr_b16 a[38:39], v214 offset:26624\n" \
"global_load_lds_dwordx4 v217, s[38:39] offset:-3072\n" \
"v_mfma_f32_16x16x32_bf16 v[24:27], a[48:51], a[8:11], v[24:27]\n" \
"ds_read_b64_tr_b16 a[40:41], v215 offset:16384\n" \
"v_mfma_f32_16x16x32_bf16 v[24:27], a[52:55], a[12:15], v[24:27]\n" \
"ds_read_b128 a[8:11], v213 offset:256\n" \
"v_mfma_f32_16x16x32_bf16 v[40:43], a[48:51], a[16:19], v[40:43]\n" \
"ds_read_b128 a[12:15], v213 offset:2304\n" \
"v_mfma_f32_16x16x32_bf16 v[40:43], a[52:55], a[20:23], v[40:43]\n" \
"ds_read_b64_tr_b16 a[42:43], v215 offset:18432\n" \
"global_load_lds_dwordx4 v218, s[38:39] offset:-2048\n" \
"v_mfma_f32_16x16x32_bf16 v[44:47], a[56:59], a[16:19], v[44:47]\n" \
"ds_read_b64_tr_b16 a[44:45], v215 offset:24576\n" \
"v_mfma_f32_16x16x32_bf16 v[44:47], a[60:63], a[20:23], v[44:47]\n" \
"ds_read_b128 a[16:19], v213 offset:4096\n" \
"v_mfma_f32_16x16x32_bf16 v[60:63], a[56:59], a[24:27], v[60:63]\n" \
"ds_read_b128 a[20:23], v213 offset:6144\n" \
"global_load_lds_dwordx4 v219, s[38:39] offset:-1024\n" \
"v_mfma_f32_16x16x32_bf16 v[60:63], a[60:63], a[28:31], v[60:63]\n" \
"s_add_u32 s38, s38, 0x20000\n" \
"s_addc_u32 s39, s39, 0\n" \
"ds_read_b64_tr_b16 a[46:47], v215 offset:26624\n" \
"v_mfma_f32_16x16x32_bf16 v[56:59], a[48:51], a[24:27], v[56:59]\n" \
"s_sub_u32 s75, s75, 32768\n" \
"s_cselect_b32 s75, 131072, s75\n" \
"v_mfma_f32_16x16x32_bf16 v[56:59], a[52:55], a[28:31], v[56:59]\n" \
"s_waitcnt lgkmcnt(3)\n" \
"s_cmp_eq_u32 s78, 0\n" \
"s_cbranch_scc0 Lrole1_%=\n" \
"s_sub_u32 s72, s64, 14\n" \
"s_call_b64 s[68:69], Lo0_%=\n" \
"buffer_load_dword v220, off, s[56:59], s79 sc1\n" \
"s_mov_b32 s72, 2\n" \
"s_call_b64 s[68:69], Lo0_%=\n" \
"v_mfma_f32_16x16x32_bf16 v[0:3], a[32:35], a[0:3], v[0:3]\n" \
"s_lshl_b32 s71, s61, 4\n" \
"ds_read_b128 a[24:27], v213 offset:4352\n" \
"v_subrev_u32 v72, s71, v251\n" \
"v_mfma_f32_16x16x32_bf16 v[0:3], a[36:39], a[4:7], v[0:3]\n" \
"s_waitcnt lgkmcnt(1)\n" \
"ds_read_b128 a[28:31], v213 offset:6400\n" \
"v_mov_b32 v73, 0x3f80\n" \
"v_mfma_f32_16x16x32_bf16 v[4:7], a[40:43], a[0:3], v[4:7]\n" \
"s_sub_u32 s74, s74, 32768\n" \
"s_cselect_b32 s74, 131072, s74\n" \
"ds_read_b64_tr_b16 a[48:49], v214 offset:16896\n" \
"v_mfma_f32_16x16x32_bf16 v[4:7], a[44:47], a[4:7], v[4:7]\n" \
"ds_read_b64_tr_b16 a[50:51], v214 offset:18944\n" \
"v_mov_b32 v74, 0x3f800000\n" \
"v_mfma_f32_16x16x32_bf16 v[20:23], a[40:43], a[8:11], v[20:23]\n" \
"ds_read_b64_tr_b16 a[52:53], v214 offset:25088\n" \
"v_cmp_eq_u32 vcc, 0, v72\n" \
"v_mfma_f32_16x16x32_bf16 v[20:23], a[44:47], a[12:15], v[20:23]\n" \
"ds_read_b64_tr_b16 a[54:55], v214 offset:27136\n" \
"v_cmp_eq_u32 s[66:67], 1, v72\n" \
"v_mfma_f32_16x16x32_bf16 v[16:19], a[32:35], a[8:11], v[16:19]\n" \
"ds_read_b64_tr_b16 a[56:57], v215 offset:16896\n" \
"v_mov_b32 v66, 0\n" \
"v_mfma_f32_16x16x32_bf16 v[16:19], a[36:39], a[12:15], v[16:19]\n" \
"ds_read_b64_tr_b16 a[58:59], v215 offset:18944\n" \
"v_mov_b32 v68, 0\n" \
"v_mfma_f32_16x16x32_bf16 v[32:35], a[32:35], a[16:19], v[32:35]\n" \
"ds_read_b64_tr_b16 a[60:61], v215 offset:25088\n" \
"v_cndmask_b32 v64, 0, v73, vcc\n" \
"v_mfma_f32_16x16x32_bf16 v[32:35], a[36:39], a[20:23], v[32:35]\n" \
"ds_read_b64_tr_b16 a[62:63], v215 offset:27136\n" \
"v_cndmask_b32 v64, v64, v74, s[66:67]\n" \
"v_mfma_f32_16x16x32_bf16 v[36:39], a[40:43], a[16:19], v[36:39]\n" \
"v_add_u32 v213, s74, v211\n" \
"v_add_u32 v214, s74, v212\n" \
"v_mfma_f32_16x16x32_bf16 v[36:39], a[44:47], a[20:23], v[36:39]\n" \
"s_waitcnt lgkmcnt(3)\n" \
"v_xor_b32 v215, 32, v214\n" \
"v_mov_b32 v70, v64\n" \
"v_mfma_f32_16x16x32_bf16 v[52:55], a[40:43], a[24:27], v[52:55]\n" \
"v_cmp_eq_u32 vcc, 2, v72\n" \
"v_cmp_eq_u32 s[66:67], 3, v72\n" \
"v_mfma_f32_16x16x32_bf16 v[52:55], a[44:47], a[28:31], v[52:55]\n" \
"v_mov_b32 v67, 0\n" \
"v_mov_b32 v69, 0\n" \
"v_mfma_f32_16x16x32_bf16 v[48:51], a[32:35], a[24:27], v[48:51]\n" \
"s_waitcnt vmcnt(17) lgkmcnt(0)\n" \
"v_cndmask_b32 v65, 0, v73, vcc\n" \
"v_cndmask_b32 v65, v65, v74, s[66:67]\n" \
"v_mfma_f32_16x16x32_bf16 v[48:51], a[36:39], a[28:31], v[48:51]\n" \
"s_barrier\n" \
"v_mov_b32 v71, v65\n" \
"v_mfma_f32_16x16x32_bf16 v[8:11], a[48:51], a[0:3], v[8:11]\n" \
"s_lshl_b32 s83, s95, 1\n" \
"s_add_u32 s83, s83, s74\n" \
"ds_read_b64_tr_b16 a[32:33], v214 offset:16384\n" \
"v_mfma_f32_16x16x32_bf16 v[8:11], a[52:55], a[4:7], v[8:11]\n" \
"s_waitcnt vmcnt(16)\n" \
"ds_read_b64_tr_b16 a[34:35], v214 offset:18432\n" \
"v_cmp_ne_u32 vcc, s60, v220\n" \
"v_mfma_f32_16x16x32_bf16 v[12:15], a[56:59], a[0:3], v[12:15]\n" \
"ds_read_b64_tr_b16 a[36:37], v214 offset:24576\n" \
"v_mfma_f32_16x16x32_bf16 v[12:15], a[60:63], a[4:7], v[12:15]\n" \
"s_and_b32 vcc_hi, vcc_lo, 1\n" \
"s_cbranch_scc1 Lpl4_%=\n" \
"Lpr4_%=:\n" \
"ds_read_b128 a[0:3], v213 offset:0\n" \
"v_mfma_f32_16x16x32_bf16 v[28:31], a[56:59], a[8:11], v[28:31]\n" \
"ds_read_b128 a[4:7], v213 offset:2048\n" \
"v_mfma_f32_16x16x32_bf16 v[28:31], a[60:63], a[12:15], v[28:31]\n" \
"s_add_u32 s71, s76, 0\n" \
"ds_read_b64_tr_b16 a[38:39], v214 offset:26624\n" \
"buffer_load_dwordx4 v[176:179], v248, s[56:59], s71 offen offset:0 sc1\n" \
"v_mfma_f32_16x16x32_bf16 v[24:27], a[48:51], a[8:11], v[24:27]\n" \
"ds_read_b64_tr_b16 a[40:41], v215 offset:16384\n" \
"v_mfma_f32_16x16x32_bf16 v[24:27], a[52:55], a[12:15], v[24:27]\n" \
"buffer_load_dwordx4 v[180:183], v248, s[56:59], s71 offen offset:1024 sc1\n" \
"ds_read_b128 a[8:11], v213 offset:256\n" \
"v_mfma_f32_16x16x32_bf16 v[40:43], a[48:51], a[16:19], v[40:43]\n" \
"ds_read_b128 a[12:15], v213 offset:2304\n" \
"v_mfma_f32_16x16x32_bf16 v[40:43], a[52:55], a[20:23], v[40:43]\n" \
"ds_read_b64_tr_b16 a[42:43], v215 offset:18432\n" \
"v_mfma_f32_16x16x32_bf16 v[44:47], a[56:59], a[16:19], v[44:47]\n" \
"ds_read_b64_tr_b16 a[44:45], v215 offset:24576\n" \
"v_mfma_f32_16x16x32_bf16 v[44:47], a[60:63], a[20:23], v[44:47]\n" \
"ds_read_b128 a[16:19], v213 offset:4096\n" \
"v_mfma_f32_16x16x32_bf16 v[60:63], a[56:59], a[24:27], v[60:63]\n" \
"ds_read_b128 a[20:23], v213 offset:6144\n" \
"v_mfma_f32_16x16x32_bf16 v[60:63], a[60:63], a[28:31], v[60:63]\n" \
"ds_read_b64_tr_b16 a[46:47], v215 offset:26624\n" \
"v_mfma_f32_16x16x32_bf16 v[56:59], a[48:51], a[24:27], v[56:59]\n" \
"buffer_load_dwordx4 v[184:187], v248, s[56:59], s71 offen offset:2048 sc1\n" \
"v_mfma_f32_16x16x32_bf16 v[56:59], a[52:55], a[28:31], v[56:59]\n" \
"s_waitcnt lgkmcnt(3)\n" \
"buffer_load_dwordx4 v[188:191], v248, s[56:59], s71 offen offset:3072 sc1\n" \
"v_mfma_f32_16x16x32_bf16 v[0:3], a[32:35], a[0:3], v[0:3]\n" \
"ds_read_b128 a[24:27], v213 offset:4352\n" \
"s_add_u32 s71, s76, 4096\n" \
"v_mfma_f32_16x16x32_bf16 v[0:3], a[36:39], a[4:7], v[0:3]\n" \
"s_waitcnt lgkmcnt(1)\n" \
"buffer_load_dwordx4 v[192:195], v248, s[56:59], s71 offen offset:0 sc1\n" \
"ds_read_b128 a[28:31], v213 offset:6400\n" \
"v_mfma_f32_16x16x32_bf16 v[4:7], a[40:43], a[0:3], v[4:7]\n" \
"s_sub_u32 s74, s74, 32768\n" \
"s_cselect_b32 s74, 131072, s74\n" \
"ds_read_b64_tr_b16 a[48:49], v214 offset:16896\n" \
"v_mfma_f32_16x16x32_bf16 v[4:7], a[44:47], a[4:7], v[4:7]\n" \
"ds_read_b64_tr_b16 a[50:51], v214 offset:18944\n" \
"v_lshrrev_b32 v132, 4, v248\n" \
"v_mfma_f32_16x16x32_bf16 v[20:23], a[40:43], a[8:11], v[20:23]\n" \
"ds_read_b64_tr_b16 a[52:53], v214 offset:25088\n" \
"v_and_b32 v133, 15, v132\n" \
"v_mfma_f32_16x16x32_bf16 v[20:23], a[44:47], a[12:15], v[20:23]\n" \
"ds_read_b64_tr_b16 a[54:55], v214 offset:27136\n" \
"v_mul_u32_u24 v128, 144, v133\n" \
"v_mfma_f32_16x16x32_bf16 v[16:19], a[32:35], a[8:11], v[16:19]\n" \
"ds_read_b64_tr_b16 a[56:57], v215 offset:16896\n" \
"v_lshrrev_b32 v133, 4, v132\n" \
"v_mfma_f32_16x16x32_bf16 v[16:19], a[36:39], a[12:15], v[16:19]\n" \
"ds_read_b64_tr_b16 a[58:59], v215 offset:18944\n" \
"v_lshl_add_u32 v128, v133, 3, v128\n" \
"v_mfma_f32_16x16x32_bf16 v[32:35], a[32:35], a[16:19], v[32:35]\n" \
"ds_read_b64_tr_b16 a[60:61], v215 offset:25088\n" \
"v_add_u32 v128, s83, v128\n" \
"v_mfma_f32_16x16x32_bf16 v[32:35], a[36:39], a[20:23], v[32:35]\n" \
"ds_read_b64_tr_b16 a[62:63], v215 offset:27136\n" \
"v_lshrrev_b32 v133, 3, v132\n" \
"v_mfma_f32_16x16x32_bf16 v[36:39], a[40:43], a[16:19], v[36:39]\n" \
"v_add_u32 v213, s74, v211\n" \
"v_add_u32 v214, s74, v212\n" \
"v_mfma_f32_16x16x32_bf16 v[36:39], a[44:47], a[20:23], v[36:39]\n" \
"s_waitcnt lgkmcnt(3)\n" \
"v_xor_b32 v215, 32, v214\n" \
"v_and_b32 v134, 7, v132\n" \
"v_mfma_f32_16x16x32_bf16 v[52:55], a[40:43], a[24:27], v[52:55]\n" \
"buffer_load_dwordx4 v[196:199], v248, s[56:59], s71 offen offset:1024 sc1\n" \
"v_mfma_f32_16x16x32_bf16 v[52:55], a[44:47], a[28:31], v[52:55]\n" \
"buffer_load_dwordx4 v[200:203], v248, s[56:59], s71 offen offset:2048 sc1\n" \
"v_mfma_f32_16x16x32_bf16 v[48:51], a[32:35], a[24:27], v[48:51]\n" \
"s_waitcnt vmcnt(7) lgkmcnt(0)\n" \
"buffer_load_dwordx4 v[204:207], v248, s[56:59], s71 offen offset:3072 sc1\n" \
"v_mfma_f32_16x16x32_bf16 v[48:51], a[36:39], a[28:31], v[48:51]\n" \
"s_barrier\n" \
"v_mfma_f32_16x16x32_bf16 v[8:11], a[48:51], a[0:3], v[8:11]\n" \
"s_mov_b32 s70, 0x20000000\n" \
"ds_read_b128 a[192:195], v213 offset:0\n" \
"ds_read_b64_tr_b16 a[128:129], v214 offset:16384\n" \
"v_mfma_f32_16x16x32_bf16 v[8:11], a[52:55], a[4:7], v[8:11]\n" \
"s_sub_u32 s74, s74, 32768\n" \
"s_cselect_b32 s74, 131072, s74\n" \
"ds_read_b64_tr_b16 a[130:131], v214 offset:18432\n" \
"v_mfma_f32_16x16x32_bf16 v[12:15], a[56:59], a[0:3], v[12:15]\n" \
"ds_read_b128 a[196:199], v213 offset:2048\n" \
"ds_read_b64_tr_b16 a[132:133], v214 offset:24576\n" \
"v_mfma_f32_16x16x32_bf16 v[12:15], a[60:63], a[4:7], v[12:15]\n" \
"ds_read_b64_tr_b16 a[134:135], v214 offset:26624\n" \
"v_add_u32 v226, s74, v211\n" \
"v_mfma_f32_16x16x32_bf16 v[28:31], a[56:59], a[8:11], v[28:31]\n" \
"ds_read_b128 a[200:203], v213 offset:256\n" \
"ds_read_b64_tr_b16 a[136:137], v215 offset:16384\n" \
"v_mfma_f32_16x16x32_bf16 v[28:31], a[60:63], a[12:15], v[28:31]\n" \
"ds_read_b64_tr_b16 a[138:139], v215 offset:18432\n" \
"v_add_u32 v227, s74, v212\n" \
"v_mfma_f32_16x16x32_bf16 v[24:27], a[48:51], a[8:11], v[24:27]\n" \
"ds_read_b128 a[204:207], v213 offset:2304\n" \
"ds_read_b64_tr_b16 a[140:141], v215 offset:24576\n" \
"v_mfma_f32_16x16x32_bf16 v[24:27], a[52:55], a[12:15], v[24:27]\n" \
"ds_read_b64_tr_b16 a[142:143], v215 offset:26624\n" \
"v_xor_b32 v228, 32, v227\n" \
"v_mfma_f32_16x16x32_bf16 v[40:43], a[48:51], a[16:19], v[40:43]\n" \
"s_cmp_eq_u32 s79, 65601536\n" \
"s_cselect_b32 s70, 65593344, s70\n" \
"ds_read_b64_tr_b16 a[144:145], v214 offset:16896\n" \
"v_mfma_f32_16x16x32_bf16 v[40:43], a[52:55], a[20:23], v[40:43]\n" \
"buffer_store_dword v224, v225, s[56:59], s70 offen sc1\n" \
"ds_read_b64_tr_b16 a[146:147], v214 offset:18944\n" \
"v_mfma_f32_16x16x32_bf16 v[44:47], a[56:59], a[16:19], v[44:47]\n" \
"ds_read_b64_tr_b16 a[148:149], v214 offset:25088\n" \
"v_mul_u32_u24 v129, 144, v133\n" \
"v_mfma_f32_16x16x32_bf16 v[44:47], a[60:63], a[20:23], v[44:47]\n" \
"ds_read_b64_tr_b16 a[150:151], v214 offset:27136\n" \
"v_lshl_add_u32 v129, v134, 4, v129\n" \
"v_mfma_f32_16x16x32_bf16 v[60:63], a[56:59], a[24:27], v[60:63]\n" \
"ds_read_b64_tr_b16 a[152:153], v215 offset:16896\n" \
"v_add_u32 v129, s83, v129\n" \
"v_mfma_f32_16x16x32_bf16 v[60:63], a[60:63], a[28:31], v[60:63]\n" \
"ds_read_b64_tr_b16 a[154:155], v215 offset:18944\n" \
"v_lshlrev_b32 v134, 4, v134\n" \
"v_mfma_f32_16x16x32_bf16 v[56:59], a[48:51], a[24:27], v[56:59]\n" \
"ds_read_b64_tr_b16 a[156:157], v215 offset:25088\n" \
"v_lshl_add_u32 v130, v133, 12, v134\n" \
"v_mfma_f32_16x16x32_bf16 v[56:59], a[52:55], a[28:31], v[56:59]\n" \
"s_waitcnt lgkmcnt(4)\n" \
"ds_read_b64_tr_b16 a[158:159], v215 offset:27136\n" \
"v_mfma_f32_16x16x32_bf16 v[0:3], a[128:131], a[192:195], v[0:3]\n" \
"ds_read_b128 a[224:227], v226 offset:0\n" \
"ds_read_b64_tr_b16 a[160:161], v227 offset:16384\n" \
"v_mfma_f32_16x16x32_bf16 v[0:3], a[132:135], a[196:199], v[0:3]\n" \
"ds_read_b64_tr_b16 a[162:163], v227 offset:18432\n" \
"v_mfma_f32_16x16x32_bf16 v[16:19], a[128:131], a[200:203], v[16:19]\n" \
"ds_read_b64_tr_b16 a[164:165], v227 offset:24576\n" \
"v_mfma_f32_16x16x32_bf16 v[16:19], a[132:135], a[204:207], v[16:19]\n" \
"ds_read_b128 a[228:231], v226 offset:2048\n" \
"ds_read_b64_tr_b16 a[166:167], v227 offset:26624\n" \
"v_mfma_f32_16x16x32_bf16 v[20:23], a[136:139], a[200:203], v[20:23]\n" \
"ds_read_b64_tr_b16 a[168:169], v228 offset:16384\n" \
"v_mfma_f32_16x16x32_bf16 v[20:23], a[140:143], a[204:207], v[20:23]\n" \
"ds_read_b64_tr_b16 a[170:171], v228 offset:18432\n" \
"v_mfma_f32_16x16x32_bf16 v[4:7], a[136:139], a[192:195], v[4:7]\n" \
"ds_read_b128 a[232:235], v226 offset:256\n" \
"ds_read_b64_tr_b16 a[172:173], v228 offset:24576\n" \
"v_mfma_f32_16x16x32_bf16 v[4:7], a[140:143], a[196:199], v[4:7]\n" \
"s_waitcnt lgkmcnt(6)\n" \
"ds_read_b64_tr_b16 a[174:175], v228 offset:26624\n" \
"v_mfma_f32_16x16x32_bf16 v[8:11], a[144:147], a[192:195], v[8:11]\n" \
"ds_read_b64_tr_b16 a[176:177], v227 offset:16896\n" \
"v_mfma_f32_16x16x32_bf16 v[8:11], a[148:151], a[196:199], v[8:11]\n" \
"ds_read_b128 a[236:239], v226 offset:2304\n" \
"ds_read_b64_tr_b16 a[178:179], v227 offset:18944\n" \
"v_mfma_f32_16x16x32_bf16 v[24:27], a[144:147], a[200:203], v[24:27]\n" \
"ds_read_b64_tr_b16 a[180:181], v227 offset:25088\n" \
"v_mfma_f32_16x16x32_bf16 v[24:27], a[148:151], a[204:207], v[24:27]\n" \
"ds_read_b64_tr_b16 a[182:183], v227 offset:27136\n" \
"v_mfma_f32_16x16x32_bf16 v[28:31], a[152:155], a[200:203], v[28:31]\n" \
"ds_read_b64_tr_b16 a[184:185], v228 offset:16896\n" \
"v_mfma_f32_16x16x32_bf16 v[28:31], a[156:159], a[204:207], v[28:31]\n" \
"ds_read_b64_tr_b16 a[186:187], v228 offset:18944\n" \
"v_mfma_f32_16x16x32_bf16 v[12:15], a[152:155], a[192:195], v[12:15]\n" \
"ds_read_b64_tr_b16 a[188:189], v228 offset:25088\n" \
"v_mfma_f32_16x16x32_bf16 v[12:15], a[156:159], a[196:199], v[12:15]\n" \
"s_waitcnt lgkmcnt(4)\n" \
"ds_read_b64_tr_b16 a[190:191], v228 offset:27136\n" \
"v_mfma_f32_16x16x32_bf16 v[0:3], a[160:163], a[224:227], v[0:3]\n" \
"s_waitcnt vmcnt(1)\n" \
"ds_read_b128 a[208:211], v213 offset:4096\n" \
"v_mfma_f32_16x16x32_bf16 v[0:3], a[164:167], a[228:231], v[0:3]\n" \
"ds_read_b128 a[212:215], v213 offset:6144\n" \
"v_mfma_f32_16x16x32_bf16 v[0:3], v[64:67], v[176:179], v[0:3]\n" \
"ds_read_b128 a[240:243], v226 offset:4096\n" \
"v_mfma_f32_16x16x32_bf16 v[16:19], a[160:163], a[232:235], v[16:19]\n" \
"ds_read_b128 a[244:247], v226 offset:6144\n" \
"v_mfma_f32_16x16x32_bf16 v[16:19], a[164:167], a[236:239], v[16:19]\n" \
"ds_read_b128 a[216:219], v213 offset:4352\n" \
"v_mfma_f32_16x16x32_bf16 v[16:19], v[64:67], v[184:187], v[16:19]\n" \
"ds_read_b128 a[220:223], v213 offset:6400\n" \
"v_cvt_pk_bf16_f32 v0, v0, v1\n" \
"v_mfma_f32_16x16x32_bf16 v[20:23], a[168:171], a[232:235], v[20:23]\n" \
"ds_read_b128 a[248:251], v226 offset:4352\n" \
"v_cvt_pk_bf16_f32 v1, v2, v3\n" \
"v_mfma_f32_16x16x32_bf16 v[20:23], a[172:175], a[236:239], v[20:23]\n" \
"ds_read_b128 a[252:255], v226 offset:6400\n" \
"v_mfma_f32_16x16x32_bf16 v[20:23], v[68:71], v[184:187], v[20:23]\n" \
"ds_write_b64 v128, v[0:1] offset:0\n" \
"v_mfma_f32_16x16x32_bf16 v[4:7], a[168:171], a[224:227], v[4:7]\n" \
"v_cvt_pk_bf16_f32 v16, v16, v17\n" \
"v_cvt_pk_bf16_f32 v17, v18, v19\n" \
"v_mfma_f32_16x16x32_bf16 v[4:7], a[172:175], a[228:231], v[4:7]\n" \
"ds_write_b64 v128, v[16:17] offset:2304\n" \
"v_mfma_f32_16x16x32_bf16 v[4:7], v[68:71], v[176:179], v[4:7]\n" \
"s_waitcnt lgkmcnt(3)\n" \
"v_cvt_pk_bf16_f32 v20, v20, v21\n" \
"v_cvt_pk_bf16_f32 v21, v22, v23\n" \
"v_mfma_f32_16x16x32_bf16 v[8:11], a[176:179], a[224:227], v[8:11]\n" \
"ds_write_b64 v128, v[20:21] offset:2336\n" \
"v_mfma_f32_16x16x32_bf16 v[8:11], a[180:183], a[228:231], v[8:11]\n" \
"v_mfma_f32_16x16x32_bf16 v[8:11], v[64:67], v[180:183], v[8:11]\n" \
"v_cvt_pk_bf16_f32 v4, v4, v5\n" \
"v_cvt_pk_bf16_f32 v5, v6, v7\n" \
"v_mfma_f32_16x16x32_bf16 v[24:27], a[176:179], a[232:235], v[24:27]\n" \
"ds_write_b64 v128, v[4:5] offset:32\n" \
"v_mfma_f32_16x16x32_bf16 v[24:27], a[180:183], a[236:239], v[24:27]\n" \
"v_mfma_f32_16x16x32_bf16 v[24:27], v[64:67], v[188:191], v[24:27]\n" \
"v_cvt_pk_bf16_f32 v8, v8, v9\n" \
"v_cvt_pk_bf16_f32 v9, v10, v11\n" \
"v_mfma_f32_16x16x32_bf16 v[28:31], a[184:187], a[232:235], v[28:31]\n" \
"ds_write_b64 v128, v[8:9] offset:64\n" \
"v_mfma_f32_16x16x32_bf16 v[28:31], a[188:191], a[236:239], v[28:31]\n" \
"v_mfma_f32_16x16x32_bf16 v[28:31], v[68:71], v[188:191], v[28:31]\n" \
"v_cvt_pk_bf16_f32 v24, v24, v25\n" \
"v_cvt_pk_bf16_f32 v25, v26, v27\n" \
"v_mfma_f32_16x16x32_bf16 v[12:15], a[184:187], a[224:227], v[12:15]\n" \
"ds_write_b64 v128, v[24:25] offset:2368\n" \
"v_mfma_f32_16x16x32_bf16 v[12:15], a[188:191], a[228:231], v[12:15]\n" \
"v_mfma_f32_16x16x32_bf16 v[12:15], v[68:71], v[180:183], v[12:15]\n" \
"v_cvt_pk_bf16_f32 v28, v28, v29\n" \
"v_cvt_pk_bf16_f32 v29, v30, v31\n" \
"v_mfma_f32_16x16x32_bf16 v[32:35], a[128:131], a[208:211], v[32:35]\n" \
"ds_write_b64 v128, v[28:29] offset:2400\n" \
"v_mfma_f32_16x16x32_bf16 v[32:35], a[132:135], a[212:215], v[32:35]\n" \
"v_mfma_f32_16x16x32_bf16 v[32:35], a[160:163], a[240:243], v[32:35]\n" \
"v_cvt_pk_bf16_f32 v12, v12, v13\n" \
"v_cvt_pk_bf16_f32 v13, v14, v15\n" \
"v_mfma_f32_16x16x32_bf16 v[32:35], a[164:167], a[244:247], v[32:35]\n" \
"ds_write_b64 v128, v[12:13] offset:96\n" \
"v_mfma_f32_16x16x32_bf16 v[32:35], v[64:67], v[192:195], v[32:35]\n" \
"ds_read_b128 v[140:143], v129 offset:0\n" \
"v_mfma_f32_16x16x32_bf16 v[36:39], a[168:171], a[240:243], v[36:39]\n" \
"ds_read_b128 v[144:147], v129 offset:1152\n" \
"v_mfma_f32_16x16x32_bf16 v[36:39], a[172:175], a[244:247], v[36:39]\n" \
"ds_read_b128 v[148:151], v129 offset:2304\n" \
"v_mfma_f32_16x16x32_bf16 v[36:39], a[136:139], a[208:211], v[36:39]\n" \
"ds_read_b128 v[152:155], v129 offset:3456\n" \
"v_mfma_f32_16x16x32_bf16 v[36:39], a[140:143], a[212:215], v[36:39]\n" \
"v_cvt_pk_bf16_f32 v32, v32, v33\n" \
"v_cvt_pk_bf16_f32 v33, v34, v35\n" \
"v_mfma_f32_16x16x32_bf16 v[36:39], v[68:71], v[192:195], v[36:39]\n" \
"ds_write_b64 v128, v[32:33] offset:0\n" \
"v_mfma_f32_16x16x32_bf16 v[40:43], a[144:147], a[208:211], v[40:43]\n" \
"v_mfma_f32_16x16x32_bf16 v[40:43], a[148:151], a[212:215], v[40:43]\n" \
"s_add_u32 s70, s77, 0\n" \
"s_waitcnt lgkmcnt(4)\n" \
"v_mfma_f32_16x16x32_bf16 v[40:43], a[176:179], a[240:243], v[40:43]\n" \
"v_cvt_pk_bf16_f32 v36, v36, v37\n" \
"v_cvt_pk_bf16_f32 v37, v38, v39\n" \
"v_mfma_f32_16x16x32_bf16 v[40:43], a[180:183], a[244:247], v[40:43]\n" \
"buffer_store_dwordx4 v[140:143], v130, s[52:55], s70 offen sc1\n" \
"ds_write_b64 v128, v[36:37] offset:32\n" \
"v_mfma_f32_16x16x32_bf16 v[40:43], v[64:67], v[196:199], v[40:43]\n" \
"v_mfma_f32_16x16x32_bf16 v[44:47], a[184:187], a[240:243], v[44:47]\n" \
"v_mfma_f32_16x16x32_bf16 v[44:47], a[188:191], a[244:247], v[44:47]\n" \
"v_mfma_f32_16x16x32_bf16 v[44:47], a[152:155], a[208:211], v[44:47]\n" \
"s_add_u32 s70, s77, 32768\n" \
"v_cvt_pk_bf16_f32 v40, v40, v41\n" \
"v_cvt_pk_bf16_f32 v41, v42, v43\n" \
"v_mfma_f32_16x16x32_bf16 v[44:47], a[156:159], a[212:215], v[44:47]\n" \
"s_waitcnt lgkmcnt(4)\n" \
"buffer_store_dwordx4 v[144:147], v130, s[52:55], s70 offen sc1\n" \
"ds_write_b64 v128, v[40:41] offset:64\n" \
"v_mfma_f32_16x16x32_bf16 v[44:47], v[68:71], v[196:199], v[44:47]\n" \
"v_mfma_f32_16x16x32_bf16 v[48:51], a[128:131], a[216:219], v[48:51]\n" \
"v_mfma_f32_16x16x32_bf16 v[48:51], a[132:135], a[220:223], v[48:51]\n" \
"v_mfma_f32_16x16x32_bf16 v[48:51], a[160:163], a[248:251], v[48:51]\n" \
"s_add_u32 s70, s77, 65536\n" \
"v_cvt_pk_bf16_f32 v44, v44, v45\n" \
"v_cvt_pk_bf16_f32 v45, v46, v47\n" \
"v_mfma_f32_16x16x32_bf16 v[48:51], a[164:167], a[252:255], v[48:51]\n" \
"s_waitcnt lgkmcnt(4)\n" \
"buffer_store_dwordx4 v[148:151], v130, s[52:55], s70 offen sc1\n" \
"ds_write_b64 v128, v[44:45] offset:96\n" \
"v_mfma_f32_16x16x32_bf16 v[48:51], v[64:67], v[200:203], v[48:51]\n" \
"ds_read_b128 v[156:159], v129 offset:0\n" \
"v_mfma_f32_16x16x32_bf16 v[52:55], a[168:171], a[248:251], v[52:55]\n" \
"ds_read_b128 v[160:163], v129 offset:1152\n" \
"v_mfma_f32_16x16x32_bf16 v[52:55], a[172:175], a[252:255], v[52:55]\n" \
"v_mfma_f32_16x16x32_bf16 v[52:55], a[136:139], a[216:219], v[52:55]\n" \
"s_add_u32 s70, s77, 98304\n" \
"v_cvt_pk_bf16_f32 v48, v48, v49\n" \
"v_cvt_pk_bf16_f32 v49, v50, v51\n" \
"v_mfma_f32_16x16x32_bf16 v[52:55], a[140:143], a[220:223], v[52:55]\n" \
"s_waitcnt lgkmcnt(6)\n" \
"buffer_store_dwordx4 v[152:155], v130, s[52:55], s70 offen sc1\n" \
"ds_write_b64 v128, v[48:49] offset:2304\n" \
"v_mfma_f32_16x16x32_bf16 v[52:55], v[68:71], v[200:203], v[52:55]\n" \
"v_mfma_f32_16x16x32_bf16 v[56:59], a[144:147], a[216:219], v[56:59]\n" \
"v_mfma_f32_16x16x32_bf16 v[56:59], a[148:151], a[220:223], v[56:59]\n" \
"v_mfma_f32_16x16x32_bf16 v[56:59], a[176:179], a[248:251], v[56:59]\n" \
"s_add_u32 s70, s77, 131072\n" \
"v_cvt_pk_bf16_f32 v52, v52, v53\n" \
"v_cvt_pk_bf16_f32 v53, v54, v55\n" \
"v_mfma_f32_16x16x32_bf16 v[56:59], a[180:183], a[252:255], v[56:59]\n" \
"s_waitcnt lgkmcnt(2)\n" \
"buffer_store_dwordx4 v[156:159], v130, s[52:55], s70 offen sc1\n" \
"ds_write_b64 v128, v[52:53] offset:2336\n" \
"v_mfma_f32_16x16x32_bf16 v[56:59], v[64:67], v[204:207], v[56:59]\n" \
"v_mfma_f32_16x16x32_bf16 v[60:63], a[184:187], a[248:251], v[60:63]\n" \
"v_mfma_f32_16x16x32_bf16 v[60:63], a[188:191], a[252:255], v[60:63]\n" \
"v_mfma_f32_16x16x32_bf16 v[60:63], a[152:155], a[216:219], v[60:63]\n" \
"s_add_u32 s70, s77, 163840\n" \
"v_cvt_pk_bf16_f32 v56, v56, v57\n" \
"v_cvt_pk_bf16_f32 v57, v58, v59\n" \
"v_mfma_f32_16x16x32_bf16 v[60:63], a[156:159], a[220:223], v[60:63]\n" \
"s_waitcnt lgkmcnt(2)\n" \
"buffer_store_dwordx4 v[160:163], v130, s[52:55], s70 offen sc1\n" \
"ds_write_b64 v128, v[56:57] offset:2368\n" \
"v_mfma_f32_16x16x32_bf16 v[60:63], v[68:71], v[204:207], v[60:63]\n" \
"s_nop 7\n" \
"v_cvt_pk_bf16_f32 v60, v60, v61\n" \
"v_cvt_pk_bf16_f32 v61, v62, v63\n" \
"ds_write_b64 v128, v[60:61] offset:2400\n" \
"ds_read_b128 v[164:167], v129 offset:2304\n" \
"ds_read_b128 v[168:171], v129 offset:3456\n" \
"s_add_u32 s70, s77, 196608\n" \
"s_waitcnt lgkmcnt(1)\n" \
"buffer_store_dwordx4 v[164:167], v130, s[52:55], s70 offen sc1\n" \
"s_add_u32 s70, s77, 229376\n" \
"s_waitcnt lgkmcnt(0)\n" \
"buffer_store_dwordx4 v[168:171], v130, s[52:55], s70 offen sc1\n" \
"s_endpgm\n" \
"Lpg12624_%=:\n" \
"s_cbranch_execz Lpg13597_%=\n" \
"Lo0_%=:\n" \
"v_mfma_f32_16x16x32_bf16 v[0:3], a[32:35], a[0:3], v[0:3]\n" \
"ds_read_b128 a[24:27], v213 offset:4352\n" \
"s_add_u32 m0, s75, s95\n" \
"v_mfma_f32_16x16x32_bf16 v[0:3], a[36:39], a[4:7], v[0:3]\n" \
"s_waitcnt lgkmcnt(1)\n" \
"ds_read_b128 a[28:31], v213 offset:6400\n" \
"global_load_lds_dwordx4 v222, s[36:37] offset:0 sc1\n" \
"v_mfma_f32_16x16x32_bf16 v[4:7], a[40:43], a[0:3], v[4:7]\n" \
"s_sub_u32 s74, s74, 32768\n" \
"s_cselect_b32 s74, 131072, s74\n" \
"ds_read_b64_tr_b16 a[48:49], v214 offset:16896\n" \
"v_mfma_f32_16x16x32_bf16 v[4:7], a[44:47], a[4:7], v[4:7]\n" \
"ds_read_b64_tr_b16 a[50:51], v214 offset:18944\n" \
"v_mfma_f32_16x16x32_bf16 v[20:23], a[40:43], a[8:11], v[20:23]\n" \
"ds_read_b64_tr_b16 a[52:53], v214 offset:25088\n" \
"v_mfma_f32_16x16x32_bf16 v[20:23], a[44:47], a[12:15], v[20:23]\n" \
"ds_read_b64_tr_b16 a[54:55], v214 offset:27136\n" \
"global_load_lds_dwordx4 v222, s[36:37] offset:1024 sc1\n" \
"v_mfma_f32_16x16x32_bf16 v[16:19], a[32:35], a[8:11], v[16:19]\n" \
"ds_read_b64_tr_b16 a[56:57], v215 offset:16896\n" \
"v_mfma_f32_16x16x32_bf16 v[16:19], a[36:39], a[12:15], v[16:19]\n" \
"ds_read_b64_tr_b16 a[58:59], v215 offset:18944\n" \
"v_mfma_f32_16x16x32_bf16 v[32:35], a[32:35], a[16:19], v[32:35]\n" \
"ds_read_b64_tr_b16 a[60:61], v215 offset:25088\n" \
"v_mfma_f32_16x16x32_bf16 v[32:35], a[36:39], a[20:23], v[32:35]\n" \
"ds_read_b64_tr_b16 a[62:63], v215 offset:27136\n" \
"global_load_lds_dwordx4 v222, s[36:37] offset:2048 sc1\n" \
"v_mfma_f32_16x16x32_bf16 v[36:39], a[40:43], a[16:19], v[36:39]\n" \
"v_add_u32 v213, s74, v211\n" \
"v_add_u32 v214, s74, v212\n" \
"v_mfma_f32_16x16x32_bf16 v[36:39], a[44:47], a[20:23], v[36:39]\n" \
"s_waitcnt lgkmcnt(3)\n" \
"v_xor_b32 v215, 32, v214\n" \
"v_mfma_f32_16x16x32_bf16 v[52:55], a[40:43], a[24:27], v[52:55]\n" \
"v_mfma_f32_16x16x32_bf16 v[52:55], a[44:47], a[28:31], v[52:55]\n" \
"global_load_lds_dwordx4 v222, s[36:37] offset:3072 sc1\n" \
"v_mfma_f32_16x16x32_bf16 v[48:51], a[32:35], a[24:27], v[48:51]\n" \
"s_waitcnt vmcnt(20) lgkmcnt(0)\n" \
"s_add_u32 s36, s36, 0x1000\n" \
"v_mfma_f32_16x16x32_bf16 v[48:51], a[36:39], a[28:31], v[48:51]\n" \
"s_barrier\n" \
"v_mfma_f32_16x16x32_bf16 v[8:11], a[48:51], a[0:3], v[8:11]\n" \
"s_add_u32 m0, m0, 0x5000\n" \
"ds_read_b64_tr_b16 a[32:33], v214 offset:16384\n" \
"v_mfma_f32_16x16x32_bf16 v[8:11], a[52:55], a[4:7], v[8:11]\n" \
"ds_read_b64_tr_b16 a[34:35], v214 offset:18432\n" \
"global_load_lds_dwordx4 v216, s[38:39] offset:-4096\n" \
"v_mfma_f32_16x16x32_bf16 v[12:15], a[56:59], a[0:3], v[12:15]\n" \
"ds_read_b64_tr_b16 a[36:37], v214 offset:24576\n" \
"v_mfma_f32_16x16x32_bf16 v[12:15], a[60:63], a[4:7], v[12:15]\n" \
"ds_read_b128 a[0:3], v213 offset:0\n" \
"v_mfma_f32_16x16x32_bf16 v[28:31], a[56:59], a[8:11], v[28:31]\n" \
"ds_read_b128 a[4:7], v213 offset:2048\n" \
"v_mfma_f32_16x16x32_bf16 v[28:31], a[60:63], a[12:15], v[28:31]\n" \
"ds_read_b64_tr_b16 a[38:39], v214 offset:26624\n" \
"global_load_lds_dwordx4 v217, s[38:39] offset:-3072\n" \
"v_mfma_f32_16x16x32_bf16 v[24:27], a[48:51], a[8:11], v[24:27]\n" \
"ds_read_b64_tr_b16 a[40:41], v215 offset:16384\n" \
"v_mfma_f32_16x16x32_bf16 v[24:27], a[52:55], a[12:15], v[24:27]\n" \
"ds_read_b128 a[8:11], v213 offset:256\n" \
"v_mfma_f32_16x16x32_bf16 v[40:43], a[48:51], a[16:19], v[40:43]\n" \
"ds_read_b128 a[12:15], v213 offset:2304\n" \
"v_mfma_f32_16x16x32_bf16 v[40:43], a[52:55], a[20:23], v[40:43]\n" \
"ds_read_b64_tr_b16 a[42:43], v215 offset:18432\n" \
"global_load_lds_dwordx4 v218, s[38:39] offset:-2048\n" \
"v_mfma_f32_16x16x32_bf16 v[44:47], a[56:59], a[16:19], v[44:47]\n" \
"ds_read_b64_tr_b16 a[44:45], v215 offset:24576\n" \
"v_mfma_f32_16x16x32_bf16 v[44:47], a[60:63], a[20:23], v[44:47]\n" \
"ds_read_b128 a[16:19], v213 offset:4096\n" \
"global_load_lds_dwordx4 v219, s[38:39] offset:-1024\n" \
"v_mfma_f32_16x16x32_bf16 v[60:63], a[56:59], a[24:27], v[60:63]\n" \
"s_add_u32 s38, s38, 0x20000\n" \
"s_addc_u32 s39, s39, 0\n" \
"ds_read_b128 a[20:23], v213 offset:6144\n" \
"v_mfma_f32_16x16x32_bf16 v[60:63], a[60:63], a[28:31], v[60:63]\n" \
"s_sub_u32 s75, s75, 32768\n" \
"s_cselect_b32 s75, 131072, s75\n" \
"ds_read_b64_tr_b16 a[46:47], v215 offset:26624\n" \
"v_mfma_f32_16x16x32_bf16 v[56:59], a[48:51], a[24:27], v[56:59]\n" \
"s_sub_u32 s72, s72, 1\n" \
"s_cmp_lg_u32 s72, 0\n" \
"v_mfma_f32_16x16x32_bf16 v[56:59], a[52:55], a[28:31], v[56:59]\n" \
"s_waitcnt lgkmcnt(3)\n" \
"s_cbranch_scc1 Lo0_%=\n" \
"s_setpc_b64 s[68:69]\n" \
"Lrole1_%=:\n" \
"Lpg12712_%=:\n" \
"s_cbranch_execz Lcb1_%=\n" \
"s_sub_u32 s72, 50, s64\n" \
"s_call_b64 s[68:69], Lo0_%=\n" \
"buffer_load_dword v220, v221, s[56:59], s82 offen sc1\n" \
"s_mov_b32 s72, 2\n" \
"s_call_b64 s[68:69], Lo0_%=\n" \
"s_waitcnt vmcnt(16)\n" \
"v_cmp_ne_u32 vcc, s60, v220\n" \
"s_nop 1\n" \
"s_and_b32 vcc_hi, vcc_lo, 65535\n" \
"s_cbranch_scc1 Lpl5_%=\n" \
"Lpr5_%=:\n" \
"s_mov_b32 s36, s73\n" \
"s_mov_b64 s[38:39], s[40:41]\n" \
"s_mov_b32 s72, 4\n" \
"s_call_b64 s[68:69], Lo0_%=\n" \
"v_mfma_f32_16x16x32_bf16 v[64:67], a[32:35], a[0:3], 0\n" \
"ds_read_b128 a[24:27], v213 offset:4352\n" \
"s_add_u32 m0, s75, s95\n" \
"v_mfma_f32_16x16x32_bf16 v[64:67], a[36:39], a[4:7], v[64:67]\n" \
"s_waitcnt lgkmcnt(1)\n" \
"ds_read_b128 a[28:31], v213 offset:6400\n" \
"global_load_lds_dwordx4 v222, s[36:37] offset:0 sc1\n" \
"Lpg12735_%=:\n" \
"s_cbranch_execz Lcb1_%=\n" \
"v_mfma_f32_16x16x32_bf16 v[68:71], a[40:43], a[0:3], 0\n" \
"s_sub_u32 s74, s74, 32768\n" \
"s_cselect_b32 s74, 131072, s74\n" \
"ds_read_b64_tr_b16 a[48:49], v214 offset:16896\n" \
"v_mfma_f32_16x16x32_bf16 v[68:71], a[44:47], a[4:7], v[68:71]\n" \
"ds_read_b64_tr_b16 a[50:51], v214 offset:18944\n" \
"v_cvt_pk_bf16_f32 v0, v0, v1\n" \
"v_mfma_f32_16x16x32_bf16 v[84:87], a[40:43], a[8:11], 0\n" \
"ds_read_b64_tr_b16 a[52:53], v214 offset:25088\n" \
"v_cvt_pk_bf16_f32 v1, v2, v3\n" \
"v_mfma_f32_16x16x32_bf16 v[84:87], a[44:47], a[12:15], v[84:87]\n" \
"ds_read_b64_tr_b16 a[54:55], v214 offset:27136\n" \
"global_load_lds_dwordx4 v222, s[36:37] offset:1024 sc1\n" \
"v_mfma_f32_16x16x32_bf16 v[80:83], a[32:35], a[8:11], 0\n" \
"ds_read_b64_tr_b16 a[56:57], v215 offset:16896\n" \
"v_cvt_pk_bf16_f32 v2, v4, v5\n" \
"v_mfma_f32_16x16x32_bf16 v[80:83], a[36:39], a[12:15], v[80:83]\n" \
"ds_read_b64_tr_b16 a[58:59], v215 offset:18944\n" \
"v_cvt_pk_bf16_f32 v3, v6, v7\n" \
"v_mfma_f32_16x16x32_bf16 v[96:99], a[32:35], a[16:19], 0\n" \
"s_add_u32 s71, s76, 0\n" \
"ds_read_b64_tr_b16 a[60:61], v215 offset:25088\n" \
"v_mfma_f32_16x16x32_bf16 v[96:99], a[36:39], a[20:23], v[96:99]\n" \
"ds_read_b64_tr_b16 a[62:63], v215 offset:27136\n" \
"global_load_lds_dwordx4 v222, s[36:37] offset:2048 sc1\n" \
"v_mfma_f32_16x16x32_bf16 v[100:103], a[40:43], a[16:19], 0\n" \
"v_add_u32 v213, s74, v211\n" \
"v_add_u32 v214, s74, v212\n" \
"v_mfma_f32_16x16x32_bf16 v[100:103], a[44:47], a[20:23], v[100:103]\n" \
"s_waitcnt lgkmcnt(3)\n" \
"v_xor_b32 v215, 32, v214\n" \
"v_mfma_f32_16x16x32_bf16 v[116:119], a[40:43], a[24:27], 0\n" \
"v_mfma_f32_16x16x32_bf16 v[116:119], a[44:47], a[28:31], v[116:119]\n" \
"global_load_lds_dwordx4 v222, s[36:37] offset:3072 sc1\n" \
"v_mfma_f32_16x16x32_bf16 v[112:115], a[32:35], a[24:27], 0\n" \
"s_waitcnt vmcnt(20) lgkmcnt(0)\n" \
"s_add_u32 s36, s36, 0x1000\n" \
"v_mfma_f32_16x16x32_bf16 v[112:115], a[36:39], a[28:31], v[112:115]\n" \
"s_barrier\n" \
"v_mfma_f32_16x16x32_bf16 v[72:75], a[48:51], a[0:3], 0\n" \
"s_add_u32 m0, m0, 0x5000\n" \
"ds_read_b64_tr_b16 a[32:33], v214 offset:16384\n" \
"v_mfma_f32_16x16x32_bf16 v[72:75], a[52:55], a[4:7], v[72:75]\n" \
"ds_read_b64_tr_b16 a[34:35], v214 offset:18432\n" \
"global_load_lds_dwordx4 v216, s[38:39] offset:-4096\n" \
"v_mfma_f32_16x16x32_bf16 v[76:79], a[56:59], a[0:3], 0\n" \
"ds_read_b64_tr_b16 a[36:37], v214 offset:24576\n" \
"v_mfma_f32_16x16x32_bf16 v[76:79], a[60:63], a[4:7], v[76:79]\n" \
"ds_read_b128 a[0:3], v213 offset:0\n" \
"v_mfma_f32_16x16x32_bf16 v[92:95], a[56:59], a[8:11], 0\n" \
"ds_read_b128 a[4:7], v213 offset:2048\n" \
"v_mfma_f32_16x16x32_bf16 v[92:95], a[60:63], a[12:15], v[92:95]\n" \
"ds_read_b64_tr_b16 a[38:39], v214 offset:26624\n" \
"global_load_lds_dwordx4 v217, s[38:39] offset:-3072\n" \
"v_mfma_f32_16x16x32_bf16 v[88:91], a[48:51], a[8:11], 0\n" \
"ds_read_b64_tr_b16 a[40:41], v215 offset:16384\n" \
"v_mfma_f32_16x16x32_bf16 v[88:91], a[52:55], a[12:15], v[88:91]\n" \
"buffer_store_dwordx4 v[0:3], v248, s[56:59], s71 offen offset:0 sc1\n" \
"ds_read_b128 a[8:11], v213 offset:256\n" \
"v_cvt_pk_bf16_f32 v4, v8, v9\n" \
"v_cvt_pk_bf16_f32 v5, v10, v11\n" \
"v_cvt_pk_bf16_f32 v6, v12, v13\n" \
"v_mfma_f32_16x16x32_bf16 v[104:107], a[48:51], a[16:19], 0\n" \
"ds_read_b128 a[12:15], v213 offset:2304\n" \
"v_cvt_pk_bf16_f32 v7, v14, v15\n" \
"v_mfma_f32_16x16x32_bf16 v[104:107], a[52:55], a[20:23], v[104:107]\n" \
"ds_read_b64_tr_b16 a[42:43], v215 offset:18432\n" \
"global_load_lds_dwordx4 v218, s[38:39] offset:-2048\n" \
"v_mfma_f32_16x16x32_bf16 v[108:111], a[56:59], a[16:19], 0\n" \
"ds_read_b64_tr_b16 a[44:45], v215 offset:24576\n" \
"v_mfma_f32_16x16x32_bf16 v[108:111], a[60:63], a[20:23], v[108:111]\n" \
"buffer_store_dwordx4 v[4:7], v248, s[56:59], s71 offen offset:1024 sc1\n" \
"ds_read_b128 a[16:19], v213 offset:4096\n" \
"v_cvt_pk_bf16_f32 v8, v16, v17\n" \
"v_cvt_pk_bf16_f32 v9, v18, v19\n" \
"v_cvt_pk_bf16_f32 v10, v20, v21\n" \
"v_mfma_f32_16x16x32_bf16 v[124:127], a[56:59], a[24:27], 0\n" \
"ds_read_b128 a[20:23], v213 offset:6144\n" \
"global_load_lds_dwordx4 v219, s[38:39] offset:-1024\n" \
"v_mfma_f32_16x16x32_bf16 v[124:127], a[60:63], a[28:31], v[124:127]\n" \
"s_add_u32 s38, s38, 0x20000\n" \
"s_addc_u32 s39, s39, 0\n" \
"ds_read_b64_tr_b16 a[46:47], v215 offset:26624\n" \
"v_mfma_f32_16x16x32_bf16 v[120:123], a[48:51], a[24:27], 0\n" \
"s_sub_u32 s75, s75, 32768\n" \
"s_cselect_b32 s75, 131072, s75\n" \
"v_cvt_pk_bf16_f32 v11, v22, v23\n" \
"v_mfma_f32_16x16x32_bf16 v[120:123], a[52:55], a[28:31], v[120:123]\n" \
"s_waitcnt lgkmcnt(3)\n" \
"buffer_store_dwordx4 v[8:11], v248, s[56:59], s71 offen offset:2048 sc1\n" \
"v_cvt_pk_bf16_f32 v12, v24, v25\n" \
"v_cvt_pk_bf16_f32 v13, v26, v27\n" \
"v_cvt_pk_bf16_f32 v14, v28, v29\n" \
"v_cvt_pk_bf16_f32 v15, v30, v31\n" \
"v_mfma_f32_16x16x32_bf16 v[64:67], a[32:35], a[0:3], v[64:67]\n" \
"ds_read_b128 a[24:27], v213 offset:4352\n" \
"s_add_u32 m0, s75, s95\n" \
"v_mfma_f32_16x16x32_bf16 v[64:67], a[36:39], a[4:7], v[64:67]\n" \
"s_waitcnt lgkmcnt(1)\n" \
"ds_read_b128 a[28:31], v213 offset:6400\n" \
"global_load_lds_dwordx4 v222, s[36:37] offset:0 sc1\n" \
"v_mfma_f32_16x16x32_bf16 v[68:71], a[40:43], a[0:3], v[68:71]\n" \
"s_sub_u32 s74, s74, 32768\n" \
"s_cselect_b32 s74, 131072, s74\n" \
"ds_read_b64_tr_b16 a[48:49], v214 offset:16896\n" \
"v_mfma_f32_16x16x32_bf16 v[68:71], a[44:47], a[4:7], v[68:71]\n" \
"ds_read_b64_tr_b16 a[50:51], v214 offset:18944\n" \
"v_mfma_f32_16x16x32_bf16 v[84:87], a[40:43], a[8:11], v[84:87]\n" \
"ds_read_b64_tr_b16 a[52:53], v214 offset:25088\n" \
"v_mfma_f32_16x16x32_bf16 v[84:87], a[44:47], a[12:15], v[84:87]\n" \
"ds_read_b64_tr_b16 a[54:55], v214 offset:27136\n" \
"global_load_lds_dwordx4 v222, s[36:37] offset:1024 sc1\n" \
"v_mfma_f32_16x16x32_bf16 v[80:83], a[32:35], a[8:11], v[80:83]\n" \
"ds_read_b64_tr_b16 a[56:57], v215 offset:16896\n" \
"v_mfma_f32_16x16x32_bf16 v[80:83], a[36:39], a[12:15], v[80:83]\n" \
"ds_read_b64_tr_b16 a[58:59], v215 offset:18944\n" \
"v_mfma_f32_16x16x32_bf16 v[96:99], a[32:35], a[16:19], v[96:99]\n" \
"ds_read_b64_tr_b16 a[60:61], v215 offset:25088\n" \
"v_mfma_f32_16x16x32_bf16 v[96:99], a[36:39], a[20:23], v[96:99]\n" \
"ds_read_b64_tr_b16 a[62:63], v215 offset:27136\n" \
"global_load_lds_dwordx4 v222, s[36:37] offset:2048 sc1\n" \
"v_mfma_f32_16x16x32_bf16 v[100:103], a[40:43], a[16:19], v[100:103]\n" \
"v_add_u32 v213, s74, v211\n" \
"v_add_u32 v214, s74, v212\n" \
"v_mfma_f32_16x16x32_bf16 v[100:103], a[44:47], a[20:23], v[100:103]\n" \
"s_waitcnt lgkmcnt(3)\n" \
"v_xor_b32 v215, 32, v214\n" \
"v_mfma_f32_16x16x32_bf16 v[116:119], a[40:43], a[24:27], v[116:119]\n" \
"v_mfma_f32_16x16x32_bf16 v[116:119], a[44:47], a[28:31], v[116:119]\n" \
"global_load_lds_dwordx4 v222, s[36:37] offset:3072 sc1\n" \
"v_mfma_f32_16x16x32_bf16 v[112:115], a[32:35], a[24:27], v[112:115]\n" \
"s_waitcnt vmcnt(23) lgkmcnt(0)\n" \
"s_add_u32 s36, s36, 0x1000\n" \
"v_mfma_f32_16x16x32_bf16 v[112:115], a[36:39], a[28:31], v[112:115]\n" \
"s_barrier\n" \
"v_mfma_f32_16x16x32_bf16 v[72:75], a[48:51], a[0:3], v[72:75]\n" \
"s_add_u32 m0, m0, 0x5000\n" \
"ds_read_b64_tr_b16 a[32:33], v214 offset:16384\n" \
"v_mfma_f32_16x16x32_bf16 v[72:75], a[52:55], a[4:7], v[72:75]\n" \
"ds_read_b64_tr_b16 a[34:35], v214 offset:18432\n" \
"global_load_lds_dwordx4 v216, s[38:39] offset:-4096\n" \
"v_mfma_f32_16x16x32_bf16 v[76:79], a[56:59], a[0:3], v[76:79]\n" \
"ds_read_b64_tr_b16 a[36:37], v214 offset:24576\n" \
"v_mfma_f32_16x16x32_bf16 v[76:79], a[60:63], a[4:7], v[76:79]\n" \
"ds_read_b128 a[0:3], v213 offset:0\n" \
"v_mfma_f32_16x16x32_bf16 v[92:95], a[56:59], a[8:11], v[92:95]\n" \
"ds_read_b128 a[4:7], v213 offset:2048\n" \
"v_mfma_f32_16x16x32_bf16 v[92:95], a[60:63], a[12:15], v[92:95]\n" \
"ds_read_b64_tr_b16 a[38:39], v214 offset:26624\n" \
"global_load_lds_dwordx4 v217, s[38:39] offset:-3072\n" \
"v_mfma_f32_16x16x32_bf16 v[88:91], a[48:51], a[8:11], v[88:91]\n" \
"ds_read_b64_tr_b16 a[40:41], v215 offset:16384\n" \
"v_mfma_f32_16x16x32_bf16 v[88:91], a[52:55], a[12:15], v[88:91]\n" \
"buffer_store_dwordx4 v[12:15], v248, s[56:59], s71 offen offset:3072 sc1\n" \
"ds_read_b128 a[8:11], v213 offset:256\n" \
"v_cvt_pk_bf16_f32 v16, v32, v33\n" \
"v_cvt_pk_bf16_f32 v17, v34, v35\n" \
"v_cvt_pk_bf16_f32 v18, v36, v37\n" \
"v_mfma_f32_16x16x32_bf16 v[104:107], a[48:51], a[16:19], v[104:107]\n" \
"ds_read_b128 a[12:15], v213 offset:2304\n" \
"v_cvt_pk_bf16_f32 v19, v38, v39\n" \
"v_mfma_f32_16x16x32_bf16 v[104:107], a[52:55], a[20:23], v[104:107]\n" \
"s_add_u32 s71, s76, 4096\n" \
"ds_read_b64_tr_b16 a[42:43], v215 offset:18432\n" \
"global_load_lds_dwordx4 v218, s[38:39] offset:-2048\n" \
"v_mfma_f32_16x16x32_bf16 v[108:111], a[56:59], a[16:19], v[108:111]\n" \
"ds_read_b64_tr_b16 a[44:45], v215 offset:24576\n" \
"v_mfma_f32_16x16x32_bf16 v[108:111], a[60:63], a[20:23], v[108:111]\n" \
"buffer_store_dwordx4 v[16:19], v248, s[56:59], s71 offen offset:0 sc1\n" \
"ds_read_b128 a[16:19], v213 offset:4096\n" \
"v_cvt_pk_bf16_f32 v20, v40, v41\n" \
"v_cvt_pk_bf16_f32 v21, v42, v43\n" \
"v_cvt_pk_bf16_f32 v22, v44, v45\n" \
"v_mfma_f32_16x16x32_bf16 v[124:127], a[56:59], a[24:27], v[124:127]\n" \
"ds_read_b128 a[20:23], v213 offset:6144\n" \
"global_load_lds_dwordx4 v219, s[38:39] offset:-1024\n" \
"v_mfma_f32_16x16x32_bf16 v[124:127], a[60:63], a[28:31], v[124:127]\n" \
"s_add_u32 s38, s38, 0x20000\n" \
"s_addc_u32 s39, s39, 0\n" \
"ds_read_b64_tr_b16 a[46:47], v215 offset:26624\n" \
"v_mfma_f32_16x16x32_bf16 v[120:123], a[48:51], a[24:27], v[120:123]\n" \
"s_sub_u32 s75, s75, 32768\n" \
"s_cselect_b32 s75, 131072, s75\n" \
"v_cvt_pk_bf16_f32 v23, v46, v47\n" \
"v_mfma_f32_16x16x32_bf16 v[120:123], a[52:55], a[28:31], v[120:123]\n" \
"s_waitcnt lgkmcnt(3)\n" \
"buffer_store_dwordx4 v[20:23], v248, s[56:59], s71 offen offset:1024 sc1\n" \
"v_cvt_pk_bf16_f32 v24, v48, v49\n" \
"v_cvt_pk_bf16_f32 v25, v50, v51\n" \
"v_cvt_pk_bf16_f32 v26, v52, v53\n" \
"v_cvt_pk_bf16_f32 v27, v54, v55\n" \
"v_mfma_f32_16x16x32_bf16 v[64:67], a[32:35], a[0:3], v[64:67]\n" \
"ds_read_b128 a[24:27], v213 offset:4352\n" \
"s_add_u32 m0, s75, s95\n" \
"v_mfma_f32_16x16x32_bf16 v[64:67], a[36:39], a[4:7], v[64:67]\n" \
"s_waitcnt lgkmcnt(1)\n" \
"ds_read_b128 a[28:31], v213 offset:6400\n" \
"global_load_lds_dwordx4 v222, s[36:37] offset:0 sc1\n" \
"v_mfma_f32_16x16x32_bf16 v[68:71], a[40:43], a[0:3], v[68:71]\n" \
"s_sub_u32 s74, s74, 32768\n" \
"s_cselect_b32 s74, 131072, s74\n" \
"ds_read_b64_tr_b16 a[48:49], v214 offset:16896\n" \
"v_mfma_f32_16x16x32_bf16 v[68:71], a[44:47], a[4:7], v[68:71]\n" \
"ds_read_b64_tr_b16 a[50:51], v214 offset:18944\n" \
"v_mfma_f32_16x16x32_bf16 v[84:87], a[40:43], a[8:11], v[84:87]\n" \
"ds_read_b64_tr_b16 a[52:53], v214 offset:25088\n" \
"v_mfma_f32_16x16x32_bf16 v[84:87], a[44:47], a[12:15], v[84:87]\n" \
"ds_read_b64_tr_b16 a[54:55], v214 offset:27136\n" \
"global_load_lds_dwordx4 v222, s[36:37] offset:1024 sc1\n" \
"v_mfma_f32_16x16x32_bf16 v[80:83], a[32:35], a[8:11], v[80:83]\n" \
"ds_read_b64_tr_b16 a[56:57], v215 offset:16896\n" \
"v_mfma_f32_16x16x32_bf16 v[80:83], a[36:39], a[12:15], v[80:83]\n" \
"ds_read_b64_tr_b16 a[58:59], v215 offset:18944\n" \
"v_mfma_f32_16x16x32_bf16 v[96:99], a[32:35], a[16:19], v[96:99]\n" \
"ds_read_b64_tr_b16 a[60:61], v215 offset:25088\n" \
"v_mfma_f32_16x16x32_bf16 v[96:99], a[36:39], a[20:23], v[96:99]\n" \
"ds_read_b64_tr_b16 a[62:63], v215 offset:27136\n" \
"global_load_lds_dwordx4 v222, s[36:37] offset:2048 sc1\n" \
"v_mfma_f32_16x16x32_bf16 v[100:103], a[40:43], a[16:19], v[100:103]\n" \
"v_add_u32 v213, s74, v211\n" \
"v_add_u32 v214, s74, v212\n" \
"v_mfma_f32_16x16x32_bf16 v[100:103], a[44:47], a[20:23], v[100:103]\n" \
"s_waitcnt lgkmcnt(3)\n" \
"v_xor_b32 v215, 32, v214\n" \
"v_mfma_f32_16x16x32_bf16 v[116:119], a[40:43], a[24:27], v[116:119]\n" \
"v_mfma_f32_16x16x32_bf16 v[116:119], a[44:47], a[28:31], v[116:119]\n" \
"global_load_lds_dwordx4 v222, s[36:37] offset:3072 sc1\n" \
"v_mfma_f32_16x16x32_bf16 v[112:115], a[32:35], a[24:27], v[112:115]\n" \
"s_waitcnt vmcnt(26) lgkmcnt(0)\n" \
"s_add_u32 s36, s36, 0x1000\n" \
"v_mfma_f32_16x16x32_bf16 v[112:115], a[36:39], a[28:31], v[112:115]\n" \
"s_barrier\n" \
"v_mfma_f32_16x16x32_bf16 v[72:75], a[48:51], a[0:3], v[72:75]\n" \
"s_add_u32 m0, m0, 0x5000\n" \
"ds_read_b64_tr_b16 a[32:33], v214 offset:16384\n" \
"v_mfma_f32_16x16x32_bf16 v[72:75], a[52:55], a[4:7], v[72:75]\n" \
"ds_read_b64_tr_b16 a[34:35], v214 offset:18432\n" \
"global_load_lds_dwordx4 v216, s[38:39] offset:-4096\n" \
"v_mfma_f32_16x16x32_bf16 v[76:79], a[56:59], a[0:3], v[76:79]\n" \
"ds_read_b64_tr_b16 a[36:37], v214 offset:24576\n" \
"v_mfma_f32_16x16x32_bf16 v[76:79], a[60:63], a[4:7], v[76:79]\n" \
"ds_read_b128 a[0:3], v213 offset:0\n" \
"v_mfma_f32_16x16x32_bf16 v[92:95], a[56:59], a[8:11], v[92:95]\n" \
"ds_read_b128 a[4:7], v213 offset:2048\n" \
"v_mfma_f32_16x16x32_bf16 v[92:95], a[60:63], a[12:15], v[92:95]\n" \
"ds_read_b64_tr_b16 a[38:39], v214 offset:26624\n" \
"global_load_lds_dwordx4 v217, s[38:39] offset:-3072\n" \
"v_mfma_f32_16x16x32_bf16 v[88:91], a[48:51], a[8:11], v[88:91]\n" \
"ds_read_b64_tr_b16 a[40:41], v215 offset:16384\n" \
"v_mfma_f32_16x16x32_bf16 v[88:91], a[52:55], a[12:15], v[88:91]\n" \
"buffer_store_dwordx4 v[24:27], v248, s[56:59], s71 offen offset:2048 sc1\n" \
"ds_read_b128 a[8:11], v213 offset:256\n" \
"v_cvt_pk_bf16_f32 v28, v56, v57\n" \
"v_cvt_pk_bf16_f32 v29, v58, v59\n" \
"v_cvt_pk_bf16_f32 v30, v60, v61\n" \
"v_mfma_f32_16x16x32_bf16 v[104:107], a[48:51], a[16:19], v[104:107]\n" \
"ds_read_b128 a[12:15], v213 offset:2304\n" \
"v_cvt_pk_bf16_f32 v31, v62, v63\n" \
"v_mfma_f32_16x16x32_bf16 v[104:107], a[52:55], a[20:23], v[104:107]\n" \
"ds_read_b64_tr_b16 a[42:43], v215 offset:18432\n" \
"global_load_lds_dwordx4 v218, s[38:39] offset:-2048\n" \
"v_mfma_f32_16x16x32_bf16 v[108:111], a[56:59], a[16:19], v[108:111]\n" \
"ds_read_b64_tr_b16 a[44:45], v215 offset:24576\n" \
"v_mfma_f32_16x16x32_bf16 v[108:111], a[60:63], a[20:23], v[108:111]\n" \
"buffer_store_dwordx4 v[28:31], v248, s[56:59], s71 offen offset:3072 sc1\n" \
"ds_read_b128 a[16:19], v213 offset:4096\n" \
"v_mfma_f32_16x16x32_bf16 v[124:127], a[56:59], a[24:27], v[124:127]\n" \
"ds_read_b128 a[20:23], v213 offset:6144\n" \
"global_load_lds_dwordx4 v219, s[38:39] offset:-1024\n" \
"v_mfma_f32_16x16x32_bf16 v[124:127], a[60:63], a[28:31], v[124:127]\n" \
"s_add_u32 s38, s38, 0x20000\n" \
"s_addc_u32 s39, s39, 0\n" \
"ds_read_b64_tr_b16 a[46:47], v215 offset:26624\n" \
"v_mfma_f32_16x16x32_bf16 v[120:123], a[48:51], a[24:27], v[120:123]\n" \
"s_sub_u32 s75, s75, 32768\n" \
"s_cselect_b32 s75, 131072, s75\n" \
"v_mfma_f32_16x16x32_bf16 v[120:123], a[52:55], a[28:31], v[120:123]\n" \
"s_waitcnt lgkmcnt(3)\n" \
"v_mfma_f32_16x16x32_bf16 v[64:67], a[32:35], a[0:3], v[64:67]\n" \
"ds_read_b128 a[24:27], v213 offset:4352\n" \
"s_add_u32 m0, s75, s95\n" \
"v_mfma_f32_16x16x32_bf16 v[64:67], a[36:39], a[4:7], v[64:67]\n" \
"s_waitcnt lgkmcnt(1)\n" \
"ds_read_b128 a[28:31], v213 offset:6400\n" \
"global_load_lds_dwordx4 v222, s[36:37] offset:0 sc1\n" \
"v_mfma_f32_16x16x32_bf16 v[68:71], a[40:43], a[0:3], v[68:71]\n" \
"s_sub_u32 s74, s74, 32768\n" \
"s_cselect_b32 s74, 131072, s74\n" \
"ds_read_b64_tr_b16 a[48:49], v214 offset:16896\n" \
"v_mfma_f32_16x16x32_bf16 v[68:71], a[44:47], a[4:7], v[68:71]\n" \
"ds_read_b64_tr_b16 a[50:51], v214 offset:18944\n" \
"v_mfma_f32_16x16x32_bf16 v[84:87], a[40:43], a[8:11], v[84:87]\n" \
"ds_read_b64_tr_b16 a[52:53], v214 offset:25088\n" \
"v_mfma_f32_16x16x32_bf16 v[84:87], a[44:47], a[12:15], v[84:87]\n" \
"ds_read_b64_tr_b16 a[54:55], v214 offset:27136\n" \
"global_load_lds_dwordx4 v222, s[36:37] offset:1024 sc1\n" \
"v_mfma_f32_16x16x32_bf16 v[80:83], a[32:35], a[8:11], v[80:83]\n" \
"ds_read_b64_tr_b16 a[56:57], v215 offset:16896\n" \
"v_mfma_f32_16x16x32_bf16 v[80:83], a[36:39], a[12:15], v[80:83]\n" \
"ds_read_b64_tr_b16 a[58:59], v215 offset:18944\n" \
"v_mfma_f32_16x16x32_bf16 v[96:99], a[32:35], a[16:19], v[96:99]\n" \
"ds_read_b64_tr_b16 a[60:61], v215 offset:25088\n" \
"v_mfma_f32_16x16x32_bf16 v[96:99], a[36:39], a[20:23], v[96:99]\n" \
"ds_read_b64_tr_b16 a[62:63], v215 offset:27136\n" \
"global_load_lds_dwordx4 v222, s[36:37] offset:2048 sc1\n" \
"v_mfma_f32_16x16x32_bf16 v[100:103], a[40:43], a[16:19], v[100:103]\n" \
"v_add_u32 v213, s74, v211\n" \
"v_add_u32 v214, s74, v212\n" \
"v_mfma_f32_16x16x32_bf16 v[100:103], a[44:47], a[20:23], v[100:103]\n" \
"s_waitcnt lgkmcnt(3)\n" \
"v_xor_b32 v215, 32, v214\n" \
"v_mfma_f32_16x16x32_bf16 v[116:119], a[40:43], a[24:27], v[116:119]\n" \
"v_mfma_f32_16x16x32_bf16 v[116:119], a[44:47], a[28:31], v[116:119]\n" \
"global_load_lds_dwordx4 v222, s[36:37] offset:3072 sc1\n" \
"v_mfma_f32_16x16x32_bf16 v[112:115], a[32:35], a[24:27], v[112:115]\n" \
"s_waitcnt vmcnt(26) lgkmcnt(0)\n" \
"s_add_u32 s36, s36, 0x1000\n" \
"v_mfma_f32_16x16x32_bf16 v[112:115], a[36:39], a[28:31], v[112:115]\n" \
"s_barrier\n" \
"v_mfma_f32_16x16x32_bf16 v[72:75], a[48:51], a[0:3], v[72:75]\n" \
"s_add_u32 m0, m0, 0x5000\n" \
"ds_read_b64_tr_b16 a[32:33], v214 offset:16384\n" \
"v_mfma_f32_16x16x32_bf16 v[72:75], a[52:55], a[4:7], v[72:75]\n" \
"ds_read_b64_tr_b16 a[34:35], v214 offset:18432\n" \
"global_load_lds_dwordx4 v216, s[38:39] offset:-4096\n" \
"v_mfma_f32_16x16x32_bf16 v[76:79], a[56:59], a[0:3], v[76:79]\n" \
"ds_read_b64_tr_b16 a[36:37], v214 offset:24576\n" \
"v_mfma_f32_16x16x32_bf16 v[76:79], a[60:63], a[4:7], v[76:79]\n" \
"ds_read_b128 a[0:3], v213 offset:0\n" \
"v_mfma_f32_16x16x32_bf16 v[92:95], a[56:59], a[8:11], v[92:95]\n" \
"ds_read_b128 a[4:7], v213 offset:2048\n" \
"v_mfma_f32_16x16x32_bf16 v[92:95], a[60:63], a[12:15], v[92:95]\n" \
"ds_read_b64_tr_b16 a[38:39], v214 offset:26624\n" \
"global_load_lds_dwordx4 v217, s[38:39] offset:-3072\n" \
"v_mfma_f32_16x16x32_bf16 v[88:91], a[48:51], a[8:11], v[88:91]\n" \
"ds_read_b64_tr_b16 a[40:41], v215 offset:16384\n" \
"v_mfma_f32_16x16x32_bf16 v[88:91], a[52:55], a[12:15], v[88:91]\n" \
"ds_read_b128 a[8:11], v213 offset:256\n" \
"v_mfma_f32_16x16x32_bf16 v[104:107], a[48:51], a[16:19], v[104:107]\n" \
"ds_read_b128 a[12:15], v213 offset:2304\n" \
"v_mfma_f32_16x16x32_bf16 v[104:107], a[52:55], a[20:23], v[104:107]\n" \
"ds_read_b64_tr_b16 a[42:43], v215 offset:18432\n" \
"global_load_lds_dwordx4 v218, s[38:39] offset:-2048\n" \
"v_mfma_f32_16x16x32_bf16 v[108:111], a[56:59], a[16:19], v[108:111]\n" \
"ds_read_b64_tr_b16 a[44:45], v215 offset:24576\n" \
"v_mfma_f32_16x16x32_bf16 v[108:111], a[60:63], a[20:23], v[108:111]\n" \
"ds_read_b128 a[16:19], v213 offset:4096\n" \
"v_mfma_f32_16x16x32_bf16 v[124:127], a[56:59], a[24:27], v[124:127]\n" \
"ds_read_b128 a[20:23], v213 offset:6144\n" \
"global_load_lds_dwordx4 v219, s[38:39] offset:-1024\n" \
"v_mfma_f32_16x16x32_bf16 v[124:127], a[60:63], a[28:31], v[124:127]\n" \
"s_add_u32 s38, s38, 0x20000\n" \
"s_addc_u32 s39, s39, 0\n" \
"ds_read_b64_tr_b16 a[46:47], v215 offset:26624\n" \
"v_mfma_f32_16x16x32_bf16 v[120:123], a[48:51], a[24:27], v[120:123]\n" \
"s_sub_u32 s75, s75, 32768\n" \
"s_cselect_b32 s75, 131072, s75\n" \
"v_mfma_f32_16x16x32_bf16 v[120:123], a[52:55], a[28:31], v[120:123]\n" \
"s_waitcnt lgkmcnt(3)\n" \
"v_mfma_f32_16x16x32_bf16 v[64:67], a[32:35], a[0:3], v[64:67]\n" \
"ds_read_b128 a[24:27], v213 offset:4352\n" \
"s_add_u32 m0, s75, s95\n" \
"v_mfma_f32_16x16x32_bf16 v[64:67], a[36:39], a[4:7], v[64:67]\n" \
"s_waitcnt lgkmcnt(1)\n" \
"ds_read_b128 a[28:31], v213 offset:6400\n" \
"global_load_lds_dwordx4 v222, s[36:37] offset:0 sc1\n" \
"v_mfma_f32_16x16x32_bf16 v[68:71], a[40:43], a[0:3], v[68:71]\n" \
"s_sub_u32 s74, s74, 32768\n" \
"s_cselect_b32 s74, 131072, s74\n" \
"ds_read_b64_tr_b16 a[48:49], v214 offset:16896\n" \
"v_mfma_f32_16x16x32_bf16 v[68:71], a[44:47], a[4:7], v[68:71]\n" \
"ds_read_b64_tr_b16 a[50:51], v214 offset:18944\n" \
"v_mfma_f32_16x16x32_bf16 v[84:87], a[40:43], a[8:11], v[84:87]\n" \
"ds_read_b64_tr_b16 a[52:53], v214 offset:25088\n" \
"v_mfma_f32_16x16x32_bf16 v[84:87], a[44:47], a[12:15], v[84:87]\n" \
"ds_read_b64_tr_b16 a[54:55], v214 offset:27136\n" \
"global_load_lds_dwordx4 v222, s[36:37] offset:1024 sc1\n" \
"v_mfma_f32_16x16x32_bf16 v[80:83], a[32:35], a[8:11], v[80:83]\n" \
"ds_read_b64_tr_b16 a[56:57], v215 offset:16896\n" \
"v_mfma_f32_16x16x32_bf16 v[80:83], a[36:39], a[12:15], v[80:83]\n" \
"ds_read_b64_tr_b16 a[58:59], v215 offset:18944\n" \
"v_mfma_f32_16x16x32_bf16 v[96:99], a[32:35], a[16:19], v[96:99]\n" \
"ds_read_b64_tr_b16 a[60:61], v215 offset:25088\n" \
"v_mfma_f32_16x16x32_bf16 v[96:99], a[36:39], a[20:23], v[96:99]\n" \
"ds_read_b64_tr_b16 a[62:63], v215 offset:27136\n" \
"global_load_lds_dwordx4 v222, s[36:37] offset:2048 sc1\n" \
"v_mfma_f32_16x16x32_bf16 v[100:103], a[40:43], a[16:19], v[100:103]\n" \
"v_add_u32 v213, s74, v211\n" \
"v_add_u32 v214, s74, v212\n" \
"v_mfma_f32_16x16x32_bf16 v[100:103], a[44:47], a[20:23], v[100:103]\n" \
"s_waitcnt lgkmcnt(3)\n" \
"v_xor_b32 v215, 32, v214\n" \
"v_mfma_f32_16x16x32_bf16 v[116:119], a[40:43], a[24:27], v[116:119]\n" \
"v_mfma_f32_16x16x32_bf16 v[116:119], a[44:47], a[28:31], v[116:119]\n" \
"global_load_lds_dwordx4 v222, s[36:37] offset:3072 sc1\n" \
"v_mfma_f32_16x16x32_bf16 v[112:115], a[32:35], a[24:27], v[112:115]\n" \
"s_waitcnt vmcnt(23) lgkmcnt(0)\n" \
"s_add_u32 s36, s36, 0x1000\n" \
"v_mfma_f32_16x16x32_bf16 v[112:115], a[36:39], a[28:31], v[112:115]\n" \
"s_barrier\n" \
"v_mfma_f32_16x16x32_bf16 v[72:75], a[48:51], a[0:3], v[72:75]\n" \
"s_add_u32 m0, m0, 0x5000\n" \
"ds_read_b64_tr_b16 a[32:33], v214 offset:16384\n" \
"v_mfma_f32_16x16x32_bf16 v[72:75], a[52:55], a[4:7], v[72:75]\n" \
"ds_read_b64_tr_b16 a[34:35], v214 offset:18432\n" \
"global_load_lds_dwordx4 v216, s[38:39] offset:-4096\n" \
"v_mfma_f32_16x16x32_bf16 v[76:79], a[56:59], a[0:3], v[76:79]\n" \
"ds_read_b64_tr_b16 a[36:37], v214 offset:24576\n" \
"v_mfma_f32_16x16x32_bf16 v[76:79], a[60:63], a[4:7], v[76:79]\n" \
"ds_read_b128 a[0:3], v213 offset:0\n" \
"v_mfma_f32_16x16x32_bf16 v[92:95], a[56:59], a[8:11], v[92:95]\n" \
"ds_read_b128 a[4:7], v213 offset:2048\n" \
"v_mfma_f32_16x16x32_bf16 v[92:95], a[60:63], a[12:15], v[92:95]\n" \
"ds_read_b64_tr_b16 a[38:39], v214 offset:26624\n" \
"global_load_lds_dwordx4 v217, s[38:39] offset:-3072\n" \
"v_mfma_f32_16x16x32_bf16 v[88:91], a[48:51], a[8:11], v[88:91]\n" \
"ds_read_b64_tr_b16 a[40:41], v215 offset:16384\n" \
"v_mfma_f32_16x16x32_bf16 v[88:91], a[52:55], a[12:15], v[88:91]\n" \
"ds_read_b128 a[8:11], v213 offset:256\n" \
"v_mfma_f32_16x16x32_bf16 v[104:107], a[48:51], a[16:19], v[104:107]\n" \
"ds_read_b128 a[12:15], v213 offset:2304\n" \
"v_mfma_f32_16x16x32_bf16 v[104:107], a[52:55], a[20:23], v[104:107]\n" \
"ds_read_b64_tr_b16 a[42:43], v215 offset:18432\n" \
"global_load_lds_dwordx4 v218, s[38:39] offset:-2048\n" \
"v_mfma_f32_16x16x32_bf16 v[108:111], a[56:59], a[16:19], v[108:111]\n" \
"ds_read_b64_tr_b16 a[44:45], v215 offset:24576\n" \
"v_mfma_f32_16x16x32_bf16 v[108:111], a[60:63], a[20:23], v[108:111]\n" \
"ds_read_b128 a[16:19], v213 offset:4096\n" \
"v_mfma_f32_16x16x32_bf16 v[124:127], a[56:59], a[24:27], v[124:127]\n" \
"ds_read_b128 a[20:23], v213 offset:6144\n" \
"global_load_lds_dwordx4 v219, s[38:39] offset:-1024\n" \
"v_mfma_f32_16x16x32_bf16 v[124:127], a[60:63], a[28:31], v[124:127]\n" \
"s_add_u32 s38, s38, 0x20000\n" \
"s_addc_u32 s39, s39, 0\n" \
"ds_read_b64_tr_b16 a[46:47], v215 offset:26624\n" \
"v_mfma_f32_16x16x32_bf16 v[120:123], a[48:51], a[24:27], v[120:123]\n" \
"s_sub_u32 s75, s75, 32768\n" \
"s_cselect_b32 s75, 131072, s75\n" \
"v_mfma_f32_16x16x32_bf16 v[120:123], a[52:55], a[28:31], v[120:123]\n" \
"s_waitcnt lgkmcnt(3)\n" \
"v_mfma_f32_16x16x32_bf16 v[64:67], a[32:35], a[0:3], v[64:67]\n" \
"ds_read_b128 a[24:27], v213 offset:4352\n" \
"s_add_u32 m0, s75, s95\n" \
"v_mfma_f32_16x16x32_bf16 v[64:67], a[36:39], a[4:7], v[64:67]\n" \
"s_waitcnt lgkmcnt(1)\n" \
"ds_read_b128 a[28:31], v213 offset:6400\n" \
"global_load_lds_dwordx4 v222, s[36:37] offset:0 sc1\n" \
"v_mfma_f32_16x16x32_bf16 v[68:71], a[40:43], a[0:3], v[68:71]\n" \
"s_sub_u32 s74, s74, 32768\n" \
"s_cselect_b32 s74, 131072, s74\n" \
"ds_read_b64_tr_b16 a[48:49], v214 offset:16896\n" \
"v_mfma_f32_16x16x32_bf16 v[68:71], a[44:47], a[4:7], v[68:71]\n" \
"ds_read_b64_tr_b16 a[50:51], v214 offset:18944\n" \
"v_mfma_f32_16x16x32_bf16 v[84:87], a[40:43], a[8:11], v[84:87]\n" \
"ds_read_b64_tr_b16 a[52:53], v214 offset:25088\n" \
"v_mfma_f32_16x16x32_bf16 v[84:87], a[44:47], a[12:15], v[84:87]\n" \
"ds_read_b64_tr_b16 a[54:55], v214 offset:27136\n" \
"global_load_lds_dwordx4 v222, s[36:37] offset:1024 sc1\n" \
"v_mfma_f32_16x16x32_bf16 v[80:83], a[32:35], a[8:11], v[80:83]\n" \
"ds_read_b64_tr_b16 a[56:57], v215 offset:16896\n" \
"v_mfma_f32_16x16x32_bf16 v[80:83], a[36:39], a[12:15], v[80:83]\n" \
"ds_read_b64_tr_b16 a[58:59], v215 offset:18944\n" \
"v_mfma_f32_16x16x32_bf16 v[96:99], a[32:35], a[16:19], v[96:99]\n" \
"ds_read_b64_tr_b16 a[60:61], v215 offset:25088\n" \
"v_mfma_f32_16x16x32_bf16 v[96:99], a[36:39], a[20:23], v[96:99]\n" \
"ds_read_b64_tr_b16 a[62:63], v215 offset:27136\n" \
"global_load_lds_dwordx4 v222, s[36:37] offset:2048 sc1\n" \
"v_mfma_f32_16x16x32_bf16 v[100:103], a[40:43], a[16:19], v[100:103]\n" \
"v_add_u32 v213, s74, v211\n" \
"v_add_u32 v214, s74, v212\n" \
"v_mfma_f32_16x16x32_bf16 v[100:103], a[44:47], a[20:23], v[100:103]\n" \
"s_waitcnt lgkmcnt(3)\n" \
"v_xor_b32 v215, 32, v214\n" \
"v_mfma_f32_16x16x32_bf16 v[116:119], a[40:43], a[24:27], v[116:119]\n" \
"v_mfma_f32_16x16x32_bf16 v[116:119], a[44:47], a[28:31], v[116:119]\n" \
"global_load_lds_dwordx4 v222, s[36:37] offset:3072 sc1\n" \
"v_mfma_f32_16x16x32_bf16 v[112:115], a[32:35], a[24:27], v[112:115]\n" \
"s_waitcnt vmcnt(20) lgkmcnt(0)\n" \
"s_add_u32 s36, s36, 0x1000\n" \
"v_mfma_f32_16x16x32_bf16 v[112:115], a[36:39], a[28:31], v[112:115]\n" \
"s_barrier\n" \
"v_mfma_f32_16x16x32_bf16 v[72:75], a[48:51], a[0:3], v[72:75]\n" \
"s_add_u32 m0, m0, 0x5000\n" \
"ds_read_b64_tr_b16 a[32:33], v214 offset:16384\n" \
"v_mfma_f32_16x16x32_bf16 v[72:75], a[52:55], a[4:7], v[72:75]\n" \
"ds_read_b64_tr_b16 a[34:35], v214 offset:18432\n" \
"global_load_lds_dwordx4 v216, s[38:39] offset:-4096\n" \
"v_mfma_f32_16x16x32_bf16 v[76:79], a[56:59], a[0:3], v[76:79]\n" \
"ds_read_b64_tr_b16 a[36:37], v214 offset:24576\n" \
"v_mfma_f32_16x16x32_bf16 v[76:79], a[60:63], a[4:7], v[76:79]\n" \
"buffer_store_dword v224, v225, s[56:59], s79 offen sc1\n" \
"ds_read_b128 a[0:3], v213 offset:0\n" \
"v_mfma_f32_16x16x32_bf16 v[92:95], a[56:59], a[8:11], v[92:95]\n" \
"ds_read_b128 a[4:7], v213 offset:2048\n" \
"v_mfma_f32_16x16x32_bf16 v[92:95], a[60:63], a[12:15], v[92:95]\n" \
"ds_read_b64_tr_b16 a[38:39], v214 offset:26624\n" \
"global_load_lds_dwordx4 v217, s[38:39] offset:-3072\n" \
"v_mfma_f32_16x16x32_bf16 v[88:91], a[48:51], a[8:11], v[88:91]\n" \
"ds_read_b64_tr_b16 a[40:41], v215 offset:16384\n" \
"v_mfma_f32_16x16x32_bf16 v[88:91], a[52:55], a[12:15], v[88:91]\n" \
"ds_read_b128 a[8:11], v213 offset:256\n" \
"v_mfma_f32_16x16x32_bf16 v[104:107], a[48:51], a[16:19], v[104:107]\n" \
"ds_read_b128 a[12:15], v213 offset:2304\n" \
"v_mfma_f32_16x16x32_bf16 v[104:107], a[52:55], a[20:23], v[104:107]\n" \
"ds_read_b64_tr_b16 a[42:43], v215 offset:18432\n" \
"global_load_lds_dwordx4 v218, s[38:39] offset:-2048\n" \
"v_mfma_f32_16x16x32_bf16 v[108:111], a[56:59], a[16:19], v[108:111]\n" \
"ds_read_b64_tr_b16 a[44:45], v215 offset:24576\n" \
"v_mfma_f32_16x16x32_bf16 v[108:111], a[60:63], a[20:23], v[108:111]\n" \
"ds_read_b128 a[16:19], v213 offset:4096\n" \
"v_mfma_f32_16x16x32_bf16 v[124:127], a[56:59], a[24:27], v[124:127]\n" \
"ds_read_b128 a[20:23], v213 offset:6144\n" \
"global_load_lds_dwordx4 v219, s[38:39] offset:-1024\n" \
"v_mfma_f32_16x16x32_bf16 v[124:127], a[60:63], a[28:31], v[124:127]\n" \
"s_add_u32 s38, s38, 0x20000\n" \
"s_addc_u32 s39, s39, 0\n" \
"ds_read_b64_tr_b16 a[46:47], v215 offset:26624\n" \
"v_mfma_f32_16x16x32_bf16 v[120:123], a[48:51], a[24:27], v[120:123]\n" \
"s_sub_u32 s75, s75, 32768\n" \
"s_cselect_b32 s75, 131072, s75\n" \
"v_mfma_f32_16x16x32_bf16 v[120:123], a[52:55], a[28:31], v[120:123]\n" \
"s_waitcnt lgkmcnt(3)\n" \
"s_mov_b32 s72, 6\n" \
"s_call_b64 s[68:69], Lo64_%=\n" \
"v_mfma_f32_16x16x32_bf16 v[64:67], a[32:35], a[0:3], v[64:67]\n" \
"ds_read_b128 a[24:27], v213 offset:4352\n" \
"v_mfma_f32_16x16x32_bf16 v[64:67], a[36:39], a[4:7], v[64:67]\n" \
"s_waitcnt lgkmcnt(1)\n" \
"ds_read_b128 a[28:31], v213 offset:6400\n" \
"v_mfma_f32_16x16x32_bf16 v[68:71], a[40:43], a[0:3], v[68:71]\n" \
"s_sub_u32 s74, s74, 32768\n" \
"s_cselect_b32 s74, 131072, s74\n" \
"ds_read_b64_tr_b16 a[48:49], v214 offset:16896\n" \
"v_mfma_f32_16x16x32_bf16 v[68:71], a[44:47], a[4:7], v[68:71]\n" \
"ds_read_b64_tr_b16 a[50:51], v214 offset:18944\n" \
"v_mfma_f32_16x16x32_bf16 v[84:87], a[40:43], a[8:11], v[84:87]\n" \
"ds_read_b64_tr_b16 a[52:53], v214 offset:25088\n" \
"v_mfma_f32_16x16x32_bf16 v[84:87], a[44:47], a[12:15], v[84:87]\n" \
"ds_read_b64_tr_b16 a[54:55], v214 offset:27136\n" \
"v_mfma_f32_16x16x32_bf16 v[80:83], a[32:35], a[8:11], v[80:83]\n" \
"ds_read_b64_tr_b16 a[56:57], v215 offset:16896\n" \
"v_mfma_f32_16x16x32_bf16 v[80:83], a[36:39], a[12:15], v[80:83]\n" \
"Lpg13278_%=:\n" \
"s_cbranch_execz Lcb1_%=\n" \
"ds_read_b64_tr_b16 a[58:59], v215 offset:18944\n" \
"v_mfma_f32_16x16x32_bf16 v[96:99], a[32:35], a[16:19], v[96:99]\n" \
"ds_read_b64_tr_b16 a[60:61], v215 offset:25088\n" \
"v_mfma_f32_16x16x32_bf16 v[96:99], a[36:39], a[20:23], v[96:99]\n" \
"ds_read_b64_tr_b16 a[62:63], v215 offset:27136\n" \
"v_mfma_f32_16x16x32_bf16 v[100:103], a[40:43], a[16:19], v[100:103]\n" \
"v_add_u32 v213, s74, v211\n" \
"v_add_u32 v214, s74, v212\n" \
"v_mfma_f32_16x16x32_bf16 v[100:103], a[44:47], a[20:23], v[100:103]\n" \
"s_waitcnt lgkmcnt(3)\n" \
"v_xor_b32 v215, 32, v214\n" \
"v_mfma_f32_16x16x32_bf16 v[116:119], a[40:43], a[24:27], v[116:119]\n" \
"v_mfma_f32_16x16x32_bf16 v[116:119], a[44:47], a[28:31], v[116:119]\n" \
"v_mfma_f32_16x16x32_bf16 v[112:115], a[32:35], a[24:27], v[112:115]\n" \
"s_waitcnt vmcnt(16) lgkmcnt(0)\n" \
"v_mfma_f32_16x16x32_bf16 v[112:115], a[36:39], a[28:31], v[112:115]\n" \
"s_barrier\n" \
"v_mfma_f32_16x16x32_bf16 v[72:75], a[48:51], a[0:3], v[72:75]\n" \
"s_lshl_b32 s83, s95, 1\n" \
"s_add_u32 s83, s83, s74\n" \
"ds_read_b64_tr_b16 a[32:33], v214 offset:16384\n" \
"v_mfma_f32_16x16x32_bf16 v[72:75], a[52:55], a[4:7], v[72:75]\n" \
"ds_read_b64_tr_b16 a[34:35], v214 offset:18432\n" \
"v_mfma_f32_16x16x32_bf16 v[76:79], a[56:59], a[0:3], v[76:79]\n" \
"ds_read_b64_tr_b16 a[36:37], v214 offset:24576\n" \
"v_mfma_f32_16x16x32_bf16 v[76:79], a[60:63], a[4:7], v[76:79]\n" \
"ds_read_b128 a[0:3], v213 offset:0\n" \
"v_mfma_f32_16x16x32_bf16 v[92:95], a[56:59], a[8:11], v[92:95]\n" \
"ds_read_b128 a[4:7], v213 offset:2048\n" \
"v_mfma_f32_16x16x32_bf16 v[92:95], a[60:63], a[12:15], v[92:95]\n" \
"ds_read_b64_tr_b16 a[38:39], v214 offset:26624\n" \
"v_mfma_f32_16x16x32_bf16 v[88:91], a[48:51], a[8:11], v[88:91]\n" \
"ds_read_b64_tr_b16 a[40:41], v215 offset:16384\n" \
"v_mfma_f32_16x16x32_bf16 v[88:91], a[52:55], a[12:15], v[88:91]\n" \
"ds_read_b128 a[8:11], v213 offset:256\n" \
"v_mfma_f32_16x16x32_bf16 v[104:107], a[48:51], a[16:19], v[104:107]\n" \
"ds_read_b128 a[12:15], v213 offset:2304\n" \
"v_mfma_f32_16x16x32_bf16 v[104:107], a[52:55], a[20:23], v[104:107]\n" \
"ds_read_b64_tr_b16 a[42:43], v215 offset:18432\n" \
"v_mfma_f32_16x16x32_bf16 v[108:111], a[56:59], a[16:19], v[108:111]\n" \
"ds_read_b64_tr_b16 a[44:45], v215 offset:24576\n" \
"v_mfma_f32_16x16x32_bf16 v[108:111], a[60:63], a[20:23], v[108:111]\n" \
"ds_read_b128 a[16:19], v213 offset:4096\n" \
"v_mfma_f32_16x16x32_bf16 v[124:127], a[56:59], a[24:27], v[124:127]\n" \
"ds_read_b128 a[20:23], v213 offset:6144\n" \
"v_mfma_f32_16x16x32_bf16 v[124:127], a[60:63], a[28:31], v[124:127]\n" \
"ds_read_b64_tr_b16 a[46:47], v215 offset:26624\n" \
"v_mfma_f32_16x16x32_bf16 v[120:123], a[48:51], a[24:27], v[120:123]\n" \
"v_mfma_f32_16x16x32_bf16 v[120:123], a[52:55], a[28:31], v[120:123]\n" \
"s_waitcnt lgkmcnt(3)\n" \
"v_mfma_f32_16x16x32_bf16 v[64:67], a[32:35], a[0:3], v[64:67]\n" \
"ds_read_b128 a[24:27], v213 offset:4352\n" \
"v_mfma_f32_16x16x32_bf16 v[64:67], a[36:39], a[4:7], v[64:67]\n" \
"s_waitcnt lgkmcnt(1)\n" \
"ds_read_b128 a[28:31], v213 offset:6400\n" \
"v_lshrrev_b32 v132, 4, v248\n" \
"v_mfma_f32_16x16x32_bf16 v[68:71], a[40:43], a[0:3], v[68:71]\n" \
"s_sub_u32 s74, s74, 32768\n" \
"s_cselect_b32 s74, 131072, s74\n" \
"ds_read_b64_tr_b16 a[48:49], v214 offset:16896\n" \
"v_mfma_f32_16x16x32_bf16 v[68:71], a[44:47], a[4:7], v[68:71]\n" \
"ds_read_b64_tr_b16 a[50:51], v214 offset:18944\n" \
"v_and_b32 v133, 15, v132\n" \
"v_mfma_f32_16x16x32_bf16 v[84:87], a[40:43], a[8:11], v[84:87]\n" \
"ds_read_b64_tr_b16 a[52:53], v214 offset:25088\n" \
"v_mul_u32_u24 v128, 144, v133\n" \
"v_mfma_f32_16x16x32_bf16 v[84:87], a[44:47], a[12:15], v[84:87]\n" \
"ds_read_b64_tr_b16 a[54:55], v214 offset:27136\n" \
"v_lshrrev_b32 v133, 4, v132\n" \
"v_mfma_f32_16x16x32_bf16 v[80:83], a[32:35], a[8:11], v[80:83]\n" \
"ds_read_b64_tr_b16 a[56:57], v215 offset:16896\n" \
"v_lshl_add_u32 v128, v133, 3, v128\n" \
"v_mfma_f32_16x16x32_bf16 v[80:83], a[36:39], a[12:15], v[80:83]\n" \
"ds_read_b64_tr_b16 a[58:59], v215 offset:18944\n" \
"v_add_u32 v128, s83, v128\n" \
"v_mfma_f32_16x16x32_bf16 v[96:99], a[32:35], a[16:19], v[96:99]\n" \
"ds_read_b64_tr_b16 a[60:61], v215 offset:25088\n" \
"v_lshrrev_b32 v133, 3, v132\n" \
"v_mfma_f32_16x16x32_bf16 v[96:99], a[36:39], a[20:23], v[96:99]\n" \
"ds_read_b64_tr_b16 a[62:63], v215 offset:27136\n" \
"v_and_b32 v134, 7, v132\n" \
"v_mfma_f32_16x16x32_bf16 v[100:103], a[40:43], a[16:19], v[100:103]\n" \
"v_add_u32 v213, s74, v211\n" \
"v_add_u32 v214, s74, v212\n" \
"v_mfma_f32_16x16x32_bf16 v[100:103], a[44:47], a[20:23], v[100:103]\n" \
"s_waitcnt lgkmcnt(3)\n" \
"v_xor_b32 v215, 32, v214\n" \
"v_mul_u32_u24 v129, 144, v133\n" \
"v_mfma_f32_16x16x32_bf16 v[116:119], a[40:43], a[24:27], v[116:119]\n" \
"v_lshl_add_u32 v129, v134, 4, v129\n" \
"v_add_u32 v129, s83, v129\n" \
"v_mfma_f32_16x16x32_bf16 v[116:119], a[44:47], a[28:31], v[116:119]\n" \
"v_lshlrev_b32 v134, 4, v134\n" \
"v_lshl_add_u32 v130, v133, 13, v134\n" \
"v_mfma_f32_16x16x32_bf16 v[112:115], a[32:35], a[24:27], v[112:115]\n" \
"s_waitcnt vmcnt(0) lgkmcnt(0)\n" \
"v_mfma_f32_16x16x32_bf16 v[112:115], a[36:39], a[28:31], v[112:115]\n" \
"s_barrier\n" \
"v_mfma_f32_16x16x32_bf16 v[72:75], a[48:51], a[0:3], v[72:75]\n" \
"ds_read_b128 a[192:195], v213 offset:0\n" \
"ds_read_b64_tr_b16 a[128:129], v214 offset:16384\n" \
"v_mfma_f32_16x16x32_bf16 v[72:75], a[52:55], a[4:7], v[72:75]\n" \
"s_sub_u32 s74, s74, 32768\n" \
"s_cselect_b32 s74, 131072, s74\n" \
"ds_read_b64_tr_b16 a[130:131], v214 offset:18432\n" \
"v_mfma_f32_16x16x32_bf16 v[76:79], a[56:59], a[0:3], v[76:79]\n" \
"ds_read_b128 a[196:199], v213 offset:2048\n" \
"ds_read_b64_tr_b16 a[132:133], v214 offset:24576\n" \
"v_mfma_f32_16x16x32_bf16 v[76:79], a[60:63], a[4:7], v[76:79]\n" \
"ds_read_b64_tr_b16 a[134:135], v214 offset:26624\n" \
"v_add_u32 v226, s74, v211\n" \
"v_mfma_f32_16x16x32_bf16 v[92:95], a[56:59], a[8:11], v[92:95]\n" \
"ds_read_b128 a[200:203], v213 offset:256\n" \
"ds_read_b64_tr_b16 a[136:137], v215 offset:16384\n" \
"v_mfma_f32_16x16x32_bf16 v[92:95], a[60:63], a[12:15], v[92:95]\n" \
"ds_read_b64_tr_b16 a[138:139], v215 offset:18432\n" \
"v_add_u32 v227, s74, v212\n" \
"v_mfma_f32_16x16x32_bf16 v[88:91], a[48:51], a[8:11], v[88:91]\n" \
"ds_read_b128 a[204:207], v213 offset:2304\n" \
"ds_read_b64_tr_b16 a[140:141], v215 offset:24576\n" \
"v_mfma_f32_16x16x32_bf16 v[88:91], a[52:55], a[12:15], v[88:91]\n" \
"ds_read_b64_tr_b16 a[142:143], v215 offset:26624\n" \
"v_xor_b32 v228, 32, v227\n" \
"v_mfma_f32_16x16x32_bf16 v[104:107], a[48:51], a[16:19], v[104:107]\n" \
"ds_read_b64_tr_b16 a[144:145], v214 offset:16896\n" \
"v_mfma_f32_16x16x32_bf16 v[104:107], a[52:55], a[20:23], v[104:107]\n" \
"ds_read_b64_tr_b16 a[146:147], v214 offset:18944\n" \
"v_mfma_f32_16x16x32_bf16 v[108:111], a[56:59], a[16:19], v[108:111]\n" \
"ds_read_b64_tr_b16 a[148:149], v214 offset:25088\n" \
"v_mfma_f32_16x16x32_bf16 v[108:111], a[60:63], a[20:23], v[108:111]\n" \
"ds_read_b64_tr_b16 a[150:151], v214 offset:27136\n" \
"v_mfma_f32_16x16x32_bf16 v[124:127], a[56:59], a[24:27], v[124:127]\n" \
"ds_read_b64_tr_b16 a[152:153], v215 offset:16896\n" \
"v_mfma_f32_16x16x32_bf16 v[124:127], a[60:63], a[28:31], v[124:127]\n" \
"ds_read_b64_tr_b16 a[154:155], v215 offset:18944\n" \
"v_mfma_f32_16x16x32_bf16 v[120:123], a[48:51], a[24:27], v[120:123]\n" \
"ds_read_b64_tr_b16 a[156:157], v215 offset:25088\n" \
"v_mfma_f32_16x16x32_bf16 v[120:123], a[52:55], a[28:31], v[120:123]\n" \
"s_waitcnt lgkmcnt(4)\n" \
"ds_read_b64_tr_b16 a[158:159], v215 offset:27136\n" \
"v_mfma_f32_16x16x32_bf16 v[64:67], a[128:131], a[192:195], v[64:67]\n" \
"ds_read_b128 a[224:227], v226 offset:0\n" \
"ds_read_b64_tr_b16 a[160:161], v227 offset:16384\n" \
"v_mfma_f32_16x16x32_bf16 v[64:67], a[132:135], a[196:199], v[64:67]\n" \
"ds_read_b64_tr_b16 a[162:163], v227 offset:18432\n" \
"v_mfma_f32_16x16x32_bf16 v[80:83], a[128:131], a[200:203], v[80:83]\n" \
"ds_read_b64_tr_b16 a[164:165], v227 offset:24576\n" \
"v_mfma_f32_16x16x32_bf16 v[80:83], a[132:135], a[204:207], v[80:83]\n" \
"ds_read_b128 a[228:231], v226 offset:2048\n" \
"ds_read_b64_tr_b16 a[166:167], v227 offset:26624\n" \
"v_mfma_f32_16x16x32_bf16 v[84:87], a[136:139], a[200:203], v[84:87]\n" \
"ds_read_b64_tr_b16 a[168:169], v228 offset:16384\n" \
"v_mfma_f32_16x16x32_bf16 v[84:87], a[140:143], a[204:207], v[84:87]\n" \
"ds_read_b64_tr_b16 a[170:171], v228 offset:18432\n" \
"v_mfma_f32_16x16x32_bf16 v[68:71], a[136:139], a[192:195], v[68:71]\n" \
"ds_read_b128 a[232:235], v226 offset:256\n" \
"ds_read_b64_tr_b16 a[172:173], v228 offset:24576\n" \
"v_mfma_f32_16x16x32_bf16 v[68:71], a[140:143], a[196:199], v[68:71]\n" \
"s_waitcnt lgkmcnt(6)\n" \
"ds_read_b64_tr_b16 a[174:175], v228 offset:26624\n" \
"v_mfma_f32_16x16x32_bf16 v[72:75], a[144:147], a[192:195], v[72:75]\n" \
"ds_read_b64_tr_b16 a[176:177], v227 offset:16896\n" \
"v_mfma_f32_16x16x32_bf16 v[72:75], a[148:151], a[196:199], v[72:75]\n" \
"ds_read_b128 a[236:239], v226 offset:2304\n" \
"ds_read_b64_tr_b16 a[178:179], v227 offset:18944\n" \
"v_mfma_f32_16x16x32_bf16 v[88:91], a[144:147], a[200:203], v[88:91]\n" \
"ds_read_b64_tr_b16 a[180:181], v227 offset:25088\n" \
"v_mfma_f32_16x16x32_bf16 v[88:91], a[148:151], a[204:207], v[88:91]\n" \
"ds_read_b64_tr_b16 a[182:183], v227 offset:27136\n" \
"v_mfma_f32_16x16x32_bf16 v[92:95], a[152:155], a[200:203], v[92:95]\n" \
"ds_read_b64_tr_b16 a[184:185], v228 offset:16896\n" \
"v_mfma_f32_16x16x32_bf16 v[92:95], a[156:159], a[204:207], v[92:95]\n" \
"ds_read_b64_tr_b16 a[186:187], v228 offset:18944\n" \
"v_mfma_f32_16x16x32_bf16 v[76:79], a[152:155], a[192:195], v[76:79]\n" \
"ds_read_b64_tr_b16 a[188:189], v228 offset:25088\n" \
"v_mfma_f32_16x16x32_bf16 v[76:79], a[156:159], a[196:199], v[76:79]\n" \
"s_waitcnt lgkmcnt(4)\n" \
"ds_read_b64_tr_b16 a[190:191], v228 offset:27136\n" \
"v_mfma_f32_16x16x32_bf16 v[64:67], a[160:163], a[224:227], v[64:67]\n" \
"ds_read_b128 a[208:211], v213 offset:4096\n" \
"v_mfma_f32_16x16x32_bf16 v[64:67], a[164:167], a[228:231], v[64:67]\n" \
"ds_read_b128 a[212:215], v213 offset:6144\n" \
"v_mfma_f32_16x16x32_bf16 v[80:83], a[160:163], a[232:235], v[80:83]\n" \
"ds_read_b128 a[240:243], v226 offset:4096\n" \
"v_mfma_f32_16x16x32_bf16 v[80:83], a[164:167], a[236:239], v[80:83]\n" \
"ds_read_b128 a[244:247], v226 offset:6144\n" \
"v_mfma_f32_16x16x32_bf16 v[84:87], a[168:171], a[232:235], v[84:87]\n" \
"ds_read_b128 a[216:219], v213 offset:4352\n" \
"v_cvt_pk_bf16_f32 v64, v64, v65\n" \
"v_mfma_f32_16x16x32_bf16 v[84:87], a[172:175], a[236:239], v[84:87]\n" \
"ds_read_b128 a[220:223], v213 offset:6400\n" \
"v_cvt_pk_bf16_f32 v65, v66, v67\n" \
"v_mfma_f32_16x16x32_bf16 v[68:71], a[168:171], a[224:227], v[68:71]\n" \
"ds_read_b128 a[248:251], v226 offset:4352\n" \
"v_mfma_f32_16x16x32_bf16 v[68:71], a[172:175], a[228:231], v[68:71]\n" \
"s_waitcnt lgkmcnt(4)\n" \
"ds_read_b128 a[252:255], v226 offset:6400\n" \
"v_mfma_f32_16x16x32_bf16 v[72:75], a[176:179], a[224:227], v[72:75]\n" \
"ds_write_b64 v128, v[64:65] offset:0\n" \
"v_mfma_f32_16x16x32_bf16 v[72:75], a[180:183], a[228:231], v[72:75]\n" \
"v_cvt_pk_bf16_f32 v80, v80, v81\n" \
"v_cvt_pk_bf16_f32 v81, v82, v83\n" \
"v_mfma_f32_16x16x32_bf16 v[88:91], a[176:179], a[232:235], v[88:91]\n" \
"ds_write_b64 v128, v[80:81] offset:2304\n" \
"v_mfma_f32_16x16x32_bf16 v[88:91], a[180:183], a[236:239], v[88:91]\n" \
"v_cvt_pk_bf16_f32 v84, v84, v85\n" \
"v_cvt_pk_bf16_f32 v85, v86, v87\n" \
"v_mfma_f32_16x16x32_bf16 v[92:95], a[184:187], a[232:235], v[92:95]\n" \
"ds_write_b64 v128, v[84:85] offset:2336\n" \
"v_mfma_f32_16x16x32_bf16 v[92:95], a[188:191], a[236:239], v[92:95]\n" \
"v_cvt_pk_bf16_f32 v68, v68, v69\n" \
"v_cvt_pk_bf16_f32 v69, v70, v71\n" \
"v_mfma_f32_16x16x32_bf16 v[76:79], a[184:187], a[224:227], v[76:79]\n" \
"ds_write_b64 v128, v[68:69] offset:32\n" \
"v_mfma_f32_16x16x32_bf16 v[76:79], a[188:191], a[228:231], v[76:79]\n" \
"v_cvt_pk_bf16_f32 v72, v72, v73\n" \
"v_cvt_pk_bf16_f32 v73, v74, v75\n" \
"v_mfma_f32_16x16x32_bf16 v[96:99], a[128:131], a[208:211], v[96:99]\n" \
"ds_write_b64 v128, v[72:73] offset:64\n" \
"v_mfma_f32_16x16x32_bf16 v[96:99], a[132:135], a[212:215], v[96:99]\n" \
"s_waitcnt lgkmcnt(2)\n" \
"v_cvt_pk_bf16_f32 v88, v88, v89\n" \
"v_cvt_pk_bf16_f32 v89, v90, v91\n" \
"v_mfma_f32_16x16x32_bf16 v[96:99], a[160:163], a[240:243], v[96:99]\n" \
"ds_write_b64 v128, v[88:89] offset:2368\n" \
"v_mfma_f32_16x16x32_bf16 v[96:99], a[164:167], a[244:247], v[96:99]\n" \
"v_cvt_pk_bf16_f32 v92, v92, v93\n" \
"v_cvt_pk_bf16_f32 v93, v94, v95\n" \
"v_mfma_f32_16x16x32_bf16 v[100:103], a[168:171], a[240:243], v[100:103]\n" \
"ds_write_b64 v128, v[92:93] offset:2400\n" \
"v_mfma_f32_16x16x32_bf16 v[100:103], a[172:175], a[244:247], v[100:103]\n" \
"v_cvt_pk_bf16_f32 v76, v76, v77\n" \
"v_cvt_pk_bf16_f32 v77, v78, v79\n" \
"v_mfma_f32_16x16x32_bf16 v[100:103], a[136:139], a[208:211], v[100:103]\n" \
"ds_write_b64 v128, v[76:77] offset:96\n" \
"v_mfma_f32_16x16x32_bf16 v[100:103], a[140:143], a[212:215], v[100:103]\n" \
"ds_read_b128 v[140:143], v129 offset:0\n" \
"v_mfma_f32_16x16x32_bf16 v[104:107], a[144:147], a[208:211], v[104:107]\n" \
"ds_read_b128 v[144:147], v129 offset:1152\n" \
"v_mfma_f32_16x16x32_bf16 v[104:107], a[148:151], a[212:215], v[104:107]\n" \
"ds_read_b128 v[148:151], v129 offset:2304\n" \
"v_mfma_f32_16x16x32_bf16 v[104:107], a[176:179], a[240:243], v[104:107]\n" \
"ds_read_b128 v[152:155], v129 offset:3456\n" \
"v_mfma_f32_16x16x32_bf16 v[104:107], a[180:183], a[244:247], v[104:107]\n" \
"v_cvt_pk_bf16_f32 v96, v96, v97\n" \
"v_cvt_pk_bf16_f32 v97, v98, v99\n" \
"v_mfma_f32_16x16x32_bf16 v[108:111], a[184:187], a[240:243], v[108:111]\n" \
"ds_write_b64 v128, v[96:97] offset:0\n" \
"v_mfma_f32_16x16x32_bf16 v[108:111], a[188:191], a[244:247], v[108:111]\n" \
"v_cvt_pk_bf16_f32 v100, v100, v101\n" \
"v_cvt_pk_bf16_f32 v101, v102, v103\n" \
"v_mfma_f32_16x16x32_bf16 v[108:111], a[152:155], a[208:211], v[108:111]\n" \
"s_add_u32 s70, s77, 0\n" \
"ds_write_b64 v128, v[100:101] offset:32\n" \
"v_mfma_f32_16x16x32_bf16 v[108:111], a[156:159], a[212:215], v[108:111]\n" \
"s_waitcnt lgkmcnt(5)\n" \
"v_cvt_pk_bf16_f32 v104, v104, v105\n" \
"v_cvt_pk_bf16_f32 v105, v106, v107\n" \
"v_mfma_f32_16x16x32_bf16 v[112:115], a[128:131], a[216:219], v[112:115]\n" \
"buffer_store_dwordx4 v[140:143], v130, s[52:55], s70 offen sc1\n" \
"ds_write_b64 v128, v[104:105] offset:64\n" \
"v_mfma_f32_16x16x32_bf16 v[112:115], a[132:135], a[220:223], v[112:115]\n" \
"v_mfma_f32_16x16x32_bf16 v[112:115], a[160:163], a[248:251], v[112:115]\n" \
"v_cvt_pk_bf16_f32 v108, v108, v109\n" \
"v_cvt_pk_bf16_f32 v109, v110, v111\n" \
"v_mfma_f32_16x16x32_bf16 v[112:115], a[164:167], a[252:255], v[112:115]\n" \
"ds_write_b64 v128, v[108:109] offset:96\n" \
"v_mfma_f32_16x16x32_bf16 v[116:119], a[168:171], a[248:251], v[116:119]\n" \
"s_add_u32 s70, s77, 65536\n" \
"ds_read_b128 v[156:159], v129 offset:0\n" \
"s_waitcnt lgkmcnt(7)\n" \
"v_mfma_f32_16x16x32_bf16 v[116:119], a[172:175], a[252:255], v[116:119]\n" \
"buffer_store_dwordx4 v[144:147], v130, s[52:55], s70 offen sc1\n" \
"ds_read_b128 v[160:163], v129 offset:1152\n" \
"v_mfma_f32_16x16x32_bf16 v[116:119], a[136:139], a[216:219], v[116:119]\n" \
"v_cvt_pk_bf16_f32 v112, v112, v113\n" \
"v_cvt_pk_bf16_f32 v113, v114, v115\n" \
"v_mfma_f32_16x16x32_bf16 v[116:119], a[140:143], a[220:223], v[116:119]\n" \
"ds_write_b64 v128, v[112:113] offset:2304\n" \
"v_mfma_f32_16x16x32_bf16 v[120:123], a[144:147], a[216:219], v[120:123]\n" \
"v_mfma_f32_16x16x32_bf16 v[120:123], a[148:151], a[220:223], v[120:123]\n" \
"s_add_u32 s70, s77, 4096\n" \
"s_waitcnt lgkmcnt(8)\n" \
"v_mfma_f32_16x16x32_bf16 v[120:123], a[176:179], a[248:251], v[120:123]\n" \
"buffer_store_dwordx4 v[148:151], v130, s[52:55], s70 offen sc1\n" \
"v_cvt_pk_bf16_f32 v116, v116, v117\n" \
"v_cvt_pk_bf16_f32 v117, v118, v119\n" \
"v_mfma_f32_16x16x32_bf16 v[120:123], a[180:183], a[252:255], v[120:123]\n" \
"ds_write_b64 v128, v[116:117] offset:2336\n" \
"v_mfma_f32_16x16x32_bf16 v[124:127], a[184:187], a[248:251], v[124:127]\n" \
"v_mfma_f32_16x16x32_bf16 v[124:127], a[188:191], a[252:255], v[124:127]\n" \
"v_mfma_f32_16x16x32_bf16 v[124:127], a[152:155], a[216:219], v[124:127]\n" \
"s_add_u32 s70, s77, 69632\n" \
"v_cvt_pk_bf16_f32 v120, v120, v121\n" \
"v_cvt_pk_bf16_f32 v121, v122, v123\n" \
"v_mfma_f32_16x16x32_bf16 v[124:127], a[156:159], a[220:223], v[124:127]\n" \
"s_waitcnt lgkmcnt(8)\n" \
"ds_write_b64 v128, v[120:121] offset:2368\n" \
"buffer_store_dwordx4 v[152:155], v130, s[52:55], s70 offen sc1\n" \
"s_add_u32 s70, s77, 131072\n" \
"s_waitcnt lgkmcnt(4)\n" \
"s_nop 2\n" \
"v_cvt_pk_bf16_f32 v124, v124, v125\n" \
"v_cvt_pk_bf16_f32 v125, v126, v127\n" \
"ds_write_b64 v128, v[124:125] offset:2400\n" \
"ds_read_b128 v[164:167], v129 offset:2304\n" \
"ds_read_b128 v[168:171], v129 offset:3456\n" \
"buffer_store_dwordx4 v[156:159], v130, s[52:55], s70 offen sc1\n" \
"s_add_u32 s70, s77, 196608\n" \
"s_waitcnt lgkmcnt(6)\n" \
"buffer_store_dwordx4 v[160:163], v130, s[52:55], s70 offen sc1\n" \
"s_add_u32 s70, s77, 135168\n" \
"s_waitcnt lgkmcnt(1)\n" \
"buffer_store_dwordx4 v[164:167], v130, s[52:55], s70 offen sc1\n" \
"s_add_u32 s70, s77, 200704\n" \
"s_waitcnt lgkmcnt(0)\n" \
"buffer_store_dwordx4 v[168:171], v130, s[52:55], s70 offen sc1\n" \
"s_endpgm\n" \
"Lpg13597_%=:\n" \
"s_cbranch_execz Lcb1_%=\n" \
"Lo64_%=:\n" \
"v_mfma_f32_16x16x32_bf16 v[64:67], a[32:35], a[0:3], v[64:67]\n" \
"ds_read_b128 a[24:27], v213 offset:4352\n" \
"s_add_u32 m0, s75, s95\n" \
"v_mfma_f32_16x16x32_bf16 v[64:67], a[36:39], a[4:7], v[64:67]\n" \
"s_waitcnt lgkmcnt(1)\n" \
"ds_read_b128 a[28:31], v213 offset:6400\n" \
"global_load_lds_dwordx4 v222, s[36:37] offset:0 sc1\n" \
"v_mfma_f32_16x16x32_bf16 v[68:71], a[40:43], a[0:3], v[68:71]\n" \
"s_sub_u32 s74, s74, 32768\n" \
"s_cselect_b32 s74, 131072, s74\n" \
"ds_read_b64_tr_b16 a[48:49], v214 offset:16896\n" \
"v_mfma_f32_16x16x32_bf16 v[68:71], a[44:47], a[4:7], v[68:71]\n" \
"ds_read_b64_tr_b16 a[50:51], v214 offset:18944\n" \
"v_mfma_f32_16x16x32_bf16 v[84:87], a[40:43], a[8:11], v[84:87]\n" \
"ds_read_b64_tr_b16 a[52:53], v214 offset:25088\n" \
"v_mfma_f32_16x16x32_bf16 v[84:87], a[44:47], a[12:15], v[84:87]\n" \
"ds_read_b64_tr_b16 a[54:55], v214 offset:27136\n" \
"global_load_lds_dwordx4 v222, s[36:37] offset:1024 sc1\n" \
"v_mfma_f32_16x16x32_bf16 v[80:83], a[32:35], a[8:11], v[80:83]\n" \
"ds_read_b64_tr_b16 a[56:57], v215 offset:16896\n" \
"v_mfma_f32_16x16x32_bf16 v[80:83], a[36:39], a[12:15], v[80:83]\n" \
"ds_read_b64_tr_b16 a[58:59], v215 offset:18944\n" \
"v_mfma_f32_16x16x32_bf16 v[96:99], a[32:35], a[16:19], v[96:99]\n" \
"ds_read_b64_tr_b16 a[60:61], v215 offset:25088\n" \
"v_mfma_f32_16x16x32_bf16 v[96:99], a[36:39], a[20:23], v[96:99]\n" \
"ds_read_b64_tr_b16 a[62:63], v215 offset:27136\n" \
"global_load_lds_dwordx4 v222, s[36:37] offset:2048 sc1\n" \
"v_mfma_f32_16x16x32_bf16 v[100:103], a[40:43], a[16:19], v[100:103]\n" \
"v_add_u32 v213, s74, v211\n" \
"v_add_u32 v214, s74, v212\n" \
"v_mfma_f32_16x16x32_bf16 v[100:103], a[44:47], a[20:23], v[100:103]\n" \
"s_waitcnt lgkmcnt(3)\n" \
"v_xor_b32 v215, 32, v214\n" \
"v_mfma_f32_16x16x32_bf16 v[116:119], a[40:43], a[24:27], v[116:119]\n" \
"v_mfma_f32_16x16x32_bf16 v[116:119], a[44:47], a[28:31], v[116:119]\n" \
"global_load_lds_dwordx4 v222, s[36:37] offset:3072 sc1\n" \
"v_mfma_f32_16x16x32_bf16 v[112:115], a[32:35], a[24:27], v[112:115]\n" \
"s_waitcnt vmcnt(20) lgkmcnt(0)\n" \
"s_add_u32 s36, s36, 0x1000\n" \
"v_mfma_f32_16x16x32_bf16 v[112:115], a[36:39], a[28:31], v[112:115]\n" \
"s_barrier\n" \
"v_mfma_f32_16x16x32_bf16 v[72:75], a[48:51], a[0:3], v[72:75]\n" \
"s_add_u32 m0, m0, 0x5000\n" \
"ds_read_b64_tr_b16 a[32:33], v214 offset:16384\n" \
"v_mfma_f32_16x16x32_bf16 v[72:75], a[52:55], a[4:7], v[72:75]\n" \
"ds_read_b64_tr_b16 a[34:35], v214 offset:18432\n" \
"global_load_lds_dwordx4 v216, s[38:39] offset:-4096\n" \
"v_mfma_f32_16x16x32_bf16 v[76:79], a[56:59], a[0:3], v[76:79]\n" \
"ds_read_b64_tr_b16 a[36:37], v214 offset:24576\n" \
"v_mfma_f32_16x16x32_bf16 v[76:79], a[60:63], a[4:7], v[76:79]\n" \
"ds_read_b128 a[0:3], v213 offset:0\n" \
"v_mfma_f32_16x16x32_bf16 v[92:95], a[56:59], a[8:11], v[92:95]\n" \
"ds_read_b128 a[4:7], v213 offset:2048\n" \
"v_mfma_f32_16x16x32_bf16 v[92:95], a[60:63], a[12:15], v[92:95]\n" \
"ds_read_b64_tr_b16 a[38:39], v214 offset:26624\n" \
"global_load_lds_dwordx4 v217, s[38:39] offset:-3072\n" \
"v_mfma_f32_16x16x32_bf16 v[88:91], a[48:51], a[8:11], v[88:91]\n" \
"ds_read_b64_tr_b16 a[40:41], v215 offset:16384\n" \
"v_mfma_f32_16x16x32_bf16 v[88:91], a[52:55], a[12:15], v[88:91]\n" \
"ds_read_b128 a[8:11], v213 offset:256\n" \
"v_mfma_f32_16x16x32_bf16 v[104:107], a[48:51], a[16:19], v[104:107]\n" \
"ds_read_b128 a[12:15], v213 offset:2304\n" \
"v_mfma_f32_16x16x32_bf16 v[104:107], a[52:55], a[20:23], v[104:107]\n" \
"ds_read_b64_tr_b16 a[42:43], v215 offset:18432\n" \
"global_load_lds_dwordx4 v218, s[38:39] offset:-2048\n" \
"v_mfma_f32_16x16x32_bf16 v[108:111], a[56:59], a[16:19], v[108:111]\n" \
"ds_read_b64_tr_b16 a[44:45], v215 offset:24576\n" \
"v_mfma_f32_16x16x32_bf16 v[108:111], a[60:63], a[20:23], v[108:111]\n" \
"ds_read_b128 a[16:19], v213 offset:4096\n" \
"global_load_lds_dwordx4 v219, s[38:39] offset:-1024\n" \
"v_mfma_f32_16x16x32_bf16 v[124:127], a[56:59], a[24:27], v[124:127]\n" \
"s_add_u32 s38, s38, 0x20000\n" \
"s_addc_u32 s39, s39, 0\n" \
"ds_read_b128 a[20:23], v213 offset:6144\n" \
"v_mfma_f32_16x16x32_bf16 v[124:127], a[60:63], a[28:31], v[124:127]\n" \
"s_sub_u32 s75, s75, 32768\n" \
"s_cselect_b32 s75, 131072, s75\n" \
"ds_read_b64_tr_b16 a[46:47], v215 offset:26624\n" \
"v_mfma_f32_16x16x32_bf16 v[120:123], a[48:51], a[24:27], v[120:123]\n" \
"s_sub_u32 s72, s72, 1\n" \
"s_cmp_lg_u32 s72, 0\n" \
"v_mfma_f32_16x16x32_bf16 v[120:123], a[52:55], a[28:31], v[120:123]\n" \
"s_waitcnt lgkmcnt(3)\n" \
"s_cbranch_scc1 Lo64_%=\n" \
"s_setpc_b64 s[68:69]\n" \
"Lhot_end_%=:\n" \
"Lmask0_%=:\n" \
"Lpg13686_%=:\n" \
"s_cbranch_execz Lcb0_%=\n" \
"s_waitcnt lgkmcnt(0)\n" \
"v_subrev_u32 v0, 0, v251\n" \
"v_subrev_u32 v1, 1, v251\n" \
"v_subrev_u32 v2, 2, v251\n" \
"v_subrev_u32 v3, 3, v251\n" \
"v_subrev_u32 v4, 16, v251\n" \
"v_subrev_u32 v5, 17, v251\n" \
"v_subrev_u32 v6, 18, v251\n" \
"v_subrev_u32 v7, 19, v251\n" \
"v_subrev_u32 v32, 32, v251\n" \
"v_subrev_u32 v33, 33, v251\n" \
"v_subrev_u32 v34, 34, v251\n" \
"v_subrev_u32 v35, 35, v251\n" \
"v_subrev_u32 v36, 48, v251\n" \
"v_subrev_u32 v37, 49, v251\n" \
"v_subrev_u32 v38, 50, v251\n" \
"v_subrev_u32 v39, 51, v251\n" \
"v_ashrrev_i32 v0, 31, v0\n" \
"v_ashrrev_i32 v1, 31, v1\n" \
"v_ashrrev_i32 v2, 31, v2\n" \
"v_ashrrev_i32 v3, 31, v3\n" \
"v_ashrrev_i32 v4, 31, v4\n" \
"v_ashrrev_i32 v5, 31, v5\n" \
"v_ashrrev_i32 v6, 31, v6\n" \
"v_ashrrev_i32 v7, 31, v7\n" \
"v_ashrrev_i32 v32, 31, v32\n" \
"v_ashrrev_i32 v33, 31, v33\n" \
"v_ashrrev_i32 v34, 31, v34\n" \
"v_ashrrev_i32 v35, 31, v35\n" \
"v_ashrrev_i32 v36, 31, v36\n" \
"v_ashrrev_i32 v37, 31, v37\n" \
"v_ashrrev_i32 v38, 31, v38\n" \
"v_ashrrev_i32 v39, 31, v39\n" \
"v_and_b32 v0, 0xff800000, v0\n" \
"v_and_b32 v1, 0xff800000, v1\n" \
"v_and_b32 v2, 0xff800000, v2\n" \
"v_and_b32 v3, 0xff800000, v3\n" \
"v_and_b32 v4, 0xff800000, v4\n" \
"v_and_b32 v5, 0xff800000, v5\n" \
"v_and_b32 v6, 0xff800000, v6\n" \
"v_and_b32 v7, 0xff800000, v7\n" \
"v_and_b32 v32, 0xff800000, v32\n" \
"v_and_b32 v33, 0xff800000, v33\n" \
"v_and_b32 v34, 0xff800000, v34\n" \
"v_and_b32 v35, 0xff800000, v35\n" \
"v_and_b32 v36, 0xff800000, v36\n" \
"v_and_b32 v37, 0xff800000, v37\n" \
"v_and_b32 v38, 0xff800000, v38\n" \
"v_and_b32 v39, 0xff800000, v39\n" \
"s_branch Lmask0r_%=\n" \
"Lpl1_%=:\n" \
"s_mov_b32 s31, 0\n" \
"Lpk1_%=:\n" \
"s_sleep 2\n" \
"buffer_load_dword v244, v245, s[56:59], s91 offen sc1\n" \
"s_waitcnt vmcnt(0)\n" \
"v_cmp_ne_u32 vcc, s60, v244\n" \
"s_nop 1\n" \
"s_and_b32 vcc_hi, vcc_lo, 255\n" \
"s_cbranch_scc0 Lpr1_%=\n" \
"s_add_u32 s31, s31, 1\n" \
"s_cmp_lt_u32 s31, 0x40000\n" \
"s_cbranch_scc1 Lpk1_%=\n" \
"s_branch Lpr1_%=\n" \
"Lrs0_%=:\n" \
"s_call_b64 s[68:69], Lrest_%=\n" \
"s_branch Lrr0_%=\n" \
"Lrest_%=:\n" \
"s_mov_b32 s31, 0\n" \
"s_waitcnt vmcnt(0)\n" \
"Lrk_%=:\n" \
"v_lshrrev_b32 v253, 2, v248\n" \
"s_sleep 2\n" \
"buffer_load_dword v253, v253, s[56:59], s79 offen sc1\n" \
"s_waitcnt vmcnt(0)\n" \
"v_cmp_ne_u32 s[52:53], s60, v253\n" \
"s_add_u32 s31, s31, 1\n" \
"s_cmp_lt_u32 s31, 0x40000\n" \
"s_cselect_b32 s54, 0xff, 0\n" \
"s_and_b32 s52, s52, s54\n" \
"s_cmp_lg_u32 s52, 0\n" \
"s_cbranch_scc1 Lrk_%=\n" \
"s_mov_b64 s[10:11], 0\n" \
"s_setpc_b64 s[68:69]\n" \
"Lrs1_%=:\n" \
"s_call_b64 s[68:69], Lrest_%=\n" \
"s_branch Lrr1_%=\n" \
"Lrs2_%=:\n" \
"s_call_b64 s[68:69], Lrest_%=\n" \
"s_branch Lrr2_%=\n" \
"Lrs3_%=:\n" \
"s_call_b64 s[68:69], Lrest_%=\n" \
"s_branch Lrr3_%=\n" \
"LF7_%=:\n" \
"v_mfma_f32_16x16x32_bf16 v[24:27], a[192:195], a[176:179], v[236:239]\n" \
"s_waitcnt vmcnt(12)\n" \
"ds_read_b128 v[192:195], v246 offset:16384\n" \
"s_bfe_u32 s88, %1, 0x20001\n" \
"v_mfma_f32_16x16x32_bf16 v[24:27], a[196:199], a[180:183], v[24:27]\n" \
"s_lshl_b32 s88, s88, 2\n" \
"ds_read_b128 v[196:199], v246 offset:18432\n" \
"v_cvt_pk_bf16_f32 v91, v94, v95\n" \
"v_mfma_f32_16x16x32_bf16 v[24:27], a[200:203], a[184:187], v[24:27]\n" \
"s_lshr_b32 s70, %1, 6\n" \
"ds_read_b128 v[200:203], v246 offset:20480\n" \
"s_add_u32 s88, s88, s70\n" \
"v_mfma_f32_16x16x32_bf16 v[24:27], a[204:207], a[188:191], v[24:27]\n" \
"ds_read_b128 a[252:255], v246 offset:14592\n" \
"v_mov_b32 v78, v240\n" \
"v_bfe_u32 v69, v248, 8, 1\n" \
"v_bfe_u32 v70, v248, 6, 2\n" \
"v_exp_f32 v104, v104\n" \
"v_exp_f32 v105, v105\n" \
"v_add_f32 v241, v241, v104\n" \
"v_exp_f32 v106, v106\n" \
"v_add_f32 v241, v241, v105\n" \
"v_cvt_pk_bf16_f32 v104, v104, v105\n" \
"v_exp_f32 v107, v107\n" \
"v_add_f32 v241, v241, v106\n" \
"v_exp_f32 v108, v108\n" \
"v_add_f32 v241, v241, v107\n" \
"v_mfma_f32_16x16x32_bf16 v[16:19], a[200:203], a[168:171], v[232:235]\n" \
"s_lshr_b32 s71, s22, 7\n" \
"ds_read_b128 v[204:207], v246 offset:22528\n" \
"s_sub_u32 s71, s63, s71\n" \
"v_mfma_f32_16x16x32_bf16 v[16:19], a[204:207], a[172:175], v[16:19]\n" \
"s_add_u32 s71, s71, 6\n" \
"ds_read_b128 v[208:211], v246 offset:16640\n" \
"s_and_b32 s71, s71, 7\n" \
"v_mfma_f32_16x16x32_bf16 v[16:19], a[192:195], a[160:163], v[16:19]\n" \
"ds_read_b128 v[212:215], v246 offset:18688\n" \
"v_lshl_add_u32 v70, v69, 3, v70\n" \
"v_mfma_f32_16x16x32_bf16 v[16:19], a[196:199], a[164:167], v[16:19]\n" \
"ds_read_b128 v[216:219], v246 offset:20736\n" \
"v_mul_u32_u24 v70, 6144, v70\n" \
"v_cvt_pk_bf16_f32 v105, v106, v107\n" \
"v_exp_f32 v109, v109\n" \
"v_add_f32 v241, v241, v108\n" \
"v_exp_f32 v110, v110\n" \
"v_add_f32 v241, v241, v109\n" \
"v_cvt_pk_bf16_f32 v106, v108, v109\n" \
"v_exp_f32 v111, v111\n" \
"v_add_f32 v241, v241, v110\n" \
"v_exp_f32 v112, v112\n" \
"v_add_f32 v241, v241, v111\n" \
"v_permlane16_swap_b32 v78, v240\n" \
"v_add_f32 v240, v240, v78\n" \
"v_mov_b32 v78, v240\n" \
"v_mfma_f32_16x16x32_bf16 v[20:23], a[208:211], a[160:163], v[232:235]\n" \
"s_and_b32 s70, %1, 1\n" \
"ds_read_b128 v[220:223], v246 offset:22784\n" \
"s_lshl_b32 s70, s70, 3\n" \
"v_mfma_f32_16x16x32_bf16 v[20:23], a[212:215], a[164:167], v[20:23]\n" \
"s_add_u32 s70, s70, s71\n" \
"ds_read_b128 a[192:195], v247 offset:16384\n" \
"v_lshlrev_b32 v69, 5, v69\n" \
"v_mfma_f32_16x16x32_bf16 v[20:23], a[216:219], a[168:171], v[20:23]\n" \
"ds_read_b128 a[196:199], v247 offset:24576\n" \
"v_and_b32 v71, 0x30, v248\n" \
"v_mfma_f32_16x16x32_bf16 v[20:23], a[220:223], a[172:175], v[20:23]\n" \
"s_lshl_b32 s87, s70, 12\n" \
"s_lshl_b32 s70, s70, 7\n" \
"ds_read_b128 a[200:203], v247 offset:17408\n" \
"v_cvt_pk_bf16_f32 v107, v110, v111\n" \
"v_exp_f32 v113, v113\n" \
"v_add_f32 v242, v242, v112\n" \
"v_exp_f32 v114, v114\n" \
"v_add_f32 v242, v242, v113\n" \
"v_cvt_pk_bf16_f32 v112, v112, v113\n" \
"v_exp_f32 v115, v115\n" \
"v_add_f32 v242, v242, v114\n" \
"v_exp_f32 v116, v116\n" \
"v_add_f32 v242, v242, v115\n" \
"v_permlane32_swap_b32 v78, v240\n" \
"v_add_f32 v240, v240, v78\n" \
"v_rcp_f32 v78, v240\n" \
"v_mul_f32 v240, 0x4d800000, v240\n" \
"v_add_u32 v79, 0x1c90080, v249\n" \
"v_cndmask_b32 v79, v249, v79, s[16:17]\n" \
"v_accvgpr_read_b32 v96, a0\n" \
"v_accvgpr_read_b32 v97, a1\n" \
"v_accvgpr_read_b32 v98, a2\n" \
"v_accvgpr_read_b32 v99, a3\n" \
"v_accvgpr_read_b32 v100, a4\n" \
"v_accvgpr_read_b32 v101, a5\n" \
"v_accvgpr_read_b32 v102, a6\n" \
"v_accvgpr_read_b32 v103, a7\n" \
"v_pk_mul_f32 v[96:97], v[96:97], v[78:79] op_sel_hi:[1,0]\n" \
"v_pk_mul_f32 v[98:99], v[98:99], v[78:79] op_sel_hi:[1,0]\n" \
"v_pk_mul_f32 v[100:101], v[100:101], v[78:79] op_sel_hi:[1,0]\n" \
"v_pk_mul_f32 v[102:103], v[102:103], v[78:79] op_sel_hi:[1,0]\n" \
"v_cvt_pk_bf16_f32 v224, v96, v97\n" \
"v_cvt_pk_bf16_f32 v225, v98, v99\n" \
"v_mfma_f32_16x16x32_bf16 v[28:31], a[216:219], a[184:187], v[236:239]\n" \
"s_mul_i32 s84, s61, 6144\n" \
"ds_read_b128 a[204:207], v247 offset:25600\n" \
"s_lshl_b32 s71, s84, 4\n" \
"v_mfma_f32_16x16x32_bf16 v[28:31], a[220:223], a[188:191], v[28:31]\n" \
"v_xor_b32 v69, v69, v71\n" \
"v_lshrrev_b32 v71, 9, v248\n" \
"v_mfma_f32_16x16x32_bf16 v[28:31], a[208:211], a[176:179], v[28:31]\n" \
"s_add_u32 s70, s70, s71\n" \
"s_add_u32 s70, s70, 4096\n" \
"v_lshl_add_u32 v70, v71, 6, v70\n" \
"v_mfma_f32_16x16x32_bf16 v[28:31], a[212:215], a[180:183], v[28:31]\n" \
"s_waitcnt lgkmcnt(2)\n" \
"ds_read_b128 a[208:211], v247 offset:18432\n" \
"v_add_u32 v253, v69, v70\n" \
"v_cvt_pk_bf16_f32 v226, v100, v101\n" \
"v_cvt_pk_bf16_f32 v227, v102, v103\n" \
"v_cvt_pk_bf16_f32 v113, v114, v115\n" \
"v_exp_f32 v117, v117\n" \
"v_add_f32 v242, v242, v116\n" \
"v_pk_mul_f32 v[96:97], v[96:97], v[98:99]\n" \
"v_pk_fma_f32 v[96:97], v[100:101], v[102:103], v[96:97]\n" \
"v_fma_f32 v240, v96, v97, v240\n" \
"v_mfma_f32_16x16x32_bf16 v[8:11], v[192:195], a[176:179], v[236:239]\n" \
"s_add_u32 s48, s22, s70\n" \
"s_addc_u32 s49, s23, 0\n" \
"ds_read_b128 a[212:215], v247 offset:26624\n" \
"v_mfma_f32_16x16x32_bf16 v[8:11], v[196:199], a[180:183], v[8:11]\n" \
"s_add_u32 s50, s48, 23552\n" \
"s_addc_u32 s51, s49, 0\n" \
"ds_read_b128 a[216:219], v247 offset:19456\n" \
"v_mfma_f32_16x16x32_bf16 v[8:11], v[200:203], a[184:187], v[8:11]\n" \
"s_lshr_b32 s89, s22, 7\n" \
"ds_read_b128 a[220:223], v247 offset:27648\n" \
"s_sub_u32 s89, 3, s89\n" \
"v_mfma_f32_16x16x32_bf16 v[8:11], v[204:207], a[188:191], v[8:11]\n" \
"buffer_store_dwordx4 v[224:227], v79, s[56:59], s77 offen offset:0 sc1\n" \
"ds_read_b128 v[192:195], v246 offset:24576\n" \
"v_bfe_u32 v84, v248, 7, 2\n" \
"s_and_b32 s89, s89, 7\n" \
"v_accvgpr_read_b32 v96, a8\n" \
"v_accvgpr_read_b32 v97, a9\n" \
"v_accvgpr_read_b32 v98, a10\n" \
"v_accvgpr_read_b32 v99, a11\n" \
"v_accvgpr_read_b32 v100, a12\n" \
"v_accvgpr_read_b32 v101, a13\n" \
"v_accvgpr_read_b32 v102, a14\n" \
"v_accvgpr_read_b32 v103, a15\n" \
"v_pk_mul_f32 v[96:97], v[96:97], v[78:79] op_sel_hi:[1,0]\n" \
"v_pk_mul_f32 v[98:99], v[98:99], v[78:79] op_sel_hi:[1,0]\n" \
"v_pk_mul_f32 v[100:101], v[100:101], v[78:79] op_sel_hi:[1,0]\n" \
"v_pk_mul_f32 v[102:103], v[102:103], v[78:79] op_sel_hi:[1,0]\n" \
"v_cvt_pk_bf16_f32 v228, v96, v97\n" \
"v_cvt_pk_bf16_f32 v229, v98, v99\n" \
"v_cvt_pk_bf16_f32 v230, v100, v101\n" \
"v_cvt_pk_bf16_f32 v231, v102, v103\n" \
"v_pk_mul_f32 v[96:97], v[96:97], v[98:99]\n" \
"v_pk_fma_f32 v[96:97], v[100:101], v[102:103], v[96:97]\n" \
"v_fma_f32 v240, v96, v97, v240\n" \
"v_mfma_f32_16x16x32_bf16 v[56:59], a[232:235], a[184:187], v[236:239]\n" \
"s_and_b32 s90, %1, 1\n" \
"ds_read_b128 v[196:199], v246 offset:26624\n" \
"v_bfe_u32 v85, v248, 5, 2\n" \
"v_mfma_f32_16x16x32_bf16 v[56:59], a[236:239], a[188:191], v[56:59]\n" \
"s_lshl_b32 s90, s90, 3\n" \
"ds_read_b128 v[200:203], v246 offset:28672\n" \
"s_add_u32 s89, s89, s90\n" \
"v_mfma_f32_16x16x32_bf16 v[56:59], a[224:227], a[176:179], v[56:59]\n" \
"ds_read_b128 v[204:207], v246 offset:30720\n" \
"v_min_u32 v85, 2, v85\n" \
"v_mfma_f32_16x16x32_bf16 v[56:59], a[228:231], a[180:183], v[56:59]\n" \
"s_lshl_b32 s89, s89, 7\n" \
"s_lshr_b32 s90, %1, 3\n" \
"v_mad_u32_u24 v84, v84, 3, v85\n" \
"s_lshl_b32 s90, s90, 2\n" \
"v_mfma_f32_16x16x32_bf16 v[48:51], a[224:227], a[160:163], v[232:235]\n" \
"s_add_u32 s90, s90, s61\n" \
"v_and_b32 v85, 16, v248\n" \
"s_mul_i32 s90, s90, 24576\n" \
"v_mfma_f32_16x16x32_bf16 v[48:51], a[228:231], a[164:167], v[48:51]\n" \
"s_add_u32 s89, s89, s90\n" \
"v_lshl_add_u32 v85, v85, 2, s89\n" \
"v_lshl_add_u32 v84, v84, 11, v85\n" \
"v_mfma_f32_16x16x32_bf16 v[48:51], a[232:235], a[168:171], v[48:51]\n" \
"s_add_u32 m0, s84, 143360\n" \
"v_exp_f32 v118, v118\n" \
"v_mfma_f32_16x16x32_bf16 v[48:51], a[236:239], a[172:175], v[48:51]\n" \
"ds_read_b128 a[224:227], v247 offset:20480\n" \
"global_load_lds_dwordx4 v253, s[48:49] offset:-4096\n" \
"v_mfma_f32_16x16x32_bf16 v[12:15], v[216:219], a[184:187], v[236:239]\n" \
"ds_read_b128 a[228:231], v247 offset:28672\n" \
"v_add_f32 v242, v242, v117\n" \
"v_mfma_f32_16x16x32_bf16 v[12:15], v[220:223], a[188:191], v[12:15]\n" \
"ds_read_b128 a[232:235], v247 offset:21504\n" \
"v_cvt_pk_bf16_f32 v114, v116, v117\n" \
"v_mfma_f32_16x16x32_bf16 v[12:15], v[208:211], a[176:179], v[12:15]\n" \
"ds_read_b128 a[236:239], v247 offset:29696\n" \
"global_load_lds_dwordx4 v253, s[48:49] offset:-2048\n" \
"v_mfma_f32_16x16x32_bf16 v[12:15], v[212:215], a[180:183], v[12:15]\n" \
"ds_read_b128 v[208:211], v246 offset:24832\n" \
"buffer_store_dwordx4 v[228:231], v79, s[56:59], s77 offen offset:2048 sc1\n" \
"v_exp_f32 v119, v119\n" \
"v_add_f32 v242, v242, v118\n" \
"v_exp_f32 v120, v120\n" \
"v_add_f32 v242, v242, v119\n" \
"v_mfma_f32_16x16x32_bf16 v[60:63], a[240:243], a[176:179], v[236:239]\n" \
"s_add_u32 s77, s77, 0x1000\n" \
"ds_read_b128 v[212:215], v246 offset:26880\n" \
"v_cvt_pk_bf16_f32 v115, v118, v119\n" \
"v_mfma_f32_16x16x32_bf16 v[60:63], a[244:247], a[180:183], v[60:63]\n" \
"ds_read_b128 v[216:219], v246 offset:28928\n" \
"global_load_lds_dwordx4 v253, s[48:49] offset:0\n" \
"v_mfma_f32_16x16x32_bf16 v[60:63], a[248:251], a[184:187], v[60:63]\n" \
"s_add_u32 s48, s48, 0x120000\n" \
"s_addc_u32 s49, s49, 0\n" \
"ds_read_b128 v[220:223], v246 offset:30976\n" \
"v_mfma_f32_16x16x32_bf16 v[60:63], a[252:255], a[188:191], v[60:63]\n" \
"v_accvgpr_read_b32 v96, a16\n" \
"v_accvgpr_read_b32 v97, a17\n" \
"v_accvgpr_read_b32 v98, a18\n" \
"v_accvgpr_read_b32 v99, a19\n" \
"v_accvgpr_read_b32 v100, a20\n" \
"v_accvgpr_read_b32 v101, a21\n" \
"v_accvgpr_read_b32 v102, a22\n" \
"v_accvgpr_read_b32 v103, a23\n" \
"v_exp_f32 v121, v121\n" \
"v_pk_mul_f32 v[96:97], v[96:97], v[78:79] op_sel_hi:[1,0]\n" \
"v_pk_mul_f32 v[98:99], v[98:99], v[78:79] op_sel_hi:[1,0]\n" \
"v_pk_mul_f32 v[100:101], v[100:101], v[78:79] op_sel_hi:[1,0]\n" \
"v_pk_mul_f32 v[102:103], v[102:103], v[78:79] op_sel_hi:[1,0]\n" \
"v_cvt_pk_bf16_f32 v224, v96, v97\n" \
"v_cvt_pk_bf16_f32 v225, v98, v99\n" \
"v_cvt_pk_bf16_f32 v226, v100, v101\n" \
"v_cvt_pk_bf16_f32 v227, v102, v103\n" \
"v_pk_mul_f32 v[96:97], v[96:97], v[98:99]\n" \
"v_pk_fma_f32 v[96:97], v[100:101], v[102:103], v[96:97]\n" \
"v_fma_f32 v240, v96, v97, v240\n" \
"v_mfma_f32_16x16x32_bf16 v[52:55], a[248:251], a[168:171], v[232:235]\n" \
"global_load_lds_dwordx4 v253, s[50:51] offset:-3072\n" \
"v_add_f32 v243, v243, v120\n" \
"v_mfma_f32_16x16x32_bf16 v[52:55], a[252:255], a[172:175], v[52:55]\n" \
"v_exp_f32 v122, v122\n" \
"v_mfma_f32_16x16x32_bf16 v[52:55], a[240:243], a[160:163], v[52:55]\n" \
"v_add_f32 v243, v243, v121\n" \
"v_cvt_pk_bf16_f32 v120, v120, v121\n" \
"v_mfma_f32_16x16x32_bf16 v[52:55], a[244:247], a[164:167], v[52:55]\n" \
"s_waitcnt lgkmcnt(0)\n" \
"buffer_store_dwordx4 v[224:227], v79, s[56:59], s77 offen offset:0 sc1\n" \
"ds_read_b128 a[240:243], v247 offset:22528\n" \
"global_load_lds_dwordx4 v253, s[50:51] offset:-1024\n" \
"v_accvgpr_read_b32 v96, a24\n" \
"v_accvgpr_read_b32 v97, a25\n" \
"v_accvgpr_read_b32 v98, a26\n" \
"v_accvgpr_read_b32 v99, a27\n" \
"v_accvgpr_read_b32 v100, a28\n" \
"v_accvgpr_read_b32 v101, a29\n" \
"v_accvgpr_read_b32 v102, a30\n" \
"v_accvgpr_read_b32 v103, a31\n" \
"v_exp_f32 v123, v123\n" \
"v_add_f32 v243, v243, v122\n" \
"v_exp_f32 v124, v124\n" \
"v_pk_mul_f32 v[96:97], v[96:97], v[78:79] op_sel_hi:[1,0]\n" \
"v_pk_mul_f32 v[98:99], v[98:99], v[78:79] op_sel_hi:[1,0]\n" \
"v_pk_mul_f32 v[100:101], v[100:101], v[78:79] op_sel_hi:[1,0]\n" \
"v_pk_mul_f32 v[102:103], v[102:103], v[78:79] op_sel_hi:[1,0]\n" \
"v_cvt_pk_bf16_f32 v228, v96, v97\n" \
"v_cvt_pk_bf16_f32 v229, v98, v99\n" \
"v_cvt_pk_bf16_f32 v230, v100, v101\n" \
"v_cvt_pk_bf16_f32 v231, v102, v103\n" \
"v_pk_mul_f32 v[96:97], v[96:97], v[98:99]\n" \
"v_pk_fma_f32 v[96:97], v[100:101], v[102:103], v[96:97]\n" \
"v_fma_f32 v240, v96, v97, v240\n" \
"v_mfma_f32_16x16x32_bf16 v[40:43], v[192:195], a[176:179], v[236:239]\n" \
"ds_read_b128 a[244:247], v247 offset:30720\n" \
"v_add_f32 v243, v243, v123\n" \
"v_mfma_f32_16x16x32_bf16 v[40:43], v[196:199], a[180:183], v[40:43]\n" \
"ds_read_b128 a[248:251], v247 offset:23552\n" \
"v_cvt_pk_bf16_f32 v121, v122, v123\n" \
"v_mfma_f32_16x16x32_bf16 v[40:43], v[200:203], a[184:187], v[40:43]\n" \
"ds_read_b128 a[252:255], v247 offset:31744\n" \
"global_load_lds_dwordx4 v253, s[50:51] offset:1024\n" \
"v_mfma_f32_16x16x32_bf16 v[40:43], v[204:207], a[188:191], v[40:43]\n" \
"s_add_u32 s50, s50, 0x120000\n" \
"s_addc_u32 s51, s51, 0\n" \
"v_exp_f32 v125, v125\n" \
"v_add_f32 v243, v243, v124\n" \
"v_mfma_f32_16x16x32_bf16 v[44:47], v[216:219], a[184:187], v[236:239]\n" \
"v_exp_f32 v126, v126\n" \
"v_mfma_f32_16x16x32_bf16 v[44:47], v[220:223], a[188:191], v[44:47]\n" \
"global_load_dword v85, v84, s[22:23]\n" \
"v_mfma_f32_16x16x32_bf16 v[44:47], v[208:211], a[176:179], v[44:47]\n" \
"s_waitcnt vmcnt(10) lgkmcnt(0)\n" \
"v_add_u32 v247, 0x10000, v248\n" \
"v_add_f32 v243, v243, v125\n" \
"v_mfma_f32_16x16x32_bf16 v[44:47], v[212:215], a[180:183], v[44:47]\n" \
"s_barrier\n" \
"v_cvt_pk_bf16_f32 v122, v124, v125\n" \
"v_mfma_f32_16x16x32_bf16 a[32:35], a[192:195], v[72:75], a[32:35]\n" \
"v_exp_f32 v127, v127\n" \
"v_add_f32 v243, v243, v126\n" \
"v_mfma_f32_16x16x32_bf16 a[32:35], a[196:199], v[104:107], a[32:35]\n" \
"v_add_f32 v243, v243, v127\n" \
"v_cvt_pk_bf16_f32 v123, v126, v127\n" \
"v_mfma_f32_16x16x32_bf16 v[128:131], a[192:195], v[80:83], v[128:131]\n" \
"ds_read_b128 v[192:195], v247 offset:16384\n" \
"v_lshrrev_b32 v71, 7, v248\n" \
"v_mfma_f32_16x16x32_bf16 v[128:131], a[196:199], v[112:115], v[128:131]\n" \
"buffer_store_dwordx4 v[228:231], v79, s[56:59], s77 offen offset:2048 sc1\n" \
"v_cmp_gt_i32 vcc, 0, v251\n" \
"v_cmp_gt_i32 s[64:65], 1, v251\n" \
"v_cmp_gt_i32 s[66:67], 2, v251\n" \
"v_cndmask_b32 v8, v8, v254, vcc\n" \
"v_cndmask_b32 v16, v16, v254, vcc\n" \
"v_cmp_gt_i32 vcc, 3, v251\n" \
"v_cndmask_b32 v9, v9, v254, s[64:65]\n" \
"v_cndmask_b32 v17, v17, v254, s[64:65]\n" \
"v_cmp_gt_i32 s[64:65], 16, v251\n" \
"v_cndmask_b32 v10, v10, v254, s[66:67]\n" \
"v_and_b32 v76, 0x70, v248\n" \
"s_lshl_b32 s70, s88, 18\n" \
"v_mfma_f32_16x16x32_bf16 v[160:163], a[192:195], v[88:91], v[160:163]\n" \
"s_lshl_b32 s71, s61, 16\n" \
"s_add_u32 s71, s71, s70\n" \
"v_lshlrev_b32 v77, 4, v71\n" \
"v_mfma_f32_16x16x32_bf16 v[160:163], a[196:199], v[120:123], v[160:163]\n" \
"s_sub_u32 s77, s77, 4096\n" \
"ds_read_b128 a[192:195], v247 offset:0\n" \
"ds_read_b128 v[196:199], v247 offset:24576\n" \
"v_cndmask_b32 v18, v18, v254, s[66:67]\n" \
"v_cmp_gt_i32 s[66:67], 17, v251\n" \
"v_cndmask_b32 v11, v11, v254, vcc\n" \
"v_cndmask_b32 v19, v19, v254, vcc\n" \
"v_cmp_gt_i32 vcc, 18, v251\n" \
"v_cndmask_b32 v12, v12, v254, s[64:65]\n" \
"v_cndmask_b32 v20, v20, v254, s[64:65]\n" \
"v_cmp_gt_i32 s[64:65], 19, v251\n" \
"v_cndmask_b32 v13, v13, v254, s[66:67]\n" \
"v_cndmask_b32 v21, v21, v254, s[66:67]\n" \
"v_mfma_f32_16x16x32_bf16 v[164:167], a[200:203], v[88:91], v[164:167]\n" \
"v_xor_b32 v77, v77, v76\n" \
"v_cmp_gt_i32 s[66:67], 32, v251\n" \
"v_mfma_f32_16x16x32_bf16 v[164:167], a[204:207], v[120:123], v[164:167]\n" \
"s_add_u32 s42, s20, s71\n" \
"s_addc_u32 s43, s21, 0\n" \
"ds_read_b128 a[196:199], v247 offset:8192\n" \
"v_cndmask_b32 v14, v14, v254, vcc\n" \
"v_cndmask_b32 v22, v22, v254, vcc\n" \
"v_cmp_gt_i32 vcc, 33, v251\n" \
"v_cndmask_b32 v15, v15, v254, s[64:65]\n" \
"v_cndmask_b32 v23, v23, v254, s[64:65]\n" \
"v_cmp_gt_i32 s[64:65], 34, v251\n" \
"v_cndmask_b32 v40, v40, v254, s[66:67]\n" \
"v_cndmask_b32 v48, v48, v254, s[66:67]\n" \
"v_cmp_gt_i32 s[66:67], 35, v251\n" \
"v_cndmask_b32 v41, v41, v254, vcc\n" \
"v_mfma_f32_16x16x32_bf16 v[132:135], a[200:203], v[80:83], v[132:135]\n" \
"ds_read_b128 v[200:203], v247 offset:17408\n" \
"v_lshl_add_u32 v252, v71, 12, v77\n" \
"v_mfma_f32_16x16x32_bf16 v[132:135], a[204:207], v[112:115], v[132:135]\n" \
"s_add_u32 s44, s42, 31744\n" \
"s_addc_u32 s45, s43, 0\n" \
"s_max_u32 s70, s70, 32768\n" \
"v_cndmask_b32 v49, v49, v254, vcc\n" \
"v_cmp_gt_i32 vcc, 48, v251\n" \
"v_cndmask_b32 v42, v42, v254, s[64:65]\n" \
"v_cndmask_b32 v50, v50, v254, s[64:65]\n" \
"v_cmp_gt_i32 s[64:65], 49, v251\n" \
"v_cndmask_b32 v43, v43, v254, s[66:67]\n" \
"v_cndmask_b32 v51, v51, v254, s[66:67]\n" \
"v_cmp_gt_i32 s[66:67], 50, v251\n" \
"v_cndmask_b32 v44, v44, v254, vcc\n" \
"v_cndmask_b32 v52, v52, v254, vcc\n" \
"v_mfma_f32_16x16x32_bf16 a[36:39], a[200:203], v[72:75], a[36:39]\n" \
"s_sub_u32 s70, s70, 32768\n" \
"s_and_b32 s71, s61, 1\n" \
"s_lshl_b32 s71, s71, 2\n" \
"v_mfma_f32_16x16x32_bf16 a[36:39], a[204:207], v[104:107], a[36:39]\n" \
"ds_read_b128 a[200:203], v247 offset:1024\n" \
"ds_read_b128 v[204:207], v247 offset:25600\n" \
"v_cmp_gt_i32 vcc, 51, v251\n" \
"v_cndmask_b32 v45, v45, v254, s[64:65]\n" \
"v_cndmask_b32 v53, v53, v254, s[64:65]\n" \
"v_cndmask_b32 v46, v46, v254, s[66:67]\n" \
"v_cndmask_b32 v54, v54, v254, s[66:67]\n" \
"v_cndmask_b32 v47, v47, v254, vcc\n" \
"v_cndmask_b32 v55, v55, v254, vcc\n" \
"v_exp_f32 v8, v8\n" \
"v_exp_f32 v9, v9\n" \
"v_add_f32 v243, v243, v8\n" \
"v_mfma_f32_16x16x32_bf16 a[40:43], a[208:211], v[72:75], a[40:43]\n" \
"v_and_or_b32 v71, v71, 3, s71\n" \
"s_lshr_b32 s71, s61, 1\n" \
"v_mfma_f32_16x16x32_bf16 a[40:43], a[212:215], v[104:107], a[40:43]\n" \
"s_lshl_b32 s71, s71, 11\n" \
"ds_read_b128 a[204:207], v247 offset:9216\n" \
"v_lshlrev_b32 v77, 4, v71\n" \
"v_exp_f32 v10, v10\n" \
"v_add_f32 v243, v243, v9\n" \
"v_cvt_pk_bf16_f32 v8, v8, v9\n" \
"v_exp_f32 v11, v11\n" \
"v_add_f32 v243, v243, v10\n" \
"v_exp_f32 v12, v12\n" \
"v_add_f32 v243, v243, v11\n" \
"v_cvt_pk_bf16_f32 v9, v10, v11\n" \
"v_exp_f32 v13, v13\n" \
"v_add_f32 v243, v243, v12\n" \
"v_mfma_f32_16x16x32_bf16 v[136:139], a[208:211], v[80:83], v[136:139]\n" \
"s_add_u32 s70, s70, s71\n" \
"ds_read_b128 v[208:211], v247 offset:18432\n" \
"v_xor_b32 v77, v77, v76\n" \
"v_mfma_f32_16x16x32_bf16 v[136:139], a[212:215], v[112:115], v[136:139]\n" \
"s_add_u32 s46, s20, s70\n" \
"s_addc_u32 s47, s21, 0\n" \
"v_lshl_add_u32 v246, v71, 12, v77\n" \
"v_exp_f32 v14, v14\n" \
"v_add_f32 v243, v243, v13\n" \
"v_cvt_pk_bf16_f32 v10, v12, v13\n" \
"v_exp_f32 v15, v15\n" \
"v_add_f32 v243, v243, v14\n" \
"v_exp_f32 v40, v40\n" \
"v_add_f32 v243, v243, v15\n" \
"v_cvt_pk_bf16_f32 v11, v14, v15\n" \
"v_exp_f32 v41, v41\n" \
"v_add_f32 v243, v243, v40\n" \
"v_mfma_f32_16x16x32_bf16 v[168:171], a[208:211], v[88:91], v[168:171]\n" \
"s_lshl_b32 s85, s61, 10\n" \
"s_mov_b64 s[12:13], s[20:21]\n" \
"s_and_b32 s13, s13, 0xffff\n" \
"v_mfma_f32_16x16x32_bf16 v[168:171], a[212:215], v[120:123], v[168:171]\n" \
"s_mov_b32 s14, 4194304\n" \
"ds_read_b128 a[208:211], v247 offset:2048\n" \
"s_mov_b32 s15, 0x20000\n" \
"ds_read_b128 v[212:215], v247 offset:26624\n" \
"v_exp_f32 v42, v42\n" \
"v_add_f32 v243, v243, v41\n" \
"v_cvt_pk_bf16_f32 v40, v40, v41\n" \
"v_mfma_f32_16x16x32_bf16 v[172:175], a[216:219], v[88:91], v[172:175]\n" \
"s_mov_b32 s46, s70\n" \
"v_lshrrev_b32 v71, 9, v248\n" \
"v_lshl_add_u32 v246, v71, 30, v246\n" \
"v_mfma_f32_16x16x32_bf16 v[172:175], a[220:223], v[120:123], v[172:175]\n" \
"s_add_u32 m0, s95, 118784\n" \
"ds_read_b128 a[212:215], v247 offset:10240\n" \
"v_exp_f32 v43, v43\n" \
"v_add_f32 v243, v243, v42\n" \
"v_exp_f32 v44, v44\n" \
"v_add_f32 v243, v243, v43\n" \
"v_cvt_pk_bf16_f32 v41, v42, v43\n" \
"v_exp_f32 v45, v45\n" \
"v_add_f32 v243, v243, v44\n" \
"v_exp_f32 v46, v46\n" \
"v_add_f32 v243, v243, v45\n" \
"v_cvt_pk_bf16_f32 v42, v44, v45\n" \
"v_mfma_f32_16x16x32_bf16 v[140:143], a[216:219], v[80:83], v[140:143]\n" \
"ds_read_b128 v[216:219], v247 offset:19456\n" \
"global_load_lds_dwordx4 v252, s[42:43] offset:0\n" \
"v_mfma_f32_16x16x32_bf16 v[140:143], a[220:223], v[112:115], v[140:143]\n" \
"v_exp_f32 v47, v47\n" \
"v_add_f32 v243, v243, v46\n" \
"v_mfma_f32_16x16x32_bf16 a[44:47], a[216:219], v[72:75], a[44:47]\n" \
"v_add_f32 v243, v243, v47\n" \
"v_cvt_pk_bf16_f32 v43, v46, v47\n" \
"v_mfma_f32_16x16x32_bf16 a[44:47], a[220:223], v[104:107], a[44:47]\n" \
"ds_read_b128 a[216:219], v247 offset:3072\n" \
"global_load_lds_dwordx4 v252, s[42:43] offset:2048\n" \
"ds_read_b128 v[220:223], v247 offset:27648\n" \
"v_exp_f32 v24, v24\n" \
"v_exp_f32 v25, v25\n" \
"v_add_f32 v243, v243, v24\n" \
"v_exp_f32 v26, v26\n" \
"v_mfma_f32_16x16x32_bf16 a[48:51], a[224:227], v[72:75], a[48:51]\n" \
"s_add_u32 s42, s42, 0x180\n" \
"s_addc_u32 s43, s43, 0\n" \
"v_add_f32 v243, v243, v25\n" \
"v_mfma_f32_16x16x32_bf16 a[48:51], a[228:231], v[104:107], a[48:51]\n" \
"ds_read_b128 a[220:223], v247 offset:11264\n" \
"v_cvt_pk_bf16_f32 v24, v24, v25\n" \
"v_exp_f32 v27, v27\n" \
"v_add_f32 v243, v243, v26\n" \
"v_exp_f32 v28, v28\n" \
"v_add_f32 v243, v243, v27\n" \
"v_cvt_pk_bf16_f32 v25, v26, v27\n" \
"v_exp_f32 v29, v29\n" \
"v_add_f32 v243, v243, v28\n" \
"v_exp_f32 v30, v30\n" \
"v_add_f32 v243, v243, v29\n" \
"v_mfma_f32_16x16x32_bf16 v[144:147], a[224:227], v[80:83], v[144:147]\n" \
"ds_read_b128 v[224:227], v247 offset:20480\n" \
"global_load_lds_dwordx4 v252, s[44:45] offset:1024\n" \
"v_mfma_f32_16x16x32_bf16 v[144:147], a[228:231], v[112:115], v[144:147]\n" \
"v_cvt_pk_bf16_f32 v26, v28, v29\n" \
"v_exp_f32 v31, v31\n" \
"v_add_f32 v243, v243, v30\n" \
"v_exp_f32 v56, v56\n" \
"v_add_f32 v243, v243, v31\n" \
"v_cvt_pk_bf16_f32 v27, v30, v31\n" \
"v_exp_f32 v57, v57\n" \
"v_add_f32 v243, v243, v56\n" \
"v_exp_f32 v58, v58\n" \
"v_mfma_f32_16x16x32_bf16 v[176:179], a[224:227], v[88:91], v[176:179]\n" \
"v_add_f32 v243, v243, v57\n" \
"v_cvt_pk_bf16_f32 v56, v56, v57\n" \
"v_mfma_f32_16x16x32_bf16 v[176:179], a[228:231], v[120:123], v[176:179]\n" \
"ds_read_b128 a[224:227], v247 offset:4096\n" \
"global_load_lds_dwordx4 v252, s[44:45] offset:3072\n" \
"ds_read_b128 v[228:231], v247 offset:28672\n" \
"v_exp_f32 v59, v59\n" \
"v_add_f32 v243, v243, v58\n" \
"v_exp_f32 v60, v60\n" \
"v_add_f32 v243, v243, v59\n" \
"v_mfma_f32_16x16x32_bf16 v[180:183], a[232:235], v[88:91], v[180:183]\n" \
"s_add_u32 s44, s44, 0x180\n" \
"s_addc_u32 s45, s45, 0\n" \
"v_cvt_pk_bf16_f32 v57, v58, v59\n" \
"v_mfma_f32_16x16x32_bf16 v[180:183], a[236:239], v[120:123], v[180:183]\n" \
"s_add_u32 m0, s85, 135168\n" \
"ds_read_b128 a[228:231], v247 offset:12288\n" \
"v_exp_f32 v61, v61\n" \
"v_add_f32 v243, v243, v60\n" \
"v_exp_f32 v62, v62\n" \
"v_add_f32 v243, v243, v61\n" \
"v_cvt_pk_bf16_f32 v58, v60, v61\n" \
"v_exp_f32 v63, v63\n" \
"v_add_f32 v243, v243, v62\n" \
"v_mfma_f32_16x16x32_bf16 v[148:151], a[232:235], v[80:83], v[148:151]\n" \
"ds_read_b128 v[232:235], v247 offset:21504\n" \
"v_add_f32 v243, v243, v63\n" \
"v_mfma_f32_16x16x32_bf16 v[148:151], a[236:239], v[112:115], v[148:151]\n" \
"buffer_load_dwordx4 v246, s[12:15], s46 offen lds\n" \
"v_cvt_pk_bf16_f32 v59, v62, v63\n" \
"v_exp_f32 v16, v16\n" \
"v_mfma_f32_16x16x32_bf16 a[52:55], a[232:235], v[72:75], a[52:55]\n" \
"s_add_u32 s46, s46, 0x180\n" \
"v_exp_f32 v17, v17\n" \
"v_mfma_f32_16x16x32_bf16 a[52:55], a[236:239], v[104:107], a[52:55]\n" \
"ds_read_b128 a[232:235], v247 offset:5120\n" \
"ds_read_b128 v[236:239], v247 offset:29696\n" \
"v_add_f32 v242, v242, v16\n" \
"v_mfma_f32_16x16x32_bf16 a[56:59], a[240:243], v[72:75], a[56:59]\n" \
"v_exp_f32 v18, v18\n" \
"v_mfma_f32_16x16x32_bf16 a[56:59], a[244:247], v[104:107], a[56:59]\n" \
"ds_read_b128 a[236:239], v247 offset:13312\n" \
"v_add_f32 v242, v242, v17\n" \
"v_mfma_f32_16x16x32_bf16 v[152:155], a[240:243], v[80:83], v[152:155]\n" \
"ds_read_b128 v[0:3], v247 offset:22528\n" \
"v_cvt_pk_bf16_f32 v16, v16, v17\n" \
"v_mfma_f32_16x16x32_bf16 v[152:155], a[244:247], v[112:115], v[152:155]\n" \
"v_exp_f32 v19, v19\n" \
"v_add_f32 v242, v242, v18\n" \
"v_mfma_f32_16x16x32_bf16 v[184:187], a[240:243], v[88:91], v[184:187]\n" \
"v_exp_f32 v20, v20\n" \
"v_mfma_f32_16x16x32_bf16 v[184:187], a[244:247], v[120:123], v[184:187]\n" \
"ds_read_b128 a[240:243], v247 offset:6144\n" \
"ds_read_b128 v[4:7], v247 offset:30720\n" \
"v_mfma_f32_16x16x32_bf16 a[60:63], a[248:251], v[72:75], a[60:63]\n" \
"v_add_f32 v242, v242, v19\n" \
"v_cvt_pk_bf16_f32 v17, v18, v19\n" \
"v_mfma_f32_16x16x32_bf16 a[60:63], a[252:255], v[104:107], a[60:63]\n" \
"ds_read_b128 a[244:247], v247 offset:14336\n" \
"v_exp_f32 v21, v21\n" \
"v_mfma_f32_16x16x32_bf16 v[156:159], a[248:251], v[80:83], v[156:159]\n" \
"ds_read_b128 v[32:35], v247 offset:23552\n" \
"v_add_f32 v242, v242, v20\n" \
"v_mfma_f32_16x16x32_bf16 v[156:159], a[252:255], v[112:115], v[156:159]\n" \
"v_exp_f32 v22, v22\n" \
"v_mfma_f32_16x16x32_bf16 v[188:191], a[248:251], v[88:91], v[188:191]\n" \
"v_add_f32 v242, v242, v21\n" \
"v_cvt_pk_bf16_f32 v18, v20, v21\n" \
"v_mfma_f32_16x16x32_bf16 v[188:191], a[252:255], v[120:123], v[188:191]\n" \
"s_waitcnt lgkmcnt(2)\n" \
"ds_read_b128 a[248:251], v247 offset:7168\n" \
"ds_read_b128 v[36:39], v247 offset:31744\n" \
"v_mfma_f32_16x16x32_bf16 v[160:163], v[192:195], v[8:11], v[160:163]\n" \
"v_exp_f32 v23, v23\n" \
"v_mfma_f32_16x16x32_bf16 v[160:163], v[196:199], v[40:43], v[160:163]\n" \
"ds_read_b128 a[252:255], v247 offset:15360\n" \
"v_add_f32 v242, v242, v22\n" \
"v_exp_f32 v48, v48\n" \
"v_mfma_f32_16x16x32_bf16 v[164:167], v[200:203], v[8:11], v[164:167]\n" \
"v_add_f32 v242, v242, v23\n" \
"v_cvt_pk_bf16_f32 v19, v22, v23\n" \
"v_mfma_f32_16x16x32_bf16 v[164:167], v[204:207], v[40:43], v[164:167]\n" \
"v_mov_b32 v112, v241\n" \
"v_exp_f32 v49, v49\n" \
"v_add_f32 v242, v242, v48\n" \
"v_mfma_f32_16x16x32_bf16 v[168:171], v[208:211], v[8:11], v[168:171]\n" \
"v_exp_f32 v50, v50\n" \
"v_mfma_f32_16x16x32_bf16 v[168:171], v[212:215], v[40:43], v[168:171]\n" \
"v_add_f32 v242, v242, v49\n" \
"v_cvt_pk_bf16_f32 v48, v48, v49\n" \
"v_permlane16_swap_b32 v112, v241\n" \
"v_add_f32 v241, v241, v112\n" \
"v_mov_b32 v112, v241\n" \
"v_mfma_f32_16x16x32_bf16 v[172:175], v[216:219], v[8:11], v[172:175]\n" \
"s_waitcnt vmcnt(0) lgkmcnt(0)\n" \
"v_exp_f32 v51, v51\n" \
"v_mfma_f32_16x16x32_bf16 v[172:175], v[220:223], v[40:43], v[172:175]\n" \
"s_barrier\n" \
"v_add_f32 v242, v242, v50\n" \
"v_permlane32_swap_b32 v112, v241\n" \
"v_add_f32 v241, v241, v112\n" \
"v_rcp_f32 v112, v241\n" \
"v_mul_f32 v241, 0x4d800000, v241\n" \
"v_add_u32 v113, 0x1c90080, v249\n" \
"v_cndmask_b32 v113, v249, v113, s[16:17]\n" \
"v_mfma_f32_16x16x32_bf16 v[176:179], v[224:227], v[8:11], v[176:179]\n" \
"s_add_u32 m0, s84, 90112\n" \
"v_exp_f32 v52, v52\n" \
"v_mfma_f32_16x16x32_bf16 v[176:179], v[228:231], v[40:43], v[176:179]\n" \
"global_load_lds_dwordx4 v253, s[48:49] offset:-4096\n" \
"v_add_f32 v242, v242, v51\n" \
"v_mfma_f32_16x16x32_bf16 v[180:183], v[232:235], v[8:11], v[180:183]\n" \
"global_load_lds_dwordx4 v253, s[48:49] offset:-2048\n" \
"v_cvt_pk_bf16_f32 v49, v50, v51\n" \
"v_mfma_f32_16x16x32_bf16 v[180:183], v[236:239], v[40:43], v[180:183]\n" \
"s_add_u32 s77, s77, 532480\n" \
"v_accvgpr_read_b32 v96, a32\n" \
"v_accvgpr_read_b32 v97, a33\n" \
"v_accvgpr_read_b32 v98, a34\n" \
"v_accvgpr_read_b32 v99, a35\n" \
"v_accvgpr_read_b32 v100, a36\n" \
"v_accvgpr_read_b32 v101, a37\n" \
"v_accvgpr_read_b32 v102, a38\n" \
"v_accvgpr_read_b32 v103, a39\n" \
"v_exp_f32 v53, v53\n" \
"v_pk_mul_f32 v[96:97], v[96:97], v[112:113] op_sel_hi:[1,0]\n" \
"v_pk_mul_f32 v[98:99], v[98:99], v[112:113] op_sel_hi:[1,0]\n" \
"v_pk_mul_f32 v[100:101], v[100:101], v[112:113] op_sel_hi:[1,0]\n" \
"v_pk_mul_f32 v[102:103], v[102:103], v[112:113] op_sel_hi:[1,0]\n" \
"v_cvt_pk_bf16_f32 v104, v96, v97\n" \
"v_cvt_pk_bf16_f32 v105, v98, v99\n" \
"v_cvt_pk_bf16_f32 v106, v100, v101\n" \
"v_cvt_pk_bf16_f32 v107, v102, v103\n" \
"v_pk_mul_f32 v[96:97], v[96:97], v[98:99]\n" \
"v_pk_fma_f32 v[96:97], v[100:101], v[102:103], v[96:97]\n" \
"v_fma_f32 v241, v96, v97, v241\n" \
"v_mfma_f32_16x16x32_bf16 v[184:187], v[0:3], v[8:11], v[184:187]\n" \
"global_load_lds_dwordx4 v253, s[48:49] offset:0\n" \
"v_add_f32 v242, v242, v52\n" \
"v_mfma_f32_16x16x32_bf16 v[184:187], v[4:7], v[40:43], v[184:187]\n" \
"s_add_u32 s48, s48, 0x300000\n" \
"s_addc_u32 s49, s49, 0\n" \
"v_exp_f32 v54, v54\n" \
"v_add_f32 v242, v242, v53\n" \
"v_mfma_f32_16x16x32_bf16 v[188:191], v[32:35], v[8:11], v[188:191]\n" \
"global_load_lds_dwordx4 v253, s[50:51] offset:-3072\n" \
"v_cvt_pk_bf16_f32 v50, v52, v53\n" \
"v_mfma_f32_16x16x32_bf16 v[188:191], v[36:39], v[40:43], v[188:191]\n" \
"buffer_store_dwordx4 v[104:107], v113, s[56:59], s77 offen offset:0 sc1\n" \
"v_accvgpr_read_b32 v96, a40\n" \
"v_accvgpr_read_b32 v97, a41\n" \
"v_accvgpr_read_b32 v98, a42\n" \
"v_accvgpr_read_b32 v99, a43\n" \
"v_accvgpr_read_b32 v100, a44\n" \
"v_accvgpr_read_b32 v101, a45\n" \
"v_accvgpr_read_b32 v102, a46\n" \
"v_accvgpr_read_b32 v103, a47\n" \
"v_exp_f32 v55, v55\n" \
"v_add_f32 v242, v242, v54\n" \
"v_pk_mul_f32 v[96:97], v[96:97], v[112:113] op_sel_hi:[1,0]\n" \
"v_pk_mul_f32 v[98:99], v[98:99], v[112:113] op_sel_hi:[1,0]\n" \
"v_pk_mul_f32 v[100:101], v[100:101], v[112:113] op_sel_hi:[1,0]\n" \
"v_pk_mul_f32 v[102:103], v[102:103], v[112:113] op_sel_hi:[1,0]\n" \
"v_cvt_pk_bf16_f32 v108, v96, v97\n" \
"v_cvt_pk_bf16_f32 v109, v98, v99\n" \
"v_cvt_pk_bf16_f32 v110, v100, v101\n" \
"v_cvt_pk_bf16_f32 v111, v102, v103\n" \
"v_pk_mul_f32 v[96:97], v[96:97], v[98:99]\n" \
"v_pk_fma_f32 v[96:97], v[100:101], v[102:103], v[96:97]\n" \
"v_fma_f32 v241, v96, v97, v241\n" \
"v_mfma_f32_16x16x32_bf16 v[160:163], a[192:195], v[24:27], v[160:163]\n" \
"global_load_lds_dwordx4 v253, s[50:51] offset:-1024\n" \
"v_add_f32 v242, v242, v55\n" \
"v_mfma_f32_16x16x32_bf16 v[160:163], a[196:199], v[56:59], v[160:163]\n" \
"buffer_store_dwordx4 v[108:111], v113, s[56:59], s77 offen offset:2048 sc1\n" \
"v_cvt_pk_bf16_f32 v51, v54, v55\n" \
"s_movk_i32 s89, 0xc0\n" \
"s_movk_i32 s90, 288\n" \
"v_lshrrev_b32 v211, 4, v248\n" \
"v_mfma_f32_16x16x32_bf16 v[164:167], a[200:203], v[24:27], v[164:167]\n" \
"global_load_lds_dwordx4 v253, s[50:51] offset:1024\n" \
"v_lshrrev_b32 v214, 8, v248\n" \
"v_mfma_f32_16x16x32_bf16 v[164:167], a[204:207], v[56:59], v[164:167]\n" \
"s_add_u32 s50, s50, 0x300000\n" \
"s_addc_u32 s51, s51, 0\n" \
"v_and_b32 v215, 7, v211\n" \
"s_add_u32 m0, s95, 65536\n" \
"v_mfma_f32_16x16x32_bf16 v[168:171], a[208:211], v[24:27], v[168:171]\n" \
"global_load_lds_dwordx4 v252, s[42:43] offset:0\n" \
"v_xor_b32 v214, v214, v215\n" \
"v_mfma_f32_16x16x32_bf16 v[168:171], a[212:215], v[56:59], v[168:171]\n" \
"s_lshr_b32 s71, s61, 1\n" \
"s_lshl_b32 s70, s71, 11\n" \
"s_add_u32 s70, s70, 118784\n" \
"v_and_b32 v216, 15, v211\n" \
"v_mfma_f32_16x16x32_bf16 v[172:175], a[216:219], v[24:27], v[172:175]\n" \
"global_load_lds_dwordx4 v252, s[42:43] offset:2048\n" \
"v_lshlrev_b32 v213, 7, v216\n" \
"v_mfma_f32_16x16x32_bf16 v[172:175], a[220:223], v[56:59], v[172:175]\n" \
"s_add_u32 s42, s42, 0x400\n" \
"s_addc_u32 s43, s43, 0\n" \
"v_lshl_add_u32 v213, v214, 4, v213\n" \
"v_add_u32 v205, s70, v213\n" \
"v_xor_b32 v206, 64, v205\n" \
"v_lshrrev_b32 v216, 2, v216\n" \
"v_lshl_add_u32 v216, v216, 9, v213\n" \
"v_add_u32 v207, 135168, v216\n" \
"v_mfma_f32_16x16x32_bf16 v[176:179], a[224:227], v[24:27], v[176:179]\n" \
"global_load_lds_dwordx4 v252, s[44:45] offset:1024\n" \
"v_xor_b32 v208, 64, v207\n" \
"v_mfma_f32_16x16x32_bf16 v[176:179], a[228:231], v[56:59], v[176:179]\n" \
"s_and_b32 s70, s61, 1\n" \
"s_lshl_b32 s70, s70, 9\n" \
"s_add_u32 s70, s70, 139264\n" \
"v_mfma_f32_16x16x32_bf16 v[180:183], a[232:235], v[24:27], v[180:183]\n" \
"s_lshl_b32 s71, s71, 5\n" \
"global_load_lds_dwordx4 v252, s[44:45] offset:3072\n" \
"v_mfma_f32_16x16x32_bf16 v[180:183], a[236:239], v[56:59], v[180:183]\n" \
"s_add_u32 s44, s44, 0x400\n" \
"s_addc_u32 s45, s45, 0\n" \
"v_and_b32 v213, 3, v211\n" \
"v_lshlrev_b32 v213, 3, v213\n" \
"s_add_u32 m0, s85, 81920\n" \
"v_mfma_f32_16x16x32_bf16 v[184:187], a[240:243], v[24:27], v[184:187]\n" \
"buffer_load_dwordx4 v246, s[12:15], s46 offen lds\n" \
"v_mfma_f32_16x16x32_bf16 v[184:187], a[244:247], v[56:59], v[184:187]\n" \
"s_add_u32 s46, s46, 0x400\n" \
"v_and_or_b32 v213, v248, s89, v213\n" \
"v_bfe_u32 v214, v211, 4, 1\n" \
"v_mad_u32_u24 v213, v214, s90, v213\n" \
"v_lshrrev_b32 v214, 5, v211\n" \
"v_mul_u32_u24 v214, 6144, v214\n" \
"v_add3_u32 v213, v213, v214, s70\n" \
"v_mfma_f32_16x16x32_bf16 v[188:191], a[248:251], v[24:27], v[188:191]\n" \
"s_add_u32 m0, s95, 0\n" \
"v_xor_b32 v209, s71, v213\n" \
"v_xor_b32 v210, 32, v209\n" \
"v_mfma_f32_16x16x32_bf16 v[188:191], a[252:255], v[56:59], v[188:191]\n" \
"s_add_u32 s77, s77, 0x1000\n" \
"ds_read_b128 a[64:67], v207 offset:0\n" \
"ds_read_b64_tr_b16 a[96:97], v209 offset:2048\n" \
"v_accvgpr_read_b32 v96, a48\n" \
"v_accvgpr_read_b32 v97, a49\n" \
"v_accvgpr_read_b32 v98, a50\n" \
"v_accvgpr_read_b32 v99, a51\n" \
"v_accvgpr_read_b32 v100, a52\n" \
"v_accvgpr_read_b32 v101, a53\n" \
"v_accvgpr_read_b32 v102, a54\n" \
"v_accvgpr_read_b32 v103, a55\n" \
"v_pk_mul_f32 v[96:97], v[96:97], v[112:113] op_sel_hi:[1,0]\n" \
"v_pk_mul_f32 v[98:99], v[98:99], v[112:113] op_sel_hi:[1,0]\n" \
"v_pk_mul_f32 v[100:101], v[100:101], v[112:113] op_sel_hi:[1,0]\n" \
"v_pk_mul_f32 v[102:103], v[102:103], v[112:113] op_sel_hi:[1,0]\n" \
"v_cvt_pk_bf16_f32 v104, v96, v97\n" \
"v_cvt_pk_bf16_f32 v105, v98, v99\n" \
"v_cvt_pk_bf16_f32 v106, v100, v101\n" \
"v_cvt_pk_bf16_f32 v107, v102, v103\n" \
"v_pk_mul_f32 v[96:97], v[96:97], v[98:99]\n" \
"v_pk_fma_f32 v[96:97], v[100:101], v[102:103], v[96:97]\n" \
"v_mfma_f32_16x16x32_bf16 v[128:131], a[192:195], v[16:19], v[128:131]\n" \
"ds_read_b64_tr_b16 a[98:99], v209 offset:3072\n" \
"global_load_lds_dwordx4 v252, s[42:43] offset:0\n" \
"v_mfma_f32_16x16x32_bf16 v[128:131], a[196:199], v[48:51], v[128:131]\n" \
"ds_read_b128 a[68:71], v208 offset:0\n" \
"v_fma_f32 v241, v96, v97, v241\n" \
"v_mfma_f32_16x16x32_bf16 v[132:135], a[200:203], v[16:19], v[132:135]\n" \
"buffer_store_dwordx4 v[104:107], v113, s[56:59], s77 offen offset:0 sc1\n" \
"ds_read_b64_tr_b16 a[100:101], v209 offset:14336\n" \
"global_load_lds_dwordx4 v252, s[42:43] offset:2048\n" \
"ds_read_b64_tr_b16 a[102:103], v209 offset:15360\n" \
"v_accvgpr_read_b32 v96, a56\n" \
"v_mfma_f32_16x16x32_bf16 v[132:135], a[204:207], v[48:51], v[132:135]\n" \
"s_add_u32 s42, s42, 0xfffffb00\n" \
"s_addc_u32 s43, s43, -1\n" \
"ds_read_b128 a[72:75], v205 offset:0\n" \
"v_mfma_f32_16x16x32_bf16 v[136:139], a[208:211], v[16:19], v[136:139]\n" \
"ds_read_b64_tr_b16 a[104:105], v209 offset:0\n" \
"global_load_lds_dwordx4 v252, s[44:45] offset:1024\n" \
"v_mfma_f32_16x16x32_bf16 v[136:139], a[212:215], v[48:51], v[136:139]\n" \
"ds_read_b128 a[76:79], v206 offset:0\n" \
"ds_read_b64_tr_b16 a[106:107], v209 offset:1024\n" \
"v_mfma_f32_16x16x32_bf16 v[140:143], a[216:219], v[16:19], v[140:143]\n" \
"ds_read_b64_tr_b16 a[108:109], v209 offset:12288\n" \
"v_accvgpr_read_b32 v97, a57\n" \
"v_mfma_f32_16x16x32_bf16 v[140:143], a[220:223], v[48:51], v[140:143]\n" \
"ds_read_b64_tr_b16 a[110:111], v209 offset:13312\n" \
"global_load_lds_dwordx4 v252, s[44:45] offset:3072\n" \
"v_mfma_f32_16x16x32_bf16 v[144:147], a[224:227], v[16:19], v[144:147]\n" \
"ds_read_b128 a[80:83], v205 offset:4096\n" \
"ds_read_b64_tr_b16 a[112:113], v210 offset:0\n" \
"v_mfma_f32_16x16x32_bf16 v[144:147], a[228:231], v[48:51], v[144:147]\n" \
"s_add_u32 s44, s44, 0xfffffb00\n" \
"s_addc_u32 s45, s45, -1\n" \
"ds_read_b64_tr_b16 a[114:115], v210 offset:1024\n" \
"v_mfma_f32_16x16x32_bf16 v[148:151], a[232:235], v[16:19], v[148:151]\n" \
"s_add_u32 m0, s85, 16384\n" \
"ds_read_b64_tr_b16 a[116:117], v210 offset:12288\n" \
"v_accvgpr_read_b32 v98, a58\n" \
"v_mfma_f32_16x16x32_bf16 v[148:151], a[236:239], v[48:51], v[148:151]\n" \
"ds_read_b128 a[84:87], v206 offset:4096\n" \
"ds_read_b64_tr_b16 a[118:119], v210 offset:13312\n" \
"v_mfma_f32_16x16x32_bf16 v[152:155], a[240:243], v[16:19], v[152:155]\n" \
"ds_read_b128 a[88:91], v205 offset:8192\n" \
"v_accvgpr_read_b32 v99, a59\n" \
"v_mfma_f32_16x16x32_bf16 v[152:155], a[244:247], v[48:51], v[152:155]\n" \
"ds_read_b128 a[92:95], v206 offset:8192\n" \
"v_accvgpr_read_b32 v100, a60\n" \
"v_accvgpr_read_b32 v101, a61\n" \
"v_accvgpr_read_b32 v102, a62\n" \
"v_accvgpr_read_b32 v103, a63\n" \
"v_pk_mul_f32 v[96:97], v[96:97], v[112:113] op_sel_hi:[1,0]\n" \
"v_pk_mul_f32 v[98:99], v[98:99], v[112:113] op_sel_hi:[1,0]\n" \
"v_pk_mul_f32 v[100:101], v[100:101], v[112:113] op_sel_hi:[1,0]\n" \
"v_pk_mul_f32 v[102:103], v[102:103], v[112:113] op_sel_hi:[1,0]\n" \
"v_cvt_pk_bf16_f32 v108, v96, v97\n" \
"v_cvt_pk_bf16_f32 v109, v98, v99\n" \
"v_cvt_pk_bf16_f32 v110, v100, v101\n" \
"v_cvt_pk_bf16_f32 v111, v102, v103\n" \
"v_pk_mul_f32 v[96:97], v[96:97], v[98:99]\n" \
"v_pk_fma_f32 v[96:97], v[100:101], v[102:103], v[96:97]\n" \
"v_fma_f32 v241, v96, v97, v241\n" \
"v_mfma_f32_16x16x32_bf16 v[156:159], a[248:251], v[16:19], v[156:159]\n" \
"buffer_load_dwordx4 v246, s[12:15], s46 offen lds\n" \
"v_mfma_f32_16x16x32_bf16 v[156:159], a[252:255], v[48:51], v[156:159]\n" \
"s_waitcnt lgkmcnt(4)\n" \
"buffer_store_dwordx4 v[108:111], v113, s[56:59], s77 offen offset:2048 sc1\n" \
"s_sub_u32 s77, s77, 536576\n" \
"s_branch Ljoin_%=\n" \
"Lexact_%=:\n" \
"v_mov_b32 v190, 0xff800000\n" \
"v_add_u32 v191, 0x1c90080, v249\n" \
"v_cndmask_b32 v191, v249, v191, s[16:17]\n" \
"s_and_b32 s80, s61, 2\n" \
"s_lshl_b32 s80, s80, 4\n" \
"v_subrev_u32 v186, s80, v251\n" \
"v_accvgpr_read_b32 v160, a128\n" \
"v_accvgpr_read_b32 v161, a129\n" \
"v_accvgpr_read_b32 v162, a130\n" \
"v_accvgpr_read_b32 v163, a131\n" \
"v_accvgpr_read_b32 v164, a132\n" \
"v_accvgpr_read_b32 v165, a133\n" \
"v_accvgpr_read_b32 v166, a134\n" \
"v_accvgpr_read_b32 v167, a135\n" \
"v_accvgpr_read_b32 v168, a136\n" \
"v_accvgpr_read_b32 v169, a137\n" \
"v_accvgpr_read_b32 v170, a138\n" \
"v_accvgpr_read_b32 v171, a139\n" \
"v_accvgpr_read_b32 v172, a140\n" \
"v_accvgpr_read_b32 v173, a141\n" \
"v_accvgpr_read_b32 v174, a142\n" \
"v_accvgpr_read_b32 v175, a143\n" \
"s_lshl_b32 s75, s63, 2\n" \
"s_lshr_b32 s80, s61, 1\n" \
"s_add_u32 s75, s75, s80\n" \
"s_add_u32 s75, s75, 0\n" \
"s_add_u32 s76, s77, 0\n" \
"s_call_b64 s[68:69], Lex1_%=\n" \
"v_accvgpr_read_b32 v160, a144\n" \
"v_accvgpr_read_b32 v161, a145\n" \
"v_accvgpr_read_b32 v162, a146\n" \
"v_accvgpr_read_b32 v163, a147\n" \
"v_accvgpr_read_b32 v164, a148\n" \
"v_accvgpr_read_b32 v165, a149\n" \
"v_accvgpr_read_b32 v166, a150\n" \
"v_accvgpr_read_b32 v167, a151\n" \
"v_accvgpr_read_b32 v168, a152\n" \
"v_accvgpr_read_b32 v169, a153\n" \
"v_accvgpr_read_b32 v170, a154\n" \
"v_accvgpr_read_b32 v171, a155\n" \
"v_accvgpr_read_b32 v172, a156\n" \
"v_accvgpr_read_b32 v173, a157\n" \
"v_accvgpr_read_b32 v174, a158\n" \
"v_accvgpr_read_b32 v175, a159\n" \
"s_lshl_b32 s75, s63, 2\n" \
"s_lshr_b32 s80, s61, 1\n" \
"s_add_u32 s75, s75, s80\n" \
"s_add_u32 s75, s75, 2\n" \
"s_add_u32 s76, s77, 532480\n" \
"s_call_b64 s[68:69], Lex1_%=\n" \
"v_accvgpr_read_b32 v160, a160\n" \
"v_accvgpr_read_b32 v161, a161\n" \
"v_accvgpr_read_b32 v162, a162\n" \
"v_accvgpr_read_b32 v163, a163\n" \
"v_accvgpr_read_b32 v164, a164\n" \
"v_accvgpr_read_b32 v165, a165\n" \
"v_accvgpr_read_b32 v166, a166\n" \
"v_accvgpr_read_b32 v167, a167\n" \
"v_accvgpr_read_b32 v168, a168\n" \
"v_accvgpr_read_b32 v169, a169\n" \
"v_accvgpr_read_b32 v170, a170\n" \
"v_accvgpr_read_b32 v171, a171\n" \
"v_accvgpr_read_b32 v172, a172\n" \
"v_accvgpr_read_b32 v173, a173\n" \
"v_accvgpr_read_b32 v174, a174\n" \
"v_accvgpr_read_b32 v175, a175\n" \
"s_lshl_b32 s75, s63, 2\n" \
"s_sub_u32 s75, 60, s75\n" \
"s_lshr_b32 s80, s61, 1\n" \
"s_add_u32 s75, s75, s80\n" \
"s_add_u32 s75, s75, 0\n" \
"s_add_u32 s76, s78, 0\n" \
"s_call_b64 s[68:69], Lex1_%=\n" \
"v_accvgpr_read_b32 v160, a176\n" \
"v_accvgpr_read_b32 v161, a177\n" \
"v_accvgpr_read_b32 v162, a178\n" \
"v_accvgpr_read_b32 v163, a179\n" \
"v_accvgpr_read_b32 v164, a180\n" \
"v_accvgpr_read_b32 v165, a181\n" \
"v_accvgpr_read_b32 v166, a182\n" \
"v_accvgpr_read_b32 v167, a183\n" \
"v_accvgpr_read_b32 v168, a184\n" \
"v_accvgpr_read_b32 v169, a185\n" \
"v_accvgpr_read_b32 v170, a186\n" \
"v_accvgpr_read_b32 v171, a187\n" \
"v_accvgpr_read_b32 v172, a188\n" \
"v_accvgpr_read_b32 v173, a189\n" \
"v_accvgpr_read_b32 v174, a190\n" \
"v_accvgpr_read_b32 v175, a191\n" \
"s_lshl_b32 s75, s63, 2\n" \
"s_sub_u32 s75, 60, s75\n" \
"s_lshr_b32 s80, s61, 1\n" \
"s_add_u32 s75, s75, s80\n" \
"s_add_u32 s75, s75, 2\n" \
"s_add_u32 s76, s78, 532480\n" \
"s_call_b64 s[68:69], Lex1_%=\n" \
"s_branch Lexr_%=\n" \
"Lex1_%=:\n" \
"v_mov_b32 v128, 0\n" \
"v_mov_b32 v129, 0\n" \
"v_mov_b32 v130, 0\n" \
"v_mov_b32 v131, 0\n" \
"v_mov_b32 v132, 0\n" \
"v_mov_b32 v133, 0\n" \
"v_mov_b32 v134, 0\n" \
"v_mov_b32 v135, 0\n" \
"v_mov_b32 v136, 0\n" \
"v_mov_b32 v137, 0\n" \
"v_mov_b32 v138, 0\n" \
"v_mov_b32 v139, 0\n" \
"v_mov_b32 v140, 0\n" \
"v_mov_b32 v141, 0\n" \
"v_mov_b32 v142, 0\n" \
"v_mov_b32 v143, 0\n" \
"v_mov_b32 v144, 0\n" \
"v_mov_b32 v145, 0\n" \
"v_mov_b32 v146, 0\n" \
"v_mov_b32 v147, 0\n" \
"v_mov_b32 v148, 0\n" \
"v_mov_b32 v149, 0\n" \
"v_mov_b32 v150, 0\n" \
"v_mov_b32 v151, 0\n" \
"v_mov_b32 v152, 0\n" \
"v_mov_b32 v153, 0\n" \
"v_mov_b32 v154, 0\n" \
"v_mov_b32 v155, 0\n" \
"v_mov_b32 v156, 0\n" \
"v_mov_b32 v157, 0\n" \
"v_mov_b32 v158, 0\n" \
"v_mov_b32 v159, 0\n" \
"v_mov_b32 v184, 0xf149f2ca\n" \
"v_mov_b32 v185, 0\n" \
"s_mov_b32 s73, 0\n" \
"s_mul_i32 s74, s62, 557824\n" \
"Lex2_%=:\n" \
"s_add_u32 s80, s74, 4096\n" \
"buffer_load_dwordx4 a[0:3], v249, s[56:59], s74 offen offset:0 sc1\n" \
"buffer_load_dwordx4 a[4:7], v249, s[56:59], s74 offen offset:2048 sc1\n" \
"buffer_load_dwordx4 a[8:11], v249, s[56:59], s80 offen offset:0 sc1\n" \
"buffer_load_dwordx4 a[12:15], v249, s[56:59], s80 offen offset:2048 sc1\n" \
"buffer_load_dwordx4 a[16:19], v249, s[56:59], s74 offen offset:256 sc1\n" \
"buffer_load_dwordx4 a[20:23], v249, s[56:59], s74 offen offset:2304 sc1\n" \
"buffer_load_dwordx4 a[24:27], v249, s[56:59], s80 offen offset:256 sc1\n" \
"buffer_load_dwordx4 a[28:31], v249, s[56:59], s80 offen offset:2304 sc1\n" \
"s_add_u32 s80, s74, 17850368\n" \
"s_add_u32 s81, s80, 4096\n" \
"buffer_load_dwordx4 a[32:35], v248, s[56:59], s80 offen offset:0 sc1\n" \
"buffer_load_dwordx4 a[36:39], v248, s[56:59], s80 offen offset:1024 sc1\n" \
"buffer_load_dwordx4 a[40:43], v248, s[56:59], s80 offen offset:2048 sc1\n" \
"buffer_load_dwordx4 a[44:47], v248, s[56:59], s80 offen offset:3072 sc1\n" \
"buffer_load_dwordx4 a[48:51], v248, s[56:59], s81 offen offset:0 sc1\n" \
"buffer_load_dwordx4 a[52:55], v248, s[56:59], s81 offen offset:1024 sc1\n" \
"buffer_load_dwordx4 a[56:59], v248, s[56:59], s81 offen offset:2048 sc1\n" \
"buffer_load_dwordx4 a[60:63], v248, s[56:59], s81 offen offset:3072 sc1\n" \
"s_waitcnt vmcnt(0)\n" \
"v_mfma_f32_16x16x32_bf16 v[176:179], a[0:3], v[160:163], 0\n" \
"v_mfma_f32_16x16x32_bf16 v[176:179], a[4:7], v[164:167], v[176:179]\n" \
"v_mfma_f32_16x16x32_bf16 v[176:179], a[8:11], v[168:171], v[176:179]\n" \
"v_mfma_f32_16x16x32_bf16 v[176:179], a[12:15], v[172:175], v[176:179]\n" \
"v_mfma_f32_16x16x32_bf16 v[180:183], a[16:19], v[160:163], 0\n" \
"v_mfma_f32_16x16x32_bf16 v[180:183], a[20:23], v[164:167], v[180:183]\n" \
"v_mfma_f32_16x16x32_bf16 v[180:183], a[24:27], v[168:171], v[180:183]\n" \
"v_mfma_f32_16x16x32_bf16 v[180:183], a[28:31], v[172:175], v[180:183]\n" \
"s_nop 7\n" \
"s_nop 1\n" \
"s_cmp_eq_u32 s73, s75\n" \
"s_cbranch_scc0 Lex3_%=\n" \
"v_cmp_gt_i32 vcc, 0, v186\n" \
"s_nop 1\n" \
"v_cndmask_b32 v176, v176, v190, vcc\n" \
"v_cmp_gt_i32 vcc, 1, v186\n" \
"s_nop 1\n" \
"v_cndmask_b32 v177, v177, v190, vcc\n" \
"v_cmp_gt_i32 vcc, 2, v186\n" \
"s_nop 1\n" \
"v_cndmask_b32 v178, v178, v190, vcc\n" \
"v_cmp_gt_i32 vcc, 3, v186\n" \
"s_nop 1\n" \
"v_cndmask_b32 v179, v179, v190, vcc\n" \
"v_cmp_gt_i32 vcc, 16, v186\n" \
"s_nop 1\n" \
"v_cndmask_b32 v180, v180, v190, vcc\n" \
"v_cmp_gt_i32 vcc, 17, v186\n" \
"s_nop 1\n" \
"v_cndmask_b32 v181, v181, v190, vcc\n" \
"v_cmp_gt_i32 vcc, 18, v186\n" \
"s_nop 1\n" \
"v_cndmask_b32 v182, v182, v190, vcc\n" \
"v_cmp_gt_i32 vcc, 19, v186\n" \
"s_nop 1\n" \
"v_cndmask_b32 v183, v183, v190, vcc\n" \
"Lex3_%=:\n" \
"v_max3_f32 v187, v176, v177, v178\n" \
"v_max3_f32 v187, v187, v179, v180\n" \
"v_max3_f32 v187, v187, v181, v182\n" \
"v_max3_f32 v187, v187, v183, v184\n" \
"v_mov_b32 v188, v187\n" \
"s_nop 1\n" \
"v_permlane16_swap_b32 v188, v187\n" \
"v_max_f32 v187, v187, v188\n" \
"v_mov_b32 v188, v187\n" \
"s_nop 1\n" \
"v_permlane32_swap_b32 v188, v187\n" \
"v_max_f32 v187, v187, v188\n" \
"v_sub_f32 v189, v184, v187\n" \
"v_exp_f32 v189, v189\n" \
"v_mov_b32 v184, v187\n" \
"v_sub_f32 v176, v176, v187\n" \
"v_sub_f32 v177, v177, v187\n" \
"v_sub_f32 v178, v178, v187\n" \
"v_sub_f32 v179, v179, v187\n" \
"v_sub_f32 v180, v180, v187\n" \
"v_sub_f32 v181, v181, v187\n" \
"v_sub_f32 v182, v182, v187\n" \
"v_sub_f32 v183, v183, v187\n" \
"v_exp_f32 v176, v176\n" \
"v_exp_f32 v177, v177\n" \
"v_exp_f32 v178, v178\n" \
"v_exp_f32 v179, v179\n" \
"v_exp_f32 v180, v180\n" \
"v_exp_f32 v181, v181\n" \
"v_exp_f32 v182, v182\n" \
"v_exp_f32 v183, v183\n" \
"v_mul_f32 v185, v185, v189\n" \
"v_add_f32 v185, v185, v176\n" \
"v_add_f32 v185, v185, v177\n" \
"v_add_f32 v185, v185, v178\n" \
"v_add_f32 v185, v185, v179\n" \
"v_add_f32 v185, v185, v180\n" \
"v_add_f32 v185, v185, v181\n" \
"v_add_f32 v185, v185, v182\n" \
"v_add_f32 v185, v185, v183\n" \
"v_cvt_pk_bf16_f32 v112, v176, v177\n" \
"v_cvt_pk_bf16_f32 v113, v178, v179\n" \
"v_cvt_pk_bf16_f32 v114, v180, v181\n" \
"v_cvt_pk_bf16_f32 v115, v182, v183\n" \
"v_mul_f32 v128, v128, v189\n" \
"v_mul_f32 v129, v129, v189\n" \
"v_mul_f32 v130, v130, v189\n" \
"v_mul_f32 v131, v131, v189\n" \
"v_mul_f32 v132, v132, v189\n" \
"v_mul_f32 v133, v133, v189\n" \
"v_mul_f32 v134, v134, v189\n" \
"v_mul_f32 v135, v135, v189\n" \
"v_mul_f32 v136, v136, v189\n" \
"v_mul_f32 v137, v137, v189\n" \
"v_mul_f32 v138, v138, v189\n" \
"v_mul_f32 v139, v139, v189\n" \
"v_mul_f32 v140, v140, v189\n" \
"v_mul_f32 v141, v141, v189\n" \
"v_mul_f32 v142, v142, v189\n" \
"v_mul_f32 v143, v143, v189\n" \
"v_mul_f32 v144, v144, v189\n" \
"v_mul_f32 v145, v145, v189\n" \
"v_mul_f32 v146, v146, v189\n" \
"v_mul_f32 v147, v147, v189\n" \
"v_mul_f32 v148, v148, v189\n" \
"v_mul_f32 v149, v149, v189\n" \
"v_mul_f32 v150, v150, v189\n" \
"v_mul_f32 v151, v151, v189\n" \
"v_mul_f32 v152, v152, v189\n" \
"v_mul_f32 v153, v153, v189\n" \
"v_mul_f32 v154, v154, v189\n" \
"v_mul_f32 v155, v155, v189\n" \
"v_mul_f32 v156, v156, v189\n" \
"v_mul_f32 v157, v157, v189\n" \
"v_mul_f32 v158, v158, v189\n" \
"v_mul_f32 v159, v159, v189\n" \
"s_nop 1\n" \
"v_mfma_f32_16x16x32_bf16 v[128:131], a[32:35], v[112:115], v[128:131]\n" \
"v_mfma_f32_16x16x32_bf16 v[132:135], a[36:39], v[112:115], v[132:135]\n" \
"v_mfma_f32_16x16x32_bf16 v[136:139], a[40:43], v[112:115], v[136:139]\n" \
"v_mfma_f32_16x16x32_bf16 v[140:143], a[44:47], v[112:115], v[140:143]\n" \
"v_mfma_f32_16x16x32_bf16 v[144:147], a[48:51], v[112:115], v[144:147]\n" \
"v_mfma_f32_16x16x32_bf16 v[148:151], a[52:55], v[112:115], v[148:151]\n" \
"v_mfma_f32_16x16x32_bf16 v[152:155], a[56:59], v[112:115], v[152:155]\n" \
"v_mfma_f32_16x16x32_bf16 v[156:159], a[60:63], v[112:115], v[156:159]\n" \
"s_nop 7\n" \
"s_nop 1\n" \
"s_and_b32 s80, s73, 1\n" \
"s_mul_i32 s80, s80, 1024\n" \
"s_add_u32 s74, s74, s80\n" \
"s_add_u32 s74, s74, 8192\n" \
"s_add_u32 s73, s73, 1\n" \
"s_cmp_le_u32 s73, s75\n" \
"s_cbranch_scc1 Lex2_%=\n" \
"v_mov_b32 v187, v185\n" \
"s_nop 1\n" \
"v_permlane16_swap_b32 v187, v185\n" \
"v_add_f32 v185, v185, v187\n" \
"v_mov_b32 v187, v185\n" \
"s_nop 1\n" \
"v_permlane32_swap_b32 v187, v185\n" \
"v_add_f32 v185, v185, v187\n" \
"v_rcp_f32 v185, v185\n" \
"s_nop 0\n" \
"v_mul_f32 v128, v128, v185\n" \
"v_mul_f32 v129, v129, v185\n" \
"v_mul_f32 v130, v130, v185\n" \
"v_mul_f32 v131, v131, v185\n" \
"v_mul_f32 v132, v132, v185\n" \
"v_mul_f32 v133, v133, v185\n" \
"v_mul_f32 v134, v134, v185\n" \
"v_mul_f32 v135, v135, v185\n" \
"v_mul_f32 v136, v136, v185\n" \
"v_mul_f32 v137, v137, v185\n" \
"v_mul_f32 v138, v138, v185\n" \
"v_mul_f32 v139, v139, v185\n" \
"v_mul_f32 v140, v140, v185\n" \
"v_mul_f32 v141, v141, v185\n" \
"v_mul_f32 v142, v142, v185\n" \
"v_mul_f32 v143, v143, v185\n" \
"v_mul_f32 v144, v144, v185\n" \
"v_mul_f32 v145, v145, v185\n" \
"v_mul_f32 v146, v146, v185\n" \
"v_mul_f32 v147, v147, v185\n" \
"v_mul_f32 v148, v148, v185\n" \
"v_mul_f32 v149, v149, v185\n" \
"v_mul_f32 v150, v150, v185\n" \
"v_mul_f32 v151, v151, v185\n" \
"v_mul_f32 v152, v152, v185\n" \
"v_mul_f32 v153, v153, v185\n" \
"v_mul_f32 v154, v154, v185\n" \
"v_mul_f32 v155, v155, v185\n" \
"v_mul_f32 v156, v156, v185\n" \
"v_mul_f32 v157, v157, v185\n" \
"v_mul_f32 v158, v158, v185\n" \
"v_mul_f32 v159, v159, v185\n" \
"s_add_u32 s80, s76, 4096\n" \
"v_cvt_pk_bf16_f32 v112, v128, v129\n" \
"v_cvt_pk_bf16_f32 v113, v130, v131\n" \
"v_cvt_pk_bf16_f32 v114, v132, v133\n" \
"v_cvt_pk_bf16_f32 v115, v134, v135\n" \
"buffer_store_dwordx4 v[112:115], v191, s[56:59], s76 offen offset:0 sc1\n" \
"v_cvt_pk_bf16_f32 v116, v136, v137\n" \
"v_cvt_pk_bf16_f32 v117, v138, v139\n" \
"v_cvt_pk_bf16_f32 v118, v140, v141\n" \
"v_cvt_pk_bf16_f32 v119, v142, v143\n" \
"buffer_store_dwordx4 v[116:119], v191, s[56:59], s76 offen offset:2048 sc1\n" \
"v_cvt_pk_bf16_f32 v120, v144, v145\n" \
"v_cvt_pk_bf16_f32 v121, v146, v147\n" \
"v_cvt_pk_bf16_f32 v122, v148, v149\n" \
"v_cvt_pk_bf16_f32 v123, v150, v151\n" \
"buffer_store_dwordx4 v[120:123], v191, s[56:59], s80 offen offset:0 sc1\n" \
"v_cvt_pk_bf16_f32 v124, v152, v153\n" \
"v_cvt_pk_bf16_f32 v125, v154, v155\n" \
"v_cvt_pk_bf16_f32 v126, v156, v157\n" \
"v_cvt_pk_bf16_f32 v127, v158, v159\n" \
"buffer_store_dwordx4 v[124:127], v191, s[56:59], s80 offen offset:2048 sc1\n" \
"s_waitcnt vmcnt(0)\n" \
"s_setpc_b64 s[68:69]\n" \
"Lpl2_%=:\n" \
"s_mov_b32 s31, 0\n" \
"Lpk2_%=:\n" \
"s_sleep 2\n" \
"buffer_load_dword v220, v221, s[56:59], s81 offen sc1\n" \
"s_waitcnt vmcnt(0)\n" \
"v_cmp_ne_u32 vcc, s60, v220\n" \
"s_nop 1\n" \
"s_and_b32 vcc_hi, vcc_lo, s80\n" \
"s_cbranch_scc0 Lpr2_%=\n" \
"s_add_u32 s31, s31, 1\n" \
"s_cmp_lt_u32 s31, 0x40000\n" \
"s_cbranch_scc1 Lpk2_%=\n" \
"s_branch Lpr2_%=\n" \
"Lpl3_%=:\n" \
"s_mov_b32 s31, 0\n" \
"Lpk3_%=:\n" \
"s_sleep 2\n" \
"buffer_load_dwordx2 v[226:227], v228, s[56:59], s30 offen sc1\n" \
"s_waitcnt vmcnt(0)\n" \
"v_xor_b32 v226, v226, v227\n" \
"v_cmp_ne_u32 vcc, s60, v226\n" \
"s_nop 1\n" \
"s_and_b32 vcc_hi, vcc_lo, -1\n" \
"s_cbranch_scc0 Lpr3_%=\n" \
"s_add_u32 s31, s31, 1\n" \
"s_cmp_lt_u32 s31, 0x40000\n" \
"s_cbranch_scc1 Lpk3_%=\n" \
"s_branch Lpr3_%=\n" \
"Lpl4_%=:\n" \
"s_mov_b32 s31, 0\n" \
"Lpk4_%=:\n" \
"s_sleep 2\n" \
"buffer_load_dword v220, off, s[56:59], s79 sc1\n" \
"s_waitcnt vmcnt(0)\n" \
"v_cmp_ne_u32 vcc, s60, v220\n" \
"s_nop 1\n" \
"s_and_b32 vcc_hi, vcc_lo, 1\n" \
"s_cbranch_scc0 Lpr4_%=\n" \
"s_add_u32 s31, s31, 1\n" \
"s_cmp_lt_u32 s31, 0x40000\n" \
"s_cbranch_scc1 Lpk4_%=\n" \
"s_branch Lpr4_%=\n" \
"Lpl5_%=:\n" \
"s_mov_b32 s31, 0\n" \
"Lpk5_%=:\n" \
"s_sleep 2\n" \
"buffer_load_dword v220, v221, s[56:59], s82 offen sc1\n" \
"s_waitcnt vmcnt(0)\n" \
"v_cmp_ne_u32 vcc, s60, v220\n" \
"s_nop 1\n" \
"s_and_b32 vcc_hi, vcc_lo, 65535\n" \
"s_cbranch_scc0 Lpr5_%=\n" \
"s_add_u32 s31, s31, 1\n" \
"s_cmp_lt_u32 s31, 0x40000\n" \
"s_cbranch_scc1 Lpk5_%=\n" \
"s_branch Lpr5_%=\n" \
"Lcode_end_%=:\n" \
".p2align 12\n" \
""
#define MK_ASM_CLOBBERS "v1", "v2", "v3", "v4", "v5", "v6", "v7", "v8", "v9", "v10", "v11", "v12", "v13", "v14", "v15", "v16", "v17", "v18", "v19", "v20", "v21", "v22", "v23", "v24", "v25", "v26", "v27", "v28", "v29", "v30", "v31", "v32", "v33", "v34", "v35", "v36", "v37", "v38", "v39", "v40", "v41", "v42", "v43", "v44", "v45", "v46", "v47", "v48", "v49", "v50", "v51", "v52", "v53", "v54", "v55", "v56", "v57", "v58", "v59", "v60", "v61", "v62", "v63", "v64", "v65", "v66", "v67", "v68", "v69", "v70", "v71", "v72", "v73", "v74", "v75", "v76", "v77", "v78", "v79", "v80", "v81", "v82", "v83", "v84", "v85", "v86", "v87", "v88", "v89", "v90", "v91", "v92", "v93", "v94", "v95", "v96", "v97", "v98", "v99", "v100", "v101", "v102", "v103", "v104", "v105", "v106", "v107", "v108", "v109", "v110", "v111", "v112", "v113", "v114", "v115", "v116", "v117", "v118", "v119", "v120", "v121", "v122", "v123", "v124", "v125", "v126", "v127", "v128", "v129", "v130", "v131", "v132", "v133", "v134", "v135", "v136", "v137", "v138", "v139", "v140", "v141", "v142", "v143", "v144", "v145", "v146", "v147", "v148", "v149", "v150", "v151", "v152", "v153", "v154", "v155", "v156", "v157", "v158", "v159", "v160", "v161", "v162", "v163", "v164", "v165", "v166", "v167", "v168", "v169", "v170", "v171", "v172", "v173", "v174", "v175", "v176", "v177", "v178", "v179", "v180", "v181", "v182", "v183", "v184", "v185", "v186", "v187", "v188", "v189", "v190", "v191", "v192", "v193", "v194", "v195", "v196", "v197", "v198", "v199", "v200", "v201", "v202", "v203", "v204", "v205", "v206", "v207", "v208", "v209", "v210", "v211", "v212", "v213", "v214", "v215", "v216", "v217", "v218", "v219", "v220", "v221", "v222", "v223", "v224", "v225", "v226", "v227", "v228", "v229", "v230", "v231", "v232", "v233", "v234", "v235", "v236", "v237", "v238", "v239", "v240", "v241", "v242", "v243", "v244", "v245", "v246", "v247", "v248", "v249", "v250", "v251", "v252", "v253", "v254", "v255", "a0", "a1", "a2", "a3", "a4", "a5", "a6", "a7", "a8", "a9", "a10", "a11", "a12", "a13", "a14", "a15", "a16", "a17", "a18", "a19", "a20", "a21", "a22", "a23", "a24", "a25", "a26", "a27", "a28", "a29", "a30", "a31", "a32", "a33", "a34", "a35", "a36", "a37", "a38", "a39", "a40", "a41", "a42", "a43", "a44", "a45", "a46", "a47", "a48", "a49", "a50", "a51", "a52", "a53", "a54", "a55", "a56", "a57", "a58", "a59", "a60", "a61", "a62", "a63", "a64", "a65", "a66", "a67", "a68", "a69", "a70", "a71", "a72", "a73", "a74", "a75", "a76", "a77", "a78", "a79", "a80", "a81", "a82", "a83", "a84", "a85", "a86", "a87", "a88", "a89", "a90", "a91", "a92", "a93", "a94", "a95", "a96", "a97", "a98", "a99", "a100", "a101", "a102", "a103", "a104", "a105", "a106", "a107", "a108", "a109", "a110", "a111", "a112", "a113", "a114", "a115", "a116", "a117", "a118", "a119", "a120", "a121", "a122", "a123", "a124", "a125", "a126", "a127", "a128", "a129", "a130", "a131", "a132", "a133", "a134", "a135", "a136", "a137", "a138", "a139", "a140", "a141", "a142", "a143", "a144", "a145", "a146", "a147", "a148", "a149", "a150", "a151", "a152", "a153", "a154", "a155", "a156", "a157", "a158", "a159", "a160", "a161", "a162", "a163", "a164", "a165", "a166", "a167", "a168", "a169", "a170", "a171", "a172", "a173", "a174", "a175", "a176", "a177", "a178", "a179", "a180", "a181", "a182", "a183", "a184", "a185", "a186", "a187", "a188", "a189", "a190", "a191", "a192", "a193", "a194", "a195", "a196", "a197", "a198", "a199", "a200", "a201", "a202", "a203", "a204", "a205", "a206", "a207", "a208", "a209", "a210", "a211", "a212", "a213", "a214", "a215", "a216", "a217", "a218", "a219", "a220", "a221", "a222", "a223", "a224", "a225", "a226", "a227", "a228", "a229", "a230", "a231", "a232", "a233", "a234", "a235", "a236", "a237", "a238", "a239", "a240", "a241", "a242", "a243", "a244", "a245", "a246", "a247", "a248", "a249", "a250", "a251", "a252", "a253", "a254", "a255", "s8", "s9", "s10", "s11", "s12", "s13", "s14", "s15", "s16", "s17", "s18", "s19", "s20", "s21", "s22", "s23", "s24", "s25", "s26", "s27", "s28", "s29", "s30", "s31", "s36", "s37", "s38", "s39", "s40", "s41", "s42", "s43", "s44", "s45", "s46", "s47", "s48", "s49", "s50", "s51", "s52", "s53", "s54", "s55", "s56", "s57", "s58", "s59", "s60", "s61", "s62", "s63", "s64", "s65", "s66", "s67", "s68", "s69", "s70", "s71", "s72", "s73", "s74", "s75", "s76", "s77", "s78", "s79", "s80", "s81", "s82", "s83", "s84", "s85", "s86", "s87", "s88", "s89", "s90", "s91", "s92", "s93", "s94", "s95", "vcc", "scc", "memory"
namespace mk {
constexpr int LDS_BYTES = 163840;
struct Args { const void* in[9]; void* out; void* ws; };
__global__ __launch_bounds__(256, 1) void mega_k(const Args a) {
    extern __shared__ __attribute__((aligned(16))) unsigned char shm[];
    const unsigned ldsb = (unsigned)(size_t)(LAS unsigned char*)shm;
    asm volatile(MK_ASM_BODY
                 :
                 : "s"(__builtin_amdgcn_kernarg_segment_ptr()), "s"(blockIdx.x), "v"(threadIdx.x), "s"(ldsb)
                 : MK_ASM_CLOBBERS);
}
}
extern "C" void kernel_launch(void* const* d_in, const int* in_sizes, int n_in, void* d_out, int out_size, void* d_ws, size_t ws_size, hipStream_t stream) {
    (void)hipFuncSetAttribute((const void*)mk::mega_k, hipFuncAttributeMaxDynamicSharedMemorySize, mk::LDS_BYTES);
    mk::Args a;
    for (int i = 0; i < 9; ++i) a.in[i] = d_in[i];
    a.out = d_out; a.ws = (char*)d_ws + ((ws_size - WS_END) & ~(size_t)0x1fffff);
    mk::mega_k<<<256, 256, mk::LDS_BYTES, stream>>>(a);
}
```

```cpp
#include <hip/hip_runtime.h>
#include <stdint.h>
#define LAS __attribute__((address_space(3)))
constexpr size_t LAY_TS = 17408, LAY_HS = 557824, LAY_CRS = 266240, LAY_ORS = 69632, WS_K = 0, WS_V = 17850368, WS_CTX = 35700736, WS_OB = 52740096, WS_FL = 65585152, LAY_EXD = 29950080, WS_END = 87147648;
#define MK_ASM_BODY \
"s_load_dwordx16 s[8:23], %0, 0x0\n" \
"s_load_dwordx4 s[24:27], %0, 0x40\n" \
"s_load_dwordx2 s[28:29], %0, 0x50\n" \
"v_readfirstlane_b32 s61, %2\n" \
"s_lshr_b32 s61, s61, 6\n" \
"v_mbcnt_lo_u32_b32 v52, -1, 0\n" \
"v_mbcnt_hi_u32_b32 v52, -1, v52\n" \
"v_lshlrev_b32 v248, 4, v52\n" \
"s_getpc_b64 s[64:65]\n" \
"Lpc_%=:\n" \
"s_lshr_b32 s70, %1, 3\n" \
"s_lshl_b32 s70, s70, 2\n" \
"s_add_u32 s70, s70, s61\n" \
"s_and_b32 s71, s70, 127\n" \
"s_lshl_b32 s71, s71, 10\n" \
"v_lshl_add_u32 v53, v248, 0, s71\n" \
"s_mov_b32 s71, Lcode_end_%=-Lpc_%=-4\n" \
"v_min_u32 v53, s71, v53\n" \
"global_load_dword v253, v53, s[64:65]\n" \
"s_and_b32 s62, %1, 7\n" \
"s_lshl_b32 s62, s62, 2\n" \
"s_lshr_b32 s70, %1, 6\n" \
"s_add_u32 s62, s62, s70\n" \
"s_bfe_u32 s63, %1, 0x30003\n" \
"s_lshl_b32 s87, s61, 13\n" \
"s_lshl_b32 s95, s61, 12\n" \
"v_bfe_u32 v53, v52, 4, 1\n" \
"v_bfe_u32 v54, v52, 2, 2\n" \
"v_lshl_add_u32 v54, v53, 3, v54\n" \
"s_lshl_b32 s71, s61, 4\n" \
"v_add_u32 v54, s71, v54\n" \
"v_lshlrev_b32 v54, 13, v54\n" \
"v_lshlrev_b32 v53, 1, v53\n" \
"v_and_b32 v55, 3, v52\n" \
"v_xor_b32 v53, v53, v55\n" \
"v_lshl_add_u32 v54, v53, 4, v54\n" \
"v_lshrrev_b32 v53, 5, v52\n" \
"v_lshl_add_u32 v54, v53, 6, v54\n" \
"v_add_u32 v40, 4096, v54\n" \
"v_add_u32 v41, 3072, v54\n" \
"v_add_u32 v42, 2176, v54\n" \
"v_add_u32 v43, 1152, v54\n" \
"v_add_u32 v44, 32768, v54\n" \
"v_add_u32 v45, 31744, v54\n" \
"v_add_u32 v46, 30848, v54\n" \
"v_add_u32 v47, 29824, v54\n" \
"v_lshrrev_b32 v55, 3, v52\n" \
"v_and_b32 v56, 7, v52\n" \
"v_xor_b32 v56, v56, v55\n" \
"v_lshlrev_b32 v56, 4, v56\n" \
"v_lshl_add_u32 v57, v55, 10, v56\n" \
"v_add_u32 v32, 528384, v57\n" \
"v_add_u32 v33, 535552, v57\n" \
"v_add_u32 v34, 542720, v57\n" \
"v_add_u32 v35, 549888, v57\n" \
"v_add_u32 v36, 0, v57\n" \
"v_add_u32 v37, 7168, v57\n" \
"v_add_u32 v38, 14336, v57\n" \
"v_add_u32 v39, 21504, v57\n" \
"s_lshl_b32 s88, s62, 8\n" \
"s_lshl_b32 s89, s63, 18\n" \
"s_lshl_b32 s70, s63, 16\n" \
"s_and_b32 s71, s87, 0x4000\n" \
"s_lshl_b32 s71, s71, 1\n" \
"s_add_u32 s70, s70, s71\n" \
"s_andn2_b32 s71, 1, s61\n" \
"s_lshl_b32 s71, s71, 20\n" \
"s_add_u32 s70, s70, s71\n" \
"s_lshl_b32 s66, %1, 2\n" \
"s_add_u32 s66, s66, s61\n" \
"s_lshl_b32 s66, s66, 10\n" \
"s_lshr_b32 s67, %1, 3\n" \
"s_lshl_b32 s67, s67, 2\n" \
"s_add_u32 s67, s67, s61\n" \
"s_lshl_b32 s67, s67, 13\n" \
"v_lshrrev_b32 v63, 6, v248\n" \
"v_lshlrev_b32 v63, 8, v63\n" \
"v_and_or_b32 v63, v248, 48, v63\n" \
"s_lshl_b32 s64, s67, 1\n" \
"v_lshl_add_u32 v63, v63, 2, s64\n" \
"s_and_b32 s64, %1, 7\n" \
"s_lshl_b32 s64, s64, 10\n" \
"s_add_u32 s67, s67, s64\n" \
"s_add_u32 s73, s63, s62\n" \
"s_and_b32 s73, s73, 1\n" \
"s_mov_b32 s74, 128\n" \
"s_cmp_lg_u32 s73, 0\n" \
"s_cselect_b64 exec, -1, 0\n" \
"v_swap_b32 v32, v40\n" \
"v_swap_b32 v33, v41\n" \
"v_swap_b32 v34, v42\n" \
"v_swap_b32 v35, v43\n" \
"v_swap_b32 v36, v44\n" \
"v_swap_b32 v37, v45\n" \
"v_swap_b32 v38, v46\n" \
"v_swap_b32 v39, v47\n" \
"s_mov_b64 exec, -1\n" \
"s_cselect_b32 s76, s70, s88\n" \
"s_cselect_b32 s77, s88, s70\n" \
"s_cselect_b32 s75, 524288, s74\n" \
"s_cselect_b32 s74, s74, 524288\n" \
"s_waitcnt lgkmcnt(0)\n" \
"s_cselect_b64 s[52:53], s[10:11], s[14:15]\n" \
"s_cselect_b64 s[54:55], s[14:15], s[10:11]\n" \
"s_cselect_b64 s[50:51], s[10:11], s[16:17]\n" \
"s_cselect_b64 s[40:41], s[16:17], s[10:11]\n" \
"s_add_u32 s52, s52, s76\n" \
"s_addc_u32 s53, s53, 0\n" \
"s_add_u32 s50, s50, s76\n" \
"s_addc_u32 s51, s51, 0\n" \
"s_add_u32 m0, s87, 36864\n" \
"s_nop 0\n" \
"global_load_lds_dwordx4 v40, s[52:53] offset:-4096\n" \
"global_load_lds_dwordx4 v41, s[50:51] offset:-3072\n" \
"global_load_lds_dwordx4 v42, s[52:53] offset:-2048\n" \
"global_load_lds_dwordx4 v43, s[50:51] offset:-1024\n" \
"global_load_lds_dwordx4 v44, s[52:53] offset:0\n" \
"global_load_lds_dwordx4 v45, s[50:51] offset:1024\n" \
"global_load_lds_dwordx4 v46, s[52:53] offset:2048\n" \
"global_load_lds_dwordx4 v47, s[50:51] offset:3072\n" \
"s_add_u32 s52, s52, s74\n" \
"s_addc_u32 s53, s53, 0\n" \
"s_add_u32 s50, s50, s74\n" \
"s_addc_u32 s51, s51, 0\n" \
"s_add_u32 s54, s54, s77\n" \
"s_addc_u32 s55, s55, 0\n" \
"s_add_u32 s40, s40, s77\n" \
"s_addc_u32 s41, s41, 0\n" \
"s_add_u32 m0, s87, 4096\n" \
"s_nop 0\n" \
"global_load_lds_dwordx4 v32, s[54:55] offset:-4096\n" \
"global_load_lds_dwordx4 v33, s[40:41] offset:-3072\n" \
"global_load_lds_dwordx4 v34, s[54:55] offset:-2048\n" \
"global_load_lds_dwordx4 v35, s[40:41] offset:-1024\n" \
"global_load_lds_dwordx4 v36, s[54:55] offset:0\n" \
"global_load_lds_dwordx4 v37, s[40:41] offset:1024\n" \
"global_load_lds_dwordx4 v38, s[54:55] offset:2048\n" \
"global_load_lds_dwordx4 v39, s[40:41] offset:3072\n" \
"s_add_u32 s54, s54, s75\n" \
"s_addc_u32 s55, s55, 0\n" \
"s_add_u32 s40, s40, s75\n" \
"s_addc_u32 s41, s41, 0\n" \
"s_mov_b64 s[56:57], s[28:29]\n" \
"s_and_b32 s57, s57, 0xffff\n" \
"s_mov_b32 s58, 87147648\n" \
"s_mov_b32 s59, 0x20000\n" \
"v_add_u32 v228, 4096, v54\n" \
"v_add_u32 v229, 3200, v54\n" \
"v_lshl_add_u32 v57, v55, 11, v56\n" \
"v_add_u32 v224, 0, v57\n" \
"v_add_u32 v225, 15360, v57\n" \
"v_add_u32 v226, 129024, v57\n" \
"v_add_u32 v227, 144384, v57\n" \
"s_lshl_b32 s70, s61, 15\n" \
"s_add_u32 s70, s70, 0\n" \
"s_add_u32 s71, s70, s89\n" \
"s_add_u32 s71, s71, 4096\n" \
"s_add_u32 s48, s8, s71\n" \
"s_addc_u32 s49, s9, 0\n" \
"s_sub_u32 s71, s70, s89\n" \
"s_add_u32 s71, s71, 3932160\n" \
"s_add_u32 s46, s8, s71\n" \
"s_addc_u32 s47, s9, 0\n" \
"s_add_u32 s70, s88, 0\n" \
"s_add_u32 s44, s12, s70\n" \
"s_addc_u32 s45, s13, 0\n" \
"s_add_u32 s42, s44, 30720\n" \
"s_addc_u32 s43, s45, 0\n" \
"s_lshr_b32 s70, s28, 31\n" \
"s_lshl_b32 s70, s70, 30\n" \
"s_sub_u32 s28, s28, s70\n" \
"v_add_u32 v250, s70, v248\n" \
"v_lshlrev_b32 v245, 2, v52\n" \
"v_mov_b32 v254, 0x30000\n" \
"v_lshrrev_b32 v55, 4, v52\n" \
"v_and_b32 v249, 0xf0, v248\n" \
"v_lshl_add_u32 v249, v55, 9, v249\n" \
"v_and_b32 v53, 15, v52\n" \
"s_lshl_b32 s70, s61, 4\n" \
"v_lshlrev_b32 v54, 2, v55\n" \
"v_sub_u32 v251, v53, v54\n" \
"v_add_u32 v251, s70, v251\n" \
"v_and_b32 v54, 7, v52\n" \
"v_xor_b32 v54, v54, v55\n" \
"v_lshlrev_b32 v53, 7, v53\n" \
"s_add_u32 s71, s87, 131072\n" \
"s_lshr_b32 s70, s61, 1\n" \
"s_lshl_b32 s70, s70, 14\n" \
"s_lshl_b32 s76, s73, 15\n" \
"s_add_u32 s70, s70, s76\n" \
"v_xor_b32 v56, 0, v54\n" \
"v_lshl_add_u32 v56, v56, 4, v53\n" \
"v_add_u32 v230, s71, v56\n" \
"v_add_u32 v48, s70, v56\n" \
"v_xor_b32 v56, 4, v54\n" \
"v_lshl_add_u32 v56, v56, 4, v53\n" \
"v_add_u32 v231, s71, v56\n" \
"v_add_u32 v49, s70, v56\n" \
"v_and_b32 v53, 3, v52\n" \
"v_lshlrev_b32 v53, 3, v53\n" \
"v_bfe_u32 v54, v52, 2, 2\n" \
"v_lshl_add_u32 v53, v54, 6, v53\n" \
"v_bfe_u32 v54, v52, 4, 1\n" \
"v_lshl_add_u32 v53, v54, 8, v53\n" \
"v_lshl_add_u32 v53, v54, 5, v53\n" \
"v_lshrrev_b32 v54, 5, v52\n" \
"s_and_b32 s70, s61, 1\n" \
"s_lshl_b32 s70, s70, 11\n" \
"s_xor_b32 s76, s76, 0x8000\n" \
"s_add_u32 s70, s70, s76\n" \
"v_lshl_add_u32 v56, v54, 13, v53\n" \
"v_add_u32 v50, s70, v56\n" \
"v_xor_b32 v51, 32, v50\n" \
"v_lshl_add_u32 v232, v54, 12, v53\n" \
"v_xor_b32 v233, 32, v232\n" \
"s_mov_b64 exec, 0\n" \
"s_cmp_eq_u32 s61, 0\n" \
"s_cbranch_scc1 Lst0x0_%=\n" \
"s_cmp_eq_u32 s61, 1\n" \
"s_cbranch_scc1 Lst0x1_%=\n" \
"s_cmp_eq_u32 s61, 2\n" \
"s_cbranch_scc1 Lst0x2_%=\n" \
"s_branch Lst0x3_%=\n" \
"Lcb0_%=:\n" \
"s_mov_b64 exec, -1\n" \
"s_add_u32 m0, s87, 135168\n" \
"s_nop 0\n" \
"global_load_lds_dwordx4 v40, s[52:53] offset:-4096\n" \
"global_load_lds_dwordx4 v41, s[50:51] offset:-3072\n" \
"global_load_lds_dwordx4 v42, s[52:53] offset:-2048\n" \
"global_load_lds_dwordx4 v43, s[50:51] offset:-1024\n" \
"global_load_lds_dwordx4 v44, s[52:53] offset:0\n" \
"global_load_lds_dwordx4 v45, s[50:51] offset:1024\n" \
"global_load_lds_dwordx4 v46, s[52:53] offset:2048\n" \
"global_load_lds_dwordx4 v47, s[50:51] offset:3072\n" \
"s_add_u32 s52, s52, s74\n" \
"s_addc_u32 s53, s53, 0\n" \
"s_add_u32 s50, s50, s74\n" \
"s_addc_u32 s51, s51, 0\n" \
"s_waitcnt vmcnt(8)\n" \
"s_barrier\n" \
"ds_read_b128 v[64:67], v48 offset:0\n" \
"ds_read_b64_tr_b16 v[128:129], v50 offset:0\n" \
"ds_read_b64_tr_b16 v[130:131], v50 offset:4096\n" \
"ds_read_b128 v[68:71], v49 offset:0\n" \
"ds_read_b64_tr_b16 v[132:133], v50 offset:16384\n" \
"ds_read_b64_tr_b16 v[134:135], v50 offset:20480\n" \
"ds_read_b128 v[72:75], v48 offset:2048\n" \
"ds_read_b64_tr_b16 v[136:137], v51 offset:0\n" \
"ds_read_b64_tr_b16 v[138:139], v51 offset:4096\n" \
"ds_read_b128 v[76:79], v49 offset:2048\n" \
"ds_read_b64_tr_b16 v[140:141], v51 offset:16384\n" \
"ds_read_b64_tr_b16 v[142:143], v51 offset:20480\n" \
"ds_read_b64_tr_b16 v[144:145], v50 offset:512\n" \
"ds_read_b64_tr_b16 v[146:147], v50 offset:4608\n" \
"ds_read_b64_tr_b16 v[148:149], v50 offset:16896\n" \
"ds_read_b64_tr_b16 v[150:151], v50 offset:20992\n" \
"ds_read_b64_tr_b16 v[152:153], v51 offset:512\n" \
"ds_read_b64_tr_b16 v[154:155], v51 offset:4608\n" \
"ds_read_b64_tr_b16 v[156:157], v51 offset:16896\n" \
"ds_read_b64_tr_b16 v[158:159], v51 offset:20992\n" \
"s_waitcnt lgkmcnt(14)\n" \
"v_mfma_f32_16x16x32_bf16 a[0:3], v[128:131], v[64:67], 0\n" \
"ds_read_b128 v[120:123], v48 offset:14336\n" \
"s_add_u32 m0, s87, 69632\n" \
"v_mfma_f32_16x16x32_bf16 a[0:3], v[132:135], v[68:71], a[0:3]\n" \
"s_waitcnt lgkmcnt(9)\n" \
"ds_read_b128 v[80:83], v48 offset:4096\n" \
"global_load_lds_dwordx4 v32, s[54:55] offset:-4096\n" \
"v_mfma_f32_16x16x32_bf16 a[4:7], v[136:139], v[64:67], 0\n" \
"ds_read_b128 v[124:127], v49 offset:14336\n" \
"v_mfma_f32_16x16x32_bf16 a[4:7], v[140:143], v[68:71], a[4:7]\n" \
"s_waitcnt lgkmcnt(7)\n" \
"ds_read_b128 v[84:87], v49 offset:4096\n" \
"v_mfma_f32_16x16x32_bf16 a[8:11], v[144:147], v[64:67], 0\n" \
"ds_read_b64_tr_b16 v[160:161], v50 offset:1024\n" \
"global_load_lds_dwordx4 v33, s[40:41] offset:-3072\n" \
"v_mfma_f32_16x16x32_bf16 a[8:11], v[148:151], v[68:71], a[8:11]\n" \
"s_waitcnt lgkmcnt(4)\n" \
"ds_read_b128 v[88:91], v48 offset:6144\n" \
"v_mfma_f32_16x16x32_bf16 a[12:15], v[152:155], v[64:67], 0\n" \
"ds_read_b64_tr_b16 v[162:163], v50 offset:5120\n" \
"v_mfma_f32_16x16x32_bf16 a[12:15], v[156:159], v[68:71], a[12:15]\n" \
"ds_read_b128 v[92:95], v49 offset:6144\n" \
"global_load_lds_dwordx4 v34, s[54:55] offset:-2048\n" \
"v_mfma_f32_16x16x32_bf16 a[44:47], v[152:155], v[72:75], 0\n" \
"ds_read_b64_tr_b16 v[164:165], v50 offset:17408\n" \
"v_mfma_f32_16x16x32_bf16 a[44:47], v[156:159], v[76:79], a[44:47]\n" \
"ds_read_b128 v[96:99], v48 offset:8192\n" \
"v_mfma_f32_16x16x32_bf16 a[40:43], v[144:147], v[72:75], 0\n" \
"ds_read_b64_tr_b16 v[166:167], v50 offset:21504\n" \
"global_load_lds_dwordx4 v35, s[40:41] offset:-1024\n" \
"v_mfma_f32_16x16x32_bf16 a[40:43], v[148:151], v[76:79], a[40:43]\n" \
"ds_read_b128 v[100:103], v49 offset:8192\n" \
"v_mfma_f32_16x16x32_bf16 a[36:39], v[136:139], v[72:75], 0\n" \
"ds_read_b64_tr_b16 v[168:169], v51 offset:1024\n" \
"v_mfma_f32_16x16x32_bf16 a[36:39], v[140:143], v[76:79], a[36:39]\n" \
"ds_read_b128 v[104:107], v48 offset:10240\n" \
"global_load_lds_dwordx4 v36, s[54:55] offset:0\n" \
"v_mfma_f32_16x16x32_bf16 a[32:35], v[128:131], v[72:75], 0\n" \
"ds_read_b64_tr_b16 v[170:171], v51 offset:5120\n" \
"v_mfma_f32_16x16x32_bf16 a[32:35], v[132:135], v[76:79], a[32:35]\n" \
"s_waitcnt lgkmcnt(4)\n" \
"ds_read_b128 v[108:111], v49 offset:10240\n" \
"v_mfma_f32_16x16x32_bf16 a[64:67], v[128:131], v[80:83], 0\n" \
"ds_read_b64_tr_b16 v[172:173], v51 offset:17408\n" \
"global_load_lds_dwordx4 v37, s[40:41] offset:1024\n" \
"v_mfma_f32_16x16x32_bf16 a[64:67], v[132:135], v[84:87], a[64:67]\n" \
"ds_read_b128 v[112:115], v48 offset:12288\n" \
"v_mfma_f32_16x16x32_bf16 a[68:71], v[136:139], v[80:83], 0\n" \
"ds_read_b64_tr_b16 v[174:175], v51 offset:21504\n" \
"v_mfma_f32_16x16x32_bf16 a[68:71], v[140:143], v[84:87], a[68:71]\n" \
"ds_read_b128 v[116:119], v49 offset:12288\n" \
"global_load_lds_dwordx4 v38, s[54:55] offset:2048\n" \
"v_mfma_f32_16x16x32_bf16 a[72:75], v[144:147], v[80:83], 0\n" \
"ds_read_b64_tr_b16 v[176:177], v50 offset:1536\n" \
"v_mfma_f32_16x16x32_bf16 a[72:75], v[148:151], v[84:87], a[72:75]\n" \
"global_load_lds_dwordx4 v39, s[40:41] offset:3072\n" \
"v_mfma_f32_16x16x32_bf16 a[76:79], v[152:155], v[80:83], 0\n" \
"s_add_u32 s54, s54, s75\n" \
"s_addc_u32 s55, s55, 0\n" \
"ds_read_b64_tr_b16 v[178:179], v50 offset:5632\n" \
"v_mfma_f32_16x16x32_bf16 a[76:79], v[156:159], v[84:87], a[76:79]\n" \
"s_add_u32 s40, s40, s75\n" \
"s_addc_u32 s41, s41, 0\n" \
"s_mov_b32 s90, 65593344\n" \
"v_mfma_f32_16x16x32_bf16 a[108:111], v[152:155], v[88:91], 0\n" \
"ds_read_b64_tr_b16 v[180:181], v50 offset:17920\n" \
"v_mfma_f32_16x16x32_bf16 a[108:111], v[156:159], v[92:95], a[108:111]\n" \
"buffer_load_dword v244, off, s[56:59], s90 sc1\n" \
"v_mfma_f32_16x16x32_bf16 a[104:107], v[144:147], v[88:91], 0\n" \
"ds_read_b64_tr_b16 v[182:183], v50 offset:22016\n" \
"v_mfma_f32_16x16x32_bf16 a[104:107], v[148:151], v[92:95], a[104:107]\n" \
"s_add_u32 m0, s87, 102400\n" \
"v_mfma_f32_16x16x32_bf16 a[100:103], v[136:139], v[88:91], 0\n" \
"ds_read_b64_tr_b16 v[184:185], v51 offset:1536\n" \
"global_load_lds_dwordx4 v40, s[52:53] offset:-4096\n" \
"v_mfma_f32_16x16x32_bf16 a[100:103], v[140:143], v[92:95], a[100:103]\n" \
"v_mfma_f32_16x16x32_bf16 a[96:99], v[128:131], v[88:91], 0\n" \
"ds_read_b64_tr_b16 v[186:187], v51 offset:5632\n" \
"v_mfma_f32_16x16x32_bf16 a[96:99], v[132:135], v[92:95], a[96:99]\n" \
"s_waitcnt lgkmcnt(2)\n" \
"global_load_lds_dwordx4 v41, s[50:51] offset:-3072\n" \
"v_mfma_f32_16x16x32_bf16 a[128:131], v[128:131], v[96:99], 0\n" \
"ds_read_b64_tr_b16 v[188:189], v51 offset:17920\n" \
"v_mfma_f32_16x16x32_bf16 a[128:131], v[132:135], v[100:103], a[128:131]\n" \
"v_mfma_f32_16x16x32_bf16 a[132:135], v[136:139], v[96:99], 0\n" \
"ds_read_b64_tr_b16 v[190:191], v51 offset:22016\n" \
"global_load_lds_dwordx4 v42, s[52:53] offset:-2048\n" \
"v_mfma_f32_16x16x32_bf16 a[132:135], v[140:143], v[100:103], a[132:135]\n" \
"v_mfma_f32_16x16x32_bf16 a[136:139], v[144:147], v[96:99], 0\n" \
"v_mfma_f32_16x16x32_bf16 a[136:139], v[148:151], v[100:103], a[136:139]\n" \
"global_load_lds_dwordx4 v43, s[50:51] offset:-1024\n" \
"v_mfma_f32_16x16x32_bf16 a[140:143], v[152:155], v[96:99], 0\n" \
"v_mfma_f32_16x16x32_bf16 a[140:143], v[156:159], v[100:103], a[140:143]\n" \
"global_load_lds_dwordx4 v44, s[52:53] offset:0\n" \
"v_mfma_f32_16x16x32_bf16 a[172:175], v[152:155], v[104:107], 0\n" \
"s_mul_i32 s76, s73, 0x8000\n" \
"s_add_u32 s77, s76, 0x10000\n" \
"s_sub_u32 s78, 0x18000, s76\n" \
"v_mfma_f32_16x16x32_bf16 a[172:175], v[156:159], v[108:111], a[172:175]\n" \
"v_add_u32 v48, s77, v48\n" \
"v_add_u32 v49, s77, v49\n" \
"v_mfma_f32_16x16x32_bf16 a[168:171], v[144:147], v[104:107], 0\n" \
"v_add_u32 v50, s78, v50\n" \
"v_add_u32 v51, s78, v51\n" \
"v_mfma_f32_16x16x32_bf16 a[168:171], v[148:151], v[108:111], a[168:171]\n" \
"global_load_lds_dwordx4 v45, s[50:51] offset:1024\n" \
"v_mfma_f32_16x16x32_bf16 a[164:167], v[136:139], v[104:107], 0\n" \
"v_mfma_f32_16x16x32_bf16 a[164:167], v[140:143], v[108:111], a[164:167]\n" \
"v_mfma_f32_16x16x32_bf16 a[160:163], v[128:131], v[104:107], 0\n" \
"global_load_lds_dwordx4 v46, s[52:53] offset:2048\n" \
"v_mfma_f32_16x16x32_bf16 a[160:163], v[132:135], v[108:111], a[160:163]\n" \
"v_mfma_f32_16x16x32_bf16 a[192:195], v[128:131], v[112:115], 0\n" \
"v_mfma_f32_16x16x32_bf16 a[192:195], v[132:135], v[116:119], a[192:195]\n" \
"global_load_lds_dwordx4 v47, s[50:51] offset:3072\n" \
"v_mfma_f32_16x16x32_bf16 a[196:199], v[136:139], v[112:115], 0\n" \
"s_add_u32 s52, s52, s74\n" \
"s_addc_u32 s53, s53, 0\n" \
"v_mfma_f32_16x16x32_bf16 a[196:199], v[140:143], v[116:119], a[196:199]\n" \
"s_add_u32 s50, s50, s74\n" \
"s_addc_u32 s51, s51, 0\n" \
"v_mfma_f32_16x16x32_bf16 a[200:203], v[144:147], v[112:115], 0\n" \
"v_mfma_f32_16x16x32_bf16 a[200:203], v[148:151], v[116:119], a[200:203]\n" \
"v_mfma_f32_16x16x32_bf16 a[204:207], v[152:155], v[112:115], 0\n" \
"v_mfma_f32_16x16x32_bf16 a[204:207], v[156:159], v[116:119], a[204:207]\n" \
"v_mfma_f32_16x16x32_bf16 a[236:239], v[152:155], v[120:123], 0\n" \
"v_mfma_f32_16x16x32_bf16 a[236:239], v[156:159], v[124:127], a[236:239]\n" \
"v_mfma_f32_16x16x32_bf16 a[232:235], v[144:147], v[120:123], 0\n" \
"v_mfma_f32_16x16x32_bf16 a[232:235], v[148:151], v[124:127], a[232:235]\n" \
"v_mfma_f32_16x16x32_bf16 a[228:231], v[136:139], v[120:123], 0\n" \
"v_mfma_f32_16x16x32_bf16 a[228:231], v[140:143], v[124:127], a[228:231]\n" \
"v_mfma_f32_16x16x32_bf16 a[224:227], v[128:131], v[120:123], 0\n" \
"s_waitcnt vmcnt(9) lgkmcnt(0)\n" \
"v_mfma_f32_16x16x32_bf16 a[224:227], v[132:135], v[124:127], a[224:227]\n" \
"s_barrier\n" \
"v_mfma_f32_16x16x32_bf16 a[16:19], v[64:67], v[160:163], 0\n" \
"s_add_u32 m0, s87, 36864\n" \
"ds_read_b64_tr_b16 v[128:129], v50 offset:0\n" \
"v_mfma_f32_16x16x32_bf16 a[16:19], v[68:71], v[164:167], a[16:19]\n" \
"global_load_lds_dwordx4 v32, s[54:55] offset:-4096\n" \
"v_mfma_f32_16x16x32_bf16 a[20:23], v[64:67], v[168:171], 0\n" \
"ds_read_b64_tr_b16 v[130:131], v50 offset:4096\n" \
"v_mfma_f32_16x16x32_bf16 a[20:23], v[68:71], v[172:175], a[20:23]\n" \
"v_mfma_f32_16x16x32_bf16 a[24:27], v[64:67], v[176:179], 0\n" \
"ds_read_b64_tr_b16 v[132:133], v50 offset:16384\n" \
"v_mfma_f32_16x16x32_bf16 a[24:27], v[68:71], v[180:183], a[24:27]\n" \
"global_load_lds_dwordx4 v33, s[40:41] offset:-3072\n" \
"v_mfma_f32_16x16x32_bf16 a[28:31], v[64:67], v[184:187], 0\n" \
"ds_read_b64_tr_b16 v[134:135], v50 offset:20480\n" \
"v_mfma_f32_16x16x32_bf16 a[28:31], v[68:71], v[188:191], a[28:31]\n" \
"v_mfma_f32_16x16x32_bf16 a[60:63], v[72:75], v[184:187], 0\n" \
"ds_read_b128 v[64:67], v48 offset:0\n" \
"v_mfma_f32_16x16x32_bf16 a[60:63], v[76:79], v[188:191], a[60:63]\n" \
"global_load_lds_dwordx4 v34, s[54:55] offset:-2048\n" \
"v_mfma_f32_16x16x32_bf16 a[56:59], v[72:75], v[176:179], 0\n" \
"ds_read_b64_tr_b16 v[136:137], v51 offset:0\n" \
"v_mfma_f32_16x16x32_bf16 a[56:59], v[76:79], v[180:183], a[56:59]\n" \
"v_mfma_f32_16x16x32_bf16 a[52:55], v[72:75], v[168:171], 0\n" \
"ds_read_b128 v[68:71], v49 offset:0\n" \
"v_mfma_f32_16x16x32_bf16 a[52:55], v[76:79], v[172:175], a[52:55]\n" \
"global_load_lds_dwordx4 v35, s[40:41] offset:-1024\n" \
"v_mfma_f32_16x16x32_bf16 a[48:51], v[72:75], v[160:163], 0\n" \
"ds_read_b64_tr_b16 v[138:139], v51 offset:4096\n" \
"v_mfma_f32_16x16x32_bf16 a[48:51], v[76:79], v[164:167], a[48:51]\n" \
"v_mfma_f32_16x16x32_bf16 a[80:83], v[80:83], v[160:163], 0\n" \
"ds_read_b128 v[72:75], v48 offset:2048\n" \
"v_mfma_f32_16x16x32_bf16 a[80:83], v[84:87], v[164:167], a[80:83]\n" \
"global_load_lds_dwordx4 v36, s[54:55] offset:0\n" \
"v_mfma_f32_16x16x32_bf16 a[84:87], v[80:83], v[168:171], 0\n" \
"ds_read_b64_tr_b16 v[140:141], v51 offset:16384\n" \
"v_mfma_f32_16x16x32_bf16 a[84:87], v[84:87], v[172:175], a[84:87]\n" \
"v_mfma_f32_16x16x32_bf16 a[88:91], v[80:83], v[176:179], 0\n" \
"ds_read_b128 v[76:79], v49 offset:2048\n" \
"v_mfma_f32_16x16x32_bf16 a[88:91], v[84:87], v[180:183], a[88:91]\n" \
"global_load_lds_dwordx4 v37, s[40:41] offset:1024\n" \
"v_mfma_f32_16x16x32_bf16 a[92:95], v[80:83], v[184:187], 0\n" \
"ds_read_b64_tr_b16 v[142:143], v51 offset:20480\n" \
"v_mfma_f32_16x16x32_bf16 a[92:95], v[84:87], v[188:191], a[92:95]\n" \
"v_mfma_f32_16x16x32_bf16 a[124:127], v[88:91], v[184:187], 0\n" \
"ds_read_b128 v[80:83], v48 offset:4096\n" \
"v_mfma_f32_16x16x32_bf16 a[124:127], v[92:95], v[188:191], a[124:127]\n" \
"global_load_lds_dwordx4 v38, s[54:55] offset:2048\n" \
"v_mfma_f32_16x16x32_bf16 a[120:123], v[88:91], v[176:179], 0\n" \
"ds_read_b64_tr_b16 v[144:145], v50 offset:512\n" \
"v_mfma_f32_16x16x32_bf16 a[120:123], v[92:95], v[180:183], a[120:123]\n" \
"v_mfma_f32_16x16x32_bf16 a[116:119], v[88:91], v[168:171], 0\n" \
"ds_read_b128 v[84:87], v49 offset:4096\n" \
"global_load_lds_dwordx4 v39, s[40:41] offset:3072\n" \
"v_mfma_f32_16x16x32_bf16 a[116:119], v[92:95], v[172:175], a[116:119]\n" \
"s_add_u32 s54, s54, s75\n" \
"s_addc_u32 s55, s55, 0\n" \
"v_mfma_f32_16x16x32_bf16 a[112:115], v[88:91], v[160:163], 0\n" \
"s_add_u32 s40, s40, s75\n" \
"s_addc_u32 s41, s41, 0\n" \
"ds_read_b64_tr_b16 v[146:147], v50 offset:4608\n" \
"v_mfma_f32_16x16x32_bf16 a[112:115], v[92:95], v[164:167], a[112:115]\n" \
"s_add_u32 s68, s66, 0x0\n" \
"s_add_u32 s64, s10, s68\n" \
"s_addc_u32 s65, s11, 0\n" \
"v_mfma_f32_16x16x32_bf16 a[144:147], v[96:99], v[160:163], 0\n" \
"ds_read_b128 v[88:91], v48 offset:6144\n" \
"v_mfma_f32_16x16x32_bf16 a[144:147], v[100:103], v[164:167], a[144:147]\n" \
"global_load_dword v253, v248, s[64:65]\n" \
"v_mfma_f32_16x16x32_bf16 a[148:151], v[96:99], v[168:171], 0\n" \
"ds_read_b64_tr_b16 v[148:149], v50 offset:16896\n" \
"v_mfma_f32_16x16x32_bf16 a[148:151], v[100:103], v[172:175], a[148:151]\n" \
"v_mfma_f32_16x16x32_bf16 a[152:155], v[96:99], v[176:179], 0\n" \
"ds_read_b128 v[92:95], v49 offset:6144\n" \
"v_mfma_f32_16x16x32_bf16 a[152:155], v[100:103], v[180:183], a[152:155]\n" \
"v_mfma_f32_16x16x32_bf16 a[156:159], v[96:99], v[184:187], 0\n" \
"ds_read_b64_tr_b16 v[150:151], v50 offset:20992\n" \
"v_mfma_f32_16x16x32_bf16 a[156:159], v[100:103], v[188:191], a[156:159]\n" \
"v_mfma_f32_16x16x32_bf16 a[188:191], v[104:107], v[184:187], 0\n" \
"ds_read_b128 v[96:99], v48 offset:8192\n" \
"v_mfma_f32_16x16x32_bf16 a[188:191], v[108:111], v[188:191], a[188:191]\n" \
"v_mfma_f32_16x16x32_bf16 a[184:187], v[104:107], v[176:179], 0\n" \
"ds_read_b64_tr_b16 v[152:153], v51 offset:512\n" \
"v_mfma_f32_16x16x32_bf16 a[184:187], v[108:111], v[180:183], a[184:187]\n" \
"v_mfma_f32_16x16x32_bf16 a[180:183], v[104:107], v[168:171], 0\n" \
"ds_read_b128 v[100:103], v49 offset:8192\n" \
"v_mfma_f32_16x16x32_bf16 a[180:183], v[108:111], v[172:175], a[180:183]\n" \
"v_mfma_f32_16x16x32_bf16 a[176:179], v[104:107], v[160:163], 0\n" \
"ds_read_b64_tr_b16 v[154:155], v51 offset:4608\n" \
"v_mfma_f32_16x16x32_bf16 a[176:179], v[108:111], v[164:167], a[176:179]\n" \
"v_mfma_f32_16x16x32_bf16 a[208:211], v[112:115], v[160:163], 0\n" \
"ds_read_b128 v[104:107], v48 offset:10240\n" \
"v_mfma_f32_16x16x32_bf16 a[208:211], v[116:119], v[164:167], a[208:211]\n" \
"v_mfma_f32_16x16x32_bf16 a[212:215], v[112:115], v[168:171], 0\n" \
"ds_read_b64_tr_b16 v[156:157], v51 offset:16896\n" \
"v_mfma_f32_16x16x32_bf16 a[212:215], v[116:119], v[172:175], a[212:215]\n" \
"v_mfma_f32_16x16x32_bf16 a[216:219], v[112:115], v[176:179], 0\n" \
"ds_read_b128 v[108:111], v49 offset:10240\n" \
"v_mfma_f32_16x16x32_bf16 a[216:219], v[116:119], v[180:183], a[216:219]\n" \
"v_mfma_f32_16x16x32_bf16 a[220:223], v[112:115], v[184:187], 0\n" \
"ds_read_b64_tr_b16 v[158:159], v51 offset:20992\n" \
"v_mfma_f32_16x16x32_bf16 a[220:223], v[116:119], v[188:191], a[220:223]\n" \
"v_mfma_f32_16x16x32_bf16 a[252:255], v[120:123], v[184:187], 0\n" \
"ds_read_b128 v[112:115], v48 offset:12288\n" \
"v_mfma_f32_16x16x32_bf16 a[252:255], v[124:127], v[188:191], a[252:255]\n" \
"v_mfma_f32_16x16x32_bf16 a[248:251], v[120:123], v[176:179], 0\n" \
"v_mfma_f32_16x16x32_bf16 a[248:251], v[124:127], v[180:183], a[248:251]\n" \
"v_mfma_f32_16x16x32_bf16 a[244:247], v[120:123], v[168:171], 0\n" \
"ds_read_b128 v[116:119], v49 offset:12288\n" \
"v_mfma_f32_16x16x32_bf16 a[244:247], v[124:127], v[172:175], a[244:247]\n" \
"v_mfma_f32_16x16x32_bf16 a[240:243], v[120:123], v[160:163], 0\n" \
"v_mfma_f32_16x16x32_bf16 a[240:243], v[124:127], v[164:167], a[240:243]\n" \
"s_waitcnt lgkmcnt(1)\n" \
"v_mfma_f32_16x16x32_bf16 a[0:3], v[128:131], v[64:67], a[0:3]\n" \
"ds_read_b128 v[120:123], v48 offset:14336\n" \
"v_mfma_f32_16x16x32_bf16 a[0:3], v[132:135], v[68:71], a[0:3]\n" \
"v_mfma_f32_16x16x32_bf16 a[4:7], v[136:139], v[64:67], a[4:7]\n" \
"ds_read_b128 v[124:127], v49 offset:14336\n" \
"v_mfma_f32_16x16x32_bf16 a[4:7], v[140:143], v[68:71], a[4:7]\n" \
"s_add_u32 s68, s66, 0x100000\n" \
"v_mfma_f32_16x16x32_bf16 a[8:11], v[144:147], v[64:67], a[8:11]\n" \
"s_add_u32 s64, s10, s68\n" \
"s_addc_u32 s65, s11, 0\n" \
"ds_read_b64_tr_b16 v[160:161], v50 offset:1024\n" \
"v_mfma_f32_16x16x32_bf16 a[8:11], v[148:151], v[68:71], a[8:11]\n" \
"global_load_dword v253, v248, s[64:65]\n" \
"v_mfma_f32_16x16x32_bf16 a[12:15], v[152:155], v[64:67], a[12:15]\n" \
"s_add_u32 m0, s87, 4096\n" \
"ds_read_b64_tr_b16 v[162:163], v50 offset:5120\n" \
"v_mfma_f32_16x16x32_bf16 a[12:15], v[156:159], v[68:71], a[12:15]\n" \
"global_load_lds_dwordx4 v40, s[52:53] offset:-4096\n" \
"v_mfma_f32_16x16x32_bf16 a[44:47], v[152:155], v[72:75], a[44:47]\n" \
"ds_read_b64_tr_b16 v[164:165], v50 offset:17408\n" \
"v_mfma_f32_16x16x32_bf16 a[44:47], v[156:159], v[76:79], a[44:47]\n" \
"v_mfma_f32_16x16x32_bf16 a[40:43], v[144:147], v[72:75], a[40:43]\n" \
"ds_read_b64_tr_b16 v[166:167], v50 offset:21504\n" \
"v_mfma_f32_16x16x32_bf16 a[40:43], v[148:151], v[76:79], a[40:43]\n" \
"global_load_lds_dwordx4 v41, s[50:51] offset:-3072\n" \
"v_mfma_f32_16x16x32_bf16 a[36:39], v[136:139], v[72:75], a[36:39]\n" \
"ds_read_b64_tr_b16 v[168:169], v51 offset:1024\n" \
"v_mfma_f32_16x16x32_bf16 a[36:39], v[140:143], v[76:79], a[36:39]\n" \
"v_mfma_f32_16x16x32_bf16 a[32:35], v[128:131], v[72:75], a[32:35]\n" \
"ds_read_b64_tr_b16 v[170:171], v51 offset:5120\n" \
"v_mfma_f32_16x16x32_bf16 a[32:35], v[132:135], v[76:79], a[32:35]\n" \
"global_load_lds_dwordx4 v42, s[52:53] offset:-2048\n" \
"v_mfma_f32_16x16x32_bf16 a[64:67], v[128:131], v[80:83], a[64:67]\n" \
"ds_read_b64_tr_b16 v[172:173], v51 offset:17408\n" \
"v_mfma_f32_16x16x32_bf16 a[64:67], v[132:135], v[84:87], a[64:67]\n" \
"v_mfma_f32_16x16x32_bf16 a[68:71], v[136:139], v[80:83], a[68:71]\n" \
"ds_read_b64_tr_b16 v[174:175], v51 offset:21504\n" \
"v_mfma_f32_16x16x32_bf16 a[68:71], v[140:143], v[84:87], a[68:71]\n" \
"global_load_lds_dwordx4 v43, s[50:51] offset:-1024\n" \
"v_mfma_f32_16x16x32_bf16 a[72:75], v[144:147], v[80:83], a[72:75]\n" \
"ds_read_b64_tr_b16 v[176:177], v50 offset:1536\n" \
"v_mfma_f32_16x16x32_bf16 a[72:75], v[148:151], v[84:87], a[72:75]\n" \
"v_mfma_f32_16x16x32_bf16 a[76:79], v[152:155], v[80:83], a[76:79]\n" \
"ds_read_b64_tr_b16 v[178:179], v50 offset:5632\n" \
"v_mfma_f32_16x16x32_bf16 a[76:79], v[156:159], v[84:87], a[76:79]\n" \
"global_load_lds_dwordx4 v44, s[52:53] offset:0\n" \
"v_mfma_f32_16x16x32_bf16 a[108:111], v[152:155], v[88:91], a[108:111]\n" \
"ds_read_b64_tr_b16 v[180:181], v50 offset:17920\n" \
"v_mfma_f32_16x16x32_bf16 a[108:111], v[156:159], v[92:95], a[108:111]\n" \
"v_mfma_f32_16x16x32_bf16 a[104:107], v[144:147], v[88:91], a[104:107]\n" \
"ds_read_b64_tr_b16 v[182:183], v50 offset:22016\n" \
"v_mfma_f32_16x16x32_bf16 a[104:107], v[148:151], v[92:95], a[104:107]\n" \
"global_load_lds_dwordx4 v45, s[50:51] offset:1024\n" \
"v_mfma_f32_16x16x32_bf16 a[100:103], v[136:139], v[88:91], a[100:103]\n" \
"ds_read_b64_tr_b16 v[184:185], v51 offset:1536\n" \
"v_mfma_f32_16x16x32_bf16 a[100:103], v[140:143], v[92:95], a[100:103]\n" \
"v_mfma_f32_16x16x32_bf16 a[96:99], v[128:131], v[88:91], a[96:99]\n" \
"ds_read_b64_tr_b16 v[186:187], v51 offset:5632\n" \
"v_mfma_f32_16x16x32_bf16 a[96:99], v[132:135], v[92:95], a[96:99]\n" \
"global_load_lds_dwordx4 v46, s[52:53] offset:2048\n" \
"v_mfma_f32_16x16x32_bf16 a[128:131], v[128:131], v[96:99], a[128:131]\n" \
"ds_read_b64_tr_b16 v[188:189], v51 offset:17920\n" \
"v_mfma_f32_16x16x32_bf16 a[128:131], v[132:135], v[100:103], a[128:131]\n" \
"v_mfma_f32_16x16x32_bf16 a[132:135], v[136:139], v[96:99], a[132:135]\n" \
"ds_read_b64_tr_b16 v[190:191], v51 offset:22016\n" \
"v_mfma_f32_16x16x32_bf16 a[132:135], v[140:143], v[100:103], a[132:135]\n" \
"global_load_lds_dwordx4 v47, s[50:51] offset:3072\n" \
"v_mfma_f32_16x16x32_bf16 a[136:139], v[144:147], v[96:99], a[136:139]\n" \
"s_add_u32 s52, s52, s74\n" \
"s_addc_u32 s53, s53, 0\n" \
"v_mfma_f32_16x16x32_bf16 a[136:139], v[148:151], v[100:103], a[136:139]\n" \
"s_add_u32 s50, s50, s74\n" \
"s_addc_u32 s51, s51, 0\n" \
"v_mfma_f32_16x16x32_bf16 a[140:143], v[152:155], v[96:99], a[140:143]\n" \
"v_mfma_f32_16x16x32_bf16 a[140:143], v[156:159], v[100:103], a[140:143]\n" \
"v_mfma_f32_16x16x32_bf16 a[172:175], v[152:155], v[104:107], a[172:175]\n" \
"s_mul_i32 s76, s73, 0x0\n" \
"s_add_u32 s77, s76, 0xffff8000\n" \
"s_sub_u32 s78, 0xffff8000, s76\n" \
"v_mfma_f32_16x16x32_bf16 a[172:175], v[156:159], v[108:111], a[172:175]\n" \
"v_add_u32 v48, s77, v48\n" \
"v_add_u32 v49, s77, v49\n" \
"v_mfma_f32_16x16x32_bf16 a[168:171], v[144:147], v[104:107], a[168:171]\n" \
"v_add_u32 v50, s78, v50\n" \
"v_add_u32 v51, s78, v51\n" \
"v_mfma_f32_16x16x32_bf16 a[168:171], v[148:151], v[108:111], a[168:171]\n" \
"v_mfma_f32_16x16x32_bf16 a[164:167], v[136:139], v[104:107], a[164:167]\n" \
"v_mfma_f32_16x16x32_bf16 a[164:167], v[140:143], v[108:111], a[164:167]\n" \
"v_mfma_f32_16x16x32_bf16 a[160:163], v[128:131], v[104:107], a[160:163]\n" \
"v_mfma_f32_16x16x32_bf16 a[160:163], v[132:135], v[108:111], a[160:163]\n" \
"s_waitcnt lgkmcnt(0)\n" \
"v_mfma_f32_16x16x32_bf16 a[192:195], v[128:131], v[112:115], a[192:195]\n" \
"v_mfma_f32_16x16x32_bf16 a[192:195], v[132:135], v[116:119], a[192:195]\n" \
"v_mfma_f32_16x16x32_bf16 a[196:199], v[136:139], v[112:115], a[196:199]\n" \
"v_mfma_f32_16x16x32_bf16 a[196:199], v[140:143], v[116:119], a[196:199]\n" \
"v_mfma_f32_16x16x32_bf16 a[200:203], v[144:147], v[112:115], a[200:203]\n" \
"v_mfma_f32_16x16x32_bf16 a[200:203], v[148:151], v[116:119], a[200:203]\n" \
"v_mfma_f32_16x16x32_bf16 a[204:207], v[152:155], v[112:115], a[204:207]\n" \
"v_mfma_f32_16x16x32_bf16 a[204:207], v[156:159], v[116:119], a[204:207]\n" \
"v_mfma_f32_16x16x32_bf16 a[236:239], v[152:155], v[120:123], a[236:239]\n" \
"v_mfma_f32_16x16x32_bf16 a[236:239], v[156:159], v[124:127], a[236:239]\n" \
"Lpg609_%=:\n" \
"Lst0x0_%=:\n" \
"s_cbranch_execz Lpg2767_%=\n" \
"v_mfma_f32_16x16x32_bf16 a[232:235], v[144:147], v[120:123], a[232:235]\n" \
"v_mfma_f32_16x16x32_bf16 a[232:235], v[148:151], v[124:127], a[232:235]\n" \
"v_mfma_f32_16x16x32_bf16 a[228:231], v[136:139], v[120:123], a[228:231]\n" \
"v_mfma_f32_16x16x32_bf16 a[228:231], v[140:143], v[124:127], a[228:231]\n" \
"v_mfma_f32_16x16x32_bf16 a[224:227], v[128:131], v[120:123], a[224:227]\n" \
"s_waitcnt vmcnt(10) lgkmcnt(0)\n" \
"v_mfma_f32_16x16x32_bf16 a[224:227], v[132:135], v[124:127], a[224:227]\n" \
"s_barrier\n" \
"v_mfma_f32_16x16x32_bf16 a[16:19], v[64:67], v[160:163], a[16:19]\n" \
"s_add_u32 m0, s87, 135168\n" \
"ds_read_b64_tr_b16 v[128:129], v50 offset:0\n" \
"v_readfirstlane_b32 s60, v244\n" \
"v_mfma_f32_16x16x32_bf16 a[16:19], v[68:71], v[164:167], a[16:19]\n" \
"s_add_u32 s60, s60, 0x9e3779b9\n" \
"global_load_lds_dwordx4 v32, s[54:55] offset:-4096\n" \
"v_mfma_f32_16x16x32_bf16 a[20:23], v[64:67], v[168:171], a[20:23]\n" \
"s_mul_i32 s88, s62, 557824\n" \
"s_mul_i32 s89, s63, 17408\n" \
"ds_read_b64_tr_b16 v[130:131], v50 offset:4096\n" \
"v_mfma_f32_16x16x32_bf16 a[20:23], v[68:71], v[172:175], a[20:23]\n" \
"s_add_u32 s88, s88, s89\n" \
"s_add_u32 s88, s88, s95\n" \
"s_add_u32 s89, s88, 17850368\n" \
"v_mfma_f32_16x16x32_bf16 a[24:27], v[64:67], v[176:179], a[24:27]\n" \
"s_lshl_b32 s91, s62, 7\n" \
"s_add_u32 s91, s91, 65585152\n" \
"ds_read_b64_tr_b16 v[132:133], v50 offset:16384\n" \
"v_mfma_f32_16x16x32_bf16 a[24:27], v[68:71], v[180:183], a[24:27]\n" \
"s_mov_b32 s92, 0x3e0293ee\n" \
"global_load_lds_dwordx4 v33, s[40:41] offset:-3072\n" \
"v_mfma_f32_16x16x32_bf16 a[28:31], v[64:67], v[184:187], a[28:31]\n" \
"ds_read_b64_tr_b16 v[134:135], v50 offset:20480\n" \
"v_mfma_f32_16x16x32_bf16 a[28:31], v[68:71], v[188:191], a[28:31]\n" \
"v_mfma_f32_16x16x32_bf16 a[60:63], v[72:75], v[184:187], a[60:63]\n" \
"ds_read_b128 v[64:67], v48 offset:0\n" \
"v_mfma_f32_16x16x32_bf16 a[60:63], v[76:79], v[188:191], a[60:63]\n" \
"global_load_lds_dwordx4 v34, s[54:55] offset:-2048\n" \
"v_mfma_f32_16x16x32_bf16 a[56:59], v[72:75], v[176:179], a[56:59]\n" \
"ds_read_b64_tr_b16 v[136:137], v51 offset:0\n" \
"v_mfma_f32_16x16x32_bf16 a[56:59], v[76:79], v[180:183], a[56:59]\n" \
"v_mfma_f32_16x16x32_bf16 a[52:55], v[72:75], v[168:171], a[52:55]\n" \
"ds_read_b128 v[68:71], v49 offset:0\n" \
"v_mfma_f32_16x16x32_bf16 a[52:55], v[76:79], v[172:175], a[52:55]\n" \
"global_load_lds_dwordx4 v35, s[40:41] offset:-1024\n" \
"v_mfma_f32_16x16x32_bf16 a[48:51], v[72:75], v[160:163], a[48:51]\n" \
"ds_read_b64_tr_b16 v[138:139], v51 offset:4096\n" \
"v_mfma_f32_16x16x32_bf16 a[48:51], v[76:79], v[164:167], a[48:51]\n" \
"v_mfma_f32_16x16x32_bf16 a[80:83], v[80:83], v[160:163], a[80:83]\n" \
"ds_read_b128 v[72:75], v48 offset:2048\n" \
"v_mfma_f32_16x16x32_bf16 a[80:83], v[84:87], v[164:167], a[80:83]\n" \
"global_load_lds_dwordx4 v36, s[54:55] offset:0\n" \
"v_mfma_f32_16x16x32_bf16 a[84:87], v[80:83], v[168:171], a[84:87]\n" \
"ds_read_b64_tr_b16 v[140:141], v51 offset:16384\n" \
"v_mfma_f32_16x16x32_bf16 a[84:87], v[84:87], v[172:175], a[84:87]\n" \
"v_mfma_f32_16x16x32_bf16 a[88:91], v[80:83], v[176:179], a[88:91]\n" \
"ds_read_b128 v[76:79], v49 offset:2048\n" \
"v_mfma_f32_16x16x32_bf16 a[88:91], v[84:87], v[180:183], a[88:91]\n" \
"global_load_lds_dwordx4 v37, s[40:41] offset:1024\n" \
"v_mfma_f32_16x16x32_bf16 a[92:95], v[80:83], v[184:187], a[92:95]\n" \
"ds_read_b64_tr_b16 v[142:143], v51 offset:20480\n" \
"v_mfma_f32_16x16x32_bf16 a[92:95], v[84:87], v[188:191], a[92:95]\n" \
"v_mfma_f32_16x16x32_bf16 a[124:127], v[88:91], v[184:187], a[124:127]\n" \
"ds_read_b128 v[80:83], v48 offset:4096\n" \
"v_mfma_f32_16x16x32_bf16 a[124:127], v[92:95], v[188:191], a[124:127]\n" \
"global_load_lds_dwordx4 v38, s[54:55] offset:2048\n" \
"v_mfma_f32_16x16x32_bf16 a[120:123], v[88:91], v[176:179], a[120:123]\n" \
"ds_read_b64_tr_b16 v[144:145], v50 offset:512\n" \
"v_mfma_f32_16x16x32_bf16 a[120:123], v[92:95], v[180:183], a[120:123]\n" \
"v_mfma_f32_16x16x32_bf16 a[116:119], v[88:91], v[168:171], a[116:119]\n" \
"ds_read_b128 v[84:87], v49 offset:4096\n" \
"global_load_lds_dwordx4 v39, s[40:41] offset:3072\n" \
"v_mfma_f32_16x16x32_bf16 a[116:119], v[92:95], v[172:175], a[116:119]\n" \
"s_add_u32 s54, s54, s75\n" \
"s_addc_u32 s55, s55, 0\n" \
"v_mfma_f32_16x16x32_bf16 a[112:115], v[88:91], v[160:163], a[112:115]\n" \
"s_add_u32 s40, s40, s75\n" \
"s_addc_u32 s41, s41, 0\n" \
"ds_read_b64_tr_b16 v[146:147], v50 offset:4608\n" \
"v_mfma_f32_16x16x32_bf16 a[112:115], v[92:95], v[164:167], a[112:115]\n" \
"s_add_u32 s68, s67, 0x200000\n" \
"s_add_u32 s64, s14, s68\n" \
"s_addc_u32 s65, s15, 0\n" \
"v_mfma_f32_16x16x32_bf16 a[144:147], v[96:99], v[160:163], a[144:147]\n" \
"ds_read_b128 v[88:91], v48 offset:6144\n" \
"v_mfma_f32_16x16x32_bf16 a[144:147], v[100:103], v[164:167], a[144:147]\n" \
"global_load_dword v253, v248, s[64:65]\n" \
"v_mfma_f32_16x16x32_bf16 a[148:151], v[96:99], v[168:171], a[148:151]\n" \
"s_add_u32 s68, s67, 0x200000\n" \
"ds_read_b64_tr_b16 v[148:149], v50 offset:16896\n" \
"v_mfma_f32_16x16x32_bf16 a[148:151], v[100:103], v[172:175], a[148:151]\n" \
"s_add_u32 s64, s16, s68\n" \
"s_addc_u32 s65, s17, 0\n" \
"v_mfma_f32_16x16x32_bf16 a[152:155], v[96:99], v[176:179], a[152:155]\n" \
"ds_read_b128 v[92:95], v49 offset:6144\n" \
"v_mfma_f32_16x16x32_bf16 a[152:155], v[100:103], v[180:183], a[152:155]\n" \
"global_load_dword v253, v248, s[64:65]\n" \
"v_mfma_f32_16x16x32_bf16 a[156:159], v[96:99], v[184:187], a[156:159]\n" \
"ds_read_b64_tr_b16 v[150:151], v50 offset:20992\n" \
"v_mfma_f32_16x16x32_bf16 a[156:159], v[100:103], v[188:191], a[156:159]\n" \
"v_mfma_f32_16x16x32_bf16 a[188:191], v[104:107], v[184:187], a[188:191]\n" \
"ds_read_b128 v[96:99], v48 offset:8192\n" \
"v_mfma_f32_16x16x32_bf16 a[188:191], v[108:111], v[188:191], a[188:191]\n" \
"v_mfma_f32_16x16x32_bf16 a[184:187], v[104:107], v[176:179], a[184:187]\n" \
"ds_read_b64_tr_b16 v[152:153], v51 offset:512\n" \
"v_mfma_f32_16x16x32_bf16 a[184:187], v[108:111], v[180:183], a[184:187]\n" \
"v_mfma_f32_16x16x32_bf16 a[180:183], v[104:107], v[168:171], a[180:183]\n" \
"ds_read_b128 v[100:103], v49 offset:8192\n" \
"v_mfma_f32_16x16x32_bf16 a[180:183], v[108:111], v[172:175], a[180:183]\n" \
"v_mfma_f32_16x16x32_bf16 a[176:179], v[104:107], v[160:163], a[176:179]\n" \
"ds_read_b64_tr_b16 v[154:155], v51 offset:4608\n" \
"v_mfma_f32_16x16x32_bf16 a[176:179], v[108:111], v[164:167], a[176:179]\n" \
"v_mfma_f32_16x16x32_bf16 a[208:211], v[112:115], v[160:163], a[208:211]\n" \
"ds_read_b128 v[104:107], v48 offset:10240\n" \
"v_mfma_f32_16x16x32_bf16 a[208:211], v[116:119], v[164:167], a[208:211]\n" \
"v_mfma_f32_16x16x32_bf16 a[212:215], v[112:115], v[168:171], a[212:215]\n" \
"ds_read_b64_tr_b16 v[156:157], v51 offset:16896\n" \
"v_mfma_f32_16x16x32_bf16 a[212:215], v[116:119], v[172:175], a[212:215]\n" \
"v_mfma_f32_16x16x32_bf16 a[216:219], v[112:115], v[176:179], a[216:219]\n" \
"ds_read_b128 v[108:111], v49 offset:10240\n" \
"v_mfma_f32_16x16x32_bf16 a[216:219], v[116:119], v[180:183], a[216:219]\n" \
"v_mfma_f32_16x16x32_bf16 a[220:223], v[112:115], v[184:187], a[220:223]\n" \
"ds_read_b64_tr_b16 v[158:159], v51 offset:20992\n" \
"v_mfma_f32_16x16x32_bf16 a[220:223], v[116:119], v[188:191], a[220:223]\n" \
"v_mfma_f32_16x16x32_bf16 a[252:255], v[120:123], v[184:187], a[252:255]\n" \
"ds_read_b128 v[112:115], v48 offset:12288\n" \
"v_mfma_f32_16x16x32_bf16 a[252:255], v[124:127], v[188:191], a[252:255]\n" \
"v_mfma_f32_16x16x32_bf16 a[248:251], v[120:123], v[176:179], a[248:251]\n" \
"v_mfma_f32_16x16x32_bf16 a[248:251], v[124:127], v[180:183], a[248:251]\n" \
"v_mfma_f32_16x16x32_bf16 a[244:247], v[120:123], v[168:171], a[244:247]\n" \
"ds_read_b128 v[116:119], v49 offset:12288\n" \
"v_mfma_f32_16x16x32_bf16 a[244:247], v[124:127], v[172:175], a[244:247]\n" \
"v_mfma_f32_16x16x32_bf16 a[240:243], v[120:123], v[160:163], a[240:243]\n" \
"v_mfma_f32_16x16x32_bf16 a[240:243], v[124:127], v[164:167], a[240:243]\n" \
"s_waitcnt lgkmcnt(1)\n" \
"v_mfma_f32_16x16x32_bf16 a[0:3], v[128:131], v[64:67], a[0:3]\n" \
"ds_read_b128 v[120:123], v48 offset:14336\n" \
"v_mfma_f32_16x16x32_bf16 a[0:3], v[132:135], v[68:71], a[0:3]\n" \
"v_mfma_f32_16x16x32_bf16 a[4:7], v[136:139], v[64:67], a[4:7]\n" \
"ds_read_b128 v[124:127], v49 offset:14336\n" \
"v_mfma_f32_16x16x32_bf16 a[4:7], v[140:143], v[68:71], a[4:7]\n" \
"s_add_u32 s68, s67, 0x300000\n" \
"v_mfma_f32_16x16x32_bf16 a[8:11], v[144:147], v[64:67], a[8:11]\n" \
"s_add_u32 s64, s14, s68\n" \
"s_addc_u32 s65, s15, 0\n" \
"ds_read_b64_tr_b16 v[160:161], v50 offset:1024\n" \
"v_mfma_f32_16x16x32_bf16 a[8:11], v[148:151], v[68:71], a[8:11]\n" \
"global_load_dword v253, v248, s[64:65]\n" \
"v_mfma_f32_16x16x32_bf16 a[12:15], v[152:155], v[64:67], a[12:15]\n" \
"s_add_u32 m0, s87, 69632\n" \
"ds_read_b64_tr_b16 v[162:163], v50 offset:5120\n" \
"v_mfma_f32_16x16x32_bf16 a[12:15], v[156:159], v[68:71], a[12:15]\n" \
"global_load_lds_dwordx4 v40, s[52:53] offset:-4096\n" \
"v_mfma_f32_16x16x32_bf16 a[44:47], v[152:155], v[72:75], a[44:47]\n" \
"ds_read_b64_tr_b16 v[164:165], v50 offset:17408\n" \
"v_mfma_f32_16x16x32_bf16 a[44:47], v[156:159], v[76:79], a[44:47]\n" \
"v_mfma_f32_16x16x32_bf16 a[40:43], v[144:147], v[72:75], a[40:43]\n" \
"ds_read_b64_tr_b16 v[166:167], v50 offset:21504\n" \
"v_mfma_f32_16x16x32_bf16 a[40:43], v[148:151], v[76:79], a[40:43]\n" \
"global_load_lds_dwordx4 v41, s[50:51] offset:-3072\n" \
"v_mfma_f32_16x16x32_bf16 a[36:39], v[136:139], v[72:75], a[36:39]\n" \
"ds_read_b64_tr_b16 v[168:169], v51 offset:1024\n" \
"v_mfma_f32_16x16x32_bf16 a[36:39], v[140:143], v[76:79], a[36:39]\n" \
"v_mfma_f32_16x16x32_bf16 a[32:35], v[128:131], v[72:75], a[32:35]\n" \
"ds_read_b64_tr_b16 v[170:171], v51 offset:5120\n" \
"v_mfma_f32_16x16x32_bf16 a[32:35], v[132:135], v[76:79], a[32:35]\n" \
"global_load_lds_dwordx4 v42, s[52:53] offset:-2048\n" \
"v_mfma_f32_16x16x32_bf16 a[64:67], v[128:131], v[80:83], a[64:67]\n" \
"ds_read_b64_tr_b16 v[172:173], v51 offset:17408\n" \
"v_mfma_f32_16x16x32_bf16 a[64:67], v[132:135], v[84:87], a[64:67]\n" \
"v_mfma_f32_16x16x32_bf16 a[68:71], v[136:139], v[80:83], a[68:71]\n" \
"ds_read_b64_tr_b16 v[174:175], v51 offset:21504\n" \
"v_mfma_f32_16x16x32_bf16 a[68:71], v[140:143], v[84:87], a[68:71]\n" \
"global_load_lds_dwordx4 v43, s[50:51] offset:-1024\n" \
"v_mfma_f32_16x16x32_bf16 a[72:75], v[144:147], v[80:83], a[72:75]\n" \
"ds_read_b64_tr_b16 v[176:177], v50 offset:1536\n" \
"v_mfma_f32_16x16x32_bf16 a[72:75], v[148:151], v[84:87], a[72:75]\n" \
"v_mfma_f32_16x16x32_bf16 a[76:79], v[152:155], v[80:83], a[76:79]\n" \
"ds_read_b64_tr_b16 v[178:179], v50 offset:5632\n" \
"v_mfma_f32_16x16x32_bf16 a[76:79], v[156:159], v[84:87], a[76:79]\n" \
"global_load_lds_dwordx4 v44, s[52:53] offset:0\n" \
"v_mfma_f32_16x16x32_bf16 a[108:111], v[152:155], v[88:91], a[108:111]\n" \
"ds_read_b64_tr_b16 v[180:181], v50 offset:17920\n" \
"v_mfma_f32_16x16x32_bf16 a[108:111], v[156:159], v[92:95], a[108:111]\n" \
"v_mfma_f32_16x16x32_bf16 a[104:107], v[144:147], v[88:91], a[104:107]\n" \
"ds_read_b64_tr_b16 v[182:183], v50 offset:22016\n" \
"v_mfma_f32_16x16x32_bf16 a[104:107], v[148:151], v[92:95], a[104:107]\n" \
"global_load_lds_dwordx4 v45, s[50:51] offset:1024\n" \
"v_mfma_f32_16x16x32_bf16 a[100:103], v[136:139], v[88:91], a[100:103]\n" \
"ds_read_b64_tr_b16 v[184:185], v51 offset:1536\n" \
"v_mfma_f32_16x16x32_bf16 a[100:103], v[140:143], v[92:95], a[100:103]\n" \
"v_mfma_f32_16x16x32_bf16 a[96:99], v[128:131], v[88:91], a[96:99]\n" \
"ds_read_b64_tr_b16 v[186:187], v51 offset:5632\n" \
"v_mfma_f32_16x16x32_bf16 a[96:99], v[132:135], v[92:95], a[96:99]\n" \
"global_load_lds_dwordx4 v46, s[52:53] offset:2048\n" \
"v_mfma_f32_16x16x32_bf16 a[128:131], v[128:131], v[96:99], a[128:131]\n" \
"ds_read_b64_tr_b16 v[188:189], v51 offset:17920\n" \
"v_mfma_f32_16x16x32_bf16 a[128:131], v[132:135], v[100:103], a[128:131]\n" \
"v_mfma_f32_16x16x32_bf16 a[132:135], v[136:139], v[96:99], a[132:135]\n" \
"ds_read_b64_tr_b16 v[190:191], v51 offset:22016\n" \
"v_mfma_f32_16x16x32_bf16 a[132:135], v[140:143], v[100:103], a[132:135]\n" \
"global_load_lds_dwordx4 v47, s[50:51] offset:3072\n" \
"v_mfma_f32_16x16x32_bf16 a[136:139], v[144:147], v[96:99], a[136:139]\n" \
"s_add_u32 s52, s52, s74\n" \
"s_addc_u32 s53, s53, 0\n" \
"v_mfma_f32_16x16x32_bf16 a[136:139], v[148:151], v[100:103], a[136:139]\n" \
"s_add_u32 s50, s50, s74\n" \
"s_addc_u32 s51, s51, 0\n" \
"v_mfma_f32_16x16x32_bf16 a[140:143], v[152:155], v[96:99], a[140:143]\n" \
"v_mfma_f32_16x16x32_bf16 a[140:143], v[156:159], v[100:103], a[140:143]\n" \
"v_mfma_f32_16x16x32_bf16 a[172:175], v[152:155], v[104:107], a[172:175]\n" \
"s_mul_i32 s76, s73, 0xfffd0000\n" \
"s_add_u32 s77, s76, 0x18000\n" \
"s_sub_u32 s78, 0xfffe8000, s76\n" \
"v_mfma_f32_16x16x32_bf16 a[172:175], v[156:159], v[108:111], a[172:175]\n" \
"v_add_u32 v48, s77, v48\n" \
"v_add_u32 v49, s77, v49\n" \
"v_mfma_f32_16x16x32_bf16 a[168:171], v[144:147], v[104:107], a[168:171]\n" \
"v_add_u32 v50, s78, v50\n" \
"v_add_u32 v51, s78, v51\n" \
"v_mfma_f32_16x16x32_bf16 a[168:171], v[148:151], v[108:111], a[168:171]\n" \
"v_mfma_f32_16x16x32_bf16 a[164:167], v[136:139], v[104:107], a[164:167]\n" \
"v_mfma_f32_16x16x32_bf16 a[164:167], v[140:143], v[108:111], a[164:167]\n" \
"v_mfma_f32_16x16x32_bf16 a[160:163], v[128:131], v[104:107], a[160:163]\n" \
"v_mfma_f32_16x16x32_bf16 a[160:163], v[132:135], v[108:111], a[160:163]\n" \
"s_waitcnt lgkmcnt(0)\n" \
"v_mfma_f32_16x16x32_bf16 a[192:195], v[128:131], v[112:115], a[192:195]\n" \
"v_mfma_f32_16x16x32_bf16 a[192:195], v[132:135], v[116:119], a[192:195]\n" \
"v_mfma_f32_16x16x32_bf16 a[196:199], v[136:139], v[112:115], a[196:199]\n" \
"v_mfma_f32_16x16x32_bf16 a[196:199], v[140:143], v[116:119], a[196:199]\n" \
"v_mfma_f32_16x16x32_bf16 a[200:203], v[144:147], v[112:115], a[200:203]\n" \
"v_mfma_f32_16x16x32_bf16 a[200:203], v[148:151], v[116:119], a[200:203]\n" \
"v_mfma_f32_16x16x32_bf16 a[204:207], v[152:155], v[112:115], a[204:207]\n" \
"v_mfma_f32_16x16x32_bf16 a[204:207], v[156:159], v[116:119], a[204:207]\n" \
"v_mfma_f32_16x16x32_bf16 a[236:239], v[152:155], v[120:123], a[236:239]\n" \
"v_mfma_f32_16x16x32_bf16 a[236:239], v[156:159], v[124:127], a[236:239]\n" \
"v_mfma_f32_16x16x32_bf16 a[232:235], v[144:147], v[120:123], a[232:235]\n" \
"v_mfma_f32_16x16x32_bf16 a[232:235], v[148:151], v[124:127], a[232:235]\n" \
"v_mfma_f32_16x16x32_bf16 a[228:231], v[136:139], v[120:123], a[228:231]\n" \
"v_mfma_f32_16x16x32_bf16 a[228:231], v[140:143], v[124:127], a[228:231]\n" \
"v_mfma_f32_16x16x32_bf16 a[224:227], v[128:131], v[120:123], a[224:227]\n" \
"s_waitcnt vmcnt(11) lgkmcnt(0)\n" \
"v_mfma_f32_16x16x32_bf16 a[224:227], v[132:135], v[124:127], a[224:227]\n" \
"s_barrier\n" \
"v_mfma_f32_16x16x32_bf16 a[16:19], v[64:67], v[160:163], a[16:19]\n" \
"s_add_u32 m0, s87, 102400\n" \
"ds_read_b64_tr_b16 v[128:129], v50 offset:0\n" \
"v_mfma_f32_16x16x32_bf16 a[16:19], v[68:71], v[164:167], a[16:19]\n" \
"global_load_lds_dwordx4 v32, s[54:55] offset:-4096\n" \
"v_mfma_f32_16x16x32_bf16 a[20:23], v[64:67], v[168:171], a[20:23]\n" \
"ds_read_b64_tr_b16 v[130:131], v50 offset:4096\n" \
"v_mfma_f32_16x16x32_bf16 a[20:23], v[68:71], v[172:175], a[20:23]\n" \
"v_mfma_f32_16x16x32_bf16 a[24:27], v[64:67], v[176:179], a[24:27]\n" \
"ds_read_b64_tr_b16 v[132:133], v50 offset:16384\n" \
"v_mfma_f32_16x16x32_bf16 a[24:27], v[68:71], v[180:183], a[24:27]\n" \
"global_load_lds_dwordx4 v33, s[40:41] offset:-3072\n" \
"v_mfma_f32_16x16x32_bf16 a[28:31], v[64:67], v[184:187], a[28:31]\n" \
"ds_read_b64_tr_b16 v[134:135], v50 offset:20480\n" \
"v_mfma_f32_16x16x32_bf16 a[28:31], v[68:71], v[188:191], a[28:31]\n" \
"v_mfma_f32_16x16x32_bf16 a[60:63], v[72:75], v[184:187], a[60:63]\n" \
"ds_read_b128 v[64:67], v48 offset:0\n" \
"v_mfma_f32_16x16x32_bf16 a[60:63], v[76:79], v[188:191], a[60:63]\n" \
"global_load_lds_dwordx4 v34, s[54:55] offset:-2048\n" \
"v_mfma_f32_16x16x32_bf16 a[56:59], v[72:75], v[176:179], a[56:59]\n" \
"ds_read_b64_tr_b16 v[136:137], v51 offset:0\n" \
"v_mfma_f32_16x16x32_bf16 a[56:59], v[76:79], v[180:183], a[56:59]\n" \
"v_mfma_f32_16x16x32_bf16 a[52:55], v[72:75], v[168:171], a[52:55]\n" \
"ds_read_b128 v[68:71], v49 offset:0\n" \
"v_mfma_f32_16x16x32_bf16 a[52:55], v[76:79], v[172:175], a[52:55]\n" \
"global_load_lds_dwordx4 v35, s[40:41] offset:-1024\n" \
"v_mfma_f32_16x16x32_bf16 a[48:51], v[72:75], v[160:163], a[48:51]\n" \
"ds_read_b64_tr_b16 v[138:139], v51 offset:4096\n" \
"v_mfma_f32_16x16x32_bf16 a[48:51], v[76:79], v[164:167], a[48:51]\n" \
"v_mfma_f32_16x16x32_bf16 a[80:83], v[80:83], v[160:163], a[80:83]\n" \
"ds_read_b128 v[72:75], v48 offset:2048\n" \
"v_mfma_f32_16x16x32_bf16 a[80:83], v[84:87], v[164:167], a[80:83]\n" \
"global_load_lds_dwordx4 v36, s[54:55] offset:0\n" \
"v_mfma_f32_16x16x32_bf16 a[84:87], v[80:83], v[168:171], a[84:87]\n" \
"ds_read_b64_tr_b16 v[140:141], v51 offset:16384\n" \
"v_mfma_f32_16x16x32_bf16 a[84:87], v[84:87], v[172:175], a[84:87]\n" \
"v_mfma_f32_16x16x32_bf16 a[88:91], v[80:83], v[176:179], a[88:91]\n" \
"ds_read_b128 v[76:79], v49 offset:2048\n" \
"v_mfma_f32_16x16x32_bf16 a[88:91], v[84:87], v[180:183], a[88:91]\n" \
"global_load_lds_dwordx4 v37, s[40:41] offset:1024\n" \
"v_mfma_f32_16x16x32_bf16 a[92:95], v[80:83], v[184:187], a[92:95]\n" \
"ds_read_b64_tr_b16 v[142:143], v51 offset:20480\n" \
"v_mfma_f32_16x16x32_bf16 a[92:95], v[84:87], v[188:191], a[92:95]\n" \
"v_mfma_f32_16x16x32_bf16 a[124:127], v[88:91], v[184:187], a[124:127]\n" \
"ds_read_b128 v[80:83], v48 offset:4096\n" \
"v_mfma_f32_16x16x32_bf16 a[124:127], v[92:95], v[188:191], a[124:127]\n" \
"global_load_lds_dwordx4 v38, s[54:55] offset:2048\n" \
"v_mfma_f32_16x16x32_bf16 a[120:123], v[88:91], v[176:179], a[120:123]\n" \
"ds_read_b64_tr_b16 v[144:145], v50 offset:512\n" \
"v_mfma_f32_16x16x32_bf16 a[120:123], v[92:95], v[180:183], a[120:123]\n" \
"v_mfma_f32_16x16x32_bf16 a[116:119], v[88:91], v[168:171], a[116:119]\n" \
"ds_read_b128 v[84:87], v49 offset:4096\n" \
"global_load_lds_dwordx4 v39, s[40:41] offset:3072\n" \
"v_mfma_f32_16x16x32_bf16 a[116:119], v[92:95], v[172:175], a[116:119]\n" \
"s_add_u32 s54, s54, s75\n" \
"s_addc_u32 s55, s55, 0\n" \
"v_mfma_f32_16x16x32_bf16 a[112:115], v[88:91], v[160:163], a[112:115]\n" \
"s_add_u32 s40, s40, s75\n" \
"s_addc_u32 s41, s41, 0\n" \
"ds_read_b64_tr_b16 v[146:147], v50 offset:4608\n" \
"v_mfma_f32_16x16x32_bf16 a[112:115], v[92:95], v[164:167], a[112:115]\n" \
"s_add_u32 s68, s67, 0x300000\n" \
"s_add_u32 s64, s16, s68\n" \
"s_addc_u32 s65, s17, 0\n" \
"v_mfma_f32_16x16x32_bf16 a[144:147], v[96:99], v[160:163], a[144:147]\n" \
"ds_read_b128 v[88:91], v48 offset:6144\n" \
"v_mfma_f32_16x16x32_bf16 a[144:147], v[100:103], v[164:167], a[144:147]\n" \
"global_load_dword v253, v248, s[64:65]\n" \
"v_mfma_f32_16x16x32_bf16 a[148:151], v[96:99], v[168:171], a[148:151]\n" \
"s_add_u32 s68, s66, 0x0\n" \
"ds_read_b64_tr_b16 v[148:149], v50 offset:16896\n" \
"v_mfma_f32_16x16x32_bf16 a[148:151], v[100:103], v[172:175], a[148:151]\n" \
"s_add_u32 s64, s8, s68\n" \
"s_addc_u32 s65, s9, 0\n" \
"v_mfma_f32_16x16x32_bf16 a[152:155], v[96:99], v[176:179], a[152:155]\n" \
"ds_read_b128 v[92:95], v49 offset:6144\n" \
"v_mfma_f32_16x16x32_bf16 a[152:155], v[100:103], v[180:183], a[152:155]\n" \
"global_load_dword v253, v248, s[64:65]\n" \
"v_mfma_f32_16x16x32_bf16 a[156:159], v[96:99], v[184:187], a[156:159]\n" \
"ds_read_b64_tr_b16 v[150:151], v50 offset:20992\n" \
"v_mfma_f32_16x16x32_bf16 a[156:159], v[100:103], v[188:191], a[156:159]\n" \
"v_mfma_f32_16x16x32_bf16 a[188:191], v[104:107], v[184:187], a[188:191]\n" \
"ds_read_b128 v[96:99], v48 offset:8192\n" \
"v_mfma_f32_16x16x32_bf16 a[188:191], v[108:111], v[188:191], a[188:191]\n" \
"v_mfma_f32_16x16x32_bf16 a[184:187], v[104:107], v[176:179], a[184:187]\n" \
"ds_read_b64_tr_b16 v[152:153], v51 offset:512\n" \
"v_mfma_f32_16x16x32_bf16 a[184:187], v[108:111], v[180:183], a[184:187]\n" \
"v_mfma_f32_16x16x32_bf16 a[180:183], v[104:107], v[168:171], a[180:183]\n" \
"ds_read_b128 v[100:103], v49 offset:8192\n" \
"v_mfma_f32_16x16x32_bf16 a[180:183], v[108:111], v[172:175], a[180:183]\n" \
"v_mfma_f32_16x16x32_bf16 a[176:179], v[104:107], v[160:163], a[176:179]\n" \
"ds_read_b64_tr_b16 v[154:155], v51 offset:4608\n" \
"v_mfma_f32_16x16x32_bf16 a[176:179], v[108:111], v[164:167], a[176:179]\n" \
"v_mfma_f32_16x16x32_bf16 a[208:211], v[112:115], v[160:163], a[208:211]\n" \
"ds_read_b128 v[104:107], v48 offset:10240\n" \
"v_mfma_f32_16x16x32_bf16 a[208:211], v[116:119], v[164:167], a[208:211]\n" \
"v_mfma_f32_16x16x32_bf16 a[212:215], v[112:115], v[168:171], a[212:215]\n" \
"ds_read_b64_tr_b16 v[156:157], v51 offset:16896\n" \
"v_mfma_f32_16x16x32_bf16 a[212:215], v[116:119], v[172:175], a[212:215]\n" \
"v_mfma_f32_16x16x32_bf16 a[216:219], v[112:115], v[176:179], a[216:219]\n" \
"ds_read_b128 v[108:111], v49 offset:10240\n" \
"v_mfma_f32_16x16x32_bf16 a[216:219], v[116:119], v[180:183], a[216:219]\n" \
"v_mfma_f32_16x16x32_bf16 a[220:223], v[112:115], v[184:187], a[220:223]\n" \
"ds_read_b64_tr_b16 v[158:159], v51 offset:20992\n" \
"v_mfma_f32_16x16x32_bf16 a[220:223], v[116:119], v[188:191], a[220:223]\n" \
"v_mfma_f32_16x16x32_bf16 a[252:255], v[120:123], v[184:187], a[252:255]\n" \
"ds_read_b128 v[112:115], v48 offset:12288\n" \
"v_mfma_f32_16x16x32_bf16 a[252:255], v[124:127], v[188:191], a[252:255]\n" \
"v_mfma_f32_16x16x32_bf16 a[248:251], v[120:123], v[176:179], a[248:251]\n" \
"v_mfma_f32_16x16x32_bf16 a[248:251], v[124:127], v[180:183], a[248:251]\n" \
"v_mfma_f32_16x16x32_bf16 a[244:247], v[120:123], v[168:171], a[244:247]\n" \
"ds_read_b128 v[116:119], v49 offset:12288\n" \
"v_mfma_f32_16x16x32_bf16 a[244:247], v[124:127], v[172:175], a[244:247]\n" \
"v_mfma_f32_16x16x32_bf16 a[240:243], v[120:123], v[160:163], a[240:243]\n" \
"v_mfma_f32_16x16x32_bf16 a[240:243], v[124:127], v[164:167], a[240:243]\n" \
"s_waitcnt lgkmcnt(1)\n" \
"v_mfma_f32_16x16x32_bf16 a[0:3], v[128:131], v[64:67], a[0:3]\n" \
"ds_read_b128 v[120:123], v48 offset:14336\n" \
"v_mfma_f32_16x16x32_bf16 a[0:3], v[132:135], v[68:71], a[0:3]\n" \
"v_mfma_f32_16x16x32_bf16 a[4:7], v[136:139], v[64:67], a[4:7]\n" \
"ds_read_b128 v[124:127], v49 offset:14336\n" \
"v_mfma_f32_16x16x32_bf16 a[4:7], v[140:143], v[68:71], a[4:7]\n" \
"s_add_u32 s68, s66, 0x100000\n" \
"v_mfma_f32_16x16x32_bf16 a[8:11], v[144:147], v[64:67], a[8:11]\n" \
"s_add_u32 s64, s8, s68\n" \
"s_addc_u32 s65, s9, 0\n" \
"ds_read_b64_tr_b16 v[160:161], v50 offset:1024\n" \
"v_mfma_f32_16x16x32_bf16 a[8:11], v[148:151], v[68:71], a[8:11]\n" \
"global_load_dword v253, v248, s[64:65]\n" \
"v_mfma_f32_16x16x32_bf16 a[12:15], v[152:155], v[64:67], a[12:15]\n" \
"s_add_u32 m0, s87, 36864\n" \
"ds_read_b64_tr_b16 v[162:163], v50 offset:5120\n" \
"v_mfma_f32_16x16x32_bf16 a[12:15], v[156:159], v[68:71], a[12:15]\n" \
"global_load_lds_dwordx4 v40, s[52:53] offset:-4096\n" \
"v_mfma_f32_16x16x32_bf16 a[44:47], v[152:155], v[72:75], a[44:47]\n" \
"ds_read_b64_tr_b16 v[164:165], v50 offset:17408\n" \
"v_mfma_f32_16x16x32_bf16 a[44:47], v[156:159], v[76:79], a[44:47]\n" \
"v_mfma_f32_16x16x32_bf16 a[40:43], v[144:147], v[72:75], a[40:43]\n" \
"ds_read_b64_tr_b16 v[166:167], v50 offset:21504\n" \
"v_mfma_f32_16x16x32_bf16 a[40:43], v[148:151], v[76:79], a[40:43]\n" \
"global_load_lds_dwordx4 v41, s[50:51] offset:-3072\n" \
"v_mfma_f32_16x16x32_bf16 a[36:39], v[136:139], v[72:75], a[36:39]\n" \
"ds_read_b64_tr_b16 v[168:169], v51 offset:1024\n" \
"v_mfma_f32_16x16x32_bf16 a[36:39], v[140:143], v[76:79], a[36:39]\n" \
"v_mfma_f32_16x16x32_bf16 a[32:35], v[128:131], v[72:75], a[32:35]\n" \
"ds_read_b64_tr_b16 v[170:171], v51 offset:5120\n" \
"v_mfma_f32_16x16x32_bf16 a[32:35], v[132:135], v[76:79], a[32:35]\n" \
"global_load_lds_dwordx4 v42, s[52:53] offset:-2048\n" \
"v_mfma_f32_16x16x32_bf16 a[64:67], v[128:131], v[80:83], a[64:67]\n" \
"ds_read_b64_tr_b16 v[172:173], v51 offset:17408\n" \
"v_mfma_f32_16x16x32_bf16 a[64:67], v[132:135], v[84:87], a[64:67]\n" \
"v_mfma_f32_16x16x32_bf16 a[68:71], v[136:139], v[80:83], a[68:71]\n" \
"ds_read_b64_tr_b16 v[174:175], v51 offset:21504\n" \
"v_mfma_f32_16x16x32_bf16 a[68:71], v[140:143], v[84:87], a[68:71]\n" \
"global_load_lds_dwordx4 v43, s[50:51] offset:-1024\n" \
"v_mfma_f32_16x16x32_bf16 a[72:75], v[144:147], v[80:83], a[72:75]\n" \
"ds_read_b64_tr_b16 v[176:177], v50 offset:1536\n" \
"v_mfma_f32_16x16x32_bf16 a[72:75], v[148:151], v[84:87], a[72:75]\n" \
"v_mfma_f32_16x16x32_bf16 a[76:79], v[152:155], v[80:83], a[76:79]\n" \
"ds_read_b64_tr_b16 v[178:179], v50 offset:5632\n" \
"v_mfma_f32_16x16x32_bf16 a[76:79], v[156:159], v[84:87], a[76:79]\n" \
"global_load_lds_dwordx4 v44, s[52:53] offset:0\n" \
"v_mfma_f32_16x16x32_bf16 a[108:111], v[152:155], v[88:91], a[108:111]\n" \
"ds_read_b64_tr_b16 v[180:181], v50 offset:17920\n" \
"v_mfma_f32_16x16x32_bf16 a[108:111], v[156:159], v[92:95], a[108:111]\n" \
"v_mfma_f32_16x16x32_bf16 a[104:107], v[144:147], v[88:91], a[104:107]\n" \
"ds_read_b64_tr_b16 v[182:183], v50 offset:22016\n" \
"v_mfma_f32_16x16x32_bf16 a[104:107], v[148:151], v[92:95], a[104:107]\n" \
"global_load_lds_dwordx4 v45, s[50:51] offset:1024\n" \
"v_mfma_f32_16x16x32_bf16 a[100:103], v[136:139], v[88:91], a[100:103]\n" \
"ds_read_b64_tr_b16 v[184:185], v51 offset:1536\n" \
"v_mfma_f32_16x16x32_bf16 a[100:103], v[140:143], v[92:95], a[100:103]\n" \
"v_mfma_f32_16x16x32_bf16 a[96:99], v[128:131], v[88:91], a[96:99]\n" \
"ds_read_b64_tr_b16 v[186:187], v51 offset:5632\n" \
"v_mfma_f32_16x16x32_bf16 a[96:99], v[132:135], v[92:95], a[96:99]\n" \
"global_load_lds_dwordx4 v46, s[52:53] offset:2048\n" \
"v_mfma_f32_16x16x32_bf16 a[128:131], v[128:131], v[96:99], a[128:131]\n" \
"ds_read_b64_tr_b16 v[188:189], v51 offset:17920\n" \
"v_mfma_f32_16x16x32_bf16 a[128:131], v[132:135], v[100:103], a[128:131]\n" \
"v_mfma_f32_16x16x32_bf16 a[132:135], v[136:139], v[96:99], a[132:135]\n" \
"ds_read_b64_tr_b16 v[190:191], v51 offset:22016\n" \
"v_mfma_f32_16x16x32_bf16 a[132:135], v[140:143], v[100:103], a[132:135]\n" \
"global_load_lds_dwordx4 v47, s[50:51] offset:3072\n" \
"v_mfma_f32_16x16x32_bf16 a[136:139], v[144:147], v[96:99], a[136:139]\n" \
"s_add_u32 s52, s52, s74\n" \
"s_addc_u32 s53, s53, 0\n" \
"v_mfma_f32_16x16x32_bf16 a[136:139], v[148:151], v[100:103], a[136:139]\n" \
"s_add_u32 s50, s50, s74\n" \
"s_addc_u32 s51, s51, 0\n" \
"v_mfma_f32_16x16x32_bf16 a[140:143], v[152:155], v[96:99], a[140:143]\n" \
"v_mfma_f32_16x16x32_bf16 a[140:143], v[156:159], v[100:103], a[140:143]\n" \
"v_mfma_f32_16x16x32_bf16 a[172:175], v[152:155], v[104:107], a[172:175]\n" \
"s_mul_i32 s76, s73, 0x18000\n" \
"s_add_u32 s77, s76, 0xffff8000\n" \
"s_sub_u32 s78, 0x10000, s76\n" \
"v_mfma_f32_16x16x32_bf16 a[172:175], v[156:159], v[108:111], a[172:175]\n" \
"v_add_u32 v48, s77, v48\n" \
"v_add_u32 v49, s77, v49\n" \
"v_mfma_f32_16x16x32_bf16 a[168:171], v[144:147], v[104:107], a[168:171]\n" \
"v_add_u32 v50, s78, v50\n" \
"v_add_u32 v51, s78, v51\n" \
"v_mfma_f32_16x16x32_bf16 a[168:171], v[148:151], v[108:111], a[168:171]\n" \
"v_mfma_f32_16x16x32_bf16 a[164:167], v[136:139], v[104:107], a[164:167]\n" \
"v_mfma_f32_16x16x32_bf16 a[164:167], v[140:143], v[108:111], a[164:167]\n" \
"v_mfma_f32_16x16x32_bf16 a[160:163], v[128:131], v[104:107], a[160:163]\n" \
"v_mfma_f32_16x16x32_bf16 a[160:163], v[132:135], v[108:111], a[160:163]\n" \
"s_waitcnt lgkmcnt(0)\n" \
"v_mfma_f32_16x16x32_bf16 a[192:195], v[128:131], v[112:115], a[192:195]\n" \
"v_mfma_f32_16x16x32_bf16 a[192:195], v[132:135], v[116:119], a[192:195]\n" \
"v_mfma_f32_16x16x32_bf16 a[196:199], v[136:139], v[112:115], a[196:199]\n" \
"v_mfma_f32_16x16x32_bf16 a[196:199], v[140:143], v[116:119], a[196:199]\n" \
"v_mfma_f32_16x16x32_bf16 a[200:203], v[144:147], v[112:115], a[200:203]\n" \
"v_mfma_f32_16x16x32_bf16 a[200:203], v[148:151], v[116:119], a[200:203]\n" \
"v_mfma_f32_16x16x32_bf16 a[204:207], v[152:155], v[112:115], a[204:207]\n" \
"v_mfma_f32_16x16x32_bf16 a[204:207], v[156:159], v[116:119], a[204:207]\n" \
"v_mfma_f32_16x16x32_bf16 a[236:239], v[152:155], v[120:123], a[236:239]\n" \
"v_mfma_f32_16x16x32_bf16 a[236:239], v[156:159], v[124:127], a[236:239]\n" \
"v_mfma_f32_16x16x32_bf16 a[232:235], v[144:147], v[120:123], a[232:235]\n" \
"v_mfma_f32_16x16x32_bf16 a[232:235], v[148:151], v[124:127], a[232:235]\n" \
"v_mfma_f32_16x16x32_bf16 a[228:231], v[136:139], v[120:123], a[228:231]\n" \
"v_mfma_f32_16x16x32_bf16 a[228:231], v[140:143], v[124:127], a[228:231]\n" \
"v_mfma_f32_16x16x32_bf16 a[224:227], v[128:131], v[120:123], a[224:227]\n" \
"s_waitcnt vmcnt(11) lgkmcnt(0)\n" \
"v_mfma_f32_16x16x32_bf16 a[224:227], v[132:135], v[124:127], a[224:227]\n" \
"s_barrier\n" \
"v_mfma_f32_16x16x32_bf16 a[16:19], v[64:67], v[160:163], a[16:19]\n" \
"s_add_u32 m0, s87, 4096\n" \
"ds_read_b64_tr_b16 v[128:129], v50 offset:0\n" \
"v_mfma_f32_16x16x32_bf16 a[16:19], v[68:71], v[164:167], a[16:19]\n" \
"global_load_lds_dwordx4 v32, s[54:55] offset:-4096\n" \
"v_mfma_f32_16x16x32_bf16 a[20:23], v[64:67], v[168:171], a[20:23]\n" \
"ds_read_b64_tr_b16 v[130:131], v50 offset:4096\n" \
"v_mfma_f32_16x16x32_bf16 a[20:23], v[68:71], v[172:175], a[20:23]\n" \
"v_mfma_f32_16x16x32_bf16 a[24:27], v[64:67], v[176:179], a[24:27]\n" \
"ds_read_b64_tr_b16 v[132:133], v50 offset:16384\n" \
"v_mfma_f32_16x16x32_bf16 a[24:27], v[68:71], v[180:183], a[24:27]\n" \
"global_load_lds_dwordx4 v33, s[40:41] offset:-3072\n" \
"v_mfma_f32_16x16x32_bf16 a[28:31], v[64:67], v[184:187], a[28:31]\n" \
"ds_read_b64_tr_b16 v[134:135], v50 offset:20480\n" \
"v_mfma_f32_16x16x32_bf16 a[28:31], v[68:71], v[188:191], a[28:31]\n" \
"v_mfma_f32_16x16x32_bf16 a[60:63], v[72:75], v[184:187], a[60:63]\n" \
"ds_read_b128 v[64:67], v48 offset:0\n" \
"v_mfma_f32_16x16x32_bf16 a[60:63], v[76:79], v[188:191], a[60:63]\n" \
"global_load_lds_dwordx4 v34, s[54:55] offset:-2048\n" \
"v_mfma_f32_16x16x32_bf16 a[56:59], v[72:75], v[176:179], a[56:59]\n" \
"ds_read_b64_tr_b16 v[136:137], v51 offset:0\n" \
"v_mfma_f32_16x16x32_bf16 a[56:59], v[76:79], v[180:183], a[56:59]\n" \
"v_mfma_f32_16x16x32_bf16 a[52:55], v[72:75], v[168:171], a[52:55]\n" \
"ds_read_b128 v[68:71], v49 offset:0\n" \
"v_mfma_f32_16x16x32_bf16 a[52:55], v[76:79], v[172:175], a[52:55]\n" \
"global_load_lds_dwordx4 v35, s[40:41] offset:-1024\n" \
"v_mfma_f32_16x16x32_bf16 a[48:51], v[72:75], v[160:163], a[48:51]\n" \
"ds_read_b64_tr_b16 v[138:139], v51 offset:4096\n" \
"v_mfma_f32_16x16x32_bf16 a[48:51], v[76:79], v[164:167], a[48:51]\n" \
"v_mfma_f32_16x16x32_bf16 a[80:83], v[80:83], v[160:163], a[80:83]\n" \
"ds_read_b128 v[72:75], v48 offset:2048\n" \
"v_mfma_f32_16x16x32_bf16 a[80:83], v[84:87], v[164:167], a[80:83]\n" \
"global_load_lds_dwordx4 v36, s[54:55] offset:0\n" \
"v_mfma_f32_16x16x32_bf16 a[84:87], v[80:83], v[168:171], a[84:87]\n" \
"ds_read_b64_tr_b16 v[140:141], v51 offset:16384\n" \
"v_mfma_f32_16x16x32_bf16 a[84:87], v[84:87], v[172:175], a[84:87]\n" \
"v_mfma_f32_16x16x32_bf16 a[88:91], v[80:83], v[176:179], a[88:91]\n" \
"ds_read_b128 v[76:79], v49 offset:2048\n" \
"v_mfma_f32_16x16x32_bf16 a[88:91], v[84:87], v[180:183], a[88:91]\n" \
"global_load_lds_dwordx4 v37, s[40:41] offset:1024\n" \
"v_mfma_f32_16x16x32_bf16 a[92:95], v[80:83], v[184:187], a[92:95]\n" \
"ds_read_b64_tr_b16 v[142:143], v51 offset:20480\n" \
"v_mfma_f32_16x16x32_bf16 a[92:95], v[84:87], v[188:191], a[92:95]\n" \
"v_mfma_f32_16x16x32_bf16 a[124:127], v[88:91], v[184:187], a[124:127]\n" \
"ds_read_b128 v[80:83], v48 offset:4096\n" \
"v_mfma_f32_16x16x32_bf16 a[124:127], v[92:95], v[188:191], a[124:127]\n" \
"global_load_lds_dwordx4 v38, s[54:55] offset:2048\n" \
"v_mfma_f32_16x16x32_bf16 a[120:123], v[88:91], v[176:179], a[120:123]\n" \
"ds_read_b64_tr_b16 v[144:145], v50 offset:512\n" \
"v_mfma_f32_16x16x32_bf16 a[120:123], v[92:95], v[180:183], a[120:123]\n" \
"v_mfma_f32_16x16x32_bf16 a[116:119], v[88:91], v[168:171], a[116:119]\n" \
"ds_read_b128 v[84:87], v49 offset:4096\n" \
"global_load_lds_dwordx4 v39, s[40:41] offset:3072\n" \
"v_mfma_f32_16x16x32_bf16 a[116:119], v[92:95], v[172:175], a[116:119]\n" \
"s_add_u32 s54, s54, s75\n" \
"s_addc_u32 s55, s55, 0\n" \
"v_mfma_f32_16x16x32_bf16 a[112:115], v[88:91], v[160:163], a[112:115]\n" \
"s_add_u32 s40, s40, s75\n" \
"s_addc_u32 s41, s41, 0\n" \
"ds_read_b64_tr_b16 v[146:147], v50 offset:4608\n" \
"v_mfma_f32_16x16x32_bf16 a[112:115], v[92:95], v[164:167], a[112:115]\n" \
"s_add_u32 s68, s66, 0x200000\n" \
"s_add_u32 s64, s8, s68\n" \
"s_addc_u32 s65, s9, 0\n" \
"v_mfma_f32_16x16x32_bf16 a[144:147], v[96:99], v[160:163], a[144:147]\n" \
"ds_read_b128 v[88:91], v48 offset:6144\n" \
"v_mfma_f32_16x16x32_bf16 a[144:147], v[100:103], v[164:167], a[144:147]\n" \
"global_load_dword v253, v248, s[64:65]\n" \
"v_mfma_f32_16x16x32_bf16 a[148:151], v[96:99], v[168:171], a[148:151]\n" \
"s_add_u32 s68, s67, 0x0\n" \
"ds_read_b64_tr_b16 v[148:149], v50 offset:16896\n" \
"v_mfma_f32_16x16x32_bf16 a[148:151], v[100:103], v[172:175], a[148:151]\n" \
"s_add_u32 s64, s12, s68\n" \
"Lpg1151_%=:\n" \
"Lst0x1_%=:\n" \
"s_cbranch_execz Lpg3309_%=\n" \
"s_addc_u32 s65, s13, 0\n" \
"v_mfma_f32_16x16x32_bf16 a[152:155], v[96:99], v[176:179], a[152:155]\n" \
"ds_read_b128 v[92:95], v49 offset:6144\n" \
"v_mfma_f32_16x16x32_bf16 a[152:155], v[100:103], v[180:183], a[152:155]\n" \
"global_load_dword v253, v248, s[64:65]\n" \
"v_mfma_f32_16x16x32_bf16 a[156:159], v[96:99], v[184:187], a[156:159]\n" \
"ds_read_b64_tr_b16 v[150:151], v50 offset:20992\n" \
"v_mfma_f32_16x16x32_bf16 a[156:159], v[100:103], v[188:191], a[156:159]\n" \
"v_mfma_f32_16x16x32_bf16 a[188:191], v[104:107], v[184:187], a[188:191]\n" \
"ds_read_b128 v[96:99], v48 offset:8192\n" \
"v_mfma_f32_16x16x32_bf16 a[188:191], v[108:111], v[188:191], a[188:191]\n" \
"v_mfma_f32_16x16x32_bf16 a[184:187], v[104:107], v[176:179], a[184:187]\n" \
"ds_read_b64_tr_b16 v[152:153], v51 offset:512\n" \
"v_mfma_f32_16x16x32_bf16 a[184:187], v[108:111], v[180:183], a[184:187]\n" \
"v_mfma_f32_16x16x32_bf16 a[180:183], v[104:107], v[168:171], a[180:183]\n" \
"ds_read_b128 v[100:103], v49 offset:8192\n" \
"v_mfma_f32_16x16x32_bf16 a[180:183], v[108:111], v[172:175], a[180:183]\n" \
"v_mfma_f32_16x16x32_bf16 a[176:179], v[104:107], v[160:163], a[176:179]\n" \
"ds_read_b64_tr_b16 v[154:155], v51 offset:4608\n" \
"v_mfma_f32_16x16x32_bf16 a[176:179], v[108:111], v[164:167], a[176:179]\n" \
"v_mfma_f32_16x16x32_bf16 a[208:211], v[112:115], v[160:163], a[208:211]\n" \
"ds_read_b128 v[104:107], v48 offset:10240\n" \
"v_mfma_f32_16x16x32_bf16 a[208:211], v[116:119], v[164:167], a[208:211]\n" \
"v_mfma_f32_16x16x32_bf16 a[212:215], v[112:115], v[168:171], a[212:215]\n" \
"ds_read_b64_tr_b16 v[156:157], v51 offset:16896\n" \
"v_mfma_f32_16x16x32_bf16 a[212:215], v[116:119], v[172:175], a[212:215]\n" \
"v_mfma_f32_16x16x32_bf16 a[216:219], v[112:115], v[176:179], a[216:219]\n" \
"ds_read_b128 v[108:111], v49 offset:10240\n" \
"v_mfma_f32_16x16x32_bf16 a[216:219], v[116:119], v[180:183], a[216:219]\n" \
"v_mfma_f32_16x16x32_bf16 a[220:223], v[112:115], v[184:187], a[220:223]\n" \
"ds_read_b64_tr_b16 v[158:159], v51 offset:20992\n" \
"v_mfma_f32_16x16x32_bf16 a[220:223], v[116:119], v[188:191], a[220:223]\n" \
"v_mfma_f32_16x16x32_bf16 a[252:255], v[120:123], v[184:187], a[252:255]\n" \
"ds_read_b128 v[112:115], v48 offset:12288\n" \
"v_mfma_f32_16x16x32_bf16 a[252:255], v[124:127], v[188:191], a[252:255]\n" \
"v_mfma_f32_16x16x32_bf16 a[248:251], v[120:123], v[176:179], a[248:251]\n" \
"v_mfma_f32_16x16x32_bf16 a[248:251], v[124:127], v[180:183], a[248:251]\n" \
"v_mfma_f32_16x16x32_bf16 a[244:247], v[120:123], v[168:171], a[244:247]\n" \
"ds_read_b128 v[116:119], v49 offset:12288\n" \
"v_mfma_f32_16x16x32_bf16 a[244:247], v[124:127], v[172:175], a[244:247]\n" \
"v_mfma_f32_16x16x32_bf16 a[240:243], v[120:123], v[160:163], a[240:243]\n" \
"v_mfma_f32_16x16x32_bf16 a[240:243], v[124:127], v[164:167], a[240:243]\n" \
"s_waitcnt lgkmcnt(1)\n" \
"v_mfma_f32_16x16x32_bf16 a[0:3], v[128:131], v[64:67], a[0:3]\n" \
"ds_read_b128 v[120:123], v48 offset:14336\n" \
"v_mfma_f32_16x16x32_bf16 a[0:3], v[132:135], v[68:71], a[0:3]\n" \
"v_mfma_f32_16x16x32_bf16 a[4:7], v[136:139], v[64:67], a[4:7]\n" \
"ds_read_b128 v[124:127], v49 offset:14336\n" \
"v_mfma_f32_16x16x32_bf16 a[4:7], v[140:143], v[68:71], a[4:7]\n" \
"s_add_u32 s68, s66, 0x300000\n" \
"v_mfma_f32_16x16x32_bf16 a[8:11], v[144:147], v[64:67], a[8:11]\n" \
"s_add_u32 s64, s8, s68\n" \
"s_addc_u32 s65, s9, 0\n" \
"ds_read_b64_tr_b16 v[160:161], v50 offset:1024\n" \
"v_mfma_f32_16x16x32_bf16 a[8:11], v[148:151], v[68:71], a[8:11]\n" \
"global_load_dword v253, v248, s[64:65]\n" \
"v_mfma_f32_16x16x32_bf16 a[12:15], v[152:155], v[64:67], a[12:15]\n" \
"s_add_u32 m0, s87, 135168\n" \
"ds_read_b64_tr_b16 v[162:163], v50 offset:5120\n" \
"v_mfma_f32_16x16x32_bf16 a[12:15], v[156:159], v[68:71], a[12:15]\n" \
"global_load_lds_dwordx4 v40, s[52:53] offset:-4096\n" \
"v_mfma_f32_16x16x32_bf16 a[44:47], v[152:155], v[72:75], a[44:47]\n" \
"ds_read_b64_tr_b16 v[164:165], v50 offset:17408\n" \
"v_mfma_f32_16x16x32_bf16 a[44:47], v[156:159], v[76:79], a[44:47]\n" \
"v_mfma_f32_16x16x32_bf16 a[40:43], v[144:147], v[72:75], a[40:43]\n" \
"ds_read_b64_tr_b16 v[166:167], v50 offset:21504\n" \
"v_mfma_f32_16x16x32_bf16 a[40:43], v[148:151], v[76:79], a[40:43]\n" \
"global_load_lds_dwordx4 v41, s[50:51] offset:-3072\n" \
"v_mfma_f32_16x16x32_bf16 a[36:39], v[136:139], v[72:75], a[36:39]\n" \
"ds_read_b64_tr_b16 v[168:169], v51 offset:1024\n" \
"v_mfma_f32_16x16x32_bf16 a[36:39], v[140:143], v[76:79], a[36:39]\n" \
"v_mfma_f32_16x16x32_bf16 a[32:35], v[128:131], v[72:75], a[32:35]\n" \
"ds_read_b64_tr_b16 v[170:171], v51 offset:5120\n" \
"v_mfma_f32_16x16x32_bf16 a[32:35], v[132:135], v[76:79], a[32:35]\n" \
"global_load_lds_dwordx4 v42, s[52:53] offset:-2048\n" \
"v_mfma_f32_16x16x32_bf16 a[64:67], v[128:131], v[80:83], a[64:67]\n" \
"ds_read_b64_tr_b16 v[172:173], v51 offset:17408\n" \
"v_mfma_f32_16x16x32_bf16 a[64:67], v[132:135], v[84:87], a[64:67]\n" \
"v_mfma_f32_16x16x32_bf16 a[68:71], v[136:139], v[80:83], a[68:71]\n" \
"ds_read_b64_tr_b16 v[174:175], v51 offset:21504\n" \
"v_mfma_f32_16x16x32_bf16 a[68:71], v[140:143], v[84:87], a[68:71]\n" \
"global_load_lds_dwordx4 v43, s[50:51] offset:-1024\n" \
"v_mfma_f32_16x16x32_bf16 a[72:75], v[144:147], v[80:83], a[72:75]\n" \
"ds_read_b64_tr_b16 v[176:177], v50 offset:1536\n" \
"v_mfma_f32_16x16x32_bf16 a[72:75], v[148:151], v[84:87], a[72:75]\n" \
"v_mfma_f32_16x16x32_bf16 a[76:79], v[152:155], v[80:83], a[76:79]\n" \
"ds_read_b64_tr_b16 v[178:179], v50 offset:5632\n" \
"v_mfma_f32_16x16x32_bf16 a[76:79], v[156:159], v[84:87], a[76:79]\n" \
"global_load_lds_dwordx4 v44, s[52:53] offset:0\n" \
"v_mfma_f32_16x16x32_bf16 a[108:111], v[152:155], v[88:91], a[108:111]\n" \
"ds_read_b64_tr_b16 v[180:181], v50 offset:17920\n" \
"v_mfma_f32_16x16x32_bf16 a[108:111], v[156:159], v[92:95], a[108:111]\n" \
"v_mfma_f32_16x16x32_bf16 a[104:107], v[144:147], v[88:91], a[104:107]\n" \
"ds_read_b64_tr_b16 v[182:183], v50 offset:22016\n" \
"v_mfma_f32_16x16x32_bf16 a[104:107], v[148:151], v[92:95], a[104:107]\n" \
"global_load_lds_dwordx4 v45, s[50:51] offset:1024\n" \
"v_mfma_f32_16x16x32_bf16 a[100:103], v[136:139], v[88:91], a[100:103]\n" \
"ds_read_b64_tr_b16 v[184:185], v51 offset:1536\n" \
"v_mfma_f32_16x16x32_bf16 a[100:103], v[140:143], v[92:95], a[100:103]\n" \
"v_mfma_f32_16x16x32_bf16 a[96:99], v[128:131], v[88:91], a[96:99]\n" \
"ds_read_b64_tr_b16 v[186:187], v51 offset:5632\n" \
"v_mfma_f32_16x16x32_bf16 a[96:99], v[132:135], v[92:95], a[96:99]\n" \
"global_load_lds_dwordx4 v46, s[52:53] offset:2048\n" \
"v_mfma_f32_16x16x32_bf16 a[128:131], v[128:131], v[96:99], a[128:131]\n" \
"ds_read_b64_tr_b16 v[188:189], v51 offset:17920\n" \
"v_mfma_f32_16x16x32_bf16 a[128:131], v[132:135], v[100:103], a[128:131]\n" \
"v_mfma_f32_16x16x32_bf16 a[132:135], v[136:139], v[96:99], a[132:135]\n" \
"ds_read_b64_tr_b16 v[190:191], v51 offset:22016\n" \
"v_mfma_f32_16x16x32_bf16 a[132:135], v[140:143], v[100:103], a[132:135]\n" \
"global_load_lds_dwordx4 v47, s[50:51] offset:3072\n" \
"v_mfma_f32_16x16x32_bf16 a[136:139], v[144:147], v[96:99], a[136:139]\n" \
"s_add_u32 s52, s52, s74\n" \
"s_addc_u32 s53, s53, 0\n" \
"v_mfma_f32_16x16x32_bf16 a[136:139], v[148:151], v[100:103], a[136:139]\n" \
"s_add_u32 s50, s50, s74\n" \
"s_addc_u32 s51, s51, 0\n" \
"v_mfma_f32_16x16x32_bf16 a[140:143], v[152:155], v[96:99], a[140:143]\n" \
"v_mfma_f32_16x16x32_bf16 a[140:143], v[156:159], v[100:103], a[140:143]\n" \
"v_mfma_f32_16x16x32_bf16 a[172:175], v[152:155], v[104:107], a[172:175]\n" \
"s_mul_i32 s76, s73, 0x10000\n" \
"s_add_u32 s77, s76, 0xfffe8000\n" \
"s_sub_u32 s78, 0xffff8000, s76\n" \
"v_mfma_f32_16x16x32_bf16 a[172:175], v[156:159], v[108:111], a[172:175]\n" \
"v_add_u32 v48, s77, v48\n" \
"v_add_u32 v49, s77, v49\n" \
"v_mfma_f32_16x16x32_bf16 a[168:171], v[144:147], v[104:107], a[168:171]\n" \
"v_add_u32 v50, s78, v50\n" \
"v_add_u32 v51, s78, v51\n" \
"v_mfma_f32_16x16x32_bf16 a[168:171], v[148:151], v[108:111], a[168:171]\n" \
"v_mfma_f32_16x16x32_bf16 a[164:167], v[136:139], v[104:107], a[164:167]\n" \
"v_mfma_f32_16x16x32_bf16 a[164:167], v[140:143], v[108:111], a[164:167]\n" \
"v_mfma_f32_16x16x32_bf16 a[160:163], v[128:131], v[104:107], a[160:163]\n" \
"v_mfma_f32_16x16x32_bf16 a[160:163], v[132:135], v[108:111], a[160:163]\n" \
"s_waitcnt lgkmcnt(0)\n" \
"v_mfma_f32_16x16x32_bf16 a[192:195], v[128:131], v[112:115], a[192:195]\n" \
"v_mfma_f32_16x16x32_bf16 a[192:195], v[132:135], v[116:119], a[192:195]\n" \
"v_mfma_f32_16x16x32_bf16 a[196:199], v[136:139], v[112:115], a[196:199]\n" \
"v_mfma_f32_16x16x32_bf16 a[196:199], v[140:143], v[116:119], a[196:199]\n" \
"v_mfma_f32_16x16x32_bf16 a[200:203], v[144:147], v[112:115], a[200:203]\n" \
"v_mfma_f32_16x16x32_bf16 a[200:203], v[148:151], v[116:119], a[200:203]\n" \
"v_mfma_f32_16x16x32_bf16 a[204:207], v[152:155], v[112:115], a[204:207]\n" \
"v_mfma_f32_16x16x32_bf16 a[204:207], v[156:159], v[116:119], a[204:207]\n" \
"v_mfma_f32_16x16x32_bf16 a[236:239], v[152:155], v[120:123], a[236:239]\n" \
"v_mfma_f32_16x16x32_bf16 a[236:239], v[156:159], v[124:127], a[236:239]\n" \
"v_mfma_f32_16x16x32_bf16 a[232:235], v[144:147], v[120:123], a[232:235]\n" \
"v_mfma_f32_16x16x32_bf16 a[232:235], v[148:151], v[124:127], a[232:235]\n" \
"v_mfma_f32_16x16x32_bf16 a[228:231], v[136:139], v[120:123], a[228:231]\n" \
"v_mfma_f32_16x16x32_bf16 a[228:231], v[140:143], v[124:127], a[228:231]\n" \
"v_mfma_f32_16x16x32_bf16 a[224:227], v[128:131], v[120:123], a[224:227]\n" \
"s_waitcnt vmcnt(11) lgkmcnt(0)\n" \
"v_mfma_f32_16x16x32_bf16 a[224:227], v[132:135], v[124:127], a[224:227]\n" \
"s_barrier\n" \
"v_mfma_f32_16x16x32_bf16 a[16:19], v[64:67], v[160:163], a[16:19]\n" \
"s_add_u32 m0, s87, 69632\n" \
"ds_read_b64_tr_b16 v[128:129], v50 offset:0\n" \
"v_mfma_f32_16x16x32_bf16 a[16:19], v[68:71], v[164:167], a[16:19]\n" \
"global_load_lds_dwordx4 v32, s[54:55] offset:-4096\n" \
"v_mfma_f32_16x16x32_bf16 a[20:23], v[64:67], v[168:171], a[20:23]\n" \
"ds_read_b64_tr_b16 v[130:131], v50 offset:4096\n" \
"v_mfma_f32_16x16x32_bf16 a[20:23], v[68:71], v[172:175], a[20:23]\n" \
"v_mfma_f32_16x16x32_bf16 a[24:27], v[64:67], v[176:179], a[24:27]\n" \
"ds_read_b64_tr_b16 v[132:133], v50 offset:16384\n" \
"v_mfma_f32_16x16x32_bf16 a[24:27], v[68:71], v[180:183], a[24:27]\n" \
"global_load_lds_dwordx4 v33, s[40:41] offset:-3072\n" \
"v_mfma_f32_16x16x32_bf16 a[28:31], v[64:67], v[184:187], a[28:31]\n" \
"ds_read_b64_tr_b16 v[134:135], v50 offset:20480\n" \
"v_mfma_f32_16x16x32_bf16 a[28:31], v[68:71], v[188:191], a[28:31]\n" \
"v_mfma_f32_16x16x32_bf16 a[60:63], v[72:75], v[184:187], a[60:63]\n" \
"ds_read_b128 v[64:67], v48 offset:0\n" \
"v_mfma_f32_16x16x32_bf16 a[60:63], v[76:79], v[188:191], a[60:63]\n" \
"global_load_lds_dwordx4 v34, s[54:55] offset:-2048\n" \
"v_mfma_f32_16x16x32_bf16 a[56:59], v[72:75], v[176:179], a[56:59]\n" \
"ds_read_b64_tr_b16 v[136:137], v51 offset:0\n" \
"v_mfma_f32_16x16x32_bf16 a[56:59], v[76:79], v[180:183], a[56:59]\n" \
"v_mfma_f32_16x16x32_bf16 a[52:55], v[72:75], v[168:171], a[52:55]\n" \
"ds_read_b128 v[68:71], v49 offset:0\n" \
"v_mfma_f32_16x16x32_bf16 a[52:55], v[76:79], v[172:175], a[52:55]\n" \
"global_load_lds_dwordx4 v35, s[40:41] offset:-1024\n" \
"v_mfma_f32_16x16x32_bf16 a[48:51], v[72:75], v[160:163], a[48:51]\n" \
"ds_read_b64_tr_b16 v[138:139], v51 offset:4096\n" \
"v_mfma_f32_16x16x32_bf16 a[48:51], v[76:79], v[164:167], a[48:51]\n" \
"v_mfma_f32_16x16x32_bf16 a[80:83], v[80:83], v[160:163], a[80:83]\n" \
"ds_read_b128 v[72:75], v48 offset:2048\n" \
"v_mfma_f32_16x16x32_bf16 a[80:83], v[84:87], v[164:167], a[80:83]\n" \
"global_load_lds_dwordx4 v36, s[54:55] offset:0\n" \
"v_mfma_f32_16x16x32_bf16 a[84:87], v[80:83], v[168:171], a[84:87]\n" \
"ds_read_b64_tr_b16 v[140:141], v51 offset:16384\n" \
"v_mfma_f32_16x16x32_bf16 a[84:87], v[84:87], v[172:175], a[84:87]\n" \
"v_mfma_f32_16x16x32_bf16 a[88:91], v[80:83], v[176:179], a[88:91]\n" \
"ds_read_b128 v[76:79], v49 offset:2048\n" \
"v_mfma_f32_16x16x32_bf16 a[88:91], v[84:87], v[180:183], a[88:91]\n" \
"global_load_lds_dwordx4 v37, s[40:41] offset:1024\n" \
"v_mfma_f32_16x16x32_bf16 a[92:95], v[80:83], v[184:187], a[92:95]\n" \
"ds_read_b64_tr_b16 v[142:143], v51 offset:20480\n" \
"v_mfma_f32_16x16x32_bf16 a[92:95], v[84:87], v[188:191], a[92:95]\n" \
"v_mfma_f32_16x16x32_bf16 a[124:127], v[88:91], v[184:187], a[124:127]\n" \
"ds_read_b128 v[80:83], v48 offset:4096\n" \
"v_mfma_f32_16x16x32_bf16 a[124:127], v[92:95], v[188:191], a[124:127]\n" \
"global_load_lds_dwordx4 v38, s[54:55] offset:2048\n" \
"v_mfma_f32_16x16x32_bf16 a[120:123], v[88:91], v[176:179], a[120:123]\n" \
"ds_read_b64_tr_b16 v[144:145], v50 offset:512\n" \
"v_mfma_f32_16x16x32_bf16 a[120:123], v[92:95], v[180:183], a[120:123]\n" \
"v_mfma_f32_16x16x32_bf16 a[116:119], v[88:91], v[168:171], a[116:119]\n" \
"ds_read_b128 v[84:87], v49 offset:4096\n" \
"global_load_lds_dwordx4 v39, s[40:41] offset:3072\n" \
"v_mfma_f32_16x16x32_bf16 a[116:119], v[92:95], v[172:175], a[116:119]\n" \
"s_add_u32 s54, s54, s75\n" \
"s_addc_u32 s55, s55, 0\n" \
"v_mfma_f32_16x16x32_bf16 a[112:115], v[88:91], v[160:163], a[112:115]\n" \
"s_add_u32 s40, s40, s75\n" \
"s_addc_u32 s41, s41, 0\n" \
"ds_read_b64_tr_b16 v[146:147], v50 offset:4608\n" \
"v_mfma_f32_16x16x32_bf16 a[112:115], v[92:95], v[164:167], a[112:115]\n" \
"s_add_u32 s68, s67, 0x100000\n" \
"s_add_u32 s64, s12, s68\n" \
"s_addc_u32 s65, s13, 0\n" \
"v_mfma_f32_16x16x32_bf16 a[144:147], v[96:99], v[160:163], a[144:147]\n" \
"ds_read_b128 v[88:91], v48 offset:6144\n" \
"v_mfma_f32_16x16x32_bf16 a[144:147], v[100:103], v[164:167], a[144:147]\n" \
"global_load_dword v253, v248, s[64:65]\n" \
"v_mfma_f32_16x16x32_bf16 a[148:151], v[96:99], v[168:171], a[148:151]\n" \
"s_add_u32 s68, s67, 0x500000\n" \
"ds_read_b64_tr_b16 v[148:149], v50 offset:16896\n" \
"v_mfma_f32_16x16x32_bf16 a[148:151], v[100:103], v[172:175], a[148:151]\n" \
"s_add_u32 s64, s12, s68\n" \
"s_addc_u32 s65, s13, 0\n" \
"v_mfma_f32_16x16x32_bf16 a[152:155], v[96:99], v[176:179], a[152:155]\n" \
"ds_read_b128 v[92:95], v49 offset:6144\n" \
"v_mfma_f32_16x16x32_bf16 a[152:155], v[100:103], v[180:183], a[152:155]\n" \
"global_load_dword v253, v248, s[64:65]\n" \
"v_mfma_f32_16x16x32_bf16 a[156:159], v[96:99], v[184:187], a[156:159]\n" \
"ds_read_b64_tr_b16 v[150:151], v50 offset:20992\n" \
"v_mfma_f32_16x16x32_bf16 a[156:159], v[100:103], v[188:191], a[156:159]\n" \
"v_mfma_f32_16x16x32_bf16 a[188:191], v[104:107], v[184:187], a[188:191]\n" \
"ds_read_b128 v[96:99], v48 offset:8192\n" \
"v_mfma_f32_16x16x32_bf16 a[188:191], v[108:111], v[188:191], a[188:191]\n" \
"v_mfma_f32_16x16x32_bf16 a[184:187], v[104:107], v[176:179], a[184:187]\n" \
"ds_read_b64_tr_b16 v[152:153], v51 offset:512\n" \
"v_mfma_f32_16x16x32_bf16 a[184:187], v[108:111], v[180:183], a[184:187]\n" \
"v_mfma_f32_16x16x32_bf16 a[180:183], v[104:107], v[168:171], a[180:183]\n" \
"ds_read_b128 v[100:103], v49 offset:8192\n" \
"v_mfma_f32_16x16x32_bf16 a[180:183], v[108:111], v[172:175], a[180:183]\n" \
"v_mfma_f32_16x16x32_bf16 a[176:179], v[104:107], v[160:163], a[176:179]\n" \
"ds_read_b64_tr_b16 v[154:155], v51 offset:4608\n" \
"v_mfma_f32_16x16x32_bf16 a[176:179], v[108:111], v[164:167], a[176:179]\n" \
"v_mfma_f32_16x16x32_bf16 a[208:211], v[112:115], v[160:163], a[208:211]\n" \
"ds_read_b128 v[104:107], v48 offset:10240\n" \
"v_mfma_f32_16x16x32_bf16 a[208:211], v[116:119], v[164:167], a[208:211]\n" \
"v_mfma_f32_16x16x32_bf16 a[212:215], v[112:115], v[168:171], a[212:215]\n" \
"ds_read_b64_tr_b16 v[156:157], v51 offset:16896\n" \
"v_mfma_f32_16x16x32_bf16 a[212:215], v[116:119], v[172:175], a[212:215]\n" \
"v_mfma_f32_16x16x32_bf16 a[216:219], v[112:115], v[176:179], a[216:219]\n" \
"ds_read_b128 v[108:111], v49 offset:10240\n" \
"v_mfma_f32_16x16x32_bf16 a[216:219], v[116:119], v[180:183], a[216:219]\n" \
"v_mfma_f32_16x16x32_bf16 a[220:223], v[112:115], v[184:187], a[220:223]\n" \
"ds_read_b64_tr_b16 v[158:159], v51 offset:20992\n" \
"v_mfma_f32_16x16x32_bf16 a[220:223], v[116:119], v[188:191], a[220:223]\n" \
"v_mfma_f32_16x16x32_bf16 a[252:255], v[120:123], v[184:187], a[252:255]\n" \
"ds_read_b128 v[112:115], v48 offset:12288\n" \
"v_mfma_f32_16x16x32_bf16 a[252:255], v[124:127], v[188:191], a[252:255]\n" \
"v_mfma_f32_16x16x32_bf16 a[248:251], v[120:123], v[176:179], a[248:251]\n" \
"v_mfma_f32_16x16x32_bf16 a[248:251], v[124:127], v[180:183], a[248:251]\n" \
"v_mfma_f32_16x16x32_bf16 a[244:247], v[120:123], v[168:171], a[244:247]\n" \
"ds_read_b128 v[116:119], v49 offset:12288\n" \
"v_mfma_f32_16x16x32_bf16 a[244:247], v[124:127], v[172:175], a[244:247]\n" \
"v_mfma_f32_16x16x32_bf16 a[240:243], v[120:123], v[160:163], a[240:243]\n" \
"v_mfma_f32_16x16x32_bf16 a[240:243], v[124:127], v[164:167], a[240:243]\n" \
"s_waitcnt lgkmcnt(1)\n" \
"v_mfma_f32_16x16x32_bf16 a[0:3], v[128:131], v[64:67], a[0:3]\n" \
"ds_read_b128 v[120:123], v48 offset:14336\n" \
"v_mfma_f32_16x16x32_bf16 a[0:3], v[132:135], v[68:71], a[0:3]\n" \
"v_mfma_f32_16x16x32_bf16 a[4:7], v[136:139], v[64:67], a[4:7]\n" \
"ds_read_b128 v[124:127], v49 offset:14336\n" \
"v_mfma_f32_16x16x32_bf16 a[4:7], v[140:143], v[68:71], a[4:7]\n" \
"s_add_u32 s68, s67, 0x200000\n" \
"v_mfma_f32_16x16x32_bf16 a[8:11], v[144:147], v[64:67], a[8:11]\n" \
"s_add_u32 s64, s12, s68\n" \
"s_addc_u32 s65, s13, 0\n" \
"ds_read_b64_tr_b16 v[160:161], v50 offset:1024\n" \
"v_mfma_f32_16x16x32_bf16 a[8:11], v[148:151], v[68:71], a[8:11]\n" \
"global_load_dword v253, v248, s[64:65]\n" \
"v_mfma_f32_16x16x32_bf16 a[12:15], v[152:155], v[64:67], a[12:15]\n" \
"s_add_u32 m0, s87, 102400\n" \
"ds_read_b64_tr_b16 v[162:163], v50 offset:5120\n" \
"v_mfma_f32_16x16x32_bf16 a[12:15], v[156:159], v[68:71], a[12:15]\n" \
"global_load_lds_dwordx4 v40, s[52:53] offset:-4096\n" \
"v_mfma_f32_16x16x32_bf16 a[44:47], v[152:155], v[72:75], a[44:47]\n" \
"ds_read_b64_tr_b16 v[164:165], v50 offset:17408\n" \
"v_mfma_f32_16x16x32_bf16 a[44:47], v[156:159], v[76:79], a[44:47]\n" \
"v_mfma_f32_16x16x32_bf16 a[40:43], v[144:147], v[72:75], a[40:43]\n" \
"ds_read_b64_tr_b16 v[166:167], v50 offset:21504\n" \
"v_mfma_f32_16x16x32_bf16 a[40:43], v[148:151], v[76:79], a[40:43]\n" \
"global_load_lds_dwordx4 v41, s[50:51] offset:-3072\n" \
"v_mfma_f32_16x16x32_bf16 a[36:39], v[136:139], v[72:75], a[36:39]\n" \
"ds_read_b64_tr_b16 v[168:169], v51 offset:1024\n" \
"v_mfma_f32_16x16x32_bf16 a[36:39], v[140:143], v[76:79], a[36:39]\n" \
"v_mfma_f32_16x16x32_bf16 a[32:35], v[128:131], v[72:75], a[32:35]\n" \
"ds_read_b64_tr_b16 v[170:171], v51 offset:5120\n" \
"v_mfma_f32_16x16x32_bf16 a[32:35], v[132:135], v[76:79], a[32:35]\n" \
"global_load_lds_dwordx4 v42, s[52:53] offset:-2048\n" \
"v_mfma_f32_16x16x32_bf16 a[64:67], v[128:131], v[80:83], a[64:67]\n" \
"ds_read_b64_tr_b16 v[172:173], v51 offset:17408\n" \
"v_mfma_f32_16x16x32_bf16 a[64:67], v[132:135], v[84:87], a[64:67]\n" \
"v_mfma_f32_16x16x32_bf16 a[68:71], v[136:139], v[80:83], a[68:71]\n" \
"ds_read_b64_tr_b16 v[174:175], v51 offset:21504\n" \
"v_mfma_f32_16x16x32_bf16 a[68:71], v[140:143], v[84:87], a[68:71]\n" \
"global_load_lds_dwordx4 v43, s[50:51] offset:-1024\n" \
"v_mfma_f32_16x16x32_bf16 a[72:75], v[144:147], v[80:83], a[72:75]\n" \
"ds_read_b64_tr_b16 v[176:177], v50 offset:1536\n" \
"v_mfma_f32_16x16x32_bf16 a[72:75], v[148:151], v[84:87], a[72:75]\n" \
"v_mfma_f32_16x16x32_bf16 a[76:79], v[152:155], v[80:83], a[76:79]\n" \
"ds_read_b64_tr_b16 v[178:179], v50 offset:5632\n" \
"v_mfma_f32_16x16x32_bf16 a[76:79], v[156:159], v[84:87], a[76:79]\n" \
"global_load_lds_dwordx4 v44, s[52:53] offset:0\n" \
"v_mfma_f32_16x16x32_bf16 a[108:111], v[152:155], v[88:91], a[108:111]\n" \
"ds_read_b64_tr_b16 v[180:181], v50 offset:17920\n" \
"v_mfma_f32_16x16x32_bf16 a[108:111], v[156:159], v[92:95], a[108:111]\n" \
"v_mfma_f32_16x16x32_bf16 a[104:107], v[144:147], v[88:91], a[104:107]\n" \
"ds_read_b64_tr_b16 v[182:183], v50 offset:22016\n" \
"v_mfma_f32_16x16x32_bf16 a[104:107], v[148:151], v[92:95], a[104:107]\n" \
"global_load_lds_dwordx4 v45, s[50:51] offset:1024\n" \
"v_mfma_f32_16x16x32_bf16 a[100:103], v[136:139], v[88:91], a[100:103]\n" \
"ds_read_b64_tr_b16 v[184:185], v51 offset:1536\n" \
"v_mfma_f32_16x16x32_bf16 a[100:103], v[140:143], v[92:95], a[100:103]\n" \
"v_mfma_f32_16x16x32_bf16 a[96:99], v[128:131], v[88:91], a[96:99]\n" \
"ds_read_b64_tr_b16 v[186:187], v51 offset:5632\n" \
"v_mfma_f32_16x16x32_bf16 a[96:99], v[132:135], v[92:95], a[96:99]\n" \
"global_load_lds_dwordx4 v46, s[52:53] offset:2048\n" \
"v_mfma_f32_16x16x32_bf16 a[128:131], v[128:131], v[96:99], a[128:131]\n" \
"ds_read_b64_tr_b16 v[188:189], v51 offset:17920\n" \
"v_mfma_f32_16x16x32_bf16 a[128:131], v[132:135], v[100:103], a[128:131]\n" \
"v_mfma_f32_16x16x32_bf16 a[132:135], v[136:139], v[96:99], a[132:135]\n" \
"ds_read_b64_tr_b16 v[190:191], v51 offset:22016\n" \
"v_mfma_f32_16x16x32_bf16 a[132:135], v[140:143], v[100:103], a[132:135]\n" \
"global_load_lds_dwordx4 v47, s[50:51] offset:3072\n" \
"v_mfma_f32_16x16x32_bf16 a[136:139], v[144:147], v[96:99], a[136:139]\n" \
"v_mfma_f32_16x16x32_bf16 a[136:139], v[148:151], v[100:103], a[136:139]\n" \
"v_mfma_f32_16x16x32_bf16 a[140:143], v[152:155], v[96:99], a[140:143]\n" \
"v_mfma_f32_16x16x32_bf16 a[140:143], v[156:159], v[100:103], a[140:143]\n" \
"v_mfma_f32_16x16x32_bf16 a[172:175], v[152:155], v[104:107], a[172:175]\n" \
"s_mul_i32 s76, s73, 0x8000\n" \
"s_add_u32 s77, s76, 0x10000\n" \
"s_sub_u32 s78, 0x18000, s76\n" \
"v_mfma_f32_16x16x32_bf16 a[172:175], v[156:159], v[108:111], a[172:175]\n" \
"v_add_u32 v48, s77, v48\n" \
"v_add_u32 v49, s77, v49\n" \
"v_mfma_f32_16x16x32_bf16 a[168:171], v[144:147], v[104:107], a[168:171]\n" \
"v_add_u32 v50, s78, v50\n" \
"v_add_u32 v51, s78, v51\n" \
"v_mfma_f32_16x16x32_bf16 a[168:171], v[148:151], v[108:111], a[168:171]\n" \
"v_mfma_f32_16x16x32_bf16 a[164:167], v[136:139], v[104:107], a[164:167]\n" \
"v_mfma_f32_16x16x32_bf16 a[164:167], v[140:143], v[108:111], a[164:167]\n" \
"v_mfma_f32_16x16x32_bf16 a[160:163], v[128:131], v[104:107], a[160:163]\n" \
"v_mfma_f32_16x16x32_bf16 a[160:163], v[132:135], v[108:111], a[160:163]\n" \
"s_waitcnt lgkmcnt(0)\n" \
"v_mfma_f32_16x16x32_bf16 a[192:195], v[128:131], v[112:115], a[192:195]\n" \
"v_mfma_f32_16x16x32_bf16 a[192:195], v[132:135], v[116:119], a[192:195]\n" \
"v_mfma_f32_16x16x32_bf16 a[196:199], v[136:139], v[112:115], a[196:199]\n" \
"v_mfma_f32_16x16x32_bf16 a[196:199], v[140:143], v[116:119], a[196:199]\n" \
"v_mfma_f32_16x16x32_bf16 a[200:203], v[144:147], v[112:115], a[200:203]\n" \
"v_mfma_f32_16x16x32_bf16 a[200:203], v[148:151], v[116:119], a[200:203]\n" \
"v_mfma_f32_16x16x32_bf16 a[204:207], v[152:155], v[112:115], a[204:207]\n" \
"v_mfma_f32_16x16x32_bf16 a[204:207], v[156:159], v[116:119], a[204:207]\n" \
"v_mfma_f32_16x16x32_bf16 a[236:239], v[152:155], v[120:123], a[236:239]\n" \
"v_mfma_f32_16x16x32_bf16 a[236:239], v[156:159], v[124:127], a[236:239]\n" \
"v_mfma_f32_16x16x32_bf16 a[232:235], v[144:147], v[120:123], a[232:235]\n" \
"v_mfma_f32_16x16x32_bf16 a[232:235], v[148:151], v[124:127], a[232:235]\n" \
"v_mfma_f32_16x16x32_bf16 a[228:231], v[136:139], v[120:123], a[228:231]\n" \
"v_mfma_f32_16x16x32_bf16 a[228:231], v[140:143], v[124:127], a[228:231]\n" \
"v_mfma_f32_16x16x32_bf16 a[224:227], v[128:131], v[120:123], a[224:227]\n" \
"s_waitcnt vmcnt(11) lgkmcnt(0)\n" \
"v_mfma_f32_16x16x32_bf16 a[224:227], v[132:135], v[124:127], a[224:227]\n" \
"s_barrier\n" \
"v_mfma_f32_16x16x32_bf16 a[16:19], v[64:67], v[160:163], a[16:19]\n" \
"s_add_u32 m0, s87, 36864\n" \
"ds_read_b64_tr_b16 v[128:129], v50 offset:0\n" \
"v_mfma_f32_16x16x32_bf16 a[16:19], v[68:71], v[164:167], a[16:19]\n" \
"global_load_lds_dwordx4 v32, s[54:55] offset:-4096\n" \
"v_mfma_f32_16x16x32_bf16 a[20:23], v[64:67], v[168:171], a[20:23]\n" \
"ds_read_b64_tr_b16 v[130:131], v50 offset:4096\n" \
"v_mfma_f32_16x16x32_bf16 a[20:23], v[68:71], v[172:175], a[20:23]\n" \
"v_mfma_f32_16x16x32_bf16 a[24:27], v[64:67], v[176:179], a[24:27]\n" \
"ds_read_b64_tr_b16 v[132:133], v50 offset:16384\n" \
"v_mfma_f32_16x16x32_bf16 a[24:27], v[68:71], v[180:183], a[24:27]\n" \
"global_load_lds_dwordx4 v33, s[40:41] offset:-3072\n" \
"v_mfma_f32_16x16x32_bf16 a[28:31], v[64:67], v[184:187], a[28:31]\n" \
"ds_read_b64_tr_b16 v[134:135], v50 offset:20480\n" \
"v_mfma_f32_16x16x32_bf16 a[28:31], v[68:71], v[188:191], a[28:31]\n" \
"v_mfma_f32_16x16x32_bf16 a[60:63], v[72:75], v[184:187], a[60:63]\n" \
"ds_read_b128 v[64:67], v48 offset:0\n" \
"v_mfma_f32_16x16x32_bf16 a[60:63], v[76:79], v[188:191], a[60:63]\n" \
"global_load_lds_dwordx4 v34, s[54:55] offset:-2048\n" \
"v_mfma_f32_16x16x32_bf16 a[56:59], v[72:75], v[176:179], a[56:59]\n" \
"ds_read_b64_tr_b16 v[136:137], v51 offset:0\n" \
"v_mfma_f32_16x16x32_bf16 a[56:59], v[76:79], v[180:183], a[56:59]\n" \
"v_mfma_f32_16x16x32_bf16 a[52:55], v[72:75], v[168:171], a[52:55]\n" \
"ds_read_b128 v[68:71], v49 offset:0\n" \
"v_mfma_f32_16x16x32_bf16 a[52:55], v[76:79], v[172:175], a[52:55]\n" \
"global_load_lds_dwordx4 v35, s[40:41] offset:-1024\n" \
"v_mfma_f32_16x16x32_bf16 a[48:51], v[72:75], v[160:163], a[48:51]\n" \
"ds_read_b64_tr_b16 v[138:139], v51 offset:4096\n" \
"v_mfma_f32_16x16x32_bf16 a[48:51], v[76:79], v[164:167], a[48:51]\n" \
"v_mfma_f32_16x16x32_bf16 a[80:83], v[80:83], v[160:163], a[80:83]\n" \
"ds_read_b128 v[72:75], v48 offset:2048\n" \
"v_mfma_f32_16x16x32_bf16 a[80:83], v[84:87], v[164:167], a[80:83]\n" \
"global_load_lds_dwordx4 v36, s[54:55] offset:0\n" \
"v_mfma_f32_16x16x32_bf16 a[84:87], v[80:83], v[168:171], a[84:87]\n" \
"ds_read_b64_tr_b16 v[140:141], v51 offset:16384\n" \
"v_mfma_f32_16x16x32_bf16 a[84:87], v[84:87], v[172:175], a[84:87]\n" \
"v_mfma_f32_16x16x32_bf16 a[88:91], v[80:83], v[176:179], a[88:91]\n" \
"ds_read_b128 v[76:79], v49 offset:2048\n" \
"v_mfma_f32_16x16x32_bf16 a[88:91], v[84:87], v[180:183], a[88:91]\n" \
"global_load_lds_dwordx4 v37, s[40:41] offset:1024\n" \
"v_mfma_f32_16x16x32_bf16 a[92:95], v[80:83], v[184:187], a[92:95]\n" \
"ds_read_b64_tr_b16 v[142:143], v51 offset:20480\n" \
"v_mfma_f32_16x16x32_bf16 a[92:95], v[84:87], v[188:191], a[92:95]\n" \
"v_mfma_f32_16x16x32_bf16 a[124:127], v[88:91], v[184:187], a[124:127]\n" \
"ds_read_b128 v[80:83], v48 offset:4096\n" \
"v_mfma_f32_16x16x32_bf16 a[124:127], v[92:95], v[188:191], a[124:127]\n" \
"global_load_lds_dwordx4 v38, s[54:55] offset:2048\n" \
"v_mfma_f32_16x16x32_bf16 a[120:123], v[88:91], v[176:179], a[120:123]\n" \
"ds_read_b64_tr_b16 v[144:145], v50 offset:512\n" \
"v_mfma_f32_16x16x32_bf16 a[120:123], v[92:95], v[180:183], a[120:123]\n" \
"v_mfma_f32_16x16x32_bf16 a[116:119], v[88:91], v[168:171], a[116:119]\n" \
"ds_read_b128 v[84:87], v49 offset:4096\n" \
"v_mfma_f32_16x16x32_bf16 a[116:119], v[92:95], v[172:175], a[116:119]\n" \
"global_load_lds_dwordx4 v39, s[40:41] offset:3072\n" \
"s_add_u32 s68, s67, 0x300000\n" \
"v_mfma_f32_16x16x32_bf16 a[112:115], v[88:91], v[160:163], a[112:115]\n" \
"s_add_u32 s64, s12, s68\n" \
"s_addc_u32 s65, s13, 0\n" \
"ds_read_b64_tr_b16 v[146:147], v50 offset:4608\n" \
"v_mfma_f32_16x16x32_bf16 a[112:115], v[92:95], v[164:167], a[112:115]\n" \
"v_mfma_f32_16x16x32_bf16 a[144:147], v[96:99], v[160:163], a[144:147]\n" \
"ds_read_b128 v[88:91], v48 offset:6144\n" \
"v_mfma_f32_16x16x32_bf16 a[144:147], v[100:103], v[164:167], a[144:147]\n" \
"global_load_dword v253, v248, s[64:65]\n" \
"v_mfma_f32_16x16x32_bf16 a[148:151], v[96:99], v[168:171], a[148:151]\n" \
"s_add_u32 s68, s67, 0x400000\n" \
"ds_read_b64_tr_b16 v[148:149], v50 offset:16896\n" \
"v_mfma_f32_16x16x32_bf16 a[148:151], v[100:103], v[172:175], a[148:151]\n" \
"s_add_u32 s64, s12, s68\n" \
"s_addc_u32 s65, s13, 0\n" \
"v_mfma_f32_16x16x32_bf16 a[152:155], v[96:99], v[176:179], a[152:155]\n" \
"ds_read_b128 v[92:95], v49 offset:6144\n" \
"v_mfma_f32_16x16x32_bf16 a[152:155], v[100:103], v[180:183], a[152:155]\n" \
"global_load_dword v253, v248, s[64:65]\n" \
"v_mfma_f32_16x16x32_bf16 a[156:159], v[96:99], v[184:187], a[156:159]\n" \
"ds_read_b64_tr_b16 v[150:151], v50 offset:20992\n" \
"v_mfma_f32_16x16x32_bf16 a[156:159], v[100:103], v[188:191], a[156:159]\n" \
"v_mfma_f32_16x16x32_bf16 a[188:191], v[104:107], v[184:187], a[188:191]\n" \
"ds_read_b128 v[96:99], v48 offset:8192\n" \
"v_mfma_f32_16x16x32_bf16 a[188:191], v[108:111], v[188:191], a[188:191]\n" \
"v_mfma_f32_16x16x32_bf16 a[184:187], v[104:107], v[176:179], a[184:187]\n" \
"ds_read_b64_tr_b16 v[152:153], v51 offset:512\n" \
"v_mfma_f32_16x16x32_bf16 a[184:187], v[108:111], v[180:183], a[184:187]\n" \
"v_mfma_f32_16x16x32_bf16 a[180:183], v[104:107], v[168:171], a[180:183]\n" \
"ds_read_b128 v[100:103], v49 offset:8192\n" \
"v_mfma_f32_16x16x32_bf16 a[180:183], v[108:111], v[172:175], a[180:183]\n" \
"v_mfma_f32_16x16x32_bf16 a[176:179], v[104:107], v[160:163], a[176:179]\n" \
"ds_read_b64_tr_b16 v[154:155], v51 offset:4608\n" \
"v_mfma_f32_16x16x32_bf16 a[176:179], v[108:111], v[164:167], a[176:179]\n" \
"v_mfma_f32_16x16x32_bf16 a[208:211], v[112:115], v[160:163], a[208:211]\n" \
"ds_read_b128 v[104:107], v48 offset:10240\n" \
"v_mfma_f32_16x16x32_bf16 a[208:211], v[116:119], v[164:167], a[208:211]\n" \
"v_mfma_f32_16x16x32_bf16 a[212:215], v[112:115], v[168:171], a[212:215]\n" \
"ds_read_b64_tr_b16 v[156:157], v51 offset:16896\n" \
"v_mfma_f32_16x16x32_bf16 a[212:215], v[116:119], v[172:175], a[212:215]\n" \
"v_mfma_f32_16x16x32_bf16 a[216:219], v[112:115], v[176:179], a[216:219]\n" \
"ds_read_b128 v[108:111], v49 offset:10240\n" \
"v_mfma_f32_16x16x32_bf16 a[216:219], v[116:119], v[180:183], a[216:219]\n" \
"v_mfma_f32_16x16x32_bf16 a[220:223], v[112:115], v[184:187], a[220:223]\n" \
"ds_read_b64_tr_b16 v[158:159], v51 offset:20992\n" \
"v_mfma_f32_16x16x32_bf16 a[220:223], v[116:119], v[188:191], a[220:223]\n" \
"v_mfma_f32_16x16x32_bf16 a[252:255], v[120:123], v[184:187], a[252:255]\n" \
"ds_read_b128 v[112:115], v48 offset:12288\n" \
"v_mfma_f32_16x16x32_bf16 a[252:255], v[124:127], v[188:191], a[252:255]\n" \
"v_mfma_f32_16x16x32_bf16 a[248:251], v[120:123], v[176:179], a[248:251]\n" \
"v_mfma_f32_16x16x32_bf16 a[248:251], v[124:127], v[180:183], a[248:251]\n" \
"v_mfma_f32_16x16x32_bf16 a[244:247], v[120:123], v[168:171], a[244:247]\n" \
"ds_read_b128 v[116:119], v49 offset:12288\n" \
"v_mfma_f32_16x16x32_bf16 a[244:247], v[124:127], v[172:175], a[244:247]\n" \
"v_mfma_f32_16x16x32_bf16 a[240:243], v[120:123], v[160:163], a[240:243]\n" \
"v_mfma_f32_16x16x32_bf16 a[240:243], v[124:127], v[164:167], a[240:243]\n" \
"s_waitcnt lgkmcnt(1)\n" \
"v_mfma_f32_16x16x32_bf16 a[0:3], v[128:131], v[64:67], a[0:3]\n" \
"ds_read_b128 v[120:123], v48 offset:14336\n" \
"v_mfma_f32_16x16x32_bf16 a[0:3], v[132:135], v[68:71], a[0:3]\n" \
"v_mfma_f32_16x16x32_bf16 a[4:7], v[136:139], v[64:67], a[4:7]\n" \
"ds_read_b128 v[124:127], v49 offset:14336\n" \
"s_add_u32 m0, s95, 4096\n" \
"v_mfma_f32_16x16x32_bf16 a[4:7], v[140:143], v[68:71], a[4:7]\n" \
"global_load_lds_dwordx4 v228, s[44:45] offset:-4096\n" \
"v_mfma_f32_16x16x32_bf16 a[8:11], v[144:147], v[64:67], a[8:11]\n" \
"ds_read_b64_tr_b16 v[160:161], v50 offset:1024\n" \
"v_mfma_f32_16x16x32_bf16 a[8:11], v[148:151], v[68:71], a[8:11]\n" \
"v_mfma_f32_16x16x32_bf16 a[12:15], v[152:155], v[64:67], a[12:15]\n" \
"ds_read_b64_tr_b16 v[162:163], v50 offset:5120\n" \
"v_mfma_f32_16x16x32_bf16 a[12:15], v[156:159], v[68:71], a[12:15]\n" \
"global_load_lds_dwordx4 v229, s[44:45] offset:-3072\n" \
"v_mfma_f32_16x16x32_bf16 a[44:47], v[152:155], v[72:75], a[44:47]\n" \
"s_add_u32 s44, s44, 0x180000\n" \
"s_addc_u32 s45, s45, 0\n" \
"ds_read_b64_tr_b16 v[164:165], v50 offset:17408\n" \
"v_mfma_f32_16x16x32_bf16 a[44:47], v[156:159], v[76:79], a[44:47]\n" \
"v_mfma_f32_16x16x32_bf16 a[40:43], v[144:147], v[72:75], a[40:43]\n" \
"ds_read_b64_tr_b16 v[166:167], v50 offset:21504\n" \
"v_mfma_f32_16x16x32_bf16 a[40:43], v[148:151], v[76:79], a[40:43]\n" \
"global_load_lds_dwordx4 v228, s[42:43] offset:-2048\n" \
"v_mfma_f32_16x16x32_bf16 a[36:39], v[136:139], v[72:75], a[36:39]\n" \
"ds_read_b64_tr_b16 v[168:169], v51 offset:1024\n" \
"v_mfma_f32_16x16x32_bf16 a[36:39], v[140:143], v[76:79], a[36:39]\n" \
"v_mfma_f32_16x16x32_bf16 a[32:35], v[128:131], v[72:75], a[32:35]\n" \
"ds_read_b64_tr_b16 v[170:171], v51 offset:5120\n" \
"v_mfma_f32_16x16x32_bf16 a[32:35], v[132:135], v[76:79], a[32:35]\n" \
"global_load_lds_dwordx4 v229, s[42:43] offset:-1024\n" \
"v_mfma_f32_16x16x32_bf16 a[64:67], v[128:131], v[80:83], a[64:67]\n" \
"s_add_u32 s42, s42, 0x180000\n" \
"s_addc_u32 s43, s43, 0\n" \
"ds_read_b64_tr_b16 v[172:173], v51 offset:17408\n" \
"v_mfma_f32_16x16x32_bf16 a[64:67], v[132:135], v[84:87], a[64:67]\n" \
"v_mfma_f32_16x16x32_bf16 a[68:71], v[136:139], v[80:83], a[68:71]\n" \
"s_add_u32 m0, s95, 20480\n" \
"ds_read_b64_tr_b16 v[174:175], v51 offset:21504\n" \
"v_mfma_f32_16x16x32_bf16 a[68:71], v[140:143], v[84:87], a[68:71]\n" \
"global_load_lds_dwordx4 v228, s[44:45] offset:-4096\n" \
"v_mfma_f32_16x16x32_bf16 a[72:75], v[144:147], v[80:83], a[72:75]\n" \
"ds_read_b64_tr_b16 v[176:177], v50 offset:1536\n" \
"v_mfma_f32_16x16x32_bf16 a[72:75], v[148:151], v[84:87], a[72:75]\n" \
"v_mfma_f32_16x16x32_bf16 a[76:79], v[152:155], v[80:83], a[76:79]\n" \
"ds_read_b64_tr_b16 v[178:179], v50 offset:5632\n" \
"v_mfma_f32_16x16x32_bf16 a[76:79], v[156:159], v[84:87], a[76:79]\n" \
"Lpg1684_%=:\n" \
"Lst0x2_%=:\n" \
"s_cbranch_execz Lpg3850_%=\n" \
"global_load_lds_dwordx4 v229, s[44:45] offset:-3072\n" \
"v_mfma_f32_16x16x32_bf16 a[108:111], v[152:155], v[88:91], a[108:111]\n" \
"s_add_u32 s44, s44, 0x400000\n" \
"s_addc_u32 s45, s45, 0\n" \
"ds_read_b64_tr_b16 v[180:181], v50 offset:17920\n" \
"v_mfma_f32_16x16x32_bf16 a[108:111], v[156:159], v[92:95], a[108:111]\n" \
"v_mfma_f32_16x16x32_bf16 a[104:107], v[144:147], v[88:91], a[104:107]\n" \
"ds_read_b64_tr_b16 v[182:183], v50 offset:22016\n" \
"v_mfma_f32_16x16x32_bf16 a[104:107], v[148:151], v[92:95], a[104:107]\n" \
"global_load_lds_dwordx4 v228, s[42:43] offset:-2048\n" \
"v_mfma_f32_16x16x32_bf16 a[100:103], v[136:139], v[88:91], a[100:103]\n" \
"ds_read_b64_tr_b16 v[184:185], v51 offset:1536\n" \
"v_mfma_f32_16x16x32_bf16 a[100:103], v[140:143], v[92:95], a[100:103]\n" \
"v_mfma_f32_16x16x32_bf16 a[96:99], v[128:131], v[88:91], a[96:99]\n" \
"ds_read_b64_tr_b16 v[186:187], v51 offset:5632\n" \
"v_mfma_f32_16x16x32_bf16 a[96:99], v[132:135], v[92:95], a[96:99]\n" \
"global_load_lds_dwordx4 v229, s[42:43] offset:-1024\n" \
"v_mfma_f32_16x16x32_bf16 a[128:131], v[128:131], v[96:99], a[128:131]\n" \
"s_add_u32 s42, s42, 0x400000\n" \
"s_addc_u32 s43, s43, 0\n" \
"ds_read_b64_tr_b16 v[188:189], v51 offset:17920\n" \
"v_mfma_f32_16x16x32_bf16 a[128:131], v[132:135], v[100:103], a[128:131]\n" \
"v_mfma_f32_16x16x32_bf16 a[132:135], v[136:139], v[96:99], a[132:135]\n" \
"ds_read_b64_tr_b16 v[190:191], v51 offset:22016\n" \
"v_mfma_f32_16x16x32_bf16 a[132:135], v[140:143], v[100:103], a[132:135]\n" \
"v_mfma_f32_16x16x32_bf16 a[136:139], v[144:147], v[96:99], a[136:139]\n" \
"v_mfma_f32_16x16x32_bf16 a[136:139], v[148:151], v[100:103], a[136:139]\n" \
"v_mfma_f32_16x16x32_bf16 a[140:143], v[152:155], v[96:99], a[140:143]\n" \
"v_mfma_f32_16x16x32_bf16 a[140:143], v[156:159], v[100:103], a[140:143]\n" \
"v_mfma_f32_16x16x32_bf16 a[172:175], v[152:155], v[104:107], a[172:175]\n" \
"s_mul_i32 s76, s73, 0x0\n" \
"s_add_u32 s77, s76, 0xffff8000\n" \
"s_sub_u32 s78, 0xffff8000, s76\n" \
"v_mfma_f32_16x16x32_bf16 a[172:175], v[156:159], v[108:111], a[172:175]\n" \
"v_add_u32 v48, s77, v48\n" \
"v_add_u32 v49, s77, v49\n" \
"v_mfma_f32_16x16x32_bf16 a[168:171], v[144:147], v[104:107], a[168:171]\n" \
"v_add_u32 v50, s78, v50\n" \
"v_add_u32 v51, s78, v51\n" \
"v_mfma_f32_16x16x32_bf16 a[168:171], v[148:151], v[108:111], a[168:171]\n" \
"v_mfma_f32_16x16x32_bf16 a[164:167], v[136:139], v[104:107], a[164:167]\n" \
"v_mfma_f32_16x16x32_bf16 a[164:167], v[140:143], v[108:111], a[164:167]\n" \
"v_mfma_f32_16x16x32_bf16 a[160:163], v[128:131], v[104:107], a[160:163]\n" \
"v_mfma_f32_16x16x32_bf16 a[160:163], v[132:135], v[108:111], a[160:163]\n" \
"s_waitcnt lgkmcnt(0)\n" \
"v_mfma_f32_16x16x32_bf16 a[192:195], v[128:131], v[112:115], a[192:195]\n" \
"v_mfma_f32_16x16x32_bf16 a[192:195], v[132:135], v[116:119], a[192:195]\n" \
"v_mfma_f32_16x16x32_bf16 a[196:199], v[136:139], v[112:115], a[196:199]\n" \
"v_mfma_f32_16x16x32_bf16 a[196:199], v[140:143], v[116:119], a[196:199]\n" \
"v_mfma_f32_16x16x32_bf16 a[200:203], v[144:147], v[112:115], a[200:203]\n" \
"v_mfma_f32_16x16x32_bf16 a[200:203], v[148:151], v[116:119], a[200:203]\n" \
"v_mfma_f32_16x16x32_bf16 a[204:207], v[152:155], v[112:115], a[204:207]\n" \
"v_mfma_f32_16x16x32_bf16 a[204:207], v[156:159], v[116:119], a[204:207]\n" \
"v_mfma_f32_16x16x32_bf16 a[236:239], v[152:155], v[120:123], a[236:239]\n" \
"v_mfma_f32_16x16x32_bf16 a[236:239], v[156:159], v[124:127], a[236:239]\n" \
"v_mfma_f32_16x16x32_bf16 a[232:235], v[144:147], v[120:123], a[232:235]\n" \
"v_mfma_f32_16x16x32_bf16 a[232:235], v[148:151], v[124:127], a[232:235]\n" \
"v_mfma_f32_16x16x32_bf16 a[228:231], v[136:139], v[120:123], a[228:231]\n" \
"v_mfma_f32_16x16x32_bf16 a[228:231], v[140:143], v[124:127], a[228:231]\n" \
"v_mfma_f32_16x16x32_bf16 a[224:227], v[128:131], v[120:123], a[224:227]\n" \
"s_waitcnt vmcnt(10) lgkmcnt(0)\n" \
"v_mfma_f32_16x16x32_bf16 a[224:227], v[132:135], v[124:127], a[224:227]\n" \
"s_barrier\n" \
"v_mfma_f32_16x16x32_bf16 a[16:19], v[64:67], v[160:163], a[16:19]\n" \
"s_add_u32 m0, s87, 135168\n" \
"ds_read_b64_tr_b16 v[128:129], v50 offset:0\n" \
"v_mfma_f32_16x16x32_bf16 a[16:19], v[68:71], v[164:167], a[16:19]\n" \
"global_load_lds_dwordx4 v224, s[48:49] offset:-4096\n" \
"v_mfma_f32_16x16x32_bf16 a[20:23], v[64:67], v[168:171], a[20:23]\n" \
"ds_read_b64_tr_b16 v[130:131], v50 offset:4096\n" \
"v_mfma_f32_16x16x32_bf16 a[20:23], v[68:71], v[172:175], a[20:23]\n" \
"v_mfma_f32_16x16x32_bf16 a[24:27], v[64:67], v[176:179], a[24:27]\n" \
"ds_read_b64_tr_b16 v[132:133], v50 offset:16384\n" \
"v_mfma_f32_16x16x32_bf16 a[24:27], v[68:71], v[180:183], a[24:27]\n" \
"global_load_lds_dwordx4 v225, s[48:49] offset:-3072\n" \
"v_mfma_f32_16x16x32_bf16 a[28:31], v[64:67], v[184:187], a[28:31]\n" \
"ds_read_b64_tr_b16 v[134:135], v50 offset:20480\n" \
"v_mfma_f32_16x16x32_bf16 a[28:31], v[68:71], v[188:191], a[28:31]\n" \
"v_mfma_f32_16x16x32_bf16 a[60:63], v[72:75], v[184:187], a[60:63]\n" \
"ds_read_b128 v[64:67], v48 offset:0\n" \
"v_mfma_f32_16x16x32_bf16 a[60:63], v[76:79], v[188:191], a[60:63]\n" \
"global_load_lds_dwordx4 v226, s[48:49] offset:-2048\n" \
"v_mfma_f32_16x16x32_bf16 a[56:59], v[72:75], v[176:179], a[56:59]\n" \
"ds_read_b64_tr_b16 v[136:137], v51 offset:0\n" \
"v_mfma_f32_16x16x32_bf16 a[56:59], v[76:79], v[180:183], a[56:59]\n" \
"v_mfma_f32_16x16x32_bf16 a[52:55], v[72:75], v[168:171], a[52:55]\n" \
"ds_read_b128 v[68:71], v49 offset:0\n" \
"v_mfma_f32_16x16x32_bf16 a[52:55], v[76:79], v[172:175], a[52:55]\n" \
"global_load_lds_dwordx4 v227, s[48:49] offset:-1024\n" \
"v_mfma_f32_16x16x32_bf16 a[48:51], v[72:75], v[160:163], a[48:51]\n" \
"s_add_u32 s48, s48, 0x180\n" \
"s_addc_u32 s49, s49, 0\n" \
"ds_read_b64_tr_b16 v[138:139], v51 offset:4096\n" \
"v_mfma_f32_16x16x32_bf16 a[48:51], v[76:79], v[164:167], a[48:51]\n" \
"v_mfma_f32_16x16x32_bf16 a[80:83], v[80:83], v[160:163], a[80:83]\n" \
"ds_read_b128 v[72:75], v48 offset:2048\n" \
"v_mfma_f32_16x16x32_bf16 a[80:83], v[84:87], v[164:167], a[80:83]\n" \
"global_load_lds_dwordx4 v224, s[46:47] offset:0\n" \
"v_mfma_f32_16x16x32_bf16 a[84:87], v[80:83], v[168:171], a[84:87]\n" \
"ds_read_b64_tr_b16 v[140:141], v51 offset:16384\n" \
"v_mfma_f32_16x16x32_bf16 a[84:87], v[84:87], v[172:175], a[84:87]\n" \
"v_mfma_f32_16x16x32_bf16 a[88:91], v[80:83], v[176:179], a[88:91]\n" \
"ds_read_b128 v[76:79], v49 offset:2048\n" \
"v_mfma_f32_16x16x32_bf16 a[88:91], v[84:87], v[180:183], a[88:91]\n" \
"global_load_lds_dwordx4 v225, s[46:47] offset:1024\n" \
"v_mfma_f32_16x16x32_bf16 a[92:95], v[80:83], v[184:187], a[92:95]\n" \
"ds_read_b64_tr_b16 v[142:143], v51 offset:20480\n" \
"v_mfma_f32_16x16x32_bf16 a[92:95], v[84:87], v[188:191], a[92:95]\n" \
"v_mfma_f32_16x16x32_bf16 a[124:127], v[88:91], v[184:187], a[124:127]\n" \
"ds_read_b128 v[80:83], v48 offset:4096\n" \
"v_mfma_f32_16x16x32_bf16 a[124:127], v[92:95], v[188:191], a[124:127]\n" \
"global_load_lds_dwordx4 v226, s[46:47] offset:2048\n" \
"v_mfma_f32_16x16x32_bf16 a[120:123], v[88:91], v[176:179], a[120:123]\n" \
"ds_read_b64_tr_b16 v[144:145], v50 offset:512\n" \
"v_mfma_f32_16x16x32_bf16 a[120:123], v[92:95], v[180:183], a[120:123]\n" \
"v_mfma_f32_16x16x32_bf16 a[116:119], v[88:91], v[168:171], a[116:119]\n" \
"ds_read_b128 v[84:87], v49 offset:4096\n" \
"v_mfma_f32_16x16x32_bf16 a[116:119], v[92:95], v[172:175], a[116:119]\n" \
"global_load_lds_dwordx4 v227, s[46:47] offset:3072\n" \
"v_mfma_f32_16x16x32_bf16 a[112:115], v[88:91], v[160:163], a[112:115]\n" \
"s_add_u32 s46, s46, 0x180\n" \
"s_addc_u32 s47, s47, 0\n" \
"ds_read_b64_tr_b16 v[146:147], v50 offset:4608\n" \
"v_mfma_f32_16x16x32_bf16 a[112:115], v[92:95], v[164:167], a[112:115]\n" \
"s_add_u32 s68, s67, 0x600000\n" \
"s_add_u32 s64, s12, s68\n" \
"s_addc_u32 s65, s13, 0\n" \
"v_mfma_f32_16x16x32_bf16 a[144:147], v[96:99], v[160:163], a[144:147]\n" \
"ds_read_b128 v[88:91], v48 offset:6144\n" \
"v_mfma_f32_16x16x32_bf16 a[144:147], v[100:103], v[164:167], a[144:147]\n" \
"global_load_dword v253, v248, s[64:65]\n" \
"v_mfma_f32_16x16x32_bf16 a[148:151], v[96:99], v[168:171], a[148:151]\n" \
"s_add_u32 s68, s67, 0x700000\n" \
"ds_read_b64_tr_b16 v[148:149], v50 offset:16896\n" \
"v_mfma_f32_16x16x32_bf16 a[148:151], v[100:103], v[172:175], a[148:151]\n" \
"s_add_u32 s64, s12, s68\n" \
"s_addc_u32 s65, s13, 0\n" \
"v_mfma_f32_16x16x32_bf16 a[152:155], v[96:99], v[176:179], a[152:155]\n" \
"ds_read_b128 v[92:95], v49 offset:6144\n" \
"v_mfma_f32_16x16x32_bf16 a[152:155], v[100:103], v[180:183], a[152:155]\n" \
"global_load_dword v253, v248, s[64:65]\n" \
"v_mfma_f32_16x16x32_bf16 a[156:159], v[96:99], v[184:187], a[156:159]\n" \
"s_lshr_b32 s68, s66, 2\n" \
"s_and_b32 s68, s68, 0xffff\n" \
"ds_read_b64_tr_b16 v[150:151], v50 offset:20992\n" \
"v_mfma_f32_16x16x32_bf16 a[156:159], v[100:103], v[188:191], a[156:159]\n" \
"s_add_u32 s68, s68, 65585152\n" \
"v_mfma_f32_16x16x32_bf16 a[188:191], v[104:107], v[184:187], a[188:191]\n" \
"ds_read_b128 v[96:99], v48 offset:8192\n" \
"v_mfma_f32_16x16x32_bf16 a[188:191], v[108:111], v[188:191], a[188:191]\n" \
"buffer_load_dword v253, v245, s[56:59], s68 offen sc1\n" \
"v_mfma_f32_16x16x32_bf16 a[184:187], v[104:107], v[176:179], a[184:187]\n" \
"ds_read_b64_tr_b16 v[152:153], v51 offset:512\n" \
"v_mfma_f32_16x16x32_bf16 a[184:187], v[108:111], v[180:183], a[184:187]\n" \
"v_mfma_f32_16x16x32_bf16 a[180:183], v[104:107], v[168:171], a[180:183]\n" \
"ds_read_b128 v[100:103], v49 offset:8192\n" \
"v_mfma_f32_16x16x32_bf16 a[180:183], v[108:111], v[172:175], a[180:183]\n" \
"v_mfma_f32_16x16x32_bf16 a[176:179], v[104:107], v[160:163], a[176:179]\n" \
"ds_read_b64_tr_b16 v[154:155], v51 offset:4608\n" \
"v_mfma_f32_16x16x32_bf16 a[176:179], v[108:111], v[164:167], a[176:179]\n" \
"v_mfma_f32_16x16x32_bf16 a[208:211], v[112:115], v[160:163], a[208:211]\n" \
"ds_read_b128 v[104:107], v48 offset:10240\n" \
"v_mfma_f32_16x16x32_bf16 a[208:211], v[116:119], v[164:167], a[208:211]\n" \
"v_mfma_f32_16x16x32_bf16 a[212:215], v[112:115], v[168:171], a[212:215]\n" \
"ds_read_b64_tr_b16 v[156:157], v51 offset:16896\n" \
"v_mfma_f32_16x16x32_bf16 a[212:215], v[116:119], v[172:175], a[212:215]\n" \
"v_mfma_f32_16x16x32_bf16 a[216:219], v[112:115], v[176:179], a[216:219]\n" \
"ds_read_b128 v[108:111], v49 offset:10240\n" \
"v_mfma_f32_16x16x32_bf16 a[216:219], v[116:119], v[180:183], a[216:219]\n" \
"v_mfma_f32_16x16x32_bf16 a[220:223], v[112:115], v[184:187], a[220:223]\n" \
"ds_read_b64_tr_b16 v[158:159], v51 offset:20992\n" \
"v_mfma_f32_16x16x32_bf16 a[220:223], v[116:119], v[188:191], a[220:223]\n" \
"v_mfma_f32_16x16x32_bf16 a[252:255], v[120:123], v[184:187], a[252:255]\n" \
"ds_read_b128 v[112:115], v48 offset:12288\n" \
"v_mfma_f32_16x16x32_bf16 a[252:255], v[124:127], v[188:191], a[252:255]\n" \
"v_mfma_f32_16x16x32_bf16 a[248:251], v[120:123], v[176:179], a[248:251]\n" \
"v_mfma_f32_16x16x32_bf16 a[248:251], v[124:127], v[180:183], a[248:251]\n" \
"v_mfma_f32_16x16x32_bf16 a[244:247], v[120:123], v[168:171], a[244:247]\n" \
"ds_read_b128 v[116:119], v49 offset:12288\n" \
"v_mfma_f32_16x16x32_bf16 a[244:247], v[124:127], v[172:175], a[244:247]\n" \
"v_mfma_f32_16x16x32_bf16 a[240:243], v[120:123], v[160:163], a[240:243]\n" \
"v_mfma_f32_16x16x32_bf16 a[240:243], v[124:127], v[164:167], a[240:243]\n" \
"s_waitcnt lgkmcnt(1)\n" \
"v_mfma_f32_16x16x32_bf16 a[0:3], v[128:131], v[64:67], a[0:3]\n" \
"ds_read_b128 v[120:123], v48 offset:14336\n" \
"v_mfma_f32_16x16x32_bf16 a[0:3], v[132:135], v[68:71], a[0:3]\n" \
"v_mfma_f32_16x16x32_bf16 a[4:7], v[136:139], v[64:67], a[4:7]\n" \
"ds_read_b128 v[124:127], v49 offset:14336\n" \
"s_add_u32 m0, s87, 69632\n" \
"v_mfma_f32_16x16x32_bf16 a[4:7], v[140:143], v[68:71], a[4:7]\n" \
"global_load_lds_dwordx4 v224, s[48:49] offset:-4096\n" \
"v_mfma_f32_16x16x32_bf16 a[8:11], v[144:147], v[64:67], a[8:11]\n" \
"ds_read_b64_tr_b16 v[160:161], v50 offset:1024\n" \
"v_mfma_f32_16x16x32_bf16 a[8:11], v[148:151], v[68:71], a[8:11]\n" \
"v_mfma_f32_16x16x32_bf16 a[12:15], v[152:155], v[64:67], a[12:15]\n" \
"ds_read_b64_tr_b16 v[162:163], v50 offset:5120\n" \
"v_mfma_f32_16x16x32_bf16 a[12:15], v[156:159], v[68:71], a[12:15]\n" \
"global_load_lds_dwordx4 v225, s[48:49] offset:-3072\n" \
"v_mfma_f32_16x16x32_bf16 a[44:47], v[152:155], v[72:75], a[44:47]\n" \
"ds_read_b64_tr_b16 v[164:165], v50 offset:17408\n" \
"v_mfma_f32_16x16x32_bf16 a[44:47], v[156:159], v[76:79], a[44:47]\n" \
"v_mfma_f32_16x16x32_bf16 a[40:43], v[144:147], v[72:75], a[40:43]\n" \
"ds_read_b64_tr_b16 v[166:167], v50 offset:21504\n" \
"v_mfma_f32_16x16x32_bf16 a[40:43], v[148:151], v[76:79], a[40:43]\n" \
"global_load_lds_dwordx4 v226, s[48:49] offset:-2048\n" \
"v_mfma_f32_16x16x32_bf16 a[36:39], v[136:139], v[72:75], a[36:39]\n" \
"ds_read_b64_tr_b16 v[168:169], v51 offset:1024\n" \
"v_mfma_f32_16x16x32_bf16 a[36:39], v[140:143], v[76:79], a[36:39]\n" \
"v_mfma_f32_16x16x32_bf16 a[32:35], v[128:131], v[72:75], a[32:35]\n" \
"ds_read_b64_tr_b16 v[170:171], v51 offset:5120\n" \
"v_mfma_f32_16x16x32_bf16 a[32:35], v[132:135], v[76:79], a[32:35]\n" \
"global_load_lds_dwordx4 v227, s[48:49] offset:-1024\n" \
"v_mfma_f32_16x16x32_bf16 a[64:67], v[128:131], v[80:83], a[64:67]\n" \
"s_add_u32 s48, s48, 0x400\n" \
"s_addc_u32 s49, s49, 0\n" \
"ds_read_b64_tr_b16 v[172:173], v51 offset:17408\n" \
"v_mfma_f32_16x16x32_bf16 a[64:67], v[132:135], v[84:87], a[64:67]\n" \
"v_mfma_f32_16x16x32_bf16 a[68:71], v[136:139], v[80:83], a[68:71]\n" \
"ds_read_b64_tr_b16 v[174:175], v51 offset:21504\n" \
"v_mfma_f32_16x16x32_bf16 a[68:71], v[140:143], v[84:87], a[68:71]\n" \
"global_load_lds_dwordx4 v224, s[46:47] offset:0\n" \
"v_mfma_f32_16x16x32_bf16 a[72:75], v[144:147], v[80:83], a[72:75]\n" \
"ds_read_b64_tr_b16 v[176:177], v50 offset:1536\n" \
"v_mfma_f32_16x16x32_bf16 a[72:75], v[148:151], v[84:87], a[72:75]\n" \
"v_mfma_f32_16x16x32_bf16 a[76:79], v[152:155], v[80:83], a[76:79]\n" \
"ds_read_b64_tr_b16 v[178:179], v50 offset:5632\n" \
"v_mfma_f32_16x16x32_bf16 a[76:79], v[156:159], v[84:87], a[76:79]\n" \
"global_load_lds_dwordx4 v225, s[46:47] offset:1024\n" \
"v_mfma_f32_16x16x32_bf16 a[108:111], v[152:155], v[88:91], a[108:111]\n" \
"ds_read_b64_tr_b16 v[180:181], v50 offset:17920\n" \
"v_mfma_f32_16x16x32_bf16 a[108:111], v[156:159], v[92:95], a[108:111]\n" \
"v_mfma_f32_16x16x32_bf16 a[104:107], v[144:147], v[88:91], a[104:107]\n" \
"ds_read_b64_tr_b16 v[182:183], v50 offset:22016\n" \
"v_mfma_f32_16x16x32_bf16 a[104:107], v[148:151], v[92:95], a[104:107]\n" \
"global_load_lds_dwordx4 v226, s[46:47] offset:2048\n" \
"v_mfma_f32_16x16x32_bf16 a[100:103], v[136:139], v[88:91], a[100:103]\n" \
"ds_read_b64_tr_b16 v[184:185], v51 offset:1536\n" \
"v_mfma_f32_16x16x32_bf16 a[100:103], v[140:143], v[92:95], a[100:103]\n" \
"v_mfma_f32_16x16x32_bf16 a[96:99], v[128:131], v[88:91], a[96:99]\n" \
"ds_read_b64_tr_b16 v[186:187], v51 offset:5632\n" \
"v_mfma_f32_16x16x32_bf16 a[96:99], v[132:135], v[92:95], a[96:99]\n" \
"global_load_lds_dwordx4 v227, s[46:47] offset:3072\n" \
"v_mfma_f32_16x16x32_bf16 a[128:131], v[128:131], v[96:99], a[128:131]\n" \
"s_add_u32 s46, s46, 0x400\n" \
"s_addc_u32 s47, s47, 0\n" \
"ds_read_b64_tr_b16 v[188:189], v51 offset:17920\n" \
"v_mfma_f32_16x16x32_bf16 a[128:131], v[132:135], v[100:103], a[128:131]\n" \
"v_mfma_f32_16x16x32_bf16 a[132:135], v[136:139], v[96:99], a[132:135]\n" \
"ds_read_b64_tr_b16 v[190:191], v51 offset:22016\n" \
"v_mfma_f32_16x16x32_bf16 a[132:135], v[140:143], v[100:103], a[132:135]\n" \
"v_mfma_f32_16x16x32_bf16 a[136:139], v[144:147], v[96:99], a[136:139]\n" \
"v_mfma_f32_16x16x32_bf16 a[136:139], v[148:151], v[100:103], a[136:139]\n" \
"v_mfma_f32_16x16x32_bf16 a[140:143], v[152:155], v[96:99], a[140:143]\n" \
"v_mfma_f32_16x16x32_bf16 a[140:143], v[156:159], v[100:103], a[140:143]\n" \
"v_mfma_f32_16x16x32_bf16 a[172:175], v[152:155], v[104:107], a[172:175]\n" \
"v_mfma_f32_16x16x32_bf16 a[172:175], v[156:159], v[108:111], a[172:175]\n" \
"v_mfma_f32_16x16x32_bf16 a[168:171], v[144:147], v[104:107], a[168:171]\n" \
"v_mfma_f32_16x16x32_bf16 a[168:171], v[148:151], v[108:111], a[168:171]\n" \
"v_mfma_f32_16x16x32_bf16 a[164:167], v[136:139], v[104:107], a[164:167]\n" \
"v_mfma_f32_16x16x32_bf16 a[164:167], v[140:143], v[108:111], a[164:167]\n" \
"v_mfma_f32_16x16x32_bf16 a[160:163], v[128:131], v[104:107], a[160:163]\n" \
"v_mfma_f32_16x16x32_bf16 a[160:163], v[132:135], v[108:111], a[160:163]\n" \
"s_waitcnt lgkmcnt(0)\n" \
"v_mfma_f32_16x16x32_bf16 a[192:195], v[128:131], v[112:115], a[192:195]\n" \
"v_mfma_f32_16x16x32_bf16 a[192:195], v[132:135], v[116:119], a[192:195]\n" \
"v_mfma_f32_16x16x32_bf16 a[196:199], v[136:139], v[112:115], a[196:199]\n" \
"v_mfma_f32_16x16x32_bf16 a[196:199], v[140:143], v[116:119], a[196:199]\n" \
"v_mfma_f32_16x16x32_bf16 a[200:203], v[144:147], v[112:115], a[200:203]\n" \
"v_mfma_f32_16x16x32_bf16 a[200:203], v[148:151], v[116:119], a[200:203]\n" \
"v_mfma_f32_16x16x32_bf16 a[204:207], v[152:155], v[112:115], a[204:207]\n" \
"v_mfma_f32_16x16x32_bf16 a[204:207], v[156:159], v[116:119], a[204:207]\n" \
"v_mfma_f32_16x16x32_bf16 a[236:239], v[152:155], v[120:123], a[236:239]\n" \
"v_mfma_f32_16x16x32_bf16 a[236:239], v[156:159], v[124:127], a[236:239]\n" \
"v_mfma_f32_16x16x32_bf16 a[232:235], v[144:147], v[120:123], a[232:235]\n" \
"v_mfma_f32_16x16x32_bf16 a[232:235], v[148:151], v[124:127], a[232:235]\n" \
"v_mfma_f32_16x16x32_bf16 a[228:231], v[136:139], v[120:123], a[228:231]\n" \
"v_mfma_f32_16x16x32_bf16 a[228:231], v[140:143], v[124:127], a[228:231]\n" \
"v_mfma_f32_16x16x32_bf16 a[224:227], v[128:131], v[120:123], a[224:227]\n" \
"s_waitcnt vmcnt(11) lgkmcnt(0)\n" \
"v_mfma_f32_16x16x32_bf16 a[224:227], v[132:135], v[124:127], a[224:227]\n" \
"s_barrier\n" \
"v_mfma_f32_16x16x32_bf16 a[16:19], v[64:67], v[160:163], a[16:19]\n" \
"s_add_u32 m0, s87, 102400\n" \
"v_mfma_f32_16x16x32_bf16 a[16:19], v[68:71], v[164:167], a[16:19]\n" \
"global_load_lds_dwordx4 v224, s[48:49] offset:-4096\n" \
"v_mfma_f32_16x16x32_bf16 a[20:23], v[64:67], v[168:171], a[20:23]\n" \
"ds_read_b64_tr_b16 v[0:1], v232 offset:0\n" \
"v_mfma_f32_16x16x32_bf16 a[20:23], v[68:71], v[172:175], a[20:23]\n" \
"v_accvgpr_read_b32 v234, a192\n" \
"v_accvgpr_read_b32 v235, a193\n" \
"v_mfma_f32_16x16x32_bf16 a[24:27], v[64:67], v[176:179], a[24:27]\n" \
"ds_read_b64_tr_b16 v[2:3], v232 offset:2048\n" \
"v_accvgpr_read_b32 v236, a194\n" \
"v_mfma_f32_16x16x32_bf16 a[24:27], v[68:71], v[180:183], a[24:27]\n" \
"global_load_lds_dwordx4 v225, s[48:49] offset:-3072\n" \
"v_accvgpr_read_b32 v237, a195\n" \
"v_mfma_f32_16x16x32_bf16 a[28:31], v[64:67], v[184:187], a[28:31]\n" \
"ds_read_b64_tr_b16 v[4:5], v232 offset:8192\n" \
"v_mul_f32 v234, s92, v234\n" \
"v_mfma_f32_16x16x32_bf16 a[28:31], v[68:71], v[188:191], a[28:31]\n" \
"v_mul_f32 v235, s92, v235\n" \
"v_mul_f32 v236, s92, v236\n" \
"v_mfma_f32_16x16x32_bf16 a[60:63], v[72:75], v[184:187], a[60:63]\n" \
"ds_read_b64_tr_b16 v[6:7], v232 offset:10240\n" \
"v_mul_f32 v237, s92, v237\n" \
"v_mfma_f32_16x16x32_bf16 a[60:63], v[76:79], v[188:191], a[60:63]\n" \
"global_load_lds_dwordx4 v226, s[48:49] offset:-2048\n" \
"v_cvt_pk_bf16_f32 v234, v234, v235\n" \
"v_mfma_f32_16x16x32_bf16 a[56:59], v[72:75], v[176:179], a[56:59]\n" \
"ds_read_b64_tr_b16 v[8:9], v233 offset:0\n" \
"v_cvt_pk_bf16_f32 v236, v236, v237\n" \
"v_mfma_f32_16x16x32_bf16 a[56:59], v[76:79], v[180:183], a[56:59]\n" \
"v_accvgpr_write_b32 a192, v234\n" \
"v_accvgpr_write_b32 a193, v236\n" \
"v_mfma_f32_16x16x32_bf16 a[52:55], v[72:75], v[168:171], a[52:55]\n" \
"ds_read_b64_tr_b16 v[10:11], v233 offset:2048\n" \
"v_accvgpr_read_b32 v234, a196\n" \
"v_mfma_f32_16x16x32_bf16 a[52:55], v[76:79], v[172:175], a[52:55]\n" \
"global_load_lds_dwordx4 v227, s[48:49] offset:-1024\n" \
"v_accvgpr_read_b32 v235, a197\n" \
"v_mfma_f32_16x16x32_bf16 a[48:51], v[72:75], v[160:163], a[48:51]\n" \
"s_add_u32 s48, s48, 0xfffffb00\n" \
"s_addc_u32 s49, s49, -1\n" \
"ds_read_b64_tr_b16 v[12:13], v233 offset:8192\n" \
"v_mfma_f32_16x16x32_bf16 a[48:51], v[76:79], v[164:167], a[48:51]\n" \
"v_accvgpr_read_b32 v236, a198\n" \
"v_accvgpr_read_b32 v237, a199\n" \
"v_mfma_f32_16x16x32_bf16 a[80:83], v[80:83], v[160:163], a[80:83]\n" \
"ds_read_b64_tr_b16 v[14:15], v233 offset:10240\n" \
"v_mul_f32 v234, s92, v234\n" \
"v_mfma_f32_16x16x32_bf16 a[80:83], v[84:87], v[164:167], a[80:83]\n" \
"global_load_lds_dwordx4 v224, s[46:47] offset:0\n" \
"v_mul_f32 v235, s92, v235\n" \
"v_mfma_f32_16x16x32_bf16 a[84:87], v[80:83], v[168:171], a[84:87]\n" \
"ds_read_b64_tr_b16 v[16:17], v232 offset:512\n" \
"v_mul_f32 v236, s92, v236\n" \
"v_mfma_f32_16x16x32_bf16 a[84:87], v[84:87], v[172:175], a[84:87]\n" \
"v_mul_f32 v237, s92, v237\n" \
"v_cvt_pk_bf16_f32 v234, v234, v235\n" \
"v_mfma_f32_16x16x32_bf16 a[88:91], v[80:83], v[176:179], a[88:91]\n" \
"ds_read_b64_tr_b16 v[18:19], v232 offset:2560\n" \
"v_cvt_pk_bf16_f32 v236, v236, v237\n" \
"v_mfma_f32_16x16x32_bf16 a[88:91], v[84:87], v[180:183], a[88:91]\n" \
"global_load_lds_dwordx4 v225, s[46:47] offset:1024\n" \
"v_accvgpr_write_b32 a194, v234\n" \
"v_mfma_f32_16x16x32_bf16 a[92:95], v[80:83], v[184:187], a[92:95]\n" \
"ds_read_b64_tr_b16 v[20:21], v232 offset:8704\n" \
"v_accvgpr_write_b32 a195, v236\n" \
"v_mfma_f32_16x16x32_bf16 a[92:95], v[84:87], v[188:191], a[92:95]\n" \
"v_accvgpr_read_b32 v234, a224\n" \
"v_accvgpr_read_b32 v235, a225\n" \
"v_mfma_f32_16x16x32_bf16 a[124:127], v[88:91], v[184:187], a[124:127]\n" \
"ds_read_b64_tr_b16 v[22:23], v232 offset:10752\n" \
"v_accvgpr_read_b32 v236, a226\n" \
"v_mfma_f32_16x16x32_bf16 a[124:127], v[92:95], v[188:191], a[124:127]\n" \
"global_load_lds_dwordx4 v226, s[46:47] offset:2048\n" \
"v_accvgpr_read_b32 v237, a227\n" \
"v_mfma_f32_16x16x32_bf16 a[120:123], v[88:91], v[176:179], a[120:123]\n" \
"ds_read_b64_tr_b16 v[24:25], v233 offset:512\n" \
"v_mul_f32 v234, s92, v234\n" \
"v_mfma_f32_16x16x32_bf16 a[120:123], v[92:95], v[180:183], a[120:123]\n" \
"v_mul_f32 v235, s92, v235\n" \
"v_mul_f32 v236, s92, v236\n" \
"v_mfma_f32_16x16x32_bf16 a[116:119], v[88:91], v[168:171], a[116:119]\n" \
"ds_read_b64_tr_b16 v[26:27], v233 offset:2560\n" \
"v_mul_f32 v237, s92, v237\n" \
"v_mfma_f32_16x16x32_bf16 a[116:119], v[92:95], v[172:175], a[116:119]\n" \
"global_load_lds_dwordx4 v227, s[46:47] offset:3072\n" \
"v_cvt_pk_bf16_f32 v234, v234, v235\n" \
"v_mfma_f32_16x16x32_bf16 a[112:115], v[88:91], v[160:163], a[112:115]\n" \
"s_add_u32 s46, s46, 0xfffffb00\n" \
"s_addc_u32 s47, s47, -1\n" \
"ds_read_b64_tr_b16 v[28:29], v233 offset:8704\n" \
"v_mfma_f32_16x16x32_bf16 a[112:115], v[92:95], v[164:167], a[112:115]\n" \
"v_cvt_pk_bf16_f32 v236, v236, v237\n" \
"v_accvgpr_write_b32 a224, v234\n" \
"v_mfma_f32_16x16x32_bf16 a[144:147], v[96:99], v[160:163], a[144:147]\n" \
"s_add_u32 m0, s95, 36864\n" \
"ds_read_b64_tr_b16 v[30:31], v233 offset:10752\n" \
"v_accvgpr_write_b32 a225, v236\n" \
"v_mfma_f32_16x16x32_bf16 a[144:147], v[100:103], v[164:167], a[144:147]\n" \
"global_load_lds_dwordx4 v228, s[44:45] offset:-4096\n" \
"v_accvgpr_read_b32 v234, a228\n" \
"v_mfma_f32_16x16x32_bf16 a[148:151], v[96:99], v[168:171], a[148:151]\n" \
"ds_read_b128 v[192:195], v230 offset:0\n" \
"v_accvgpr_read_b32 v235, a229\n" \
"v_mfma_f32_16x16x32_bf16 a[148:151], v[100:103], v[172:175], a[148:151]\n" \
"v_accvgpr_read_b32 v236, a230\n" \
"v_accvgpr_read_b32 v237, a231\n" \
"v_mfma_f32_16x16x32_bf16 a[152:155], v[96:99], v[176:179], a[152:155]\n" \
"ds_read_b128 v[196:199], v231 offset:0\n" \
"v_mul_f32 v234, s92, v234\n" \
"v_mfma_f32_16x16x32_bf16 a[152:155], v[100:103], v[180:183], a[152:155]\n" \
"global_load_lds_dwordx4 v229, s[44:45] offset:-3072\n" \
"v_mul_f32 v235, s92, v235\n" \
"v_mfma_f32_16x16x32_bf16 a[156:159], v[96:99], v[184:187], a[156:159]\n" \
"s_add_u32 s44, s44, 0xffb00000\n" \
"s_addc_u32 s45, s45, -1\n" \
"ds_read_b128 v[200:203], v230 offset:2048\n" \
"v_mfma_f32_16x16x32_bf16 a[156:159], v[100:103], v[188:191], a[156:159]\n" \
"v_mul_f32 v236, s92, v236\n" \
"v_mul_f32 v237, s92, v237\n" \
"v_mfma_f32_16x16x32_bf16 a[188:191], v[104:107], v[184:187], a[188:191]\n" \
"ds_read_b128 v[204:207], v231 offset:2048\n" \
"v_cvt_pk_bf16_f32 v234, v234, v235\n" \
"v_mfma_f32_16x16x32_bf16 a[188:191], v[108:111], v[188:191], a[188:191]\n" \
"global_load_lds_dwordx4 v228, s[42:43] offset:-2048\n" \
"v_cvt_pk_bf16_f32 v236, v236, v237\n" \
"v_mfma_f32_16x16x32_bf16 a[184:187], v[104:107], v[176:179], a[184:187]\n" \
"ds_read_b128 v[208:211], v230 offset:4096\n" \
"v_accvgpr_write_b32 a226, v234\n" \
"v_mfma_f32_16x16x32_bf16 a[184:187], v[108:111], v[180:183], a[184:187]\n" \
"v_accvgpr_write_b32 a227, v236\n" \
"v_accvgpr_read_b32 v234, a200\n" \
"v_mfma_f32_16x16x32_bf16 a[180:183], v[104:107], v[168:171], a[180:183]\n" \
"ds_read_b128 v[212:215], v231 offset:4096\n" \
"v_accvgpr_read_b32 v235, a201\n" \
"v_mfma_f32_16x16x32_bf16 a[180:183], v[108:111], v[172:175], a[180:183]\n" \
"global_load_lds_dwordx4 v229, s[42:43] offset:-1024\n" \
"v_accvgpr_read_b32 v236, a202\n" \
"v_mfma_f32_16x16x32_bf16 a[176:179], v[104:107], v[160:163], a[176:179]\n" \
"s_add_u32 s42, s42, 0xffb00000\n" \
"s_addc_u32 s43, s43, -1\n" \
"v_accvgpr_read_b32 v237, a203\n" \
"v_mfma_f32_16x16x32_bf16 a[176:179], v[108:111], v[164:167], a[176:179]\n" \
"v_mul_f32 v234, s92, v234\n" \
"v_mul_f32 v235, s92, v235\n" \
"v_mfma_f32_16x16x32_bf16 a[208:211], v[112:115], v[160:163], a[208:211]\n" \
"s_add_u32 m0, s95, 53248\n" \
"v_mul_f32 v236, s92, v236\n" \
"v_mul_f32 v237, s92, v237\n" \
"v_mfma_f32_16x16x32_bf16 a[208:211], v[116:119], v[164:167], a[208:211]\n" \
"global_load_lds_dwordx4 v228, s[44:45] offset:-4096\n" \
"v_cvt_pk_bf16_f32 v234, v234, v235\n" \
"v_mfma_f32_16x16x32_bf16 a[212:215], v[112:115], v[168:171], a[212:215]\n" \
"v_cvt_pk_bf16_f32 v236, v236, v237\n" \
"v_accvgpr_write_b32 a200, v234\n" \
"v_mfma_f32_16x16x32_bf16 a[212:215], v[116:119], v[172:175], a[212:215]\n" \
"v_accvgpr_write_b32 a201, v236\n" \
"v_accvgpr_read_b32 v234, a204\n" \
"v_mfma_f32_16x16x32_bf16 a[216:219], v[112:115], v[176:179], a[216:219]\n" \
"v_accvgpr_read_b32 v235, a205\n" \
"v_accvgpr_read_b32 v236, a206\n" \
"v_mfma_f32_16x16x32_bf16 a[216:219], v[116:119], v[180:183], a[216:219]\n" \
"global_load_lds_dwordx4 v229, s[44:45] offset:-3072\n" \
"v_accvgpr_read_b32 v237, a207\n" \
"v_mfma_f32_16x16x32_bf16 a[220:223], v[112:115], v[184:187], a[220:223]\n" \
"s_add_u32 s44, s44, 0x80000\n" \
"s_addc_u32 s45, s45, 0\n" \
"v_mul_f32 v234, s92, v234\n" \
"v_mfma_f32_16x16x32_bf16 a[220:223], v[116:119], v[188:191], a[220:223]\n" \
"v_mul_f32 v235, s92, v235\n" \
"v_mul_f32 v236, s92, v236\n" \
"v_mfma_f32_16x16x32_bf16 a[252:255], v[120:123], v[184:187], a[252:255]\n" \
"v_mul_f32 v237, s92, v237\n" \
"v_cvt_pk_bf16_f32 v234, v234, v235\n" \
"v_mfma_f32_16x16x32_bf16 a[252:255], v[124:127], v[188:191], a[252:255]\n" \
"global_load_lds_dwordx4 v228, s[42:43] offset:-2048\n" \
"v_cvt_pk_bf16_f32 v236, v236, v237\n" \
"v_mfma_f32_16x16x32_bf16 a[248:251], v[120:123], v[176:179], a[248:251]\n" \
"v_accvgpr_write_b32 a202, v234\n" \
"v_accvgpr_write_b32 a203, v236\n" \
"v_mfma_f32_16x16x32_bf16 a[248:251], v[124:127], v[180:183], a[248:251]\n" \
"v_accvgpr_read_b32 v234, a232\n" \
"v_accvgpr_read_b32 v235, a233\n" \
"v_mfma_f32_16x16x32_bf16 a[244:247], v[120:123], v[168:171], a[244:247]\n" \
"v_accvgpr_read_b32 v236, a234\n" \
"v_accvgpr_read_b32 v237, a235\n" \
"v_mfma_f32_16x16x32_bf16 a[244:247], v[124:127], v[172:175], a[244:247]\n" \
"global_load_lds_dwordx4 v229, s[42:43] offset:-1024\n" \
"v_mul_f32 v234, s92, v234\n" \
"v_mfma_f32_16x16x32_bf16 a[240:243], v[120:123], v[160:163], a[240:243]\n" \
"s_add_u32 s42, s42, 0x80000\n" \
"s_addc_u32 s43, s43, 0\n" \
"v_mul_f32 v235, s92, v235\n" \
"v_mfma_f32_16x16x32_bf16 a[240:243], v[124:127], v[164:167], a[240:243]\n" \
"s_waitcnt lgkmcnt(0)\n" \
"v_mul_f32 v236, s92, v236\n" \
"v_mul_f32 v237, s92, v237\n" \
"v_mfma_f32_16x16x32_bf16 v[64:67], v[0:3], v[192:195], 0\n" \
"ds_read_b128 v[216:219], v230 offset:6144\n" \
"v_cvt_pk_bf16_f32 v234, v234, v235\n" \
"v_mfma_f32_16x16x32_bf16 v[64:67], v[4:7], v[196:199], v[64:67]\n" \
"ds_read_b128 v[220:223], v231 offset:6144\n" \
"v_cvt_pk_bf16_f32 v236, v236, v237\n" \
"v_mfma_f32_16x16x32_bf16 v[68:71], v[8:11], v[192:195], 0\n" \
"s_add_u32 s90, s88, 0\n" \
"ds_read_b64_tr_b16 v[32:33], v232 offset:1024\n" \
"v_accvgpr_write_b32 a232, v234\n" \
"v_mfma_f32_16x16x32_bf16 v[68:71], v[12:15], v[196:199], v[68:71]\n" \
"buffer_store_dwordx4 a[192:195], v249, s[56:59], s90 offen offset:0 sc1\n" \
"v_accvgpr_write_b32 a233, v236\n" \
"v_accvgpr_read_b32 v234, a236\n" \
"v_accvgpr_read_b32 v235, a237\n" \
"v_accvgpr_read_b32 v236, a238\n" \
"v_mfma_f32_16x16x32_bf16 v[72:75], v[16:19], v[192:195], 0\n" \
"ds_read_b64_tr_b16 v[34:35], v232 offset:3072\n" \
"v_accvgpr_read_b32 v237, a239\n" \
"v_mfma_f32_16x16x32_bf16 v[72:75], v[20:23], v[196:199], v[72:75]\n" \
"ds_read_b64_tr_b16 v[36:37], v232 offset:9216\n" \
"v_mul_f32 v234, s92, v234\n" \
"v_mfma_f32_16x16x32_bf16 v[76:79], v[24:27], v[192:195], 0\n" \
"ds_read_b64_tr_b16 v[38:39], v232 offset:11264\n" \
"v_mul_f32 v235, s92, v235\n" \
"v_mfma_f32_16x16x32_bf16 v[76:79], v[28:31], v[196:199], v[76:79]\n" \
"buffer_store_dwordx4 a[224:227], v249, s[56:59], s90 offen offset:256 sc1\n" \
"v_mul_f32 v236, s92, v236\n" \
"v_mul_f32 v237, s92, v237\n" \
"v_cvt_pk_bf16_f32 v234, v234, v235\n" \
"v_cvt_pk_bf16_f32 v236, v236, v237\n" \
"v_mfma_f32_16x16x32_bf16 v[108:111], v[24:27], v[200:203], 0\n" \
"ds_read_b64_tr_b16 v[40:41], v233 offset:1024\n" \
"v_accvgpr_write_b32 a234, v234\n" \
"v_mfma_f32_16x16x32_bf16 v[108:111], v[28:31], v[204:207], v[108:111]\n" \
"ds_read_b64_tr_b16 v[42:43], v233 offset:3072\n" \
"v_accvgpr_write_b32 a235, v236\n" \
"v_mfma_f32_16x16x32_bf16 v[104:107], v[16:19], v[200:203], 0\n" \
"ds_read_b64_tr_b16 v[44:45], v233 offset:9216\n" \
"v_accvgpr_read_b32 v234, a208\n" \
"v_mfma_f32_16x16x32_bf16 v[104:107], v[20:23], v[204:207], v[104:107]\n" \
"v_accvgpr_read_b32 v235, a209\n" \
"v_accvgpr_read_b32 v236, a210\n" \
"v_mfma_f32_16x16x32_bf16 v[100:103], v[8:11], v[200:203], 0\n" \
"ds_read_b64_tr_b16 v[46:47], v233 offset:11264\n" \
"v_accvgpr_read_b32 v237, a211\n" \
"v_mfma_f32_16x16x32_bf16 v[100:103], v[12:15], v[204:207], v[100:103]\n" \
"ds_read_b64_tr_b16 v[48:49], v232 offset:1536\n" \
"v_cvt_pk_bf16_f32 v234, v234, v235\n" \
"v_mfma_f32_16x16x32_bf16 v[96:99], v[0:3], v[200:203], 0\n" \
"ds_read_b64_tr_b16 v[50:51], v232 offset:3584\n" \
"v_cvt_pk_bf16_f32 v236, v236, v237\n" \
"v_mfma_f32_16x16x32_bf16 v[96:99], v[4:7], v[204:207], v[96:99]\n" \
"v_accvgpr_write_b32 a208, v234\n" \
"v_accvgpr_write_b32 a209, v236\n" \
"v_mfma_f32_16x16x32_bf16 v[128:131], v[0:3], v[208:211], 0\n" \
"ds_read_b64_tr_b16 v[52:53], v232 offset:9728\n" \
"v_accvgpr_read_b32 v234, a240\n" \
"v_mfma_f32_16x16x32_bf16 v[128:131], v[4:7], v[212:215], v[128:131]\n" \
"ds_read_b64_tr_b16 v[54:55], v232 offset:11776\n" \
"v_accvgpr_read_b32 v235, a241\n" \
"v_mfma_f32_16x16x32_bf16 v[132:135], v[8:11], v[208:211], 0\n" \
"ds_read_b64_tr_b16 v[56:57], v233 offset:1536\n" \
"v_accvgpr_read_b32 v236, a242\n" \
"Lpg2228_%=:\n" \
"Lst0x3_%=:\n" \
"s_cbranch_execz Lpg4399_%=\n" \
"v_mfma_f32_16x16x32_bf16 v[132:135], v[12:15], v[212:215], v[132:135]\n" \
"v_accvgpr_read_b32 v237, a243\n" \
"v_cvt_pk_bf16_f32 v234, v234, v235\n" \
"v_mfma_f32_16x16x32_bf16 v[136:139], v[16:19], v[208:211], 0\n" \
"ds_read_b64_tr_b16 v[58:59], v233 offset:3584\n" \
"v_cvt_pk_bf16_f32 v236, v236, v237\n" \
"v_mfma_f32_16x16x32_bf16 v[136:139], v[20:23], v[212:215], v[136:139]\n" \
"ds_read_b64_tr_b16 v[60:61], v233 offset:9728\n" \
"v_accvgpr_write_b32 a210, v234\n" \
"v_mfma_f32_16x16x32_bf16 v[140:143], v[24:27], v[208:211], 0\n" \
"ds_read_b64_tr_b16 v[62:63], v233 offset:11776\n" \
"v_accvgpr_write_b32 a211, v236\n" \
"v_mfma_f32_16x16x32_bf16 v[140:143], v[28:31], v[212:215], v[140:143]\n" \
"s_waitcnt lgkmcnt(3)\n" \
"v_accvgpr_read_b32 v234, a212\n" \
"v_accvgpr_read_b32 v235, a213\n" \
"v_mfma_f32_16x16x32_bf16 v[172:175], v[24:27], v[216:219], 0\n" \
"v_add_u32 v230, 0xffff0000, v230\n" \
"v_add_u32 v231, 0xffff0000, v231\n" \
"v_mfma_f32_16x16x32_bf16 v[172:175], v[28:31], v[220:223], v[172:175]\n" \
"v_add_u32 v232, 0x4000, v232\n" \
"v_add_u32 v233, 0x4000, v233\n" \
"v_mfma_f32_16x16x32_bf16 v[168:171], v[16:19], v[216:219], 0\n" \
"v_accvgpr_read_b32 v236, a214\n" \
"v_accvgpr_read_b32 v237, a215\n" \
"v_mfma_f32_16x16x32_bf16 v[168:171], v[20:23], v[220:223], v[168:171]\n" \
"v_cvt_pk_bf16_f32 v234, v234, v235\n" \
"v_cvt_pk_bf16_f32 v236, v236, v237\n" \
"v_mfma_f32_16x16x32_bf16 v[164:167], v[8:11], v[216:219], 0\n" \
"v_accvgpr_write_b32 a212, v234\n" \
"v_accvgpr_write_b32 a213, v236\n" \
"v_mfma_f32_16x16x32_bf16 v[164:167], v[12:15], v[220:223], v[164:167]\n" \
"v_accvgpr_read_b32 v234, a244\n" \
"v_accvgpr_read_b32 v235, a245\n" \
"v_mfma_f32_16x16x32_bf16 v[160:163], v[0:3], v[216:219], 0\n" \
"s_waitcnt vmcnt(18) lgkmcnt(0)\n" \
"v_accvgpr_read_b32 v236, a246\n" \
"v_accvgpr_read_b32 v237, a247\n" \
"v_mfma_f32_16x16x32_bf16 v[160:163], v[4:7], v[220:223], v[160:163]\n" \
"s_barrier\n" \
"v_mfma_f32_16x16x32_bf16 v[80:83], v[32:35], v[192:195], 0\n" \
"ds_read_b64_tr_b16 v[0:1], v232 offset:0\n" \
"v_cvt_pk_bf16_f32 v234, v234, v235\n" \
"v_mfma_f32_16x16x32_bf16 v[80:83], v[36:39], v[196:199], v[80:83]\n" \
"ds_read_b64_tr_b16 v[2:3], v232 offset:2048\n" \
"v_cvt_pk_bf16_f32 v236, v236, v237\n" \
"v_mfma_f32_16x16x32_bf16 v[84:87], v[40:43], v[192:195], 0\n" \
"s_add_u32 m0, s87, 135168\n" \
"ds_read_b64_tr_b16 v[4:5], v232 offset:8192\n" \
"v_accvgpr_write_b32 a214, v234\n" \
"v_mfma_f32_16x16x32_bf16 v[84:87], v[44:47], v[196:199], v[84:87]\n" \
"global_load_lds_dwordx4 v224, s[48:49] offset:-4096\n" \
"v_accvgpr_write_b32 a215, v236\n" \
"v_mfma_f32_16x16x32_bf16 v[88:91], v[48:51], v[192:195], 0\n" \
"ds_read_b64_tr_b16 v[6:7], v232 offset:10240\n" \
"v_accvgpr_read_b32 v234, a216\n" \
"v_mfma_f32_16x16x32_bf16 v[88:91], v[52:55], v[196:199], v[88:91]\n" \
"ds_read_b64_tr_b16 v[8:9], v233 offset:0\n" \
"v_accvgpr_read_b32 v235, a217\n" \
"v_mfma_f32_16x16x32_bf16 v[92:95], v[56:59], v[192:195], 0\n" \
"ds_read_b64_tr_b16 v[10:11], v233 offset:2048\n" \
"v_accvgpr_read_b32 v236, a218\n" \
"v_mfma_f32_16x16x32_bf16 v[92:95], v[60:63], v[196:199], v[92:95]\n" \
"global_load_lds_dwordx4 v225, s[48:49] offset:-3072\n" \
"v_accvgpr_read_b32 v237, a219\n" \
"v_mfma_f32_16x16x32_bf16 v[124:127], v[56:59], v[200:203], 0\n" \
"ds_read_b128 v[192:195], v230 offset:0\n" \
"v_cvt_pk_bf16_f32 v234, v234, v235\n" \
"v_mfma_f32_16x16x32_bf16 v[124:127], v[60:63], v[204:207], v[124:127]\n" \
"ds_read_b128 v[196:199], v231 offset:0\n" \
"v_cvt_pk_bf16_f32 v236, v236, v237\n" \
"v_mfma_f32_16x16x32_bf16 v[120:123], v[48:51], v[200:203], 0\n" \
"ds_read_b64_tr_b16 v[12:13], v233 offset:8192\n" \
"v_accvgpr_write_b32 a216, v234\n" \
"v_mfma_f32_16x16x32_bf16 v[120:123], v[52:55], v[204:207], v[120:123]\n" \
"global_load_lds_dwordx4 v226, s[48:49] offset:-2048\n" \
"v_accvgpr_write_b32 a217, v236\n" \
"v_mfma_f32_16x16x32_bf16 v[116:119], v[40:43], v[200:203], 0\n" \
"ds_read_b64_tr_b16 v[14:15], v233 offset:10240\n" \
"v_accvgpr_read_b32 v234, a248\n" \
"v_mfma_f32_16x16x32_bf16 v[116:119], v[44:47], v[204:207], v[116:119]\n" \
"ds_read_b64_tr_b16 v[16:17], v232 offset:512\n" \
"v_accvgpr_read_b32 v235, a249\n" \
"v_mfma_f32_16x16x32_bf16 v[112:115], v[32:35], v[200:203], 0\n" \
"ds_read_b64_tr_b16 v[18:19], v232 offset:2560\n" \
"v_accvgpr_read_b32 v236, a250\n" \
"v_mfma_f32_16x16x32_bf16 v[112:115], v[36:39], v[204:207], v[112:115]\n" \
"global_load_lds_dwordx4 v227, s[48:49] offset:-1024\n" \
"v_accvgpr_read_b32 v237, a251\n" \
"v_mfma_f32_16x16x32_bf16 v[144:147], v[32:35], v[208:211], 0\n" \
"s_add_u32 s48, s48, 0x80\n" \
"s_addc_u32 s49, s49, 0\n" \
"ds_read_b128 v[200:203], v230 offset:2048\n" \
"v_mfma_f32_16x16x32_bf16 v[144:147], v[36:39], v[212:215], v[144:147]\n" \
"ds_read_b128 v[204:207], v231 offset:2048\n" \
"v_cvt_pk_bf16_f32 v234, v234, v235\n" \
"v_mfma_f32_16x16x32_bf16 v[148:151], v[40:43], v[208:211], 0\n" \
"ds_read_b64_tr_b16 v[20:21], v232 offset:8704\n" \
"v_cvt_pk_bf16_f32 v236, v236, v237\n" \
"v_mfma_f32_16x16x32_bf16 v[148:151], v[44:47], v[212:215], v[148:151]\n" \
"global_load_lds_dwordx4 v224, s[46:47] offset:0\n" \
"v_accvgpr_write_b32 a218, v234\n" \
"v_mfma_f32_16x16x32_bf16 v[152:155], v[48:51], v[208:211], 0\n" \
"ds_read_b64_tr_b16 v[22:23], v232 offset:10752\n" \
"v_accvgpr_write_b32 a219, v236\n" \
"v_mfma_f32_16x16x32_bf16 v[152:155], v[52:55], v[212:215], v[152:155]\n" \
"ds_read_b64_tr_b16 v[24:25], v233 offset:512\n" \
"v_accvgpr_read_b32 v234, a220\n" \
"v_mfma_f32_16x16x32_bf16 v[156:159], v[56:59], v[208:211], 0\n" \
"ds_read_b64_tr_b16 v[26:27], v233 offset:2560\n" \
"v_accvgpr_read_b32 v235, a221\n" \
"v_mfma_f32_16x16x32_bf16 v[156:159], v[60:63], v[212:215], v[156:159]\n" \
"global_load_lds_dwordx4 v225, s[46:47] offset:1024\n" \
"v_accvgpr_read_b32 v236, a222\n" \
"v_mfma_f32_16x16x32_bf16 v[188:191], v[56:59], v[216:219], 0\n" \
"ds_read_b128 v[208:211], v230 offset:4096\n" \
"v_accvgpr_read_b32 v237, a223\n" \
"v_mfma_f32_16x16x32_bf16 v[188:191], v[60:63], v[220:223], v[188:191]\n" \
"ds_read_b128 v[212:215], v231 offset:4096\n" \
"v_cvt_pk_bf16_f32 v234, v234, v235\n" \
"v_mfma_f32_16x16x32_bf16 v[184:187], v[48:51], v[216:219], 0\n" \
"ds_read_b64_tr_b16 v[28:29], v233 offset:8704\n" \
"v_cvt_pk_bf16_f32 v236, v236, v237\n" \
"v_mfma_f32_16x16x32_bf16 v[184:187], v[52:55], v[220:223], v[184:187]\n" \
"global_load_lds_dwordx4 v226, s[46:47] offset:2048\n" \
"v_accvgpr_write_b32 a220, v234\n" \
"v_mfma_f32_16x16x32_bf16 v[180:183], v[40:43], v[216:219], 0\n" \
"ds_read_b64_tr_b16 v[30:31], v233 offset:10752\n" \
"v_accvgpr_write_b32 a221, v236\n" \
"v_mfma_f32_16x16x32_bf16 v[180:183], v[44:47], v[220:223], v[180:183]\n" \
"v_accvgpr_read_b32 v234, a252\n" \
"v_accvgpr_read_b32 v235, a253\n" \
"v_mfma_f32_16x16x32_bf16 v[176:179], v[32:35], v[216:219], 0\n" \
"v_accvgpr_read_b32 v236, a254\n" \
"v_accvgpr_read_b32 v237, a255\n" \
"v_mfma_f32_16x16x32_bf16 v[176:179], v[36:39], v[220:223], v[176:179]\n" \
"s_waitcnt lgkmcnt(1)\n" \
"global_load_lds_dwordx4 v227, s[46:47] offset:3072\n" \
"v_cvt_pk_bf16_f32 v234, v234, v235\n" \
"v_mfma_f32_16x16x32_bf16 v[64:67], v[0:3], v[192:195], v[64:67]\n" \
"s_add_u32 s46, s46, 0x80\n" \
"s_addc_u32 s47, s47, 0\n" \
"ds_read_b128 v[216:219], v230 offset:6144\n" \
"v_mfma_f32_16x16x32_bf16 v[64:67], v[4:7], v[196:199], v[64:67]\n" \
"ds_read_b128 v[220:223], v231 offset:6144\n" \
"v_cvt_pk_bf16_f32 v236, v236, v237\n" \
"v_mfma_f32_16x16x32_bf16 v[68:71], v[8:11], v[192:195], v[68:71]\n" \
"ds_read_b64_tr_b16 v[32:33], v232 offset:1024\n" \
"v_accvgpr_write_b32 a222, v234\n" \
"v_mfma_f32_16x16x32_bf16 v[68:71], v[12:15], v[196:199], v[68:71]\n" \
"buffer_store_dwordx4 a[200:203], v249, s[56:59], s90 offen offset:2048 sc1\n" \
"v_accvgpr_write_b32 a223, v236\n" \
"v_accvgpr_read_b32 v234, a128\n" \
"v_accvgpr_read_b32 v235, a129\n" \
"v_accvgpr_read_b32 v236, a130\n" \
"v_mfma_f32_16x16x32_bf16 v[72:75], v[16:19], v[192:195], v[72:75]\n" \
"ds_read_b64_tr_b16 v[34:35], v232 offset:3072\n" \
"v_accvgpr_read_b32 v237, a131\n" \
"v_mfma_f32_16x16x32_bf16 v[72:75], v[20:23], v[196:199], v[72:75]\n" \
"s_waitcnt lgkmcnt(3)\n" \
"ds_read_b64_tr_b16 v[36:37], v232 offset:9216\n" \
"v_mul_f32 v234, s92, v234\n" \
"v_mfma_f32_16x16x32_bf16 v[76:79], v[24:27], v[192:195], v[76:79]\n" \
"ds_read_b64_tr_b16 v[38:39], v232 offset:11264\n" \
"v_mul_f32 v235, s92, v235\n" \
"v_mfma_f32_16x16x32_bf16 v[76:79], v[28:31], v[196:199], v[76:79]\n" \
"v_mul_f32 v236, s92, v236\n" \
"v_mul_f32 v237, s92, v237\n" \
"v_mfma_f32_16x16x32_bf16 v[108:111], v[24:27], v[200:203], v[108:111]\n" \
"ds_read_b64_tr_b16 v[40:41], v233 offset:1024\n" \
"v_cvt_pk_bf16_f32 v234, v234, v235\n" \
"v_mfma_f32_16x16x32_bf16 v[108:111], v[28:31], v[204:207], v[108:111]\n" \
"ds_read_b64_tr_b16 v[42:43], v233 offset:3072\n" \
"v_cvt_pk_bf16_f32 v236, v236, v237\n" \
"v_mfma_f32_16x16x32_bf16 v[104:107], v[16:19], v[200:203], v[104:107]\n" \
"ds_read_b64_tr_b16 v[44:45], v233 offset:9216\n" \
"v_accvgpr_write_b32 a128, v234\n" \
"v_mfma_f32_16x16x32_bf16 v[104:107], v[20:23], v[204:207], v[104:107]\n" \
"v_accvgpr_write_b32 a129, v236\n" \
"v_accvgpr_read_b32 v234, a132\n" \
"v_mfma_f32_16x16x32_bf16 v[100:103], v[8:11], v[200:203], v[100:103]\n" \
"ds_read_b64_tr_b16 v[46:47], v233 offset:11264\n" \
"v_accvgpr_read_b32 v235, a133\n" \
"v_mfma_f32_16x16x32_bf16 v[100:103], v[12:15], v[204:207], v[100:103]\n" \
"ds_read_b64_tr_b16 v[48:49], v232 offset:1536\n" \
"v_accvgpr_read_b32 v236, a134\n" \
"v_mfma_f32_16x16x32_bf16 v[96:99], v[0:3], v[200:203], v[96:99]\n" \
"s_add_u32 m0, s95, 4096\n" \
"ds_read_b64_tr_b16 v[50:51], v232 offset:3584\n" \
"v_accvgpr_read_b32 v237, a135\n" \
"v_mfma_f32_16x16x32_bf16 v[96:99], v[4:7], v[204:207], v[96:99]\n" \
"global_load_lds_dwordx4 v228, s[44:45] offset:-4096\n" \
"v_mul_f32 v234, s92, v234\n" \
"v_mfma_f32_16x16x32_bf16 v[128:131], v[0:3], v[208:211], v[128:131]\n" \
"ds_read_b64_tr_b16 v[52:53], v232 offset:9728\n" \
"v_mul_f32 v235, s92, v235\n" \
"v_mfma_f32_16x16x32_bf16 v[128:131], v[4:7], v[212:215], v[128:131]\n" \
"ds_read_b64_tr_b16 v[54:55], v232 offset:11776\n" \
"v_mul_f32 v236, s92, v236\n" \
"v_mfma_f32_16x16x32_bf16 v[132:135], v[8:11], v[208:211], v[132:135]\n" \
"ds_read_b64_tr_b16 v[56:57], v233 offset:1536\n" \
"v_mul_f32 v237, s92, v237\n" \
"v_mfma_f32_16x16x32_bf16 v[132:135], v[12:15], v[212:215], v[132:135]\n" \
"global_load_lds_dwordx4 v229, s[44:45] offset:-3072\n" \
"v_cvt_pk_bf16_f32 v234, v234, v235\n" \
"v_mfma_f32_16x16x32_bf16 v[136:139], v[16:19], v[208:211], v[136:139]\n" \
"s_add_u32 s44, s44, 0x100000\n" \
"s_addc_u32 s45, s45, 0\n" \
"ds_read_b64_tr_b16 v[58:59], v233 offset:3584\n" \
"v_mfma_f32_16x16x32_bf16 v[136:139], v[20:23], v[212:215], v[136:139]\n" \
"ds_read_b64_tr_b16 v[60:61], v233 offset:9728\n" \
"v_cvt_pk_bf16_f32 v236, v236, v237\n" \
"v_mfma_f32_16x16x32_bf16 v[140:143], v[24:27], v[208:211], v[140:143]\n" \
"ds_read_b64_tr_b16 v[62:63], v233 offset:11776\n" \
"v_accvgpr_write_b32 a130, v234\n" \
"v_mfma_f32_16x16x32_bf16 v[140:143], v[28:31], v[212:215], v[140:143]\n" \
"s_waitcnt lgkmcnt(3)\n" \
"global_load_lds_dwordx4 v228, s[42:43] offset:-2048\n" \
"v_accvgpr_write_b32 a131, v236\n" \
"v_mfma_f32_16x16x32_bf16 v[172:175], v[24:27], v[216:219], v[172:175]\n" \
"v_add_u32 v230, 0x8000, v230\n" \
"v_add_u32 v231, 0x8000, v231\n" \
"v_mfma_f32_16x16x32_bf16 v[172:175], v[28:31], v[220:223], v[172:175]\n" \
"v_add_u32 v232, 0x4000, v232\n" \
"v_add_u32 v233, 0x4000, v233\n" \
"v_mfma_f32_16x16x32_bf16 v[168:171], v[16:19], v[216:219], v[168:171]\n" \
"v_accvgpr_read_b32 v234, a160\n" \
"v_accvgpr_read_b32 v235, a161\n" \
"v_mfma_f32_16x16x32_bf16 v[168:171], v[20:23], v[220:223], v[168:171]\n" \
"global_load_lds_dwordx4 v229, s[42:43] offset:-1024\n" \
"v_accvgpr_read_b32 v236, a162\n" \
"v_mfma_f32_16x16x32_bf16 v[164:167], v[8:11], v[216:219], v[164:167]\n" \
"s_add_u32 s42, s42, 0x100000\n" \
"s_addc_u32 s43, s43, 0\n" \
"v_accvgpr_read_b32 v237, a163\n" \
"v_mfma_f32_16x16x32_bf16 v[164:167], v[12:15], v[220:223], v[164:167]\n" \
"v_mul_f32 v234, s92, v234\n" \
"v_mul_f32 v235, s92, v235\n" \
"v_mfma_f32_16x16x32_bf16 v[160:163], v[0:3], v[216:219], v[160:163]\n" \
"s_waitcnt vmcnt(19) lgkmcnt(0)\n" \
"v_mul_f32 v236, s92, v236\n" \
"v_mul_f32 v237, s92, v237\n" \
"v_mfma_f32_16x16x32_bf16 v[160:163], v[4:7], v[220:223], v[160:163]\n" \
"s_barrier\n" \
"v_mfma_f32_16x16x32_bf16 v[80:83], v[32:35], v[192:195], v[80:83]\n" \
"ds_read_b64_tr_b16 v[0:1], v232 offset:0\n" \
"v_cvt_pk_bf16_f32 v234, v234, v235\n" \
"v_mfma_f32_16x16x32_bf16 v[80:83], v[36:39], v[196:199], v[80:83]\n" \
"ds_read_b64_tr_b16 v[2:3], v232 offset:2048\n" \
"v_cvt_pk_bf16_f32 v236, v236, v237\n" \
"v_mfma_f32_16x16x32_bf16 v[84:87], v[40:43], v[192:195], v[84:87]\n" \
"s_add_u32 m0, s87, 69632\n" \
"ds_read_b64_tr_b16 v[4:5], v232 offset:8192\n" \
"v_accvgpr_write_b32 a160, v234\n" \
"v_mfma_f32_16x16x32_bf16 v[84:87], v[44:47], v[196:199], v[84:87]\n" \
"global_load_lds_dwordx4 v224, s[48:49] offset:-4096\n" \
"v_accvgpr_write_b32 a161, v236\n" \
"v_mfma_f32_16x16x32_bf16 v[88:91], v[48:51], v[192:195], v[88:91]\n" \
"ds_read_b64_tr_b16 v[6:7], v232 offset:10240\n" \
"v_accvgpr_read_b32 v234, a164\n" \
"v_mfma_f32_16x16x32_bf16 v[88:91], v[52:55], v[196:199], v[88:91]\n" \
"ds_read_b64_tr_b16 v[8:9], v233 offset:0\n" \
"v_accvgpr_read_b32 v235, a165\n" \
"v_mfma_f32_16x16x32_bf16 v[92:95], v[56:59], v[192:195], v[92:95]\n" \
"ds_read_b64_tr_b16 v[10:11], v233 offset:2048\n" \
"v_accvgpr_read_b32 v236, a166\n" \
"v_mfma_f32_16x16x32_bf16 v[92:95], v[60:63], v[196:199], v[92:95]\n" \
"global_load_lds_dwordx4 v225, s[48:49] offset:-3072\n" \
"v_accvgpr_read_b32 v237, a167\n" \
"v_mfma_f32_16x16x32_bf16 v[124:127], v[56:59], v[200:203], v[124:127]\n" \
"ds_read_b128 v[192:195], v230 offset:0\n" \
"v_mul_f32 v234, s92, v234\n" \
"v_mfma_f32_16x16x32_bf16 v[124:127], v[60:63], v[204:207], v[124:127]\n" \
"ds_read_b128 v[196:199], v231 offset:0\n" \
"v_mul_f32 v235, s92, v235\n" \
"v_mfma_f32_16x16x32_bf16 v[120:123], v[48:51], v[200:203], v[120:123]\n" \
"ds_read_b64_tr_b16 v[12:13], v233 offset:8192\n" \
"v_mul_f32 v236, s92, v236\n" \
"v_mfma_f32_16x16x32_bf16 v[120:123], v[52:55], v[204:207], v[120:123]\n" \
"global_load_lds_dwordx4 v226, s[48:49] offset:-2048\n" \
"v_mul_f32 v237, s92, v237\n" \
"v_mfma_f32_16x16x32_bf16 v[116:119], v[40:43], v[200:203], v[116:119]\n" \
"ds_read_b64_tr_b16 v[14:15], v233 offset:10240\n" \
"v_cvt_pk_bf16_f32 v234, v234, v235\n" \
"v_mfma_f32_16x16x32_bf16 v[116:119], v[44:47], v[204:207], v[116:119]\n" \
"ds_read_b64_tr_b16 v[16:17], v232 offset:512\n" \
"v_cvt_pk_bf16_f32 v236, v236, v237\n" \
"v_mfma_f32_16x16x32_bf16 v[112:115], v[32:35], v[200:203], v[112:115]\n" \
"ds_read_b64_tr_b16 v[18:19], v232 offset:2560\n" \
"v_accvgpr_write_b32 a162, v234\n" \
"v_mfma_f32_16x16x32_bf16 v[112:115], v[36:39], v[204:207], v[112:115]\n" \
"global_load_lds_dwordx4 v227, s[48:49] offset:-1024\n" \
"v_accvgpr_write_b32 a163, v236\n" \
"v_mfma_f32_16x16x32_bf16 v[144:147], v[32:35], v[208:211], v[144:147]\n" \
"s_add_u32 s48, s48, 0x100\n" \
"s_addc_u32 s49, s49, 0\n" \
"ds_read_b128 v[200:203], v230 offset:2048\n" \
"v_mfma_f32_16x16x32_bf16 v[144:147], v[36:39], v[212:215], v[144:147]\n" \
"ds_read_b128 v[204:207], v231 offset:2048\n" \
"v_accvgpr_read_b32 v234, a136\n" \
"v_mfma_f32_16x16x32_bf16 v[148:151], v[40:43], v[208:211], v[148:151]\n" \
"ds_read_b64_tr_b16 v[20:21], v232 offset:8704\n" \
"v_accvgpr_read_b32 v235, a137\n" \
"v_mfma_f32_16x16x32_bf16 v[148:151], v[44:47], v[212:215], v[148:151]\n" \
"global_load_lds_dwordx4 v224, s[46:47] offset:0\n" \
"v_accvgpr_read_b32 v236, a138\n" \
"v_mfma_f32_16x16x32_bf16 v[152:155], v[48:51], v[208:211], v[152:155]\n" \
"ds_read_b64_tr_b16 v[22:23], v232 offset:10752\n" \
"v_accvgpr_read_b32 v237, a139\n" \
"v_mfma_f32_16x16x32_bf16 v[152:155], v[52:55], v[212:215], v[152:155]\n" \
"ds_read_b64_tr_b16 v[24:25], v233 offset:512\n" \
"v_mul_f32 v234, s92, v234\n" \
"v_mfma_f32_16x16x32_bf16 v[156:159], v[56:59], v[208:211], v[156:159]\n" \
"ds_read_b64_tr_b16 v[26:27], v233 offset:2560\n" \
"v_mul_f32 v235, s92, v235\n" \
"v_mfma_f32_16x16x32_bf16 v[156:159], v[60:63], v[212:215], v[156:159]\n" \
"global_load_lds_dwordx4 v225, s[46:47] offset:1024\n" \
"v_mul_f32 v236, s92, v236\n" \
"v_mfma_f32_16x16x32_bf16 v[188:191], v[56:59], v[216:219], v[188:191]\n" \
"ds_read_b128 v[208:211], v230 offset:4096\n" \
"v_mul_f32 v237, s92, v237\n" \
"v_mfma_f32_16x16x32_bf16 v[188:191], v[60:63], v[220:223], v[188:191]\n" \
"ds_read_b128 v[212:215], v231 offset:4096\n" \
"v_cvt_pk_bf16_f32 v234, v234, v235\n" \
"v_mfma_f32_16x16x32_bf16 v[184:187], v[48:51], v[216:219], v[184:187]\n" \
"ds_read_b64_tr_b16 v[28:29], v233 offset:8704\n" \
"v_cvt_pk_bf16_f32 v236, v236, v237\n" \
"v_mfma_f32_16x16x32_bf16 v[184:187], v[52:55], v[220:223], v[184:187]\n" \
"global_load_lds_dwordx4 v226, s[46:47] offset:2048\n" \
"v_accvgpr_write_b32 a136, v234\n" \
"v_mfma_f32_16x16x32_bf16 v[180:183], v[40:43], v[216:219], v[180:183]\n" \
"ds_read_b64_tr_b16 v[30:31], v233 offset:10752\n" \
"v_accvgpr_write_b32 a137, v236\n" \
"v_mfma_f32_16x16x32_bf16 v[180:183], v[44:47], v[220:223], v[180:183]\n" \
"v_accvgpr_read_b32 v234, a140\n" \
"v_accvgpr_read_b32 v235, a141\n" \
"v_mfma_f32_16x16x32_bf16 v[176:179], v[32:35], v[216:219], v[176:179]\n" \
"v_accvgpr_read_b32 v236, a142\n" \
"v_accvgpr_read_b32 v237, a143\n" \
"v_mfma_f32_16x16x32_bf16 v[176:179], v[36:39], v[220:223], v[176:179]\n" \
"s_waitcnt lgkmcnt(1)\n" \
"global_load_lds_dwordx4 v227, s[46:47] offset:3072\n" \
"v_mul_f32 v234, s92, v234\n" \
"v_mfma_f32_16x16x32_bf16 v[64:67], v[0:3], v[192:195], v[64:67]\n" \
"s_add_u32 s46, s46, 0x100\n" \
"s_addc_u32 s47, s47, 0\n" \
"ds_read_b128 v[216:219], v230 offset:6144\n" \
"v_mfma_f32_16x16x32_bf16 v[64:67], v[4:7], v[196:199], v[64:67]\n" \
"ds_read_b128 v[220:223], v231 offset:6144\n" \
"v_mul_f32 v235, s92, v235\n" \
"v_mfma_f32_16x16x32_bf16 v[68:71], v[8:11], v[192:195], v[68:71]\n" \
"ds_read_b64_tr_b16 v[32:33], v232 offset:1024\n" \
"v_mul_f32 v236, s92, v236\n" \
"v_mfma_f32_16x16x32_bf16 v[68:71], v[12:15], v[196:199], v[68:71]\n" \
"buffer_store_dwordx4 a[232:235], v249, s[56:59], s90 offen offset:2304 sc1\n" \
"v_mul_f32 v237, s92, v237\n" \
"v_cvt_pk_bf16_f32 v234, v234, v235\n" \
"v_cvt_pk_bf16_f32 v236, v236, v237\n" \
"v_accvgpr_write_b32 a138, v234\n" \
"v_mfma_f32_16x16x32_bf16 v[72:75], v[16:19], v[192:195], v[72:75]\n" \
"ds_read_b64_tr_b16 v[34:35], v232 offset:3072\n" \
"v_accvgpr_write_b32 a139, v236\n" \
"v_mfma_f32_16x16x32_bf16 v[72:75], v[20:23], v[196:199], v[72:75]\n" \
"s_waitcnt lgkmcnt(3)\n" \
"ds_read_b64_tr_b16 v[36:37], v232 offset:9216\n" \
"v_accvgpr_read_b32 v234, a168\n" \
"v_mfma_f32_16x16x32_bf16 v[76:79], v[24:27], v[192:195], v[76:79]\n" \
"ds_read_b64_tr_b16 v[38:39], v232 offset:11264\n" \
"v_accvgpr_read_b32 v235, a169\n" \
"v_mfma_f32_16x16x32_bf16 v[76:79], v[28:31], v[196:199], v[76:79]\n" \
"v_accvgpr_read_b32 v236, a170\n" \
"v_accvgpr_read_b32 v237, a171\n" \
"v_mfma_f32_16x16x32_bf16 v[108:111], v[24:27], v[200:203], v[108:111]\n" \
"ds_read_b64_tr_b16 v[40:41], v233 offset:1024\n" \
"v_mul_f32 v234, s92, v234\n" \
"v_mfma_f32_16x16x32_bf16 v[108:111], v[28:31], v[204:207], v[108:111]\n" \
"ds_read_b64_tr_b16 v[42:43], v233 offset:3072\n" \
"v_mul_f32 v235, s92, v235\n" \
"v_mfma_f32_16x16x32_bf16 v[104:107], v[16:19], v[200:203], v[104:107]\n" \
"ds_read_b64_tr_b16 v[44:45], v233 offset:9216\n" \
"v_mul_f32 v236, s92, v236\n" \
"v_mfma_f32_16x16x32_bf16 v[104:107], v[20:23], v[204:207], v[104:107]\n" \
"v_mul_f32 v237, s92, v237\n" \
"v_cvt_pk_bf16_f32 v234, v234, v235\n" \
"v_mfma_f32_16x16x32_bf16 v[100:103], v[8:11], v[200:203], v[100:103]\n" \
"ds_read_b64_tr_b16 v[46:47], v233 offset:11264\n" \
"v_cvt_pk_bf16_f32 v236, v236, v237\n" \
"v_mfma_f32_16x16x32_bf16 v[100:103], v[12:15], v[204:207], v[100:103]\n" \
"ds_read_b64_tr_b16 v[48:49], v232 offset:1536\n" \
"v_accvgpr_write_b32 a168, v234\n" \
"v_mfma_f32_16x16x32_bf16 v[96:99], v[0:3], v[200:203], v[96:99]\n" \
"s_add_u32 m0, s95, 20480\n" \
"ds_read_b64_tr_b16 v[50:51], v232 offset:3584\n" \
"v_accvgpr_write_b32 a169, v236\n" \
"v_mfma_f32_16x16x32_bf16 v[96:99], v[4:7], v[204:207], v[96:99]\n" \
"global_load_lds_dwordx4 v228, s[44:45] offset:-4096\n" \
"v_accvgpr_read_b32 v234, a172\n" \
"v_mfma_f32_16x16x32_bf16 v[128:131], v[0:3], v[208:211], v[128:131]\n" \
"ds_read_b64_tr_b16 v[52:53], v232 offset:9728\n" \
"v_accvgpr_read_b32 v235, a173\n" \
"v_mfma_f32_16x16x32_bf16 v[128:131], v[4:7], v[212:215], v[128:131]\n" \
"ds_read_b64_tr_b16 v[54:55], v232 offset:11776\n" \
"v_accvgpr_read_b32 v236, a174\n" \
"v_mfma_f32_16x16x32_bf16 v[132:135], v[8:11], v[208:211], v[132:135]\n" \
"ds_read_b64_tr_b16 v[56:57], v233 offset:1536\n" \
"v_accvgpr_read_b32 v237, a175\n" \
"v_mfma_f32_16x16x32_bf16 v[132:135], v[12:15], v[212:215], v[132:135]\n" \
"global_load_lds_dwordx4 v229, s[44:45] offset:-3072\n" \
"v_mul_f32 v234, s92, v234\n" \
"v_mfma_f32_16x16x32_bf16 v[136:139], v[16:19], v[208:211], v[136:139]\n" \
"s_add_u32 s44, s44, 0x80000\n" \
"s_addc_u32 s45, s45, 0\n" \
"ds_read_b64_tr_b16 v[58:59], v233 offset:3584\n" \
"v_mfma_f32_16x16x32_bf16 v[136:139], v[20:23], v[212:215], v[136:139]\n" \
"ds_read_b64_tr_b16 v[60:61], v233 offset:9728\n" \
"v_mul_f32 v235, s92, v235\n" \
"v_mfma_f32_16x16x32_bf16 v[140:143], v[24:27], v[208:211], v[140:143]\n" \
"ds_read_b64_tr_b16 v[62:63], v233 offset:11776\n" \
"v_mul_f32 v236, s92, v236\n" \
"v_mfma_f32_16x16x32_bf16 v[140:143], v[28:31], v[212:215], v[140:143]\n" \
"s_waitcnt lgkmcnt(3)\n" \
"global_load_lds_dwordx4 v228, s[42:43] offset:-2048\n" \
"v_mul_f32 v237, s92, v237\n" \
"v_mfma_f32_16x16x32_bf16 v[172:175], v[24:27], v[216:219], v[172:175]\n" \
"v_add_u32 v230, 0x8000, v230\n" \
"v_add_u32 v231, 0x8000, v231\n" \
"v_mfma_f32_16x16x32_bf16 v[172:175], v[28:31], v[220:223], v[172:175]\n" \
"v_add_u32 v232, 0x4000, v232\n" \
"v_add_u32 v233, 0x4000, v233\n" \
"v_mfma_f32_16x16x32_bf16 v[168:171], v[16:19], v[216:219], v[168:171]\n" \
"v_cvt_pk_bf16_f32 v234, v234, v235\n" \
"v_cvt_pk_bf16_f32 v236, v236, v237\n" \
"v_mfma_f32_16x16x32_bf16 v[168:171], v[20:23], v[220:223], v[168:171]\n" \
"global_load_lds_dwordx4 v229, s[42:43] offset:-1024\n" \
"v_accvgpr_write_b32 a170, v234\n" \
"v_mfma_f32_16x16x32_bf16 v[164:167], v[8:11], v[216:219], v[164:167]\n" \
"s_add_u32 s42, s42, 0x80000\n" \
"s_addc_u32 s43, s43, 0\n" \
"v_accvgpr_write_b32 a171, v236\n" \
"v_mfma_f32_16x16x32_bf16 v[164:167], v[12:15], v[220:223], v[164:167]\n" \
"v_accvgpr_read_b32 v234, a144\n" \
"v_accvgpr_read_b32 v235, a145\n" \
"v_mfma_f32_16x16x32_bf16 v[160:163], v[0:3], v[216:219], v[160:163]\n" \
"s_waitcnt vmcnt(18) lgkmcnt(0)\n" \
"v_accvgpr_read_b32 v236, a146\n" \
"v_accvgpr_read_b32 v237, a147\n" \
"v_mfma_f32_16x16x32_bf16 v[160:163], v[4:7], v[220:223], v[160:163]\n" \
"s_barrier\n" \
"v_mfma_f32_16x16x32_bf16 v[80:83], v[32:35], v[192:195], v[80:83]\n" \
"ds_read_b64_tr_b16 v[0:1], v232 offset:0\n" \
"v_cvt_pk_bf16_f32 v234, v234, v235\n" \
"v_mfma_f32_16x16x32_bf16 v[80:83], v[36:39], v[196:199], v[80:83]\n" \
"ds_read_b64_tr_b16 v[2:3], v232 offset:2048\n" \
"v_cvt_pk_bf16_f32 v236, v236, v237\n" \
"v_mfma_f32_16x16x32_bf16 v[84:87], v[40:43], v[192:195], v[84:87]\n" \
"s_add_u32 m0, s87, 102400\n" \
"ds_read_b64_tr_b16 v[4:5], v232 offset:8192\n" \
"v_accvgpr_write_b32 a144, v234\n" \
"v_mfma_f32_16x16x32_bf16 v[84:87], v[44:47], v[196:199], v[84:87]\n" \
"global_load_lds_dwordx4 v224, s[48:49] offset:-4096\n" \
"v_accvgpr_write_b32 a145, v236\n" \
"v_mfma_f32_16x16x32_bf16 v[88:91], v[48:51], v[192:195], v[88:91]\n" \
"ds_read_b64_tr_b16 v[6:7], v232 offset:10240\n" \
"v_accvgpr_read_b32 v234, a176\n" \
"v_mfma_f32_16x16x32_bf16 v[88:91], v[52:55], v[196:199], v[88:91]\n" \
"ds_read_b64_tr_b16 v[8:9], v233 offset:0\n" \
"v_accvgpr_read_b32 v235, a177\n" \
"v_mfma_f32_16x16x32_bf16 v[92:95], v[56:59], v[192:195], v[92:95]\n" \
"ds_read_b64_tr_b16 v[10:11], v233 offset:2048\n" \
"v_accvgpr_read_b32 v236, a178\n" \
"v_mfma_f32_16x16x32_bf16 v[92:95], v[60:63], v[196:199], v[92:95]\n" \
"global_load_lds_dwordx4 v225, s[48:49] offset:-3072\n" \
"v_accvgpr_read_b32 v237, a179\n" \
"v_mfma_f32_16x16x32_bf16 v[124:127], v[56:59], v[200:203], v[124:127]\n" \
"ds_read_b128 v[192:195], v230 offset:0\n" \
"v_cvt_pk_bf16_f32 v234, v234, v235\n" \
"v_mfma_f32_16x16x32_bf16 v[124:127], v[60:63], v[204:207], v[124:127]\n" \
"ds_read_b128 v[196:199], v231 offset:0\n" \
"v_cvt_pk_bf16_f32 v236, v236, v237\n" \
"v_mfma_f32_16x16x32_bf16 v[120:123], v[48:51], v[200:203], v[120:123]\n" \
"ds_read_b64_tr_b16 v[12:13], v233 offset:8192\n" \
"v_accvgpr_write_b32 a146, v234\n" \
"v_mfma_f32_16x16x32_bf16 v[120:123], v[52:55], v[204:207], v[120:123]\n" \
"global_load_lds_dwordx4 v226, s[48:49] offset:-2048\n" \
"v_accvgpr_write_b32 a147, v236\n" \
"v_mfma_f32_16x16x32_bf16 v[116:119], v[40:43], v[200:203], v[116:119]\n" \
"ds_read_b64_tr_b16 v[14:15], v233 offset:10240\n" \
"v_accvgpr_read_b32 v234, a148\n" \
"v_mfma_f32_16x16x32_bf16 v[116:119], v[44:47], v[204:207], v[116:119]\n" \
"ds_read_b64_tr_b16 v[16:17], v232 offset:512\n" \
"v_accvgpr_read_b32 v235, a149\n" \
"v_mfma_f32_16x16x32_bf16 v[112:115], v[32:35], v[200:203], v[112:115]\n" \
"ds_read_b64_tr_b16 v[18:19], v232 offset:2560\n" \
"v_accvgpr_read_b32 v236, a150\n" \
"v_mfma_f32_16x16x32_bf16 v[112:115], v[36:39], v[204:207], v[112:115]\n" \
"global_load_lds_dwordx4 v227, s[48:49] offset:-1024\n" \
"v_accvgpr_read_b32 v237, a151\n" \
"v_mfma_f32_16x16x32_bf16 v[144:147], v[32:35], v[208:211], v[144:147]\n" \
"s_add_u32 s48, s48, 0x80\n" \
"s_addc_u32 s49, s49, 0\n" \
"ds_read_b128 v[200:203], v230 offset:2048\n" \
"v_mfma_f32_16x16x32_bf16 v[144:147], v[36:39], v[212:215], v[144:147]\n" \
"ds_read_b128 v[204:207], v231 offset:2048\n" \
"v_cvt_pk_bf16_f32 v234, v234, v235\n" \
"v_mfma_f32_16x16x32_bf16 v[148:151], v[40:43], v[208:211], v[148:151]\n" \
"ds_read_b64_tr_b16 v[20:21], v232 offset:8704\n" \
"v_cvt_pk_bf16_f32 v236, v236, v237\n" \
"v_mfma_f32_16x16x32_bf16 v[148:151], v[44:47], v[212:215], v[148:151]\n" \
"global_load_lds_dwordx4 v224, s[46:47] offset:0\n" \
"v_accvgpr_write_b32 a148, v234\n" \
"v_mfma_f32_16x16x32_bf16 v[152:155], v[48:51], v[208:211], v[152:155]\n" \
"ds_read_b64_tr_b16 v[22:23], v232 offset:10752\n" \
"v_accvgpr_write_b32 a149, v236\n" \
"v_mfma_f32_16x16x32_bf16 v[152:155], v[52:55], v[212:215], v[152:155]\n" \
"ds_read_b64_tr_b16 v[24:25], v233 offset:512\n" \
"v_accvgpr_read_b32 v234, a180\n" \
"v_mfma_f32_16x16x32_bf16 v[156:159], v[56:59], v[208:211], v[156:159]\n" \
"ds_read_b64_tr_b16 v[26:27], v233 offset:2560\n" \
"v_accvgpr_read_b32 v235, a181\n" \
"v_mfma_f32_16x16x32_bf16 v[156:159], v[60:63], v[212:215], v[156:159]\n" \
"global_load_lds_dwordx4 v225, s[46:47] offset:1024\n" \
"v_accvgpr_read_b32 v236, a182\n" \
"v_mfma_f32_16x16x32_bf16 v[188:191], v[56:59], v[216:219], v[188:191]\n" \
"ds_read_b128 v[208:211], v230 offset:4096\n" \
"v_accvgpr_read_b32 v237, a183\n" \
"v_mfma_f32_16x16x32_bf16 v[188:191], v[60:63], v[220:223], v[188:191]\n" \
"ds_read_b128 v[212:215], v231 offset:4096\n" \
"v_cvt_pk_bf16_f32 v234, v234, v235\n" \
"v_mfma_f32_16x16x32_bf16 v[184:187], v[48:51], v[216:219], v[184:187]\n" \
"ds_read_b64_tr_b16 v[28:29], v233 offset:8704\n" \
"v_cvt_pk_bf16_f32 v236, v236, v237\n" \
"v_mfma_f32_16x16x32_bf16 v[184:187], v[52:55], v[220:223], v[184:187]\n" \
"global_load_lds_dwordx4 v226, s[46:47] offset:2048\n" \
"v_accvgpr_write_b32 a150, v234\n" \
"v_mfma_f32_16x16x32_bf16 v[180:183], v[40:43], v[216:219], v[180:183]\n" \
"ds_read_b64_tr_b16 v[30:31], v233 offset:10752\n" \
"v_accvgpr_write_b32 a151, v236\n" \
"v_mfma_f32_16x16x32_bf16 v[180:183], v[44:47], v[220:223], v[180:183]\n" \
"Lpg2767_%=:\n" \
"s_cbranch_execz Lpg4936_%=\n" \
"v_accvgpr_read_b32 v234, a152\n" \
"v_accvgpr_read_b32 v235, a153\n" \
"v_mfma_f32_16x16x32_bf16 v[176:179], v[32:35], v[216:219], v[176:179]\n" \
"v_accvgpr_read_b32 v236, a154\n" \
"v_accvgpr_read_b32 v237, a155\n" \
"v_mfma_f32_16x16x32_bf16 v[176:179], v[36:39], v[220:223], v[176:179]\n" \
"s_waitcnt lgkmcnt(1)\n" \
"global_load_lds_dwordx4 v227, s[46:47] offset:3072\n" \
"v_cvt_pk_bf16_f32 v234, v234, v235\n" \
"v_mfma_f32_16x16x32_bf16 v[64:67], v[0:3], v[192:195], v[64:67]\n" \
"s_add_u32 s46, s46, 0x80\n" \
"s_addc_u32 s47, s47, 0\n" \
"ds_read_b128 v[216:219], v230 offset:6144\n" \
"v_mfma_f32_16x16x32_bf16 v[64:67], v[4:7], v[196:199], v[64:67]\n" \
"s_add_u32 s90, s89, 0\n" \
"ds_read_b128 v[220:223], v231 offset:6144\n" \
"v_cvt_pk_bf16_f32 v236, v236, v237\n" \
"v_mfma_f32_16x16x32_bf16 v[68:71], v[8:11], v[192:195], v[68:71]\n" \
"ds_read_b64_tr_b16 v[32:33], v232 offset:1024\n" \
"v_accvgpr_write_b32 a152, v234\n" \
"v_mfma_f32_16x16x32_bf16 v[68:71], v[12:15], v[196:199], v[68:71]\n" \
"buffer_store_dwordx4 a[208:211], v248, s[56:59], s90 offen offset:0 sc1\n" \
"v_accvgpr_write_b32 a153, v236\n" \
"v_accvgpr_read_b32 v234, a184\n" \
"v_accvgpr_read_b32 v235, a185\n" \
"v_accvgpr_read_b32 v236, a186\n" \
"v_mfma_f32_16x16x32_bf16 v[72:75], v[16:19], v[192:195], v[72:75]\n" \
"ds_read_b64_tr_b16 v[34:35], v232 offset:3072\n" \
"v_accvgpr_read_b32 v237, a187\n" \
"v_mfma_f32_16x16x32_bf16 v[72:75], v[20:23], v[196:199], v[72:75]\n" \
"s_waitcnt lgkmcnt(3)\n" \
"ds_read_b64_tr_b16 v[36:37], v232 offset:9216\n" \
"v_cvt_pk_bf16_f32 v234, v234, v235\n" \
"v_mfma_f32_16x16x32_bf16 v[76:79], v[24:27], v[192:195], v[76:79]\n" \
"ds_read_b64_tr_b16 v[38:39], v232 offset:11264\n" \
"v_cvt_pk_bf16_f32 v236, v236, v237\n" \
"v_mfma_f32_16x16x32_bf16 v[76:79], v[28:31], v[196:199], v[76:79]\n" \
"v_accvgpr_write_b32 a154, v234\n" \
"v_accvgpr_write_b32 a155, v236\n" \
"v_mfma_f32_16x16x32_bf16 v[108:111], v[24:27], v[200:203], v[108:111]\n" \
"ds_read_b64_tr_b16 v[40:41], v233 offset:1024\n" \
"v_accvgpr_read_b32 v234, a156\n" \
"v_mfma_f32_16x16x32_bf16 v[108:111], v[28:31], v[204:207], v[108:111]\n" \
"ds_read_b64_tr_b16 v[42:43], v233 offset:3072\n" \
"v_accvgpr_read_b32 v235, a157\n" \
"v_mfma_f32_16x16x32_bf16 v[104:107], v[16:19], v[200:203], v[104:107]\n" \
"ds_read_b64_tr_b16 v[44:45], v233 offset:9216\n" \
"v_accvgpr_read_b32 v236, a158\n" \
"v_mfma_f32_16x16x32_bf16 v[104:107], v[20:23], v[204:207], v[104:107]\n" \
"v_accvgpr_read_b32 v237, a159\n" \
"v_cvt_pk_bf16_f32 v234, v234, v235\n" \
"v_mfma_f32_16x16x32_bf16 v[100:103], v[8:11], v[200:203], v[100:103]\n" \
"ds_read_b64_tr_b16 v[46:47], v233 offset:11264\n" \
"v_cvt_pk_bf16_f32 v236, v236, v237\n" \
"v_mfma_f32_16x16x32_bf16 v[100:103], v[12:15], v[204:207], v[100:103]\n" \
"ds_read_b64_tr_b16 v[48:49], v232 offset:1536\n" \
"v_accvgpr_write_b32 a156, v234\n" \
"v_mfma_f32_16x16x32_bf16 v[96:99], v[0:3], v[200:203], v[96:99]\n" \
"s_add_u32 m0, s95, 36864\n" \
"ds_read_b64_tr_b16 v[50:51], v232 offset:3584\n" \
"v_accvgpr_write_b32 a157, v236\n" \
"v_mfma_f32_16x16x32_bf16 v[96:99], v[4:7], v[204:207], v[96:99]\n" \
"global_load_lds_dwordx4 v228, s[44:45] offset:-4096\n" \
"v_accvgpr_read_b32 v234, a188\n" \
"v_mfma_f32_16x16x32_bf16 v[128:131], v[0:3], v[208:211], v[128:131]\n" \
"ds_read_b64_tr_b16 v[52:53], v232 offset:9728\n" \
"v_accvgpr_read_b32 v235, a189\n" \
"v_mfma_f32_16x16x32_bf16 v[128:131], v[4:7], v[212:215], v[128:131]\n" \
"ds_read_b64_tr_b16 v[54:55], v232 offset:11776\n" \
"v_accvgpr_read_b32 v236, a190\n" \
"v_mfma_f32_16x16x32_bf16 v[132:135], v[8:11], v[208:211], v[132:135]\n" \
"ds_read_b64_tr_b16 v[56:57], v233 offset:1536\n" \
"v_accvgpr_read_b32 v237, a191\n" \
"v_mfma_f32_16x16x32_bf16 v[132:135], v[12:15], v[212:215], v[132:135]\n" \
"global_load_lds_dwordx4 v229, s[44:45] offset:-3072\n" \
"v_cvt_pk_bf16_f32 v234, v234, v235\n" \
"v_mfma_f32_16x16x32_bf16 v[136:139], v[16:19], v[208:211], v[136:139]\n" \
"s_add_u32 s44, s44, 0x80000\n" \
"s_addc_u32 s45, s45, 0\n" \
"ds_read_b64_tr_b16 v[58:59], v233 offset:3584\n" \
"v_mfma_f32_16x16x32_bf16 v[136:139], v[20:23], v[212:215], v[136:139]\n" \
"ds_read_b64_tr_b16 v[60:61], v233 offset:9728\n" \
"v_cvt_pk_bf16_f32 v236, v236, v237\n" \
"v_mfma_f32_16x16x32_bf16 v[140:143], v[24:27], v[208:211], v[140:143]\n" \
"ds_read_b64_tr_b16 v[62:63], v233 offset:11776\n" \
"v_accvgpr_write_b32 a158, v234\n" \
"v_mfma_f32_16x16x32_bf16 v[140:143], v[28:31], v[212:215], v[140:143]\n" \
"s_waitcnt lgkmcnt(3)\n" \
"global_load_lds_dwordx4 v228, s[42:43] offset:-2048\n" \
"v_accvgpr_write_b32 a159, v236\n" \
"v_mfma_f32_16x16x32_bf16 v[172:175], v[24:27], v[216:219], v[172:175]\n" \
"v_add_u32 v230, 0xffff0000, v230\n" \
"v_add_u32 v231, 0xffff0000, v231\n" \
"v_mfma_f32_16x16x32_bf16 v[172:175], v[28:31], v[220:223], v[172:175]\n" \
"v_add_u32 v232, 0xffff4000, v232\n" \
"v_add_u32 v233, 0xffff4000, v233\n" \
"v_mfma_f32_16x16x32_bf16 v[168:171], v[16:19], v[216:219], v[168:171]\n" \
"v_accvgpr_read_b32 v234, a64\n" \
"v_accvgpr_read_b32 v235, a65\n" \
"v_mfma_f32_16x16x32_bf16 v[168:171], v[20:23], v[220:223], v[168:171]\n" \
"global_load_lds_dwordx4 v229, s[42:43] offset:-1024\n" \
"v_accvgpr_read_b32 v236, a66\n" \
"v_mfma_f32_16x16x32_bf16 v[164:167], v[8:11], v[216:219], v[164:167]\n" \
"s_add_u32 s42, s42, 0x80000\n" \
"s_addc_u32 s43, s43, 0\n" \
"v_accvgpr_read_b32 v237, a67\n" \
"v_mfma_f32_16x16x32_bf16 v[164:167], v[12:15], v[220:223], v[164:167]\n" \
"v_mul_f32 v234, s92, v234\n" \
"v_mul_f32 v235, s92, v235\n" \
"v_mfma_f32_16x16x32_bf16 v[160:163], v[0:3], v[216:219], v[160:163]\n" \
"s_waitcnt vmcnt(18) lgkmcnt(0)\n" \
"v_mul_f32 v236, s92, v236\n" \
"v_mul_f32 v237, s92, v237\n" \
"v_mfma_f32_16x16x32_bf16 v[160:163], v[4:7], v[220:223], v[160:163]\n" \
"s_barrier\n" \
"v_mfma_f32_16x16x32_bf16 v[80:83], v[32:35], v[192:195], v[80:83]\n" \
"ds_read_b64_tr_b16 v[0:1], v232 offset:0\n" \
"v_cvt_pk_bf16_f32 v234, v234, v235\n" \
"v_mfma_f32_16x16x32_bf16 v[80:83], v[36:39], v[196:199], v[80:83]\n" \
"ds_read_b64_tr_b16 v[2:3], v232 offset:2048\n" \
"v_cvt_pk_bf16_f32 v236, v236, v237\n" \
"v_mfma_f32_16x16x32_bf16 v[84:87], v[40:43], v[192:195], v[84:87]\n" \
"s_add_u32 m0, s87, 135168\n" \
"ds_read_b64_tr_b16 v[4:5], v232 offset:8192\n" \
"v_accvgpr_write_b32 a64, v234\n" \
"v_mfma_f32_16x16x32_bf16 v[84:87], v[44:47], v[196:199], v[84:87]\n" \
"global_load_lds_dwordx4 v224, s[48:49] offset:-4096\n" \
"v_accvgpr_write_b32 a65, v236\n" \
"v_mfma_f32_16x16x32_bf16 v[88:91], v[48:51], v[192:195], v[88:91]\n" \
"ds_read_b64_tr_b16 v[6:7], v232 offset:10240\n" \
"v_accvgpr_read_b32 v234, a68\n" \
"v_mfma_f32_16x16x32_bf16 v[88:91], v[52:55], v[196:199], v[88:91]\n" \
"ds_read_b64_tr_b16 v[8:9], v233 offset:0\n" \
"v_accvgpr_read_b32 v235, a69\n" \
"v_mfma_f32_16x16x32_bf16 v[92:95], v[56:59], v[192:195], v[92:95]\n" \
"ds_read_b64_tr_b16 v[10:11], v233 offset:2048\n" \
"v_accvgpr_read_b32 v236, a70\n" \
"v_mfma_f32_16x16x32_bf16 v[92:95], v[60:63], v[196:199], v[92:95]\n" \
"global_load_lds_dwordx4 v225, s[48:49] offset:-3072\n" \
"v_accvgpr_read_b32 v237, a71\n" \
"v_mfma_f32_16x16x32_bf16 v[124:127], v[56:59], v[200:203], v[124:127]\n" \
"ds_read_b128 v[192:195], v230 offset:0\n" \
"v_mul_f32 v234, s92, v234\n" \
"v_mfma_f32_16x16x32_bf16 v[124:127], v[60:63], v[204:207], v[124:127]\n" \
"ds_read_b128 v[196:199], v231 offset:0\n" \
"v_mul_f32 v235, s92, v235\n" \
"v_mfma_f32_16x16x32_bf16 v[120:123], v[48:51], v[200:203], v[120:123]\n" \
"ds_read_b64_tr_b16 v[12:13], v233 offset:8192\n" \
"v_mul_f32 v236, s92, v236\n" \
"v_mfma_f32_16x16x32_bf16 v[120:123], v[52:55], v[204:207], v[120:123]\n" \
"global_load_lds_dwordx4 v226, s[48:49] offset:-2048\n" \
"v_mul_f32 v237, s92, v237\n" \
"v_mfma_f32_16x16x32_bf16 v[116:119], v[40:43], v[200:203], v[116:119]\n" \
"ds_read_b64_tr_b16 v[14:15], v233 offset:10240\n" \
"v_cvt_pk_bf16_f32 v234, v234, v235\n" \
"v_mfma_f32_16x16x32_bf16 v[116:119], v[44:47], v[204:207], v[116:119]\n" \
"ds_read_b64_tr_b16 v[16:17], v232 offset:512\n" \
"v_cvt_pk_bf16_f32 v236, v236, v237\n" \
"v_mfma_f32_16x16x32_bf16 v[112:115], v[32:35], v[200:203], v[112:115]\n" \
"ds_read_b64_tr_b16 v[18:19], v232 offset:2560\n" \
"v_accvgpr_write_b32 a66, v234\n" \
"v_mfma_f32_16x16x32_bf16 v[112:115], v[36:39], v[204:207], v[112:115]\n" \
"global_load_lds_dwordx4 v227, s[48:49] offset:-1024\n" \
"v_accvgpr_write_b32 a67, v236\n" \
"v_mfma_f32_16x16x32_bf16 v[144:147], v[32:35], v[208:211], v[144:147]\n" \
"s_add_u32 s48, s48, 0x80\n" \
"s_addc_u32 s49, s49, 0\n" \
"ds_read_b128 v[200:203], v230 offset:2048\n" \
"v_mfma_f32_16x16x32_bf16 v[144:147], v[36:39], v[212:215], v[144:147]\n" \
"ds_read_b128 v[204:207], v231 offset:2048\n" \
"v_accvgpr_read_b32 v234, a96\n" \
"v_mfma_f32_16x16x32_bf16 v[148:151], v[40:43], v[208:211], v[148:151]\n" \
"ds_read_b64_tr_b16 v[20:21], v232 offset:8704\n" \
"v_accvgpr_read_b32 v235, a97\n" \
"v_mfma_f32_16x16x32_bf16 v[148:151], v[44:47], v[212:215], v[148:151]\n" \
"global_load_lds_dwordx4 v224, s[46:47] offset:0\n" \
"v_accvgpr_read_b32 v236, a98\n" \
"v_mfma_f32_16x16x32_bf16 v[152:155], v[48:51], v[208:211], v[152:155]\n" \
"ds_read_b64_tr_b16 v[22:23], v232 offset:10752\n" \
"v_accvgpr_read_b32 v237, a99\n" \
"v_mfma_f32_16x16x32_bf16 v[152:155], v[52:55], v[212:215], v[152:155]\n" \
"ds_read_b64_tr_b16 v[24:25], v233 offset:512\n" \
"v_mul_f32 v234, s92, v234\n" \
"v_mfma_f32_16x16x32_bf16 v[156:159], v[56:59], v[208:211], v[156:159]\n" \
"ds_read_b64_tr_b16 v[26:27], v233 offset:2560\n" \
"v_mul_f32 v235, s92, v235\n" \
"v_mfma_f32_16x16x32_bf16 v[156:159], v[60:63], v[212:215], v[156:159]\n" \
"global_load_lds_dwordx4 v225, s[46:47] offset:1024\n" \
"v_mul_f32 v236, s92, v236\n" \
"v_mfma_f32_16x16x32_bf16 v[188:191], v[56:59], v[216:219], v[188:191]\n" \
"ds_read_b128 v[208:211], v230 offset:4096\n" \
"v_mul_f32 v237, s92, v237\n" \
"v_mfma_f32_16x16x32_bf16 v[188:191], v[60:63], v[220:223], v[188:191]\n" \
"ds_read_b128 v[212:215], v231 offset:4096\n" \
"v_cvt_pk_bf16_f32 v234, v234, v235\n" \
"v_mfma_f32_16x16x32_bf16 v[184:187], v[48:51], v[216:219], v[184:187]\n" \
"ds_read_b64_tr_b16 v[28:29], v233 offset:8704\n" \
"v_cvt_pk_bf16_f32 v236, v236, v237\n" \
"v_mfma_f32_16x16x32_bf16 v[184:187], v[52:55], v[220:223], v[184:187]\n" \
"global_load_lds_dwordx4 v226, s[46:47] offset:2048\n" \
"v_accvgpr_write_b32 a96, v234\n" \
"v_mfma_f32_16x16x32_bf16 v[180:183], v[40:43], v[216:219], v[180:183]\n" \
"ds_read_b64_tr_b16 v[30:31], v233 offset:10752\n" \
"v_accvgpr_write_b32 a97, v236\n" \
"v_mfma_f32_16x16x32_bf16 v[180:183], v[44:47], v[220:223], v[180:183]\n" \
"v_accvgpr_read_b32 v234, a100\n" \
"v_accvgpr_read_b32 v235, a101\n" \
"v_mfma_f32_16x16x32_bf16 v[176:179], v[32:35], v[216:219], v[176:179]\n" \
"v_accvgpr_read_b32 v236, a102\n" \
"v_accvgpr_read_b32 v237, a103\n" \
"v_mfma_f32_16x16x32_bf16 v[176:179], v[36:39], v[220:223], v[176:179]\n" \
"s_waitcnt lgkmcnt(1)\n" \
"global_load_lds_dwordx4 v227, s[46:47] offset:3072\n" \
"v_mul_f32 v234, s92, v234\n" \
"v_mfma_f32_16x16x32_bf16 v[64:67], v[0:3], v[192:195], v[64:67]\n" \
"s_add_u32 s46, s46, 0x80\n" \
"s_addc_u32 s47, s47, 0\n" \
"ds_read_b128 v[216:219], v230 offset:6144\n" \
"v_mfma_f32_16x16x32_bf16 v[64:67], v[4:7], v[196:199], v[64:67]\n" \
"ds_read_b128 v[220:223], v231 offset:6144\n" \
"v_mul_f32 v235, s92, v235\n" \
"v_mfma_f32_16x16x32_bf16 v[68:71], v[8:11], v[192:195], v[68:71]\n" \
"ds_read_b64_tr_b16 v[32:33], v232 offset:1024\n" \
"v_mul_f32 v236, s92, v236\n" \
"v_mfma_f32_16x16x32_bf16 v[68:71], v[12:15], v[196:199], v[68:71]\n" \
"buffer_store_dwordx4 a[212:215], v248, s[56:59], s90 offen offset:1024 sc1\n" \
"v_mul_f32 v237, s92, v237\n" \
"v_cvt_pk_bf16_f32 v234, v234, v235\n" \
"v_cvt_pk_bf16_f32 v236, v236, v237\n" \
"v_accvgpr_write_b32 a98, v234\n" \
"v_mfma_f32_16x16x32_bf16 v[72:75], v[16:19], v[192:195], v[72:75]\n" \
"ds_read_b64_tr_b16 v[34:35], v232 offset:3072\n" \
"v_accvgpr_write_b32 a99, v236\n" \
"v_mfma_f32_16x16x32_bf16 v[72:75], v[20:23], v[196:199], v[72:75]\n" \
"s_waitcnt lgkmcnt(3)\n" \
"ds_read_b64_tr_b16 v[36:37], v232 offset:9216\n" \
"v_accvgpr_read_b32 v234, a72\n" \
"v_mfma_f32_16x16x32_bf16 v[76:79], v[24:27], v[192:195], v[76:79]\n" \
"ds_read_b64_tr_b16 v[38:39], v232 offset:11264\n" \
"v_accvgpr_read_b32 v235, a73\n" \
"v_mfma_f32_16x16x32_bf16 v[76:79], v[28:31], v[196:199], v[76:79]\n" \
"buffer_store_dwordx4 a[216:219], v248, s[56:59], s90 offen offset:2048 sc1\n" \
"v_accvgpr_read_b32 v236, a74\n" \
"v_accvgpr_read_b32 v237, a75\n" \
"v_mul_f32 v234, s92, v234\n" \
"v_mul_f32 v235, s92, v235\n" \
"v_mfma_f32_16x16x32_bf16 v[108:111], v[24:27], v[200:203], v[108:111]\n" \
"ds_read_b64_tr_b16 v[40:41], v233 offset:1024\n" \
"v_mul_f32 v236, s92, v236\n" \
"v_mfma_f32_16x16x32_bf16 v[108:111], v[28:31], v[204:207], v[108:111]\n" \
"ds_read_b64_tr_b16 v[42:43], v233 offset:3072\n" \
"v_mul_f32 v237, s92, v237\n" \
"v_mfma_f32_16x16x32_bf16 v[104:107], v[16:19], v[200:203], v[104:107]\n" \
"ds_read_b64_tr_b16 v[44:45], v233 offset:9216\n" \
"v_cvt_pk_bf16_f32 v234, v234, v235\n" \
"v_mfma_f32_16x16x32_bf16 v[104:107], v[20:23], v[204:207], v[104:107]\n" \
"v_cvt_pk_bf16_f32 v236, v236, v237\n" \
"v_accvgpr_write_b32 a72, v234\n" \
"v_mfma_f32_16x16x32_bf16 v[100:103], v[8:11], v[200:203], v[100:103]\n" \
"ds_read_b64_tr_b16 v[46:47], v233 offset:11264\n" \
"v_accvgpr_write_b32 a73, v236\n" \
"v_mfma_f32_16x16x32_bf16 v[100:103], v[12:15], v[204:207], v[100:103]\n" \
"ds_read_b64_tr_b16 v[48:49], v232 offset:1536\n" \
"v_accvgpr_read_b32 v234, a76\n" \
"v_mfma_f32_16x16x32_bf16 v[96:99], v[0:3], v[200:203], v[96:99]\n" \
"s_add_u32 m0, s95, 53248\n" \
"ds_read_b64_tr_b16 v[50:51], v232 offset:3584\n" \
"v_accvgpr_read_b32 v235, a77\n" \
"v_mfma_f32_16x16x32_bf16 v[96:99], v[4:7], v[204:207], v[96:99]\n" \
"global_load_lds_dwordx4 v228, s[44:45] offset:-4096\n" \
"v_accvgpr_read_b32 v236, a78\n" \
"v_mfma_f32_16x16x32_bf16 v[128:131], v[0:3], v[208:211], v[128:131]\n" \
"ds_read_b64_tr_b16 v[52:53], v232 offset:9728\n" \
"v_accvgpr_read_b32 v237, a79\n" \
"v_mfma_f32_16x16x32_bf16 v[128:131], v[4:7], v[212:215], v[128:131]\n" \
"ds_read_b64_tr_b16 v[54:55], v232 offset:11776\n" \
"v_mul_f32 v234, s92, v234\n" \
"v_mfma_f32_16x16x32_bf16 v[132:135], v[8:11], v[208:211], v[132:135]\n" \
"ds_read_b64_tr_b16 v[56:57], v233 offset:1536\n" \
"v_mul_f32 v235, s92, v235\n" \
"v_mfma_f32_16x16x32_bf16 v[132:135], v[12:15], v[212:215], v[132:135]\n" \
"global_load_lds_dwordx4 v229, s[44:45] offset:-3072\n" \
"v_mul_f32 v236, s92, v236\n" \
"v_mfma_f32_16x16x32_bf16 v[136:139], v[16:19], v[208:211], v[136:139]\n" \
"s_add_u32 s44, s44, 0x80000\n" \
"s_addc_u32 s45, s45, 0\n" \
"ds_read_b64_tr_b16 v[58:59], v233 offset:3584\n" \
"v_mfma_f32_16x16x32_bf16 v[136:139], v[20:23], v[212:215], v[136:139]\n" \
"ds_read_b64_tr_b16 v[60:61], v233 offset:9728\n" \
"v_mul_f32 v237, s92, v237\n" \
"v_mfma_f32_16x16x32_bf16 v[140:143], v[24:27], v[208:211], v[140:143]\n" \
"ds_read_b64_tr_b16 v[62:63], v233 offset:11776\n" \
"v_cvt_pk_bf16_f32 v234, v234, v235\n" \
"v_mfma_f32_16x16x32_bf16 v[140:143], v[28:31], v[212:215], v[140:143]\n" \
"s_waitcnt lgkmcnt(3)\n" \
"global_load_lds_dwordx4 v228, s[42:43] offset:-2048\n" \
"v_cvt_pk_bf16_f32 v236, v236, v237\n" \
"v_mfma_f32_16x16x32_bf16 v[172:175], v[24:27], v[216:219], v[172:175]\n" \
"v_add_u32 v230, 0x8000, v230\n" \
"v_add_u32 v231, 0x8000, v231\n" \
"v_mfma_f32_16x16x32_bf16 v[172:175], v[28:31], v[220:223], v[172:175]\n" \
"v_add_u32 v232, 0x4000, v232\n" \
"v_add_u32 v233, 0x4000, v233\n" \
"v_mfma_f32_16x16x32_bf16 v[168:171], v[16:19], v[216:219], v[168:171]\n" \
"v_accvgpr_write_b32 a74, v234\n" \
"v_accvgpr_write_b32 a75, v236\n" \
"v_mfma_f32_16x16x32_bf16 v[168:171], v[20:23], v[220:223], v[168:171]\n" \
"global_load_lds_dwordx4 v229, s[42:43] offset:-1024\n" \
"v_accvgpr_read_b32 v234, a104\n" \
"v_mfma_f32_16x16x32_bf16 v[164:167], v[8:11], v[216:219], v[164:167]\n" \
"s_add_u32 s42, s42, 0x80000\n" \
"s_addc_u32 s43, s43, 0\n" \
"v_accvgpr_read_b32 v235, a105\n" \
"v_mfma_f32_16x16x32_bf16 v[164:167], v[12:15], v[220:223], v[164:167]\n" \
"v_accvgpr_read_b32 v236, a106\n" \
"v_accvgpr_read_b32 v237, a107\n" \
"v_mfma_f32_16x16x32_bf16 v[160:163], v[0:3], v[216:219], v[160:163]\n" \
"s_waitcnt vmcnt(19) lgkmcnt(0)\n" \
"v_mul_f32 v234, s92, v234\n" \
"v_mul_f32 v235, s92, v235\n" \
"v_mfma_f32_16x16x32_bf16 v[160:163], v[4:7], v[220:223], v[160:163]\n" \
"s_barrier\n" \
"v_mfma_f32_16x16x32_bf16 v[80:83], v[32:35], v[192:195], v[80:83]\n" \
"ds_read_b64_tr_b16 v[0:1], v232 offset:0\n" \
"v_mul_f32 v236, s92, v236\n" \
"v_mfma_f32_16x16x32_bf16 v[80:83], v[36:39], v[196:199], v[80:83]\n" \
"ds_read_b64_tr_b16 v[2:3], v232 offset:2048\n" \
"v_mul_f32 v237, s92, v237\n" \
"v_mfma_f32_16x16x32_bf16 v[84:87], v[40:43], v[192:195], v[84:87]\n" \
"s_add_u32 m0, s87, 69632\n" \
"ds_read_b64_tr_b16 v[4:5], v232 offset:8192\n" \
"v_cvt_pk_bf16_f32 v234, v234, v235\n" \
"v_mfma_f32_16x16x32_bf16 v[84:87], v[44:47], v[196:199], v[84:87]\n" \
"global_load_lds_dwordx4 v224, s[48:49] offset:-4096\n" \
"v_cvt_pk_bf16_f32 v236, v236, v237\n" \
"v_mfma_f32_16x16x32_bf16 v[88:91], v[48:51], v[192:195], v[88:91]\n" \
"ds_read_b64_tr_b16 v[6:7], v232 offset:10240\n" \
"v_accvgpr_write_b32 a104, v234\n" \
"v_mfma_f32_16x16x32_bf16 v[88:91], v[52:55], v[196:199], v[88:91]\n" \
"ds_read_b64_tr_b16 v[8:9], v233 offset:0\n" \
"v_accvgpr_write_b32 a105, v236\n" \
"v_mfma_f32_16x16x32_bf16 v[92:95], v[56:59], v[192:195], v[92:95]\n" \
"ds_read_b64_tr_b16 v[10:11], v233 offset:2048\n" \
"v_accvgpr_read_b32 v234, a108\n" \
"v_mfma_f32_16x16x32_bf16 v[92:95], v[60:63], v[196:199], v[92:95]\n" \
"global_load_lds_dwordx4 v225, s[48:49] offset:-3072\n" \
"v_accvgpr_read_b32 v235, a109\n" \
"v_mfma_f32_16x16x32_bf16 v[124:127], v[56:59], v[200:203], v[124:127]\n" \
"ds_read_b128 v[192:195], v230 offset:0\n" \
"v_accvgpr_read_b32 v236, a110\n" \
"v_mfma_f32_16x16x32_bf16 v[124:127], v[60:63], v[204:207], v[124:127]\n" \
"ds_read_b128 v[196:199], v231 offset:0\n" \
"v_accvgpr_read_b32 v237, a111\n" \
"v_mfma_f32_16x16x32_bf16 v[120:123], v[48:51], v[200:203], v[120:123]\n" \
"ds_read_b64_tr_b16 v[12:13], v233 offset:8192\n" \
"v_mul_f32 v234, s92, v234\n" \
"v_mfma_f32_16x16x32_bf16 v[120:123], v[52:55], v[204:207], v[120:123]\n" \
"global_load_lds_dwordx4 v226, s[48:49] offset:-2048\n" \
"v_mul_f32 v235, s92, v235\n" \
"v_mfma_f32_16x16x32_bf16 v[116:119], v[40:43], v[200:203], v[116:119]\n" \
"ds_read_b64_tr_b16 v[14:15], v233 offset:10240\n" \
"v_mul_f32 v236, s92, v236\n" \
"v_mfma_f32_16x16x32_bf16 v[116:119], v[44:47], v[204:207], v[116:119]\n" \
"ds_read_b64_tr_b16 v[16:17], v232 offset:512\n" \
"v_mul_f32 v237, s92, v237\n" \
"v_mfma_f32_16x16x32_bf16 v[112:115], v[32:35], v[200:203], v[112:115]\n" \
"ds_read_b64_tr_b16 v[18:19], v232 offset:2560\n" \
"v_cvt_pk_bf16_f32 v234, v234, v235\n" \
"v_mfma_f32_16x16x32_bf16 v[112:115], v[36:39], v[204:207], v[112:115]\n" \
"global_load_lds_dwordx4 v227, s[48:49] offset:-1024\n" \
"v_cvt_pk_bf16_f32 v236, v236, v237\n" \
"v_mfma_f32_16x16x32_bf16 v[144:147], v[32:35], v[208:211], v[144:147]\n" \
"s_add_u32 s48, s48, 0x80\n" \
"s_addc_u32 s49, s49, 0\n" \
"ds_read_b128 v[200:203], v230 offset:2048\n" \
"v_mfma_f32_16x16x32_bf16 v[144:147], v[36:39], v[212:215], v[144:147]\n" \
"ds_read_b128 v[204:207], v231 offset:2048\n" \
"v_accvgpr_write_b32 a106, v234\n" \
"v_mfma_f32_16x16x32_bf16 v[148:151], v[40:43], v[208:211], v[148:151]\n" \
"ds_read_b64_tr_b16 v[20:21], v232 offset:8704\n" \
"v_accvgpr_write_b32 a107, v236\n" \
"v_mfma_f32_16x16x32_bf16 v[148:151], v[44:47], v[212:215], v[148:151]\n" \
"global_load_lds_dwordx4 v224, s[46:47] offset:0\n" \
"v_accvgpr_read_b32 v234, a80\n" \
"v_mfma_f32_16x16x32_bf16 v[152:155], v[48:51], v[208:211], v[152:155]\n" \
"ds_read_b64_tr_b16 v[22:23], v232 offset:10752\n" \
"v_accvgpr_read_b32 v235, a81\n" \
"v_mfma_f32_16x16x32_bf16 v[152:155], v[52:55], v[212:215], v[152:155]\n" \
"ds_read_b64_tr_b16 v[24:25], v233 offset:512\n" \
"v_accvgpr_read_b32 v236, a82\n" \
"v_mfma_f32_16x16x32_bf16 v[156:159], v[56:59], v[208:211], v[156:159]\n" \
"ds_read_b64_tr_b16 v[26:27], v233 offset:2560\n" \
"v_accvgpr_read_b32 v237, a83\n" \
"v_mfma_f32_16x16x32_bf16 v[156:159], v[60:63], v[212:215], v[156:159]\n" \
"global_load_lds_dwordx4 v225, s[46:47] offset:1024\n" \
"v_cvt_pk_bf16_f32 v234, v234, v235\n" \
"v_mfma_f32_16x16x32_bf16 v[188:191], v[56:59], v[216:219], v[188:191]\n" \
"ds_read_b128 v[208:211], v230 offset:4096\n" \
"v_cvt_pk_bf16_f32 v236, v236, v237\n" \
"v_mfma_f32_16x16x32_bf16 v[188:191], v[60:63], v[220:223], v[188:191]\n" \
"ds_read_b128 v[212:215], v231 offset:4096\n" \
"v_accvgpr_write_b32 a80, v234\n" \
"v_mfma_f32_16x16x32_bf16 v[184:187], v[48:51], v[216:219], v[184:187]\n" \
"ds_read_b64_tr_b16 v[28:29], v233 offset:8704\n" \
"v_accvgpr_write_b32 a81, v236\n" \
"v_mfma_f32_16x16x32_bf16 v[184:187], v[52:55], v[220:223], v[184:187]\n" \
"global_load_lds_dwordx4 v226, s[46:47] offset:2048\n" \
"v_accvgpr_read_b32 v234, a112\n" \
"v_mfma_f32_16x16x32_bf16 v[180:183], v[40:43], v[216:219], v[180:183]\n" \
"ds_read_b64_tr_b16 v[30:31], v233 offset:10752\n" \
"v_accvgpr_read_b32 v235, a113\n" \
"v_mfma_f32_16x16x32_bf16 v[180:183], v[44:47], v[220:223], v[180:183]\n" \
"v_accvgpr_read_b32 v236, a114\n" \
"v_accvgpr_read_b32 v237, a115\n" \
"v_mfma_f32_16x16x32_bf16 v[176:179], v[32:35], v[216:219], v[176:179]\n" \
"v_cvt_pk_bf16_f32 v234, v234, v235\n" \
"v_cvt_pk_bf16_f32 v236, v236, v237\n" \
"v_mfma_f32_16x16x32_bf16 v[176:179], v[36:39], v[220:223], v[176:179]\n" \
"s_waitcnt lgkmcnt(1)\n" \
"global_load_lds_dwordx4 v227, s[46:47] offset:3072\n" \
"v_accvgpr_write_b32 a82, v234\n" \
"v_mfma_f32_16x16x32_bf16 v[64:67], v[0:3], v[192:195], v[64:67]\n" \
"s_add_u32 s46, s46, 0x80\n" \
"s_addc_u32 s47, s47, 0\n" \
"ds_read_b128 v[216:219], v230 offset:6144\n" \
"v_mfma_f32_16x16x32_bf16 v[64:67], v[4:7], v[196:199], v[64:67]\n" \
"ds_read_b128 v[220:223], v231 offset:6144\n" \
"v_accvgpr_write_b32 a83, v236\n" \
"v_mfma_f32_16x16x32_bf16 v[68:71], v[8:11], v[192:195], v[68:71]\n" \
"ds_read_b64_tr_b16 v[32:33], v232 offset:1024\n" \
"v_accvgpr_read_b32 v234, a84\n" \
"v_mfma_f32_16x16x32_bf16 v[68:71], v[12:15], v[196:199], v[68:71]\n" \
"buffer_store_dwordx4 a[220:223], v248, s[56:59], s90 offen offset:3072 sc1\n" \
"v_accvgpr_read_b32 v235, a85\n" \
"v_accvgpr_read_b32 v236, a86\n" \
"v_accvgpr_read_b32 v237, a87\n" \
"v_cvt_pk_bf16_f32 v234, v234, v235\n" \
"v_mfma_f32_16x16x32_bf16 v[72:75], v[16:19], v[192:195], v[72:75]\n" \
"ds_read_b64_tr_b16 v[34:35], v232 offset:3072\n" \
"v_cvt_pk_bf16_f32 v236, v236, v237\n" \
"v_mfma_f32_16x16x32_bf16 v[72:75], v[20:23], v[196:199], v[72:75]\n" \
"s_waitcnt lgkmcnt(3)\n" \
"ds_read_b64_tr_b16 v[36:37], v232 offset:9216\n" \
"v_accvgpr_write_b32 a84, v234\n" \
"v_mfma_f32_16x16x32_bf16 v[76:79], v[24:27], v[192:195], v[76:79]\n" \
"ds_read_b64_tr_b16 v[38:39], v232 offset:11264\n" \
"v_accvgpr_write_b32 a85, v236\n" \
"v_mfma_f32_16x16x32_bf16 v[76:79], v[28:31], v[196:199], v[76:79]\n" \
"v_accvgpr_read_b32 v234, a116\n" \
"v_accvgpr_read_b32 v235, a117\n" \
"v_mfma_f32_16x16x32_bf16 v[108:111], v[24:27], v[200:203], v[108:111]\n" \
"ds_read_b64_tr_b16 v[40:41], v233 offset:1024\n" \
"v_accvgpr_read_b32 v236, a118\n" \
"v_mfma_f32_16x16x32_bf16 v[108:111], v[28:31], v[204:207], v[108:111]\n" \
"ds_read_b64_tr_b16 v[42:43], v233 offset:3072\n" \
"v_accvgpr_read_b32 v237, a119\n" \
"v_mfma_f32_16x16x32_bf16 v[104:107], v[16:19], v[200:203], v[104:107]\n" \
"ds_read_b64_tr_b16 v[44:45], v233 offset:9216\n" \
"v_cvt_pk_bf16_f32 v234, v234, v235\n" \
"v_mfma_f32_16x16x32_bf16 v[104:107], v[20:23], v[204:207], v[104:107]\n" \
"v_cvt_pk_bf16_f32 v236, v236, v237\n" \
"v_accvgpr_write_b32 a86, v234\n" \
"v_mfma_f32_16x16x32_bf16 v[100:103], v[8:11], v[200:203], v[100:103]\n" \
"ds_read_b64_tr_b16 v[46:47], v233 offset:11264\n" \
"v_accvgpr_write_b32 a87, v236\n" \
"v_mfma_f32_16x16x32_bf16 v[100:103], v[12:15], v[204:207], v[100:103]\n" \
"ds_read_b64_tr_b16 v[48:49], v232 offset:1536\n" \
"v_accvgpr_read_b32 v234, a88\n" \
"v_mfma_f32_16x16x32_bf16 v[96:99], v[0:3], v[200:203], v[96:99]\n" \
"s_add_u32 m0, s95, 4096\n" \
"ds_read_b64_tr_b16 v[50:51], v232 offset:3584\n" \
"v_accvgpr_read_b32 v235, a89\n" \
"v_mfma_f32_16x16x32_bf16 v[96:99], v[4:7], v[204:207], v[96:99]\n" \
"global_load_lds_dwordx4 v228, s[44:45] offset:-4096\n" \
"v_accvgpr_read_b32 v236, a90\n" \
"v_mfma_f32_16x16x32_bf16 v[128:131], v[0:3], v[208:211], v[128:131]\n" \
"ds_read_b64_tr_b16 v[52:53], v232 offset:9728\n" \
"v_accvgpr_read_b32 v237, a91\n" \
"v_mfma_f32_16x16x32_bf16 v[128:131], v[4:7], v[212:215], v[128:131]\n" \
"ds_read_b64_tr_b16 v[54:55], v232 offset:11776\n" \
"v_cvt_pk_bf16_f32 v234, v234, v235\n" \
"v_mfma_f32_16x16x32_bf16 v[132:135], v[8:11], v[208:211], v[132:135]\n" \
"ds_read_b64_tr_b16 v[56:57], v233 offset:1536\n" \
"v_cvt_pk_bf16_f32 v236, v236, v237\n" \
"v_mfma_f32_16x16x32_bf16 v[132:135], v[12:15], v[212:215], v[132:135]\n" \
"global_load_lds_dwordx4 v229, s[44:45] offset:-3072\n" \
"v_accvgpr_write_b32 a88, v234\n" \
"v_mfma_f32_16x16x32_bf16 v[136:139], v[16:19], v[208:211], v[136:139]\n" \
"s_add_u32 s44, s44, 0x80000\n" \
"s_addc_u32 s45, s45, 0\n" \
"ds_read_b64_tr_b16 v[58:59], v233 offset:3584\n" \
"v_mfma_f32_16x16x32_bf16 v[136:139], v[20:23], v[212:215], v[136:139]\n" \
"ds_read_b64_tr_b16 v[60:61], v233 offset:9728\n" \
"v_accvgpr_write_b32 a89, v236\n" \
"v_mfma_f32_16x16x32_bf16 v[140:143], v[24:27], v[208:211], v[140:143]\n" \
"ds_read_b64_tr_b16 v[62:63], v233 offset:11776\n" \
"v_accvgpr_read_b32 v234, a120\n" \
"v_mfma_f32_16x16x32_bf16 v[140:143], v[28:31], v[212:215], v[140:143]\n" \
"s_waitcnt lgkmcnt(3)\n" \
"global_load_lds_dwordx4 v228, s[42:43] offset:-2048\n" \
"v_accvgpr_read_b32 v235, a121\n" \
"v_mfma_f32_16x16x32_bf16 v[172:175], v[24:27], v[216:219], v[172:175]\n" \
"v_add_u32 v230, 0x8000, v230\n" \
"v_add_u32 v231, 0x8000, v231\n" \
"v_mfma_f32_16x16x32_bf16 v[172:175], v[28:31], v[220:223], v[172:175]\n" \
"v_add_u32 v232, 0x4000, v232\n" \
"v_add_u32 v233, 0x4000, v233\n" \
"v_mfma_f32_16x16x32_bf16 v[168:171], v[16:19], v[216:219], v[168:171]\n" \
"v_accvgpr_read_b32 v236, a122\n" \
"v_accvgpr_read_b32 v237, a123\n" \
"v_mfma_f32_16x16x32_bf16 v[168:171], v[20:23], v[220:223], v[168:171]\n" \
"global_load_lds_dwordx4 v229, s[42:43] offset:-1024\n" \
"v_cvt_pk_bf16_f32 v234, v234, v235\n" \
"v_mfma_f32_16x16x32_bf16 v[164:167], v[8:11], v[216:219], v[164:167]\n" \
"s_add_u32 s42, s42, 0x80000\n" \
"s_addc_u32 s43, s43, 0\n" \
"v_cvt_pk_bf16_f32 v236, v236, v237\n" \
"v_mfma_f32_16x16x32_bf16 v[164:167], v[12:15], v[220:223], v[164:167]\n" \
"v_accvgpr_write_b32 a90, v234\n" \
"v_accvgpr_write_b32 a91, v236\n" \
"v_mfma_f32_16x16x32_bf16 v[160:163], v[0:3], v[216:219], v[160:163]\n" \
"s_waitcnt vmcnt(19) lgkmcnt(0)\n" \
"v_accvgpr_read_b32 v234, a92\n" \
"v_accvgpr_read_b32 v235, a93\n" \
"v_mfma_f32_16x16x32_bf16 v[160:163], v[4:7], v[220:223], v[160:163]\n" \
"s_barrier\n" \
"v_mfma_f32_16x16x32_bf16 v[80:83], v[32:35], v[192:195], v[80:83]\n" \
"ds_read_b64_tr_b16 v[0:1], v232 offset:0\n" \
"v_accvgpr_read_b32 v236, a94\n" \
"v_mfma_f32_16x16x32_bf16 v[80:83], v[36:39], v[196:199], v[80:83]\n" \
"ds_read_b64_tr_b16 v[2:3], v232 offset:2048\n" \
"v_accvgpr_read_b32 v237, a95\n" \
"v_mfma_f32_16x16x32_bf16 v[84:87], v[40:43], v[192:195], v[84:87]\n" \
"s_add_u32 m0, s87, 102400\n" \
"ds_read_b64_tr_b16 v[4:5], v232 offset:8192\n" \
"v_cvt_pk_bf16_f32 v234, v234, v235\n" \
"v_mfma_f32_16x16x32_bf16 v[84:87], v[44:47], v[196:199], v[84:87]\n" \
"global_load_lds_dwordx4 v224, s[48:49] offset:-4096\n" \
"v_cvt_pk_bf16_f32 v236, v236, v237\n" \
"v_mfma_f32_16x16x32_bf16 v[88:91], v[48:51], v[192:195], v[88:91]\n" \
"ds_read_b64_tr_b16 v[6:7], v232 offset:10240\n" \
"Lpg3309_%=:\n" \
"s_cbranch_execz Lpg5391_%=\n" \
"v_accvgpr_write_b32 a92, v234\n" \
"v_mfma_f32_16x16x32_bf16 v[88:91], v[52:55], v[196:199], v[88:91]\n" \
"ds_read_b64_tr_b16 v[8:9], v233 offset:0\n" \
"v_accvgpr_write_b32 a93, v236\n" \
"v_mfma_f32_16x16x32_bf16 v[92:95], v[56:59], v[192:195], v[92:95]\n" \
"ds_read_b64_tr_b16 v[10:11], v233 offset:2048\n" \
"v_accvgpr_read_b32 v234, a124\n" \
"v_mfma_f32_16x16x32_bf16 v[92:95], v[60:63], v[196:199], v[92:95]\n" \
"global_load_lds_dwordx4 v225, s[48:49] offset:-3072\n" \
"v_accvgpr_read_b32 v235, a125\n" \
"v_mfma_f32_16x16x32_bf16 v[124:127], v[56:59], v[200:203], v[124:127]\n" \
"ds_read_b128 v[192:195], v230 offset:0\n" \
"v_accvgpr_read_b32 v236, a126\n" \
"v_mfma_f32_16x16x32_bf16 v[124:127], v[60:63], v[204:207], v[124:127]\n" \
"ds_read_b128 v[196:199], v231 offset:0\n" \
"v_accvgpr_read_b32 v237, a127\n" \
"v_mfma_f32_16x16x32_bf16 v[120:123], v[48:51], v[200:203], v[120:123]\n" \
"ds_read_b64_tr_b16 v[12:13], v233 offset:8192\n" \
"v_cvt_pk_bf16_f32 v234, v234, v235\n" \
"v_mfma_f32_16x16x32_bf16 v[120:123], v[52:55], v[204:207], v[120:123]\n" \
"global_load_lds_dwordx4 v226, s[48:49] offset:-2048\n" \
"v_cvt_pk_bf16_f32 v236, v236, v237\n" \
"v_mfma_f32_16x16x32_bf16 v[116:119], v[40:43], v[200:203], v[116:119]\n" \
"ds_read_b64_tr_b16 v[14:15], v233 offset:10240\n" \
"v_accvgpr_write_b32 a94, v234\n" \
"v_mfma_f32_16x16x32_bf16 v[116:119], v[44:47], v[204:207], v[116:119]\n" \
"ds_read_b64_tr_b16 v[16:17], v232 offset:512\n" \
"v_accvgpr_write_b32 a95, v236\n" \
"v_mfma_f32_16x16x32_bf16 v[112:115], v[32:35], v[200:203], v[112:115]\n" \
"ds_read_b64_tr_b16 v[18:19], v232 offset:2560\n" \
"v_accvgpr_read_b32 v234, a0\n" \
"v_mfma_f32_16x16x32_bf16 v[112:115], v[36:39], v[204:207], v[112:115]\n" \
"global_load_lds_dwordx4 v227, s[48:49] offset:-1024\n" \
"v_accvgpr_read_b32 v235, a1\n" \
"v_mfma_f32_16x16x32_bf16 v[144:147], v[32:35], v[208:211], v[144:147]\n" \
"s_add_u32 s48, s48, 0x80\n" \
"s_addc_u32 s49, s49, 0\n" \
"ds_read_b128 v[200:203], v230 offset:2048\n" \
"v_mfma_f32_16x16x32_bf16 v[144:147], v[36:39], v[212:215], v[144:147]\n" \
"ds_read_b128 v[204:207], v231 offset:2048\n" \
"v_accvgpr_read_b32 v236, a2\n" \
"v_mfma_f32_16x16x32_bf16 v[148:151], v[40:43], v[208:211], v[148:151]\n" \
"ds_read_b64_tr_b16 v[20:21], v232 offset:8704\n" \
"v_accvgpr_read_b32 v237, a3\n" \
"v_mfma_f32_16x16x32_bf16 v[148:151], v[44:47], v[212:215], v[148:151]\n" \
"global_load_lds_dwordx4 v224, s[46:47] offset:0\n" \
"v_mul_f32 v234, s92, v234\n" \
"v_mfma_f32_16x16x32_bf16 v[152:155], v[48:51], v[208:211], v[152:155]\n" \
"ds_read_b64_tr_b16 v[22:23], v232 offset:10752\n" \
"v_mul_f32 v235, s92, v235\n" \
"v_mfma_f32_16x16x32_bf16 v[152:155], v[52:55], v[212:215], v[152:155]\n" \
"ds_read_b64_tr_b16 v[24:25], v233 offset:512\n" \
"v_mul_f32 v236, s92, v236\n" \
"v_mfma_f32_16x16x32_bf16 v[156:159], v[56:59], v[208:211], v[156:159]\n" \
"ds_read_b64_tr_b16 v[26:27], v233 offset:2560\n" \
"v_mul_f32 v237, s92, v237\n" \
"v_mfma_f32_16x16x32_bf16 v[156:159], v[60:63], v[212:215], v[156:159]\n" \
"global_load_lds_dwordx4 v225, s[46:47] offset:1024\n" \
"v_cvt_pk_bf16_f32 v234, v234, v235\n" \
"v_mfma_f32_16x16x32_bf16 v[188:191], v[56:59], v[216:219], v[188:191]\n" \
"ds_read_b128 v[208:211], v230 offset:4096\n" \
"v_cvt_pk_bf16_f32 v236, v236, v237\n" \
"v_mfma_f32_16x16x32_bf16 v[188:191], v[60:63], v[220:223], v[188:191]\n" \
"ds_read_b128 v[212:215], v231 offset:4096\n" \
"v_accvgpr_write_b32 a0, v234\n" \
"v_mfma_f32_16x16x32_bf16 v[184:187], v[48:51], v[216:219], v[184:187]\n" \
"ds_read_b64_tr_b16 v[28:29], v233 offset:8704\n" \
"v_accvgpr_write_b32 a1, v236\n" \
"v_mfma_f32_16x16x32_bf16 v[184:187], v[52:55], v[220:223], v[184:187]\n" \
"global_load_lds_dwordx4 v226, s[46:47] offset:2048\n" \
"v_accvgpr_read_b32 v234, a4\n" \
"v_mfma_f32_16x16x32_bf16 v[180:183], v[40:43], v[216:219], v[180:183]\n" \
"ds_read_b64_tr_b16 v[30:31], v233 offset:10752\n" \
"v_accvgpr_read_b32 v235, a5\n" \
"v_mfma_f32_16x16x32_bf16 v[180:183], v[44:47], v[220:223], v[180:183]\n" \
"v_accvgpr_read_b32 v236, a6\n" \
"v_accvgpr_read_b32 v237, a7\n" \
"v_mfma_f32_16x16x32_bf16 v[176:179], v[32:35], v[216:219], v[176:179]\n" \
"v_mul_f32 v234, s92, v234\n" \
"v_mul_f32 v235, s92, v235\n" \
"v_mfma_f32_16x16x32_bf16 v[176:179], v[36:39], v[220:223], v[176:179]\n" \
"s_waitcnt lgkmcnt(1)\n" \
"global_load_lds_dwordx4 v227, s[46:47] offset:3072\n" \
"v_mul_f32 v236, s92, v236\n" \
"v_mfma_f32_16x16x32_bf16 v[64:67], v[0:3], v[192:195], v[64:67]\n" \
"s_add_u32 s46, s46, 0x80\n" \
"s_addc_u32 s47, s47, 0\n" \
"ds_read_b128 v[216:219], v230 offset:6144\n" \
"v_mfma_f32_16x16x32_bf16 v[64:67], v[4:7], v[196:199], v[64:67]\n" \
"s_add_u32 s90, s88, 139264\n" \
"ds_read_b128 v[220:223], v231 offset:6144\n" \
"v_mul_f32 v237, s92, v237\n" \
"v_mfma_f32_16x16x32_bf16 v[68:71], v[8:11], v[192:195], v[68:71]\n" \
"ds_read_b64_tr_b16 v[32:33], v232 offset:1024\n" \
"v_cvt_pk_bf16_f32 v234, v234, v235\n" \
"v_mfma_f32_16x16x32_bf16 v[68:71], v[12:15], v[196:199], v[68:71]\n" \
"buffer_store_dwordx4 a[128:131], v249, s[56:59], s90 offen offset:0 sc1\n" \
"v_cvt_pk_bf16_f32 v236, v236, v237\n" \
"v_accvgpr_write_b32 a2, v234\n" \
"v_accvgpr_write_b32 a3, v236\n" \
"v_accvgpr_read_b32 v234, a32\n" \
"v_mfma_f32_16x16x32_bf16 v[72:75], v[16:19], v[192:195], v[72:75]\n" \
"ds_read_b64_tr_b16 v[34:35], v232 offset:3072\n" \
"v_accvgpr_read_b32 v235, a33\n" \
"v_mfma_f32_16x16x32_bf16 v[72:75], v[20:23], v[196:199], v[72:75]\n" \
"s_waitcnt lgkmcnt(3)\n" \
"ds_read_b64_tr_b16 v[36:37], v232 offset:9216\n" \
"v_accvgpr_read_b32 v236, a34\n" \
"v_mfma_f32_16x16x32_bf16 v[76:79], v[24:27], v[192:195], v[76:79]\n" \
"ds_read_b64_tr_b16 v[38:39], v232 offset:11264\n" \
"v_accvgpr_read_b32 v237, a35\n" \
"v_mfma_f32_16x16x32_bf16 v[76:79], v[28:31], v[196:199], v[76:79]\n" \
"v_mul_f32 v234, s92, v234\n" \
"v_mul_f32 v235, s92, v235\n" \
"v_mfma_f32_16x16x32_bf16 v[108:111], v[24:27], v[200:203], v[108:111]\n" \
"ds_read_b64_tr_b16 v[40:41], v233 offset:1024\n" \
"v_mul_f32 v236, s92, v236\n" \
"v_mfma_f32_16x16x32_bf16 v[108:111], v[28:31], v[204:207], v[108:111]\n" \
"ds_read_b64_tr_b16 v[42:43], v233 offset:3072\n" \
"v_mul_f32 v237, s92, v237\n" \
"v_mfma_f32_16x16x32_bf16 v[104:107], v[16:19], v[200:203], v[104:107]\n" \
"ds_read_b64_tr_b16 v[44:45], v233 offset:9216\n" \
"v_cvt_pk_bf16_f32 v234, v234, v235\n" \
"v_mfma_f32_16x16x32_bf16 v[104:107], v[20:23], v[204:207], v[104:107]\n" \
"v_cvt_pk_bf16_f32 v236, v236, v237\n" \
"v_accvgpr_write_b32 a32, v234\n" \
"v_mfma_f32_16x16x32_bf16 v[100:103], v[8:11], v[200:203], v[100:103]\n" \
"ds_read_b64_tr_b16 v[46:47], v233 offset:11264\n" \
"v_accvgpr_write_b32 a33, v236\n" \
"v_mfma_f32_16x16x32_bf16 v[100:103], v[12:15], v[204:207], v[100:103]\n" \
"ds_read_b64_tr_b16 v[48:49], v232 offset:1536\n" \
"v_accvgpr_read_b32 v234, a36\n" \
"v_mfma_f32_16x16x32_bf16 v[96:99], v[0:3], v[200:203], v[96:99]\n" \
"s_add_u32 m0, s95, 20480\n" \
"ds_read_b64_tr_b16 v[50:51], v232 offset:3584\n" \
"v_accvgpr_read_b32 v235, a37\n" \
"v_mfma_f32_16x16x32_bf16 v[96:99], v[4:7], v[204:207], v[96:99]\n" \
"global_load_lds_dwordx4 v228, s[44:45] offset:-4096\n" \
"v_accvgpr_read_b32 v236, a38\n" \
"v_mfma_f32_16x16x32_bf16 v[128:131], v[0:3], v[208:211], v[128:131]\n" \
"ds_read_b64_tr_b16 v[52:53], v232 offset:9728\n" \
"v_accvgpr_read_b32 v237, a39\n" \
"v_mfma_f32_16x16x32_bf16 v[128:131], v[4:7], v[212:215], v[128:131]\n" \
"ds_read_b64_tr_b16 v[54:55], v232 offset:11776\n" \
"v_mul_f32 v234, s92, v234\n" \
"v_mfma_f32_16x16x32_bf16 v[132:135], v[8:11], v[208:211], v[132:135]\n" \
"ds_read_b64_tr_b16 v[56:57], v233 offset:1536\n" \
"v_mul_f32 v235, s92, v235\n" \
"v_mfma_f32_16x16x32_bf16 v[132:135], v[12:15], v[212:215], v[132:135]\n" \
"global_load_lds_dwordx4 v229, s[44:45] offset:-3072\n" \
"v_mul_f32 v236, s92, v236\n" \
"v_mfma_f32_16x16x32_bf16 v[136:139], v[16:19], v[208:211], v[136:139]\n" \
"s_add_u32 s44, s44, 0x80000\n" \
"s_addc_u32 s45, s45, 0\n" \
"ds_read_b64_tr_b16 v[58:59], v233 offset:3584\n" \
"v_mfma_f32_16x16x32_bf16 v[136:139], v[20:23], v[212:215], v[136:139]\n" \
"ds_read_b64_tr_b16 v[60:61], v233 offset:9728\n" \
"v_mul_f32 v237, s92, v237\n" \
"v_mfma_f32_16x16x32_bf16 v[140:143], v[24:27], v[208:211], v[140:143]\n" \
"ds_read_b64_tr_b16 v[62:63], v233 offset:11776\n" \
"v_cvt_pk_bf16_f32 v234, v234, v235\n" \
"v_mfma_f32_16x16x32_bf16 v[140:143], v[28:31], v[212:215], v[140:143]\n" \
"s_waitcnt lgkmcnt(3)\n" \
"global_load_lds_dwordx4 v228, s[42:43] offset:-2048\n" \
"v_cvt_pk_bf16_f32 v236, v236, v237\n" \
"v_mfma_f32_16x16x32_bf16 v[172:175], v[24:27], v[216:219], v[172:175]\n" \
"v_add_u32 v230, 0xffff0000, v230\n" \
"v_add_u32 v231, 0xffff0000, v231\n" \
"v_mfma_f32_16x16x32_bf16 v[172:175], v[28:31], v[220:223], v[172:175]\n" \
"v_add_u32 v232, 0x4000, v232\n" \
"v_add_u32 v233, 0x4000, v233\n" \
"v_mfma_f32_16x16x32_bf16 v[168:171], v[16:19], v[216:219], v[168:171]\n" \
"v_accvgpr_write_b32 a34, v234\n" \
"v_accvgpr_write_b32 a35, v236\n" \
"v_mfma_f32_16x16x32_bf16 v[168:171], v[20:23], v[220:223], v[168:171]\n" \
"global_load_lds_dwordx4 v229, s[42:43] offset:-1024\n" \
"v_accvgpr_read_b32 v234, a8\n" \
"v_mfma_f32_16x16x32_bf16 v[164:167], v[8:11], v[216:219], v[164:167]\n" \
"s_add_u32 s42, s42, 0x80000\n" \
"s_addc_u32 s43, s43, 0\n" \
"v_accvgpr_read_b32 v235, a9\n" \
"v_mfma_f32_16x16x32_bf16 v[164:167], v[12:15], v[220:223], v[164:167]\n" \
"v_accvgpr_read_b32 v236, a10\n" \
"v_accvgpr_read_b32 v237, a11\n" \
"v_mfma_f32_16x16x32_bf16 v[160:163], v[0:3], v[216:219], v[160:163]\n" \
"s_waitcnt vmcnt(18) lgkmcnt(0)\n" \
"v_mul_f32 v234, s92, v234\n" \
"v_mul_f32 v235, s92, v235\n" \
"v_mfma_f32_16x16x32_bf16 v[160:163], v[4:7], v[220:223], v[160:163]\n" \
"s_barrier\n" \
"v_mfma_f32_16x16x32_bf16 v[80:83], v[32:35], v[192:195], v[80:83]\n" \
"ds_read_b64_tr_b16 v[0:1], v232 offset:0\n" \
"v_mul_f32 v236, s92, v236\n" \
"v_mfma_f32_16x16x32_bf16 v[80:83], v[36:39], v[196:199], v[80:83]\n" \
"ds_read_b64_tr_b16 v[2:3], v232 offset:2048\n" \
"v_mul_f32 v237, s92, v237\n" \
"v_mfma_f32_16x16x32_bf16 v[84:87], v[40:43], v[192:195], v[84:87]\n" \
"s_add_u32 m0, s87, 135168\n" \
"ds_read_b64_tr_b16 v[4:5], v232 offset:8192\n" \
"v_cvt_pk_bf16_f32 v234, v234, v235\n" \
"v_mfma_f32_16x16x32_bf16 v[84:87], v[44:47], v[196:199], v[84:87]\n" \
"global_load_lds_dwordx4 v224, s[48:49] offset:-4096\n" \
"v_cvt_pk_bf16_f32 v236, v236, v237\n" \
"v_mfma_f32_16x16x32_bf16 v[88:91], v[48:51], v[192:195], v[88:91]\n" \
"ds_read_b64_tr_b16 v[6:7], v232 offset:10240\n" \
"v_accvgpr_write_b32 a8, v234\n" \
"v_mfma_f32_16x16x32_bf16 v[88:91], v[52:55], v[196:199], v[88:91]\n" \
"ds_read_b64_tr_b16 v[8:9], v233 offset:0\n" \
"v_accvgpr_write_b32 a9, v236\n" \
"v_mfma_f32_16x16x32_bf16 v[92:95], v[56:59], v[192:195], v[92:95]\n" \
"ds_read_b64_tr_b16 v[10:11], v233 offset:2048\n" \
"v_accvgpr_read_b32 v234, a12\n" \
"v_mfma_f32_16x16x32_bf16 v[92:95], v[60:63], v[196:199], v[92:95]\n" \
"global_load_lds_dwordx4 v225, s[48:49] offset:-3072\n" \
"v_accvgpr_read_b32 v235, a13\n" \
"v_mfma_f32_16x16x32_bf16 v[124:127], v[56:59], v[200:203], v[124:127]\n" \
"ds_read_b128 v[192:195], v230 offset:0\n" \
"v_accvgpr_read_b32 v236, a14\n" \
"v_mfma_f32_16x16x32_bf16 v[124:127], v[60:63], v[204:207], v[124:127]\n" \
"ds_read_b128 v[196:199], v231 offset:0\n" \
"v_accvgpr_read_b32 v237, a15\n" \
"v_mfma_f32_16x16x32_bf16 v[120:123], v[48:51], v[200:203], v[120:123]\n" \
"ds_read_b64_tr_b16 v[12:13], v233 offset:8192\n" \
"v_mul_f32 v234, s92, v234\n" \
"v_mfma_f32_16x16x32_bf16 v[120:123], v[52:55], v[204:207], v[120:123]\n" \
"global_load_lds_dwordx4 v226, s[48:49] offset:-2048\n" \
"v_mul_f32 v235, s92, v235\n" \
"v_mfma_f32_16x16x32_bf16 v[116:119], v[40:43], v[200:203], v[116:119]\n" \
"ds_read_b64_tr_b16 v[14:15], v233 offset:10240\n" \
"v_mul_f32 v236, s92, v236\n" \
"v_mfma_f32_16x16x32_bf16 v[116:119], v[44:47], v[204:207], v[116:119]\n" \
"ds_read_b64_tr_b16 v[16:17], v232 offset:512\n" \
"v_mul_f32 v237, s92, v237\n" \
"v_mfma_f32_16x16x32_bf16 v[112:115], v[32:35], v[200:203], v[112:115]\n" \
"ds_read_b64_tr_b16 v[18:19], v232 offset:2560\n" \
"v_cvt_pk_bf16_f32 v234, v234, v235\n" \
"v_mfma_f32_16x16x32_bf16 v[112:115], v[36:39], v[204:207], v[112:115]\n" \
"global_load_lds_dwordx4 v227, s[48:49] offset:-1024\n" \
"v_cvt_pk_bf16_f32 v236, v236, v237\n" \
"v_mfma_f32_16x16x32_bf16 v[144:147], v[32:35], v[208:211], v[144:147]\n" \
"s_add_u32 s48, s48, 0x80\n" \
"s_addc_u32 s49, s49, 0\n" \
"ds_read_b128 v[200:203], v230 offset:2048\n" \
"v_mfma_f32_16x16x32_bf16 v[144:147], v[36:39], v[212:215], v[144:147]\n" \
"ds_read_b128 v[204:207], v231 offset:2048\n" \
"v_accvgpr_write_b32 a10, v234\n" \
"v_mfma_f32_16x16x32_bf16 v[148:151], v[40:43], v[208:211], v[148:151]\n" \
"ds_read_b64_tr_b16 v[20:21], v232 offset:8704\n" \
"v_accvgpr_write_b32 a11, v236\n" \
"v_mfma_f32_16x16x32_bf16 v[148:151], v[44:47], v[212:215], v[148:151]\n" \
"global_load_lds_dwordx4 v224, s[46:47] offset:0\n" \
"v_accvgpr_read_b32 v234, a40\n" \
"v_mfma_f32_16x16x32_bf16 v[152:155], v[48:51], v[208:211], v[152:155]\n" \
"ds_read_b64_tr_b16 v[22:23], v232 offset:10752\n" \
"v_accvgpr_read_b32 v235, a41\n" \
"v_mfma_f32_16x16x32_bf16 v[152:155], v[52:55], v[212:215], v[152:155]\n" \
"ds_read_b64_tr_b16 v[24:25], v233 offset:512\n" \
"v_accvgpr_read_b32 v236, a42\n" \
"v_mfma_f32_16x16x32_bf16 v[156:159], v[56:59], v[208:211], v[156:159]\n" \
"ds_read_b64_tr_b16 v[26:27], v233 offset:2560\n" \
"v_accvgpr_read_b32 v237, a43\n" \
"v_mfma_f32_16x16x32_bf16 v[156:159], v[60:63], v[212:215], v[156:159]\n" \
"global_load_lds_dwordx4 v225, s[46:47] offset:1024\n" \
"v_mul_f32 v234, s92, v234\n" \
"v_mfma_f32_16x16x32_bf16 v[188:191], v[56:59], v[216:219], v[188:191]\n" \
"ds_read_b128 v[208:211], v230 offset:4096\n" \
"v_mul_f32 v235, s92, v235\n" \
"v_mfma_f32_16x16x32_bf16 v[188:191], v[60:63], v[220:223], v[188:191]\n" \
"ds_read_b128 v[212:215], v231 offset:4096\n" \
"v_mul_f32 v236, s92, v236\n" \
"v_mfma_f32_16x16x32_bf16 v[184:187], v[48:51], v[216:219], v[184:187]\n" \
"ds_read_b64_tr_b16 v[28:29], v233 offset:8704\n" \
"v_mul_f32 v237, s92, v237\n" \
"v_mfma_f32_16x16x32_bf16 v[184:187], v[52:55], v[220:223], v[184:187]\n" \
"global_load_lds_dwordx4 v226, s[46:47] offset:2048\n" \
"v_cvt_pk_bf16_f32 v234, v234, v235\n" \
"v_mfma_f32_16x16x32_bf16 v[180:183], v[40:43], v[216:219], v[180:183]\n" \
"ds_read_b64_tr_b16 v[30:31], v233 offset:10752\n" \
"v_cvt_pk_bf16_f32 v236, v236, v237\n" \
"v_mfma_f32_16x16x32_bf16 v[180:183], v[44:47], v[220:223], v[180:183]\n" \
"v_accvgpr_write_b32 a40, v234\n" \
"v_accvgpr_write_b32 a41, v236\n" \
"v_mfma_f32_16x16x32_bf16 v[176:179], v[32:35], v[216:219], v[176:179]\n" \
"v_accvgpr_read_b32 v234, a44\n" \
"v_accvgpr_read_b32 v235, a45\n" \
"v_mfma_f32_16x16x32_bf16 v[176:179], v[36:39], v[220:223], v[176:179]\n" \
"s_waitcnt lgkmcnt(1)\n" \
"global_load_lds_dwordx4 v227, s[46:47] offset:3072\n" \
"v_accvgpr_read_b32 v236, a46\n" \
"v_mfma_f32_16x16x32_bf16 v[64:67], v[0:3], v[192:195], v[64:67]\n" \
"s_add_u32 s46, s46, 0x80\n" \
"s_addc_u32 s47, s47, 0\n" \
"ds_read_b128 v[216:219], v230 offset:6144\n" \
"v_mfma_f32_16x16x32_bf16 v[64:67], v[4:7], v[196:199], v[64:67]\n" \
"ds_read_b128 v[220:223], v231 offset:6144\n" \
"v_accvgpr_read_b32 v237, a47\n" \
"v_mfma_f32_16x16x32_bf16 v[68:71], v[8:11], v[192:195], v[68:71]\n" \
"ds_read_b64_tr_b16 v[32:33], v232 offset:1024\n" \
"v_mul_f32 v234, s92, v234\n" \
"v_mfma_f32_16x16x32_bf16 v[68:71], v[12:15], v[196:199], v[68:71]\n" \
"buffer_store_dwordx4 a[160:163], v249, s[56:59], s90 offen offset:256 sc1\n" \
"v_mul_f32 v235, s92, v235\n" \
"v_mul_f32 v236, s92, v236\n" \
"v_mul_f32 v237, s92, v237\n" \
"v_cvt_pk_bf16_f32 v234, v234, v235\n" \
"v_mfma_f32_16x16x32_bf16 v[72:75], v[16:19], v[192:195], v[72:75]\n" \
"ds_read_b64_tr_b16 v[34:35], v232 offset:3072\n" \
"v_cvt_pk_bf16_f32 v236, v236, v237\n" \
"v_mfma_f32_16x16x32_bf16 v[72:75], v[20:23], v[196:199], v[72:75]\n" \
"s_waitcnt lgkmcnt(3)\n" \
"ds_read_b64_tr_b16 v[36:37], v232 offset:9216\n" \
"v_accvgpr_write_b32 a42, v234\n" \
"v_mfma_f32_16x16x32_bf16 v[76:79], v[24:27], v[192:195], v[76:79]\n" \
"ds_read_b64_tr_b16 v[38:39], v232 offset:11264\n" \
"v_accvgpr_write_b32 a43, v236\n" \
"v_mfma_f32_16x16x32_bf16 v[76:79], v[28:31], v[196:199], v[76:79]\n" \
"v_accvgpr_read_b32 v234, a16\n" \
"v_accvgpr_read_b32 v235, a17\n" \
"v_mfma_f32_16x16x32_bf16 v[108:111], v[24:27], v[200:203], v[108:111]\n" \
"ds_read_b64_tr_b16 v[40:41], v233 offset:1024\n" \
"v_accvgpr_read_b32 v236, a18\n" \
"v_mfma_f32_16x16x32_bf16 v[108:111], v[28:31], v[204:207], v[108:111]\n" \
"ds_read_b64_tr_b16 v[42:43], v233 offset:3072\n" \
"v_accvgpr_read_b32 v237, a19\n" \
"v_mfma_f32_16x16x32_bf16 v[104:107], v[16:19], v[200:203], v[104:107]\n" \
"ds_read_b64_tr_b16 v[44:45], v233 offset:9216\n" \
"v_cvt_pk_bf16_f32 v234, v234, v235\n" \
"v_mfma_f32_16x16x32_bf16 v[104:107], v[20:23], v[204:207], v[104:107]\n" \
"v_cvt_pk_bf16_f32 v236, v236, v237\n" \
"v_accvgpr_write_b32 a16, v234\n" \
"v_mfma_f32_16x16x32_bf16 v[100:103], v[8:11], v[200:203], v[100:103]\n" \
"ds_read_b64_tr_b16 v[46:47], v233 offset:11264\n" \
"v_accvgpr_write_b32 a17, v236\n" \
"v_mfma_f32_16x16x32_bf16 v[100:103], v[12:15], v[204:207], v[100:103]\n" \
"ds_read_b64_tr_b16 v[48:49], v232 offset:1536\n" \
"v_accvgpr_read_b32 v234, a48\n" \
"v_mfma_f32_16x16x32_bf16 v[96:99], v[0:3], v[200:203], v[96:99]\n" \
"s_add_u32 m0, s95, 36864\n" \
"ds_read_b64_tr_b16 v[50:51], v232 offset:3584\n" \
"v_accvgpr_read_b32 v235, a49\n" \
"v_mfma_f32_16x16x32_bf16 v[96:99], v[4:7], v[204:207], v[96:99]\n" \
"global_load_lds_dwordx4 v228, s[44:45] offset:-4096\n" \
"v_accvgpr_read_b32 v236, a50\n" \
"v_mfma_f32_16x16x32_bf16 v[128:131], v[0:3], v[208:211], v[128:131]\n" \
"ds_read_b64_tr_b16 v[52:53], v232 offset:9728\n" \
"v_accvgpr_read_b32 v237, a51\n" \
"v_mfma_f32_16x16x32_bf16 v[128:131], v[4:7], v[212:215], v[128:131]\n" \
"ds_read_b64_tr_b16 v[54:55], v232 offset:11776\n" \
"v_cvt_pk_bf16_f32 v234, v234, v235\n" \
"v_mfma_f32_16x16x32_bf16 v[132:135], v[8:11], v[208:211], v[132:135]\n" \
"ds_read_b64_tr_b16 v[56:57], v233 offset:1536\n" \
"v_cvt_pk_bf16_f32 v236, v236, v237\n" \
"v_mfma_f32_16x16x32_bf16 v[132:135], v[12:15], v[212:215], v[132:135]\n" \
"global_load_lds_dwordx4 v229, s[44:45] offset:-3072\n" \
"v_accvgpr_write_b32 a18, v234\n" \
"v_mfma_f32_16x16x32_bf16 v[136:139], v[16:19], v[208:211], v[136:139]\n" \
"s_add_u32 s44, s44, 0x80000\n" \
"s_addc_u32 s45, s45, 0\n" \
"ds_read_b64_tr_b16 v[58:59], v233 offset:3584\n" \
"v_mfma_f32_16x16x32_bf16 v[136:139], v[20:23], v[212:215], v[136:139]\n" \
"ds_read_b64_tr_b16 v[60:61], v233 offset:9728\n" \
"v_accvgpr_write_b32 a19, v236\n" \
"v_mfma_f32_16x16x32_bf16 v[140:143], v[24:27], v[208:211], v[140:143]\n" \
"ds_read_b64_tr_b16 v[62:63], v233 offset:11776\n" \
"v_accvgpr_read_b32 v234, a20\n" \
"v_mfma_f32_16x16x32_bf16 v[140:143], v[28:31], v[212:215], v[140:143]\n" \
"s_waitcnt lgkmcnt(3)\n" \
"global_load_lds_dwordx4 v228, s[42:43] offset:-2048\n" \
"v_accvgpr_read_b32 v235, a21\n" \
"v_mfma_f32_16x16x32_bf16 v[172:175], v[24:27], v[216:219], v[172:175]\n" \
"v_add_u32 v230, 0x8000, v230\n" \
"v_add_u32 v231, 0x8000, v231\n" \
"v_mfma_f32_16x16x32_bf16 v[172:175], v[28:31], v[220:223], v[172:175]\n" \
"v_add_u32 v232, 0xffff4000, v232\n" \
"v_add_u32 v233, 0xffff4000, v233\n" \
"v_mfma_f32_16x16x32_bf16 v[168:171], v[16:19], v[216:219], v[168:171]\n" \
"v_accvgpr_read_b32 v236, a22\n" \
"v_accvgpr_read_b32 v237, a23\n" \
"v_mfma_f32_16x16x32_bf16 v[168:171], v[20:23], v[220:223], v[168:171]\n" \
"global_load_lds_dwordx4 v229, s[42:43] offset:-1024\n" \
"v_cvt_pk_bf16_f32 v234, v234, v235\n" \
"v_mfma_f32_16x16x32_bf16 v[164:167], v[8:11], v[216:219], v[164:167]\n" \
"s_add_u32 s42, s42, 0x80000\n" \
"s_addc_u32 s43, s43, 0\n" \
"v_cvt_pk_bf16_f32 v236, v236, v237\n" \
"v_mfma_f32_16x16x32_bf16 v[164:167], v[12:15], v[220:223], v[164:167]\n" \
"v_accvgpr_write_b32 a20, v234\n" \
"v_accvgpr_write_b32 a21, v236\n" \
"v_mfma_f32_16x16x32_bf16 v[160:163], v[0:3], v[216:219], v[160:163]\n" \
"s_waitcnt vmcnt(18) lgkmcnt(0)\n" \
"v_accvgpr_read_b32 v234, a52\n" \
"v_accvgpr_read_b32 v235, a53\n" \
"v_mfma_f32_16x16x32_bf16 v[160:163], v[4:7], v[220:223], v[160:163]\n" \
"s_barrier\n" \
"v_mfma_f32_16x16x32_bf16 v[80:83], v[32:35], v[192:195], v[80:83]\n" \
"ds_read_b64_tr_b16 v[0:1], v232 offset:0\n" \
"v_accvgpr_read_b32 v236, a54\n" \
"v_mfma_f32_16x16x32_bf16 v[80:83], v[36:39], v[196:199], v[80:83]\n" \
"ds_read_b64_tr_b16 v[2:3], v232 offset:2048\n" \
"v_accvgpr_read_b32 v237, a55\n" \
"v_mfma_f32_16x16x32_bf16 v[84:87], v[40:43], v[192:195], v[84:87]\n" \
"s_add_u32 m0, s87, 69632\n" \
"ds_read_b64_tr_b16 v[4:5], v232 offset:8192\n" \
"v_cvt_pk_bf16_f32 v234, v234, v235\n" \
"v_mfma_f32_16x16x32_bf16 v[84:87], v[44:47], v[196:199], v[84:87]\n" \
"global_load_lds_dwordx4 v224, s[48:49] offset:-4096\n" \
"v_cvt_pk_bf16_f32 v236, v236, v237\n" \
"v_mfma_f32_16x16x32_bf16 v[88:91], v[48:51], v[192:195], v[88:91]\n" \
"ds_read_b64_tr_b16 v[6:7], v232 offset:10240\n" \
"v_accvgpr_write_b32 a22, v234\n" \
"v_mfma_f32_16x16x32_bf16 v[88:91], v[52:55], v[196:199], v[88:91]\n" \
"ds_read_b64_tr_b16 v[8:9], v233 offset:0\n" \
"v_accvgpr_write_b32 a23, v236\n" \
"v_mfma_f32_16x16x32_bf16 v[92:95], v[56:59], v[192:195], v[92:95]\n" \
"ds_read_b64_tr_b16 v[10:11], v233 offset:2048\n" \
"v_accvgpr_read_b32 v234, a24\n" \
"v_mfma_f32_16x16x32_bf16 v[92:95], v[60:63], v[196:199], v[92:95]\n" \
"global_load_lds_dwordx4 v225, s[48:49] offset:-3072\n" \
"v_accvgpr_read_b32 v235, a25\n" \
"v_mfma_f32_16x16x32_bf16 v[124:127], v[56:59], v[200:203], v[124:127]\n" \
"ds_read_b128 v[192:195], v230 offset:0\n" \
"v_accvgpr_read_b32 v236, a26\n" \
"v_mfma_f32_16x16x32_bf16 v[124:127], v[60:63], v[204:207], v[124:127]\n" \
"ds_read_b128 v[196:199], v231 offset:0\n" \
"v_accvgpr_read_b32 v237, a27\n" \
"v_mfma_f32_16x16x32_bf16 v[120:123], v[48:51], v[200:203], v[120:123]\n" \
"ds_read_b64_tr_b16 v[12:13], v233 offset:8192\n" \
"v_cvt_pk_bf16_f32 v234, v234, v235\n" \
"v_mfma_f32_16x16x32_bf16 v[120:123], v[52:55], v[204:207], v[120:123]\n" \
"global_load_lds_dwordx4 v226, s[48:49] offset:-2048\n" \
"v_cvt_pk_bf16_f32 v236, v236, v237\n" \
"v_mfma_f32_16x16x32_bf16 v[116:119], v[40:43], v[200:203], v[116:119]\n" \
"ds_read_b64_tr_b16 v[14:15], v233 offset:10240\n" \
"v_accvgpr_write_b32 a24, v234\n" \
"v_mfma_f32_16x16x32_bf16 v[116:119], v[44:47], v[204:207], v[116:119]\n" \
"ds_read_b64_tr_b16 v[16:17], v232 offset:512\n" \
"v_accvgpr_write_b32 a25, v236\n" \
"v_mfma_f32_16x16x32_bf16 v[112:115], v[32:35], v[200:203], v[112:115]\n" \
"ds_read_b64_tr_b16 v[18:19], v232 offset:2560\n" \
"v_accvgpr_read_b32 v234, a56\n" \
"v_mfma_f32_16x16x32_bf16 v[112:115], v[36:39], v[204:207], v[112:115]\n" \
"global_load_lds_dwordx4 v227, s[48:49] offset:-1024\n" \
"v_accvgpr_read_b32 v235, a57\n" \
"v_mfma_f32_16x16x32_bf16 v[144:147], v[32:35], v[208:211], v[144:147]\n" \
"s_add_u32 s48, s48, 0x80\n" \
"s_addc_u32 s49, s49, 0\n" \
"ds_read_b128 v[200:203], v230 offset:2048\n" \
"v_mfma_f32_16x16x32_bf16 v[144:147], v[36:39], v[212:215], v[144:147]\n" \
"ds_read_b128 v[204:207], v231 offset:2048\n" \
"v_accvgpr_read_b32 v236, a58\n" \
"v_mfma_f32_16x16x32_bf16 v[148:151], v[40:43], v[208:211], v[148:151]\n" \
"ds_read_b64_tr_b16 v[20:21], v232 offset:8704\n" \
"v_accvgpr_read_b32 v237, a59\n" \
"v_mfma_f32_16x16x32_bf16 v[148:151], v[44:47], v[212:215], v[148:151]\n" \
"global_load_lds_dwordx4 v224, s[46:47] offset:0\n" \
"v_cvt_pk_bf16_f32 v234, v234, v235\n" \
"v_mfma_f32_16x16x32_bf16 v[152:155], v[48:51], v[208:211], v[152:155]\n" \
"ds_read_b64_tr_b16 v[22:23], v232 offset:10752\n" \
"v_cvt_pk_bf16_f32 v236, v236, v237\n" \
"v_mfma_f32_16x16x32_bf16 v[152:155], v[52:55], v[212:215], v[152:155]\n" \
"ds_read_b64_tr_b16 v[24:25], v233 offset:512\n" \
"v_accvgpr_write_b32 a26, v234\n" \
"v_mfma_f32_16x16x32_bf16 v[156:159], v[56:59], v[208:211], v[156:159]\n" \
"ds_read_b64_tr_b16 v[26:27], v233 offset:2560\n" \
"v_accvgpr_write_b32 a27, v236\n" \
"v_mfma_f32_16x16x32_bf16 v[156:159], v[60:63], v[212:215], v[156:159]\n" \
"global_load_lds_dwordx4 v225, s[46:47] offset:1024\n" \
"v_accvgpr_read_b32 v234, a28\n" \
"v_mfma_f32_16x16x32_bf16 v[188:191], v[56:59], v[216:219], v[188:191]\n" \
"ds_read_b128 v[208:211], v230 offset:4096\n" \
"v_accvgpr_read_b32 v235, a29\n" \
"v_mfma_f32_16x16x32_bf16 v[188:191], v[60:63], v[220:223], v[188:191]\n" \
"ds_read_b128 v[212:215], v231 offset:4096\n" \
"v_accvgpr_read_b32 v236, a30\n" \
"v_mfma_f32_16x16x32_bf16 v[184:187], v[48:51], v[216:219], v[184:187]\n" \
"ds_read_b64_tr_b16 v[28:29], v233 offset:8704\n" \
"v_accvgpr_read_b32 v237, a31\n" \
"v_mfma_f32_16x16x32_bf16 v[184:187], v[52:55], v[220:223], v[184:187]\n" \
"global_load_lds_dwordx4 v226, s[46:47] offset:2048\n" \
"v_cvt_pk_bf16_f32 v234, v234, v235\n" \
"v_mfma_f32_16x16x32_bf16 v[180:183], v[40:43], v[216:219], v[180:183]\n" \
"ds_read_b64_tr_b16 v[30:31], v233 offset:10752\n" \
"v_cvt_pk_bf16_f32 v236, v236, v237\n" \
"v_mfma_f32_16x16x32_bf16 v[180:183], v[44:47], v[220:223], v[180:183]\n" \
"v_accvgpr_write_b32 a28, v234\n" \
"v_accvgpr_write_b32 a29, v236\n" \
"v_mfma_f32_16x16x32_bf16 v[176:179], v[32:35], v[216:219], v[176:179]\n" \
"v_accvgpr_read_b32 v234, a60\n" \
"v_accvgpr_read_b32 v235, a61\n" \
"v_mfma_f32_16x16x32_bf16 v[176:179], v[36:39], v[220:223], v[176:179]\n" \
"s_waitcnt lgkmcnt(1)\n" \
"global_load_lds_dwordx4 v227, s[46:47] offset:3072\n" \
"v_accvgpr_read_b32 v236, a62\n" \
"v_mfma_f32_16x16x32_bf16 v[64:67], v[0:3], v[192:195], v[64:67]\n" \
"s_add_u32 s46, s46, 0x80\n" \
"s_addc_u32 s47, s47, 0\n" \
"ds_read_b128 v[216:219], v230 offset:6144\n" \
"v_mfma_f32_16x16x32_bf16 v[64:67], v[4:7], v[196:199], v[64:67]\n" \
"ds_read_b128 v[220:223], v231 offset:6144\n" \
"v_accvgpr_read_b32 v237, a63\n" \
"v_mfma_f32_16x16x32_bf16 v[68:71], v[8:11], v[192:195], v[68:71]\n" \
"ds_read_b64_tr_b16 v[32:33], v232 offset:1024\n" \
"v_cvt_pk_bf16_f32 v234, v234, v235\n" \
"v_mfma_f32_16x16x32_bf16 v[68:71], v[12:15], v[196:199], v[68:71]\n" \
"buffer_store_dwordx4 a[136:139], v249, s[56:59], s90 offen offset:2048 sc1\n" \
"v_cvt_pk_bf16_f32 v236, v236, v237\n" \
"v_accvgpr_write_b32 a30, v234\n" \
"v_accvgpr_write_b32 a31, v236\n" \
"v_mfma_f32_16x16x32_bf16 v[72:75], v[16:19], v[192:195], v[72:75]\n" \
"ds_read_b64_tr_b16 v[34:35], v232 offset:3072\n" \
"v_mfma_f32_16x16x32_bf16 v[72:75], v[20:23], v[196:199], v[72:75]\n" \
"s_waitcnt lgkmcnt(3)\n" \
"ds_read_b64_tr_b16 v[36:37], v232 offset:9216\n" \
"v_mfma_f32_16x16x32_bf16 v[76:79], v[24:27], v[192:195], v[76:79]\n" \
"ds_read_b64_tr_b16 v[38:39], v232 offset:11264\n" \
"v_mfma_f32_16x16x32_bf16 v[76:79], v[28:31], v[196:199], v[76:79]\n" \
"buffer_store_dwordx4 a[168:171], v249, s[56:59], s90 offen offset:2304 sc1\n" \
"v_mfma_f32_16x16x32_bf16 v[108:111], v[24:27], v[200:203], v[108:111]\n" \
"s_lshl_b32 s70, s63, 2\n" \
"ds_read_b64_tr_b16 v[40:41], v233 offset:1024\n" \
"v_mfma_f32_16x16x32_bf16 v[108:111], v[28:31], v[204:207], v[108:111]\n" \
"s_add_u32 s70, s70, s91\n" \
"ds_read_b64_tr_b16 v[42:43], v233 offset:3072\n" \
"v_mov_b32 v238, s60\n" \
"v_mfma_f32_16x16x32_bf16 v[104:107], v[16:19], v[200:203], v[104:107]\n" \
"ds_read_b64_tr_b16 v[44:45], v233 offset:9216\n" \
"v_lshlrev_b32 v239, 28, s61\n" \
"v_mfma_f32_16x16x32_bf16 v[104:107], v[20:23], v[204:207], v[104:107]\n" \
"buffer_store_dword v238, v239, s[56:59], s70 offen sc1\n" \
"v_mfma_f32_16x16x32_bf16 v[100:103], v[8:11], v[200:203], v[100:103]\n" \
"ds_read_b64_tr_b16 v[46:47], v233 offset:11264\n" \
"v_mfma_f32_16x16x32_bf16 v[100:103], v[12:15], v[204:207], v[100:103]\n" \
"ds_read_b64_tr_b16 v[48:49], v232 offset:1536\n" \
"v_mfma_f32_16x16x32_bf16 v[96:99], v[0:3], v[200:203], v[96:99]\n" \
"s_add_u32 m0, s95, 53248\n" \
"ds_read_b64_tr_b16 v[50:51], v232 offset:3584\n" \
"v_mfma_f32_16x16x32_bf16 v[96:99], v[4:7], v[204:207], v[96:99]\n" \
"global_load_lds_dwordx4 v228, s[44:45] offset:-4096\n" \
"v_mfma_f32_16x16x32_bf16 v[128:131], v[0:3], v[208:211], v[128:131]\n" \
"ds_read_b64_tr_b16 v[52:53], v232 offset:9728\n" \
"Lpg3850_%=:\n" \
"s_cbranch_execz Lpg5534_%=\n" \
"v_mfma_f32_16x16x32_bf16 v[128:131], v[4:7], v[212:215], v[128:131]\n" \
"ds_read_b64_tr_b16 v[54:55], v232 offset:11776\n" \
"v_mfma_f32_16x16x32_bf16 v[132:135], v[8:11], v[208:211], v[132:135]\n" \
"ds_read_b64_tr_b16 v[56:57], v233 offset:1536\n" \
"v_mfma_f32_16x16x32_bf16 v[132:135], v[12:15], v[212:215], v[132:135]\n" \
"global_load_lds_dwordx4 v229, s[44:45] offset:-3072\n" \
"v_mfma_f32_16x16x32_bf16 v[136:139], v[16:19], v[208:211], v[136:139]\n" \
"s_add_u32 s44, s44, 0x100000\n" \
"s_addc_u32 s45, s45, 0\n" \
"ds_read_b64_tr_b16 v[58:59], v233 offset:3584\n" \
"v_mfma_f32_16x16x32_bf16 v[136:139], v[20:23], v[212:215], v[136:139]\n" \
"ds_read_b64_tr_b16 v[60:61], v233 offset:9728\n" \
"v_mfma_f32_16x16x32_bf16 v[140:143], v[24:27], v[208:211], v[140:143]\n" \
"ds_read_b64_tr_b16 v[62:63], v233 offset:11776\n" \
"v_mfma_f32_16x16x32_bf16 v[140:143], v[28:31], v[212:215], v[140:143]\n" \
"s_waitcnt lgkmcnt(3)\n" \
"global_load_lds_dwordx4 v228, s[42:43] offset:-2048\n" \
"v_mfma_f32_16x16x32_bf16 v[172:175], v[24:27], v[216:219], v[172:175]\n" \
"v_add_u32 v230, 0x8000, v230\n" \
"v_add_u32 v231, 0x8000, v231\n" \
"v_mfma_f32_16x16x32_bf16 v[172:175], v[28:31], v[220:223], v[172:175]\n" \
"v_add_u32 v232, 0x4000, v232\n" \
"v_add_u32 v233, 0x4000, v233\n" \
"v_mfma_f32_16x16x32_bf16 v[168:171], v[16:19], v[216:219], v[168:171]\n" \
"v_mfma_f32_16x16x32_bf16 v[168:171], v[20:23], v[220:223], v[168:171]\n" \
"global_load_lds_dwordx4 v229, s[42:43] offset:-1024\n" \
"v_mfma_f32_16x16x32_bf16 v[164:167], v[8:11], v[216:219], v[164:167]\n" \
"s_add_u32 s42, s42, 0x100000\n" \
"s_addc_u32 s43, s43, 0\n" \
"v_mfma_f32_16x16x32_bf16 v[164:167], v[12:15], v[220:223], v[164:167]\n" \
"v_mfma_f32_16x16x32_bf16 v[160:163], v[0:3], v[216:219], v[160:163]\n" \
"s_waitcnt vmcnt(20) lgkmcnt(0)\n" \
"v_mfma_f32_16x16x32_bf16 v[160:163], v[4:7], v[220:223], v[160:163]\n" \
"s_barrier\n" \
"v_mfma_f32_16x16x32_bf16 v[80:83], v[32:35], v[192:195], v[80:83]\n" \
"ds_read_b64_tr_b16 v[0:1], v232 offset:0\n" \
"v_mfma_f32_16x16x32_bf16 v[80:83], v[36:39], v[196:199], v[80:83]\n" \
"ds_read_b64_tr_b16 v[2:3], v232 offset:2048\n" \
"v_mfma_f32_16x16x32_bf16 v[84:87], v[40:43], v[192:195], v[84:87]\n" \
"s_add_u32 m0, s87, 102400\n" \
"ds_read_b64_tr_b16 v[4:5], v232 offset:8192\n" \
"v_mfma_f32_16x16x32_bf16 v[84:87], v[44:47], v[196:199], v[84:87]\n" \
"global_load_lds_dwordx4 v224, s[48:49] offset:-4096\n" \
"v_mfma_f32_16x16x32_bf16 v[88:91], v[48:51], v[192:195], v[88:91]\n" \
"ds_read_b64_tr_b16 v[6:7], v232 offset:10240\n" \
"v_mfma_f32_16x16x32_bf16 v[88:91], v[52:55], v[196:199], v[88:91]\n" \
"ds_read_b64_tr_b16 v[8:9], v233 offset:0\n" \
"v_mfma_f32_16x16x32_bf16 v[92:95], v[56:59], v[192:195], v[92:95]\n" \
"ds_read_b64_tr_b16 v[10:11], v233 offset:2048\n" \
"v_mfma_f32_16x16x32_bf16 v[92:95], v[60:63], v[196:199], v[92:95]\n" \
"global_load_lds_dwordx4 v225, s[48:49] offset:-3072\n" \
"v_mfma_f32_16x16x32_bf16 v[124:127], v[56:59], v[200:203], v[124:127]\n" \
"ds_read_b128 v[192:195], v230 offset:0\n" \
"v_mfma_f32_16x16x32_bf16 v[124:127], v[60:63], v[204:207], v[124:127]\n" \
"ds_read_b128 v[196:199], v231 offset:0\n" \
"v_mfma_f32_16x16x32_bf16 v[120:123], v[48:51], v[200:203], v[120:123]\n" \
"ds_read_b64_tr_b16 v[12:13], v233 offset:8192\n" \
"v_mfma_f32_16x16x32_bf16 v[120:123], v[52:55], v[204:207], v[120:123]\n" \
"global_load_lds_dwordx4 v226, s[48:49] offset:-2048\n" \
"v_mfma_f32_16x16x32_bf16 v[116:119], v[40:43], v[200:203], v[116:119]\n" \
"ds_read_b64_tr_b16 v[14:15], v233 offset:10240\n" \
"v_mfma_f32_16x16x32_bf16 v[116:119], v[44:47], v[204:207], v[116:119]\n" \
"ds_read_b64_tr_b16 v[16:17], v232 offset:512\n" \
"v_mfma_f32_16x16x32_bf16 v[112:115], v[32:35], v[200:203], v[112:115]\n" \
"ds_read_b64_tr_b16 v[18:19], v232 offset:2560\n" \
"v_mfma_f32_16x16x32_bf16 v[112:115], v[36:39], v[204:207], v[112:115]\n" \
"global_load_lds_dwordx4 v227, s[48:49] offset:-1024\n" \
"v_mfma_f32_16x16x32_bf16 v[144:147], v[32:35], v[208:211], v[144:147]\n" \
"s_add_u32 s48, s48, 0x100\n" \
"s_addc_u32 s49, s49, 0\n" \
"ds_read_b128 v[200:203], v230 offset:2048\n" \
"v_mfma_f32_16x16x32_bf16 v[144:147], v[36:39], v[212:215], v[144:147]\n" \
"ds_read_b128 v[204:207], v231 offset:2048\n" \
"v_mfma_f32_16x16x32_bf16 v[148:151], v[40:43], v[208:211], v[148:151]\n" \
"ds_read_b64_tr_b16 v[20:21], v232 offset:8704\n" \
"v_mfma_f32_16x16x32_bf16 v[148:151], v[44:47], v[212:215], v[148:151]\n" \
"global_load_lds_dwordx4 v224, s[46:47] offset:0\n" \
"v_mfma_f32_16x16x32_bf16 v[152:155], v[48:51], v[208:211], v[152:155]\n" \
"ds_read_b64_tr_b16 v[22:23], v232 offset:10752\n" \
"v_mfma_f32_16x16x32_bf16 v[152:155], v[52:55], v[212:215], v[152:155]\n" \
"ds_read_b64_tr_b16 v[24:25], v233 offset:512\n" \
"v_mfma_f32_16x16x32_bf16 v[156:159], v[56:59], v[208:211], v[156:159]\n" \
"ds_read_b64_tr_b16 v[26:27], v233 offset:2560\n" \
"v_mfma_f32_16x16x32_bf16 v[156:159], v[60:63], v[212:215], v[156:159]\n" \
"global_load_lds_dwordx4 v225, s[46:47] offset:1024\n" \
"v_mfma_f32_16x16x32_bf16 v[188:191], v[56:59], v[216:219], v[188:191]\n" \
"ds_read_b128 v[208:211], v230 offset:4096\n" \
"v_mfma_f32_16x16x32_bf16 v[188:191], v[60:63], v[220:223], v[188:191]\n" \
"ds_read_b128 v[212:215], v231 offset:4096\n" \
"v_mfma_f32_16x16x32_bf16 v[184:187], v[48:51], v[216:219], v[184:187]\n" \
"ds_read_b64_tr_b16 v[28:29], v233 offset:8704\n" \
"v_mfma_f32_16x16x32_bf16 v[184:187], v[52:55], v[220:223], v[184:187]\n" \
"global_load_lds_dwordx4 v226, s[46:47] offset:2048\n" \
"v_mfma_f32_16x16x32_bf16 v[180:183], v[40:43], v[216:219], v[180:183]\n" \
"ds_read_b64_tr_b16 v[30:31], v233 offset:10752\n" \
"v_mfma_f32_16x16x32_bf16 v[180:183], v[44:47], v[220:223], v[180:183]\n" \
"v_mfma_f32_16x16x32_bf16 v[176:179], v[32:35], v[216:219], v[176:179]\n" \
"v_mfma_f32_16x16x32_bf16 v[176:179], v[36:39], v[220:223], v[176:179]\n" \
"s_waitcnt lgkmcnt(1)\n" \
"global_load_lds_dwordx4 v227, s[46:47] offset:3072\n" \
"v_mfma_f32_16x16x32_bf16 v[64:67], v[0:3], v[192:195], v[64:67]\n" \
"s_add_u32 s46, s46, 0x100\n" \
"s_addc_u32 s47, s47, 0\n" \
"ds_read_b128 v[216:219], v230 offset:6144\n" \
"v_mfma_f32_16x16x32_bf16 v[64:67], v[4:7], v[196:199], v[64:67]\n" \
"ds_read_b128 v[220:223], v231 offset:6144\n" \
"s_add_u32 s90, s89, 139264\n" \
"v_mfma_f32_16x16x32_bf16 v[68:71], v[8:11], v[192:195], v[68:71]\n" \
"ds_read_b64_tr_b16 v[32:33], v232 offset:1024\n" \
"v_mfma_f32_16x16x32_bf16 v[68:71], v[12:15], v[196:199], v[68:71]\n" \
"buffer_store_dwordx4 a[144:147], v248, s[56:59], s90 offen offset:0 sc1\n" \
"v_mfma_f32_16x16x32_bf16 v[72:75], v[16:19], v[192:195], v[72:75]\n" \
"ds_read_b64_tr_b16 v[34:35], v232 offset:3072\n" \
"v_mfma_f32_16x16x32_bf16 v[72:75], v[20:23], v[196:199], v[72:75]\n" \
"s_waitcnt lgkmcnt(3)\n" \
"ds_read_b64_tr_b16 v[36:37], v232 offset:9216\n" \
"v_mfma_f32_16x16x32_bf16 v[76:79], v[24:27], v[192:195], v[76:79]\n" \
"ds_read_b64_tr_b16 v[38:39], v232 offset:11264\n" \
"v_mfma_f32_16x16x32_bf16 v[76:79], v[28:31], v[196:199], v[76:79]\n" \
"v_mfma_f32_16x16x32_bf16 v[108:111], v[24:27], v[200:203], v[108:111]\n" \
"ds_read_b64_tr_b16 v[40:41], v233 offset:1024\n" \
"v_mfma_f32_16x16x32_bf16 v[108:111], v[28:31], v[204:207], v[108:111]\n" \
"ds_read_b64_tr_b16 v[42:43], v233 offset:3072\n" \
"v_mfma_f32_16x16x32_bf16 v[104:107], v[16:19], v[200:203], v[104:107]\n" \
"ds_read_b64_tr_b16 v[44:45], v233 offset:9216\n" \
"v_mfma_f32_16x16x32_bf16 v[104:107], v[20:23], v[204:207], v[104:107]\n" \
"v_mfma_f32_16x16x32_bf16 v[100:103], v[8:11], v[200:203], v[100:103]\n" \
"ds_read_b64_tr_b16 v[46:47], v233 offset:11264\n" \
"v_mfma_f32_16x16x32_bf16 v[100:103], v[12:15], v[204:207], v[100:103]\n" \
"ds_read_b64_tr_b16 v[48:49], v232 offset:1536\n" \
"v_mfma_f32_16x16x32_bf16 v[96:99], v[0:3], v[200:203], v[96:99]\n" \
"s_add_u32 m0, s95, 4096\n" \
"ds_read_b64_tr_b16 v[50:51], v232 offset:3584\n" \
"v_mfma_f32_16x16x32_bf16 v[96:99], v[4:7], v[204:207], v[96:99]\n" \
"global_load_lds_dwordx4 v228, s[44:45] offset:-4096\n" \
"v_mfma_f32_16x16x32_bf16 v[128:131], v[0:3], v[208:211], v[128:131]\n" \
"ds_read_b64_tr_b16 v[52:53], v232 offset:9728\n" \
"v_mfma_f32_16x16x32_bf16 v[128:131], v[4:7], v[212:215], v[128:131]\n" \
"ds_read_b64_tr_b16 v[54:55], v232 offset:11776\n" \
"v_mfma_f32_16x16x32_bf16 v[132:135], v[8:11], v[208:211], v[132:135]\n" \
"ds_read_b64_tr_b16 v[56:57], v233 offset:1536\n" \
"v_mfma_f32_16x16x32_bf16 v[132:135], v[12:15], v[212:215], v[132:135]\n" \
"global_load_lds_dwordx4 v229, s[44:45] offset:-3072\n" \
"v_mfma_f32_16x16x32_bf16 v[136:139], v[16:19], v[208:211], v[136:139]\n" \
"s_add_u32 s44, s44, 0x80000\n" \
"s_addc_u32 s45, s45, 0\n" \
"ds_read_b64_tr_b16 v[58:59], v233 offset:3584\n" \
"v_mfma_f32_16x16x32_bf16 v[136:139], v[20:23], v[212:215], v[136:139]\n" \
"ds_read_b64_tr_b16 v[60:61], v233 offset:9728\n" \
"v_mfma_f32_16x16x32_bf16 v[140:143], v[24:27], v[208:211], v[140:143]\n" \
"ds_read_b64_tr_b16 v[62:63], v233 offset:11776\n" \
"v_mfma_f32_16x16x32_bf16 v[140:143], v[28:31], v[212:215], v[140:143]\n" \
"s_waitcnt lgkmcnt(3)\n" \
"global_load_lds_dwordx4 v228, s[42:43] offset:-2048\n" \
"v_mfma_f32_16x16x32_bf16 v[172:175], v[24:27], v[216:219], v[172:175]\n" \
"v_add_u32 v230, 0xffff0000, v230\n" \
"v_add_u32 v231, 0xffff0000, v231\n" \
"v_mfma_f32_16x16x32_bf16 v[172:175], v[28:31], v[220:223], v[172:175]\n" \
"v_add_u32 v232, 0x4000, v232\n" \
"v_add_u32 v233, 0x4000, v233\n" \
"v_mfma_f32_16x16x32_bf16 v[168:171], v[16:19], v[216:219], v[168:171]\n" \
"v_mfma_f32_16x16x32_bf16 v[168:171], v[20:23], v[220:223], v[168:171]\n" \
"global_load_lds_dwordx4 v229, s[42:43] offset:-1024\n" \
"v_mfma_f32_16x16x32_bf16 v[164:167], v[8:11], v[216:219], v[164:167]\n" \
"s_add_u32 s42, s42, 0x80000\n" \
"s_addc_u32 s43, s43, 0\n" \
"v_mfma_f32_16x16x32_bf16 v[164:167], v[12:15], v[220:223], v[164:167]\n" \
"v_mfma_f32_16x16x32_bf16 v[160:163], v[0:3], v[216:219], v[160:163]\n" \
"s_waitcnt vmcnt(20) lgkmcnt(0)\n" \
"v_mfma_f32_16x16x32_bf16 v[160:163], v[4:7], v[220:223], v[160:163]\n" \
"s_barrier\n" \
"v_mfma_f32_16x16x32_bf16 v[80:83], v[32:35], v[192:195], v[80:83]\n" \
"ds_read_b64_tr_b16 v[0:1], v232 offset:0\n" \
"v_mfma_f32_16x16x32_bf16 v[80:83], v[36:39], v[196:199], v[80:83]\n" \
"ds_read_b64_tr_b16 v[2:3], v232 offset:2048\n" \
"v_mfma_f32_16x16x32_bf16 v[84:87], v[40:43], v[192:195], v[84:87]\n" \
"s_add_u32 m0, s87, 135168\n" \
"ds_read_b64_tr_b16 v[4:5], v232 offset:8192\n" \
"v_mfma_f32_16x16x32_bf16 v[84:87], v[44:47], v[196:199], v[84:87]\n" \
"global_load_lds_dwordx4 v224, s[48:49] offset:-4096\n" \
"v_mfma_f32_16x16x32_bf16 v[88:91], v[48:51], v[192:195], v[88:91]\n" \
"ds_read_b64_tr_b16 v[6:7], v232 offset:10240\n" \
"v_mfma_f32_16x16x32_bf16 v[88:91], v[52:55], v[196:199], v[88:91]\n" \
"ds_read_b64_tr_b16 v[8:9], v233 offset:0\n" \
"v_mfma_f32_16x16x32_bf16 v[92:95], v[56:59], v[192:195], v[92:95]\n" \
"ds_read_b64_tr_b16 v[10:11], v233 offset:2048\n" \
"v_mfma_f32_16x16x32_bf16 v[92:95], v[60:63], v[196:199], v[92:95]\n" \
"global_load_lds_dwordx4 v225, s[48:49] offset:-3072\n" \
"v_mfma_f32_16x16x32_bf16 v[124:127], v[56:59], v[200:203], v[124:127]\n" \
"ds_read_b128 v[192:195], v230 offset:0\n" \
"v_mfma_f32_16x16x32_bf16 v[124:127], v[60:63], v[204:207], v[124:127]\n" \
"ds_read_b128 v[196:199], v231 offset:0\n" \
"v_mfma_f32_16x16x32_bf16 v[120:123], v[48:51], v[200:203], v[120:123]\n" \
"ds_read_b64_tr_b16 v[12:13], v233 offset:8192\n" \
"v_mfma_f32_16x16x32_bf16 v[120:123], v[52:55], v[204:207], v[120:123]\n" \
"global_load_lds_dwordx4 v226, s[48:49] offset:-2048\n" \
"v_mfma_f32_16x16x32_bf16 v[116:119], v[40:43], v[200:203], v[116:119]\n" \
"ds_read_b64_tr_b16 v[14:15], v233 offset:10240\n" \
"v_mfma_f32_16x16x32_bf16 v[116:119], v[44:47], v[204:207], v[116:119]\n" \
"ds_read_b64_tr_b16 v[16:17], v232 offset:512\n" \
"v_mfma_f32_16x16x32_bf16 v[112:115], v[32:35], v[200:203], v[112:115]\n" \
"ds_read_b64_tr_b16 v[18:19], v232 offset:2560\n" \
"v_mfma_f32_16x16x32_bf16 v[112:115], v[36:39], v[204:207], v[112:115]\n" \
"global_load_lds_dwordx4 v227, s[48:49] offset:-1024\n" \
"v_mfma_f32_16x16x32_bf16 v[144:147], v[32:35], v[208:211], v[144:147]\n" \
"s_add_u32 s48, s48, 0x80\n" \
"s_addc_u32 s49, s49, 0\n" \
"ds_read_b128 v[200:203], v230 offset:2048\n" \
"v_mfma_f32_16x16x32_bf16 v[144:147], v[36:39], v[212:215], v[144:147]\n" \
"ds_read_b128 v[204:207], v231 offset:2048\n" \
"v_mfma_f32_16x16x32_bf16 v[148:151], v[40:43], v[208:211], v[148:151]\n" \
"ds_read_b64_tr_b16 v[20:21], v232 offset:8704\n" \
"v_mfma_f32_16x16x32_bf16 v[148:151], v[44:47], v[212:215], v[148:151]\n" \
"global_load_lds_dwordx4 v224, s[46:47] offset:0\n" \
"v_mfma_f32_16x16x32_bf16 v[152:155], v[48:51], v[208:211], v[152:155]\n" \
"ds_read_b64_tr_b16 v[22:23], v232 offset:10752\n" \
"v_mfma_f32_16x16x32_bf16 v[152:155], v[52:55], v[212:215], v[152:155]\n" \
"ds_read_b64_tr_b16 v[24:25], v233 offset:512\n" \
"v_mfma_f32_16x16x32_bf16 v[156:159], v[56:59], v[208:211], v[156:159]\n" \
"ds_read_b64_tr_b16 v[26:27], v233 offset:2560\n" \
"v_mfma_f32_16x16x32_bf16 v[156:159], v[60:63], v[212:215], v[156:159]\n" \
"global_load_lds_dwordx4 v225, s[46:47] offset:1024\n" \
"v_mfma_f32_16x16x32_bf16 v[188:191], v[56:59], v[216:219], v[188:191]\n" \
"ds_read_b128 v[208:211], v230 offset:4096\n" \
"v_mfma_f32_16x16x32_bf16 v[188:191], v[60:63], v[220:223], v[188:191]\n" \
"ds_read_b128 v[212:215], v231 offset:4096\n" \
"v_mfma_f32_16x16x32_bf16 v[184:187], v[48:51], v[216:219], v[184:187]\n" \
"ds_read_b64_tr_b16 v[28:29], v233 offset:8704\n" \
"v_mfma_f32_16x16x32_bf16 v[184:187], v[52:55], v[220:223], v[184:187]\n" \
"global_load_lds_dwordx4 v226, s[46:47] offset:2048\n" \
"v_mfma_f32_16x16x32_bf16 v[180:183], v[40:43], v[216:219], v[180:183]\n" \
"ds_read_b64_tr_b16 v[30:31], v233 offset:10752\n" \
"v_mfma_f32_16x16x32_bf16 v[180:183], v[44:47], v[220:223], v[180:183]\n" \
"v_mfma_f32_16x16x32_bf16 v[176:179], v[32:35], v[216:219], v[176:179]\n" \
"v_mfma_f32_16x16x32_bf16 v[176:179], v[36:39], v[220:223], v[176:179]\n" \
"s_waitcnt lgkmcnt(1)\n" \
"global_load_lds_dwordx4 v227, s[46:47] offset:3072\n" \
"v_mfma_f32_16x16x32_bf16 v[64:67], v[0:3], v[192:195], v[64:67]\n" \
"s_add_u32 s46, s46, 0x80\n" \
"s_addc_u32 s47, s47, 0\n" \
"ds_read_b128 v[216:219], v230 offset:6144\n" \
"v_mfma_f32_16x16x32_bf16 v[64:67], v[4:7], v[196:199], v[64:67]\n" \
"ds_read_b128 v[220:223], v231 offset:6144\n" \
"v_mfma_f32_16x16x32_bf16 v[68:71], v[8:11], v[192:195], v[68:71]\n" \
"ds_read_b64_tr_b16 v[32:33], v232 offset:1024\n" \
"v_mfma_f32_16x16x32_bf16 v[68:71], v[12:15], v[196:199], v[68:71]\n" \
"buffer_store_dwordx4 a[148:151], v248, s[56:59], s90 offen offset:1024 sc1\n" \
"v_mfma_f32_16x16x32_bf16 v[72:75], v[16:19], v[192:195], v[72:75]\n" \
"ds_read_b64_tr_b16 v[34:35], v232 offset:3072\n" \
"v_mfma_f32_16x16x32_bf16 v[72:75], v[20:23], v[196:199], v[72:75]\n" \
"s_waitcnt lgkmcnt(3)\n" \
"ds_read_b64_tr_b16 v[36:37], v232 offset:9216\n" \
"v_mfma_f32_16x16x32_bf16 v[76:79], v[24:27], v[192:195], v[76:79]\n" \
"ds_read_b64_tr_b16 v[38:39], v232 offset:11264\n" \
"v_mfma_f32_16x16x32_bf16 v[76:79], v[28:31], v[196:199], v[76:79]\n" \
"v_mfma_f32_16x16x32_bf16 v[108:111], v[24:27], v[200:203], v[108:111]\n" \
"ds_read_b64_tr_b16 v[40:41], v233 offset:1024\n" \
"v_mfma_f32_16x16x32_bf16 v[108:111], v[28:31], v[204:207], v[108:111]\n" \
"ds_read_b64_tr_b16 v[42:43], v233 offset:3072\n" \
"v_mfma_f32_16x16x32_bf16 v[104:107], v[16:19], v[200:203], v[104:107]\n" \
"ds_read_b64_tr_b16 v[44:45], v233 offset:9216\n" \
"v_mfma_f32_16x16x32_bf16 v[104:107], v[20:23], v[204:207], v[104:107]\n" \
"v_mfma_f32_16x16x32_bf16 v[100:103], v[8:11], v[200:203], v[100:103]\n" \
"ds_read_b64_tr_b16 v[46:47], v233 offset:11264\n" \
"v_mfma_f32_16x16x32_bf16 v[100:103], v[12:15], v[204:207], v[100:103]\n" \
"ds_read_b64_tr_b16 v[48:49], v232 offset:1536\n" \
"v_mfma_f32_16x16x32_bf16 v[96:99], v[0:3], v[200:203], v[96:99]\n" \
"s_add_u32 m0, s95, 20480\n" \
"ds_read_b64_tr_b16 v[50:51], v232 offset:3584\n" \
"v_mfma_f32_16x16x32_bf16 v[96:99], v[4:7], v[204:207], v[96:99]\n" \
"global_load_lds_dwordx4 v228, s[44:45] offset:-4096\n" \
"v_mfma_f32_16x16x32_bf16 v[128:131], v[0:3], v[208:211], v[128:131]\n" \
"ds_read_b64_tr_b16 v[52:53], v232 offset:9728\n" \
"v_mfma_f32_16x16x32_bf16 v[128:131], v[4:7], v[212:215], v[128:131]\n" \
"ds_read_b64_tr_b16 v[54:55], v232 offset:11776\n" \
"v_mfma_f32_16x16x32_bf16 v[132:135], v[8:11], v[208:211], v[132:135]\n" \
"ds_read_b64_tr_b16 v[56:57], v233 offset:1536\n" \
"v_mfma_f32_16x16x32_bf16 v[132:135], v[12:15], v[212:215], v[132:135]\n" \
"global_load_lds_dwordx4 v229, s[44:45] offset:-3072\n" \
"v_mfma_f32_16x16x32_bf16 v[136:139], v[16:19], v[208:211], v[136:139]\n" \
"s_add_u32 s44, s44, 0x80000\n" \
"s_addc_u32 s45, s45, 0\n" \
"ds_read_b64_tr_b16 v[58:59], v233 offset:3584\n" \
"v_mfma_f32_16x16x32_bf16 v[136:139], v[20:23], v[212:215], v[136:139]\n" \
"ds_read_b64_tr_b16 v[60:61], v233 offset:9728\n" \
"v_mfma_f32_16x16x32_bf16 v[140:143], v[24:27], v[208:211], v[140:143]\n" \
"ds_read_b64_tr_b16 v[62:63], v233 offset:11776\n" \
"v_mfma_f32_16x16x32_bf16 v[140:143], v[28:31], v[212:215], v[140:143]\n" \
"s_waitcnt lgkmcnt(3)\n" \
"global_load_lds_dwordx4 v228, s[42:43] offset:-2048\n" \
"v_mfma_f32_16x16x32_bf16 v[172:175], v[24:27], v[216:219], v[172:175]\n" \
"v_add_u32 v230, 0x8000, v230\n" \
"v_add_u32 v231, 0x8000, v231\n" \
"v_mfma_f32_16x16x32_bf16 v[172:175], v[28:31], v[220:223], v[172:175]\n" \
"v_add_u32 v232, 0x4000, v232\n" \
"v_add_u32 v233, 0x4000, v233\n" \
"v_mfma_f32_16x16x32_bf16 v[168:171], v[16:19], v[216:219], v[168:171]\n" \
"v_mfma_f32_16x16x32_bf16 v[168:171], v[20:23], v[220:223], v[168:171]\n" \
"global_load_lds_dwordx4 v229, s[42:43] offset:-1024\n" \
"v_mfma_f32_16x16x32_bf16 v[164:167], v[8:11], v[216:219], v[164:167]\n" \
"s_add_u32 s42, s42, 0x80000\n" \
"s_addc_u32 s43, s43, 0\n" \
"v_mfma_f32_16x16x32_bf16 v[164:167], v[12:15], v[220:223], v[164:167]\n" \
"v_mfma_f32_16x16x32_bf16 v[160:163], v[0:3], v[216:219], v[160:163]\n" \
"s_waitcnt vmcnt(18) lgkmcnt(0)\n" \
"v_mfma_f32_16x16x32_bf16 v[160:163], v[4:7], v[220:223], v[160:163]\n" \
"s_barrier\n" \
"v_mfma_f32_16x16x32_bf16 v[80:83], v[32:35], v[192:195], v[80:83]\n" \
"ds_read_b64_tr_b16 v[0:1], v232 offset:0\n" \
"v_mfma_f32_16x16x32_bf16 v[80:83], v[36:39], v[196:199], v[80:83]\n" \
"ds_read_b64_tr_b16 v[2:3], v232 offset:2048\n" \
"v_mfma_f32_16x16x32_bf16 v[84:87], v[40:43], v[192:195], v[84:87]\n" \
"s_add_u32 m0, s87, 69632\n" \
"ds_read_b64_tr_b16 v[4:5], v232 offset:8192\n" \
"v_mfma_f32_16x16x32_bf16 v[84:87], v[44:47], v[196:199], v[84:87]\n" \
"global_load_lds_dwordx4 v224, s[48:49] offset:-4096\n" \
"v_mfma_f32_16x16x32_bf16 v[88:91], v[48:51], v[192:195], v[88:91]\n" \
"ds_read_b64_tr_b16 v[6:7], v232 offset:10240\n" \
"v_mfma_f32_16x16x32_bf16 v[88:91], v[52:55], v[196:199], v[88:91]\n" \
"ds_read_b64_tr_b16 v[8:9], v233 offset:0\n" \
"v_mfma_f32_16x16x32_bf16 v[92:95], v[56:59], v[192:195], v[92:95]\n" \
"ds_read_b64_tr_b16 v[10:11], v233 offset:2048\n" \
"v_mfma_f32_16x16x32_bf16 v[92:95], v[60:63], v[196:199], v[92:95]\n" \
"global_load_lds_dwordx4 v225, s[48:49] offset:-3072\n" \
"v_mfma_f32_16x16x32_bf16 v[124:127], v[56:59], v[200:203], v[124:127]\n" \
"ds_read_b128 v[192:195], v230 offset:0\n" \
"v_mfma_f32_16x16x32_bf16 v[124:127], v[60:63], v[204:207], v[124:127]\n" \
"ds_read_b128 v[196:199], v231 offset:0\n" \
"v_mfma_f32_16x16x32_bf16 v[120:123], v[48:51], v[200:203], v[120:123]\n" \
"ds_read_b64_tr_b16 v[12:13], v233 offset:8192\n" \
"v_mfma_f32_16x16x32_bf16 v[120:123], v[52:55], v[204:207], v[120:123]\n" \
"global_load_lds_dwordx4 v226, s[48:49] offset:-2048\n" \
"v_mfma_f32_16x16x32_bf16 v[116:119], v[40:43], v[200:203], v[116:119]\n" \
"ds_read_b64_tr_b16 v[14:15], v233 offset:10240\n" \
"v_mfma_f32_16x16x32_bf16 v[116:119], v[44:47], v[204:207], v[116:119]\n" \
"ds_read_b64_tr_b16 v[16:17], v232 offset:512\n" \
"v_mfma_f32_16x16x32_bf16 v[112:115], v[32:35], v[200:203], v[112:115]\n" \
"ds_read_b64_tr_b16 v[18:19], v232 offset:2560\n" \
"v_mfma_f32_16x16x32_bf16 v[112:115], v[36:39], v[204:207], v[112:115]\n" \
"global_load_lds_dwordx4 v227, s[48:49] offset:-1024\n" \
"v_mfma_f32_16x16x32_bf16 v[144:147], v[32:35], v[208:211], v[144:147]\n" \
"s_add_u32 s48, s48, 0x80\n" \
"s_addc_u32 s49, s49, 0\n" \
"ds_read_b128 v[200:203], v230 offset:2048\n" \
"v_mfma_f32_16x16x32_bf16 v[144:147], v[36:39], v[212:215], v[144:147]\n" \
"ds_read_b128 v[204:207], v231 offset:2048\n" \
"v_mfma_f32_16x16x32_bf16 v[148:151], v[40:43], v[208:211], v[148:151]\n" \
"ds_read_b64_tr_b16 v[20:21], v232 offset:8704\n" \
"v_mfma_f32_16x16x32_bf16 v[148:151], v[44:47], v[212:215], v[148:151]\n" \
"global_load_lds_dwordx4 v224, s[46:47] offset:0\n" \
"v_mfma_f32_16x16x32_bf16 v[152:155], v[48:51], v[208:211], v[152:155]\n" \
"ds_read_b64_tr_b16 v[22:23], v232 offset:10752\n" \
"v_mfma_f32_16x16x32_bf16 v[152:155], v[52:55], v[212:215], v[152:155]\n" \
"ds_read_b64_tr_b16 v[24:25], v233 offset:512\n" \
"v_mfma_f32_16x16x32_bf16 v[156:159], v[56:59], v[208:211], v[156:159]\n" \
"ds_read_b64_tr_b16 v[26:27], v233 offset:2560\n" \
"v_mfma_f32_16x16x32_bf16 v[156:159], v[60:63], v[212:215], v[156:159]\n" \
"global_load_lds_dwordx4 v225, s[46:47] offset:1024\n" \
"v_mfma_f32_16x16x32_bf16 v[188:191], v[56:59], v[216:219], v[188:191]\n" \
"ds_read_b128 v[208:211], v230 offset:4096\n" \
"v_mfma_f32_16x16x32_bf16 v[188:191], v[60:63], v[220:223], v[188:191]\n" \
"ds_read_b128 v[212:215], v231 offset:4096\n" \
"v_mfma_f32_16x16x32_bf16 v[184:187], v[48:51], v[216:219], v[184:187]\n" \
"ds_read_b64_tr_b16 v[28:29], v233 offset:8704\n" \
"v_mfma_f32_16x16x32_bf16 v[184:187], v[52:55], v[220:223], v[184:187]\n" \
"global_load_lds_dwordx4 v226, s[46:47] offset:2048\n" \
"v_mfma_f32_16x16x32_bf16 v[180:183], v[40:43], v[216:219], v[180:183]\n" \
"ds_read_b64_tr_b16 v[30:31], v233 offset:10752\n" \
"v_mfma_f32_16x16x32_bf16 v[180:183], v[44:47], v[220:223], v[180:183]\n" \
"v_mfma_f32_16x16x32_bf16 v[176:179], v[32:35], v[216:219], v[176:179]\n" \
"v_mfma_f32_16x16x32_bf16 v[176:179], v[36:39], v[220:223], v[176:179]\n" \
"s_waitcnt lgkmcnt(1)\n" \
"global_load_lds_dwordx4 v227, s[46:47] offset:3072\n" \
"v_mfma_f32_16x16x32_bf16 v[64:67], v[0:3], v[192:195], v[64:67]\n" \
"s_add_u32 s46, s46, 0x80\n" \
"s_addc_u32 s47, s47, 0\n" \
"ds_read_b128 v[216:219], v230 offset:6144\n" \
"v_mfma_f32_16x16x32_bf16 v[64:67], v[4:7], v[196:199], v[64:67]\n" \
"s_mul_i32 s77, s62, 557824\n" \
"ds_read_b128 v[220:223], v231 offset:6144\n" \
"s_add_u32 s77, s77, s95\n" \
"v_mfma_f32_16x16x32_bf16 v[68:71], v[8:11], v[192:195], v[68:71]\n" \
"s_add_u32 s36, s28, s77\n" \
"s_mov_b32 s37, s29\n" \
"ds_read_b64_tr_b16 v[32:33], v232 offset:1024\n" \
"v_mfma_f32_16x16x32_bf16 v[68:71], v[12:15], v[196:199], v[68:71]\n" \
"buffer_store_dwordx4 a[152:155], v248, s[56:59], s90 offen offset:2048 sc1\n" \
"s_add_u32 s77, s77, 17850368\n" \
"s_add_u32 s38, s28, s77\n" \
"s_mov_b32 s39, s29\n" \
"s_and_b32 s74, s63, 1\n" \
"v_mfma_f32_16x16x32_bf16 v[72:75], v[16:19], v[192:195], v[72:75]\n" \
"s_lshl_b32 s74, s74, 15\n" \
"s_xor_b32 s74, s74, 0x18000\n" \
"ds_read_b64_tr_b16 v[34:35], v232 offset:3072\n" \
"v_mfma_f32_16x16x32_bf16 v[72:75], v[20:23], v[196:199], v[72:75]\n" \
"s_waitcnt lgkmcnt(3)\n" \
"ds_read_b64_tr_b16 v[36:37], v232 offset:9216\n" \
"v_add_u32 v247, s74, v248\n" \
"v_mfma_f32_16x16x32_bf16 v[76:79], v[24:27], v[192:195], v[76:79]\n" \
"s_add_u32 s74, s74, s95\n" \
"s_lshr_b32 s83, s61, 1\n" \
"ds_read_b64_tr_b16 v[38:39], v232 offset:11264\n" \
"v_mfma_f32_16x16x32_bf16 v[76:79], v[28:31], v[196:199], v[76:79]\n" \
"buffer_store_dwordx4 a[156:159], v248, s[56:59], s90 offen offset:3072 sc1\n" \
"s_lshl_b32 s75, s63, 2\n" \
"s_add_u32 s77, s83, s75\n" \
"s_mul_i32 s77, s77, 266240\n" \
"s_sub_u32 s78, s83, s75\n" \
"v_mfma_f32_16x16x32_bf16 v[108:111], v[24:27], v[200:203], v[108:111]\n" \
"s_add_u32 s78, s78, 60\n" \
"s_mul_i32 s78, s78, 266240\n" \
"ds_read_b64_tr_b16 v[40:41], v233 offset:1024\n" \
"v_mfma_f32_16x16x32_bf16 v[108:111], v[28:31], v[204:207], v[108:111]\n" \
"s_and_b32 s82, s61, 1\n" \
"ds_read_b64_tr_b16 v[42:43], v233 offset:3072\n" \
"v_mfma_f32_16x16x32_bf16 v[104:107], v[16:19], v[200:203], v[104:107]\n" \
"s_cmp_eq_u32 s82, 0\n" \
"s_cselect_b32 s16, 0, 0xff00\n" \
"ds_read_b64_tr_b16 v[44:45], v233 offset:9216\n" \
"v_mfma_f32_16x16x32_bf16 v[104:107], v[20:23], v[204:207], v[104:107]\n" \
"buffer_load_dword v244, v245, s[56:59], s91 offen sc1\n" \
"v_mfma_f32_16x16x32_bf16 v[100:103], v[8:11], v[200:203], v[100:103]\n" \
"s_mov_b32 s17, s16\n" \
"s_lshl_b32 s75, s82, 8\n" \
"ds_read_b64_tr_b16 v[46:47], v233 offset:11264\n" \
"v_mfma_f32_16x16x32_bf16 v[100:103], v[12:15], v[204:207], v[100:103]\n" \
"s_lshl_b32 s76, s62, 13\n" \
"s_add_u32 s75, s75, s76\n" \
"ds_read_b64_tr_b16 v[48:49], v232 offset:1536\n" \
"v_mfma_f32_16x16x32_bf16 v[96:99], v[0:3], v[200:203], v[96:99]\n" \
"s_add_u32 m0, s95, 36864\n" \
"s_add_u32 s75, s75, 35700736\n" \
"ds_read_b64_tr_b16 v[50:51], v232 offset:3584\n" \
"v_mfma_f32_16x16x32_bf16 v[96:99], v[4:7], v[204:207], v[96:99]\n" \
"s_add_u32 s77, s77, s75\n" \
"global_load_lds_dwordx4 v228, s[44:45] offset:-4096\n" \
"v_mfma_f32_16x16x32_bf16 v[128:131], v[0:3], v[208:211], v[128:131]\n" \
"s_add_u32 s78, s78, s75\n" \
"s_mul_i32 s83, s83, 9216\n" \
"ds_read_b64_tr_b16 v[52:53], v232 offset:9728\n" \
"v_mfma_f32_16x16x32_bf16 v[128:131], v[4:7], v[212:215], v[128:131]\n" \
"s_lshl_b32 s75, s82, 12\n" \
"s_add_u32 s83, s83, s75\n" \
"ds_read_b64_tr_b16 v[54:55], v232 offset:11776\n" \
"v_mfma_f32_16x16x32_bf16 v[132:135], v[8:11], v[208:211], v[132:135]\n" \
"s_add_u32 s83, s83, 4096\n" \
"s_lshl_b32 s82, s82, 14\n" \
"ds_read_b64_tr_b16 v[56:57], v233 offset:1536\n" \
"v_mfma_f32_16x16x32_bf16 v[132:135], v[12:15], v[212:215], v[132:135]\n" \
"global_load_lds_dwordx4 v229, s[44:45] offset:-3072\n" \
"v_mfma_f32_16x16x32_bf16 v[136:139], v[16:19], v[208:211], v[136:139]\n" \
"s_add_u32 s44, s44, 0x80000\n" \
"s_addc_u32 s45, s45, 0\n" \
"ds_read_b64_tr_b16 v[58:59], v233 offset:3584\n" \
"v_mfma_f32_16x16x32_bf16 v[136:139], v[20:23], v[212:215], v[136:139]\n" \
"s_cmp_eq_u32 s63, 7\n" \
"s_cselect_b32 s82, 0, s82\n" \
"ds_read_b64_tr_b16 v[60:61], v233 offset:9728\n" \
"v_mfma_f32_16x16x32_bf16 v[140:143], v[24:27], v[208:211], v[140:143]\n" \
"s_mov_b32 s75, s63\n" \
"s_sub_u32 s76, 7, s63\n" \
"ds_read_b64_tr_b16 v[62:63], v233 offset:11776\n" \
"v_mfma_f32_16x16x32_bf16 v[140:143], v[28:31], v[212:215], v[140:143]\n" \
"s_waitcnt lgkmcnt(3)\n" \
"global_load_lds_dwordx4 v228, s[42:43] offset:-2048\n" \
"s_mov_b32 s73, s95\n" \
"v_mfma_f32_16x16x32_bf16 v[172:175], v[24:27], v[216:219], v[172:175]\n" \
"s_movk_i32 s94, 0x207\n" \
"s_mov_b64 s[40:41], s[14:15]\n" \
"s_mov_b32 s80, 0\n" \
"v_mfma_f32_16x16x32_bf16 v[172:175], v[28:31], v[220:223], v[172:175]\n" \
"s_mov_b32 s81, 0\n" \
"v_add_u32 v230, 0x8000, v230\n" \
"v_add_u32 v231, 0x8000, v231\n" \
"v_mfma_f32_16x16x32_bf16 v[168:171], v[16:19], v[216:219], v[168:171]\n" \
"s_cmp_eq_u32 s63, 7\n" \
"s_cselect_b64 s[40:41], s[18:19], s[40:41]\n" \
"s_cselect_b32 s80, 12, s80\n" \
"v_mfma_f32_16x16x32_bf16 v[168:171], v[20:23], v[220:223], v[168:171]\n" \
"global_load_lds_dwordx4 v229, s[42:43] offset:-1024\n" \
"v_add_u32 v232, 0xffff4000, v232\n" \
"v_mfma_f32_16x16x32_bf16 v[164:167], v[8:11], v[216:219], v[164:167]\n" \
"s_add_u32 s42, s42, 0x80000\n" \
"s_addc_u32 s43, s43, 0\n" \
"v_add_u32 v233, 0xffff4000, v233\n" \
"v_mfma_f32_16x16x32_bf16 v[164:167], v[12:15], v[220:223], v[164:167]\n" \
"s_cmp_eq_u32 s63, 6\n" \
"s_cselect_b64 s[40:41], s[22:23], s[40:41]\n" \
"s_cselect_b32 s80, 12, s80\n" \
"v_mfma_f32_16x16x32_bf16 v[160:163], v[0:3], v[216:219], v[160:163]\n" \
"s_waitcnt vmcnt(20) lgkmcnt(0)\n" \
"v_mfma_f32_16x16x32_bf16 v[160:163], v[4:7], v[220:223], v[160:163]\n" \
"s_barrier\n" \
"v_mfma_f32_16x16x32_bf16 v[80:83], v[32:35], v[192:195], v[80:83]\n" \
"ds_read_b64_tr_b16 v[0:1], v232 offset:0\n" \
"v_mfma_f32_16x16x32_bf16 v[80:83], v[36:39], v[196:199], v[80:83]\n" \
"ds_read_b64_tr_b16 v[2:3], v232 offset:2048\n" \
"v_mfma_f32_16x16x32_bf16 v[84:87], v[40:43], v[192:195], v[84:87]\n" \
"s_add_u32 m0, s87, 102400\n" \
"ds_read_b64_tr_b16 v[4:5], v232 offset:8192\n" \
"v_mfma_f32_16x16x32_bf16 v[84:87], v[44:47], v[196:199], v[84:87]\n" \
"global_load_lds_dwordx4 v224, s[48:49] offset:-4096\n" \
"v_mfma_f32_16x16x32_bf16 v[88:91], v[48:51], v[192:195], v[88:91]\n" \
"ds_read_b64_tr_b16 v[6:7], v232 offset:10240\n" \
"v_mfma_f32_16x16x32_bf16 v[88:91], v[52:55], v[196:199], v[88:91]\n" \
"ds_read_b64_tr_b16 v[8:9], v233 offset:0\n" \
"v_mfma_f32_16x16x32_bf16 v[92:95], v[56:59], v[192:195], v[92:95]\n" \
"ds_read_b64_tr_b16 v[10:11], v233 offset:2048\n" \
"v_mfma_f32_16x16x32_bf16 v[92:95], v[60:63], v[196:199], v[92:95]\n" \
"global_load_lds_dwordx4 v225, s[48:49] offset:-3072\n" \
"v_mfma_f32_16x16x32_bf16 v[124:127], v[56:59], v[200:203], v[124:127]\n" \
"ds_read_b128 v[192:195], v230 offset:0\n" \
"v_mfma_f32_16x16x32_bf16 v[124:127], v[60:63], v[204:207], v[124:127]\n" \
"ds_read_b128 v[196:199], v231 offset:0\n" \
"v_mfma_f32_16x16x32_bf16 v[120:123], v[48:51], v[200:203], v[120:123]\n" \
"ds_read_b64_tr_b16 v[12:13], v233 offset:8192\n" \
"v_mfma_f32_16x16x32_bf16 v[120:123], v[52:55], v[204:207], v[120:123]\n" \
"global_load_lds_dwordx4 v226, s[48:49] offset:-2048\n" \
"v_mfma_f32_16x16x32_bf16 v[116:119], v[40:43], v[200:203], v[116:119]\n" \
"ds_read_b64_tr_b16 v[14:15], v233 offset:10240\n" \
"v_mfma_f32_16x16x32_bf16 v[116:119], v[44:47], v[204:207], v[116:119]\n" \
"ds_read_b64_tr_b16 v[16:17], v232 offset:512\n" \
"v_mfma_f32_16x16x32_bf16 v[112:115], v[32:35], v[200:203], v[112:115]\n" \
"ds_read_b64_tr_b16 v[18:19], v232 offset:2560\n" \
"v_mfma_f32_16x16x32_bf16 v[112:115], v[36:39], v[204:207], v[112:115]\n" \
"global_load_lds_dwordx4 v227, s[48:49] offset:-1024\n" \
"v_mfma_f32_16x16x32_bf16 v[144:147], v[32:35], v[208:211], v[144:147]\n" \
"s_add_u32 s48, s48, 0x80\n" \
"s_addc_u32 s49, s49, 0\n" \
"ds_read_b128 v[200:203], v230 offset:2048\n" \
"v_mfma_f32_16x16x32_bf16 v[144:147], v[36:39], v[212:215], v[144:147]\n" \
"ds_read_b128 v[204:207], v231 offset:2048\n" \
"v_mfma_f32_16x16x32_bf16 v[148:151], v[40:43], v[208:211], v[148:151]\n" \
"ds_read_b64_tr_b16 v[20:21], v232 offset:8704\n" \
"v_mfma_f32_16x16x32_bf16 v[148:151], v[44:47], v[212:215], v[148:151]\n" \
"global_load_lds_dwordx4 v224, s[46:47] offset:0\n" \
"v_mfma_f32_16x16x32_bf16 v[152:155], v[48:51], v[208:211], v[152:155]\n" \
"ds_read_b64_tr_b16 v[22:23], v232 offset:10752\n" \
"v_mfma_f32_16x16x32_bf16 v[152:155], v[52:55], v[212:215], v[152:155]\n" \
"ds_read_b64_tr_b16 v[24:25], v233 offset:512\n" \
"v_mfma_f32_16x16x32_bf16 v[156:159], v[56:59], v[208:211], v[156:159]\n" \
"ds_read_b64_tr_b16 v[26:27], v233 offset:2560\n" \
"v_mfma_f32_16x16x32_bf16 v[156:159], v[60:63], v[212:215], v[156:159]\n" \
"global_load_lds_dwordx4 v225, s[46:47] offset:1024\n" \
"v_mfma_f32_16x16x32_bf16 v[188:191], v[56:59], v[216:219], v[188:191]\n" \
"ds_read_b128 v[208:211], v230 offset:4096\n" \
"v_mfma_f32_16x16x32_bf16 v[188:191], v[60:63], v[220:223], v[188:191]\n" \
"Lpg4399_%=:\n" \
"s_cbranch_execz Lpg5578_%=\n" \
"ds_read_b128 v[212:215], v231 offset:4096\n" \
"v_mfma_f32_16x16x32_bf16 v[184:187], v[48:51], v[216:219], v[184:187]\n" \
"ds_read_b64_tr_b16 v[28:29], v233 offset:8704\n" \
"v_mfma_f32_16x16x32_bf16 v[184:187], v[52:55], v[220:223], v[184:187]\n" \
"global_load_lds_dwordx4 v226, s[46:47] offset:2048\n" \
"v_mfma_f32_16x16x32_bf16 v[180:183], v[40:43], v[216:219], v[180:183]\n" \
"ds_read_b64_tr_b16 v[30:31], v233 offset:10752\n" \
"v_mfma_f32_16x16x32_bf16 v[180:183], v[44:47], v[220:223], v[180:183]\n" \
"s_cmp_eq_u32 s63, 5\n" \
"s_cselect_b64 s[40:41], s[18:19], s[40:41]\n" \
"s_cselect_b32 s80, 4, s80\n" \
"v_mfma_f32_16x16x32_bf16 v[176:179], v[32:35], v[216:219], v[176:179]\n" \
"s_cmp_eq_u32 s63, 5\n" \
"s_cselect_b32 s81, 384, s81\n" \
"v_mfma_f32_16x16x32_bf16 v[176:179], v[36:39], v[220:223], v[176:179]\n" \
"s_waitcnt lgkmcnt(1)\n" \
"global_load_lds_dwordx4 v227, s[46:47] offset:3072\n" \
"v_mfma_f32_16x16x32_bf16 v[64:67], v[0:3], v[192:195], v[64:67]\n" \
"s_add_u32 s46, s46, 0x80\n" \
"s_addc_u32 s47, s47, 0\n" \
"ds_read_b128 v[216:219], v230 offset:6144\n" \
"v_mfma_f32_16x16x32_bf16 v[64:67], v[4:7], v[196:199], v[64:67]\n" \
"ds_read_b128 v[220:223], v231 offset:6144\n" \
"s_add_u32 s90, s88, 278528\n" \
"v_mfma_f32_16x16x32_bf16 v[68:71], v[8:11], v[192:195], v[68:71]\n" \
"ds_read_b64_tr_b16 v[32:33], v232 offset:1024\n" \
"v_mfma_f32_16x16x32_bf16 v[68:71], v[12:15], v[196:199], v[68:71]\n" \
"buffer_store_dwordx4 a[64:67], v249, s[56:59], s90 offen offset:0 sc1\n" \
"s_cmp_eq_u32 s63, 4\n" \
"s_cselect_b64 s[40:41], s[20:21], s[40:41]\n" \
"s_cselect_b32 s80, 8, s80\n" \
"v_mfma_f32_16x16x32_bf16 v[72:75], v[16:19], v[192:195], v[72:75]\n" \
"ds_read_b64_tr_b16 v[34:35], v232 offset:3072\n" \
"v_mfma_f32_16x16x32_bf16 v[72:75], v[20:23], v[196:199], v[72:75]\n" \
"s_waitcnt lgkmcnt(3)\n" \
"ds_read_b64_tr_b16 v[36:37], v232 offset:9216\n" \
"v_mfma_f32_16x16x32_bf16 v[76:79], v[24:27], v[192:195], v[76:79]\n" \
"ds_read_b128 a[64:67], v254\n" \
"ds_read_b64_tr_b16 v[38:39], v232 offset:11264\n" \
"v_mfma_f32_16x16x32_bf16 v[76:79], v[28:31], v[196:199], v[76:79]\n" \
"s_cmp_eq_u32 s63, 2\n" \
"s_cselect_b64 s[40:41], s[24:25], s[40:41]\n" \
"s_cselect_b32 s80, 4, s80\n" \
"v_mfma_f32_16x16x32_bf16 v[108:111], v[24:27], v[200:203], v[108:111]\n" \
"s_add_u32 s81, s81, s62\n" \
"s_lshl_b32 s81, s81, 2\n" \
"ds_read_b64_tr_b16 v[40:41], v233 offset:1024\n" \
"v_mfma_f32_16x16x32_bf16 v[108:111], v[28:31], v[204:207], v[108:111]\n" \
"s_add_u32 s81, s81, s61\n" \
"s_lshl_b32 s81, s81, 12\n" \
"ds_read_b64_tr_b16 v[42:43], v233 offset:3072\n" \
"v_mfma_f32_16x16x32_bf16 v[104:107], v[16:19], v[200:203], v[104:107]\n" \
"s_add_u32 s40, s40, s81\n" \
"s_addc_u32 s41, s41, 0\n" \
"ds_read_b64_tr_b16 v[44:45], v233 offset:9216\n" \
"v_mfma_f32_16x16x32_bf16 v[104:107], v[20:23], v[204:207], v[104:107]\n" \
"s_min_u32 s81, s80, 1\n" \
"s_lshl_b32 s81, s81, 2\n" \
"v_mul_u32_u24 v252, s81, v248\n" \
"v_mfma_f32_16x16x32_bf16 v[100:103], v[8:11], v[200:203], v[100:103]\n" \
"ds_read_b128 a[68:71], v254\n" \
"ds_read_b64_tr_b16 v[46:47], v233 offset:11264\n" \
"v_mfma_f32_16x16x32_bf16 v[100:103], v[12:15], v[204:207], v[100:103]\n" \
"ds_read_b64_tr_b16 v[48:49], v232 offset:1536\n" \
"v_mfma_f32_16x16x32_bf16 v[96:99], v[0:3], v[200:203], v[96:99]\n" \
"s_add_u32 m0, s95, 53248\n" \
"ds_read_b64_tr_b16 v[50:51], v232 offset:3584\n" \
"v_mfma_f32_16x16x32_bf16 v[96:99], v[4:7], v[204:207], v[96:99]\n" \
"global_load_lds_dwordx4 v228, s[44:45] offset:-4096\n" \
"v_mfma_f32_16x16x32_bf16 v[128:131], v[0:3], v[208:211], v[128:131]\n" \
"ds_read_b64_tr_b16 v[52:53], v232 offset:9728\n" \
"v_mfma_f32_16x16x32_bf16 v[128:131], v[4:7], v[212:215], v[128:131]\n" \
"ds_read_b64_tr_b16 v[54:55], v232 offset:11776\n" \
"v_mfma_f32_16x16x32_bf16 v[132:135], v[8:11], v[208:211], v[132:135]\n" \
"ds_read_b64_tr_b16 v[56:57], v233 offset:1536\n" \
"v_mfma_f32_16x16x32_bf16 v[132:135], v[12:15], v[212:215], v[132:135]\n" \
"global_load_lds_dwordx4 v229, s[44:45] offset:-3072\n" \
"v_mfma_f32_16x16x32_bf16 v[136:139], v[16:19], v[208:211], v[136:139]\n" \
"ds_read_b64_tr_b16 v[58:59], v233 offset:3584\n" \
"v_mfma_f32_16x16x32_bf16 v[136:139], v[20:23], v[212:215], v[136:139]\n" \
"ds_read_b64_tr_b16 v[60:61], v233 offset:9728\n" \
"v_mfma_f32_16x16x32_bf16 v[140:143], v[24:27], v[208:211], v[140:143]\n" \
"ds_read_b64_tr_b16 v[62:63], v233 offset:11776\n" \
"v_mfma_f32_16x16x32_bf16 v[140:143], v[28:31], v[212:215], v[140:143]\n" \
"s_waitcnt lgkmcnt(3)\n" \
"global_load_lds_dwordx4 v228, s[42:43] offset:-2048\n" \
"v_mfma_f32_16x16x32_bf16 v[172:175], v[24:27], v[216:219], v[172:175]\n" \
"v_add_u32 v230, 0xffff0000, v230\n" \
"v_add_u32 v231, 0xffff0000, v231\n" \
"v_mfma_f32_16x16x32_bf16 v[172:175], v[28:31], v[220:223], v[172:175]\n" \
"v_add_u32 v232, 0x4000, v232\n" \
"v_add_u32 v233, 0x4000, v233\n" \
"v_mfma_f32_16x16x32_bf16 v[168:171], v[16:19], v[216:219], v[168:171]\n" \
"v_mfma_f32_16x16x32_bf16 v[168:171], v[20:23], v[220:223], v[168:171]\n" \
"global_load_lds_dwordx4 v229, s[42:43] offset:-1024\n" \
"v_mfma_f32_16x16x32_bf16 v[164:167], v[8:11], v[216:219], v[164:167]\n" \
"v_mfma_f32_16x16x32_bf16 v[164:167], v[12:15], v[220:223], v[164:167]\n" \
"v_mfma_f32_16x16x32_bf16 v[160:163], v[0:3], v[216:219], v[160:163]\n" \
"s_waitcnt vmcnt(20) lgkmcnt(0)\n" \
"v_mfma_f32_16x16x32_bf16 v[160:163], v[4:7], v[220:223], v[160:163]\n" \
"s_barrier\n" \
"v_mfma_f32_16x16x32_bf16 v[80:83], v[32:35], v[192:195], v[80:83]\n" \
"s_waitcnt vmcnt(17)\n" \
"ds_read_b64_tr_b16 v[0:1], v232 offset:0\n" \
"v_cmp_ne_u32 vcc, s60, v244\n" \
"v_mfma_f32_16x16x32_bf16 v[80:83], v[36:39], v[196:199], v[80:83]\n" \
"ds_read_b64_tr_b16 v[2:3], v232 offset:2048\n" \
"v_mfma_f32_16x16x32_bf16 v[84:87], v[40:43], v[192:195], v[84:87]\n" \
"s_add_u32 m0, s87, 135168\n" \
"ds_read_b64_tr_b16 v[4:5], v232 offset:8192\n" \
"v_mfma_f32_16x16x32_bf16 v[84:87], v[44:47], v[196:199], v[84:87]\n" \
"s_and_b32 vcc_hi, vcc_lo, 255\n" \
"s_cbranch_scc1 Lpl1_%=\n" \
"Lpr1_%=:\n" \
"global_load_lds_dwordx4 v224, s[48:49] offset:-4096\n" \
"v_mfma_f32_16x16x32_bf16 v[88:91], v[48:51], v[192:195], v[88:91]\n" \
"s_add_u32 s79, s91, 12288\n" \
"ds_read_b64_tr_b16 v[6:7], v232 offset:10240\n" \
"v_mfma_f32_16x16x32_bf16 v[88:91], v[52:55], v[196:199], v[88:91]\n" \
"ds_read_b64_tr_b16 v[8:9], v233 offset:0\n" \
"v_mfma_f32_16x16x32_bf16 v[92:95], v[56:59], v[192:195], v[92:95]\n" \
"ds_read_b64_tr_b16 v[10:11], v233 offset:2048\n" \
"v_mfma_f32_16x16x32_bf16 v[92:95], v[60:63], v[196:199], v[92:95]\n" \
"global_load_lds_dwordx4 v225, s[48:49] offset:-3072\n" \
"v_mfma_f32_16x16x32_bf16 v[124:127], v[56:59], v[200:203], v[124:127]\n" \
"ds_read_b128 v[192:195], v230 offset:0\n" \
"v_mfma_f32_16x16x32_bf16 v[124:127], v[60:63], v[204:207], v[124:127]\n" \
"ds_read_b128 v[196:199], v231 offset:0\n" \
"v_mfma_f32_16x16x32_bf16 v[120:123], v[48:51], v[200:203], v[120:123]\n" \
"ds_read_b64_tr_b16 v[12:13], v233 offset:8192\n" \
"v_mfma_f32_16x16x32_bf16 v[120:123], v[52:55], v[204:207], v[120:123]\n" \
"global_load_lds_dwordx4 v226, s[48:49] offset:-2048\n" \
"v_mfma_f32_16x16x32_bf16 v[116:119], v[40:43], v[200:203], v[116:119]\n" \
"ds_read_b64_tr_b16 v[14:15], v233 offset:10240\n" \
"v_mfma_f32_16x16x32_bf16 v[116:119], v[44:47], v[204:207], v[116:119]\n" \
"ds_read_b64_tr_b16 v[16:17], v232 offset:512\n" \
"v_mfma_f32_16x16x32_bf16 v[112:115], v[32:35], v[200:203], v[112:115]\n" \
"ds_read_b64_tr_b16 v[18:19], v232 offset:2560\n" \
"v_mfma_f32_16x16x32_bf16 v[112:115], v[36:39], v[204:207], v[112:115]\n" \
"global_load_lds_dwordx4 v227, s[48:49] offset:-1024\n" \
"v_mfma_f32_16x16x32_bf16 v[144:147], v[32:35], v[208:211], v[144:147]\n" \
"ds_read_b128 v[200:203], v230 offset:2048\n" \
"v_mfma_f32_16x16x32_bf16 v[144:147], v[36:39], v[212:215], v[144:147]\n" \
"ds_read_b128 v[204:207], v231 offset:2048\n" \
"v_mfma_f32_16x16x32_bf16 v[148:151], v[40:43], v[208:211], v[148:151]\n" \
"ds_read_b64_tr_b16 v[20:21], v232 offset:8704\n" \
"v_mfma_f32_16x16x32_bf16 v[148:151], v[44:47], v[212:215], v[148:151]\n" \
"global_load_lds_dwordx4 v224, s[46:47] offset:0\n" \
"v_mfma_f32_16x16x32_bf16 v[152:155], v[48:51], v[208:211], v[152:155]\n" \
"ds_read_b64_tr_b16 v[22:23], v232 offset:10752\n" \
"v_mfma_f32_16x16x32_bf16 v[152:155], v[52:55], v[212:215], v[152:155]\n" \
"ds_read_b64_tr_b16 v[24:25], v233 offset:512\n" \
"v_mfma_f32_16x16x32_bf16 v[156:159], v[56:59], v[208:211], v[156:159]\n" \
"ds_read_b64_tr_b16 v[26:27], v233 offset:2560\n" \
"v_mfma_f32_16x16x32_bf16 v[156:159], v[60:63], v[212:215], v[156:159]\n" \
"global_load_lds_dwordx4 v225, s[46:47] offset:1024\n" \
"v_mfma_f32_16x16x32_bf16 v[188:191], v[56:59], v[216:219], v[188:191]\n" \
"ds_read_b128 v[208:211], v230 offset:4096\n" \
"v_mfma_f32_16x16x32_bf16 v[188:191], v[60:63], v[220:223], v[188:191]\n" \
"ds_read_b128 v[212:215], v231 offset:4096\n" \
"v_mfma_f32_16x16x32_bf16 v[184:187], v[48:51], v[216:219], v[184:187]\n" \
"ds_read_b64_tr_b16 v[28:29], v233 offset:8704\n" \
"v_mfma_f32_16x16x32_bf16 v[184:187], v[52:55], v[220:223], v[184:187]\n" \
"global_load_lds_dwordx4 v226, s[46:47] offset:2048\n" \
"v_mfma_f32_16x16x32_bf16 v[180:183], v[40:43], v[216:219], v[180:183]\n" \
"ds_read_b64_tr_b16 v[30:31], v233 offset:10752\n" \
"v_mfma_f32_16x16x32_bf16 v[180:183], v[44:47], v[220:223], v[180:183]\n" \
"v_mfma_f32_16x16x32_bf16 v[176:179], v[32:35], v[216:219], v[176:179]\n" \
"v_mfma_f32_16x16x32_bf16 v[176:179], v[36:39], v[220:223], v[176:179]\n" \
"s_waitcnt lgkmcnt(1)\n" \
"global_load_lds_dwordx4 v227, s[46:47] offset:3072\n" \
"v_mfma_f32_16x16x32_bf16 v[64:67], v[0:3], v[192:195], v[64:67]\n" \
"ds_read_b128 v[216:219], v230 offset:6144\n" \
"v_mfma_f32_16x16x32_bf16 v[64:67], v[4:7], v[196:199], v[64:67]\n" \
"ds_read_b128 v[220:223], v231 offset:6144\n" \
"v_mfma_f32_16x16x32_bf16 v[68:71], v[8:11], v[192:195], v[68:71]\n" \
"ds_read_b128 v[240:243], v254\n" \
"ds_read_b64_tr_b16 v[32:33], v232 offset:1024\n" \
"v_mfma_f32_16x16x32_bf16 v[68:71], v[12:15], v[196:199], v[68:71]\n" \
"buffer_store_dwordx4 a[96:99], v249, s[56:59], s90 offen offset:256 sc1\n" \
"v_and_b32 v238, 0xf0, v248\n" \
"v_mov_b32 v239, 0x3f803f80\n" \
"v_cmp_eq_u32 vcc, 32, v238\n" \
"v_cmp_eq_u32 s[64:65], 48, v238\n" \
"v_mfma_f32_16x16x32_bf16 v[72:75], v[16:19], v[192:195], v[72:75]\n" \
"ds_read_b64_tr_b16 v[34:35], v232 offset:3072\n" \
"v_mov_b32 v244, 0\n" \
"v_mfma_f32_16x16x32_bf16 v[72:75], v[20:23], v[196:199], v[72:75]\n" \
"s_waitcnt lgkmcnt(4)\n" \
"ds_read_b64_tr_b16 v[36:37], v232 offset:9216\n" \
"v_mov_b32 v245, 0\n" \
"v_mfma_f32_16x16x32_bf16 v[76:79], v[24:27], v[192:195], v[76:79]\n" \
"ds_read_b128 a[96:99], v254\n" \
"ds_read_b64_tr_b16 v[38:39], v232 offset:11264\n" \
"v_mfma_f32_16x16x32_bf16 v[76:79], v[28:31], v[196:199], v[76:79]\n" \
"buffer_store_dwordx4 a[72:75], v249, s[56:59], s90 offen offset:2048 sc1\n" \
"ds_read_b128 a[100:103], v254\n" \
"v_cndmask_b32 v238, 0, v239, vcc\n" \
"v_cndmask_b32 v239, 0, v239, s[64:65]\n" \
"v_mfma_f32_16x16x32_bf16 v[108:111], v[24:27], v[200:203], v[108:111]\n" \
"ds_read_b64_tr_b16 v[40:41], v233 offset:1024\n" \
"v_mfma_f32_16x16x32_bf16 v[108:111], v[28:31], v[204:207], v[108:111]\n" \
"ds_read_b64_tr_b16 v[42:43], v233 offset:3072\n" \
"v_mfma_f32_16x16x32_bf16 v[104:107], v[16:19], v[200:203], v[104:107]\n" \
"ds_read_b128 a[72:75], v254\n" \
"ds_read_b64_tr_b16 v[44:45], v233 offset:9216\n" \
"v_mfma_f32_16x16x32_bf16 v[104:107], v[20:23], v[204:207], v[104:107]\n" \
"ds_read_b128 a[76:79], v254\n" \
"v_mfma_f32_16x16x32_bf16 v[100:103], v[8:11], v[200:203], v[100:103]\n" \
"ds_read_b64_tr_b16 v[46:47], v233 offset:11264\n" \
"v_mfma_f32_16x16x32_bf16 v[100:103], v[12:15], v[204:207], v[100:103]\n" \
"ds_read_b64_tr_b16 v[48:49], v232 offset:1536\n" \
"v_mfma_f32_16x16x32_bf16 v[96:99], v[0:3], v[200:203], v[96:99]\n" \
"s_add_u32 m0, s95, 0\n" \
"ds_read_b64_tr_b16 v[50:51], v232 offset:3584\n" \
"v_mfma_f32_16x16x32_bf16 v[96:99], v[4:7], v[204:207], v[96:99]\n" \
"global_load_lds_dwordx4 v250, s[36:37] offset:0 sc1\n" \
"v_mfma_f32_16x16x32_bf16 v[128:131], v[0:3], v[208:211], v[128:131]\n" \
"ds_read_b64_tr_b16 v[52:53], v232 offset:9728\n" \
"v_mfma_f32_16x16x32_bf16 v[128:131], v[4:7], v[212:215], v[128:131]\n" \
"ds_read_b64_tr_b16 v[54:55], v232 offset:11776\n" \
"v_mfma_f32_16x16x32_bf16 v[132:135], v[8:11], v[208:211], v[132:135]\n" \
"ds_read_b64_tr_b16 v[56:57], v233 offset:1536\n" \
"v_mfma_f32_16x16x32_bf16 v[132:135], v[12:15], v[212:215], v[132:135]\n" \
"global_load_lds_dwordx4 v250, s[36:37] offset:1024 sc1\n" \
"v_mfma_f32_16x16x32_bf16 v[136:139], v[16:19], v[208:211], v[136:139]\n" \
"ds_read_b64_tr_b16 v[58:59], v233 offset:3584\n" \
"v_mfma_f32_16x16x32_bf16 v[136:139], v[20:23], v[212:215], v[136:139]\n" \
"ds_read_b64_tr_b16 v[60:61], v233 offset:9728\n" \
"v_mfma_f32_16x16x32_bf16 v[140:143], v[24:27], v[208:211], v[140:143]\n" \
"ds_read_b64_tr_b16 v[62:63], v233 offset:11776\n" \
"v_mfma_f32_16x16x32_bf16 v[140:143], v[28:31], v[212:215], v[140:143]\n" \
"s_waitcnt lgkmcnt(3)\n" \
"global_load_lds_dwordx4 v250, s[36:37] offset:2048 sc1\n" \
"v_mfma_f32_16x16x32_bf16 v[172:175], v[24:27], v[216:219], v[172:175]\n" \
"v_add_u32 v230, 0x8000, v230\n" \
"v_add_u32 v231, 0x8000, v231\n" \
"v_mfma_f32_16x16x32_bf16 v[172:175], v[28:31], v[220:223], v[172:175]\n" \
"v_add_u32 v232, 0x4000, v232\n" \
"v_add_u32 v233, 0x4000, v233\n" \
"v_mfma_f32_16x16x32_bf16 v[168:171], v[16:19], v[216:219], v[168:171]\n" \
"v_mfma_f32_16x16x32_bf16 v[168:171], v[20:23], v[220:223], v[168:171]\n" \
"global_load_lds_dwordx4 v250, s[36:37] offset:3072 sc1\n" \
"v_mfma_f32_16x16x32_bf16 v[164:167], v[8:11], v[216:219], v[164:167]\n" \
"s_add_u32 s36, s36, 17408\n" \
"v_mfma_f32_16x16x32_bf16 v[164:167], v[12:15], v[220:223], v[164:167]\n" \
"v_mfma_f32_16x16x32_bf16 v[160:163], v[0:3], v[216:219], v[160:163]\n" \
"s_waitcnt vmcnt(19) lgkmcnt(0)\n" \
"v_mfma_f32_16x16x32_bf16 v[160:163], v[4:7], v[220:223], v[160:163]\n" \
"s_barrier\n" \
"v_mfma_f32_16x16x32_bf16 v[80:83], v[32:35], v[192:195], v[80:83]\n" \
"ds_read_b64_tr_b16 v[0:1], v232 offset:0\n" \
"v_mfma_f32_16x16x32_bf16 v[80:83], v[36:39], v[196:199], v[80:83]\n" \
"ds_read_b64_tr_b16 v[2:3], v232 offset:2048\n" \
"v_mfma_f32_16x16x32_bf16 v[84:87], v[40:43], v[192:195], v[84:87]\n" \
"s_add_u32 m0, s95, 16384\n" \
"ds_read_b64_tr_b16 v[4:5], v232 offset:8192\n" \
"v_mfma_f32_16x16x32_bf16 v[84:87], v[44:47], v[196:199], v[84:87]\n" \
"global_load_lds_dwordx4 v250, s[36:37] offset:0 sc1\n" \
"v_mfma_f32_16x16x32_bf16 v[88:91], v[48:51], v[192:195], v[88:91]\n" \
"ds_read_b64_tr_b16 v[6:7], v232 offset:10240\n" \
"v_mfma_f32_16x16x32_bf16 v[88:91], v[52:55], v[196:199], v[88:91]\n" \
"ds_read_b64_tr_b16 v[8:9], v233 offset:0\n" \
"v_mfma_f32_16x16x32_bf16 v[92:95], v[56:59], v[192:195], v[92:95]\n" \
"ds_read_b64_tr_b16 v[10:11], v233 offset:2048\n" \
"v_mfma_f32_16x16x32_bf16 v[92:95], v[60:63], v[196:199], v[92:95]\n" \
"global_load_lds_dwordx4 v250, s[36:37] offset:1024 sc1\n" \
"v_mfma_f32_16x16x32_bf16 v[124:127], v[56:59], v[200:203], v[124:127]\n" \
"ds_read_b128 v[192:195], v230 offset:0\n" \
"v_mfma_f32_16x16x32_bf16 v[124:127], v[60:63], v[204:207], v[124:127]\n" \
"ds_read_b128 v[196:199], v231 offset:0\n" \
"v_mfma_f32_16x16x32_bf16 v[120:123], v[48:51], v[200:203], v[120:123]\n" \
"ds_read_b64_tr_b16 v[12:13], v233 offset:8192\n" \
"v_mfma_f32_16x16x32_bf16 v[120:123], v[52:55], v[204:207], v[120:123]\n" \
"global_load_lds_dwordx4 v250, s[36:37] offset:2048 sc1\n" \
"v_mfma_f32_16x16x32_bf16 v[116:119], v[40:43], v[200:203], v[116:119]\n" \
"ds_read_b64_tr_b16 v[14:15], v233 offset:10240\n" \
"v_mfma_f32_16x16x32_bf16 v[116:119], v[44:47], v[204:207], v[116:119]\n" \
"ds_read_b64_tr_b16 v[16:17], v232 offset:512\n" \
"v_mfma_f32_16x16x32_bf16 v[112:115], v[32:35], v[200:203], v[112:115]\n" \
"ds_read_b64_tr_b16 v[18:19], v232 offset:2560\n" \
"v_mfma_f32_16x16x32_bf16 v[112:115], v[36:39], v[204:207], v[112:115]\n" \
"global_load_lds_dwordx4 v250, s[36:37] offset:3072 sc1\n" \
"v_mfma_f32_16x16x32_bf16 v[144:147], v[32:35], v[208:211], v[144:147]\n" \
"ds_read_b128 v[200:203], v230 offset:2048\n" \
"s_add_u32 s36, s36, 17408\n" \
"v_mfma_f32_16x16x32_bf16 v[144:147], v[36:39], v[212:215], v[144:147]\n" \
"ds_read_b128 v[204:207], v231 offset:2048\n" \
"v_mfma_f32_16x16x32_bf16 v[148:151], v[40:43], v[208:211], v[148:151]\n" \
"ds_read_b64_tr_b16 v[20:21], v232 offset:8704\n" \
"v_mfma_f32_16x16x32_bf16 v[148:151], v[44:47], v[212:215], v[148:151]\n" \
"buffer_store_dwordx4 a[104:107], v249, s[56:59], s90 offen offset:2304 sc1\n" \
"v_mfma_f32_16x16x32_bf16 v[152:155], v[48:51], v[208:211], v[152:155]\n" \
"s_add_u32 s90, s89, 278528\n" \
"ds_read_b64_tr_b16 v[22:23], v232 offset:10752\n" \
"v_mfma_f32_16x16x32_bf16 v[152:155], v[52:55], v[212:215], v[152:155]\n" \
"ds_read_b64_tr_b16 v[24:25], v233 offset:512\n" \
"v_mfma_f32_16x16x32_bf16 v[156:159], v[56:59], v[208:211], v[156:159]\n" \
"ds_read_b128 a[104:107], v254\n" \
"ds_read_b64_tr_b16 v[26:27], v233 offset:2560\n" \
"v_mfma_f32_16x16x32_bf16 v[156:159], v[60:63], v[212:215], v[156:159]\n" \
"buffer_store_dwordx4 a[80:83], v248, s[56:59], s90 offen offset:0 sc1\n" \
"ds_read_b128 a[108:111], v254\n" \
"v_mfma_f32_16x16x32_bf16 v[188:191], v[56:59], v[216:219], v[188:191]\n" \
"ds_read_b128 v[208:211], v230 offset:4096\n" \
"v_mfma_f32_16x16x32_bf16 v[188:191], v[60:63], v[220:223], v[188:191]\n" \
"ds_read_b128 v[212:215], v231 offset:4096\n" \
"v_mfma_f32_16x16x32_bf16 v[184:187], v[48:51], v[216:219], v[184:187]\n" \
"ds_read_b128 a[80:83], v254\n" \
"ds_read_b64_tr_b16 v[28:29], v233 offset:8704\n" \
"v_mfma_f32_16x16x32_bf16 v[184:187], v[52:55], v[220:223], v[184:187]\n" \
"buffer_store_dwordx4 a[84:87], v248, s[56:59], s90 offen offset:1024 sc1\n" \
"ds_read_b128 a[112:115], v254\n" \
"v_mfma_f32_16x16x32_bf16 v[180:183], v[40:43], v[216:219], v[180:183]\n" \
"ds_read_b64_tr_b16 v[30:31], v233 offset:10752\n" \
"v_mfma_f32_16x16x32_bf16 v[180:183], v[44:47], v[220:223], v[180:183]\n" \
"v_mfma_f32_16x16x32_bf16 v[176:179], v[32:35], v[216:219], v[176:179]\n" \
"ds_read_b128 a[84:87], v254\n" \
"v_mfma_f32_16x16x32_bf16 v[176:179], v[36:39], v[220:223], v[176:179]\n" \
"s_waitcnt lgkmcnt(3)\n" \
"ds_read_b128 a[116:119], v254\n" \
"v_mfma_f32_16x16x32_bf16 v[64:67], v[0:3], v[192:195], v[64:67]\n" \
"ds_read_b128 v[216:219], v230 offset:6144\n" \
"v_mfma_f32_16x16x32_bf16 v[64:67], v[4:7], v[196:199], v[64:67]\n" \
"ds_read_b128 v[220:223], v231 offset:6144\n" \
"v_mfma_f32_16x16x32_bf16 v[68:71], v[8:11], v[192:195], v[68:71]\n" \
"ds_read_b64_tr_b16 v[32:33], v232 offset:1024\n" \
"v_mfma_f32_16x16x32_bf16 v[68:71], v[12:15], v[196:199], v[68:71]\n" \
"v_mfma_f32_16x16x32_bf16 v[72:75], v[16:19], v[192:195], v[72:75]\n" \
"ds_read_b64_tr_b16 v[34:35], v232 offset:3072\n" \
"v_mfma_f32_16x16x32_bf16 v[72:75], v[20:23], v[196:199], v[72:75]\n" \
"s_waitcnt lgkmcnt(3)\n" \
"ds_read_b64_tr_b16 v[36:37], v232 offset:9216\n" \
"v_mfma_f32_16x16x32_bf16 v[76:79], v[24:27], v[192:195], v[76:79]\n" \
"ds_read_b64_tr_b16 v[38:39], v232 offset:11264\n" \
"v_mfma_f32_16x16x32_bf16 v[76:79], v[28:31], v[196:199], v[76:79]\n" \
"v_mfma_f32_16x16x32_bf16 v[108:111], v[24:27], v[200:203], v[108:111]\n" \
"ds_read_b64_tr_b16 v[40:41], v233 offset:1024\n" \
"v_mfma_f32_16x16x32_bf16 v[108:111], v[28:31], v[204:207], v[108:111]\n" \
"ds_read_b64_tr_b16 v[42:43], v233 offset:3072\n" \
"v_mfma_f32_16x16x32_bf16 v[104:107], v[16:19], v[200:203], v[104:107]\n" \
"ds_read_b64_tr_b16 v[44:45], v233 offset:9216\n" \
"v_mfma_f32_16x16x32_bf16 v[104:107], v[20:23], v[204:207], v[104:107]\n" \
"v_mfma_f32_16x16x32_bf16 v[100:103], v[8:11], v[200:203], v[100:103]\n" \
"ds_read_b64_tr_b16 v[46:47], v233 offset:11264\n" \
"v_mfma_f32_16x16x32_bf16 v[100:103], v[12:15], v[204:207], v[100:103]\n" \
"ds_read_b64_tr_b16 v[48:49], v232 offset:1536\n" \
"v_mfma_f32_16x16x32_bf16 v[96:99], v[0:3], v[200:203], v[96:99]\n" \
"ds_read_b64_tr_b16 v[50:51], v232 offset:3584\n" \
"v_mfma_f32_16x16x32_bf16 v[96:99], v[4:7], v[204:207], v[96:99]\n" \
"v_mfma_f32_16x16x32_bf16 v[128:131], v[0:3], v[208:211], v[128:131]\n" \
"ds_read_b64_tr_b16 v[52:53], v232 offset:9728\n" \
"v_mfma_f32_16x16x32_bf16 v[128:131], v[4:7], v[212:215], v[128:131]\n" \
"ds_read_b64_tr_b16 v[54:55], v232 offset:11776\n" \
"v_mfma_f32_16x16x32_bf16 v[132:135], v[8:11], v[208:211], v[132:135]\n" \
"ds_read_b64_tr_b16 v[56:57], v233 offset:1536\n" \
"v_mfma_f32_16x16x32_bf16 v[132:135], v[12:15], v[212:215], v[132:135]\n" \
"v_mfma_f32_16x16x32_bf16 v[136:139], v[16:19], v[208:211], v[136:139]\n" \
"ds_read_b64_tr_b16 v[58:59], v233 offset:3584\n" \
"v_mfma_f32_16x16x32_bf16 v[136:139], v[20:23], v[212:215], v[136:139]\n" \
"ds_read_b64_tr_b16 v[60:61], v233 offset:9728\n" \
"v_mfma_f32_16x16x32_bf16 v[140:143], v[24:27], v[208:211], v[140:143]\n" \
"ds_read_b64_tr_b16 v[62:63], v233 offset:11776\n" \
"v_mfma_f32_16x16x32_bf16 v[140:143], v[28:31], v[212:215], v[140:143]\n" \
"s_waitcnt lgkmcnt(3)\n" \
"v_mfma_f32_16x16x32_bf16 v[172:175], v[24:27], v[216:219], v[172:175]\n" \
"v_add_u32 v230, 0x8000, v230\n" \
"v_add_u32 v231, 0x8000, v231\n" \
"v_mfma_f32_16x16x32_bf16 v[172:175], v[28:31], v[220:223], v[172:175]\n" \
"v_add_u32 v232, 0x4000, v232\n" \
"v_add_u32 v233, 0x4000, v233\n" \
"v_mfma_f32_16x16x32_bf16 v[168:171], v[16:19], v[216:219], v[168:171]\n" \
"v_mfma_f32_16x16x32_bf16 v[168:171], v[20:23], v[220:223], v[168:171]\n" \
"v_mfma_f32_16x16x32_bf16 v[164:167], v[8:11], v[216:219], v[164:167]\n" \
"v_mfma_f32_16x16x32_bf16 v[164:167], v[12:15], v[220:223], v[164:167]\n" \
"v_mfma_f32_16x16x32_bf16 v[160:163], v[0:3], v[216:219], v[160:163]\n" \
"s_waitcnt vmcnt(13) lgkmcnt(0)\n" \
"v_mfma_f32_16x16x32_bf16 v[160:163], v[4:7], v[220:223], v[160:163]\n" \
"s_barrier\n" \
"v_mfma_f32_16x16x32_bf16 v[80:83], v[32:35], v[192:195], v[80:83]\n" \
"ds_read_b64_tr_b16 v[0:1], v232 offset:0\n" \
"v_mfma_f32_16x16x32_bf16 v[80:83], v[36:39], v[196:199], v[80:83]\n" \
"ds_read_b64_tr_b16 v[2:3], v232 offset:2048\n" \
"v_mfma_f32_16x16x32_bf16 v[84:87], v[40:43], v[192:195], v[84:87]\n" \
"s_add_u32 m0, s95, 32768\n" \
"ds_read_b64_tr_b16 v[4:5], v232 offset:8192\n" \
"v_mfma_f32_16x16x32_bf16 v[84:87], v[44:47], v[196:199], v[84:87]\n" \
"global_load_lds_dwordx4 v250, s[36:37] offset:0 sc1\n" \
"v_mfma_f32_16x16x32_bf16 v[88:91], v[48:51], v[192:195], v[88:91]\n" \
"ds_read_b64_tr_b16 v[6:7], v232 offset:10240\n" \
"v_mfma_f32_16x16x32_bf16 v[88:91], v[52:55], v[196:199], v[88:91]\n" \
"ds_read_b64_tr_b16 v[8:9], v233 offset:0\n" \
"v_mfma_f32_16x16x32_bf16 v[92:95], v[56:59], v[192:195], v[92:95]\n" \
"ds_read_b64_tr_b16 v[10:11], v233 offset:2048\n" \
"v_mfma_f32_16x16x32_bf16 v[92:95], v[60:63], v[196:199], v[92:95]\n" \
"global_load_lds_dwordx4 v250, s[36:37] offset:1024 sc1\n" \
"v_mfma_f32_16x16x32_bf16 v[124:127], v[56:59], v[200:203], v[124:127]\n" \
"ds_read_b128 v[192:195], v230 offset:0\n" \
"v_mfma_f32_16x16x32_bf16 v[124:127], v[60:63], v[204:207], v[124:127]\n" \
"ds_read_b128 v[196:199], v231 offset:0\n" \
"v_mfma_f32_16x16x32_bf16 v[120:123], v[48:51], v[200:203], v[120:123]\n" \
"ds_read_b64_tr_b16 v[12:13], v233 offset:8192\n" \
"v_mfma_f32_16x16x32_bf16 v[120:123], v[52:55], v[204:207], v[120:123]\n" \
"global_load_lds_dwordx4 v250, s[36:37] offset:2048 sc1\n" \
"v_mfma_f32_16x16x32_bf16 v[116:119], v[40:43], v[200:203], v[116:119]\n" \
"ds_read_b64_tr_b16 v[14:15], v233 offset:10240\n" \
"v_mfma_f32_16x16x32_bf16 v[116:119], v[44:47], v[204:207], v[116:119]\n" \
"ds_read_b64_tr_b16 v[16:17], v232 offset:512\n" \
"v_mfma_f32_16x16x32_bf16 v[112:115], v[32:35], v[200:203], v[112:115]\n" \
"ds_read_b64_tr_b16 v[18:19], v232 offset:2560\n" \
"v_mfma_f32_16x16x32_bf16 v[112:115], v[36:39], v[204:207], v[112:115]\n" \
"global_load_lds_dwordx4 v250, s[36:37] offset:3072 sc1\n" \
"v_mfma_f32_16x16x32_bf16 v[144:147], v[32:35], v[208:211], v[144:147]\n" \
"ds_read_b128 v[200:203], v230 offset:2048\n" \
"s_add_u32 s36, s36, 17408\n" \
"v_mfma_f32_16x16x32_bf16 v[144:147], v[36:39], v[212:215], v[144:147]\n" \
"ds_read_b128 v[204:207], v231 offset:2048\n" \
"v_mfma_f32_16x16x32_bf16 v[148:151], v[40:43], v[208:211], v[148:151]\n" \
"ds_read_b64_tr_b16 v[20:21], v232 offset:8704\n" \
"v_mfma_f32_16x16x32_bf16 v[148:151], v[44:47], v[212:215], v[148:151]\n" \
"buffer_store_dwordx4 a[88:91], v248, s[56:59], s90 offen offset:2048 sc1\n" \
"v_mfma_f32_16x16x32_bf16 v[152:155], v[48:51], v[208:211], v[152:155]\n" \
"ds_read_b64_tr_b16 v[22:23], v232 offset:10752\n" \
"v_mfma_f32_16x16x32_bf16 v[152:155], v[52:55], v[212:215], v[152:155]\n" \
"ds_read_b64_tr_b16 v[24:25], v233 offset:512\n" \
"v_mfma_f32_16x16x32_bf16 v[156:159], v[56:59], v[208:211], v[156:159]\n" \
"ds_read_b128 a[88:91], v254\n" \
"ds_read_b64_tr_b16 v[26:27], v233 offset:2560\n" \
"v_mfma_f32_16x16x32_bf16 v[156:159], v[60:63], v[212:215], v[156:159]\n" \
"buffer_store_dwordx4 a[92:95], v248, s[56:59], s90 offen offset:3072 sc1\n" \
"v_mfma_f32_16x16x32_bf16 v[188:191], v[56:59], v[216:219], v[188:191]\n" \
"ds_read_b128 v[208:211], v230 offset:4096\n" \
"s_add_u32 s90, s88, 417792\n" \
"v_mfma_f32_16x16x32_bf16 v[188:191], v[60:63], v[220:223], v[188:191]\n" \
"ds_read_b128 v[212:215], v231 offset:4096\n" \
"v_mfma_f32_16x16x32_bf16 v[184:187], v[48:51], v[216:219], v[184:187]\n" \
"ds_read_b128 a[92:95], v254\n" \
"ds_read_b64_tr_b16 v[28:29], v233 offset:8704\n" \
"v_mfma_f32_16x16x32_bf16 v[184:187], v[52:55], v[220:223], v[184:187]\n" \
"buffer_store_dwordx4 a[0:3], v249, s[56:59], s90 offen offset:0 sc1\n" \
"v_accvgpr_write_b32 a120, v238\n" \
"v_accvgpr_write_b32 a121, v238\n" \
"v_accvgpr_write_b32 a122, v238\n" \
"v_accvgpr_write_b32 a123, v238\n" \
"v_mfma_f32_16x16x32_bf16 v[180:183], v[40:43], v[216:219], v[180:183]\n" \
"ds_read_b64_tr_b16 v[30:31], v233 offset:10752\n" \
"v_accvgpr_write_b32 a124, v239\n" \
"v_mfma_f32_16x16x32_bf16 v[180:183], v[44:47], v[220:223], v[180:183]\n" \
"v_accvgpr_write_b32 a125, v239\n" \
"v_accvgpr_write_b32 a126, v239\n" \
"v_mfma_f32_16x16x32_bf16 v[176:179], v[32:35], v[216:219], v[176:179]\n" \
"ds_read_b128 a[0:3], v254\n" \
"v_accvgpr_write_b32 a127, v239\n" \
"v_mfma_f32_16x16x32_bf16 v[176:179], v[36:39], v[220:223], v[176:179]\n" \
"s_waitcnt lgkmcnt(2)\n" \
"ds_read_b128 a[4:7], v254\n" \
"v_mfma_f32_16x16x32_bf16 v[64:67], v[0:3], v[192:195], v[64:67]\n" \
"ds_read_b128 v[216:219], v230 offset:6144\n" \
"v_mfma_f32_16x16x32_bf16 v[64:67], v[4:7], v[196:199], v[64:67]\n" \
"ds_read_b128 v[220:223], v231 offset:6144\n" \
"v_mfma_f32_16x16x32_bf16 v[68:71], v[8:11], v[192:195], v[68:71]\n" \
"ds_read_b64_tr_b16 v[32:33], v232 offset:1024\n" \
"v_mfma_f32_16x16x32_bf16 v[68:71], v[12:15], v[196:199], v[68:71]\n" \
"v_mfma_f32_16x16x32_bf16 v[72:75], v[16:19], v[192:195], v[72:75]\n" \
"ds_read_b64_tr_b16 v[34:35], v232 offset:3072\n" \
"v_mfma_f32_16x16x32_bf16 v[72:75], v[20:23], v[196:199], v[72:75]\n" \
"s_waitcnt lgkmcnt(3)\n" \
"ds_read_b64_tr_b16 v[36:37], v232 offset:9216\n" \
"v_mfma_f32_16x16x32_bf16 v[76:79], v[24:27], v[192:195], v[76:79]\n" \
"ds_read_b64_tr_b16 v[38:39], v232 offset:11264\n" \
"v_cvt_pk_bf16_f32 v64, v64, v65\n" \
"v_mfma_f32_16x16x32_bf16 v[76:79], v[28:31], v[196:199], v[76:79]\n" \
"v_cvt_pk_bf16_f32 v65, v66, v67\n" \
"v_cvt_pk_bf16_f32 v66, v68, v69\n" \
"v_cvt_pk_bf16_f32 v67, v70, v71\n" \
"v_accvgpr_write_b32 a128, v64\n" \
"v_accvgpr_write_b32 a129, v65\n" \
"v_mfma_f32_16x16x32_bf16 v[96:99], v[0:3], v[200:203], v[96:99]\n" \
"ds_read_b64_tr_b16 v[40:41], v233 offset:1024\n" \
"v_accvgpr_write_b32 a130, v66\n" \
"v_mfma_f32_16x16x32_bf16 v[96:99], v[4:7], v[204:207], v[96:99]\n" \
"ds_read_b64_tr_b16 v[42:43], v233 offset:3072\n" \
"v_accvgpr_write_b32 a131, v67\n" \
"v_mfma_f32_16x16x32_bf16 v[100:103], v[8:11], v[200:203], v[100:103]\n" \
"ds_read_b64_tr_b16 v[44:45], v233 offset:9216\n" \
"v_cvt_pk_bf16_f32 v72, v72, v73\n" \
"v_mfma_f32_16x16x32_bf16 v[100:103], v[12:15], v[204:207], v[100:103]\n" \
"v_cvt_pk_bf16_f32 v73, v74, v75\n" \
"v_cvt_pk_bf16_f32 v74, v76, v77\n" \
"v_cvt_pk_bf16_f32 v75, v78, v79\n" \
"v_accvgpr_write_b32 a132, v72\n" \
"v_accvgpr_write_b32 a133, v73\n" \
"v_mfma_f32_16x16x32_bf16 v[104:107], v[16:19], v[200:203], v[104:107]\n" \
"ds_read_b64_tr_b16 v[46:47], v233 offset:11264\n" \
"v_accvgpr_write_b32 a134, v74\n" \
"v_mfma_f32_16x16x32_bf16 v[104:107], v[20:23], v[204:207], v[104:107]\n" \
"ds_read_b64_tr_b16 v[48:49], v232 offset:1536\n" \
"v_accvgpr_write_b32 a135, v75\n" \
"v_mfma_f32_16x16x32_bf16 v[108:111], v[24:27], v[200:203], v[108:111]\n" \
"ds_read_b64_tr_b16 v[50:51], v232 offset:3584\n" \
"v_cvt_pk_bf16_f32 v96, v96, v97\n" \
"v_mfma_f32_16x16x32_bf16 v[108:111], v[28:31], v[204:207], v[108:111]\n" \
"v_cvt_pk_bf16_f32 v97, v98, v99\n" \
"v_cvt_pk_bf16_f32 v98, v100, v101\n" \
"v_cvt_pk_bf16_f32 v99, v102, v103\n" \
"v_accvgpr_write_b32 a144, v96\n" \
"v_accvgpr_write_b32 a145, v97\n" \
"v_mfma_f32_16x16x32_bf16 v[128:131], v[0:3], v[208:211], v[128:131]\n" \
"ds_read_b64_tr_b16 v[52:53], v232 offset:9728\n" \
"v_accvgpr_write_b32 a146, v98\n" \
"v_mfma_f32_16x16x32_bf16 v[128:131], v[4:7], v[212:215], v[128:131]\n" \
"ds_read_b64_tr_b16 v[54:55], v232 offset:11776\n" \
"v_accvgpr_write_b32 a147, v99\n" \
"v_mfma_f32_16x16x32_bf16 v[132:135], v[8:11], v[208:211], v[132:135]\n" \
"ds_read_b64_tr_b16 v[56:57], v233 offset:1536\n" \
"v_cvt_pk_bf16_f32 v104, v104, v105\n" \
"v_mfma_f32_16x16x32_bf16 v[132:135], v[12:15], v[212:215], v[132:135]\n" \
"v_cvt_pk_bf16_f32 v105, v106, v107\n" \
"v_cvt_pk_bf16_f32 v106, v108, v109\n" \
"v_cvt_pk_bf16_f32 v107, v110, v111\n" \
"v_accvgpr_write_b32 a148, v104\n" \
"v_accvgpr_write_b32 a149, v105\n" \
"v_mfma_f32_16x16x32_bf16 v[136:139], v[16:19], v[208:211], v[136:139]\n" \
"ds_read_b64_tr_b16 v[58:59], v233 offset:3584\n" \
"v_accvgpr_write_b32 a150, v106\n" \
"v_mfma_f32_16x16x32_bf16 v[136:139], v[20:23], v[212:215], v[136:139]\n" \
"ds_read_b64_tr_b16 v[60:61], v233 offset:9728\n" \
"v_accvgpr_write_b32 a151, v107\n" \
"v_mfma_f32_16x16x32_bf16 v[140:143], v[24:27], v[208:211], v[140:143]\n" \
"ds_read_b64_tr_b16 v[62:63], v233 offset:11776\n" \
"v_cvt_pk_bf16_f32 v128, v128, v129\n" \
"v_mfma_f32_16x16x32_bf16 v[140:143], v[28:31], v[212:215], v[140:143]\n" \
"s_waitcnt lgkmcnt(3)\n" \
"v_cvt_pk_bf16_f32 v129, v130, v131\n" \
"v_cvt_pk_bf16_f32 v130, v132, v133\n" \
"v_cvt_pk_bf16_f32 v131, v134, v135\n" \
"Lpg4936_%=:\n" \
"s_cbranch_execz Lpg6171_%=\n" \
"v_mfma_f32_16x16x32_bf16 v[160:163], v[0:3], v[216:219], v[160:163]\n" \
"v_accvgpr_write_b32 a160, v128\n" \
"v_accvgpr_write_b32 a161, v129\n" \
"v_mfma_f32_16x16x32_bf16 v[160:163], v[4:7], v[220:223], v[160:163]\n" \
"v_accvgpr_write_b32 a162, v130\n" \
"v_accvgpr_write_b32 a163, v131\n" \
"v_mfma_f32_16x16x32_bf16 v[164:167], v[8:11], v[216:219], v[164:167]\n" \
"v_cvt_pk_bf16_f32 v136, v136, v137\n" \
"v_cvt_pk_bf16_f32 v137, v138, v139\n" \
"v_mfma_f32_16x16x32_bf16 v[164:167], v[12:15], v[220:223], v[164:167]\n" \
"v_cvt_pk_bf16_f32 v138, v140, v141\n" \
"v_cvt_pk_bf16_f32 v139, v142, v143\n" \
"v_mfma_f32_16x16x32_bf16 v[168:171], v[16:19], v[216:219], v[168:171]\n" \
"v_accvgpr_write_b32 a164, v136\n" \
"v_accvgpr_write_b32 a165, v137\n" \
"v_mfma_f32_16x16x32_bf16 v[168:171], v[20:23], v[220:223], v[168:171]\n" \
"v_accvgpr_write_b32 a166, v138\n" \
"v_accvgpr_write_b32 a167, v139\n" \
"v_mfma_f32_16x16x32_bf16 v[172:175], v[24:27], v[216:219], v[172:175]\n" \
"s_waitcnt vmcnt(10) lgkmcnt(0)\n" \
"v_cvt_pk_bf16_f32 v160, v160, v161\n" \
"v_cvt_pk_bf16_f32 v161, v162, v163\n" \
"v_mfma_f32_16x16x32_bf16 v[172:175], v[28:31], v[220:223], v[172:175]\n" \
"s_barrier\n" \
"v_cvt_pk_bf16_f32 v162, v164, v165\n" \
"v_cvt_pk_bf16_f32 v163, v166, v167\n" \
"v_accvgpr_write_b32 a176, v160\n" \
"v_accvgpr_write_b32 a177, v161\n" \
"v_mfma_f32_16x16x32_bf16 v[80:83], v[32:35], v[192:195], v[80:83]\n" \
"s_mov_b32 m0, s74\n" \
"v_accvgpr_write_b32 a178, v162\n" \
"v_accvgpr_write_b32 a179, v163\n" \
"v_mfma_f32_16x16x32_bf16 v[80:83], v[36:39], v[196:199], v[80:83]\n" \
"ds_read_b128 a[192:195], v249 offset:0\n" \
"global_load_lds_dwordx4 v250, s[38:39] offset:0 sc1\n" \
"v_mfma_f32_16x16x32_bf16 v[84:87], v[40:43], v[192:195], v[84:87]\n" \
"ds_read_b128 v[0:3], v254\n" \
"v_cvt_pk_bf16_f32 v168, v168, v169\n" \
"v_mfma_f32_16x16x32_bf16 v[84:87], v[44:47], v[196:199], v[84:87]\n" \
"ds_read_b128 a[196:199], v249 offset:2048\n" \
"global_load_lds_dwordx4 v250, s[38:39] offset:1024 sc1\n" \
"v_cvt_pk_bf16_f32 v169, v170, v171\n" \
"v_cvt_pk_bf16_f32 v170, v172, v173\n" \
"v_cvt_pk_bf16_f32 v171, v174, v175\n" \
"v_accvgpr_write_b32 a180, v168\n" \
"v_accvgpr_write_b32 a181, v169\n" \
"v_accvgpr_write_b32 a182, v170\n" \
"v_mfma_f32_16x16x32_bf16 v[88:91], v[48:51], v[192:195], v[88:91]\n" \
"ds_read_b128 v[4:7], v254\n" \
"v_accvgpr_write_b32 a183, v171\n" \
"v_mfma_f32_16x16x32_bf16 v[88:91], v[52:55], v[196:199], v[88:91]\n" \
"ds_read_b128 a[200:203], v249 offset:4096\n" \
"global_load_lds_dwordx4 v250, s[38:39] offset:2048 sc1\n" \
"v_mfma_f32_16x16x32_bf16 v[92:95], v[56:59], v[192:195], v[92:95]\n" \
"v_cvt_pk_bf16_f32 v80, v80, v81\n" \
"v_cvt_pk_bf16_f32 v81, v82, v83\n" \
"v_mfma_f32_16x16x32_bf16 v[92:95], v[60:63], v[196:199], v[92:95]\n" \
"ds_read_b128 a[204:207], v249 offset:6144\n" \
"global_load_lds_dwordx4 v250, s[38:39] offset:3072 sc1\n" \
"v_cvt_pk_bf16_f32 v82, v84, v85\n" \
"v_cvt_pk_bf16_f32 v83, v86, v87\n" \
"v_accvgpr_write_b32 a136, v80\n" \
"v_accvgpr_write_b32 a137, v81\n" \
"v_accvgpr_write_b32 a138, v82\n" \
"v_mfma_f32_16x16x32_bf16 v[112:115], v[32:35], v[200:203], v[112:115]\n" \
"s_add_u32 s38, s38, 17408\n" \
"v_accvgpr_write_b32 a139, v83\n" \
"s_add_u32 m0, m0, 0x4000\n" \
"v_mfma_f32_16x16x32_bf16 v[112:115], v[36:39], v[204:207], v[112:115]\n" \
"ds_read_b128 a[208:211], v249 offset:256\n" \
"global_load_lds_dwordx4 v250, s[38:39] offset:0 sc1\n" \
"v_mfma_f32_16x16x32_bf16 v[116:119], v[40:43], v[200:203], v[116:119]\n" \
"v_cvt_pk_bf16_f32 v88, v88, v89\n" \
"v_cvt_pk_bf16_f32 v89, v90, v91\n" \
"v_mfma_f32_16x16x32_bf16 v[116:119], v[44:47], v[204:207], v[116:119]\n" \
"ds_read_b128 a[212:215], v249 offset:2304\n" \
"global_load_lds_dwordx4 v250, s[38:39] offset:1024 sc1\n" \
"v_cvt_pk_bf16_f32 v90, v92, v93\n" \
"v_cvt_pk_bf16_f32 v91, v94, v95\n" \
"v_accvgpr_write_b32 a140, v88\n" \
"v_accvgpr_write_b32 a141, v89\n" \
"v_mfma_f32_16x16x32_bf16 v[120:123], v[48:51], v[200:203], v[120:123]\n" \
"v_accvgpr_write_b32 a142, v90\n" \
"v_accvgpr_write_b32 a143, v91\n" \
"v_mfma_f32_16x16x32_bf16 v[120:123], v[52:55], v[204:207], v[120:123]\n" \
"ds_read_b128 a[216:219], v249 offset:4352\n" \
"global_load_lds_dwordx4 v250, s[38:39] offset:2048 sc1\n" \
"v_mfma_f32_16x16x32_bf16 v[124:127], v[56:59], v[200:203], v[124:127]\n" \
"v_cvt_pk_bf16_f32 v112, v112, v113\n" \
"v_cvt_pk_bf16_f32 v113, v114, v115\n" \
"v_mfma_f32_16x16x32_bf16 v[124:127], v[60:63], v[204:207], v[124:127]\n" \
"ds_read_b128 a[220:223], v249 offset:6400\n" \
"global_load_lds_dwordx4 v250, s[38:39] offset:3072 sc1\n" \
"v_cvt_pk_bf16_f32 v114, v116, v117\n" \
"v_cvt_pk_bf16_f32 v115, v118, v119\n" \
"v_mfma_f32_16x16x32_bf16 v[144:147], v[32:35], v[208:211], v[144:147]\n" \
"s_add_u32 s38, s38, 17408\n" \
"v_accvgpr_write_b32 a152, v112\n" \
"s_xor_b32 s74, s74, 0x8000\n" \
"v_mfma_f32_16x16x32_bf16 v[144:147], v[36:39], v[212:215], v[144:147]\n" \
"ds_read_b128 a[224:227], v249 offset:8192\n" \
"v_accvgpr_write_b32 a153, v113\n" \
"v_mfma_f32_16x16x32_bf16 v[148:151], v[40:43], v[208:211], v[148:151]\n" \
"v_accvgpr_write_b32 a154, v114\n" \
"v_accvgpr_write_b32 a155, v115\n" \
"v_mfma_f32_16x16x32_bf16 v[148:151], v[44:47], v[212:215], v[148:151]\n" \
"buffer_store_dwordx4 a[32:35], v249, s[56:59], s90 offen offset:256 sc1\n" \
"ds_read_b128 a[228:231], v249 offset:10240\n" \
"v_cvt_pk_bf16_f32 v120, v120, v121\n" \
"v_cvt_pk_bf16_f32 v121, v122, v123\n" \
"v_cvt_pk_bf16_f32 v122, v124, v125\n" \
"v_mfma_f32_16x16x32_bf16 v[152:155], v[48:51], v[208:211], v[152:155]\n" \
"v_cvt_pk_bf16_f32 v123, v126, v127\n" \
"v_accvgpr_write_b32 a156, v120\n" \
"v_mfma_f32_16x16x32_bf16 v[152:155], v[52:55], v[212:215], v[152:155]\n" \
"ds_read_b128 a[232:235], v249 offset:12288\n" \
"v_accvgpr_write_b32 a157, v121\n" \
"v_mfma_f32_16x16x32_bf16 v[156:159], v[56:59], v[208:211], v[156:159]\n" \
"ds_read_b128 a[32:35], v254\n" \
"v_accvgpr_write_b32 a158, v122\n" \
"v_mfma_f32_16x16x32_bf16 v[156:159], v[60:63], v[212:215], v[156:159]\n" \
"ds_read_b128 a[236:239], v249 offset:14336\n" \
"v_accvgpr_write_b32 a159, v123\n" \
"v_mfma_f32_16x16x32_bf16 v[176:179], v[32:35], v[216:219], v[176:179]\n" \
"ds_read_b128 a[240:243], v249 offset:8448\n" \
"v_cvt_pk_bf16_f32 v144, v144, v145\n" \
"v_mfma_f32_16x16x32_bf16 v[176:179], v[36:39], v[220:223], v[176:179]\n" \
"ds_read_b128 a[244:247], v249 offset:10496\n" \
"v_cvt_pk_bf16_f32 v145, v146, v147\n" \
"v_mfma_f32_16x16x32_bf16 v[180:183], v[40:43], v[216:219], v[180:183]\n" \
"ds_read_b128 a[248:251], v249 offset:12544\n" \
"v_cvt_pk_bf16_f32 v146, v148, v149\n" \
"v_mfma_f32_16x16x32_bf16 v[180:183], v[44:47], v[220:223], v[180:183]\n" \
"ds_read_b128 a[252:255], v249 offset:14592\n" \
"v_cvt_pk_bf16_f32 v147, v150, v151\n" \
"v_mfma_f32_16x16x32_bf16 v[184:187], v[48:51], v[216:219], v[184:187]\n" \
"ds_read_b128 a[36:39], v254\n" \
"v_accvgpr_write_b32 a168, v144\n" \
"v_mfma_f32_16x16x32_bf16 v[184:187], v[52:55], v[220:223], v[184:187]\n" \
"ds_read_b128 v[32:35], v254\n" \
"v_accvgpr_write_b32 a169, v145\n" \
"v_mfma_f32_16x16x32_bf16 v[188:191], v[56:59], v[216:219], v[188:191]\n" \
"ds_read_b128 v[36:39], v254\n" \
"v_accvgpr_write_b32 a170, v146\n" \
"v_mfma_f32_16x16x32_bf16 v[188:191], v[60:63], v[220:223], v[188:191]\n" \
"s_waitcnt lgkmcnt(4)\n" \
"buffer_store_dwordx4 a[8:11], v249, s[56:59], s90 offen offset:2048 sc1\n" \
"v_accvgpr_write_b32 a171, v147\n" \
"s_cmp_eq_u32 s63, 0\n" \
"s_cbranch_scc1 Lmask0_%=\n" \
"Lmask0r_%=:\n" \
"v_mfma_f32_16x16x32_bf16 v[0:3], a[192:195], a[128:131], v[0:3]\n" \
"s_lshl_b32 s70, s95, 1\n" \
"v_cvt_pk_bf16_f32 v152, v152, v153\n" \
"v_cvt_pk_bf16_f32 v153, v154, v155\n" \
"v_mfma_f32_16x16x32_bf16 v[0:3], a[196:199], a[132:135], v[0:3]\n" \
"s_add_u32 s70, s70, 131072\n" \
"v_cvt_pk_bf16_f32 v154, v156, v157\n" \
"v_cvt_pk_bf16_f32 v155, v158, v159\n" \
"v_mfma_f32_16x16x32_bf16 v[0:3], a[200:203], a[136:139], v[0:3]\n" \
"ds_read_b128 a[8:11], v254\n" \
"v_accvgpr_write_b32 a172, v152\n" \
"v_mfma_f32_16x16x32_bf16 v[0:3], a[204:207], a[140:143], v[0:3]\n" \
"ds_read_b128 a[12:15], v254\n" \
"v_accvgpr_write_b32 a173, v153\n" \
"v_mov_b32 v201, 0xf149f2ca\n" \
"v_mfma_f32_16x16x32_bf16 v[8:11], a[192:195], a[144:147], 0\n" \
"v_accvgpr_write_b32 a174, v154\n" \
"v_accvgpr_write_b32 a175, v155\n" \
"v_mfma_f32_16x16x32_bf16 v[8:11], a[196:199], a[148:151], v[8:11]\n" \
"v_cvt_pk_bf16_f32 v176, v176, v177\n" \
"v_cvt_pk_bf16_f32 v177, v178, v179\n" \
"v_mfma_f32_16x16x32_bf16 v[8:11], a[200:203], a[152:155], v[8:11]\n" \
"v_cvt_pk_bf16_f32 v178, v180, v181\n" \
"v_cvt_pk_bf16_f32 v179, v182, v183\n" \
"v_mfma_f32_16x16x32_bf16 v[8:11], a[204:207], a[156:159], v[8:11]\n" \
"v_accvgpr_write_b32 a184, v176\n" \
"v_accvgpr_write_b32 a185, v177\n" \
"v_max3_f32 v192, v0, v1, v2\n" \
"v_max3_f32 v192, v192, v3, v201\n" \
"v_mfma_f32_16x16x32_bf16 v[16:19], a[192:195], a[160:163], 0\n" \
"v_accvgpr_write_b32 a186, v178\n" \
"v_accvgpr_write_b32 a187, v179\n" \
"v_mfma_f32_16x16x32_bf16 v[16:19], a[196:199], a[164:167], v[16:19]\n" \
"v_cvt_pk_bf16_f32 v184, v184, v185\n" \
"v_cvt_pk_bf16_f32 v185, v186, v187\n" \
"v_mfma_f32_16x16x32_bf16 v[16:19], a[200:203], a[168:171], v[16:19]\n" \
"v_cvt_pk_bf16_f32 v186, v188, v189\n" \
"v_cvt_pk_bf16_f32 v187, v190, v191\n" \
"v_mfma_f32_16x16x32_bf16 v[16:19], a[204:207], a[172:175], v[16:19]\n" \
"v_max3_f32 v193, v8, v9, v10\n" \
"v_accvgpr_write_b32 a188, v184\n" \
"v_accvgpr_write_b32 a189, v185\n" \
"v_mfma_f32_16x16x32_bf16 v[24:27], a[192:195], a[176:179], 0\n" \
"v_accvgpr_write_b32 a190, v186\n" \
"v_accvgpr_write_b32 a191, v187\n" \
"v_mfma_f32_16x16x32_bf16 v[24:27], a[196:199], a[180:183], v[24:27]\n" \
"v_add_u32 v200, s70, v248\n" \
"v_max3_f32 v193, v193, v11, v201\n" \
"v_mfma_f32_16x16x32_bf16 v[24:27], a[200:203], a[184:187], v[24:27]\n" \
"ds_read_b128 v[128:131], v254\n" \
"ds_read_b128 v[132:135], v254\n" \
"v_max3_f32 v194, v16, v17, v18\n" \
"v_max3_f32 v194, v194, v19, v201\n" \
"v_mfma_f32_16x16x32_bf16 v[24:27], a[204:207], a[188:191], v[24:27]\n" \
"s_waitcnt lgkmcnt(2)\n" \
"ds_read_b128 v[136:139], v254\n" \
"ds_read_b128 v[140:143], v254\n" \
"v_mfma_f32_16x16x32_bf16 v[4:7], a[208:211], a[128:131], v[4:7]\n" \
"ds_read_b128 v[144:147], v254\n" \
"ds_read_b128 v[148:151], v254\n" \
"v_mfma_f32_16x16x32_bf16 v[4:7], a[212:215], a[132:135], v[4:7]\n" \
"ds_read_b128 a[192:195], v249 offset:16384\n" \
"ds_read_b128 v[152:155], v254\n" \
"v_mfma_f32_16x16x32_bf16 v[4:7], a[216:219], a[136:139], v[4:7]\n" \
"ds_read_b128 v[156:159], v254\n" \
"ds_read_b128 v[160:163], v254\n" \
"v_max3_f32 v195, v24, v25, v26\n" \
"v_max3_f32 v195, v195, v27, v201\n" \
"v_mov_b32 v196, v192\n" \
"v_mov_b32 v197, v193\n" \
"v_mov_b32 v198, v194\n" \
"v_mov_b32 v199, v195\n" \
"v_permlane16_swap_b32 v196, v192\n" \
"v_permlane16_swap_b32 v197, v193\n" \
"v_permlane16_swap_b32 v198, v194\n" \
"v_permlane16_swap_b32 v199, v195\n" \
"v_max_f32 v192, v192, v196\n" \
"v_max_f32 v193, v193, v197\n" \
"v_mfma_f32_16x16x32_bf16 v[4:7], a[220:223], a[140:143], v[4:7]\n" \
"ds_read_b128 a[196:199], v249 offset:18432\n" \
"ds_read_b128 v[164:167], v254\n" \
"v_max_f32 v194, v194, v198\n" \
"v_max_f32 v195, v195, v199\n" \
"v_mov_b32 v196, v192\n" \
"v_mov_b32 v197, v193\n" \
"v_mov_b32 v198, v194\n" \
"v_mov_b32 v199, v195\n" \
"v_permlane32_swap_b32 v196, v192\n" \
"v_permlane32_swap_b32 v197, v193\n" \
"v_permlane32_swap_b32 v198, v194\n" \
"v_permlane32_swap_b32 v199, v195\n" \
"v_max_f32 v192, v192, v196\n" \
"v_max_f32 v193, v193, v197\n" \
"v_mfma_f32_16x16x32_bf16 v[12:15], a[216:219], a[152:155], 0\n" \
"ds_read_b128 v[168:171], v254\n" \
"ds_read_b128 v[172:175], v254\n" \
"v_max_f32 v194, v194, v198\n" \
"v_max_f32 v195, v195, v199\n" \
"v_sub_f32 v224, 0, v192\n" \
"v_sub_f32 v225, 0, v192\n" \
"v_sub_f32 v226, 0, v192\n" \
"v_sub_f32 v227, 0, v192\n" \
"v_add_f32 v32, v32, v224\n" \
"v_mfma_f32_16x16x32_bf16 v[12:15], a[220:223], a[156:159], v[12:15]\n" \
"ds_read_b128 a[200:203], v249 offset:20480\n" \
"ds_read_b128 v[176:179], v254\n" \
"v_mfma_f32_16x16x32_bf16 v[12:15], a[208:211], a[144:147], v[12:15]\n" \
"ds_read_b128 v[180:183], v254\n" \
"ds_read_b128 v[184:187], v254\n" \
"v_mfma_f32_16x16x32_bf16 v[12:15], a[212:215], a[148:151], v[12:15]\n" \
"ds_read_b128 a[204:207], v249 offset:22528\n" \
"buffer_store_dwordx4 a[40:43], v249, s[56:59], s90 offen offset:2304 sc1\n" \
"ds_write_b128 v200, a[160:163] offset:0\n" \
"s_add_u32 s90, s89, 417792\n" \
"v_mfma_f32_16x16x32_bf16 v[20:23], a[208:211], a[160:163], 0\n" \
"v_add_f32 v33, v33, v224\n" \
"v_add_f32 v34, v34, v224\n" \
"v_mfma_f32_16x16x32_bf16 v[20:23], a[212:215], a[164:167], v[20:23]\n" \
"v_add_f32 v35, v35, v224\n" \
"v_sub_f32 v228, 0, v193\n" \
"v_mfma_f32_16x16x32_bf16 v[20:23], a[216:219], a[168:171], v[20:23]\n" \
"ds_read_b128 a[40:43], v254\n" \
"ds_read_b128 v[188:191], v254\n" \
"v_mfma_f32_16x16x32_bf16 v[20:23], a[220:223], a[172:175], v[20:23]\n" \
"buffer_store_dwordx4 a[16:19], v248, s[56:59], s90 offen offset:0 sc1\n" \
"ds_write_b128 v200, a[164:167] offset:1024\n" \
"v_sub_f32 v229, 0, v193\n" \
"v_mfma_f32_16x16x32_bf16 v[28:31], a[216:219], a[184:187], 0\n" \
"v_sub_f32 v230, 0, v193\n" \
"v_sub_f32 v231, 0, v193\n" \
"v_mfma_f32_16x16x32_bf16 v[28:31], a[220:223], a[188:191], v[28:31]\n" \
"v_sub_f32 v232, 0, v194\n" \
"v_sub_f32 v233, 0, v194\n" \
"v_mfma_f32_16x16x32_bf16 v[28:31], a[208:211], a[176:179], v[28:31]\n" \
"buffer_store_dwordx4 a[20:23], v248, s[56:59], s90 offen offset:1024 sc1\n" \
"ds_write_b128 v200, a[168:171] offset:2048\n" \
"v_sub_f32 v234, 0, v194\n" \
"v_mfma_f32_16x16x32_bf16 v[28:31], a[212:215], a[180:183], v[28:31]\n" \
"v_sub_f32 v235, 0, v194\n" \
"v_sub_f32 v236, 0, v195\n" \
"v_mfma_f32_16x16x32_bf16 v[32:35], a[224:227], a[128:131], v[32:35]\n" \
"v_sub_f32 v237, 0, v195\n" \
"v_sub_f32 v238, 0, v195\n" \
"v_mfma_f32_16x16x32_bf16 v[32:35], a[228:231], a[132:135], v[32:35]\n" \
"ds_read_b128 a[208:211], v249 offset:16640\n" \
"v_sub_f32 v239, 0, v195\n" \
"v_mfma_f32_16x16x32_bf16 v[32:35], a[232:235], a[136:139], v[32:35]\n" \
"ds_read_b128 a[44:47], v254\n" \
"v_add_f32 v36, v36, v224\n" \
"v_mfma_f32_16x16x32_bf16 v[32:35], a[236:239], a[140:143], v[32:35]\n" \
"ds_read_b128 a[212:215], v249 offset:18688\n" \
"ds_read_b128 a[16:19], v254\n" \
"v_mfma_f32_16x16x32_bf16 v[40:43], a[232:235], a[152:155], v[228:231]\n" \
"ds_read_b128 a[48:51], v254\n" \
"v_add_f32 v37, v37, v224\n" \
"v_mfma_f32_16x16x32_bf16 v[40:43], a[236:239], a[156:159], v[40:43]\n" \
"ds_read_b128 a[216:219], v249 offset:20736\n" \
"v_add_f32 v38, v38, v224\n" \
"v_mfma_f32_16x16x32_bf16 v[40:43], a[224:227], a[144:147], v[40:43]\n" \
"ds_read_b128 a[20:23], v254\n" \
"v_add_f32 v39, v39, v224\n" \
"v_mfma_f32_16x16x32_bf16 v[40:43], a[228:231], a[148:151], v[40:43]\n" \
"buffer_store_dwordx4 a[24:27], v248, s[56:59], s90 offen offset:2048 sc1\n" \
"ds_read_b128 a[220:223], v249 offset:22784\n" \
"ds_write_b128 v200, a[172:175] offset:3072\n" \
"v_pk_add_f32 v[0:1], v[0:1], v[224:225]\n" \
"v_pk_add_f32 v[2:3], v[2:3], v[224:225]\n" \
"v_pk_add_f32 v[4:5], v[4:5], v[224:225]\n" \
"v_pk_add_f32 v[6:7], v[6:7], v[224:225]\n" \
"v_exp_f32 v0, v0\n" \
"v_exp_f32 v1, v1\n" \
"v_exp_f32 v2, v2\n" \
"v_cvt_pk_bf16_f32 v0, v0, v1\n" \
"v_exp_f32 v3, v3\n" \
"v_exp_f32 v4, v4\n" \
"v_cvt_pk_bf16_f32 v1, v2, v3\n" \
"v_mfma_f32_16x16x32_bf16 v[48:51], a[224:227], a[160:163], v[232:235]\n" \
"v_exp_f32 v5, v5\n" \
"v_mfma_f32_16x16x32_bf16 v[48:51], a[228:231], a[164:167], v[48:51]\n" \
"v_exp_f32 v6, v6\n" \
"v_mfma_f32_16x16x32_bf16 v[48:51], a[232:235], a[168:171], v[48:51]\n" \
"ds_read_b128 a[52:55], v254\n" \
"v_cvt_pk_bf16_f32 v2, v4, v5\n" \
"v_mfma_f32_16x16x32_bf16 v[48:51], a[236:239], a[172:175], v[48:51]\n" \
"buffer_store_dwordx4 a[28:31], v248, s[56:59], s90 offen offset:3072 sc1\n" \
"ds_write_b128 v200, a[176:179] offset:4096\n" \
"v_exp_f32 v7, v7\n" \
"v_pk_add_f32 v[8:9], v[8:9], v[228:229]\n" \
"v_pk_add_f32 v[10:11], v[10:11], v[228:229]\n" \
"v_pk_add_f32 v[12:13], v[12:13], v[228:229]\n" \
"v_pk_add_f32 v[14:15], v[14:15], v[228:229]\n" \
"v_exp_f32 v8, v8\n" \
"v_cvt_pk_bf16_f32 v3, v6, v7\n" \
"v_exp_f32 v9, v9\n" \
"v_exp_f32 v10, v10\n" \
"v_cvt_pk_bf16_f32 v8, v8, v9\n" \
"v_exp_f32 v11, v11\n" \
"v_mfma_f32_16x16x32_bf16 v[56:59], a[232:235], a[184:187], v[236:239]\n" \
"global_load_dword v253, v252, s[40:41]\n" \
"v_mfma_f32_16x16x32_bf16 v[56:59], a[236:239], a[188:191], v[56:59]\n" \
"s_sub_u32 s80, s80, 1\n" \
"v_exp_f32 v12, v12\n" \
"v_mfma_f32_16x16x32_bf16 v[56:59], a[224:227], a[176:179], v[56:59]\n" \
"s_cmp_gt_i32 s80, 0\n" \
"ds_write_b128 v200, a[180:183] offset:5120\n" \
"v_cvt_pk_bf16_f32 v9, v10, v11\n" \
"v_exp_f32 v13, v13\n" \
"v_exp_f32 v14, v14\n" \
"v_cvt_pk_bf16_f32 v10, v12, v13\n" \
"v_exp_f32 v15, v15\n" \
"v_pk_add_f32 v[16:17], v[16:17], v[232:233]\n" \
"v_pk_add_f32 v[18:19], v[18:19], v[232:233]\n" \
"v_pk_add_f32 v[20:21], v[20:21], v[232:233]\n" \
"v_pk_add_f32 v[22:23], v[22:23], v[232:233]\n" \
"v_exp_f32 v16, v16\n" \
"v_cvt_pk_bf16_f32 v11, v14, v15\n" \
"v_mfma_f32_16x16x32_bf16 v[56:59], a[228:231], a[180:183], v[56:59]\n" \
"s_cselect_b32 s81, 0x80000, 0\n" \
"v_exp_f32 v17, v17\n" \
"v_mfma_f32_16x16x32_bf16 v[36:39], a[240:243], a[128:131], v[36:39]\n" \
"s_add_u32 s40, s40, s81\n" \
"v_exp_f32 v18, v18\n" \
"v_mfma_f32_16x16x32_bf16 v[36:39], a[244:247], a[132:135], v[36:39]\n" \
"ds_read_b128 a[224:227], v249 offset:24576\n" \
"v_cvt_pk_bf16_f32 v16, v16, v17\n" \
"v_mfma_f32_16x16x32_bf16 v[240:243], a[120:123], v[0:3], v[240:243]\n" \
"v_exp_f32 v19, v19\n" \
"v_mfma_f32_16x16x32_bf16 v[240:243], a[124:127], v[8:11], v[240:243]\n" \
"s_addc_u32 s41, s41, 0\n" \
"v_exp_f32 v20, v20\n" \
"v_mfma_f32_16x16x32_bf16 v[36:39], a[248:251], a[136:139], v[36:39]\n" \
"s_add_u32 m0, s95, 49152\n" \
"ds_read_b128 a[24:27], v254\n" \
"v_cvt_pk_bf16_f32 v17, v18, v19\n" \
"v_mfma_f32_16x16x32_bf16 v[36:39], a[252:255], a[140:143], v[36:39]\n" \
"ds_read_b128 a[228:231], v249 offset:26624\n" \
"global_load_lds_dwordx4 v250, s[36:37] offset:0 sc1\n" \
"ds_read_b128 a[56:59], v254\n" \
"v_exp_f32 v21, v21\n" \
"v_exp_f32 v22, v22\n" \
"v_mfma_f32_16x16x32_bf16 v[44:47], a[248:251], a[152:155], v[228:231]\n" \
"ds_read_b128 a[28:31], v254\n" \
"v_cvt_pk_bf16_f32 v18, v20, v21\n" \
"v_mfma_f32_16x16x32_bf16 v[44:47], a[252:255], a[156:159], v[44:47]\n" \
"ds_read_b128 a[232:235], v249 offset:28672\n" \
"global_load_lds_dwordx4 v250, s[36:37] offset:1024 sc1\n" \
"v_mfma_f32_16x16x32_bf16 v[44:47], a[240:243], a[144:147], v[44:47]\n" \
"ds_read_b128 a[60:63], v254\n" \
"global_load_lds_dwordx4 v250, s[36:37] offset:2048 sc1\n" \
"v_mfma_f32_16x16x32_bf16 v[44:47], a[244:247], a[148:151], v[44:47]\n" \
"ds_read_b128 a[236:239], v249 offset:30720\n" \
"global_load_lds_dwordx4 v250, s[36:37] offset:3072 sc1\n" \
"ds_write_b128 v200, a[184:187] offset:6144\n" \
"v_exp_f32 v23, v23\n" \
"v_pk_add_f32 v[24:25], v[24:25], v[236:237]\n" \
"v_pk_add_f32 v[26:27], v[26:27], v[236:237]\n" \
"v_pk_add_f32 v[28:29], v[28:29], v[236:237]\n" \
"v_pk_add_f32 v[30:31], v[30:31], v[236:237]\n" \
"v_exp_f32 v24, v24\n" \
"v_cvt_pk_bf16_f32 v19, v22, v23\n" \
"v_exp_f32 v25, v25\n" \
"v_exp_f32 v26, v26\n" \
"v_cvt_pk_bf16_f32 v24, v24, v25\n" \
"v_mfma_f32_16x16x32_bf16 v[242:245], a[120:123], v[16:19], v[242:245]\n" \
"v_exp_f32 v27, v27\n" \
"v_mfma_f32_16x16x32_bf16 v[52:55], a[240:243], a[160:163], v[232:235]\n" \
"s_add_u32 s36, s36, 17408\n" \
"v_mov_b32 v202, s60\n" \
"v_lshlrev_b32 v203, 28, s61\n" \
"v_mfma_f32_16x16x32_bf16 v[52:55], a[244:247], a[164:167], v[52:55]\n" \
"s_mov_b32 s42, s60\n" \
"v_mov_b32 v246, v249\n" \
"v_mov_b32 v254, 0xff800000\n" \
"v_mfma_f32_16x16x32_bf16 v[52:55], a[248:251], a[168:171], v[52:55]\n" \
"s_mov_b32 s43, s60\n" \
"v_exp_f32 v28, v28\n" \
"v_mfma_f32_16x16x32_bf16 v[52:55], a[252:255], a[172:175], v[52:55]\n" \
"s_mov_b64 s[8:9], 0\n" \
"ds_write_b128 v200, a[188:191] offset:7168\n" \
"v_cvt_pk_bf16_f32 v25, v26, v27\n" \
"v_exp_f32 v29, v29\n" \
"v_mfma_f32_16x16x32_bf16 v[60:63], a[248:251], a[184:187], v[236:239]\n" \
"s_mov_b64 s[10:11], -1\n" \
"v_exp_f32 v30, v30\n" \
"v_cvt_pk_bf16_f32 v26, v28, v29\n" \
"v_mfma_f32_16x16x32_bf16 v[60:63], a[252:255], a[188:191], v[60:63]\n" \
"s_mov_b64 s[92:93], 0\n" \
"v_exp_f32 v31, v31\n" \
"v_mfma_f32_16x16x32_bf16 v[60:63], a[240:243], a[176:179], v[60:63]\n" \
"v_cvt_pk_bf16_f32 v27, v30, v31\n" \
"s_lshl_b32 s84, s63, 2\n" \
"v_mfma_f32_16x16x32_bf16 v[60:63], a[244:247], a[180:183], v[60:63]\n" \
"v_mfma_f32_16x16x32_bf16 v[242:245], a[124:127], v[24:27], v[242:245]\n" \
"s_add_u32 s84, s84, s79\n" \
"ds_read_b128 a[240:243], v249 offset:24832\n" \
"ds_read_b128 a[244:247], v249 offset:26880\n" \
"ds_read_b128 a[248:251], v249 offset:28928\n" \
"s_cmp_eq_u32 s63, 0\n" \
"s_waitcnt vmcnt(16) lgkmcnt(3)\n" \
"s_barrier\n" \
"s_memrealtime s[12:13]\n" \
"s_cbranch_scc1 LA1l_%=\n" \
"s_branch LA1_%=\n" \
"Lpg5391_%=:\n" \
"s_cbranch_execz Lpg6293_%=\n" \
"Lloop_%=:\n" \
"v_mfma_f32_16x16x32_bf16 a[0:3], a[192:195], v[64:67], a[0:3]\n" \
"v_exp_f32 v0, v0\n" \
"v_mfma_f32_16x16x32_bf16 a[0:3], a[196:199], v[96:99], a[0:3]\n" \
"v_cvt_pk_bf16_f32 v123, v126, v127\n" \
"v_add_f32 v242, v242, v0\n" \
"v_exp_f32 v1, v1\n" \
"v_mfma_f32_16x16x32_bf16 a[32:35], a[192:195], v[72:75], a[32:35]\n" \
"s_add_u32 m0, s73, 0x4000\n" \
"ds_read_b128 a[244:247], v247 offset:30720\n" \
"v_add_f32 v242, v242, v1\n" \
"v_mfma_f32_16x16x32_bf16 a[32:35], a[196:199], v[104:107], a[32:35]\n" \
"ds_read_b128 a[248:251], v247 offset:23552\n" \
"global_load_lds_dwordx4 v250, s[36:37] offset:0 sc1\n" \
"v_mfma_f32_16x16x32_bf16 v[128:131], a[192:195], v[80:83], v[128:131]\n" \
"ds_read_b128 a[252:255], v247 offset:31744\n" \
"v_exp_f32 v2, v2\n" \
"v_mfma_f32_16x16x32_bf16 v[128:131], a[196:199], v[112:115], v[128:131]\n" \
"v_add_f32 v242, v242, v2\n" \
"v_cvt_pk_bf16_f32 v0, v0, v1\n" \
"v_mfma_f32_16x16x32_bf16 v[160:163], a[192:195], v[88:91], v[160:163]\n" \
"v_exp_f32 v3, v3\n" \
"v_mfma_f32_16x16x32_bf16 v[160:163], a[196:199], v[120:123], v[160:163]\n" \
"ds_read_b128 a[192:195], v246 offset:16384\n" \
"v_xor_b32 v247, 0x8000, v247\n" \
"v_add_f32 v242, v242, v3\n" \
"v_mfma_f32_16x16x32_bf16 v[164:167], a[200:203], v[88:91], v[164:167]\n" \
"v_exp_f32 v4, v4\n" \
"v_mfma_f32_16x16x32_bf16 v[164:167], a[204:207], v[120:123], v[164:167]\n" \
"v_add_f32 v242, v242, v4\n" \
"v_cvt_pk_bf16_f32 v1, v2, v3\n" \
"v_mfma_f32_16x16x32_bf16 v[132:135], a[200:203], v[80:83], v[132:135]\n" \
"v_exp_f32 v5, v5\n" \
"v_mfma_f32_16x16x32_bf16 v[132:135], a[204:207], v[112:115], v[132:135]\n" \
"ds_read_b128 a[196:199], v246 offset:18432\n" \
"v_add_f32 v242, v242, v5\n" \
"v_mfma_f32_16x16x32_bf16 a[36:39], a[200:203], v[72:75], a[36:39]\n" \
"v_exp_f32 v6, v6\n" \
"v_mfma_f32_16x16x32_bf16 a[36:39], a[204:207], v[104:107], a[36:39]\n" \
"v_add_f32 v242, v242, v6\n" \
"v_cvt_pk_bf16_f32 v2, v4, v5\n" \
"v_mfma_f32_16x16x32_bf16 a[4:7], a[200:203], v[64:67], a[4:7]\n" \
"v_exp_f32 v7, v7\n" \
"v_mfma_f32_16x16x32_bf16 a[4:7], a[204:207], v[96:99], a[4:7]\n" \
"ds_read_b128 a[200:203], v246 offset:20480\n" \
"global_load_lds_dwordx4 v250, s[36:37] offset:1024 sc1\n" \
"v_add_f32 v242, v242, v7\n" \
"v_mfma_f32_16x16x32_bf16 a[8:11], a[208:211], v[64:67], a[8:11]\n" \
"v_exp_f32 v8, v8\n" \
"v_mfma_f32_16x16x32_bf16 a[8:11], a[212:215], v[96:99], a[8:11]\n" \
"v_add_f32 v243, v243, v8\n" \
"v_cvt_pk_bf16_f32 v3, v6, v7\n" \
"v_mfma_f32_16x16x32_bf16 a[40:43], a[208:211], v[72:75], a[40:43]\n" \
"v_exp_f32 v9, v9\n" \
"v_mfma_f32_16x16x32_bf16 a[40:43], a[212:215], v[104:107], a[40:43]\n" \
"ds_read_b128 a[204:207], v246 offset:22528\n" \
"v_add_f32 v243, v243, v9\n" \
"v_mfma_f32_16x16x32_bf16 v[136:139], a[208:211], v[80:83], v[136:139]\n" \
"v_exp_f32 v10, v10\n" \
"v_mfma_f32_16x16x32_bf16 v[136:139], a[212:215], v[112:115], v[136:139]\n" \
"v_add_f32 v243, v243, v10\n" \
"v_cvt_pk_bf16_f32 v8, v8, v9\n" \
"v_mfma_f32_16x16x32_bf16 v[168:171], a[208:211], v[88:91], v[168:171]\n" \
"v_exp_f32 v11, v11\n" \
"v_mfma_f32_16x16x32_bf16 v[168:171], a[212:215], v[120:123], v[168:171]\n" \
"ds_read_b128 a[208:211], v246 offset:16640\n" \
"v_add_f32 v243, v243, v11\n" \
"v_mfma_f32_16x16x32_bf16 v[172:175], a[216:219], v[88:91], v[172:175]\n" \
"v_exp_f32 v12, v12\n" \
"v_mfma_f32_16x16x32_bf16 v[172:175], a[220:223], v[120:123], v[172:175]\n" \
"v_add_f32 v243, v243, v12\n" \
"v_cvt_pk_bf16_f32 v9, v10, v11\n" \
"v_mfma_f32_16x16x32_bf16 v[140:143], a[216:219], v[80:83], v[140:143]\n" \
"v_exp_f32 v13, v13\n" \
"v_mfma_f32_16x16x32_bf16 v[140:143], a[220:223], v[112:115], v[140:143]\n" \
"ds_read_b128 a[212:215], v246 offset:18688\n" \
"global_load_lds_dwordx4 v250, s[36:37] offset:2048 sc1\n" \
"v_add_f32 v243, v243, v13\n" \
"v_mfma_f32_16x16x32_bf16 a[44:47], a[216:219], v[72:75], a[44:47]\n" \
"v_exp_f32 v14, v14\n" \
"v_mfma_f32_16x16x32_bf16 a[44:47], a[220:223], v[104:107], a[44:47]\n" \
"v_add_f32 v243, v243, v14\n" \
"v_cvt_pk_bf16_f32 v10, v12, v13\n" \
"v_mfma_f32_16x16x32_bf16 a[12:15], a[216:219], v[64:67], a[12:15]\n" \
"s_waitcnt lgkmcnt(6)\n" \
"v_exp_f32 v15, v15\n" \
"v_mfma_f32_16x16x32_bf16 a[12:15], a[220:223], v[96:99], a[12:15]\n" \
"ds_read_b128 a[216:219], v246 offset:20736\n" \
"v_add_f32 v243, v243, v15\n" \
"v_mfma_f32_16x16x32_bf16 a[16:19], a[224:227], v[64:67], a[16:19]\n" \
"v_exp_f32 v16, v16\n" \
"v_mfma_f32_16x16x32_bf16 a[16:19], a[228:231], v[96:99], a[16:19]\n" \
"v_add_f32 v244, v244, v16\n" \
"v_cvt_pk_bf16_f32 v11, v14, v15\n" \
"v_mfma_f32_16x16x32_bf16 a[48:51], a[224:227], v[72:75], a[48:51]\n" \
"v_exp_f32 v17, v17\n" \
"v_mfma_f32_16x16x32_bf16 a[48:51], a[228:231], v[104:107], a[48:51]\n" \
"ds_read_b128 a[220:223], v246 offset:22784\n" \
"global_load_lds_dwordx4 v250, s[36:37] offset:3072 sc1\n" \
"v_add_f32 v244, v244, v17\n" \
"v_mfma_f32_16x16x32_bf16 v[144:147], a[224:227], v[80:83], v[144:147]\n" \
"s_add_u32 s36, s36, 17408\n" \
"v_exp_f32 v18, v18\n" \
"v_mfma_f32_16x16x32_bf16 v[144:147], a[228:231], v[112:115], v[144:147]\n" \
"s_xor_b32 s73, s73, 0x8000\n" \
"v_add_f32 v244, v244, v18\n" \
"v_cvt_pk_bf16_f32 v16, v16, v17\n" \
"v_mfma_f32_16x16x32_bf16 v[176:179], a[224:227], v[88:91], v[176:179]\n" \
"s_xor_b64 s[52:53], s[44:45], s[42:43]\n" \
"v_exp_f32 v19, v19\n" \
"v_mfma_f32_16x16x32_bf16 v[176:179], a[228:231], v[120:123], v[176:179]\n" \
"s_xor_b64 s[54:55], s[46:47], s[42:43]\n" \
"ds_read_b128 a[224:227], v246 offset:24576\n" \
"v_add_f32 v244, v244, v19\n" \
"v_mfma_f32_16x16x32_bf16 v[180:183], a[232:235], v[88:91], v[180:183]\n" \
"s_or_b64 s[52:53], s[52:53], s[54:55]\n" \
"v_exp_f32 v20, v20\n" \
"v_mfma_f32_16x16x32_bf16 v[180:183], a[236:239], v[120:123], v[180:183]\n" \
"s_xor_b64 s[54:55], s[48:49], s[42:43]\n" \
"v_add_f32 v244, v244, v20\n" \
"v_cvt_pk_bf16_f32 v17, v18, v19\n" \
"v_mfma_f32_16x16x32_bf16 v[148:151], a[232:235], v[80:83], v[148:151]\n" \
"s_or_b64 s[52:53], s[52:53], s[54:55]\n" \
"v_exp_f32 v21, v21\n" \
"v_mfma_f32_16x16x32_bf16 v[148:151], a[236:239], v[112:115], v[148:151]\n" \
"s_xor_b64 s[54:55], s[50:51], s[42:43]\n" \
"ds_read_b128 a[228:231], v246 offset:26624\n" \
"v_add_f32 v244, v244, v21\n" \
"v_exp_f32 v22, v22\n" \
"v_mfma_f32_16x16x32_bf16 a[52:55], a[232:235], v[72:75], a[52:55]\n" \
"s_or_b64 s[52:53], s[52:53], s[54:55]\n" \
"v_add_f32 v244, v244, v22\n" \
"v_cvt_pk_bf16_f32 v18, v20, v21\n" \
"v_mfma_f32_16x16x32_bf16 a[52:55], a[236:239], v[104:107], a[52:55]\n" \
"s_and_b64 s[52:53], s[52:53], s[8:9]\n" \
"s_cbranch_scc1 Lrs0_%=\n" \
"Lrr0_%=:\n" \
"v_mfma_f32_16x16x32_bf16 a[20:23], a[232:235], v[64:67], a[20:23]\n" \
"s_mov_b64 s[8:9], s[10:11]\n" \
"v_exp_f32 v23, v23\n" \
"v_mfma_f32_16x16x32_bf16 a[20:23], a[236:239], v[96:99], a[20:23]\n" \
"ds_read_b128 a[232:235], v246 offset:28672\n" \
"Lpg5534_%=:\n" \
"s_cbranch_execz Lpg6771_%=\n" \
"v_add_f32 v244, v244, v23\n" \
"v_mfma_f32_16x16x32_bf16 a[24:27], a[240:243], v[64:67], a[24:27]\n" \
"v_exp_f32 v24, v24\n" \
"v_mfma_f32_16x16x32_bf16 a[24:27], a[244:247], v[96:99], a[24:27]\n" \
"v_add_f32 v245, v245, v24\n" \
"v_cvt_pk_bf16_f32 v19, v22, v23\n" \
"v_mfma_f32_16x16x32_bf16 a[56:59], a[240:243], v[72:75], a[56:59]\n" \
"v_exp_f32 v25, v25\n" \
"v_mfma_f32_16x16x32_bf16 a[56:59], a[244:247], v[104:107], a[56:59]\n" \
"ds_read_b128 a[236:239], v246 offset:30720\n" \
"v_add_f32 v245, v245, v25\n" \
"v_mfma_f32_16x16x32_bf16 v[152:155], a[240:243], v[80:83], v[152:155]\n" \
"v_exp_f32 v26, v26\n" \
"v_mfma_f32_16x16x32_bf16 v[152:155], a[244:247], v[112:115], v[152:155]\n" \
"v_add_f32 v245, v245, v26\n" \
"v_cvt_pk_bf16_f32 v24, v24, v25\n" \
"v_mfma_f32_16x16x32_bf16 v[184:187], a[240:243], v[88:91], v[184:187]\n" \
"v_exp_f32 v27, v27\n" \
"v_mfma_f32_16x16x32_bf16 v[184:187], a[244:247], v[120:123], v[184:187]\n" \
"ds_read_b128 a[240:243], v246 offset:24832\n" \
"v_add_f32 v245, v245, v27\n" \
"v_mfma_f32_16x16x32_bf16 v[188:191], a[248:251], v[88:91], v[188:191]\n" \
"v_exp_f32 v28, v28\n" \
"v_mfma_f32_16x16x32_bf16 v[188:191], a[252:255], v[120:123], v[188:191]\n" \
"v_add_f32 v245, v245, v28\n" \
"v_cvt_pk_bf16_f32 v25, v26, v27\n" \
"v_mfma_f32_16x16x32_bf16 v[156:159], a[248:251], v[80:83], v[156:159]\n" \
"v_exp_f32 v29, v29\n" \
"v_mfma_f32_16x16x32_bf16 v[156:159], a[252:255], v[112:115], v[156:159]\n" \
"ds_read_b128 a[244:247], v246 offset:26880\n" \
"v_add_f32 v245, v245, v29\n" \
"v_mfma_f32_16x16x32_bf16 a[60:63], a[248:251], v[72:75], a[60:63]\n" \
"v_exp_f32 v30, v30\n" \
"v_mfma_f32_16x16x32_bf16 a[60:63], a[252:255], v[104:107], a[60:63]\n" \
"v_add_f32 v245, v245, v30\n" \
"v_cvt_pk_bf16_f32 v26, v28, v29\n" \
"v_mfma_f32_16x16x32_bf16 a[28:31], a[248:251], v[64:67], a[28:31]\n" \
"s_waitcnt lgkmcnt(6)\n" \
"v_exp_f32 v31, v31\n" \
"v_mfma_f32_16x16x32_bf16 a[28:31], a[252:255], v[96:99], a[28:31]\n" \
"ds_read_b128 a[248:251], v246 offset:28928\n" \
"v_add_f32 v245, v245, v31\n" \
"LA1_%=:\n" \
"Lpg5578_%=:\n" \
"s_cbranch_execz Lpg7030_%=\n" \
"v_mfma_f32_16x16x32_bf16 v[64:67], a[192:195], a[128:131], v[224:227]\n" \
"v_exp_f32 v32, v32\n" \
"v_mfma_f32_16x16x32_bf16 v[64:67], a[196:199], a[132:135], v[64:67]\n" \
"v_cvt_pk_bf16_f32 v27, v30, v31\n" \
"v_add_f32 v242, v242, v32\n" \
"v_mfma_f32_16x16x32_bf16 v[64:67], a[200:203], a[136:139], v[64:67]\n" \
"s_mov_b32 m0, s74\n" \
"v_exp_f32 v33, v33\n" \
"v_mfma_f32_16x16x32_bf16 v[64:67], a[204:207], a[140:143], v[64:67]\n" \
"ds_read_b128 a[252:255], v246 offset:30976\n" \
"global_load_lds_dwordx4 v250, s[38:39] offset:0 sc1\n" \
"v_add_f32 v242, v242, v33\n" \
"v_mfma_f32_16x16x32_bf16 v[72:75], a[200:203], a[152:155], v[228:231]\n" \
"v_exp_f32 v34, v34\n" \
"v_mfma_f32_16x16x32_bf16 v[72:75], a[204:207], a[156:159], v[72:75]\n" \
"v_add_f32 v242, v242, v34\n" \
"v_cvt_pk_bf16_f32 v32, v32, v33\n" \
"v_mfma_f32_16x16x32_bf16 v[72:75], a[192:195], a[144:147], v[72:75]\n" \
"v_exp_f32 v35, v35\n" \
"v_mfma_f32_16x16x32_bf16 v[72:75], a[196:199], a[148:151], v[72:75]\n" \
"v_add_f32 v242, v242, v35\n" \
"v_exp_f32 v36, v36\n" \
"v_mfma_f32_16x16x32_bf16 v[80:83], a[192:195], a[160:163], v[232:235]\n" \
"v_add_f32 v242, v242, v36\n" \
"v_cvt_pk_bf16_f32 v33, v34, v35\n" \
"v_mfma_f32_16x16x32_bf16 v[80:83], a[196:199], a[164:167], v[80:83]\n" \
"v_exp_f32 v37, v37\n" \
"v_mfma_f32_16x16x32_bf16 v[80:83], a[200:203], a[168:171], v[80:83]\n" \
"v_add_f32 v242, v242, v37\n" \
"v_mfma_f32_16x16x32_bf16 v[80:83], a[204:207], a[172:175], v[80:83]\n" \
"v_exp_f32 v38, v38\n" \
"v_mfma_f32_16x16x32_bf16 v[88:91], a[200:203], a[184:187], v[236:239]\n" \
"v_add_f32 v242, v242, v38\n" \
"v_cvt_pk_bf16_f32 v34, v36, v37\n" \
"v_mfma_f32_16x16x32_bf16 v[88:91], a[204:207], a[188:191], v[88:91]\n" \
"v_exp_f32 v39, v39\n" \
"v_mfma_f32_16x16x32_bf16 v[88:91], a[192:195], a[176:179], v[88:91]\n" \
"v_add_f32 v242, v242, v39\n" \
"v_mfma_f32_16x16x32_bf16 v[88:91], a[196:199], a[180:183], v[88:91]\n" \
"ds_read_b128 a[192:195], v247 offset:0\n" \
"v_xor_b32 v246, 0x8000, v246\n" \
"v_mfma_f32_16x16x32_bf16 v[92:95], a[208:211], a[176:179], v[236:239]\n" \
"v_exp_f32 v40, v40\n" \
"v_mfma_f32_16x16x32_bf16 v[92:95], a[212:215], a[180:183], v[92:95]\n" \
"v_add_f32 v243, v243, v40\n" \
"v_cvt_pk_bf16_f32 v35, v38, v39\n" \
"v_mfma_f32_16x16x32_bf16 v[92:95], a[216:219], a[184:187], v[92:95]\n" \
"v_exp_f32 v41, v41\n" \
"v_mfma_f32_16x16x32_bf16 v[92:95], a[220:223], a[188:191], v[92:95]\n" \
"ds_read_b128 a[196:199], v247 offset:8192\n" \
"global_load_lds_dwordx4 v250, s[38:39] offset:1024 sc1\n" \
"v_add_f32 v243, v243, v41\n" \
"v_mfma_f32_16x16x32_bf16 v[84:87], a[216:219], a[168:171], v[232:235]\n" \
"v_exp_f32 v42, v42\n" \
"v_mfma_f32_16x16x32_bf16 v[84:87], a[220:223], a[172:175], v[84:87]\n" \
"v_add_f32 v243, v243, v42\n" \
"v_cvt_pk_bf16_f32 v40, v40, v41\n" \
"v_mfma_f32_16x16x32_bf16 v[84:87], a[208:211], a[160:163], v[84:87]\n" \
"v_exp_f32 v43, v43\n" \
"v_mfma_f32_16x16x32_bf16 v[84:87], a[212:215], a[164:167], v[84:87]\n" \
"ds_read_b128 a[200:203], v247 offset:1024\n" \
"v_add_f32 v243, v243, v43\n" \
"v_mfma_f32_16x16x32_bf16 v[76:79], a[208:211], a[144:147], v[228:231]\n" \
"v_exp_f32 v44, v44\n" \
"v_mfma_f32_16x16x32_bf16 v[76:79], a[212:215], a[148:151], v[76:79]\n" \
"v_add_f32 v243, v243, v44\n" \
"v_cvt_pk_bf16_f32 v41, v42, v43\n" \
"v_mfma_f32_16x16x32_bf16 v[76:79], a[216:219], a[152:155], v[76:79]\n" \
"v_exp_f32 v45, v45\n" \
"v_mfma_f32_16x16x32_bf16 v[76:79], a[220:223], a[156:159], v[76:79]\n" \
"ds_read_b128 a[204:207], v247 offset:9216\n" \
"global_load_lds_dwordx4 v250, s[38:39] offset:2048 sc1\n" \
"v_add_f32 v243, v243, v45\n" \
"v_mfma_f32_16x16x32_bf16 v[68:71], a[216:219], a[136:139], v[224:227]\n" \
"v_exp_f32 v46, v46\n" \
"v_mfma_f32_16x16x32_bf16 v[68:71], a[220:223], a[140:143], v[68:71]\n" \
"v_add_f32 v243, v243, v46\n" \
"v_cvt_pk_bf16_f32 v42, v44, v45\n" \
"v_mfma_f32_16x16x32_bf16 v[68:71], a[208:211], a[128:131], v[68:71]\n" \
"s_waitcnt lgkmcnt(4)\n" \
"v_exp_f32 v47, v47\n" \
"v_mfma_f32_16x16x32_bf16 v[68:71], a[212:215], a[132:135], v[68:71]\n" \
"ds_read_b128 a[208:211], v247 offset:2048\n" \
"v_add_f32 v243, v243, v47\n" \
"v_mfma_f32_16x16x32_bf16 v[96:99], a[224:227], a[128:131], v[224:227]\n" \
"v_exp_f32 v48, v48\n" \
"v_mfma_f32_16x16x32_bf16 v[96:99], a[228:231], a[132:135], v[96:99]\n" \
"v_add_f32 v244, v244, v48\n" \
"v_cvt_pk_bf16_f32 v43, v46, v47\n" \
"v_mfma_f32_16x16x32_bf16 v[96:99], a[232:235], a[136:139], v[96:99]\n" \
"v_exp_f32 v49, v49\n" \
"v_mfma_f32_16x16x32_bf16 v[96:99], a[236:239], a[140:143], v[96:99]\n" \
"ds_read_b128 a[212:215], v247 offset:10240\n" \
"v_add_f32 v244, v244, v49\n" \
"v_mfma_f32_16x16x32_bf16 v[104:107], a[232:235], a[152:155], v[228:231]\n" \
"v_exp_f32 v50, v50\n" \
"v_mfma_f32_16x16x32_bf16 v[104:107], a[236:239], a[156:159], v[104:107]\n" \
"v_add_f32 v244, v244, v50\n" \
"v_cvt_pk_bf16_f32 v48, v48, v49\n" \
"v_mfma_f32_16x16x32_bf16 v[104:107], a[224:227], a[144:147], v[104:107]\n" \
"v_exp_f32 v51, v51\n" \
"v_mfma_f32_16x16x32_bf16 v[104:107], a[228:231], a[148:151], v[104:107]\n" \
"ds_read_b128 a[216:219], v247 offset:3072\n" \
"global_load_lds_dwordx4 v250, s[38:39] offset:3072 sc1\n" \
"v_add_f32 v244, v244, v51\n" \
"v_mfma_f32_16x16x32_bf16 v[112:115], a[224:227], a[160:163], v[232:235]\n" \
"s_add_u32 s38, s38, 17408\n" \
"v_exp_f32 v52, v52\n" \
"v_mfma_f32_16x16x32_bf16 v[112:115], a[228:231], a[164:167], v[112:115]\n" \
"v_add_f32 v244, v244, v52\n" \
"v_cvt_pk_bf16_f32 v49, v50, v51\n" \
"v_mfma_f32_16x16x32_bf16 v[112:115], a[232:235], a[168:171], v[112:115]\n" \
"v_exp_f32 v53, v53\n" \
"v_mfma_f32_16x16x32_bf16 v[112:115], a[236:239], a[172:175], v[112:115]\n" \
"ds_read_b128 a[220:223], v247 offset:11264\n" \
"v_add_f32 v244, v244, v53\n" \
"v_exp_f32 v54, v54\n" \
"v_mfma_f32_16x16x32_bf16 v[120:123], a[232:235], a[184:187], v[236:239]\n" \
"global_load_dword v253, v252, s[40:41]\n" \
"v_mfma_f32_16x16x32_bf16 v[120:123], a[236:239], a[188:191], v[120:123]\n" \
"s_sub_u32 s80, s80, 1\n" \
"v_add_f32 v244, v244, v54\n" \
"v_cvt_pk_bf16_f32 v50, v52, v53\n" \
"v_mfma_f32_16x16x32_bf16 v[120:123], a[224:227], a[176:179], v[120:123]\n" \
"s_cmp_gt_i32 s80, 0\n" \
"v_exp_f32 v55, v55\n" \
"v_mfma_f32_16x16x32_bf16 v[120:123], a[228:231], a[180:183], v[120:123]\n" \
"s_cselect_b32 s81, 0x80000, 0\n" \
"ds_read_b128 a[224:227], v247 offset:4096\n" \
"v_add_f32 v244, v244, v55\n" \
"v_mfma_f32_16x16x32_bf16 v[124:127], a[240:243], a[176:179], v[236:239]\n" \
"s_add_u32 s40, s40, s81\n" \
"v_exp_f32 v56, v56\n" \
"v_mfma_f32_16x16x32_bf16 v[124:127], a[244:247], a[180:183], v[124:127]\n" \
"s_addc_u32 s41, s41, 0\n" \
"v_add_f32 v245, v245, v56\n" \
"v_cvt_pk_bf16_f32 v51, v54, v55\n" \
"v_mfma_f32_16x16x32_bf16 v[124:127], a[248:251], a[184:187], v[124:127]\n" \
"v_exp_f32 v57, v57\n" \
"v_mfma_f32_16x16x32_bf16 v[124:127], a[252:255], a[188:191], v[124:127]\n" \
"ds_read_b128 a[228:231], v247 offset:12288\n" \
"v_add_f32 v245, v245, v57\n" \
"v_mfma_f32_16x16x32_bf16 v[116:119], a[248:251], a[168:171], v[232:235]\n" \
"v_exp_f32 v58, v58\n" \
"v_mfma_f32_16x16x32_bf16 v[116:119], a[252:255], a[172:175], v[116:119]\n" \
"v_add_f32 v245, v245, v58\n" \
"v_cvt_pk_bf16_f32 v56, v56, v57\n" \
"v_mfma_f32_16x16x32_bf16 v[116:119], a[240:243], a[160:163], v[116:119]\n" \
"v_exp_f32 v59, v59\n" \
"v_mfma_f32_16x16x32_bf16 v[116:119], a[244:247], a[164:167], v[116:119]\n" \
"ds_read_b128 a[232:235], v247 offset:5120\n" \
"v_add_f32 v245, v245, v59\n" \
"v_mfma_f32_16x16x32_bf16 v[108:111], a[240:243], a[144:147], v[228:231]\n" \
"v_exp_f32 v60, v60\n" \
"v_mfma_f32_16x16x32_bf16 v[108:111], a[244:247], a[148:151], v[108:111]\n" \
"v_add_f32 v245, v245, v60\n" \
"v_cvt_pk_bf16_f32 v57, v58, v59\n" \
"v_mfma_f32_16x16x32_bf16 v[108:111], a[248:251], a[152:155], v[108:111]\n" \
"v_exp_f32 v61, v61\n" \
"v_mfma_f32_16x16x32_bf16 v[108:111], a[252:255], a[156:159], v[108:111]\n" \
"ds_read_b128 a[236:239], v247 offset:13312\n" \
"v_add_f32 v245, v245, v61\n" \
"v_mfma_f32_16x16x32_bf16 v[100:103], a[248:251], a[136:139], v[224:227]\n" \
"v_exp_f32 v62, v62\n" \
"v_mfma_f32_16x16x32_bf16 v[100:103], a[252:255], a[140:143], v[100:103]\n" \
"v_add_f32 v245, v245, v62\n" \
"v_cvt_pk_bf16_f32 v58, v60, v61\n" \
"v_mfma_f32_16x16x32_bf16 v[100:103], a[240:243], a[128:131], v[100:103]\n" \
"s_waitcnt vmcnt(10) lgkmcnt(4)\n" \
"v_exp_f32 v63, v63\n" \
"v_mfma_f32_16x16x32_bf16 v[100:103], a[244:247], a[132:135], v[100:103]\n" \
"s_barrier\n" \
"ds_read_b128 a[240:243], v247 offset:6144\n" \
"v_add_f32 v245, v245, v63\n" \
"v_mfma_f32_16x16x32_bf16 a[0:3], a[192:195], v[0:3], a[0:3]\n" \
"buffer_store_dword v202, v203, s[56:59], s84 offen sc1\n" \
"v_mfma_f32_16x16x32_bf16 a[0:3], a[196:199], v[32:35], a[0:3]\n" \
"s_mov_b32 s84, 0x20000000\n" \
"v_cvt_pk_bf16_f32 v59, v62, v63\n" \
"v_exp_f32 v64, v64\n" \
"v_mfma_f32_16x16x32_bf16 a[32:35], a[192:195], v[8:11], a[32:35]\n" \
"s_mov_b32 m0, s73\n" \
"ds_read_b128 a[244:247], v247 offset:14336\n" \
"v_add_f32 v242, v242, v64\n" \
"v_mfma_f32_16x16x32_bf16 a[32:35], a[196:199], v[40:43], a[32:35]\n" \
"ds_read_b128 a[248:251], v247 offset:7168\n" \
"global_load_lds_dwordx4 v250, s[36:37] offset:0 sc1\n" \
"v_exp_f32 v65, v65\n" \
"v_mfma_f32_16x16x32_bf16 v[128:131], a[192:195], v[16:19], v[128:131]\n" \
"ds_read_b128 a[252:255], v247 offset:15360\n" \
"v_add_f32 v242, v242, v65\n" \
"v_exp_f32 v66, v66\n" \
"v_mfma_f32_16x16x32_bf16 v[128:131], a[196:199], v[48:51], v[128:131]\n" \
"v_add_f32 v242, v242, v66\n" \
"v_cvt_pk_bf16_f32 v64, v64, v65\n" \
"v_mfma_f32_16x16x32_bf16 v[160:163], a[192:195], v[24:27], v[160:163]\n" \
"v_exp_f32 v67, v67\n" \
"v_mfma_f32_16x16x32_bf16 v[160:163], a[196:199], v[56:59], v[160:163]\n" \
"ds_read_b128 a[192:195], v246 offset:0\n" \
"v_add_f32 v242, v242, v67\n" \
"v_mfma_f32_16x16x32_bf16 v[164:167], a[200:203], v[24:27], v[164:167]\n" \
"v_exp_f32 v68, v68\n" \
"v_mfma_f32_16x16x32_bf16 v[164:167], a[204:207], v[56:59], v[164:167]\n" \
"v_add_f32 v242, v242, v68\n" \
"v_cvt_pk_bf16_f32 v65, v66, v67\n" \
"v_mfma_f32_16x16x32_bf16 v[132:135], a[200:203], v[16:19], v[132:135]\n" \
"v_exp_f32 v69, v69\n" \
"v_mfma_f32_16x16x32_bf16 v[132:135], a[204:207], v[48:51], v[132:135]\n" \
"ds_read_b128 a[196:199], v246 offset:2048\n" \
"v_add_f32 v242, v242, v69\n" \
"v_mfma_f32_16x16x32_bf16 a[36:39], a[200:203], v[8:11], a[36:39]\n" \
"v_exp_f32 v70, v70\n" \
"v_mfma_f32_16x16x32_bf16 a[36:39], a[204:207], v[40:43], a[36:39]\n" \
"v_add_f32 v242, v242, v70\n" \
"v_cvt_pk_bf16_f32 v66, v68, v69\n" \
"v_mfma_f32_16x16x32_bf16 a[4:7], a[200:203], v[0:3], a[4:7]\n" \
"v_exp_f32 v71, v71\n" \
"v_mfma_f32_16x16x32_bf16 a[4:7], a[204:207], v[32:35], a[4:7]\n" \
"ds_read_b128 a[200:203], v246 offset:4096\n" \
"global_load_lds_dwordx4 v250, s[36:37] offset:1024 sc1\n" \
"v_add_f32 v242, v242, v71\n" \
"v_mfma_f32_16x16x32_bf16 a[8:11], a[208:211], v[0:3], a[8:11]\n" \
"v_exp_f32 v72, v72\n" \
"v_mfma_f32_16x16x32_bf16 a[8:11], a[212:215], v[32:35], a[8:11]\n" \
"v_add_f32 v243, v243, v72\n" \
"v_cvt_pk_bf16_f32 v67, v70, v71\n" \
"v_mfma_f32_16x16x32_bf16 a[40:43], a[208:211], v[8:11], a[40:43]\n" \
"v_exp_f32 v73, v73\n" \
"v_mfma_f32_16x16x32_bf16 a[40:43], a[212:215], v[40:43], a[40:43]\n" \
"ds_read_b128 a[204:207], v246 offset:6144\n" \
"v_add_f32 v243, v243, v73\n" \
"v_mfma_f32_16x16x32_bf16 v[136:139], a[208:211], v[16:19], v[136:139]\n" \
"v_exp_f32 v74, v74\n" \
"v_mfma_f32_16x16x32_bf16 v[136:139], a[212:215], v[48:51], v[136:139]\n" \
"v_add_f32 v243, v243, v74\n" \
"v_cvt_pk_bf16_f32 v72, v72, v73\n" \
"v_mfma_f32_16x16x32_bf16 v[168:171], a[208:211], v[24:27], v[168:171]\n" \
"v_exp_f32 v75, v75\n" \
"v_mfma_f32_16x16x32_bf16 v[168:171], a[212:215], v[56:59], v[168:171]\n" \
"ds_read_b128 a[208:211], v246 offset:256\n" \
"v_add_f32 v243, v243, v75\n" \
"v_mfma_f32_16x16x32_bf16 v[172:175], a[216:219], v[24:27], v[172:175]\n" \
"v_exp_f32 v76, v76\n" \
"v_mfma_f32_16x16x32_bf16 v[172:175], a[220:223], v[56:59], v[172:175]\n" \
"v_add_f32 v243, v243, v76\n" \
"v_cvt_pk_bf16_f32 v73, v74, v75\n" \
"v_mfma_f32_16x16x32_bf16 v[140:143], a[216:219], v[16:19], v[140:143]\n" \
"v_exp_f32 v77, v77\n" \
"v_mfma_f32_16x16x32_bf16 v[140:143], a[220:223], v[48:51], v[140:143]\n" \
"ds_read_b128 a[212:215], v246 offset:2304\n" \
"global_load_lds_dwordx4 v250, s[36:37] offset:2048 sc1\n" \
"v_add_f32 v243, v243, v77\n" \
"v_mfma_f32_16x16x32_bf16 a[44:47], a[216:219], v[8:11], a[44:47]\n" \
"v_exp_f32 v78, v78\n" \
"v_mfma_f32_16x16x32_bf16 a[44:47], a[220:223], v[40:43], a[44:47]\n" \
"v_add_f32 v243, v243, v78\n" \
"v_cvt_pk_bf16_f32 v74, v76, v77\n" \
"v_mfma_f32_16x16x32_bf16 a[12:15], a[216:219], v[0:3], a[12:15]\n" \
"s_waitcnt lgkmcnt(6)\n" \
"v_exp_f32 v79, v79\n" \
"v_mfma_f32_16x16x32_bf16 a[12:15], a[220:223], v[32:35], a[12:15]\n" \
"ds_read_b128 a[216:219], v246 offset:4352\n" \
"v_add_f32 v243, v243, v79\n" \
"v_mfma_f32_16x16x32_bf16 a[16:19], a[224:227], v[0:3], a[16:19]\n" \
"v_exp_f32 v80, v80\n" \
"v_mfma_f32_16x16x32_bf16 a[16:19], a[228:231], v[32:35], a[16:19]\n" \
"v_add_f32 v244, v244, v80\n" \
"v_cvt_pk_bf16_f32 v75, v78, v79\n" \
"v_mfma_f32_16x16x32_bf16 a[48:51], a[224:227], v[8:11], a[48:51]\n" \
"v_exp_f32 v81, v81\n" \
"v_mfma_f32_16x16x32_bf16 a[48:51], a[228:231], v[40:43], a[48:51]\n" \
"ds_read_b128 a[220:223], v246 offset:6400\n" \
"v_add_f32 v244, v244, v81\n" \
"v_mfma_f32_16x16x32_bf16 v[144:147], a[224:227], v[16:19], v[144:147]\n" \
"v_exp_f32 v82, v82\n" \
"v_mfma_f32_16x16x32_bf16 v[144:147], a[228:231], v[48:51], v[144:147]\n" \
"v_add_f32 v244, v244, v82\n" \
"v_cvt_pk_bf16_f32 v80, v80, v81\n" \
"v_mfma_f32_16x16x32_bf16 v[176:179], a[224:227], v[24:27], v[176:179]\n" \
"v_exp_f32 v83, v83\n" \
"v_mfma_f32_16x16x32_bf16 v[176:179], a[228:231], v[56:59], v[176:179]\n" \
"ds_read_b128 a[224:227], v246 offset:8192\n" \
"global_load_lds_dwordx4 v250, s[36:37] offset:3072 sc1\n" \
"v_add_f32 v244, v244, v83\n" \
"v_mfma_f32_16x16x32_bf16 v[180:183], a[232:235], v[24:27], v[180:183]\n" \
"s_add_u32 s36, s36, 17408\n" \
"v_exp_f32 v84, v84\n" \
"v_mfma_f32_16x16x32_bf16 v[180:183], a[236:239], v[56:59], v[180:183]\n" \
"v_add_f32 v244, v244, v84\n" \
"v_cvt_pk_bf16_f32 v81, v82, v83\n" \
"v_mfma_f32_16x16x32_bf16 v[148:151], a[232:235], v[16:19], v[148:151]\n" \
"v_exp_f32 v85, v85\n" \
"v_mfma_f32_16x16x32_bf16 v[148:151], a[236:239], v[48:51], v[148:151]\n" \
"ds_read_b128 a[228:231], v246 offset:10240\n" \
"v_add_f32 v244, v244, v85\n" \
"v_mfma_f32_16x16x32_bf16 a[52:55], a[232:235], v[8:11], a[52:55]\n" \
"v_exp_f32 v86, v86\n" \
"v_mfma_f32_16x16x32_bf16 a[52:55], a[236:239], v[40:43], a[52:55]\n" \
"v_add_f32 v244, v244, v86\n" \
"v_cvt_pk_bf16_f32 v82, v84, v85\n" \
"v_mfma_f32_16x16x32_bf16 a[20:23], a[232:235], v[0:3], a[20:23]\n" \
"v_exp_f32 v87, v87\n" \
"v_mfma_f32_16x16x32_bf16 a[20:23], a[236:239], v[32:35], a[20:23]\n" \
"ds_read_b128 a[232:235], v246 offset:12288\n" \
"v_add_f32 v244, v244, v87\n" \
"v_mfma_f32_16x16x32_bf16 a[24:27], a[240:243], v[0:3], a[24:27]\n" \
"v_exp_f32 v88, v88\n" \
"v_mfma_f32_16x16x32_bf16 a[24:27], a[244:247], v[32:35], a[24:27]\n" \
"v_add_f32 v245, v245, v88\n" \
"v_cvt_pk_bf16_f32 v83, v86, v87\n" \
"v_mfma_f32_16x16x32_bf16 a[56:59], a[240:243], v[8:11], a[56:59]\n" \
"v_exp_f32 v89, v89\n" \
"v_mfma_f32_16x16x32_bf16 a[56:59], a[244:247], v[40:43], a[56:59]\n" \
"ds_read_b128 a[236:239], v246 offset:14336\n" \
"v_add_f32 v245, v245, v89\n" \
"v_mfma_f32_16x16x32_bf16 v[152:155], a[240:243], v[16:19], v[152:155]\n" \
"v_exp_f32 v90, v90\n" \
"v_mfma_f32_16x16x32_bf16 v[152:155], a[244:247], v[48:51], v[152:155]\n" \
"v_add_f32 v245, v245, v90\n" \
"v_cvt_pk_bf16_f32 v88, v88, v89\n" \
"v_mfma_f32_16x16x32_bf16 v[184:187], a[240:243], v[24:27], v[184:187]\n" \
"v_exp_f32 v91, v91\n" \
"v_mfma_f32_16x16x32_bf16 v[184:187], a[244:247], v[56:59], v[184:187]\n" \
"ds_read_b128 a[240:243], v246 offset:8448\n" \
"v_add_f32 v245, v245, v91\n" \
"v_mfma_f32_16x16x32_bf16 v[188:191], a[248:251], v[24:27], v[188:191]\n" \
"v_exp_f32 v92, v92\n" \
"v_mfma_f32_16x16x32_bf16 v[188:191], a[252:255], v[56:59], v[188:191]\n" \
"v_add_f32 v245, v245, v92\n" \
"v_cvt_pk_bf16_f32 v89, v90, v91\n" \
"v_mfma_f32_16x16x32_bf16 v[156:159], a[248:251], v[16:19], v[156:159]\n" \
"v_exp_f32 v93, v93\n" \
"v_mfma_f32_16x16x32_bf16 v[156:159], a[252:255], v[48:51], v[156:159]\n" \
"ds_read_b128 a[244:247], v246 offset:10496\n" \
"v_add_f32 v245, v245, v93\n" \
"v_mfma_f32_16x16x32_bf16 a[60:63], a[248:251], v[8:11], a[60:63]\n" \
"v_exp_f32 v94, v94\n" \
"v_mfma_f32_16x16x32_bf16 a[60:63], a[252:255], v[40:43], a[60:63]\n" \
"v_add_f32 v245, v245, v94\n" \
"v_cvt_pk_bf16_f32 v90, v92, v93\n" \
"v_mfma_f32_16x16x32_bf16 a[28:31], a[248:251], v[0:3], a[28:31]\n" \
"s_waitcnt lgkmcnt(6)\n" \
"v_exp_f32 v95, v95\n" \
"v_mfma_f32_16x16x32_bf16 a[28:31], a[252:255], v[32:35], a[28:31]\n" \
"ds_read_b128 a[248:251], v246 offset:12544\n" \
"v_add_f32 v245, v245, v95\n" \
"v_mfma_f32_16x16x32_bf16 v[0:3], a[192:195], a[128:131], v[224:227]\n" \
"v_exp_f32 v96, v96\n" \
"v_mfma_f32_16x16x32_bf16 v[0:3], a[196:199], a[132:135], v[0:3]\n" \
"v_cvt_pk_bf16_f32 v91, v94, v95\n" \
"v_add_f32 v242, v242, v96\n" \
"v_mfma_f32_16x16x32_bf16 v[0:3], a[200:203], a[136:139], v[0:3]\n" \
"s_add_u32 m0, s74, 0x4000\n" \
"v_exp_f32 v97, v97\n" \
"v_mfma_f32_16x16x32_bf16 v[0:3], a[204:207], a[140:143], v[0:3]\n" \
"ds_read_b128 a[252:255], v246 offset:14592\n" \
"global_load_lds_dwordx4 v250, s[38:39] offset:0 sc1\n" \
"v_add_f32 v242, v242, v97\n" \
"v_mfma_f32_16x16x32_bf16 v[8:11], a[200:203], a[152:155], v[228:231]\n" \
"v_exp_f32 v98, v98\n" \
"v_mfma_f32_16x16x32_bf16 v[8:11], a[204:207], a[156:159], v[8:11]\n" \
"v_add_f32 v242, v242, v98\n" \
"v_cvt_pk_bf16_f32 v96, v96, v97\n" \
"v_mfma_f32_16x16x32_bf16 v[8:11], a[192:195], a[144:147], v[8:11]\n" \
"v_exp_f32 v99, v99\n" \
"v_mfma_f32_16x16x32_bf16 v[8:11], a[196:199], a[148:151], v[8:11]\n" \
"v_add_f32 v242, v242, v99\n" \
"v_exp_f32 v100, v100\n" \
"v_mfma_f32_16x16x32_bf16 v[16:19], a[192:195], a[160:163], v[232:235]\n" \
"v_add_f32 v242, v242, v100\n" \
"v_cvt_pk_bf16_f32 v97, v98, v99\n" \
"v_mfma_f32_16x16x32_bf16 v[16:19], a[196:199], a[164:167], v[16:19]\n" \
"v_exp_f32 v101, v101\n" \
"v_mfma_f32_16x16x32_bf16 v[16:19], a[200:203], a[168:171], v[16:19]\n" \
"v_add_f32 v242, v242, v101\n" \
"v_mfma_f32_16x16x32_bf16 v[16:19], a[204:207], a[172:175], v[16:19]\n" \
"v_exp_f32 v102, v102\n" \
"v_mfma_f32_16x16x32_bf16 v[24:27], a[200:203], a[184:187], v[236:239]\n" \
"v_add_f32 v242, v242, v102\n" \
"v_cvt_pk_bf16_f32 v98, v100, v101\n" \
"v_mfma_f32_16x16x32_bf16 v[24:27], a[204:207], a[188:191], v[24:27]\n" \
"v_exp_f32 v103, v103\n" \
"v_mfma_f32_16x16x32_bf16 v[24:27], a[192:195], a[176:179], v[24:27]\n" \
"v_add_f32 v242, v242, v103\n" \
"v_mfma_f32_16x16x32_bf16 v[24:27], a[196:199], a[180:183], v[24:27]\n" \
"ds_read_b128 a[192:195], v247 offset:16384\n" \
"global_load_lds_dwordx4 v250, s[38:39] offset:1024 sc1\n" \
"v_mfma_f32_16x16x32_bf16 v[28:31], a[208:211], a[176:179], v[236:239]\n" \
"v_exp_f32 v104, v104\n" \
"v_mfma_f32_16x16x32_bf16 v[28:31], a[212:215], a[180:183], v[28:31]\n" \
"v_add_f32 v243, v243, v104\n" \
"v_cvt_pk_bf16_f32 v99, v102, v103\n" \
"v_mfma_f32_16x16x32_bf16 v[28:31], a[216:219], a[184:187], v[28:31]\n" \
"v_exp_f32 v105, v105\n" \
"v_mfma_f32_16x16x32_bf16 v[28:31], a[220:223], a[188:191], v[28:31]\n" \
"ds_read_b128 a[196:199], v247 offset:24576\n" \
"v_add_f32 v243, v243, v105\n" \
"v_mfma_f32_16x16x32_bf16 v[20:23], a[216:219], a[168:171], v[232:235]\n" \
"v_exp_f32 v106, v106\n" \
"v_mfma_f32_16x16x32_bf16 v[20:23], a[220:223], a[172:175], v[20:23]\n" \
"v_add_f32 v243, v243, v106\n" \
"v_cvt_pk_bf16_f32 v104, v104, v105\n" \
"v_mfma_f32_16x16x32_bf16 v[20:23], a[208:211], a[160:163], v[20:23]\n" \
"v_exp_f32 v107, v107\n" \
"v_mfma_f32_16x16x32_bf16 v[20:23], a[212:215], a[164:167], v[20:23]\n" \
"ds_read_b128 a[200:203], v247 offset:17408\n" \
"v_add_f32 v243, v243, v107\n" \
"v_mfma_f32_16x16x32_bf16 v[12:15], a[208:211], a[144:147], v[228:231]\n" \
"v_exp_f32 v108, v108\n" \
"v_mfma_f32_16x16x32_bf16 v[12:15], a[212:215], a[148:151], v[12:15]\n" \
"v_add_f32 v243, v243, v108\n" \
"v_cvt_pk_bf16_f32 v105, v106, v107\n" \
"v_mfma_f32_16x16x32_bf16 v[12:15], a[216:219], a[152:155], v[12:15]\n" \
"v_exp_f32 v109, v109\n" \
"v_mfma_f32_16x16x32_bf16 v[12:15], a[220:223], a[156:159], v[12:15]\n" \
"ds_read_b128 a[204:207], v247 offset:25600\n" \
"global_load_lds_dwordx4 v250, s[38:39] offset:2048 sc1\n" \
"v_add_f32 v243, v243, v109\n" \
"v_mfma_f32_16x16x32_bf16 v[4:7], a[216:219], a[136:139], v[224:227]\n" \
"v_exp_f32 v110, v110\n" \
"v_mfma_f32_16x16x32_bf16 v[4:7], a[220:223], a[140:143], v[4:7]\n" \
"v_add_f32 v243, v243, v110\n" \
"v_cvt_pk_bf16_f32 v106, v108, v109\n" \
"v_mfma_f32_16x16x32_bf16 v[4:7], a[208:211], a[128:131], v[4:7]\n" \
"s_waitcnt lgkmcnt(4)\n" \
"v_exp_f32 v111, v111\n" \
"v_mfma_f32_16x16x32_bf16 v[4:7], a[212:215], a[132:135], v[4:7]\n" \
"ds_read_b128 a[208:211], v247 offset:18432\n" \
"v_add_f32 v243, v243, v111\n" \
"v_mfma_f32_16x16x32_bf16 v[32:35], a[224:227], a[128:131], v[224:227]\n" \
"v_exp_f32 v112, v112\n" \
"v_mfma_f32_16x16x32_bf16 v[32:35], a[228:231], a[132:135], v[32:35]\n" \
"v_add_f32 v244, v244, v112\n" \
"v_cvt_pk_bf16_f32 v107, v110, v111\n" \
"v_mfma_f32_16x16x32_bf16 v[32:35], a[232:235], a[136:139], v[32:35]\n" \
"v_exp_f32 v113, v113\n" \
"v_mfma_f32_16x16x32_bf16 v[32:35], a[236:239], a[140:143], v[32:35]\n" \
"ds_read_b128 a[212:215], v247 offset:26624\n" \
"v_add_f32 v244, v244, v113\n" \
"v_mfma_f32_16x16x32_bf16 v[40:43], a[232:235], a[152:155], v[228:231]\n" \
"v_exp_f32 v114, v114\n" \
"v_mfma_f32_16x16x32_bf16 v[40:43], a[236:239], a[156:159], v[40:43]\n" \
"v_add_f32 v244, v244, v114\n" \
"v_cvt_pk_bf16_f32 v112, v112, v113\n" \
"v_mfma_f32_16x16x32_bf16 v[40:43], a[224:227], a[144:147], v[40:43]\n" \
"v_exp_f32 v115, v115\n" \
"v_mfma_f32_16x16x32_bf16 v[40:43], a[228:231], a[148:151], v[40:43]\n" \
"ds_read_b128 a[216:219], v247 offset:19456\n" \
"global_load_lds_dwordx4 v250, s[38:39] offset:3072 sc1\n" \
"v_add_f32 v244, v244, v115\n" \
"v_exp_f32 v116, v116\n" \
"v_mfma_f32_16x16x32_bf16 v[48:51], a[224:227], a[160:163], v[232:235]\n" \
"s_add_u32 s38, s38, 17408\n" \
"v_add_f32 v244, v244, v116\n" \
"v_cvt_pk_bf16_f32 v113, v114, v115\n" \
"v_mfma_f32_16x16x32_bf16 v[48:51], a[228:231], a[164:167], v[48:51]\n" \
"global_load_dword v253, v252, s[40:41]\n" \
"v_mfma_f32_16x16x32_bf16 v[48:51], a[232:235], a[168:171], v[48:51]\n" \
"s_sub_u32 s80, s80, 1\n" \
"v_exp_f32 v117, v117\n" \
"v_mfma_f32_16x16x32_bf16 v[48:51], a[236:239], a[172:175], v[48:51]\n" \
"s_cmp_gt_i32 s80, 0\n" \
"ds_read_b128 a[220:223], v247 offset:27648\n" \
"v_add_f32 v244, v244, v117\n" \
"v_mfma_f32_16x16x32_bf16 v[56:59], a[232:235], a[184:187], v[236:239]\n" \
"s_cselect_b32 s81, 0x80000, 0\n" \
"v_exp_f32 v118, v118\n" \
"v_mfma_f32_16x16x32_bf16 v[56:59], a[236:239], a[188:191], v[56:59]\n" \
"s_add_u32 s40, s40, s81\n" \
"v_add_f32 v244, v244, v118\n" \
"v_cvt_pk_bf16_f32 v114, v116, v117\n" \
"v_mfma_f32_16x16x32_bf16 v[56:59], a[224:227], a[176:179], v[56:59]\n" \
"s_addc_u32 s41, s41, 0\n" \
"v_exp_f32 v119, v119\n" \
"v_mfma_f32_16x16x32_bf16 v[56:59], a[228:231], a[180:183], v[56:59]\n" \
"s_load_dwordx8 s[44:51], s[56:57], s79 glc\n" \
"ds_read_b128 a[224:227], v247 offset:20480\n" \
"v_add_f32 v244, v244, v119\n" \
"v_mfma_f32_16x16x32_bf16 v[60:63], a[240:243], a[176:179], v[236:239]\n" \
"s_xor_b32 s74, s74, 0x8000\n" \
"v_exp_f32 v120, v120\n" \
"v_mfma_f32_16x16x32_bf16 v[60:63], a[244:247], a[180:183], v[60:63]\n" \
"s_sub_u32 s75, s75, 1\n" \
"v_add_f32 v245, v245, v120\n" \
"v_cvt_pk_bf16_f32 v115, v118, v119\n" \
"v_mfma_f32_16x16x32_bf16 v[60:63], a[248:251], a[184:187], v[60:63]\n" \
"s_cmp_lg_u32 s75, 0\n" \
"v_exp_f32 v121, v121\n" \
"v_mfma_f32_16x16x32_bf16 v[60:63], a[252:255], a[188:191], v[60:63]\n" \
"ds_read_b128 a[228:231], v247 offset:28672\n" \
"v_add_f32 v245, v245, v121\n" \
"v_mfma_f32_16x16x32_bf16 v[52:55], a[248:251], a[168:171], v[232:235]\n" \
"v_exp_f32 v122, v122\n" \
"v_mfma_f32_16x16x32_bf16 v[52:55], a[252:255], a[172:175], v[52:55]\n" \
"v_add_f32 v245, v245, v122\n" \
"v_cvt_pk_bf16_f32 v120, v120, v121\n" \
"v_mfma_f32_16x16x32_bf16 v[52:55], a[240:243], a[160:163], v[52:55]\n" \
"v_exp_f32 v123, v123\n" \
"v_mfma_f32_16x16x32_bf16 v[52:55], a[244:247], a[164:167], v[52:55]\n" \
"ds_read_b128 a[232:235], v247 offset:21504\n" \
"v_add_f32 v245, v245, v123\n" \
"v_mfma_f32_16x16x32_bf16 v[44:47], a[240:243], a[144:147], v[228:231]\n" \
"v_exp_f32 v124, v124\n" \
"v_mfma_f32_16x16x32_bf16 v[44:47], a[244:247], a[148:151], v[44:47]\n" \
"v_add_f32 v245, v245, v124\n" \
"v_cvt_pk_bf16_f32 v121, v122, v123\n" \
"v_mfma_f32_16x16x32_bf16 v[44:47], a[248:251], a[152:155], v[44:47]\n" \
"v_exp_f32 v125, v125\n" \
"v_mfma_f32_16x16x32_bf16 v[44:47], a[252:255], a[156:159], v[44:47]\n" \
"ds_read_b128 a[236:239], v247 offset:29696\n" \
"v_add_f32 v245, v245, v125\n" \
"v_mfma_f32_16x16x32_bf16 v[36:39], a[248:251], a[136:139], v[224:227]\n" \
"v_exp_f32 v126, v126\n" \
"v_mfma_f32_16x16x32_bf16 v[36:39], a[252:255], a[140:143], v[36:39]\n" \
"v_add_f32 v245, v245, v126\n" \
"v_cvt_pk_bf16_f32 v122, v124, v125\n" \
"v_mfma_f32_16x16x32_bf16 v[36:39], a[240:243], a[128:131], v[36:39]\n" \
"s_waitcnt vmcnt(11) lgkmcnt(4)\n" \
"v_exp_f32 v127, v127\n" \
"v_mfma_f32_16x16x32_bf16 v[36:39], a[244:247], a[132:135], v[36:39]\n" \
"s_barrier\n" \
"ds_read_b128 a[240:243], v247 offset:22528\n" \
"v_add_f32 v245, v245, v127\n" \
"s_cbranch_scc1 Lloop_%=\n" \
"v_mfma_f32_16x16x32_bf16 a[0:3], a[192:195], v[64:67], a[0:3]\n" \
"v_exp_f32 v8, v8\n" \
"v_mfma_f32_16x16x32_bf16 a[0:3], a[196:199], v[96:99], a[0:3]\n" \
"v_cvt_pk_bf16_f32 v123, v126, v127\n" \
"v_exp_f32 v9, v9\n" \
"v_mfma_f32_16x16x32_bf16 a[32:35], a[192:195], v[72:75], a[32:35]\n" \
"s_add_u32 m0, s73, 0x4000\n" \
"ds_read_b128 a[244:247], v247 offset:30720\n" \
"v_exp_f32 v10, v10\n" \
"v_mfma_f32_16x16x32_bf16 a[32:35], a[196:199], v[104:107], a[32:35]\n" \
"ds_read_b128 a[248:251], v247 offset:23552\n" \
"global_load_lds_dwordx4 v250, s[36:37] offset:0 sc1\n" \
"v_mfma_f32_16x16x32_bf16 v[128:131], a[192:195], v[80:83], v[128:131]\n" \
"ds_read_b128 a[252:255], v247 offset:31744\n" \
"v_cvt_pk_bf16_f32 v8, v8, v9\n" \
"v_mfma_f32_16x16x32_bf16 v[128:131], a[196:199], v[112:115], v[128:131]\n" \
"v_exp_f32 v11, v11\n" \
"v_mfma_f32_16x16x32_bf16 v[160:163], a[192:195], v[88:91], v[160:163]\n" \
"v_exp_f32 v12, v12\n" \
"v_mfma_f32_16x16x32_bf16 v[160:163], a[196:199], v[120:123], v[160:163]\n" \
"ds_read_b128 a[192:195], v246 offset:16384\n" \
"v_xor_b32 v247, 0x8000, v247\n" \
"v_cvt_pk_bf16_f32 v9, v10, v11\n" \
"v_mfma_f32_16x16x32_bf16 v[164:167], a[200:203], v[88:91], v[164:167]\n" \
"v_exp_f32 v13, v13\n" \
"v_mfma_f32_16x16x32_bf16 v[164:167], a[204:207], v[120:123], v[164:167]\n" \
"v_exp_f32 v14, v14\n" \
"v_cvt_pk_bf16_f32 v10, v12, v13\n" \
"v_mfma_f32_16x16x32_bf16 v[132:135], a[200:203], v[80:83], v[132:135]\n" \
"v_exp_f32 v15, v15\n" \
"v_mfma_f32_16x16x32_bf16 v[132:135], a[204:207], v[112:115], v[132:135]\n" \
"ds_read_b128 a[196:199], v246 offset:18432\n" \
"v_cmp_gt_i32 vcc, 0, v251\n" \
"v_cmp_gt_i32 s[64:65], 1, v251\n" \
"v_mfma_f32_16x16x32_bf16 a[36:39], a[200:203], v[72:75], a[36:39]\n" \
"v_cmp_gt_i32 s[66:67], 2, v251\n" \
"v_cndmask_b32 v0, v0, v254, vcc\n" \
"v_mfma_f32_16x16x32_bf16 a[36:39], a[204:207], v[104:107], a[36:39]\n" \
"v_cmp_gt_i32 vcc, 3, v251\n" \
"v_cndmask_b32 v1, v1, v254, s[64:65]\n" \
"v_mfma_f32_16x16x32_bf16 a[4:7], a[200:203], v[64:67], a[4:7]\n" \
"v_cmp_gt_i32 s[64:65], 16, v251\n" \
"v_cndmask_b32 v2, v2, v254, s[66:67]\n" \
"v_mfma_f32_16x16x32_bf16 a[4:7], a[204:207], v[96:99], a[4:7]\n" \
"ds_read_b128 a[200:203], v246 offset:20480\n" \
"global_load_lds_dwordx4 v250, s[36:37] offset:1024 sc1\n" \
"v_mfma_f32_16x16x32_bf16 a[8:11], a[208:211], v[64:67], a[8:11]\n" \
"v_cmp_gt_i32 s[66:67], 17, v251\n" \
"v_cndmask_b32 v3, v3, v254, vcc\n" \
"v_mfma_f32_16x16x32_bf16 a[8:11], a[212:215], v[96:99], a[8:11]\n" \
"v_cmp_gt_i32 vcc, 18, v251\n" \
"v_cndmask_b32 v4, v4, v254, s[64:65]\n" \
"v_mfma_f32_16x16x32_bf16 a[40:43], a[208:211], v[72:75], a[40:43]\n" \
"v_cmp_gt_i32 s[64:65], 19, v251\n" \
"v_cndmask_b32 v5, v5, v254, s[66:67]\n" \
"v_mfma_f32_16x16x32_bf16 a[40:43], a[212:215], v[104:107], a[40:43]\n" \
"ds_read_b128 a[204:207], v246 offset:22528\n" \
"v_cmp_gt_i32 s[66:67], 32, v251\n" \
"v_mfma_f32_16x16x32_bf16 v[136:139], a[208:211], v[80:83], v[136:139]\n" \
"v_cndmask_b32 v6, v6, v254, vcc\n" \
"v_cmp_gt_i32 vcc, 33, v251\n" \
"v_mfma_f32_16x16x32_bf16 v[136:139], a[212:215], v[112:115], v[136:139]\n" \
"v_cndmask_b32 v7, v7, v254, s[64:65]\n" \
"v_cmp_gt_i32 s[64:65], 34, v251\n" \
"v_mfma_f32_16x16x32_bf16 v[168:171], a[208:211], v[88:91], v[168:171]\n" \
"v_cndmask_b32 v32, v32, v254, s[66:67]\n" \
"v_cmp_gt_i32 s[66:67], 35, v251\n" \
"v_mfma_f32_16x16x32_bf16 v[168:171], a[212:215], v[120:123], v[168:171]\n" \
"Lpg6171_%=:\n" \
"s_cbranch_execz Lpg7555_%=\n" \
"ds_read_b128 a[208:211], v246 offset:16640\n" \
"v_cndmask_b32 v33, v33, v254, vcc\n" \
"v_mfma_f32_16x16x32_bf16 v[172:175], a[216:219], v[88:91], v[172:175]\n" \
"v_cmp_gt_i32 vcc, 48, v251\n" \
"v_cndmask_b32 v34, v34, v254, s[64:65]\n" \
"v_mfma_f32_16x16x32_bf16 v[172:175], a[220:223], v[120:123], v[172:175]\n" \
"v_cmp_gt_i32 s[64:65], 49, v251\n" \
"v_cndmask_b32 v35, v35, v254, s[66:67]\n" \
"v_mfma_f32_16x16x32_bf16 v[140:143], a[216:219], v[80:83], v[140:143]\n" \
"v_cmp_gt_i32 s[66:67], 50, v251\n" \
"v_cndmask_b32 v36, v36, v254, vcc\n" \
"v_mfma_f32_16x16x32_bf16 v[140:143], a[220:223], v[112:115], v[140:143]\n" \
"ds_read_b128 a[212:215], v246 offset:18688\n" \
"global_load_lds_dwordx4 v250, s[36:37] offset:2048 sc1\n" \
"v_mfma_f32_16x16x32_bf16 a[44:47], a[216:219], v[72:75], a[44:47]\n" \
"v_cmp_gt_i32 vcc, 51, v251\n" \
"v_cndmask_b32 v37, v37, v254, s[64:65]\n" \
"v_mfma_f32_16x16x32_bf16 a[44:47], a[220:223], v[104:107], a[44:47]\n" \
"v_cndmask_b32 v38, v38, v254, s[66:67]\n" \
"v_cndmask_b32 v39, v39, v254, vcc\n" \
"v_mfma_f32_16x16x32_bf16 a[12:15], a[216:219], v[64:67], a[12:15]\n" \
"s_waitcnt lgkmcnt(6)\n" \
"v_exp_f32 v0, v0\n" \
"v_mfma_f32_16x16x32_bf16 a[12:15], a[220:223], v[96:99], a[12:15]\n" \
"ds_read_b128 a[216:219], v246 offset:20736\n" \
"v_cvt_pk_bf16_f32 v11, v14, v15\n" \
"v_mfma_f32_16x16x32_bf16 a[16:19], a[224:227], v[64:67], a[16:19]\n" \
"v_exp_f32 v1, v1\n" \
"v_mfma_f32_16x16x32_bf16 a[16:19], a[228:231], v[96:99], a[16:19]\n" \
"v_exp_f32 v2, v2\n" \
"v_cvt_pk_bf16_f32 v0, v0, v1\n" \
"v_mfma_f32_16x16x32_bf16 a[48:51], a[224:227], v[72:75], a[48:51]\n" \
"v_exp_f32 v3, v3\n" \
"v_mfma_f32_16x16x32_bf16 a[48:51], a[228:231], v[104:107], a[48:51]\n" \
"ds_read_b128 a[220:223], v246 offset:22784\n" \
"global_load_lds_dwordx4 v250, s[36:37] offset:3072 sc1\n" \
"v_mfma_f32_16x16x32_bf16 v[144:147], a[224:227], v[80:83], v[144:147]\n" \
"s_add_u32 s36, s36, 17408\n" \
"v_exp_f32 v4, v4\n" \
"v_mfma_f32_16x16x32_bf16 v[144:147], a[228:231], v[112:115], v[144:147]\n" \
"s_xor_b32 s73, s73, 0x8000\n" \
"v_cvt_pk_bf16_f32 v1, v2, v3\n" \
"v_exp_f32 v5, v5\n" \
"v_mfma_f32_16x16x32_bf16 v[176:179], a[224:227], v[88:91], v[176:179]\n" \
"s_xor_b64 s[52:53], s[44:45], s[42:43]\n" \
"v_exp_f32 v6, v6\n" \
"v_mfma_f32_16x16x32_bf16 v[176:179], a[228:231], v[120:123], v[176:179]\n" \
"s_xor_b64 s[54:55], s[46:47], s[42:43]\n" \
"ds_read_b128 a[224:227], v246 offset:24576\n" \
"v_cvt_pk_bf16_f32 v2, v4, v5\n" \
"v_mfma_f32_16x16x32_bf16 v[180:183], a[232:235], v[88:91], v[180:183]\n" \
"s_or_b64 s[52:53], s[52:53], s[54:55]\n" \
"v_exp_f32 v7, v7\n" \
"v_mfma_f32_16x16x32_bf16 v[180:183], a[236:239], v[120:123], v[180:183]\n" \
"s_xor_b64 s[54:55], s[48:49], s[42:43]\n" \
"v_exp_f32 v16, v16\n" \
"v_cvt_pk_bf16_f32 v3, v6, v7\n" \
"v_mfma_f32_16x16x32_bf16 v[148:151], a[232:235], v[80:83], v[148:151]\n" \
"s_or_b64 s[52:53], s[52:53], s[54:55]\n" \
"v_exp_f32 v17, v17\n" \
"v_mfma_f32_16x16x32_bf16 v[148:151], a[236:239], v[112:115], v[148:151]\n" \
"ds_read_b128 a[228:231], v246 offset:26624\n" \
"s_xor_b64 s[54:55], s[50:51], s[42:43]\n" \
"v_mfma_f32_16x16x32_bf16 a[52:55], a[232:235], v[72:75], a[52:55]\n" \
"s_or_b64 s[52:53], s[52:53], s[54:55]\n" \
"v_exp_f32 v18, v18\n" \
"v_mfma_f32_16x16x32_bf16 a[52:55], a[236:239], v[104:107], a[52:55]\n" \
"v_cvt_pk_bf16_f32 v16, v16, v17\n" \
"v_mfma_f32_16x16x32_bf16 v[240:243], a[124:127], v[8:11], v[240:243]\n" \
"v_exp_f32 v19, v19\n" \
"v_mfma_f32_16x16x32_bf16 v[240:243], a[120:123], v[0:3], v[240:243]\n" \
"s_and_b64 s[52:53], s[52:53], s[8:9]\n" \
"s_cbranch_scc1 Lrs1_%=\n" \
"Lrr1_%=:\n" \
"v_mfma_f32_16x16x32_bf16 a[20:23], a[232:235], v[64:67], a[20:23]\n" \
"s_mov_b64 s[8:9], s[10:11]\n" \
"v_exp_f32 v20, v20\n" \
"v_mfma_f32_16x16x32_bf16 a[20:23], a[236:239], v[96:99], a[20:23]\n" \
"ds_read_b128 a[232:235], v246 offset:28672\n" \
"v_cvt_pk_bf16_f32 v17, v18, v19\n" \
"v_mfma_f32_16x16x32_bf16 a[24:27], a[240:243], v[64:67], a[24:27]\n" \
"v_exp_f32 v21, v21\n" \
"v_mfma_f32_16x16x32_bf16 a[24:27], a[244:247], v[96:99], a[24:27]\n" \
"v_exp_f32 v22, v22\n" \
"v_cvt_pk_bf16_f32 v18, v20, v21\n" \
"v_mfma_f32_16x16x32_bf16 a[56:59], a[240:243], v[72:75], a[56:59]\n" \
"v_exp_f32 v23, v23\n" \
"v_mfma_f32_16x16x32_bf16 a[56:59], a[244:247], v[104:107], a[56:59]\n" \
"ds_read_b128 a[236:239], v246 offset:30720\n" \
"v_exp_f32 v24, v24\n" \
"v_mfma_f32_16x16x32_bf16 v[152:155], a[240:243], v[80:83], v[152:155]\n" \
"v_cvt_pk_bf16_f32 v19, v22, v23\n" \
"v_mfma_f32_16x16x32_bf16 v[152:155], a[244:247], v[112:115], v[152:155]\n" \
"v_exp_f32 v25, v25\n" \
"v_mfma_f32_16x16x32_bf16 v[184:187], a[240:243], v[88:91], v[184:187]\n" \
"v_exp_f32 v26, v26\n" \
"v_mfma_f32_16x16x32_bf16 v[184:187], a[244:247], v[120:123], v[184:187]\n" \
"ds_read_b128 a[240:243], v246 offset:24832\n" \
"v_cvt_pk_bf16_f32 v24, v24, v25\n" \
"v_mfma_f32_16x16x32_bf16 v[242:245], a[120:123], v[16:19], v[242:245]\n" \
"v_exp_f32 v27, v27\n" \
"v_mfma_f32_16x16x32_bf16 v[188:191], a[248:251], v[88:91], v[188:191]\n" \
"v_exp_f32 v28, v28\n" \
"v_mfma_f32_16x16x32_bf16 v[188:191], a[252:255], v[120:123], v[188:191]\n" \
"v_cvt_pk_bf16_f32 v25, v26, v27\n" \
"v_exp_f32 v29, v29\n" \
"v_mfma_f32_16x16x32_bf16 v[156:159], a[248:251], v[80:83], v[156:159]\n" \
"v_exp_f32 v30, v30\n" \
"v_mfma_f32_16x16x32_bf16 v[156:159], a[252:255], v[112:115], v[156:159]\n" \
"ds_read_b128 a[244:247], v246 offset:26880\n" \
"v_cvt_pk_bf16_f32 v26, v28, v29\n" \
"v_mfma_f32_16x16x32_bf16 a[60:63], a[248:251], v[72:75], a[60:63]\n" \
"v_exp_f32 v31, v31\n" \
"v_mfma_f32_16x16x32_bf16 a[60:63], a[252:255], v[104:107], a[60:63]\n" \
"v_cvt_pk_bf16_f32 v27, v30, v31\n" \
"v_mfma_f32_16x16x32_bf16 a[28:31], a[248:251], v[64:67], a[28:31]\n" \
"s_waitcnt lgkmcnt(6)\n" \
"v_mfma_f32_16x16x32_bf16 v[242:245], a[124:127], v[24:27], v[242:245]\n" \
"v_mfma_f32_16x16x32_bf16 a[28:31], a[252:255], v[96:99], a[28:31]\n" \
"ds_read_b128 a[248:251], v246 offset:28928\n" \
"LA1l_%=:\n" \
"Lpg6293_%=:\n" \
"s_cbranch_execz Lpg13477_%=\n" \
"s_getpc_b64 s[40:41]\n" \
"Lpd_%=:\n" \
"s_lshl_b32 s80, s62, 2\n" \
"s_add_u32 s80, s80, s61\n" \
"s_and_b32 s80, s80, 15\n" \
"s_lshl_b32 s80, s80, 12\n" \
"s_mov_b32 s81, Lcode_end_%=-Lpd_%=-4\n" \
"v_lshl_add_u32 v252, v248, 2, s80\n" \
"v_min_u32 v252, s81, v252\n" \
"s_waitcnt lgkmcnt(0)\n" \
"v_mfma_f32_16x16x32_bf16 v[72:75], a[192:195], a[144:147], v[228:231]\n" \
"v_exp_f32 v32, v32\n" \
"v_mfma_f32_16x16x32_bf16 v[72:75], a[196:199], a[148:151], v[72:75]\n" \
"v_cvt_pk_bf16_f32 v27, v30, v31\n" \
"v_mfma_f32_16x16x32_bf16 v[72:75], a[200:203], a[152:155], v[72:75]\n" \
"s_mov_b32 m0, s74\n" \
"v_exp_f32 v33, v33\n" \
"v_mfma_f32_16x16x32_bf16 v[72:75], a[204:207], a[156:159], v[72:75]\n" \
"ds_read_b128 a[252:255], v246 offset:30976\n" \
"global_load_lds_dwordx4 v250, s[38:39] offset:0 sc1\n" \
"v_mfma_f32_16x16x32_bf16 v[80:83], a[200:203], a[168:171], v[232:235]\n" \
"s_lshl_b32 s70, s95, 1\n" \
"v_exp_f32 v34, v34\n" \
"v_mfma_f32_16x16x32_bf16 v[80:83], a[204:207], a[172:175], v[80:83]\n" \
"s_xor_b32 s70, s70, 139264\n" \
"v_cvt_pk_bf16_f32 v32, v32, v33\n" \
"v_add_u32 v64, s70, v248\n" \
"v_mfma_f32_16x16x32_bf16 v[80:83], a[192:195], a[160:163], v[80:83]\n" \
"ds_read_b128 v[192:195], v64 offset:0\n" \
"v_exp_f32 v35, v35\n" \
"v_mfma_f32_16x16x32_bf16 v[80:83], a[196:199], a[164:167], v[80:83]\n" \
"ds_read_b128 v[196:199], v64 offset:1024\n" \
"v_exp_f32 v36, v36\n" \
"v_mfma_f32_16x16x32_bf16 v[88:91], a[192:195], a[176:179], v[236:239]\n" \
"ds_read_b128 v[200:203], v64 offset:2048\n" \
"v_cvt_pk_bf16_f32 v33, v34, v35\n" \
"v_mfma_f32_16x16x32_bf16 v[88:91], a[196:199], a[180:183], v[88:91]\n" \
"ds_read_b128 v[204:207], v64 offset:3072\n" \
"v_exp_f32 v37, v37\n" \
"v_exp_f32 v38, v38\n" \
"v_mfma_f32_16x16x32_bf16 v[88:91], a[200:203], a[184:187], v[88:91]\n" \
"ds_read_b128 v[208:211], v64 offset:4096\n" \
"v_cvt_pk_bf16_f32 v34, v36, v37\n" \
"v_mfma_f32_16x16x32_bf16 v[88:91], a[204:207], a[188:191], v[88:91]\n" \
"ds_read_b128 a[192:195], v247 offset:0\n" \
"v_exp_f32 v39, v39\n" \
"v_mfma_f32_16x16x32_bf16 v[92:95], a[216:219], a[184:187], v[236:239]\n" \
"ds_read_b128 v[212:215], v64 offset:5120\n" \
"global_load_lds_dwordx4 v250, s[38:39] offset:1024 sc1\n" \
"v_mfma_f32_16x16x32_bf16 v[92:95], a[220:223], a[188:191], v[92:95]\n" \
"ds_read_b128 v[216:219], v64 offset:6144\n" \
"v_exp_f32 v40, v40\n" \
"v_mfma_f32_16x16x32_bf16 v[92:95], a[208:211], a[176:179], v[92:95]\n" \
"ds_read_b128 a[196:199], v247 offset:8192\n" \
"v_cvt_pk_bf16_f32 v35, v38, v39\n" \
"v_mfma_f32_16x16x32_bf16 v[92:95], a[212:215], a[180:183], v[92:95]\n" \
"ds_read_b128 v[220:223], v64 offset:7168\n" \
"v_exp_f32 v41, v41\n" \
"v_mfma_f32_16x16x32_bf16 v[84:87], a[208:211], a[160:163], v[232:235]\n" \
"ds_write_b128 v64, v[232:235]\n" \
"v_exp_f32 v42, v42\n" \
"v_mfma_f32_16x16x32_bf16 v[84:87], a[212:215], a[164:167], v[84:87]\n" \
"ds_read_b128 a[200:203], v247 offset:1024\n" \
"v_cvt_pk_bf16_f32 v40, v40, v41\n" \
"v_mfma_f32_16x16x32_bf16 v[84:87], a[216:219], a[168:171], v[84:87]\n" \
"v_exp_f32 v43, v43\n" \
"v_mfma_f32_16x16x32_bf16 v[84:87], a[220:223], a[172:175], v[84:87]\n" \
"global_load_lds_dwordx4 v250, s[38:39] offset:2048 sc1\n" \
"ds_write_b128 v64, v[236:239] offset:1024\n" \
"v_exp_f32 v44, v44\n" \
"v_mfma_f32_16x16x32_bf16 v[76:79], a[216:219], a[152:155], v[228:231]\n" \
"ds_read_b128 a[204:207], v247 offset:9216\n" \
"v_xor_b32 v65, 0x2000, v64\n" \
"v_mfma_f32_16x16x32_bf16 v[76:79], a[220:223], a[156:159], v[76:79]\n" \
"v_xor_b32 v246, 0x8000, v246\n" \
"v_add_u32 v246, s82, v246\n" \
"v_mfma_f32_16x16x32_bf16 v[76:79], a[208:211], a[144:147], v[76:79]\n" \
"s_waitcnt lgkmcnt(4)\n" \
"v_mov_b32 v66, s60\n" \
"v_lshlrev_b32 v67, 28, s61\n" \
"v_mfma_f32_16x16x32_bf16 v[76:79], a[212:215], a[148:151], v[76:79]\n" \
"ds_read_b128 a[208:211], v247 offset:2048\n" \
"v_cvt_pk_bf16_f32 v41, v42, v43\n" \
"v_mfma_f32_16x16x32_bf16 v[104:107], a[224:227], a[144:147], v[228:231]\n" \
"v_exp_f32 v45, v45\n" \
"v_mfma_f32_16x16x32_bf16 v[104:107], a[228:231], a[148:151], v[104:107]\n" \
"v_exp_f32 v46, v46\n" \
"v_mfma_f32_16x16x32_bf16 v[104:107], a[232:235], a[152:155], v[104:107]\n" \
"ds_read_b128 a[212:215], v247 offset:10240\n" \
"v_cvt_pk_bf16_f32 v42, v44, v45\n" \
"v_mfma_f32_16x16x32_bf16 v[104:107], a[236:239], a[156:159], v[104:107]\n" \
"v_exp_f32 v47, v47\n" \
"v_mfma_f32_16x16x32_bf16 v[112:115], a[232:235], a[168:171], v[232:235]\n" \
"v_exp_f32 v48, v48\n" \
"v_cvt_pk_bf16_f32 v43, v46, v47\n" \
"v_mfma_f32_16x16x32_bf16 v[112:115], a[236:239], a[172:175], v[112:115]\n" \
"ds_read_b128 a[216:219], v247 offset:3072\n" \
"global_load_lds_dwordx4 v250, s[38:39] offset:3072 sc1\n" \
"v_mfma_f32_16x16x32_bf16 v[112:115], a[224:227], a[160:163], v[112:115]\n" \
"v_exp_f32 v49, v49\n" \
"v_mfma_f32_16x16x32_bf16 v[112:115], a[228:231], a[164:167], v[112:115]\n" \
"v_exp_f32 v50, v50\n" \
"v_cvt_pk_bf16_f32 v48, v48, v49\n" \
"v_mfma_f32_16x16x32_bf16 v[240:243], a[120:123], v[32:35], v[240:243]\n" \
"v_exp_f32 v51, v51\n" \
"v_mfma_f32_16x16x32_bf16 v[240:243], a[124:127], v[40:43], v[240:243]\n" \
"s_add_u32 s38, s38, 17408\n" \
"v_exp_f32 v52, v52\n" \
"v_mfma_f32_16x16x32_bf16 v[120:123], a[224:227], a[176:179], v[236:239]\n" \
"ds_read_b128 a[220:223], v247 offset:11264\n" \
"v_cvt_pk_bf16_f32 v49, v50, v51\n" \
"v_mfma_f32_16x16x32_bf16 v[120:123], a[228:231], a[180:183], v[120:123]\n" \
"v_exp_f32 v53, v53\n" \
"v_mfma_f32_16x16x32_bf16 v[120:123], a[232:235], a[184:187], v[120:123]\n" \
"v_exp_f32 v54, v54\n" \
"v_mfma_f32_16x16x32_bf16 v[120:123], a[236:239], a[188:191], v[120:123]\n" \
"ds_read_b128 a[224:227], v247 offset:4096\n" \
"v_cvt_pk_bf16_f32 v50, v52, v53\n" \
"v_exp_f32 v55, v55\n" \
"v_mfma_f32_16x16x32_bf16 v[124:127], a[248:251], a[184:187], v[236:239]\n" \
"global_load_dword v253, v252, s[40:41]\n" \
"v_mfma_f32_16x16x32_bf16 v[124:127], a[252:255], a[188:191], v[124:127]\n" \
"v_exp_f32 v56, v56\n" \
"v_mfma_f32_16x16x32_bf16 v[124:127], a[240:243], a[176:179], v[124:127]\n" \
"ds_read_b128 a[228:231], v247 offset:12288\n" \
"v_cvt_pk_bf16_f32 v51, v54, v55\n" \
"v_mfma_f32_16x16x32_bf16 v[124:127], a[244:247], a[180:183], v[124:127]\n" \
"v_exp_f32 v57, v57\n" \
"v_exp_f32 v58, v58\n" \
"v_cvt_pk_bf16_f32 v56, v56, v57\n" \
"v_mfma_f32_16x16x32_bf16 v[242:245], a[120:123], v[48:51], v[242:245]\n" \
"v_exp_f32 v59, v59\n" \
"v_mfma_f32_16x16x32_bf16 v[116:119], a[240:243], a[160:163], v[232:235]\n" \
"v_exp_f32 v60, v60\n" \
"v_mfma_f32_16x16x32_bf16 v[116:119], a[244:247], a[164:167], v[116:119]\n" \
"ds_read_b128 a[232:235], v247 offset:5120\n" \
"v_cvt_pk_bf16_f32 v57, v58, v59\n" \
"v_mfma_f32_16x16x32_bf16 v[116:119], a[248:251], a[168:171], v[116:119]\n" \
"v_exp_f32 v61, v61\n" \
"v_mfma_f32_16x16x32_bf16 v[116:119], a[252:255], a[172:175], v[116:119]\n" \
"v_exp_f32 v62, v62\n" \
"v_cvt_pk_bf16_f32 v58, v60, v61\n" \
"v_mfma_f32_16x16x32_bf16 v[108:111], a[248:251], a[152:155], v[228:231]\n" \
"ds_read_b128 a[236:239], v247 offset:13312\n" \
"v_exp_f32 v63, v63\n" \
"v_mfma_f32_16x16x32_bf16 v[108:111], a[252:255], a[156:159], v[108:111]\n" \
"v_cvt_pk_bf16_f32 v59, v62, v63\n" \
"v_mfma_f32_16x16x32_bf16 v[108:111], a[240:243], a[144:147], v[108:111]\n" \
"s_waitcnt vmcnt(5) lgkmcnt(4)\n" \
"v_mfma_f32_16x16x32_bf16 v[242:245], a[124:127], v[56:59], v[242:245]\n" \
"v_mfma_f32_16x16x32_bf16 v[108:111], a[244:247], a[148:151], v[108:111]\n" \
"s_barrier\n" \
"ds_read_b128 a[240:243], v247 offset:6144\n" \
"v_mfma_f32_16x16x32_bf16 a[0:3], a[192:195], v[0:3], a[0:3]\n" \
"buffer_store_dword v66, v67, s[56:59], s84 offen sc1\n" \
"v_mfma_f32_16x16x32_bf16 a[0:3], a[196:199], v[32:35], a[0:3]\n" \
"s_add_u32 s73, s73, s95\n" \
"v_cvt_pk_bf16_f32 v59, v62, v63\n" \
"v_exp_f32 v80, v80\n" \
"v_mfma_f32_16x16x32_bf16 a[32:35], a[192:195], v[8:11], a[32:35]\n" \
"s_add_u32 s36, s36, s83\n" \
"ds_read_b128 a[244:247], v247 offset:14336\n" \
"v_exp_f32 v81, v81\n" \
"v_mfma_f32_16x16x32_bf16 a[32:35], a[196:199], v[40:43], a[32:35]\n" \
"s_add_u32 s73, s73, 4096\n" \
"ds_read_b128 a[248:251], v247 offset:7168\n" \
"v_exp_f32 v82, v82\n" \
"v_mfma_f32_16x16x32_bf16 v[128:131], a[192:195], v[16:19], v[128:131]\n" \
"ds_read_b128 a[252:255], v247 offset:15360\n" \
"v_cvt_pk_bf16_f32 v80, v80, v81\n" \
"v_mfma_f32_16x16x32_bf16 v[128:131], a[196:199], v[48:51], v[128:131]\n" \
"ds_read_b128 v[224:227], v65\n" \
"v_exp_f32 v83, v83\n" \
"v_mfma_f32_16x16x32_bf16 v[160:163], a[192:195], v[24:27], v[160:163]\n" \
"s_mov_b32 m0, s73\n" \
"ds_read_b128 v[228:231], v65 offset:1024\n" \
"v_xor_b32 v252, 0x8000, v246\n" \
"v_mfma_f32_16x16x32_bf16 v[160:163], a[196:199], v[56:59], v[160:163]\n" \
"ds_read_b128 a[192:195], v246 offset:0\n" \
"global_load_lds_dwordx4 v250, s[36:37] offset:-4096 sc1\n" \
"v_mfma_f32_16x16x32_bf16 v[164:167], a[200:203], v[24:27], v[164:167]\n" \
"v_exp_f32 v84, v84\n" \
"v_mfma_f32_16x16x32_bf16 v[164:167], a[204:207], v[56:59], v[164:167]\n" \
"v_cvt_pk_bf16_f32 v81, v82, v83\n" \
"v_exp_f32 v85, v85\n" \
"v_mfma_f32_16x16x32_bf16 v[132:135], a[200:203], v[16:19], v[132:135]\n" \
"v_exp_f32 v86, v86\n" \
"v_mfma_f32_16x16x32_bf16 v[132:135], a[204:207], v[48:51], v[132:135]\n" \
"ds_read_b128 a[196:199], v246 offset:2048\n" \
"global_load_lds_dwordx4 v250, s[36:37] offset:-3072 sc1\n" \
"v_mfma_f32_16x16x32_bf16 a[36:39], a[200:203], v[8:11], a[36:39]\n" \
"v_cvt_pk_bf16_f32 v82, v84, v85\n" \
"v_mfma_f32_16x16x32_bf16 a[36:39], a[204:207], v[40:43], a[36:39]\n" \
"v_exp_f32 v87, v87\n" \
"v_mfma_f32_16x16x32_bf16 a[4:7], a[200:203], v[0:3], a[4:7]\n" \
"v_cmp_gt_i32 vcc, 0, v251\n" \
"v_cmp_gt_i32 s[64:65], 1, v251\n" \
"v_mfma_f32_16x16x32_bf16 a[4:7], a[204:207], v[32:35], a[4:7]\n" \
"ds_read_b128 a[200:203], v246 offset:4096\n" \
"global_load_lds_dwordx4 v250, s[36:37] offset:-2048 sc1\n" \
"v_mfma_f32_16x16x32_bf16 a[8:11], a[208:211], v[0:3], a[8:11]\n" \
"v_cmp_gt_i32 s[66:67], 2, v251\n" \
"v_cndmask_b32 v72, v72, v254, vcc\n" \
"v_mfma_f32_16x16x32_bf16 a[8:11], a[212:215], v[32:35], a[8:11]\n" \
"v_cmp_gt_i32 vcc, 3, v251\n" \
"v_cndmask_b32 v73, v73, v254, s[64:65]\n" \
"v_mfma_f32_16x16x32_bf16 a[40:43], a[208:211], v[8:11], a[40:43]\n" \
"v_cmp_gt_i32 s[64:65], 16, v251\n" \
"v_cndmask_b32 v74, v74, v254, s[66:67]\n" \
"v_mfma_f32_16x16x32_bf16 a[40:43], a[212:215], v[40:43], a[40:43]\n" \
"ds_read_b128 a[204:207], v246 offset:6144\n" \
"global_load_lds_dwordx4 v250, s[36:37] offset:-1024 sc1\n" \
"v_mfma_f32_16x16x32_bf16 v[136:139], a[208:211], v[16:19], v[136:139]\n" \
"v_cmp_gt_i32 s[66:67], 17, v251\n" \
"v_cndmask_b32 v75, v75, v254, vcc\n" \
"v_mfma_f32_16x16x32_bf16 v[136:139], a[212:215], v[48:51], v[136:139]\n" \
"v_cmp_gt_i32 vcc, 18, v251\n" \
"v_cndmask_b32 v76, v76, v254, s[64:65]\n" \
"v_mfma_f32_16x16x32_bf16 v[168:171], a[208:211], v[24:27], v[168:171]\n" \
"v_cmp_gt_i32 s[64:65], 19, v251\n" \
"v_cndmask_b32 v77, v77, v254, s[66:67]\n" \
"v_mfma_f32_16x16x32_bf16 v[168:171], a[212:215], v[56:59], v[168:171]\n" \
"ds_read_b128 a[208:211], v246 offset:256\n" \
"global_load_lds_dwordx4 v250, s[36:37] offset:0 sc1\n" \
"v_mfma_f32_16x16x32_bf16 v[172:175], a[216:219], v[24:27], v[172:175]\n" \
"v_cmp_gt_i32 s[66:67], 32, v251\n" \
"v_cndmask_b32 v78, v78, v254, vcc\n" \
"v_mfma_f32_16x16x32_bf16 v[172:175], a[220:223], v[56:59], v[172:175]\n" \
"v_cmp_gt_i32 vcc, 33, v251\n" \
"v_cndmask_b32 v79, v79, v254, s[64:65]\n" \
"v_mfma_f32_16x16x32_bf16 v[140:143], a[216:219], v[16:19], v[140:143]\n" \
"v_cmp_gt_i32 s[64:65], 34, v251\n" \
"v_cndmask_b32 v104, v104, v254, s[66:67]\n" \
"v_mfma_f32_16x16x32_bf16 v[140:143], a[220:223], v[48:51], v[140:143]\n" \
"ds_read_b128 a[212:215], v246 offset:2304\n" \
"global_load_lds_dwordx4 v250, s[36:37] offset:1024 sc1\n" \
"v_mfma_f32_16x16x32_bf16 a[44:47], a[216:219], v[8:11], a[44:47]\n" \
"v_cmp_gt_i32 s[66:67], 35, v251\n" \
"v_cndmask_b32 v105, v105, v254, vcc\n" \
"v_mfma_f32_16x16x32_bf16 a[44:47], a[220:223], v[40:43], a[44:47]\n" \
"v_cmp_gt_i32 vcc, 48, v251\n" \
"v_cndmask_b32 v106, v106, v254, s[64:65]\n" \
"v_mfma_f32_16x16x32_bf16 a[12:15], a[216:219], v[0:3], a[12:15]\n" \
"s_waitcnt lgkmcnt(6)\n" \
"v_cmp_gt_i32 s[64:65], 49, v251\n" \
"v_cndmask_b32 v107, v107, v254, s[66:67]\n" \
"v_mfma_f32_16x16x32_bf16 a[12:15], a[220:223], v[32:35], a[12:15]\n" \
"ds_read_b128 a[216:219], v246 offset:4352\n" \
"global_load_lds_dwordx4 v250, s[36:37] offset:2048 sc1\n" \
"v_mfma_f32_16x16x32_bf16 a[16:19], a[224:227], v[0:3], a[16:19]\n" \
"v_cmp_gt_i32 s[66:67], 50, v251\n" \
"v_cndmask_b32 v108, v108, v254, vcc\n" \
"v_mfma_f32_16x16x32_bf16 a[16:19], a[228:231], v[32:35], a[16:19]\n" \
"v_cmp_gt_i32 vcc, 51, v251\n" \
"v_cndmask_b32 v109, v109, v254, s[64:65]\n" \
"v_mfma_f32_16x16x32_bf16 a[48:51], a[224:227], v[8:11], a[48:51]\n" \
"v_cndmask_b32 v110, v110, v254, s[66:67]\n" \
"v_cndmask_b32 v111, v111, v254, vcc\n" \
"v_mfma_f32_16x16x32_bf16 a[48:51], a[228:231], v[40:43], a[48:51]\n" \
"ds_read_b128 a[220:223], v246 offset:6400\n" \
"global_load_lds_dwordx4 v250, s[36:37] offset:3072 sc1\n" \
"v_mfma_f32_16x16x32_bf16 v[144:147], a[224:227], v[16:19], v[144:147]\n" \
"s_add_u32 s36, s36, 34816\n" \
"v_exp_f32 v72, v72\n" \
"v_mfma_f32_16x16x32_bf16 v[144:147], a[228:231], v[48:51], v[144:147]\n" \
"v_cvt_pk_bf16_f32 v83, v86, v87\n" \
"v_mfma_f32_16x16x32_bf16 v[176:179], a[224:227], v[24:27], v[176:179]\n" \
"s_add_u32 m0, s74, 0x4000\n" \
"v_exp_f32 v73, v73\n" \
"v_mfma_f32_16x16x32_bf16 v[176:179], a[228:231], v[56:59], v[176:179]\n" \
"ds_read_b128 a[224:227], v246 offset:8192\n" \
"global_load_lds_dwordx4 v250, s[38:39] offset:0 sc1\n" \
"v_mfma_f32_16x16x32_bf16 v[180:183], a[232:235], v[24:27], v[180:183]\n" \
"v_exp_f32 v74, v74\n" \
"v_mfma_f32_16x16x32_bf16 v[180:183], a[236:239], v[56:59], v[180:183]\n" \
"v_cvt_pk_bf16_f32 v72, v72, v73\n" \
"v_mfma_f32_16x16x32_bf16 v[242:245], a[120:123], v[80:83], v[242:245]\n" \
"v_exp_f32 v75, v75\n" \
"v_mfma_f32_16x16x32_bf16 v[148:151], a[232:235], v[16:19], v[148:151]\n" \
"v_exp_f32 v76, v76\n" \
"v_mfma_f32_16x16x32_bf16 v[148:151], a[236:239], v[48:51], v[148:151]\n" \
"ds_read_b128 a[228:231], v246 offset:10240\n" \
"global_load_lds_dwordx4 v250, s[38:39] offset:1024 sc1\n" \
"v_mfma_f32_16x16x32_bf16 a[52:55], a[232:235], v[8:11], a[52:55]\n" \
"v_cvt_pk_bf16_f32 v73, v74, v75\n" \
"v_mfma_f32_16x16x32_bf16 a[52:55], a[236:239], v[40:43], a[52:55]\n" \
"v_exp_f32 v77, v77\n" \
"v_mfma_f32_16x16x32_bf16 a[20:23], a[232:235], v[0:3], a[20:23]\n" \
"v_exp_f32 v78, v78\n" \
"v_mfma_f32_16x16x32_bf16 a[20:23], a[236:239], v[32:35], a[20:23]\n" \
"ds_read_b128 a[232:235], v246 offset:12288\n" \
"v_cvt_pk_bf16_f32 v74, v76, v77\n" \
"v_mfma_f32_16x16x32_bf16 a[24:27], a[240:243], v[0:3], a[24:27]\n" \
"global_load_lds_dwordx4 v250, s[38:39] offset:2048 sc1\n" \
"global_load_lds_dwordx4 v250, s[38:39] offset:3072 sc1\n" \
"v_mfma_f32_16x16x32_bf16 a[24:27], a[244:247], v[32:35], a[24:27]\n" \
"v_exp_f32 v79, v79\n" \
"v_mfma_f32_16x16x32_bf16 a[56:59], a[240:243], v[8:11], a[56:59]\n" \
"s_add_u32 s38, s38, 17408\n" \
"v_exp_f32 v88, v88\n" \
"v_mfma_f32_16x16x32_bf16 a[56:59], a[244:247], v[40:43], a[56:59]\n" \
"s_add_u32 s38, s38, s83\n" \
"ds_read_b128 a[236:239], v246 offset:14336\n" \
"v_cvt_pk_bf16_f32 v75, v78, v79\n" \
"v_mfma_f32_16x16x32_bf16 v[152:155], a[240:243], v[16:19], v[152:155]\n" \
"s_add_u32 s74, s74, s95\n" \
"v_exp_f32 v89, v89\n" \
"v_mfma_f32_16x16x32_bf16 v[152:155], a[244:247], v[48:51], v[152:155]\n" \
"v_exp_f32 v90, v90\n" \
"v_cvt_pk_bf16_f32 v88, v88, v89\n" \
"v_mfma_f32_16x16x32_bf16 v[240:243], a[124:127], v[72:75], v[240:243]\n" \
"s_add_u32 s74, s74, 4096\n" \
"v_exp_f32 v91, v91\n" \
"v_mfma_f32_16x16x32_bf16 v[184:187], a[240:243], v[24:27], v[184:187]\n" \
"s_cmp_eq_u32 s63, 7\n" \
"v_exp_f32 v92, v92\n" \
"v_mfma_f32_16x16x32_bf16 v[184:187], a[244:247], v[56:59], v[184:187]\n" \
"ds_read_b128 a[240:243], v246 offset:8448\n" \
"v_cvt_pk_bf16_f32 v89, v90, v91\n" \
"v_mfma_f32_16x16x32_bf16 v[188:191], a[248:251], v[24:27], v[188:191]\n" \
"v_exp_f32 v93, v93\n" \
"v_mfma_f32_16x16x32_bf16 v[188:191], a[252:255], v[56:59], v[188:191]\n" \
"v_exp_f32 v94, v94\n" \
"v_mfma_f32_16x16x32_bf16 v[156:159], a[248:251], v[16:19], v[156:159]\n" \
"v_cvt_pk_bf16_f32 v90, v92, v93\n" \
"v_mfma_f32_16x16x32_bf16 v[156:159], a[252:255], v[48:51], v[156:159]\n" \
"ds_read_b128 a[244:247], v246 offset:10496\n" \
"v_mfma_f32_16x16x32_bf16 a[60:63], a[248:251], v[8:11], a[60:63]\n" \
"v_exp_f32 v95, v95\n" \
"v_mfma_f32_16x16x32_bf16 a[60:63], a[252:255], v[40:43], a[60:63]\n" \
"v_cvt_pk_bf16_f32 v91, v94, v95\n" \
"v_mfma_f32_16x16x32_bf16 a[28:31], a[248:251], v[0:3], a[28:31]\n" \
"s_waitcnt lgkmcnt(6)\n" \
"v_mfma_f32_16x16x32_bf16 v[242:245], a[124:127], v[88:91], v[242:245]\n" \
"v_mfma_f32_16x16x32_bf16 a[28:31], a[252:255], v[32:35], a[28:31]\n" \
"ds_read_b128 a[248:251], v246 offset:12544\n" \
"s_cbranch_scc1 LF7_%=\n" \
"v_mfma_f32_16x16x32_bf16 v[0:3], a[192:195], v[192:195], v[224:227]\n" \
"s_load_dwordx8 s[44:51], s[56:57], s79 glc\n" \
"v_exp_f32 v104, v104\n" \
"v_mfma_f32_16x16x32_bf16 v[0:3], a[196:199], v[196:199], v[0:3]\n" \
"v_cvt_pk_bf16_f32 v91, v94, v95\n" \
"v_mfma_f32_16x16x32_bf16 v[0:3], a[200:203], v[200:203], v[0:3]\n" \
"v_exp_f32 v105, v105\n" \
"v_mfma_f32_16x16x32_bf16 v[0:3], a[204:207], v[204:207], v[0:3]\n" \
"ds_read_b128 a[252:255], v246 offset:14592\n" \
"v_mov_b32 v78, v242\n" \
"v_mfma_f32_16x16x32_bf16 v[8:11], a[200:203], v[216:219], v[228:231]\n" \
"v_exp_f32 v106, v106\n" \
"v_mfma_f32_16x16x32_bf16 v[8:11], a[204:207], v[220:223], v[8:11]\n" \
"v_cvt_pk_bf16_f32 v104, v104, v105\n" \
"v_mov_b32 v76, v78\n" \
"v_mfma_f32_16x16x32_bf16 v[8:11], a[192:195], v[208:211], v[8:11]\n" \
"v_exp_f32 v107, v107\n" \
"v_mfma_f32_16x16x32_bf16 v[8:11], a[196:199], v[212:215], v[8:11]\n" \
"v_exp_f32 v108, v108\n" \
"v_mfma_f32_16x16x32_bf16 v[16:19], a[192:195], a[160:163], v[232:235]\n" \
"v_cvt_pk_bf16_f32 v105, v106, v107\n" \
"v_mfma_f32_16x16x32_bf16 v[16:19], a[196:199], a[164:167], v[16:19]\n" \
"v_exp_f32 v109, v109\n" \
"v_mfma_f32_16x16x32_bf16 v[16:19], a[200:203], a[168:171], v[16:19]\n" \
"v_exp_f32 v110, v110\n" \
"v_mfma_f32_16x16x32_bf16 v[16:19], a[204:207], a[172:175], v[16:19]\n" \
"v_cvt_pk_bf16_f32 v106, v108, v109\n" \
"v_permlane16_swap_b32 v76, v78\n" \
"v_mfma_f32_16x16x32_bf16 v[24:27], a[200:203], a[184:187], v[236:239]\n" \
"v_exp_f32 v111, v111\n" \
"v_mfma_f32_16x16x32_bf16 v[24:27], a[204:207], a[188:191], v[24:27]\n" \
"v_exp_f32 v112, v112\n" \
"v_mfma_f32_16x16x32_bf16 v[24:27], a[192:195], a[176:179], v[24:27]\n" \
"v_cvt_pk_bf16_f32 v107, v110, v111\n" \
"v_add_f32 v78, v78, v76\n" \
"v_mfma_f32_16x16x32_bf16 v[24:27], a[196:199], a[180:183], v[24:27]\n" \
"ds_read_b128 a[192:195], v247 offset:16384\n" \
"v_mov_b32 v76, v78\n" \
"v_mfma_f32_16x16x32_bf16 v[28:31], a[208:211], a[176:179], v[236:239]\n" \
"v_exp_f32 v113, v113\n" \
"v_mfma_f32_16x16x32_bf16 v[28:31], a[212:215], a[180:183], v[28:31]\n" \
"v_exp_f32 v114, v114\n" \
"v_mfma_f32_16x16x32_bf16 v[28:31], a[216:219], a[184:187], v[28:31]\n" \
"v_cvt_pk_bf16_f32 v112, v112, v113\n" \
"v_mfma_f32_16x16x32_bf16 v[28:31], a[220:223], a[188:191], v[28:31]\n" \
"v_permlane32_swap_b32 v76, v78\n" \
"v_mfma_f32_16x16x32_bf16 v[240:243], a[124:127], v[104:107], v[240:243]\n" \
"ds_read_b128 a[196:199], v247 offset:24576\n" \
"v_exp_f32 v115, v115\n" \
"v_mfma_f32_16x16x32_bf16 v[20:23], a[216:219], a[168:171], v[232:235]\n" \
"v_exp_f32 v116, v116\n" \
"v_mfma_f32_16x16x32_bf16 v[20:23], a[220:223], a[172:175], v[20:23]\n" \
"v_cvt_pk_bf16_f32 v113, v114, v115\n" \
"v_add_f32 v78, v78, v76\n" \
"v_mfma_f32_16x16x32_bf16 v[20:23], a[208:211], a[160:163], v[20:23]\n" \
"v_exp_f32 v117, v117\n" \
"v_mfma_f32_16x16x32_bf16 v[20:23], a[212:215], a[164:167], v[20:23]\n" \
"ds_read_b128 a[200:203], v247 offset:17408\n" \
"v_rcp_f32 v76, v78\n" \
"v_mfma_f32_16x16x32_bf16 v[12:15], a[208:211], v[208:211], v[228:231]\n" \
"v_exp_f32 v118, v118\n" \
"v_mfma_f32_16x16x32_bf16 v[12:15], a[212:215], v[212:215], v[12:15]\n" \
"v_cvt_pk_bf16_f32 v114, v116, v117\n" \
"v_mul_f32 v78, 0x4d800000, v78\n" \
"v_mfma_f32_16x16x32_bf16 v[12:15], a[216:219], v[216:219], v[12:15]\n" \
"v_exp_f32 v119, v119\n" \
"v_mfma_f32_16x16x32_bf16 v[12:15], a[220:223], v[220:223], v[12:15]\n" \
"ds_read_b128 a[204:207], v247 offset:25600\n" \
"v_add_u32 v77, 0x1c90080, v249\n" \
"v_mfma_f32_16x16x32_bf16 v[4:7], a[216:219], v[200:203], v[224:227]\n" \
"v_exp_f32 v120, v120\n" \
"v_mfma_f32_16x16x32_bf16 v[4:7], a[220:223], v[204:207], v[4:7]\n" \
"v_cvt_pk_bf16_f32 v115, v118, v119\n" \
"v_cndmask_b32 v77, v249, v77, s[16:17]\n" \
"v_mfma_f32_16x16x32_bf16 v[4:7], a[208:211], v[192:195], v[4:7]\n" \
"s_waitcnt lgkmcnt(4)\n" \
"v_exp_f32 v121, v121\n" \
"v_mfma_f32_16x16x32_bf16 v[4:7], a[212:215], v[196:199], v[4:7]\n" \
"ds_read_b128 a[208:211], v247 offset:18432\n" \
"v_accvgpr_read_b32 v64, a0\n" \
"v_mfma_f32_16x16x32_bf16 v[32:35], a[224:227], v[192:195], v[224:227]\n" \
"v_exp_f32 v122, v122\n" \
"v_mfma_f32_16x16x32_bf16 v[32:35], a[228:231], v[196:199], v[32:35]\n" \
"v_cvt_pk_bf16_f32 v120, v120, v121\n" \
"v_accvgpr_read_b32 v65, a1\n" \
"v_mfma_f32_16x16x32_bf16 v[32:35], a[232:235], v[200:203], v[32:35]\n" \
"v_accvgpr_read_b32 v66, a2\n" \
"v_accvgpr_read_b32 v67, a3\n" \
"v_mfma_f32_16x16x32_bf16 v[32:35], a[236:239], v[204:207], v[32:35]\n" \
"ds_read_b128 a[212:215], v247 offset:26624\n" \
"v_accvgpr_read_b32 v68, a4\n" \
"v_mfma_f32_16x16x32_bf16 v[242:245], a[120:123], v[112:115], v[242:245]\n" \
"v_exp_f32 v123, v123\n" \
"v_mfma_f32_16x16x32_bf16 v[40:43], a[232:235], v[216:219], v[228:231]\n" \
"v_accvgpr_read_b32 v69, a5\n" \
"v_accvgpr_read_b32 v70, a6\n" \
"v_accvgpr_read_b32 v71, a7\n" \
"v_exp_f32 v124, v124\n" \
"v_pk_mul_f32 v[64:65], v[64:65], v[76:77] op_sel_hi:[1,0]\n" \
"v_pk_mul_f32 v[66:67], v[66:67], v[76:77] op_sel_hi:[1,0]\n" \
"v_pk_mul_f32 v[68:69], v[68:69], v[76:77] op_sel_hi:[1,0]\n" \
"v_pk_mul_f32 v[70:71], v[70:71], v[76:77] op_sel_hi:[1,0]\n" \
"v_mfma_f32_16x16x32_bf16 v[40:43], a[236:239], v[220:223], v[40:43]\n" \
"v_cvt_pk_bf16_f32 v121, v122, v123\n" \
"v_cvt_pk_bf16_f32 v96, v64, v65\n" \
"v_mfma_f32_16x16x32_bf16 v[40:43], a[224:227], v[208:211], v[40:43]\n" \
"v_exp_f32 v125, v125\n" \
"v_mfma_f32_16x16x32_bf16 v[40:43], a[228:231], v[212:215], v[40:43]\n" \
"ds_read_b128 a[216:219], v247 offset:19456\n" \
"v_cvt_pk_bf16_f32 v97, v66, v67\n" \
"v_cvt_pk_bf16_f32 v98, v68, v69\n" \
"v_cvt_pk_bf16_f32 v99, v70, v71\n" \
"v_pk_mul_f32 v[64:65], v[64:65], v[66:67]\n" \
"v_pk_fma_f32 v[64:65], v[68:69], v[70:71], v[64:65]\n" \
"v_mfma_f32_16x16x32_bf16 v[48:51], a[224:227], a[160:163], v[232:235]\n" \
"v_exp_f32 v126, v126\n" \
"v_mfma_f32_16x16x32_bf16 v[48:51], a[228:231], a[164:167], v[48:51]\n" \
"v_cvt_pk_bf16_f32 v122, v124, v125\n" \
"v_fma_f32 v78, v64, v65, v78\n" \
"v_mfma_f32_16x16x32_bf16 v[48:51], a[232:235], a[168:171], v[48:51]\n" \
"v_exp_f32 v127, v127\n" \
"v_mfma_f32_16x16x32_bf16 v[48:51], a[236:239], a[172:175], v[48:51]\n" \
"buffer_store_dwordx4 v[96:99], v77, s[56:59], s77 offen offset:0 sc1\n" \
"ds_read_b128 a[220:223], v247 offset:27648\n" \
"v_cvt_pk_bf16_f32 v123, v126, v127\n" \
"v_accvgpr_read_b32 v64, a8\n" \
"v_accvgpr_read_b32 v65, a9\n" \
"v_mfma_f32_16x16x32_bf16 v[56:59], a[232:235], a[184:187], v[236:239]\n" \
"s_xor_b64 s[52:53], s[44:45], s[42:43]\n" \
"v_accvgpr_read_b32 v66, a10\n" \
"v_accvgpr_read_b32 v67, a11\n" \
"v_mfma_f32_16x16x32_bf16 v[56:59], a[236:239], a[188:191], v[56:59]\n" \
"s_xor_b64 s[54:55], s[46:47], s[42:43]\n" \
"v_accvgpr_read_b32 v68, a12\n" \
"v_accvgpr_read_b32 v69, a13\n" \
"v_accvgpr_read_b32 v70, a14\n" \
"v_accvgpr_read_b32 v71, a15\n" \
"v_pk_mul_f32 v[64:65], v[64:65], v[76:77] op_sel_hi:[1,0]\n" \
"v_pk_mul_f32 v[66:67], v[66:67], v[76:77] op_sel_hi:[1,0]\n" \
"v_pk_mul_f32 v[68:69], v[68:69], v[76:77] op_sel_hi:[1,0]\n" \
"Lpg6771_%=:\n" \
"s_cbranch_execz Lcb0_%=\n" \
"v_pk_mul_f32 v[70:71], v[70:71], v[76:77] op_sel_hi:[1,0]\n" \
"v_cvt_pk_bf16_f32 v100, v64, v65\n" \
"v_cvt_pk_bf16_f32 v101, v66, v67\n" \
"v_cvt_pk_bf16_f32 v102, v68, v69\n" \
"v_cvt_pk_bf16_f32 v103, v70, v71\n" \
"v_pk_mul_f32 v[64:65], v[64:65], v[66:67]\n" \
"v_pk_fma_f32 v[64:65], v[68:69], v[70:71], v[64:65]\n" \
"v_mfma_f32_16x16x32_bf16 v[56:59], a[224:227], a[176:179], v[56:59]\n" \
"v_fma_f32 v78, v64, v65, v78\n" \
"s_or_b64 s[52:53], s[52:53], s[54:55]\n" \
"v_mfma_f32_16x16x32_bf16 v[56:59], a[228:231], a[180:183], v[56:59]\n" \
"ds_read_b128 a[224:227], v247 offset:20480\n" \
"v_mfma_f32_16x16x32_bf16 v[242:245], a[124:127], v[120:123], v[242:245]\n" \
"buffer_store_dwordx4 v[100:103], v77, s[56:59], s77 offen offset:2048 sc1\n" \
"s_xor_b64 s[54:55], s[48:49], s[42:43]\n" \
"s_add_u32 s77, s77, 0x1000\n" \
"v_accvgpr_read_b32 v64, a16\n" \
"v_accvgpr_read_b32 v65, a17\n" \
"v_mfma_f32_16x16x32_bf16 v[60:63], a[240:243], a[176:179], v[236:239]\n" \
"s_or_b64 s[52:53], s[52:53], s[54:55]\n" \
"v_accvgpr_read_b32 v66, a18\n" \
"v_accvgpr_read_b32 v67, a19\n" \
"v_mfma_f32_16x16x32_bf16 v[60:63], a[244:247], a[180:183], v[60:63]\n" \
"s_xor_b64 s[54:55], s[50:51], s[42:43]\n" \
"v_accvgpr_read_b32 v68, a20\n" \
"v_accvgpr_read_b32 v69, a21\n" \
"v_accvgpr_read_b32 v70, a22\n" \
"v_accvgpr_read_b32 v71, a23\n" \
"v_pk_mul_f32 v[64:65], v[64:65], v[76:77] op_sel_hi:[1,0]\n" \
"v_pk_mul_f32 v[66:67], v[66:67], v[76:77] op_sel_hi:[1,0]\n" \
"v_pk_mul_f32 v[68:69], v[68:69], v[76:77] op_sel_hi:[1,0]\n" \
"v_pk_mul_f32 v[70:71], v[70:71], v[76:77] op_sel_hi:[1,0]\n" \
"v_mfma_f32_16x16x32_bf16 v[60:63], a[248:251], a[184:187], v[60:63]\n" \
"v_cvt_pk_bf16_f32 v96, v64, v65\n" \
"v_cvt_pk_bf16_f32 v97, v66, v67\n" \
"v_cvt_pk_bf16_f32 v98, v68, v69\n" \
"v_cvt_pk_bf16_f32 v99, v70, v71\n" \
"s_or_b64 s[52:53], s[52:53], s[54:55]\n" \
"v_pk_mul_f32 v[64:65], v[64:65], v[66:67]\n" \
"v_pk_fma_f32 v[64:65], v[68:69], v[70:71], v[64:65]\n" \
"v_mfma_f32_16x16x32_bf16 v[60:63], a[252:255], a[188:191], v[60:63]\n" \
"ds_read_b128 a[228:231], v247 offset:28672\n" \
"v_fma_f32 v78, v64, v65, v78\n" \
"v_mfma_f32_16x16x32_bf16 v[52:55], a[248:251], a[168:171], v[232:235]\n" \
"buffer_store_dwordx4 v[96:99], v77, s[56:59], s77 offen offset:0 sc1\n" \
"s_and_b64 s[52:53], s[52:53], s[8:9]\n" \
"s_cbranch_scc1 Lrs2_%=\n" \
"Lrr2_%=:\n" \
"v_mfma_f32_16x16x32_bf16 v[52:55], a[252:255], a[172:175], v[52:55]\n" \
"s_mov_b64 s[8:9], s[10:11]\n" \
"v_accvgpr_read_b32 v64, a24\n" \
"v_accvgpr_read_b32 v65, a25\n" \
"v_mfma_f32_16x16x32_bf16 v[52:55], a[240:243], a[160:163], v[52:55]\n" \
"v_accvgpr_read_b32 v66, a26\n" \
"v_accvgpr_read_b32 v67, a27\n" \
"v_mfma_f32_16x16x32_bf16 v[52:55], a[244:247], a[164:167], v[52:55]\n" \
"ds_read_b128 a[232:235], v247 offset:21504\n" \
"v_accvgpr_read_b32 v68, a28\n" \
"v_accvgpr_read_b32 v69, a29\n" \
"v_accvgpr_read_b32 v70, a30\n" \
"v_accvgpr_read_b32 v71, a31\n" \
"v_pk_mul_f32 v[64:65], v[64:65], v[76:77] op_sel_hi:[1,0]\n" \
"v_pk_mul_f32 v[66:67], v[66:67], v[76:77] op_sel_hi:[1,0]\n" \
"v_pk_mul_f32 v[68:69], v[68:69], v[76:77] op_sel_hi:[1,0]\n" \
"v_pk_mul_f32 v[70:71], v[70:71], v[76:77] op_sel_hi:[1,0]\n" \
"v_mfma_f32_16x16x32_bf16 v[44:47], a[240:243], v[208:211], v[228:231]\n" \
"v_cvt_pk_bf16_f32 v100, v64, v65\n" \
"v_cvt_pk_bf16_f32 v101, v66, v67\n" \
"v_cvt_pk_bf16_f32 v102, v68, v69\n" \
"v_cvt_pk_bf16_f32 v103, v70, v71\n" \
"v_pk_mul_f32 v[64:65], v[64:65], v[66:67]\n" \
"v_pk_fma_f32 v[64:65], v[68:69], v[70:71], v[64:65]\n" \
"v_mfma_f32_16x16x32_bf16 v[44:47], a[244:247], v[212:215], v[44:47]\n" \
"v_fma_f32 v78, v64, v65, v78\n" \
"v_mfma_f32_16x16x32_bf16 v[44:47], a[248:251], v[216:219], v[44:47]\n" \
"v_mfma_f32_16x16x32_bf16 v[44:47], a[252:255], v[220:223], v[44:47]\n" \
"buffer_store_dwordx4 v[100:103], v77, s[56:59], s77 offen offset:2048 sc1\n" \
"ds_read_b128 a[236:239], v247 offset:29696\n" \
"v_cmp_class_f32 vcc, v78, s94\n" \
"s_sub_u32 s77, s77, 4096\n" \
"v_mfma_f32_16x16x32_bf16 v[36:39], a[248:251], v[200:203], v[224:227]\n" \
"s_or_b64 s[92:93], s[92:93], vcc\n" \
"v_mfma_f32_16x16x32_bf16 v[36:39], a[252:255], v[204:207], v[36:39]\n" \
"v_mfma_f32_16x16x32_bf16 v[36:39], a[240:243], v[192:195], v[36:39]\n" \
"s_waitcnt vmcnt(8) lgkmcnt(4)\n" \
"v_mfma_f32_16x16x32_bf16 v[36:39], a[244:247], v[196:199], v[36:39]\n" \
"s_barrier\n" \
"ds_read_b128 a[240:243], v247 offset:22528\n" \
"v_mfma_f32_16x16x32_bf16 a[32:35], a[192:195], v[72:75], a[32:35]\n" \
"v_exp_f32 v0, v0\n" \
"v_mfma_f32_16x16x32_bf16 a[32:35], a[196:199], v[104:107], a[32:35]\n" \
"v_cvt_pk_bf16_f32 v123, v126, v127\n" \
"v_exp_f32 v1, v1\n" \
"v_exp_f32 v2, v2\n" \
"v_cvt_pk_bf16_f32 v0, v0, v1\n" \
"v_exp_f32 v3, v3\n" \
"v_exp_f32 v4, v4\n" \
"v_mfma_f32_16x16x32_bf16 v[128:131], a[192:195], v[80:83], v[128:131]\n" \
"s_xor_b32 m0, s73, 0x8000\n" \
"ds_read_b128 a[244:247], v247 offset:30720\n" \
"v_cvt_pk_bf16_f32 v1, v2, v3\n" \
"v_mfma_f32_16x16x32_bf16 v[128:131], a[196:199], v[112:115], v[128:131]\n" \
"ds_read_b128 a[248:251], v247 offset:23552\n" \
"global_load_lds_dwordx4 v250, s[36:37] offset:-4096 sc1\n" \
"v_mfma_f32_16x16x32_bf16 v[160:163], a[192:195], v[88:91], v[160:163]\n" \
"ds_read_b128 a[252:255], v247 offset:31744\n" \
"global_load_lds_dwordx4 v250, s[36:37] offset:-3072 sc1\n" \
"v_mfma_f32_16x16x32_bf16 v[160:163], a[196:199], v[120:123], v[160:163]\n" \
"s_add_u32 s70, s82, 0x10000\n" \
"ds_read_b128 a[192:195], v252 offset:0\n" \
"v_add_u32 v247, s70, v248\n" \
"v_mfma_f32_16x16x32_bf16 v[164:167], a[200:203], v[88:91], v[164:167]\n" \
"v_accvgpr_mov_b32 a0, a120\n" \
"v_accvgpr_mov_b32 a1, a121\n" \
"v_mfma_f32_16x16x32_bf16 v[164:167], a[204:207], v[120:123], v[164:167]\n" \
"v_accvgpr_mov_b32 a2, a122\n" \
"v_accvgpr_mov_b32 a3, a123\n" \
"v_mfma_f32_16x16x32_bf16 v[132:135], a[200:203], v[80:83], v[132:135]\n" \
"ds_read_b128 a[196:199], v252 offset:2048\n" \
"v_accvgpr_mov_b32 a4, a124\n" \
"v_mfma_f32_16x16x32_bf16 v[132:135], a[204:207], v[112:115], v[132:135]\n" \
"v_accvgpr_mov_b32 a5, a125\n" \
"v_accvgpr_mov_b32 a6, a126\n" \
"v_mfma_f32_16x16x32_bf16 a[36:39], a[200:203], v[72:75], a[36:39]\n" \
"v_accvgpr_mov_b32 a7, a127\n" \
"v_mov_b32 v64, 0x30000\n" \
"v_mfma_f32_16x16x32_bf16 a[36:39], a[204:207], v[104:107], a[36:39]\n" \
"ds_read_b128 a[200:203], v252 offset:4096\n" \
"global_load_lds_dwordx4 v250, s[36:37] offset:-2048 sc1\n" \
"v_mfma_f32_16x16x32_bf16 a[40:43], a[208:211], v[72:75], a[40:43]\n" \
"v_exp_f32 v5, v5\n" \
"v_mfma_f32_16x16x32_bf16 a[40:43], a[212:215], v[104:107], a[40:43]\n" \
"v_exp_f32 v6, v6\n" \
"v_mfma_f32_16x16x32_bf16 v[136:139], a[208:211], v[80:83], v[136:139]\n" \
"ds_read_b128 a[204:207], v252 offset:6144\n" \
"v_cvt_pk_bf16_f32 v2, v4, v5\n" \
"v_mfma_f32_16x16x32_bf16 v[136:139], a[212:215], v[112:115], v[136:139]\n" \
"v_exp_f32 v7, v7\n" \
"v_mfma_f32_16x16x32_bf16 v[168:171], a[208:211], v[88:91], v[168:171]\n" \
"v_exp_f32 v8, v8\n" \
"v_mfma_f32_16x16x32_bf16 v[168:171], a[212:215], v[120:123], v[168:171]\n" \
"ds_read_b128 a[208:211], v252 offset:256\n" \
"global_load_lds_dwordx4 v250, s[36:37] offset:-1024 sc1\n" \
"v_cvt_pk_bf16_f32 v3, v6, v7\n" \
"v_mfma_f32_16x16x32_bf16 v[172:175], a[216:219], v[88:91], v[172:175]\n" \
"v_exp_f32 v9, v9\n" \
"v_mfma_f32_16x16x32_bf16 v[172:175], a[220:223], v[120:123], v[172:175]\n" \
"v_exp_f32 v10, v10\n" \
"v_cvt_pk_bf16_f32 v8, v8, v9\n" \
"v_mfma_f32_16x16x32_bf16 v[140:143], a[216:219], v[80:83], v[140:143]\n" \
"ds_read_b128 a[212:215], v252 offset:2304\n" \
"global_load_lds_dwordx4 v250, s[36:37] offset:0 sc1\n" \
"v_mfma_f32_16x16x32_bf16 v[140:143], a[220:223], v[112:115], v[140:143]\n" \
"v_exp_f32 v11, v11\n" \
"v_mfma_f32_16x16x32_bf16 a[44:47], a[216:219], v[72:75], a[44:47]\n" \
"s_waitcnt lgkmcnt(6)\n" \
"v_exp_f32 v12, v12\n" \
"v_mfma_f32_16x16x32_bf16 a[44:47], a[220:223], v[104:107], a[44:47]\n" \
"ds_read_b128 a[216:219], v252 offset:4352\n" \
"v_cvt_pk_bf16_f32 v9, v10, v11\n" \
"v_mfma_f32_16x16x32_bf16 a[48:51], a[224:227], v[72:75], a[48:51]\n" \
"v_exp_f32 v13, v13\n" \
"v_mfma_f32_16x16x32_bf16 a[48:51], a[228:231], v[104:107], a[48:51]\n" \
"v_exp_f32 v14, v14\n" \
"v_cvt_pk_bf16_f32 v10, v12, v13\n" \
"v_mfma_f32_16x16x32_bf16 v[144:147], a[224:227], v[80:83], v[144:147]\n" \
"ds_read_b128 a[220:223], v252 offset:6400\n" \
"global_load_lds_dwordx4 v250, s[36:37] offset:1024 sc1\n" \
"v_mfma_f32_16x16x32_bf16 v[144:147], a[228:231], v[112:115], v[144:147]\n" \
"v_exp_f32 v15, v15\n" \
"v_mfma_f32_16x16x32_bf16 v[176:179], a[224:227], v[88:91], v[176:179]\n" \
"v_exp_f32 v16, v16\n" \
"v_mfma_f32_16x16x32_bf16 v[176:179], a[228:231], v[120:123], v[176:179]\n" \
"ds_read_b128 a[224:227], v252 offset:8192\n" \
"global_load_lds_dwordx4 v250, s[36:37] offset:2048 sc1\n" \
"v_cvt_pk_bf16_f32 v11, v14, v15\n" \
"v_mfma_f32_16x16x32_bf16 v[180:183], a[232:235], v[88:91], v[180:183]\n" \
"v_exp_f32 v17, v17\n" \
"v_mfma_f32_16x16x32_bf16 v[180:183], a[236:239], v[120:123], v[180:183]\n" \
"v_exp_f32 v18, v18\n" \
"v_cvt_pk_bf16_f32 v16, v16, v17\n" \
"v_mfma_f32_16x16x32_bf16 v[238:241], a[120:123], v[0:3], v[238:241]\n" \
"v_exp_f32 v19, v19\n" \
"v_mfma_f32_16x16x32_bf16 v[238:241], a[124:127], v[8:11], v[238:241]\n" \
"v_exp_f32 v20, v20\n" \
"v_cvt_pk_bf16_f32 v17, v18, v19\n" \
"v_mfma_f32_16x16x32_bf16 v[148:151], a[232:235], v[80:83], v[148:151]\n" \
"ds_read_b128 a[228:231], v252 offset:10240\n" \
"global_load_lds_dwordx4 v250, s[36:37] offset:3072 sc1\n" \
"v_mfma_f32_16x16x32_bf16 v[148:151], a[236:239], v[112:115], v[148:151]\n" \
"v_exp_f32 v21, v21\n" \
"v_mfma_f32_16x16x32_bf16 a[52:55], a[232:235], v[72:75], a[52:55]\n" \
"v_exp_f32 v22, v22\n" \
"v_mfma_f32_16x16x32_bf16 a[52:55], a[236:239], v[104:107], a[52:55]\n" \
"s_add_u32 s36, s36, 34816\n" \
"ds_read_b128 a[232:235], v252 offset:12288\n" \
"v_cvt_pk_bf16_f32 v18, v20, v21\n" \
"v_mfma_f32_16x16x32_bf16 a[56:59], a[240:243], v[72:75], a[56:59]\n" \
"v_exp_f32 v23, v23\n" \
"v_mfma_f32_16x16x32_bf16 a[56:59], a[244:247], v[104:107], a[56:59]\n" \
"v_exp_f32 v24, v24\n" \
"v_mfma_f32_16x16x32_bf16 v[152:155], a[240:243], v[80:83], v[152:155]\n" \
"ds_read_b128 a[236:239], v252 offset:14336\n" \
"v_cvt_pk_bf16_f32 v19, v22, v23\n" \
"v_mfma_f32_16x16x32_bf16 v[152:155], a[244:247], v[112:115], v[152:155]\n" \
"v_exp_f32 v25, v25\n" \
"v_exp_f32 v26, v26\n" \
"v_cvt_pk_bf16_f32 v24, v24, v25\n" \
"v_mfma_f32_16x16x32_bf16 v[242:245], a[120:123], v[16:19], v[242:245]\n" \
"v_exp_f32 v27, v27\n" \
"v_mfma_f32_16x16x32_bf16 v[184:187], a[240:243], v[88:91], v[184:187]\n" \
"s_load_dwordx8 s[44:51], s[56:57], s79 glc\n" \
"v_exp_f32 v28, v28\n" \
"v_mfma_f32_16x16x32_bf16 v[184:187], a[244:247], v[120:123], v[184:187]\n" \
"ds_read_b128 a[240:243], v252 offset:8448\n" \
"v_cvt_pk_bf16_f32 v25, v26, v27\n" \
"v_exp_f32 v29, v29\n" \
"v_mfma_f32_16x16x32_bf16 v[188:191], a[248:251], v[88:91], v[188:191]\n" \
"v_exp_f32 v30, v30\n" \
"v_mfma_f32_16x16x32_bf16 v[188:191], a[252:255], v[120:123], v[188:191]\n" \
"v_cvt_pk_bf16_f32 v26, v28, v29\n" \
"v_exp_f32 v31, v31\n" \
"v_mfma_f32_16x16x32_bf16 v[156:159], a[248:251], v[80:83], v[156:159]\n" \
"ds_read_b128 a[244:247], v252 offset:10496\n" \
"v_cvt_pk_bf16_f32 v27, v30, v31\n" \
"v_mfma_f32_16x16x32_bf16 v[156:159], a[252:255], v[112:115], v[156:159]\n" \
"v_mfma_f32_16x16x32_bf16 v[242:245], a[124:127], v[24:27], v[242:245]\n" \
"v_mfma_f32_16x16x32_bf16 a[60:63], a[248:251], v[72:75], a[60:63]\n" \
"s_waitcnt vmcnt(12) lgkmcnt(6)\n" \
"v_mfma_f32_16x16x32_bf16 a[60:63], a[252:255], v[104:107], a[60:63]\n" \
"s_barrier\n" \
"ds_read_b128 a[248:251], v252 offset:12544\n" \
"ds_read_b128 a[120:123], v64\n" \
"ds_read_b128 a[124:127], v64\n" \
"s_mov_b64 exec, 0\n" \
"s_cmp_eq_u32 s61, 0\n" \
"s_cbranch_scc1 Lst1x0_%=\n" \
"s_cmp_eq_u32 s61, 1\n" \
"s_cbranch_scc1 Lst1x1_%=\n" \
"s_cmp_eq_u32 s61, 2\n" \
"s_cbranch_scc1 Lst1x2_%=\n" \
"s_branch Lst1x3_%=\n" \
"Lcb1_%=:\n" \
"s_mov_b64 exec, -1\n" \
"s_waitcnt lgkmcnt(0)\n" \
"s_cmp_lg_u32 s63, 0\n" \
"s_cbranch_scc1 LA1g_%=\n" \
"s_xor_b64 s[52:53], s[44:45], s[42:43]\n" \
"s_xor_b64 s[54:55], s[46:47], s[42:43]\n" \
"s_or_b64 s[52:53], s[52:53], s[54:55]\n" \
"s_xor_b64 s[54:55], s[48:49], s[42:43]\n" \
"s_or_b64 s[52:53], s[52:53], s[54:55]\n" \
"s_xor_b64 s[54:55], s[50:51], s[42:43]\n" \
"s_or_b64 s[52:53], s[52:53], s[54:55]\n" \
"s_and_b64 s[52:53], s[52:53], s[8:9]\n" \
"s_cbranch_scc1 Lrs3_%=\n" \
"Lrr3_%=:\n" \
"s_branch LA1g_%=\n" \
"Lpg7030_%=:\n" \
"s_cbranch_execz Lcb0_%=\n" \
"Lloop2_%=:\n" \
"v_mfma_f32_16x16x32_bf16 a[64:67], a[192:195], v[0:3], a[64:67]\n" \
"v_exp_f32 v64, v64\n" \
"v_mfma_f32_16x16x32_bf16 a[64:67], a[196:199], v[32:35], a[64:67]\n" \
"v_cvt_pk_bf16_f32 v59, v62, v63\n" \
"v_add_f32 v240, v240, v64\n" \
"v_exp_f32 v65, v65\n" \
"v_mfma_f32_16x16x32_bf16 a[96:99], a[192:195], v[8:11], a[96:99]\n" \
"s_mov_b32 m0, s73\n" \
"ds_read_b128 a[244:247], v247 offset:14336\n" \
"v_add_f32 v240, v240, v65\n" \
"v_mfma_f32_16x16x32_bf16 a[96:99], a[196:199], v[40:43], a[96:99]\n" \
"ds_read_b128 a[248:251], v247 offset:7168\n" \
"global_load_lds_dwordx4 v250, s[36:37] offset:-4096 sc1\n" \
"v_mfma_f32_16x16x32_bf16 v[128:131], a[192:195], v[16:19], v[128:131]\n" \
"ds_read_b128 a[252:255], v247 offset:15360\n" \
"v_exp_f32 v66, v66\n" \
"v_mfma_f32_16x16x32_bf16 v[128:131], a[196:199], v[48:51], v[128:131]\n" \
"v_add_f32 v240, v240, v66\n" \
"v_cvt_pk_bf16_f32 v64, v64, v65\n" \
"v_mfma_f32_16x16x32_bf16 v[160:163], a[192:195], v[24:27], v[160:163]\n" \
"v_exp_f32 v67, v67\n" \
"v_mfma_f32_16x16x32_bf16 v[160:163], a[196:199], v[56:59], v[160:163]\n" \
"ds_read_b128 a[192:195], v246 offset:0\n" \
"global_load_lds_dwordx4 v250, s[36:37] offset:-3072 sc1\n" \
"v_add_f32 v240, v240, v67\n" \
"v_mfma_f32_16x16x32_bf16 v[164:167], a[200:203], v[24:27], v[164:167]\n" \
"v_exp_f32 v68, v68\n" \
"v_mfma_f32_16x16x32_bf16 v[164:167], a[204:207], v[56:59], v[164:167]\n" \
"v_add_f32 v240, v240, v68\n" \
"v_cvt_pk_bf16_f32 v65, v66, v67\n" \
"v_mfma_f32_16x16x32_bf16 v[132:135], a[200:203], v[16:19], v[132:135]\n" \
"v_exp_f32 v69, v69\n" \
"v_mfma_f32_16x16x32_bf16 v[132:135], a[204:207], v[48:51], v[132:135]\n" \
"ds_read_b128 a[196:199], v246 offset:2048\n" \
"v_add_f32 v240, v240, v69\n" \
"v_mfma_f32_16x16x32_bf16 a[100:103], a[200:203], v[8:11], a[100:103]\n" \
"v_exp_f32 v70, v70\n" \
"v_mfma_f32_16x16x32_bf16 a[100:103], a[204:207], v[40:43], a[100:103]\n" \
"v_add_f32 v240, v240, v70\n" \
"v_cvt_pk_bf16_f32 v66, v68, v69\n" \
"v_mfma_f32_16x16x32_bf16 a[68:71], a[200:203], v[0:3], a[68:71]\n" \
"v_exp_f32 v71, v71\n" \
"v_mfma_f32_16x16x32_bf16 a[68:71], a[204:207], v[32:35], a[68:71]\n" \
"ds_read_b128 a[200:203], v246 offset:4096\n" \
"global_load_lds_dwordx4 v250, s[36:37] offset:-2048 sc1\n" \
"v_add_f32 v240, v240, v71\n" \
"v_mfma_f32_16x16x32_bf16 a[72:75], a[208:211], v[0:3], a[72:75]\n" \
"v_exp_f32 v72, v72\n" \
"v_mfma_f32_16x16x32_bf16 a[72:75], a[212:215], v[32:35], a[72:75]\n" \
"v_add_f32 v241, v241, v72\n" \
"v_cvt_pk_bf16_f32 v67, v70, v71\n" \
"v_mfma_f32_16x16x32_bf16 a[104:107], a[208:211], v[8:11], a[104:107]\n" \
"v_exp_f32 v73, v73\n" \
"v_mfma_f32_16x16x32_bf16 a[104:107], a[212:215], v[40:43], a[104:107]\n" \
"ds_read_b128 a[204:207], v246 offset:6144\n" \
"global_load_lds_dwordx4 v250, s[36:37] offset:-1024 sc1\n" \
"v_add_f32 v241, v241, v73\n" \
"v_mfma_f32_16x16x32_bf16 v[136:139], a[208:211], v[16:19], v[136:139]\n" \
"v_exp_f32 v74, v74\n" \
"v_mfma_f32_16x16x32_bf16 v[136:139], a[212:215], v[48:51], v[136:139]\n" \
"v_add_f32 v241, v241, v74\n" \
"v_cvt_pk_bf16_f32 v72, v72, v73\n" \
"v_mfma_f32_16x16x32_bf16 v[168:171], a[208:211], v[24:27], v[168:171]\n" \
"v_exp_f32 v75, v75\n" \
"v_mfma_f32_16x16x32_bf16 v[168:171], a[212:215], v[56:59], v[168:171]\n" \
"ds_read_b128 a[208:211], v246 offset:256\n" \
"v_add_f32 v241, v241, v75\n" \
"v_mfma_f32_16x16x32_bf16 v[172:175], a[216:219], v[24:27], v[172:175]\n" \
"v_exp_f32 v76, v76\n" \
"v_mfma_f32_16x16x32_bf16 v[172:175], a[220:223], v[56:59], v[172:175]\n" \
"v_add_f32 v241, v241, v76\n" \
"v_cvt_pk_bf16_f32 v73, v74, v75\n" \
"v_mfma_f32_16x16x32_bf16 v[140:143], a[216:219], v[16:19], v[140:143]\n" \
"v_exp_f32 v77, v77\n" \
"v_mfma_f32_16x16x32_bf16 v[140:143], a[220:223], v[48:51], v[140:143]\n" \
"ds_read_b128 a[212:215], v246 offset:2304\n" \
"global_load_lds_dwordx4 v250, s[36:37] offset:0 sc1\n" \
"v_add_f32 v241, v241, v77\n" \
"v_mfma_f32_16x16x32_bf16 a[108:111], a[216:219], v[8:11], a[108:111]\n" \
"v_exp_f32 v78, v78\n" \
"v_mfma_f32_16x16x32_bf16 a[108:111], a[220:223], v[40:43], a[108:111]\n" \
"v_add_f32 v241, v241, v78\n" \
"v_cvt_pk_bf16_f32 v74, v76, v77\n" \
"v_mfma_f32_16x16x32_bf16 a[76:79], a[216:219], v[0:3], a[76:79]\n" \
"s_waitcnt lgkmcnt(6)\n" \
"v_exp_f32 v79, v79\n" \
"v_mfma_f32_16x16x32_bf16 a[76:79], a[220:223], v[32:35], a[76:79]\n" \
"ds_read_b128 a[216:219], v246 offset:4352\n" \
"global_load_lds_dwordx4 v250, s[36:37] offset:1024 sc1\n" \
"v_add_f32 v241, v241, v79\n" \
"v_mfma_f32_16x16x32_bf16 a[80:83], a[224:227], v[0:3], a[80:83]\n" \
"v_exp_f32 v80, v80\n" \
"v_mfma_f32_16x16x32_bf16 a[80:83], a[228:231], v[32:35], a[80:83]\n" \
"v_add_f32 v244, v244, v80\n" \
"v_cvt_pk_bf16_f32 v75, v78, v79\n" \
"v_mfma_f32_16x16x32_bf16 a[112:115], a[224:227], v[8:11], a[112:115]\n" \
"v_exp_f32 v81, v81\n" \
"v_mfma_f32_16x16x32_bf16 a[112:115], a[228:231], v[40:43], a[112:115]\n" \
"ds_read_b128 a[220:223], v246 offset:6400\n" \
"v_add_f32 v244, v244, v81\n" \
"v_mfma_f32_16x16x32_bf16 v[144:147], a[224:227], v[16:19], v[144:147]\n" \
"v_exp_f32 v82, v82\n" \
"v_mfma_f32_16x16x32_bf16 v[144:147], a[228:231], v[48:51], v[144:147]\n" \
"v_add_f32 v244, v244, v82\n" \
"v_cvt_pk_bf16_f32 v80, v80, v81\n" \
"v_mfma_f32_16x16x32_bf16 v[176:179], a[224:227], v[24:27], v[176:179]\n" \
"v_exp_f32 v83, v83\n" \
"v_mfma_f32_16x16x32_bf16 v[176:179], a[228:231], v[56:59], v[176:179]\n" \
"ds_read_b128 a[224:227], v246 offset:8192\n" \
"global_load_lds_dwordx4 v250, s[36:37] offset:2048 sc1\n" \
"v_add_f32 v244, v244, v83\n" \
"v_mfma_f32_16x16x32_bf16 v[180:183], a[232:235], v[24:27], v[180:183]\n" \
"v_exp_f32 v84, v84\n" \
"v_mfma_f32_16x16x32_bf16 v[180:183], a[236:239], v[56:59], v[180:183]\n" \
"v_add_f32 v244, v244, v84\n" \
"v_cvt_pk_bf16_f32 v81, v82, v83\n" \
"v_mfma_f32_16x16x32_bf16 v[148:151], a[232:235], v[16:19], v[148:151]\n" \
"v_exp_f32 v85, v85\n" \
"v_mfma_f32_16x16x32_bf16 v[148:151], a[236:239], v[48:51], v[148:151]\n" \
"ds_read_b128 a[228:231], v246 offset:10240\n" \
"global_load_lds_dwordx4 v250, s[36:37] offset:3072 sc1\n" \
"v_add_f32 v244, v244, v85\n" \
"v_mfma_f32_16x16x32_bf16 a[116:119], a[232:235], v[8:11], a[116:119]\n" \
"s_add_u32 s36, s36, 34816\n" \
"v_exp_f32 v86, v86\n" \
"v_mfma_f32_16x16x32_bf16 a[116:119], a[236:239], v[40:43], a[116:119]\n" \
"v_add_f32 v244, v244, v86\n" \
"v_cvt_pk_bf16_f32 v82, v84, v85\n" \
"v_mfma_f32_16x16x32_bf16 a[84:87], a[232:235], v[0:3], a[84:87]\n" \
"v_exp_f32 v87, v87\n" \
"v_mfma_f32_16x16x32_bf16 a[84:87], a[236:239], v[32:35], a[84:87]\n" \
"ds_read_b128 a[232:235], v246 offset:12288\n" \
"v_add_f32 v244, v244, v87\n" \
"v_mfma_f32_16x16x32_bf16 a[88:91], a[240:243], v[0:3], a[88:91]\n" \
"v_exp_f32 v88, v88\n" \
"v_mfma_f32_16x16x32_bf16 a[88:91], a[244:247], v[32:35], a[88:91]\n" \
"v_add_f32 v245, v245, v88\n" \
"v_cvt_pk_bf16_f32 v83, v86, v87\n" \
"v_mfma_f32_16x16x32_bf16 a[120:123], a[240:243], v[8:11], a[120:123]\n" \
"v_exp_f32 v89, v89\n" \
"v_mfma_f32_16x16x32_bf16 a[120:123], a[244:247], v[40:43], a[120:123]\n" \
"ds_read_b128 a[236:239], v246 offset:14336\n" \
"v_add_f32 v245, v245, v89\n" \
"v_mfma_f32_16x16x32_bf16 v[152:155], a[240:243], v[16:19], v[152:155]\n" \
"v_exp_f32 v90, v90\n" \
"v_mfma_f32_16x16x32_bf16 v[152:155], a[244:247], v[48:51], v[152:155]\n" \
"v_add_f32 v245, v245, v90\n" \
"v_cvt_pk_bf16_f32 v88, v88, v89\n" \
"v_mfma_f32_16x16x32_bf16 v[184:187], a[240:243], v[24:27], v[184:187]\n" \
"v_exp_f32 v91, v91\n" \
"v_mfma_f32_16x16x32_bf16 v[184:187], a[244:247], v[56:59], v[184:187]\n" \
"ds_read_b128 a[240:243], v246 offset:8448\n" \
"v_add_f32 v245, v245, v91\n" \
"v_mfma_f32_16x16x32_bf16 v[188:191], a[248:251], v[24:27], v[188:191]\n" \
"v_exp_f32 v92, v92\n" \
"v_mfma_f32_16x16x32_bf16 v[188:191], a[252:255], v[56:59], v[188:191]\n" \
"v_add_f32 v245, v245, v92\n" \
"v_cvt_pk_bf16_f32 v89, v90, v91\n" \
"v_mfma_f32_16x16x32_bf16 v[156:159], a[248:251], v[16:19], v[156:159]\n" \
"v_exp_f32 v93, v93\n" \
"v_mfma_f32_16x16x32_bf16 v[156:159], a[252:255], v[48:51], v[156:159]\n" \
"ds_read_b128 a[244:247], v246 offset:10496\n" \
"v_add_f32 v245, v245, v93\n" \
"v_mfma_f32_16x16x32_bf16 a[124:127], a[248:251], v[8:11], a[124:127]\n" \
"v_exp_f32 v94, v94\n" \
"v_mfma_f32_16x16x32_bf16 a[124:127], a[252:255], v[40:43], a[124:127]\n" \
"v_add_f32 v245, v245, v94\n" \
"v_cvt_pk_bf16_f32 v90, v92, v93\n" \
"v_mfma_f32_16x16x32_bf16 a[92:95], a[248:251], v[0:3], a[92:95]\n" \
"s_waitcnt vmcnt(8) lgkmcnt(6)\n" \
"v_exp_f32 v95, v95\n" \
"v_mfma_f32_16x16x32_bf16 a[92:95], a[252:255], v[32:35], a[92:95]\n" \
"s_barrier\n" \
"ds_read_b128 a[248:251], v246 offset:12544\n" \
"v_add_f32 v245, v245, v95\n" \
"v_mfma_f32_16x16x32_bf16 v[0:3], a[192:195], v[192:195], v[224:227]\n" \
"v_exp_f32 v96, v96\n" \
"v_mfma_f32_16x16x32_bf16 v[0:3], a[196:199], v[196:199], v[0:3]\n" \
"v_cvt_pk_bf16_f32 v91, v94, v95\n" \
"v_add_f32 v240, v240, v96\n" \
"v_mfma_f32_16x16x32_bf16 v[0:3], a[200:203], v[200:203], v[0:3]\n" \
"s_mov_b32 m0, s74\n" \
"v_exp_f32 v97, v97\n" \
"v_mfma_f32_16x16x32_bf16 v[0:3], a[204:207], v[204:207], v[0:3]\n" \
"ds_read_b128 a[252:255], v246 offset:14592\n" \
"v_add_f32 v240, v240, v97\n" \
"v_mfma_f32_16x16x32_bf16 v[8:11], a[200:203], v[216:219], v[228:231]\n" \
"global_load_lds_dwordx4 v250, s[38:39] offset:-4096 sc1\n" \
"global_load_lds_dwordx4 v250, s[38:39] offset:-3072 sc1\n" \
"v_mfma_f32_16x16x32_bf16 v[8:11], a[204:207], v[220:223], v[8:11]\n" \
"v_exp_f32 v98, v98\n" \
"v_mfma_f32_16x16x32_bf16 v[8:11], a[192:195], v[208:211], v[8:11]\n" \
"v_add_f32 v240, v240, v98\n" \
"v_cvt_pk_bf16_f32 v96, v96, v97\n" \
"v_mfma_f32_16x16x32_bf16 v[8:11], a[196:199], v[212:215], v[8:11]\n" \
"v_exp_f32 v99, v99\n" \
"v_mfma_f32_16x16x32_bf16 v[16:19], a[192:195], a[160:163], v[232:235]\n" \
"v_add_f32 v240, v240, v99\n" \
"v_exp_f32 v100, v100\n" \
"v_mfma_f32_16x16x32_bf16 v[16:19], a[196:199], a[164:167], v[16:19]\n" \
"v_add_f32 v240, v240, v100\n" \
"v_cvt_pk_bf16_f32 v97, v98, v99\n" \
"v_mfma_f32_16x16x32_bf16 v[16:19], a[200:203], a[168:171], v[16:19]\n" \
"v_exp_f32 v101, v101\n" \
"v_mfma_f32_16x16x32_bf16 v[16:19], a[204:207], a[172:175], v[16:19]\n" \
"v_add_f32 v240, v240, v101\n" \
"v_exp_f32 v102, v102\n" \
"v_mfma_f32_16x16x32_bf16 v[24:27], a[200:203], a[184:187], v[236:239]\n" \
"v_add_f32 v240, v240, v102\n" \
"v_cvt_pk_bf16_f32 v98, v100, v101\n" \
"v_mfma_f32_16x16x32_bf16 v[24:27], a[204:207], a[188:191], v[24:27]\n" \
"v_exp_f32 v103, v103\n" \
"v_mfma_f32_16x16x32_bf16 v[24:27], a[192:195], a[176:179], v[24:27]\n" \
"v_add_f32 v240, v240, v103\n" \
"v_mfma_f32_16x16x32_bf16 v[24:27], a[196:199], a[180:183], v[24:27]\n" \
"ds_read_b128 a[192:195], v247 offset:32768\n" \
"global_load_lds_dwordx4 v250, s[38:39] offset:-2048 sc1\n" \
"v_mfma_f32_16x16x32_bf16 v[28:31], a[208:211], a[176:179], v[236:239]\n" \
"v_exp_f32 v104, v104\n" \
"v_mfma_f32_16x16x32_bf16 v[28:31], a[212:215], a[180:183], v[28:31]\n" \
"v_add_f32 v241, v241, v104\n" \
"v_cvt_pk_bf16_f32 v99, v102, v103\n" \
"v_mfma_f32_16x16x32_bf16 v[28:31], a[216:219], a[184:187], v[28:31]\n" \
"v_exp_f32 v105, v105\n" \
"v_mfma_f32_16x16x32_bf16 v[28:31], a[220:223], a[188:191], v[28:31]\n" \
"ds_read_b128 a[196:199], v247 offset:40960\n" \
"global_load_lds_dwordx4 v250, s[38:39] offset:-1024 sc1\n" \
"v_add_f32 v241, v241, v105\n" \
"v_mfma_f32_16x16x32_bf16 v[20:23], a[216:219], a[168:171], v[232:235]\n" \
"v_exp_f32 v106, v106\n" \
"v_mfma_f32_16x16x32_bf16 v[20:23], a[220:223], a[172:175], v[20:23]\n" \
"v_add_f32 v241, v241, v106\n" \
"v_cvt_pk_bf16_f32 v104, v104, v105\n" \
"v_mfma_f32_16x16x32_bf16 v[20:23], a[208:211], a[160:163], v[20:23]\n" \
"v_exp_f32 v107, v107\n" \
"v_mfma_f32_16x16x32_bf16 v[20:23], a[212:215], a[164:167], v[20:23]\n" \
"ds_read_b128 a[200:203], v247 offset:33792\n" \
"v_add_f32 v241, v241, v107\n" \
"v_mfma_f32_16x16x32_bf16 v[12:15], a[208:211], v[208:211], v[228:231]\n" \
"v_exp_f32 v108, v108\n" \
"v_mfma_f32_16x16x32_bf16 v[12:15], a[212:215], v[212:215], v[12:15]\n" \
"v_add_f32 v241, v241, v108\n" \
"v_cvt_pk_bf16_f32 v105, v106, v107\n" \
"v_mfma_f32_16x16x32_bf16 v[12:15], a[216:219], v[216:219], v[12:15]\n" \
"v_exp_f32 v109, v109\n" \
"v_mfma_f32_16x16x32_bf16 v[12:15], a[220:223], v[220:223], v[12:15]\n" \
"ds_read_b128 a[204:207], v247 offset:41984\n" \
"global_load_lds_dwordx4 v250, s[38:39] offset:0 sc1\n" \
"v_add_f32 v241, v241, v109\n" \
"v_mfma_f32_16x16x32_bf16 v[4:7], a[216:219], v[200:203], v[224:227]\n" \
"v_exp_f32 v110, v110\n" \
"v_mfma_f32_16x16x32_bf16 v[4:7], a[220:223], v[204:207], v[4:7]\n" \
"v_add_f32 v241, v241, v110\n" \
"v_cvt_pk_bf16_f32 v106, v108, v109\n" \
"v_mfma_f32_16x16x32_bf16 v[4:7], a[208:211], v[192:195], v[4:7]\n" \
"s_waitcnt lgkmcnt(4)\n" \
"v_exp_f32 v111, v111\n" \
"v_mfma_f32_16x16x32_bf16 v[4:7], a[212:215], v[196:199], v[4:7]\n" \
"ds_read_b128 a[208:211], v247 offset:34816\n" \
"global_load_lds_dwordx4 v250, s[38:39] offset:1024 sc1\n" \
"v_add_f32 v241, v241, v111\n" \
"v_mfma_f32_16x16x32_bf16 v[32:35], a[224:227], v[192:195], v[224:227]\n" \
"v_exp_f32 v112, v112\n" \
"v_mfma_f32_16x16x32_bf16 v[32:35], a[228:231], v[196:199], v[32:35]\n" \
"v_add_f32 v244, v244, v112\n" \
"v_cvt_pk_bf16_f32 v107, v110, v111\n" \
"v_mfma_f32_16x16x32_bf16 v[32:35], a[232:235], v[200:203], v[32:35]\n" \
"v_exp_f32 v113, v113\n" \
"v_mfma_f32_16x16x32_bf16 v[32:35], a[236:239], v[204:207], v[32:35]\n" \
"ds_read_b128 a[212:215], v247 offset:43008\n" \
"v_add_f32 v244, v244, v113\n" \
"v_mfma_f32_16x16x32_bf16 v[40:43], a[232:235], v[216:219], v[228:231]\n" \
"v_exp_f32 v114, v114\n" \
"v_mfma_f32_16x16x32_bf16 v[40:43], a[236:239], v[220:223], v[40:43]\n" \
"v_add_f32 v244, v244, v114\n" \
"v_cvt_pk_bf16_f32 v112, v112, v113\n" \
"v_mfma_f32_16x16x32_bf16 v[40:43], a[224:227], v[208:211], v[40:43]\n" \
"v_exp_f32 v115, v115\n" \
"v_mfma_f32_16x16x32_bf16 v[40:43], a[228:231], v[212:215], v[40:43]\n" \
"ds_read_b128 a[216:219], v247 offset:35840\n" \
"global_load_lds_dwordx4 v250, s[38:39] offset:2048 sc1\n" \
"v_add_f32 v244, v244, v115\n" \
"v_mfma_f32_16x16x32_bf16 v[48:51], a[224:227], a[160:163], v[232:235]\n" \
"v_exp_f32 v116, v116\n" \
"v_mfma_f32_16x16x32_bf16 v[48:51], a[228:231], a[164:167], v[48:51]\n" \
"v_add_f32 v244, v244, v116\n" \
"v_cvt_pk_bf16_f32 v113, v114, v115\n" \
"v_mfma_f32_16x16x32_bf16 v[48:51], a[232:235], a[168:171], v[48:51]\n" \
"v_exp_f32 v117, v117\n" \
"v_mfma_f32_16x16x32_bf16 v[48:51], a[236:239], a[172:175], v[48:51]\n" \
"ds_read_b128 a[220:223], v247 offset:44032\n" \
"global_load_lds_dwordx4 v250, s[38:39] offset:3072 sc1\n" \
"v_add_f32 v244, v244, v117\n" \
"v_mfma_f32_16x16x32_bf16 v[56:59], a[232:235], a[184:187], v[236:239]\n" \
"s_add_u32 s38, s38, 34816\n" \
"v_exp_f32 v118, v118\n" \
"v_mfma_f32_16x16x32_bf16 v[56:59], a[236:239], a[188:191], v[56:59]\n" \
"v_add_f32 v244, v244, v118\n" \
"v_cvt_pk_bf16_f32 v114, v116, v117\n" \
"v_mfma_f32_16x16x32_bf16 v[56:59], a[224:227], a[176:179], v[56:59]\n" \
"v_exp_f32 v119, v119\n" \
"v_mfma_f32_16x16x32_bf16 v[56:59], a[228:231], a[180:183], v[56:59]\n" \
"ds_read_b128 a[224:227], v247 offset:36864\n" \
"v_add_f32 v244, v244, v119\n" \
"v_mfma_f32_16x16x32_bf16 v[60:63], a[240:243], a[176:179], v[236:239]\n" \
"v_exp_f32 v120, v120\n" \
"v_mfma_f32_16x16x32_bf16 v[60:63], a[244:247], a[180:183], v[60:63]\n" \
"v_add_f32 v245, v245, v120\n" \
"v_cvt_pk_bf16_f32 v115, v118, v119\n" \
"v_mfma_f32_16x16x32_bf16 v[60:63], a[248:251], a[184:187], v[60:63]\n" \
"v_exp_f32 v121, v121\n" \
"v_mfma_f32_16x16x32_bf16 v[60:63], a[252:255], a[188:191], v[60:63]\n" \
"ds_read_b128 a[228:231], v247 offset:45056\n" \
"v_add_f32 v245, v245, v121\n" \
"v_mfma_f32_16x16x32_bf16 v[52:55], a[248:251], a[168:171], v[232:235]\n" \
"v_exp_f32 v122, v122\n" \
"v_mfma_f32_16x16x32_bf16 v[52:55], a[252:255], a[172:175], v[52:55]\n" \
"v_add_f32 v245, v245, v122\n" \
"v_cvt_pk_bf16_f32 v120, v120, v121\n" \
"v_mfma_f32_16x16x32_bf16 v[52:55], a[240:243], a[160:163], v[52:55]\n" \
"v_exp_f32 v123, v123\n" \
"v_mfma_f32_16x16x32_bf16 v[52:55], a[244:247], a[164:167], v[52:55]\n" \
"ds_read_b128 a[232:235], v247 offset:37888\n" \
"v_add_f32 v245, v245, v123\n" \
"v_mfma_f32_16x16x32_bf16 v[44:47], a[240:243], v[208:211], v[228:231]\n" \
"v_exp_f32 v124, v124\n" \
"v_mfma_f32_16x16x32_bf16 v[44:47], a[244:247], v[212:215], v[44:47]\n" \
"v_add_f32 v245, v245, v124\n" \
"v_cvt_pk_bf16_f32 v121, v122, v123\n" \
"v_mfma_f32_16x16x32_bf16 v[44:47], a[248:251], v[216:219], v[44:47]\n" \
"v_exp_f32 v125, v125\n" \
"v_mfma_f32_16x16x32_bf16 v[44:47], a[252:255], v[220:223], v[44:47]\n" \
"ds_read_b128 a[236:239], v247 offset:46080\n" \
"v_add_f32 v245, v245, v125\n" \
"v_mfma_f32_16x16x32_bf16 v[36:39], a[248:251], v[200:203], v[224:227]\n" \
"v_exp_f32 v126, v126\n" \
"v_mfma_f32_16x16x32_bf16 v[36:39], a[252:255], v[204:207], v[36:39]\n" \
"v_add_f32 v245, v245, v126\n" \
"v_cvt_pk_bf16_f32 v122, v124, v125\n" \
"v_mfma_f32_16x16x32_bf16 v[36:39], a[240:243], v[192:195], v[36:39]\n" \
"s_waitcnt vmcnt(8) lgkmcnt(4)\n" \
"v_exp_f32 v127, v127\n" \
"v_mfma_f32_16x16x32_bf16 v[36:39], a[244:247], v[196:199], v[36:39]\n" \
"s_barrier\n" \
"ds_read_b128 a[240:243], v247 offset:38912\n" \
"v_add_f32 v245, v245, v127\n" \
"v_mfma_f32_16x16x32_bf16 a[64:67], a[192:195], v[64:67], a[64:67]\n" \
"v_exp_f32 v0, v0\n" \
"v_mfma_f32_16x16x32_bf16 a[64:67], a[196:199], v[96:99], a[64:67]\n" \
"v_cvt_pk_bf16_f32 v123, v126, v127\n" \
"v_add_f32 v240, v240, v0\n" \
"v_exp_f32 v1, v1\n" \
"v_mfma_f32_16x16x32_bf16 a[96:99], a[192:195], v[72:75], a[96:99]\n" \
"s_xor_b32 m0, s73, 0x8000\n" \
"ds_read_b128 a[244:247], v247 offset:47104\n" \
"v_add_f32 v240, v240, v1\n" \
"v_mfma_f32_16x16x32_bf16 a[96:99], a[196:199], v[104:107], a[96:99]\n" \
"ds_read_b128 a[248:251], v247 offset:39936\n" \
"global_load_lds_dwordx4 v250, s[36:37] offset:-4096 sc1\n" \
"v_mfma_f32_16x16x32_bf16 v[128:131], a[192:195], v[80:83], v[128:131]\n" \
"ds_read_b128 a[252:255], v247 offset:48128\n" \
"v_exp_f32 v2, v2\n" \
"v_mfma_f32_16x16x32_bf16 v[128:131], a[196:199], v[112:115], v[128:131]\n" \
"v_add_f32 v240, v240, v2\n" \
"v_cvt_pk_bf16_f32 v0, v0, v1\n" \
"v_mfma_f32_16x16x32_bf16 v[160:163], a[192:195], v[88:91], v[160:163]\n" \
"Lpg7398_%=:\n" \
"Lst1x0_%=:\n" \
"s_cbranch_execz Lpg9736_%=\n" \
"v_exp_f32 v3, v3\n" \
"v_mfma_f32_16x16x32_bf16 v[160:163], a[196:199], v[120:123], v[160:163]\n" \
"ds_read_b128 a[192:195], v252 offset:0\n" \
"global_load_lds_dwordx4 v250, s[36:37] offset:-3072 sc1\n" \
"v_add_f32 v240, v240, v3\n" \
"v_mfma_f32_16x16x32_bf16 v[164:167], a[200:203], v[88:91], v[164:167]\n" \
"v_exp_f32 v4, v4\n" \
"v_mfma_f32_16x16x32_bf16 v[164:167], a[204:207], v[120:123], v[164:167]\n" \
"v_add_f32 v240, v240, v4\n" \
"v_cvt_pk_bf16_f32 v1, v2, v3\n" \
"v_mfma_f32_16x16x32_bf16 v[132:135], a[200:203], v[80:83], v[132:135]\n" \
"v_exp_f32 v5, v5\n" \
"v_mfma_f32_16x16x32_bf16 v[132:135], a[204:207], v[112:115], v[132:135]\n" \
"ds_read_b128 a[196:199], v252 offset:2048\n" \
"v_add_f32 v240, v240, v5\n" \
"v_mfma_f32_16x16x32_bf16 a[100:103], a[200:203], v[72:75], a[100:103]\n" \
"v_exp_f32 v6, v6\n" \
"v_mfma_f32_16x16x32_bf16 a[100:103], a[204:207], v[104:107], a[100:103]\n" \
"v_add_f32 v240, v240, v6\n" \
"v_cvt_pk_bf16_f32 v2, v4, v5\n" \
"v_mfma_f32_16x16x32_bf16 a[68:71], a[200:203], v[64:67], a[68:71]\n" \
"v_exp_f32 v7, v7\n" \
"v_mfma_f32_16x16x32_bf16 a[68:71], a[204:207], v[96:99], a[68:71]\n" \
"ds_read_b128 a[200:203], v252 offset:4096\n" \
"global_load_lds_dwordx4 v250, s[36:37] offset:-2048 sc1\n" \
"v_add_f32 v240, v240, v7\n" \
"v_mfma_f32_16x16x32_bf16 a[72:75], a[208:211], v[64:67], a[72:75]\n" \
"v_exp_f32 v8, v8\n" \
"v_mfma_f32_16x16x32_bf16 a[72:75], a[212:215], v[96:99], a[72:75]\n" \
"v_add_f32 v241, v241, v8\n" \
"v_cvt_pk_bf16_f32 v3, v6, v7\n" \
"v_mfma_f32_16x16x32_bf16 a[104:107], a[208:211], v[72:75], a[104:107]\n" \
"v_exp_f32 v9, v9\n" \
"v_mfma_f32_16x16x32_bf16 a[104:107], a[212:215], v[104:107], a[104:107]\n" \
"ds_read_b128 a[204:207], v252 offset:6144\n" \
"global_load_lds_dwordx4 v250, s[36:37] offset:-1024 sc1\n" \
"v_add_f32 v241, v241, v9\n" \
"v_mfma_f32_16x16x32_bf16 v[136:139], a[208:211], v[80:83], v[136:139]\n" \
"v_exp_f32 v10, v10\n" \
"v_mfma_f32_16x16x32_bf16 v[136:139], a[212:215], v[112:115], v[136:139]\n" \
"v_add_f32 v241, v241, v10\n" \
"v_cvt_pk_bf16_f32 v8, v8, v9\n" \
"v_mfma_f32_16x16x32_bf16 v[168:171], a[208:211], v[88:91], v[168:171]\n" \
"v_exp_f32 v11, v11\n" \
"v_mfma_f32_16x16x32_bf16 v[168:171], a[212:215], v[120:123], v[168:171]\n" \
"ds_read_b128 a[208:211], v252 offset:256\n" \
"v_add_f32 v241, v241, v11\n" \
"v_mfma_f32_16x16x32_bf16 v[172:175], a[216:219], v[88:91], v[172:175]\n" \
"v_exp_f32 v12, v12\n" \
"v_mfma_f32_16x16x32_bf16 v[172:175], a[220:223], v[120:123], v[172:175]\n" \
"v_add_f32 v241, v241, v12\n" \
"v_cvt_pk_bf16_f32 v9, v10, v11\n" \
"v_mfma_f32_16x16x32_bf16 v[140:143], a[216:219], v[80:83], v[140:143]\n" \
"v_exp_f32 v13, v13\n" \
"v_mfma_f32_16x16x32_bf16 v[140:143], a[220:223], v[112:115], v[140:143]\n" \
"ds_read_b128 a[212:215], v252 offset:2304\n" \
"global_load_lds_dwordx4 v250, s[36:37] offset:0 sc1\n" \
"v_add_f32 v241, v241, v13\n" \
"v_mfma_f32_16x16x32_bf16 a[108:111], a[216:219], v[72:75], a[108:111]\n" \
"v_exp_f32 v14, v14\n" \
"v_mfma_f32_16x16x32_bf16 a[108:111], a[220:223], v[104:107], a[108:111]\n" \
"v_add_f32 v241, v241, v14\n" \
"v_cvt_pk_bf16_f32 v10, v12, v13\n" \
"v_mfma_f32_16x16x32_bf16 a[76:79], a[216:219], v[64:67], a[76:79]\n" \
"s_waitcnt lgkmcnt(6)\n" \
"v_exp_f32 v15, v15\n" \
"v_mfma_f32_16x16x32_bf16 a[76:79], a[220:223], v[96:99], a[76:79]\n" \
"ds_read_b128 a[216:219], v252 offset:4352\n" \
"global_load_lds_dwordx4 v250, s[36:37] offset:1024 sc1\n" \
"v_add_f32 v241, v241, v15\n" \
"v_mfma_f32_16x16x32_bf16 a[80:83], a[224:227], v[64:67], a[80:83]\n" \
"v_exp_f32 v16, v16\n" \
"v_mfma_f32_16x16x32_bf16 a[80:83], a[228:231], v[96:99], a[80:83]\n" \
"v_add_f32 v244, v244, v16\n" \
"v_cvt_pk_bf16_f32 v11, v14, v15\n" \
"v_mfma_f32_16x16x32_bf16 a[112:115], a[224:227], v[72:75], a[112:115]\n" \
"v_exp_f32 v17, v17\n" \
"v_mfma_f32_16x16x32_bf16 a[112:115], a[228:231], v[104:107], a[112:115]\n" \
"ds_read_b128 a[220:223], v252 offset:6400\n" \
"v_add_f32 v244, v244, v17\n" \
"v_mfma_f32_16x16x32_bf16 v[144:147], a[224:227], v[80:83], v[144:147]\n" \
"v_exp_f32 v18, v18\n" \
"v_mfma_f32_16x16x32_bf16 v[144:147], a[228:231], v[112:115], v[144:147]\n" \
"v_add_f32 v244, v244, v18\n" \
"v_cvt_pk_bf16_f32 v16, v16, v17\n" \
"v_mfma_f32_16x16x32_bf16 v[176:179], a[224:227], v[88:91], v[176:179]\n" \
"v_exp_f32 v19, v19\n" \
"v_mfma_f32_16x16x32_bf16 v[176:179], a[228:231], v[120:123], v[176:179]\n" \
"ds_read_b128 a[224:227], v252 offset:8192\n" \
"global_load_lds_dwordx4 v250, s[36:37] offset:2048 sc1\n" \
"v_add_f32 v244, v244, v19\n" \
"v_mfma_f32_16x16x32_bf16 v[180:183], a[232:235], v[88:91], v[180:183]\n" \
"v_exp_f32 v20, v20\n" \
"v_mfma_f32_16x16x32_bf16 v[180:183], a[236:239], v[120:123], v[180:183]\n" \
"v_add_f32 v244, v244, v20\n" \
"v_cvt_pk_bf16_f32 v17, v18, v19\n" \
"v_mfma_f32_16x16x32_bf16 v[148:151], a[232:235], v[80:83], v[148:151]\n" \
"v_exp_f32 v21, v21\n" \
"v_mfma_f32_16x16x32_bf16 v[148:151], a[236:239], v[112:115], v[148:151]\n" \
"ds_read_b128 a[228:231], v252 offset:10240\n" \
"global_load_lds_dwordx4 v250, s[36:37] offset:3072 sc1\n" \
"v_add_f32 v244, v244, v21\n" \
"v_mfma_f32_16x16x32_bf16 a[116:119], a[232:235], v[72:75], a[116:119]\n" \
"s_add_u32 s36, s36, 34816\n" \
"v_exp_f32 v22, v22\n" \
"v_mfma_f32_16x16x32_bf16 a[116:119], a[236:239], v[104:107], a[116:119]\n" \
"v_add_f32 v244, v244, v22\n" \
"v_cvt_pk_bf16_f32 v18, v20, v21\n" \
"v_mfma_f32_16x16x32_bf16 a[84:87], a[232:235], v[64:67], a[84:87]\n" \
"v_exp_f32 v23, v23\n" \
"v_mfma_f32_16x16x32_bf16 a[84:87], a[236:239], v[96:99], a[84:87]\n" \
"ds_read_b128 a[232:235], v252 offset:12288\n" \
"v_add_f32 v244, v244, v23\n" \
"v_mfma_f32_16x16x32_bf16 a[88:91], a[240:243], v[64:67], a[88:91]\n" \
"v_exp_f32 v24, v24\n" \
"v_mfma_f32_16x16x32_bf16 a[88:91], a[244:247], v[96:99], a[88:91]\n" \
"v_add_f32 v245, v245, v24\n" \
"v_cvt_pk_bf16_f32 v19, v22, v23\n" \
"v_mfma_f32_16x16x32_bf16 a[120:123], a[240:243], v[72:75], a[120:123]\n" \
"v_exp_f32 v25, v25\n" \
"v_mfma_f32_16x16x32_bf16 a[120:123], a[244:247], v[104:107], a[120:123]\n" \
"ds_read_b128 a[236:239], v252 offset:14336\n" \
"v_add_f32 v245, v245, v25\n" \
"v_mfma_f32_16x16x32_bf16 v[152:155], a[240:243], v[80:83], v[152:155]\n" \
"v_exp_f32 v26, v26\n" \
"v_mfma_f32_16x16x32_bf16 v[152:155], a[244:247], v[112:115], v[152:155]\n" \
"v_add_f32 v245, v245, v26\n" \
"v_cvt_pk_bf16_f32 v24, v24, v25\n" \
"v_mfma_f32_16x16x32_bf16 v[184:187], a[240:243], v[88:91], v[184:187]\n" \
"v_exp_f32 v27, v27\n" \
"v_mfma_f32_16x16x32_bf16 v[184:187], a[244:247], v[120:123], v[184:187]\n" \
"ds_read_b128 a[240:243], v252 offset:8448\n" \
"v_add_f32 v245, v245, v27\n" \
"v_mfma_f32_16x16x32_bf16 v[188:191], a[248:251], v[88:91], v[188:191]\n" \
"v_exp_f32 v28, v28\n" \
"v_mfma_f32_16x16x32_bf16 v[188:191], a[252:255], v[120:123], v[188:191]\n" \
"v_add_f32 v245, v245, v28\n" \
"v_cvt_pk_bf16_f32 v25, v26, v27\n" \
"v_mfma_f32_16x16x32_bf16 v[156:159], a[248:251], v[80:83], v[156:159]\n" \
"v_exp_f32 v29, v29\n" \
"v_mfma_f32_16x16x32_bf16 v[156:159], a[252:255], v[112:115], v[156:159]\n" \
"ds_read_b128 a[244:247], v252 offset:10496\n" \
"v_add_f32 v245, v245, v29\n" \
"v_mfma_f32_16x16x32_bf16 a[124:127], a[248:251], v[72:75], a[124:127]\n" \
"v_exp_f32 v30, v30\n" \
"v_mfma_f32_16x16x32_bf16 a[124:127], a[252:255], v[104:107], a[124:127]\n" \
"v_add_f32 v245, v245, v30\n" \
"v_cvt_pk_bf16_f32 v26, v28, v29\n" \
"v_mfma_f32_16x16x32_bf16 a[92:95], a[248:251], v[64:67], a[92:95]\n" \
"s_waitcnt vmcnt(8) lgkmcnt(6)\n" \
"v_exp_f32 v31, v31\n" \
"v_mfma_f32_16x16x32_bf16 a[92:95], a[252:255], v[96:99], a[92:95]\n" \
"s_barrier\n" \
"ds_read_b128 a[248:251], v252 offset:12544\n" \
"v_add_f32 v245, v245, v31\n" \
"LA1g_%=:\n" \
"Lpg7555_%=:\n" \
"s_cbranch_execz Lcb0_%=\n" \
"v_mfma_f32_16x16x32_bf16 v[64:67], a[192:195], v[192:195], v[224:227]\n" \
"v_exp_f32 v32, v32\n" \
"v_mfma_f32_16x16x32_bf16 v[64:67], a[196:199], v[196:199], v[64:67]\n" \
"v_cvt_pk_bf16_f32 v27, v30, v31\n" \
"v_add_f32 v240, v240, v32\n" \
"v_mfma_f32_16x16x32_bf16 v[64:67], a[200:203], v[200:203], v[64:67]\n" \
"s_add_u32 m0, s74, 0x8000\n" \
"v_exp_f32 v33, v33\n" \
"v_mfma_f32_16x16x32_bf16 v[64:67], a[204:207], v[204:207], v[64:67]\n" \
"ds_read_b128 a[252:255], v252 offset:14592\n" \
"v_add_f32 v240, v240, v33\n" \
"v_mfma_f32_16x16x32_bf16 v[72:75], a[200:203], v[216:219], v[228:231]\n" \
"global_load_lds_dwordx4 v250, s[38:39] offset:-4096 sc1\n" \
"global_load_lds_dwordx4 v250, s[38:39] offset:-3072 sc1\n" \
"v_mfma_f32_16x16x32_bf16 v[72:75], a[204:207], v[220:223], v[72:75]\n" \
"v_exp_f32 v34, v34\n" \
"v_mfma_f32_16x16x32_bf16 v[72:75], a[192:195], v[208:211], v[72:75]\n" \
"v_add_f32 v240, v240, v34\n" \
"v_cvt_pk_bf16_f32 v32, v32, v33\n" \
"v_mfma_f32_16x16x32_bf16 v[72:75], a[196:199], v[212:215], v[72:75]\n" \
"v_exp_f32 v35, v35\n" \
"v_mfma_f32_16x16x32_bf16 v[80:83], a[192:195], a[160:163], v[232:235]\n" \
"v_add_f32 v240, v240, v35\n" \
"v_exp_f32 v36, v36\n" \
"v_mfma_f32_16x16x32_bf16 v[80:83], a[196:199], a[164:167], v[80:83]\n" \
"v_add_f32 v240, v240, v36\n" \
"v_cvt_pk_bf16_f32 v33, v34, v35\n" \
"v_mfma_f32_16x16x32_bf16 v[80:83], a[200:203], a[168:171], v[80:83]\n" \
"v_exp_f32 v37, v37\n" \
"v_mfma_f32_16x16x32_bf16 v[80:83], a[204:207], a[172:175], v[80:83]\n" \
"v_add_f32 v240, v240, v37\n" \
"v_exp_f32 v38, v38\n" \
"v_mfma_f32_16x16x32_bf16 v[88:91], a[200:203], a[184:187], v[236:239]\n" \
"v_add_f32 v240, v240, v38\n" \
"v_cvt_pk_bf16_f32 v34, v36, v37\n" \
"v_mfma_f32_16x16x32_bf16 v[88:91], a[204:207], a[188:191], v[88:91]\n" \
"v_exp_f32 v39, v39\n" \
"v_mfma_f32_16x16x32_bf16 v[88:91], a[192:195], a[176:179], v[88:91]\n" \
"v_add_f32 v240, v240, v39\n" \
"v_mfma_f32_16x16x32_bf16 v[88:91], a[196:199], a[180:183], v[88:91]\n" \
"ds_read_b128 a[192:195], v247 offset:0\n" \
"global_load_lds_dwordx4 v250, s[38:39] offset:-2048 sc1\n" \
"v_mfma_f32_16x16x32_bf16 v[92:95], a[208:211], a[176:179], v[236:239]\n" \
"v_exp_f32 v40, v40\n" \
"v_mfma_f32_16x16x32_bf16 v[92:95], a[212:215], a[180:183], v[92:95]\n" \
"v_add_f32 v241, v241, v40\n" \
"v_cvt_pk_bf16_f32 v35, v38, v39\n" \
"v_mfma_f32_16x16x32_bf16 v[92:95], a[216:219], a[184:187], v[92:95]\n" \
"v_exp_f32 v41, v41\n" \
"v_mfma_f32_16x16x32_bf16 v[92:95], a[220:223], a[188:191], v[92:95]\n" \
"ds_read_b128 a[196:199], v247 offset:8192\n" \
"global_load_lds_dwordx4 v250, s[38:39] offset:-1024 sc1\n" \
"v_add_f32 v241, v241, v41\n" \
"v_mfma_f32_16x16x32_bf16 v[84:87], a[216:219], a[168:171], v[232:235]\n" \
"v_exp_f32 v42, v42\n" \
"v_mfma_f32_16x16x32_bf16 v[84:87], a[220:223], a[172:175], v[84:87]\n" \
"v_add_f32 v241, v241, v42\n" \
"v_cvt_pk_bf16_f32 v40, v40, v41\n" \
"v_mfma_f32_16x16x32_bf16 v[84:87], a[208:211], a[160:163], v[84:87]\n" \
"v_exp_f32 v43, v43\n" \
"v_mfma_f32_16x16x32_bf16 v[84:87], a[212:215], a[164:167], v[84:87]\n" \
"ds_read_b128 a[200:203], v247 offset:1024\n" \
"v_add_f32 v241, v241, v43\n" \
"v_mfma_f32_16x16x32_bf16 v[76:79], a[208:211], v[208:211], v[228:231]\n" \
"v_exp_f32 v44, v44\n" \
"v_mfma_f32_16x16x32_bf16 v[76:79], a[212:215], v[212:215], v[76:79]\n" \
"v_add_f32 v241, v241, v44\n" \
"v_cvt_pk_bf16_f32 v41, v42, v43\n" \
"v_mfma_f32_16x16x32_bf16 v[76:79], a[216:219], v[216:219], v[76:79]\n" \
"v_exp_f32 v45, v45\n" \
"v_mfma_f32_16x16x32_bf16 v[76:79], a[220:223], v[220:223], v[76:79]\n" \
"ds_read_b128 a[204:207], v247 offset:9216\n" \
"global_load_lds_dwordx4 v250, s[38:39] offset:0 sc1\n" \
"v_add_f32 v241, v241, v45\n" \
"v_mfma_f32_16x16x32_bf16 v[68:71], a[216:219], v[200:203], v[224:227]\n" \
"v_exp_f32 v46, v46\n" \
"v_mfma_f32_16x16x32_bf16 v[68:71], a[220:223], v[204:207], v[68:71]\n" \
"v_add_f32 v241, v241, v46\n" \
"v_cvt_pk_bf16_f32 v42, v44, v45\n" \
"v_mfma_f32_16x16x32_bf16 v[68:71], a[208:211], v[192:195], v[68:71]\n" \
"s_waitcnt lgkmcnt(4)\n" \
"v_exp_f32 v47, v47\n" \
"v_mfma_f32_16x16x32_bf16 v[68:71], a[212:215], v[196:199], v[68:71]\n" \
"ds_read_b128 a[208:211], v247 offset:2048\n" \
"global_load_lds_dwordx4 v250, s[38:39] offset:1024 sc1\n" \
"v_add_f32 v241, v241, v47\n" \
"v_mfma_f32_16x16x32_bf16 v[96:99], a[224:227], v[192:195], v[224:227]\n" \
"v_exp_f32 v48, v48\n" \
"v_mfma_f32_16x16x32_bf16 v[96:99], a[228:231], v[196:199], v[96:99]\n" \
"v_add_f32 v244, v244, v48\n" \
"v_cvt_pk_bf16_f32 v43, v46, v47\n" \
"v_mfma_f32_16x16x32_bf16 v[96:99], a[232:235], v[200:203], v[96:99]\n" \
"v_exp_f32 v49, v49\n" \
"v_mfma_f32_16x16x32_bf16 v[96:99], a[236:239], v[204:207], v[96:99]\n" \
"ds_read_b128 a[212:215], v247 offset:10240\n" \
"v_add_f32 v244, v244, v49\n" \
"v_mfma_f32_16x16x32_bf16 v[104:107], a[232:235], v[216:219], v[228:231]\n" \
"v_exp_f32 v50, v50\n" \
"v_mfma_f32_16x16x32_bf16 v[104:107], a[236:239], v[220:223], v[104:107]\n" \
"v_add_f32 v244, v244, v50\n" \
"v_cvt_pk_bf16_f32 v48, v48, v49\n" \
"v_mfma_f32_16x16x32_bf16 v[104:107], a[224:227], v[208:211], v[104:107]\n" \
"v_exp_f32 v51, v51\n" \
"v_mfma_f32_16x16x32_bf16 v[104:107], a[228:231], v[212:215], v[104:107]\n" \
"ds_read_b128 a[216:219], v247 offset:3072\n" \
"global_load_lds_dwordx4 v250, s[38:39] offset:2048 sc1\n" \
"v_add_f32 v244, v244, v51\n" \
"v_mfma_f32_16x16x32_bf16 v[112:115], a[224:227], a[160:163], v[232:235]\n" \
"v_exp_f32 v52, v52\n" \
"v_mfma_f32_16x16x32_bf16 v[112:115], a[228:231], a[164:167], v[112:115]\n" \
"v_add_f32 v244, v244, v52\n" \
"v_cvt_pk_bf16_f32 v49, v50, v51\n" \
"v_mfma_f32_16x16x32_bf16 v[112:115], a[232:235], a[168:171], v[112:115]\n" \
"v_exp_f32 v53, v53\n" \
"v_mfma_f32_16x16x32_bf16 v[112:115], a[236:239], a[172:175], v[112:115]\n" \
"ds_read_b128 a[220:223], v247 offset:11264\n" \
"global_load_lds_dwordx4 v250, s[38:39] offset:3072 sc1\n" \
"v_add_f32 v244, v244, v53\n" \
"v_mfma_f32_16x16x32_bf16 v[120:123], a[232:235], a[184:187], v[236:239]\n" \
"s_add_u32 s38, s38, 34816\n" \
"v_exp_f32 v54, v54\n" \
"v_mfma_f32_16x16x32_bf16 v[120:123], a[236:239], a[188:191], v[120:123]\n" \
"s_sub_u32 s76, s76, 1\n" \
"v_add_f32 v244, v244, v54\n" \
"v_cvt_pk_bf16_f32 v50, v52, v53\n" \
"v_mfma_f32_16x16x32_bf16 v[120:123], a[224:227], a[176:179], v[120:123]\n" \
"s_cmp_lg_u32 s76, 0\n" \
"v_exp_f32 v55, v55\n" \
"v_mfma_f32_16x16x32_bf16 v[120:123], a[228:231], a[180:183], v[120:123]\n" \
"ds_read_b128 a[224:227], v247 offset:4096\n" \
"v_add_f32 v244, v244, v55\n" \
"v_mfma_f32_16x16x32_bf16 v[124:127], a[240:243], a[176:179], v[236:239]\n" \
"v_exp_f32 v56, v56\n" \
"v_mfma_f32_16x16x32_bf16 v[124:127], a[244:247], a[180:183], v[124:127]\n" \
"v_add_f32 v245, v245, v56\n" \
"v_cvt_pk_bf16_f32 v51, v54, v55\n" \
"v_mfma_f32_16x16x32_bf16 v[124:127], a[248:251], a[184:187], v[124:127]\n" \
"v_exp_f32 v57, v57\n" \
"v_mfma_f32_16x16x32_bf16 v[124:127], a[252:255], a[188:191], v[124:127]\n" \
"ds_read_b128 a[228:231], v247 offset:12288\n" \
"v_add_f32 v245, v245, v57\n" \
"v_mfma_f32_16x16x32_bf16 v[116:119], a[248:251], a[168:171], v[232:235]\n" \
"v_exp_f32 v58, v58\n" \
"v_mfma_f32_16x16x32_bf16 v[116:119], a[252:255], a[172:175], v[116:119]\n" \
"v_add_f32 v245, v245, v58\n" \
"v_cvt_pk_bf16_f32 v56, v56, v57\n" \
"v_mfma_f32_16x16x32_bf16 v[116:119], a[240:243], a[160:163], v[116:119]\n" \
"v_exp_f32 v59, v59\n" \
"v_mfma_f32_16x16x32_bf16 v[116:119], a[244:247], a[164:167], v[116:119]\n" \
"ds_read_b128 a[232:235], v247 offset:5120\n" \
"v_add_f32 v245, v245, v59\n" \
"v_mfma_f32_16x16x32_bf16 v[108:111], a[240:243], v[208:211], v[228:231]\n" \
"v_exp_f32 v60, v60\n" \
"v_mfma_f32_16x16x32_bf16 v[108:111], a[244:247], v[212:215], v[108:111]\n" \
"v_add_f32 v245, v245, v60\n" \
"v_cvt_pk_bf16_f32 v57, v58, v59\n" \
"v_mfma_f32_16x16x32_bf16 v[108:111], a[248:251], v[216:219], v[108:111]\n" \
"v_exp_f32 v61, v61\n" \
"v_mfma_f32_16x16x32_bf16 v[108:111], a[252:255], v[220:223], v[108:111]\n" \
"ds_read_b128 a[236:239], v247 offset:13312\n" \
"v_add_f32 v245, v245, v61\n" \
"v_mfma_f32_16x16x32_bf16 v[100:103], a[248:251], v[200:203], v[224:227]\n" \
"v_exp_f32 v62, v62\n" \
"v_mfma_f32_16x16x32_bf16 v[100:103], a[252:255], v[204:207], v[100:103]\n" \
"v_add_f32 v245, v245, v62\n" \
"v_cvt_pk_bf16_f32 v58, v60, v61\n" \
"v_mfma_f32_16x16x32_bf16 v[100:103], a[240:243], v[192:195], v[100:103]\n" \
"s_waitcnt vmcnt(8) lgkmcnt(4)\n" \
"v_exp_f32 v63, v63\n" \
"v_mfma_f32_16x16x32_bf16 v[100:103], a[244:247], v[196:199], v[100:103]\n" \
"s_barrier\n" \
"ds_read_b128 a[240:243], v247 offset:6144\n" \
"v_add_f32 v245, v245, v63\n" \
"s_cbranch_scc1 Lloop2_%=\n" \
"v_mfma_f32_16x16x32_bf16 a[64:67], a[192:195], v[0:3], a[64:67]\n" \
"s_bfe_u32 s88, %1, 0x20001\n" \
"v_subrev_u32 v246, s82, v246\n" \
"s_lshl_b32 s88, s88, 2\n" \
"v_mfma_f32_16x16x32_bf16 a[64:67], a[196:199], v[32:35], a[64:67]\n" \
"s_memrealtime s[52:53]\n" \
"v_cvt_pk_bf16_f32 v59, v62, v63\n" \
"s_lshr_b32 s70, %1, 6\n" \
"v_mfma_f32_16x16x32_bf16 a[96:99], a[192:195], v[8:11], a[96:99]\n" \
"s_add_u32 s88, s88, s70\n" \
"ds_read_b128 a[244:247], v247 offset:14336\n" \
"v_mov_b32 v216, v243\n" \
"v_mfma_f32_16x16x32_bf16 a[96:99], a[196:199], v[40:43], a[96:99]\n" \
"s_lshr_b32 s71, s22, 7\n" \
"ds_read_b128 a[248:251], v247 offset:7168\n" \
"v_exp_f32 v64, v64\n" \
"v_mfma_f32_16x16x32_bf16 v[128:131], a[192:195], v[16:19], v[128:131]\n" \
"s_sub_u32 s71, s63, s71\n" \
"ds_read_b128 a[252:255], v247 offset:15360\n" \
"v_mov_b32 v214, v216\n" \
"v_mfma_f32_16x16x32_bf16 v[128:131], a[196:199], v[48:51], v[128:131]\n" \
"s_add_u32 s71, s71, 6\n" \
"v_exp_f32 v65, v65\n" \
"v_mfma_f32_16x16x32_bf16 v[160:163], a[192:195], v[24:27], v[160:163]\n" \
"s_and_b32 s71, s71, 7\n" \
"v_exp_f32 v66, v66\n" \
"v_mfma_f32_16x16x32_bf16 v[160:163], a[196:199], v[56:59], v[160:163]\n" \
"s_and_b32 s70, %1, 1\n" \
"ds_read_b128 a[192:195], v246 offset:0\n" \
"v_cvt_pk_bf16_f32 v64, v64, v65\n" \
"v_permlane16_swap_b32 v214, v216\n" \
"v_add_f32 v216, v216, v214\n" \
"v_mov_b32 v214, v216\n" \
"v_mfma_f32_16x16x32_bf16 v[164:167], a[200:203], v[24:27], v[164:167]\n" \
"s_lshl_b32 s70, s70, 3\n" \
"v_exp_f32 v67, v67\n" \
"v_mfma_f32_16x16x32_bf16 v[164:167], a[204:207], v[56:59], v[164:167]\n" \
"s_waitcnt lgkmcnt(0)\n" \
"v_exp_f32 v68, v68\n" \
"v_cvt_pk_bf16_f32 v65, v66, v67\n" \
"v_permlane32_swap_b32 v214, v216\n" \
"v_add_f32 v216, v216, v214\n" \
"v_rcp_f32 v214, v216\n" \
"v_mul_f32 v216, 0x4d800000, v216\n" \
"v_add_u32 v215, 0x1c90080, v249\n" \
"v_cndmask_b32 v215, v249, v215, s[16:17]\n" \
"s_add_u32 s77, s77, 532480\n" \
"v_accvgpr_read_b32 v192, a32\n" \
"v_mfma_f32_16x16x32_bf16 v[132:135], a[200:203], v[16:19], v[132:135]\n" \
"s_barrier\n" \
"v_mfma_f32_16x16x32_bf16 v[132:135], a[204:207], v[48:51], v[132:135]\n" \
"s_and_b32 s54, %1, 7\n" \
"ds_read_b128 a[196:199], v246 offset:2048\n" \
"s_mov_b32 m0, s74\n" \
"v_mfma_f32_16x16x32_bf16 a[100:103], a[200:203], v[8:11], a[100:103]\n" \
"s_lshl_b32 s54, s54, 2\n" \
"v_exp_f32 v69, v69\n" \
"v_mfma_f32_16x16x32_bf16 a[100:103], a[204:207], v[40:43], a[100:103]\n" \
"s_lshr_b32 s55, %1, 6\n" \
"v_exp_f32 v70, v70\n" \
"v_mfma_f32_16x16x32_bf16 a[68:71], a[200:203], v[0:3], a[68:71]\n" \
"s_add_u32 s54, s54, s55\n" \
"v_cvt_pk_bf16_f32 v66, v68, v69\n" \
"v_accvgpr_read_b32 v193, a33\n" \
"v_mfma_f32_16x16x32_bf16 a[68:71], a[204:207], v[32:35], a[68:71]\n" \
"s_lshl_b32 s54, s54, 3\n" \
"ds_read_b128 a[200:203], v246 offset:4096\n" \
"global_load_lds_dwordx4 v250, s[38:39] offset:-4096 sc1\n" \
"global_load_lds_dwordx4 v250, s[38:39] offset:-3072 sc1\n" \
"v_mfma_f32_16x16x32_bf16 a[72:75], a[208:211], v[0:3], a[72:75]\n" \
"s_add_u32 s54, s54, 65593856\n" \
"v_exp_f32 v71, v71\n" \
"v_mfma_f32_16x16x32_bf16 a[72:75], a[212:215], v[32:35], a[72:75]\n" \
"s_sub_u32 s53, s52, s12\n" \
"v_exp_f32 v72, v72\n" \
"v_mfma_f32_16x16x32_bf16 a[104:107], a[208:211], v[8:11], a[104:107]\n" \
"s_mul_i32 s53, s53, 3\n" \
"v_cvt_pk_bf16_f32 v67, v70, v71\n" \
"v_accvgpr_read_b32 v194, a34\n" \
"v_mfma_f32_16x16x32_bf16 a[104:107], a[212:215], v[40:43], a[104:107]\n" \
"s_lshl_b32 s52, s52, 1\n" \
"ds_read_b128 a[204:207], v246 offset:6144\n" \
"global_load_lds_dwordx4 v250, s[38:39] offset:-2048 sc1\n" \
"v_mfma_f32_16x16x32_bf16 v[136:139], a[208:211], v[16:19], v[136:139]\n" \
"s_add_u32 s52, s52, s53\n" \
"v_mov_b32 v211, s52\n" \
"v_xor_b32 v210, s60, v211\n" \
"v_mfma_f32_16x16x32_bf16 v[136:139], a[212:215], v[48:51], v[136:139]\n" \
"s_add_u32 s70, s70, s71\n" \
"v_exp_f32 v73, v73\n" \
"v_mfma_f32_16x16x32_bf16 v[168:171], a[208:211], v[24:27], v[168:171]\n" \
"s_lshl_b32 s87, s70, 12\n" \
"v_exp_f32 v74, v74\n" \
"v_mfma_f32_16x16x32_bf16 v[168:171], a[212:215], v[56:59], v[168:171]\n" \
"s_lshl_b32 s70, s70, 7\n" \
"ds_read_b128 a[208:211], v246 offset:256\n" \
"v_lshlrev_b32 v212, 28, s61\n" \
"v_mfma_f32_16x16x32_bf16 v[172:175], a[216:219], v[24:27], v[172:175]\n" \
"s_cmp_eq_u32 s63, 3\n" \
"s_cselect_b32 s54, s54, 0x20000000\n" \
"v_cvt_pk_bf16_f32 v72, v72, v73\n" \
"v_mfma_f32_16x16x32_bf16 v[172:175], a[220:223], v[56:59], v[172:175]\n" \
"s_mul_i32 s84, s61, 6144\n" \
"v_exp_f32 v75, v75\n" \
"v_mfma_f32_16x16x32_bf16 v[140:143], a[216:219], v[16:19], v[140:143]\n" \
"s_lshl_b32 s71, s84, 4\n" \
"v_exp_f32 v76, v76\n" \
"v_mfma_f32_16x16x32_bf16 v[140:143], a[220:223], v[48:51], v[140:143]\n" \
"s_add_u32 s70, s70, s71\n" \
"ds_read_b128 a[212:215], v246 offset:2304\n" \
"global_load_lds_dwordx4 v250, s[38:39] offset:-1024 sc1\n" \
"v_cvt_pk_bf16_f32 v73, v74, v75\n" \
"v_mfma_f32_16x16x32_bf16 a[108:111], a[216:219], v[8:11], a[108:111]\n" \
"s_add_u32 s70, s70, 4096\n" \
"v_exp_f32 v77, v77\n" \
"v_mfma_f32_16x16x32_bf16 a[108:111], a[220:223], v[40:43], a[108:111]\n" \
"s_add_u32 s48, s22, s70\n" \
"s_addc_u32 s49, s23, 0\n" \
"v_accvgpr_read_b32 v195, a35\n" \
"v_mfma_f32_16x16x32_bf16 a[76:79], a[216:219], v[0:3], a[76:79]\n" \
"s_waitcnt lgkmcnt(6)\n" \
"global_load_lds_dwordx4 v250, s[38:39] offset:0 sc1\n" \
"global_load_lds_dwordx4 v250, s[38:39] offset:1024 sc1\n" \
"v_mfma_f32_16x16x32_bf16 a[76:79], a[220:223], v[32:35], a[76:79]\n" \
"s_add_u32 s50, s48, 23552\n" \
"s_addc_u32 s51, s49, 0\n" \
"ds_read_b128 a[216:219], v246 offset:4352\n" \
"v_mfma_f32_16x16x32_bf16 a[80:83], a[224:227], v[0:3], a[80:83]\n" \
"s_lshr_b32 s89, s22, 7\n" \
"v_exp_f32 v78, v78\n" \
"v_mfma_f32_16x16x32_bf16 a[80:83], a[228:231], v[32:35], a[80:83]\n" \
"s_sub_u32 s89, 3, s89\n" \
"v_cvt_pk_bf16_f32 v74, v76, v77\n" \
"v_accvgpr_read_b32 v196, a36\n" \
"v_accvgpr_read_b32 v197, a37\n" \
"v_accvgpr_read_b32 v198, a38\n" \
"v_accvgpr_read_b32 v199, a39\n" \
"v_pk_mul_f32 v[192:193], v[192:193], v[214:215] op_sel_hi:[1,0]\n" \
"v_pk_mul_f32 v[194:195], v[194:195], v[214:215] op_sel_hi:[1,0]\n" \
"v_pk_mul_f32 v[196:197], v[196:197], v[214:215] op_sel_hi:[1,0]\n" \
"v_pk_mul_f32 v[198:199], v[198:199], v[214:215] op_sel_hi:[1,0]\n" \
"v_mfma_f32_16x16x32_bf16 a[112:115], a[224:227], v[8:11], a[112:115]\n" \
"s_and_b32 s89, s89, 7\n" \
"v_exp_f32 v79, v79\n" \
"v_mfma_f32_16x16x32_bf16 a[112:115], a[228:231], v[40:43], a[112:115]\n" \
"s_and_b32 s90, %1, 1\n" \
"ds_read_b128 a[220:223], v246 offset:6400\n" \
"global_load_lds_dwordx4 v250, s[38:39] offset:2048 sc1\n" \
"v_mfma_f32_16x16x32_bf16 v[144:147], a[224:227], v[16:19], v[144:147]\n" \
"s_lshl_b32 s90, s90, 3\n" \
"v_exp_f32 v80, v80\n" \
"v_mfma_f32_16x16x32_bf16 v[144:147], a[228:231], v[48:51], v[144:147]\n" \
"s_add_u32 s89, s89, s90\n" \
"v_cvt_pk_bf16_f32 v75, v78, v79\n" \
"v_cvt_pk_bf16_f32 v200, v192, v193\n" \
"v_cvt_pk_bf16_f32 v201, v194, v195\n" \
"v_cvt_pk_bf16_f32 v202, v196, v197\n" \
"v_cvt_pk_bf16_f32 v203, v198, v199\n" \
"v_pk_mul_f32 v[192:193], v[192:193], v[194:195]\n" \
"v_pk_fma_f32 v[192:193], v[196:197], v[198:199], v[192:193]\n" \
"v_mfma_f32_16x16x32_bf16 v[176:179], a[224:227], v[24:27], v[176:179]\n" \
"s_lshl_b32 s89, s89, 7\n" \
"v_exp_f32 v81, v81\n" \
"v_mfma_f32_16x16x32_bf16 v[176:179], a[228:231], v[56:59], v[176:179]\n" \
"s_lshr_b32 s90, %1, 3\n" \
"ds_read_b128 a[224:227], v246 offset:8192\n" \
"global_load_lds_dwordx4 v250, s[38:39] offset:3072 sc1\n" \
"v_mfma_f32_16x16x32_bf16 v[180:183], a[232:235], v[24:27], v[180:183]\n" \
"s_lshl_b32 s90, s90, 2\n" \
"v_exp_f32 v82, v82\n" \
"v_mfma_f32_16x16x32_bf16 v[180:183], a[236:239], v[56:59], v[180:183]\n" \
"v_fma_f32 v216, v192, v193, v216\n" \
"v_cvt_pk_bf16_f32 v80, v80, v81\n" \
"v_mfma_f32_16x16x32_bf16 v[238:241], a[0:3], v[64:67], v[238:241]\n" \
"s_add_u32 s90, s90, s61\n" \
"v_exp_f32 v83, v83\n" \
"v_mfma_f32_16x16x32_bf16 v[238:241], a[4:7], v[72:75], v[238:241]\n" \
"buffer_store_dwordx2 v[210:211], v212, s[56:59], s54 offen sc1\n" \
"v_mfma_f32_16x16x32_bf16 v[148:151], a[232:235], v[16:19], v[148:151]\n" \
"s_mul_i32 s90, s90, 24576\n" \
"v_exp_f32 v84, v84\n" \
"v_mfma_f32_16x16x32_bf16 v[148:151], a[236:239], v[48:51], v[148:151]\n" \
"s_add_u32 s89, s89, s90\n" \
"ds_read_b128 a[228:231], v246 offset:10240\n" \
"buffer_store_dwordx4 v[200:203], v215, s[56:59], s77 offen offset:0 sc1\n" \
"v_cvt_pk_bf16_f32 v81, v82, v83\n" \
"v_accvgpr_read_b32 v192, a40\n" \
"v_accvgpr_read_b32 v193, a41\n" \
"v_accvgpr_read_b32 v194, a42\n" \
"v_mfma_f32_16x16x32_bf16 a[116:119], a[232:235], v[8:11], a[116:119]\n" \
"s_lshl_b32 s70, s88, 18\n" \
"v_exp_f32 v85, v85\n" \
"v_mfma_f32_16x16x32_bf16 a[116:119], a[236:239], v[40:43], a[116:119]\n" \
"s_lshl_b32 s71, s61, 16\n" \
"v_accvgpr_read_b32 v195, a43\n" \
"v_accvgpr_read_b32 v196, a44\n" \
"v_accvgpr_read_b32 v197, a45\n" \
"v_accvgpr_read_b32 v198, a46\n" \
"v_accvgpr_read_b32 v199, a47\n" \
"v_exp_f32 v86, v86\n" \
"v_pk_mul_f32 v[192:193], v[192:193], v[214:215] op_sel_hi:[1,0]\n" \
"v_pk_mul_f32 v[194:195], v[194:195], v[214:215] op_sel_hi:[1,0]\n" \
"v_pk_mul_f32 v[196:197], v[196:197], v[214:215] op_sel_hi:[1,0]\n" \
"v_pk_mul_f32 v[198:199], v[198:199], v[214:215] op_sel_hi:[1,0]\n" \
"v_cvt_pk_bf16_f32 v204, v192, v193\n" \
"v_cvt_pk_bf16_f32 v205, v194, v195\n" \
"v_cvt_pk_bf16_f32 v206, v196, v197\n" \
"v_cvt_pk_bf16_f32 v207, v198, v199\n" \
"v_pk_mul_f32 v[192:193], v[192:193], v[194:195]\n" \
"v_pk_fma_f32 v[192:193], v[196:197], v[198:199], v[192:193]\n" \
"v_mfma_f32_16x16x32_bf16 a[84:87], a[232:235], v[0:3], a[84:87]\n" \
"s_add_u32 s71, s71, s70\n" \
"v_cvt_pk_bf16_f32 v82, v84, v85\n" \
"v_fma_f32 v216, v192, v193, v216\n" \
"v_mfma_f32_16x16x32_bf16 a[84:87], a[236:239], v[32:35], a[84:87]\n" \
"s_add_u32 s42, s20, s71\n" \
"s_addc_u32 s43, s21, 0\n" \
"ds_read_b128 a[232:235], v246 offset:12288\n" \
"buffer_store_dwordx4 v[204:207], v215, s[56:59], s77 offen offset:2048 sc1\n" \
"s_add_u32 s77, s77, 0x1000\n" \
"v_accvgpr_read_b32 v192, a48\n" \
"v_accvgpr_read_b32 v193, a49\n" \
"v_accvgpr_read_b32 v194, a50\n" \
"v_mfma_f32_16x16x32_bf16 a[88:91], a[240:243], v[0:3], a[88:91]\n" \
"s_add_u32 s44, s42, 31744\n" \
"s_addc_u32 s45, s43, 0\n" \
"v_accvgpr_read_b32 v195, a51\n" \
"v_mfma_f32_16x16x32_bf16 a[88:91], a[244:247], v[32:35], a[88:91]\n" \
"s_max_u32 s70, s70, 32768\n" \
"v_accvgpr_read_b32 v196, a52\n" \
"v_accvgpr_read_b32 v197, a53\n" \
"v_accvgpr_read_b32 v198, a54\n" \
"v_accvgpr_read_b32 v199, a55\n" \
"v_exp_f32 v87, v87\n" \
"v_pk_mul_f32 v[192:193], v[192:193], v[214:215] op_sel_hi:[1,0]\n" \
"v_pk_mul_f32 v[194:195], v[194:195], v[214:215] op_sel_hi:[1,0]\n" \
"v_pk_mul_f32 v[196:197], v[196:197], v[214:215] op_sel_hi:[1,0]\n" \
"v_pk_mul_f32 v[198:199], v[198:199], v[214:215] op_sel_hi:[1,0]\n" \
"v_cvt_pk_bf16_f32 v200, v192, v193\n" \
"v_cvt_pk_bf16_f32 v201, v194, v195\n" \
"v_cvt_pk_bf16_f32 v202, v196, v197\n" \
"v_cvt_pk_bf16_f32 v203, v198, v199\n" \
"v_pk_mul_f32 v[192:193], v[192:193], v[194:195]\n" \
"v_pk_fma_f32 v[192:193], v[196:197], v[198:199], v[192:193]\n" \
"v_fma_f32 v216, v192, v193, v216\n" \
"v_mfma_f32_16x16x32_bf16 a[120:123], a[240:243], v[8:11], a[120:123]\n" \
"s_sub_u32 s70, s70, 32768\n" \
"v_exp_f32 v88, v88\n" \
"v_mfma_f32_16x16x32_bf16 a[120:123], a[244:247], v[40:43], a[120:123]\n" \
"s_and_b32 s86, s61, 1\n" \
"ds_read_b128 a[236:239], v246 offset:14336\n" \
"buffer_store_dwordx4 v[200:203], v215, s[56:59], s77 offen offset:0 sc1\n" \
"v_cvt_pk_bf16_f32 v83, v86, v87\n" \
"v_accvgpr_read_b32 v192, a56\n" \
"v_accvgpr_read_b32 v193, a57\n" \
"v_accvgpr_read_b32 v194, a58\n" \
"v_mfma_f32_16x16x32_bf16 v[152:155], a[240:243], v[16:19], v[152:155]\n" \
"s_lshl_b32 s86, s86, 2\n" \
"v_exp_f32 v89, v89\n" \
"v_mfma_f32_16x16x32_bf16 v[152:155], a[244:247], v[48:51], v[152:155]\n" \
"v_accvgpr_read_b32 v195, a59\n" \
"v_accvgpr_read_b32 v196, a60\n" \
"v_exp_f32 v90, v90\n" \
"v_cvt_pk_bf16_f32 v88, v88, v89\n" \
"v_mfma_f32_16x16x32_bf16 v[242:245], a[0:3], v[80:83], v[242:245]\n" \
"s_lshr_b32 s71, s61, 1\n" \
"v_exp_f32 v91, v91\n" \
"v_mfma_f32_16x16x32_bf16 v[184:187], a[240:243], v[24:27], v[184:187]\n" \
"s_lshl_b32 s71, s71, 11\n" \
"v_exp_f32 v92, v92\n" \
"v_mfma_f32_16x16x32_bf16 v[184:187], a[244:247], v[56:59], v[184:187]\n" \
"s_add_u32 s70, s70, s71\n" \
"ds_read_b128 a[240:243], v246 offset:8448\n" \
"v_cvt_pk_bf16_f32 v89, v90, v91\n" \
"v_accvgpr_read_b32 v197, a61\n" \
"v_accvgpr_read_b32 v198, a62\n" \
"v_accvgpr_read_b32 v199, a63\n" \
"v_pk_mul_f32 v[192:193], v[192:193], v[214:215] op_sel_hi:[1,0]\n" \
"v_pk_mul_f32 v[194:195], v[194:195], v[214:215] op_sel_hi:[1,0]\n" \
"v_pk_mul_f32 v[196:197], v[196:197], v[214:215] op_sel_hi:[1,0]\n" \
"v_pk_mul_f32 v[198:199], v[198:199], v[214:215] op_sel_hi:[1,0]\n" \
"v_cvt_pk_bf16_f32 v204, v192, v193\n" \
"v_cvt_pk_bf16_f32 v205, v194, v195\n" \
"v_cvt_pk_bf16_f32 v206, v196, v197\n" \
"v_cvt_pk_bf16_f32 v207, v198, v199\n" \
"v_pk_mul_f32 v[192:193], v[192:193], v[194:195]\n" \
"v_pk_fma_f32 v[192:193], v[196:197], v[198:199], v[192:193]\n" \
"v_mfma_f32_16x16x32_bf16 v[188:191], a[248:251], v[24:27], v[188:191]\n" \
"s_mov_b32 s46, s70\n" \
"v_exp_f32 v93, v93\n" \
"v_mfma_f32_16x16x32_bf16 v[188:191], a[252:255], v[56:59], v[188:191]\n" \
"s_lshl_b32 s85, s61, 10\n" \
"v_exp_f32 v94, v94\n" \
"v_mfma_f32_16x16x32_bf16 v[156:159], a[248:251], v[16:19], v[156:159]\n" \
"s_mov_b64 s[12:13], s[20:21]\n" \
"v_cvt_pk_bf16_f32 v90, v92, v93\n" \
"Lpg8026_%=:\n" \
"Lst1x1_%=:\n" \
"s_cbranch_execz Lpg10302_%=\n" \
"v_fma_f32 v216, v192, v193, v216\n" \
"v_mfma_f32_16x16x32_bf16 v[156:159], a[252:255], v[48:51], v[156:159]\n" \
"buffer_store_dwordx4 v[204:207], v215, s[56:59], s77 offen offset:2048 sc1\n" \
"ds_read_b128 a[244:247], v246 offset:10496\n" \
"s_and_b32 s13, s13, 0xffff\n" \
"v_cmp_class_f32 vcc, v216, s94\n" \
"s_sub_u32 s77, s77, 536576\n" \
"v_mfma_f32_16x16x32_bf16 a[124:127], a[248:251], v[8:11], a[124:127]\n" \
"s_mov_b32 s14, 4194304\n" \
"v_exp_f32 v95, v95\n" \
"v_mfma_f32_16x16x32_bf16 a[124:127], a[252:255], v[40:43], a[124:127]\n" \
"s_mov_b32 s15, 0x20000\n" \
"v_cvt_pk_bf16_f32 v91, v94, v95\n" \
"s_or_b64 s[92:93], s[92:93], vcc\n" \
"v_mfma_f32_16x16x32_bf16 a[92:95], a[248:251], v[0:3], a[92:95]\n" \
"s_waitcnt vmcnt(13) lgkmcnt(6)\n" \
"v_mfma_f32_16x16x32_bf16 v[242:245], a[4:7], v[88:91], v[242:245]\n" \
"v_mfma_f32_16x16x32_bf16 a[92:95], a[252:255], v[32:35], a[92:95]\n" \
"s_barrier\n" \
"ds_read_b128 a[248:251], v246 offset:12544\n" \
"v_mfma_f32_16x16x32_bf16 v[24:27], a[192:195], a[176:179], v[236:239]\n" \
"ds_read_b128 v[192:195], v246 offset:16384\n" \
"v_bfe_u32 v69, v248, 8, 1\n" \
"v_mfma_f32_16x16x32_bf16 v[24:27], a[196:199], a[180:183], v[24:27]\n" \
"ds_read_b128 v[196:199], v246 offset:18432\n" \
"v_cvt_pk_bf16_f32 v91, v94, v95\n" \
"v_mfma_f32_16x16x32_bf16 v[24:27], a[200:203], a[184:187], v[24:27]\n" \
"ds_read_b128 v[200:203], v246 offset:20480\n" \
"v_bfe_u32 v70, v248, 6, 2\n" \
"v_mfma_f32_16x16x32_bf16 v[24:27], a[204:207], a[188:191], v[24:27]\n" \
"ds_read_b128 a[252:255], v246 offset:14592\n" \
"v_lshl_add_u32 v70, v69, 3, v70\n" \
"v_exp_f32 v96, v96\n" \
"v_exp_f32 v97, v97\n" \
"v_exp_f32 v98, v98\n" \
"v_cvt_pk_bf16_f32 v96, v96, v97\n" \
"v_exp_f32 v99, v99\n" \
"v_exp_f32 v100, v100\n" \
"v_cvt_pk_bf16_f32 v97, v98, v99\n" \
"v_exp_f32 v101, v101\n" \
"v_exp_f32 v102, v102\n" \
"v_cvt_pk_bf16_f32 v98, v100, v101\n" \
"v_mfma_f32_16x16x32_bf16 v[16:19], a[200:203], a[168:171], v[232:235]\n" \
"ds_read_b128 v[204:207], v246 offset:22528\n" \
"v_mul_u32_u24 v70, 6144, v70\n" \
"v_mfma_f32_16x16x32_bf16 v[16:19], a[204:207], a[172:175], v[16:19]\n" \
"ds_read_b128 v[208:211], v246 offset:16640\n" \
"v_lshlrev_b32 v69, 5, v69\n" \
"v_mfma_f32_16x16x32_bf16 v[16:19], a[192:195], a[160:163], v[16:19]\n" \
"ds_read_b128 v[212:215], v246 offset:18688\n" \
"v_and_b32 v71, 0x30, v248\n" \
"v_mfma_f32_16x16x32_bf16 v[16:19], a[196:199], a[164:167], v[16:19]\n" \
"ds_read_b128 v[216:219], v246 offset:20736\n" \
"v_xor_b32 v69, v69, v71\n" \
"v_exp_f32 v103, v103\n" \
"v_exp_f32 v104, v104\n" \
"v_cvt_pk_bf16_f32 v99, v102, v103\n" \
"v_exp_f32 v105, v105\n" \
"v_exp_f32 v106, v106\n" \
"v_mfma_f32_16x16x32_bf16 v[20:23], a[208:211], a[160:163], v[232:235]\n" \
"ds_read_b128 v[220:223], v246 offset:22784\n" \
"v_lshrrev_b32 v71, 9, v248\n" \
"v_mfma_f32_16x16x32_bf16 v[20:23], a[212:215], a[164:167], v[20:23]\n" \
"ds_read_b128 a[192:195], v247 offset:32768\n" \
"v_lshl_add_u32 v70, v71, 6, v70\n" \
"v_mfma_f32_16x16x32_bf16 v[20:23], a[216:219], a[168:171], v[20:23]\n" \
"ds_read_b128 a[196:199], v247 offset:40960\n" \
"v_add_u32 v253, v69, v70\n" \
"v_mfma_f32_16x16x32_bf16 v[20:23], a[220:223], a[172:175], v[20:23]\n" \
"ds_read_b128 a[200:203], v247 offset:33792\n" \
"v_bfe_u32 v84, v248, 7, 2\n" \
"v_mfma_f32_16x16x32_bf16 v[28:31], a[216:219], a[184:187], v[236:239]\n" \
"ds_read_b128 a[204:207], v247 offset:41984\n" \
"v_bfe_u32 v85, v248, 5, 2\n" \
"v_mfma_f32_16x16x32_bf16 v[28:31], a[220:223], a[188:191], v[28:31]\n" \
"v_min_u32 v85, 2, v85\n" \
"v_mad_u32_u24 v84, v84, 3, v85\n" \
"v_mfma_f32_16x16x32_bf16 v[28:31], a[208:211], a[176:179], v[28:31]\n" \
"v_and_b32 v85, 16, v248\n" \
"v_lshl_add_u32 v85, v85, 2, s89\n" \
"v_mfma_f32_16x16x32_bf16 v[28:31], a[212:215], a[180:183], v[28:31]\n" \
"s_waitcnt lgkmcnt(2)\n" \
"ds_read_b128 a[208:211], v247 offset:34816\n" \
"v_lshl_add_u32 v84, v84, 11, v85\n" \
"v_mfma_f32_16x16x32_bf16 v[8:11], v[192:195], a[176:179], v[236:239]\n" \
"s_add_u32 m0, s84, 143360\n" \
"ds_read_b128 a[212:215], v247 offset:43008\n" \
"v_cvt_pk_bf16_f32 v104, v104, v105\n" \
"v_mfma_f32_16x16x32_bf16 v[8:11], v[196:199], a[180:183], v[8:11]\n" \
"ds_read_b128 a[216:219], v247 offset:35840\n" \
"global_load_lds_dwordx4 v253, s[48:49] offset:-4096\n" \
"v_mfma_f32_16x16x32_bf16 v[8:11], v[200:203], a[184:187], v[8:11]\n" \
"ds_read_b128 a[220:223], v247 offset:44032\n" \
"v_exp_f32 v107, v107\n" \
"v_mfma_f32_16x16x32_bf16 v[8:11], v[204:207], a[188:191], v[8:11]\n" \
"ds_read_b128 v[192:195], v246 offset:24576\n" \
"v_exp_f32 v108, v108\n" \
"v_mfma_f32_16x16x32_bf16 v[56:59], a[232:235], a[184:187], v[236:239]\n" \
"ds_read_b128 v[196:199], v246 offset:26624\n" \
"global_load_lds_dwordx4 v253, s[48:49] offset:-2048\n" \
"v_mfma_f32_16x16x32_bf16 v[56:59], a[236:239], a[188:191], v[56:59]\n" \
"ds_read_b128 v[200:203], v246 offset:28672\n" \
"v_cvt_pk_bf16_f32 v105, v106, v107\n" \
"v_mfma_f32_16x16x32_bf16 v[56:59], a[224:227], a[176:179], v[56:59]\n" \
"ds_read_b128 v[204:207], v246 offset:30720\n" \
"global_load_lds_dwordx4 v253, s[48:49] offset:0\n" \
"v_mfma_f32_16x16x32_bf16 v[56:59], a[228:231], a[180:183], v[56:59]\n" \
"v_exp_f32 v109, v109\n" \
"v_exp_f32 v110, v110\n" \
"v_mfma_f32_16x16x32_bf16 v[48:51], a[224:227], a[160:163], v[232:235]\n" \
"s_add_u32 s48, s48, 0x120000\n" \
"s_addc_u32 s49, s49, 0\n" \
"v_cvt_pk_bf16_f32 v106, v108, v109\n" \
"v_mfma_f32_16x16x32_bf16 v[48:51], a[228:231], a[164:167], v[48:51]\n" \
"v_exp_f32 v111, v111\n" \
"v_mfma_f32_16x16x32_bf16 v[48:51], a[232:235], a[168:171], v[48:51]\n" \
"v_exp_f32 v112, v112\n" \
"v_mfma_f32_16x16x32_bf16 v[48:51], a[236:239], a[172:175], v[48:51]\n" \
"ds_read_b128 a[224:227], v247 offset:36864\n" \
"global_load_lds_dwordx4 v253, s[50:51] offset:-3072\n" \
"v_mfma_f32_16x16x32_bf16 v[12:15], v[216:219], a[184:187], v[236:239]\n" \
"ds_read_b128 a[228:231], v247 offset:45056\n" \
"v_cvt_pk_bf16_f32 v107, v110, v111\n" \
"v_mfma_f32_16x16x32_bf16 v[12:15], v[220:223], a[188:191], v[12:15]\n" \
"ds_read_b128 a[232:235], v247 offset:37888\n" \
"global_load_lds_dwordx4 v253, s[50:51] offset:-1024\n" \
"v_mfma_f32_16x16x32_bf16 v[12:15], v[208:211], a[176:179], v[12:15]\n" \
"ds_read_b128 a[236:239], v247 offset:46080\n" \
"v_exp_f32 v113, v113\n" \
"v_mfma_f32_16x16x32_bf16 v[12:15], v[212:215], a[180:183], v[12:15]\n" \
"ds_read_b128 v[208:211], v246 offset:24832\n" \
"v_exp_f32 v114, v114\n" \
"v_mfma_f32_16x16x32_bf16 v[60:63], a[240:243], a[176:179], v[236:239]\n" \
"ds_read_b128 v[212:215], v246 offset:26880\n" \
"global_load_lds_dwordx4 v253, s[50:51] offset:1024\n" \
"v_mfma_f32_16x16x32_bf16 v[60:63], a[244:247], a[180:183], v[60:63]\n" \
"s_add_u32 s50, s50, 0x120000\n" \
"s_addc_u32 s51, s51, 0\n" \
"ds_read_b128 v[216:219], v246 offset:28928\n" \
"v_mfma_f32_16x16x32_bf16 v[60:63], a[248:251], a[184:187], v[60:63]\n" \
"ds_read_b128 v[220:223], v246 offset:30976\n" \
"v_cvt_pk_bf16_f32 v112, v112, v113\n" \
"v_mfma_f32_16x16x32_bf16 v[60:63], a[252:255], a[188:191], v[60:63]\n" \
"global_load_dword v85, v84, s[22:23]\n" \
"v_mfma_f32_16x16x32_bf16 v[52:55], a[248:251], a[168:171], v[232:235]\n" \
"v_exp_f32 v115, v115\n" \
"v_mfma_f32_16x16x32_bf16 v[52:55], a[252:255], a[172:175], v[52:55]\n" \
"v_exp_f32 v116, v116\n" \
"v_cvt_pk_bf16_f32 v113, v114, v115\n" \
"v_mfma_f32_16x16x32_bf16 v[52:55], a[240:243], a[160:163], v[52:55]\n" \
"v_exp_f32 v117, v117\n" \
"v_mfma_f32_16x16x32_bf16 v[52:55], a[244:247], a[164:167], v[52:55]\n" \
"s_waitcnt lgkmcnt(0)\n" \
"ds_read_b128 a[240:243], v247 offset:38912\n" \
"v_exp_f32 v118, v118\n" \
"v_mfma_f32_16x16x32_bf16 v[40:43], v[192:195], a[176:179], v[236:239]\n" \
"ds_read_b128 a[244:247], v247 offset:47104\n" \
"v_cvt_pk_bf16_f32 v114, v116, v117\n" \
"v_mfma_f32_16x16x32_bf16 v[40:43], v[196:199], a[180:183], v[40:43]\n" \
"ds_read_b128 a[248:251], v247 offset:39936\n" \
"v_exp_f32 v119, v119\n" \
"v_exp_f32 v120, v120\n" \
"v_mfma_f32_16x16x32_bf16 v[40:43], v[200:203], a[184:187], v[40:43]\n" \
"ds_read_b128 a[252:255], v247 offset:48128\n" \
"v_cvt_pk_bf16_f32 v115, v118, v119\n" \
"v_mfma_f32_16x16x32_bf16 v[40:43], v[204:207], a[188:191], v[40:43]\n" \
"v_exp_f32 v121, v121\n" \
"v_exp_f32 v122, v122\n" \
"v_cvt_pk_bf16_f32 v120, v120, v121\n" \
"v_mfma_f32_16x16x32_bf16 v[44:47], v[216:219], a[184:187], v[236:239]\n" \
"v_exp_f32 v123, v123\n" \
"v_mfma_f32_16x16x32_bf16 v[44:47], v[220:223], a[188:191], v[44:47]\n" \
"v_exp_f32 v124, v124\n" \
"v_mfma_f32_16x16x32_bf16 v[44:47], v[208:211], a[176:179], v[44:47]\n" \
"s_waitcnt vmcnt(12) lgkmcnt(0)\n" \
"v_add_u32 v247, 0x10000, v248\n" \
"v_cvt_pk_bf16_f32 v121, v122, v123\n" \
"v_mfma_f32_16x16x32_bf16 v[44:47], v[212:215], a[180:183], v[44:47]\n" \
"s_barrier\n" \
"v_exp_f32 v125, v125\n" \
"v_exp_f32 v126, v126\n" \
"v_cvt_pk_bf16_f32 v122, v124, v125\n" \
"v_mfma_f32_16x16x32_bf16 a[64:67], a[192:195], v[64:67], a[64:67]\n" \
"ds_read_b128 v[192:195], v247 offset:16384\n" \
"v_exp_f32 v127, v127\n" \
"v_mfma_f32_16x16x32_bf16 a[64:67], a[196:199], v[96:99], a[64:67]\n" \
"v_cvt_pk_bf16_f32 v123, v126, v127\n" \
"v_mfma_f32_16x16x32_bf16 a[96:99], a[192:195], v[72:75], a[96:99]\n" \
"s_lshl_b32 s70, s61, 14\n" \
"v_add_u32 v124, s70, v248\n" \
"v_xor_b32 v125, 0x4000, v124\n" \
"v_mfma_f32_16x16x32_bf16 a[96:99], a[196:199], v[104:107], a[96:99]\n" \
"s_lshl_b32 s70, s61, 9\n" \
"v_cmp_gt_i32 vcc, 0, v251\n" \
"v_cmp_gt_i32 s[64:65], 1, v251\n" \
"v_cmp_gt_i32 s[66:67], 2, v251\n" \
"v_cndmask_b32 v8, v8, v254, vcc\n" \
"v_mfma_f32_16x16x32_bf16 v[238:241], a[0:3], v[96:99], v[238:241]\n" \
"v_cndmask_b32 v16, v16, v254, vcc\n" \
"v_cmp_gt_i32 vcc, 3, v251\n" \
"v_mfma_f32_16x16x32_bf16 v[238:241], a[4:7], v[104:107], v[238:241]\n" \
"v_cndmask_b32 v9, v9, v254, s[64:65]\n" \
"v_cndmask_b32 v17, v17, v254, s[64:65]\n" \
"v_mfma_f32_16x16x32_bf16 v[128:131], a[192:195], v[80:83], v[128:131]\n" \
"s_add_u32 s70, s70, 110592\n" \
"ds_read_b128 v[196:199], v247 offset:24576\n" \
"v_lshrrev_b32 v126, 1, v248\n" \
"v_mfma_f32_16x16x32_bf16 v[128:131], a[196:199], v[112:115], v[128:131]\n" \
"v_add_u32 v126, s70, v126\n" \
"v_xor_b32 v127, 0x200, v126\n" \
"v_cmp_gt_i32 s[64:65], 16, v251\n" \
"v_cndmask_b32 v10, v10, v254, s[66:67]\n" \
"v_cndmask_b32 v18, v18, v254, s[66:67]\n" \
"v_cmp_gt_i32 s[66:67], 17, v251\n" \
"v_cndmask_b32 v11, v11, v254, vcc\n" \
"v_cndmask_b32 v19, v19, v254, vcc\n" \
"v_mfma_f32_16x16x32_bf16 v[160:163], a[192:195], v[88:91], v[160:163]\n" \
"v_lshrrev_b32 v71, 7, v248\n" \
"v_and_b32 v76, 0x70, v248\n" \
"v_mfma_f32_16x16x32_bf16 v[160:163], a[196:199], v[120:123], v[160:163]\n" \
"ds_read_b128 a[192:195], v247 offset:0\n" \
"v_lshlrev_b32 v77, 4, v71\n" \
"v_cmp_gt_i32 vcc, 18, v251\n" \
"v_cndmask_b32 v12, v12, v254, s[64:65]\n" \
"v_mfma_f32_16x16x32_bf16 v[242:245], a[0:3], v[112:115], v[242:245]\n" \
"v_cndmask_b32 v20, v20, v254, s[64:65]\n" \
"v_cmp_gt_i32 s[64:65], 19, v251\n" \
"v_mfma_f32_16x16x32_bf16 v[242:245], a[4:7], v[120:123], v[242:245]\n" \
"v_cndmask_b32 v13, v13, v254, s[66:67]\n" \
"v_cndmask_b32 v21, v21, v254, s[66:67]\n" \
"v_cmp_gt_i32 s[66:67], 32, v251\n" \
"v_cndmask_b32 v14, v14, v254, vcc\n" \
"v_mfma_f32_16x16x32_bf16 v[164:167], a[200:203], v[88:91], v[164:167]\n" \
"ds_read_b128 v[200:203], v247 offset:17408\n" \
"v_xor_b32 v77, v77, v76\n" \
"v_mfma_f32_16x16x32_bf16 v[164:167], a[204:207], v[120:123], v[164:167]\n" \
"ds_write_b128 v124, a[64:67] offset:0\n" \
"v_lshl_add_u32 v252, v71, 12, v77\n" \
"v_cndmask_b32 v22, v22, v254, vcc\n" \
"v_cmp_gt_i32 vcc, 33, v251\n" \
"v_cndmask_b32 v15, v15, v254, s[64:65]\n" \
"v_cndmask_b32 v23, v23, v254, s[64:65]\n" \
"v_cmp_gt_i32 s[64:65], 34, v251\n" \
"v_cndmask_b32 v40, v40, v254, s[66:67]\n" \
"v_cndmask_b32 v48, v48, v254, s[66:67]\n" \
"v_cmp_gt_i32 s[66:67], 35, v251\n" \
"v_cndmask_b32 v41, v41, v254, vcc\n" \
"v_cndmask_b32 v49, v49, v254, vcc\n" \
"v_mfma_f32_16x16x32_bf16 v[132:135], a[200:203], v[80:83], v[132:135]\n" \
"v_and_or_b32 v71, v71, 3, s86\n" \
"v_lshlrev_b32 v77, 4, v71\n" \
"v_mfma_f32_16x16x32_bf16 v[132:135], a[204:207], v[112:115], v[132:135]\n" \
"ds_read_b128 a[196:199], v247 offset:8192\n" \
"v_xor_b32 v77, v77, v76\n" \
"v_cmp_gt_i32 vcc, 48, v251\n" \
"v_cndmask_b32 v42, v42, v254, s[64:65]\n" \
"v_mfma_f32_16x16x32_bf16 a[100:103], a[200:203], v[72:75], a[100:103]\n" \
"ds_read_b128 v[204:207], v247 offset:25600\n" \
"v_lshl_add_u32 v246, v71, 12, v77\n" \
"v_mfma_f32_16x16x32_bf16 a[100:103], a[204:207], v[104:107], a[100:103]\n" \
"ds_write_b128 v124, a[96:99] offset:8192\n" \
"v_cndmask_b32 v50, v50, v254, s[64:65]\n" \
"v_mfma_f32_16x16x32_bf16 a[68:71], a[200:203], v[64:67], a[68:71]\n" \
"v_lshrrev_b32 v71, 9, v248\n" \
"v_lshl_add_u32 v246, v71, 30, v246\n" \
"v_mfma_f32_16x16x32_bf16 a[68:71], a[204:207], v[96:99], a[68:71]\n" \
"s_add_u32 m0, s95, 118784\n" \
"ds_read_b128 a[200:203], v247 offset:1024\n" \
"v_cmp_gt_i32 s[64:65], 49, v251\n" \
"v_mfma_f32_16x16x32_bf16 a[72:75], a[208:211], v[64:67], a[72:75]\n" \
"ds_read_b128 v[208:211], v247 offset:18432\n" \
"global_load_lds_dwordx4 v252, s[42:43] offset:0\n" \
"v_mfma_f32_16x16x32_bf16 a[72:75], a[212:215], v[96:99], a[72:75]\n" \
"ds_write_b128 v124, a[100:103] offset:9216\n" \
"global_load_lds_dwordx4 v252, s[42:43] offset:2048\n" \
"v_mfma_f32_16x16x32_bf16 a[104:107], a[208:211], v[72:75], a[104:107]\n" \
"v_cndmask_b32 v43, v43, v254, s[66:67]\n" \
"v_cndmask_b32 v51, v51, v254, s[66:67]\n" \
"v_mfma_f32_16x16x32_bf16 a[104:107], a[212:215], v[104:107], a[104:107]\n" \
"ds_read_b128 a[204:207], v247 offset:9216\n" \
"ds_write_b64 v126, v[240:241]\n" \
"v_mfma_f32_16x16x32_bf16 v[136:139], a[208:211], v[80:83], v[136:139]\n" \
"s_add_u32 s42, s42, 0x180\n" \
"s_addc_u32 s43, s43, 0\n" \
"ds_read_b128 v[212:215], v247 offset:26624\n" \
"v_mfma_f32_16x16x32_bf16 v[136:139], a[212:215], v[112:115], v[136:139]\n" \
"ds_write_b128 v124, a[68:71] offset:1024\n" \
"global_load_lds_dwordx4 v252, s[44:45] offset:1024\n" \
"v_mfma_f32_16x16x32_bf16 v[168:171], a[208:211], v[88:91], v[168:171]\n" \
"v_cmp_gt_i32 s[66:67], 50, v251\n" \
"v_cndmask_b32 v44, v44, v254, vcc\n" \
"v_mfma_f32_16x16x32_bf16 v[168:171], a[212:215], v[120:123], v[168:171]\n" \
"ds_read_b128 a[208:211], v247 offset:2048\n" \
"v_cndmask_b32 v52, v52, v254, vcc\n" \
"v_mfma_f32_16x16x32_bf16 v[172:175], a[216:219], v[88:91], v[172:175]\n" \
"ds_read_b128 v[216:219], v247 offset:19456\n" \
"v_cmp_gt_i32 vcc, 51, v251\n" \
"v_mfma_f32_16x16x32_bf16 v[172:175], a[220:223], v[120:123], v[172:175]\n" \
"ds_write_b128 v124, a[72:75] offset:2048\n" \
"global_load_lds_dwordx4 v252, s[44:45] offset:3072\n" \
"v_mfma_f32_16x16x32_bf16 v[140:143], a[216:219], v[80:83], v[140:143]\n" \
"s_add_u32 s44, s44, 0x180\n" \
"s_addc_u32 s45, s45, 0\n" \
"v_cndmask_b32 v45, v45, v254, s[64:65]\n" \
"v_mfma_f32_16x16x32_bf16 v[140:143], a[220:223], v[112:115], v[140:143]\n" \
"s_add_u32 m0, s85, 135168\n" \
"ds_read_b128 a[212:215], v247 offset:10240\n" \
"v_cndmask_b32 v53, v53, v254, s[64:65]\n" \
"v_mfma_f32_16x16x32_bf16 a[108:111], a[216:219], v[72:75], a[108:111]\n" \
"ds_read_b128 v[220:223], v247 offset:27648\n" \
"v_cndmask_b32 v46, v46, v254, s[66:67]\n" \
"v_mfma_f32_16x16x32_bf16 a[108:111], a[220:223], v[104:107], a[108:111]\n" \
"ds_write_b128 v124, a[104:107] offset:10240\n" \
"v_cndmask_b32 v54, v54, v254, s[66:67]\n" \
"v_mfma_f32_16x16x32_bf16 a[76:79], a[216:219], v[64:67], a[76:79]\n" \
"buffer_load_dwordx4 v246, s[12:15], s46 offen lds\n" \
"v_mfma_f32_16x16x32_bf16 a[76:79], a[220:223], v[96:99], a[76:79]\n" \
"s_add_u32 s46, s46, 0x180\n" \
"ds_read_b128 a[216:219], v247 offset:3072\n" \
"v_cndmask_b32 v47, v47, v254, vcc\n" \
"v_mfma_f32_16x16x32_bf16 a[80:83], a[224:227], v[64:67], a[80:83]\n" \
"ds_read_b128 v[224:227], v247 offset:20480\n" \
"v_cndmask_b32 v55, v55, v254, vcc\n" \
"v_mfma_f32_16x16x32_bf16 a[80:83], a[228:231], v[96:99], a[80:83]\n" \
"ds_write_b128 v124, a[108:111] offset:11264\n" \
"v_exp_f32 v8, v8\n" \
"v_exp_f32 v9, v9\n" \
"v_exp_f32 v10, v10\n" \
"v_cvt_pk_bf16_f32 v8, v8, v9\n" \
"v_mfma_f32_16x16x32_bf16 a[112:115], a[224:227], v[72:75], a[112:115]\n" \
"v_exp_f32 v11, v11\n" \
"v_mfma_f32_16x16x32_bf16 a[112:115], a[228:231], v[104:107], a[112:115]\n" \
"ds_read_b128 a[220:223], v247 offset:11264\n" \
"v_exp_f32 v12, v12\n" \
"v_mfma_f32_16x16x32_bf16 v[144:147], a[224:227], v[80:83], v[144:147]\n" \
"ds_read_b128 v[228:231], v247 offset:28672\n" \
"v_cvt_pk_bf16_f32 v9, v10, v11\n" \
"v_mfma_f32_16x16x32_bf16 v[144:147], a[228:231], v[112:115], v[144:147]\n" \
"ds_write_b128 v124, a[76:79] offset:3072\n" \
"v_exp_f32 v13, v13\n" \
"v_exp_f32 v14, v14\n" \
"v_cvt_pk_bf16_f32 v10, v12, v13\n" \
"v_mfma_f32_16x16x32_bf16 v[176:179], a[224:227], v[88:91], v[176:179]\n" \
"v_exp_f32 v15, v15\n" \
"v_mfma_f32_16x16x32_bf16 v[176:179], a[228:231], v[120:123], v[176:179]\n" \
"ds_read_b128 a[224:227], v247 offset:4096\n" \
"v_exp_f32 v40, v40\n" \
"v_mfma_f32_16x16x32_bf16 v[180:183], a[232:235], v[88:91], v[180:183]\n" \
"ds_read_b128 v[232:235], v247 offset:21504\n" \
"v_cvt_pk_bf16_f32 v11, v14, v15\n" \
"v_mfma_f32_16x16x32_bf16 v[180:183], a[236:239], v[120:123], v[180:183]\n" \
"ds_write_b128 v124, a[80:83] offset:4096\n" \
"v_exp_f32 v41, v41\n" \
"v_exp_f32 v42, v42\n" \
"v_cvt_pk_bf16_f32 v40, v40, v41\n" \
"v_mfma_f32_16x16x32_bf16 v[148:151], a[232:235], v[80:83], v[148:151]\n" \
"v_exp_f32 v43, v43\n" \
"v_mfma_f32_16x16x32_bf16 v[148:151], a[236:239], v[112:115], v[148:151]\n" \
"ds_read_b128 a[228:231], v247 offset:12288\n" \
"v_exp_f32 v44, v44\n" \
"v_mfma_f32_16x16x32_bf16 a[116:119], a[232:235], v[72:75], a[116:119]\n" \
"ds_read_b128 v[236:239], v247 offset:29696\n" \
"v_cvt_pk_bf16_f32 v41, v42, v43\n" \
"v_mfma_f32_16x16x32_bf16 a[116:119], a[236:239], v[104:107], a[116:119]\n" \
"ds_write_b128 v124, a[112:115] offset:12288\n" \
"v_exp_f32 v45, v45\n" \
"v_exp_f32 v46, v46\n" \
"v_cvt_pk_bf16_f32 v42, v44, v45\n" \
"v_mfma_f32_16x16x32_bf16 a[84:87], a[232:235], v[64:67], a[84:87]\n" \
"v_exp_f32 v47, v47\n" \
"v_mfma_f32_16x16x32_bf16 a[84:87], a[236:239], v[96:99], a[84:87]\n" \
"ds_read_b128 a[232:235], v247 offset:5120\n" \
"v_exp_f32 v24, v24\n" \
"v_mfma_f32_16x16x32_bf16 a[88:91], a[240:243], v[64:67], a[88:91]\n" \
"ds_read_b128 v[0:3], v247 offset:22528\n" \
"v_cvt_pk_bf16_f32 v43, v46, v47\n" \
"v_mfma_f32_16x16x32_bf16 a[88:91], a[244:247], v[96:99], a[88:91]\n" \
"ds_write_b128 v124, a[116:119] offset:13312\n" \
"v_exp_f32 v25, v25\n" \
"v_exp_f32 v26, v26\n" \
"v_cvt_pk_bf16_f32 v24, v24, v25\n" \
"v_mfma_f32_16x16x32_bf16 v[242:245], a[4:7], v[8:11], v[242:245]\n" \
"v_exp_f32 v27, v27\n" \
"v_mfma_f32_16x16x32_bf16 v[242:245], a[4:7], v[40:43], v[242:245]\n" \
"v_exp_f32 v28, v28\n" \
"v_cvt_pk_bf16_f32 v25, v26, v27\n" \
"v_mfma_f32_16x16x32_bf16 a[120:123], a[240:243], v[72:75], a[120:123]\n" \
"v_exp_f32 v29, v29\n" \
"v_mfma_f32_16x16x32_bf16 a[120:123], a[244:247], v[104:107], a[120:123]\n" \
"ds_read_b128 a[236:239], v247 offset:13312\n" \
"v_exp_f32 v30, v30\n" \
"v_mfma_f32_16x16x32_bf16 v[152:155], a[240:243], v[80:83], v[152:155]\n" \
"ds_read_b128 v[4:7], v247 offset:30720\n" \
"v_cvt_pk_bf16_f32 v26, v28, v29\n" \
"v_mfma_f32_16x16x32_bf16 v[152:155], a[244:247], v[112:115], v[152:155]\n" \
"ds_write_b128 v124, a[84:87] offset:5120\n" \
"v_exp_f32 v31, v31\n" \
"v_exp_f32 v56, v56\n" \
"v_cvt_pk_bf16_f32 v27, v30, v31\n" \
"v_mfma_f32_16x16x32_bf16 v[184:187], a[240:243], v[88:91], v[184:187]\n" \
"v_exp_f32 v57, v57\n" \
"v_mfma_f32_16x16x32_bf16 v[184:187], a[244:247], v[120:123], v[184:187]\n" \
"ds_read_b128 a[240:243], v247 offset:6144\n" \
"v_exp_f32 v58, v58\n" \
"v_mfma_f32_16x16x32_bf16 a[92:95], a[248:251], v[64:67], a[92:95]\n" \
"ds_read_b128 v[32:35], v247 offset:23552\n" \
"v_cvt_pk_bf16_f32 v56, v56, v57\n" \
"v_mfma_f32_16x16x32_bf16 a[92:95], a[252:255], v[96:99], a[92:95]\n" \
"ds_write_b128 v124, a[88:91] offset:6144\n" \
"v_exp_f32 v59, v59\n" \
"v_exp_f32 v60, v60\n" \
"v_cvt_pk_bf16_f32 v57, v58, v59\n" \
"v_mfma_f32_16x16x32_bf16 a[124:127], a[248:251], v[72:75], a[124:127]\n" \
"v_exp_f32 v61, v61\n" \
"v_mfma_f32_16x16x32_bf16 a[124:127], a[252:255], v[104:107], a[124:127]\n" \
"ds_read_b128 a[244:247], v247 offset:14336\n" \
"v_exp_f32 v62, v62\n" \
"v_mfma_f32_16x16x32_bf16 v[156:159], a[248:251], v[80:83], v[156:159]\n" \
"ds_read_b128 v[36:39], v247 offset:31744\n" \
"v_cvt_pk_bf16_f32 v58, v60, v61\n" \
"v_mfma_f32_16x16x32_bf16 v[156:159], a[252:255], v[112:115], v[156:159]\n" \
"ds_write_b128 v124, a[120:123] offset:14336\n" \
"v_exp_f32 v63, v63\n" \
"v_mfma_f32_16x16x32_bf16 v[188:191], a[248:251], v[88:91], v[188:191]\n" \
"v_exp_f32 v16, v16\n" \
"v_mfma_f32_16x16x32_bf16 v[188:191], a[252:255], v[120:123], v[188:191]\n" \
"s_waitcnt lgkmcnt(3)\n" \
"ds_read_b128 a[248:251], v247 offset:7168\n" \
"ds_write_b128 v124, a[92:95] offset:7168\n" \
"v_cvt_pk_bf16_f32 v59, v62, v63\n" \
"v_exp_f32 v17, v17\n" \
"v_mfma_f32_16x16x32_bf16 v[160:163], v[192:195], v[8:11], v[160:163]\n" \
"v_exp_f32 v18, v18\n" \
"v_mfma_f32_16x16x32_bf16 v[160:163], v[196:199], v[40:43], v[160:163]\n" \
"ds_read_b128 a[252:255], v247 offset:15360\n" \
"v_cvt_pk_bf16_f32 v16, v16, v17\n" \
"v_mfma_f32_16x16x32_bf16 v[242:245], a[4:7], v[24:27], v[242:245]\n" \
"v_exp_f32 v19, v19\n" \
"v_mfma_f32_16x16x32_bf16 v[242:245], a[4:7], v[56:59], v[242:245]\n" \
"v_exp_f32 v20, v20\n" \
"v_cvt_pk_bf16_f32 v17, v18, v19\n" \
"v_exp_f32 v21, v21\n" \
"v_mfma_f32_16x16x32_bf16 v[164:167], v[200:203], v[8:11], v[164:167]\n" \
"v_exp_f32 v22, v22\n" \
"v_mfma_f32_16x16x32_bf16 v[164:167], v[204:207], v[40:43], v[164:167]\n" \
"ds_write_b128 v124, a[124:127] offset:15360\n" \
"v_cvt_pk_bf16_f32 v18, v20, v21\n" \
"v_exp_f32 v23, v23\n" \
"v_mfma_f32_16x16x32_bf16 v[168:171], v[208:211], v[8:11], v[168:171]\n" \
"v_exp_f32 v48, v48\n" \
"v_mfma_f32_16x16x32_bf16 v[168:171], v[212:215], v[40:43], v[168:171]\n" \
"v_cvt_pk_bf16_f32 v19, v22, v23\n" \
"v_exp_f32 v49, v49\n" \
"v_mfma_f32_16x16x32_bf16 v[172:175], v[216:219], v[8:11], v[172:175]\n" \
"s_waitcnt vmcnt(0) lgkmcnt(0)\n" \
"v_exp_f32 v50, v50\n" \
"v_mfma_f32_16x16x32_bf16 v[172:175], v[220:223], v[40:43], v[172:175]\n" \
"s_barrier\n" \
"v_cvt_pk_bf16_f32 v48, v48, v49\n" \
"v_mfma_f32_16x16x32_bf16 v[242:245], a[0:3], v[16:19], v[242:245]\n" \
"v_exp_f32 v51, v51\n" \
"v_exp_f32 v52, v52\n" \
"v_mfma_f32_16x16x32_bf16 v[176:179], v[224:227], v[8:11], v[176:179]\n" \
"s_add_u32 m0, s84, 90112\n" \
"ds_read_b64 v[240:241], v127\n" \
"v_cvt_pk_bf16_f32 v49, v50, v51\n" \
"v_mfma_f32_16x16x32_bf16 v[176:179], v[228:231], v[40:43], v[176:179]\n" \
"ds_read_b128 v[64:67], v125 offset:0\n" \
"global_load_lds_dwordx4 v253, s[48:49] offset:-4096\n" \
"v_exp_f32 v53, v53\n" \
"v_exp_f32 v54, v54\n" \
"v_mfma_f32_16x16x32_bf16 v[180:183], v[232:235], v[8:11], v[180:183]\n" \
"ds_read_b128 v[68:71], v125 offset:1024\n" \
"global_load_lds_dwordx4 v253, s[48:49] offset:-2048\n" \
"v_mfma_f32_16x16x32_bf16 v[180:183], v[236:239], v[40:43], v[180:183]\n" \
"ds_read_b128 v[72:75], v125 offset:2048\n" \
"v_cvt_pk_bf16_f32 v50, v52, v53\n" \
"v_mfma_f32_16x16x32_bf16 v[184:187], v[0:3], v[8:11], v[184:187]\n" \
"ds_read_b128 v[76:79], v125 offset:3072\n" \
"global_load_lds_dwordx4 v253, s[48:49] offset:0\n" \
"v_mfma_f32_16x16x32_bf16 v[184:187], v[4:7], v[40:43], v[184:187]\n" \
"s_add_u32 s48, s48, 0x300000\n" \
"s_addc_u32 s49, s49, 0\n" \
"ds_read_b128 v[80:83], v125 offset:4096\n" \
"v_mfma_f32_16x16x32_bf16 v[188:191], v[32:35], v[8:11], v[188:191]\n" \
"ds_read_b128 v[84:87], v125 offset:5120\n" \
"global_load_lds_dwordx4 v253, s[50:51] offset:-3072\n" \
"v_mfma_f32_16x16x32_bf16 v[188:191], v[36:39], v[40:43], v[188:191]\n" \
"s_waitcnt lgkmcnt(5)\n" \
"ds_read_b128 v[88:91], v125 offset:6144\n" \
"v_exp_f32 v55, v55\n" \
"v_mfma_f32_16x16x32_bf16 v[160:163], a[192:195], v[24:27], v[160:163]\n" \
"ds_read_b128 v[92:95], v125 offset:7168\n" \
"global_load_lds_dwordx4 v253, s[50:51] offset:-1024\n" \
"v_mfma_f32_16x16x32_bf16 v[160:163], a[196:199], v[56:59], v[160:163]\n" \
"s_movk_i32 s89, 0xc0\n" \
"ds_read_b128 v[96:99], v125 offset:8192\n" \
"global_load_lds_dwordx4 v253, s[50:51] offset:1024\n" \
"v_mfma_f32_16x16x32_bf16 v[164:167], a[200:203], v[24:27], v[164:167]\n" \
"s_add_u32 s50, s50, 0x300000\n" \
"s_addc_u32 s51, s51, 0\n" \
"ds_read_b128 v[100:103], v125 offset:9216\n" \
"v_mfma_f32_16x16x32_bf16 v[164:167], a[204:207], v[56:59], v[164:167]\n" \
"s_add_u32 m0, s95, 65536\n" \
"s_movk_i32 s90, 288\n" \
"ds_read_b128 v[104:107], v125 offset:10240\n" \
"v_lshrrev_b32 v211, 4, v248\n" \
"v_lshrrev_b32 v214, 8, v248\n" \
"v_and_b32 v215, 7, v211\n" \
"v_xor_b32 v214, v214, v215\n" \
"v_pk_add_f32 v[128:129], v[128:129], v[64:65]\n" \
"v_pk_add_f32 v[130:131], v[130:131], v[66:67]\n" \
"s_waitcnt lgkmcnt(9)\n" \
"v_pk_add_f32 v[132:133], v[132:133], v[68:69]\n" \
"v_pk_add_f32 v[134:135], v[134:135], v[70:71]\n" \
"s_waitcnt lgkmcnt(8)\n" \
"v_pk_add_f32 v[136:137], v[136:137], v[72:73]\n" \
"v_pk_add_f32 v[138:139], v[138:139], v[74:75]\n" \
"s_waitcnt lgkmcnt(7)\n" \
"v_pk_add_f32 v[140:141], v[140:141], v[76:77]\n" \
"v_pk_add_f32 v[142:143], v[142:143], v[78:79]\n" \
"s_waitcnt lgkmcnt(6)\n" \
"v_pk_add_f32 v[144:145], v[144:145], v[80:81]\n" \
"v_pk_add_f32 v[146:147], v[146:147], v[82:83]\n" \
"s_waitcnt lgkmcnt(5)\n" \
"v_pk_add_f32 v[148:149], v[148:149], v[84:85]\n" \
"v_pk_add_f32 v[150:151], v[150:151], v[86:87]\n" \
"s_waitcnt lgkmcnt(4)\n" \
"v_pk_add_f32 v[152:153], v[152:153], v[88:89]\n" \
"v_pk_add_f32 v[154:155], v[154:155], v[90:91]\n" \
"s_waitcnt lgkmcnt(3)\n" \
"v_pk_add_f32 v[156:157], v[156:157], v[92:93]\n" \
"v_pk_add_f32 v[158:159], v[158:159], v[94:95]\n" \
"v_mfma_f32_16x16x32_bf16 v[168:171], a[208:211], v[24:27], v[168:171]\n" \
"s_lshr_b32 s71, s61, 1\n" \
"ds_read_b128 v[108:111], v125 offset:11264\n" \
"global_load_lds_dwordx4 v252, s[42:43] offset:0\n" \
"v_mfma_f32_16x16x32_bf16 v[168:171], a[212:215], v[56:59], v[168:171]\n" \
"s_lshl_b32 s70, s71, 11\n" \
"ds_read_b128 v[112:115], v125 offset:12288\n" \
"v_cvt_pk_bf16_f32 v51, v54, v55\n" \
"v_mfma_f32_16x16x32_bf16 v[172:175], a[216:219], v[24:27], v[172:175]\n" \
"s_waitcnt lgkmcnt(4)\n" \
"s_add_u32 s70, s70, 118784\n" \
"ds_read_b128 v[116:119], v125 offset:13312\n" \
"global_load_lds_dwordx4 v252, s[42:43] offset:2048\n" \
"v_mfma_f32_16x16x32_bf16 v[242:245], a[0:3], v[48:51], v[242:245]\n" \
"v_and_b32 v216, 15, v211\n" \
"v_lshlrev_b32 v213, 7, v216\n" \
"v_lshl_add_u32 v213, v214, 4, v213\n" \
"v_mfma_f32_16x16x32_bf16 v[172:175], a[220:223], v[56:59], v[172:175]\n" \
"s_add_u32 s42, s42, 0x400\n" \
"s_addc_u32 s43, s43, 0\n" \
"ds_read_b128 v[120:123], v125 offset:14336\n" \
"v_add_u32 v205, s70, v213\n" \
"v_xor_b32 v206, 64, v205\n" \
"v_lshrrev_b32 v216, 2, v216\n" \
"v_lshl_add_u32 v216, v216, 9, v213\n" \
"v_add_u32 v207, 135168, v216\n" \
"v_xor_b32 v208, 64, v207\n" \
"v_pk_add_f32 v[160:161], v[160:161], v[96:97]\n" \
"v_pk_add_f32 v[162:163], v[162:163], v[98:99]\n" \
"v_mfma_f32_16x16x32_bf16 v[176:179], a[224:227], v[24:27], v[176:179]\n" \
"s_and_b32 s70, s61, 1\n" \
"ds_read_b128 v[124:127], v125 offset:15360\n" \
"s_lshl_b32 s70, s70, 9\n" \
"v_mfma_f32_16x16x32_bf16 v[176:179], a[228:231], v[56:59], v[176:179]\n" \
"s_waitcnt lgkmcnt(6)\n" \
"global_load_lds_dwordx4 v252, s[44:45] offset:1024\n" \
"global_load_lds_dwordx4 v252, s[44:45] offset:3072\n" \
"v_mfma_f32_16x16x32_bf16 v[180:183], a[232:235], v[24:27], v[180:183]\n" \
"s_add_u32 s70, s70, 139264\n" \
"s_add_u32 s44, s44, 0x400\n" \
"s_addc_u32 s45, s45, 0\n" \
"v_mfma_f32_16x16x32_bf16 v[180:183], a[236:239], v[56:59], v[180:183]\n" \
"s_add_u32 m0, s85, 81920\n" \
"s_lshl_b32 s71, s71, 5\n" \
"v_and_b32 v213, 3, v211\n" \
"v_lshlrev_b32 v213, 3, v213\n" \
"v_mfma_f32_16x16x32_bf16 v[184:187], a[240:243], v[24:27], v[184:187]\n" \
"s_waitcnt lgkmcnt(0)\n" \
"v_and_or_b32 v213, v248, s89, v213\n" \
"v_bfe_u32 v214, v211, 4, 1\n" \
"v_mfma_f32_16x16x32_bf16 v[184:187], a[244:247], v[56:59], v[184:187]\n" \
"s_barrier\n" \
"v_mad_u32_u24 v213, v214, s90, v213\n" \
"v_lshrrev_b32 v214, 5, v211\n" \
"v_mul_u32_u24 v214, 6144, v214\n" \
"v_add3_u32 v213, v213, v214, s70\n" \
"v_pk_add_f32 v[164:165], v[164:165], v[100:101]\n" \
"v_pk_add_f32 v[166:167], v[166:167], v[102:103]\n" \
"v_pk_add_f32 v[168:169], v[168:169], v[104:105]\n" \
"v_pk_add_f32 v[170:171], v[170:171], v[106:107]\n" \
"v_pk_add_f32 v[172:173], v[172:173], v[108:109]\n" \
"v_pk_add_f32 v[174:175], v[174:175], v[110:111]\n" \
"Lpg8622_%=:\n" \
"Lst1x2_%=:\n" \
"s_cbranch_execz Lpg10886_%=\n" \
"v_mfma_f32_16x16x32_bf16 v[188:191], a[248:251], v[24:27], v[188:191]\n" \
"v_xor_b32 v209, s71, v213\n" \
"v_xor_b32 v210, 32, v209\n" \
"v_mfma_f32_16x16x32_bf16 v[188:191], a[252:255], v[56:59], v[188:191]\n" \
"ds_read_b128 a[64:67], v207 offset:0\n" \
"ds_read_b64_tr_b16 a[96:97], v209 offset:2048\n" \
"v_mfma_f32_16x16x32_bf16 v[128:131], a[192:195], v[16:19], v[128:131]\n" \
"ds_read_b64_tr_b16 a[98:99], v209 offset:3072\n" \
"v_mfma_f32_16x16x32_bf16 v[128:131], a[196:199], v[48:51], v[128:131]\n" \
"ds_read_b128 a[68:71], v208 offset:0\n" \
"v_mfma_f32_16x16x32_bf16 v[132:135], a[200:203], v[16:19], v[132:135]\n" \
"ds_read_b64_tr_b16 a[100:101], v209 offset:14336\n" \
"ds_read_b64_tr_b16 a[102:103], v209 offset:15360\n" \
"buffer_load_dwordx4 v246, s[12:15], s46 offen lds\n" \
"v_pk_add_f32 v[176:177], v[176:177], v[112:113]\n" \
"v_pk_add_f32 v[178:179], v[178:179], v[114:115]\n" \
"v_pk_add_f32 v[180:181], v[180:181], v[116:117]\n" \
"v_pk_add_f32 v[182:183], v[182:183], v[118:119]\n" \
"v_pk_add_f32 v[184:185], v[184:185], v[120:121]\n" \
"v_pk_add_f32 v[186:187], v[186:187], v[122:123]\n" \
"v_pk_add_f32 v[188:189], v[188:189], v[124:125]\n" \
"v_pk_add_f32 v[190:191], v[190:191], v[126:127]\n" \
"v_mfma_f32_16x16x32_bf16 v[132:135], a[204:207], v[48:51], v[132:135]\n" \
"ds_read_b128 a[72:75], v205 offset:0\n" \
"s_add_u32 s46, s46, 0x400\n" \
"v_mfma_f32_16x16x32_bf16 v[136:139], a[208:211], v[16:19], v[136:139]\n" \
"s_add_u32 m0, s95, 0\n" \
"ds_read_b64_tr_b16 a[104:105], v209 offset:0\n" \
"v_mfma_f32_16x16x32_bf16 v[136:139], a[212:215], v[48:51], v[136:139]\n" \
"ds_read_b128 a[76:79], v206 offset:0\n" \
"ds_read_b64_tr_b16 a[106:107], v209 offset:1024\n" \
"v_mfma_f32_16x16x32_bf16 v[140:143], a[216:219], v[16:19], v[140:143]\n" \
"ds_read_b64_tr_b16 a[108:109], v209 offset:12288\n" \
"global_load_lds_dwordx4 v252, s[42:43] offset:0\n" \
"v_mfma_f32_16x16x32_bf16 v[140:143], a[220:223], v[48:51], v[140:143]\n" \
"ds_read_b64_tr_b16 a[110:111], v209 offset:13312\n" \
"global_load_lds_dwordx4 v252, s[42:43] offset:2048\n" \
"v_mfma_f32_16x16x32_bf16 v[144:147], a[224:227], v[16:19], v[144:147]\n" \
"ds_read_b128 a[80:83], v205 offset:4096\n" \
"ds_read_b64_tr_b16 a[112:113], v210 offset:0\n" \
"v_mfma_f32_16x16x32_bf16 v[144:147], a[228:231], v[48:51], v[144:147]\n" \
"s_add_u32 s42, s42, 0xfffffb00\n" \
"s_addc_u32 s43, s43, -1\n" \
"ds_read_b64_tr_b16 a[114:115], v210 offset:1024\n" \
"v_mfma_f32_16x16x32_bf16 v[148:151], a[232:235], v[16:19], v[148:151]\n" \
"ds_read_b64_tr_b16 a[116:117], v210 offset:12288\n" \
"global_load_lds_dwordx4 v252, s[44:45] offset:1024\n" \
"v_mfma_f32_16x16x32_bf16 v[148:151], a[236:239], v[48:51], v[148:151]\n" \
"ds_read_b128 a[84:87], v206 offset:4096\n" \
"ds_read_b64_tr_b16 a[118:119], v210 offset:13312\n" \
"v_mfma_f32_16x16x32_bf16 v[152:155], a[240:243], v[16:19], v[152:155]\n" \
"ds_read_b128 a[88:91], v205 offset:8192\n" \
"global_load_lds_dwordx4 v252, s[44:45] offset:3072\n" \
"v_mfma_f32_16x16x32_bf16 v[152:155], a[244:247], v[48:51], v[152:155]\n" \
"s_add_u32 s44, s44, 0xfffffb00\n" \
"s_addc_u32 s45, s45, -1\n" \
"ds_read_b128 a[92:95], v206 offset:8192\n" \
"v_mfma_f32_16x16x32_bf16 v[156:159], a[248:251], v[16:19], v[156:159]\n" \
"s_add_u32 m0, s85, 16384\n" \
"v_add_f32 v244, v244, v240\n" \
"v_add_f32 v245, v245, v241\n" \
"v_mfma_f32_16x16x32_bf16 v[156:159], a[252:255], v[48:51], v[156:159]\n" \
"s_waitcnt lgkmcnt(4)\n" \
"buffer_load_dwordx4 v246, s[12:15], s46 offen lds\n" \
"Ljoin_%=:\n" \
"v_mfma_f32_16x16x32_bf16 v[32:35], a[96:99], a[64:67], 0\n" \
"s_add_u32 s46, s46, 0xfffffb00\n" \
"ds_read_b128 a[192:195], v205 offset:12288\n" \
"s_add_u32 m0, s84, 24576\n" \
"v_mfma_f32_16x16x32_bf16 v[32:35], a[100:103], a[68:71], v[32:35]\n" \
"global_load_lds_dwordx4 v253, s[48:49] offset:-4096\n" \
"v_mfma_f32_16x16x32_bf16 v[40:43], a[96:99], a[72:75], 0\n" \
"ds_read_b128 a[196:199], v206 offset:12288\n" \
"v_mfma_f32_16x16x32_bf16 v[40:43], a[100:103], a[76:79], v[40:43]\n" \
"ds_read_b64_tr_b16 a[208:209], v209 offset:4096\n" \
"v_mov_b32 v232, v244\n" \
"v_mfma_f32_16x16x32_bf16 v[0:3], a[104:107], a[72:75], 0\n" \
"ds_read_b64_tr_b16 a[210:211], v209 offset:5120\n" \
"global_load_lds_dwordx4 v253, s[48:49] offset:-2048\n" \
"v_mfma_f32_16x16x32_bf16 v[0:3], a[108:111], a[76:79], v[0:3]\n" \
"s_waitcnt lgkmcnt(3)\n" \
"v_permlane16_swap_b32 v232, v244\n" \
"v_add_f32 v244, v244, v232\n" \
"v_mfma_f32_16x16x32_bf16 v[4:7], a[112:115], a[72:75], 0\n" \
"ds_read_b64_tr_b16 a[212:213], v209 offset:16384\n" \
"v_mov_b32 v232, v244\n" \
"v_mfma_f32_16x16x32_bf16 v[4:7], a[116:119], a[76:79], v[4:7]\n" \
"ds_read_b64_tr_b16 a[214:215], v209 offset:17408\n" \
"global_load_lds_dwordx4 v253, s[48:49] offset:0\n" \
"v_mfma_f32_16x16x32_bf16 v[12:15], a[112:115], a[80:83], 0\n" \
"s_add_u32 s48, s48, 0xffc40000\n" \
"s_addc_u32 s49, s49, -1\n" \
"ds_read_b64_tr_b16 a[216:217], v210 offset:4096\n" \
"v_mfma_f32_16x16x32_bf16 v[12:15], a[116:119], a[84:87], v[12:15]\n" \
"v_permlane32_swap_b32 v232, v244\n" \
"v_mfma_f32_16x16x32_bf16 v[8:11], a[104:107], a[80:83], 0\n" \
"ds_read_b64_tr_b16 a[218:219], v210 offset:5120\n" \
"global_load_lds_dwordx4 v253, s[50:51] offset:-3072\n" \
"v_mfma_f32_16x16x32_bf16 v[8:11], a[108:111], a[84:87], v[8:11]\n" \
"ds_read_b64_tr_b16 a[220:221], v210 offset:16384\n" \
"v_add_f32 v244, v244, v232\n" \
"v_mfma_f32_16x16x32_bf16 v[48:51], a[96:99], a[80:83], 0\n" \
"ds_read_b64_tr_b16 a[222:223], v210 offset:17408\n" \
"v_mfma_f32_16x16x32_bf16 v[48:51], a[100:103], a[84:87], v[48:51]\n" \
"global_load_lds_dwordx4 v253, s[50:51] offset:-1024\n" \
"v_rcp_f32 v232, v244\n" \
"v_mfma_f32_16x16x32_bf16 v[56:59], a[96:99], a[88:91], 0\n" \
"ds_read_b64_tr_b16 a[200:201], v210 offset:2048\n" \
"v_mul_f32 v244, 0x4d800000, v244\n" \
"v_mfma_f32_16x16x32_bf16 v[56:59], a[100:103], a[92:95], v[56:59]\n" \
"ds_read_b64_tr_b16 a[202:203], v210 offset:3072\n" \
"v_add_u32 v233, 0x1c90080, v249\n" \
"v_mfma_f32_16x16x32_bf16 v[16:19], a[104:107], a[88:91], 0\n" \
"ds_read_b64_tr_b16 a[204:205], v210 offset:14336\n" \
"global_load_lds_dwordx4 v253, s[50:51] offset:1024\n" \
"v_mfma_f32_16x16x32_bf16 v[16:19], a[108:111], a[92:95], v[16:19]\n" \
"s_add_u32 s50, s50, 0xffc40000\n" \
"s_addc_u32 s51, s51, -1\n" \
"v_cndmask_b32 v233, v249, v233, s[16:17]\n" \
"v_mfma_f32_16x16x32_bf16 v[20:23], a[112:115], a[88:91], 0\n" \
"ds_read_b64_tr_b16 a[206:207], v210 offset:15360\n" \
"v_mul_f32 v128, v128, v232\n" \
"v_mfma_f32_16x16x32_bf16 v[20:23], a[116:119], a[92:95], v[20:23]\n" \
"s_waitcnt lgkmcnt(3)\n" \
"v_add_u32 v207, 0xffff3000, v207\n" \
"v_add_u32 v208, 0xffff3000, v208\n" \
"v_mfma_f32_16x16x32_bf16 v[28:31], a[112:115], a[192:195], 0\n" \
"v_add_u32 v205, 0xffff3000, v205\n" \
"v_add_u32 v206, 0xffff3000, v206\n" \
"v_mfma_f32_16x16x32_bf16 v[28:31], a[116:119], a[196:199], v[28:31]\n" \
"v_add_u32 v209, 0xffff3000, v209\n" \
"v_add_u32 v210, 0xffff3000, v210\n" \
"v_mfma_f32_16x16x32_bf16 v[24:27], a[104:107], a[192:195], 0\n" \
"v_mul_f32 v129, v129, v232\n" \
"v_mul_f32 v130, v130, v232\n" \
"v_mfma_f32_16x16x32_bf16 v[24:27], a[108:111], a[196:199], v[24:27]\n" \
"v_mul_f32 v131, v131, v232\n" \
"v_mul_f32 v132, v132, v232\n" \
"v_mfma_f32_16x16x32_bf16 v[64:67], a[96:99], a[192:195], 0\n" \
"s_waitcnt vmcnt(11) lgkmcnt(0)\n" \
"v_mul_f32 v133, v133, v232\n" \
"v_mul_f32 v134, v134, v232\n" \
"v_mfma_f32_16x16x32_bf16 v[64:67], a[100:103], a[196:199], v[64:67]\n" \
"s_barrier\n" \
"v_mfma_f32_16x16x32_bf16 v[72:75], a[64:67], a[208:211], 0\n" \
"ds_read_b64_tr_b16 a[96:97], v209 offset:2048\n" \
"v_mul_f32 v135, v135, v232\n" \
"v_mfma_f32_16x16x32_bf16 v[72:75], a[68:71], a[212:215], v[72:75]\n" \
"ds_read_b64_tr_b16 a[98:99], v209 offset:3072\n" \
"v_fma_f32 v244, v128, v129, v244\n" \
"v_mfma_f32_16x16x32_bf16 v[80:83], a[72:75], a[208:211], 0\n" \
"s_add_u32 m0, s95, 118784\n" \
"ds_read_b128 a[64:67], v207 offset:0\n" \
"ds_read_b64_tr_b16 a[100:101], v209 offset:14336\n" \
"v_mfma_f32_16x16x32_bf16 v[80:83], a[76:79], a[212:215], v[80:83]\n" \
"global_load_lds_dwordx4 v252, s[42:43] offset:0\n" \
"v_cvt_pk_bf16_f32 v128, v128, v129\n" \
"v_mfma_f32_16x16x32_bf16 v[84:87], a[72:75], a[216:219], 0\n" \
"ds_read_b128 a[68:71], v208 offset:0\n" \
"ds_read_b64_tr_b16 a[102:103], v209 offset:15360\n" \
"v_mfma_f32_16x16x32_bf16 v[84:87], a[76:79], a[220:223], v[84:87]\n" \
"ds_read_b64_tr_b16 a[104:105], v209 offset:0\n" \
"v_fma_f32 v244, v130, v131, v244\n" \
"v_mfma_f32_16x16x32_bf16 v[44:47], a[200:203], a[72:75], 0\n" \
"ds_read_b64_tr_b16 a[106:107], v209 offset:1024\n" \
"v_cvt_pk_bf16_f32 v129, v130, v131\n" \
"v_mfma_f32_16x16x32_bf16 v[44:47], a[204:207], a[76:79], v[44:47]\n" \
"global_load_lds_dwordx4 v252, s[42:43] offset:2048\n" \
"v_fma_f32 v244, v132, v133, v244\n" \
"v_mfma_f32_16x16x32_bf16 v[52:55], a[200:203], a[80:83], 0\n" \
"s_add_u32 s42, s42, 0x80\n" \
"s_addc_u32 s43, s43, 0\n" \
"ds_read_b128 a[72:75], v205 offset:0\n" \
"v_mfma_f32_16x16x32_bf16 v[52:55], a[204:207], a[84:87], v[52:55]\n" \
"ds_read_b128 a[76:79], v206 offset:0\n" \
"v_cvt_pk_bf16_f32 v130, v132, v133\n" \
"v_mfma_f32_16x16x32_bf16 v[92:95], a[80:83], a[216:219], 0\n" \
"ds_read_b64_tr_b16 a[108:109], v209 offset:12288\n" \
"v_fma_f32 v244, v134, v135, v244\n" \
"v_mfma_f32_16x16x32_bf16 v[92:95], a[84:87], a[220:223], v[92:95]\n" \
"global_load_lds_dwordx4 v252, s[44:45] offset:1024\n" \
"v_cvt_pk_bf16_f32 v131, v134, v135\n" \
"v_mfma_f32_16x16x32_bf16 v[88:91], a[80:83], a[208:211], 0\n" \
"ds_read_b64_tr_b16 a[110:111], v209 offset:13312\n" \
"v_mfma_f32_16x16x32_bf16 v[88:91], a[84:87], a[212:215], v[88:91]\n" \
"buffer_store_dwordx4 v[128:131], v233, s[56:59], s78 offen offset:0 sc1\n" \
"ds_read_b64_tr_b16 a[112:113], v210 offset:0\n" \
"v_mul_f32 v136, v136, v232\n" \
"v_mul_f32 v137, v137, v232\n" \
"v_mul_f32 v138, v138, v232\n" \
"v_mfma_f32_16x16x32_bf16 v[96:99], a[88:91], a[208:211], 0\n" \
"ds_read_b128 a[80:83], v205 offset:4096\n" \
"v_mul_f32 v139, v139, v232\n" \
"v_mfma_f32_16x16x32_bf16 v[96:99], a[92:95], a[212:215], v[96:99]\n" \
"global_load_lds_dwordx4 v252, s[44:45] offset:3072\n" \
"v_mul_f32 v140, v140, v232\n" \
"v_mfma_f32_16x16x32_bf16 v[100:103], a[88:91], a[216:219], 0\n" \
"s_add_u32 s44, s44, 0x80\n" \
"s_addc_u32 s45, s45, 0\n" \
"ds_read_b128 a[84:87], v206 offset:4096\n" \
"v_mfma_f32_16x16x32_bf16 v[100:103], a[92:95], a[220:223], v[100:103]\n" \
"ds_read_b64_tr_b16 a[114:115], v210 offset:1024\n" \
"v_mul_f32 v141, v141, v232\n" \
"v_mfma_f32_16x16x32_bf16 v[60:63], a[200:203], a[88:91], 0\n" \
"s_add_u32 m0, s85, 135168\n" \
"ds_read_b64_tr_b16 a[116:117], v210 offset:12288\n" \
"v_mul_f32 v142, v142, v232\n" \
"v_mfma_f32_16x16x32_bf16 v[60:63], a[204:207], a[92:95], v[60:63]\n" \
"buffer_load_dwordx4 v246, s[12:15], s46 offen lds\n" \
"v_mfma_f32_16x16x32_bf16 v[68:71], a[200:203], a[192:195], 0\n" \
"s_add_u32 s46, s46, 0x80\n" \
"ds_read_b128 a[88:91], v205 offset:8192\n" \
"v_mul_f32 v143, v143, v232\n" \
"v_mfma_f32_16x16x32_bf16 v[68:71], a[204:207], a[196:199], v[68:71]\n" \
"ds_read_b128 a[92:95], v206 offset:8192\n" \
"v_fma_f32 v244, v136, v137, v244\n" \
"v_mfma_f32_16x16x32_bf16 v[108:111], a[192:195], a[216:219], 0\n" \
"s_add_u32 m0, s84, 143360\n" \
"ds_read_b64_tr_b16 a[118:119], v210 offset:13312\n" \
"v_cvt_pk_bf16_f32 v136, v136, v137\n" \
"v_mfma_f32_16x16x32_bf16 v[108:111], a[196:199], a[220:223], v[108:111]\n" \
"global_load_lds_dwordx4 v253, s[48:49] offset:-4096\n" \
"v_fma_f32 v244, v138, v139, v244\n" \
"v_mfma_f32_16x16x32_bf16 v[104:107], a[192:195], a[208:211], 0\n" \
"v_cvt_pk_bf16_f32 v137, v138, v139\n" \
"v_fma_f32 v244, v140, v141, v244\n" \
"v_mfma_f32_16x16x32_bf16 v[104:107], a[196:199], a[212:215], v[104:107]\n" \
"s_waitcnt lgkmcnt(2)\n" \
"v_cvt_pk_bf16_f32 v138, v140, v141\n" \
"v_fma_f32 v244, v142, v143, v244\n" \
"v_mfma_f32_16x16x32_bf16 v[32:35], a[96:99], a[64:67], v[32:35]\n" \
"ds_read_b128 a[192:195], v205 offset:12288\n" \
"v_cvt_pk_bf16_f32 v139, v142, v143\n" \
"v_mfma_f32_16x16x32_bf16 v[32:35], a[100:103], a[68:71], v[32:35]\n" \
"buffer_store_dwordx4 v[136:139], v233, s[56:59], s78 offen offset:2048 sc1\n" \
"global_load_lds_dwordx4 v253, s[48:49] offset:-2048\n" \
"s_add_u32 s78, s78, 0x1000\n" \
"v_mul_f32 v144, v144, v232\n" \
"v_mul_f32 v145, v145, v232\n" \
"v_mfma_f32_16x16x32_bf16 v[40:43], a[96:99], a[72:75], v[40:43]\n" \
"ds_read_b128 a[196:199], v206 offset:12288\n" \
"v_mul_f32 v146, v146, v232\n" \
"v_mfma_f32_16x16x32_bf16 v[40:43], a[100:103], a[76:79], v[40:43]\n" \
"ds_read_b64_tr_b16 a[208:209], v209 offset:4096\n" \
"v_mul_f32 v147, v147, v232\n" \
"v_mfma_f32_16x16x32_bf16 v[0:3], a[104:107], a[72:75], v[0:3]\n" \
"ds_read_b64_tr_b16 a[210:211], v209 offset:5120\n" \
"v_mul_f32 v148, v148, v232\n" \
"v_mfma_f32_16x16x32_bf16 v[0:3], a[108:111], a[76:79], v[0:3]\n" \
"s_waitcnt lgkmcnt(3)\n" \
"global_load_lds_dwordx4 v253, s[48:49] offset:0\n" \
"v_mul_f32 v149, v149, v232\n" \
"v_mfma_f32_16x16x32_bf16 v[4:7], a[112:115], a[72:75], v[4:7]\n" \
"s_add_u32 s48, s48, 0x60000\n" \
"s_addc_u32 s49, s49, 0\n" \
"ds_read_b64_tr_b16 a[212:213], v209 offset:16384\n" \
"v_mfma_f32_16x16x32_bf16 v[4:7], a[116:119], a[76:79], v[4:7]\n" \
"ds_read_b64_tr_b16 a[214:215], v209 offset:17408\n" \
"v_mul_f32 v150, v150, v232\n" \
"v_mfma_f32_16x16x32_bf16 v[12:15], a[112:115], a[80:83], v[12:15]\n" \
"ds_read_b64_tr_b16 a[216:217], v210 offset:4096\n" \
"v_mul_f32 v151, v151, v232\n" \
"v_mfma_f32_16x16x32_bf16 v[12:15], a[116:119], a[84:87], v[12:15]\n" \
"global_load_lds_dwordx4 v253, s[50:51] offset:-3072\n" \
"v_fma_f32 v244, v144, v145, v244\n" \
"v_mfma_f32_16x16x32_bf16 v[8:11], a[104:107], a[80:83], v[8:11]\n" \
"ds_read_b64_tr_b16 a[218:219], v210 offset:5120\n" \
"v_cvt_pk_bf16_f32 v144, v144, v145\n" \
"v_mfma_f32_16x16x32_bf16 v[8:11], a[108:111], a[84:87], v[8:11]\n" \
"ds_read_b64_tr_b16 a[220:221], v210 offset:16384\n" \
"v_fma_f32 v244, v146, v147, v244\n" \
"v_mfma_f32_16x16x32_bf16 v[48:51], a[96:99], a[80:83], v[48:51]\n" \
"ds_read_b64_tr_b16 a[222:223], v210 offset:17408\n" \
"v_cvt_pk_bf16_f32 v145, v146, v147\n" \
"v_mfma_f32_16x16x32_bf16 v[48:51], a[100:103], a[84:87], v[48:51]\n" \
"global_load_lds_dwordx4 v253, s[50:51] offset:-1024\n" \
"v_fma_f32 v244, v148, v149, v244\n" \
"v_mfma_f32_16x16x32_bf16 v[56:59], a[96:99], a[88:91], v[56:59]\n" \
"ds_read_b64_tr_b16 a[200:201], v210 offset:2048\n" \
"v_cvt_pk_bf16_f32 v146, v148, v149\n" \
"v_mfma_f32_16x16x32_bf16 v[56:59], a[100:103], a[92:95], v[56:59]\n" \
"ds_read_b64_tr_b16 a[202:203], v210 offset:3072\n" \
"v_fma_f32 v244, v150, v151, v244\n" \
"v_mfma_f32_16x16x32_bf16 v[16:19], a[104:107], a[88:91], v[16:19]\n" \
"ds_read_b64_tr_b16 a[204:205], v210 offset:14336\n" \
"v_cvt_pk_bf16_f32 v147, v150, v151\n" \
"v_mfma_f32_16x16x32_bf16 v[16:19], a[108:111], a[92:95], v[16:19]\n" \
"buffer_store_dwordx4 v[144:147], v233, s[56:59], s78 offen offset:0 sc1\n" \
"global_load_lds_dwordx4 v253, s[50:51] offset:1024\n" \
"v_mul_f32 v152, v152, v232\n" \
"v_mul_f32 v153, v153, v232\n" \
"v_mul_f32 v154, v154, v232\n" \
"v_mfma_f32_16x16x32_bf16 v[20:23], a[112:115], a[88:91], v[20:23]\n" \
"s_add_u32 s50, s50, 0x60000\n" \
"s_addc_u32 s51, s51, 0\n" \
"ds_read_b64_tr_b16 a[206:207], v210 offset:15360\n" \
"v_mfma_f32_16x16x32_bf16 v[20:23], a[116:119], a[92:95], v[20:23]\n" \
"s_waitcnt lgkmcnt(3)\n" \
"v_add_u32 v207, 0xffff0000, v207\n" \
"v_add_u32 v208, 0xffff0000, v208\n" \
"v_mfma_f32_16x16x32_bf16 v[28:31], a[112:115], a[192:195], v[28:31]\n" \
"v_add_u32 v205, 0xffff0000, v205\n" \
"v_add_u32 v206, 0xffff0000, v206\n" \
"v_mfma_f32_16x16x32_bf16 v[28:31], a[116:119], a[196:199], v[28:31]\n" \
"v_add_u32 v209, 0xffff0000, v209\n" \
"v_add_u32 v210, 0xffff0000, v210\n" \
"v_mfma_f32_16x16x32_bf16 v[24:27], a[104:107], a[192:195], v[24:27]\n" \
"v_mul_f32 v155, v155, v232\n" \
"v_mul_f32 v156, v156, v232\n" \
"v_mfma_f32_16x16x32_bf16 v[24:27], a[108:111], a[196:199], v[24:27]\n" \
"v_mul_f32 v157, v157, v232\n" \
"v_mul_f32 v158, v158, v232\n" \
"v_mfma_f32_16x16x32_bf16 v[64:67], a[96:99], a[192:195], v[64:67]\n" \
"s_waitcnt vmcnt(14) lgkmcnt(0)\n" \
"v_mul_f32 v159, v159, v232\n" \
"v_fma_f32 v244, v152, v153, v244\n" \
"v_mfma_f32_16x16x32_bf16 v[64:67], a[100:103], a[196:199], v[64:67]\n" \
"s_barrier\n" \
"v_mfma_f32_16x16x32_bf16 v[72:75], a[64:67], a[208:211], v[72:75]\n" \
"ds_read_b64_tr_b16 a[96:97], v209 offset:2048\n" \
"v_cvt_pk_bf16_f32 v152, v152, v153\n" \
"v_mfma_f32_16x16x32_bf16 v[72:75], a[68:71], a[212:215], v[72:75]\n" \
"ds_read_b64_tr_b16 a[98:99], v209 offset:3072\n" \
"v_fma_f32 v244, v154, v155, v244\n" \
"v_mfma_f32_16x16x32_bf16 v[80:83], a[72:75], a[208:211], v[80:83]\n" \
"s_add_u32 m0, s95, 65536\n" \
"ds_read_b128 a[64:67], v207 offset:0\n" \
"ds_read_b64_tr_b16 a[100:101], v209 offset:14336\n" \
"v_mfma_f32_16x16x32_bf16 v[80:83], a[76:79], a[212:215], v[80:83]\n" \
"global_load_lds_dwordx4 v252, s[42:43] offset:0\n" \
"v_cvt_pk_bf16_f32 v153, v154, v155\n" \
"v_mfma_f32_16x16x32_bf16 v[84:87], a[72:75], a[216:219], v[84:87]\n" \
"ds_read_b128 a[68:71], v208 offset:0\n" \
"ds_read_b64_tr_b16 a[102:103], v209 offset:15360\n" \
"v_mfma_f32_16x16x32_bf16 v[84:87], a[76:79], a[220:223], v[84:87]\n" \
"ds_read_b64_tr_b16 a[104:105], v209 offset:0\n" \
"v_fma_f32 v244, v156, v157, v244\n" \
"v_mfma_f32_16x16x32_bf16 v[44:47], a[200:203], a[72:75], v[44:47]\n" \
"ds_read_b64_tr_b16 a[106:107], v209 offset:1024\n" \
"v_cvt_pk_bf16_f32 v154, v156, v157\n" \
"v_mfma_f32_16x16x32_bf16 v[44:47], a[204:207], a[76:79], v[44:47]\n" \
"global_load_lds_dwordx4 v252, s[42:43] offset:2048\n" \
"v_fma_f32 v244, v158, v159, v244\n" \
"v_mfma_f32_16x16x32_bf16 v[52:55], a[200:203], a[80:83], v[52:55]\n" \
"s_add_u32 s42, s42, 0x100\n" \
"s_addc_u32 s43, s43, 0\n" \
"ds_read_b128 a[72:75], v205 offset:0\n" \
"v_mfma_f32_16x16x32_bf16 v[52:55], a[204:207], a[84:87], v[52:55]\n" \
"ds_read_b128 a[76:79], v206 offset:0\n" \
"v_cvt_pk_bf16_f32 v155, v158, v159\n" \
"v_mfma_f32_16x16x32_bf16 v[92:95], a[80:83], a[216:219], v[92:95]\n" \
"ds_read_b64_tr_b16 a[108:109], v209 offset:12288\n" \
"v_mfma_f32_16x16x32_bf16 v[92:95], a[84:87], a[220:223], v[92:95]\n" \
"buffer_store_dwordx4 v[152:155], v233, s[56:59], s78 offen offset:2048 sc1\n" \
"global_load_lds_dwordx4 v252, s[44:45] offset:1024\n" \
"v_cmp_class_f32 vcc, v244, s94\n" \
"s_sub_u32 s78, s78, 4096\n" \
"v_mfma_f32_16x16x32_bf16 v[88:91], a[80:83], a[208:211], v[88:91]\n" \
"s_or_b64 s[92:93], s[92:93], vcc\n" \
"ds_read_b64_tr_b16 a[110:111], v209 offset:13312\n" \
"v_mov_b32 v234, v245\n" \
"v_mfma_f32_16x16x32_bf16 v[88:91], a[84:87], a[212:215], v[88:91]\n" \
"ds_read_b64_tr_b16 a[112:113], v210 offset:0\n" \
"v_mfma_f32_16x16x32_bf16 v[96:99], a[88:91], a[208:211], v[96:99]\n" \
"ds_read_b128 a[80:83], v205 offset:4096\n" \
"v_mfma_f32_16x16x32_bf16 v[96:99], a[92:95], a[212:215], v[96:99]\n" \
"global_load_lds_dwordx4 v252, s[44:45] offset:3072\n" \
"v_permlane16_swap_b32 v234, v245\n" \
"v_mfma_f32_16x16x32_bf16 v[100:103], a[88:91], a[216:219], v[100:103]\n" \
"s_add_u32 s44, s44, 0x100\n" \
"s_addc_u32 s45, s45, 0\n" \
"ds_read_b128 a[84:87], v206 offset:4096\n" \
"v_mfma_f32_16x16x32_bf16 v[100:103], a[92:95], a[220:223], v[100:103]\n" \
"ds_read_b64_tr_b16 a[114:115], v210 offset:1024\n" \
"v_add_f32 v245, v245, v234\n" \
"v_mfma_f32_16x16x32_bf16 v[60:63], a[200:203], a[88:91], v[60:63]\n" \
"s_add_u32 m0, s85, 81920\n" \
"ds_read_b64_tr_b16 a[116:117], v210 offset:12288\n" \
"v_mov_b32 v234, v245\n" \
"v_mfma_f32_16x16x32_bf16 v[60:63], a[204:207], a[92:95], v[60:63]\n" \
"buffer_load_dwordx4 v246, s[12:15], s46 offen lds\n" \
"v_mfma_f32_16x16x32_bf16 v[68:71], a[200:203], a[192:195], v[68:71]\n" \
"ds_read_b128 a[88:91], v205 offset:8192\n" \
"s_add_u32 s46, s46, 0x100\n" \
"v_mfma_f32_16x16x32_bf16 v[68:71], a[204:207], a[196:199], v[68:71]\n" \
"ds_read_b128 a[92:95], v206 offset:8192\n" \
"v_permlane32_swap_b32 v234, v245\n" \
"v_mfma_f32_16x16x32_bf16 v[108:111], a[192:195], a[216:219], v[108:111]\n" \
"s_add_u32 m0, s84, 90112\n" \
"ds_read_b64_tr_b16 a[118:119], v210 offset:13312\n" \
"v_add_f32 v245, v245, v234\n" \
"v_mfma_f32_16x16x32_bf16 v[108:111], a[196:199], a[220:223], v[108:111]\n" \
"global_load_lds_dwordx4 v253, s[48:49] offset:-4096\n" \
"v_mfma_f32_16x16x32_bf16 v[104:107], a[192:195], a[208:211], v[104:107]\n" \
"v_rcp_f32 v234, v245\n" \
"v_mfma_f32_16x16x32_bf16 v[104:107], a[196:199], a[212:215], v[104:107]\n" \
"s_waitcnt lgkmcnt(2)\n" \
"v_mul_f32 v245, 0x4d800000, v245\n" \
"v_add_u32 v235, 0x1c90080, v249\n" \
"v_mfma_f32_16x16x32_bf16 v[32:35], a[96:99], a[64:67], v[32:35]\n" \
"ds_read_b128 a[192:195], v205 offset:12288\n" \
"v_cndmask_b32 v235, v249, v235, s[16:17]\n" \
"v_mfma_f32_16x16x32_bf16 v[32:35], a[100:103], a[68:71], v[32:35]\n" \
"s_add_u32 s78, s78, 532480\n" \
"global_load_lds_dwordx4 v253, s[48:49] offset:-2048\n" \
"v_mfma_f32_16x16x32_bf16 v[40:43], a[96:99], a[72:75], v[40:43]\n" \
"ds_read_b128 a[196:199], v206 offset:12288\n" \
"v_mul_f32 v160, v160, v234\n" \
"v_mfma_f32_16x16x32_bf16 v[40:43], a[100:103], a[76:79], v[40:43]\n" \
"ds_read_b64_tr_b16 a[208:209], v209 offset:4096\n" \
"v_mul_f32 v161, v161, v234\n" \
"v_mfma_f32_16x16x32_bf16 v[0:3], a[104:107], a[72:75], v[0:3]\n" \
"ds_read_b64_tr_b16 a[210:211], v209 offset:5120\n" \
"v_mul_f32 v162, v162, v234\n" \
"v_mfma_f32_16x16x32_bf16 v[0:3], a[108:111], a[76:79], v[0:3]\n" \
"s_waitcnt lgkmcnt(3)\n" \
"global_load_lds_dwordx4 v253, s[48:49] offset:0\n" \
"v_mul_f32 v163, v163, v234\n" \
"v_mfma_f32_16x16x32_bf16 v[4:7], a[112:115], a[72:75], v[4:7]\n" \
"s_add_u32 s48, s48, 0xc0000\n" \
"s_addc_u32 s49, s49, 0\n" \
"ds_read_b64_tr_b16 a[212:213], v209 offset:16384\n" \
"v_mfma_f32_16x16x32_bf16 v[4:7], a[116:119], a[76:79], v[4:7]\n" \
"ds_read_b64_tr_b16 a[214:215], v209 offset:17408\n" \
"v_mul_f32 v164, v164, v234\n" \
"v_mfma_f32_16x16x32_bf16 v[12:15], a[112:115], a[80:83], v[12:15]\n" \
"ds_read_b64_tr_b16 a[216:217], v210 offset:4096\n" \
"v_mul_f32 v165, v165, v234\n" \
"v_mfma_f32_16x16x32_bf16 v[12:15], a[116:119], a[84:87], v[12:15]\n" \
"global_load_lds_dwordx4 v253, s[50:51] offset:-3072\n" \
"v_mul_f32 v166, v166, v234\n" \
"v_mfma_f32_16x16x32_bf16 v[8:11], a[104:107], a[80:83], v[8:11]\n" \
"ds_read_b64_tr_b16 a[218:219], v210 offset:5120\n" \
"v_mul_f32 v167, v167, v234\n" \
"v_mfma_f32_16x16x32_bf16 v[8:11], a[108:111], a[84:87], v[8:11]\n" \
"ds_read_b64_tr_b16 a[220:221], v210 offset:16384\n" \
"v_fma_f32 v245, v160, v161, v245\n" \
"v_mfma_f32_16x16x32_bf16 v[48:51], a[96:99], a[80:83], v[48:51]\n" \
"ds_read_b64_tr_b16 a[222:223], v210 offset:17408\n" \
"v_cvt_pk_bf16_f32 v160, v160, v161\n" \
"v_mfma_f32_16x16x32_bf16 v[48:51], a[100:103], a[84:87], v[48:51]\n" \
"global_load_lds_dwordx4 v253, s[50:51] offset:-1024\n" \
"v_fma_f32 v245, v162, v163, v245\n" \
"v_mfma_f32_16x16x32_bf16 v[56:59], a[96:99], a[88:91], v[56:59]\n" \
"ds_read_b64_tr_b16 a[200:201], v210 offset:2048\n" \
"v_cvt_pk_bf16_f32 v161, v162, v163\n" \
"v_mfma_f32_16x16x32_bf16 v[56:59], a[100:103], a[92:95], v[56:59]\n" \
"ds_read_b64_tr_b16 a[202:203], v210 offset:3072\n" \
"v_fma_f32 v245, v164, v165, v245\n" \
"v_mfma_f32_16x16x32_bf16 v[16:19], a[104:107], a[88:91], v[16:19]\n" \
"ds_read_b64_tr_b16 a[204:205], v210 offset:14336\n" \
"v_cvt_pk_bf16_f32 v162, v164, v165\n" \
"v_mfma_f32_16x16x32_bf16 v[16:19], a[108:111], a[92:95], v[16:19]\n" \
"global_load_lds_dwordx4 v253, s[50:51] offset:1024\n" \
"v_fma_f32 v245, v166, v167, v245\n" \
"v_mfma_f32_16x16x32_bf16 v[20:23], a[112:115], a[88:91], v[20:23]\n" \
"s_add_u32 s50, s50, 0xc0000\n" \
"s_addc_u32 s51, s51, 0\n" \
"ds_read_b64_tr_b16 a[206:207], v210 offset:15360\n" \
"v_mfma_f32_16x16x32_bf16 v[20:23], a[116:119], a[92:95], v[20:23]\n" \
"s_waitcnt lgkmcnt(3)\n" \
"v_add_u32 v207, 0x1d000, v207\n" \
"v_add_u32 v208, 0x1d000, v208\n" \
"v_mfma_f32_16x16x32_bf16 v[28:31], a[112:115], a[192:195], v[28:31]\n" \
"v_add_u32 v205, 0x1d000, v205\n" \
"v_add_u32 v206, 0x1d000, v206\n" \
"v_mfma_f32_16x16x32_bf16 v[28:31], a[116:119], a[196:199], v[28:31]\n" \
"v_add_u32 v209, 0x1d000, v209\n" \
"v_add_u32 v210, 0x1d000, v210\n" \
"v_mfma_f32_16x16x32_bf16 v[24:27], a[104:107], a[192:195], v[24:27]\n" \
"v_cvt_pk_bf16_f32 v163, v166, v167\n" \
"v_mfma_f32_16x16x32_bf16 v[24:27], a[108:111], a[196:199], v[24:27]\n" \
"buffer_store_dwordx4 v[160:163], v235, s[56:59], s78 offen offset:0 sc1\n" \
"v_mul_f32 v168, v168, v234\n" \
"v_mul_f32 v169, v169, v234\n" \
"v_mul_f32 v170, v170, v234\n" \
"v_mul_f32 v171, v171, v234\n" \
"v_mfma_f32_16x16x32_bf16 v[64:67], a[96:99], a[192:195], v[64:67]\n" \
"s_waitcnt vmcnt(13) lgkmcnt(0)\n" \
"v_mul_f32 v172, v172, v234\n" \
"v_mul_f32 v173, v173, v234\n" \
"v_mfma_f32_16x16x32_bf16 v[64:67], a[100:103], a[196:199], v[64:67]\n" \
"s_barrier\n" \
"v_mfma_f32_16x16x32_bf16 v[72:75], a[64:67], a[208:211], v[72:75]\n" \
"ds_read_b64_tr_b16 a[96:97], v209 offset:2048\n" \
"v_mul_f32 v174, v174, v234\n" \
"v_mfma_f32_16x16x32_bf16 v[72:75], a[68:71], a[212:215], v[72:75]\n" \
"ds_read_b64_tr_b16 a[98:99], v209 offset:3072\n" \
"v_mul_f32 v175, v175, v234\n" \
"v_mfma_f32_16x16x32_bf16 v[80:83], a[72:75], a[208:211], v[80:83]\n" \
"s_add_u32 m0, s95, 0\n" \
"ds_read_b128 a[64:67], v207 offset:0\n" \
"ds_read_b64_tr_b16 a[100:101], v209 offset:14336\n" \
"v_mfma_f32_16x16x32_bf16 v[80:83], a[76:79], a[212:215], v[80:83]\n" \
"global_load_lds_dwordx4 v252, s[42:43] offset:0\n" \
"v_fma_f32 v245, v168, v169, v245\n" \
"v_mfma_f32_16x16x32_bf16 v[84:87], a[72:75], a[216:219], v[84:87]\n" \
"ds_read_b128 a[68:71], v208 offset:0\n" \
"ds_read_b64_tr_b16 a[102:103], v209 offset:15360\n" \
"v_mfma_f32_16x16x32_bf16 v[84:87], a[76:79], a[220:223], v[84:87]\n" \
"ds_read_b64_tr_b16 a[104:105], v209 offset:0\n" \
"v_cvt_pk_bf16_f32 v168, v168, v169\n" \
"v_mfma_f32_16x16x32_bf16 v[44:47], a[200:203], a[72:75], v[44:47]\n" \
"ds_read_b64_tr_b16 a[106:107], v209 offset:1024\n" \
"v_fma_f32 v245, v170, v171, v245\n" \
"v_mfma_f32_16x16x32_bf16 v[44:47], a[204:207], a[76:79], v[44:47]\n" \
"global_load_lds_dwordx4 v252, s[42:43] offset:2048\n" \
"v_cvt_pk_bf16_f32 v169, v170, v171\n" \
"v_mfma_f32_16x16x32_bf16 v[52:55], a[200:203], a[80:83], v[52:55]\n" \
"s_add_u32 s42, s42, 0x80\n" \
"s_addc_u32 s43, s43, 0\n" \
"ds_read_b128 a[72:75], v205 offset:0\n" \
"v_mfma_f32_16x16x32_bf16 v[52:55], a[204:207], a[84:87], v[52:55]\n" \
"ds_read_b128 a[76:79], v206 offset:0\n" \
"v_fma_f32 v245, v172, v173, v245\n" \
"v_mfma_f32_16x16x32_bf16 v[92:95], a[80:83], a[216:219], v[92:95]\n" \
"ds_read_b64_tr_b16 a[108:109], v209 offset:12288\n" \
"v_cvt_pk_bf16_f32 v170, v172, v173\n" \
"v_mfma_f32_16x16x32_bf16 v[92:95], a[84:87], a[220:223], v[92:95]\n" \
"global_load_lds_dwordx4 v252, s[44:45] offset:1024\n" \
"v_fma_f32 v245, v174, v175, v245\n" \
"v_mfma_f32_16x16x32_bf16 v[88:91], a[80:83], a[208:211], v[88:91]\n" \
"ds_read_b64_tr_b16 a[110:111], v209 offset:13312\n" \
"v_cvt_pk_bf16_f32 v171, v174, v175\n" \
"v_mfma_f32_16x16x32_bf16 v[88:91], a[84:87], a[212:215], v[88:91]\n" \
"buffer_store_dwordx4 v[168:171], v235, s[56:59], s78 offen offset:2048 sc1\n" \
"ds_read_b64_tr_b16 a[112:113], v210 offset:0\n" \
"s_add_u32 s78, s78, 0x1000\n" \
"v_mul_f32 v176, v176, v234\n" \
"v_mul_f32 v177, v177, v234\n" \
"v_mfma_f32_16x16x32_bf16 v[96:99], a[88:91], a[208:211], v[96:99]\n" \
"ds_read_b128 a[80:83], v205 offset:4096\n" \
"v_mul_f32 v178, v178, v234\n" \
"v_mfma_f32_16x16x32_bf16 v[96:99], a[92:95], a[212:215], v[96:99]\n" \
"global_load_lds_dwordx4 v252, s[44:45] offset:3072\n" \
"v_mul_f32 v179, v179, v234\n" \
"v_mfma_f32_16x16x32_bf16 v[100:103], a[88:91], a[216:219], v[100:103]\n" \
"s_add_u32 s44, s44, 0x80\n" \
"s_addc_u32 s45, s45, 0\n" \
"ds_read_b128 a[84:87], v206 offset:4096\n" \
"v_mfma_f32_16x16x32_bf16 v[100:103], a[92:95], a[220:223], v[100:103]\n" \
"ds_read_b64_tr_b16 a[114:115], v210 offset:1024\n" \
"v_mul_f32 v180, v180, v234\n" \
"v_mfma_f32_16x16x32_bf16 v[60:63], a[200:203], a[88:91], v[60:63]\n" \
"s_add_u32 m0, s85, 16384\n" \
"ds_read_b64_tr_b16 a[116:117], v210 offset:12288\n" \
"v_mul_f32 v181, v181, v234\n" \
"v_mfma_f32_16x16x32_bf16 v[60:63], a[204:207], a[92:95], v[60:63]\n" \
"buffer_load_dwordx4 v246, s[12:15], s46 offen lds\n" \
"v_mfma_f32_16x16x32_bf16 v[68:71], a[200:203], a[192:195], v[68:71]\n" \
"s_add_u32 s46, s46, 0x80\n" \
"ds_read_b128 a[88:91], v205 offset:8192\n" \
"v_mul_f32 v182, v182, v234\n" \
"v_mfma_f32_16x16x32_bf16 v[68:71], a[204:207], a[196:199], v[68:71]\n" \
"ds_read_b128 a[92:95], v206 offset:8192\n" \
"v_mul_f32 v183, v183, v234\n" \
"v_mfma_f32_16x16x32_bf16 v[108:111], a[192:195], a[216:219], v[108:111]\n" \
"s_add_u32 m0, s84, 24576\n" \
"ds_read_b64_tr_b16 a[118:119], v210 offset:13312\n" \
"v_fma_f32 v245, v176, v177, v245\n" \
"v_mfma_f32_16x16x32_bf16 v[108:111], a[196:199], a[220:223], v[108:111]\n" \
"global_load_lds_dwordx4 v253, s[48:49] offset:-4096\n" \
"v_cvt_pk_bf16_f32 v176, v176, v177\n" \
"v_mfma_f32_16x16x32_bf16 v[104:107], a[192:195], a[208:211], v[104:107]\n" \
"v_fma_f32 v245, v178, v179, v245\n" \
"v_cvt_pk_bf16_f32 v177, v178, v179\n" \
"Lpg9189_%=:\n" \
"Lst1x3_%=:\n" \
"s_cbranch_execz Lpg11454_%=\n" \
"v_mfma_f32_16x16x32_bf16 v[104:107], a[196:199], a[212:215], v[104:107]\n" \
"s_waitcnt lgkmcnt(2)\n" \
"v_fma_f32 v245, v180, v181, v245\n" \
"v_cvt_pk_bf16_f32 v178, v180, v181\n" \
"v_mfma_f32_16x16x32_bf16 v[32:35], a[96:99], a[64:67], v[32:35]\n" \
"ds_read_b128 a[192:195], v205 offset:12288\n" \
"v_fma_f32 v245, v182, v183, v245\n" \
"v_mfma_f32_16x16x32_bf16 v[32:35], a[100:103], a[68:71], v[32:35]\n" \
"global_load_lds_dwordx4 v253, s[48:49] offset:-2048\n" \
"v_cvt_pk_bf16_f32 v179, v182, v183\n" \
"v_mfma_f32_16x16x32_bf16 v[40:43], a[96:99], a[72:75], v[40:43]\n" \
"ds_read_b128 a[196:199], v206 offset:12288\n" \
"v_mfma_f32_16x16x32_bf16 v[40:43], a[100:103], a[76:79], v[40:43]\n" \
"buffer_store_dwordx4 v[176:179], v235, s[56:59], s78 offen offset:0 sc1\n" \
"ds_read_b64_tr_b16 a[208:209], v209 offset:4096\n" \
"v_mul_f32 v184, v184, v234\n" \
"v_mul_f32 v185, v185, v234\n" \
"v_mul_f32 v186, v186, v234\n" \
"v_mfma_f32_16x16x32_bf16 v[0:3], a[104:107], a[72:75], v[0:3]\n" \
"ds_read_b64_tr_b16 a[210:211], v209 offset:5120\n" \
"v_mul_f32 v187, v187, v234\n" \
"v_mfma_f32_16x16x32_bf16 v[0:3], a[108:111], a[76:79], v[0:3]\n" \
"s_waitcnt lgkmcnt(3)\n" \
"global_load_lds_dwordx4 v253, s[48:49] offset:0\n" \
"v_mul_f32 v188, v188, v234\n" \
"v_mfma_f32_16x16x32_bf16 v[4:7], a[112:115], a[72:75], v[4:7]\n" \
"s_add_u32 s48, s48, 0x60000\n" \
"s_addc_u32 s49, s49, 0\n" \
"ds_read_b64_tr_b16 a[212:213], v209 offset:16384\n" \
"v_mfma_f32_16x16x32_bf16 v[4:7], a[116:119], a[76:79], v[4:7]\n" \
"ds_read_b64_tr_b16 a[214:215], v209 offset:17408\n" \
"v_mul_f32 v189, v189, v234\n" \
"v_mfma_f32_16x16x32_bf16 v[12:15], a[112:115], a[80:83], v[12:15]\n" \
"ds_read_b64_tr_b16 a[216:217], v210 offset:4096\n" \
"v_mul_f32 v190, v190, v234\n" \
"v_mfma_f32_16x16x32_bf16 v[12:15], a[116:119], a[84:87], v[12:15]\n" \
"global_load_lds_dwordx4 v253, s[50:51] offset:-3072\n" \
"v_mul_f32 v191, v191, v234\n" \
"v_mfma_f32_16x16x32_bf16 v[8:11], a[104:107], a[80:83], v[8:11]\n" \
"ds_read_b64_tr_b16 a[218:219], v210 offset:5120\n" \
"v_fma_f32 v245, v184, v185, v245\n" \
"v_mfma_f32_16x16x32_bf16 v[8:11], a[108:111], a[84:87], v[8:11]\n" \
"ds_read_b64_tr_b16 a[220:221], v210 offset:16384\n" \
"v_cvt_pk_bf16_f32 v184, v184, v185\n" \
"v_mfma_f32_16x16x32_bf16 v[48:51], a[96:99], a[80:83], v[48:51]\n" \
"ds_read_b64_tr_b16 a[222:223], v210 offset:17408\n" \
"v_fma_f32 v245, v186, v187, v245\n" \
"v_mfma_f32_16x16x32_bf16 v[48:51], a[100:103], a[84:87], v[48:51]\n" \
"global_load_lds_dwordx4 v253, s[50:51] offset:-1024\n" \
"v_cvt_pk_bf16_f32 v185, v186, v187\n" \
"v_mfma_f32_16x16x32_bf16 v[56:59], a[96:99], a[88:91], v[56:59]\n" \
"ds_read_b64_tr_b16 a[200:201], v210 offset:2048\n" \
"v_fma_f32 v245, v188, v189, v245\n" \
"v_mfma_f32_16x16x32_bf16 v[56:59], a[100:103], a[92:95], v[56:59]\n" \
"ds_read_b64_tr_b16 a[202:203], v210 offset:3072\n" \
"v_cvt_pk_bf16_f32 v186, v188, v189\n" \
"v_mfma_f32_16x16x32_bf16 v[16:19], a[104:107], a[88:91], v[16:19]\n" \
"ds_read_b64_tr_b16 a[204:205], v210 offset:14336\n" \
"v_fma_f32 v245, v190, v191, v245\n" \
"v_mfma_f32_16x16x32_bf16 v[16:19], a[108:111], a[92:95], v[16:19]\n" \
"global_load_lds_dwordx4 v253, s[50:51] offset:1024\n" \
"v_cvt_pk_bf16_f32 v187, v190, v191\n" \
"v_mfma_f32_16x16x32_bf16 v[20:23], a[112:115], a[88:91], v[20:23]\n" \
"s_add_u32 s50, s50, 0x60000\n" \
"s_addc_u32 s51, s51, 0\n" \
"ds_read_b64_tr_b16 a[206:207], v210 offset:15360\n" \
"v_mfma_f32_16x16x32_bf16 v[20:23], a[116:119], a[92:95], v[20:23]\n" \
"s_waitcnt lgkmcnt(3)\n" \
"buffer_store_dwordx4 v[184:187], v235, s[56:59], s78 offen offset:2048 sc1\n" \
"v_add_u32 v207, 0xffff3000, v207\n" \
"v_add_u32 v208, 0xffff3000, v208\n" \
"v_cmp_class_f32 vcc, v245, s94\n" \
"s_sub_u32 s78, s78, 536576\n" \
"v_mfma_f32_16x16x32_bf16 v[28:31], a[112:115], a[192:195], v[28:31]\n" \
"v_add_u32 v205, 0xffff3000, v205\n" \
"v_add_u32 v206, 0xffff3000, v206\n" \
"v_mfma_f32_16x16x32_bf16 v[28:31], a[116:119], a[196:199], v[28:31]\n" \
"v_add_u32 v209, 0xffff3000, v209\n" \
"v_add_u32 v210, 0xffff3000, v210\n" \
"v_mfma_f32_16x16x32_bf16 v[24:27], a[104:107], a[192:195], v[24:27]\n" \
"v_mfma_f32_16x16x32_bf16 v[24:27], a[108:111], a[196:199], v[24:27]\n" \
"s_or_b64 s[92:93], s[92:93], vcc\n" \
"s_cbranch_scc1 Lexact_%=\n" \
"Lexr_%=:\n" \
"v_mfma_f32_16x16x32_bf16 v[64:67], a[96:99], a[192:195], v[64:67]\n" \
"s_waitcnt vmcnt(15) lgkmcnt(0)\n" \
"v_mfma_f32_16x16x32_bf16 v[64:67], a[100:103], a[196:199], v[64:67]\n" \
"s_barrier\n" \
"v_mfma_f32_16x16x32_bf16 v[72:75], a[64:67], a[208:211], v[72:75]\n" \
"ds_read_b64_tr_b16 a[96:97], v209 offset:2048\n" \
"v_mfma_f32_16x16x32_bf16 v[72:75], a[68:71], a[212:215], v[72:75]\n" \
"ds_read_b64_tr_b16 a[98:99], v209 offset:3072\n" \
"v_mfma_f32_16x16x32_bf16 v[80:83], a[72:75], a[208:211], v[80:83]\n" \
"s_add_u32 m0, s95, 118784\n" \
"ds_read_b128 a[64:67], v207 offset:0\n" \
"ds_read_b64_tr_b16 a[100:101], v209 offset:14336\n" \
"v_mfma_f32_16x16x32_bf16 v[80:83], a[76:79], a[212:215], v[80:83]\n" \
"global_load_lds_dwordx4 v252, s[42:43] offset:0\n" \
"v_mfma_f32_16x16x32_bf16 v[84:87], a[72:75], a[216:219], v[84:87]\n" \
"ds_read_b128 a[68:71], v208 offset:0\n" \
"ds_read_b64_tr_b16 a[102:103], v209 offset:15360\n" \
"v_mfma_f32_16x16x32_bf16 v[84:87], a[76:79], a[220:223], v[84:87]\n" \
"ds_read_b64_tr_b16 a[104:105], v209 offset:0\n" \
"v_mfma_f32_16x16x32_bf16 v[44:47], a[200:203], a[72:75], v[44:47]\n" \
"ds_read_b64_tr_b16 a[106:107], v209 offset:1024\n" \
"v_mfma_f32_16x16x32_bf16 v[44:47], a[204:207], a[76:79], v[44:47]\n" \
"global_load_lds_dwordx4 v252, s[42:43] offset:2048\n" \
"v_mfma_f32_16x16x32_bf16 v[52:55], a[200:203], a[80:83], v[52:55]\n" \
"s_add_u32 s42, s42, 0x80\n" \
"s_addc_u32 s43, s43, 0\n" \
"ds_read_b128 a[72:75], v205 offset:0\n" \
"v_mfma_f32_16x16x32_bf16 v[52:55], a[204:207], a[84:87], v[52:55]\n" \
"ds_read_b128 a[76:79], v206 offset:0\n" \
"v_mfma_f32_16x16x32_bf16 v[92:95], a[80:83], a[216:219], v[92:95]\n" \
"ds_read_b64_tr_b16 a[108:109], v209 offset:12288\n" \
"v_mfma_f32_16x16x32_bf16 v[92:95], a[84:87], a[220:223], v[92:95]\n" \
"global_load_lds_dwordx4 v252, s[44:45] offset:1024\n" \
"v_mfma_f32_16x16x32_bf16 v[88:91], a[80:83], a[208:211], v[88:91]\n" \
"ds_read_b64_tr_b16 a[110:111], v209 offset:13312\n" \
"v_mfma_f32_16x16x32_bf16 v[88:91], a[84:87], a[212:215], v[88:91]\n" \
"ds_read_b64_tr_b16 a[112:113], v210 offset:0\n" \
"v_mfma_f32_16x16x32_bf16 v[96:99], a[88:91], a[208:211], v[96:99]\n" \
"ds_read_b128 a[80:83], v205 offset:4096\n" \
"v_mfma_f32_16x16x32_bf16 v[96:99], a[92:95], a[212:215], v[96:99]\n" \
"global_load_lds_dwordx4 v252, s[44:45] offset:3072\n" \
"v_mfma_f32_16x16x32_bf16 v[100:103], a[88:91], a[216:219], v[100:103]\n" \
"s_add_u32 s44, s44, 0x80\n" \
"s_addc_u32 s45, s45, 0\n" \
"ds_read_b128 a[84:87], v206 offset:4096\n" \
"v_mfma_f32_16x16x32_bf16 v[100:103], a[92:95], a[220:223], v[100:103]\n" \
"ds_read_b64_tr_b16 a[114:115], v210 offset:1024\n" \
"v_mfma_f32_16x16x32_bf16 v[60:63], a[200:203], a[88:91], v[60:63]\n" \
"s_add_u32 m0, s85, 135168\n" \
"ds_read_b64_tr_b16 a[116:117], v210 offset:12288\n" \
"v_mfma_f32_16x16x32_bf16 v[60:63], a[204:207], a[92:95], v[60:63]\n" \
"buffer_load_dwordx4 v246, s[12:15], s46 offen lds\n" \
"v_mfma_f32_16x16x32_bf16 v[68:71], a[200:203], a[192:195], v[68:71]\n" \
"ds_read_b128 a[88:91], v205 offset:8192\n" \
"s_add_u32 s46, s46, 0x80\n" \
"v_mfma_f32_16x16x32_bf16 v[68:71], a[204:207], a[196:199], v[68:71]\n" \
"ds_read_b128 a[92:95], v206 offset:8192\n" \
"v_mfma_f32_16x16x32_bf16 v[108:111], a[192:195], a[216:219], v[108:111]\n" \
"s_add_u32 m0, s84, 143360\n" \
"ds_read_b64_tr_b16 a[118:119], v210 offset:13312\n" \
"v_mfma_f32_16x16x32_bf16 v[108:111], a[196:199], a[220:223], v[108:111]\n" \
"global_load_lds_dwordx4 v253, s[48:49] offset:-4096\n" \
"v_mfma_f32_16x16x32_bf16 v[104:107], a[192:195], a[208:211], v[104:107]\n" \
"v_mfma_f32_16x16x32_bf16 v[104:107], a[196:199], a[212:215], v[104:107]\n" \
"s_waitcnt lgkmcnt(2)\n" \
"v_mfma_f32_16x16x32_bf16 v[32:35], a[96:99], a[64:67], v[32:35]\n" \
"ds_read_b128 a[192:195], v205 offset:12288\n" \
"v_mfma_f32_16x16x32_bf16 v[32:35], a[100:103], a[68:71], v[32:35]\n" \
"global_load_lds_dwordx4 v253, s[48:49] offset:-2048\n" \
"v_mfma_f32_16x16x32_bf16 v[40:43], a[96:99], a[72:75], v[40:43]\n" \
"ds_read_b128 a[196:199], v206 offset:12288\n" \
"v_mfma_f32_16x16x32_bf16 v[40:43], a[100:103], a[76:79], v[40:43]\n" \
"ds_read_b64_tr_b16 a[208:209], v209 offset:4096\n" \
"v_mfma_f32_16x16x32_bf16 v[0:3], a[104:107], a[72:75], v[0:3]\n" \
"ds_read_b64_tr_b16 a[210:211], v209 offset:5120\n" \
"v_mfma_f32_16x16x32_bf16 v[0:3], a[108:111], a[76:79], v[0:3]\n" \
"s_waitcnt lgkmcnt(3)\n" \
"global_load_lds_dwordx4 v253, s[48:49] offset:0\n" \
"v_mfma_f32_16x16x32_bf16 v[4:7], a[112:115], a[72:75], v[4:7]\n" \
"s_add_u32 s48, s48, 0x60000\n" \
"s_addc_u32 s49, s49, 0\n" \
"ds_read_b64_tr_b16 a[212:213], v209 offset:16384\n" \
"v_mfma_f32_16x16x32_bf16 v[4:7], a[116:119], a[76:79], v[4:7]\n" \
"ds_read_b64_tr_b16 a[214:215], v209 offset:17408\n" \
"v_mfma_f32_16x16x32_bf16 v[12:15], a[112:115], a[80:83], v[12:15]\n" \
"ds_read_b64_tr_b16 a[216:217], v210 offset:4096\n" \
"v_mfma_f32_16x16x32_bf16 v[12:15], a[116:119], a[84:87], v[12:15]\n" \
"global_load_lds_dwordx4 v253, s[50:51] offset:-3072\n" \
"v_mfma_f32_16x16x32_bf16 v[8:11], a[104:107], a[80:83], v[8:11]\n" \
"ds_read_b64_tr_b16 a[218:219], v210 offset:5120\n" \
"v_mfma_f32_16x16x32_bf16 v[8:11], a[108:111], a[84:87], v[8:11]\n" \
"ds_read_b64_tr_b16 a[220:221], v210 offset:16384\n" \
"v_mfma_f32_16x16x32_bf16 v[48:51], a[96:99], a[80:83], v[48:51]\n" \
"ds_read_b64_tr_b16 a[222:223], v210 offset:17408\n" \
"v_mfma_f32_16x16x32_bf16 v[48:51], a[100:103], a[84:87], v[48:51]\n" \
"global_load_lds_dwordx4 v253, s[50:51] offset:-1024\n" \
"v_mfma_f32_16x16x32_bf16 v[56:59], a[96:99], a[88:91], v[56:59]\n" \
"ds_read_b64_tr_b16 a[200:201], v210 offset:2048\n" \
"v_mfma_f32_16x16x32_bf16 v[56:59], a[100:103], a[92:95], v[56:59]\n" \
"ds_read_b64_tr_b16 a[202:203], v210 offset:3072\n" \
"v_mfma_f32_16x16x32_bf16 v[16:19], a[104:107], a[88:91], v[16:19]\n" \
"ds_read_b64_tr_b16 a[204:205], v210 offset:14336\n" \
"v_mfma_f32_16x16x32_bf16 v[16:19], a[108:111], a[92:95], v[16:19]\n" \
"global_load_lds_dwordx4 v253, s[50:51] offset:1024\n" \
"v_mfma_f32_16x16x32_bf16 v[20:23], a[112:115], a[88:91], v[20:23]\n" \
"s_add_u32 s50, s50, 0x60000\n" \
"s_addc_u32 s51, s51, 0\n" \
"ds_read_b64_tr_b16 a[206:207], v210 offset:15360\n" \
"v_mfma_f32_16x16x32_bf16 v[20:23], a[116:119], a[92:95], v[20:23]\n" \
"s_waitcnt lgkmcnt(3)\n" \
"v_add_u32 v207, 0xffff0000, v207\n" \
"v_add_u32 v208, 0xffff0000, v208\n" \
"v_mfma_f32_16x16x32_bf16 v[28:31], a[112:115], a[192:195], v[28:31]\n" \
"v_add_u32 v205, 0xffff0000, v205\n" \
"v_add_u32 v206, 0xffff0000, v206\n" \
"v_mfma_f32_16x16x32_bf16 v[28:31], a[116:119], a[196:199], v[28:31]\n" \
"v_add_u32 v209, 0xffff0000, v209\n" \
"v_add_u32 v210, 0xffff0000, v210\n" \
"v_mfma_f32_16x16x32_bf16 v[24:27], a[104:107], a[192:195], v[24:27]\n" \
"v_mfma_f32_16x16x32_bf16 v[24:27], a[108:111], a[196:199], v[24:27]\n" \
"s_lshl_b32 s70, s63, 7\n" \
"s_lshl_b32 s71, s62, 2\n" \
"s_add_u32 s71, s71, 65589248\n" \
"v_mfma_f32_16x16x32_bf16 v[64:67], a[96:99], a[192:195], v[64:67]\n" \
"s_waitcnt vmcnt(11) lgkmcnt(0)\n" \
"s_add_u32 s72, s71, s70\n" \
"s_sub_u32 s71, s71, s70\n" \
"v_mfma_f32_16x16x32_bf16 v[64:67], a[100:103], a[196:199], v[64:67]\n" \
"s_barrier\n" \
"v_mfma_f32_16x16x32_bf16 v[72:75], a[64:67], a[208:211], v[72:75]\n" \
"s_add_u32 s71, s71, 1920\n" \
"ds_read_b64_tr_b16 a[96:97], v209 offset:2048\n" \
"v_mov_b32 v236, s60\n" \
"v_mfma_f32_16x16x32_bf16 v[72:75], a[68:71], a[212:215], v[72:75]\n" \
"ds_read_b64_tr_b16 a[98:99], v209 offset:3072\n" \
"v_lshlrev_b32 v237, 28, s61\n" \
"v_mfma_f32_16x16x32_bf16 v[80:83], a[72:75], a[208:211], v[80:83]\n" \
"s_add_u32 m0, s95, 65536\n" \
"ds_read_b128 a[64:67], v207 offset:0\n" \
"ds_read_b64_tr_b16 a[100:101], v209 offset:14336\n" \
"v_mfma_f32_16x16x32_bf16 v[80:83], a[76:79], a[212:215], v[80:83]\n" \
"buffer_store_dword v236, v237, s[56:59], s72 offen sc1\n" \
"global_load_lds_dwordx4 v252, s[42:43] offset:0\n" \
"v_mfma_f32_16x16x32_bf16 v[84:87], a[72:75], a[216:219], v[84:87]\n" \
"ds_read_b128 a[68:71], v208 offset:0\n" \
"ds_read_b64_tr_b16 a[102:103], v209 offset:15360\n" \
"v_mfma_f32_16x16x32_bf16 v[84:87], a[76:79], a[220:223], v[84:87]\n" \
"buffer_store_dword v236, v237, s[56:59], s71 offen sc1\n" \
"ds_read_b64_tr_b16 a[104:105], v209 offset:0\n" \
"v_mfma_f32_16x16x32_bf16 v[44:47], a[200:203], a[72:75], v[44:47]\n" \
"ds_read_b64_tr_b16 a[106:107], v209 offset:1024\n" \
"v_mfma_f32_16x16x32_bf16 v[44:47], a[204:207], a[76:79], v[44:47]\n" \
"global_load_lds_dwordx4 v252, s[42:43] offset:2048\n" \
"v_mfma_f32_16x16x32_bf16 v[52:55], a[200:203], a[80:83], v[52:55]\n" \
"s_add_u32 s42, s42, 0x80\n" \
"s_addc_u32 s43, s43, 0\n" \
"ds_read_b128 a[72:75], v205 offset:0\n" \
"v_mfma_f32_16x16x32_bf16 v[52:55], a[204:207], a[84:87], v[52:55]\n" \
"ds_read_b128 a[76:79], v206 offset:0\n" \
"v_mfma_f32_16x16x32_bf16 v[92:95], a[80:83], a[216:219], v[92:95]\n" \
"ds_read_b64_tr_b16 a[108:109], v209 offset:12288\n" \
"v_mfma_f32_16x16x32_bf16 v[92:95], a[84:87], a[220:223], v[92:95]\n" \
"global_load_lds_dwordx4 v252, s[44:45] offset:1024\n" \
"v_mfma_f32_16x16x32_bf16 v[88:91], a[80:83], a[208:211], v[88:91]\n" \
"ds_read_b64_tr_b16 a[110:111], v209 offset:13312\n" \
"v_mfma_f32_16x16x32_bf16 v[88:91], a[84:87], a[212:215], v[88:91]\n" \
"ds_read_b64_tr_b16 a[112:113], v210 offset:0\n" \
"v_mfma_f32_16x16x32_bf16 v[96:99], a[88:91], a[208:211], v[96:99]\n" \
"ds_read_b128 a[80:83], v205 offset:4096\n" \
"v_mfma_f32_16x16x32_bf16 v[96:99], a[92:95], a[212:215], v[96:99]\n" \
"global_load_lds_dwordx4 v252, s[44:45] offset:3072\n" \
"v_mfma_f32_16x16x32_bf16 v[100:103], a[88:91], a[216:219], v[100:103]\n" \
"s_add_u32 s44, s44, 0x80\n" \
"s_addc_u32 s45, s45, 0\n" \
"ds_read_b128 a[84:87], v206 offset:4096\n" \
"v_mfma_f32_16x16x32_bf16 v[100:103], a[92:95], a[220:223], v[100:103]\n" \
"ds_read_b64_tr_b16 a[114:115], v210 offset:1024\n" \
"v_mfma_f32_16x16x32_bf16 v[60:63], a[200:203], a[88:91], v[60:63]\n" \
"s_add_u32 m0, s85, 81920\n" \
"ds_read_b64_tr_b16 a[116:117], v210 offset:12288\n" \
"v_mfma_f32_16x16x32_bf16 v[60:63], a[204:207], a[92:95], v[60:63]\n" \
"buffer_load_dwordx4 v246, s[12:15], s46 offen lds\n" \
"v_mfma_f32_16x16x32_bf16 v[68:71], a[200:203], a[192:195], v[68:71]\n" \
"ds_read_b128 a[88:91], v205 offset:8192\n" \
"s_add_u32 s46, s46, 0x80\n" \
"v_mfma_f32_16x16x32_bf16 v[68:71], a[204:207], a[196:199], v[68:71]\n" \
"ds_read_b128 a[92:95], v206 offset:8192\n" \
"v_mfma_f32_16x16x32_bf16 v[108:111], a[192:195], a[216:219], v[108:111]\n" \
"s_add_u32 m0, s84, 90112\n" \
"ds_read_b64_tr_b16 a[118:119], v210 offset:13312\n" \
"v_mfma_f32_16x16x32_bf16 v[108:111], a[196:199], a[220:223], v[108:111]\n" \
"global_load_lds_dwordx4 v253, s[48:49] offset:-4096\n" \
"v_mfma_f32_16x16x32_bf16 v[104:107], a[192:195], a[208:211], v[104:107]\n" \
"v_mfma_f32_16x16x32_bf16 v[104:107], a[196:199], a[212:215], v[104:107]\n" \
"s_waitcnt lgkmcnt(2)\n" \
"v_mfma_f32_16x16x32_bf16 v[32:35], a[96:99], a[64:67], v[32:35]\n" \
"s_lshr_b32 s89, s22, 7\n" \
"ds_read_b128 a[192:195], v205 offset:12288\n" \
"s_sub_u32 s89, 3, s89\n" \
"v_mfma_f32_16x16x32_bf16 v[32:35], a[100:103], a[68:71], v[32:35]\n" \
"global_load_lds_dwordx4 v253, s[48:49] offset:-2048\n" \
"v_bfe_u32 v203, v248, 7, 2\n" \
"v_mfma_f32_16x16x32_bf16 v[40:43], a[96:99], a[72:75], v[40:43]\n" \
"s_and_b32 s89, s89, 7\n" \
"ds_read_b128 a[196:199], v206 offset:12288\n" \
"s_and_b32 s90, %1, 1\n" \
"v_mfma_f32_16x16x32_bf16 v[40:43], a[100:103], a[76:79], v[40:43]\n" \
"ds_read_b64_tr_b16 a[208:209], v209 offset:4096\n" \
"v_bfe_u32 v204, v248, 5, 2\n" \
"v_mfma_f32_16x16x32_bf16 v[0:3], a[104:107], a[72:75], v[0:3]\n" \
"s_lshl_b32 s90, s90, 3\n" \
"s_add_u32 s89, s89, s90\n" \
"ds_read_b64_tr_b16 a[210:211], v209 offset:5120\n" \
"v_mfma_f32_16x16x32_bf16 v[0:3], a[108:111], a[76:79], v[0:3]\n" \
"s_waitcnt lgkmcnt(3)\n" \
"global_load_lds_dwordx4 v253, s[48:49] offset:0\n" \
"v_min_u32 v204, 2, v204\n" \
"v_mfma_f32_16x16x32_bf16 v[4:7], a[112:115], a[72:75], v[4:7]\n" \
"s_add_u32 s48, s48, 0x60000\n" \
"s_addc_u32 s49, s49, 0\n" \
"ds_read_b64_tr_b16 a[212:213], v209 offset:16384\n" \
"v_mfma_f32_16x16x32_bf16 v[4:7], a[116:119], a[76:79], v[4:7]\n" \
"s_lshl_b32 s89, s89, 7\n" \
"s_lshr_b32 s90, %1, 3\n" \
"ds_read_b64_tr_b16 a[214:215], v209 offset:17408\n" \
"v_mfma_f32_16x16x32_bf16 v[12:15], a[112:115], a[80:83], v[12:15]\n" \
"ds_read_b64_tr_b16 a[216:217], v210 offset:4096\n" \
"v_mad_u32_u24 v203, v203, 3, v204\n" \
"v_mfma_f32_16x16x32_bf16 v[12:15], a[116:119], a[84:87], v[12:15]\n" \
"s_lshl_b32 s90, s90, 2\n" \
"global_load_lds_dwordx4 v253, s[50:51] offset:-3072\n" \
"v_mfma_f32_16x16x32_bf16 v[8:11], a[104:107], a[80:83], v[8:11]\n" \
"s_add_u32 s90, s90, s61\n" \
"ds_read_b64_tr_b16 a[218:219], v210 offset:5120\n" \
"v_and_b32 v204, 16, v248\n" \
"v_mfma_f32_16x16x32_bf16 v[8:11], a[108:111], a[84:87], v[8:11]\n" \
"s_add_u32 s90, s90, 128\n" \
"s_mul_i32 s90, s90, 24576\n" \
"ds_read_b64_tr_b16 a[220:221], v210 offset:16384\n" \
"v_mfma_f32_16x16x32_bf16 v[48:51], a[96:99], a[80:83], v[48:51]\n" \
"s_add_u32 s89, s89, s90\n" \
"ds_read_b64_tr_b16 a[222:223], v210 offset:17408\n" \
"v_lshl_add_u32 v204, v204, 2, s89\n" \
"v_mfma_f32_16x16x32_bf16 v[48:51], a[100:103], a[84:87], v[48:51]\n" \
"global_load_lds_dwordx4 v253, s[50:51] offset:-1024\n" \
"v_lshl_add_u32 v203, v203, 11, v204\n" \
"v_mfma_f32_16x16x32_bf16 v[56:59], a[96:99], a[88:91], v[56:59]\n" \
"ds_read_b64_tr_b16 a[200:201], v210 offset:2048\n" \
"v_mfma_f32_16x16x32_bf16 v[56:59], a[100:103], a[92:95], v[56:59]\n" \
"ds_read_b64_tr_b16 a[202:203], v210 offset:3072\n" \
"v_mfma_f32_16x16x32_bf16 v[16:19], a[104:107], a[88:91], v[16:19]\n" \
"ds_read_b64_tr_b16 a[204:205], v210 offset:14336\n" \
"v_mfma_f32_16x16x32_bf16 v[16:19], a[108:111], a[92:95], v[16:19]\n" \
"global_load_lds_dwordx4 v253, s[50:51] offset:1024\n" \
"v_mfma_f32_16x16x32_bf16 v[20:23], a[112:115], a[88:91], v[20:23]\n" \
"s_add_u32 s50, s50, 0x60000\n" \
"s_addc_u32 s51, s51, 0\n" \
"ds_read_b64_tr_b16 a[206:207], v210 offset:15360\n" \
"v_mfma_f32_16x16x32_bf16 v[20:23], a[116:119], a[92:95], v[20:23]\n" \
"s_waitcnt lgkmcnt(3)\n" \
"v_add_u32 v207, 0x1d000, v207\n" \
"v_add_u32 v208, 0x1d000, v208\n" \
"v_mfma_f32_16x16x32_bf16 v[28:31], a[112:115], a[192:195], v[28:31]\n" \
"v_add_u32 v205, 0x1d000, v205\n" \
"v_add_u32 v206, 0x1d000, v206\n" \
"v_mfma_f32_16x16x32_bf16 v[28:31], a[116:119], a[196:199], v[28:31]\n" \
"v_add_u32 v209, 0x1d000, v209\n" \
"v_add_u32 v210, 0x1d000, v210\n" \
"v_mfma_f32_16x16x32_bf16 v[24:27], a[104:107], a[192:195], v[24:27]\n" \
"v_mfma_f32_16x16x32_bf16 v[24:27], a[108:111], a[196:199], v[24:27]\n" \
"v_mfma_f32_16x16x32_bf16 v[64:67], a[96:99], a[192:195], v[64:67]\n" \
"s_waitcnt vmcnt(13) lgkmcnt(0)\n" \
"v_mfma_f32_16x16x32_bf16 v[64:67], a[100:103], a[196:199], v[64:67]\n" \
"s_barrier\n" \
"v_mfma_f32_16x16x32_bf16 v[72:75], a[64:67], a[208:211], v[72:75]\n" \
"ds_read_b64_tr_b16 a[96:97], v209 offset:2048\n" \
"v_mfma_f32_16x16x32_bf16 v[72:75], a[68:71], a[212:215], v[72:75]\n" \
"ds_read_b64_tr_b16 a[98:99], v209 offset:3072\n" \
"v_mfma_f32_16x16x32_bf16 v[80:83], a[72:75], a[208:211], v[80:83]\n" \
"s_add_u32 m0, s95, 0\n" \
"ds_read_b128 a[64:67], v207 offset:0\n" \
"ds_read_b64_tr_b16 a[100:101], v209 offset:14336\n" \
"v_mfma_f32_16x16x32_bf16 v[80:83], a[76:79], a[212:215], v[80:83]\n" \
"global_load_lds_dwordx4 v252, s[42:43] offset:0\n" \
"v_mfma_f32_16x16x32_bf16 v[84:87], a[72:75], a[216:219], v[84:87]\n" \
"ds_read_b128 a[68:71], v208 offset:0\n" \
"ds_read_b64_tr_b16 a[102:103], v209 offset:15360\n" \
"v_mfma_f32_16x16x32_bf16 v[84:87], a[76:79], a[220:223], v[84:87]\n" \
"ds_read_b64_tr_b16 a[104:105], v209 offset:0\n" \
"v_mfma_f32_16x16x32_bf16 v[44:47], a[200:203], a[72:75], v[44:47]\n" \
"ds_read_b64_tr_b16 a[106:107], v209 offset:1024\n" \
"v_mfma_f32_16x16x32_bf16 v[44:47], a[204:207], a[76:79], v[44:47]\n" \
"global_load_lds_dwordx4 v252, s[42:43] offset:2048\n" \
"v_mfma_f32_16x16x32_bf16 v[52:55], a[200:203], a[80:83], v[52:55]\n" \
"s_add_u32 s42, s42, 0x80\n" \
"s_addc_u32 s43, s43, 0\n" \
"ds_read_b128 a[72:75], v205 offset:0\n" \
"v_mfma_f32_16x16x32_bf16 v[52:55], a[204:207], a[84:87], v[52:55]\n" \
"ds_read_b128 a[76:79], v206 offset:0\n" \
"v_mfma_f32_16x16x32_bf16 v[92:95], a[80:83], a[216:219], v[92:95]\n" \
"ds_read_b64_tr_b16 a[108:109], v209 offset:12288\n" \
"v_mfma_f32_16x16x32_bf16 v[92:95], a[84:87], a[220:223], v[92:95]\n" \
"global_load_lds_dwordx4 v252, s[44:45] offset:1024\n" \
"v_mfma_f32_16x16x32_bf16 v[88:91], a[80:83], a[208:211], v[88:91]\n" \
"ds_read_b64_tr_b16 a[110:111], v209 offset:13312\n" \
"v_mfma_f32_16x16x32_bf16 v[88:91], a[84:87], a[212:215], v[88:91]\n" \
"ds_read_b64_tr_b16 a[112:113], v210 offset:0\n" \
"v_mfma_f32_16x16x32_bf16 v[96:99], a[88:91], a[208:211], v[96:99]\n" \
"ds_read_b128 a[80:83], v205 offset:4096\n" \
"v_mfma_f32_16x16x32_bf16 v[96:99], a[92:95], a[212:215], v[96:99]\n" \
"global_load_lds_dwordx4 v252, s[44:45] offset:3072\n" \
"v_mfma_f32_16x16x32_bf16 v[100:103], a[88:91], a[216:219], v[100:103]\n" \
"s_add_u32 s44, s44, 0x80\n" \
"s_addc_u32 s45, s45, 0\n" \
"ds_read_b128 a[84:87], v206 offset:4096\n" \
"v_mfma_f32_16x16x32_bf16 v[100:103], a[92:95], a[220:223], v[100:103]\n" \
"ds_read_b64_tr_b16 a[114:115], v210 offset:1024\n" \
"v_mfma_f32_16x16x32_bf16 v[60:63], a[200:203], a[88:91], v[60:63]\n" \
"s_add_u32 m0, s85, 16384\n" \
"ds_read_b64_tr_b16 a[116:117], v210 offset:12288\n" \
"v_mfma_f32_16x16x32_bf16 v[60:63], a[204:207], a[92:95], v[60:63]\n" \
"buffer_load_dwordx4 v246, s[12:15], s46 offen lds\n" \
"v_mfma_f32_16x16x32_bf16 v[68:71], a[200:203], a[192:195], v[68:71]\n" \
"ds_read_b128 a[88:91], v205 offset:8192\n" \
"s_add_u32 s46, s46, 0x80\n" \
"v_mfma_f32_16x16x32_bf16 v[68:71], a[204:207], a[196:199], v[68:71]\n" \
"ds_read_b128 a[92:95], v206 offset:8192\n" \
"v_mfma_f32_16x16x32_bf16 v[108:111], a[192:195], a[216:219], v[108:111]\n" \
"s_add_u32 m0, s84, 24576\n" \
"ds_read_b64_tr_b16 a[118:119], v210 offset:13312\n" \
"v_mfma_f32_16x16x32_bf16 v[108:111], a[196:199], a[220:223], v[108:111]\n" \
"global_load_lds_dwordx4 v253, s[48:49] offset:-4096\n" \
"v_mfma_f32_16x16x32_bf16 v[104:107], a[192:195], a[208:211], v[104:107]\n" \
"v_mfma_f32_16x16x32_bf16 v[104:107], a[196:199], a[212:215], v[104:107]\n" \
"s_waitcnt lgkmcnt(2)\n" \
"v_mfma_f32_16x16x32_bf16 v[32:35], a[96:99], a[64:67], v[32:35]\n" \
"s_mov_b32 s30, 65593856\n" \
"ds_read_b128 a[192:195], v205 offset:12288\n" \
"v_lshrrev_b32 v228, 1, v248\n" \
"v_mfma_f32_16x16x32_bf16 v[32:35], a[100:103], a[68:71], v[32:35]\n" \
"global_load_lds_dwordx4 v253, s[48:49] offset:-2048\n" \
"v_mfma_f32_16x16x32_bf16 v[40:43], a[96:99], a[72:75], v[40:43]\n" \
"ds_read_b128 a[196:199], v206 offset:12288\n" \
"v_mfma_f32_16x16x32_bf16 v[40:43], a[100:103], a[76:79], v[40:43]\n" \
"ds_read_b64_tr_b16 a[208:209], v209 offset:4096\n" \
"v_mfma_f32_16x16x32_bf16 v[0:3], a[104:107], a[72:75], v[0:3]\n" \
"ds_read_b64_tr_b16 a[210:211], v209 offset:5120\n" \
"v_mfma_f32_16x16x32_bf16 v[0:3], a[108:111], a[76:79], v[0:3]\n" \
"s_waitcnt lgkmcnt(3)\n" \
"global_load_lds_dwordx4 v253, s[48:49] offset:0\n" \
"v_mfma_f32_16x16x32_bf16 v[4:7], a[112:115], a[72:75], v[4:7]\n" \
"s_add_u32 s48, s48, 0x60000\n" \
"s_addc_u32 s49, s49, 0\n" \
"ds_read_b64_tr_b16 a[212:213], v209 offset:16384\n" \
"v_mfma_f32_16x16x32_bf16 v[4:7], a[116:119], a[76:79], v[4:7]\n" \
"ds_read_b64_tr_b16 a[214:215], v209 offset:17408\n" \
"v_mfma_f32_16x16x32_bf16 v[12:15], a[112:115], a[80:83], v[12:15]\n" \
"ds_read_b64_tr_b16 a[216:217], v210 offset:4096\n" \
"v_mfma_f32_16x16x32_bf16 v[12:15], a[116:119], a[84:87], v[12:15]\n" \
"global_load_lds_dwordx4 v253, s[50:51] offset:-3072\n" \
"v_mfma_f32_16x16x32_bf16 v[8:11], a[104:107], a[80:83], v[8:11]\n" \
"ds_read_b64_tr_b16 a[218:219], v210 offset:5120\n" \
"v_mfma_f32_16x16x32_bf16 v[8:11], a[108:111], a[84:87], v[8:11]\n" \
"ds_read_b64_tr_b16 a[220:221], v210 offset:16384\n" \
"v_mfma_f32_16x16x32_bf16 v[48:51], a[96:99], a[80:83], v[48:51]\n" \
"ds_read_b64_tr_b16 a[222:223], v210 offset:17408\n" \
"v_mfma_f32_16x16x32_bf16 v[48:51], a[100:103], a[84:87], v[48:51]\n" \
"global_load_lds_dwordx4 v253, s[50:51] offset:-1024\n" \
"v_mfma_f32_16x16x32_bf16 v[56:59], a[96:99], a[88:91], v[56:59]\n" \
"ds_read_b64_tr_b16 a[200:201], v210 offset:2048\n" \
"v_mfma_f32_16x16x32_bf16 v[56:59], a[100:103], a[92:95], v[56:59]\n" \
"ds_read_b64_tr_b16 a[202:203], v210 offset:3072\n" \
"v_mfma_f32_16x16x32_bf16 v[16:19], a[104:107], a[88:91], v[16:19]\n" \
"ds_read_b64_tr_b16 a[204:205], v210 offset:14336\n" \
"v_mfma_f32_16x16x32_bf16 v[16:19], a[108:111], a[92:95], v[16:19]\n" \
"global_load_lds_dwordx4 v253, s[50:51] offset:1024\n" \
"v_mfma_f32_16x16x32_bf16 v[20:23], a[112:115], a[88:91], v[20:23]\n" \
"s_add_u32 s50, s50, 0x60000\n" \
"s_addc_u32 s51, s51, 0\n" \
"ds_read_b64_tr_b16 a[206:207], v210 offset:15360\n" \
"v_mfma_f32_16x16x32_bf16 v[20:23], a[116:119], a[92:95], v[20:23]\n" \
"s_waitcnt lgkmcnt(3)\n" \
"v_add_u32 v207, 0xffff3000, v207\n" \
"v_add_u32 v208, 0xffff3000, v208\n" \
"v_mfma_f32_16x16x32_bf16 v[28:31], a[112:115], a[192:195], v[28:31]\n" \
"v_add_u32 v205, 0xffff3000, v205\n" \
"v_add_u32 v206, 0xffff3000, v206\n" \
"v_mfma_f32_16x16x32_bf16 v[28:31], a[116:119], a[196:199], v[28:31]\n" \
"global_load_dword v204, v203, s[22:23]\n" \
"v_mfma_f32_16x16x32_bf16 v[24:27], a[104:107], a[192:195], v[24:27]\n" \
"v_add_u32 v209, 0xffff3000, v209\n" \
"v_add_u32 v210, 0xffff3000, v210\n" \
"v_mfma_f32_16x16x32_bf16 v[24:27], a[108:111], a[196:199], v[24:27]\n" \
"v_mfma_f32_16x16x32_bf16 v[64:67], a[96:99], a[192:195], v[64:67]\n" \
"s_waitcnt vmcnt(12) lgkmcnt(0)\n" \
"v_mfma_f32_16x16x32_bf16 v[64:67], a[100:103], a[196:199], v[64:67]\n" \
"s_barrier\n" \
"v_mfma_f32_16x16x32_bf16 v[72:75], a[64:67], a[208:211], v[72:75]\n" \
"ds_read_b64_tr_b16 a[96:97], v209 offset:2048\n" \
"v_mfma_f32_16x16x32_bf16 v[72:75], a[68:71], a[212:215], v[72:75]\n" \
"ds_read_b64_tr_b16 a[98:99], v209 offset:3072\n" \
"v_mfma_f32_16x16x32_bf16 v[80:83], a[72:75], a[208:211], v[80:83]\n" \
"s_add_u32 m0, s95, 118784\n" \
"ds_read_b128 a[64:67], v207 offset:0\n" \
"ds_read_b64_tr_b16 a[100:101], v209 offset:14336\n" \
"v_mfma_f32_16x16x32_bf16 v[80:83], a[76:79], a[212:215], v[80:83]\n" \
"global_load_lds_dwordx4 v252, s[42:43] offset:0\n" \
"v_mfma_f32_16x16x32_bf16 v[84:87], a[72:75], a[216:219], v[84:87]\n" \
"ds_read_b128 a[68:71], v208 offset:0\n" \
"ds_read_b64_tr_b16 a[102:103], v209 offset:15360\n" \
"v_mfma_f32_16x16x32_bf16 v[84:87], a[76:79], a[220:223], v[84:87]\n" \
"ds_read_b64_tr_b16 a[104:105], v209 offset:0\n" \
"v_mfma_f32_16x16x32_bf16 v[44:47], a[200:203], a[72:75], v[44:47]\n" \
"ds_read_b64_tr_b16 a[106:107], v209 offset:1024\n" \
"v_mfma_f32_16x16x32_bf16 v[44:47], a[204:207], a[76:79], v[44:47]\n" \
"global_load_lds_dwordx4 v252, s[42:43] offset:2048\n" \
"v_mfma_f32_16x16x32_bf16 v[52:55], a[200:203], a[80:83], v[52:55]\n" \
"s_add_u32 s42, s42, 0x80\n" \
"s_addc_u32 s43, s43, 0\n" \
"ds_read_b128 a[72:75], v205 offset:0\n" \
"v_mfma_f32_16x16x32_bf16 v[52:55], a[204:207], a[84:87], v[52:55]\n" \
"ds_read_b128 a[76:79], v206 offset:0\n" \
"v_mfma_f32_16x16x32_bf16 v[92:95], a[80:83], a[216:219], v[92:95]\n" \
"ds_read_b64_tr_b16 a[108:109], v209 offset:12288\n" \
"v_mfma_f32_16x16x32_bf16 v[92:95], a[84:87], a[220:223], v[92:95]\n" \
"global_load_lds_dwordx4 v252, s[44:45] offset:1024\n" \
"v_mfma_f32_16x16x32_bf16 v[88:91], a[80:83], a[208:211], v[88:91]\n" \
"ds_read_b64_tr_b16 a[110:111], v209 offset:13312\n" \
"v_mfma_f32_16x16x32_bf16 v[88:91], a[84:87], a[212:215], v[88:91]\n" \
"ds_read_b64_tr_b16 a[112:113], v210 offset:0\n" \
"v_mfma_f32_16x16x32_bf16 v[96:99], a[88:91], a[208:211], v[96:99]\n" \
"ds_read_b128 a[80:83], v205 offset:4096\n" \
"v_mfma_f32_16x16x32_bf16 v[96:99], a[92:95], a[212:215], v[96:99]\n" \
"global_load_lds_dwordx4 v252, s[44:45] offset:3072\n" \
"v_mfma_f32_16x16x32_bf16 v[100:103], a[88:91], a[216:219], v[100:103]\n" \
"s_add_u32 s44, s44, 0x80\n" \
"s_addc_u32 s45, s45, 0\n" \
"ds_read_b128 a[84:87], v206 offset:4096\n" \
"v_mfma_f32_16x16x32_bf16 v[100:103], a[92:95], a[220:223], v[100:103]\n" \
"ds_read_b64_tr_b16 a[114:115], v210 offset:1024\n" \
"v_mfma_f32_16x16x32_bf16 v[60:63], a[200:203], a[88:91], v[60:63]\n" \
"s_add_u32 m0, s85, 135168\n" \
"ds_read_b64_tr_b16 a[116:117], v210 offset:12288\n" \
"v_mfma_f32_16x16x32_bf16 v[60:63], a[204:207], a[92:95], v[60:63]\n" \
"buffer_load_dwordx4 v246, s[12:15], s46 offen lds\n" \
"v_mfma_f32_16x16x32_bf16 v[68:71], a[200:203], a[192:195], v[68:71]\n" \
"ds_read_b128 a[88:91], v205 offset:8192\n" \
"s_add_u32 s46, s46, 0x80\n" \
"v_mfma_f32_16x16x32_bf16 v[68:71], a[204:207], a[196:199], v[68:71]\n" \
"ds_read_b128 a[92:95], v206 offset:8192\n" \
"v_mfma_f32_16x16x32_bf16 v[108:111], a[192:195], a[216:219], v[108:111]\n" \
"s_add_u32 m0, s84, 143360\n" \
"ds_read_b64_tr_b16 a[118:119], v210 offset:13312\n" \
"v_mfma_f32_16x16x32_bf16 v[108:111], a[196:199], a[220:223], v[108:111]\n" \
"global_load_lds_dwordx4 v253, s[48:49] offset:-4096\n" \
"v_mfma_f32_16x16x32_bf16 v[104:107], a[192:195], a[208:211], v[104:107]\n" \
"v_mfma_f32_16x16x32_bf16 v[104:107], a[196:199], a[212:215], v[104:107]\n" \
"s_waitcnt lgkmcnt(2)\n" \
"v_mfma_f32_16x16x32_bf16 v[32:35], a[96:99], a[64:67], v[32:35]\n" \
"ds_read_b128 a[192:195], v205 offset:12288\n" \
"v_mfma_f32_16x16x32_bf16 v[32:35], a[100:103], a[68:71], v[32:35]\n" \
"global_load_lds_dwordx4 v253, s[48:49] offset:-2048\n" \
"Lpg9736_%=:\n" \
"s_cbranch_execz Lpg12038_%=\n" \
"v_mfma_f32_16x16x32_bf16 v[40:43], a[96:99], a[72:75], v[40:43]\n" \
"ds_read_b128 a[196:199], v206 offset:12288\n" \
"v_mfma_f32_16x16x32_bf16 v[40:43], a[100:103], a[76:79], v[40:43]\n" \
"ds_read_b64_tr_b16 a[208:209], v209 offset:4096\n" \
"v_mfma_f32_16x16x32_bf16 v[0:3], a[104:107], a[72:75], v[0:3]\n" \
"ds_read_b64_tr_b16 a[210:211], v209 offset:5120\n" \
"v_mfma_f32_16x16x32_bf16 v[0:3], a[108:111], a[76:79], v[0:3]\n" \
"s_waitcnt lgkmcnt(3)\n" \
"global_load_lds_dwordx4 v253, s[48:49] offset:0\n" \
"v_mfma_f32_16x16x32_bf16 v[4:7], a[112:115], a[72:75], v[4:7]\n" \
"s_add_u32 s48, s48, 0x60000\n" \
"s_addc_u32 s49, s49, 0\n" \
"ds_read_b64_tr_b16 a[212:213], v209 offset:16384\n" \
"v_mfma_f32_16x16x32_bf16 v[4:7], a[116:119], a[76:79], v[4:7]\n" \
"ds_read_b64_tr_b16 a[214:215], v209 offset:17408\n" \
"v_mfma_f32_16x16x32_bf16 v[12:15], a[112:115], a[80:83], v[12:15]\n" \
"ds_read_b64_tr_b16 a[216:217], v210 offset:4096\n" \
"v_mfma_f32_16x16x32_bf16 v[12:15], a[116:119], a[84:87], v[12:15]\n" \
"global_load_lds_dwordx4 v253, s[50:51] offset:-3072\n" \
"v_mfma_f32_16x16x32_bf16 v[8:11], a[104:107], a[80:83], v[8:11]\n" \
"ds_read_b64_tr_b16 a[218:219], v210 offset:5120\n" \
"v_mfma_f32_16x16x32_bf16 v[8:11], a[108:111], a[84:87], v[8:11]\n" \
"ds_read_b64_tr_b16 a[220:221], v210 offset:16384\n" \
"v_mfma_f32_16x16x32_bf16 v[48:51], a[96:99], a[80:83], v[48:51]\n" \
"ds_read_b64_tr_b16 a[222:223], v210 offset:17408\n" \
"v_mfma_f32_16x16x32_bf16 v[48:51], a[100:103], a[84:87], v[48:51]\n" \
"global_load_lds_dwordx4 v253, s[50:51] offset:-1024\n" \
"v_mfma_f32_16x16x32_bf16 v[56:59], a[96:99], a[88:91], v[56:59]\n" \
"ds_read_b64_tr_b16 a[200:201], v210 offset:2048\n" \
"v_mfma_f32_16x16x32_bf16 v[56:59], a[100:103], a[92:95], v[56:59]\n" \
"ds_read_b64_tr_b16 a[202:203], v210 offset:3072\n" \
"v_mfma_f32_16x16x32_bf16 v[16:19], a[104:107], a[88:91], v[16:19]\n" \
"ds_read_b64_tr_b16 a[204:205], v210 offset:14336\n" \
"v_mfma_f32_16x16x32_bf16 v[16:19], a[108:111], a[92:95], v[16:19]\n" \
"global_load_lds_dwordx4 v253, s[50:51] offset:1024\n" \
"v_mfma_f32_16x16x32_bf16 v[20:23], a[112:115], a[88:91], v[20:23]\n" \
"s_add_u32 s50, s50, 0x60000\n" \
"s_addc_u32 s51, s51, 0\n" \
"ds_read_b64_tr_b16 a[206:207], v210 offset:15360\n" \
"v_mfma_f32_16x16x32_bf16 v[20:23], a[116:119], a[92:95], v[20:23]\n" \
"s_waitcnt lgkmcnt(3)\n" \
"v_add_u32 v207, 0xffff0000, v207\n" \
"v_add_u32 v208, 0xffff0000, v208\n" \
"v_mfma_f32_16x16x32_bf16 v[28:31], a[112:115], a[192:195], v[28:31]\n" \
"v_add_u32 v205, 0xffff0000, v205\n" \
"v_add_u32 v206, 0xffff0000, v206\n" \
"v_mfma_f32_16x16x32_bf16 v[28:31], a[116:119], a[196:199], v[28:31]\n" \
"buffer_load_dwordx2 v[226:227], v228, s[56:59], s30 offen sc1\n" \
"v_mfma_f32_16x16x32_bf16 v[24:27], a[104:107], a[192:195], v[24:27]\n" \
"v_add_u32 v209, 0xffff0000, v209\n" \
"v_add_u32 v210, 0xffff0000, v210\n" \
"v_mfma_f32_16x16x32_bf16 v[24:27], a[108:111], a[196:199], v[24:27]\n" \
"v_mfma_f32_16x16x32_bf16 v[64:67], a[96:99], a[192:195], v[64:67]\n" \
"s_waitcnt vmcnt(13) lgkmcnt(0)\n" \
"v_mfma_f32_16x16x32_bf16 v[64:67], a[100:103], a[196:199], v[64:67]\n" \
"s_barrier\n" \
"v_mfma_f32_16x16x32_bf16 v[72:75], a[64:67], a[208:211], v[72:75]\n" \
"ds_read_b64_tr_b16 a[96:97], v209 offset:2048\n" \
"v_mfma_f32_16x16x32_bf16 v[72:75], a[68:71], a[212:215], v[72:75]\n" \
"ds_read_b64_tr_b16 a[98:99], v209 offset:3072\n" \
"v_mfma_f32_16x16x32_bf16 v[80:83], a[72:75], a[208:211], v[80:83]\n" \
"s_add_u32 m0, s95, 65536\n" \
"ds_read_b128 a[64:67], v207 offset:0\n" \
"ds_read_b64_tr_b16 a[100:101], v209 offset:14336\n" \
"v_mfma_f32_16x16x32_bf16 v[80:83], a[76:79], a[212:215], v[80:83]\n" \
"global_load_lds_dwordx4 v252, s[42:43] offset:0\n" \
"v_mfma_f32_16x16x32_bf16 v[84:87], a[72:75], a[216:219], v[84:87]\n" \
"ds_read_b128 a[68:71], v208 offset:0\n" \
"ds_read_b64_tr_b16 a[102:103], v209 offset:15360\n" \
"v_mfma_f32_16x16x32_bf16 v[84:87], a[76:79], a[220:223], v[84:87]\n" \
"ds_read_b64_tr_b16 a[104:105], v209 offset:0\n" \
"v_mfma_f32_16x16x32_bf16 v[44:47], a[200:203], a[72:75], v[44:47]\n" \
"ds_read_b64_tr_b16 a[106:107], v209 offset:1024\n" \
"v_mfma_f32_16x16x32_bf16 v[44:47], a[204:207], a[76:79], v[44:47]\n" \
"global_load_lds_dwordx4 v252, s[42:43] offset:2048\n" \
"v_mfma_f32_16x16x32_bf16 v[52:55], a[200:203], a[80:83], v[52:55]\n" \
"s_add_u32 s42, s42, 0x80\n" \
"s_addc_u32 s43, s43, 0\n" \
"ds_read_b128 a[72:75], v205 offset:0\n" \
"v_mfma_f32_16x16x32_bf16 v[52:55], a[204:207], a[84:87], v[52:55]\n" \
"ds_read_b128 a[76:79], v206 offset:0\n" \
"v_mfma_f32_16x16x32_bf16 v[92:95], a[80:83], a[216:219], v[92:95]\n" \
"ds_read_b64_tr_b16 a[108:109], v209 offset:12288\n" \
"v_mfma_f32_16x16x32_bf16 v[92:95], a[84:87], a[220:223], v[92:95]\n" \
"global_load_lds_dwordx4 v252, s[44:45] offset:1024\n" \
"v_mfma_f32_16x16x32_bf16 v[88:91], a[80:83], a[208:211], v[88:91]\n" \
"ds_read_b64_tr_b16 a[110:111], v209 offset:13312\n" \
"v_mfma_f32_16x16x32_bf16 v[88:91], a[84:87], a[212:215], v[88:91]\n" \
"ds_read_b64_tr_b16 a[112:113], v210 offset:0\n" \
"v_mfma_f32_16x16x32_bf16 v[96:99], a[88:91], a[208:211], v[96:99]\n" \
"ds_read_b128 a[80:83], v205 offset:4096\n" \
"v_mfma_f32_16x16x32_bf16 v[96:99], a[92:95], a[212:215], v[96:99]\n" \
"global_load_lds_dwordx4 v252, s[44:45] offset:3072\n" \
"v_mfma_f32_16x16x32_bf16 v[100:103], a[88:91], a[216:219], v[100:103]\n" \
"s_add_u32 s44, s44, 0x80\n" \
"s_addc_u32 s45, s45, 0\n" \
"ds_read_b128 a[84:87], v206 offset:4096\n" \
"v_mfma_f32_16x16x32_bf16 v[100:103], a[92:95], a[220:223], v[100:103]\n" \
"ds_read_b64_tr_b16 a[114:115], v210 offset:1024\n" \
"v_mfma_f32_16x16x32_bf16 v[60:63], a[200:203], a[88:91], v[60:63]\n" \
"s_add_u32 m0, s85, 81920\n" \
"ds_read_b64_tr_b16 a[116:117], v210 offset:12288\n" \
"v_mfma_f32_16x16x32_bf16 v[60:63], a[204:207], a[92:95], v[60:63]\n" \
"buffer_load_dwordx4 v246, s[12:15], s46 offen lds\n" \
"v_mfma_f32_16x16x32_bf16 v[68:71], a[200:203], a[192:195], v[68:71]\n" \
"ds_read_b128 a[88:91], v205 offset:8192\n" \
"s_add_u32 s46, s46, 0x80\n" \
"v_mfma_f32_16x16x32_bf16 v[68:71], a[204:207], a[196:199], v[68:71]\n" \
"ds_read_b128 a[92:95], v206 offset:8192\n" \
"v_mfma_f32_16x16x32_bf16 v[108:111], a[192:195], a[216:219], v[108:111]\n" \
"s_add_u32 m0, s84, 90112\n" \
"ds_read_b64_tr_b16 a[118:119], v210 offset:13312\n" \
"v_mfma_f32_16x16x32_bf16 v[108:111], a[196:199], a[220:223], v[108:111]\n" \
"global_load_lds_dwordx4 v253, s[48:49] offset:-4096\n" \
"v_mfma_f32_16x16x32_bf16 v[104:107], a[192:195], a[208:211], v[104:107]\n" \
"v_mfma_f32_16x16x32_bf16 v[104:107], a[196:199], a[212:215], v[104:107]\n" \
"s_waitcnt lgkmcnt(2)\n" \
"v_mfma_f32_16x16x32_bf16 v[32:35], a[96:99], a[64:67], v[32:35]\n" \
"ds_read_b128 a[192:195], v205 offset:12288\n" \
"v_mfma_f32_16x16x32_bf16 v[32:35], a[100:103], a[68:71], v[32:35]\n" \
"global_load_lds_dwordx4 v253, s[48:49] offset:-2048\n" \
"v_mfma_f32_16x16x32_bf16 v[40:43], a[96:99], a[72:75], v[40:43]\n" \
"ds_read_b128 a[196:199], v206 offset:12288\n" \
"v_mfma_f32_16x16x32_bf16 v[40:43], a[100:103], a[76:79], v[40:43]\n" \
"ds_read_b64_tr_b16 a[208:209], v209 offset:4096\n" \
"v_mfma_f32_16x16x32_bf16 v[0:3], a[104:107], a[72:75], v[0:3]\n" \
"ds_read_b64_tr_b16 a[210:211], v209 offset:5120\n" \
"v_mfma_f32_16x16x32_bf16 v[0:3], a[108:111], a[76:79], v[0:3]\n" \
"s_waitcnt lgkmcnt(3)\n" \
"global_load_lds_dwordx4 v253, s[48:49] offset:0\n" \
"v_mfma_f32_16x16x32_bf16 v[4:7], a[112:115], a[72:75], v[4:7]\n" \
"s_add_u32 s48, s48, 0x60000\n" \
"s_addc_u32 s49, s49, 0\n" \
"ds_read_b64_tr_b16 a[212:213], v209 offset:16384\n" \
"v_mfma_f32_16x16x32_bf16 v[4:7], a[116:119], a[76:79], v[4:7]\n" \
"ds_read_b64_tr_b16 a[214:215], v209 offset:17408\n" \
"v_mfma_f32_16x16x32_bf16 v[12:15], a[112:115], a[80:83], v[12:15]\n" \
"ds_read_b64_tr_b16 a[216:217], v210 offset:4096\n" \
"v_mfma_f32_16x16x32_bf16 v[12:15], a[116:119], a[84:87], v[12:15]\n" \
"global_load_lds_dwordx4 v253, s[50:51] offset:-3072\n" \
"v_mfma_f32_16x16x32_bf16 v[8:11], a[104:107], a[80:83], v[8:11]\n" \
"ds_read_b64_tr_b16 a[218:219], v210 offset:5120\n" \
"v_mfma_f32_16x16x32_bf16 v[8:11], a[108:111], a[84:87], v[8:11]\n" \
"ds_read_b64_tr_b16 a[220:221], v210 offset:16384\n" \
"v_mfma_f32_16x16x32_bf16 v[48:51], a[96:99], a[80:83], v[48:51]\n" \
"ds_read_b64_tr_b16 a[222:223], v210 offset:17408\n" \
"v_mfma_f32_16x16x32_bf16 v[48:51], a[100:103], a[84:87], v[48:51]\n" \
"global_load_lds_dwordx4 v253, s[50:51] offset:-1024\n" \
"v_mfma_f32_16x16x32_bf16 v[56:59], a[96:99], a[88:91], v[56:59]\n" \
"ds_read_b64_tr_b16 a[200:201], v210 offset:2048\n" \
"v_mfma_f32_16x16x32_bf16 v[56:59], a[100:103], a[92:95], v[56:59]\n" \
"ds_read_b64_tr_b16 a[202:203], v210 offset:3072\n" \
"v_mfma_f32_16x16x32_bf16 v[16:19], a[104:107], a[88:91], v[16:19]\n" \
"ds_read_b64_tr_b16 a[204:205], v210 offset:14336\n" \
"v_mfma_f32_16x16x32_bf16 v[16:19], a[108:111], a[92:95], v[16:19]\n" \
"global_load_lds_dwordx4 v253, s[50:51] offset:1024\n" \
"v_mfma_f32_16x16x32_bf16 v[20:23], a[112:115], a[88:91], v[20:23]\n" \
"s_add_u32 s50, s50, 0x60000\n" \
"s_addc_u32 s51, s51, 0\n" \
"ds_read_b64_tr_b16 a[206:207], v210 offset:15360\n" \
"v_mfma_f32_16x16x32_bf16 v[20:23], a[116:119], a[92:95], v[20:23]\n" \
"s_waitcnt lgkmcnt(3)\n" \
"v_add_u32 v207, 0x1d000, v207\n" \
"v_add_u32 v208, 0x1d000, v208\n" \
"v_mfma_f32_16x16x32_bf16 v[28:31], a[112:115], a[192:195], v[28:31]\n" \
"v_add_u32 v205, 0x1d000, v205\n" \
"v_add_u32 v206, 0x1d000, v206\n" \
"v_mfma_f32_16x16x32_bf16 v[28:31], a[116:119], a[196:199], v[28:31]\n" \
"v_add_u32 v209, 0x1d000, v209\n" \
"v_add_u32 v210, 0x1d000, v210\n" \
"v_mfma_f32_16x16x32_bf16 v[24:27], a[104:107], a[192:195], v[24:27]\n" \
"v_mfma_f32_16x16x32_bf16 v[24:27], a[108:111], a[196:199], v[24:27]\n" \
"v_mfma_f32_16x16x32_bf16 v[64:67], a[96:99], a[192:195], v[64:67]\n" \
"s_waitcnt vmcnt(12) lgkmcnt(0)\n" \
"v_mfma_f32_16x16x32_bf16 v[64:67], a[100:103], a[196:199], v[64:67]\n" \
"s_barrier\n" \
"v_mfma_f32_16x16x32_bf16 v[72:75], a[64:67], a[208:211], v[72:75]\n" \
"ds_read_b64_tr_b16 a[96:97], v209 offset:2048\n" \
"s_waitcnt vmcnt(11)\n" \
"v_mfma_f32_16x16x32_bf16 v[72:75], a[68:71], a[212:215], v[72:75]\n" \
"ds_read_b64_tr_b16 a[98:99], v209 offset:3072\n" \
"v_mfma_f32_16x16x32_bf16 v[80:83], a[72:75], a[208:211], v[80:83]\n" \
"s_add_u32 m0, s95, 0\n" \
"ds_read_b128 a[64:67], v207 offset:0\n" \
"ds_read_b64_tr_b16 a[100:101], v209 offset:14336\n" \
"v_mfma_f32_16x16x32_bf16 v[80:83], a[76:79], a[212:215], v[80:83]\n" \
"global_load_lds_dwordx4 v252, s[42:43] offset:0\n" \
"v_mfma_f32_16x16x32_bf16 v[84:87], a[72:75], a[216:219], v[84:87]\n" \
"ds_read_b128 a[68:71], v208 offset:0\n" \
"ds_read_b64_tr_b16 a[102:103], v209 offset:15360\n" \
"v_mfma_f32_16x16x32_bf16 v[84:87], a[76:79], a[220:223], v[84:87]\n" \
"ds_read_b64_tr_b16 a[104:105], v209 offset:0\n" \
"v_mfma_f32_16x16x32_bf16 v[44:47], a[200:203], a[72:75], v[44:47]\n" \
"ds_read_b64_tr_b16 a[106:107], v209 offset:1024\n" \
"v_mfma_f32_16x16x32_bf16 v[44:47], a[204:207], a[76:79], v[44:47]\n" \
"global_load_lds_dwordx4 v252, s[42:43] offset:2048\n" \
"v_mfma_f32_16x16x32_bf16 v[52:55], a[200:203], a[80:83], v[52:55]\n" \
"s_add_u32 s42, s42, 0x100\n" \
"s_addc_u32 s43, s43, 0\n" \
"ds_read_b128 a[72:75], v205 offset:0\n" \
"v_mfma_f32_16x16x32_bf16 v[52:55], a[204:207], a[84:87], v[52:55]\n" \
"ds_read_b128 a[76:79], v206 offset:0\n" \
"v_mfma_f32_16x16x32_bf16 v[92:95], a[80:83], a[216:219], v[92:95]\n" \
"ds_read_b64_tr_b16 a[108:109], v209 offset:12288\n" \
"v_mfma_f32_16x16x32_bf16 v[92:95], a[84:87], a[220:223], v[92:95]\n" \
"global_load_lds_dwordx4 v252, s[44:45] offset:1024\n" \
"v_mfma_f32_16x16x32_bf16 v[88:91], a[80:83], a[208:211], v[88:91]\n" \
"ds_read_b64_tr_b16 a[110:111], v209 offset:13312\n" \
"v_mfma_f32_16x16x32_bf16 v[88:91], a[84:87], a[212:215], v[88:91]\n" \
"ds_read_b64_tr_b16 a[112:113], v210 offset:0\n" \
"v_mfma_f32_16x16x32_bf16 v[96:99], a[88:91], a[208:211], v[96:99]\n" \
"ds_read_b128 a[80:83], v205 offset:4096\n" \
"v_mfma_f32_16x16x32_bf16 v[96:99], a[92:95], a[212:215], v[96:99]\n" \
"global_load_lds_dwordx4 v252, s[44:45] offset:3072\n" \
"v_mfma_f32_16x16x32_bf16 v[100:103], a[88:91], a[216:219], v[100:103]\n" \
"s_add_u32 s44, s44, 0x100\n" \
"s_addc_u32 s45, s45, 0\n" \
"ds_read_b128 a[84:87], v206 offset:4096\n" \
"v_mfma_f32_16x16x32_bf16 v[100:103], a[92:95], a[220:223], v[100:103]\n" \
"ds_read_b64_tr_b16 a[114:115], v210 offset:1024\n" \
"v_mfma_f32_16x16x32_bf16 v[60:63], a[200:203], a[88:91], v[60:63]\n" \
"s_add_u32 m0, s85, 16384\n" \
"ds_read_b64_tr_b16 a[116:117], v210 offset:12288\n" \
"v_mfma_f32_16x16x32_bf16 v[60:63], a[204:207], a[92:95], v[60:63]\n" \
"buffer_load_dwordx4 v246, s[12:15], s46 offen lds\n" \
"v_mfma_f32_16x16x32_bf16 v[68:71], a[200:203], a[192:195], v[68:71]\n" \
"ds_read_b128 a[88:91], v205 offset:8192\n" \
"s_add_u32 s46, s46, 0x100\n" \
"v_mfma_f32_16x16x32_bf16 v[68:71], a[204:207], a[196:199], v[68:71]\n" \
"ds_read_b128 a[92:95], v206 offset:8192\n" \
"v_mfma_f32_16x16x32_bf16 v[108:111], a[192:195], a[216:219], v[108:111]\n" \
"s_add_u32 m0, s84, 24576\n" \
"ds_read_b64_tr_b16 a[118:119], v210 offset:13312\n" \
"v_mfma_f32_16x16x32_bf16 v[108:111], a[196:199], a[220:223], v[108:111]\n" \
"global_load_lds_dwordx4 v253, s[48:49] offset:-4096\n" \
"v_mfma_f32_16x16x32_bf16 v[104:107], a[192:195], a[208:211], v[104:107]\n" \
"v_xor_b32 v226, v226, v227\n" \
"v_cmp_ne_u32 vcc, s60, v226\n" \
"v_mfma_f32_16x16x32_bf16 v[104:107], a[196:199], a[212:215], v[104:107]\n" \
"s_waitcnt lgkmcnt(2)\n" \
"v_mfma_f32_16x16x32_bf16 v[32:35], a[96:99], a[64:67], v[32:35]\n" \
"ds_read_b128 a[192:195], v205 offset:12288\n" \
"v_mfma_f32_16x16x32_bf16 v[32:35], a[100:103], a[68:71], v[32:35]\n" \
"global_load_lds_dwordx4 v253, s[48:49] offset:-2048\n" \
"v_mfma_f32_16x16x32_bf16 v[40:43], a[96:99], a[72:75], v[40:43]\n" \
"ds_read_b128 a[196:199], v206 offset:12288\n" \
"v_mfma_f32_16x16x32_bf16 v[40:43], a[100:103], a[76:79], v[40:43]\n" \
"ds_read_b64_tr_b16 a[208:209], v209 offset:4096\n" \
"v_mfma_f32_16x16x32_bf16 v[0:3], a[104:107], a[72:75], v[0:3]\n" \
"ds_read_b64_tr_b16 a[210:211], v209 offset:5120\n" \
"v_mfma_f32_16x16x32_bf16 v[0:3], a[108:111], a[76:79], v[0:3]\n" \
"s_waitcnt lgkmcnt(3)\n" \
"global_load_lds_dwordx4 v253, s[48:49] offset:0\n" \
"v_mfma_f32_16x16x32_bf16 v[4:7], a[112:115], a[72:75], v[4:7]\n" \
"s_add_u32 s48, s48, 0xc0000\n" \
"s_addc_u32 s49, s49, 0\n" \
"ds_read_b64_tr_b16 a[212:213], v209 offset:16384\n" \
"v_mfma_f32_16x16x32_bf16 v[4:7], a[116:119], a[76:79], v[4:7]\n" \
"ds_read_b64_tr_b16 a[214:215], v209 offset:17408\n" \
"v_mfma_f32_16x16x32_bf16 v[12:15], a[112:115], a[80:83], v[12:15]\n" \
"ds_read_b64_tr_b16 a[216:217], v210 offset:4096\n" \
"v_mfma_f32_16x16x32_bf16 v[12:15], a[116:119], a[84:87], v[12:15]\n" \
"global_load_lds_dwordx4 v253, s[50:51] offset:-3072\n" \
"v_mfma_f32_16x16x32_bf16 v[8:11], a[104:107], a[80:83], v[8:11]\n" \
"ds_read_b64_tr_b16 a[218:219], v210 offset:5120\n" \
"v_mfma_f32_16x16x32_bf16 v[8:11], a[108:111], a[84:87], v[8:11]\n" \
"ds_read_b64_tr_b16 a[220:221], v210 offset:16384\n" \
"v_mfma_f32_16x16x32_bf16 v[48:51], a[96:99], a[80:83], v[48:51]\n" \
"ds_read_b64_tr_b16 a[222:223], v210 offset:17408\n" \
"v_mfma_f32_16x16x32_bf16 v[48:51], a[100:103], a[84:87], v[48:51]\n" \
"global_load_lds_dwordx4 v253, s[50:51] offset:-1024\n" \
"v_mfma_f32_16x16x32_bf16 v[56:59], a[96:99], a[88:91], v[56:59]\n" \
"ds_read_b64_tr_b16 a[200:201], v210 offset:2048\n" \
"v_mfma_f32_16x16x32_bf16 v[56:59], a[100:103], a[92:95], v[56:59]\n" \
"ds_read_b64_tr_b16 a[202:203], v210 offset:3072\n" \
"v_mfma_f32_16x16x32_bf16 v[16:19], a[104:107], a[88:91], v[16:19]\n" \
"ds_read_b64_tr_b16 a[204:205], v210 offset:14336\n" \
"v_mfma_f32_16x16x32_bf16 v[16:19], a[108:111], a[92:95], v[16:19]\n" \
"global_load_lds_dwordx4 v253, s[50:51] offset:1024\n" \
"v_mfma_f32_16x16x32_bf16 v[20:23], a[112:115], a[88:91], v[20:23]\n" \
"s_add_u32 s50, s50, 0xc0000\n" \
"s_addc_u32 s51, s51, 0\n" \
"ds_read_b64_tr_b16 a[206:207], v210 offset:15360\n" \
"v_mfma_f32_16x16x32_bf16 v[20:23], a[116:119], a[92:95], v[20:23]\n" \
"s_waitcnt lgkmcnt(3)\n" \
"v_add_u32 v207, 0xffff3000, v207\n" \
"v_add_u32 v208, 0xffff3000, v208\n" \
"v_mfma_f32_16x16x32_bf16 v[28:31], a[112:115], a[192:195], v[28:31]\n" \
"v_add_u32 v205, 0xffff3000, v205\n" \
"v_add_u32 v206, 0xffff3000, v206\n" \
"v_mfma_f32_16x16x32_bf16 v[28:31], a[116:119], a[196:199], v[28:31]\n" \
"v_add_u32 v209, 0xffff3000, v209\n" \
"v_add_u32 v210, 0xffff3000, v210\n" \
"v_mfma_f32_16x16x32_bf16 v[24:27], a[104:107], a[192:195], v[24:27]\n" \
"v_mfma_f32_16x16x32_bf16 v[24:27], a[108:111], a[196:199], v[24:27]\n" \
"s_and_b32 vcc_hi, vcc_lo, -1\n" \
"s_cbranch_scc1 Lpl3_%=\n" \
"Lpr3_%=:\n" \
"v_mfma_f32_16x16x32_bf16 v[64:67], a[96:99], a[192:195], v[64:67]\n" \
"s_waitcnt vmcnt(11) lgkmcnt(0)\n" \
"v_readfirstlane_b32 s71, v227\n" \
"s_and_b32 s70, %1, 7\n" \
"v_mfma_f32_16x16x32_bf16 v[64:67], a[100:103], a[196:199], v[64:67]\n" \
"s_barrier\n" \
"v_mfma_f32_16x16x32_bf16 v[72:75], a[64:67], a[208:211], v[72:75]\n" \
"s_lshl_b32 s78, s70, 2\n" \
"ds_read_b64_tr_b16 a[96:97], v209 offset:2048\n" \
"v_mfma_f32_16x16x32_bf16 v[72:75], a[68:71], a[212:215], v[72:75]\n" \
"ds_read_b64_tr_b16 a[98:99], v209 offset:3072\n" \
"v_subrev_u32 v227, s71, v227\n" \
"v_mfma_f32_16x16x32_bf16 v[80:83], a[72:75], a[208:211], v[80:83]\n" \
"s_add_u32 m0, s95, 118784\n" \
"ds_read_b128 a[64:67], v207 offset:0\n" \
"ds_read_b64_tr_b16 a[100:101], v209 offset:14336\n" \
"v_mfma_f32_16x16x32_bf16 v[80:83], a[76:79], a[212:215], v[80:83]\n" \
"global_load_lds_dwordx4 v252, s[42:43] offset:0\n" \
"v_add_u32_dpp v227, v227, v227 quad_perm:[1,0,3,2] row_mask:0xf bank_mask:0xf\n" \
"v_mfma_f32_16x16x32_bf16 v[84:87], a[72:75], a[216:219], v[84:87]\n" \
"ds_read_b128 a[68:71], v208 offset:0\n" \
"ds_read_b64_tr_b16 a[102:103], v209 offset:15360\n" \
"v_mfma_f32_16x16x32_bf16 v[84:87], a[76:79], a[220:223], v[84:87]\n" \
"ds_read_b64_tr_b16 a[104:105], v209 offset:0\n" \
"v_add_u32_dpp v227, v227, v227 quad_perm:[2,3,0,1] row_mask:0xf bank_mask:0xf\n" \
"v_mfma_f32_16x16x32_bf16 v[44:47], a[200:203], a[72:75], v[44:47]\n" \
"ds_read_b64_tr_b16 a[106:107], v209 offset:1024\n" \
"v_readlane_b32 s71, v227, s78\n" \
"v_mfma_f32_16x16x32_bf16 v[44:47], a[204:207], a[76:79], v[44:47]\n" \
"global_load_lds_dwordx4 v252, s[42:43] offset:2048\n" \
"v_lshrrev_b32 v226, 6, v248\n" \
"v_mfma_f32_16x16x32_bf16 v[52:55], a[200:203], a[80:83], v[52:55]\n" \
"s_add_u32 s42, s42, 0x80\n" \
"s_addc_u32 s43, s43, 0\n" \
"ds_read_b128 a[72:75], v205 offset:0\n" \
"v_mfma_f32_16x16x32_bf16 v[52:55], a[204:207], a[84:87], v[52:55]\n" \
"ds_read_b128 a[76:79], v206 offset:0\n" \
"v_subrev_u32 v227, s71, v227\n" \
"v_mfma_f32_16x16x32_bf16 v[92:95], a[80:83], a[216:219], v[92:95]\n" \
"ds_read_b64_tr_b16 a[108:109], v209 offset:12288\n" \
"v_lshl_add_u32 v227, v227, 3, v226\n" \
"v_mfma_f32_16x16x32_bf16 v[92:95], a[84:87], a[220:223], v[92:95]\n" \
"global_load_lds_dwordx4 v252, s[44:45] offset:1024\n" \
"v_cmp_gt_i32 vcc, s70, v227\n" \
"v_mfma_f32_16x16x32_bf16 v[88:91], a[80:83], a[208:211], v[88:91]\n" \
"ds_read_b64_tr_b16 a[110:111], v209 offset:13312\n" \
"v_mfma_f32_16x16x32_bf16 v[88:91], a[84:87], a[212:215], v[88:91]\n" \
"s_bcnt1_i32_b32 s70, vcc_lo\n" \
"s_lshr_b32 s70, s70, 2\n" \
"ds_read_b64_tr_b16 a[112:113], v210 offset:0\n" \
"v_mfma_f32_16x16x32_bf16 v[96:99], a[88:91], a[208:211], v[96:99]\n" \
"s_lshr_b32 s78, s70, 2\n" \
"ds_read_b128 a[80:83], v205 offset:4096\n" \
"s_mul_i32 s71, s78, 7\n" \
"v_mfma_f32_16x16x32_bf16 v[96:99], a[92:95], a[212:215], v[96:99]\n" \
"s_xor_b32 s70, s70, s71\n" \
"global_load_lds_dwordx4 v252, s[44:45] offset:3072\n" \
"v_mfma_f32_16x16x32_bf16 v[100:103], a[88:91], a[216:219], v[100:103]\n" \
"s_add_u32 s44, s44, 0x80\n" \
"s_addc_u32 s45, s45, 0\n" \
"ds_read_b128 a[84:87], v206 offset:4096\n" \
"v_mfma_f32_16x16x32_bf16 v[100:103], a[92:95], a[220:223], v[100:103]\n" \
"s_xor_b32 s78, s78, 1\n" \
"s_lshl_b32 s65, s70, 2\n" \
"ds_read_b64_tr_b16 a[114:115], v210 offset:1024\n" \
"v_mfma_f32_16x16x32_bf16 v[60:63], a[200:203], a[88:91], v[60:63]\n" \
"s_add_u32 m0, s85, 135168\n" \
"s_lshr_b32 s64, 0x8654, s65\n" \
"ds_read_b64_tr_b16 a[116:117], v210 offset:12288\n" \
"v_mfma_f32_16x16x32_bf16 v[60:63], a[204:207], a[92:95], v[60:63]\n" \
"buffer_load_dwordx4 v246, s[12:15], s46 offen lds\n" \
"v_mfma_f32_16x16x32_bf16 v[68:71], a[200:203], a[192:195], v[68:71]\n" \
"s_add_u32 s46, s46, 0x80\n" \
"ds_read_b128 a[88:91], v205 offset:8192\n" \
"s_and_b32 s64, s64, 15\n" \
"v_mfma_f32_16x16x32_bf16 v[68:71], a[204:207], a[196:199], v[68:71]\n" \
"s_add_u32 s64, s64, 32\n" \
"ds_read_b128 a[92:95], v206 offset:8192\n" \
"s_lshr_b32 s70, %1, 6\n" \
"v_mfma_f32_16x16x32_bf16 v[108:111], a[192:195], a[216:219], v[108:111]\n" \
"s_add_u32 m0, s84, 143360\n" \
"s_add_u32 s65, s65, s70\n" \
"ds_read_b64_tr_b16 a[118:119], v210 offset:13312\n" \
"v_mfma_f32_16x16x32_bf16 v[108:111], a[196:199], a[220:223], v[108:111]\n" \
"s_and_b32 s30, %1, 1\n" \
"global_load_lds_dwordx4 v253, s[48:49] offset:-4096\n" \
"v_mfma_f32_16x16x32_bf16 v[104:107], a[192:195], a[208:211], v[104:107]\n" \
"s_lshl_b32 s30, s30, 3\n" \
"s_add_u32 s30, s30, s63\n" \
"s_lshl_b32 s30, s30, 2\n" \
"v_mfma_f32_16x16x32_bf16 v[104:107], a[196:199], a[212:215], v[104:107]\n" \
"s_waitcnt lgkmcnt(2)\n" \
"s_lshl_b32 s70, s88, 7\n" \
"s_add_u32 s30, s30, s70\n" \
"v_mfma_f32_16x16x32_bf16 v[32:35], a[96:99], a[64:67], v[32:35]\n" \
"s_add_u32 s30, s30, 65591296\n" \
"ds_read_b128 a[192:195], v205 offset:12288\n" \
"v_lshrrev_b32 v221, 2, v248\n" \
"v_mfma_f32_16x16x32_bf16 v[32:35], a[100:103], a[68:71], v[32:35]\n" \
"s_lshl_b32 s81, s65, 7\n" \
"global_load_lds_dwordx4 v253, s[48:49] offset:-2048\n" \
"v_mfma_f32_16x16x32_bf16 v[40:43], a[96:99], a[72:75], v[40:43]\n" \
"s_add_u32 s82, s81, 65591296\n" \
"ds_read_b128 a[196:199], v206 offset:12288\n" \
"s_add_u32 s81, s81, 65589248\n" \
"v_mfma_f32_16x16x32_bf16 v[40:43], a[100:103], a[76:79], v[40:43]\n" \
"s_mov_b64 s[52:53], s[26:27]\n" \
"s_and_b32 s53, s53, 0xffff\n" \
"ds_read_b64_tr_b16 a[208:209], v209 offset:4096\n" \
"v_mfma_f32_16x16x32_bf16 v[0:3], a[104:107], a[72:75], v[0:3]\n" \
"s_mov_b32 s54, 8388608\n" \
"s_mov_b32 s55, 0x20000\n" \
"ds_read_b64_tr_b16 a[210:211], v209 offset:5120\n" \
"v_mfma_f32_16x16x32_bf16 v[0:3], a[108:111], a[76:79], v[0:3]\n" \
"s_waitcnt lgkmcnt(3)\n" \
"global_load_lds_dwordx4 v253, s[48:49] offset:0\n" \
"s_lshr_b32 s83, s63, 1\n" \
"v_mfma_f32_16x16x32_bf16 v[4:7], a[112:115], a[72:75], v[4:7]\n" \
"s_add_u32 s48, s48, 0x60000\n" \
"s_addc_u32 s49, s49, 0\n" \
"ds_read_b64_tr_b16 a[212:213], v209 offset:16384\n" \
"v_mfma_f32_16x16x32_bf16 v[4:7], a[116:119], a[76:79], v[4:7]\n" \
"s_add_u32 s83, s83, 2\n" \
"s_lshr_b32 s70, s18, 8\n" \
"ds_read_b64_tr_b16 a[214:215], v209 offset:17408\n" \
"v_mfma_f32_16x16x32_bf16 v[12:15], a[112:115], a[80:83], v[12:15]\n" \
"s_sub_u32 s83, s83, s70\n" \
"s_and_b32 s83, s83, 3\n" \
"ds_read_b64_tr_b16 a[216:217], v210 offset:4096\n" \
"v_mfma_f32_16x16x32_bf16 v[12:15], a[116:119], a[84:87], v[12:15]\n" \
"s_and_b32 s70, s63, 1\n" \
"global_load_lds_dwordx4 v253, s[50:51] offset:-3072\n" \
"v_mfma_f32_16x16x32_bf16 v[8:11], a[104:107], a[80:83], v[8:11]\n" \
"s_lshl_b32 s70, s70, 2\n" \
"s_or_b32 s83, s83, s70\n" \
"ds_read_b64_tr_b16 a[218:219], v210 offset:5120\n" \
"v_mfma_f32_16x16x32_bf16 v[8:11], a[108:111], a[84:87], v[8:11]\n" \
"s_mul_i32 s75, s78, s64\n" \
"ds_read_b64_tr_b16 a[220:221], v210 offset:16384\n" \
"v_lshrrev_b32 v228, 4, v248\n" \
"v_mfma_f32_16x16x32_bf16 v[48:51], a[96:99], a[80:83], v[48:51]\n" \
"s_lshl_b32 s70, s65, 2\n" \
"s_add_u32 s70, s70, s61\n" \
"ds_read_b64_tr_b16 a[222:223], v210 offset:17408\n" \
"v_mfma_f32_16x16x32_bf16 v[48:51], a[100:103], a[84:87], v[48:51]\n" \
"s_mul_i32 s73, s70, 69632\n" \
"global_load_lds_dwordx4 v253, s[50:51] offset:-1024\n" \
"v_mfma_f32_16x16x32_bf16 v[56:59], a[96:99], a[88:91], v[56:59]\n" \
"s_add_u32 s73, s73, 52740096\n" \
"s_add_u32 s73, s73, s28\n" \
"ds_read_b64_tr_b16 a[200:201], v210 offset:2048\n" \
"v_mfma_f32_16x16x32_bf16 v[56:59], a[100:103], a[92:95], v[56:59]\n" \
"s_mul_i32 s70, s70, 266240\n" \
"s_lshl_b32 s71, s75, 12\n" \
"ds_read_b64_tr_b16 a[202:203], v210 offset:3072\n" \
"v_mfma_f32_16x16x32_bf16 v[16:19], a[104:107], a[88:91], v[16:19]\n" \
"s_add_u32 s70, s70, s71\n" \
"s_add_u32 s70, s70, 35700736\n" \
"ds_read_b64_tr_b16 a[204:205], v210 offset:14336\n" \
"v_mfma_f32_16x16x32_bf16 v[16:19], a[108:111], a[92:95], v[16:19]\n" \
"s_add_u32 s36, s28, s70\n" \
"global_load_lds_dwordx4 v253, s[50:51] offset:1024\n" \
"v_mfma_f32_16x16x32_bf16 v[20:23], a[112:115], a[88:91], v[20:23]\n" \
"s_add_u32 s50, s50, 0x60000\n" \
"s_addc_u32 s51, s51, 0\n" \
"ds_read_b64_tr_b16 a[206:207], v210 offset:15360\n" \
"v_mfma_f32_16x16x32_bf16 v[20:23], a[116:119], a[92:95], v[20:23]\n" \
"s_waitcnt lgkmcnt(3)\n" \
"v_add_u32 v207, 0xffff0000, v207\n" \
"v_add_u32 v208, 0xffff0000, v208\n" \
"v_mfma_f32_16x16x32_bf16 v[28:31], a[112:115], a[192:195], v[28:31]\n" \
"s_mov_b32 s37, s29\n" \
"v_add_u32 v205, 0xffff0000, v205\n" \
"v_add_u32 v206, 0xffff0000, v206\n" \
"v_mfma_f32_16x16x32_bf16 v[28:31], a[116:119], a[196:199], v[28:31]\n" \
"s_lshr_b32 s74, s61, 1\n" \
"v_add_u32 v209, 0xffff0000, v209\n" \
"v_add_u32 v210, 0xffff0000, v210\n" \
"v_mfma_f32_16x16x32_bf16 v[24:27], a[104:107], a[192:195], v[24:27]\n" \
"s_lshl_b32 s74, s74, 16\n" \
"s_and_b32 s70, s61, 1\n" \
"s_lshl_b32 s70, s70, 14\n" \
"v_mfma_f32_16x16x32_bf16 v[24:27], a[108:111], a[196:199], v[24:27]\n" \
"s_add_u32 s74, s74, s70\n" \
"s_lshl_b32 s71, s83, 8\n" \
"s_add_u32 s74, s74, s71\n" \
"v_mfma_f32_16x16x32_bf16 v[64:67], a[96:99], a[192:195], v[64:67]\n" \
"s_waitcnt vmcnt(11) lgkmcnt(0)\n" \
"s_add_u32 s40, s24, s74\n" \
"s_addc_u32 s41, s25, 0\n" \
"v_mfma_f32_16x16x32_bf16 v[64:67], a[100:103], a[196:199], v[64:67]\n" \
"s_barrier\n" \
"v_mfma_f32_16x16x32_bf16 v[72:75], a[64:67], a[208:211], v[72:75]\n" \
"s_lshl_b32 s70, s75, 17\n" \
"s_add_u32 s70, s70, s74\n" \
"ds_read_b64_tr_b16 a[96:97], v209 offset:2048\n" \
"v_mfma_f32_16x16x32_bf16 v[72:75], a[68:71], a[212:215], v[72:75]\n" \
"s_add_u32 s38, s18, s70\n" \
"s_addc_u32 s39, s19, 0\n" \
"ds_read_b64_tr_b16 a[98:99], v209 offset:3072\n" \
"v_mfma_f32_16x16x32_bf16 v[80:83], a[72:75], a[208:211], v[80:83]\n" \
"s_add_u32 m0, s95, 65536\n" \
"ds_read_b128 a[64:67], v207 offset:0\n" \
"ds_read_b64_tr_b16 a[100:101], v209 offset:14336\n" \
"v_mfma_f32_16x16x32_bf16 v[80:83], a[76:79], a[212:215], v[80:83]\n" \
"global_load_lds_dwordx4 v252, s[42:43] offset:0\n" \
"v_bfe_u32 v229, v228, 2, 3\n" \
"v_mfma_f32_16x16x32_bf16 v[84:87], a[72:75], a[216:219], v[84:87]\n" \
"ds_read_b128 a[68:71], v208 offset:0\n" \
"ds_read_b64_tr_b16 a[102:103], v209 offset:15360\n" \
"v_mfma_f32_16x16x32_bf16 v[84:87], a[76:79], a[220:223], v[84:87]\n" \
"ds_read_b64_tr_b16 a[104:105], v209 offset:0\n" \
"v_lshlrev_b32 v229, 11, v229\n" \
"v_mfma_f32_16x16x32_bf16 v[44:47], a[200:203], a[72:75], v[44:47]\n" \
"ds_read_b64_tr_b16 a[106:107], v209 offset:1024\n" \
"v_lshrrev_b32 v230, 5, v228\n" \
"v_mfma_f32_16x16x32_bf16 v[44:47], a[204:207], a[76:79], v[44:47]\n" \
"global_load_lds_dwordx4 v252, s[42:43] offset:2048\n" \
"v_lshl_add_u32 v229, v230, 6, v229\n" \
"v_mfma_f32_16x16x32_bf16 v[52:55], a[200:203], a[80:83], v[52:55]\n" \
"s_add_u32 s42, s42, 0x80\n" \
"s_addc_u32 s43, s43, 0\n" \
"ds_read_b128 a[72:75], v205 offset:0\n" \
"v_mfma_f32_16x16x32_bf16 v[52:55], a[204:207], a[84:87], v[52:55]\n" \
"ds_read_b128 a[76:79], v206 offset:0\n" \
"v_and_b32 v230, 3, v228\n" \
"v_mfma_f32_16x16x32_bf16 v[92:95], a[80:83], a[216:219], v[92:95]\n" \
"ds_read_b64_tr_b16 a[108:109], v209 offset:12288\n" \
"v_bfe_u32 v231, v228, 4, 1\n" \
"v_mfma_f32_16x16x32_bf16 v[92:95], a[84:87], a[220:223], v[92:95]\n" \
"global_load_lds_dwordx4 v252, s[44:45] offset:1024\n" \
"v_lshlrev_b32 v231, 1, v231\n" \
"v_mfma_f32_16x16x32_bf16 v[88:91], a[80:83], a[208:211], v[88:91]\n" \
"ds_read_b64_tr_b16 a[110:111], v209 offset:13312\n" \
"v_xor_b32 v230, v230, v231\n" \
"v_mfma_f32_16x16x32_bf16 v[88:91], a[84:87], a[212:215], v[88:91]\n" \
"ds_read_b64_tr_b16 a[112:113], v210 offset:0\n" \
"v_lshl_add_u32 v229, v230, 4, v229\n" \
"v_mfma_f32_16x16x32_bf16 v[96:99], a[88:91], a[208:211], v[96:99]\n" \
"ds_read_b128 a[80:83], v205 offset:4096\n" \
"v_add_u32 v216, 4096, v229\n" \
"v_mfma_f32_16x16x32_bf16 v[96:99], a[92:95], a[212:215], v[96:99]\n" \
"global_load_lds_dwordx4 v252, s[44:45] offset:3072\n" \
"v_add_u32 v217, 3200, v229\n" \
"v_mfma_f32_16x16x32_bf16 v[100:103], a[88:91], a[216:219], v[100:103]\n" \
"s_add_u32 s44, s44, 0x80\n" \
"s_addc_u32 s45, s45, 0\n" \
"ds_read_b128 a[84:87], v206 offset:4096\n" \
"v_mfma_f32_16x16x32_bf16 v[100:103], a[92:95], a[220:223], v[100:103]\n" \
"ds_read_b64_tr_b16 a[114:115], v210 offset:1024\n" \
"v_add_u32 v218, 34816, v229\n" \
"v_mfma_f32_16x16x32_bf16 v[60:63], a[200:203], a[88:91], v[60:63]\n" \
"s_add_u32 m0, s85, 81920\n" \
"ds_read_b64_tr_b16 a[116:117], v210 offset:12288\n" \
"v_add_u32 v219, 33920, v229\n" \
"v_mfma_f32_16x16x32_bf16 v[60:63], a[204:207], a[92:95], v[60:63]\n" \
"buffer_load_dwordx4 v246, s[12:15], s46 offen lds\n" \
"v_mfma_f32_16x16x32_bf16 v[68:71], a[200:203], a[192:195], v[68:71]\n" \
"s_add_u32 s46, s46, 0x80\n" \
"ds_read_b128 a[88:91], v205 offset:8192\n" \
"s_mov_b32 s75, 65536\n" \
"v_mfma_f32_16x16x32_bf16 v[68:71], a[204:207], a[196:199], v[68:71]\n" \
"s_lshr_b32 s70, s61, 1\n" \
"ds_read_b128 a[92:95], v206 offset:8192\n" \
"s_lshl_b32 s71, s70, 13\n" \
"v_mfma_f32_16x16x32_bf16 v[108:111], a[192:195], a[216:219], v[108:111]\n" \
"s_add_u32 m0, s84, 90112\n" \
"Lpg10302_%=:\n" \
"s_cbranch_execz Lpg12415_%=\n" \
"ds_read_b64_tr_b16 a[118:119], v210 offset:13312\n" \
"v_add_u32 v211, s71, v249\n" \
"v_mfma_f32_16x16x32_bf16 v[108:111], a[196:199], a[220:223], v[108:111]\n" \
"s_lshl_b32 s71, s65, 1\n" \
"global_load_lds_dwordx4 v253, s[48:49] offset:-4096\n" \
"v_mfma_f32_16x16x32_bf16 v[104:107], a[192:195], a[208:211], v[104:107]\n" \
"s_add_u32 s70, s70, s71\n" \
"s_lshl_b32 s77, s70, 18\n" \
"s_lshl_b32 s71, s83, 8\n" \
"v_mfma_f32_16x16x32_bf16 v[104:107], a[196:199], a[212:215], v[104:107]\n" \
"s_waitcnt lgkmcnt(2)\n" \
"s_add_u32 s77, s77, s71\n" \
"s_lshl_b32 s71, s78, 11\n" \
"v_mfma_f32_16x16x32_bf16 v[32:35], a[96:99], a[64:67], v[32:35]\n" \
"s_add_u32 s77, s77, s71\n" \
"ds_read_b128 a[192:195], v205 offset:12288\n" \
"s_and_b32 s70, s61, 1\n" \
"v_mfma_f32_16x16x32_bf16 v[32:35], a[100:103], a[68:71], v[32:35]\n" \
"s_lshl_b32 s71, s70, 7\n" \
"global_load_lds_dwordx4 v253, s[48:49] offset:-2048\n" \
"v_mfma_f32_16x16x32_bf16 v[40:43], a[96:99], a[72:75], v[40:43]\n" \
"s_add_u32 s77, s77, s71\n" \
"ds_read_b128 a[196:199], v206 offset:12288\n" \
"v_and_b32 v229, 3, v228\n" \
"v_mfma_f32_16x16x32_bf16 v[40:43], a[100:103], a[76:79], v[40:43]\n" \
"ds_read_b64_tr_b16 a[208:209], v209 offset:4096\n" \
"v_lshlrev_b32 v229, 3, v229\n" \
"v_mfma_f32_16x16x32_bf16 v[0:3], a[104:107], a[72:75], v[0:3]\n" \
"ds_read_b64_tr_b16 a[210:211], v209 offset:5120\n" \
"v_bfe_u32 v230, v228, 2, 2\n" \
"v_mfma_f32_16x16x32_bf16 v[0:3], a[108:111], a[76:79], v[0:3]\n" \
"s_waitcnt lgkmcnt(3)\n" \
"global_load_lds_dwordx4 v253, s[48:49] offset:0\n" \
"v_lshl_add_u32 v229, v230, 6, v229\n" \
"v_mfma_f32_16x16x32_bf16 v[4:7], a[112:115], a[72:75], v[4:7]\n" \
"s_add_u32 s48, s48, 0x60000\n" \
"s_addc_u32 s49, s49, 0\n" \
"ds_read_b64_tr_b16 a[212:213], v209 offset:16384\n" \
"v_mfma_f32_16x16x32_bf16 v[4:7], a[116:119], a[76:79], v[4:7]\n" \
"ds_read_b64_tr_b16 a[214:215], v209 offset:17408\n" \
"v_bfe_u32 v230, v228, 4, 1\n" \
"v_mfma_f32_16x16x32_bf16 v[12:15], a[112:115], a[80:83], v[12:15]\n" \
"ds_read_b64_tr_b16 a[216:217], v210 offset:4096\n" \
"v_lshl_add_u32 v229, v230, 5, v229\n" \
"v_mfma_f32_16x16x32_bf16 v[12:15], a[116:119], a[84:87], v[12:15]\n" \
"global_load_lds_dwordx4 v253, s[50:51] offset:-3072\n" \
"v_lshl_add_u32 v229, v230, 8, v229\n" \
"v_mfma_f32_16x16x32_bf16 v[8:11], a[104:107], a[80:83], v[8:11]\n" \
"ds_read_b64_tr_b16 a[218:219], v210 offset:5120\n" \
"v_lshrrev_b32 v230, 5, v228\n" \
"v_mfma_f32_16x16x32_bf16 v[8:11], a[108:111], a[84:87], v[8:11]\n" \
"ds_read_b64_tr_b16 a[220:221], v210 offset:16384\n" \
"v_lshl_add_u32 v229, v230, 12, v229\n" \
"v_mfma_f32_16x16x32_bf16 v[48:51], a[96:99], a[80:83], v[48:51]\n" \
"s_lshl_b32 s71, s70, 10\n" \
"ds_read_b64_tr_b16 a[222:223], v210 offset:17408\n" \
"v_add_u32 v212, s71, v229\n" \
"v_mfma_f32_16x16x32_bf16 v[48:51], a[100:103], a[84:87], v[48:51]\n" \
"s_lshl_b32 s70, s65, 3\n" \
"global_load_lds_dwordx4 v253, s[50:51] offset:-1024\n" \
"v_mfma_f32_16x16x32_bf16 v[56:59], a[96:99], a[88:91], v[56:59]\n" \
"s_add_u32 s70, s70, s83\n" \
"s_lshl_b32 s76, s70, 16\n" \
"ds_read_b64_tr_b16 a[200:201], v210 offset:2048\n" \
"v_mfma_f32_16x16x32_bf16 v[56:59], a[100:103], a[92:95], v[56:59]\n" \
"s_lshl_b32 s71, s61, 14\n" \
"s_add_u32 s76, s76, s71\n" \
"ds_read_b64_tr_b16 a[202:203], v210 offset:3072\n" \
"v_mfma_f32_16x16x32_bf16 v[16:19], a[104:107], a[88:91], v[16:19]\n" \
"s_add_u32 s76, s76, 57196544\n" \
"s_lshl_b32 s79, s70, 7\n" \
"ds_read_b64_tr_b16 a[204:205], v210 offset:14336\n" \
"v_mfma_f32_16x16x32_bf16 v[16:19], a[108:111], a[92:95], v[16:19]\n" \
"s_add_u32 s79, s79, 65601536\n" \
"global_load_lds_dwordx4 v253, s[50:51] offset:1024\n" \
"v_mfma_f32_16x16x32_bf16 v[20:23], a[112:115], a[88:91], v[20:23]\n" \
"s_add_u32 s50, s50, 0x60000\n" \
"s_addc_u32 s51, s51, 0\n" \
"ds_read_b64_tr_b16 a[206:207], v210 offset:15360\n" \
"v_mfma_f32_16x16x32_bf16 v[20:23], a[116:119], a[92:95], v[20:23]\n" \
"s_waitcnt lgkmcnt(3)\n" \
"v_add_u32 v207, 0x1d000, v207\n" \
"v_add_u32 v208, 0x1d000, v208\n" \
"v_mfma_f32_16x16x32_bf16 v[28:31], a[112:115], a[192:195], v[28:31]\n" \
"s_lshr_b32 s70, s64, 1\n" \
"v_add_u32 v205, 0x1d000, v205\n" \
"v_add_u32 v206, 0x1d000, v206\n" \
"v_mfma_f32_16x16x32_bf16 v[28:31], a[116:119], a[196:199], v[28:31]\n" \
"s_lshl_b32 s70, -1, s70\n" \
"v_add_u32 v209, 0x1d000, v209\n" \
"v_add_u32 v210, 0x1d000, v210\n" \
"v_mfma_f32_16x16x32_bf16 v[24:27], a[104:107], a[192:195], v[24:27]\n" \
"s_add_u32 s71, s64, 1\n" \
"s_lshr_b32 s71, s71, 1\n" \
"s_bfm_b32 s71, s71, 0\n" \
"v_mfma_f32_16x16x32_bf16 v[24:27], a[108:111], a[196:199], v[24:27]\n" \
"s_cmp_eq_u32 s78, 0\n" \
"s_cselect_b32 s80, s71, s70\n" \
"s_mov_b32 s16, 0xff000000\n" \
"v_mfma_f32_16x16x32_bf16 v[64:67], a[96:99], a[192:195], v[64:67]\n" \
"s_waitcnt vmcnt(11) lgkmcnt(0)\n" \
"s_mov_b32 s17, 0\n" \
"v_add_u32 v222, 0x1c90080, v250\n" \
"v_mfma_f32_16x16x32_bf16 v[64:67], a[100:103], a[196:199], v[64:67]\n" \
"s_barrier\n" \
"v_mfma_f32_16x16x32_bf16 v[72:75], a[64:67], a[208:211], v[72:75]\n" \
"ds_read_b64_tr_b16 a[96:97], v209 offset:2048\n" \
"v_cndmask_b32 v222, v250, v222, s[16:17]\n" \
"v_mfma_f32_16x16x32_bf16 v[72:75], a[68:71], a[212:215], v[72:75]\n" \
"s_mov_b32 s74, 98304\n" \
"ds_read_b64_tr_b16 a[98:99], v209 offset:3072\n" \
"v_mfma_f32_16x16x32_bf16 v[80:83], a[72:75], a[208:211], v[80:83]\n" \
"s_add_u32 m0, s95, 0\n" \
"ds_read_b128 a[64:67], v207 offset:0\n" \
"ds_read_b64_tr_b16 a[100:101], v209 offset:14336\n" \
"v_mfma_f32_16x16x32_bf16 v[80:83], a[76:79], a[212:215], v[80:83]\n" \
"global_load_lds_dwordx4 v252, s[42:43] offset:0\n" \
"v_mfma_f32_16x16x32_bf16 v[84:87], a[72:75], a[216:219], v[84:87]\n" \
"ds_read_b128 a[68:71], v208 offset:0\n" \
"ds_read_b64_tr_b16 a[102:103], v209 offset:15360\n" \
"v_mfma_f32_16x16x32_bf16 v[84:87], a[76:79], a[220:223], v[84:87]\n" \
"s_sub_u32 s74, s74, 32768\n" \
"s_cselect_b32 s74, 131072, s74\n" \
"ds_read_b64_tr_b16 a[104:105], v209 offset:0\n" \
"v_mfma_f32_16x16x32_bf16 v[44:47], a[200:203], a[72:75], v[44:47]\n" \
"ds_read_b64_tr_b16 a[106:107], v209 offset:1024\n" \
"v_add_u32 v213, s74, v211\n" \
"v_mfma_f32_16x16x32_bf16 v[44:47], a[204:207], a[76:79], v[44:47]\n" \
"global_load_lds_dwordx4 v252, s[42:43] offset:2048\n" \
"v_add_u32 v214, s74, v212\n" \
"v_mfma_f32_16x16x32_bf16 v[52:55], a[200:203], a[80:83], v[52:55]\n" \
"s_add_u32 s42, s42, 0x80\n" \
"s_addc_u32 s43, s43, 0\n" \
"ds_read_b128 a[72:75], v205 offset:0\n" \
"v_mfma_f32_16x16x32_bf16 v[52:55], a[204:207], a[84:87], v[52:55]\n" \
"ds_read_b128 a[76:79], v206 offset:0\n" \
"v_xor_b32 v215, 32, v214\n" \
"v_mfma_f32_16x16x32_bf16 v[92:95], a[80:83], a[216:219], v[92:95]\n" \
"s_and_b32 s89, s61, 1\n" \
"s_lshl_b32 s89, s89, 11\n" \
"ds_read_b64_tr_b16 a[108:109], v209 offset:12288\n" \
"v_mfma_f32_16x16x32_bf16 v[92:95], a[84:87], a[220:223], v[92:95]\n" \
"s_add_u32 s87, s87, s89\n" \
"global_load_lds_dwordx4 v252, s[44:45] offset:1024\n" \
"v_mfma_f32_16x16x32_bf16 v[88:91], a[80:83], a[208:211], v[88:91]\n" \
"s_add_u32 s87, s87, 52740096\n" \
"ds_read_b64_tr_b16 a[110:111], v209 offset:13312\n" \
"v_mfma_f32_16x16x32_bf16 v[88:91], a[84:87], a[212:215], v[88:91]\n" \
"s_cmp_eq_u32 s88, 0\n" \
"s_cselect_b32 s86, -1, 0\n" \
"ds_read_b64_tr_b16 a[112:113], v210 offset:0\n" \
"v_mfma_f32_16x16x32_bf16 v[96:99], a[88:91], a[208:211], v[96:99]\n" \
"s_mul_i32 s89, s88, 278528\n" \
"ds_read_b128 a[80:83], v205 offset:4096\n" \
"s_add_u32 s87, s87, s89\n" \
"v_mfma_f32_16x16x32_bf16 v[96:99], a[92:95], a[212:215], v[96:99]\n" \
"s_lshr_b32 s89, s61, 1\n" \
"global_load_lds_dwordx4 v252, s[44:45] offset:3072\n" \
"v_mfma_f32_16x16x32_bf16 v[100:103], a[88:91], a[216:219], v[100:103]\n" \
"s_add_u32 s44, s44, 0x80\n" \
"s_addc_u32 s45, s45, 0\n" \
"ds_read_b128 a[84:87], v206 offset:4096\n" \
"v_mfma_f32_16x16x32_bf16 v[100:103], a[92:95], a[220:223], v[100:103]\n" \
"s_lshl_b32 s89, s89, 8\n" \
"s_add_u32 s87, s87, s89\n" \
"ds_read_b64_tr_b16 a[114:115], v210 offset:1024\n" \
"v_mfma_f32_16x16x32_bf16 v[60:63], a[200:203], a[88:91], v[60:63]\n" \
"s_add_u32 m0, s85, 16384\n" \
"s_lshl_b32 s89, s61, 4\n" \
"ds_read_b64_tr_b16 a[116:117], v210 offset:12288\n" \
"v_mfma_f32_16x16x32_bf16 v[60:63], a[204:207], a[92:95], v[60:63]\n" \
"buffer_load_dwordx4 v246, s[12:15], s46 offen lds\n" \
"v_mfma_f32_16x16x32_bf16 v[68:71], a[200:203], a[192:195], v[68:71]\n" \
"s_add_u32 s46, s46, 0x80\n" \
"ds_read_b128 a[88:91], v205 offset:8192\n" \
"v_subrev_u32 v147, s89, v251\n" \
"v_mfma_f32_16x16x32_bf16 v[68:71], a[204:207], a[196:199], v[68:71]\n" \
"s_lshr_b32 s89, s61, 1\n" \
"ds_read_b128 a[92:95], v206 offset:8192\n" \
"s_lshl_b32 s90, s89, 9\n" \
"v_mfma_f32_16x16x32_bf16 v[108:111], a[192:195], a[216:219], v[108:111]\n" \
"s_add_u32 m0, s84, 24576\n" \
"s_add_u32 s90, s90, 511\n" \
"ds_read_b64_tr_b16 a[118:119], v210 offset:13312\n" \
"v_mfma_f32_16x16x32_bf16 v[108:111], a[196:199], a[220:223], v[108:111]\n" \
"global_load_lds_dwordx4 v253, s[48:49] offset:-4096\n" \
"v_sub_u32 v145, s90, v248\n" \
"v_mfma_f32_16x16x32_bf16 v[104:107], a[192:195], a[208:211], v[104:107]\n" \
"s_lshl_b32 s90, s89, 3\n" \
"s_not_b32 s90, s90\n" \
"v_sub_u32 v146, s90, v147\n" \
"v_mfma_f32_16x16x32_bf16 v[104:107], a[196:199], a[212:215], v[104:107]\n" \
"s_waitcnt lgkmcnt(2)\n" \
"v_subrev_u32 v152, 0, v147\n" \
"v_sub_u32 v144, 7, v147\n" \
"v_mfma_f32_16x16x32_bf16 v[32:35], a[96:99], a[64:67], v[32:35]\n" \
"ds_read_b128 a[192:195], v205 offset:12288\n" \
"v_or_b32 v152, v152, v144\n" \
"v_mfma_f32_16x16x32_bf16 v[32:35], a[100:103], a[68:71], v[32:35]\n" \
"global_load_lds_dwordx4 v253, s[48:49] offset:-2048\n" \
"v_ashrrev_i32 v152, 31, v152\n" \
"v_mfma_f32_16x16x32_bf16 v[40:43], a[96:99], a[72:75], v[40:43]\n" \
"ds_read_b128 a[196:199], v206 offset:12288\n" \
"v_and_b32 v152, 0xff800000, v152\n" \
"v_mfma_f32_16x16x32_bf16 v[40:43], a[100:103], a[76:79], v[40:43]\n" \
"ds_read_b64_tr_b16 a[208:209], v209 offset:4096\n" \
"v_mov_b32 v160, v152\n" \
"v_mfma_f32_16x16x32_bf16 v[0:3], a[104:107], a[72:75], v[0:3]\n" \
"ds_read_b64_tr_b16 a[210:211], v209 offset:5120\n" \
"v_mov_b32 v168, v152\n" \
"v_mfma_f32_16x16x32_bf16 v[0:3], a[108:111], a[76:79], v[0:3]\n" \
"s_waitcnt lgkmcnt(3)\n" \
"global_load_lds_dwordx4 v253, s[48:49] offset:0\n" \
"v_mov_b32 v176, v152\n" \
"v_mfma_f32_16x16x32_bf16 v[4:7], a[112:115], a[72:75], v[4:7]\n" \
"s_add_u32 s48, s48, 0x60000\n" \
"s_addc_u32 s49, s49, 0\n" \
"ds_read_b64_tr_b16 a[212:213], v209 offset:16384\n" \
"v_mfma_f32_16x16x32_bf16 v[4:7], a[116:119], a[76:79], v[4:7]\n" \
"ds_read_b64_tr_b16 a[214:215], v209 offset:17408\n" \
"v_sub_u32 v156, -9, v147\n" \
"v_mfma_f32_16x16x32_bf16 v[12:15], a[112:115], a[80:83], v[12:15]\n" \
"ds_read_b64_tr_b16 a[216:217], v210 offset:4096\n" \
"v_ashrrev_i32 v156, 31, v156\n" \
"v_mfma_f32_16x16x32_bf16 v[12:15], a[116:119], a[84:87], v[12:15]\n" \
"global_load_lds_dwordx4 v253, s[50:51] offset:-3072\n" \
"v_and_b32 v156, 0xff800000, v156\n" \
"v_mfma_f32_16x16x32_bf16 v[8:11], a[104:107], a[80:83], v[8:11]\n" \
"ds_read_b64_tr_b16 a[218:219], v210 offset:5120\n" \
"v_mov_b32 v164, v156\n" \
"v_mfma_f32_16x16x32_bf16 v[8:11], a[108:111], a[84:87], v[8:11]\n" \
"ds_read_b64_tr_b16 a[220:221], v210 offset:16384\n" \
"v_mov_b32 v172, v156\n" \
"v_mfma_f32_16x16x32_bf16 v[48:51], a[96:99], a[80:83], v[48:51]\n" \
"ds_read_b64_tr_b16 a[222:223], v210 offset:17408\n" \
"v_add_u32 v148, 0, v146\n" \
"v_mfma_f32_16x16x32_bf16 v[48:51], a[100:103], a[84:87], v[48:51]\n" \
"global_load_lds_dwordx4 v253, s[50:51] offset:-1024\n" \
"v_or3_b32 v148, v148, v145, s86\n" \
"v_mfma_f32_16x16x32_bf16 v[56:59], a[96:99], a[88:91], v[56:59]\n" \
"ds_read_b64_tr_b16 a[200:201], v210 offset:2048\n" \
"v_ashrrev_i32 v148, 31, v148\n" \
"v_mfma_f32_16x16x32_bf16 v[56:59], a[100:103], a[92:95], v[56:59]\n" \
"ds_read_b64_tr_b16 a[202:203], v210 offset:3072\n" \
"v_and_b32 v148, 0xff800000, v148\n" \
"v_mfma_f32_16x16x32_bf16 v[16:19], a[104:107], a[88:91], v[16:19]\n" \
"ds_read_b64_tr_b16 a[204:205], v210 offset:14336\n" \
"v_subrev_u32 v153, 1, v147\n" \
"v_mfma_f32_16x16x32_bf16 v[16:19], a[108:111], a[92:95], v[16:19]\n" \
"global_load_lds_dwordx4 v253, s[50:51] offset:1024\n" \
"v_sub_u32 v144, 8, v147\n" \
"v_mfma_f32_16x16x32_bf16 v[20:23], a[112:115], a[88:91], v[20:23]\n" \
"s_add_u32 s50, s50, 0x60000\n" \
"s_addc_u32 s51, s51, 0\n" \
"ds_read_b64_tr_b16 a[206:207], v210 offset:15360\n" \
"v_mfma_f32_16x16x32_bf16 v[20:23], a[116:119], a[92:95], v[20:23]\n" \
"s_waitcnt lgkmcnt(3)\n" \
"v_add_u32 v207, 0xffff3000, v207\n" \
"v_add_u32 v208, 0xffff3000, v208\n" \
"v_mfma_f32_16x16x32_bf16 v[28:31], a[112:115], a[192:195], v[28:31]\n" \
"v_add_u32 v205, 0xffff3000, v205\n" \
"v_add_u32 v206, 0xffff3000, v206\n" \
"v_mfma_f32_16x16x32_bf16 v[28:31], a[116:119], a[196:199], v[28:31]\n" \
"buffer_load_dword v220, v221, s[56:59], s81 offen sc1\n" \
"v_mfma_f32_16x16x32_bf16 v[24:27], a[104:107], a[192:195], v[24:27]\n" \
"v_add_u32 v209, 0xffff3000, v209\n" \
"v_add_u32 v210, 0xffff3000, v210\n" \
"v_mfma_f32_16x16x32_bf16 v[24:27], a[108:111], a[196:199], v[24:27]\n" \
"v_or_b32 v153, v153, v144\n" \
"v_ashrrev_i32 v153, 31, v153\n" \
"v_mfma_f32_16x16x32_bf16 v[64:67], a[96:99], a[192:195], v[64:67]\n" \
"s_waitcnt vmcnt(12) lgkmcnt(0)\n" \
"v_and_b32 v153, 0xff800000, v153\n" \
"v_mov_b32 v161, v153\n" \
"v_mfma_f32_16x16x32_bf16 v[64:67], a[100:103], a[196:199], v[64:67]\n" \
"s_barrier\n" \
"v_mfma_f32_16x16x32_bf16 v[72:75], a[64:67], a[208:211], v[72:75]\n" \
"ds_read_b64_tr_b16 a[96:97], v209 offset:2048\n" \
"v_mov_b32 v169, v153\n" \
"v_mfma_f32_16x16x32_bf16 v[72:75], a[68:71], a[212:215], v[72:75]\n" \
"ds_read_b64_tr_b16 a[98:99], v209 offset:3072\n" \
"v_mov_b32 v177, v153\n" \
"v_mfma_f32_16x16x32_bf16 v[80:83], a[72:75], a[208:211], v[80:83]\n" \
"s_add_u32 m0, s95, 118784\n" \
"ds_read_b128 a[64:67], v207 offset:0\n" \
"ds_read_b64_tr_b16 a[100:101], v209 offset:14336\n" \
"v_mfma_f32_16x16x32_bf16 v[80:83], a[76:79], a[212:215], v[80:83]\n" \
"global_load_lds_dwordx4 v252, s[42:43] offset:0\n" \
"v_sub_u32 v157, -8, v147\n" \
"v_mfma_f32_16x16x32_bf16 v[84:87], a[72:75], a[216:219], v[84:87]\n" \
"ds_read_b128 a[68:71], v208 offset:0\n" \
"ds_read_b64_tr_b16 a[102:103], v209 offset:15360\n" \
"v_mfma_f32_16x16x32_bf16 v[84:87], a[76:79], a[220:223], v[84:87]\n" \
"ds_read_b64_tr_b16 a[104:105], v209 offset:0\n" \
"v_ashrrev_i32 v157, 31, v157\n" \
"v_mfma_f32_16x16x32_bf16 v[44:47], a[200:203], a[72:75], v[44:47]\n" \
"ds_read_b64_tr_b16 a[106:107], v209 offset:1024\n" \
"v_and_b32 v157, 0xff800000, v157\n" \
"v_mfma_f32_16x16x32_bf16 v[44:47], a[204:207], a[76:79], v[44:47]\n" \
"global_load_lds_dwordx4 v252, s[42:43] offset:2048\n" \
"v_mov_b32 v165, v157\n" \
"v_mfma_f32_16x16x32_bf16 v[52:55], a[200:203], a[80:83], v[52:55]\n" \
"ds_read_b128 a[72:75], v205 offset:0\n" \
"v_mov_b32 v173, v157\n" \
"v_mfma_f32_16x16x32_bf16 v[52:55], a[204:207], a[84:87], v[52:55]\n" \
"ds_read_b128 a[76:79], v206 offset:0\n" \
"v_add_u32 v149, 1, v146\n" \
"v_mfma_f32_16x16x32_bf16 v[92:95], a[80:83], a[216:219], v[92:95]\n" \
"ds_read_b64_tr_b16 a[108:109], v209 offset:12288\n" \
"v_or3_b32 v149, v149, v145, s86\n" \
"v_mfma_f32_16x16x32_bf16 v[92:95], a[84:87], a[220:223], v[92:95]\n" \
"global_load_lds_dwordx4 v252, s[44:45] offset:1024\n" \
"v_ashrrev_i32 v149, 31, v149\n" \
"v_mfma_f32_16x16x32_bf16 v[88:91], a[80:83], a[208:211], v[88:91]\n" \
"ds_read_b64_tr_b16 a[110:111], v209 offset:13312\n" \
"v_and_b32 v149, 0xff800000, v149\n" \
"v_mfma_f32_16x16x32_bf16 v[88:91], a[84:87], a[212:215], v[88:91]\n" \
"ds_read_b64_tr_b16 a[112:113], v210 offset:0\n" \
"v_subrev_u32 v154, 2, v147\n" \
"v_mfma_f32_16x16x32_bf16 v[96:99], a[88:91], a[208:211], v[96:99]\n" \
"ds_read_b128 a[80:83], v205 offset:4096\n" \
"v_sub_u32 v144, 9, v147\n" \
"v_mfma_f32_16x16x32_bf16 v[96:99], a[92:95], a[212:215], v[96:99]\n" \
"global_load_lds_dwordx4 v252, s[44:45] offset:3072\n" \
"v_or_b32 v154, v154, v144\n" \
"v_mfma_f32_16x16x32_bf16 v[100:103], a[88:91], a[216:219], v[100:103]\n" \
"ds_read_b128 a[84:87], v206 offset:4096\n" \
"v_ashrrev_i32 v154, 31, v154\n" \
"v_mfma_f32_16x16x32_bf16 v[100:103], a[92:95], a[220:223], v[100:103]\n" \
"ds_read_b64_tr_b16 a[114:115], v210 offset:1024\n" \
"v_and_b32 v154, 0xff800000, v154\n" \
"v_mfma_f32_16x16x32_bf16 v[60:63], a[200:203], a[88:91], v[60:63]\n" \
"s_add_u32 m0, s85, 135168\n" \
"ds_read_b64_tr_b16 a[116:117], v210 offset:12288\n" \
"v_mov_b32 v162, v154\n" \
"v_mfma_f32_16x16x32_bf16 v[60:63], a[204:207], a[92:95], v[60:63]\n" \
"buffer_load_dwordx4 v246, s[12:15], s46 offen lds\n" \
"v_mfma_f32_16x16x32_bf16 v[68:71], a[200:203], a[192:195], v[68:71]\n" \
"ds_read_b128 a[88:91], v205 offset:8192\n" \
"v_mov_b32 v170, v154\n" \
"v_mfma_f32_16x16x32_bf16 v[68:71], a[204:207], a[196:199], v[68:71]\n" \
"ds_read_b128 a[92:95], v206 offset:8192\n" \
"v_mov_b32 v178, v154\n" \
"v_mfma_f32_16x16x32_bf16 v[108:111], a[192:195], a[216:219], v[108:111]\n" \
"s_add_u32 m0, s84, 143360\n" \
"ds_read_b64_tr_b16 a[118:119], v210 offset:13312\n" \
"v_sub_u32 v158, -7, v147\n" \
"v_mfma_f32_16x16x32_bf16 v[108:111], a[196:199], a[220:223], v[108:111]\n" \
"global_load_lds_dwordx4 v253, s[48:49] offset:-4096\n" \
"v_ashrrev_i32 v158, 31, v158\n" \
"v_mfma_f32_16x16x32_bf16 v[104:107], a[192:195], a[208:211], v[104:107]\n" \
"v_and_b32 v158, 0xff800000, v158\n" \
"v_mov_b32 v166, v158\n" \
"v_mfma_f32_16x16x32_bf16 v[104:107], a[196:199], a[212:215], v[104:107]\n" \
"s_waitcnt lgkmcnt(2)\n" \
"v_mov_b32 v174, v158\n" \
"v_add_u32 v150, 2, v146\n" \
"v_mfma_f32_16x16x32_bf16 v[32:35], a[96:99], a[64:67], v[32:35]\n" \
"ds_read_b128 a[192:195], v205 offset:12288\n" \
"v_or3_b32 v150, v150, v145, s86\n" \
"v_mfma_f32_16x16x32_bf16 v[32:35], a[100:103], a[68:71], v[32:35]\n" \
"global_load_lds_dwordx4 v253, s[48:49] offset:-2048\n" \
"v_ashrrev_i32 v150, 31, v150\n" \
"v_mfma_f32_16x16x32_bf16 v[40:43], a[96:99], a[72:75], v[40:43]\n" \
"ds_read_b128 a[196:199], v206 offset:12288\n" \
"v_and_b32 v150, 0xff800000, v150\n" \
"v_mfma_f32_16x16x32_bf16 v[40:43], a[100:103], a[76:79], v[40:43]\n" \
"ds_read_b64_tr_b16 a[208:209], v209 offset:4096\n" \
"v_subrev_u32 v155, 3, v147\n" \
"v_mfma_f32_16x16x32_bf16 v[0:3], a[104:107], a[72:75], v[0:3]\n" \
"ds_read_b64_tr_b16 a[210:211], v209 offset:5120\n" \
"v_sub_u32 v144, 10, v147\n" \
"v_mfma_f32_16x16x32_bf16 v[0:3], a[108:111], a[76:79], v[0:3]\n" \
"s_waitcnt lgkmcnt(3)\n" \
"global_load_lds_dwordx4 v253, s[48:49] offset:0\n" \
"v_or_b32 v155, v155, v144\n" \
"v_mfma_f32_16x16x32_bf16 v[4:7], a[112:115], a[72:75], v[4:7]\n" \
"ds_read_b64_tr_b16 a[212:213], v209 offset:16384\n" \
"v_ashrrev_i32 v155, 31, v155\n" \
"v_mfma_f32_16x16x32_bf16 v[4:7], a[116:119], a[76:79], v[4:7]\n" \
"ds_read_b64_tr_b16 a[214:215], v209 offset:17408\n" \
"v_and_b32 v155, 0xff800000, v155\n" \
"v_mfma_f32_16x16x32_bf16 v[12:15], a[112:115], a[80:83], v[12:15]\n" \
"ds_read_b64_tr_b16 a[216:217], v210 offset:4096\n" \
"v_mov_b32 v163, v155\n" \
"v_mfma_f32_16x16x32_bf16 v[12:15], a[116:119], a[84:87], v[12:15]\n" \
"global_load_lds_dwordx4 v253, s[50:51] offset:-3072\n" \
"v_mov_b32 v171, v155\n" \
"v_mfma_f32_16x16x32_bf16 v[8:11], a[104:107], a[80:83], v[8:11]\n" \
"ds_read_b64_tr_b16 a[218:219], v210 offset:5120\n" \
"v_mov_b32 v179, v155\n" \
"v_mfma_f32_16x16x32_bf16 v[8:11], a[108:111], a[84:87], v[8:11]\n" \
"ds_read_b64_tr_b16 a[220:221], v210 offset:16384\n" \
"v_sub_u32 v159, -6, v147\n" \
"v_mfma_f32_16x16x32_bf16 v[48:51], a[96:99], a[80:83], v[48:51]\n" \
"ds_read_b64_tr_b16 a[222:223], v210 offset:17408\n" \
"v_ashrrev_i32 v159, 31, v159\n" \
"v_mfma_f32_16x16x32_bf16 v[48:51], a[100:103], a[84:87], v[48:51]\n" \
"global_load_lds_dwordx4 v253, s[50:51] offset:-1024\n" \
"v_and_b32 v159, 0xff800000, v159\n" \
"v_mfma_f32_16x16x32_bf16 v[56:59], a[96:99], a[88:91], v[56:59]\n" \
"ds_read_b64_tr_b16 a[200:201], v210 offset:2048\n" \
"v_mov_b32 v167, v159\n" \
"v_mfma_f32_16x16x32_bf16 v[56:59], a[100:103], a[92:95], v[56:59]\n" \
"ds_read_b64_tr_b16 a[202:203], v210 offset:3072\n" \
"v_mov_b32 v175, v159\n" \
"v_mfma_f32_16x16x32_bf16 v[16:19], a[104:107], a[88:91], v[16:19]\n" \
"ds_read_b64_tr_b16 a[204:205], v210 offset:14336\n" \
"v_add_u32 v151, 3, v146\n" \
"v_mfma_f32_16x16x32_bf16 v[16:19], a[108:111], a[92:95], v[16:19]\n" \
"global_load_lds_dwordx4 v253, s[50:51] offset:1024\n" \
"v_or3_b32 v151, v151, v145, s86\n" \
"v_mfma_f32_16x16x32_bf16 v[20:23], a[112:115], a[88:91], v[20:23]\n" \
"ds_read_b64_tr_b16 a[206:207], v210 offset:15360\n" \
"v_ashrrev_i32 v151, 31, v151\n" \
"v_mfma_f32_16x16x32_bf16 v[20:23], a[116:119], a[92:95], v[20:23]\n" \
"s_waitcnt lgkmcnt(3)\n" \
"v_add_u32 v207, 0xffff0000, v207\n" \
"v_add_u32 v208, 0xffff0000, v208\n" \
"v_mfma_f32_16x16x32_bf16 v[28:31], a[112:115], a[192:195], v[28:31]\n" \
"v_add_u32 v205, 0xffff0000, v205\n" \
"v_add_u32 v206, 0xffff0000, v206\n" \
"v_mfma_f32_16x16x32_bf16 v[28:31], a[116:119], a[196:199], v[28:31]\n" \
"v_add_u32 v209, 0xffff0000, v209\n" \
"v_add_u32 v210, 0xffff0000, v210\n" \
"v_mfma_f32_16x16x32_bf16 v[24:27], a[104:107], a[192:195], v[24:27]\n" \
"v_and_b32 v151, 0xff800000, v151\n" \
"v_mov_b32 v144, 0x3e8293ee\n" \
"v_mfma_f32_16x16x32_bf16 v[24:27], a[108:111], a[196:199], v[24:27]\n" \
"s_add_u32 s90, s95, 117760\n" \
"v_add_u32 v223, s90, v248\n" \
"v_mfma_f32_16x16x32_bf16 v[64:67], a[96:99], a[192:195], v[64:67]\n" \
"s_waitcnt vmcnt(12) lgkmcnt(0)\n" \
"s_cmp_lg_u32 s89, 0\n" \
"s_cselect_b64 s[92:93], -1, 0\n" \
"v_mfma_f32_16x16x32_bf16 v[64:67], a[100:103], a[196:199], v[64:67]\n" \
"s_barrier\n" \
"v_mfma_f32_16x16x32_bf16 v[72:75], a[64:67], a[208:211], v[72:75]\n" \
"s_waitcnt vmcnt(11)\n" \
"ds_read_b64_tr_b16 a[96:97], v209 offset:2048\n" \
"v_cmp_ne_u32 vcc, s60, v220\n" \
"v_mfma_f32_16x16x32_bf16 v[72:75], a[68:71], a[212:215], v[72:75]\n" \
"ds_read_b64_tr_b16 a[98:99], v209 offset:3072\n" \
"v_xor_b32 v145, 0x2000, v223\n" \
"v_mfma_f32_16x16x32_bf16 v[80:83], a[72:75], a[208:211], v[80:83]\n" \
"ds_read_b128 a[64:67], v207 offset:0\n" \
"ds_read_b64_tr_b16 a[100:101], v209 offset:14336\n" \
"v_mfma_f32_16x16x32_bf16 v[80:83], a[76:79], a[212:215], v[80:83]\n" \
"s_and_b32 vcc_hi, vcc_lo, s80\n" \
"s_cbranch_scc1 Lpl2_%=\n" \
"Lpr2_%=:\n" \
"v_mfma_f32_16x16x32_bf16 v[84:87], a[72:75], a[216:219], v[84:87]\n" \
"s_add_u32 m0, s75, s95\n" \
"ds_read_b128 a[68:71], v208 offset:0\n" \
"ds_read_b64_tr_b16 a[102:103], v209 offset:15360\n" \
"v_mfma_f32_16x16x32_bf16 v[84:87], a[76:79], a[220:223], v[84:87]\n" \
"ds_read_b64_tr_b16 a[104:105], v209 offset:0\n" \
"global_load_lds_dwordx4 v222, s[36:37] offset:0 sc1\n" \
"v_mfma_f32_16x16x32_bf16 v[44:47], a[200:203], a[72:75], v[44:47]\n" \
"s_cmp_eq_u32 s89, 0\n" \
"s_cselect_b32 s16, 0, 0xff00\n" \
"ds_read_b64_tr_b16 a[106:107], v209 offset:1024\n" \
"v_mfma_f32_16x16x32_bf16 v[44:47], a[204:207], a[76:79], v[44:47]\n" \
"s_mov_b32 s17, s16\n" \
"v_add_u32 v146, 0x1c90080, v249\n" \
"v_cndmask_b32 v146, v249, v146, s[16:17]\n" \
"v_mfma_f32_16x16x32_bf16 v[52:55], a[200:203], a[80:83], v[52:55]\n" \
"ds_read_b128 a[72:75], v205 offset:0\n" \
"v_mfma_f32_16x16x32_bf16 v[52:55], a[204:207], a[84:87], v[52:55]\n" \
"ds_read_b128 a[76:79], v206 offset:0\n" \
"global_load_lds_dwordx4 v222, s[36:37] offset:1024 sc1\n" \
"v_mfma_f32_16x16x32_bf16 v[92:95], a[80:83], a[216:219], v[92:95]\n" \
"ds_read_b64_tr_b16 a[108:109], v209 offset:12288\n" \
"v_mfma_f32_16x16x32_bf16 v[92:95], a[84:87], a[220:223], v[92:95]\n" \
"v_mfma_f32_16x16x32_bf16 v[88:91], a[80:83], a[208:211], v[88:91]\n" \
"ds_read_b64_tr_b16 a[110:111], v209 offset:13312\n" \
"v_mfma_f32_16x16x32_bf16 v[88:91], a[84:87], a[212:215], v[88:91]\n" \
"ds_read_b64_tr_b16 a[112:113], v210 offset:0\n" \
"global_load_lds_dwordx4 v222, s[36:37] offset:2048 sc1\n" \
"v_mfma_f32_16x16x32_bf16 v[96:99], a[88:91], a[208:211], v[96:99]\n" \
"ds_read_b128 a[80:83], v205 offset:4096\n" \
"v_mfma_f32_16x16x32_bf16 v[96:99], a[92:95], a[212:215], v[96:99]\n" \
"v_mfma_f32_16x16x32_bf16 v[100:103], a[88:91], a[216:219], v[100:103]\n" \
"ds_read_b128 a[84:87], v206 offset:4096\n" \
"v_mfma_f32_16x16x32_bf16 v[100:103], a[92:95], a[220:223], v[100:103]\n" \
"ds_read_b64_tr_b16 a[114:115], v210 offset:1024\n" \
"global_load_lds_dwordx4 v222, s[36:37] offset:3072 sc1\n" \
"v_mfma_f32_16x16x32_bf16 v[60:63], a[200:203], a[88:91], v[60:63]\n" \
"s_add_u32 s36, s36, 0x1000\n" \
"ds_read_b64_tr_b16 a[116:117], v210 offset:12288\n" \
"v_mfma_f32_16x16x32_bf16 v[60:63], a[204:207], a[92:95], v[60:63]\n" \
"v_mfma_f32_16x16x32_bf16 v[68:71], a[200:203], a[192:195], v[68:71]\n" \
"ds_read_b128 a[88:91], v205 offset:8192\n" \
"s_add_u32 m0, m0, 0x5000\n" \
"v_mfma_f32_16x16x32_bf16 v[68:71], a[204:207], a[196:199], v[68:71]\n" \
"ds_read_b128 a[92:95], v206 offset:8192\n" \
"global_load_lds_dwordx4 v216, s[38:39] offset:-4096\n" \
"v_mfma_f32_16x16x32_bf16 v[108:111], a[192:195], a[216:219], v[108:111]\n" \
"ds_read_b64_tr_b16 a[118:119], v210 offset:13312\n" \
"v_mfma_f32_16x16x32_bf16 v[108:111], a[196:199], a[220:223], v[108:111]\n" \
"v_mfma_f32_16x16x32_bf16 v[104:107], a[192:195], a[208:211], v[104:107]\n" \
"v_mfma_f32_16x16x32_bf16 v[104:107], a[196:199], a[212:215], v[104:107]\n" \
"s_waitcnt lgkmcnt(2)\n" \
"global_load_lds_dwordx4 v217, s[38:39] offset:-3072\n" \
"v_mfma_f32_16x16x32_bf16 v[32:35], a[96:99], a[64:67], v[32:35]\n" \
"ds_read_b128 a[192:195], v205 offset:12288\n" \
"v_mfma_f32_16x16x32_bf16 v[32:35], a[100:103], a[68:71], v[32:35]\n" \
"v_mfma_f32_16x16x32_bf16 v[40:43], a[96:99], a[72:75], v[40:43]\n" \
"ds_read_b128 a[196:199], v206 offset:12288\n" \
"v_mfma_f32_16x16x32_bf16 v[40:43], a[100:103], a[76:79], v[40:43]\n" \
"ds_read_b64_tr_b16 a[208:209], v209 offset:4096\n" \
"global_load_lds_dwordx4 v218, s[38:39] offset:-2048\n" \
"v_mfma_f32_16x16x32_bf16 v[0:3], a[104:107], a[72:75], v[0:3]\n" \
"ds_read_b64_tr_b16 a[210:211], v209 offset:5120\n" \
"v_mfma_f32_16x16x32_bf16 v[0:3], a[108:111], a[76:79], v[0:3]\n" \
"s_waitcnt lgkmcnt(3)\n" \
"v_mfma_f32_16x16x32_bf16 v[4:7], a[112:115], a[72:75], v[4:7]\n" \
"ds_read_b64_tr_b16 a[212:213], v209 offset:16384\n" \
"v_mfma_f32_16x16x32_bf16 v[4:7], a[116:119], a[76:79], v[4:7]\n" \
"ds_read_b64_tr_b16 a[214:215], v209 offset:17408\n" \
"global_load_lds_dwordx4 v219, s[38:39] offset:-1024\n" \
"v_mfma_f32_16x16x32_bf16 v[12:15], a[112:115], a[80:83], v[12:15]\n" \
"s_add_u32 s38, s38, 0x20000\n" \
"s_addc_u32 s39, s39, 0\n" \
"ds_read_b64_tr_b16 a[216:217], v210 offset:4096\n" \
"v_mfma_f32_16x16x32_bf16 v[12:15], a[116:119], a[84:87], v[12:15]\n" \
"s_sub_u32 s75, s75, 32768\n" \
"s_cselect_b32 s75, 131072, s75\n" \
"v_mfma_f32_16x16x32_bf16 v[8:11], a[104:107], a[80:83], v[8:11]\n" \
"ds_read_b64_tr_b16 a[218:219], v210 offset:5120\n" \
"v_mfma_f32_16x16x32_bf16 v[8:11], a[108:111], a[84:87], v[8:11]\n" \
"ds_read_b64_tr_b16 a[220:221], v210 offset:16384\n" \
"v_mfma_f32_16x16x32_bf16 v[48:51], a[96:99], a[80:83], v[48:51]\n" \
"ds_read_b64_tr_b16 a[222:223], v210 offset:17408\n" \
"v_mfma_f32_16x16x32_bf16 v[48:51], a[100:103], a[84:87], v[48:51]\n" \
"v_mfma_f32_16x16x32_bf16 v[56:59], a[96:99], a[88:91], v[56:59]\n" \
"ds_read_b64_tr_b16 a[200:201], v210 offset:2048\n" \
"v_mfma_f32_16x16x32_bf16 v[56:59], a[100:103], a[92:95], v[56:59]\n" \
"ds_read_b64_tr_b16 a[202:203], v210 offset:3072\n" \
"v_mfma_f32_16x16x32_bf16 v[16:19], a[104:107], a[88:91], v[16:19]\n" \
"ds_read_b64_tr_b16 a[204:205], v210 offset:14336\n" \
"v_mfma_f32_16x16x32_bf16 v[16:19], a[108:111], a[92:95], v[16:19]\n" \
"v_mfma_f32_16x16x32_bf16 v[20:23], a[112:115], a[88:91], v[20:23]\n" \
"ds_read_b64_tr_b16 a[206:207], v210 offset:15360\n" \
"v_mfma_f32_16x16x32_bf16 v[20:23], a[116:119], a[92:95], v[20:23]\n" \
"s_waitcnt lgkmcnt(3)\n" \
"v_add_u32 v207, 0x1d000, v207\n" \
"v_add_u32 v208, 0x1d000, v208\n" \
"v_mfma_f32_16x16x32_bf16 v[28:31], a[112:115], a[192:195], v[28:31]\n" \
"v_add_u32 v205, 0x1d000, v205\n" \
"v_add_u32 v206, 0x1d000, v206\n" \
"v_mfma_f32_16x16x32_bf16 v[28:31], a[116:119], a[196:199], v[28:31]\n" \
"v_add_u32 v209, 0x1d000, v209\n" \
"v_add_u32 v210, 0x1d000, v210\n" \
"v_mfma_f32_16x16x32_bf16 v[24:27], a[104:107], a[192:195], v[24:27]\n" \
"v_mfma_f32_16x16x32_bf16 v[24:27], a[108:111], a[196:199], v[24:27]\n" \
"v_mfma_f32_16x16x32_bf16 v[64:67], a[96:99], a[192:195], v[64:67]\n" \
"s_waitcnt vmcnt(8) lgkmcnt(0)\n" \
"v_mfma_f32_16x16x32_bf16 v[64:67], a[100:103], a[196:199], v[64:67]\n" \
"s_barrier\n" \
"v_mfma_f32_16x16x32_bf16 v[72:75], a[64:67], a[208:211], v[72:75]\n" \
"ds_read_b64_tr_b16 a[96:97], v209 offset:2048\n" \
"v_mfma_f32_16x16x32_bf16 v[72:75], a[68:71], a[212:215], v[72:75]\n" \
"ds_read_b64_tr_b16 a[98:99], v209 offset:3072\n" \
"v_mfma_f32_16x16x32_bf16 v[80:83], a[72:75], a[208:211], v[80:83]\n" \
"s_add_u32 m0, s75, s95\n" \
"ds_read_b128 a[64:67], v207 offset:0\n" \
"ds_read_b64_tr_b16 a[100:101], v209 offset:14336\n" \
"v_mfma_f32_16x16x32_bf16 v[80:83], a[76:79], a[212:215], v[80:83]\n" \
"global_load_lds_dwordx4 v222, s[36:37] offset:0 sc1\n" \
"v_mfma_f32_16x16x32_bf16 v[84:87], a[72:75], a[216:219], v[84:87]\n" \
"ds_read_b128 a[68:71], v208 offset:0\n" \
"ds_read_b64_tr_b16 a[102:103], v209 offset:15360\n" \
"v_mfma_f32_16x16x32_bf16 v[84:87], a[76:79], a[220:223], v[84:87]\n" \
"ds_read_b64_tr_b16 a[104:105], v209 offset:0\n" \
"v_mfma_f32_16x16x32_bf16 v[44:47], a[200:203], a[72:75], v[44:47]\n" \
"ds_read_b64_tr_b16 a[106:107], v209 offset:1024\n" \
"v_mfma_f32_16x16x32_bf16 v[44:47], a[204:207], a[76:79], v[44:47]\n" \
"global_load_lds_dwordx4 v222, s[36:37] offset:1024 sc1\n" \
"v_mfma_f32_16x16x32_bf16 v[52:55], a[200:203], a[80:83], v[52:55]\n" \
"ds_read_b128 a[72:75], v205 offset:0\n" \
"v_mfma_f32_16x16x32_bf16 v[52:55], a[204:207], a[84:87], v[52:55]\n" \
"ds_read_b128 a[76:79], v206 offset:0\n" \
"Lpg10886_%=:\n" \
"s_cbranch_execz Lpg12503_%=\n" \
"v_mfma_f32_16x16x32_bf16 v[92:95], a[80:83], a[216:219], v[92:95]\n" \
"ds_read_b64_tr_b16 a[108:109], v209 offset:12288\n" \
"v_mfma_f32_16x16x32_bf16 v[92:95], a[84:87], a[220:223], v[92:95]\n" \
"global_load_lds_dwordx4 v222, s[36:37] offset:2048 sc1\n" \
"v_mfma_f32_16x16x32_bf16 v[88:91], a[80:83], a[208:211], v[88:91]\n" \
"ds_read_b64_tr_b16 a[110:111], v209 offset:13312\n" \
"v_mfma_f32_16x16x32_bf16 v[88:91], a[84:87], a[212:215], v[88:91]\n" \
"ds_read_b64_tr_b16 a[112:113], v210 offset:0\n" \
"v_mfma_f32_16x16x32_bf16 v[96:99], a[88:91], a[208:211], v[96:99]\n" \
"ds_read_b128 a[80:83], v205 offset:4096\n" \
"v_mfma_f32_16x16x32_bf16 v[96:99], a[92:95], a[212:215], v[96:99]\n" \
"global_load_lds_dwordx4 v222, s[36:37] offset:3072 sc1\n" \
"v_mfma_f32_16x16x32_bf16 v[100:103], a[88:91], a[216:219], v[100:103]\n" \
"ds_read_b128 a[84:87], v206 offset:4096\n" \
"s_add_u32 s36, s36, 0x1000\n" \
"v_mfma_f32_16x16x32_bf16 v[100:103], a[92:95], a[220:223], v[100:103]\n" \
"ds_read_b64_tr_b16 a[114:115], v210 offset:1024\n" \
"v_mfma_f32_16x16x32_bf16 v[60:63], a[200:203], a[88:91], v[60:63]\n" \
"s_add_u32 m0, m0, 0x5000\n" \
"ds_read_b64_tr_b16 a[116:117], v210 offset:12288\n" \
"v_mfma_f32_16x16x32_bf16 v[60:63], a[204:207], a[92:95], v[60:63]\n" \
"global_load_lds_dwordx4 v216, s[38:39] offset:-4096\n" \
"v_mfma_f32_16x16x32_bf16 v[68:71], a[200:203], a[192:195], v[68:71]\n" \
"ds_read_b128 a[88:91], v205 offset:8192\n" \
"v_mfma_f32_16x16x32_bf16 v[68:71], a[204:207], a[196:199], v[68:71]\n" \
"ds_read_b128 a[92:95], v206 offset:8192\n" \
"v_mfma_f32_16x16x32_bf16 v[108:111], a[192:195], a[216:219], v[108:111]\n" \
"ds_read_b64_tr_b16 a[118:119], v210 offset:13312\n" \
"v_mfma_f32_16x16x32_bf16 v[108:111], a[196:199], a[220:223], v[108:111]\n" \
"global_load_lds_dwordx4 v217, s[38:39] offset:-3072\n" \
"v_mfma_f32_16x16x32_bf16 v[104:107], a[192:195], a[208:211], v[104:107]\n" \
"v_mfma_f32_16x16x32_bf16 v[104:107], a[196:199], a[212:215], v[104:107]\n" \
"s_waitcnt lgkmcnt(2)\n" \
"v_mfma_f32_16x16x32_bf16 v[32:35], a[96:99], a[64:67], v[32:35]\n" \
"ds_read_b128 a[192:195], v205 offset:12288\n" \
"v_mfma_f32_16x16x32_bf16 v[32:35], a[100:103], a[68:71], v[32:35]\n" \
"global_load_lds_dwordx4 v218, s[38:39] offset:-2048\n" \
"v_mfma_f32_16x16x32_bf16 v[40:43], a[96:99], a[72:75], v[40:43]\n" \
"ds_read_b128 a[196:199], v206 offset:12288\n" \
"v_mfma_f32_16x16x32_bf16 v[40:43], a[100:103], a[76:79], v[40:43]\n" \
"ds_read_b64_tr_b16 a[208:209], v209 offset:4096\n" \
"v_mfma_f32_16x16x32_bf16 v[0:3], a[104:107], a[72:75], v[0:3]\n" \
"ds_read_b64_tr_b16 a[210:211], v209 offset:5120\n" \
"v_mfma_f32_16x16x32_bf16 v[0:3], a[108:111], a[76:79], v[0:3]\n" \
"s_waitcnt lgkmcnt(3)\n" \
"global_load_lds_dwordx4 v219, s[38:39] offset:-1024\n" \
"v_mfma_f32_16x16x32_bf16 v[4:7], a[112:115], a[72:75], v[4:7]\n" \
"s_add_u32 s38, s38, 0x20000\n" \
"s_addc_u32 s39, s39, 0\n" \
"ds_read_b64_tr_b16 a[212:213], v209 offset:16384\n" \
"v_mfma_f32_16x16x32_bf16 v[4:7], a[116:119], a[76:79], v[4:7]\n" \
"s_sub_u32 s75, s75, 32768\n" \
"s_cselect_b32 s75, 131072, s75\n" \
"ds_read_b64_tr_b16 a[214:215], v209 offset:17408\n" \
"v_mfma_f32_16x16x32_bf16 v[48:51], a[96:99], a[80:83], v[48:51]\n" \
"s_add_u32 m0, s75, s95\n" \
"ds_read_b64_tr_b16 a[216:217], v210 offset:4096\n" \
"v_mfma_f32_16x16x32_bf16 v[48:51], a[100:103], a[84:87], v[48:51]\n" \
"global_load_lds_dwordx4 v222, s[36:37] offset:0 sc1\n" \
"v_mfma_f32_16x16x32_bf16 v[8:11], a[104:107], a[80:83], v[8:11]\n" \
"ds_read_b64_tr_b16 a[218:219], v210 offset:5120\n" \
"v_cvt_pk_bf16_f32 v112, v0, v1\n" \
"v_mfma_f32_16x16x32_bf16 v[8:11], a[108:111], a[84:87], v[8:11]\n" \
"ds_read_b64_tr_b16 a[220:221], v210 offset:16384\n" \
"v_cvt_pk_bf16_f32 v113, v2, v3\n" \
"v_mfma_f32_16x16x32_bf16 v[12:15], a[112:115], a[80:83], v[12:15]\n" \
"ds_read_b64_tr_b16 a[222:223], v210 offset:17408\n" \
"v_cvt_pk_bf16_f32 v114, v4, v5\n" \
"v_mfma_f32_16x16x32_bf16 v[12:15], a[116:119], a[84:87], v[12:15]\n" \
"global_load_lds_dwordx4 v222, s[36:37] offset:1024 sc1\n" \
"v_cvt_pk_bf16_f32 v115, v6, v7\n" \
"v_mfma_f32_16x16x32_bf16 v[56:59], a[96:99], a[88:91], v[56:59]\n" \
"ds_read_b64_tr_b16 a[200:201], v210 offset:2048\n" \
"v_mfma_f32_16x16x32_bf16 v[56:59], a[100:103], a[92:95], v[56:59]\n" \
"ds_read_b64_tr_b16 a[202:203], v210 offset:3072\n" \
"v_mfma_f32_16x16x32_bf16 v[16:19], a[104:107], a[88:91], v[16:19]\n" \
"ds_read_b64_tr_b16 a[204:205], v210 offset:14336\n" \
"v_cvt_pk_bf16_f32 v116, v8, v9\n" \
"v_mfma_f32_16x16x32_bf16 v[16:19], a[108:111], a[92:95], v[16:19]\n" \
"global_load_lds_dwordx4 v222, s[36:37] offset:2048 sc1\n" \
"v_cvt_pk_bf16_f32 v117, v10, v11\n" \
"v_mfma_f32_16x16x32_bf16 v[20:23], a[112:115], a[88:91], v[20:23]\n" \
"ds_read_b64_tr_b16 a[206:207], v210 offset:15360\n" \
"v_cvt_pk_bf16_f32 v118, v12, v13\n" \
"v_mfma_f32_16x16x32_bf16 v[20:23], a[116:119], a[92:95], v[20:23]\n" \
"s_waitcnt lgkmcnt(3)\n" \
"v_cvt_pk_bf16_f32 v119, v14, v15\n" \
"v_mfma_f32_16x16x32_bf16 v[64:67], a[96:99], a[192:195], v[64:67]\n" \
"v_mfma_f32_16x16x32_bf16 v[64:67], a[100:103], a[196:199], v[64:67]\n" \
"global_load_lds_dwordx4 v222, s[36:37] offset:3072 sc1\n" \
"v_mfma_f32_16x16x32_bf16 v[24:27], a[104:107], a[192:195], v[24:27]\n" \
"v_cvt_pk_bf16_f32 v120, v16, v17\n" \
"v_cvt_pk_bf16_f32 v121, v18, v19\n" \
"v_mfma_f32_16x16x32_bf16 v[24:27], a[108:111], a[196:199], v[24:27]\n" \
"v_cvt_pk_bf16_f32 v122, v20, v21\n" \
"v_cvt_pk_bf16_f32 v123, v22, v23\n" \
"v_mfma_f32_16x16x32_bf16 v[28:31], a[112:115], a[192:195], v[28:31]\n" \
"s_waitcnt vmcnt(12) lgkmcnt(0)\n" \
"v_mfma_f32_16x16x32_bf16 v[28:31], a[116:119], a[196:199], v[28:31]\n" \
"s_barrier\n" \
"v_mfma_f32_16x16x32_bf16 v[72:75], a[64:67], a[208:211], v[72:75]\n" \
"ds_read_b64_tr_b16 a[32:33], v214 offset:16384\n" \
"v_mfma_f32_16x16x32_bf16 v[72:75], a[68:71], a[212:215], v[72:75]\n" \
"v_mfma_f32_16x16x32_bf16 v[44:47], a[200:203], a[72:75], v[44:47]\n" \
"s_add_u32 s36, s36, 0x1000\n" \
"s_add_u32 m0, m0, 0x5000\n" \
"ds_read_b64_tr_b16 a[34:35], v214 offset:18432\n" \
"v_mfma_f32_16x16x32_bf16 v[44:47], a[204:207], a[76:79], v[44:47]\n" \
"ds_read_b64_tr_b16 a[36:37], v214 offset:24576\n" \
"global_load_lds_dwordx4 v216, s[38:39] offset:-4096\n" \
"v_mfma_f32_16x16x32_bf16 v[80:83], a[72:75], a[208:211], v[80:83]\n" \
"v_cvt_pk_bf16_f32 v124, v24, v25\n" \
"v_cvt_pk_bf16_f32 v125, v26, v27\n" \
"v_mfma_f32_16x16x32_bf16 v[80:83], a[76:79], a[212:215], v[80:83]\n" \
"ds_read_b64_tr_b16 a[38:39], v214 offset:26624\n" \
"v_cvt_pk_bf16_f32 v126, v28, v29\n" \
"v_mfma_f32_16x16x32_bf16 v[84:87], a[72:75], a[216:219], v[84:87]\n" \
"ds_read_b128 a[0:3], v213 offset:0\n" \
"v_cvt_pk_bf16_f32 v127, v30, v31\n" \
"v_mfma_f32_16x16x32_bf16 v[84:87], a[76:79], a[220:223], v[84:87]\n" \
"global_load_lds_dwordx4 v217, s[38:39] offset:-3072\n" \
"v_cvt_pk_bf16_f32 v128, v32, v33\n" \
"v_mfma_f32_16x16x32_bf16 v[52:55], a[200:203], a[80:83], v[52:55]\n" \
"ds_read_b128 a[4:7], v213 offset:2048\n" \
"v_cvt_pk_bf16_f32 v129, v34, v35\n" \
"v_mfma_f32_16x16x32_bf16 v[52:55], a[204:207], a[84:87], v[52:55]\n" \
"ds_read_b64_tr_b16 a[40:41], v215 offset:16384\n" \
"v_cvt_pk_bf16_f32 v130, v72, v73\n" \
"v_mfma_f32_16x16x32_bf16 v[88:91], a[80:83], a[208:211], v[88:91]\n" \
"v_cvt_pk_bf16_f32 v131, v74, v75\n" \
"v_mfma_f32_16x16x32_bf16 v[88:91], a[84:87], a[212:215], v[88:91]\n" \
"ds_read_b64_tr_b16 a[42:43], v215 offset:18432\n" \
"global_load_lds_dwordx4 v218, s[38:39] offset:-2048\n" \
"v_mfma_f32_16x16x32_bf16 v[92:95], a[80:83], a[216:219], v[92:95]\n" \
"ds_read_b64_tr_b16 a[44:45], v215 offset:24576\n" \
"v_mfma_f32_16x16x32_bf16 v[92:95], a[84:87], a[220:223], v[92:95]\n" \
"ds_write_b128 v145, v[128:131]\n" \
"v_cvt_pk_bf16_f32 v132, v40, v41\n" \
"v_mfma_f32_16x16x32_bf16 v[60:63], a[200:203], a[88:91], v[60:63]\n" \
"ds_read_b64_tr_b16 a[46:47], v215 offset:26624\n" \
"v_cvt_pk_bf16_f32 v133, v42, v43\n" \
"v_mfma_f32_16x16x32_bf16 v[60:63], a[204:207], a[92:95], v[60:63]\n" \
"ds_read_b128 a[8:11], v213 offset:256\n" \
"global_load_lds_dwordx4 v219, s[38:39] offset:-1024\n" \
"v_mfma_f32_16x16x32_bf16 v[96:99], a[88:91], a[208:211], v[96:99]\n" \
"s_add_u32 s38, s38, 0x20000\n" \
"s_addc_u32 s39, s39, 0\n" \
"v_cvt_pk_bf16_f32 v134, v44, v45\n" \
"v_mfma_f32_16x16x32_bf16 v[96:99], a[92:95], a[212:215], v[96:99]\n" \
"s_sub_u32 s75, s75, 32768\n" \
"s_cselect_b32 s75, 131072, s75\n" \
"ds_read_b128 a[12:15], v213 offset:2304\n" \
"v_mfma_f32_16x16x32_bf16 v[100:103], a[88:91], a[216:219], v[100:103]\n" \
"s_add_u32 m0, s75, s95\n" \
"ds_read_b128 a[16:19], v213 offset:4096\n" \
"v_cvt_pk_bf16_f32 v135, v46, v47\n" \
"v_mfma_f32_16x16x32_bf16 v[100:103], a[92:95], a[220:223], v[100:103]\n" \
"global_load_lds_dwordx4 v222, s[36:37] offset:0 sc1\n" \
"v_cvt_pk_bf16_f32 v136, v48, v49\n" \
"v_mfma_f32_16x16x32_bf16 v[68:71], a[200:203], a[192:195], v[68:71]\n" \
"ds_read_b128 a[20:23], v213 offset:6144\n" \
"v_cvt_pk_bf16_f32 v137, v50, v51\n" \
"v_mfma_f32_16x16x32_bf16 v[68:71], a[204:207], a[196:199], v[68:71]\n" \
"v_cvt_pk_bf16_f32 v138, v52, v53\n" \
"v_cvt_pk_bf16_f32 v139, v54, v55\n" \
"v_mfma_f32_16x16x32_bf16 v[104:107], a[192:195], a[208:211], v[104:107]\n" \
"v_cvt_pk_bf16_f32 v140, v56, v57\n" \
"v_cvt_pk_bf16_f32 v141, v58, v59\n" \
"v_mfma_f32_16x16x32_bf16 v[104:107], a[196:199], a[212:215], v[104:107]\n" \
"global_load_lds_dwordx4 v222, s[36:37] offset:1024 sc1\n" \
"v_cvt_pk_bf16_f32 v142, v60, v61\n" \
"v_mfma_f32_16x16x32_bf16 v[108:111], a[192:195], a[216:219], v[108:111]\n" \
"v_cvt_pk_bf16_f32 v143, v62, v63\n" \
"v_mfma_f32_16x16x32_bf16 v[108:111], a[196:199], a[220:223], v[108:111]\n" \
"s_waitcnt lgkmcnt(0)\n" \
"v_mfma_f32_16x16x32_bf16 v[0:3], a[32:35], a[0:3], 0\n" \
"ds_read_b128 a[24:27], v213 offset:4352\n" \
"v_mfma_f32_16x16x32_bf16 v[0:3], a[36:39], a[4:7], v[0:3]\n" \
"ds_read_b128 a[28:31], v213 offset:6400\n" \
"global_load_lds_dwordx4 v222, s[36:37] offset:2048 sc1\n" \
"v_mfma_f32_16x16x32_bf16 v[4:7], a[40:43], a[0:3], 0\n" \
"s_sub_u32 s74, s74, 32768\n" \
"s_cselect_b32 s74, 131072, s74\n" \
"ds_read_b64_tr_b16 a[48:49], v214 offset:16896\n" \
"v_mfma_f32_16x16x32_bf16 v[4:7], a[44:47], a[4:7], v[4:7]\n" \
"ds_read_b64_tr_b16 a[50:51], v214 offset:18944\n" \
"v_cvt_pk_bf16_f32 v64, v64, v65\n" \
"v_mfma_f32_16x16x32_bf16 v[20:23], a[40:43], a[8:11], 0\n" \
"ds_read_b64_tr_b16 a[52:53], v214 offset:25088\n" \
"global_load_lds_dwordx4 v222, s[36:37] offset:3072 sc1\n" \
"v_mfma_f32_16x16x32_bf16 v[20:23], a[44:47], a[12:15], v[20:23]\n" \
"s_add_u32 s36, s36, 0x1000\n" \
"ds_read_b64_tr_b16 a[54:55], v214 offset:27136\n" \
"v_cvt_pk_bf16_f32 v65, v66, v67\n" \
"v_mfma_f32_16x16x32_bf16 v[16:19], a[32:35], a[8:11], 0\n" \
"s_add_u32 m0, m0, 0x5000\n" \
"ds_read_b64_tr_b16 a[56:57], v215 offset:16896\n" \
"v_cvt_pk_bf16_f32 v66, v68, v69\n" \
"v_mfma_f32_16x16x32_bf16 v[16:19], a[36:39], a[12:15], v[16:19]\n" \
"ds_read_b64_tr_b16 a[58:59], v215 offset:18944\n" \
"global_load_lds_dwordx4 v216, s[38:39] offset:-4096\n" \
"v_mfma_f32_16x16x32_bf16 v[32:35], a[32:35], a[16:19], 0\n" \
"ds_read_b64_tr_b16 a[60:61], v215 offset:25088\n" \
"v_cvt_pk_bf16_f32 v67, v70, v71\n" \
"v_mfma_f32_16x16x32_bf16 v[32:35], a[36:39], a[20:23], v[32:35]\n" \
"ds_read_b64_tr_b16 a[62:63], v215 offset:27136\n" \
"v_cvt_pk_bf16_f32 v68, v72, v73\n" \
"v_mfma_f32_16x16x32_bf16 v[36:39], a[40:43], a[16:19], 0\n" \
"global_load_lds_dwordx4 v217, s[38:39] offset:-3072\n" \
"v_add_u32 v213, s74, v211\n" \
"v_mfma_f32_16x16x32_bf16 v[36:39], a[44:47], a[20:23], v[36:39]\n" \
"s_waitcnt lgkmcnt(3)\n" \
"v_add_u32 v214, s74, v212\n" \
"v_xor_b32 v215, 32, v214\n" \
"v_mfma_f32_16x16x32_bf16 v[52:55], a[40:43], a[24:27], 0\n" \
"v_cvt_pk_bf16_f32 v69, v74, v75\n" \
"v_cvt_pk_bf16_f32 v70, v80, v81\n" \
"v_mfma_f32_16x16x32_bf16 v[52:55], a[44:47], a[28:31], v[52:55]\n" \
"global_load_lds_dwordx4 v218, s[38:39] offset:-2048\n" \
"v_cvt_pk_bf16_f32 v71, v82, v83\n" \
"v_mfma_f32_16x16x32_bf16 v[48:51], a[32:35], a[24:27], 0\n" \
"s_waitcnt vmcnt(15) lgkmcnt(0)\n" \
"global_load_lds_dwordx4 v219, s[38:39] offset:-1024\n" \
"v_cvt_pk_bf16_f32 v194, v84, v85\n" \
"v_mfma_f32_16x16x32_bf16 v[48:51], a[36:39], a[28:31], v[48:51]\n" \
"s_barrier\n" \
"v_cvt_pk_bf16_f32 v195, v86, v87\n" \
"v_mfma_f32_16x16x32_bf16 v[8:11], a[48:51], a[0:3], 0\n" \
"ds_read_b64 v[130:131], v223\n" \
"ds_read_b64_tr_b16 a[32:33], v214 offset:16384\n" \
"v_mfma_f32_16x16x32_bf16 v[8:11], a[52:55], a[4:7], v[8:11]\n" \
"ds_read_b64 v[192:193], v223 offset:8\n" \
"ds_read_b64_tr_b16 a[34:35], v214 offset:18432\n" \
"v_mfma_f32_16x16x32_bf16 v[12:15], a[56:59], a[0:3], 0\n" \
"s_add_u32 s38, s38, 0x20000\n" \
"s_addc_u32 s39, s39, 0\n" \
"ds_read_b64_tr_b16 a[36:37], v214 offset:24576\n" \
"v_mfma_f32_16x16x32_bf16 v[12:15], a[60:63], a[4:7], v[12:15]\n" \
"s_sub_u32 s75, s75, 32768\n" \
"s_cselect_b32 s75, 131072, s75\n" \
"ds_read_b128 a[0:3], v213 offset:0\n" \
"v_mfma_f32_16x16x32_bf16 v[28:31], a[56:59], a[8:11], 0\n" \
"s_add_u32 m0, s75, s95\n" \
"ds_read_b128 a[4:7], v213 offset:2048\n" \
"v_cvt_pk_bf16_f32 v72, v88, v89\n" \
"v_mfma_f32_16x16x32_bf16 v[28:31], a[60:63], a[12:15], v[28:31]\n" \
"ds_read_b64_tr_b16 a[38:39], v214 offset:26624\n" \
"global_load_lds_dwordx4 v222, s[36:37] offset:0 sc1\n" \
"v_mfma_f32_16x16x32_bf16 v[24:27], a[48:51], a[8:11], 0\n" \
"ds_read_b64_tr_b16 a[40:41], v215 offset:16384\n" \
"v_cvt_pk_bf16_f32 v73, v90, v91\n" \
"v_mfma_f32_16x16x32_bf16 v[24:27], a[52:55], a[12:15], v[24:27]\n" \
"s_waitcnt lgkmcnt(4)\n" \
"ds_read_b128 a[8:11], v213 offset:256\n" \
"global_load_lds_dwordx4 v222, s[36:37] offset:1024 sc1\n" \
"v_mfma_f32_16x16x32_bf16 v[40:43], a[48:51], a[16:19], 0\n" \
"ds_read_b128 a[12:15], v213 offset:2304\n" \
"v_cvt_pk_bf16_f32 v196, v92, v93\n" \
"v_mfma_f32_16x16x32_bf16 v[40:43], a[52:55], a[20:23], v[40:43]\n" \
"ds_read_b64_tr_b16 a[42:43], v215 offset:18432\n" \
"v_cvt_pk_bf16_f32 v197, v94, v95\n" \
"v_mfma_f32_16x16x32_bf16 v[44:47], a[56:59], a[16:19], 0\n" \
"ds_read_b64_tr_b16 a[44:45], v215 offset:24576\n" \
"global_load_lds_dwordx4 v222, s[36:37] offset:2048 sc1\n" \
"v_mfma_f32_16x16x32_bf16 v[44:47], a[60:63], a[20:23], v[44:47]\n" \
"ds_read_b128 a[16:19], v213 offset:4096\n" \
"v_cvt_pk_bf16_f32 v74, v96, v97\n" \
"v_mfma_f32_16x16x32_bf16 v[60:63], a[56:59], a[24:27], 0\n" \
"ds_read_b128 a[20:23], v213 offset:6144\n" \
"global_load_lds_dwordx4 v222, s[36:37] offset:3072 sc1\n" \
"v_mfma_f32_16x16x32_bf16 v[60:63], a[60:63], a[28:31], v[60:63]\n" \
"ds_read_b64_tr_b16 a[46:47], v215 offset:26624\n" \
"v_cvt_pk_bf16_f32 v75, v98, v99\n" \
"v_mfma_f32_16x16x32_bf16 v[56:59], a[48:51], a[24:27], 0\n" \
"v_cvt_pk_bf16_f32 v198, v100, v101\n" \
"v_cvt_pk_bf16_f32 v199, v102, v103\n" \
"v_mfma_f32_16x16x32_bf16 v[56:59], a[52:55], a[28:31], v[56:59]\n" \
"s_waitcnt lgkmcnt(3)\n" \
"v_cvt_pk_bf16_f32 v76, v104, v105\n" \
"v_cvt_pk_bf16_f32 v77, v106, v107\n" \
"v_mfma_f32_16x16x32_bf16 v[0:3], a[32:35], a[0:3], v[0:3]\n" \
"v_cvt_pk_bf16_f32 v200, v108, v109\n" \
"v_cvt_pk_bf16_f32 v201, v110, v111\n" \
"v_mfma_f32_16x16x32_bf16 v[176:179], v[64:67], v[124:127], v[176:179]\n" \
"v_mfma_f32_16x16x32_bf16 v[172:175], v[140:143], v[124:127], v[172:175]\n" \
"s_add_u32 s36, s36, 0x1000\n" \
"ds_read_b128 a[24:27], v213 offset:4352\n" \
"s_add_u32 m0, m0, 0x5000\n" \
"v_mfma_f32_16x16x32_bf16 v[0:3], a[36:39], a[4:7], v[0:3]\n" \
"s_waitcnt lgkmcnt(1)\n" \
"ds_read_b128 a[28:31], v213 offset:6400\n" \
"global_load_lds_dwordx4 v216, s[38:39] offset:-4096\n" \
"v_mfma_f32_16x16x32_bf16 v[168:171], v[140:143], v[120:123], v[168:171]\n" \
"v_mfma_f32_16x16x32_bf16 v[164:167], v[136:139], v[120:123], v[164:167]\n" \
"v_mfma_f32_16x16x32_bf16 v[160:163], v[136:139], v[116:119], v[160:163]\n" \
"v_mfma_f32_16x16x32_bf16 v[156:159], v[132:135], v[116:119], v[156:159]\n" \
"v_mfma_f32_16x16x32_bf16 v[152:155], v[132:135], v[112:115], v[152:155]\n" \
"v_mfma_f32_16x16x32_bf16 v[148:151], v[128:131], v[112:115], v[148:151]\n" \
"v_mfma_f32_16x16x32_bf16 v[4:7], a[40:43], a[0:3], v[4:7]\n" \
"s_sub_u32 s74, s74, 32768\n" \
"s_cselect_b32 s74, 131072, s74\n" \
"ds_read_b64_tr_b16 a[48:49], v214 offset:16896\n" \
"v_mfma_f32_16x16x32_bf16 v[4:7], a[44:47], a[4:7], v[4:7]\n" \
"ds_read_b64_tr_b16 a[50:51], v214 offset:18944\n" \
"v_max3_f32 v183, v172, v173, v174\n" \
"v_max3_f32 v182, v164, v165, v166\n" \
"v_max3_f32 v181, v156, v157, v158\n" \
"v_max3_f32 v180, v148, v149, v150\n" \
"v_max3_f32 v183, v183, v175, v176\n" \
"v_max3_f32 v182, v182, v167, v168\n" \
"v_mfma_f32_16x16x32_bf16 v[20:23], a[40:43], a[8:11], v[20:23]\n" \
"ds_read_b64_tr_b16 a[52:53], v214 offset:25088\n" \
"global_load_lds_dwordx4 v217, s[38:39] offset:-3072\n" \
"v_mfma_f32_16x16x32_bf16 v[20:23], a[44:47], a[12:15], v[20:23]\n" \
"ds_read_b64_tr_b16 a[54:55], v214 offset:27136\n" \
"v_max3_f32 v181, v181, v159, v160\n" \
"v_max3_f32 v180, v180, v151, v152\n" \
"v_max3_f32 v183, v183, v177, v178\n" \
"v_max3_f32 v182, v182, v169, v170\n" \
"v_max3_f32 v181, v181, v161, v162\n" \
"v_max3_f32 v180, v180, v153, v154\n" \
"v_mfma_f32_16x16x32_bf16 v[16:19], a[32:35], a[8:11], v[16:19]\n" \
"ds_read_b64_tr_b16 a[56:57], v215 offset:16896\n" \
"v_max_f32 v183, v183, v179\n" \
"v_mfma_f32_16x16x32_bf16 v[16:19], a[36:39], a[12:15], v[16:19]\n" \
"ds_read_b64_tr_b16 a[58:59], v215 offset:18944\n" \
"global_load_lds_dwordx4 v218, s[38:39] offset:-2048\n" \
"v_max_f32 v182, v182, v171\n" \
"v_max_f32 v181, v181, v163\n" \
"v_max_f32 v180, v180, v155\n" \
"v_mov_b32 v187, v183\n" \
"v_mov_b32 v186, v182\n" \
"v_mov_b32 v185, v181\n" \
"v_mfma_f32_16x16x32_bf16 v[32:35], a[32:35], a[16:19], v[32:35]\n" \
"ds_read_b64_tr_b16 a[60:61], v215 offset:25088\n" \
"v_mov_b32 v184, v180\n" \
"v_mfma_f32_16x16x32_bf16 v[32:35], a[36:39], a[20:23], v[32:35]\n" \
"ds_read_b64_tr_b16 a[62:63], v215 offset:27136\n" \
"v_permlane16_swap_b32 v187, v183\n" \
"v_permlane16_swap_b32 v186, v182\n" \
"v_permlane16_swap_b32 v185, v181\n" \
"v_permlane16_swap_b32 v184, v180\n" \
"v_max_f32 v183, v183, v187\n" \
"v_max_f32 v182, v182, v186\n" \
"v_mfma_f32_16x16x32_bf16 v[36:39], a[40:43], a[16:19], v[36:39]\n" \
"global_load_lds_dwordx4 v219, s[38:39] offset:-1024\n" \
"v_add_u32 v213, s74, v211\n" \
"v_mfma_f32_16x16x32_bf16 v[36:39], a[44:47], a[20:23], v[36:39]\n" \
"s_waitcnt lgkmcnt(3)\n" \
"v_max_f32 v181, v181, v185\n" \
"v_max_f32 v180, v180, v184\n" \
"v_mov_b32 v187, v183\n" \
"v_mov_b32 v186, v182\n" \
"v_mov_b32 v185, v181\n" \
"v_mov_b32 v184, v180\n" \
"s_add_u32 s38, s38, 0x20000\n" \
"s_addc_u32 s39, s39, 0\n" \
"v_mfma_f32_16x16x32_bf16 v[52:55], a[40:43], a[24:27], v[52:55]\n" \
"s_sub_u32 s75, s75, 32768\n" \
"s_cselect_b32 s75, 131072, s75\n" \
"s_add_u32 m0, s75, s95\n" \
"v_mfma_f32_16x16x32_bf16 v[52:55], a[44:47], a[28:31], v[52:55]\n" \
"global_load_lds_dwordx4 v222, s[36:37] offset:0 sc1\n" \
"v_add_u32 v214, s74, v212\n" \
"v_permlane32_swap_b32 v187, v183\n" \
"v_permlane32_swap_b32 v186, v182\n" \
"v_permlane32_swap_b32 v185, v181\n" \
"v_permlane32_swap_b32 v184, v180\n" \
"v_max_f32 v183, v183, v187\n" \
"v_max_f32 v182, v182, v186\n" \
"v_mfma_f32_16x16x32_bf16 v[48:51], a[32:35], a[24:27], v[48:51]\n" \
"s_waitcnt vmcnt(17) lgkmcnt(0)\n" \
"v_xor_b32 v215, 32, v214\n" \
"v_max_f32 v181, v181, v185\n" \
"v_mfma_f32_16x16x32_bf16 v[48:51], a[36:39], a[28:31], v[48:51]\n" \
"s_barrier\n" \
"v_max_f32 v180, v180, v184\n" \
"v_mul_f32 v183, v183, v144\n" \
"v_mul_f32 v182, v182, v144\n" \
"v_mul_f32 v181, v181, v144\n" \
"v_mul_f32 v180, v180, v144\n" \
"v_fma_f32 v172, v172, v144, -v183\n" \
"v_mfma_f32_16x16x32_bf16 v[8:11], a[48:51], a[0:3], v[8:11]\n" \
"ds_read_b64_tr_b16 a[32:33], v214 offset:16384\n" \
"v_fma_f32 v164, v164, v144, -v182\n" \
"v_mfma_f32_16x16x32_bf16 v[8:11], a[52:55], a[4:7], v[8:11]\n" \
"ds_read_b64_tr_b16 a[34:35], v214 offset:18432\n" \
"global_load_lds_dwordx4 v222, s[36:37] offset:1024 sc1\n" \
"v_fma_f32 v156, v156, v144, -v181\n" \
"v_exp_f32 v172, v172\n" \
"v_fma_f32 v148, v148, v144, -v180\n" \
"v_exp_f32 v164, v164\n" \
"v_fma_f32 v173, v173, v144, -v183\n" \
"v_exp_f32 v156, v156\n" \
"v_mfma_f32_16x16x32_bf16 v[12:15], a[56:59], a[0:3], v[12:15]\n" \
"ds_read_b64_tr_b16 a[36:37], v214 offset:24576\n" \
"v_fma_f32 v165, v165, v144, -v182\n" \
"v_mfma_f32_16x16x32_bf16 v[12:15], a[60:63], a[4:7], v[12:15]\n" \
"ds_read_b128 a[0:3], v213 offset:0\n" \
"v_exp_f32 v148, v148\n" \
"v_fma_f32 v157, v157, v144, -v181\n" \
"v_exp_f32 v173, v173\n" \
"v_fma_f32 v149, v149, v144, -v180\n" \
"v_exp_f32 v165, v165\n" \
"v_mfma_f32_16x16x32_bf16 v[28:31], a[56:59], a[8:11], v[28:31]\n" \
"ds_read_b128 a[4:7], v213 offset:2048\n" \
"global_load_lds_dwordx4 v222, s[36:37] offset:2048 sc1\n" \
"v_mfma_f32_16x16x32_bf16 v[28:31], a[60:63], a[12:15], v[28:31]\n" \
"ds_read_b64_tr_b16 a[38:39], v214 offset:26624\n" \
"v_fma_f32 v174, v174, v144, -v183\n" \
"v_exp_f32 v157, v157\n" \
"v_mfma_f32_16x16x32_bf16 v[24:27], a[48:51], a[8:11], v[24:27]\n" \
"ds_read_b64_tr_b16 a[40:41], v215 offset:16384\n" \
"v_fma_f32 v166, v166, v144, -v182\n" \
"v_mfma_f32_16x16x32_bf16 v[24:27], a[52:55], a[12:15], v[24:27]\n" \
"ds_read_b128 a[8:11], v213 offset:256\n" \
"global_load_lds_dwordx4 v222, s[36:37] offset:3072 sc1\n" \
"v_exp_f32 v149, v149\n" \
"v_mfma_f32_16x16x32_bf16 v[40:43], a[48:51], a[16:19], v[40:43]\n" \
"s_add_u32 s36, s36, 0x1000\n" \
"ds_read_b128 a[12:15], v213 offset:2304\n" \
"v_fma_f32 v158, v158, v144, -v181\n" \
"v_mfma_f32_16x16x32_bf16 v[40:43], a[52:55], a[20:23], v[40:43]\n" \
"s_add_u32 m0, m0, 0x5000\n" \
"ds_read_b64_tr_b16 a[42:43], v215 offset:18432\n" \
"v_exp_f32 v174, v174\n" \
"v_add_f32 v191, v172, v173\n" \
"v_fma_f32 v150, v150, v144, -v180\n" \
"v_mfma_f32_16x16x32_bf16 v[44:47], a[56:59], a[16:19], v[44:47]\n" \
"ds_read_b64_tr_b16 a[44:45], v215 offset:24576\n" \
"global_load_lds_dwordx4 v216, s[38:39] offset:-4096\n" \
"v_mfma_f32_16x16x32_bf16 v[44:47], a[60:63], a[20:23], v[44:47]\n" \
"ds_read_b128 a[16:19], v213 offset:4096\n" \
"v_exp_f32 v166, v166\n" \
"v_add_f32 v190, v164, v165\n" \
"v_mfma_f32_16x16x32_bf16 v[60:63], a[56:59], a[24:27], v[60:63]\n" \
"ds_read_b128 a[20:23], v213 offset:6144\n" \
"global_load_lds_dwordx4 v217, s[38:39] offset:-3072\n" \
"v_mfma_f32_16x16x32_bf16 v[60:63], a[60:63], a[28:31], v[60:63]\n" \
"ds_read_b64_tr_b16 a[46:47], v215 offset:26624\n" \
"v_fma_f32 v175, v175, v144, -v183\n" \
"v_exp_f32 v158, v158\n" \
"v_mfma_f32_16x16x32_bf16 v[56:59], a[48:51], a[24:27], v[56:59]\n" \
"v_add_f32 v189, v156, v157\n" \
"v_fma_f32 v167, v167, v144, -v182\n" \
"v_mfma_f32_16x16x32_bf16 v[56:59], a[52:55], a[28:31], v[56:59]\n" \
"s_waitcnt lgkmcnt(3)\n" \
"v_exp_f32 v150, v150\n" \
"v_mfma_f32_16x16x32_bf16 v[0:3], a[32:35], a[0:3], v[0:3]\n" \
"ds_read_b128 a[24:27], v213 offset:4352\n" \
"v_add_f32 v188, v148, v149\n" \
"v_mfma_f32_16x16x32_bf16 v[0:3], a[36:39], a[4:7], v[0:3]\n" \
"s_waitcnt lgkmcnt(1)\n" \
"ds_read_b128 a[28:31], v213 offset:6400\n" \
"global_load_lds_dwordx4 v218, s[38:39] offset:-2048\n" \
"v_fma_f32 v159, v159, v144, -v181\n" \
"v_exp_f32 v175, v175\n" \
"v_mfma_f32_16x16x32_bf16 v[4:7], a[40:43], a[0:3], v[4:7]\n" \
"s_sub_u32 s74, s74, 32768\n" \
"s_cselect_b32 s74, 131072, s74\n" \
"ds_read_b64_tr_b16 a[48:49], v214 offset:16896\n" \
"v_mfma_f32_16x16x32_bf16 v[4:7], a[44:47], a[4:7], v[4:7]\n" \
"ds_read_b64_tr_b16 a[50:51], v214 offset:18944\n" \
"global_load_lds_dwordx4 v219, s[38:39] offset:-1024\n" \
"v_add_f32 v191, v191, v174\n" \
"v_fma_f32 v151, v151, v144, -v180\n" \
"v_mfma_f32_16x16x32_bf16 v[20:23], a[40:43], a[8:11], v[20:23]\n" \
"s_add_u32 s38, s38, 0x20000\n" \
"s_addc_u32 s39, s39, 0\n" \
"ds_read_b64_tr_b16 a[52:53], v214 offset:25088\n" \
"v_mfma_f32_16x16x32_bf16 v[20:23], a[44:47], a[12:15], v[20:23]\n" \
"s_sub_u32 s75, s75, 32768\n" \
"s_cselect_b32 s75, 131072, s75\n" \
"ds_read_b64_tr_b16 a[54:55], v214 offset:27136\n" \
"v_exp_f32 v167, v167\n" \
"v_add_f32 v190, v190, v166\n" \
"v_mfma_f32_16x16x32_bf16 v[16:19], a[32:35], a[8:11], v[16:19]\n" \
"s_add_u32 m0, s75, s95\n" \
"ds_read_b64_tr_b16 a[56:57], v215 offset:16896\n" \
"v_fma_f32 v176, v176, v144, -v183\n" \
"v_mfma_f32_16x16x32_bf16 v[16:19], a[36:39], a[12:15], v[16:19]\n" \
"ds_read_b64_tr_b16 a[58:59], v215 offset:18944\n" \
"global_load_lds_dwordx4 v222, s[36:37] offset:0 sc1\n" \
"v_exp_f32 v159, v159\n" \
"v_add_f32 v189, v189, v158\n" \
"v_mfma_f32_16x16x32_bf16 v[32:35], a[32:35], a[16:19], v[32:35]\n" \
"ds_read_b64_tr_b16 a[60:61], v215 offset:25088\n" \
"v_fma_f32 v168, v168, v144, -v182\n" \
"v_mfma_f32_16x16x32_bf16 v[32:35], a[36:39], a[20:23], v[32:35]\n" \
"ds_read_b64_tr_b16 a[62:63], v215 offset:27136\n" \
"v_exp_f32 v151, v151\n" \
"v_add_f32 v188, v188, v150\n" \
"v_fma_f32 v160, v160, v144, -v181\n" \
"v_mfma_f32_16x16x32_bf16 v[36:39], a[40:43], a[16:19], v[36:39]\n" \
"global_load_lds_dwordx4 v222, s[36:37] offset:1024 sc1\n" \
"v_add_u32 v213, s74, v211\n" \
"v_mfma_f32_16x16x32_bf16 v[36:39], a[44:47], a[20:23], v[36:39]\n" \
"s_waitcnt lgkmcnt(3)\n" \
"v_add_u32 v214, s74, v212\n" \
"v_xor_b32 v215, 32, v214\n" \
"v_exp_f32 v176, v176\n" \
"v_mfma_f32_16x16x32_bf16 v[52:55], a[40:43], a[24:27], v[52:55]\n" \
"v_add_f32 v191, v191, v175\n" \
"v_fma_f32 v152, v152, v144, -v180\n" \
"v_mfma_f32_16x16x32_bf16 v[52:55], a[44:47], a[28:31], v[52:55]\n" \
"global_load_lds_dwordx4 v222, s[36:37] offset:2048 sc1\n" \
"v_exp_f32 v168, v168\n" \
"v_mfma_f32_16x16x32_bf16 v[48:51], a[32:35], a[24:27], v[48:51]\n" \
"s_waitcnt vmcnt(19) lgkmcnt(0)\n" \
"v_add_f32 v190, v190, v167\n" \
"v_fma_f32 v177, v177, v144, -v183\n" \
"v_mfma_f32_16x16x32_bf16 v[48:51], a[36:39], a[28:31], v[48:51]\n" \
"s_barrier\n" \
"v_exp_f32 v160, v160\n" \
"v_mfma_f32_16x16x32_bf16 v[8:11], a[48:51], a[0:3], v[8:11]\n" \
"ds_read_b64_tr_b16 a[32:33], v214 offset:16384\n" \
"v_add_f32 v189, v189, v159\n" \
"v_mfma_f32_16x16x32_bf16 v[8:11], a[52:55], a[4:7], v[8:11]\n" \
"ds_read_b64_tr_b16 a[34:35], v214 offset:18432\n" \
"global_load_lds_dwordx4 v222, s[36:37] offset:3072 sc1\n" \
"v_fma_f32 v169, v169, v144, -v182\n" \
"v_exp_f32 v152, v152\n" \
"v_mfma_f32_16x16x32_bf16 v[12:15], a[56:59], a[0:3], v[12:15]\n" \
"s_add_u32 s36, s36, 0x1000\n" \
"ds_read_b64_tr_b16 a[36:37], v214 offset:24576\n" \
"v_add_f32 v188, v188, v151\n" \
"v_mfma_f32_16x16x32_bf16 v[12:15], a[60:63], a[4:7], v[12:15]\n" \
"s_add_u32 m0, m0, 0x5000\n" \
"ds_read_b128 a[0:3], v213 offset:0\n" \
"v_fma_f32 v161, v161, v144, -v181\n" \
"v_exp_f32 v177, v177\n" \
"v_mfma_f32_16x16x32_bf16 v[28:31], a[56:59], a[8:11], v[28:31]\n" \
"ds_read_b128 a[4:7], v213 offset:2048\n" \
"global_load_lds_dwordx4 v216, s[38:39] offset:-4096\n" \
"v_mfma_f32_16x16x32_bf16 v[28:31], a[60:63], a[12:15], v[28:31]\n" \
"ds_read_b64_tr_b16 a[38:39], v214 offset:26624\n" \
"v_add_f32 v191, v191, v176\n" \
"v_mfma_f32_16x16x32_bf16 v[24:27], a[48:51], a[8:11], v[24:27]\n" \
"ds_read_b64_tr_b16 a[40:41], v215 offset:16384\n" \
"v_fma_f32 v153, v153, v144, -v180\n" \
"v_mfma_f32_16x16x32_bf16 v[24:27], a[52:55], a[12:15], v[24:27]\n" \
"ds_read_b128 a[8:11], v213 offset:256\n" \
"global_load_lds_dwordx4 v217, s[38:39] offset:-3072\n" \
"v_exp_f32 v169, v169\n" \
"v_add_f32 v190, v190, v168\n" \
"v_mfma_f32_16x16x32_bf16 v[40:43], a[48:51], a[16:19], v[40:43]\n" \
"ds_read_b128 a[12:15], v213 offset:2304\n" \
"v_fma_f32 v178, v178, v144, -v183\n" \
"v_mfma_f32_16x16x32_bf16 v[40:43], a[52:55], a[20:23], v[40:43]\n" \
"ds_read_b64_tr_b16 a[42:43], v215 offset:18432\n" \
"v_exp_f32 v161, v161\n" \
"v_add_f32 v189, v189, v160\n" \
"v_fma_f32 v170, v170, v144, -v182\n" \
"v_mfma_f32_16x16x32_bf16 v[44:47], a[56:59], a[16:19], v[44:47]\n" \
"ds_read_b64_tr_b16 a[44:45], v215 offset:24576\n" \
"global_load_lds_dwordx4 v218, s[38:39] offset:-2048\n" \
"v_mfma_f32_16x16x32_bf16 v[44:47], a[60:63], a[20:23], v[44:47]\n" \
"ds_read_b128 a[16:19], v213 offset:4096\n" \
"v_exp_f32 v153, v153\n" \
"v_add_f32 v188, v188, v152\n" \
"v_fma_f32 v162, v162, v144, -v181\n" \
"v_mfma_f32_16x16x32_bf16 v[60:63], a[56:59], a[24:27], v[60:63]\n" \
"ds_read_b128 a[20:23], v213 offset:6144\n" \
"global_load_lds_dwordx4 v219, s[38:39] offset:-1024\n" \
"v_mfma_f32_16x16x32_bf16 v[60:63], a[60:63], a[28:31], v[60:63]\n" \
"s_add_u32 s38, s38, 0x20000\n" \
"s_addc_u32 s39, s39, 0\n" \
"Lpg11454_%=:\n" \
"s_cbranch_execz Lpg12602_%=\n" \
"ds_read_b64_tr_b16 a[46:47], v215 offset:26624\n" \
"v_exp_f32 v178, v178\n" \
"v_add_f32 v191, v191, v177\n" \
"v_mfma_f32_16x16x32_bf16 v[56:59], a[48:51], a[24:27], v[56:59]\n" \
"s_sub_u32 s75, s75, 32768\n" \
"s_cselect_b32 s75, 131072, s75\n" \
"v_fma_f32 v154, v154, v144, -v180\n" \
"v_mfma_f32_16x16x32_bf16 v[56:59], a[52:55], a[28:31], v[56:59]\n" \
"s_waitcnt lgkmcnt(3)\n" \
"v_exp_f32 v170, v170\n" \
"v_add_f32 v190, v190, v169\n" \
"v_mfma_f32_16x16x32_bf16 v[0:3], a[32:35], a[0:3], v[0:3]\n" \
"s_add_u32 m0, s75, s95\n" \
"ds_read_b128 a[24:27], v213 offset:4352\n" \
"v_fma_f32 v179, v179, v144, -v183\n" \
"v_mfma_f32_16x16x32_bf16 v[0:3], a[36:39], a[4:7], v[0:3]\n" \
"s_waitcnt lgkmcnt(1)\n" \
"ds_read_b128 a[28:31], v213 offset:6400\n" \
"global_load_lds_dwordx4 v222, s[36:37] offset:0 sc1\n" \
"v_exp_f32 v162, v162\n" \
"v_mfma_f32_16x16x32_bf16 v[4:7], a[40:43], a[0:3], v[4:7]\n" \
"s_sub_u32 s74, s74, 32768\n" \
"s_cselect_b32 s74, 131072, s74\n" \
"ds_read_b64_tr_b16 a[48:49], v214 offset:16896\n" \
"v_mfma_f32_16x16x32_bf16 v[4:7], a[44:47], a[4:7], v[4:7]\n" \
"ds_read_b64_tr_b16 a[50:51], v214 offset:18944\n" \
"v_add_f32 v189, v189, v161\n" \
"v_fma_f32 v171, v171, v144, -v182\n" \
"v_exp_f32 v154, v154\n" \
"v_mfma_f32_16x16x32_bf16 v[20:23], a[40:43], a[8:11], v[20:23]\n" \
"ds_read_b64_tr_b16 a[52:53], v214 offset:25088\n" \
"v_add_f32 v188, v188, v153\n" \
"v_mfma_f32_16x16x32_bf16 v[20:23], a[44:47], a[12:15], v[20:23]\n" \
"ds_read_b64_tr_b16 a[54:55], v214 offset:27136\n" \
"global_load_lds_dwordx4 v222, s[36:37] offset:1024 sc1\n" \
"v_mfma_f32_16x16x32_bf16 v[16:19], a[32:35], a[8:11], v[16:19]\n" \
"ds_read_b64_tr_b16 a[56:57], v215 offset:16896\n" \
"v_fma_f32 v163, v163, v144, -v181\n" \
"v_mfma_f32_16x16x32_bf16 v[16:19], a[36:39], a[12:15], v[16:19]\n" \
"ds_read_b64_tr_b16 a[58:59], v215 offset:18944\n" \
"v_exp_f32 v179, v179\n" \
"v_add_f32 v191, v191, v178\n" \
"v_mfma_f32_16x16x32_bf16 v[32:35], a[32:35], a[16:19], v[32:35]\n" \
"ds_read_b64_tr_b16 a[60:61], v215 offset:25088\n" \
"v_fma_f32 v155, v155, v144, -v180\n" \
"v_mfma_f32_16x16x32_bf16 v[32:35], a[36:39], a[20:23], v[32:35]\n" \
"ds_read_b64_tr_b16 a[62:63], v215 offset:27136\n" \
"global_load_lds_dwordx4 v222, s[36:37] offset:2048 sc1\n" \
"v_exp_f32 v171, v171\n" \
"v_add_f32 v190, v190, v170\n" \
"v_mfma_f32_16x16x32_bf16 v[36:39], a[40:43], a[16:19], v[36:39]\n" \
"v_add_u32 v213, s74, v211\n" \
"v_add_u32 v214, s74, v212\n" \
"v_mfma_f32_16x16x32_bf16 v[36:39], a[44:47], a[20:23], v[36:39]\n" \
"s_waitcnt lgkmcnt(3)\n" \
"v_xor_b32 v215, 32, v214\n" \
"v_exp_f32 v163, v163\n" \
"v_add_f32 v189, v189, v162\n" \
"v_exp_f32 v155, v155\n" \
"v_mfma_f32_16x16x32_bf16 v[52:55], a[40:43], a[24:27], v[52:55]\n" \
"v_add_f32 v188, v188, v154\n" \
"v_add_f32 v191, v191, v179\n" \
"v_mfma_f32_16x16x32_bf16 v[52:55], a[44:47], a[28:31], v[52:55]\n" \
"global_load_lds_dwordx4 v222, s[36:37] offset:3072 sc1\n" \
"v_add_f32 v190, v190, v171\n" \
"v_mfma_f32_16x16x32_bf16 v[48:51], a[32:35], a[24:27], v[48:51]\n" \
"s_waitcnt vmcnt(20) lgkmcnt(0)\n" \
"v_add_f32 v189, v189, v163\n" \
"s_add_u32 s36, s36, 0x1000\n" \
"v_mfma_f32_16x16x32_bf16 v[48:51], a[36:39], a[28:31], v[48:51]\n" \
"s_barrier\n" \
"v_add_f32 v188, v188, v155\n" \
"v_mfma_f32_16x16x32_bf16 v[8:11], a[48:51], a[0:3], v[8:11]\n" \
"s_add_u32 m0, m0, 0x5000\n" \
"ds_read_b64_tr_b16 a[32:33], v214 offset:16384\n" \
"v_mov_b32 v187, v191\n" \
"v_mfma_f32_16x16x32_bf16 v[8:11], a[52:55], a[4:7], v[8:11]\n" \
"ds_read_b64_tr_b16 a[34:35], v214 offset:18432\n" \
"global_load_lds_dwordx4 v216, s[38:39] offset:-4096\n" \
"v_mfma_f32_16x16x32_bf16 v[12:15], a[56:59], a[0:3], v[12:15]\n" \
"ds_read_b64_tr_b16 a[36:37], v214 offset:24576\n" \
"v_mov_b32 v186, v190\n" \
"v_mfma_f32_16x16x32_bf16 v[12:15], a[60:63], a[4:7], v[12:15]\n" \
"ds_read_b128 a[0:3], v213 offset:0\n" \
"v_mov_b32 v185, v189\n" \
"v_mfma_f32_16x16x32_bf16 v[28:31], a[56:59], a[8:11], v[28:31]\n" \
"ds_read_b128 a[4:7], v213 offset:2048\n" \
"v_mov_b32 v184, v188\n" \
"v_mfma_f32_16x16x32_bf16 v[28:31], a[60:63], a[12:15], v[28:31]\n" \
"ds_read_b64_tr_b16 a[38:39], v214 offset:26624\n" \
"global_load_lds_dwordx4 v217, s[38:39] offset:-3072\n" \
"v_permlane16_swap_b32 v187, v191\n" \
"v_permlane16_swap_b32 v186, v190\n" \
"v_permlane16_swap_b32 v185, v189\n" \
"v_permlane16_swap_b32 v184, v188\n" \
"v_add_f32 v191, v191, v187\n" \
"v_mfma_f32_16x16x32_bf16 v[24:27], a[48:51], a[8:11], v[24:27]\n" \
"ds_read_b64_tr_b16 a[40:41], v215 offset:16384\n" \
"v_add_f32 v190, v190, v186\n" \
"v_mfma_f32_16x16x32_bf16 v[24:27], a[52:55], a[12:15], v[24:27]\n" \
"ds_read_b128 a[8:11], v213 offset:256\n" \
"v_add_f32 v189, v189, v185\n" \
"v_add_f32 v188, v188, v184\n" \
"v_mfma_f32_16x16x32_bf16 v[40:43], a[48:51], a[16:19], v[40:43]\n" \
"ds_read_b128 a[12:15], v213 offset:2304\n" \
"v_mov_b32 v187, v191\n" \
"v_mfma_f32_16x16x32_bf16 v[40:43], a[52:55], a[20:23], v[40:43]\n" \
"ds_read_b64_tr_b16 a[42:43], v215 offset:18432\n" \
"global_load_lds_dwordx4 v218, s[38:39] offset:-2048\n" \
"v_mfma_f32_16x16x32_bf16 v[44:47], a[56:59], a[16:19], v[44:47]\n" \
"ds_read_b64_tr_b16 a[44:45], v215 offset:24576\n" \
"v_mov_b32 v186, v190\n" \
"v_mfma_f32_16x16x32_bf16 v[44:47], a[60:63], a[20:23], v[44:47]\n" \
"ds_read_b128 a[16:19], v213 offset:4096\n" \
"v_mov_b32 v185, v189\n" \
"v_mfma_f32_16x16x32_bf16 v[60:63], a[56:59], a[24:27], v[60:63]\n" \
"ds_read_b128 a[20:23], v213 offset:6144\n" \
"global_load_lds_dwordx4 v219, s[38:39] offset:-1024\n" \
"v_mfma_f32_16x16x32_bf16 v[60:63], a[60:63], a[28:31], v[60:63]\n" \
"s_add_u32 s38, s38, 0x20000\n" \
"s_addc_u32 s39, s39, 0\n" \
"ds_read_b64_tr_b16 a[46:47], v215 offset:26624\n" \
"v_mfma_f32_16x16x32_bf16 v[56:59], a[48:51], a[24:27], v[56:59]\n" \
"s_sub_u32 s75, s75, 32768\n" \
"s_cselect_b32 s75, 131072, s75\n" \
"v_mov_b32 v184, v188\n" \
"v_mfma_f32_16x16x32_bf16 v[56:59], a[52:55], a[28:31], v[56:59]\n" \
"s_waitcnt lgkmcnt(3)\n" \
"v_permlane32_swap_b32 v187, v191\n" \
"v_permlane32_swap_b32 v186, v190\n" \
"v_permlane32_swap_b32 v185, v189\n" \
"v_permlane32_swap_b32 v184, v188\n" \
"v_add_f32 v191, v191, v187\n" \
"v_mfma_f32_16x16x32_bf16 v[0:3], a[32:35], a[0:3], v[0:3]\n" \
"s_add_u32 m0, s75, s95\n" \
"ds_read_b128 a[24:27], v213 offset:4352\n" \
"v_add_f32 v190, v190, v186\n" \
"v_mfma_f32_16x16x32_bf16 v[0:3], a[36:39], a[4:7], v[0:3]\n" \
"s_waitcnt lgkmcnt(1)\n" \
"ds_read_b128 a[28:31], v213 offset:6400\n" \
"global_load_lds_dwordx4 v222, s[36:37] offset:0 sc1\n" \
"v_mfma_f32_16x16x32_bf16 v[4:7], a[40:43], a[0:3], v[4:7]\n" \
"s_sub_u32 s74, s74, 32768\n" \
"s_cselect_b32 s74, 131072, s74\n" \
"ds_read_b64_tr_b16 a[48:49], v214 offset:16896\n" \
"v_mfma_f32_16x16x32_bf16 v[4:7], a[44:47], a[4:7], v[4:7]\n" \
"ds_read_b64_tr_b16 a[50:51], v214 offset:18944\n" \
"v_add_f32 v189, v189, v185\n" \
"v_mfma_f32_16x16x32_bf16 v[20:23], a[40:43], a[8:11], v[20:23]\n" \
"ds_read_b64_tr_b16 a[52:53], v214 offset:25088\n" \
"v_add_f32 v188, v188, v184\n" \
"v_mfma_f32_16x16x32_bf16 v[20:23], a[44:47], a[12:15], v[20:23]\n" \
"ds_read_b64_tr_b16 a[54:55], v214 offset:27136\n" \
"global_load_lds_dwordx4 v222, s[36:37] offset:1024 sc1\n" \
"v_rcp_f32 v191, v191\n" \
"v_rcp_f32 v190, v190\n" \
"v_rcp_f32 v189, v189\n" \
"v_rcp_f32 v188, v188\n" \
"v_mul_f32 v172, v172, v191\n" \
"v_mfma_f32_16x16x32_bf16 v[16:19], a[32:35], a[8:11], v[16:19]\n" \
"ds_read_b64_tr_b16 a[56:57], v215 offset:16896\n" \
"v_mul_f32 v164, v164, v190\n" \
"v_mfma_f32_16x16x32_bf16 v[16:19], a[36:39], a[12:15], v[16:19]\n" \
"ds_read_b64_tr_b16 a[58:59], v215 offset:18944\n" \
"v_mul_f32 v156, v156, v189\n" \
"v_mul_f32 v148, v148, v188\n" \
"v_mfma_f32_16x16x32_bf16 v[32:35], a[32:35], a[16:19], v[32:35]\n" \
"ds_read_b64_tr_b16 a[60:61], v215 offset:25088\n" \
"v_mul_f32 v173, v173, v191\n" \
"v_mfma_f32_16x16x32_bf16 v[32:35], a[36:39], a[20:23], v[32:35]\n" \
"ds_read_b64_tr_b16 a[62:63], v215 offset:27136\n" \
"global_load_lds_dwordx4 v222, s[36:37] offset:2048 sc1\n" \
"v_mul_f32 v165, v165, v190\n" \
"v_mul_f32 v157, v157, v189\n" \
"v_mul_f32 v149, v149, v188\n" \
"v_mfma_f32_16x16x32_bf16 v[36:39], a[40:43], a[16:19], v[36:39]\n" \
"v_add_u32 v213, s74, v211\n" \
"v_add_u32 v214, s74, v212\n" \
"v_mfma_f32_16x16x32_bf16 v[36:39], a[44:47], a[20:23], v[36:39]\n" \
"s_waitcnt lgkmcnt(3)\n" \
"v_xor_b32 v215, 32, v214\n" \
"v_mul_f32 v174, v174, v191\n" \
"v_mul_f32 v166, v166, v190\n" \
"v_mul_f32 v158, v158, v189\n" \
"v_mfma_f32_16x16x32_bf16 v[52:55], a[40:43], a[24:27], v[52:55]\n" \
"v_mul_f32 v150, v150, v188\n" \
"v_mul_f32 v175, v175, v191\n" \
"v_mfma_f32_16x16x32_bf16 v[52:55], a[44:47], a[28:31], v[52:55]\n" \
"global_load_lds_dwordx4 v222, s[36:37] offset:3072 sc1\n" \
"v_mul_f32 v167, v167, v190\n" \
"v_mul_f32 v159, v159, v189\n" \
"v_mfma_f32_16x16x32_bf16 v[48:51], a[32:35], a[24:27], v[48:51]\n" \
"s_waitcnt vmcnt(20) lgkmcnt(0)\n" \
"v_mul_f32 v151, v151, v188\n" \
"s_add_u32 s36, s36, 0x1000\n" \
"v_mfma_f32_16x16x32_bf16 v[48:51], a[36:39], a[28:31], v[48:51]\n" \
"s_barrier\n" \
"v_mul_f32 v176, v176, v191\n" \
"v_mul_f32 v168, v168, v190\n" \
"v_mul_f32 v160, v160, v189\n" \
"v_mfma_f32_16x16x32_bf16 v[8:11], a[48:51], a[0:3], v[8:11]\n" \
"s_add_u32 m0, m0, 0x5000\n" \
"ds_read_b64_tr_b16 a[32:33], v214 offset:16384\n" \
"v_mul_f32 v152, v152, v188\n" \
"v_mfma_f32_16x16x32_bf16 v[8:11], a[52:55], a[4:7], v[8:11]\n" \
"ds_read_b64_tr_b16 a[34:35], v214 offset:18432\n" \
"global_load_lds_dwordx4 v216, s[38:39] offset:-4096\n" \
"v_mul_f32 v177, v177, v191\n" \
"v_mul_f32 v169, v169, v190\n" \
"v_mfma_f32_16x16x32_bf16 v[12:15], a[56:59], a[0:3], v[12:15]\n" \
"ds_read_b64_tr_b16 a[36:37], v214 offset:24576\n" \
"v_mul_f32 v161, v161, v189\n" \
"v_mfma_f32_16x16x32_bf16 v[12:15], a[60:63], a[4:7], v[12:15]\n" \
"ds_read_b128 a[0:3], v213 offset:0\n" \
"v_mul_f32 v153, v153, v188\n" \
"v_mul_f32 v178, v178, v191\n" \
"v_mul_f32 v170, v170, v190\n" \
"v_mfma_f32_16x16x32_bf16 v[28:31], a[56:59], a[8:11], v[28:31]\n" \
"ds_read_b128 a[4:7], v213 offset:2048\n" \
"v_mul_f32 v162, v162, v189\n" \
"v_mfma_f32_16x16x32_bf16 v[28:31], a[60:63], a[12:15], v[28:31]\n" \
"ds_read_b64_tr_b16 a[38:39], v214 offset:26624\n" \
"global_load_lds_dwordx4 v217, s[38:39] offset:-3072\n" \
"v_mul_f32 v154, v154, v188\n" \
"v_mul_f32 v179, v179, v191\n" \
"v_mfma_f32_16x16x32_bf16 v[24:27], a[48:51], a[8:11], v[24:27]\n" \
"ds_read_b64_tr_b16 a[40:41], v215 offset:16384\n" \
"v_mul_f32 v171, v171, v190\n" \
"v_mfma_f32_16x16x32_bf16 v[24:27], a[52:55], a[12:15], v[24:27]\n" \
"ds_read_b128 a[8:11], v213 offset:256\n" \
"v_mul_f32 v163, v163, v189\n" \
"v_mul_f32 v155, v155, v188\n" \
"v_cvt_pk_bf16_f32 v172, v172, v173\n" \
"v_mfma_f32_16x16x32_bf16 v[40:43], a[48:51], a[16:19], v[40:43]\n" \
"ds_read_b128 a[12:15], v213 offset:2304\n" \
"v_cvt_pk_bf16_f32 v173, v174, v175\n" \
"v_mfma_f32_16x16x32_bf16 v[40:43], a[52:55], a[20:23], v[40:43]\n" \
"ds_read_b64_tr_b16 a[42:43], v215 offset:18432\n" \
"global_load_lds_dwordx4 v218, s[38:39] offset:-2048\n" \
"v_cvt_pk_bf16_f32 v174, v176, v177\n" \
"v_cvt_pk_bf16_f32 v175, v178, v179\n" \
"v_mfma_f32_16x16x32_bf16 v[44:47], a[56:59], a[16:19], v[44:47]\n" \
"ds_read_b64_tr_b16 a[44:45], v215 offset:24576\n" \
"v_cvt_pk_bf16_f32 v164, v164, v165\n" \
"v_mfma_f32_16x16x32_bf16 v[44:47], a[60:63], a[20:23], v[44:47]\n" \
"ds_read_b128 a[16:19], v213 offset:4096\n" \
"v_cvt_pk_bf16_f32 v165, v166, v167\n" \
"v_cvt_pk_bf16_f32 v166, v168, v169\n" \
"v_cvt_pk_bf16_f32 v167, v170, v171\n" \
"v_mfma_f32_16x16x32_bf16 v[60:63], a[56:59], a[24:27], v[60:63]\n" \
"ds_read_b128 a[20:23], v213 offset:6144\n" \
"global_load_lds_dwordx4 v219, s[38:39] offset:-1024\n" \
"v_mfma_f32_16x16x32_bf16 v[60:63], a[60:63], a[28:31], v[60:63]\n" \
"s_add_u32 s38, s38, 0x20000\n" \
"s_addc_u32 s39, s39, 0\n" \
"ds_read_b64_tr_b16 a[46:47], v215 offset:26624\n" \
"v_mfma_f32_16x16x32_bf16 v[56:59], a[48:51], a[24:27], v[56:59]\n" \
"s_sub_u32 s75, s75, 32768\n" \
"s_cselect_b32 s75, 131072, s75\n" \
"v_cvt_pk_bf16_f32 v156, v156, v157\n" \
"v_mfma_f32_16x16x32_bf16 v[56:59], a[52:55], a[28:31], v[56:59]\n" \
"s_waitcnt lgkmcnt(3)\n" \
"v_cvt_pk_bf16_f32 v157, v158, v159\n" \
"v_cvt_pk_bf16_f32 v158, v160, v161\n" \
"v_mfma_f32_16x16x32_bf16 v[0:3], a[32:35], a[0:3], v[0:3]\n" \
"s_add_u32 m0, s75, s95\n" \
"ds_read_b128 a[24:27], v213 offset:4352\n" \
"v_cvt_pk_bf16_f32 v159, v162, v163\n" \
"v_mfma_f32_16x16x32_bf16 v[0:3], a[36:39], a[4:7], v[0:3]\n" \
"s_waitcnt lgkmcnt(1)\n" \
"ds_read_b128 a[28:31], v213 offset:6400\n" \
"global_load_lds_dwordx4 v222, s[36:37] offset:0 sc1\n" \
"v_mfma_f32_16x16x32_bf16 v[4:7], a[40:43], a[0:3], v[4:7]\n" \
"s_sub_u32 s74, s74, 32768\n" \
"s_cselect_b32 s74, 131072, s74\n" \
"ds_read_b64_tr_b16 a[48:49], v214 offset:16896\n" \
"v_mfma_f32_16x16x32_bf16 v[4:7], a[44:47], a[4:7], v[4:7]\n" \
"ds_read_b64_tr_b16 a[50:51], v214 offset:18944\n" \
"v_cvt_pk_bf16_f32 v148, v148, v149\n" \
"v_mfma_f32_16x16x32_bf16 v[20:23], a[40:43], a[8:11], v[20:23]\n" \
"ds_read_b64_tr_b16 a[52:53], v214 offset:25088\n" \
"v_cvt_pk_bf16_f32 v149, v150, v151\n" \
"v_mfma_f32_16x16x32_bf16 v[20:23], a[44:47], a[12:15], v[20:23]\n" \
"ds_read_b64_tr_b16 a[54:55], v214 offset:27136\n" \
"global_load_lds_dwordx4 v222, s[36:37] offset:1024 sc1\n" \
"v_mfma_f32_16x16x32_bf16 v[16:19], a[32:35], a[8:11], v[16:19]\n" \
"ds_read_b64_tr_b16 a[56:57], v215 offset:16896\n" \
"v_cvt_pk_bf16_f32 v150, v152, v153\n" \
"v_mfma_f32_16x16x32_bf16 v[16:19], a[36:39], a[12:15], v[16:19]\n" \
"ds_read_b64_tr_b16 a[58:59], v215 offset:18944\n" \
"v_cvt_pk_bf16_f32 v151, v154, v155\n" \
"v_mfma_f32_16x16x32_bf16 v[32:35], a[32:35], a[16:19], v[32:35]\n" \
"v_mfma_f32_16x16x32_bf16 v[136:139], v[74:77], v[172:175], 0\n" \
"v_mfma_f32_16x16x32_bf16 v[140:143], v[198:201], v[172:175], 0\n" \
"v_mfma_f32_16x16x32_bf16 v[128:131], v[72:75], v[164:167], 0\n" \
"v_mfma_f32_16x16x32_bf16 v[132:135], v[196:199], v[164:167], 0\n" \
"v_mfma_f32_16x16x32_bf16 v[120:123], v[70:73], v[156:159], 0\n" \
"v_mfma_f32_16x16x32_bf16 v[124:127], v[194:197], v[156:159], 0\n" \
"ds_read_b64_tr_b16 a[60:61], v215 offset:25088\n" \
"v_mfma_f32_16x16x32_bf16 v[32:35], a[36:39], a[20:23], v[32:35]\n" \
"v_mfma_f32_16x16x32_bf16 v[112:115], v[68:71], v[148:151], 0\n" \
"v_mfma_f32_16x16x32_bf16 v[116:119], v[192:195], v[148:151], 0\n" \
"ds_read_b64_tr_b16 a[62:63], v215 offset:27136\n" \
"global_load_lds_dwordx4 v222, s[36:37] offset:2048 sc1\n" \
"v_mfma_f32_16x16x32_bf16 v[36:39], a[40:43], a[16:19], v[36:39]\n" \
"v_add_u32 v213, s74, v211\n" \
"v_add_u32 v214, s74, v212\n" \
"v_mfma_f32_16x16x32_bf16 v[36:39], a[44:47], a[20:23], v[36:39]\n" \
"s_waitcnt lgkmcnt(3)\n" \
"v_xor_b32 v215, 32, v214\n" \
"v_cvt_pk_bf16_f32 v136, v136, v137\n" \
"v_cvt_pk_bf16_f32 v137, v138, v139\n" \
"v_mfma_f32_16x16x32_bf16 v[52:55], a[40:43], a[24:27], v[52:55]\n" \
"v_cvt_pk_bf16_f32 v138, v140, v141\n" \
"v_cvt_pk_bf16_f32 v139, v142, v143\n" \
"v_mfma_f32_16x16x32_bf16 v[52:55], a[44:47], a[28:31], v[52:55]\n" \
"global_load_lds_dwordx4 v222, s[36:37] offset:3072 sc1\n" \
"v_cndmask_b32 v140, v136, v138, s[92:93]\n" \
"v_mfma_f32_16x16x32_bf16 v[48:51], a[32:35], a[24:27], v[48:51]\n" \
"s_waitcnt vmcnt(20) lgkmcnt(0)\n" \
"v_cndmask_b32 v141, v137, v139, s[92:93]\n" \
"s_add_u32 s36, s36, 0x1000\n" \
"v_mfma_f32_16x16x32_bf16 v[48:51], a[36:39], a[28:31], v[48:51]\n" \
"s_barrier\n" \
"v_cndmask_b32 v142, v138, v136, s[92:93]\n" \
"v_mfma_f32_16x16x32_bf16 v[8:11], a[48:51], a[0:3], v[8:11]\n" \
"s_add_u32 m0, m0, 0x5000\n" \
"ds_read_b64_tr_b16 a[32:33], v214 offset:16384\n" \
"v_cndmask_b32 v143, v139, v137, s[92:93]\n" \
"v_mfma_f32_16x16x32_bf16 v[8:11], a[52:55], a[4:7], v[8:11]\n" \
"s_add_u32 s71, s87, 208896\n" \
"ds_read_b64_tr_b16 a[34:35], v214 offset:18432\n" \
"global_load_lds_dwordx4 v216, s[38:39] offset:-4096\n" \
"v_mfma_f32_16x16x32_bf16 v[12:15], a[56:59], a[0:3], v[12:15]\n" \
"ds_read_b64_tr_b16 a[36:37], v214 offset:24576\n" \
"buffer_store_dwordx4 v[140:143], v146, s[56:59], s71 offen sc1\n" \
"v_cvt_pk_bf16_f32 v128, v128, v129\n" \
"v_cvt_pk_bf16_f32 v129, v130, v131\n" \
"v_cvt_pk_bf16_f32 v130, v132, v133\n" \
"v_cvt_pk_bf16_f32 v131, v134, v135\n" \
"v_cndmask_b32 v132, v128, v130, s[92:93]\n" \
"v_mfma_f32_16x16x32_bf16 v[12:15], a[60:63], a[4:7], v[12:15]\n" \
"ds_read_b128 a[0:3], v213 offset:0\n" \
"v_cndmask_b32 v133, v129, v131, s[92:93]\n" \
"v_cndmask_b32 v134, v130, v128, s[92:93]\n" \
"v_mfma_f32_16x16x32_bf16 v[28:31], a[56:59], a[8:11], v[28:31]\n" \
"ds_read_b128 a[4:7], v213 offset:2048\n" \
"v_cndmask_b32 v135, v131, v129, s[92:93]\n" \
"v_mfma_f32_16x16x32_bf16 v[28:31], a[60:63], a[12:15], v[28:31]\n" \
"s_add_u32 s71, s87, 139264\n" \
"ds_read_b64_tr_b16 a[38:39], v214 offset:26624\n" \
"global_load_lds_dwordx4 v217, s[38:39] offset:-3072\n" \
"v_mfma_f32_16x16x32_bf16 v[24:27], a[48:51], a[8:11], v[24:27]\n" \
"ds_read_b64_tr_b16 a[40:41], v215 offset:16384\n" \
"buffer_store_dwordx4 v[132:135], v146, s[56:59], s71 offen sc1\n" \
"v_cvt_pk_bf16_f32 v120, v120, v121\n" \
"v_cvt_pk_bf16_f32 v121, v122, v123\n" \
"v_cvt_pk_bf16_f32 v122, v124, v125\n" \
"v_cvt_pk_bf16_f32 v123, v126, v127\n" \
"v_cndmask_b32 v124, v120, v122, s[92:93]\n" \
"v_mfma_f32_16x16x32_bf16 v[24:27], a[52:55], a[12:15], v[24:27]\n" \
"ds_read_b128 a[8:11], v213 offset:256\n" \
"v_cndmask_b32 v125, v121, v123, s[92:93]\n" \
"v_cndmask_b32 v126, v122, v120, s[92:93]\n" \
"v_mfma_f32_16x16x32_bf16 v[40:43], a[48:51], a[16:19], v[40:43]\n" \
"ds_read_b128 a[12:15], v213 offset:2304\n" \
"v_cndmask_b32 v127, v123, v121, s[92:93]\n" \
"v_mfma_f32_16x16x32_bf16 v[40:43], a[52:55], a[20:23], v[40:43]\n" \
"s_add_u32 s71, s87, 69632\n" \
"ds_read_b64_tr_b16 a[42:43], v215 offset:18432\n" \
"global_load_lds_dwordx4 v218, s[38:39] offset:-2048\n" \
"v_mfma_f32_16x16x32_bf16 v[44:47], a[56:59], a[16:19], v[44:47]\n" \
"ds_read_b64_tr_b16 a[44:45], v215 offset:24576\n" \
"buffer_store_dwordx4 v[124:127], v146, s[56:59], s71 offen sc1\n" \
"v_cvt_pk_bf16_f32 v112, v112, v113\n" \
"v_cvt_pk_bf16_f32 v113, v114, v115\n" \
"v_cvt_pk_bf16_f32 v114, v116, v117\n" \
"v_cvt_pk_bf16_f32 v115, v118, v119\n" \
"v_cndmask_b32 v116, v112, v114, s[92:93]\n" \
"v_mfma_f32_16x16x32_bf16 v[44:47], a[60:63], a[20:23], v[44:47]\n" \
"ds_read_b128 a[16:19], v213 offset:4096\n" \
"v_cndmask_b32 v117, v113, v115, s[92:93]\n" \
"v_cndmask_b32 v118, v114, v112, s[92:93]\n" \
"v_cndmask_b32 v119, v115, v113, s[92:93]\n" \
"v_mfma_f32_16x16x32_bf16 v[60:63], a[56:59], a[24:27], v[60:63]\n" \
"s_add_u32 s71, s87, 0\n" \
"ds_read_b128 a[20:23], v213 offset:6144\n" \
"global_load_lds_dwordx4 v219, s[38:39] offset:-1024\n" \
"v_mfma_f32_16x16x32_bf16 v[60:63], a[60:63], a[28:31], v[60:63]\n" \
"buffer_store_dwordx4 v[116:119], v146, s[56:59], s71 offen sc1\n" \
"s_add_u32 s38, s38, 0x20000\n" \
"s_addc_u32 s39, s39, 0\n" \
"ds_read_b64_tr_b16 a[46:47], v215 offset:26624\n" \
"v_mfma_f32_16x16x32_bf16 v[56:59], a[48:51], a[24:27], v[56:59]\n" \
"s_sub_u32 s75, s75, 32768\n" \
"s_cselect_b32 s75, 131072, s75\n" \
"v_mfma_f32_16x16x32_bf16 v[56:59], a[52:55], a[28:31], v[56:59]\n" \
"s_waitcnt lgkmcnt(3)\n" \
"v_mfma_f32_16x16x32_bf16 v[0:3], a[32:35], a[0:3], v[0:3]\n" \
"s_add_u32 m0, s75, s95\n" \
"ds_read_b128 a[24:27], v213 offset:4352\n" \
"v_mov_b32 v224, s60\n" \
"v_mfma_f32_16x16x32_bf16 v[0:3], a[36:39], a[4:7], v[0:3]\n" \
"s_waitcnt lgkmcnt(1)\n" \
"ds_read_b128 a[28:31], v213 offset:6400\n" \
"global_load_lds_dwordx4 v222, s[36:37] offset:0 sc1\n" \
"v_mfma_f32_16x16x32_bf16 v[4:7], a[40:43], a[0:3], v[4:7]\n" \
"s_sub_u32 s74, s74, 32768\n" \
"s_cselect_b32 s74, 131072, s74\n" \
"ds_read_b64_tr_b16 a[48:49], v214 offset:16896\n" \
"v_mfma_f32_16x16x32_bf16 v[4:7], a[44:47], a[4:7], v[4:7]\n" \
"ds_read_b64_tr_b16 a[50:51], v214 offset:18944\n" \
"v_lshlrev_b32 v225, 28, s61\n" \
"v_mfma_f32_16x16x32_bf16 v[20:23], a[40:43], a[8:11], v[20:23]\n" \
"ds_read_b64_tr_b16 a[52:53], v214 offset:25088\n" \
"v_mfma_f32_16x16x32_bf16 v[20:23], a[44:47], a[12:15], v[20:23]\n" \
"ds_read_b64_tr_b16 a[54:55], v214 offset:27136\n" \
"global_load_lds_dwordx4 v222, s[36:37] offset:1024 sc1\n" \
"v_mfma_f32_16x16x32_bf16 v[16:19], a[32:35], a[8:11], v[16:19]\n" \
"ds_read_b64_tr_b16 a[56:57], v215 offset:16896\n" \
"v_mfma_f32_16x16x32_bf16 v[16:19], a[36:39], a[12:15], v[16:19]\n" \
"ds_read_b64_tr_b16 a[58:59], v215 offset:18944\n" \
"v_mfma_f32_16x16x32_bf16 v[32:35], a[32:35], a[16:19], v[32:35]\n" \
"ds_read_b64_tr_b16 a[60:61], v215 offset:25088\n" \
"v_mfma_f32_16x16x32_bf16 v[32:35], a[36:39], a[20:23], v[32:35]\n" \
"ds_read_b64_tr_b16 a[62:63], v215 offset:27136\n" \
"global_load_lds_dwordx4 v222, s[36:37] offset:2048 sc1\n" \
"v_mfma_f32_16x16x32_bf16 v[36:39], a[40:43], a[16:19], v[36:39]\n" \
"v_add_u32 v213, s74, v211\n" \
"v_add_u32 v214, s74, v212\n" \
"v_mfma_f32_16x16x32_bf16 v[36:39], a[44:47], a[20:23], v[36:39]\n" \
"s_waitcnt lgkmcnt(3)\n" \
"v_xor_b32 v215, 32, v214\n" \
"v_mfma_f32_16x16x32_bf16 v[52:55], a[40:43], a[24:27], v[52:55]\n" \
"v_mfma_f32_16x16x32_bf16 v[52:55], a[44:47], a[28:31], v[52:55]\n" \
"global_load_lds_dwordx4 v222, s[36:37] offset:3072 sc1\n" \
"v_mfma_f32_16x16x32_bf16 v[48:51], a[32:35], a[24:27], v[48:51]\n" \
"s_waitcnt vmcnt(24) lgkmcnt(0)\n" \
"s_add_u32 s36, s36, 0x1000\n" \
"v_mfma_f32_16x16x32_bf16 v[48:51], a[36:39], a[28:31], v[48:51]\n" \
"s_barrier\n" \
"v_mfma_f32_16x16x32_bf16 v[8:11], a[48:51], a[0:3], v[8:11]\n" \
"s_add_u32 m0, m0, 0x5000\n" \
"ds_read_b64_tr_b16 a[32:33], v214 offset:16384\n" \
"v_mfma_f32_16x16x32_bf16 v[8:11], a[52:55], a[4:7], v[8:11]\n" \
"ds_read_b64_tr_b16 a[34:35], v214 offset:18432\n" \
"global_load_lds_dwordx4 v216, s[38:39] offset:-4096\n" \
"v_mfma_f32_16x16x32_bf16 v[12:15], a[56:59], a[0:3], v[12:15]\n" \
"ds_read_b64_tr_b16 a[36:37], v214 offset:24576\n" \
"v_mfma_f32_16x16x32_bf16 v[12:15], a[60:63], a[4:7], v[12:15]\n" \
"ds_read_b128 a[0:3], v213 offset:0\n" \
"v_mfma_f32_16x16x32_bf16 v[28:31], a[56:59], a[8:11], v[28:31]\n" \
"ds_read_b128 a[4:7], v213 offset:2048\n" \
"v_mfma_f32_16x16x32_bf16 v[28:31], a[60:63], a[12:15], v[28:31]\n" \
"ds_read_b64_tr_b16 a[38:39], v214 offset:26624\n" \
"global_load_lds_dwordx4 v217, s[38:39] offset:-3072\n" \
"v_mfma_f32_16x16x32_bf16 v[24:27], a[48:51], a[8:11], v[24:27]\n" \
"ds_read_b64_tr_b16 a[40:41], v215 offset:16384\n" \
"v_mfma_f32_16x16x32_bf16 v[24:27], a[52:55], a[12:15], v[24:27]\n" \
"ds_read_b128 a[8:11], v213 offset:256\n" \
"v_mfma_f32_16x16x32_bf16 v[40:43], a[48:51], a[16:19], v[40:43]\n" \
"ds_read_b128 a[12:15], v213 offset:2304\n" \
"v_mfma_f32_16x16x32_bf16 v[40:43], a[52:55], a[20:23], v[40:43]\n" \
"ds_read_b64_tr_b16 a[42:43], v215 offset:18432\n" \
"global_load_lds_dwordx4 v218, s[38:39] offset:-2048\n" \
"v_mfma_f32_16x16x32_bf16 v[44:47], a[56:59], a[16:19], v[44:47]\n" \
"ds_read_b64_tr_b16 a[44:45], v215 offset:24576\n" \
"v_mfma_f32_16x16x32_bf16 v[44:47], a[60:63], a[20:23], v[44:47]\n" \
"ds_read_b128 a[16:19], v213 offset:4096\n" \
"v_mfma_f32_16x16x32_bf16 v[60:63], a[56:59], a[24:27], v[60:63]\n" \
"ds_read_b128 a[20:23], v213 offset:6144\n" \
"global_load_lds_dwordx4 v219, s[38:39] offset:-1024\n" \
"v_mfma_f32_16x16x32_bf16 v[60:63], a[60:63], a[28:31], v[60:63]\n" \
"s_add_u32 s38, s38, 0x20000\n" \
"s_addc_u32 s39, s39, 0\n" \
"ds_read_b64_tr_b16 a[46:47], v215 offset:26624\n" \
"v_mfma_f32_16x16x32_bf16 v[56:59], a[48:51], a[24:27], v[56:59]\n" \
"s_sub_u32 s75, s75, 32768\n" \
"s_cselect_b32 s75, 131072, s75\n" \
"v_mfma_f32_16x16x32_bf16 v[56:59], a[52:55], a[28:31], v[56:59]\n" \
"s_waitcnt lgkmcnt(3)\n" \
"v_mfma_f32_16x16x32_bf16 v[0:3], a[32:35], a[0:3], v[0:3]\n" \
"ds_read_b128 a[24:27], v213 offset:4352\n" \
"s_add_u32 m0, s75, s95\n" \
"v_mfma_f32_16x16x32_bf16 v[0:3], a[36:39], a[4:7], v[0:3]\n" \
"s_waitcnt lgkmcnt(1)\n" \
"ds_read_b128 a[28:31], v213 offset:6400\n" \
"global_load_lds_dwordx4 v222, s[36:37] offset:0 sc1\n" \
"v_mfma_f32_16x16x32_bf16 v[4:7], a[40:43], a[0:3], v[4:7]\n" \
"s_sub_u32 s74, s74, 32768\n" \
"s_cselect_b32 s74, 131072, s74\n" \
"ds_read_b64_tr_b16 a[48:49], v214 offset:16896\n" \
"v_mfma_f32_16x16x32_bf16 v[4:7], a[44:47], a[4:7], v[4:7]\n" \
"ds_read_b64_tr_b16 a[50:51], v214 offset:18944\n" \
"v_mfma_f32_16x16x32_bf16 v[20:23], a[40:43], a[8:11], v[20:23]\n" \
"ds_read_b64_tr_b16 a[52:53], v214 offset:25088\n" \
"v_mfma_f32_16x16x32_bf16 v[20:23], a[44:47], a[12:15], v[20:23]\n" \
"ds_read_b64_tr_b16 a[54:55], v214 offset:27136\n" \
"global_load_lds_dwordx4 v222, s[36:37] offset:1024 sc1\n" \
"v_mfma_f32_16x16x32_bf16 v[16:19], a[32:35], a[8:11], v[16:19]\n" \
"ds_read_b64_tr_b16 a[56:57], v215 offset:16896\n" \
"v_mfma_f32_16x16x32_bf16 v[16:19], a[36:39], a[12:15], v[16:19]\n" \
"ds_read_b64_tr_b16 a[58:59], v215 offset:18944\n" \
"v_mfma_f32_16x16x32_bf16 v[32:35], a[32:35], a[16:19], v[32:35]\n" \
"ds_read_b64_tr_b16 a[60:61], v215 offset:25088\n" \
"v_mfma_f32_16x16x32_bf16 v[32:35], a[36:39], a[20:23], v[32:35]\n" \
"ds_read_b64_tr_b16 a[62:63], v215 offset:27136\n" \
"global_load_lds_dwordx4 v222, s[36:37] offset:2048 sc1\n" \
"v_mfma_f32_16x16x32_bf16 v[36:39], a[40:43], a[16:19], v[36:39]\n" \
"v_add_u32 v213, s74, v211\n" \
"v_add_u32 v214, s74, v212\n" \
"v_mfma_f32_16x16x32_bf16 v[36:39], a[44:47], a[20:23], v[36:39]\n" \
"s_waitcnt lgkmcnt(3)\n" \
"v_xor_b32 v215, 32, v214\n" \
"v_mfma_f32_16x16x32_bf16 v[52:55], a[40:43], a[24:27], v[52:55]\n" \
"v_mfma_f32_16x16x32_bf16 v[52:55], a[44:47], a[28:31], v[52:55]\n" \
"global_load_lds_dwordx4 v222, s[36:37] offset:3072 sc1\n" \
"v_mfma_f32_16x16x32_bf16 v[48:51], a[32:35], a[24:27], v[48:51]\n" \
"s_waitcnt vmcnt(12) lgkmcnt(0)\n" \
"s_add_u32 s36, s36, 0x1000\n" \
"v_mfma_f32_16x16x32_bf16 v[48:51], a[36:39], a[28:31], v[48:51]\n" \
"s_barrier\n" \
"v_mfma_f32_16x16x32_bf16 v[8:11], a[48:51], a[0:3], v[8:11]\n" \
"s_add_u32 m0, m0, 0x5000\n" \
"ds_read_b64_tr_b16 a[32:33], v214 offset:16384\n" \
"v_mfma_f32_16x16x32_bf16 v[8:11], a[52:55], a[4:7], v[8:11]\n" \
"ds_read_b64_tr_b16 a[34:35], v214 offset:18432\n" \
"global_load_lds_dwordx4 v216, s[38:39] offset:-4096\n" \
"v_mfma_f32_16x16x32_bf16 v[12:15], a[56:59], a[0:3], v[12:15]\n" \
"ds_read_b64_tr_b16 a[36:37], v214 offset:24576\n" \
"v_mfma_f32_16x16x32_bf16 v[12:15], a[60:63], a[4:7], v[12:15]\n" \
"buffer_store_dword v224, v225, s[56:59], s30 offen sc1\n" \
"ds_read_b128 a[0:3], v213 offset:0\n" \
"v_mfma_f32_16x16x32_bf16 v[28:31], a[56:59], a[8:11], v[28:31]\n" \
"ds_read_b128 a[4:7], v213 offset:2048\n" \
"v_mfma_f32_16x16x32_bf16 v[28:31], a[60:63], a[12:15], v[28:31]\n" \
"ds_read_b64_tr_b16 a[38:39], v214 offset:26624\n" \
"global_load_lds_dwordx4 v217, s[38:39] offset:-3072\n" \
"v_mfma_f32_16x16x32_bf16 v[24:27], a[48:51], a[8:11], v[24:27]\n" \
"ds_read_b64_tr_b16 a[40:41], v215 offset:16384\n" \
"v_mfma_f32_16x16x32_bf16 v[24:27], a[52:55], a[12:15], v[24:27]\n" \
"ds_read_b128 a[8:11], v213 offset:256\n" \
"v_mfma_f32_16x16x32_bf16 v[40:43], a[48:51], a[16:19], v[40:43]\n" \
"ds_read_b128 a[12:15], v213 offset:2304\n" \
"v_mfma_f32_16x16x32_bf16 v[40:43], a[52:55], a[20:23], v[40:43]\n" \
"ds_read_b64_tr_b16 a[42:43], v215 offset:18432\n" \
"global_load_lds_dwordx4 v218, s[38:39] offset:-2048\n" \
"v_mfma_f32_16x16x32_bf16 v[44:47], a[56:59], a[16:19], v[44:47]\n" \
"ds_read_b64_tr_b16 a[44:45], v215 offset:24576\n" \
"v_mfma_f32_16x16x32_bf16 v[44:47], a[60:63], a[20:23], v[44:47]\n" \
"ds_read_b128 a[16:19], v213 offset:4096\n" \
"v_mfma_f32_16x16x32_bf16 v[60:63], a[56:59], a[24:27], v[60:63]\n" \
"ds_read_b128 a[20:23], v213 offset:6144\n" \
"global_load_lds_dwordx4 v219, s[38:39] offset:-1024\n" \
"v_mfma_f32_16x16x32_bf16 v[60:63], a[60:63], a[28:31], v[60:63]\n" \
"s_add_u32 s38, s38, 0x20000\n" \
"s_addc_u32 s39, s39, 0\n" \
"ds_read_b64_tr_b16 a[46:47], v215 offset:26624\n" \
"v_mfma_f32_16x16x32_bf16 v[56:59], a[48:51], a[24:27], v[56:59]\n" \
"s_sub_u32 s75, s75, 32768\n" \
"s_cselect_b32 s75, 131072, s75\n" \
"v_mfma_f32_16x16x32_bf16 v[56:59], a[52:55], a[28:31], v[56:59]\n" \
"s_waitcnt lgkmcnt(3)\n" \
"s_cmp_eq_u32 s78, 0\n" \
"s_cbranch_scc0 Lrole1_%=\n" \
"s_sub_u32 s72, s64, 14\n" \
"s_call_b64 s[68:69], Lo0_%=\n" \
"buffer_load_dword v220, off, s[56:59], s79 sc1\n" \
"s_mov_b32 s72, 2\n" \
"s_call_b64 s[68:69], Lo0_%=\n" \
"v_mfma_f32_16x16x32_bf16 v[0:3], a[32:35], a[0:3], v[0:3]\n" \
"s_lshl_b32 s71, s61, 4\n" \
"ds_read_b128 a[24:27], v213 offset:4352\n" \
"v_subrev_u32 v72, s71, v251\n" \
"v_mfma_f32_16x16x32_bf16 v[0:3], a[36:39], a[4:7], v[0:3]\n" \
"s_waitcnt lgkmcnt(1)\n" \
"ds_read_b128 a[28:31], v213 offset:6400\n" \
"v_mov_b32 v73, 0x3f80\n" \
"v_mfma_f32_16x16x32_bf16 v[4:7], a[40:43], a[0:3], v[4:7]\n" \
"s_sub_u32 s74, s74, 32768\n" \
"s_cselect_b32 s74, 131072, s74\n" \
"ds_read_b64_tr_b16 a[48:49], v214 offset:16896\n" \
"v_mfma_f32_16x16x32_bf16 v[4:7], a[44:47], a[4:7], v[4:7]\n" \
"Lpg12038_%=:\n" \
"s_cbranch_execz Lpg13150_%=\n" \
"ds_read_b64_tr_b16 a[50:51], v214 offset:18944\n" \
"v_mov_b32 v74, 0x3f800000\n" \
"v_mfma_f32_16x16x32_bf16 v[20:23], a[40:43], a[8:11], v[20:23]\n" \
"ds_read_b64_tr_b16 a[52:53], v214 offset:25088\n" \
"v_cmp_eq_u32 vcc, 0, v72\n" \
"v_mfma_f32_16x16x32_bf16 v[20:23], a[44:47], a[12:15], v[20:23]\n" \
"ds_read_b64_tr_b16 a[54:55], v214 offset:27136\n" \
"v_cmp_eq_u32 s[66:67], 1, v72\n" \
"v_mfma_f32_16x16x32_bf16 v[16:19], a[32:35], a[8:11], v[16:19]\n" \
"ds_read_b64_tr_b16 a[56:57], v215 offset:16896\n" \
"v_mov_b32 v66, 0\n" \
"v_mfma_f32_16x16x32_bf16 v[16:19], a[36:39], a[12:15], v[16:19]\n" \
"ds_read_b64_tr_b16 a[58:59], v215 offset:18944\n" \
"v_mov_b32 v68, 0\n" \
"v_mfma_f32_16x16x32_bf16 v[32:35], a[32:35], a[16:19], v[32:35]\n" \
"ds_read_b64_tr_b16 a[60:61], v215 offset:25088\n" \
"v_cndmask_b32 v64, 0, v73, vcc\n" \
"v_mfma_f32_16x16x32_bf16 v[32:35], a[36:39], a[20:23], v[32:35]\n" \
"ds_read_b64_tr_b16 a[62:63], v215 offset:27136\n" \
"v_cndmask_b32 v64, v64, v74, s[66:67]\n" \
"v_mfma_f32_16x16x32_bf16 v[36:39], a[40:43], a[16:19], v[36:39]\n" \
"v_add_u32 v213, s74, v211\n" \
"v_add_u32 v214, s74, v212\n" \
"v_mfma_f32_16x16x32_bf16 v[36:39], a[44:47], a[20:23], v[36:39]\n" \
"s_waitcnt lgkmcnt(3)\n" \
"v_xor_b32 v215, 32, v214\n" \
"v_mov_b32 v70, v64\n" \
"v_mfma_f32_16x16x32_bf16 v[52:55], a[40:43], a[24:27], v[52:55]\n" \
"v_cmp_eq_u32 vcc, 2, v72\n" \
"v_cmp_eq_u32 s[66:67], 3, v72\n" \
"v_mfma_f32_16x16x32_bf16 v[52:55], a[44:47], a[28:31], v[52:55]\n" \
"v_mov_b32 v67, 0\n" \
"v_mov_b32 v69, 0\n" \
"v_mfma_f32_16x16x32_bf16 v[48:51], a[32:35], a[24:27], v[48:51]\n" \
"s_waitcnt vmcnt(17) lgkmcnt(0)\n" \
"v_cndmask_b32 v65, 0, v73, vcc\n" \
"v_cndmask_b32 v65, v65, v74, s[66:67]\n" \
"v_mfma_f32_16x16x32_bf16 v[48:51], a[36:39], a[28:31], v[48:51]\n" \
"s_barrier\n" \
"v_mov_b32 v71, v65\n" \
"v_mfma_f32_16x16x32_bf16 v[8:11], a[48:51], a[0:3], v[8:11]\n" \
"s_lshl_b32 s83, s95, 1\n" \
"s_add_u32 s83, s83, s74\n" \
"ds_read_b64_tr_b16 a[32:33], v214 offset:16384\n" \
"v_mfma_f32_16x16x32_bf16 v[8:11], a[52:55], a[4:7], v[8:11]\n" \
"s_waitcnt vmcnt(16)\n" \
"ds_read_b64_tr_b16 a[34:35], v214 offset:18432\n" \
"v_cmp_ne_u32 vcc, s60, v220\n" \
"v_mfma_f32_16x16x32_bf16 v[12:15], a[56:59], a[0:3], v[12:15]\n" \
"ds_read_b64_tr_b16 a[36:37], v214 offset:24576\n" \
"v_mfma_f32_16x16x32_bf16 v[12:15], a[60:63], a[4:7], v[12:15]\n" \
"s_and_b32 vcc_hi, vcc_lo, 1\n" \
"s_cbranch_scc1 Lpl4_%=\n" \
"Lpr4_%=:\n" \
"ds_read_b128 a[0:3], v213 offset:0\n" \
"v_mfma_f32_16x16x32_bf16 v[28:31], a[56:59], a[8:11], v[28:31]\n" \
"ds_read_b128 a[4:7], v213 offset:2048\n" \
"v_mfma_f32_16x16x32_bf16 v[28:31], a[60:63], a[12:15], v[28:31]\n" \
"s_add_u32 s71, s76, 0\n" \
"ds_read_b64_tr_b16 a[38:39], v214 offset:26624\n" \
"buffer_load_dwordx4 v[176:179], v248, s[56:59], s71 offen offset:0 sc1\n" \
"v_mfma_f32_16x16x32_bf16 v[24:27], a[48:51], a[8:11], v[24:27]\n" \
"ds_read_b64_tr_b16 a[40:41], v215 offset:16384\n" \
"v_mfma_f32_16x16x32_bf16 v[24:27], a[52:55], a[12:15], v[24:27]\n" \
"buffer_load_dwordx4 v[180:183], v248, s[56:59], s71 offen offset:1024 sc1\n" \
"ds_read_b128 a[8:11], v213 offset:256\n" \
"v_mfma_f32_16x16x32_bf16 v[40:43], a[48:51], a[16:19], v[40:43]\n" \
"ds_read_b128 a[12:15], v213 offset:2304\n" \
"v_mfma_f32_16x16x32_bf16 v[40:43], a[52:55], a[20:23], v[40:43]\n" \
"ds_read_b64_tr_b16 a[42:43], v215 offset:18432\n" \
"v_mfma_f32_16x16x32_bf16 v[44:47], a[56:59], a[16:19], v[44:47]\n" \
"ds_read_b64_tr_b16 a[44:45], v215 offset:24576\n" \
"v_mfma_f32_16x16x32_bf16 v[44:47], a[60:63], a[20:23], v[44:47]\n" \
"ds_read_b128 a[16:19], v213 offset:4096\n" \
"v_mfma_f32_16x16x32_bf16 v[60:63], a[56:59], a[24:27], v[60:63]\n" \
"ds_read_b128 a[20:23], v213 offset:6144\n" \
"v_mfma_f32_16x16x32_bf16 v[60:63], a[60:63], a[28:31], v[60:63]\n" \
"ds_read_b64_tr_b16 a[46:47], v215 offset:26624\n" \
"v_mfma_f32_16x16x32_bf16 v[56:59], a[48:51], a[24:27], v[56:59]\n" \
"buffer_load_dwordx4 v[184:187], v248, s[56:59], s71 offen offset:2048 sc1\n" \
"v_mfma_f32_16x16x32_bf16 v[56:59], a[52:55], a[28:31], v[56:59]\n" \
"s_waitcnt lgkmcnt(3)\n" \
"buffer_load_dwordx4 v[188:191], v248, s[56:59], s71 offen offset:3072 sc1\n" \
"v_mfma_f32_16x16x32_bf16 v[0:3], a[32:35], a[0:3], v[0:3]\n" \
"ds_read_b128 a[24:27], v213 offset:4352\n" \
"s_add_u32 s71, s76, 4096\n" \
"v_mfma_f32_16x16x32_bf16 v[0:3], a[36:39], a[4:7], v[0:3]\n" \
"s_waitcnt lgkmcnt(1)\n" \
"buffer_load_dwordx4 v[192:195], v248, s[56:59], s71 offen offset:0 sc1\n" \
"ds_read_b128 a[28:31], v213 offset:6400\n" \
"v_mfma_f32_16x16x32_bf16 v[4:7], a[40:43], a[0:3], v[4:7]\n" \
"s_sub_u32 s74, s74, 32768\n" \
"s_cselect_b32 s74, 131072, s74\n" \
"ds_read_b64_tr_b16 a[48:49], v214 offset:16896\n" \
"v_mfma_f32_16x16x32_bf16 v[4:7], a[44:47], a[4:7], v[4:7]\n" \
"ds_read_b64_tr_b16 a[50:51], v214 offset:18944\n" \
"v_lshrrev_b32 v132, 4, v248\n" \
"v_mfma_f32_16x16x32_bf16 v[20:23], a[40:43], a[8:11], v[20:23]\n" \
"ds_read_b64_tr_b16 a[52:53], v214 offset:25088\n" \
"v_and_b32 v133, 15, v132\n" \
"v_mfma_f32_16x16x32_bf16 v[20:23], a[44:47], a[12:15], v[20:23]\n" \
"ds_read_b64_tr_b16 a[54:55], v214 offset:27136\n" \
"v_mul_u32_u24 v128, 144, v133\n" \
"v_mfma_f32_16x16x32_bf16 v[16:19], a[32:35], a[8:11], v[16:19]\n" \
"ds_read_b64_tr_b16 a[56:57], v215 offset:16896\n" \
"v_lshrrev_b32 v133, 4, v132\n" \
"v_mfma_f32_16x16x32_bf16 v[16:19], a[36:39], a[12:15], v[16:19]\n" \
"ds_read_b64_tr_b16 a[58:59], v215 offset:18944\n" \
"v_lshl_add_u32 v128, v133, 3, v128\n" \
"v_mfma_f32_16x16x32_bf16 v[32:35], a[32:35], a[16:19], v[32:35]\n" \
"ds_read_b64_tr_b16 a[60:61], v215 offset:25088\n" \
"v_add_u32 v128, s83, v128\n" \
"v_mfma_f32_16x16x32_bf16 v[32:35], a[36:39], a[20:23], v[32:35]\n" \
"ds_read_b64_tr_b16 a[62:63], v215 offset:27136\n" \
"v_lshrrev_b32 v133, 3, v132\n" \
"v_mfma_f32_16x16x32_bf16 v[36:39], a[40:43], a[16:19], v[36:39]\n" \
"v_add_u32 v213, s74, v211\n" \
"v_add_u32 v214, s74, v212\n" \
"v_mfma_f32_16x16x32_bf16 v[36:39], a[44:47], a[20:23], v[36:39]\n" \
"s_waitcnt lgkmcnt(3)\n" \
"v_xor_b32 v215, 32, v214\n" \
"v_and_b32 v134, 7, v132\n" \
"v_mfma_f32_16x16x32_bf16 v[52:55], a[40:43], a[24:27], v[52:55]\n" \
"buffer_load_dwordx4 v[196:199], v248, s[56:59], s71 offen offset:1024 sc1\n" \
"v_mfma_f32_16x16x32_bf16 v[52:55], a[44:47], a[28:31], v[52:55]\n" \
"buffer_load_dwordx4 v[200:203], v248, s[56:59], s71 offen offset:2048 sc1\n" \
"v_mfma_f32_16x16x32_bf16 v[48:51], a[32:35], a[24:27], v[48:51]\n" \
"s_waitcnt vmcnt(7) lgkmcnt(0)\n" \
"buffer_load_dwordx4 v[204:207], v248, s[56:59], s71 offen offset:3072 sc1\n" \
"v_mfma_f32_16x16x32_bf16 v[48:51], a[36:39], a[28:31], v[48:51]\n" \
"s_barrier\n" \
"v_mfma_f32_16x16x32_bf16 v[8:11], a[48:51], a[0:3], v[8:11]\n" \
"s_mov_b32 s70, 0x20000000\n" \
"ds_read_b128 a[192:195], v213 offset:0\n" \
"ds_read_b64_tr_b16 a[128:129], v214 offset:16384\n" \
"v_mfma_f32_16x16x32_bf16 v[8:11], a[52:55], a[4:7], v[8:11]\n" \
"s_sub_u32 s74, s74, 32768\n" \
"s_cselect_b32 s74, 131072, s74\n" \
"ds_read_b64_tr_b16 a[130:131], v214 offset:18432\n" \
"v_mfma_f32_16x16x32_bf16 v[12:15], a[56:59], a[0:3], v[12:15]\n" \
"ds_read_b128 a[196:199], v213 offset:2048\n" \
"ds_read_b64_tr_b16 a[132:133], v214 offset:24576\n" \
"v_mfma_f32_16x16x32_bf16 v[12:15], a[60:63], a[4:7], v[12:15]\n" \
"ds_read_b64_tr_b16 a[134:135], v214 offset:26624\n" \
"v_add_u32 v226, s74, v211\n" \
"v_mfma_f32_16x16x32_bf16 v[28:31], a[56:59], a[8:11], v[28:31]\n" \
"ds_read_b128 a[200:203], v213 offset:256\n" \
"ds_read_b64_tr_b16 a[136:137], v215 offset:16384\n" \
"v_mfma_f32_16x16x32_bf16 v[28:31], a[60:63], a[12:15], v[28:31]\n" \
"ds_read_b64_tr_b16 a[138:139], v215 offset:18432\n" \
"v_add_u32 v227, s74, v212\n" \
"v_mfma_f32_16x16x32_bf16 v[24:27], a[48:51], a[8:11], v[24:27]\n" \
"ds_read_b128 a[204:207], v213 offset:2304\n" \
"ds_read_b64_tr_b16 a[140:141], v215 offset:24576\n" \
"v_mfma_f32_16x16x32_bf16 v[24:27], a[52:55], a[12:15], v[24:27]\n" \
"ds_read_b64_tr_b16 a[142:143], v215 offset:26624\n" \
"v_xor_b32 v228, 32, v227\n" \
"v_mfma_f32_16x16x32_bf16 v[40:43], a[48:51], a[16:19], v[40:43]\n" \
"s_cmp_eq_u32 s79, 65601536\n" \
"s_cselect_b32 s70, 65593344, s70\n" \
"ds_read_b64_tr_b16 a[144:145], v214 offset:16896\n" \
"v_mfma_f32_16x16x32_bf16 v[40:43], a[52:55], a[20:23], v[40:43]\n" \
"buffer_store_dword v224, v225, s[56:59], s70 offen sc1\n" \
"ds_read_b64_tr_b16 a[146:147], v214 offset:18944\n" \
"v_mfma_f32_16x16x32_bf16 v[44:47], a[56:59], a[16:19], v[44:47]\n" \
"ds_read_b64_tr_b16 a[148:149], v214 offset:25088\n" \
"v_mul_u32_u24 v129, 144, v133\n" \
"v_mfma_f32_16x16x32_bf16 v[44:47], a[60:63], a[20:23], v[44:47]\n" \
"ds_read_b64_tr_b16 a[150:151], v214 offset:27136\n" \
"v_lshl_add_u32 v129, v134, 4, v129\n" \
"v_mfma_f32_16x16x32_bf16 v[60:63], a[56:59], a[24:27], v[60:63]\n" \
"ds_read_b64_tr_b16 a[152:153], v215 offset:16896\n" \
"v_add_u32 v129, s83, v129\n" \
"v_mfma_f32_16x16x32_bf16 v[60:63], a[60:63], a[28:31], v[60:63]\n" \
"ds_read_b64_tr_b16 a[154:155], v215 offset:18944\n" \
"v_lshlrev_b32 v134, 4, v134\n" \
"v_mfma_f32_16x16x32_bf16 v[56:59], a[48:51], a[24:27], v[56:59]\n" \
"ds_read_b64_tr_b16 a[156:157], v215 offset:25088\n" \
"v_lshl_add_u32 v130, v133, 12, v134\n" \
"v_mfma_f32_16x16x32_bf16 v[56:59], a[52:55], a[28:31], v[56:59]\n" \
"s_waitcnt lgkmcnt(4)\n" \
"ds_read_b64_tr_b16 a[158:159], v215 offset:27136\n" \
"v_mfma_f32_16x16x32_bf16 v[0:3], a[128:131], a[192:195], v[0:3]\n" \
"ds_read_b128 a[224:227], v226 offset:0\n" \
"ds_read_b64_tr_b16 a[160:161], v227 offset:16384\n" \
"v_mfma_f32_16x16x32_bf16 v[0:3], a[132:135], a[196:199], v[0:3]\n" \
"ds_read_b64_tr_b16 a[162:163], v227 offset:18432\n" \
"v_mfma_f32_16x16x32_bf16 v[16:19], a[128:131], a[200:203], v[16:19]\n" \
"ds_read_b64_tr_b16 a[164:165], v227 offset:24576\n" \
"v_mfma_f32_16x16x32_bf16 v[16:19], a[132:135], a[204:207], v[16:19]\n" \
"ds_read_b128 a[228:231], v226 offset:2048\n" \
"ds_read_b64_tr_b16 a[166:167], v227 offset:26624\n" \
"v_mfma_f32_16x16x32_bf16 v[20:23], a[136:139], a[200:203], v[20:23]\n" \
"ds_read_b64_tr_b16 a[168:169], v228 offset:16384\n" \
"v_mfma_f32_16x16x32_bf16 v[20:23], a[140:143], a[204:207], v[20:23]\n" \
"ds_read_b64_tr_b16 a[170:171], v228 offset:18432\n" \
"v_mfma_f32_16x16x32_bf16 v[4:7], a[136:139], a[192:195], v[4:7]\n" \
"ds_read_b128 a[232:235], v226 offset:256\n" \
"ds_read_b64_tr_b16 a[172:173], v228 offset:24576\n" \
"v_mfma_f32_16x16x32_bf16 v[4:7], a[140:143], a[196:199], v[4:7]\n" \
"s_waitcnt lgkmcnt(6)\n" \
"ds_read_b64_tr_b16 a[174:175], v228 offset:26624\n" \
"v_mfma_f32_16x16x32_bf16 v[8:11], a[144:147], a[192:195], v[8:11]\n" \
"ds_read_b64_tr_b16 a[176:177], v227 offset:16896\n" \
"v_mfma_f32_16x16x32_bf16 v[8:11], a[148:151], a[196:199], v[8:11]\n" \
"ds_read_b128 a[236:239], v226 offset:2304\n" \
"ds_read_b64_tr_b16 a[178:179], v227 offset:18944\n" \
"v_mfma_f32_16x16x32_bf16 v[24:27], a[144:147], a[200:203], v[24:27]\n" \
"ds_read_b64_tr_b16 a[180:181], v227 offset:25088\n" \
"v_mfma_f32_16x16x32_bf16 v[24:27], a[148:151], a[204:207], v[24:27]\n" \
"ds_read_b64_tr_b16 a[182:183], v227 offset:27136\n" \
"v_mfma_f32_16x16x32_bf16 v[28:31], a[152:155], a[200:203], v[28:31]\n" \
"ds_read_b64_tr_b16 a[184:185], v228 offset:16896\n" \
"v_mfma_f32_16x16x32_bf16 v[28:31], a[156:159], a[204:207], v[28:31]\n" \
"ds_read_b64_tr_b16 a[186:187], v228 offset:18944\n" \
"v_mfma_f32_16x16x32_bf16 v[12:15], a[152:155], a[192:195], v[12:15]\n" \
"ds_read_b64_tr_b16 a[188:189], v228 offset:25088\n" \
"v_mfma_f32_16x16x32_bf16 v[12:15], a[156:159], a[196:199], v[12:15]\n" \
"s_waitcnt lgkmcnt(4)\n" \
"ds_read_b64_tr_b16 a[190:191], v228 offset:27136\n" \
"v_mfma_f32_16x16x32_bf16 v[0:3], a[160:163], a[224:227], v[0:3]\n" \
"s_waitcnt vmcnt(1)\n" \
"ds_read_b128 a[208:211], v213 offset:4096\n" \
"v_mfma_f32_16x16x32_bf16 v[0:3], a[164:167], a[228:231], v[0:3]\n" \
"ds_read_b128 a[212:215], v213 offset:6144\n" \
"v_mfma_f32_16x16x32_bf16 v[0:3], v[64:67], v[176:179], v[0:3]\n" \
"ds_read_b128 a[240:243], v226 offset:4096\n" \
"v_mfma_f32_16x16x32_bf16 v[16:19], a[160:163], a[232:235], v[16:19]\n" \
"ds_read_b128 a[244:247], v226 offset:6144\n" \
"v_mfma_f32_16x16x32_bf16 v[16:19], a[164:167], a[236:239], v[16:19]\n" \
"ds_read_b128 a[216:219], v213 offset:4352\n" \
"v_mfma_f32_16x16x32_bf16 v[16:19], v[64:67], v[184:187], v[16:19]\n" \
"ds_read_b128 a[220:223], v213 offset:6400\n" \
"v_cvt_pk_bf16_f32 v0, v0, v1\n" \
"v_mfma_f32_16x16x32_bf16 v[20:23], a[168:171], a[232:235], v[20:23]\n" \
"ds_read_b128 a[248:251], v226 offset:4352\n" \
"v_cvt_pk_bf16_f32 v1, v2, v3\n" \
"v_mfma_f32_16x16x32_bf16 v[20:23], a[172:175], a[236:239], v[20:23]\n" \
"ds_read_b128 a[252:255], v226 offset:6400\n" \
"v_mfma_f32_16x16x32_bf16 v[20:23], v[68:71], v[184:187], v[20:23]\n" \
"ds_write_b64 v128, v[0:1] offset:0\n" \
"v_mfma_f32_16x16x32_bf16 v[4:7], a[168:171], a[224:227], v[4:7]\n" \
"v_cvt_pk_bf16_f32 v16, v16, v17\n" \
"v_cvt_pk_bf16_f32 v17, v18, v19\n" \
"v_mfma_f32_16x16x32_bf16 v[4:7], a[172:175], a[228:231], v[4:7]\n" \
"ds_write_b64 v128, v[16:17] offset:2304\n" \
"v_mfma_f32_16x16x32_bf16 v[4:7], v[68:71], v[176:179], v[4:7]\n" \
"s_waitcnt lgkmcnt(3)\n" \
"v_cvt_pk_bf16_f32 v20, v20, v21\n" \
"v_cvt_pk_bf16_f32 v21, v22, v23\n" \
"v_mfma_f32_16x16x32_bf16 v[8:11], a[176:179], a[224:227], v[8:11]\n" \
"ds_write_b64 v128, v[20:21] offset:2336\n" \
"v_mfma_f32_16x16x32_bf16 v[8:11], a[180:183], a[228:231], v[8:11]\n" \
"v_mfma_f32_16x16x32_bf16 v[8:11], v[64:67], v[180:183], v[8:11]\n" \
"v_cvt_pk_bf16_f32 v4, v4, v5\n" \
"v_cvt_pk_bf16_f32 v5, v6, v7\n" \
"v_mfma_f32_16x16x32_bf16 v[24:27], a[176:179], a[232:235], v[24:27]\n" \
"ds_write_b64 v128, v[4:5] offset:32\n" \
"v_mfma_f32_16x16x32_bf16 v[24:27], a[180:183], a[236:239], v[24:27]\n" \
"v_mfma_f32_16x16x32_bf16 v[24:27], v[64:67], v[188:191], v[24:27]\n" \
"v_cvt_pk_bf16_f32 v8, v8, v9\n" \
"v_cvt_pk_bf16_f32 v9, v10, v11\n" \
"v_mfma_f32_16x16x32_bf16 v[28:31], a[184:187], a[232:235], v[28:31]\n" \
"ds_write_b64 v128, v[8:9] offset:64\n" \
"v_mfma_f32_16x16x32_bf16 v[28:31], a[188:191], a[236:239], v[28:31]\n" \
"v_mfma_f32_16x16x32_bf16 v[28:31], v[68:71], v[188:191], v[28:31]\n" \
"v_cvt_pk_bf16_f32 v24, v24, v25\n" \
"v_cvt_pk_bf16_f32 v25, v26, v27\n" \
"v_mfma_f32_16x16x32_bf16 v[12:15], a[184:187], a[224:227], v[12:15]\n" \
"ds_write_b64 v128, v[24:25] offset:2368\n" \
"v_mfma_f32_16x16x32_bf16 v[12:15], a[188:191], a[228:231], v[12:15]\n" \
"v_mfma_f32_16x16x32_bf16 v[12:15], v[68:71], v[180:183], v[12:15]\n" \
"v_cvt_pk_bf16_f32 v28, v28, v29\n" \
"v_cvt_pk_bf16_f32 v29, v30, v31\n" \
"v_mfma_f32_16x16x32_bf16 v[32:35], a[128:131], a[208:211], v[32:35]\n" \
"ds_write_b64 v128, v[28:29] offset:2400\n" \
"v_mfma_f32_16x16x32_bf16 v[32:35], a[132:135], a[212:215], v[32:35]\n" \
"v_mfma_f32_16x16x32_bf16 v[32:35], a[160:163], a[240:243], v[32:35]\n" \
"v_cvt_pk_bf16_f32 v12, v12, v13\n" \
"v_cvt_pk_bf16_f32 v13, v14, v15\n" \
"v_mfma_f32_16x16x32_bf16 v[32:35], a[164:167], a[244:247], v[32:35]\n" \
"ds_write_b64 v128, v[12:13] offset:96\n" \
"v_mfma_f32_16x16x32_bf16 v[32:35], v[64:67], v[192:195], v[32:35]\n" \
"ds_read_b128 v[140:143], v129 offset:0\n" \
"v_mfma_f32_16x16x32_bf16 v[36:39], a[168:171], a[240:243], v[36:39]\n" \
"ds_read_b128 v[144:147], v129 offset:1152\n" \
"v_mfma_f32_16x16x32_bf16 v[36:39], a[172:175], a[244:247], v[36:39]\n" \
"ds_read_b128 v[148:151], v129 offset:2304\n" \
"v_mfma_f32_16x16x32_bf16 v[36:39], a[136:139], a[208:211], v[36:39]\n" \
"ds_read_b128 v[152:155], v129 offset:3456\n" \
"v_mfma_f32_16x16x32_bf16 v[36:39], a[140:143], a[212:215], v[36:39]\n" \
"v_cvt_pk_bf16_f32 v32, v32, v33\n" \
"v_cvt_pk_bf16_f32 v33, v34, v35\n" \
"v_mfma_f32_16x16x32_bf16 v[36:39], v[68:71], v[192:195], v[36:39]\n" \
"ds_write_b64 v128, v[32:33] offset:0\n" \
"v_mfma_f32_16x16x32_bf16 v[40:43], a[144:147], a[208:211], v[40:43]\n" \
"v_mfma_f32_16x16x32_bf16 v[40:43], a[148:151], a[212:215], v[40:43]\n" \
"s_add_u32 s70, s77, 0\n" \
"s_waitcnt lgkmcnt(4)\n" \
"v_mfma_f32_16x16x32_bf16 v[40:43], a[176:179], a[240:243], v[40:43]\n" \
"v_cvt_pk_bf16_f32 v36, v36, v37\n" \
"v_cvt_pk_bf16_f32 v37, v38, v39\n" \
"v_mfma_f32_16x16x32_bf16 v[40:43], a[180:183], a[244:247], v[40:43]\n" \
"buffer_store_dwordx4 v[140:143], v130, s[52:55], s70 offen sc1\n" \
"ds_write_b64 v128, v[36:37] offset:32\n" \
"v_mfma_f32_16x16x32_bf16 v[40:43], v[64:67], v[196:199], v[40:43]\n" \
"v_mfma_f32_16x16x32_bf16 v[44:47], a[184:187], a[240:243], v[44:47]\n" \
"v_mfma_f32_16x16x32_bf16 v[44:47], a[188:191], a[244:247], v[44:47]\n" \
"v_mfma_f32_16x16x32_bf16 v[44:47], a[152:155], a[208:211], v[44:47]\n" \
"s_add_u32 s70, s77, 32768\n" \
"v_cvt_pk_bf16_f32 v40, v40, v41\n" \
"v_cvt_pk_bf16_f32 v41, v42, v43\n" \
"v_mfma_f32_16x16x32_bf16 v[44:47], a[156:159], a[212:215], v[44:47]\n" \
"s_waitcnt lgkmcnt(4)\n" \
"buffer_store_dwordx4 v[144:147], v130, s[52:55], s70 offen sc1\n" \
"ds_write_b64 v128, v[40:41] offset:64\n" \
"v_mfma_f32_16x16x32_bf16 v[44:47], v[68:71], v[196:199], v[44:47]\n" \
"v_mfma_f32_16x16x32_bf16 v[48:51], a[128:131], a[216:219], v[48:51]\n" \
"v_mfma_f32_16x16x32_bf16 v[48:51], a[132:135], a[220:223], v[48:51]\n" \
"v_mfma_f32_16x16x32_bf16 v[48:51], a[160:163], a[248:251], v[48:51]\n" \
"s_add_u32 s70, s77, 65536\n" \
"v_cvt_pk_bf16_f32 v44, v44, v45\n" \
"v_cvt_pk_bf16_f32 v45, v46, v47\n" \
"v_mfma_f32_16x16x32_bf16 v[48:51], a[164:167], a[252:255], v[48:51]\n" \
"s_waitcnt lgkmcnt(4)\n" \
"buffer_store_dwordx4 v[148:151], v130, s[52:55], s70 offen sc1\n" \
"ds_write_b64 v128, v[44:45] offset:96\n" \
"v_mfma_f32_16x16x32_bf16 v[48:51], v[64:67], v[200:203], v[48:51]\n" \
"ds_read_b128 v[156:159], v129 offset:0\n" \
"v_mfma_f32_16x16x32_bf16 v[52:55], a[168:171], a[248:251], v[52:55]\n" \
"ds_read_b128 v[160:163], v129 offset:1152\n" \
"v_mfma_f32_16x16x32_bf16 v[52:55], a[172:175], a[252:255], v[52:55]\n" \
"v_mfma_f32_16x16x32_bf16 v[52:55], a[136:139], a[216:219], v[52:55]\n" \
"s_add_u32 s70, s77, 98304\n" \
"v_cvt_pk_bf16_f32 v48, v48, v49\n" \
"v_cvt_pk_bf16_f32 v49, v50, v51\n" \
"v_mfma_f32_16x16x32_bf16 v[52:55], a[140:143], a[220:223], v[52:55]\n" \
"s_waitcnt lgkmcnt(6)\n" \
"buffer_store_dwordx4 v[152:155], v130, s[52:55], s70 offen sc1\n" \
"ds_write_b64 v128, v[48:49] offset:2304\n" \
"v_mfma_f32_16x16x32_bf16 v[52:55], v[68:71], v[200:203], v[52:55]\n" \
"v_mfma_f32_16x16x32_bf16 v[56:59], a[144:147], a[216:219], v[56:59]\n" \
"v_mfma_f32_16x16x32_bf16 v[56:59], a[148:151], a[220:223], v[56:59]\n" \
"v_mfma_f32_16x16x32_bf16 v[56:59], a[176:179], a[248:251], v[56:59]\n" \
"s_add_u32 s70, s77, 131072\n" \
"v_cvt_pk_bf16_f32 v52, v52, v53\n" \
"v_cvt_pk_bf16_f32 v53, v54, v55\n" \
"v_mfma_f32_16x16x32_bf16 v[56:59], a[180:183], a[252:255], v[56:59]\n" \
"s_waitcnt lgkmcnt(2)\n" \
"buffer_store_dwordx4 v[156:159], v130, s[52:55], s70 offen sc1\n" \
"ds_write_b64 v128, v[52:53] offset:2336\n" \
"v_mfma_f32_16x16x32_bf16 v[56:59], v[64:67], v[204:207], v[56:59]\n" \
"v_mfma_f32_16x16x32_bf16 v[60:63], a[184:187], a[248:251], v[60:63]\n" \
"v_mfma_f32_16x16x32_bf16 v[60:63], a[188:191], a[252:255], v[60:63]\n" \
"v_mfma_f32_16x16x32_bf16 v[60:63], a[152:155], a[216:219], v[60:63]\n" \
"s_add_u32 s70, s77, 163840\n" \
"v_cvt_pk_bf16_f32 v56, v56, v57\n" \
"v_cvt_pk_bf16_f32 v57, v58, v59\n" \
"v_mfma_f32_16x16x32_bf16 v[60:63], a[156:159], a[220:223], v[60:63]\n" \
"s_waitcnt lgkmcnt(2)\n" \
"buffer_store_dwordx4 v[160:163], v130, s[52:55], s70 offen sc1\n" \
"ds_write_b64 v128, v[56:57] offset:2368\n" \
"v_mfma_f32_16x16x32_bf16 v[60:63], v[68:71], v[204:207], v[60:63]\n" \
"s_nop 7\n" \
"v_cvt_pk_bf16_f32 v60, v60, v61\n" \
"v_cvt_pk_bf16_f32 v61, v62, v63\n" \
"ds_write_b64 v128, v[60:61] offset:2400\n" \
"ds_read_b128 v[164:167], v129 offset:2304\n" \
"ds_read_b128 v[168:171], v129 offset:3456\n" \
"s_add_u32 s70, s77, 196608\n" \
"s_waitcnt lgkmcnt(1)\n" \
"buffer_store_dwordx4 v[164:167], v130, s[52:55], s70 offen sc1\n" \
"s_add_u32 s70, s77, 229376\n" \
"s_waitcnt lgkmcnt(0)\n" \
"buffer_store_dwordx4 v[168:171], v130, s[52:55], s70 offen sc1\n" \
"s_endpgm\n" \
"Lpg12415_%=:\n" \
"s_cbranch_execz Lpg13388_%=\n" \
"Lo0_%=:\n" \
"v_mfma_f32_16x16x32_bf16 v[0:3], a[32:35], a[0:3], v[0:3]\n" \
"ds_read_b128 a[24:27], v213 offset:4352\n" \
"s_add_u32 m0, s75, s95\n" \
"v_mfma_f32_16x16x32_bf16 v[0:3], a[36:39], a[4:7], v[0:3]\n" \
"s_waitcnt lgkmcnt(1)\n" \
"ds_read_b128 a[28:31], v213 offset:6400\n" \
"global_load_lds_dwordx4 v222, s[36:37] offset:0 sc1\n" \
"v_mfma_f32_16x16x32_bf16 v[4:7], a[40:43], a[0:3], v[4:7]\n" \
"s_sub_u32 s74, s74, 32768\n" \
"s_cselect_b32 s74, 131072, s74\n" \
"ds_read_b64_tr_b16 a[48:49], v214 offset:16896\n" \
"v_mfma_f32_16x16x32_bf16 v[4:7], a[44:47], a[4:7], v[4:7]\n" \
"ds_read_b64_tr_b16 a[50:51], v214 offset:18944\n" \
"v_mfma_f32_16x16x32_bf16 v[20:23], a[40:43], a[8:11], v[20:23]\n" \
"ds_read_b64_tr_b16 a[52:53], v214 offset:25088\n" \
"v_mfma_f32_16x16x32_bf16 v[20:23], a[44:47], a[12:15], v[20:23]\n" \
"ds_read_b64_tr_b16 a[54:55], v214 offset:27136\n" \
"global_load_lds_dwordx4 v222, s[36:37] offset:1024 sc1\n" \
"v_mfma_f32_16x16x32_bf16 v[16:19], a[32:35], a[8:11], v[16:19]\n" \
"ds_read_b64_tr_b16 a[56:57], v215 offset:16896\n" \
"v_mfma_f32_16x16x32_bf16 v[16:19], a[36:39], a[12:15], v[16:19]\n" \
"ds_read_b64_tr_b16 a[58:59], v215 offset:18944\n" \
"v_mfma_f32_16x16x32_bf16 v[32:35], a[32:35], a[16:19], v[32:35]\n" \
"ds_read_b64_tr_b16 a[60:61], v215 offset:25088\n" \
"v_mfma_f32_16x16x32_bf16 v[32:35], a[36:39], a[20:23], v[32:35]\n" \
"ds_read_b64_tr_b16 a[62:63], v215 offset:27136\n" \
"global_load_lds_dwordx4 v222, s[36:37] offset:2048 sc1\n" \
"v_mfma_f32_16x16x32_bf16 v[36:39], a[40:43], a[16:19], v[36:39]\n" \
"v_add_u32 v213, s74, v211\n" \
"v_add_u32 v214, s74, v212\n" \
"v_mfma_f32_16x16x32_bf16 v[36:39], a[44:47], a[20:23], v[36:39]\n" \
"s_waitcnt lgkmcnt(3)\n" \
"v_xor_b32 v215, 32, v214\n" \
"v_mfma_f32_16x16x32_bf16 v[52:55], a[40:43], a[24:27], v[52:55]\n" \
"v_mfma_f32_16x16x32_bf16 v[52:55], a[44:47], a[28:31], v[52:55]\n" \
"global_load_lds_dwordx4 v222, s[36:37] offset:3072 sc1\n" \
"v_mfma_f32_16x16x32_bf16 v[48:51], a[32:35], a[24:27], v[48:51]\n" \
"s_waitcnt vmcnt(20) lgkmcnt(0)\n" \
"s_add_u32 s36, s36, 0x1000\n" \
"v_mfma_f32_16x16x32_bf16 v[48:51], a[36:39], a[28:31], v[48:51]\n" \
"s_barrier\n" \
"v_mfma_f32_16x16x32_bf16 v[8:11], a[48:51], a[0:3], v[8:11]\n" \
"s_add_u32 m0, m0, 0x5000\n" \
"ds_read_b64_tr_b16 a[32:33], v214 offset:16384\n" \
"v_mfma_f32_16x16x32_bf16 v[8:11], a[52:55], a[4:7], v[8:11]\n" \
"ds_read_b64_tr_b16 a[34:35], v214 offset:18432\n" \
"global_load_lds_dwordx4 v216, s[38:39] offset:-4096\n" \
"v_mfma_f32_16x16x32_bf16 v[12:15], a[56:59], a[0:3], v[12:15]\n" \
"ds_read_b64_tr_b16 a[36:37], v214 offset:24576\n" \
"v_mfma_f32_16x16x32_bf16 v[12:15], a[60:63], a[4:7], v[12:15]\n" \
"ds_read_b128 a[0:3], v213 offset:0\n" \
"v_mfma_f32_16x16x32_bf16 v[28:31], a[56:59], a[8:11], v[28:31]\n" \
"ds_read_b128 a[4:7], v213 offset:2048\n" \
"v_mfma_f32_16x16x32_bf16 v[28:31], a[60:63], a[12:15], v[28:31]\n" \
"ds_read_b64_tr_b16 a[38:39], v214 offset:26624\n" \
"global_load_lds_dwordx4 v217, s[38:39] offset:-3072\n" \
"v_mfma_f32_16x16x32_bf16 v[24:27], a[48:51], a[8:11], v[24:27]\n" \
"ds_read_b64_tr_b16 a[40:41], v215 offset:16384\n" \
"v_mfma_f32_16x16x32_bf16 v[24:27], a[52:55], a[12:15], v[24:27]\n" \
"ds_read_b128 a[8:11], v213 offset:256\n" \
"v_mfma_f32_16x16x32_bf16 v[40:43], a[48:51], a[16:19], v[40:43]\n" \
"ds_read_b128 a[12:15], v213 offset:2304\n" \
"v_mfma_f32_16x16x32_bf16 v[40:43], a[52:55], a[20:23], v[40:43]\n" \
"ds_read_b64_tr_b16 a[42:43], v215 offset:18432\n" \
"global_load_lds_dwordx4 v218, s[38:39] offset:-2048\n" \
"v_mfma_f32_16x16x32_bf16 v[44:47], a[56:59], a[16:19], v[44:47]\n" \
"ds_read_b64_tr_b16 a[44:45], v215 offset:24576\n" \
"v_mfma_f32_16x16x32_bf16 v[44:47], a[60:63], a[20:23], v[44:47]\n" \
"ds_read_b128 a[16:19], v213 offset:4096\n" \
"global_load_lds_dwordx4 v219, s[38:39] offset:-1024\n" \
"v_mfma_f32_16x16x32_bf16 v[60:63], a[56:59], a[24:27], v[60:63]\n" \
"s_add_u32 s38, s38, 0x20000\n" \
"s_addc_u32 s39, s39, 0\n" \
"ds_read_b128 a[20:23], v213 offset:6144\n" \
"v_mfma_f32_16x16x32_bf16 v[60:63], a[60:63], a[28:31], v[60:63]\n" \
"s_sub_u32 s75, s75, 32768\n" \
"s_cselect_b32 s75, 131072, s75\n" \
"ds_read_b64_tr_b16 a[46:47], v215 offset:26624\n" \
"v_mfma_f32_16x16x32_bf16 v[56:59], a[48:51], a[24:27], v[56:59]\n" \
"s_sub_u32 s72, s72, 1\n" \
"s_cmp_lg_u32 s72, 0\n" \
"v_mfma_f32_16x16x32_bf16 v[56:59], a[52:55], a[28:31], v[56:59]\n" \
"s_waitcnt lgkmcnt(3)\n" \
"s_cbranch_scc1 Lo0_%=\n" \
"s_setpc_b64 s[68:69]\n" \
"Lrole1_%=:\n" \
"Lpg12503_%=:\n" \
"s_cbranch_execz Lcb1_%=\n" \
"s_sub_u32 s72, 50, s64\n" \
"s_call_b64 s[68:69], Lo0_%=\n" \
"buffer_load_dword v220, v221, s[56:59], s82 offen sc1\n" \
"s_mov_b32 s72, 2\n" \
"s_call_b64 s[68:69], Lo0_%=\n" \
"s_waitcnt vmcnt(16)\n" \
"v_cmp_ne_u32 vcc, s60, v220\n" \
"s_nop 1\n" \
"s_and_b32 vcc_hi, vcc_lo, 65535\n" \
"s_cbranch_scc1 Lpl5_%=\n" \
"Lpr5_%=:\n" \
"s_mov_b32 s36, s73\n" \
"s_mov_b64 s[38:39], s[40:41]\n" \
"s_mov_b32 s72, 4\n" \
"s_call_b64 s[68:69], Lo0_%=\n" \
"v_mfma_f32_16x16x32_bf16 v[64:67], a[32:35], a[0:3], 0\n" \
"ds_read_b128 a[24:27], v213 offset:4352\n" \
"s_add_u32 m0, s75, s95\n" \
"v_mfma_f32_16x16x32_bf16 v[64:67], a[36:39], a[4:7], v[64:67]\n" \
"s_waitcnt lgkmcnt(1)\n" \
"ds_read_b128 a[28:31], v213 offset:6400\n" \
"global_load_lds_dwordx4 v222, s[36:37] offset:0 sc1\n" \
"v_mfma_f32_16x16x32_bf16 v[68:71], a[40:43], a[0:3], 0\n" \
"s_sub_u32 s74, s74, 32768\n" \
"s_cselect_b32 s74, 131072, s74\n" \
"ds_read_b64_tr_b16 a[48:49], v214 offset:16896\n" \
"v_mfma_f32_16x16x32_bf16 v[68:71], a[44:47], a[4:7], v[68:71]\n" \
"ds_read_b64_tr_b16 a[50:51], v214 offset:18944\n" \
"v_cvt_pk_bf16_f32 v0, v0, v1\n" \
"v_mfma_f32_16x16x32_bf16 v[84:87], a[40:43], a[8:11], 0\n" \
"ds_read_b64_tr_b16 a[52:53], v214 offset:25088\n" \
"v_cvt_pk_bf16_f32 v1, v2, v3\n" \
"v_mfma_f32_16x16x32_bf16 v[84:87], a[44:47], a[12:15], v[84:87]\n" \
"ds_read_b64_tr_b16 a[54:55], v214 offset:27136\n" \
"global_load_lds_dwordx4 v222, s[36:37] offset:1024 sc1\n" \
"v_mfma_f32_16x16x32_bf16 v[80:83], a[32:35], a[8:11], 0\n" \
"ds_read_b64_tr_b16 a[56:57], v215 offset:16896\n" \
"v_cvt_pk_bf16_f32 v2, v4, v5\n" \
"v_mfma_f32_16x16x32_bf16 v[80:83], a[36:39], a[12:15], v[80:83]\n" \
"ds_read_b64_tr_b16 a[58:59], v215 offset:18944\n" \
"v_cvt_pk_bf16_f32 v3, v6, v7\n" \
"v_mfma_f32_16x16x32_bf16 v[96:99], a[32:35], a[16:19], 0\n" \
"s_add_u32 s71, s76, 0\n" \
"ds_read_b64_tr_b16 a[60:61], v215 offset:25088\n" \
"v_mfma_f32_16x16x32_bf16 v[96:99], a[36:39], a[20:23], v[96:99]\n" \
"ds_read_b64_tr_b16 a[62:63], v215 offset:27136\n" \
"global_load_lds_dwordx4 v222, s[36:37] offset:2048 sc1\n" \
"v_mfma_f32_16x16x32_bf16 v[100:103], a[40:43], a[16:19], 0\n" \
"v_add_u32 v213, s74, v211\n" \
"v_add_u32 v214, s74, v212\n" \
"v_mfma_f32_16x16x32_bf16 v[100:103], a[44:47], a[20:23], v[100:103]\n" \
"s_waitcnt lgkmcnt(3)\n" \
"v_xor_b32 v215, 32, v214\n" \
"v_mfma_f32_16x16x32_bf16 v[116:119], a[40:43], a[24:27], 0\n" \
"v_mfma_f32_16x16x32_bf16 v[116:119], a[44:47], a[28:31], v[116:119]\n" \
"global_load_lds_dwordx4 v222, s[36:37] offset:3072 sc1\n" \
"v_mfma_f32_16x16x32_bf16 v[112:115], a[32:35], a[24:27], 0\n" \
"s_waitcnt vmcnt(20) lgkmcnt(0)\n" \
"s_add_u32 s36, s36, 0x1000\n" \
"v_mfma_f32_16x16x32_bf16 v[112:115], a[36:39], a[28:31], v[112:115]\n" \
"s_barrier\n" \
"v_mfma_f32_16x16x32_bf16 v[72:75], a[48:51], a[0:3], 0\n" \
"s_add_u32 m0, m0, 0x5000\n" \
"ds_read_b64_tr_b16 a[32:33], v214 offset:16384\n" \
"v_mfma_f32_16x16x32_bf16 v[72:75], a[52:55], a[4:7], v[72:75]\n" \
"ds_read_b64_tr_b16 a[34:35], v214 offset:18432\n" \
"global_load_lds_dwordx4 v216, s[38:39] offset:-4096\n" \
"v_mfma_f32_16x16x32_bf16 v[76:79], a[56:59], a[0:3], 0\n" \
"ds_read_b64_tr_b16 a[36:37], v214 offset:24576\n" \
"v_mfma_f32_16x16x32_bf16 v[76:79], a[60:63], a[4:7], v[76:79]\n" \
"ds_read_b128 a[0:3], v213 offset:0\n" \
"v_mfma_f32_16x16x32_bf16 v[92:95], a[56:59], a[8:11], 0\n" \
"ds_read_b128 a[4:7], v213 offset:2048\n" \
"v_mfma_f32_16x16x32_bf16 v[92:95], a[60:63], a[12:15], v[92:95]\n" \
"ds_read_b64_tr_b16 a[38:39], v214 offset:26624\n" \
"global_load_lds_dwordx4 v217, s[38:39] offset:-3072\n" \
"v_mfma_f32_16x16x32_bf16 v[88:91], a[48:51], a[8:11], 0\n" \
"ds_read_b64_tr_b16 a[40:41], v215 offset:16384\n" \
"v_mfma_f32_16x16x32_bf16 v[88:91], a[52:55], a[12:15], v[88:91]\n" \
"buffer_store_dwordx4 v[0:3], v248, s[56:59], s71 offen offset:0 sc1\n" \
"ds_read_b128 a[8:11], v213 offset:256\n" \
"v_cvt_pk_bf16_f32 v4, v8, v9\n" \
"v_cvt_pk_bf16_f32 v5, v10, v11\n" \
"v_cvt_pk_bf16_f32 v6, v12, v13\n" \
"v_mfma_f32_16x16x32_bf16 v[104:107], a[48:51], a[16:19], 0\n" \
"ds_read_b128 a[12:15], v213 offset:2304\n" \
"v_cvt_pk_bf16_f32 v7, v14, v15\n" \
"v_mfma_f32_16x16x32_bf16 v[104:107], a[52:55], a[20:23], v[104:107]\n" \
"ds_read_b64_tr_b16 a[42:43], v215 offset:18432\n" \
"global_load_lds_dwordx4 v218, s[38:39] offset:-2048\n" \
"v_mfma_f32_16x16x32_bf16 v[108:111], a[56:59], a[16:19], 0\n" \
"ds_read_b64_tr_b16 a[44:45], v215 offset:24576\n" \
"v_mfma_f32_16x16x32_bf16 v[108:111], a[60:63], a[20:23], v[108:111]\n" \
"buffer_store_dwordx4 v[4:7], v248, s[56:59], s71 offen offset:1024 sc1\n" \
"ds_read_b128 a[16:19], v213 offset:4096\n" \
"v_cvt_pk_bf16_f32 v8, v16, v17\n" \
"v_cvt_pk_bf16_f32 v9, v18, v19\n" \
"v_cvt_pk_bf16_f32 v10, v20, v21\n" \
"Lpg12602_%=:\n" \
"s_cbranch_execz Lcb1_%=\n" \
"v_mfma_f32_16x16x32_bf16 v[124:127], a[56:59], a[24:27], 0\n" \
"ds_read_b128 a[20:23], v213 offset:6144\n" \
"global_load_lds_dwordx4 v219, s[38:39] offset:-1024\n" \
"v_mfma_f32_16x16x32_bf16 v[124:127], a[60:63], a[28:31], v[124:127]\n" \
"s_add_u32 s38, s38, 0x20000\n" \
"s_addc_u32 s39, s39, 0\n" \
"ds_read_b64_tr_b16 a[46:47], v215 offset:26624\n" \
"v_mfma_f32_16x16x32_bf16 v[120:123], a[48:51], a[24:27], 0\n" \
"s_sub_u32 s75, s75, 32768\n" \
"s_cselect_b32 s75, 131072, s75\n" \
"v_cvt_pk_bf16_f32 v11, v22, v23\n" \
"v_mfma_f32_16x16x32_bf16 v[120:123], a[52:55], a[28:31], v[120:123]\n" \
"s_waitcnt lgkmcnt(3)\n" \
"buffer_store_dwordx4 v[8:11], v248, s[56:59], s71 offen offset:2048 sc1\n" \
"v_cvt_pk_bf16_f32 v12, v24, v25\n" \
"v_cvt_pk_bf16_f32 v13, v26, v27\n" \
"v_cvt_pk_bf16_f32 v14, v28, v29\n" \
"v_cvt_pk_bf16_f32 v15, v30, v31\n" \
"v_mfma_f32_16x16x32_bf16 v[64:67], a[32:35], a[0:3], v[64:67]\n" \
"ds_read_b128 a[24:27], v213 offset:4352\n" \
"s_add_u32 m0, s75, s95\n" \
"v_mfma_f32_16x16x32_bf16 v[64:67], a[36:39], a[4:7], v[64:67]\n" \
"s_waitcnt lgkmcnt(1)\n" \
"ds_read_b128 a[28:31], v213 offset:6400\n" \
"global_load_lds_dwordx4 v222, s[36:37] offset:0 sc1\n" \
"v_mfma_f32_16x16x32_bf16 v[68:71], a[40:43], a[0:3], v[68:71]\n" \
"s_sub_u32 s74, s74, 32768\n" \
"s_cselect_b32 s74, 131072, s74\n" \
"ds_read_b64_tr_b16 a[48:49], v214 offset:16896\n" \
"v_mfma_f32_16x16x32_bf16 v[68:71], a[44:47], a[4:7], v[68:71]\n" \
"ds_read_b64_tr_b16 a[50:51], v214 offset:18944\n" \
"v_mfma_f32_16x16x32_bf16 v[84:87], a[40:43], a[8:11], v[84:87]\n" \
"ds_read_b64_tr_b16 a[52:53], v214 offset:25088\n" \
"v_mfma_f32_16x16x32_bf16 v[84:87], a[44:47], a[12:15], v[84:87]\n" \
"ds_read_b64_tr_b16 a[54:55], v214 offset:27136\n" \
"global_load_lds_dwordx4 v222, s[36:37] offset:1024 sc1\n" \
"v_mfma_f32_16x16x32_bf16 v[80:83], a[32:35], a[8:11], v[80:83]\n" \
"ds_read_b64_tr_b16 a[56:57], v215 offset:16896\n" \
"v_mfma_f32_16x16x32_bf16 v[80:83], a[36:39], a[12:15], v[80:83]\n" \
"ds_read_b64_tr_b16 a[58:59], v215 offset:18944\n" \
"v_mfma_f32_16x16x32_bf16 v[96:99], a[32:35], a[16:19], v[96:99]\n" \
"ds_read_b64_tr_b16 a[60:61], v215 offset:25088\n" \
"v_mfma_f32_16x16x32_bf16 v[96:99], a[36:39], a[20:23], v[96:99]\n" \
"ds_read_b64_tr_b16 a[62:63], v215 offset:27136\n" \
"global_load_lds_dwordx4 v222, s[36:37] offset:2048 sc1\n" \
"v_mfma_f32_16x16x32_bf16 v[100:103], a[40:43], a[16:19], v[100:103]\n" \
"v_add_u32 v213, s74, v211\n" \
"v_add_u32 v214, s74, v212\n" \
"v_mfma_f32_16x16x32_bf16 v[100:103], a[44:47], a[20:23], v[100:103]\n" \
"s_waitcnt lgkmcnt(3)\n" \
"v_xor_b32 v215, 32, v214\n" \
"v_mfma_f32_16x16x32_bf16 v[116:119], a[40:43], a[24:27], v[116:119]\n" \
"v_mfma_f32_16x16x32_bf16 v[116:119], a[44:47], a[28:31], v[116:119]\n" \
"global_load_lds_dwordx4 v222, s[36:37] offset:3072 sc1\n" \
"v_mfma_f32_16x16x32_bf16 v[112:115], a[32:35], a[24:27], v[112:115]\n" \
"s_waitcnt vmcnt(23) lgkmcnt(0)\n" \
"s_add_u32 s36, s36, 0x1000\n" \
"v_mfma_f32_16x16x32_bf16 v[112:115], a[36:39], a[28:31], v[112:115]\n" \
"s_barrier\n" \
"v_mfma_f32_16x16x32_bf16 v[72:75], a[48:51], a[0:3], v[72:75]\n" \
"s_add_u32 m0, m0, 0x5000\n" \
"ds_read_b64_tr_b16 a[32:33], v214 offset:16384\n" \
"v_mfma_f32_16x16x32_bf16 v[72:75], a[52:55], a[4:7], v[72:75]\n" \
"ds_read_b64_tr_b16 a[34:35], v214 offset:18432\n" \
"global_load_lds_dwordx4 v216, s[38:39] offset:-4096\n" \
"v_mfma_f32_16x16x32_bf16 v[76:79], a[56:59], a[0:3], v[76:79]\n" \
"ds_read_b64_tr_b16 a[36:37], v214 offset:24576\n" \
"v_mfma_f32_16x16x32_bf16 v[76:79], a[60:63], a[4:7], v[76:79]\n" \
"ds_read_b128 a[0:3], v213 offset:0\n" \
"v_mfma_f32_16x16x32_bf16 v[92:95], a[56:59], a[8:11], v[92:95]\n" \
"ds_read_b128 a[4:7], v213 offset:2048\n" \
"v_mfma_f32_16x16x32_bf16 v[92:95], a[60:63], a[12:15], v[92:95]\n" \
"ds_read_b64_tr_b16 a[38:39], v214 offset:26624\n" \
"global_load_lds_dwordx4 v217, s[38:39] offset:-3072\n" \
"v_mfma_f32_16x16x32_bf16 v[88:91], a[48:51], a[8:11], v[88:91]\n" \
"ds_read_b64_tr_b16 a[40:41], v215 offset:16384\n" \
"v_mfma_f32_16x16x32_bf16 v[88:91], a[52:55], a[12:15], v[88:91]\n" \
"buffer_store_dwordx4 v[12:15], v248, s[56:59], s71 offen offset:3072 sc1\n" \
"ds_read_b128 a[8:11], v213 offset:256\n" \
"v_cvt_pk_bf16_f32 v16, v32, v33\n" \
"v_cvt_pk_bf16_f32 v17, v34, v35\n" \
"v_cvt_pk_bf16_f32 v18, v36, v37\n" \
"v_mfma_f32_16x16x32_bf16 v[104:107], a[48:51], a[16:19], v[104:107]\n" \
"ds_read_b128 a[12:15], v213 offset:2304\n" \
"v_cvt_pk_bf16_f32 v19, v38, v39\n" \
"v_mfma_f32_16x16x32_bf16 v[104:107], a[52:55], a[20:23], v[104:107]\n" \
"s_add_u32 s71, s76, 4096\n" \
"ds_read_b64_tr_b16 a[42:43], v215 offset:18432\n" \
"global_load_lds_dwordx4 v218, s[38:39] offset:-2048\n" \
"v_mfma_f32_16x16x32_bf16 v[108:111], a[56:59], a[16:19], v[108:111]\n" \
"ds_read_b64_tr_b16 a[44:45], v215 offset:24576\n" \
"v_mfma_f32_16x16x32_bf16 v[108:111], a[60:63], a[20:23], v[108:111]\n" \
"buffer_store_dwordx4 v[16:19], v248, s[56:59], s71 offen offset:0 sc1\n" \
"ds_read_b128 a[16:19], v213 offset:4096\n" \
"v_cvt_pk_bf16_f32 v20, v40, v41\n" \
"v_cvt_pk_bf16_f32 v21, v42, v43\n" \
"v_cvt_pk_bf16_f32 v22, v44, v45\n" \
"v_mfma_f32_16x16x32_bf16 v[124:127], a[56:59], a[24:27], v[124:127]\n" \
"ds_read_b128 a[20:23], v213 offset:6144\n" \
"global_load_lds_dwordx4 v219, s[38:39] offset:-1024\n" \
"v_mfma_f32_16x16x32_bf16 v[124:127], a[60:63], a[28:31], v[124:127]\n" \
"s_add_u32 s38, s38, 0x20000\n" \
"s_addc_u32 s39, s39, 0\n" \
"ds_read_b64_tr_b16 a[46:47], v215 offset:26624\n" \
"v_mfma_f32_16x16x32_bf16 v[120:123], a[48:51], a[24:27], v[120:123]\n" \
"s_sub_u32 s75, s75, 32768\n" \
"s_cselect_b32 s75, 131072, s75\n" \
"v_cvt_pk_bf16_f32 v23, v46, v47\n" \
"v_mfma_f32_16x16x32_bf16 v[120:123], a[52:55], a[28:31], v[120:123]\n" \
"s_waitcnt lgkmcnt(3)\n" \
"buffer_store_dwordx4 v[20:23], v248, s[56:59], s71 offen offset:1024 sc1\n" \
"v_cvt_pk_bf16_f32 v24, v48, v49\n" \
"v_cvt_pk_bf16_f32 v25, v50, v51\n" \
"v_cvt_pk_bf16_f32 v26, v52, v53\n" \
"v_cvt_pk_bf16_f32 v27, v54, v55\n" \
"v_mfma_f32_16x16x32_bf16 v[64:67], a[32:35], a[0:3], v[64:67]\n" \
"ds_read_b128 a[24:27], v213 offset:4352\n" \
"s_add_u32 m0, s75, s95\n" \
"v_mfma_f32_16x16x32_bf16 v[64:67], a[36:39], a[4:7], v[64:67]\n" \
"s_waitcnt lgkmcnt(1)\n" \
"ds_read_b128 a[28:31], v213 offset:6400\n" \
"global_load_lds_dwordx4 v222, s[36:37] offset:0 sc1\n" \
"v_mfma_f32_16x16x32_bf16 v[68:71], a[40:43], a[0:3], v[68:71]\n" \
"s_sub_u32 s74, s74, 32768\n" \
"s_cselect_b32 s74, 131072, s74\n" \
"ds_read_b64_tr_b16 a[48:49], v214 offset:16896\n" \
"v_mfma_f32_16x16x32_bf16 v[68:71], a[44:47], a[4:7], v[68:71]\n" \
"ds_read_b64_tr_b16 a[50:51], v214 offset:18944\n" \
"v_mfma_f32_16x16x32_bf16 v[84:87], a[40:43], a[8:11], v[84:87]\n" \
"ds_read_b64_tr_b16 a[52:53], v214 offset:25088\n" \
"v_mfma_f32_16x16x32_bf16 v[84:87], a[44:47], a[12:15], v[84:87]\n" \
"ds_read_b64_tr_b16 a[54:55], v214 offset:27136\n" \
"global_load_lds_dwordx4 v222, s[36:37] offset:1024 sc1\n" \
"v_mfma_f32_16x16x32_bf16 v[80:83], a[32:35], a[8:11], v[80:83]\n" \
"ds_read_b64_tr_b16 a[56:57], v215 offset:16896\n" \
"v_mfma_f32_16x16x32_bf16 v[80:83], a[36:39], a[12:15], v[80:83]\n" \
"ds_read_b64_tr_b16 a[58:59], v215 offset:18944\n" \
"v_mfma_f32_16x16x32_bf16 v[96:99], a[32:35], a[16:19], v[96:99]\n" \
"ds_read_b64_tr_b16 a[60:61], v215 offset:25088\n" \
"v_mfma_f32_16x16x32_bf16 v[96:99], a[36:39], a[20:23], v[96:99]\n" \
"ds_read_b64_tr_b16 a[62:63], v215 offset:27136\n" \
"global_load_lds_dwordx4 v222, s[36:37] offset:2048 sc1\n" \
"v_mfma_f32_16x16x32_bf16 v[100:103], a[40:43], a[16:19], v[100:103]\n" \
"v_add_u32 v213, s74, v211\n" \
"v_add_u32 v214, s74, v212\n" \
"v_mfma_f32_16x16x32_bf16 v[100:103], a[44:47], a[20:23], v[100:103]\n" \
"s_waitcnt lgkmcnt(3)\n" \
"v_xor_b32 v215, 32, v214\n" \
"v_mfma_f32_16x16x32_bf16 v[116:119], a[40:43], a[24:27], v[116:119]\n" \
"v_mfma_f32_16x16x32_bf16 v[116:119], a[44:47], a[28:31], v[116:119]\n" \
"global_load_lds_dwordx4 v222, s[36:37] offset:3072 sc1\n" \
"v_mfma_f32_16x16x32_bf16 v[112:115], a[32:35], a[24:27], v[112:115]\n" \
"s_waitcnt vmcnt(26) lgkmcnt(0)\n" \
"s_add_u32 s36, s36, 0x1000\n" \
"v_mfma_f32_16x16x32_bf16 v[112:115], a[36:39], a[28:31], v[112:115]\n" \
"s_barrier\n" \
"v_mfma_f32_16x16x32_bf16 v[72:75], a[48:51], a[0:3], v[72:75]\n" \
"s_add_u32 m0, m0, 0x5000\n" \
"ds_read_b64_tr_b16 a[32:33], v214 offset:16384\n" \
"v_mfma_f32_16x16x32_bf16 v[72:75], a[52:55], a[4:7], v[72:75]\n" \
"ds_read_b64_tr_b16 a[34:35], v214 offset:18432\n" \
"global_load_lds_dwordx4 v216, s[38:39] offset:-4096\n" \
"v_mfma_f32_16x16x32_bf16 v[76:79], a[56:59], a[0:3], v[76:79]\n" \
"ds_read_b64_tr_b16 a[36:37], v214 offset:24576\n" \
"v_mfma_f32_16x16x32_bf16 v[76:79], a[60:63], a[4:7], v[76:79]\n" \
"ds_read_b128 a[0:3], v213 offset:0\n" \
"v_mfma_f32_16x16x32_bf16 v[92:95], a[56:59], a[8:11], v[92:95]\n" \
"ds_read_b128 a[4:7], v213 offset:2048\n" \
"v_mfma_f32_16x16x32_bf16 v[92:95], a[60:63], a[12:15], v[92:95]\n" \
"ds_read_b64_tr_b16 a[38:39], v214 offset:26624\n" \
"global_load_lds_dwordx4 v217, s[38:39] offset:-3072\n" \
"v_mfma_f32_16x16x32_bf16 v[88:91], a[48:51], a[8:11], v[88:91]\n" \
"ds_read_b64_tr_b16 a[40:41], v215 offset:16384\n" \
"v_mfma_f32_16x16x32_bf16 v[88:91], a[52:55], a[12:15], v[88:91]\n" \
"buffer_store_dwordx4 v[24:27], v248, s[56:59], s71 offen offset:2048 sc1\n" \
"ds_read_b128 a[8:11], v213 offset:256\n" \
"v_cvt_pk_bf16_f32 v28, v56, v57\n" \
"v_cvt_pk_bf16_f32 v29, v58, v59\n" \
"v_cvt_pk_bf16_f32 v30, v60, v61\n" \
"v_mfma_f32_16x16x32_bf16 v[104:107], a[48:51], a[16:19], v[104:107]\n" \
"ds_read_b128 a[12:15], v213 offset:2304\n" \
"v_cvt_pk_bf16_f32 v31, v62, v63\n" \
"v_mfma_f32_16x16x32_bf16 v[104:107], a[52:55], a[20:23], v[104:107]\n" \
"ds_read_b64_tr_b16 a[42:43], v215 offset:18432\n" \
"global_load_lds_dwordx4 v218, s[38:39] offset:-2048\n" \
"v_mfma_f32_16x16x32_bf16 v[108:111], a[56:59], a[16:19], v[108:111]\n" \
"ds_read_b64_tr_b16 a[44:45], v215 offset:24576\n" \
"v_mfma_f32_16x16x32_bf16 v[108:111], a[60:63], a[20:23], v[108:111]\n" \
"buffer_store_dwordx4 v[28:31], v248, s[56:59], s71 offen offset:3072 sc1\n" \
"ds_read_b128 a[16:19], v213 offset:4096\n" \
"v_mfma_f32_16x16x32_bf16 v[124:127], a[56:59], a[24:27], v[124:127]\n" \
"ds_read_b128 a[20:23], v213 offset:6144\n" \
"global_load_lds_dwordx4 v219, s[38:39] offset:-1024\n" \
"v_mfma_f32_16x16x32_bf16 v[124:127], a[60:63], a[28:31], v[124:127]\n" \
"s_add_u32 s38, s38, 0x20000\n" \
"s_addc_u32 s39, s39, 0\n" \
"ds_read_b64_tr_b16 a[46:47], v215 offset:26624\n" \
"v_mfma_f32_16x16x32_bf16 v[120:123], a[48:51], a[24:27], v[120:123]\n" \
"s_sub_u32 s75, s75, 32768\n" \
"s_cselect_b32 s75, 131072, s75\n" \
"v_mfma_f32_16x16x32_bf16 v[120:123], a[52:55], a[28:31], v[120:123]\n" \
"s_waitcnt lgkmcnt(3)\n" \
"v_mfma_f32_16x16x32_bf16 v[64:67], a[32:35], a[0:3], v[64:67]\n" \
"ds_read_b128 a[24:27], v213 offset:4352\n" \
"s_add_u32 m0, s75, s95\n" \
"v_mfma_f32_16x16x32_bf16 v[64:67], a[36:39], a[4:7], v[64:67]\n" \
"s_waitcnt lgkmcnt(1)\n" \
"ds_read_b128 a[28:31], v213 offset:6400\n" \
"global_load_lds_dwordx4 v222, s[36:37] offset:0 sc1\n" \
"v_mfma_f32_16x16x32_bf16 v[68:71], a[40:43], a[0:3], v[68:71]\n" \
"s_sub_u32 s74, s74, 32768\n" \
"s_cselect_b32 s74, 131072, s74\n" \
"ds_read_b64_tr_b16 a[48:49], v214 offset:16896\n" \
"v_mfma_f32_16x16x32_bf16 v[68:71], a[44:47], a[4:7], v[68:71]\n" \
"ds_read_b64_tr_b16 a[50:51], v214 offset:18944\n" \
"v_mfma_f32_16x16x32_bf16 v[84:87], a[40:43], a[8:11], v[84:87]\n" \
"ds_read_b64_tr_b16 a[52:53], v214 offset:25088\n" \
"v_mfma_f32_16x16x32_bf16 v[84:87], a[44:47], a[12:15], v[84:87]\n" \
"ds_read_b64_tr_b16 a[54:55], v214 offset:27136\n" \
"global_load_lds_dwordx4 v222, s[36:37] offset:1024 sc1\n" \
"v_mfma_f32_16x16x32_bf16 v[80:83], a[32:35], a[8:11], v[80:83]\n" \
"ds_read_b64_tr_b16 a[56:57], v215 offset:16896\n" \
"v_mfma_f32_16x16x32_bf16 v[80:83], a[36:39], a[12:15], v[80:83]\n" \
"ds_read_b64_tr_b16 a[58:59], v215 offset:18944\n" \
"v_mfma_f32_16x16x32_bf16 v[96:99], a[32:35], a[16:19], v[96:99]\n" \
"ds_read_b64_tr_b16 a[60:61], v215 offset:25088\n" \
"v_mfma_f32_16x16x32_bf16 v[96:99], a[36:39], a[20:23], v[96:99]\n" \
"ds_read_b64_tr_b16 a[62:63], v215 offset:27136\n" \
"global_load_lds_dwordx4 v222, s[36:37] offset:2048 sc1\n" \
"v_mfma_f32_16x16x32_bf16 v[100:103], a[40:43], a[16:19], v[100:103]\n" \
"v_add_u32 v213, s74, v211\n" \
"v_add_u32 v214, s74, v212\n" \
"v_mfma_f32_16x16x32_bf16 v[100:103], a[44:47], a[20:23], v[100:103]\n" \
"s_waitcnt lgkmcnt(3)\n" \
"v_xor_b32 v215, 32, v214\n" \
"v_mfma_f32_16x16x32_bf16 v[116:119], a[40:43], a[24:27], v[116:119]\n" \
"v_mfma_f32_16x16x32_bf16 v[116:119], a[44:47], a[28:31], v[116:119]\n" \
"global_load_lds_dwordx4 v222, s[36:37] offset:3072 sc1\n" \
"v_mfma_f32_16x16x32_bf16 v[112:115], a[32:35], a[24:27], v[112:115]\n" \
"s_waitcnt vmcnt(26) lgkmcnt(0)\n" \
"s_add_u32 s36, s36, 0x1000\n" \
"v_mfma_f32_16x16x32_bf16 v[112:115], a[36:39], a[28:31], v[112:115]\n" \
"s_barrier\n" \
"v_mfma_f32_16x16x32_bf16 v[72:75], a[48:51], a[0:3], v[72:75]\n" \
"s_add_u32 m0, m0, 0x5000\n" \
"ds_read_b64_tr_b16 a[32:33], v214 offset:16384\n" \
"v_mfma_f32_16x16x32_bf16 v[72:75], a[52:55], a[4:7], v[72:75]\n" \
"ds_read_b64_tr_b16 a[34:35], v214 offset:18432\n" \
"global_load_lds_dwordx4 v216, s[38:39] offset:-4096\n" \
"v_mfma_f32_16x16x32_bf16 v[76:79], a[56:59], a[0:3], v[76:79]\n" \
"ds_read_b64_tr_b16 a[36:37], v214 offset:24576\n" \
"v_mfma_f32_16x16x32_bf16 v[76:79], a[60:63], a[4:7], v[76:79]\n" \
"ds_read_b128 a[0:3], v213 offset:0\n" \
"v_mfma_f32_16x16x32_bf16 v[92:95], a[56:59], a[8:11], v[92:95]\n" \
"ds_read_b128 a[4:7], v213 offset:2048\n" \
"v_mfma_f32_16x16x32_bf16 v[92:95], a[60:63], a[12:15], v[92:95]\n" \
"ds_read_b64_tr_b16 a[38:39], v214 offset:26624\n" \
"global_load_lds_dwordx4 v217, s[38:39] offset:-3072\n" \
"v_mfma_f32_16x16x32_bf16 v[88:91], a[48:51], a[8:11], v[88:91]\n" \
"ds_read_b64_tr_b16 a[40:41], v215 offset:16384\n" \
"v_mfma_f32_16x16x32_bf16 v[88:91], a[52:55], a[12:15], v[88:91]\n" \
"ds_read_b128 a[8:11], v213 offset:256\n" \
"v_mfma_f32_16x16x32_bf16 v[104:107], a[48:51], a[16:19], v[104:107]\n" \
"ds_read_b128 a[12:15], v213 offset:2304\n" \
"v_mfma_f32_16x16x32_bf16 v[104:107], a[52:55], a[20:23], v[104:107]\n" \
"ds_read_b64_tr_b16 a[42:43], v215 offset:18432\n" \
"global_load_lds_dwordx4 v218, s[38:39] offset:-2048\n" \
"v_mfma_f32_16x16x32_bf16 v[108:111], a[56:59], a[16:19], v[108:111]\n" \
"ds_read_b64_tr_b16 a[44:45], v215 offset:24576\n" \
"v_mfma_f32_16x16x32_bf16 v[108:111], a[60:63], a[20:23], v[108:111]\n" \
"ds_read_b128 a[16:19], v213 offset:4096\n" \
"v_mfma_f32_16x16x32_bf16 v[124:127], a[56:59], a[24:27], v[124:127]\n" \
"ds_read_b128 a[20:23], v213 offset:6144\n" \
"global_load_lds_dwordx4 v219, s[38:39] offset:-1024\n" \
"v_mfma_f32_16x16x32_bf16 v[124:127], a[60:63], a[28:31], v[124:127]\n" \
"s_add_u32 s38, s38, 0x20000\n" \
"s_addc_u32 s39, s39, 0\n" \
"ds_read_b64_tr_b16 a[46:47], v215 offset:26624\n" \
"v_mfma_f32_16x16x32_bf16 v[120:123], a[48:51], a[24:27], v[120:123]\n" \
"s_sub_u32 s75, s75, 32768\n" \
"s_cselect_b32 s75, 131072, s75\n" \
"v_mfma_f32_16x16x32_bf16 v[120:123], a[52:55], a[28:31], v[120:123]\n" \
"s_waitcnt lgkmcnt(3)\n" \
"v_mfma_f32_16x16x32_bf16 v[64:67], a[32:35], a[0:3], v[64:67]\n" \
"ds_read_b128 a[24:27], v213 offset:4352\n" \
"s_add_u32 m0, s75, s95\n" \
"v_mfma_f32_16x16x32_bf16 v[64:67], a[36:39], a[4:7], v[64:67]\n" \
"s_waitcnt lgkmcnt(1)\n" \
"ds_read_b128 a[28:31], v213 offset:6400\n" \
"global_load_lds_dwordx4 v222, s[36:37] offset:0 sc1\n" \
"v_mfma_f32_16x16x32_bf16 v[68:71], a[40:43], a[0:3], v[68:71]\n" \
"s_sub_u32 s74, s74, 32768\n" \
"s_cselect_b32 s74, 131072, s74\n" \
"ds_read_b64_tr_b16 a[48:49], v214 offset:16896\n" \
"v_mfma_f32_16x16x32_bf16 v[68:71], a[44:47], a[4:7], v[68:71]\n" \
"ds_read_b64_tr_b16 a[50:51], v214 offset:18944\n" \
"v_mfma_f32_16x16x32_bf16 v[84:87], a[40:43], a[8:11], v[84:87]\n" \
"ds_read_b64_tr_b16 a[52:53], v214 offset:25088\n" \
"v_mfma_f32_16x16x32_bf16 v[84:87], a[44:47], a[12:15], v[84:87]\n" \
"ds_read_b64_tr_b16 a[54:55], v214 offset:27136\n" \
"global_load_lds_dwordx4 v222, s[36:37] offset:1024 sc1\n" \
"v_mfma_f32_16x16x32_bf16 v[80:83], a[32:35], a[8:11], v[80:83]\n" \
"ds_read_b64_tr_b16 a[56:57], v215 offset:16896\n" \
"v_mfma_f32_16x16x32_bf16 v[80:83], a[36:39], a[12:15], v[80:83]\n" \
"ds_read_b64_tr_b16 a[58:59], v215 offset:18944\n" \
"v_mfma_f32_16x16x32_bf16 v[96:99], a[32:35], a[16:19], v[96:99]\n" \
"ds_read_b64_tr_b16 a[60:61], v215 offset:25088\n" \
"v_mfma_f32_16x16x32_bf16 v[96:99], a[36:39], a[20:23], v[96:99]\n" \
"ds_read_b64_tr_b16 a[62:63], v215 offset:27136\n" \
"global_load_lds_dwordx4 v222, s[36:37] offset:2048 sc1\n" \
"v_mfma_f32_16x16x32_bf16 v[100:103], a[40:43], a[16:19], v[100:103]\n" \
"v_add_u32 v213, s74, v211\n" \
"v_add_u32 v214, s74, v212\n" \
"v_mfma_f32_16x16x32_bf16 v[100:103], a[44:47], a[20:23], v[100:103]\n" \
"s_waitcnt lgkmcnt(3)\n" \
"v_xor_b32 v215, 32, v214\n" \
"v_mfma_f32_16x16x32_bf16 v[116:119], a[40:43], a[24:27], v[116:119]\n" \
"v_mfma_f32_16x16x32_bf16 v[116:119], a[44:47], a[28:31], v[116:119]\n" \
"global_load_lds_dwordx4 v222, s[36:37] offset:3072 sc1\n" \
"v_mfma_f32_16x16x32_bf16 v[112:115], a[32:35], a[24:27], v[112:115]\n" \
"s_waitcnt vmcnt(23) lgkmcnt(0)\n" \
"s_add_u32 s36, s36, 0x1000\n" \
"v_mfma_f32_16x16x32_bf16 v[112:115], a[36:39], a[28:31], v[112:115]\n" \
"s_barrier\n" \
"v_mfma_f32_16x16x32_bf16 v[72:75], a[48:51], a[0:3], v[72:75]\n" \
"s_add_u32 m0, m0, 0x5000\n" \
"ds_read_b64_tr_b16 a[32:33], v214 offset:16384\n" \
"v_mfma_f32_16x16x32_bf16 v[72:75], a[52:55], a[4:7], v[72:75]\n" \
"ds_read_b64_tr_b16 a[34:35], v214 offset:18432\n" \
"global_load_lds_dwordx4 v216, s[38:39] offset:-4096\n" \
"v_mfma_f32_16x16x32_bf16 v[76:79], a[56:59], a[0:3], v[76:79]\n" \
"ds_read_b64_tr_b16 a[36:37], v214 offset:24576\n" \
"v_mfma_f32_16x16x32_bf16 v[76:79], a[60:63], a[4:7], v[76:79]\n" \
"ds_read_b128 a[0:3], v213 offset:0\n" \
"v_mfma_f32_16x16x32_bf16 v[92:95], a[56:59], a[8:11], v[92:95]\n" \
"ds_read_b128 a[4:7], v213 offset:2048\n" \
"v_mfma_f32_16x16x32_bf16 v[92:95], a[60:63], a[12:15], v[92:95]\n" \
"ds_read_b64_tr_b16 a[38:39], v214 offset:26624\n" \
"global_load_lds_dwordx4 v217, s[38:39] offset:-3072\n" \
"v_mfma_f32_16x16x32_bf16 v[88:91], a[48:51], a[8:11], v[88:91]\n" \
"ds_read_b64_tr_b16 a[40:41], v215 offset:16384\n" \
"v_mfma_f32_16x16x32_bf16 v[88:91], a[52:55], a[12:15], v[88:91]\n" \
"ds_read_b128 a[8:11], v213 offset:256\n" \
"v_mfma_f32_16x16x32_bf16 v[104:107], a[48:51], a[16:19], v[104:107]\n" \
"ds_read_b128 a[12:15], v213 offset:2304\n" \
"v_mfma_f32_16x16x32_bf16 v[104:107], a[52:55], a[20:23], v[104:107]\n" \
"ds_read_b64_tr_b16 a[42:43], v215 offset:18432\n" \
"global_load_lds_dwordx4 v218, s[38:39] offset:-2048\n" \
"v_mfma_f32_16x16x32_bf16 v[108:111], a[56:59], a[16:19], v[108:111]\n" \
"ds_read_b64_tr_b16 a[44:45], v215 offset:24576\n" \
"v_mfma_f32_16x16x32_bf16 v[108:111], a[60:63], a[20:23], v[108:111]\n" \
"ds_read_b128 a[16:19], v213 offset:4096\n" \
"v_mfma_f32_16x16x32_bf16 v[124:127], a[56:59], a[24:27], v[124:127]\n" \
"ds_read_b128 a[20:23], v213 offset:6144\n" \
"global_load_lds_dwordx4 v219, s[38:39] offset:-1024\n" \
"v_mfma_f32_16x16x32_bf16 v[124:127], a[60:63], a[28:31], v[124:127]\n" \
"s_add_u32 s38, s38, 0x20000\n" \
"s_addc_u32 s39, s39, 0\n" \
"ds_read_b64_tr_b16 a[46:47], v215 offset:26624\n" \
"v_mfma_f32_16x16x32_bf16 v[120:123], a[48:51], a[24:27], v[120:123]\n" \
"s_sub_u32 s75, s75, 32768\n" \
"s_cselect_b32 s75, 131072, s75\n" \
"v_mfma_f32_16x16x32_bf16 v[120:123], a[52:55], a[28:31], v[120:123]\n" \
"s_waitcnt lgkmcnt(3)\n" \
"v_mfma_f32_16x16x32_bf16 v[64:67], a[32:35], a[0:3], v[64:67]\n" \
"ds_read_b128 a[24:27], v213 offset:4352\n" \
"s_add_u32 m0, s75, s95\n" \
"v_mfma_f32_16x16x32_bf16 v[64:67], a[36:39], a[4:7], v[64:67]\n" \
"s_waitcnt lgkmcnt(1)\n" \
"ds_read_b128 a[28:31], v213 offset:6400\n" \
"global_load_lds_dwordx4 v222, s[36:37] offset:0 sc1\n" \
"v_mfma_f32_16x16x32_bf16 v[68:71], a[40:43], a[0:3], v[68:71]\n" \
"s_sub_u32 s74, s74, 32768\n" \
"s_cselect_b32 s74, 131072, s74\n" \
"ds_read_b64_tr_b16 a[48:49], v214 offset:16896\n" \
"v_mfma_f32_16x16x32_bf16 v[68:71], a[44:47], a[4:7], v[68:71]\n" \
"ds_read_b64_tr_b16 a[50:51], v214 offset:18944\n" \
"v_mfma_f32_16x16x32_bf16 v[84:87], a[40:43], a[8:11], v[84:87]\n" \
"ds_read_b64_tr_b16 a[52:53], v214 offset:25088\n" \
"v_mfma_f32_16x16x32_bf16 v[84:87], a[44:47], a[12:15], v[84:87]\n" \
"ds_read_b64_tr_b16 a[54:55], v214 offset:27136\n" \
"global_load_lds_dwordx4 v222, s[36:37] offset:1024 sc1\n" \
"v_mfma_f32_16x16x32_bf16 v[80:83], a[32:35], a[8:11], v[80:83]\n" \
"ds_read_b64_tr_b16 a[56:57], v215 offset:16896\n" \
"v_mfma_f32_16x16x32_bf16 v[80:83], a[36:39], a[12:15], v[80:83]\n" \
"ds_read_b64_tr_b16 a[58:59], v215 offset:18944\n" \
"v_mfma_f32_16x16x32_bf16 v[96:99], a[32:35], a[16:19], v[96:99]\n" \
"ds_read_b64_tr_b16 a[60:61], v215 offset:25088\n" \
"v_mfma_f32_16x16x32_bf16 v[96:99], a[36:39], a[20:23], v[96:99]\n" \
"ds_read_b64_tr_b16 a[62:63], v215 offset:27136\n" \
"global_load_lds_dwordx4 v222, s[36:37] offset:2048 sc1\n" \
"v_mfma_f32_16x16x32_bf16 v[100:103], a[40:43], a[16:19], v[100:103]\n" \
"v_add_u32 v213, s74, v211\n" \
"v_add_u32 v214, s74, v212\n" \
"v_mfma_f32_16x16x32_bf16 v[100:103], a[44:47], a[20:23], v[100:103]\n" \
"s_waitcnt lgkmcnt(3)\n" \
"v_xor_b32 v215, 32, v214\n" \
"v_mfma_f32_16x16x32_bf16 v[116:119], a[40:43], a[24:27], v[116:119]\n" \
"v_mfma_f32_16x16x32_bf16 v[116:119], a[44:47], a[28:31], v[116:119]\n" \
"global_load_lds_dwordx4 v222, s[36:37] offset:3072 sc1\n" \
"v_mfma_f32_16x16x32_bf16 v[112:115], a[32:35], a[24:27], v[112:115]\n" \
"s_waitcnt vmcnt(20) lgkmcnt(0)\n" \
"s_add_u32 s36, s36, 0x1000\n" \
"v_mfma_f32_16x16x32_bf16 v[112:115], a[36:39], a[28:31], v[112:115]\n" \
"s_barrier\n" \
"v_mfma_f32_16x16x32_bf16 v[72:75], a[48:51], a[0:3], v[72:75]\n" \
"s_add_u32 m0, m0, 0x5000\n" \
"ds_read_b64_tr_b16 a[32:33], v214 offset:16384\n" \
"v_mfma_f32_16x16x32_bf16 v[72:75], a[52:55], a[4:7], v[72:75]\n" \
"ds_read_b64_tr_b16 a[34:35], v214 offset:18432\n" \
"global_load_lds_dwordx4 v216, s[38:39] offset:-4096\n" \
"v_mfma_f32_16x16x32_bf16 v[76:79], a[56:59], a[0:3], v[76:79]\n" \
"ds_read_b64_tr_b16 a[36:37], v214 offset:24576\n" \
"v_mfma_f32_16x16x32_bf16 v[76:79], a[60:63], a[4:7], v[76:79]\n" \
"buffer_store_dword v224, v225, s[56:59], s79 offen sc1\n" \
"ds_read_b128 a[0:3], v213 offset:0\n" \
"v_mfma_f32_16x16x32_bf16 v[92:95], a[56:59], a[8:11], v[92:95]\n" \
"ds_read_b128 a[4:7], v213 offset:2048\n" \
"v_mfma_f32_16x16x32_bf16 v[92:95], a[60:63], a[12:15], v[92:95]\n" \
"ds_read_b64_tr_b16 a[38:39], v214 offset:26624\n" \
"global_load_lds_dwordx4 v217, s[38:39] offset:-3072\n" \
"v_mfma_f32_16x16x32_bf16 v[88:91], a[48:51], a[8:11], v[88:91]\n" \
"ds_read_b64_tr_b16 a[40:41], v215 offset:16384\n" \
"v_mfma_f32_16x16x32_bf16 v[88:91], a[52:55], a[12:15], v[88:91]\n" \
"ds_read_b128 a[8:11], v213 offset:256\n" \
"v_mfma_f32_16x16x32_bf16 v[104:107], a[48:51], a[16:19], v[104:107]\n" \
"ds_read_b128 a[12:15], v213 offset:2304\n" \
"v_mfma_f32_16x16x32_bf16 v[104:107], a[52:55], a[20:23], v[104:107]\n" \
"ds_read_b64_tr_b16 a[42:43], v215 offset:18432\n" \
"global_load_lds_dwordx4 v218, s[38:39] offset:-2048\n" \
"v_mfma_f32_16x16x32_bf16 v[108:111], a[56:59], a[16:19], v[108:111]\n" \
"ds_read_b64_tr_b16 a[44:45], v215 offset:24576\n" \
"v_mfma_f32_16x16x32_bf16 v[108:111], a[60:63], a[20:23], v[108:111]\n" \
"ds_read_b128 a[16:19], v213 offset:4096\n" \
"v_mfma_f32_16x16x32_bf16 v[124:127], a[56:59], a[24:27], v[124:127]\n" \
"ds_read_b128 a[20:23], v213 offset:6144\n" \
"global_load_lds_dwordx4 v219, s[38:39] offset:-1024\n" \
"v_mfma_f32_16x16x32_bf16 v[124:127], a[60:63], a[28:31], v[124:127]\n" \
"s_add_u32 s38, s38, 0x20000\n" \
"s_addc_u32 s39, s39, 0\n" \
"ds_read_b64_tr_b16 a[46:47], v215 offset:26624\n" \
"v_mfma_f32_16x16x32_bf16 v[120:123], a[48:51], a[24:27], v[120:123]\n" \
"s_sub_u32 s75, s75, 32768\n" \
"s_cselect_b32 s75, 131072, s75\n" \
"v_mfma_f32_16x16x32_bf16 v[120:123], a[52:55], a[28:31], v[120:123]\n" \
"s_waitcnt lgkmcnt(3)\n" \
"s_mov_b32 s72, 6\n" \
"s_call_b64 s[68:69], Lo64_%=\n" \
"v_mfma_f32_16x16x32_bf16 v[64:67], a[32:35], a[0:3], v[64:67]\n" \
"ds_read_b128 a[24:27], v213 offset:4352\n" \
"v_mfma_f32_16x16x32_bf16 v[64:67], a[36:39], a[4:7], v[64:67]\n" \
"s_waitcnt lgkmcnt(1)\n" \
"ds_read_b128 a[28:31], v213 offset:6400\n" \
"v_mfma_f32_16x16x32_bf16 v[68:71], a[40:43], a[0:3], v[68:71]\n" \
"s_sub_u32 s74, s74, 32768\n" \
"s_cselect_b32 s74, 131072, s74\n" \
"ds_read_b64_tr_b16 a[48:49], v214 offset:16896\n" \
"v_mfma_f32_16x16x32_bf16 v[68:71], a[44:47], a[4:7], v[68:71]\n" \
"ds_read_b64_tr_b16 a[50:51], v214 offset:18944\n" \
"v_mfma_f32_16x16x32_bf16 v[84:87], a[40:43], a[8:11], v[84:87]\n" \
"ds_read_b64_tr_b16 a[52:53], v214 offset:25088\n" \
"v_mfma_f32_16x16x32_bf16 v[84:87], a[44:47], a[12:15], v[84:87]\n" \
"ds_read_b64_tr_b16 a[54:55], v214 offset:27136\n" \
"v_mfma_f32_16x16x32_bf16 v[80:83], a[32:35], a[8:11], v[80:83]\n" \
"ds_read_b64_tr_b16 a[56:57], v215 offset:16896\n" \
"v_mfma_f32_16x16x32_bf16 v[80:83], a[36:39], a[12:15], v[80:83]\n" \
"ds_read_b64_tr_b16 a[58:59], v215 offset:18944\n" \
"v_mfma_f32_16x16x32_bf16 v[96:99], a[32:35], a[16:19], v[96:99]\n" \
"ds_read_b64_tr_b16 a[60:61], v215 offset:25088\n" \
"v_mfma_f32_16x16x32_bf16 v[96:99], a[36:39], a[20:23], v[96:99]\n" \
"ds_read_b64_tr_b16 a[62:63], v215 offset:27136\n" \
"v_mfma_f32_16x16x32_bf16 v[100:103], a[40:43], a[16:19], v[100:103]\n" \
"v_add_u32 v213, s74, v211\n" \
"v_add_u32 v214, s74, v212\n" \
"v_mfma_f32_16x16x32_bf16 v[100:103], a[44:47], a[20:23], v[100:103]\n" \
"s_waitcnt lgkmcnt(3)\n" \
"v_xor_b32 v215, 32, v214\n" \
"v_mfma_f32_16x16x32_bf16 v[116:119], a[40:43], a[24:27], v[116:119]\n" \
"v_mfma_f32_16x16x32_bf16 v[116:119], a[44:47], a[28:31], v[116:119]\n" \
"v_mfma_f32_16x16x32_bf16 v[112:115], a[32:35], a[24:27], v[112:115]\n" \
"s_waitcnt vmcnt(16) lgkmcnt(0)\n" \
"v_mfma_f32_16x16x32_bf16 v[112:115], a[36:39], a[28:31], v[112:115]\n" \
"s_barrier\n" \
"v_mfma_f32_16x16x32_bf16 v[72:75], a[48:51], a[0:3], v[72:75]\n" \
"s_lshl_b32 s83, s95, 1\n" \
"s_add_u32 s83, s83, s74\n" \
"ds_read_b64_tr_b16 a[32:33], v214 offset:16384\n" \
"v_mfma_f32_16x16x32_bf16 v[72:75], a[52:55], a[4:7], v[72:75]\n" \
"ds_read_b64_tr_b16 a[34:35], v214 offset:18432\n" \
"v_mfma_f32_16x16x32_bf16 v[76:79], a[56:59], a[0:3], v[76:79]\n" \
"ds_read_b64_tr_b16 a[36:37], v214 offset:24576\n" \
"v_mfma_f32_16x16x32_bf16 v[76:79], a[60:63], a[4:7], v[76:79]\n" \
"ds_read_b128 a[0:3], v213 offset:0\n" \
"v_mfma_f32_16x16x32_bf16 v[92:95], a[56:59], a[8:11], v[92:95]\n" \
"ds_read_b128 a[4:7], v213 offset:2048\n" \
"v_mfma_f32_16x16x32_bf16 v[92:95], a[60:63], a[12:15], v[92:95]\n" \
"ds_read_b64_tr_b16 a[38:39], v214 offset:26624\n" \
"v_mfma_f32_16x16x32_bf16 v[88:91], a[48:51], a[8:11], v[88:91]\n" \
"ds_read_b64_tr_b16 a[40:41], v215 offset:16384\n" \
"v_mfma_f32_16x16x32_bf16 v[88:91], a[52:55], a[12:15], v[88:91]\n" \
"ds_read_b128 a[8:11], v213 offset:256\n" \
"v_mfma_f32_16x16x32_bf16 v[104:107], a[48:51], a[16:19], v[104:107]\n" \
"ds_read_b128 a[12:15], v213 offset:2304\n" \
"v_mfma_f32_16x16x32_bf16 v[104:107], a[52:55], a[20:23], v[104:107]\n" \
"ds_read_b64_tr_b16 a[42:43], v215 offset:18432\n" \
"v_mfma_f32_16x16x32_bf16 v[108:111], a[56:59], a[16:19], v[108:111]\n" \
"ds_read_b64_tr_b16 a[44:45], v215 offset:24576\n" \
"v_mfma_f32_16x16x32_bf16 v[108:111], a[60:63], a[20:23], v[108:111]\n" \
"ds_read_b128 a[16:19], v213 offset:4096\n" \
"v_mfma_f32_16x16x32_bf16 v[124:127], a[56:59], a[24:27], v[124:127]\n" \
"ds_read_b128 a[20:23], v213 offset:6144\n" \
"v_mfma_f32_16x16x32_bf16 v[124:127], a[60:63], a[28:31], v[124:127]\n" \
"ds_read_b64_tr_b16 a[46:47], v215 offset:26624\n" \
"v_mfma_f32_16x16x32_bf16 v[120:123], a[48:51], a[24:27], v[120:123]\n" \
"v_mfma_f32_16x16x32_bf16 v[120:123], a[52:55], a[28:31], v[120:123]\n" \
"s_waitcnt lgkmcnt(3)\n" \
"v_mfma_f32_16x16x32_bf16 v[64:67], a[32:35], a[0:3], v[64:67]\n" \
"ds_read_b128 a[24:27], v213 offset:4352\n" \
"v_mfma_f32_16x16x32_bf16 v[64:67], a[36:39], a[4:7], v[64:67]\n" \
"s_waitcnt lgkmcnt(1)\n" \
"ds_read_b128 a[28:31], v213 offset:6400\n" \
"v_lshrrev_b32 v132, 4, v248\n" \
"v_mfma_f32_16x16x32_bf16 v[68:71], a[40:43], a[0:3], v[68:71]\n" \
"s_sub_u32 s74, s74, 32768\n" \
"s_cselect_b32 s74, 131072, s74\n" \
"ds_read_b64_tr_b16 a[48:49], v214 offset:16896\n" \
"v_mfma_f32_16x16x32_bf16 v[68:71], a[44:47], a[4:7], v[68:71]\n" \
"ds_read_b64_tr_b16 a[50:51], v214 offset:18944\n" \
"v_and_b32 v133, 15, v132\n" \
"v_mfma_f32_16x16x32_bf16 v[84:87], a[40:43], a[8:11], v[84:87]\n" \
"ds_read_b64_tr_b16 a[52:53], v214 offset:25088\n" \
"v_mul_u32_u24 v128, 144, v133\n" \
"v_mfma_f32_16x16x32_bf16 v[84:87], a[44:47], a[12:15], v[84:87]\n" \
"ds_read_b64_tr_b16 a[54:55], v214 offset:27136\n" \
"v_lshrrev_b32 v133, 4, v132\n" \
"v_mfma_f32_16x16x32_bf16 v[80:83], a[32:35], a[8:11], v[80:83]\n" \
"ds_read_b64_tr_b16 a[56:57], v215 offset:16896\n" \
"v_lshl_add_u32 v128, v133, 3, v128\n" \
"v_mfma_f32_16x16x32_bf16 v[80:83], a[36:39], a[12:15], v[80:83]\n" \
"ds_read_b64_tr_b16 a[58:59], v215 offset:18944\n" \
"v_add_u32 v128, s83, v128\n" \
"v_mfma_f32_16x16x32_bf16 v[96:99], a[32:35], a[16:19], v[96:99]\n" \
"ds_read_b64_tr_b16 a[60:61], v215 offset:25088\n" \
"v_lshrrev_b32 v133, 3, v132\n" \
"v_mfma_f32_16x16x32_bf16 v[96:99], a[36:39], a[20:23], v[96:99]\n" \
"ds_read_b64_tr_b16 a[62:63], v215 offset:27136\n" \
"v_and_b32 v134, 7, v132\n" \
"Lpg13150_%=:\n" \
"s_cbranch_execz Lcb1_%=\n" \
"v_mfma_f32_16x16x32_bf16 v[100:103], a[40:43], a[16:19], v[100:103]\n" \
"v_add_u32 v213, s74, v211\n" \
"v_add_u32 v214, s74, v212\n" \
"v_mfma_f32_16x16x32_bf16 v[100:103], a[44:47], a[20:23], v[100:103]\n" \
"s_waitcnt lgkmcnt(3)\n" \
"v_xor_b32 v215, 32, v214\n" \
"v_mul_u32_u24 v129, 144, v133\n" \
"v_mfma_f32_16x16x32_bf16 v[116:119], a[40:43], a[24:27], v[116:119]\n" \
"v_lshl_add_u32 v129, v134, 4, v129\n" \
"v_add_u32 v129, s83, v129\n" \
"v_mfma_f32_16x16x32_bf16 v[116:119], a[44:47], a[28:31], v[116:119]\n" \
"v_lshlrev_b32 v134, 4, v134\n" \
"v_lshl_add_u32 v130, v133, 13, v134\n" \
"v_mfma_f32_16x16x32_bf16 v[112:115], a[32:35], a[24:27], v[112:115]\n" \
"s_waitcnt vmcnt(0) lgkmcnt(0)\n" \
"v_mfma_f32_16x16x32_bf16 v[112:115], a[36:39], a[28:31], v[112:115]\n" \
"s_barrier\n" \
"v_mfma_f32_16x16x32_bf16 v[72:75], a[48:51], a[0:3], v[72:75]\n" \
"ds_read_b128 a[192:195], v213 offset:0\n" \
"ds_read_b64_tr_b16 a[128:129], v214 offset:16384\n" \
"v_mfma_f32_16x16x32_bf16 v[72:75], a[52:55], a[4:7], v[72:75]\n" \
"s_sub_u32 s74, s74, 32768\n" \
"s_cselect_b32 s74, 131072, s74\n" \
"ds_read_b64_tr_b16 a[130:131], v214 offset:18432\n" \
"v_mfma_f32_16x16x32_bf16 v[76:79], a[56:59], a[0:3], v[76:79]\n" \
"ds_read_b128 a[196:199], v213 offset:2048\n" \
"ds_read_b64_tr_b16 a[132:133], v214 offset:24576\n" \
"v_mfma_f32_16x16x32_bf16 v[76:79], a[60:63], a[4:7], v[76:79]\n" \
"ds_read_b64_tr_b16 a[134:135], v214 offset:26624\n" \
"v_add_u32 v226, s74, v211\n" \
"v_mfma_f32_16x16x32_bf16 v[92:95], a[56:59], a[8:11], v[92:95]\n" \
"ds_read_b128 a[200:203], v213 offset:256\n" \
"ds_read_b64_tr_b16 a[136:137], v215 offset:16384\n" \
"v_mfma_f32_16x16x32_bf16 v[92:95], a[60:63], a[12:15], v[92:95]\n" \
"ds_read_b64_tr_b16 a[138:139], v215 offset:18432\n" \
"v_add_u32 v227, s74, v212\n" \
"v_mfma_f32_16x16x32_bf16 v[88:91], a[48:51], a[8:11], v[88:91]\n" \
"ds_read_b128 a[204:207], v213 offset:2304\n" \
"ds_read_b64_tr_b16 a[140:141], v215 offset:24576\n" \
"v_mfma_f32_16x16x32_bf16 v[88:91], a[52:55], a[12:15], v[88:91]\n" \
"ds_read_b64_tr_b16 a[142:143], v215 offset:26624\n" \
"v_xor_b32 v228, 32, v227\n" \
"v_mfma_f32_16x16x32_bf16 v[104:107], a[48:51], a[16:19], v[104:107]\n" \
"ds_read_b64_tr_b16 a[144:145], v214 offset:16896\n" \
"v_mfma_f32_16x16x32_bf16 v[104:107], a[52:55], a[20:23], v[104:107]\n" \
"ds_read_b64_tr_b16 a[146:147], v214 offset:18944\n" \
"v_mfma_f32_16x16x32_bf16 v[108:111], a[56:59], a[16:19], v[108:111]\n" \
"ds_read_b64_tr_b16 a[148:149], v214 offset:25088\n" \
"v_mfma_f32_16x16x32_bf16 v[108:111], a[60:63], a[20:23], v[108:111]\n" \
"ds_read_b64_tr_b16 a[150:151], v214 offset:27136\n" \
"v_mfma_f32_16x16x32_bf16 v[124:127], a[56:59], a[24:27], v[124:127]\n" \
"ds_read_b64_tr_b16 a[152:153], v215 offset:16896\n" \
"v_mfma_f32_16x16x32_bf16 v[124:127], a[60:63], a[28:31], v[124:127]\n" \
"ds_read_b64_tr_b16 a[154:155], v215 offset:18944\n" \
"v_mfma_f32_16x16x32_bf16 v[120:123], a[48:51], a[24:27], v[120:123]\n" \
"ds_read_b64_tr_b16 a[156:157], v215 offset:25088\n" \
"v_mfma_f32_16x16x32_bf16 v[120:123], a[52:55], a[28:31], v[120:123]\n" \
"s_waitcnt lgkmcnt(4)\n" \
"ds_read_b64_tr_b16 a[158:159], v215 offset:27136\n" \
"v_mfma_f32_16x16x32_bf16 v[64:67], a[128:131], a[192:195], v[64:67]\n" \
"ds_read_b128 a[224:227], v226 offset:0\n" \
"ds_read_b64_tr_b16 a[160:161], v227 offset:16384\n" \
"v_mfma_f32_16x16x32_bf16 v[64:67], a[132:135], a[196:199], v[64:67]\n" \
"ds_read_b64_tr_b16 a[162:163], v227 offset:18432\n" \
"v_mfma_f32_16x16x32_bf16 v[80:83], a[128:131], a[200:203], v[80:83]\n" \
"ds_read_b64_tr_b16 a[164:165], v227 offset:24576\n" \
"v_mfma_f32_16x16x32_bf16 v[80:83], a[132:135], a[204:207], v[80:83]\n" \
"ds_read_b128 a[228:231], v226 offset:2048\n" \
"ds_read_b64_tr_b16 a[166:167], v227 offset:26624\n" \
"v_mfma_f32_16x16x32_bf16 v[84:87], a[136:139], a[200:203], v[84:87]\n" \
"ds_read_b64_tr_b16 a[168:169], v228 offset:16384\n" \
"v_mfma_f32_16x16x32_bf16 v[84:87], a[140:143], a[204:207], v[84:87]\n" \
"ds_read_b64_tr_b16 a[170:171], v228 offset:18432\n" \
"v_mfma_f32_16x16x32_bf16 v[68:71], a[136:139], a[192:195], v[68:71]\n" \
"ds_read_b128 a[232:235], v226 offset:256\n" \
"ds_read_b64_tr_b16 a[172:173], v228 offset:24576\n" \
"v_mfma_f32_16x16x32_bf16 v[68:71], a[140:143], a[196:199], v[68:71]\n" \
"s_waitcnt lgkmcnt(6)\n" \
"ds_read_b64_tr_b16 a[174:175], v228 offset:26624\n" \
"v_mfma_f32_16x16x32_bf16 v[72:75], a[144:147], a[192:195], v[72:75]\n" \
"ds_read_b64_tr_b16 a[176:177], v227 offset:16896\n" \
"v_mfma_f32_16x16x32_bf16 v[72:75], a[148:151], a[196:199], v[72:75]\n" \
"ds_read_b128 a[236:239], v226 offset:2304\n" \
"ds_read_b64_tr_b16 a[178:179], v227 offset:18944\n" \
"v_mfma_f32_16x16x32_bf16 v[88:91], a[144:147], a[200:203], v[88:91]\n" \
"ds_read_b64_tr_b16 a[180:181], v227 offset:25088\n" \
"v_mfma_f32_16x16x32_bf16 v[88:91], a[148:151], a[204:207], v[88:91]\n" \
"ds_read_b64_tr_b16 a[182:183], v227 offset:27136\n" \
"v_mfma_f32_16x16x32_bf16 v[92:95], a[152:155], a[200:203], v[92:95]\n" \
"ds_read_b64_tr_b16 a[184:185], v228 offset:16896\n" \
"v_mfma_f32_16x16x32_bf16 v[92:95], a[156:159], a[204:207], v[92:95]\n" \
"ds_read_b64_tr_b16 a[186:187], v228 offset:18944\n" \
"v_mfma_f32_16x16x32_bf16 v[76:79], a[152:155], a[192:195], v[76:79]\n" \
"ds_read_b64_tr_b16 a[188:189], v228 offset:25088\n" \
"v_mfma_f32_16x16x32_bf16 v[76:79], a[156:159], a[196:199], v[76:79]\n" \
"s_waitcnt lgkmcnt(4)\n" \
"ds_read_b64_tr_b16 a[190:191], v228 offset:27136\n" \
"v_mfma_f32_16x16x32_bf16 v[64:67], a[160:163], a[224:227], v[64:67]\n" \
"ds_read_b128 a[208:211], v213 offset:4096\n" \
"v_mfma_f32_16x16x32_bf16 v[64:67], a[164:167], a[228:231], v[64:67]\n" \
"ds_read_b128 a[212:215], v213 offset:6144\n" \
"v_mfma_f32_16x16x32_bf16 v[80:83], a[160:163], a[232:235], v[80:83]\n" \
"ds_read_b128 a[240:243], v226 offset:4096\n" \
"v_mfma_f32_16x16x32_bf16 v[80:83], a[164:167], a[236:239], v[80:83]\n" \
"ds_read_b128 a[244:247], v226 offset:6144\n" \
"v_mfma_f32_16x16x32_bf16 v[84:87], a[168:171], a[232:235], v[84:87]\n" \
"ds_read_b128 a[216:219], v213 offset:4352\n" \
"v_cvt_pk_bf16_f32 v64, v64, v65\n" \
"v_mfma_f32_16x16x32_bf16 v[84:87], a[172:175], a[236:239], v[84:87]\n" \
"ds_read_b128 a[220:223], v213 offset:6400\n" \
"v_cvt_pk_bf16_f32 v65, v66, v67\n" \
"v_mfma_f32_16x16x32_bf16 v[68:71], a[168:171], a[224:227], v[68:71]\n" \
"ds_read_b128 a[248:251], v226 offset:4352\n" \
"v_mfma_f32_16x16x32_bf16 v[68:71], a[172:175], a[228:231], v[68:71]\n" \
"s_waitcnt lgkmcnt(4)\n" \
"ds_read_b128 a[252:255], v226 offset:6400\n" \
"v_mfma_f32_16x16x32_bf16 v[72:75], a[176:179], a[224:227], v[72:75]\n" \
"ds_write_b64 v128, v[64:65] offset:0\n" \
"v_mfma_f32_16x16x32_bf16 v[72:75], a[180:183], a[228:231], v[72:75]\n" \
"v_cvt_pk_bf16_f32 v80, v80, v81\n" \
"v_cvt_pk_bf16_f32 v81, v82, v83\n" \
"v_mfma_f32_16x16x32_bf16 v[88:91], a[176:179], a[232:235], v[88:91]\n" \
"ds_write_b64 v128, v[80:81] offset:2304\n" \
"v_mfma_f32_16x16x32_bf16 v[88:91], a[180:183], a[236:239], v[88:91]\n" \
"v_cvt_pk_bf16_f32 v84, v84, v85\n" \
"v_cvt_pk_bf16_f32 v85, v86, v87\n" \
"v_mfma_f32_16x16x32_bf16 v[92:95], a[184:187], a[232:235], v[92:95]\n" \
"ds_write_b64 v128, v[84:85] offset:2336\n" \
"v_mfma_f32_16x16x32_bf16 v[92:95], a[188:191], a[236:239], v[92:95]\n" \
"v_cvt_pk_bf16_f32 v68, v68, v69\n" \
"v_cvt_pk_bf16_f32 v69, v70, v71\n" \
"v_mfma_f32_16x16x32_bf16 v[76:79], a[184:187], a[224:227], v[76:79]\n" \
"ds_write_b64 v128, v[68:69] offset:32\n" \
"v_mfma_f32_16x16x32_bf16 v[76:79], a[188:191], a[228:231], v[76:79]\n" \
"v_cvt_pk_bf16_f32 v72, v72, v73\n" \
"v_cvt_pk_bf16_f32 v73, v74, v75\n" \
"v_mfma_f32_16x16x32_bf16 v[96:99], a[128:131], a[208:211], v[96:99]\n" \
"ds_write_b64 v128, v[72:73] offset:64\n" \
"v_mfma_f32_16x16x32_bf16 v[96:99], a[132:135], a[212:215], v[96:99]\n" \
"s_waitcnt lgkmcnt(2)\n" \
"v_cvt_pk_bf16_f32 v88, v88, v89\n" \
"v_cvt_pk_bf16_f32 v89, v90, v91\n" \
"v_mfma_f32_16x16x32_bf16 v[96:99], a[160:163], a[240:243], v[96:99]\n" \
"ds_write_b64 v128, v[88:89] offset:2368\n" \
"v_mfma_f32_16x16x32_bf16 v[96:99], a[164:167], a[244:247], v[96:99]\n" \
"v_cvt_pk_bf16_f32 v92, v92, v93\n" \
"v_cvt_pk_bf16_f32 v93, v94, v95\n" \
"v_mfma_f32_16x16x32_bf16 v[100:103], a[168:171], a[240:243], v[100:103]\n" \
"ds_write_b64 v128, v[92:93] offset:2400\n" \
"v_mfma_f32_16x16x32_bf16 v[100:103], a[172:175], a[244:247], v[100:103]\n" \
"v_cvt_pk_bf16_f32 v76, v76, v77\n" \
"v_cvt_pk_bf16_f32 v77, v78, v79\n" \
"v_mfma_f32_16x16x32_bf16 v[100:103], a[136:139], a[208:211], v[100:103]\n" \
"ds_write_b64 v128, v[76:77] offset:96\n" \
"v_mfma_f32_16x16x32_bf16 v[100:103], a[140:143], a[212:215], v[100:103]\n" \
"ds_read_b128 v[140:143], v129 offset:0\n" \
"v_mfma_f32_16x16x32_bf16 v[104:107], a[144:147], a[208:211], v[104:107]\n" \
"ds_read_b128 v[144:147], v129 offset:1152\n" \
"v_mfma_f32_16x16x32_bf16 v[104:107], a[148:151], a[212:215], v[104:107]\n" \
"ds_read_b128 v[148:151], v129 offset:2304\n" \
"v_mfma_f32_16x16x32_bf16 v[104:107], a[176:179], a[240:243], v[104:107]\n" \
"ds_read_b128 v[152:155], v129 offset:3456\n" \
"v_mfma_f32_16x16x32_bf16 v[104:107], a[180:183], a[244:247], v[104:107]\n" \
"v_cvt_pk_bf16_f32 v96, v96, v97\n" \
"v_cvt_pk_bf16_f32 v97, v98, v99\n" \
"v_mfma_f32_16x16x32_bf16 v[108:111], a[184:187], a[240:243], v[108:111]\n" \
"ds_write_b64 v128, v[96:97] offset:0\n" \
"v_mfma_f32_16x16x32_bf16 v[108:111], a[188:191], a[244:247], v[108:111]\n" \
"v_cvt_pk_bf16_f32 v100, v100, v101\n" \
"v_cvt_pk_bf16_f32 v101, v102, v103\n" \
"v_mfma_f32_16x16x32_bf16 v[108:111], a[152:155], a[208:211], v[108:111]\n" \
"s_add_u32 s70, s77, 0\n" \
"ds_write_b64 v128, v[100:101] offset:32\n" \
"v_mfma_f32_16x16x32_bf16 v[108:111], a[156:159], a[212:215], v[108:111]\n" \
"s_waitcnt lgkmcnt(5)\n" \
"v_cvt_pk_bf16_f32 v104, v104, v105\n" \
"v_cvt_pk_bf16_f32 v105, v106, v107\n" \
"v_mfma_f32_16x16x32_bf16 v[112:115], a[128:131], a[216:219], v[112:115]\n" \
"buffer_store_dwordx4 v[140:143], v130, s[52:55], s70 offen sc1\n" \
"ds_write_b64 v128, v[104:105] offset:64\n" \
"v_mfma_f32_16x16x32_bf16 v[112:115], a[132:135], a[220:223], v[112:115]\n" \
"v_mfma_f32_16x16x32_bf16 v[112:115], a[160:163], a[248:251], v[112:115]\n" \
"v_cvt_pk_bf16_f32 v108, v108, v109\n" \
"v_cvt_pk_bf16_f32 v109, v110, v111\n" \
"v_mfma_f32_16x16x32_bf16 v[112:115], a[164:167], a[252:255], v[112:115]\n" \
"ds_write_b64 v128, v[108:109] offset:96\n" \
"v_mfma_f32_16x16x32_bf16 v[116:119], a[168:171], a[248:251], v[116:119]\n" \
"s_add_u32 s70, s77, 65536\n" \
"ds_read_b128 v[156:159], v129 offset:0\n" \
"s_waitcnt lgkmcnt(7)\n" \
"v_mfma_f32_16x16x32_bf16 v[116:119], a[172:175], a[252:255], v[116:119]\n" \
"buffer_store_dwordx4 v[144:147], v130, s[52:55], s70 offen sc1\n" \
"ds_read_b128 v[160:163], v129 offset:1152\n" \
"v_mfma_f32_16x16x32_bf16 v[116:119], a[136:139], a[216:219], v[116:119]\n" \
"v_cvt_pk_bf16_f32 v112, v112, v113\n" \
"v_cvt_pk_bf16_f32 v113, v114, v115\n" \
"v_mfma_f32_16x16x32_bf16 v[116:119], a[140:143], a[220:223], v[116:119]\n" \
"ds_write_b64 v128, v[112:113] offset:2304\n" \
"v_mfma_f32_16x16x32_bf16 v[120:123], a[144:147], a[216:219], v[120:123]\n" \
"v_mfma_f32_16x16x32_bf16 v[120:123], a[148:151], a[220:223], v[120:123]\n" \
"s_add_u32 s70, s77, 4096\n" \
"s_waitcnt lgkmcnt(8)\n" \
"v_mfma_f32_16x16x32_bf16 v[120:123], a[176:179], a[248:251], v[120:123]\n" \
"buffer_store_dwordx4 v[148:151], v130, s[52:55], s70 offen sc1\n" \
"v_cvt_pk_bf16_f32 v116, v116, v117\n" \
"v_cvt_pk_bf16_f32 v117, v118, v119\n" \
"v_mfma_f32_16x16x32_bf16 v[120:123], a[180:183], a[252:255], v[120:123]\n" \
"ds_write_b64 v128, v[116:117] offset:2336\n" \
"v_mfma_f32_16x16x32_bf16 v[124:127], a[184:187], a[248:251], v[124:127]\n" \
"v_mfma_f32_16x16x32_bf16 v[124:127], a[188:191], a[252:255], v[124:127]\n" \
"v_mfma_f32_16x16x32_bf16 v[124:127], a[152:155], a[216:219], v[124:127]\n" \
"s_add_u32 s70, s77, 69632\n" \
"v_cvt_pk_bf16_f32 v120, v120, v121\n" \
"v_cvt_pk_bf16_f32 v121, v122, v123\n" \
"v_mfma_f32_16x16x32_bf16 v[124:127], a[156:159], a[220:223], v[124:127]\n" \
"s_waitcnt lgkmcnt(8)\n" \
"ds_write_b64 v128, v[120:121] offset:2368\n" \
"buffer_store_dwordx4 v[152:155], v130, s[52:55], s70 offen sc1\n" \
"s_add_u32 s70, s77, 131072\n" \
"s_waitcnt lgkmcnt(4)\n" \
"s_nop 2\n" \
"v_cvt_pk_bf16_f32 v124, v124, v125\n" \
"v_cvt_pk_bf16_f32 v125, v126, v127\n" \
"ds_write_b64 v128, v[124:125] offset:2400\n" \
"ds_read_b128 v[164:167], v129 offset:2304\n" \
"ds_read_b128 v[168:171], v129 offset:3456\n" \
"buffer_store_dwordx4 v[156:159], v130, s[52:55], s70 offen sc1\n" \
"s_add_u32 s70, s77, 196608\n" \
"s_waitcnt lgkmcnt(6)\n" \
"buffer_store_dwordx4 v[160:163], v130, s[52:55], s70 offen sc1\n" \
"s_add_u32 s70, s77, 135168\n" \
"s_waitcnt lgkmcnt(1)\n" \
"buffer_store_dwordx4 v[164:167], v130, s[52:55], s70 offen sc1\n" \
"s_add_u32 s70, s77, 200704\n" \
"s_waitcnt lgkmcnt(0)\n" \
"buffer_store_dwordx4 v[168:171], v130, s[52:55], s70 offen sc1\n" \
"s_endpgm\n" \
"Lpg13388_%=:\n" \
"s_cbranch_execz Lcb1_%=\n" \
"Lo64_%=:\n" \
"v_mfma_f32_16x16x32_bf16 v[64:67], a[32:35], a[0:3], v[64:67]\n" \
"ds_read_b128 a[24:27], v213 offset:4352\n" \
"s_add_u32 m0, s75, s95\n" \
"v_mfma_f32_16x16x32_bf16 v[64:67], a[36:39], a[4:7], v[64:67]\n" \
"s_waitcnt lgkmcnt(1)\n" \
"ds_read_b128 a[28:31], v213 offset:6400\n" \
"global_load_lds_dwordx4 v222, s[36:37] offset:0 sc1\n" \
"v_mfma_f32_16x16x32_bf16 v[68:71], a[40:43], a[0:3], v[68:71]\n" \
"s_sub_u32 s74, s74, 32768\n" \
"s_cselect_b32 s74, 131072, s74\n" \
"ds_read_b64_tr_b16 a[48:49], v214 offset:16896\n" \
"v_mfma_f32_16x16x32_bf16 v[68:71], a[44:47], a[4:7], v[68:71]\n" \
"ds_read_b64_tr_b16 a[50:51], v214 offset:18944\n" \
"v_mfma_f32_16x16x32_bf16 v[84:87], a[40:43], a[8:11], v[84:87]\n" \
"ds_read_b64_tr_b16 a[52:53], v214 offset:25088\n" \
"v_mfma_f32_16x16x32_bf16 v[84:87], a[44:47], a[12:15], v[84:87]\n" \
"ds_read_b64_tr_b16 a[54:55], v214 offset:27136\n" \
"global_load_lds_dwordx4 v222, s[36:37] offset:1024 sc1\n" \
"v_mfma_f32_16x16x32_bf16 v[80:83], a[32:35], a[8:11], v[80:83]\n" \
"ds_read_b64_tr_b16 a[56:57], v215 offset:16896\n" \
"v_mfma_f32_16x16x32_bf16 v[80:83], a[36:39], a[12:15], v[80:83]\n" \
"ds_read_b64_tr_b16 a[58:59], v215 offset:18944\n" \
"v_mfma_f32_16x16x32_bf16 v[96:99], a[32:35], a[16:19], v[96:99]\n" \
"ds_read_b64_tr_b16 a[60:61], v215 offset:25088\n" \
"v_mfma_f32_16x16x32_bf16 v[96:99], a[36:39], a[20:23], v[96:99]\n" \
"ds_read_b64_tr_b16 a[62:63], v215 offset:27136\n" \
"global_load_lds_dwordx4 v222, s[36:37] offset:2048 sc1\n" \
"v_mfma_f32_16x16x32_bf16 v[100:103], a[40:43], a[16:19], v[100:103]\n" \
"v_add_u32 v213, s74, v211\n" \
"v_add_u32 v214, s74, v212\n" \
"v_mfma_f32_16x16x32_bf16 v[100:103], a[44:47], a[20:23], v[100:103]\n" \
"s_waitcnt lgkmcnt(3)\n" \
"v_xor_b32 v215, 32, v214\n" \
"v_mfma_f32_16x16x32_bf16 v[116:119], a[40:43], a[24:27], v[116:119]\n" \
"v_mfma_f32_16x16x32_bf16 v[116:119], a[44:47], a[28:31], v[116:119]\n" \
"global_load_lds_dwordx4 v222, s[36:37] offset:3072 sc1\n" \
"v_mfma_f32_16x16x32_bf16 v[112:115], a[32:35], a[24:27], v[112:115]\n" \
"s_waitcnt vmcnt(20) lgkmcnt(0)\n" \
"s_add_u32 s36, s36, 0x1000\n" \
"v_mfma_f32_16x16x32_bf16 v[112:115], a[36:39], a[28:31], v[112:115]\n" \
"s_barrier\n" \
"v_mfma_f32_16x16x32_bf16 v[72:75], a[48:51], a[0:3], v[72:75]\n" \
"s_add_u32 m0, m0, 0x5000\n" \
"ds_read_b64_tr_b16 a[32:33], v214 offset:16384\n" \
"v_mfma_f32_16x16x32_bf16 v[72:75], a[52:55], a[4:7], v[72:75]\n" \
"ds_read_b64_tr_b16 a[34:35], v214 offset:18432\n" \
"global_load_lds_dwordx4 v216, s[38:39] offset:-4096\n" \
"v_mfma_f32_16x16x32_bf16 v[76:79], a[56:59], a[0:3], v[76:79]\n" \
"ds_read_b64_tr_b16 a[36:37], v214 offset:24576\n" \
"v_mfma_f32_16x16x32_bf16 v[76:79], a[60:63], a[4:7], v[76:79]\n" \
"ds_read_b128 a[0:3], v213 offset:0\n" \
"v_mfma_f32_16x16x32_bf16 v[92:95], a[56:59], a[8:11], v[92:95]\n" \
"ds_read_b128 a[4:7], v213 offset:2048\n" \
"v_mfma_f32_16x16x32_bf16 v[92:95], a[60:63], a[12:15], v[92:95]\n" \
"ds_read_b64_tr_b16 a[38:39], v214 offset:26624\n" \
"global_load_lds_dwordx4 v217, s[38:39] offset:-3072\n" \
"v_mfma_f32_16x16x32_bf16 v[88:91], a[48:51], a[8:11], v[88:91]\n" \
"ds_read_b64_tr_b16 a[40:41], v215 offset:16384\n" \
"v_mfma_f32_16x16x32_bf16 v[88:91], a[52:55], a[12:15], v[88:91]\n" \
"ds_read_b128 a[8:11], v213 offset:256\n" \
"v_mfma_f32_16x16x32_bf16 v[104:107], a[48:51], a[16:19], v[104:107]\n" \
"ds_read_b128 a[12:15], v213 offset:2304\n" \
"v_mfma_f32_16x16x32_bf16 v[104:107], a[52:55], a[20:23], v[104:107]\n" \
"ds_read_b64_tr_b16 a[42:43], v215 offset:18432\n" \
"global_load_lds_dwordx4 v218, s[38:39] offset:-2048\n" \
"v_mfma_f32_16x16x32_bf16 v[108:111], a[56:59], a[16:19], v[108:111]\n" \
"ds_read_b64_tr_b16 a[44:45], v215 offset:24576\n" \
"v_mfma_f32_16x16x32_bf16 v[108:111], a[60:63], a[20:23], v[108:111]\n" \
"ds_read_b128 a[16:19], v213 offset:4096\n" \
"global_load_lds_dwordx4 v219, s[38:39] offset:-1024\n" \
"v_mfma_f32_16x16x32_bf16 v[124:127], a[56:59], a[24:27], v[124:127]\n" \
"s_add_u32 s38, s38, 0x20000\n" \
"s_addc_u32 s39, s39, 0\n" \
"ds_read_b128 a[20:23], v213 offset:6144\n" \
"v_mfma_f32_16x16x32_bf16 v[124:127], a[60:63], a[28:31], v[124:127]\n" \
"s_sub_u32 s75, s75, 32768\n" \
"s_cselect_b32 s75, 131072, s75\n" \
"ds_read_b64_tr_b16 a[46:47], v215 offset:26624\n" \
"v_mfma_f32_16x16x32_bf16 v[120:123], a[48:51], a[24:27], v[120:123]\n" \
"s_sub_u32 s72, s72, 1\n" \
"s_cmp_lg_u32 s72, 0\n" \
"v_mfma_f32_16x16x32_bf16 v[120:123], a[52:55], a[28:31], v[120:123]\n" \
"s_waitcnt lgkmcnt(3)\n" \
"s_cbranch_scc1 Lo64_%=\n" \
"s_setpc_b64 s[68:69]\n" \
"Lhot_end_%=:\n" \
"Lmask0_%=:\n" \
"Lpg13477_%=:\n" \
"s_cbranch_execz Lcb0_%=\n" \
"s_waitcnt lgkmcnt(0)\n" \
"v_subrev_u32 v0, 0, v251\n" \
"v_subrev_u32 v1, 1, v251\n" \
"v_subrev_u32 v2, 2, v251\n" \
"v_subrev_u32 v3, 3, v251\n" \
"v_subrev_u32 v4, 16, v251\n" \
"v_subrev_u32 v5, 17, v251\n" \
"v_subrev_u32 v6, 18, v251\n" \
"v_subrev_u32 v7, 19, v251\n" \
"v_subrev_u32 v32, 32, v251\n" \
"v_subrev_u32 v33, 33, v251\n" \
"v_subrev_u32 v34, 34, v251\n" \
"v_subrev_u32 v35, 35, v251\n" \
"v_subrev_u32 v36, 48, v251\n" \
"v_subrev_u32 v37, 49, v251\n" \
"v_subrev_u32 v38, 50, v251\n" \
"v_subrev_u32 v39, 51, v251\n" \
"v_ashrrev_i32 v0, 31, v0\n" \
"v_ashrrev_i32 v1, 31, v1\n" \
"v_ashrrev_i32 v2, 31, v2\n" \
"v_ashrrev_i32 v3, 31, v3\n" \
"v_ashrrev_i32 v4, 31, v4\n" \
"v_ashrrev_i32 v5, 31, v5\n" \
"v_ashrrev_i32 v6, 31, v6\n" \
"v_ashrrev_i32 v7, 31, v7\n" \
"v_ashrrev_i32 v32, 31, v32\n" \
"v_ashrrev_i32 v33, 31, v33\n" \
"v_ashrrev_i32 v34, 31, v34\n" \
"v_ashrrev_i32 v35, 31, v35\n" \
"v_ashrrev_i32 v36, 31, v36\n" \
"v_ashrrev_i32 v37, 31, v37\n" \
"v_ashrrev_i32 v38, 31, v38\n" \
"v_ashrrev_i32 v39, 31, v39\n" \
"v_and_b32 v0, 0xff800000, v0\n" \
"v_and_b32 v1, 0xff800000, v1\n" \
"v_and_b32 v2, 0xff800000, v2\n" \
"v_and_b32 v3, 0xff800000, v3\n" \
"v_and_b32 v4, 0xff800000, v4\n" \
"v_and_b32 v5, 0xff800000, v5\n" \
"v_and_b32 v6, 0xff800000, v6\n" \
"v_and_b32 v7, 0xff800000, v7\n" \
"v_and_b32 v32, 0xff800000, v32\n" \
"v_and_b32 v33, 0xff800000, v33\n" \
"v_and_b32 v34, 0xff800000, v34\n" \
"v_and_b32 v35, 0xff800000, v35\n" \
"v_and_b32 v36, 0xff800000, v36\n" \
"v_and_b32 v37, 0xff800000, v37\n" \
"v_and_b32 v38, 0xff800000, v38\n" \
"v_and_b32 v39, 0xff800000, v39\n" \
"s_branch Lmask0r_%=\n" \
"Lpl1_%=:\n" \
"s_mov_b32 s31, 0\n" \
"Lpk1_%=:\n" \
"s_sleep 2\n" \
"buffer_load_dword v244, v245, s[56:59], s91 offen sc1\n" \
"s_waitcnt vmcnt(0)\n" \
"v_cmp_ne_u32 vcc, s60, v244\n" \
"s_nop 1\n" \
"s_and_b32 vcc_hi, vcc_lo, 255\n" \
"s_cbranch_scc0 Lpr1_%=\n" \
"s_add_u32 s31, s31, 1\n" \
"s_cmp_lt_u32 s31, 0x40000\n" \
"s_cbranch_scc1 Lpk1_%=\n" \
"s_branch Lpr1_%=\n" \
"Lrs0_%=:\n" \
"s_call_b64 s[68:69], Lrest_%=\n" \
"s_branch Lrr0_%=\n" \
"Lrest_%=:\n" \
"s_mov_b32 s31, 0\n" \
"s_waitcnt vmcnt(0)\n" \
"Lrk_%=:\n" \
"v_lshrrev_b32 v253, 2, v248\n" \
"s_sleep 2\n" \
"buffer_load_dword v253, v253, s[56:59], s79 offen sc1\n" \
"s_waitcnt vmcnt(0)\n" \
"v_cmp_ne_u32 s[52:53], s60, v253\n" \
"s_add_u32 s31, s31, 1\n" \
"s_cmp_lt_u32 s31, 0x40000\n" \
"s_cselect_b32 s54, 0xff, 0\n" \
"s_and_b32 s52, s52, s54\n" \
"s_cmp_lg_u32 s52, 0\n" \
"s_cbranch_scc1 Lrk_%=\n" \
"s_mov_b64 s[10:11], 0\n" \
"s_setpc_b64 s[68:69]\n" \
"Lrs1_%=:\n" \
"s_call_b64 s[68:69], Lrest_%=\n" \
"s_branch Lrr1_%=\n" \
"Lrs2_%=:\n" \
"s_call_b64 s[68:69], Lrest_%=\n" \
"s_branch Lrr2_%=\n" \
"Lrs3_%=:\n" \
"s_call_b64 s[68:69], Lrest_%=\n" \
"s_branch Lrr3_%=\n" \
"LF7_%=:\n" \
"v_mfma_f32_16x16x32_bf16 v[24:27], a[192:195], a[176:179], v[236:239]\n" \
"s_waitcnt vmcnt(13)\n" \
"ds_read_b128 v[192:195], v246 offset:16384\n" \
"s_bfe_u32 s88, %1, 0x20001\n" \
"v_mfma_f32_16x16x32_bf16 v[24:27], a[196:199], a[180:183], v[24:27]\n" \
"s_lshl_b32 s88, s88, 2\n" \
"ds_read_b128 v[196:199], v246 offset:18432\n" \
"v_cvt_pk_bf16_f32 v91, v94, v95\n" \
"v_mfma_f32_16x16x32_bf16 v[24:27], a[200:203], a[184:187], v[24:27]\n" \
"s_lshr_b32 s70, %1, 6\n" \
"ds_read_b128 v[200:203], v246 offset:20480\n" \
"v_mov_b32 v66, v242\n" \
"v_mov_b32 v64, v66\n" \
"v_exp_f32 v104, v104\n" \
"v_exp_f32 v105, v105\n" \
"v_exp_f32 v106, v106\n" \
"v_mfma_f32_16x16x32_bf16 v[24:27], a[204:207], a[188:191], v[24:27]\n" \
"s_add_u32 s88, s88, s70\n" \
"ds_read_b128 a[252:255], v246 offset:14592\n" \
"v_bfe_u32 v69, v248, 8, 1\n" \
"v_bfe_u32 v70, v248, 6, 2\n" \
"v_mfma_f32_16x16x32_bf16 v[16:19], a[200:203], a[168:171], v[232:235]\n" \
"s_lshr_b32 s71, s22, 7\n" \
"ds_read_b128 v[204:207], v246 offset:22528\n" \
"v_cvt_pk_bf16_f32 v104, v104, v105\n" \
"v_mfma_f32_16x16x32_bf16 v[16:19], a[204:207], a[172:175], v[16:19]\n" \
"s_sub_u32 s71, s63, s71\n" \
"ds_read_b128 v[208:211], v246 offset:16640\n" \
"v_permlane16_swap_b32 v64, v66\n" \
"v_add_f32 v66, v66, v64\n" \
"v_mov_b32 v64, v66\n" \
"v_exp_f32 v107, v107\n" \
"v_exp_f32 v108, v108\n" \
"v_mfma_f32_16x16x32_bf16 v[16:19], a[192:195], a[160:163], v[16:19]\n" \
"s_add_u32 s71, s71, 6\n" \
"ds_read_b128 v[212:215], v246 offset:18688\n" \
"v_cvt_pk_bf16_f32 v105, v106, v107\n" \
"v_mfma_f32_16x16x32_bf16 v[16:19], a[196:199], a[164:167], v[16:19]\n" \
"s_and_b32 s71, s71, 7\n" \
"ds_read_b128 v[216:219], v246 offset:20736\n" \
"v_lshl_add_u32 v70, v69, 3, v70\n" \
"v_mul_u32_u24 v70, 6144, v70\n" \
"v_permlane32_swap_b32 v64, v66\n" \
"v_add_f32 v66, v66, v64\n" \
"v_rcp_f32 v64, v66\n" \
"v_mul_f32 v66, 0x4d800000, v66\n" \
"v_add_u32 v65, 0x1c90080, v249\n" \
"v_cndmask_b32 v65, v249, v65, s[16:17]\n" \
"v_accvgpr_read_b32 v96, a0\n" \
"v_accvgpr_read_b32 v97, a1\n" \
"v_accvgpr_read_b32 v98, a2\n" \
"v_accvgpr_read_b32 v99, a3\n" \
"v_accvgpr_read_b32 v100, a4\n" \
"v_accvgpr_read_b32 v101, a5\n" \
"v_accvgpr_read_b32 v102, a6\n" \
"v_accvgpr_read_b32 v103, a7\n" \
"v_exp_f32 v109, v109\n" \
"v_exp_f32 v110, v110\n" \
"v_pk_mul_f32 v[96:97], v[96:97], v[64:65] op_sel_hi:[1,0]\n" \
"v_pk_mul_f32 v[98:99], v[98:99], v[64:65] op_sel_hi:[1,0]\n" \
"v_pk_mul_f32 v[100:101], v[100:101], v[64:65] op_sel_hi:[1,0]\n" \
"v_pk_mul_f32 v[102:103], v[102:103], v[64:65] op_sel_hi:[1,0]\n" \
"v_cvt_pk_bf16_f32 v224, v96, v97\n" \
"v_cvt_pk_bf16_f32 v225, v98, v99\n" \
"v_mfma_f32_16x16x32_bf16 v[20:23], a[208:211], a[160:163], v[232:235]\n" \
"s_and_b32 s70, %1, 1\n" \
"ds_read_b128 v[220:223], v246 offset:22784\n" \
"s_lshl_b32 s70, s70, 3\n" \
"v_mfma_f32_16x16x32_bf16 v[20:23], a[212:215], a[164:167], v[20:23]\n" \
"s_add_u32 s70, s70, s71\n" \
"ds_read_b128 a[192:195], v247 offset:16384\n" \
"v_cvt_pk_bf16_f32 v226, v100, v101\n" \
"v_cvt_pk_bf16_f32 v227, v102, v103\n" \
"v_lshlrev_b32 v69, 5, v69\n" \
"v_and_b32 v71, 0x30, v248\n" \
"v_pk_mul_f32 v[96:97], v[96:97], v[98:99]\n" \
"v_pk_fma_f32 v[96:97], v[100:101], v[102:103], v[96:97]\n" \
"v_fma_f32 v66, v96, v97, v66\n" \
"v_mfma_f32_16x16x32_bf16 v[20:23], a[216:219], a[168:171], v[20:23]\n" \
"s_lshl_b32 s87, s70, 12\n" \
"ds_read_b128 a[196:199], v247 offset:24576\n" \
"v_cvt_pk_bf16_f32 v106, v108, v109\n" \
"v_mfma_f32_16x16x32_bf16 v[20:23], a[220:223], a[172:175], v[20:23]\n" \
"s_lshl_b32 s70, s70, 7\n" \
"ds_read_b128 a[200:203], v247 offset:17408\n" \
"buffer_store_dwordx4 v[224:227], v65, s[56:59], s77 offen offset:0 sc1\n" \
"v_exp_f32 v111, v111\n" \
"v_exp_f32 v112, v112\n" \
"v_accvgpr_read_b32 v96, a8\n" \
"v_accvgpr_read_b32 v97, a9\n" \
"v_mfma_f32_16x16x32_bf16 v[28:31], a[216:219], a[184:187], v[236:239]\n" \
"s_mul_i32 s84, s61, 6144\n" \
"ds_read_b128 a[204:207], v247 offset:25600\n" \
"v_cvt_pk_bf16_f32 v107, v110, v111\n" \
"v_mfma_f32_16x16x32_bf16 v[28:31], a[220:223], a[188:191], v[28:31]\n" \
"s_lshl_b32 s71, s84, 4\n" \
"v_xor_b32 v69, v69, v71\n" \
"v_lshrrev_b32 v71, 9, v248\n" \
"v_mfma_f32_16x16x32_bf16 v[28:31], a[208:211], a[176:179], v[28:31]\n" \
"s_add_u32 s70, s70, s71\n" \
"s_add_u32 s70, s70, 4096\n" \
"v_lshl_add_u32 v70, v71, 6, v70\n" \
"v_mfma_f32_16x16x32_bf16 v[28:31], a[212:215], a[180:183], v[28:31]\n" \
"s_waitcnt lgkmcnt(2)\n" \
"ds_read_b128 a[208:211], v247 offset:18432\n" \
"v_add_u32 v253, v69, v70\n" \
"v_accvgpr_read_b32 v98, a10\n" \
"v_accvgpr_read_b32 v99, a11\n" \
"v_accvgpr_read_b32 v100, a12\n" \
"v_accvgpr_read_b32 v101, a13\n" \
"v_accvgpr_read_b32 v102, a14\n" \
"v_accvgpr_read_b32 v103, a15\n" \
"v_exp_f32 v113, v113\n" \
"v_exp_f32 v114, v114\n" \
"v_pk_mul_f32 v[96:97], v[96:97], v[64:65] op_sel_hi:[1,0]\n" \
"v_pk_mul_f32 v[98:99], v[98:99], v[64:65] op_sel_hi:[1,0]\n" \
"v_pk_mul_f32 v[100:101], v[100:101], v[64:65] op_sel_hi:[1,0]\n" \
"v_pk_mul_f32 v[102:103], v[102:103], v[64:65] op_sel_hi:[1,0]\n" \
"v_cvt_pk_bf16_f32 v228, v96, v97\n" \
"v_cvt_pk_bf16_f32 v229, v98, v99\n" \
"v_cvt_pk_bf16_f32 v230, v100, v101\n" \
"v_cvt_pk_bf16_f32 v231, v102, v103\n" \
"v_pk_mul_f32 v[96:97], v[96:97], v[98:99]\n" \
"v_pk_fma_f32 v[96:97], v[100:101], v[102:103], v[96:97]\n" \
"v_fma_f32 v66, v96, v97, v66\n" \
"v_mfma_f32_16x16x32_bf16 v[8:11], v[192:195], a[176:179], v[236:239]\n" \
"s_add_u32 s48, s22, s70\n" \
"s_addc_u32 s49, s23, 0\n" \
"ds_read_b128 a[212:215], v247 offset:26624\n" \
"v_mfma_f32_16x16x32_bf16 v[8:11], v[196:199], a[180:183], v[8:11]\n" \
"s_add_u32 s50, s48, 23552\n" \
"s_addc_u32 s51, s49, 0\n" \
"ds_read_b128 a[216:219], v247 offset:19456\n" \
"v_mfma_f32_16x16x32_bf16 v[8:11], v[200:203], a[184:187], v[8:11]\n" \
"s_lshr_b32 s89, s22, 7\n" \
"ds_read_b128 a[220:223], v247 offset:27648\n" \
"v_cvt_pk_bf16_f32 v112, v112, v113\n" \
"v_mfma_f32_16x16x32_bf16 v[8:11], v[204:207], a[188:191], v[8:11]\n" \
"s_sub_u32 s89, 3, s89\n" \
"ds_read_b128 v[192:195], v246 offset:24576\n" \
"v_bfe_u32 v84, v248, 7, 2\n" \
"s_and_b32 s89, s89, 7\n" \
"v_mfma_f32_16x16x32_bf16 v[56:59], a[232:235], a[184:187], v[236:239]\n" \
"s_and_b32 s90, %1, 1\n" \
"ds_read_b128 v[196:199], v246 offset:26624\n" \
"v_bfe_u32 v85, v248, 5, 2\n" \
"v_mfma_f32_16x16x32_bf16 v[56:59], a[236:239], a[188:191], v[56:59]\n" \
"s_lshl_b32 s90, s90, 3\n" \
"ds_read_b128 v[200:203], v246 offset:28672\n" \
"v_exp_f32 v115, v115\n" \
"v_mfma_f32_16x16x32_bf16 v[56:59], a[224:227], a[176:179], v[56:59]\n" \
"s_add_u32 s89, s89, s90\n" \
"ds_read_b128 v[204:207], v246 offset:30720\n" \
"v_min_u32 v85, 2, v85\n" \
"v_mfma_f32_16x16x32_bf16 v[56:59], a[228:231], a[180:183], v[56:59]\n" \
"s_lshl_b32 s89, s89, 7\n" \
"s_lshr_b32 s90, %1, 3\n" \
"v_mad_u32_u24 v84, v84, 3, v85\n" \
"s_lshl_b32 s90, s90, 2\n" \
"v_mfma_f32_16x16x32_bf16 v[48:51], a[224:227], a[160:163], v[232:235]\n" \
"s_add_u32 s90, s90, s61\n" \
"v_and_b32 v85, 16, v248\n" \
"s_mul_i32 s90, s90, 24576\n" \
"v_mfma_f32_16x16x32_bf16 v[48:51], a[228:231], a[164:167], v[48:51]\n" \
"s_add_u32 s89, s89, s90\n" \
"v_lshl_add_u32 v85, v85, 2, s89\n" \
"v_lshl_add_u32 v84, v84, 11, v85\n" \
"v_mfma_f32_16x16x32_bf16 v[48:51], a[232:235], a[168:171], v[48:51]\n" \
"s_add_u32 m0, s84, 143360\n" \
"v_exp_f32 v116, v116\n" \
"v_mfma_f32_16x16x32_bf16 v[48:51], a[236:239], a[172:175], v[48:51]\n" \
"ds_read_b128 a[224:227], v247 offset:20480\n" \
"global_load_lds_dwordx4 v253, s[48:49] offset:-4096\n" \
"v_mfma_f32_16x16x32_bf16 v[12:15], v[216:219], a[184:187], v[236:239]\n" \
"ds_read_b128 a[228:231], v247 offset:28672\n" \
"v_cvt_pk_bf16_f32 v113, v114, v115\n" \
"v_mfma_f32_16x16x32_bf16 v[12:15], v[220:223], a[188:191], v[12:15]\n" \
"ds_read_b128 a[232:235], v247 offset:21504\n" \
"v_exp_f32 v117, v117\n" \
"v_mfma_f32_16x16x32_bf16 v[12:15], v[208:211], a[176:179], v[12:15]\n" \
"ds_read_b128 a[236:239], v247 offset:29696\n" \
"global_load_lds_dwordx4 v253, s[48:49] offset:-2048\n" \
"v_mfma_f32_16x16x32_bf16 v[12:15], v[212:215], a[180:183], v[12:15]\n" \
"ds_read_b128 v[208:211], v246 offset:24832\n" \
"v_exp_f32 v118, v118\n" \
"v_mfma_f32_16x16x32_bf16 v[60:63], a[240:243], a[176:179], v[236:239]\n" \
"ds_read_b128 v[212:215], v246 offset:26880\n" \
"v_cvt_pk_bf16_f32 v114, v116, v117\n" \
"v_mfma_f32_16x16x32_bf16 v[60:63], a[244:247], a[180:183], v[60:63]\n" \
"ds_read_b128 v[216:219], v246 offset:28928\n" \
"global_load_lds_dwordx4 v253, s[48:49] offset:0\n" \
"v_mfma_f32_16x16x32_bf16 v[60:63], a[248:251], a[184:187], v[60:63]\n" \
"s_add_u32 s48, s48, 0x120000\n" \
"s_addc_u32 s49, s49, 0\n" \
"ds_read_b128 v[220:223], v246 offset:30976\n" \
"v_mfma_f32_16x16x32_bf16 v[60:63], a[252:255], a[188:191], v[60:63]\n" \
"buffer_store_dwordx4 v[228:231], v65, s[56:59], s77 offen offset:2048 sc1\n" \
"v_exp_f32 v119, v119\n" \
"v_exp_f32 v120, v120\n" \
"v_cvt_pk_bf16_f32 v115, v118, v119\n" \
"v_mfma_f32_16x16x32_bf16 v[52:55], a[248:251], a[168:171], v[232:235]\n" \
"global_load_lds_dwordx4 v253, s[50:51] offset:-3072\n" \
"global_load_lds_dwordx4 v253, s[50:51] offset:-1024\n" \
"v_mfma_f32_16x16x32_bf16 v[52:55], a[252:255], a[172:175], v[52:55]\n" \
"s_add_u32 s77, s77, 0x1000\n" \
"v_exp_f32 v121, v121\n" \
"v_mfma_f32_16x16x32_bf16 v[52:55], a[240:243], a[160:163], v[52:55]\n" \
"v_exp_f32 v122, v122\n" \
"v_mfma_f32_16x16x32_bf16 v[52:55], a[244:247], a[164:167], v[52:55]\n" \
"s_waitcnt lgkmcnt(0)\n" \
"ds_read_b128 a[240:243], v247 offset:22528\n" \
"v_accvgpr_read_b32 v96, a16\n" \
"v_accvgpr_read_b32 v97, a17\n" \
"v_accvgpr_read_b32 v98, a18\n" \
"v_accvgpr_read_b32 v99, a19\n" \
"v_accvgpr_read_b32 v100, a20\n" \
"v_accvgpr_read_b32 v101, a21\n" \
"v_accvgpr_read_b32 v102, a22\n" \
"v_accvgpr_read_b32 v103, a23\n" \
"v_cvt_pk_bf16_f32 v120, v120, v121\n" \
"v_exp_f32 v123, v123\n" \
"v_pk_mul_f32 v[96:97], v[96:97], v[64:65] op_sel_hi:[1,0]\n" \
"v_pk_mul_f32 v[98:99], v[98:99], v[64:65] op_sel_hi:[1,0]\n" \
"v_pk_mul_f32 v[100:101], v[100:101], v[64:65] op_sel_hi:[1,0]\n" \
"v_pk_mul_f32 v[102:103], v[102:103], v[64:65] op_sel_hi:[1,0]\n" \
"v_cvt_pk_bf16_f32 v224, v96, v97\n" \
"v_cvt_pk_bf16_f32 v225, v98, v99\n" \
"v_cvt_pk_bf16_f32 v226, v100, v101\n" \
"v_cvt_pk_bf16_f32 v227, v102, v103\n" \
"v_pk_mul_f32 v[96:97], v[96:97], v[98:99]\n" \
"v_pk_fma_f32 v[96:97], v[100:101], v[102:103], v[96:97]\n" \
"v_fma_f32 v66, v96, v97, v66\n" \
"v_mfma_f32_16x16x32_bf16 v[40:43], v[192:195], a[176:179], v[236:239]\n" \
"buffer_store_dwordx4 v[224:227], v65, s[56:59], s77 offen offset:0 sc1\n" \
"ds_read_b128 a[244:247], v247 offset:30720\n" \
"v_accvgpr_read_b32 v96, a24\n" \
"v_accvgpr_read_b32 v97, a25\n" \
"v_accvgpr_read_b32 v98, a26\n" \
"v_accvgpr_read_b32 v99, a27\n" \
"v_accvgpr_read_b32 v100, a28\n" \
"v_accvgpr_read_b32 v101, a29\n" \
"v_accvgpr_read_b32 v102, a30\n" \
"v_accvgpr_read_b32 v103, a31\n" \
"v_exp_f32 v124, v124\n" \
"v_pk_mul_f32 v[96:97], v[96:97], v[64:65] op_sel_hi:[1,0]\n" \
"v_pk_mul_f32 v[98:99], v[98:99], v[64:65] op_sel_hi:[1,0]\n" \
"v_pk_mul_f32 v[100:101], v[100:101], v[64:65] op_sel_hi:[1,0]\n" \
"v_pk_mul_f32 v[102:103], v[102:103], v[64:65] op_sel_hi:[1,0]\n" \
"v_mfma_f32_16x16x32_bf16 v[40:43], v[196:199], a[180:183], v[40:43]\n" \
"ds_read_b128 a[248:251], v247 offset:23552\n" \
"v_cvt_pk_bf16_f32 v121, v122, v123\n" \
"v_mfma_f32_16x16x32_bf16 v[40:43], v[200:203], a[184:187], v[40:43]\n" \
"ds_read_b128 a[252:255], v247 offset:31744\n" \
"global_load_lds_dwordx4 v253, s[50:51] offset:1024\n" \
"v_mfma_f32_16x16x32_bf16 v[40:43], v[204:207], a[188:191], v[40:43]\n" \
"s_add_u32 s50, s50, 0x120000\n" \
"s_addc_u32 s51, s51, 0\n" \
"v_exp_f32 v125, v125\n" \
"v_mfma_f32_16x16x32_bf16 v[44:47], v[216:219], a[184:187], v[236:239]\n" \
"v_exp_f32 v126, v126\n" \
"v_mfma_f32_16x16x32_bf16 v[44:47], v[220:223], a[188:191], v[44:47]\n" \
"global_load_dword v85, v84, s[22:23]\n" \
"v_mfma_f32_16x16x32_bf16 v[44:47], v[208:211], a[176:179], v[44:47]\n" \
"s_waitcnt vmcnt(10) lgkmcnt(0)\n" \
"v_add_u32 v247, 0x10000, v248\n" \
"v_cvt_pk_bf16_f32 v122, v124, v125\n" \
"v_mfma_f32_16x16x32_bf16 v[44:47], v[212:215], a[180:183], v[44:47]\n" \
"s_barrier\n" \
"v_cvt_pk_bf16_f32 v228, v96, v97\n" \
"v_cvt_pk_bf16_f32 v229, v98, v99\n" \
"v_cvt_pk_bf16_f32 v230, v100, v101\n" \
"v_cvt_pk_bf16_f32 v231, v102, v103\n" \
"v_pk_mul_f32 v[96:97], v[96:97], v[98:99]\n" \
"v_pk_fma_f32 v[96:97], v[100:101], v[102:103], v[96:97]\n" \
"v_mfma_f32_16x16x32_bf16 a[32:35], a[192:195], v[72:75], a[32:35]\n" \
"v_exp_f32 v127, v127\n" \
"v_mfma_f32_16x16x32_bf16 a[32:35], a[196:199], v[104:107], a[32:35]\n" \
"v_cvt_pk_bf16_f32 v123, v126, v127\n" \
"v_fma_f32 v66, v96, v97, v66\n" \
"v_mfma_f32_16x16x32_bf16 v[128:131], a[192:195], v[80:83], v[128:131]\n" \
"ds_read_b128 v[192:195], v247 offset:16384\n" \
"v_cmp_gt_i32 vcc, 0, v251\n" \
"v_mfma_f32_16x16x32_bf16 v[128:131], a[196:199], v[112:115], v[128:131]\n" \
"buffer_store_dwordx4 v[228:231], v65, s[56:59], s77 offen offset:2048 sc1\n" \
"v_lshrrev_b32 v71, 7, v248\n" \
"v_and_b32 v76, 0x70, v248\n" \
"v_cmp_class_f32 s[54:55], v66, s94\n" \
"s_sub_u32 s77, s77, 4096\n" \
"v_mfma_f32_16x16x32_bf16 v[160:163], a[192:195], v[88:91], v[160:163]\n" \
"s_lshl_b32 s70, s88, 18\n" \
"v_cmp_gt_i32 s[64:65], 1, v251\n" \
"v_cmp_gt_i32 s[66:67], 2, v251\n" \
"v_mfma_f32_16x16x32_bf16 v[160:163], a[196:199], v[120:123], v[160:163]\n" \
"s_lshl_b32 s71, s61, 16\n" \
"ds_read_b128 a[192:195], v247 offset:0\n" \
"v_cndmask_b32 v8, v8, v254, vcc\n" \
"v_mfma_f32_16x16x32_bf16 v[240:243], a[124:127], v[104:107], v[240:243]\n" \
"s_add_u32 s71, s71, s70\n" \
"ds_read_b128 v[196:199], v247 offset:24576\n" \
"v_cndmask_b32 v16, v16, v254, vcc\n" \
"v_mfma_f32_16x16x32_bf16 v[164:167], a[200:203], v[88:91], v[164:167]\n" \
"s_or_b64 s[92:93], s[92:93], s[54:55]\n" \
"v_lshlrev_b32 v77, 4, v71\n" \
"v_xor_b32 v77, v77, v76\n" \
"v_mfma_f32_16x16x32_bf16 v[164:167], a[204:207], v[120:123], v[164:167]\n" \
"s_add_u32 s42, s20, s71\n" \
"s_addc_u32 s43, s21, 0\n" \
"ds_read_b128 a[196:199], v247 offset:8192\n" \
"v_cmp_gt_i32 vcc, 3, v251\n" \
"v_cndmask_b32 v9, v9, v254, s[64:65]\n" \
"v_cndmask_b32 v17, v17, v254, s[64:65]\n" \
"v_cmp_gt_i32 s[64:65], 16, v251\n" \
"v_cndmask_b32 v10, v10, v254, s[66:67]\n" \
"v_cndmask_b32 v18, v18, v254, s[66:67]\n" \
"v_cmp_gt_i32 s[66:67], 17, v251\n" \
"v_mfma_f32_16x16x32_bf16 v[132:135], a[200:203], v[80:83], v[132:135]\n" \
"ds_read_b128 v[200:203], v247 offset:17408\n" \
"v_lshl_add_u32 v252, v71, 12, v77\n" \
"v_mfma_f32_16x16x32_bf16 v[132:135], a[204:207], v[112:115], v[132:135]\n" \
"s_add_u32 s44, s42, 31744\n" \
"s_addc_u32 s45, s43, 0\n" \
"v_cndmask_b32 v11, v11, v254, vcc\n" \
"v_mfma_f32_16x16x32_bf16 a[36:39], a[200:203], v[72:75], a[36:39]\n" \
"s_max_u32 s70, s70, 32768\n" \
"v_cndmask_b32 v19, v19, v254, vcc\n" \
"v_cmp_gt_i32 vcc, 18, v251\n" \
"v_mfma_f32_16x16x32_bf16 a[36:39], a[204:207], v[104:107], a[36:39]\n" \
"s_sub_u32 s70, s70, 32768\n" \
"ds_read_b128 a[200:203], v247 offset:1024\n" \
"v_cndmask_b32 v12, v12, v254, s[64:65]\n" \
"v_mfma_f32_16x16x32_bf16 v[242:245], a[120:123], v[112:115], v[242:245]\n" \
"s_and_b32 s86, s61, 1\n" \
"ds_read_b128 v[204:207], v247 offset:25600\n" \
"v_cndmask_b32 v20, v20, v254, s[64:65]\n" \
"v_mfma_f32_16x16x32_bf16 a[40:43], a[208:211], v[72:75], a[40:43]\n" \
"s_lshl_b32 s86, s86, 2\n" \
"v_cmp_gt_i32 s[64:65], 19, v251\n" \
"v_and_or_b32 v71, v71, 3, s86\n" \
"v_mfma_f32_16x16x32_bf16 v[242:245], a[124:127], v[120:123], v[242:245]\n" \
"v_cndmask_b32 v13, v13, v254, s[66:67]\n" \
"v_cndmask_b32 v21, v21, v254, s[66:67]\n" \
"v_cmp_gt_i32 s[66:67], 32, v251\n" \
"v_mfma_f32_16x16x32_bf16 a[40:43], a[212:215], v[104:107], a[40:43]\n" \
"s_lshr_b32 s71, s61, 1\n" \
"ds_read_b128 a[204:207], v247 offset:9216\n" \
"v_cndmask_b32 v14, v14, v254, vcc\n" \
"v_cndmask_b32 v22, v22, v254, vcc\n" \
"v_cmp_gt_i32 vcc, 33, v251\n" \
"v_cndmask_b32 v15, v15, v254, s[64:65]\n" \
"v_cndmask_b32 v23, v23, v254, s[64:65]\n" \
"v_cmp_gt_i32 s[64:65], 34, v251\n" \
"v_cndmask_b32 v40, v40, v254, s[66:67]\n" \
"v_cndmask_b32 v48, v48, v254, s[66:67]\n" \
"v_cmp_gt_i32 s[66:67], 35, v251\n" \
"v_cndmask_b32 v41, v41, v254, vcc\n" \
"v_mfma_f32_16x16x32_bf16 v[136:139], a[208:211], v[80:83], v[136:139]\n" \
"s_lshl_b32 s71, s71, 11\n" \
"ds_read_b128 v[208:211], v247 offset:18432\n" \
"v_lshlrev_b32 v77, 4, v71\n" \
"v_mfma_f32_16x16x32_bf16 v[136:139], a[212:215], v[112:115], v[136:139]\n" \
"s_add_u32 s70, s70, s71\n" \
"v_mov_b32 v66, v243\n" \
"v_cndmask_b32 v49, v49, v254, vcc\n" \
"v_cmp_gt_i32 vcc, 48, v251\n" \
"v_cndmask_b32 v42, v42, v254, s[64:65]\n" \
"v_cndmask_b32 v50, v50, v254, s[64:65]\n" \
"v_cmp_gt_i32 s[64:65], 49, v251\n" \
"v_cndmask_b32 v43, v43, v254, s[66:67]\n" \
"v_cndmask_b32 v51, v51, v254, s[66:67]\n" \
"v_cmp_gt_i32 s[66:67], 50, v251\n" \
"v_cndmask_b32 v44, v44, v254, vcc\n" \
"v_cndmask_b32 v52, v52, v254, vcc\n" \
"v_xor_b32 v77, v77, v76\n" \
"v_mfma_f32_16x16x32_bf16 v[168:171], a[208:211], v[88:91], v[168:171]\n" \
"s_mov_b32 s46, s70\n" \
"v_cmp_gt_i32 vcc, 51, v251\n" \
"v_lshl_add_u32 v246, v71, 12, v77\n" \
"v_mfma_f32_16x16x32_bf16 v[168:171], a[212:215], v[120:123], v[168:171]\n" \
"s_lshl_b32 s85, s61, 10\n" \
"ds_read_b128 a[208:211], v247 offset:2048\n" \
"s_mov_b64 s[12:13], s[20:21]\n" \
"ds_read_b128 v[212:215], v247 offset:26624\n" \
"v_cndmask_b32 v45, v45, v254, s[64:65]\n" \
"v_cndmask_b32 v53, v53, v254, s[64:65]\n" \
"v_mfma_f32_16x16x32_bf16 v[172:175], a[216:219], v[88:91], v[172:175]\n" \
"s_and_b32 s13, s13, 0xffff\n" \
"v_cndmask_b32 v46, v46, v254, s[66:67]\n" \
"v_cndmask_b32 v54, v54, v254, s[66:67]\n" \
"v_mfma_f32_16x16x32_bf16 v[172:175], a[220:223], v[120:123], v[172:175]\n" \
"s_mov_b32 s14, 4194304\n" \
"ds_read_b128 a[212:215], v247 offset:10240\n" \
"v_cndmask_b32 v47, v47, v254, vcc\n" \
"v_mfma_f32_16x16x32_bf16 v[140:143], a[216:219], v[80:83], v[140:143]\n" \
"s_mov_b32 s15, 0x20000\n" \
"ds_read_b128 v[216:219], v247 offset:19456\n" \
"v_cndmask_b32 v55, v55, v254, vcc\n" \
"v_mfma_f32_16x16x32_bf16 v[140:143], a[220:223], v[112:115], v[140:143]\n" \
"v_lshrrev_b32 v71, 9, v248\n" \
"v_lshl_add_u32 v246, v71, 30, v246\n" \
"v_exp_f32 v8, v8\n" \
"v_exp_f32 v9, v9\n" \
"v_mfma_f32_16x16x32_bf16 a[44:47], a[216:219], v[72:75], a[44:47]\n" \
"s_add_u32 m0, s95, 118784\n" \
"v_exp_f32 v10, v10\n" \
"v_mfma_f32_16x16x32_bf16 a[44:47], a[220:223], v[104:107], a[44:47]\n" \
"ds_read_b128 a[216:219], v247 offset:3072\n" \
"global_load_lds_dwordx4 v252, s[42:43] offset:0\n" \
"ds_read_b128 v[220:223], v247 offset:27648\n" \
"v_cvt_pk_bf16_f32 v8, v8, v9\n" \
"v_mfma_f32_16x16x32_bf16 a[48:51], a[224:227], v[72:75], a[48:51]\n" \
"v_exp_f32 v11, v11\n" \
"v_mfma_f32_16x16x32_bf16 a[48:51], a[228:231], v[104:107], a[48:51]\n" \
"ds_read_b128 a[220:223], v247 offset:11264\n" \
"v_exp_f32 v12, v12\n" \
"v_mfma_f32_16x16x32_bf16 v[144:147], a[224:227], v[80:83], v[144:147]\n" \
"ds_read_b128 v[224:227], v247 offset:20480\n" \
"v_cvt_pk_bf16_f32 v9, v10, v11\n" \
"v_mfma_f32_16x16x32_bf16 v[144:147], a[228:231], v[112:115], v[144:147]\n" \
"v_exp_f32 v13, v13\n" \
"v_mfma_f32_16x16x32_bf16 v[176:179], a[224:227], v[88:91], v[176:179]\n" \
"v_exp_f32 v14, v14\n" \
"v_mfma_f32_16x16x32_bf16 v[176:179], a[228:231], v[120:123], v[176:179]\n" \
"ds_read_b128 a[224:227], v247 offset:4096\n" \
"global_load_lds_dwordx4 v252, s[42:43] offset:2048\n" \
"ds_read_b128 v[228:231], v247 offset:28672\n" \
"v_mfma_f32_16x16x32_bf16 v[180:183], a[232:235], v[88:91], v[180:183]\n" \
"s_add_u32 s42, s42, 0x180\n" \
"s_addc_u32 s43, s43, 0\n" \
"v_cvt_pk_bf16_f32 v10, v12, v13\n" \
"v_mfma_f32_16x16x32_bf16 v[180:183], a[236:239], v[120:123], v[180:183]\n" \
"ds_read_b128 a[228:231], v247 offset:12288\n" \
"v_exp_f32 v15, v15\n" \
"v_exp_f32 v40, v40\n" \
"v_mfma_f32_16x16x32_bf16 v[148:151], a[232:235], v[80:83], v[148:151]\n" \
"ds_read_b128 v[232:235], v247 offset:21504\n" \
"v_cvt_pk_bf16_f32 v11, v14, v15\n" \
"v_mfma_f32_16x16x32_bf16 v[148:151], a[236:239], v[112:115], v[148:151]\n" \
"v_exp_f32 v41, v41\n" \
"v_mfma_f32_16x16x32_bf16 a[52:55], a[232:235], v[72:75], a[52:55]\n" \
"v_exp_f32 v42, v42\n" \
"v_mfma_f32_16x16x32_bf16 a[52:55], a[236:239], v[104:107], a[52:55]\n" \
"ds_read_b128 a[232:235], v247 offset:5120\n" \
"global_load_lds_dwordx4 v252, s[44:45] offset:1024\n" \
"ds_read_b128 v[236:239], v247 offset:29696\n" \
"v_cvt_pk_bf16_f32 v40, v40, v41\n" \
"v_mfma_f32_16x16x32_bf16 a[56:59], a[240:243], v[72:75], a[56:59]\n" \
"v_exp_f32 v43, v43\n" \
"v_mfma_f32_16x16x32_bf16 a[56:59], a[244:247], v[104:107], a[56:59]\n" \
"ds_read_b128 a[236:239], v247 offset:13312\n" \
"v_exp_f32 v44, v44\n" \
"v_mfma_f32_16x16x32_bf16 v[152:155], a[240:243], v[80:83], v[152:155]\n" \
"ds_read_b128 v[0:3], v247 offset:22528\n" \
"v_cvt_pk_bf16_f32 v41, v42, v43\n" \
"v_mfma_f32_16x16x32_bf16 v[152:155], a[244:247], v[112:115], v[152:155]\n" \
"v_exp_f32 v45, v45\n" \
"v_mfma_f32_16x16x32_bf16 v[184:187], a[240:243], v[88:91], v[184:187]\n" \
"v_exp_f32 v46, v46\n" \
"v_mfma_f32_16x16x32_bf16 v[184:187], a[244:247], v[120:123], v[184:187]\n" \
"ds_read_b128 a[240:243], v247 offset:6144\n" \
"global_load_lds_dwordx4 v252, s[44:45] offset:3072\n" \
"ds_read_b128 v[4:7], v247 offset:30720\n" \
"v_mfma_f32_16x16x32_bf16 a[60:63], a[248:251], v[72:75], a[60:63]\n" \
"s_add_u32 s44, s44, 0x180\n" \
"s_addc_u32 s45, s45, 0\n" \
"v_cvt_pk_bf16_f32 v42, v44, v45\n" \
"v_mfma_f32_16x16x32_bf16 a[60:63], a[252:255], v[104:107], a[60:63]\n" \
"s_add_u32 m0, s85, 135168\n" \
"ds_read_b128 a[244:247], v247 offset:14336\n" \
"v_exp_f32 v47, v47\n" \
"v_exp_f32 v24, v24\n" \
"v_mfma_f32_16x16x32_bf16 v[156:159], a[248:251], v[80:83], v[156:159]\n" \
"ds_read_b128 v[32:35], v247 offset:23552\n" \
"v_cvt_pk_bf16_f32 v43, v46, v47\n" \
"v_mfma_f32_16x16x32_bf16 v[156:159], a[252:255], v[112:115], v[156:159]\n" \
"buffer_load_dwordx4 v246, s[12:15], s46 offen lds\n" \
"v_exp_f32 v25, v25\n" \
"v_mfma_f32_16x16x32_bf16 v[188:191], a[248:251], v[88:91], v[188:191]\n" \
"s_add_u32 s46, s46, 0x180\n" \
"v_exp_f32 v26, v26\n" \
"v_mfma_f32_16x16x32_bf16 v[188:191], a[252:255], v[120:123], v[188:191]\n" \
"s_waitcnt lgkmcnt(2)\n" \
"ds_read_b128 a[248:251], v247 offset:7168\n" \
"v_cvt_pk_bf16_f32 v24, v24, v25\n" \
"v_mfma_f32_16x16x32_bf16 v[242:245], a[124:127], v[8:11], v[242:245]\n" \
"v_exp_f32 v27, v27\n" \
"v_mfma_f32_16x16x32_bf16 v[242:245], a[124:127], v[40:43], v[242:245]\n" \
"ds_read_b128 v[36:39], v247 offset:31744\n" \
"v_exp_f32 v28, v28\n" \
"v_mfma_f32_16x16x32_bf16 v[160:163], v[192:195], v[8:11], v[160:163]\n" \
"v_cvt_pk_bf16_f32 v25, v26, v27\n" \
"v_exp_f32 v29, v29\n" \
"v_exp_f32 v30, v30\n" \
"v_cvt_pk_bf16_f32 v26, v28, v29\n" \
"v_exp_f32 v31, v31\n" \
"v_exp_f32 v56, v56\n" \
"v_cvt_pk_bf16_f32 v27, v30, v31\n" \
"v_exp_f32 v57, v57\n" \
"v_exp_f32 v58, v58\n" \
"v_cvt_pk_bf16_f32 v56, v56, v57\n" \
"v_mfma_f32_16x16x32_bf16 v[160:163], v[196:199], v[40:43], v[160:163]\n" \
"ds_read_b128 a[252:255], v247 offset:15360\n" \
"v_exp_f32 v59, v59\n" \
"v_mfma_f32_16x16x32_bf16 v[164:167], v[200:203], v[8:11], v[164:167]\n" \
"v_exp_f32 v60, v60\n" \
"v_mfma_f32_16x16x32_bf16 v[164:167], v[204:207], v[40:43], v[164:167]\n" \
"v_mov_b32 v64, v66\n" \
"v_cvt_pk_bf16_f32 v57, v58, v59\n" \
"v_exp_f32 v61, v61\n" \
"v_mfma_f32_16x16x32_bf16 v[168:171], v[208:211], v[8:11], v[168:171]\n" \
"v_exp_f32 v62, v62\n" \
"v_mfma_f32_16x16x32_bf16 v[168:171], v[212:215], v[40:43], v[168:171]\n" \
"v_cvt_pk_bf16_f32 v58, v60, v61\n" \
"v_exp_f32 v63, v63\n" \
"v_permlane16_swap_b32 v64, v66\n" \
"v_add_f32 v66, v66, v64\n" \
"v_mov_b32 v64, v66\n" \
"v_mfma_f32_16x16x32_bf16 v[172:175], v[216:219], v[8:11], v[172:175]\n" \
"s_waitcnt vmcnt(0) lgkmcnt(0)\n" \
"v_exp_f32 v16, v16\n" \
"v_mfma_f32_16x16x32_bf16 v[172:175], v[220:223], v[40:43], v[172:175]\n" \
"s_barrier\n" \
"v_cvt_pk_bf16_f32 v59, v62, v63\n" \
"v_exp_f32 v17, v17\n" \
"v_permlane32_swap_b32 v64, v66\n" \
"v_add_f32 v66, v66, v64\n" \
"v_rcp_f32 v64, v66\n" \
"v_mul_f32 v66, 0x4d800000, v66\n" \
"v_add_u32 v65, 0x1c90080, v249\n" \
"v_cndmask_b32 v65, v249, v65, s[16:17]\n" \
"v_mfma_f32_16x16x32_bf16 v[176:179], v[224:227], v[8:11], v[176:179]\n" \
"s_add_u32 m0, s84, 90112\n" \
"v_exp_f32 v18, v18\n" \
"v_mfma_f32_16x16x32_bf16 v[176:179], v[228:231], v[40:43], v[176:179]\n" \
"global_load_lds_dwordx4 v253, s[48:49] offset:-4096\n" \
"global_load_lds_dwordx4 v253, s[48:49] offset:-2048\n" \
"v_cvt_pk_bf16_f32 v16, v16, v17\n" \
"v_mfma_f32_16x16x32_bf16 v[242:245], a[124:127], v[24:27], v[242:245]\n" \
"v_exp_f32 v19, v19\n" \
"v_mfma_f32_16x16x32_bf16 v[242:245], a[124:127], v[56:59], v[242:245]\n" \
"v_exp_f32 v20, v20\n" \
"v_cvt_pk_bf16_f32 v17, v18, v19\n" \
"v_mfma_f32_16x16x32_bf16 v[180:183], v[232:235], v[8:11], v[180:183]\n" \
"s_add_u32 s77, s77, 532480\n" \
"v_exp_f32 v21, v21\n" \
"v_mfma_f32_16x16x32_bf16 v[180:183], v[236:239], v[40:43], v[180:183]\n" \
"v_accvgpr_read_b32 v96, a32\n" \
"v_accvgpr_read_b32 v97, a33\n" \
"v_accvgpr_read_b32 v98, a34\n" \
"v_accvgpr_read_b32 v99, a35\n" \
"v_accvgpr_read_b32 v100, a36\n" \
"v_accvgpr_read_b32 v101, a37\n" \
"v_accvgpr_read_b32 v102, a38\n" \
"v_accvgpr_read_b32 v103, a39\n" \
"v_exp_f32 v22, v22\n" \
"v_pk_mul_f32 v[96:97], v[96:97], v[64:65] op_sel_hi:[1,0]\n" \
"v_pk_mul_f32 v[98:99], v[98:99], v[64:65] op_sel_hi:[1,0]\n" \
"v_pk_mul_f32 v[100:101], v[100:101], v[64:65] op_sel_hi:[1,0]\n" \
"v_pk_mul_f32 v[102:103], v[102:103], v[64:65] op_sel_hi:[1,0]\n" \
"v_cvt_pk_bf16_f32 v104, v96, v97\n" \
"v_cvt_pk_bf16_f32 v105, v98, v99\n" \
"v_cvt_pk_bf16_f32 v106, v100, v101\n" \
"v_cvt_pk_bf16_f32 v107, v102, v103\n" \
"v_pk_mul_f32 v[96:97], v[96:97], v[98:99]\n" \
"v_pk_fma_f32 v[96:97], v[100:101], v[102:103], v[96:97]\n" \
"v_fma_f32 v66, v96, v97, v66\n" \
"v_mfma_f32_16x16x32_bf16 v[184:187], v[0:3], v[8:11], v[184:187]\n" \
"global_load_lds_dwordx4 v253, s[48:49] offset:0\n" \
"v_cvt_pk_bf16_f32 v18, v20, v21\n" \
"v_mfma_f32_16x16x32_bf16 v[184:187], v[4:7], v[40:43], v[184:187]\n" \
"s_add_u32 s48, s48, 0x300000\n" \
"s_addc_u32 s49, s49, 0\n" \
"v_exp_f32 v23, v23\n" \
"v_exp_f32 v48, v48\n" \
"v_mfma_f32_16x16x32_bf16 v[188:191], v[32:35], v[8:11], v[188:191]\n" \
"global_load_lds_dwordx4 v253, s[50:51] offset:-3072\n" \
"v_cvt_pk_bf16_f32 v19, v22, v23\n" \
"v_mfma_f32_16x16x32_bf16 v[188:191], v[36:39], v[40:43], v[188:191]\n" \
"buffer_store_dwordx4 v[104:107], v65, s[56:59], s77 offen offset:0 sc1\n" \
"v_accvgpr_read_b32 v96, a40\n" \
"v_accvgpr_read_b32 v97, a41\n" \
"v_accvgpr_read_b32 v98, a42\n" \
"v_accvgpr_read_b32 v99, a43\n" \
"v_accvgpr_read_b32 v100, a44\n" \
"v_accvgpr_read_b32 v101, a45\n" \
"v_accvgpr_read_b32 v102, a46\n" \
"v_accvgpr_read_b32 v103, a47\n" \
"v_exp_f32 v49, v49\n" \
"v_pk_mul_f32 v[96:97], v[96:97], v[64:65] op_sel_hi:[1,0]\n" \
"v_pk_mul_f32 v[98:99], v[98:99], v[64:65] op_sel_hi:[1,0]\n" \
"v_pk_mul_f32 v[100:101], v[100:101], v[64:65] op_sel_hi:[1,0]\n" \
"v_pk_mul_f32 v[102:103], v[102:103], v[64:65] op_sel_hi:[1,0]\n" \
"v_cvt_pk_bf16_f32 v108, v96, v97\n" \
"v_cvt_pk_bf16_f32 v109, v98, v99\n" \
"v_cvt_pk_bf16_f32 v110, v100, v101\n" \
"v_cvt_pk_bf16_f32 v111, v102, v103\n" \
"v_pk_mul_f32 v[96:97], v[96:97], v[98:99]\n" \
"v_pk_fma_f32 v[96:97], v[100:101], v[102:103], v[96:97]\n" \
"v_mfma_f32_16x16x32_bf16 v[160:163], a[192:195], v[24:27], v[160:163]\n" \
"global_load_lds_dwordx4 v253, s[50:51] offset:-1024\n" \
"global_load_lds_dwordx4 v253, s[50:51] offset:1024\n" \
"v_mfma_f32_16x16x32_bf16 v[160:163], a[196:199], v[56:59], v[160:163]\n" \
"s_movk_i32 s89, 0xc0\n" \
"v_exp_f32 v50, v50\n" \
"v_cvt_pk_bf16_f32 v48, v48, v49\n" \
"v_mfma_f32_16x16x32_bf16 v[242:245], a[120:123], v[16:19], v[242:245]\n" \
"s_movk_i32 s90, 288\n" \
"v_exp_f32 v51, v51\n" \
"v_lshrrev_b32 v211, 4, v248\n" \
"v_mfma_f32_16x16x32_bf16 v[164:167], a[200:203], v[24:27], v[164:167]\n" \
"s_add_u32 s50, s50, 0x300000\n" \
"s_addc_u32 s51, s51, 0\n" \
"v_lshrrev_b32 v214, 8, v248\n" \
"v_mfma_f32_16x16x32_bf16 v[164:167], a[204:207], v[56:59], v[164:167]\n" \
"s_add_u32 m0, s95, 65536\n" \
"v_and_b32 v215, 7, v211\n" \
"v_xor_b32 v214, v214, v215\n" \
"v_exp_f32 v52, v52\n" \
"v_cvt_pk_bf16_f32 v49, v50, v51\n" \
"v_exp_f32 v53, v53\n" \
"v_exp_f32 v54, v54\n" \
"v_cvt_pk_bf16_f32 v50, v52, v53\n" \
"v_exp_f32 v55, v55\n" \
"v_mfma_f32_16x16x32_bf16 v[168:171], a[208:211], v[24:27], v[168:171]\n" \
"global_load_lds_dwordx4 v252, s[42:43] offset:0\n" \
"global_load_lds_dwordx4 v252, s[42:43] offset:2048\n" \
"v_mfma_f32_16x16x32_bf16 v[168:171], a[212:215], v[56:59], v[168:171]\n" \
"s_lshr_b32 s71, s61, 1\n" \
"v_cvt_pk_bf16_f32 v51, v54, v55\n" \
"s_lshl_b32 s70, s71, 11\n" \
"v_mfma_f32_16x16x32_bf16 v[172:175], a[216:219], v[24:27], v[172:175]\n" \
"s_add_u32 s70, s70, 118784\n" \
"v_and_b32 v216, 15, v211\n" \
"v_lshlrev_b32 v213, 7, v216\n" \
"v_mfma_f32_16x16x32_bf16 v[172:175], a[220:223], v[56:59], v[172:175]\n" \
"v_fma_f32 v66, v96, v97, v66\n" \
"v_lshl_add_u32 v213, v214, 4, v213\n" \
"v_mfma_f32_16x16x32_bf16 v[242:245], a[120:123], v[48:51], v[242:245]\n" \
"s_add_u32 s42, s42, 0x400\n" \
"s_addc_u32 s43, s43, 0\n" \
"v_add_u32 v205, s70, v213\n" \
"v_mfma_f32_16x16x32_bf16 v[176:179], a[224:227], v[24:27], v[176:179]\n" \
"global_load_lds_dwordx4 v252, s[44:45] offset:1024\n" \
"global_load_lds_dwordx4 v252, s[44:45] offset:3072\n" \
"v_mfma_f32_16x16x32_bf16 v[176:179], a[228:231], v[56:59], v[176:179]\n" \
"v_xor_b32 v206, 64, v205\n" \
"v_lshrrev_b32 v216, 2, v216\n" \
"v_mfma_f32_16x16x32_bf16 v[180:183], a[232:235], v[24:27], v[180:183]\n" \
"s_add_u32 s44, s44, 0x400\n" \
"s_addc_u32 s45, s45, 0\n" \
"v_lshl_add_u32 v216, v216, 9, v213\n" \
"v_mfma_f32_16x16x32_bf16 v[180:183], a[236:239], v[56:59], v[180:183]\n" \
"s_add_u32 m0, s85, 81920\n" \
"v_add_u32 v207, 135168, v216\n" \
"v_xor_b32 v208, 64, v207\n" \
"v_mfma_f32_16x16x32_bf16 v[184:187], a[240:243], v[24:27], v[184:187]\n" \
"s_and_b32 s70, s61, 1\n" \
"s_lshl_b32 s70, s70, 9\n" \
"s_add_u32 s70, s70, 139264\n" \
"v_mfma_f32_16x16x32_bf16 v[184:187], a[244:247], v[56:59], v[184:187]\n" \
"s_lshl_b32 s71, s71, 5\n" \
"v_and_b32 v213, 3, v211\n" \
"v_lshlrev_b32 v213, 3, v213\n" \
"v_and_or_b32 v213, v248, s89, v213\n" \
"v_bfe_u32 v214, v211, 4, 1\n" \
"v_mfma_f32_16x16x32_bf16 v[188:191], a[248:251], v[24:27], v[188:191]\n" \
"buffer_store_dwordx4 v[108:111], v65, s[56:59], s77 offen offset:2048 sc1\n" \
"v_mad_u32_u24 v213, v214, s90, v213\n" \
"v_lshrrev_b32 v214, 5, v211\n" \
"v_mul_u32_u24 v214, 6144, v214\n" \
"v_add3_u32 v213, v213, v214, s70\n" \
"v_xor_b32 v209, s71, v213\n" \
"v_xor_b32 v210, 32, v209\n" \
"v_mfma_f32_16x16x32_bf16 v[188:191], a[252:255], v[56:59], v[188:191]\n" \
"s_add_u32 s77, s77, 0x1000\n" \
"ds_read_b128 a[64:67], v207 offset:0\n" \
"ds_read_b64_tr_b16 a[96:97], v209 offset:2048\n" \
"v_accvgpr_read_b32 v96, a48\n" \
"v_accvgpr_read_b32 v97, a49\n" \
"v_accvgpr_read_b32 v98, a50\n" \
"v_accvgpr_read_b32 v99, a51\n" \
"v_accvgpr_read_b32 v100, a52\n" \
"v_accvgpr_read_b32 v101, a53\n" \
"v_accvgpr_read_b32 v102, a54\n" \
"v_accvgpr_read_b32 v103, a55\n" \
"v_pk_mul_f32 v[96:97], v[96:97], v[64:65] op_sel_hi:[1,0]\n" \
"v_pk_mul_f32 v[98:99], v[98:99], v[64:65] op_sel_hi:[1,0]\n" \
"v_pk_mul_f32 v[100:101], v[100:101], v[64:65] op_sel_hi:[1,0]\n" \
"v_pk_mul_f32 v[102:103], v[102:103], v[64:65] op_sel_hi:[1,0]\n" \
"v_cvt_pk_bf16_f32 v104, v96, v97\n" \
"v_cvt_pk_bf16_f32 v105, v98, v99\n" \
"v_cvt_pk_bf16_f32 v106, v100, v101\n" \
"v_cvt_pk_bf16_f32 v107, v102, v103\n" \
"v_pk_mul_f32 v[96:97], v[96:97], v[98:99]\n" \
"v_pk_fma_f32 v[96:97], v[100:101], v[102:103], v[96:97]\n" \
"v_mfma_f32_16x16x32_bf16 v[128:131], a[192:195], v[16:19], v[128:131]\n" \
"ds_read_b64_tr_b16 a[98:99], v209 offset:3072\n" \
"v_fma_f32 v66, v96, v97, v66\n" \
"v_mfma_f32_16x16x32_bf16 v[128:131], a[196:199], v[48:51], v[128:131]\n" \
"buffer_store_dwordx4 v[104:107], v65, s[56:59], s77 offen offset:0 sc1\n" \
"ds_read_b128 a[68:71], v208 offset:0\n" \
"v_accvgpr_read_b32 v96, a56\n" \
"v_accvgpr_read_b32 v97, a57\n" \
"v_accvgpr_read_b32 v98, a58\n" \
"v_accvgpr_read_b32 v99, a59\n" \
"v_accvgpr_read_b32 v100, a60\n" \
"v_accvgpr_read_b32 v101, a61\n" \
"v_accvgpr_read_b32 v102, a62\n" \
"v_accvgpr_read_b32 v103, a63\n" \
"v_pk_mul_f32 v[96:97], v[96:97], v[64:65] op_sel_hi:[1,0]\n" \
"v_pk_mul_f32 v[98:99], v[98:99], v[64:65] op_sel_hi:[1,0]\n" \
"v_pk_mul_f32 v[100:101], v[100:101], v[64:65] op_sel_hi:[1,0]\n" \
"v_pk_mul_f32 v[102:103], v[102:103], v[64:65] op_sel_hi:[1,0]\n" \
"v_cvt_pk_bf16_f32 v108, v96, v97\n" \
"v_cvt_pk_bf16_f32 v109, v98, v99\n" \
"v_cvt_pk_bf16_f32 v110, v100, v101\n" \
"v_cvt_pk_bf16_f32 v111, v102, v103\n" \
"v_pk_mul_f32 v[96:97], v[96:97], v[98:99]\n" \
"v_pk_fma_f32 v[96:97], v[100:101], v[102:103], v[96:97]\n" \
"v_fma_f32 v66, v96, v97, v66\n" \
"v_mfma_f32_16x16x32_bf16 v[132:135], a[200:203], v[16:19], v[132:135]\n" \
"buffer_store_dwordx4 v[108:111], v65, s[56:59], s77 offen offset:2048 sc1\n" \
"ds_read_b64_tr_b16 a[100:101], v209 offset:14336\n" \
"buffer_load_dwordx4 v246, s[12:15], s46 offen lds\n" \
"ds_read_b64_tr_b16 a[102:103], v209 offset:15360\n" \
"v_mfma_f32_16x16x32_bf16 v[132:135], a[204:207], v[48:51], v[132:135]\n" \
"s_add_u32 s46, s46, 0x400\n" \
"ds_read_b128 a[72:75], v205 offset:0\n" \
"v_cmp_class_f32 s[54:55], v66, s94\n" \
"v_mfma_f32_16x16x32_bf16 v[136:139], a[208:211], v[16:19], v[136:139]\n" \
"s_add_u32 m0, s95, 0\n" \
"s_sub_u32 s77, s77, 536576\n" \
"ds_read_b64_tr_b16 a[104:105], v209 offset:0\n" \
"v_mfma_f32_16x16x32_bf16 v[136:139], a[212:215], v[48:51], v[136:139]\n" \
"s_or_b64 s[92:93], s[92:93], s[54:55]\n" \
"ds_read_b128 a[76:79], v206 offset:0\n" \
"ds_read_b64_tr_b16 a[106:107], v209 offset:1024\n" \
"v_mfma_f32_16x16x32_bf16 v[140:143], a[216:219], v[16:19], v[140:143]\n" \
"ds_read_b64_tr_b16 a[108:109], v209 offset:12288\n" \
"global_load_lds_dwordx4 v252, s[42:43] offset:0\n" \
"v_mfma_f32_16x16x32_bf16 v[140:143], a[220:223], v[48:51], v[140:143]\n" \
"ds_read_b64_tr_b16 a[110:111], v209 offset:13312\n" \
"global_load_lds_dwordx4 v252, s[42:43] offset:2048\n" \
"v_mfma_f32_16x16x32_bf16 v[144:147], a[224:227], v[16:19], v[144:147]\n" \
"ds_read_b128 a[80:83], v205 offset:4096\n" \
"ds_read_b64_tr_b16 a[112:113], v210 offset:0\n" \
"v_mfma_f32_16x16x32_bf16 v[144:147], a[228:231], v[48:51], v[144:147]\n" \
"s_add_u32 s42, s42, 0xfffffb00\n" \
"s_addc_u32 s43, s43, -1\n" \
"ds_read_b64_tr_b16 a[114:115], v210 offset:1024\n" \
"v_mfma_f32_16x16x32_bf16 v[148:151], a[232:235], v[16:19], v[148:151]\n" \
"ds_read_b64_tr_b16 a[116:117], v210 offset:12288\n" \
"global_load_lds_dwordx4 v252, s[44:45] offset:1024\n" \
"v_mfma_f32_16x16x32_bf16 v[148:151], a[236:239], v[48:51], v[148:151]\n" \
"ds_read_b128 a[84:87], v206 offset:4096\n" \
"ds_read_b64_tr_b16 a[118:119], v210 offset:13312\n" \
"v_mfma_f32_16x16x32_bf16 v[152:155], a[240:243], v[16:19], v[152:155]\n" \
"ds_read_b128 a[88:91], v205 offset:8192\n" \
"global_load_lds_dwordx4 v252, s[44:45] offset:3072\n" \
"v_mfma_f32_16x16x32_bf16 v[152:155], a[244:247], v[48:51], v[152:155]\n" \
"s_add_u32 s44, s44, 0xfffffb00\n" \
"s_addc_u32 s45, s45, -1\n" \
"ds_read_b128 a[92:95], v206 offset:8192\n" \
"v_mfma_f32_16x16x32_bf16 v[156:159], a[248:251], v[16:19], v[156:159]\n" \
"s_add_u32 m0, s85, 16384\n" \
"v_mfma_f32_16x16x32_bf16 v[156:159], a[252:255], v[48:51], v[156:159]\n" \
"s_waitcnt lgkmcnt(4)\n" \
"buffer_load_dwordx4 v246, s[12:15], s46 offen lds\n" \
"s_branch Ljoin_%=\n" \
"Lexact_%=:\n" \
"v_mov_b32 v190, 0xff800000\n" \
"v_add_u32 v191, 0x1c90080, v249\n" \
"v_cndmask_b32 v191, v249, v191, s[16:17]\n" \
"s_and_b32 s80, s61, 2\n" \
"s_lshl_b32 s80, s80, 4\n" \
"v_subrev_u32 v186, s80, v251\n" \
"v_accvgpr_read_b32 v160, a128\n" \
"v_accvgpr_read_b32 v161, a129\n" \
"v_accvgpr_read_b32 v162, a130\n" \
"v_accvgpr_read_b32 v163, a131\n" \
"v_accvgpr_read_b32 v164, a132\n" \
"v_accvgpr_read_b32 v165, a133\n" \
"v_accvgpr_read_b32 v166, a134\n" \
"v_accvgpr_read_b32 v167, a135\n" \
"v_accvgpr_read_b32 v168, a136\n" \
"v_accvgpr_read_b32 v169, a137\n" \
"v_accvgpr_read_b32 v170, a138\n" \
"v_accvgpr_read_b32 v171, a139\n" \
"v_accvgpr_read_b32 v172, a140\n" \
"v_accvgpr_read_b32 v173, a141\n" \
"v_accvgpr_read_b32 v174, a142\n" \
"v_accvgpr_read_b32 v175, a143\n" \
"s_lshl_b32 s75, s63, 2\n" \
"s_lshr_b32 s80, s61, 1\n" \
"s_add_u32 s75, s75, s80\n" \
"s_add_u32 s75, s75, 0\n" \
"s_add_u32 s76, s77, 0\n" \
"s_call_b64 s[68:69], Lex1_%=\n" \
"v_accvgpr_read_b32 v160, a144\n" \
"v_accvgpr_read_b32 v161, a145\n" \
"v_accvgpr_read_b32 v162, a146\n" \
"v_accvgpr_read_b32 v163, a147\n" \
"v_accvgpr_read_b32 v164, a148\n" \
"v_accvgpr_read_b32 v165, a149\n" \
"v_accvgpr_read_b32 v166, a150\n" \
"v_accvgpr_read_b32 v167, a151\n" \
"v_accvgpr_read_b32 v168, a152\n" \
"v_accvgpr_read_b32 v169, a153\n" \
"v_accvgpr_read_b32 v170, a154\n" \
"v_accvgpr_read_b32 v171, a155\n" \
"v_accvgpr_read_b32 v172, a156\n" \
"v_accvgpr_read_b32 v173, a157\n" \
"v_accvgpr_read_b32 v174, a158\n" \
"v_accvgpr_read_b32 v175, a159\n" \
"s_lshl_b32 s75, s63, 2\n" \
"s_lshr_b32 s80, s61, 1\n" \
"s_add_u32 s75, s75, s80\n" \
"s_add_u32 s75, s75, 2\n" \
"s_add_u32 s76, s77, 532480\n" \
"s_call_b64 s[68:69], Lex1_%=\n" \
"v_accvgpr_read_b32 v160, a160\n" \
"v_accvgpr_read_b32 v161, a161\n" \
"v_accvgpr_read_b32 v162, a162\n" \
"v_accvgpr_read_b32 v163, a163\n" \
"v_accvgpr_read_b32 v164, a164\n" \
"v_accvgpr_read_b32 v165, a165\n" \
"v_accvgpr_read_b32 v166, a166\n" \
"v_accvgpr_read_b32 v167, a167\n" \
"v_accvgpr_read_b32 v168, a168\n" \
"v_accvgpr_read_b32 v169, a169\n" \
"v_accvgpr_read_b32 v170, a170\n" \
"v_accvgpr_read_b32 v171, a171\n" \
"v_accvgpr_read_b32 v172, a172\n" \
"v_accvgpr_read_b32 v173, a173\n" \
"v_accvgpr_read_b32 v174, a174\n" \
"v_accvgpr_read_b32 v175, a175\n" \
"s_lshl_b32 s75, s63, 2\n" \
"s_sub_u32 s75, 60, s75\n" \
"s_lshr_b32 s80, s61, 1\n" \
"s_add_u32 s75, s75, s80\n" \
"s_add_u32 s75, s75, 0\n" \
"s_add_u32 s76, s78, 0\n" \
"s_call_b64 s[68:69], Lex1_%=\n" \
"v_accvgpr_read_b32 v160, a176\n" \
"v_accvgpr_read_b32 v161, a177\n" \
"v_accvgpr_read_b32 v162, a178\n" \
"v_accvgpr_read_b32 v163, a179\n" \
"v_accvgpr_read_b32 v164, a180\n" \
"v_accvgpr_read_b32 v165, a181\n" \
"v_accvgpr_read_b32 v166, a182\n" \
"v_accvgpr_read_b32 v167, a183\n" \
"v_accvgpr_read_b32 v168, a184\n" \
"v_accvgpr_read_b32 v169, a185\n" \
"v_accvgpr_read_b32 v170, a186\n" \
"v_accvgpr_read_b32 v171, a187\n" \
"v_accvgpr_read_b32 v172, a188\n" \
"v_accvgpr_read_b32 v173, a189\n" \
"v_accvgpr_read_b32 v174, a190\n" \
"v_accvgpr_read_b32 v175, a191\n" \
"s_lshl_b32 s75, s63, 2\n" \
"s_sub_u32 s75, 60, s75\n" \
"s_lshr_b32 s80, s61, 1\n" \
"s_add_u32 s75, s75, s80\n" \
"s_add_u32 s75, s75, 2\n" \
"s_add_u32 s76, s78, 532480\n" \
"s_call_b64 s[68:69], Lex1_%=\n" \
"s_branch Lexr_%=\n" \
"Lex1_%=:\n" \
"v_mov_b32 v128, 0\n" \
"v_mov_b32 v129, 0\n" \
"v_mov_b32 v130, 0\n" \
"v_mov_b32 v131, 0\n" \
"v_mov_b32 v132, 0\n" \
"v_mov_b32 v133, 0\n" \
"v_mov_b32 v134, 0\n" \
"v_mov_b32 v135, 0\n" \
"v_mov_b32 v136, 0\n" \
"v_mov_b32 v137, 0\n" \
"v_mov_b32 v138, 0\n" \
"v_mov_b32 v139, 0\n" \
"v_mov_b32 v140, 0\n" \
"v_mov_b32 v141, 0\n" \
"v_mov_b32 v142, 0\n" \
"v_mov_b32 v143, 0\n" \
"v_mov_b32 v144, 0\n" \
"v_mov_b32 v145, 0\n" \
"v_mov_b32 v146, 0\n" \
"v_mov_b32 v147, 0\n" \
"v_mov_b32 v148, 0\n" \
"v_mov_b32 v149, 0\n" \
"v_mov_b32 v150, 0\n" \
"v_mov_b32 v151, 0\n" \
"v_mov_b32 v152, 0\n" \
"v_mov_b32 v153, 0\n" \
"v_mov_b32 v154, 0\n" \
"v_mov_b32 v155, 0\n" \
"v_mov_b32 v156, 0\n" \
"v_mov_b32 v157, 0\n" \
"v_mov_b32 v158, 0\n" \
"v_mov_b32 v159, 0\n" \
"v_mov_b32 v184, 0xf149f2ca\n" \
"v_mov_b32 v185, 0\n" \
"s_mov_b32 s73, 0\n" \
"s_mul_i32 s74, s62, 557824\n" \
"Lex2_%=:\n" \
"s_add_u32 s80, s74, 4096\n" \
"buffer_load_dwordx4 a[0:3], v249, s[56:59], s74 offen offset:0 sc1\n" \
"buffer_load_dwordx4 a[4:7], v249, s[56:59], s74 offen offset:2048 sc1\n" \
"buffer_load_dwordx4 a[8:11], v249, s[56:59], s80 offen offset:0 sc1\n" \
"buffer_load_dwordx4 a[12:15], v249, s[56:59], s80 offen offset:2048 sc1\n" \
"buffer_load_dwordx4 a[16:19], v249, s[56:59], s74 offen offset:256 sc1\n" \
"buffer_load_dwordx4 a[20:23], v249, s[56:59], s74 offen offset:2304 sc1\n" \
"buffer_load_dwordx4 a[24:27], v249, s[56:59], s80 offen offset:256 sc1\n" \
"buffer_load_dwordx4 a[28:31], v249, s[56:59], s80 offen offset:2304 sc1\n" \
"s_add_u32 s80, s74, 17850368\n" \
"s_add_u32 s81, s80, 4096\n" \
"buffer_load_dwordx4 a[32:35], v248, s[56:59], s80 offen offset:0 sc1\n" \
"buffer_load_dwordx4 a[36:39], v248, s[56:59], s80 offen offset:1024 sc1\n" \
"buffer_load_dwordx4 a[40:43], v248, s[56:59], s80 offen offset:2048 sc1\n" \
"buffer_load_dwordx4 a[44:47], v248, s[56:59], s80 offen offset:3072 sc1\n" \
"buffer_load_dwordx4 a[48:51], v248, s[56:59], s81 offen offset:0 sc1\n" \
"buffer_load_dwordx4 a[52:55], v248, s[56:59], s81 offen offset:1024 sc1\n" \
"buffer_load_dwordx4 a[56:59], v248, s[56:59], s81 offen offset:2048 sc1\n" \
"buffer_load_dwordx4 a[60:63], v248, s[56:59], s81 offen offset:3072 sc1\n" \
"s_waitcnt vmcnt(0)\n" \
"v_mfma_f32_16x16x32_bf16 v[176:179], a[0:3], v[160:163], 0\n" \
"v_mfma_f32_16x16x32_bf16 v[176:179], a[4:7], v[164:167], v[176:179]\n" \
"v_mfma_f32_16x16x32_bf16 v[176:179], a[8:11], v[168:171], v[176:179]\n" \
"v_mfma_f32_16x16x32_bf16 v[176:179], a[12:15], v[172:175], v[176:179]\n" \
"v_mfma_f32_16x16x32_bf16 v[180:183], a[16:19], v[160:163], 0\n" \
"v_mfma_f32_16x16x32_bf16 v[180:183], a[20:23], v[164:167], v[180:183]\n" \
"v_mfma_f32_16x16x32_bf16 v[180:183], a[24:27], v[168:171], v[180:183]\n" \
"v_mfma_f32_16x16x32_bf16 v[180:183], a[28:31], v[172:175], v[180:183]\n" \
"s_nop 7\n" \
"s_nop 1\n" \
"s_cmp_eq_u32 s73, s75\n" \
"s_cbranch_scc0 Lex3_%=\n" \
"v_cmp_gt_i32 vcc, 0, v186\n" \
"s_nop 1\n" \
"v_cndmask_b32 v176, v176, v190, vcc\n" \
"v_cmp_gt_i32 vcc, 1, v186\n" \
"s_nop 1\n" \
"v_cndmask_b32 v177, v177, v190, vcc\n" \
"v_cmp_gt_i32 vcc, 2, v186\n" \
"s_nop 1\n" \
"v_cndmask_b32 v178, v178, v190, vcc\n" \
"v_cmp_gt_i32 vcc, 3, v186\n" \
"s_nop 1\n" \
"v_cndmask_b32 v179, v179, v190, vcc\n" \
"v_cmp_gt_i32 vcc, 16, v186\n" \
"s_nop 1\n" \
"v_cndmask_b32 v180, v180, v190, vcc\n" \
"v_cmp_gt_i32 vcc, 17, v186\n" \
"s_nop 1\n" \
"v_cndmask_b32 v181, v181, v190, vcc\n" \
"v_cmp_gt_i32 vcc, 18, v186\n" \
"s_nop 1\n" \
"v_cndmask_b32 v182, v182, v190, vcc\n" \
"v_cmp_gt_i32 vcc, 19, v186\n" \
"s_nop 1\n" \
"v_cndmask_b32 v183, v183, v190, vcc\n" \
"Lex3_%=:\n" \
"v_max3_f32 v187, v176, v177, v178\n" \
"v_max3_f32 v187, v187, v179, v180\n" \
"v_max3_f32 v187, v187, v181, v182\n" \
"v_max3_f32 v187, v187, v183, v184\n" \
"v_mov_b32 v188, v187\n" \
"s_nop 1\n" \
"v_permlane16_swap_b32 v188, v187\n" \
"v_max_f32 v187, v187, v188\n" \
"v_mov_b32 v188, v187\n" \
"s_nop 1\n" \
"v_permlane32_swap_b32 v188, v187\n" \
"v_max_f32 v187, v187, v188\n" \
"v_sub_f32 v189, v184, v187\n" \
"v_exp_f32 v189, v189\n" \
"v_mov_b32 v184, v187\n" \
"v_sub_f32 v176, v176, v187\n" \
"v_sub_f32 v177, v177, v187\n" \
"v_sub_f32 v178, v178, v187\n" \
"v_sub_f32 v179, v179, v187\n" \
"v_sub_f32 v180, v180, v187\n" \
"v_sub_f32 v181, v181, v187\n" \
"v_sub_f32 v182, v182, v187\n" \
"v_sub_f32 v183, v183, v187\n" \
"v_exp_f32 v176, v176\n" \
"v_exp_f32 v177, v177\n" \
"v_exp_f32 v178, v178\n" \
"v_exp_f32 v179, v179\n" \
"v_exp_f32 v180, v180\n" \
"v_exp_f32 v181, v181\n" \
"v_exp_f32 v182, v182\n" \
"v_exp_f32 v183, v183\n" \
"v_mul_f32 v185, v185, v189\n" \
"v_add_f32 v185, v185, v176\n" \
"v_add_f32 v185, v185, v177\n" \
"v_add_f32 v185, v185, v178\n" \
"v_add_f32 v185, v185, v179\n" \
"v_add_f32 v185, v185, v180\n" \
"v_add_f32 v185, v185, v181\n" \
"v_add_f32 v185, v185, v182\n" \
"v_add_f32 v185, v185, v183\n" \
"v_cvt_pk_bf16_f32 v112, v176, v177\n" \
"v_cvt_pk_bf16_f32 v113, v178, v179\n" \
"v_cvt_pk_bf16_f32 v114, v180, v181\n" \
"v_cvt_pk_bf16_f32 v115, v182, v183\n" \
"v_mul_f32 v128, v128, v189\n" \
"v_mul_f32 v129, v129, v189\n" \
"v_mul_f32 v130, v130, v189\n" \
"v_mul_f32 v131, v131, v189\n" \
"v_mul_f32 v132, v132, v189\n" \
"v_mul_f32 v133, v133, v189\n" \
"v_mul_f32 v134, v134, v189\n" \
"v_mul_f32 v135, v135, v189\n" \
"v_mul_f32 v136, v136, v189\n" \
"v_mul_f32 v137, v137, v189\n" \
"v_mul_f32 v138, v138, v189\n" \
"v_mul_f32 v139, v139, v189\n" \
"v_mul_f32 v140, v140, v189\n" \
"v_mul_f32 v141, v141, v189\n" \
"v_mul_f32 v142, v142, v189\n" \
"v_mul_f32 v143, v143, v189\n" \
"v_mul_f32 v144, v144, v189\n" \
"v_mul_f32 v145, v145, v189\n" \
"v_mul_f32 v146, v146, v189\n" \
"v_mul_f32 v147, v147, v189\n" \
"v_mul_f32 v148, v148, v189\n" \
"v_mul_f32 v149, v149, v189\n" \
"v_mul_f32 v150, v150, v189\n" \
"v_mul_f32 v151, v151, v189\n" \
"v_mul_f32 v152, v152, v189\n" \
"v_mul_f32 v153, v153, v189\n" \
"v_mul_f32 v154, v154, v189\n" \
"v_mul_f32 v155, v155, v189\n" \
"v_mul_f32 v156, v156, v189\n" \
"v_mul_f32 v157, v157, v189\n" \
"v_mul_f32 v158, v158, v189\n" \
"v_mul_f32 v159, v159, v189\n" \
"s_nop 1\n" \
"v_mfma_f32_16x16x32_bf16 v[128:131], a[32:35], v[112:115], v[128:131]\n" \
"v_mfma_f32_16x16x32_bf16 v[132:135], a[36:39], v[112:115], v[132:135]\n" \
"v_mfma_f32_16x16x32_bf16 v[136:139], a[40:43], v[112:115], v[136:139]\n" \
"v_mfma_f32_16x16x32_bf16 v[140:143], a[44:47], v[112:115], v[140:143]\n" \
"v_mfma_f32_16x16x32_bf16 v[144:147], a[48:51], v[112:115], v[144:147]\n" \
"v_mfma_f32_16x16x32_bf16 v[148:151], a[52:55], v[112:115], v[148:151]\n" \
"v_mfma_f32_16x16x32_bf16 v[152:155], a[56:59], v[112:115], v[152:155]\n" \
"v_mfma_f32_16x16x32_bf16 v[156:159], a[60:63], v[112:115], v[156:159]\n" \
"s_nop 7\n" \
"s_nop 1\n" \
"s_and_b32 s80, s73, 1\n" \
"s_mul_i32 s80, s80, 1024\n" \
"s_add_u32 s74, s74, s80\n" \
"s_add_u32 s74, s74, 8192\n" \
"s_add_u32 s73, s73, 1\n" \
"s_cmp_le_u32 s73, s75\n" \
"s_cbranch_scc1 Lex2_%=\n" \
"v_mov_b32 v187, v185\n" \
"s_nop 1\n" \
"v_permlane16_swap_b32 v187, v185\n" \
"v_add_f32 v185, v185, v187\n" \
"v_mov_b32 v187, v185\n" \
"s_nop 1\n" \
"v_permlane32_swap_b32 v187, v185\n" \
"v_add_f32 v185, v185, v187\n" \
"v_rcp_f32 v185, v185\n" \
"s_nop 0\n" \
"v_mul_f32 v128, v128, v185\n" \
"v_mul_f32 v129, v129, v185\n" \
"v_mul_f32 v130, v130, v185\n" \
"v_mul_f32 v131, v131, v185\n" \
"v_mul_f32 v132, v132, v185\n" \
"v_mul_f32 v133, v133, v185\n" \
"v_mul_f32 v134, v134, v185\n" \
"v_mul_f32 v135, v135, v185\n" \
"v_mul_f32 v136, v136, v185\n" \
"v_mul_f32 v137, v137, v185\n" \
"v_mul_f32 v138, v138, v185\n" \
"v_mul_f32 v139, v139, v185\n" \
"v_mul_f32 v140, v140, v185\n" \
"v_mul_f32 v141, v141, v185\n" \
"v_mul_f32 v142, v142, v185\n" \
"v_mul_f32 v143, v143, v185\n" \
"v_mul_f32 v144, v144, v185\n" \
"v_mul_f32 v145, v145, v185\n" \
"v_mul_f32 v146, v146, v185\n" \
"v_mul_f32 v147, v147, v185\n" \
"v_mul_f32 v148, v148, v185\n" \
"v_mul_f32 v149, v149, v185\n" \
"v_mul_f32 v150, v150, v185\n" \
"v_mul_f32 v151, v151, v185\n" \
"v_mul_f32 v152, v152, v185\n" \
"v_mul_f32 v153, v153, v185\n" \
"v_mul_f32 v154, v154, v185\n" \
"v_mul_f32 v155, v155, v185\n" \
"v_mul_f32 v156, v156, v185\n" \
"v_mul_f32 v157, v157, v185\n" \
"v_mul_f32 v158, v158, v185\n" \
"v_mul_f32 v159, v159, v185\n" \
"s_add_u32 s80, s76, 4096\n" \
"v_cvt_pk_bf16_f32 v112, v128, v129\n" \
"v_cvt_pk_bf16_f32 v113, v130, v131\n" \
"v_cvt_pk_bf16_f32 v114, v132, v133\n" \
"v_cvt_pk_bf16_f32 v115, v134, v135\n" \
"buffer_store_dwordx4 v[112:115], v191, s[56:59], s76 offen offset:0 sc1\n" \
"v_cvt_pk_bf16_f32 v116, v136, v137\n" \
"v_cvt_pk_bf16_f32 v117, v138, v139\n" \
"v_cvt_pk_bf16_f32 v118, v140, v141\n" \
"v_cvt_pk_bf16_f32 v119, v142, v143\n" \
"buffer_store_dwordx4 v[116:119], v191, s[56:59], s76 offen offset:2048 sc1\n" \
"v_cvt_pk_bf16_f32 v120, v144, v145\n" \
"v_cvt_pk_bf16_f32 v121, v146, v147\n" \
"v_cvt_pk_bf16_f32 v122, v148, v149\n" \
"v_cvt_pk_bf16_f32 v123, v150, v151\n" \
"buffer_store_dwordx4 v[120:123], v191, s[56:59], s80 offen offset:0 sc1\n" \
"v_cvt_pk_bf16_f32 v124, v152, v153\n" \
"v_cvt_pk_bf16_f32 v125, v154, v155\n" \
"v_cvt_pk_bf16_f32 v126, v156, v157\n" \
"v_cvt_pk_bf16_f32 v127, v158, v159\n" \
"buffer_store_dwordx4 v[124:127], v191, s[56:59], s80 offen offset:2048 sc1\n" \
"s_waitcnt vmcnt(0)\n" \
"s_setpc_b64 s[68:69]\n" \
"Lpl2_%=:\n" \
"s_mov_b32 s31, 0\n" \
"Lpk2_%=:\n" \
"s_sleep 2\n" \
"buffer_load_dword v220, v221, s[56:59], s81 offen sc1\n" \
"s_waitcnt vmcnt(0)\n" \
"v_cmp_ne_u32 vcc, s60, v220\n" \
"s_nop 1\n" \
"s_and_b32 vcc_hi, vcc_lo, s80\n" \
"s_cbranch_scc0 Lpr2_%=\n" \
"s_add_u32 s31, s31, 1\n" \
"s_cmp_lt_u32 s31, 0x40000\n" \
"s_cbranch_scc1 Lpk2_%=\n" \
"s_branch Lpr2_%=\n" \
"Lpl3_%=:\n" \
"s_mov_b32 s31, 0\n" \
"Lpk3_%=:\n" \
"s_sleep 2\n" \
"buffer_load_dwordx2 v[226:227], v228, s[56:59], s30 offen sc1\n" \
"s_waitcnt vmcnt(0)\n" \
"v_xor_b32 v226, v226, v227\n" \
"v_cmp_ne_u32 vcc, s60, v226\n" \
"s_nop 1\n" \
"s_and_b32 vcc_hi, vcc_lo, -1\n" \
"s_cbranch_scc0 Lpr3_%=\n" \
"s_add_u32 s31, s31, 1\n" \
"s_cmp_lt_u32 s31, 0x40000\n" \
"s_cbranch_scc1 Lpk3_%=\n" \
"s_branch Lpr3_%=\n" \
"Lpl4_%=:\n" \
"s_mov_b32 s31, 0\n" \
"Lpk4_%=:\n" \
"s_sleep 2\n" \
"buffer_load_dword v220, off, s[56:59], s79 sc1\n" \
"s_waitcnt vmcnt(0)\n" \
"v_cmp_ne_u32 vcc, s60, v220\n" \
"s_nop 1\n" \
"s_and_b32 vcc_hi, vcc_lo, 1\n" \
"s_cbranch_scc0 Lpr4_%=\n" \
"s_add_u32 s31, s31, 1\n" \
"s_cmp_lt_u32 s31, 0x40000\n" \
"s_cbranch_scc1 Lpk4_%=\n" \
"s_branch Lpr4_%=\n" \
"Lpl5_%=:\n" \
"s_mov_b32 s31, 0\n" \
"Lpk5_%=:\n" \
"s_sleep 2\n" \
"buffer_load_dword v220, v221, s[56:59], s82 offen sc1\n" \
"s_waitcnt vmcnt(0)\n" \
"v_cmp_ne_u32 vcc, s60, v220\n" \
"s_nop 1\n" \
"s_and_b32 vcc_hi, vcc_lo, 65535\n" \
"s_cbranch_scc0 Lpr5_%=\n" \
"s_add_u32 s31, s31, 1\n" \
"s_cmp_lt_u32 s31, 0x40000\n" \
"s_cbranch_scc1 Lpk5_%=\n" \
"s_branch Lpr5_%=\n" \
"Lcode_end_%=:\n" \
".p2align 12\n" \
""
#define MK_ASM_CLOBBERS "v1", "v2", "v3", "v4", "v5", "v6", "v7", "v8", "v9", "v10", "v11", "v12", "v13", "v14", "v15", "v16", "v17", "v18", "v19", "v20", "v21", "v22", "v23", "v24", "v25", "v26", "v27", "v28", "v29", "v30", "v31", "v32", "v33", "v34", "v35", "v36", "v37", "v38", "v39", "v40", "v41", "v42", "v43", "v44", "v45", "v46", "v47", "v48", "v49", "v50", "v51", "v52", "v53", "v54", "v55", "v56", "v57", "v58", "v59", "v60", "v61", "v62", "v63", "v64", "v65", "v66", "v67", "v68", "v69", "v70", "v71", "v72", "v73", "v74", "v75", "v76", "v77", "v78", "v79", "v80", "v81", "v82", "v83", "v84", "v85", "v86", "v87", "v88", "v89", "v90", "v91", "v92", "v93", "v94", "v95", "v96", "v97", "v98", "v99", "v100", "v101", "v102", "v103", "v104", "v105", "v106", "v107", "v108", "v109", "v110", "v111", "v112", "v113", "v114", "v115", "v116", "v117", "v118", "v119", "v120", "v121", "v122", "v123", "v124", "v125", "v126", "v127", "v128", "v129", "v130", "v131", "v132", "v133", "v134", "v135", "v136", "v137", "v138", "v139", "v140", "v141", "v142", "v143", "v144", "v145", "v146", "v147", "v148", "v149", "v150", "v151", "v152", "v153", "v154", "v155", "v156", "v157", "v158", "v159", "v160", "v161", "v162", "v163", "v164", "v165", "v166", "v167", "v168", "v169", "v170", "v171", "v172", "v173", "v174", "v175", "v176", "v177", "v178", "v179", "v180", "v181", "v182", "v183", "v184", "v185", "v186", "v187", "v188", "v189", "v190", "v191", "v192", "v193", "v194", "v195", "v196", "v197", "v198", "v199", "v200", "v201", "v202", "v203", "v204", "v205", "v206", "v207", "v208", "v209", "v210", "v211", "v212", "v213", "v214", "v215", "v216", "v217", "v218", "v219", "v220", "v221", "v222", "v223", "v224", "v225", "v226", "v227", "v228", "v229", "v230", "v231", "v232", "v233", "v234", "v235", "v236", "v237", "v238", "v239", "v240", "v241", "v242", "v243", "v244", "v245", "v246", "v247", "v248", "v249", "v250", "v251", "v252", "v253", "v254", "v255", "a0", "a1", "a2", "a3", "a4", "a5", "a6", "a7", "a8", "a9", "a10", "a11", "a12", "a13", "a14", "a15", "a16", "a17", "a18", "a19", "a20", "a21", "a22", "a23", "a24", "a25", "a26", "a27", "a28", "a29", "a30", "a31", "a32", "a33", "a34", "a35", "a36", "a37", "a38", "a39", "a40", "a41", "a42", "a43", "a44", "a45", "a46", "a47", "a48", "a49", "a50", "a51", "a52", "a53", "a54", "a55", "a56", "a57", "a58", "a59", "a60", "a61", "a62", "a63", "a64", "a65", "a66", "a67", "a68", "a69", "a70", "a71", "a72", "a73", "a74", "a75", "a76", "a77", "a78", "a79", "a80", "a81", "a82", "a83", "a84", "a85", "a86", "a87", "a88", "a89", "a90", "a91", "a92", "a93", "a94", "a95", "a96", "a97", "a98", "a99", "a100", "a101", "a102", "a103", "a104", "a105", "a106", "a107", "a108", "a109", "a110", "a111", "a112", "a113", "a114", "a115", "a116", "a117", "a118", "a119", "a120", "a121", "a122", "a123", "a124", "a125", "a126", "a127", "a128", "a129", "a130", "a131", "a132", "a133", "a134", "a135", "a136", "a137", "a138", "a139", "a140", "a141", "a142", "a143", "a144", "a145", "a146", "a147", "a148", "a149", "a150", "a151", "a152", "a153", "a154", "a155", "a156", "a157", "a158", "a159", "a160", "a161", "a162", "a163", "a164", "a165", "a166", "a167", "a168", "a169", "a170", "a171", "a172", "a173", "a174", "a175", "a176", "a177", "a178", "a179", "a180", "a181", "a182", "a183", "a184", "a185", "a186", "a187", "a188", "a189", "a190", "a191", "a192", "a193", "a194", "a195", "a196", "a197", "a198", "a199", "a200", "a201", "a202", "a203", "a204", "a205", "a206", "a207", "a208", "a209", "a210", "a211", "a212", "a213", "a214", "a215", "a216", "a217", "a218", "a219", "a220", "a221", "a222", "a223", "a224", "a225", "a226", "a227", "a228", "a229", "a230", "a231", "a232", "a233", "a234", "a235", "a236", "a237", "a238", "a239", "a240", "a241", "a242", "a243", "a244", "a245", "a246", "a247", "a248", "a249", "a250", "a251", "a252", "a253", "a254", "a255", "s8", "s9", "s10", "s11", "s12", "s13", "s14", "s15", "s16", "s17", "s18", "s19", "s20", "s21", "s22", "s23", "s24", "s25", "s26", "s27", "s28", "s29", "s30", "s31", "s36", "s37", "s38", "s39", "s40", "s41", "s42", "s43", "s44", "s45", "s46", "s47", "s48", "s49", "s50", "s51", "s52", "s53", "s54", "s55", "s56", "s57", "s58", "s59", "s60", "s61", "s62", "s63", "s64", "s65", "s66", "s67", "s68", "s69", "s70", "s71", "s72", "s73", "s74", "s75", "s76", "s77", "s78", "s79", "s80", "s81", "s82", "s83", "s84", "s85", "s86", "s87", "s88", "s89", "s90", "s91", "s92", "s93", "s94", "s95", "vcc", "scc", "memory"
namespace mk {
constexpr int LDS_BYTES = 163840;
struct Args { const void* in[9]; void* out; void* ws; };
__global__ __launch_bounds__(256, 1) void mega_k(const Args a) {
    extern __shared__ __attribute__((aligned(16))) unsigned char shm[];
    const unsigned ldsb = (unsigned)(size_t)(LAS unsigned char*)shm;
    asm volatile(MK_ASM_BODY
                 :
                 : "s"(__builtin_amdgcn_kernarg_segment_ptr()), "s"(blockIdx.x), "v"(threadIdx.x), "s"(ldsb)
                 : MK_ASM_CLOBBERS);
}
}
extern "C" void kernel_launch(void* const* d_in, const int* in_sizes, int n_in, void* d_out, int out_size, void* d_ws, size_t ws_size, hipStream_t stream) {
    (void)hipFuncSetAttribute((const void*)mk::mega_k, hipFuncAttributeMaxDynamicSharedMemorySize, mk::LDS_BYTES);
    mk::Args a;
    for (int i = 0; i < 9; ++i) a.in[i] = d_in[i];
    a.out = d_out; a.ws = (char*)d_ws + ((ws_size - WS_END) & ~(size_t)0x1fffff);
    mk::mega_k<<<256, 256, mk::LDS_BYTES, stream>>>(a);
}
```

```cpp
#include <hip/hip_runtime.h>
#include <stdint.h>
#define LAS __attribute__((address_space(3)))
constexpr size_t LAY_TS = 17408, LAY_HS = 557824, LAY_CRS = 266240, LAY_ORS = 69632, WS_K = 0, WS_V = 17850368, WS_CTX = 35700736, WS_OB = 52740096, WS_FL = 65585152, LAY_EXD = 29950080, WS_END = 87147648;
#define MK_ASM_BODY \
"s_load_dwordx16 s[8:23], %0, 0x0\n" \
"s_load_dwordx4 s[24:27], %0, 0x40\n" \
"s_load_dwordx2 s[28:29], %0, 0x50\n" \
"v_readfirstlane_b32 s61, %2\n" \
"s_lshr_b32 s61, s61, 6\n" \
"v_mbcnt_lo_u32_b32 v52, -1, 0\n" \
"v_mbcnt_hi_u32_b32 v52, -1, v52\n" \
"v_lshlrev_b32 v248, 4, v52\n" \
"s_getpc_b64 s[64:65]\n" \
"Lpc_%=:\n" \
"s_lshr_b32 s70, %1, 3\n" \
"s_lshl_b32 s70, s70, 2\n" \
"s_add_u32 s70, s70, s61\n" \
"s_and_b32 s71, s70, 127\n" \
"s_lshl_b32 s71, s71, 10\n" \
"v_lshl_add_u32 v53, v248, 0, s71\n" \
"s_mov_b32 s71, Lcode_end_%=-Lpc_%=-4\n" \
"v_min_u32 v53, s71, v53\n" \
"global_load_dword v253, v53, s[64:65]\n" \
"s_and_b32 s62, %1, 7\n" \
"s_lshl_b32 s62, s62, 2\n" \
"s_lshr_b32 s70, %1, 6\n" \
"s_add_u32 s62, s62, s70\n" \
"s_bfe_u32 s63, %1, 0x30003\n" \
"s_lshl_b32 s87, s61, 13\n" \
"s_lshl_b32 s95, s61, 12\n" \
"v_bfe_u32 v53, v52, 4, 1\n" \
"v_bfe_u32 v54, v52, 2, 2\n" \
"v_lshl_add_u32 v54, v53, 3, v54\n" \
"s_lshl_b32 s71, s61, 4\n" \
"v_add_u32 v54, s71, v54\n" \
"v_lshlrev_b32 v54, 13, v54\n" \
"v_lshlrev_b32 v53, 1, v53\n" \
"v_and_b32 v55, 3, v52\n" \
"v_xor_b32 v53, v53, v55\n" \
"v_lshl_add_u32 v54, v53, 4, v54\n" \
"v_lshrrev_b32 v53, 5, v52\n" \
"v_lshl_add_u32 v54, v53, 6, v54\n" \
"v_add_u32 v40, 4096, v54\n" \
"v_add_u32 v41, 3072, v54\n" \
"v_add_u32 v42, 2176, v54\n" \
"v_add_u32 v43, 1152, v54\n" \
"v_add_u32 v44, 32768, v54\n" \
"v_add_u32 v45, 31744, v54\n" \
"v_add_u32 v46, 30848, v54\n" \
"v_add_u32 v47, 29824, v54\n" \
"v_lshrrev_b32 v55, 3, v52\n" \
"v_and_b32 v56, 7, v52\n" \
"v_xor_b32 v56, v56, v55\n" \
"v_lshlrev_b32 v56, 4, v56\n" \
"v_lshl_add_u32 v57, v55, 10, v56\n" \
"v_add_u32 v32, 528384, v57\n" \
"v_add_u32 v33, 535552, v57\n" \
"v_add_u32 v34, 542720, v57\n" \
"v_add_u32 v35, 549888, v57\n" \
"v_add_u32 v36, 0, v57\n" \
"v_add_u32 v37, 7168, v57\n" \
"v_add_u32 v38, 14336, v57\n" \
"v_add_u32 v39, 21504, v57\n" \
"s_lshl_b32 s88, s62, 8\n" \
"s_lshl_b32 s89, s63, 18\n" \
"s_lshl_b32 s70, s63, 16\n" \
"s_and_b32 s71, s87, 0x4000\n" \
"s_lshl_b32 s71, s71, 1\n" \
"s_add_u32 s70, s70, s71\n" \
"s_andn2_b32 s71, 1, s61\n" \
"s_lshl_b32 s71, s71, 20\n" \
"s_add_u32 s70, s70, s71\n" \
"s_lshl_b32 s66, %1, 2\n" \
"s_add_u32 s66, s66, s61\n" \
"s_lshl_b32 s66, s66, 10\n" \
"s_lshr_b32 s67, %1, 3\n" \
"s_lshl_b32 s67, s67, 2\n" \
"s_add_u32 s67, s67, s61\n" \
"s_lshl_b32 s67, s67, 13\n" \
"v_lshrrev_b32 v63, 6, v248\n" \
"v_lshlrev_b32 v63, 8, v63\n" \
"v_and_or_b32 v63, v248, 48, v63\n" \
"s_lshl_b32 s64, s67, 1\n" \
"v_lshl_add_u32 v63, v63, 2, s64\n" \
"s_and_b32 s64, %1, 7\n" \
"s_lshl_b32 s64, s64, 10\n" \
"s_add_u32 s67, s67, s64\n" \
"s_add_u32 s73, s63, s62\n" \
"s_and_b32 s73, s73, 1\n" \
"s_mov_b32 s74, 128\n" \
"s_cmp_lg_u32 s73, 0\n" \
"s_cselect_b64 exec, -1, 0\n" \
"v_swap_b32 v32, v40\n" \
"v_swap_b32 v33, v41\n" \
"v_swap_b32 v34, v42\n" \
"v_swap_b32 v35, v43\n" \
"v_swap_b32 v36, v44\n" \
"v_swap_b32 v37, v45\n" \
"v_swap_b32 v38, v46\n" \
"v_swap_b32 v39, v47\n" \
"s_mov_b64 exec, -1\n" \
"s_cselect_b32 s76, s70, s88\n" \
"s_cselect_b32 s77, s88, s70\n" \
"s_cselect_b32 s75, 524288, s74\n" \
"s_cselect_b32 s74, s74, 524288\n" \
"s_waitcnt lgkmcnt(0)\n" \
"s_cselect_b64 s[52:53], s[10:11], s[14:15]\n" \
"s_cselect_b64 s[54:55], s[14:15], s[10:11]\n" \
"s_cselect_b64 s[50:51], s[10:11], s[16:17]\n" \
"s_cselect_b64 s[40:41], s[16:17], s[10:11]\n" \
"s_add_u32 s52, s52, s76\n" \
"s_addc_u32 s53, s53, 0\n" \
"s_add_u32 s50, s50, s76\n" \
"s_addc_u32 s51, s51, 0\n" \
"s_add_u32 m0, s87, 36864\n" \
"s_nop 0\n" \
"global_load_lds_dwordx4 v40, s[52:53] offset:-4096\n" \
"global_load_lds_dwordx4 v41, s[50:51] offset:-3072\n" \
"global_load_lds_dwordx4 v42, s[52:53] offset:-2048\n" \
"global_load_lds_dwordx4 v43, s[50:51] offset:-1024\n" \
"global_load_lds_dwordx4 v44, s[52:53] offset:0\n" \
"global_load_lds_dwordx4 v45, s[50:51] offset:1024\n" \
"global_load_lds_dwordx4 v46, s[52:53] offset:2048\n" \
"global_load_lds_dwordx4 v47, s[50:51] offset:3072\n" \
"s_add_u32 s52, s52, s74\n" \
"s_addc_u32 s53, s53, 0\n" \
"s_add_u32 s50, s50, s74\n" \
"s_addc_u32 s51, s51, 0\n" \
"s_add_u32 s54, s54, s77\n" \
"s_addc_u32 s55, s55, 0\n" \
"s_add_u32 s40, s40, s77\n" \
"s_addc_u32 s41, s41, 0\n" \
"s_add_u32 m0, s87, 4096\n" \
"s_nop 0\n" \
"global_load_lds_dwordx4 v32, s[54:55] offset:-4096\n" \
"global_load_lds_dwordx4 v33, s[40:41] offset:-3072\n" \
"global_load_lds_dwordx4 v34, s[54:55] offset:-2048\n" \
"global_load_lds_dwordx4 v35, s[40:41] offset:-1024\n" \
"global_load_lds_dwordx4 v36, s[54:55] offset:0\n" \
"global_load_lds_dwordx4 v37, s[40:41] offset:1024\n" \
"global_load_lds_dwordx4 v38, s[54:55] offset:2048\n" \
"global_load_lds_dwordx4 v39, s[40:41] offset:3072\n" \
"s_add_u32 s54, s54, s75\n" \
"s_addc_u32 s55, s55, 0\n" \
"s_add_u32 s40, s40, s75\n" \
"s_addc_u32 s41, s41, 0\n" \
"s_mov_b64 s[56:57], s[28:29]\n" \
"s_and_b32 s57, s57, 0xffff\n" \
"s_mov_b32 s58, 87147648\n" \
"s_mov_b32 s59, 0x20000\n" \
"v_add_u32 v228, 4096, v54\n" \
"v_add_u32 v229, 3200, v54\n" \
"v_lshl_add_u32 v57, v55, 11, v56\n" \
"v_add_u32 v224, 0, v57\n" \
"v_add_u32 v225, 15360, v57\n" \
"v_add_u32 v226, 129024, v57\n" \
"v_add_u32 v227, 144384, v57\n" \
"s_lshl_b32 s70, s61, 15\n" \
"s_add_u32 s70, s70, 0\n" \
"s_add_u32 s71, s70, s89\n" \
"s_add_u32 s71, s71, 4096\n" \
"s_add_u32 s48, s8, s71\n" \
"s_addc_u32 s49, s9, 0\n" \
"s_sub_u32 s71, s70, s89\n" \
"s_add_u32 s71, s71, 3932160\n" \
"s_add_u32 s46, s8, s71\n" \
"s_addc_u32 s47, s9, 0\n" \
"s_add_u32 s70, s88, 0\n" \
"s_add_u32 s44, s12, s70\n" \
"s_addc_u32 s45, s13, 0\n" \
"s_add_u32 s42, s44, 30720\n" \
"s_addc_u32 s43, s45, 0\n" \
"s_lshr_b32 s70, s28, 31\n" \
"s_lshl_b32 s70, s70, 30\n" \
"s_sub_u32 s28, s28, s70\n" \
"v_add_u32 v250, s70, v248\n" \
"v_lshlrev_b32 v245, 2, v52\n" \
"v_mov_b32 v254, 0x30000\n" \
"v_lshrrev_b32 v55, 4, v52\n" \
"v_and_b32 v249, 0xf0, v248\n" \
"v_lshl_add_u32 v249, v55, 9, v249\n" \
"v_and_b32 v53, 15, v52\n" \
"s_lshl_b32 s70, s61, 4\n" \
"v_lshlrev_b32 v54, 2, v55\n" \
"v_sub_u32 v251, v53, v54\n" \
"v_add_u32 v251, s70, v251\n" \
"v_and_b32 v54, 7, v52\n" \
"v_xor_b32 v54, v54, v55\n" \
"v_lshlrev_b32 v53, 7, v53\n" \
"s_add_u32 s71, s87, 131072\n" \
"s_lshr_b32 s70, s61, 1\n" \
"s_lshl_b32 s70, s70, 14\n" \
"s_lshl_b32 s76, s73, 15\n" \
"s_add_u32 s70, s70, s76\n" \
"v_xor_b32 v56, 0, v54\n" \
"v_lshl_add_u32 v56, v56, 4, v53\n" \
"v_add_u32 v230, s71, v56\n" \
"v_add_u32 v48, s70, v56\n" \
"v_xor_b32 v56, 4, v54\n" \
"v_lshl_add_u32 v56, v56, 4, v53\n" \
"v_add_u32 v231, s71, v56\n" \
"v_add_u32 v49, s70, v56\n" \
"v_and_b32 v53, 3, v52\n" \
"v_lshlrev_b32 v53, 3, v53\n" \
"v_bfe_u32 v54, v52, 2, 2\n" \
"v_lshl_add_u32 v53, v54, 6, v53\n" \
"v_bfe_u32 v54, v52, 4, 1\n" \
"v_lshl_add_u32 v53, v54, 8, v53\n" \
"v_lshl_add_u32 v53, v54, 5, v53\n" \
"v_lshrrev_b32 v54, 5, v52\n" \
"s_and_b32 s70, s61, 1\n" \
"s_lshl_b32 s70, s70, 11\n" \
"s_xor_b32 s76, s76, 0x8000\n" \
"s_add_u32 s70, s70, s76\n" \
"v_lshl_add_u32 v56, v54, 13, v53\n" \
"v_add_u32 v50, s70, v56\n" \
"v_xor_b32 v51, 32, v50\n" \
"v_lshl_add_u32 v232, v54, 12, v53\n" \
"v_xor_b32 v233, 32, v232\n" \
"s_mov_b64 exec, 0\n" \
"s_cmp_eq_u32 s61, 0\n" \
"s_cbranch_scc1 Lst0x0_%=\n" \
"s_cmp_eq_u32 s61, 1\n" \
"s_cbranch_scc1 Lst0x1_%=\n" \
"s_cmp_eq_u32 s61, 2\n" \
"s_cbranch_scc1 Lst0x2_%=\n" \
"s_branch Lst0x3_%=\n" \
"Lcb0_%=:\n" \
"s_mov_b64 exec, -1\n" \
"s_add_u32 m0, s87, 135168\n" \
"s_nop 0\n" \
"global_load_lds_dwordx4 v40, s[52:53] offset:-4096\n" \
"global_load_lds_dwordx4 v41, s[50:51] offset:-3072\n" \
"global_load_lds_dwordx4 v42, s[52:53] offset:-2048\n" \
"global_load_lds_dwordx4 v43, s[50:51] offset:-1024\n" \
"global_load_lds_dwordx4 v44, s[52:53] offset:0\n" \
"global_load_lds_dwordx4 v45, s[50:51] offset:1024\n" \
"global_load_lds_dwordx4 v46, s[52:53] offset:2048\n" \
"global_load_lds_dwordx4 v47, s[50:51] offset:3072\n" \
"s_add_u32 s52, s52, s74\n" \
"s_addc_u32 s53, s53, 0\n" \
"s_add_u32 s50, s50, s74\n" \
"s_addc_u32 s51, s51, 0\n" \
"s_waitcnt vmcnt(8)\n" \
"s_barrier\n" \
"ds_read_b128 v[64:67], v48 offset:0\n" \
"ds_read_b64_tr_b16 v[128:129], v50 offset:0\n" \
"ds_read_b64_tr_b16 v[130:131], v50 offset:4096\n" \
"ds_read_b128 v[68:71], v49 offset:0\n" \
"ds_read_b64_tr_b16 v[132:133], v50 offset:16384\n" \
"ds_read_b64_tr_b16 v[134:135], v50 offset:20480\n" \
"ds_read_b128 v[72:75], v48 offset:2048\n" \
"ds_read_b64_tr_b16 v[136:137], v51 offset:0\n" \
"ds_read_b64_tr_b16 v[138:139], v51 offset:4096\n" \
"ds_read_b128 v[76:79], v49 offset:2048\n" \
"ds_read_b64_tr_b16 v[140:141], v51 offset:16384\n" \
"ds_read_b64_tr_b16 v[142:143], v51 offset:20480\n" \
"ds_read_b64_tr_b16 v[144:145], v50 offset:512\n" \
"ds_read_b64_tr_b16 v[146:147], v50 offset:4608\n" \
"ds_read_b64_tr_b16 v[148:149], v50 offset:16896\n" \
"ds_read_b64_tr_b16 v[150:151], v50 offset:20992\n" \
"ds_read_b64_tr_b16 v[152:153], v51 offset:512\n" \
"ds_read_b64_tr_b16 v[154:155], v51 offset:4608\n" \
"ds_read_b64_tr_b16 v[156:157], v51 offset:16896\n" \
"ds_read_b64_tr_b16 v[158:159], v51 offset:20992\n" \
"s_waitcnt lgkmcnt(14)\n" \
"v_mfma_f32_16x16x32_bf16 a[0:3], v[128:131], v[64:67], 0\n" \
"ds_read_b128 v[120:123], v48 offset:14336\n" \
"s_add_u32 m0, s87, 69632\n" \
"v_mfma_f32_16x16x32_bf16 a[0:3], v[132:135], v[68:71], a[0:3]\n" \
"s_waitcnt lgkmcnt(9)\n" \
"ds_read_b128 v[80:83], v48 offset:4096\n" \
"global_load_lds_dwordx4 v32, s[54:55] offset:-4096\n" \
"v_mfma_f32_16x16x32_bf16 a[4:7], v[136:139], v[64:67], 0\n" \
"ds_read_b128 v[124:127], v49 offset:14336\n" \
"v_mfma_f32_16x16x32_bf16 a[4:7], v[140:143], v[68:71], a[4:7]\n" \
"s_waitcnt lgkmcnt(7)\n" \
"ds_read_b128 v[84:87], v49 offset:4096\n" \
"v_mfma_f32_16x16x32_bf16 a[8:11], v[144:147], v[64:67], 0\n" \
"ds_read_b64_tr_b16 v[160:161], v50 offset:1024\n" \
"global_load_lds_dwordx4 v33, s[40:41] offset:-3072\n" \
"v_mfma_f32_16x16x32_bf16 a[8:11], v[148:151], v[68:71], a[8:11]\n" \
"s_waitcnt lgkmcnt(4)\n" \
"ds_read_b128 v[88:91], v48 offset:6144\n" \
"v_mfma_f32_16x16x32_bf16 a[12:15], v[152:155], v[64:67], 0\n" \
"ds_read_b64_tr_b16 v[162:163], v50 offset:5120\n" \
"v_mfma_f32_16x16x32_bf16 a[12:15], v[156:159], v[68:71], a[12:15]\n" \
"ds_read_b128 v[92:95], v49 offset:6144\n" \
"global_load_lds_dwordx4 v34, s[54:55] offset:-2048\n" \
"v_mfma_f32_16x16x32_bf16 a[44:47], v[152:155], v[72:75], 0\n" \
"ds_read_b64_tr_b16 v[164:165], v50 offset:17408\n" \
"v_mfma_f32_16x16x32_bf16 a[44:47], v[156:159], v[76:79], a[44:47]\n" \
"ds_read_b128 v[96:99], v48 offset:8192\n" \
"v_mfma_f32_16x16x32_bf16 a[40:43], v[144:147], v[72:75], 0\n" \
"ds_read_b64_tr_b16 v[166:167], v50 offset:21504\n" \
"global_load_lds_dwordx4 v35, s[40:41] offset:-1024\n" \
"v_mfma_f32_16x16x32_bf16 a[40:43], v[148:151], v[76:79], a[40:43]\n" \
"ds_read_b128 v[100:103], v49 offset:8192\n" \
"v_mfma_f32_16x16x32_bf16 a[36:39], v[136:139], v[72:75], 0\n" \
"ds_read_b64_tr_b16 v[168:169], v51 offset:1024\n" \
"v_mfma_f32_16x16x32_bf16 a[36:39], v[140:143], v[76:79], a[36:39]\n" \
"ds_read_b128 v[104:107], v48 offset:10240\n" \
"global_load_lds_dwordx4 v36, s[54:55] offset:0\n" \
"v_mfma_f32_16x16x32_bf16 a[32:35], v[128:131], v[72:75], 0\n" \
"ds_read_b64_tr_b16 v[170:171], v51 offset:5120\n" \
"v_mfma_f32_16x16x32_bf16 a[32:35], v[132:135], v[76:79], a[32:35]\n" \
"s_waitcnt lgkmcnt(4)\n" \
"ds_read_b128 v[108:111], v49 offset:10240\n" \
"v_mfma_f32_16x16x32_bf16 a[64:67], v[128:131], v[80:83], 0\n" \
"ds_read_b64_tr_b16 v[172:173], v51 offset:17408\n" \
"global_load_lds_dwordx4 v37, s[40:41] offset:1024\n" \
"v_mfma_f32_16x16x32_bf16 a[64:67], v[132:135], v[84:87], a[64:67]\n" \
"ds_read_b128 v[112:115], v48 offset:12288\n" \
"v_mfma_f32_16x16x32_bf16 a[68:71], v[136:139], v[80:83], 0\n" \
"ds_read_b64_tr_b16 v[174:175], v51 offset:21504\n" \
"v_mfma_f32_16x16x32_bf16 a[68:71], v[140:143], v[84:87], a[68:71]\n" \
"ds_read_b128 v[116:119], v49 offset:12288\n" \
"global_load_lds_dwordx4 v38, s[54:55] offset:2048\n" \
"v_mfma_f32_16x16x32_bf16 a[72:75], v[144:147], v[80:83], 0\n" \
"ds_read_b64_tr_b16 v[176:177], v50 offset:1536\n" \
"v_mfma_f32_16x16x32_bf16 a[72:75], v[148:151], v[84:87], a[72:75]\n" \
"global_load_lds_dwordx4 v39, s[40:41] offset:3072\n" \
"v_mfma_f32_16x16x32_bf16 a[76:79], v[152:155], v[80:83], 0\n" \
"s_add_u32 s54, s54, s75\n" \
"s_addc_u32 s55, s55, 0\n" \
"ds_read_b64_tr_b16 v[178:179], v50 offset:5632\n" \
"v_mfma_f32_16x16x32_bf16 a[76:79], v[156:159], v[84:87], a[76:79]\n" \
"s_add_u32 s40, s40, s75\n" \
"s_addc_u32 s41, s41, 0\n" \
"s_mov_b32 s90, 65593344\n" \
"v_mfma_f32_16x16x32_bf16 a[108:111], v[152:155], v[88:91], 0\n" \
"ds_read_b64_tr_b16 v[180:181], v50 offset:17920\n" \
"v_mfma_f32_16x16x32_bf16 a[108:111], v[156:159], v[92:95], a[108:111]\n" \
"buffer_load_dword v244, off, s[56:59], s90 sc1\n" \
"v_mfma_f32_16x16x32_bf16 a[104:107], v[144:147], v[88:91], 0\n" \
"ds_read_b64_tr_b16 v[182:183], v50 offset:22016\n" \
"v_mfma_f32_16x16x32_bf16 a[104:107], v[148:151], v[92:95], a[104:107]\n" \
"s_add_u32 m0, s87, 102400\n" \
"v_mfma_f32_16x16x32_bf16 a[100:103], v[136:139], v[88:91], 0\n" \
"ds_read_b64_tr_b16 v[184:185], v51 offset:1536\n" \
"global_load_lds_dwordx4 v40, s[52:53] offset:-4096\n" \
"v_mfma_f32_16x16x32_bf16 a[100:103], v[140:143], v[92:95], a[100:103]\n" \
"v_mfma_f32_16x16x32_bf16 a[96:99], v[128:131], v[88:91], 0\n" \
"ds_read_b64_tr_b16 v[186:187], v51 offset:5632\n" \
"v_mfma_f32_16x16x32_bf16 a[96:99], v[132:135], v[92:95], a[96:99]\n" \
"s_waitcnt lgkmcnt(2)\n" \
"global_load_lds_dwordx4 v41, s[50:51] offset:-3072\n" \
"v_mfma_f32_16x16x32_bf16 a[128:131], v[128:131], v[96:99], 0\n" \
"ds_read_b64_tr_b16 v[188:189], v51 offset:17920\n" \
"v_mfma_f32_16x16x32_bf16 a[128:131], v[132:135], v[100:103], a[128:131]\n" \
"v_mfma_f32_16x16x32_bf16 a[132:135], v[136:139], v[96:99], 0\n" \
"ds_read_b64_tr_b16 v[190:191], v51 offset:22016\n" \
"global_load_lds_dwordx4 v42, s[52:53] offset:-2048\n" \
"v_mfma_f32_16x16x32_bf16 a[132:135], v[140:143], v[100:103], a[132:135]\n" \
"v_mfma_f32_16x16x32_bf16 a[136:139], v[144:147], v[96:99], 0\n" \
"v_mfma_f32_16x16x32_bf16 a[136:139], v[148:151], v[100:103], a[136:139]\n" \
"global_load_lds_dwordx4 v43, s[50:51] offset:-1024\n" \
"v_mfma_f32_16x16x32_bf16 a[140:143], v[152:155], v[96:99], 0\n" \
"v_mfma_f32_16x16x32_bf16 a[140:143], v[156:159], v[100:103], a[140:143]\n" \
"global_load_lds_dwordx4 v44, s[52:53] offset:0\n" \
"v_mfma_f32_16x16x32_bf16 a[172:175], v[152:155], v[104:107], 0\n" \
"s_mul_i32 s76, s73, 0x8000\n" \
"s_add_u32 s77, s76, 0x10000\n" \
"s_sub_u32 s78, 0x18000, s76\n" \
"v_mfma_f32_16x16x32_bf16 a[172:175], v[156:159], v[108:111], a[172:175]\n" \
"v_add_u32 v48, s77, v48\n" \
"v_add_u32 v49, s77, v49\n" \
"v_mfma_f32_16x16x32_bf16 a[168:171], v[144:147], v[104:107], 0\n" \
"v_add_u32 v50, s78, v50\n" \
"v_add_u32 v51, s78, v51\n" \
"v_mfma_f32_16x16x32_bf16 a[168:171], v[148:151], v[108:111], a[168:171]\n" \
"global_load_lds_dwordx4 v45, s[50:51] offset:1024\n" \
"v_mfma_f32_16x16x32_bf16 a[164:167], v[136:139], v[104:107], 0\n" \
"v_mfma_f32_16x16x32_bf16 a[164:167], v[140:143], v[108:111], a[164:167]\n" \
"v_mfma_f32_16x16x32_bf16 a[160:163], v[128:131], v[104:107], 0\n" \
"global_load_lds_dwordx4 v46, s[52:53] offset:2048\n" \
"v_mfma_f32_16x16x32_bf16 a[160:163], v[132:135], v[108:111], a[160:163]\n" \
"v_mfma_f32_16x16x32_bf16 a[192:195], v[128:131], v[112:115], 0\n" \
"v_mfma_f32_16x16x32_bf16 a[192:195], v[132:135], v[116:119], a[192:195]\n" \
"global_load_lds_dwordx4 v47, s[50:51] offset:3072\n" \
"v_mfma_f32_16x16x32_bf16 a[196:199], v[136:139], v[112:115], 0\n" \
"s_add_u32 s52, s52, s74\n" \
"s_addc_u32 s53, s53, 0\n" \
"v_mfma_f32_16x16x32_bf16 a[196:199], v[140:143], v[116:119], a[196:199]\n" \
"s_add_u32 s50, s50, s74\n" \
"s_addc_u32 s51, s51, 0\n" \
"v_mfma_f32_16x16x32_bf16 a[200:203], v[144:147], v[112:115], 0\n" \
"v_mfma_f32_16x16x32_bf16 a[200:203], v[148:151], v[116:119], a[200:203]\n" \
"v_mfma_f32_16x16x32_bf16 a[204:207], v[152:155], v[112:115], 0\n" \
"v_mfma_f32_16x16x32_bf16 a[204:207], v[156:159], v[116:119], a[204:207]\n" \
"v_mfma_f32_16x16x32_bf16 a[236:239], v[152:155], v[120:123], 0\n" \
"v_mfma_f32_16x16x32_bf16 a[236:239], v[156:159], v[124:127], a[236:239]\n" \
"v_mfma_f32_16x16x32_bf16 a[232:235], v[144:147], v[120:123], 0\n" \
"v_mfma_f32_16x16x32_bf16 a[232:235], v[148:151], v[124:127], a[232:235]\n" \
"v_mfma_f32_16x16x32_bf16 a[228:231], v[136:139], v[120:123], 0\n" \
"v_mfma_f32_16x16x32_bf16 a[228:231], v[140:143], v[124:127], a[228:231]\n" \
"s_waitcnt vmcnt(9) lgkmcnt(0)\n" \
"v_mfma_f32_16x16x32_bf16 a[224:227], v[128:131], v[120:123], 0\n" \
"v_mfma_f32_16x16x32_bf16 a[224:227], v[132:135], v[124:127], a[224:227]\n" \
"s_barrier\n" \
"v_mfma_f32_16x16x32_bf16 a[16:19], v[64:67], v[160:163], 0\n" \
"s_add_u32 m0, s87, 36864\n" \
"ds_read_b64_tr_b16 v[128:129], v50 offset:0\n" \
"v_mfma_f32_16x16x32_bf16 a[16:19], v[68:71], v[164:167], a[16:19]\n" \
"global_load_lds_dwordx4 v32, s[54:55] offset:-4096\n" \
"v_mfma_f32_16x16x32_bf16 a[20:23], v[64:67], v[168:171], 0\n" \
"ds_read_b64_tr_b16 v[130:131], v50 offset:4096\n" \
"v_mfma_f32_16x16x32_bf16 a[20:23], v[68:71], v[172:175], a[20:23]\n" \
"v_mfma_f32_16x16x32_bf16 a[24:27], v[64:67], v[176:179], 0\n" \
"ds_read_b64_tr_b16 v[132:133], v50 offset:16384\n" \
"v_mfma_f32_16x16x32_bf16 a[24:27], v[68:71], v[180:183], a[24:27]\n" \
"global_load_lds_dwordx4 v33, s[40:41] offset:-3072\n" \
"v_mfma_f32_16x16x32_bf16 a[28:31], v[64:67], v[184:187], 0\n" \
"ds_read_b64_tr_b16 v[134:135], v50 offset:20480\n" \
"v_mfma_f32_16x16x32_bf16 a[28:31], v[68:71], v[188:191], a[28:31]\n" \
"v_mfma_f32_16x16x32_bf16 a[60:63], v[72:75], v[184:187], 0\n" \
"ds_read_b128 v[64:67], v48 offset:0\n" \
"v_mfma_f32_16x16x32_bf16 a[60:63], v[76:79], v[188:191], a[60:63]\n" \
"global_load_lds_dwordx4 v34, s[54:55] offset:-2048\n" \
"v_mfma_f32_16x16x32_bf16 a[56:59], v[72:75], v[176:179], 0\n" \
"ds_read_b64_tr_b16 v[136:137], v51 offset:0\n" \
"v_mfma_f32_16x16x32_bf16 a[56:59], v[76:79], v[180:183], a[56:59]\n" \
"v_mfma_f32_16x16x32_bf16 a[52:55], v[72:75], v[168:171], 0\n" \
"ds_read_b128 v[68:71], v49 offset:0\n" \
"v_mfma_f32_16x16x32_bf16 a[52:55], v[76:79], v[172:175], a[52:55]\n" \
"global_load_lds_dwordx4 v35, s[40:41] offset:-1024\n" \
"v_mfma_f32_16x16x32_bf16 a[48:51], v[72:75], v[160:163], 0\n" \
"ds_read_b64_tr_b16 v[138:139], v51 offset:4096\n" \
"v_mfma_f32_16x16x32_bf16 a[48:51], v[76:79], v[164:167], a[48:51]\n" \
"v_mfma_f32_16x16x32_bf16 a[80:83], v[80:83], v[160:163], 0\n" \
"ds_read_b128 v[72:75], v48 offset:2048\n" \
"v_mfma_f32_16x16x32_bf16 a[80:83], v[84:87], v[164:167], a[80:83]\n" \
"global_load_lds_dwordx4 v36, s[54:55] offset:0\n" \
"v_mfma_f32_16x16x32_bf16 a[84:87], v[80:83], v[168:171], 0\n" \
"ds_read_b64_tr_b16 v[140:141], v51 offset:16384\n" \
"v_mfma_f32_16x16x32_bf16 a[84:87], v[84:87], v[172:175], a[84:87]\n" \
"v_mfma_f32_16x16x32_bf16 a[88:91], v[80:83], v[176:179], 0\n" \
"ds_read_b128 v[76:79], v49 offset:2048\n" \
"v_mfma_f32_16x16x32_bf16 a[88:91], v[84:87], v[180:183], a[88:91]\n" \
"global_load_lds_dwordx4 v37, s[40:41] offset:1024\n" \
"v_mfma_f32_16x16x32_bf16 a[92:95], v[80:83], v[184:187], 0\n" \
"ds_read_b64_tr_b16 v[142:143], v51 offset:20480\n" \
"v_mfma_f32_16x16x32_bf16 a[92:95], v[84:87], v[188:191], a[92:95]\n" \
"v_mfma_f32_16x16x32_bf16 a[124:127], v[88:91], v[184:187], 0\n" \
"ds_read_b128 v[80:83], v48 offset:4096\n" \
"v_mfma_f32_16x16x32_bf16 a[124:127], v[92:95], v[188:191], a[124:127]\n" \
"global_load_lds_dwordx4 v38, s[54:55] offset:2048\n" \
"v_mfma_f32_16x16x32_bf16 a[120:123], v[88:91], v[176:179], 0\n" \
"ds_read_b64_tr_b16 v[144:145], v50 offset:512\n" \
"v_mfma_f32_16x16x32_bf16 a[120:123], v[92:95], v[180:183], a[120:123]\n" \
"v_mfma_f32_16x16x32_bf16 a[116:119], v[88:91], v[168:171], 0\n" \
"ds_read_b128 v[84:87], v49 offset:4096\n" \
"global_load_lds_dwordx4 v39, s[40:41] offset:3072\n" \
"v_mfma_f32_16x16x32_bf16 a[116:119], v[92:95], v[172:175], a[116:119]\n" \
"s_add_u32 s54, s54, s75\n" \
"s_addc_u32 s55, s55, 0\n" \
"v_mfma_f32_16x16x32_bf16 a[112:115], v[88:91], v[160:163], 0\n" \
"s_add_u32 s40, s40, s75\n" \
"s_addc_u32 s41, s41, 0\n" \
"ds_read_b64_tr_b16 v[146:147], v50 offset:4608\n" \
"v_mfma_f32_16x16x32_bf16 a[112:115], v[92:95], v[164:167], a[112:115]\n" \
"s_add_u32 s68, s66, 0x0\n" \
"s_add_u32 s64, s10, s68\n" \
"s_addc_u32 s65, s11, 0\n" \
"v_mfma_f32_16x16x32_bf16 a[144:147], v[96:99], v[160:163], 0\n" \
"ds_read_b128 v[88:91], v48 offset:6144\n" \
"v_mfma_f32_16x16x32_bf16 a[144:147], v[100:103], v[164:167], a[144:147]\n" \
"global_load_dword v253, v248, s[64:65]\n" \
"v_mfma_f32_16x16x32_bf16 a[148:151], v[96:99], v[168:171], 0\n" \
"ds_read_b64_tr_b16 v[148:149], v50 offset:16896\n" \
"v_mfma_f32_16x16x32_bf16 a[148:151], v[100:103], v[172:175], a[148:151]\n" \
"v_mfma_f32_16x16x32_bf16 a[152:155], v[96:99], v[176:179], 0\n" \
"ds_read_b128 v[92:95], v49 offset:6144\n" \
"v_mfma_f32_16x16x32_bf16 a[152:155], v[100:103], v[180:183], a[152:155]\n" \
"v_mfma_f32_16x16x32_bf16 a[156:159], v[96:99], v[184:187], 0\n" \
"ds_read_b64_tr_b16 v[150:151], v50 offset:20992\n" \
"v_mfma_f32_16x16x32_bf16 a[156:159], v[100:103], v[188:191], a[156:159]\n" \
"v_mfma_f32_16x16x32_bf16 a[188:191], v[104:107], v[184:187], 0\n" \
"ds_read_b128 v[96:99], v48 offset:8192\n" \
"v_mfma_f32_16x16x32_bf16 a[188:191], v[108:111], v[188:191], a[188:191]\n" \
"v_mfma_f32_16x16x32_bf16 a[184:187], v[104:107], v[176:179], 0\n" \
"ds_read_b64_tr_b16 v[152:153], v51 offset:512\n" \
"v_mfma_f32_16x16x32_bf16 a[184:187], v[108:111], v[180:183], a[184:187]\n" \
"v_mfma_f32_16x16x32_bf16 a[180:183], v[104:107], v[168:171], 0\n" \
"ds_read_b128 v[100:103], v49 offset:8192\n" \
"v_mfma_f32_16x16x32_bf16 a[180:183], v[108:111], v[172:175], a[180:183]\n" \
"v_mfma_f32_16x16x32_bf16 a[176:179], v[104:107], v[160:163], 0\n" \
"ds_read_b64_tr_b16 v[154:155], v51 offset:4608\n" \
"v_mfma_f32_16x16x32_bf16 a[176:179], v[108:111], v[164:167], a[176:179]\n" \
"v_mfma_f32_16x16x32_bf16 a[208:211], v[112:115], v[160:163], 0\n" \
"ds_read_b128 v[104:107], v48 offset:10240\n" \
"v_mfma_f32_16x16x32_bf16 a[208:211], v[116:119], v[164:167], a[208:211]\n" \
"v_mfma_f32_16x16x32_bf16 a[212:215], v[112:115], v[168:171], 0\n" \
"ds_read_b64_tr_b16 v[156:157], v51 offset:16896\n" \
"v_mfma_f32_16x16x32_bf16 a[212:215], v[116:119], v[172:175], a[212:215]\n" \
"v_mfma_f32_16x16x32_bf16 a[216:219], v[112:115], v[176:179], 0\n" \
"ds_read_b128 v[108:111], v49 offset:10240\n" \
"v_mfma_f32_16x16x32_bf16 a[216:219], v[116:119], v[180:183], a[216:219]\n" \
"v_mfma_f32_16x16x32_bf16 a[220:223], v[112:115], v[184:187], 0\n" \
"ds_read_b64_tr_b16 v[158:159], v51 offset:20992\n" \
"v_mfma_f32_16x16x32_bf16 a[220:223], v[116:119], v[188:191], a[220:223]\n" \
"v_mfma_f32_16x16x32_bf16 a[252:255], v[120:123], v[184:187], 0\n" \
"ds_read_b128 v[112:115], v48 offset:12288\n" \
"v_mfma_f32_16x16x32_bf16 a[252:255], v[124:127], v[188:191], a[252:255]\n" \
"v_mfma_f32_16x16x32_bf16 a[248:251], v[120:123], v[176:179], 0\n" \
"v_mfma_f32_16x16x32_bf16 a[248:251], v[124:127], v[180:183], a[248:251]\n" \
"v_mfma_f32_16x16x32_bf16 a[244:247], v[120:123], v[168:171], 0\n" \
"ds_read_b128 v[116:119], v49 offset:12288\n" \
"v_mfma_f32_16x16x32_bf16 a[244:247], v[124:127], v[172:175], a[244:247]\n" \
"v_mfma_f32_16x16x32_bf16 a[240:243], v[120:123], v[160:163], 0\n" \
"v_mfma_f32_16x16x32_bf16 a[240:243], v[124:127], v[164:167], a[240:243]\n" \
"s_waitcnt lgkmcnt(1)\n" \
"v_mfma_f32_16x16x32_bf16 a[0:3], v[128:131], v[64:67], a[0:3]\n" \
"ds_read_b128 v[120:123], v48 offset:14336\n" \
"v_mfma_f32_16x16x32_bf16 a[0:3], v[132:135], v[68:71], a[0:3]\n" \
"v_mfma_f32_16x16x32_bf16 a[4:7], v[136:139], v[64:67], a[4:7]\n" \
"ds_read_b128 v[124:127], v49 offset:14336\n" \
"v_mfma_f32_16x16x32_bf16 a[4:7], v[140:143], v[68:71], a[4:7]\n" \
"s_add_u32 s68, s66, 0x100000\n" \
"v_mfma_f32_16x16x32_bf16 a[8:11], v[144:147], v[64:67], a[8:11]\n" \
"s_add_u32 s64, s10, s68\n" \
"s_addc_u32 s65, s11, 0\n" \
"ds_read_b64_tr_b16 v[160:161], v50 offset:1024\n" \
"v_mfma_f32_16x16x32_bf16 a[8:11], v[148:151], v[68:71], a[8:11]\n" \
"global_load_dword v253, v248, s[64:65]\n" \
"v_mfma_f32_16x16x32_bf16 a[12:15], v[152:155], v[64:67], a[12:15]\n" \
"s_add_u32 m0, s87, 4096\n" \
"ds_read_b64_tr_b16 v[162:163], v50 offset:5120\n" \
"v_mfma_f32_16x16x32_bf16 a[12:15], v[156:159], v[68:71], a[12:15]\n" \
"global_load_lds_dwordx4 v40, s[52:53] offset:-4096\n" \
"v_mfma_f32_16x16x32_bf16 a[44:47], v[152:155], v[72:75], a[44:47]\n" \
"ds_read_b64_tr_b16 v[164:165], v50 offset:17408\n" \
"v_mfma_f32_16x16x32_bf16 a[44:47], v[156:159], v[76:79], a[44:47]\n" \
"v_mfma_f32_16x16x32_bf16 a[40:43], v[144:147], v[72:75], a[40:43]\n" \
"ds_read_b64_tr_b16 v[166:167], v50 offset:21504\n" \
"v_mfma_f32_16x16x32_bf16 a[40:43], v[148:151], v[76:79], a[40:43]\n" \
"global_load_lds_dwordx4 v41, s[50:51] offset:-3072\n" \
"v_mfma_f32_16x16x32_bf16 a[36:39], v[136:139], v[72:75], a[36:39]\n" \
"ds_read_b64_tr_b16 v[168:169], v51 offset:1024\n" \
"v_mfma_f32_16x16x32_bf16 a[36:39], v[140:143], v[76:79], a[36:39]\n" \
"v_mfma_f32_16x16x32_bf16 a[32:35], v[128:131], v[72:75], a[32:35]\n" \
"ds_read_b64_tr_b16 v[170:171], v51 offset:5120\n" \
"v_mfma_f32_16x16x32_bf16 a[32:35], v[132:135], v[76:79], a[32:35]\n" \
"global_load_lds_dwordx4 v42, s[52:53] offset:-2048\n" \
"v_mfma_f32_16x16x32_bf16 a[64:67], v[128:131], v[80:83], a[64:67]\n" \
"ds_read_b64_tr_b16 v[172:173], v51 offset:17408\n" \
"v_mfma_f32_16x16x32_bf16 a[64:67], v[132:135], v[84:87], a[64:67]\n" \
"v_mfma_f32_16x16x32_bf16 a[68:71], v[136:139], v[80:83], a[68:71]\n" \
"ds_read_b64_tr_b16 v[174:175], v51 offset:21504\n" \
"v_mfma_f32_16x16x32_bf16 a[68:71], v[140:143], v[84:87], a[68:71]\n" \
"global_load_lds_dwordx4 v43, s[50:51] offset:-1024\n" \
"v_mfma_f32_16x16x32_bf16 a[72:75], v[144:147], v[80:83], a[72:75]\n" \
"ds_read_b64_tr_b16 v[176:177], v50 offset:1536\n" \
"v_mfma_f32_16x16x32_bf16 a[72:75], v[148:151], v[84:87], a[72:75]\n" \
"v_mfma_f32_16x16x32_bf16 a[76:79], v[152:155], v[80:83], a[76:79]\n" \
"ds_read_b64_tr_b16 v[178:179], v50 offset:5632\n" \
"v_mfma_f32_16x16x32_bf16 a[76:79], v[156:159], v[84:87], a[76:79]\n" \
"global_load_lds_dwordx4 v44, s[52:53] offset:0\n" \
"v_mfma_f32_16x16x32_bf16 a[108:111], v[152:155], v[88:91], a[108:111]\n" \
"ds_read_b64_tr_b16 v[180:181], v50 offset:17920\n" \
"v_mfma_f32_16x16x32_bf16 a[108:111], v[156:159], v[92:95], a[108:111]\n" \
"v_mfma_f32_16x16x32_bf16 a[104:107], v[144:147], v[88:91], a[104:107]\n" \
"ds_read_b64_tr_b16 v[182:183], v50 offset:22016\n" \
"v_mfma_f32_16x16x32_bf16 a[104:107], v[148:151], v[92:95], a[104:107]\n" \
"global_load_lds_dwordx4 v45, s[50:51] offset:1024\n" \
"v_mfma_f32_16x16x32_bf16 a[100:103], v[136:139], v[88:91], a[100:103]\n" \
"ds_read_b64_tr_b16 v[184:185], v51 offset:1536\n" \
"v_mfma_f32_16x16x32_bf16 a[100:103], v[140:143], v[92:95], a[100:103]\n" \
"v_mfma_f32_16x16x32_bf16 a[96:99], v[128:131], v[88:91], a[96:99]\n" \
"ds_read_b64_tr_b16 v[186:187], v51 offset:5632\n" \
"v_mfma_f32_16x16x32_bf16 a[96:99], v[132:135], v[92:95], a[96:99]\n" \
"global_load_lds_dwordx4 v46, s[52:53] offset:2048\n" \
"v_mfma_f32_16x16x32_bf16 a[128:131], v[128:131], v[96:99], a[128:131]\n" \
"ds_read_b64_tr_b16 v[188:189], v51 offset:17920\n" \
"v_mfma_f32_16x16x32_bf16 a[128:131], v[132:135], v[100:103], a[128:131]\n" \
"v_mfma_f32_16x16x32_bf16 a[132:135], v[136:139], v[96:99], a[132:135]\n" \
"ds_read_b64_tr_b16 v[190:191], v51 offset:22016\n" \
"v_mfma_f32_16x16x32_bf16 a[132:135], v[140:143], v[100:103], a[132:135]\n" \
"global_load_lds_dwordx4 v47, s[50:51] offset:3072\n" \
"v_mfma_f32_16x16x32_bf16 a[136:139], v[144:147], v[96:99], a[136:139]\n" \
"s_add_u32 s52, s52, s74\n" \
"s_addc_u32 s53, s53, 0\n" \
"v_mfma_f32_16x16x32_bf16 a[136:139], v[148:151], v[100:103], a[136:139]\n" \
"s_add_u32 s50, s50, s74\n" \
"s_addc_u32 s51, s51, 0\n" \
"v_mfma_f32_16x16x32_bf16 a[140:143], v[152:155], v[96:99], a[140:143]\n" \
"v_mfma_f32_16x16x32_bf16 a[140:143], v[156:159], v[100:103], a[140:143]\n" \
"v_mfma_f32_16x16x32_bf16 a[172:175], v[152:155], v[104:107], a[172:175]\n" \
"s_mul_i32 s76, s73, 0x0\n" \
"s_add_u32 s77, s76, 0xffff8000\n" \
"s_sub_u32 s78, 0xffff8000, s76\n" \
"v_mfma_f32_16x16x32_bf16 a[172:175], v[156:159], v[108:111], a[172:175]\n" \
"v_add_u32 v48, s77, v48\n" \
"v_add_u32 v49, s77, v49\n" \
"v_mfma_f32_16x16x32_bf16 a[168:171], v[144:147], v[104:107], a[168:171]\n" \
"v_add_u32 v50, s78, v50\n" \
"v_add_u32 v51, s78, v51\n" \
"v_mfma_f32_16x16x32_bf16 a[168:171], v[148:151], v[108:111], a[168:171]\n" \
"v_mfma_f32_16x16x32_bf16 a[164:167], v[136:139], v[104:107], a[164:167]\n" \
"v_mfma_f32_16x16x32_bf16 a[164:167], v[140:143], v[108:111], a[164:167]\n" \
"v_mfma_f32_16x16x32_bf16 a[160:163], v[128:131], v[104:107], a[160:163]\n" \
"v_mfma_f32_16x16x32_bf16 a[160:163], v[132:135], v[108:111], a[160:163]\n" \
"s_waitcnt lgkmcnt(0)\n" \
"v_mfma_f32_16x16x32_bf16 a[192:195], v[128:131], v[112:115], a[192:195]\n" \
"v_mfma_f32_16x16x32_bf16 a[192:195], v[132:135], v[116:119], a[192:195]\n" \
"v_mfma_f32_16x16x32_bf16 a[196:199], v[136:139], v[112:115], a[196:199]\n" \
"v_mfma_f32_16x16x32_bf16 a[196:199], v[140:143], v[116:119], a[196:199]\n" \
"v_mfma_f32_16x16x32_bf16 a[200:203], v[144:147], v[112:115], a[200:203]\n" \
"v_mfma_f32_16x16x32_bf16 a[200:203], v[148:151], v[116:119], a[200:203]\n" \
"v_mfma_f32_16x16x32_bf16 a[204:207], v[152:155], v[112:115], a[204:207]\n" \
"v_mfma_f32_16x16x32_bf16 a[204:207], v[156:159], v[116:119], a[204:207]\n" \
"v_mfma_f32_16x16x32_bf16 a[236:239], v[152:155], v[120:123], a[236:239]\n" \
"v_mfma_f32_16x16x32_bf16 a[236:239], v[156:159], v[124:127], a[236:239]\n" \
"Lpg609_%=:\n" \
"Lst0x0_%=:\n" \
"s_cbranch_execz Lpg2767_%=\n" \
"v_mfma_f32_16x16x32_bf16 a[232:235], v[144:147], v[120:123], a[232:235]\n" \
"v_mfma_f32_16x16x32_bf16 a[232:235], v[148:151], v[124:127], a[232:235]\n" \
"v_mfma_f32_16x16x32_bf16 a[228:231], v[136:139], v[120:123], a[228:231]\n" \
"v_mfma_f32_16x16x32_bf16 a[228:231], v[140:143], v[124:127], a[228:231]\n" \
"s_waitcnt vmcnt(10) lgkmcnt(0)\n" \
"v_mfma_f32_16x16x32_bf16 a[224:227], v[128:131], v[120:123], a[224:227]\n" \
"v_mfma_f32_16x16x32_bf16 a[224:227], v[132:135], v[124:127], a[224:227]\n" \
"s_barrier\n" \
"v_mfma_f32_16x16x32_bf16 a[16:19], v[64:67], v[160:163], a[16:19]\n" \
"s_add_u32 m0, s87, 135168\n" \
"ds_read_b64_tr_b16 v[128:129], v50 offset:0\n" \
"v_readfirstlane_b32 s60, v244\n" \
"v_mfma_f32_16x16x32_bf16 a[16:19], v[68:71], v[164:167], a[16:19]\n" \
"s_add_u32 s60, s60, 0x9e3779b9\n" \
"global_load_lds_dwordx4 v32, s[54:55] offset:-4096\n" \
"v_mfma_f32_16x16x32_bf16 a[20:23], v[64:67], v[168:171], a[20:23]\n" \
"s_mul_i32 s88, s62, 557824\n" \
"s_mul_i32 s89, s63, 17408\n" \
"ds_read_b64_tr_b16 v[130:131], v50 offset:4096\n" \
"v_mfma_f32_16x16x32_bf16 a[20:23], v[68:71], v[172:175], a[20:23]\n" \
"s_add_u32 s88, s88, s89\n" \
"s_add_u32 s88, s88, s95\n" \
"s_add_u32 s89, s88, 17850368\n" \
"v_mfma_f32_16x16x32_bf16 a[24:27], v[64:67], v[176:179], a[24:27]\n" \
"s_lshl_b32 s91, s62, 7\n" \
"s_add_u32 s91, s91, 65585152\n" \
"ds_read_b64_tr_b16 v[132:133], v50 offset:16384\n" \
"v_mfma_f32_16x16x32_bf16 a[24:27], v[68:71], v[180:183], a[24:27]\n" \
"s_mov_b32 s92, 0x3e0293ee\n" \
"global_load_lds_dwordx4 v33, s[40:41] offset:-3072\n" \
"v_mfma_f32_16x16x32_bf16 a[28:31], v[64:67], v[184:187], a[28:31]\n" \
"ds_read_b64_tr_b16 v[134:135], v50 offset:20480\n" \
"v_mfma_f32_16x16x32_bf16 a[28:31], v[68:71], v[188:191], a[28:31]\n" \
"v_mfma_f32_16x16x32_bf16 a[60:63], v[72:75], v[184:187], a[60:63]\n" \
"ds_read_b128 v[64:67], v48 offset:0\n" \
"v_mfma_f32_16x16x32_bf16 a[60:63], v[76:79], v[188:191], a[60:63]\n" \
"global_load_lds_dwordx4 v34, s[54:55] offset:-2048\n" \
"v_mfma_f32_16x16x32_bf16 a[56:59], v[72:75], v[176:179], a[56:59]\n" \
"ds_read_b64_tr_b16 v[136:137], v51 offset:0\n" \
"v_mfma_f32_16x16x32_bf16 a[56:59], v[76:79], v[180:183], a[56:59]\n" \
"v_mfma_f32_16x16x32_bf16 a[52:55], v[72:75], v[168:171], a[52:55]\n" \
"ds_read_b128 v[68:71], v49 offset:0\n" \
"v_mfma_f32_16x16x32_bf16 a[52:55], v[76:79], v[172:175], a[52:55]\n" \
"global_load_lds_dwordx4 v35, s[40:41] offset:-1024\n" \
"v_mfma_f32_16x16x32_bf16 a[48:51], v[72:75], v[160:163], a[48:51]\n" \
"ds_read_b64_tr_b16 v[138:139], v51 offset:4096\n" \
"v_mfma_f32_16x16x32_bf16 a[48:51], v[76:79], v[164:167], a[48:51]\n" \
"v_mfma_f32_16x16x32_bf16 a[80:83], v[80:83], v[160:163], a[80:83]\n" \
"ds_read_b128 v[72:75], v48 offset:2048\n" \
"v_mfma_f32_16x16x32_bf16 a[80:83], v[84:87], v[164:167], a[80:83]\n" \
"global_load_lds_dwordx4 v36, s[54:55] offset:0\n" \
"v_mfma_f32_16x16x32_bf16 a[84:87], v[80:83], v[168:171], a[84:87]\n" \
"ds_read_b64_tr_b16 v[140:141], v51 offset:16384\n" \
"v_mfma_f32_16x16x32_bf16 a[84:87], v[84:87], v[172:175], a[84:87]\n" \
"v_mfma_f32_16x16x32_bf16 a[88:91], v[80:83], v[176:179], a[88:91]\n" \
"ds_read_b128 v[76:79], v49 offset:2048\n" \
"v_mfma_f32_16x16x32_bf16 a[88:91], v[84:87], v[180:183], a[88:91]\n" \
"global_load_lds_dwordx4 v37, s[40:41] offset:1024\n" \
"v_mfma_f32_16x16x32_bf16 a[92:95], v[80:83], v[184:187], a[92:95]\n" \
"ds_read_b64_tr_b16 v[142:143], v51 offset:20480\n" \
"v_mfma_f32_16x16x32_bf16 a[92:95], v[84:87], v[188:191], a[92:95]\n" \
"v_mfma_f32_16x16x32_bf16 a[124:127], v[88:91], v[184:187], a[124:127]\n" \
"ds_read_b128 v[80:83], v48 offset:4096\n" \
"v_mfma_f32_16x16x32_bf16 a[124:127], v[92:95], v[188:191], a[124:127]\n" \
"global_load_lds_dwordx4 v38, s[54:55] offset:2048\n" \
"v_mfma_f32_16x16x32_bf16 a[120:123], v[88:91], v[176:179], a[120:123]\n" \
"ds_read_b64_tr_b16 v[144:145], v50 offset:512\n" \
"v_mfma_f32_16x16x32_bf16 a[120:123], v[92:95], v[180:183], a[120:123]\n" \
"v_mfma_f32_16x16x32_bf16 a[116:119], v[88:91], v[168:171], a[116:119]\n" \
"ds_read_b128 v[84:87], v49 offset:4096\n" \
"global_load_lds_dwordx4 v39, s[40:41] offset:3072\n" \
"v_mfma_f32_16x16x32_bf16 a[116:119], v[92:95], v[172:175], a[116:119]\n" \
"s_add_u32 s54, s54, s75\n" \
"s_addc_u32 s55, s55, 0\n" \
"v_mfma_f32_16x16x32_bf16 a[112:115], v[88:91], v[160:163], a[112:115]\n" \
"s_add_u32 s40, s40, s75\n" \
"s_addc_u32 s41, s41, 0\n" \
"ds_read_b64_tr_b16 v[146:147], v50 offset:4608\n" \
"v_mfma_f32_16x16x32_bf16 a[112:115], v[92:95], v[164:167], a[112:115]\n" \
"s_add_u32 s68, s67, 0x200000\n" \
"s_add_u32 s64, s14, s68\n" \
"s_addc_u32 s65, s15, 0\n" \
"v_mfma_f32_16x16x32_bf16 a[144:147], v[96:99], v[160:163], a[144:147]\n" \
"ds_read_b128 v[88:91], v48 offset:6144\n" \
"v_mfma_f32_16x16x32_bf16 a[144:147], v[100:103], v[164:167], a[144:147]\n" \
"global_load_dword v253, v248, s[64:65]\n" \
"v_mfma_f32_16x16x32_bf16 a[148:151], v[96:99], v[168:171], a[148:151]\n" \
"s_add_u32 s68, s67, 0x200000\n" \
"ds_read_b64_tr_b16 v[148:149], v50 offset:16896\n" \
"v_mfma_f32_16x16x32_bf16 a[148:151], v[100:103], v[172:175], a[148:151]\n" \
"s_add_u32 s64, s16, s68\n" \
"s_addc_u32 s65, s17, 0\n" \
"v_mfma_f32_16x16x32_bf16 a[152:155], v[96:99], v[176:179], a[152:155]\n" \
"ds_read_b128 v[92:95], v49 offset:6144\n" \
"v_mfma_f32_16x16x32_bf16 a[152:155], v[100:103], v[180:183], a[152:155]\n" \
"global_load_dword v253, v248, s[64:65]\n" \
"v_mfma_f32_16x16x32_bf16 a[156:159], v[96:99], v[184:187], a[156:159]\n" \
"ds_read_b64_tr_b16 v[150:151], v50 offset:20992\n" \
"v_mfma_f32_16x16x32_bf16 a[156:159], v[100:103], v[188:191], a[156:159]\n" \
"v_mfma_f32_16x16x32_bf16 a[188:191], v[104:107], v[184:187], a[188:191]\n" \
"ds_read_b128 v[96:99], v48 offset:8192\n" \
"v_mfma_f32_16x16x32_bf16 a[188:191], v[108:111], v[188:191], a[188:191]\n" \
"v_mfma_f32_16x16x32_bf16 a[184:187], v[104:107], v[176:179], a[184:187]\n" \
"ds_read_b64_tr_b16 v[152:153], v51 offset:512\n" \
"v_mfma_f32_16x16x32_bf16 a[184:187], v[108:111], v[180:183], a[184:187]\n" \
"v_mfma_f32_16x16x32_bf16 a[180:183], v[104:107], v[168:171], a[180:183]\n" \
"ds_read_b128 v[100:103], v49 offset:8192\n" \
"v_mfma_f32_16x16x32_bf16 a[180:183], v[108:111], v[172:175], a[180:183]\n" \
"v_mfma_f32_16x16x32_bf16 a[176:179], v[104:107], v[160:163], a[176:179]\n" \
"ds_read_b64_tr_b16 v[154:155], v51 offset:4608\n" \
"v_mfma_f32_16x16x32_bf16 a[176:179], v[108:111], v[164:167], a[176:179]\n" \
"v_mfma_f32_16x16x32_bf16 a[208:211], v[112:115], v[160:163], a[208:211]\n" \
"ds_read_b128 v[104:107], v48 offset:10240\n" \
"v_mfma_f32_16x16x32_bf16 a[208:211], v[116:119], v[164:167], a[208:211]\n" \
"v_mfma_f32_16x16x32_bf16 a[212:215], v[112:115], v[168:171], a[212:215]\n" \
"ds_read_b64_tr_b16 v[156:157], v51 offset:16896\n" \
"v_mfma_f32_16x16x32_bf16 a[212:215], v[116:119], v[172:175], a[212:215]\n" \
"v_mfma_f32_16x16x32_bf16 a[216:219], v[112:115], v[176:179], a[216:219]\n" \
"ds_read_b128 v[108:111], v49 offset:10240\n" \
"v_mfma_f32_16x16x32_bf16 a[216:219], v[116:119], v[180:183], a[216:219]\n" \
"v_mfma_f32_16x16x32_bf16 a[220:223], v[112:115], v[184:187], a[220:223]\n" \
"ds_read_b64_tr_b16 v[158:159], v51 offset:20992\n" \
"v_mfma_f32_16x16x32_bf16 a[220:223], v[116:119], v[188:191], a[220:223]\n" \
"v_mfma_f32_16x16x32_bf16 a[252:255], v[120:123], v[184:187], a[252:255]\n" \
"ds_read_b128 v[112:115], v48 offset:12288\n" \
"v_mfma_f32_16x16x32_bf16 a[252:255], v[124:127], v[188:191], a[252:255]\n" \
"v_mfma_f32_16x16x32_bf16 a[248:251], v[120:123], v[176:179], a[248:251]\n" \
"v_mfma_f32_16x16x32_bf16 a[248:251], v[124:127], v[180:183], a[248:251]\n" \
"v_mfma_f32_16x16x32_bf16 a[244:247], v[120:123], v[168:171], a[244:247]\n" \
"ds_read_b128 v[116:119], v49 offset:12288\n" \
"v_mfma_f32_16x16x32_bf16 a[244:247], v[124:127], v[172:175], a[244:247]\n" \
"v_mfma_f32_16x16x32_bf16 a[240:243], v[120:123], v[160:163], a[240:243]\n" \
"v_mfma_f32_16x16x32_bf16 a[240:243], v[124:127], v[164:167], a[240:243]\n" \
"s_waitcnt lgkmcnt(1)\n" \
"v_mfma_f32_16x16x32_bf16 a[0:3], v[128:131], v[64:67], a[0:3]\n" \
"ds_read_b128 v[120:123], v48 offset:14336\n" \
"v_mfma_f32_16x16x32_bf16 a[0:3], v[132:135], v[68:71], a[0:3]\n" \
"v_mfma_f32_16x16x32_bf16 a[4:7], v[136:139], v[64:67], a[4:7]\n" \
"ds_read_b128 v[124:127], v49 offset:14336\n" \
"v_mfma_f32_16x16x32_bf16 a[4:7], v[140:143], v[68:71], a[4:7]\n" \
"s_add_u32 s68, s67, 0x300000\n" \
"v_mfma_f32_16x16x32_bf16 a[8:11], v[144:147], v[64:67], a[8:11]\n" \
"s_add_u32 s64, s14, s68\n" \
"s_addc_u32 s65, s15, 0\n" \
"ds_read_b64_tr_b16 v[160:161], v50 offset:1024\n" \
"v_mfma_f32_16x16x32_bf16 a[8:11], v[148:151], v[68:71], a[8:11]\n" \
"global_load_dword v253, v248, s[64:65]\n" \
"v_mfma_f32_16x16x32_bf16 a[12:15], v[152:155], v[64:67], a[12:15]\n" \
"s_add_u32 m0, s87, 69632\n" \
"ds_read_b64_tr_b16 v[162:163], v50 offset:5120\n" \
"v_mfma_f32_16x16x32_bf16 a[12:15], v[156:159], v[68:71], a[12:15]\n" \
"global_load_lds_dwordx4 v40, s[52:53] offset:-4096\n" \
"v_mfma_f32_16x16x32_bf16 a[44:47], v[152:155], v[72:75], a[44:47]\n" \
"ds_read_b64_tr_b16 v[164:165], v50 offset:17408\n" \
"v_mfma_f32_16x16x32_bf16 a[44:47], v[156:159], v[76:79], a[44:47]\n" \
"v_mfma_f32_16x16x32_bf16 a[40:43], v[144:147], v[72:75], a[40:43]\n" \
"ds_read_b64_tr_b16 v[166:167], v50 offset:21504\n" \
"v_mfma_f32_16x16x32_bf16 a[40:43], v[148:151], v[76:79], a[40:43]\n" \
"global_load_lds_dwordx4 v41, s[50:51] offset:-3072\n" \
"v_mfma_f32_16x16x32_bf16 a[36:39], v[136:139], v[72:75], a[36:39]\n" \
"ds_read_b64_tr_b16 v[168:169], v51 offset:1024\n" \
"v_mfma_f32_16x16x32_bf16 a[36:39], v[140:143], v[76:79], a[36:39]\n" \
"v_mfma_f32_16x16x32_bf16 a[32:35], v[128:131], v[72:75], a[32:35]\n" \
"ds_read_b64_tr_b16 v[170:171], v51 offset:5120\n" \
"v_mfma_f32_16x16x32_bf16 a[32:35], v[132:135], v[76:79], a[32:35]\n" \
"global_load_lds_dwordx4 v42, s[52:53] offset:-2048\n" \
"v_mfma_f32_16x16x32_bf16 a[64:67], v[128:131], v[80:83], a[64:67]\n" \
"ds_read_b64_tr_b16 v[172:173], v51 offset:17408\n" \
"v_mfma_f32_16x16x32_bf16 a[64:67], v[132:135], v[84:87], a[64:67]\n" \
"v_mfma_f32_16x16x32_bf16 a[68:71], v[136:139], v[80:83], a[68:71]\n" \
"ds_read_b64_tr_b16 v[174:175], v51 offset:21504\n" \
"v_mfma_f32_16x16x32_bf16 a[68:71], v[140:143], v[84:87], a[68:71]\n" \
"global_load_lds_dwordx4 v43, s[50:51] offset:-1024\n" \
"v_mfma_f32_16x16x32_bf16 a[72:75], v[144:147], v[80:83], a[72:75]\n" \
"ds_read_b64_tr_b16 v[176:177], v50 offset:1536\n" \
"v_mfma_f32_16x16x32_bf16 a[72:75], v[148:151], v[84:87], a[72:75]\n" \
"v_mfma_f32_16x16x32_bf16 a[76:79], v[152:155], v[80:83], a[76:79]\n" \
"ds_read_b64_tr_b16 v[178:179], v50 offset:5632\n" \
"v_mfma_f32_16x16x32_bf16 a[76:79], v[156:159], v[84:87], a[76:79]\n" \
"global_load_lds_dwordx4 v44, s[52:53] offset:0\n" \
"v_mfma_f32_16x16x32_bf16 a[108:111], v[152:155], v[88:91], a[108:111]\n" \
"ds_read_b64_tr_b16 v[180:181], v50 offset:17920\n" \
"v_mfma_f32_16x16x32_bf16 a[108:111], v[156:159], v[92:95], a[108:111]\n" \
"v_mfma_f32_16x16x32_bf16 a[104:107], v[144:147], v[88:91], a[104:107]\n" \
"ds_read_b64_tr_b16 v[182:183], v50 offset:22016\n" \
"v_mfma_f32_16x16x32_bf16 a[104:107], v[148:151], v[92:95], a[104:107]\n" \
"global_load_lds_dwordx4 v45, s[50:51] offset:1024\n" \
"v_mfma_f32_16x16x32_bf16 a[100:103], v[136:139], v[88:91], a[100:103]\n" \
"ds_read_b64_tr_b16 v[184:185], v51 offset:1536\n" \
"v_mfma_f32_16x16x32_bf16 a[100:103], v[140:143], v[92:95], a[100:103]\n" \
"v_mfma_f32_16x16x32_bf16 a[96:99], v[128:131], v[88:91], a[96:99]\n" \
"ds_read_b64_tr_b16 v[186:187], v51 offset:5632\n" \
"v_mfma_f32_16x16x32_bf16 a[96:99], v[132:135], v[92:95], a[96:99]\n" \
"global_load_lds_dwordx4 v46, s[52:53] offset:2048\n" \
"v_mfma_f32_16x16x32_bf16 a[128:131], v[128:131], v[96:99], a[128:131]\n" \
"ds_read_b64_tr_b16 v[188:189], v51 offset:17920\n" \
"v_mfma_f32_16x16x32_bf16 a[128:131], v[132:135], v[100:103], a[128:131]\n" \
"v_mfma_f32_16x16x32_bf16 a[132:135], v[136:139], v[96:99], a[132:135]\n" \
"ds_read_b64_tr_b16 v[190:191], v51 offset:22016\n" \
"v_mfma_f32_16x16x32_bf16 a[132:135], v[140:143], v[100:103], a[132:135]\n" \
"global_load_lds_dwordx4 v47, s[50:51] offset:3072\n" \
"v_mfma_f32_16x16x32_bf16 a[136:139], v[144:147], v[96:99], a[136:139]\n" \
"s_add_u32 s52, s52, s74\n" \
"s_addc_u32 s53, s53, 0\n" \
"v_mfma_f32_16x16x32_bf16 a[136:139], v[148:151], v[100:103], a[136:139]\n" \
"s_add_u32 s50, s50, s74\n" \
"s_addc_u32 s51, s51, 0\n" \
"v_mfma_f32_16x16x32_bf16 a[140:143], v[152:155], v[96:99], a[140:143]\n" \
"v_mfma_f32_16x16x32_bf16 a[140:143], v[156:159], v[100:103], a[140:143]\n" \
"v_mfma_f32_16x16x32_bf16 a[172:175], v[152:155], v[104:107], a[172:175]\n" \
"s_mul_i32 s76, s73, 0xfffd0000\n" \
"s_add_u32 s77, s76, 0x18000\n" \
"s_sub_u32 s78, 0xfffe8000, s76\n" \
"v_mfma_f32_16x16x32_bf16 a[172:175], v[156:159], v[108:111], a[172:175]\n" \
"v_add_u32 v48, s77, v48\n" \
"v_add_u32 v49, s77, v49\n" \
"v_mfma_f32_16x16x32_bf16 a[168:171], v[144:147], v[104:107], a[168:171]\n" \
"v_add_u32 v50, s78, v50\n" \
"v_add_u32 v51, s78, v51\n" \
"v_mfma_f32_16x16x32_bf16 a[168:171], v[148:151], v[108:111], a[168:171]\n" \
"v_mfma_f32_16x16x32_bf16 a[164:167], v[136:139], v[104:107], a[164:167]\n" \
"v_mfma_f32_16x16x32_bf16 a[164:167], v[140:143], v[108:111], a[164:167]\n" \
"v_mfma_f32_16x16x32_bf16 a[160:163], v[128:131], v[104:107], a[160:163]\n" \
"v_mfma_f32_16x16x32_bf16 a[160:163], v[132:135], v[108:111], a[160:163]\n" \
"s_waitcnt lgkmcnt(0)\n" \
"v_mfma_f32_16x16x32_bf16 a[192:195], v[128:131], v[112:115], a[192:195]\n" \
"v_mfma_f32_16x16x32_bf16 a[192:195], v[132:135], v[116:119], a[192:195]\n" \
"v_mfma_f32_16x16x32_bf16 a[196:199], v[136:139], v[112:115], a[196:199]\n" \
"v_mfma_f32_16x16x32_bf16 a[196:199], v[140:143], v[116:119], a[196:199]\n" \
"v_mfma_f32_16x16x32_bf16 a[200:203], v[144:147], v[112:115], a[200:203]\n" \
"v_mfma_f32_16x16x32_bf16 a[200:203], v[148:151], v[116:119], a[200:203]\n" \
"v_mfma_f32_16x16x32_bf16 a[204:207], v[152:155], v[112:115], a[204:207]\n" \
"v_mfma_f32_16x16x32_bf16 a[204:207], v[156:159], v[116:119], a[204:207]\n" \
"v_mfma_f32_16x16x32_bf16 a[236:239], v[152:155], v[120:123], a[236:239]\n" \
"v_mfma_f32_16x16x32_bf16 a[236:239], v[156:159], v[124:127], a[236:239]\n" \
"v_mfma_f32_16x16x32_bf16 a[232:235], v[144:147], v[120:123], a[232:235]\n" \
"v_mfma_f32_16x16x32_bf16 a[232:235], v[148:151], v[124:127], a[232:235]\n" \
"v_mfma_f32_16x16x32_bf16 a[228:231], v[136:139], v[120:123], a[228:231]\n" \
"v_mfma_f32_16x16x32_bf16 a[228:231], v[140:143], v[124:127], a[228:231]\n" \
"s_waitcnt vmcnt(11) lgkmcnt(0)\n" \
"v_mfma_f32_16x16x32_bf16 a[224:227], v[128:131], v[120:123], a[224:227]\n" \
"v_mfma_f32_16x16x32_bf16 a[224:227], v[132:135], v[124:127], a[224:227]\n" \
"s_barrier\n" \
"v_mfma_f32_16x16x32_bf16 a[16:19], v[64:67], v[160:163], a[16:19]\n" \
"s_add_u32 m0, s87, 102400\n" \
"ds_read_b64_tr_b16 v[128:129], v50 offset:0\n" \
"v_mfma_f32_16x16x32_bf16 a[16:19], v[68:71], v[164:167], a[16:19]\n" \
"global_load_lds_dwordx4 v32, s[54:55] offset:-4096\n" \
"v_mfma_f32_16x16x32_bf16 a[20:23], v[64:67], v[168:171], a[20:23]\n" \
"ds_read_b64_tr_b16 v[130:131], v50 offset:4096\n" \
"v_mfma_f32_16x16x32_bf16 a[20:23], v[68:71], v[172:175], a[20:23]\n" \
"v_mfma_f32_16x16x32_bf16 a[24:27], v[64:67], v[176:179], a[24:27]\n" \
"ds_read_b64_tr_b16 v[132:133], v50 offset:16384\n" \
"v_mfma_f32_16x16x32_bf16 a[24:27], v[68:71], v[180:183], a[24:27]\n" \
"global_load_lds_dwordx4 v33, s[40:41] offset:-3072\n" \
"v_mfma_f32_16x16x32_bf16 a[28:31], v[64:67], v[184:187], a[28:31]\n" \
"ds_read_b64_tr_b16 v[134:135], v50 offset:20480\n" \
"v_mfma_f32_16x16x32_bf16 a[28:31], v[68:71], v[188:191], a[28:31]\n" \
"v_mfma_f32_16x16x32_bf16 a[60:63], v[72:75], v[184:187], a[60:63]\n" \
"ds_read_b128 v[64:67], v48 offset:0\n" \
"v_mfma_f32_16x16x32_bf16 a[60:63], v[76:79], v[188:191], a[60:63]\n" \
"global_load_lds_dwordx4 v34, s[54:55] offset:-2048\n" \
"v_mfma_f32_16x16x32_bf16 a[56:59], v[72:75], v[176:179], a[56:59]\n" \
"ds_read_b64_tr_b16 v[136:137], v51 offset:0\n" \
"v_mfma_f32_16x16x32_bf16 a[56:59], v[76:79], v[180:183], a[56:59]\n" \
"v_mfma_f32_16x16x32_bf16 a[52:55], v[72:75], v[168:171], a[52:55]\n" \
"ds_read_b128 v[68:71], v49 offset:0\n" \
"v_mfma_f32_16x16x32_bf16 a[52:55], v[76:79], v[172:175], a[52:55]\n" \
"global_load_lds_dwordx4 v35, s[40:41] offset:-1024\n" \
"v_mfma_f32_16x16x32_bf16 a[48:51], v[72:75], v[160:163], a[48:51]\n" \
"ds_read_b64_tr_b16 v[138:139], v51 offset:4096\n" \
"v_mfma_f32_16x16x32_bf16 a[48:51], v[76:79], v[164:167], a[48:51]\n" \
"v_mfma_f32_16x16x32_bf16 a[80:83], v[80:83], v[160:163], a[80:83]\n" \
"ds_read_b128 v[72:75], v48 offset:2048\n" \
"v_mfma_f32_16x16x32_bf16 a[80:83], v[84:87], v[164:167], a[80:83]\n" \
"global_load_lds_dwordx4 v36, s[54:55] offset:0\n" \
"v_mfma_f32_16x16x32_bf16 a[84:87], v[80:83], v[168:171], a[84:87]\n" \
"ds_read_b64_tr_b16 v[140:141], v51 offset:16384\n" \
"v_mfma_f32_16x16x32_bf16 a[84:87], v[84:87], v[172:175], a[84:87]\n" \
"v_mfma_f32_16x16x32_bf16 a[88:91], v[80:83], v[176:179], a[88:91]\n" \
"ds_read_b128 v[76:79], v49 offset:2048\n" \
"v_mfma_f32_16x16x32_bf16 a[88:91], v[84:87], v[180:183], a[88:91]\n" \
"global_load_lds_dwordx4 v37, s[40:41] offset:1024\n" \
"v_mfma_f32_16x16x32_bf16 a[92:95], v[80:83], v[184:187], a[92:95]\n" \
"ds_read_b64_tr_b16 v[142:143], v51 offset:20480\n" \
"v_mfma_f32_16x16x32_bf16 a[92:95], v[84:87], v[188:191], a[92:95]\n" \
"v_mfma_f32_16x16x32_bf16 a[124:127], v[88:91], v[184:187], a[124:127]\n" \
"ds_read_b128 v[80:83], v48 offset:4096\n" \
"v_mfma_f32_16x16x32_bf16 a[124:127], v[92:95], v[188:191], a[124:127]\n" \
"global_load_lds_dwordx4 v38, s[54:55] offset:2048\n" \
"v_mfma_f32_16x16x32_bf16 a[120:123], v[88:91], v[176:179], a[120:123]\n" \
"ds_read_b64_tr_b16 v[144:145], v50 offset:512\n" \
"v_mfma_f32_16x16x32_bf16 a[120:123], v[92:95], v[180:183], a[120:123]\n" \
"v_mfma_f32_16x16x32_bf16 a[116:119], v[88:91], v[168:171], a[116:119]\n" \
"ds_read_b128 v[84:87], v49 offset:4096\n" \
"global_load_lds_dwordx4 v39, s[40:41] offset:3072\n" \
"v_mfma_f32_16x16x32_bf16 a[116:119], v[92:95], v[172:175], a[116:119]\n" \
"s_add_u32 s54, s54, s75\n" \
"s_addc_u32 s55, s55, 0\n" \
"v_mfma_f32_16x16x32_bf16 a[112:115], v[88:91], v[160:163], a[112:115]\n" \
"s_add_u32 s40, s40, s75\n" \
"s_addc_u32 s41, s41, 0\n" \
"ds_read_b64_tr_b16 v[146:147], v50 offset:4608\n" \
"v_mfma_f32_16x16x32_bf16 a[112:115], v[92:95], v[164:167], a[112:115]\n" \
"s_add_u32 s68, s67, 0x300000\n" \
"s_add_u32 s64, s16, s68\n" \
"s_addc_u32 s65, s17, 0\n" \
"v_mfma_f32_16x16x32_bf16 a[144:147], v[96:99], v[160:163], a[144:147]\n" \
"ds_read_b128 v[88:91], v48 offset:6144\n" \
"v_mfma_f32_16x16x32_bf16 a[144:147], v[100:103], v[164:167], a[144:147]\n" \
"global_load_dword v253, v248, s[64:65]\n" \
"v_mfma_f32_16x16x32_bf16 a[148:151], v[96:99], v[168:171], a[148:151]\n" \
"s_add_u32 s68, s66, 0x0\n" \
"ds_read_b64_tr_b16 v[148:149], v50 offset:16896\n" \
"v_mfma_f32_16x16x32_bf16 a[148:151], v[100:103], v[172:175], a[148:151]\n" \
"s_add_u32 s64, s8, s68\n" \
"s_addc_u32 s65, s9, 0\n" \
"v_mfma_f32_16x16x32_bf16 a[152:155], v[96:99], v[176:179], a[152:155]\n" \
"ds_read_b128 v[92:95], v49 offset:6144\n" \
"v_mfma_f32_16x16x32_bf16 a[152:155], v[100:103], v[180:183], a[152:155]\n" \
"global_load_dword v253, v248, s[64:65]\n" \
"v_mfma_f32_16x16x32_bf16 a[156:159], v[96:99], v[184:187], a[156:159]\n" \
"ds_read_b64_tr_b16 v[150:151], v50 offset:20992\n" \
"v_mfma_f32_16x16x32_bf16 a[156:159], v[100:103], v[188:191], a[156:159]\n" \
"v_mfma_f32_16x16x32_bf16 a[188:191], v[104:107], v[184:187], a[188:191]\n" \
"ds_read_b128 v[96:99], v48 offset:8192\n" \
"v_mfma_f32_16x16x32_bf16 a[188:191], v[108:111], v[188:191], a[188:191]\n" \
"v_mfma_f32_16x16x32_bf16 a[184:187], v[104:107], v[176:179], a[184:187]\n" \
"ds_read_b64_tr_b16 v[152:153], v51 offset:512\n" \
"v_mfma_f32_16x16x32_bf16 a[184:187], v[108:111], v[180:183], a[184:187]\n" \
"v_mfma_f32_16x16x32_bf16 a[180:183], v[104:107], v[168:171], a[180:183]\n" \
"ds_read_b128 v[100:103], v49 offset:8192\n" \
"v_mfma_f32_16x16x32_bf16 a[180:183], v[108:111], v[172:175], a[180:183]\n" \
"v_mfma_f32_16x16x32_bf16 a[176:179], v[104:107], v[160:163], a[176:179]\n" \
"ds_read_b64_tr_b16 v[154:155], v51 offset:4608\n" \
"v_mfma_f32_16x16x32_bf16 a[176:179], v[108:111], v[164:167], a[176:179]\n" \
"v_mfma_f32_16x16x32_bf16 a[208:211], v[112:115], v[160:163], a[208:211]\n" \
"ds_read_b128 v[104:107], v48 offset:10240\n" \
"v_mfma_f32_16x16x32_bf16 a[208:211], v[116:119], v[164:167], a[208:211]\n" \
"v_mfma_f32_16x16x32_bf16 a[212:215], v[112:115], v[168:171], a[212:215]\n" \
"ds_read_b64_tr_b16 v[156:157], v51 offset:16896\n" \
"v_mfma_f32_16x16x32_bf16 a[212:215], v[116:119], v[172:175], a[212:215]\n" \
"v_mfma_f32_16x16x32_bf16 a[216:219], v[112:115], v[176:179], a[216:219]\n" \
"ds_read_b128 v[108:111], v49 offset:10240\n" \
"v_mfma_f32_16x16x32_bf16 a[216:219], v[116:119], v[180:183], a[216:219]\n" \
"v_mfma_f32_16x16x32_bf16 a[220:223], v[112:115], v[184:187], a[220:223]\n" \
"ds_read_b64_tr_b16 v[158:159], v51 offset:20992\n" \
"v_mfma_f32_16x16x32_bf16 a[220:223], v[116:119], v[188:191], a[220:223]\n" \
"v_mfma_f32_16x16x32_bf16 a[252:255], v[120:123], v[184:187], a[252:255]\n" \
"ds_read_b128 v[112:115], v48 offset:12288\n" \
"v_mfma_f32_16x16x32_bf16 a[252:255], v[124:127], v[188:191], a[252:255]\n" \
"v_mfma_f32_16x16x32_bf16 a[248:251], v[120:123], v[176:179], a[248:251]\n" \
"v_mfma_f32_16x16x32_bf16 a[248:251], v[124:127], v[180:183], a[248:251]\n" \
"v_mfma_f32_16x16x32_bf16 a[244:247], v[120:123], v[168:171], a[244:247]\n" \
"ds_read_b128 v[116:119], v49 offset:12288\n" \
"v_mfma_f32_16x16x32_bf16 a[244:247], v[124:127], v[172:175], a[244:247]\n" \
"v_mfma_f32_16x16x32_bf16 a[240:243], v[120:123], v[160:163], a[240:243]\n" \
"v_mfma_f32_16x16x32_bf16 a[240:243], v[124:127], v[164:167], a[240:243]\n" \
"s_waitcnt lgkmcnt(1)\n" \
"v_mfma_f32_16x16x32_bf16 a[0:3], v[128:131], v[64:67], a[0:3]\n" \
"ds_read_b128 v[120:123], v48 offset:14336\n" \
"v_mfma_f32_16x16x32_bf16 a[0:3], v[132:135], v[68:71], a[0:3]\n" \
"v_mfma_f32_16x16x32_bf16 a[4:7], v[136:139], v[64:67], a[4:7]\n" \
"ds_read_b128 v[124:127], v49 offset:14336\n" \
"v_mfma_f32_16x16x32_bf16 a[4:7], v[140:143], v[68:71], a[4:7]\n" \
"s_add_u32 s68, s66, 0x100000\n" \
"v_mfma_f32_16x16x32_bf16 a[8:11], v[144:147], v[64:67], a[8:11]\n" \
"s_add_u32 s64, s8, s68\n" \
"s_addc_u32 s65, s9, 0\n" \
"ds_read_b64_tr_b16 v[160:161], v50 offset:1024\n" \
"v_mfma_f32_16x16x32_bf16 a[8:11], v[148:151], v[68:71], a[8:11]\n" \
"global_load_dword v253, v248, s[64:65]\n" \
"v_mfma_f32_16x16x32_bf16 a[12:15], v[152:155], v[64:67], a[12:15]\n" \
"s_add_u32 m0, s87, 36864\n" \
"ds_read_b64_tr_b16 v[162:163], v50 offset:5120\n" \
"v_mfma_f32_16x16x32_bf16 a[12:15], v[156:159], v[68:71], a[12:15]\n" \
"global_load_lds_dwordx4 v40, s[52:53] offset:-4096\n" \
"v_mfma_f32_16x16x32_bf16 a[44:47], v[152:155], v[72:75], a[44:47]\n" \
"ds_read_b64_tr_b16 v[164:165], v50 offset:17408\n" \
"v_mfma_f32_16x16x32_bf16 a[44:47], v[156:159], v[76:79], a[44:47]\n" \
"v_mfma_f32_16x16x32_bf16 a[40:43], v[144:147], v[72:75], a[40:43]\n" \
"ds_read_b64_tr_b16 v[166:167], v50 offset:21504\n" \
"v_mfma_f32_16x16x32_bf16 a[40:43], v[148:151], v[76:79], a[40:43]\n" \
"global_load_lds_dwordx4 v41, s[50:51] offset:-3072\n" \
"v_mfma_f32_16x16x32_bf16 a[36:39], v[136:139], v[72:75], a[36:39]\n" \
"ds_read_b64_tr_b16 v[168:169], v51 offset:1024\n" \
"v_mfma_f32_16x16x32_bf16 a[36:39], v[140:143], v[76:79], a[36:39]\n" \
"v_mfma_f32_16x16x32_bf16 a[32:35], v[128:131], v[72:75], a[32:35]\n" \
"ds_read_b64_tr_b16 v[170:171], v51 offset:5120\n" \
"v_mfma_f32_16x16x32_bf16 a[32:35], v[132:135], v[76:79], a[32:35]\n" \
"global_load_lds_dwordx4 v42, s[52:53] offset:-2048\n" \
"v_mfma_f32_16x16x32_bf16 a[64:67], v[128:131], v[80:83], a[64:67]\n" \
"ds_read_b64_tr_b16 v[172:173], v51 offset:17408\n" \
"v_mfma_f32_16x16x32_bf16 a[64:67], v[132:135], v[84:87], a[64:67]\n" \
"v_mfma_f32_16x16x32_bf16 a[68:71], v[136:139], v[80:83], a[68:71]\n" \
"ds_read_b64_tr_b16 v[174:175], v51 offset:21504\n" \
"v_mfma_f32_16x16x32_bf16 a[68:71], v[140:143], v[84:87], a[68:71]\n" \
"global_load_lds_dwordx4 v43, s[50:51] offset:-1024\n" \
"v_mfma_f32_16x16x32_bf16 a[72:75], v[144:147], v[80:83], a[72:75]\n" \
"ds_read_b64_tr_b16 v[176:177], v50 offset:1536\n" \
"v_mfma_f32_16x16x32_bf16 a[72:75], v[148:151], v[84:87], a[72:75]\n" \
"v_mfma_f32_16x16x32_bf16 a[76:79], v[152:155], v[80:83], a[76:79]\n" \
"ds_read_b64_tr_b16 v[178:179], v50 offset:5632\n" \
"v_mfma_f32_16x16x32_bf16 a[76:79], v[156:159], v[84:87], a[76:79]\n" \
"global_load_lds_dwordx4 v44, s[52:53] offset:0\n" \
"v_mfma_f32_16x16x32_bf16 a[108:111], v[152:155], v[88:91], a[108:111]\n" \
"ds_read_b64_tr_b16 v[180:181], v50 offset:17920\n" \
"v_mfma_f32_16x16x32_bf16 a[108:111], v[156:159], v[92:95], a[108:111]\n" \
"v_mfma_f32_16x16x32_bf16 a[104:107], v[144:147], v[88:91], a[104:107]\n" \
"ds_read_b64_tr_b16 v[182:183], v50 offset:22016\n" \
"v_mfma_f32_16x16x32_bf16 a[104:107], v[148:151], v[92:95], a[104:107]\n" \
"global_load_lds_dwordx4 v45, s[50:51] offset:1024\n" \
"v_mfma_f32_16x16x32_bf16 a[100:103], v[136:139], v[88:91], a[100:103]\n" \
"ds_read_b64_tr_b16 v[184:185], v51 offset:1536\n" \
"v_mfma_f32_16x16x32_bf16 a[100:103], v[140:143], v[92:95], a[100:103]\n" \
"v_mfma_f32_16x16x32_bf16 a[96:99], v[128:131], v[88:91], a[96:99]\n" \
"ds_read_b64_tr_b16 v[186:187], v51 offset:5632\n" \
"v_mfma_f32_16x16x32_bf16 a[96:99], v[132:135], v[92:95], a[96:99]\n" \
"global_load_lds_dwordx4 v46, s[52:53] offset:2048\n" \
"v_mfma_f32_16x16x32_bf16 a[128:131], v[128:131], v[96:99], a[128:131]\n" \
"ds_read_b64_tr_b16 v[188:189], v51 offset:17920\n" \
"v_mfma_f32_16x16x32_bf16 a[128:131], v[132:135], v[100:103], a[128:131]\n" \
"v_mfma_f32_16x16x32_bf16 a[132:135], v[136:139], v[96:99], a[132:135]\n" \
"ds_read_b64_tr_b16 v[190:191], v51 offset:22016\n" \
"v_mfma_f32_16x16x32_bf16 a[132:135], v[140:143], v[100:103], a[132:135]\n" \
"global_load_lds_dwordx4 v47, s[50:51] offset:3072\n" \
"v_mfma_f32_16x16x32_bf16 a[136:139], v[144:147], v[96:99], a[136:139]\n" \
"s_add_u32 s52, s52, s74\n" \
"s_addc_u32 s53, s53, 0\n" \
"v_mfma_f32_16x16x32_bf16 a[136:139], v[148:151], v[100:103], a[136:139]\n" \
"s_add_u32 s50, s50, s74\n" \
"s_addc_u32 s51, s51, 0\n" \
"v_mfma_f32_16x16x32_bf16 a[140:143], v[152:155], v[96:99], a[140:143]\n" \
"v_mfma_f32_16x16x32_bf16 a[140:143], v[156:159], v[100:103], a[140:143]\n" \
"v_mfma_f32_16x16x32_bf16 a[172:175], v[152:155], v[104:107], a[172:175]\n" \
"s_mul_i32 s76, s73, 0x18000\n" \
"s_add_u32 s77, s76, 0xffff8000\n" \
"s_sub_u32 s78, 0x10000, s76\n" \
"v_mfma_f32_16x16x32_bf16 a[172:175], v[156:159], v[108:111], a[172:175]\n" \
"v_add_u32 v48, s77, v48\n" \
"v_add_u32 v49, s77, v49\n" \
"v_mfma_f32_16x16x32_bf16 a[168:171], v[144:147], v[104:107], a[168:171]\n" \
"v_add_u32 v50, s78, v50\n" \
"v_add_u32 v51, s78, v51\n" \
"v_mfma_f32_16x16x32_bf16 a[168:171], v[148:151], v[108:111], a[168:171]\n" \
"v_mfma_f32_16x16x32_bf16 a[164:167], v[136:139], v[104:107], a[164:167]\n" \
"v_mfma_f32_16x16x32_bf16 a[164:167], v[140:143], v[108:111], a[164:167]\n" \
"v_mfma_f32_16x16x32_bf16 a[160:163], v[128:131], v[104:107], a[160:163]\n" \
"v_mfma_f32_16x16x32_bf16 a[160:163], v[132:135], v[108:111], a[160:163]\n" \
"s_waitcnt lgkmcnt(0)\n" \
"v_mfma_f32_16x16x32_bf16 a[192:195], v[128:131], v[112:115], a[192:195]\n" \
"v_mfma_f32_16x16x32_bf16 a[192:195], v[132:135], v[116:119], a[192:195]\n" \
"v_mfma_f32_16x16x32_bf16 a[196:199], v[136:139], v[112:115], a[196:199]\n" \
"v_mfma_f32_16x16x32_bf16 a[196:199], v[140:143], v[116:119], a[196:199]\n" \
"v_mfma_f32_16x16x32_bf16 a[200:203], v[144:147], v[112:115], a[200:203]\n" \
"v_mfma_f32_16x16x32_bf16 a[200:203], v[148:151], v[116:119], a[200:203]\n" \
"v_mfma_f32_16x16x32_bf16 a[204:207], v[152:155], v[112:115], a[204:207]\n" \
"v_mfma_f32_16x16x32_bf16 a[204:207], v[156:159], v[116:119], a[204:207]\n" \
"v_mfma_f32_16x16x32_bf16 a[236:239], v[152:155], v[120:123], a[236:239]\n" \
"v_mfma_f32_16x16x32_bf16 a[236:239], v[156:159], v[124:127], a[236:239]\n" \
"v_mfma_f32_16x16x32_bf16 a[232:235], v[144:147], v[120:123], a[232:235]\n" \
"v_mfma_f32_16x16x32_bf16 a[232:235], v[148:151], v[124:127], a[232:235]\n" \
"v_mfma_f32_16x16x32_bf16 a[228:231], v[136:139], v[120:123], a[228:231]\n" \
"v_mfma_f32_16x16x32_bf16 a[228:231], v[140:143], v[124:127], a[228:231]\n" \
"s_waitcnt vmcnt(11) lgkmcnt(0)\n" \
"v_mfma_f32_16x16x32_bf16 a[224:227], v[128:131], v[120:123], a[224:227]\n" \
"v_mfma_f32_16x16x32_bf16 a[224:227], v[132:135], v[124:127], a[224:227]\n" \
"s_barrier\n" \
"v_mfma_f32_16x16x32_bf16 a[16:19], v[64:67], v[160:163], a[16:19]\n" \
"s_add_u32 m0, s87, 4096\n" \
"ds_read_b64_tr_b16 v[128:129], v50 offset:0\n" \
"v_mfma_f32_16x16x32_bf16 a[16:19], v[68:71], v[164:167], a[16:19]\n" \
"global_load_lds_dwordx4 v32, s[54:55] offset:-4096\n" \
"v_mfma_f32_16x16x32_bf16 a[20:23], v[64:67], v[168:171], a[20:23]\n" \
"ds_read_b64_tr_b16 v[130:131], v50 offset:4096\n" \
"v_mfma_f32_16x16x32_bf16 a[20:23], v[68:71], v[172:175], a[20:23]\n" \
"v_mfma_f32_16x16x32_bf16 a[24:27], v[64:67], v[176:179], a[24:27]\n" \
"ds_read_b64_tr_b16 v[132:133], v50 offset:16384\n" \
"v_mfma_f32_16x16x32_bf16 a[24:27], v[68:71], v[180:183], a[24:27]\n" \
"global_load_lds_dwordx4 v33, s[40:41] offset:-3072\n" \
"v_mfma_f32_16x16x32_bf16 a[28:31], v[64:67], v[184:187], a[28:31]\n" \
"ds_read_b64_tr_b16 v[134:135], v50 offset:20480\n" \
"v_mfma_f32_16x16x32_bf16 a[28:31], v[68:71], v[188:191], a[28:31]\n" \
"v_mfma_f32_16x16x32_bf16 a[60:63], v[72:75], v[184:187], a[60:63]\n" \
"ds_read_b128 v[64:67], v48 offset:0\n" \
"v_mfma_f32_16x16x32_bf16 a[60:63], v[76:79], v[188:191], a[60:63]\n" \
"global_load_lds_dwordx4 v34, s[54:55] offset:-2048\n" \
"v_mfma_f32_16x16x32_bf16 a[56:59], v[72:75], v[176:179], a[56:59]\n" \
"ds_read_b64_tr_b16 v[136:137], v51 offset:0\n" \
"v_mfma_f32_16x16x32_bf16 a[56:59], v[76:79], v[180:183], a[56:59]\n" \
"v_mfma_f32_16x16x32_bf16 a[52:55], v[72:75], v[168:171], a[52:55]\n" \
"ds_read_b128 v[68:71], v49 offset:0\n" \
"v_mfma_f32_16x16x32_bf16 a[52:55], v[76:79], v[172:175], a[52:55]\n" \
"global_load_lds_dwordx4 v35, s[40:41] offset:-1024\n" \
"v_mfma_f32_16x16x32_bf16 a[48:51], v[72:75], v[160:163], a[48:51]\n" \
"ds_read_b64_tr_b16 v[138:139], v51 offset:4096\n" \
"v_mfma_f32_16x16x32_bf16 a[48:51], v[76:79], v[164:167], a[48:51]\n" \
"v_mfma_f32_16x16x32_bf16 a[80:83], v[80:83], v[160:163], a[80:83]\n" \
"ds_read_b128 v[72:75], v48 offset:2048\n" \
"v_mfma_f32_16x16x32_bf16 a[80:83], v[84:87], v[164:167], a[80:83]\n" \
"global_load_lds_dwordx4 v36, s[54:55] offset:0\n" \
"v_mfma_f32_16x16x32_bf16 a[84:87], v[80:83], v[168:171], a[84:87]\n" \
"ds_read_b64_tr_b16 v[140:141], v51 offset:16384\n" \
"v_mfma_f32_16x16x32_bf16 a[84:87], v[84:87], v[172:175], a[84:87]\n" \
"v_mfma_f32_16x16x32_bf16 a[88:91], v[80:83], v[176:179], a[88:91]\n" \
"ds_read_b128 v[76:79], v49 offset:2048\n" \
"v_mfma_f32_16x16x32_bf16 a[88:91], v[84:87], v[180:183], a[88:91]\n" \
"global_load_lds_dwordx4 v37, s[40:41] offset:1024\n" \
"v_mfma_f32_16x16x32_bf16 a[92:95], v[80:83], v[184:187], a[92:95]\n" \
"ds_read_b64_tr_b16 v[142:143], v51 offset:20480\n" \
"v_mfma_f32_16x16x32_bf16 a[92:95], v[84:87], v[188:191], a[92:95]\n" \
"v_mfma_f32_16x16x32_bf16 a[124:127], v[88:91], v[184:187], a[124:127]\n" \
"ds_read_b128 v[80:83], v48 offset:4096\n" \
"v_mfma_f32_16x16x32_bf16 a[124:127], v[92:95], v[188:191], a[124:127]\n" \
"global_load_lds_dwordx4 v38, s[54:55] offset:2048\n" \
"v_mfma_f32_16x16x32_bf16 a[120:123], v[88:91], v[176:179], a[120:123]\n" \
"ds_read_b64_tr_b16 v[144:145], v50 offset:512\n" \
"v_mfma_f32_16x16x32_bf16 a[120:123], v[92:95], v[180:183], a[120:123]\n" \
"v_mfma_f32_16x16x32_bf16 a[116:119], v[88:91], v[168:171], a[116:119]\n" \
"ds_read_b128 v[84:87], v49 offset:4096\n" \
"global_load_lds_dwordx4 v39, s[40:41] offset:3072\n" \
"v_mfma_f32_16x16x32_bf16 a[116:119], v[92:95], v[172:175], a[116:119]\n" \
"s_add_u32 s54, s54, s75\n" \
"s_addc_u32 s55, s55, 0\n" \
"v_mfma_f32_16x16x32_bf16 a[112:115], v[88:91], v[160:163], a[112:115]\n" \
"s_add_u32 s40, s40, s75\n" \
"s_addc_u32 s41, s41, 0\n" \
"ds_read_b64_tr_b16 v[146:147], v50 offset:4608\n" \
"v_mfma_f32_16x16x32_bf16 a[112:115], v[92:95], v[164:167], a[112:115]\n" \
"s_add_u32 s68, s66, 0x200000\n" \
"s_add_u32 s64, s8, s68\n" \
"s_addc_u32 s65, s9, 0\n" \
"v_mfma_f32_16x16x32_bf16 a[144:147], v[96:99], v[160:163], a[144:147]\n" \
"ds_read_b128 v[88:91], v48 offset:6144\n" \
"v_mfma_f32_16x16x32_bf16 a[144:147], v[100:103], v[164:167], a[144:147]\n" \
"global_load_dword v253, v248, s[64:65]\n" \
"v_mfma_f32_16x16x32_bf16 a[148:151], v[96:99], v[168:171], a[148:151]\n" \
"s_add_u32 s68, s67, 0x0\n" \
"ds_read_b64_tr_b16 v[148:149], v50 offset:16896\n" \
"v_mfma_f32_16x16x32_bf16 a[148:151], v[100:103], v[172:175], a[148:151]\n" \
"s_add_u32 s64, s12, s68\n" \
"Lpg1151_%=:\n" \
"Lst0x1_%=:\n" \
"s_cbranch_execz Lpg3309_%=\n" \
"s_addc_u32 s65, s13, 0\n" \
"v_mfma_f32_16x16x32_bf16 a[152:155], v[96:99], v[176:179], a[152:155]\n" \
"ds_read_b128 v[92:95], v49 offset:6144\n" \
"v_mfma_f32_16x16x32_bf16 a[152:155], v[100:103], v[180:183], a[152:155]\n" \
"global_load_dword v253, v248, s[64:65]\n" \
"v_mfma_f32_16x16x32_bf16 a[156:159], v[96:99], v[184:187], a[156:159]\n" \
"ds_read_b64_tr_b16 v[150:151], v50 offset:20992\n" \
"v_mfma_f32_16x16x32_bf16 a[156:159], v[100:103], v[188:191], a[156:159]\n" \
"v_mfma_f32_16x16x32_bf16 a[188:191], v[104:107], v[184:187], a[188:191]\n" \
"ds_read_b128 v[96:99], v48 offset:8192\n" \
"v_mfma_f32_16x16x32_bf16 a[188:191], v[108:111], v[188:191], a[188:191]\n" \
"v_mfma_f32_16x16x32_bf16 a[184:187], v[104:107], v[176:179], a[184:187]\n" \
"ds_read_b64_tr_b16 v[152:153], v51 offset:512\n" \
"v_mfma_f32_16x16x32_bf16 a[184:187], v[108:111], v[180:183], a[184:187]\n" \
"v_mfma_f32_16x16x32_bf16 a[180:183], v[104:107], v[168:171], a[180:183]\n" \
"ds_read_b128 v[100:103], v49 offset:8192\n" \
"v_mfma_f32_16x16x32_bf16 a[180:183], v[108:111], v[172:175], a[180:183]\n" \
"v_mfma_f32_16x16x32_bf16 a[176:179], v[104:107], v[160:163], a[176:179]\n" \
"ds_read_b64_tr_b16 v[154:155], v51 offset:4608\n" \
"v_mfma_f32_16x16x32_bf16 a[176:179], v[108:111], v[164:167], a[176:179]\n" \
"v_mfma_f32_16x16x32_bf16 a[208:211], v[112:115], v[160:163], a[208:211]\n" \
"ds_read_b128 v[104:107], v48 offset:10240\n" \
"v_mfma_f32_16x16x32_bf16 a[208:211], v[116:119], v[164:167], a[208:211]\n" \
"v_mfma_f32_16x16x32_bf16 a[212:215], v[112:115], v[168:171], a[212:215]\n" \
"ds_read_b64_tr_b16 v[156:157], v51 offset:16896\n" \
"v_mfma_f32_16x16x32_bf16 a[212:215], v[116:119], v[172:175], a[212:215]\n" \
"v_mfma_f32_16x16x32_bf16 a[216:219], v[112:115], v[176:179], a[216:219]\n" \
"ds_read_b128 v[108:111], v49 offset:10240\n" \
"v_mfma_f32_16x16x32_bf16 a[216:219], v[116:119], v[180:183], a[216:219]\n" \
"v_mfma_f32_16x16x32_bf16 a[220:223], v[112:115], v[184:187], a[220:223]\n" \
"ds_read_b64_tr_b16 v[158:159], v51 offset:20992\n" \
"v_mfma_f32_16x16x32_bf16 a[220:223], v[116:119], v[188:191], a[220:223]\n" \
"v_mfma_f32_16x16x32_bf16 a[252:255], v[120:123], v[184:187], a[252:255]\n" \
"ds_read_b128 v[112:115], v48 offset:12288\n" \
"v_mfma_f32_16x16x32_bf16 a[252:255], v[124:127], v[188:191], a[252:255]\n" \
"v_mfma_f32_16x16x32_bf16 a[248:251], v[120:123], v[176:179], a[248:251]\n" \
"v_mfma_f32_16x16x32_bf16 a[248:251], v[124:127], v[180:183], a[248:251]\n" \
"v_mfma_f32_16x16x32_bf16 a[244:247], v[120:123], v[168:171], a[244:247]\n" \
"ds_read_b128 v[116:119], v49 offset:12288\n" \
"v_mfma_f32_16x16x32_bf16 a[244:247], v[124:127], v[172:175], a[244:247]\n" \
"v_mfma_f32_16x16x32_bf16 a[240:243], v[120:123], v[160:163], a[240:243]\n" \
"v_mfma_f32_16x16x32_bf16 a[240:243], v[124:127], v[164:167], a[240:243]\n" \
"s_waitcnt lgkmcnt(1)\n" \
"v_mfma_f32_16x16x32_bf16 a[0:3], v[128:131], v[64:67], a[0:3]\n" \
"ds_read_b128 v[120:123], v48 offset:14336\n" \
"v_mfma_f32_16x16x32_bf16 a[0:3], v[132:135], v[68:71], a[0:3]\n" \
"v_mfma_f32_16x16x32_bf16 a[4:7], v[136:139], v[64:67], a[4:7]\n" \
"ds_read_b128 v[124:127], v49 offset:14336\n" \
"v_mfma_f32_16x16x32_bf16 a[4:7], v[140:143], v[68:71], a[4:7]\n" \
"s_add_u32 s68, s66, 0x300000\n" \
"v_mfma_f32_16x16x32_bf16 a[8:11], v[144:147], v[64:67], a[8:11]\n" \
"s_add_u32 s64, s8, s68\n" \
"s_addc_u32 s65, s9, 0\n" \
"ds_read_b64_tr_b16 v[160:161], v50 offset:1024\n" \
"v_mfma_f32_16x16x32_bf16 a[8:11], v[148:151], v[68:71], a[8:11]\n" \
"global_load_dword v253, v248, s[64:65]\n" \
"v_mfma_f32_16x16x32_bf16 a[12:15], v[152:155], v[64:67], a[12:15]\n" \
"s_add_u32 m0, s87, 135168\n" \
"ds_read_b64_tr_b16 v[162:163], v50 offset:5120\n" \
"v_mfma_f32_16x16x32_bf16 a[12:15], v[156:159], v[68:71], a[12:15]\n" \
"global_load_lds_dwordx4 v40, s[52:53] offset:-4096\n" \
"v_mfma_f32_16x16x32_bf16 a[44:47], v[152:155], v[72:75], a[44:47]\n" \
"ds_read_b64_tr_b16 v[164:165], v50 offset:17408\n" \
"v_mfma_f32_16x16x32_bf16 a[44:47], v[156:159], v[76:79], a[44:47]\n" \
"v_mfma_f32_16x16x32_bf16 a[40:43], v[144:147], v[72:75], a[40:43]\n" \
"ds_read_b64_tr_b16 v[166:167], v50 offset:21504\n" \
"v_mfma_f32_16x16x32_bf16 a[40:43], v[148:151], v[76:79], a[40:43]\n" \
"global_load_lds_dwordx4 v41, s[50:51] offset:-3072\n" \
"v_mfma_f32_16x16x32_bf16 a[36:39], v[136:139], v[72:75], a[36:39]\n" \
"ds_read_b64_tr_b16 v[168:169], v51 offset:1024\n" \
"v_mfma_f32_16x16x32_bf16 a[36:39], v[140:143], v[76:79], a[36:39]\n" \
"v_mfma_f32_16x16x32_bf16 a[32:35], v[128:131], v[72:75], a[32:35]\n" \
"ds_read_b64_tr_b16 v[170:171], v51 offset:5120\n" \
"v_mfma_f32_16x16x32_bf16 a[32:35], v[132:135], v[76:79], a[32:35]\n" \
"global_load_lds_dwordx4 v42, s[52:53] offset:-2048\n" \
"v_mfma_f32_16x16x32_bf16 a[64:67], v[128:131], v[80:83], a[64:67]\n" \
"ds_read_b64_tr_b16 v[172:173], v51 offset:17408\n" \
"v_mfma_f32_16x16x32_bf16 a[64:67], v[132:135], v[84:87], a[64:67]\n" \
"v_mfma_f32_16x16x32_bf16 a[68:71], v[136:139], v[80:83], a[68:71]\n" \
"ds_read_b64_tr_b16 v[174:175], v51 offset:21504\n" \
"v_mfma_f32_16x16x32_bf16 a[68:71], v[140:143], v[84:87], a[68:71]\n" \
"global_load_lds_dwordx4 v43, s[50:51] offset:-1024\n" \
"v_mfma_f32_16x16x32_bf16 a[72:75], v[144:147], v[80:83], a[72:75]\n" \
"ds_read_b64_tr_b16 v[176:177], v50 offset:1536\n" \
"v_mfma_f32_16x16x32_bf16 a[72:75], v[148:151], v[84:87], a[72:75]\n" \
"v_mfma_f32_16x16x32_bf16 a[76:79], v[152:155], v[80:83], a[76:79]\n" \
"ds_read_b64_tr_b16 v[178:179], v50 offset:5632\n" \
"v_mfma_f32_16x16x32_bf16 a[76:79], v[156:159], v[84:87], a[76:79]\n" \
"global_load_lds_dwordx4 v44, s[52:53] offset:0\n" \
"v_mfma_f32_16x16x32_bf16 a[108:111], v[152:155], v[88:91], a[108:111]\n" \
"ds_read_b64_tr_b16 v[180:181], v50 offset:17920\n" \
"v_mfma_f32_16x16x32_bf16 a[108:111], v[156:159], v[92:95], a[108:111]\n" \
"v_mfma_f32_16x16x32_bf16 a[104:107], v[144:147], v[88:91], a[104:107]\n" \
"ds_read_b64_tr_b16 v[182:183], v50 offset:22016\n" \
"v_mfma_f32_16x16x32_bf16 a[104:107], v[148:151], v[92:95], a[104:107]\n" \
"global_load_lds_dwordx4 v45, s[50:51] offset:1024\n" \
"v_mfma_f32_16x16x32_bf16 a[100:103], v[136:139], v[88:91], a[100:103]\n" \
"ds_read_b64_tr_b16 v[184:185], v51 offset:1536\n" \
"v_mfma_f32_16x16x32_bf16 a[100:103], v[140:143], v[92:95], a[100:103]\n" \
"v_mfma_f32_16x16x32_bf16 a[96:99], v[128:131], v[88:91], a[96:99]\n" \
"ds_read_b64_tr_b16 v[186:187], v51 offset:5632\n" \
"v_mfma_f32_16x16x32_bf16 a[96:99], v[132:135], v[92:95], a[96:99]\n" \
"global_load_lds_dwordx4 v46, s[52:53] offset:2048\n" \
"v_mfma_f32_16x16x32_bf16 a[128:131], v[128:131], v[96:99], a[128:131]\n" \
"ds_read_b64_tr_b16 v[188:189], v51 offset:17920\n" \
"v_mfma_f32_16x16x32_bf16 a[128:131], v[132:135], v[100:103], a[128:131]\n" \
"v_mfma_f32_16x16x32_bf16 a[132:135], v[136:139], v[96:99], a[132:135]\n" \
"ds_read_b64_tr_b16 v[190:191], v51 offset:22016\n" \
"v_mfma_f32_16x16x32_bf16 a[132:135], v[140:143], v[100:103], a[132:135]\n" \
"global_load_lds_dwordx4 v47, s[50:51] offset:3072\n" \
"v_mfma_f32_16x16x32_bf16 a[136:139], v[144:147], v[96:99], a[136:139]\n" \
"s_add_u32 s52, s52, s74\n" \
"s_addc_u32 s53, s53, 0\n" \
"v_mfma_f32_16x16x32_bf16 a[136:139], v[148:151], v[100:103], a[136:139]\n" \
"s_add_u32 s50, s50, s74\n" \
"s_addc_u32 s51, s51, 0\n" \
"v_mfma_f32_16x16x32_bf16 a[140:143], v[152:155], v[96:99], a[140:143]\n" \
"v_mfma_f32_16x16x32_bf16 a[140:143], v[156:159], v[100:103], a[140:143]\n" \
"v_mfma_f32_16x16x32_bf16 a[172:175], v[152:155], v[104:107], a[172:175]\n" \
"s_mul_i32 s76, s73, 0x10000\n" \
"s_add_u32 s77, s76, 0xfffe8000\n" \
"s_sub_u32 s78, 0xffff8000, s76\n" \
"v_mfma_f32_16x16x32_bf16 a[172:175], v[156:159], v[108:111], a[172:175]\n" \
"v_add_u32 v48, s77, v48\n" \
"v_add_u32 v49, s77, v49\n" \
"v_mfma_f32_16x16x32_bf16 a[168:171], v[144:147], v[104:107], a[168:171]\n" \
"v_add_u32 v50, s78, v50\n" \
"v_add_u32 v51, s78, v51\n" \
"v_mfma_f32_16x16x32_bf16 a[168:171], v[148:151], v[108:111], a[168:171]\n" \
"v_mfma_f32_16x16x32_bf16 a[164:167], v[136:139], v[104:107], a[164:167]\n" \
"v_mfma_f32_16x16x32_bf16 a[164:167], v[140:143], v[108:111], a[164:167]\n" \
"v_mfma_f32_16x16x32_bf16 a[160:163], v[128:131], v[104:107], a[160:163]\n" \
"v_mfma_f32_16x16x32_bf16 a[160:163], v[132:135], v[108:111], a[160:163]\n" \
"s_waitcnt lgkmcnt(0)\n" \
"v_mfma_f32_16x16x32_bf16 a[192:195], v[128:131], v[112:115], a[192:195]\n" \
"v_mfma_f32_16x16x32_bf16 a[192:195], v[132:135], v[116:119], a[192:195]\n" \
"v_mfma_f32_16x16x32_bf16 a[196:199], v[136:139], v[112:115], a[196:199]\n" \
"v_mfma_f32_16x16x32_bf16 a[196:199], v[140:143], v[116:119], a[196:199]\n" \
"v_mfma_f32_16x16x32_bf16 a[200:203], v[144:147], v[112:115], a[200:203]\n" \
"v_mfma_f32_16x16x32_bf16 a[200:203], v[148:151], v[116:119], a[200:203]\n" \
"v_mfma_f32_16x16x32_bf16 a[204:207], v[152:155], v[112:115], a[204:207]\n" \
"v_mfma_f32_16x16x32_bf16 a[204:207], v[156:159], v[116:119], a[204:207]\n" \
"v_mfma_f32_16x16x32_bf16 a[236:239], v[152:155], v[120:123], a[236:239]\n" \
"v_mfma_f32_16x16x32_bf16 a[236:239], v[156:159], v[124:127], a[236:239]\n" \
"v_mfma_f32_16x16x32_bf16 a[232:235], v[144:147], v[120:123], a[232:235]\n" \
"v_mfma_f32_16x16x32_bf16 a[232:235], v[148:151], v[124:127], a[232:235]\n" \
"v_mfma_f32_16x16x32_bf16 a[228:231], v[136:139], v[120:123], a[228:231]\n" \
"v_mfma_f32_16x16x32_bf16 a[228:231], v[140:143], v[124:127], a[228:231]\n" \
"s_waitcnt vmcnt(11) lgkmcnt(0)\n" \
"v_mfma_f32_16x16x32_bf16 a[224:227], v[128:131], v[120:123], a[224:227]\n" \
"v_mfma_f32_16x16x32_bf16 a[224:227], v[132:135], v[124:127], a[224:227]\n" \
"s_barrier\n" \
"v_mfma_f32_16x16x32_bf16 a[16:19], v[64:67], v[160:163], a[16:19]\n" \
"s_add_u32 m0, s87, 69632\n" \
"ds_read_b64_tr_b16 v[128:129], v50 offset:0\n" \
"v_mfma_f32_16x16x32_bf16 a[16:19], v[68:71], v[164:167], a[16:19]\n" \
"global_load_lds_dwordx4 v32, s[54:55] offset:-4096\n" \
"v_mfma_f32_16x16x32_bf16 a[20:23], v[64:67], v[168:171], a[20:23]\n" \
"ds_read_b64_tr_b16 v[130:131], v50 offset:4096\n" \
"v_mfma_f32_16x16x32_bf16 a[20:23], v[68:71], v[172:175], a[20:23]\n" \
"v_mfma_f32_16x16x32_bf16 a[24:27], v[64:67], v[176:179], a[24:27]\n" \
"ds_read_b64_tr_b16 v[132:133], v50 offset:16384\n" \
"v_mfma_f32_16x16x32_bf16 a[24:27], v[68:71], v[180:183], a[24:27]\n" \
"global_load_lds_dwordx4 v33, s[40:41] offset:-3072\n" \
"v_mfma_f32_16x16x32_bf16 a[28:31], v[64:67], v[184:187], a[28:31]\n" \
"ds_read_b64_tr_b16 v[134:135], v50 offset:20480\n" \
"v_mfma_f32_16x16x32_bf16 a[28:31], v[68:71], v[188:191], a[28:31]\n" \
"v_mfma_f32_16x16x32_bf16 a[60:63], v[72:75], v[184:187], a[60:63]\n" \
"ds_read_b128 v[64:67], v48 offset:0\n" \
"v_mfma_f32_16x16x32_bf16 a[60:63], v[76:79], v[188:191], a[60:63]\n" \
"global_load_lds_dwordx4 v34, s[54:55] offset:-2048\n" \
"v_mfma_f32_16x16x32_bf16 a[56:59], v[72:75], v[176:179], a[56:59]\n" \
"ds_read_b64_tr_b16 v[136:137], v51 offset:0\n" \
"v_mfma_f32_16x16x32_bf16 a[56:59], v[76:79], v[180:183], a[56:59]\n" \
"v_mfma_f32_16x16x32_bf16 a[52:55], v[72:75], v[168:171], a[52:55]\n" \
"ds_read_b128 v[68:71], v49 offset:0\n" \
"v_mfma_f32_16x16x32_bf16 a[52:55], v[76:79], v[172:175], a[52:55]\n" \
"global_load_lds_dwordx4 v35, s[40:41] offset:-1024\n" \
"v_mfma_f32_16x16x32_bf16 a[48:51], v[72:75], v[160:163], a[48:51]\n" \
"ds_read_b64_tr_b16 v[138:139], v51 offset:4096\n" \
"v_mfma_f32_16x16x32_bf16 a[48:51], v[76:79], v[164:167], a[48:51]\n" \
"v_mfma_f32_16x16x32_bf16 a[80:83], v[80:83], v[160:163], a[80:83]\n" \
"ds_read_b128 v[72:75], v48 offset:2048\n" \
"v_mfma_f32_16x16x32_bf16 a[80:83], v[84:87], v[164:167], a[80:83]\n" \
"global_load_lds_dwordx4 v36, s[54:55] offset:0\n" \
"v_mfma_f32_16x16x32_bf16 a[84:87], v[80:83], v[168:171], a[84:87]\n" \
"ds_read_b64_tr_b16 v[140:141], v51 offset:16384\n" \
"v_mfma_f32_16x16x32_bf16 a[84:87], v[84:87], v[172:175], a[84:87]\n" \
"v_mfma_f32_16x16x32_bf16 a[88:91], v[80:83], v[176:179], a[88:91]\n" \
"ds_read_b128 v[76:79], v49 offset:2048\n" \
"v_mfma_f32_16x16x32_bf16 a[88:91], v[84:87], v[180:183], a[88:91]\n" \
"global_load_lds_dwordx4 v37, s[40:41] offset:1024\n" \
"v_mfma_f32_16x16x32_bf16 a[92:95], v[80:83], v[184:187], a[92:95]\n" \
"ds_read_b64_tr_b16 v[142:143], v51 offset:20480\n" \
"v_mfma_f32_16x16x32_bf16 a[92:95], v[84:87], v[188:191], a[92:95]\n" \
"v_mfma_f32_16x16x32_bf16 a[124:127], v[88:91], v[184:187], a[124:127]\n" \
"ds_read_b128 v[80:83], v48 offset:4096\n" \
"v_mfma_f32_16x16x32_bf16 a[124:127], v[92:95], v[188:191], a[124:127]\n" \
"global_load_lds_dwordx4 v38, s[54:55] offset:2048\n" \
"v_mfma_f32_16x16x32_bf16 a[120:123], v[88:91], v[176:179], a[120:123]\n" \
"ds_read_b64_tr_b16 v[144:145], v50 offset:512\n" \
"v_mfma_f32_16x16x32_bf16 a[120:123], v[92:95], v[180:183], a[120:123]\n" \
"v_mfma_f32_16x16x32_bf16 a[116:119], v[88:91], v[168:171], a[116:119]\n" \
"ds_read_b128 v[84:87], v49 offset:4096\n" \
"global_load_lds_dwordx4 v39, s[40:41] offset:3072\n" \
"v_mfma_f32_16x16x32_bf16 a[116:119], v[92:95], v[172:175], a[116:119]\n" \
"s_add_u32 s54, s54, s75\n" \
"s_addc_u32 s55, s55, 0\n" \
"v_mfma_f32_16x16x32_bf16 a[112:115], v[88:91], v[160:163], a[112:115]\n" \
"s_add_u32 s40, s40, s75\n" \
"s_addc_u32 s41, s41, 0\n" \
"ds_read_b64_tr_b16 v[146:147], v50 offset:4608\n" \
"v_mfma_f32_16x16x32_bf16 a[112:115], v[92:95], v[164:167], a[112:115]\n" \
"s_add_u32 s68, s67, 0x100000\n" \
"s_add_u32 s64, s12, s68\n" \
"s_addc_u32 s65, s13, 0\n" \
"v_mfma_f32_16x16x32_bf16 a[144:147], v[96:99], v[160:163], a[144:147]\n" \
"ds_read_b128 v[88:91], v48 offset:6144\n" \
"v_mfma_f32_16x16x32_bf16 a[144:147], v[100:103], v[164:167], a[144:147]\n" \
"global_load_dword v253, v248, s[64:65]\n" \
"v_mfma_f32_16x16x32_bf16 a[148:151], v[96:99], v[168:171], a[148:151]\n" \
"s_add_u32 s68, s67, 0x500000\n" \
"ds_read_b64_tr_b16 v[148:149], v50 offset:16896\n" \
"v_mfma_f32_16x16x32_bf16 a[148:151], v[100:103], v[172:175], a[148:151]\n" \
"s_add_u32 s64, s12, s68\n" \
"s_addc_u32 s65, s13, 0\n" \
"v_mfma_f32_16x16x32_bf16 a[152:155], v[96:99], v[176:179], a[152:155]\n" \
"ds_read_b128 v[92:95], v49 offset:6144\n" \
"v_mfma_f32_16x16x32_bf16 a[152:155], v[100:103], v[180:183], a[152:155]\n" \
"global_load_dword v253, v248, s[64:65]\n" \
"v_mfma_f32_16x16x32_bf16 a[156:159], v[96:99], v[184:187], a[156:159]\n" \
"ds_read_b64_tr_b16 v[150:151], v50 offset:20992\n" \
"v_mfma_f32_16x16x32_bf16 a[156:159], v[100:103], v[188:191], a[156:159]\n" \
"v_mfma_f32_16x16x32_bf16 a[188:191], v[104:107], v[184:187], a[188:191]\n" \
"ds_read_b128 v[96:99], v48 offset:8192\n" \
"v_mfma_f32_16x16x32_bf16 a[188:191], v[108:111], v[188:191], a[188:191]\n" \
"v_mfma_f32_16x16x32_bf16 a[184:187], v[104:107], v[176:179], a[184:187]\n" \
"ds_read_b64_tr_b16 v[152:153], v51 offset:512\n" \
"v_mfma_f32_16x16x32_bf16 a[184:187], v[108:111], v[180:183], a[184:187]\n" \
"v_mfma_f32_16x16x32_bf16 a[180:183], v[104:107], v[168:171], a[180:183]\n" \
"ds_read_b128 v[100:103], v49 offset:8192\n" \
"v_mfma_f32_16x16x32_bf16 a[180:183], v[108:111], v[172:175], a[180:183]\n" \
"v_mfma_f32_16x16x32_bf16 a[176:179], v[104:107], v[160:163], a[176:179]\n" \
"ds_read_b64_tr_b16 v[154:155], v51 offset:4608\n" \
"v_mfma_f32_16x16x32_bf16 a[176:179], v[108:111], v[164:167], a[176:179]\n" \
"v_mfma_f32_16x16x32_bf16 a[208:211], v[112:115], v[160:163], a[208:211]\n" \
"ds_read_b128 v[104:107], v48 offset:10240\n" \
"v_mfma_f32_16x16x32_bf16 a[208:211], v[116:119], v[164:167], a[208:211]\n" \
"v_mfma_f32_16x16x32_bf16 a[212:215], v[112:115], v[168:171], a[212:215]\n" \
"ds_read_b64_tr_b16 v[156:157], v51 offset:16896\n" \
"v_mfma_f32_16x16x32_bf16 a[212:215], v[116:119], v[172:175], a[212:215]\n" \
"v_mfma_f32_16x16x32_bf16 a[216:219], v[112:115], v[176:179], a[216:219]\n" \
"ds_read_b128 v[108:111], v49 offset:10240\n" \
"v_mfma_f32_16x16x32_bf16 a[216:219], v[116:119], v[180:183], a[216:219]\n" \
"v_mfma_f32_16x16x32_bf16 a[220:223], v[112:115], v[184:187], a[220:223]\n" \
"ds_read_b64_tr_b16 v[158:159], v51 offset:20992\n" \
"v_mfma_f32_16x16x32_bf16 a[220:223], v[116:119], v[188:191], a[220:223]\n" \
"v_mfma_f32_16x16x32_bf16 a[252:255], v[120:123], v[184:187], a[252:255]\n" \
"ds_read_b128 v[112:115], v48 offset:12288\n" \
"v_mfma_f32_16x16x32_bf16 a[252:255], v[124:127], v[188:191], a[252:255]\n" \
"v_mfma_f32_16x16x32_bf16 a[248:251], v[120:123], v[176:179], a[248:251]\n" \
"v_mfma_f32_16x16x32_bf16 a[248:251], v[124:127], v[180:183], a[248:251]\n" \
"v_mfma_f32_16x16x32_bf16 a[244:247], v[120:123], v[168:171], a[244:247]\n" \
"ds_read_b128 v[116:119], v49 offset:12288\n" \
"v_mfma_f32_16x16x32_bf16 a[244:247], v[124:127], v[172:175], a[244:247]\n" \
"v_mfma_f32_16x16x32_bf16 a[240:243], v[120:123], v[160:163], a[240:243]\n" \
"v_mfma_f32_16x16x32_bf16 a[240:243], v[124:127], v[164:167], a[240:243]\n" \
"s_waitcnt lgkmcnt(1)\n" \
"v_mfma_f32_16x16x32_bf16 a[0:3], v[128:131], v[64:67], a[0:3]\n" \
"ds_read_b128 v[120:123], v48 offset:14336\n" \
"v_mfma_f32_16x16x32_bf16 a[0:3], v[132:135], v[68:71], a[0:3]\n" \
"v_mfma_f32_16x16x32_bf16 a[4:7], v[136:139], v[64:67], a[4:7]\n" \
"ds_read_b128 v[124:127], v49 offset:14336\n" \
"v_mfma_f32_16x16x32_bf16 a[4:7], v[140:143], v[68:71], a[4:7]\n" \
"s_add_u32 s68, s67, 0x200000\n" \
"v_mfma_f32_16x16x32_bf16 a[8:11], v[144:147], v[64:67], a[8:11]\n" \
"s_add_u32 s64, s12, s68\n" \
"s_addc_u32 s65, s13, 0\n" \
"ds_read_b64_tr_b16 v[160:161], v50 offset:1024\n" \
"v_mfma_f32_16x16x32_bf16 a[8:11], v[148:151], v[68:71], a[8:11]\n" \
"global_load_dword v253, v248, s[64:65]\n" \
"v_mfma_f32_16x16x32_bf16 a[12:15], v[152:155], v[64:67], a[12:15]\n" \
"s_add_u32 m0, s87, 102400\n" \
"ds_read_b64_tr_b16 v[162:163], v50 offset:5120\n" \
"v_mfma_f32_16x16x32_bf16 a[12:15], v[156:159], v[68:71], a[12:15]\n" \
"global_load_lds_dwordx4 v40, s[52:53] offset:-4096\n" \
"v_mfma_f32_16x16x32_bf16 a[44:47], v[152:155], v[72:75], a[44:47]\n" \
"ds_read_b64_tr_b16 v[164:165], v50 offset:17408\n" \
"v_mfma_f32_16x16x32_bf16 a[44:47], v[156:159], v[76:79], a[44:47]\n" \
"v_mfma_f32_16x16x32_bf16 a[40:43], v[144:147], v[72:75], a[40:43]\n" \
"ds_read_b64_tr_b16 v[166:167], v50 offset:21504\n" \
"v_mfma_f32_16x16x32_bf16 a[40:43], v[148:151], v[76:79], a[40:43]\n" \
"global_load_lds_dwordx4 v41, s[50:51] offset:-3072\n" \
"v_mfma_f32_16x16x32_bf16 a[36:39], v[136:139], v[72:75], a[36:39]\n" \
"ds_read_b64_tr_b16 v[168:169], v51 offset:1024\n" \
"v_mfma_f32_16x16x32_bf16 a[36:39], v[140:143], v[76:79], a[36:39]\n" \
"v_mfma_f32_16x16x32_bf16 a[32:35], v[128:131], v[72:75], a[32:35]\n" \
"ds_read_b64_tr_b16 v[170:171], v51 offset:5120\n" \
"v_mfma_f32_16x16x32_bf16 a[32:35], v[132:135], v[76:79], a[32:35]\n" \
"global_load_lds_dwordx4 v42, s[52:53] offset:-2048\n" \
"v_mfma_f32_16x16x32_bf16 a[64:67], v[128:131], v[80:83], a[64:67]\n" \
"ds_read_b64_tr_b16 v[172:173], v51 offset:17408\n" \
"v_mfma_f32_16x16x32_bf16 a[64:67], v[132:135], v[84:87], a[64:67]\n" \
"v_mfma_f32_16x16x32_bf16 a[68:71], v[136:139], v[80:83], a[68:71]\n" \
"ds_read_b64_tr_b16 v[174:175], v51 offset:21504\n" \
"v_mfma_f32_16x16x32_bf16 a[68:71], v[140:143], v[84:87], a[68:71]\n" \
"global_load_lds_dwordx4 v43, s[50:51] offset:-1024\n" \
"v_mfma_f32_16x16x32_bf16 a[72:75], v[144:147], v[80:83], a[72:75]\n" \
"ds_read_b64_tr_b16 v[176:177], v50 offset:1536\n" \
"v_mfma_f32_16x16x32_bf16 a[72:75], v[148:151], v[84:87], a[72:75]\n" \
"v_mfma_f32_16x16x32_bf16 a[76:79], v[152:155], v[80:83], a[76:79]\n" \
"ds_read_b64_tr_b16 v[178:179], v50 offset:5632\n" \
"v_mfma_f32_16x16x32_bf16 a[76:79], v[156:159], v[84:87], a[76:79]\n" \
"global_load_lds_dwordx4 v44, s[52:53] offset:0\n" \
"v_mfma_f32_16x16x32_bf16 a[108:111], v[152:155], v[88:91], a[108:111]\n" \
"ds_read_b64_tr_b16 v[180:181], v50 offset:17920\n" \
"v_mfma_f32_16x16x32_bf16 a[108:111], v[156:159], v[92:95], a[108:111]\n" \
"v_mfma_f32_16x16x32_bf16 a[104:107], v[144:147], v[88:91], a[104:107]\n" \
"ds_read_b64_tr_b16 v[182:183], v50 offset:22016\n" \
"v_mfma_f32_16x16x32_bf16 a[104:107], v[148:151], v[92:95], a[104:107]\n" \
"global_load_lds_dwordx4 v45, s[50:51] offset:1024\n" \
"v_mfma_f32_16x16x32_bf16 a[100:103], v[136:139], v[88:91], a[100:103]\n" \
"ds_read_b64_tr_b16 v[184:185], v51 offset:1536\n" \
"v_mfma_f32_16x16x32_bf16 a[100:103], v[140:143], v[92:95], a[100:103]\n" \
"v_mfma_f32_16x16x32_bf16 a[96:99], v[128:131], v[88:91], a[96:99]\n" \
"ds_read_b64_tr_b16 v[186:187], v51 offset:5632\n" \
"v_mfma_f32_16x16x32_bf16 a[96:99], v[132:135], v[92:95], a[96:99]\n" \
"global_load_lds_dwordx4 v46, s[52:53] offset:2048\n" \
"v_mfma_f32_16x16x32_bf16 a[128:131], v[128:131], v[96:99], a[128:131]\n" \
"ds_read_b64_tr_b16 v[188:189], v51 offset:17920\n" \
"v_mfma_f32_16x16x32_bf16 a[128:131], v[132:135], v[100:103], a[128:131]\n" \
"v_mfma_f32_16x16x32_bf16 a[132:135], v[136:139], v[96:99], a[132:135]\n" \
"ds_read_b64_tr_b16 v[190:191], v51 offset:22016\n" \
"v_mfma_f32_16x16x32_bf16 a[132:135], v[140:143], v[100:103], a[132:135]\n" \
"global_load_lds_dwordx4 v47, s[50:51] offset:3072\n" \
"v_mfma_f32_16x16x32_bf16 a[136:139], v[144:147], v[96:99], a[136:139]\n" \
"v_mfma_f32_16x16x32_bf16 a[136:139], v[148:151], v[100:103], a[136:139]\n" \
"v_mfma_f32_16x16x32_bf16 a[140:143], v[152:155], v[96:99], a[140:143]\n" \
"v_mfma_f32_16x16x32_bf16 a[140:143], v[156:159], v[100:103], a[140:143]\n" \
"v_mfma_f32_16x16x32_bf16 a[172:175], v[152:155], v[104:107], a[172:175]\n" \
"s_mul_i32 s76, s73, 0x8000\n" \
"s_add_u32 s77, s76, 0x10000\n" \
"s_sub_u32 s78, 0x18000, s76\n" \
"v_mfma_f32_16x16x32_bf16 a[172:175], v[156:159], v[108:111], a[172:175]\n" \
"v_add_u32 v48, s77, v48\n" \
"v_add_u32 v49, s77, v49\n" \
"v_mfma_f32_16x16x32_bf16 a[168:171], v[144:147], v[104:107], a[168:171]\n" \
"v_add_u32 v50, s78, v50\n" \
"v_add_u32 v51, s78, v51\n" \
"v_mfma_f32_16x16x32_bf16 a[168:171], v[148:151], v[108:111], a[168:171]\n" \
"v_mfma_f32_16x16x32_bf16 a[164:167], v[136:139], v[104:107], a[164:167]\n" \
"v_mfma_f32_16x16x32_bf16 a[164:167], v[140:143], v[108:111], a[164:167]\n" \
"v_mfma_f32_16x16x32_bf16 a[160:163], v[128:131], v[104:107], a[160:163]\n" \
"v_mfma_f32_16x16x32_bf16 a[160:163], v[132:135], v[108:111], a[160:163]\n" \
"s_waitcnt lgkmcnt(0)\n" \
"v_mfma_f32_16x16x32_bf16 a[192:195], v[128:131], v[112:115], a[192:195]\n" \
"v_mfma_f32_16x16x32_bf16 a[192:195], v[132:135], v[116:119], a[192:195]\n" \
"v_mfma_f32_16x16x32_bf16 a[196:199], v[136:139], v[112:115], a[196:199]\n" \
"v_mfma_f32_16x16x32_bf16 a[196:199], v[140:143], v[116:119], a[196:199]\n" \
"v_mfma_f32_16x16x32_bf16 a[200:203], v[144:147], v[112:115], a[200:203]\n" \
"v_mfma_f32_16x16x32_bf16 a[200:203], v[148:151], v[116:119], a[200:203]\n" \
"v_mfma_f32_16x16x32_bf16 a[204:207], v[152:155], v[112:115], a[204:207]\n" \
"v_mfma_f32_16x16x32_bf16 a[204:207], v[156:159], v[116:119], a[204:207]\n" \
"v_mfma_f32_16x16x32_bf16 a[236:239], v[152:155], v[120:123], a[236:239]\n" \
"v_mfma_f32_16x16x32_bf16 a[236:239], v[156:159], v[124:127], a[236:239]\n" \
"v_mfma_f32_16x16x32_bf16 a[232:235], v[144:147], v[120:123], a[232:235]\n" \
"v_mfma_f32_16x16x32_bf16 a[232:235], v[148:151], v[124:127], a[232:235]\n" \
"v_mfma_f32_16x16x32_bf16 a[228:231], v[136:139], v[120:123], a[228:231]\n" \
"v_mfma_f32_16x16x32_bf16 a[228:231], v[140:143], v[124:127], a[228:231]\n" \
"s_waitcnt vmcnt(11) lgkmcnt(0)\n" \
"v_mfma_f32_16x16x32_bf16 a[224:227], v[128:131], v[120:123], a[224:227]\n" \
"v_mfma_f32_16x16x32_bf16 a[224:227], v[132:135], v[124:127], a[224:227]\n" \
"s_barrier\n" \
"v_mfma_f32_16x16x32_bf16 a[16:19], v[64:67], v[160:163], a[16:19]\n" \
"s_add_u32 m0, s87, 36864\n" \
"ds_read_b64_tr_b16 v[128:129], v50 offset:0\n" \
"v_mfma_f32_16x16x32_bf16 a[16:19], v[68:71], v[164:167], a[16:19]\n" \
"global_load_lds_dwordx4 v32, s[54:55] offset:-4096\n" \
"v_mfma_f32_16x16x32_bf16 a[20:23], v[64:67], v[168:171], a[20:23]\n" \
"ds_read_b64_tr_b16 v[130:131], v50 offset:4096\n" \
"v_mfma_f32_16x16x32_bf16 a[20:23], v[68:71], v[172:175], a[20:23]\n" \
"v_mfma_f32_16x16x32_bf16 a[24:27], v[64:67], v[176:179], a[24:27]\n" \
"ds_read_b64_tr_b16 v[132:133], v50 offset:16384\n" \
"v_mfma_f32_16x16x32_bf16 a[24:27], v[68:71], v[180:183], a[24:27]\n" \
"global_load_lds_dwordx4 v33, s[40:41] offset:-3072\n" \
"v_mfma_f32_16x16x32_bf16 a[28:31], v[64:67], v[184:187], a[28:31]\n" \
"ds_read_b64_tr_b16 v[134:135], v50 offset:20480\n" \
"v_mfma_f32_16x16x32_bf16 a[28:31], v[68:71], v[188:191], a[28:31]\n" \
"v_mfma_f32_16x16x32_bf16 a[60:63], v[72:75], v[184:187], a[60:63]\n" \
"ds_read_b128 v[64:67], v48 offset:0\n" \
"v_mfma_f32_16x16x32_bf16 a[60:63], v[76:79], v[188:191], a[60:63]\n" \
"global_load_lds_dwordx4 v34, s[54:55] offset:-2048\n" \
"v_mfma_f32_16x16x32_bf16 a[56:59], v[72:75], v[176:179], a[56:59]\n" \
"ds_read_b64_tr_b16 v[136:137], v51 offset:0\n" \
"v_mfma_f32_16x16x32_bf16 a[56:59], v[76:79], v[180:183], a[56:59]\n" \
"v_mfma_f32_16x16x32_bf16 a[52:55], v[72:75], v[168:171], a[52:55]\n" \
"ds_read_b128 v[68:71], v49 offset:0\n" \
"v_mfma_f32_16x16x32_bf16 a[52:55], v[76:79], v[172:175], a[52:55]\n" \
"global_load_lds_dwordx4 v35, s[40:41] offset:-1024\n" \
"v_mfma_f32_16x16x32_bf16 a[48:51], v[72:75], v[160:163], a[48:51]\n" \
"ds_read_b64_tr_b16 v[138:139], v51 offset:4096\n" \
"v_mfma_f32_16x16x32_bf16 a[48:51], v[76:79], v[164:167], a[48:51]\n" \
"v_mfma_f32_16x16x32_bf16 a[80:83], v[80:83], v[160:163], a[80:83]\n" \
"ds_read_b128 v[72:75], v48 offset:2048\n" \
"v_mfma_f32_16x16x32_bf16 a[80:83], v[84:87], v[164:167], a[80:83]\n" \
"global_load_lds_dwordx4 v36, s[54:55] offset:0\n" \
"v_mfma_f32_16x16x32_bf16 a[84:87], v[80:83], v[168:171], a[84:87]\n" \
"ds_read_b64_tr_b16 v[140:141], v51 offset:16384\n" \
"v_mfma_f32_16x16x32_bf16 a[84:87], v[84:87], v[172:175], a[84:87]\n" \
"v_mfma_f32_16x16x32_bf16 a[88:91], v[80:83], v[176:179], a[88:91]\n" \
"ds_read_b128 v[76:79], v49 offset:2048\n" \
"v_mfma_f32_16x16x32_bf16 a[88:91], v[84:87], v[180:183], a[88:91]\n" \
"global_load_lds_dwordx4 v37, s[40:41] offset:1024\n" \
"v_mfma_f32_16x16x32_bf16 a[92:95], v[80:83], v[184:187], a[92:95]\n" \
"ds_read_b64_tr_b16 v[142:143], v51 offset:20480\n" \
"v_mfma_f32_16x16x32_bf16 a[92:95], v[84:87], v[188:191], a[92:95]\n" \
"v_mfma_f32_16x16x32_bf16 a[124:127], v[88:91], v[184:187], a[124:127]\n" \
"ds_read_b128 v[80:83], v48 offset:4096\n" \
"v_mfma_f32_16x16x32_bf16 a[124:127], v[92:95], v[188:191], a[124:127]\n" \
"global_load_lds_dwordx4 v38, s[54:55] offset:2048\n" \
"v_mfma_f32_16x16x32_bf16 a[120:123], v[88:91], v[176:179], a[120:123]\n" \
"ds_read_b64_tr_b16 v[144:145], v50 offset:512\n" \
"v_mfma_f32_16x16x32_bf16 a[120:123], v[92:95], v[180:183], a[120:123]\n" \
"v_mfma_f32_16x16x32_bf16 a[116:119], v[88:91], v[168:171], a[116:119]\n" \
"ds_read_b128 v[84:87], v49 offset:4096\n" \
"v_mfma_f32_16x16x32_bf16 a[116:119], v[92:95], v[172:175], a[116:119]\n" \
"global_load_lds_dwordx4 v39, s[40:41] offset:3072\n" \
"s_add_u32 s68, s67, 0x300000\n" \
"v_mfma_f32_16x16x32_bf16 a[112:115], v[88:91], v[160:163], a[112:115]\n" \
"s_add_u32 s64, s12, s68\n" \
"s_addc_u32 s65, s13, 0\n" \
"ds_read_b64_tr_b16 v[146:147], v50 offset:4608\n" \
"v_mfma_f32_16x16x32_bf16 a[112:115], v[92:95], v[164:167], a[112:115]\n" \
"v_mfma_f32_16x16x32_bf16 a[144:147], v[96:99], v[160:163], a[144:147]\n" \
"ds_read_b128 v[88:91], v48 offset:6144\n" \
"v_mfma_f32_16x16x32_bf16 a[144:147], v[100:103], v[164:167], a[144:147]\n" \
"global_load_dword v253, v248, s[64:65]\n" \
"v_mfma_f32_16x16x32_bf16 a[148:151], v[96:99], v[168:171], a[148:151]\n" \
"s_add_u32 s68, s67, 0x400000\n" \
"ds_read_b64_tr_b16 v[148:149], v50 offset:16896\n" \
"v_mfma_f32_16x16x32_bf16 a[148:151], v[100:103], v[172:175], a[148:151]\n" \
"s_add_u32 s64, s12, s68\n" \
"s_addc_u32 s65, s13, 0\n" \
"v_mfma_f32_16x16x32_bf16 a[152:155], v[96:99], v[176:179], a[152:155]\n" \
"ds_read_b128 v[92:95], v49 offset:6144\n" \
"v_mfma_f32_16x16x32_bf16 a[152:155], v[100:103], v[180:183], a[152:155]\n" \
"global_load_dword v253, v248, s[64:65]\n" \
"v_mfma_f32_16x16x32_bf16 a[156:159], v[96:99], v[184:187], a[156:159]\n" \
"ds_read_b64_tr_b16 v[150:151], v50 offset:20992\n" \
"v_mfma_f32_16x16x32_bf16 a[156:159], v[100:103], v[188:191], a[156:159]\n" \
"v_mfma_f32_16x16x32_bf16 a[188:191], v[104:107], v[184:187], a[188:191]\n" \
"ds_read_b128 v[96:99], v48 offset:8192\n" \
"v_mfma_f32_16x16x32_bf16 a[188:191], v[108:111], v[188:191], a[188:191]\n" \
"v_mfma_f32_16x16x32_bf16 a[184:187], v[104:107], v[176:179], a[184:187]\n" \
"ds_read_b64_tr_b16 v[152:153], v51 offset:512\n" \
"v_mfma_f32_16x16x32_bf16 a[184:187], v[108:111], v[180:183], a[184:187]\n" \
"v_mfma_f32_16x16x32_bf16 a[180:183], v[104:107], v[168:171], a[180:183]\n" \
"ds_read_b128 v[100:103], v49 offset:8192\n" \
"v_mfma_f32_16x16x32_bf16 a[180:183], v[108:111], v[172:175], a[180:183]\n" \
"v_mfma_f32_16x16x32_bf16 a[176:179], v[104:107], v[160:163], a[176:179]\n" \
"ds_read_b64_tr_b16 v[154:155], v51 offset:4608\n" \
"v_mfma_f32_16x16x32_bf16 a[176:179], v[108:111], v[164:167], a[176:179]\n" \
"v_mfma_f32_16x16x32_bf16 a[208:211], v[112:115], v[160:163], a[208:211]\n" \
"ds_read_b128 v[104:107], v48 offset:10240\n" \
"v_mfma_f32_16x16x32_bf16 a[208:211], v[116:119], v[164:167], a[208:211]\n" \
"v_mfma_f32_16x16x32_bf16 a[212:215], v[112:115], v[168:171], a[212:215]\n" \
"ds_read_b64_tr_b16 v[156:157], v51 offset:16896\n" \
"v_mfma_f32_16x16x32_bf16 a[212:215], v[116:119], v[172:175], a[212:215]\n" \
"v_mfma_f32_16x16x32_bf16 a[216:219], v[112:115], v[176:179], a[216:219]\n" \
"ds_read_b128 v[108:111], v49 offset:10240\n" \
"v_mfma_f32_16x16x32_bf16 a[216:219], v[116:119], v[180:183], a[216:219]\n" \
"v_mfma_f32_16x16x32_bf16 a[220:223], v[112:115], v[184:187], a[220:223]\n" \
"ds_read_b64_tr_b16 v[158:159], v51 offset:20992\n" \
"v_mfma_f32_16x16x32_bf16 a[220:223], v[116:119], v[188:191], a[220:223]\n" \
"v_mfma_f32_16x16x32_bf16 a[252:255], v[120:123], v[184:187], a[252:255]\n" \
"ds_read_b128 v[112:115], v48 offset:12288\n" \
"v_mfma_f32_16x16x32_bf16 a[252:255], v[124:127], v[188:191], a[252:255]\n" \
"v_mfma_f32_16x16x32_bf16 a[248:251], v[120:123], v[176:179], a[248:251]\n" \
"v_mfma_f32_16x16x32_bf16 a[248:251], v[124:127], v[180:183], a[248:251]\n" \
"v_mfma_f32_16x16x32_bf16 a[244:247], v[120:123], v[168:171], a[244:247]\n" \
"ds_read_b128 v[116:119], v49 offset:12288\n" \
"v_mfma_f32_16x16x32_bf16 a[244:247], v[124:127], v[172:175], a[244:247]\n" \
"v_mfma_f32_16x16x32_bf16 a[240:243], v[120:123], v[160:163], a[240:243]\n" \
"v_mfma_f32_16x16x32_bf16 a[240:243], v[124:127], v[164:167], a[240:243]\n" \
"s_waitcnt lgkmcnt(1)\n" \
"v_mfma_f32_16x16x32_bf16 a[0:3], v[128:131], v[64:67], a[0:3]\n" \
"ds_read_b128 v[120:123], v48 offset:14336\n" \
"v_mfma_f32_16x16x32_bf16 a[0:3], v[132:135], v[68:71], a[0:3]\n" \
"v_mfma_f32_16x16x32_bf16 a[4:7], v[136:139], v[64:67], a[4:7]\n" \
"ds_read_b128 v[124:127], v49 offset:14336\n" \
"s_add_u32 m0, s95, 4096\n" \
"v_mfma_f32_16x16x32_bf16 a[4:7], v[140:143], v[68:71], a[4:7]\n" \
"global_load_lds_dwordx4 v228, s[44:45] offset:-4096\n" \
"v_mfma_f32_16x16x32_bf16 a[8:11], v[144:147], v[64:67], a[8:11]\n" \
"ds_read_b64_tr_b16 v[160:161], v50 offset:1024\n" \
"v_mfma_f32_16x16x32_bf16 a[8:11], v[148:151], v[68:71], a[8:11]\n" \
"v_mfma_f32_16x16x32_bf16 a[12:15], v[152:155], v[64:67], a[12:15]\n" \
"ds_read_b64_tr_b16 v[162:163], v50 offset:5120\n" \
"v_mfma_f32_16x16x32_bf16 a[12:15], v[156:159], v[68:71], a[12:15]\n" \
"global_load_lds_dwordx4 v229, s[44:45] offset:-3072\n" \
"v_mfma_f32_16x16x32_bf16 a[44:47], v[152:155], v[72:75], a[44:47]\n" \
"s_add_u32 s44, s44, 0x180000\n" \
"s_addc_u32 s45, s45, 0\n" \
"ds_read_b64_tr_b16 v[164:165], v50 offset:17408\n" \
"v_mfma_f32_16x16x32_bf16 a[44:47], v[156:159], v[76:79], a[44:47]\n" \
"v_mfma_f32_16x16x32_bf16 a[40:43], v[144:147], v[72:75], a[40:43]\n" \
"ds_read_b64_tr_b16 v[166:167], v50 offset:21504\n" \
"v_mfma_f32_16x16x32_bf16 a[40:43], v[148:151], v[76:79], a[40:43]\n" \
"global_load_lds_dwordx4 v228, s[42:43] offset:-2048\n" \
"v_mfma_f32_16x16x32_bf16 a[36:39], v[136:139], v[72:75], a[36:39]\n" \
"ds_read_b64_tr_b16 v[168:169], v51 offset:1024\n" \
"v_mfma_f32_16x16x32_bf16 a[36:39], v[140:143], v[76:79], a[36:39]\n" \
"v_mfma_f32_16x16x32_bf16 a[32:35], v[128:131], v[72:75], a[32:35]\n" \
"ds_read_b64_tr_b16 v[170:171], v51 offset:5120\n" \
"v_mfma_f32_16x16x32_bf16 a[32:35], v[132:135], v[76:79], a[32:35]\n" \
"global_load_lds_dwordx4 v229, s[42:43] offset:-1024\n" \
"v_mfma_f32_16x16x32_bf16 a[64:67], v[128:131], v[80:83], a[64:67]\n" \
"s_add_u32 s42, s42, 0x180000\n" \
"s_addc_u32 s43, s43, 0\n" \
"ds_read_b64_tr_b16 v[172:173], v51 offset:17408\n" \
"v_mfma_f32_16x16x32_bf16 a[64:67], v[132:135], v[84:87], a[64:67]\n" \
"v_mfma_f32_16x16x32_bf16 a[68:71], v[136:139], v[80:83], a[68:71]\n" \
"s_add_u32 m0, s95, 20480\n" \
"ds_read_b64_tr_b16 v[174:175], v51 offset:21504\n" \
"v_mfma_f32_16x16x32_bf16 a[68:71], v[140:143], v[84:87], a[68:71]\n" \
"global_load_lds_dwordx4 v228, s[44:45] offset:-4096\n" \
"v_mfma_f32_16x16x32_bf16 a[72:75], v[144:147], v[80:83], a[72:75]\n" \
"ds_read_b64_tr_b16 v[176:177], v50 offset:1536\n" \
"v_mfma_f32_16x16x32_bf16 a[72:75], v[148:151], v[84:87], a[72:75]\n" \
"v_mfma_f32_16x16x32_bf16 a[76:79], v[152:155], v[80:83], a[76:79]\n" \
"ds_read_b64_tr_b16 v[178:179], v50 offset:5632\n" \
"v_mfma_f32_16x16x32_bf16 a[76:79], v[156:159], v[84:87], a[76:79]\n" \
"Lpg1684_%=:\n" \
"Lst0x2_%=:\n" \
"s_cbranch_execz Lpg3850_%=\n" \
"global_load_lds_dwordx4 v229, s[44:45] offset:-3072\n" \
"v_mfma_f32_16x16x32_bf16 a[108:111], v[152:155], v[88:91], a[108:111]\n" \
"s_add_u32 s44, s44, 0x400000\n" \
"s_addc_u32 s45, s45, 0\n" \
"ds_read_b64_tr_b16 v[180:181], v50 offset:17920\n" \
"v_mfma_f32_16x16x32_bf16 a[108:111], v[156:159], v[92:95], a[108:111]\n" \
"v_mfma_f32_16x16x32_bf16 a[104:107], v[144:147], v[88:91], a[104:107]\n" \
"ds_read_b64_tr_b16 v[182:183], v50 offset:22016\n" \
"v_mfma_f32_16x16x32_bf16 a[104:107], v[148:151], v[92:95], a[104:107]\n" \
"global_load_lds_dwordx4 v228, s[42:43] offset:-2048\n" \
"v_mfma_f32_16x16x32_bf16 a[100:103], v[136:139], v[88:91], a[100:103]\n" \
"ds_read_b64_tr_b16 v[184:185], v51 offset:1536\n" \
"v_mfma_f32_16x16x32_bf16 a[100:103], v[140:143], v[92:95], a[100:103]\n" \
"v_mfma_f32_16x16x32_bf16 a[96:99], v[128:131], v[88:91], a[96:99]\n" \
"ds_read_b64_tr_b16 v[186:187], v51 offset:5632\n" \
"v_mfma_f32_16x16x32_bf16 a[96:99], v[132:135], v[92:95], a[96:99]\n" \
"global_load_lds_dwordx4 v229, s[42:43] offset:-1024\n" \
"v_mfma_f32_16x16x32_bf16 a[128:131], v[128:131], v[96:99], a[128:131]\n" \
"s_add_u32 s42, s42, 0x400000\n" \
"s_addc_u32 s43, s43, 0\n" \
"ds_read_b64_tr_b16 v[188:189], v51 offset:17920\n" \
"v_mfma_f32_16x16x32_bf16 a[128:131], v[132:135], v[100:103], a[128:131]\n" \
"v_mfma_f32_16x16x32_bf16 a[132:135], v[136:139], v[96:99], a[132:135]\n" \
"ds_read_b64_tr_b16 v[190:191], v51 offset:22016\n" \
"v_mfma_f32_16x16x32_bf16 a[132:135], v[140:143], v[100:103], a[132:135]\n" \
"v_mfma_f32_16x16x32_bf16 a[136:139], v[144:147], v[96:99], a[136:139]\n" \
"v_mfma_f32_16x16x32_bf16 a[136:139], v[148:151], v[100:103], a[136:139]\n" \
"v_mfma_f32_16x16x32_bf16 a[140:143], v[152:155], v[96:99], a[140:143]\n" \
"v_mfma_f32_16x16x32_bf16 a[140:143], v[156:159], v[100:103], a[140:143]\n" \
"v_mfma_f32_16x16x32_bf16 a[172:175], v[152:155], v[104:107], a[172:175]\n" \
"s_mul_i32 s76, s73, 0x0\n" \
"s_add_u32 s77, s76, 0xffff8000\n" \
"s_sub_u32 s78, 0xffff8000, s76\n" \
"v_mfma_f32_16x16x32_bf16 a[172:175], v[156:159], v[108:111], a[172:175]\n" \
"v_add_u32 v48, s77, v48\n" \
"v_add_u32 v49, s77, v49\n" \
"v_mfma_f32_16x16x32_bf16 a[168:171], v[144:147], v[104:107], a[168:171]\n" \
"v_add_u32 v50, s78, v50\n" \
"v_add_u32 v51, s78, v51\n" \
"v_mfma_f32_16x16x32_bf16 a[168:171], v[148:151], v[108:111], a[168:171]\n" \
"v_mfma_f32_16x16x32_bf16 a[164:167], v[136:139], v[104:107], a[164:167]\n" \
"v_mfma_f32_16x16x32_bf16 a[164:167], v[140:143], v[108:111], a[164:167]\n" \
"v_mfma_f32_16x16x32_bf16 a[160:163], v[128:131], v[104:107], a[160:163]\n" \
"v_mfma_f32_16x16x32_bf16 a[160:163], v[132:135], v[108:111], a[160:163]\n" \
"s_waitcnt lgkmcnt(0)\n" \
"v_mfma_f32_16x16x32_bf16 a[192:195], v[128:131], v[112:115], a[192:195]\n" \
"v_mfma_f32_16x16x32_bf16 a[192:195], v[132:135], v[116:119], a[192:195]\n" \
"v_mfma_f32_16x16x32_bf16 a[196:199], v[136:139], v[112:115], a[196:199]\n" \
"v_mfma_f32_16x16x32_bf16 a[196:199], v[140:143], v[116:119], a[196:199]\n" \
"v_mfma_f32_16x16x32_bf16 a[200:203], v[144:147], v[112:115], a[200:203]\n" \
"v_mfma_f32_16x16x32_bf16 a[200:203], v[148:151], v[116:119], a[200:203]\n" \
"v_mfma_f32_16x16x32_bf16 a[204:207], v[152:155], v[112:115], a[204:207]\n" \
"v_mfma_f32_16x16x32_bf16 a[204:207], v[156:159], v[116:119], a[204:207]\n" \
"v_mfma_f32_16x16x32_bf16 a[236:239], v[152:155], v[120:123], a[236:239]\n" \
"v_mfma_f32_16x16x32_bf16 a[236:239], v[156:159], v[124:127], a[236:239]\n" \
"v_mfma_f32_16x16x32_bf16 a[232:235], v[144:147], v[120:123], a[232:235]\n" \
"v_mfma_f32_16x16x32_bf16 a[232:235], v[148:151], v[124:127], a[232:235]\n" \
"v_mfma_f32_16x16x32_bf16 a[228:231], v[136:139], v[120:123], a[228:231]\n" \
"v_mfma_f32_16x16x32_bf16 a[228:231], v[140:143], v[124:127], a[228:231]\n" \
"s_waitcnt vmcnt(10) lgkmcnt(0)\n" \
"v_mfma_f32_16x16x32_bf16 a[224:227], v[128:131], v[120:123], a[224:227]\n" \
"v_mfma_f32_16x16x32_bf16 a[224:227], v[132:135], v[124:127], a[224:227]\n" \
"s_barrier\n" \
"v_mfma_f32_16x16x32_bf16 a[16:19], v[64:67], v[160:163], a[16:19]\n" \
"s_add_u32 m0, s87, 135168\n" \
"ds_read_b64_tr_b16 v[128:129], v50 offset:0\n" \
"v_mfma_f32_16x16x32_bf16 a[16:19], v[68:71], v[164:167], a[16:19]\n" \
"global_load_lds_dwordx4 v224, s[48:49] offset:-4096\n" \
"v_mfma_f32_16x16x32_bf16 a[20:23], v[64:67], v[168:171], a[20:23]\n" \
"ds_read_b64_tr_b16 v[130:131], v50 offset:4096\n" \
"v_mfma_f32_16x16x32_bf16 a[20:23], v[68:71], v[172:175], a[20:23]\n" \
"v_mfma_f32_16x16x32_bf16 a[24:27], v[64:67], v[176:179], a[24:27]\n" \
"ds_read_b64_tr_b16 v[132:133], v50 offset:16384\n" \
"v_mfma_f32_16x16x32_bf16 a[24:27], v[68:71], v[180:183], a[24:27]\n" \
"global_load_lds_dwordx4 v225, s[48:49] offset:-3072\n" \
"v_mfma_f32_16x16x32_bf16 a[28:31], v[64:67], v[184:187], a[28:31]\n" \
"ds_read_b64_tr_b16 v[134:135], v50 offset:20480\n" \
"v_mfma_f32_16x16x32_bf16 a[28:31], v[68:71], v[188:191], a[28:31]\n" \
"v_mfma_f32_16x16x32_bf16 a[60:63], v[72:75], v[184:187], a[60:63]\n" \
"ds_read_b128 v[64:67], v48 offset:0\n" \
"v_mfma_f32_16x16x32_bf16 a[60:63], v[76:79], v[188:191], a[60:63]\n" \
"global_load_lds_dwordx4 v226, s[48:49] offset:-2048\n" \
"v_mfma_f32_16x16x32_bf16 a[56:59], v[72:75], v[176:179], a[56:59]\n" \
"ds_read_b64_tr_b16 v[136:137], v51 offset:0\n" \
"v_mfma_f32_16x16x32_bf16 a[56:59], v[76:79], v[180:183], a[56:59]\n" \
"v_mfma_f32_16x16x32_bf16 a[52:55], v[72:75], v[168:171], a[52:55]\n" \
"ds_read_b128 v[68:71], v49 offset:0\n" \
"v_mfma_f32_16x16x32_bf16 a[52:55], v[76:79], v[172:175], a[52:55]\n" \
"global_load_lds_dwordx4 v227, s[48:49] offset:-1024\n" \
"v_mfma_f32_16x16x32_bf16 a[48:51], v[72:75], v[160:163], a[48:51]\n" \
"s_add_u32 s48, s48, 0x180\n" \
"s_addc_u32 s49, s49, 0\n" \
"ds_read_b64_tr_b16 v[138:139], v51 offset:4096\n" \
"v_mfma_f32_16x16x32_bf16 a[48:51], v[76:79], v[164:167], a[48:51]\n" \
"v_mfma_f32_16x16x32_bf16 a[80:83], v[80:83], v[160:163], a[80:83]\n" \
"ds_read_b128 v[72:75], v48 offset:2048\n" \
"v_mfma_f32_16x16x32_bf16 a[80:83], v[84:87], v[164:167], a[80:83]\n" \
"global_load_lds_dwordx4 v224, s[46:47] offset:0\n" \
"v_mfma_f32_16x16x32_bf16 a[84:87], v[80:83], v[168:171], a[84:87]\n" \
"ds_read_b64_tr_b16 v[140:141], v51 offset:16384\n" \
"v_mfma_f32_16x16x32_bf16 a[84:87], v[84:87], v[172:175], a[84:87]\n" \
"v_mfma_f32_16x16x32_bf16 a[88:91], v[80:83], v[176:179], a[88:91]\n" \
"ds_read_b128 v[76:79], v49 offset:2048\n" \
"v_mfma_f32_16x16x32_bf16 a[88:91], v[84:87], v[180:183], a[88:91]\n" \
"global_load_lds_dwordx4 v225, s[46:47] offset:1024\n" \
"v_mfma_f32_16x16x32_bf16 a[92:95], v[80:83], v[184:187], a[92:95]\n" \
"ds_read_b64_tr_b16 v[142:143], v51 offset:20480\n" \
"v_mfma_f32_16x16x32_bf16 a[92:95], v[84:87], v[188:191], a[92:95]\n" \
"v_mfma_f32_16x16x32_bf16 a[124:127], v[88:91], v[184:187], a[124:127]\n" \
"ds_read_b128 v[80:83], v48 offset:4096\n" \
"v_mfma_f32_16x16x32_bf16 a[124:127], v[92:95], v[188:191], a[124:127]\n" \
"global_load_lds_dwordx4 v226, s[46:47] offset:2048\n" \
"v_mfma_f32_16x16x32_bf16 a[120:123], v[88:91], v[176:179], a[120:123]\n" \
"ds_read_b64_tr_b16 v[144:145], v50 offset:512\n" \
"v_mfma_f32_16x16x32_bf16 a[120:123], v[92:95], v[180:183], a[120:123]\n" \
"v_mfma_f32_16x16x32_bf16 a[116:119], v[88:91], v[168:171], a[116:119]\n" \
"ds_read_b128 v[84:87], v49 offset:4096\n" \
"v_mfma_f32_16x16x32_bf16 a[116:119], v[92:95], v[172:175], a[116:119]\n" \
"global_load_lds_dwordx4 v227, s[46:47] offset:3072\n" \
"v_mfma_f32_16x16x32_bf16 a[112:115], v[88:91], v[160:163], a[112:115]\n" \
"s_add_u32 s46, s46, 0x180\n" \
"s_addc_u32 s47, s47, 0\n" \
"ds_read_b64_tr_b16 v[146:147], v50 offset:4608\n" \
"v_mfma_f32_16x16x32_bf16 a[112:115], v[92:95], v[164:167], a[112:115]\n" \
"s_add_u32 s68, s67, 0x600000\n" \
"s_add_u32 s64, s12, s68\n" \
"s_addc_u32 s65, s13, 0\n" \
"v_mfma_f32_16x16x32_bf16 a[144:147], v[96:99], v[160:163], a[144:147]\n" \
"ds_read_b128 v[88:91], v48 offset:6144\n" \
"v_mfma_f32_16x16x32_bf16 a[144:147], v[100:103], v[164:167], a[144:147]\n" \
"global_load_dword v253, v248, s[64:65]\n" \
"v_mfma_f32_16x16x32_bf16 a[148:151], v[96:99], v[168:171], a[148:151]\n" \
"s_add_u32 s68, s67, 0x700000\n" \
"ds_read_b64_tr_b16 v[148:149], v50 offset:16896\n" \
"v_mfma_f32_16x16x32_bf16 a[148:151], v[100:103], v[172:175], a[148:151]\n" \
"s_add_u32 s64, s12, s68\n" \
"s_addc_u32 s65, s13, 0\n" \
"v_mfma_f32_16x16x32_bf16 a[152:155], v[96:99], v[176:179], a[152:155]\n" \
"ds_read_b128 v[92:95], v49 offset:6144\n" \
"v_mfma_f32_16x16x32_bf16 a[152:155], v[100:103], v[180:183], a[152:155]\n" \
"global_load_dword v253, v248, s[64:65]\n" \
"v_mfma_f32_16x16x32_bf16 a[156:159], v[96:99], v[184:187], a[156:159]\n" \
"s_lshr_b32 s68, s66, 2\n" \
"s_and_b32 s68, s68, 0xffff\n" \
"ds_read_b64_tr_b16 v[150:151], v50 offset:20992\n" \
"v_mfma_f32_16x16x32_bf16 a[156:159], v[100:103], v[188:191], a[156:159]\n" \
"s_add_u32 s68, s68, 65585152\n" \
"v_mfma_f32_16x16x32_bf16 a[188:191], v[104:107], v[184:187], a[188:191]\n" \
"ds_read_b128 v[96:99], v48 offset:8192\n" \
"v_mfma_f32_16x16x32_bf16 a[188:191], v[108:111], v[188:191], a[188:191]\n" \
"buffer_load_dword v253, v245, s[56:59], s68 offen sc1\n" \
"v_mfma_f32_16x16x32_bf16 a[184:187], v[104:107], v[176:179], a[184:187]\n" \
"ds_read_b64_tr_b16 v[152:153], v51 offset:512\n" \
"v_mfma_f32_16x16x32_bf16 a[184:187], v[108:111], v[180:183], a[184:187]\n" \
"v_mfma_f32_16x16x32_bf16 a[180:183], v[104:107], v[168:171], a[180:183]\n" \
"ds_read_b128 v[100:103], v49 offset:8192\n" \
"v_mfma_f32_16x16x32_bf16 a[180:183], v[108:111], v[172:175], a[180:183]\n" \
"v_mfma_f32_16x16x32_bf16 a[176:179], v[104:107], v[160:163], a[176:179]\n" \
"ds_read_b64_tr_b16 v[154:155], v51 offset:4608\n" \
"v_mfma_f32_16x16x32_bf16 a[176:179], v[108:111], v[164:167], a[176:179]\n" \
"v_mfma_f32_16x16x32_bf16 a[208:211], v[112:115], v[160:163], a[208:211]\n" \
"ds_read_b128 v[104:107], v48 offset:10240\n" \
"v_mfma_f32_16x16x32_bf16 a[208:211], v[116:119], v[164:167], a[208:211]\n" \
"v_mfma_f32_16x16x32_bf16 a[212:215], v[112:115], v[168:171], a[212:215]\n" \
"ds_read_b64_tr_b16 v[156:157], v51 offset:16896\n" \
"v_mfma_f32_16x16x32_bf16 a[212:215], v[116:119], v[172:175], a[212:215]\n" \
"v_mfma_f32_16x16x32_bf16 a[216:219], v[112:115], v[176:179], a[216:219]\n" \
"ds_read_b128 v[108:111], v49 offset:10240\n" \
"v_mfma_f32_16x16x32_bf16 a[216:219], v[116:119], v[180:183], a[216:219]\n" \
"v_mfma_f32_16x16x32_bf16 a[220:223], v[112:115], v[184:187], a[220:223]\n" \
"ds_read_b64_tr_b16 v[158:159], v51 offset:20992\n" \
"v_mfma_f32_16x16x32_bf16 a[220:223], v[116:119], v[188:191], a[220:223]\n" \
"v_mfma_f32_16x16x32_bf16 a[252:255], v[120:123], v[184:187], a[252:255]\n" \
"ds_read_b128 v[112:115], v48 offset:12288\n" \
"v_mfma_f32_16x16x32_bf16 a[252:255], v[124:127], v[188:191], a[252:255]\n" \
"v_mfma_f32_16x16x32_bf16 a[248:251], v[120:123], v[176:179], a[248:251]\n" \
"v_mfma_f32_16x16x32_bf16 a[248:251], v[124:127], v[180:183], a[248:251]\n" \
"v_mfma_f32_16x16x32_bf16 a[244:247], v[120:123], v[168:171], a[244:247]\n" \
"ds_read_b128 v[116:119], v49 offset:12288\n" \
"v_mfma_f32_16x16x32_bf16 a[244:247], v[124:127], v[172:175], a[244:247]\n" \
"v_mfma_f32_16x16x32_bf16 a[240:243], v[120:123], v[160:163], a[240:243]\n" \
"v_mfma_f32_16x16x32_bf16 a[240:243], v[124:127], v[164:167], a[240:243]\n" \
"s_waitcnt lgkmcnt(1)\n" \
"v_mfma_f32_16x16x32_bf16 a[0:3], v[128:131], v[64:67], a[0:3]\n" \
"ds_read_b128 v[120:123], v48 offset:14336\n" \
"v_mfma_f32_16x16x32_bf16 a[0:3], v[132:135], v[68:71], a[0:3]\n" \
"v_mfma_f32_16x16x32_bf16 a[4:7], v[136:139], v[64:67], a[4:7]\n" \
"ds_read_b128 v[124:127], v49 offset:14336\n" \
"s_add_u32 m0, s87, 69632\n" \
"v_mfma_f32_16x16x32_bf16 a[4:7], v[140:143], v[68:71], a[4:7]\n" \
"global_load_lds_dwordx4 v224, s[48:49] offset:-4096\n" \
"v_mfma_f32_16x16x32_bf16 a[8:11], v[144:147], v[64:67], a[8:11]\n" \
"ds_read_b64_tr_b16 v[160:161], v50 offset:1024\n" \
"v_mfma_f32_16x16x32_bf16 a[8:11], v[148:151], v[68:71], a[8:11]\n" \
"v_mfma_f32_16x16x32_bf16 a[12:15], v[152:155], v[64:67], a[12:15]\n" \
"ds_read_b64_tr_b16 v[162:163], v50 offset:5120\n" \
"v_mfma_f32_16x16x32_bf16 a[12:15], v[156:159], v[68:71], a[12:15]\n" \
"global_load_lds_dwordx4 v225, s[48:49] offset:-3072\n" \
"v_mfma_f32_16x16x32_bf16 a[44:47], v[152:155], v[72:75], a[44:47]\n" \
"ds_read_b64_tr_b16 v[164:165], v50 offset:17408\n" \
"v_mfma_f32_16x16x32_bf16 a[44:47], v[156:159], v[76:79], a[44:47]\n" \
"v_mfma_f32_16x16x32_bf16 a[40:43], v[144:147], v[72:75], a[40:43]\n" \
"ds_read_b64_tr_b16 v[166:167], v50 offset:21504\n" \
"v_mfma_f32_16x16x32_bf16 a[40:43], v[148:151], v[76:79], a[40:43]\n" \
"global_load_lds_dwordx4 v226, s[48:49] offset:-2048\n" \
"v_mfma_f32_16x16x32_bf16 a[36:39], v[136:139], v[72:75], a[36:39]\n" \
"ds_read_b64_tr_b16 v[168:169], v51 offset:1024\n" \
"v_mfma_f32_16x16x32_bf16 a[36:39], v[140:143], v[76:79], a[36:39]\n" \
"v_mfma_f32_16x16x32_bf16 a[32:35], v[128:131], v[72:75], a[32:35]\n" \
"ds_read_b64_tr_b16 v[170:171], v51 offset:5120\n" \
"v_mfma_f32_16x16x32_bf16 a[32:35], v[132:135], v[76:79], a[32:35]\n" \
"global_load_lds_dwordx4 v227, s[48:49] offset:-1024\n" \
"v_mfma_f32_16x16x32_bf16 a[64:67], v[128:131], v[80:83], a[64:67]\n" \
"s_add_u32 s48, s48, 0x400\n" \
"s_addc_u32 s49, s49, 0\n" \
"ds_read_b64_tr_b16 v[172:173], v51 offset:17408\n" \
"v_mfma_f32_16x16x32_bf16 a[64:67], v[132:135], v[84:87], a[64:67]\n" \
"v_mfma_f32_16x16x32_bf16 a[68:71], v[136:139], v[80:83], a[68:71]\n" \
"ds_read_b64_tr_b16 v[174:175], v51 offset:21504\n" \
"v_mfma_f32_16x16x32_bf16 a[68:71], v[140:143], v[84:87], a[68:71]\n" \
"global_load_lds_dwordx4 v224, s[46:47] offset:0\n" \
"v_mfma_f32_16x16x32_bf16 a[72:75], v[144:147], v[80:83], a[72:75]\n" \
"ds_read_b64_tr_b16 v[176:177], v50 offset:1536\n" \
"v_mfma_f32_16x16x32_bf16 a[72:75], v[148:151], v[84:87], a[72:75]\n" \
"v_mfma_f32_16x16x32_bf16 a[76:79], v[152:155], v[80:83], a[76:79]\n" \
"ds_read_b64_tr_b16 v[178:179], v50 offset:5632\n" \
"v_mfma_f32_16x16x32_bf16 a[76:79], v[156:159], v[84:87], a[76:79]\n" \
"global_load_lds_dwordx4 v225, s[46:47] offset:1024\n" \
"v_mfma_f32_16x16x32_bf16 a[108:111], v[152:155], v[88:91], a[108:111]\n" \
"ds_read_b64_tr_b16 v[180:181], v50 offset:17920\n" \
"v_mfma_f32_16x16x32_bf16 a[108:111], v[156:159], v[92:95], a[108:111]\n" \
"v_mfma_f32_16x16x32_bf16 a[104:107], v[144:147], v[88:91], a[104:107]\n" \
"ds_read_b64_tr_b16 v[182:183], v50 offset:22016\n" \
"v_mfma_f32_16x16x32_bf16 a[104:107], v[148:151], v[92:95], a[104:107]\n" \
"global_load_lds_dwordx4 v226, s[46:47] offset:2048\n" \
"v_mfma_f32_16x16x32_bf16 a[100:103], v[136:139], v[88:91], a[100:103]\n" \
"ds_read_b64_tr_b16 v[184:185], v51 offset:1536\n" \
"v_mfma_f32_16x16x32_bf16 a[100:103], v[140:143], v[92:95], a[100:103]\n" \
"v_mfma_f32_16x16x32_bf16 a[96:99], v[128:131], v[88:91], a[96:99]\n" \
"ds_read_b64_tr_b16 v[186:187], v51 offset:5632\n" \
"v_mfma_f32_16x16x32_bf16 a[96:99], v[132:135], v[92:95], a[96:99]\n" \
"global_load_lds_dwordx4 v227, s[46:47] offset:3072\n" \
"v_mfma_f32_16x16x32_bf16 a[128:131], v[128:131], v[96:99], a[128:131]\n" \
"s_add_u32 s46, s46, 0x400\n" \
"s_addc_u32 s47, s47, 0\n" \
"ds_read_b64_tr_b16 v[188:189], v51 offset:17920\n" \
"v_mfma_f32_16x16x32_bf16 a[128:131], v[132:135], v[100:103], a[128:131]\n" \
"v_mfma_f32_16x16x32_bf16 a[132:135], v[136:139], v[96:99], a[132:135]\n" \
"ds_read_b64_tr_b16 v[190:191], v51 offset:22016\n" \
"v_mfma_f32_16x16x32_bf16 a[132:135], v[140:143], v[100:103], a[132:135]\n" \
"v_mfma_f32_16x16x32_bf16 a[136:139], v[144:147], v[96:99], a[136:139]\n" \
"v_mfma_f32_16x16x32_bf16 a[136:139], v[148:151], v[100:103], a[136:139]\n" \
"v_mfma_f32_16x16x32_bf16 a[140:143], v[152:155], v[96:99], a[140:143]\n" \
"v_mfma_f32_16x16x32_bf16 a[140:143], v[156:159], v[100:103], a[140:143]\n" \
"v_mfma_f32_16x16x32_bf16 a[172:175], v[152:155], v[104:107], a[172:175]\n" \
"v_mfma_f32_16x16x32_bf16 a[172:175], v[156:159], v[108:111], a[172:175]\n" \
"v_mfma_f32_16x16x32_bf16 a[168:171], v[144:147], v[104:107], a[168:171]\n" \
"v_mfma_f32_16x16x32_bf16 a[168:171], v[148:151], v[108:111], a[168:171]\n" \
"v_mfma_f32_16x16x32_bf16 a[164:167], v[136:139], v[104:107], a[164:167]\n" \
"v_mfma_f32_16x16x32_bf16 a[164:167], v[140:143], v[108:111], a[164:167]\n" \
"v_mfma_f32_16x16x32_bf16 a[160:163], v[128:131], v[104:107], a[160:163]\n" \
"v_mfma_f32_16x16x32_bf16 a[160:163], v[132:135], v[108:111], a[160:163]\n" \
"s_waitcnt lgkmcnt(0)\n" \
"v_mfma_f32_16x16x32_bf16 a[192:195], v[128:131], v[112:115], a[192:195]\n" \
"v_mfma_f32_16x16x32_bf16 a[192:195], v[132:135], v[116:119], a[192:195]\n" \
"v_mfma_f32_16x16x32_bf16 a[196:199], v[136:139], v[112:115], a[196:199]\n" \
"v_mfma_f32_16x16x32_bf16 a[196:199], v[140:143], v[116:119], a[196:199]\n" \
"v_mfma_f32_16x16x32_bf16 a[200:203], v[144:147], v[112:115], a[200:203]\n" \
"v_mfma_f32_16x16x32_bf16 a[200:203], v[148:151], v[116:119], a[200:203]\n" \
"v_mfma_f32_16x16x32_bf16 a[204:207], v[152:155], v[112:115], a[204:207]\n" \
"v_mfma_f32_16x16x32_bf16 a[204:207], v[156:159], v[116:119], a[204:207]\n" \
"v_mfma_f32_16x16x32_bf16 a[236:239], v[152:155], v[120:123], a[236:239]\n" \
"v_mfma_f32_16x16x32_bf16 a[236:239], v[156:159], v[124:127], a[236:239]\n" \
"v_mfma_f32_16x16x32_bf16 a[232:235], v[144:147], v[120:123], a[232:235]\n" \
"v_mfma_f32_16x16x32_bf16 a[232:235], v[148:151], v[124:127], a[232:235]\n" \
"v_mfma_f32_16x16x32_bf16 a[228:231], v[136:139], v[120:123], a[228:231]\n" \
"v_mfma_f32_16x16x32_bf16 a[228:231], v[140:143], v[124:127], a[228:231]\n" \
"s_waitcnt vmcnt(11) lgkmcnt(0)\n" \
"v_mfma_f32_16x16x32_bf16 a[224:227], v[128:131], v[120:123], a[224:227]\n" \
"v_mfma_f32_16x16x32_bf16 a[224:227], v[132:135], v[124:127], a[224:227]\n" \
"s_barrier\n" \
"v_mfma_f32_16x16x32_bf16 a[16:19], v[64:67], v[160:163], a[16:19]\n" \
"s_add_u32 m0, s87, 102400\n" \
"v_mfma_f32_16x16x32_bf16 a[16:19], v[68:71], v[164:167], a[16:19]\n" \
"global_load_lds_dwordx4 v224, s[48:49] offset:-4096\n" \
"v_mfma_f32_16x16x32_bf16 a[20:23], v[64:67], v[168:171], a[20:23]\n" \
"ds_read_b64_tr_b16 v[0:1], v232 offset:0\n" \
"v_mfma_f32_16x16x32_bf16 a[20:23], v[68:71], v[172:175], a[20:23]\n" \
"v_accvgpr_read_b32 v234, a192\n" \
"v_accvgpr_read_b32 v235, a193\n" \
"v_mfma_f32_16x16x32_bf16 a[24:27], v[64:67], v[176:179], a[24:27]\n" \
"ds_read_b64_tr_b16 v[2:3], v232 offset:2048\n" \
"v_accvgpr_read_b32 v236, a194\n" \
"v_mfma_f32_16x16x32_bf16 a[24:27], v[68:71], v[180:183], a[24:27]\n" \
"global_load_lds_dwordx4 v225, s[48:49] offset:-3072\n" \
"v_accvgpr_read_b32 v237, a195\n" \
"v_mfma_f32_16x16x32_bf16 a[28:31], v[64:67], v[184:187], a[28:31]\n" \
"ds_read_b64_tr_b16 v[4:5], v232 offset:8192\n" \
"v_mul_f32 v234, s92, v234\n" \
"v_mfma_f32_16x16x32_bf16 a[28:31], v[68:71], v[188:191], a[28:31]\n" \
"v_mul_f32 v235, s92, v235\n" \
"v_mul_f32 v236, s92, v236\n" \
"v_mfma_f32_16x16x32_bf16 a[60:63], v[72:75], v[184:187], a[60:63]\n" \
"ds_read_b64_tr_b16 v[6:7], v232 offset:10240\n" \
"v_mul_f32 v237, s92, v237\n" \
"v_mfma_f32_16x16x32_bf16 a[60:63], v[76:79], v[188:191], a[60:63]\n" \
"global_load_lds_dwordx4 v226, s[48:49] offset:-2048\n" \
"v_cvt_pk_bf16_f32 v234, v234, v235\n" \
"v_mfma_f32_16x16x32_bf16 a[56:59], v[72:75], v[176:179], a[56:59]\n" \
"ds_read_b64_tr_b16 v[8:9], v233 offset:0\n" \
"v_cvt_pk_bf16_f32 v236, v236, v237\n" \
"v_mfma_f32_16x16x32_bf16 a[56:59], v[76:79], v[180:183], a[56:59]\n" \
"v_accvgpr_write_b32 a192, v234\n" \
"v_accvgpr_write_b32 a193, v236\n" \
"v_mfma_f32_16x16x32_bf16 a[52:55], v[72:75], v[168:171], a[52:55]\n" \
"ds_read_b64_tr_b16 v[10:11], v233 offset:2048\n" \
"v_accvgpr_read_b32 v234, a196\n" \
"v_mfma_f32_16x16x32_bf16 a[52:55], v[76:79], v[172:175], a[52:55]\n" \
"global_load_lds_dwordx4 v227, s[48:49] offset:-1024\n" \
"v_accvgpr_read_b32 v235, a197\n" \
"v_mfma_f32_16x16x32_bf16 a[48:51], v[72:75], v[160:163], a[48:51]\n" \
"s_add_u32 s48, s48, 0xfffffb00\n" \
"s_addc_u32 s49, s49, -1\n" \
"ds_read_b64_tr_b16 v[12:13], v233 offset:8192\n" \
"v_mfma_f32_16x16x32_bf16 a[48:51], v[76:79], v[164:167], a[48:51]\n" \
"v_accvgpr_read_b32 v236, a198\n" \
"v_accvgpr_read_b32 v237, a199\n" \
"v_mfma_f32_16x16x32_bf16 a[80:83], v[80:83], v[160:163], a[80:83]\n" \
"ds_read_b64_tr_b16 v[14:15], v233 offset:10240\n" \
"v_mul_f32 v234, s92, v234\n" \
"v_mfma_f32_16x16x32_bf16 a[80:83], v[84:87], v[164:167], a[80:83]\n" \
"global_load_lds_dwordx4 v224, s[46:47] offset:0\n" \
"v_mul_f32 v235, s92, v235\n" \
"v_mfma_f32_16x16x32_bf16 a[84:87], v[80:83], v[168:171], a[84:87]\n" \
"ds_read_b64_tr_b16 v[16:17], v232 offset:512\n" \
"v_mul_f32 v236, s92, v236\n" \
"v_mfma_f32_16x16x32_bf16 a[84:87], v[84:87], v[172:175], a[84:87]\n" \
"v_mul_f32 v237, s92, v237\n" \
"v_cvt_pk_bf16_f32 v234, v234, v235\n" \
"v_mfma_f32_16x16x32_bf16 a[88:91], v[80:83], v[176:179], a[88:91]\n" \
"ds_read_b64_tr_b16 v[18:19], v232 offset:2560\n" \
"v_cvt_pk_bf16_f32 v236, v236, v237\n" \
"v_mfma_f32_16x16x32_bf16 a[88:91], v[84:87], v[180:183], a[88:91]\n" \
"global_load_lds_dwordx4 v225, s[46:47] offset:1024\n" \
"v_accvgpr_write_b32 a194, v234\n" \
"v_mfma_f32_16x16x32_bf16 a[92:95], v[80:83], v[184:187], a[92:95]\n" \
"ds_read_b64_tr_b16 v[20:21], v232 offset:8704\n" \
"v_accvgpr_write_b32 a195, v236\n" \
"v_mfma_f32_16x16x32_bf16 a[92:95], v[84:87], v[188:191], a[92:95]\n" \
"v_accvgpr_read_b32 v234, a224\n" \
"v_accvgpr_read_b32 v235, a225\n" \
"v_mfma_f32_16x16x32_bf16 a[124:127], v[88:91], v[184:187], a[124:127]\n" \
"ds_read_b64_tr_b16 v[22:23], v232 offset:10752\n" \
"v_accvgpr_read_b32 v236, a226\n" \
"v_mfma_f32_16x16x32_bf16 a[124:127], v[92:95], v[188:191], a[124:127]\n" \
"global_load_lds_dwordx4 v226, s[46:47] offset:2048\n" \
"v_accvgpr_read_b32 v237, a227\n" \
"v_mfma_f32_16x16x32_bf16 a[120:123], v[88:91], v[176:179], a[120:123]\n" \
"ds_read_b64_tr_b16 v[24:25], v233 offset:512\n" \
"v_mul_f32 v234, s92, v234\n" \
"v_mfma_f32_16x16x32_bf16 a[120:123], v[92:95], v[180:183], a[120:123]\n" \
"v_mul_f32 v235, s92, v235\n" \
"v_mul_f32 v236, s92, v236\n" \
"v_mfma_f32_16x16x32_bf16 a[116:119], v[88:91], v[168:171], a[116:119]\n" \
"ds_read_b64_tr_b16 v[26:27], v233 offset:2560\n" \
"v_mul_f32 v237, s92, v237\n" \
"v_mfma_f32_16x16x32_bf16 a[116:119], v[92:95], v[172:175], a[116:119]\n" \
"global_load_lds_dwordx4 v227, s[46:47] offset:3072\n" \
"v_cvt_pk_bf16_f32 v234, v234, v235\n" \
"v_mfma_f32_16x16x32_bf16 a[112:115], v[88:91], v[160:163], a[112:115]\n" \
"s_add_u32 s46, s46, 0xfffffb00\n" \
"s_addc_u32 s47, s47, -1\n" \
"ds_read_b64_tr_b16 v[28:29], v233 offset:8704\n" \
"v_mfma_f32_16x16x32_bf16 a[112:115], v[92:95], v[164:167], a[112:115]\n" \
"v_cvt_pk_bf16_f32 v236, v236, v237\n" \
"v_accvgpr_write_b32 a224, v234\n" \
"v_mfma_f32_16x16x32_bf16 a[144:147], v[96:99], v[160:163], a[144:147]\n" \
"s_add_u32 m0, s95, 36864\n" \
"ds_read_b64_tr_b16 v[30:31], v233 offset:10752\n" \
"v_accvgpr_write_b32 a225, v236\n" \
"v_mfma_f32_16x16x32_bf16 a[144:147], v[100:103], v[164:167], a[144:147]\n" \
"global_load_lds_dwordx4 v228, s[44:45] offset:-4096\n" \
"v_accvgpr_read_b32 v234, a228\n" \
"v_mfma_f32_16x16x32_bf16 a[148:151], v[96:99], v[168:171], a[148:151]\n" \
"ds_read_b128 v[192:195], v230 offset:0\n" \
"v_accvgpr_read_b32 v235, a229\n" \
"v_mfma_f32_16x16x32_bf16 a[148:151], v[100:103], v[172:175], a[148:151]\n" \
"v_accvgpr_read_b32 v236, a230\n" \
"v_accvgpr_read_b32 v237, a231\n" \
"v_mfma_f32_16x16x32_bf16 a[152:155], v[96:99], v[176:179], a[152:155]\n" \
"ds_read_b128 v[196:199], v231 offset:0\n" \
"v_mul_f32 v234, s92, v234\n" \
"v_mfma_f32_16x16x32_bf16 a[152:155], v[100:103], v[180:183], a[152:155]\n" \
"global_load_lds_dwordx4 v229, s[44:45] offset:-3072\n" \
"v_mul_f32 v235, s92, v235\n" \
"v_mfma_f32_16x16x32_bf16 a[156:159], v[96:99], v[184:187], a[156:159]\n" \
"s_add_u32 s44, s44, 0xffb00000\n" \
"s_addc_u32 s45, s45, -1\n" \
"ds_read_b128 v[200:203], v230 offset:2048\n" \
"v_mfma_f32_16x16x32_bf16 a[156:159], v[100:103], v[188:191], a[156:159]\n" \
"v_mul_f32 v236, s92, v236\n" \
"v_mul_f32 v237, s92, v237\n" \
"v_mfma_f32_16x16x32_bf16 a[188:191], v[104:107], v[184:187], a[188:191]\n" \
"ds_read_b128 v[204:207], v231 offset:2048\n" \
"v_cvt_pk_bf16_f32 v234, v234, v235\n" \
"v_mfma_f32_16x16x32_bf16 a[188:191], v[108:111], v[188:191], a[188:191]\n" \
"global_load_lds_dwordx4 v228, s[42:43] offset:-2048\n" \
"v_cvt_pk_bf16_f32 v236, v236, v237\n" \
"v_mfma_f32_16x16x32_bf16 a[184:187], v[104:107], v[176:179], a[184:187]\n" \
"ds_read_b128 v[208:211], v230 offset:4096\n" \
"v_accvgpr_write_b32 a226, v234\n" \
"v_mfma_f32_16x16x32_bf16 a[184:187], v[108:111], v[180:183], a[184:187]\n" \
"v_accvgpr_write_b32 a227, v236\n" \
"v_accvgpr_read_b32 v234, a200\n" \
"v_mfma_f32_16x16x32_bf16 a[180:183], v[104:107], v[168:171], a[180:183]\n" \
"ds_read_b128 v[212:215], v231 offset:4096\n" \
"v_accvgpr_read_b32 v235, a201\n" \
"v_mfma_f32_16x16x32_bf16 a[180:183], v[108:111], v[172:175], a[180:183]\n" \
"global_load_lds_dwordx4 v229, s[42:43] offset:-1024\n" \
"v_accvgpr_read_b32 v236, a202\n" \
"v_mfma_f32_16x16x32_bf16 a[176:179], v[104:107], v[160:163], a[176:179]\n" \
"s_add_u32 s42, s42, 0xffb00000\n" \
"s_addc_u32 s43, s43, -1\n" \
"v_accvgpr_read_b32 v237, a203\n" \
"v_mfma_f32_16x16x32_bf16 a[176:179], v[108:111], v[164:167], a[176:179]\n" \
"v_mul_f32 v234, s92, v234\n" \
"v_mul_f32 v235, s92, v235\n" \
"v_mfma_f32_16x16x32_bf16 a[208:211], v[112:115], v[160:163], a[208:211]\n" \
"s_add_u32 m0, s95, 53248\n" \
"v_mul_f32 v236, s92, v236\n" \
"v_mul_f32 v237, s92, v237\n" \
"v_mfma_f32_16x16x32_bf16 a[208:211], v[116:119], v[164:167], a[208:211]\n" \
"global_load_lds_dwordx4 v228, s[44:45] offset:-4096\n" \
"v_cvt_pk_bf16_f32 v234, v234, v235\n" \
"v_mfma_f32_16x16x32_bf16 a[212:215], v[112:115], v[168:171], a[212:215]\n" \
"v_cvt_pk_bf16_f32 v236, v236, v237\n" \
"v_accvgpr_write_b32 a200, v234\n" \
"v_mfma_f32_16x16x32_bf16 a[212:215], v[116:119], v[172:175], a[212:215]\n" \
"v_accvgpr_write_b32 a201, v236\n" \
"v_accvgpr_read_b32 v234, a204\n" \
"v_mfma_f32_16x16x32_bf16 a[216:219], v[112:115], v[176:179], a[216:219]\n" \
"v_accvgpr_read_b32 v235, a205\n" \
"v_accvgpr_read_b32 v236, a206\n" \
"v_mfma_f32_16x16x32_bf16 a[216:219], v[116:119], v[180:183], a[216:219]\n" \
"global_load_lds_dwordx4 v229, s[44:45] offset:-3072\n" \
"v_accvgpr_read_b32 v237, a207\n" \
"v_mfma_f32_16x16x32_bf16 a[220:223], v[112:115], v[184:187], a[220:223]\n" \
"s_add_u32 s44, s44, 0x80000\n" \
"s_addc_u32 s45, s45, 0\n" \
"v_mul_f32 v234, s92, v234\n" \
"v_mfma_f32_16x16x32_bf16 a[220:223], v[116:119], v[188:191], a[220:223]\n" \
"v_mul_f32 v235, s92, v235\n" \
"v_mul_f32 v236, s92, v236\n" \
"v_mfma_f32_16x16x32_bf16 a[252:255], v[120:123], v[184:187], a[252:255]\n" \
"v_mul_f32 v237, s92, v237\n" \
"v_cvt_pk_bf16_f32 v234, v234, v235\n" \
"v_mfma_f32_16x16x32_bf16 a[252:255], v[124:127], v[188:191], a[252:255]\n" \
"global_load_lds_dwordx4 v228, s[42:43] offset:-2048\n" \
"v_cvt_pk_bf16_f32 v236, v236, v237\n" \
"v_mfma_f32_16x16x32_bf16 a[248:251], v[120:123], v[176:179], a[248:251]\n" \
"v_accvgpr_write_b32 a202, v234\n" \
"v_accvgpr_write_b32 a203, v236\n" \
"v_mfma_f32_16x16x32_bf16 a[248:251], v[124:127], v[180:183], a[248:251]\n" \
"v_accvgpr_read_b32 v234, a232\n" \
"v_accvgpr_read_b32 v235, a233\n" \
"v_mfma_f32_16x16x32_bf16 a[244:247], v[120:123], v[168:171], a[244:247]\n" \
"v_accvgpr_read_b32 v236, a234\n" \
"v_accvgpr_read_b32 v237, a235\n" \
"v_mfma_f32_16x16x32_bf16 a[244:247], v[124:127], v[172:175], a[244:247]\n" \
"global_load_lds_dwordx4 v229, s[42:43] offset:-1024\n" \
"v_mul_f32 v234, s92, v234\n" \
"v_mfma_f32_16x16x32_bf16 a[240:243], v[120:123], v[160:163], a[240:243]\n" \
"s_add_u32 s42, s42, 0x80000\n" \
"s_addc_u32 s43, s43, 0\n" \
"v_mul_f32 v235, s92, v235\n" \
"v_mfma_f32_16x16x32_bf16 a[240:243], v[124:127], v[164:167], a[240:243]\n" \
"s_waitcnt lgkmcnt(0)\n" \
"v_mul_f32 v236, s92, v236\n" \
"v_mul_f32 v237, s92, v237\n" \
"v_mfma_f32_16x16x32_bf16 v[64:67], v[0:3], v[192:195], 0\n" \
"ds_read_b128 v[216:219], v230 offset:6144\n" \
"v_cvt_pk_bf16_f32 v234, v234, v235\n" \
"v_mfma_f32_16x16x32_bf16 v[64:67], v[4:7], v[196:199], v[64:67]\n" \
"ds_read_b128 v[220:223], v231 offset:6144\n" \
"v_cvt_pk_bf16_f32 v236, v236, v237\n" \
"v_mfma_f32_16x16x32_bf16 v[68:71], v[8:11], v[192:195], 0\n" \
"s_add_u32 s90, s88, 0\n" \
"ds_read_b64_tr_b16 v[32:33], v232 offset:1024\n" \
"v_accvgpr_write_b32 a232, v234\n" \
"v_mfma_f32_16x16x32_bf16 v[68:71], v[12:15], v[196:199], v[68:71]\n" \
"buffer_store_dwordx4 a[192:195], v249, s[56:59], s90 offen offset:0 sc1\n" \
"v_accvgpr_write_b32 a233, v236\n" \
"v_accvgpr_read_b32 v234, a236\n" \
"v_accvgpr_read_b32 v235, a237\n" \
"v_accvgpr_read_b32 v236, a238\n" \
"v_mfma_f32_16x16x32_bf16 v[72:75], v[16:19], v[192:195], 0\n" \
"ds_read_b64_tr_b16 v[34:35], v232 offset:3072\n" \
"v_accvgpr_read_b32 v237, a239\n" \
"v_mfma_f32_16x16x32_bf16 v[72:75], v[20:23], v[196:199], v[72:75]\n" \
"ds_read_b64_tr_b16 v[36:37], v232 offset:9216\n" \
"v_mul_f32 v234, s92, v234\n" \
"v_mfma_f32_16x16x32_bf16 v[76:79], v[24:27], v[192:195], 0\n" \
"ds_read_b64_tr_b16 v[38:39], v232 offset:11264\n" \
"v_mul_f32 v235, s92, v235\n" \
"v_mfma_f32_16x16x32_bf16 v[76:79], v[28:31], v[196:199], v[76:79]\n" \
"buffer_store_dwordx4 a[224:227], v249, s[56:59], s90 offen offset:256 sc1\n" \
"v_mul_f32 v236, s92, v236\n" \
"v_mul_f32 v237, s92, v237\n" \
"v_cvt_pk_bf16_f32 v234, v234, v235\n" \
"v_cvt_pk_bf16_f32 v236, v236, v237\n" \
"v_mfma_f32_16x16x32_bf16 v[108:111], v[24:27], v[200:203], 0\n" \
"ds_read_b64_tr_b16 v[40:41], v233 offset:1024\n" \
"v_accvgpr_write_b32 a234, v234\n" \
"v_mfma_f32_16x16x32_bf16 v[108:111], v[28:31], v[204:207], v[108:111]\n" \
"ds_read_b64_tr_b16 v[42:43], v233 offset:3072\n" \
"v_accvgpr_write_b32 a235, v236\n" \
"v_mfma_f32_16x16x32_bf16 v[104:107], v[16:19], v[200:203], 0\n" \
"ds_read_b64_tr_b16 v[44:45], v233 offset:9216\n" \
"v_accvgpr_read_b32 v234, a208\n" \
"v_mfma_f32_16x16x32_bf16 v[104:107], v[20:23], v[204:207], v[104:107]\n" \
"v_accvgpr_read_b32 v235, a209\n" \
"v_accvgpr_read_b32 v236, a210\n" \
"v_mfma_f32_16x16x32_bf16 v[100:103], v[8:11], v[200:203], 0\n" \
"ds_read_b64_tr_b16 v[46:47], v233 offset:11264\n" \
"v_accvgpr_read_b32 v237, a211\n" \
"v_mfma_f32_16x16x32_bf16 v[100:103], v[12:15], v[204:207], v[100:103]\n" \
"ds_read_b64_tr_b16 v[48:49], v232 offset:1536\n" \
"v_cvt_pk_bf16_f32 v234, v234, v235\n" \
"v_mfma_f32_16x16x32_bf16 v[96:99], v[0:3], v[200:203], 0\n" \
"ds_read_b64_tr_b16 v[50:51], v232 offset:3584\n" \
"v_cvt_pk_bf16_f32 v236, v236, v237\n" \
"v_mfma_f32_16x16x32_bf16 v[96:99], v[4:7], v[204:207], v[96:99]\n" \
"v_accvgpr_write_b32 a208, v234\n" \
"v_accvgpr_write_b32 a209, v236\n" \
"v_mfma_f32_16x16x32_bf16 v[128:131], v[0:3], v[208:211], 0\n" \
"ds_read_b64_tr_b16 v[52:53], v232 offset:9728\n" \
"v_accvgpr_read_b32 v234, a240\n" \
"v_mfma_f32_16x16x32_bf16 v[128:131], v[4:7], v[212:215], v[128:131]\n" \
"ds_read_b64_tr_b16 v[54:55], v232 offset:11776\n" \
"v_accvgpr_read_b32 v235, a241\n" \
"v_mfma_f32_16x16x32_bf16 v[132:135], v[8:11], v[208:211], 0\n" \
"ds_read_b64_tr_b16 v[56:57], v233 offset:1536\n" \
"v_accvgpr_read_b32 v236, a242\n" \
"Lpg2228_%=:\n" \
"Lst0x3_%=:\n" \
"s_cbranch_execz Lpg4399_%=\n" \
"v_mfma_f32_16x16x32_bf16 v[132:135], v[12:15], v[212:215], v[132:135]\n" \
"v_accvgpr_read_b32 v237, a243\n" \
"v_cvt_pk_bf16_f32 v234, v234, v235\n" \
"v_mfma_f32_16x16x32_bf16 v[136:139], v[16:19], v[208:211], 0\n" \
"ds_read_b64_tr_b16 v[58:59], v233 offset:3584\n" \
"v_cvt_pk_bf16_f32 v236, v236, v237\n" \
"v_mfma_f32_16x16x32_bf16 v[136:139], v[20:23], v[212:215], v[136:139]\n" \
"ds_read_b64_tr_b16 v[60:61], v233 offset:9728\n" \
"v_accvgpr_write_b32 a210, v234\n" \
"v_mfma_f32_16x16x32_bf16 v[140:143], v[24:27], v[208:211], 0\n" \
"ds_read_b64_tr_b16 v[62:63], v233 offset:11776\n" \
"v_accvgpr_write_b32 a211, v236\n" \
"v_mfma_f32_16x16x32_bf16 v[140:143], v[28:31], v[212:215], v[140:143]\n" \
"s_waitcnt lgkmcnt(3)\n" \
"v_accvgpr_read_b32 v234, a212\n" \
"v_accvgpr_read_b32 v235, a213\n" \
"v_mfma_f32_16x16x32_bf16 v[172:175], v[24:27], v[216:219], 0\n" \
"v_add_u32 v230, 0xffff0000, v230\n" \
"v_add_u32 v231, 0xffff0000, v231\n" \
"v_mfma_f32_16x16x32_bf16 v[172:175], v[28:31], v[220:223], v[172:175]\n" \
"v_add_u32 v232, 0x4000, v232\n" \
"v_add_u32 v233, 0x4000, v233\n" \
"v_mfma_f32_16x16x32_bf16 v[168:171], v[16:19], v[216:219], 0\n" \
"v_accvgpr_read_b32 v236, a214\n" \
"v_accvgpr_read_b32 v237, a215\n" \
"v_mfma_f32_16x16x32_bf16 v[168:171], v[20:23], v[220:223], v[168:171]\n" \
"v_cvt_pk_bf16_f32 v234, v234, v235\n" \
"v_cvt_pk_bf16_f32 v236, v236, v237\n" \
"v_mfma_f32_16x16x32_bf16 v[164:167], v[8:11], v[216:219], 0\n" \
"v_accvgpr_write_b32 a212, v234\n" \
"v_accvgpr_write_b32 a213, v236\n" \
"v_mfma_f32_16x16x32_bf16 v[164:167], v[12:15], v[220:223], v[164:167]\n" \
"s_waitcnt vmcnt(18) lgkmcnt(0)\n" \
"v_accvgpr_read_b32 v234, a244\n" \
"v_accvgpr_read_b32 v235, a245\n" \
"v_mfma_f32_16x16x32_bf16 v[160:163], v[0:3], v[216:219], 0\n" \
"v_accvgpr_read_b32 v236, a246\n" \
"v_accvgpr_read_b32 v237, a247\n" \
"v_mfma_f32_16x16x32_bf16 v[160:163], v[4:7], v[220:223], v[160:163]\n" \
"s_barrier\n" \
"v_mfma_f32_16x16x32_bf16 v[80:83], v[32:35], v[192:195], 0\n" \
"ds_read_b64_tr_b16 v[0:1], v232 offset:0\n" \
"v_cvt_pk_bf16_f32 v234, v234, v235\n" \
"v_mfma_f32_16x16x32_bf16 v[80:83], v[36:39], v[196:199], v[80:83]\n" \
"ds_read_b64_tr_b16 v[2:3], v232 offset:2048\n" \
"v_cvt_pk_bf16_f32 v236, v236, v237\n" \
"v_mfma_f32_16x16x32_bf16 v[84:87], v[40:43], v[192:195], 0\n" \
"s_add_u32 m0, s87, 135168\n" \
"ds_read_b64_tr_b16 v[4:5], v232 offset:8192\n" \
"v_accvgpr_write_b32 a214, v234\n" \
"v_mfma_f32_16x16x32_bf16 v[84:87], v[44:47], v[196:199], v[84:87]\n" \
"global_load_lds_dwordx4 v224, s[48:49] offset:-4096\n" \
"v_accvgpr_write_b32 a215, v236\n" \
"v_mfma_f32_16x16x32_bf16 v[88:91], v[48:51], v[192:195], 0\n" \
"ds_read_b64_tr_b16 v[6:7], v232 offset:10240\n" \
"v_accvgpr_read_b32 v234, a216\n" \
"v_mfma_f32_16x16x32_bf16 v[88:91], v[52:55], v[196:199], v[88:91]\n" \
"ds_read_b64_tr_b16 v[8:9], v233 offset:0\n" \
"v_accvgpr_read_b32 v235, a217\n" \
"v_mfma_f32_16x16x32_bf16 v[92:95], v[56:59], v[192:195], 0\n" \
"ds_read_b64_tr_b16 v[10:11], v233 offset:2048\n" \
"v_accvgpr_read_b32 v236, a218\n" \
"v_mfma_f32_16x16x32_bf16 v[92:95], v[60:63], v[196:199], v[92:95]\n" \
"global_load_lds_dwordx4 v225, s[48:49] offset:-3072\n" \
"v_accvgpr_read_b32 v237, a219\n" \
"v_mfma_f32_16x16x32_bf16 v[124:127], v[56:59], v[200:203], 0\n" \
"ds_read_b128 v[192:195], v230 offset:0\n" \
"v_cvt_pk_bf16_f32 v234, v234, v235\n" \
"v_mfma_f32_16x16x32_bf16 v[124:127], v[60:63], v[204:207], v[124:127]\n" \
"ds_read_b128 v[196:199], v231 offset:0\n" \
"v_cvt_pk_bf16_f32 v236, v236, v237\n" \
"v_mfma_f32_16x16x32_bf16 v[120:123], v[48:51], v[200:203], 0\n" \
"ds_read_b64_tr_b16 v[12:13], v233 offset:8192\n" \
"v_accvgpr_write_b32 a216, v234\n" \
"v_mfma_f32_16x16x32_bf16 v[120:123], v[52:55], v[204:207], v[120:123]\n" \
"global_load_lds_dwordx4 v226, s[48:49] offset:-2048\n" \
"v_accvgpr_write_b32 a217, v236\n" \
"v_mfma_f32_16x16x32_bf16 v[116:119], v[40:43], v[200:203], 0\n" \
"ds_read_b64_tr_b16 v[14:15], v233 offset:10240\n" \
"v_accvgpr_read_b32 v234, a248\n" \
"v_mfma_f32_16x16x32_bf16 v[116:119], v[44:47], v[204:207], v[116:119]\n" \
"ds_read_b64_tr_b16 v[16:17], v232 offset:512\n" \
"v_accvgpr_read_b32 v235, a249\n" \
"v_mfma_f32_16x16x32_bf16 v[112:115], v[32:35], v[200:203], 0\n" \
"ds_read_b64_tr_b16 v[18:19], v232 offset:2560\n" \
"v_accvgpr_read_b32 v236, a250\n" \
"v_mfma_f32_16x16x32_bf16 v[112:115], v[36:39], v[204:207], v[112:115]\n" \
"global_load_lds_dwordx4 v227, s[48:49] offset:-1024\n" \
"v_accvgpr_read_b32 v237, a251\n" \
"v_mfma_f32_16x16x32_bf16 v[144:147], v[32:35], v[208:211], 0\n" \
"s_add_u32 s48, s48, 0x80\n" \
"s_addc_u32 s49, s49, 0\n" \
"ds_read_b128 v[200:203], v230 offset:2048\n" \
"v_mfma_f32_16x16x32_bf16 v[144:147], v[36:39], v[212:215], v[144:147]\n" \
"ds_read_b128 v[204:207], v231 offset:2048\n" \
"v_cvt_pk_bf16_f32 v234, v234, v235\n" \
"v_mfma_f32_16x16x32_bf16 v[148:151], v[40:43], v[208:211], 0\n" \
"ds_read_b64_tr_b16 v[20:21], v232 offset:8704\n" \
"v_cvt_pk_bf16_f32 v236, v236, v237\n" \
"v_mfma_f32_16x16x32_bf16 v[148:151], v[44:47], v[212:215], v[148:151]\n" \
"global_load_lds_dwordx4 v224, s[46:47] offset:0\n" \
"v_accvgpr_write_b32 a218, v234\n" \
"v_mfma_f32_16x16x32_bf16 v[152:155], v[48:51], v[208:211], 0\n" \
"ds_read_b64_tr_b16 v[22:23], v232 offset:10752\n" \
"v_accvgpr_write_b32 a219, v236\n" \
"v_mfma_f32_16x16x32_bf16 v[152:155], v[52:55], v[212:215], v[152:155]\n" \
"ds_read_b64_tr_b16 v[24:25], v233 offset:512\n" \
"v_accvgpr_read_b32 v234, a220\n" \
"v_mfma_f32_16x16x32_bf16 v[156:159], v[56:59], v[208:211], 0\n" \
"ds_read_b64_tr_b16 v[26:27], v233 offset:2560\n" \
"v_accvgpr_read_b32 v235, a221\n" \
"v_mfma_f32_16x16x32_bf16 v[156:159], v[60:63], v[212:215], v[156:159]\n" \
"global_load_lds_dwordx4 v225, s[46:47] offset:1024\n" \
"v_accvgpr_read_b32 v236, a222\n" \
"v_mfma_f32_16x16x32_bf16 v[188:191], v[56:59], v[216:219], 0\n" \
"ds_read_b128 v[208:211], v230 offset:4096\n" \
"v_accvgpr_read_b32 v237, a223\n" \
"v_mfma_f32_16x16x32_bf16 v[188:191], v[60:63], v[220:223], v[188:191]\n" \
"ds_read_b128 v[212:215], v231 offset:4096\n" \
"v_cvt_pk_bf16_f32 v234, v234, v235\n" \
"v_mfma_f32_16x16x32_bf16 v[184:187], v[48:51], v[216:219], 0\n" \
"ds_read_b64_tr_b16 v[28:29], v233 offset:8704\n" \
"v_cvt_pk_bf16_f32 v236, v236, v237\n" \
"v_mfma_f32_16x16x32_bf16 v[184:187], v[52:55], v[220:223], v[184:187]\n" \
"global_load_lds_dwordx4 v226, s[46:47] offset:2048\n" \
"v_accvgpr_write_b32 a220, v234\n" \
"v_mfma_f32_16x16x32_bf16 v[180:183], v[40:43], v[216:219], 0\n" \
"ds_read_b64_tr_b16 v[30:31], v233 offset:10752\n" \
"v_accvgpr_write_b32 a221, v236\n" \
"v_mfma_f32_16x16x32_bf16 v[180:183], v[44:47], v[220:223], v[180:183]\n" \
"v_accvgpr_read_b32 v234, a252\n" \
"v_accvgpr_read_b32 v235, a253\n" \
"v_mfma_f32_16x16x32_bf16 v[176:179], v[32:35], v[216:219], 0\n" \
"v_accvgpr_read_b32 v236, a254\n" \
"v_accvgpr_read_b32 v237, a255\n" \
"v_mfma_f32_16x16x32_bf16 v[176:179], v[36:39], v[220:223], v[176:179]\n" \
"s_waitcnt lgkmcnt(1)\n" \
"global_load_lds_dwordx4 v227, s[46:47] offset:3072\n" \
"v_cvt_pk_bf16_f32 v234, v234, v235\n" \
"v_mfma_f32_16x16x32_bf16 v[64:67], v[0:3], v[192:195], v[64:67]\n" \
"s_add_u32 s46, s46, 0x80\n" \
"s_addc_u32 s47, s47, 0\n" \
"ds_read_b128 v[216:219], v230 offset:6144\n" \
"v_mfma_f32_16x16x32_bf16 v[64:67], v[4:7], v[196:199], v[64:67]\n" \
"ds_read_b128 v[220:223], v231 offset:6144\n" \
"v_cvt_pk_bf16_f32 v236, v236, v237\n" \
"v_mfma_f32_16x16x32_bf16 v[68:71], v[8:11], v[192:195], v[68:71]\n" \
"ds_read_b64_tr_b16 v[32:33], v232 offset:1024\n" \
"v_accvgpr_write_b32 a222, v234\n" \
"v_mfma_f32_16x16x32_bf16 v[68:71], v[12:15], v[196:199], v[68:71]\n" \
"buffer_store_dwordx4 a[200:203], v249, s[56:59], s90 offen offset:2048 sc1\n" \
"v_accvgpr_write_b32 a223, v236\n" \
"v_accvgpr_read_b32 v234, a128\n" \
"v_accvgpr_read_b32 v235, a129\n" \
"v_accvgpr_read_b32 v236, a130\n" \
"v_mfma_f32_16x16x32_bf16 v[72:75], v[16:19], v[192:195], v[72:75]\n" \
"ds_read_b64_tr_b16 v[34:35], v232 offset:3072\n" \
"v_accvgpr_read_b32 v237, a131\n" \
"v_mfma_f32_16x16x32_bf16 v[72:75], v[20:23], v[196:199], v[72:75]\n" \
"s_waitcnt lgkmcnt(3)\n" \
"ds_read_b64_tr_b16 v[36:37], v232 offset:9216\n" \
"v_mul_f32 v234, s92, v234\n" \
"v_mfma_f32_16x16x32_bf16 v[76:79], v[24:27], v[192:195], v[76:79]\n" \
"ds_read_b64_tr_b16 v[38:39], v232 offset:11264\n" \
"v_mul_f32 v235, s92, v235\n" \
"v_mfma_f32_16x16x32_bf16 v[76:79], v[28:31], v[196:199], v[76:79]\n" \
"v_mul_f32 v236, s92, v236\n" \
"v_mul_f32 v237, s92, v237\n" \
"v_mfma_f32_16x16x32_bf16 v[108:111], v[24:27], v[200:203], v[108:111]\n" \
"ds_read_b64_tr_b16 v[40:41], v233 offset:1024\n" \
"v_cvt_pk_bf16_f32 v234, v234, v235\n" \
"v_mfma_f32_16x16x32_bf16 v[108:111], v[28:31], v[204:207], v[108:111]\n" \
"ds_read_b64_tr_b16 v[42:43], v233 offset:3072\n" \
"v_cvt_pk_bf16_f32 v236, v236, v237\n" \
"v_mfma_f32_16x16x32_bf16 v[104:107], v[16:19], v[200:203], v[104:107]\n" \
"ds_read_b64_tr_b16 v[44:45], v233 offset:9216\n" \
"v_accvgpr_write_b32 a128, v234\n" \
"v_mfma_f32_16x16x32_bf16 v[104:107], v[20:23], v[204:207], v[104:107]\n" \
"v_accvgpr_write_b32 a129, v236\n" \
"v_accvgpr_read_b32 v234, a132\n" \
"v_mfma_f32_16x16x32_bf16 v[100:103], v[8:11], v[200:203], v[100:103]\n" \
"ds_read_b64_tr_b16 v[46:47], v233 offset:11264\n" \
"v_accvgpr_read_b32 v235, a133\n" \
"v_mfma_f32_16x16x32_bf16 v[100:103], v[12:15], v[204:207], v[100:103]\n" \
"ds_read_b64_tr_b16 v[48:49], v232 offset:1536\n" \
"v_accvgpr_read_b32 v236, a134\n" \
"v_mfma_f32_16x16x32_bf16 v[96:99], v[0:3], v[200:203], v[96:99]\n" \
"s_add_u32 m0, s95, 4096\n" \
"ds_read_b64_tr_b16 v[50:51], v232 offset:3584\n" \
"v_accvgpr_read_b32 v237, a135\n" \
"v_mfma_f32_16x16x32_bf16 v[96:99], v[4:7], v[204:207], v[96:99]\n" \
"global_load_lds_dwordx4 v228, s[44:45] offset:-4096\n" \
"v_mul_f32 v234, s92, v234\n" \
"v_mfma_f32_16x16x32_bf16 v[128:131], v[0:3], v[208:211], v[128:131]\n" \
"ds_read_b64_tr_b16 v[52:53], v232 offset:9728\n" \
"v_mul_f32 v235, s92, v235\n" \
"v_mfma_f32_16x16x32_bf16 v[128:131], v[4:7], v[212:215], v[128:131]\n" \
"ds_read_b64_tr_b16 v[54:55], v232 offset:11776\n" \
"v_mul_f32 v236, s92, v236\n" \
"v_mfma_f32_16x16x32_bf16 v[132:135], v[8:11], v[208:211], v[132:135]\n" \
"ds_read_b64_tr_b16 v[56:57], v233 offset:1536\n" \
"v_mul_f32 v237, s92, v237\n" \
"v_mfma_f32_16x16x32_bf16 v[132:135], v[12:15], v[212:215], v[132:135]\n" \
"global_load_lds_dwordx4 v229, s[44:45] offset:-3072\n" \
"v_cvt_pk_bf16_f32 v234, v234, v235\n" \
"v_mfma_f32_16x16x32_bf16 v[136:139], v[16:19], v[208:211], v[136:139]\n" \
"s_add_u32 s44, s44, 0x100000\n" \
"s_addc_u32 s45, s45, 0\n" \
"ds_read_b64_tr_b16 v[58:59], v233 offset:3584\n" \
"v_mfma_f32_16x16x32_bf16 v[136:139], v[20:23], v[212:215], v[136:139]\n" \
"ds_read_b64_tr_b16 v[60:61], v233 offset:9728\n" \
"v_cvt_pk_bf16_f32 v236, v236, v237\n" \
"v_mfma_f32_16x16x32_bf16 v[140:143], v[24:27], v[208:211], v[140:143]\n" \
"ds_read_b64_tr_b16 v[62:63], v233 offset:11776\n" \
"v_accvgpr_write_b32 a130, v234\n" \
"v_mfma_f32_16x16x32_bf16 v[140:143], v[28:31], v[212:215], v[140:143]\n" \
"s_waitcnt lgkmcnt(3)\n" \
"global_load_lds_dwordx4 v228, s[42:43] offset:-2048\n" \
"v_accvgpr_write_b32 a131, v236\n" \
"v_mfma_f32_16x16x32_bf16 v[172:175], v[24:27], v[216:219], v[172:175]\n" \
"v_add_u32 v230, 0x8000, v230\n" \
"v_add_u32 v231, 0x8000, v231\n" \
"v_mfma_f32_16x16x32_bf16 v[172:175], v[28:31], v[220:223], v[172:175]\n" \
"v_add_u32 v232, 0x4000, v232\n" \
"v_add_u32 v233, 0x4000, v233\n" \
"v_mfma_f32_16x16x32_bf16 v[168:171], v[16:19], v[216:219], v[168:171]\n" \
"v_accvgpr_read_b32 v234, a160\n" \
"v_accvgpr_read_b32 v235, a161\n" \
"v_mfma_f32_16x16x32_bf16 v[168:171], v[20:23], v[220:223], v[168:171]\n" \
"global_load_lds_dwordx4 v229, s[42:43] offset:-1024\n" \
"v_accvgpr_read_b32 v236, a162\n" \
"v_mfma_f32_16x16x32_bf16 v[164:167], v[8:11], v[216:219], v[164:167]\n" \
"s_add_u32 s42, s42, 0x100000\n" \
"s_addc_u32 s43, s43, 0\n" \
"v_accvgpr_read_b32 v237, a163\n" \
"v_mfma_f32_16x16x32_bf16 v[164:167], v[12:15], v[220:223], v[164:167]\n" \
"s_waitcnt vmcnt(19) lgkmcnt(0)\n" \
"v_mul_f32 v234, s92, v234\n" \
"v_mul_f32 v235, s92, v235\n" \
"v_mfma_f32_16x16x32_bf16 v[160:163], v[0:3], v[216:219], v[160:163]\n" \
"v_mul_f32 v236, s92, v236\n" \
"v_mul_f32 v237, s92, v237\n" \
"v_mfma_f32_16x16x32_bf16 v[160:163], v[4:7], v[220:223], v[160:163]\n" \
"s_barrier\n" \
"v_mfma_f32_16x16x32_bf16 v[80:83], v[32:35], v[192:195], v[80:83]\n" \
"ds_read_b64_tr_b16 v[0:1], v232 offset:0\n" \
"v_cvt_pk_bf16_f32 v234, v234, v235\n" \
"v_mfma_f32_16x16x32_bf16 v[80:83], v[36:39], v[196:199], v[80:83]\n" \
"ds_read_b64_tr_b16 v[2:3], v232 offset:2048\n" \
"v_cvt_pk_bf16_f32 v236, v236, v237\n" \
"v_mfma_f32_16x16x32_bf16 v[84:87], v[40:43], v[192:195], v[84:87]\n" \
"s_add_u32 m0, s87, 69632\n" \
"ds_read_b64_tr_b16 v[4:5], v232 offset:8192\n" \
"v_accvgpr_write_b32 a160, v234\n" \
"v_mfma_f32_16x16x32_bf16 v[84:87], v[44:47], v[196:199], v[84:87]\n" \
"global_load_lds_dwordx4 v224, s[48:49] offset:-4096\n" \
"v_accvgpr_write_b32 a161, v236\n" \
"v_mfma_f32_16x16x32_bf16 v[88:91], v[48:51], v[192:195], v[88:91]\n" \
"ds_read_b64_tr_b16 v[6:7], v232 offset:10240\n" \
"v_accvgpr_read_b32 v234, a164\n" \
"v_mfma_f32_16x16x32_bf16 v[88:91], v[52:55], v[196:199], v[88:91]\n" \
"ds_read_b64_tr_b16 v[8:9], v233 offset:0\n" \
"v_accvgpr_read_b32 v235, a165\n" \
"v_mfma_f32_16x16x32_bf16 v[92:95], v[56:59], v[192:195], v[92:95]\n" \
"ds_read_b64_tr_b16 v[10:11], v233 offset:2048\n" \
"v_accvgpr_read_b32 v236, a166\n" \
"v_mfma_f32_16x16x32_bf16 v[92:95], v[60:63], v[196:199], v[92:95]\n" \
"global_load_lds_dwordx4 v225, s[48:49] offset:-3072\n" \
"v_accvgpr_read_b32 v237, a167\n" \
"v_mfma_f32_16x16x32_bf16 v[124:127], v[56:59], v[200:203], v[124:127]\n" \
"ds_read_b128 v[192:195], v230 offset:0\n" \
"v_mul_f32 v234, s92, v234\n" \
"v_mfma_f32_16x16x32_bf16 v[124:127], v[60:63], v[204:207], v[124:127]\n" \
"ds_read_b128 v[196:199], v231 offset:0\n" \
"v_mul_f32 v235, s92, v235\n" \
"v_mfma_f32_16x16x32_bf16 v[120:123], v[48:51], v[200:203], v[120:123]\n" \
"ds_read_b64_tr_b16 v[12:13], v233 offset:8192\n" \
"v_mul_f32 v236, s92, v236\n" \
"v_mfma_f32_16x16x32_bf16 v[120:123], v[52:55], v[204:207], v[120:123]\n" \
"global_load_lds_dwordx4 v226, s[48:49] offset:-2048\n" \
"v_mul_f32 v237, s92, v237\n" \
"v_mfma_f32_16x16x32_bf16 v[116:119], v[40:43], v[200:203], v[116:119]\n" \
"ds_read_b64_tr_b16 v[14:15], v233 offset:10240\n" \
"v_cvt_pk_bf16_f32 v234, v234, v235\n" \
"v_mfma_f32_16x16x32_bf16 v[116:119], v[44:47], v[204:207], v[116:119]\n" \
"ds_read_b64_tr_b16 v[16:17], v232 offset:512\n" \
"v_cvt_pk_bf16_f32 v236, v236, v237\n" \
"v_mfma_f32_16x16x32_bf16 v[112:115], v[32:35], v[200:203], v[112:115]\n" \
"ds_read_b64_tr_b16 v[18:19], v232 offset:2560\n" \
"v_accvgpr_write_b32 a162, v234\n" \
"v_mfma_f32_16x16x32_bf16 v[112:115], v[36:39], v[204:207], v[112:115]\n" \
"global_load_lds_dwordx4 v227, s[48:49] offset:-1024\n" \
"v_accvgpr_write_b32 a163, v236\n" \
"v_mfma_f32_16x16x32_bf16 v[144:147], v[32:35], v[208:211], v[144:147]\n" \
"s_add_u32 s48, s48, 0x100\n" \
"s_addc_u32 s49, s49, 0\n" \
"ds_read_b128 v[200:203], v230 offset:2048\n" \
"v_mfma_f32_16x16x32_bf16 v[144:147], v[36:39], v[212:215], v[144:147]\n" \
"ds_read_b128 v[204:207], v231 offset:2048\n" \
"v_accvgpr_read_b32 v234, a136\n" \
"v_mfma_f32_16x16x32_bf16 v[148:151], v[40:43], v[208:211], v[148:151]\n" \
"ds_read_b64_tr_b16 v[20:21], v232 offset:8704\n" \
"v_accvgpr_read_b32 v235, a137\n" \
"v_mfma_f32_16x16x32_bf16 v[148:151], v[44:47], v[212:215], v[148:151]\n" \
"global_load_lds_dwordx4 v224, s[46:47] offset:0\n" \
"v_accvgpr_read_b32 v236, a138\n" \
"v_mfma_f32_16x16x32_bf16 v[152:155], v[48:51], v[208:211], v[152:155]\n" \
"ds_read_b64_tr_b16 v[22:23], v232 offset:10752\n" \
"v_accvgpr_read_b32 v237, a139\n" \
"v_mfma_f32_16x16x32_bf16 v[152:155], v[52:55], v[212:215], v[152:155]\n" \
"ds_read_b64_tr_b16 v[24:25], v233 offset:512\n" \
"v_mul_f32 v234, s92, v234\n" \
"v_mfma_f32_16x16x32_bf16 v[156:159], v[56:59], v[208:211], v[156:159]\n" \
"ds_read_b64_tr_b16 v[26:27], v233 offset:2560\n" \
"v_mul_f32 v235, s92, v235\n" \
"v_mfma_f32_16x16x32_bf16 v[156:159], v[60:63], v[212:215], v[156:159]\n" \
"global_load_lds_dwordx4 v225, s[46:47] offset:1024\n" \
"v_mul_f32 v236, s92, v236\n" \
"v_mfma_f32_16x16x32_bf16 v[188:191], v[56:59], v[216:219], v[188:191]\n" \
"ds_read_b128 v[208:211], v230 offset:4096\n" \
"v_mul_f32 v237, s92, v237\n" \
"v_mfma_f32_16x16x32_bf16 v[188:191], v[60:63], v[220:223], v[188:191]\n" \
"ds_read_b128 v[212:215], v231 offset:4096\n" \
"v_cvt_pk_bf16_f32 v234, v234, v235\n" \
"v_mfma_f32_16x16x32_bf16 v[184:187], v[48:51], v[216:219], v[184:187]\n" \
"ds_read_b64_tr_b16 v[28:29], v233 offset:8704\n" \
"v_cvt_pk_bf16_f32 v236, v236, v237\n" \
"v_mfma_f32_16x16x32_bf16 v[184:187], v[52:55], v[220:223], v[184:187]\n" \
"global_load_lds_dwordx4 v226, s[46:47] offset:2048\n" \
"v_accvgpr_write_b32 a136, v234\n" \
"v_mfma_f32_16x16x32_bf16 v[180:183], v[40:43], v[216:219], v[180:183]\n" \
"ds_read_b64_tr_b16 v[30:31], v233 offset:10752\n" \
"v_accvgpr_write_b32 a137, v236\n" \
"v_mfma_f32_16x16x32_bf16 v[180:183], v[44:47], v[220:223], v[180:183]\n" \
"v_accvgpr_read_b32 v234, a140\n" \
"v_accvgpr_read_b32 v235, a141\n" \
"v_mfma_f32_16x16x32_bf16 v[176:179], v[32:35], v[216:219], v[176:179]\n" \
"v_accvgpr_read_b32 v236, a142\n" \
"v_accvgpr_read_b32 v237, a143\n" \
"v_mfma_f32_16x16x32_bf16 v[176:179], v[36:39], v[220:223], v[176:179]\n" \
"s_waitcnt lgkmcnt(1)\n" \
"global_load_lds_dwordx4 v227, s[46:47] offset:3072\n" \
"v_mul_f32 v234, s92, v234\n" \
"v_mfma_f32_16x16x32_bf16 v[64:67], v[0:3], v[192:195], v[64:67]\n" \
"s_add_u32 s46, s46, 0x100\n" \
"s_addc_u32 s47, s47, 0\n" \
"ds_read_b128 v[216:219], v230 offset:6144\n" \
"v_mfma_f32_16x16x32_bf16 v[64:67], v[4:7], v[196:199], v[64:67]\n" \
"ds_read_b128 v[220:223], v231 offset:6144\n" \
"v_mul_f32 v235, s92, v235\n" \
"v_mfma_f32_16x16x32_bf16 v[68:71], v[8:11], v[192:195], v[68:71]\n" \
"ds_read_b64_tr_b16 v[32:33], v232 offset:1024\n" \
"v_mul_f32 v236, s92, v236\n" \
"v_mfma_f32_16x16x32_bf16 v[68:71], v[12:15], v[196:199], v[68:71]\n" \
"buffer_store_dwordx4 a[232:235], v249, s[56:59], s90 offen offset:2304 sc1\n" \
"v_mul_f32 v237, s92, v237\n" \
"v_cvt_pk_bf16_f32 v234, v234, v235\n" \
"v_cvt_pk_bf16_f32 v236, v236, v237\n" \
"v_accvgpr_write_b32 a138, v234\n" \
"v_mfma_f32_16x16x32_bf16 v[72:75], v[16:19], v[192:195], v[72:75]\n" \
"ds_read_b64_tr_b16 v[34:35], v232 offset:3072\n" \
"v_accvgpr_write_b32 a139, v236\n" \
"v_mfma_f32_16x16x32_bf16 v[72:75], v[20:23], v[196:199], v[72:75]\n" \
"s_waitcnt lgkmcnt(3)\n" \
"ds_read_b64_tr_b16 v[36:37], v232 offset:9216\n" \
"v_accvgpr_read_b32 v234, a168\n" \
"v_mfma_f32_16x16x32_bf16 v[76:79], v[24:27], v[192:195], v[76:79]\n" \
"ds_read_b64_tr_b16 v[38:39], v232 offset:11264\n" \
"v_accvgpr_read_b32 v235, a169\n" \
"v_mfma_f32_16x16x32_bf16 v[76:79], v[28:31], v[196:199], v[76:79]\n" \
"v_accvgpr_read_b32 v236, a170\n" \
"v_accvgpr_read_b32 v237, a171\n" \
"v_mfma_f32_16x16x32_bf16 v[108:111], v[24:27], v[200:203], v[108:111]\n" \
"ds_read_b64_tr_b16 v[40:41], v233 offset:1024\n" \
"v_mul_f32 v234, s92, v234\n" \
"v_mfma_f32_16x16x32_bf16 v[108:111], v[28:31], v[204:207], v[108:111]\n" \
"ds_read_b64_tr_b16 v[42:43], v233 offset:3072\n" \
"v_mul_f32 v235, s92, v235\n" \
"v_mfma_f32_16x16x32_bf16 v[104:107], v[16:19], v[200:203], v[104:107]\n" \
"ds_read_b64_tr_b16 v[44:45], v233 offset:9216\n" \
"v_mul_f32 v236, s92, v236\n" \
"v_mfma_f32_16x16x32_bf16 v[104:107], v[20:23], v[204:207], v[104:107]\n" \
"v_mul_f32 v237, s92, v237\n" \
"v_cvt_pk_bf16_f32 v234, v234, v235\n" \
"v_mfma_f32_16x16x32_bf16 v[100:103], v[8:11], v[200:203], v[100:103]\n" \
"ds_read_b64_tr_b16 v[46:47], v233 offset:11264\n" \
"v_cvt_pk_bf16_f32 v236, v236, v237\n" \
"v_mfma_f32_16x16x32_bf16 v[100:103], v[12:15], v[204:207], v[100:103]\n" \
"ds_read_b64_tr_b16 v[48:49], v232 offset:1536\n" \
"v_accvgpr_write_b32 a168, v234\n" \
"v_mfma_f32_16x16x32_bf16 v[96:99], v[0:3], v[200:203], v[96:99]\n" \
"s_add_u32 m0, s95, 20480\n" \
"ds_read_b64_tr_b16 v[50:51], v232 offset:3584\n" \
"v_accvgpr_write_b32 a169, v236\n" \
"v_mfma_f32_16x16x32_bf16 v[96:99], v[4:7], v[204:207], v[96:99]\n" \
"global_load_lds_dwordx4 v228, s[44:45] offset:-4096\n" \
"v_accvgpr_read_b32 v234, a172\n" \
"v_mfma_f32_16x16x32_bf16 v[128:131], v[0:3], v[208:211], v[128:131]\n" \
"ds_read_b64_tr_b16 v[52:53], v232 offset:9728\n" \
"v_accvgpr_read_b32 v235, a173\n" \
"v_mfma_f32_16x16x32_bf16 v[128:131], v[4:7], v[212:215], v[128:131]\n" \
"ds_read_b64_tr_b16 v[54:55], v232 offset:11776\n" \
"v_accvgpr_read_b32 v236, a174\n" \
"v_mfma_f32_16x16x32_bf16 v[132:135], v[8:11], v[208:211], v[132:135]\n" \
"ds_read_b64_tr_b16 v[56:57], v233 offset:1536\n" \
"v_accvgpr_read_b32 v237, a175\n" \
"v_mfma_f32_16x16x32_bf16 v[132:135], v[12:15], v[212:215], v[132:135]\n" \
"global_load_lds_dwordx4 v229, s[44:45] offset:-3072\n" \
"v_mul_f32 v234, s92, v234\n" \
"v_mfma_f32_16x16x32_bf16 v[136:139], v[16:19], v[208:211], v[136:139]\n" \
"s_add_u32 s44, s44, 0x80000\n" \
"s_addc_u32 s45, s45, 0\n" \
"ds_read_b64_tr_b16 v[58:59], v233 offset:3584\n" \
"v_mfma_f32_16x16x32_bf16 v[136:139], v[20:23], v[212:215], v[136:139]\n" \
"ds_read_b64_tr_b16 v[60:61], v233 offset:9728\n" \
"v_mul_f32 v235, s92, v235\n" \
"v_mfma_f32_16x16x32_bf16 v[140:143], v[24:27], v[208:211], v[140:143]\n" \
"ds_read_b64_tr_b16 v[62:63], v233 offset:11776\n" \
"v_mul_f32 v236, s92, v236\n" \
"v_mfma_f32_16x16x32_bf16 v[140:143], v[28:31], v[212:215], v[140:143]\n" \
"s_waitcnt lgkmcnt(3)\n" \
"global_load_lds_dwordx4 v228, s[42:43] offset:-2048\n" \
"v_mul_f32 v237, s92, v237\n" \
"v_mfma_f32_16x16x32_bf16 v[172:175], v[24:27], v[216:219], v[172:175]\n" \
"v_add_u32 v230, 0x8000, v230\n" \
"v_add_u32 v231, 0x8000, v231\n" \
"v_mfma_f32_16x16x32_bf16 v[172:175], v[28:31], v[220:223], v[172:175]\n" \
"v_add_u32 v232, 0x4000, v232\n" \
"v_add_u32 v233, 0x4000, v233\n" \
"v_mfma_f32_16x16x32_bf16 v[168:171], v[16:19], v[216:219], v[168:171]\n" \
"v_cvt_pk_bf16_f32 v234, v234, v235\n" \
"v_cvt_pk_bf16_f32 v236, v236, v237\n" \
"v_mfma_f32_16x16x32_bf16 v[168:171], v[20:23], v[220:223], v[168:171]\n" \
"global_load_lds_dwordx4 v229, s[42:43] offset:-1024\n" \
"v_accvgpr_write_b32 a170, v234\n" \
"v_mfma_f32_16x16x32_bf16 v[164:167], v[8:11], v[216:219], v[164:167]\n" \
"s_add_u32 s42, s42, 0x80000\n" \
"s_addc_u32 s43, s43, 0\n" \
"v_accvgpr_write_b32 a171, v236\n" \
"v_mfma_f32_16x16x32_bf16 v[164:167], v[12:15], v[220:223], v[164:167]\n" \
"s_waitcnt vmcnt(18) lgkmcnt(0)\n" \
"v_accvgpr_read_b32 v234, a144\n" \
"v_accvgpr_read_b32 v235, a145\n" \
"v_mfma_f32_16x16x32_bf16 v[160:163], v[0:3], v[216:219], v[160:163]\n" \
"v_accvgpr_read_b32 v236, a146\n" \
"v_accvgpr_read_b32 v237, a147\n" \
"v_mfma_f32_16x16x32_bf16 v[160:163], v[4:7], v[220:223], v[160:163]\n" \
"s_barrier\n" \
"v_mfma_f32_16x16x32_bf16 v[80:83], v[32:35], v[192:195], v[80:83]\n" \
"ds_read_b64_tr_b16 v[0:1], v232 offset:0\n" \
"v_cvt_pk_bf16_f32 v234, v234, v235\n" \
"v_mfma_f32_16x16x32_bf16 v[80:83], v[36:39], v[196:199], v[80:83]\n" \
"ds_read_b64_tr_b16 v[2:3], v232 offset:2048\n" \
"v_cvt_pk_bf16_f32 v236, v236, v237\n" \
"v_mfma_f32_16x16x32_bf16 v[84:87], v[40:43], v[192:195], v[84:87]\n" \
"s_add_u32 m0, s87, 102400\n" \
"ds_read_b64_tr_b16 v[4:5], v232 offset:8192\n" \
"v_accvgpr_write_b32 a144, v234\n" \
"v_mfma_f32_16x16x32_bf16 v[84:87], v[44:47], v[196:199], v[84:87]\n" \
"global_load_lds_dwordx4 v224, s[48:49] offset:-4096\n" \
"v_accvgpr_write_b32 a145, v236\n" \
"v_mfma_f32_16x16x32_bf16 v[88:91], v[48:51], v[192:195], v[88:91]\n" \
"ds_read_b64_tr_b16 v[6:7], v232 offset:10240\n" \
"v_accvgpr_read_b32 v234, a176\n" \
"v_mfma_f32_16x16x32_bf16 v[88:91], v[52:55], v[196:199], v[88:91]\n" \
"ds_read_b64_tr_b16 v[8:9], v233 offset:0\n" \
"v_accvgpr_read_b32 v235, a177\n" \
"v_mfma_f32_16x16x32_bf16 v[92:95], v[56:59], v[192:195], v[92:95]\n" \
"ds_read_b64_tr_b16 v[10:11], v233 offset:2048\n" \
"v_accvgpr_read_b32 v236, a178\n" \
"v_mfma_f32_16x16x32_bf16 v[92:95], v[60:63], v[196:199], v[92:95]\n" \
"global_load_lds_dwordx4 v225, s[48:49] offset:-3072\n" \
"v_accvgpr_read_b32 v237, a179\n" \
"v_mfma_f32_16x16x32_bf16 v[124:127], v[56:59], v[200:203], v[124:127]\n" \
"ds_read_b128 v[192:195], v230 offset:0\n" \
"v_cvt_pk_bf16_f32 v234, v234, v235\n" \
"v_mfma_f32_16x16x32_bf16 v[124:127], v[60:63], v[204:207], v[124:127]\n" \
"ds_read_b128 v[196:199], v231 offset:0\n" \
"v_cvt_pk_bf16_f32 v236, v236, v237\n" \
"v_mfma_f32_16x16x32_bf16 v[120:123], v[48:51], v[200:203], v[120:123]\n" \
"ds_read_b64_tr_b16 v[12:13], v233 offset:8192\n" \
"v_accvgpr_write_b32 a146, v234\n" \
"v_mfma_f32_16x16x32_bf16 v[120:123], v[52:55], v[204:207], v[120:123]\n" \
"global_load_lds_dwordx4 v226, s[48:49] offset:-2048\n" \
"v_accvgpr_write_b32 a147, v236\n" \
"v_mfma_f32_16x16x32_bf16 v[116:119], v[40:43], v[200:203], v[116:119]\n" \
"ds_read_b64_tr_b16 v[14:15], v233 offset:10240\n" \
"v_accvgpr_read_b32 v234, a148\n" \
"v_mfma_f32_16x16x32_bf16 v[116:119], v[44:47], v[204:207], v[116:119]\n" \
"ds_read_b64_tr_b16 v[16:17], v232 offset:512\n" \
"v_accvgpr_read_b32 v235, a149\n" \
"v_mfma_f32_16x16x32_bf16 v[112:115], v[32:35], v[200:203], v[112:115]\n" \
"ds_read_b64_tr_b16 v[18:19], v232 offset:2560\n" \
"v_accvgpr_read_b32 v236, a150\n" \
"v_mfma_f32_16x16x32_bf16 v[112:115], v[36:39], v[204:207], v[112:115]\n" \
"global_load_lds_dwordx4 v227, s[48:49] offset:-1024\n" \
"v_accvgpr_read_b32 v237, a151\n" \
"v_mfma_f32_16x16x32_bf16 v[144:147], v[32:35], v[208:211], v[144:147]\n" \
"s_add_u32 s48, s48, 0x80\n" \
"s_addc_u32 s49, s49, 0\n" \
"ds_read_b128 v[200:203], v230 offset:2048\n" \
"v_mfma_f32_16x16x32_bf16 v[144:147], v[36:39], v[212:215], v[144:147]\n" \
"ds_read_b128 v[204:207], v231 offset:2048\n" \
"v_cvt_pk_bf16_f32 v234, v234, v235\n" \
"v_mfma_f32_16x16x32_bf16 v[148:151], v[40:43], v[208:211], v[148:151]\n" \
"ds_read_b64_tr_b16 v[20:21], v232 offset:8704\n" \
"v_cvt_pk_bf16_f32 v236, v236, v237\n" \
"v_mfma_f32_16x16x32_bf16 v[148:151], v[44:47], v[212:215], v[148:151]\n" \
"global_load_lds_dwordx4 v224, s[46:47] offset:0\n" \
"v_accvgpr_write_b32 a148, v234\n" \
"v_mfma_f32_16x16x32_bf16 v[152:155], v[48:51], v[208:211], v[152:155]\n" \
"ds_read_b64_tr_b16 v[22:23], v232 offset:10752\n" \
"v_accvgpr_write_b32 a149, v236\n" \
"v_mfma_f32_16x16x32_bf16 v[152:155], v[52:55], v[212:215], v[152:155]\n" \
"ds_read_b64_tr_b16 v[24:25], v233 offset:512\n" \
"v_accvgpr_read_b32 v234, a180\n" \
"v_mfma_f32_16x16x32_bf16 v[156:159], v[56:59], v[208:211], v[156:159]\n" \
"ds_read_b64_tr_b16 v[26:27], v233 offset:2560\n" \
"v_accvgpr_read_b32 v235, a181\n" \
"v_mfma_f32_16x16x32_bf16 v[156:159], v[60:63], v[212:215], v[156:159]\n" \
"global_load_lds_dwordx4 v225, s[46:47] offset:1024\n" \
"v_accvgpr_read_b32 v236, a182\n" \
"v_mfma_f32_16x16x32_bf16 v[188:191], v[56:59], v[216:219], v[188:191]\n" \
"ds_read_b128 v[208:211], v230 offset:4096\n" \
"v_accvgpr_read_b32 v237, a183\n" \
"v_mfma_f32_16x16x32_bf16 v[188:191], v[60:63], v[220:223], v[188:191]\n" \
"ds_read_b128 v[212:215], v231 offset:4096\n" \
"v_cvt_pk_bf16_f32 v234, v234, v235\n" \
"v_mfma_f32_16x16x32_bf16 v[184:187], v[48:51], v[216:219], v[184:187]\n" \
"ds_read_b64_tr_b16 v[28:29], v233 offset:8704\n" \
"v_cvt_pk_bf16_f32 v236, v236, v237\n" \
"v_mfma_f32_16x16x32_bf16 v[184:187], v[52:55], v[220:223], v[184:187]\n" \
"global_load_lds_dwordx4 v226, s[46:47] offset:2048\n" \
"v_accvgpr_write_b32 a150, v234\n" \
"v_mfma_f32_16x16x32_bf16 v[180:183], v[40:43], v[216:219], v[180:183]\n" \
"ds_read_b64_tr_b16 v[30:31], v233 offset:10752\n" \
"v_accvgpr_write_b32 a151, v236\n" \
"v_mfma_f32_16x16x32_bf16 v[180:183], v[44:47], v[220:223], v[180:183]\n" \
"Lpg2767_%=:\n" \
"s_cbranch_execz Lpg4936_%=\n" \
"v_accvgpr_read_b32 v234, a152\n" \
"v_accvgpr_read_b32 v235, a153\n" \
"v_mfma_f32_16x16x32_bf16 v[176:179], v[32:35], v[216:219], v[176:179]\n" \
"v_accvgpr_read_b32 v236, a154\n" \
"v_accvgpr_read_b32 v237, a155\n" \
"v_mfma_f32_16x16x32_bf16 v[176:179], v[36:39], v[220:223], v[176:179]\n" \
"s_waitcnt lgkmcnt(1)\n" \
"global_load_lds_dwordx4 v227, s[46:47] offset:3072\n" \
"v_cvt_pk_bf16_f32 v234, v234, v235\n" \
"v_mfma_f32_16x16x32_bf16 v[64:67], v[0:3], v[192:195], v[64:67]\n" \
"s_add_u32 s46, s46, 0x80\n" \
"s_addc_u32 s47, s47, 0\n" \
"ds_read_b128 v[216:219], v230 offset:6144\n" \
"v_mfma_f32_16x16x32_bf16 v[64:67], v[4:7], v[196:199], v[64:67]\n" \
"s_add_u32 s90, s89, 0\n" \
"ds_read_b128 v[220:223], v231 offset:6144\n" \
"v_cvt_pk_bf16_f32 v236, v236, v237\n" \
"v_mfma_f32_16x16x32_bf16 v[68:71], v[8:11], v[192:195], v[68:71]\n" \
"ds_read_b64_tr_b16 v[32:33], v232 offset:1024\n" \
"v_accvgpr_write_b32 a152, v234\n" \
"v_mfma_f32_16x16x32_bf16 v[68:71], v[12:15], v[196:199], v[68:71]\n" \
"buffer_store_dwordx4 a[208:211], v248, s[56:59], s90 offen offset:0 sc1\n" \
"v_accvgpr_write_b32 a153, v236\n" \
"v_accvgpr_read_b32 v234, a184\n" \
"v_accvgpr_read_b32 v235, a185\n" \
"v_accvgpr_read_b32 v236, a186\n" \
"v_mfma_f32_16x16x32_bf16 v[72:75], v[16:19], v[192:195], v[72:75]\n" \
"ds_read_b64_tr_b16 v[34:35], v232 offset:3072\n" \
"v_accvgpr_read_b32 v237, a187\n" \
"v_mfma_f32_16x16x32_bf16 v[72:75], v[20:23], v[196:199], v[72:75]\n" \
"s_waitcnt lgkmcnt(3)\n" \
"ds_read_b64_tr_b16 v[36:37], v232 offset:9216\n" \
"v_cvt_pk_bf16_f32 v234, v234, v235\n" \
"v_mfma_f32_16x16x32_bf16 v[76:79], v[24:27], v[192:195], v[76:79]\n" \
"ds_read_b64_tr_b16 v[38:39], v232 offset:11264\n" \
"v_cvt_pk_bf16_f32 v236, v236, v237\n" \
"v_mfma_f32_16x16x32_bf16 v[76:79], v[28:31], v[196:199], v[76:79]\n" \
"v_accvgpr_write_b32 a154, v234\n" \
"v_accvgpr_write_b32 a155, v236\n" \
"v_mfma_f32_16x16x32_bf16 v[108:111], v[24:27], v[200:203], v[108:111]\n" \
"ds_read_b64_tr_b16 v[40:41], v233 offset:1024\n" \
"v_accvgpr_read_b32 v234, a156\n" \
"v_mfma_f32_16x16x32_bf16 v[108:111], v[28:31], v[204:207], v[108:111]\n" \
"ds_read_b64_tr_b16 v[42:43], v233 offset:3072\n" \
"v_accvgpr_read_b32 v235, a157\n" \
"v_mfma_f32_16x16x32_bf16 v[104:107], v[16:19], v[200:203], v[104:107]\n" \
"ds_read_b64_tr_b16 v[44:45], v233 offset:9216\n" \
"v_accvgpr_read_b32 v236, a158\n" \
"v_mfma_f32_16x16x32_bf16 v[104:107], v[20:23], v[204:207], v[104:107]\n" \
"v_accvgpr_read_b32 v237, a159\n" \
"v_cvt_pk_bf16_f32 v234, v234, v235\n" \
"v_mfma_f32_16x16x32_bf16 v[100:103], v[8:11], v[200:203], v[100:103]\n" \
"ds_read_b64_tr_b16 v[46:47], v233 offset:11264\n" \
"v_cvt_pk_bf16_f32 v236, v236, v237\n" \
"v_mfma_f32_16x16x32_bf16 v[100:103], v[12:15], v[204:207], v[100:103]\n" \
"ds_read_b64_tr_b16 v[48:49], v232 offset:1536\n" \
"v_accvgpr_write_b32 a156, v234\n" \
"v_mfma_f32_16x16x32_bf16 v[96:99], v[0:3], v[200:203], v[96:99]\n" \
"s_add_u32 m0, s95, 36864\n" \
"ds_read_b64_tr_b16 v[50:51], v232 offset:3584\n" \
"v_accvgpr_write_b32 a157, v236\n" \
"v_mfma_f32_16x16x32_bf16 v[96:99], v[4:7], v[204:207], v[96:99]\n" \
"global_load_lds_dwordx4 v228, s[44:45] offset:-4096\n" \
"v_accvgpr_read_b32 v234, a188\n" \
"v_mfma_f32_16x16x32_bf16 v[128:131], v[0:3], v[208:211], v[128:131]\n" \
"ds_read_b64_tr_b16 v[52:53], v232 offset:9728\n" \
"v_accvgpr_read_b32 v235, a189\n" \
"v_mfma_f32_16x16x32_bf16 v[128:131], v[4:7], v[212:215], v[128:131]\n" \
"ds_read_b64_tr_b16 v[54:55], v232 offset:11776\n" \
"v_accvgpr_read_b32 v236, a190\n" \
"v_mfma_f32_16x16x32_bf16 v[132:135], v[8:11], v[208:211], v[132:135]\n" \
"ds_read_b64_tr_b16 v[56:57], v233 offset:1536\n" \
"v_accvgpr_read_b32 v237, a191\n" \
"v_mfma_f32_16x16x32_bf16 v[132:135], v[12:15], v[212:215], v[132:135]\n" \
"global_load_lds_dwordx4 v229, s[44:45] offset:-3072\n" \
"v_cvt_pk_bf16_f32 v234, v234, v235\n" \
"v_mfma_f32_16x16x32_bf16 v[136:139], v[16:19], v[208:211], v[136:139]\n" \
"s_add_u32 s44, s44, 0x80000\n" \
"s_addc_u32 s45, s45, 0\n" \
"ds_read_b64_tr_b16 v[58:59], v233 offset:3584\n" \
"v_mfma_f32_16x16x32_bf16 v[136:139], v[20:23], v[212:215], v[136:139]\n" \
"ds_read_b64_tr_b16 v[60:61], v233 offset:9728\n" \
"v_cvt_pk_bf16_f32 v236, v236, v237\n" \
"v_mfma_f32_16x16x32_bf16 v[140:143], v[24:27], v[208:211], v[140:143]\n" \
"ds_read_b64_tr_b16 v[62:63], v233 offset:11776\n" \
"v_accvgpr_write_b32 a158, v234\n" \
"v_mfma_f32_16x16x32_bf16 v[140:143], v[28:31], v[212:215], v[140:143]\n" \
"s_waitcnt lgkmcnt(3)\n" \
"global_load_lds_dwordx4 v228, s[42:43] offset:-2048\n" \
"v_accvgpr_write_b32 a159, v236\n" \
"v_mfma_f32_16x16x32_bf16 v[172:175], v[24:27], v[216:219], v[172:175]\n" \
"v_add_u32 v230, 0xffff0000, v230\n" \
"v_add_u32 v231, 0xffff0000, v231\n" \
"v_mfma_f32_16x16x32_bf16 v[172:175], v[28:31], v[220:223], v[172:175]\n" \
"v_add_u32 v232, 0xffff4000, v232\n" \
"v_add_u32 v233, 0xffff4000, v233\n" \
"v_mfma_f32_16x16x32_bf16 v[168:171], v[16:19], v[216:219], v[168:171]\n" \
"v_accvgpr_read_b32 v234, a64\n" \
"v_accvgpr_read_b32 v235, a65\n" \
"v_mfma_f32_16x16x32_bf16 v[168:171], v[20:23], v[220:223], v[168:171]\n" \
"global_load_lds_dwordx4 v229, s[42:43] offset:-1024\n" \
"v_accvgpr_read_b32 v236, a66\n" \
"v_mfma_f32_16x16x32_bf16 v[164:167], v[8:11], v[216:219], v[164:167]\n" \
"s_add_u32 s42, s42, 0x80000\n" \
"s_addc_u32 s43, s43, 0\n" \
"v_accvgpr_read_b32 v237, a67\n" \
"v_mfma_f32_16x16x32_bf16 v[164:167], v[12:15], v[220:223], v[164:167]\n" \
"s_waitcnt vmcnt(18) lgkmcnt(0)\n" \
"v_mul_f32 v234, s92, v234\n" \
"v_mul_f32 v235, s92, v235\n" \
"v_mfma_f32_16x16x32_bf16 v[160:163], v[0:3], v[216:219], v[160:163]\n" \
"v_mul_f32 v236, s92, v236\n" \
"v_mul_f32 v237, s92, v237\n" \
"v_mfma_f32_16x16x32_bf16 v[160:163], v[4:7], v[220:223], v[160:163]\n" \
"s_barrier\n" \
"v_mfma_f32_16x16x32_bf16 v[80:83], v[32:35], v[192:195], v[80:83]\n" \
"ds_read_b64_tr_b16 v[0:1], v232 offset:0\n" \
"v_cvt_pk_bf16_f32 v234, v234, v235\n" \
"v_mfma_f32_16x16x32_bf16 v[80:83], v[36:39], v[196:199], v[80:83]\n" \
"ds_read_b64_tr_b16 v[2:3], v232 offset:2048\n" \
"v_cvt_pk_bf16_f32 v236, v236, v237\n" \
"v_mfma_f32_16x16x32_bf16 v[84:87], v[40:43], v[192:195], v[84:87]\n" \
"s_add_u32 m0, s87, 135168\n" \
"ds_read_b64_tr_b16 v[4:5], v232 offset:8192\n" \
"v_accvgpr_write_b32 a64, v234\n" \
"v_mfma_f32_16x16x32_bf16 v[84:87], v[44:47], v[196:199], v[84:87]\n" \
"global_load_lds_dwordx4 v224, s[48:49] offset:-4096\n" \
"v_accvgpr_write_b32 a65, v236\n" \
"v_mfma_f32_16x16x32_bf16 v[88:91], v[48:51], v[192:195], v[88:91]\n" \
"ds_read_b64_tr_b16 v[6:7], v232 offset:10240\n" \
"v_accvgpr_read_b32 v234, a68\n" \
"v_mfma_f32_16x16x32_bf16 v[88:91], v[52:55], v[196:199], v[88:91]\n" \
"ds_read_b64_tr_b16 v[8:9], v233 offset:0\n" \
"v_accvgpr_read_b32 v235, a69\n" \
"v_mfma_f32_16x16x32_bf16 v[92:95], v[56:59], v[192:195], v[92:95]\n" \
"ds_read_b64_tr_b16 v[10:11], v233 offset:2048\n" \
"v_accvgpr_read_b32 v236, a70\n" \
"v_mfma_f32_16x16x32_bf16 v[92:95], v[60:63], v[196:199], v[92:95]\n" \
"global_load_lds_dwordx4 v225, s[48:49] offset:-3072\n" \
"v_accvgpr_read_b32 v237, a71\n" \
"v_mfma_f32_16x16x32_bf16 v[124:127], v[56:59], v[200:203], v[124:127]\n" \
"ds_read_b128 v[192:195], v230 offset:0\n" \
"v_mul_f32 v234, s92, v234\n" \
"v_mfma_f32_16x16x32_bf16 v[124:127], v[60:63], v[204:207], v[124:127]\n" \
"ds_read_b128 v[196:199], v231 offset:0\n" \
"v_mul_f32 v235, s92, v235\n" \
"v_mfma_f32_16x16x32_bf16 v[120:123], v[48:51], v[200:203], v[120:123]\n" \
"ds_read_b64_tr_b16 v[12:13], v233 offset:8192\n" \
"v_mul_f32 v236, s92, v236\n" \
"v_mfma_f32_16x16x32_bf16 v[120:123], v[52:55], v[204:207], v[120:123]\n" \
"global_load_lds_dwordx4 v226, s[48:49] offset:-2048\n" \
"v_mul_f32 v237, s92, v237\n" \
"v_mfma_f32_16x16x32_bf16 v[116:119], v[40:43], v[200:203], v[116:119]\n" \
"ds_read_b64_tr_b16 v[14:15], v233 offset:10240\n" \
"v_cvt_pk_bf16_f32 v234, v234, v235\n" \
"v_mfma_f32_16x16x32_bf16 v[116:119], v[44:47], v[204:207], v[116:119]\n" \
"ds_read_b64_tr_b16 v[16:17], v232 offset:512\n" \
"v_cvt_pk_bf16_f32 v236, v236, v237\n" \
"v_mfma_f32_16x16x32_bf16 v[112:115], v[32:35], v[200:203], v[112:115]\n" \
"ds_read_b64_tr_b16 v[18:19], v232 offset:2560\n" \
"v_accvgpr_write_b32 a66, v234\n" \
"v_mfma_f32_16x16x32_bf16 v[112:115], v[36:39], v[204:207], v[112:115]\n" \
"global_load_lds_dwordx4 v227, s[48:49] offset:-1024\n" \
"v_accvgpr_write_b32 a67, v236\n" \
"v_mfma_f32_16x16x32_bf16 v[144:147], v[32:35], v[208:211], v[144:147]\n" \
"s_add_u32 s48, s48, 0x80\n" \
"s_addc_u32 s49, s49, 0\n" \
"ds_read_b128 v[200:203], v230 offset:2048\n" \
"v_mfma_f32_16x16x32_bf16 v[144:147], v[36:39], v[212:215], v[144:147]\n" \
"ds_read_b128 v[204:207], v231 offset:2048\n" \
"v_accvgpr_read_b32 v234, a96\n" \
"v_mfma_f32_16x16x32_bf16 v[148:151], v[40:43], v[208:211], v[148:151]\n" \
"ds_read_b64_tr_b16 v[20:21], v232 offset:8704\n" \
"v_accvgpr_read_b32 v235, a97\n" \
"v_mfma_f32_16x16x32_bf16 v[148:151], v[44:47], v[212:215], v[148:151]\n" \
"global_load_lds_dwordx4 v224, s[46:47] offset:0\n" \
"v_accvgpr_read_b32 v236, a98\n" \
"v_mfma_f32_16x16x32_bf16 v[152:155], v[48:51], v[208:211], v[152:155]\n" \
"ds_read_b64_tr_b16 v[22:23], v232 offset:10752\n" \
"v_accvgpr_read_b32 v237, a99\n" \
"v_mfma_f32_16x16x32_bf16 v[152:155], v[52:55], v[212:215], v[152:155]\n" \
"ds_read_b64_tr_b16 v[24:25], v233 offset:512\n" \
"v_mul_f32 v234, s92, v234\n" \
"v_mfma_f32_16x16x32_bf16 v[156:159], v[56:59], v[208:211], v[156:159]\n" \
"ds_read_b64_tr_b16 v[26:27], v233 offset:2560\n" \
"v_mul_f32 v235, s92, v235\n" \
"v_mfma_f32_16x16x32_bf16 v[156:159], v[60:63], v[212:215], v[156:159]\n" \
"global_load_lds_dwordx4 v225, s[46:47] offset:1024\n" \
"v_mul_f32 v236, s92, v236\n" \
"v_mfma_f32_16x16x32_bf16 v[188:191], v[56:59], v[216:219], v[188:191]\n" \
"ds_read_b128 v[208:211], v230 offset:4096\n" \
"v_mul_f32 v237, s92, v237\n" \
"v_mfma_f32_16x16x32_bf16 v[188:191], v[60:63], v[220:223], v[188:191]\n" \
"ds_read_b128 v[212:215], v231 offset:4096\n" \
"v_cvt_pk_bf16_f32 v234, v234, v235\n" \
"v_mfma_f32_16x16x32_bf16 v[184:187], v[48:51], v[216:219], v[184:187]\n" \
"ds_read_b64_tr_b16 v[28:29], v233 offset:8704\n" \
"v_cvt_pk_bf16_f32 v236, v236, v237\n" \
"v_mfma_f32_16x16x32_bf16 v[184:187], v[52:55], v[220:223], v[184:187]\n" \
"global_load_lds_dwordx4 v226, s[46:47] offset:2048\n" \
"v_accvgpr_write_b32 a96, v234\n" \
"v_mfma_f32_16x16x32_bf16 v[180:183], v[40:43], v[216:219], v[180:183]\n" \
"ds_read_b64_tr_b16 v[30:31], v233 offset:10752\n" \
"v_accvgpr_write_b32 a97, v236\n" \
"v_mfma_f32_16x16x32_bf16 v[180:183], v[44:47], v[220:223], v[180:183]\n" \
"v_accvgpr_read_b32 v234, a100\n" \
"v_accvgpr_read_b32 v235, a101\n" \
"v_mfma_f32_16x16x32_bf16 v[176:179], v[32:35], v[216:219], v[176:179]\n" \
"v_accvgpr_read_b32 v236, a102\n" \
"v_accvgpr_read_b32 v237, a103\n" \
"v_mfma_f32_16x16x32_bf16 v[176:179], v[36:39], v[220:223], v[176:179]\n" \
"s_waitcnt lgkmcnt(1)\n" \
"global_load_lds_dwordx4 v227, s[46:47] offset:3072\n" \
"v_mul_f32 v234, s92, v234\n" \
"v_mfma_f32_16x16x32_bf16 v[64:67], v[0:3], v[192:195], v[64:67]\n" \
"s_add_u32 s46, s46, 0x80\n" \
"s_addc_u32 s47, s47, 0\n" \
"ds_read_b128 v[216:219], v230 offset:6144\n" \
"v_mfma_f32_16x16x32_bf16 v[64:67], v[4:7], v[196:199], v[64:67]\n" \
"ds_read_b128 v[220:223], v231 offset:6144\n" \
"v_mul_f32 v235, s92, v235\n" \
"v_mfma_f32_16x16x32_bf16 v[68:71], v[8:11], v[192:195], v[68:71]\n" \
"ds_read_b64_tr_b16 v[32:33], v232 offset:1024\n" \
"v_mul_f32 v236, s92, v236\n" \
"v_mfma_f32_16x16x32_bf16 v[68:71], v[12:15], v[196:199], v[68:71]\n" \
"buffer_store_dwordx4 a[212:215], v248, s[56:59], s90 offen offset:1024 sc1\n" \
"v_mul_f32 v237, s92, v237\n" \
"v_cvt_pk_bf16_f32 v234, v234, v235\n" \
"v_cvt_pk_bf16_f32 v236, v236, v237\n" \
"v_accvgpr_write_b32 a98, v234\n" \
"v_mfma_f32_16x16x32_bf16 v[72:75], v[16:19], v[192:195], v[72:75]\n" \
"ds_read_b64_tr_b16 v[34:35], v232 offset:3072\n" \
"v_accvgpr_write_b32 a99, v236\n" \
"v_mfma_f32_16x16x32_bf16 v[72:75], v[20:23], v[196:199], v[72:75]\n" \
"s_waitcnt lgkmcnt(3)\n" \
"ds_read_b64_tr_b16 v[36:37], v232 offset:9216\n" \
"v_accvgpr_read_b32 v234, a72\n" \
"v_mfma_f32_16x16x32_bf16 v[76:79], v[24:27], v[192:195], v[76:79]\n" \
"ds_read_b64_tr_b16 v[38:39], v232 offset:11264\n" \
"v_accvgpr_read_b32 v235, a73\n" \
"v_mfma_f32_16x16x32_bf16 v[76:79], v[28:31], v[196:199], v[76:79]\n" \
"buffer_store_dwordx4 a[216:219], v248, s[56:59], s90 offen offset:2048 sc1\n" \
"v_accvgpr_read_b32 v236, a74\n" \
"v_accvgpr_read_b32 v237, a75\n" \
"v_mul_f32 v234, s92, v234\n" \
"v_mul_f32 v235, s92, v235\n" \
"v_mfma_f32_16x16x32_bf16 v[108:111], v[24:27], v[200:203], v[108:111]\n" \
"ds_read_b64_tr_b16 v[40:41], v233 offset:1024\n" \
"v_mul_f32 v236, s92, v236\n" \
"v_mfma_f32_16x16x32_bf16 v[108:111], v[28:31], v[204:207], v[108:111]\n" \
"ds_read_b64_tr_b16 v[42:43], v233 offset:3072\n" \
"v_mul_f32 v237, s92, v237\n" \
"v_mfma_f32_16x16x32_bf16 v[104:107], v[16:19], v[200:203], v[104:107]\n" \
"ds_read_b64_tr_b16 v[44:45], v233 offset:9216\n" \
"v_cvt_pk_bf16_f32 v234, v234, v235\n" \
"v_mfma_f32_16x16x32_bf16 v[104:107], v[20:23], v[204:207], v[104:107]\n" \
"v_cvt_pk_bf16_f32 v236, v236, v237\n" \
"v_accvgpr_write_b32 a72, v234\n" \
"v_mfma_f32_16x16x32_bf16 v[100:103], v[8:11], v[200:203], v[100:103]\n" \
"ds_read_b64_tr_b16 v[46:47], v233 offset:11264\n" \
"v_accvgpr_write_b32 a73, v236\n" \
"v_mfma_f32_16x16x32_bf16 v[100:103], v[12:15], v[204:207], v[100:103]\n" \
"ds_read_b64_tr_b16 v[48:49], v232 offset:1536\n" \
"v_accvgpr_read_b32 v234, a76\n" \
"v_mfma_f32_16x16x32_bf16 v[96:99], v[0:3], v[200:203], v[96:99]\n" \
"s_add_u32 m0, s95, 53248\n" \
"ds_read_b64_tr_b16 v[50:51], v232 offset:3584\n" \
"v_accvgpr_read_b32 v235, a77\n" \
"v_mfma_f32_16x16x32_bf16 v[96:99], v[4:7], v[204:207], v[96:99]\n" \
"global_load_lds_dwordx4 v228, s[44:45] offset:-4096\n" \
"v_accvgpr_read_b32 v236, a78\n" \
"v_mfma_f32_16x16x32_bf16 v[128:131], v[0:3], v[208:211], v[128:131]\n" \
"ds_read_b64_tr_b16 v[52:53], v232 offset:9728\n" \
"v_accvgpr_read_b32 v237, a79\n" \
"v_mfma_f32_16x16x32_bf16 v[128:131], v[4:7], v[212:215], v[128:131]\n" \
"ds_read_b64_tr_b16 v[54:55], v232 offset:11776\n" \
"v_mul_f32 v234, s92, v234\n" \
"v_mfma_f32_16x16x32_bf16 v[132:135], v[8:11], v[208:211], v[132:135]\n" \
"ds_read_b64_tr_b16 v[56:57], v233 offset:1536\n" \
"v_mul_f32 v235, s92, v235\n" \
"v_mfma_f32_16x16x32_bf16 v[132:135], v[12:15], v[212:215], v[132:135]\n" \
"global_load_lds_dwordx4 v229, s[44:45] offset:-3072\n" \
"v_mul_f32 v236, s92, v236\n" \
"v_mfma_f32_16x16x32_bf16 v[136:139], v[16:19], v[208:211], v[136:139]\n" \
"s_add_u32 s44, s44, 0x80000\n" \
"s_addc_u32 s45, s45, 0\n" \
"ds_read_b64_tr_b16 v[58:59], v233 offset:3584\n" \
"v_mfma_f32_16x16x32_bf16 v[136:139], v[20:23], v[212:215], v[136:139]\n" \
"ds_read_b64_tr_b16 v[60:61], v233 offset:9728\n" \
"v_mul_f32 v237, s92, v237\n" \
"v_mfma_f32_16x16x32_bf16 v[140:143], v[24:27], v[208:211], v[140:143]\n" \
"ds_read_b64_tr_b16 v[62:63], v233 offset:11776\n" \
"v_cvt_pk_bf16_f32 v234, v234, v235\n" \
"v_mfma_f32_16x16x32_bf16 v[140:143], v[28:31], v[212:215], v[140:143]\n" \
"s_waitcnt lgkmcnt(3)\n" \
"global_load_lds_dwordx4 v228, s[42:43] offset:-2048\n" \
"v_cvt_pk_bf16_f32 v236, v236, v237\n" \
"v_mfma_f32_16x16x32_bf16 v[172:175], v[24:27], v[216:219], v[172:175]\n" \
"v_add_u32 v230, 0x8000, v230\n" \
"v_add_u32 v231, 0x8000, v231\n" \
"v_mfma_f32_16x16x32_bf16 v[172:175], v[28:31], v[220:223], v[172:175]\n" \
"v_add_u32 v232, 0x4000, v232\n" \
"v_add_u32 v233, 0x4000, v233\n" \
"v_mfma_f32_16x16x32_bf16 v[168:171], v[16:19], v[216:219], v[168:171]\n" \
"v_accvgpr_write_b32 a74, v234\n" \
"v_accvgpr_write_b32 a75, v236\n" \
"v_mfma_f32_16x16x32_bf16 v[168:171], v[20:23], v[220:223], v[168:171]\n" \
"global_load_lds_dwordx4 v229, s[42:43] offset:-1024\n" \
"v_accvgpr_read_b32 v234, a104\n" \
"v_mfma_f32_16x16x32_bf16 v[164:167], v[8:11], v[216:219], v[164:167]\n" \
"s_add_u32 s42, s42, 0x80000\n" \
"s_addc_u32 s43, s43, 0\n" \
"v_accvgpr_read_b32 v235, a105\n" \
"v_mfma_f32_16x16x32_bf16 v[164:167], v[12:15], v[220:223], v[164:167]\n" \
"s_waitcnt vmcnt(19) lgkmcnt(0)\n" \
"v_accvgpr_read_b32 v236, a106\n" \
"v_accvgpr_read_b32 v237, a107\n" \
"v_mfma_f32_16x16x32_bf16 v[160:163], v[0:3], v[216:219], v[160:163]\n" \
"v_mul_f32 v234, s92, v234\n" \
"v_mul_f32 v235, s92, v235\n" \
"v_mfma_f32_16x16x32_bf16 v[160:163], v[4:7], v[220:223], v[160:163]\n" \
"s_barrier\n" \
"v_mfma_f32_16x16x32_bf16 v[80:83], v[32:35], v[192:195], v[80:83]\n" \
"ds_read_b64_tr_b16 v[0:1], v232 offset:0\n" \
"v_mul_f32 v236, s92, v236\n" \
"v_mfma_f32_16x16x32_bf16 v[80:83], v[36:39], v[196:199], v[80:83]\n" \
"ds_read_b64_tr_b16 v[2:3], v232 offset:2048\n" \
"v_mul_f32 v237, s92, v237\n" \
"v_mfma_f32_16x16x32_bf16 v[84:87], v[40:43], v[192:195], v[84:87]\n" \
"s_add_u32 m0, s87, 69632\n" \
"ds_read_b64_tr_b16 v[4:5], v232 offset:8192\n" \
"v_cvt_pk_bf16_f32 v234, v234, v235\n" \
"v_mfma_f32_16x16x32_bf16 v[84:87], v[44:47], v[196:199], v[84:87]\n" \
"global_load_lds_dwordx4 v224, s[48:49] offset:-4096\n" \
"v_cvt_pk_bf16_f32 v236, v236, v237\n" \
"v_mfma_f32_16x16x32_bf16 v[88:91], v[48:51], v[192:195], v[88:91]\n" \
"ds_read_b64_tr_b16 v[6:7], v232 offset:10240\n" \
"v_accvgpr_write_b32 a104, v234\n" \
"v_mfma_f32_16x16x32_bf16 v[88:91], v[52:55], v[196:199], v[88:91]\n" \
"ds_read_b64_tr_b16 v[8:9], v233 offset:0\n" \
"v_accvgpr_write_b32 a105, v236\n" \
"v_mfma_f32_16x16x32_bf16 v[92:95], v[56:59], v[192:195], v[92:95]\n" \
"ds_read_b64_tr_b16 v[10:11], v233 offset:2048\n" \
"v_accvgpr_read_b32 v234, a108\n" \
"v_mfma_f32_16x16x32_bf16 v[92:95], v[60:63], v[196:199], v[92:95]\n" \
"global_load_lds_dwordx4 v225, s[48:49] offset:-3072\n" \
"v_accvgpr_read_b32 v235, a109\n" \
"v_mfma_f32_16x16x32_bf16 v[124:127], v[56:59], v[200:203], v[124:127]\n" \
"ds_read_b128 v[192:195], v230 offset:0\n" \
"v_accvgpr_read_b32 v236, a110\n" \
"v_mfma_f32_16x16x32_bf16 v[124:127], v[60:63], v[204:207], v[124:127]\n" \
"ds_read_b128 v[196:199], v231 offset:0\n" \
"v_accvgpr_read_b32 v237, a111\n" \
"v_mfma_f32_16x16x32_bf16 v[120:123], v[48:51], v[200:203], v[120:123]\n" \
"ds_read_b64_tr_b16 v[12:13], v233 offset:8192\n" \
"v_mul_f32 v234, s92, v234\n" \
"v_mfma_f32_16x16x32_bf16 v[120:123], v[52:55], v[204:207], v[120:123]\n" \
"global_load_lds_dwordx4 v226, s[48:49] offset:-2048\n" \
"v_mul_f32 v235, s92, v235\n" \
"v_mfma_f32_16x16x32_bf16 v[116:119], v[40:43], v[200:203], v[116:119]\n" \
"ds_read_b64_tr_b16 v[14:15], v233 offset:10240\n" \
"v_mul_f32 v236, s92, v236\n" \
"v_mfma_f32_16x16x32_bf16 v[116:119], v[44:47], v[204:207], v[116:119]\n" \
"ds_read_b64_tr_b16 v[16:17], v232 offset:512\n" \
"v_mul_f32 v237, s92, v237\n" \
"v_mfma_f32_16x16x32_bf16 v[112:115], v[32:35], v[200:203], v[112:115]\n" \
"ds_read_b64_tr_b16 v[18:19], v232 offset:2560\n" \
"v_cvt_pk_bf16_f32 v234, v234, v235\n" \
"v_mfma_f32_16x16x32_bf16 v[112:115], v[36:39], v[204:207], v[112:115]\n" \
"global_load_lds_dwordx4 v227, s[48:49] offset:-1024\n" \
"v_cvt_pk_bf16_f32 v236, v236, v237\n" \
"v_mfma_f32_16x16x32_bf16 v[144:147], v[32:35], v[208:211], v[144:147]\n" \
"s_add_u32 s48, s48, 0x80\n" \
"s_addc_u32 s49, s49, 0\n" \
"ds_read_b128 v[200:203], v230 offset:2048\n" \
"v_mfma_f32_16x16x32_bf16 v[144:147], v[36:39], v[212:215], v[144:147]\n" \
"ds_read_b128 v[204:207], v231 offset:2048\n" \
"v_accvgpr_write_b32 a106, v234\n" \
"v_mfma_f32_16x16x32_bf16 v[148:151], v[40:43], v[208:211], v[148:151]\n" \
"ds_read_b64_tr_b16 v[20:21], v232 offset:8704\n" \
"v_accvgpr_write_b32 a107, v236\n" \
"v_mfma_f32_16x16x32_bf16 v[148:151], v[44:47], v[212:215], v[148:151]\n" \
"global_load_lds_dwordx4 v224, s[46:47] offset:0\n" \
"v_accvgpr_read_b32 v234, a80\n" \
"v_mfma_f32_16x16x32_bf16 v[152:155], v[48:51], v[208:211], v[152:155]\n" \
"ds_read_b64_tr_b16 v[22:23], v232 offset:10752\n" \
"v_accvgpr_read_b32 v235, a81\n" \
"v_mfma_f32_16x16x32_bf16 v[152:155], v[52:55], v[212:215], v[152:155]\n" \
"ds_read_b64_tr_b16 v[24:25], v233 offset:512\n" \
"v_accvgpr_read_b32 v236, a82\n" \
"v_mfma_f32_16x16x32_bf16 v[156:159], v[56:59], v[208:211], v[156:159]\n" \
"ds_read_b64_tr_b16 v[26:27], v233 offset:2560\n" \
"v_accvgpr_read_b32 v237, a83\n" \
"v_mfma_f32_16x16x32_bf16 v[156:159], v[60:63], v[212:215], v[156:159]\n" \
"global_load_lds_dwordx4 v225, s[46:47] offset:1024\n" \
"v_cvt_pk_bf16_f32 v234, v234, v235\n" \
"v_mfma_f32_16x16x32_bf16 v[188:191], v[56:59], v[216:219], v[188:191]\n" \
"ds_read_b128 v[208:211], v230 offset:4096\n" \
"v_cvt_pk_bf16_f32 v236, v236, v237\n" \
"v_mfma_f32_16x16x32_bf16 v[188:191], v[60:63], v[220:223], v[188:191]\n" \
"ds_read_b128 v[212:215], v231 offset:4096\n" \
"v_accvgpr_write_b32 a80, v234\n" \
"v_mfma_f32_16x16x32_bf16 v[184:187], v[48:51], v[216:219], v[184:187]\n" \
"ds_read_b64_tr_b16 v[28:29], v233 offset:8704\n" \
"v_accvgpr_write_b32 a81, v236\n" \
"v_mfma_f32_16x16x32_bf16 v[184:187], v[52:55], v[220:223], v[184:187]\n" \
"global_load_lds_dwordx4 v226, s[46:47] offset:2048\n" \
"v_accvgpr_read_b32 v234, a112\n" \
"v_mfma_f32_16x16x32_bf16 v[180:183], v[40:43], v[216:219], v[180:183]\n" \
"ds_read_b64_tr_b16 v[30:31], v233 offset:10752\n" \
"v_accvgpr_read_b32 v235, a113\n" \
"v_mfma_f32_16x16x32_bf16 v[180:183], v[44:47], v[220:223], v[180:183]\n" \
"v_accvgpr_read_b32 v236, a114\n" \
"v_accvgpr_read_b32 v237, a115\n" \
"v_mfma_f32_16x16x32_bf16 v[176:179], v[32:35], v[216:219], v[176:179]\n" \
"v_cvt_pk_bf16_f32 v234, v234, v235\n" \
"v_cvt_pk_bf16_f32 v236, v236, v237\n" \
"v_mfma_f32_16x16x32_bf16 v[176:179], v[36:39], v[220:223], v[176:179]\n" \
"s_waitcnt lgkmcnt(1)\n" \
"global_load_lds_dwordx4 v227, s[46:47] offset:3072\n" \
"v_accvgpr_write_b32 a82, v234\n" \
"v_mfma_f32_16x16x32_bf16 v[64:67], v[0:3], v[192:195], v[64:67]\n" \
"s_add_u32 s46, s46, 0x80\n" \
"s_addc_u32 s47, s47, 0\n" \
"ds_read_b128 v[216:219], v230 offset:6144\n" \
"v_mfma_f32_16x16x32_bf16 v[64:67], v[4:7], v[196:199], v[64:67]\n" \
"ds_read_b128 v[220:223], v231 offset:6144\n" \
"v_accvgpr_write_b32 a83, v236\n" \
"v_mfma_f32_16x16x32_bf16 v[68:71], v[8:11], v[192:195], v[68:71]\n" \
"ds_read_b64_tr_b16 v[32:33], v232 offset:1024\n" \
"v_accvgpr_read_b32 v234, a84\n" \
"v_mfma_f32_16x16x32_bf16 v[68:71], v[12:15], v[196:199], v[68:71]\n" \
"buffer_store_dwordx4 a[220:223], v248, s[56:59], s90 offen offset:3072 sc1\n" \
"v_accvgpr_read_b32 v235, a85\n" \
"v_accvgpr_read_b32 v236, a86\n" \
"v_accvgpr_read_b32 v237, a87\n" \
"v_cvt_pk_bf16_f32 v234, v234, v235\n" \
"v_mfma_f32_16x16x32_bf16 v[72:75], v[16:19], v[192:195], v[72:75]\n" \
"ds_read_b64_tr_b16 v[34:35], v232 offset:3072\n" \
"v_cvt_pk_bf16_f32 v236, v236, v237\n" \
"v_mfma_f32_16x16x32_bf16 v[72:75], v[20:23], v[196:199], v[72:75]\n" \
"s_waitcnt lgkmcnt(3)\n" \
"ds_read_b64_tr_b16 v[36:37], v232 offset:9216\n" \
"v_accvgpr_write_b32 a84, v234\n" \
"v_mfma_f32_16x16x32_bf16 v[76:79], v[24:27], v[192:195], v[76:79]\n" \
"ds_read_b64_tr_b16 v[38:39], v232 offset:11264\n" \
"v_accvgpr_write_b32 a85, v236\n" \
"v_mfma_f32_16x16x32_bf16 v[76:79], v[28:31], v[196:199], v[76:79]\n" \
"v_accvgpr_read_b32 v234, a116\n" \
"v_accvgpr_read_b32 v235, a117\n" \
"v_mfma_f32_16x16x32_bf16 v[108:111], v[24:27], v[200:203], v[108:111]\n" \
"ds_read_b64_tr_b16 v[40:41], v233 offset:1024\n" \
"v_accvgpr_read_b32 v236, a118\n" \
"v_mfma_f32_16x16x32_bf16 v[108:111], v[28:31], v[204:207], v[108:111]\n" \
"ds_read_b64_tr_b16 v[42:43], v233 offset:3072\n" \
"v_accvgpr_read_b32 v237, a119\n" \
"v_mfma_f32_16x16x32_bf16 v[104:107], v[16:19], v[200:203], v[104:107]\n" \
"ds_read_b64_tr_b16 v[44:45], v233 offset:9216\n" \
"v_cvt_pk_bf16_f32 v234, v234, v235\n" \
"v_mfma_f32_16x16x32_bf16 v[104:107], v[20:23], v[204:207], v[104:107]\n" \
"v_cvt_pk_bf16_f32 v236, v236, v237\n" \
"v_accvgpr_write_b32 a86, v234\n" \
"v_mfma_f32_16x16x32_bf16 v[100:103], v[8:11], v[200:203], v[100:103]\n" \
"ds_read_b64_tr_b16 v[46:47], v233 offset:11264\n" \
"v_accvgpr_write_b32 a87, v236\n" \
"v_mfma_f32_16x16x32_bf16 v[100:103], v[12:15], v[204:207], v[100:103]\n" \
"ds_read_b64_tr_b16 v[48:49], v232 offset:1536\n" \
"v_accvgpr_read_b32 v234, a88\n" \
"v_mfma_f32_16x16x32_bf16 v[96:99], v[0:3], v[200:203], v[96:99]\n" \
"s_add_u32 m0, s95, 4096\n" \
"ds_read_b64_tr_b16 v[50:51], v232 offset:3584\n" \
"v_accvgpr_read_b32 v235, a89\n" \
"v_mfma_f32_16x16x32_bf16 v[96:99], v[4:7], v[204:207], v[96:99]\n" \
"global_load_lds_dwordx4 v228, s[44:45] offset:-4096\n" \
"v_accvgpr_read_b32 v236, a90\n" \
"v_mfma_f32_16x16x32_bf16 v[128:131], v[0:3], v[208:211], v[128:131]\n" \
"ds_read_b64_tr_b16 v[52:53], v232 offset:9728\n" \
"v_accvgpr_read_b32 v237, a91\n" \
"v_mfma_f32_16x16x32_bf16 v[128:131], v[4:7], v[212:215], v[128:131]\n" \
"ds_read_b64_tr_b16 v[54:55], v232 offset:11776\n" \
"v_cvt_pk_bf16_f32 v234, v234, v235\n" \
"v_mfma_f32_16x16x32_bf16 v[132:135], v[8:11], v[208:211], v[132:135]\n" \
"ds_read_b64_tr_b16 v[56:57], v233 offset:1536\n" \
"v_cvt_pk_bf16_f32 v236, v236, v237\n" \
"v_mfma_f32_16x16x32_bf16 v[132:135], v[12:15], v[212:215], v[132:135]\n" \
"global_load_lds_dwordx4 v229, s[44:45] offset:-3072\n" \
"v_accvgpr_write_b32 a88, v234\n" \
"v_mfma_f32_16x16x32_bf16 v[136:139], v[16:19], v[208:211], v[136:139]\n" \
"s_add_u32 s44, s44, 0x80000\n" \
"s_addc_u32 s45, s45, 0\n" \
"ds_read_b64_tr_b16 v[58:59], v233 offset:3584\n" \
"v_mfma_f32_16x16x32_bf16 v[136:139], v[20:23], v[212:215], v[136:139]\n" \
"ds_read_b64_tr_b16 v[60:61], v233 offset:9728\n" \
"v_accvgpr_write_b32 a89, v236\n" \
"v_mfma_f32_16x16x32_bf16 v[140:143], v[24:27], v[208:211], v[140:143]\n" \
"ds_read_b64_tr_b16 v[62:63], v233 offset:11776\n" \
"v_accvgpr_read_b32 v234, a120\n" \
"v_mfma_f32_16x16x32_bf16 v[140:143], v[28:31], v[212:215], v[140:143]\n" \
"s_waitcnt lgkmcnt(3)\n" \
"global_load_lds_dwordx4 v228, s[42:43] offset:-2048\n" \
"v_accvgpr_read_b32 v235, a121\n" \
"v_mfma_f32_16x16x32_bf16 v[172:175], v[24:27], v[216:219], v[172:175]\n" \
"v_add_u32 v230, 0x8000, v230\n" \
"v_add_u32 v231, 0x8000, v231\n" \
"v_mfma_f32_16x16x32_bf16 v[172:175], v[28:31], v[220:223], v[172:175]\n" \
"v_add_u32 v232, 0x4000, v232\n" \
"v_add_u32 v233, 0x4000, v233\n" \
"v_mfma_f32_16x16x32_bf16 v[168:171], v[16:19], v[216:219], v[168:171]\n" \
"v_accvgpr_read_b32 v236, a122\n" \
"v_accvgpr_read_b32 v237, a123\n" \
"v_mfma_f32_16x16x32_bf16 v[168:171], v[20:23], v[220:223], v[168:171]\n" \
"global_load_lds_dwordx4 v229, s[42:43] offset:-1024\n" \
"v_cvt_pk_bf16_f32 v234, v234, v235\n" \
"v_mfma_f32_16x16x32_bf16 v[164:167], v[8:11], v[216:219], v[164:167]\n" \
"s_add_u32 s42, s42, 0x80000\n" \
"s_addc_u32 s43, s43, 0\n" \
"v_cvt_pk_bf16_f32 v236, v236, v237\n" \
"v_mfma_f32_16x16x32_bf16 v[164:167], v[12:15], v[220:223], v[164:167]\n" \
"s_waitcnt vmcnt(19) lgkmcnt(0)\n" \
"v_accvgpr_write_b32 a90, v234\n" \
"v_accvgpr_write_b32 a91, v236\n" \
"v_mfma_f32_16x16x32_bf16 v[160:163], v[0:3], v[216:219], v[160:163]\n" \
"v_accvgpr_read_b32 v234, a92\n" \
"v_accvgpr_read_b32 v235, a93\n" \
"v_mfma_f32_16x16x32_bf16 v[160:163], v[4:7], v[220:223], v[160:163]\n" \
"s_barrier\n" \
"v_mfma_f32_16x16x32_bf16 v[80:83], v[32:35], v[192:195], v[80:83]\n" \
"ds_read_b64_tr_b16 v[0:1], v232 offset:0\n" \
"v_accvgpr_read_b32 v236, a94\n" \
"v_mfma_f32_16x16x32_bf16 v[80:83], v[36:39], v[196:199], v[80:83]\n" \
"ds_read_b64_tr_b16 v[2:3], v232 offset:2048\n" \
"v_accvgpr_read_b32 v237, a95\n" \
"v_mfma_f32_16x16x32_bf16 v[84:87], v[40:43], v[192:195], v[84:87]\n" \
"s_add_u32 m0, s87, 102400\n" \
"ds_read_b64_tr_b16 v[4:5], v232 offset:8192\n" \
"v_cvt_pk_bf16_f32 v234, v234, v235\n" \
"v_mfma_f32_16x16x32_bf16 v[84:87], v[44:47], v[196:199], v[84:87]\n" \
"global_load_lds_dwordx4 v224, s[48:49] offset:-4096\n" \
"v_cvt_pk_bf16_f32 v236, v236, v237\n" \
"v_mfma_f32_16x16x32_bf16 v[88:91], v[48:51], v[192:195], v[88:91]\n" \
"ds_read_b64_tr_b16 v[6:7], v232 offset:10240\n" \
"Lpg3309_%=:\n" \
"s_cbranch_execz Lpg5391_%=\n" \
"v_accvgpr_write_b32 a92, v234\n" \
"v_mfma_f32_16x16x32_bf16 v[88:91], v[52:55], v[196:199], v[88:91]\n" \
"ds_read_b64_tr_b16 v[8:9], v233 offset:0\n" \
"v_accvgpr_write_b32 a93, v236\n" \
"v_mfma_f32_16x16x32_bf16 v[92:95], v[56:59], v[192:195], v[92:95]\n" \
"ds_read_b64_tr_b16 v[10:11], v233 offset:2048\n" \
"v_accvgpr_read_b32 v234, a124\n" \
"v_mfma_f32_16x16x32_bf16 v[92:95], v[60:63], v[196:199], v[92:95]\n" \
"global_load_lds_dwordx4 v225, s[48:49] offset:-3072\n" \
"v_accvgpr_read_b32 v235, a125\n" \
"v_mfma_f32_16x16x32_bf16 v[124:127], v[56:59], v[200:203], v[124:127]\n" \
"ds_read_b128 v[192:195], v230 offset:0\n" \
"v_accvgpr_read_b32 v236, a126\n" \
"v_mfma_f32_16x16x32_bf16 v[124:127], v[60:63], v[204:207], v[124:127]\n" \
"ds_read_b128 v[196:199], v231 offset:0\n" \
"v_accvgpr_read_b32 v237, a127\n" \
"v_mfma_f32_16x16x32_bf16 v[120:123], v[48:51], v[200:203], v[120:123]\n" \
"ds_read_b64_tr_b16 v[12:13], v233 offset:8192\n" \
"v_cvt_pk_bf16_f32 v234, v234, v235\n" \
"v_mfma_f32_16x16x32_bf16 v[120:123], v[52:55], v[204:207], v[120:123]\n" \
"global_load_lds_dwordx4 v226, s[48:49] offset:-2048\n" \
"v_cvt_pk_bf16_f32 v236, v236, v237\n" \
"v_mfma_f32_16x16x32_bf16 v[116:119], v[40:43], v[200:203], v[116:119]\n" \
"ds_read_b64_tr_b16 v[14:15], v233 offset:10240\n" \
"v_accvgpr_write_b32 a94, v234\n" \
"v_mfma_f32_16x16x32_bf16 v[116:119], v[44:47], v[204:207], v[116:119]\n" \
"ds_read_b64_tr_b16 v[16:17], v232 offset:512\n" \
"v_accvgpr_write_b32 a95, v236\n" \
"v_mfma_f32_16x16x32_bf16 v[112:115], v[32:35], v[200:203], v[112:115]\n" \
"ds_read_b64_tr_b16 v[18:19], v232 offset:2560\n" \
"v_accvgpr_read_b32 v234, a0\n" \
"v_mfma_f32_16x16x32_bf16 v[112:115], v[36:39], v[204:207], v[112:115]\n" \
"global_load_lds_dwordx4 v227, s[48:49] offset:-1024\n" \
"v_accvgpr_read_b32 v235, a1\n" \
"v_mfma_f32_16x16x32_bf16 v[144:147], v[32:35], v[208:211], v[144:147]\n" \
"s_add_u32 s48, s48, 0x80\n" \
"s_addc_u32 s49, s49, 0\n" \
"ds_read_b128 v[200:203], v230 offset:2048\n" \
"v_mfma_f32_16x16x32_bf16 v[144:147], v[36:39], v[212:215], v[144:147]\n" \
"ds_read_b128 v[204:207], v231 offset:2048\n" \
"v_accvgpr_read_b32 v236, a2\n" \
"v_mfma_f32_16x16x32_bf16 v[148:151], v[40:43], v[208:211], v[148:151]\n" \
"ds_read_b64_tr_b16 v[20:21], v232 offset:8704\n" \
"v_accvgpr_read_b32 v237, a3\n" \
"v_mfma_f32_16x16x32_bf16 v[148:151], v[44:47], v[212:215], v[148:151]\n" \
"global_load_lds_dwordx4 v224, s[46:47] offset:0\n" \
"v_mul_f32 v234, s92, v234\n" \
"v_mfma_f32_16x16x32_bf16 v[152:155], v[48:51], v[208:211], v[152:155]\n" \
"ds_read_b64_tr_b16 v[22:23], v232 offset:10752\n" \
"v_mul_f32 v235, s92, v235\n" \
"v_mfma_f32_16x16x32_bf16 v[152:155], v[52:55], v[212:215], v[152:155]\n" \
"ds_read_b64_tr_b16 v[24:25], v233 offset:512\n" \
"v_mul_f32 v236, s92, v236\n" \
"v_mfma_f32_16x16x32_bf16 v[156:159], v[56:59], v[208:211], v[156:159]\n" \
"ds_read_b64_tr_b16 v[26:27], v233 offset:2560\n" \
"v_mul_f32 v237, s92, v237\n" \
"v_mfma_f32_16x16x32_bf16 v[156:159], v[60:63], v[212:215], v[156:159]\n" \
"global_load_lds_dwordx4 v225, s[46:47] offset:1024\n" \
"v_cvt_pk_bf16_f32 v234, v234, v235\n" \
"v_mfma_f32_16x16x32_bf16 v[188:191], v[56:59], v[216:219], v[188:191]\n" \
"ds_read_b128 v[208:211], v230 offset:4096\n" \
"v_cvt_pk_bf16_f32 v236, v236, v237\n" \
"v_mfma_f32_16x16x32_bf16 v[188:191], v[60:63], v[220:223], v[188:191]\n" \
"ds_read_b128 v[212:215], v231 offset:4096\n" \
"v_accvgpr_write_b32 a0, v234\n" \
"v_mfma_f32_16x16x32_bf16 v[184:187], v[48:51], v[216:219], v[184:187]\n" \
"ds_read_b64_tr_b16 v[28:29], v233 offset:8704\n" \
"v_accvgpr_write_b32 a1, v236\n" \
"v_mfma_f32_16x16x32_bf16 v[184:187], v[52:55], v[220:223], v[184:187]\n" \
"global_load_lds_dwordx4 v226, s[46:47] offset:2048\n" \
"v_accvgpr_read_b32 v234, a4\n" \
"v_mfma_f32_16x16x32_bf16 v[180:183], v[40:43], v[216:219], v[180:183]\n" \
"ds_read_b64_tr_b16 v[30:31], v233 offset:10752\n" \
"v_accvgpr_read_b32 v235, a5\n" \
"v_mfma_f32_16x16x32_bf16 v[180:183], v[44:47], v[220:223], v[180:183]\n" \
"v_accvgpr_read_b32 v236, a6\n" \
"v_accvgpr_read_b32 v237, a7\n" \
"v_mfma_f32_16x16x32_bf16 v[176:179], v[32:35], v[216:219], v[176:179]\n" \
"v_mul_f32 v234, s92, v234\n" \
"v_mul_f32 v235, s92, v235\n" \
"v_mfma_f32_16x16x32_bf16 v[176:179], v[36:39], v[220:223], v[176:179]\n" \
"s_waitcnt lgkmcnt(1)\n" \
"global_load_lds_dwordx4 v227, s[46:47] offset:3072\n" \
"v_mul_f32 v236, s92, v236\n" \
"v_mfma_f32_16x16x32_bf16 v[64:67], v[0:3], v[192:195], v[64:67]\n" \
"s_add_u32 s46, s46, 0x80\n" \
"s_addc_u32 s47, s47, 0\n" \
"ds_read_b128 v[216:219], v230 offset:6144\n" \
"v_mfma_f32_16x16x32_bf16 v[64:67], v[4:7], v[196:199], v[64:67]\n" \
"s_add_u32 s90, s88, 139264\n" \
"ds_read_b128 v[220:223], v231 offset:6144\n" \
"v_mul_f32 v237, s92, v237\n" \
"v_mfma_f32_16x16x32_bf16 v[68:71], v[8:11], v[192:195], v[68:71]\n" \
"ds_read_b64_tr_b16 v[32:33], v232 offset:1024\n" \
"v_cvt_pk_bf16_f32 v234, v234, v235\n" \
"v_mfma_f32_16x16x32_bf16 v[68:71], v[12:15], v[196:199], v[68:71]\n" \
"buffer_store_dwordx4 a[128:131], v249, s[56:59], s90 offen offset:0 sc1\n" \
"v_cvt_pk_bf16_f32 v236, v236, v237\n" \
"v_accvgpr_write_b32 a2, v234\n" \
"v_accvgpr_write_b32 a3, v236\n" \
"v_accvgpr_read_b32 v234, a32\n" \
"v_mfma_f32_16x16x32_bf16 v[72:75], v[16:19], v[192:195], v[72:75]\n" \
"ds_read_b64_tr_b16 v[34:35], v232 offset:3072\n" \
"v_accvgpr_read_b32 v235, a33\n" \
"v_mfma_f32_16x16x32_bf16 v[72:75], v[20:23], v[196:199], v[72:75]\n" \
"s_waitcnt lgkmcnt(3)\n" \
"ds_read_b64_tr_b16 v[36:37], v232 offset:9216\n" \
"v_accvgpr_read_b32 v236, a34\n" \
"v_mfma_f32_16x16x32_bf16 v[76:79], v[24:27], v[192:195], v[76:79]\n" \
"ds_read_b64_tr_b16 v[38:39], v232 offset:11264\n" \
"v_accvgpr_read_b32 v237, a35\n" \
"v_mfma_f32_16x16x32_bf16 v[76:79], v[28:31], v[196:199], v[76:79]\n" \
"v_mul_f32 v234, s92, v234\n" \
"v_mul_f32 v235, s92, v235\n" \
"v_mfma_f32_16x16x32_bf16 v[108:111], v[24:27], v[200:203], v[108:111]\n" \
"ds_read_b64_tr_b16 v[40:41], v233 offset:1024\n" \
"v_mul_f32 v236, s92, v236\n" \
"v_mfma_f32_16x16x32_bf16 v[108:111], v[28:31], v[204:207], v[108:111]\n" \
"ds_read_b64_tr_b16 v[42:43], v233 offset:3072\n" \
"v_mul_f32 v237, s92, v237\n" \
"v_mfma_f32_16x16x32_bf16 v[104:107], v[16:19], v[200:203], v[104:107]\n" \
"ds_read_b64_tr_b16 v[44:45], v233 offset:9216\n" \
"v_cvt_pk_bf16_f32 v234, v234, v235\n" \
"v_mfma_f32_16x16x32_bf16 v[104:107], v[20:23], v[204:207], v[104:107]\n" \
"v_cvt_pk_bf16_f32 v236, v236, v237\n" \
"v_accvgpr_write_b32 a32, v234\n" \
"v_mfma_f32_16x16x32_bf16 v[100:103], v[8:11], v[200:203], v[100:103]\n" \
"ds_read_b64_tr_b16 v[46:47], v233 offset:11264\n" \
"v_accvgpr_write_b32 a33, v236\n" \
"v_mfma_f32_16x16x32_bf16 v[100:103], v[12:15], v[204:207], v[100:103]\n" \
"ds_read_b64_tr_b16 v[48:49], v232 offset:1536\n" \
"v_accvgpr_read_b32 v234, a36\n" \
"v_mfma_f32_16x16x32_bf16 v[96:99], v[0:3], v[200:203], v[96:99]\n" \
"s_add_u32 m0, s95, 20480\n" \
"ds_read_b64_tr_b16 v[50:51], v232 offset:3584\n" \
"v_accvgpr_read_b32 v235, a37\n" \
"v_mfma_f32_16x16x32_bf16 v[96:99], v[4:7], v[204:207], v[96:99]\n" \
"global_load_lds_dwordx4 v228, s[44:45] offset:-4096\n" \
"v_accvgpr_read_b32 v236, a38\n" \
"v_mfma_f32_16x16x32_bf16 v[128:131], v[0:3], v[208:211], v[128:131]\n" \
"ds_read_b64_tr_b16 v[52:53], v232 offset:9728\n" \
"v_accvgpr_read_b32 v237, a39\n" \
"v_mfma_f32_16x16x32_bf16 v[128:131], v[4:7], v[212:215], v[128:131]\n" \
"ds_read_b64_tr_b16 v[54:55], v232 offset:11776\n" \
"v_mul_f32 v234, s92, v234\n" \
"v_mfma_f32_16x16x32_bf16 v[132:135], v[8:11], v[208:211], v[132:135]\n" \
"ds_read_b64_tr_b16 v[56:57], v233 offset:1536\n" \
"v_mul_f32 v235, s92, v235\n" \
"v_mfma_f32_16x16x32_bf16 v[132:135], v[12:15], v[212:215], v[132:135]\n" \
"global_load_lds_dwordx4 v229, s[44:45] offset:-3072\n" \
"v_mul_f32 v236, s92, v236\n" \
"v_mfma_f32_16x16x32_bf16 v[136:139], v[16:19], v[208:211], v[136:139]\n" \
"s_add_u32 s44, s44, 0x80000\n" \
"s_addc_u32 s45, s45, 0\n" \
"ds_read_b64_tr_b16 v[58:59], v233 offset:3584\n" \
"v_mfma_f32_16x16x32_bf16 v[136:139], v[20:23], v[212:215], v[136:139]\n" \
"ds_read_b64_tr_b16 v[60:61], v233 offset:9728\n" \
"v_mul_f32 v237, s92, v237\n" \
"v_mfma_f32_16x16x32_bf16 v[140:143], v[24:27], v[208:211], v[140:143]\n" \
"ds_read_b64_tr_b16 v[62:63], v233 offset:11776\n" \
"v_cvt_pk_bf16_f32 v234, v234, v235\n" \
"v_mfma_f32_16x16x32_bf16 v[140:143], v[28:31], v[212:215], v[140:143]\n" \
"s_waitcnt lgkmcnt(3)\n" \
"global_load_lds_dwordx4 v228, s[42:43] offset:-2048\n" \
"v_cvt_pk_bf16_f32 v236, v236, v237\n" \
"v_mfma_f32_16x16x32_bf16 v[172:175], v[24:27], v[216:219], v[172:175]\n" \
"v_add_u32 v230, 0xffff0000, v230\n" \
"v_add_u32 v231, 0xffff0000, v231\n" \
"v_mfma_f32_16x16x32_bf16 v[172:175], v[28:31], v[220:223], v[172:175]\n" \
"v_add_u32 v232, 0x4000, v232\n" \
"v_add_u32 v233, 0x4000, v233\n" \
"v_mfma_f32_16x16x32_bf16 v[168:171], v[16:19], v[216:219], v[168:171]\n" \
"v_accvgpr_write_b32 a34, v234\n" \
"v_accvgpr_write_b32 a35, v236\n" \
"v_mfma_f32_16x16x32_bf16 v[168:171], v[20:23], v[220:223], v[168:171]\n" \
"global_load_lds_dwordx4 v229, s[42:43] offset:-1024\n" \
"v_accvgpr_read_b32 v234, a8\n" \
"v_mfma_f32_16x16x32_bf16 v[164:167], v[8:11], v[216:219], v[164:167]\n" \
"s_add_u32 s42, s42, 0x80000\n" \
"s_addc_u32 s43, s43, 0\n" \
"v_accvgpr_read_b32 v235, a9\n" \
"v_mfma_f32_16x16x32_bf16 v[164:167], v[12:15], v[220:223], v[164:167]\n" \
"s_waitcnt vmcnt(18) lgkmcnt(0)\n" \
"v_accvgpr_read_b32 v236, a10\n" \
"v_accvgpr_read_b32 v237, a11\n" \
"v_mfma_f32_16x16x32_bf16 v[160:163], v[0:3], v[216:219], v[160:163]\n" \
"v_mul_f32 v234, s92, v234\n" \
"v_mul_f32 v235, s92, v235\n" \
"v_mfma_f32_16x16x32_bf16 v[160:163], v[4:7], v[220:223], v[160:163]\n" \
"s_barrier\n" \
"v_mfma_f32_16x16x32_bf16 v[80:83], v[32:35], v[192:195], v[80:83]\n" \
"ds_read_b64_tr_b16 v[0:1], v232 offset:0\n" \
"v_mul_f32 v236, s92, v236\n" \
"v_mfma_f32_16x16x32_bf16 v[80:83], v[36:39], v[196:199], v[80:83]\n" \
"ds_read_b64_tr_b16 v[2:3], v232 offset:2048\n" \
"v_mul_f32 v237, s92, v237\n" \
"v_mfma_f32_16x16x32_bf16 v[84:87], v[40:43], v[192:195], v[84:87]\n" \
"s_add_u32 m0, s87, 135168\n" \
"ds_read_b64_tr_b16 v[4:5], v232 offset:8192\n" \
"v_cvt_pk_bf16_f32 v234, v234, v235\n" \
"v_mfma_f32_16x16x32_bf16 v[84:87], v[44:47], v[196:199], v[84:87]\n" \
"global_load_lds_dwordx4 v224, s[48:49] offset:-4096\n" \
"v_cvt_pk_bf16_f32 v236, v236, v237\n" \
"v_mfma_f32_16x16x32_bf16 v[88:91], v[48:51], v[192:195], v[88:91]\n" \
"ds_read_b64_tr_b16 v[6:7], v232 offset:10240\n" \
"v_accvgpr_write_b32 a8, v234\n" \
"v_mfma_f32_16x16x32_bf16 v[88:91], v[52:55], v[196:199], v[88:91]\n" \
"ds_read_b64_tr_b16 v[8:9], v233 offset:0\n" \
"v_accvgpr_write_b32 a9, v236\n" \
"v_mfma_f32_16x16x32_bf16 v[92:95], v[56:59], v[192:195], v[92:95]\n" \
"ds_read_b64_tr_b16 v[10:11], v233 offset:2048\n" \
"v_accvgpr_read_b32 v234, a12\n" \
"v_mfma_f32_16x16x32_bf16 v[92:95], v[60:63], v[196:199], v[92:95]\n" \
"global_load_lds_dwordx4 v225, s[48:49] offset:-3072\n" \
"v_accvgpr_read_b32 v235, a13\n" \
"v_mfma_f32_16x16x32_bf16 v[124:127], v[56:59], v[200:203], v[124:127]\n" \
"ds_read_b128 v[192:195], v230 offset:0\n" \
"v_accvgpr_read_b32 v236, a14\n" \
"v_mfma_f32_16x16x32_bf16 v[124:127], v[60:63], v[204:207], v[124:127]\n" \
"ds_read_b128 v[196:199], v231 offset:0\n" \
"v_accvgpr_read_b32 v237, a15\n" \
"v_mfma_f32_16x16x32_bf16 v[120:123], v[48:51], v[200:203], v[120:123]\n" \
"ds_read_b64_tr_b16 v[12:13], v233 offset:8192\n" \
"v_mul_f32 v234, s92, v234\n" \
"v_mfma_f32_16x16x32_bf16 v[120:123], v[52:55], v[204:207], v[120:123]\n" \
"global_load_lds_dwordx4 v226, s[48:49] offset:-2048\n" \
"v_mul_f32 v235, s92, v235\n" \
"v_mfma_f32_16x16x32_bf16 v[116:119], v[40:43], v[200:203], v[116:119]\n" \
"ds_read_b64_tr_b16 v[14:15], v233 offset:10240\n" \
"v_mul_f32 v236, s92, v236\n" \
"v_mfma_f32_16x16x32_bf16 v[116:119], v[44:47], v[204:207], v[116:119]\n" \
"ds_read_b64_tr_b16 v[16:17], v232 offset:512\n" \
"v_mul_f32 v237, s92, v237\n" \
"v_mfma_f32_16x16x32_bf16 v[112:115], v[32:35], v[200:203], v[112:115]\n" \
"ds_read_b64_tr_b16 v[18:19], v232 offset:2560\n" \
"v_cvt_pk_bf16_f32 v234, v234, v235\n" \
"v_mfma_f32_16x16x32_bf16 v[112:115], v[36:39], v[204:207], v[112:115]\n" \
"global_load_lds_dwordx4 v227, s[48:49] offset:-1024\n" \
"v_cvt_pk_bf16_f32 v236, v236, v237\n" \
"v_mfma_f32_16x16x32_bf16 v[144:147], v[32:35], v[208:211], v[144:147]\n" \
"s_add_u32 s48, s48, 0x80\n" \
"s_addc_u32 s49, s49, 0\n" \
"ds_read_b128 v[200:203], v230 offset:2048\n" \
"v_mfma_f32_16x16x32_bf16 v[144:147], v[36:39], v[212:215], v[144:147]\n" \
"ds_read_b128 v[204:207], v231 offset:2048\n" \
"v_accvgpr_write_b32 a10, v234\n" \
"v_mfma_f32_16x16x32_bf16 v[148:151], v[40:43], v[208:211], v[148:151]\n" \
"ds_read_b64_tr_b16 v[20:21], v232 offset:8704\n" \
"v_accvgpr_write_b32 a11, v236\n" \
"v_mfma_f32_16x16x32_bf16 v[148:151], v[44:47], v[212:215], v[148:151]\n" \
"global_load_lds_dwordx4 v224, s[46:47] offset:0\n" \
"v_accvgpr_read_b32 v234, a40\n" \
"v_mfma_f32_16x16x32_bf16 v[152:155], v[48:51], v[208:211], v[152:155]\n" \
"ds_read_b64_tr_b16 v[22:23], v232 offset:10752\n" \
"v_accvgpr_read_b32 v235, a41\n" \
"v_mfma_f32_16x16x32_bf16 v[152:155], v[52:55], v[212:215], v[152:155]\n" \
"ds_read_b64_tr_b16 v[24:25], v233 offset:512\n" \
"v_accvgpr_read_b32 v236, a42\n" \
"v_mfma_f32_16x16x32_bf16 v[156:159], v[56:59], v[208:211], v[156:159]\n" \
"ds_read_b64_tr_b16 v[26:27], v233 offset:2560\n" \
"v_accvgpr_read_b32 v237, a43\n" \
"v_mfma_f32_16x16x32_bf16 v[156:159], v[60:63], v[212:215], v[156:159]\n" \
"global_load_lds_dwordx4 v225, s[46:47] offset:1024\n" \
"v_mul_f32 v234, s92, v234\n" \
"v_mfma_f32_16x16x32_bf16 v[188:191], v[56:59], v[216:219], v[188:191]\n" \
"ds_read_b128 v[208:211], v230 offset:4096\n" \
"v_mul_f32 v235, s92, v235\n" \
"v_mfma_f32_16x16x32_bf16 v[188:191], v[60:63], v[220:223], v[188:191]\n" \
"ds_read_b128 v[212:215], v231 offset:4096\n" \
"v_mul_f32 v236, s92, v236\n" \
"v_mfma_f32_16x16x32_bf16 v[184:187], v[48:51], v[216:219], v[184:187]\n" \
"ds_read_b64_tr_b16 v[28:29], v233 offset:8704\n" \
"v_mul_f32 v237, s92, v237\n" \
"v_mfma_f32_16x16x32_bf16 v[184:187], v[52:55], v[220:223], v[184:187]\n" \
"global_load_lds_dwordx4 v226, s[46:47] offset:2048\n" \
"v_cvt_pk_bf16_f32 v234, v234, v235\n" \
"v_mfma_f32_16x16x32_bf16 v[180:183], v[40:43], v[216:219], v[180:183]\n" \
"ds_read_b64_tr_b16 v[30:31], v233 offset:10752\n" \
"v_cvt_pk_bf16_f32 v236, v236, v237\n" \
"v_mfma_f32_16x16x32_bf16 v[180:183], v[44:47], v[220:223], v[180:183]\n" \
"v_accvgpr_write_b32 a40, v234\n" \
"v_accvgpr_write_b32 a41, v236\n" \
"v_mfma_f32_16x16x32_bf16 v[176:179], v[32:35], v[216:219], v[176:179]\n" \
"v_accvgpr_read_b32 v234, a44\n" \
"v_accvgpr_read_b32 v235, a45\n" \
"v_mfma_f32_16x16x32_bf16 v[176:179], v[36:39], v[220:223], v[176:179]\n" \
"s_waitcnt lgkmcnt(1)\n" \
"global_load_lds_dwordx4 v227, s[46:47] offset:3072\n" \
"v_accvgpr_read_b32 v236, a46\n" \
"v_mfma_f32_16x16x32_bf16 v[64:67], v[0:3], v[192:195], v[64:67]\n" \
"s_add_u32 s46, s46, 0x80\n" \
"s_addc_u32 s47, s47, 0\n" \
"ds_read_b128 v[216:219], v230 offset:6144\n" \
"v_mfma_f32_16x16x32_bf16 v[64:67], v[4:7], v[196:199], v[64:67]\n" \
"ds_read_b128 v[220:223], v231 offset:6144\n" \
"v_accvgpr_read_b32 v237, a47\n" \
"v_mfma_f32_16x16x32_bf16 v[68:71], v[8:11], v[192:195], v[68:71]\n" \
"ds_read_b64_tr_b16 v[32:33], v232 offset:1024\n" \
"v_mul_f32 v234, s92, v234\n" \
"v_mfma_f32_16x16x32_bf16 v[68:71], v[12:15], v[196:199], v[68:71]\n" \
"buffer_store_dwordx4 a[160:163], v249, s[56:59], s90 offen offset:256 sc1\n" \
"v_mul_f32 v235, s92, v235\n" \
"v_mul_f32 v236, s92, v236\n" \
"v_mul_f32 v237, s92, v237\n" \
"v_cvt_pk_bf16_f32 v234, v234, v235\n" \
"v_mfma_f32_16x16x32_bf16 v[72:75], v[16:19], v[192:195], v[72:75]\n" \
"ds_read_b64_tr_b16 v[34:35], v232 offset:3072\n" \
"v_cvt_pk_bf16_f32 v236, v236, v237\n" \
"v_mfma_f32_16x16x32_bf16 v[72:75], v[20:23], v[196:199], v[72:75]\n" \
"s_waitcnt lgkmcnt(3)\n" \
"ds_read_b64_tr_b16 v[36:37], v232 offset:9216\n" \
"v_accvgpr_write_b32 a42, v234\n" \
"v_mfma_f32_16x16x32_bf16 v[76:79], v[24:27], v[192:195], v[76:79]\n" \
"ds_read_b64_tr_b16 v[38:39], v232 offset:11264\n" \
"v_accvgpr_write_b32 a43, v236\n" \
"v_mfma_f32_16x16x32_bf16 v[76:79], v[28:31], v[196:199], v[76:79]\n" \
"v_accvgpr_read_b32 v234, a16\n" \
"v_accvgpr_read_b32 v235, a17\n" \
"v_mfma_f32_16x16x32_bf16 v[108:111], v[24:27], v[200:203], v[108:111]\n" \
"ds_read_b64_tr_b16 v[40:41], v233 offset:1024\n" \
"v_accvgpr_read_b32 v236, a18\n" \
"v_mfma_f32_16x16x32_bf16 v[108:111], v[28:31], v[204:207], v[108:111]\n" \
"ds_read_b64_tr_b16 v[42:43], v233 offset:3072\n" \
"v_accvgpr_read_b32 v237, a19\n" \
"v_mfma_f32_16x16x32_bf16 v[104:107], v[16:19], v[200:203], v[104:107]\n" \
"ds_read_b64_tr_b16 v[44:45], v233 offset:9216\n" \
"v_cvt_pk_bf16_f32 v234, v234, v235\n" \
"v_mfma_f32_16x16x32_bf16 v[104:107], v[20:23], v[204:207], v[104:107]\n" \
"v_cvt_pk_bf16_f32 v236, v236, v237\n" \
"v_accvgpr_write_b32 a16, v234\n" \
"v_mfma_f32_16x16x32_bf16 v[100:103], v[8:11], v[200:203], v[100:103]\n" \
"ds_read_b64_tr_b16 v[46:47], v233 offset:11264\n" \
"v_accvgpr_write_b32 a17, v236\n" \
"v_mfma_f32_16x16x32_bf16 v[100:103], v[12:15], v[204:207], v[100:103]\n" \
"ds_read_b64_tr_b16 v[48:49], v232 offset:1536\n" \
"v_accvgpr_read_b32 v234, a48\n" \
"v_mfma_f32_16x16x32_bf16 v[96:99], v[0:3], v[200:203], v[96:99]\n" \
"s_add_u32 m0, s95, 36864\n" \
"ds_read_b64_tr_b16 v[50:51], v232 offset:3584\n" \
"v_accvgpr_read_b32 v235, a49\n" \
"v_mfma_f32_16x16x32_bf16 v[96:99], v[4:7], v[204:207], v[96:99]\n" \
"global_load_lds_dwordx4 v228, s[44:45] offset:-4096\n" \
"v_accvgpr_read_b32 v236, a50\n" \
"v_mfma_f32_16x16x32_bf16 v[128:131], v[0:3], v[208:211], v[128:131]\n" \
"ds_read_b64_tr_b16 v[52:53], v232 offset:9728\n" \
"v_accvgpr_read_b32 v237, a51\n" \
"v_mfma_f32_16x16x32_bf16 v[128:131], v[4:7], v[212:215], v[128:131]\n" \
"ds_read_b64_tr_b16 v[54:55], v232 offset:11776\n" \
"v_cvt_pk_bf16_f32 v234, v234, v235\n" \
"v_mfma_f32_16x16x32_bf16 v[132:135], v[8:11], v[208:211], v[132:135]\n" \
"ds_read_b64_tr_b16 v[56:57], v233 offset:1536\n" \
"v_cvt_pk_bf16_f32 v236, v236, v237\n" \
"v_mfma_f32_16x16x32_bf16 v[132:135], v[12:15], v[212:215], v[132:135]\n" \
"global_load_lds_dwordx4 v229, s[44:45] offset:-3072\n" \
"v_accvgpr_write_b32 a18, v234\n" \
"v_mfma_f32_16x16x32_bf16 v[136:139], v[16:19], v[208:211], v[136:139]\n" \
"s_add_u32 s44, s44, 0x80000\n" \
"s_addc_u32 s45, s45, 0\n" \
"ds_read_b64_tr_b16 v[58:59], v233 offset:3584\n" \
"v_mfma_f32_16x16x32_bf16 v[136:139], v[20:23], v[212:215], v[136:139]\n" \
"ds_read_b64_tr_b16 v[60:61], v233 offset:9728\n" \
"v_accvgpr_write_b32 a19, v236\n" \
"v_mfma_f32_16x16x32_bf16 v[140:143], v[24:27], v[208:211], v[140:143]\n" \
"ds_read_b64_tr_b16 v[62:63], v233 offset:11776\n" \
"v_accvgpr_read_b32 v234, a20\n" \
"v_mfma_f32_16x16x32_bf16 v[140:143], v[28:31], v[212:215], v[140:143]\n" \
"s_waitcnt lgkmcnt(3)\n" \
"global_load_lds_dwordx4 v228, s[42:43] offset:-2048\n" \
"v_accvgpr_read_b32 v235, a21\n" \
"v_mfma_f32_16x16x32_bf16 v[172:175], v[24:27], v[216:219], v[172:175]\n" \
"v_add_u32 v230, 0x8000, v230\n" \
"v_add_u32 v231, 0x8000, v231\n" \
"v_mfma_f32_16x16x32_bf16 v[172:175], v[28:31], v[220:223], v[172:175]\n" \
"v_add_u32 v232, 0xffff4000, v232\n" \
"v_add_u32 v233, 0xffff4000, v233\n" \
"v_mfma_f32_16x16x32_bf16 v[168:171], v[16:19], v[216:219], v[168:171]\n" \
"v_accvgpr_read_b32 v236, a22\n" \
"v_accvgpr_read_b32 v237, a23\n" \
"v_mfma_f32_16x16x32_bf16 v[168:171], v[20:23], v[220:223], v[168:171]\n" \
"global_load_lds_dwordx4 v229, s[42:43] offset:-1024\n" \
"v_cvt_pk_bf16_f32 v234, v234, v235\n" \
"v_mfma_f32_16x16x32_bf16 v[164:167], v[8:11], v[216:219], v[164:167]\n" \
"s_add_u32 s42, s42, 0x80000\n" \
"s_addc_u32 s43, s43, 0\n" \
"v_cvt_pk_bf16_f32 v236, v236, v237\n" \
"v_mfma_f32_16x16x32_bf16 v[164:167], v[12:15], v[220:223], v[164:167]\n" \
"s_waitcnt vmcnt(18) lgkmcnt(0)\n" \
"v_accvgpr_write_b32 a20, v234\n" \
"v_accvgpr_write_b32 a21, v236\n" \
"v_mfma_f32_16x16x32_bf16 v[160:163], v[0:3], v[216:219], v[160:163]\n" \
"v_accvgpr_read_b32 v234, a52\n" \
"v_accvgpr_read_b32 v235, a53\n" \
"v_mfma_f32_16x16x32_bf16 v[160:163], v[4:7], v[220:223], v[160:163]\n" \
"s_barrier\n" \
"v_mfma_f32_16x16x32_bf16 v[80:83], v[32:35], v[192:195], v[80:83]\n" \
"ds_read_b64_tr_b16 v[0:1], v232 offset:0\n" \
"v_accvgpr_read_b32 v236, a54\n" \
"v_mfma_f32_16x16x32_bf16 v[80:83], v[36:39], v[196:199], v[80:83]\n" \
"ds_read_b64_tr_b16 v[2:3], v232 offset:2048\n" \
"v_accvgpr_read_b32 v237, a55\n" \
"v_mfma_f32_16x16x32_bf16 v[84:87], v[40:43], v[192:195], v[84:87]\n" \
"s_add_u32 m0, s87, 69632\n" \
"ds_read_b64_tr_b16 v[4:5], v232 offset:8192\n" \
"v_cvt_pk_bf16_f32 v234, v234, v235\n" \
"v_mfma_f32_16x16x32_bf16 v[84:87], v[44:47], v[196:199], v[84:87]\n" \
"global_load_lds_dwordx4 v224, s[48:49] offset:-4096\n" \
"v_cvt_pk_bf16_f32 v236, v236, v237\n" \
"v_mfma_f32_16x16x32_bf16 v[88:91], v[48:51], v[192:195], v[88:91]\n" \
"ds_read_b64_tr_b16 v[6:7], v232 offset:10240\n" \
"v_accvgpr_write_b32 a22, v234\n" \
"v_mfma_f32_16x16x32_bf16 v[88:91], v[52:55], v[196:199], v[88:91]\n" \
"ds_read_b64_tr_b16 v[8:9], v233 offset:0\n" \
"v_accvgpr_write_b32 a23, v236\n" \
"v_mfma_f32_16x16x32_bf16 v[92:95], v[56:59], v[192:195], v[92:95]\n" \
"ds_read_b64_tr_b16 v[10:11], v233 offset:2048\n" \
"v_accvgpr_read_b32 v234, a24\n" \
"v_mfma_f32_16x16x32_bf16 v[92:95], v[60:63], v[196:199], v[92:95]\n" \
"global_load_lds_dwordx4 v225, s[48:49] offset:-3072\n" \
"v_accvgpr_read_b32 v235, a25\n" \
"v_mfma_f32_16x16x32_bf16 v[124:127], v[56:59], v[200:203], v[124:127]\n" \
"ds_read_b128 v[192:195], v230 offset:0\n" \
"v_accvgpr_read_b32 v236, a26\n" \
"v_mfma_f32_16x16x32_bf16 v[124:127], v[60:63], v[204:207], v[124:127]\n" \
"ds_read_b128 v[196:199], v231 offset:0\n" \
"v_accvgpr_read_b32 v237, a27\n" \
"v_mfma_f32_16x16x32_bf16 v[120:123], v[48:51], v[200:203], v[120:123]\n" \
"ds_read_b64_tr_b16 v[12:13], v233 offset:8192\n" \
"v_cvt_pk_bf16_f32 v234, v234, v235\n" \
"v_mfma_f32_16x16x32_bf16 v[120:123], v[52:55], v[204:207], v[120:123]\n" \
"global_load_lds_dwordx4 v226, s[48:49] offset:-2048\n" \
"v_cvt_pk_bf16_f32 v236, v236, v237\n" \
"v_mfma_f32_16x16x32_bf16 v[116:119], v[40:43], v[200:203], v[116:119]\n" \
"ds_read_b64_tr_b16 v[14:15], v233 offset:10240\n" \
"v_accvgpr_write_b32 a24, v234\n" \
"v_mfma_f32_16x16x32_bf16 v[116:119], v[44:47], v[204:207], v[116:119]\n" \
"ds_read_b64_tr_b16 v[16:17], v232 offset:512\n" \
"v_accvgpr_write_b32 a25, v236\n" \
"v_mfma_f32_16x16x32_bf16 v[112:115], v[32:35], v[200:203], v[112:115]\n" \
"ds_read_b64_tr_b16 v[18:19], v232 offset:2560\n" \
"v_accvgpr_read_b32 v234, a56\n" \
"v_mfma_f32_16x16x32_bf16 v[112:115], v[36:39], v[204:207], v[112:115]\n" \
"global_load_lds_dwordx4 v227, s[48:49] offset:-1024\n" \
"v_accvgpr_read_b32 v235, a57\n" \
"v_mfma_f32_16x16x32_bf16 v[144:147], v[32:35], v[208:211], v[144:147]\n" \
"s_add_u32 s48, s48, 0x80\n" \
"s_addc_u32 s49, s49, 0\n" \
"ds_read_b128 v[200:203], v230 offset:2048\n" \
"v_mfma_f32_16x16x32_bf16 v[144:147], v[36:39], v[212:215], v[144:147]\n" \
"ds_read_b128 v[204:207], v231 offset:2048\n" \
"v_accvgpr_read_b32 v236, a58\n" \
"v_mfma_f32_16x16x32_bf16 v[148:151], v[40:43], v[208:211], v[148:151]\n" \
"ds_read_b64_tr_b16 v[20:21], v232 offset:8704\n" \
"v_accvgpr_read_b32 v237, a59\n" \
"v_mfma_f32_16x16x32_bf16 v[148:151], v[44:47], v[212:215], v[148:151]\n" \
"global_load_lds_dwordx4 v224, s[46:47] offset:0\n" \
"v_cvt_pk_bf16_f32 v234, v234, v235\n" \
"v_mfma_f32_16x16x32_bf16 v[152:155], v[48:51], v[208:211], v[152:155]\n" \
"ds_read_b64_tr_b16 v[22:23], v232 offset:10752\n" \
"v_cvt_pk_bf16_f32 v236, v236, v237\n" \
"v_mfma_f32_16x16x32_bf16 v[152:155], v[52:55], v[212:215], v[152:155]\n" \
"ds_read_b64_tr_b16 v[24:25], v233 offset:512\n" \
"v_accvgpr_write_b32 a26, v234\n" \
"v_mfma_f32_16x16x32_bf16 v[156:159], v[56:59], v[208:211], v[156:159]\n" \
"ds_read_b64_tr_b16 v[26:27], v233 offset:2560\n" \
"v_accvgpr_write_b32 a27, v236\n" \
"v_mfma_f32_16x16x32_bf16 v[156:159], v[60:63], v[212:215], v[156:159]\n" \
"global_load_lds_dwordx4 v225, s[46:47] offset:1024\n" \
"v_accvgpr_read_b32 v234, a28\n" \
"v_mfma_f32_16x16x32_bf16 v[188:191], v[56:59], v[216:219], v[188:191]\n" \
"ds_read_b128 v[208:211], v230 offset:4096\n" \
"v_accvgpr_read_b32 v235, a29\n" \
"v_mfma_f32_16x16x32_bf16 v[188:191], v[60:63], v[220:223], v[188:191]\n" \
"ds_read_b128 v[212:215], v231 offset:4096\n" \
"v_accvgpr_read_b32 v236, a30\n" \
"v_mfma_f32_16x16x32_bf16 v[184:187], v[48:51], v[216:219], v[184:187]\n" \
"ds_read_b64_tr_b16 v[28:29], v233 offset:8704\n" \
"v_accvgpr_read_b32 v237, a31\n" \
"v_mfma_f32_16x16x32_bf16 v[184:187], v[52:55], v[220:223], v[184:187]\n" \
"global_load_lds_dwordx4 v226, s[46:47] offset:2048\n" \
"v_cvt_pk_bf16_f32 v234, v234, v235\n" \
"v_mfma_f32_16x16x32_bf16 v[180:183], v[40:43], v[216:219], v[180:183]\n" \
"ds_read_b64_tr_b16 v[30:31], v233 offset:10752\n" \
"v_cvt_pk_bf16_f32 v236, v236, v237\n" \
"v_mfma_f32_16x16x32_bf16 v[180:183], v[44:47], v[220:223], v[180:183]\n" \
"v_accvgpr_write_b32 a28, v234\n" \
"v_accvgpr_write_b32 a29, v236\n" \
"v_mfma_f32_16x16x32_bf16 v[176:179], v[32:35], v[216:219], v[176:179]\n" \
"v_accvgpr_read_b32 v234, a60\n" \
"v_accvgpr_read_b32 v235, a61\n" \
"v_mfma_f32_16x16x32_bf16 v[176:179], v[36:39], v[220:223], v[176:179]\n" \
"s_waitcnt lgkmcnt(1)\n" \
"global_load_lds_dwordx4 v227, s[46:47] offset:3072\n" \
"v_accvgpr_read_b32 v236, a62\n" \
"v_mfma_f32_16x16x32_bf16 v[64:67], v[0:3], v[192:195], v[64:67]\n" \
"s_add_u32 s46, s46, 0x80\n" \
"s_addc_u32 s47, s47, 0\n" \
"ds_read_b128 v[216:219], v230 offset:6144\n" \
"v_mfma_f32_16x16x32_bf16 v[64:67], v[4:7], v[196:199], v[64:67]\n" \
"ds_read_b128 v[220:223], v231 offset:6144\n" \
"v_accvgpr_read_b32 v237, a63\n" \
"v_mfma_f32_16x16x32_bf16 v[68:71], v[8:11], v[192:195], v[68:71]\n" \
"ds_read_b64_tr_b16 v[32:33], v232 offset:1024\n" \
"v_cvt_pk_bf16_f32 v234, v234, v235\n" \
"v_mfma_f32_16x16x32_bf16 v[68:71], v[12:15], v[196:199], v[68:71]\n" \
"buffer_store_dwordx4 a[136:139], v249, s[56:59], s90 offen offset:2048 sc1\n" \
"v_cvt_pk_bf16_f32 v236, v236, v237\n" \
"v_accvgpr_write_b32 a30, v234\n" \
"v_accvgpr_write_b32 a31, v236\n" \
"v_mfma_f32_16x16x32_bf16 v[72:75], v[16:19], v[192:195], v[72:75]\n" \
"ds_read_b64_tr_b16 v[34:35], v232 offset:3072\n" \
"v_mfma_f32_16x16x32_bf16 v[72:75], v[20:23], v[196:199], v[72:75]\n" \
"s_waitcnt lgkmcnt(3)\n" \
"ds_read_b64_tr_b16 v[36:37], v232 offset:9216\n" \
"v_mfma_f32_16x16x32_bf16 v[76:79], v[24:27], v[192:195], v[76:79]\n" \
"ds_read_b64_tr_b16 v[38:39], v232 offset:11264\n" \
"v_mfma_f32_16x16x32_bf16 v[76:79], v[28:31], v[196:199], v[76:79]\n" \
"buffer_store_dwordx4 a[168:171], v249, s[56:59], s90 offen offset:2304 sc1\n" \
"v_mfma_f32_16x16x32_bf16 v[108:111], v[24:27], v[200:203], v[108:111]\n" \
"s_lshl_b32 s70, s63, 2\n" \
"ds_read_b64_tr_b16 v[40:41], v233 offset:1024\n" \
"v_mfma_f32_16x16x32_bf16 v[108:111], v[28:31], v[204:207], v[108:111]\n" \
"s_add_u32 s70, s70, s91\n" \
"ds_read_b64_tr_b16 v[42:43], v233 offset:3072\n" \
"v_mov_b32 v238, s60\n" \
"v_mfma_f32_16x16x32_bf16 v[104:107], v[16:19], v[200:203], v[104:107]\n" \
"ds_read_b64_tr_b16 v[44:45], v233 offset:9216\n" \
"v_lshlrev_b32 v239, 28, s61\n" \
"v_mfma_f32_16x16x32_bf16 v[104:107], v[20:23], v[204:207], v[104:107]\n" \
"buffer_store_dword v238, v239, s[56:59], s70 offen sc1\n" \
"v_mfma_f32_16x16x32_bf16 v[100:103], v[8:11], v[200:203], v[100:103]\n" \
"ds_read_b64_tr_b16 v[46:47], v233 offset:11264\n" \
"v_mfma_f32_16x16x32_bf16 v[100:103], v[12:15], v[204:207], v[100:103]\n" \
"ds_read_b64_tr_b16 v[48:49], v232 offset:1536\n" \
"v_mfma_f32_16x16x32_bf16 v[96:99], v[0:3], v[200:203], v[96:99]\n" \
"s_add_u32 m0, s95, 53248\n" \
"ds_read_b64_tr_b16 v[50:51], v232 offset:3584\n" \
"v_mfma_f32_16x16x32_bf16 v[96:99], v[4:7], v[204:207], v[96:99]\n" \
"global_load_lds_dwordx4 v228, s[44:45] offset:-4096\n" \
"v_mfma_f32_16x16x32_bf16 v[128:131], v[0:3], v[208:211], v[128:131]\n" \
"ds_read_b64_tr_b16 v[52:53], v232 offset:9728\n" \
"Lpg3850_%=:\n" \
"s_cbranch_execz Lpg5534_%=\n" \
"v_mfma_f32_16x16x32_bf16 v[128:131], v[4:7], v[212:215], v[128:131]\n" \
"ds_read_b64_tr_b16 v[54:55], v232 offset:11776\n" \
"v_mfma_f32_16x16x32_bf16 v[132:135], v[8:11], v[208:211], v[132:135]\n" \
"ds_read_b64_tr_b16 v[56:57], v233 offset:1536\n" \
"v_mfma_f32_16x16x32_bf16 v[132:135], v[12:15], v[212:215], v[132:135]\n" \
"global_load_lds_dwordx4 v229, s[44:45] offset:-3072\n" \
"v_mfma_f32_16x16x32_bf16 v[136:139], v[16:19], v[208:211], v[136:139]\n" \
"s_add_u32 s44, s44, 0x100000\n" \
"s_addc_u32 s45, s45, 0\n" \
"ds_read_b64_tr_b16 v[58:59], v233 offset:3584\n" \
"v_mfma_f32_16x16x32_bf16 v[136:139], v[20:23], v[212:215], v[136:139]\n" \
"ds_read_b64_tr_b16 v[60:61], v233 offset:9728\n" \
"v_mfma_f32_16x16x32_bf16 v[140:143], v[24:27], v[208:211], v[140:143]\n" \
"ds_read_b64_tr_b16 v[62:63], v233 offset:11776\n" \
"v_mfma_f32_16x16x32_bf16 v[140:143], v[28:31], v[212:215], v[140:143]\n" \
"s_waitcnt lgkmcnt(3)\n" \
"global_load_lds_dwordx4 v228, s[42:43] offset:-2048\n" \
"v_mfma_f32_16x16x32_bf16 v[172:175], v[24:27], v[216:219], v[172:175]\n" \
"v_add_u32 v230, 0x8000, v230\n" \
"v_add_u32 v231, 0x8000, v231\n" \
"v_mfma_f32_16x16x32_bf16 v[172:175], v[28:31], v[220:223], v[172:175]\n" \
"v_add_u32 v232, 0x4000, v232\n" \
"v_add_u32 v233, 0x4000, v233\n" \
"v_mfma_f32_16x16x32_bf16 v[168:171], v[16:19], v[216:219], v[168:171]\n" \
"v_mfma_f32_16x16x32_bf16 v[168:171], v[20:23], v[220:223], v[168:171]\n" \
"global_load_lds_dwordx4 v229, s[42:43] offset:-1024\n" \
"v_mfma_f32_16x16x32_bf16 v[164:167], v[8:11], v[216:219], v[164:167]\n" \
"s_add_u32 s42, s42, 0x100000\n" \
"s_addc_u32 s43, s43, 0\n" \
"v_mfma_f32_16x16x32_bf16 v[164:167], v[12:15], v[220:223], v[164:167]\n" \
"s_waitcnt vmcnt(20) lgkmcnt(0)\n" \
"v_mfma_f32_16x16x32_bf16 v[160:163], v[0:3], v[216:219], v[160:163]\n" \
"v_mfma_f32_16x16x32_bf16 v[160:163], v[4:7], v[220:223], v[160:163]\n" \
"s_barrier\n" \
"v_mfma_f32_16x16x32_bf16 v[80:83], v[32:35], v[192:195], v[80:83]\n" \
"ds_read_b64_tr_b16 v[0:1], v232 offset:0\n" \
"v_mfma_f32_16x16x32_bf16 v[80:83], v[36:39], v[196:199], v[80:83]\n" \
"ds_read_b64_tr_b16 v[2:3], v232 offset:2048\n" \
"v_mfma_f32_16x16x32_bf16 v[84:87], v[40:43], v[192:195], v[84:87]\n" \
"s_add_u32 m0, s87, 102400\n" \
"ds_read_b64_tr_b16 v[4:5], v232 offset:8192\n" \
"v_mfma_f32_16x16x32_bf16 v[84:87], v[44:47], v[196:199], v[84:87]\n" \
"global_load_lds_dwordx4 v224, s[48:49] offset:-4096\n" \
"v_mfma_f32_16x16x32_bf16 v[88:91], v[48:51], v[192:195], v[88:91]\n" \
"ds_read_b64_tr_b16 v[6:7], v232 offset:10240\n" \
"v_mfma_f32_16x16x32_bf16 v[88:91], v[52:55], v[196:199], v[88:91]\n" \
"ds_read_b64_tr_b16 v[8:9], v233 offset:0\n" \
"v_mfma_f32_16x16x32_bf16 v[92:95], v[56:59], v[192:195], v[92:95]\n" \
"ds_read_b64_tr_b16 v[10:11], v233 offset:2048\n" \
"v_mfma_f32_16x16x32_bf16 v[92:95], v[60:63], v[196:199], v[92:95]\n" \
"global_load_lds_dwordx4 v225, s[48:49] offset:-3072\n" \
"v_mfma_f32_16x16x32_bf16 v[124:127], v[56:59], v[200:203], v[124:127]\n" \
"ds_read_b128 v[192:195], v230 offset:0\n" \
"v_mfma_f32_16x16x32_bf16 v[124:127], v[60:63], v[204:207], v[124:127]\n" \
"ds_read_b128 v[196:199], v231 offset:0\n" \
"v_mfma_f32_16x16x32_bf16 v[120:123], v[48:51], v[200:203], v[120:123]\n" \
"ds_read_b64_tr_b16 v[12:13], v233 offset:8192\n" \
"v_mfma_f32_16x16x32_bf16 v[120:123], v[52:55], v[204:207], v[120:123]\n" \
"global_load_lds_dwordx4 v226, s[48:49] offset:-2048\n" \
"v_mfma_f32_16x16x32_bf16 v[116:119], v[40:43], v[200:203], v[116:119]\n" \
"ds_read_b64_tr_b16 v[14:15], v233 offset:10240\n" \
"v_mfma_f32_16x16x32_bf16 v[116:119], v[44:47], v[204:207], v[116:119]\n" \
"ds_read_b64_tr_b16 v[16:17], v232 offset:512\n" \
"v_mfma_f32_16x16x32_bf16 v[112:115], v[32:35], v[200:203], v[112:115]\n" \
"ds_read_b64_tr_b16 v[18:19], v232 offset:2560\n" \
"v_mfma_f32_16x16x32_bf16 v[112:115], v[36:39], v[204:207], v[112:115]\n" \
"global_load_lds_dwordx4 v227, s[48:49] offset:-1024\n" \
"v_mfma_f32_16x16x32_bf16 v[144:147], v[32:35], v[208:211], v[144:147]\n" \
"s_add_u32 s48, s48, 0x100\n" \
"s_addc_u32 s49, s49, 0\n" \
"ds_read_b128 v[200:203], v230 offset:2048\n" \
"v_mfma_f32_16x16x32_bf16 v[144:147], v[36:39], v[212:215], v[144:147]\n" \
"ds_read_b128 v[204:207], v231 offset:2048\n" \
"v_mfma_f32_16x16x32_bf16 v[148:151], v[40:43], v[208:211], v[148:151]\n" \
"ds_read_b64_tr_b16 v[20:21], v232 offset:8704\n" \
"v_mfma_f32_16x16x32_bf16 v[148:151], v[44:47], v[212:215], v[148:151]\n" \
"global_load_lds_dwordx4 v224, s[46:47] offset:0\n" \
"v_mfma_f32_16x16x32_bf16 v[152:155], v[48:51], v[208:211], v[152:155]\n" \
"ds_read_b64_tr_b16 v[22:23], v232 offset:10752\n" \
"v_mfma_f32_16x16x32_bf16 v[152:155], v[52:55], v[212:215], v[152:155]\n" \
"ds_read_b64_tr_b16 v[24:25], v233 offset:512\n" \
"v_mfma_f32_16x16x32_bf16 v[156:159], v[56:59], v[208:211], v[156:159]\n" \
"ds_read_b64_tr_b16 v[26:27], v233 offset:2560\n" \
"v_mfma_f32_16x16x32_bf16 v[156:159], v[60:63], v[212:215], v[156:159]\n" \
"global_load_lds_dwordx4 v225, s[46:47] offset:1024\n" \
"v_mfma_f32_16x16x32_bf16 v[188:191], v[56:59], v[216:219], v[188:191]\n" \
"ds_read_b128 v[208:211], v230 offset:4096\n" \
"v_mfma_f32_16x16x32_bf16 v[188:191], v[60:63], v[220:223], v[188:191]\n" \
"ds_read_b128 v[212:215], v231 offset:4096\n" \
"v_mfma_f32_16x16x32_bf16 v[184:187], v[48:51], v[216:219], v[184:187]\n" \
"ds_read_b64_tr_b16 v[28:29], v233 offset:8704\n" \
"v_mfma_f32_16x16x32_bf16 v[184:187], v[52:55], v[220:223], v[184:187]\n" \
"global_load_lds_dwordx4 v226, s[46:47] offset:2048\n" \
"v_mfma_f32_16x16x32_bf16 v[180:183], v[40:43], v[216:219], v[180:183]\n" \
"ds_read_b64_tr_b16 v[30:31], v233 offset:10752\n" \
"v_mfma_f32_16x16x32_bf16 v[180:183], v[44:47], v[220:223], v[180:183]\n" \
"v_mfma_f32_16x16x32_bf16 v[176:179], v[32:35], v[216:219], v[176:179]\n" \
"v_mfma_f32_16x16x32_bf16 v[176:179], v[36:39], v[220:223], v[176:179]\n" \
"s_waitcnt lgkmcnt(1)\n" \
"global_load_lds_dwordx4 v227, s[46:47] offset:3072\n" \
"v_mfma_f32_16x16x32_bf16 v[64:67], v[0:3], v[192:195], v[64:67]\n" \
"s_add_u32 s46, s46, 0x100\n" \
"s_addc_u32 s47, s47, 0\n" \
"ds_read_b128 v[216:219], v230 offset:6144\n" \
"v_mfma_f32_16x16x32_bf16 v[64:67], v[4:7], v[196:199], v[64:67]\n" \
"ds_read_b128 v[220:223], v231 offset:6144\n" \
"s_add_u32 s90, s89, 139264\n" \
"v_mfma_f32_16x16x32_bf16 v[68:71], v[8:11], v[192:195], v[68:71]\n" \
"ds_read_b64_tr_b16 v[32:33], v232 offset:1024\n" \
"v_mfma_f32_16x16x32_bf16 v[68:71], v[12:15], v[196:199], v[68:71]\n" \
"buffer_store_dwordx4 a[144:147], v248, s[56:59], s90 offen offset:0 sc1\n" \
"v_mfma_f32_16x16x32_bf16 v[72:75], v[16:19], v[192:195], v[72:75]\n" \
"ds_read_b64_tr_b16 v[34:35], v232 offset:3072\n" \
"v_mfma_f32_16x16x32_bf16 v[72:75], v[20:23], v[196:199], v[72:75]\n" \
"s_waitcnt lgkmcnt(3)\n" \
"ds_read_b64_tr_b16 v[36:37], v232 offset:9216\n" \
"v_mfma_f32_16x16x32_bf16 v[76:79], v[24:27], v[192:195], v[76:79]\n" \
"ds_read_b64_tr_b16 v[38:39], v232 offset:11264\n" \
"v_mfma_f32_16x16x32_bf16 v[76:79], v[28:31], v[196:199], v[76:79]\n" \
"v_mfma_f32_16x16x32_bf16 v[108:111], v[24:27], v[200:203], v[108:111]\n" \
"ds_read_b64_tr_b16 v[40:41], v233 offset:1024\n" \
"v_mfma_f32_16x16x32_bf16 v[108:111], v[28:31], v[204:207], v[108:111]\n" \
"ds_read_b64_tr_b16 v[42:43], v233 offset:3072\n" \
"v_mfma_f32_16x16x32_bf16 v[104:107], v[16:19], v[200:203], v[104:107]\n" \
"ds_read_b64_tr_b16 v[44:45], v233 offset:9216\n" \
"v_mfma_f32_16x16x32_bf16 v[104:107], v[20:23], v[204:207], v[104:107]\n" \
"v_mfma_f32_16x16x32_bf16 v[100:103], v[8:11], v[200:203], v[100:103]\n" \
"ds_read_b64_tr_b16 v[46:47], v233 offset:11264\n" \
"v_mfma_f32_16x16x32_bf16 v[100:103], v[12:15], v[204:207], v[100:103]\n" \
"ds_read_b64_tr_b16 v[48:49], v232 offset:1536\n" \
"v_mfma_f32_16x16x32_bf16 v[96:99], v[0:3], v[200:203], v[96:99]\n" \
"s_add_u32 m0, s95, 4096\n" \
"ds_read_b64_tr_b16 v[50:51], v232 offset:3584\n" \
"v_mfma_f32_16x16x32_bf16 v[96:99], v[4:7], v[204:207], v[96:99]\n" \
"global_load_lds_dwordx4 v228, s[44:45] offset:-4096\n" \
"v_mfma_f32_16x16x32_bf16 v[128:131], v[0:3], v[208:211], v[128:131]\n" \
"ds_read_b64_tr_b16 v[52:53], v232 offset:9728\n" \
"v_mfma_f32_16x16x32_bf16 v[128:131], v[4:7], v[212:215], v[128:131]\n" \
"ds_read_b64_tr_b16 v[54:55], v232 offset:11776\n" \
"v_mfma_f32_16x16x32_bf16 v[132:135], v[8:11], v[208:211], v[132:135]\n" \
"ds_read_b64_tr_b16 v[56:57], v233 offset:1536\n" \
"v_mfma_f32_16x16x32_bf16 v[132:135], v[12:15], v[212:215], v[132:135]\n" \
"global_load_lds_dwordx4 v229, s[44:45] offset:-3072\n" \
"v_mfma_f32_16x16x32_bf16 v[136:139], v[16:19], v[208:211], v[136:139]\n" \
"s_add_u32 s44, s44, 0x80000\n" \
"s_addc_u32 s45, s45, 0\n" \
"ds_read_b64_tr_b16 v[58:59], v233 offset:3584\n" \
"v_mfma_f32_16x16x32_bf16 v[136:139], v[20:23], v[212:215], v[136:139]\n" \
"ds_read_b64_tr_b16 v[60:61], v233 offset:9728\n" \
"v_mfma_f32_16x16x32_bf16 v[140:143], v[24:27], v[208:211], v[140:143]\n" \
"ds_read_b64_tr_b16 v[62:63], v233 offset:11776\n" \
"v_mfma_f32_16x16x32_bf16 v[140:143], v[28:31], v[212:215], v[140:143]\n" \
"s_waitcnt lgkmcnt(3)\n" \
"global_load_lds_dwordx4 v228, s[42:43] offset:-2048\n" \
"v_mfma_f32_16x16x32_bf16 v[172:175], v[24:27], v[216:219], v[172:175]\n" \
"v_add_u32 v230, 0xffff0000, v230\n" \
"v_add_u32 v231, 0xffff0000, v231\n" \
"v_mfma_f32_16x16x32_bf16 v[172:175], v[28:31], v[220:223], v[172:175]\n" \
"v_add_u32 v232, 0x4000, v232\n" \
"v_add_u32 v233, 0x4000, v233\n" \
"v_mfma_f32_16x16x32_bf16 v[168:171], v[16:19], v[216:219], v[168:171]\n" \
"v_mfma_f32_16x16x32_bf16 v[168:171], v[20:23], v[220:223], v[168:171]\n" \
"global_load_lds_dwordx4 v229, s[42:43] offset:-1024\n" \
"v_mfma_f32_16x16x32_bf16 v[164:167], v[8:11], v[216:219], v[164:167]\n" \
"s_add_u32 s42, s42, 0x80000\n" \
"s_addc_u32 s43, s43, 0\n" \
"v_mfma_f32_16x16x32_bf16 v[164:167], v[12:15], v[220:223], v[164:167]\n" \
"s_waitcnt vmcnt(20) lgkmcnt(0)\n" \
"v_mfma_f32_16x16x32_bf16 v[160:163], v[0:3], v[216:219], v[160:163]\n" \
"v_mfma_f32_16x16x32_bf16 v[160:163], v[4:7], v[220:223], v[160:163]\n" \
"s_barrier\n" \
"v_mfma_f32_16x16x32_bf16 v[80:83], v[32:35], v[192:195], v[80:83]\n" \
"ds_read_b64_tr_b16 v[0:1], v232 offset:0\n" \
"v_mfma_f32_16x16x32_bf16 v[80:83], v[36:39], v[196:199], v[80:83]\n" \
"ds_read_b64_tr_b16 v[2:3], v232 offset:2048\n" \
"v_mfma_f32_16x16x32_bf16 v[84:87], v[40:43], v[192:195], v[84:87]\n" \
"s_add_u32 m0, s87, 135168\n" \
"ds_read_b64_tr_b16 v[4:5], v232 offset:8192\n" \
"v_mfma_f32_16x16x32_bf16 v[84:87], v[44:47], v[196:199], v[84:87]\n" \
"global_load_lds_dwordx4 v224, s[48:49] offset:-4096\n" \
"v_mfma_f32_16x16x32_bf16 v[88:91], v[48:51], v[192:195], v[88:91]\n" \
"ds_read_b64_tr_b16 v[6:7], v232 offset:10240\n" \
"v_mfma_f32_16x16x32_bf16 v[88:91], v[52:55], v[196:199], v[88:91]\n" \
"ds_read_b64_tr_b16 v[8:9], v233 offset:0\n" \
"v_mfma_f32_16x16x32_bf16 v[92:95], v[56:59], v[192:195], v[92:95]\n" \
"ds_read_b64_tr_b16 v[10:11], v233 offset:2048\n" \
"v_mfma_f32_16x16x32_bf16 v[92:95], v[60:63], v[196:199], v[92:95]\n" \
"global_load_lds_dwordx4 v225, s[48:49] offset:-3072\n" \
"v_mfma_f32_16x16x32_bf16 v[124:127], v[56:59], v[200:203], v[124:127]\n" \
"ds_read_b128 v[192:195], v230 offset:0\n" \
"v_mfma_f32_16x16x32_bf16 v[124:127], v[60:63], v[204:207], v[124:127]\n" \
"ds_read_b128 v[196:199], v231 offset:0\n" \
"v_mfma_f32_16x16x32_bf16 v[120:123], v[48:51], v[200:203], v[120:123]\n" \
"ds_read_b64_tr_b16 v[12:13], v233 offset:8192\n" \
"v_mfma_f32_16x16x32_bf16 v[120:123], v[52:55], v[204:207], v[120:123]\n" \
"global_load_lds_dwordx4 v226, s[48:49] offset:-2048\n" \
"v_mfma_f32_16x16x32_bf16 v[116:119], v[40:43], v[200:203], v[116:119]\n" \
"ds_read_b64_tr_b16 v[14:15], v233 offset:10240\n" \
"v_mfma_f32_16x16x32_bf16 v[116:119], v[44:47], v[204:207], v[116:119]\n" \
"ds_read_b64_tr_b16 v[16:17], v232 offset:512\n" \
"v_mfma_f32_16x16x32_bf16 v[112:115], v[32:35], v[200:203], v[112:115]\n" \
"ds_read_b64_tr_b16 v[18:19], v232 offset:2560\n" \
"v_mfma_f32_16x16x32_bf16 v[112:115], v[36:39], v[204:207], v[112:115]\n" \
"global_load_lds_dwordx4 v227, s[48:49] offset:-1024\n" \
"v_mfma_f32_16x16x32_bf16 v[144:147], v[32:35], v[208:211], v[144:147]\n" \
"s_add_u32 s48, s48, 0x80\n" \
"s_addc_u32 s49, s49, 0\n" \
"ds_read_b128 v[200:203], v230 offset:2048\n" \
"v_mfma_f32_16x16x32_bf16 v[144:147], v[36:39], v[212:215], v[144:147]\n" \
"ds_read_b128 v[204:207], v231 offset:2048\n" \
"v_mfma_f32_16x16x32_bf16 v[148:151], v[40:43], v[208:211], v[148:151]\n" \
"ds_read_b64_tr_b16 v[20:21], v232 offset:8704\n" \
"v_mfma_f32_16x16x32_bf16 v[148:151], v[44:47], v[212:215], v[148:151]\n" \
"global_load_lds_dwordx4 v224, s[46:47] offset:0\n" \
"v_mfma_f32_16x16x32_bf16 v[152:155], v[48:51], v[208:211], v[152:155]\n" \
"ds_read_b64_tr_b16 v[22:23], v232 offset:10752\n" \
"v_mfma_f32_16x16x32_bf16 v[152:155], v[52:55], v[212:215], v[152:155]\n" \
"ds_read_b64_tr_b16 v[24:25], v233 offset:512\n" \
"v_mfma_f32_16x16x32_bf16 v[156:159], v[56:59], v[208:211], v[156:159]\n" \
"ds_read_b64_tr_b16 v[26:27], v233 offset:2560\n" \
"v_mfma_f32_16x16x32_bf16 v[156:159], v[60:63], v[212:215], v[156:159]\n" \
"global_load_lds_dwordx4 v225, s[46:47] offset:1024\n" \
"v_mfma_f32_16x16x32_bf16 v[188:191], v[56:59], v[216:219], v[188:191]\n" \
"ds_read_b128 v[208:211], v230 offset:4096\n" \
"v_mfma_f32_16x16x32_bf16 v[188:191], v[60:63], v[220:223], v[188:191]\n" \
"ds_read_b128 v[212:215], v231 offset:4096\n" \
"v_mfma_f32_16x16x32_bf16 v[184:187], v[48:51], v[216:219], v[184:187]\n" \
"ds_read_b64_tr_b16 v[28:29], v233 offset:8704\n" \
"v_mfma_f32_16x16x32_bf16 v[184:187], v[52:55], v[220:223], v[184:187]\n" \
"global_load_lds_dwordx4 v226, s[46:47] offset:2048\n" \
"v_mfma_f32_16x16x32_bf16 v[180:183], v[40:43], v[216:219], v[180:183]\n" \
"ds_read_b64_tr_b16 v[30:31], v233 offset:10752\n" \
"v_mfma_f32_16x16x32_bf16 v[180:183], v[44:47], v[220:223], v[180:183]\n" \
"v_mfma_f32_16x16x32_bf16 v[176:179], v[32:35], v[216:219], v[176:179]\n" \
"v_mfma_f32_16x16x32_bf16 v[176:179], v[36:39], v[220:223], v[176:179]\n" \
"s_waitcnt lgkmcnt(1)\n" \
"global_load_lds_dwordx4 v227, s[46:47] offset:3072\n" \
"v_mfma_f32_16x16x32_bf16 v[64:67], v[0:3], v[192:195], v[64:67]\n" \
"s_add_u32 s46, s46, 0x80\n" \
"s_addc_u32 s47, s47, 0\n" \
"ds_read_b128 v[216:219], v230 offset:6144\n" \
"v_mfma_f32_16x16x32_bf16 v[64:67], v[4:7], v[196:199], v[64:67]\n" \
"ds_read_b128 v[220:223], v231 offset:6144\n" \
"v_mfma_f32_16x16x32_bf16 v[68:71], v[8:11], v[192:195], v[68:71]\n" \
"ds_read_b64_tr_b16 v[32:33], v232 offset:1024\n" \
"v_mfma_f32_16x16x32_bf16 v[68:71], v[12:15], v[196:199], v[68:71]\n" \
"buffer_store_dwordx4 a[148:151], v248, s[56:59], s90 offen offset:1024 sc1\n" \
"v_mfma_f32_16x16x32_bf16 v[72:75], v[16:19], v[192:195], v[72:75]\n" \
"ds_read_b64_tr_b16 v[34:35], v232 offset:3072\n" \
"v_mfma_f32_16x16x32_bf16 v[72:75], v[20:23], v[196:199], v[72:75]\n" \
"s_waitcnt lgkmcnt(3)\n" \
"ds_read_b64_tr_b16 v[36:37], v232 offset:9216\n" \
"v_mfma_f32_16x16x32_bf16 v[76:79], v[24:27], v[192:195], v[76:79]\n" \
"ds_read_b64_tr_b16 v[38:39], v232 offset:11264\n" \
"v_mfma_f32_16x16x32_bf16 v[76:79], v[28:31], v[196:199], v[76:79]\n" \
"v_mfma_f32_16x16x32_bf16 v[108:111], v[24:27], v[200:203], v[108:111]\n" \
"ds_read_b64_tr_b16 v[40:41], v233 offset:1024\n" \
"v_mfma_f32_16x16x32_bf16 v[108:111], v[28:31], v[204:207], v[108:111]\n" \
"ds_read_b64_tr_b16 v[42:43], v233 offset:3072\n" \
"v_mfma_f32_16x16x32_bf16 v[104:107], v[16:19], v[200:203], v[104:107]\n" \
"ds_read_b64_tr_b16 v[44:45], v233 offset:9216\n" \
"v_mfma_f32_16x16x32_bf16 v[104:107], v[20:23], v[204:207], v[104:107]\n" \
"v_mfma_f32_16x16x32_bf16 v[100:103], v[8:11], v[200:203], v[100:103]\n" \
"ds_read_b64_tr_b16 v[46:47], v233 offset:11264\n" \
"v_mfma_f32_16x16x32_bf16 v[100:103], v[12:15], v[204:207], v[100:103]\n" \
"ds_read_b64_tr_b16 v[48:49], v232 offset:1536\n" \
"v_mfma_f32_16x16x32_bf16 v[96:99], v[0:3], v[200:203], v[96:99]\n" \
"s_add_u32 m0, s95, 20480\n" \
"ds_read_b64_tr_b16 v[50:51], v232 offset:3584\n" \
"v_mfma_f32_16x16x32_bf16 v[96:99], v[4:7], v[204:207], v[96:99]\n" \
"global_load_lds_dwordx4 v228, s[44:45] offset:-4096\n" \
"v_mfma_f32_16x16x32_bf16 v[128:131], v[0:3], v[208:211], v[128:131]\n" \
"ds_read_b64_tr_b16 v[52:53], v232 offset:9728\n" \
"v_mfma_f32_16x16x32_bf16 v[128:131], v[4:7], v[212:215], v[128:131]\n" \
"ds_read_b64_tr_b16 v[54:55], v232 offset:11776\n" \
"v_mfma_f32_16x16x32_bf16 v[132:135], v[8:11], v[208:211], v[132:135]\n" \
"ds_read_b64_tr_b16 v[56:57], v233 offset:1536\n" \
"v_mfma_f32_16x16x32_bf16 v[132:135], v[12:15], v[212:215], v[132:135]\n" \
"global_load_lds_dwordx4 v229, s[44:45] offset:-3072\n" \
"v_mfma_f32_16x16x32_bf16 v[136:139], v[16:19], v[208:211], v[136:139]\n" \
"s_add_u32 s44, s44, 0x80000\n" \
"s_addc_u32 s45, s45, 0\n" \
"ds_read_b64_tr_b16 v[58:59], v233 offset:3584\n" \
"v_mfma_f32_16x16x32_bf16 v[136:139], v[20:23], v[212:215], v[136:139]\n" \
"ds_read_b64_tr_b16 v[60:61], v233 offset:9728\n" \
"v_mfma_f32_16x16x32_bf16 v[140:143], v[24:27], v[208:211], v[140:143]\n" \
"ds_read_b64_tr_b16 v[62:63], v233 offset:11776\n" \
"v_mfma_f32_16x16x32_bf16 v[140:143], v[28:31], v[212:215], v[140:143]\n" \
"s_waitcnt lgkmcnt(3)\n" \
"global_load_lds_dwordx4 v228, s[42:43] offset:-2048\n" \
"v_mfma_f32_16x16x32_bf16 v[172:175], v[24:27], v[216:219], v[172:175]\n" \
"v_add_u32 v230, 0x8000, v230\n" \
"v_add_u32 v231, 0x8000, v231\n" \
"v_mfma_f32_16x16x32_bf16 v[172:175], v[28:31], v[220:223], v[172:175]\n" \
"v_add_u32 v232, 0x4000, v232\n" \
"v_add_u32 v233, 0x4000, v233\n" \
"v_mfma_f32_16x16x32_bf16 v[168:171], v[16:19], v[216:219], v[168:171]\n" \
"v_mfma_f32_16x16x32_bf16 v[168:171], v[20:23], v[220:223], v[168:171]\n" \
"global_load_lds_dwordx4 v229, s[42:43] offset:-1024\n" \
"v_mfma_f32_16x16x32_bf16 v[164:167], v[8:11], v[216:219], v[164:167]\n" \
"s_add_u32 s42, s42, 0x80000\n" \
"s_addc_u32 s43, s43, 0\n" \
"v_mfma_f32_16x16x32_bf16 v[164:167], v[12:15], v[220:223], v[164:167]\n" \
"s_waitcnt vmcnt(18) lgkmcnt(0)\n" \
"v_mfma_f32_16x16x32_bf16 v[160:163], v[0:3], v[216:219], v[160:163]\n" \
"v_mfma_f32_16x16x32_bf16 v[160:163], v[4:7], v[220:223], v[160:163]\n" \
"s_barrier\n" \
"v_mfma_f32_16x16x32_bf16 v[80:83], v[32:35], v[192:195], v[80:83]\n" \
"ds_read_b64_tr_b16 v[0:1], v232 offset:0\n" \
"v_mfma_f32_16x16x32_bf16 v[80:83], v[36:39], v[196:199], v[80:83]\n" \
"ds_read_b64_tr_b16 v[2:3], v232 offset:2048\n" \
"v_mfma_f32_16x16x32_bf16 v[84:87], v[40:43], v[192:195], v[84:87]\n" \
"s_add_u32 m0, s87, 69632\n" \
"ds_read_b64_tr_b16 v[4:5], v232 offset:8192\n" \
"v_mfma_f32_16x16x32_bf16 v[84:87], v[44:47], v[196:199], v[84:87]\n" \
"global_load_lds_dwordx4 v224, s[48:49] offset:-4096\n" \
"v_mfma_f32_16x16x32_bf16 v[88:91], v[48:51], v[192:195], v[88:91]\n" \
"ds_read_b64_tr_b16 v[6:7], v232 offset:10240\n" \
"v_mfma_f32_16x16x32_bf16 v[88:91], v[52:55], v[196:199], v[88:91]\n" \
"ds_read_b64_tr_b16 v[8:9], v233 offset:0\n" \
"v_mfma_f32_16x16x32_bf16 v[92:95], v[56:59], v[192:195], v[92:95]\n" \
"ds_read_b64_tr_b16 v[10:11], v233 offset:2048\n" \
"v_mfma_f32_16x16x32_bf16 v[92:95], v[60:63], v[196:199], v[92:95]\n" \
"global_load_lds_dwordx4 v225, s[48:49] offset:-3072\n" \
"v_mfma_f32_16x16x32_bf16 v[124:127], v[56:59], v[200:203], v[124:127]\n" \
"ds_read_b128 v[192:195], v230 offset:0\n" \
"v_mfma_f32_16x16x32_bf16 v[124:127], v[60:63], v[204:207], v[124:127]\n" \
"ds_read_b128 v[196:199], v231 offset:0\n" \
"v_mfma_f32_16x16x32_bf16 v[120:123], v[48:51], v[200:203], v[120:123]\n" \
"ds_read_b64_tr_b16 v[12:13], v233 offset:8192\n" \
"v_mfma_f32_16x16x32_bf16 v[120:123], v[52:55], v[204:207], v[120:123]\n" \
"global_load_lds_dwordx4 v226, s[48:49] offset:-2048\n" \
"v_mfma_f32_16x16x32_bf16 v[116:119], v[40:43], v[200:203], v[116:119]\n" \
"ds_read_b64_tr_b16 v[14:15], v233 offset:10240\n" \
"v_mfma_f32_16x16x32_bf16 v[116:119], v[44:47], v[204:207], v[116:119]\n" \
"ds_read_b64_tr_b16 v[16:17], v232 offset:512\n" \
"v_mfma_f32_16x16x32_bf16 v[112:115], v[32:35], v[200:203], v[112:115]\n" \
"ds_read_b64_tr_b16 v[18:19], v232 offset:2560\n" \
"v_mfma_f32_16x16x32_bf16 v[112:115], v[36:39], v[204:207], v[112:115]\n" \
"global_load_lds_dwordx4 v227, s[48:49] offset:-1024\n" \
"v_mfma_f32_16x16x32_bf16 v[144:147], v[32:35], v[208:211], v[144:147]\n" \
"s_add_u32 s48, s48, 0x80\n" \
"s_addc_u32 s49, s49, 0\n" \
"ds_read_b128 v[200:203], v230 offset:2048\n" \
"v_mfma_f32_16x16x32_bf16 v[144:147], v[36:39], v[212:215], v[144:147]\n" \
"ds_read_b128 v[204:207], v231 offset:2048\n" \
"v_mfma_f32_16x16x32_bf16 v[148:151], v[40:43], v[208:211], v[148:151]\n" \
"ds_read_b64_tr_b16 v[20:21], v232 offset:8704\n" \
"v_mfma_f32_16x16x32_bf16 v[148:151], v[44:47], v[212:215], v[148:151]\n" \
"global_load_lds_dwordx4 v224, s[46:47] offset:0\n" \
"v_mfma_f32_16x16x32_bf16 v[152:155], v[48:51], v[208:211], v[152:155]\n" \
"ds_read_b64_tr_b16 v[22:23], v232 offset:10752\n" \
"v_mfma_f32_16x16x32_bf16 v[152:155], v[52:55], v[212:215], v[152:155]\n" \
"ds_read_b64_tr_b16 v[24:25], v233 offset:512\n" \
"v_mfma_f32_16x16x32_bf16 v[156:159], v[56:59], v[208:211], v[156:159]\n" \
"ds_read_b64_tr_b16 v[26:27], v233 offset:2560\n" \
"v_mfma_f32_16x16x32_bf16 v[156:159], v[60:63], v[212:215], v[156:159]\n" \
"global_load_lds_dwordx4 v225, s[46:47] offset:1024\n" \
"v_mfma_f32_16x16x32_bf16 v[188:191], v[56:59], v[216:219], v[188:191]\n" \
"ds_read_b128 v[208:211], v230 offset:4096\n" \
"v_mfma_f32_16x16x32_bf16 v[188:191], v[60:63], v[220:223], v[188:191]\n" \
"ds_read_b128 v[212:215], v231 offset:4096\n" \
"v_mfma_f32_16x16x32_bf16 v[184:187], v[48:51], v[216:219], v[184:187]\n" \
"ds_read_b64_tr_b16 v[28:29], v233 offset:8704\n" \
"v_mfma_f32_16x16x32_bf16 v[184:187], v[52:55], v[220:223], v[184:187]\n" \
"global_load_lds_dwordx4 v226, s[46:47] offset:2048\n" \
"v_mfma_f32_16x16x32_bf16 v[180:183], v[40:43], v[216:219], v[180:183]\n" \
"ds_read_b64_tr_b16 v[30:31], v233 offset:10752\n" \
"v_mfma_f32_16x16x32_bf16 v[180:183], v[44:47], v[220:223], v[180:183]\n" \
"v_mfma_f32_16x16x32_bf16 v[176:179], v[32:35], v[216:219], v[176:179]\n" \
"v_mfma_f32_16x16x32_bf16 v[176:179], v[36:39], v[220:223], v[176:179]\n" \
"s_waitcnt lgkmcnt(1)\n" \
"global_load_lds_dwordx4 v227, s[46:47] offset:3072\n" \
"v_mfma_f32_16x16x32_bf16 v[64:67], v[0:3], v[192:195], v[64:67]\n" \
"s_add_u32 s46, s46, 0x80\n" \
"s_addc_u32 s47, s47, 0\n" \
"ds_read_b128 v[216:219], v230 offset:6144\n" \
"v_mfma_f32_16x16x32_bf16 v[64:67], v[4:7], v[196:199], v[64:67]\n" \
"s_mul_i32 s77, s62, 557824\n" \
"ds_read_b128 v[220:223], v231 offset:6144\n" \
"s_add_u32 s77, s77, s95\n" \
"v_mfma_f32_16x16x32_bf16 v[68:71], v[8:11], v[192:195], v[68:71]\n" \
"s_add_u32 s36, s28, s77\n" \
"s_mov_b32 s37, s29\n" \
"ds_read_b64_tr_b16 v[32:33], v232 offset:1024\n" \
"v_mfma_f32_16x16x32_bf16 v[68:71], v[12:15], v[196:199], v[68:71]\n" \
"buffer_store_dwordx4 a[152:155], v248, s[56:59], s90 offen offset:2048 sc1\n" \
"s_add_u32 s77, s77, 17850368\n" \
"s_add_u32 s38, s28, s77\n" \
"s_mov_b32 s39, s29\n" \
"s_and_b32 s74, s63, 1\n" \
"v_mfma_f32_16x16x32_bf16 v[72:75], v[16:19], v[192:195], v[72:75]\n" \
"s_lshl_b32 s74, s74, 15\n" \
"s_xor_b32 s74, s74, 0x18000\n" \
"ds_read_b64_tr_b16 v[34:35], v232 offset:3072\n" \
"v_mfma_f32_16x16x32_bf16 v[72:75], v[20:23], v[196:199], v[72:75]\n" \
"s_waitcnt lgkmcnt(3)\n" \
"ds_read_b64_tr_b16 v[36:37], v232 offset:9216\n" \
"v_add_u32 v247, s74, v248\n" \
"v_mfma_f32_16x16x32_bf16 v[76:79], v[24:27], v[192:195], v[76:79]\n" \
"s_add_u32 s74, s74, s95\n" \
"s_lshr_b32 s83, s61, 1\n" \
"ds_read_b64_tr_b16 v[38:39], v232 offset:11264\n" \
"v_mfma_f32_16x16x32_bf16 v[76:79], v[28:31], v[196:199], v[76:79]\n" \
"buffer_store_dwordx4 a[156:159], v248, s[56:59], s90 offen offset:3072 sc1\n" \
"s_lshl_b32 s75, s63, 2\n" \
"s_add_u32 s77, s83, s75\n" \
"s_mul_i32 s77, s77, 266240\n" \
"s_sub_u32 s78, s83, s75\n" \
"v_mfma_f32_16x16x32_bf16 v[108:111], v[24:27], v[200:203], v[108:111]\n" \
"s_add_u32 s78, s78, 60\n" \
"s_mul_i32 s78, s78, 266240\n" \
"ds_read_b64_tr_b16 v[40:41], v233 offset:1024\n" \
"v_mfma_f32_16x16x32_bf16 v[108:111], v[28:31], v[204:207], v[108:111]\n" \
"s_and_b32 s82, s61, 1\n" \
"ds_read_b64_tr_b16 v[42:43], v233 offset:3072\n" \
"v_mfma_f32_16x16x32_bf16 v[104:107], v[16:19], v[200:203], v[104:107]\n" \
"s_cmp_eq_u32 s82, 0\n" \
"s_cselect_b32 s16, 0, 0xff00\n" \
"ds_read_b64_tr_b16 v[44:45], v233 offset:9216\n" \
"v_mfma_f32_16x16x32_bf16 v[104:107], v[20:23], v[204:207], v[104:107]\n" \
"buffer_load_dword v244, v245, s[56:59], s91 offen sc1\n" \
"v_mfma_f32_16x16x32_bf16 v[100:103], v[8:11], v[200:203], v[100:103]\n" \
"s_mov_b32 s17, s16\n" \
"s_lshl_b32 s75, s82, 8\n" \
"ds_read_b64_tr_b16 v[46:47], v233 offset:11264\n" \
"v_mfma_f32_16x16x32_bf16 v[100:103], v[12:15], v[204:207], v[100:103]\n" \
"s_lshl_b32 s76, s62, 13\n" \
"s_add_u32 s75, s75, s76\n" \
"ds_read_b64_tr_b16 v[48:49], v232 offset:1536\n" \
"v_mfma_f32_16x16x32_bf16 v[96:99], v[0:3], v[200:203], v[96:99]\n" \
"s_add_u32 m0, s95, 36864\n" \
"s_add_u32 s75, s75, 35700736\n" \
"ds_read_b64_tr_b16 v[50:51], v232 offset:3584\n" \
"v_mfma_f32_16x16x32_bf16 v[96:99], v[4:7], v[204:207], v[96:99]\n" \
"s_add_u32 s77, s77, s75\n" \
"global_load_lds_dwordx4 v228, s[44:45] offset:-4096\n" \
"v_mfma_f32_16x16x32_bf16 v[128:131], v[0:3], v[208:211], v[128:131]\n" \
"s_add_u32 s78, s78, s75\n" \
"s_mul_i32 s83, s83, 9216\n" \
"ds_read_b64_tr_b16 v[52:53], v232 offset:9728\n" \
"v_mfma_f32_16x16x32_bf16 v[128:131], v[4:7], v[212:215], v[128:131]\n" \
"s_lshl_b32 s75, s82, 12\n" \
"s_add_u32 s83, s83, s75\n" \
"ds_read_b64_tr_b16 v[54:55], v232 offset:11776\n" \
"v_mfma_f32_16x16x32_bf16 v[132:135], v[8:11], v[208:211], v[132:135]\n" \
"s_add_u32 s83, s83, 4096\n" \
"s_lshl_b32 s82, s82, 14\n" \
"ds_read_b64_tr_b16 v[56:57], v233 offset:1536\n" \
"v_mfma_f32_16x16x32_bf16 v[132:135], v[12:15], v[212:215], v[132:135]\n" \
"global_load_lds_dwordx4 v229, s[44:45] offset:-3072\n" \
"v_mfma_f32_16x16x32_bf16 v[136:139], v[16:19], v[208:211], v[136:139]\n" \
"s_add_u32 s44, s44, 0x80000\n" \
"s_addc_u32 s45, s45, 0\n" \
"ds_read_b64_tr_b16 v[58:59], v233 offset:3584\n" \
"v_mfma_f32_16x16x32_bf16 v[136:139], v[20:23], v[212:215], v[136:139]\n" \
"s_cmp_eq_u32 s63, 7\n" \
"s_cselect_b32 s82, 0, s82\n" \
"ds_read_b64_tr_b16 v[60:61], v233 offset:9728\n" \
"v_mfma_f32_16x16x32_bf16 v[140:143], v[24:27], v[208:211], v[140:143]\n" \
"s_mov_b32 s75, s63\n" \
"s_sub_u32 s76, 7, s63\n" \
"ds_read_b64_tr_b16 v[62:63], v233 offset:11776\n" \
"v_mfma_f32_16x16x32_bf16 v[140:143], v[28:31], v[212:215], v[140:143]\n" \
"s_waitcnt lgkmcnt(3)\n" \
"global_load_lds_dwordx4 v228, s[42:43] offset:-2048\n" \
"s_mov_b32 s73, s95\n" \
"v_mfma_f32_16x16x32_bf16 v[172:175], v[24:27], v[216:219], v[172:175]\n" \
"s_movk_i32 s94, 0x207\n" \
"s_mov_b64 s[40:41], s[14:15]\n" \
"s_mov_b32 s80, 0\n" \
"v_mfma_f32_16x16x32_bf16 v[172:175], v[28:31], v[220:223], v[172:175]\n" \
"s_mov_b32 s81, 0\n" \
"v_add_u32 v230, 0x8000, v230\n" \
"v_add_u32 v231, 0x8000, v231\n" \
"v_mfma_f32_16x16x32_bf16 v[168:171], v[16:19], v[216:219], v[168:171]\n" \
"s_cmp_eq_u32 s63, 7\n" \
"s_cselect_b64 s[40:41], s[18:19], s[40:41]\n" \
"s_cselect_b32 s80, 12, s80\n" \
"v_mfma_f32_16x16x32_bf16 v[168:171], v[20:23], v[220:223], v[168:171]\n" \
"global_load_lds_dwordx4 v229, s[42:43] offset:-1024\n" \
"v_add_u32 v232, 0xffff4000, v232\n" \
"v_mfma_f32_16x16x32_bf16 v[164:167], v[8:11], v[216:219], v[164:167]\n" \
"s_add_u32 s42, s42, 0x80000\n" \
"s_addc_u32 s43, s43, 0\n" \
"v_add_u32 v233, 0xffff4000, v233\n" \
"v_mfma_f32_16x16x32_bf16 v[164:167], v[12:15], v[220:223], v[164:167]\n" \
"s_waitcnt vmcnt(20) lgkmcnt(0)\n" \
"v_mfma_f32_16x16x32_bf16 v[160:163], v[0:3], v[216:219], v[160:163]\n" \
"s_cmp_eq_u32 s63, 6\n" \
"s_cselect_b64 s[40:41], s[22:23], s[40:41]\n" \
"s_cselect_b32 s80, 12, s80\n" \
"v_mfma_f32_16x16x32_bf16 v[160:163], v[4:7], v[220:223], v[160:163]\n" \
"s_barrier\n" \
"v_mfma_f32_16x16x32_bf16 v[80:83], v[32:35], v[192:195], v[80:83]\n" \
"ds_read_b64_tr_b16 v[0:1], v232 offset:0\n" \
"v_mfma_f32_16x16x32_bf16 v[80:83], v[36:39], v[196:199], v[80:83]\n" \
"ds_read_b64_tr_b16 v[2:3], v232 offset:2048\n" \
"v_mfma_f32_16x16x32_bf16 v[84:87], v[40:43], v[192:195], v[84:87]\n" \
"s_add_u32 m0, s87, 102400\n" \
"ds_read_b64_tr_b16 v[4:5], v232 offset:8192\n" \
"v_mfma_f32_16x16x32_bf16 v[84:87], v[44:47], v[196:199], v[84:87]\n" \
"global_load_lds_dwordx4 v224, s[48:49] offset:-4096\n" \
"v_mfma_f32_16x16x32_bf16 v[88:91], v[48:51], v[192:195], v[88:91]\n" \
"ds_read_b64_tr_b16 v[6:7], v232 offset:10240\n" \
"v_mfma_f32_16x16x32_bf16 v[88:91], v[52:55], v[196:199], v[88:91]\n" \
"ds_read_b64_tr_b16 v[8:9], v233 offset:0\n" \
"v_mfma_f32_16x16x32_bf16 v[92:95], v[56:59], v[192:195], v[92:95]\n" \
"ds_read_b64_tr_b16 v[10:11], v233 offset:2048\n" \
"v_mfma_f32_16x16x32_bf16 v[92:95], v[60:63], v[196:199], v[92:95]\n" \
"global_load_lds_dwordx4 v225, s[48:49] offset:-3072\n" \
"v_mfma_f32_16x16x32_bf16 v[124:127], v[56:59], v[200:203], v[124:127]\n" \
"ds_read_b128 v[192:195], v230 offset:0\n" \
"v_mfma_f32_16x16x32_bf16 v[124:127], v[60:63], v[204:207], v[124:127]\n" \
"ds_read_b128 v[196:199], v231 offset:0\n" \
"v_mfma_f32_16x16x32_bf16 v[120:123], v[48:51], v[200:203], v[120:123]\n" \
"ds_read_b64_tr_b16 v[12:13], v233 offset:8192\n" \
"v_mfma_f32_16x16x32_bf16 v[120:123], v[52:55], v[204:207], v[120:123]\n" \
"global_load_lds_dwordx4 v226, s[48:49] offset:-2048\n" \
"v_mfma_f32_16x16x32_bf16 v[116:119], v[40:43], v[200:203], v[116:119]\n" \
"ds_read_b64_tr_b16 v[14:15], v233 offset:10240\n" \
"v_mfma_f32_16x16x32_bf16 v[116:119], v[44:47], v[204:207], v[116:119]\n" \
"ds_read_b64_tr_b16 v[16:17], v232 offset:512\n" \
"v_mfma_f32_16x16x32_bf16 v[112:115], v[32:35], v[200:203], v[112:115]\n" \
"ds_read_b64_tr_b16 v[18:19], v232 offset:2560\n" \
"v_mfma_f32_16x16x32_bf16 v[112:115], v[36:39], v[204:207], v[112:115]\n" \
"global_load_lds_dwordx4 v227, s[48:49] offset:-1024\n" \
"v_mfma_f32_16x16x32_bf16 v[144:147], v[32:35], v[208:211], v[144:147]\n" \
"s_add_u32 s48, s48, 0x80\n" \
"s_addc_u32 s49, s49, 0\n" \
"ds_read_b128 v[200:203], v230 offset:2048\n" \
"v_mfma_f32_16x16x32_bf16 v[144:147], v[36:39], v[212:215], v[144:147]\n" \
"ds_read_b128 v[204:207], v231 offset:2048\n" \
"v_mfma_f32_16x16x32_bf16 v[148:151], v[40:43], v[208:211], v[148:151]\n" \
"ds_read_b64_tr_b16 v[20:21], v232 offset:8704\n" \
"v_mfma_f32_16x16x32_bf16 v[148:151], v[44:47], v[212:215], v[148:151]\n" \
"global_load_lds_dwordx4 v224, s[46:47] offset:0\n" \
"v_mfma_f32_16x16x32_bf16 v[152:155], v[48:51], v[208:211], v[152:155]\n" \
"ds_read_b64_tr_b16 v[22:23], v232 offset:10752\n" \
"v_mfma_f32_16x16x32_bf16 v[152:155], v[52:55], v[212:215], v[152:155]\n" \
"ds_read_b64_tr_b16 v[24:25], v233 offset:512\n" \
"v_mfma_f32_16x16x32_bf16 v[156:159], v[56:59], v[208:211], v[156:159]\n" \
"ds_read_b64_tr_b16 v[26:27], v233 offset:2560\n" \
"v_mfma_f32_16x16x32_bf16 v[156:159], v[60:63], v[212:215], v[156:159]\n" \
"global_load_lds_dwordx4 v225, s[46:47] offset:1024\n" \
"v_mfma_f32_16x16x32_bf16 v[188:191], v[56:59], v[216:219], v[188:191]\n" \
"ds_read_b128 v[208:211], v230 offset:4096\n" \
"v_mfma_f32_16x16x32_bf16 v[188:191], v[60:63], v[220:223], v[188:191]\n" \
"Lpg4399_%=:\n" \
"s_cbranch_execz Lpg5578_%=\n" \
"ds_read_b128 v[212:215], v231 offset:4096\n" \
"v_mfma_f32_16x16x32_bf16 v[184:187], v[48:51], v[216:219], v[184:187]\n" \
"ds_read_b64_tr_b16 v[28:29], v233 offset:8704\n" \
"v_mfma_f32_16x16x32_bf16 v[184:187], v[52:55], v[220:223], v[184:187]\n" \
"global_load_lds_dwordx4 v226, s[46:47] offset:2048\n" \
"v_mfma_f32_16x16x32_bf16 v[180:183], v[40:43], v[216:219], v[180:183]\n" \
"ds_read_b64_tr_b16 v[30:31], v233 offset:10752\n" \
"v_mfma_f32_16x16x32_bf16 v[180:183], v[44:47], v[220:223], v[180:183]\n" \
"s_cmp_eq_u32 s63, 5\n" \
"s_cselect_b64 s[40:41], s[18:19], s[40:41]\n" \
"s_cselect_b32 s80, 4, s80\n" \
"v_mfma_f32_16x16x32_bf16 v[176:179], v[32:35], v[216:219], v[176:179]\n" \
"s_cmp_eq_u32 s63, 5\n" \
"s_cselect_b32 s81, 384, s81\n" \
"v_mfma_f32_16x16x32_bf16 v[176:179], v[36:39], v[220:223], v[176:179]\n" \
"s_waitcnt lgkmcnt(1)\n" \
"global_load_lds_dwordx4 v227, s[46:47] offset:3072\n" \
"v_mfma_f32_16x16x32_bf16 v[64:67], v[0:3], v[192:195], v[64:67]\n" \
"s_add_u32 s46, s46, 0x80\n" \
"s_addc_u32 s47, s47, 0\n" \
"ds_read_b128 v[216:219], v230 offset:6144\n" \
"v_mfma_f32_16x16x32_bf16 v[64:67], v[4:7], v[196:199], v[64:67]\n" \
"ds_read_b128 v[220:223], v231 offset:6144\n" \
"s_add_u32 s90, s88, 278528\n" \
"v_mfma_f32_16x16x32_bf16 v[68:71], v[8:11], v[192:195], v[68:71]\n" \
"ds_read_b64_tr_b16 v[32:33], v232 offset:1024\n" \
"v_mfma_f32_16x16x32_bf16 v[68:71], v[12:15], v[196:199], v[68:71]\n" \
"buffer_store_dwordx4 a[64:67], v249, s[56:59], s90 offen offset:0 sc1\n" \
"s_cmp_eq_u32 s63, 4\n" \
"s_cselect_b64 s[40:41], s[20:21], s[40:41]\n" \
"s_cselect_b32 s80, 8, s80\n" \
"v_mfma_f32_16x16x32_bf16 v[72:75], v[16:19], v[192:195], v[72:75]\n" \
"ds_read_b64_tr_b16 v[34:35], v232 offset:3072\n" \
"v_mfma_f32_16x16x32_bf16 v[72:75], v[20:23], v[196:199], v[72:75]\n" \
"s_waitcnt lgkmcnt(3)\n" \
"ds_read_b64_tr_b16 v[36:37], v232 offset:9216\n" \
"v_mfma_f32_16x16x32_bf16 v[76:79], v[24:27], v[192:195], v[76:79]\n" \
"ds_read_b128 a[64:67], v254\n" \
"ds_read_b64_tr_b16 v[38:39], v232 offset:11264\n" \
"v_mfma_f32_16x16x32_bf16 v[76:79], v[28:31], v[196:199], v[76:79]\n" \
"s_cmp_eq_u32 s63, 2\n" \
"s_cselect_b64 s[40:41], s[24:25], s[40:41]\n" \
"s_cselect_b32 s80, 4, s80\n" \
"v_mfma_f32_16x16x32_bf16 v[108:111], v[24:27], v[200:203], v[108:111]\n" \
"s_add_u32 s81, s81, s62\n" \
"s_lshl_b32 s81, s81, 2\n" \
"ds_read_b64_tr_b16 v[40:41], v233 offset:1024\n" \
"v_mfma_f32_16x16x32_bf16 v[108:111], v[28:31], v[204:207], v[108:111]\n" \
"s_add_u32 s81, s81, s61\n" \
"s_lshl_b32 s81, s81, 12\n" \
"ds_read_b64_tr_b16 v[42:43], v233 offset:3072\n" \
"v_mfma_f32_16x16x32_bf16 v[104:107], v[16:19], v[200:203], v[104:107]\n" \
"s_add_u32 s40, s40, s81\n" \
"s_addc_u32 s41, s41, 0\n" \
"ds_read_b64_tr_b16 v[44:45], v233 offset:9216\n" \
"v_mfma_f32_16x16x32_bf16 v[104:107], v[20:23], v[204:207], v[104:107]\n" \
"s_min_u32 s81, s80, 1\n" \
"s_lshl_b32 s81, s81, 2\n" \
"v_mul_u32_u24 v252, s81, v248\n" \
"v_mfma_f32_16x16x32_bf16 v[100:103], v[8:11], v[200:203], v[100:103]\n" \
"ds_read_b128 a[68:71], v254\n" \
"ds_read_b64_tr_b16 v[46:47], v233 offset:11264\n" \
"v_mfma_f32_16x16x32_bf16 v[100:103], v[12:15], v[204:207], v[100:103]\n" \
"ds_read_b64_tr_b16 v[48:49], v232 offset:1536\n" \
"v_mfma_f32_16x16x32_bf16 v[96:99], v[0:3], v[200:203], v[96:99]\n" \
"s_add_u32 m0, s95, 53248\n" \
"ds_read_b64_tr_b16 v[50:51], v232 offset:3584\n" \
"v_mfma_f32_16x16x32_bf16 v[96:99], v[4:7], v[204:207], v[96:99]\n" \
"global_load_lds_dwordx4 v228, s[44:45] offset:-4096\n" \
"v_mfma_f32_16x16x32_bf16 v[128:131], v[0:3], v[208:211], v[128:131]\n" \
"ds_read_b64_tr_b16 v[52:53], v232 offset:9728\n" \
"v_mfma_f32_16x16x32_bf16 v[128:131], v[4:7], v[212:215], v[128:131]\n" \
"ds_read_b64_tr_b16 v[54:55], v232 offset:11776\n" \
"v_mfma_f32_16x16x32_bf16 v[132:135], v[8:11], v[208:211], v[132:135]\n" \
"ds_read_b64_tr_b16 v[56:57], v233 offset:1536\n" \
"v_mfma_f32_16x16x32_bf16 v[132:135], v[12:15], v[212:215], v[132:135]\n" \
"global_load_lds_dwordx4 v229, s[44:45] offset:-3072\n" \
"v_mfma_f32_16x16x32_bf16 v[136:139], v[16:19], v[208:211], v[136:139]\n" \
"ds_read_b64_tr_b16 v[58:59], v233 offset:3584\n" \
"v_mfma_f32_16x16x32_bf16 v[136:139], v[20:23], v[212:215], v[136:139]\n" \
"ds_read_b64_tr_b16 v[60:61], v233 offset:9728\n" \
"v_mfma_f32_16x16x32_bf16 v[140:143], v[24:27], v[208:211], v[140:143]\n" \
"ds_read_b64_tr_b16 v[62:63], v233 offset:11776\n" \
"v_mfma_f32_16x16x32_bf16 v[140:143], v[28:31], v[212:215], v[140:143]\n" \
"s_waitcnt lgkmcnt(3)\n" \
"global_load_lds_dwordx4 v228, s[42:43] offset:-2048\n" \
"v_mfma_f32_16x16x32_bf16 v[172:175], v[24:27], v[216:219], v[172:175]\n" \
"v_add_u32 v230, 0xffff0000, v230\n" \
"v_add_u32 v231, 0xffff0000, v231\n" \
"v_mfma_f32_16x16x32_bf16 v[172:175], v[28:31], v[220:223], v[172:175]\n" \
"v_add_u32 v232, 0x4000, v232\n" \
"v_add_u32 v233, 0x4000, v233\n" \
"v_mfma_f32_16x16x32_bf16 v[168:171], v[16:19], v[216:219], v[168:171]\n" \
"v_mfma_f32_16x16x32_bf16 v[168:171], v[20:23], v[220:223], v[168:171]\n" \
"global_load_lds_dwordx4 v229, s[42:43] offset:-1024\n" \
"v_mfma_f32_16x16x32_bf16 v[164:167], v[8:11], v[216:219], v[164:167]\n" \
"v_mfma_f32_16x16x32_bf16 v[164:167], v[12:15], v[220:223], v[164:167]\n" \
"s_waitcnt vmcnt(20) lgkmcnt(0)\n" \
"v_mfma_f32_16x16x32_bf16 v[160:163], v[0:3], v[216:219], v[160:163]\n" \
"v_mfma_f32_16x16x32_bf16 v[160:163], v[4:7], v[220:223], v[160:163]\n" \
"s_barrier\n" \
"v_mfma_f32_16x16x32_bf16 v[80:83], v[32:35], v[192:195], v[80:83]\n" \
"s_waitcnt vmcnt(17)\n" \
"ds_read_b64_tr_b16 v[0:1], v232 offset:0\n" \
"v_cmp_ne_u32 vcc, s60, v244\n" \
"v_mfma_f32_16x16x32_bf16 v[80:83], v[36:39], v[196:199], v[80:83]\n" \
"ds_read_b64_tr_b16 v[2:3], v232 offset:2048\n" \
"v_mfma_f32_16x16x32_bf16 v[84:87], v[40:43], v[192:195], v[84:87]\n" \
"s_add_u32 m0, s87, 135168\n" \
"ds_read_b64_tr_b16 v[4:5], v232 offset:8192\n" \
"v_mfma_f32_16x16x32_bf16 v[84:87], v[44:47], v[196:199], v[84:87]\n" \
"s_and_b32 vcc_hi, vcc_lo, 255\n" \
"s_cbranch_scc1 Lpl1_%=\n" \
"Lpr1_%=:\n" \
"global_load_lds_dwordx4 v224, s[48:49] offset:-4096\n" \
"v_mfma_f32_16x16x32_bf16 v[88:91], v[48:51], v[192:195], v[88:91]\n" \
"s_add_u32 s79, s91, 12288\n" \
"ds_read_b64_tr_b16 v[6:7], v232 offset:10240\n" \
"v_mfma_f32_16x16x32_bf16 v[88:91], v[52:55], v[196:199], v[88:91]\n" \
"ds_read_b64_tr_b16 v[8:9], v233 offset:0\n" \
"v_mfma_f32_16x16x32_bf16 v[92:95], v[56:59], v[192:195], v[92:95]\n" \
"ds_read_b64_tr_b16 v[10:11], v233 offset:2048\n" \
"v_mfma_f32_16x16x32_bf16 v[92:95], v[60:63], v[196:199], v[92:95]\n" \
"global_load_lds_dwordx4 v225, s[48:49] offset:-3072\n" \
"v_mfma_f32_16x16x32_bf16 v[124:127], v[56:59], v[200:203], v[124:127]\n" \
"ds_read_b128 v[192:195], v230 offset:0\n" \
"v_mfma_f32_16x16x32_bf16 v[124:127], v[60:63], v[204:207], v[124:127]\n" \
"ds_read_b128 v[196:199], v231 offset:0\n" \
"v_mfma_f32_16x16x32_bf16 v[120:123], v[48:51], v[200:203], v[120:123]\n" \
"ds_read_b64_tr_b16 v[12:13], v233 offset:8192\n" \
"v_mfma_f32_16x16x32_bf16 v[120:123], v[52:55], v[204:207], v[120:123]\n" \
"global_load_lds_dwordx4 v226, s[48:49] offset:-2048\n" \
"v_mfma_f32_16x16x32_bf16 v[116:119], v[40:43], v[200:203], v[116:119]\n" \
"ds_read_b64_tr_b16 v[14:15], v233 offset:10240\n" \
"v_mfma_f32_16x16x32_bf16 v[116:119], v[44:47], v[204:207], v[116:119]\n" \
"ds_read_b64_tr_b16 v[16:17], v232 offset:512\n" \
"v_mfma_f32_16x16x32_bf16 v[112:115], v[32:35], v[200:203], v[112:115]\n" \
"ds_read_b64_tr_b16 v[18:19], v232 offset:2560\n" \
"v_mfma_f32_16x16x32_bf16 v[112:115], v[36:39], v[204:207], v[112:115]\n" \
"global_load_lds_dwordx4 v227, s[48:49] offset:-1024\n" \
"v_mfma_f32_16x16x32_bf16 v[144:147], v[32:35], v[208:211], v[144:147]\n" \
"ds_read_b128 v[200:203], v230 offset:2048\n" \
"v_mfma_f32_16x16x32_bf16 v[144:147], v[36:39], v[212:215], v[144:147]\n" \
"ds_read_b128 v[204:207], v231 offset:2048\n" \
"v_mfma_f32_16x16x32_bf16 v[148:151], v[40:43], v[208:211], v[148:151]\n" \
"ds_read_b64_tr_b16 v[20:21], v232 offset:8704\n" \
"v_mfma_f32_16x16x32_bf16 v[148:151], v[44:47], v[212:215], v[148:151]\n" \
"global_load_lds_dwordx4 v224, s[46:47] offset:0\n" \
"v_mfma_f32_16x16x32_bf16 v[152:155], v[48:51], v[208:211], v[152:155]\n" \
"ds_read_b64_tr_b16 v[22:23], v232 offset:10752\n" \
"v_mfma_f32_16x16x32_bf16 v[152:155], v[52:55], v[212:215], v[152:155]\n" \
"ds_read_b64_tr_b16 v[24:25], v233 offset:512\n" \
"v_mfma_f32_16x16x32_bf16 v[156:159], v[56:59], v[208:211], v[156:159]\n" \
"ds_read_b64_tr_b16 v[26:27], v233 offset:2560\n" \
"v_mfma_f32_16x16x32_bf16 v[156:159], v[60:63], v[212:215], v[156:159]\n" \
"global_load_lds_dwordx4 v225, s[46:47] offset:1024\n" \
"v_mfma_f32_16x16x32_bf16 v[188:191], v[56:59], v[216:219], v[188:191]\n" \
"ds_read_b128 v[208:211], v230 offset:4096\n" \
"v_mfma_f32_16x16x32_bf16 v[188:191], v[60:63], v[220:223], v[188:191]\n" \
"ds_read_b128 v[212:215], v231 offset:4096\n" \
"v_mfma_f32_16x16x32_bf16 v[184:187], v[48:51], v[216:219], v[184:187]\n" \
"ds_read_b64_tr_b16 v[28:29], v233 offset:8704\n" \
"v_mfma_f32_16x16x32_bf16 v[184:187], v[52:55], v[220:223], v[184:187]\n" \
"global_load_lds_dwordx4 v226, s[46:47] offset:2048\n" \
"v_mfma_f32_16x16x32_bf16 v[180:183], v[40:43], v[216:219], v[180:183]\n" \
"ds_read_b64_tr_b16 v[30:31], v233 offset:10752\n" \
"v_mfma_f32_16x16x32_bf16 v[180:183], v[44:47], v[220:223], v[180:183]\n" \
"v_mfma_f32_16x16x32_bf16 v[176:179], v[32:35], v[216:219], v[176:179]\n" \
"v_mfma_f32_16x16x32_bf16 v[176:179], v[36:39], v[220:223], v[176:179]\n" \
"s_waitcnt lgkmcnt(1)\n" \
"global_load_lds_dwordx4 v227, s[46:47] offset:3072\n" \
"v_mfma_f32_16x16x32_bf16 v[64:67], v[0:3], v[192:195], v[64:67]\n" \
"ds_read_b128 v[216:219], v230 offset:6144\n" \
"v_mfma_f32_16x16x32_bf16 v[64:67], v[4:7], v[196:199], v[64:67]\n" \
"ds_read_b128 v[220:223], v231 offset:6144\n" \
"v_mfma_f32_16x16x32_bf16 v[68:71], v[8:11], v[192:195], v[68:71]\n" \
"ds_read_b128 v[240:243], v254\n" \
"ds_read_b64_tr_b16 v[32:33], v232 offset:1024\n" \
"v_mfma_f32_16x16x32_bf16 v[68:71], v[12:15], v[196:199], v[68:71]\n" \
"buffer_store_dwordx4 a[96:99], v249, s[56:59], s90 offen offset:256 sc1\n" \
"v_and_b32 v238, 0xf0, v248\n" \
"v_mov_b32 v239, 0x3f803f80\n" \
"v_cmp_eq_u32 vcc, 32, v238\n" \
"v_cmp_eq_u32 s[64:65], 48, v238\n" \
"v_mfma_f32_16x16x32_bf16 v[72:75], v[16:19], v[192:195], v[72:75]\n" \
"ds_read_b64_tr_b16 v[34:35], v232 offset:3072\n" \
"v_mov_b32 v244, 0\n" \
"v_mfma_f32_16x16x32_bf16 v[72:75], v[20:23], v[196:199], v[72:75]\n" \
"s_waitcnt lgkmcnt(4)\n" \
"ds_read_b64_tr_b16 v[36:37], v232 offset:9216\n" \
"v_mov_b32 v245, 0\n" \
"v_mfma_f32_16x16x32_bf16 v[76:79], v[24:27], v[192:195], v[76:79]\n" \
"ds_read_b128 a[96:99], v254\n" \
"ds_read_b64_tr_b16 v[38:39], v232 offset:11264\n" \
"v_mfma_f32_16x16x32_bf16 v[76:79], v[28:31], v[196:199], v[76:79]\n" \
"buffer_store_dwordx4 a[72:75], v249, s[56:59], s90 offen offset:2048 sc1\n" \
"ds_read_b128 a[100:103], v254\n" \
"v_cndmask_b32 v238, 0, v239, vcc\n" \
"v_cndmask_b32 v239, 0, v239, s[64:65]\n" \
"v_mfma_f32_16x16x32_bf16 v[108:111], v[24:27], v[200:203], v[108:111]\n" \
"ds_read_b64_tr_b16 v[40:41], v233 offset:1024\n" \
"v_mfma_f32_16x16x32_bf16 v[108:111], v[28:31], v[204:207], v[108:111]\n" \
"ds_read_b64_tr_b16 v[42:43], v233 offset:3072\n" \
"v_mfma_f32_16x16x32_bf16 v[104:107], v[16:19], v[200:203], v[104:107]\n" \
"ds_read_b128 a[72:75], v254\n" \
"ds_read_b64_tr_b16 v[44:45], v233 offset:9216\n" \
"v_mfma_f32_16x16x32_bf16 v[104:107], v[20:23], v[204:207], v[104:107]\n" \
"ds_read_b128 a[76:79], v254\n" \
"v_mfma_f32_16x16x32_bf16 v[100:103], v[8:11], v[200:203], v[100:103]\n" \
"ds_read_b64_tr_b16 v[46:47], v233 offset:11264\n" \
"v_mfma_f32_16x16x32_bf16 v[100:103], v[12:15], v[204:207], v[100:103]\n" \
"ds_read_b64_tr_b16 v[48:49], v232 offset:1536\n" \
"v_mfma_f32_16x16x32_bf16 v[96:99], v[0:3], v[200:203], v[96:99]\n" \
"s_add_u32 m0, s95, 0\n" \
"ds_read_b64_tr_b16 v[50:51], v232 offset:3584\n" \
"v_mfma_f32_16x16x32_bf16 v[96:99], v[4:7], v[204:207], v[96:99]\n" \
"global_load_lds_dwordx4 v250, s[36:37] offset:0 sc1\n" \
"v_mfma_f32_16x16x32_bf16 v[128:131], v[0:3], v[208:211], v[128:131]\n" \
"ds_read_b64_tr_b16 v[52:53], v232 offset:9728\n" \
"v_mfma_f32_16x16x32_bf16 v[128:131], v[4:7], v[212:215], v[128:131]\n" \
"ds_read_b64_tr_b16 v[54:55], v232 offset:11776\n" \
"v_mfma_f32_16x16x32_bf16 v[132:135], v[8:11], v[208:211], v[132:135]\n" \
"ds_read_b64_tr_b16 v[56:57], v233 offset:1536\n" \
"v_mfma_f32_16x16x32_bf16 v[132:135], v[12:15], v[212:215], v[132:135]\n" \
"global_load_lds_dwordx4 v250, s[36:37] offset:1024 sc1\n" \
"v_mfma_f32_16x16x32_bf16 v[136:139], v[16:19], v[208:211], v[136:139]\n" \
"ds_read_b64_tr_b16 v[58:59], v233 offset:3584\n" \
"v_mfma_f32_16x16x32_bf16 v[136:139], v[20:23], v[212:215], v[136:139]\n" \
"ds_read_b64_tr_b16 v[60:61], v233 offset:9728\n" \
"v_mfma_f32_16x16x32_bf16 v[140:143], v[24:27], v[208:211], v[140:143]\n" \
"ds_read_b64_tr_b16 v[62:63], v233 offset:11776\n" \
"v_mfma_f32_16x16x32_bf16 v[140:143], v[28:31], v[212:215], v[140:143]\n" \
"s_waitcnt lgkmcnt(3)\n" \
"global_load_lds_dwordx4 v250, s[36:37] offset:2048 sc1\n" \
"v_mfma_f32_16x16x32_bf16 v[172:175], v[24:27], v[216:219], v[172:175]\n" \
"v_add_u32 v230, 0x8000, v230\n" \
"v_add_u32 v231, 0x8000, v231\n" \
"v_mfma_f32_16x16x32_bf16 v[172:175], v[28:31], v[220:223], v[172:175]\n" \
"v_add_u32 v232, 0x4000, v232\n" \
"v_add_u32 v233, 0x4000, v233\n" \
"v_mfma_f32_16x16x32_bf16 v[168:171], v[16:19], v[216:219], v[168:171]\n" \
"v_mfma_f32_16x16x32_bf16 v[168:171], v[20:23], v[220:223], v[168:171]\n" \
"global_load_lds_dwordx4 v250, s[36:37] offset:3072 sc1\n" \
"v_mfma_f32_16x16x32_bf16 v[164:167], v[8:11], v[216:219], v[164:167]\n" \
"s_add_u32 s36, s36, 17408\n" \
"v_mfma_f32_16x16x32_bf16 v[164:167], v[12:15], v[220:223], v[164:167]\n" \
"s_waitcnt vmcnt(19) lgkmcnt(0)\n" \
"v_mfma_f32_16x16x32_bf16 v[160:163], v[0:3], v[216:219], v[160:163]\n" \
"v_mfma_f32_16x16x32_bf16 v[160:163], v[4:7], v[220:223], v[160:163]\n" \
"s_barrier\n" \
"v_mfma_f32_16x16x32_bf16 v[80:83], v[32:35], v[192:195], v[80:83]\n" \
"ds_read_b64_tr_b16 v[0:1], v232 offset:0\n" \
"v_mfma_f32_16x16x32_bf16 v[80:83], v[36:39], v[196:199], v[80:83]\n" \
"ds_read_b64_tr_b16 v[2:3], v232 offset:2048\n" \
"v_mfma_f32_16x16x32_bf16 v[84:87], v[40:43], v[192:195], v[84:87]\n" \
"s_add_u32 m0, s95, 16384\n" \
"ds_read_b64_tr_b16 v[4:5], v232 offset:8192\n" \
"v_mfma_f32_16x16x32_bf16 v[84:87], v[44:47], v[196:199], v[84:87]\n" \
"global_load_lds_dwordx4 v250, s[36:37] offset:0 sc1\n" \
"v_mfma_f32_16x16x32_bf16 v[88:91], v[48:51], v[192:195], v[88:91]\n" \
"ds_read_b64_tr_b16 v[6:7], v232 offset:10240\n" \
"v_mfma_f32_16x16x32_bf16 v[88:91], v[52:55], v[196:199], v[88:91]\n" \
"ds_read_b64_tr_b16 v[8:9], v233 offset:0\n" \
"v_mfma_f32_16x16x32_bf16 v[92:95], v[56:59], v[192:195], v[92:95]\n" \
"ds_read_b64_tr_b16 v[10:11], v233 offset:2048\n" \
"v_mfma_f32_16x16x32_bf16 v[92:95], v[60:63], v[196:199], v[92:95]\n" \
"global_load_lds_dwordx4 v250, s[36:37] offset:1024 sc1\n" \
"v_mfma_f32_16x16x32_bf16 v[124:127], v[56:59], v[200:203], v[124:127]\n" \
"ds_read_b128 v[192:195], v230 offset:0\n" \
"v_mfma_f32_16x16x32_bf16 v[124:127], v[60:63], v[204:207], v[124:127]\n" \
"ds_read_b128 v[196:199], v231 offset:0\n" \
"v_mfma_f32_16x16x32_bf16 v[120:123], v[48:51], v[200:203], v[120:123]\n" \
"ds_read_b64_tr_b16 v[12:13], v233 offset:8192\n" \
"v_mfma_f32_16x16x32_bf16 v[120:123], v[52:55], v[204:207], v[120:123]\n" \
"global_load_lds_dwordx4 v250, s[36:37] offset:2048 sc1\n" \
"v_mfma_f32_16x16x32_bf16 v[116:119], v[40:43], v[200:203], v[116:119]\n" \
"ds_read_b64_tr_b16 v[14:15], v233 offset:10240\n" \
"v_mfma_f32_16x16x32_bf16 v[116:119], v[44:47], v[204:207], v[116:119]\n" \
"ds_read_b64_tr_b16 v[16:17], v232 offset:512\n" \
"v_mfma_f32_16x16x32_bf16 v[112:115], v[32:35], v[200:203], v[112:115]\n" \
"ds_read_b64_tr_b16 v[18:19], v232 offset:2560\n" \
"v_mfma_f32_16x16x32_bf16 v[112:115], v[36:39], v[204:207], v[112:115]\n" \
"global_load_lds_dwordx4 v250, s[36:37] offset:3072 sc1\n" \
"v_mfma_f32_16x16x32_bf16 v[144:147], v[32:35], v[208:211], v[144:147]\n" \
"ds_read_b128 v[200:203], v230 offset:2048\n" \
"s_add_u32 s36, s36, 17408\n" \
"v_mfma_f32_16x16x32_bf16 v[144:147], v[36:39], v[212:215], v[144:147]\n" \
"ds_read_b128 v[204:207], v231 offset:2048\n" \
"v_mfma_f32_16x16x32_bf16 v[148:151], v[40:43], v[208:211], v[148:151]\n" \
"ds_read_b64_tr_b16 v[20:21], v232 offset:8704\n" \
"v_mfma_f32_16x16x32_bf16 v[148:151], v[44:47], v[212:215], v[148:151]\n" \
"buffer_store_dwordx4 a[104:107], v249, s[56:59], s90 offen offset:2304 sc1\n" \
"v_mfma_f32_16x16x32_bf16 v[152:155], v[48:51], v[208:211], v[152:155]\n" \
"s_add_u32 s90, s89, 278528\n" \
"ds_read_b64_tr_b16 v[22:23], v232 offset:10752\n" \
"v_mfma_f32_16x16x32_bf16 v[152:155], v[52:55], v[212:215], v[152:155]\n" \
"ds_read_b64_tr_b16 v[24:25], v233 offset:512\n" \
"v_mfma_f32_16x16x32_bf16 v[156:159], v[56:59], v[208:211], v[156:159]\n" \
"ds_read_b128 a[104:107], v254\n" \
"ds_read_b64_tr_b16 v[26:27], v233 offset:2560\n" \
"v_mfma_f32_16x16x32_bf16 v[156:159], v[60:63], v[212:215], v[156:159]\n" \
"buffer_store_dwordx4 a[80:83], v248, s[56:59], s90 offen offset:0 sc1\n" \
"ds_read_b128 a[108:111], v254\n" \
"v_mfma_f32_16x16x32_bf16 v[188:191], v[56:59], v[216:219], v[188:191]\n" \
"ds_read_b128 v[208:211], v230 offset:4096\n" \
"v_mfma_f32_16x16x32_bf16 v[188:191], v[60:63], v[220:223], v[188:191]\n" \
"ds_read_b128 v[212:215], v231 offset:4096\n" \
"v_mfma_f32_16x16x32_bf16 v[184:187], v[48:51], v[216:219], v[184:187]\n" \
"ds_read_b128 a[80:83], v254\n" \
"ds_read_b64_tr_b16 v[28:29], v233 offset:8704\n" \
"v_mfma_f32_16x16x32_bf16 v[184:187], v[52:55], v[220:223], v[184:187]\n" \
"buffer_store_dwordx4 a[84:87], v248, s[56:59], s90 offen offset:1024 sc1\n" \
"ds_read_b128 a[112:115], v254\n" \
"v_mfma_f32_16x16x32_bf16 v[180:183], v[40:43], v[216:219], v[180:183]\n" \
"ds_read_b64_tr_b16 v[30:31], v233 offset:10752\n" \
"v_mfma_f32_16x16x32_bf16 v[180:183], v[44:47], v[220:223], v[180:183]\n" \
"v_mfma_f32_16x16x32_bf16 v[176:179], v[32:35], v[216:219], v[176:179]\n" \
"ds_read_b128 a[84:87], v254\n" \
"v_mfma_f32_16x16x32_bf16 v[176:179], v[36:39], v[220:223], v[176:179]\n" \
"s_waitcnt lgkmcnt(3)\n" \
"ds_read_b128 a[116:119], v254\n" \
"v_mfma_f32_16x16x32_bf16 v[64:67], v[0:3], v[192:195], v[64:67]\n" \
"ds_read_b128 v[216:219], v230 offset:6144\n" \
"v_mfma_f32_16x16x32_bf16 v[64:67], v[4:7], v[196:199], v[64:67]\n" \
"ds_read_b128 v[220:223], v231 offset:6144\n" \
"v_mfma_f32_16x16x32_bf16 v[68:71], v[8:11], v[192:195], v[68:71]\n" \
"ds_read_b64_tr_b16 v[32:33], v232 offset:1024\n" \
"v_mfma_f32_16x16x32_bf16 v[68:71], v[12:15], v[196:199], v[68:71]\n" \
"v_mfma_f32_16x16x32_bf16 v[72:75], v[16:19], v[192:195], v[72:75]\n" \
"ds_read_b64_tr_b16 v[34:35], v232 offset:3072\n" \
"v_mfma_f32_16x16x32_bf16 v[72:75], v[20:23], v[196:199], v[72:75]\n" \
"s_waitcnt lgkmcnt(3)\n" \
"ds_read_b64_tr_b16 v[36:37], v232 offset:9216\n" \
"v_mfma_f32_16x16x32_bf16 v[76:79], v[24:27], v[192:195], v[76:79]\n" \
"ds_read_b64_tr_b16 v[38:39], v232 offset:11264\n" \
"v_mfma_f32_16x16x32_bf16 v[76:79], v[28:31], v[196:199], v[76:79]\n" \
"v_mfma_f32_16x16x32_bf16 v[108:111], v[24:27], v[200:203], v[108:111]\n" \
"ds_read_b64_tr_b16 v[40:41], v233 offset:1024\n" \
"v_mfma_f32_16x16x32_bf16 v[108:111], v[28:31], v[204:207], v[108:111]\n" \
"ds_read_b64_tr_b16 v[42:43], v233 offset:3072\n" \
"v_mfma_f32_16x16x32_bf16 v[104:107], v[16:19], v[200:203], v[104:107]\n" \
"ds_read_b64_tr_b16 v[44:45], v233 offset:9216\n" \
"v_mfma_f32_16x16x32_bf16 v[104:107], v[20:23], v[204:207], v[104:107]\n" \
"v_mfma_f32_16x16x32_bf16 v[100:103], v[8:11], v[200:203], v[100:103]\n" \
"ds_read_b64_tr_b16 v[46:47], v233 offset:11264\n" \
"v_mfma_f32_16x16x32_bf16 v[100:103], v[12:15], v[204:207], v[100:103]\n" \
"ds_read_b64_tr_b16 v[48:49], v232 offset:1536\n" \
"v_mfma_f32_16x16x32_bf16 v[96:99], v[0:3], v[200:203], v[96:99]\n" \
"ds_read_b64_tr_b16 v[50:51], v232 offset:3584\n" \
"v_mfma_f32_16x16x32_bf16 v[96:99], v[4:7], v[204:207], v[96:99]\n" \
"v_mfma_f32_16x16x32_bf16 v[128:131], v[0:3], v[208:211], v[128:131]\n" \
"ds_read_b64_tr_b16 v[52:53], v232 offset:9728\n" \
"v_mfma_f32_16x16x32_bf16 v[128:131], v[4:7], v[212:215], v[128:131]\n" \
"ds_read_b64_tr_b16 v[54:55], v232 offset:11776\n" \
"v_mfma_f32_16x16x32_bf16 v[132:135], v[8:11], v[208:211], v[132:135]\n" \
"ds_read_b64_tr_b16 v[56:57], v233 offset:1536\n" \
"v_mfma_f32_16x16x32_bf16 v[132:135], v[12:15], v[212:215], v[132:135]\n" \
"v_mfma_f32_16x16x32_bf16 v[136:139], v[16:19], v[208:211], v[136:139]\n" \
"ds_read_b64_tr_b16 v[58:59], v233 offset:3584\n" \
"v_mfma_f32_16x16x32_bf16 v[136:139], v[20:23], v[212:215], v[136:139]\n" \
"ds_read_b64_tr_b16 v[60:61], v233 offset:9728\n" \
"v_mfma_f32_16x16x32_bf16 v[140:143], v[24:27], v[208:211], v[140:143]\n" \
"ds_read_b64_tr_b16 v[62:63], v233 offset:11776\n" \
"v_mfma_f32_16x16x32_bf16 v[140:143], v[28:31], v[212:215], v[140:143]\n" \
"s_waitcnt lgkmcnt(3)\n" \
"v_mfma_f32_16x16x32_bf16 v[172:175], v[24:27], v[216:219], v[172:175]\n" \
"v_add_u32 v230, 0x8000, v230\n" \
"v_add_u32 v231, 0x8000, v231\n" \
"v_mfma_f32_16x16x32_bf16 v[172:175], v[28:31], v[220:223], v[172:175]\n" \
"v_add_u32 v232, 0x4000, v232\n" \
"v_add_u32 v233, 0x4000, v233\n" \
"v_mfma_f32_16x16x32_bf16 v[168:171], v[16:19], v[216:219], v[168:171]\n" \
"v_mfma_f32_16x16x32_bf16 v[168:171], v[20:23], v[220:223], v[168:171]\n" \
"v_mfma_f32_16x16x32_bf16 v[164:167], v[8:11], v[216:219], v[164:167]\n" \
"v_mfma_f32_16x16x32_bf16 v[164:167], v[12:15], v[220:223], v[164:167]\n" \
"s_waitcnt vmcnt(13) lgkmcnt(0)\n" \
"v_mfma_f32_16x16x32_bf16 v[160:163], v[0:3], v[216:219], v[160:163]\n" \
"v_mfma_f32_16x16x32_bf16 v[160:163], v[4:7], v[220:223], v[160:163]\n" \
"s_barrier\n" \
"v_mfma_f32_16x16x32_bf16 v[80:83], v[32:35], v[192:195], v[80:83]\n" \
"ds_read_b64_tr_b16 v[0:1], v232 offset:0\n" \
"v_mfma_f32_16x16x32_bf16 v[80:83], v[36:39], v[196:199], v[80:83]\n" \
"ds_read_b64_tr_b16 v[2:3], v232 offset:2048\n" \
"v_mfma_f32_16x16x32_bf16 v[84:87], v[40:43], v[192:195], v[84:87]\n" \
"s_add_u32 m0, s95, 32768\n" \
"ds_read_b64_tr_b16 v[4:5], v232 offset:8192\n" \
"v_mfma_f32_16x16x32_bf16 v[84:87], v[44:47], v[196:199], v[84:87]\n" \
"global_load_lds_dwordx4 v250, s[36:37] offset:0 sc1\n" \
"v_mfma_f32_16x16x32_bf16 v[88:91], v[48:51], v[192:195], v[88:91]\n" \
"ds_read_b64_tr_b16 v[6:7], v232 offset:10240\n" \
"v_mfma_f32_16x16x32_bf16 v[88:91], v[52:55], v[196:199], v[88:91]\n" \
"ds_read_b64_tr_b16 v[8:9], v233 offset:0\n" \
"v_mfma_f32_16x16x32_bf16 v[92:95], v[56:59], v[192:195], v[92:95]\n" \
"ds_read_b64_tr_b16 v[10:11], v233 offset:2048\n" \
"v_mfma_f32_16x16x32_bf16 v[92:95], v[60:63], v[196:199], v[92:95]\n" \
"global_load_lds_dwordx4 v250, s[36:37] offset:1024 sc1\n" \
"v_mfma_f32_16x16x32_bf16 v[124:127], v[56:59], v[200:203], v[124:127]\n" \
"ds_read_b128 v[192:195], v230 offset:0\n" \
"v_mfma_f32_16x16x32_bf16 v[124:127], v[60:63], v[204:207], v[124:127]\n" \
"ds_read_b128 v[196:199], v231 offset:0\n" \
"v_mfma_f32_16x16x32_bf16 v[120:123], v[48:51], v[200:203], v[120:123]\n" \
"ds_read_b64_tr_b16 v[12:13], v233 offset:8192\n" \
"v_mfma_f32_16x16x32_bf16 v[120:123], v[52:55], v[204:207], v[120:123]\n" \
"global_load_lds_dwordx4 v250, s[36:37] offset:2048 sc1\n" \
"v_mfma_f32_16x16x32_bf16 v[116:119], v[40:43], v[200:203], v[116:119]\n" \
"ds_read_b64_tr_b16 v[14:15], v233 offset:10240\n" \
"v_mfma_f32_16x16x32_bf16 v[116:119], v[44:47], v[204:207], v[116:119]\n" \
"ds_read_b64_tr_b16 v[16:17], v232 offset:512\n" \
"v_mfma_f32_16x16x32_bf16 v[112:115], v[32:35], v[200:203], v[112:115]\n" \
"ds_read_b64_tr_b16 v[18:19], v232 offset:2560\n" \
"v_mfma_f32_16x16x32_bf16 v[112:115], v[36:39], v[204:207], v[112:115]\n" \
"global_load_lds_dwordx4 v250, s[36:37] offset:3072 sc1\n" \
"v_mfma_f32_16x16x32_bf16 v[144:147], v[32:35], v[208:211], v[144:147]\n" \
"ds_read_b128 v[200:203], v230 offset:2048\n" \
"s_add_u32 s36, s36, 17408\n" \
"v_mfma_f32_16x16x32_bf16 v[144:147], v[36:39], v[212:215], v[144:147]\n" \
"ds_read_b128 v[204:207], v231 offset:2048\n" \
"v_mfma_f32_16x16x32_bf16 v[148:151], v[40:43], v[208:211], v[148:151]\n" \
"ds_read_b64_tr_b16 v[20:21], v232 offset:8704\n" \
"v_mfma_f32_16x16x32_bf16 v[148:151], v[44:47], v[212:215], v[148:151]\n" \
"buffer_store_dwordx4 a[88:91], v248, s[56:59], s90 offen offset:2048 sc1\n" \
"v_mfma_f32_16x16x32_bf16 v[152:155], v[48:51], v[208:211], v[152:155]\n" \
"ds_read_b64_tr_b16 v[22:23], v232 offset:10752\n" \
"v_mfma_f32_16x16x32_bf16 v[152:155], v[52:55], v[212:215], v[152:155]\n" \
"ds_read_b64_tr_b16 v[24:25], v233 offset:512\n" \
"v_mfma_f32_16x16x32_bf16 v[156:159], v[56:59], v[208:211], v[156:159]\n" \
"ds_read_b128 a[88:91], v254\n" \
"ds_read_b64_tr_b16 v[26:27], v233 offset:2560\n" \
"v_mfma_f32_16x16x32_bf16 v[156:159], v[60:63], v[212:215], v[156:159]\n" \
"buffer_store_dwordx4 a[92:95], v248, s[56:59], s90 offen offset:3072 sc1\n" \
"v_mfma_f32_16x16x32_bf16 v[188:191], v[56:59], v[216:219], v[188:191]\n" \
"ds_read_b128 v[208:211], v230 offset:4096\n" \
"s_add_u32 s90, s88, 417792\n" \
"v_mfma_f32_16x16x32_bf16 v[188:191], v[60:63], v[220:223], v[188:191]\n" \
"ds_read_b128 v[212:215], v231 offset:4096\n" \
"v_mfma_f32_16x16x32_bf16 v[184:187], v[48:51], v[216:219], v[184:187]\n" \
"ds_read_b128 a[92:95], v254\n" \
"ds_read_b64_tr_b16 v[28:29], v233 offset:8704\n" \
"v_mfma_f32_16x16x32_bf16 v[184:187], v[52:55], v[220:223], v[184:187]\n" \
"buffer_store_dwordx4 a[0:3], v249, s[56:59], s90 offen offset:0 sc1\n" \
"v_accvgpr_write_b32 a120, v238\n" \
"v_accvgpr_write_b32 a121, v238\n" \
"v_accvgpr_write_b32 a122, v238\n" \
"v_accvgpr_write_b32 a123, v238\n" \
"v_mfma_f32_16x16x32_bf16 v[180:183], v[40:43], v[216:219], v[180:183]\n" \
"ds_read_b64_tr_b16 v[30:31], v233 offset:10752\n" \
"v_accvgpr_write_b32 a124, v239\n" \
"v_mfma_f32_16x16x32_bf16 v[180:183], v[44:47], v[220:223], v[180:183]\n" \
"v_accvgpr_write_b32 a125, v239\n" \
"v_accvgpr_write_b32 a126, v239\n" \
"v_mfma_f32_16x16x32_bf16 v[176:179], v[32:35], v[216:219], v[176:179]\n" \
"ds_read_b128 a[0:3], v254\n" \
"v_accvgpr_write_b32 a127, v239\n" \
"v_mfma_f32_16x16x32_bf16 v[176:179], v[36:39], v[220:223], v[176:179]\n" \
"s_waitcnt lgkmcnt(2)\n" \
"ds_read_b128 a[4:7], v254\n" \
"v_mfma_f32_16x16x32_bf16 v[64:67], v[0:3], v[192:195], v[64:67]\n" \
"ds_read_b128 v[216:219], v230 offset:6144\n" \
"v_mfma_f32_16x16x32_bf16 v[64:67], v[4:7], v[196:199], v[64:67]\n" \
"ds_read_b128 v[220:223], v231 offset:6144\n" \
"v_mfma_f32_16x16x32_bf16 v[68:71], v[8:11], v[192:195], v[68:71]\n" \
"ds_read_b64_tr_b16 v[32:33], v232 offset:1024\n" \
"v_mfma_f32_16x16x32_bf16 v[68:71], v[12:15], v[196:199], v[68:71]\n" \
"v_mfma_f32_16x16x32_bf16 v[72:75], v[16:19], v[192:195], v[72:75]\n" \
"ds_read_b64_tr_b16 v[34:35], v232 offset:3072\n" \
"v_mfma_f32_16x16x32_bf16 v[72:75], v[20:23], v[196:199], v[72:75]\n" \
"s_waitcnt lgkmcnt(3)\n" \
"ds_read_b64_tr_b16 v[36:37], v232 offset:9216\n" \
"v_mfma_f32_16x16x32_bf16 v[76:79], v[24:27], v[192:195], v[76:79]\n" \
"ds_read_b64_tr_b16 v[38:39], v232 offset:11264\n" \
"v_cvt_pk_bf16_f32 v64, v64, v65\n" \
"v_mfma_f32_16x16x32_bf16 v[76:79], v[28:31], v[196:199], v[76:79]\n" \
"v_cvt_pk_bf16_f32 v65, v66, v67\n" \
"v_cvt_pk_bf16_f32 v66, v68, v69\n" \
"v_cvt_pk_bf16_f32 v67, v70, v71\n" \
"v_accvgpr_write_b32 a128, v64\n" \
"v_accvgpr_write_b32 a129, v65\n" \
"v_mfma_f32_16x16x32_bf16 v[96:99], v[0:3], v[200:203], v[96:99]\n" \
"ds_read_b64_tr_b16 v[40:41], v233 offset:1024\n" \
"v_accvgpr_write_b32 a130, v66\n" \
"v_mfma_f32_16x16x32_bf16 v[96:99], v[4:7], v[204:207], v[96:99]\n" \
"ds_read_b64_tr_b16 v[42:43], v233 offset:3072\n" \
"v_accvgpr_write_b32 a131, v67\n" \
"v_mfma_f32_16x16x32_bf16 v[100:103], v[8:11], v[200:203], v[100:103]\n" \
"ds_read_b64_tr_b16 v[44:45], v233 offset:9216\n" \
"v_cvt_pk_bf16_f32 v72, v72, v73\n" \
"v_mfma_f32_16x16x32_bf16 v[100:103], v[12:15], v[204:207], v[100:103]\n" \
"v_cvt_pk_bf16_f32 v73, v74, v75\n" \
"v_cvt_pk_bf16_f32 v74, v76, v77\n" \
"v_cvt_pk_bf16_f32 v75, v78, v79\n" \
"v_accvgpr_write_b32 a132, v72\n" \
"v_accvgpr_write_b32 a133, v73\n" \
"v_mfma_f32_16x16x32_bf16 v[104:107], v[16:19], v[200:203], v[104:107]\n" \
"ds_read_b64_tr_b16 v[46:47], v233 offset:11264\n" \
"v_accvgpr_write_b32 a134, v74\n" \
"v_mfma_f32_16x16x32_bf16 v[104:107], v[20:23], v[204:207], v[104:107]\n" \
"ds_read_b64_tr_b16 v[48:49], v232 offset:1536\n" \
"v_accvgpr_write_b32 a135, v75\n" \
"v_mfma_f32_16x16x32_bf16 v[108:111], v[24:27], v[200:203], v[108:111]\n" \
"ds_read_b64_tr_b16 v[50:51], v232 offset:3584\n" \
"v_cvt_pk_bf16_f32 v96, v96, v97\n" \
"v_mfma_f32_16x16x32_bf16 v[108:111], v[28:31], v[204:207], v[108:111]\n" \
"v_cvt_pk_bf16_f32 v97, v98, v99\n" \
"v_cvt_pk_bf16_f32 v98, v100, v101\n" \
"v_cvt_pk_bf16_f32 v99, v102, v103\n" \
"v_accvgpr_write_b32 a144, v96\n" \
"v_accvgpr_write_b32 a145, v97\n" \
"v_mfma_f32_16x16x32_bf16 v[128:131], v[0:3], v[208:211], v[128:131]\n" \
"ds_read_b64_tr_b16 v[52:53], v232 offset:9728\n" \
"v_accvgpr_write_b32 a146, v98\n" \
"v_mfma_f32_16x16x32_bf16 v[128:131], v[4:7], v[212:215], v[128:131]\n" \
"ds_read_b64_tr_b16 v[54:55], v232 offset:11776\n" \
"v_accvgpr_write_b32 a147, v99\n" \
"v_mfma_f32_16x16x32_bf16 v[132:135], v[8:11], v[208:211], v[132:135]\n" \
"ds_read_b64_tr_b16 v[56:57], v233 offset:1536\n" \
"v_cvt_pk_bf16_f32 v104, v104, v105\n" \
"v_mfma_f32_16x16x32_bf16 v[132:135], v[12:15], v[212:215], v[132:135]\n" \
"v_cvt_pk_bf16_f32 v105, v106, v107\n" \
"v_cvt_pk_bf16_f32 v106, v108, v109\n" \
"v_cvt_pk_bf16_f32 v107, v110, v111\n" \
"v_accvgpr_write_b32 a148, v104\n" \
"v_accvgpr_write_b32 a149, v105\n" \
"v_mfma_f32_16x16x32_bf16 v[136:139], v[16:19], v[208:211], v[136:139]\n" \
"ds_read_b64_tr_b16 v[58:59], v233 offset:3584\n" \
"v_accvgpr_write_b32 a150, v106\n" \
"v_mfma_f32_16x16x32_bf16 v[136:139], v[20:23], v[212:215], v[136:139]\n" \
"ds_read_b64_tr_b16 v[60:61], v233 offset:9728\n" \
"v_accvgpr_write_b32 a151, v107\n" \
"v_mfma_f32_16x16x32_bf16 v[140:143], v[24:27], v[208:211], v[140:143]\n" \
"ds_read_b64_tr_b16 v[62:63], v233 offset:11776\n" \
"v_cvt_pk_bf16_f32 v128, v128, v129\n" \
"v_mfma_f32_16x16x32_bf16 v[140:143], v[28:31], v[212:215], v[140:143]\n" \
"s_waitcnt lgkmcnt(3)\n" \
"v_cvt_pk_bf16_f32 v129, v130, v131\n" \
"v_cvt_pk_bf16_f32 v130, v132, v133\n" \
"v_cvt_pk_bf16_f32 v131, v134, v135\n" \
"Lpg4936_%=:\n" \
"s_cbranch_execz Lpg6171_%=\n" \
"v_mfma_f32_16x16x32_bf16 v[160:163], v[0:3], v[216:219], v[160:163]\n" \
"v_accvgpr_write_b32 a160, v128\n" \
"v_accvgpr_write_b32 a161, v129\n" \
"v_mfma_f32_16x16x32_bf16 v[160:163], v[4:7], v[220:223], v[160:163]\n" \
"v_accvgpr_write_b32 a162, v130\n" \
"v_accvgpr_write_b32 a163, v131\n" \
"v_mfma_f32_16x16x32_bf16 v[164:167], v[8:11], v[216:219], v[164:167]\n" \
"v_cvt_pk_bf16_f32 v136, v136, v137\n" \
"v_cvt_pk_bf16_f32 v137, v138, v139\n" \
"v_mfma_f32_16x16x32_bf16 v[164:167], v[12:15], v[220:223], v[164:167]\n" \
"v_cvt_pk_bf16_f32 v138, v140, v141\n" \
"v_cvt_pk_bf16_f32 v139, v142, v143\n" \
"v_mfma_f32_16x16x32_bf16 v[168:171], v[16:19], v[216:219], v[168:171]\n" \
"v_accvgpr_write_b32 a164, v136\n" \
"v_accvgpr_write_b32 a165, v137\n" \
"v_mfma_f32_16x16x32_bf16 v[168:171], v[20:23], v[220:223], v[168:171]\n" \
"s_waitcnt vmcnt(10) lgkmcnt(0)\n" \
"v_accvgpr_write_b32 a166, v138\n" \
"v_accvgpr_write_b32 a167, v139\n" \
"v_mfma_f32_16x16x32_bf16 v[172:175], v[24:27], v[216:219], v[172:175]\n" \
"v_cvt_pk_bf16_f32 v160, v160, v161\n" \
"v_cvt_pk_bf16_f32 v161, v162, v163\n" \
"v_mfma_f32_16x16x32_bf16 v[172:175], v[28:31], v[220:223], v[172:175]\n" \
"s_barrier\n" \
"v_cvt_pk_bf16_f32 v162, v164, v165\n" \
"v_cvt_pk_bf16_f32 v163, v166, v167\n" \
"v_accvgpr_write_b32 a176, v160\n" \
"v_accvgpr_write_b32 a177, v161\n" \
"v_mfma_f32_16x16x32_bf16 v[80:83], v[32:35], v[192:195], v[80:83]\n" \
"s_mov_b32 m0, s74\n" \
"v_accvgpr_write_b32 a178, v162\n" \
"v_accvgpr_write_b32 a179, v163\n" \
"v_mfma_f32_16x16x32_bf16 v[80:83], v[36:39], v[196:199], v[80:83]\n" \
"ds_read_b128 a[192:195], v249 offset:0\n" \
"global_load_lds_dwordx4 v250, s[38:39] offset:0 sc1\n" \
"v_mfma_f32_16x16x32_bf16 v[84:87], v[40:43], v[192:195], v[84:87]\n" \
"ds_read_b128 v[0:3], v254\n" \
"v_cvt_pk_bf16_f32 v168, v168, v169\n" \
"v_mfma_f32_16x16x32_bf16 v[84:87], v[44:47], v[196:199], v[84:87]\n" \
"ds_read_b128 a[196:199], v249 offset:2048\n" \
"global_load_lds_dwordx4 v250, s[38:39] offset:1024 sc1\n" \
"v_cvt_pk_bf16_f32 v169, v170, v171\n" \
"v_cvt_pk_bf16_f32 v170, v172, v173\n" \
"v_cvt_pk_bf16_f32 v171, v174, v175\n" \
"v_accvgpr_write_b32 a180, v168\n" \
"v_accvgpr_write_b32 a181, v169\n" \
"v_accvgpr_write_b32 a182, v170\n" \
"v_mfma_f32_16x16x32_bf16 v[88:91], v[48:51], v[192:195], v[88:91]\n" \
"ds_read_b128 v[4:7], v254\n" \
"v_accvgpr_write_b32 a183, v171\n" \
"v_mfma_f32_16x16x32_bf16 v[88:91], v[52:55], v[196:199], v[88:91]\n" \
"ds_read_b128 a[200:203], v249 offset:4096\n" \
"global_load_lds_dwordx4 v250, s[38:39] offset:2048 sc1\n" \
"v_mfma_f32_16x16x32_bf16 v[92:95], v[56:59], v[192:195], v[92:95]\n" \
"v_cvt_pk_bf16_f32 v80, v80, v81\n" \
"v_cvt_pk_bf16_f32 v81, v82, v83\n" \
"v_mfma_f32_16x16x32_bf16 v[92:95], v[60:63], v[196:199], v[92:95]\n" \
"ds_read_b128 a[204:207], v249 offset:6144\n" \
"global_load_lds_dwordx4 v250, s[38:39] offset:3072 sc1\n" \
"v_cvt_pk_bf16_f32 v82, v84, v85\n" \
"v_cvt_pk_bf16_f32 v83, v86, v87\n" \
"v_accvgpr_write_b32 a136, v80\n" \
"v_accvgpr_write_b32 a137, v81\n" \
"v_accvgpr_write_b32 a138, v82\n" \
"v_mfma_f32_16x16x32_bf16 v[112:115], v[32:35], v[200:203], v[112:115]\n" \
"s_add_u32 s38, s38, 17408\n" \
"v_accvgpr_write_b32 a139, v83\n" \
"s_add_u32 m0, m0, 0x4000\n" \
"v_mfma_f32_16x16x32_bf16 v[112:115], v[36:39], v[204:207], v[112:115]\n" \
"ds_read_b128 a[208:211], v249 offset:256\n" \
"global_load_lds_dwordx4 v250, s[38:39] offset:0 sc1\n" \
"v_mfma_f32_16x16x32_bf16 v[116:119], v[40:43], v[200:203], v[116:119]\n" \
"v_cvt_pk_bf16_f32 v88, v88, v89\n" \
"v_cvt_pk_bf16_f32 v89, v90, v91\n" \
"v_mfma_f32_16x16x32_bf16 v[116:119], v[44:47], v[204:207], v[116:119]\n" \
"ds_read_b128 a[212:215], v249 offset:2304\n" \
"global_load_lds_dwordx4 v250, s[38:39] offset:1024 sc1\n" \
"v_cvt_pk_bf16_f32 v90, v92, v93\n" \
"v_cvt_pk_bf16_f32 v91, v94, v95\n" \
"v_accvgpr_write_b32 a140, v88\n" \
"v_accvgpr_write_b32 a141, v89\n" \
"v_mfma_f32_16x16x32_bf16 v[120:123], v[48:51], v[200:203], v[120:123]\n" \
"v_accvgpr_write_b32 a142, v90\n" \
"v_accvgpr_write_b32 a143, v91\n" \
"v_mfma_f32_16x16x32_bf16 v[120:123], v[52:55], v[204:207], v[120:123]\n" \
"ds_read_b128 a[216:219], v249 offset:4352\n" \
"global_load_lds_dwordx4 v250, s[38:39] offset:2048 sc1\n" \
"v_mfma_f32_16x16x32_bf16 v[124:127], v[56:59], v[200:203], v[124:127]\n" \
"v_cvt_pk_bf16_f32 v112, v112, v113\n" \
"v_cvt_pk_bf16_f32 v113, v114, v115\n" \
"v_mfma_f32_16x16x32_bf16 v[124:127], v[60:63], v[204:207], v[124:127]\n" \
"ds_read_b128 a[220:223], v249 offset:6400\n" \
"global_load_lds_dwordx4 v250, s[38:39] offset:3072 sc1\n" \
"v_cvt_pk_bf16_f32 v114, v116, v117\n" \
"v_cvt_pk_bf16_f32 v115, v118, v119\n" \
"v_mfma_f32_16x16x32_bf16 v[144:147], v[32:35], v[208:211], v[144:147]\n" \
"s_add_u32 s38, s38, 17408\n" \
"v_accvgpr_write_b32 a152, v112\n" \
"s_xor_b32 s74, s74, 0x8000\n" \
"v_mfma_f32_16x16x32_bf16 v[144:147], v[36:39], v[212:215], v[144:147]\n" \
"ds_read_b128 a[224:227], v249 offset:8192\n" \
"v_accvgpr_write_b32 a153, v113\n" \
"v_mfma_f32_16x16x32_bf16 v[148:151], v[40:43], v[208:211], v[148:151]\n" \
"v_accvgpr_write_b32 a154, v114\n" \
"v_accvgpr_write_b32 a155, v115\n" \
"v_mfma_f32_16x16x32_bf16 v[148:151], v[44:47], v[212:215], v[148:151]\n" \
"buffer_store_dwordx4 a[32:35], v249, s[56:59], s90 offen offset:256 sc1\n" \
"ds_read_b128 a[228:231], v249 offset:10240\n" \
"v_cvt_pk_bf16_f32 v120, v120, v121\n" \
"v_cvt_pk_bf16_f32 v121, v122, v123\n" \
"v_cvt_pk_bf16_f32 v122, v124, v125\n" \
"v_mfma_f32_16x16x32_bf16 v[152:155], v[48:51], v[208:211], v[152:155]\n" \
"v_cvt_pk_bf16_f32 v123, v126, v127\n" \
"v_accvgpr_write_b32 a156, v120\n" \
"v_mfma_f32_16x16x32_bf16 v[152:155], v[52:55], v[212:215], v[152:155]\n" \
"ds_read_b128 a[232:235], v249 offset:12288\n" \
"v_accvgpr_write_b32 a157, v121\n" \
"v_mfma_f32_16x16x32_bf16 v[156:159], v[56:59], v[208:211], v[156:159]\n" \
"ds_read_b128 a[32:35], v254\n" \
"v_accvgpr_write_b32 a158, v122\n" \
"v_mfma_f32_16x16x32_bf16 v[156:159], v[60:63], v[212:215], v[156:159]\n" \
"ds_read_b128 a[236:239], v249 offset:14336\n" \
"v_accvgpr_write_b32 a159, v123\n" \
"v_mfma_f32_16x16x32_bf16 v[176:179], v[32:35], v[216:219], v[176:179]\n" \
"ds_read_b128 a[240:243], v249 offset:8448\n" \
"v_cvt_pk_bf16_f32 v144, v144, v145\n" \
"v_mfma_f32_16x16x32_bf16 v[176:179], v[36:39], v[220:223], v[176:179]\n" \
"ds_read_b128 a[244:247], v249 offset:10496\n" \
"v_cvt_pk_bf16_f32 v145, v146, v147\n" \
"v_mfma_f32_16x16x32_bf16 v[180:183], v[40:43], v[216:219], v[180:183]\n" \
"ds_read_b128 a[248:251], v249 offset:12544\n" \
"v_cvt_pk_bf16_f32 v146, v148, v149\n" \
"v_mfma_f32_16x16x32_bf16 v[180:183], v[44:47], v[220:223], v[180:183]\n" \
"ds_read_b128 a[252:255], v249 offset:14592\n" \
"v_cvt_pk_bf16_f32 v147, v150, v151\n" \
"v_mfma_f32_16x16x32_bf16 v[184:187], v[48:51], v[216:219], v[184:187]\n" \
"ds_read_b128 a[36:39], v254\n" \
"v_accvgpr_write_b32 a168, v144\n" \
"v_mfma_f32_16x16x32_bf16 v[184:187], v[52:55], v[220:223], v[184:187]\n" \
"ds_read_b128 v[32:35], v254\n" \
"v_accvgpr_write_b32 a169, v145\n" \
"v_mfma_f32_16x16x32_bf16 v[188:191], v[56:59], v[216:219], v[188:191]\n" \
"ds_read_b128 v[36:39], v254\n" \
"v_accvgpr_write_b32 a170, v146\n" \
"v_mfma_f32_16x16x32_bf16 v[188:191], v[60:63], v[220:223], v[188:191]\n" \
"s_waitcnt lgkmcnt(4)\n" \
"buffer_store_dwordx4 a[8:11], v249, s[56:59], s90 offen offset:2048 sc1\n" \
"v_accvgpr_write_b32 a171, v147\n" \
"s_cmp_eq_u32 s63, 0\n" \
"s_cbranch_scc1 Lmask0_%=\n" \
"Lmask0r_%=:\n" \
"v_mfma_f32_16x16x32_bf16 v[0:3], a[192:195], a[128:131], v[0:3]\n" \
"s_lshl_b32 s70, s95, 1\n" \
"v_cvt_pk_bf16_f32 v152, v152, v153\n" \
"v_cvt_pk_bf16_f32 v153, v154, v155\n" \
"v_mfma_f32_16x16x32_bf16 v[0:3], a[196:199], a[132:135], v[0:3]\n" \
"s_add_u32 s70, s70, 131072\n" \
"v_cvt_pk_bf16_f32 v154, v156, v157\n" \
"v_cvt_pk_bf16_f32 v155, v158, v159\n" \
"v_mfma_f32_16x16x32_bf16 v[0:3], a[200:203], a[136:139], v[0:3]\n" \
"ds_read_b128 a[8:11], v254\n" \
"v_accvgpr_write_b32 a172, v152\n" \
"v_mfma_f32_16x16x32_bf16 v[0:3], a[204:207], a[140:143], v[0:3]\n" \
"ds_read_b128 a[12:15], v254\n" \
"v_accvgpr_write_b32 a173, v153\n" \
"v_mov_b32 v201, 0xf149f2ca\n" \
"v_mfma_f32_16x16x32_bf16 v[8:11], a[192:195], a[144:147], 0\n" \
"v_accvgpr_write_b32 a174, v154\n" \
"v_accvgpr_write_b32 a175, v155\n" \
"v_mfma_f32_16x16x32_bf16 v[8:11], a[196:199], a[148:151], v[8:11]\n" \
"v_cvt_pk_bf16_f32 v176, v176, v177\n" \
"v_cvt_pk_bf16_f32 v177, v178, v179\n" \
"v_mfma_f32_16x16x32_bf16 v[8:11], a[200:203], a[152:155], v[8:11]\n" \
"v_cvt_pk_bf16_f32 v178, v180, v181\n" \
"v_cvt_pk_bf16_f32 v179, v182, v183\n" \
"v_mfma_f32_16x16x32_bf16 v[8:11], a[204:207], a[156:159], v[8:11]\n" \
"v_accvgpr_write_b32 a184, v176\n" \
"v_accvgpr_write_b32 a185, v177\n" \
"v_max3_f32 v192, v0, v1, v2\n" \
"v_max3_f32 v192, v192, v3, v201\n" \
"v_mfma_f32_16x16x32_bf16 v[16:19], a[192:195], a[160:163], 0\n" \
"v_accvgpr_write_b32 a186, v178\n" \
"v_accvgpr_write_b32 a187, v179\n" \
"v_mfma_f32_16x16x32_bf16 v[16:19], a[196:199], a[164:167], v[16:19]\n" \
"v_cvt_pk_bf16_f32 v184, v184, v185\n" \
"v_cvt_pk_bf16_f32 v185, v186, v187\n" \
"v_mfma_f32_16x16x32_bf16 v[16:19], a[200:203], a[168:171], v[16:19]\n" \
"v_cvt_pk_bf16_f32 v186, v188, v189\n" \
"v_cvt_pk_bf16_f32 v187, v190, v191\n" \
"v_mfma_f32_16x16x32_bf16 v[16:19], a[204:207], a[172:175], v[16:19]\n" \
"v_max3_f32 v193, v8, v9, v10\n" \
"v_accvgpr_write_b32 a188, v184\n" \
"v_accvgpr_write_b32 a189, v185\n" \
"v_mfma_f32_16x16x32_bf16 v[24:27], a[192:195], a[176:179], 0\n" \
"v_accvgpr_write_b32 a190, v186\n" \
"v_accvgpr_write_b32 a191, v187\n" \
"v_mfma_f32_16x16x32_bf16 v[24:27], a[196:199], a[180:183], v[24:27]\n" \
"v_add_u32 v200, s70, v248\n" \
"v_max3_f32 v193, v193, v11, v201\n" \
"v_mfma_f32_16x16x32_bf16 v[24:27], a[200:203], a[184:187], v[24:27]\n" \
"ds_read_b128 v[128:131], v254\n" \
"ds_read_b128 v[132:135], v254\n" \
"v_max3_f32 v194, v16, v17, v18\n" \
"v_max3_f32 v194, v194, v19, v201\n" \
"v_mfma_f32_16x16x32_bf16 v[24:27], a[204:207], a[188:191], v[24:27]\n" \
"s_waitcnt lgkmcnt(2)\n" \
"ds_read_b128 v[136:139], v254\n" \
"ds_read_b128 v[140:143], v254\n" \
"v_mfma_f32_16x16x32_bf16 v[4:7], a[208:211], a[128:131], v[4:7]\n" \
"ds_read_b128 v[144:147], v254\n" \
"ds_read_b128 v[148:151], v254\n" \
"v_mfma_f32_16x16x32_bf16 v[4:7], a[212:215], a[132:135], v[4:7]\n" \
"ds_read_b128 a[192:195], v249 offset:16384\n" \
"ds_read_b128 v[152:155], v254\n" \
"v_mfma_f32_16x16x32_bf16 v[4:7], a[216:219], a[136:139], v[4:7]\n" \
"ds_read_b128 v[156:159], v254\n" \
"ds_read_b128 v[160:163], v254\n" \
"v_max3_f32 v195, v24, v25, v26\n" \
"v_max3_f32 v195, v195, v27, v201\n" \
"v_mov_b32 v196, v192\n" \
"v_mov_b32 v197, v193\n" \
"v_mov_b32 v198, v194\n" \
"v_mov_b32 v199, v195\n" \
"v_permlane16_swap_b32 v196, v192\n" \
"v_permlane16_swap_b32 v197, v193\n" \
"v_permlane16_swap_b32 v198, v194\n" \
"v_permlane16_swap_b32 v199, v195\n" \
"v_max_f32 v192, v192, v196\n" \
"v_max_f32 v193, v193, v197\n" \
"v_mfma_f32_16x16x32_bf16 v[4:7], a[220:223], a[140:143], v[4:7]\n" \
"ds_read_b128 a[196:199], v249 offset:18432\n" \
"ds_read_b128 v[164:167], v254\n" \
"v_max_f32 v194, v194, v198\n" \
"v_max_f32 v195, v195, v199\n" \
"v_mov_b32 v196, v192\n" \
"v_mov_b32 v197, v193\n" \
"v_mov_b32 v198, v194\n" \
"v_mov_b32 v199, v195\n" \
"v_permlane32_swap_b32 v196, v192\n" \
"v_permlane32_swap_b32 v197, v193\n" \
"v_permlane32_swap_b32 v198, v194\n" \
"v_permlane32_swap_b32 v199, v195\n" \
"v_max_f32 v192, v192, v196\n" \
"v_max_f32 v193, v193, v197\n" \
"v_mfma_f32_16x16x32_bf16 v[12:15], a[216:219], a[152:155], 0\n" \
"ds_read_b128 v[168:171], v254\n" \
"ds_read_b128 v[172:175], v254\n" \
"v_max_f32 v194, v194, v198\n" \
"v_max_f32 v195, v195, v199\n" \
"v_sub_f32 v224, 0, v192\n" \
"v_sub_f32 v225, 0, v192\n" \
"v_sub_f32 v226, 0, v192\n" \
"v_sub_f32 v227, 0, v192\n" \
"v_add_f32 v32, v32, v224\n" \
"v_mfma_f32_16x16x32_bf16 v[12:15], a[220:223], a[156:159], v[12:15]\n" \
"ds_read_b128 a[200:203], v249 offset:20480\n" \
"ds_read_b128 v[176:179], v254\n" \
"v_mfma_f32_16x16x32_bf16 v[12:15], a[208:211], a[144:147], v[12:15]\n" \
"ds_read_b128 v[180:183], v254\n" \
"ds_read_b128 v[184:187], v254\n" \
"v_mfma_f32_16x16x32_bf16 v[12:15], a[212:215], a[148:151], v[12:15]\n" \
"ds_read_b128 a[204:207], v249 offset:22528\n" \
"buffer_store_dwordx4 a[40:43], v249, s[56:59], s90 offen offset:2304 sc1\n" \
"ds_write_b128 v200, a[160:163] offset:0\n" \
"s_add_u32 s90, s89, 417792\n" \
"v_mfma_f32_16x16x32_bf16 v[20:23], a[208:211], a[160:163], 0\n" \
"v_add_f32 v33, v33, v224\n" \
"v_add_f32 v34, v34, v224\n" \
"v_mfma_f32_16x16x32_bf16 v[20:23], a[212:215], a[164:167], v[20:23]\n" \
"v_add_f32 v35, v35, v224\n" \
"v_sub_f32 v228, 0, v193\n" \
"v_mfma_f32_16x16x32_bf16 v[20:23], a[216:219], a[168:171], v[20:23]\n" \
"ds_read_b128 a[40:43], v254\n" \
"ds_read_b128 v[188:191], v254\n" \
"v_mfma_f32_16x16x32_bf16 v[20:23], a[220:223], a[172:175], v[20:23]\n" \
"buffer_store_dwordx4 a[16:19], v248, s[56:59], s90 offen offset:0 sc1\n" \
"ds_write_b128 v200, a[164:167] offset:1024\n" \
"v_sub_f32 v229, 0, v193\n" \
"v_mfma_f32_16x16x32_bf16 v[28:31], a[216:219], a[184:187], 0\n" \
"v_sub_f32 v230, 0, v193\n" \
"v_sub_f32 v231, 0, v193\n" \
"v_mfma_f32_16x16x32_bf16 v[28:31], a[220:223], a[188:191], v[28:31]\n" \
"v_sub_f32 v232, 0, v194\n" \
"v_sub_f32 v233, 0, v194\n" \
"v_mfma_f32_16x16x32_bf16 v[28:31], a[208:211], a[176:179], v[28:31]\n" \
"buffer_store_dwordx4 a[20:23], v248, s[56:59], s90 offen offset:1024 sc1\n" \
"ds_write_b128 v200, a[168:171] offset:2048\n" \
"v_sub_f32 v234, 0, v194\n" \
"v_mfma_f32_16x16x32_bf16 v[28:31], a[212:215], a[180:183], v[28:31]\n" \
"v_sub_f32 v235, 0, v194\n" \
"v_sub_f32 v236, 0, v195\n" \
"v_mfma_f32_16x16x32_bf16 v[32:35], a[224:227], a[128:131], v[32:35]\n" \
"v_sub_f32 v237, 0, v195\n" \
"v_sub_f32 v238, 0, v195\n" \
"v_mfma_f32_16x16x32_bf16 v[32:35], a[228:231], a[132:135], v[32:35]\n" \
"ds_read_b128 a[208:211], v249 offset:16640\n" \
"v_sub_f32 v239, 0, v195\n" \
"v_mfma_f32_16x16x32_bf16 v[32:35], a[232:235], a[136:139], v[32:35]\n" \
"ds_read_b128 a[44:47], v254\n" \
"v_add_f32 v36, v36, v224\n" \
"v_mfma_f32_16x16x32_bf16 v[32:35], a[236:239], a[140:143], v[32:35]\n" \
"ds_read_b128 a[212:215], v249 offset:18688\n" \
"ds_read_b128 a[16:19], v254\n" \
"v_mfma_f32_16x16x32_bf16 v[40:43], a[232:235], a[152:155], v[228:231]\n" \
"ds_read_b128 a[48:51], v254\n" \
"v_add_f32 v37, v37, v224\n" \
"v_mfma_f32_16x16x32_bf16 v[40:43], a[236:239], a[156:159], v[40:43]\n" \
"ds_read_b128 a[216:219], v249 offset:20736\n" \
"v_add_f32 v38, v38, v224\n" \
"v_mfma_f32_16x16x32_bf16 v[40:43], a[224:227], a[144:147], v[40:43]\n" \
"ds_read_b128 a[20:23], v254\n" \
"v_add_f32 v39, v39, v224\n" \
"v_mfma_f32_16x16x32_bf16 v[40:43], a[228:231], a[148:151], v[40:43]\n" \
"buffer_store_dwordx4 a[24:27], v248, s[56:59], s90 offen offset:2048 sc1\n" \
"ds_read_b128 a[220:223], v249 offset:22784\n" \
"ds_write_b128 v200, a[172:175] offset:3072\n" \
"v_pk_add_f32 v[0:1], v[0:1], v[224:225]\n" \
"v_pk_add_f32 v[2:3], v[2:3], v[224:225]\n" \
"v_pk_add_f32 v[4:5], v[4:5], v[224:225]\n" \
"v_pk_add_f32 v[6:7], v[6:7], v[224:225]\n" \
"v_exp_f32 v0, v0\n" \
"v_exp_f32 v1, v1\n" \
"v_exp_f32 v2, v2\n" \
"v_cvt_pk_bf16_f32 v0, v0, v1\n" \
"v_exp_f32 v3, v3\n" \
"v_exp_f32 v4, v4\n" \
"v_cvt_pk_bf16_f32 v1, v2, v3\n" \
"v_mfma_f32_16x16x32_bf16 v[48:51], a[224:227], a[160:163], v[232:235]\n" \
"v_exp_f32 v5, v5\n" \
"v_mfma_f32_16x16x32_bf16 v[48:51], a[228:231], a[164:167], v[48:51]\n" \
"v_exp_f32 v6, v6\n" \
"v_mfma_f32_16x16x32_bf16 v[48:51], a[232:235], a[168:171], v[48:51]\n" \
"ds_read_b128 a[52:55], v254\n" \
"v_cvt_pk_bf16_f32 v2, v4, v5\n" \
"v_mfma_f32_16x16x32_bf16 v[48:51], a[236:239], a[172:175], v[48:51]\n" \
"buffer_store_dwordx4 a[28:31], v248, s[56:59], s90 offen offset:3072 sc1\n" \
"ds_write_b128 v200, a[176:179] offset:4096\n" \
"v_exp_f32 v7, v7\n" \
"v_pk_add_f32 v[8:9], v[8:9], v[228:229]\n" \
"v_pk_add_f32 v[10:11], v[10:11], v[228:229]\n" \
"v_pk_add_f32 v[12:13], v[12:13], v[228:229]\n" \
"v_pk_add_f32 v[14:15], v[14:15], v[228:229]\n" \
"v_exp_f32 v8, v8\n" \
"v_cvt_pk_bf16_f32 v3, v6, v7\n" \
"v_exp_f32 v9, v9\n" \
"v_exp_f32 v10, v10\n" \
"v_cvt_pk_bf16_f32 v8, v8, v9\n" \
"v_exp_f32 v11, v11\n" \
"v_mfma_f32_16x16x32_bf16 v[56:59], a[232:235], a[184:187], v[236:239]\n" \
"global_load_dword v253, v252, s[40:41]\n" \
"v_mfma_f32_16x16x32_bf16 v[56:59], a[236:239], a[188:191], v[56:59]\n" \
"s_sub_u32 s80, s80, 1\n" \
"v_exp_f32 v12, v12\n" \
"v_mfma_f32_16x16x32_bf16 v[56:59], a[224:227], a[176:179], v[56:59]\n" \
"s_cmp_gt_i32 s80, 0\n" \
"ds_write_b128 v200, a[180:183] offset:5120\n" \
"v_cvt_pk_bf16_f32 v9, v10, v11\n" \
"v_exp_f32 v13, v13\n" \
"v_exp_f32 v14, v14\n" \
"v_cvt_pk_bf16_f32 v10, v12, v13\n" \
"v_exp_f32 v15, v15\n" \
"v_pk_add_f32 v[16:17], v[16:17], v[232:233]\n" \
"v_pk_add_f32 v[18:19], v[18:19], v[232:233]\n" \
"v_pk_add_f32 v[20:21], v[20:21], v[232:233]\n" \
"v_pk_add_f32 v[22:23], v[22:23], v[232:233]\n" \
"v_exp_f32 v16, v16\n" \
"v_cvt_pk_bf16_f32 v11, v14, v15\n" \
"v_mfma_f32_16x16x32_bf16 v[56:59], a[228:231], a[180:183], v[56:59]\n" \
"s_cselect_b32 s81, 0x80000, 0\n" \
"v_exp_f32 v17, v17\n" \
"v_mfma_f32_16x16x32_bf16 v[36:39], a[240:243], a[128:131], v[36:39]\n" \
"s_add_u32 s40, s40, s81\n" \
"v_exp_f32 v18, v18\n" \
"v_mfma_f32_16x16x32_bf16 v[36:39], a[244:247], a[132:135], v[36:39]\n" \
"ds_read_b128 a[224:227], v249 offset:24576\n" \
"v_cvt_pk_bf16_f32 v16, v16, v17\n" \
"v_mfma_f32_16x16x32_bf16 v[240:243], a[120:123], v[0:3], v[240:243]\n" \
"v_exp_f32 v19, v19\n" \
"v_mfma_f32_16x16x32_bf16 v[240:243], a[124:127], v[8:11], v[240:243]\n" \
"s_addc_u32 s41, s41, 0\n" \
"v_exp_f32 v20, v20\n" \
"v_mfma_f32_16x16x32_bf16 v[36:39], a[248:251], a[136:139], v[36:39]\n" \
"s_add_u32 m0, s95, 49152\n" \
"ds_read_b128 a[24:27], v254\n" \
"v_cvt_pk_bf16_f32 v17, v18, v19\n" \
"v_mfma_f32_16x16x32_bf16 v[36:39], a[252:255], a[140:143], v[36:39]\n" \
"ds_read_b128 a[228:231], v249 offset:26624\n" \
"global_load_lds_dwordx4 v250, s[36:37] offset:0 sc1\n" \
"ds_read_b128 a[56:59], v254\n" \
"v_exp_f32 v21, v21\n" \
"v_exp_f32 v22, v22\n" \
"v_mfma_f32_16x16x32_bf16 v[44:47], a[248:251], a[152:155], v[228:231]\n" \
"ds_read_b128 a[28:31], v254\n" \
"v_cvt_pk_bf16_f32 v18, v20, v21\n" \
"v_mfma_f32_16x16x32_bf16 v[44:47], a[252:255], a[156:159], v[44:47]\n" \
"ds_read_b128 a[232:235], v249 offset:28672\n" \
"global_load_lds_dwordx4 v250, s[36:37] offset:1024 sc1\n" \
"v_mfma_f32_16x16x32_bf16 v[44:47], a[240:243], a[144:147], v[44:47]\n" \
"ds_read_b128 a[60:63], v254\n" \
"global_load_lds_dwordx4 v250, s[36:37] offset:2048 sc1\n" \
"v_mfma_f32_16x16x32_bf16 v[44:47], a[244:247], a[148:151], v[44:47]\n" \
"ds_read_b128 a[236:239], v249 offset:30720\n" \
"global_load_lds_dwordx4 v250, s[36:37] offset:3072 sc1\n" \
"ds_write_b128 v200, a[184:187] offset:6144\n" \
"v_exp_f32 v23, v23\n" \
"v_pk_add_f32 v[24:25], v[24:25], v[236:237]\n" \
"v_pk_add_f32 v[26:27], v[26:27], v[236:237]\n" \
"v_pk_add_f32 v[28:29], v[28:29], v[236:237]\n" \
"v_pk_add_f32 v[30:31], v[30:31], v[236:237]\n" \
"v_exp_f32 v24, v24\n" \
"v_cvt_pk_bf16_f32 v19, v22, v23\n" \
"v_exp_f32 v25, v25\n" \
"v_exp_f32 v26, v26\n" \
"v_cvt_pk_bf16_f32 v24, v24, v25\n" \
"v_mfma_f32_16x16x32_bf16 v[242:245], a[120:123], v[16:19], v[242:245]\n" \
"v_exp_f32 v27, v27\n" \
"v_mfma_f32_16x16x32_bf16 v[52:55], a[240:243], a[160:163], v[232:235]\n" \
"s_add_u32 s36, s36, 17408\n" \
"v_mov_b32 v202, s60\n" \
"v_lshlrev_b32 v203, 28, s61\n" \
"v_mfma_f32_16x16x32_bf16 v[52:55], a[244:247], a[164:167], v[52:55]\n" \
"s_mov_b32 s42, s60\n" \
"v_mov_b32 v246, v249\n" \
"v_mov_b32 v254, 0xff800000\n" \
"v_mfma_f32_16x16x32_bf16 v[52:55], a[248:251], a[168:171], v[52:55]\n" \
"s_mov_b32 s43, s60\n" \
"v_exp_f32 v28, v28\n" \
"v_mfma_f32_16x16x32_bf16 v[52:55], a[252:255], a[172:175], v[52:55]\n" \
"s_mov_b64 s[8:9], 0\n" \
"ds_write_b128 v200, a[188:191] offset:7168\n" \
"v_cvt_pk_bf16_f32 v25, v26, v27\n" \
"v_exp_f32 v29, v29\n" \
"v_mfma_f32_16x16x32_bf16 v[60:63], a[248:251], a[184:187], v[236:239]\n" \
"s_mov_b64 s[10:11], -1\n" \
"v_exp_f32 v30, v30\n" \
"v_cvt_pk_bf16_f32 v26, v28, v29\n" \
"v_mfma_f32_16x16x32_bf16 v[60:63], a[252:255], a[188:191], v[60:63]\n" \
"s_mov_b64 s[92:93], 0\n" \
"v_exp_f32 v31, v31\n" \
"v_mfma_f32_16x16x32_bf16 v[60:63], a[240:243], a[176:179], v[60:63]\n" \
"v_cvt_pk_bf16_f32 v27, v30, v31\n" \
"s_lshl_b32 s84, s63, 2\n" \
"v_mfma_f32_16x16x32_bf16 v[60:63], a[244:247], a[180:183], v[60:63]\n" \
"v_mfma_f32_16x16x32_bf16 v[242:245], a[124:127], v[24:27], v[242:245]\n" \
"s_add_u32 s84, s84, s79\n" \
"ds_read_b128 a[240:243], v249 offset:24832\n" \
"ds_read_b128 a[244:247], v249 offset:26880\n" \
"ds_read_b128 a[248:251], v249 offset:28928\n" \
"s_cmp_eq_u32 s63, 0\n" \
"s_waitcnt vmcnt(16) lgkmcnt(3)\n" \
"s_barrier\n" \
"s_memrealtime s[12:13]\n" \
"s_cbranch_scc1 LA1l_%=\n" \
"s_branch LA1_%=\n" \
"Lpg5391_%=:\n" \
"s_cbranch_execz Lpg6293_%=\n" \
"Lloop_%=:\n" \
"v_mfma_f32_16x16x32_bf16 a[0:3], a[192:195], v[64:67], a[0:3]\n" \
"v_exp_f32 v0, v0\n" \
"v_mfma_f32_16x16x32_bf16 a[0:3], a[196:199], v[96:99], a[0:3]\n" \
"v_cvt_pk_bf16_f32 v123, v126, v127\n" \
"v_add_f32 v242, v242, v0\n" \
"v_exp_f32 v1, v1\n" \
"v_mfma_f32_16x16x32_bf16 a[32:35], a[192:195], v[72:75], a[32:35]\n" \
"s_add_u32 m0, s73, 0x4000\n" \
"ds_read_b128 a[244:247], v247 offset:30720\n" \
"v_add_f32 v242, v242, v1\n" \
"v_mfma_f32_16x16x32_bf16 a[32:35], a[196:199], v[104:107], a[32:35]\n" \
"ds_read_b128 a[248:251], v247 offset:23552\n" \
"global_load_lds_dwordx4 v250, s[36:37] offset:0 sc1\n" \
"v_mfma_f32_16x16x32_bf16 v[128:131], a[192:195], v[80:83], v[128:131]\n" \
"ds_read_b128 a[252:255], v247 offset:31744\n" \
"v_exp_f32 v2, v2\n" \
"v_mfma_f32_16x16x32_bf16 v[128:131], a[196:199], v[112:115], v[128:131]\n" \
"v_add_f32 v242, v242, v2\n" \
"v_cvt_pk_bf16_f32 v0, v0, v1\n" \
"v_mfma_f32_16x16x32_bf16 v[160:163], a[192:195], v[88:91], v[160:163]\n" \
"v_exp_f32 v3, v3\n" \
"v_mfma_f32_16x16x32_bf16 v[160:163], a[196:199], v[120:123], v[160:163]\n" \
"ds_read_b128 a[192:195], v246 offset:16384\n" \
"v_xor_b32 v247, 0x8000, v247\n" \
"v_add_f32 v242, v242, v3\n" \
"v_mfma_f32_16x16x32_bf16 v[164:167], a[200:203], v[88:91], v[164:167]\n" \
"v_exp_f32 v4, v4\n" \
"v_mfma_f32_16x16x32_bf16 v[164:167], a[204:207], v[120:123], v[164:167]\n" \
"v_add_f32 v242, v242, v4\n" \
"v_cvt_pk_bf16_f32 v1, v2, v3\n" \
"v_mfma_f32_16x16x32_bf16 v[132:135], a[200:203], v[80:83], v[132:135]\n" \
"v_exp_f32 v5, v5\n" \
"v_mfma_f32_16x16x32_bf16 v[132:135], a[204:207], v[112:115], v[132:135]\n" \
"ds_read_b128 a[196:199], v246 offset:18432\n" \
"v_add_f32 v242, v242, v5\n" \
"v_mfma_f32_16x16x32_bf16 a[36:39], a[200:203], v[72:75], a[36:39]\n" \
"v_exp_f32 v6, v6\n" \
"v_mfma_f32_16x16x32_bf16 a[36:39], a[204:207], v[104:107], a[36:39]\n" \
"v_add_f32 v242, v242, v6\n" \
"v_cvt_pk_bf16_f32 v2, v4, v5\n" \
"v_mfma_f32_16x16x32_bf16 a[4:7], a[200:203], v[64:67], a[4:7]\n" \
"v_exp_f32 v7, v7\n" \
"v_mfma_f32_16x16x32_bf16 a[4:7], a[204:207], v[96:99], a[4:7]\n" \
"ds_read_b128 a[200:203], v246 offset:20480\n" \
"global_load_lds_dwordx4 v250, s[36:37] offset:1024 sc1\n" \
"v_add_f32 v242, v242, v7\n" \
"v_mfma_f32_16x16x32_bf16 a[8:11], a[208:211], v[64:67], a[8:11]\n" \
"v_exp_f32 v8, v8\n" \
"v_mfma_f32_16x16x32_bf16 a[8:11], a[212:215], v[96:99], a[8:11]\n" \
"v_add_f32 v243, v243, v8\n" \
"v_cvt_pk_bf16_f32 v3, v6, v7\n" \
"v_mfma_f32_16x16x32_bf16 a[40:43], a[208:211], v[72:75], a[40:43]\n" \
"v_exp_f32 v9, v9\n" \
"v_mfma_f32_16x16x32_bf16 a[40:43], a[212:215], v[104:107], a[40:43]\n" \
"ds_read_b128 a[204:207], v246 offset:22528\n" \
"v_add_f32 v243, v243, v9\n" \
"v_mfma_f32_16x16x32_bf16 v[136:139], a[208:211], v[80:83], v[136:139]\n" \
"v_exp_f32 v10, v10\n" \
"v_mfma_f32_16x16x32_bf16 v[136:139], a[212:215], v[112:115], v[136:139]\n" \
"v_add_f32 v243, v243, v10\n" \
"v_cvt_pk_bf16_f32 v8, v8, v9\n" \
"v_mfma_f32_16x16x32_bf16 v[168:171], a[208:211], v[88:91], v[168:171]\n" \
"v_exp_f32 v11, v11\n" \
"v_mfma_f32_16x16x32_bf16 v[168:171], a[212:215], v[120:123], v[168:171]\n" \
"ds_read_b128 a[208:211], v246 offset:16640\n" \
"v_add_f32 v243, v243, v11\n" \
"v_mfma_f32_16x16x32_bf16 v[172:175], a[216:219], v[88:91], v[172:175]\n" \
"v_exp_f32 v12, v12\n" \
"v_mfma_f32_16x16x32_bf16 v[172:175], a[220:223], v[120:123], v[172:175]\n" \
"v_add_f32 v243, v243, v12\n" \
"v_cvt_pk_bf16_f32 v9, v10, v11\n" \
"v_mfma_f32_16x16x32_bf16 v[140:143], a[216:219], v[80:83], v[140:143]\n" \
"v_exp_f32 v13, v13\n" \
"v_mfma_f32_16x16x32_bf16 v[140:143], a[220:223], v[112:115], v[140:143]\n" \
"ds_read_b128 a[212:215], v246 offset:18688\n" \
"global_load_lds_dwordx4 v250, s[36:37] offset:2048 sc1\n" \
"v_add_f32 v243, v243, v13\n" \
"v_mfma_f32_16x16x32_bf16 a[44:47], a[216:219], v[72:75], a[44:47]\n" \
"v_exp_f32 v14, v14\n" \
"v_mfma_f32_16x16x32_bf16 a[44:47], a[220:223], v[104:107], a[44:47]\n" \
"v_add_f32 v243, v243, v14\n" \
"v_cvt_pk_bf16_f32 v10, v12, v13\n" \
"v_mfma_f32_16x16x32_bf16 a[12:15], a[216:219], v[64:67], a[12:15]\n" \
"s_waitcnt lgkmcnt(6)\n" \
"v_exp_f32 v15, v15\n" \
"v_mfma_f32_16x16x32_bf16 a[12:15], a[220:223], v[96:99], a[12:15]\n" \
"ds_read_b128 a[216:219], v246 offset:20736\n" \
"v_add_f32 v243, v243, v15\n" \
"v_mfma_f32_16x16x32_bf16 a[16:19], a[224:227], v[64:67], a[16:19]\n" \
"v_exp_f32 v16, v16\n" \
"v_mfma_f32_16x16x32_bf16 a[16:19], a[228:231], v[96:99], a[16:19]\n" \
"v_add_f32 v244, v244, v16\n" \
"v_cvt_pk_bf16_f32 v11, v14, v15\n" \
"v_mfma_f32_16x16x32_bf16 a[48:51], a[224:227], v[72:75], a[48:51]\n" \
"v_exp_f32 v17, v17\n" \
"v_mfma_f32_16x16x32_bf16 a[48:51], a[228:231], v[104:107], a[48:51]\n" \
"ds_read_b128 a[220:223], v246 offset:22784\n" \
"global_load_lds_dwordx4 v250, s[36:37] offset:3072 sc1\n" \
"v_add_f32 v244, v244, v17\n" \
"v_mfma_f32_16x16x32_bf16 v[144:147], a[224:227], v[80:83], v[144:147]\n" \
"s_add_u32 s36, s36, 17408\n" \
"v_exp_f32 v18, v18\n" \
"v_mfma_f32_16x16x32_bf16 v[144:147], a[228:231], v[112:115], v[144:147]\n" \
"s_xor_b32 s73, s73, 0x8000\n" \
"v_add_f32 v244, v244, v18\n" \
"v_cvt_pk_bf16_f32 v16, v16, v17\n" \
"v_mfma_f32_16x16x32_bf16 v[176:179], a[224:227], v[88:91], v[176:179]\n" \
"s_xor_b64 s[52:53], s[44:45], s[42:43]\n" \
"v_exp_f32 v19, v19\n" \
"v_mfma_f32_16x16x32_bf16 v[176:179], a[228:231], v[120:123], v[176:179]\n" \
"s_xor_b64 s[54:55], s[46:47], s[42:43]\n" \
"ds_read_b128 a[224:227], v246 offset:24576\n" \
"v_add_f32 v244, v244, v19\n" \
"v_mfma_f32_16x16x32_bf16 v[180:183], a[232:235], v[88:91], v[180:183]\n" \
"s_or_b64 s[52:53], s[52:53], s[54:55]\n" \
"v_exp_f32 v20, v20\n" \
"v_mfma_f32_16x16x32_bf16 v[180:183], a[236:239], v[120:123], v[180:183]\n" \
"s_xor_b64 s[54:55], s[48:49], s[42:43]\n" \
"v_add_f32 v244, v244, v20\n" \
"v_cvt_pk_bf16_f32 v17, v18, v19\n" \
"v_mfma_f32_16x16x32_bf16 v[148:151], a[232:235], v[80:83], v[148:151]\n" \
"s_or_b64 s[52:53], s[52:53], s[54:55]\n" \
"v_exp_f32 v21, v21\n" \
"v_mfma_f32_16x16x32_bf16 v[148:151], a[236:239], v[112:115], v[148:151]\n" \
"s_xor_b64 s[54:55], s[50:51], s[42:43]\n" \
"ds_read_b128 a[228:231], v246 offset:26624\n" \
"v_add_f32 v244, v244, v21\n" \
"v_exp_f32 v22, v22\n" \
"v_mfma_f32_16x16x32_bf16 a[52:55], a[232:235], v[72:75], a[52:55]\n" \
"s_or_b64 s[52:53], s[52:53], s[54:55]\n" \
"v_add_f32 v244, v244, v22\n" \
"v_cvt_pk_bf16_f32 v18, v20, v21\n" \
"v_mfma_f32_16x16x32_bf16 a[52:55], a[236:239], v[104:107], a[52:55]\n" \
"s_and_b64 s[52:53], s[52:53], s[8:9]\n" \
"s_cbranch_scc1 Lrs0_%=\n" \
"Lrr0_%=:\n" \
"v_mfma_f32_16x16x32_bf16 a[20:23], a[232:235], v[64:67], a[20:23]\n" \
"s_mov_b64 s[8:9], s[10:11]\n" \
"v_exp_f32 v23, v23\n" \
"v_mfma_f32_16x16x32_bf16 a[20:23], a[236:239], v[96:99], a[20:23]\n" \
"ds_read_b128 a[232:235], v246 offset:28672\n" \
"Lpg5534_%=:\n" \
"s_cbranch_execz Lpg6771_%=\n" \
"v_add_f32 v244, v244, v23\n" \
"v_mfma_f32_16x16x32_bf16 a[24:27], a[240:243], v[64:67], a[24:27]\n" \
"v_exp_f32 v24, v24\n" \
"v_mfma_f32_16x16x32_bf16 a[24:27], a[244:247], v[96:99], a[24:27]\n" \
"v_add_f32 v245, v245, v24\n" \
"v_cvt_pk_bf16_f32 v19, v22, v23\n" \
"v_mfma_f32_16x16x32_bf16 a[56:59], a[240:243], v[72:75], a[56:59]\n" \
"v_exp_f32 v25, v25\n" \
"v_mfma_f32_16x16x32_bf16 a[56:59], a[244:247], v[104:107], a[56:59]\n" \
"ds_read_b128 a[236:239], v246 offset:30720\n" \
"v_add_f32 v245, v245, v25\n" \
"v_mfma_f32_16x16x32_bf16 v[152:155], a[240:243], v[80:83], v[152:155]\n" \
"v_exp_f32 v26, v26\n" \
"v_mfma_f32_16x16x32_bf16 v[152:155], a[244:247], v[112:115], v[152:155]\n" \
"v_add_f32 v245, v245, v26\n" \
"v_cvt_pk_bf16_f32 v24, v24, v25\n" \
"v_mfma_f32_16x16x32_bf16 v[184:187], a[240:243], v[88:91], v[184:187]\n" \
"v_exp_f32 v27, v27\n" \
"v_mfma_f32_16x16x32_bf16 v[184:187], a[244:247], v[120:123], v[184:187]\n" \
"ds_read_b128 a[240:243], v246 offset:24832\n" \
"v_add_f32 v245, v245, v27\n" \
"v_mfma_f32_16x16x32_bf16 v[188:191], a[248:251], v[88:91], v[188:191]\n" \
"v_exp_f32 v28, v28\n" \
"v_mfma_f32_16x16x32_bf16 v[188:191], a[252:255], v[120:123], v[188:191]\n" \
"v_add_f32 v245, v245, v28\n" \
"v_cvt_pk_bf16_f32 v25, v26, v27\n" \
"v_mfma_f32_16x16x32_bf16 v[156:159], a[248:251], v[80:83], v[156:159]\n" \
"v_exp_f32 v29, v29\n" \
"v_mfma_f32_16x16x32_bf16 v[156:159], a[252:255], v[112:115], v[156:159]\n" \
"ds_read_b128 a[244:247], v246 offset:26880\n" \
"v_add_f32 v245, v245, v29\n" \
"v_mfma_f32_16x16x32_bf16 a[60:63], a[248:251], v[72:75], a[60:63]\n" \
"v_exp_f32 v30, v30\n" \
"v_mfma_f32_16x16x32_bf16 a[60:63], a[252:255], v[104:107], a[60:63]\n" \
"v_add_f32 v245, v245, v30\n" \
"v_cvt_pk_bf16_f32 v26, v28, v29\n" \
"v_mfma_f32_16x16x32_bf16 a[28:31], a[248:251], v[64:67], a[28:31]\n" \
"s_waitcnt lgkmcnt(6)\n" \
"v_exp_f32 v31, v31\n" \
"v_mfma_f32_16x16x32_bf16 a[28:31], a[252:255], v[96:99], a[28:31]\n" \
"ds_read_b128 a[248:251], v246 offset:28928\n" \
"v_add_f32 v245, v245, v31\n" \
"LA1_%=:\n" \
"Lpg5578_%=:\n" \
"s_cbranch_execz Lpg7030_%=\n" \
"v_mfma_f32_16x16x32_bf16 v[64:67], a[192:195], a[128:131], v[224:227]\n" \
"v_exp_f32 v32, v32\n" \
"v_mfma_f32_16x16x32_bf16 v[64:67], a[196:199], a[132:135], v[64:67]\n" \
"v_cvt_pk_bf16_f32 v27, v30, v31\n" \
"v_add_f32 v242, v242, v32\n" \
"v_mfma_f32_16x16x32_bf16 v[64:67], a[200:203], a[136:139], v[64:67]\n" \
"s_mov_b32 m0, s74\n" \
"v_exp_f32 v33, v33\n" \
"v_mfma_f32_16x16x32_bf16 v[64:67], a[204:207], a[140:143], v[64:67]\n" \
"ds_read_b128 a[252:255], v246 offset:30976\n" \
"global_load_lds_dwordx4 v250, s[38:39] offset:0 sc1\n" \
"v_add_f32 v242, v242, v33\n" \
"v_mfma_f32_16x16x32_bf16 v[72:75], a[200:203], a[152:155], v[228:231]\n" \
"v_exp_f32 v34, v34\n" \
"v_mfma_f32_16x16x32_bf16 v[72:75], a[204:207], a[156:159], v[72:75]\n" \
"v_add_f32 v242, v242, v34\n" \
"v_cvt_pk_bf16_f32 v32, v32, v33\n" \
"v_mfma_f32_16x16x32_bf16 v[72:75], a[192:195], a[144:147], v[72:75]\n" \
"v_exp_f32 v35, v35\n" \
"v_mfma_f32_16x16x32_bf16 v[72:75], a[196:199], a[148:151], v[72:75]\n" \
"v_add_f32 v242, v242, v35\n" \
"v_exp_f32 v36, v36\n" \
"v_mfma_f32_16x16x32_bf16 v[80:83], a[192:195], a[160:163], v[232:235]\n" \
"v_add_f32 v242, v242, v36\n" \
"v_cvt_pk_bf16_f32 v33, v34, v35\n" \
"v_mfma_f32_16x16x32_bf16 v[80:83], a[196:199], a[164:167], v[80:83]\n" \
"v_exp_f32 v37, v37\n" \
"v_mfma_f32_16x16x32_bf16 v[80:83], a[200:203], a[168:171], v[80:83]\n" \
"v_add_f32 v242, v242, v37\n" \
"v_mfma_f32_16x16x32_bf16 v[80:83], a[204:207], a[172:175], v[80:83]\n" \
"v_exp_f32 v38, v38\n" \
"v_mfma_f32_16x16x32_bf16 v[88:91], a[200:203], a[184:187], v[236:239]\n" \
"v_add_f32 v242, v242, v38\n" \
"v_cvt_pk_bf16_f32 v34, v36, v37\n" \
"v_mfma_f32_16x16x32_bf16 v[88:91], a[204:207], a[188:191], v[88:91]\n" \
"v_exp_f32 v39, v39\n" \
"v_mfma_f32_16x16x32_bf16 v[88:91], a[192:195], a[176:179], v[88:91]\n" \
"v_add_f32 v242, v242, v39\n" \
"v_mfma_f32_16x16x32_bf16 v[88:91], a[196:199], a[180:183], v[88:91]\n" \
"ds_read_b128 a[192:195], v247 offset:0\n" \
"v_xor_b32 v246, 0x8000, v246\n" \
"v_mfma_f32_16x16x32_bf16 v[92:95], a[208:211], a[176:179], v[236:239]\n" \
"v_exp_f32 v40, v40\n" \
"v_mfma_f32_16x16x32_bf16 v[92:95], a[212:215], a[180:183], v[92:95]\n" \
"v_add_f32 v243, v243, v40\n" \
"v_cvt_pk_bf16_f32 v35, v38, v39\n" \
"v_mfma_f32_16x16x32_bf16 v[92:95], a[216:219], a[184:187], v[92:95]\n" \
"v_exp_f32 v41, v41\n" \
"v_mfma_f32_16x16x32_bf16 v[92:95], a[220:223], a[188:191], v[92:95]\n" \
"ds_read_b128 a[196:199], v247 offset:8192\n" \
"global_load_lds_dwordx4 v250, s[38:39] offset:1024 sc1\n" \
"v_add_f32 v243, v243, v41\n" \
"v_mfma_f32_16x16x32_bf16 v[84:87], a[216:219], a[168:171], v[232:235]\n" \
"v_exp_f32 v42, v42\n" \
"v_mfma_f32_16x16x32_bf16 v[84:87], a[220:223], a[172:175], v[84:87]\n" \
"v_add_f32 v243, v243, v42\n" \
"v_cvt_pk_bf16_f32 v40, v40, v41\n" \
"v_mfma_f32_16x16x32_bf16 v[84:87], a[208:211], a[160:163], v[84:87]\n" \
"v_exp_f32 v43, v43\n" \
"v_mfma_f32_16x16x32_bf16 v[84:87], a[212:215], a[164:167], v[84:87]\n" \
"ds_read_b128 a[200:203], v247 offset:1024\n" \
"v_add_f32 v243, v243, v43\n" \
"v_mfma_f32_16x16x32_bf16 v[76:79], a[208:211], a[144:147], v[228:231]\n" \
"v_exp_f32 v44, v44\n" \
"v_mfma_f32_16x16x32_bf16 v[76:79], a[212:215], a[148:151], v[76:79]\n" \
"v_add_f32 v243, v243, v44\n" \
"v_cvt_pk_bf16_f32 v41, v42, v43\n" \
"v_mfma_f32_16x16x32_bf16 v[76:79], a[216:219], a[152:155], v[76:79]\n" \
"v_exp_f32 v45, v45\n" \
"v_mfma_f32_16x16x32_bf16 v[76:79], a[220:223], a[156:159], v[76:79]\n" \
"ds_read_b128 a[204:207], v247 offset:9216\n" \
"global_load_lds_dwordx4 v250, s[38:39] offset:2048 sc1\n" \
"v_add_f32 v243, v243, v45\n" \
"v_mfma_f32_16x16x32_bf16 v[68:71], a[216:219], a[136:139], v[224:227]\n" \
"v_exp_f32 v46, v46\n" \
"v_mfma_f32_16x16x32_bf16 v[68:71], a[220:223], a[140:143], v[68:71]\n" \
"v_add_f32 v243, v243, v46\n" \
"v_cvt_pk_bf16_f32 v42, v44, v45\n" \
"v_mfma_f32_16x16x32_bf16 v[68:71], a[208:211], a[128:131], v[68:71]\n" \
"s_waitcnt lgkmcnt(4)\n" \
"v_exp_f32 v47, v47\n" \
"v_mfma_f32_16x16x32_bf16 v[68:71], a[212:215], a[132:135], v[68:71]\n" \
"ds_read_b128 a[208:211], v247 offset:2048\n" \
"v_add_f32 v243, v243, v47\n" \
"v_mfma_f32_16x16x32_bf16 v[96:99], a[224:227], a[128:131], v[224:227]\n" \
"v_exp_f32 v48, v48\n" \
"v_mfma_f32_16x16x32_bf16 v[96:99], a[228:231], a[132:135], v[96:99]\n" \
"v_add_f32 v244, v244, v48\n" \
"v_cvt_pk_bf16_f32 v43, v46, v47\n" \
"v_mfma_f32_16x16x32_bf16 v[96:99], a[232:235], a[136:139], v[96:99]\n" \
"v_exp_f32 v49, v49\n" \
"v_mfma_f32_16x16x32_bf16 v[96:99], a[236:239], a[140:143], v[96:99]\n" \
"ds_read_b128 a[212:215], v247 offset:10240\n" \
"v_add_f32 v244, v244, v49\n" \
"v_mfma_f32_16x16x32_bf16 v[104:107], a[232:235], a[152:155], v[228:231]\n" \
"v_exp_f32 v50, v50\n" \
"v_mfma_f32_16x16x32_bf16 v[104:107], a[236:239], a[156:159], v[104:107]\n" \
"v_add_f32 v244, v244, v50\n" \
"v_cvt_pk_bf16_f32 v48, v48, v49\n" \
"v_mfma_f32_16x16x32_bf16 v[104:107], a[224:227], a[144:147], v[104:107]\n" \
"v_exp_f32 v51, v51\n" \
"v_mfma_f32_16x16x32_bf16 v[104:107], a[228:231], a[148:151], v[104:107]\n" \
"ds_read_b128 a[216:219], v247 offset:3072\n" \
"global_load_lds_dwordx4 v250, s[38:39] offset:3072 sc1\n" \
"v_add_f32 v244, v244, v51\n" \
"v_mfma_f32_16x16x32_bf16 v[112:115], a[224:227], a[160:163], v[232:235]\n" \
"s_add_u32 s38, s38, 17408\n" \
"v_exp_f32 v52, v52\n" \
"v_mfma_f32_16x16x32_bf16 v[112:115], a[228:231], a[164:167], v[112:115]\n" \
"v_add_f32 v244, v244, v52\n" \
"v_cvt_pk_bf16_f32 v49, v50, v51\n" \
"v_mfma_f32_16x16x32_bf16 v[112:115], a[232:235], a[168:171], v[112:115]\n" \
"v_exp_f32 v53, v53\n" \
"v_mfma_f32_16x16x32_bf16 v[112:115], a[236:239], a[172:175], v[112:115]\n" \
"ds_read_b128 a[220:223], v247 offset:11264\n" \
"v_add_f32 v244, v244, v53\n" \
"v_exp_f32 v54, v54\n" \
"v_mfma_f32_16x16x32_bf16 v[120:123], a[232:235], a[184:187], v[236:239]\n" \
"global_load_dword v253, v252, s[40:41]\n" \
"v_mfma_f32_16x16x32_bf16 v[120:123], a[236:239], a[188:191], v[120:123]\n" \
"s_sub_u32 s80, s80, 1\n" \
"v_add_f32 v244, v244, v54\n" \
"v_cvt_pk_bf16_f32 v50, v52, v53\n" \
"v_mfma_f32_16x16x32_bf16 v[120:123], a[224:227], a[176:179], v[120:123]\n" \
"s_cmp_gt_i32 s80, 0\n" \
"v_exp_f32 v55, v55\n" \
"v_mfma_f32_16x16x32_bf16 v[120:123], a[228:231], a[180:183], v[120:123]\n" \
"s_cselect_b32 s81, 0x80000, 0\n" \
"ds_read_b128 a[224:227], v247 offset:4096\n" \
"v_add_f32 v244, v244, v55\n" \
"v_mfma_f32_16x16x32_bf16 v[124:127], a[240:243], a[176:179], v[236:239]\n" \
"s_add_u32 s40, s40, s81\n" \
"v_exp_f32 v56, v56\n" \
"v_mfma_f32_16x16x32_bf16 v[124:127], a[244:247], a[180:183], v[124:127]\n" \
"s_addc_u32 s41, s41, 0\n" \
"v_add_f32 v245, v245, v56\n" \
"v_cvt_pk_bf16_f32 v51, v54, v55\n" \
"v_mfma_f32_16x16x32_bf16 v[124:127], a[248:251], a[184:187], v[124:127]\n" \
"v_exp_f32 v57, v57\n" \
"v_mfma_f32_16x16x32_bf16 v[124:127], a[252:255], a[188:191], v[124:127]\n" \
"ds_read_b128 a[228:231], v247 offset:12288\n" \
"v_add_f32 v245, v245, v57\n" \
"v_mfma_f32_16x16x32_bf16 v[116:119], a[248:251], a[168:171], v[232:235]\n" \
"v_exp_f32 v58, v58\n" \
"v_mfma_f32_16x16x32_bf16 v[116:119], a[252:255], a[172:175], v[116:119]\n" \
"v_add_f32 v245, v245, v58\n" \
"v_cvt_pk_bf16_f32 v56, v56, v57\n" \
"v_mfma_f32_16x16x32_bf16 v[116:119], a[240:243], a[160:163], v[116:119]\n" \
"v_exp_f32 v59, v59\n" \
"v_mfma_f32_16x16x32_bf16 v[116:119], a[244:247], a[164:167], v[116:119]\n" \
"ds_read_b128 a[232:235], v247 offset:5120\n" \
"v_add_f32 v245, v245, v59\n" \
"v_mfma_f32_16x16x32_bf16 v[108:111], a[240:243], a[144:147], v[228:231]\n" \
"v_exp_f32 v60, v60\n" \
"v_mfma_f32_16x16x32_bf16 v[108:111], a[244:247], a[148:151], v[108:111]\n" \
"v_add_f32 v245, v245, v60\n" \
"v_cvt_pk_bf16_f32 v57, v58, v59\n" \
"v_mfma_f32_16x16x32_bf16 v[108:111], a[248:251], a[152:155], v[108:111]\n" \
"v_exp_f32 v61, v61\n" \
"v_mfma_f32_16x16x32_bf16 v[108:111], a[252:255], a[156:159], v[108:111]\n" \
"ds_read_b128 a[236:239], v247 offset:13312\n" \
"v_add_f32 v245, v245, v61\n" \
"v_mfma_f32_16x16x32_bf16 v[100:103], a[248:251], a[136:139], v[224:227]\n" \
"v_exp_f32 v62, v62\n" \
"v_mfma_f32_16x16x32_bf16 v[100:103], a[252:255], a[140:143], v[100:103]\n" \
"v_add_f32 v245, v245, v62\n" \
"v_cvt_pk_bf16_f32 v58, v60, v61\n" \
"v_mfma_f32_16x16x32_bf16 v[100:103], a[240:243], a[128:131], v[100:103]\n" \
"s_waitcnt vmcnt(10) lgkmcnt(4)\n" \
"v_exp_f32 v63, v63\n" \
"v_mfma_f32_16x16x32_bf16 v[100:103], a[244:247], a[132:135], v[100:103]\n" \
"s_barrier\n" \
"ds_read_b128 a[240:243], v247 offset:6144\n" \
"v_add_f32 v245, v245, v63\n" \
"v_mfma_f32_16x16x32_bf16 a[0:3], a[192:195], v[0:3], a[0:3]\n" \
"buffer_store_dword v202, v203, s[56:59], s84 offen sc1\n" \
"v_mfma_f32_16x16x32_bf16 a[0:3], a[196:199], v[32:35], a[0:3]\n" \
"s_mov_b32 s84, 0x20000000\n" \
"v_cvt_pk_bf16_f32 v59, v62, v63\n" \
"v_exp_f32 v64, v64\n" \
"v_mfma_f32_16x16x32_bf16 a[32:35], a[192:195], v[8:11], a[32:35]\n" \
"s_mov_b32 m0, s73\n" \
"ds_read_b128 a[244:247], v247 offset:14336\n" \
"v_add_f32 v242, v242, v64\n" \
"v_mfma_f32_16x16x32_bf16 a[32:35], a[196:199], v[40:43], a[32:35]\n" \
"ds_read_b128 a[248:251], v247 offset:7168\n" \
"global_load_lds_dwordx4 v250, s[36:37] offset:0 sc1\n" \
"v_exp_f32 v65, v65\n" \
"v_mfma_f32_16x16x32_bf16 v[128:131], a[192:195], v[16:19], v[128:131]\n" \
"ds_read_b128 a[252:255], v247 offset:15360\n" \
"v_add_f32 v242, v242, v65\n" \
"v_exp_f32 v66, v66\n" \
"v_mfma_f32_16x16x32_bf16 v[128:131], a[196:199], v[48:51], v[128:131]\n" \
"v_add_f32 v242, v242, v66\n" \
"v_cvt_pk_bf16_f32 v64, v64, v65\n" \
"v_mfma_f32_16x16x32_bf16 v[160:163], a[192:195], v[24:27], v[160:163]\n" \
"v_exp_f32 v67, v67\n" \
"v_mfma_f32_16x16x32_bf16 v[160:163], a[196:199], v[56:59], v[160:163]\n" \
"ds_read_b128 a[192:195], v246 offset:0\n" \
"v_add_f32 v242, v242, v67\n" \
"v_mfma_f32_16x16x32_bf16 v[164:167], a[200:203], v[24:27], v[164:167]\n" \
"v_exp_f32 v68, v68\n" \
"v_mfma_f32_16x16x32_bf16 v[164:167], a[204:207], v[56:59], v[164:167]\n" \
"v_add_f32 v242, v242, v68\n" \
"v_cvt_pk_bf16_f32 v65, v66, v67\n" \
"v_mfma_f32_16x16x32_bf16 v[132:135], a[200:203], v[16:19], v[132:135]\n" \
"v_exp_f32 v69, v69\n" \
"v_mfma_f32_16x16x32_bf16 v[132:135], a[204:207], v[48:51], v[132:135]\n" \
"ds_read_b128 a[196:199], v246 offset:2048\n" \
"v_add_f32 v242, v242, v69\n" \
"v_mfma_f32_16x16x32_bf16 a[36:39], a[200:203], v[8:11], a[36:39]\n" \
"v_exp_f32 v70, v70\n" \
"v_mfma_f32_16x16x32_bf16 a[36:39], a[204:207], v[40:43], a[36:39]\n" \
"v_add_f32 v242, v242, v70\n" \
"v_cvt_pk_bf16_f32 v66, v68, v69\n" \
"v_mfma_f32_16x16x32_bf16 a[4:7], a[200:203], v[0:3], a[4:7]\n" \
"v_exp_f32 v71, v71\n" \
"v_mfma_f32_16x16x32_bf16 a[4:7], a[204:207], v[32:35], a[4:7]\n" \
"ds_read_b128 a[200:203], v246 offset:4096\n" \
"global_load_lds_dwordx4 v250, s[36:37] offset:1024 sc1\n" \
"v_add_f32 v242, v242, v71\n" \
"v_mfma_f32_16x16x32_bf16 a[8:11], a[208:211], v[0:3], a[8:11]\n" \
"v_exp_f32 v72, v72\n" \
"v_mfma_f32_16x16x32_bf16 a[8:11], a[212:215], v[32:35], a[8:11]\n" \
"v_add_f32 v243, v243, v72\n" \
"v_cvt_pk_bf16_f32 v67, v70, v71\n" \
"v_mfma_f32_16x16x32_bf16 a[40:43], a[208:211], v[8:11], a[40:43]\n" \
"v_exp_f32 v73, v73\n" \
"v_mfma_f32_16x16x32_bf16 a[40:43], a[212:215], v[40:43], a[40:43]\n" \
"ds_read_b128 a[204:207], v246 offset:6144\n" \
"v_add_f32 v243, v243, v73\n" \
"v_mfma_f32_16x16x32_bf16 v[136:139], a[208:211], v[16:19], v[136:139]\n" \
"v_exp_f32 v74, v74\n" \
"v_mfma_f32_16x16x32_bf16 v[136:139], a[212:215], v[48:51], v[136:139]\n" \
"v_add_f32 v243, v243, v74\n" \
"v_cvt_pk_bf16_f32 v72, v72, v73\n" \
"v_mfma_f32_16x16x32_bf16 v[168:171], a[208:211], v[24:27], v[168:171]\n" \
"v_exp_f32 v75, v75\n" \
"v_mfma_f32_16x16x32_bf16 v[168:171], a[212:215], v[56:59], v[168:171]\n" \
"ds_read_b128 a[208:211], v246 offset:256\n" \
"v_add_f32 v243, v243, v75\n" \
"v_mfma_f32_16x16x32_bf16 v[172:175], a[216:219], v[24:27], v[172:175]\n" \
"v_exp_f32 v76, v76\n" \
"v_mfma_f32_16x16x32_bf16 v[172:175], a[220:223], v[56:59], v[172:175]\n" \
"v_add_f32 v243, v243, v76\n" \
"v_cvt_pk_bf16_f32 v73, v74, v75\n" \
"v_mfma_f32_16x16x32_bf16 v[140:143], a[216:219], v[16:19], v[140:143]\n" \
"v_exp_f32 v77, v77\n" \
"v_mfma_f32_16x16x32_bf16 v[140:143], a[220:223], v[48:51], v[140:143]\n" \
"ds_read_b128 a[212:215], v246 offset:2304\n" \
"global_load_lds_dwordx4 v250, s[36:37] offset:2048 sc1\n" \
"v_add_f32 v243, v243, v77\n" \
"v_mfma_f32_16x16x32_bf16 a[44:47], a[216:219], v[8:11], a[44:47]\n" \
"v_exp_f32 v78, v78\n" \
"v_mfma_f32_16x16x32_bf16 a[44:47], a[220:223], v[40:43], a[44:47]\n" \
"v_add_f32 v243, v243, v78\n" \
"v_cvt_pk_bf16_f32 v74, v76, v77\n" \
"v_mfma_f32_16x16x32_bf16 a[12:15], a[216:219], v[0:3], a[12:15]\n" \
"s_waitcnt lgkmcnt(6)\n" \
"v_exp_f32 v79, v79\n" \
"v_mfma_f32_16x16x32_bf16 a[12:15], a[220:223], v[32:35], a[12:15]\n" \
"ds_read_b128 a[216:219], v246 offset:4352\n" \
"v_add_f32 v243, v243, v79\n" \
"v_mfma_f32_16x16x32_bf16 a[16:19], a[224:227], v[0:3], a[16:19]\n" \
"v_exp_f32 v80, v80\n" \
"v_mfma_f32_16x16x32_bf16 a[16:19], a[228:231], v[32:35], a[16:19]\n" \
"v_add_f32 v244, v244, v80\n" \
"v_cvt_pk_bf16_f32 v75, v78, v79\n" \
"v_mfma_f32_16x16x32_bf16 a[48:51], a[224:227], v[8:11], a[48:51]\n" \
"v_exp_f32 v81, v81\n" \
"v_mfma_f32_16x16x32_bf16 a[48:51], a[228:231], v[40:43], a[48:51]\n" \
"ds_read_b128 a[220:223], v246 offset:6400\n" \
"v_add_f32 v244, v244, v81\n" \
"v_mfma_f32_16x16x32_bf16 v[144:147], a[224:227], v[16:19], v[144:147]\n" \
"v_exp_f32 v82, v82\n" \
"v_mfma_f32_16x16x32_bf16 v[144:147], a[228:231], v[48:51], v[144:147]\n" \
"v_add_f32 v244, v244, v82\n" \
"v_cvt_pk_bf16_f32 v80, v80, v81\n" \
"v_mfma_f32_16x16x32_bf16 v[176:179], a[224:227], v[24:27], v[176:179]\n" \
"v_exp_f32 v83, v83\n" \
"v_mfma_f32_16x16x32_bf16 v[176:179], a[228:231], v[56:59], v[176:179]\n" \
"ds_read_b128 a[224:227], v246 offset:8192\n" \
"global_load_lds_dwordx4 v250, s[36:37] offset:3072 sc1\n" \
"v_add_f32 v244, v244, v83\n" \
"v_mfma_f32_16x16x32_bf16 v[180:183], a[232:235], v[24:27], v[180:183]\n" \
"s_add_u32 s36, s36, 17408\n" \
"v_exp_f32 v84, v84\n" \
"v_mfma_f32_16x16x32_bf16 v[180:183], a[236:239], v[56:59], v[180:183]\n" \
"v_add_f32 v244, v244, v84\n" \
"v_cvt_pk_bf16_f32 v81, v82, v83\n" \
"v_mfma_f32_16x16x32_bf16 v[148:151], a[232:235], v[16:19], v[148:151]\n" \
"v_exp_f32 v85, v85\n" \
"v_mfma_f32_16x16x32_bf16 v[148:151], a[236:239], v[48:51], v[148:151]\n" \
"ds_read_b128 a[228:231], v246 offset:10240\n" \
"v_add_f32 v244, v244, v85\n" \
"v_mfma_f32_16x16x32_bf16 a[52:55], a[232:235], v[8:11], a[52:55]\n" \
"v_exp_f32 v86, v86\n" \
"v_mfma_f32_16x16x32_bf16 a[52:55], a[236:239], v[40:43], a[52:55]\n" \
"v_add_f32 v244, v244, v86\n" \
"v_cvt_pk_bf16_f32 v82, v84, v85\n" \
"v_mfma_f32_16x16x32_bf16 a[20:23], a[232:235], v[0:3], a[20:23]\n" \
"v_exp_f32 v87, v87\n" \
"v_mfma_f32_16x16x32_bf16 a[20:23], a[236:239], v[32:35], a[20:23]\n" \
"ds_read_b128 a[232:235], v246 offset:12288\n" \
"v_add_f32 v244, v244, v87\n" \
"v_mfma_f32_16x16x32_bf16 a[24:27], a[240:243], v[0:3], a[24:27]\n" \
"v_exp_f32 v88, v88\n" \
"v_mfma_f32_16x16x32_bf16 a[24:27], a[244:247], v[32:35], a[24:27]\n" \
"v_add_f32 v245, v245, v88\n" \
"v_cvt_pk_bf16_f32 v83, v86, v87\n" \
"v_mfma_f32_16x16x32_bf16 a[56:59], a[240:243], v[8:11], a[56:59]\n" \
"v_exp_f32 v89, v89\n" \
"v_mfma_f32_16x16x32_bf16 a[56:59], a[244:247], v[40:43], a[56:59]\n" \
"ds_read_b128 a[236:239], v246 offset:14336\n" \
"v_add_f32 v245, v245, v89\n" \
"v_mfma_f32_16x16x32_bf16 v[152:155], a[240:243], v[16:19], v[152:155]\n" \
"v_exp_f32 v90, v90\n" \
"v_mfma_f32_16x16x32_bf16 v[152:155], a[244:247], v[48:51], v[152:155]\n" \
"v_add_f32 v245, v245, v90\n" \
"v_cvt_pk_bf16_f32 v88, v88, v89\n" \
"v_mfma_f32_16x16x32_bf16 v[184:187], a[240:243], v[24:27], v[184:187]\n" \
"v_exp_f32 v91, v91\n" \
"v_mfma_f32_16x16x32_bf16 v[184:187], a[244:247], v[56:59], v[184:187]\n" \
"ds_read_b128 a[240:243], v246 offset:8448\n" \
"v_add_f32 v245, v245, v91\n" \
"v_mfma_f32_16x16x32_bf16 v[188:191], a[248:251], v[24:27], v[188:191]\n" \
"v_exp_f32 v92, v92\n" \
"v_mfma_f32_16x16x32_bf16 v[188:191], a[252:255], v[56:59], v[188:191]\n" \
"v_add_f32 v245, v245, v92\n" \
"v_cvt_pk_bf16_f32 v89, v90, v91\n" \
"v_mfma_f32_16x16x32_bf16 v[156:159], a[248:251], v[16:19], v[156:159]\n" \
"v_exp_f32 v93, v93\n" \
"v_mfma_f32_16x16x32_bf16 v[156:159], a[252:255], v[48:51], v[156:159]\n" \
"ds_read_b128 a[244:247], v246 offset:10496\n" \
"v_add_f32 v245, v245, v93\n" \
"v_mfma_f32_16x16x32_bf16 a[60:63], a[248:251], v[8:11], a[60:63]\n" \
"v_exp_f32 v94, v94\n" \
"v_mfma_f32_16x16x32_bf16 a[60:63], a[252:255], v[40:43], a[60:63]\n" \
"v_add_f32 v245, v245, v94\n" \
"v_cvt_pk_bf16_f32 v90, v92, v93\n" \
"v_mfma_f32_16x16x32_bf16 a[28:31], a[248:251], v[0:3], a[28:31]\n" \
"s_waitcnt lgkmcnt(6)\n" \
"v_exp_f32 v95, v95\n" \
"v_mfma_f32_16x16x32_bf16 a[28:31], a[252:255], v[32:35], a[28:31]\n" \
"ds_read_b128 a[248:251], v246 offset:12544\n" \
"v_add_f32 v245, v245, v95\n" \
"v_mfma_f32_16x16x32_bf16 v[0:3], a[192:195], a[128:131], v[224:227]\n" \
"v_exp_f32 v96, v96\n" \
"v_mfma_f32_16x16x32_bf16 v[0:3], a[196:199], a[132:135], v[0:3]\n" \
"v_cvt_pk_bf16_f32 v91, v94, v95\n" \
"v_add_f32 v242, v242, v96\n" \
"v_mfma_f32_16x16x32_bf16 v[0:3], a[200:203], a[136:139], v[0:3]\n" \
"s_add_u32 m0, s74, 0x4000\n" \
"v_exp_f32 v97, v97\n" \
"v_mfma_f32_16x16x32_bf16 v[0:3], a[204:207], a[140:143], v[0:3]\n" \
"ds_read_b128 a[252:255], v246 offset:14592\n" \
"global_load_lds_dwordx4 v250, s[38:39] offset:0 sc1\n" \
"v_add_f32 v242, v242, v97\n" \
"v_mfma_f32_16x16x32_bf16 v[8:11], a[200:203], a[152:155], v[228:231]\n" \
"v_exp_f32 v98, v98\n" \
"v_mfma_f32_16x16x32_bf16 v[8:11], a[204:207], a[156:159], v[8:11]\n" \
"v_add_f32 v242, v242, v98\n" \
"v_cvt_pk_bf16_f32 v96, v96, v97\n" \
"v_mfma_f32_16x16x32_bf16 v[8:11], a[192:195], a[144:147], v[8:11]\n" \
"v_exp_f32 v99, v99\n" \
"v_mfma_f32_16x16x32_bf16 v[8:11], a[196:199], a[148:151], v[8:11]\n" \
"v_add_f32 v242, v242, v99\n" \
"v_exp_f32 v100, v100\n" \
"v_mfma_f32_16x16x32_bf16 v[16:19], a[192:195], a[160:163], v[232:235]\n" \
"v_add_f32 v242, v242, v100\n" \
"v_cvt_pk_bf16_f32 v97, v98, v99\n" \
"v_mfma_f32_16x16x32_bf16 v[16:19], a[196:199], a[164:167], v[16:19]\n" \
"v_exp_f32 v101, v101\n" \
"v_mfma_f32_16x16x32_bf16 v[16:19], a[200:203], a[168:171], v[16:19]\n" \
"v_add_f32 v242, v242, v101\n" \
"v_mfma_f32_16x16x32_bf16 v[16:19], a[204:207], a[172:175], v[16:19]\n" \
"v_exp_f32 v102, v102\n" \
"v_mfma_f32_16x16x32_bf16 v[24:27], a[200:203], a[184:187], v[236:239]\n" \
"v_add_f32 v242, v242, v102\n" \
"v_cvt_pk_bf16_f32 v98, v100, v101\n" \
"v_mfma_f32_16x16x32_bf16 v[24:27], a[204:207], a[188:191], v[24:27]\n" \
"v_exp_f32 v103, v103\n" \
"v_mfma_f32_16x16x32_bf16 v[24:27], a[192:195], a[176:179], v[24:27]\n" \
"v_add_f32 v242, v242, v103\n" \
"v_mfma_f32_16x16x32_bf16 v[24:27], a[196:199], a[180:183], v[24:27]\n" \
"ds_read_b128 a[192:195], v247 offset:16384\n" \
"global_load_lds_dwordx4 v250, s[38:39] offset:1024 sc1\n" \
"v_mfma_f32_16x16x32_bf16 v[28:31], a[208:211], a[176:179], v[236:239]\n" \
"v_exp_f32 v104, v104\n" \
"v_mfma_f32_16x16x32_bf16 v[28:31], a[212:215], a[180:183], v[28:31]\n" \
"v_add_f32 v243, v243, v104\n" \
"v_cvt_pk_bf16_f32 v99, v102, v103\n" \
"v_mfma_f32_16x16x32_bf16 v[28:31], a[216:219], a[184:187], v[28:31]\n" \
"v_exp_f32 v105, v105\n" \
"v_mfma_f32_16x16x32_bf16 v[28:31], a[220:223], a[188:191], v[28:31]\n" \
"ds_read_b128 a[196:199], v247 offset:24576\n" \
"v_add_f32 v243, v243, v105\n" \
"v_mfma_f32_16x16x32_bf16 v[20:23], a[216:219], a[168:171], v[232:235]\n" \
"v_exp_f32 v106, v106\n" \
"v_mfma_f32_16x16x32_bf16 v[20:23], a[220:223], a[172:175], v[20:23]\n" \
"v_add_f32 v243, v243, v106\n" \
"v_cvt_pk_bf16_f32 v104, v104, v105\n" \
"v_mfma_f32_16x16x32_bf16 v[20:23], a[208:211], a[160:163], v[20:23]\n" \
"v_exp_f32 v107, v107\n" \
"v_mfma_f32_16x16x32_bf16 v[20:23], a[212:215], a[164:167], v[20:23]\n" \
"ds_read_b128 a[200:203], v247 offset:17408\n" \
"v_add_f32 v243, v243, v107\n" \
"v_mfma_f32_16x16x32_bf16 v[12:15], a[208:211], a[144:147], v[228:231]\n" \
"v_exp_f32 v108, v108\n" \
"v_mfma_f32_16x16x32_bf16 v[12:15], a[212:215], a[148:151], v[12:15]\n" \
"v_add_f32 v243, v243, v108\n" \
"v_cvt_pk_bf16_f32 v105, v106, v107\n" \
"v_mfma_f32_16x16x32_bf16 v[12:15], a[216:219], a[152:155], v[12:15]\n" \
"v_exp_f32 v109, v109\n" \
"v_mfma_f32_16x16x32_bf16 v[12:15], a[220:223], a[156:159], v[12:15]\n" \
"ds_read_b128 a[204:207], v247 offset:25600\n" \
"global_load_lds_dwordx4 v250, s[38:39] offset:2048 sc1\n" \
"v_add_f32 v243, v243, v109\n" \
"v_mfma_f32_16x16x32_bf16 v[4:7], a[216:219], a[136:139], v[224:227]\n" \
"v_exp_f32 v110, v110\n" \
"v_mfma_f32_16x16x32_bf16 v[4:7], a[220:223], a[140:143], v[4:7]\n" \
"v_add_f32 v243, v243, v110\n" \
"v_cvt_pk_bf16_f32 v106, v108, v109\n" \
"v_mfma_f32_16x16x32_bf16 v[4:7], a[208:211], a[128:131], v[4:7]\n" \
"s_waitcnt lgkmcnt(4)\n" \
"v_exp_f32 v111, v111\n" \
"v_mfma_f32_16x16x32_bf16 v[4:7], a[212:215], a[132:135], v[4:7]\n" \
"ds_read_b128 a[208:211], v247 offset:18432\n" \
"v_add_f32 v243, v243, v111\n" \
"v_mfma_f32_16x16x32_bf16 v[32:35], a[224:227], a[128:131], v[224:227]\n" \
"v_exp_f32 v112, v112\n" \
"v_mfma_f32_16x16x32_bf16 v[32:35], a[228:231], a[132:135], v[32:35]\n" \
"v_add_f32 v244, v244, v112\n" \
"v_cvt_pk_bf16_f32 v107, v110, v111\n" \
"v_mfma_f32_16x16x32_bf16 v[32:35], a[232:235], a[136:139], v[32:35]\n" \
"v_exp_f32 v113, v113\n" \
"v_mfma_f32_16x16x32_bf16 v[32:35], a[236:239], a[140:143], v[32:35]\n" \
"ds_read_b128 a[212:215], v247 offset:26624\n" \
"v_add_f32 v244, v244, v113\n" \
"v_mfma_f32_16x16x32_bf16 v[40:43], a[232:235], a[152:155], v[228:231]\n" \
"v_exp_f32 v114, v114\n" \
"v_mfma_f32_16x16x32_bf16 v[40:43], a[236:239], a[156:159], v[40:43]\n" \
"v_add_f32 v244, v244, v114\n" \
"v_cvt_pk_bf16_f32 v112, v112, v113\n" \
"v_mfma_f32_16x16x32_bf16 v[40:43], a[224:227], a[144:147], v[40:43]\n" \
"v_exp_f32 v115, v115\n" \
"v_mfma_f32_16x16x32_bf16 v[40:43], a[228:231], a[148:151], v[40:43]\n" \
"ds_read_b128 a[216:219], v247 offset:19456\n" \
"global_load_lds_dwordx4 v250, s[38:39] offset:3072 sc1\n" \
"v_add_f32 v244, v244, v115\n" \
"v_exp_f32 v116, v116\n" \
"v_mfma_f32_16x16x32_bf16 v[48:51], a[224:227], a[160:163], v[232:235]\n" \
"s_add_u32 s38, s38, 17408\n" \
"v_add_f32 v244, v244, v116\n" \
"v_cvt_pk_bf16_f32 v113, v114, v115\n" \
"v_mfma_f32_16x16x32_bf16 v[48:51], a[228:231], a[164:167], v[48:51]\n" \
"global_load_dword v253, v252, s[40:41]\n" \
"v_mfma_f32_16x16x32_bf16 v[48:51], a[232:235], a[168:171], v[48:51]\n" \
"s_sub_u32 s80, s80, 1\n" \
"v_exp_f32 v117, v117\n" \
"v_mfma_f32_16x16x32_bf16 v[48:51], a[236:239], a[172:175], v[48:51]\n" \
"s_cmp_gt_i32 s80, 0\n" \
"ds_read_b128 a[220:223], v247 offset:27648\n" \
"v_add_f32 v244, v244, v117\n" \
"v_mfma_f32_16x16x32_bf16 v[56:59], a[232:235], a[184:187], v[236:239]\n" \
"s_cselect_b32 s81, 0x80000, 0\n" \
"v_exp_f32 v118, v118\n" \
"v_mfma_f32_16x16x32_bf16 v[56:59], a[236:239], a[188:191], v[56:59]\n" \
"s_add_u32 s40, s40, s81\n" \
"v_add_f32 v244, v244, v118\n" \
"v_cvt_pk_bf16_f32 v114, v116, v117\n" \
"v_mfma_f32_16x16x32_bf16 v[56:59], a[224:227], a[176:179], v[56:59]\n" \
"s_addc_u32 s41, s41, 0\n" \
"v_exp_f32 v119, v119\n" \
"v_mfma_f32_16x16x32_bf16 v[56:59], a[228:231], a[180:183], v[56:59]\n" \
"s_load_dwordx8 s[44:51], s[56:57], s79 glc\n" \
"ds_read_b128 a[224:227], v247 offset:20480\n" \
"v_add_f32 v244, v244, v119\n" \
"v_mfma_f32_16x16x32_bf16 v[60:63], a[240:243], a[176:179], v[236:239]\n" \
"s_xor_b32 s74, s74, 0x8000\n" \
"v_exp_f32 v120, v120\n" \
"v_mfma_f32_16x16x32_bf16 v[60:63], a[244:247], a[180:183], v[60:63]\n" \
"s_sub_u32 s75, s75, 1\n" \
"v_add_f32 v245, v245, v120\n" \
"v_cvt_pk_bf16_f32 v115, v118, v119\n" \
"v_mfma_f32_16x16x32_bf16 v[60:63], a[248:251], a[184:187], v[60:63]\n" \
"s_cmp_lg_u32 s75, 0\n" \
"v_exp_f32 v121, v121\n" \
"v_mfma_f32_16x16x32_bf16 v[60:63], a[252:255], a[188:191], v[60:63]\n" \
"ds_read_b128 a[228:231], v247 offset:28672\n" \
"v_add_f32 v245, v245, v121\n" \
"v_mfma_f32_16x16x32_bf16 v[52:55], a[248:251], a[168:171], v[232:235]\n" \
"v_exp_f32 v122, v122\n" \
"v_mfma_f32_16x16x32_bf16 v[52:55], a[252:255], a[172:175], v[52:55]\n" \
"v_add_f32 v245, v245, v122\n" \
"v_cvt_pk_bf16_f32 v120, v120, v121\n" \
"v_mfma_f32_16x16x32_bf16 v[52:55], a[240:243], a[160:163], v[52:55]\n" \
"v_exp_f32 v123, v123\n" \
"v_mfma_f32_16x16x32_bf16 v[52:55], a[244:247], a[164:167], v[52:55]\n" \
"ds_read_b128 a[232:235], v247 offset:21504\n" \
"v_add_f32 v245, v245, v123\n" \
"v_mfma_f32_16x16x32_bf16 v[44:47], a[240:243], a[144:147], v[228:231]\n" \
"v_exp_f32 v124, v124\n" \
"v_mfma_f32_16x16x32_bf16 v[44:47], a[244:247], a[148:151], v[44:47]\n" \
"v_add_f32 v245, v245, v124\n" \
"v_cvt_pk_bf16_f32 v121, v122, v123\n" \
"v_mfma_f32_16x16x32_bf16 v[44:47], a[248:251], a[152:155], v[44:47]\n" \
"v_exp_f32 v125, v125\n" \
"v_mfma_f32_16x16x32_bf16 v[44:47], a[252:255], a[156:159], v[44:47]\n" \
"ds_read_b128 a[236:239], v247 offset:29696\n" \
"v_add_f32 v245, v245, v125\n" \
"v_mfma_f32_16x16x32_bf16 v[36:39], a[248:251], a[136:139], v[224:227]\n" \
"v_exp_f32 v126, v126\n" \
"v_mfma_f32_16x16x32_bf16 v[36:39], a[252:255], a[140:143], v[36:39]\n" \
"v_add_f32 v245, v245, v126\n" \
"v_cvt_pk_bf16_f32 v122, v124, v125\n" \
"v_mfma_f32_16x16x32_bf16 v[36:39], a[240:243], a[128:131], v[36:39]\n" \
"s_waitcnt vmcnt(11) lgkmcnt(4)\n" \
"v_exp_f32 v127, v127\n" \
"v_mfma_f32_16x16x32_bf16 v[36:39], a[244:247], a[132:135], v[36:39]\n" \
"s_barrier\n" \
"ds_read_b128 a[240:243], v247 offset:22528\n" \
"v_add_f32 v245, v245, v127\n" \
"s_cbranch_scc1 Lloop_%=\n" \
"v_mfma_f32_16x16x32_bf16 a[0:3], a[192:195], v[64:67], a[0:3]\n" \
"v_exp_f32 v8, v8\n" \
"v_mfma_f32_16x16x32_bf16 a[0:3], a[196:199], v[96:99], a[0:3]\n" \
"v_cvt_pk_bf16_f32 v123, v126, v127\n" \
"v_exp_f32 v9, v9\n" \
"v_mfma_f32_16x16x32_bf16 a[32:35], a[192:195], v[72:75], a[32:35]\n" \
"s_add_u32 m0, s73, 0x4000\n" \
"ds_read_b128 a[244:247], v247 offset:30720\n" \
"v_exp_f32 v10, v10\n" \
"v_mfma_f32_16x16x32_bf16 a[32:35], a[196:199], v[104:107], a[32:35]\n" \
"ds_read_b128 a[248:251], v247 offset:23552\n" \
"global_load_lds_dwordx4 v250, s[36:37] offset:0 sc1\n" \
"v_mfma_f32_16x16x32_bf16 v[128:131], a[192:195], v[80:83], v[128:131]\n" \
"ds_read_b128 a[252:255], v247 offset:31744\n" \
"v_cvt_pk_bf16_f32 v8, v8, v9\n" \
"v_mfma_f32_16x16x32_bf16 v[128:131], a[196:199], v[112:115], v[128:131]\n" \
"v_exp_f32 v11, v11\n" \
"v_mfma_f32_16x16x32_bf16 v[160:163], a[192:195], v[88:91], v[160:163]\n" \
"v_exp_f32 v12, v12\n" \
"v_mfma_f32_16x16x32_bf16 v[160:163], a[196:199], v[120:123], v[160:163]\n" \
"ds_read_b128 a[192:195], v246 offset:16384\n" \
"v_xor_b32 v247, 0x8000, v247\n" \
"v_cvt_pk_bf16_f32 v9, v10, v11\n" \
"v_mfma_f32_16x16x32_bf16 v[164:167], a[200:203], v[88:91], v[164:167]\n" \
"v_exp_f32 v13, v13\n" \
"v_mfma_f32_16x16x32_bf16 v[164:167], a[204:207], v[120:123], v[164:167]\n" \
"v_exp_f32 v14, v14\n" \
"v_cvt_pk_bf16_f32 v10, v12, v13\n" \
"v_mfma_f32_16x16x32_bf16 v[132:135], a[200:203], v[80:83], v[132:135]\n" \
"v_exp_f32 v15, v15\n" \
"v_mfma_f32_16x16x32_bf16 v[132:135], a[204:207], v[112:115], v[132:135]\n" \
"ds_read_b128 a[196:199], v246 offset:18432\n" \
"v_cmp_gt_i32 vcc, 0, v251\n" \
"v_cmp_gt_i32 s[64:65], 1, v251\n" \
"v_mfma_f32_16x16x32_bf16 a[36:39], a[200:203], v[72:75], a[36:39]\n" \
"v_cmp_gt_i32 s[66:67], 2, v251\n" \
"v_cndmask_b32 v0, v0, v254, vcc\n" \
"v_mfma_f32_16x16x32_bf16 a[36:39], a[204:207], v[104:107], a[36:39]\n" \
"v_cmp_gt_i32 vcc, 3, v251\n" \
"v_cndmask_b32 v1, v1, v254, s[64:65]\n" \
"v_mfma_f32_16x16x32_bf16 a[4:7], a[200:203], v[64:67], a[4:7]\n" \
"v_cmp_gt_i32 s[64:65], 16, v251\n" \
"v_cndmask_b32 v2, v2, v254, s[66:67]\n" \
"v_mfma_f32_16x16x32_bf16 a[4:7], a[204:207], v[96:99], a[4:7]\n" \
"ds_read_b128 a[200:203], v246 offset:20480\n" \
"global_load_lds_dwordx4 v250, s[36:37] offset:1024 sc1\n" \
"v_mfma_f32_16x16x32_bf16 a[8:11], a[208:211], v[64:67], a[8:11]\n" \
"v_cmp_gt_i32 s[66:67], 17, v251\n" \
"v_cndmask_b32 v3, v3, v254, vcc\n" \
"v_mfma_f32_16x16x32_bf16 a[8:11], a[212:215], v[96:99], a[8:11]\n" \
"v_cmp_gt_i32 vcc, 18, v251\n" \
"v_cndmask_b32 v4, v4, v254, s[64:65]\n" \
"v_mfma_f32_16x16x32_bf16 a[40:43], a[208:211], v[72:75], a[40:43]\n" \
"v_cmp_gt_i32 s[64:65], 19, v251\n" \
"v_cndmask_b32 v5, v5, v254, s[66:67]\n" \
"v_mfma_f32_16x16x32_bf16 a[40:43], a[212:215], v[104:107], a[40:43]\n" \
"ds_read_b128 a[204:207], v246 offset:22528\n" \
"v_cmp_gt_i32 s[66:67], 32, v251\n" \
"v_mfma_f32_16x16x32_bf16 v[136:139], a[208:211], v[80:83], v[136:139]\n" \
"v_cndmask_b32 v6, v6, v254, vcc\n" \
"v_cmp_gt_i32 vcc, 33, v251\n" \
"v_mfma_f32_16x16x32_bf16 v[136:139], a[212:215], v[112:115], v[136:139]\n" \
"v_cndmask_b32 v7, v7, v254, s[64:65]\n" \
"v_cmp_gt_i32 s[64:65], 34, v251\n" \
"v_mfma_f32_16x16x32_bf16 v[168:171], a[208:211], v[88:91], v[168:171]\n" \
"v_cndmask_b32 v32, v32, v254, s[66:67]\n" \
"v_cmp_gt_i32 s[66:67], 35, v251\n" \
"v_mfma_f32_16x16x32_bf16 v[168:171], a[212:215], v[120:123], v[168:171]\n" \
"Lpg6171_%=:\n" \
"s_cbranch_execz Lpg7555_%=\n" \
"ds_read_b128 a[208:211], v246 offset:16640\n" \
"v_cndmask_b32 v33, v33, v254, vcc\n" \
"v_mfma_f32_16x16x32_bf16 v[172:175], a[216:219], v[88:91], v[172:175]\n" \
"v_cmp_gt_i32 vcc, 48, v251\n" \
"v_cndmask_b32 v34, v34, v254, s[64:65]\n" \
"v_mfma_f32_16x16x32_bf16 v[172:175], a[220:223], v[120:123], v[172:175]\n" \
"v_cmp_gt_i32 s[64:65], 49, v251\n" \
"v_cndmask_b32 v35, v35, v254, s[66:67]\n" \
"v_mfma_f32_16x16x32_bf16 v[140:143], a[216:219], v[80:83], v[140:143]\n" \
"v_cmp_gt_i32 s[66:67], 50, v251\n" \
"v_cndmask_b32 v36, v36, v254, vcc\n" \
"v_mfma_f32_16x16x32_bf16 v[140:143], a[220:223], v[112:115], v[140:143]\n" \
"ds_read_b128 a[212:215], v246 offset:18688\n" \
"global_load_lds_dwordx4 v250, s[36:37] offset:2048 sc1\n" \
"v_mfma_f32_16x16x32_bf16 a[44:47], a[216:219], v[72:75], a[44:47]\n" \
"v_cmp_gt_i32 vcc, 51, v251\n" \
"v_cndmask_b32 v37, v37, v254, s[64:65]\n" \
"v_mfma_f32_16x16x32_bf16 a[44:47], a[220:223], v[104:107], a[44:47]\n" \
"v_cndmask_b32 v38, v38, v254, s[66:67]\n" \
"v_cndmask_b32 v39, v39, v254, vcc\n" \
"v_mfma_f32_16x16x32_bf16 a[12:15], a[216:219], v[64:67], a[12:15]\n" \
"s_waitcnt lgkmcnt(6)\n" \
"v_exp_f32 v0, v0\n" \
"v_mfma_f32_16x16x32_bf16 a[12:15], a[220:223], v[96:99], a[12:15]\n" \
"ds_read_b128 a[216:219], v246 offset:20736\n" \
"v_cvt_pk_bf16_f32 v11, v14, v15\n" \
"v_mfma_f32_16x16x32_bf16 a[16:19], a[224:227], v[64:67], a[16:19]\n" \
"v_exp_f32 v1, v1\n" \
"v_mfma_f32_16x16x32_bf16 a[16:19], a[228:231], v[96:99], a[16:19]\n" \
"v_exp_f32 v2, v2\n" \
"v_cvt_pk_bf16_f32 v0, v0, v1\n" \
"v_mfma_f32_16x16x32_bf16 a[48:51], a[224:227], v[72:75], a[48:51]\n" \
"v_exp_f32 v3, v3\n" \
"v_mfma_f32_16x16x32_bf16 a[48:51], a[228:231], v[104:107], a[48:51]\n" \
"ds_read_b128 a[220:223], v246 offset:22784\n" \
"global_load_lds_dwordx4 v250, s[36:37] offset:3072 sc1\n" \
"v_mfma_f32_16x16x32_bf16 v[144:147], a[224:227], v[80:83], v[144:147]\n" \
"s_add_u32 s36, s36, 17408\n" \
"v_exp_f32 v4, v4\n" \
"v_mfma_f32_16x16x32_bf16 v[144:147], a[228:231], v[112:115], v[144:147]\n" \
"s_xor_b32 s73, s73, 0x8000\n" \
"v_cvt_pk_bf16_f32 v1, v2, v3\n" \
"v_exp_f32 v5, v5\n" \
"v_mfma_f32_16x16x32_bf16 v[176:179], a[224:227], v[88:91], v[176:179]\n" \
"s_xor_b64 s[52:53], s[44:45], s[42:43]\n" \
"v_exp_f32 v6, v6\n" \
"v_mfma_f32_16x16x32_bf16 v[176:179], a[228:231], v[120:123], v[176:179]\n" \
"s_xor_b64 s[54:55], s[46:47], s[42:43]\n" \
"ds_read_b128 a[224:227], v246 offset:24576\n" \
"v_cvt_pk_bf16_f32 v2, v4, v5\n" \
"v_mfma_f32_16x16x32_bf16 v[180:183], a[232:235], v[88:91], v[180:183]\n" \
"s_or_b64 s[52:53], s[52:53], s[54:55]\n" \
"v_exp_f32 v7, v7\n" \
"v_mfma_f32_16x16x32_bf16 v[180:183], a[236:239], v[120:123], v[180:183]\n" \
"s_xor_b64 s[54:55], s[48:49], s[42:43]\n" \
"v_exp_f32 v16, v16\n" \
"v_cvt_pk_bf16_f32 v3, v6, v7\n" \
"v_mfma_f32_16x16x32_bf16 v[148:151], a[232:235], v[80:83], v[148:151]\n" \
"s_or_b64 s[52:53], s[52:53], s[54:55]\n" \
"v_exp_f32 v17, v17\n" \
"v_mfma_f32_16x16x32_bf16 v[148:151], a[236:239], v[112:115], v[148:151]\n" \
"ds_read_b128 a[228:231], v246 offset:26624\n" \
"s_xor_b64 s[54:55], s[50:51], s[42:43]\n" \
"v_mfma_f32_16x16x32_bf16 a[52:55], a[232:235], v[72:75], a[52:55]\n" \
"s_or_b64 s[52:53], s[52:53], s[54:55]\n" \
"v_exp_f32 v18, v18\n" \
"v_mfma_f32_16x16x32_bf16 a[52:55], a[236:239], v[104:107], a[52:55]\n" \
"v_cvt_pk_bf16_f32 v16, v16, v17\n" \
"v_mfma_f32_16x16x32_bf16 v[240:243], a[124:127], v[8:11], v[240:243]\n" \
"v_exp_f32 v19, v19\n" \
"v_mfma_f32_16x16x32_bf16 v[240:243], a[120:123], v[0:3], v[240:243]\n" \
"s_and_b64 s[52:53], s[52:53], s[8:9]\n" \
"s_cbranch_scc1 Lrs1_%=\n" \
"Lrr1_%=:\n" \
"v_mfma_f32_16x16x32_bf16 a[20:23], a[232:235], v[64:67], a[20:23]\n" \
"s_mov_b64 s[8:9], s[10:11]\n" \
"v_exp_f32 v20, v20\n" \
"v_mfma_f32_16x16x32_bf16 a[20:23], a[236:239], v[96:99], a[20:23]\n" \
"ds_read_b128 a[232:235], v246 offset:28672\n" \
"v_cvt_pk_bf16_f32 v17, v18, v19\n" \
"v_mfma_f32_16x16x32_bf16 a[24:27], a[240:243], v[64:67], a[24:27]\n" \
"v_exp_f32 v21, v21\n" \
"v_mfma_f32_16x16x32_bf16 a[24:27], a[244:247], v[96:99], a[24:27]\n" \
"v_exp_f32 v22, v22\n" \
"v_cvt_pk_bf16_f32 v18, v20, v21\n" \
"v_mfma_f32_16x16x32_bf16 a[56:59], a[240:243], v[72:75], a[56:59]\n" \
"v_exp_f32 v23, v23\n" \
"v_mfma_f32_16x16x32_bf16 a[56:59], a[244:247], v[104:107], a[56:59]\n" \
"ds_read_b128 a[236:239], v246 offset:30720\n" \
"v_exp_f32 v24, v24\n" \
"v_mfma_f32_16x16x32_bf16 v[152:155], a[240:243], v[80:83], v[152:155]\n" \
"v_cvt_pk_bf16_f32 v19, v22, v23\n" \
"v_mfma_f32_16x16x32_bf16 v[152:155], a[244:247], v[112:115], v[152:155]\n" \
"v_exp_f32 v25, v25\n" \
"v_mfma_f32_16x16x32_bf16 v[184:187], a[240:243], v[88:91], v[184:187]\n" \
"v_exp_f32 v26, v26\n" \
"v_mfma_f32_16x16x32_bf16 v[184:187], a[244:247], v[120:123], v[184:187]\n" \
"ds_read_b128 a[240:243], v246 offset:24832\n" \
"v_cvt_pk_bf16_f32 v24, v24, v25\n" \
"v_mfma_f32_16x16x32_bf16 v[242:245], a[120:123], v[16:19], v[242:245]\n" \
"v_exp_f32 v27, v27\n" \
"v_mfma_f32_16x16x32_bf16 v[188:191], a[248:251], v[88:91], v[188:191]\n" \
"v_exp_f32 v28, v28\n" \
"v_mfma_f32_16x16x32_bf16 v[188:191], a[252:255], v[120:123], v[188:191]\n" \
"v_cvt_pk_bf16_f32 v25, v26, v27\n" \
"v_exp_f32 v29, v29\n" \
"v_mfma_f32_16x16x32_bf16 v[156:159], a[248:251], v[80:83], v[156:159]\n" \
"v_exp_f32 v30, v30\n" \
"v_mfma_f32_16x16x32_bf16 v[156:159], a[252:255], v[112:115], v[156:159]\n" \
"ds_read_b128 a[244:247], v246 offset:26880\n" \
"v_cvt_pk_bf16_f32 v26, v28, v29\n" \
"v_mfma_f32_16x16x32_bf16 a[60:63], a[248:251], v[72:75], a[60:63]\n" \
"v_exp_f32 v31, v31\n" \
"v_mfma_f32_16x16x32_bf16 a[60:63], a[252:255], v[104:107], a[60:63]\n" \
"v_cvt_pk_bf16_f32 v27, v30, v31\n" \
"v_mfma_f32_16x16x32_bf16 a[28:31], a[248:251], v[64:67], a[28:31]\n" \
"s_waitcnt lgkmcnt(6)\n" \
"v_mfma_f32_16x16x32_bf16 v[242:245], a[124:127], v[24:27], v[242:245]\n" \
"v_mfma_f32_16x16x32_bf16 a[28:31], a[252:255], v[96:99], a[28:31]\n" \
"ds_read_b128 a[248:251], v246 offset:28928\n" \
"LA1l_%=:\n" \
"Lpg6293_%=:\n" \
"s_cbranch_execz Lpg13433_%=\n" \
"s_getpc_b64 s[40:41]\n" \
"Lpd_%=:\n" \
"s_lshl_b32 s80, s62, 2\n" \
"s_add_u32 s80, s80, s61\n" \
"s_and_b32 s80, s80, 15\n" \
"s_lshl_b32 s80, s80, 12\n" \
"s_mov_b32 s81, Lcode_end_%=-Lpd_%=-4\n" \
"v_lshl_add_u32 v252, v248, 2, s80\n" \
"v_min_u32 v252, s81, v252\n" \
"s_waitcnt lgkmcnt(0)\n" \
"v_mfma_f32_16x16x32_bf16 v[72:75], a[192:195], a[144:147], v[228:231]\n" \
"v_exp_f32 v32, v32\n" \
"v_mfma_f32_16x16x32_bf16 v[72:75], a[196:199], a[148:151], v[72:75]\n" \
"v_cvt_pk_bf16_f32 v27, v30, v31\n" \
"v_mfma_f32_16x16x32_bf16 v[72:75], a[200:203], a[152:155], v[72:75]\n" \
"s_mov_b32 m0, s74\n" \
"v_exp_f32 v33, v33\n" \
"v_mfma_f32_16x16x32_bf16 v[72:75], a[204:207], a[156:159], v[72:75]\n" \
"ds_read_b128 a[252:255], v246 offset:30976\n" \
"global_load_lds_dwordx4 v250, s[38:39] offset:0 sc1\n" \
"v_mfma_f32_16x16x32_bf16 v[80:83], a[200:203], a[168:171], v[232:235]\n" \
"s_lshl_b32 s70, s95, 1\n" \
"v_exp_f32 v34, v34\n" \
"v_mfma_f32_16x16x32_bf16 v[80:83], a[204:207], a[172:175], v[80:83]\n" \
"s_xor_b32 s70, s70, 139264\n" \
"v_cvt_pk_bf16_f32 v32, v32, v33\n" \
"v_add_u32 v64, s70, v248\n" \
"v_mfma_f32_16x16x32_bf16 v[80:83], a[192:195], a[160:163], v[80:83]\n" \
"ds_read_b128 v[192:195], v64 offset:0\n" \
"v_exp_f32 v35, v35\n" \
"v_mfma_f32_16x16x32_bf16 v[80:83], a[196:199], a[164:167], v[80:83]\n" \
"ds_read_b128 v[196:199], v64 offset:1024\n" \
"v_exp_f32 v36, v36\n" \
"v_mfma_f32_16x16x32_bf16 v[88:91], a[192:195], a[176:179], v[236:239]\n" \
"ds_read_b128 v[200:203], v64 offset:2048\n" \
"v_cvt_pk_bf16_f32 v33, v34, v35\n" \
"v_mfma_f32_16x16x32_bf16 v[88:91], a[196:199], a[180:183], v[88:91]\n" \
"ds_read_b128 v[204:207], v64 offset:3072\n" \
"v_exp_f32 v37, v37\n" \
"v_exp_f32 v38, v38\n" \
"v_mfma_f32_16x16x32_bf16 v[88:91], a[200:203], a[184:187], v[88:91]\n" \
"ds_read_b128 v[208:211], v64 offset:4096\n" \
"v_cvt_pk_bf16_f32 v34, v36, v37\n" \
"v_mfma_f32_16x16x32_bf16 v[88:91], a[204:207], a[188:191], v[88:91]\n" \
"ds_read_b128 a[192:195], v247 offset:0\n" \
"v_exp_f32 v39, v39\n" \
"v_mfma_f32_16x16x32_bf16 v[92:95], a[216:219], a[184:187], v[236:239]\n" \
"ds_read_b128 v[212:215], v64 offset:5120\n" \
"global_load_lds_dwordx4 v250, s[38:39] offset:1024 sc1\n" \
"v_mfma_f32_16x16x32_bf16 v[92:95], a[220:223], a[188:191], v[92:95]\n" \
"ds_read_b128 v[216:219], v64 offset:6144\n" \
"v_exp_f32 v40, v40\n" \
"v_mfma_f32_16x16x32_bf16 v[92:95], a[208:211], a[176:179], v[92:95]\n" \
"ds_read_b128 a[196:199], v247 offset:8192\n" \
"v_cvt_pk_bf16_f32 v35, v38, v39\n" \
"v_mfma_f32_16x16x32_bf16 v[92:95], a[212:215], a[180:183], v[92:95]\n" \
"ds_read_b128 v[220:223], v64 offset:7168\n" \
"v_exp_f32 v41, v41\n" \
"v_mfma_f32_16x16x32_bf16 v[84:87], a[208:211], a[160:163], v[232:235]\n" \
"ds_write_b128 v64, v[232:235]\n" \
"v_exp_f32 v42, v42\n" \
"v_mfma_f32_16x16x32_bf16 v[84:87], a[212:215], a[164:167], v[84:87]\n" \
"ds_read_b128 a[200:203], v247 offset:1024\n" \
"v_cvt_pk_bf16_f32 v40, v40, v41\n" \
"v_mfma_f32_16x16x32_bf16 v[84:87], a[216:219], a[168:171], v[84:87]\n" \
"v_exp_f32 v43, v43\n" \
"v_mfma_f32_16x16x32_bf16 v[84:87], a[220:223], a[172:175], v[84:87]\n" \
"global_load_lds_dwordx4 v250, s[38:39] offset:2048 sc1\n" \
"ds_write_b128 v64, v[236:239] offset:1024\n" \
"v_exp_f32 v44, v44\n" \
"v_mfma_f32_16x16x32_bf16 v[76:79], a[216:219], a[152:155], v[228:231]\n" \
"ds_read_b128 a[204:207], v247 offset:9216\n" \
"v_xor_b32 v65, 0x2000, v64\n" \
"v_mfma_f32_16x16x32_bf16 v[76:79], a[220:223], a[156:159], v[76:79]\n" \
"v_xor_b32 v246, 0x8000, v246\n" \
"v_add_u32 v246, s82, v246\n" \
"v_mfma_f32_16x16x32_bf16 v[76:79], a[208:211], a[144:147], v[76:79]\n" \
"s_waitcnt lgkmcnt(4)\n" \
"v_mov_b32 v66, s60\n" \
"v_lshlrev_b32 v67, 28, s61\n" \
"v_mfma_f32_16x16x32_bf16 v[76:79], a[212:215], a[148:151], v[76:79]\n" \
"ds_read_b128 a[208:211], v247 offset:2048\n" \
"v_cvt_pk_bf16_f32 v41, v42, v43\n" \
"v_mfma_f32_16x16x32_bf16 v[104:107], a[224:227], a[144:147], v[228:231]\n" \
"v_exp_f32 v45, v45\n" \
"v_mfma_f32_16x16x32_bf16 v[104:107], a[228:231], a[148:151], v[104:107]\n" \
"v_exp_f32 v46, v46\n" \
"v_mfma_f32_16x16x32_bf16 v[104:107], a[232:235], a[152:155], v[104:107]\n" \
"ds_read_b128 a[212:215], v247 offset:10240\n" \
"v_cvt_pk_bf16_f32 v42, v44, v45\n" \
"v_mfma_f32_16x16x32_bf16 v[104:107], a[236:239], a[156:159], v[104:107]\n" \
"v_exp_f32 v47, v47\n" \
"v_mfma_f32_16x16x32_bf16 v[112:115], a[232:235], a[168:171], v[232:235]\n" \
"v_exp_f32 v48, v48\n" \
"v_cvt_pk_bf16_f32 v43, v46, v47\n" \
"v_mfma_f32_16x16x32_bf16 v[112:115], a[236:239], a[172:175], v[112:115]\n" \
"ds_read_b128 a[216:219], v247 offset:3072\n" \
"global_load_lds_dwordx4 v250, s[38:39] offset:3072 sc1\n" \
"v_mfma_f32_16x16x32_bf16 v[112:115], a[224:227], a[160:163], v[112:115]\n" \
"v_exp_f32 v49, v49\n" \
"v_mfma_f32_16x16x32_bf16 v[112:115], a[228:231], a[164:167], v[112:115]\n" \
"v_exp_f32 v50, v50\n" \
"v_cvt_pk_bf16_f32 v48, v48, v49\n" \
"v_mfma_f32_16x16x32_bf16 v[240:243], a[120:123], v[32:35], v[240:243]\n" \
"v_exp_f32 v51, v51\n" \
"v_mfma_f32_16x16x32_bf16 v[240:243], a[124:127], v[40:43], v[240:243]\n" \
"s_add_u32 s38, s38, 17408\n" \
"v_exp_f32 v52, v52\n" \
"v_mfma_f32_16x16x32_bf16 v[120:123], a[224:227], a[176:179], v[236:239]\n" \
"ds_read_b128 a[220:223], v247 offset:11264\n" \
"v_cvt_pk_bf16_f32 v49, v50, v51\n" \
"v_mfma_f32_16x16x32_bf16 v[120:123], a[228:231], a[180:183], v[120:123]\n" \
"v_exp_f32 v53, v53\n" \
"v_mfma_f32_16x16x32_bf16 v[120:123], a[232:235], a[184:187], v[120:123]\n" \
"v_exp_f32 v54, v54\n" \
"v_mfma_f32_16x16x32_bf16 v[120:123], a[236:239], a[188:191], v[120:123]\n" \
"ds_read_b128 a[224:227], v247 offset:4096\n" \
"v_cvt_pk_bf16_f32 v50, v52, v53\n" \
"v_exp_f32 v55, v55\n" \
"v_mfma_f32_16x16x32_bf16 v[124:127], a[248:251], a[184:187], v[236:239]\n" \
"global_load_dword v253, v252, s[40:41]\n" \
"v_mfma_f32_16x16x32_bf16 v[124:127], a[252:255], a[188:191], v[124:127]\n" \
"v_exp_f32 v56, v56\n" \
"v_mfma_f32_16x16x32_bf16 v[124:127], a[240:243], a[176:179], v[124:127]\n" \
"ds_read_b128 a[228:231], v247 offset:12288\n" \
"v_cvt_pk_bf16_f32 v51, v54, v55\n" \
"v_mfma_f32_16x16x32_bf16 v[124:127], a[244:247], a[180:183], v[124:127]\n" \
"v_exp_f32 v57, v57\n" \
"v_exp_f32 v58, v58\n" \
"v_cvt_pk_bf16_f32 v56, v56, v57\n" \
"v_mfma_f32_16x16x32_bf16 v[242:245], a[120:123], v[48:51], v[242:245]\n" \
"v_exp_f32 v59, v59\n" \
"v_mfma_f32_16x16x32_bf16 v[116:119], a[240:243], a[160:163], v[232:235]\n" \
"v_exp_f32 v60, v60\n" \
"v_mfma_f32_16x16x32_bf16 v[116:119], a[244:247], a[164:167], v[116:119]\n" \
"ds_read_b128 a[232:235], v247 offset:5120\n" \
"v_cvt_pk_bf16_f32 v57, v58, v59\n" \
"v_mfma_f32_16x16x32_bf16 v[116:119], a[248:251], a[168:171], v[116:119]\n" \
"v_exp_f32 v61, v61\n" \
"v_mfma_f32_16x16x32_bf16 v[116:119], a[252:255], a[172:175], v[116:119]\n" \
"v_exp_f32 v62, v62\n" \
"v_cvt_pk_bf16_f32 v58, v60, v61\n" \
"v_mfma_f32_16x16x32_bf16 v[108:111], a[248:251], a[152:155], v[228:231]\n" \
"ds_read_b128 a[236:239], v247 offset:13312\n" \
"v_exp_f32 v63, v63\n" \
"v_mfma_f32_16x16x32_bf16 v[108:111], a[252:255], a[156:159], v[108:111]\n" \
"v_cvt_pk_bf16_f32 v59, v62, v63\n" \
"v_mfma_f32_16x16x32_bf16 v[108:111], a[240:243], a[144:147], v[108:111]\n" \
"s_waitcnt vmcnt(5) lgkmcnt(4)\n" \
"v_mfma_f32_16x16x32_bf16 v[242:245], a[124:127], v[56:59], v[242:245]\n" \
"v_mfma_f32_16x16x32_bf16 v[108:111], a[244:247], a[148:151], v[108:111]\n" \
"s_barrier\n" \
"ds_read_b128 a[240:243], v247 offset:6144\n" \
"v_mfma_f32_16x16x32_bf16 a[0:3], a[192:195], v[0:3], a[0:3]\n" \
"buffer_store_dword v66, v67, s[56:59], s84 offen sc1\n" \
"v_mfma_f32_16x16x32_bf16 a[0:3], a[196:199], v[32:35], a[0:3]\n" \
"s_add_u32 s73, s73, s95\n" \
"v_cvt_pk_bf16_f32 v59, v62, v63\n" \
"v_exp_f32 v80, v80\n" \
"v_mfma_f32_16x16x32_bf16 a[32:35], a[192:195], v[8:11], a[32:35]\n" \
"s_add_u32 s36, s36, s83\n" \
"ds_read_b128 a[244:247], v247 offset:14336\n" \
"v_exp_f32 v81, v81\n" \
"v_mfma_f32_16x16x32_bf16 a[32:35], a[196:199], v[40:43], a[32:35]\n" \
"s_add_u32 s73, s73, 4096\n" \
"ds_read_b128 a[248:251], v247 offset:7168\n" \
"v_exp_f32 v82, v82\n" \
"v_mfma_f32_16x16x32_bf16 v[128:131], a[192:195], v[16:19], v[128:131]\n" \
"ds_read_b128 a[252:255], v247 offset:15360\n" \
"v_cvt_pk_bf16_f32 v80, v80, v81\n" \
"v_mfma_f32_16x16x32_bf16 v[128:131], a[196:199], v[48:51], v[128:131]\n" \
"ds_read_b128 v[224:227], v65\n" \
"v_exp_f32 v83, v83\n" \
"v_mfma_f32_16x16x32_bf16 v[160:163], a[192:195], v[24:27], v[160:163]\n" \
"s_mov_b32 m0, s73\n" \
"ds_read_b128 v[228:231], v65 offset:1024\n" \
"v_xor_b32 v252, 0x8000, v246\n" \
"v_mfma_f32_16x16x32_bf16 v[160:163], a[196:199], v[56:59], v[160:163]\n" \
"ds_read_b128 a[192:195], v246 offset:0\n" \
"global_load_lds_dwordx4 v250, s[36:37] offset:-4096 sc1\n" \
"v_mfma_f32_16x16x32_bf16 v[164:167], a[200:203], v[24:27], v[164:167]\n" \
"v_exp_f32 v84, v84\n" \
"v_mfma_f32_16x16x32_bf16 v[164:167], a[204:207], v[56:59], v[164:167]\n" \
"v_cvt_pk_bf16_f32 v81, v82, v83\n" \
"v_exp_f32 v85, v85\n" \
"v_mfma_f32_16x16x32_bf16 v[132:135], a[200:203], v[16:19], v[132:135]\n" \
"v_exp_f32 v86, v86\n" \
"v_mfma_f32_16x16x32_bf16 v[132:135], a[204:207], v[48:51], v[132:135]\n" \
"ds_read_b128 a[196:199], v246 offset:2048\n" \
"global_load_lds_dwordx4 v250, s[36:37] offset:-3072 sc1\n" \
"v_mfma_f32_16x16x32_bf16 a[36:39], a[200:203], v[8:11], a[36:39]\n" \
"v_cvt_pk_bf16_f32 v82, v84, v85\n" \
"v_mfma_f32_16x16x32_bf16 a[36:39], a[204:207], v[40:43], a[36:39]\n" \
"v_exp_f32 v87, v87\n" \
"v_mfma_f32_16x16x32_bf16 a[4:7], a[200:203], v[0:3], a[4:7]\n" \
"v_cmp_gt_i32 vcc, 0, v251\n" \
"v_cmp_gt_i32 s[64:65], 1, v251\n" \
"v_mfma_f32_16x16x32_bf16 a[4:7], a[204:207], v[32:35], a[4:7]\n" \
"ds_read_b128 a[200:203], v246 offset:4096\n" \
"global_load_lds_dwordx4 v250, s[36:37] offset:-2048 sc1\n" \
"v_mfma_f32_16x16x32_bf16 a[8:11], a[208:211], v[0:3], a[8:11]\n" \
"v_cmp_gt_i32 s[66:67], 2, v251\n" \
"v_cndmask_b32 v72, v72, v254, vcc\n" \
"v_mfma_f32_16x16x32_bf16 a[8:11], a[212:215], v[32:35], a[8:11]\n" \
"v_cmp_gt_i32 vcc, 3, v251\n" \
"v_cndmask_b32 v73, v73, v254, s[64:65]\n" \
"v_mfma_f32_16x16x32_bf16 a[40:43], a[208:211], v[8:11], a[40:43]\n" \
"v_cmp_gt_i32 s[64:65], 16, v251\n" \
"v_cndmask_b32 v74, v74, v254, s[66:67]\n" \
"v_mfma_f32_16x16x32_bf16 a[40:43], a[212:215], v[40:43], a[40:43]\n" \
"ds_read_b128 a[204:207], v246 offset:6144\n" \
"global_load_lds_dwordx4 v250, s[36:37] offset:-1024 sc1\n" \
"v_mfma_f32_16x16x32_bf16 v[136:139], a[208:211], v[16:19], v[136:139]\n" \
"v_cmp_gt_i32 s[66:67], 17, v251\n" \
"v_cndmask_b32 v75, v75, v254, vcc\n" \
"v_mfma_f32_16x16x32_bf16 v[136:139], a[212:215], v[48:51], v[136:139]\n" \
"v_cmp_gt_i32 vcc, 18, v251\n" \
"v_cndmask_b32 v76, v76, v254, s[64:65]\n" \
"v_mfma_f32_16x16x32_bf16 v[168:171], a[208:211], v[24:27], v[168:171]\n" \
"v_cmp_gt_i32 s[64:65], 19, v251\n" \
"v_cndmask_b32 v77, v77, v254, s[66:67]\n" \
"v_mfma_f32_16x16x32_bf16 v[168:171], a[212:215], v[56:59], v[168:171]\n" \
"ds_read_b128 a[208:211], v246 offset:256\n" \
"global_load_lds_dwordx4 v250, s[36:37] offset:0 sc1\n" \
"v_mfma_f32_16x16x32_bf16 v[172:175], a[216:219], v[24:27], v[172:175]\n" \
"v_cmp_gt_i32 s[66:67], 32, v251\n" \
"v_cndmask_b32 v78, v78, v254, vcc\n" \
"v_mfma_f32_16x16x32_bf16 v[172:175], a[220:223], v[56:59], v[172:175]\n" \
"v_cmp_gt_i32 vcc, 33, v251\n" \
"v_cndmask_b32 v79, v79, v254, s[64:65]\n" \
"v_mfma_f32_16x16x32_bf16 v[140:143], a[216:219], v[16:19], v[140:143]\n" \
"v_cmp_gt_i32 s[64:65], 34, v251\n" \
"v_cndmask_b32 v104, v104, v254, s[66:67]\n" \
"v_mfma_f32_16x16x32_bf16 v[140:143], a[220:223], v[48:51], v[140:143]\n" \
"ds_read_b128 a[212:215], v246 offset:2304\n" \
"global_load_lds_dwordx4 v250, s[36:37] offset:1024 sc1\n" \
"v_mfma_f32_16x16x32_bf16 a[44:47], a[216:219], v[8:11], a[44:47]\n" \
"v_cmp_gt_i32 s[66:67], 35, v251\n" \
"v_cndmask_b32 v105, v105, v254, vcc\n" \
"v_mfma_f32_16x16x32_bf16 a[44:47], a[220:223], v[40:43], a[44:47]\n" \
"v_cmp_gt_i32 vcc, 48, v251\n" \
"v_cndmask_b32 v106, v106, v254, s[64:65]\n" \
"v_mfma_f32_16x16x32_bf16 a[12:15], a[216:219], v[0:3], a[12:15]\n" \
"s_waitcnt lgkmcnt(6)\n" \
"v_cmp_gt_i32 s[64:65], 49, v251\n" \
"v_cndmask_b32 v107, v107, v254, s[66:67]\n" \
"v_mfma_f32_16x16x32_bf16 a[12:15], a[220:223], v[32:35], a[12:15]\n" \
"ds_read_b128 a[216:219], v246 offset:4352\n" \
"global_load_lds_dwordx4 v250, s[36:37] offset:2048 sc1\n" \
"v_mfma_f32_16x16x32_bf16 a[16:19], a[224:227], v[0:3], a[16:19]\n" \
"v_cmp_gt_i32 s[66:67], 50, v251\n" \
"v_cndmask_b32 v108, v108, v254, vcc\n" \
"v_mfma_f32_16x16x32_bf16 a[16:19], a[228:231], v[32:35], a[16:19]\n" \
"v_cmp_gt_i32 vcc, 51, v251\n" \
"v_cndmask_b32 v109, v109, v254, s[64:65]\n" \
"v_mfma_f32_16x16x32_bf16 a[48:51], a[224:227], v[8:11], a[48:51]\n" \
"v_cndmask_b32 v110, v110, v254, s[66:67]\n" \
"v_cndmask_b32 v111, v111, v254, vcc\n" \
"v_mfma_f32_16x16x32_bf16 a[48:51], a[228:231], v[40:43], a[48:51]\n" \
"ds_read_b128 a[220:223], v246 offset:6400\n" \
"global_load_lds_dwordx4 v250, s[36:37] offset:3072 sc1\n" \
"v_mfma_f32_16x16x32_bf16 v[144:147], a[224:227], v[16:19], v[144:147]\n" \
"s_add_u32 s36, s36, 34816\n" \
"v_exp_f32 v72, v72\n" \
"v_mfma_f32_16x16x32_bf16 v[144:147], a[228:231], v[48:51], v[144:147]\n" \
"v_cvt_pk_bf16_f32 v83, v86, v87\n" \
"v_mfma_f32_16x16x32_bf16 v[176:179], a[224:227], v[24:27], v[176:179]\n" \
"s_add_u32 m0, s74, 0x4000\n" \
"v_exp_f32 v73, v73\n" \
"v_mfma_f32_16x16x32_bf16 v[176:179], a[228:231], v[56:59], v[176:179]\n" \
"ds_read_b128 a[224:227], v246 offset:8192\n" \
"global_load_lds_dwordx4 v250, s[38:39] offset:0 sc1\n" \
"v_mfma_f32_16x16x32_bf16 v[180:183], a[232:235], v[24:27], v[180:183]\n" \
"v_exp_f32 v74, v74\n" \
"v_mfma_f32_16x16x32_bf16 v[180:183], a[236:239], v[56:59], v[180:183]\n" \
"v_cvt_pk_bf16_f32 v72, v72, v73\n" \
"v_mfma_f32_16x16x32_bf16 v[242:245], a[120:123], v[80:83], v[242:245]\n" \
"v_exp_f32 v75, v75\n" \
"v_mfma_f32_16x16x32_bf16 v[148:151], a[232:235], v[16:19], v[148:151]\n" \
"v_exp_f32 v76, v76\n" \
"v_mfma_f32_16x16x32_bf16 v[148:151], a[236:239], v[48:51], v[148:151]\n" \
"ds_read_b128 a[228:231], v246 offset:10240\n" \
"global_load_lds_dwordx4 v250, s[38:39] offset:1024 sc1\n" \
"v_mfma_f32_16x16x32_bf16 a[52:55], a[232:235], v[8:11], a[52:55]\n" \
"v_cvt_pk_bf16_f32 v73, v74, v75\n" \
"v_mfma_f32_16x16x32_bf16 a[52:55], a[236:239], v[40:43], a[52:55]\n" \
"v_exp_f32 v77, v77\n" \
"v_mfma_f32_16x16x32_bf16 a[20:23], a[232:235], v[0:3], a[20:23]\n" \
"v_exp_f32 v78, v78\n" \
"v_mfma_f32_16x16x32_bf16 a[20:23], a[236:239], v[32:35], a[20:23]\n" \
"ds_read_b128 a[232:235], v246 offset:12288\n" \
"v_cvt_pk_bf16_f32 v74, v76, v77\n" \
"v_mfma_f32_16x16x32_bf16 a[24:27], a[240:243], v[0:3], a[24:27]\n" \
"global_load_lds_dwordx4 v250, s[38:39] offset:2048 sc1\n" \
"global_load_lds_dwordx4 v250, s[38:39] offset:3072 sc1\n" \
"v_mfma_f32_16x16x32_bf16 a[24:27], a[244:247], v[32:35], a[24:27]\n" \
"v_exp_f32 v79, v79\n" \
"v_mfma_f32_16x16x32_bf16 a[56:59], a[240:243], v[8:11], a[56:59]\n" \
"s_add_u32 s38, s38, 17408\n" \
"v_exp_f32 v88, v88\n" \
"v_mfma_f32_16x16x32_bf16 a[56:59], a[244:247], v[40:43], a[56:59]\n" \
"s_add_u32 s38, s38, s83\n" \
"ds_read_b128 a[236:239], v246 offset:14336\n" \
"v_cvt_pk_bf16_f32 v75, v78, v79\n" \
"v_mfma_f32_16x16x32_bf16 v[152:155], a[240:243], v[16:19], v[152:155]\n" \
"s_add_u32 s74, s74, s95\n" \
"v_exp_f32 v89, v89\n" \
"v_mfma_f32_16x16x32_bf16 v[152:155], a[244:247], v[48:51], v[152:155]\n" \
"v_exp_f32 v90, v90\n" \
"v_cvt_pk_bf16_f32 v88, v88, v89\n" \
"v_mfma_f32_16x16x32_bf16 v[240:243], a[124:127], v[72:75], v[240:243]\n" \
"s_add_u32 s74, s74, 4096\n" \
"v_exp_f32 v91, v91\n" \
"v_mfma_f32_16x16x32_bf16 v[184:187], a[240:243], v[24:27], v[184:187]\n" \
"s_cmp_eq_u32 s63, 7\n" \
"v_exp_f32 v92, v92\n" \
"v_mfma_f32_16x16x32_bf16 v[184:187], a[244:247], v[56:59], v[184:187]\n" \
"ds_read_b128 a[240:243], v246 offset:8448\n" \
"v_cvt_pk_bf16_f32 v89, v90, v91\n" \
"v_mfma_f32_16x16x32_bf16 v[188:191], a[248:251], v[24:27], v[188:191]\n" \
"v_exp_f32 v93, v93\n" \
"v_mfma_f32_16x16x32_bf16 v[188:191], a[252:255], v[56:59], v[188:191]\n" \
"v_exp_f32 v94, v94\n" \
"v_mfma_f32_16x16x32_bf16 v[156:159], a[248:251], v[16:19], v[156:159]\n" \
"v_cvt_pk_bf16_f32 v90, v92, v93\n" \
"v_mfma_f32_16x16x32_bf16 v[156:159], a[252:255], v[48:51], v[156:159]\n" \
"ds_read_b128 a[244:247], v246 offset:10496\n" \
"v_mfma_f32_16x16x32_bf16 a[60:63], a[248:251], v[8:11], a[60:63]\n" \
"v_exp_f32 v95, v95\n" \
"v_mfma_f32_16x16x32_bf16 a[60:63], a[252:255], v[40:43], a[60:63]\n" \
"v_cvt_pk_bf16_f32 v91, v94, v95\n" \
"v_mfma_f32_16x16x32_bf16 a[28:31], a[248:251], v[0:3], a[28:31]\n" \
"s_waitcnt lgkmcnt(6)\n" \
"v_mfma_f32_16x16x32_bf16 v[242:245], a[124:127], v[88:91], v[242:245]\n" \
"v_mfma_f32_16x16x32_bf16 a[28:31], a[252:255], v[32:35], a[28:31]\n" \
"ds_read_b128 a[248:251], v246 offset:12544\n" \
"s_cbranch_scc1 LF7_%=\n" \
"v_mfma_f32_16x16x32_bf16 v[0:3], a[192:195], v[192:195], v[224:227]\n" \
"s_load_dwordx8 s[44:51], s[56:57], s79 glc\n" \
"v_exp_f32 v104, v104\n" \
"v_mfma_f32_16x16x32_bf16 v[0:3], a[196:199], v[196:199], v[0:3]\n" \
"v_cvt_pk_bf16_f32 v91, v94, v95\n" \
"v_mfma_f32_16x16x32_bf16 v[0:3], a[200:203], v[200:203], v[0:3]\n" \
"v_exp_f32 v105, v105\n" \
"v_mfma_f32_16x16x32_bf16 v[0:3], a[204:207], v[204:207], v[0:3]\n" \
"ds_read_b128 a[252:255], v246 offset:14592\n" \
"v_mov_b32 v78, v242\n" \
"v_mfma_f32_16x16x32_bf16 v[8:11], a[200:203], v[216:219], v[228:231]\n" \
"v_exp_f32 v106, v106\n" \
"v_mfma_f32_16x16x32_bf16 v[8:11], a[204:207], v[220:223], v[8:11]\n" \
"v_cvt_pk_bf16_f32 v104, v104, v105\n" \
"v_mov_b32 v76, v78\n" \
"v_mfma_f32_16x16x32_bf16 v[8:11], a[192:195], v[208:211], v[8:11]\n" \
"v_exp_f32 v107, v107\n" \
"v_mfma_f32_16x16x32_bf16 v[8:11], a[196:199], v[212:215], v[8:11]\n" \
"v_exp_f32 v108, v108\n" \
"v_mfma_f32_16x16x32_bf16 v[16:19], a[192:195], a[160:163], v[232:235]\n" \
"v_cvt_pk_bf16_f32 v105, v106, v107\n" \
"v_mfma_f32_16x16x32_bf16 v[16:19], a[196:199], a[164:167], v[16:19]\n" \
"v_exp_f32 v109, v109\n" \
"v_mfma_f32_16x16x32_bf16 v[16:19], a[200:203], a[168:171], v[16:19]\n" \
"v_exp_f32 v110, v110\n" \
"v_mfma_f32_16x16x32_bf16 v[16:19], a[204:207], a[172:175], v[16:19]\n" \
"v_cvt_pk_bf16_f32 v106, v108, v109\n" \
"v_permlane16_swap_b32 v76, v78\n" \
"v_mfma_f32_16x16x32_bf16 v[24:27], a[200:203], a[184:187], v[236:239]\n" \
"v_exp_f32 v111, v111\n" \
"v_mfma_f32_16x16x32_bf16 v[24:27], a[204:207], a[188:191], v[24:27]\n" \
"v_exp_f32 v112, v112\n" \
"v_mfma_f32_16x16x32_bf16 v[24:27], a[192:195], a[176:179], v[24:27]\n" \
"v_cvt_pk_bf16_f32 v107, v110, v111\n" \
"v_add_f32 v78, v78, v76\n" \
"v_mfma_f32_16x16x32_bf16 v[24:27], a[196:199], a[180:183], v[24:27]\n" \
"ds_read_b128 a[192:195], v247 offset:16384\n" \
"v_mov_b32 v76, v78\n" \
"v_mfma_f32_16x16x32_bf16 v[28:31], a[208:211], a[176:179], v[236:239]\n" \
"v_exp_f32 v113, v113\n" \
"v_mfma_f32_16x16x32_bf16 v[28:31], a[212:215], a[180:183], v[28:31]\n" \
"v_exp_f32 v114, v114\n" \
"v_mfma_f32_16x16x32_bf16 v[28:31], a[216:219], a[184:187], v[28:31]\n" \
"v_cvt_pk_bf16_f32 v112, v112, v113\n" \
"v_mfma_f32_16x16x32_bf16 v[28:31], a[220:223], a[188:191], v[28:31]\n" \
"v_permlane32_swap_b32 v76, v78\n" \
"v_mfma_f32_16x16x32_bf16 v[240:243], a[124:127], v[104:107], v[240:243]\n" \
"ds_read_b128 a[196:199], v247 offset:24576\n" \
"v_exp_f32 v115, v115\n" \
"v_mfma_f32_16x16x32_bf16 v[20:23], a[216:219], a[168:171], v[232:235]\n" \
"v_exp_f32 v116, v116\n" \
"v_mfma_f32_16x16x32_bf16 v[20:23], a[220:223], a[172:175], v[20:23]\n" \
"v_cvt_pk_bf16_f32 v113, v114, v115\n" \
"v_add_f32 v78, v78, v76\n" \
"v_mfma_f32_16x16x32_bf16 v[20:23], a[208:211], a[160:163], v[20:23]\n" \
"v_exp_f32 v117, v117\n" \
"v_mfma_f32_16x16x32_bf16 v[20:23], a[212:215], a[164:167], v[20:23]\n" \
"ds_read_b128 a[200:203], v247 offset:17408\n" \
"v_rcp_f32 v76, v78\n" \
"v_mfma_f32_16x16x32_bf16 v[12:15], a[208:211], v[208:211], v[228:231]\n" \
"v_exp_f32 v118, v118\n" \
"v_mfma_f32_16x16x32_bf16 v[12:15], a[212:215], v[212:215], v[12:15]\n" \
"v_cvt_pk_bf16_f32 v114, v116, v117\n" \
"v_mul_f32 v78, 0x4d800000, v78\n" \
"v_mfma_f32_16x16x32_bf16 v[12:15], a[216:219], v[216:219], v[12:15]\n" \
"v_exp_f32 v119, v119\n" \
"v_mfma_f32_16x16x32_bf16 v[12:15], a[220:223], v[220:223], v[12:15]\n" \
"ds_read_b128 a[204:207], v247 offset:25600\n" \
"v_add_u32 v77, 0x1c90080, v249\n" \
"v_mfma_f32_16x16x32_bf16 v[4:7], a[216:219], v[200:203], v[224:227]\n" \
"v_exp_f32 v120, v120\n" \
"v_mfma_f32_16x16x32_bf16 v[4:7], a[220:223], v[204:207], v[4:7]\n" \
"v_cvt_pk_bf16_f32 v115, v118, v119\n" \
"v_cndmask_b32 v77, v249, v77, s[16:17]\n" \
"v_mfma_f32_16x16x32_bf16 v[4:7], a[208:211], v[192:195], v[4:7]\n" \
"s_waitcnt lgkmcnt(4)\n" \
"v_exp_f32 v121, v121\n" \
"v_mfma_f32_16x16x32_bf16 v[4:7], a[212:215], v[196:199], v[4:7]\n" \
"ds_read_b128 a[208:211], v247 offset:18432\n" \
"v_accvgpr_read_b32 v64, a0\n" \
"v_mfma_f32_16x16x32_bf16 v[32:35], a[224:227], v[192:195], v[224:227]\n" \
"v_exp_f32 v122, v122\n" \
"v_mfma_f32_16x16x32_bf16 v[32:35], a[228:231], v[196:199], v[32:35]\n" \
"v_cvt_pk_bf16_f32 v120, v120, v121\n" \
"v_accvgpr_read_b32 v65, a1\n" \
"v_mfma_f32_16x16x32_bf16 v[32:35], a[232:235], v[200:203], v[32:35]\n" \
"v_accvgpr_read_b32 v66, a2\n" \
"v_accvgpr_read_b32 v67, a3\n" \
"v_mfma_f32_16x16x32_bf16 v[32:35], a[236:239], v[204:207], v[32:35]\n" \
"ds_read_b128 a[212:215], v247 offset:26624\n" \
"v_accvgpr_read_b32 v68, a4\n" \
"v_mfma_f32_16x16x32_bf16 v[242:245], a[120:123], v[112:115], v[242:245]\n" \
"v_exp_f32 v123, v123\n" \
"v_mfma_f32_16x16x32_bf16 v[40:43], a[232:235], v[216:219], v[228:231]\n" \
"v_accvgpr_read_b32 v69, a5\n" \
"v_accvgpr_read_b32 v70, a6\n" \
"v_accvgpr_read_b32 v71, a7\n" \
"v_exp_f32 v124, v124\n" \
"v_pk_mul_f32 v[64:65], v[64:65], v[76:77] op_sel_hi:[1,0]\n" \
"v_pk_mul_f32 v[66:67], v[66:67], v[76:77] op_sel_hi:[1,0]\n" \
"v_pk_mul_f32 v[68:69], v[68:69], v[76:77] op_sel_hi:[1,0]\n" \
"v_pk_mul_f32 v[70:71], v[70:71], v[76:77] op_sel_hi:[1,0]\n" \
"v_mfma_f32_16x16x32_bf16 v[40:43], a[236:239], v[220:223], v[40:43]\n" \
"v_cvt_pk_bf16_f32 v121, v122, v123\n" \
"v_cvt_pk_bf16_f32 v96, v64, v65\n" \
"v_mfma_f32_16x16x32_bf16 v[40:43], a[224:227], v[208:211], v[40:43]\n" \
"v_exp_f32 v125, v125\n" \
"v_mfma_f32_16x16x32_bf16 v[40:43], a[228:231], v[212:215], v[40:43]\n" \
"ds_read_b128 a[216:219], v247 offset:19456\n" \
"v_cvt_pk_bf16_f32 v97, v66, v67\n" \
"v_cvt_pk_bf16_f32 v98, v68, v69\n" \
"v_cvt_pk_bf16_f32 v99, v70, v71\n" \
"v_pk_mul_f32 v[64:65], v[64:65], v[66:67]\n" \
"v_pk_fma_f32 v[64:65], v[68:69], v[70:71], v[64:65]\n" \
"v_mfma_f32_16x16x32_bf16 v[48:51], a[224:227], a[160:163], v[232:235]\n" \
"v_exp_f32 v126, v126\n" \
"v_mfma_f32_16x16x32_bf16 v[48:51], a[228:231], a[164:167], v[48:51]\n" \
"v_cvt_pk_bf16_f32 v122, v124, v125\n" \
"v_fma_f32 v78, v64, v65, v78\n" \
"v_mfma_f32_16x16x32_bf16 v[48:51], a[232:235], a[168:171], v[48:51]\n" \
"v_exp_f32 v127, v127\n" \
"v_mfma_f32_16x16x32_bf16 v[48:51], a[236:239], a[172:175], v[48:51]\n" \
"buffer_store_dwordx4 v[96:99], v77, s[56:59], s77 offen offset:0 sc1\n" \
"ds_read_b128 a[220:223], v247 offset:27648\n" \
"v_cvt_pk_bf16_f32 v123, v126, v127\n" \
"v_accvgpr_read_b32 v64, a8\n" \
"v_accvgpr_read_b32 v65, a9\n" \
"v_mfma_f32_16x16x32_bf16 v[56:59], a[232:235], a[184:187], v[236:239]\n" \
"s_xor_b64 s[52:53], s[44:45], s[42:43]\n" \
"v_accvgpr_read_b32 v66, a10\n" \
"v_accvgpr_read_b32 v67, a11\n" \
"v_mfma_f32_16x16x32_bf16 v[56:59], a[236:239], a[188:191], v[56:59]\n" \
"s_xor_b64 s[54:55], s[46:47], s[42:43]\n" \
"v_accvgpr_read_b32 v68, a12\n" \
"v_accvgpr_read_b32 v69, a13\n" \
"v_accvgpr_read_b32 v70, a14\n" \
"v_accvgpr_read_b32 v71, a15\n" \
"v_pk_mul_f32 v[64:65], v[64:65], v[76:77] op_sel_hi:[1,0]\n" \
"v_pk_mul_f32 v[66:67], v[66:67], v[76:77] op_sel_hi:[1,0]\n" \
"v_pk_mul_f32 v[68:69], v[68:69], v[76:77] op_sel_hi:[1,0]\n" \
"Lpg6771_%=:\n" \
"s_cbranch_execz Lcb0_%=\n" \
"v_pk_mul_f32 v[70:71], v[70:71], v[76:77] op_sel_hi:[1,0]\n" \
"v_cvt_pk_bf16_f32 v100, v64, v65\n" \
"v_cvt_pk_bf16_f32 v101, v66, v67\n" \
"v_cvt_pk_bf16_f32 v102, v68, v69\n" \
"v_cvt_pk_bf16_f32 v103, v70, v71\n" \
"v_pk_mul_f32 v[64:65], v[64:65], v[66:67]\n" \
"v_pk_fma_f32 v[64:65], v[68:69], v[70:71], v[64:65]\n" \
"v_mfma_f32_16x16x32_bf16 v[56:59], a[224:227], a[176:179], v[56:59]\n" \
"v_fma_f32 v78, v64, v65, v78\n" \
"s_or_b64 s[52:53], s[52:53], s[54:55]\n" \
"v_mfma_f32_16x16x32_bf16 v[56:59], a[228:231], a[180:183], v[56:59]\n" \
"ds_read_b128 a[224:227], v247 offset:20480\n" \
"v_mfma_f32_16x16x32_bf16 v[242:245], a[124:127], v[120:123], v[242:245]\n" \
"buffer_store_dwordx4 v[100:103], v77, s[56:59], s77 offen offset:2048 sc1\n" \
"s_xor_b64 s[54:55], s[48:49], s[42:43]\n" \
"s_add_u32 s77, s77, 0x1000\n" \
"v_accvgpr_read_b32 v64, a16\n" \
"v_accvgpr_read_b32 v65, a17\n" \
"v_mfma_f32_16x16x32_bf16 v[60:63], a[240:243], a[176:179], v[236:239]\n" \
"s_or_b64 s[52:53], s[52:53], s[54:55]\n" \
"v_accvgpr_read_b32 v66, a18\n" \
"v_accvgpr_read_b32 v67, a19\n" \
"v_mfma_f32_16x16x32_bf16 v[60:63], a[244:247], a[180:183], v[60:63]\n" \
"s_xor_b64 s[54:55], s[50:51], s[42:43]\n" \
"v_accvgpr_read_b32 v68, a20\n" \
"v_accvgpr_read_b32 v69, a21\n" \
"v_accvgpr_read_b32 v70, a22\n" \
"v_accvgpr_read_b32 v71, a23\n" \
"v_pk_mul_f32 v[64:65], v[64:65], v[76:77] op_sel_hi:[1,0]\n" \
"v_pk_mul_f32 v[66:67], v[66:67], v[76:77] op_sel_hi:[1,0]\n" \
"v_pk_mul_f32 v[68:69], v[68:69], v[76:77] op_sel_hi:[1,0]\n" \
"v_pk_mul_f32 v[70:71], v[70:71], v[76:77] op_sel_hi:[1,0]\n" \
"v_mfma_f32_16x16x32_bf16 v[60:63], a[248:251], a[184:187], v[60:63]\n" \
"v_cvt_pk_bf16_f32 v96, v64, v65\n" \
"v_cvt_pk_bf16_f32 v97, v66, v67\n" \
"v_cvt_pk_bf16_f32 v98, v68, v69\n" \
"v_cvt_pk_bf16_f32 v99, v70, v71\n" \
"s_or_b64 s[52:53], s[52:53], s[54:55]\n" \
"v_pk_mul_f32 v[64:65], v[64:65], v[66:67]\n" \
"v_pk_fma_f32 v[64:65], v[68:69], v[70:71], v[64:65]\n" \
"v_mfma_f32_16x16x32_bf16 v[60:63], a[252:255], a[188:191], v[60:63]\n" \
"ds_read_b128 a[228:231], v247 offset:28672\n" \
"v_fma_f32 v78, v64, v65, v78\n" \
"v_mfma_f32_16x16x32_bf16 v[52:55], a[248:251], a[168:171], v[232:235]\n" \
"buffer_store_dwordx4 v[96:99], v77, s[56:59], s77 offen offset:0 sc1\n" \
"s_and_b64 s[52:53], s[52:53], s[8:9]\n" \
"s_cbranch_scc1 Lrs2_%=\n" \
"Lrr2_%=:\n" \
"v_mfma_f32_16x16x32_bf16 v[52:55], a[252:255], a[172:175], v[52:55]\n" \
"s_mov_b64 s[8:9], s[10:11]\n" \
"v_accvgpr_read_b32 v64, a24\n" \
"v_accvgpr_read_b32 v65, a25\n" \
"v_mfma_f32_16x16x32_bf16 v[52:55], a[240:243], a[160:163], v[52:55]\n" \
"v_accvgpr_read_b32 v66, a26\n" \
"v_accvgpr_read_b32 v67, a27\n" \
"v_mfma_f32_16x16x32_bf16 v[52:55], a[244:247], a[164:167], v[52:55]\n" \
"ds_read_b128 a[232:235], v247 offset:21504\n" \
"v_accvgpr_read_b32 v68, a28\n" \
"v_accvgpr_read_b32 v69, a29\n" \
"v_accvgpr_read_b32 v70, a30\n" \
"v_accvgpr_read_b32 v71, a31\n" \
"v_pk_mul_f32 v[64:65], v[64:65], v[76:77] op_sel_hi:[1,0]\n" \
"v_pk_mul_f32 v[66:67], v[66:67], v[76:77] op_sel_hi:[1,0]\n" \
"v_pk_mul_f32 v[68:69], v[68:69], v[76:77] op_sel_hi:[1,0]\n" \
"v_pk_mul_f32 v[70:71], v[70:71], v[76:77] op_sel_hi:[1,0]\n" \
"v_mfma_f32_16x16x32_bf16 v[44:47], a[240:243], v[208:211], v[228:231]\n" \
"v_cvt_pk_bf16_f32 v100, v64, v65\n" \
"v_cvt_pk_bf16_f32 v101, v66, v67\n" \
"v_cvt_pk_bf16_f32 v102, v68, v69\n" \
"v_cvt_pk_bf16_f32 v103, v70, v71\n" \
"v_pk_mul_f32 v[64:65], v[64:65], v[66:67]\n" \
"v_pk_fma_f32 v[64:65], v[68:69], v[70:71], v[64:65]\n" \
"v_mfma_f32_16x16x32_bf16 v[44:47], a[244:247], v[212:215], v[44:47]\n" \
"v_fma_f32 v78, v64, v65, v78\n" \
"v_mfma_f32_16x16x32_bf16 v[44:47], a[248:251], v[216:219], v[44:47]\n" \
"v_mfma_f32_16x16x32_bf16 v[44:47], a[252:255], v[220:223], v[44:47]\n" \
"buffer_store_dwordx4 v[100:103], v77, s[56:59], s77 offen offset:2048 sc1\n" \
"ds_read_b128 a[236:239], v247 offset:29696\n" \
"v_cmp_class_f32 vcc, v78, s94\n" \
"s_sub_u32 s77, s77, 4096\n" \
"v_mfma_f32_16x16x32_bf16 v[36:39], a[248:251], v[200:203], v[224:227]\n" \
"s_or_b64 s[92:93], s[92:93], vcc\n" \
"v_mfma_f32_16x16x32_bf16 v[36:39], a[252:255], v[204:207], v[36:39]\n" \
"v_mfma_f32_16x16x32_bf16 v[36:39], a[240:243], v[192:195], v[36:39]\n" \
"s_waitcnt vmcnt(8) lgkmcnt(4)\n" \
"v_mfma_f32_16x16x32_bf16 v[36:39], a[244:247], v[196:199], v[36:39]\n" \
"s_barrier\n" \
"ds_read_b128 a[240:243], v247 offset:22528\n" \
"v_mfma_f32_16x16x32_bf16 a[32:35], a[192:195], v[72:75], a[32:35]\n" \
"v_exp_f32 v0, v0\n" \
"v_mfma_f32_16x16x32_bf16 a[32:35], a[196:199], v[104:107], a[32:35]\n" \
"v_cvt_pk_bf16_f32 v123, v126, v127\n" \
"v_exp_f32 v1, v1\n" \
"v_exp_f32 v2, v2\n" \
"v_cvt_pk_bf16_f32 v0, v0, v1\n" \
"v_exp_f32 v3, v3\n" \
"v_exp_f32 v4, v4\n" \
"v_mfma_f32_16x16x32_bf16 v[128:131], a[192:195], v[80:83], v[128:131]\n" \
"s_xor_b32 m0, s73, 0x8000\n" \
"ds_read_b128 a[244:247], v247 offset:30720\n" \
"v_cvt_pk_bf16_f32 v1, v2, v3\n" \
"v_mfma_f32_16x16x32_bf16 v[128:131], a[196:199], v[112:115], v[128:131]\n" \
"ds_read_b128 a[248:251], v247 offset:23552\n" \
"global_load_lds_dwordx4 v250, s[36:37] offset:-4096 sc1\n" \
"v_mfma_f32_16x16x32_bf16 v[160:163], a[192:195], v[88:91], v[160:163]\n" \
"ds_read_b128 a[252:255], v247 offset:31744\n" \
"global_load_lds_dwordx4 v250, s[36:37] offset:-3072 sc1\n" \
"v_mfma_f32_16x16x32_bf16 v[160:163], a[196:199], v[120:123], v[160:163]\n" \
"s_add_u32 s70, s82, 0x10000\n" \
"ds_read_b128 a[192:195], v252 offset:0\n" \
"v_add_u32 v247, s70, v248\n" \
"v_mfma_f32_16x16x32_bf16 v[164:167], a[200:203], v[88:91], v[164:167]\n" \
"v_accvgpr_mov_b32 a0, a120\n" \
"v_accvgpr_mov_b32 a1, a121\n" \
"v_mfma_f32_16x16x32_bf16 v[164:167], a[204:207], v[120:123], v[164:167]\n" \
"v_accvgpr_mov_b32 a2, a122\n" \
"v_accvgpr_mov_b32 a3, a123\n" \
"v_mfma_f32_16x16x32_bf16 v[132:135], a[200:203], v[80:83], v[132:135]\n" \
"ds_read_b128 a[196:199], v252 offset:2048\n" \
"v_accvgpr_mov_b32 a4, a124\n" \
"v_mfma_f32_16x16x32_bf16 v[132:135], a[204:207], v[112:115], v[132:135]\n" \
"v_accvgpr_mov_b32 a5, a125\n" \
"v_accvgpr_mov_b32 a6, a126\n" \
"v_mfma_f32_16x16x32_bf16 a[36:39], a[200:203], v[72:75], a[36:39]\n" \
"v_accvgpr_mov_b32 a7, a127\n" \
"v_mov_b32 v64, 0x30000\n" \
"v_mfma_f32_16x16x32_bf16 a[36:39], a[204:207], v[104:107], a[36:39]\n" \
"ds_read_b128 a[200:203], v252 offset:4096\n" \
"global_load_lds_dwordx4 v250, s[36:37] offset:-2048 sc1\n" \
"v_mfma_f32_16x16x32_bf16 a[40:43], a[208:211], v[72:75], a[40:43]\n" \
"v_exp_f32 v5, v5\n" \
"v_mfma_f32_16x16x32_bf16 a[40:43], a[212:215], v[104:107], a[40:43]\n" \
"v_exp_f32 v6, v6\n" \
"v_mfma_f32_16x16x32_bf16 v[136:139], a[208:211], v[80:83], v[136:139]\n" \
"ds_read_b128 a[204:207], v252 offset:6144\n" \
"v_cvt_pk_bf16_f32 v2, v4, v5\n" \
"v_mfma_f32_16x16x32_bf16 v[136:139], a[212:215], v[112:115], v[136:139]\n" \
"v_exp_f32 v7, v7\n" \
"v_mfma_f32_16x16x32_bf16 v[168:171], a[208:211], v[88:91], v[168:171]\n" \
"v_exp_f32 v8, v8\n" \
"v_mfma_f32_16x16x32_bf16 v[168:171], a[212:215], v[120:123], v[168:171]\n" \
"ds_read_b128 a[208:211], v252 offset:256\n" \
"global_load_lds_dwordx4 v250, s[36:37] offset:-1024 sc1\n" \
"v_cvt_pk_bf16_f32 v3, v6, v7\n" \
"v_mfma_f32_16x16x32_bf16 v[172:175], a[216:219], v[88:91], v[172:175]\n" \
"v_exp_f32 v9, v9\n" \
"v_mfma_f32_16x16x32_bf16 v[172:175], a[220:223], v[120:123], v[172:175]\n" \
"v_exp_f32 v10, v10\n" \
"v_cvt_pk_bf16_f32 v8, v8, v9\n" \
"v_mfma_f32_16x16x32_bf16 v[140:143], a[216:219], v[80:83], v[140:143]\n" \
"ds_read_b128 a[212:215], v252 offset:2304\n" \
"global_load_lds_dwordx4 v250, s[36:37] offset:0 sc1\n" \
"v_mfma_f32_16x16x32_bf16 v[140:143], a[220:223], v[112:115], v[140:143]\n" \
"v_exp_f32 v11, v11\n" \
"v_mfma_f32_16x16x32_bf16 a[44:47], a[216:219], v[72:75], a[44:47]\n" \
"s_waitcnt lgkmcnt(6)\n" \
"v_exp_f32 v12, v12\n" \
"v_mfma_f32_16x16x32_bf16 a[44:47], a[220:223], v[104:107], a[44:47]\n" \
"ds_read_b128 a[216:219], v252 offset:4352\n" \
"v_cvt_pk_bf16_f32 v9, v10, v11\n" \
"v_mfma_f32_16x16x32_bf16 a[48:51], a[224:227], v[72:75], a[48:51]\n" \
"v_exp_f32 v13, v13\n" \
"v_mfma_f32_16x16x32_bf16 a[48:51], a[228:231], v[104:107], a[48:51]\n" \
"v_exp_f32 v14, v14\n" \
"v_cvt_pk_bf16_f32 v10, v12, v13\n" \
"v_mfma_f32_16x16x32_bf16 v[144:147], a[224:227], v[80:83], v[144:147]\n" \
"ds_read_b128 a[220:223], v252 offset:6400\n" \
"global_load_lds_dwordx4 v250, s[36:37] offset:1024 sc1\n" \
"v_mfma_f32_16x16x32_bf16 v[144:147], a[228:231], v[112:115], v[144:147]\n" \
"v_exp_f32 v15, v15\n" \
"v_mfma_f32_16x16x32_bf16 v[176:179], a[224:227], v[88:91], v[176:179]\n" \
"v_exp_f32 v16, v16\n" \
"v_mfma_f32_16x16x32_bf16 v[176:179], a[228:231], v[120:123], v[176:179]\n" \
"ds_read_b128 a[224:227], v252 offset:8192\n" \
"global_load_lds_dwordx4 v250, s[36:37] offset:2048 sc1\n" \
"v_cvt_pk_bf16_f32 v11, v14, v15\n" \
"v_mfma_f32_16x16x32_bf16 v[180:183], a[232:235], v[88:91], v[180:183]\n" \
"v_exp_f32 v17, v17\n" \
"v_mfma_f32_16x16x32_bf16 v[180:183], a[236:239], v[120:123], v[180:183]\n" \
"v_exp_f32 v18, v18\n" \
"v_cvt_pk_bf16_f32 v16, v16, v17\n" \
"v_mfma_f32_16x16x32_bf16 v[238:241], a[120:123], v[0:3], v[238:241]\n" \
"v_exp_f32 v19, v19\n" \
"v_mfma_f32_16x16x32_bf16 v[238:241], a[124:127], v[8:11], v[238:241]\n" \
"v_exp_f32 v20, v20\n" \
"v_cvt_pk_bf16_f32 v17, v18, v19\n" \
"v_mfma_f32_16x16x32_bf16 v[148:151], a[232:235], v[80:83], v[148:151]\n" \
"ds_read_b128 a[228:231], v252 offset:10240\n" \
"global_load_lds_dwordx4 v250, s[36:37] offset:3072 sc1\n" \
"v_mfma_f32_16x16x32_bf16 v[148:151], a[236:239], v[112:115], v[148:151]\n" \
"v_exp_f32 v21, v21\n" \
"v_mfma_f32_16x16x32_bf16 a[52:55], a[232:235], v[72:75], a[52:55]\n" \
"v_exp_f32 v22, v22\n" \
"v_mfma_f32_16x16x32_bf16 a[52:55], a[236:239], v[104:107], a[52:55]\n" \
"s_add_u32 s36, s36, 34816\n" \
"ds_read_b128 a[232:235], v252 offset:12288\n" \
"v_cvt_pk_bf16_f32 v18, v20, v21\n" \
"v_mfma_f32_16x16x32_bf16 a[56:59], a[240:243], v[72:75], a[56:59]\n" \
"v_exp_f32 v23, v23\n" \
"v_mfma_f32_16x16x32_bf16 a[56:59], a[244:247], v[104:107], a[56:59]\n" \
"v_exp_f32 v24, v24\n" \
"v_mfma_f32_16x16x32_bf16 v[152:155], a[240:243], v[80:83], v[152:155]\n" \
"ds_read_b128 a[236:239], v252 offset:14336\n" \
"v_cvt_pk_bf16_f32 v19, v22, v23\n" \
"v_mfma_f32_16x16x32_bf16 v[152:155], a[244:247], v[112:115], v[152:155]\n" \
"v_exp_f32 v25, v25\n" \
"v_exp_f32 v26, v26\n" \
"v_cvt_pk_bf16_f32 v24, v24, v25\n" \
"v_mfma_f32_16x16x32_bf16 v[242:245], a[120:123], v[16:19], v[242:245]\n" \
"v_exp_f32 v27, v27\n" \
"v_mfma_f32_16x16x32_bf16 v[184:187], a[240:243], v[88:91], v[184:187]\n" \
"s_load_dwordx8 s[44:51], s[56:57], s79 glc\n" \
"v_exp_f32 v28, v28\n" \
"v_mfma_f32_16x16x32_bf16 v[184:187], a[244:247], v[120:123], v[184:187]\n" \
"ds_read_b128 a[240:243], v252 offset:8448\n" \
"v_cvt_pk_bf16_f32 v25, v26, v27\n" \
"v_exp_f32 v29, v29\n" \
"v_mfma_f32_16x16x32_bf16 v[188:191], a[248:251], v[88:91], v[188:191]\n" \
"v_exp_f32 v30, v30\n" \
"v_mfma_f32_16x16x32_bf16 v[188:191], a[252:255], v[120:123], v[188:191]\n" \
"v_cvt_pk_bf16_f32 v26, v28, v29\n" \
"v_exp_f32 v31, v31\n" \
"v_mfma_f32_16x16x32_bf16 v[156:159], a[248:251], v[80:83], v[156:159]\n" \
"ds_read_b128 a[244:247], v252 offset:10496\n" \
"v_cvt_pk_bf16_f32 v27, v30, v31\n" \
"v_mfma_f32_16x16x32_bf16 v[156:159], a[252:255], v[112:115], v[156:159]\n" \
"v_mfma_f32_16x16x32_bf16 v[242:245], a[124:127], v[24:27], v[242:245]\n" \
"v_mfma_f32_16x16x32_bf16 a[60:63], a[248:251], v[72:75], a[60:63]\n" \
"s_waitcnt vmcnt(12) lgkmcnt(6)\n" \
"v_mfma_f32_16x16x32_bf16 a[60:63], a[252:255], v[104:107], a[60:63]\n" \
"s_barrier\n" \
"ds_read_b128 a[248:251], v252 offset:12544\n" \
"ds_read_b128 a[120:123], v64\n" \
"ds_read_b128 a[124:127], v64\n" \
"s_mov_b64 exec, 0\n" \
"s_cmp_eq_u32 s61, 0\n" \
"s_cbranch_scc1 Lst1x0_%=\n" \
"s_cmp_eq_u32 s61, 1\n" \
"s_cbranch_scc1 Lst1x1_%=\n" \
"s_cmp_eq_u32 s61, 2\n" \
"s_cbranch_scc1 Lst1x2_%=\n" \
"s_branch Lst1x3_%=\n" \
"Lcb1_%=:\n" \
"s_mov_b64 exec, -1\n" \
"s_waitcnt lgkmcnt(0)\n" \
"s_cmp_lg_u32 s63, 0\n" \
"s_cbranch_scc1 LA1g_%=\n" \
"s_xor_b64 s[52:53], s[44:45], s[42:43]\n" \
"s_xor_b64 s[54:55], s[46:47], s[42:43]\n" \
"s_or_b64 s[52:53], s[52:53], s[54:55]\n" \
"s_xor_b64 s[54:55], s[48:49], s[42:43]\n" \
"s_or_b64 s[52:53], s[52:53], s[54:55]\n" \
"s_xor_b64 s[54:55], s[50:51], s[42:43]\n" \
"s_or_b64 s[52:53], s[52:53], s[54:55]\n" \
"s_and_b64 s[52:53], s[52:53], s[8:9]\n" \
"s_cbranch_scc1 Lrs3_%=\n" \
"Lrr3_%=:\n" \
"s_branch LA1g_%=\n" \
"Lpg7030_%=:\n" \
"s_cbranch_execz Lcb0_%=\n" \
"Lloop2_%=:\n" \
"v_mfma_f32_16x16x32_bf16 a[64:67], a[192:195], v[0:3], a[64:67]\n" \
"v_exp_f32 v64, v64\n" \
"v_mfma_f32_16x16x32_bf16 a[64:67], a[196:199], v[32:35], a[64:67]\n" \
"v_cvt_pk_bf16_f32 v59, v62, v63\n" \
"v_add_f32 v240, v240, v64\n" \
"v_exp_f32 v65, v65\n" \
"v_mfma_f32_16x16x32_bf16 a[96:99], a[192:195], v[8:11], a[96:99]\n" \
"s_mov_b32 m0, s73\n" \
"ds_read_b128 a[244:247], v247 offset:14336\n" \
"v_add_f32 v240, v240, v65\n" \
"v_mfma_f32_16x16x32_bf16 a[96:99], a[196:199], v[40:43], a[96:99]\n" \
"ds_read_b128 a[248:251], v247 offset:7168\n" \
"global_load_lds_dwordx4 v250, s[36:37] offset:-4096 sc1\n" \
"v_mfma_f32_16x16x32_bf16 v[128:131], a[192:195], v[16:19], v[128:131]\n" \
"ds_read_b128 a[252:255], v247 offset:15360\n" \
"v_exp_f32 v66, v66\n" \
"v_mfma_f32_16x16x32_bf16 v[128:131], a[196:199], v[48:51], v[128:131]\n" \
"v_add_f32 v240, v240, v66\n" \
"v_cvt_pk_bf16_f32 v64, v64, v65\n" \
"v_mfma_f32_16x16x32_bf16 v[160:163], a[192:195], v[24:27], v[160:163]\n" \
"v_exp_f32 v67, v67\n" \
"v_mfma_f32_16x16x32_bf16 v[160:163], a[196:199], v[56:59], v[160:163]\n" \
"ds_read_b128 a[192:195], v246 offset:0\n" \
"global_load_lds_dwordx4 v250, s[36:37] offset:-3072 sc1\n" \
"v_add_f32 v240, v240, v67\n" \
"v_mfma_f32_16x16x32_bf16 v[164:167], a[200:203], v[24:27], v[164:167]\n" \
"v_exp_f32 v68, v68\n" \
"v_mfma_f32_16x16x32_bf16 v[164:167], a[204:207], v[56:59], v[164:167]\n" \
"v_add_f32 v240, v240, v68\n" \
"v_cvt_pk_bf16_f32 v65, v66, v67\n" \
"v_mfma_f32_16x16x32_bf16 v[132:135], a[200:203], v[16:19], v[132:135]\n" \
"v_exp_f32 v69, v69\n" \
"v_mfma_f32_16x16x32_bf16 v[132:135], a[204:207], v[48:51], v[132:135]\n" \
"ds_read_b128 a[196:199], v246 offset:2048\n" \
"v_add_f32 v240, v240, v69\n" \
"v_mfma_f32_16x16x32_bf16 a[100:103], a[200:203], v[8:11], a[100:103]\n" \
"v_exp_f32 v70, v70\n" \
"v_mfma_f32_16x16x32_bf16 a[100:103], a[204:207], v[40:43], a[100:103]\n" \
"v_add_f32 v240, v240, v70\n" \
"v_cvt_pk_bf16_f32 v66, v68, v69\n" \
"v_mfma_f32_16x16x32_bf16 a[68:71], a[200:203], v[0:3], a[68:71]\n" \
"v_exp_f32 v71, v71\n" \
"v_mfma_f32_16x16x32_bf16 a[68:71], a[204:207], v[32:35], a[68:71]\n" \
"ds_read_b128 a[200:203], v246 offset:4096\n" \
"global_load_lds_dwordx4 v250, s[36:37] offset:-2048 sc1\n" \
"v_add_f32 v240, v240, v71\n" \
"v_mfma_f32_16x16x32_bf16 a[72:75], a[208:211], v[0:3], a[72:75]\n" \
"v_exp_f32 v72, v72\n" \
"v_mfma_f32_16x16x32_bf16 a[72:75], a[212:215], v[32:35], a[72:75]\n" \
"v_add_f32 v241, v241, v72\n" \
"v_cvt_pk_bf16_f32 v67, v70, v71\n" \
"v_mfma_f32_16x16x32_bf16 a[104:107], a[208:211], v[8:11], a[104:107]\n" \
"v_exp_f32 v73, v73\n" \
"v_mfma_f32_16x16x32_bf16 a[104:107], a[212:215], v[40:43], a[104:107]\n" \
"ds_read_b128 a[204:207], v246 offset:6144\n" \
"global_load_lds_dwordx4 v250, s[36:37] offset:-1024 sc1\n" \
"v_add_f32 v241, v241, v73\n" \
"v_mfma_f32_16x16x32_bf16 v[136:139], a[208:211], v[16:19], v[136:139]\n" \
"v_exp_f32 v74, v74\n" \
"v_mfma_f32_16x16x32_bf16 v[136:139], a[212:215], v[48:51], v[136:139]\n" \
"v_add_f32 v241, v241, v74\n" \
"v_cvt_pk_bf16_f32 v72, v72, v73\n" \
"v_mfma_f32_16x16x32_bf16 v[168:171], a[208:211], v[24:27], v[168:171]\n" \
"v_exp_f32 v75, v75\n" \
"v_mfma_f32_16x16x32_bf16 v[168:171], a[212:215], v[56:59], v[168:171]\n" \
"ds_read_b128 a[208:211], v246 offset:256\n" \
"v_add_f32 v241, v241, v75\n" \
"v_mfma_f32_16x16x32_bf16 v[172:175], a[216:219], v[24:27], v[172:175]\n" \
"v_exp_f32 v76, v76\n" \
"v_mfma_f32_16x16x32_bf16 v[172:175], a[220:223], v[56:59], v[172:175]\n" \
"v_add_f32 v241, v241, v76\n" \
"v_cvt_pk_bf16_f32 v73, v74, v75\n" \
"v_mfma_f32_16x16x32_bf16 v[140:143], a[216:219], v[16:19], v[140:143]\n" \
"v_exp_f32 v77, v77\n" \
"v_mfma_f32_16x16x32_bf16 v[140:143], a[220:223], v[48:51], v[140:143]\n" \
"ds_read_b128 a[212:215], v246 offset:2304\n" \
"global_load_lds_dwordx4 v250, s[36:37] offset:0 sc1\n" \
"v_add_f32 v241, v241, v77\n" \
"v_mfma_f32_16x16x32_bf16 a[108:111], a[216:219], v[8:11], a[108:111]\n" \
"v_exp_f32 v78, v78\n" \
"v_mfma_f32_16x16x32_bf16 a[108:111], a[220:223], v[40:43], a[108:111]\n" \
"v_add_f32 v241, v241, v78\n" \
"v_cvt_pk_bf16_f32 v74, v76, v77\n" \
"v_mfma_f32_16x16x32_bf16 a[76:79], a[216:219], v[0:3], a[76:79]\n" \
"s_waitcnt lgkmcnt(6)\n" \
"v_exp_f32 v79, v79\n" \
"v_mfma_f32_16x16x32_bf16 a[76:79], a[220:223], v[32:35], a[76:79]\n" \
"ds_read_b128 a[216:219], v246 offset:4352\n" \
"global_load_lds_dwordx4 v250, s[36:37] offset:1024 sc1\n" \
"v_add_f32 v241, v241, v79\n" \
"v_mfma_f32_16x16x32_bf16 a[80:83], a[224:227], v[0:3], a[80:83]\n" \
"v_exp_f32 v80, v80\n" \
"v_mfma_f32_16x16x32_bf16 a[80:83], a[228:231], v[32:35], a[80:83]\n" \
"v_add_f32 v244, v244, v80\n" \
"v_cvt_pk_bf16_f32 v75, v78, v79\n" \
"v_mfma_f32_16x16x32_bf16 a[112:115], a[224:227], v[8:11], a[112:115]\n" \
"v_exp_f32 v81, v81\n" \
"v_mfma_f32_16x16x32_bf16 a[112:115], a[228:231], v[40:43], a[112:115]\n" \
"ds_read_b128 a[220:223], v246 offset:6400\n" \
"v_add_f32 v244, v244, v81\n" \
"v_mfma_f32_16x16x32_bf16 v[144:147], a[224:227], v[16:19], v[144:147]\n" \
"v_exp_f32 v82, v82\n" \
"v_mfma_f32_16x16x32_bf16 v[144:147], a[228:231], v[48:51], v[144:147]\n" \
"v_add_f32 v244, v244, v82\n" \
"v_cvt_pk_bf16_f32 v80, v80, v81\n" \
"v_mfma_f32_16x16x32_bf16 v[176:179], a[224:227], v[24:27], v[176:179]\n" \
"v_exp_f32 v83, v83\n" \
"v_mfma_f32_16x16x32_bf16 v[176:179], a[228:231], v[56:59], v[176:179]\n" \
"ds_read_b128 a[224:227], v246 offset:8192\n" \
"global_load_lds_dwordx4 v250, s[36:37] offset:2048 sc1\n" \
"v_add_f32 v244, v244, v83\n" \
"v_mfma_f32_16x16x32_bf16 v[180:183], a[232:235], v[24:27], v[180:183]\n" \
"v_exp_f32 v84, v84\n" \
"v_mfma_f32_16x16x32_bf16 v[180:183], a[236:239], v[56:59], v[180:183]\n" \
"v_add_f32 v244, v244, v84\n" \
"v_cvt_pk_bf16_f32 v81, v82, v83\n" \
"v_mfma_f32_16x16x32_bf16 v[148:151], a[232:235], v[16:19], v[148:151]\n" \
"v_exp_f32 v85, v85\n" \
"v_mfma_f32_16x16x32_bf16 v[148:151], a[236:239], v[48:51], v[148:151]\n" \
"ds_read_b128 a[228:231], v246 offset:10240\n" \
"global_load_lds_dwordx4 v250, s[36:37] offset:3072 sc1\n" \
"v_add_f32 v244, v244, v85\n" \
"v_mfma_f32_16x16x32_bf16 a[116:119], a[232:235], v[8:11], a[116:119]\n" \
"s_add_u32 s36, s36, 34816\n" \
"v_exp_f32 v86, v86\n" \
"v_mfma_f32_16x16x32_bf16 a[116:119], a[236:239], v[40:43], a[116:119]\n" \
"v_add_f32 v244, v244, v86\n" \
"v_cvt_pk_bf16_f32 v82, v84, v85\n" \
"v_mfma_f32_16x16x32_bf16 a[84:87], a[232:235], v[0:3], a[84:87]\n" \
"v_exp_f32 v87, v87\n" \
"v_mfma_f32_16x16x32_bf16 a[84:87], a[236:239], v[32:35], a[84:87]\n" \
"ds_read_b128 a[232:235], v246 offset:12288\n" \
"v_add_f32 v244, v244, v87\n" \
"v_mfma_f32_16x16x32_bf16 a[88:91], a[240:243], v[0:3], a[88:91]\n" \
"v_exp_f32 v88, v88\n" \
"v_mfma_f32_16x16x32_bf16 a[88:91], a[244:247], v[32:35], a[88:91]\n" \
"v_add_f32 v245, v245, v88\n" \
"v_cvt_pk_bf16_f32 v83, v86, v87\n" \
"v_mfma_f32_16x16x32_bf16 a[120:123], a[240:243], v[8:11], a[120:123]\n" \
"v_exp_f32 v89, v89\n" \
"v_mfma_f32_16x16x32_bf16 a[120:123], a[244:247], v[40:43], a[120:123]\n" \
"ds_read_b128 a[236:239], v246 offset:14336\n" \
"v_add_f32 v245, v245, v89\n" \
"v_mfma_f32_16x16x32_bf16 v[152:155], a[240:243], v[16:19], v[152:155]\n" \
"v_exp_f32 v90, v90\n" \
"v_mfma_f32_16x16x32_bf16 v[152:155], a[244:247], v[48:51], v[152:155]\n" \
"v_add_f32 v245, v245, v90\n" \
"v_cvt_pk_bf16_f32 v88, v88, v89\n" \
"v_mfma_f32_16x16x32_bf16 v[184:187], a[240:243], v[24:27], v[184:187]\n" \
"v_exp_f32 v91, v91\n" \
"v_mfma_f32_16x16x32_bf16 v[184:187], a[244:247], v[56:59], v[184:187]\n" \
"ds_read_b128 a[240:243], v246 offset:8448\n" \
"v_add_f32 v245, v245, v91\n" \
"v_mfma_f32_16x16x32_bf16 v[188:191], a[248:251], v[24:27], v[188:191]\n" \
"v_exp_f32 v92, v92\n" \
"v_mfma_f32_16x16x32_bf16 v[188:191], a[252:255], v[56:59], v[188:191]\n" \
"v_add_f32 v245, v245, v92\n" \
"v_cvt_pk_bf16_f32 v89, v90, v91\n" \
"v_mfma_f32_16x16x32_bf16 v[156:159], a[248:251], v[16:19], v[156:159]\n" \
"v_exp_f32 v93, v93\n" \
"v_mfma_f32_16x16x32_bf16 v[156:159], a[252:255], v[48:51], v[156:159]\n" \
"ds_read_b128 a[244:247], v246 offset:10496\n" \
"v_add_f32 v245, v245, v93\n" \
"v_mfma_f32_16x16x32_bf16 a[124:127], a[248:251], v[8:11], a[124:127]\n" \
"v_exp_f32 v94, v94\n" \
"v_mfma_f32_16x16x32_bf16 a[124:127], a[252:255], v[40:43], a[124:127]\n" \
"v_add_f32 v245, v245, v94\n" \
"v_cvt_pk_bf16_f32 v90, v92, v93\n" \
"v_mfma_f32_16x16x32_bf16 a[92:95], a[248:251], v[0:3], a[92:95]\n" \
"s_waitcnt vmcnt(8) lgkmcnt(6)\n" \
"v_exp_f32 v95, v95\n" \
"v_mfma_f32_16x16x32_bf16 a[92:95], a[252:255], v[32:35], a[92:95]\n" \
"s_barrier\n" \
"ds_read_b128 a[248:251], v246 offset:12544\n" \
"v_add_f32 v245, v245, v95\n" \
"v_mfma_f32_16x16x32_bf16 v[0:3], a[192:195], v[192:195], v[224:227]\n" \
"v_exp_f32 v96, v96\n" \
"v_mfma_f32_16x16x32_bf16 v[0:3], a[196:199], v[196:199], v[0:3]\n" \
"v_cvt_pk_bf16_f32 v91, v94, v95\n" \
"v_add_f32 v240, v240, v96\n" \
"v_mfma_f32_16x16x32_bf16 v[0:3], a[200:203], v[200:203], v[0:3]\n" \
"s_mov_b32 m0, s74\n" \
"v_exp_f32 v97, v97\n" \
"v_mfma_f32_16x16x32_bf16 v[0:3], a[204:207], v[204:207], v[0:3]\n" \
"ds_read_b128 a[252:255], v246 offset:14592\n" \
"v_add_f32 v240, v240, v97\n" \
"v_mfma_f32_16x16x32_bf16 v[8:11], a[200:203], v[216:219], v[228:231]\n" \
"global_load_lds_dwordx4 v250, s[38:39] offset:-4096 sc1\n" \
"global_load_lds_dwordx4 v250, s[38:39] offset:-3072 sc1\n" \
"v_mfma_f32_16x16x32_bf16 v[8:11], a[204:207], v[220:223], v[8:11]\n" \
"v_exp_f32 v98, v98\n" \
"v_mfma_f32_16x16x32_bf16 v[8:11], a[192:195], v[208:211], v[8:11]\n" \
"v_add_f32 v240, v240, v98\n" \
"v_cvt_pk_bf16_f32 v96, v96, v97\n" \
"v_mfma_f32_16x16x32_bf16 v[8:11], a[196:199], v[212:215], v[8:11]\n" \
"v_exp_f32 v99, v99\n" \
"v_mfma_f32_16x16x32_bf16 v[16:19], a[192:195], a[160:163], v[232:235]\n" \
"v_add_f32 v240, v240, v99\n" \
"v_exp_f32 v100, v100\n" \
"v_mfma_f32_16x16x32_bf16 v[16:19], a[196:199], a[164:167], v[16:19]\n" \
"v_add_f32 v240, v240, v100\n" \
"v_cvt_pk_bf16_f32 v97, v98, v99\n" \
"v_mfma_f32_16x16x32_bf16 v[16:19], a[200:203], a[168:171], v[16:19]\n" \
"v_exp_f32 v101, v101\n" \
"v_mfma_f32_16x16x32_bf16 v[16:19], a[204:207], a[172:175], v[16:19]\n" \
"v_add_f32 v240, v240, v101\n" \
"v_exp_f32 v102, v102\n" \
"v_mfma_f32_16x16x32_bf16 v[24:27], a[200:203], a[184:187], v[236:239]\n" \
"v_add_f32 v240, v240, v102\n" \
"v_cvt_pk_bf16_f32 v98, v100, v101\n" \
"v_mfma_f32_16x16x32_bf16 v[24:27], a[204:207], a[188:191], v[24:27]\n" \
"v_exp_f32 v103, v103\n" \
"v_mfma_f32_16x16x32_bf16 v[24:27], a[192:195], a[176:179], v[24:27]\n" \
"v_add_f32 v240, v240, v103\n" \
"v_mfma_f32_16x16x32_bf16 v[24:27], a[196:199], a[180:183], v[24:27]\n" \
"ds_read_b128 a[192:195], v247 offset:32768\n" \
"global_load_lds_dwordx4 v250, s[38:39] offset:-2048 sc1\n" \
"v_mfma_f32_16x16x32_bf16 v[28:31], a[208:211], a[176:179], v[236:239]\n" \
"v_exp_f32 v104, v104\n" \
"v_mfma_f32_16x16x32_bf16 v[28:31], a[212:215], a[180:183], v[28:31]\n" \
"v_add_f32 v241, v241, v104\n" \
"v_cvt_pk_bf16_f32 v99, v102, v103\n" \
"v_mfma_f32_16x16x32_bf16 v[28:31], a[216:219], a[184:187], v[28:31]\n" \
"v_exp_f32 v105, v105\n" \
"v_mfma_f32_16x16x32_bf16 v[28:31], a[220:223], a[188:191], v[28:31]\n" \
"ds_read_b128 a[196:199], v247 offset:40960\n" \
"global_load_lds_dwordx4 v250, s[38:39] offset:-1024 sc1\n" \
"v_add_f32 v241, v241, v105\n" \
"v_mfma_f32_16x16x32_bf16 v[20:23], a[216:219], a[168:171], v[232:235]\n" \
"v_exp_f32 v106, v106\n" \
"v_mfma_f32_16x16x32_bf16 v[20:23], a[220:223], a[172:175], v[20:23]\n" \
"v_add_f32 v241, v241, v106\n" \
"v_cvt_pk_bf16_f32 v104, v104, v105\n" \
"v_mfma_f32_16x16x32_bf16 v[20:23], a[208:211], a[160:163], v[20:23]\n" \
"v_exp_f32 v107, v107\n" \
"v_mfma_f32_16x16x32_bf16 v[20:23], a[212:215], a[164:167], v[20:23]\n" \
"ds_read_b128 a[200:203], v247 offset:33792\n" \
"v_add_f32 v241, v241, v107\n" \
"v_mfma_f32_16x16x32_bf16 v[12:15], a[208:211], v[208:211], v[228:231]\n" \
"v_exp_f32 v108, v108\n" \
"v_mfma_f32_16x16x32_bf16 v[12:15], a[212:215], v[212:215], v[12:15]\n" \
"v_add_f32 v241, v241, v108\n" \
"v_cvt_pk_bf16_f32 v105, v106, v107\n" \
"v_mfma_f32_16x16x32_bf16 v[12:15], a[216:219], v[216:219], v[12:15]\n" \
"v_exp_f32 v109, v109\n" \
"v_mfma_f32_16x16x32_bf16 v[12:15], a[220:223], v[220:223], v[12:15]\n" \
"ds_read_b128 a[204:207], v247 offset:41984\n" \
"global_load_lds_dwordx4 v250, s[38:39] offset:0 sc1\n" \
"v_add_f32 v241, v241, v109\n" \
"v_mfma_f32_16x16x32_bf16 v[4:7], a[216:219], v[200:203], v[224:227]\n" \
"v_exp_f32 v110, v110\n" \
"v_mfma_f32_16x16x32_bf16 v[4:7], a[220:223], v[204:207], v[4:7]\n" \
"v_add_f32 v241, v241, v110\n" \
"v_cvt_pk_bf16_f32 v106, v108, v109\n" \
"v_mfma_f32_16x16x32_bf16 v[4:7], a[208:211], v[192:195], v[4:7]\n" \
"s_waitcnt lgkmcnt(4)\n" \
"v_exp_f32 v111, v111\n" \
"v_mfma_f32_16x16x32_bf16 v[4:7], a[212:215], v[196:199], v[4:7]\n" \
"ds_read_b128 a[208:211], v247 offset:34816\n" \
"global_load_lds_dwordx4 v250, s[38:39] offset:1024 sc1\n" \
"v_add_f32 v241, v241, v111\n" \
"v_mfma_f32_16x16x32_bf16 v[32:35], a[224:227], v[192:195], v[224:227]\n" \
"v_exp_f32 v112, v112\n" \
"v_mfma_f32_16x16x32_bf16 v[32:35], a[228:231], v[196:199], v[32:35]\n" \
"v_add_f32 v244, v244, v112\n" \
"v_cvt_pk_bf16_f32 v107, v110, v111\n" \
"v_mfma_f32_16x16x32_bf16 v[32:35], a[232:235], v[200:203], v[32:35]\n" \
"v_exp_f32 v113, v113\n" \
"v_mfma_f32_16x16x32_bf16 v[32:35], a[236:239], v[204:207], v[32:35]\n" \
"ds_read_b128 a[212:215], v247 offset:43008\n" \
"v_add_f32 v244, v244, v113\n" \
"v_mfma_f32_16x16x32_bf16 v[40:43], a[232:235], v[216:219], v[228:231]\n" \
"v_exp_f32 v114, v114\n" \
"v_mfma_f32_16x16x32_bf16 v[40:43], a[236:239], v[220:223], v[40:43]\n" \
"v_add_f32 v244, v244, v114\n" \
"v_cvt_pk_bf16_f32 v112, v112, v113\n" \
"v_mfma_f32_16x16x32_bf16 v[40:43], a[224:227], v[208:211], v[40:43]\n" \
"v_exp_f32 v115, v115\n" \
"v_mfma_f32_16x16x32_bf16 v[40:43], a[228:231], v[212:215], v[40:43]\n" \
"ds_read_b128 a[216:219], v247 offset:35840\n" \
"global_load_lds_dwordx4 v250, s[38:39] offset:2048 sc1\n" \
"v_add_f32 v244, v244, v115\n" \
"v_mfma_f32_16x16x32_bf16 v[48:51], a[224:227], a[160:163], v[232:235]\n" \
"v_exp_f32 v116, v116\n" \
"v_mfma_f32_16x16x32_bf16 v[48:51], a[228:231], a[164:167], v[48:51]\n" \
"v_add_f32 v244, v244, v116\n" \
"v_cvt_pk_bf16_f32 v113, v114, v115\n" \
"v_mfma_f32_16x16x32_bf16 v[48:51], a[232:235], a[168:171], v[48:51]\n" \
"v_exp_f32 v117, v117\n" \
"v_mfma_f32_16x16x32_bf16 v[48:51], a[236:239], a[172:175], v[48:51]\n" \
"ds_read_b128 a[220:223], v247 offset:44032\n" \
"global_load_lds_dwordx4 v250, s[38:39] offset:3072 sc1\n" \
"v_add_f32 v244, v244, v117\n" \
"v_mfma_f32_16x16x32_bf16 v[56:59], a[232:235], a[184:187], v[236:239]\n" \
"s_add_u32 s38, s38, 34816\n" \
"v_exp_f32 v118, v118\n" \
"v_mfma_f32_16x16x32_bf16 v[56:59], a[236:239], a[188:191], v[56:59]\n" \
"v_add_f32 v244, v244, v118\n" \
"v_cvt_pk_bf16_f32 v114, v116, v117\n" \
"v_mfma_f32_16x16x32_bf16 v[56:59], a[224:227], a[176:179], v[56:59]\n" \
"v_exp_f32 v119, v119\n" \
"v_mfma_f32_16x16x32_bf16 v[56:59], a[228:231], a[180:183], v[56:59]\n" \
"ds_read_b128 a[224:227], v247 offset:36864\n" \
"v_add_f32 v244, v244, v119\n" \
"v_mfma_f32_16x16x32_bf16 v[60:63], a[240:243], a[176:179], v[236:239]\n" \
"v_exp_f32 v120, v120\n" \
"v_mfma_f32_16x16x32_bf16 v[60:63], a[244:247], a[180:183], v[60:63]\n" \
"v_add_f32 v245, v245, v120\n" \
"v_cvt_pk_bf16_f32 v115, v118, v119\n" \
"v_mfma_f32_16x16x32_bf16 v[60:63], a[248:251], a[184:187], v[60:63]\n" \
"v_exp_f32 v121, v121\n" \
"v_mfma_f32_16x16x32_bf16 v[60:63], a[252:255], a[188:191], v[60:63]\n" \
"ds_read_b128 a[228:231], v247 offset:45056\n" \
"v_add_f32 v245, v245, v121\n" \
"v_mfma_f32_16x16x32_bf16 v[52:55], a[248:251], a[168:171], v[232:235]\n" \
"v_exp_f32 v122, v122\n" \
"v_mfma_f32_16x16x32_bf16 v[52:55], a[252:255], a[172:175], v[52:55]\n" \
"v_add_f32 v245, v245, v122\n" \
"v_cvt_pk_bf16_f32 v120, v120, v121\n" \
"v_mfma_f32_16x16x32_bf16 v[52:55], a[240:243], a[160:163], v[52:55]\n" \
"v_exp_f32 v123, v123\n" \
"v_mfma_f32_16x16x32_bf16 v[52:55], a[244:247], a[164:167], v[52:55]\n" \
"ds_read_b128 a[232:235], v247 offset:37888\n" \
"v_add_f32 v245, v245, v123\n" \
"v_mfma_f32_16x16x32_bf16 v[44:47], a[240:243], v[208:211], v[228:231]\n" \
"v_exp_f32 v124, v124\n" \
"v_mfma_f32_16x16x32_bf16 v[44:47], a[244:247], v[212:215], v[44:47]\n" \
"v_add_f32 v245, v245, v124\n" \
"v_cvt_pk_bf16_f32 v121, v122, v123\n" \
"v_mfma_f32_16x16x32_bf16 v[44:47], a[248:251], v[216:219], v[44:47]\n" \
"v_exp_f32 v125, v125\n" \
"v_mfma_f32_16x16x32_bf16 v[44:47], a[252:255], v[220:223], v[44:47]\n" \
"ds_read_b128 a[236:239], v247 offset:46080\n" \
"v_add_f32 v245, v245, v125\n" \
"v_mfma_f32_16x16x32_bf16 v[36:39], a[248:251], v[200:203], v[224:227]\n" \
"v_exp_f32 v126, v126\n" \
"v_mfma_f32_16x16x32_bf16 v[36:39], a[252:255], v[204:207], v[36:39]\n" \
"v_add_f32 v245, v245, v126\n" \
"v_cvt_pk_bf16_f32 v122, v124, v125\n" \
"v_mfma_f32_16x16x32_bf16 v[36:39], a[240:243], v[192:195], v[36:39]\n" \
"s_waitcnt vmcnt(8) lgkmcnt(4)\n" \
"v_exp_f32 v127, v127\n" \
"v_mfma_f32_16x16x32_bf16 v[36:39], a[244:247], v[196:199], v[36:39]\n" \
"s_barrier\n" \
"ds_read_b128 a[240:243], v247 offset:38912\n" \
"v_add_f32 v245, v245, v127\n" \
"v_mfma_f32_16x16x32_bf16 a[64:67], a[192:195], v[64:67], a[64:67]\n" \
"v_exp_f32 v0, v0\n" \
"v_mfma_f32_16x16x32_bf16 a[64:67], a[196:199], v[96:99], a[64:67]\n" \
"v_cvt_pk_bf16_f32 v123, v126, v127\n" \
"v_add_f32 v240, v240, v0\n" \
"v_exp_f32 v1, v1\n" \
"v_mfma_f32_16x16x32_bf16 a[96:99], a[192:195], v[72:75], a[96:99]\n" \
"s_xor_b32 m0, s73, 0x8000\n" \
"ds_read_b128 a[244:247], v247 offset:47104\n" \
"v_add_f32 v240, v240, v1\n" \
"v_mfma_f32_16x16x32_bf16 a[96:99], a[196:199], v[104:107], a[96:99]\n" \
"ds_read_b128 a[248:251], v247 offset:39936\n" \
"global_load_lds_dwordx4 v250, s[36:37] offset:-4096 sc1\n" \
"v_mfma_f32_16x16x32_bf16 v[128:131], a[192:195], v[80:83], v[128:131]\n" \
"ds_read_b128 a[252:255], v247 offset:48128\n" \
"v_exp_f32 v2, v2\n" \
"v_mfma_f32_16x16x32_bf16 v[128:131], a[196:199], v[112:115], v[128:131]\n" \
"v_add_f32 v240, v240, v2\n" \
"v_cvt_pk_bf16_f32 v0, v0, v1\n" \
"v_mfma_f32_16x16x32_bf16 v[160:163], a[192:195], v[88:91], v[160:163]\n" \
"Lpg7398_%=:\n" \
"Lst1x0_%=:\n" \
"s_cbranch_execz Lpg9736_%=\n" \
"v_exp_f32 v3, v3\n" \
"v_mfma_f32_16x16x32_bf16 v[160:163], a[196:199], v[120:123], v[160:163]\n" \
"ds_read_b128 a[192:195], v252 offset:0\n" \
"global_load_lds_dwordx4 v250, s[36:37] offset:-3072 sc1\n" \
"v_add_f32 v240, v240, v3\n" \
"v_mfma_f32_16x16x32_bf16 v[164:167], a[200:203], v[88:91], v[164:167]\n" \
"v_exp_f32 v4, v4\n" \
"v_mfma_f32_16x16x32_bf16 v[164:167], a[204:207], v[120:123], v[164:167]\n" \
"v_add_f32 v240, v240, v4\n" \
"v_cvt_pk_bf16_f32 v1, v2, v3\n" \
"v_mfma_f32_16x16x32_bf16 v[132:135], a[200:203], v[80:83], v[132:135]\n" \
"v_exp_f32 v5, v5\n" \
"v_mfma_f32_16x16x32_bf16 v[132:135], a[204:207], v[112:115], v[132:135]\n" \
"ds_read_b128 a[196:199], v252 offset:2048\n" \
"v_add_f32 v240, v240, v5\n" \
"v_mfma_f32_16x16x32_bf16 a[100:103], a[200:203], v[72:75], a[100:103]\n" \
"v_exp_f32 v6, v6\n" \
"v_mfma_f32_16x16x32_bf16 a[100:103], a[204:207], v[104:107], a[100:103]\n" \
"v_add_f32 v240, v240, v6\n" \
"v_cvt_pk_bf16_f32 v2, v4, v5\n" \
"v_mfma_f32_16x16x32_bf16 a[68:71], a[200:203], v[64:67], a[68:71]\n" \
"v_exp_f32 v7, v7\n" \
"v_mfma_f32_16x16x32_bf16 a[68:71], a[204:207], v[96:99], a[68:71]\n" \
"ds_read_b128 a[200:203], v252 offset:4096\n" \
"global_load_lds_dwordx4 v250, s[36:37] offset:-2048 sc1\n" \
"v_add_f32 v240, v240, v7\n" \
"v_mfma_f32_16x16x32_bf16 a[72:75], a[208:211], v[64:67], a[72:75]\n" \
"v_exp_f32 v8, v8\n" \
"v_mfma_f32_16x16x32_bf16 a[72:75], a[212:215], v[96:99], a[72:75]\n" \
"v_add_f32 v241, v241, v8\n" \
"v_cvt_pk_bf16_f32 v3, v6, v7\n" \
"v_mfma_f32_16x16x32_bf16 a[104:107], a[208:211], v[72:75], a[104:107]\n" \
"v_exp_f32 v9, v9\n" \
"v_mfma_f32_16x16x32_bf16 a[104:107], a[212:215], v[104:107], a[104:107]\n" \
"ds_read_b128 a[204:207], v252 offset:6144\n" \
"global_load_lds_dwordx4 v250, s[36:37] offset:-1024 sc1\n" \
"v_add_f32 v241, v241, v9\n" \
"v_mfma_f32_16x16x32_bf16 v[136:139], a[208:211], v[80:83], v[136:139]\n" \
"v_exp_f32 v10, v10\n" \
"v_mfma_f32_16x16x32_bf16 v[136:139], a[212:215], v[112:115], v[136:139]\n" \
"v_add_f32 v241, v241, v10\n" \
"v_cvt_pk_bf16_f32 v8, v8, v9\n" \
"v_mfma_f32_16x16x32_bf16 v[168:171], a[208:211], v[88:91], v[168:171]\n" \
"v_exp_f32 v11, v11\n" \
"v_mfma_f32_16x16x32_bf16 v[168:171], a[212:215], v[120:123], v[168:171]\n" \
"ds_read_b128 a[208:211], v252 offset:256\n" \
"v_add_f32 v241, v241, v11\n" \
"v_mfma_f32_16x16x32_bf16 v[172:175], a[216:219], v[88:91], v[172:175]\n" \
"v_exp_f32 v12, v12\n" \
"v_mfma_f32_16x16x32_bf16 v[172:175], a[220:223], v[120:123], v[172:175]\n" \
"v_add_f32 v241, v241, v12\n" \
"v_cvt_pk_bf16_f32 v9, v10, v11\n" \
"v_mfma_f32_16x16x32_bf16 v[140:143], a[216:219], v[80:83], v[140:143]\n" \
"v_exp_f32 v13, v13\n" \
"v_mfma_f32_16x16x32_bf16 v[140:143], a[220:223], v[112:115], v[140:143]\n" \
"ds_read_b128 a[212:215], v252 offset:2304\n" \
"global_load_lds_dwordx4 v250, s[36:37] offset:0 sc1\n" \
"v_add_f32 v241, v241, v13\n" \
"v_mfma_f32_16x16x32_bf16 a[108:111], a[216:219], v[72:75], a[108:111]\n" \
"v_exp_f32 v14, v14\n" \
"v_mfma_f32_16x16x32_bf16 a[108:111], a[220:223], v[104:107], a[108:111]\n" \
"v_add_f32 v241, v241, v14\n" \
"v_cvt_pk_bf16_f32 v10, v12, v13\n" \
"v_mfma_f32_16x16x32_bf16 a[76:79], a[216:219], v[64:67], a[76:79]\n" \
"s_waitcnt lgkmcnt(6)\n" \
"v_exp_f32 v15, v15\n" \
"v_mfma_f32_16x16x32_bf16 a[76:79], a[220:223], v[96:99], a[76:79]\n" \
"ds_read_b128 a[216:219], v252 offset:4352\n" \
"global_load_lds_dwordx4 v250, s[36:37] offset:1024 sc1\n" \
"v_add_f32 v241, v241, v15\n" \
"v_mfma_f32_16x16x32_bf16 a[80:83], a[224:227], v[64:67], a[80:83]\n" \
"v_exp_f32 v16, v16\n" \
"v_mfma_f32_16x16x32_bf16 a[80:83], a[228:231], v[96:99], a[80:83]\n" \
"v_add_f32 v244, v244, v16\n" \
"v_cvt_pk_bf16_f32 v11, v14, v15\n" \
"v_mfma_f32_16x16x32_bf16 a[112:115], a[224:227], v[72:75], a[112:115]\n" \
"v_exp_f32 v17, v17\n" \
"v_mfma_f32_16x16x32_bf16 a[112:115], a[228:231], v[104:107], a[112:115]\n" \
"ds_read_b128 a[220:223], v252 offset:6400\n" \
"v_add_f32 v244, v244, v17\n" \
"v_mfma_f32_16x16x32_bf16 v[144:147], a[224:227], v[80:83], v[144:147]\n" \
"v_exp_f32 v18, v18\n" \
"v_mfma_f32_16x16x32_bf16 v[144:147], a[228:231], v[112:115], v[144:147]\n" \
"v_add_f32 v244, v244, v18\n" \
"v_cvt_pk_bf16_f32 v16, v16, v17\n" \
"v_mfma_f32_16x16x32_bf16 v[176:179], a[224:227], v[88:91], v[176:179]\n" \
"v_exp_f32 v19, v19\n" \
"v_mfma_f32_16x16x32_bf16 v[176:179], a[228:231], v[120:123], v[176:179]\n" \
"ds_read_b128 a[224:227], v252 offset:8192\n" \
"global_load_lds_dwordx4 v250, s[36:37] offset:2048 sc1\n" \
"v_add_f32 v244, v244, v19\n" \
"v_mfma_f32_16x16x32_bf16 v[180:183], a[232:235], v[88:91], v[180:183]\n" \
"v_exp_f32 v20, v20\n" \
"v_mfma_f32_16x16x32_bf16 v[180:183], a[236:239], v[120:123], v[180:183]\n" \
"v_add_f32 v244, v244, v20\n" \
"v_cvt_pk_bf16_f32 v17, v18, v19\n" \
"v_mfma_f32_16x16x32_bf16 v[148:151], a[232:235], v[80:83], v[148:151]\n" \
"v_exp_f32 v21, v21\n" \
"v_mfma_f32_16x16x32_bf16 v[148:151], a[236:239], v[112:115], v[148:151]\n" \
"ds_read_b128 a[228:231], v252 offset:10240\n" \
"global_load_lds_dwordx4 v250, s[36:37] offset:3072 sc1\n" \
"v_add_f32 v244, v244, v21\n" \
"v_mfma_f32_16x16x32_bf16 a[116:119], a[232:235], v[72:75], a[116:119]\n" \
"s_add_u32 s36, s36, 34816\n" \
"v_exp_f32 v22, v22\n" \
"v_mfma_f32_16x16x32_bf16 a[116:119], a[236:239], v[104:107], a[116:119]\n" \
"v_add_f32 v244, v244, v22\n" \
"v_cvt_pk_bf16_f32 v18, v20, v21\n" \
"v_mfma_f32_16x16x32_bf16 a[84:87], a[232:235], v[64:67], a[84:87]\n" \
"v_exp_f32 v23, v23\n" \
"v_mfma_f32_16x16x32_bf16 a[84:87], a[236:239], v[96:99], a[84:87]\n" \
"ds_read_b128 a[232:235], v252 offset:12288\n" \
"v_add_f32 v244, v244, v23\n" \
"v_mfma_f32_16x16x32_bf16 a[88:91], a[240:243], v[64:67], a[88:91]\n" \
"v_exp_f32 v24, v24\n" \
"v_mfma_f32_16x16x32_bf16 a[88:91], a[244:247], v[96:99], a[88:91]\n" \
"v_add_f32 v245, v245, v24\n" \
"v_cvt_pk_bf16_f32 v19, v22, v23\n" \
"v_mfma_f32_16x16x32_bf16 a[120:123], a[240:243], v[72:75], a[120:123]\n" \
"v_exp_f32 v25, v25\n" \
"v_mfma_f32_16x16x32_bf16 a[120:123], a[244:247], v[104:107], a[120:123]\n" \
"ds_read_b128 a[236:239], v252 offset:14336\n" \
"v_add_f32 v245, v245, v25\n" \
"v_mfma_f32_16x16x32_bf16 v[152:155], a[240:243], v[80:83], v[152:155]\n" \
"v_exp_f32 v26, v26\n" \
"v_mfma_f32_16x16x32_bf16 v[152:155], a[244:247], v[112:115], v[152:155]\n" \
"v_add_f32 v245, v245, v26\n" \
"v_cvt_pk_bf16_f32 v24, v24, v25\n" \
"v_mfma_f32_16x16x32_bf16 v[184:187], a[240:243], v[88:91], v[184:187]\n" \
"v_exp_f32 v27, v27\n" \
"v_mfma_f32_16x16x32_bf16 v[184:187], a[244:247], v[120:123], v[184:187]\n" \
"ds_read_b128 a[240:243], v252 offset:8448\n" \
"v_add_f32 v245, v245, v27\n" \
"v_mfma_f32_16x16x32_bf16 v[188:191], a[248:251], v[88:91], v[188:191]\n" \
"v_exp_f32 v28, v28\n" \
"v_mfma_f32_16x16x32_bf16 v[188:191], a[252:255], v[120:123], v[188:191]\n" \
"v_add_f32 v245, v245, v28\n" \
"v_cvt_pk_bf16_f32 v25, v26, v27\n" \
"v_mfma_f32_16x16x32_bf16 v[156:159], a[248:251], v[80:83], v[156:159]\n" \
"v_exp_f32 v29, v29\n" \
"v_mfma_f32_16x16x32_bf16 v[156:159], a[252:255], v[112:115], v[156:159]\n" \
"ds_read_b128 a[244:247], v252 offset:10496\n" \
"v_add_f32 v245, v245, v29\n" \
"v_mfma_f32_16x16x32_bf16 a[124:127], a[248:251], v[72:75], a[124:127]\n" \
"v_exp_f32 v30, v30\n" \
"v_mfma_f32_16x16x32_bf16 a[124:127], a[252:255], v[104:107], a[124:127]\n" \
"v_add_f32 v245, v245, v30\n" \
"v_cvt_pk_bf16_f32 v26, v28, v29\n" \
"v_mfma_f32_16x16x32_bf16 a[92:95], a[248:251], v[64:67], a[92:95]\n" \
"s_waitcnt vmcnt(8) lgkmcnt(6)\n" \
"v_exp_f32 v31, v31\n" \
"v_mfma_f32_16x16x32_bf16 a[92:95], a[252:255], v[96:99], a[92:95]\n" \
"s_barrier\n" \
"ds_read_b128 a[248:251], v252 offset:12544\n" \
"v_add_f32 v245, v245, v31\n" \
"LA1g_%=:\n" \
"Lpg7555_%=:\n" \
"s_cbranch_execz Lcb0_%=\n" \
"v_mfma_f32_16x16x32_bf16 v[64:67], a[192:195], v[192:195], v[224:227]\n" \
"v_exp_f32 v32, v32\n" \
"v_mfma_f32_16x16x32_bf16 v[64:67], a[196:199], v[196:199], v[64:67]\n" \
"v_cvt_pk_bf16_f32 v27, v30, v31\n" \
"v_add_f32 v240, v240, v32\n" \
"v_mfma_f32_16x16x32_bf16 v[64:67], a[200:203], v[200:203], v[64:67]\n" \
"s_add_u32 m0, s74, 0x8000\n" \
"v_exp_f32 v33, v33\n" \
"v_mfma_f32_16x16x32_bf16 v[64:67], a[204:207], v[204:207], v[64:67]\n" \
"ds_read_b128 a[252:255], v252 offset:14592\n" \
"v_add_f32 v240, v240, v33\n" \
"v_mfma_f32_16x16x32_bf16 v[72:75], a[200:203], v[216:219], v[228:231]\n" \
"global_load_lds_dwordx4 v250, s[38:39] offset:-4096 sc1\n" \
"global_load_lds_dwordx4 v250, s[38:39] offset:-3072 sc1\n" \
"v_mfma_f32_16x16x32_bf16 v[72:75], a[204:207], v[220:223], v[72:75]\n" \
"v_exp_f32 v34, v34\n" \
"v_mfma_f32_16x16x32_bf16 v[72:75], a[192:195], v[208:211], v[72:75]\n" \
"v_add_f32 v240, v240, v34\n" \
"v_cvt_pk_bf16_f32 v32, v32, v33\n" \
"v_mfma_f32_16x16x32_bf16 v[72:75], a[196:199], v[212:215], v[72:75]\n" \
"v_exp_f32 v35, v35\n" \
"v_mfma_f32_16x16x32_bf16 v[80:83], a[192:195], a[160:163], v[232:235]\n" \
"v_add_f32 v240, v240, v35\n" \
"v_exp_f32 v36, v36\n" \
"v_mfma_f32_16x16x32_bf16 v[80:83], a[196:199], a[164:167], v[80:83]\n" \
"v_add_f32 v240, v240, v36\n" \
"v_cvt_pk_bf16_f32 v33, v34, v35\n" \
"v_mfma_f32_16x16x32_bf16 v[80:83], a[200:203], a[168:171], v[80:83]\n" \
"v_exp_f32 v37, v37\n" \
"v_mfma_f32_16x16x32_bf16 v[80:83], a[204:207], a[172:175], v[80:83]\n" \
"v_add_f32 v240, v240, v37\n" \
"v_exp_f32 v38, v38\n" \
"v_mfma_f32_16x16x32_bf16 v[88:91], a[200:203], a[184:187], v[236:239]\n" \
"v_add_f32 v240, v240, v38\n" \
"v_cvt_pk_bf16_f32 v34, v36, v37\n" \
"v_mfma_f32_16x16x32_bf16 v[88:91], a[204:207], a[188:191], v[88:91]\n" \
"v_exp_f32 v39, v39\n" \
"v_mfma_f32_16x16x32_bf16 v[88:91], a[192:195], a[176:179], v[88:91]\n" \
"v_add_f32 v240, v240, v39\n" \
"v_mfma_f32_16x16x32_bf16 v[88:91], a[196:199], a[180:183], v[88:91]\n" \
"ds_read_b128 a[192:195], v247 offset:0\n" \
"global_load_lds_dwordx4 v250, s[38:39] offset:-2048 sc1\n" \
"v_mfma_f32_16x16x32_bf16 v[92:95], a[208:211], a[176:179], v[236:239]\n" \
"v_exp_f32 v40, v40\n" \
"v_mfma_f32_16x16x32_bf16 v[92:95], a[212:215], a[180:183], v[92:95]\n" \
"v_add_f32 v241, v241, v40\n" \
"v_cvt_pk_bf16_f32 v35, v38, v39\n" \
"v_mfma_f32_16x16x32_bf16 v[92:95], a[216:219], a[184:187], v[92:95]\n" \
"v_exp_f32 v41, v41\n" \
"v_mfma_f32_16x16x32_bf16 v[92:95], a[220:223], a[188:191], v[92:95]\n" \
"ds_read_b128 a[196:199], v247 offset:8192\n" \
"global_load_lds_dwordx4 v250, s[38:39] offset:-1024 sc1\n" \
"v_add_f32 v241, v241, v41\n" \
"v_mfma_f32_16x16x32_bf16 v[84:87], a[216:219], a[168:171], v[232:235]\n" \
"v_exp_f32 v42, v42\n" \
"v_mfma_f32_16x16x32_bf16 v[84:87], a[220:223], a[172:175], v[84:87]\n" \
"v_add_f32 v241, v241, v42\n" \
"v_cvt_pk_bf16_f32 v40, v40, v41\n" \
"v_mfma_f32_16x16x32_bf16 v[84:87], a[208:211], a[160:163], v[84:87]\n" \
"v_exp_f32 v43, v43\n" \
"v_mfma_f32_16x16x32_bf16 v[84:87], a[212:215], a[164:167], v[84:87]\n" \
"ds_read_b128 a[200:203], v247 offset:1024\n" \
"v_add_f32 v241, v241, v43\n" \
"v_mfma_f32_16x16x32_bf16 v[76:79], a[208:211], v[208:211], v[228:231]\n" \
"v_exp_f32 v44, v44\n" \
"v_mfma_f32_16x16x32_bf16 v[76:79], a[212:215], v[212:215], v[76:79]\n" \
"v_add_f32 v241, v241, v44\n" \
"v_cvt_pk_bf16_f32 v41, v42, v43\n" \
"v_mfma_f32_16x16x32_bf16 v[76:79], a[216:219], v[216:219], v[76:79]\n" \
"v_exp_f32 v45, v45\n" \
"v_mfma_f32_16x16x32_bf16 v[76:79], a[220:223], v[220:223], v[76:79]\n" \
"ds_read_b128 a[204:207], v247 offset:9216\n" \
"global_load_lds_dwordx4 v250, s[38:39] offset:0 sc1\n" \
"v_add_f32 v241, v241, v45\n" \
"v_mfma_f32_16x16x32_bf16 v[68:71], a[216:219], v[200:203], v[224:227]\n" \
"v_exp_f32 v46, v46\n" \
"v_mfma_f32_16x16x32_bf16 v[68:71], a[220:223], v[204:207], v[68:71]\n" \
"v_add_f32 v241, v241, v46\n" \
"v_cvt_pk_bf16_f32 v42, v44, v45\n" \
"v_mfma_f32_16x16x32_bf16 v[68:71], a[208:211], v[192:195], v[68:71]\n" \
"s_waitcnt lgkmcnt(4)\n" \
"v_exp_f32 v47, v47\n" \
"v_mfma_f32_16x16x32_bf16 v[68:71], a[212:215], v[196:199], v[68:71]\n" \
"ds_read_b128 a[208:211], v247 offset:2048\n" \
"global_load_lds_dwordx4 v250, s[38:39] offset:1024 sc1\n" \
"v_add_f32 v241, v241, v47\n" \
"v_mfma_f32_16x16x32_bf16 v[96:99], a[224:227], v[192:195], v[224:227]\n" \
"v_exp_f32 v48, v48\n" \
"v_mfma_f32_16x16x32_bf16 v[96:99], a[228:231], v[196:199], v[96:99]\n" \
"v_add_f32 v244, v244, v48\n" \
"v_cvt_pk_bf16_f32 v43, v46, v47\n" \
"v_mfma_f32_16x16x32_bf16 v[96:99], a[232:235], v[200:203], v[96:99]\n" \
"v_exp_f32 v49, v49\n" \
"v_mfma_f32_16x16x32_bf16 v[96:99], a[236:239], v[204:207], v[96:99]\n" \
"ds_read_b128 a[212:215], v247 offset:10240\n" \
"v_add_f32 v244, v244, v49\n" \
"v_mfma_f32_16x16x32_bf16 v[104:107], a[232:235], v[216:219], v[228:231]\n" \
"v_exp_f32 v50, v50\n" \
"v_mfma_f32_16x16x32_bf16 v[104:107], a[236:239], v[220:223], v[104:107]\n" \
"v_add_f32 v244, v244, v50\n" \
"v_cvt_pk_bf16_f32 v48, v48, v49\n" \
"v_mfma_f32_16x16x32_bf16 v[104:107], a[224:227], v[208:211], v[104:107]\n" \
"v_exp_f32 v51, v51\n" \
"v_mfma_f32_16x16x32_bf16 v[104:107], a[228:231], v[212:215], v[104:107]\n" \
"ds_read_b128 a[216:219], v247 offset:3072\n" \
"global_load_lds_dwordx4 v250, s[38:39] offset:2048 sc1\n" \
"v_add_f32 v244, v244, v51\n" \
"v_mfma_f32_16x16x32_bf16 v[112:115], a[224:227], a[160:163], v[232:235]\n" \
"v_exp_f32 v52, v52\n" \
"v_mfma_f32_16x16x32_bf16 v[112:115], a[228:231], a[164:167], v[112:115]\n" \
"v_add_f32 v244, v244, v52\n" \
"v_cvt_pk_bf16_f32 v49, v50, v51\n" \
"v_mfma_f32_16x16x32_bf16 v[112:115], a[232:235], a[168:171], v[112:115]\n" \
"v_exp_f32 v53, v53\n" \
"v_mfma_f32_16x16x32_bf16 v[112:115], a[236:239], a[172:175], v[112:115]\n" \
"ds_read_b128 a[220:223], v247 offset:11264\n" \
"global_load_lds_dwordx4 v250, s[38:39] offset:3072 sc1\n" \
"v_add_f32 v244, v244, v53\n" \
"v_mfma_f32_16x16x32_bf16 v[120:123], a[232:235], a[184:187], v[236:239]\n" \
"s_add_u32 s38, s38, 34816\n" \
"v_exp_f32 v54, v54\n" \
"v_mfma_f32_16x16x32_bf16 v[120:123], a[236:239], a[188:191], v[120:123]\n" \
"s_sub_u32 s76, s76, 1\n" \
"v_add_f32 v244, v244, v54\n" \
"v_cvt_pk_bf16_f32 v50, v52, v53\n" \
"v_mfma_f32_16x16x32_bf16 v[120:123], a[224:227], a[176:179], v[120:123]\n" \
"s_cmp_lg_u32 s76, 0\n" \
"v_exp_f32 v55, v55\n" \
"v_mfma_f32_16x16x32_bf16 v[120:123], a[228:231], a[180:183], v[120:123]\n" \
"ds_read_b128 a[224:227], v247 offset:4096\n" \
"v_add_f32 v244, v244, v55\n" \
"v_mfma_f32_16x16x32_bf16 v[124:127], a[240:243], a[176:179], v[236:239]\n" \
"v_exp_f32 v56, v56\n" \
"v_mfma_f32_16x16x32_bf16 v[124:127], a[244:247], a[180:183], v[124:127]\n" \
"v_add_f32 v245, v245, v56\n" \
"v_cvt_pk_bf16_f32 v51, v54, v55\n" \
"v_mfma_f32_16x16x32_bf16 v[124:127], a[248:251], a[184:187], v[124:127]\n" \
"v_exp_f32 v57, v57\n" \
"v_mfma_f32_16x16x32_bf16 v[124:127], a[252:255], a[188:191], v[124:127]\n" \
"ds_read_b128 a[228:231], v247 offset:12288\n" \
"v_add_f32 v245, v245, v57\n" \
"v_mfma_f32_16x16x32_bf16 v[116:119], a[248:251], a[168:171], v[232:235]\n" \
"v_exp_f32 v58, v58\n" \
"v_mfma_f32_16x16x32_bf16 v[116:119], a[252:255], a[172:175], v[116:119]\n" \
"v_add_f32 v245, v245, v58\n" \
"v_cvt_pk_bf16_f32 v56, v56, v57\n" \
"v_mfma_f32_16x16x32_bf16 v[116:119], a[240:243], a[160:163], v[116:119]\n" \
"v_exp_f32 v59, v59\n" \
"v_mfma_f32_16x16x32_bf16 v[116:119], a[244:247], a[164:167], v[116:119]\n" \
"ds_read_b128 a[232:235], v247 offset:5120\n" \
"v_add_f32 v245, v245, v59\n" \
"v_mfma_f32_16x16x32_bf16 v[108:111], a[240:243], v[208:211], v[228:231]\n" \
"v_exp_f32 v60, v60\n" \
"v_mfma_f32_16x16x32_bf16 v[108:111], a[244:247], v[212:215], v[108:111]\n" \
"v_add_f32 v245, v245, v60\n" \
"v_cvt_pk_bf16_f32 v57, v58, v59\n" \
"v_mfma_f32_16x16x32_bf16 v[108:111], a[248:251], v[216:219], v[108:111]\n" \
"v_exp_f32 v61, v61\n" \
"v_mfma_f32_16x16x32_bf16 v[108:111], a[252:255], v[220:223], v[108:111]\n" \
"ds_read_b128 a[236:239], v247 offset:13312\n" \
"v_add_f32 v245, v245, v61\n" \
"v_mfma_f32_16x16x32_bf16 v[100:103], a[248:251], v[200:203], v[224:227]\n" \
"v_exp_f32 v62, v62\n" \
"v_mfma_f32_16x16x32_bf16 v[100:103], a[252:255], v[204:207], v[100:103]\n" \
"v_add_f32 v245, v245, v62\n" \
"v_cvt_pk_bf16_f32 v58, v60, v61\n" \
"v_mfma_f32_16x16x32_bf16 v[100:103], a[240:243], v[192:195], v[100:103]\n" \
"s_waitcnt vmcnt(8) lgkmcnt(4)\n" \
"v_exp_f32 v63, v63\n" \
"v_mfma_f32_16x16x32_bf16 v[100:103], a[244:247], v[196:199], v[100:103]\n" \
"s_barrier\n" \
"ds_read_b128 a[240:243], v247 offset:6144\n" \
"v_add_f32 v245, v245, v63\n" \
"s_cbranch_scc1 Lloop2_%=\n" \
"v_mfma_f32_16x16x32_bf16 a[64:67], a[192:195], v[0:3], a[64:67]\n" \
"s_bfe_u32 s88, %1, 0x20001\n" \
"v_subrev_u32 v246, s82, v246\n" \
"s_lshl_b32 s88, s88, 2\n" \
"v_mfma_f32_16x16x32_bf16 a[64:67], a[196:199], v[32:35], a[64:67]\n" \
"s_memrealtime s[52:53]\n" \
"v_cvt_pk_bf16_f32 v59, v62, v63\n" \
"s_lshr_b32 s70, %1, 6\n" \
"v_mfma_f32_16x16x32_bf16 a[96:99], a[192:195], v[8:11], a[96:99]\n" \
"s_add_u32 s88, s88, s70\n" \
"ds_read_b128 a[244:247], v247 offset:14336\n" \
"v_mov_b32 v216, v243\n" \
"v_mfma_f32_16x16x32_bf16 a[96:99], a[196:199], v[40:43], a[96:99]\n" \
"s_lshr_b32 s71, s22, 7\n" \
"ds_read_b128 a[248:251], v247 offset:7168\n" \
"v_exp_f32 v64, v64\n" \
"v_mfma_f32_16x16x32_bf16 v[128:131], a[192:195], v[16:19], v[128:131]\n" \
"s_sub_u32 s71, s63, s71\n" \
"ds_read_b128 a[252:255], v247 offset:15360\n" \
"v_mov_b32 v214, v216\n" \
"v_mfma_f32_16x16x32_bf16 v[128:131], a[196:199], v[48:51], v[128:131]\n" \
"s_add_u32 s71, s71, 6\n" \
"v_exp_f32 v65, v65\n" \
"v_mfma_f32_16x16x32_bf16 v[160:163], a[192:195], v[24:27], v[160:163]\n" \
"s_and_b32 s71, s71, 7\n" \
"v_exp_f32 v66, v66\n" \
"v_mfma_f32_16x16x32_bf16 v[160:163], a[196:199], v[56:59], v[160:163]\n" \
"s_and_b32 s70, %1, 1\n" \
"ds_read_b128 a[192:195], v246 offset:0\n" \
"v_cvt_pk_bf16_f32 v64, v64, v65\n" \
"v_permlane16_swap_b32 v214, v216\n" \
"v_add_f32 v216, v216, v214\n" \
"v_mov_b32 v214, v216\n" \
"v_mfma_f32_16x16x32_bf16 v[164:167], a[200:203], v[24:27], v[164:167]\n" \
"s_lshl_b32 s70, s70, 3\n" \
"v_exp_f32 v67, v67\n" \
"v_mfma_f32_16x16x32_bf16 v[164:167], a[204:207], v[56:59], v[164:167]\n" \
"s_waitcnt lgkmcnt(0)\n" \
"v_exp_f32 v68, v68\n" \
"v_cvt_pk_bf16_f32 v65, v66, v67\n" \
"v_permlane32_swap_b32 v214, v216\n" \
"v_add_f32 v216, v216, v214\n" \
"v_rcp_f32 v214, v216\n" \
"v_mul_f32 v216, 0x4d800000, v216\n" \
"v_add_u32 v215, 0x1c90080, v249\n" \
"v_cndmask_b32 v215, v249, v215, s[16:17]\n" \
"s_add_u32 s77, s77, 532480\n" \
"v_accvgpr_read_b32 v192, a32\n" \
"v_mfma_f32_16x16x32_bf16 v[132:135], a[200:203], v[16:19], v[132:135]\n" \
"s_barrier\n" \
"v_mfma_f32_16x16x32_bf16 v[132:135], a[204:207], v[48:51], v[132:135]\n" \
"s_and_b32 s54, %1, 7\n" \
"ds_read_b128 a[196:199], v246 offset:2048\n" \
"s_mov_b32 m0, s74\n" \
"v_mfma_f32_16x16x32_bf16 a[100:103], a[200:203], v[8:11], a[100:103]\n" \
"s_lshl_b32 s54, s54, 2\n" \
"v_exp_f32 v69, v69\n" \
"v_mfma_f32_16x16x32_bf16 a[100:103], a[204:207], v[40:43], a[100:103]\n" \
"s_lshr_b32 s55, %1, 6\n" \
"v_exp_f32 v70, v70\n" \
"v_mfma_f32_16x16x32_bf16 a[68:71], a[200:203], v[0:3], a[68:71]\n" \
"s_add_u32 s54, s54, s55\n" \
"v_cvt_pk_bf16_f32 v66, v68, v69\n" \
"v_accvgpr_read_b32 v193, a33\n" \
"v_mfma_f32_16x16x32_bf16 a[68:71], a[204:207], v[32:35], a[68:71]\n" \
"s_lshl_b32 s54, s54, 3\n" \
"ds_read_b128 a[200:203], v246 offset:4096\n" \
"global_load_lds_dwordx4 v250, s[38:39] offset:-4096 sc1\n" \
"global_load_lds_dwordx4 v250, s[38:39] offset:-3072 sc1\n" \
"v_mfma_f32_16x16x32_bf16 a[72:75], a[208:211], v[0:3], a[72:75]\n" \
"s_add_u32 s54, s54, 65593856\n" \
"v_exp_f32 v71, v71\n" \
"v_mfma_f32_16x16x32_bf16 a[72:75], a[212:215], v[32:35], a[72:75]\n" \
"s_sub_u32 s53, s52, s12\n" \
"v_exp_f32 v72, v72\n" \
"v_mfma_f32_16x16x32_bf16 a[104:107], a[208:211], v[8:11], a[104:107]\n" \
"s_mul_i32 s53, s53, 3\n" \
"v_cvt_pk_bf16_f32 v67, v70, v71\n" \
"v_accvgpr_read_b32 v194, a34\n" \
"v_mfma_f32_16x16x32_bf16 a[104:107], a[212:215], v[40:43], a[104:107]\n" \
"s_lshl_b32 s52, s52, 1\n" \
"ds_read_b128 a[204:207], v246 offset:6144\n" \
"global_load_lds_dwordx4 v250, s[38:39] offset:-2048 sc1\n" \
"v_mfma_f32_16x16x32_bf16 v[136:139], a[208:211], v[16:19], v[136:139]\n" \
"s_add_u32 s52, s52, s53\n" \
"v_mov_b32 v211, s52\n" \
"v_xor_b32 v210, s60, v211\n" \
"v_mfma_f32_16x16x32_bf16 v[136:139], a[212:215], v[48:51], v[136:139]\n" \
"s_add_u32 s70, s70, s71\n" \
"v_exp_f32 v73, v73\n" \
"v_mfma_f32_16x16x32_bf16 v[168:171], a[208:211], v[24:27], v[168:171]\n" \
"s_lshl_b32 s87, s70, 12\n" \
"v_exp_f32 v74, v74\n" \
"v_mfma_f32_16x16x32_bf16 v[168:171], a[212:215], v[56:59], v[168:171]\n" \
"s_lshl_b32 s70, s70, 7\n" \
"ds_read_b128 a[208:211], v246 offset:256\n" \
"v_lshlrev_b32 v212, 28, s61\n" \
"v_mfma_f32_16x16x32_bf16 v[172:175], a[216:219], v[24:27], v[172:175]\n" \
"s_cmp_eq_u32 s63, 3\n" \
"s_cselect_b32 s54, s54, 0x20000000\n" \
"v_cvt_pk_bf16_f32 v72, v72, v73\n" \
"v_mfma_f32_16x16x32_bf16 v[172:175], a[220:223], v[56:59], v[172:175]\n" \
"s_mul_i32 s84, s61, 6144\n" \
"v_exp_f32 v75, v75\n" \
"v_mfma_f32_16x16x32_bf16 v[140:143], a[216:219], v[16:19], v[140:143]\n" \
"s_lshl_b32 s71, s84, 4\n" \
"v_exp_f32 v76, v76\n" \
"v_mfma_f32_16x16x32_bf16 v[140:143], a[220:223], v[48:51], v[140:143]\n" \
"s_add_u32 s70, s70, s71\n" \
"ds_read_b128 a[212:215], v246 offset:2304\n" \
"global_load_lds_dwordx4 v250, s[38:39] offset:-1024 sc1\n" \
"v_cvt_pk_bf16_f32 v73, v74, v75\n" \
"v_mfma_f32_16x16x32_bf16 a[108:111], a[216:219], v[8:11], a[108:111]\n" \
"s_add_u32 s70, s70, 4096\n" \
"v_exp_f32 v77, v77\n" \
"v_mfma_f32_16x16x32_bf16 a[108:111], a[220:223], v[40:43], a[108:111]\n" \
"s_add_u32 s48, s22, s70\n" \
"s_addc_u32 s49, s23, 0\n" \
"v_accvgpr_read_b32 v195, a35\n" \
"v_mfma_f32_16x16x32_bf16 a[76:79], a[216:219], v[0:3], a[76:79]\n" \
"s_waitcnt lgkmcnt(6)\n" \
"global_load_lds_dwordx4 v250, s[38:39] offset:0 sc1\n" \
"global_load_lds_dwordx4 v250, s[38:39] offset:1024 sc1\n" \
"v_mfma_f32_16x16x32_bf16 a[76:79], a[220:223], v[32:35], a[76:79]\n" \
"s_add_u32 s50, s48, 23552\n" \
"s_addc_u32 s51, s49, 0\n" \
"ds_read_b128 a[216:219], v246 offset:4352\n" \
"v_mfma_f32_16x16x32_bf16 a[80:83], a[224:227], v[0:3], a[80:83]\n" \
"s_lshr_b32 s89, s22, 7\n" \
"v_exp_f32 v78, v78\n" \
"v_mfma_f32_16x16x32_bf16 a[80:83], a[228:231], v[32:35], a[80:83]\n" \
"s_sub_u32 s89, 3, s89\n" \
"v_cvt_pk_bf16_f32 v74, v76, v77\n" \
"v_accvgpr_read_b32 v196, a36\n" \
"v_accvgpr_read_b32 v197, a37\n" \
"v_accvgpr_read_b32 v198, a38\n" \
"v_accvgpr_read_b32 v199, a39\n" \
"v_pk_mul_f32 v[192:193], v[192:193], v[214:215] op_sel_hi:[1,0]\n" \
"v_pk_mul_f32 v[194:195], v[194:195], v[214:215] op_sel_hi:[1,0]\n" \
"v_pk_mul_f32 v[196:197], v[196:197], v[214:215] op_sel_hi:[1,0]\n" \
"v_pk_mul_f32 v[198:199], v[198:199], v[214:215] op_sel_hi:[1,0]\n" \
"v_mfma_f32_16x16x32_bf16 a[112:115], a[224:227], v[8:11], a[112:115]\n" \
"s_and_b32 s89, s89, 7\n" \
"v_exp_f32 v79, v79\n" \
"v_mfma_f32_16x16x32_bf16 a[112:115], a[228:231], v[40:43], a[112:115]\n" \
"s_and_b32 s90, %1, 1\n" \
"ds_read_b128 a[220:223], v246 offset:6400\n" \
"global_load_lds_dwordx4 v250, s[38:39] offset:2048 sc1\n" \
"v_mfma_f32_16x16x32_bf16 v[144:147], a[224:227], v[16:19], v[144:147]\n" \
"s_lshl_b32 s90, s90, 3\n" \
"v_exp_f32 v80, v80\n" \
"v_mfma_f32_16x16x32_bf16 v[144:147], a[228:231], v[48:51], v[144:147]\n" \
"s_add_u32 s89, s89, s90\n" \
"v_cvt_pk_bf16_f32 v75, v78, v79\n" \
"v_cvt_pk_bf16_f32 v200, v192, v193\n" \
"v_cvt_pk_bf16_f32 v201, v194, v195\n" \
"v_cvt_pk_bf16_f32 v202, v196, v197\n" \
"v_cvt_pk_bf16_f32 v203, v198, v199\n" \
"v_pk_mul_f32 v[192:193], v[192:193], v[194:195]\n" \
"v_pk_fma_f32 v[192:193], v[196:197], v[198:199], v[192:193]\n" \
"v_mfma_f32_16x16x32_bf16 v[176:179], a[224:227], v[24:27], v[176:179]\n" \
"s_lshl_b32 s89, s89, 7\n" \
"v_exp_f32 v81, v81\n" \
"v_mfma_f32_16x16x32_bf16 v[176:179], a[228:231], v[56:59], v[176:179]\n" \
"s_lshr_b32 s90, %1, 3\n" \
"ds_read_b128 a[224:227], v246 offset:8192\n" \
"global_load_lds_dwordx4 v250, s[38:39] offset:3072 sc1\n" \
"v_mfma_f32_16x16x32_bf16 v[180:183], a[232:235], v[24:27], v[180:183]\n" \
"s_lshl_b32 s90, s90, 2\n" \
"v_exp_f32 v82, v82\n" \
"v_mfma_f32_16x16x32_bf16 v[180:183], a[236:239], v[56:59], v[180:183]\n" \
"v_fma_f32 v216, v192, v193, v216\n" \
"v_cvt_pk_bf16_f32 v80, v80, v81\n" \
"v_mfma_f32_16x16x32_bf16 v[238:241], a[0:3], v[64:67], v[238:241]\n" \
"s_add_u32 s90, s90, s61\n" \
"v_exp_f32 v83, v83\n" \
"v_mfma_f32_16x16x32_bf16 v[238:241], a[4:7], v[72:75], v[238:241]\n" \
"buffer_store_dwordx2 v[210:211], v212, s[56:59], s54 offen sc1\n" \
"v_mfma_f32_16x16x32_bf16 v[148:151], a[232:235], v[16:19], v[148:151]\n" \
"s_mul_i32 s90, s90, 24576\n" \
"v_exp_f32 v84, v84\n" \
"v_mfma_f32_16x16x32_bf16 v[148:151], a[236:239], v[48:51], v[148:151]\n" \
"s_add_u32 s89, s89, s90\n" \
"ds_read_b128 a[228:231], v246 offset:10240\n" \
"buffer_store_dwordx4 v[200:203], v215, s[56:59], s77 offen offset:0 sc1\n" \
"v_cvt_pk_bf16_f32 v81, v82, v83\n" \
"v_accvgpr_read_b32 v192, a40\n" \
"v_accvgpr_read_b32 v193, a41\n" \
"v_accvgpr_read_b32 v194, a42\n" \
"v_mfma_f32_16x16x32_bf16 a[116:119], a[232:235], v[8:11], a[116:119]\n" \
"s_lshl_b32 s70, s88, 18\n" \
"v_exp_f32 v85, v85\n" \
"v_mfma_f32_16x16x32_bf16 a[116:119], a[236:239], v[40:43], a[116:119]\n" \
"s_lshl_b32 s71, s61, 16\n" \
"v_accvgpr_read_b32 v195, a43\n" \
"v_accvgpr_read_b32 v196, a44\n" \
"v_accvgpr_read_b32 v197, a45\n" \
"v_accvgpr_read_b32 v198, a46\n" \
"v_accvgpr_read_b32 v199, a47\n" \
"v_exp_f32 v86, v86\n" \
"v_pk_mul_f32 v[192:193], v[192:193], v[214:215] op_sel_hi:[1,0]\n" \
"v_pk_mul_f32 v[194:195], v[194:195], v[214:215] op_sel_hi:[1,0]\n" \
"v_pk_mul_f32 v[196:197], v[196:197], v[214:215] op_sel_hi:[1,0]\n" \
"v_pk_mul_f32 v[198:199], v[198:199], v[214:215] op_sel_hi:[1,0]\n" \
"v_cvt_pk_bf16_f32 v204, v192, v193\n" \
"v_cvt_pk_bf16_f32 v205, v194, v195\n" \
"v_cvt_pk_bf16_f32 v206, v196, v197\n" \
"v_cvt_pk_bf16_f32 v207, v198, v199\n" \
"v_pk_mul_f32 v[192:193], v[192:193], v[194:195]\n" \
"v_pk_fma_f32 v[192:193], v[196:197], v[198:199], v[192:193]\n" \
"v_mfma_f32_16x16x32_bf16 a[84:87], a[232:235], v[0:3], a[84:87]\n" \
"s_add_u32 s71, s71, s70\n" \
"v_cvt_pk_bf16_f32 v82, v84, v85\n" \
"v_fma_f32 v216, v192, v193, v216\n" \
"v_mfma_f32_16x16x32_bf16 a[84:87], a[236:239], v[32:35], a[84:87]\n" \
"s_add_u32 s42, s20, s71\n" \
"s_addc_u32 s43, s21, 0\n" \
"ds_read_b128 a[232:235], v246 offset:12288\n" \
"buffer_store_dwordx4 v[204:207], v215, s[56:59], s77 offen offset:2048 sc1\n" \
"s_add_u32 s77, s77, 0x1000\n" \
"v_accvgpr_read_b32 v192, a48\n" \
"v_accvgpr_read_b32 v193, a49\n" \
"v_accvgpr_read_b32 v194, a50\n" \
"v_mfma_f32_16x16x32_bf16 a[88:91], a[240:243], v[0:3], a[88:91]\n" \
"s_add_u32 s44, s42, 31744\n" \
"s_addc_u32 s45, s43, 0\n" \
"v_accvgpr_read_b32 v195, a51\n" \
"v_mfma_f32_16x16x32_bf16 a[88:91], a[244:247], v[32:35], a[88:91]\n" \
"s_max_u32 s70, s70, 32768\n" \
"v_accvgpr_read_b32 v196, a52\n" \
"v_accvgpr_read_b32 v197, a53\n" \
"v_accvgpr_read_b32 v198, a54\n" \
"v_accvgpr_read_b32 v199, a55\n" \
"v_exp_f32 v87, v87\n" \
"v_pk_mul_f32 v[192:193], v[192:193], v[214:215] op_sel_hi:[1,0]\n" \
"v_pk_mul_f32 v[194:195], v[194:195], v[214:215] op_sel_hi:[1,0]\n" \
"v_pk_mul_f32 v[196:197], v[196:197], v[214:215] op_sel_hi:[1,0]\n" \
"v_pk_mul_f32 v[198:199], v[198:199], v[214:215] op_sel_hi:[1,0]\n" \
"v_cvt_pk_bf16_f32 v200, v192, v193\n" \
"v_cvt_pk_bf16_f32 v201, v194, v195\n" \
"v_cvt_pk_bf16_f32 v202, v196, v197\n" \
"v_cvt_pk_bf16_f32 v203, v198, v199\n" \
"v_pk_mul_f32 v[192:193], v[192:193], v[194:195]\n" \
"v_pk_fma_f32 v[192:193], v[196:197], v[198:199], v[192:193]\n" \
"v_fma_f32 v216, v192, v193, v216\n" \
"v_mfma_f32_16x16x32_bf16 a[120:123], a[240:243], v[8:11], a[120:123]\n" \
"s_sub_u32 s70, s70, 32768\n" \
"v_exp_f32 v88, v88\n" \
"v_mfma_f32_16x16x32_bf16 a[120:123], a[244:247], v[40:43], a[120:123]\n" \
"s_and_b32 s86, s61, 1\n" \
"ds_read_b128 a[236:239], v246 offset:14336\n" \
"buffer_store_dwordx4 v[200:203], v215, s[56:59], s77 offen offset:0 sc1\n" \
"v_cvt_pk_bf16_f32 v83, v86, v87\n" \
"v_accvgpr_read_b32 v192, a56\n" \
"v_accvgpr_read_b32 v193, a57\n" \
"v_accvgpr_read_b32 v194, a58\n" \
"v_mfma_f32_16x16x32_bf16 v[152:155], a[240:243], v[16:19], v[152:155]\n" \
"s_lshl_b32 s86, s86, 2\n" \
"v_exp_f32 v89, v89\n" \
"v_mfma_f32_16x16x32_bf16 v[152:155], a[244:247], v[48:51], v[152:155]\n" \
"v_accvgpr_read_b32 v195, a59\n" \
"v_accvgpr_read_b32 v196, a60\n" \
"v_exp_f32 v90, v90\n" \
"v_cvt_pk_bf16_f32 v88, v88, v89\n" \
"v_mfma_f32_16x16x32_bf16 v[242:245], a[0:3], v[80:83], v[242:245]\n" \
"s_lshr_b32 s71, s61, 1\n" \
"v_exp_f32 v91, v91\n" \
"v_mfma_f32_16x16x32_bf16 v[184:187], a[240:243], v[24:27], v[184:187]\n" \
"s_lshl_b32 s71, s71, 11\n" \
"v_exp_f32 v92, v92\n" \
"v_mfma_f32_16x16x32_bf16 v[184:187], a[244:247], v[56:59], v[184:187]\n" \
"s_add_u32 s70, s70, s71\n" \
"ds_read_b128 a[240:243], v246 offset:8448\n" \
"v_cvt_pk_bf16_f32 v89, v90, v91\n" \
"v_accvgpr_read_b32 v197, a61\n" \
"v_accvgpr_read_b32 v198, a62\n" \
"v_accvgpr_read_b32 v199, a63\n" \
"v_pk_mul_f32 v[192:193], v[192:193], v[214:215] op_sel_hi:[1,0]\n" \
"v_pk_mul_f32 v[194:195], v[194:195], v[214:215] op_sel_hi:[1,0]\n" \
"v_pk_mul_f32 v[196:197], v[196:197], v[214:215] op_sel_hi:[1,0]\n" \
"v_pk_mul_f32 v[198:199], v[198:199], v[214:215] op_sel_hi:[1,0]\n" \
"v_cvt_pk_bf16_f32 v204, v192, v193\n" \
"v_cvt_pk_bf16_f32 v205, v194, v195\n" \
"v_cvt_pk_bf16_f32 v206, v196, v197\n" \
"v_cvt_pk_bf16_f32 v207, v198, v199\n" \
"v_pk_mul_f32 v[192:193], v[192:193], v[194:195]\n" \
"v_pk_fma_f32 v[192:193], v[196:197], v[198:199], v[192:193]\n" \
"v_mfma_f32_16x16x32_bf16 v[188:191], a[248:251], v[24:27], v[188:191]\n" \
"s_mov_b32 s46, s70\n" \
"v_exp_f32 v93, v93\n" \
"v_mfma_f32_16x16x32_bf16 v[188:191], a[252:255], v[56:59], v[188:191]\n" \
"s_lshl_b32 s85, s61, 10\n" \
"v_exp_f32 v94, v94\n" \
"v_mfma_f32_16x16x32_bf16 v[156:159], a[248:251], v[16:19], v[156:159]\n" \
"s_mov_b64 s[12:13], s[20:21]\n" \
"v_cvt_pk_bf16_f32 v90, v92, v93\n" \
"Lpg8026_%=:\n" \
"Lst1x1_%=:\n" \
"s_cbranch_execz Lpg10302_%=\n" \
"v_fma_f32 v216, v192, v193, v216\n" \
"v_mfma_f32_16x16x32_bf16 v[156:159], a[252:255], v[48:51], v[156:159]\n" \
"buffer_store_dwordx4 v[204:207], v215, s[56:59], s77 offen offset:2048 sc1\n" \
"ds_read_b128 a[244:247], v246 offset:10496\n" \
"s_and_b32 s13, s13, 0xffff\n" \
"v_cmp_class_f32 vcc, v216, s94\n" \
"s_sub_u32 s77, s77, 536576\n" \
"v_mfma_f32_16x16x32_bf16 a[124:127], a[248:251], v[8:11], a[124:127]\n" \
"s_mov_b32 s14, 4194304\n" \
"v_exp_f32 v95, v95\n" \
"v_mfma_f32_16x16x32_bf16 a[124:127], a[252:255], v[40:43], a[124:127]\n" \
"s_mov_b32 s15, 0x20000\n" \
"v_cvt_pk_bf16_f32 v91, v94, v95\n" \
"s_or_b64 s[92:93], s[92:93], vcc\n" \
"v_mfma_f32_16x16x32_bf16 a[92:95], a[248:251], v[0:3], a[92:95]\n" \
"s_waitcnt vmcnt(13) lgkmcnt(6)\n" \
"v_mfma_f32_16x16x32_bf16 v[242:245], a[4:7], v[88:91], v[242:245]\n" \
"v_mfma_f32_16x16x32_bf16 a[92:95], a[252:255], v[32:35], a[92:95]\n" \
"s_barrier\n" \
"ds_read_b128 a[248:251], v246 offset:12544\n" \
"v_mfma_f32_16x16x32_bf16 v[24:27], a[192:195], a[176:179], v[236:239]\n" \
"ds_read_b128 v[192:195], v246 offset:16384\n" \
"v_bfe_u32 v69, v248, 8, 1\n" \
"v_mfma_f32_16x16x32_bf16 v[24:27], a[196:199], a[180:183], v[24:27]\n" \
"ds_read_b128 v[196:199], v246 offset:18432\n" \
"v_cvt_pk_bf16_f32 v91, v94, v95\n" \
"v_mfma_f32_16x16x32_bf16 v[24:27], a[200:203], a[184:187], v[24:27]\n" \
"ds_read_b128 v[200:203], v246 offset:20480\n" \
"v_bfe_u32 v70, v248, 6, 2\n" \
"v_mfma_f32_16x16x32_bf16 v[24:27], a[204:207], a[188:191], v[24:27]\n" \
"ds_read_b128 a[252:255], v246 offset:14592\n" \
"v_lshl_add_u32 v70, v69, 3, v70\n" \
"v_exp_f32 v96, v96\n" \
"v_exp_f32 v97, v97\n" \
"v_exp_f32 v98, v98\n" \
"v_cvt_pk_bf16_f32 v96, v96, v97\n" \
"v_exp_f32 v99, v99\n" \
"v_exp_f32 v100, v100\n" \
"v_cvt_pk_bf16_f32 v97, v98, v99\n" \
"v_exp_f32 v101, v101\n" \
"v_exp_f32 v102, v102\n" \
"v_cvt_pk_bf16_f32 v98, v100, v101\n" \
"v_mfma_f32_16x16x32_bf16 v[16:19], a[200:203], a[168:171], v[232:235]\n" \
"ds_read_b128 v[204:207], v246 offset:22528\n" \
"v_mul_u32_u24 v70, 6144, v70\n" \
"v_mfma_f32_16x16x32_bf16 v[16:19], a[204:207], a[172:175], v[16:19]\n" \
"ds_read_b128 v[208:211], v246 offset:16640\n" \
"v_lshlrev_b32 v69, 5, v69\n" \
"v_mfma_f32_16x16x32_bf16 v[16:19], a[192:195], a[160:163], v[16:19]\n" \
"ds_read_b128 v[212:215], v246 offset:18688\n" \
"v_and_b32 v71, 0x30, v248\n" \
"v_mfma_f32_16x16x32_bf16 v[16:19], a[196:199], a[164:167], v[16:19]\n" \
"ds_read_b128 v[216:219], v246 offset:20736\n" \
"v_xor_b32 v69, v69, v71\n" \
"v_exp_f32 v103, v103\n" \
"v_exp_f32 v104, v104\n" \
"v_cvt_pk_bf16_f32 v99, v102, v103\n" \
"v_exp_f32 v105, v105\n" \
"v_exp_f32 v106, v106\n" \
"v_mfma_f32_16x16x32_bf16 v[20:23], a[208:211], a[160:163], v[232:235]\n" \
"ds_read_b128 v[220:223], v246 offset:22784\n" \
"v_lshrrev_b32 v71, 9, v248\n" \
"v_mfma_f32_16x16x32_bf16 v[20:23], a[212:215], a[164:167], v[20:23]\n" \
"ds_read_b128 a[192:195], v247 offset:32768\n" \
"v_lshl_add_u32 v70, v71, 6, v70\n" \
"v_mfma_f32_16x16x32_bf16 v[20:23], a[216:219], a[168:171], v[20:23]\n" \
"ds_read_b128 a[196:199], v247 offset:40960\n" \
"v_add_u32 v253, v69, v70\n" \
"v_mfma_f32_16x16x32_bf16 v[20:23], a[220:223], a[172:175], v[20:23]\n" \
"ds_read_b128 a[200:203], v247 offset:33792\n" \
"v_bfe_u32 v84, v248, 7, 2\n" \
"v_mfma_f32_16x16x32_bf16 v[28:31], a[216:219], a[184:187], v[236:239]\n" \
"ds_read_b128 a[204:207], v247 offset:41984\n" \
"v_bfe_u32 v85, v248, 5, 2\n" \
"v_mfma_f32_16x16x32_bf16 v[28:31], a[220:223], a[188:191], v[28:31]\n" \
"v_min_u32 v85, 2, v85\n" \
"v_mad_u32_u24 v84, v84, 3, v85\n" \
"v_mfma_f32_16x16x32_bf16 v[28:31], a[208:211], a[176:179], v[28:31]\n" \
"v_and_b32 v85, 16, v248\n" \
"v_lshl_add_u32 v85, v85, 2, s89\n" \
"v_mfma_f32_16x16x32_bf16 v[28:31], a[212:215], a[180:183], v[28:31]\n" \
"s_waitcnt lgkmcnt(2)\n" \
"ds_read_b128 a[208:211], v247 offset:34816\n" \
"v_lshl_add_u32 v84, v84, 11, v85\n" \
"v_mfma_f32_16x16x32_bf16 v[8:11], v[192:195], a[176:179], v[236:239]\n" \
"s_add_u32 m0, s84, 143360\n" \
"ds_read_b128 a[212:215], v247 offset:43008\n" \
"v_cvt_pk_bf16_f32 v104, v104, v105\n" \
"v_mfma_f32_16x16x32_bf16 v[8:11], v[196:199], a[180:183], v[8:11]\n" \
"ds_read_b128 a[216:219], v247 offset:35840\n" \
"global_load_lds_dwordx4 v253, s[48:49] offset:-4096\n" \
"v_mfma_f32_16x16x32_bf16 v[8:11], v[200:203], a[184:187], v[8:11]\n" \
"ds_read_b128 a[220:223], v247 offset:44032\n" \
"v_exp_f32 v107, v107\n" \
"v_mfma_f32_16x16x32_bf16 v[8:11], v[204:207], a[188:191], v[8:11]\n" \
"ds_read_b128 v[192:195], v246 offset:24576\n" \
"v_exp_f32 v108, v108\n" \
"v_mfma_f32_16x16x32_bf16 v[56:59], a[232:235], a[184:187], v[236:239]\n" \
"ds_read_b128 v[196:199], v246 offset:26624\n" \
"global_load_lds_dwordx4 v253, s[48:49] offset:-2048\n" \
"v_mfma_f32_16x16x32_bf16 v[56:59], a[236:239], a[188:191], v[56:59]\n" \
"ds_read_b128 v[200:203], v246 offset:28672\n" \
"v_cvt_pk_bf16_f32 v105, v106, v107\n" \
"v_mfma_f32_16x16x32_bf16 v[56:59], a[224:227], a[176:179], v[56:59]\n" \
"ds_read_b128 v[204:207], v246 offset:30720\n" \
"global_load_lds_dwordx4 v253, s[48:49] offset:0\n" \
"v_mfma_f32_16x16x32_bf16 v[56:59], a[228:231], a[180:183], v[56:59]\n" \
"v_exp_f32 v109, v109\n" \
"v_exp_f32 v110, v110\n" \
"v_mfma_f32_16x16x32_bf16 v[48:51], a[224:227], a[160:163], v[232:235]\n" \
"s_add_u32 s48, s48, 0x120000\n" \
"s_addc_u32 s49, s49, 0\n" \
"v_cvt_pk_bf16_f32 v106, v108, v109\n" \
"v_mfma_f32_16x16x32_bf16 v[48:51], a[228:231], a[164:167], v[48:51]\n" \
"v_exp_f32 v111, v111\n" \
"v_mfma_f32_16x16x32_bf16 v[48:51], a[232:235], a[168:171], v[48:51]\n" \
"v_exp_f32 v112, v112\n" \
"v_mfma_f32_16x16x32_bf16 v[48:51], a[236:239], a[172:175], v[48:51]\n" \
"ds_read_b128 a[224:227], v247 offset:36864\n" \
"global_load_lds_dwordx4 v253, s[50:51] offset:-3072\n" \
"v_mfma_f32_16x16x32_bf16 v[12:15], v[216:219], a[184:187], v[236:239]\n" \
"ds_read_b128 a[228:231], v247 offset:45056\n" \
"v_cvt_pk_bf16_f32 v107, v110, v111\n" \
"v_mfma_f32_16x16x32_bf16 v[12:15], v[220:223], a[188:191], v[12:15]\n" \
"ds_read_b128 a[232:235], v247 offset:37888\n" \
"global_load_lds_dwordx4 v253, s[50:51] offset:-1024\n" \
"v_mfma_f32_16x16x32_bf16 v[12:15], v[208:211], a[176:179], v[12:15]\n" \
"ds_read_b128 a[236:239], v247 offset:46080\n" \
"v_exp_f32 v113, v113\n" \
"v_mfma_f32_16x16x32_bf16 v[12:15], v[212:215], a[180:183], v[12:15]\n" \
"ds_read_b128 v[208:211], v246 offset:24832\n" \
"v_exp_f32 v114, v114\n" \
"v_mfma_f32_16x16x32_bf16 v[60:63], a[240:243], a[176:179], v[236:239]\n" \
"ds_read_b128 v[212:215], v246 offset:26880\n" \
"global_load_lds_dwordx4 v253, s[50:51] offset:1024\n" \
"v_mfma_f32_16x16x32_bf16 v[60:63], a[244:247], a[180:183], v[60:63]\n" \
"s_add_u32 s50, s50, 0x120000\n" \
"s_addc_u32 s51, s51, 0\n" \
"ds_read_b128 v[216:219], v246 offset:28928\n" \
"v_mfma_f32_16x16x32_bf16 v[60:63], a[248:251], a[184:187], v[60:63]\n" \
"ds_read_b128 v[220:223], v246 offset:30976\n" \
"v_cvt_pk_bf16_f32 v112, v112, v113\n" \
"v_mfma_f32_16x16x32_bf16 v[60:63], a[252:255], a[188:191], v[60:63]\n" \
"global_load_dword v85, v84, s[22:23]\n" \
"v_mfma_f32_16x16x32_bf16 v[52:55], a[248:251], a[168:171], v[232:235]\n" \
"v_exp_f32 v115, v115\n" \
"v_mfma_f32_16x16x32_bf16 v[52:55], a[252:255], a[172:175], v[52:55]\n" \
"v_exp_f32 v116, v116\n" \
"v_cvt_pk_bf16_f32 v113, v114, v115\n" \
"v_mfma_f32_16x16x32_bf16 v[52:55], a[240:243], a[160:163], v[52:55]\n" \
"v_exp_f32 v117, v117\n" \
"v_mfma_f32_16x16x32_bf16 v[52:55], a[244:247], a[164:167], v[52:55]\n" \
"s_waitcnt lgkmcnt(0)\n" \
"ds_read_b128 a[240:243], v247 offset:38912\n" \
"v_exp_f32 v118, v118\n" \
"v_mfma_f32_16x16x32_bf16 v[40:43], v[192:195], a[176:179], v[236:239]\n" \
"ds_read_b128 a[244:247], v247 offset:47104\n" \
"v_cvt_pk_bf16_f32 v114, v116, v117\n" \
"v_mfma_f32_16x16x32_bf16 v[40:43], v[196:199], a[180:183], v[40:43]\n" \
"ds_read_b128 a[248:251], v247 offset:39936\n" \
"v_exp_f32 v119, v119\n" \
"v_exp_f32 v120, v120\n" \
"v_mfma_f32_16x16x32_bf16 v[40:43], v[200:203], a[184:187], v[40:43]\n" \
"ds_read_b128 a[252:255], v247 offset:48128\n" \
"v_cvt_pk_bf16_f32 v115, v118, v119\n" \
"v_mfma_f32_16x16x32_bf16 v[40:43], v[204:207], a[188:191], v[40:43]\n" \
"v_exp_f32 v121, v121\n" \
"v_exp_f32 v122, v122\n" \
"v_cvt_pk_bf16_f32 v120, v120, v121\n" \
"v_mfma_f32_16x16x32_bf16 v[44:47], v[216:219], a[184:187], v[236:239]\n" \
"v_exp_f32 v123, v123\n" \
"v_mfma_f32_16x16x32_bf16 v[44:47], v[220:223], a[188:191], v[44:47]\n" \
"s_waitcnt vmcnt(12) lgkmcnt(0)\n" \
"v_exp_f32 v124, v124\n" \
"v_mfma_f32_16x16x32_bf16 v[44:47], v[208:211], a[176:179], v[44:47]\n" \
"v_add_u32 v247, 0x10000, v248\n" \
"v_cvt_pk_bf16_f32 v121, v122, v123\n" \
"v_mfma_f32_16x16x32_bf16 v[44:47], v[212:215], a[180:183], v[44:47]\n" \
"s_barrier\n" \
"v_exp_f32 v125, v125\n" \
"v_exp_f32 v126, v126\n" \
"v_cvt_pk_bf16_f32 v122, v124, v125\n" \
"v_mfma_f32_16x16x32_bf16 a[64:67], a[192:195], v[64:67], a[64:67]\n" \
"ds_read_b128 v[192:195], v247 offset:16384\n" \
"v_exp_f32 v127, v127\n" \
"v_mfma_f32_16x16x32_bf16 a[64:67], a[196:199], v[96:99], a[64:67]\n" \
"v_cvt_pk_bf16_f32 v123, v126, v127\n" \
"v_mfma_f32_16x16x32_bf16 a[96:99], a[192:195], v[72:75], a[96:99]\n" \
"s_lshl_b32 s70, s61, 14\n" \
"v_add_u32 v124, s70, v248\n" \
"v_xor_b32 v125, 0x4000, v124\n" \
"v_mfma_f32_16x16x32_bf16 a[96:99], a[196:199], v[104:107], a[96:99]\n" \
"s_lshl_b32 s70, s61, 9\n" \
"v_cmp_gt_i32 vcc, 0, v251\n" \
"v_cmp_gt_i32 s[64:65], 1, v251\n" \
"v_cmp_gt_i32 s[66:67], 2, v251\n" \
"v_cndmask_b32 v8, v8, v254, vcc\n" \
"v_mfma_f32_16x16x32_bf16 v[238:241], a[0:3], v[96:99], v[238:241]\n" \
"v_cndmask_b32 v16, v16, v254, vcc\n" \
"v_cmp_gt_i32 vcc, 3, v251\n" \
"v_mfma_f32_16x16x32_bf16 v[238:241], a[4:7], v[104:107], v[238:241]\n" \
"v_cndmask_b32 v9, v9, v254, s[64:65]\n" \
"v_cndmask_b32 v17, v17, v254, s[64:65]\n" \
"v_mfma_f32_16x16x32_bf16 v[128:131], a[192:195], v[80:83], v[128:131]\n" \
"s_add_u32 s70, s70, 110592\n" \
"ds_read_b128 v[196:199], v247 offset:24576\n" \
"v_lshrrev_b32 v126, 1, v248\n" \
"v_mfma_f32_16x16x32_bf16 v[128:131], a[196:199], v[112:115], v[128:131]\n" \
"v_add_u32 v126, s70, v126\n" \
"v_xor_b32 v127, 0x200, v126\n" \
"v_cmp_gt_i32 s[64:65], 16, v251\n" \
"v_cndmask_b32 v10, v10, v254, s[66:67]\n" \
"v_cndmask_b32 v18, v18, v254, s[66:67]\n" \
"v_cmp_gt_i32 s[66:67], 17, v251\n" \
"v_cndmask_b32 v11, v11, v254, vcc\n" \
"v_cndmask_b32 v19, v19, v254, vcc\n" \
"v_mfma_f32_16x16x32_bf16 v[160:163], a[192:195], v[88:91], v[160:163]\n" \
"v_lshrrev_b32 v71, 7, v248\n" \
"v_and_b32 v76, 0x70, v248\n" \
"v_mfma_f32_16x16x32_bf16 v[160:163], a[196:199], v[120:123], v[160:163]\n" \
"ds_read_b128 a[192:195], v247 offset:0\n" \
"v_lshlrev_b32 v77, 4, v71\n" \
"v_cmp_gt_i32 vcc, 18, v251\n" \
"v_cndmask_b32 v12, v12, v254, s[64:65]\n" \
"v_mfma_f32_16x16x32_bf16 v[242:245], a[0:3], v[112:115], v[242:245]\n" \
"v_cndmask_b32 v20, v20, v254, s[64:65]\n" \
"v_cmp_gt_i32 s[64:65], 19, v251\n" \
"v_mfma_f32_16x16x32_bf16 v[242:245], a[4:7], v[120:123], v[242:245]\n" \
"v_cndmask_b32 v13, v13, v254, s[66:67]\n" \
"v_cndmask_b32 v21, v21, v254, s[66:67]\n" \
"v_cmp_gt_i32 s[66:67], 32, v251\n" \
"v_cndmask_b32 v14, v14, v254, vcc\n" \
"v_mfma_f32_16x16x32_bf16 v[164:167], a[200:203], v[88:91], v[164:167]\n" \
"ds_read_b128 v[200:203], v247 offset:17408\n" \
"v_xor_b32 v77, v77, v76\n" \
"v_mfma_f32_16x16x32_bf16 v[164:167], a[204:207], v[120:123], v[164:167]\n" \
"ds_write_b128 v124, a[64:67] offset:0\n" \
"v_lshl_add_u32 v252, v71, 12, v77\n" \
"v_cndmask_b32 v22, v22, v254, vcc\n" \
"v_cmp_gt_i32 vcc, 33, v251\n" \
"v_cndmask_b32 v15, v15, v254, s[64:65]\n" \
"v_cndmask_b32 v23, v23, v254, s[64:65]\n" \
"v_cmp_gt_i32 s[64:65], 34, v251\n" \
"v_cndmask_b32 v40, v40, v254, s[66:67]\n" \
"v_cndmask_b32 v48, v48, v254, s[66:67]\n" \
"v_cmp_gt_i32 s[66:67], 35, v251\n" \
"v_cndmask_b32 v41, v41, v254, vcc\n" \
"v_cndmask_b32 v49, v49, v254, vcc\n" \
"v_mfma_f32_16x16x32_bf16 v[132:135], a[200:203], v[80:83], v[132:135]\n" \
"v_and_or_b32 v71, v71, 3, s86\n" \
"v_lshlrev_b32 v77, 4, v71\n" \
"v_mfma_f32_16x16x32_bf16 v[132:135], a[204:207], v[112:115], v[132:135]\n" \
"ds_read_b128 a[196:199], v247 offset:8192\n" \
"v_xor_b32 v77, v77, v76\n" \
"v_cmp_gt_i32 vcc, 48, v251\n" \
"v_cndmask_b32 v42, v42, v254, s[64:65]\n" \
"v_mfma_f32_16x16x32_bf16 a[100:103], a[200:203], v[72:75], a[100:103]\n" \
"ds_read_b128 v[204:207], v247 offset:25600\n" \
"v_lshl_add_u32 v246, v71, 12, v77\n" \
"v_mfma_f32_16x16x32_bf16 a[100:103], a[204:207], v[104:107], a[100:103]\n" \
"ds_write_b128 v124, a[96:99] offset:8192\n" \
"v_cndmask_b32 v50, v50, v254, s[64:65]\n" \
"v_mfma_f32_16x16x32_bf16 a[68:71], a[200:203], v[64:67], a[68:71]\n" \
"v_lshrrev_b32 v71, 9, v248\n" \
"v_lshl_add_u32 v246, v71, 30, v246\n" \
"v_mfma_f32_16x16x32_bf16 a[68:71], a[204:207], v[96:99], a[68:71]\n" \
"s_add_u32 m0, s95, 118784\n" \
"ds_read_b128 a[200:203], v247 offset:1024\n" \
"v_cmp_gt_i32 s[64:65], 49, v251\n" \
"v_mfma_f32_16x16x32_bf16 a[72:75], a[208:211], v[64:67], a[72:75]\n" \
"ds_read_b128 v[208:211], v247 offset:18432\n" \
"global_load_lds_dwordx4 v252, s[42:43] offset:0\n" \
"v_mfma_f32_16x16x32_bf16 a[72:75], a[212:215], v[96:99], a[72:75]\n" \
"ds_write_b128 v124, a[100:103] offset:9216\n" \
"global_load_lds_dwordx4 v252, s[42:43] offset:2048\n" \
"v_mfma_f32_16x16x32_bf16 a[104:107], a[208:211], v[72:75], a[104:107]\n" \
"v_cndmask_b32 v43, v43, v254, s[66:67]\n" \
"v_cndmask_b32 v51, v51, v254, s[66:67]\n" \
"v_mfma_f32_16x16x32_bf16 a[104:107], a[212:215], v[104:107], a[104:107]\n" \
"ds_read_b128 a[204:207], v247 offset:9216\n" \
"ds_write_b64 v126, v[240:241]\n" \
"v_mfma_f32_16x16x32_bf16 v[136:139], a[208:211], v[80:83], v[136:139]\n" \
"s_add_u32 s42, s42, 0x180\n" \
"s_addc_u32 s43, s43, 0\n" \
"ds_read_b128 v[212:215], v247 offset:26624\n" \
"v_mfma_f32_16x16x32_bf16 v[136:139], a[212:215], v[112:115], v[136:139]\n" \
"ds_write_b128 v124, a[68:71] offset:1024\n" \
"global_load_lds_dwordx4 v252, s[44:45] offset:1024\n" \
"v_mfma_f32_16x16x32_bf16 v[168:171], a[208:211], v[88:91], v[168:171]\n" \
"v_cmp_gt_i32 s[66:67], 50, v251\n" \
"v_cndmask_b32 v44, v44, v254, vcc\n" \
"v_mfma_f32_16x16x32_bf16 v[168:171], a[212:215], v[120:123], v[168:171]\n" \
"ds_read_b128 a[208:211], v247 offset:2048\n" \
"v_cndmask_b32 v52, v52, v254, vcc\n" \
"v_mfma_f32_16x16x32_bf16 v[172:175], a[216:219], v[88:91], v[172:175]\n" \
"ds_read_b128 v[216:219], v247 offset:19456\n" \
"v_cmp_gt_i32 vcc, 51, v251\n" \
"v_mfma_f32_16x16x32_bf16 v[172:175], a[220:223], v[120:123], v[172:175]\n" \
"ds_write_b128 v124, a[72:75] offset:2048\n" \
"global_load_lds_dwordx4 v252, s[44:45] offset:3072\n" \
"v_mfma_f32_16x16x32_bf16 v[140:143], a[216:219], v[80:83], v[140:143]\n" \
"s_add_u32 s44, s44, 0x180\n" \
"s_addc_u32 s45, s45, 0\n" \
"v_cndmask_b32 v45, v45, v254, s[64:65]\n" \
"v_mfma_f32_16x16x32_bf16 v[140:143], a[220:223], v[112:115], v[140:143]\n" \
"s_add_u32 m0, s85, 135168\n" \
"ds_read_b128 a[212:215], v247 offset:10240\n" \
"v_cndmask_b32 v53, v53, v254, s[64:65]\n" \
"v_mfma_f32_16x16x32_bf16 a[108:111], a[216:219], v[72:75], a[108:111]\n" \
"ds_read_b128 v[220:223], v247 offset:27648\n" \
"v_cndmask_b32 v46, v46, v254, s[66:67]\n" \
"v_mfma_f32_16x16x32_bf16 a[108:111], a[220:223], v[104:107], a[108:111]\n" \
"ds_write_b128 v124, a[104:107] offset:10240\n" \
"v_cndmask_b32 v54, v54, v254, s[66:67]\n" \
"v_mfma_f32_16x16x32_bf16 a[76:79], a[216:219], v[64:67], a[76:79]\n" \
"buffer_load_dwordx4 v246, s[12:15], s46 offen lds\n" \
"v_mfma_f32_16x16x32_bf16 a[76:79], a[220:223], v[96:99], a[76:79]\n" \
"s_add_u32 s46, s46, 0x180\n" \
"ds_read_b128 a[216:219], v247 offset:3072\n" \
"v_cndmask_b32 v47, v47, v254, vcc\n" \
"v_mfma_f32_16x16x32_bf16 a[80:83], a[224:227], v[64:67], a[80:83]\n" \
"ds_read_b128 v[224:227], v247 offset:20480\n" \
"v_cndmask_b32 v55, v55, v254, vcc\n" \
"v_mfma_f32_16x16x32_bf16 a[80:83], a[228:231], v[96:99], a[80:83]\n" \
"ds_write_b128 v124, a[108:111] offset:11264\n" \
"v_exp_f32 v8, v8\n" \
"v_exp_f32 v9, v9\n" \
"v_exp_f32 v10, v10\n" \
"v_cvt_pk_bf16_f32 v8, v8, v9\n" \
"v_mfma_f32_16x16x32_bf16 a[112:115], a[224:227], v[72:75], a[112:115]\n" \
"v_exp_f32 v11, v11\n" \
"v_mfma_f32_16x16x32_bf16 a[112:115], a[228:231], v[104:107], a[112:115]\n" \
"ds_read_b128 a[220:223], v247 offset:11264\n" \
"v_exp_f32 v12, v12\n" \
"v_mfma_f32_16x16x32_bf16 v[144:147], a[224:227], v[80:83], v[144:147]\n" \
"ds_read_b128 v[228:231], v247 offset:28672\n" \
"v_cvt_pk_bf16_f32 v9, v10, v11\n" \
"v_mfma_f32_16x16x32_bf16 v[144:147], a[228:231], v[112:115], v[144:147]\n" \
"ds_write_b128 v124, a[76:79] offset:3072\n" \
"v_exp_f32 v13, v13\n" \
"v_exp_f32 v14, v14\n" \
"v_cvt_pk_bf16_f32 v10, v12, v13\n" \
"v_mfma_f32_16x16x32_bf16 v[176:179], a[224:227], v[88:91], v[176:179]\n" \
"v_exp_f32 v15, v15\n" \
"v_mfma_f32_16x16x32_bf16 v[176:179], a[228:231], v[120:123], v[176:179]\n" \
"ds_read_b128 a[224:227], v247 offset:4096\n" \
"v_exp_f32 v40, v40\n" \
"v_mfma_f32_16x16x32_bf16 v[180:183], a[232:235], v[88:91], v[180:183]\n" \
"ds_read_b128 v[232:235], v247 offset:21504\n" \
"v_cvt_pk_bf16_f32 v11, v14, v15\n" \
"v_mfma_f32_16x16x32_bf16 v[180:183], a[236:239], v[120:123], v[180:183]\n" \
"ds_write_b128 v124, a[80:83] offset:4096\n" \
"v_exp_f32 v41, v41\n" \
"v_exp_f32 v42, v42\n" \
"v_cvt_pk_bf16_f32 v40, v40, v41\n" \
"v_mfma_f32_16x16x32_bf16 v[148:151], a[232:235], v[80:83], v[148:151]\n" \
"v_exp_f32 v43, v43\n" \
"v_mfma_f32_16x16x32_bf16 v[148:151], a[236:239], v[112:115], v[148:151]\n" \
"ds_read_b128 a[228:231], v247 offset:12288\n" \
"v_exp_f32 v44, v44\n" \
"v_mfma_f32_16x16x32_bf16 a[116:119], a[232:235], v[72:75], a[116:119]\n" \
"ds_read_b128 v[236:239], v247 offset:29696\n" \
"v_cvt_pk_bf16_f32 v41, v42, v43\n" \
"v_mfma_f32_16x16x32_bf16 a[116:119], a[236:239], v[104:107], a[116:119]\n" \
"ds_write_b128 v124, a[112:115] offset:12288\n" \
"v_exp_f32 v45, v45\n" \
"v_exp_f32 v46, v46\n" \
"v_cvt_pk_bf16_f32 v42, v44, v45\n" \
"v_mfma_f32_16x16x32_bf16 a[84:87], a[232:235], v[64:67], a[84:87]\n" \
"v_exp_f32 v47, v47\n" \
"v_mfma_f32_16x16x32_bf16 a[84:87], a[236:239], v[96:99], a[84:87]\n" \
"ds_read_b128 a[232:235], v247 offset:5120\n" \
"v_exp_f32 v24, v24\n" \
"v_mfma_f32_16x16x32_bf16 a[88:91], a[240:243], v[64:67], a[88:91]\n" \
"ds_read_b128 v[0:3], v247 offset:22528\n" \
"v_cvt_pk_bf16_f32 v43, v46, v47\n" \
"v_mfma_f32_16x16x32_bf16 a[88:91], a[244:247], v[96:99], a[88:91]\n" \
"ds_write_b128 v124, a[116:119] offset:13312\n" \
"v_exp_f32 v25, v25\n" \
"v_exp_f32 v26, v26\n" \
"v_cvt_pk_bf16_f32 v24, v24, v25\n" \
"v_mfma_f32_16x16x32_bf16 v[242:245], a[4:7], v[8:11], v[242:245]\n" \
"v_exp_f32 v27, v27\n" \
"v_mfma_f32_16x16x32_bf16 v[242:245], a[4:7], v[40:43], v[242:245]\n" \
"v_exp_f32 v28, v28\n" \
"v_cvt_pk_bf16_f32 v25, v26, v27\n" \
"v_mfma_f32_16x16x32_bf16 a[120:123], a[240:243], v[72:75], a[120:123]\n" \
"v_exp_f32 v29, v29\n" \
"v_mfma_f32_16x16x32_bf16 a[120:123], a[244:247], v[104:107], a[120:123]\n" \
"ds_read_b128 a[236:239], v247 offset:13312\n" \
"v_exp_f32 v30, v30\n" \
"v_mfma_f32_16x16x32_bf16 v[152:155], a[240:243], v[80:83], v[152:155]\n" \
"ds_read_b128 v[4:7], v247 offset:30720\n" \
"v_cvt_pk_bf16_f32 v26, v28, v29\n" \
"v_mfma_f32_16x16x32_bf16 v[152:155], a[244:247], v[112:115], v[152:155]\n" \
"ds_write_b128 v124, a[84:87] offset:5120\n" \
"v_exp_f32 v31, v31\n" \
"v_exp_f32 v56, v56\n" \
"v_cvt_pk_bf16_f32 v27, v30, v31\n" \
"v_mfma_f32_16x16x32_bf16 v[184:187], a[240:243], v[88:91], v[184:187]\n" \
"v_exp_f32 v57, v57\n" \
"v_mfma_f32_16x16x32_bf16 v[184:187], a[244:247], v[120:123], v[184:187]\n" \
"ds_read_b128 a[240:243], v247 offset:6144\n" \
"v_exp_f32 v58, v58\n" \
"v_mfma_f32_16x16x32_bf16 a[92:95], a[248:251], v[64:67], a[92:95]\n" \
"ds_read_b128 v[32:35], v247 offset:23552\n" \
"v_cvt_pk_bf16_f32 v56, v56, v57\n" \
"v_mfma_f32_16x16x32_bf16 a[92:95], a[252:255], v[96:99], a[92:95]\n" \
"ds_write_b128 v124, a[88:91] offset:6144\n" \
"v_exp_f32 v59, v59\n" \
"v_exp_f32 v60, v60\n" \
"v_cvt_pk_bf16_f32 v57, v58, v59\n" \
"v_mfma_f32_16x16x32_bf16 a[124:127], a[248:251], v[72:75], a[124:127]\n" \
"v_exp_f32 v61, v61\n" \
"v_mfma_f32_16x16x32_bf16 a[124:127], a[252:255], v[104:107], a[124:127]\n" \
"ds_read_b128 a[244:247], v247 offset:14336\n" \
"v_exp_f32 v62, v62\n" \
"v_mfma_f32_16x16x32_bf16 v[156:159], a[248:251], v[80:83], v[156:159]\n" \
"ds_read_b128 v[36:39], v247 offset:31744\n" \
"v_cvt_pk_bf16_f32 v58, v60, v61\n" \
"v_mfma_f32_16x16x32_bf16 v[156:159], a[252:255], v[112:115], v[156:159]\n" \
"ds_write_b128 v124, a[120:123] offset:14336\n" \
"v_exp_f32 v63, v63\n" \
"v_mfma_f32_16x16x32_bf16 v[188:191], a[248:251], v[88:91], v[188:191]\n" \
"v_exp_f32 v16, v16\n" \
"v_mfma_f32_16x16x32_bf16 v[188:191], a[252:255], v[120:123], v[188:191]\n" \
"s_waitcnt lgkmcnt(3)\n" \
"ds_read_b128 a[248:251], v247 offset:7168\n" \
"ds_write_b128 v124, a[92:95] offset:7168\n" \
"v_cvt_pk_bf16_f32 v59, v62, v63\n" \
"v_exp_f32 v17, v17\n" \
"v_mfma_f32_16x16x32_bf16 v[160:163], v[192:195], v[8:11], v[160:163]\n" \
"v_exp_f32 v18, v18\n" \
"v_mfma_f32_16x16x32_bf16 v[160:163], v[196:199], v[40:43], v[160:163]\n" \
"ds_read_b128 a[252:255], v247 offset:15360\n" \
"v_cvt_pk_bf16_f32 v16, v16, v17\n" \
"v_mfma_f32_16x16x32_bf16 v[242:245], a[4:7], v[24:27], v[242:245]\n" \
"v_exp_f32 v19, v19\n" \
"v_mfma_f32_16x16x32_bf16 v[242:245], a[4:7], v[56:59], v[242:245]\n" \
"v_exp_f32 v20, v20\n" \
"v_cvt_pk_bf16_f32 v17, v18, v19\n" \
"v_exp_f32 v21, v21\n" \
"v_mfma_f32_16x16x32_bf16 v[164:167], v[200:203], v[8:11], v[164:167]\n" \
"v_exp_f32 v22, v22\n" \
"v_mfma_f32_16x16x32_bf16 v[164:167], v[204:207], v[40:43], v[164:167]\n" \
"ds_write_b128 v124, a[124:127] offset:15360\n" \
"v_cvt_pk_bf16_f32 v18, v20, v21\n" \
"v_exp_f32 v23, v23\n" \
"v_mfma_f32_16x16x32_bf16 v[168:171], v[208:211], v[8:11], v[168:171]\n" \
"v_exp_f32 v48, v48\n" \
"v_mfma_f32_16x16x32_bf16 v[168:171], v[212:215], v[40:43], v[168:171]\n" \
"s_waitcnt vmcnt(0) lgkmcnt(0)\n" \
"v_cvt_pk_bf16_f32 v19, v22, v23\n" \
"v_exp_f32 v49, v49\n" \
"v_mfma_f32_16x16x32_bf16 v[172:175], v[216:219], v[8:11], v[172:175]\n" \
"v_exp_f32 v50, v50\n" \
"v_mfma_f32_16x16x32_bf16 v[172:175], v[220:223], v[40:43], v[172:175]\n" \
"s_barrier\n" \
"v_cvt_pk_bf16_f32 v48, v48, v49\n" \
"v_mfma_f32_16x16x32_bf16 v[242:245], a[0:3], v[16:19], v[242:245]\n" \
"v_exp_f32 v51, v51\n" \
"v_exp_f32 v52, v52\n" \
"v_mfma_f32_16x16x32_bf16 v[176:179], v[224:227], v[8:11], v[176:179]\n" \
"s_add_u32 m0, s84, 90112\n" \
"ds_read_b64 v[240:241], v127\n" \
"v_cvt_pk_bf16_f32 v49, v50, v51\n" \
"v_mfma_f32_16x16x32_bf16 v[176:179], v[228:231], v[40:43], v[176:179]\n" \
"ds_read_b128 v[64:67], v125 offset:0\n" \
"global_load_lds_dwordx4 v253, s[48:49] offset:-4096\n" \
"v_exp_f32 v53, v53\n" \
"v_exp_f32 v54, v54\n" \
"v_mfma_f32_16x16x32_bf16 v[180:183], v[232:235], v[8:11], v[180:183]\n" \
"ds_read_b128 v[68:71], v125 offset:1024\n" \
"global_load_lds_dwordx4 v253, s[48:49] offset:-2048\n" \
"v_mfma_f32_16x16x32_bf16 v[180:183], v[236:239], v[40:43], v[180:183]\n" \
"ds_read_b128 v[72:75], v125 offset:2048\n" \
"v_cvt_pk_bf16_f32 v50, v52, v53\n" \
"v_mfma_f32_16x16x32_bf16 v[184:187], v[0:3], v[8:11], v[184:187]\n" \
"ds_read_b128 v[76:79], v125 offset:3072\n" \
"global_load_lds_dwordx4 v253, s[48:49] offset:0\n" \
"v_mfma_f32_16x16x32_bf16 v[184:187], v[4:7], v[40:43], v[184:187]\n" \
"s_add_u32 s48, s48, 0x300000\n" \
"s_addc_u32 s49, s49, 0\n" \
"ds_read_b128 v[80:83], v125 offset:4096\n" \
"v_mfma_f32_16x16x32_bf16 v[188:191], v[32:35], v[8:11], v[188:191]\n" \
"ds_read_b128 v[84:87], v125 offset:5120\n" \
"global_load_lds_dwordx4 v253, s[50:51] offset:-3072\n" \
"v_mfma_f32_16x16x32_bf16 v[188:191], v[36:39], v[40:43], v[188:191]\n" \
"s_waitcnt lgkmcnt(5)\n" \
"ds_read_b128 v[88:91], v125 offset:6144\n" \
"v_exp_f32 v55, v55\n" \
"v_mfma_f32_16x16x32_bf16 v[160:163], a[192:195], v[24:27], v[160:163]\n" \
"ds_read_b128 v[92:95], v125 offset:7168\n" \
"global_load_lds_dwordx4 v253, s[50:51] offset:-1024\n" \
"v_mfma_f32_16x16x32_bf16 v[160:163], a[196:199], v[56:59], v[160:163]\n" \
"s_movk_i32 s89, 0xc0\n" \
"ds_read_b128 v[96:99], v125 offset:8192\n" \
"global_load_lds_dwordx4 v253, s[50:51] offset:1024\n" \
"v_mfma_f32_16x16x32_bf16 v[164:167], a[200:203], v[24:27], v[164:167]\n" \
"s_add_u32 s50, s50, 0x300000\n" \
"s_addc_u32 s51, s51, 0\n" \
"ds_read_b128 v[100:103], v125 offset:9216\n" \
"v_mfma_f32_16x16x32_bf16 v[164:167], a[204:207], v[56:59], v[164:167]\n" \
"s_add_u32 m0, s95, 65536\n" \
"s_movk_i32 s90, 288\n" \
"ds_read_b128 v[104:107], v125 offset:10240\n" \
"v_lshrrev_b32 v211, 4, v248\n" \
"v_lshrrev_b32 v214, 8, v248\n" \
"v_and_b32 v215, 7, v211\n" \
"v_xor_b32 v214, v214, v215\n" \
"v_pk_add_f32 v[128:129], v[128:129], v[64:65]\n" \
"v_pk_add_f32 v[130:131], v[130:131], v[66:67]\n" \
"s_waitcnt lgkmcnt(9)\n" \
"v_pk_add_f32 v[132:133], v[132:133], v[68:69]\n" \
"v_pk_add_f32 v[134:135], v[134:135], v[70:71]\n" \
"s_waitcnt lgkmcnt(8)\n" \
"v_pk_add_f32 v[136:137], v[136:137], v[72:73]\n" \
"v_pk_add_f32 v[138:139], v[138:139], v[74:75]\n" \
"s_waitcnt lgkmcnt(7)\n" \
"v_pk_add_f32 v[140:141], v[140:141], v[76:77]\n" \
"v_pk_add_f32 v[142:143], v[142:143], v[78:79]\n" \
"s_waitcnt lgkmcnt(6)\n" \
"v_pk_add_f32 v[144:145], v[144:145], v[80:81]\n" \
"v_pk_add_f32 v[146:147], v[146:147], v[82:83]\n" \
"s_waitcnt lgkmcnt(5)\n" \
"v_pk_add_f32 v[148:149], v[148:149], v[84:85]\n" \
"v_pk_add_f32 v[150:151], v[150:151], v[86:87]\n" \
"s_waitcnt lgkmcnt(4)\n" \
"v_pk_add_f32 v[152:153], v[152:153], v[88:89]\n" \
"v_pk_add_f32 v[154:155], v[154:155], v[90:91]\n" \
"s_waitcnt lgkmcnt(3)\n" \
"v_pk_add_f32 v[156:157], v[156:157], v[92:93]\n" \
"v_pk_add_f32 v[158:159], v[158:159], v[94:95]\n" \
"v_mfma_f32_16x16x32_bf16 v[168:171], a[208:211], v[24:27], v[168:171]\n" \
"s_lshr_b32 s71, s61, 1\n" \
"ds_read_b128 v[108:111], v125 offset:11264\n" \
"global_load_lds_dwordx4 v252, s[42:43] offset:0\n" \
"v_mfma_f32_16x16x32_bf16 v[168:171], a[212:215], v[56:59], v[168:171]\n" \
"s_lshl_b32 s70, s71, 11\n" \
"ds_read_b128 v[112:115], v125 offset:12288\n" \
"v_cvt_pk_bf16_f32 v51, v54, v55\n" \
"v_mfma_f32_16x16x32_bf16 v[172:175], a[216:219], v[24:27], v[172:175]\n" \
"s_waitcnt lgkmcnt(4)\n" \
"s_add_u32 s70, s70, 118784\n" \
"ds_read_b128 v[116:119], v125 offset:13312\n" \
"global_load_lds_dwordx4 v252, s[42:43] offset:2048\n" \
"v_mfma_f32_16x16x32_bf16 v[242:245], a[0:3], v[48:51], v[242:245]\n" \
"v_and_b32 v216, 15, v211\n" \
"v_lshlrev_b32 v213, 7, v216\n" \
"v_lshl_add_u32 v213, v214, 4, v213\n" \
"v_mfma_f32_16x16x32_bf16 v[172:175], a[220:223], v[56:59], v[172:175]\n" \
"s_add_u32 s42, s42, 0x400\n" \
"s_addc_u32 s43, s43, 0\n" \
"ds_read_b128 v[120:123], v125 offset:14336\n" \
"v_add_u32 v205, s70, v213\n" \
"v_xor_b32 v206, 64, v205\n" \
"v_lshrrev_b32 v216, 2, v216\n" \
"v_lshl_add_u32 v216, v216, 9, v213\n" \
"v_add_u32 v207, 135168, v216\n" \
"v_xor_b32 v208, 64, v207\n" \
"v_pk_add_f32 v[160:161], v[160:161], v[96:97]\n" \
"v_pk_add_f32 v[162:163], v[162:163], v[98:99]\n" \
"v_mfma_f32_16x16x32_bf16 v[176:179], a[224:227], v[24:27], v[176:179]\n" \
"s_and_b32 s70, s61, 1\n" \
"ds_read_b128 v[124:127], v125 offset:15360\n" \
"s_lshl_b32 s70, s70, 9\n" \
"v_mfma_f32_16x16x32_bf16 v[176:179], a[228:231], v[56:59], v[176:179]\n" \
"s_waitcnt lgkmcnt(6)\n" \
"global_load_lds_dwordx4 v252, s[44:45] offset:1024\n" \
"global_load_lds_dwordx4 v252, s[44:45] offset:3072\n" \
"v_mfma_f32_16x16x32_bf16 v[180:183], a[232:235], v[24:27], v[180:183]\n" \
"s_add_u32 s70, s70, 139264\n" \
"s_add_u32 s44, s44, 0x400\n" \
"s_addc_u32 s45, s45, 0\n" \
"v_mfma_f32_16x16x32_bf16 v[180:183], a[236:239], v[56:59], v[180:183]\n" \
"s_waitcnt lgkmcnt(0)\n" \
"s_lshl_b32 s71, s71, 5\n" \
"v_and_b32 v213, 3, v211\n" \
"v_lshlrev_b32 v213, 3, v213\n" \
"v_mfma_f32_16x16x32_bf16 v[184:187], a[240:243], v[24:27], v[184:187]\n" \
"s_add_u32 m0, s85, 81920\n" \
"v_and_or_b32 v213, v248, s89, v213\n" \
"v_bfe_u32 v214, v211, 4, 1\n" \
"v_mfma_f32_16x16x32_bf16 v[184:187], a[244:247], v[56:59], v[184:187]\n" \
"s_barrier\n" \
"v_mad_u32_u24 v213, v214, s90, v213\n" \
"v_lshrrev_b32 v214, 5, v211\n" \
"v_mul_u32_u24 v214, 6144, v214\n" \
"v_add3_u32 v213, v213, v214, s70\n" \
"v_pk_add_f32 v[164:165], v[164:165], v[100:101]\n" \
"v_pk_add_f32 v[166:167], v[166:167], v[102:103]\n" \
"v_pk_add_f32 v[168:169], v[168:169], v[104:105]\n" \
"v_pk_add_f32 v[170:171], v[170:171], v[106:107]\n" \
"v_pk_add_f32 v[172:173], v[172:173], v[108:109]\n" \
"v_pk_add_f32 v[174:175], v[174:175], v[110:111]\n" \
"Lpg8622_%=:\n" \
"Lst1x2_%=:\n" \
"s_cbranch_execz Lpg10886_%=\n" \
"v_mfma_f32_16x16x32_bf16 v[188:191], a[248:251], v[24:27], v[188:191]\n" \
"v_xor_b32 v209, s71, v213\n" \
"v_xor_b32 v210, 32, v209\n" \
"v_mfma_f32_16x16x32_bf16 v[188:191], a[252:255], v[56:59], v[188:191]\n" \
"ds_read_b128 a[64:67], v207 offset:0\n" \
"ds_read_b64_tr_b16 a[96:97], v209 offset:2048\n" \
"v_mfma_f32_16x16x32_bf16 v[128:131], a[192:195], v[16:19], v[128:131]\n" \
"ds_read_b64_tr_b16 a[98:99], v209 offset:3072\n" \
"v_mfma_f32_16x16x32_bf16 v[128:131], a[196:199], v[48:51], v[128:131]\n" \
"ds_read_b128 a[68:71], v208 offset:0\n" \
"v_mfma_f32_16x16x32_bf16 v[132:135], a[200:203], v[16:19], v[132:135]\n" \
"ds_read_b64_tr_b16 a[100:101], v209 offset:14336\n" \
"ds_read_b64_tr_b16 a[102:103], v209 offset:15360\n" \
"buffer_load_dwordx4 v246, s[12:15], s46 offen lds\n" \
"v_pk_add_f32 v[176:177], v[176:177], v[112:113]\n" \
"v_pk_add_f32 v[178:179], v[178:179], v[114:115]\n" \
"v_pk_add_f32 v[180:181], v[180:181], v[116:117]\n" \
"v_pk_add_f32 v[182:183], v[182:183], v[118:119]\n" \
"v_pk_add_f32 v[184:185], v[184:185], v[120:121]\n" \
"v_pk_add_f32 v[186:187], v[186:187], v[122:123]\n" \
"v_pk_add_f32 v[188:189], v[188:189], v[124:125]\n" \
"v_pk_add_f32 v[190:191], v[190:191], v[126:127]\n" \
"v_mfma_f32_16x16x32_bf16 v[132:135], a[204:207], v[48:51], v[132:135]\n" \
"ds_read_b128 a[72:75], v205 offset:0\n" \
"s_add_u32 s46, s46, 0x400\n" \
"v_mfma_f32_16x16x32_bf16 v[136:139], a[208:211], v[16:19], v[136:139]\n" \
"s_add_u32 m0, s95, 0\n" \
"ds_read_b64_tr_b16 a[104:105], v209 offset:0\n" \
"v_mfma_f32_16x16x32_bf16 v[136:139], a[212:215], v[48:51], v[136:139]\n" \
"ds_read_b128 a[76:79], v206 offset:0\n" \
"ds_read_b64_tr_b16 a[106:107], v209 offset:1024\n" \
"v_mfma_f32_16x16x32_bf16 v[140:143], a[216:219], v[16:19], v[140:143]\n" \
"ds_read_b64_tr_b16 a[108:109], v209 offset:12288\n" \
"global_load_lds_dwordx4 v252, s[42:43] offset:0\n" \
"v_mfma_f32_16x16x32_bf16 v[140:143], a[220:223], v[48:51], v[140:143]\n" \
"ds_read_b64_tr_b16 a[110:111], v209 offset:13312\n" \
"global_load_lds_dwordx4 v252, s[42:43] offset:2048\n" \
"v_mfma_f32_16x16x32_bf16 v[144:147], a[224:227], v[16:19], v[144:147]\n" \
"ds_read_b128 a[80:83], v205 offset:4096\n" \
"ds_read_b64_tr_b16 a[112:113], v210 offset:0\n" \
"v_mfma_f32_16x16x32_bf16 v[144:147], a[228:231], v[48:51], v[144:147]\n" \
"s_add_u32 s42, s42, 0xfffffb00\n" \
"s_addc_u32 s43, s43, -1\n" \
"ds_read_b64_tr_b16 a[114:115], v210 offset:1024\n" \
"v_mfma_f32_16x16x32_bf16 v[148:151], a[232:235], v[16:19], v[148:151]\n" \
"ds_read_b64_tr_b16 a[116:117], v210 offset:12288\n" \
"global_load_lds_dwordx4 v252, s[44:45] offset:1024\n" \
"v_mfma_f32_16x16x32_bf16 v[148:151], a[236:239], v[48:51], v[148:151]\n" \
"ds_read_b128 a[84:87], v206 offset:4096\n" \
"ds_read_b64_tr_b16 a[118:119], v210 offset:13312\n" \
"v_mfma_f32_16x16x32_bf16 v[152:155], a[240:243], v[16:19], v[152:155]\n" \
"ds_read_b128 a[88:91], v205 offset:8192\n" \
"global_load_lds_dwordx4 v252, s[44:45] offset:3072\n" \
"v_mfma_f32_16x16x32_bf16 v[152:155], a[244:247], v[48:51], v[152:155]\n" \
"s_add_u32 s44, s44, 0xfffffb00\n" \
"s_addc_u32 s45, s45, -1\n" \
"ds_read_b128 a[92:95], v206 offset:8192\n" \
"v_mfma_f32_16x16x32_bf16 v[156:159], a[248:251], v[16:19], v[156:159]\n" \
"s_add_u32 m0, s85, 16384\n" \
"v_add_f32 v244, v244, v240\n" \
"v_add_f32 v245, v245, v241\n" \
"v_mfma_f32_16x16x32_bf16 v[156:159], a[252:255], v[48:51], v[156:159]\n" \
"s_waitcnt lgkmcnt(4)\n" \
"buffer_load_dwordx4 v246, s[12:15], s46 offen lds\n" \
"Ljoin_%=:\n" \
"v_mfma_f32_16x16x32_bf16 v[32:35], a[96:99], a[64:67], 0\n" \
"s_add_u32 s46, s46, 0xfffffb00\n" \
"ds_read_b128 a[192:195], v205 offset:12288\n" \
"s_add_u32 m0, s84, 24576\n" \
"v_mfma_f32_16x16x32_bf16 v[32:35], a[100:103], a[68:71], v[32:35]\n" \
"global_load_lds_dwordx4 v253, s[48:49] offset:-4096\n" \
"v_mfma_f32_16x16x32_bf16 v[40:43], a[96:99], a[72:75], 0\n" \
"ds_read_b128 a[196:199], v206 offset:12288\n" \
"v_mfma_f32_16x16x32_bf16 v[40:43], a[100:103], a[76:79], v[40:43]\n" \
"ds_read_b64_tr_b16 a[208:209], v209 offset:4096\n" \
"v_mov_b32 v232, v244\n" \
"v_mfma_f32_16x16x32_bf16 v[0:3], a[104:107], a[72:75], 0\n" \
"ds_read_b64_tr_b16 a[210:211], v209 offset:5120\n" \
"global_load_lds_dwordx4 v253, s[48:49] offset:-2048\n" \
"v_mfma_f32_16x16x32_bf16 v[0:3], a[108:111], a[76:79], v[0:3]\n" \
"s_waitcnt lgkmcnt(3)\n" \
"v_permlane16_swap_b32 v232, v244\n" \
"v_mfma_f32_16x16x32_bf16 v[4:7], a[112:115], a[72:75], 0\n" \
"ds_read_b64_tr_b16 a[212:213], v209 offset:16384\n" \
"v_add_f32 v244, v244, v232\n" \
"v_mfma_f32_16x16x32_bf16 v[4:7], a[116:119], a[76:79], v[4:7]\n" \
"ds_read_b64_tr_b16 a[214:215], v209 offset:17408\n" \
"global_load_lds_dwordx4 v253, s[48:49] offset:0\n" \
"v_mfma_f32_16x16x32_bf16 v[12:15], a[112:115], a[80:83], 0\n" \
"s_add_u32 s48, s48, 0xffc40000\n" \
"s_addc_u32 s49, s49, -1\n" \
"ds_read_b64_tr_b16 a[216:217], v210 offset:4096\n" \
"v_mfma_f32_16x16x32_bf16 v[12:15], a[116:119], a[84:87], v[12:15]\n" \
"v_mov_b32 v232, v244\n" \
"v_mfma_f32_16x16x32_bf16 v[8:11], a[104:107], a[80:83], 0\n" \
"ds_read_b64_tr_b16 a[218:219], v210 offset:5120\n" \
"global_load_lds_dwordx4 v253, s[50:51] offset:-3072\n" \
"v_mfma_f32_16x16x32_bf16 v[8:11], a[108:111], a[84:87], v[8:11]\n" \
"ds_read_b64_tr_b16 a[220:221], v210 offset:16384\n" \
"v_mfma_f32_16x16x32_bf16 v[48:51], a[96:99], a[80:83], 0\n" \
"ds_read_b64_tr_b16 a[222:223], v210 offset:17408\n" \
"v_mfma_f32_16x16x32_bf16 v[48:51], a[100:103], a[84:87], v[48:51]\n" \
"global_load_lds_dwordx4 v253, s[50:51] offset:-1024\n" \
"v_mfma_f32_16x16x32_bf16 v[56:59], a[96:99], a[88:91], 0\n" \
"ds_read_b64_tr_b16 a[200:201], v210 offset:2048\n" \
"v_mfma_f32_16x16x32_bf16 v[56:59], a[100:103], a[92:95], v[56:59]\n" \
"ds_read_b64_tr_b16 a[202:203], v210 offset:3072\n" \
"v_mfma_f32_16x16x32_bf16 v[16:19], a[104:107], a[88:91], 0\n" \
"ds_read_b64_tr_b16 a[204:205], v210 offset:14336\n" \
"global_load_lds_dwordx4 v253, s[50:51] offset:1024\n" \
"v_mfma_f32_16x16x32_bf16 v[16:19], a[108:111], a[92:95], v[16:19]\n" \
"s_add_u32 s50, s50, 0xffc40000\n" \
"s_addc_u32 s51, s51, -1\n" \
"v_mfma_f32_16x16x32_bf16 v[20:23], a[112:115], a[88:91], 0\n" \
"ds_read_b64_tr_b16 a[206:207], v210 offset:15360\n" \
"v_mfma_f32_16x16x32_bf16 v[20:23], a[116:119], a[92:95], v[20:23]\n" \
"s_waitcnt lgkmcnt(3)\n" \
"v_add_u32 v207, 0xffff3000, v207\n" \
"v_add_u32 v208, 0xffff3000, v208\n" \
"v_mfma_f32_16x16x32_bf16 v[28:31], a[112:115], a[192:195], 0\n" \
"v_add_u32 v205, 0xffff3000, v205\n" \
"v_add_u32 v206, 0xffff3000, v206\n" \
"v_mfma_f32_16x16x32_bf16 v[28:31], a[116:119], a[196:199], v[28:31]\n" \
"v_add_u32 v209, 0xffff3000, v209\n" \
"v_add_u32 v210, 0xffff3000, v210\n" \
"v_mfma_f32_16x16x32_bf16 v[24:27], a[104:107], a[192:195], 0\n" \
"v_permlane32_swap_b32 v232, v244\n" \
"v_mfma_f32_16x16x32_bf16 v[24:27], a[108:111], a[196:199], v[24:27]\n" \
"s_waitcnt vmcnt(11) lgkmcnt(0)\n" \
"v_add_f32 v244, v244, v232\n" \
"v_mfma_f32_16x16x32_bf16 v[64:67], a[96:99], a[192:195], 0\n" \
"v_rcp_f32 v232, v244\n" \
"v_mfma_f32_16x16x32_bf16 v[64:67], a[100:103], a[196:199], v[64:67]\n" \
"s_barrier\n" \
"v_mul_f32 v244, 0x4d800000, v244\n" \
"v_mfma_f32_16x16x32_bf16 v[72:75], a[64:67], a[208:211], 0\n" \
"ds_read_b64_tr_b16 a[96:97], v209 offset:2048\n" \
"v_add_u32 v233, 0x1c90080, v249\n" \
"v_mfma_f32_16x16x32_bf16 v[72:75], a[68:71], a[212:215], v[72:75]\n" \
"ds_read_b64_tr_b16 a[98:99], v209 offset:3072\n" \
"v_cndmask_b32 v233, v249, v233, s[16:17]\n" \
"v_mfma_f32_16x16x32_bf16 v[80:83], a[72:75], a[208:211], 0\n" \
"s_add_u32 m0, s95, 118784\n" \
"ds_read_b128 a[64:67], v207 offset:0\n" \
"ds_read_b64_tr_b16 a[100:101], v209 offset:14336\n" \
"v_mfma_f32_16x16x32_bf16 v[80:83], a[76:79], a[212:215], v[80:83]\n" \
"global_load_lds_dwordx4 v252, s[42:43] offset:0\n" \
"v_mul_f32 v128, v128, v232\n" \
"v_mfma_f32_16x16x32_bf16 v[84:87], a[72:75], a[216:219], 0\n" \
"ds_read_b128 a[68:71], v208 offset:0\n" \
"ds_read_b64_tr_b16 a[102:103], v209 offset:15360\n" \
"v_mfma_f32_16x16x32_bf16 v[84:87], a[76:79], a[220:223], v[84:87]\n" \
"ds_read_b64_tr_b16 a[104:105], v209 offset:0\n" \
"v_mul_f32 v129, v129, v232\n" \
"v_mfma_f32_16x16x32_bf16 v[44:47], a[200:203], a[72:75], 0\n" \
"ds_read_b64_tr_b16 a[106:107], v209 offset:1024\n" \
"v_mul_f32 v130, v130, v232\n" \
"v_mfma_f32_16x16x32_bf16 v[44:47], a[204:207], a[76:79], v[44:47]\n" \
"global_load_lds_dwordx4 v252, s[42:43] offset:2048\n" \
"v_mul_f32 v131, v131, v232\n" \
"v_mfma_f32_16x16x32_bf16 v[52:55], a[200:203], a[80:83], 0\n" \
"s_add_u32 s42, s42, 0x80\n" \
"s_addc_u32 s43, s43, 0\n" \
"ds_read_b128 a[72:75], v205 offset:0\n" \
"v_mfma_f32_16x16x32_bf16 v[52:55], a[204:207], a[84:87], v[52:55]\n" \
"ds_read_b128 a[76:79], v206 offset:0\n" \
"v_mul_f32 v132, v132, v232\n" \
"v_mfma_f32_16x16x32_bf16 v[92:95], a[80:83], a[216:219], 0\n" \
"ds_read_b64_tr_b16 a[108:109], v209 offset:12288\n" \
"v_mul_f32 v133, v133, v232\n" \
"v_mfma_f32_16x16x32_bf16 v[92:95], a[84:87], a[220:223], v[92:95]\n" \
"global_load_lds_dwordx4 v252, s[44:45] offset:1024\n" \
"v_mul_f32 v134, v134, v232\n" \
"v_mfma_f32_16x16x32_bf16 v[88:91], a[80:83], a[208:211], 0\n" \
"ds_read_b64_tr_b16 a[110:111], v209 offset:13312\n" \
"v_mul_f32 v135, v135, v232\n" \
"v_mfma_f32_16x16x32_bf16 v[88:91], a[84:87], a[212:215], v[88:91]\n" \
"ds_read_b64_tr_b16 a[112:113], v210 offset:0\n" \
"v_fma_f32 v244, v128, v129, v244\n" \
"v_mfma_f32_16x16x32_bf16 v[96:99], a[88:91], a[208:211], 0\n" \
"ds_read_b128 a[80:83], v205 offset:4096\n" \
"v_cvt_pk_bf16_f32 v128, v128, v129\n" \
"v_mfma_f32_16x16x32_bf16 v[96:99], a[92:95], a[212:215], v[96:99]\n" \
"global_load_lds_dwordx4 v252, s[44:45] offset:3072\n" \
"v_fma_f32 v244, v130, v131, v244\n" \
"v_mfma_f32_16x16x32_bf16 v[100:103], a[88:91], a[216:219], 0\n" \
"s_add_u32 s44, s44, 0x80\n" \
"s_addc_u32 s45, s45, 0\n" \
"ds_read_b128 a[84:87], v206 offset:4096\n" \
"v_mfma_f32_16x16x32_bf16 v[100:103], a[92:95], a[220:223], v[100:103]\n" \
"ds_read_b64_tr_b16 a[114:115], v210 offset:1024\n" \
"v_cvt_pk_bf16_f32 v129, v130, v131\n" \
"v_mfma_f32_16x16x32_bf16 v[60:63], a[200:203], a[88:91], 0\n" \
"s_add_u32 m0, s85, 135168\n" \
"ds_read_b64_tr_b16 a[116:117], v210 offset:12288\n" \
"v_fma_f32 v244, v132, v133, v244\n" \
"v_mfma_f32_16x16x32_bf16 v[60:63], a[204:207], a[92:95], v[60:63]\n" \
"buffer_load_dwordx4 v246, s[12:15], s46 offen lds\n" \
"v_mfma_f32_16x16x32_bf16 v[68:71], a[200:203], a[192:195], 0\n" \
"s_add_u32 s46, s46, 0x80\n" \
"ds_read_b128 a[88:91], v205 offset:8192\n" \
"v_cvt_pk_bf16_f32 v130, v132, v133\n" \
"v_mfma_f32_16x16x32_bf16 v[68:71], a[204:207], a[196:199], v[68:71]\n" \
"ds_read_b128 a[92:95], v206 offset:8192\n" \
"v_fma_f32 v244, v134, v135, v244\n" \
"v_mfma_f32_16x16x32_bf16 v[108:111], a[192:195], a[216:219], 0\n" \
"s_add_u32 m0, s84, 143360\n" \
"ds_read_b64_tr_b16 a[118:119], v210 offset:13312\n" \
"v_cvt_pk_bf16_f32 v131, v134, v135\n" \
"v_mfma_f32_16x16x32_bf16 v[108:111], a[196:199], a[220:223], v[108:111]\n" \
"buffer_store_dwordx4 v[128:131], v233, s[56:59], s78 offen offset:0 sc1\n" \
"global_load_lds_dwordx4 v253, s[48:49] offset:-4096\n" \
"v_mul_f32 v136, v136, v232\n" \
"v_mul_f32 v137, v137, v232\n" \
"v_mul_f32 v138, v138, v232\n" \
"v_mfma_f32_16x16x32_bf16 v[104:107], a[192:195], a[208:211], 0\n" \
"v_mul_f32 v139, v139, v232\n" \
"v_mul_f32 v140, v140, v232\n" \
"v_mfma_f32_16x16x32_bf16 v[104:107], a[196:199], a[212:215], v[104:107]\n" \
"s_waitcnt lgkmcnt(2)\n" \
"v_mul_f32 v141, v141, v232\n" \
"v_mul_f32 v142, v142, v232\n" \
"v_mfma_f32_16x16x32_bf16 v[32:35], a[96:99], a[64:67], v[32:35]\n" \
"ds_read_b128 a[192:195], v205 offset:12288\n" \
"v_mul_f32 v143, v143, v232\n" \
"v_mfma_f32_16x16x32_bf16 v[32:35], a[100:103], a[68:71], v[32:35]\n" \
"global_load_lds_dwordx4 v253, s[48:49] offset:-2048\n" \
"v_fma_f32 v244, v136, v137, v244\n" \
"v_mfma_f32_16x16x32_bf16 v[40:43], a[96:99], a[72:75], v[40:43]\n" \
"ds_read_b128 a[196:199], v206 offset:12288\n" \
"v_cvt_pk_bf16_f32 v136, v136, v137\n" \
"v_mfma_f32_16x16x32_bf16 v[40:43], a[100:103], a[76:79], v[40:43]\n" \
"ds_read_b64_tr_b16 a[208:209], v209 offset:4096\n" \
"v_fma_f32 v244, v138, v139, v244\n" \
"v_mfma_f32_16x16x32_bf16 v[0:3], a[104:107], a[72:75], v[0:3]\n" \
"ds_read_b64_tr_b16 a[210:211], v209 offset:5120\n" \
"v_cvt_pk_bf16_f32 v137, v138, v139\n" \
"v_mfma_f32_16x16x32_bf16 v[0:3], a[108:111], a[76:79], v[0:3]\n" \
"s_waitcnt lgkmcnt(3)\n" \
"global_load_lds_dwordx4 v253, s[48:49] offset:0\n" \
"v_fma_f32 v244, v140, v141, v244\n" \
"v_mfma_f32_16x16x32_bf16 v[4:7], a[112:115], a[72:75], v[4:7]\n" \
"s_add_u32 s48, s48, 0x60000\n" \
"s_addc_u32 s49, s49, 0\n" \
"ds_read_b64_tr_b16 a[212:213], v209 offset:16384\n" \
"v_mfma_f32_16x16x32_bf16 v[4:7], a[116:119], a[76:79], v[4:7]\n" \
"ds_read_b64_tr_b16 a[214:215], v209 offset:17408\n" \
"v_cvt_pk_bf16_f32 v138, v140, v141\n" \
"v_mfma_f32_16x16x32_bf16 v[12:15], a[112:115], a[80:83], v[12:15]\n" \
"ds_read_b64_tr_b16 a[216:217], v210 offset:4096\n" \
"v_fma_f32 v244, v142, v143, v244\n" \
"v_mfma_f32_16x16x32_bf16 v[12:15], a[116:119], a[84:87], v[12:15]\n" \
"global_load_lds_dwordx4 v253, s[50:51] offset:-3072\n" \
"v_cvt_pk_bf16_f32 v139, v142, v143\n" \
"v_mfma_f32_16x16x32_bf16 v[8:11], a[104:107], a[80:83], v[8:11]\n" \
"ds_read_b64_tr_b16 a[218:219], v210 offset:5120\n" \
"v_mfma_f32_16x16x32_bf16 v[8:11], a[108:111], a[84:87], v[8:11]\n" \
"buffer_store_dwordx4 v[136:139], v233, s[56:59], s78 offen offset:2048 sc1\n" \
"ds_read_b64_tr_b16 a[220:221], v210 offset:16384\n" \
"s_add_u32 s78, s78, 0x1000\n" \
"v_mul_f32 v144, v144, v232\n" \
"v_mul_f32 v145, v145, v232\n" \
"v_mfma_f32_16x16x32_bf16 v[48:51], a[96:99], a[80:83], v[48:51]\n" \
"ds_read_b64_tr_b16 a[222:223], v210 offset:17408\n" \
"v_mul_f32 v146, v146, v232\n" \
"v_mfma_f32_16x16x32_bf16 v[48:51], a[100:103], a[84:87], v[48:51]\n" \
"global_load_lds_dwordx4 v253, s[50:51] offset:-1024\n" \
"v_mul_f32 v147, v147, v232\n" \
"v_mfma_f32_16x16x32_bf16 v[56:59], a[96:99], a[88:91], v[56:59]\n" \
"ds_read_b64_tr_b16 a[200:201], v210 offset:2048\n" \
"v_mul_f32 v148, v148, v232\n" \
"v_mfma_f32_16x16x32_bf16 v[56:59], a[100:103], a[92:95], v[56:59]\n" \
"ds_read_b64_tr_b16 a[202:203], v210 offset:3072\n" \
"v_mul_f32 v149, v149, v232\n" \
"v_mfma_f32_16x16x32_bf16 v[16:19], a[104:107], a[88:91], v[16:19]\n" \
"ds_read_b64_tr_b16 a[204:205], v210 offset:14336\n" \
"v_mul_f32 v150, v150, v232\n" \
"v_mfma_f32_16x16x32_bf16 v[16:19], a[108:111], a[92:95], v[16:19]\n" \
"global_load_lds_dwordx4 v253, s[50:51] offset:1024\n" \
"v_mul_f32 v151, v151, v232\n" \
"v_mfma_f32_16x16x32_bf16 v[20:23], a[112:115], a[88:91], v[20:23]\n" \
"s_add_u32 s50, s50, 0x60000\n" \
"s_addc_u32 s51, s51, 0\n" \
"ds_read_b64_tr_b16 a[206:207], v210 offset:15360\n" \
"v_mfma_f32_16x16x32_bf16 v[20:23], a[116:119], a[92:95], v[20:23]\n" \
"s_waitcnt lgkmcnt(3)\n" \
"v_add_u32 v207, 0xffff0000, v207\n" \
"v_add_u32 v208, 0xffff0000, v208\n" \
"v_mfma_f32_16x16x32_bf16 v[28:31], a[112:115], a[192:195], v[28:31]\n" \
"v_add_u32 v205, 0xffff0000, v205\n" \
"v_add_u32 v206, 0xffff0000, v206\n" \
"v_mfma_f32_16x16x32_bf16 v[28:31], a[116:119], a[196:199], v[28:31]\n" \
"v_add_u32 v209, 0xffff0000, v209\n" \
"v_add_u32 v210, 0xffff0000, v210\n" \
"v_mfma_f32_16x16x32_bf16 v[24:27], a[104:107], a[192:195], v[24:27]\n" \
"v_fma_f32 v244, v144, v145, v244\n" \
"v_cvt_pk_bf16_f32 v144, v144, v145\n" \
"v_mfma_f32_16x16x32_bf16 v[24:27], a[108:111], a[196:199], v[24:27]\n" \
"s_waitcnt vmcnt(13) lgkmcnt(0)\n" \
"v_fma_f32 v244, v146, v147, v244\n" \
"v_cvt_pk_bf16_f32 v145, v146, v147\n" \
"v_mfma_f32_16x16x32_bf16 v[64:67], a[96:99], a[192:195], v[64:67]\n" \
"v_fma_f32 v244, v148, v149, v244\n" \
"v_cvt_pk_bf16_f32 v146, v148, v149\n" \
"v_mfma_f32_16x16x32_bf16 v[64:67], a[100:103], a[196:199], v[64:67]\n" \
"s_barrier\n" \
"v_fma_f32 v244, v150, v151, v244\n" \
"v_mfma_f32_16x16x32_bf16 v[72:75], a[64:67], a[208:211], v[72:75]\n" \
"ds_read_b64_tr_b16 a[96:97], v209 offset:2048\n" \
"v_cvt_pk_bf16_f32 v147, v150, v151\n" \
"v_mfma_f32_16x16x32_bf16 v[72:75], a[68:71], a[212:215], v[72:75]\n" \
"buffer_store_dwordx4 v[144:147], v233, s[56:59], s78 offen offset:0 sc1\n" \
"ds_read_b64_tr_b16 a[98:99], v209 offset:3072\n" \
"v_mul_f32 v152, v152, v232\n" \
"v_mul_f32 v153, v153, v232\n" \
"v_mul_f32 v154, v154, v232\n" \
"v_mfma_f32_16x16x32_bf16 v[80:83], a[72:75], a[208:211], v[80:83]\n" \
"s_add_u32 m0, s95, 65536\n" \
"ds_read_b128 a[64:67], v207 offset:0\n" \
"ds_read_b64_tr_b16 a[100:101], v209 offset:14336\n" \
"v_mfma_f32_16x16x32_bf16 v[80:83], a[76:79], a[212:215], v[80:83]\n" \
"global_load_lds_dwordx4 v252, s[42:43] offset:0\n" \
"v_mul_f32 v155, v155, v232\n" \
"v_mfma_f32_16x16x32_bf16 v[84:87], a[72:75], a[216:219], v[84:87]\n" \
"ds_read_b128 a[68:71], v208 offset:0\n" \
"ds_read_b64_tr_b16 a[102:103], v209 offset:15360\n" \
"v_mfma_f32_16x16x32_bf16 v[84:87], a[76:79], a[220:223], v[84:87]\n" \
"ds_read_b64_tr_b16 a[104:105], v209 offset:0\n" \
"v_mul_f32 v156, v156, v232\n" \
"v_mfma_f32_16x16x32_bf16 v[44:47], a[200:203], a[72:75], v[44:47]\n" \
"ds_read_b64_tr_b16 a[106:107], v209 offset:1024\n" \
"v_mul_f32 v157, v157, v232\n" \
"v_mfma_f32_16x16x32_bf16 v[44:47], a[204:207], a[76:79], v[44:47]\n" \
"global_load_lds_dwordx4 v252, s[42:43] offset:2048\n" \
"v_mul_f32 v158, v158, v232\n" \
"v_mfma_f32_16x16x32_bf16 v[52:55], a[200:203], a[80:83], v[52:55]\n" \
"s_add_u32 s42, s42, 0x100\n" \
"s_addc_u32 s43, s43, 0\n" \
"ds_read_b128 a[72:75], v205 offset:0\n" \
"v_mfma_f32_16x16x32_bf16 v[52:55], a[204:207], a[84:87], v[52:55]\n" \
"ds_read_b128 a[76:79], v206 offset:0\n" \
"v_mul_f32 v159, v159, v232\n" \
"v_mfma_f32_16x16x32_bf16 v[92:95], a[80:83], a[216:219], v[92:95]\n" \
"ds_read_b64_tr_b16 a[108:109], v209 offset:12288\n" \
"v_fma_f32 v244, v152, v153, v244\n" \
"v_mfma_f32_16x16x32_bf16 v[92:95], a[84:87], a[220:223], v[92:95]\n" \
"global_load_lds_dwordx4 v252, s[44:45] offset:1024\n" \
"v_cvt_pk_bf16_f32 v152, v152, v153\n" \
"v_mfma_f32_16x16x32_bf16 v[88:91], a[80:83], a[208:211], v[88:91]\n" \
"ds_read_b64_tr_b16 a[110:111], v209 offset:13312\n" \
"v_fma_f32 v244, v154, v155, v244\n" \
"v_mfma_f32_16x16x32_bf16 v[88:91], a[84:87], a[212:215], v[88:91]\n" \
"ds_read_b64_tr_b16 a[112:113], v210 offset:0\n" \
"v_cvt_pk_bf16_f32 v153, v154, v155\n" \
"v_mfma_f32_16x16x32_bf16 v[96:99], a[88:91], a[208:211], v[96:99]\n" \
"ds_read_b128 a[80:83], v205 offset:4096\n" \
"v_fma_f32 v244, v156, v157, v244\n" \
"v_mfma_f32_16x16x32_bf16 v[96:99], a[92:95], a[212:215], v[96:99]\n" \
"global_load_lds_dwordx4 v252, s[44:45] offset:3072\n" \
"v_cvt_pk_bf16_f32 v154, v156, v157\n" \
"v_mfma_f32_16x16x32_bf16 v[100:103], a[88:91], a[216:219], v[100:103]\n" \
"s_add_u32 s44, s44, 0x100\n" \
"s_addc_u32 s45, s45, 0\n" \
"ds_read_b128 a[84:87], v206 offset:4096\n" \
"v_mfma_f32_16x16x32_bf16 v[100:103], a[92:95], a[220:223], v[100:103]\n" \
"ds_read_b64_tr_b16 a[114:115], v210 offset:1024\n" \
"v_fma_f32 v244, v158, v159, v244\n" \
"v_mfma_f32_16x16x32_bf16 v[60:63], a[200:203], a[88:91], v[60:63]\n" \
"s_add_u32 m0, s85, 81920\n" \
"ds_read_b64_tr_b16 a[116:117], v210 offset:12288\n" \
"v_cvt_pk_bf16_f32 v155, v158, v159\n" \
"v_mfma_f32_16x16x32_bf16 v[60:63], a[204:207], a[92:95], v[60:63]\n" \
"buffer_store_dwordx4 v[152:155], v233, s[56:59], s78 offen offset:2048 sc1\n" \
"buffer_load_dwordx4 v246, s[12:15], s46 offen lds\n" \
"v_cmp_class_f32 vcc, v244, s94\n" \
"s_sub_u32 s78, s78, 4096\n" \
"v_mfma_f32_16x16x32_bf16 v[68:71], a[200:203], a[192:195], v[68:71]\n" \
"s_add_u32 s46, s46, 0x100\n" \
"ds_read_b128 a[88:91], v205 offset:8192\n" \
"s_or_b64 s[92:93], s[92:93], vcc\n" \
"v_mfma_f32_16x16x32_bf16 v[68:71], a[204:207], a[196:199], v[68:71]\n" \
"ds_read_b128 a[92:95], v206 offset:8192\n" \
"v_mov_b32 v234, v245\n" \
"v_mfma_f32_16x16x32_bf16 v[108:111], a[192:195], a[216:219], v[108:111]\n" \
"s_add_u32 m0, s84, 90112\n" \
"ds_read_b64_tr_b16 a[118:119], v210 offset:13312\n" \
"v_mfma_f32_16x16x32_bf16 v[108:111], a[196:199], a[220:223], v[108:111]\n" \
"global_load_lds_dwordx4 v253, s[48:49] offset:-4096\n" \
"v_mfma_f32_16x16x32_bf16 v[104:107], a[192:195], a[208:211], v[104:107]\n" \
"v_permlane16_swap_b32 v234, v245\n" \
"v_mfma_f32_16x16x32_bf16 v[104:107], a[196:199], a[212:215], v[104:107]\n" \
"s_waitcnt lgkmcnt(2)\n" \
"v_add_f32 v245, v245, v234\n" \
"v_mov_b32 v234, v245\n" \
"v_mfma_f32_16x16x32_bf16 v[32:35], a[96:99], a[64:67], v[32:35]\n" \
"ds_read_b128 a[192:195], v205 offset:12288\n" \
"v_mfma_f32_16x16x32_bf16 v[32:35], a[100:103], a[68:71], v[32:35]\n" \
"global_load_lds_dwordx4 v253, s[48:49] offset:-2048\n" \
"v_permlane32_swap_b32 v234, v245\n" \
"v_mfma_f32_16x16x32_bf16 v[40:43], a[96:99], a[72:75], v[40:43]\n" \
"ds_read_b128 a[196:199], v206 offset:12288\n" \
"v_add_f32 v245, v245, v234\n" \
"v_mfma_f32_16x16x32_bf16 v[40:43], a[100:103], a[76:79], v[40:43]\n" \
"ds_read_b64_tr_b16 a[208:209], v209 offset:4096\n" \
"v_rcp_f32 v234, v245\n" \
"v_mfma_f32_16x16x32_bf16 v[0:3], a[104:107], a[72:75], v[0:3]\n" \
"ds_read_b64_tr_b16 a[210:211], v209 offset:5120\n" \
"v_mul_f32 v245, 0x4d800000, v245\n" \
"v_mfma_f32_16x16x32_bf16 v[0:3], a[108:111], a[76:79], v[0:3]\n" \
"s_waitcnt lgkmcnt(3)\n" \
"global_load_lds_dwordx4 v253, s[48:49] offset:0\n" \
"v_add_u32 v235, 0x1c90080, v249\n" \
"v_mfma_f32_16x16x32_bf16 v[4:7], a[112:115], a[72:75], v[4:7]\n" \
"s_add_u32 s48, s48, 0xc0000\n" \
"s_addc_u32 s49, s49, 0\n" \
"ds_read_b64_tr_b16 a[212:213], v209 offset:16384\n" \
"v_mfma_f32_16x16x32_bf16 v[4:7], a[116:119], a[76:79], v[4:7]\n" \
"ds_read_b64_tr_b16 a[214:215], v209 offset:17408\n" \
"v_cndmask_b32 v235, v249, v235, s[16:17]\n" \
"v_mfma_f32_16x16x32_bf16 v[12:15], a[112:115], a[80:83], v[12:15]\n" \
"s_add_u32 s78, s78, 532480\n" \
"ds_read_b64_tr_b16 a[216:217], v210 offset:4096\n" \
"v_mul_f32 v160, v160, v234\n" \
"v_mfma_f32_16x16x32_bf16 v[12:15], a[116:119], a[84:87], v[12:15]\n" \
"global_load_lds_dwordx4 v253, s[50:51] offset:-3072\n" \
"v_mul_f32 v161, v161, v234\n" \
"v_mfma_f32_16x16x32_bf16 v[8:11], a[104:107], a[80:83], v[8:11]\n" \
"ds_read_b64_tr_b16 a[218:219], v210 offset:5120\n" \
"v_mul_f32 v162, v162, v234\n" \
"v_mfma_f32_16x16x32_bf16 v[8:11], a[108:111], a[84:87], v[8:11]\n" \
"ds_read_b64_tr_b16 a[220:221], v210 offset:16384\n" \
"v_mul_f32 v163, v163, v234\n" \
"v_mfma_f32_16x16x32_bf16 v[48:51], a[96:99], a[80:83], v[48:51]\n" \
"ds_read_b64_tr_b16 a[222:223], v210 offset:17408\n" \
"v_mul_f32 v164, v164, v234\n" \
"v_mfma_f32_16x16x32_bf16 v[48:51], a[100:103], a[84:87], v[48:51]\n" \
"global_load_lds_dwordx4 v253, s[50:51] offset:-1024\n" \
"v_mul_f32 v165, v165, v234\n" \
"v_mfma_f32_16x16x32_bf16 v[56:59], a[96:99], a[88:91], v[56:59]\n" \
"ds_read_b64_tr_b16 a[200:201], v210 offset:2048\n" \
"v_mul_f32 v166, v166, v234\n" \
"v_mfma_f32_16x16x32_bf16 v[56:59], a[100:103], a[92:95], v[56:59]\n" \
"ds_read_b64_tr_b16 a[202:203], v210 offset:3072\n" \
"v_mul_f32 v167, v167, v234\n" \
"v_mfma_f32_16x16x32_bf16 v[16:19], a[104:107], a[88:91], v[16:19]\n" \
"ds_read_b64_tr_b16 a[204:205], v210 offset:14336\n" \
"v_fma_f32 v245, v160, v161, v245\n" \
"v_mfma_f32_16x16x32_bf16 v[16:19], a[108:111], a[92:95], v[16:19]\n" \
"global_load_lds_dwordx4 v253, s[50:51] offset:1024\n" \
"v_cvt_pk_bf16_f32 v160, v160, v161\n" \
"v_mfma_f32_16x16x32_bf16 v[20:23], a[112:115], a[88:91], v[20:23]\n" \
"s_add_u32 s50, s50, 0xc0000\n" \
"s_addc_u32 s51, s51, 0\n" \
"ds_read_b64_tr_b16 a[206:207], v210 offset:15360\n" \
"v_mfma_f32_16x16x32_bf16 v[20:23], a[116:119], a[92:95], v[20:23]\n" \
"s_waitcnt lgkmcnt(3)\n" \
"v_add_u32 v207, 0x1d000, v207\n" \
"v_add_u32 v208, 0x1d000, v208\n" \
"v_mfma_f32_16x16x32_bf16 v[28:31], a[112:115], a[192:195], v[28:31]\n" \
"v_add_u32 v205, 0x1d000, v205\n" \
"v_add_u32 v206, 0x1d000, v206\n" \
"v_mfma_f32_16x16x32_bf16 v[28:31], a[116:119], a[196:199], v[28:31]\n" \
"v_add_u32 v209, 0x1d000, v209\n" \
"v_add_u32 v210, 0x1d000, v210\n" \
"v_mfma_f32_16x16x32_bf16 v[24:27], a[104:107], a[192:195], v[24:27]\n" \
"v_fma_f32 v245, v162, v163, v245\n" \
"v_cvt_pk_bf16_f32 v161, v162, v163\n" \
"v_mfma_f32_16x16x32_bf16 v[24:27], a[108:111], a[196:199], v[24:27]\n" \
"s_waitcnt vmcnt(13) lgkmcnt(0)\n" \
"v_fma_f32 v245, v164, v165, v245\n" \
"v_cvt_pk_bf16_f32 v162, v164, v165\n" \
"v_mfma_f32_16x16x32_bf16 v[64:67], a[96:99], a[192:195], v[64:67]\n" \
"v_fma_f32 v245, v166, v167, v245\n" \
"v_cvt_pk_bf16_f32 v163, v166, v167\n" \
"v_mfma_f32_16x16x32_bf16 v[64:67], a[100:103], a[196:199], v[64:67]\n" \
"s_barrier\n" \
"buffer_store_dwordx4 v[160:163], v235, s[56:59], s78 offen offset:0 sc1\n" \
"v_mul_f32 v168, v168, v234\n" \
"v_mul_f32 v169, v169, v234\n" \
"v_mul_f32 v170, v170, v234\n" \
"v_mul_f32 v171, v171, v234\n" \
"v_mfma_f32_16x16x32_bf16 v[72:75], a[64:67], a[208:211], v[72:75]\n" \
"ds_read_b64_tr_b16 a[96:97], v209 offset:2048\n" \
"v_mul_f32 v172, v172, v234\n" \
"v_mfma_f32_16x16x32_bf16 v[72:75], a[68:71], a[212:215], v[72:75]\n" \
"ds_read_b64_tr_b16 a[98:99], v209 offset:3072\n" \
"v_mul_f32 v173, v173, v234\n" \
"v_mfma_f32_16x16x32_bf16 v[80:83], a[72:75], a[208:211], v[80:83]\n" \
"s_add_u32 m0, s95, 0\n" \
"ds_read_b128 a[64:67], v207 offset:0\n" \
"ds_read_b64_tr_b16 a[100:101], v209 offset:14336\n" \
"v_mfma_f32_16x16x32_bf16 v[80:83], a[76:79], a[212:215], v[80:83]\n" \
"global_load_lds_dwordx4 v252, s[42:43] offset:0\n" \
"v_mul_f32 v174, v174, v234\n" \
"v_mfma_f32_16x16x32_bf16 v[84:87], a[72:75], a[216:219], v[84:87]\n" \
"ds_read_b128 a[68:71], v208 offset:0\n" \
"ds_read_b64_tr_b16 a[102:103], v209 offset:15360\n" \
"v_mfma_f32_16x16x32_bf16 v[84:87], a[76:79], a[220:223], v[84:87]\n" \
"ds_read_b64_tr_b16 a[104:105], v209 offset:0\n" \
"v_mul_f32 v175, v175, v234\n" \
"v_mfma_f32_16x16x32_bf16 v[44:47], a[200:203], a[72:75], v[44:47]\n" \
"ds_read_b64_tr_b16 a[106:107], v209 offset:1024\n" \
"v_fma_f32 v245, v168, v169, v245\n" \
"v_mfma_f32_16x16x32_bf16 v[44:47], a[204:207], a[76:79], v[44:47]\n" \
"global_load_lds_dwordx4 v252, s[42:43] offset:2048\n" \
"v_cvt_pk_bf16_f32 v168, v168, v169\n" \
"v_mfma_f32_16x16x32_bf16 v[52:55], a[200:203], a[80:83], v[52:55]\n" \
"s_add_u32 s42, s42, 0x80\n" \
"s_addc_u32 s43, s43, 0\n" \
"ds_read_b128 a[72:75], v205 offset:0\n" \
"v_mfma_f32_16x16x32_bf16 v[52:55], a[204:207], a[84:87], v[52:55]\n" \
"ds_read_b128 a[76:79], v206 offset:0\n" \
"v_fma_f32 v245, v170, v171, v245\n" \
"v_mfma_f32_16x16x32_bf16 v[92:95], a[80:83], a[216:219], v[92:95]\n" \
"ds_read_b64_tr_b16 a[108:109], v209 offset:12288\n" \
"v_cvt_pk_bf16_f32 v169, v170, v171\n" \
"v_mfma_f32_16x16x32_bf16 v[92:95], a[84:87], a[220:223], v[92:95]\n" \
"global_load_lds_dwordx4 v252, s[44:45] offset:1024\n" \
"v_fma_f32 v245, v172, v173, v245\n" \
"v_mfma_f32_16x16x32_bf16 v[88:91], a[80:83], a[208:211], v[88:91]\n" \
"ds_read_b64_tr_b16 a[110:111], v209 offset:13312\n" \
"v_cvt_pk_bf16_f32 v170, v172, v173\n" \
"v_mfma_f32_16x16x32_bf16 v[88:91], a[84:87], a[212:215], v[88:91]\n" \
"ds_read_b64_tr_b16 a[112:113], v210 offset:0\n" \
"v_fma_f32 v245, v174, v175, v245\n" \
"v_mfma_f32_16x16x32_bf16 v[96:99], a[88:91], a[208:211], v[96:99]\n" \
"ds_read_b128 a[80:83], v205 offset:4096\n" \
"v_cvt_pk_bf16_f32 v171, v174, v175\n" \
"v_mfma_f32_16x16x32_bf16 v[96:99], a[92:95], a[212:215], v[96:99]\n" \
"buffer_store_dwordx4 v[168:171], v235, s[56:59], s78 offen offset:2048 sc1\n" \
"global_load_lds_dwordx4 v252, s[44:45] offset:3072\n" \
"s_add_u32 s78, s78, 0x1000\n" \
"v_mul_f32 v176, v176, v234\n" \
"v_mul_f32 v177, v177, v234\n" \
"v_mfma_f32_16x16x32_bf16 v[100:103], a[88:91], a[216:219], v[100:103]\n" \
"s_add_u32 s44, s44, 0x80\n" \
"s_addc_u32 s45, s45, 0\n" \
"ds_read_b128 a[84:87], v206 offset:4096\n" \
"v_mfma_f32_16x16x32_bf16 v[100:103], a[92:95], a[220:223], v[100:103]\n" \
"ds_read_b64_tr_b16 a[114:115], v210 offset:1024\n" \
"v_mul_f32 v178, v178, v234\n" \
"v_mfma_f32_16x16x32_bf16 v[60:63], a[200:203], a[88:91], v[60:63]\n" \
"s_add_u32 m0, s85, 16384\n" \
"ds_read_b64_tr_b16 a[116:117], v210 offset:12288\n" \
"v_mul_f32 v179, v179, v234\n" \
"v_mfma_f32_16x16x32_bf16 v[60:63], a[204:207], a[92:95], v[60:63]\n" \
"buffer_load_dwordx4 v246, s[12:15], s46 offen lds\n" \
"v_mfma_f32_16x16x32_bf16 v[68:71], a[200:203], a[192:195], v[68:71]\n" \
"s_add_u32 s46, s46, 0x80\n" \
"ds_read_b128 a[88:91], v205 offset:8192\n" \
"v_mul_f32 v180, v180, v234\n" \
"v_mfma_f32_16x16x32_bf16 v[68:71], a[204:207], a[196:199], v[68:71]\n" \
"ds_read_b128 a[92:95], v206 offset:8192\n" \
"v_mul_f32 v181, v181, v234\n" \
"v_mfma_f32_16x16x32_bf16 v[108:111], a[192:195], a[216:219], v[108:111]\n" \
"s_add_u32 m0, s84, 24576\n" \
"ds_read_b64_tr_b16 a[118:119], v210 offset:13312\n" \
"v_mul_f32 v182, v182, v234\n" \
"v_mfma_f32_16x16x32_bf16 v[108:111], a[196:199], a[220:223], v[108:111]\n" \
"global_load_lds_dwordx4 v253, s[48:49] offset:-4096\n" \
"v_mul_f32 v183, v183, v234\n" \
"v_mfma_f32_16x16x32_bf16 v[104:107], a[192:195], a[208:211], v[104:107]\n" \
"v_fma_f32 v245, v176, v177, v245\n" \
"v_cvt_pk_bf16_f32 v176, v176, v177\n" \
"v_mfma_f32_16x16x32_bf16 v[104:107], a[196:199], a[212:215], v[104:107]\n" \
"s_waitcnt lgkmcnt(2)\n" \
"Lpg9189_%=:\n" \
"Lst1x3_%=:\n" \
"s_cbranch_execz Lpg11447_%=\n" \
"v_fma_f32 v245, v178, v179, v245\n" \
"v_cvt_pk_bf16_f32 v177, v178, v179\n" \
"v_mfma_f32_16x16x32_bf16 v[32:35], a[96:99], a[64:67], v[32:35]\n" \
"ds_read_b128 a[192:195], v205 offset:12288\n" \
"v_fma_f32 v245, v180, v181, v245\n" \
"v_mfma_f32_16x16x32_bf16 v[32:35], a[100:103], a[68:71], v[32:35]\n" \
"global_load_lds_dwordx4 v253, s[48:49] offset:-2048\n" \
"v_cvt_pk_bf16_f32 v178, v180, v181\n" \
"v_mfma_f32_16x16x32_bf16 v[40:43], a[96:99], a[72:75], v[40:43]\n" \
"ds_read_b128 a[196:199], v206 offset:12288\n" \
"v_fma_f32 v245, v182, v183, v245\n" \
"v_mfma_f32_16x16x32_bf16 v[40:43], a[100:103], a[76:79], v[40:43]\n" \
"ds_read_b64_tr_b16 a[208:209], v209 offset:4096\n" \
"v_cvt_pk_bf16_f32 v179, v182, v183\n" \
"v_mfma_f32_16x16x32_bf16 v[0:3], a[104:107], a[72:75], v[0:3]\n" \
"ds_read_b64_tr_b16 a[210:211], v209 offset:5120\n" \
"v_mfma_f32_16x16x32_bf16 v[0:3], a[108:111], a[76:79], v[0:3]\n" \
"s_waitcnt lgkmcnt(3)\n" \
"buffer_store_dwordx4 v[176:179], v235, s[56:59], s78 offen offset:0 sc1\n" \
"global_load_lds_dwordx4 v253, s[48:49] offset:0\n" \
"v_mul_f32 v184, v184, v234\n" \
"v_mul_f32 v185, v185, v234\n" \
"v_mul_f32 v186, v186, v234\n" \
"v_mfma_f32_16x16x32_bf16 v[4:7], a[112:115], a[72:75], v[4:7]\n" \
"s_add_u32 s48, s48, 0x60000\n" \
"s_addc_u32 s49, s49, 0\n" \
"ds_read_b64_tr_b16 a[212:213], v209 offset:16384\n" \
"v_mfma_f32_16x16x32_bf16 v[4:7], a[116:119], a[76:79], v[4:7]\n" \
"ds_read_b64_tr_b16 a[214:215], v209 offset:17408\n" \
"v_mul_f32 v187, v187, v234\n" \
"v_mfma_f32_16x16x32_bf16 v[12:15], a[112:115], a[80:83], v[12:15]\n" \
"ds_read_b64_tr_b16 a[216:217], v210 offset:4096\n" \
"v_mul_f32 v188, v188, v234\n" \
"v_mfma_f32_16x16x32_bf16 v[12:15], a[116:119], a[84:87], v[12:15]\n" \
"global_load_lds_dwordx4 v253, s[50:51] offset:-3072\n" \
"v_mul_f32 v189, v189, v234\n" \
"v_mfma_f32_16x16x32_bf16 v[8:11], a[104:107], a[80:83], v[8:11]\n" \
"ds_read_b64_tr_b16 a[218:219], v210 offset:5120\n" \
"v_mul_f32 v190, v190, v234\n" \
"v_mfma_f32_16x16x32_bf16 v[8:11], a[108:111], a[84:87], v[8:11]\n" \
"ds_read_b64_tr_b16 a[220:221], v210 offset:16384\n" \
"v_mul_f32 v191, v191, v234\n" \
"v_mfma_f32_16x16x32_bf16 v[48:51], a[96:99], a[80:83], v[48:51]\n" \
"ds_read_b64_tr_b16 a[222:223], v210 offset:17408\n" \
"v_fma_f32 v245, v184, v185, v245\n" \
"v_mfma_f32_16x16x32_bf16 v[48:51], a[100:103], a[84:87], v[48:51]\n" \
"global_load_lds_dwordx4 v253, s[50:51] offset:-1024\n" \
"v_cvt_pk_bf16_f32 v184, v184, v185\n" \
"v_mfma_f32_16x16x32_bf16 v[56:59], a[96:99], a[88:91], v[56:59]\n" \
"ds_read_b64_tr_b16 a[200:201], v210 offset:2048\n" \
"v_fma_f32 v245, v186, v187, v245\n" \
"v_mfma_f32_16x16x32_bf16 v[56:59], a[100:103], a[92:95], v[56:59]\n" \
"ds_read_b64_tr_b16 a[202:203], v210 offset:3072\n" \
"v_cvt_pk_bf16_f32 v185, v186, v187\n" \
"v_mfma_f32_16x16x32_bf16 v[16:19], a[104:107], a[88:91], v[16:19]\n" \
"ds_read_b64_tr_b16 a[204:205], v210 offset:14336\n" \
"v_fma_f32 v245, v188, v189, v245\n" \
"v_mfma_f32_16x16x32_bf16 v[16:19], a[108:111], a[92:95], v[16:19]\n" \
"global_load_lds_dwordx4 v253, s[50:51] offset:1024\n" \
"v_cvt_pk_bf16_f32 v186, v188, v189\n" \
"v_mfma_f32_16x16x32_bf16 v[20:23], a[112:115], a[88:91], v[20:23]\n" \
"s_add_u32 s50, s50, 0x60000\n" \
"s_addc_u32 s51, s51, 0\n" \
"ds_read_b64_tr_b16 a[206:207], v210 offset:15360\n" \
"v_mfma_f32_16x16x32_bf16 v[20:23], a[116:119], a[92:95], v[20:23]\n" \
"s_waitcnt lgkmcnt(3)\n" \
"v_add_u32 v207, 0xffff3000, v207\n" \
"v_add_u32 v208, 0xffff3000, v208\n" \
"v_mfma_f32_16x16x32_bf16 v[28:31], a[112:115], a[192:195], v[28:31]\n" \
"v_add_u32 v205, 0xffff3000, v205\n" \
"v_add_u32 v206, 0xffff3000, v206\n" \
"v_mfma_f32_16x16x32_bf16 v[28:31], a[116:119], a[196:199], v[28:31]\n" \
"v_add_u32 v209, 0xffff3000, v209\n" \
"v_add_u32 v210, 0xffff3000, v210\n" \
"v_mfma_f32_16x16x32_bf16 v[24:27], a[104:107], a[192:195], v[24:27]\n" \
"v_fma_f32 v245, v190, v191, v245\n" \
"v_cvt_pk_bf16_f32 v187, v190, v191\n" \
"v_mfma_f32_16x16x32_bf16 v[24:27], a[108:111], a[196:199], v[24:27]\n" \
"s_waitcnt vmcnt(14) lgkmcnt(0)\n" \
"buffer_store_dwordx4 v[184:187], v235, s[56:59], s78 offen offset:2048 sc1\n" \
"v_cmp_class_f32 vcc, v245, s94\n" \
"s_sub_u32 s78, s78, 536576\n" \
"v_mfma_f32_16x16x32_bf16 v[64:67], a[96:99], a[192:195], v[64:67]\n" \
"s_or_b64 s[92:93], s[92:93], vcc\n" \
"s_cbranch_scc1 Lexact_%=\n" \
"Lexr_%=:\n" \
"v_mfma_f32_16x16x32_bf16 v[64:67], a[100:103], a[196:199], v[64:67]\n" \
"s_barrier\n" \
"v_mfma_f32_16x16x32_bf16 v[72:75], a[64:67], a[208:211], v[72:75]\n" \
"ds_read_b64_tr_b16 a[96:97], v209 offset:2048\n" \
"v_mfma_f32_16x16x32_bf16 v[72:75], a[68:71], a[212:215], v[72:75]\n" \
"ds_read_b64_tr_b16 a[98:99], v209 offset:3072\n" \
"v_mfma_f32_16x16x32_bf16 v[80:83], a[72:75], a[208:211], v[80:83]\n" \
"s_add_u32 m0, s95, 118784\n" \
"ds_read_b128 a[64:67], v207 offset:0\n" \
"ds_read_b64_tr_b16 a[100:101], v209 offset:14336\n" \
"v_mfma_f32_16x16x32_bf16 v[80:83], a[76:79], a[212:215], v[80:83]\n" \
"global_load_lds_dwordx4 v252, s[42:43] offset:0\n" \
"v_mfma_f32_16x16x32_bf16 v[84:87], a[72:75], a[216:219], v[84:87]\n" \
"ds_read_b128 a[68:71], v208 offset:0\n" \
"ds_read_b64_tr_b16 a[102:103], v209 offset:15360\n" \
"v_mfma_f32_16x16x32_bf16 v[84:87], a[76:79], a[220:223], v[84:87]\n" \
"ds_read_b64_tr_b16 a[104:105], v209 offset:0\n" \
"v_mfma_f32_16x16x32_bf16 v[44:47], a[200:203], a[72:75], v[44:47]\n" \
"ds_read_b64_tr_b16 a[106:107], v209 offset:1024\n" \
"v_mfma_f32_16x16x32_bf16 v[44:47], a[204:207], a[76:79], v[44:47]\n" \
"global_load_lds_dwordx4 v252, s[42:43] offset:2048\n" \
"v_mfma_f32_16x16x32_bf16 v[52:55], a[200:203], a[80:83], v[52:55]\n" \
"s_add_u32 s42, s42, 0x80\n" \
"s_addc_u32 s43, s43, 0\n" \
"ds_read_b128 a[72:75], v205 offset:0\n" \
"v_mfma_f32_16x16x32_bf16 v[52:55], a[204:207], a[84:87], v[52:55]\n" \
"ds_read_b128 a[76:79], v206 offset:0\n" \
"v_mfma_f32_16x16x32_bf16 v[92:95], a[80:83], a[216:219], v[92:95]\n" \
"ds_read_b64_tr_b16 a[108:109], v209 offset:12288\n" \
"v_mfma_f32_16x16x32_bf16 v[92:95], a[84:87], a[220:223], v[92:95]\n" \
"global_load_lds_dwordx4 v252, s[44:45] offset:1024\n" \
"v_mfma_f32_16x16x32_bf16 v[88:91], a[80:83], a[208:211], v[88:91]\n" \
"ds_read_b64_tr_b16 a[110:111], v209 offset:13312\n" \
"v_mfma_f32_16x16x32_bf16 v[88:91], a[84:87], a[212:215], v[88:91]\n" \
"ds_read_b64_tr_b16 a[112:113], v210 offset:0\n" \
"v_mfma_f32_16x16x32_bf16 v[96:99], a[88:91], a[208:211], v[96:99]\n" \
"ds_read_b128 a[80:83], v205 offset:4096\n" \
"v_mfma_f32_16x16x32_bf16 v[96:99], a[92:95], a[212:215], v[96:99]\n" \
"global_load_lds_dwordx4 v252, s[44:45] offset:3072\n" \
"v_mfma_f32_16x16x32_bf16 v[100:103], a[88:91], a[216:219], v[100:103]\n" \
"s_add_u32 s44, s44, 0x80\n" \
"s_addc_u32 s45, s45, 0\n" \
"ds_read_b128 a[84:87], v206 offset:4096\n" \
"v_mfma_f32_16x16x32_bf16 v[100:103], a[92:95], a[220:223], v[100:103]\n" \
"ds_read_b64_tr_b16 a[114:115], v210 offset:1024\n" \
"v_mfma_f32_16x16x32_bf16 v[60:63], a[200:203], a[88:91], v[60:63]\n" \
"s_add_u32 m0, s85, 135168\n" \
"ds_read_b64_tr_b16 a[116:117], v210 offset:12288\n" \
"v_mfma_f32_16x16x32_bf16 v[60:63], a[204:207], a[92:95], v[60:63]\n" \
"buffer_load_dwordx4 v246, s[12:15], s46 offen lds\n" \
"v_mfma_f32_16x16x32_bf16 v[68:71], a[200:203], a[192:195], v[68:71]\n" \
"ds_read_b128 a[88:91], v205 offset:8192\n" \
"s_add_u32 s46, s46, 0x80\n" \
"v_mfma_f32_16x16x32_bf16 v[68:71], a[204:207], a[196:199], v[68:71]\n" \
"ds_read_b128 a[92:95], v206 offset:8192\n" \
"v_mfma_f32_16x16x32_bf16 v[108:111], a[192:195], a[216:219], v[108:111]\n" \
"s_add_u32 m0, s84, 143360\n" \
"ds_read_b64_tr_b16 a[118:119], v210 offset:13312\n" \
"v_mfma_f32_16x16x32_bf16 v[108:111], a[196:199], a[220:223], v[108:111]\n" \
"global_load_lds_dwordx4 v253, s[48:49] offset:-4096\n" \
"v_mfma_f32_16x16x32_bf16 v[104:107], a[192:195], a[208:211], v[104:107]\n" \
"v_mfma_f32_16x16x32_bf16 v[104:107], a[196:199], a[212:215], v[104:107]\n" \
"s_waitcnt lgkmcnt(2)\n" \
"v_mfma_f32_16x16x32_bf16 v[32:35], a[96:99], a[64:67], v[32:35]\n" \
"ds_read_b128 a[192:195], v205 offset:12288\n" \
"v_mfma_f32_16x16x32_bf16 v[32:35], a[100:103], a[68:71], v[32:35]\n" \
"global_load_lds_dwordx4 v253, s[48:49] offset:-2048\n" \
"v_mfma_f32_16x16x32_bf16 v[40:43], a[96:99], a[72:75], v[40:43]\n" \
"ds_read_b128 a[196:199], v206 offset:12288\n" \
"v_mfma_f32_16x16x32_bf16 v[40:43], a[100:103], a[76:79], v[40:43]\n" \
"ds_read_b64_tr_b16 a[208:209], v209 offset:4096\n" \
"v_mfma_f32_16x16x32_bf16 v[0:3], a[104:107], a[72:75], v[0:3]\n" \
"ds_read_b64_tr_b16 a[210:211], v209 offset:5120\n" \
"v_mfma_f32_16x16x32_bf16 v[0:3], a[108:111], a[76:79], v[0:3]\n" \
"s_waitcnt lgkmcnt(3)\n" \
"global_load_lds_dwordx4 v253, s[48:49] offset:0\n" \
"v_mfma_f32_16x16x32_bf16 v[4:7], a[112:115], a[72:75], v[4:7]\n" \
"s_add_u32 s48, s48, 0x60000\n" \
"s_addc_u32 s49, s49, 0\n" \
"ds_read_b64_tr_b16 a[212:213], v209 offset:16384\n" \
"v_mfma_f32_16x16x32_bf16 v[4:7], a[116:119], a[76:79], v[4:7]\n" \
"ds_read_b64_tr_b16 a[214:215], v209 offset:17408\n" \
"v_mfma_f32_16x16x32_bf16 v[12:15], a[112:115], a[80:83], v[12:15]\n" \
"ds_read_b64_tr_b16 a[216:217], v210 offset:4096\n" \
"v_mfma_f32_16x16x32_bf16 v[12:15], a[116:119], a[84:87], v[12:15]\n" \
"global_load_lds_dwordx4 v253, s[50:51] offset:-3072\n" \
"v_mfma_f32_16x16x32_bf16 v[8:11], a[104:107], a[80:83], v[8:11]\n" \
"ds_read_b64_tr_b16 a[218:219], v210 offset:5120\n" \
"v_mfma_f32_16x16x32_bf16 v[8:11], a[108:111], a[84:87], v[8:11]\n" \
"ds_read_b64_tr_b16 a[220:221], v210 offset:16384\n" \
"v_mfma_f32_16x16x32_bf16 v[48:51], a[96:99], a[80:83], v[48:51]\n" \
"ds_read_b64_tr_b16 a[222:223], v210 offset:17408\n" \
"v_mfma_f32_16x16x32_bf16 v[48:51], a[100:103], a[84:87], v[48:51]\n" \
"global_load_lds_dwordx4 v253, s[50:51] offset:-1024\n" \
"v_mfma_f32_16x16x32_bf16 v[56:59], a[96:99], a[88:91], v[56:59]\n" \
"ds_read_b64_tr_b16 a[200:201], v210 offset:2048\n" \
"v_mfma_f32_16x16x32_bf16 v[56:59], a[100:103], a[92:95], v[56:59]\n" \
"ds_read_b64_tr_b16 a[202:203], v210 offset:3072\n" \
"v_mfma_f32_16x16x32_bf16 v[16:19], a[104:107], a[88:91], v[16:19]\n" \
"ds_read_b64_tr_b16 a[204:205], v210 offset:14336\n" \
"v_mfma_f32_16x16x32_bf16 v[16:19], a[108:111], a[92:95], v[16:19]\n" \
"global_load_lds_dwordx4 v253, s[50:51] offset:1024\n" \
"v_mfma_f32_16x16x32_bf16 v[20:23], a[112:115], a[88:91], v[20:23]\n" \
"s_add_u32 s50, s50, 0x60000\n" \
"s_addc_u32 s51, s51, 0\n" \
"ds_read_b64_tr_b16 a[206:207], v210 offset:15360\n" \
"v_mfma_f32_16x16x32_bf16 v[20:23], a[116:119], a[92:95], v[20:23]\n" \
"s_waitcnt lgkmcnt(3)\n" \
"v_add_u32 v207, 0xffff0000, v207\n" \
"v_add_u32 v208, 0xffff0000, v208\n" \
"v_mfma_f32_16x16x32_bf16 v[28:31], a[112:115], a[192:195], v[28:31]\n" \
"v_add_u32 v205, 0xffff0000, v205\n" \
"v_add_u32 v206, 0xffff0000, v206\n" \
"v_mfma_f32_16x16x32_bf16 v[28:31], a[116:119], a[196:199], v[28:31]\n" \
"v_add_u32 v209, 0xffff0000, v209\n" \
"v_add_u32 v210, 0xffff0000, v210\n" \
"v_mfma_f32_16x16x32_bf16 v[24:27], a[104:107], a[192:195], v[24:27]\n" \
"v_mfma_f32_16x16x32_bf16 v[24:27], a[108:111], a[196:199], v[24:27]\n" \
"s_waitcnt vmcnt(11) lgkmcnt(0)\n" \
"s_lshl_b32 s70, s63, 7\n" \
"s_lshl_b32 s71, s62, 2\n" \
"v_mfma_f32_16x16x32_bf16 v[64:67], a[96:99], a[192:195], v[64:67]\n" \
"s_add_u32 s71, s71, 65589248\n" \
"s_add_u32 s72, s71, s70\n" \
"s_sub_u32 s71, s71, s70\n" \
"v_mfma_f32_16x16x32_bf16 v[64:67], a[100:103], a[196:199], v[64:67]\n" \
"s_barrier\n" \
"v_mfma_f32_16x16x32_bf16 v[72:75], a[64:67], a[208:211], v[72:75]\n" \
"s_add_u32 s71, s71, 1920\n" \
"ds_read_b64_tr_b16 a[96:97], v209 offset:2048\n" \
"v_mov_b32 v236, s60\n" \
"v_mfma_f32_16x16x32_bf16 v[72:75], a[68:71], a[212:215], v[72:75]\n" \
"ds_read_b64_tr_b16 a[98:99], v209 offset:3072\n" \
"v_lshlrev_b32 v237, 28, s61\n" \
"v_mfma_f32_16x16x32_bf16 v[80:83], a[72:75], a[208:211], v[80:83]\n" \
"s_add_u32 m0, s95, 65536\n" \
"ds_read_b128 a[64:67], v207 offset:0\n" \
"ds_read_b64_tr_b16 a[100:101], v209 offset:14336\n" \
"v_mfma_f32_16x16x32_bf16 v[80:83], a[76:79], a[212:215], v[80:83]\n" \
"buffer_store_dword v236, v237, s[56:59], s72 offen sc1\n" \
"global_load_lds_dwordx4 v252, s[42:43] offset:0\n" \
"v_mfma_f32_16x16x32_bf16 v[84:87], a[72:75], a[216:219], v[84:87]\n" \
"ds_read_b128 a[68:71], v208 offset:0\n" \
"ds_read_b64_tr_b16 a[102:103], v209 offset:15360\n" \
"v_mfma_f32_16x16x32_bf16 v[84:87], a[76:79], a[220:223], v[84:87]\n" \
"buffer_store_dword v236, v237, s[56:59], s71 offen sc1\n" \
"ds_read_b64_tr_b16 a[104:105], v209 offset:0\n" \
"v_mfma_f32_16x16x32_bf16 v[44:47], a[200:203], a[72:75], v[44:47]\n" \
"ds_read_b64_tr_b16 a[106:107], v209 offset:1024\n" \
"v_mfma_f32_16x16x32_bf16 v[44:47], a[204:207], a[76:79], v[44:47]\n" \
"global_load_lds_dwordx4 v252, s[42:43] offset:2048\n" \
"v_mfma_f32_16x16x32_bf16 v[52:55], a[200:203], a[80:83], v[52:55]\n" \
"s_add_u32 s42, s42, 0x80\n" \
"s_addc_u32 s43, s43, 0\n" \
"ds_read_b128 a[72:75], v205 offset:0\n" \
"v_mfma_f32_16x16x32_bf16 v[52:55], a[204:207], a[84:87], v[52:55]\n" \
"ds_read_b128 a[76:79], v206 offset:0\n" \
"v_mfma_f32_16x16x32_bf16 v[92:95], a[80:83], a[216:219], v[92:95]\n" \
"ds_read_b64_tr_b16 a[108:109], v209 offset:12288\n" \
"v_mfma_f32_16x16x32_bf16 v[92:95], a[84:87], a[220:223], v[92:95]\n" \
"global_load_lds_dwordx4 v252, s[44:45] offset:1024\n" \
"v_mfma_f32_16x16x32_bf16 v[88:91], a[80:83], a[208:211], v[88:91]\n" \
"ds_read_b64_tr_b16 a[110:111], v209 offset:13312\n" \
"v_mfma_f32_16x16x32_bf16 v[88:91], a[84:87], a[212:215], v[88:91]\n" \
"ds_read_b64_tr_b16 a[112:113], v210 offset:0\n" \
"v_mfma_f32_16x16x32_bf16 v[96:99], a[88:91], a[208:211], v[96:99]\n" \
"ds_read_b128 a[80:83], v205 offset:4096\n" \
"v_mfma_f32_16x16x32_bf16 v[96:99], a[92:95], a[212:215], v[96:99]\n" \
"global_load_lds_dwordx4 v252, s[44:45] offset:3072\n" \
"v_mfma_f32_16x16x32_bf16 v[100:103], a[88:91], a[216:219], v[100:103]\n" \
"s_add_u32 s44, s44, 0x80\n" \
"s_addc_u32 s45, s45, 0\n" \
"ds_read_b128 a[84:87], v206 offset:4096\n" \
"v_mfma_f32_16x16x32_bf16 v[100:103], a[92:95], a[220:223], v[100:103]\n" \
"ds_read_b64_tr_b16 a[114:115], v210 offset:1024\n" \
"v_mfma_f32_16x16x32_bf16 v[60:63], a[200:203], a[88:91], v[60:63]\n" \
"s_add_u32 m0, s85, 81920\n" \
"ds_read_b64_tr_b16 a[116:117], v210 offset:12288\n" \
"v_mfma_f32_16x16x32_bf16 v[60:63], a[204:207], a[92:95], v[60:63]\n" \
"buffer_load_dwordx4 v246, s[12:15], s46 offen lds\n" \
"v_mfma_f32_16x16x32_bf16 v[68:71], a[200:203], a[192:195], v[68:71]\n" \
"ds_read_b128 a[88:91], v205 offset:8192\n" \
"s_add_u32 s46, s46, 0x80\n" \
"v_mfma_f32_16x16x32_bf16 v[68:71], a[204:207], a[196:199], v[68:71]\n" \
"ds_read_b128 a[92:95], v206 offset:8192\n" \
"v_mfma_f32_16x16x32_bf16 v[108:111], a[192:195], a[216:219], v[108:111]\n" \
"s_add_u32 m0, s84, 90112\n" \
"ds_read_b64_tr_b16 a[118:119], v210 offset:13312\n" \
"v_mfma_f32_16x16x32_bf16 v[108:111], a[196:199], a[220:223], v[108:111]\n" \
"global_load_lds_dwordx4 v253, s[48:49] offset:-4096\n" \
"v_mfma_f32_16x16x32_bf16 v[104:107], a[192:195], a[208:211], v[104:107]\n" \
"v_mfma_f32_16x16x32_bf16 v[104:107], a[196:199], a[212:215], v[104:107]\n" \
"s_waitcnt lgkmcnt(2)\n" \
"v_mfma_f32_16x16x32_bf16 v[32:35], a[96:99], a[64:67], v[32:35]\n" \
"s_lshr_b32 s89, s22, 7\n" \
"ds_read_b128 a[192:195], v205 offset:12288\n" \
"s_sub_u32 s89, 3, s89\n" \
"v_mfma_f32_16x16x32_bf16 v[32:35], a[100:103], a[68:71], v[32:35]\n" \
"global_load_lds_dwordx4 v253, s[48:49] offset:-2048\n" \
"v_bfe_u32 v203, v248, 7, 2\n" \
"v_mfma_f32_16x16x32_bf16 v[40:43], a[96:99], a[72:75], v[40:43]\n" \
"s_and_b32 s89, s89, 7\n" \
"ds_read_b128 a[196:199], v206 offset:12288\n" \
"s_and_b32 s90, %1, 1\n" \
"v_mfma_f32_16x16x32_bf16 v[40:43], a[100:103], a[76:79], v[40:43]\n" \
"ds_read_b64_tr_b16 a[208:209], v209 offset:4096\n" \
"v_bfe_u32 v204, v248, 5, 2\n" \
"v_mfma_f32_16x16x32_bf16 v[0:3], a[104:107], a[72:75], v[0:3]\n" \
"s_lshl_b32 s90, s90, 3\n" \
"s_add_u32 s89, s89, s90\n" \
"ds_read_b64_tr_b16 a[210:211], v209 offset:5120\n" \
"v_mfma_f32_16x16x32_bf16 v[0:3], a[108:111], a[76:79], v[0:3]\n" \
"s_waitcnt lgkmcnt(3)\n" \
"global_load_lds_dwordx4 v253, s[48:49] offset:0\n" \
"v_min_u32 v204, 2, v204\n" \
"v_mfma_f32_16x16x32_bf16 v[4:7], a[112:115], a[72:75], v[4:7]\n" \
"s_add_u32 s48, s48, 0x60000\n" \
"s_addc_u32 s49, s49, 0\n" \
"ds_read_b64_tr_b16 a[212:213], v209 offset:16384\n" \
"v_mfma_f32_16x16x32_bf16 v[4:7], a[116:119], a[76:79], v[4:7]\n" \
"s_lshl_b32 s89, s89, 7\n" \
"s_lshr_b32 s90, %1, 3\n" \
"ds_read_b64_tr_b16 a[214:215], v209 offset:17408\n" \
"v_mfma_f32_16x16x32_bf16 v[12:15], a[112:115], a[80:83], v[12:15]\n" \
"ds_read_b64_tr_b16 a[216:217], v210 offset:4096\n" \
"v_mad_u32_u24 v203, v203, 3, v204\n" \
"v_mfma_f32_16x16x32_bf16 v[12:15], a[116:119], a[84:87], v[12:15]\n" \
"s_lshl_b32 s90, s90, 2\n" \
"global_load_lds_dwordx4 v253, s[50:51] offset:-3072\n" \
"v_mfma_f32_16x16x32_bf16 v[8:11], a[104:107], a[80:83], v[8:11]\n" \
"s_add_u32 s90, s90, s61\n" \
"ds_read_b64_tr_b16 a[218:219], v210 offset:5120\n" \
"v_and_b32 v204, 16, v248\n" \
"v_mfma_f32_16x16x32_bf16 v[8:11], a[108:111], a[84:87], v[8:11]\n" \
"s_add_u32 s90, s90, 128\n" \
"s_mul_i32 s90, s90, 24576\n" \
"ds_read_b64_tr_b16 a[220:221], v210 offset:16384\n" \
"v_mfma_f32_16x16x32_bf16 v[48:51], a[96:99], a[80:83], v[48:51]\n" \
"s_add_u32 s89, s89, s90\n" \
"ds_read_b64_tr_b16 a[222:223], v210 offset:17408\n" \
"v_lshl_add_u32 v204, v204, 2, s89\n" \
"v_mfma_f32_16x16x32_bf16 v[48:51], a[100:103], a[84:87], v[48:51]\n" \
"global_load_lds_dwordx4 v253, s[50:51] offset:-1024\n" \
"v_lshl_add_u32 v203, v203, 11, v204\n" \
"v_mfma_f32_16x16x32_bf16 v[56:59], a[96:99], a[88:91], v[56:59]\n" \
"ds_read_b64_tr_b16 a[200:201], v210 offset:2048\n" \
"v_mfma_f32_16x16x32_bf16 v[56:59], a[100:103], a[92:95], v[56:59]\n" \
"ds_read_b64_tr_b16 a[202:203], v210 offset:3072\n" \
"v_mfma_f32_16x16x32_bf16 v[16:19], a[104:107], a[88:91], v[16:19]\n" \
"ds_read_b64_tr_b16 a[204:205], v210 offset:14336\n" \
"v_mfma_f32_16x16x32_bf16 v[16:19], a[108:111], a[92:95], v[16:19]\n" \
"global_load_lds_dwordx4 v253, s[50:51] offset:1024\n" \
"v_mfma_f32_16x16x32_bf16 v[20:23], a[112:115], a[88:91], v[20:23]\n" \
"s_add_u32 s50, s50, 0x60000\n" \
"s_addc_u32 s51, s51, 0\n" \
"ds_read_b64_tr_b16 a[206:207], v210 offset:15360\n" \
"v_mfma_f32_16x16x32_bf16 v[20:23], a[116:119], a[92:95], v[20:23]\n" \
"s_waitcnt lgkmcnt(3)\n" \
"v_add_u32 v207, 0x1d000, v207\n" \
"v_add_u32 v208, 0x1d000, v208\n" \
"v_mfma_f32_16x16x32_bf16 v[28:31], a[112:115], a[192:195], v[28:31]\n" \
"v_add_u32 v205, 0x1d000, v205\n" \
"v_add_u32 v206, 0x1d000, v206\n" \
"v_mfma_f32_16x16x32_bf16 v[28:31], a[116:119], a[196:199], v[28:31]\n" \
"v_add_u32 v209, 0x1d000, v209\n" \
"v_add_u32 v210, 0x1d000, v210\n" \
"v_mfma_f32_16x16x32_bf16 v[24:27], a[104:107], a[192:195], v[24:27]\n" \
"v_mfma_f32_16x16x32_bf16 v[24:27], a[108:111], a[196:199], v[24:27]\n" \
"s_waitcnt vmcnt(13) lgkmcnt(0)\n" \
"v_mfma_f32_16x16x32_bf16 v[64:67], a[96:99], a[192:195], v[64:67]\n" \
"v_mfma_f32_16x16x32_bf16 v[64:67], a[100:103], a[196:199], v[64:67]\n" \
"s_barrier\n" \
"v_mfma_f32_16x16x32_bf16 v[72:75], a[64:67], a[208:211], v[72:75]\n" \
"ds_read_b64_tr_b16 a[96:97], v209 offset:2048\n" \
"v_mfma_f32_16x16x32_bf16 v[72:75], a[68:71], a[212:215], v[72:75]\n" \
"ds_read_b64_tr_b16 a[98:99], v209 offset:3072\n" \
"v_mfma_f32_16x16x32_bf16 v[80:83], a[72:75], a[208:211], v[80:83]\n" \
"s_add_u32 m0, s95, 0\n" \
"ds_read_b128 a[64:67], v207 offset:0\n" \
"ds_read_b64_tr_b16 a[100:101], v209 offset:14336\n" \
"v_mfma_f32_16x16x32_bf16 v[80:83], a[76:79], a[212:215], v[80:83]\n" \
"global_load_lds_dwordx4 v252, s[42:43] offset:0\n" \
"v_mfma_f32_16x16x32_bf16 v[84:87], a[72:75], a[216:219], v[84:87]\n" \
"ds_read_b128 a[68:71], v208 offset:0\n" \
"ds_read_b64_tr_b16 a[102:103], v209 offset:15360\n" \
"v_mfma_f32_16x16x32_bf16 v[84:87], a[76:79], a[220:223], v[84:87]\n" \
"ds_read_b64_tr_b16 a[104:105], v209 offset:0\n" \
"v_mfma_f32_16x16x32_bf16 v[44:47], a[200:203], a[72:75], v[44:47]\n" \
"ds_read_b64_tr_b16 a[106:107], v209 offset:1024\n" \
"v_mfma_f32_16x16x32_bf16 v[44:47], a[204:207], a[76:79], v[44:47]\n" \
"global_load_lds_dwordx4 v252, s[42:43] offset:2048\n" \
"v_mfma_f32_16x16x32_bf16 v[52:55], a[200:203], a[80:83], v[52:55]\n" \
"s_add_u32 s42, s42, 0x80\n" \
"s_addc_u32 s43, s43, 0\n" \
"ds_read_b128 a[72:75], v205 offset:0\n" \
"v_mfma_f32_16x16x32_bf16 v[52:55], a[204:207], a[84:87], v[52:55]\n" \
"ds_read_b128 a[76:79], v206 offset:0\n" \
"v_mfma_f32_16x16x32_bf16 v[92:95], a[80:83], a[216:219], v[92:95]\n" \
"ds_read_b64_tr_b16 a[108:109], v209 offset:12288\n" \
"v_mfma_f32_16x16x32_bf16 v[92:95], a[84:87], a[220:223], v[92:95]\n" \
"global_load_lds_dwordx4 v252, s[44:45] offset:1024\n" \
"v_mfma_f32_16x16x32_bf16 v[88:91], a[80:83], a[208:211], v[88:91]\n" \
"ds_read_b64_tr_b16 a[110:111], v209 offset:13312\n" \
"v_mfma_f32_16x16x32_bf16 v[88:91], a[84:87], a[212:215], v[88:91]\n" \
"ds_read_b64_tr_b16 a[112:113], v210 offset:0\n" \
"v_mfma_f32_16x16x32_bf16 v[96:99], a[88:91], a[208:211], v[96:99]\n" \
"ds_read_b128 a[80:83], v205 offset:4096\n" \
"v_mfma_f32_16x16x32_bf16 v[96:99], a[92:95], a[212:215], v[96:99]\n" \
"global_load_lds_dwordx4 v252, s[44:45] offset:3072\n" \
"v_mfma_f32_16x16x32_bf16 v[100:103], a[88:91], a[216:219], v[100:103]\n" \
"s_add_u32 s44, s44, 0x80\n" \
"s_addc_u32 s45, s45, 0\n" \
"ds_read_b128 a[84:87], v206 offset:4096\n" \
"v_mfma_f32_16x16x32_bf16 v[100:103], a[92:95], a[220:223], v[100:103]\n" \
"ds_read_b64_tr_b16 a[114:115], v210 offset:1024\n" \
"v_mfma_f32_16x16x32_bf16 v[60:63], a[200:203], a[88:91], v[60:63]\n" \
"s_add_u32 m0, s85, 16384\n" \
"ds_read_b64_tr_b16 a[116:117], v210 offset:12288\n" \
"v_mfma_f32_16x16x32_bf16 v[60:63], a[204:207], a[92:95], v[60:63]\n" \
"buffer_load_dwordx4 v246, s[12:15], s46 offen lds\n" \
"v_mfma_f32_16x16x32_bf16 v[68:71], a[200:203], a[192:195], v[68:71]\n" \
"ds_read_b128 a[88:91], v205 offset:8192\n" \
"s_add_u32 s46, s46, 0x80\n" \
"v_mfma_f32_16x16x32_bf16 v[68:71], a[204:207], a[196:199], v[68:71]\n" \
"ds_read_b128 a[92:95], v206 offset:8192\n" \
"v_mfma_f32_16x16x32_bf16 v[108:111], a[192:195], a[216:219], v[108:111]\n" \
"s_add_u32 m0, s84, 24576\n" \
"ds_read_b64_tr_b16 a[118:119], v210 offset:13312\n" \
"v_mfma_f32_16x16x32_bf16 v[108:111], a[196:199], a[220:223], v[108:111]\n" \
"global_load_lds_dwordx4 v253, s[48:49] offset:-4096\n" \
"v_mfma_f32_16x16x32_bf16 v[104:107], a[192:195], a[208:211], v[104:107]\n" \
"v_mfma_f32_16x16x32_bf16 v[104:107], a[196:199], a[212:215], v[104:107]\n" \
"s_waitcnt lgkmcnt(2)\n" \
"v_mfma_f32_16x16x32_bf16 v[32:35], a[96:99], a[64:67], v[32:35]\n" \
"s_mov_b32 s30, 65593856\n" \
"ds_read_b128 a[192:195], v205 offset:12288\n" \
"v_lshrrev_b32 v228, 1, v248\n" \
"v_mfma_f32_16x16x32_bf16 v[32:35], a[100:103], a[68:71], v[32:35]\n" \
"global_load_lds_dwordx4 v253, s[48:49] offset:-2048\n" \
"v_mfma_f32_16x16x32_bf16 v[40:43], a[96:99], a[72:75], v[40:43]\n" \
"ds_read_b128 a[196:199], v206 offset:12288\n" \
"v_mfma_f32_16x16x32_bf16 v[40:43], a[100:103], a[76:79], v[40:43]\n" \
"ds_read_b64_tr_b16 a[208:209], v209 offset:4096\n" \
"v_mfma_f32_16x16x32_bf16 v[0:3], a[104:107], a[72:75], v[0:3]\n" \
"ds_read_b64_tr_b16 a[210:211], v209 offset:5120\n" \
"v_mfma_f32_16x16x32_bf16 v[0:3], a[108:111], a[76:79], v[0:3]\n" \
"s_waitcnt lgkmcnt(3)\n" \
"global_load_lds_dwordx4 v253, s[48:49] offset:0\n" \
"v_mfma_f32_16x16x32_bf16 v[4:7], a[112:115], a[72:75], v[4:7]\n" \
"s_add_u32 s48, s48, 0x60000\n" \
"s_addc_u32 s49, s49, 0\n" \
"ds_read_b64_tr_b16 a[212:213], v209 offset:16384\n" \
"v_mfma_f32_16x16x32_bf16 v[4:7], a[116:119], a[76:79], v[4:7]\n" \
"ds_read_b64_tr_b16 a[214:215], v209 offset:17408\n" \
"v_mfma_f32_16x16x32_bf16 v[12:15], a[112:115], a[80:83], v[12:15]\n" \
"ds_read_b64_tr_b16 a[216:217], v210 offset:4096\n" \
"v_mfma_f32_16x16x32_bf16 v[12:15], a[116:119], a[84:87], v[12:15]\n" \
"global_load_lds_dwordx4 v253, s[50:51] offset:-3072\n" \
"v_mfma_f32_16x16x32_bf16 v[8:11], a[104:107], a[80:83], v[8:11]\n" \
"ds_read_b64_tr_b16 a[218:219], v210 offset:5120\n" \
"v_mfma_f32_16x16x32_bf16 v[8:11], a[108:111], a[84:87], v[8:11]\n" \
"ds_read_b64_tr_b16 a[220:221], v210 offset:16384\n" \
"v_mfma_f32_16x16x32_bf16 v[48:51], a[96:99], a[80:83], v[48:51]\n" \
"ds_read_b64_tr_b16 a[222:223], v210 offset:17408\n" \
"v_mfma_f32_16x16x32_bf16 v[48:51], a[100:103], a[84:87], v[48:51]\n" \
"global_load_lds_dwordx4 v253, s[50:51] offset:-1024\n" \
"v_mfma_f32_16x16x32_bf16 v[56:59], a[96:99], a[88:91], v[56:59]\n" \
"ds_read_b64_tr_b16 a[200:201], v210 offset:2048\n" \
"v_mfma_f32_16x16x32_bf16 v[56:59], a[100:103], a[92:95], v[56:59]\n" \
"ds_read_b64_tr_b16 a[202:203], v210 offset:3072\n" \
"v_mfma_f32_16x16x32_bf16 v[16:19], a[104:107], a[88:91], v[16:19]\n" \
"ds_read_b64_tr_b16 a[204:205], v210 offset:14336\n" \
"v_mfma_f32_16x16x32_bf16 v[16:19], a[108:111], a[92:95], v[16:19]\n" \
"global_load_lds_dwordx4 v253, s[50:51] offset:1024\n" \
"v_mfma_f32_16x16x32_bf16 v[20:23], a[112:115], a[88:91], v[20:23]\n" \
"s_add_u32 s50, s50, 0x60000\n" \
"s_addc_u32 s51, s51, 0\n" \
"ds_read_b64_tr_b16 a[206:207], v210 offset:15360\n" \
"v_mfma_f32_16x16x32_bf16 v[20:23], a[116:119], a[92:95], v[20:23]\n" \
"s_waitcnt lgkmcnt(3)\n" \
"v_add_u32 v207, 0xffff3000, v207\n" \
"v_add_u32 v208, 0xffff3000, v208\n" \
"v_mfma_f32_16x16x32_bf16 v[28:31], a[112:115], a[192:195], v[28:31]\n" \
"v_add_u32 v205, 0xffff3000, v205\n" \
"v_add_u32 v206, 0xffff3000, v206\n" \
"v_mfma_f32_16x16x32_bf16 v[28:31], a[116:119], a[196:199], v[28:31]\n" \
"global_load_dword v204, v203, s[22:23]\n" \
"v_mfma_f32_16x16x32_bf16 v[24:27], a[104:107], a[192:195], v[24:27]\n" \
"v_add_u32 v209, 0xffff3000, v209\n" \
"v_add_u32 v210, 0xffff3000, v210\n" \
"v_mfma_f32_16x16x32_bf16 v[24:27], a[108:111], a[196:199], v[24:27]\n" \
"s_waitcnt vmcnt(12) lgkmcnt(0)\n" \
"v_mfma_f32_16x16x32_bf16 v[64:67], a[96:99], a[192:195], v[64:67]\n" \
"v_mfma_f32_16x16x32_bf16 v[64:67], a[100:103], a[196:199], v[64:67]\n" \
"s_barrier\n" \
"v_mfma_f32_16x16x32_bf16 v[72:75], a[64:67], a[208:211], v[72:75]\n" \
"ds_read_b64_tr_b16 a[96:97], v209 offset:2048\n" \
"v_mfma_f32_16x16x32_bf16 v[72:75], a[68:71], a[212:215], v[72:75]\n" \
"ds_read_b64_tr_b16 a[98:99], v209 offset:3072\n" \
"v_mfma_f32_16x16x32_bf16 v[80:83], a[72:75], a[208:211], v[80:83]\n" \
"s_add_u32 m0, s95, 118784\n" \
"ds_read_b128 a[64:67], v207 offset:0\n" \
"ds_read_b64_tr_b16 a[100:101], v209 offset:14336\n" \
"v_mfma_f32_16x16x32_bf16 v[80:83], a[76:79], a[212:215], v[80:83]\n" \
"global_load_lds_dwordx4 v252, s[42:43] offset:0\n" \
"v_mfma_f32_16x16x32_bf16 v[84:87], a[72:75], a[216:219], v[84:87]\n" \
"ds_read_b128 a[68:71], v208 offset:0\n" \
"ds_read_b64_tr_b16 a[102:103], v209 offset:15360\n" \
"v_mfma_f32_16x16x32_bf16 v[84:87], a[76:79], a[220:223], v[84:87]\n" \
"ds_read_b64_tr_b16 a[104:105], v209 offset:0\n" \
"v_mfma_f32_16x16x32_bf16 v[44:47], a[200:203], a[72:75], v[44:47]\n" \
"ds_read_b64_tr_b16 a[106:107], v209 offset:1024\n" \
"v_mfma_f32_16x16x32_bf16 v[44:47], a[204:207], a[76:79], v[44:47]\n" \
"global_load_lds_dwordx4 v252, s[42:43] offset:2048\n" \
"v_mfma_f32_16x16x32_bf16 v[52:55], a[200:203], a[80:83], v[52:55]\n" \
"s_add_u32 s42, s42, 0x80\n" \
"s_addc_u32 s43, s43, 0\n" \
"ds_read_b128 a[72:75], v205 offset:0\n" \
"v_mfma_f32_16x16x32_bf16 v[52:55], a[204:207], a[84:87], v[52:55]\n" \
"ds_read_b128 a[76:79], v206 offset:0\n" \
"v_mfma_f32_16x16x32_bf16 v[92:95], a[80:83], a[216:219], v[92:95]\n" \
"ds_read_b64_tr_b16 a[108:109], v209 offset:12288\n" \
"v_mfma_f32_16x16x32_bf16 v[92:95], a[84:87], a[220:223], v[92:95]\n" \
"global_load_lds_dwordx4 v252, s[44:45] offset:1024\n" \
"v_mfma_f32_16x16x32_bf16 v[88:91], a[80:83], a[208:211], v[88:91]\n" \
"ds_read_b64_tr_b16 a[110:111], v209 offset:13312\n" \
"v_mfma_f32_16x16x32_bf16 v[88:91], a[84:87], a[212:215], v[88:91]\n" \
"ds_read_b64_tr_b16 a[112:113], v210 offset:0\n" \
"v_mfma_f32_16x16x32_bf16 v[96:99], a[88:91], a[208:211], v[96:99]\n" \
"ds_read_b128 a[80:83], v205 offset:4096\n" \
"v_mfma_f32_16x16x32_bf16 v[96:99], a[92:95], a[212:215], v[96:99]\n" \
"global_load_lds_dwordx4 v252, s[44:45] offset:3072\n" \
"v_mfma_f32_16x16x32_bf16 v[100:103], a[88:91], a[216:219], v[100:103]\n" \
"s_add_u32 s44, s44, 0x80\n" \
"s_addc_u32 s45, s45, 0\n" \
"ds_read_b128 a[84:87], v206 offset:4096\n" \
"v_mfma_f32_16x16x32_bf16 v[100:103], a[92:95], a[220:223], v[100:103]\n" \
"ds_read_b64_tr_b16 a[114:115], v210 offset:1024\n" \
"v_mfma_f32_16x16x32_bf16 v[60:63], a[200:203], a[88:91], v[60:63]\n" \
"s_add_u32 m0, s85, 135168\n" \
"ds_read_b64_tr_b16 a[116:117], v210 offset:12288\n" \
"v_mfma_f32_16x16x32_bf16 v[60:63], a[204:207], a[92:95], v[60:63]\n" \
"buffer_load_dwordx4 v246, s[12:15], s46 offen lds\n" \
"v_mfma_f32_16x16x32_bf16 v[68:71], a[200:203], a[192:195], v[68:71]\n" \
"ds_read_b128 a[88:91], v205 offset:8192\n" \
"s_add_u32 s46, s46, 0x80\n" \
"v_mfma_f32_16x16x32_bf16 v[68:71], a[204:207], a[196:199], v[68:71]\n" \
"ds_read_b128 a[92:95], v206 offset:8192\n" \
"v_mfma_f32_16x16x32_bf16 v[108:111], a[192:195], a[216:219], v[108:111]\n" \
"s_add_u32 m0, s84, 143360\n" \
"ds_read_b64_tr_b16 a[118:119], v210 offset:13312\n" \
"v_mfma_f32_16x16x32_bf16 v[108:111], a[196:199], a[220:223], v[108:111]\n" \
"global_load_lds_dwordx4 v253, s[48:49] offset:-4096\n" \
"v_mfma_f32_16x16x32_bf16 v[104:107], a[192:195], a[208:211], v[104:107]\n" \
"v_mfma_f32_16x16x32_bf16 v[104:107], a[196:199], a[212:215], v[104:107]\n" \
"s_waitcnt lgkmcnt(2)\n" \
"v_mfma_f32_16x16x32_bf16 v[32:35], a[96:99], a[64:67], v[32:35]\n" \
"ds_read_b128 a[192:195], v205 offset:12288\n" \
"v_mfma_f32_16x16x32_bf16 v[32:35], a[100:103], a[68:71], v[32:35]\n" \
"global_load_lds_dwordx4 v253, s[48:49] offset:-2048\n" \
"Lpg9736_%=:\n" \
"s_cbranch_execz Lpg12014_%=\n" \
"v_mfma_f32_16x16x32_bf16 v[40:43], a[96:99], a[72:75], v[40:43]\n" \
"ds_read_b128 a[196:199], v206 offset:12288\n" \
"v_mfma_f32_16x16x32_bf16 v[40:43], a[100:103], a[76:79], v[40:43]\n" \
"ds_read_b64_tr_b16 a[208:209], v209 offset:4096\n" \
"v_mfma_f32_16x16x32_bf16 v[0:3], a[104:107], a[72:75], v[0:3]\n" \
"ds_read_b64_tr_b16 a[210:211], v209 offset:5120\n" \
"v_mfma_f32_16x16x32_bf16 v[0:3], a[108:111], a[76:79], v[0:3]\n" \
"s_waitcnt lgkmcnt(3)\n" \
"global_load_lds_dwordx4 v253, s[48:49] offset:0\n" \
"v_mfma_f32_16x16x32_bf16 v[4:7], a[112:115], a[72:75], v[4:7]\n" \
"s_add_u32 s48, s48, 0x60000\n" \
"s_addc_u32 s49, s49, 0\n" \
"ds_read_b64_tr_b16 a[212:213], v209 offset:16384\n" \
"v_mfma_f32_16x16x32_bf16 v[4:7], a[116:119], a[76:79], v[4:7]\n" \
"ds_read_b64_tr_b16 a[214:215], v209 offset:17408\n" \
"v_mfma_f32_16x16x32_bf16 v[12:15], a[112:115], a[80:83], v[12:15]\n" \
"ds_read_b64_tr_b16 a[216:217], v210 offset:4096\n" \
"v_mfma_f32_16x16x32_bf16 v[12:15], a[116:119], a[84:87], v[12:15]\n" \
"global_load_lds_dwordx4 v253, s[50:51] offset:-3072\n" \
"v_mfma_f32_16x16x32_bf16 v[8:11], a[104:107], a[80:83], v[8:11]\n" \
"ds_read_b64_tr_b16 a[218:219], v210 offset:5120\n" \
"v_mfma_f32_16x16x32_bf16 v[8:11], a[108:111], a[84:87], v[8:11]\n" \
"ds_read_b64_tr_b16 a[220:221], v210 offset:16384\n" \
"v_mfma_f32_16x16x32_bf16 v[48:51], a[96:99], a[80:83], v[48:51]\n" \
"ds_read_b64_tr_b16 a[222:223], v210 offset:17408\n" \
"v_mfma_f32_16x16x32_bf16 v[48:51], a[100:103], a[84:87], v[48:51]\n" \
"global_load_lds_dwordx4 v253, s[50:51] offset:-1024\n" \
"v_mfma_f32_16x16x32_bf16 v[56:59], a[96:99], a[88:91], v[56:59]\n" \
"ds_read_b64_tr_b16 a[200:201], v210 offset:2048\n" \
"v_mfma_f32_16x16x32_bf16 v[56:59], a[100:103], a[92:95], v[56:59]\n" \
"ds_read_b64_tr_b16 a[202:203], v210 offset:3072\n" \
"v_mfma_f32_16x16x32_bf16 v[16:19], a[104:107], a[88:91], v[16:19]\n" \
"ds_read_b64_tr_b16 a[204:205], v210 offset:14336\n" \
"v_mfma_f32_16x16x32_bf16 v[16:19], a[108:111], a[92:95], v[16:19]\n" \
"global_load_lds_dwordx4 v253, s[50:51] offset:1024\n" \
"v_mfma_f32_16x16x32_bf16 v[20:23], a[112:115], a[88:91], v[20:23]\n" \
"s_add_u32 s50, s50, 0x60000\n" \
"s_addc_u32 s51, s51, 0\n" \
"ds_read_b64_tr_b16 a[206:207], v210 offset:15360\n" \
"v_mfma_f32_16x16x32_bf16 v[20:23], a[116:119], a[92:95], v[20:23]\n" \
"s_waitcnt lgkmcnt(3)\n" \
"v_add_u32 v207, 0xffff0000, v207\n" \
"v_add_u32 v208, 0xffff0000, v208\n" \
"v_mfma_f32_16x16x32_bf16 v[28:31], a[112:115], a[192:195], v[28:31]\n" \
"v_add_u32 v205, 0xffff0000, v205\n" \
"v_add_u32 v206, 0xffff0000, v206\n" \
"v_mfma_f32_16x16x32_bf16 v[28:31], a[116:119], a[196:199], v[28:31]\n" \
"buffer_load_dwordx2 v[226:227], v228, s[56:59], s30 offen sc1\n" \
"v_mfma_f32_16x16x32_bf16 v[24:27], a[104:107], a[192:195], v[24:27]\n" \
"v_add_u32 v209, 0xffff0000, v209\n" \
"v_add_u32 v210, 0xffff0000, v210\n" \
"v_mfma_f32_16x16x32_bf16 v[24:27], a[108:111], a[196:199], v[24:27]\n" \
"s_waitcnt vmcnt(13) lgkmcnt(0)\n" \
"v_mfma_f32_16x16x32_bf16 v[64:67], a[96:99], a[192:195], v[64:67]\n" \
"v_mfma_f32_16x16x32_bf16 v[64:67], a[100:103], a[196:199], v[64:67]\n" \
"s_barrier\n" \
"v_mfma_f32_16x16x32_bf16 v[72:75], a[64:67], a[208:211], v[72:75]\n" \
"ds_read_b64_tr_b16 a[96:97], v209 offset:2048\n" \
"v_mfma_f32_16x16x32_bf16 v[72:75], a[68:71], a[212:215], v[72:75]\n" \
"ds_read_b64_tr_b16 a[98:99], v209 offset:3072\n" \
"v_mfma_f32_16x16x32_bf16 v[80:83], a[72:75], a[208:211], v[80:83]\n" \
"s_add_u32 m0, s95, 65536\n" \
"ds_read_b128 a[64:67], v207 offset:0\n" \
"ds_read_b64_tr_b16 a[100:101], v209 offset:14336\n" \
"v_mfma_f32_16x16x32_bf16 v[80:83], a[76:79], a[212:215], v[80:83]\n" \
"global_load_lds_dwordx4 v252, s[42:43] offset:0\n" \
"v_mfma_f32_16x16x32_bf16 v[84:87], a[72:75], a[216:219], v[84:87]\n" \
"ds_read_b128 a[68:71], v208 offset:0\n" \
"ds_read_b64_tr_b16 a[102:103], v209 offset:15360\n" \
"v_mfma_f32_16x16x32_bf16 v[84:87], a[76:79], a[220:223], v[84:87]\n" \
"ds_read_b64_tr_b16 a[104:105], v209 offset:0\n" \
"v_mfma_f32_16x16x32_bf16 v[44:47], a[200:203], a[72:75], v[44:47]\n" \
"ds_read_b64_tr_b16 a[106:107], v209 offset:1024\n" \
"v_mfma_f32_16x16x32_bf16 v[44:47], a[204:207], a[76:79], v[44:47]\n" \
"global_load_lds_dwordx4 v252, s[42:43] offset:2048\n" \
"v_mfma_f32_16x16x32_bf16 v[52:55], a[200:203], a[80:83], v[52:55]\n" \
"s_add_u32 s42, s42, 0x80\n" \
"s_addc_u32 s43, s43, 0\n" \
"ds_read_b128 a[72:75], v205 offset:0\n" \
"v_mfma_f32_16x16x32_bf16 v[52:55], a[204:207], a[84:87], v[52:55]\n" \
"ds_read_b128 a[76:79], v206 offset:0\n" \
"v_mfma_f32_16x16x32_bf16 v[92:95], a[80:83], a[216:219], v[92:95]\n" \
"ds_read_b64_tr_b16 a[108:109], v209 offset:12288\n" \
"v_mfma_f32_16x16x32_bf16 v[92:95], a[84:87], a[220:223], v[92:95]\n" \
"global_load_lds_dwordx4 v252, s[44:45] offset:1024\n" \
"v_mfma_f32_16x16x32_bf16 v[88:91], a[80:83], a[208:211], v[88:91]\n" \
"ds_read_b64_tr_b16 a[110:111], v209 offset:13312\n" \
"v_mfma_f32_16x16x32_bf16 v[88:91], a[84:87], a[212:215], v[88:91]\n" \
"ds_read_b64_tr_b16 a[112:113], v210 offset:0\n" \
"v_mfma_f32_16x16x32_bf16 v[96:99], a[88:91], a[208:211], v[96:99]\n" \
"ds_read_b128 a[80:83], v205 offset:4096\n" \
"v_mfma_f32_16x16x32_bf16 v[96:99], a[92:95], a[212:215], v[96:99]\n" \
"global_load_lds_dwordx4 v252, s[44:45] offset:3072\n" \
"v_mfma_f32_16x16x32_bf16 v[100:103], a[88:91], a[216:219], v[100:103]\n" \
"s_add_u32 s44, s44, 0x80\n" \
"s_addc_u32 s45, s45, 0\n" \
"ds_read_b128 a[84:87], v206 offset:4096\n" \
"v_mfma_f32_16x16x32_bf16 v[100:103], a[92:95], a[220:223], v[100:103]\n" \
"ds_read_b64_tr_b16 a[114:115], v210 offset:1024\n" \
"v_mfma_f32_16x16x32_bf16 v[60:63], a[200:203], a[88:91], v[60:63]\n" \
"s_add_u32 m0, s85, 81920\n" \
"ds_read_b64_tr_b16 a[116:117], v210 offset:12288\n" \
"v_mfma_f32_16x16x32_bf16 v[60:63], a[204:207], a[92:95], v[60:63]\n" \
"buffer_load_dwordx4 v246, s[12:15], s46 offen lds\n" \
"v_mfma_f32_16x16x32_bf16 v[68:71], a[200:203], a[192:195], v[68:71]\n" \
"ds_read_b128 a[88:91], v205 offset:8192\n" \
"s_add_u32 s46, s46, 0x80\n" \
"v_mfma_f32_16x16x32_bf16 v[68:71], a[204:207], a[196:199], v[68:71]\n" \
"ds_read_b128 a[92:95], v206 offset:8192\n" \
"v_mfma_f32_16x16x32_bf16 v[108:111], a[192:195], a[216:219], v[108:111]\n" \
"s_add_u32 m0, s84, 90112\n" \
"ds_read_b64_tr_b16 a[118:119], v210 offset:13312\n" \
"v_mfma_f32_16x16x32_bf16 v[108:111], a[196:199], a[220:223], v[108:111]\n" \
"global_load_lds_dwordx4 v253, s[48:49] offset:-4096\n" \
"v_mfma_f32_16x16x32_bf16 v[104:107], a[192:195], a[208:211], v[104:107]\n" \
"v_mfma_f32_16x16x32_bf16 v[104:107], a[196:199], a[212:215], v[104:107]\n" \
"s_waitcnt lgkmcnt(2)\n" \
"v_mfma_f32_16x16x32_bf16 v[32:35], a[96:99], a[64:67], v[32:35]\n" \
"ds_read_b128 a[192:195], v205 offset:12288\n" \
"v_mfma_f32_16x16x32_bf16 v[32:35], a[100:103], a[68:71], v[32:35]\n" \
"global_load_lds_dwordx4 v253, s[48:49] offset:-2048\n" \
"v_mfma_f32_16x16x32_bf16 v[40:43], a[96:99], a[72:75], v[40:43]\n" \
"ds_read_b128 a[196:199], v206 offset:12288\n" \
"v_mfma_f32_16x16x32_bf16 v[40:43], a[100:103], a[76:79], v[40:43]\n" \
"ds_read_b64_tr_b16 a[208:209], v209 offset:4096\n" \
"v_mfma_f32_16x16x32_bf16 v[0:3], a[104:107], a[72:75], v[0:3]\n" \
"ds_read_b64_tr_b16 a[210:211], v209 offset:5120\n" \
"v_mfma_f32_16x16x32_bf16 v[0:3], a[108:111], a[76:79], v[0:3]\n" \
"s_waitcnt lgkmcnt(3)\n" \
"global_load_lds_dwordx4 v253, s[48:49] offset:0\n" \
"v_mfma_f32_16x16x32_bf16 v[4:7], a[112:115], a[72:75], v[4:7]\n" \
"s_add_u32 s48, s48, 0x60000\n" \
"s_addc_u32 s49, s49, 0\n" \
"ds_read_b64_tr_b16 a[212:213], v209 offset:16384\n" \
"v_mfma_f32_16x16x32_bf16 v[4:7], a[116:119], a[76:79], v[4:7]\n" \
"ds_read_b64_tr_b16 a[214:215], v209 offset:17408\n" \
"v_mfma_f32_16x16x32_bf16 v[12:15], a[112:115], a[80:83], v[12:15]\n" \
"ds_read_b64_tr_b16 a[216:217], v210 offset:4096\n" \
"v_mfma_f32_16x16x32_bf16 v[12:15], a[116:119], a[84:87], v[12:15]\n" \
"global_load_lds_dwordx4 v253, s[50:51] offset:-3072\n" \
"v_mfma_f32_16x16x32_bf16 v[8:11], a[104:107], a[80:83], v[8:11]\n" \
"ds_read_b64_tr_b16 a[218:219], v210 offset:5120\n" \
"v_mfma_f32_16x16x32_bf16 v[8:11], a[108:111], a[84:87], v[8:11]\n" \
"ds_read_b64_tr_b16 a[220:221], v210 offset:16384\n" \
"v_mfma_f32_16x16x32_bf16 v[48:51], a[96:99], a[80:83], v[48:51]\n" \
"ds_read_b64_tr_b16 a[222:223], v210 offset:17408\n" \
"v_mfma_f32_16x16x32_bf16 v[48:51], a[100:103], a[84:87], v[48:51]\n" \
"global_load_lds_dwordx4 v253, s[50:51] offset:-1024\n" \
"v_mfma_f32_16x16x32_bf16 v[56:59], a[96:99], a[88:91], v[56:59]\n" \
"ds_read_b64_tr_b16 a[200:201], v210 offset:2048\n" \
"v_mfma_f32_16x16x32_bf16 v[56:59], a[100:103], a[92:95], v[56:59]\n" \
"ds_read_b64_tr_b16 a[202:203], v210 offset:3072\n" \
"v_mfma_f32_16x16x32_bf16 v[16:19], a[104:107], a[88:91], v[16:19]\n" \
"ds_read_b64_tr_b16 a[204:205], v210 offset:14336\n" \
"v_mfma_f32_16x16x32_bf16 v[16:19], a[108:111], a[92:95], v[16:19]\n" \
"global_load_lds_dwordx4 v253, s[50:51] offset:1024\n" \
"v_mfma_f32_16x16x32_bf16 v[20:23], a[112:115], a[88:91], v[20:23]\n" \
"s_add_u32 s50, s50, 0x60000\n" \
"s_addc_u32 s51, s51, 0\n" \
"ds_read_b64_tr_b16 a[206:207], v210 offset:15360\n" \
"v_mfma_f32_16x16x32_bf16 v[20:23], a[116:119], a[92:95], v[20:23]\n" \
"s_waitcnt lgkmcnt(3)\n" \
"v_add_u32 v207, 0x1d000, v207\n" \
"v_add_u32 v208, 0x1d000, v208\n" \
"v_mfma_f32_16x16x32_bf16 v[28:31], a[112:115], a[192:195], v[28:31]\n" \
"v_add_u32 v205, 0x1d000, v205\n" \
"v_add_u32 v206, 0x1d000, v206\n" \
"v_mfma_f32_16x16x32_bf16 v[28:31], a[116:119], a[196:199], v[28:31]\n" \
"v_add_u32 v209, 0x1d000, v209\n" \
"v_add_u32 v210, 0x1d000, v210\n" \
"v_mfma_f32_16x16x32_bf16 v[24:27], a[104:107], a[192:195], v[24:27]\n" \
"v_mfma_f32_16x16x32_bf16 v[24:27], a[108:111], a[196:199], v[24:27]\n" \
"s_waitcnt vmcnt(12) lgkmcnt(0)\n" \
"v_mfma_f32_16x16x32_bf16 v[64:67], a[96:99], a[192:195], v[64:67]\n" \
"v_mfma_f32_16x16x32_bf16 v[64:67], a[100:103], a[196:199], v[64:67]\n" \
"s_barrier\n" \
"v_mfma_f32_16x16x32_bf16 v[72:75], a[64:67], a[208:211], v[72:75]\n" \
"ds_read_b64_tr_b16 a[96:97], v209 offset:2048\n" \
"s_waitcnt vmcnt(11)\n" \
"v_mfma_f32_16x16x32_bf16 v[72:75], a[68:71], a[212:215], v[72:75]\n" \
"ds_read_b64_tr_b16 a[98:99], v209 offset:3072\n" \
"v_mfma_f32_16x16x32_bf16 v[80:83], a[72:75], a[208:211], v[80:83]\n" \
"s_add_u32 m0, s95, 0\n" \
"ds_read_b128 a[64:67], v207 offset:0\n" \
"ds_read_b64_tr_b16 a[100:101], v209 offset:14336\n" \
"v_mfma_f32_16x16x32_bf16 v[80:83], a[76:79], a[212:215], v[80:83]\n" \
"global_load_lds_dwordx4 v252, s[42:43] offset:0\n" \
"v_mfma_f32_16x16x32_bf16 v[84:87], a[72:75], a[216:219], v[84:87]\n" \
"ds_read_b128 a[68:71], v208 offset:0\n" \
"ds_read_b64_tr_b16 a[102:103], v209 offset:15360\n" \
"v_mfma_f32_16x16x32_bf16 v[84:87], a[76:79], a[220:223], v[84:87]\n" \
"ds_read_b64_tr_b16 a[104:105], v209 offset:0\n" \
"v_mfma_f32_16x16x32_bf16 v[44:47], a[200:203], a[72:75], v[44:47]\n" \
"ds_read_b64_tr_b16 a[106:107], v209 offset:1024\n" \
"v_mfma_f32_16x16x32_bf16 v[44:47], a[204:207], a[76:79], v[44:47]\n" \
"global_load_lds_dwordx4 v252, s[42:43] offset:2048\n" \
"v_mfma_f32_16x16x32_bf16 v[52:55], a[200:203], a[80:83], v[52:55]\n" \
"s_add_u32 s42, s42, 0x100\n" \
"s_addc_u32 s43, s43, 0\n" \
"ds_read_b128 a[72:75], v205 offset:0\n" \
"v_mfma_f32_16x16x32_bf16 v[52:55], a[204:207], a[84:87], v[52:55]\n" \
"ds_read_b128 a[76:79], v206 offset:0\n" \
"v_mfma_f32_16x16x32_bf16 v[92:95], a[80:83], a[216:219], v[92:95]\n" \
"ds_read_b64_tr_b16 a[108:109], v209 offset:12288\n" \
"v_mfma_f32_16x16x32_bf16 v[92:95], a[84:87], a[220:223], v[92:95]\n" \
"global_load_lds_dwordx4 v252, s[44:45] offset:1024\n" \
"v_mfma_f32_16x16x32_bf16 v[88:91], a[80:83], a[208:211], v[88:91]\n" \
"ds_read_b64_tr_b16 a[110:111], v209 offset:13312\n" \
"v_mfma_f32_16x16x32_bf16 v[88:91], a[84:87], a[212:215], v[88:91]\n" \
"ds_read_b64_tr_b16 a[112:113], v210 offset:0\n" \
"v_mfma_f32_16x16x32_bf16 v[96:99], a[88:91], a[208:211], v[96:99]\n" \
"ds_read_b128 a[80:83], v205 offset:4096\n" \
"v_mfma_f32_16x16x32_bf16 v[96:99], a[92:95], a[212:215], v[96:99]\n" \
"global_load_lds_dwordx4 v252, s[44:45] offset:3072\n" \
"v_mfma_f32_16x16x32_bf16 v[100:103], a[88:91], a[216:219], v[100:103]\n" \
"s_add_u32 s44, s44, 0x100\n" \
"s_addc_u32 s45, s45, 0\n" \
"ds_read_b128 a[84:87], v206 offset:4096\n" \
"v_mfma_f32_16x16x32_bf16 v[100:103], a[92:95], a[220:223], v[100:103]\n" \
"ds_read_b64_tr_b16 a[114:115], v210 offset:1024\n" \
"v_mfma_f32_16x16x32_bf16 v[60:63], a[200:203], a[88:91], v[60:63]\n" \
"s_add_u32 m0, s85, 16384\n" \
"ds_read_b64_tr_b16 a[116:117], v210 offset:12288\n" \
"v_mfma_f32_16x16x32_bf16 v[60:63], a[204:207], a[92:95], v[60:63]\n" \
"buffer_load_dwordx4 v246, s[12:15], s46 offen lds\n" \
"v_mfma_f32_16x16x32_bf16 v[68:71], a[200:203], a[192:195], v[68:71]\n" \
"ds_read_b128 a[88:91], v205 offset:8192\n" \
"s_add_u32 s46, s46, 0x100\n" \
"v_mfma_f32_16x16x32_bf16 v[68:71], a[204:207], a[196:199], v[68:71]\n" \
"ds_read_b128 a[92:95], v206 offset:8192\n" \
"v_mfma_f32_16x16x32_bf16 v[108:111], a[192:195], a[216:219], v[108:111]\n" \
"s_add_u32 m0, s84, 24576\n" \
"ds_read_b64_tr_b16 a[118:119], v210 offset:13312\n" \
"v_mfma_f32_16x16x32_bf16 v[108:111], a[196:199], a[220:223], v[108:111]\n" \
"global_load_lds_dwordx4 v253, s[48:49] offset:-4096\n" \
"v_mfma_f32_16x16x32_bf16 v[104:107], a[192:195], a[208:211], v[104:107]\n" \
"v_xor_b32 v226, v226, v227\n" \
"v_cmp_ne_u32 vcc, s60, v226\n" \
"v_mfma_f32_16x16x32_bf16 v[104:107], a[196:199], a[212:215], v[104:107]\n" \
"s_waitcnt lgkmcnt(2)\n" \
"v_mfma_f32_16x16x32_bf16 v[32:35], a[96:99], a[64:67], v[32:35]\n" \
"ds_read_b128 a[192:195], v205 offset:12288\n" \
"v_mfma_f32_16x16x32_bf16 v[32:35], a[100:103], a[68:71], v[32:35]\n" \
"global_load_lds_dwordx4 v253, s[48:49] offset:-2048\n" \
"v_mfma_f32_16x16x32_bf16 v[40:43], a[96:99], a[72:75], v[40:43]\n" \
"ds_read_b128 a[196:199], v206 offset:12288\n" \
"v_mfma_f32_16x16x32_bf16 v[40:43], a[100:103], a[76:79], v[40:43]\n" \
"ds_read_b64_tr_b16 a[208:209], v209 offset:4096\n" \
"v_mfma_f32_16x16x32_bf16 v[0:3], a[104:107], a[72:75], v[0:3]\n" \
"ds_read_b64_tr_b16 a[210:211], v209 offset:5120\n" \
"v_mfma_f32_16x16x32_bf16 v[0:3], a[108:111], a[76:79], v[0:3]\n" \
"s_waitcnt lgkmcnt(3)\n" \
"global_load_lds_dwordx4 v253, s[48:49] offset:0\n" \
"v_mfma_f32_16x16x32_bf16 v[4:7], a[112:115], a[72:75], v[4:7]\n" \
"s_add_u32 s48, s48, 0xc0000\n" \
"s_addc_u32 s49, s49, 0\n" \
"ds_read_b64_tr_b16 a[212:213], v209 offset:16384\n" \
"v_mfma_f32_16x16x32_bf16 v[4:7], a[116:119], a[76:79], v[4:7]\n" \
"ds_read_b64_tr_b16 a[214:215], v209 offset:17408\n" \
"v_mfma_f32_16x16x32_bf16 v[12:15], a[112:115], a[80:83], v[12:15]\n" \
"ds_read_b64_tr_b16 a[216:217], v210 offset:4096\n" \
"v_mfma_f32_16x16x32_bf16 v[12:15], a[116:119], a[84:87], v[12:15]\n" \
"global_load_lds_dwordx4 v253, s[50:51] offset:-3072\n" \
"v_mfma_f32_16x16x32_bf16 v[8:11], a[104:107], a[80:83], v[8:11]\n" \
"ds_read_b64_tr_b16 a[218:219], v210 offset:5120\n" \
"v_mfma_f32_16x16x32_bf16 v[8:11], a[108:111], a[84:87], v[8:11]\n" \
"ds_read_b64_tr_b16 a[220:221], v210 offset:16384\n" \
"v_mfma_f32_16x16x32_bf16 v[48:51], a[96:99], a[80:83], v[48:51]\n" \
"ds_read_b64_tr_b16 a[222:223], v210 offset:17408\n" \
"v_mfma_f32_16x16x32_bf16 v[48:51], a[100:103], a[84:87], v[48:51]\n" \
"global_load_lds_dwordx4 v253, s[50:51] offset:-1024\n" \
"v_mfma_f32_16x16x32_bf16 v[56:59], a[96:99], a[88:91], v[56:59]\n" \
"ds_read_b64_tr_b16 a[200:201], v210 offset:2048\n" \
"v_mfma_f32_16x16x32_bf16 v[56:59], a[100:103], a[92:95], v[56:59]\n" \
"ds_read_b64_tr_b16 a[202:203], v210 offset:3072\n" \
"v_mfma_f32_16x16x32_bf16 v[16:19], a[104:107], a[88:91], v[16:19]\n" \
"ds_read_b64_tr_b16 a[204:205], v210 offset:14336\n" \
"v_mfma_f32_16x16x32_bf16 v[16:19], a[108:111], a[92:95], v[16:19]\n" \
"global_load_lds_dwordx4 v253, s[50:51] offset:1024\n" \
"v_mfma_f32_16x16x32_bf16 v[20:23], a[112:115], a[88:91], v[20:23]\n" \
"s_add_u32 s50, s50, 0xc0000\n" \
"s_addc_u32 s51, s51, 0\n" \
"ds_read_b64_tr_b16 a[206:207], v210 offset:15360\n" \
"v_mfma_f32_16x16x32_bf16 v[20:23], a[116:119], a[92:95], v[20:23]\n" \
"s_waitcnt lgkmcnt(3)\n" \
"v_add_u32 v207, 0xffff3000, v207\n" \
"v_add_u32 v208, 0xffff3000, v208\n" \
"v_mfma_f32_16x16x32_bf16 v[28:31], a[112:115], a[192:195], v[28:31]\n" \
"v_add_u32 v205, 0xffff3000, v205\n" \
"v_add_u32 v206, 0xffff3000, v206\n" \
"v_mfma_f32_16x16x32_bf16 v[28:31], a[116:119], a[196:199], v[28:31]\n" \
"v_add_u32 v209, 0xffff3000, v209\n" \
"v_add_u32 v210, 0xffff3000, v210\n" \
"v_mfma_f32_16x16x32_bf16 v[24:27], a[104:107], a[192:195], v[24:27]\n" \
"s_and_b32 vcc_hi, vcc_lo, -1\n" \
"s_cbranch_scc1 Lpl3_%=\n" \
"Lpr3_%=:\n" \
"v_mfma_f32_16x16x32_bf16 v[24:27], a[108:111], a[196:199], v[24:27]\n" \
"s_waitcnt vmcnt(11) lgkmcnt(0)\n" \
"v_readfirstlane_b32 s71, v227\n" \
"s_and_b32 s70, %1, 7\n" \
"v_mfma_f32_16x16x32_bf16 v[64:67], a[96:99], a[192:195], v[64:67]\n" \
"s_lshl_b32 s78, s70, 2\n" \
"v_mfma_f32_16x16x32_bf16 v[64:67], a[100:103], a[196:199], v[64:67]\n" \
"s_barrier\n" \
"v_mfma_f32_16x16x32_bf16 v[72:75], a[64:67], a[208:211], v[72:75]\n" \
"ds_read_b64_tr_b16 a[96:97], v209 offset:2048\n" \
"v_subrev_u32 v227, s71, v227\n" \
"v_mfma_f32_16x16x32_bf16 v[72:75], a[68:71], a[212:215], v[72:75]\n" \
"ds_read_b64_tr_b16 a[98:99], v209 offset:3072\n" \
"v_mfma_f32_16x16x32_bf16 v[80:83], a[72:75], a[208:211], v[80:83]\n" \
"s_add_u32 m0, s95, 118784\n" \
"ds_read_b128 a[64:67], v207 offset:0\n" \
"ds_read_b64_tr_b16 a[100:101], v209 offset:14336\n" \
"v_mfma_f32_16x16x32_bf16 v[80:83], a[76:79], a[212:215], v[80:83]\n" \
"global_load_lds_dwordx4 v252, s[42:43] offset:0\n" \
"v_add_u32_dpp v227, v227, v227 quad_perm:[1,0,3,2] row_mask:0xf bank_mask:0xf\n" \
"v_mfma_f32_16x16x32_bf16 v[84:87], a[72:75], a[216:219], v[84:87]\n" \
"ds_read_b128 a[68:71], v208 offset:0\n" \
"ds_read_b64_tr_b16 a[102:103], v209 offset:15360\n" \
"v_mfma_f32_16x16x32_bf16 v[84:87], a[76:79], a[220:223], v[84:87]\n" \
"ds_read_b64_tr_b16 a[104:105], v209 offset:0\n" \
"v_add_u32_dpp v227, v227, v227 quad_perm:[2,3,0,1] row_mask:0xf bank_mask:0xf\n" \
"v_mfma_f32_16x16x32_bf16 v[44:47], a[200:203], a[72:75], v[44:47]\n" \
"ds_read_b64_tr_b16 a[106:107], v209 offset:1024\n" \
"v_readlane_b32 s71, v227, s78\n" \
"v_mfma_f32_16x16x32_bf16 v[44:47], a[204:207], a[76:79], v[44:47]\n" \
"global_load_lds_dwordx4 v252, s[42:43] offset:2048\n" \
"v_lshrrev_b32 v226, 6, v248\n" \
"v_mfma_f32_16x16x32_bf16 v[52:55], a[200:203], a[80:83], v[52:55]\n" \
"s_add_u32 s42, s42, 0x80\n" \
"s_addc_u32 s43, s43, 0\n" \
"ds_read_b128 a[72:75], v205 offset:0\n" \
"v_mfma_f32_16x16x32_bf16 v[52:55], a[204:207], a[84:87], v[52:55]\n" \
"ds_read_b128 a[76:79], v206 offset:0\n" \
"v_subrev_u32 v227, s71, v227\n" \
"v_mfma_f32_16x16x32_bf16 v[92:95], a[80:83], a[216:219], v[92:95]\n" \
"ds_read_b64_tr_b16 a[108:109], v209 offset:12288\n" \
"v_lshl_add_u32 v227, v227, 3, v226\n" \
"v_mfma_f32_16x16x32_bf16 v[92:95], a[84:87], a[220:223], v[92:95]\n" \
"global_load_lds_dwordx4 v252, s[44:45] offset:1024\n" \
"v_cmp_gt_i32 vcc, s70, v227\n" \
"v_mfma_f32_16x16x32_bf16 v[88:91], a[80:83], a[208:211], v[88:91]\n" \
"ds_read_b64_tr_b16 a[110:111], v209 offset:13312\n" \
"v_mfma_f32_16x16x32_bf16 v[88:91], a[84:87], a[212:215], v[88:91]\n" \
"s_bcnt1_i32_b32 s70, vcc_lo\n" \
"s_lshr_b32 s70, s70, 2\n" \
"ds_read_b64_tr_b16 a[112:113], v210 offset:0\n" \
"v_mfma_f32_16x16x32_bf16 v[96:99], a[88:91], a[208:211], v[96:99]\n" \
"s_lshr_b32 s78, s70, 2\n" \
"ds_read_b128 a[80:83], v205 offset:4096\n" \
"s_mul_i32 s71, s78, 7\n" \
"v_mfma_f32_16x16x32_bf16 v[96:99], a[92:95], a[212:215], v[96:99]\n" \
"s_xor_b32 s70, s70, s71\n" \
"global_load_lds_dwordx4 v252, s[44:45] offset:3072\n" \
"v_mfma_f32_16x16x32_bf16 v[100:103], a[88:91], a[216:219], v[100:103]\n" \
"s_add_u32 s44, s44, 0x80\n" \
"s_addc_u32 s45, s45, 0\n" \
"ds_read_b128 a[84:87], v206 offset:4096\n" \
"v_mfma_f32_16x16x32_bf16 v[100:103], a[92:95], a[220:223], v[100:103]\n" \
"s_xor_b32 s78, s78, 1\n" \
"s_lshl_b32 s65, s70, 2\n" \
"ds_read_b64_tr_b16 a[114:115], v210 offset:1024\n" \
"v_mfma_f32_16x16x32_bf16 v[60:63], a[200:203], a[88:91], v[60:63]\n" \
"s_add_u32 m0, s85, 135168\n" \
"s_lshr_b32 s64, 0x9764, s65\n" \
"ds_read_b64_tr_b16 a[116:117], v210 offset:12288\n" \
"v_mfma_f32_16x16x32_bf16 v[60:63], a[204:207], a[92:95], v[60:63]\n" \
"buffer_load_dwordx4 v246, s[12:15], s46 offen lds\n" \
"v_mfma_f32_16x16x32_bf16 v[68:71], a[200:203], a[192:195], v[68:71]\n" \
"s_add_u32 s46, s46, 0x80\n" \
"ds_read_b128 a[88:91], v205 offset:8192\n" \
"s_and_b32 s64, s64, 15\n" \
"v_mfma_f32_16x16x32_bf16 v[68:71], a[204:207], a[196:199], v[68:71]\n" \
"s_add_u32 s64, s64, 32\n" \
"ds_read_b128 a[92:95], v206 offset:8192\n" \
"s_lshr_b32 s70, %1, 6\n" \
"v_mfma_f32_16x16x32_bf16 v[108:111], a[192:195], a[216:219], v[108:111]\n" \
"s_add_u32 m0, s84, 143360\n" \
"s_add_u32 s65, s65, s70\n" \
"ds_read_b64_tr_b16 a[118:119], v210 offset:13312\n" \
"v_mfma_f32_16x16x32_bf16 v[108:111], a[196:199], a[220:223], v[108:111]\n" \
"s_and_b32 s30, %1, 1\n" \
"global_load_lds_dwordx4 v253, s[48:49] offset:-4096\n" \
"v_mfma_f32_16x16x32_bf16 v[104:107], a[192:195], a[208:211], v[104:107]\n" \
"s_lshl_b32 s30, s30, 3\n" \
"s_add_u32 s30, s30, s63\n" \
"s_lshl_b32 s30, s30, 2\n" \
"v_mfma_f32_16x16x32_bf16 v[104:107], a[196:199], a[212:215], v[104:107]\n" \
"s_waitcnt lgkmcnt(2)\n" \
"s_lshl_b32 s70, s88, 7\n" \
"s_add_u32 s30, s30, s70\n" \
"v_mfma_f32_16x16x32_bf16 v[32:35], a[96:99], a[64:67], v[32:35]\n" \
"s_add_u32 s30, s30, 65591296\n" \
"ds_read_b128 a[192:195], v205 offset:12288\n" \
"v_lshrrev_b32 v221, 2, v248\n" \
"v_mfma_f32_16x16x32_bf16 v[32:35], a[100:103], a[68:71], v[32:35]\n" \
"s_lshl_b32 s81, s65, 7\n" \
"global_load_lds_dwordx4 v253, s[48:49] offset:-2048\n" \
"v_mfma_f32_16x16x32_bf16 v[40:43], a[96:99], a[72:75], v[40:43]\n" \
"s_add_u32 s82, s81, 65591296\n" \
"ds_read_b128 a[196:199], v206 offset:12288\n" \
"s_add_u32 s81, s81, 65589248\n" \
"v_mfma_f32_16x16x32_bf16 v[40:43], a[100:103], a[76:79], v[40:43]\n" \
"s_mov_b64 s[52:53], s[26:27]\n" \
"s_and_b32 s53, s53, 0xffff\n" \
"ds_read_b64_tr_b16 a[208:209], v209 offset:4096\n" \
"v_mfma_f32_16x16x32_bf16 v[0:3], a[104:107], a[72:75], v[0:3]\n" \
"s_mov_b32 s54, 8388608\n" \
"s_mov_b32 s55, 0x20000\n" \
"ds_read_b64_tr_b16 a[210:211], v209 offset:5120\n" \
"v_mfma_f32_16x16x32_bf16 v[0:3], a[108:111], a[76:79], v[0:3]\n" \
"s_waitcnt lgkmcnt(3)\n" \
"global_load_lds_dwordx4 v253, s[48:49] offset:0\n" \
"s_lshr_b32 s83, s63, 1\n" \
"v_mfma_f32_16x16x32_bf16 v[4:7], a[112:115], a[72:75], v[4:7]\n" \
"s_add_u32 s48, s48, 0x60000\n" \
"s_addc_u32 s49, s49, 0\n" \
"ds_read_b64_tr_b16 a[212:213], v209 offset:16384\n" \
"v_mfma_f32_16x16x32_bf16 v[4:7], a[116:119], a[76:79], v[4:7]\n" \
"s_add_u32 s83, s83, 2\n" \
"s_lshr_b32 s70, s18, 8\n" \
"ds_read_b64_tr_b16 a[214:215], v209 offset:17408\n" \
"v_mfma_f32_16x16x32_bf16 v[12:15], a[112:115], a[80:83], v[12:15]\n" \
"s_sub_u32 s83, s83, s70\n" \
"s_and_b32 s83, s83, 3\n" \
"ds_read_b64_tr_b16 a[216:217], v210 offset:4096\n" \
"v_mfma_f32_16x16x32_bf16 v[12:15], a[116:119], a[84:87], v[12:15]\n" \
"s_and_b32 s70, s63, 1\n" \
"global_load_lds_dwordx4 v253, s[50:51] offset:-3072\n" \
"v_mfma_f32_16x16x32_bf16 v[8:11], a[104:107], a[80:83], v[8:11]\n" \
"s_lshl_b32 s70, s70, 2\n" \
"s_or_b32 s83, s83, s70\n" \
"ds_read_b64_tr_b16 a[218:219], v210 offset:5120\n" \
"v_mfma_f32_16x16x32_bf16 v[8:11], a[108:111], a[84:87], v[8:11]\n" \
"s_mul_i32 s75, s78, s64\n" \
"ds_read_b64_tr_b16 a[220:221], v210 offset:16384\n" \
"v_lshrrev_b32 v228, 4, v248\n" \
"v_mfma_f32_16x16x32_bf16 v[48:51], a[96:99], a[80:83], v[48:51]\n" \
"s_lshl_b32 s70, s65, 2\n" \
"s_add_u32 s70, s70, s61\n" \
"ds_read_b64_tr_b16 a[222:223], v210 offset:17408\n" \
"v_mfma_f32_16x16x32_bf16 v[48:51], a[100:103], a[84:87], v[48:51]\n" \
"s_mul_i32 s73, s70, 69632\n" \
"global_load_lds_dwordx4 v253, s[50:51] offset:-1024\n" \
"v_mfma_f32_16x16x32_bf16 v[56:59], a[96:99], a[88:91], v[56:59]\n" \
"s_add_u32 s73, s73, 52740096\n" \
"s_add_u32 s73, s73, s28\n" \
"ds_read_b64_tr_b16 a[200:201], v210 offset:2048\n" \
"v_mfma_f32_16x16x32_bf16 v[56:59], a[100:103], a[92:95], v[56:59]\n" \
"s_mul_i32 s70, s70, 266240\n" \
"s_lshl_b32 s71, s75, 12\n" \
"ds_read_b64_tr_b16 a[202:203], v210 offset:3072\n" \
"v_mfma_f32_16x16x32_bf16 v[16:19], a[104:107], a[88:91], v[16:19]\n" \
"s_add_u32 s70, s70, s71\n" \
"s_add_u32 s70, s70, 35700736\n" \
"ds_read_b64_tr_b16 a[204:205], v210 offset:14336\n" \
"v_mfma_f32_16x16x32_bf16 v[16:19], a[108:111], a[92:95], v[16:19]\n" \
"s_add_u32 s36, s28, s70\n" \
"global_load_lds_dwordx4 v253, s[50:51] offset:1024\n" \
"v_mfma_f32_16x16x32_bf16 v[20:23], a[112:115], a[88:91], v[20:23]\n" \
"s_add_u32 s50, s50, 0x60000\n" \
"s_addc_u32 s51, s51, 0\n" \
"ds_read_b64_tr_b16 a[206:207], v210 offset:15360\n" \
"v_mfma_f32_16x16x32_bf16 v[20:23], a[116:119], a[92:95], v[20:23]\n" \
"s_waitcnt lgkmcnt(3)\n" \
"v_add_u32 v207, 0xffff0000, v207\n" \
"v_add_u32 v208, 0xffff0000, v208\n" \
"v_mfma_f32_16x16x32_bf16 v[28:31], a[112:115], a[192:195], v[28:31]\n" \
"s_mov_b32 s37, s29\n" \
"v_add_u32 v205, 0xffff0000, v205\n" \
"v_add_u32 v206, 0xffff0000, v206\n" \
"v_mfma_f32_16x16x32_bf16 v[28:31], a[116:119], a[196:199], v[28:31]\n" \
"s_lshr_b32 s74, s61, 1\n" \
"v_add_u32 v209, 0xffff0000, v209\n" \
"v_add_u32 v210, 0xffff0000, v210\n" \
"v_mfma_f32_16x16x32_bf16 v[24:27], a[104:107], a[192:195], v[24:27]\n" \
"s_lshl_b32 s74, s74, 16\n" \
"s_and_b32 s70, s61, 1\n" \
"s_lshl_b32 s70, s70, 14\n" \
"v_mfma_f32_16x16x32_bf16 v[24:27], a[108:111], a[196:199], v[24:27]\n" \
"s_waitcnt vmcnt(11) lgkmcnt(0)\n" \
"s_add_u32 s74, s74, s70\n" \
"s_lshl_b32 s71, s83, 8\n" \
"v_mfma_f32_16x16x32_bf16 v[64:67], a[96:99], a[192:195], v[64:67]\n" \
"s_add_u32 s74, s74, s71\n" \
"s_add_u32 s40, s24, s74\n" \
"s_addc_u32 s41, s25, 0\n" \
"v_mfma_f32_16x16x32_bf16 v[64:67], a[100:103], a[196:199], v[64:67]\n" \
"s_barrier\n" \
"v_mfma_f32_16x16x32_bf16 v[72:75], a[64:67], a[208:211], v[72:75]\n" \
"s_lshl_b32 s70, s75, 17\n" \
"s_add_u32 s70, s70, s74\n" \
"ds_read_b64_tr_b16 a[96:97], v209 offset:2048\n" \
"v_mfma_f32_16x16x32_bf16 v[72:75], a[68:71], a[212:215], v[72:75]\n" \
"s_add_u32 s38, s18, s70\n" \
"s_addc_u32 s39, s19, 0\n" \
"ds_read_b64_tr_b16 a[98:99], v209 offset:3072\n" \
"v_mfma_f32_16x16x32_bf16 v[80:83], a[72:75], a[208:211], v[80:83]\n" \
"s_add_u32 m0, s95, 65536\n" \
"ds_read_b128 a[64:67], v207 offset:0\n" \
"ds_read_b64_tr_b16 a[100:101], v209 offset:14336\n" \
"v_mfma_f32_16x16x32_bf16 v[80:83], a[76:79], a[212:215], v[80:83]\n" \
"global_load_lds_dwordx4 v252, s[42:43] offset:0\n" \
"v_bfe_u32 v229, v228, 2, 3\n" \
"v_mfma_f32_16x16x32_bf16 v[84:87], a[72:75], a[216:219], v[84:87]\n" \
"ds_read_b128 a[68:71], v208 offset:0\n" \
"ds_read_b64_tr_b16 a[102:103], v209 offset:15360\n" \
"v_mfma_f32_16x16x32_bf16 v[84:87], a[76:79], a[220:223], v[84:87]\n" \
"ds_read_b64_tr_b16 a[104:105], v209 offset:0\n" \
"v_lshlrev_b32 v229, 11, v229\n" \
"v_mfma_f32_16x16x32_bf16 v[44:47], a[200:203], a[72:75], v[44:47]\n" \
"ds_read_b64_tr_b16 a[106:107], v209 offset:1024\n" \
"v_lshrrev_b32 v230, 5, v228\n" \
"v_mfma_f32_16x16x32_bf16 v[44:47], a[204:207], a[76:79], v[44:47]\n" \
"global_load_lds_dwordx4 v252, s[42:43] offset:2048\n" \
"v_lshl_add_u32 v229, v230, 6, v229\n" \
"v_mfma_f32_16x16x32_bf16 v[52:55], a[200:203], a[80:83], v[52:55]\n" \
"s_add_u32 s42, s42, 0x80\n" \
"s_addc_u32 s43, s43, 0\n" \
"ds_read_b128 a[72:75], v205 offset:0\n" \
"v_mfma_f32_16x16x32_bf16 v[52:55], a[204:207], a[84:87], v[52:55]\n" \
"ds_read_b128 a[76:79], v206 offset:0\n" \
"v_and_b32 v230, 3, v228\n" \
"v_mfma_f32_16x16x32_bf16 v[92:95], a[80:83], a[216:219], v[92:95]\n" \
"ds_read_b64_tr_b16 a[108:109], v209 offset:12288\n" \
"v_bfe_u32 v231, v228, 4, 1\n" \
"v_mfma_f32_16x16x32_bf16 v[92:95], a[84:87], a[220:223], v[92:95]\n" \
"global_load_lds_dwordx4 v252, s[44:45] offset:1024\n" \
"v_lshlrev_b32 v231, 1, v231\n" \
"v_mfma_f32_16x16x32_bf16 v[88:91], a[80:83], a[208:211], v[88:91]\n" \
"ds_read_b64_tr_b16 a[110:111], v209 offset:13312\n" \
"v_xor_b32 v230, v230, v231\n" \
"v_mfma_f32_16x16x32_bf16 v[88:91], a[84:87], a[212:215], v[88:91]\n" \
"ds_read_b64_tr_b16 a[112:113], v210 offset:0\n" \
"v_lshl_add_u32 v229, v230, 4, v229\n" \
"v_mfma_f32_16x16x32_bf16 v[96:99], a[88:91], a[208:211], v[96:99]\n" \
"ds_read_b128 a[80:83], v205 offset:4096\n" \
"v_add_u32 v216, 4096, v229\n" \
"v_mfma_f32_16x16x32_bf16 v[96:99], a[92:95], a[212:215], v[96:99]\n" \
"global_load_lds_dwordx4 v252, s[44:45] offset:3072\n" \
"v_add_u32 v217, 3200, v229\n" \
"v_mfma_f32_16x16x32_bf16 v[100:103], a[88:91], a[216:219], v[100:103]\n" \
"s_add_u32 s44, s44, 0x80\n" \
"s_addc_u32 s45, s45, 0\n" \
"ds_read_b128 a[84:87], v206 offset:4096\n" \
"v_mfma_f32_16x16x32_bf16 v[100:103], a[92:95], a[220:223], v[100:103]\n" \
"ds_read_b64_tr_b16 a[114:115], v210 offset:1024\n" \
"v_add_u32 v218, 34816, v229\n" \
"v_mfma_f32_16x16x32_bf16 v[60:63], a[200:203], a[88:91], v[60:63]\n" \
"s_add_u32 m0, s85, 81920\n" \
"ds_read_b64_tr_b16 a[116:117], v210 offset:12288\n" \
"v_add_u32 v219, 33920, v229\n" \
"v_mfma_f32_16x16x32_bf16 v[60:63], a[204:207], a[92:95], v[60:63]\n" \
"buffer_load_dwordx4 v246, s[12:15], s46 offen lds\n" \
"v_mfma_f32_16x16x32_bf16 v[68:71], a[200:203], a[192:195], v[68:71]\n" \
"s_add_u32 s46, s46, 0x80\n" \
"ds_read_b128 a[88:91], v205 offset:8192\n" \
"s_mov_b32 s75, 65536\n" \
"v_mfma_f32_16x16x32_bf16 v[68:71], a[204:207], a[196:199], v[68:71]\n" \
"s_lshr_b32 s70, s61, 1\n" \
"ds_read_b128 a[92:95], v206 offset:8192\n" \
"s_lshl_b32 s71, s70, 13\n" \
"v_mfma_f32_16x16x32_bf16 v[108:111], a[192:195], a[216:219], v[108:111]\n" \
"s_add_u32 m0, s84, 90112\n" \
"Lpg10302_%=:\n" \
"s_cbranch_execz Lpg12371_%=\n" \
"ds_read_b64_tr_b16 a[118:119], v210 offset:13312\n" \
"v_add_u32 v211, s71, v249\n" \
"v_mfma_f32_16x16x32_bf16 v[108:111], a[196:199], a[220:223], v[108:111]\n" \
"s_lshl_b32 s71, s65, 1\n" \
"global_load_lds_dwordx4 v253, s[48:49] offset:-4096\n" \
"v_mfma_f32_16x16x32_bf16 v[104:107], a[192:195], a[208:211], v[104:107]\n" \
"s_add_u32 s70, s70, s71\n" \
"s_lshl_b32 s77, s70, 18\n" \
"s_lshl_b32 s71, s83, 8\n" \
"v_mfma_f32_16x16x32_bf16 v[104:107], a[196:199], a[212:215], v[104:107]\n" \
"s_waitcnt lgkmcnt(2)\n" \
"s_add_u32 s77, s77, s71\n" \
"s_lshl_b32 s71, s78, 11\n" \
"v_mfma_f32_16x16x32_bf16 v[32:35], a[96:99], a[64:67], v[32:35]\n" \
"s_add_u32 s77, s77, s71\n" \
"ds_read_b128 a[192:195], v205 offset:12288\n" \
"s_and_b32 s70, s61, 1\n" \
"v_mfma_f32_16x16x32_bf16 v[32:35], a[100:103], a[68:71], v[32:35]\n" \
"s_lshl_b32 s71, s70, 7\n" \
"global_load_lds_dwordx4 v253, s[48:49] offset:-2048\n" \
"v_mfma_f32_16x16x32_bf16 v[40:43], a[96:99], a[72:75], v[40:43]\n" \
"s_add_u32 s77, s77, s71\n" \
"ds_read_b128 a[196:199], v206 offset:12288\n" \
"v_and_b32 v229, 3, v228\n" \
"v_mfma_f32_16x16x32_bf16 v[40:43], a[100:103], a[76:79], v[40:43]\n" \
"ds_read_b64_tr_b16 a[208:209], v209 offset:4096\n" \
"v_lshlrev_b32 v229, 3, v229\n" \
"v_mfma_f32_16x16x32_bf16 v[0:3], a[104:107], a[72:75], v[0:3]\n" \
"ds_read_b64_tr_b16 a[210:211], v209 offset:5120\n" \
"v_bfe_u32 v230, v228, 2, 2\n" \
"v_mfma_f32_16x16x32_bf16 v[0:3], a[108:111], a[76:79], v[0:3]\n" \
"s_waitcnt lgkmcnt(3)\n" \
"global_load_lds_dwordx4 v253, s[48:49] offset:0\n" \
"v_lshl_add_u32 v229, v230, 6, v229\n" \
"v_mfma_f32_16x16x32_bf16 v[4:7], a[112:115], a[72:75], v[4:7]\n" \
"s_add_u32 s48, s48, 0x60000\n" \
"s_addc_u32 s49, s49, 0\n" \
"ds_read_b64_tr_b16 a[212:213], v209 offset:16384\n" \
"v_mfma_f32_16x16x32_bf16 v[4:7], a[116:119], a[76:79], v[4:7]\n" \
"ds_read_b64_tr_b16 a[214:215], v209 offset:17408\n" \
"v_bfe_u32 v230, v228, 4, 1\n" \
"v_mfma_f32_16x16x32_bf16 v[12:15], a[112:115], a[80:83], v[12:15]\n" \
"ds_read_b64_tr_b16 a[216:217], v210 offset:4096\n" \
"v_lshl_add_u32 v229, v230, 5, v229\n" \
"v_mfma_f32_16x16x32_bf16 v[12:15], a[116:119], a[84:87], v[12:15]\n" \
"global_load_lds_dwordx4 v253, s[50:51] offset:-3072\n" \
"v_lshl_add_u32 v229, v230, 8, v229\n" \
"v_mfma_f32_16x16x32_bf16 v[8:11], a[104:107], a[80:83], v[8:11]\n" \
"ds_read_b64_tr_b16 a[218:219], v210 offset:5120\n" \
"v_lshrrev_b32 v230, 5, v228\n" \
"v_mfma_f32_16x16x32_bf16 v[8:11], a[108:111], a[84:87], v[8:11]\n" \
"ds_read_b64_tr_b16 a[220:221], v210 offset:16384\n" \
"v_lshl_add_u32 v229, v230, 12, v229\n" \
"v_mfma_f32_16x16x32_bf16 v[48:51], a[96:99], a[80:83], v[48:51]\n" \
"s_lshl_b32 s71, s70, 10\n" \
"ds_read_b64_tr_b16 a[222:223], v210 offset:17408\n" \
"v_add_u32 v212, s71, v229\n" \
"v_mfma_f32_16x16x32_bf16 v[48:51], a[100:103], a[84:87], v[48:51]\n" \
"s_lshl_b32 s70, s65, 3\n" \
"global_load_lds_dwordx4 v253, s[50:51] offset:-1024\n" \
"v_mfma_f32_16x16x32_bf16 v[56:59], a[96:99], a[88:91], v[56:59]\n" \
"s_add_u32 s70, s70, s83\n" \
"s_lshl_b32 s76, s70, 16\n" \
"ds_read_b64_tr_b16 a[200:201], v210 offset:2048\n" \
"v_mfma_f32_16x16x32_bf16 v[56:59], a[100:103], a[92:95], v[56:59]\n" \
"s_lshl_b32 s71, s61, 14\n" \
"s_add_u32 s76, s76, s71\n" \
"ds_read_b64_tr_b16 a[202:203], v210 offset:3072\n" \
"v_mfma_f32_16x16x32_bf16 v[16:19], a[104:107], a[88:91], v[16:19]\n" \
"s_add_u32 s76, s76, 57196544\n" \
"s_lshl_b32 s79, s70, 7\n" \
"ds_read_b64_tr_b16 a[204:205], v210 offset:14336\n" \
"v_mfma_f32_16x16x32_bf16 v[16:19], a[108:111], a[92:95], v[16:19]\n" \
"s_add_u32 s79, s79, 65601536\n" \
"global_load_lds_dwordx4 v253, s[50:51] offset:1024\n" \
"v_mfma_f32_16x16x32_bf16 v[20:23], a[112:115], a[88:91], v[20:23]\n" \
"s_add_u32 s50, s50, 0x60000\n" \
"s_addc_u32 s51, s51, 0\n" \
"ds_read_b64_tr_b16 a[206:207], v210 offset:15360\n" \
"v_mfma_f32_16x16x32_bf16 v[20:23], a[116:119], a[92:95], v[20:23]\n" \
"s_waitcnt lgkmcnt(3)\n" \
"v_add_u32 v207, 0x1d000, v207\n" \
"v_add_u32 v208, 0x1d000, v208\n" \
"v_mfma_f32_16x16x32_bf16 v[28:31], a[112:115], a[192:195], v[28:31]\n" \
"s_lshr_b32 s70, s64, 1\n" \
"v_add_u32 v205, 0x1d000, v205\n" \
"v_add_u32 v206, 0x1d000, v206\n" \
"v_mfma_f32_16x16x32_bf16 v[28:31], a[116:119], a[196:199], v[28:31]\n" \
"s_lshl_b32 s70, -1, s70\n" \
"v_add_u32 v209, 0x1d000, v209\n" \
"v_add_u32 v210, 0x1d000, v210\n" \
"v_mfma_f32_16x16x32_bf16 v[24:27], a[104:107], a[192:195], v[24:27]\n" \
"s_add_u32 s71, s64, 1\n" \
"s_lshr_b32 s71, s71, 1\n" \
"s_bfm_b32 s71, s71, 0\n" \
"v_mfma_f32_16x16x32_bf16 v[24:27], a[108:111], a[196:199], v[24:27]\n" \
"s_waitcnt vmcnt(11) lgkmcnt(0)\n" \
"s_cmp_eq_u32 s78, 0\n" \
"s_cselect_b32 s80, s71, s70\n" \
"v_mfma_f32_16x16x32_bf16 v[64:67], a[96:99], a[192:195], v[64:67]\n" \
"s_mov_b32 s16, 0xff000000\n" \
"s_mov_b32 s17, 0\n" \
"v_add_u32 v222, 0x1c90080, v250\n" \
"v_mfma_f32_16x16x32_bf16 v[64:67], a[100:103], a[196:199], v[64:67]\n" \
"s_barrier\n" \
"v_mfma_f32_16x16x32_bf16 v[72:75], a[64:67], a[208:211], v[72:75]\n" \
"ds_read_b64_tr_b16 a[96:97], v209 offset:2048\n" \
"v_cndmask_b32 v222, v250, v222, s[16:17]\n" \
"v_mfma_f32_16x16x32_bf16 v[72:75], a[68:71], a[212:215], v[72:75]\n" \
"s_mov_b32 s74, 98304\n" \
"ds_read_b64_tr_b16 a[98:99], v209 offset:3072\n" \
"v_mfma_f32_16x16x32_bf16 v[80:83], a[72:75], a[208:211], v[80:83]\n" \
"s_add_u32 m0, s95, 0\n" \
"ds_read_b128 a[64:67], v207 offset:0\n" \
"ds_read_b64_tr_b16 a[100:101], v209 offset:14336\n" \
"v_mfma_f32_16x16x32_bf16 v[80:83], a[76:79], a[212:215], v[80:83]\n" \
"global_load_lds_dwordx4 v252, s[42:43] offset:0\n" \
"v_mfma_f32_16x16x32_bf16 v[84:87], a[72:75], a[216:219], v[84:87]\n" \
"ds_read_b128 a[68:71], v208 offset:0\n" \
"ds_read_b64_tr_b16 a[102:103], v209 offset:15360\n" \
"v_mfma_f32_16x16x32_bf16 v[84:87], a[76:79], a[220:223], v[84:87]\n" \
"s_sub_u32 s74, s74, 32768\n" \
"s_cselect_b32 s74, 131072, s74\n" \
"ds_read_b64_tr_b16 a[104:105], v209 offset:0\n" \
"v_mfma_f32_16x16x32_bf16 v[44:47], a[200:203], a[72:75], v[44:47]\n" \
"ds_read_b64_tr_b16 a[106:107], v209 offset:1024\n" \
"v_add_u32 v213, s74, v211\n" \
"v_mfma_f32_16x16x32_bf16 v[44:47], a[204:207], a[76:79], v[44:47]\n" \
"global_load_lds_dwordx4 v252, s[42:43] offset:2048\n" \
"v_add_u32 v214, s74, v212\n" \
"v_mfma_f32_16x16x32_bf16 v[52:55], a[200:203], a[80:83], v[52:55]\n" \
"s_add_u32 s42, s42, 0x80\n" \
"s_addc_u32 s43, s43, 0\n" \
"ds_read_b128 a[72:75], v205 offset:0\n" \
"v_mfma_f32_16x16x32_bf16 v[52:55], a[204:207], a[84:87], v[52:55]\n" \
"ds_read_b128 a[76:79], v206 offset:0\n" \
"v_xor_b32 v215, 32, v214\n" \
"v_mfma_f32_16x16x32_bf16 v[92:95], a[80:83], a[216:219], v[92:95]\n" \
"s_and_b32 s89, s61, 1\n" \
"s_lshl_b32 s89, s89, 11\n" \
"ds_read_b64_tr_b16 a[108:109], v209 offset:12288\n" \
"v_mfma_f32_16x16x32_bf16 v[92:95], a[84:87], a[220:223], v[92:95]\n" \
"s_add_u32 s87, s87, s89\n" \
"global_load_lds_dwordx4 v252, s[44:45] offset:1024\n" \
"v_mfma_f32_16x16x32_bf16 v[88:91], a[80:83], a[208:211], v[88:91]\n" \
"s_add_u32 s87, s87, 52740096\n" \
"ds_read_b64_tr_b16 a[110:111], v209 offset:13312\n" \
"v_mfma_f32_16x16x32_bf16 v[88:91], a[84:87], a[212:215], v[88:91]\n" \
"s_cmp_eq_u32 s88, 0\n" \
"s_cselect_b32 s86, -1, 0\n" \
"ds_read_b64_tr_b16 a[112:113], v210 offset:0\n" \
"v_mfma_f32_16x16x32_bf16 v[96:99], a[88:91], a[208:211], v[96:99]\n" \
"s_mul_i32 s89, s88, 278528\n" \
"ds_read_b128 a[80:83], v205 offset:4096\n" \
"s_add_u32 s87, s87, s89\n" \
"v_mfma_f32_16x16x32_bf16 v[96:99], a[92:95], a[212:215], v[96:99]\n" \
"s_lshr_b32 s89, s61, 1\n" \
"global_load_lds_dwordx4 v252, s[44:45] offset:3072\n" \
"v_mfma_f32_16x16x32_bf16 v[100:103], a[88:91], a[216:219], v[100:103]\n" \
"s_add_u32 s44, s44, 0x80\n" \
"s_addc_u32 s45, s45, 0\n" \
"ds_read_b128 a[84:87], v206 offset:4096\n" \
"v_mfma_f32_16x16x32_bf16 v[100:103], a[92:95], a[220:223], v[100:103]\n" \
"s_lshl_b32 s89, s89, 8\n" \
"s_add_u32 s87, s87, s89\n" \
"ds_read_b64_tr_b16 a[114:115], v210 offset:1024\n" \
"v_mfma_f32_16x16x32_bf16 v[60:63], a[200:203], a[88:91], v[60:63]\n" \
"s_add_u32 m0, s85, 16384\n" \
"s_lshl_b32 s89, s61, 4\n" \
"ds_read_b64_tr_b16 a[116:117], v210 offset:12288\n" \
"v_mfma_f32_16x16x32_bf16 v[60:63], a[204:207], a[92:95], v[60:63]\n" \
"buffer_load_dwordx4 v246, s[12:15], s46 offen lds\n" \
"v_mfma_f32_16x16x32_bf16 v[68:71], a[200:203], a[192:195], v[68:71]\n" \
"s_add_u32 s46, s46, 0x80\n" \
"ds_read_b128 a[88:91], v205 offset:8192\n" \
"v_subrev_u32 v147, s89, v251\n" \
"v_mfma_f32_16x16x32_bf16 v[68:71], a[204:207], a[196:199], v[68:71]\n" \
"s_lshr_b32 s89, s61, 1\n" \
"ds_read_b128 a[92:95], v206 offset:8192\n" \
"s_lshl_b32 s90, s89, 9\n" \
"v_mfma_f32_16x16x32_bf16 v[108:111], a[192:195], a[216:219], v[108:111]\n" \
"s_add_u32 m0, s84, 24576\n" \
"s_add_u32 s90, s90, 511\n" \
"ds_read_b64_tr_b16 a[118:119], v210 offset:13312\n" \
"v_mfma_f32_16x16x32_bf16 v[108:111], a[196:199], a[220:223], v[108:111]\n" \
"global_load_lds_dwordx4 v253, s[48:49] offset:-4096\n" \
"v_sub_u32 v145, s90, v248\n" \
"v_mfma_f32_16x16x32_bf16 v[104:107], a[192:195], a[208:211], v[104:107]\n" \
"s_lshl_b32 s90, s89, 3\n" \
"s_not_b32 s90, s90\n" \
"v_sub_u32 v146, s90, v147\n" \
"v_mfma_f32_16x16x32_bf16 v[104:107], a[196:199], a[212:215], v[104:107]\n" \
"s_waitcnt lgkmcnt(2)\n" \
"v_subrev_u32 v152, 0, v147\n" \
"v_sub_u32 v144, 7, v147\n" \
"v_mfma_f32_16x16x32_bf16 v[32:35], a[96:99], a[64:67], v[32:35]\n" \
"ds_read_b128 a[192:195], v205 offset:12288\n" \
"v_or_b32 v152, v152, v144\n" \
"v_mfma_f32_16x16x32_bf16 v[32:35], a[100:103], a[68:71], v[32:35]\n" \
"global_load_lds_dwordx4 v253, s[48:49] offset:-2048\n" \
"v_ashrrev_i32 v152, 31, v152\n" \
"v_mfma_f32_16x16x32_bf16 v[40:43], a[96:99], a[72:75], v[40:43]\n" \
"ds_read_b128 a[196:199], v206 offset:12288\n" \
"v_and_b32 v152, 0xff800000, v152\n" \
"v_mfma_f32_16x16x32_bf16 v[40:43], a[100:103], a[76:79], v[40:43]\n" \
"ds_read_b64_tr_b16 a[208:209], v209 offset:4096\n" \
"v_mov_b32 v160, v152\n" \
"v_mfma_f32_16x16x32_bf16 v[0:3], a[104:107], a[72:75], v[0:3]\n" \
"ds_read_b64_tr_b16 a[210:211], v209 offset:5120\n" \
"v_mov_b32 v168, v152\n" \
"v_mfma_f32_16x16x32_bf16 v[0:3], a[108:111], a[76:79], v[0:3]\n" \
"s_waitcnt lgkmcnt(3)\n" \
"global_load_lds_dwordx4 v253, s[48:49] offset:0\n" \
"v_mov_b32 v176, v152\n" \
"v_mfma_f32_16x16x32_bf16 v[4:7], a[112:115], a[72:75], v[4:7]\n" \
"s_add_u32 s48, s48, 0x60000\n" \
"s_addc_u32 s49, s49, 0\n" \
"ds_read_b64_tr_b16 a[212:213], v209 offset:16384\n" \
"v_mfma_f32_16x16x32_bf16 v[4:7], a[116:119], a[76:79], v[4:7]\n" \
"ds_read_b64_tr_b16 a[214:215], v209 offset:17408\n" \
"v_sub_u32 v156, -9, v147\n" \
"v_mfma_f32_16x16x32_bf16 v[12:15], a[112:115], a[80:83], v[12:15]\n" \
"ds_read_b64_tr_b16 a[216:217], v210 offset:4096\n" \
"v_ashrrev_i32 v156, 31, v156\n" \
"v_mfma_f32_16x16x32_bf16 v[12:15], a[116:119], a[84:87], v[12:15]\n" \
"global_load_lds_dwordx4 v253, s[50:51] offset:-3072\n" \
"v_and_b32 v156, 0xff800000, v156\n" \
"v_mfma_f32_16x16x32_bf16 v[8:11], a[104:107], a[80:83], v[8:11]\n" \
"ds_read_b64_tr_b16 a[218:219], v210 offset:5120\n" \
"v_mov_b32 v164, v156\n" \
"v_mfma_f32_16x16x32_bf16 v[8:11], a[108:111], a[84:87], v[8:11]\n" \
"ds_read_b64_tr_b16 a[220:221], v210 offset:16384\n" \
"v_mov_b32 v172, v156\n" \
"v_mfma_f32_16x16x32_bf16 v[48:51], a[96:99], a[80:83], v[48:51]\n" \
"ds_read_b64_tr_b16 a[222:223], v210 offset:17408\n" \
"v_add_u32 v148, 0, v146\n" \
"v_mfma_f32_16x16x32_bf16 v[48:51], a[100:103], a[84:87], v[48:51]\n" \
"global_load_lds_dwordx4 v253, s[50:51] offset:-1024\n" \
"v_or3_b32 v148, v148, v145, s86\n" \
"v_mfma_f32_16x16x32_bf16 v[56:59], a[96:99], a[88:91], v[56:59]\n" \
"ds_read_b64_tr_b16 a[200:201], v210 offset:2048\n" \
"v_ashrrev_i32 v148, 31, v148\n" \
"v_mfma_f32_16x16x32_bf16 v[56:59], a[100:103], a[92:95], v[56:59]\n" \
"ds_read_b64_tr_b16 a[202:203], v210 offset:3072\n" \
"v_and_b32 v148, 0xff800000, v148\n" \
"v_mfma_f32_16x16x32_bf16 v[16:19], a[104:107], a[88:91], v[16:19]\n" \
"ds_read_b64_tr_b16 a[204:205], v210 offset:14336\n" \
"v_subrev_u32 v153, 1, v147\n" \
"v_mfma_f32_16x16x32_bf16 v[16:19], a[108:111], a[92:95], v[16:19]\n" \
"global_load_lds_dwordx4 v253, s[50:51] offset:1024\n" \
"v_sub_u32 v144, 8, v147\n" \
"v_mfma_f32_16x16x32_bf16 v[20:23], a[112:115], a[88:91], v[20:23]\n" \
"s_add_u32 s50, s50, 0x60000\n" \
"s_addc_u32 s51, s51, 0\n" \
"ds_read_b64_tr_b16 a[206:207], v210 offset:15360\n" \
"v_mfma_f32_16x16x32_bf16 v[20:23], a[116:119], a[92:95], v[20:23]\n" \
"s_waitcnt lgkmcnt(3)\n" \
"v_add_u32 v207, 0xffff3000, v207\n" \
"v_add_u32 v208, 0xffff3000, v208\n" \
"v_mfma_f32_16x16x32_bf16 v[28:31], a[112:115], a[192:195], v[28:31]\n" \
"v_add_u32 v205, 0xffff3000, v205\n" \
"v_add_u32 v206, 0xffff3000, v206\n" \
"v_mfma_f32_16x16x32_bf16 v[28:31], a[116:119], a[196:199], v[28:31]\n" \
"buffer_load_dword v220, v221, s[56:59], s81 offen sc1\n" \
"v_mfma_f32_16x16x32_bf16 v[24:27], a[104:107], a[192:195], v[24:27]\n" \
"v_add_u32 v209, 0xffff3000, v209\n" \
"v_add_u32 v210, 0xffff3000, v210\n" \
"v_mfma_f32_16x16x32_bf16 v[24:27], a[108:111], a[196:199], v[24:27]\n" \
"s_waitcnt vmcnt(12) lgkmcnt(0)\n" \
"v_or_b32 v153, v153, v144\n" \
"v_ashrrev_i32 v153, 31, v153\n" \
"v_mfma_f32_16x16x32_bf16 v[64:67], a[96:99], a[192:195], v[64:67]\n" \
"v_and_b32 v153, 0xff800000, v153\n" \
"v_mov_b32 v161, v153\n" \
"v_mfma_f32_16x16x32_bf16 v[64:67], a[100:103], a[196:199], v[64:67]\n" \
"s_barrier\n" \
"v_mfma_f32_16x16x32_bf16 v[72:75], a[64:67], a[208:211], v[72:75]\n" \
"ds_read_b64_tr_b16 a[96:97], v209 offset:2048\n" \
"v_mov_b32 v169, v153\n" \
"v_mfma_f32_16x16x32_bf16 v[72:75], a[68:71], a[212:215], v[72:75]\n" \
"ds_read_b64_tr_b16 a[98:99], v209 offset:3072\n" \
"v_mov_b32 v177, v153\n" \
"v_mfma_f32_16x16x32_bf16 v[80:83], a[72:75], a[208:211], v[80:83]\n" \
"s_add_u32 m0, s95, 118784\n" \
"ds_read_b128 a[64:67], v207 offset:0\n" \
"ds_read_b64_tr_b16 a[100:101], v209 offset:14336\n" \
"v_mfma_f32_16x16x32_bf16 v[80:83], a[76:79], a[212:215], v[80:83]\n" \
"global_load_lds_dwordx4 v252, s[42:43] offset:0\n" \
"v_sub_u32 v157, -8, v147\n" \
"v_mfma_f32_16x16x32_bf16 v[84:87], a[72:75], a[216:219], v[84:87]\n" \
"ds_read_b128 a[68:71], v208 offset:0\n" \
"ds_read_b64_tr_b16 a[102:103], v209 offset:15360\n" \
"v_mfma_f32_16x16x32_bf16 v[84:87], a[76:79], a[220:223], v[84:87]\n" \
"ds_read_b64_tr_b16 a[104:105], v209 offset:0\n" \
"v_ashrrev_i32 v157, 31, v157\n" \
"v_mfma_f32_16x16x32_bf16 v[44:47], a[200:203], a[72:75], v[44:47]\n" \
"ds_read_b64_tr_b16 a[106:107], v209 offset:1024\n" \
"v_and_b32 v157, 0xff800000, v157\n" \
"v_mfma_f32_16x16x32_bf16 v[44:47], a[204:207], a[76:79], v[44:47]\n" \
"global_load_lds_dwordx4 v252, s[42:43] offset:2048\n" \
"v_mov_b32 v165, v157\n" \
"v_mfma_f32_16x16x32_bf16 v[52:55], a[200:203], a[80:83], v[52:55]\n" \
"ds_read_b128 a[72:75], v205 offset:0\n" \
"v_mov_b32 v173, v157\n" \
"v_mfma_f32_16x16x32_bf16 v[52:55], a[204:207], a[84:87], v[52:55]\n" \
"ds_read_b128 a[76:79], v206 offset:0\n" \
"v_add_u32 v149, 1, v146\n" \
"v_mfma_f32_16x16x32_bf16 v[92:95], a[80:83], a[216:219], v[92:95]\n" \
"ds_read_b64_tr_b16 a[108:109], v209 offset:12288\n" \
"v_or3_b32 v149, v149, v145, s86\n" \
"v_mfma_f32_16x16x32_bf16 v[92:95], a[84:87], a[220:223], v[92:95]\n" \
"global_load_lds_dwordx4 v252, s[44:45] offset:1024\n" \
"v_ashrrev_i32 v149, 31, v149\n" \
"v_mfma_f32_16x16x32_bf16 v[88:91], a[80:83], a[208:211], v[88:91]\n" \
"ds_read_b64_tr_b16 a[110:111], v209 offset:13312\n" \
"v_and_b32 v149, 0xff800000, v149\n" \
"v_mfma_f32_16x16x32_bf16 v[88:91], a[84:87], a[212:215], v[88:91]\n" \
"ds_read_b64_tr_b16 a[112:113], v210 offset:0\n" \
"v_subrev_u32 v154, 2, v147\n" \
"v_mfma_f32_16x16x32_bf16 v[96:99], a[88:91], a[208:211], v[96:99]\n" \
"ds_read_b128 a[80:83], v205 offset:4096\n" \
"v_sub_u32 v144, 9, v147\n" \
"v_mfma_f32_16x16x32_bf16 v[96:99], a[92:95], a[212:215], v[96:99]\n" \
"global_load_lds_dwordx4 v252, s[44:45] offset:3072\n" \
"v_or_b32 v154, v154, v144\n" \
"v_mfma_f32_16x16x32_bf16 v[100:103], a[88:91], a[216:219], v[100:103]\n" \
"ds_read_b128 a[84:87], v206 offset:4096\n" \
"v_ashrrev_i32 v154, 31, v154\n" \
"v_mfma_f32_16x16x32_bf16 v[100:103], a[92:95], a[220:223], v[100:103]\n" \
"ds_read_b64_tr_b16 a[114:115], v210 offset:1024\n" \
"v_and_b32 v154, 0xff800000, v154\n" \
"v_mfma_f32_16x16x32_bf16 v[60:63], a[200:203], a[88:91], v[60:63]\n" \
"s_add_u32 m0, s85, 135168\n" \
"ds_read_b64_tr_b16 a[116:117], v210 offset:12288\n" \
"v_mov_b32 v162, v154\n" \
"v_mfma_f32_16x16x32_bf16 v[60:63], a[204:207], a[92:95], v[60:63]\n" \
"buffer_load_dwordx4 v246, s[12:15], s46 offen lds\n" \
"v_mfma_f32_16x16x32_bf16 v[68:71], a[200:203], a[192:195], v[68:71]\n" \
"ds_read_b128 a[88:91], v205 offset:8192\n" \
"v_mov_b32 v170, v154\n" \
"v_mfma_f32_16x16x32_bf16 v[68:71], a[204:207], a[196:199], v[68:71]\n" \
"ds_read_b128 a[92:95], v206 offset:8192\n" \
"v_mov_b32 v178, v154\n" \
"v_mfma_f32_16x16x32_bf16 v[108:111], a[192:195], a[216:219], v[108:111]\n" \
"s_add_u32 m0, s84, 143360\n" \
"ds_read_b64_tr_b16 a[118:119], v210 offset:13312\n" \
"v_sub_u32 v158, -7, v147\n" \
"v_mfma_f32_16x16x32_bf16 v[108:111], a[196:199], a[220:223], v[108:111]\n" \
"global_load_lds_dwordx4 v253, s[48:49] offset:-4096\n" \
"v_ashrrev_i32 v158, 31, v158\n" \
"v_mfma_f32_16x16x32_bf16 v[104:107], a[192:195], a[208:211], v[104:107]\n" \
"v_and_b32 v158, 0xff800000, v158\n" \
"v_mov_b32 v166, v158\n" \
"v_mfma_f32_16x16x32_bf16 v[104:107], a[196:199], a[212:215], v[104:107]\n" \
"s_waitcnt lgkmcnt(2)\n" \
"v_mov_b32 v174, v158\n" \
"v_add_u32 v150, 2, v146\n" \
"v_mfma_f32_16x16x32_bf16 v[32:35], a[96:99], a[64:67], v[32:35]\n" \
"ds_read_b128 a[192:195], v205 offset:12288\n" \
"v_or3_b32 v150, v150, v145, s86\n" \
"v_mfma_f32_16x16x32_bf16 v[32:35], a[100:103], a[68:71], v[32:35]\n" \
"global_load_lds_dwordx4 v253, s[48:49] offset:-2048\n" \
"v_ashrrev_i32 v150, 31, v150\n" \
"v_mfma_f32_16x16x32_bf16 v[40:43], a[96:99], a[72:75], v[40:43]\n" \
"ds_read_b128 a[196:199], v206 offset:12288\n" \
"v_and_b32 v150, 0xff800000, v150\n" \
"v_mfma_f32_16x16x32_bf16 v[40:43], a[100:103], a[76:79], v[40:43]\n" \
"ds_read_b64_tr_b16 a[208:209], v209 offset:4096\n" \
"v_subrev_u32 v155, 3, v147\n" \
"v_mfma_f32_16x16x32_bf16 v[0:3], a[104:107], a[72:75], v[0:3]\n" \
"ds_read_b64_tr_b16 a[210:211], v209 offset:5120\n" \
"v_sub_u32 v144, 10, v147\n" \
"v_mfma_f32_16x16x32_bf16 v[0:3], a[108:111], a[76:79], v[0:3]\n" \
"s_waitcnt lgkmcnt(3)\n" \
"global_load_lds_dwordx4 v253, s[48:49] offset:0\n" \
"v_or_b32 v155, v155, v144\n" \
"v_mfma_f32_16x16x32_bf16 v[4:7], a[112:115], a[72:75], v[4:7]\n" \
"ds_read_b64_tr_b16 a[212:213], v209 offset:16384\n" \
"v_ashrrev_i32 v155, 31, v155\n" \
"v_mfma_f32_16x16x32_bf16 v[4:7], a[116:119], a[76:79], v[4:7]\n" \
"ds_read_b64_tr_b16 a[214:215], v209 offset:17408\n" \
"v_and_b32 v155, 0xff800000, v155\n" \
"v_mfma_f32_16x16x32_bf16 v[12:15], a[112:115], a[80:83], v[12:15]\n" \
"ds_read_b64_tr_b16 a[216:217], v210 offset:4096\n" \
"v_mov_b32 v163, v155\n" \
"v_mfma_f32_16x16x32_bf16 v[12:15], a[116:119], a[84:87], v[12:15]\n" \
"global_load_lds_dwordx4 v253, s[50:51] offset:-3072\n" \
"v_mov_b32 v171, v155\n" \
"v_mfma_f32_16x16x32_bf16 v[8:11], a[104:107], a[80:83], v[8:11]\n" \
"ds_read_b64_tr_b16 a[218:219], v210 offset:5120\n" \
"v_mov_b32 v179, v155\n" \
"v_mfma_f32_16x16x32_bf16 v[8:11], a[108:111], a[84:87], v[8:11]\n" \
"ds_read_b64_tr_b16 a[220:221], v210 offset:16384\n" \
"v_sub_u32 v159, -6, v147\n" \
"v_mfma_f32_16x16x32_bf16 v[48:51], a[96:99], a[80:83], v[48:51]\n" \
"ds_read_b64_tr_b16 a[222:223], v210 offset:17408\n" \
"v_ashrrev_i32 v159, 31, v159\n" \
"v_mfma_f32_16x16x32_bf16 v[48:51], a[100:103], a[84:87], v[48:51]\n" \
"global_load_lds_dwordx4 v253, s[50:51] offset:-1024\n" \
"v_and_b32 v159, 0xff800000, v159\n" \
"v_mfma_f32_16x16x32_bf16 v[56:59], a[96:99], a[88:91], v[56:59]\n" \
"ds_read_b64_tr_b16 a[200:201], v210 offset:2048\n" \
"v_mov_b32 v167, v159\n" \
"v_mfma_f32_16x16x32_bf16 v[56:59], a[100:103], a[92:95], v[56:59]\n" \
"ds_read_b64_tr_b16 a[202:203], v210 offset:3072\n" \
"v_mov_b32 v175, v159\n" \
"v_mfma_f32_16x16x32_bf16 v[16:19], a[104:107], a[88:91], v[16:19]\n" \
"ds_read_b64_tr_b16 a[204:205], v210 offset:14336\n" \
"v_add_u32 v151, 3, v146\n" \
"v_mfma_f32_16x16x32_bf16 v[16:19], a[108:111], a[92:95], v[16:19]\n" \
"global_load_lds_dwordx4 v253, s[50:51] offset:1024\n" \
"v_or3_b32 v151, v151, v145, s86\n" \
"v_mfma_f32_16x16x32_bf16 v[20:23], a[112:115], a[88:91], v[20:23]\n" \
"ds_read_b64_tr_b16 a[206:207], v210 offset:15360\n" \
"v_ashrrev_i32 v151, 31, v151\n" \
"v_mfma_f32_16x16x32_bf16 v[20:23], a[116:119], a[92:95], v[20:23]\n" \
"s_waitcnt lgkmcnt(3)\n" \
"v_add_u32 v207, 0xffff0000, v207\n" \
"v_add_u32 v208, 0xffff0000, v208\n" \
"v_mfma_f32_16x16x32_bf16 v[28:31], a[112:115], a[192:195], v[28:31]\n" \
"v_add_u32 v205, 0xffff0000, v205\n" \
"v_add_u32 v206, 0xffff0000, v206\n" \
"v_mfma_f32_16x16x32_bf16 v[28:31], a[116:119], a[196:199], v[28:31]\n" \
"v_add_u32 v209, 0xffff0000, v209\n" \
"v_add_u32 v210, 0xffff0000, v210\n" \
"v_mfma_f32_16x16x32_bf16 v[24:27], a[104:107], a[192:195], v[24:27]\n" \
"v_and_b32 v151, 0xff800000, v151\n" \
"v_mov_b32 v144, 0x3e8293ee\n" \
"v_mfma_f32_16x16x32_bf16 v[24:27], a[108:111], a[196:199], v[24:27]\n" \
"s_waitcnt vmcnt(12) lgkmcnt(0)\n" \
"s_add_u32 s90, s95, 117760\n" \
"v_add_u32 v223, s90, v248\n" \
"v_mfma_f32_16x16x32_bf16 v[64:67], a[96:99], a[192:195], v[64:67]\n" \
"s_cmp_lg_u32 s89, 0\n" \
"s_cselect_b64 s[92:93], -1, 0\n" \
"v_xor_b32 v145, 0x2000, v223\n" \
"v_mfma_f32_16x16x32_bf16 v[64:67], a[100:103], a[196:199], v[64:67]\n" \
"s_barrier\n" \
"v_mfma_f32_16x16x32_bf16 v[72:75], a[64:67], a[208:211], v[72:75]\n" \
"s_waitcnt vmcnt(11)\n" \
"ds_read_b64_tr_b16 a[96:97], v209 offset:2048\n" \
"v_cmp_ne_u32 vcc, s60, v220\n" \
"v_mfma_f32_16x16x32_bf16 v[72:75], a[68:71], a[212:215], v[72:75]\n" \
"s_cmp_eq_u32 s89, 0\n" \
"s_cselect_b32 s16, 0, 0xff00\n" \
"ds_read_b64_tr_b16 a[98:99], v209 offset:3072\n" \
"v_mfma_f32_16x16x32_bf16 v[80:83], a[72:75], a[208:211], v[80:83]\n" \
"s_mov_b32 s17, s16\n" \
"ds_read_b128 a[64:67], v207 offset:0\n" \
"ds_read_b64_tr_b16 a[100:101], v209 offset:14336\n" \
"v_mfma_f32_16x16x32_bf16 v[80:83], a[76:79], a[212:215], v[80:83]\n" \
"s_and_b32 vcc_hi, vcc_lo, s80\n" \
"s_cbranch_scc1 Lpl2_%=\n" \
"Lpr2_%=:\n" \
"v_mfma_f32_16x16x32_bf16 v[84:87], a[72:75], a[216:219], v[84:87]\n" \
"s_add_u32 m0, s75, s95\n" \
"ds_read_b128 a[68:71], v208 offset:0\n" \
"ds_read_b64_tr_b16 a[102:103], v209 offset:15360\n" \
"v_mfma_f32_16x16x32_bf16 v[84:87], a[76:79], a[220:223], v[84:87]\n" \
"ds_read_b64_tr_b16 a[104:105], v209 offset:0\n" \
"global_load_lds_dwordx4 v222, s[36:37] offset:0 sc1\n" \
"v_mfma_f32_16x16x32_bf16 v[44:47], a[200:203], a[72:75], v[44:47]\n" \
"ds_read_b64_tr_b16 a[106:107], v209 offset:1024\n" \
"v_add_u32 v146, 0x1c90080, v249\n" \
"v_mfma_f32_16x16x32_bf16 v[44:47], a[204:207], a[76:79], v[44:47]\n" \
"v_cndmask_b32 v146, v249, v146, s[16:17]\n" \
"v_mfma_f32_16x16x32_bf16 v[52:55], a[200:203], a[80:83], v[52:55]\n" \
"ds_read_b128 a[72:75], v205 offset:0\n" \
"v_mfma_f32_16x16x32_bf16 v[52:55], a[204:207], a[84:87], v[52:55]\n" \
"ds_read_b128 a[76:79], v206 offset:0\n" \
"global_load_lds_dwordx4 v222, s[36:37] offset:1024 sc1\n" \
"v_mfma_f32_16x16x32_bf16 v[92:95], a[80:83], a[216:219], v[92:95]\n" \
"ds_read_b64_tr_b16 a[108:109], v209 offset:12288\n" \
"v_mfma_f32_16x16x32_bf16 v[92:95], a[84:87], a[220:223], v[92:95]\n" \
"v_mfma_f32_16x16x32_bf16 v[88:91], a[80:83], a[208:211], v[88:91]\n" \
"ds_read_b64_tr_b16 a[110:111], v209 offset:13312\n" \
"v_mfma_f32_16x16x32_bf16 v[88:91], a[84:87], a[212:215], v[88:91]\n" \
"ds_read_b64_tr_b16 a[112:113], v210 offset:0\n" \
"global_load_lds_dwordx4 v222, s[36:37] offset:2048 sc1\n" \
"v_mfma_f32_16x16x32_bf16 v[96:99], a[88:91], a[208:211], v[96:99]\n" \
"ds_read_b128 a[80:83], v205 offset:4096\n" \
"v_mfma_f32_16x16x32_bf16 v[96:99], a[92:95], a[212:215], v[96:99]\n" \
"v_mfma_f32_16x16x32_bf16 v[100:103], a[88:91], a[216:219], v[100:103]\n" \
"ds_read_b128 a[84:87], v206 offset:4096\n" \
"v_mfma_f32_16x16x32_bf16 v[100:103], a[92:95], a[220:223], v[100:103]\n" \
"ds_read_b64_tr_b16 a[114:115], v210 offset:1024\n" \
"global_load_lds_dwordx4 v222, s[36:37] offset:3072 sc1\n" \
"v_mfma_f32_16x16x32_bf16 v[60:63], a[200:203], a[88:91], v[60:63]\n" \
"s_add_u32 s36, s36, 0x1000\n" \
"ds_read_b64_tr_b16 a[116:117], v210 offset:12288\n" \
"v_mfma_f32_16x16x32_bf16 v[60:63], a[204:207], a[92:95], v[60:63]\n" \
"v_mfma_f32_16x16x32_bf16 v[68:71], a[200:203], a[192:195], v[68:71]\n" \
"ds_read_b128 a[88:91], v205 offset:8192\n" \
"s_add_u32 m0, m0, 0x5000\n" \
"v_mfma_f32_16x16x32_bf16 v[68:71], a[204:207], a[196:199], v[68:71]\n" \
"ds_read_b128 a[92:95], v206 offset:8192\n" \
"global_load_lds_dwordx4 v216, s[38:39] offset:-4096\n" \
"v_mfma_f32_16x16x32_bf16 v[108:111], a[192:195], a[216:219], v[108:111]\n" \
"ds_read_b64_tr_b16 a[118:119], v210 offset:13312\n" \
"v_mfma_f32_16x16x32_bf16 v[108:111], a[196:199], a[220:223], v[108:111]\n" \
"v_mfma_f32_16x16x32_bf16 v[104:107], a[192:195], a[208:211], v[104:107]\n" \
"v_mfma_f32_16x16x32_bf16 v[104:107], a[196:199], a[212:215], v[104:107]\n" \
"s_waitcnt lgkmcnt(2)\n" \
"global_load_lds_dwordx4 v217, s[38:39] offset:-3072\n" \
"v_mfma_f32_16x16x32_bf16 v[32:35], a[96:99], a[64:67], v[32:35]\n" \
"ds_read_b128 a[192:195], v205 offset:12288\n" \
"v_mfma_f32_16x16x32_bf16 v[32:35], a[100:103], a[68:71], v[32:35]\n" \
"v_mfma_f32_16x16x32_bf16 v[40:43], a[96:99], a[72:75], v[40:43]\n" \
"ds_read_b128 a[196:199], v206 offset:12288\n" \
"v_mfma_f32_16x16x32_bf16 v[40:43], a[100:103], a[76:79], v[40:43]\n" \
"ds_read_b64_tr_b16 a[208:209], v209 offset:4096\n" \
"global_load_lds_dwordx4 v218, s[38:39] offset:-2048\n" \
"v_mfma_f32_16x16x32_bf16 v[0:3], a[104:107], a[72:75], v[0:3]\n" \
"ds_read_b64_tr_b16 a[210:211], v209 offset:5120\n" \
"v_mfma_f32_16x16x32_bf16 v[0:3], a[108:111], a[76:79], v[0:3]\n" \
"s_waitcnt lgkmcnt(3)\n" \
"v_mfma_f32_16x16x32_bf16 v[4:7], a[112:115], a[72:75], v[4:7]\n" \
"ds_read_b64_tr_b16 a[212:213], v209 offset:16384\n" \
"v_mfma_f32_16x16x32_bf16 v[4:7], a[116:119], a[76:79], v[4:7]\n" \
"ds_read_b64_tr_b16 a[214:215], v209 offset:17408\n" \
"global_load_lds_dwordx4 v219, s[38:39] offset:-1024\n" \
"v_mfma_f32_16x16x32_bf16 v[12:15], a[112:115], a[80:83], v[12:15]\n" \
"s_add_u32 s38, s38, 0x20000\n" \
"s_addc_u32 s39, s39, 0\n" \
"ds_read_b64_tr_b16 a[216:217], v210 offset:4096\n" \
"v_mfma_f32_16x16x32_bf16 v[12:15], a[116:119], a[84:87], v[12:15]\n" \
"s_sub_u32 s75, s75, 32768\n" \
"s_cselect_b32 s75, 131072, s75\n" \
"v_mfma_f32_16x16x32_bf16 v[8:11], a[104:107], a[80:83], v[8:11]\n" \
"ds_read_b64_tr_b16 a[218:219], v210 offset:5120\n" \
"v_mfma_f32_16x16x32_bf16 v[8:11], a[108:111], a[84:87], v[8:11]\n" \
"ds_read_b64_tr_b16 a[220:221], v210 offset:16384\n" \
"v_mfma_f32_16x16x32_bf16 v[48:51], a[96:99], a[80:83], v[48:51]\n" \
"ds_read_b64_tr_b16 a[222:223], v210 offset:17408\n" \
"v_mfma_f32_16x16x32_bf16 v[48:51], a[100:103], a[84:87], v[48:51]\n" \
"v_mfma_f32_16x16x32_bf16 v[56:59], a[96:99], a[88:91], v[56:59]\n" \
"ds_read_b64_tr_b16 a[200:201], v210 offset:2048\n" \
"v_mfma_f32_16x16x32_bf16 v[56:59], a[100:103], a[92:95], v[56:59]\n" \
"ds_read_b64_tr_b16 a[202:203], v210 offset:3072\n" \
"v_mfma_f32_16x16x32_bf16 v[16:19], a[104:107], a[88:91], v[16:19]\n" \
"ds_read_b64_tr_b16 a[204:205], v210 offset:14336\n" \
"v_mfma_f32_16x16x32_bf16 v[16:19], a[108:111], a[92:95], v[16:19]\n" \
"v_mfma_f32_16x16x32_bf16 v[20:23], a[112:115], a[88:91], v[20:23]\n" \
"ds_read_b64_tr_b16 a[206:207], v210 offset:15360\n" \
"v_mfma_f32_16x16x32_bf16 v[20:23], a[116:119], a[92:95], v[20:23]\n" \
"s_waitcnt lgkmcnt(3)\n" \
"v_add_u32 v207, 0x1d000, v207\n" \
"v_add_u32 v208, 0x1d000, v208\n" \
"v_mfma_f32_16x16x32_bf16 v[28:31], a[112:115], a[192:195], v[28:31]\n" \
"v_add_u32 v205, 0x1d000, v205\n" \
"v_add_u32 v206, 0x1d000, v206\n" \
"v_mfma_f32_16x16x32_bf16 v[28:31], a[116:119], a[196:199], v[28:31]\n" \
"v_add_u32 v209, 0x1d000, v209\n" \
"v_add_u32 v210, 0x1d000, v210\n" \
"v_mfma_f32_16x16x32_bf16 v[24:27], a[104:107], a[192:195], v[24:27]\n" \
"v_mfma_f32_16x16x32_bf16 v[24:27], a[108:111], a[196:199], v[24:27]\n" \
"s_waitcnt vmcnt(8) lgkmcnt(0)\n" \
"v_mfma_f32_16x16x32_bf16 v[64:67], a[96:99], a[192:195], v[64:67]\n" \
"v_mfma_f32_16x16x32_bf16 v[64:67], a[100:103], a[196:199], v[64:67]\n" \
"s_barrier\n" \
"v_mfma_f32_16x16x32_bf16 v[72:75], a[64:67], a[208:211], v[72:75]\n" \
"ds_read_b64_tr_b16 a[96:97], v209 offset:2048\n" \
"v_mfma_f32_16x16x32_bf16 v[72:75], a[68:71], a[212:215], v[72:75]\n" \
"ds_read_b64_tr_b16 a[98:99], v209 offset:3072\n" \
"v_mfma_f32_16x16x32_bf16 v[80:83], a[72:75], a[208:211], v[80:83]\n" \
"s_add_u32 m0, s75, s95\n" \
"ds_read_b128 a[64:67], v207 offset:0\n" \
"ds_read_b64_tr_b16 a[100:101], v209 offset:14336\n" \
"v_mfma_f32_16x16x32_bf16 v[80:83], a[76:79], a[212:215], v[80:83]\n" \
"global_load_lds_dwordx4 v222, s[36:37] offset:0 sc1\n" \
"v_mfma_f32_16x16x32_bf16 v[84:87], a[72:75], a[216:219], v[84:87]\n" \
"ds_read_b128 a[68:71], v208 offset:0\n" \
"ds_read_b64_tr_b16 a[102:103], v209 offset:15360\n" \
"v_mfma_f32_16x16x32_bf16 v[84:87], a[76:79], a[220:223], v[84:87]\n" \
"ds_read_b64_tr_b16 a[104:105], v209 offset:0\n" \
"v_mfma_f32_16x16x32_bf16 v[44:47], a[200:203], a[72:75], v[44:47]\n" \
"ds_read_b64_tr_b16 a[106:107], v209 offset:1024\n" \
"v_mfma_f32_16x16x32_bf16 v[44:47], a[204:207], a[76:79], v[44:47]\n" \
"global_load_lds_dwordx4 v222, s[36:37] offset:1024 sc1\n" \
"v_mfma_f32_16x16x32_bf16 v[52:55], a[200:203], a[80:83], v[52:55]\n" \
"ds_read_b128 a[72:75], v205 offset:0\n" \
"v_mfma_f32_16x16x32_bf16 v[52:55], a[204:207], a[84:87], v[52:55]\n" \
"ds_read_b128 a[76:79], v206 offset:0\n" \
"Lpg10886_%=:\n" \
"s_cbranch_execz Lpg12459_%=\n" \
"v_mfma_f32_16x16x32_bf16 v[92:95], a[80:83], a[216:219], v[92:95]\n" \
"ds_read_b64_tr_b16 a[108:109], v209 offset:12288\n" \
"v_mfma_f32_16x16x32_bf16 v[92:95], a[84:87], a[220:223], v[92:95]\n" \
"global_load_lds_dwordx4 v222, s[36:37] offset:2048 sc1\n" \
"v_mfma_f32_16x16x32_bf16 v[88:91], a[80:83], a[208:211], v[88:91]\n" \
"ds_read_b64_tr_b16 a[110:111], v209 offset:13312\n" \
"v_mfma_f32_16x16x32_bf16 v[88:91], a[84:87], a[212:215], v[88:91]\n" \
"ds_read_b64_tr_b16 a[112:113], v210 offset:0\n" \
"v_mfma_f32_16x16x32_bf16 v[96:99], a[88:91], a[208:211], v[96:99]\n" \
"ds_read_b128 a[80:83], v205 offset:4096\n" \
"v_mfma_f32_16x16x32_bf16 v[96:99], a[92:95], a[212:215], v[96:99]\n" \
"global_load_lds_dwordx4 v222, s[36:37] offset:3072 sc1\n" \
"v_mfma_f32_16x16x32_bf16 v[100:103], a[88:91], a[216:219], v[100:103]\n" \
"ds_read_b128 a[84:87], v206 offset:4096\n" \
"s_add_u32 s36, s36, 0x1000\n" \
"v_mfma_f32_16x16x32_bf16 v[100:103], a[92:95], a[220:223], v[100:103]\n" \
"ds_read_b64_tr_b16 a[114:115], v210 offset:1024\n" \
"v_mfma_f32_16x16x32_bf16 v[60:63], a[200:203], a[88:91], v[60:63]\n" \
"s_add_u32 m0, m0, 0x5000\n" \
"ds_read_b64_tr_b16 a[116:117], v210 offset:12288\n" \
"v_mfma_f32_16x16x32_bf16 v[60:63], a[204:207], a[92:95], v[60:63]\n" \
"global_load_lds_dwordx4 v216, s[38:39] offset:-4096\n" \
"v_mfma_f32_16x16x32_bf16 v[68:71], a[200:203], a[192:195], v[68:71]\n" \
"ds_read_b128 a[88:91], v205 offset:8192\n" \
"v_mfma_f32_16x16x32_bf16 v[68:71], a[204:207], a[196:199], v[68:71]\n" \
"ds_read_b128 a[92:95], v206 offset:8192\n" \
"v_mfma_f32_16x16x32_bf16 v[108:111], a[192:195], a[216:219], v[108:111]\n" \
"ds_read_b64_tr_b16 a[118:119], v210 offset:13312\n" \
"v_mfma_f32_16x16x32_bf16 v[108:111], a[196:199], a[220:223], v[108:111]\n" \
"global_load_lds_dwordx4 v217, s[38:39] offset:-3072\n" \
"v_mfma_f32_16x16x32_bf16 v[104:107], a[192:195], a[208:211], v[104:107]\n" \
"v_mfma_f32_16x16x32_bf16 v[104:107], a[196:199], a[212:215], v[104:107]\n" \
"s_waitcnt lgkmcnt(2)\n" \
"v_mfma_f32_16x16x32_bf16 v[32:35], a[96:99], a[64:67], v[32:35]\n" \
"ds_read_b128 a[192:195], v205 offset:12288\n" \
"v_mfma_f32_16x16x32_bf16 v[32:35], a[100:103], a[68:71], v[32:35]\n" \
"global_load_lds_dwordx4 v218, s[38:39] offset:-2048\n" \
"v_mfma_f32_16x16x32_bf16 v[40:43], a[96:99], a[72:75], v[40:43]\n" \
"ds_read_b128 a[196:199], v206 offset:12288\n" \
"v_mfma_f32_16x16x32_bf16 v[40:43], a[100:103], a[76:79], v[40:43]\n" \
"ds_read_b64_tr_b16 a[208:209], v209 offset:4096\n" \
"v_mfma_f32_16x16x32_bf16 v[0:3], a[104:107], a[72:75], v[0:3]\n" \
"ds_read_b64_tr_b16 a[210:211], v209 offset:5120\n" \
"v_mfma_f32_16x16x32_bf16 v[0:3], a[108:111], a[76:79], v[0:3]\n" \
"s_waitcnt lgkmcnt(3)\n" \
"global_load_lds_dwordx4 v219, s[38:39] offset:-1024\n" \
"v_mfma_f32_16x16x32_bf16 v[4:7], a[112:115], a[72:75], v[4:7]\n" \
"s_add_u32 s38, s38, 0x20000\n" \
"s_addc_u32 s39, s39, 0\n" \
"ds_read_b64_tr_b16 a[212:213], v209 offset:16384\n" \
"v_mfma_f32_16x16x32_bf16 v[4:7], a[116:119], a[76:79], v[4:7]\n" \
"s_sub_u32 s75, s75, 32768\n" \
"s_cselect_b32 s75, 131072, s75\n" \
"ds_read_b64_tr_b16 a[214:215], v209 offset:17408\n" \
"v_mfma_f32_16x16x32_bf16 v[48:51], a[96:99], a[80:83], v[48:51]\n" \
"s_add_u32 m0, s75, s95\n" \
"ds_read_b64_tr_b16 a[216:217], v210 offset:4096\n" \
"v_mfma_f32_16x16x32_bf16 v[48:51], a[100:103], a[84:87], v[48:51]\n" \
"global_load_lds_dwordx4 v222, s[36:37] offset:0 sc1\n" \
"v_mfma_f32_16x16x32_bf16 v[8:11], a[104:107], a[80:83], v[8:11]\n" \
"ds_read_b64_tr_b16 a[218:219], v210 offset:5120\n" \
"v_cvt_pk_bf16_f32 v112, v0, v1\n" \
"v_mfma_f32_16x16x32_bf16 v[8:11], a[108:111], a[84:87], v[8:11]\n" \
"ds_read_b64_tr_b16 a[220:221], v210 offset:16384\n" \
"v_cvt_pk_bf16_f32 v113, v2, v3\n" \
"v_mfma_f32_16x16x32_bf16 v[12:15], a[112:115], a[80:83], v[12:15]\n" \
"ds_read_b64_tr_b16 a[222:223], v210 offset:17408\n" \
"v_cvt_pk_bf16_f32 v114, v4, v5\n" \
"v_mfma_f32_16x16x32_bf16 v[12:15], a[116:119], a[84:87], v[12:15]\n" \
"global_load_lds_dwordx4 v222, s[36:37] offset:1024 sc1\n" \
"v_cvt_pk_bf16_f32 v115, v6, v7\n" \
"v_mfma_f32_16x16x32_bf16 v[56:59], a[96:99], a[88:91], v[56:59]\n" \
"ds_read_b64_tr_b16 a[200:201], v210 offset:2048\n" \
"v_mfma_f32_16x16x32_bf16 v[56:59], a[100:103], a[92:95], v[56:59]\n" \
"ds_read_b64_tr_b16 a[202:203], v210 offset:3072\n" \
"v_mfma_f32_16x16x32_bf16 v[16:19], a[104:107], a[88:91], v[16:19]\n" \
"ds_read_b64_tr_b16 a[204:205], v210 offset:14336\n" \
"v_cvt_pk_bf16_f32 v116, v8, v9\n" \
"v_mfma_f32_16x16x32_bf16 v[16:19], a[108:111], a[92:95], v[16:19]\n" \
"global_load_lds_dwordx4 v222, s[36:37] offset:2048 sc1\n" \
"v_cvt_pk_bf16_f32 v117, v10, v11\n" \
"v_mfma_f32_16x16x32_bf16 v[20:23], a[112:115], a[88:91], v[20:23]\n" \
"ds_read_b64_tr_b16 a[206:207], v210 offset:15360\n" \
"v_cvt_pk_bf16_f32 v118, v12, v13\n" \
"v_mfma_f32_16x16x32_bf16 v[20:23], a[116:119], a[92:95], v[20:23]\n" \
"s_waitcnt lgkmcnt(3)\n" \
"v_cvt_pk_bf16_f32 v119, v14, v15\n" \
"v_mfma_f32_16x16x32_bf16 v[64:67], a[96:99], a[192:195], v[64:67]\n" \
"v_mfma_f32_16x16x32_bf16 v[64:67], a[100:103], a[196:199], v[64:67]\n" \
"global_load_lds_dwordx4 v222, s[36:37] offset:3072 sc1\n" \
"v_mfma_f32_16x16x32_bf16 v[24:27], a[104:107], a[192:195], v[24:27]\n" \
"v_cvt_pk_bf16_f32 v120, v16, v17\n" \
"v_cvt_pk_bf16_f32 v121, v18, v19\n" \
"v_mfma_f32_16x16x32_bf16 v[24:27], a[108:111], a[196:199], v[24:27]\n" \
"s_waitcnt vmcnt(12) lgkmcnt(0)\n" \
"v_cvt_pk_bf16_f32 v122, v20, v21\n" \
"v_cvt_pk_bf16_f32 v123, v22, v23\n" \
"v_mfma_f32_16x16x32_bf16 v[28:31], a[112:115], a[192:195], v[28:31]\n" \
"v_mfma_f32_16x16x32_bf16 v[28:31], a[116:119], a[196:199], v[28:31]\n" \
"s_barrier\n" \
"v_mfma_f32_16x16x32_bf16 v[72:75], a[64:67], a[208:211], v[72:75]\n" \
"ds_read_b64_tr_b16 a[32:33], v214 offset:16384\n" \
"v_mfma_f32_16x16x32_bf16 v[72:75], a[68:71], a[212:215], v[72:75]\n" \
"v_mfma_f32_16x16x32_bf16 v[44:47], a[200:203], a[72:75], v[44:47]\n" \
"s_add_u32 s36, s36, 0x1000\n" \
"s_add_u32 m0, m0, 0x5000\n" \
"ds_read_b64_tr_b16 a[34:35], v214 offset:18432\n" \
"v_mfma_f32_16x16x32_bf16 v[44:47], a[204:207], a[76:79], v[44:47]\n" \
"ds_read_b64_tr_b16 a[36:37], v214 offset:24576\n" \
"global_load_lds_dwordx4 v216, s[38:39] offset:-4096\n" \
"v_mfma_f32_16x16x32_bf16 v[80:83], a[72:75], a[208:211], v[80:83]\n" \
"v_cvt_pk_bf16_f32 v124, v24, v25\n" \
"v_cvt_pk_bf16_f32 v125, v26, v27\n" \
"v_mfma_f32_16x16x32_bf16 v[80:83], a[76:79], a[212:215], v[80:83]\n" \
"ds_read_b64_tr_b16 a[38:39], v214 offset:26624\n" \
"v_cvt_pk_bf16_f32 v126, v28, v29\n" \
"v_mfma_f32_16x16x32_bf16 v[84:87], a[72:75], a[216:219], v[84:87]\n" \
"ds_read_b128 a[0:3], v213 offset:0\n" \
"v_cvt_pk_bf16_f32 v127, v30, v31\n" \
"v_mfma_f32_16x16x32_bf16 v[84:87], a[76:79], a[220:223], v[84:87]\n" \
"global_load_lds_dwordx4 v217, s[38:39] offset:-3072\n" \
"v_cvt_pk_bf16_f32 v128, v32, v33\n" \
"v_mfma_f32_16x16x32_bf16 v[52:55], a[200:203], a[80:83], v[52:55]\n" \
"ds_read_b128 a[4:7], v213 offset:2048\n" \
"v_cvt_pk_bf16_f32 v129, v34, v35\n" \
"v_mfma_f32_16x16x32_bf16 v[52:55], a[204:207], a[84:87], v[52:55]\n" \
"ds_read_b64_tr_b16 a[40:41], v215 offset:16384\n" \
"v_cvt_pk_bf16_f32 v130, v72, v73\n" \
"v_mfma_f32_16x16x32_bf16 v[88:91], a[80:83], a[208:211], v[88:91]\n" \
"v_cvt_pk_bf16_f32 v131, v74, v75\n" \
"v_mfma_f32_16x16x32_bf16 v[88:91], a[84:87], a[212:215], v[88:91]\n" \
"ds_read_b64_tr_b16 a[42:43], v215 offset:18432\n" \
"global_load_lds_dwordx4 v218, s[38:39] offset:-2048\n" \
"v_mfma_f32_16x16x32_bf16 v[92:95], a[80:83], a[216:219], v[92:95]\n" \
"ds_read_b64_tr_b16 a[44:45], v215 offset:24576\n" \
"v_mfma_f32_16x16x32_bf16 v[92:95], a[84:87], a[220:223], v[92:95]\n" \
"ds_write_b128 v145, v[128:131]\n" \
"v_cvt_pk_bf16_f32 v132, v40, v41\n" \
"v_mfma_f32_16x16x32_bf16 v[60:63], a[200:203], a[88:91], v[60:63]\n" \
"ds_read_b64_tr_b16 a[46:47], v215 offset:26624\n" \
"v_cvt_pk_bf16_f32 v133, v42, v43\n" \
"v_mfma_f32_16x16x32_bf16 v[60:63], a[204:207], a[92:95], v[60:63]\n" \
"ds_read_b128 a[8:11], v213 offset:256\n" \
"global_load_lds_dwordx4 v219, s[38:39] offset:-1024\n" \
"v_mfma_f32_16x16x32_bf16 v[96:99], a[88:91], a[208:211], v[96:99]\n" \
"s_add_u32 s38, s38, 0x20000\n" \
"s_addc_u32 s39, s39, 0\n" \
"v_cvt_pk_bf16_f32 v134, v44, v45\n" \
"v_mfma_f32_16x16x32_bf16 v[96:99], a[92:95], a[212:215], v[96:99]\n" \
"s_sub_u32 s75, s75, 32768\n" \
"s_cselect_b32 s75, 131072, s75\n" \
"ds_read_b128 a[12:15], v213 offset:2304\n" \
"v_mfma_f32_16x16x32_bf16 v[100:103], a[88:91], a[216:219], v[100:103]\n" \
"s_add_u32 m0, s75, s95\n" \
"ds_read_b128 a[16:19], v213 offset:4096\n" \
"v_cvt_pk_bf16_f32 v135, v46, v47\n" \
"v_mfma_f32_16x16x32_bf16 v[100:103], a[92:95], a[220:223], v[100:103]\n" \
"global_load_lds_dwordx4 v222, s[36:37] offset:0 sc1\n" \
"v_cvt_pk_bf16_f32 v136, v48, v49\n" \
"v_mfma_f32_16x16x32_bf16 v[68:71], a[200:203], a[192:195], v[68:71]\n" \
"ds_read_b128 a[20:23], v213 offset:6144\n" \
"v_cvt_pk_bf16_f32 v137, v50, v51\n" \
"v_mfma_f32_16x16x32_bf16 v[68:71], a[204:207], a[196:199], v[68:71]\n" \
"v_cvt_pk_bf16_f32 v138, v52, v53\n" \
"v_cvt_pk_bf16_f32 v139, v54, v55\n" \
"v_mfma_f32_16x16x32_bf16 v[104:107], a[192:195], a[208:211], v[104:107]\n" \
"v_cvt_pk_bf16_f32 v140, v56, v57\n" \
"v_cvt_pk_bf16_f32 v141, v58, v59\n" \
"v_mfma_f32_16x16x32_bf16 v[104:107], a[196:199], a[212:215], v[104:107]\n" \
"global_load_lds_dwordx4 v222, s[36:37] offset:1024 sc1\n" \
"v_cvt_pk_bf16_f32 v142, v60, v61\n" \
"v_mfma_f32_16x16x32_bf16 v[108:111], a[192:195], a[216:219], v[108:111]\n" \
"v_cvt_pk_bf16_f32 v143, v62, v63\n" \
"v_mfma_f32_16x16x32_bf16 v[108:111], a[196:199], a[220:223], v[108:111]\n" \
"s_waitcnt lgkmcnt(0)\n" \
"v_mfma_f32_16x16x32_bf16 v[0:3], a[32:35], a[0:3], 0\n" \
"ds_read_b128 a[24:27], v213 offset:4352\n" \
"v_mfma_f32_16x16x32_bf16 v[0:3], a[36:39], a[4:7], v[0:3]\n" \
"ds_read_b128 a[28:31], v213 offset:6400\n" \
"global_load_lds_dwordx4 v222, s[36:37] offset:2048 sc1\n" \
"v_mfma_f32_16x16x32_bf16 v[4:7], a[40:43], a[0:3], 0\n" \
"s_sub_u32 s74, s74, 32768\n" \
"s_cselect_b32 s74, 131072, s74\n" \
"ds_read_b64_tr_b16 a[48:49], v214 offset:16896\n" \
"v_mfma_f32_16x16x32_bf16 v[4:7], a[44:47], a[4:7], v[4:7]\n" \
"ds_read_b64_tr_b16 a[50:51], v214 offset:18944\n" \
"v_cvt_pk_bf16_f32 v64, v64, v65\n" \
"v_mfma_f32_16x16x32_bf16 v[20:23], a[40:43], a[8:11], 0\n" \
"ds_read_b64_tr_b16 a[52:53], v214 offset:25088\n" \
"global_load_lds_dwordx4 v222, s[36:37] offset:3072 sc1\n" \
"v_mfma_f32_16x16x32_bf16 v[20:23], a[44:47], a[12:15], v[20:23]\n" \
"s_add_u32 s36, s36, 0x1000\n" \
"ds_read_b64_tr_b16 a[54:55], v214 offset:27136\n" \
"v_cvt_pk_bf16_f32 v65, v66, v67\n" \
"v_mfma_f32_16x16x32_bf16 v[16:19], a[32:35], a[8:11], 0\n" \
"s_add_u32 m0, m0, 0x5000\n" \
"ds_read_b64_tr_b16 a[56:57], v215 offset:16896\n" \
"v_cvt_pk_bf16_f32 v66, v68, v69\n" \
"v_mfma_f32_16x16x32_bf16 v[16:19], a[36:39], a[12:15], v[16:19]\n" \
"ds_read_b64_tr_b16 a[58:59], v215 offset:18944\n" \
"global_load_lds_dwordx4 v216, s[38:39] offset:-4096\n" \
"v_mfma_f32_16x16x32_bf16 v[32:35], a[32:35], a[16:19], 0\n" \
"ds_read_b64_tr_b16 a[60:61], v215 offset:25088\n" \
"v_cvt_pk_bf16_f32 v67, v70, v71\n" \
"v_mfma_f32_16x16x32_bf16 v[32:35], a[36:39], a[20:23], v[32:35]\n" \
"ds_read_b64_tr_b16 a[62:63], v215 offset:27136\n" \
"v_cvt_pk_bf16_f32 v68, v72, v73\n" \
"v_mfma_f32_16x16x32_bf16 v[36:39], a[40:43], a[16:19], 0\n" \
"global_load_lds_dwordx4 v217, s[38:39] offset:-3072\n" \
"v_add_u32 v213, s74, v211\n" \
"v_mfma_f32_16x16x32_bf16 v[36:39], a[44:47], a[20:23], v[36:39]\n" \
"s_waitcnt lgkmcnt(3)\n" \
"v_add_u32 v214, s74, v212\n" \
"v_xor_b32 v215, 32, v214\n" \
"v_mfma_f32_16x16x32_bf16 v[52:55], a[40:43], a[24:27], 0\n" \
"v_cvt_pk_bf16_f32 v69, v74, v75\n" \
"v_cvt_pk_bf16_f32 v70, v80, v81\n" \
"v_mfma_f32_16x16x32_bf16 v[52:55], a[44:47], a[28:31], v[52:55]\n" \
"s_waitcnt vmcnt(14) lgkmcnt(0)\n" \
"global_load_lds_dwordx4 v218, s[38:39] offset:-2048\n" \
"v_cvt_pk_bf16_f32 v71, v82, v83\n" \
"v_mfma_f32_16x16x32_bf16 v[48:51], a[32:35], a[24:27], 0\n" \
"global_load_lds_dwordx4 v219, s[38:39] offset:-1024\n" \
"v_cvt_pk_bf16_f32 v194, v84, v85\n" \
"v_mfma_f32_16x16x32_bf16 v[48:51], a[36:39], a[28:31], v[48:51]\n" \
"s_barrier\n" \
"v_cvt_pk_bf16_f32 v195, v86, v87\n" \
"v_mfma_f32_16x16x32_bf16 v[8:11], a[48:51], a[0:3], 0\n" \
"ds_read_b64 v[130:131], v223\n" \
"ds_read_b64_tr_b16 a[32:33], v214 offset:16384\n" \
"v_mfma_f32_16x16x32_bf16 v[8:11], a[52:55], a[4:7], v[8:11]\n" \
"ds_read_b64 v[192:193], v223 offset:8\n" \
"ds_read_b64_tr_b16 a[34:35], v214 offset:18432\n" \
"v_mfma_f32_16x16x32_bf16 v[12:15], a[56:59], a[0:3], 0\n" \
"s_add_u32 s38, s38, 0x20000\n" \
"s_addc_u32 s39, s39, 0\n" \
"ds_read_b64_tr_b16 a[36:37], v214 offset:24576\n" \
"v_mfma_f32_16x16x32_bf16 v[12:15], a[60:63], a[4:7], v[12:15]\n" \
"s_sub_u32 s75, s75, 32768\n" \
"s_cselect_b32 s75, 131072, s75\n" \
"ds_read_b128 a[0:3], v213 offset:0\n" \
"v_mfma_f32_16x16x32_bf16 v[28:31], a[56:59], a[8:11], 0\n" \
"s_add_u32 m0, s75, s95\n" \
"ds_read_b128 a[4:7], v213 offset:2048\n" \
"v_cvt_pk_bf16_f32 v72, v88, v89\n" \
"v_mfma_f32_16x16x32_bf16 v[28:31], a[60:63], a[12:15], v[28:31]\n" \
"ds_read_b64_tr_b16 a[38:39], v214 offset:26624\n" \
"global_load_lds_dwordx4 v222, s[36:37] offset:0 sc1\n" \
"v_mfma_f32_16x16x32_bf16 v[24:27], a[48:51], a[8:11], 0\n" \
"ds_read_b64_tr_b16 a[40:41], v215 offset:16384\n" \
"v_cvt_pk_bf16_f32 v73, v90, v91\n" \
"v_mfma_f32_16x16x32_bf16 v[24:27], a[52:55], a[12:15], v[24:27]\n" \
"s_waitcnt lgkmcnt(4)\n" \
"ds_read_b128 a[8:11], v213 offset:256\n" \
"global_load_lds_dwordx4 v222, s[36:37] offset:1024 sc1\n" \
"v_mfma_f32_16x16x32_bf16 v[40:43], a[48:51], a[16:19], 0\n" \
"ds_read_b128 a[12:15], v213 offset:2304\n" \
"v_cvt_pk_bf16_f32 v196, v92, v93\n" \
"v_mfma_f32_16x16x32_bf16 v[40:43], a[52:55], a[20:23], v[40:43]\n" \
"ds_read_b64_tr_b16 a[42:43], v215 offset:18432\n" \
"v_cvt_pk_bf16_f32 v197, v94, v95\n" \
"v_mfma_f32_16x16x32_bf16 v[44:47], a[56:59], a[16:19], 0\n" \
"ds_read_b64_tr_b16 a[44:45], v215 offset:24576\n" \
"global_load_lds_dwordx4 v222, s[36:37] offset:2048 sc1\n" \
"v_mfma_f32_16x16x32_bf16 v[44:47], a[60:63], a[20:23], v[44:47]\n" \
"ds_read_b128 a[16:19], v213 offset:4096\n" \
"v_cvt_pk_bf16_f32 v74, v96, v97\n" \
"v_mfma_f32_16x16x32_bf16 v[60:63], a[56:59], a[24:27], 0\n" \
"ds_read_b128 a[20:23], v213 offset:6144\n" \
"global_load_lds_dwordx4 v222, s[36:37] offset:3072 sc1\n" \
"v_mfma_f32_16x16x32_bf16 v[60:63], a[60:63], a[28:31], v[60:63]\n" \
"ds_read_b64_tr_b16 a[46:47], v215 offset:26624\n" \
"v_cvt_pk_bf16_f32 v75, v98, v99\n" \
"v_mfma_f32_16x16x32_bf16 v[56:59], a[48:51], a[24:27], 0\n" \
"v_cvt_pk_bf16_f32 v198, v100, v101\n" \
"v_cvt_pk_bf16_f32 v199, v102, v103\n" \
"v_mfma_f32_16x16x32_bf16 v[56:59], a[52:55], a[28:31], v[56:59]\n" \
"s_waitcnt lgkmcnt(3)\n" \
"v_cvt_pk_bf16_f32 v76, v104, v105\n" \
"v_cvt_pk_bf16_f32 v77, v106, v107\n" \
"v_mfma_f32_16x16x32_bf16 v[0:3], a[32:35], a[0:3], v[0:3]\n" \
"v_cvt_pk_bf16_f32 v200, v108, v109\n" \
"v_cvt_pk_bf16_f32 v201, v110, v111\n" \
"v_mfma_f32_16x16x32_bf16 v[176:179], v[64:67], v[124:127], v[176:179]\n" \
"v_mfma_f32_16x16x32_bf16 v[172:175], v[140:143], v[124:127], v[172:175]\n" \
"s_add_u32 s36, s36, 0x1000\n" \
"ds_read_b128 a[24:27], v213 offset:4352\n" \
"s_add_u32 m0, m0, 0x5000\n" \
"v_mfma_f32_16x16x32_bf16 v[0:3], a[36:39], a[4:7], v[0:3]\n" \
"s_waitcnt lgkmcnt(1)\n" \
"ds_read_b128 a[28:31], v213 offset:6400\n" \
"global_load_lds_dwordx4 v216, s[38:39] offset:-4096\n" \
"v_mfma_f32_16x16x32_bf16 v[168:171], v[140:143], v[120:123], v[168:171]\n" \
"v_mfma_f32_16x16x32_bf16 v[164:167], v[136:139], v[120:123], v[164:167]\n" \
"v_mfma_f32_16x16x32_bf16 v[160:163], v[136:139], v[116:119], v[160:163]\n" \
"v_mfma_f32_16x16x32_bf16 v[156:159], v[132:135], v[116:119], v[156:159]\n" \
"v_mfma_f32_16x16x32_bf16 v[152:155], v[132:135], v[112:115], v[152:155]\n" \
"v_mfma_f32_16x16x32_bf16 v[148:151], v[128:131], v[112:115], v[148:151]\n" \
"v_mfma_f32_16x16x32_bf16 v[4:7], a[40:43], a[0:3], v[4:7]\n" \
"s_sub_u32 s74, s74, 32768\n" \
"s_cselect_b32 s74, 131072, s74\n" \
"ds_read_b64_tr_b16 a[48:49], v214 offset:16896\n" \
"v_mfma_f32_16x16x32_bf16 v[4:7], a[44:47], a[4:7], v[4:7]\n" \
"ds_read_b64_tr_b16 a[50:51], v214 offset:18944\n" \
"v_max3_f32 v183, v172, v173, v174\n" \
"v_max3_f32 v182, v164, v165, v166\n" \
"v_max3_f32 v181, v156, v157, v158\n" \
"v_max3_f32 v180, v148, v149, v150\n" \
"v_max3_f32 v183, v183, v175, v176\n" \
"v_max3_f32 v182, v182, v167, v168\n" \
"v_mfma_f32_16x16x32_bf16 v[20:23], a[40:43], a[8:11], v[20:23]\n" \
"ds_read_b64_tr_b16 a[52:53], v214 offset:25088\n" \
"global_load_lds_dwordx4 v217, s[38:39] offset:-3072\n" \
"v_mfma_f32_16x16x32_bf16 v[20:23], a[44:47], a[12:15], v[20:23]\n" \
"ds_read_b64_tr_b16 a[54:55], v214 offset:27136\n" \
"v_max3_f32 v181, v181, v159, v160\n" \
"v_max3_f32 v180, v180, v151, v152\n" \
"v_max3_f32 v183, v183, v177, v178\n" \
"v_max3_f32 v182, v182, v169, v170\n" \
"v_max3_f32 v181, v181, v161, v162\n" \
"v_max3_f32 v180, v180, v153, v154\n" \
"v_mfma_f32_16x16x32_bf16 v[16:19], a[32:35], a[8:11], v[16:19]\n" \
"ds_read_b64_tr_b16 a[56:57], v215 offset:16896\n" \
"v_max_f32 v183, v183, v179\n" \
"v_mfma_f32_16x16x32_bf16 v[16:19], a[36:39], a[12:15], v[16:19]\n" \
"ds_read_b64_tr_b16 a[58:59], v215 offset:18944\n" \
"global_load_lds_dwordx4 v218, s[38:39] offset:-2048\n" \
"v_max_f32 v182, v182, v171\n" \
"v_max_f32 v181, v181, v163\n" \
"v_max_f32 v180, v180, v155\n" \
"v_mov_b32 v187, v183\n" \
"v_mov_b32 v186, v182\n" \
"v_mov_b32 v185, v181\n" \
"v_mfma_f32_16x16x32_bf16 v[32:35], a[32:35], a[16:19], v[32:35]\n" \
"ds_read_b64_tr_b16 a[60:61], v215 offset:25088\n" \
"v_mov_b32 v184, v180\n" \
"v_mfma_f32_16x16x32_bf16 v[32:35], a[36:39], a[20:23], v[32:35]\n" \
"ds_read_b64_tr_b16 a[62:63], v215 offset:27136\n" \
"v_permlane16_swap_b32 v187, v183\n" \
"v_permlane16_swap_b32 v186, v182\n" \
"v_permlane16_swap_b32 v185, v181\n" \
"v_permlane16_swap_b32 v184, v180\n" \
"v_max_f32 v183, v183, v187\n" \
"v_max_f32 v182, v182, v186\n" \
"v_mfma_f32_16x16x32_bf16 v[36:39], a[40:43], a[16:19], v[36:39]\n" \
"global_load_lds_dwordx4 v219, s[38:39] offset:-1024\n" \
"v_add_u32 v213, s74, v211\n" \
"v_mfma_f32_16x16x32_bf16 v[36:39], a[44:47], a[20:23], v[36:39]\n" \
"s_waitcnt lgkmcnt(3)\n" \
"v_max_f32 v181, v181, v185\n" \
"v_max_f32 v180, v180, v184\n" \
"v_mov_b32 v187, v183\n" \
"v_mov_b32 v186, v182\n" \
"v_mov_b32 v185, v181\n" \
"v_mov_b32 v184, v180\n" \
"s_add_u32 s38, s38, 0x20000\n" \
"s_addc_u32 s39, s39, 0\n" \
"v_mfma_f32_16x16x32_bf16 v[52:55], a[40:43], a[24:27], v[52:55]\n" \
"s_sub_u32 s75, s75, 32768\n" \
"s_cselect_b32 s75, 131072, s75\n" \
"s_add_u32 m0, s75, s95\n" \
"v_mfma_f32_16x16x32_bf16 v[52:55], a[44:47], a[28:31], v[52:55]\n" \
"s_waitcnt vmcnt(16) lgkmcnt(0)\n" \
"global_load_lds_dwordx4 v222, s[36:37] offset:0 sc1\n" \
"v_add_u32 v214, s74, v212\n" \
"v_permlane32_swap_b32 v187, v183\n" \
"v_permlane32_swap_b32 v186, v182\n" \
"v_permlane32_swap_b32 v185, v181\n" \
"v_permlane32_swap_b32 v184, v180\n" \
"v_max_f32 v183, v183, v187\n" \
"v_max_f32 v182, v182, v186\n" \
"v_mfma_f32_16x16x32_bf16 v[48:51], a[32:35], a[24:27], v[48:51]\n" \
"v_xor_b32 v215, 32, v214\n" \
"v_max_f32 v181, v181, v185\n" \
"v_mfma_f32_16x16x32_bf16 v[48:51], a[36:39], a[28:31], v[48:51]\n" \
"s_barrier\n" \
"v_max_f32 v180, v180, v184\n" \
"v_mul_f32 v183, v183, v144\n" \
"v_mul_f32 v182, v182, v144\n" \
"v_mfma_f32_16x16x32_bf16 v[8:11], a[48:51], a[0:3], v[8:11]\n" \
"ds_read_b64_tr_b16 a[32:33], v214 offset:16384\n" \
"v_mul_f32 v181, v181, v144\n" \
"v_mfma_f32_16x16x32_bf16 v[8:11], a[52:55], a[4:7], v[8:11]\n" \
"ds_read_b64_tr_b16 a[34:35], v214 offset:18432\n" \
"global_load_lds_dwordx4 v222, s[36:37] offset:1024 sc1\n" \
"v_mul_f32 v180, v180, v144\n" \
"v_fma_f32 v172, v172, v144, -v183\n" \
"v_mfma_f32_16x16x32_bf16 v[12:15], a[56:59], a[0:3], v[12:15]\n" \
"ds_read_b64_tr_b16 a[36:37], v214 offset:24576\n" \
"v_fma_f32 v164, v164, v144, -v182\n" \
"v_mfma_f32_16x16x32_bf16 v[12:15], a[60:63], a[4:7], v[12:15]\n" \
"ds_read_b128 a[0:3], v213 offset:0\n" \
"v_fma_f32 v156, v156, v144, -v181\n" \
"v_mfma_f32_16x16x32_bf16 v[28:31], a[56:59], a[8:11], v[28:31]\n" \
"ds_read_b128 a[4:7], v213 offset:2048\n" \
"global_load_lds_dwordx4 v222, s[36:37] offset:2048 sc1\n" \
"v_mfma_f32_16x16x32_bf16 v[28:31], a[60:63], a[12:15], v[28:31]\n" \
"ds_read_b64_tr_b16 a[38:39], v214 offset:26624\n" \
"v_exp_f32 v172, v172\n" \
"v_mfma_f32_16x16x32_bf16 v[24:27], a[48:51], a[8:11], v[24:27]\n" \
"ds_read_b64_tr_b16 a[40:41], v215 offset:16384\n" \
"v_fma_f32 v148, v148, v144, -v180\n" \
"v_mfma_f32_16x16x32_bf16 v[24:27], a[52:55], a[12:15], v[24:27]\n" \
"ds_read_b128 a[8:11], v213 offset:256\n" \
"global_load_lds_dwordx4 v222, s[36:37] offset:3072 sc1\n" \
"v_exp_f32 v164, v164\n" \
"v_mfma_f32_16x16x32_bf16 v[40:43], a[48:51], a[16:19], v[40:43]\n" \
"s_add_u32 s36, s36, 0x1000\n" \
"ds_read_b128 a[12:15], v213 offset:2304\n" \
"v_fma_f32 v173, v173, v144, -v183\n" \
"v_mfma_f32_16x16x32_bf16 v[40:43], a[52:55], a[20:23], v[40:43]\n" \
"s_add_u32 m0, m0, 0x5000\n" \
"ds_read_b64_tr_b16 a[42:43], v215 offset:18432\n" \
"v_exp_f32 v156, v156\n" \
"v_fma_f32 v165, v165, v144, -v182\n" \
"v_mfma_f32_16x16x32_bf16 v[44:47], a[56:59], a[16:19], v[44:47]\n" \
"ds_read_b64_tr_b16 a[44:45], v215 offset:24576\n" \
"global_load_lds_dwordx4 v216, s[38:39] offset:-4096\n" \
"v_mfma_f32_16x16x32_bf16 v[44:47], a[60:63], a[20:23], v[44:47]\n" \
"ds_read_b128 a[16:19], v213 offset:4096\n" \
"v_exp_f32 v148, v148\n" \
"v_mfma_f32_16x16x32_bf16 v[60:63], a[56:59], a[24:27], v[60:63]\n" \
"ds_read_b128 a[20:23], v213 offset:6144\n" \
"global_load_lds_dwordx4 v217, s[38:39] offset:-3072\n" \
"v_mfma_f32_16x16x32_bf16 v[60:63], a[60:63], a[28:31], v[60:63]\n" \
"ds_read_b64_tr_b16 a[46:47], v215 offset:26624\n" \
"v_fma_f32 v157, v157, v144, -v181\n" \
"v_mfma_f32_16x16x32_bf16 v[56:59], a[48:51], a[24:27], v[56:59]\n" \
"v_exp_f32 v173, v173\n" \
"v_mfma_f32_16x16x32_bf16 v[56:59], a[52:55], a[28:31], v[56:59]\n" \
"s_waitcnt lgkmcnt(3)\n" \
"v_fma_f32 v149, v149, v144, -v180\n" \
"v_exp_f32 v165, v165\n" \
"v_mfma_f32_16x16x32_bf16 v[0:3], a[32:35], a[0:3], v[0:3]\n" \
"ds_read_b128 a[24:27], v213 offset:4352\n" \
"v_fma_f32 v174, v174, v144, -v183\n" \
"v_mfma_f32_16x16x32_bf16 v[0:3], a[36:39], a[4:7], v[0:3]\n" \
"s_waitcnt lgkmcnt(1)\n" \
"ds_read_b128 a[28:31], v213 offset:6400\n" \
"global_load_lds_dwordx4 v218, s[38:39] offset:-2048\n" \
"v_exp_f32 v157, v157\n" \
"v_fma_f32 v166, v166, v144, -v182\n" \
"v_mfma_f32_16x16x32_bf16 v[4:7], a[40:43], a[0:3], v[4:7]\n" \
"s_sub_u32 s74, s74, 32768\n" \
"s_cselect_b32 s74, 131072, s74\n" \
"ds_read_b64_tr_b16 a[48:49], v214 offset:16896\n" \
"v_mfma_f32_16x16x32_bf16 v[4:7], a[44:47], a[4:7], v[4:7]\n" \
"ds_read_b64_tr_b16 a[50:51], v214 offset:18944\n" \
"global_load_lds_dwordx4 v219, s[38:39] offset:-1024\n" \
"v_exp_f32 v149, v149\n" \
"v_fma_f32 v158, v158, v144, -v181\n" \
"v_mfma_f32_16x16x32_bf16 v[20:23], a[40:43], a[8:11], v[20:23]\n" \
"s_add_u32 s38, s38, 0x20000\n" \
"s_addc_u32 s39, s39, 0\n" \
"ds_read_b64_tr_b16 a[52:53], v214 offset:25088\n" \
"v_mfma_f32_16x16x32_bf16 v[20:23], a[44:47], a[12:15], v[20:23]\n" \
"s_sub_u32 s75, s75, 32768\n" \
"s_cselect_b32 s75, 131072, s75\n" \
"ds_read_b64_tr_b16 a[54:55], v214 offset:27136\n" \
"v_exp_f32 v174, v174\n" \
"v_mfma_f32_16x16x32_bf16 v[16:19], a[32:35], a[8:11], v[16:19]\n" \
"s_add_u32 m0, s75, s95\n" \
"ds_read_b64_tr_b16 a[56:57], v215 offset:16896\n" \
"v_fma_f32 v150, v150, v144, -v180\n" \
"v_mfma_f32_16x16x32_bf16 v[16:19], a[36:39], a[12:15], v[16:19]\n" \
"ds_read_b64_tr_b16 a[58:59], v215 offset:18944\n" \
"global_load_lds_dwordx4 v222, s[36:37] offset:0 sc1\n" \
"v_exp_f32 v166, v166\n" \
"v_mfma_f32_16x16x32_bf16 v[32:35], a[32:35], a[16:19], v[32:35]\n" \
"ds_read_b64_tr_b16 a[60:61], v215 offset:25088\n" \
"v_fma_f32 v175, v175, v144, -v183\n" \
"v_mfma_f32_16x16x32_bf16 v[32:35], a[36:39], a[20:23], v[32:35]\n" \
"ds_read_b64_tr_b16 a[62:63], v215 offset:27136\n" \
"v_exp_f32 v158, v158\n" \
"v_fma_f32 v167, v167, v144, -v182\n" \
"v_mfma_f32_16x16x32_bf16 v[36:39], a[40:43], a[16:19], v[36:39]\n" \
"global_load_lds_dwordx4 v222, s[36:37] offset:1024 sc1\n" \
"v_add_u32 v213, s74, v211\n" \
"v_mfma_f32_16x16x32_bf16 v[36:39], a[44:47], a[20:23], v[36:39]\n" \
"s_waitcnt lgkmcnt(3)\n" \
"v_add_u32 v214, s74, v212\n" \
"v_xor_b32 v215, 32, v214\n" \
"v_mfma_f32_16x16x32_bf16 v[52:55], a[40:43], a[24:27], v[52:55]\n" \
"v_exp_f32 v150, v150\n" \
"v_mfma_f32_16x16x32_bf16 v[52:55], a[44:47], a[28:31], v[52:55]\n" \
"s_waitcnt vmcnt(18) lgkmcnt(0)\n" \
"global_load_lds_dwordx4 v222, s[36:37] offset:2048 sc1\n" \
"v_fma_f32 v159, v159, v144, -v181\n" \
"v_mfma_f32_16x16x32_bf16 v[48:51], a[32:35], a[24:27], v[48:51]\n" \
"v_exp_f32 v175, v175\n" \
"v_mfma_f32_16x16x32_bf16 v[48:51], a[36:39], a[28:31], v[48:51]\n" \
"s_barrier\n" \
"v_fma_f32 v151, v151, v144, -v180\n" \
"v_exp_f32 v167, v167\n" \
"v_mfma_f32_16x16x32_bf16 v[8:11], a[48:51], a[0:3], v[8:11]\n" \
"ds_read_b64_tr_b16 a[32:33], v214 offset:16384\n" \
"v_fma_f32 v176, v176, v144, -v183\n" \
"v_mfma_f32_16x16x32_bf16 v[8:11], a[52:55], a[4:7], v[8:11]\n" \
"ds_read_b64_tr_b16 a[34:35], v214 offset:18432\n" \
"global_load_lds_dwordx4 v222, s[36:37] offset:3072 sc1\n" \
"v_exp_f32 v159, v159\n" \
"v_mfma_f32_16x16x32_bf16 v[12:15], a[56:59], a[0:3], v[12:15]\n" \
"s_add_u32 s36, s36, 0x1000\n" \
"ds_read_b64_tr_b16 a[36:37], v214 offset:24576\n" \
"v_fma_f32 v168, v168, v144, -v182\n" \
"v_mfma_f32_16x16x32_bf16 v[12:15], a[60:63], a[4:7], v[12:15]\n" \
"s_add_u32 m0, m0, 0x5000\n" \
"ds_read_b128 a[0:3], v213 offset:0\n" \
"v_exp_f32 v151, v151\n" \
"v_fma_f32 v160, v160, v144, -v181\n" \
"v_mfma_f32_16x16x32_bf16 v[28:31], a[56:59], a[8:11], v[28:31]\n" \
"ds_read_b128 a[4:7], v213 offset:2048\n" \
"global_load_lds_dwordx4 v216, s[38:39] offset:-4096\n" \
"v_mfma_f32_16x16x32_bf16 v[28:31], a[60:63], a[12:15], v[28:31]\n" \
"ds_read_b64_tr_b16 a[38:39], v214 offset:26624\n" \
"v_exp_f32 v176, v176\n" \
"v_mfma_f32_16x16x32_bf16 v[24:27], a[48:51], a[8:11], v[24:27]\n" \
"ds_read_b64_tr_b16 a[40:41], v215 offset:16384\n" \
"v_fma_f32 v152, v152, v144, -v180\n" \
"v_mfma_f32_16x16x32_bf16 v[24:27], a[52:55], a[12:15], v[24:27]\n" \
"ds_read_b128 a[8:11], v213 offset:256\n" \
"global_load_lds_dwordx4 v217, s[38:39] offset:-3072\n" \
"v_exp_f32 v168, v168\n" \
"v_mfma_f32_16x16x32_bf16 v[40:43], a[48:51], a[16:19], v[40:43]\n" \
"ds_read_b128 a[12:15], v213 offset:2304\n" \
"v_fma_f32 v177, v177, v144, -v183\n" \
"v_mfma_f32_16x16x32_bf16 v[40:43], a[52:55], a[20:23], v[40:43]\n" \
"ds_read_b64_tr_b16 a[42:43], v215 offset:18432\n" \
"v_exp_f32 v160, v160\n" \
"v_fma_f32 v169, v169, v144, -v182\n" \
"v_mfma_f32_16x16x32_bf16 v[44:47], a[56:59], a[16:19], v[44:47]\n" \
"ds_read_b64_tr_b16 a[44:45], v215 offset:24576\n" \
"global_load_lds_dwordx4 v218, s[38:39] offset:-2048\n" \
"v_mfma_f32_16x16x32_bf16 v[44:47], a[60:63], a[20:23], v[44:47]\n" \
"ds_read_b128 a[16:19], v213 offset:4096\n" \
"v_exp_f32 v152, v152\n" \
"v_fma_f32 v161, v161, v144, -v181\n" \
"v_mfma_f32_16x16x32_bf16 v[60:63], a[56:59], a[24:27], v[60:63]\n" \
"ds_read_b128 a[20:23], v213 offset:6144\n" \
"global_load_lds_dwordx4 v219, s[38:39] offset:-1024\n" \
"v_mfma_f32_16x16x32_bf16 v[60:63], a[60:63], a[28:31], v[60:63]\n" \
"s_add_u32 s38, s38, 0x20000\n" \
"s_addc_u32 s39, s39, 0\n" \
"ds_read_b64_tr_b16 a[46:47], v215 offset:26624\n" \
"v_exp_f32 v177, v177\n" \
"v_mfma_f32_16x16x32_bf16 v[56:59], a[48:51], a[24:27], v[56:59]\n" \
"s_sub_u32 s75, s75, 32768\n" \
"s_cselect_b32 s75, 131072, s75\n" \
"v_fma_f32 v153, v153, v144, -v180\n" \
"v_mfma_f32_16x16x32_bf16 v[56:59], a[52:55], a[28:31], v[56:59]\n" \
"s_waitcnt lgkmcnt(3)\n" \
"v_exp_f32 v169, v169\n" \
"v_mfma_f32_16x16x32_bf16 v[0:3], a[32:35], a[0:3], v[0:3]\n" \
"s_add_u32 m0, s75, s95\n" \
"ds_read_b128 a[24:27], v213 offset:4352\n" \
"v_fma_f32 v178, v178, v144, -v183\n" \
"v_mfma_f32_16x16x32_bf16 v[0:3], a[36:39], a[4:7], v[0:3]\n" \
"s_waitcnt lgkmcnt(1)\n" \
"ds_read_b128 a[28:31], v213 offset:6400\n" \
"global_load_lds_dwordx4 v222, s[36:37] offset:0 sc1\n" \
"Lpg11447_%=:\n" \
"s_cbranch_execz Lpg12577_%=\n" \
"v_mfma_f32_16x16x32_bf16 v[4:7], a[40:43], a[0:3], v[4:7]\n" \
"s_sub_u32 s74, s74, 32768\n" \
"s_cselect_b32 s74, 131072, s74\n" \
"ds_read_b64_tr_b16 a[48:49], v214 offset:16896\n" \
"v_mfma_f32_16x16x32_bf16 v[4:7], a[44:47], a[4:7], v[4:7]\n" \
"ds_read_b64_tr_b16 a[50:51], v214 offset:18944\n" \
"v_exp_f32 v161, v161\n" \
"v_mfma_f32_16x16x32_bf16 v[20:23], a[40:43], a[8:11], v[20:23]\n" \
"ds_read_b64_tr_b16 a[52:53], v214 offset:25088\n" \
"v_fma_f32 v170, v170, v144, -v182\n" \
"v_mfma_f32_16x16x32_bf16 v[20:23], a[44:47], a[12:15], v[20:23]\n" \
"ds_read_b64_tr_b16 a[54:55], v214 offset:27136\n" \
"global_load_lds_dwordx4 v222, s[36:37] offset:1024 sc1\n" \
"v_exp_f32 v153, v153\n" \
"v_mfma_f32_16x16x32_bf16 v[16:19], a[32:35], a[8:11], v[16:19]\n" \
"ds_read_b64_tr_b16 a[56:57], v215 offset:16896\n" \
"v_fma_f32 v162, v162, v144, -v181\n" \
"v_mfma_f32_16x16x32_bf16 v[16:19], a[36:39], a[12:15], v[16:19]\n" \
"ds_read_b64_tr_b16 a[58:59], v215 offset:18944\n" \
"v_exp_f32 v178, v178\n" \
"v_mfma_f32_16x16x32_bf16 v[32:35], a[32:35], a[16:19], v[32:35]\n" \
"ds_read_b64_tr_b16 a[60:61], v215 offset:25088\n" \
"v_fma_f32 v154, v154, v144, -v180\n" \
"v_mfma_f32_16x16x32_bf16 v[32:35], a[36:39], a[20:23], v[32:35]\n" \
"ds_read_b64_tr_b16 a[62:63], v215 offset:27136\n" \
"global_load_lds_dwordx4 v222, s[36:37] offset:2048 sc1\n" \
"v_exp_f32 v170, v170\n" \
"v_mfma_f32_16x16x32_bf16 v[36:39], a[40:43], a[16:19], v[36:39]\n" \
"v_add_u32 v213, s74, v211\n" \
"v_add_u32 v214, s74, v212\n" \
"v_mfma_f32_16x16x32_bf16 v[36:39], a[44:47], a[20:23], v[36:39]\n" \
"s_waitcnt lgkmcnt(3)\n" \
"v_xor_b32 v215, 32, v214\n" \
"v_fma_f32 v179, v179, v144, -v183\n" \
"v_mfma_f32_16x16x32_bf16 v[52:55], a[40:43], a[24:27], v[52:55]\n" \
"v_exp_f32 v162, v162\n" \
"v_mfma_f32_16x16x32_bf16 v[52:55], a[44:47], a[28:31], v[52:55]\n" \
"s_waitcnt vmcnt(19) lgkmcnt(0)\n" \
"global_load_lds_dwordx4 v222, s[36:37] offset:3072 sc1\n" \
"v_fma_f32 v171, v171, v144, -v182\n" \
"v_mfma_f32_16x16x32_bf16 v[48:51], a[32:35], a[24:27], v[48:51]\n" \
"s_add_u32 s36, s36, 0x1000\n" \
"v_exp_f32 v154, v154\n" \
"v_mfma_f32_16x16x32_bf16 v[48:51], a[36:39], a[28:31], v[48:51]\n" \
"s_barrier\n" \
"v_fma_f32 v163, v163, v144, -v181\n" \
"v_exp_f32 v179, v179\n" \
"v_mfma_f32_16x16x32_bf16 v[8:11], a[48:51], a[0:3], v[8:11]\n" \
"s_add_u32 m0, m0, 0x5000\n" \
"ds_read_b64_tr_b16 a[32:33], v214 offset:16384\n" \
"v_fma_f32 v155, v155, v144, -v180\n" \
"v_mfma_f32_16x16x32_bf16 v[8:11], a[52:55], a[4:7], v[8:11]\n" \
"ds_read_b64_tr_b16 a[34:35], v214 offset:18432\n" \
"global_load_lds_dwordx4 v216, s[38:39] offset:-4096\n" \
"v_exp_f32 v171, v171\n" \
"v_exp_f32 v163, v163\n" \
"v_exp_f32 v155, v155\n" \
"v_mov_b32 v188, 0x3f803f80\n" \
"v_mfma_f32_16x16x32_bf16 v[12:15], a[56:59], a[0:3], v[12:15]\n" \
"ds_read_b64_tr_b16 a[36:37], v214 offset:24576\n" \
"v_mov_b32 v189, 0x3f803f80\n" \
"v_mfma_f32_16x16x32_bf16 v[12:15], a[60:63], a[4:7], v[12:15]\n" \
"ds_read_b128 a[0:3], v213 offset:0\n" \
"v_mov_b32 v190, 0x3f803f80\n" \
"v_mov_b32 v191, 0x3f803f80\n" \
"v_mfma_f32_16x16x32_bf16 v[28:31], a[56:59], a[8:11], v[28:31]\n" \
"ds_read_b128 a[4:7], v213 offset:2048\n" \
"v_cvt_pk_bf16_f32 v172, v172, v173\n" \
"v_mfma_f32_16x16x32_bf16 v[28:31], a[60:63], a[12:15], v[28:31]\n" \
"ds_read_b64_tr_b16 a[38:39], v214 offset:26624\n" \
"global_load_lds_dwordx4 v217, s[38:39] offset:-3072\n" \
"v_cvt_pk_bf16_f32 v173, v174, v175\n" \
"v_mfma_f32_16x16x32_bf16 v[24:27], a[48:51], a[8:11], v[24:27]\n" \
"ds_read_b64_tr_b16 a[40:41], v215 offset:16384\n" \
"v_cvt_pk_bf16_f32 v174, v176, v177\n" \
"v_mfma_f32_16x16x32_bf16 v[24:27], a[52:55], a[12:15], v[24:27]\n" \
"ds_read_b128 a[8:11], v213 offset:256\n" \
"v_cvt_pk_bf16_f32 v175, v178, v179\n" \
"v_mfma_f32_16x16x32_bf16 v[40:43], a[48:51], a[16:19], v[40:43]\n" \
"ds_read_b128 a[12:15], v213 offset:2304\n" \
"v_cvt_pk_bf16_f32 v164, v164, v165\n" \
"v_mfma_f32_16x16x32_bf16 v[40:43], a[52:55], a[20:23], v[40:43]\n" \
"ds_read_b64_tr_b16 a[42:43], v215 offset:18432\n" \
"global_load_lds_dwordx4 v218, s[38:39] offset:-2048\n" \
"v_mfma_f32_16x16x32_bf16 v[44:47], a[56:59], a[16:19], v[44:47]\n" \
"ds_read_b64_tr_b16 a[44:45], v215 offset:24576\n" \
"v_cvt_pk_bf16_f32 v165, v166, v167\n" \
"v_mfma_f32_16x16x32_bf16 v[44:47], a[60:63], a[20:23], v[44:47]\n" \
"ds_read_b128 a[16:19], v213 offset:4096\n" \
"v_cvt_pk_bf16_f32 v166, v168, v169\n" \
"v_mfma_f32_16x16x32_bf16 v[60:63], a[56:59], a[24:27], v[60:63]\n" \
"ds_read_b128 a[20:23], v213 offset:6144\n" \
"global_load_lds_dwordx4 v219, s[38:39] offset:-1024\n" \
"v_mfma_f32_16x16x32_bf16 v[60:63], a[60:63], a[28:31], v[60:63]\n" \
"s_add_u32 s38, s38, 0x20000\n" \
"s_addc_u32 s39, s39, 0\n" \
"ds_read_b64_tr_b16 a[46:47], v215 offset:26624\n" \
"v_mfma_f32_16x16x32_bf16 v[56:59], a[48:51], a[24:27], v[56:59]\n" \
"s_sub_u32 s75, s75, 32768\n" \
"s_cselect_b32 s75, 131072, s75\n" \
"v_cvt_pk_bf16_f32 v167, v170, v171\n" \
"v_mfma_f32_16x16x32_bf16 v[56:59], a[52:55], a[28:31], v[56:59]\n" \
"s_waitcnt lgkmcnt(3)\n" \
"v_cvt_pk_bf16_f32 v156, v156, v157\n" \
"v_cvt_pk_bf16_f32 v157, v158, v159\n" \
"v_mfma_f32_16x16x32_bf16 v[0:3], a[32:35], a[0:3], v[0:3]\n" \
"s_add_u32 m0, s75, s95\n" \
"ds_read_b128 a[24:27], v213 offset:4352\n" \
"v_cvt_pk_bf16_f32 v158, v160, v161\n" \
"v_mfma_f32_16x16x32_bf16 v[0:3], a[36:39], a[4:7], v[0:3]\n" \
"s_waitcnt lgkmcnt(1)\n" \
"ds_read_b128 a[28:31], v213 offset:6400\n" \
"global_load_lds_dwordx4 v222, s[36:37] offset:0 sc1\n" \
"v_mfma_f32_16x16x32_bf16 v[4:7], a[40:43], a[0:3], v[4:7]\n" \
"s_sub_u32 s74, s74, 32768\n" \
"s_cselect_b32 s74, 131072, s74\n" \
"ds_read_b64_tr_b16 a[48:49], v214 offset:16896\n" \
"v_mfma_f32_16x16x32_bf16 v[4:7], a[44:47], a[4:7], v[4:7]\n" \
"ds_read_b64_tr_b16 a[50:51], v214 offset:18944\n" \
"v_cvt_pk_bf16_f32 v159, v162, v163\n" \
"v_mfma_f32_16x16x32_bf16 v[20:23], a[40:43], a[8:11], v[20:23]\n" \
"ds_read_b64_tr_b16 a[52:53], v214 offset:25088\n" \
"v_cvt_pk_bf16_f32 v148, v148, v149\n" \
"v_mfma_f32_16x16x32_bf16 v[20:23], a[44:47], a[12:15], v[20:23]\n" \
"ds_read_b64_tr_b16 a[54:55], v214 offset:27136\n" \
"global_load_lds_dwordx4 v222, s[36:37] offset:1024 sc1\n" \
"v_mfma_f32_16x16x32_bf16 v[16:19], a[32:35], a[8:11], v[16:19]\n" \
"ds_read_b64_tr_b16 a[56:57], v215 offset:16896\n" \
"v_cvt_pk_bf16_f32 v149, v150, v151\n" \
"v_mfma_f32_16x16x32_bf16 v[16:19], a[36:39], a[12:15], v[16:19]\n" \
"ds_read_b64_tr_b16 a[58:59], v215 offset:18944\n" \
"v_cvt_pk_bf16_f32 v150, v152, v153\n" \
"v_mfma_f32_16x16x32_bf16 v[32:35], a[32:35], a[16:19], v[32:35]\n" \
"ds_read_b64_tr_b16 a[60:61], v215 offset:25088\n" \
"v_cvt_pk_bf16_f32 v151, v154, v155\n" \
"v_mfma_f32_16x16x32_bf16 v[32:35], a[36:39], a[20:23], v[32:35]\n" \
"ds_read_b64_tr_b16 a[62:63], v215 offset:27136\n" \
"global_load_lds_dwordx4 v222, s[36:37] offset:2048 sc1\n" \
"v_mfma_f32_16x16x32_bf16 v[176:179], v[188:191], v[172:175], 0\n" \
"v_mfma_f32_16x16x32_bf16 v[136:139], v[74:77], v[172:175], 0\n" \
"v_mfma_f32_16x16x32_bf16 v[140:143], v[198:201], v[172:175], 0\n" \
"v_mfma_f32_16x16x32_bf16 v[168:171], v[188:191], v[164:167], 0\n" \
"v_mfma_f32_16x16x32_bf16 v[128:131], v[72:75], v[164:167], 0\n" \
"v_mfma_f32_16x16x32_bf16 v[132:135], v[196:199], v[164:167], 0\n" \
"v_mfma_f32_16x16x32_bf16 v[36:39], a[40:43], a[16:19], v[36:39]\n" \
"v_mfma_f32_16x16x32_bf16 v[160:163], v[188:191], v[156:159], 0\n" \
"v_mfma_f32_16x16x32_bf16 v[120:123], v[70:73], v[156:159], 0\n" \
"v_mfma_f32_16x16x32_bf16 v[124:127], v[194:197], v[156:159], 0\n" \
"v_mfma_f32_16x16x32_bf16 v[152:155], v[188:191], v[148:151], 0\n" \
"v_mfma_f32_16x16x32_bf16 v[112:115], v[68:71], v[148:151], 0\n" \
"v_mfma_f32_16x16x32_bf16 v[116:119], v[192:195], v[148:151], 0\n" \
"v_add_u32 v213, s74, v211\n" \
"v_add_u32 v214, s74, v212\n" \
"v_mfma_f32_16x16x32_bf16 v[36:39], a[44:47], a[20:23], v[36:39]\n" \
"s_waitcnt lgkmcnt(3)\n" \
"v_xor_b32 v215, 32, v214\n" \
"v_rcp_f32 v187, v176\n" \
"v_rcp_f32 v186, v168\n" \
"v_rcp_f32 v185, v160\n" \
"v_mfma_f32_16x16x32_bf16 v[52:55], a[40:43], a[24:27], v[52:55]\n" \
"v_rcp_f32 v184, v152\n" \
"v_mfma_f32_16x16x32_bf16 v[52:55], a[44:47], a[28:31], v[52:55]\n" \
"s_waitcnt vmcnt(19) lgkmcnt(0)\n" \
"global_load_lds_dwordx4 v222, s[36:37] offset:3072 sc1\n" \
"v_mul_f32 v136, v136, v187\n" \
"v_mul_f32 v137, v137, v187\n" \
"v_mfma_f32_16x16x32_bf16 v[48:51], a[32:35], a[24:27], v[48:51]\n" \
"s_add_u32 s36, s36, 0x1000\n" \
"v_mul_f32 v138, v138, v187\n" \
"v_mul_f32 v139, v139, v187\n" \
"v_mfma_f32_16x16x32_bf16 v[48:51], a[36:39], a[28:31], v[48:51]\n" \
"s_barrier\n" \
"v_mul_f32 v140, v140, v187\n" \
"v_mfma_f32_16x16x32_bf16 v[8:11], a[48:51], a[0:3], v[8:11]\n" \
"s_add_u32 m0, m0, 0x5000\n" \
"ds_read_b64_tr_b16 a[32:33], v214 offset:16384\n" \
"v_mul_f32 v141, v141, v187\n" \
"v_mfma_f32_16x16x32_bf16 v[8:11], a[52:55], a[4:7], v[8:11]\n" \
"ds_read_b64_tr_b16 a[34:35], v214 offset:18432\n" \
"global_load_lds_dwordx4 v216, s[38:39] offset:-4096\n" \
"v_mul_f32 v142, v142, v187\n" \
"v_mul_f32 v143, v143, v187\n" \
"v_mfma_f32_16x16x32_bf16 v[12:15], a[56:59], a[0:3], v[12:15]\n" \
"ds_read_b64_tr_b16 a[36:37], v214 offset:24576\n" \
"v_mul_f32 v128, v128, v186\n" \
"v_mfma_f32_16x16x32_bf16 v[12:15], a[60:63], a[4:7], v[12:15]\n" \
"ds_read_b128 a[0:3], v213 offset:0\n" \
"v_mul_f32 v129, v129, v186\n" \
"v_mul_f32 v130, v130, v186\n" \
"v_mfma_f32_16x16x32_bf16 v[28:31], a[56:59], a[8:11], v[28:31]\n" \
"ds_read_b128 a[4:7], v213 offset:2048\n" \
"v_mul_f32 v131, v131, v186\n" \
"v_mfma_f32_16x16x32_bf16 v[28:31], a[60:63], a[12:15], v[28:31]\n" \
"ds_read_b64_tr_b16 a[38:39], v214 offset:26624\n" \
"global_load_lds_dwordx4 v217, s[38:39] offset:-3072\n" \
"v_mul_f32 v132, v132, v186\n" \
"v_mul_f32 v133, v133, v186\n" \
"v_mfma_f32_16x16x32_bf16 v[24:27], a[48:51], a[8:11], v[24:27]\n" \
"ds_read_b64_tr_b16 a[40:41], v215 offset:16384\n" \
"v_mul_f32 v134, v134, v186\n" \
"v_mfma_f32_16x16x32_bf16 v[24:27], a[52:55], a[12:15], v[24:27]\n" \
"ds_read_b128 a[8:11], v213 offset:256\n" \
"v_mul_f32 v135, v135, v186\n" \
"v_mul_f32 v120, v120, v185\n" \
"v_mfma_f32_16x16x32_bf16 v[40:43], a[48:51], a[16:19], v[40:43]\n" \
"ds_read_b128 a[12:15], v213 offset:2304\n" \
"v_mul_f32 v121, v121, v185\n" \
"v_mfma_f32_16x16x32_bf16 v[40:43], a[52:55], a[20:23], v[40:43]\n" \
"ds_read_b64_tr_b16 a[42:43], v215 offset:18432\n" \
"global_load_lds_dwordx4 v218, s[38:39] offset:-2048\n" \
"v_mul_f32 v122, v122, v185\n" \
"v_mul_f32 v123, v123, v185\n" \
"v_mfma_f32_16x16x32_bf16 v[44:47], a[56:59], a[16:19], v[44:47]\n" \
"ds_read_b64_tr_b16 a[44:45], v215 offset:24576\n" \
"v_mul_f32 v124, v124, v185\n" \
"v_mfma_f32_16x16x32_bf16 v[44:47], a[60:63], a[20:23], v[44:47]\n" \
"ds_read_b128 a[16:19], v213 offset:4096\n" \
"v_mul_f32 v125, v125, v185\n" \
"v_mul_f32 v126, v126, v185\n" \
"v_mfma_f32_16x16x32_bf16 v[60:63], a[56:59], a[24:27], v[60:63]\n" \
"ds_read_b128 a[20:23], v213 offset:6144\n" \
"global_load_lds_dwordx4 v219, s[38:39] offset:-1024\n" \
"v_mfma_f32_16x16x32_bf16 v[60:63], a[60:63], a[28:31], v[60:63]\n" \
"s_add_u32 s38, s38, 0x20000\n" \
"s_addc_u32 s39, s39, 0\n" \
"ds_read_b64_tr_b16 a[46:47], v215 offset:26624\n" \
"v_mul_f32 v127, v127, v185\n" \
"v_mul_f32 v112, v112, v184\n" \
"v_mul_f32 v113, v113, v184\n" \
"v_mfma_f32_16x16x32_bf16 v[56:59], a[48:51], a[24:27], v[56:59]\n" \
"s_sub_u32 s75, s75, 32768\n" \
"s_cselect_b32 s75, 131072, s75\n" \
"v_mul_f32 v114, v114, v184\n" \
"v_mfma_f32_16x16x32_bf16 v[56:59], a[52:55], a[28:31], v[56:59]\n" \
"s_waitcnt lgkmcnt(3)\n" \
"v_mul_f32 v115, v115, v184\n" \
"v_mul_f32 v116, v116, v184\n" \
"v_mfma_f32_16x16x32_bf16 v[0:3], a[32:35], a[0:3], v[0:3]\n" \
"s_add_u32 m0, s75, s95\n" \
"ds_read_b128 a[24:27], v213 offset:4352\n" \
"v_mul_f32 v117, v117, v184\n" \
"v_mfma_f32_16x16x32_bf16 v[0:3], a[36:39], a[4:7], v[0:3]\n" \
"s_waitcnt lgkmcnt(1)\n" \
"ds_read_b128 a[28:31], v213 offset:6400\n" \
"global_load_lds_dwordx4 v222, s[36:37] offset:0 sc1\n" \
"v_mul_f32 v118, v118, v184\n" \
"v_mul_f32 v119, v119, v184\n" \
"v_mfma_f32_16x16x32_bf16 v[4:7], a[40:43], a[0:3], v[4:7]\n" \
"s_sub_u32 s74, s74, 32768\n" \
"s_cselect_b32 s74, 131072, s74\n" \
"ds_read_b64_tr_b16 a[48:49], v214 offset:16896\n" \
"v_mfma_f32_16x16x32_bf16 v[4:7], a[44:47], a[4:7], v[4:7]\n" \
"ds_read_b64_tr_b16 a[50:51], v214 offset:18944\n" \
"v_cvt_pk_bf16_f32 v136, v136, v137\n" \
"v_cvt_pk_bf16_f32 v137, v138, v139\n" \
"v_cvt_pk_bf16_f32 v138, v140, v141\n" \
"v_mfma_f32_16x16x32_bf16 v[20:23], a[40:43], a[8:11], v[20:23]\n" \
"ds_read_b64_tr_b16 a[52:53], v214 offset:25088\n" \
"v_cvt_pk_bf16_f32 v139, v142, v143\n" \
"v_mfma_f32_16x16x32_bf16 v[20:23], a[44:47], a[12:15], v[20:23]\n" \
"ds_read_b64_tr_b16 a[54:55], v214 offset:27136\n" \
"global_load_lds_dwordx4 v222, s[36:37] offset:1024 sc1\n" \
"v_cndmask_b32 v140, v136, v138, s[92:93]\n" \
"v_mfma_f32_16x16x32_bf16 v[16:19], a[32:35], a[8:11], v[16:19]\n" \
"ds_read_b64_tr_b16 a[56:57], v215 offset:16896\n" \
"v_cndmask_b32 v141, v137, v139, s[92:93]\n" \
"v_mfma_f32_16x16x32_bf16 v[16:19], a[36:39], a[12:15], v[16:19]\n" \
"ds_read_b64_tr_b16 a[58:59], v215 offset:18944\n" \
"v_cndmask_b32 v142, v138, v136, s[92:93]\n" \
"v_mfma_f32_16x16x32_bf16 v[32:35], a[32:35], a[16:19], v[32:35]\n" \
"ds_read_b64_tr_b16 a[60:61], v215 offset:25088\n" \
"v_cndmask_b32 v143, v139, v137, s[92:93]\n" \
"v_mfma_f32_16x16x32_bf16 v[32:35], a[36:39], a[20:23], v[32:35]\n" \
"s_add_u32 s71, s87, 208896\n" \
"ds_read_b64_tr_b16 a[62:63], v215 offset:27136\n" \
"global_load_lds_dwordx4 v222, s[36:37] offset:2048 sc1\n" \
"v_mfma_f32_16x16x32_bf16 v[36:39], a[40:43], a[16:19], v[36:39]\n" \
"v_add_u32 v213, s74, v211\n" \
"v_add_u32 v214, s74, v212\n" \
"v_mfma_f32_16x16x32_bf16 v[36:39], a[44:47], a[20:23], v[36:39]\n" \
"s_waitcnt lgkmcnt(3)\n" \
"buffer_store_dwordx4 v[140:143], v146, s[56:59], s71 offen sc1\n" \
"v_xor_b32 v215, 32, v214\n" \
"v_cvt_pk_bf16_f32 v128, v128, v129\n" \
"v_cvt_pk_bf16_f32 v129, v130, v131\n" \
"v_cvt_pk_bf16_f32 v130, v132, v133\n" \
"v_cvt_pk_bf16_f32 v131, v134, v135\n" \
"v_mfma_f32_16x16x32_bf16 v[52:55], a[40:43], a[24:27], v[52:55]\n" \
"v_cndmask_b32 v132, v128, v130, s[92:93]\n" \
"v_cndmask_b32 v133, v129, v131, s[92:93]\n" \
"v_mfma_f32_16x16x32_bf16 v[52:55], a[44:47], a[28:31], v[52:55]\n" \
"s_waitcnt vmcnt(20) lgkmcnt(0)\n" \
"global_load_lds_dwordx4 v222, s[36:37] offset:3072 sc1\n" \
"v_cndmask_b32 v134, v130, v128, s[92:93]\n" \
"v_mfma_f32_16x16x32_bf16 v[48:51], a[32:35], a[24:27], v[48:51]\n" \
"s_add_u32 s36, s36, 0x1000\n" \
"v_cndmask_b32 v135, v131, v129, s[92:93]\n" \
"s_add_u32 s71, s87, 139264\n" \
"v_mfma_f32_16x16x32_bf16 v[48:51], a[36:39], a[28:31], v[48:51]\n" \
"s_barrier\n" \
"buffer_store_dwordx4 v[132:135], v146, s[56:59], s71 offen sc1\n" \
"v_cvt_pk_bf16_f32 v120, v120, v121\n" \
"v_cvt_pk_bf16_f32 v121, v122, v123\n" \
"v_cvt_pk_bf16_f32 v122, v124, v125\n" \
"v_cvt_pk_bf16_f32 v123, v126, v127\n" \
"v_mfma_f32_16x16x32_bf16 v[8:11], a[48:51], a[0:3], v[8:11]\n" \
"s_add_u32 m0, m0, 0x5000\n" \
"ds_read_b64_tr_b16 a[32:33], v214 offset:16384\n" \
"v_cndmask_b32 v124, v120, v122, s[92:93]\n" \
"v_mfma_f32_16x16x32_bf16 v[8:11], a[52:55], a[4:7], v[8:11]\n" \
"ds_read_b64_tr_b16 a[34:35], v214 offset:18432\n" \
"global_load_lds_dwordx4 v216, s[38:39] offset:-4096\n" \
"v_mfma_f32_16x16x32_bf16 v[12:15], a[56:59], a[0:3], v[12:15]\n" \
"ds_read_b64_tr_b16 a[36:37], v214 offset:24576\n" \
"v_cndmask_b32 v125, v121, v123, s[92:93]\n" \
"v_mfma_f32_16x16x32_bf16 v[12:15], a[60:63], a[4:7], v[12:15]\n" \
"ds_read_b128 a[0:3], v213 offset:0\n" \
"v_cndmask_b32 v126, v122, v120, s[92:93]\n" \
"v_mfma_f32_16x16x32_bf16 v[28:31], a[56:59], a[8:11], v[28:31]\n" \
"ds_read_b128 a[4:7], v213 offset:2048\n" \
"v_cndmask_b32 v127, v123, v121, s[92:93]\n" \
"v_mfma_f32_16x16x32_bf16 v[28:31], a[60:63], a[12:15], v[28:31]\n" \
"s_add_u32 s71, s87, 69632\n" \
"ds_read_b64_tr_b16 a[38:39], v214 offset:26624\n" \
"global_load_lds_dwordx4 v217, s[38:39] offset:-3072\n" \
"v_mfma_f32_16x16x32_bf16 v[24:27], a[48:51], a[8:11], v[24:27]\n" \
"ds_read_b64_tr_b16 a[40:41], v215 offset:16384\n" \
"buffer_store_dwordx4 v[124:127], v146, s[56:59], s71 offen sc1\n" \
"v_cvt_pk_bf16_f32 v112, v112, v113\n" \
"v_cvt_pk_bf16_f32 v113, v114, v115\n" \
"v_cvt_pk_bf16_f32 v114, v116, v117\n" \
"v_cvt_pk_bf16_f32 v115, v118, v119\n" \
"v_mfma_f32_16x16x32_bf16 v[24:27], a[52:55], a[12:15], v[24:27]\n" \
"ds_read_b128 a[8:11], v213 offset:256\n" \
"v_cndmask_b32 v116, v112, v114, s[92:93]\n" \
"v_mfma_f32_16x16x32_bf16 v[40:43], a[48:51], a[16:19], v[40:43]\n" \
"ds_read_b128 a[12:15], v213 offset:2304\n" \
"v_cndmask_b32 v117, v113, v115, s[92:93]\n" \
"v_mfma_f32_16x16x32_bf16 v[40:43], a[52:55], a[20:23], v[40:43]\n" \
"ds_read_b64_tr_b16 a[42:43], v215 offset:18432\n" \
"global_load_lds_dwordx4 v218, s[38:39] offset:-2048\n" \
"v_mfma_f32_16x16x32_bf16 v[44:47], a[56:59], a[16:19], v[44:47]\n" \
"ds_read_b64_tr_b16 a[44:45], v215 offset:24576\n" \
"v_cndmask_b32 v118, v114, v112, s[92:93]\n" \
"v_mfma_f32_16x16x32_bf16 v[44:47], a[60:63], a[20:23], v[44:47]\n" \
"ds_read_b128 a[16:19], v213 offset:4096\n" \
"v_cndmask_b32 v119, v115, v113, s[92:93]\n" \
"v_mfma_f32_16x16x32_bf16 v[60:63], a[56:59], a[24:27], v[60:63]\n" \
"s_add_u32 s71, s87, 0\n" \
"ds_read_b128 a[20:23], v213 offset:6144\n" \
"global_load_lds_dwordx4 v219, s[38:39] offset:-1024\n" \
"v_mfma_f32_16x16x32_bf16 v[60:63], a[60:63], a[28:31], v[60:63]\n" \
"buffer_store_dwordx4 v[116:119], v146, s[56:59], s71 offen sc1\n" \
"s_add_u32 s38, s38, 0x20000\n" \
"s_addc_u32 s39, s39, 0\n" \
"ds_read_b64_tr_b16 a[46:47], v215 offset:26624\n" \
"v_mfma_f32_16x16x32_bf16 v[56:59], a[48:51], a[24:27], v[56:59]\n" \
"s_sub_u32 s75, s75, 32768\n" \
"s_cselect_b32 s75, 131072, s75\n" \
"v_mfma_f32_16x16x32_bf16 v[56:59], a[52:55], a[28:31], v[56:59]\n" \
"s_waitcnt lgkmcnt(3)\n" \
"v_mfma_f32_16x16x32_bf16 v[0:3], a[32:35], a[0:3], v[0:3]\n" \
"s_add_u32 m0, s75, s95\n" \
"ds_read_b128 a[24:27], v213 offset:4352\n" \
"v_mov_b32 v224, s60\n" \
"v_mfma_f32_16x16x32_bf16 v[0:3], a[36:39], a[4:7], v[0:3]\n" \
"s_waitcnt lgkmcnt(1)\n" \
"ds_read_b128 a[28:31], v213 offset:6400\n" \
"global_load_lds_dwordx4 v222, s[36:37] offset:0 sc1\n" \
"v_mfma_f32_16x16x32_bf16 v[4:7], a[40:43], a[0:3], v[4:7]\n" \
"s_sub_u32 s74, s74, 32768\n" \
"s_cselect_b32 s74, 131072, s74\n" \
"ds_read_b64_tr_b16 a[48:49], v214 offset:16896\n" \
"v_mfma_f32_16x16x32_bf16 v[4:7], a[44:47], a[4:7], v[4:7]\n" \
"ds_read_b64_tr_b16 a[50:51], v214 offset:18944\n" \
"v_lshlrev_b32 v225, 28, s61\n" \
"v_mfma_f32_16x16x32_bf16 v[20:23], a[40:43], a[8:11], v[20:23]\n" \
"ds_read_b64_tr_b16 a[52:53], v214 offset:25088\n" \
"v_mfma_f32_16x16x32_bf16 v[20:23], a[44:47], a[12:15], v[20:23]\n" \
"ds_read_b64_tr_b16 a[54:55], v214 offset:27136\n" \
"global_load_lds_dwordx4 v222, s[36:37] offset:1024 sc1\n" \
"v_mfma_f32_16x16x32_bf16 v[16:19], a[32:35], a[8:11], v[16:19]\n" \
"ds_read_b64_tr_b16 a[56:57], v215 offset:16896\n" \
"v_mfma_f32_16x16x32_bf16 v[16:19], a[36:39], a[12:15], v[16:19]\n" \
"ds_read_b64_tr_b16 a[58:59], v215 offset:18944\n" \
"v_mfma_f32_16x16x32_bf16 v[32:35], a[32:35], a[16:19], v[32:35]\n" \
"ds_read_b64_tr_b16 a[60:61], v215 offset:25088\n" \
"v_mfma_f32_16x16x32_bf16 v[32:35], a[36:39], a[20:23], v[32:35]\n" \
"ds_read_b64_tr_b16 a[62:63], v215 offset:27136\n" \
"global_load_lds_dwordx4 v222, s[36:37] offset:2048 sc1\n" \
"v_mfma_f32_16x16x32_bf16 v[36:39], a[40:43], a[16:19], v[36:39]\n" \
"v_add_u32 v213, s74, v211\n" \
"v_add_u32 v214, s74, v212\n" \
"v_mfma_f32_16x16x32_bf16 v[36:39], a[44:47], a[20:23], v[36:39]\n" \
"s_waitcnt lgkmcnt(3)\n" \
"v_xor_b32 v215, 32, v214\n" \
"v_mfma_f32_16x16x32_bf16 v[52:55], a[40:43], a[24:27], v[52:55]\n" \
"v_mfma_f32_16x16x32_bf16 v[52:55], a[44:47], a[28:31], v[52:55]\n" \
"s_waitcnt vmcnt(23) lgkmcnt(0)\n" \
"global_load_lds_dwordx4 v222, s[36:37] offset:3072 sc1\n" \
"v_mfma_f32_16x16x32_bf16 v[48:51], a[32:35], a[24:27], v[48:51]\n" \
"s_add_u32 s36, s36, 0x1000\n" \
"v_mfma_f32_16x16x32_bf16 v[48:51], a[36:39], a[28:31], v[48:51]\n" \
"s_barrier\n" \
"v_mfma_f32_16x16x32_bf16 v[8:11], a[48:51], a[0:3], v[8:11]\n" \
"s_add_u32 m0, m0, 0x5000\n" \
"ds_read_b64_tr_b16 a[32:33], v214 offset:16384\n" \
"v_mfma_f32_16x16x32_bf16 v[8:11], a[52:55], a[4:7], v[8:11]\n" \
"ds_read_b64_tr_b16 a[34:35], v214 offset:18432\n" \
"global_load_lds_dwordx4 v216, s[38:39] offset:-4096\n" \
"v_mfma_f32_16x16x32_bf16 v[12:15], a[56:59], a[0:3], v[12:15]\n" \
"ds_read_b64_tr_b16 a[36:37], v214 offset:24576\n" \
"v_mfma_f32_16x16x32_bf16 v[12:15], a[60:63], a[4:7], v[12:15]\n" \
"ds_read_b128 a[0:3], v213 offset:0\n" \
"v_mfma_f32_16x16x32_bf16 v[28:31], a[56:59], a[8:11], v[28:31]\n" \
"ds_read_b128 a[4:7], v213 offset:2048\n" \
"v_mfma_f32_16x16x32_bf16 v[28:31], a[60:63], a[12:15], v[28:31]\n" \
"ds_read_b64_tr_b16 a[38:39], v214 offset:26624\n" \
"global_load_lds_dwordx4 v217, s[38:39] offset:-3072\n" \
"v_mfma_f32_16x16x32_bf16 v[24:27], a[48:51], a[8:11], v[24:27]\n" \
"ds_read_b64_tr_b16 a[40:41], v215 offset:16384\n" \
"v_mfma_f32_16x16x32_bf16 v[24:27], a[52:55], a[12:15], v[24:27]\n" \
"ds_read_b128 a[8:11], v213 offset:256\n" \
"v_mfma_f32_16x16x32_bf16 v[40:43], a[48:51], a[16:19], v[40:43]\n" \
"ds_read_b128 a[12:15], v213 offset:2304\n" \
"v_mfma_f32_16x16x32_bf16 v[40:43], a[52:55], a[20:23], v[40:43]\n" \
"ds_read_b64_tr_b16 a[42:43], v215 offset:18432\n" \
"global_load_lds_dwordx4 v218, s[38:39] offset:-2048\n" \
"v_mfma_f32_16x16x32_bf16 v[44:47], a[56:59], a[16:19], v[44:47]\n" \
"ds_read_b64_tr_b16 a[44:45], v215 offset:24576\n" \
"v_mfma_f32_16x16x32_bf16 v[44:47], a[60:63], a[20:23], v[44:47]\n" \
"ds_read_b128 a[16:19], v213 offset:4096\n" \
"v_mfma_f32_16x16x32_bf16 v[60:63], a[56:59], a[24:27], v[60:63]\n" \
"ds_read_b128 a[20:23], v213 offset:6144\n" \
"global_load_lds_dwordx4 v219, s[38:39] offset:-1024\n" \
"v_mfma_f32_16x16x32_bf16 v[60:63], a[60:63], a[28:31], v[60:63]\n" \
"s_add_u32 s38, s38, 0x20000\n" \
"s_addc_u32 s39, s39, 0\n" \
"ds_read_b64_tr_b16 a[46:47], v215 offset:26624\n" \
"v_mfma_f32_16x16x32_bf16 v[56:59], a[48:51], a[24:27], v[56:59]\n" \
"s_sub_u32 s75, s75, 32768\n" \
"s_cselect_b32 s75, 131072, s75\n" \
"v_mfma_f32_16x16x32_bf16 v[56:59], a[52:55], a[28:31], v[56:59]\n" \
"s_waitcnt lgkmcnt(3)\n" \
"v_mfma_f32_16x16x32_bf16 v[0:3], a[32:35], a[0:3], v[0:3]\n" \
"ds_read_b128 a[24:27], v213 offset:4352\n" \
"s_add_u32 m0, s75, s95\n" \
"v_mfma_f32_16x16x32_bf16 v[0:3], a[36:39], a[4:7], v[0:3]\n" \
"s_waitcnt lgkmcnt(1)\n" \
"ds_read_b128 a[28:31], v213 offset:6400\n" \
"global_load_lds_dwordx4 v222, s[36:37] offset:0 sc1\n" \
"v_mfma_f32_16x16x32_bf16 v[4:7], a[40:43], a[0:3], v[4:7]\n" \
"s_sub_u32 s74, s74, 32768\n" \
"s_cselect_b32 s74, 131072, s74\n" \
"ds_read_b64_tr_b16 a[48:49], v214 offset:16896\n" \
"v_mfma_f32_16x16x32_bf16 v[4:7], a[44:47], a[4:7], v[4:7]\n" \
"ds_read_b64_tr_b16 a[50:51], v214 offset:18944\n" \
"v_mfma_f32_16x16x32_bf16 v[20:23], a[40:43], a[8:11], v[20:23]\n" \
"ds_read_b64_tr_b16 a[52:53], v214 offset:25088\n" \
"v_mfma_f32_16x16x32_bf16 v[20:23], a[44:47], a[12:15], v[20:23]\n" \
"ds_read_b64_tr_b16 a[54:55], v214 offset:27136\n" \
"global_load_lds_dwordx4 v222, s[36:37] offset:1024 sc1\n" \
"v_mfma_f32_16x16x32_bf16 v[16:19], a[32:35], a[8:11], v[16:19]\n" \
"ds_read_b64_tr_b16 a[56:57], v215 offset:16896\n" \
"v_mfma_f32_16x16x32_bf16 v[16:19], a[36:39], a[12:15], v[16:19]\n" \
"ds_read_b64_tr_b16 a[58:59], v215 offset:18944\n" \
"v_mfma_f32_16x16x32_bf16 v[32:35], a[32:35], a[16:19], v[32:35]\n" \
"ds_read_b64_tr_b16 a[60:61], v215 offset:25088\n" \
"v_mfma_f32_16x16x32_bf16 v[32:35], a[36:39], a[20:23], v[32:35]\n" \
"ds_read_b64_tr_b16 a[62:63], v215 offset:27136\n" \
"global_load_lds_dwordx4 v222, s[36:37] offset:2048 sc1\n" \
"v_mfma_f32_16x16x32_bf16 v[36:39], a[40:43], a[16:19], v[36:39]\n" \
"v_add_u32 v213, s74, v211\n" \
"v_add_u32 v214, s74, v212\n" \
"v_mfma_f32_16x16x32_bf16 v[36:39], a[44:47], a[20:23], v[36:39]\n" \
"s_waitcnt lgkmcnt(3)\n" \
"v_xor_b32 v215, 32, v214\n" \
"v_mfma_f32_16x16x32_bf16 v[52:55], a[40:43], a[24:27], v[52:55]\n" \
"v_mfma_f32_16x16x32_bf16 v[52:55], a[44:47], a[28:31], v[52:55]\n" \
"s_waitcnt vmcnt(11) lgkmcnt(0)\n" \
"global_load_lds_dwordx4 v222, s[36:37] offset:3072 sc1\n" \
"v_mfma_f32_16x16x32_bf16 v[48:51], a[32:35], a[24:27], v[48:51]\n" \
"s_add_u32 s36, s36, 0x1000\n" \
"v_mfma_f32_16x16x32_bf16 v[48:51], a[36:39], a[28:31], v[48:51]\n" \
"s_barrier\n" \
"v_mfma_f32_16x16x32_bf16 v[8:11], a[48:51], a[0:3], v[8:11]\n" \
"s_add_u32 m0, m0, 0x5000\n" \
"ds_read_b64_tr_b16 a[32:33], v214 offset:16384\n" \
"v_mfma_f32_16x16x32_bf16 v[8:11], a[52:55], a[4:7], v[8:11]\n" \
"ds_read_b64_tr_b16 a[34:35], v214 offset:18432\n" \
"global_load_lds_dwordx4 v216, s[38:39] offset:-4096\n" \
"v_mfma_f32_16x16x32_bf16 v[12:15], a[56:59], a[0:3], v[12:15]\n" \
"ds_read_b64_tr_b16 a[36:37], v214 offset:24576\n" \
"v_mfma_f32_16x16x32_bf16 v[12:15], a[60:63], a[4:7], v[12:15]\n" \
"buffer_store_dword v224, v225, s[56:59], s30 offen sc1\n" \
"ds_read_b128 a[0:3], v213 offset:0\n" \
"v_mfma_f32_16x16x32_bf16 v[28:31], a[56:59], a[8:11], v[28:31]\n" \
"ds_read_b128 a[4:7], v213 offset:2048\n" \
"v_mfma_f32_16x16x32_bf16 v[28:31], a[60:63], a[12:15], v[28:31]\n" \
"ds_read_b64_tr_b16 a[38:39], v214 offset:26624\n" \
"global_load_lds_dwordx4 v217, s[38:39] offset:-3072\n" \
"v_mfma_f32_16x16x32_bf16 v[24:27], a[48:51], a[8:11], v[24:27]\n" \
"ds_read_b64_tr_b16 a[40:41], v215 offset:16384\n" \
"v_mfma_f32_16x16x32_bf16 v[24:27], a[52:55], a[12:15], v[24:27]\n" \
"ds_read_b128 a[8:11], v213 offset:256\n" \
"v_mfma_f32_16x16x32_bf16 v[40:43], a[48:51], a[16:19], v[40:43]\n" \
"ds_read_b128 a[12:15], v213 offset:2304\n" \
"v_mfma_f32_16x16x32_bf16 v[40:43], a[52:55], a[20:23], v[40:43]\n" \
"ds_read_b64_tr_b16 a[42:43], v215 offset:18432\n" \
"global_load_lds_dwordx4 v218, s[38:39] offset:-2048\n" \
"v_mfma_f32_16x16x32_bf16 v[44:47], a[56:59], a[16:19], v[44:47]\n" \
"ds_read_b64_tr_b16 a[44:45], v215 offset:24576\n" \
"v_mfma_f32_16x16x32_bf16 v[44:47], a[60:63], a[20:23], v[44:47]\n" \
"ds_read_b128 a[16:19], v213 offset:4096\n" \
"v_mfma_f32_16x16x32_bf16 v[60:63], a[56:59], a[24:27], v[60:63]\n" \
"ds_read_b128 a[20:23], v213 offset:6144\n" \
"global_load_lds_dwordx4 v219, s[38:39] offset:-1024\n" \
"v_mfma_f32_16x16x32_bf16 v[60:63], a[60:63], a[28:31], v[60:63]\n" \
"s_add_u32 s38, s38, 0x20000\n" \
"s_addc_u32 s39, s39, 0\n" \
"ds_read_b64_tr_b16 a[46:47], v215 offset:26624\n" \
"v_mfma_f32_16x16x32_bf16 v[56:59], a[48:51], a[24:27], v[56:59]\n" \
"s_sub_u32 s75, s75, 32768\n" \
"s_cselect_b32 s75, 131072, s75\n" \
"v_mfma_f32_16x16x32_bf16 v[56:59], a[52:55], a[28:31], v[56:59]\n" \
"s_waitcnt lgkmcnt(3)\n" \
"s_cmp_eq_u32 s78, 0\n" \
"s_cbranch_scc0 Lrole1_%=\n" \
"s_sub_u32 s72, s64, 14\n" \
"s_call_b64 s[68:69], Lo0_%=\n" \
"buffer_load_dword v220, off, s[56:59], s79 sc1\n" \
"s_mov_b32 s72, 2\n" \
"s_call_b64 s[68:69], Lo0_%=\n" \
"v_mfma_f32_16x16x32_bf16 v[0:3], a[32:35], a[0:3], v[0:3]\n" \
"s_lshl_b32 s71, s61, 4\n" \
"ds_read_b128 a[24:27], v213 offset:4352\n" \
"v_subrev_u32 v72, s71, v251\n" \
"v_mfma_f32_16x16x32_bf16 v[0:3], a[36:39], a[4:7], v[0:3]\n" \
"s_waitcnt lgkmcnt(1)\n" \
"ds_read_b128 a[28:31], v213 offset:6400\n" \
"v_mov_b32 v73, 0x3f80\n" \
"v_mfma_f32_16x16x32_bf16 v[4:7], a[40:43], a[0:3], v[4:7]\n" \
"s_sub_u32 s74, s74, 32768\n" \
"s_cselect_b32 s74, 131072, s74\n" \
"ds_read_b64_tr_b16 a[48:49], v214 offset:16896\n" \
"v_mfma_f32_16x16x32_bf16 v[4:7], a[44:47], a[4:7], v[4:7]\n" \
"ds_read_b64_tr_b16 a[50:51], v214 offset:18944\n" \
"v_mov_b32 v74, 0x3f800000\n" \
"v_mfma_f32_16x16x32_bf16 v[20:23], a[40:43], a[8:11], v[20:23]\n" \
"ds_read_b64_tr_b16 a[52:53], v214 offset:25088\n" \
"v_cmp_eq_u32 vcc, 0, v72\n" \
"v_mfma_f32_16x16x32_bf16 v[20:23], a[44:47], a[12:15], v[20:23]\n" \
"ds_read_b64_tr_b16 a[54:55], v214 offset:27136\n" \
"v_cmp_eq_u32 s[66:67], 1, v72\n" \
"v_mfma_f32_16x16x32_bf16 v[16:19], a[32:35], a[8:11], v[16:19]\n" \
"ds_read_b64_tr_b16 a[56:57], v215 offset:16896\n" \
"v_mov_b32 v66, 0\n" \
"v_mfma_f32_16x16x32_bf16 v[16:19], a[36:39], a[12:15], v[16:19]\n" \
"ds_read_b64_tr_b16 a[58:59], v215 offset:18944\n" \
"v_mov_b32 v68, 0\n" \
"v_mfma_f32_16x16x32_bf16 v[32:35], a[32:35], a[16:19], v[32:35]\n" \
"ds_read_b64_tr_b16 a[60:61], v215 offset:25088\n" \
"v_cndmask_b32 v64, 0, v73, vcc\n" \
"v_mfma_f32_16x16x32_bf16 v[32:35], a[36:39], a[20:23], v[32:35]\n" \
"ds_read_b64_tr_b16 a[62:63], v215 offset:27136\n" \
"v_cndmask_b32 v64, v64, v74, s[66:67]\n" \
"Lpg12014_%=:\n" \
"s_cbranch_execz Lpg13128_%=\n" \
"v_mfma_f32_16x16x32_bf16 v[36:39], a[40:43], a[16:19], v[36:39]\n" \
"v_add_u32 v213, s74, v211\n" \
"v_add_u32 v214, s74, v212\n" \
"v_mfma_f32_16x16x32_bf16 v[36:39], a[44:47], a[20:23], v[36:39]\n" \
"s_waitcnt lgkmcnt(3)\n" \
"v_xor_b32 v215, 32, v214\n" \
"v_mov_b32 v70, v64\n" \
"v_mfma_f32_16x16x32_bf16 v[52:55], a[40:43], a[24:27], v[52:55]\n" \
"v_cmp_eq_u32 vcc, 2, v72\n" \
"v_cmp_eq_u32 s[66:67], 3, v72\n" \
"v_mfma_f32_16x16x32_bf16 v[52:55], a[44:47], a[28:31], v[52:55]\n" \
"s_waitcnt vmcnt(17) lgkmcnt(0)\n" \
"v_mov_b32 v67, 0\n" \
"v_mov_b32 v69, 0\n" \
"v_mfma_f32_16x16x32_bf16 v[48:51], a[32:35], a[24:27], v[48:51]\n" \
"v_cndmask_b32 v65, 0, v73, vcc\n" \
"v_cndmask_b32 v65, v65, v74, s[66:67]\n" \
"v_mfma_f32_16x16x32_bf16 v[48:51], a[36:39], a[28:31], v[48:51]\n" \
"s_barrier\n" \
"v_mov_b32 v71, v65\n" \
"v_mfma_f32_16x16x32_bf16 v[8:11], a[48:51], a[0:3], v[8:11]\n" \
"s_lshl_b32 s83, s95, 1\n" \
"s_add_u32 s83, s83, s74\n" \
"ds_read_b64_tr_b16 a[32:33], v214 offset:16384\n" \
"v_mfma_f32_16x16x32_bf16 v[8:11], a[52:55], a[4:7], v[8:11]\n" \
"s_waitcnt vmcnt(16)\n" \
"ds_read_b64_tr_b16 a[34:35], v214 offset:18432\n" \
"v_cmp_ne_u32 vcc, s60, v220\n" \
"v_mfma_f32_16x16x32_bf16 v[12:15], a[56:59], a[0:3], v[12:15]\n" \
"ds_read_b64_tr_b16 a[36:37], v214 offset:24576\n" \
"v_mfma_f32_16x16x32_bf16 v[12:15], a[60:63], a[4:7], v[12:15]\n" \
"s_and_b32 vcc_hi, vcc_lo, 1\n" \
"s_cbranch_scc1 Lpl4_%=\n" \
"Lpr4_%=:\n" \
"ds_read_b128 a[0:3], v213 offset:0\n" \
"v_mfma_f32_16x16x32_bf16 v[28:31], a[56:59], a[8:11], v[28:31]\n" \
"ds_read_b128 a[4:7], v213 offset:2048\n" \
"v_mfma_f32_16x16x32_bf16 v[28:31], a[60:63], a[12:15], v[28:31]\n" \
"s_add_u32 s71, s76, 0\n" \
"ds_read_b64_tr_b16 a[38:39], v214 offset:26624\n" \
"buffer_load_dwordx4 v[176:179], v248, s[56:59], s71 offen offset:0 sc1\n" \
"v_mfma_f32_16x16x32_bf16 v[24:27], a[48:51], a[8:11], v[24:27]\n" \
"ds_read_b64_tr_b16 a[40:41], v215 offset:16384\n" \
"v_mfma_f32_16x16x32_bf16 v[24:27], a[52:55], a[12:15], v[24:27]\n" \
"buffer_load_dwordx4 v[180:183], v248, s[56:59], s71 offen offset:1024 sc1\n" \
"ds_read_b128 a[8:11], v213 offset:256\n" \
"v_mfma_f32_16x16x32_bf16 v[40:43], a[48:51], a[16:19], v[40:43]\n" \
"ds_read_b128 a[12:15], v213 offset:2304\n" \
"v_mfma_f32_16x16x32_bf16 v[40:43], a[52:55], a[20:23], v[40:43]\n" \
"ds_read_b64_tr_b16 a[42:43], v215 offset:18432\n" \
"v_mfma_f32_16x16x32_bf16 v[44:47], a[56:59], a[16:19], v[44:47]\n" \
"ds_read_b64_tr_b16 a[44:45], v215 offset:24576\n" \
"v_mfma_f32_16x16x32_bf16 v[44:47], a[60:63], a[20:23], v[44:47]\n" \
"ds_read_b128 a[16:19], v213 offset:4096\n" \
"v_mfma_f32_16x16x32_bf16 v[60:63], a[56:59], a[24:27], v[60:63]\n" \
"ds_read_b128 a[20:23], v213 offset:6144\n" \
"v_mfma_f32_16x16x32_bf16 v[60:63], a[60:63], a[28:31], v[60:63]\n" \
"ds_read_b64_tr_b16 a[46:47], v215 offset:26624\n" \
"v_mfma_f32_16x16x32_bf16 v[56:59], a[48:51], a[24:27], v[56:59]\n" \
"buffer_load_dwordx4 v[184:187], v248, s[56:59], s71 offen offset:2048 sc1\n" \
"v_mfma_f32_16x16x32_bf16 v[56:59], a[52:55], a[28:31], v[56:59]\n" \
"s_waitcnt lgkmcnt(3)\n" \
"buffer_load_dwordx4 v[188:191], v248, s[56:59], s71 offen offset:3072 sc1\n" \
"v_mfma_f32_16x16x32_bf16 v[0:3], a[32:35], a[0:3], v[0:3]\n" \
"ds_read_b128 a[24:27], v213 offset:4352\n" \
"s_add_u32 s71, s76, 4096\n" \
"v_mfma_f32_16x16x32_bf16 v[0:3], a[36:39], a[4:7], v[0:3]\n" \
"s_waitcnt lgkmcnt(1)\n" \
"buffer_load_dwordx4 v[192:195], v248, s[56:59], s71 offen offset:0 sc1\n" \
"ds_read_b128 a[28:31], v213 offset:6400\n" \
"v_mfma_f32_16x16x32_bf16 v[4:7], a[40:43], a[0:3], v[4:7]\n" \
"s_sub_u32 s74, s74, 32768\n" \
"s_cselect_b32 s74, 131072, s74\n" \
"ds_read_b64_tr_b16 a[48:49], v214 offset:16896\n" \
"v_mfma_f32_16x16x32_bf16 v[4:7], a[44:47], a[4:7], v[4:7]\n" \
"ds_read_b64_tr_b16 a[50:51], v214 offset:18944\n" \
"v_lshrrev_b32 v132, 4, v248\n" \
"v_mfma_f32_16x16x32_bf16 v[20:23], a[40:43], a[8:11], v[20:23]\n" \
"ds_read_b64_tr_b16 a[52:53], v214 offset:25088\n" \
"v_and_b32 v133, 15, v132\n" \
"v_mfma_f32_16x16x32_bf16 v[20:23], a[44:47], a[12:15], v[20:23]\n" \
"ds_read_b64_tr_b16 a[54:55], v214 offset:27136\n" \
"v_mul_u32_u24 v128, 144, v133\n" \
"v_mfma_f32_16x16x32_bf16 v[16:19], a[32:35], a[8:11], v[16:19]\n" \
"ds_read_b64_tr_b16 a[56:57], v215 offset:16896\n" \
"v_lshrrev_b32 v133, 4, v132\n" \
"v_mfma_f32_16x16x32_bf16 v[16:19], a[36:39], a[12:15], v[16:19]\n" \
"ds_read_b64_tr_b16 a[58:59], v215 offset:18944\n" \
"v_lshl_add_u32 v128, v133, 3, v128\n" \
"v_mfma_f32_16x16x32_bf16 v[32:35], a[32:35], a[16:19], v[32:35]\n" \
"ds_read_b64_tr_b16 a[60:61], v215 offset:25088\n" \
"v_add_u32 v128, s83, v128\n" \
"v_mfma_f32_16x16x32_bf16 v[32:35], a[36:39], a[20:23], v[32:35]\n" \
"ds_read_b64_tr_b16 a[62:63], v215 offset:27136\n" \
"v_lshrrev_b32 v133, 3, v132\n" \
"v_mfma_f32_16x16x32_bf16 v[36:39], a[40:43], a[16:19], v[36:39]\n" \
"v_add_u32 v213, s74, v211\n" \
"v_add_u32 v214, s74, v212\n" \
"v_mfma_f32_16x16x32_bf16 v[36:39], a[44:47], a[20:23], v[36:39]\n" \
"s_waitcnt lgkmcnt(3)\n" \
"v_xor_b32 v215, 32, v214\n" \
"v_and_b32 v134, 7, v132\n" \
"v_mfma_f32_16x16x32_bf16 v[52:55], a[40:43], a[24:27], v[52:55]\n" \
"buffer_load_dwordx4 v[196:199], v248, s[56:59], s71 offen offset:1024 sc1\n" \
"v_mfma_f32_16x16x32_bf16 v[52:55], a[44:47], a[28:31], v[52:55]\n" \
"s_waitcnt vmcnt(6) lgkmcnt(0)\n" \
"buffer_load_dwordx4 v[200:203], v248, s[56:59], s71 offen offset:2048 sc1\n" \
"v_mfma_f32_16x16x32_bf16 v[48:51], a[32:35], a[24:27], v[48:51]\n" \
"buffer_load_dwordx4 v[204:207], v248, s[56:59], s71 offen offset:3072 sc1\n" \
"v_mfma_f32_16x16x32_bf16 v[48:51], a[36:39], a[28:31], v[48:51]\n" \
"s_barrier\n" \
"v_mfma_f32_16x16x32_bf16 v[8:11], a[48:51], a[0:3], v[8:11]\n" \
"s_mov_b32 s70, 0x20000000\n" \
"ds_read_b128 a[192:195], v213 offset:0\n" \
"ds_read_b64_tr_b16 a[128:129], v214 offset:16384\n" \
"v_mfma_f32_16x16x32_bf16 v[8:11], a[52:55], a[4:7], v[8:11]\n" \
"s_sub_u32 s74, s74, 32768\n" \
"s_cselect_b32 s74, 131072, s74\n" \
"ds_read_b64_tr_b16 a[130:131], v214 offset:18432\n" \
"v_mfma_f32_16x16x32_bf16 v[12:15], a[56:59], a[0:3], v[12:15]\n" \
"ds_read_b128 a[196:199], v213 offset:2048\n" \
"ds_read_b64_tr_b16 a[132:133], v214 offset:24576\n" \
"v_mfma_f32_16x16x32_bf16 v[12:15], a[60:63], a[4:7], v[12:15]\n" \
"ds_read_b64_tr_b16 a[134:135], v214 offset:26624\n" \
"v_add_u32 v226, s74, v211\n" \
"v_mfma_f32_16x16x32_bf16 v[28:31], a[56:59], a[8:11], v[28:31]\n" \
"ds_read_b128 a[200:203], v213 offset:256\n" \
"ds_read_b64_tr_b16 a[136:137], v215 offset:16384\n" \
"v_mfma_f32_16x16x32_bf16 v[28:31], a[60:63], a[12:15], v[28:31]\n" \
"ds_read_b64_tr_b16 a[138:139], v215 offset:18432\n" \
"v_add_u32 v227, s74, v212\n" \
"v_mfma_f32_16x16x32_bf16 v[24:27], a[48:51], a[8:11], v[24:27]\n" \
"ds_read_b128 a[204:207], v213 offset:2304\n" \
"ds_read_b64_tr_b16 a[140:141], v215 offset:24576\n" \
"v_mfma_f32_16x16x32_bf16 v[24:27], a[52:55], a[12:15], v[24:27]\n" \
"ds_read_b64_tr_b16 a[142:143], v215 offset:26624\n" \
"v_xor_b32 v228, 32, v227\n" \
"v_mfma_f32_16x16x32_bf16 v[40:43], a[48:51], a[16:19], v[40:43]\n" \
"s_cmp_eq_u32 s79, 65601536\n" \
"s_cselect_b32 s70, 65593344, s70\n" \
"ds_read_b64_tr_b16 a[144:145], v214 offset:16896\n" \
"v_mfma_f32_16x16x32_bf16 v[40:43], a[52:55], a[20:23], v[40:43]\n" \
"buffer_store_dword v224, v225, s[56:59], s70 offen sc1\n" \
"ds_read_b64_tr_b16 a[146:147], v214 offset:18944\n" \
"v_mfma_f32_16x16x32_bf16 v[44:47], a[56:59], a[16:19], v[44:47]\n" \
"ds_read_b64_tr_b16 a[148:149], v214 offset:25088\n" \
"v_mul_u32_u24 v129, 144, v133\n" \
"v_mfma_f32_16x16x32_bf16 v[44:47], a[60:63], a[20:23], v[44:47]\n" \
"ds_read_b64_tr_b16 a[150:151], v214 offset:27136\n" \
"v_lshl_add_u32 v129, v134, 4, v129\n" \
"v_mfma_f32_16x16x32_bf16 v[60:63], a[56:59], a[24:27], v[60:63]\n" \
"ds_read_b64_tr_b16 a[152:153], v215 offset:16896\n" \
"v_add_u32 v129, s83, v129\n" \
"v_mfma_f32_16x16x32_bf16 v[60:63], a[60:63], a[28:31], v[60:63]\n" \
"ds_read_b64_tr_b16 a[154:155], v215 offset:18944\n" \
"v_lshlrev_b32 v134, 4, v134\n" \
"v_mfma_f32_16x16x32_bf16 v[56:59], a[48:51], a[24:27], v[56:59]\n" \
"ds_read_b64_tr_b16 a[156:157], v215 offset:25088\n" \
"v_lshl_add_u32 v130, v133, 12, v134\n" \
"v_mfma_f32_16x16x32_bf16 v[56:59], a[52:55], a[28:31], v[56:59]\n" \
"s_waitcnt lgkmcnt(4)\n" \
"ds_read_b64_tr_b16 a[158:159], v215 offset:27136\n" \
"v_mfma_f32_16x16x32_bf16 v[0:3], a[128:131], a[192:195], v[0:3]\n" \
"ds_read_b128 a[224:227], v226 offset:0\n" \
"ds_read_b64_tr_b16 a[160:161], v227 offset:16384\n" \
"v_mfma_f32_16x16x32_bf16 v[0:3], a[132:135], a[196:199], v[0:3]\n" \
"ds_read_b64_tr_b16 a[162:163], v227 offset:18432\n" \
"v_mfma_f32_16x16x32_bf16 v[16:19], a[128:131], a[200:203], v[16:19]\n" \
"ds_read_b64_tr_b16 a[164:165], v227 offset:24576\n" \
"v_mfma_f32_16x16x32_bf16 v[16:19], a[132:135], a[204:207], v[16:19]\n" \
"ds_read_b128 a[228:231], v226 offset:2048\n" \
"ds_read_b64_tr_b16 a[166:167], v227 offset:26624\n" \
"v_mfma_f32_16x16x32_bf16 v[20:23], a[136:139], a[200:203], v[20:23]\n" \
"ds_read_b64_tr_b16 a[168:169], v228 offset:16384\n" \
"v_mfma_f32_16x16x32_bf16 v[20:23], a[140:143], a[204:207], v[20:23]\n" \
"ds_read_b64_tr_b16 a[170:171], v228 offset:18432\n" \
"v_mfma_f32_16x16x32_bf16 v[4:7], a[136:139], a[192:195], v[4:7]\n" \
"ds_read_b128 a[232:235], v226 offset:256\n" \
"ds_read_b64_tr_b16 a[172:173], v228 offset:24576\n" \
"v_mfma_f32_16x16x32_bf16 v[4:7], a[140:143], a[196:199], v[4:7]\n" \
"s_waitcnt lgkmcnt(6)\n" \
"ds_read_b64_tr_b16 a[174:175], v228 offset:26624\n" \
"v_mfma_f32_16x16x32_bf16 v[8:11], a[144:147], a[192:195], v[8:11]\n" \
"ds_read_b64_tr_b16 a[176:177], v227 offset:16896\n" \
"v_mfma_f32_16x16x32_bf16 v[8:11], a[148:151], a[196:199], v[8:11]\n" \
"ds_read_b128 a[236:239], v226 offset:2304\n" \
"ds_read_b64_tr_b16 a[178:179], v227 offset:18944\n" \
"v_mfma_f32_16x16x32_bf16 v[24:27], a[144:147], a[200:203], v[24:27]\n" \
"ds_read_b64_tr_b16 a[180:181], v227 offset:25088\n" \
"v_mfma_f32_16x16x32_bf16 v[24:27], a[148:151], a[204:207], v[24:27]\n" \
"ds_read_b64_tr_b16 a[182:183], v227 offset:27136\n" \
"v_mfma_f32_16x16x32_bf16 v[28:31], a[152:155], a[200:203], v[28:31]\n" \
"ds_read_b64_tr_b16 a[184:185], v228 offset:16896\n" \
"v_mfma_f32_16x16x32_bf16 v[28:31], a[156:159], a[204:207], v[28:31]\n" \
"ds_read_b64_tr_b16 a[186:187], v228 offset:18944\n" \
"v_mfma_f32_16x16x32_bf16 v[12:15], a[152:155], a[192:195], v[12:15]\n" \
"ds_read_b64_tr_b16 a[188:189], v228 offset:25088\n" \
"v_mfma_f32_16x16x32_bf16 v[12:15], a[156:159], a[196:199], v[12:15]\n" \
"s_waitcnt lgkmcnt(4)\n" \
"ds_read_b64_tr_b16 a[190:191], v228 offset:27136\n" \
"v_mfma_f32_16x16x32_bf16 v[0:3], a[160:163], a[224:227], v[0:3]\n" \
"s_waitcnt vmcnt(1)\n" \
"ds_read_b128 a[208:211], v213 offset:4096\n" \
"v_mfma_f32_16x16x32_bf16 v[0:3], a[164:167], a[228:231], v[0:3]\n" \
"ds_read_b128 a[212:215], v213 offset:6144\n" \
"v_mfma_f32_16x16x32_bf16 v[0:3], v[64:67], v[176:179], v[0:3]\n" \
"ds_read_b128 a[240:243], v226 offset:4096\n" \
"v_mfma_f32_16x16x32_bf16 v[16:19], a[160:163], a[232:235], v[16:19]\n" \
"ds_read_b128 a[244:247], v226 offset:6144\n" \
"v_mfma_f32_16x16x32_bf16 v[16:19], a[164:167], a[236:239], v[16:19]\n" \
"ds_read_b128 a[216:219], v213 offset:4352\n" \
"v_mfma_f32_16x16x32_bf16 v[16:19], v[64:67], v[184:187], v[16:19]\n" \
"ds_read_b128 a[220:223], v213 offset:6400\n" \
"v_cvt_pk_bf16_f32 v0, v0, v1\n" \
"v_mfma_f32_16x16x32_bf16 v[20:23], a[168:171], a[232:235], v[20:23]\n" \
"ds_read_b128 a[248:251], v226 offset:4352\n" \
"v_cvt_pk_bf16_f32 v1, v2, v3\n" \
"v_mfma_f32_16x16x32_bf16 v[20:23], a[172:175], a[236:239], v[20:23]\n" \
"ds_read_b128 a[252:255], v226 offset:6400\n" \
"v_mfma_f32_16x16x32_bf16 v[20:23], v[68:71], v[184:187], v[20:23]\n" \
"ds_write_b64 v128, v[0:1] offset:0\n" \
"v_mfma_f32_16x16x32_bf16 v[4:7], a[168:171], a[224:227], v[4:7]\n" \
"v_cvt_pk_bf16_f32 v16, v16, v17\n" \
"v_cvt_pk_bf16_f32 v17, v18, v19\n" \
"v_mfma_f32_16x16x32_bf16 v[4:7], a[172:175], a[228:231], v[4:7]\n" \
"ds_write_b64 v128, v[16:17] offset:2304\n" \
"v_mfma_f32_16x16x32_bf16 v[4:7], v[68:71], v[176:179], v[4:7]\n" \
"s_waitcnt lgkmcnt(3)\n" \
"v_cvt_pk_bf16_f32 v20, v20, v21\n" \
"v_cvt_pk_bf16_f32 v21, v22, v23\n" \
"v_mfma_f32_16x16x32_bf16 v[8:11], a[176:179], a[224:227], v[8:11]\n" \
"ds_write_b64 v128, v[20:21] offset:2336\n" \
"v_mfma_f32_16x16x32_bf16 v[8:11], a[180:183], a[228:231], v[8:11]\n" \
"v_mfma_f32_16x16x32_bf16 v[8:11], v[64:67], v[180:183], v[8:11]\n" \
"v_cvt_pk_bf16_f32 v4, v4, v5\n" \
"v_cvt_pk_bf16_f32 v5, v6, v7\n" \
"v_mfma_f32_16x16x32_bf16 v[24:27], a[176:179], a[232:235], v[24:27]\n" \
"ds_write_b64 v128, v[4:5] offset:32\n" \
"v_mfma_f32_16x16x32_bf16 v[24:27], a[180:183], a[236:239], v[24:27]\n" \
"v_mfma_f32_16x16x32_bf16 v[24:27], v[64:67], v[188:191], v[24:27]\n" \
"v_cvt_pk_bf16_f32 v8, v8, v9\n" \
"v_cvt_pk_bf16_f32 v9, v10, v11\n" \
"v_mfma_f32_16x16x32_bf16 v[28:31], a[184:187], a[232:235], v[28:31]\n" \
"ds_write_b64 v128, v[8:9] offset:64\n" \
"v_mfma_f32_16x16x32_bf16 v[28:31], a[188:191], a[236:239], v[28:31]\n" \
"v_mfma_f32_16x16x32_bf16 v[28:31], v[68:71], v[188:191], v[28:31]\n" \
"v_cvt_pk_bf16_f32 v24, v24, v25\n" \
"v_cvt_pk_bf16_f32 v25, v26, v27\n" \
"v_mfma_f32_16x16x32_bf16 v[12:15], a[184:187], a[224:227], v[12:15]\n" \
"ds_write_b64 v128, v[24:25] offset:2368\n" \
"v_mfma_f32_16x16x32_bf16 v[12:15], a[188:191], a[228:231], v[12:15]\n" \
"v_mfma_f32_16x16x32_bf16 v[12:15], v[68:71], v[180:183], v[12:15]\n" \
"v_cvt_pk_bf16_f32 v28, v28, v29\n" \
"v_cvt_pk_bf16_f32 v29, v30, v31\n" \
"v_mfma_f32_16x16x32_bf16 v[32:35], a[128:131], a[208:211], v[32:35]\n" \
"ds_write_b64 v128, v[28:29] offset:2400\n" \
"v_mfma_f32_16x16x32_bf16 v[32:35], a[132:135], a[212:215], v[32:35]\n" \
"v_mfma_f32_16x16x32_bf16 v[32:35], a[160:163], a[240:243], v[32:35]\n" \
"v_cvt_pk_bf16_f32 v12, v12, v13\n" \
"v_cvt_pk_bf16_f32 v13, v14, v15\n" \
"v_mfma_f32_16x16x32_bf16 v[32:35], a[164:167], a[244:247], v[32:35]\n" \
"ds_write_b64 v128, v[12:13] offset:96\n" \
"v_mfma_f32_16x16x32_bf16 v[32:35], v[64:67], v[192:195], v[32:35]\n" \
"ds_read_b128 v[140:143], v129 offset:0\n" \
"v_mfma_f32_16x16x32_bf16 v[36:39], a[168:171], a[240:243], v[36:39]\n" \
"ds_read_b128 v[144:147], v129 offset:1152\n" \
"v_mfma_f32_16x16x32_bf16 v[36:39], a[172:175], a[244:247], v[36:39]\n" \
"ds_read_b128 v[148:151], v129 offset:2304\n" \
"v_mfma_f32_16x16x32_bf16 v[36:39], a[136:139], a[208:211], v[36:39]\n" \
"ds_read_b128 v[152:155], v129 offset:3456\n" \
"v_mfma_f32_16x16x32_bf16 v[36:39], a[140:143], a[212:215], v[36:39]\n" \
"v_cvt_pk_bf16_f32 v32, v32, v33\n" \
"v_cvt_pk_bf16_f32 v33, v34, v35\n" \
"v_mfma_f32_16x16x32_bf16 v[36:39], v[68:71], v[192:195], v[36:39]\n" \
"ds_write_b64 v128, v[32:33] offset:0\n" \
"v_mfma_f32_16x16x32_bf16 v[40:43], a[144:147], a[208:211], v[40:43]\n" \
"v_mfma_f32_16x16x32_bf16 v[40:43], a[148:151], a[212:215], v[40:43]\n" \
"s_add_u32 s70, s77, 0\n" \
"s_waitcnt lgkmcnt(4)\n" \
"v_mfma_f32_16x16x32_bf16 v[40:43], a[176:179], a[240:243], v[40:43]\n" \
"v_cvt_pk_bf16_f32 v36, v36, v37\n" \
"v_cvt_pk_bf16_f32 v37, v38, v39\n" \
"v_mfma_f32_16x16x32_bf16 v[40:43], a[180:183], a[244:247], v[40:43]\n" \
"buffer_store_dwordx4 v[140:143], v130, s[52:55], s70 offen sc1\n" \
"ds_write_b64 v128, v[36:37] offset:32\n" \
"v_mfma_f32_16x16x32_bf16 v[40:43], v[64:67], v[196:199], v[40:43]\n" \
"v_mfma_f32_16x16x32_bf16 v[44:47], a[184:187], a[240:243], v[44:47]\n" \
"v_mfma_f32_16x16x32_bf16 v[44:47], a[188:191], a[244:247], v[44:47]\n" \
"v_mfma_f32_16x16x32_bf16 v[44:47], a[152:155], a[208:211], v[44:47]\n" \
"s_add_u32 s70, s77, 32768\n" \
"v_cvt_pk_bf16_f32 v40, v40, v41\n" \
"v_cvt_pk_bf16_f32 v41, v42, v43\n" \
"v_mfma_f32_16x16x32_bf16 v[44:47], a[156:159], a[212:215], v[44:47]\n" \
"s_waitcnt lgkmcnt(4)\n" \
"buffer_store_dwordx4 v[144:147], v130, s[52:55], s70 offen sc1\n" \
"ds_write_b64 v128, v[40:41] offset:64\n" \
"v_mfma_f32_16x16x32_bf16 v[44:47], v[68:71], v[196:199], v[44:47]\n" \
"v_mfma_f32_16x16x32_bf16 v[48:51], a[128:131], a[216:219], v[48:51]\n" \
"v_mfma_f32_16x16x32_bf16 v[48:51], a[132:135], a[220:223], v[48:51]\n" \
"v_mfma_f32_16x16x32_bf16 v[48:51], a[160:163], a[248:251], v[48:51]\n" \
"s_add_u32 s70, s77, 65536\n" \
"v_cvt_pk_bf16_f32 v44, v44, v45\n" \
"v_cvt_pk_bf16_f32 v45, v46, v47\n" \
"v_mfma_f32_16x16x32_bf16 v[48:51], a[164:167], a[252:255], v[48:51]\n" \
"s_waitcnt lgkmcnt(4)\n" \
"buffer_store_dwordx4 v[148:151], v130, s[52:55], s70 offen sc1\n" \
"ds_write_b64 v128, v[44:45] offset:96\n" \
"v_mfma_f32_16x16x32_bf16 v[48:51], v[64:67], v[200:203], v[48:51]\n" \
"ds_read_b128 v[156:159], v129 offset:0\n" \
"v_mfma_f32_16x16x32_bf16 v[52:55], a[168:171], a[248:251], v[52:55]\n" \
"ds_read_b128 v[160:163], v129 offset:1152\n" \
"v_mfma_f32_16x16x32_bf16 v[52:55], a[172:175], a[252:255], v[52:55]\n" \
"v_mfma_f32_16x16x32_bf16 v[52:55], a[136:139], a[216:219], v[52:55]\n" \
"s_add_u32 s70, s77, 98304\n" \
"v_cvt_pk_bf16_f32 v48, v48, v49\n" \
"v_cvt_pk_bf16_f32 v49, v50, v51\n" \
"v_mfma_f32_16x16x32_bf16 v[52:55], a[140:143], a[220:223], v[52:55]\n" \
"s_waitcnt lgkmcnt(6)\n" \
"buffer_store_dwordx4 v[152:155], v130, s[52:55], s70 offen sc1\n" \
"ds_write_b64 v128, v[48:49] offset:2304\n" \
"v_mfma_f32_16x16x32_bf16 v[52:55], v[68:71], v[200:203], v[52:55]\n" \
"v_mfma_f32_16x16x32_bf16 v[56:59], a[144:147], a[216:219], v[56:59]\n" \
"v_mfma_f32_16x16x32_bf16 v[56:59], a[148:151], a[220:223], v[56:59]\n" \
"v_mfma_f32_16x16x32_bf16 v[56:59], a[176:179], a[248:251], v[56:59]\n" \
"s_add_u32 s70, s77, 131072\n" \
"v_cvt_pk_bf16_f32 v52, v52, v53\n" \
"v_cvt_pk_bf16_f32 v53, v54, v55\n" \
"v_mfma_f32_16x16x32_bf16 v[56:59], a[180:183], a[252:255], v[56:59]\n" \
"s_waitcnt lgkmcnt(2)\n" \
"buffer_store_dwordx4 v[156:159], v130, s[52:55], s70 offen sc1\n" \
"ds_write_b64 v128, v[52:53] offset:2336\n" \
"v_mfma_f32_16x16x32_bf16 v[56:59], v[64:67], v[204:207], v[56:59]\n" \
"v_mfma_f32_16x16x32_bf16 v[60:63], a[184:187], a[248:251], v[60:63]\n" \
"v_mfma_f32_16x16x32_bf16 v[60:63], a[188:191], a[252:255], v[60:63]\n" \
"v_mfma_f32_16x16x32_bf16 v[60:63], a[152:155], a[216:219], v[60:63]\n" \
"s_add_u32 s70, s77, 163840\n" \
"v_cvt_pk_bf16_f32 v56, v56, v57\n" \
"v_cvt_pk_bf16_f32 v57, v58, v59\n" \
"v_mfma_f32_16x16x32_bf16 v[60:63], a[156:159], a[220:223], v[60:63]\n" \
"s_waitcnt lgkmcnt(2)\n" \
"buffer_store_dwordx4 v[160:163], v130, s[52:55], s70 offen sc1\n" \
"ds_write_b64 v128, v[56:57] offset:2368\n" \
"v_mfma_f32_16x16x32_bf16 v[60:63], v[68:71], v[204:207], v[60:63]\n" \
"s_nop 7\n" \
"v_cvt_pk_bf16_f32 v60, v60, v61\n" \
"v_cvt_pk_bf16_f32 v61, v62, v63\n" \
"ds_write_b64 v128, v[60:61] offset:2400\n" \
"ds_read_b128 v[164:167], v129 offset:2304\n" \
"ds_read_b128 v[168:171], v129 offset:3456\n" \
"s_add_u32 s70, s77, 196608\n" \
"s_waitcnt lgkmcnt(1)\n" \
"buffer_store_dwordx4 v[164:167], v130, s[52:55], s70 offen sc1\n" \
"s_add_u32 s70, s77, 229376\n" \
"s_waitcnt lgkmcnt(0)\n" \
"buffer_store_dwordx4 v[168:171], v130, s[52:55], s70 offen sc1\n" \
"s_endpgm\n" \
"Lpg12371_%=:\n" \
"s_cbranch_execz Lpg13344_%=\n" \
"Lo0_%=:\n" \
"v_mfma_f32_16x16x32_bf16 v[0:3], a[32:35], a[0:3], v[0:3]\n" \
"ds_read_b128 a[24:27], v213 offset:4352\n" \
"s_add_u32 m0, s75, s95\n" \
"v_mfma_f32_16x16x32_bf16 v[0:3], a[36:39], a[4:7], v[0:3]\n" \
"s_waitcnt lgkmcnt(1)\n" \
"ds_read_b128 a[28:31], v213 offset:6400\n" \
"global_load_lds_dwordx4 v222, s[36:37] offset:0 sc1\n" \
"v_mfma_f32_16x16x32_bf16 v[4:7], a[40:43], a[0:3], v[4:7]\n" \
"s_sub_u32 s74, s74, 32768\n" \
"s_cselect_b32 s74, 131072, s74\n" \
"ds_read_b64_tr_b16 a[48:49], v214 offset:16896\n" \
"v_mfma_f32_16x16x32_bf16 v[4:7], a[44:47], a[4:7], v[4:7]\n" \
"ds_read_b64_tr_b16 a[50:51], v214 offset:18944\n" \
"v_mfma_f32_16x16x32_bf16 v[20:23], a[40:43], a[8:11], v[20:23]\n" \
"ds_read_b64_tr_b16 a[52:53], v214 offset:25088\n" \
"v_mfma_f32_16x16x32_bf16 v[20:23], a[44:47], a[12:15], v[20:23]\n" \
"ds_read_b64_tr_b16 a[54:55], v214 offset:27136\n" \
"global_load_lds_dwordx4 v222, s[36:37] offset:1024 sc1\n" \
"v_mfma_f32_16x16x32_bf16 v[16:19], a[32:35], a[8:11], v[16:19]\n" \
"ds_read_b64_tr_b16 a[56:57], v215 offset:16896\n" \
"v_mfma_f32_16x16x32_bf16 v[16:19], a[36:39], a[12:15], v[16:19]\n" \
"ds_read_b64_tr_b16 a[58:59], v215 offset:18944\n" \
"v_mfma_f32_16x16x32_bf16 v[32:35], a[32:35], a[16:19], v[32:35]\n" \
"ds_read_b64_tr_b16 a[60:61], v215 offset:25088\n" \
"v_mfma_f32_16x16x32_bf16 v[32:35], a[36:39], a[20:23], v[32:35]\n" \
"ds_read_b64_tr_b16 a[62:63], v215 offset:27136\n" \
"global_load_lds_dwordx4 v222, s[36:37] offset:2048 sc1\n" \
"v_mfma_f32_16x16x32_bf16 v[36:39], a[40:43], a[16:19], v[36:39]\n" \
"v_add_u32 v213, s74, v211\n" \
"v_add_u32 v214, s74, v212\n" \
"v_mfma_f32_16x16x32_bf16 v[36:39], a[44:47], a[20:23], v[36:39]\n" \
"s_waitcnt lgkmcnt(3)\n" \
"v_xor_b32 v215, 32, v214\n" \
"v_mfma_f32_16x16x32_bf16 v[52:55], a[40:43], a[24:27], v[52:55]\n" \
"v_mfma_f32_16x16x32_bf16 v[52:55], a[44:47], a[28:31], v[52:55]\n" \
"s_waitcnt vmcnt(19) lgkmcnt(0)\n" \
"global_load_lds_dwordx4 v222, s[36:37] offset:3072 sc1\n" \
"v_mfma_f32_16x16x32_bf16 v[48:51], a[32:35], a[24:27], v[48:51]\n" \
"s_add_u32 s36, s36, 0x1000\n" \
"v_mfma_f32_16x16x32_bf16 v[48:51], a[36:39], a[28:31], v[48:51]\n" \
"s_barrier\n" \
"v_mfma_f32_16x16x32_bf16 v[8:11], a[48:51], a[0:3], v[8:11]\n" \
"s_add_u32 m0, m0, 0x5000\n" \
"ds_read_b64_tr_b16 a[32:33], v214 offset:16384\n" \
"v_mfma_f32_16x16x32_bf16 v[8:11], a[52:55], a[4:7], v[8:11]\n" \
"ds_read_b64_tr_b16 a[34:35], v214 offset:18432\n" \
"global_load_lds_dwordx4 v216, s[38:39] offset:-4096\n" \
"v_mfma_f32_16x16x32_bf16 v[12:15], a[56:59], a[0:3], v[12:15]\n" \
"ds_read_b64_tr_b16 a[36:37], v214 offset:24576\n" \
"v_mfma_f32_16x16x32_bf16 v[12:15], a[60:63], a[4:7], v[12:15]\n" \
"ds_read_b128 a[0:3], v213 offset:0\n" \
"v_mfma_f32_16x16x32_bf16 v[28:31], a[56:59], a[8:11], v[28:31]\n" \
"ds_read_b128 a[4:7], v213 offset:2048\n" \
"v_mfma_f32_16x16x32_bf16 v[28:31], a[60:63], a[12:15], v[28:31]\n" \
"ds_read_b64_tr_b16 a[38:39], v214 offset:26624\n" \
"global_load_lds_dwordx4 v217, s[38:39] offset:-3072\n" \
"v_mfma_f32_16x16x32_bf16 v[24:27], a[48:51], a[8:11], v[24:27]\n" \
"ds_read_b64_tr_b16 a[40:41], v215 offset:16384\n" \
"v_mfma_f32_16x16x32_bf16 v[24:27], a[52:55], a[12:15], v[24:27]\n" \
"ds_read_b128 a[8:11], v213 offset:256\n" \
"v_mfma_f32_16x16x32_bf16 v[40:43], a[48:51], a[16:19], v[40:43]\n" \
"ds_read_b128 a[12:15], v213 offset:2304\n" \
"v_mfma_f32_16x16x32_bf16 v[40:43], a[52:55], a[20:23], v[40:43]\n" \
"ds_read_b64_tr_b16 a[42:43], v215 offset:18432\n" \
"global_load_lds_dwordx4 v218, s[38:39] offset:-2048\n" \
"v_mfma_f32_16x16x32_bf16 v[44:47], a[56:59], a[16:19], v[44:47]\n" \
"ds_read_b64_tr_b16 a[44:45], v215 offset:24576\n" \
"v_mfma_f32_16x16x32_bf16 v[44:47], a[60:63], a[20:23], v[44:47]\n" \
"ds_read_b128 a[16:19], v213 offset:4096\n" \
"global_load_lds_dwordx4 v219, s[38:39] offset:-1024\n" \
"v_mfma_f32_16x16x32_bf16 v[60:63], a[56:59], a[24:27], v[60:63]\n" \
"s_add_u32 s38, s38, 0x20000\n" \
"s_addc_u32 s39, s39, 0\n" \
"ds_read_b128 a[20:23], v213 offset:6144\n" \
"v_mfma_f32_16x16x32_bf16 v[60:63], a[60:63], a[28:31], v[60:63]\n" \
"s_sub_u32 s75, s75, 32768\n" \
"s_cselect_b32 s75, 131072, s75\n" \
"ds_read_b64_tr_b16 a[46:47], v215 offset:26624\n" \
"v_mfma_f32_16x16x32_bf16 v[56:59], a[48:51], a[24:27], v[56:59]\n" \
"s_sub_u32 s72, s72, 1\n" \
"s_cmp_lg_u32 s72, 0\n" \
"v_mfma_f32_16x16x32_bf16 v[56:59], a[52:55], a[28:31], v[56:59]\n" \
"s_waitcnt lgkmcnt(3)\n" \
"s_cbranch_scc1 Lo0_%=\n" \
"s_setpc_b64 s[68:69]\n" \
"Lrole1_%=:\n" \
"Lpg12459_%=:\n" \
"s_cbranch_execz Lcb1_%=\n" \
"s_sub_u32 s72, 50, s64\n" \
"s_call_b64 s[68:69], Lo0_%=\n" \
"buffer_load_dword v220, v221, s[56:59], s82 offen sc1\n" \
"s_mov_b32 s72, 2\n" \
"s_call_b64 s[68:69], Lo0_%=\n" \
"s_waitcnt vmcnt(16)\n" \
"v_cmp_ne_u32 vcc, s60, v220\n" \
"s_nop 1\n" \
"s_and_b32 vcc_hi, vcc_lo, 65535\n" \
"s_cbranch_scc1 Lpl5_%=\n" \
"Lpr5_%=:\n" \
"s_mov_b32 s36, s73\n" \
"s_mov_b64 s[38:39], s[40:41]\n" \
"s_mov_b32 s72, 4\n" \
"s_call_b64 s[68:69], Lo0_%=\n" \
"v_mfma_f32_16x16x32_bf16 v[64:67], a[32:35], a[0:3], 0\n" \
"ds_read_b128 a[24:27], v213 offset:4352\n" \
"s_add_u32 m0, s75, s95\n" \
"v_mfma_f32_16x16x32_bf16 v[64:67], a[36:39], a[4:7], v[64:67]\n" \
"s_waitcnt lgkmcnt(1)\n" \
"ds_read_b128 a[28:31], v213 offset:6400\n" \
"global_load_lds_dwordx4 v222, s[36:37] offset:0 sc1\n" \
"v_mfma_f32_16x16x32_bf16 v[68:71], a[40:43], a[0:3], 0\n" \
"s_sub_u32 s74, s74, 32768\n" \
"s_cselect_b32 s74, 131072, s74\n" \
"ds_read_b64_tr_b16 a[48:49], v214 offset:16896\n" \
"v_mfma_f32_16x16x32_bf16 v[68:71], a[44:47], a[4:7], v[68:71]\n" \
"ds_read_b64_tr_b16 a[50:51], v214 offset:18944\n" \
"v_cvt_pk_bf16_f32 v0, v0, v1\n" \
"v_mfma_f32_16x16x32_bf16 v[84:87], a[40:43], a[8:11], 0\n" \
"ds_read_b64_tr_b16 a[52:53], v214 offset:25088\n" \
"v_cvt_pk_bf16_f32 v1, v2, v3\n" \
"v_mfma_f32_16x16x32_bf16 v[84:87], a[44:47], a[12:15], v[84:87]\n" \
"ds_read_b64_tr_b16 a[54:55], v214 offset:27136\n" \
"global_load_lds_dwordx4 v222, s[36:37] offset:1024 sc1\n" \
"v_mfma_f32_16x16x32_bf16 v[80:83], a[32:35], a[8:11], 0\n" \
"ds_read_b64_tr_b16 a[56:57], v215 offset:16896\n" \
"v_cvt_pk_bf16_f32 v2, v4, v5\n" \
"v_mfma_f32_16x16x32_bf16 v[80:83], a[36:39], a[12:15], v[80:83]\n" \
"ds_read_b64_tr_b16 a[58:59], v215 offset:18944\n" \
"v_cvt_pk_bf16_f32 v3, v6, v7\n" \
"v_mfma_f32_16x16x32_bf16 v[96:99], a[32:35], a[16:19], 0\n" \
"s_add_u32 s71, s76, 0\n" \
"ds_read_b64_tr_b16 a[60:61], v215 offset:25088\n" \
"v_mfma_f32_16x16x32_bf16 v[96:99], a[36:39], a[20:23], v[96:99]\n" \
"ds_read_b64_tr_b16 a[62:63], v215 offset:27136\n" \
"global_load_lds_dwordx4 v222, s[36:37] offset:2048 sc1\n" \
"v_mfma_f32_16x16x32_bf16 v[100:103], a[40:43], a[16:19], 0\n" \
"v_add_u32 v213, s74, v211\n" \
"v_add_u32 v214, s74, v212\n" \
"v_mfma_f32_16x16x32_bf16 v[100:103], a[44:47], a[20:23], v[100:103]\n" \
"s_waitcnt lgkmcnt(3)\n" \
"v_xor_b32 v215, 32, v214\n" \
"v_mfma_f32_16x16x32_bf16 v[116:119], a[40:43], a[24:27], 0\n" \
"v_mfma_f32_16x16x32_bf16 v[116:119], a[44:47], a[28:31], v[116:119]\n" \
"s_waitcnt vmcnt(19) lgkmcnt(0)\n" \
"global_load_lds_dwordx4 v222, s[36:37] offset:3072 sc1\n" \
"v_mfma_f32_16x16x32_bf16 v[112:115], a[32:35], a[24:27], 0\n" \
"s_add_u32 s36, s36, 0x1000\n" \
"v_mfma_f32_16x16x32_bf16 v[112:115], a[36:39], a[28:31], v[112:115]\n" \
"s_barrier\n" \
"v_mfma_f32_16x16x32_bf16 v[72:75], a[48:51], a[0:3], 0\n" \
"s_add_u32 m0, m0, 0x5000\n" \
"ds_read_b64_tr_b16 a[32:33], v214 offset:16384\n" \
"v_mfma_f32_16x16x32_bf16 v[72:75], a[52:55], a[4:7], v[72:75]\n" \
"ds_read_b64_tr_b16 a[34:35], v214 offset:18432\n" \
"global_load_lds_dwordx4 v216, s[38:39] offset:-4096\n" \
"v_mfma_f32_16x16x32_bf16 v[76:79], a[56:59], a[0:3], 0\n" \
"ds_read_b64_tr_b16 a[36:37], v214 offset:24576\n" \
"v_mfma_f32_16x16x32_bf16 v[76:79], a[60:63], a[4:7], v[76:79]\n" \
"ds_read_b128 a[0:3], v213 offset:0\n" \
"v_mfma_f32_16x16x32_bf16 v[92:95], a[56:59], a[8:11], 0\n" \
"ds_read_b128 a[4:7], v213 offset:2048\n" \
"v_mfma_f32_16x16x32_bf16 v[92:95], a[60:63], a[12:15], v[92:95]\n" \
"ds_read_b64_tr_b16 a[38:39], v214 offset:26624\n" \
"global_load_lds_dwordx4 v217, s[38:39] offset:-3072\n" \
"v_mfma_f32_16x16x32_bf16 v[88:91], a[48:51], a[8:11], 0\n" \
"ds_read_b64_tr_b16 a[40:41], v215 offset:16384\n" \
"v_mfma_f32_16x16x32_bf16 v[88:91], a[52:55], a[12:15], v[88:91]\n" \
"buffer_store_dwordx4 v[0:3], v248, s[56:59], s71 offen offset:0 sc1\n" \
"ds_read_b128 a[8:11], v213 offset:256\n" \
"v_cvt_pk_bf16_f32 v4, v8, v9\n" \
"v_cvt_pk_bf16_f32 v5, v10, v11\n" \
"v_cvt_pk_bf16_f32 v6, v12, v13\n" \
"v_mfma_f32_16x16x32_bf16 v[104:107], a[48:51], a[16:19], 0\n" \
"ds_read_b128 a[12:15], v213 offset:2304\n" \
"v_cvt_pk_bf16_f32 v7, v14, v15\n" \
"v_mfma_f32_16x16x32_bf16 v[104:107], a[52:55], a[20:23], v[104:107]\n" \
"ds_read_b64_tr_b16 a[42:43], v215 offset:18432\n" \
"global_load_lds_dwordx4 v218, s[38:39] offset:-2048\n" \
"v_mfma_f32_16x16x32_bf16 v[108:111], a[56:59], a[16:19], 0\n" \
"ds_read_b64_tr_b16 a[44:45], v215 offset:24576\n" \
"v_mfma_f32_16x16x32_bf16 v[108:111], a[60:63], a[20:23], v[108:111]\n" \
"buffer_store_dwordx4 v[4:7], v248, s[56:59], s71 offen offset:1024 sc1\n" \
"ds_read_b128 a[16:19], v213 offset:4096\n" \
"v_cvt_pk_bf16_f32 v8, v16, v17\n" \
"v_cvt_pk_bf16_f32 v9, v18, v19\n" \
"v_cvt_pk_bf16_f32 v10, v20, v21\n" \
"v_mfma_f32_16x16x32_bf16 v[124:127], a[56:59], a[24:27], 0\n" \
"ds_read_b128 a[20:23], v213 offset:6144\n" \
"global_load_lds_dwordx4 v219, s[38:39] offset:-1024\n" \
"v_mfma_f32_16x16x32_bf16 v[124:127], a[60:63], a[28:31], v[124:127]\n" \
"s_add_u32 s38, s38, 0x20000\n" \
"s_addc_u32 s39, s39, 0\n" \
"ds_read_b64_tr_b16 a[46:47], v215 offset:26624\n" \
"v_mfma_f32_16x16x32_bf16 v[120:123], a[48:51], a[24:27], 0\n" \
"s_sub_u32 s75, s75, 32768\n" \
"s_cselect_b32 s75, 131072, s75\n" \
"v_cvt_pk_bf16_f32 v11, v22, v23\n" \
"v_mfma_f32_16x16x32_bf16 v[120:123], a[52:55], a[28:31], v[120:123]\n" \
"s_waitcnt lgkmcnt(3)\n" \
"buffer_store_dwordx4 v[8:11], v248, s[56:59], s71 offen offset:2048 sc1\n" \
"v_cvt_pk_bf16_f32 v12, v24, v25\n" \
"v_cvt_pk_bf16_f32 v13, v26, v27\n" \
"v_cvt_pk_bf16_f32 v14, v28, v29\n" \
"v_cvt_pk_bf16_f32 v15, v30, v31\n" \
"v_mfma_f32_16x16x32_bf16 v[64:67], a[32:35], a[0:3], v[64:67]\n" \
"Lpg12577_%=:\n" \
"s_cbranch_execz Lcb1_%=\n" \
"ds_read_b128 a[24:27], v213 offset:4352\n" \
"s_add_u32 m0, s75, s95\n" \
"v_mfma_f32_16x16x32_bf16 v[64:67], a[36:39], a[4:7], v[64:67]\n" \
"s_waitcnt lgkmcnt(1)\n" \
"ds_read_b128 a[28:31], v213 offset:6400\n" \
"global_load_lds_dwordx4 v222, s[36:37] offset:0 sc1\n" \
"v_mfma_f32_16x16x32_bf16 v[68:71], a[40:43], a[0:3], v[68:71]\n" \
"s_sub_u32 s74, s74, 32768\n" \
"s_cselect_b32 s74, 131072, s74\n" \
"ds_read_b64_tr_b16 a[48:49], v214 offset:16896\n" \
"v_mfma_f32_16x16x32_bf16 v[68:71], a[44:47], a[4:7], v[68:71]\n" \
"ds_read_b64_tr_b16 a[50:51], v214 offset:18944\n" \
"v_mfma_f32_16x16x32_bf16 v[84:87], a[40:43], a[8:11], v[84:87]\n" \
"ds_read_b64_tr_b16 a[52:53], v214 offset:25088\n" \
"v_mfma_f32_16x16x32_bf16 v[84:87], a[44:47], a[12:15], v[84:87]\n" \
"ds_read_b64_tr_b16 a[54:55], v214 offset:27136\n" \
"global_load_lds_dwordx4 v222, s[36:37] offset:1024 sc1\n" \
"v_mfma_f32_16x16x32_bf16 v[80:83], a[32:35], a[8:11], v[80:83]\n" \
"ds_read_b64_tr_b16 a[56:57], v215 offset:16896\n" \
"v_mfma_f32_16x16x32_bf16 v[80:83], a[36:39], a[12:15], v[80:83]\n" \
"ds_read_b64_tr_b16 a[58:59], v215 offset:18944\n" \
"v_mfma_f32_16x16x32_bf16 v[96:99], a[32:35], a[16:19], v[96:99]\n" \
"ds_read_b64_tr_b16 a[60:61], v215 offset:25088\n" \
"v_mfma_f32_16x16x32_bf16 v[96:99], a[36:39], a[20:23], v[96:99]\n" \
"ds_read_b64_tr_b16 a[62:63], v215 offset:27136\n" \
"global_load_lds_dwordx4 v222, s[36:37] offset:2048 sc1\n" \
"v_mfma_f32_16x16x32_bf16 v[100:103], a[40:43], a[16:19], v[100:103]\n" \
"v_add_u32 v213, s74, v211\n" \
"v_add_u32 v214, s74, v212\n" \
"v_mfma_f32_16x16x32_bf16 v[100:103], a[44:47], a[20:23], v[100:103]\n" \
"s_waitcnt lgkmcnt(3)\n" \
"v_xor_b32 v215, 32, v214\n" \
"v_mfma_f32_16x16x32_bf16 v[116:119], a[40:43], a[24:27], v[116:119]\n" \
"v_mfma_f32_16x16x32_bf16 v[116:119], a[44:47], a[28:31], v[116:119]\n" \
"s_waitcnt vmcnt(22) lgkmcnt(0)\n" \
"global_load_lds_dwordx4 v222, s[36:37] offset:3072 sc1\n" \
"v_mfma_f32_16x16x32_bf16 v[112:115], a[32:35], a[24:27], v[112:115]\n" \
"s_add_u32 s36, s36, 0x1000\n" \
"v_mfma_f32_16x16x32_bf16 v[112:115], a[36:39], a[28:31], v[112:115]\n" \
"s_barrier\n" \
"v_mfma_f32_16x16x32_bf16 v[72:75], a[48:51], a[0:3], v[72:75]\n" \
"s_add_u32 m0, m0, 0x5000\n" \
"ds_read_b64_tr_b16 a[32:33], v214 offset:16384\n" \
"v_mfma_f32_16x16x32_bf16 v[72:75], a[52:55], a[4:7], v[72:75]\n" \
"ds_read_b64_tr_b16 a[34:35], v214 offset:18432\n" \
"global_load_lds_dwordx4 v216, s[38:39] offset:-4096\n" \
"v_mfma_f32_16x16x32_bf16 v[76:79], a[56:59], a[0:3], v[76:79]\n" \
"ds_read_b64_tr_b16 a[36:37], v214 offset:24576\n" \
"v_mfma_f32_16x16x32_bf16 v[76:79], a[60:63], a[4:7], v[76:79]\n" \
"ds_read_b128 a[0:3], v213 offset:0\n" \
"v_mfma_f32_16x16x32_bf16 v[92:95], a[56:59], a[8:11], v[92:95]\n" \
"ds_read_b128 a[4:7], v213 offset:2048\n" \
"v_mfma_f32_16x16x32_bf16 v[92:95], a[60:63], a[12:15], v[92:95]\n" \
"ds_read_b64_tr_b16 a[38:39], v214 offset:26624\n" \
"global_load_lds_dwordx4 v217, s[38:39] offset:-3072\n" \
"v_mfma_f32_16x16x32_bf16 v[88:91], a[48:51], a[8:11], v[88:91]\n" \
"ds_read_b64_tr_b16 a[40:41], v215 offset:16384\n" \
"v_mfma_f32_16x16x32_bf16 v[88:91], a[52:55], a[12:15], v[88:91]\n" \
"buffer_store_dwordx4 v[12:15], v248, s[56:59], s71 offen offset:3072 sc1\n" \
"ds_read_b128 a[8:11], v213 offset:256\n" \
"v_cvt_pk_bf16_f32 v16, v32, v33\n" \
"v_cvt_pk_bf16_f32 v17, v34, v35\n" \
"v_cvt_pk_bf16_f32 v18, v36, v37\n" \
"v_mfma_f32_16x16x32_bf16 v[104:107], a[48:51], a[16:19], v[104:107]\n" \
"ds_read_b128 a[12:15], v213 offset:2304\n" \
"v_cvt_pk_bf16_f32 v19, v38, v39\n" \
"v_mfma_f32_16x16x32_bf16 v[104:107], a[52:55], a[20:23], v[104:107]\n" \
"s_add_u32 s71, s76, 4096\n" \
"ds_read_b64_tr_b16 a[42:43], v215 offset:18432\n" \
"global_load_lds_dwordx4 v218, s[38:39] offset:-2048\n" \
"v_mfma_f32_16x16x32_bf16 v[108:111], a[56:59], a[16:19], v[108:111]\n" \
"ds_read_b64_tr_b16 a[44:45], v215 offset:24576\n" \
"v_mfma_f32_16x16x32_bf16 v[108:111], a[60:63], a[20:23], v[108:111]\n" \
"buffer_store_dwordx4 v[16:19], v248, s[56:59], s71 offen offset:0 sc1\n" \
"ds_read_b128 a[16:19], v213 offset:4096\n" \
"v_cvt_pk_bf16_f32 v20, v40, v41\n" \
"v_cvt_pk_bf16_f32 v21, v42, v43\n" \
"v_cvt_pk_bf16_f32 v22, v44, v45\n" \
"v_mfma_f32_16x16x32_bf16 v[124:127], a[56:59], a[24:27], v[124:127]\n" \
"ds_read_b128 a[20:23], v213 offset:6144\n" \
"global_load_lds_dwordx4 v219, s[38:39] offset:-1024\n" \
"v_mfma_f32_16x16x32_bf16 v[124:127], a[60:63], a[28:31], v[124:127]\n" \
"s_add_u32 s38, s38, 0x20000\n" \
"s_addc_u32 s39, s39, 0\n" \
"ds_read_b64_tr_b16 a[46:47], v215 offset:26624\n" \
"v_mfma_f32_16x16x32_bf16 v[120:123], a[48:51], a[24:27], v[120:123]\n" \
"s_sub_u32 s75, s75, 32768\n" \
"s_cselect_b32 s75, 131072, s75\n" \
"v_cvt_pk_bf16_f32 v23, v46, v47\n" \
"v_mfma_f32_16x16x32_bf16 v[120:123], a[52:55], a[28:31], v[120:123]\n" \
"s_waitcnt lgkmcnt(3)\n" \
"buffer_store_dwordx4 v[20:23], v248, s[56:59], s71 offen offset:1024 sc1\n" \
"v_cvt_pk_bf16_f32 v24, v48, v49\n" \
"v_cvt_pk_bf16_f32 v25, v50, v51\n" \
"v_cvt_pk_bf16_f32 v26, v52, v53\n" \
"v_cvt_pk_bf16_f32 v27, v54, v55\n" \
"v_mfma_f32_16x16x32_bf16 v[64:67], a[32:35], a[0:3], v[64:67]\n" \
"ds_read_b128 a[24:27], v213 offset:4352\n" \
"s_add_u32 m0, s75, s95\n" \
"v_mfma_f32_16x16x32_bf16 v[64:67], a[36:39], a[4:7], v[64:67]\n" \
"s_waitcnt lgkmcnt(1)\n" \
"ds_read_b128 a[28:31], v213 offset:6400\n" \
"global_load_lds_dwordx4 v222, s[36:37] offset:0 sc1\n" \
"v_mfma_f32_16x16x32_bf16 v[68:71], a[40:43], a[0:3], v[68:71]\n" \
"s_sub_u32 s74, s74, 32768\n" \
"s_cselect_b32 s74, 131072, s74\n" \
"ds_read_b64_tr_b16 a[48:49], v214 offset:16896\n" \
"v_mfma_f32_16x16x32_bf16 v[68:71], a[44:47], a[4:7], v[68:71]\n" \
"ds_read_b64_tr_b16 a[50:51], v214 offset:18944\n" \
"v_mfma_f32_16x16x32_bf16 v[84:87], a[40:43], a[8:11], v[84:87]\n" \
"ds_read_b64_tr_b16 a[52:53], v214 offset:25088\n" \
"v_mfma_f32_16x16x32_bf16 v[84:87], a[44:47], a[12:15], v[84:87]\n" \
"ds_read_b64_tr_b16 a[54:55], v214 offset:27136\n" \
"global_load_lds_dwordx4 v222, s[36:37] offset:1024 sc1\n" \
"v_mfma_f32_16x16x32_bf16 v[80:83], a[32:35], a[8:11], v[80:83]\n" \
"ds_read_b64_tr_b16 a[56:57], v215 offset:16896\n" \
"v_mfma_f32_16x16x32_bf16 v[80:83], a[36:39], a[12:15], v[80:83]\n" \
"ds_read_b64_tr_b16 a[58:59], v215 offset:18944\n" \
"v_mfma_f32_16x16x32_bf16 v[96:99], a[32:35], a[16:19], v[96:99]\n" \
"ds_read_b64_tr_b16 a[60:61], v215 offset:25088\n" \
"v_mfma_f32_16x16x32_bf16 v[96:99], a[36:39], a[20:23], v[96:99]\n" \
"ds_read_b64_tr_b16 a[62:63], v215 offset:27136\n" \
"global_load_lds_dwordx4 v222, s[36:37] offset:2048 sc1\n" \
"v_mfma_f32_16x16x32_bf16 v[100:103], a[40:43], a[16:19], v[100:103]\n" \
"v_add_u32 v213, s74, v211\n" \
"v_add_u32 v214, s74, v212\n" \
"v_mfma_f32_16x16x32_bf16 v[100:103], a[44:47], a[20:23], v[100:103]\n" \
"s_waitcnt lgkmcnt(3)\n" \
"v_xor_b32 v215, 32, v214\n" \
"v_mfma_f32_16x16x32_bf16 v[116:119], a[40:43], a[24:27], v[116:119]\n" \
"v_mfma_f32_16x16x32_bf16 v[116:119], a[44:47], a[28:31], v[116:119]\n" \
"s_waitcnt vmcnt(25) lgkmcnt(0)\n" \
"global_load_lds_dwordx4 v222, s[36:37] offset:3072 sc1\n" \
"v_mfma_f32_16x16x32_bf16 v[112:115], a[32:35], a[24:27], v[112:115]\n" \
"s_add_u32 s36, s36, 0x1000\n" \
"v_mfma_f32_16x16x32_bf16 v[112:115], a[36:39], a[28:31], v[112:115]\n" \
"s_barrier\n" \
"v_mfma_f32_16x16x32_bf16 v[72:75], a[48:51], a[0:3], v[72:75]\n" \
"s_add_u32 m0, m0, 0x5000\n" \
"ds_read_b64_tr_b16 a[32:33], v214 offset:16384\n" \
"v_mfma_f32_16x16x32_bf16 v[72:75], a[52:55], a[4:7], v[72:75]\n" \
"ds_read_b64_tr_b16 a[34:35], v214 offset:18432\n" \
"global_load_lds_dwordx4 v216, s[38:39] offset:-4096\n" \
"v_mfma_f32_16x16x32_bf16 v[76:79], a[56:59], a[0:3], v[76:79]\n" \
"ds_read_b64_tr_b16 a[36:37], v214 offset:24576\n" \
"v_mfma_f32_16x16x32_bf16 v[76:79], a[60:63], a[4:7], v[76:79]\n" \
"ds_read_b128 a[0:3], v213 offset:0\n" \
"v_mfma_f32_16x16x32_bf16 v[92:95], a[56:59], a[8:11], v[92:95]\n" \
"ds_read_b128 a[4:7], v213 offset:2048\n" \
"v_mfma_f32_16x16x32_bf16 v[92:95], a[60:63], a[12:15], v[92:95]\n" \
"ds_read_b64_tr_b16 a[38:39], v214 offset:26624\n" \
"global_load_lds_dwordx4 v217, s[38:39] offset:-3072\n" \
"v_mfma_f32_16x16x32_bf16 v[88:91], a[48:51], a[8:11], v[88:91]\n" \
"ds_read_b64_tr_b16 a[40:41], v215 offset:16384\n" \
"v_mfma_f32_16x16x32_bf16 v[88:91], a[52:55], a[12:15], v[88:91]\n" \
"buffer_store_dwordx4 v[24:27], v248, s[56:59], s71 offen offset:2048 sc1\n" \
"ds_read_b128 a[8:11], v213 offset:256\n" \
"v_cvt_pk_bf16_f32 v28, v56, v57\n" \
"v_cvt_pk_bf16_f32 v29, v58, v59\n" \
"v_cvt_pk_bf16_f32 v30, v60, v61\n" \
"v_mfma_f32_16x16x32_bf16 v[104:107], a[48:51], a[16:19], v[104:107]\n" \
"ds_read_b128 a[12:15], v213 offset:2304\n" \
"v_cvt_pk_bf16_f32 v31, v62, v63\n" \
"v_mfma_f32_16x16x32_bf16 v[104:107], a[52:55], a[20:23], v[104:107]\n" \
"ds_read_b64_tr_b16 a[42:43], v215 offset:18432\n" \
"global_load_lds_dwordx4 v218, s[38:39] offset:-2048\n" \
"v_mfma_f32_16x16x32_bf16 v[108:111], a[56:59], a[16:19], v[108:111]\n" \
"ds_read_b64_tr_b16 a[44:45], v215 offset:24576\n" \
"v_mfma_f32_16x16x32_bf16 v[108:111], a[60:63], a[20:23], v[108:111]\n" \
"buffer_store_dwordx4 v[28:31], v248, s[56:59], s71 offen offset:3072 sc1\n" \
"ds_read_b128 a[16:19], v213 offset:4096\n" \
"v_mfma_f32_16x16x32_bf16 v[124:127], a[56:59], a[24:27], v[124:127]\n" \
"ds_read_b128 a[20:23], v213 offset:6144\n" \
"global_load_lds_dwordx4 v219, s[38:39] offset:-1024\n" \
"v_mfma_f32_16x16x32_bf16 v[124:127], a[60:63], a[28:31], v[124:127]\n" \
"s_add_u32 s38, s38, 0x20000\n" \
"s_addc_u32 s39, s39, 0\n" \
"ds_read_b64_tr_b16 a[46:47], v215 offset:26624\n" \
"v_mfma_f32_16x16x32_bf16 v[120:123], a[48:51], a[24:27], v[120:123]\n" \
"s_sub_u32 s75, s75, 32768\n" \
"s_cselect_b32 s75, 131072, s75\n" \
"v_mfma_f32_16x16x32_bf16 v[120:123], a[52:55], a[28:31], v[120:123]\n" \
"s_waitcnt lgkmcnt(3)\n" \
"v_mfma_f32_16x16x32_bf16 v[64:67], a[32:35], a[0:3], v[64:67]\n" \
"ds_read_b128 a[24:27], v213 offset:4352\n" \
"s_add_u32 m0, s75, s95\n" \
"v_mfma_f32_16x16x32_bf16 v[64:67], a[36:39], a[4:7], v[64:67]\n" \
"s_waitcnt lgkmcnt(1)\n" \
"ds_read_b128 a[28:31], v213 offset:6400\n" \
"global_load_lds_dwordx4 v222, s[36:37] offset:0 sc1\n" \
"v_mfma_f32_16x16x32_bf16 v[68:71], a[40:43], a[0:3], v[68:71]\n" \
"s_sub_u32 s74, s74, 32768\n" \
"s_cselect_b32 s74, 131072, s74\n" \
"ds_read_b64_tr_b16 a[48:49], v214 offset:16896\n" \
"v_mfma_f32_16x16x32_bf16 v[68:71], a[44:47], a[4:7], v[68:71]\n" \
"ds_read_b64_tr_b16 a[50:51], v214 offset:18944\n" \
"v_mfma_f32_16x16x32_bf16 v[84:87], a[40:43], a[8:11], v[84:87]\n" \
"ds_read_b64_tr_b16 a[52:53], v214 offset:25088\n" \
"v_mfma_f32_16x16x32_bf16 v[84:87], a[44:47], a[12:15], v[84:87]\n" \
"ds_read_b64_tr_b16 a[54:55], v214 offset:27136\n" \
"global_load_lds_dwordx4 v222, s[36:37] offset:1024 sc1\n" \
"v_mfma_f32_16x16x32_bf16 v[80:83], a[32:35], a[8:11], v[80:83]\n" \
"ds_read_b64_tr_b16 a[56:57], v215 offset:16896\n" \
"v_mfma_f32_16x16x32_bf16 v[80:83], a[36:39], a[12:15], v[80:83]\n" \
"ds_read_b64_tr_b16 a[58:59], v215 offset:18944\n" \
"v_mfma_f32_16x16x32_bf16 v[96:99], a[32:35], a[16:19], v[96:99]\n" \
"ds_read_b64_tr_b16 a[60:61], v215 offset:25088\n" \
"v_mfma_f32_16x16x32_bf16 v[96:99], a[36:39], a[20:23], v[96:99]\n" \
"ds_read_b64_tr_b16 a[62:63], v215 offset:27136\n" \
"global_load_lds_dwordx4 v222, s[36:37] offset:2048 sc1\n" \
"v_mfma_f32_16x16x32_bf16 v[100:103], a[40:43], a[16:19], v[100:103]\n" \
"v_add_u32 v213, s74, v211\n" \
"v_add_u32 v214, s74, v212\n" \
"v_mfma_f32_16x16x32_bf16 v[100:103], a[44:47], a[20:23], v[100:103]\n" \
"s_waitcnt lgkmcnt(3)\n" \
"v_xor_b32 v215, 32, v214\n" \
"v_mfma_f32_16x16x32_bf16 v[116:119], a[40:43], a[24:27], v[116:119]\n" \
"v_mfma_f32_16x16x32_bf16 v[116:119], a[44:47], a[28:31], v[116:119]\n" \
"s_waitcnt vmcnt(25) lgkmcnt(0)\n" \
"global_load_lds_dwordx4 v222, s[36:37] offset:3072 sc1\n" \
"v_mfma_f32_16x16x32_bf16 v[112:115], a[32:35], a[24:27], v[112:115]\n" \
"s_add_u32 s36, s36, 0x1000\n" \
"v_mfma_f32_16x16x32_bf16 v[112:115], a[36:39], a[28:31], v[112:115]\n" \
"s_barrier\n" \
"v_mfma_f32_16x16x32_bf16 v[72:75], a[48:51], a[0:3], v[72:75]\n" \
"s_add_u32 m0, m0, 0x5000\n" \
"ds_read_b64_tr_b16 a[32:33], v214 offset:16384\n" \
"v_mfma_f32_16x16x32_bf16 v[72:75], a[52:55], a[4:7], v[72:75]\n" \
"ds_read_b64_tr_b16 a[34:35], v214 offset:18432\n" \
"global_load_lds_dwordx4 v216, s[38:39] offset:-4096\n" \
"v_mfma_f32_16x16x32_bf16 v[76:79], a[56:59], a[0:3], v[76:79]\n" \
"ds_read_b64_tr_b16 a[36:37], v214 offset:24576\n" \
"v_mfma_f32_16x16x32_bf16 v[76:79], a[60:63], a[4:7], v[76:79]\n" \
"ds_read_b128 a[0:3], v213 offset:0\n" \
"v_mfma_f32_16x16x32_bf16 v[92:95], a[56:59], a[8:11], v[92:95]\n" \
"ds_read_b128 a[4:7], v213 offset:2048\n" \
"v_mfma_f32_16x16x32_bf16 v[92:95], a[60:63], a[12:15], v[92:95]\n" \
"ds_read_b64_tr_b16 a[38:39], v214 offset:26624\n" \
"global_load_lds_dwordx4 v217, s[38:39] offset:-3072\n" \
"v_mfma_f32_16x16x32_bf16 v[88:91], a[48:51], a[8:11], v[88:91]\n" \
"ds_read_b64_tr_b16 a[40:41], v215 offset:16384\n" \
"v_mfma_f32_16x16x32_bf16 v[88:91], a[52:55], a[12:15], v[88:91]\n" \
"ds_read_b128 a[8:11], v213 offset:256\n" \
"v_mfma_f32_16x16x32_bf16 v[104:107], a[48:51], a[16:19], v[104:107]\n" \
"ds_read_b128 a[12:15], v213 offset:2304\n" \
"v_mfma_f32_16x16x32_bf16 v[104:107], a[52:55], a[20:23], v[104:107]\n" \
"ds_read_b64_tr_b16 a[42:43], v215 offset:18432\n" \
"global_load_lds_dwordx4 v218, s[38:39] offset:-2048\n" \
"v_mfma_f32_16x16x32_bf16 v[108:111], a[56:59], a[16:19], v[108:111]\n" \
"ds_read_b64_tr_b16 a[44:45], v215 offset:24576\n" \
"v_mfma_f32_16x16x32_bf16 v[108:111], a[60:63], a[20:23], v[108:111]\n" \
"ds_read_b128 a[16:19], v213 offset:4096\n" \
"v_mfma_f32_16x16x32_bf16 v[124:127], a[56:59], a[24:27], v[124:127]\n" \
"ds_read_b128 a[20:23], v213 offset:6144\n" \
"global_load_lds_dwordx4 v219, s[38:39] offset:-1024\n" \
"v_mfma_f32_16x16x32_bf16 v[124:127], a[60:63], a[28:31], v[124:127]\n" \
"s_add_u32 s38, s38, 0x20000\n" \
"s_addc_u32 s39, s39, 0\n" \
"ds_read_b64_tr_b16 a[46:47], v215 offset:26624\n" \
"v_mfma_f32_16x16x32_bf16 v[120:123], a[48:51], a[24:27], v[120:123]\n" \
"s_sub_u32 s75, s75, 32768\n" \
"s_cselect_b32 s75, 131072, s75\n" \
"v_mfma_f32_16x16x32_bf16 v[120:123], a[52:55], a[28:31], v[120:123]\n" \
"s_waitcnt lgkmcnt(3)\n" \
"v_mfma_f32_16x16x32_bf16 v[64:67], a[32:35], a[0:3], v[64:67]\n" \
"ds_read_b128 a[24:27], v213 offset:4352\n" \
"s_add_u32 m0, s75, s95\n" \
"v_mfma_f32_16x16x32_bf16 v[64:67], a[36:39], a[4:7], v[64:67]\n" \
"s_waitcnt lgkmcnt(1)\n" \
"ds_read_b128 a[28:31], v213 offset:6400\n" \
"global_load_lds_dwordx4 v222, s[36:37] offset:0 sc1\n" \
"v_mfma_f32_16x16x32_bf16 v[68:71], a[40:43], a[0:3], v[68:71]\n" \
"s_sub_u32 s74, s74, 32768\n" \
"s_cselect_b32 s74, 131072, s74\n" \
"ds_read_b64_tr_b16 a[48:49], v214 offset:16896\n" \
"v_mfma_f32_16x16x32_bf16 v[68:71], a[44:47], a[4:7], v[68:71]\n" \
"ds_read_b64_tr_b16 a[50:51], v214 offset:18944\n" \
"v_mfma_f32_16x16x32_bf16 v[84:87], a[40:43], a[8:11], v[84:87]\n" \
"ds_read_b64_tr_b16 a[52:53], v214 offset:25088\n" \
"v_mfma_f32_16x16x32_bf16 v[84:87], a[44:47], a[12:15], v[84:87]\n" \
"ds_read_b64_tr_b16 a[54:55], v214 offset:27136\n" \
"global_load_lds_dwordx4 v222, s[36:37] offset:1024 sc1\n" \
"v_mfma_f32_16x16x32_bf16 v[80:83], a[32:35], a[8:11], v[80:83]\n" \
"ds_read_b64_tr_b16 a[56:57], v215 offset:16896\n" \
"v_mfma_f32_16x16x32_bf16 v[80:83], a[36:39], a[12:15], v[80:83]\n" \
"ds_read_b64_tr_b16 a[58:59], v215 offset:18944\n" \
"v_mfma_f32_16x16x32_bf16 v[96:99], a[32:35], a[16:19], v[96:99]\n" \
"ds_read_b64_tr_b16 a[60:61], v215 offset:25088\n" \
"v_mfma_f32_16x16x32_bf16 v[96:99], a[36:39], a[20:23], v[96:99]\n" \
"ds_read_b64_tr_b16 a[62:63], v215 offset:27136\n" \
"global_load_lds_dwordx4 v222, s[36:37] offset:2048 sc1\n" \
"v_mfma_f32_16x16x32_bf16 v[100:103], a[40:43], a[16:19], v[100:103]\n" \
"v_add_u32 v213, s74, v211\n" \
"v_add_u32 v214, s74, v212\n" \
"v_mfma_f32_16x16x32_bf16 v[100:103], a[44:47], a[20:23], v[100:103]\n" \
"s_waitcnt lgkmcnt(3)\n" \
"v_xor_b32 v215, 32, v214\n" \
"v_mfma_f32_16x16x32_bf16 v[116:119], a[40:43], a[24:27], v[116:119]\n" \
"v_mfma_f32_16x16x32_bf16 v[116:119], a[44:47], a[28:31], v[116:119]\n" \
"s_waitcnt vmcnt(22) lgkmcnt(0)\n" \
"global_load_lds_dwordx4 v222, s[36:37] offset:3072 sc1\n" \
"v_mfma_f32_16x16x32_bf16 v[112:115], a[32:35], a[24:27], v[112:115]\n" \
"s_add_u32 s36, s36, 0x1000\n" \
"v_mfma_f32_16x16x32_bf16 v[112:115], a[36:39], a[28:31], v[112:115]\n" \
"s_barrier\n" \
"v_mfma_f32_16x16x32_bf16 v[72:75], a[48:51], a[0:3], v[72:75]\n" \
"s_add_u32 m0, m0, 0x5000\n" \
"ds_read_b64_tr_b16 a[32:33], v214 offset:16384\n" \
"v_mfma_f32_16x16x32_bf16 v[72:75], a[52:55], a[4:7], v[72:75]\n" \
"ds_read_b64_tr_b16 a[34:35], v214 offset:18432\n" \
"global_load_lds_dwordx4 v216, s[38:39] offset:-4096\n" \
"v_mfma_f32_16x16x32_bf16 v[76:79], a[56:59], a[0:3], v[76:79]\n" \
"ds_read_b64_tr_b16 a[36:37], v214 offset:24576\n" \
"v_mfma_f32_16x16x32_bf16 v[76:79], a[60:63], a[4:7], v[76:79]\n" \
"ds_read_b128 a[0:3], v213 offset:0\n" \
"v_mfma_f32_16x16x32_bf16 v[92:95], a[56:59], a[8:11], v[92:95]\n" \
"ds_read_b128 a[4:7], v213 offset:2048\n" \
"v_mfma_f32_16x16x32_bf16 v[92:95], a[60:63], a[12:15], v[92:95]\n" \
"ds_read_b64_tr_b16 a[38:39], v214 offset:26624\n" \
"global_load_lds_dwordx4 v217, s[38:39] offset:-3072\n" \
"v_mfma_f32_16x16x32_bf16 v[88:91], a[48:51], a[8:11], v[88:91]\n" \
"ds_read_b64_tr_b16 a[40:41], v215 offset:16384\n" \
"v_mfma_f32_16x16x32_bf16 v[88:91], a[52:55], a[12:15], v[88:91]\n" \
"ds_read_b128 a[8:11], v213 offset:256\n" \
"v_mfma_f32_16x16x32_bf16 v[104:107], a[48:51], a[16:19], v[104:107]\n" \
"ds_read_b128 a[12:15], v213 offset:2304\n" \
"v_mfma_f32_16x16x32_bf16 v[104:107], a[52:55], a[20:23], v[104:107]\n" \
"ds_read_b64_tr_b16 a[42:43], v215 offset:18432\n" \
"global_load_lds_dwordx4 v218, s[38:39] offset:-2048\n" \
"v_mfma_f32_16x16x32_bf16 v[108:111], a[56:59], a[16:19], v[108:111]\n" \
"ds_read_b64_tr_b16 a[44:45], v215 offset:24576\n" \
"v_mfma_f32_16x16x32_bf16 v[108:111], a[60:63], a[20:23], v[108:111]\n" \
"ds_read_b128 a[16:19], v213 offset:4096\n" \
"v_mfma_f32_16x16x32_bf16 v[124:127], a[56:59], a[24:27], v[124:127]\n" \
"ds_read_b128 a[20:23], v213 offset:6144\n" \
"global_load_lds_dwordx4 v219, s[38:39] offset:-1024\n" \
"v_mfma_f32_16x16x32_bf16 v[124:127], a[60:63], a[28:31], v[124:127]\n" \
"s_add_u32 s38, s38, 0x20000\n" \
"s_addc_u32 s39, s39, 0\n" \
"ds_read_b64_tr_b16 a[46:47], v215 offset:26624\n" \
"v_mfma_f32_16x16x32_bf16 v[120:123], a[48:51], a[24:27], v[120:123]\n" \
"s_sub_u32 s75, s75, 32768\n" \
"s_cselect_b32 s75, 131072, s75\n" \
"v_mfma_f32_16x16x32_bf16 v[120:123], a[52:55], a[28:31], v[120:123]\n" \
"s_waitcnt lgkmcnt(3)\n" \
"v_mfma_f32_16x16x32_bf16 v[64:67], a[32:35], a[0:3], v[64:67]\n" \
"ds_read_b128 a[24:27], v213 offset:4352\n" \
"s_add_u32 m0, s75, s95\n" \
"v_mfma_f32_16x16x32_bf16 v[64:67], a[36:39], a[4:7], v[64:67]\n" \
"s_waitcnt lgkmcnt(1)\n" \
"ds_read_b128 a[28:31], v213 offset:6400\n" \
"global_load_lds_dwordx4 v222, s[36:37] offset:0 sc1\n" \
"v_mfma_f32_16x16x32_bf16 v[68:71], a[40:43], a[0:3], v[68:71]\n" \
"s_sub_u32 s74, s74, 32768\n" \
"s_cselect_b32 s74, 131072, s74\n" \
"ds_read_b64_tr_b16 a[48:49], v214 offset:16896\n" \
"v_mfma_f32_16x16x32_bf16 v[68:71], a[44:47], a[4:7], v[68:71]\n" \
"ds_read_b64_tr_b16 a[50:51], v214 offset:18944\n" \
"v_mfma_f32_16x16x32_bf16 v[84:87], a[40:43], a[8:11], v[84:87]\n" \
"ds_read_b64_tr_b16 a[52:53], v214 offset:25088\n" \
"v_mfma_f32_16x16x32_bf16 v[84:87], a[44:47], a[12:15], v[84:87]\n" \
"ds_read_b64_tr_b16 a[54:55], v214 offset:27136\n" \
"global_load_lds_dwordx4 v222, s[36:37] offset:1024 sc1\n" \
"v_mfma_f32_16x16x32_bf16 v[80:83], a[32:35], a[8:11], v[80:83]\n" \
"ds_read_b64_tr_b16 a[56:57], v215 offset:16896\n" \
"v_mfma_f32_16x16x32_bf16 v[80:83], a[36:39], a[12:15], v[80:83]\n" \
"ds_read_b64_tr_b16 a[58:59], v215 offset:18944\n" \
"v_mfma_f32_16x16x32_bf16 v[96:99], a[32:35], a[16:19], v[96:99]\n" \
"ds_read_b64_tr_b16 a[60:61], v215 offset:25088\n" \
"v_mfma_f32_16x16x32_bf16 v[96:99], a[36:39], a[20:23], v[96:99]\n" \
"ds_read_b64_tr_b16 a[62:63], v215 offset:27136\n" \
"global_load_lds_dwordx4 v222, s[36:37] offset:2048 sc1\n" \
"v_mfma_f32_16x16x32_bf16 v[100:103], a[40:43], a[16:19], v[100:103]\n" \
"v_add_u32 v213, s74, v211\n" \
"v_add_u32 v214, s74, v212\n" \
"v_mfma_f32_16x16x32_bf16 v[100:103], a[44:47], a[20:23], v[100:103]\n" \
"s_waitcnt lgkmcnt(3)\n" \
"v_xor_b32 v215, 32, v214\n" \
"v_mfma_f32_16x16x32_bf16 v[116:119], a[40:43], a[24:27], v[116:119]\n" \
"v_mfma_f32_16x16x32_bf16 v[116:119], a[44:47], a[28:31], v[116:119]\n" \
"s_waitcnt vmcnt(19) lgkmcnt(0)\n" \
"global_load_lds_dwordx4 v222, s[36:37] offset:3072 sc1\n" \
"v_mfma_f32_16x16x32_bf16 v[112:115], a[32:35], a[24:27], v[112:115]\n" \
"s_add_u32 s36, s36, 0x1000\n" \
"v_mfma_f32_16x16x32_bf16 v[112:115], a[36:39], a[28:31], v[112:115]\n" \
"s_barrier\n" \
"v_mfma_f32_16x16x32_bf16 v[72:75], a[48:51], a[0:3], v[72:75]\n" \
"s_add_u32 m0, m0, 0x5000\n" \
"ds_read_b64_tr_b16 a[32:33], v214 offset:16384\n" \
"v_mfma_f32_16x16x32_bf16 v[72:75], a[52:55], a[4:7], v[72:75]\n" \
"ds_read_b64_tr_b16 a[34:35], v214 offset:18432\n" \
"global_load_lds_dwordx4 v216, s[38:39] offset:-4096\n" \
"v_mfma_f32_16x16x32_bf16 v[76:79], a[56:59], a[0:3], v[76:79]\n" \
"ds_read_b64_tr_b16 a[36:37], v214 offset:24576\n" \
"v_mfma_f32_16x16x32_bf16 v[76:79], a[60:63], a[4:7], v[76:79]\n" \
"buffer_store_dword v224, v225, s[56:59], s79 offen sc1\n" \
"ds_read_b128 a[0:3], v213 offset:0\n" \
"v_mfma_f32_16x16x32_bf16 v[92:95], a[56:59], a[8:11], v[92:95]\n" \
"ds_read_b128 a[4:7], v213 offset:2048\n" \
"v_mfma_f32_16x16x32_bf16 v[92:95], a[60:63], a[12:15], v[92:95]\n" \
"ds_read_b64_tr_b16 a[38:39], v214 offset:26624\n" \
"global_load_lds_dwordx4 v217, s[38:39] offset:-3072\n" \
"v_mfma_f32_16x16x32_bf16 v[88:91], a[48:51], a[8:11], v[88:91]\n" \
"ds_read_b64_tr_b16 a[40:41], v215 offset:16384\n" \
"v_mfma_f32_16x16x32_bf16 v[88:91], a[52:55], a[12:15], v[88:91]\n" \
"ds_read_b128 a[8:11], v213 offset:256\n" \
"v_mfma_f32_16x16x32_bf16 v[104:107], a[48:51], a[16:19], v[104:107]\n" \
"ds_read_b128 a[12:15], v213 offset:2304\n" \
"v_mfma_f32_16x16x32_bf16 v[104:107], a[52:55], a[20:23], v[104:107]\n" \
"ds_read_b64_tr_b16 a[42:43], v215 offset:18432\n" \
"global_load_lds_dwordx4 v218, s[38:39] offset:-2048\n" \
"v_mfma_f32_16x16x32_bf16 v[108:111], a[56:59], a[16:19], v[108:111]\n" \
"ds_read_b64_tr_b16 a[44:45], v215 offset:24576\n" \
"v_mfma_f32_16x16x32_bf16 v[108:111], a[60:63], a[20:23], v[108:111]\n" \
"ds_read_b128 a[16:19], v213 offset:4096\n" \
"v_mfma_f32_16x16x32_bf16 v[124:127], a[56:59], a[24:27], v[124:127]\n" \
"ds_read_b128 a[20:23], v213 offset:6144\n" \
"global_load_lds_dwordx4 v219, s[38:39] offset:-1024\n" \
"v_mfma_f32_16x16x32_bf16 v[124:127], a[60:63], a[28:31], v[124:127]\n" \
"s_add_u32 s38, s38, 0x20000\n" \
"s_addc_u32 s39, s39, 0\n" \
"ds_read_b64_tr_b16 a[46:47], v215 offset:26624\n" \
"v_mfma_f32_16x16x32_bf16 v[120:123], a[48:51], a[24:27], v[120:123]\n" \
"s_sub_u32 s75, s75, 32768\n" \
"s_cselect_b32 s75, 131072, s75\n" \
"v_mfma_f32_16x16x32_bf16 v[120:123], a[52:55], a[28:31], v[120:123]\n" \
"s_waitcnt lgkmcnt(3)\n" \
"s_mov_b32 s72, 6\n" \
"s_call_b64 s[68:69], Lo64_%=\n" \
"v_mfma_f32_16x16x32_bf16 v[64:67], a[32:35], a[0:3], v[64:67]\n" \
"ds_read_b128 a[24:27], v213 offset:4352\n" \
"v_mfma_f32_16x16x32_bf16 v[64:67], a[36:39], a[4:7], v[64:67]\n" \
"s_waitcnt lgkmcnt(1)\n" \
"ds_read_b128 a[28:31], v213 offset:6400\n" \
"v_mfma_f32_16x16x32_bf16 v[68:71], a[40:43], a[0:3], v[68:71]\n" \
"s_sub_u32 s74, s74, 32768\n" \
"s_cselect_b32 s74, 131072, s74\n" \
"ds_read_b64_tr_b16 a[48:49], v214 offset:16896\n" \
"v_mfma_f32_16x16x32_bf16 v[68:71], a[44:47], a[4:7], v[68:71]\n" \
"ds_read_b64_tr_b16 a[50:51], v214 offset:18944\n" \
"v_mfma_f32_16x16x32_bf16 v[84:87], a[40:43], a[8:11], v[84:87]\n" \
"ds_read_b64_tr_b16 a[52:53], v214 offset:25088\n" \
"v_mfma_f32_16x16x32_bf16 v[84:87], a[44:47], a[12:15], v[84:87]\n" \
"ds_read_b64_tr_b16 a[54:55], v214 offset:27136\n" \
"v_mfma_f32_16x16x32_bf16 v[80:83], a[32:35], a[8:11], v[80:83]\n" \
"ds_read_b64_tr_b16 a[56:57], v215 offset:16896\n" \
"v_mfma_f32_16x16x32_bf16 v[80:83], a[36:39], a[12:15], v[80:83]\n" \
"ds_read_b64_tr_b16 a[58:59], v215 offset:18944\n" \
"v_mfma_f32_16x16x32_bf16 v[96:99], a[32:35], a[16:19], v[96:99]\n" \
"ds_read_b64_tr_b16 a[60:61], v215 offset:25088\n" \
"v_mfma_f32_16x16x32_bf16 v[96:99], a[36:39], a[20:23], v[96:99]\n" \
"ds_read_b64_tr_b16 a[62:63], v215 offset:27136\n" \
"v_mfma_f32_16x16x32_bf16 v[100:103], a[40:43], a[16:19], v[100:103]\n" \
"v_add_u32 v213, s74, v211\n" \
"v_add_u32 v214, s74, v212\n" \
"v_mfma_f32_16x16x32_bf16 v[100:103], a[44:47], a[20:23], v[100:103]\n" \
"s_waitcnt lgkmcnt(3)\n" \
"v_xor_b32 v215, 32, v214\n" \
"v_mfma_f32_16x16x32_bf16 v[116:119], a[40:43], a[24:27], v[116:119]\n" \
"v_mfma_f32_16x16x32_bf16 v[116:119], a[44:47], a[28:31], v[116:119]\n" \
"s_waitcnt vmcnt(16) lgkmcnt(0)\n" \
"v_mfma_f32_16x16x32_bf16 v[112:115], a[32:35], a[24:27], v[112:115]\n" \
"v_mfma_f32_16x16x32_bf16 v[112:115], a[36:39], a[28:31], v[112:115]\n" \
"s_barrier\n" \
"v_mfma_f32_16x16x32_bf16 v[72:75], a[48:51], a[0:3], v[72:75]\n" \
"s_lshl_b32 s83, s95, 1\n" \
"s_add_u32 s83, s83, s74\n" \
"ds_read_b64_tr_b16 a[32:33], v214 offset:16384\n" \
"v_mfma_f32_16x16x32_bf16 v[72:75], a[52:55], a[4:7], v[72:75]\n" \
"ds_read_b64_tr_b16 a[34:35], v214 offset:18432\n" \
"v_mfma_f32_16x16x32_bf16 v[76:79], a[56:59], a[0:3], v[76:79]\n" \
"ds_read_b64_tr_b16 a[36:37], v214 offset:24576\n" \
"v_mfma_f32_16x16x32_bf16 v[76:79], a[60:63], a[4:7], v[76:79]\n" \
"ds_read_b128 a[0:3], v213 offset:0\n" \
"v_mfma_f32_16x16x32_bf16 v[92:95], a[56:59], a[8:11], v[92:95]\n" \
"ds_read_b128 a[4:7], v213 offset:2048\n" \
"v_mfma_f32_16x16x32_bf16 v[92:95], a[60:63], a[12:15], v[92:95]\n" \
"ds_read_b64_tr_b16 a[38:39], v214 offset:26624\n" \
"v_mfma_f32_16x16x32_bf16 v[88:91], a[48:51], a[8:11], v[88:91]\n" \
"ds_read_b64_tr_b16 a[40:41], v215 offset:16384\n" \
"v_mfma_f32_16x16x32_bf16 v[88:91], a[52:55], a[12:15], v[88:91]\n" \
"ds_read_b128 a[8:11], v213 offset:256\n" \
"v_mfma_f32_16x16x32_bf16 v[104:107], a[48:51], a[16:19], v[104:107]\n" \
"ds_read_b128 a[12:15], v213 offset:2304\n" \
"v_mfma_f32_16x16x32_bf16 v[104:107], a[52:55], a[20:23], v[104:107]\n" \
"ds_read_b64_tr_b16 a[42:43], v215 offset:18432\n" \
"v_mfma_f32_16x16x32_bf16 v[108:111], a[56:59], a[16:19], v[108:111]\n" \
"ds_read_b64_tr_b16 a[44:45], v215 offset:24576\n" \
"v_mfma_f32_16x16x32_bf16 v[108:111], a[60:63], a[20:23], v[108:111]\n" \
"ds_read_b128 a[16:19], v213 offset:4096\n" \
"v_mfma_f32_16x16x32_bf16 v[124:127], a[56:59], a[24:27], v[124:127]\n" \
"ds_read_b128 a[20:23], v213 offset:6144\n" \
"v_mfma_f32_16x16x32_bf16 v[124:127], a[60:63], a[28:31], v[124:127]\n" \
"ds_read_b64_tr_b16 a[46:47], v215 offset:26624\n" \
"v_mfma_f32_16x16x32_bf16 v[120:123], a[48:51], a[24:27], v[120:123]\n" \
"v_mfma_f32_16x16x32_bf16 v[120:123], a[52:55], a[28:31], v[120:123]\n" \
"s_waitcnt lgkmcnt(3)\n" \
"v_mfma_f32_16x16x32_bf16 v[64:67], a[32:35], a[0:3], v[64:67]\n" \
"ds_read_b128 a[24:27], v213 offset:4352\n" \
"v_mfma_f32_16x16x32_bf16 v[64:67], a[36:39], a[4:7], v[64:67]\n" \
"s_waitcnt lgkmcnt(1)\n" \
"ds_read_b128 a[28:31], v213 offset:6400\n" \
"v_lshrrev_b32 v132, 4, v248\n" \
"v_mfma_f32_16x16x32_bf16 v[68:71], a[40:43], a[0:3], v[68:71]\n" \
"s_sub_u32 s74, s74, 32768\n" \
"s_cselect_b32 s74, 131072, s74\n" \
"ds_read_b64_tr_b16 a[48:49], v214 offset:16896\n" \
"v_mfma_f32_16x16x32_bf16 v[68:71], a[44:47], a[4:7], v[68:71]\n" \
"ds_read_b64_tr_b16 a[50:51], v214 offset:18944\n" \
"v_and_b32 v133, 15, v132\n" \
"v_mfma_f32_16x16x32_bf16 v[84:87], a[40:43], a[8:11], v[84:87]\n" \
"ds_read_b64_tr_b16 a[52:53], v214 offset:25088\n" \
"v_mul_u32_u24 v128, 144, v133\n" \
"v_mfma_f32_16x16x32_bf16 v[84:87], a[44:47], a[12:15], v[84:87]\n" \
"ds_read_b64_tr_b16 a[54:55], v214 offset:27136\n" \
"v_lshrrev_b32 v133, 4, v132\n" \
"v_mfma_f32_16x16x32_bf16 v[80:83], a[32:35], a[8:11], v[80:83]\n" \
"ds_read_b64_tr_b16 a[56:57], v215 offset:16896\n" \
"v_lshl_add_u32 v128, v133, 3, v128\n" \
"v_mfma_f32_16x16x32_bf16 v[80:83], a[36:39], a[12:15], v[80:83]\n" \
"ds_read_b64_tr_b16 a[58:59], v215 offset:18944\n" \
"v_add_u32 v128, s83, v128\n" \
"v_mfma_f32_16x16x32_bf16 v[96:99], a[32:35], a[16:19], v[96:99]\n" \
"ds_read_b64_tr_b16 a[60:61], v215 offset:25088\n" \
"v_lshrrev_b32 v133, 3, v132\n" \
"v_mfma_f32_16x16x32_bf16 v[96:99], a[36:39], a[20:23], v[96:99]\n" \
"ds_read_b64_tr_b16 a[62:63], v215 offset:27136\n" \
"v_and_b32 v134, 7, v132\n" \
"v_mfma_f32_16x16x32_bf16 v[100:103], a[40:43], a[16:19], v[100:103]\n" \
"v_add_u32 v213, s74, v211\n" \
"v_add_u32 v214, s74, v212\n" \
"v_mfma_f32_16x16x32_bf16 v[100:103], a[44:47], a[20:23], v[100:103]\n" \
"s_waitcnt lgkmcnt(3)\n" \
"v_xor_b32 v215, 32, v214\n" \
"v_mul_u32_u24 v129, 144, v133\n" \
"v_mfma_f32_16x16x32_bf16 v[116:119], a[40:43], a[24:27], v[116:119]\n" \
"v_lshl_add_u32 v129, v134, 4, v129\n" \
"v_add_u32 v129, s83, v129\n" \
"v_mfma_f32_16x16x32_bf16 v[116:119], a[44:47], a[28:31], v[116:119]\n" \
"s_waitcnt vmcnt(0) lgkmcnt(0)\n" \
"v_lshlrev_b32 v134, 4, v134\n" \
"v_lshl_add_u32 v130, v133, 13, v134\n" \
"v_mfma_f32_16x16x32_bf16 v[112:115], a[32:35], a[24:27], v[112:115]\n" \
"v_mfma_f32_16x16x32_bf16 v[112:115], a[36:39], a[28:31], v[112:115]\n" \
"s_barrier\n" \
"v_mfma_f32_16x16x32_bf16 v[72:75], a[48:51], a[0:3], v[72:75]\n" \
"ds_read_b128 a[192:195], v213 offset:0\n" \
"ds_read_b64_tr_b16 a[128:129], v214 offset:16384\n" \
"v_mfma_f32_16x16x32_bf16 v[72:75], a[52:55], a[4:7], v[72:75]\n" \
"s_sub_u32 s74, s74, 32768\n" \
"Lpg13128_%=:\n" \
"s_cbranch_execz Lcb1_%=\n" \
"s_cselect_b32 s74, 131072, s74\n" \
"ds_read_b64_tr_b16 a[130:131], v214 offset:18432\n" \
"v_mfma_f32_16x16x32_bf16 v[76:79], a[56:59], a[0:3], v[76:79]\n" \
"ds_read_b128 a[196:199], v213 offset:2048\n" \
"ds_read_b64_tr_b16 a[132:133], v214 offset:24576\n" \
"v_mfma_f32_16x16x32_bf16 v[76:79], a[60:63], a[4:7], v[76:79]\n" \
"ds_read_b64_tr_b16 a[134:135], v214 offset:26624\n" \
"v_add_u32 v226, s74, v211\n" \
"v_mfma_f32_16x16x32_bf16 v[92:95], a[56:59], a[8:11], v[92:95]\n" \
"ds_read_b128 a[200:203], v213 offset:256\n" \
"ds_read_b64_tr_b16 a[136:137], v215 offset:16384\n" \
"v_mfma_f32_16x16x32_bf16 v[92:95], a[60:63], a[12:15], v[92:95]\n" \
"ds_read_b64_tr_b16 a[138:139], v215 offset:18432\n" \
"v_add_u32 v227, s74, v212\n" \
"v_mfma_f32_16x16x32_bf16 v[88:91], a[48:51], a[8:11], v[88:91]\n" \
"ds_read_b128 a[204:207], v213 offset:2304\n" \
"ds_read_b64_tr_b16 a[140:141], v215 offset:24576\n" \
"v_mfma_f32_16x16x32_bf16 v[88:91], a[52:55], a[12:15], v[88:91]\n" \
"ds_read_b64_tr_b16 a[142:143], v215 offset:26624\n" \
"v_xor_b32 v228, 32, v227\n" \
"v_mfma_f32_16x16x32_bf16 v[104:107], a[48:51], a[16:19], v[104:107]\n" \
"ds_read_b64_tr_b16 a[144:145], v214 offset:16896\n" \
"v_mfma_f32_16x16x32_bf16 v[104:107], a[52:55], a[20:23], v[104:107]\n" \
"ds_read_b64_tr_b16 a[146:147], v214 offset:18944\n" \
"v_mfma_f32_16x16x32_bf16 v[108:111], a[56:59], a[16:19], v[108:111]\n" \
"ds_read_b64_tr_b16 a[148:149], v214 offset:25088\n" \
"v_mfma_f32_16x16x32_bf16 v[108:111], a[60:63], a[20:23], v[108:111]\n" \
"ds_read_b64_tr_b16 a[150:151], v214 offset:27136\n" \
"v_mfma_f32_16x16x32_bf16 v[124:127], a[56:59], a[24:27], v[124:127]\n" \
"ds_read_b64_tr_b16 a[152:153], v215 offset:16896\n" \
"v_mfma_f32_16x16x32_bf16 v[124:127], a[60:63], a[28:31], v[124:127]\n" \
"ds_read_b64_tr_b16 a[154:155], v215 offset:18944\n" \
"v_mfma_f32_16x16x32_bf16 v[120:123], a[48:51], a[24:27], v[120:123]\n" \
"ds_read_b64_tr_b16 a[156:157], v215 offset:25088\n" \
"v_mfma_f32_16x16x32_bf16 v[120:123], a[52:55], a[28:31], v[120:123]\n" \
"s_waitcnt lgkmcnt(4)\n" \
"ds_read_b64_tr_b16 a[158:159], v215 offset:27136\n" \
"v_mfma_f32_16x16x32_bf16 v[64:67], a[128:131], a[192:195], v[64:67]\n" \
"ds_read_b128 a[224:227], v226 offset:0\n" \
"ds_read_b64_tr_b16 a[160:161], v227 offset:16384\n" \
"v_mfma_f32_16x16x32_bf16 v[64:67], a[132:135], a[196:199], v[64:67]\n" \
"ds_read_b64_tr_b16 a[162:163], v227 offset:18432\n" \
"v_mfma_f32_16x16x32_bf16 v[80:83], a[128:131], a[200:203], v[80:83]\n" \
"ds_read_b64_tr_b16 a[164:165], v227 offset:24576\n" \
"v_mfma_f32_16x16x32_bf16 v[80:83], a[132:135], a[204:207], v[80:83]\n" \
"ds_read_b128 a[228:231], v226 offset:2048\n" \
"ds_read_b64_tr_b16 a[166:167], v227 offset:26624\n" \
"v_mfma_f32_16x16x32_bf16 v[84:87], a[136:139], a[200:203], v[84:87]\n" \
"ds_read_b64_tr_b16 a[168:169], v228 offset:16384\n" \
"v_mfma_f32_16x16x32_bf16 v[84:87], a[140:143], a[204:207], v[84:87]\n" \
"ds_read_b64_tr_b16 a[170:171], v228 offset:18432\n" \
"v_mfma_f32_16x16x32_bf16 v[68:71], a[136:139], a[192:195], v[68:71]\n" \
"ds_read_b128 a[232:235], v226 offset:256\n" \
"ds_read_b64_tr_b16 a[172:173], v228 offset:24576\n" \
"v_mfma_f32_16x16x32_bf16 v[68:71], a[140:143], a[196:199], v[68:71]\n" \
"s_waitcnt lgkmcnt(6)\n" \
"ds_read_b64_tr_b16 a[174:175], v228 offset:26624\n" \
"v_mfma_f32_16x16x32_bf16 v[72:75], a[144:147], a[192:195], v[72:75]\n" \
"ds_read_b64_tr_b16 a[176:177], v227 offset:16896\n" \
"v_mfma_f32_16x16x32_bf16 v[72:75], a[148:151], a[196:199], v[72:75]\n" \
"ds_read_b128 a[236:239], v226 offset:2304\n" \
"ds_read_b64_tr_b16 a[178:179], v227 offset:18944\n" \
"v_mfma_f32_16x16x32_bf16 v[88:91], a[144:147], a[200:203], v[88:91]\n" \
"ds_read_b64_tr_b16 a[180:181], v227 offset:25088\n" \
"v_mfma_f32_16x16x32_bf16 v[88:91], a[148:151], a[204:207], v[88:91]\n" \
"ds_read_b64_tr_b16 a[182:183], v227 offset:27136\n" \
"v_mfma_f32_16x16x32_bf16 v[92:95], a[152:155], a[200:203], v[92:95]\n" \
"ds_read_b64_tr_b16 a[184:185], v228 offset:16896\n" \
"v_mfma_f32_16x16x32_bf16 v[92:95], a[156:159], a[204:207], v[92:95]\n" \
"ds_read_b64_tr_b16 a[186:187], v228 offset:18944\n" \
"v_mfma_f32_16x16x32_bf16 v[76:79], a[152:155], a[192:195], v[76:79]\n" \
"ds_read_b64_tr_b16 a[188:189], v228 offset:25088\n" \
"v_mfma_f32_16x16x32_bf16 v[76:79], a[156:159], a[196:199], v[76:79]\n" \
"s_waitcnt lgkmcnt(4)\n" \
"ds_read_b64_tr_b16 a[190:191], v228 offset:27136\n" \
"v_mfma_f32_16x16x32_bf16 v[64:67], a[160:163], a[224:227], v[64:67]\n" \
"ds_read_b128 a[208:211], v213 offset:4096\n" \
"v_mfma_f32_16x16x32_bf16 v[64:67], a[164:167], a[228:231], v[64:67]\n" \
"ds_read_b128 a[212:215], v213 offset:6144\n" \
"v_mfma_f32_16x16x32_bf16 v[80:83], a[160:163], a[232:235], v[80:83]\n" \
"ds_read_b128 a[240:243], v226 offset:4096\n" \
"v_mfma_f32_16x16x32_bf16 v[80:83], a[164:167], a[236:239], v[80:83]\n" \
"ds_read_b128 a[244:247], v226 offset:6144\n" \
"v_mfma_f32_16x16x32_bf16 v[84:87], a[168:171], a[232:235], v[84:87]\n" \
"ds_read_b128 a[216:219], v213 offset:4352\n" \
"v_cvt_pk_bf16_f32 v64, v64, v65\n" \
"v_mfma_f32_16x16x32_bf16 v[84:87], a[172:175], a[236:239], v[84:87]\n" \
"ds_read_b128 a[220:223], v213 offset:6400\n" \
"v_cvt_pk_bf16_f32 v65, v66, v67\n" \
"v_mfma_f32_16x16x32_bf16 v[68:71], a[168:171], a[224:227], v[68:71]\n" \
"ds_read_b128 a[248:251], v226 offset:4352\n" \
"v_mfma_f32_16x16x32_bf16 v[68:71], a[172:175], a[228:231], v[68:71]\n" \
"s_waitcnt lgkmcnt(4)\n" \
"ds_read_b128 a[252:255], v226 offset:6400\n" \
"v_mfma_f32_16x16x32_bf16 v[72:75], a[176:179], a[224:227], v[72:75]\n" \
"ds_write_b64 v128, v[64:65] offset:0\n" \
"v_mfma_f32_16x16x32_bf16 v[72:75], a[180:183], a[228:231], v[72:75]\n" \
"v_cvt_pk_bf16_f32 v80, v80, v81\n" \
"v_cvt_pk_bf16_f32 v81, v82, v83\n" \
"v_mfma_f32_16x16x32_bf16 v[88:91], a[176:179], a[232:235], v[88:91]\n" \
"ds_write_b64 v128, v[80:81] offset:2304\n" \
"v_mfma_f32_16x16x32_bf16 v[88:91], a[180:183], a[236:239], v[88:91]\n" \
"v_cvt_pk_bf16_f32 v84, v84, v85\n" \
"v_cvt_pk_bf16_f32 v85, v86, v87\n" \
"v_mfma_f32_16x16x32_bf16 v[92:95], a[184:187], a[232:235], v[92:95]\n" \
"ds_write_b64 v128, v[84:85] offset:2336\n" \
"v_mfma_f32_16x16x32_bf16 v[92:95], a[188:191], a[236:239], v[92:95]\n" \
"v_cvt_pk_bf16_f32 v68, v68, v69\n" \
"v_cvt_pk_bf16_f32 v69, v70, v71\n" \
"v_mfma_f32_16x16x32_bf16 v[76:79], a[184:187], a[224:227], v[76:79]\n" \
"ds_write_b64 v128, v[68:69] offset:32\n" \
"v_mfma_f32_16x16x32_bf16 v[76:79], a[188:191], a[228:231], v[76:79]\n" \
"v_cvt_pk_bf16_f32 v72, v72, v73\n" \
"v_cvt_pk_bf16_f32 v73, v74, v75\n" \
"v_mfma_f32_16x16x32_bf16 v[96:99], a[128:131], a[208:211], v[96:99]\n" \
"ds_write_b64 v128, v[72:73] offset:64\n" \
"v_mfma_f32_16x16x32_bf16 v[96:99], a[132:135], a[212:215], v[96:99]\n" \
"s_waitcnt lgkmcnt(2)\n" \
"v_cvt_pk_bf16_f32 v88, v88, v89\n" \
"v_cvt_pk_bf16_f32 v89, v90, v91\n" \
"v_mfma_f32_16x16x32_bf16 v[96:99], a[160:163], a[240:243], v[96:99]\n" \
"ds_write_b64 v128, v[88:89] offset:2368\n" \
"v_mfma_f32_16x16x32_bf16 v[96:99], a[164:167], a[244:247], v[96:99]\n" \
"v_cvt_pk_bf16_f32 v92, v92, v93\n" \
"v_cvt_pk_bf16_f32 v93, v94, v95\n" \
"v_mfma_f32_16x16x32_bf16 v[100:103], a[168:171], a[240:243], v[100:103]\n" \
"ds_write_b64 v128, v[92:93] offset:2400\n" \
"v_mfma_f32_16x16x32_bf16 v[100:103], a[172:175], a[244:247], v[100:103]\n" \
"v_cvt_pk_bf16_f32 v76, v76, v77\n" \
"v_cvt_pk_bf16_f32 v77, v78, v79\n" \
"v_mfma_f32_16x16x32_bf16 v[100:103], a[136:139], a[208:211], v[100:103]\n" \
"ds_write_b64 v128, v[76:77] offset:96\n" \
"v_mfma_f32_16x16x32_bf16 v[100:103], a[140:143], a[212:215], v[100:103]\n" \
"ds_read_b128 v[140:143], v129 offset:0\n" \
"v_mfma_f32_16x16x32_bf16 v[104:107], a[144:147], a[208:211], v[104:107]\n" \
"ds_read_b128 v[144:147], v129 offset:1152\n" \
"v_mfma_f32_16x16x32_bf16 v[104:107], a[148:151], a[212:215], v[104:107]\n" \
"ds_read_b128 v[148:151], v129 offset:2304\n" \
"v_mfma_f32_16x16x32_bf16 v[104:107], a[176:179], a[240:243], v[104:107]\n" \
"ds_read_b128 v[152:155], v129 offset:3456\n" \
"v_mfma_f32_16x16x32_bf16 v[104:107], a[180:183], a[244:247], v[104:107]\n" \
"v_cvt_pk_bf16_f32 v96, v96, v97\n" \
"v_cvt_pk_bf16_f32 v97, v98, v99\n" \
"v_mfma_f32_16x16x32_bf16 v[108:111], a[184:187], a[240:243], v[108:111]\n" \
"ds_write_b64 v128, v[96:97] offset:0\n" \
"v_mfma_f32_16x16x32_bf16 v[108:111], a[188:191], a[244:247], v[108:111]\n" \
"v_cvt_pk_bf16_f32 v100, v100, v101\n" \
"v_cvt_pk_bf16_f32 v101, v102, v103\n" \
"v_mfma_f32_16x16x32_bf16 v[108:111], a[152:155], a[208:211], v[108:111]\n" \
"s_add_u32 s70, s77, 0\n" \
"ds_write_b64 v128, v[100:101] offset:32\n" \
"v_mfma_f32_16x16x32_bf16 v[108:111], a[156:159], a[212:215], v[108:111]\n" \
"s_waitcnt lgkmcnt(5)\n" \
"v_cvt_pk_bf16_f32 v104, v104, v105\n" \
"v_cvt_pk_bf16_f32 v105, v106, v107\n" \
"v_mfma_f32_16x16x32_bf16 v[112:115], a[128:131], a[216:219], v[112:115]\n" \
"buffer_store_dwordx4 v[140:143], v130, s[52:55], s70 offen sc1\n" \
"ds_write_b64 v128, v[104:105] offset:64\n" \
"v_mfma_f32_16x16x32_bf16 v[112:115], a[132:135], a[220:223], v[112:115]\n" \
"v_mfma_f32_16x16x32_bf16 v[112:115], a[160:163], a[248:251], v[112:115]\n" \
"v_cvt_pk_bf16_f32 v108, v108, v109\n" \
"v_cvt_pk_bf16_f32 v109, v110, v111\n" \
"v_mfma_f32_16x16x32_bf16 v[112:115], a[164:167], a[252:255], v[112:115]\n" \
"ds_write_b64 v128, v[108:109] offset:96\n" \
"v_mfma_f32_16x16x32_bf16 v[116:119], a[168:171], a[248:251], v[116:119]\n" \
"s_add_u32 s70, s77, 65536\n" \
"ds_read_b128 v[156:159], v129 offset:0\n" \
"s_waitcnt lgkmcnt(7)\n" \
"v_mfma_f32_16x16x32_bf16 v[116:119], a[172:175], a[252:255], v[116:119]\n" \
"buffer_store_dwordx4 v[144:147], v130, s[52:55], s70 offen sc1\n" \
"ds_read_b128 v[160:163], v129 offset:1152\n" \
"v_mfma_f32_16x16x32_bf16 v[116:119], a[136:139], a[216:219], v[116:119]\n" \
"v_cvt_pk_bf16_f32 v112, v112, v113\n" \
"v_cvt_pk_bf16_f32 v113, v114, v115\n" \
"v_mfma_f32_16x16x32_bf16 v[116:119], a[140:143], a[220:223], v[116:119]\n" \
"ds_write_b64 v128, v[112:113] offset:2304\n" \
"v_mfma_f32_16x16x32_bf16 v[120:123], a[144:147], a[216:219], v[120:123]\n" \
"v_mfma_f32_16x16x32_bf16 v[120:123], a[148:151], a[220:223], v[120:123]\n" \
"s_add_u32 s70, s77, 4096\n" \
"s_waitcnt lgkmcnt(8)\n" \
"v_mfma_f32_16x16x32_bf16 v[120:123], a[176:179], a[248:251], v[120:123]\n" \
"buffer_store_dwordx4 v[148:151], v130, s[52:55], s70 offen sc1\n" \
"v_cvt_pk_bf16_f32 v116, v116, v117\n" \
"v_cvt_pk_bf16_f32 v117, v118, v119\n" \
"v_mfma_f32_16x16x32_bf16 v[120:123], a[180:183], a[252:255], v[120:123]\n" \
"ds_write_b64 v128, v[116:117] offset:2336\n" \
"v_mfma_f32_16x16x32_bf16 v[124:127], a[184:187], a[248:251], v[124:127]\n" \
"v_mfma_f32_16x16x32_bf16 v[124:127], a[188:191], a[252:255], v[124:127]\n" \
"v_mfma_f32_16x16x32_bf16 v[124:127], a[152:155], a[216:219], v[124:127]\n" \
"s_add_u32 s70, s77, 69632\n" \
"v_cvt_pk_bf16_f32 v120, v120, v121\n" \
"v_cvt_pk_bf16_f32 v121, v122, v123\n" \
"v_mfma_f32_16x16x32_bf16 v[124:127], a[156:159], a[220:223], v[124:127]\n" \
"s_waitcnt lgkmcnt(8)\n" \
"ds_write_b64 v128, v[120:121] offset:2368\n" \
"buffer_store_dwordx4 v[152:155], v130, s[52:55], s70 offen sc1\n" \
"s_add_u32 s70, s77, 131072\n" \
"s_waitcnt lgkmcnt(4)\n" \
"s_nop 2\n" \
"v_cvt_pk_bf16_f32 v124, v124, v125\n" \
"v_cvt_pk_bf16_f32 v125, v126, v127\n" \
"ds_write_b64 v128, v[124:125] offset:2400\n" \
"ds_read_b128 v[164:167], v129 offset:2304\n" \
"ds_read_b128 v[168:171], v129 offset:3456\n" \
"buffer_store_dwordx4 v[156:159], v130, s[52:55], s70 offen sc1\n" \
"s_add_u32 s70, s77, 196608\n" \
"s_waitcnt lgkmcnt(6)\n" \
"buffer_store_dwordx4 v[160:163], v130, s[52:55], s70 offen sc1\n" \
"s_add_u32 s70, s77, 135168\n" \
"s_waitcnt lgkmcnt(1)\n" \
"buffer_store_dwordx4 v[164:167], v130, s[52:55], s70 offen sc1\n" \
"s_add_u32 s70, s77, 200704\n" \
"s_waitcnt lgkmcnt(0)\n" \
"buffer_store_dwordx4 v[168:171], v130, s[52:55], s70 offen sc1\n" \
"s_endpgm\n" \
"Lpg13344_%=:\n" \
"s_cbranch_execz Lcb1_%=\n" \
"Lo64_%=:\n" \
"v_mfma_f32_16x16x32_bf16 v[64:67], a[32:35], a[0:3], v[64:67]\n" \
"ds_read_b128 a[24:27], v213 offset:4352\n" \
"s_add_u32 m0, s75, s95\n" \
"v_mfma_f32_16x16x32_bf16 v[64:67], a[36:39], a[4:7], v[64:67]\n" \
"s_waitcnt lgkmcnt(1)\n" \
"ds_read_b128 a[28:31], v213 offset:6400\n" \
"global_load_lds_dwordx4 v222, s[36:37] offset:0 sc1\n" \
"v_mfma_f32_16x16x32_bf16 v[68:71], a[40:43], a[0:3], v[68:71]\n" \
"s_sub_u32 s74, s74, 32768\n" \
"s_cselect_b32 s74, 131072, s74\n" \
"ds_read_b64_tr_b16 a[48:49], v214 offset:16896\n" \
"v_mfma_f32_16x16x32_bf16 v[68:71], a[44:47], a[4:7], v[68:71]\n" \
"ds_read_b64_tr_b16 a[50:51], v214 offset:18944\n" \
"v_mfma_f32_16x16x32_bf16 v[84:87], a[40:43], a[8:11], v[84:87]\n" \
"ds_read_b64_tr_b16 a[52:53], v214 offset:25088\n" \
"v_mfma_f32_16x16x32_bf16 v[84:87], a[44:47], a[12:15], v[84:87]\n" \
"ds_read_b64_tr_b16 a[54:55], v214 offset:27136\n" \
"global_load_lds_dwordx4 v222, s[36:37] offset:1024 sc1\n" \
"v_mfma_f32_16x16x32_bf16 v[80:83], a[32:35], a[8:11], v[80:83]\n" \
"ds_read_b64_tr_b16 a[56:57], v215 offset:16896\n" \
"v_mfma_f32_16x16x32_bf16 v[80:83], a[36:39], a[12:15], v[80:83]\n" \
"ds_read_b64_tr_b16 a[58:59], v215 offset:18944\n" \
"v_mfma_f32_16x16x32_bf16 v[96:99], a[32:35], a[16:19], v[96:99]\n" \
"ds_read_b64_tr_b16 a[60:61], v215 offset:25088\n" \
"v_mfma_f32_16x16x32_bf16 v[96:99], a[36:39], a[20:23], v[96:99]\n" \
"ds_read_b64_tr_b16 a[62:63], v215 offset:27136\n" \
"global_load_lds_dwordx4 v222, s[36:37] offset:2048 sc1\n" \
"v_mfma_f32_16x16x32_bf16 v[100:103], a[40:43], a[16:19], v[100:103]\n" \
"v_add_u32 v213, s74, v211\n" \
"v_add_u32 v214, s74, v212\n" \
"v_mfma_f32_16x16x32_bf16 v[100:103], a[44:47], a[20:23], v[100:103]\n" \
"s_waitcnt lgkmcnt(3)\n" \
"v_xor_b32 v215, 32, v214\n" \
"v_mfma_f32_16x16x32_bf16 v[116:119], a[40:43], a[24:27], v[116:119]\n" \
"v_mfma_f32_16x16x32_bf16 v[116:119], a[44:47], a[28:31], v[116:119]\n" \
"s_waitcnt vmcnt(19) lgkmcnt(0)\n" \
"global_load_lds_dwordx4 v222, s[36:37] offset:3072 sc1\n" \
"v_mfma_f32_16x16x32_bf16 v[112:115], a[32:35], a[24:27], v[112:115]\n" \
"s_add_u32 s36, s36, 0x1000\n" \
"v_mfma_f32_16x16x32_bf16 v[112:115], a[36:39], a[28:31], v[112:115]\n" \
"s_barrier\n" \
"v_mfma_f32_16x16x32_bf16 v[72:75], a[48:51], a[0:3], v[72:75]\n" \
"s_add_u32 m0, m0, 0x5000\n" \
"ds_read_b64_tr_b16 a[32:33], v214 offset:16384\n" \
"v_mfma_f32_16x16x32_bf16 v[72:75], a[52:55], a[4:7], v[72:75]\n" \
"ds_read_b64_tr_b16 a[34:35], v214 offset:18432\n" \
"global_load_lds_dwordx4 v216, s[38:39] offset:-4096\n" \
"v_mfma_f32_16x16x32_bf16 v[76:79], a[56:59], a[0:3], v[76:79]\n" \
"ds_read_b64_tr_b16 a[36:37], v214 offset:24576\n" \
"v_mfma_f32_16x16x32_bf16 v[76:79], a[60:63], a[4:7], v[76:79]\n" \
"ds_read_b128 a[0:3], v213 offset:0\n" \
"v_mfma_f32_16x16x32_bf16 v[92:95], a[56:59], a[8:11], v[92:95]\n" \
"ds_read_b128 a[4:7], v213 offset:2048\n" \
"v_mfma_f32_16x16x32_bf16 v[92:95], a[60:63], a[12:15], v[92:95]\n" \
"ds_read_b64_tr_b16 a[38:39], v214 offset:26624\n" \
"global_load_lds_dwordx4 v217, s[38:39] offset:-3072\n" \
"v_mfma_f32_16x16x32_bf16 v[88:91], a[48:51], a[8:11], v[88:91]\n" \
"ds_read_b64_tr_b16 a[40:41], v215 offset:16384\n" \
"v_mfma_f32_16x16x32_bf16 v[88:91], a[52:55], a[12:15], v[88:91]\n" \
"ds_read_b128 a[8:11], v213 offset:256\n" \
"v_mfma_f32_16x16x32_bf16 v[104:107], a[48:51], a[16:19], v[104:107]\n" \
"ds_read_b128 a[12:15], v213 offset:2304\n" \
"v_mfma_f32_16x16x32_bf16 v[104:107], a[52:55], a[20:23], v[104:107]\n" \
"ds_read_b64_tr_b16 a[42:43], v215 offset:18432\n" \
"global_load_lds_dwordx4 v218, s[38:39] offset:-2048\n" \
"v_mfma_f32_16x16x32_bf16 v[108:111], a[56:59], a[16:19], v[108:111]\n" \
"ds_read_b64_tr_b16 a[44:45], v215 offset:24576\n" \
"v_mfma_f32_16x16x32_bf16 v[108:111], a[60:63], a[20:23], v[108:111]\n" \
"ds_read_b128 a[16:19], v213 offset:4096\n" \
"global_load_lds_dwordx4 v219, s[38:39] offset:-1024\n" \
"v_mfma_f32_16x16x32_bf16 v[124:127], a[56:59], a[24:27], v[124:127]\n" \
"s_add_u32 s38, s38, 0x20000\n" \
"s_addc_u32 s39, s39, 0\n" \
"ds_read_b128 a[20:23], v213 offset:6144\n" \
"v_mfma_f32_16x16x32_bf16 v[124:127], a[60:63], a[28:31], v[124:127]\n" \
"s_sub_u32 s75, s75, 32768\n" \
"s_cselect_b32 s75, 131072, s75\n" \
"ds_read_b64_tr_b16 a[46:47], v215 offset:26624\n" \
"v_mfma_f32_16x16x32_bf16 v[120:123], a[48:51], a[24:27], v[120:123]\n" \
"s_sub_u32 s72, s72, 1\n" \
"s_cmp_lg_u32 s72, 0\n" \
"v_mfma_f32_16x16x32_bf16 v[120:123], a[52:55], a[28:31], v[120:123]\n" \
"s_waitcnt lgkmcnt(3)\n" \
"s_cbranch_scc1 Lo64_%=\n" \
"s_setpc_b64 s[68:69]\n" \
"Lhot_end_%=:\n" \
"Lmask0_%=:\n" \
"Lpg13433_%=:\n" \
"s_cbranch_execz Lcb0_%=\n" \
"s_waitcnt lgkmcnt(0)\n" \
"v_subrev_u32 v0, 0, v251\n" \
"v_subrev_u32 v1, 1, v251\n" \
"v_subrev_u32 v2, 2, v251\n" \
"v_subrev_u32 v3, 3, v251\n" \
"v_subrev_u32 v4, 16, v251\n" \
"v_subrev_u32 v5, 17, v251\n" \
"v_subrev_u32 v6, 18, v251\n" \
"v_subrev_u32 v7, 19, v251\n" \
"v_subrev_u32 v32, 32, v251\n" \
"v_subrev_u32 v33, 33, v251\n" \
"v_subrev_u32 v34, 34, v251\n" \
"v_subrev_u32 v35, 35, v251\n" \
"v_subrev_u32 v36, 48, v251\n" \
"v_subrev_u32 v37, 49, v251\n" \
"v_subrev_u32 v38, 50, v251\n" \
"v_subrev_u32 v39, 51, v251\n" \
"v_ashrrev_i32 v0, 31, v0\n" \
"v_ashrrev_i32 v1, 31, v1\n" \
"v_ashrrev_i32 v2, 31, v2\n" \
"v_ashrrev_i32 v3, 31, v3\n" \
"v_ashrrev_i32 v4, 31, v4\n" \
"v_ashrrev_i32 v5, 31, v5\n" \
"v_ashrrev_i32 v6, 31, v6\n" \
"v_ashrrev_i32 v7, 31, v7\n" \
"v_ashrrev_i32 v32, 31, v32\n" \
"v_ashrrev_i32 v33, 31, v33\n" \
"v_ashrrev_i32 v34, 31, v34\n" \
"v_ashrrev_i32 v35, 31, v35\n" \
"v_ashrrev_i32 v36, 31, v36\n" \
"v_ashrrev_i32 v37, 31, v37\n" \
"v_ashrrev_i32 v38, 31, v38\n" \
"v_ashrrev_i32 v39, 31, v39\n" \
"v_and_b32 v0, 0xff800000, v0\n" \
"v_and_b32 v1, 0xff800000, v1\n" \
"v_and_b32 v2, 0xff800000, v2\n" \
"v_and_b32 v3, 0xff800000, v3\n" \
"v_and_b32 v4, 0xff800000, v4\n" \
"v_and_b32 v5, 0xff800000, v5\n" \
"v_and_b32 v6, 0xff800000, v6\n" \
"v_and_b32 v7, 0xff800000, v7\n" \
"v_and_b32 v32, 0xff800000, v32\n" \
"v_and_b32 v33, 0xff800000, v33\n" \
"v_and_b32 v34, 0xff800000, v34\n" \
"v_and_b32 v35, 0xff800000, v35\n" \
"v_and_b32 v36, 0xff800000, v36\n" \
"v_and_b32 v37, 0xff800000, v37\n" \
"v_and_b32 v38, 0xff800000, v38\n" \
"v_and_b32 v39, 0xff800000, v39\n" \
"s_branch Lmask0r_%=\n" \
"Lpl1_%=:\n" \
"s_mov_b32 s31, 0\n" \
"Lpk1_%=:\n" \
"s_sleep 2\n" \
"buffer_load_dword v244, v245, s[56:59], s91 offen sc1\n" \
"s_waitcnt vmcnt(0)\n" \
"v_cmp_ne_u32 vcc, s60, v244\n" \
"s_nop 1\n" \
"s_and_b32 vcc_hi, vcc_lo, 255\n" \
"s_cbranch_scc0 Lpr1_%=\n" \
"s_add_u32 s31, s31, 1\n" \
"s_cmp_lt_u32 s31, 0x40000\n" \
"s_cbranch_scc1 Lpk1_%=\n" \
"s_branch Lpr1_%=\n" \
"Lrs0_%=:\n" \
"s_call_b64 s[68:69], Lrest_%=\n" \
"s_branch Lrr0_%=\n" \
"Lrest_%=:\n" \
"s_mov_b32 s31, 0\n" \
"s_waitcnt vmcnt(0)\n" \
"Lrk_%=:\n" \
"v_lshrrev_b32 v253, 2, v248\n" \
"s_sleep 2\n" \
"buffer_load_dword v253, v253, s[56:59], s79 offen sc1\n" \
"s_waitcnt vmcnt(0)\n" \
"v_cmp_ne_u32 s[52:53], s60, v253\n" \
"s_add_u32 s31, s31, 1\n" \
"s_cmp_lt_u32 s31, 0x40000\n" \
"s_cselect_b32 s54, 0xff, 0\n" \
"s_and_b32 s52, s52, s54\n" \
"s_cmp_lg_u32 s52, 0\n" \
"s_cbranch_scc1 Lrk_%=\n" \
"s_mov_b64 s[10:11], 0\n" \
"s_setpc_b64 s[68:69]\n" \
"Lrs1_%=:\n" \
"s_call_b64 s[68:69], Lrest_%=\n" \
"s_branch Lrr1_%=\n" \
"Lrs2_%=:\n" \
"s_call_b64 s[68:69], Lrest_%=\n" \
"s_branch Lrr2_%=\n" \
"Lrs3_%=:\n" \
"s_call_b64 s[68:69], Lrest_%=\n" \
"s_branch Lrr3_%=\n" \
"LF7_%=:\n" \
"v_mfma_f32_16x16x32_bf16 v[24:27], a[192:195], a[176:179], v[236:239]\n" \
"s_waitcnt vmcnt(13)\n" \
"ds_read_b128 v[192:195], v246 offset:16384\n" \
"s_bfe_u32 s88, %1, 0x20001\n" \
"v_mfma_f32_16x16x32_bf16 v[24:27], a[196:199], a[180:183], v[24:27]\n" \
"s_lshl_b32 s88, s88, 2\n" \
"ds_read_b128 v[196:199], v246 offset:18432\n" \
"v_cvt_pk_bf16_f32 v91, v94, v95\n" \
"v_mfma_f32_16x16x32_bf16 v[24:27], a[200:203], a[184:187], v[24:27]\n" \
"s_lshr_b32 s70, %1, 6\n" \
"ds_read_b128 v[200:203], v246 offset:20480\n" \
"v_mov_b32 v66, v242\n" \
"v_mov_b32 v64, v66\n" \
"v_exp_f32 v104, v104\n" \
"v_exp_f32 v105, v105\n" \
"v_exp_f32 v106, v106\n" \
"v_mfma_f32_16x16x32_bf16 v[24:27], a[204:207], a[188:191], v[24:27]\n" \
"s_add_u32 s88, s88, s70\n" \
"ds_read_b128 a[252:255], v246 offset:14592\n" \
"v_bfe_u32 v69, v248, 8, 1\n" \
"v_bfe_u32 v70, v248, 6, 2\n" \
"v_mfma_f32_16x16x32_bf16 v[16:19], a[200:203], a[168:171], v[232:235]\n" \
"s_lshr_b32 s71, s22, 7\n" \
"ds_read_b128 v[204:207], v246 offset:22528\n" \
"v_cvt_pk_bf16_f32 v104, v104, v105\n" \
"v_mfma_f32_16x16x32_bf16 v[16:19], a[204:207], a[172:175], v[16:19]\n" \
"s_sub_u32 s71, s63, s71\n" \
"ds_read_b128 v[208:211], v246 offset:16640\n" \
"v_permlane16_swap_b32 v64, v66\n" \
"v_add_f32 v66, v66, v64\n" \
"v_mov_b32 v64, v66\n" \
"v_exp_f32 v107, v107\n" \
"v_exp_f32 v108, v108\n" \
"v_mfma_f32_16x16x32_bf16 v[16:19], a[192:195], a[160:163], v[16:19]\n" \
"s_add_u32 s71, s71, 6\n" \
"ds_read_b128 v[212:215], v246 offset:18688\n" \
"v_cvt_pk_bf16_f32 v105, v106, v107\n" \
"v_mfma_f32_16x16x32_bf16 v[16:19], a[196:199], a[164:167], v[16:19]\n" \
"s_and_b32 s71, s71, 7\n" \
"ds_read_b128 v[216:219], v246 offset:20736\n" \
"v_lshl_add_u32 v70, v69, 3, v70\n" \
"v_mul_u32_u24 v70, 6144, v70\n" \
"v_permlane32_swap_b32 v64, v66\n" \
"v_add_f32 v66, v66, v64\n" \
"v_rcp_f32 v64, v66\n" \
"v_mul_f32 v66, 0x4d800000, v66\n" \
"v_add_u32 v65, 0x1c90080, v249\n" \
"v_cndmask_b32 v65, v249, v65, s[16:17]\n" \
"v_accvgpr_read_b32 v96, a0\n" \
"v_accvgpr_read_b32 v97, a1\n" \
"v_accvgpr_read_b32 v98, a2\n" \
"v_accvgpr_read_b32 v99, a3\n" \
"v_accvgpr_read_b32 v100, a4\n" \
"v_accvgpr_read_b32 v101, a5\n" \
"v_accvgpr_read_b32 v102, a6\n" \
"v_accvgpr_read_b32 v103, a7\n" \
"v_exp_f32 v109, v109\n" \
"v_exp_f32 v110, v110\n" \
"v_pk_mul_f32 v[96:97], v[96:97], v[64:65] op_sel_hi:[1,0]\n" \
"v_pk_mul_f32 v[98:99], v[98:99], v[64:65] op_sel_hi:[1,0]\n" \
"v_pk_mul_f32 v[100:101], v[100:101], v[64:65] op_sel_hi:[1,0]\n" \
"v_pk_mul_f32 v[102:103], v[102:103], v[64:65] op_sel_hi:[1,0]\n" \
"v_cvt_pk_bf16_f32 v224, v96, v97\n" \
"v_cvt_pk_bf16_f32 v225, v98, v99\n" \
"v_mfma_f32_16x16x32_bf16 v[20:23], a[208:211], a[160:163], v[232:235]\n" \
"s_and_b32 s70, %1, 1\n" \
"ds_read_b128 v[220:223], v246 offset:22784\n" \
"s_lshl_b32 s70, s70, 3\n" \
"v_mfma_f32_16x16x32_bf16 v[20:23], a[212:215], a[164:167], v[20:23]\n" \
"s_add_u32 s70, s70, s71\n" \
"ds_read_b128 a[192:195], v247 offset:16384\n" \
"v_cvt_pk_bf16_f32 v226, v100, v101\n" \
"v_cvt_pk_bf16_f32 v227, v102, v103\n" \
"v_lshlrev_b32 v69, 5, v69\n" \
"v_and_b32 v71, 0x30, v248\n" \
"v_pk_mul_f32 v[96:97], v[96:97], v[98:99]\n" \
"v_pk_fma_f32 v[96:97], v[100:101], v[102:103], v[96:97]\n" \
"v_fma_f32 v66, v96, v97, v66\n" \
"v_mfma_f32_16x16x32_bf16 v[20:23], a[216:219], a[168:171], v[20:23]\n" \
"s_lshl_b32 s87, s70, 12\n" \
"ds_read_b128 a[196:199], v247 offset:24576\n" \
"v_cvt_pk_bf16_f32 v106, v108, v109\n" \
"v_mfma_f32_16x16x32_bf16 v[20:23], a[220:223], a[172:175], v[20:23]\n" \
"s_lshl_b32 s70, s70, 7\n" \
"ds_read_b128 a[200:203], v247 offset:17408\n" \
"buffer_store_dwordx4 v[224:227], v65, s[56:59], s77 offen offset:0 sc1\n" \
"v_exp_f32 v111, v111\n" \
"v_exp_f32 v112, v112\n" \
"v_accvgpr_read_b32 v96, a8\n" \
"v_accvgpr_read_b32 v97, a9\n" \
"v_mfma_f32_16x16x32_bf16 v[28:31], a[216:219], a[184:187], v[236:239]\n" \
"s_mul_i32 s84, s61, 6144\n" \
"ds_read_b128 a[204:207], v247 offset:25600\n" \
"v_cvt_pk_bf16_f32 v107, v110, v111\n" \
"v_mfma_f32_16x16x32_bf16 v[28:31], a[220:223], a[188:191], v[28:31]\n" \
"s_lshl_b32 s71, s84, 4\n" \
"v_xor_b32 v69, v69, v71\n" \
"v_lshrrev_b32 v71, 9, v248\n" \
"v_mfma_f32_16x16x32_bf16 v[28:31], a[208:211], a[176:179], v[28:31]\n" \
"s_add_u32 s70, s70, s71\n" \
"s_add_u32 s70, s70, 4096\n" \
"v_lshl_add_u32 v70, v71, 6, v70\n" \
"v_mfma_f32_16x16x32_bf16 v[28:31], a[212:215], a[180:183], v[28:31]\n" \
"s_waitcnt lgkmcnt(2)\n" \
"ds_read_b128 a[208:211], v247 offset:18432\n" \
"v_add_u32 v253, v69, v70\n" \
"v_accvgpr_read_b32 v98, a10\n" \
"v_accvgpr_read_b32 v99, a11\n" \
"v_accvgpr_read_b32 v100, a12\n" \
"v_accvgpr_read_b32 v101, a13\n" \
"v_accvgpr_read_b32 v102, a14\n" \
"v_accvgpr_read_b32 v103, a15\n" \
"v_exp_f32 v113, v113\n" \
"v_exp_f32 v114, v114\n" \
"v_pk_mul_f32 v[96:97], v[96:97], v[64:65] op_sel_hi:[1,0]\n" \
"v_pk_mul_f32 v[98:99], v[98:99], v[64:65] op_sel_hi:[1,0]\n" \
"v_pk_mul_f32 v[100:101], v[100:101], v[64:65] op_sel_hi:[1,0]\n" \
"v_pk_mul_f32 v[102:103], v[102:103], v[64:65] op_sel_hi:[1,0]\n" \
"v_cvt_pk_bf16_f32 v228, v96, v97\n" \
"v_cvt_pk_bf16_f32 v229, v98, v99\n" \
"v_cvt_pk_bf16_f32 v230, v100, v101\n" \
"v_cvt_pk_bf16_f32 v231, v102, v103\n" \
"v_pk_mul_f32 v[96:97], v[96:97], v[98:99]\n" \
"v_pk_fma_f32 v[96:97], v[100:101], v[102:103], v[96:97]\n" \
"v_fma_f32 v66, v96, v97, v66\n" \
"v_mfma_f32_16x16x32_bf16 v[8:11], v[192:195], a[176:179], v[236:239]\n" \
"s_add_u32 s48, s22, s70\n" \
"s_addc_u32 s49, s23, 0\n" \
"ds_read_b128 a[212:215], v247 offset:26624\n" \
"v_mfma_f32_16x16x32_bf16 v[8:11], v[196:199], a[180:183], v[8:11]\n" \
"s_add_u32 s50, s48, 23552\n" \
"s_addc_u32 s51, s49, 0\n" \
"ds_read_b128 a[216:219], v247 offset:19456\n" \
"v_mfma_f32_16x16x32_bf16 v[8:11], v[200:203], a[184:187], v[8:11]\n" \
"s_lshr_b32 s89, s22, 7\n" \
"ds_read_b128 a[220:223], v247 offset:27648\n" \
"v_cvt_pk_bf16_f32 v112, v112, v113\n" \
"v_mfma_f32_16x16x32_bf16 v[8:11], v[204:207], a[188:191], v[8:11]\n" \
"s_sub_u32 s89, 3, s89\n" \
"ds_read_b128 v[192:195], v246 offset:24576\n" \
"v_bfe_u32 v84, v248, 7, 2\n" \
"s_and_b32 s89, s89, 7\n" \
"v_mfma_f32_16x16x32_bf16 v[56:59], a[232:235], a[184:187], v[236:239]\n" \
"s_and_b32 s90, %1, 1\n" \
"ds_read_b128 v[196:199], v246 offset:26624\n" \
"v_bfe_u32 v85, v248, 5, 2\n" \
"v_mfma_f32_16x16x32_bf16 v[56:59], a[236:239], a[188:191], v[56:59]\n" \
"s_lshl_b32 s90, s90, 3\n" \
"ds_read_b128 v[200:203], v246 offset:28672\n" \
"v_exp_f32 v115, v115\n" \
"v_mfma_f32_16x16x32_bf16 v[56:59], a[224:227], a[176:179], v[56:59]\n" \
"s_add_u32 s89, s89, s90\n" \
"ds_read_b128 v[204:207], v246 offset:30720\n" \
"v_min_u32 v85, 2, v85\n" \
"v_mfma_f32_16x16x32_bf16 v[56:59], a[228:231], a[180:183], v[56:59]\n" \
"s_lshl_b32 s89, s89, 7\n" \
"s_lshr_b32 s90, %1, 3\n" \
"v_mad_u32_u24 v84, v84, 3, v85\n" \
"s_lshl_b32 s90, s90, 2\n" \
"v_mfma_f32_16x16x32_bf16 v[48:51], a[224:227], a[160:163], v[232:235]\n" \
"s_add_u32 s90, s90, s61\n" \
"v_and_b32 v85, 16, v248\n" \
"s_mul_i32 s90, s90, 24576\n" \
"v_mfma_f32_16x16x32_bf16 v[48:51], a[228:231], a[164:167], v[48:51]\n" \
"s_add_u32 s89, s89, s90\n" \
"v_lshl_add_u32 v85, v85, 2, s89\n" \
"v_lshl_add_u32 v84, v84, 11, v85\n" \
"v_mfma_f32_16x16x32_bf16 v[48:51], a[232:235], a[168:171], v[48:51]\n" \
"s_add_u32 m0, s84, 143360\n" \
"v_exp_f32 v116, v116\n" \
"v_mfma_f32_16x16x32_bf16 v[48:51], a[236:239], a[172:175], v[48:51]\n" \
"ds_read_b128 a[224:227], v247 offset:20480\n" \
"global_load_lds_dwordx4 v253, s[48:49] offset:-4096\n" \
"v_mfma_f32_16x16x32_bf16 v[12:15], v[216:219], a[184:187], v[236:239]\n" \
"ds_read_b128 a[228:231], v247 offset:28672\n" \
"v_cvt_pk_bf16_f32 v113, v114, v115\n" \
"v_mfma_f32_16x16x32_bf16 v[12:15], v[220:223], a[188:191], v[12:15]\n" \
"ds_read_b128 a[232:235], v247 offset:21504\n" \
"v_exp_f32 v117, v117\n" \
"v_mfma_f32_16x16x32_bf16 v[12:15], v[208:211], a[176:179], v[12:15]\n" \
"ds_read_b128 a[236:239], v247 offset:29696\n" \
"global_load_lds_dwordx4 v253, s[48:49] offset:-2048\n" \
"v_mfma_f32_16x16x32_bf16 v[12:15], v[212:215], a[180:183], v[12:15]\n" \
"ds_read_b128 v[208:211], v246 offset:24832\n" \
"v_exp_f32 v118, v118\n" \
"v_mfma_f32_16x16x32_bf16 v[60:63], a[240:243], a[176:179], v[236:239]\n" \
"ds_read_b128 v[212:215], v246 offset:26880\n" \
"v_cvt_pk_bf16_f32 v114, v116, v117\n" \
"v_mfma_f32_16x16x32_bf16 v[60:63], a[244:247], a[180:183], v[60:63]\n" \
"ds_read_b128 v[216:219], v246 offset:28928\n" \
"global_load_lds_dwordx4 v253, s[48:49] offset:0\n" \
"v_mfma_f32_16x16x32_bf16 v[60:63], a[248:251], a[184:187], v[60:63]\n" \
"s_add_u32 s48, s48, 0x120000\n" \
"s_addc_u32 s49, s49, 0\n" \
"ds_read_b128 v[220:223], v246 offset:30976\n" \
"v_mfma_f32_16x16x32_bf16 v[60:63], a[252:255], a[188:191], v[60:63]\n" \
"buffer_store_dwordx4 v[228:231], v65, s[56:59], s77 offen offset:2048 sc1\n" \
"v_exp_f32 v119, v119\n" \
"v_exp_f32 v120, v120\n" \
"v_cvt_pk_bf16_f32 v115, v118, v119\n" \
"v_mfma_f32_16x16x32_bf16 v[52:55], a[248:251], a[168:171], v[232:235]\n" \
"global_load_lds_dwordx4 v253, s[50:51] offset:-3072\n" \
"global_load_lds_dwordx4 v253, s[50:51] offset:-1024\n" \
"v_mfma_f32_16x16x32_bf16 v[52:55], a[252:255], a[172:175], v[52:55]\n" \
"s_add_u32 s77, s77, 0x1000\n" \
"v_exp_f32 v121, v121\n" \
"v_mfma_f32_16x16x32_bf16 v[52:55], a[240:243], a[160:163], v[52:55]\n" \
"v_exp_f32 v122, v122\n" \
"v_mfma_f32_16x16x32_bf16 v[52:55], a[244:247], a[164:167], v[52:55]\n" \
"s_waitcnt lgkmcnt(0)\n" \
"ds_read_b128 a[240:243], v247 offset:22528\n" \
"v_accvgpr_read_b32 v96, a16\n" \
"v_accvgpr_read_b32 v97, a17\n" \
"v_accvgpr_read_b32 v98, a18\n" \
"v_accvgpr_read_b32 v99, a19\n" \
"v_accvgpr_read_b32 v100, a20\n" \
"v_accvgpr_read_b32 v101, a21\n" \
"v_accvgpr_read_b32 v102, a22\n" \
"v_accvgpr_read_b32 v103, a23\n" \
"v_cvt_pk_bf16_f32 v120, v120, v121\n" \
"v_exp_f32 v123, v123\n" \
"v_pk_mul_f32 v[96:97], v[96:97], v[64:65] op_sel_hi:[1,0]\n" \
"v_pk_mul_f32 v[98:99], v[98:99], v[64:65] op_sel_hi:[1,0]\n" \
"v_pk_mul_f32 v[100:101], v[100:101], v[64:65] op_sel_hi:[1,0]\n" \
"v_pk_mul_f32 v[102:103], v[102:103], v[64:65] op_sel_hi:[1,0]\n" \
"v_cvt_pk_bf16_f32 v224, v96, v97\n" \
"v_cvt_pk_bf16_f32 v225, v98, v99\n" \
"v_cvt_pk_bf16_f32 v226, v100, v101\n" \
"v_cvt_pk_bf16_f32 v227, v102, v103\n" \
"v_pk_mul_f32 v[96:97], v[96:97], v[98:99]\n" \
"v_pk_fma_f32 v[96:97], v[100:101], v[102:103], v[96:97]\n" \
"v_fma_f32 v66, v96, v97, v66\n" \
"v_mfma_f32_16x16x32_bf16 v[40:43], v[192:195], a[176:179], v[236:239]\n" \
"buffer_store_dwordx4 v[224:227], v65, s[56:59], s77 offen offset:0 sc1\n" \
"ds_read_b128 a[244:247], v247 offset:30720\n" \
"v_accvgpr_read_b32 v96, a24\n" \
"v_accvgpr_read_b32 v97, a25\n" \
"v_accvgpr_read_b32 v98, a26\n" \
"v_accvgpr_read_b32 v99, a27\n" \
"v_accvgpr_read_b32 v100, a28\n" \
"v_accvgpr_read_b32 v101, a29\n" \
"v_accvgpr_read_b32 v102, a30\n" \
"v_accvgpr_read_b32 v103, a31\n" \
"v_exp_f32 v124, v124\n" \
"v_pk_mul_f32 v[96:97], v[96:97], v[64:65] op_sel_hi:[1,0]\n" \
"v_pk_mul_f32 v[98:99], v[98:99], v[64:65] op_sel_hi:[1,0]\n" \
"v_pk_mul_f32 v[100:101], v[100:101], v[64:65] op_sel_hi:[1,0]\n" \
"v_pk_mul_f32 v[102:103], v[102:103], v[64:65] op_sel_hi:[1,0]\n" \
"v_mfma_f32_16x16x32_bf16 v[40:43], v[196:199], a[180:183], v[40:43]\n" \
"ds_read_b128 a[248:251], v247 offset:23552\n" \
"v_cvt_pk_bf16_f32 v121, v122, v123\n" \
"v_mfma_f32_16x16x32_bf16 v[40:43], v[200:203], a[184:187], v[40:43]\n" \
"ds_read_b128 a[252:255], v247 offset:31744\n" \
"global_load_lds_dwordx4 v253, s[50:51] offset:1024\n" \
"v_mfma_f32_16x16x32_bf16 v[40:43], v[204:207], a[188:191], v[40:43]\n" \
"s_add_u32 s50, s50, 0x120000\n" \
"s_addc_u32 s51, s51, 0\n" \
"v_exp_f32 v125, v125\n" \
"v_mfma_f32_16x16x32_bf16 v[44:47], v[216:219], a[184:187], v[236:239]\n" \
"v_exp_f32 v126, v126\n" \
"v_mfma_f32_16x16x32_bf16 v[44:47], v[220:223], a[188:191], v[44:47]\n" \
"s_waitcnt vmcnt(9) lgkmcnt(0)\n" \
"global_load_dword v85, v84, s[22:23]\n" \
"v_mfma_f32_16x16x32_bf16 v[44:47], v[208:211], a[176:179], v[44:47]\n" \
"v_add_u32 v247, 0x10000, v248\n" \
"v_cvt_pk_bf16_f32 v122, v124, v125\n" \
"v_mfma_f32_16x16x32_bf16 v[44:47], v[212:215], a[180:183], v[44:47]\n" \
"s_barrier\n" \
"v_cvt_pk_bf16_f32 v228, v96, v97\n" \
"v_cvt_pk_bf16_f32 v229, v98, v99\n" \
"v_cvt_pk_bf16_f32 v230, v100, v101\n" \
"v_cvt_pk_bf16_f32 v231, v102, v103\n" \
"v_pk_mul_f32 v[96:97], v[96:97], v[98:99]\n" \
"v_pk_fma_f32 v[96:97], v[100:101], v[102:103], v[96:97]\n" \
"v_mfma_f32_16x16x32_bf16 a[32:35], a[192:195], v[72:75], a[32:35]\n" \
"v_exp_f32 v127, v127\n" \
"v_mfma_f32_16x16x32_bf16 a[32:35], a[196:199], v[104:107], a[32:35]\n" \
"v_cvt_pk_bf16_f32 v123, v126, v127\n" \
"v_fma_f32 v66, v96, v97, v66\n" \
"v_mfma_f32_16x16x32_bf16 v[128:131], a[192:195], v[80:83], v[128:131]\n" \
"ds_read_b128 v[192:195], v247 offset:16384\n" \
"v_cmp_gt_i32 vcc, 0, v251\n" \
"v_mfma_f32_16x16x32_bf16 v[128:131], a[196:199], v[112:115], v[128:131]\n" \
"buffer_store_dwordx4 v[228:231], v65, s[56:59], s77 offen offset:2048 sc1\n" \
"v_lshrrev_b32 v71, 7, v248\n" \
"v_and_b32 v76, 0x70, v248\n" \
"v_cmp_class_f32 s[54:55], v66, s94\n" \
"s_sub_u32 s77, s77, 4096\n" \
"v_mfma_f32_16x16x32_bf16 v[160:163], a[192:195], v[88:91], v[160:163]\n" \
"s_lshl_b32 s70, s88, 18\n" \
"v_cmp_gt_i32 s[64:65], 1, v251\n" \
"v_cmp_gt_i32 s[66:67], 2, v251\n" \
"v_mfma_f32_16x16x32_bf16 v[160:163], a[196:199], v[120:123], v[160:163]\n" \
"s_lshl_b32 s71, s61, 16\n" \
"ds_read_b128 a[192:195], v247 offset:0\n" \
"v_cndmask_b32 v8, v8, v254, vcc\n" \
"v_mfma_f32_16x16x32_bf16 v[240:243], a[124:127], v[104:107], v[240:243]\n" \
"s_add_u32 s71, s71, s70\n" \
"ds_read_b128 v[196:199], v247 offset:24576\n" \
"v_cndmask_b32 v16, v16, v254, vcc\n" \
"v_mfma_f32_16x16x32_bf16 v[164:167], a[200:203], v[88:91], v[164:167]\n" \
"s_or_b64 s[92:93], s[92:93], s[54:55]\n" \
"v_lshlrev_b32 v77, 4, v71\n" \
"v_xor_b32 v77, v77, v76\n" \
"v_mfma_f32_16x16x32_bf16 v[164:167], a[204:207], v[120:123], v[164:167]\n" \
"s_add_u32 s42, s20, s71\n" \
"s_addc_u32 s43, s21, 0\n" \
"ds_read_b128 a[196:199], v247 offset:8192\n" \
"v_cmp_gt_i32 vcc, 3, v251\n" \
"v_cndmask_b32 v9, v9, v254, s[64:65]\n" \
"v_cndmask_b32 v17, v17, v254, s[64:65]\n" \
"v_cmp_gt_i32 s[64:65], 16, v251\n" \
"v_cndmask_b32 v10, v10, v254, s[66:67]\n" \
"v_cndmask_b32 v18, v18, v254, s[66:67]\n" \
"v_cmp_gt_i32 s[66:67], 17, v251\n" \
"v_mfma_f32_16x16x32_bf16 v[132:135], a[200:203], v[80:83], v[132:135]\n" \
"ds_read_b128 v[200:203], v247 offset:17408\n" \
"v_lshl_add_u32 v252, v71, 12, v77\n" \
"v_mfma_f32_16x16x32_bf16 v[132:135], a[204:207], v[112:115], v[132:135]\n" \
"s_add_u32 s44, s42, 31744\n" \
"s_addc_u32 s45, s43, 0\n" \
"v_cndmask_b32 v11, v11, v254, vcc\n" \
"v_mfma_f32_16x16x32_bf16 a[36:39], a[200:203], v[72:75], a[36:39]\n" \
"s_max_u32 s70, s70, 32768\n" \
"v_cndmask_b32 v19, v19, v254, vcc\n" \
"v_cmp_gt_i32 vcc, 18, v251\n" \
"v_mfma_f32_16x16x32_bf16 a[36:39], a[204:207], v[104:107], a[36:39]\n" \
"s_sub_u32 s70, s70, 32768\n" \
"ds_read_b128 a[200:203], v247 offset:1024\n" \
"v_cndmask_b32 v12, v12, v254, s[64:65]\n" \
"v_mfma_f32_16x16x32_bf16 v[242:245], a[120:123], v[112:115], v[242:245]\n" \
"s_and_b32 s86, s61, 1\n" \
"ds_read_b128 v[204:207], v247 offset:25600\n" \
"v_cndmask_b32 v20, v20, v254, s[64:65]\n" \
"v_mfma_f32_16x16x32_bf16 a[40:43], a[208:211], v[72:75], a[40:43]\n" \
"s_lshl_b32 s86, s86, 2\n" \
"v_cmp_gt_i32 s[64:65], 19, v251\n" \
"v_and_or_b32 v71, v71, 3, s86\n" \
"v_mfma_f32_16x16x32_bf16 v[242:245], a[124:127], v[120:123], v[242:245]\n" \
"v_cndmask_b32 v13, v13, v254, s[66:67]\n" \
"v_cndmask_b32 v21, v21, v254, s[66:67]\n" \
"v_cmp_gt_i32 s[66:67], 32, v251\n" \
"v_mfma_f32_16x16x32_bf16 a[40:43], a[212:215], v[104:107], a[40:43]\n" \
"s_lshr_b32 s71, s61, 1\n" \
"ds_read_b128 a[204:207], v247 offset:9216\n" \
"v_cndmask_b32 v14, v14, v254, vcc\n" \
"v_cndmask_b32 v22, v22, v254, vcc\n" \
"v_cmp_gt_i32 vcc, 33, v251\n" \
"v_cndmask_b32 v15, v15, v254, s[64:65]\n" \
"v_cndmask_b32 v23, v23, v254, s[64:65]\n" \
"v_cmp_gt_i32 s[64:65], 34, v251\n" \
"v_cndmask_b32 v40, v40, v254, s[66:67]\n" \
"v_cndmask_b32 v48, v48, v254, s[66:67]\n" \
"v_cmp_gt_i32 s[66:67], 35, v251\n" \
"v_cndmask_b32 v41, v41, v254, vcc\n" \
"v_mfma_f32_16x16x32_bf16 v[136:139], a[208:211], v[80:83], v[136:139]\n" \
"s_lshl_b32 s71, s71, 11\n" \
"ds_read_b128 v[208:211], v247 offset:18432\n" \
"v_lshlrev_b32 v77, 4, v71\n" \
"v_mfma_f32_16x16x32_bf16 v[136:139], a[212:215], v[112:115], v[136:139]\n" \
"s_add_u32 s70, s70, s71\n" \
"v_mov_b32 v66, v243\n" \
"v_cndmask_b32 v49, v49, v254, vcc\n" \
"v_cmp_gt_i32 vcc, 48, v251\n" \
"v_cndmask_b32 v42, v42, v254, s[64:65]\n" \
"v_cndmask_b32 v50, v50, v254, s[64:65]\n" \
"v_cmp_gt_i32 s[64:65], 49, v251\n" \
"v_cndmask_b32 v43, v43, v254, s[66:67]\n" \
"v_cndmask_b32 v51, v51, v254, s[66:67]\n" \
"v_cmp_gt_i32 s[66:67], 50, v251\n" \
"v_cndmask_b32 v44, v44, v254, vcc\n" \
"v_cndmask_b32 v52, v52, v254, vcc\n" \
"v_xor_b32 v77, v77, v76\n" \
"v_mfma_f32_16x16x32_bf16 v[168:171], a[208:211], v[88:91], v[168:171]\n" \
"s_mov_b32 s46, s70\n" \
"v_cmp_gt_i32 vcc, 51, v251\n" \
"v_lshl_add_u32 v246, v71, 12, v77\n" \
"v_mfma_f32_16x16x32_bf16 v[168:171], a[212:215], v[120:123], v[168:171]\n" \
"s_lshl_b32 s85, s61, 10\n" \
"ds_read_b128 a[208:211], v247 offset:2048\n" \
"s_mov_b64 s[12:13], s[20:21]\n" \
"ds_read_b128 v[212:215], v247 offset:26624\n" \
"v_cndmask_b32 v45, v45, v254, s[64:65]\n" \
"v_cndmask_b32 v53, v53, v254, s[64:65]\n" \
"v_mfma_f32_16x16x32_bf16 v[172:175], a[216:219], v[88:91], v[172:175]\n" \
"s_and_b32 s13, s13, 0xffff\n" \
"v_cndmask_b32 v46, v46, v254, s[66:67]\n" \
"v_cndmask_b32 v54, v54, v254, s[66:67]\n" \
"v_mfma_f32_16x16x32_bf16 v[172:175], a[220:223], v[120:123], v[172:175]\n" \
"s_mov_b32 s14, 4194304\n" \
"ds_read_b128 a[212:215], v247 offset:10240\n" \
"v_cndmask_b32 v47, v47, v254, vcc\n" \
"v_mfma_f32_16x16x32_bf16 v[140:143], a[216:219], v[80:83], v[140:143]\n" \
"s_mov_b32 s15, 0x20000\n" \
"ds_read_b128 v[216:219], v247 offset:19456\n" \
"v_cndmask_b32 v55, v55, v254, vcc\n" \
"v_mfma_f32_16x16x32_bf16 v[140:143], a[220:223], v[112:115], v[140:143]\n" \
"v_lshrrev_b32 v71, 9, v248\n" \
"v_lshl_add_u32 v246, v71, 30, v246\n" \
"v_exp_f32 v8, v8\n" \
"v_exp_f32 v9, v9\n" \
"v_mfma_f32_16x16x32_bf16 a[44:47], a[216:219], v[72:75], a[44:47]\n" \
"s_add_u32 m0, s95, 118784\n" \
"v_exp_f32 v10, v10\n" \
"v_mfma_f32_16x16x32_bf16 a[44:47], a[220:223], v[104:107], a[44:47]\n" \
"ds_read_b128 a[216:219], v247 offset:3072\n" \
"global_load_lds_dwordx4 v252, s[42:43] offset:0\n" \
"ds_read_b128 v[220:223], v247 offset:27648\n" \
"v_cvt_pk_bf16_f32 v8, v8, v9\n" \
"v_mfma_f32_16x16x32_bf16 a[48:51], a[224:227], v[72:75], a[48:51]\n" \
"v_exp_f32 v11, v11\n" \
"v_mfma_f32_16x16x32_bf16 a[48:51], a[228:231], v[104:107], a[48:51]\n" \
"ds_read_b128 a[220:223], v247 offset:11264\n" \
"v_exp_f32 v12, v12\n" \
"v_mfma_f32_16x16x32_bf16 v[144:147], a[224:227], v[80:83], v[144:147]\n" \
"ds_read_b128 v[224:227], v247 offset:20480\n" \
"v_cvt_pk_bf16_f32 v9, v10, v11\n" \
"v_mfma_f32_16x16x32_bf16 v[144:147], a[228:231], v[112:115], v[144:147]\n" \
"v_exp_f32 v13, v13\n" \
"v_mfma_f32_16x16x32_bf16 v[176:179], a[224:227], v[88:91], v[176:179]\n" \
"v_exp_f32 v14, v14\n" \
"v_mfma_f32_16x16x32_bf16 v[176:179], a[228:231], v[120:123], v[176:179]\n" \
"ds_read_b128 a[224:227], v247 offset:4096\n" \
"global_load_lds_dwordx4 v252, s[42:43] offset:2048\n" \
"ds_read_b128 v[228:231], v247 offset:28672\n" \
"v_mfma_f32_16x16x32_bf16 v[180:183], a[232:235], v[88:91], v[180:183]\n" \
"s_add_u32 s42, s42, 0x180\n" \
"s_addc_u32 s43, s43, 0\n" \
"v_cvt_pk_bf16_f32 v10, v12, v13\n" \
"v_mfma_f32_16x16x32_bf16 v[180:183], a[236:239], v[120:123], v[180:183]\n" \
"ds_read_b128 a[228:231], v247 offset:12288\n" \
"v_exp_f32 v15, v15\n" \
"v_exp_f32 v40, v40\n" \
"v_mfma_f32_16x16x32_bf16 v[148:151], a[232:235], v[80:83], v[148:151]\n" \
"ds_read_b128 v[232:235], v247 offset:21504\n" \
"v_cvt_pk_bf16_f32 v11, v14, v15\n" \
"v_mfma_f32_16x16x32_bf16 v[148:151], a[236:239], v[112:115], v[148:151]\n" \
"v_exp_f32 v41, v41\n" \
"v_mfma_f32_16x16x32_bf16 a[52:55], a[232:235], v[72:75], a[52:55]\n" \
"v_exp_f32 v42, v42\n" \
"v_mfma_f32_16x16x32_bf16 a[52:55], a[236:239], v[104:107], a[52:55]\n" \
"ds_read_b128 a[232:235], v247 offset:5120\n" \
"global_load_lds_dwordx4 v252, s[44:45] offset:1024\n" \
"ds_read_b128 v[236:239], v247 offset:29696\n" \
"v_cvt_pk_bf16_f32 v40, v40, v41\n" \
"v_mfma_f32_16x16x32_bf16 a[56:59], a[240:243], v[72:75], a[56:59]\n" \
"v_exp_f32 v43, v43\n" \
"v_mfma_f32_16x16x32_bf16 a[56:59], a[244:247], v[104:107], a[56:59]\n" \
"ds_read_b128 a[236:239], v247 offset:13312\n" \
"v_exp_f32 v44, v44\n" \
"v_mfma_f32_16x16x32_bf16 v[152:155], a[240:243], v[80:83], v[152:155]\n" \
"ds_read_b128 v[0:3], v247 offset:22528\n" \
"v_cvt_pk_bf16_f32 v41, v42, v43\n" \
"v_mfma_f32_16x16x32_bf16 v[152:155], a[244:247], v[112:115], v[152:155]\n" \
"v_exp_f32 v45, v45\n" \
"v_mfma_f32_16x16x32_bf16 v[184:187], a[240:243], v[88:91], v[184:187]\n" \
"v_exp_f32 v46, v46\n" \
"v_mfma_f32_16x16x32_bf16 v[184:187], a[244:247], v[120:123], v[184:187]\n" \
"ds_read_b128 a[240:243], v247 offset:6144\n" \
"global_load_lds_dwordx4 v252, s[44:45] offset:3072\n" \
"ds_read_b128 v[4:7], v247 offset:30720\n" \
"v_mfma_f32_16x16x32_bf16 a[60:63], a[248:251], v[72:75], a[60:63]\n" \
"s_add_u32 s44, s44, 0x180\n" \
"s_addc_u32 s45, s45, 0\n" \
"v_cvt_pk_bf16_f32 v42, v44, v45\n" \
"v_mfma_f32_16x16x32_bf16 a[60:63], a[252:255], v[104:107], a[60:63]\n" \
"s_add_u32 m0, s85, 135168\n" \
"ds_read_b128 a[244:247], v247 offset:14336\n" \
"v_exp_f32 v47, v47\n" \
"v_exp_f32 v24, v24\n" \
"v_mfma_f32_16x16x32_bf16 v[156:159], a[248:251], v[80:83], v[156:159]\n" \
"ds_read_b128 v[32:35], v247 offset:23552\n" \
"v_cvt_pk_bf16_f32 v43, v46, v47\n" \
"v_mfma_f32_16x16x32_bf16 v[156:159], a[252:255], v[112:115], v[156:159]\n" \
"buffer_load_dwordx4 v246, s[12:15], s46 offen lds\n" \
"v_exp_f32 v25, v25\n" \
"v_mfma_f32_16x16x32_bf16 v[188:191], a[248:251], v[88:91], v[188:191]\n" \
"s_add_u32 s46, s46, 0x180\n" \
"v_exp_f32 v26, v26\n" \
"v_mfma_f32_16x16x32_bf16 v[188:191], a[252:255], v[120:123], v[188:191]\n" \
"s_waitcnt lgkmcnt(2)\n" \
"ds_read_b128 a[248:251], v247 offset:7168\n" \
"v_cvt_pk_bf16_f32 v24, v24, v25\n" \
"v_mfma_f32_16x16x32_bf16 v[242:245], a[124:127], v[8:11], v[242:245]\n" \
"v_exp_f32 v27, v27\n" \
"v_mfma_f32_16x16x32_bf16 v[242:245], a[124:127], v[40:43], v[242:245]\n" \
"ds_read_b128 v[36:39], v247 offset:31744\n" \
"v_exp_f32 v28, v28\n" \
"v_mfma_f32_16x16x32_bf16 v[160:163], v[192:195], v[8:11], v[160:163]\n" \
"v_cvt_pk_bf16_f32 v25, v26, v27\n" \
"v_exp_f32 v29, v29\n" \
"v_exp_f32 v30, v30\n" \
"v_cvt_pk_bf16_f32 v26, v28, v29\n" \
"v_exp_f32 v31, v31\n" \
"v_exp_f32 v56, v56\n" \
"v_cvt_pk_bf16_f32 v27, v30, v31\n" \
"v_exp_f32 v57, v57\n" \
"v_exp_f32 v58, v58\n" \
"v_cvt_pk_bf16_f32 v56, v56, v57\n" \
"v_mfma_f32_16x16x32_bf16 v[160:163], v[196:199], v[40:43], v[160:163]\n" \
"ds_read_b128 a[252:255], v247 offset:15360\n" \
"v_exp_f32 v59, v59\n" \
"v_mfma_f32_16x16x32_bf16 v[164:167], v[200:203], v[8:11], v[164:167]\n" \
"v_exp_f32 v60, v60\n" \
"v_mfma_f32_16x16x32_bf16 v[164:167], v[204:207], v[40:43], v[164:167]\n" \
"v_mov_b32 v64, v66\n" \
"v_cvt_pk_bf16_f32 v57, v58, v59\n" \
"v_exp_f32 v61, v61\n" \
"v_mfma_f32_16x16x32_bf16 v[168:171], v[208:211], v[8:11], v[168:171]\n" \
"v_exp_f32 v62, v62\n" \
"v_mfma_f32_16x16x32_bf16 v[168:171], v[212:215], v[40:43], v[168:171]\n" \
"s_waitcnt vmcnt(0) lgkmcnt(0)\n" \
"v_cvt_pk_bf16_f32 v58, v60, v61\n" \
"v_exp_f32 v63, v63\n" \
"v_permlane16_swap_b32 v64, v66\n" \
"v_add_f32 v66, v66, v64\n" \
"v_mov_b32 v64, v66\n" \
"v_mfma_f32_16x16x32_bf16 v[172:175], v[216:219], v[8:11], v[172:175]\n" \
"v_exp_f32 v16, v16\n" \
"v_mfma_f32_16x16x32_bf16 v[172:175], v[220:223], v[40:43], v[172:175]\n" \
"s_barrier\n" \
"v_cvt_pk_bf16_f32 v59, v62, v63\n" \
"v_exp_f32 v17, v17\n" \
"v_permlane32_swap_b32 v64, v66\n" \
"v_add_f32 v66, v66, v64\n" \
"v_rcp_f32 v64, v66\n" \
"v_mul_f32 v66, 0x4d800000, v66\n" \
"v_add_u32 v65, 0x1c90080, v249\n" \
"v_cndmask_b32 v65, v249, v65, s[16:17]\n" \
"v_mfma_f32_16x16x32_bf16 v[176:179], v[224:227], v[8:11], v[176:179]\n" \
"s_add_u32 m0, s84, 90112\n" \
"v_exp_f32 v18, v18\n" \
"v_mfma_f32_16x16x32_bf16 v[176:179], v[228:231], v[40:43], v[176:179]\n" \
"global_load_lds_dwordx4 v253, s[48:49] offset:-4096\n" \
"global_load_lds_dwordx4 v253, s[48:49] offset:-2048\n" \
"v_cvt_pk_bf16_f32 v16, v16, v17\n" \
"v_mfma_f32_16x16x32_bf16 v[242:245], a[124:127], v[24:27], v[242:245]\n" \
"v_exp_f32 v19, v19\n" \
"v_mfma_f32_16x16x32_bf16 v[242:245], a[124:127], v[56:59], v[242:245]\n" \
"v_exp_f32 v20, v20\n" \
"v_cvt_pk_bf16_f32 v17, v18, v19\n" \
"v_mfma_f32_16x16x32_bf16 v[180:183], v[232:235], v[8:11], v[180:183]\n" \
"s_add_u32 s77, s77, 532480\n" \
"v_exp_f32 v21, v21\n" \
"v_mfma_f32_16x16x32_bf16 v[180:183], v[236:239], v[40:43], v[180:183]\n" \
"v_accvgpr_read_b32 v96, a32\n" \
"v_accvgpr_read_b32 v97, a33\n" \
"v_accvgpr_read_b32 v98, a34\n" \
"v_accvgpr_read_b32 v99, a35\n" \
"v_accvgpr_read_b32 v100, a36\n" \
"v_accvgpr_read_b32 v101, a37\n" \
"v_accvgpr_read_b32 v102, a38\n" \
"v_accvgpr_read_b32 v103, a39\n" \
"v_exp_f32 v22, v22\n" \
"v_pk_mul_f32 v[96:97], v[96:97], v[64:65] op_sel_hi:[1,0]\n" \
"v_pk_mul_f32 v[98:99], v[98:99], v[64:65] op_sel_hi:[1,0]\n" \
"v_pk_mul_f32 v[100:101], v[100:101], v[64:65] op_sel_hi:[1,0]\n" \
"v_pk_mul_f32 v[102:103], v[102:103], v[64:65] op_sel_hi:[1,0]\n" \
"v_cvt_pk_bf16_f32 v104, v96, v97\n" \
"v_cvt_pk_bf16_f32 v105, v98, v99\n" \
"v_cvt_pk_bf16_f32 v106, v100, v101\n" \
"v_cvt_pk_bf16_f32 v107, v102, v103\n" \
"v_pk_mul_f32 v[96:97], v[96:97], v[98:99]\n" \
"v_pk_fma_f32 v[96:97], v[100:101], v[102:103], v[96:97]\n" \
"v_fma_f32 v66, v96, v97, v66\n" \
"v_mfma_f32_16x16x32_bf16 v[184:187], v[0:3], v[8:11], v[184:187]\n" \
"global_load_lds_dwordx4 v253, s[48:49] offset:0\n" \
"v_cvt_pk_bf16_f32 v18, v20, v21\n" \
"v_mfma_f32_16x16x32_bf16 v[184:187], v[4:7], v[40:43], v[184:187]\n" \
"s_add_u32 s48, s48, 0x300000\n" \
"s_addc_u32 s49, s49, 0\n" \
"v_exp_f32 v23, v23\n" \
"v_exp_f32 v48, v48\n" \
"v_mfma_f32_16x16x32_bf16 v[188:191], v[32:35], v[8:11], v[188:191]\n" \
"global_load_lds_dwordx4 v253, s[50:51] offset:-3072\n" \
"v_cvt_pk_bf16_f32 v19, v22, v23\n" \
"v_mfma_f32_16x16x32_bf16 v[188:191], v[36:39], v[40:43], v[188:191]\n" \
"buffer_store_dwordx4 v[104:107], v65, s[56:59], s77 offen offset:0 sc1\n" \
"v_accvgpr_read_b32 v96, a40\n" \
"v_accvgpr_read_b32 v97, a41\n" \
"v_accvgpr_read_b32 v98, a42\n" \
"v_accvgpr_read_b32 v99, a43\n" \
"v_accvgpr_read_b32 v100, a44\n" \
"v_accvgpr_read_b32 v101, a45\n" \
"v_accvgpr_read_b32 v102, a46\n" \
"v_accvgpr_read_b32 v103, a47\n" \
"v_exp_f32 v49, v49\n" \
"v_pk_mul_f32 v[96:97], v[96:97], v[64:65] op_sel_hi:[1,0]\n" \
"v_pk_mul_f32 v[98:99], v[98:99], v[64:65] op_sel_hi:[1,0]\n" \
"v_pk_mul_f32 v[100:101], v[100:101], v[64:65] op_sel_hi:[1,0]\n" \
"v_pk_mul_f32 v[102:103], v[102:103], v[64:65] op_sel_hi:[1,0]\n" \
"v_cvt_pk_bf16_f32 v108, v96, v97\n" \
"v_cvt_pk_bf16_f32 v109, v98, v99\n" \
"v_cvt_pk_bf16_f32 v110, v100, v101\n" \
"v_cvt_pk_bf16_f32 v111, v102, v103\n" \
"v_pk_mul_f32 v[96:97], v[96:97], v[98:99]\n" \
"v_pk_fma_f32 v[96:97], v[100:101], v[102:103], v[96:97]\n" \
"v_mfma_f32_16x16x32_bf16 v[160:163], a[192:195], v[24:27], v[160:163]\n" \
"global_load_lds_dwordx4 v253, s[50:51] offset:-1024\n" \
"global_load_lds_dwordx4 v253, s[50:51] offset:1024\n" \
"v_mfma_f32_16x16x32_bf16 v[160:163], a[196:199], v[56:59], v[160:163]\n" \
"s_movk_i32 s89, 0xc0\n" \
"v_exp_f32 v50, v50\n" \
"v_cvt_pk_bf16_f32 v48, v48, v49\n" \
"v_mfma_f32_16x16x32_bf16 v[242:245], a[120:123], v[16:19], v[242:245]\n" \
"s_movk_i32 s90, 288\n" \
"v_exp_f32 v51, v51\n" \
"v_lshrrev_b32 v211, 4, v248\n" \
"v_mfma_f32_16x16x32_bf16 v[164:167], a[200:203], v[24:27], v[164:167]\n" \
"s_add_u32 s50, s50, 0x300000\n" \
"s_addc_u32 s51, s51, 0\n" \
"v_lshrrev_b32 v214, 8, v248\n" \
"v_mfma_f32_16x16x32_bf16 v[164:167], a[204:207], v[56:59], v[164:167]\n" \
"s_add_u32 m0, s95, 65536\n" \
"v_and_b32 v215, 7, v211\n" \
"v_xor_b32 v214, v214, v215\n" \
"v_exp_f32 v52, v52\n" \
"v_cvt_pk_bf16_f32 v49, v50, v51\n" \
"v_exp_f32 v53, v53\n" \
"v_exp_f32 v54, v54\n" \
"v_cvt_pk_bf16_f32 v50, v52, v53\n" \
"v_exp_f32 v55, v55\n" \
"v_mfma_f32_16x16x32_bf16 v[168:171], a[208:211], v[24:27], v[168:171]\n" \
"global_load_lds_dwordx4 v252, s[42:43] offset:0\n" \
"global_load_lds_dwordx4 v252, s[42:43] offset:2048\n" \
"v_mfma_f32_16x16x32_bf16 v[168:171], a[212:215], v[56:59], v[168:171]\n" \
"s_lshr_b32 s71, s61, 1\n" \
"v_cvt_pk_bf16_f32 v51, v54, v55\n" \
"s_lshl_b32 s70, s71, 11\n" \
"v_mfma_f32_16x16x32_bf16 v[172:175], a[216:219], v[24:27], v[172:175]\n" \
"s_add_u32 s70, s70, 118784\n" \
"v_and_b32 v216, 15, v211\n" \
"v_lshlrev_b32 v213, 7, v216\n" \
"v_mfma_f32_16x16x32_bf16 v[172:175], a[220:223], v[56:59], v[172:175]\n" \
"v_fma_f32 v66, v96, v97, v66\n" \
"v_lshl_add_u32 v213, v214, 4, v213\n" \
"v_mfma_f32_16x16x32_bf16 v[242:245], a[120:123], v[48:51], v[242:245]\n" \
"s_add_u32 s42, s42, 0x400\n" \
"s_addc_u32 s43, s43, 0\n" \
"v_add_u32 v205, s70, v213\n" \
"v_mfma_f32_16x16x32_bf16 v[176:179], a[224:227], v[24:27], v[176:179]\n" \
"global_load_lds_dwordx4 v252, s[44:45] offset:1024\n" \
"global_load_lds_dwordx4 v252, s[44:45] offset:3072\n" \
"v_mfma_f32_16x16x32_bf16 v[176:179], a[228:231], v[56:59], v[176:179]\n" \
"v_xor_b32 v206, 64, v205\n" \
"v_lshrrev_b32 v216, 2, v216\n" \
"v_mfma_f32_16x16x32_bf16 v[180:183], a[232:235], v[24:27], v[180:183]\n" \
"s_add_u32 s44, s44, 0x400\n" \
"s_addc_u32 s45, s45, 0\n" \
"v_lshl_add_u32 v216, v216, 9, v213\n" \
"v_mfma_f32_16x16x32_bf16 v[180:183], a[236:239], v[56:59], v[180:183]\n" \
"s_add_u32 m0, s85, 81920\n" \
"v_add_u32 v207, 135168, v216\n" \
"v_xor_b32 v208, 64, v207\n" \
"v_mfma_f32_16x16x32_bf16 v[184:187], a[240:243], v[24:27], v[184:187]\n" \
"s_and_b32 s70, s61, 1\n" \
"s_lshl_b32 s70, s70, 9\n" \
"s_add_u32 s70, s70, 139264\n" \
"v_mfma_f32_16x16x32_bf16 v[184:187], a[244:247], v[56:59], v[184:187]\n" \
"s_lshl_b32 s71, s71, 5\n" \
"v_and_b32 v213, 3, v211\n" \
"v_lshlrev_b32 v213, 3, v213\n" \
"v_and_or_b32 v213, v248, s89, v213\n" \
"v_bfe_u32 v214, v211, 4, 1\n" \
"v_mfma_f32_16x16x32_bf16 v[188:191], a[248:251], v[24:27], v[188:191]\n" \
"buffer_store_dwordx4 v[108:111], v65, s[56:59], s77 offen offset:2048 sc1\n" \
"v_mad_u32_u24 v213, v214, s90, v213\n" \
"v_lshrrev_b32 v214, 5, v211\n" \
"v_mul_u32_u24 v214, 6144, v214\n" \
"v_add3_u32 v213, v213, v214, s70\n" \
"v_xor_b32 v209, s71, v213\n" \
"v_xor_b32 v210, 32, v209\n" \
"v_mfma_f32_16x16x32_bf16 v[188:191], a[252:255], v[56:59], v[188:191]\n" \
"s_add_u32 s77, s77, 0x1000\n" \
"ds_read_b128 a[64:67], v207 offset:0\n" \
"ds_read_b64_tr_b16 a[96:97], v209 offset:2048\n" \
"v_accvgpr_read_b32 v96, a48\n" \
"v_accvgpr_read_b32 v97, a49\n" \
"v_accvgpr_read_b32 v98, a50\n" \
"v_accvgpr_read_b32 v99, a51\n" \
"v_accvgpr_read_b32 v100, a52\n" \
"v_accvgpr_read_b32 v101, a53\n" \
"v_accvgpr_read_b32 v102, a54\n" \
"v_accvgpr_read_b32 v103, a55\n" \
"v_pk_mul_f32 v[96:97], v[96:97], v[64:65] op_sel_hi:[1,0]\n" \
"v_pk_mul_f32 v[98:99], v[98:99], v[64:65] op_sel_hi:[1,0]\n" \
"v_pk_mul_f32 v[100:101], v[100:101], v[64:65] op_sel_hi:[1,0]\n" \
"v_pk_mul_f32 v[102:103], v[102:103], v[64:65] op_sel_hi:[1,0]\n" \
"v_cvt_pk_bf16_f32 v104, v96, v97\n" \
"v_cvt_pk_bf16_f32 v105, v98, v99\n" \
"v_cvt_pk_bf16_f32 v106, v100, v101\n" \
"v_cvt_pk_bf16_f32 v107, v102, v103\n" \
"v_pk_mul_f32 v[96:97], v[96:97], v[98:99]\n" \
"v_pk_fma_f32 v[96:97], v[100:101], v[102:103], v[96:97]\n" \
"v_mfma_f32_16x16x32_bf16 v[128:131], a[192:195], v[16:19], v[128:131]\n" \
"ds_read_b64_tr_b16 a[98:99], v209 offset:3072\n" \
"v_fma_f32 v66, v96, v97, v66\n" \
"v_mfma_f32_16x16x32_bf16 v[128:131], a[196:199], v[48:51], v[128:131]\n" \
"buffer_store_dwordx4 v[104:107], v65, s[56:59], s77 offen offset:0 sc1\n" \
"ds_read_b128 a[68:71], v208 offset:0\n" \
"v_accvgpr_read_b32 v96, a56\n" \
"v_accvgpr_read_b32 v97, a57\n" \
"v_accvgpr_read_b32 v98, a58\n" \
"v_accvgpr_read_b32 v99, a59\n" \
"v_accvgpr_read_b32 v100, a60\n" \
"v_accvgpr_read_b32 v101, a61\n" \
"v_accvgpr_read_b32 v102, a62\n" \
"v_accvgpr_read_b32 v103, a63\n" \
"v_pk_mul_f32 v[96:97], v[96:97], v[64:65] op_sel_hi:[1,0]\n" \
"v_pk_mul_f32 v[98:99], v[98:99], v[64:65] op_sel_hi:[1,0]\n" \
"v_pk_mul_f32 v[100:101], v[100:101], v[64:65] op_sel_hi:[1,0]\n" \
"v_pk_mul_f32 v[102:103], v[102:103], v[64:65] op_sel_hi:[1,0]\n" \
"v_cvt_pk_bf16_f32 v108, v96, v97\n" \
"v_cvt_pk_bf16_f32 v109, v98, v99\n" \
"v_cvt_pk_bf16_f32 v110, v100, v101\n" \
"v_cvt_pk_bf16_f32 v111, v102, v103\n" \
"v_pk_mul_f32 v[96:97], v[96:97], v[98:99]\n" \
"v_pk_fma_f32 v[96:97], v[100:101], v[102:103], v[96:97]\n" \
"v_fma_f32 v66, v96, v97, v66\n" \
"v_mfma_f32_16x16x32_bf16 v[132:135], a[200:203], v[16:19], v[132:135]\n" \
"buffer_store_dwordx4 v[108:111], v65, s[56:59], s77 offen offset:2048 sc1\n" \
"ds_read_b64_tr_b16 a[100:101], v209 offset:14336\n" \
"buffer_load_dwordx4 v246, s[12:15], s46 offen lds\n" \
"ds_read_b64_tr_b16 a[102:103], v209 offset:15360\n" \
"v_mfma_f32_16x16x32_bf16 v[132:135], a[204:207], v[48:51], v[132:135]\n" \
"s_add_u32 s46, s46, 0x400\n" \
"ds_read_b128 a[72:75], v205 offset:0\n" \
"v_cmp_class_f32 s[54:55], v66, s94\n" \
"v_mfma_f32_16x16x32_bf16 v[136:139], a[208:211], v[16:19], v[136:139]\n" \
"s_add_u32 m0, s95, 0\n" \
"s_sub_u32 s77, s77, 536576\n" \
"ds_read_b64_tr_b16 a[104:105], v209 offset:0\n" \
"v_mfma_f32_16x16x32_bf16 v[136:139], a[212:215], v[48:51], v[136:139]\n" \
"s_or_b64 s[92:93], s[92:93], s[54:55]\n" \
"ds_read_b128 a[76:79], v206 offset:0\n" \
"ds_read_b64_tr_b16 a[106:107], v209 offset:1024\n" \
"v_mfma_f32_16x16x32_bf16 v[140:143], a[216:219], v[16:19], v[140:143]\n" \
"ds_read_b64_tr_b16 a[108:109], v209 offset:12288\n" \
"global_load_lds_dwordx4 v252, s[42:43] offset:0\n" \
"v_mfma_f32_16x16x32_bf16 v[140:143], a[220:223], v[48:51], v[140:143]\n" \
"ds_read_b64_tr_b16 a[110:111], v209 offset:13312\n" \
"global_load_lds_dwordx4 v252, s[42:43] offset:2048\n" \
"v_mfma_f32_16x16x32_bf16 v[144:147], a[224:227], v[16:19], v[144:147]\n" \
"ds_read_b128 a[80:83], v205 offset:4096\n" \
"ds_read_b64_tr_b16 a[112:113], v210 offset:0\n" \
"v_mfma_f32_16x16x32_bf16 v[144:147], a[228:231], v[48:51], v[144:147]\n" \
"s_add_u32 s42, s42, 0xfffffb00\n" \
"s_addc_u32 s43, s43, -1\n" \
"ds_read_b64_tr_b16 a[114:115], v210 offset:1024\n" \
"v_mfma_f32_16x16x32_bf16 v[148:151], a[232:235], v[16:19], v[148:151]\n" \
"ds_read_b64_tr_b16 a[116:117], v210 offset:12288\n" \
"global_load_lds_dwordx4 v252, s[44:45] offset:1024\n" \
"v_mfma_f32_16x16x32_bf16 v[148:151], a[236:239], v[48:51], v[148:151]\n" \
"ds_read_b128 a[84:87], v206 offset:4096\n" \
"ds_read_b64_tr_b16 a[118:119], v210 offset:13312\n" \
"v_mfma_f32_16x16x32_bf16 v[152:155], a[240:243], v[16:19], v[152:155]\n" \
"ds_read_b128 a[88:91], v205 offset:8192\n" \
"global_load_lds_dwordx4 v252, s[44:45] offset:3072\n" \
"v_mfma_f32_16x16x32_bf16 v[152:155], a[244:247], v[48:51], v[152:155]\n" \
"s_add_u32 s44, s44, 0xfffffb00\n" \
"s_addc_u32 s45, s45, -1\n" \
"ds_read_b128 a[92:95], v206 offset:8192\n" \
"v_mfma_f32_16x16x32_bf16 v[156:159], a[248:251], v[16:19], v[156:159]\n" \
"s_add_u32 m0, s85, 16384\n" \
"v_mfma_f32_16x16x32_bf16 v[156:159], a[252:255], v[48:51], v[156:159]\n" \
"s_waitcnt lgkmcnt(4)\n" \
"buffer_load_dwordx4 v246, s[12:15], s46 offen lds\n" \
"s_branch Ljoin_%=\n" \
"Lexact_%=:\n" \
"v_mov_b32 v190, 0xff800000\n" \
"v_add_u32 v191, 0x1c90080, v249\n" \
"v_cndmask_b32 v191, v249, v191, s[16:17]\n" \
"s_and_b32 s80, s61, 2\n" \
"s_lshl_b32 s80, s80, 4\n" \
"v_subrev_u32 v186, s80, v251\n" \
"v_accvgpr_read_b32 v160, a128\n" \
"v_accvgpr_read_b32 v161, a129\n" \
"v_accvgpr_read_b32 v162, a130\n" \
"v_accvgpr_read_b32 v163, a131\n" \
"v_accvgpr_read_b32 v164, a132\n" \
"v_accvgpr_read_b32 v165, a133\n" \
"v_accvgpr_read_b32 v166, a134\n" \
"v_accvgpr_read_b32 v167, a135\n" \
"v_accvgpr_read_b32 v168, a136\n" \
"v_accvgpr_read_b32 v169, a137\n" \
"v_accvgpr_read_b32 v170, a138\n" \
"v_accvgpr_read_b32 v171, a139\n" \
"v_accvgpr_read_b32 v172, a140\n" \
"v_accvgpr_read_b32 v173, a141\n" \
"v_accvgpr_read_b32 v174, a142\n" \
"v_accvgpr_read_b32 v175, a143\n" \
"s_lshl_b32 s75, s63, 2\n" \
"s_lshr_b32 s80, s61, 1\n" \
"s_add_u32 s75, s75, s80\n" \
"s_add_u32 s75, s75, 0\n" \
"s_add_u32 s76, s77, 0\n" \
"s_call_b64 s[68:69], Lex1_%=\n" \
"v_accvgpr_read_b32 v160, a144\n" \
"v_accvgpr_read_b32 v161, a145\n" \
"v_accvgpr_read_b32 v162, a146\n" \
"v_accvgpr_read_b32 v163, a147\n" \
"v_accvgpr_read_b32 v164, a148\n" \
"v_accvgpr_read_b32 v165, a149\n" \
"v_accvgpr_read_b32 v166, a150\n" \
"v_accvgpr_read_b32 v167, a151\n" \
"v_accvgpr_read_b32 v168, a152\n" \
"v_accvgpr_read_b32 v169, a153\n" \
"v_accvgpr_read_b32 v170, a154\n" \
"v_accvgpr_read_b32 v171, a155\n" \
"v_accvgpr_read_b32 v172, a156\n" \
"v_accvgpr_read_b32 v173, a157\n" \
"v_accvgpr_read_b32 v174, a158\n" \
"v_accvgpr_read_b32 v175, a159\n" \
"s_lshl_b32 s75, s63, 2\n" \
"s_lshr_b32 s80, s61, 1\n" \
"s_add_u32 s75, s75, s80\n" \
"s_add_u32 s75, s75, 2\n" \
"s_add_u32 s76, s77, 532480\n" \
"s_call_b64 s[68:69], Lex1_%=\n" \
"v_accvgpr_read_b32 v160, a160\n" \
"v_accvgpr_read_b32 v161, a161\n" \
"v_accvgpr_read_b32 v162, a162\n" \
"v_accvgpr_read_b32 v163, a163\n" \
"v_accvgpr_read_b32 v164, a164\n" \
"v_accvgpr_read_b32 v165, a165\n" \
"v_accvgpr_read_b32 v166, a166\n" \
"v_accvgpr_read_b32 v167, a167\n" \
"v_accvgpr_read_b32 v168, a168\n" \
"v_accvgpr_read_b32 v169, a169\n" \
"v_accvgpr_read_b32 v170, a170\n" \
"v_accvgpr_read_b32 v171, a171\n" \
"v_accvgpr_read_b32 v172, a172\n" \
"v_accvgpr_read_b32 v173, a173\n" \
"v_accvgpr_read_b32 v174, a174\n" \
"v_accvgpr_read_b32 v175, a175\n" \
"s_lshl_b32 s75, s63, 2\n" \
"s_sub_u32 s75, 60, s75\n" \
"s_lshr_b32 s80, s61, 1\n" \
"s_add_u32 s75, s75, s80\n" \
"s_add_u32 s75, s75, 0\n" \
"s_add_u32 s76, s78, 0\n" \
"s_call_b64 s[68:69], Lex1_%=\n" \
"v_accvgpr_read_b32 v160, a176\n" \
"v_accvgpr_read_b32 v161, a177\n" \
"v_accvgpr_read_b32 v162, a178\n" \
"v_accvgpr_read_b32 v163, a179\n" \
"v_accvgpr_read_b32 v164, a180\n" \
"v_accvgpr_read_b32 v165, a181\n" \
"v_accvgpr_read_b32 v166, a182\n" \
"v_accvgpr_read_b32 v167, a183\n" \
"v_accvgpr_read_b32 v168, a184\n" \
"v_accvgpr_read_b32 v169, a185\n" \
"v_accvgpr_read_b32 v170, a186\n" \
"v_accvgpr_read_b32 v171, a187\n" \
"v_accvgpr_read_b32 v172, a188\n" \
"v_accvgpr_read_b32 v173, a189\n" \
"v_accvgpr_read_b32 v174, a190\n" \
"v_accvgpr_read_b32 v175, a191\n" \
"s_lshl_b32 s75, s63, 2\n" \
"s_sub_u32 s75, 60, s75\n" \
"s_lshr_b32 s80, s61, 1\n" \
"s_add_u32 s75, s75, s80\n" \
"s_add_u32 s75, s75, 2\n" \
"s_add_u32 s76, s78, 532480\n" \
"s_call_b64 s[68:69], Lex1_%=\n" \
"s_branch Lexr_%=\n" \
"Lex1_%=:\n" \
"v_mov_b32 v128, 0\n" \
"v_mov_b32 v129, 0\n" \
"v_mov_b32 v130, 0\n" \
"v_mov_b32 v131, 0\n" \
"v_mov_b32 v132, 0\n" \
"v_mov_b32 v133, 0\n" \
"v_mov_b32 v134, 0\n" \
"v_mov_b32 v135, 0\n" \
"v_mov_b32 v136, 0\n" \
"v_mov_b32 v137, 0\n" \
"v_mov_b32 v138, 0\n" \
"v_mov_b32 v139, 0\n" \
"v_mov_b32 v140, 0\n" \
"v_mov_b32 v141, 0\n" \
"v_mov_b32 v142, 0\n" \
"v_mov_b32 v143, 0\n" \
"v_mov_b32 v144, 0\n" \
"v_mov_b32 v145, 0\n" \
"v_mov_b32 v146, 0\n" \
"v_mov_b32 v147, 0\n" \
"v_mov_b32 v148, 0\n" \
"v_mov_b32 v149, 0\n" \
"v_mov_b32 v150, 0\n" \
"v_mov_b32 v151, 0\n" \
"v_mov_b32 v152, 0\n" \
"v_mov_b32 v153, 0\n" \
"v_mov_b32 v154, 0\n" \
"v_mov_b32 v155, 0\n" \
"v_mov_b32 v156, 0\n" \
"v_mov_b32 v157, 0\n" \
"v_mov_b32 v158, 0\n" \
"v_mov_b32 v159, 0\n" \
"v_mov_b32 v184, 0xf149f2ca\n" \
"v_mov_b32 v185, 0\n" \
"s_mov_b32 s73, 0\n" \
"s_mul_i32 s74, s62, 557824\n" \
"Lex2_%=:\n" \
"s_add_u32 s80, s74, 4096\n" \
"buffer_load_dwordx4 a[0:3], v249, s[56:59], s74 offen offset:0 sc1\n" \
"buffer_load_dwordx4 a[4:7], v249, s[56:59], s74 offen offset:2048 sc1\n" \
"buffer_load_dwordx4 a[8:11], v249, s[56:59], s80 offen offset:0 sc1\n" \
"buffer_load_dwordx4 a[12:15], v249, s[56:59], s80 offen offset:2048 sc1\n" \
"buffer_load_dwordx4 a[16:19], v249, s[56:59], s74 offen offset:256 sc1\n" \
"buffer_load_dwordx4 a[20:23], v249, s[56:59], s74 offen offset:2304 sc1\n" \
"buffer_load_dwordx4 a[24:27], v249, s[56:59], s80 offen offset:256 sc1\n" \
"buffer_load_dwordx4 a[28:31], v249, s[56:59], s80 offen offset:2304 sc1\n" \
"s_add_u32 s80, s74, 17850368\n" \
"s_add_u32 s81, s80, 4096\n" \
"buffer_load_dwordx4 a[32:35], v248, s[56:59], s80 offen offset:0 sc1\n" \
"buffer_load_dwordx4 a[36:39], v248, s[56:59], s80 offen offset:1024 sc1\n" \
"buffer_load_dwordx4 a[40:43], v248, s[56:59], s80 offen offset:2048 sc1\n" \
"buffer_load_dwordx4 a[44:47], v248, s[56:59], s80 offen offset:3072 sc1\n" \
"buffer_load_dwordx4 a[48:51], v248, s[56:59], s81 offen offset:0 sc1\n" \
"buffer_load_dwordx4 a[52:55], v248, s[56:59], s81 offen offset:1024 sc1\n" \
"buffer_load_dwordx4 a[56:59], v248, s[56:59], s81 offen offset:2048 sc1\n" \
"buffer_load_dwordx4 a[60:63], v248, s[56:59], s81 offen offset:3072 sc1\n" \
"s_waitcnt vmcnt(0)\n" \
"v_mfma_f32_16x16x32_bf16 v[176:179], a[0:3], v[160:163], 0\n" \
"v_mfma_f32_16x16x32_bf16 v[176:179], a[4:7], v[164:167], v[176:179]\n" \
"v_mfma_f32_16x16x32_bf16 v[176:179], a[8:11], v[168:171], v[176:179]\n" \
"v_mfma_f32_16x16x32_bf16 v[176:179], a[12:15], v[172:175], v[176:179]\n" \
"v_mfma_f32_16x16x32_bf16 v[180:183], a[16:19], v[160:163], 0\n" \
"v_mfma_f32_16x16x32_bf16 v[180:183], a[20:23], v[164:167], v[180:183]\n" \
"v_mfma_f32_16x16x32_bf16 v[180:183], a[24:27], v[168:171], v[180:183]\n" \
"v_mfma_f32_16x16x32_bf16 v[180:183], a[28:31], v[172:175], v[180:183]\n" \
"s_nop 7\n" \
"s_nop 1\n" \
"s_cmp_eq_u32 s73, s75\n" \
"s_cbranch_scc0 Lex3_%=\n" \
"v_cmp_gt_i32 vcc, 0, v186\n" \
"s_nop 1\n" \
"v_cndmask_b32 v176, v176, v190, vcc\n" \
"v_cmp_gt_i32 vcc, 1, v186\n" \
"s_nop 1\n" \
"v_cndmask_b32 v177, v177, v190, vcc\n" \
"v_cmp_gt_i32 vcc, 2, v186\n" \
"s_nop 1\n" \
"v_cndmask_b32 v178, v178, v190, vcc\n" \
"v_cmp_gt_i32 vcc, 3, v186\n" \
"s_nop 1\n" \
"v_cndmask_b32 v179, v179, v190, vcc\n" \
"v_cmp_gt_i32 vcc, 16, v186\n" \
"s_nop 1\n" \
"v_cndmask_b32 v180, v180, v190, vcc\n" \
"v_cmp_gt_i32 vcc, 17, v186\n" \
"s_nop 1\n" \
"v_cndmask_b32 v181, v181, v190, vcc\n" \
"v_cmp_gt_i32 vcc, 18, v186\n" \
"s_nop 1\n" \
"v_cndmask_b32 v182, v182, v190, vcc\n" \
"v_cmp_gt_i32 vcc, 19, v186\n" \
"s_nop 1\n" \
"v_cndmask_b32 v183, v183, v190, vcc\n" \
"Lex3_%=:\n" \
"v_max3_f32 v187, v176, v177, v178\n" \
"v_max3_f32 v187, v187, v179, v180\n" \
"v_max3_f32 v187, v187, v181, v182\n" \
"v_max3_f32 v187, v187, v183, v184\n" \
"v_mov_b32 v188, v187\n" \
"s_nop 1\n" \
"v_permlane16_swap_b32 v188, v187\n" \
"v_max_f32 v187, v187, v188\n" \
"v_mov_b32 v188, v187\n" \
"s_nop 1\n" \
"v_permlane32_swap_b32 v188, v187\n" \
"v_max_f32 v187, v187, v188\n" \
"v_sub_f32 v189, v184, v187\n" \
"v_exp_f32 v189, v189\n" \
"v_mov_b32 v184, v187\n" \
"v_sub_f32 v176, v176, v187\n" \
"v_sub_f32 v177, v177, v187\n" \
"v_sub_f32 v178, v178, v187\n" \
"v_sub_f32 v179, v179, v187\n" \
"v_sub_f32 v180, v180, v187\n" \
"v_sub_f32 v181, v181, v187\n" \
"v_sub_f32 v182, v182, v187\n" \
"v_sub_f32 v183, v183, v187\n" \
"v_exp_f32 v176, v176\n" \
"v_exp_f32 v177, v177\n" \
"v_exp_f32 v178, v178\n" \
"v_exp_f32 v179, v179\n" \
"v_exp_f32 v180, v180\n" \
"v_exp_f32 v181, v181\n" \
"v_exp_f32 v182, v182\n" \
"v_exp_f32 v183, v183\n" \
"v_mul_f32 v185, v185, v189\n" \
"v_add_f32 v185, v185, v176\n" \
"v_add_f32 v185, v185, v177\n" \
"v_add_f32 v185, v185, v178\n" \
"v_add_f32 v185, v185, v179\n" \
"v_add_f32 v185, v185, v180\n" \
"v_add_f32 v185, v185, v181\n" \
"v_add_f32 v185, v185, v182\n" \
"v_add_f32 v185, v185, v183\n" \
"v_cvt_pk_bf16_f32 v112, v176, v177\n" \
"v_cvt_pk_bf16_f32 v113, v178, v179\n" \
"v_cvt_pk_bf16_f32 v114, v180, v181\n" \
"v_cvt_pk_bf16_f32 v115, v182, v183\n" \
"v_mul_f32 v128, v128, v189\n" \
"v_mul_f32 v129, v129, v189\n" \
"v_mul_f32 v130, v130, v189\n" \
"v_mul_f32 v131, v131, v189\n" \
"v_mul_f32 v132, v132, v189\n" \
"v_mul_f32 v133, v133, v189\n" \
"v_mul_f32 v134, v134, v189\n" \
"v_mul_f32 v135, v135, v189\n" \
"v_mul_f32 v136, v136, v189\n" \
"v_mul_f32 v137, v137, v189\n" \
"v_mul_f32 v138, v138, v189\n" \
"v_mul_f32 v139, v139, v189\n" \
"v_mul_f32 v140, v140, v189\n" \
"v_mul_f32 v141, v141, v189\n" \
"v_mul_f32 v142, v142, v189\n" \
"v_mul_f32 v143, v143, v189\n" \
"v_mul_f32 v144, v144, v189\n" \
"v_mul_f32 v145, v145, v189\n" \
"v_mul_f32 v146, v146, v189\n" \
"v_mul_f32 v147, v147, v189\n" \
"v_mul_f32 v148, v148, v189\n" \
"v_mul_f32 v149, v149, v189\n" \
"v_mul_f32 v150, v150, v189\n" \
"v_mul_f32 v151, v151, v189\n" \
"v_mul_f32 v152, v152, v189\n" \
"v_mul_f32 v153, v153, v189\n" \
"v_mul_f32 v154, v154, v189\n" \
"v_mul_f32 v155, v155, v189\n" \
"v_mul_f32 v156, v156, v189\n" \
"v_mul_f32 v157, v157, v189\n" \
"v_mul_f32 v158, v158, v189\n" \
"v_mul_f32 v159, v159, v189\n" \
"s_nop 1\n" \
"v_mfma_f32_16x16x32_bf16 v[128:131], a[32:35], v[112:115], v[128:131]\n" \
"v_mfma_f32_16x16x32_bf16 v[132:135], a[36:39], v[112:115], v[132:135]\n" \
"v_mfma_f32_16x16x32_bf16 v[136:139], a[40:43], v[112:115], v[136:139]\n" \
"v_mfma_f32_16x16x32_bf16 v[140:143], a[44:47], v[112:115], v[140:143]\n" \
"v_mfma_f32_16x16x32_bf16 v[144:147], a[48:51], v[112:115], v[144:147]\n" \
"v_mfma_f32_16x16x32_bf16 v[148:151], a[52:55], v[112:115], v[148:151]\n" \
"v_mfma_f32_16x16x32_bf16 v[152:155], a[56:59], v[112:115], v[152:155]\n" \
"v_mfma_f32_16x16x32_bf16 v[156:159], a[60:63], v[112:115], v[156:159]\n" \
"s_nop 7\n" \
"s_nop 1\n" \
"s_and_b32 s80, s73, 1\n" \
"s_mul_i32 s80, s80, 1024\n" \
"s_add_u32 s74, s74, s80\n" \
"s_add_u32 s74, s74, 8192\n" \
"s_add_u32 s73, s73, 1\n" \
"s_cmp_le_u32 s73, s75\n" \
"s_cbranch_scc1 Lex2_%=\n" \
"v_mov_b32 v187, v185\n" \
"s_nop 1\n" \
"v_permlane16_swap_b32 v187, v185\n" \
"v_add_f32 v185, v185, v187\n" \
"v_mov_b32 v187, v185\n" \
"s_nop 1\n" \
"v_permlane32_swap_b32 v187, v185\n" \
"v_add_f32 v185, v185, v187\n" \
"v_rcp_f32 v185, v185\n" \
"s_nop 0\n" \
"v_mul_f32 v128, v128, v185\n" \
"v_mul_f32 v129, v129, v185\n" \
"v_mul_f32 v130, v130, v185\n" \
"v_mul_f32 v131, v131, v185\n" \
"v_mul_f32 v132, v132, v185\n" \
"v_mul_f32 v133, v133, v185\n" \
"v_mul_f32 v134, v134, v185\n" \
"v_mul_f32 v135, v135, v185\n" \
"v_mul_f32 v136, v136, v185\n" \
"v_mul_f32 v137, v137, v185\n" \
"v_mul_f32 v138, v138, v185\n" \
"v_mul_f32 v139, v139, v185\n" \
"v_mul_f32 v140, v140, v185\n" \
"v_mul_f32 v141, v141, v185\n" \
"v_mul_f32 v142, v142, v185\n" \
"v_mul_f32 v143, v143, v185\n" \
"v_mul_f32 v144, v144, v185\n" \
"v_mul_f32 v145, v145, v185\n" \
"v_mul_f32 v146, v146, v185\n" \
"v_mul_f32 v147, v147, v185\n" \
"v_mul_f32 v148, v148, v185\n" \
"v_mul_f32 v149, v149, v185\n" \
"v_mul_f32 v150, v150, v185\n" \
"v_mul_f32 v151, v151, v185\n" \
"v_mul_f32 v152, v152, v185\n" \
"v_mul_f32 v153, v153, v185\n" \
"v_mul_f32 v154, v154, v185\n" \
"v_mul_f32 v155, v155, v185\n" \
"v_mul_f32 v156, v156, v185\n" \
"v_mul_f32 v157, v157, v185\n" \
"v_mul_f32 v158, v158, v185\n" \
"v_mul_f32 v159, v159, v185\n" \
"s_add_u32 s80, s76, 4096\n" \
"v_cvt_pk_bf16_f32 v112, v128, v129\n" \
"v_cvt_pk_bf16_f32 v113, v130, v131\n" \
"v_cvt_pk_bf16_f32 v114, v132, v133\n" \
"v_cvt_pk_bf16_f32 v115, v134, v135\n" \
"buffer_store_dwordx4 v[112:115], v191, s[56:59], s76 offen offset:0 sc1\n" \
"v_cvt_pk_bf16_f32 v116, v136, v137\n" \
"v_cvt_pk_bf16_f32 v117, v138, v139\n" \
"v_cvt_pk_bf16_f32 v118, v140, v141\n" \
"v_cvt_pk_bf16_f32 v119, v142, v143\n" \
"buffer_store_dwordx4 v[116:119], v191, s[56:59], s76 offen offset:2048 sc1\n" \
"v_cvt_pk_bf16_f32 v120, v144, v145\n" \
"v_cvt_pk_bf16_f32 v121, v146, v147\n" \
"v_cvt_pk_bf16_f32 v122, v148, v149\n" \
"v_cvt_pk_bf16_f32 v123, v150, v151\n" \
"buffer_store_dwordx4 v[120:123], v191, s[56:59], s80 offen offset:0 sc1\n" \
"v_cvt_pk_bf16_f32 v124, v152, v153\n" \
"v_cvt_pk_bf16_f32 v125, v154, v155\n" \
"v_cvt_pk_bf16_f32 v126, v156, v157\n" \
"v_cvt_pk_bf16_f32 v127, v158, v159\n" \
"buffer_store_dwordx4 v[124:127], v191, s[56:59], s80 offen offset:2048 sc1\n" \
"s_waitcnt vmcnt(0)\n" \
"s_setpc_b64 s[68:69]\n" \
"Lpl2_%=:\n" \
"s_mov_b32 s31, 0\n" \
"Lpk2_%=:\n" \
"s_sleep 2\n" \
"buffer_load_dword v220, v221, s[56:59], s81 offen sc1\n" \
"s_waitcnt vmcnt(0)\n" \
"v_cmp_ne_u32 vcc, s60, v220\n" \
"s_nop 1\n" \
"s_and_b32 vcc_hi, vcc_lo, s80\n" \
"s_cbranch_scc0 Lpr2_%=\n" \
"s_add_u32 s31, s31, 1\n" \
"s_cmp_lt_u32 s31, 0x40000\n" \
"s_cbranch_scc1 Lpk2_%=\n" \
"s_branch Lpr2_%=\n" \
"Lpl3_%=:\n" \
"s_mov_b32 s31, 0\n" \
"Lpk3_%=:\n" \
"s_sleep 2\n" \
"buffer_load_dwordx2 v[226:227], v228, s[56:59], s30 offen sc1\n" \
"s_waitcnt vmcnt(0)\n" \
"v_xor_b32 v226, v226, v227\n" \
"v_cmp_ne_u32 vcc, s60, v226\n" \
"s_nop 1\n" \
"s_and_b32 vcc_hi, vcc_lo, -1\n" \
"s_cbranch_scc0 Lpr3_%=\n" \
"s_add_u32 s31, s31, 1\n" \
"s_cmp_lt_u32 s31, 0x40000\n" \
"s_cbranch_scc1 Lpk3_%=\n" \
"s_branch Lpr3_%=\n" \
"Lpl4_%=:\n" \
"s_mov_b32 s31, 0\n" \
"Lpk4_%=:\n" \
"s_sleep 2\n" \
"buffer_load_dword v220, off, s[56:59], s79 sc1\n" \
"s_waitcnt vmcnt(0)\n" \
"v_cmp_ne_u32 vcc, s60, v220\n" \
"s_nop 1\n" \
"s_and_b32 vcc_hi, vcc_lo, 1\n" \
"s_cbranch_scc0 Lpr4_%=\n" \
"s_add_u32 s31, s31, 1\n" \
"s_cmp_lt_u32 s31, 0x40000\n" \
"s_cbranch_scc1 Lpk4_%=\n" \
"s_branch Lpr4_%=\n" \
"Lpl5_%=:\n" \
"s_mov_b32 s31, 0\n" \
"Lpk5_%=:\n" \
"s_sleep 2\n" \
"buffer_load_dword v220, v221, s[56:59], s82 offen sc1\n" \
"s_waitcnt vmcnt(0)\n" \
"v_cmp_ne_u32 vcc, s60, v220\n" \
"s_nop 1\n" \
"s_and_b32 vcc_hi, vcc_lo, 65535\n" \
"s_cbranch_scc0 Lpr5_%=\n" \
"s_add_u32 s31, s31, 1\n" \
"s_cmp_lt_u32 s31, 0x40000\n" \
"s_cbranch_scc1 Lpk5_%=\n" \
"s_branch Lpr5_%=\n" \
"Lcode_end_%=:\n" \
".p2align 12\n" \
""
#define MK_ASM_CLOBBERS "v1", "v2", "v3", "v4", "v5", "v6", "v7", "v8", "v9", "v10", "v11", "v12", "v13", "v14", "v15", "v16", "v17", "v18", "v19", "v20", "v21", "v22", "v23", "v24", "v25", "v26", "v27", "v28", "v29", "v30", "v31", "v32", "v33", "v34", "v35", "v36", "v37", "v38", "v39", "v40", "v41", "v42", "v43", "v44", "v45", "v46", "v47", "v48", "v49", "v50", "v51", "v52", "v53", "v54", "v55", "v56", "v57", "v58", "v59", "v60", "v61", "v62", "v63", "v64", "v65", "v66", "v67", "v68", "v69", "v70", "v71", "v72", "v73", "v74", "v75", "v76", "v77", "v78", "v79", "v80", "v81", "v82", "v83", "v84", "v85", "v86", "v87", "v88", "v89", "v90", "v91", "v92", "v93", "v94", "v95", "v96", "v97", "v98", "v99", "v100", "v101", "v102", "v103", "v104", "v105", "v106", "v107", "v108", "v109", "v110", "v111", "v112", "v113", "v114", "v115", "v116", "v117", "v118", "v119", "v120", "v121", "v122", "v123", "v124", "v125", "v126", "v127", "v128", "v129", "v130", "v131", "v132", "v133", "v134", "v135", "v136", "v137", "v138", "v139", "v140", "v141", "v142", "v143", "v144", "v145", "v146", "v147", "v148", "v149", "v150", "v151", "v152", "v153", "v154", "v155", "v156", "v157", "v158", "v159", "v160", "v161", "v162", "v163", "v164", "v165", "v166", "v167", "v168", "v169", "v170", "v171", "v172", "v173", "v174", "v175", "v176", "v177", "v178", "v179", "v180", "v181", "v182", "v183", "v184", "v185", "v186", "v187", "v188", "v189", "v190", "v191", "v192", "v193", "v194", "v195", "v196", "v197", "v198", "v199", "v200", "v201", "v202", "v203", "v204", "v205", "v206", "v207", "v208", "v209", "v210", "v211", "v212", "v213", "v214", "v215", "v216", "v217", "v218", "v219", "v220", "v221", "v222", "v223", "v224", "v225", "v226", "v227", "v228", "v229", "v230", "v231", "v232", "v233", "v234", "v235", "v236", "v237", "v238", "v239", "v240", "v241", "v242", "v243", "v244", "v245", "v246", "v247", "v248", "v249", "v250", "v251", "v252", "v253", "v254", "v255", "a0", "a1", "a2", "a3", "a4", "a5", "a6", "a7", "a8", "a9", "a10", "a11", "a12", "a13", "a14", "a15", "a16", "a17", "a18", "a19", "a20", "a21", "a22", "a23", "a24", "a25", "a26", "a27", "a28", "a29", "a30", "a31", "a32", "a33", "a34", "a35", "a36", "a37", "a38", "a39", "a40", "a41", "a42", "a43", "a44", "a45", "a46", "a47", "a48", "a49", "a50", "a51", "a52", "a53", "a54", "a55", "a56", "a57", "a58", "a59", "a60", "a61", "a62", "a63", "a64", "a65", "a66", "a67", "a68", "a69", "a70", "a71", "a72", "a73", "a74", "a75", "a76", "a77", "a78", "a79", "a80", "a81", "a82", "a83", "a84", "a85", "a86", "a87", "a88", "a89", "a90", "a91", "a92", "a93", "a94", "a95", "a96", "a97", "a98", "a99", "a100", "a101", "a102", "a103", "a104", "a105", "a106", "a107", "a108", "a109", "a110", "a111", "a112", "a113", "a114", "a115", "a116", "a117", "a118", "a119", "a120", "a121", "a122", "a123", "a124", "a125", "a126", "a127", "a128", "a129", "a130", "a131", "a132", "a133", "a134", "a135", "a136", "a137", "a138", "a139", "a140", "a141", "a142", "a143", "a144", "a145", "a146", "a147", "a148", "a149", "a150", "a151", "a152", "a153", "a154", "a155", "a156", "a157", "a158", "a159", "a160", "a161", "a162", "a163", "a164", "a165", "a166", "a167", "a168", "a169", "a170", "a171", "a172", "a173", "a174", "a175", "a176", "a177", "a178", "a179", "a180", "a181", "a182", "a183", "a184", "a185", "a186", "a187", "a188", "a189", "a190", "a191", "a192", "a193", "a194", "a195", "a196", "a197", "a198", "a199", "a200", "a201", "a202", "a203", "a204", "a205", "a206", "a207", "a208", "a209", "a210", "a211", "a212", "a213", "a214", "a215", "a216", "a217", "a218", "a219", "a220", "a221", "a222", "a223", "a224", "a225", "a226", "a227", "a228", "a229", "a230", "a231", "a232", "a233", "a234", "a235", "a236", "a237", "a238", "a239", "a240", "a241", "a242", "a243", "a244", "a245", "a246", "a247", "a248", "a249", "a250", "a251", "a252", "a253", "a254", "a255", "s8", "s9", "s10", "s11", "s12", "s13", "s14", "s15", "s16", "s17", "s18", "s19", "s20", "s21", "s22", "s23", "s24", "s25", "s26", "s27", "s28", "s29", "s30", "s31", "s36", "s37", "s38", "s39", "s40", "s41", "s42", "s43", "s44", "s45", "s46", "s47", "s48", "s49", "s50", "s51", "s52", "s53", "s54", "s55", "s56", "s57", "s58", "s59", "s60", "s61", "s62", "s63", "s64", "s65", "s66", "s67", "s68", "s69", "s70", "s71", "s72", "s73", "s74", "s75", "s76", "s77", "s78", "s79", "s80", "s81", "s82", "s83", "s84", "s85", "s86", "s87", "s88", "s89", "s90", "s91", "s92", "s93", "s94", "s95", "vcc", "scc", "memory"
namespace mk {
constexpr int LDS_BYTES = 163840;
struct Args { const void* in[9]; void* out; void* ws; };
__global__ __launch_bounds__(256, 1) void mega_k(const Args a) {
    extern __shared__ __attribute__((aligned(16))) unsigned char shm[];
    const unsigned ldsb = (unsigned)(size_t)(LAS unsigned char*)shm;
    asm volatile(MK_ASM_BODY
                 :
                 : "s"(__builtin_amdgcn_kernarg_segment_ptr()), "s"(blockIdx.x), "v"(threadIdx.x), "s"(ldsb)
                 : MK_ASM_CLOBBERS);
}
}
extern "C" void kernel_launch(void* const* d_in, const int* in_sizes, int n_in, void* d_out, int out_size, void* d_ws, size_t ws_size, hipStream_t stream) {
    (void)hipFuncSetAttribute((const void*)mk::mega_k, hipFuncAttributeMaxDynamicSharedMemorySize, mk::LDS_BYTES);
    mk::Args a;
    for (int i = 0; i < 9; ++i) a.in[i] = d_in[i];
    a.out = d_out; a.ws = (char*)d_ws + ((ws_size - WS_END) & ~(size_t)0x1fffff);
    mk::mega_k<<<256, 256, mk::LDS_BYTES, stream>>>(a);
}
```

```cpp
#include <hip/hip_runtime.h>
#include <stdint.h>
#define LAS __attribute__((address_space(3)))
constexpr size_t LAY_TS = 17408, LAY_HS = 557824, LAY_CRS = 266240, LAY_ORS = 69632, WS_K = 0, WS_V = 17850368, WS_CTX = 35700736, WS_OB = 52740096, WS_FL = 65585152, LAY_EXD = 29950080, WS_END = 87147648;
#define MK_ASM_BODY \
"s_load_dwordx16 s[8:23], %0, 0x0\n" \
"s_load_dwordx4 s[24:27], %0, 0x40\n" \
"s_load_dwordx2 s[28:29], %0, 0x50\n" \
"v_readfirstlane_b32 s61, %2\n" \
"s_lshr_b32 s61, s61, 6\n" \
"v_mbcnt_lo_u32_b32 v52, -1, 0\n" \
"v_mbcnt_hi_u32_b32 v52, -1, v52\n" \
"v_lshlrev_b32 v248, 4, v52\n" \
"s_getpc_b64 s[64:65]\n" \
"Lpc_%=:\n" \
"s_lshr_b32 s70, %1, 3\n" \
"s_lshl_b32 s70, s70, 2\n" \
"s_add_u32 s70, s70, s61\n" \
"s_and_b32 s71, s70, 127\n" \
"s_lshl_b32 s71, s71, 10\n" \
"v_lshl_add_u32 v53, v248, 0, s71\n" \
"s_mov_b32 s71, Lcode_end_%=-Lpc_%=-4\n" \
"v_min_u32 v53, s71, v53\n" \
"global_load_dword v253, v53, s[64:65]\n" \
"s_and_b32 s62, %1, 7\n" \
"s_lshl_b32 s62, s62, 2\n" \
"s_lshr_b32 s70, %1, 6\n" \
"s_add_u32 s62, s62, s70\n" \
"s_bfe_u32 s63, %1, 0x30003\n" \
"s_lshl_b32 s87, s61, 13\n" \
"v_bfe_u32 v53, v52, 4, 1\n" \
"v_bfe_u32 v54, v52, 2, 2\n" \
"v_lshl_add_u32 v54, v53, 3, v54\n" \
"s_lshl_b32 s71, s61, 4\n" \
"v_add_u32 v54, s71, v54\n" \
"v_lshlrev_b32 v54, 13, v54\n" \
"v_lshlrev_b32 v53, 1, v53\n" \
"v_and_b32 v55, 3, v52\n" \
"v_xor_b32 v53, v53, v55\n" \
"v_lshl_add_u32 v54, v53, 4, v54\n" \
"v_lshrrev_b32 v53, 5, v52\n" \
"v_lshl_add_u32 v54, v53, 6, v54\n" \
"v_add_u32 v40, 4096, v54\n" \
"v_add_u32 v41, 3072, v54\n" \
"v_add_u32 v42, 2176, v54\n" \
"v_add_u32 v43, 1152, v54\n" \
"v_add_u32 v44, 32768, v54\n" \
"v_add_u32 v45, 31744, v54\n" \
"v_add_u32 v46, 30848, v54\n" \
"v_add_u32 v47, 29824, v54\n" \
"v_lshrrev_b32 v55, 3, v52\n" \
"v_and_b32 v56, 7, v52\n" \
"v_xor_b32 v56, v56, v55\n" \
"v_lshlrev_b32 v56, 4, v56\n" \
"v_lshl_add_u32 v57, v55, 10, v56\n" \
"v_add_u32 v32, 528384, v57\n" \
"v_add_u32 v33, 535552, v57\n" \
"v_add_u32 v34, 542720, v57\n" \
"v_add_u32 v35, 549888, v57\n" \
"v_add_u32 v36, 0, v57\n" \
"v_add_u32 v37, 7168, v57\n" \
"v_add_u32 v38, 14336, v57\n" \
"v_add_u32 v39, 21504, v57\n" \
"s_lshl_b32 s88, s62, 8\n" \
"s_lshl_b32 s70, s63, 16\n" \
"s_and_b32 s71, s87, 0x4000\n" \
"s_lshl_b32 s71, s71, 1\n" \
"s_add_u32 s70, s70, s71\n" \
"s_andn2_b32 s71, 1, s61\n" \
"s_lshl_b32 s71, s71, 20\n" \
"s_add_u32 s70, s70, s71\n" \
"s_add_u32 s73, s63, s62\n" \
"s_and_b32 s73, s73, 1\n" \
"s_mov_b32 s74, 128\n" \
"s_cmp_lg_u32 s73, 0\n" \
"s_cselect_b64 exec, -1, 0\n" \
"v_swap_b32 v32, v40\n" \
"v_swap_b32 v33, v41\n" \
"v_swap_b32 v34, v42\n" \
"v_swap_b32 v35, v43\n" \
"v_swap_b32 v36, v44\n" \
"v_swap_b32 v37, v45\n" \
"v_swap_b32 v38, v46\n" \
"v_swap_b32 v39, v47\n" \
"s_mov_b64 exec, -1\n" \
"s_cselect_b32 s76, s70, s88\n" \
"s_cselect_b32 s77, s88, s70\n" \
"s_cselect_b32 s75, 524288, s74\n" \
"s_cselect_b32 s74, s74, 524288\n" \
"s_waitcnt lgkmcnt(0)\n" \
"s_cselect_b64 s[52:53], s[10:11], s[14:15]\n" \
"s_cselect_b64 s[54:55], s[14:15], s[10:11]\n" \
"s_cselect_b64 s[50:51], s[10:11], s[16:17]\n" \
"s_cselect_b64 s[40:41], s[16:17], s[10:11]\n" \
"s_add_u32 s52, s52, s76\n" \
"s_addc_u32 s53, s53, 0\n" \
"s_add_u32 s50, s50, s76\n" \
"s_addc_u32 s51, s51, 0\n" \
"s_add_u32 m0, s87, 36864\n" \
"s_nop 0\n" \
"global_load_lds_dwordx4 v40, s[52:53] offset:-4096\n" \
"s_add_u32 s54, s54, s77\n" \
"s_addc_u32 s55, s55, 0\n" \
"s_add_u32 s40, s40, s77\n" \
"s_addc_u32 s41, s41, 0\n" \
"global_load_lds_dwordx4 v41, s[50:51] offset:-3072\n" \
"global_load_lds_dwordx4 v42, s[52:53] offset:-2048\n" \
"global_load_lds_dwordx4 v43, s[50:51] offset:-1024\n" \
"global_load_lds_dwordx4 v44, s[52:53] offset:0\n" \
"global_load_lds_dwordx4 v45, s[50:51] offset:1024\n" \
"global_load_lds_dwordx4 v46, s[52:53] offset:2048\n" \
"global_load_lds_dwordx4 v47, s[50:51] offset:3072\n" \
"s_add_u32 s52, s52, s74\n" \
"s_addc_u32 s53, s53, 0\n" \
"s_add_u32 s50, s50, s74\n" \
"s_addc_u32 s51, s51, 0\n" \
"s_add_u32 m0, s87, 4096\n" \
"s_nop 0\n" \
"global_load_lds_dwordx4 v32, s[54:55] offset:-4096\n" \
"global_load_lds_dwordx4 v33, s[40:41] offset:-3072\n" \
"global_load_lds_dwordx4 v34, s[54:55] offset:-2048\n" \
"global_load_lds_dwordx4 v35, s[40:41] offset:-1024\n" \
"global_load_lds_dwordx4 v36, s[54:55] offset:0\n" \
"global_load_lds_dwordx4 v37, s[40:41] offset:1024\n" \
"global_load_lds_dwordx4 v38, s[54:55] offset:2048\n" \
"global_load_lds_dwordx4 v39, s[40:41] offset:3072\n" \
"s_add_u32 s54, s54, s75\n" \
"s_addc_u32 s55, s55, 0\n" \
"s_add_u32 s40, s40, s75\n" \
"s_addc_u32 s41, s41, 0\n" \
"s_mov_b64 s[56:57], s[28:29]\n" \
"s_and_b32 s57, s57, 0xffff\n" \
"s_mov_b32 s58, 87147648\n" \
"s_mov_b32 s59, 0x20000\n" \
"s_lshl_b32 s66, %1, 2\n" \
"s_add_u32 s66, s66, s61\n" \
"s_lshl_b32 s66, s66, 10\n" \
"s_lshr_b32 s67, %1, 3\n" \
"s_lshl_b32 s67, s67, 2\n" \
"s_add_u32 s67, s67, s61\n" \
"s_lshl_b32 s67, s67, 13\n" \
"s_and_b32 s64, %1, 7\n" \
"s_lshl_b32 s64, s64, 10\n" \
"s_add_u32 s67, s67, s64\n" \
"s_lshl_b32 s89, s63, 18\n" \
"s_lshl_b32 s95, s61, 12\n" \
"v_add_u32 v228, 4096, v54\n" \
"v_add_u32 v229, 3200, v54\n" \
"v_lshl_add_u32 v57, v55, 11, v56\n" \
"v_add_u32 v224, 0, v57\n" \
"v_add_u32 v225, 15360, v57\n" \
"v_add_u32 v226, 129024, v57\n" \
"v_add_u32 v227, 144384, v57\n" \
"s_lshl_b32 s70, s61, 15\n" \
"s_add_u32 s70, s70, 0\n" \
"s_add_u32 s71, s70, s89\n" \
"s_add_u32 s71, s71, 4096\n" \
"s_add_u32 s48, s8, s71\n" \
"s_addc_u32 s49, s9, 0\n" \
"s_sub_u32 s71, s70, s89\n" \
"s_add_u32 s71, s71, 3932160\n" \
"s_add_u32 s46, s8, s71\n" \
"s_addc_u32 s47, s9, 0\n" \
"s_add_u32 s70, s88, 0\n" \
"s_add_u32 s44, s12, s70\n" \
"s_addc_u32 s45, s13, 0\n" \
"s_add_u32 s42, s44, 30720\n" \
"s_addc_u32 s43, s45, 0\n" \
"s_lshr_b32 s70, s28, 31\n" \
"s_lshl_b32 s70, s70, 30\n" \
"s_sub_u32 s28, s28, s70\n" \
"v_add_u32 v250, s70, v248\n" \
"v_lshlrev_b32 v245, 2, v52\n" \
"v_mov_b32 v254, 0x30000\n" \
"v_lshrrev_b32 v55, 4, v52\n" \
"v_and_b32 v249, 0xf0, v248\n" \
"v_lshl_add_u32 v249, v55, 9, v249\n" \
"v_and_b32 v53, 15, v52\n" \
"s_lshl_b32 s70, s61, 4\n" \
"v_lshlrev_b32 v54, 2, v55\n" \
"v_sub_u32 v251, v53, v54\n" \
"v_add_u32 v251, s70, v251\n" \
"v_and_b32 v54, 7, v52\n" \
"v_xor_b32 v54, v54, v55\n" \
"v_lshlrev_b32 v53, 7, v53\n" \
"s_add_u32 s71, s87, 131072\n" \
"s_lshr_b32 s70, s61, 1\n" \
"s_lshl_b32 s70, s70, 14\n" \
"s_lshl_b32 s76, s73, 15\n" \
"s_add_u32 s70, s70, s76\n" \
"v_xor_b32 v56, 0, v54\n" \
"v_lshl_add_u32 v56, v56, 4, v53\n" \
"v_add_u32 v230, s71, v56\n" \
"v_add_u32 v48, s70, v56\n" \
"v_xor_b32 v56, 4, v54\n" \
"v_lshl_add_u32 v56, v56, 4, v53\n" \
"v_add_u32 v231, s71, v56\n" \
"v_add_u32 v49, s70, v56\n" \
"v_and_b32 v53, 3, v52\n" \
"v_lshlrev_b32 v53, 3, v53\n" \
"v_bfe_u32 v54, v52, 2, 2\n" \
"v_lshl_add_u32 v53, v54, 6, v53\n" \
"v_bfe_u32 v54, v52, 4, 1\n" \
"v_lshl_add_u32 v53, v54, 8, v53\n" \
"v_lshl_add_u32 v53, v54, 5, v53\n" \
"v_lshrrev_b32 v54, 5, v52\n" \
"s_and_b32 s70, s61, 1\n" \
"s_lshl_b32 s70, s70, 11\n" \
"s_xor_b32 s76, s76, 0x8000\n" \
"s_add_u32 s70, s70, s76\n" \
"v_lshl_add_u32 v56, v54, 13, v53\n" \
"v_add_u32 v50, s70, v56\n" \
"v_xor_b32 v51, 32, v50\n" \
"v_lshl_add_u32 v232, v54, 12, v53\n" \
"v_xor_b32 v233, 32, v232\n" \
"s_mov_b64 exec, 0\n" \
"s_cmp_eq_u32 s61, 0\n" \
"s_cbranch_scc1 Lst0x0_%=\n" \
"s_cmp_eq_u32 s61, 1\n" \
"s_cbranch_scc1 Lst0x1_%=\n" \
"s_cmp_eq_u32 s61, 2\n" \
"s_cbranch_scc1 Lst0x2_%=\n" \
"s_branch Lst0x3_%=\n" \
"Lcb0_%=:\n" \
"s_mov_b64 exec, -1\n" \
"s_add_u32 m0, s87, 135168\n" \
"s_nop 0\n" \
"global_load_lds_dwordx4 v40, s[52:53] offset:-4096\n" \
"global_load_lds_dwordx4 v41, s[50:51] offset:-3072\n" \
"global_load_lds_dwordx4 v42, s[52:53] offset:-2048\n" \
"global_load_lds_dwordx4 v43, s[50:51] offset:-1024\n" \
"global_load_lds_dwordx4 v44, s[52:53] offset:0\n" \
"global_load_lds_dwordx4 v45, s[50:51] offset:1024\n" \
"global_load_lds_dwordx4 v46, s[52:53] offset:2048\n" \
"global_load_lds_dwordx4 v47, s[50:51] offset:3072\n" \
"s_add_u32 s52, s52, s74\n" \
"s_addc_u32 s53, s53, 0\n" \
"s_add_u32 s50, s50, s74\n" \
"s_addc_u32 s51, s51, 0\n" \
"s_waitcnt vmcnt(8)\n" \
"s_barrier\n" \
"ds_read_b128 v[64:67], v48 offset:0\n" \
"ds_read_b64_tr_b16 v[128:129], v50 offset:0\n" \
"ds_read_b64_tr_b16 v[130:131], v50 offset:4096\n" \
"ds_read_b128 v[68:71], v49 offset:0\n" \
"ds_read_b64_tr_b16 v[132:133], v50 offset:16384\n" \
"ds_read_b64_tr_b16 v[134:135], v50 offset:20480\n" \
"ds_read_b128 v[72:75], v48 offset:2048\n" \
"ds_read_b64_tr_b16 v[136:137], v51 offset:0\n" \
"ds_read_b64_tr_b16 v[138:139], v51 offset:4096\n" \
"ds_read_b128 v[76:79], v49 offset:2048\n" \
"ds_read_b64_tr_b16 v[140:141], v51 offset:16384\n" \
"ds_read_b64_tr_b16 v[142:143], v51 offset:20480\n" \
"ds_read_b64_tr_b16 v[144:145], v50 offset:512\n" \
"ds_read_b64_tr_b16 v[146:147], v50 offset:4608\n" \
"ds_read_b64_tr_b16 v[148:149], v50 offset:16896\n" \
"ds_read_b64_tr_b16 v[150:151], v50 offset:20992\n" \
"ds_read_b64_tr_b16 v[152:153], v51 offset:512\n" \
"ds_read_b64_tr_b16 v[154:155], v51 offset:4608\n" \
"ds_read_b64_tr_b16 v[156:157], v51 offset:16896\n" \
"ds_read_b64_tr_b16 v[158:159], v51 offset:20992\n" \
"s_waitcnt lgkmcnt(14)\n" \
"v_mfma_f32_16x16x32_bf16 a[0:3], v[128:131], v[64:67], 0\n" \
"ds_read_b128 v[120:123], v48 offset:14336\n" \
"s_add_u32 m0, s87, 69632\n" \
"v_mfma_f32_16x16x32_bf16 a[0:3], v[132:135], v[68:71], a[0:3]\n" \
"s_waitcnt lgkmcnt(9)\n" \
"ds_read_b128 v[80:83], v48 offset:4096\n" \
"global_load_lds_dwordx4 v32, s[54:55] offset:-4096\n" \
"v_mfma_f32_16x16x32_bf16 a[4:7], v[136:139], v[64:67], 0\n" \
"ds_read_b128 v[124:127], v49 offset:14336\n" \
"v_mfma_f32_16x16x32_bf16 a[4:7], v[140:143], v[68:71], a[4:7]\n" \
"s_waitcnt lgkmcnt(7)\n" \
"ds_read_b128 v[84:87], v49 offset:4096\n" \
"v_mfma_f32_16x16x32_bf16 a[8:11], v[144:147], v[64:67], 0\n" \
"ds_read_b64_tr_b16 v[160:161], v50 offset:1024\n" \
"global_load_lds_dwordx4 v33, s[40:41] offset:-3072\n" \
"v_mfma_f32_16x16x32_bf16 a[8:11], v[148:151], v[68:71], a[8:11]\n" \
"s_waitcnt lgkmcnt(4)\n" \
"ds_read_b128 v[88:91], v48 offset:6144\n" \
"v_mfma_f32_16x16x32_bf16 a[12:15], v[152:155], v[64:67], 0\n" \
"ds_read_b64_tr_b16 v[162:163], v50 offset:5120\n" \
"v_mfma_f32_16x16x32_bf16 a[12:15], v[156:159], v[68:71], a[12:15]\n" \
"ds_read_b128 v[92:95], v49 offset:6144\n" \
"global_load_lds_dwordx4 v34, s[54:55] offset:-2048\n" \
"v_mfma_f32_16x16x32_bf16 a[44:47], v[152:155], v[72:75], 0\n" \
"ds_read_b64_tr_b16 v[164:165], v50 offset:17408\n" \
"v_mfma_f32_16x16x32_bf16 a[44:47], v[156:159], v[76:79], a[44:47]\n" \
"ds_read_b128 v[96:99], v48 offset:8192\n" \
"v_mfma_f32_16x16x32_bf16 a[40:43], v[144:147], v[72:75], 0\n" \
"ds_read_b64_tr_b16 v[166:167], v50 offset:21504\n" \
"global_load_lds_dwordx4 v35, s[40:41] offset:-1024\n" \
"v_mfma_f32_16x16x32_bf16 a[40:43], v[148:151], v[76:79], a[40:43]\n" \
"ds_read_b128 v[100:103], v49 offset:8192\n" \
"v_mfma_f32_16x16x32_bf16 a[36:39], v[136:139], v[72:75], 0\n" \
"ds_read_b64_tr_b16 v[168:169], v51 offset:1024\n" \
"v_mfma_f32_16x16x32_bf16 a[36:39], v[140:143], v[76:79], a[36:39]\n" \
"ds_read_b128 v[104:107], v48 offset:10240\n" \
"global_load_lds_dwordx4 v36, s[54:55] offset:0\n" \
"v_mfma_f32_16x16x32_bf16 a[32:35], v[128:131], v[72:75], 0\n" \
"ds_read_b64_tr_b16 v[170:171], v51 offset:5120\n" \
"v_mfma_f32_16x16x32_bf16 a[32:35], v[132:135], v[76:79], a[32:35]\n" \
"s_waitcnt lgkmcnt(4)\n" \
"ds_read_b128 v[108:111], v49 offset:10240\n" \
"v_mfma_f32_16x16x32_bf16 a[64:67], v[128:131], v[80:83], 0\n" \
"ds_read_b64_tr_b16 v[172:173], v51 offset:17408\n" \
"global_load_lds_dwordx4 v37, s[40:41] offset:1024\n" \
"v_mfma_f32_16x16x32_bf16 a[64:67], v[132:135], v[84:87], a[64:67]\n" \
"ds_read_b128 v[112:115], v48 offset:12288\n" \
"v_mfma_f32_16x16x32_bf16 a[68:71], v[136:139], v[80:83], 0\n" \
"ds_read_b64_tr_b16 v[174:175], v51 offset:21504\n" \
"v_mfma_f32_16x16x32_bf16 a[68:71], v[140:143], v[84:87], a[68:71]\n" \
"ds_read_b128 v[116:119], v49 offset:12288\n" \
"global_load_lds_dwordx4 v38, s[54:55] offset:2048\n" \
"v_mfma_f32_16x16x32_bf16 a[72:75], v[144:147], v[80:83], 0\n" \
"ds_read_b64_tr_b16 v[176:177], v50 offset:1536\n" \
"v_mfma_f32_16x16x32_bf16 a[72:75], v[148:151], v[84:87], a[72:75]\n" \
"global_load_lds_dwordx4 v39, s[40:41] offset:3072\n" \
"v_mfma_f32_16x16x32_bf16 a[76:79], v[152:155], v[80:83], 0\n" \
"s_add_u32 s54, s54, s75\n" \
"s_addc_u32 s55, s55, 0\n" \
"ds_read_b64_tr_b16 v[178:179], v50 offset:5632\n" \
"v_mfma_f32_16x16x32_bf16 a[76:79], v[156:159], v[84:87], a[76:79]\n" \
"s_add_u32 s40, s40, s75\n" \
"s_addc_u32 s41, s41, 0\n" \
"s_mov_b32 s90, 65593344\n" \
"v_mfma_f32_16x16x32_bf16 a[108:111], v[152:155], v[88:91], 0\n" \
"ds_read_b64_tr_b16 v[180:181], v50 offset:17920\n" \
"v_mfma_f32_16x16x32_bf16 a[108:111], v[156:159], v[92:95], a[108:111]\n" \
"buffer_load_dword v244, off, s[56:59], s90 sc1\n" \
"v_mfma_f32_16x16x32_bf16 a[104:107], v[144:147], v[88:91], 0\n" \
"ds_read_b64_tr_b16 v[182:183], v50 offset:22016\n" \
"v_mfma_f32_16x16x32_bf16 a[104:107], v[148:151], v[92:95], a[104:107]\n" \
"s_add_u32 m0, s87, 102400\n" \
"v_mfma_f32_16x16x32_bf16 a[100:103], v[136:139], v[88:91], 0\n" \
"ds_read_b64_tr_b16 v[184:185], v51 offset:1536\n" \
"global_load_lds_dwordx4 v40, s[52:53] offset:-4096\n" \
"v_mfma_f32_16x16x32_bf16 a[100:103], v[140:143], v[92:95], a[100:103]\n" \
"v_mfma_f32_16x16x32_bf16 a[96:99], v[128:131], v[88:91], 0\n" \
"ds_read_b64_tr_b16 v[186:187], v51 offset:5632\n" \
"v_mfma_f32_16x16x32_bf16 a[96:99], v[132:135], v[92:95], a[96:99]\n" \
"s_waitcnt lgkmcnt(2)\n" \
"global_load_lds_dwordx4 v41, s[50:51] offset:-3072\n" \
"v_mfma_f32_16x16x32_bf16 a[128:131], v[128:131], v[96:99], 0\n" \
"ds_read_b64_tr_b16 v[188:189], v51 offset:17920\n" \
"v_mfma_f32_16x16x32_bf16 a[128:131], v[132:135], v[100:103], a[128:131]\n" \
"v_mfma_f32_16x16x32_bf16 a[132:135], v[136:139], v[96:99], 0\n" \
"ds_read_b64_tr_b16 v[190:191], v51 offset:22016\n" \
"global_load_lds_dwordx4 v42, s[52:53] offset:-2048\n" \
"v_mfma_f32_16x16x32_bf16 a[132:135], v[140:143], v[100:103], a[132:135]\n" \
"v_mfma_f32_16x16x32_bf16 a[136:139], v[144:147], v[96:99], 0\n" \
"v_mfma_f32_16x16x32_bf16 a[136:139], v[148:151], v[100:103], a[136:139]\n" \
"global_load_lds_dwordx4 v43, s[50:51] offset:-1024\n" \
"v_mfma_f32_16x16x32_bf16 a[140:143], v[152:155], v[96:99], 0\n" \
"v_mfma_f32_16x16x32_bf16 a[140:143], v[156:159], v[100:103], a[140:143]\n" \
"global_load_lds_dwordx4 v44, s[52:53] offset:0\n" \
"v_mfma_f32_16x16x32_bf16 a[172:175], v[152:155], v[104:107], 0\n" \
"s_mul_i32 s76, s73, 0x8000\n" \
"s_add_u32 s77, s76, 0x10000\n" \
"s_sub_u32 s78, 0x18000, s76\n" \
"v_mfma_f32_16x16x32_bf16 a[172:175], v[156:159], v[108:111], a[172:175]\n" \
"v_add_u32 v48, s77, v48\n" \
"v_add_u32 v49, s77, v49\n" \
"v_mfma_f32_16x16x32_bf16 a[168:171], v[144:147], v[104:107], 0\n" \
"v_add_u32 v50, s78, v50\n" \
"v_add_u32 v51, s78, v51\n" \
"v_mfma_f32_16x16x32_bf16 a[168:171], v[148:151], v[108:111], a[168:171]\n" \
"global_load_lds_dwordx4 v45, s[50:51] offset:1024\n" \
"v_mfma_f32_16x16x32_bf16 a[164:167], v[136:139], v[104:107], 0\n" \
"v_mfma_f32_16x16x32_bf16 a[164:167], v[140:143], v[108:111], a[164:167]\n" \
"v_mfma_f32_16x16x32_bf16 a[160:163], v[128:131], v[104:107], 0\n" \
"global_load_lds_dwordx4 v46, s[52:53] offset:2048\n" \
"v_mfma_f32_16x16x32_bf16 a[160:163], v[132:135], v[108:111], a[160:163]\n" \
"v_mfma_f32_16x16x32_bf16 a[192:195], v[128:131], v[112:115], 0\n" \
"v_mfma_f32_16x16x32_bf16 a[192:195], v[132:135], v[116:119], a[192:195]\n" \
"global_load_lds_dwordx4 v47, s[50:51] offset:3072\n" \
"v_mfma_f32_16x16x32_bf16 a[196:199], v[136:139], v[112:115], 0\n" \
"s_add_u32 s52, s52, s74\n" \
"s_addc_u32 s53, s53, 0\n" \
"v_mfma_f32_16x16x32_bf16 a[196:199], v[140:143], v[116:119], a[196:199]\n" \
"s_add_u32 s50, s50, s74\n" \
"s_addc_u32 s51, s51, 0\n" \
"v_mfma_f32_16x16x32_bf16 a[200:203], v[144:147], v[112:115], 0\n" \
"v_mfma_f32_16x16x32_bf16 a[200:203], v[148:151], v[116:119], a[200:203]\n" \
"v_mfma_f32_16x16x32_bf16 a[204:207], v[152:155], v[112:115], 0\n" \
"v_mfma_f32_16x16x32_bf16 a[204:207], v[156:159], v[116:119], a[204:207]\n" \
"v_mfma_f32_16x16x32_bf16 a[236:239], v[152:155], v[120:123], 0\n" \
"v_mfma_f32_16x16x32_bf16 a[236:239], v[156:159], v[124:127], a[236:239]\n" \
"v_mfma_f32_16x16x32_bf16 a[232:235], v[144:147], v[120:123], 0\n" \
"v_mfma_f32_16x16x32_bf16 a[232:235], v[148:151], v[124:127], a[232:235]\n" \
"v_mfma_f32_16x16x32_bf16 a[228:231], v[136:139], v[120:123], 0\n" \
"v_mfma_f32_16x16x32_bf16 a[228:231], v[140:143], v[124:127], a[228:231]\n" \
"s_waitcnt vmcnt(9) lgkmcnt(0)\n" \
"v_mfma_f32_16x16x32_bf16 a[224:227], v[128:131], v[120:123], 0\n" \
"v_mfma_f32_16x16x32_bf16 a[224:227], v[132:135], v[124:127], a[224:227]\n" \
"s_barrier\n" \
"v_mfma_f32_16x16x32_bf16 a[16:19], v[64:67], v[160:163], 0\n" \
"s_add_u32 m0, s87, 36864\n" \
"ds_read_b64_tr_b16 v[128:129], v50 offset:0\n" \
"v_mfma_f32_16x16x32_bf16 a[16:19], v[68:71], v[164:167], a[16:19]\n" \
"global_load_lds_dwordx4 v32, s[54:55] offset:-4096\n" \
"v_mfma_f32_16x16x32_bf16 a[20:23], v[64:67], v[168:171], 0\n" \
"ds_read_b64_tr_b16 v[130:131], v50 offset:4096\n" \
"v_mfma_f32_16x16x32_bf16 a[20:23], v[68:71], v[172:175], a[20:23]\n" \
"v_mfma_f32_16x16x32_bf16 a[24:27], v[64:67], v[176:179], 0\n" \
"ds_read_b64_tr_b16 v[132:133], v50 offset:16384\n" \
"v_mfma_f32_16x16x32_bf16 a[24:27], v[68:71], v[180:183], a[24:27]\n" \
"global_load_lds_dwordx4 v33, s[40:41] offset:-3072\n" \
"v_mfma_f32_16x16x32_bf16 a[28:31], v[64:67], v[184:187], 0\n" \
"ds_read_b64_tr_b16 v[134:135], v50 offset:20480\n" \
"v_mfma_f32_16x16x32_bf16 a[28:31], v[68:71], v[188:191], a[28:31]\n" \
"v_mfma_f32_16x16x32_bf16 a[60:63], v[72:75], v[184:187], 0\n" \
"ds_read_b128 v[64:67], v48 offset:0\n" \
"v_mfma_f32_16x16x32_bf16 a[60:63], v[76:79], v[188:191], a[60:63]\n" \
"global_load_lds_dwordx4 v34, s[54:55] offset:-2048\n" \
"v_mfma_f32_16x16x32_bf16 a[56:59], v[72:75], v[176:179], 0\n" \
"ds_read_b64_tr_b16 v[136:137], v51 offset:0\n" \
"v_mfma_f32_16x16x32_bf16 a[56:59], v[76:79], v[180:183], a[56:59]\n" \
"v_mfma_f32_16x16x32_bf16 a[52:55], v[72:75], v[168:171], 0\n" \
"ds_read_b128 v[68:71], v49 offset:0\n" \
"v_mfma_f32_16x16x32_bf16 a[52:55], v[76:79], v[172:175], a[52:55]\n" \
"global_load_lds_dwordx4 v35, s[40:41] offset:-1024\n" \
"v_mfma_f32_16x16x32_bf16 a[48:51], v[72:75], v[160:163], 0\n" \
"ds_read_b64_tr_b16 v[138:139], v51 offset:4096\n" \
"v_mfma_f32_16x16x32_bf16 a[48:51], v[76:79], v[164:167], a[48:51]\n" \
"v_mfma_f32_16x16x32_bf16 a[80:83], v[80:83], v[160:163], 0\n" \
"ds_read_b128 v[72:75], v48 offset:2048\n" \
"v_mfma_f32_16x16x32_bf16 a[80:83], v[84:87], v[164:167], a[80:83]\n" \
"global_load_lds_dwordx4 v36, s[54:55] offset:0\n" \
"v_mfma_f32_16x16x32_bf16 a[84:87], v[80:83], v[168:171], 0\n" \
"ds_read_b64_tr_b16 v[140:141], v51 offset:16384\n" \
"v_mfma_f32_16x16x32_bf16 a[84:87], v[84:87], v[172:175], a[84:87]\n" \
"v_mfma_f32_16x16x32_bf16 a[88:91], v[80:83], v[176:179], 0\n" \
"ds_read_b128 v[76:79], v49 offset:2048\n" \
"v_mfma_f32_16x16x32_bf16 a[88:91], v[84:87], v[180:183], a[88:91]\n" \
"global_load_lds_dwordx4 v37, s[40:41] offset:1024\n" \
"v_mfma_f32_16x16x32_bf16 a[92:95], v[80:83], v[184:187], 0\n" \
"ds_read_b64_tr_b16 v[142:143], v51 offset:20480\n" \
"v_mfma_f32_16x16x32_bf16 a[92:95], v[84:87], v[188:191], a[92:95]\n" \
"v_mfma_f32_16x16x32_bf16 a[124:127], v[88:91], v[184:187], 0\n" \
"ds_read_b128 v[80:83], v48 offset:4096\n" \
"v_mfma_f32_16x16x32_bf16 a[124:127], v[92:95], v[188:191], a[124:127]\n" \
"global_load_lds_dwordx4 v38, s[54:55] offset:2048\n" \
"v_mfma_f32_16x16x32_bf16 a[120:123], v[88:91], v[176:179], 0\n" \
"ds_read_b64_tr_b16 v[144:145], v50 offset:512\n" \
"v_mfma_f32_16x16x32_bf16 a[120:123], v[92:95], v[180:183], a[120:123]\n" \
"v_mfma_f32_16x16x32_bf16 a[116:119], v[88:91], v[168:171], 0\n" \
"ds_read_b128 v[84:87], v49 offset:4096\n" \
"global_load_lds_dwordx4 v39, s[40:41] offset:3072\n" \
"v_mfma_f32_16x16x32_bf16 a[116:119], v[92:95], v[172:175], a[116:119]\n" \
"s_add_u32 s54, s54, s75\n" \
"s_addc_u32 s55, s55, 0\n" \
"v_mfma_f32_16x16x32_bf16 a[112:115], v[88:91], v[160:163], 0\n" \
"s_add_u32 s40, s40, s75\n" \
"s_addc_u32 s41, s41, 0\n" \
"ds_read_b64_tr_b16 v[146:147], v50 offset:4608\n" \
"v_mfma_f32_16x16x32_bf16 a[112:115], v[92:95], v[164:167], a[112:115]\n" \
"s_add_u32 s68, s66, 0x0\n" \
"s_add_u32 s64, s10, s68\n" \
"s_addc_u32 s65, s11, 0\n" \
"v_mfma_f32_16x16x32_bf16 a[144:147], v[96:99], v[160:163], 0\n" \
"ds_read_b128 v[88:91], v48 offset:6144\n" \
"v_mfma_f32_16x16x32_bf16 a[144:147], v[100:103], v[164:167], a[144:147]\n" \
"global_load_dword v253, v248, s[64:65]\n" \
"v_mfma_f32_16x16x32_bf16 a[148:151], v[96:99], v[168:171], 0\n" \
"ds_read_b64_tr_b16 v[148:149], v50 offset:16896\n" \
"v_mfma_f32_16x16x32_bf16 a[148:151], v[100:103], v[172:175], a[148:151]\n" \
"v_mfma_f32_16x16x32_bf16 a[152:155], v[96:99], v[176:179], 0\n" \
"ds_read_b128 v[92:95], v49 offset:6144\n" \
"v_mfma_f32_16x16x32_bf16 a[152:155], v[100:103], v[180:183], a[152:155]\n" \
"v_mfma_f32_16x16x32_bf16 a[156:159], v[96:99], v[184:187], 0\n" \
"ds_read_b64_tr_b16 v[150:151], v50 offset:20992\n" \
"v_mfma_f32_16x16x32_bf16 a[156:159], v[100:103], v[188:191], a[156:159]\n" \
"v_mfma_f32_16x16x32_bf16 a[188:191], v[104:107], v[184:187], 0\n" \
"ds_read_b128 v[96:99], v48 offset:8192\n" \
"v_mfma_f32_16x16x32_bf16 a[188:191], v[108:111], v[188:191], a[188:191]\n" \
"v_mfma_f32_16x16x32_bf16 a[184:187], v[104:107], v[176:179], 0\n" \
"ds_read_b64_tr_b16 v[152:153], v51 offset:512\n" \
"v_mfma_f32_16x16x32_bf16 a[184:187], v[108:111], v[180:183], a[184:187]\n" \
"v_mfma_f32_16x16x32_bf16 a[180:183], v[104:107], v[168:171], 0\n" \
"ds_read_b128 v[100:103], v49 offset:8192\n" \
"v_mfma_f32_16x16x32_bf16 a[180:183], v[108:111], v[172:175], a[180:183]\n" \
"v_mfma_f32_16x16x32_bf16 a[176:179], v[104:107], v[160:163], 0\n" \
"ds_read_b64_tr_b16 v[154:155], v51 offset:4608\n" \
"v_mfma_f32_16x16x32_bf16 a[176:179], v[108:111], v[164:167], a[176:179]\n" \
"v_mfma_f32_16x16x32_bf16 a[208:211], v[112:115], v[160:163], 0\n" \
"ds_read_b128 v[104:107], v48 offset:10240\n" \
"v_mfma_f32_16x16x32_bf16 a[208:211], v[116:119], v[164:167], a[208:211]\n" \
"v_mfma_f32_16x16x32_bf16 a[212:215], v[112:115], v[168:171], 0\n" \
"ds_read_b64_tr_b16 v[156:157], v51 offset:16896\n" \
"v_mfma_f32_16x16x32_bf16 a[212:215], v[116:119], v[172:175], a[212:215]\n" \
"v_mfma_f32_16x16x32_bf16 a[216:219], v[112:115], v[176:179], 0\n" \
"ds_read_b128 v[108:111], v49 offset:10240\n" \
"v_mfma_f32_16x16x32_bf16 a[216:219], v[116:119], v[180:183], a[216:219]\n" \
"v_mfma_f32_16x16x32_bf16 a[220:223], v[112:115], v[184:187], 0\n" \
"ds_read_b64_tr_b16 v[158:159], v51 offset:20992\n" \
"v_mfma_f32_16x16x32_bf16 a[220:223], v[116:119], v[188:191], a[220:223]\n" \
"v_mfma_f32_16x16x32_bf16 a[252:255], v[120:123], v[184:187], 0\n" \
"ds_read_b128 v[112:115], v48 offset:12288\n" \
"v_mfma_f32_16x16x32_bf16 a[252:255], v[124:127], v[188:191], a[252:255]\n" \
"v_mfma_f32_16x16x32_bf16 a[248:251], v[120:123], v[176:179], 0\n" \
"v_mfma_f32_16x16x32_bf16 a[248:251], v[124:127], v[180:183], a[248:251]\n" \
"v_mfma_f32_16x16x32_bf16 a[244:247], v[120:123], v[168:171], 0\n" \
"ds_read_b128 v[116:119], v49 offset:12288\n" \
"v_mfma_f32_16x16x32_bf16 a[244:247], v[124:127], v[172:175], a[244:247]\n" \
"v_mfma_f32_16x16x32_bf16 a[240:243], v[120:123], v[160:163], 0\n" \
"v_mfma_f32_16x16x32_bf16 a[240:243], v[124:127], v[164:167], a[240:243]\n" \
"s_waitcnt lgkmcnt(1)\n" \
"v_mfma_f32_16x16x32_bf16 a[0:3], v[128:131], v[64:67], a[0:3]\n" \
"ds_read_b128 v[120:123], v48 offset:14336\n" \
"v_mfma_f32_16x16x32_bf16 a[0:3], v[132:135], v[68:71], a[0:3]\n" \
"v_mfma_f32_16x16x32_bf16 a[4:7], v[136:139], v[64:67], a[4:7]\n" \
"ds_read_b128 v[124:127], v49 offset:14336\n" \
"v_mfma_f32_16x16x32_bf16 a[4:7], v[140:143], v[68:71], a[4:7]\n" \
"s_add_u32 s68, s66, 0x100000\n" \
"v_mfma_f32_16x16x32_bf16 a[8:11], v[144:147], v[64:67], a[8:11]\n" \
"s_add_u32 s64, s10, s68\n" \
"s_addc_u32 s65, s11, 0\n" \
"ds_read_b64_tr_b16 v[160:161], v50 offset:1024\n" \
"v_mfma_f32_16x16x32_bf16 a[8:11], v[148:151], v[68:71], a[8:11]\n" \
"global_load_dword v253, v248, s[64:65]\n" \
"v_mfma_f32_16x16x32_bf16 a[12:15], v[152:155], v[64:67], a[12:15]\n" \
"s_add_u32 m0, s87, 4096\n" \
"ds_read_b64_tr_b16 v[162:163], v50 offset:5120\n" \
"v_mfma_f32_16x16x32_bf16 a[12:15], v[156:159], v[68:71], a[12:15]\n" \
"global_load_lds_dwordx4 v40, s[52:53] offset:-4096\n" \
"v_mfma_f32_16x16x32_bf16 a[44:47], v[152:155], v[72:75], a[44:47]\n" \
"ds_read_b64_tr_b16 v[164:165], v50 offset:17408\n" \
"v_mfma_f32_16x16x32_bf16 a[44:47], v[156:159], v[76:79], a[44:47]\n" \
"v_mfma_f32_16x16x32_bf16 a[40:43], v[144:147], v[72:75], a[40:43]\n" \
"ds_read_b64_tr_b16 v[166:167], v50 offset:21504\n" \
"v_mfma_f32_16x16x32_bf16 a[40:43], v[148:151], v[76:79], a[40:43]\n" \
"global_load_lds_dwordx4 v41, s[50:51] offset:-3072\n" \
"v_mfma_f32_16x16x32_bf16 a[36:39], v[136:139], v[72:75], a[36:39]\n" \
"ds_read_b64_tr_b16 v[168:169], v51 offset:1024\n" \
"v_mfma_f32_16x16x32_bf16 a[36:39], v[140:143], v[76:79], a[36:39]\n" \
"v_mfma_f32_16x16x32_bf16 a[32:35], v[128:131], v[72:75], a[32:35]\n" \
"ds_read_b64_tr_b16 v[170:171], v51 offset:5120\n" \
"v_mfma_f32_16x16x32_bf16 a[32:35], v[132:135], v[76:79], a[32:35]\n" \
"global_load_lds_dwordx4 v42, s[52:53] offset:-2048\n" \
"v_mfma_f32_16x16x32_bf16 a[64:67], v[128:131], v[80:83], a[64:67]\n" \
"ds_read_b64_tr_b16 v[172:173], v51 offset:17408\n" \
"v_mfma_f32_16x16x32_bf16 a[64:67], v[132:135], v[84:87], a[64:67]\n" \
"v_mfma_f32_16x16x32_bf16 a[68:71], v[136:139], v[80:83], a[68:71]\n" \
"ds_read_b64_tr_b16 v[174:175], v51 offset:21504\n" \
"v_mfma_f32_16x16x32_bf16 a[68:71], v[140:143], v[84:87], a[68:71]\n" \
"global_load_lds_dwordx4 v43, s[50:51] offset:-1024\n" \
"v_mfma_f32_16x16x32_bf16 a[72:75], v[144:147], v[80:83], a[72:75]\n" \
"ds_read_b64_tr_b16 v[176:177], v50 offset:1536\n" \
"v_mfma_f32_16x16x32_bf16 a[72:75], v[148:151], v[84:87], a[72:75]\n" \
"v_mfma_f32_16x16x32_bf16 a[76:79], v[152:155], v[80:83], a[76:79]\n" \
"ds_read_b64_tr_b16 v[178:179], v50 offset:5632\n" \
"v_mfma_f32_16x16x32_bf16 a[76:79], v[156:159], v[84:87], a[76:79]\n" \
"global_load_lds_dwordx4 v44, s[52:53] offset:0\n" \
"v_mfma_f32_16x16x32_bf16 a[108:111], v[152:155], v[88:91], a[108:111]\n" \
"ds_read_b64_tr_b16 v[180:181], v50 offset:17920\n" \
"v_mfma_f32_16x16x32_bf16 a[108:111], v[156:159], v[92:95], a[108:111]\n" \
"v_mfma_f32_16x16x32_bf16 a[104:107], v[144:147], v[88:91], a[104:107]\n" \
"ds_read_b64_tr_b16 v[182:183], v50 offset:22016\n" \
"v_mfma_f32_16x16x32_bf16 a[104:107], v[148:151], v[92:95], a[104:107]\n" \
"global_load_lds_dwordx4 v45, s[50:51] offset:1024\n" \
"v_mfma_f32_16x16x32_bf16 a[100:103], v[136:139], v[88:91], a[100:103]\n" \
"ds_read_b64_tr_b16 v[184:185], v51 offset:1536\n" \
"v_mfma_f32_16x16x32_bf16 a[100:103], v[140:143], v[92:95], a[100:103]\n" \
"v_mfma_f32_16x16x32_bf16 a[96:99], v[128:131], v[88:91], a[96:99]\n" \
"ds_read_b64_tr_b16 v[186:187], v51 offset:5632\n" \
"v_mfma_f32_16x16x32_bf16 a[96:99], v[132:135], v[92:95], a[96:99]\n" \
"global_load_lds_dwordx4 v46, s[52:53] offset:2048\n" \
"v_mfma_f32_16x16x32_bf16 a[128:131], v[128:131], v[96:99], a[128:131]\n" \
"ds_read_b64_tr_b16 v[188:189], v51 offset:17920\n" \
"v_mfma_f32_16x16x32_bf16 a[128:131], v[132:135], v[100:103], a[128:131]\n" \
"v_mfma_f32_16x16x32_bf16 a[132:135], v[136:139], v[96:99], a[132:135]\n" \
"ds_read_b64_tr_b16 v[190:191], v51 offset:22016\n" \
"v_mfma_f32_16x16x32_bf16 a[132:135], v[140:143], v[100:103], a[132:135]\n" \
"global_load_lds_dwordx4 v47, s[50:51] offset:3072\n" \
"v_mfma_f32_16x16x32_bf16 a[136:139], v[144:147], v[96:99], a[136:139]\n" \
"s_add_u32 s52, s52, s74\n" \
"s_addc_u32 s53, s53, 0\n" \
"v_mfma_f32_16x16x32_bf16 a[136:139], v[148:151], v[100:103], a[136:139]\n" \
"s_add_u32 s50, s50, s74\n" \
"s_addc_u32 s51, s51, 0\n" \
"v_mfma_f32_16x16x32_bf16 a[140:143], v[152:155], v[96:99], a[140:143]\n" \
"v_mfma_f32_16x16x32_bf16 a[140:143], v[156:159], v[100:103], a[140:143]\n" \
"v_mfma_f32_16x16x32_bf16 a[172:175], v[152:155], v[104:107], a[172:175]\n" \
"s_mul_i32 s76, s73, 0x0\n" \
"s_add_u32 s77, s76, 0xffff8000\n" \
"s_sub_u32 s78, 0xffff8000, s76\n" \
"v_mfma_f32_16x16x32_bf16 a[172:175], v[156:159], v[108:111], a[172:175]\n" \
"v_add_u32 v48, s77, v48\n" \
"v_add_u32 v49, s77, v49\n" \
"v_mfma_f32_16x16x32_bf16 a[168:171], v[144:147], v[104:107], a[168:171]\n" \
"v_add_u32 v50, s78, v50\n" \
"v_add_u32 v51, s78, v51\n" \
"v_mfma_f32_16x16x32_bf16 a[168:171], v[148:151], v[108:111], a[168:171]\n" \
"v_mfma_f32_16x16x32_bf16 a[164:167], v[136:139], v[104:107], a[164:167]\n" \
"v_mfma_f32_16x16x32_bf16 a[164:167], v[140:143], v[108:111], a[164:167]\n" \
"v_mfma_f32_16x16x32_bf16 a[160:163], v[128:131], v[104:107], a[160:163]\n" \
"v_mfma_f32_16x16x32_bf16 a[160:163], v[132:135], v[108:111], a[160:163]\n" \
"s_waitcnt lgkmcnt(0)\n" \
"v_mfma_f32_16x16x32_bf16 a[192:195], v[128:131], v[112:115], a[192:195]\n" \
"v_mfma_f32_16x16x32_bf16 a[192:195], v[132:135], v[116:119], a[192:195]\n" \
"v_mfma_f32_16x16x32_bf16 a[196:199], v[136:139], v[112:115], a[196:199]\n" \
"v_mfma_f32_16x16x32_bf16 a[196:199], v[140:143], v[116:119], a[196:199]\n" \
"v_mfma_f32_16x16x32_bf16 a[200:203], v[144:147], v[112:115], a[200:203]\n" \
"v_mfma_f32_16x16x32_bf16 a[200:203], v[148:151], v[116:119], a[200:203]\n" \
"v_mfma_f32_16x16x32_bf16 a[204:207], v[152:155], v[112:115], a[204:207]\n" \
"v_mfma_f32_16x16x32_bf16 a[204:207], v[156:159], v[116:119], a[204:207]\n" \
"v_mfma_f32_16x16x32_bf16 a[236:239], v[152:155], v[120:123], a[236:239]\n" \
"v_mfma_f32_16x16x32_bf16 a[236:239], v[156:159], v[124:127], a[236:239]\n" \
"v_mfma_f32_16x16x32_bf16 a[232:235], v[144:147], v[120:123], a[232:235]\n" \
"v_mfma_f32_16x16x32_bf16 a[232:235], v[148:151], v[124:127], a[232:235]\n" \
"v_mfma_f32_16x16x32_bf16 a[228:231], v[136:139], v[120:123], a[228:231]\n" \
"v_mfma_f32_16x16x32_bf16 a[228:231], v[140:143], v[124:127], a[228:231]\n" \
"Lpg608_%=:\n" \
"Lst0x0_%=:\n" \
"s_cbranch_execz Lpg2766_%=\n" \
"s_waitcnt vmcnt(10) lgkmcnt(0)\n" \
"v_mfma_f32_16x16x32_bf16 a[224:227], v[128:131], v[120:123], a[224:227]\n" \
"v_mfma_f32_16x16x32_bf16 a[224:227], v[132:135], v[124:127], a[224:227]\n" \
"s_barrier\n" \
"v_mfma_f32_16x16x32_bf16 a[16:19], v[64:67], v[160:163], a[16:19]\n" \
"s_add_u32 m0, s87, 135168\n" \
"ds_read_b64_tr_b16 v[128:129], v50 offset:0\n" \
"v_readfirstlane_b32 s60, v244\n" \
"v_mfma_f32_16x16x32_bf16 a[16:19], v[68:71], v[164:167], a[16:19]\n" \
"s_add_u32 s60, s60, 0x9e3779b9\n" \
"global_load_lds_dwordx4 v32, s[54:55] offset:-4096\n" \
"v_mfma_f32_16x16x32_bf16 a[20:23], v[64:67], v[168:171], a[20:23]\n" \
"s_mul_i32 s88, s62, 557824\n" \
"s_mul_i32 s89, s63, 17408\n" \
"ds_read_b64_tr_b16 v[130:131], v50 offset:4096\n" \
"v_mfma_f32_16x16x32_bf16 a[20:23], v[68:71], v[172:175], a[20:23]\n" \
"s_add_u32 s88, s88, s89\n" \
"s_add_u32 s88, s88, s95\n" \
"s_add_u32 s89, s88, 17850368\n" \
"v_mfma_f32_16x16x32_bf16 a[24:27], v[64:67], v[176:179], a[24:27]\n" \
"s_lshl_b32 s91, s62, 7\n" \
"s_add_u32 s91, s91, 65585152\n" \
"ds_read_b64_tr_b16 v[132:133], v50 offset:16384\n" \
"v_mfma_f32_16x16x32_bf16 a[24:27], v[68:71], v[180:183], a[24:27]\n" \
"s_mov_b32 s92, 0x3e0293ee\n" \
"global_load_lds_dwordx4 v33, s[40:41] offset:-3072\n" \
"v_mfma_f32_16x16x32_bf16 a[28:31], v[64:67], v[184:187], a[28:31]\n" \
"ds_read_b64_tr_b16 v[134:135], v50 offset:20480\n" \
"v_mfma_f32_16x16x32_bf16 a[28:31], v[68:71], v[188:191], a[28:31]\n" \
"v_mfma_f32_16x16x32_bf16 a[60:63], v[72:75], v[184:187], a[60:63]\n" \
"ds_read_b128 v[64:67], v48 offset:0\n" \
"v_mfma_f32_16x16x32_bf16 a[60:63], v[76:79], v[188:191], a[60:63]\n" \
"global_load_lds_dwordx4 v34, s[54:55] offset:-2048\n" \
"v_mfma_f32_16x16x32_bf16 a[56:59], v[72:75], v[176:179], a[56:59]\n" \
"ds_read_b64_tr_b16 v[136:137], v51 offset:0\n" \
"v_mfma_f32_16x16x32_bf16 a[56:59], v[76:79], v[180:183], a[56:59]\n" \
"v_mfma_f32_16x16x32_bf16 a[52:55], v[72:75], v[168:171], a[52:55]\n" \
"ds_read_b128 v[68:71], v49 offset:0\n" \
"v_mfma_f32_16x16x32_bf16 a[52:55], v[76:79], v[172:175], a[52:55]\n" \
"global_load_lds_dwordx4 v35, s[40:41] offset:-1024\n" \
"v_mfma_f32_16x16x32_bf16 a[48:51], v[72:75], v[160:163], a[48:51]\n" \
"ds_read_b64_tr_b16 v[138:139], v51 offset:4096\n" \
"v_mfma_f32_16x16x32_bf16 a[48:51], v[76:79], v[164:167], a[48:51]\n" \
"v_mfma_f32_16x16x32_bf16 a[80:83], v[80:83], v[160:163], a[80:83]\n" \
"ds_read_b128 v[72:75], v48 offset:2048\n" \
"v_mfma_f32_16x16x32_bf16 a[80:83], v[84:87], v[164:167], a[80:83]\n" \
"global_load_lds_dwordx4 v36, s[54:55] offset:0\n" \
"v_mfma_f32_16x16x32_bf16 a[84:87], v[80:83], v[168:171], a[84:87]\n" \
"ds_read_b64_tr_b16 v[140:141], v51 offset:16384\n" \
"v_mfma_f32_16x16x32_bf16 a[84:87], v[84:87], v[172:175], a[84:87]\n" \
"v_mfma_f32_16x16x32_bf16 a[88:91], v[80:83], v[176:179], a[88:91]\n" \
"ds_read_b128 v[76:79], v49 offset:2048\n" \
"v_mfma_f32_16x16x32_bf16 a[88:91], v[84:87], v[180:183], a[88:91]\n" \
"global_load_lds_dwordx4 v37, s[40:41] offset:1024\n" \
"v_mfma_f32_16x16x32_bf16 a[92:95], v[80:83], v[184:187], a[92:95]\n" \
"ds_read_b64_tr_b16 v[142:143], v51 offset:20480\n" \
"v_mfma_f32_16x16x32_bf16 a[92:95], v[84:87], v[188:191], a[92:95]\n" \
"v_mfma_f32_16x16x32_bf16 a[124:127], v[88:91], v[184:187], a[124:127]\n" \
"ds_read_b128 v[80:83], v48 offset:4096\n" \
"v_mfma_f32_16x16x32_bf16 a[124:127], v[92:95], v[188:191], a[124:127]\n" \
"global_load_lds_dwordx4 v38, s[54:55] offset:2048\n" \
"v_mfma_f32_16x16x32_bf16 a[120:123], v[88:91], v[176:179], a[120:123]\n" \
"ds_read_b64_tr_b16 v[144:145], v50 offset:512\n" \
"v_mfma_f32_16x16x32_bf16 a[120:123], v[92:95], v[180:183], a[120:123]\n" \
"v_mfma_f32_16x16x32_bf16 a[116:119], v[88:91], v[168:171], a[116:119]\n" \
"ds_read_b128 v[84:87], v49 offset:4096\n" \
"global_load_lds_dwordx4 v39, s[40:41] offset:3072\n" \
"v_mfma_f32_16x16x32_bf16 a[116:119], v[92:95], v[172:175], a[116:119]\n" \
"s_add_u32 s54, s54, s75\n" \
"s_addc_u32 s55, s55, 0\n" \
"v_mfma_f32_16x16x32_bf16 a[112:115], v[88:91], v[160:163], a[112:115]\n" \
"s_add_u32 s40, s40, s75\n" \
"s_addc_u32 s41, s41, 0\n" \
"ds_read_b64_tr_b16 v[146:147], v50 offset:4608\n" \
"v_mfma_f32_16x16x32_bf16 a[112:115], v[92:95], v[164:167], a[112:115]\n" \
"s_add_u32 s68, s67, 0x200000\n" \
"s_add_u32 s64, s14, s68\n" \
"s_addc_u32 s65, s15, 0\n" \
"v_mfma_f32_16x16x32_bf16 a[144:147], v[96:99], v[160:163], a[144:147]\n" \
"ds_read_b128 v[88:91], v48 offset:6144\n" \
"v_mfma_f32_16x16x32_bf16 a[144:147], v[100:103], v[164:167], a[144:147]\n" \
"global_load_dword v253, v248, s[64:65]\n" \
"v_mfma_f32_16x16x32_bf16 a[148:151], v[96:99], v[168:171], a[148:151]\n" \
"s_add_u32 s68, s67, 0x200000\n" \
"ds_read_b64_tr_b16 v[148:149], v50 offset:16896\n" \
"v_mfma_f32_16x16x32_bf16 a[148:151], v[100:103], v[172:175], a[148:151]\n" \
"s_add_u32 s64, s16, s68\n" \
"s_addc_u32 s65, s17, 0\n" \
"v_mfma_f32_16x16x32_bf16 a[152:155], v[96:99], v[176:179], a[152:155]\n" \
"ds_read_b128 v[92:95], v49 offset:6144\n" \
"v_mfma_f32_16x16x32_bf16 a[152:155], v[100:103], v[180:183], a[152:155]\n" \
"global_load_dword v253, v248, s[64:65]\n" \
"v_mfma_f32_16x16x32_bf16 a[156:159], v[96:99], v[184:187], a[156:159]\n" \
"ds_read_b64_tr_b16 v[150:151], v50 offset:20992\n" \
"v_mfma_f32_16x16x32_bf16 a[156:159], v[100:103], v[188:191], a[156:159]\n" \
"v_mfma_f32_16x16x32_bf16 a[188:191], v[104:107], v[184:187], a[188:191]\n" \
"ds_read_b128 v[96:99], v48 offset:8192\n" \
"v_mfma_f32_16x16x32_bf16 a[188:191], v[108:111], v[188:191], a[188:191]\n" \
"v_mfma_f32_16x16x32_bf16 a[184:187], v[104:107], v[176:179], a[184:187]\n" \
"ds_read_b64_tr_b16 v[152:153], v51 offset:512\n" \
"v_mfma_f32_16x16x32_bf16 a[184:187], v[108:111], v[180:183], a[184:187]\n" \
"v_mfma_f32_16x16x32_bf16 a[180:183], v[104:107], v[168:171], a[180:183]\n" \
"ds_read_b128 v[100:103], v49 offset:8192\n" \
"v_mfma_f32_16x16x32_bf16 a[180:183], v[108:111], v[172:175], a[180:183]\n" \
"v_mfma_f32_16x16x32_bf16 a[176:179], v[104:107], v[160:163], a[176:179]\n" \
"ds_read_b64_tr_b16 v[154:155], v51 offset:4608\n" \
"v_mfma_f32_16x16x32_bf16 a[176:179], v[108:111], v[164:167], a[176:179]\n" \
"v_mfma_f32_16x16x32_bf16 a[208:211], v[112:115], v[160:163], a[208:211]\n" \
"ds_read_b128 v[104:107], v48 offset:10240\n" \
"v_mfma_f32_16x16x32_bf16 a[208:211], v[116:119], v[164:167], a[208:211]\n" \
"v_mfma_f32_16x16x32_bf16 a[212:215], v[112:115], v[168:171], a[212:215]\n" \
"ds_read_b64_tr_b16 v[156:157], v51 offset:16896\n" \
"v_mfma_f32_16x16x32_bf16 a[212:215], v[116:119], v[172:175], a[212:215]\n" \
"v_mfma_f32_16x16x32_bf16 a[216:219], v[112:115], v[176:179], a[216:219]\n" \
"ds_read_b128 v[108:111], v49 offset:10240\n" \
"v_mfma_f32_16x16x32_bf16 a[216:219], v[116:119], v[180:183], a[216:219]\n" \
"v_mfma_f32_16x16x32_bf16 a[220:223], v[112:115], v[184:187], a[220:223]\n" \
"ds_read_b64_tr_b16 v[158:159], v51 offset:20992\n" \
"v_mfma_f32_16x16x32_bf16 a[220:223], v[116:119], v[188:191], a[220:223]\n" \
"v_mfma_f32_16x16x32_bf16 a[252:255], v[120:123], v[184:187], a[252:255]\n" \
"ds_read_b128 v[112:115], v48 offset:12288\n" \
"v_mfma_f32_16x16x32_bf16 a[252:255], v[124:127], v[188:191], a[252:255]\n" \
"v_mfma_f32_16x16x32_bf16 a[248:251], v[120:123], v[176:179], a[248:251]\n" \
"v_mfma_f32_16x16x32_bf16 a[248:251], v[124:127], v[180:183], a[248:251]\n" \
"v_mfma_f32_16x16x32_bf16 a[244:247], v[120:123], v[168:171], a[244:247]\n" \
"ds_read_b128 v[116:119], v49 offset:12288\n" \
"v_mfma_f32_16x16x32_bf16 a[244:247], v[124:127], v[172:175], a[244:247]\n" \
"v_mfma_f32_16x16x32_bf16 a[240:243], v[120:123], v[160:163], a[240:243]\n" \
"v_mfma_f32_16x16x32_bf16 a[240:243], v[124:127], v[164:167], a[240:243]\n" \
"s_waitcnt lgkmcnt(1)\n" \
"v_mfma_f32_16x16x32_bf16 a[0:3], v[128:131], v[64:67], a[0:3]\n" \
"ds_read_b128 v[120:123], v48 offset:14336\n" \
"v_mfma_f32_16x16x32_bf16 a[0:3], v[132:135], v[68:71], a[0:3]\n" \
"v_mfma_f32_16x16x32_bf16 a[4:7], v[136:139], v[64:67], a[4:7]\n" \
"ds_read_b128 v[124:127], v49 offset:14336\n" \
"v_mfma_f32_16x16x32_bf16 a[4:7], v[140:143], v[68:71], a[4:7]\n" \
"s_add_u32 s68, s67, 0x300000\n" \
"v_mfma_f32_16x16x32_bf16 a[8:11], v[144:147], v[64:67], a[8:11]\n" \
"s_add_u32 s64, s14, s68\n" \
"s_addc_u32 s65, s15, 0\n" \
"ds_read_b64_tr_b16 v[160:161], v50 offset:1024\n" \
"v_mfma_f32_16x16x32_bf16 a[8:11], v[148:151], v[68:71], a[8:11]\n" \
"global_load_dword v253, v248, s[64:65]\n" \
"v_mfma_f32_16x16x32_bf16 a[12:15], v[152:155], v[64:67], a[12:15]\n" \
"s_add_u32 m0, s87, 69632\n" \
"ds_read_b64_tr_b16 v[162:163], v50 offset:5120\n" \
"v_mfma_f32_16x16x32_bf16 a[12:15], v[156:159], v[68:71], a[12:15]\n" \
"global_load_lds_dwordx4 v40, s[52:53] offset:-4096\n" \
"v_mfma_f32_16x16x32_bf16 a[44:47], v[152:155], v[72:75], a[44:47]\n" \
"ds_read_b64_tr_b16 v[164:165], v50 offset:17408\n" \
"v_mfma_f32_16x16x32_bf16 a[44:47], v[156:159], v[76:79], a[44:47]\n" \
"v_mfma_f32_16x16x32_bf16 a[40:43], v[144:147], v[72:75], a[40:43]\n" \
"ds_read_b64_tr_b16 v[166:167], v50 offset:21504\n" \
"v_mfma_f32_16x16x32_bf16 a[40:43], v[148:151], v[76:79], a[40:43]\n" \
"global_load_lds_dwordx4 v41, s[50:51] offset:-3072\n" \
"v_mfma_f32_16x16x32_bf16 a[36:39], v[136:139], v[72:75], a[36:39]\n" \
"ds_read_b64_tr_b16 v[168:169], v51 offset:1024\n" \
"v_mfma_f32_16x16x32_bf16 a[36:39], v[140:143], v[76:79], a[36:39]\n" \
"v_mfma_f32_16x16x32_bf16 a[32:35], v[128:131], v[72:75], a[32:35]\n" \
"ds_read_b64_tr_b16 v[170:171], v51 offset:5120\n" \
"v_mfma_f32_16x16x32_bf16 a[32:35], v[132:135], v[76:79], a[32:35]\n" \
"global_load_lds_dwordx4 v42, s[52:53] offset:-2048\n" \
"v_mfma_f32_16x16x32_bf16 a[64:67], v[128:131], v[80:83], a[64:67]\n" \
"ds_read_b64_tr_b16 v[172:173], v51 offset:17408\n" \
"v_mfma_f32_16x16x32_bf16 a[64:67], v[132:135], v[84:87], a[64:67]\n" \
"v_mfma_f32_16x16x32_bf16 a[68:71], v[136:139], v[80:83], a[68:71]\n" \
"ds_read_b64_tr_b16 v[174:175], v51 offset:21504\n" \
"v_mfma_f32_16x16x32_bf16 a[68:71], v[140:143], v[84:87], a[68:71]\n" \
"global_load_lds_dwordx4 v43, s[50:51] offset:-1024\n" \
"v_mfma_f32_16x16x32_bf16 a[72:75], v[144:147], v[80:83], a[72:75]\n" \
"ds_read_b64_tr_b16 v[176:177], v50 offset:1536\n" \
"v_mfma_f32_16x16x32_bf16 a[72:75], v[148:151], v[84:87], a[72:75]\n" \
"v_mfma_f32_16x16x32_bf16 a[76:79], v[152:155], v[80:83], a[76:79]\n" \
"ds_read_b64_tr_b16 v[178:179], v50 offset:5632\n" \
"v_mfma_f32_16x16x32_bf16 a[76:79], v[156:159], v[84:87], a[76:79]\n" \
"global_load_lds_dwordx4 v44, s[52:53] offset:0\n" \
"v_mfma_f32_16x16x32_bf16 a[108:111], v[152:155], v[88:91], a[108:111]\n" \
"ds_read_b64_tr_b16 v[180:181], v50 offset:17920\n" \
"v_mfma_f32_16x16x32_bf16 a[108:111], v[156:159], v[92:95], a[108:111]\n" \
"v_mfma_f32_16x16x32_bf16 a[104:107], v[144:147], v[88:91], a[104:107]\n" \
"ds_read_b64_tr_b16 v[182:183], v50 offset:22016\n" \
"v_mfma_f32_16x16x32_bf16 a[104:107], v[148:151], v[92:95], a[104:107]\n" \
"global_load_lds_dwordx4 v45, s[50:51] offset:1024\n" \
"v_mfma_f32_16x16x32_bf16 a[100:103], v[136:139], v[88:91], a[100:103]\n" \
"ds_read_b64_tr_b16 v[184:185], v51 offset:1536\n" \
"v_mfma_f32_16x16x32_bf16 a[100:103], v[140:143], v[92:95], a[100:103]\n" \
"v_mfma_f32_16x16x32_bf16 a[96:99], v[128:131], v[88:91], a[96:99]\n" \
"ds_read_b64_tr_b16 v[186:187], v51 offset:5632\n" \
"v_mfma_f32_16x16x32_bf16 a[96:99], v[132:135], v[92:95], a[96:99]\n" \
"global_load_lds_dwordx4 v46, s[52:53] offset:2048\n" \
"v_mfma_f32_16x16x32_bf16 a[128:131], v[128:131], v[96:99], a[128:131]\n" \
"ds_read_b64_tr_b16 v[188:189], v51 offset:17920\n" \
"v_mfma_f32_16x16x32_bf16 a[128:131], v[132:135], v[100:103], a[128:131]\n" \
"v_mfma_f32_16x16x32_bf16 a[132:135], v[136:139], v[96:99], a[132:135]\n" \
"ds_read_b64_tr_b16 v[190:191], v51 offset:22016\n" \
"v_mfma_f32_16x16x32_bf16 a[132:135], v[140:143], v[100:103], a[132:135]\n" \
"global_load_lds_dwordx4 v47, s[50:51] offset:3072\n" \
"v_mfma_f32_16x16x32_bf16 a[136:139], v[144:147], v[96:99], a[136:139]\n" \
"s_add_u32 s52, s52, s74\n" \
"s_addc_u32 s53, s53, 0\n" \
"v_mfma_f32_16x16x32_bf16 a[136:139], v[148:151], v[100:103], a[136:139]\n" \
"s_add_u32 s50, s50, s74\n" \
"s_addc_u32 s51, s51, 0\n" \
"v_mfma_f32_16x16x32_bf16 a[140:143], v[152:155], v[96:99], a[140:143]\n" \
"v_mfma_f32_16x16x32_bf16 a[140:143], v[156:159], v[100:103], a[140:143]\n" \
"v_mfma_f32_16x16x32_bf16 a[172:175], v[152:155], v[104:107], a[172:175]\n" \
"s_mul_i32 s76, s73, 0xfffd0000\n" \
"s_add_u32 s77, s76, 0x18000\n" \
"s_sub_u32 s78, 0xfffe8000, s76\n" \
"v_mfma_f32_16x16x32_bf16 a[172:175], v[156:159], v[108:111], a[172:175]\n" \
"v_add_u32 v48, s77, v48\n" \
"v_add_u32 v49, s77, v49\n" \
"v_mfma_f32_16x16x32_bf16 a[168:171], v[144:147], v[104:107], a[168:171]\n" \
"v_add_u32 v50, s78, v50\n" \
"v_add_u32 v51, s78, v51\n" \
"v_mfma_f32_16x16x32_bf16 a[168:171], v[148:151], v[108:111], a[168:171]\n" \
"v_mfma_f32_16x16x32_bf16 a[164:167], v[136:139], v[104:107], a[164:167]\n" \
"v_mfma_f32_16x16x32_bf16 a[164:167], v[140:143], v[108:111], a[164:167]\n" \
"v_mfma_f32_16x16x32_bf16 a[160:163], v[128:131], v[104:107], a[160:163]\n" \
"v_mfma_f32_16x16x32_bf16 a[160:163], v[132:135], v[108:111], a[160:163]\n" \
"s_waitcnt lgkmcnt(0)\n" \
"v_mfma_f32_16x16x32_bf16 a[192:195], v[128:131], v[112:115], a[192:195]\n" \
"v_mfma_f32_16x16x32_bf16 a[192:195], v[132:135], v[116:119], a[192:195]\n" \
"v_mfma_f32_16x16x32_bf16 a[196:199], v[136:139], v[112:115], a[196:199]\n" \
"v_mfma_f32_16x16x32_bf16 a[196:199], v[140:143], v[116:119], a[196:199]\n" \
"v_mfma_f32_16x16x32_bf16 a[200:203], v[144:147], v[112:115], a[200:203]\n" \
"v_mfma_f32_16x16x32_bf16 a[200:203], v[148:151], v[116:119], a[200:203]\n" \
"v_mfma_f32_16x16x32_bf16 a[204:207], v[152:155], v[112:115], a[204:207]\n" \
"v_mfma_f32_16x16x32_bf16 a[204:207], v[156:159], v[116:119], a[204:207]\n" \
"v_mfma_f32_16x16x32_bf16 a[236:239], v[152:155], v[120:123], a[236:239]\n" \
"v_mfma_f32_16x16x32_bf16 a[236:239], v[156:159], v[124:127], a[236:239]\n" \
"v_mfma_f32_16x16x32_bf16 a[232:235], v[144:147], v[120:123], a[232:235]\n" \
"v_mfma_f32_16x16x32_bf16 a[232:235], v[148:151], v[124:127], a[232:235]\n" \
"v_mfma_f32_16x16x32_bf16 a[228:231], v[136:139], v[120:123], a[228:231]\n" \
"v_mfma_f32_16x16x32_bf16 a[228:231], v[140:143], v[124:127], a[228:231]\n" \
"s_waitcnt vmcnt(11) lgkmcnt(0)\n" \
"v_mfma_f32_16x16x32_bf16 a[224:227], v[128:131], v[120:123], a[224:227]\n" \
"v_mfma_f32_16x16x32_bf16 a[224:227], v[132:135], v[124:127], a[224:227]\n" \
"s_barrier\n" \
"v_mfma_f32_16x16x32_bf16 a[16:19], v[64:67], v[160:163], a[16:19]\n" \
"s_add_u32 m0, s87, 102400\n" \
"ds_read_b64_tr_b16 v[128:129], v50 offset:0\n" \
"v_mfma_f32_16x16x32_bf16 a[16:19], v[68:71], v[164:167], a[16:19]\n" \
"global_load_lds_dwordx4 v32, s[54:55] offset:-4096\n" \
"v_mfma_f32_16x16x32_bf16 a[20:23], v[64:67], v[168:171], a[20:23]\n" \
"ds_read_b64_tr_b16 v[130:131], v50 offset:4096\n" \
"v_mfma_f32_16x16x32_bf16 a[20:23], v[68:71], v[172:175], a[20:23]\n" \
"v_mfma_f32_16x16x32_bf16 a[24:27], v[64:67], v[176:179], a[24:27]\n" \
"ds_read_b64_tr_b16 v[132:133], v50 offset:16384\n" \
"v_mfma_f32_16x16x32_bf16 a[24:27], v[68:71], v[180:183], a[24:27]\n" \
"global_load_lds_dwordx4 v33, s[40:41] offset:-3072\n" \
"v_mfma_f32_16x16x32_bf16 a[28:31], v[64:67], v[184:187], a[28:31]\n" \
"ds_read_b64_tr_b16 v[134:135], v50 offset:20480\n" \
"v_mfma_f32_16x16x32_bf16 a[28:31], v[68:71], v[188:191], a[28:31]\n" \
"v_mfma_f32_16x16x32_bf16 a[60:63], v[72:75], v[184:187], a[60:63]\n" \
"ds_read_b128 v[64:67], v48 offset:0\n" \
"v_mfma_f32_16x16x32_bf16 a[60:63], v[76:79], v[188:191], a[60:63]\n" \
"global_load_lds_dwordx4 v34, s[54:55] offset:-2048\n" \
"v_mfma_f32_16x16x32_bf16 a[56:59], v[72:75], v[176:179], a[56:59]\n" \
"ds_read_b64_tr_b16 v[136:137], v51 offset:0\n" \
"v_mfma_f32_16x16x32_bf16 a[56:59], v[76:79], v[180:183], a[56:59]\n" \
"v_mfma_f32_16x16x32_bf16 a[52:55], v[72:75], v[168:171], a[52:55]\n" \
"ds_read_b128 v[68:71], v49 offset:0\n" \
"v_mfma_f32_16x16x32_bf16 a[52:55], v[76:79], v[172:175], a[52:55]\n" \
"global_load_lds_dwordx4 v35, s[40:41] offset:-1024\n" \
"v_mfma_f32_16x16x32_bf16 a[48:51], v[72:75], v[160:163], a[48:51]\n" \
"ds_read_b64_tr_b16 v[138:139], v51 offset:4096\n" \
"v_mfma_f32_16x16x32_bf16 a[48:51], v[76:79], v[164:167], a[48:51]\n" \
"v_mfma_f32_16x16x32_bf16 a[80:83], v[80:83], v[160:163], a[80:83]\n" \
"ds_read_b128 v[72:75], v48 offset:2048\n" \
"v_mfma_f32_16x16x32_bf16 a[80:83], v[84:87], v[164:167], a[80:83]\n" \
"global_load_lds_dwordx4 v36, s[54:55] offset:0\n" \
"v_mfma_f32_16x16x32_bf16 a[84:87], v[80:83], v[168:171], a[84:87]\n" \
"ds_read_b64_tr_b16 v[140:141], v51 offset:16384\n" \
"v_mfma_f32_16x16x32_bf16 a[84:87], v[84:87], v[172:175], a[84:87]\n" \
"v_mfma_f32_16x16x32_bf16 a[88:91], v[80:83], v[176:179], a[88:91]\n" \
"ds_read_b128 v[76:79], v49 offset:2048\n" \
"v_mfma_f32_16x16x32_bf16 a[88:91], v[84:87], v[180:183], a[88:91]\n" \
"global_load_lds_dwordx4 v37, s[40:41] offset:1024\n" \
"v_mfma_f32_16x16x32_bf16 a[92:95], v[80:83], v[184:187], a[92:95]\n" \
"ds_read_b64_tr_b16 v[142:143], v51 offset:20480\n" \
"v_mfma_f32_16x16x32_bf16 a[92:95], v[84:87], v[188:191], a[92:95]\n" \
"v_mfma_f32_16x16x32_bf16 a[124:127], v[88:91], v[184:187], a[124:127]\n" \
"ds_read_b128 v[80:83], v48 offset:4096\n" \
"v_mfma_f32_16x16x32_bf16 a[124:127], v[92:95], v[188:191], a[124:127]\n" \
"global_load_lds_dwordx4 v38, s[54:55] offset:2048\n" \
"v_mfma_f32_16x16x32_bf16 a[120:123], v[88:91], v[176:179], a[120:123]\n" \
"ds_read_b64_tr_b16 v[144:145], v50 offset:512\n" \
"v_mfma_f32_16x16x32_bf16 a[120:123], v[92:95], v[180:183], a[120:123]\n" \
"v_mfma_f32_16x16x32_bf16 a[116:119], v[88:91], v[168:171], a[116:119]\n" \
"ds_read_b128 v[84:87], v49 offset:4096\n" \
"global_load_lds_dwordx4 v39, s[40:41] offset:3072\n" \
"v_mfma_f32_16x16x32_bf16 a[116:119], v[92:95], v[172:175], a[116:119]\n" \
"s_add_u32 s54, s54, s75\n" \
"s_addc_u32 s55, s55, 0\n" \
"v_mfma_f32_16x16x32_bf16 a[112:115], v[88:91], v[160:163], a[112:115]\n" \
"s_add_u32 s40, s40, s75\n" \
"s_addc_u32 s41, s41, 0\n" \
"ds_read_b64_tr_b16 v[146:147], v50 offset:4608\n" \
"v_mfma_f32_16x16x32_bf16 a[112:115], v[92:95], v[164:167], a[112:115]\n" \
"s_add_u32 s68, s67, 0x300000\n" \
"s_add_u32 s64, s16, s68\n" \
"s_addc_u32 s65, s17, 0\n" \
"v_mfma_f32_16x16x32_bf16 a[144:147], v[96:99], v[160:163], a[144:147]\n" \
"ds_read_b128 v[88:91], v48 offset:6144\n" \
"v_mfma_f32_16x16x32_bf16 a[144:147], v[100:103], v[164:167], a[144:147]\n" \
"global_load_dword v253, v248, s[64:65]\n" \
"v_mfma_f32_16x16x32_bf16 a[148:151], v[96:99], v[168:171], a[148:151]\n" \
"s_add_u32 s68, s66, 0x0\n" \
"ds_read_b64_tr_b16 v[148:149], v50 offset:16896\n" \
"v_mfma_f32_16x16x32_bf16 a[148:151], v[100:103], v[172:175], a[148:151]\n" \
"s_add_u32 s64, s8, s68\n" \
"s_addc_u32 s65, s9, 0\n" \
"v_mfma_f32_16x16x32_bf16 a[152:155], v[96:99], v[176:179], a[152:155]\n" \
"ds_read_b128 v[92:95], v49 offset:6144\n" \
"v_mfma_f32_16x16x32_bf16 a[152:155], v[100:103], v[180:183], a[152:155]\n" \
"global_load_dword v253, v248, s[64:65]\n" \
"v_mfma_f32_16x16x32_bf16 a[156:159], v[96:99], v[184:187], a[156:159]\n" \
"ds_read_b64_tr_b16 v[150:151], v50 offset:20992\n" \
"v_mfma_f32_16x16x32_bf16 a[156:159], v[100:103], v[188:191], a[156:159]\n" \
"v_mfma_f32_16x16x32_bf16 a[188:191], v[104:107], v[184:187], a[188:191]\n" \
"ds_read_b128 v[96:99], v48 offset:8192\n" \
"v_mfma_f32_16x16x32_bf16 a[188:191], v[108:111], v[188:191], a[188:191]\n" \
"v_mfma_f32_16x16x32_bf16 a[184:187], v[104:107], v[176:179], a[184:187]\n" \
"ds_read_b64_tr_b16 v[152:153], v51 offset:512\n" \
"v_mfma_f32_16x16x32_bf16 a[184:187], v[108:111], v[180:183], a[184:187]\n" \
"v_mfma_f32_16x16x32_bf16 a[180:183], v[104:107], v[168:171], a[180:183]\n" \
"ds_read_b128 v[100:103], v49 offset:8192\n" \
"v_mfma_f32_16x16x32_bf16 a[180:183], v[108:111], v[172:175], a[180:183]\n" \
"v_mfma_f32_16x16x32_bf16 a[176:179], v[104:107], v[160:163], a[176:179]\n" \
"ds_read_b64_tr_b16 v[154:155], v51 offset:4608\n" \
"v_mfma_f32_16x16x32_bf16 a[176:179], v[108:111], v[164:167], a[176:179]\n" \
"v_mfma_f32_16x16x32_bf16 a[208:211], v[112:115], v[160:163], a[208:211]\n" \
"ds_read_b128 v[104:107], v48 offset:10240\n" \
"v_mfma_f32_16x16x32_bf16 a[208:211], v[116:119], v[164:167], a[208:211]\n" \
"v_mfma_f32_16x16x32_bf16 a[212:215], v[112:115], v[168:171], a[212:215]\n" \
"ds_read_b64_tr_b16 v[156:157], v51 offset:16896\n" \
"v_mfma_f32_16x16x32_bf16 a[212:215], v[116:119], v[172:175], a[212:215]\n" \
"v_mfma_f32_16x16x32_bf16 a[216:219], v[112:115], v[176:179], a[216:219]\n" \
"ds_read_b128 v[108:111], v49 offset:10240\n" \
"v_mfma_f32_16x16x32_bf16 a[216:219], v[116:119], v[180:183], a[216:219]\n" \
"v_mfma_f32_16x16x32_bf16 a[220:223], v[112:115], v[184:187], a[220:223]\n" \
"ds_read_b64_tr_b16 v[158:159], v51 offset:20992\n" \
"v_mfma_f32_16x16x32_bf16 a[220:223], v[116:119], v[188:191], a[220:223]\n" \
"v_mfma_f32_16x16x32_bf16 a[252:255], v[120:123], v[184:187], a[252:255]\n" \
"ds_read_b128 v[112:115], v48 offset:12288\n" \
"v_mfma_f32_16x16x32_bf16 a[252:255], v[124:127], v[188:191], a[252:255]\n" \
"v_mfma_f32_16x16x32_bf16 a[248:251], v[120:123], v[176:179], a[248:251]\n" \
"v_mfma_f32_16x16x32_bf16 a[248:251], v[124:127], v[180:183], a[248:251]\n" \
"v_mfma_f32_16x16x32_bf16 a[244:247], v[120:123], v[168:171], a[244:247]\n" \
"ds_read_b128 v[116:119], v49 offset:12288\n" \
"v_mfma_f32_16x16x32_bf16 a[244:247], v[124:127], v[172:175], a[244:247]\n" \
"v_mfma_f32_16x16x32_bf16 a[240:243], v[120:123], v[160:163], a[240:243]\n" \
"v_mfma_f32_16x16x32_bf16 a[240:243], v[124:127], v[164:167], a[240:243]\n" \
"s_waitcnt lgkmcnt(1)\n" \
"v_mfma_f32_16x16x32_bf16 a[0:3], v[128:131], v[64:67], a[0:3]\n" \
"ds_read_b128 v[120:123], v48 offset:14336\n" \
"v_mfma_f32_16x16x32_bf16 a[0:3], v[132:135], v[68:71], a[0:3]\n" \
"v_mfma_f32_16x16x32_bf16 a[4:7], v[136:139], v[64:67], a[4:7]\n" \
"ds_read_b128 v[124:127], v49 offset:14336\n" \
"v_mfma_f32_16x16x32_bf16 a[4:7], v[140:143], v[68:71], a[4:7]\n" \
"s_add_u32 s68, s66, 0x100000\n" \
"v_mfma_f32_16x16x32_bf16 a[8:11], v[144:147], v[64:67], a[8:11]\n" \
"s_add_u32 s64, s8, s68\n" \
"s_addc_u32 s65, s9, 0\n" \
"ds_read_b64_tr_b16 v[160:161], v50 offset:1024\n" \
"v_mfma_f32_16x16x32_bf16 a[8:11], v[148:151], v[68:71], a[8:11]\n" \
"global_load_dword v253, v248, s[64:65]\n" \
"v_mfma_f32_16x16x32_bf16 a[12:15], v[152:155], v[64:67], a[12:15]\n" \
"s_add_u32 m0, s87, 36864\n" \
"ds_read_b64_tr_b16 v[162:163], v50 offset:5120\n" \
"v_mfma_f32_16x16x32_bf16 a[12:15], v[156:159], v[68:71], a[12:15]\n" \
"global_load_lds_dwordx4 v40, s[52:53] offset:-4096\n" \
"v_mfma_f32_16x16x32_bf16 a[44:47], v[152:155], v[72:75], a[44:47]\n" \
"ds_read_b64_tr_b16 v[164:165], v50 offset:17408\n" \
"v_mfma_f32_16x16x32_bf16 a[44:47], v[156:159], v[76:79], a[44:47]\n" \
"v_mfma_f32_16x16x32_bf16 a[40:43], v[144:147], v[72:75], a[40:43]\n" \
"ds_read_b64_tr_b16 v[166:167], v50 offset:21504\n" \
"v_mfma_f32_16x16x32_bf16 a[40:43], v[148:151], v[76:79], a[40:43]\n" \
"global_load_lds_dwordx4 v41, s[50:51] offset:-3072\n" \
"v_mfma_f32_16x16x32_bf16 a[36:39], v[136:139], v[72:75], a[36:39]\n" \
"ds_read_b64_tr_b16 v[168:169], v51 offset:1024\n" \
"v_mfma_f32_16x16x32_bf16 a[36:39], v[140:143], v[76:79], a[36:39]\n" \
"v_mfma_f32_16x16x32_bf16 a[32:35], v[128:131], v[72:75], a[32:35]\n" \
"ds_read_b64_tr_b16 v[170:171], v51 offset:5120\n" \
"v_mfma_f32_16x16x32_bf16 a[32:35], v[132:135], v[76:79], a[32:35]\n" \
"global_load_lds_dwordx4 v42, s[52:53] offset:-2048\n" \
"v_mfma_f32_16x16x32_bf16 a[64:67], v[128:131], v[80:83], a[64:67]\n" \
"ds_read_b64_tr_b16 v[172:173], v51 offset:17408\n" \
"v_mfma_f32_16x16x32_bf16 a[64:67], v[132:135], v[84:87], a[64:67]\n" \
"v_mfma_f32_16x16x32_bf16 a[68:71], v[136:139], v[80:83], a[68:71]\n" \
"ds_read_b64_tr_b16 v[174:175], v51 offset:21504\n" \
"v_mfma_f32_16x16x32_bf16 a[68:71], v[140:143], v[84:87], a[68:71]\n" \
"global_load_lds_dwordx4 v43, s[50:51] offset:-1024\n" \
"v_mfma_f32_16x16x32_bf16 a[72:75], v[144:147], v[80:83], a[72:75]\n" \
"ds_read_b64_tr_b16 v[176:177], v50 offset:1536\n" \
"v_mfma_f32_16x16x32_bf16 a[72:75], v[148:151], v[84:87], a[72:75]\n" \
"v_mfma_f32_16x16x32_bf16 a[76:79], v[152:155], v[80:83], a[76:79]\n" \
"ds_read_b64_tr_b16 v[178:179], v50 offset:5632\n" \
"v_mfma_f32_16x16x32_bf16 a[76:79], v[156:159], v[84:87], a[76:79]\n" \
"global_load_lds_dwordx4 v44, s[52:53] offset:0\n" \
"v_mfma_f32_16x16x32_bf16 a[108:111], v[152:155], v[88:91], a[108:111]\n" \
"ds_read_b64_tr_b16 v[180:181], v50 offset:17920\n" \
"v_mfma_f32_16x16x32_bf16 a[108:111], v[156:159], v[92:95], a[108:111]\n" \
"v_mfma_f32_16x16x32_bf16 a[104:107], v[144:147], v[88:91], a[104:107]\n" \
"ds_read_b64_tr_b16 v[182:183], v50 offset:22016\n" \
"v_mfma_f32_16x16x32_bf16 a[104:107], v[148:151], v[92:95], a[104:107]\n" \
"global_load_lds_dwordx4 v45, s[50:51] offset:1024\n" \
"v_mfma_f32_16x16x32_bf16 a[100:103], v[136:139], v[88:91], a[100:103]\n" \
"ds_read_b64_tr_b16 v[184:185], v51 offset:1536\n" \
"v_mfma_f32_16x16x32_bf16 a[100:103], v[140:143], v[92:95], a[100:103]\n" \
"v_mfma_f32_16x16x32_bf16 a[96:99], v[128:131], v[88:91], a[96:99]\n" \
"ds_read_b64_tr_b16 v[186:187], v51 offset:5632\n" \
"v_mfma_f32_16x16x32_bf16 a[96:99], v[132:135], v[92:95], a[96:99]\n" \
"global_load_lds_dwordx4 v46, s[52:53] offset:2048\n" \
"v_mfma_f32_16x16x32_bf16 a[128:131], v[128:131], v[96:99], a[128:131]\n" \
"ds_read_b64_tr_b16 v[188:189], v51 offset:17920\n" \
"v_mfma_f32_16x16x32_bf16 a[128:131], v[132:135], v[100:103], a[128:131]\n" \
"v_mfma_f32_16x16x32_bf16 a[132:135], v[136:139], v[96:99], a[132:135]\n" \
"ds_read_b64_tr_b16 v[190:191], v51 offset:22016\n" \
"v_mfma_f32_16x16x32_bf16 a[132:135], v[140:143], v[100:103], a[132:135]\n" \
"global_load_lds_dwordx4 v47, s[50:51] offset:3072\n" \
"v_mfma_f32_16x16x32_bf16 a[136:139], v[144:147], v[96:99], a[136:139]\n" \
"s_add_u32 s52, s52, s74\n" \
"s_addc_u32 s53, s53, 0\n" \
"v_mfma_f32_16x16x32_bf16 a[136:139], v[148:151], v[100:103], a[136:139]\n" \
"s_add_u32 s50, s50, s74\n" \
"s_addc_u32 s51, s51, 0\n" \
"v_mfma_f32_16x16x32_bf16 a[140:143], v[152:155], v[96:99], a[140:143]\n" \
"v_mfma_f32_16x16x32_bf16 a[140:143], v[156:159], v[100:103], a[140:143]\n" \
"v_mfma_f32_16x16x32_bf16 a[172:175], v[152:155], v[104:107], a[172:175]\n" \
"s_mul_i32 s76, s73, 0x18000\n" \
"s_add_u32 s77, s76, 0xffff8000\n" \
"s_sub_u32 s78, 0x10000, s76\n" \
"v_mfma_f32_16x16x32_bf16 a[172:175], v[156:159], v[108:111], a[172:175]\n" \
"v_add_u32 v48, s77, v48\n" \
"v_add_u32 v49, s77, v49\n" \
"v_mfma_f32_16x16x32_bf16 a[168:171], v[144:147], v[104:107], a[168:171]\n" \
"v_add_u32 v50, s78, v50\n" \
"v_add_u32 v51, s78, v51\n" \
"v_mfma_f32_16x16x32_bf16 a[168:171], v[148:151], v[108:111], a[168:171]\n" \
"v_mfma_f32_16x16x32_bf16 a[164:167], v[136:139], v[104:107], a[164:167]\n" \
"v_mfma_f32_16x16x32_bf16 a[164:167], v[140:143], v[108:111], a[164:167]\n" \
"v_mfma_f32_16x16x32_bf16 a[160:163], v[128:131], v[104:107], a[160:163]\n" \
"v_mfma_f32_16x16x32_bf16 a[160:163], v[132:135], v[108:111], a[160:163]\n" \
"s_waitcnt lgkmcnt(0)\n" \
"v_mfma_f32_16x16x32_bf16 a[192:195], v[128:131], v[112:115], a[192:195]\n" \
"v_mfma_f32_16x16x32_bf16 a[192:195], v[132:135], v[116:119], a[192:195]\n" \
"v_mfma_f32_16x16x32_bf16 a[196:199], v[136:139], v[112:115], a[196:199]\n" \
"v_mfma_f32_16x16x32_bf16 a[196:199], v[140:143], v[116:119], a[196:199]\n" \
"v_mfma_f32_16x16x32_bf16 a[200:203], v[144:147], v[112:115], a[200:203]\n" \
"v_mfma_f32_16x16x32_bf16 a[200:203], v[148:151], v[116:119], a[200:203]\n" \
"v_mfma_f32_16x16x32_bf16 a[204:207], v[152:155], v[112:115], a[204:207]\n" \
"v_mfma_f32_16x16x32_bf16 a[204:207], v[156:159], v[116:119], a[204:207]\n" \
"v_mfma_f32_16x16x32_bf16 a[236:239], v[152:155], v[120:123], a[236:239]\n" \
"v_mfma_f32_16x16x32_bf16 a[236:239], v[156:159], v[124:127], a[236:239]\n" \
"v_mfma_f32_16x16x32_bf16 a[232:235], v[144:147], v[120:123], a[232:235]\n" \
"v_mfma_f32_16x16x32_bf16 a[232:235], v[148:151], v[124:127], a[232:235]\n" \
"v_mfma_f32_16x16x32_bf16 a[228:231], v[136:139], v[120:123], a[228:231]\n" \
"v_mfma_f32_16x16x32_bf16 a[228:231], v[140:143], v[124:127], a[228:231]\n" \
"s_waitcnt vmcnt(11) lgkmcnt(0)\n" \
"v_mfma_f32_16x16x32_bf16 a[224:227], v[128:131], v[120:123], a[224:227]\n" \
"v_mfma_f32_16x16x32_bf16 a[224:227], v[132:135], v[124:127], a[224:227]\n" \
"s_barrier\n" \
"v_mfma_f32_16x16x32_bf16 a[16:19], v[64:67], v[160:163], a[16:19]\n" \
"s_add_u32 m0, s87, 4096\n" \
"ds_read_b64_tr_b16 v[128:129], v50 offset:0\n" \
"v_mfma_f32_16x16x32_bf16 a[16:19], v[68:71], v[164:167], a[16:19]\n" \
"global_load_lds_dwordx4 v32, s[54:55] offset:-4096\n" \
"v_mfma_f32_16x16x32_bf16 a[20:23], v[64:67], v[168:171], a[20:23]\n" \
"ds_read_b64_tr_b16 v[130:131], v50 offset:4096\n" \
"v_mfma_f32_16x16x32_bf16 a[20:23], v[68:71], v[172:175], a[20:23]\n" \
"v_mfma_f32_16x16x32_bf16 a[24:27], v[64:67], v[176:179], a[24:27]\n" \
"ds_read_b64_tr_b16 v[132:133], v50 offset:16384\n" \
"v_mfma_f32_16x16x32_bf16 a[24:27], v[68:71], v[180:183], a[24:27]\n" \
"global_load_lds_dwordx4 v33, s[40:41] offset:-3072\n" \
"v_mfma_f32_16x16x32_bf16 a[28:31], v[64:67], v[184:187], a[28:31]\n" \
"ds_read_b64_tr_b16 v[134:135], v50 offset:20480\n" \
"v_mfma_f32_16x16x32_bf16 a[28:31], v[68:71], v[188:191], a[28:31]\n" \
"v_mfma_f32_16x16x32_bf16 a[60:63], v[72:75], v[184:187], a[60:63]\n" \
"ds_read_b128 v[64:67], v48 offset:0\n" \
"v_mfma_f32_16x16x32_bf16 a[60:63], v[76:79], v[188:191], a[60:63]\n" \
"global_load_lds_dwordx4 v34, s[54:55] offset:-2048\n" \
"v_mfma_f32_16x16x32_bf16 a[56:59], v[72:75], v[176:179], a[56:59]\n" \
"ds_read_b64_tr_b16 v[136:137], v51 offset:0\n" \
"v_mfma_f32_16x16x32_bf16 a[56:59], v[76:79], v[180:183], a[56:59]\n" \
"v_mfma_f32_16x16x32_bf16 a[52:55], v[72:75], v[168:171], a[52:55]\n" \
"ds_read_b128 v[68:71], v49 offset:0\n" \
"v_mfma_f32_16x16x32_bf16 a[52:55], v[76:79], v[172:175], a[52:55]\n" \
"global_load_lds_dwordx4 v35, s[40:41] offset:-1024\n" \
"v_mfma_f32_16x16x32_bf16 a[48:51], v[72:75], v[160:163], a[48:51]\n" \
"ds_read_b64_tr_b16 v[138:139], v51 offset:4096\n" \
"v_mfma_f32_16x16x32_bf16 a[48:51], v[76:79], v[164:167], a[48:51]\n" \
"v_mfma_f32_16x16x32_bf16 a[80:83], v[80:83], v[160:163], a[80:83]\n" \
"ds_read_b128 v[72:75], v48 offset:2048\n" \
"v_mfma_f32_16x16x32_bf16 a[80:83], v[84:87], v[164:167], a[80:83]\n" \
"global_load_lds_dwordx4 v36, s[54:55] offset:0\n" \
"v_mfma_f32_16x16x32_bf16 a[84:87], v[80:83], v[168:171], a[84:87]\n" \
"ds_read_b64_tr_b16 v[140:141], v51 offset:16384\n" \
"v_mfma_f32_16x16x32_bf16 a[84:87], v[84:87], v[172:175], a[84:87]\n" \
"v_mfma_f32_16x16x32_bf16 a[88:91], v[80:83], v[176:179], a[88:91]\n" \
"ds_read_b128 v[76:79], v49 offset:2048\n" \
"v_mfma_f32_16x16x32_bf16 a[88:91], v[84:87], v[180:183], a[88:91]\n" \
"global_load_lds_dwordx4 v37, s[40:41] offset:1024\n" \
"v_mfma_f32_16x16x32_bf16 a[92:95], v[80:83], v[184:187], a[92:95]\n" \
"ds_read_b64_tr_b16 v[142:143], v51 offset:20480\n" \
"v_mfma_f32_16x16x32_bf16 a[92:95], v[84:87], v[188:191], a[92:95]\n" \
"v_mfma_f32_16x16x32_bf16 a[124:127], v[88:91], v[184:187], a[124:127]\n" \
"ds_read_b128 v[80:83], v48 offset:4096\n" \
"v_mfma_f32_16x16x32_bf16 a[124:127], v[92:95], v[188:191], a[124:127]\n" \
"global_load_lds_dwordx4 v38, s[54:55] offset:2048\n" \
"v_mfma_f32_16x16x32_bf16 a[120:123], v[88:91], v[176:179], a[120:123]\n" \
"ds_read_b64_tr_b16 v[144:145], v50 offset:512\n" \
"v_mfma_f32_16x16x32_bf16 a[120:123], v[92:95], v[180:183], a[120:123]\n" \
"v_mfma_f32_16x16x32_bf16 a[116:119], v[88:91], v[168:171], a[116:119]\n" \
"ds_read_b128 v[84:87], v49 offset:4096\n" \
"global_load_lds_dwordx4 v39, s[40:41] offset:3072\n" \
"v_mfma_f32_16x16x32_bf16 a[116:119], v[92:95], v[172:175], a[116:119]\n" \
"s_add_u32 s54, s54, s75\n" \
"s_addc_u32 s55, s55, 0\n" \
"v_mfma_f32_16x16x32_bf16 a[112:115], v[88:91], v[160:163], a[112:115]\n" \
"s_add_u32 s40, s40, s75\n" \
"s_addc_u32 s41, s41, 0\n" \
"ds_read_b64_tr_b16 v[146:147], v50 offset:4608\n" \
"v_mfma_f32_16x16x32_bf16 a[112:115], v[92:95], v[164:167], a[112:115]\n" \
"s_add_u32 s68, s66, 0x200000\n" \
"s_add_u32 s64, s8, s68\n" \
"s_addc_u32 s65, s9, 0\n" \
"v_mfma_f32_16x16x32_bf16 a[144:147], v[96:99], v[160:163], a[144:147]\n" \
"ds_read_b128 v[88:91], v48 offset:6144\n" \
"v_mfma_f32_16x16x32_bf16 a[144:147], v[100:103], v[164:167], a[144:147]\n" \
"global_load_dword v253, v248, s[64:65]\n" \
"v_mfma_f32_16x16x32_bf16 a[148:151], v[96:99], v[168:171], a[148:151]\n" \
"s_add_u32 s68, s67, 0x0\n" \
"ds_read_b64_tr_b16 v[148:149], v50 offset:16896\n" \
"v_mfma_f32_16x16x32_bf16 a[148:151], v[100:103], v[172:175], a[148:151]\n" \
"s_add_u32 s64, s12, s68\n" \
"s_addc_u32 s65, s13, 0\n" \
"v_mfma_f32_16x16x32_bf16 a[152:155], v[96:99], v[176:179], a[152:155]\n" \
"ds_read_b128 v[92:95], v49 offset:6144\n" \
"v_mfma_f32_16x16x32_bf16 a[152:155], v[100:103], v[180:183], a[152:155]\n" \
"Lpg1150_%=:\n" \
"Lst0x1_%=:\n" \
"s_cbranch_execz Lpg3308_%=\n" \
"global_load_dword v253, v248, s[64:65]\n" \
"v_mfma_f32_16x16x32_bf16 a[156:159], v[96:99], v[184:187], a[156:159]\n" \
"ds_read_b64_tr_b16 v[150:151], v50 offset:20992\n" \
"v_mfma_f32_16x16x32_bf16 a[156:159], v[100:103], v[188:191], a[156:159]\n" \
"v_mfma_f32_16x16x32_bf16 a[188:191], v[104:107], v[184:187], a[188:191]\n" \
"ds_read_b128 v[96:99], v48 offset:8192\n" \
"v_mfma_f32_16x16x32_bf16 a[188:191], v[108:111], v[188:191], a[188:191]\n" \
"v_mfma_f32_16x16x32_bf16 a[184:187], v[104:107], v[176:179], a[184:187]\n" \
"ds_read_b64_tr_b16 v[152:153], v51 offset:512\n" \
"v_mfma_f32_16x16x32_bf16 a[184:187], v[108:111], v[180:183], a[184:187]\n" \
"v_mfma_f32_16x16x32_bf16 a[180:183], v[104:107], v[168:171], a[180:183]\n" \
"ds_read_b128 v[100:103], v49 offset:8192\n" \
"v_mfma_f32_16x16x32_bf16 a[180:183], v[108:111], v[172:175], a[180:183]\n" \
"v_mfma_f32_16x16x32_bf16 a[176:179], v[104:107], v[160:163], a[176:179]\n" \
"ds_read_b64_tr_b16 v[154:155], v51 offset:4608\n" \
"v_mfma_f32_16x16x32_bf16 a[176:179], v[108:111], v[164:167], a[176:179]\n" \
"v_mfma_f32_16x16x32_bf16 a[208:211], v[112:115], v[160:163], a[208:211]\n" \
"ds_read_b128 v[104:107], v48 offset:10240\n" \
"v_mfma_f32_16x16x32_bf16 a[208:211], v[116:119], v[164:167], a[208:211]\n" \
"v_mfma_f32_16x16x32_bf16 a[212:215], v[112:115], v[168:171], a[212:215]\n" \
"ds_read_b64_tr_b16 v[156:157], v51 offset:16896\n" \
"v_mfma_f32_16x16x32_bf16 a[212:215], v[116:119], v[172:175], a[212:215]\n" \
"v_mfma_f32_16x16x32_bf16 a[216:219], v[112:115], v[176:179], a[216:219]\n" \
"ds_read_b128 v[108:111], v49 offset:10240\n" \
"v_mfma_f32_16x16x32_bf16 a[216:219], v[116:119], v[180:183], a[216:219]\n" \
"v_mfma_f32_16x16x32_bf16 a[220:223], v[112:115], v[184:187], a[220:223]\n" \
"ds_read_b64_tr_b16 v[158:159], v51 offset:20992\n" \
"v_mfma_f32_16x16x32_bf16 a[220:223], v[116:119], v[188:191], a[220:223]\n" \
"v_mfma_f32_16x16x32_bf16 a[252:255], v[120:123], v[184:187], a[252:255]\n" \
"ds_read_b128 v[112:115], v48 offset:12288\n" \
"v_mfma_f32_16x16x32_bf16 a[252:255], v[124:127], v[188:191], a[252:255]\n" \
"v_mfma_f32_16x16x32_bf16 a[248:251], v[120:123], v[176:179], a[248:251]\n" \
"v_mfma_f32_16x16x32_bf16 a[248:251], v[124:127], v[180:183], a[248:251]\n" \
"v_mfma_f32_16x16x32_bf16 a[244:247], v[120:123], v[168:171], a[244:247]\n" \
"ds_read_b128 v[116:119], v49 offset:12288\n" \
"v_mfma_f32_16x16x32_bf16 a[244:247], v[124:127], v[172:175], a[244:247]\n" \
"v_mfma_f32_16x16x32_bf16 a[240:243], v[120:123], v[160:163], a[240:243]\n" \
"v_mfma_f32_16x16x32_bf16 a[240:243], v[124:127], v[164:167], a[240:243]\n" \
"s_waitcnt lgkmcnt(1)\n" \
"v_mfma_f32_16x16x32_bf16 a[0:3], v[128:131], v[64:67], a[0:3]\n" \
"ds_read_b128 v[120:123], v48 offset:14336\n" \
"v_mfma_f32_16x16x32_bf16 a[0:3], v[132:135], v[68:71], a[0:3]\n" \
"v_mfma_f32_16x16x32_bf16 a[4:7], v[136:139], v[64:67], a[4:7]\n" \
"ds_read_b128 v[124:127], v49 offset:14336\n" \
"v_mfma_f32_16x16x32_bf16 a[4:7], v[140:143], v[68:71], a[4:7]\n" \
"s_add_u32 s68, s66, 0x300000\n" \
"v_mfma_f32_16x16x32_bf16 a[8:11], v[144:147], v[64:67], a[8:11]\n" \
"s_add_u32 s64, s8, s68\n" \
"s_addc_u32 s65, s9, 0\n" \
"ds_read_b64_tr_b16 v[160:161], v50 offset:1024\n" \
"v_mfma_f32_16x16x32_bf16 a[8:11], v[148:151], v[68:71], a[8:11]\n" \
"global_load_dword v253, v248, s[64:65]\n" \
"v_mfma_f32_16x16x32_bf16 a[12:15], v[152:155], v[64:67], a[12:15]\n" \
"s_add_u32 m0, s87, 135168\n" \
"ds_read_b64_tr_b16 v[162:163], v50 offset:5120\n" \
"v_mfma_f32_16x16x32_bf16 a[12:15], v[156:159], v[68:71], a[12:15]\n" \
"global_load_lds_dwordx4 v40, s[52:53] offset:-4096\n" \
"v_mfma_f32_16x16x32_bf16 a[44:47], v[152:155], v[72:75], a[44:47]\n" \
"ds_read_b64_tr_b16 v[164:165], v50 offset:17408\n" \
"v_mfma_f32_16x16x32_bf16 a[44:47], v[156:159], v[76:79], a[44:47]\n" \
"v_mfma_f32_16x16x32_bf16 a[40:43], v[144:147], v[72:75], a[40:43]\n" \
"ds_read_b64_tr_b16 v[166:167], v50 offset:21504\n" \
"v_mfma_f32_16x16x32_bf16 a[40:43], v[148:151], v[76:79], a[40:43]\n" \
"global_load_lds_dwordx4 v41, s[50:51] offset:-3072\n" \
"v_mfma_f32_16x16x32_bf16 a[36:39], v[136:139], v[72:75], a[36:39]\n" \
"ds_read_b64_tr_b16 v[168:169], v51 offset:1024\n" \
"v_mfma_f32_16x16x32_bf16 a[36:39], v[140:143], v[76:79], a[36:39]\n" \
"v_mfma_f32_16x16x32_bf16 a[32:35], v[128:131], v[72:75], a[32:35]\n" \
"ds_read_b64_tr_b16 v[170:171], v51 offset:5120\n" \
"v_mfma_f32_16x16x32_bf16 a[32:35], v[132:135], v[76:79], a[32:35]\n" \
"global_load_lds_dwordx4 v42, s[52:53] offset:-2048\n" \
"v_mfma_f32_16x16x32_bf16 a[64:67], v[128:131], v[80:83], a[64:67]\n" \
"ds_read_b64_tr_b16 v[172:173], v51 offset:17408\n" \
"v_mfma_f32_16x16x32_bf16 a[64:67], v[132:135], v[84:87], a[64:67]\n" \
"v_mfma_f32_16x16x32_bf16 a[68:71], v[136:139], v[80:83], a[68:71]\n" \
"ds_read_b64_tr_b16 v[174:175], v51 offset:21504\n" \
"v_mfma_f32_16x16x32_bf16 a[68:71], v[140:143], v[84:87], a[68:71]\n" \
"global_load_lds_dwordx4 v43, s[50:51] offset:-1024\n" \
"v_mfma_f32_16x16x32_bf16 a[72:75], v[144:147], v[80:83], a[72:75]\n" \
"ds_read_b64_tr_b16 v[176:177], v50 offset:1536\n" \
"v_mfma_f32_16x16x32_bf16 a[72:75], v[148:151], v[84:87], a[72:75]\n" \
"v_mfma_f32_16x16x32_bf16 a[76:79], v[152:155], v[80:83], a[76:79]\n" \
"ds_read_b64_tr_b16 v[178:179], v50 offset:5632\n" \
"v_mfma_f32_16x16x32_bf16 a[76:79], v[156:159], v[84:87], a[76:79]\n" \
"global_load_lds_dwordx4 v44, s[52:53] offset:0\n" \
"v_mfma_f32_16x16x32_bf16 a[108:111], v[152:155], v[88:91], a[108:111]\n" \
"ds_read_b64_tr_b16 v[180:181], v50 offset:17920\n" \
"v_mfma_f32_16x16x32_bf16 a[108:111], v[156:159], v[92:95], a[108:111]\n" \
"v_mfma_f32_16x16x32_bf16 a[104:107], v[144:147], v[88:91], a[104:107]\n" \
"ds_read_b64_tr_b16 v[182:183], v50 offset:22016\n" \
"v_mfma_f32_16x16x32_bf16 a[104:107], v[148:151], v[92:95], a[104:107]\n" \
"global_load_lds_dwordx4 v45, s[50:51] offset:1024\n" \
"v_mfma_f32_16x16x32_bf16 a[100:103], v[136:139], v[88:91], a[100:103]\n" \
"ds_read_b64_tr_b16 v[184:185], v51 offset:1536\n" \
"v_mfma_f32_16x16x32_bf16 a[100:103], v[140:143], v[92:95], a[100:103]\n" \
"v_mfma_f32_16x16x32_bf16 a[96:99], v[128:131], v[88:91], a[96:99]\n" \
"ds_read_b64_tr_b16 v[186:187], v51 offset:5632\n" \
"v_mfma_f32_16x16x32_bf16 a[96:99], v[132:135], v[92:95], a[96:99]\n" \
"global_load_lds_dwordx4 v46, s[52:53] offset:2048\n" \
"v_mfma_f32_16x16x32_bf16 a[128:131], v[128:131], v[96:99], a[128:131]\n" \
"ds_read_b64_tr_b16 v[188:189], v51 offset:17920\n" \
"v_mfma_f32_16x16x32_bf16 a[128:131], v[132:135], v[100:103], a[128:131]\n" \
"v_mfma_f32_16x16x32_bf16 a[132:135], v[136:139], v[96:99], a[132:135]\n" \
"ds_read_b64_tr_b16 v[190:191], v51 offset:22016\n" \
"v_mfma_f32_16x16x32_bf16 a[132:135], v[140:143], v[100:103], a[132:135]\n" \
"global_load_lds_dwordx4 v47, s[50:51] offset:3072\n" \
"v_mfma_f32_16x16x32_bf16 a[136:139], v[144:147], v[96:99], a[136:139]\n" \
"s_add_u32 s52, s52, s74\n" \
"s_addc_u32 s53, s53, 0\n" \
"v_mfma_f32_16x16x32_bf16 a[136:139], v[148:151], v[100:103], a[136:139]\n" \
"s_add_u32 s50, s50, s74\n" \
"s_addc_u32 s51, s51, 0\n" \
"v_mfma_f32_16x16x32_bf16 a[140:143], v[152:155], v[96:99], a[140:143]\n" \
"v_mfma_f32_16x16x32_bf16 a[140:143], v[156:159], v[100:103], a[140:143]\n" \
"v_mfma_f32_16x16x32_bf16 a[172:175], v[152:155], v[104:107], a[172:175]\n" \
"s_mul_i32 s76, s73, 0x10000\n" \
"s_add_u32 s77, s76, 0xfffe8000\n" \
"s_sub_u32 s78, 0xffff8000, s76\n" \
"v_mfma_f32_16x16x32_bf16 a[172:175], v[156:159], v[108:111], a[172:175]\n" \
"v_add_u32 v48, s77, v48\n" \
"v_add_u32 v49, s77, v49\n" \
"v_mfma_f32_16x16x32_bf16 a[168:171], v[144:147], v[104:107], a[168:171]\n" \
"v_add_u32 v50, s78, v50\n" \
"v_add_u32 v51, s78, v51\n" \
"v_mfma_f32_16x16x32_bf16 a[168:171], v[148:151], v[108:111], a[168:171]\n" \
"v_mfma_f32_16x16x32_bf16 a[164:167], v[136:139], v[104:107], a[164:167]\n" \
"v_mfma_f32_16x16x32_bf16 a[164:167], v[140:143], v[108:111], a[164:167]\n" \
"v_mfma_f32_16x16x32_bf16 a[160:163], v[128:131], v[104:107], a[160:163]\n" \
"v_mfma_f32_16x16x32_bf16 a[160:163], v[132:135], v[108:111], a[160:163]\n" \
"s_waitcnt lgkmcnt(0)\n" \
"v_mfma_f32_16x16x32_bf16 a[192:195], v[128:131], v[112:115], a[192:195]\n" \
"v_mfma_f32_16x16x32_bf16 a[192:195], v[132:135], v[116:119], a[192:195]\n" \
"v_mfma_f32_16x16x32_bf16 a[196:199], v[136:139], v[112:115], a[196:199]\n" \
"v_mfma_f32_16x16x32_bf16 a[196:199], v[140:143], v[116:119], a[196:199]\n" \
"v_mfma_f32_16x16x32_bf16 a[200:203], v[144:147], v[112:115], a[200:203]\n" \
"v_mfma_f32_16x16x32_bf16 a[200:203], v[148:151], v[116:119], a[200:203]\n" \
"v_mfma_f32_16x16x32_bf16 a[204:207], v[152:155], v[112:115], a[204:207]\n" \
"v_mfma_f32_16x16x32_bf16 a[204:207], v[156:159], v[116:119], a[204:207]\n" \
"v_mfma_f32_16x16x32_bf16 a[236:239], v[152:155], v[120:123], a[236:239]\n" \
"v_mfma_f32_16x16x32_bf16 a[236:239], v[156:159], v[124:127], a[236:239]\n" \
"v_mfma_f32_16x16x32_bf16 a[232:235], v[144:147], v[120:123], a[232:235]\n" \
"v_mfma_f32_16x16x32_bf16 a[232:235], v[148:151], v[124:127], a[232:235]\n" \
"v_mfma_f32_16x16x32_bf16 a[228:231], v[136:139], v[120:123], a[228:231]\n" \
"v_mfma_f32_16x16x32_bf16 a[228:231], v[140:143], v[124:127], a[228:231]\n" \
"s_waitcnt vmcnt(11) lgkmcnt(0)\n" \
"v_mfma_f32_16x16x32_bf16 a[224:227], v[128:131], v[120:123], a[224:227]\n" \
"v_mfma_f32_16x16x32_bf16 a[224:227], v[132:135], v[124:127], a[224:227]\n" \
"s_barrier\n" \
"v_mfma_f32_16x16x32_bf16 a[16:19], v[64:67], v[160:163], a[16:19]\n" \
"s_add_u32 m0, s87, 69632\n" \
"ds_read_b64_tr_b16 v[128:129], v50 offset:0\n" \
"v_mfma_f32_16x16x32_bf16 a[16:19], v[68:71], v[164:167], a[16:19]\n" \
"global_load_lds_dwordx4 v32, s[54:55] offset:-4096\n" \
"v_mfma_f32_16x16x32_bf16 a[20:23], v[64:67], v[168:171], a[20:23]\n" \
"ds_read_b64_tr_b16 v[130:131], v50 offset:4096\n" \
"v_mfma_f32_16x16x32_bf16 a[20:23], v[68:71], v[172:175], a[20:23]\n" \
"v_mfma_f32_16x16x32_bf16 a[24:27], v[64:67], v[176:179], a[24:27]\n" \
"ds_read_b64_tr_b16 v[132:133], v50 offset:16384\n" \
"v_mfma_f32_16x16x32_bf16 a[24:27], v[68:71], v[180:183], a[24:27]\n" \
"global_load_lds_dwordx4 v33, s[40:41] offset:-3072\n" \
"v_mfma_f32_16x16x32_bf16 a[28:31], v[64:67], v[184:187], a[28:31]\n" \
"ds_read_b64_tr_b16 v[134:135], v50 offset:20480\n" \
"v_mfma_f32_16x16x32_bf16 a[28:31], v[68:71], v[188:191], a[28:31]\n" \
"v_mfma_f32_16x16x32_bf16 a[60:63], v[72:75], v[184:187], a[60:63]\n" \
"ds_read_b128 v[64:67], v48 offset:0\n" \
"v_mfma_f32_16x16x32_bf16 a[60:63], v[76:79], v[188:191], a[60:63]\n" \
"global_load_lds_dwordx4 v34, s[54:55] offset:-2048\n" \
"v_mfma_f32_16x16x32_bf16 a[56:59], v[72:75], v[176:179], a[56:59]\n" \
"ds_read_b64_tr_b16 v[136:137], v51 offset:0\n" \
"v_mfma_f32_16x16x32_bf16 a[56:59], v[76:79], v[180:183], a[56:59]\n" \
"v_mfma_f32_16x16x32_bf16 a[52:55], v[72:75], v[168:171], a[52:55]\n" \
"ds_read_b128 v[68:71], v49 offset:0\n" \
"v_mfma_f32_16x16x32_bf16 a[52:55], v[76:79], v[172:175], a[52:55]\n" \
"global_load_lds_dwordx4 v35, s[40:41] offset:-1024\n" \
"v_mfma_f32_16x16x32_bf16 a[48:51], v[72:75], v[160:163], a[48:51]\n" \
"ds_read_b64_tr_b16 v[138:139], v51 offset:4096\n" \
"v_mfma_f32_16x16x32_bf16 a[48:51], v[76:79], v[164:167], a[48:51]\n" \
"v_mfma_f32_16x16x32_bf16 a[80:83], v[80:83], v[160:163], a[80:83]\n" \
"ds_read_b128 v[72:75], v48 offset:2048\n" \
"v_mfma_f32_16x16x32_bf16 a[80:83], v[84:87], v[164:167], a[80:83]\n" \
"global_load_lds_dwordx4 v36, s[54:55] offset:0\n" \
"v_mfma_f32_16x16x32_bf16 a[84:87], v[80:83], v[168:171], a[84:87]\n" \
"ds_read_b64_tr_b16 v[140:141], v51 offset:16384\n" \
"v_mfma_f32_16x16x32_bf16 a[84:87], v[84:87], v[172:175], a[84:87]\n" \
"v_mfma_f32_16x16x32_bf16 a[88:91], v[80:83], v[176:179], a[88:91]\n" \
"ds_read_b128 v[76:79], v49 offset:2048\n" \
"v_mfma_f32_16x16x32_bf16 a[88:91], v[84:87], v[180:183], a[88:91]\n" \
"global_load_lds_dwordx4 v37, s[40:41] offset:1024\n" \
"v_mfma_f32_16x16x32_bf16 a[92:95], v[80:83], v[184:187], a[92:95]\n" \
"ds_read_b64_tr_b16 v[142:143], v51 offset:20480\n" \
"v_mfma_f32_16x16x32_bf16 a[92:95], v[84:87], v[188:191], a[92:95]\n" \
"v_mfma_f32_16x16x32_bf16 a[124:127], v[88:91], v[184:187], a[124:127]\n" \
"ds_read_b128 v[80:83], v48 offset:4096\n" \
"v_mfma_f32_16x16x32_bf16 a[124:127], v[92:95], v[188:191], a[124:127]\n" \
"global_load_lds_dwordx4 v38, s[54:55] offset:2048\n" \
"v_mfma_f32_16x16x32_bf16 a[120:123], v[88:91], v[176:179], a[120:123]\n" \
"ds_read_b64_tr_b16 v[144:145], v50 offset:512\n" \
"v_mfma_f32_16x16x32_bf16 a[120:123], v[92:95], v[180:183], a[120:123]\n" \
"v_mfma_f32_16x16x32_bf16 a[116:119], v[88:91], v[168:171], a[116:119]\n" \
"ds_read_b128 v[84:87], v49 offset:4096\n" \
"global_load_lds_dwordx4 v39, s[40:41] offset:3072\n" \
"v_mfma_f32_16x16x32_bf16 a[116:119], v[92:95], v[172:175], a[116:119]\n" \
"s_add_u32 s54, s54, s75\n" \
"s_addc_u32 s55, s55, 0\n" \
"v_mfma_f32_16x16x32_bf16 a[112:115], v[88:91], v[160:163], a[112:115]\n" \
"s_add_u32 s40, s40, s75\n" \
"s_addc_u32 s41, s41, 0\n" \
"ds_read_b64_tr_b16 v[146:147], v50 offset:4608\n" \
"v_mfma_f32_16x16x32_bf16 a[112:115], v[92:95], v[164:167], a[112:115]\n" \
"s_add_u32 s68, s67, 0x100000\n" \
"s_add_u32 s64, s12, s68\n" \
"s_addc_u32 s65, s13, 0\n" \
"v_mfma_f32_16x16x32_bf16 a[144:147], v[96:99], v[160:163], a[144:147]\n" \
"ds_read_b128 v[88:91], v48 offset:6144\n" \
"v_mfma_f32_16x16x32_bf16 a[144:147], v[100:103], v[164:167], a[144:147]\n" \
"global_load_dword v253, v248, s[64:65]\n" \
"v_mfma_f32_16x16x32_bf16 a[148:151], v[96:99], v[168:171], a[148:151]\n" \
"s_add_u32 s68, s67, 0x500000\n" \
"ds_read_b64_tr_b16 v[148:149], v50 offset:16896\n" \
"v_mfma_f32_16x16x32_bf16 a[148:151], v[100:103], v[172:175], a[148:151]\n" \
"s_add_u32 s64, s12, s68\n" \
"s_addc_u32 s65, s13, 0\n" \
"v_mfma_f32_16x16x32_bf16 a[152:155], v[96:99], v[176:179], a[152:155]\n" \
"ds_read_b128 v[92:95], v49 offset:6144\n" \
"v_mfma_f32_16x16x32_bf16 a[152:155], v[100:103], v[180:183], a[152:155]\n" \
"global_load_dword v253, v248, s[64:65]\n" \
"v_mfma_f32_16x16x32_bf16 a[156:159], v[96:99], v[184:187], a[156:159]\n" \
"ds_read_b64_tr_b16 v[150:151], v50 offset:20992\n" \
"v_mfma_f32_16x16x32_bf16 a[156:159], v[100:103], v[188:191], a[156:159]\n" \
"v_mfma_f32_16x16x32_bf16 a[188:191], v[104:107], v[184:187], a[188:191]\n" \
"ds_read_b128 v[96:99], v48 offset:8192\n" \
"v_mfma_f32_16x16x32_bf16 a[188:191], v[108:111], v[188:191], a[188:191]\n" \
"v_mfma_f32_16x16x32_bf16 a[184:187], v[104:107], v[176:179], a[184:187]\n" \
"ds_read_b64_tr_b16 v[152:153], v51 offset:512\n" \
"v_mfma_f32_16x16x32_bf16 a[184:187], v[108:111], v[180:183], a[184:187]\n" \
"v_mfma_f32_16x16x32_bf16 a[180:183], v[104:107], v[168:171], a[180:183]\n" \
"ds_read_b128 v[100:103], v49 offset:8192\n" \
"v_mfma_f32_16x16x32_bf16 a[180:183], v[108:111], v[172:175], a[180:183]\n" \
"v_mfma_f32_16x16x32_bf16 a[176:179], v[104:107], v[160:163], a[176:179]\n" \
"ds_read_b64_tr_b16 v[154:155], v51 offset:4608\n" \
"v_mfma_f32_16x16x32_bf16 a[176:179], v[108:111], v[164:167], a[176:179]\n" \
"v_mfma_f32_16x16x32_bf16 a[208:211], v[112:115], v[160:163], a[208:211]\n" \
"ds_read_b128 v[104:107], v48 offset:10240\n" \
"v_mfma_f32_16x16x32_bf16 a[208:211], v[116:119], v[164:167], a[208:211]\n" \
"v_mfma_f32_16x16x32_bf16 a[212:215], v[112:115], v[168:171], a[212:215]\n" \
"ds_read_b64_tr_b16 v[156:157], v51 offset:16896\n" \
"v_mfma_f32_16x16x32_bf16 a[212:215], v[116:119], v[172:175], a[212:215]\n" \
"v_mfma_f32_16x16x32_bf16 a[216:219], v[112:115], v[176:179], a[216:219]\n" \
"ds_read_b128 v[108:111], v49 offset:10240\n" \
"v_mfma_f32_16x16x32_bf16 a[216:219], v[116:119], v[180:183], a[216:219]\n" \
"v_mfma_f32_16x16x32_bf16 a[220:223], v[112:115], v[184:187], a[220:223]\n" \
"ds_read_b64_tr_b16 v[158:159], v51 offset:20992\n" \
"v_mfma_f32_16x16x32_bf16 a[220:223], v[116:119], v[188:191], a[220:223]\n" \
"v_mfma_f32_16x16x32_bf16 a[252:255], v[120:123], v[184:187], a[252:255]\n" \
"ds_read_b128 v[112:115], v48 offset:12288\n" \
"v_mfma_f32_16x16x32_bf16 a[252:255], v[124:127], v[188:191], a[252:255]\n" \
"v_mfma_f32_16x16x32_bf16 a[248:251], v[120:123], v[176:179], a[248:251]\n" \
"v_mfma_f32_16x16x32_bf16 a[248:251], v[124:127], v[180:183], a[248:251]\n" \
"v_mfma_f32_16x16x32_bf16 a[244:247], v[120:123], v[168:171], a[244:247]\n" \
"ds_read_b128 v[116:119], v49 offset:12288\n" \
"v_mfma_f32_16x16x32_bf16 a[244:247], v[124:127], v[172:175], a[244:247]\n" \
"v_mfma_f32_16x16x32_bf16 a[240:243], v[120:123], v[160:163], a[240:243]\n" \
"v_mfma_f32_16x16x32_bf16 a[240:243], v[124:127], v[164:167], a[240:243]\n" \
"s_waitcnt lgkmcnt(1)\n" \
"v_mfma_f32_16x16x32_bf16 a[0:3], v[128:131], v[64:67], a[0:3]\n" \
"ds_read_b128 v[120:123], v48 offset:14336\n" \
"v_mfma_f32_16x16x32_bf16 a[0:3], v[132:135], v[68:71], a[0:3]\n" \
"v_mfma_f32_16x16x32_bf16 a[4:7], v[136:139], v[64:67], a[4:7]\n" \
"ds_read_b128 v[124:127], v49 offset:14336\n" \
"v_mfma_f32_16x16x32_bf16 a[4:7], v[140:143], v[68:71], a[4:7]\n" \
"s_add_u32 s68, s67, 0x200000\n" \
"v_mfma_f32_16x16x32_bf16 a[8:11], v[144:147], v[64:67], a[8:11]\n" \
"s_add_u32 s64, s12, s68\n" \
"s_addc_u32 s65, s13, 0\n" \
"ds_read_b64_tr_b16 v[160:161], v50 offset:1024\n" \
"v_mfma_f32_16x16x32_bf16 a[8:11], v[148:151], v[68:71], a[8:11]\n" \
"global_load_dword v253, v248, s[64:65]\n" \
"v_mfma_f32_16x16x32_bf16 a[12:15], v[152:155], v[64:67], a[12:15]\n" \
"s_add_u32 m0, s87, 102400\n" \
"ds_read_b64_tr_b16 v[162:163], v50 offset:5120\n" \
"v_mfma_f32_16x16x32_bf16 a[12:15], v[156:159], v[68:71], a[12:15]\n" \
"global_load_lds_dwordx4 v40, s[52:53] offset:-4096\n" \
"v_mfma_f32_16x16x32_bf16 a[44:47], v[152:155], v[72:75], a[44:47]\n" \
"ds_read_b64_tr_b16 v[164:165], v50 offset:17408\n" \
"v_mfma_f32_16x16x32_bf16 a[44:47], v[156:159], v[76:79], a[44:47]\n" \
"v_mfma_f32_16x16x32_bf16 a[40:43], v[144:147], v[72:75], a[40:43]\n" \
"ds_read_b64_tr_b16 v[166:167], v50 offset:21504\n" \
"v_mfma_f32_16x16x32_bf16 a[40:43], v[148:151], v[76:79], a[40:43]\n" \
"global_load_lds_dwordx4 v41, s[50:51] offset:-3072\n" \
"v_mfma_f32_16x16x32_bf16 a[36:39], v[136:139], v[72:75], a[36:39]\n" \
"ds_read_b64_tr_b16 v[168:169], v51 offset:1024\n" \
"v_mfma_f32_16x16x32_bf16 a[36:39], v[140:143], v[76:79], a[36:39]\n" \
"v_mfma_f32_16x16x32_bf16 a[32:35], v[128:131], v[72:75], a[32:35]\n" \
"ds_read_b64_tr_b16 v[170:171], v51 offset:5120\n" \
"v_mfma_f32_16x16x32_bf16 a[32:35], v[132:135], v[76:79], a[32:35]\n" \
"global_load_lds_dwordx4 v42, s[52:53] offset:-2048\n" \
"v_mfma_f32_16x16x32_bf16 a[64:67], v[128:131], v[80:83], a[64:67]\n" \
"ds_read_b64_tr_b16 v[172:173], v51 offset:17408\n" \
"v_mfma_f32_16x16x32_bf16 a[64:67], v[132:135], v[84:87], a[64:67]\n" \
"v_mfma_f32_16x16x32_bf16 a[68:71], v[136:139], v[80:83], a[68:71]\n" \
"ds_read_b64_tr_b16 v[174:175], v51 offset:21504\n" \
"v_mfma_f32_16x16x32_bf16 a[68:71], v[140:143], v[84:87], a[68:71]\n" \
"global_load_lds_dwordx4 v43, s[50:51] offset:-1024\n" \
"v_mfma_f32_16x16x32_bf16 a[72:75], v[144:147], v[80:83], a[72:75]\n" \
"ds_read_b64_tr_b16 v[176:177], v50 offset:1536\n" \
"v_mfma_f32_16x16x32_bf16 a[72:75], v[148:151], v[84:87], a[72:75]\n" \
"v_mfma_f32_16x16x32_bf16 a[76:79], v[152:155], v[80:83], a[76:79]\n" \
"ds_read_b64_tr_b16 v[178:179], v50 offset:5632\n" \
"v_mfma_f32_16x16x32_bf16 a[76:79], v[156:159], v[84:87], a[76:79]\n" \
"global_load_lds_dwordx4 v44, s[52:53] offset:0\n" \
"v_mfma_f32_16x16x32_bf16 a[108:111], v[152:155], v[88:91], a[108:111]\n" \
"ds_read_b64_tr_b16 v[180:181], v50 offset:17920\n" \
"v_mfma_f32_16x16x32_bf16 a[108:111], v[156:159], v[92:95], a[108:111]\n" \
"v_mfma_f32_16x16x32_bf16 a[104:107], v[144:147], v[88:91], a[104:107]\n" \
"ds_read_b64_tr_b16 v[182:183], v50 offset:22016\n" \
"v_mfma_f32_16x16x32_bf16 a[104:107], v[148:151], v[92:95], a[104:107]\n" \
"global_load_lds_dwordx4 v45, s[50:51] offset:1024\n" \
"v_mfma_f32_16x16x32_bf16 a[100:103], v[136:139], v[88:91], a[100:103]\n" \
"ds_read_b64_tr_b16 v[184:185], v51 offset:1536\n" \
"v_mfma_f32_16x16x32_bf16 a[100:103], v[140:143], v[92:95], a[100:103]\n" \
"v_mfma_f32_16x16x32_bf16 a[96:99], v[128:131], v[88:91], a[96:99]\n" \
"ds_read_b64_tr_b16 v[186:187], v51 offset:5632\n" \
"v_mfma_f32_16x16x32_bf16 a[96:99], v[132:135], v[92:95], a[96:99]\n" \
"global_load_lds_dwordx4 v46, s[52:53] offset:2048\n" \
"v_mfma_f32_16x16x32_bf16 a[128:131], v[128:131], v[96:99], a[128:131]\n" \
"ds_read_b64_tr_b16 v[188:189], v51 offset:17920\n" \
"v_mfma_f32_16x16x32_bf16 a[128:131], v[132:135], v[100:103], a[128:131]\n" \
"v_mfma_f32_16x16x32_bf16 a[132:135], v[136:139], v[96:99], a[132:135]\n" \
"ds_read_b64_tr_b16 v[190:191], v51 offset:22016\n" \
"v_mfma_f32_16x16x32_bf16 a[132:135], v[140:143], v[100:103], a[132:135]\n" \
"global_load_lds_dwordx4 v47, s[50:51] offset:3072\n" \
"v_mfma_f32_16x16x32_bf16 a[136:139], v[144:147], v[96:99], a[136:139]\n" \
"v_mfma_f32_16x16x32_bf16 a[136:139], v[148:151], v[100:103], a[136:139]\n" \
"v_mfma_f32_16x16x32_bf16 a[140:143], v[152:155], v[96:99], a[140:143]\n" \
"v_mfma_f32_16x16x32_bf16 a[140:143], v[156:159], v[100:103], a[140:143]\n" \
"v_mfma_f32_16x16x32_bf16 a[172:175], v[152:155], v[104:107], a[172:175]\n" \
"s_mul_i32 s76, s73, 0x8000\n" \
"s_add_u32 s77, s76, 0x10000\n" \
"s_sub_u32 s78, 0x18000, s76\n" \
"v_mfma_f32_16x16x32_bf16 a[172:175], v[156:159], v[108:111], a[172:175]\n" \
"v_add_u32 v48, s77, v48\n" \
"v_add_u32 v49, s77, v49\n" \
"v_mfma_f32_16x16x32_bf16 a[168:171], v[144:147], v[104:107], a[168:171]\n" \
"v_add_u32 v50, s78, v50\n" \
"v_add_u32 v51, s78, v51\n" \
"v_mfma_f32_16x16x32_bf16 a[168:171], v[148:151], v[108:111], a[168:171]\n" \
"v_mfma_f32_16x16x32_bf16 a[164:167], v[136:139], v[104:107], a[164:167]\n" \
"v_mfma_f32_16x16x32_bf16 a[164:167], v[140:143], v[108:111], a[164:167]\n" \
"v_mfma_f32_16x16x32_bf16 a[160:163], v[128:131], v[104:107], a[160:163]\n" \
"v_mfma_f32_16x16x32_bf16 a[160:163], v[132:135], v[108:111], a[160:163]\n" \
"s_waitcnt lgkmcnt(0)\n" \
"v_mfma_f32_16x16x32_bf16 a[192:195], v[128:131], v[112:115], a[192:195]\n" \
"v_mfma_f32_16x16x32_bf16 a[192:195], v[132:135], v[116:119], a[192:195]\n" \
"v_mfma_f32_16x16x32_bf16 a[196:199], v[136:139], v[112:115], a[196:199]\n" \
"v_mfma_f32_16x16x32_bf16 a[196:199], v[140:143], v[116:119], a[196:199]\n" \
"v_mfma_f32_16x16x32_bf16 a[200:203], v[144:147], v[112:115], a[200:203]\n" \
"v_mfma_f32_16x16x32_bf16 a[200:203], v[148:151], v[116:119], a[200:203]\n" \
"v_mfma_f32_16x16x32_bf16 a[204:207], v[152:155], v[112:115], a[204:207]\n" \
"v_mfma_f32_16x16x32_bf16 a[204:207], v[156:159], v[116:119], a[204:207]\n" \
"v_mfma_f32_16x16x32_bf16 a[236:239], v[152:155], v[120:123], a[236:239]\n" \
"v_mfma_f32_16x16x32_bf16 a[236:239], v[156:159], v[124:127], a[236:239]\n" \
"v_mfma_f32_16x16x32_bf16 a[232:235], v[144:147], v[120:123], a[232:235]\n" \
"v_mfma_f32_16x16x32_bf16 a[232:235], v[148:151], v[124:127], a[232:235]\n" \
"v_mfma_f32_16x16x32_bf16 a[228:231], v[136:139], v[120:123], a[228:231]\n" \
"v_mfma_f32_16x16x32_bf16 a[228:231], v[140:143], v[124:127], a[228:231]\n" \
"s_waitcnt vmcnt(11) lgkmcnt(0)\n" \
"v_mfma_f32_16x16x32_bf16 a[224:227], v[128:131], v[120:123], a[224:227]\n" \
"v_mfma_f32_16x16x32_bf16 a[224:227], v[132:135], v[124:127], a[224:227]\n" \
"s_barrier\n" \
"v_mfma_f32_16x16x32_bf16 a[16:19], v[64:67], v[160:163], a[16:19]\n" \
"s_add_u32 m0, s87, 36864\n" \
"ds_read_b64_tr_b16 v[128:129], v50 offset:0\n" \
"v_mfma_f32_16x16x32_bf16 a[16:19], v[68:71], v[164:167], a[16:19]\n" \
"global_load_lds_dwordx4 v32, s[54:55] offset:-4096\n" \
"v_mfma_f32_16x16x32_bf16 a[20:23], v[64:67], v[168:171], a[20:23]\n" \
"ds_read_b64_tr_b16 v[130:131], v50 offset:4096\n" \
"v_mfma_f32_16x16x32_bf16 a[20:23], v[68:71], v[172:175], a[20:23]\n" \
"v_mfma_f32_16x16x32_bf16 a[24:27], v[64:67], v[176:179], a[24:27]\n" \
"ds_read_b64_tr_b16 v[132:133], v50 offset:16384\n" \
"v_mfma_f32_16x16x32_bf16 a[24:27], v[68:71], v[180:183], a[24:27]\n" \
"global_load_lds_dwordx4 v33, s[40:41] offset:-3072\n" \
"v_mfma_f32_16x16x32_bf16 a[28:31], v[64:67], v[184:187], a[28:31]\n" \
"ds_read_b64_tr_b16 v[134:135], v50 offset:20480\n" \
"v_mfma_f32_16x16x32_bf16 a[28:31], v[68:71], v[188:191], a[28:31]\n" \
"v_mfma_f32_16x16x32_bf16 a[60:63], v[72:75], v[184:187], a[60:63]\n" \
"ds_read_b128 v[64:67], v48 offset:0\n" \
"v_mfma_f32_16x16x32_bf16 a[60:63], v[76:79], v[188:191], a[60:63]\n" \
"global_load_lds_dwordx4 v34, s[54:55] offset:-2048\n" \
"v_mfma_f32_16x16x32_bf16 a[56:59], v[72:75], v[176:179], a[56:59]\n" \
"ds_read_b64_tr_b16 v[136:137], v51 offset:0\n" \
"v_mfma_f32_16x16x32_bf16 a[56:59], v[76:79], v[180:183], a[56:59]\n" \
"v_mfma_f32_16x16x32_bf16 a[52:55], v[72:75], v[168:171], a[52:55]\n" \
"ds_read_b128 v[68:71], v49 offset:0\n" \
"v_mfma_f32_16x16x32_bf16 a[52:55], v[76:79], v[172:175], a[52:55]\n" \
"global_load_lds_dwordx4 v35, s[40:41] offset:-1024\n" \
"v_mfma_f32_16x16x32_bf16 a[48:51], v[72:75], v[160:163], a[48:51]\n" \
"ds_read_b64_tr_b16 v[138:139], v51 offset:4096\n" \
"v_mfma_f32_16x16x32_bf16 a[48:51], v[76:79], v[164:167], a[48:51]\n" \
"v_mfma_f32_16x16x32_bf16 a[80:83], v[80:83], v[160:163], a[80:83]\n" \
"ds_read_b128 v[72:75], v48 offset:2048\n" \
"v_mfma_f32_16x16x32_bf16 a[80:83], v[84:87], v[164:167], a[80:83]\n" \
"global_load_lds_dwordx4 v36, s[54:55] offset:0\n" \
"v_mfma_f32_16x16x32_bf16 a[84:87], v[80:83], v[168:171], a[84:87]\n" \
"ds_read_b64_tr_b16 v[140:141], v51 offset:16384\n" \
"v_mfma_f32_16x16x32_bf16 a[84:87], v[84:87], v[172:175], a[84:87]\n" \
"v_mfma_f32_16x16x32_bf16 a[88:91], v[80:83], v[176:179], a[88:91]\n" \
"ds_read_b128 v[76:79], v49 offset:2048\n" \
"v_mfma_f32_16x16x32_bf16 a[88:91], v[84:87], v[180:183], a[88:91]\n" \
"global_load_lds_dwordx4 v37, s[40:41] offset:1024\n" \
"v_mfma_f32_16x16x32_bf16 a[92:95], v[80:83], v[184:187], a[92:95]\n" \
"ds_read_b64_tr_b16 v[142:143], v51 offset:20480\n" \
"v_mfma_f32_16x16x32_bf16 a[92:95], v[84:87], v[188:191], a[92:95]\n" \
"v_mfma_f32_16x16x32_bf16 a[124:127], v[88:91], v[184:187], a[124:127]\n" \
"ds_read_b128 v[80:83], v48 offset:4096\n" \
"v_mfma_f32_16x16x32_bf16 a[124:127], v[92:95], v[188:191], a[124:127]\n" \
"global_load_lds_dwordx4 v38, s[54:55] offset:2048\n" \
"v_mfma_f32_16x16x32_bf16 a[120:123], v[88:91], v[176:179], a[120:123]\n" \
"ds_read_b64_tr_b16 v[144:145], v50 offset:512\n" \
"v_mfma_f32_16x16x32_bf16 a[120:123], v[92:95], v[180:183], a[120:123]\n" \
"v_mfma_f32_16x16x32_bf16 a[116:119], v[88:91], v[168:171], a[116:119]\n" \
"ds_read_b128 v[84:87], v49 offset:4096\n" \
"v_mfma_f32_16x16x32_bf16 a[116:119], v[92:95], v[172:175], a[116:119]\n" \
"global_load_lds_dwordx4 v39, s[40:41] offset:3072\n" \
"s_add_u32 s68, s67, 0x300000\n" \
"v_mfma_f32_16x16x32_bf16 a[112:115], v[88:91], v[160:163], a[112:115]\n" \
"s_add_u32 s64, s12, s68\n" \
"s_addc_u32 s65, s13, 0\n" \
"ds_read_b64_tr_b16 v[146:147], v50 offset:4608\n" \
"v_mfma_f32_16x16x32_bf16 a[112:115], v[92:95], v[164:167], a[112:115]\n" \
"v_mfma_f32_16x16x32_bf16 a[144:147], v[96:99], v[160:163], a[144:147]\n" \
"ds_read_b128 v[88:91], v48 offset:6144\n" \
"v_mfma_f32_16x16x32_bf16 a[144:147], v[100:103], v[164:167], a[144:147]\n" \
"global_load_dword v253, v248, s[64:65]\n" \
"v_mfma_f32_16x16x32_bf16 a[148:151], v[96:99], v[168:171], a[148:151]\n" \
"s_add_u32 s68, s67, 0x400000\n" \
"ds_read_b64_tr_b16 v[148:149], v50 offset:16896\n" \
"v_mfma_f32_16x16x32_bf16 a[148:151], v[100:103], v[172:175], a[148:151]\n" \
"s_add_u32 s64, s12, s68\n" \
"s_addc_u32 s65, s13, 0\n" \
"v_mfma_f32_16x16x32_bf16 a[152:155], v[96:99], v[176:179], a[152:155]\n" \
"ds_read_b128 v[92:95], v49 offset:6144\n" \
"v_mfma_f32_16x16x32_bf16 a[152:155], v[100:103], v[180:183], a[152:155]\n" \
"global_load_dword v253, v248, s[64:65]\n" \
"v_mfma_f32_16x16x32_bf16 a[156:159], v[96:99], v[184:187], a[156:159]\n" \
"ds_read_b64_tr_b16 v[150:151], v50 offset:20992\n" \
"v_mfma_f32_16x16x32_bf16 a[156:159], v[100:103], v[188:191], a[156:159]\n" \
"v_mfma_f32_16x16x32_bf16 a[188:191], v[104:107], v[184:187], a[188:191]\n" \
"ds_read_b128 v[96:99], v48 offset:8192\n" \
"v_mfma_f32_16x16x32_bf16 a[188:191], v[108:111], v[188:191], a[188:191]\n" \
"v_mfma_f32_16x16x32_bf16 a[184:187], v[104:107], v[176:179], a[184:187]\n" \
"ds_read_b64_tr_b16 v[152:153], v51 offset:512\n" \
"v_mfma_f32_16x16x32_bf16 a[184:187], v[108:111], v[180:183], a[184:187]\n" \
"v_mfma_f32_16x16x32_bf16 a[180:183], v[104:107], v[168:171], a[180:183]\n" \
"ds_read_b128 v[100:103], v49 offset:8192\n" \
"v_mfma_f32_16x16x32_bf16 a[180:183], v[108:111], v[172:175], a[180:183]\n" \
"v_mfma_f32_16x16x32_bf16 a[176:179], v[104:107], v[160:163], a[176:179]\n" \
"ds_read_b64_tr_b16 v[154:155], v51 offset:4608\n" \
"v_mfma_f32_16x16x32_bf16 a[176:179], v[108:111], v[164:167], a[176:179]\n" \
"v_mfma_f32_16x16x32_bf16 a[208:211], v[112:115], v[160:163], a[208:211]\n" \
"ds_read_b128 v[104:107], v48 offset:10240\n" \
"v_mfma_f32_16x16x32_bf16 a[208:211], v[116:119], v[164:167], a[208:211]\n" \
"v_mfma_f32_16x16x32_bf16 a[212:215], v[112:115], v[168:171], a[212:215]\n" \
"ds_read_b64_tr_b16 v[156:157], v51 offset:16896\n" \
"v_mfma_f32_16x16x32_bf16 a[212:215], v[116:119], v[172:175], a[212:215]\n" \
"v_mfma_f32_16x16x32_bf16 a[216:219], v[112:115], v[176:179], a[216:219]\n" \
"ds_read_b128 v[108:111], v49 offset:10240\n" \
"v_mfma_f32_16x16x32_bf16 a[216:219], v[116:119], v[180:183], a[216:219]\n" \
"v_mfma_f32_16x16x32_bf16 a[220:223], v[112:115], v[184:187], a[220:223]\n" \
"ds_read_b64_tr_b16 v[158:159], v51 offset:20992\n" \
"v_mfma_f32_16x16x32_bf16 a[220:223], v[116:119], v[188:191], a[220:223]\n" \
"v_mfma_f32_16x16x32_bf16 a[252:255], v[120:123], v[184:187], a[252:255]\n" \
"ds_read_b128 v[112:115], v48 offset:12288\n" \
"v_mfma_f32_16x16x32_bf16 a[252:255], v[124:127], v[188:191], a[252:255]\n" \
"v_mfma_f32_16x16x32_bf16 a[248:251], v[120:123], v[176:179], a[248:251]\n" \
"v_mfma_f32_16x16x32_bf16 a[248:251], v[124:127], v[180:183], a[248:251]\n" \
"v_mfma_f32_16x16x32_bf16 a[244:247], v[120:123], v[168:171], a[244:247]\n" \
"ds_read_b128 v[116:119], v49 offset:12288\n" \
"v_mfma_f32_16x16x32_bf16 a[244:247], v[124:127], v[172:175], a[244:247]\n" \
"v_mfma_f32_16x16x32_bf16 a[240:243], v[120:123], v[160:163], a[240:243]\n" \
"v_mfma_f32_16x16x32_bf16 a[240:243], v[124:127], v[164:167], a[240:243]\n" \
"s_waitcnt lgkmcnt(1)\n" \
"v_mfma_f32_16x16x32_bf16 a[0:3], v[128:131], v[64:67], a[0:3]\n" \
"ds_read_b128 v[120:123], v48 offset:14336\n" \
"v_mfma_f32_16x16x32_bf16 a[0:3], v[132:135], v[68:71], a[0:3]\n" \
"v_mfma_f32_16x16x32_bf16 a[4:7], v[136:139], v[64:67], a[4:7]\n" \
"ds_read_b128 v[124:127], v49 offset:14336\n" \
"s_add_u32 m0, s95, 4096\n" \
"v_mfma_f32_16x16x32_bf16 a[4:7], v[140:143], v[68:71], a[4:7]\n" \
"global_load_lds_dwordx4 v228, s[44:45] offset:-4096\n" \
"v_mfma_f32_16x16x32_bf16 a[8:11], v[144:147], v[64:67], a[8:11]\n" \
"ds_read_b64_tr_b16 v[160:161], v50 offset:1024\n" \
"v_mfma_f32_16x16x32_bf16 a[8:11], v[148:151], v[68:71], a[8:11]\n" \
"v_mfma_f32_16x16x32_bf16 a[12:15], v[152:155], v[64:67], a[12:15]\n" \
"ds_read_b64_tr_b16 v[162:163], v50 offset:5120\n" \
"v_mfma_f32_16x16x32_bf16 a[12:15], v[156:159], v[68:71], a[12:15]\n" \
"global_load_lds_dwordx4 v229, s[44:45] offset:-3072\n" \
"v_mfma_f32_16x16x32_bf16 a[44:47], v[152:155], v[72:75], a[44:47]\n" \
"s_add_u32 s44, s44, 0x180000\n" \
"s_addc_u32 s45, s45, 0\n" \
"ds_read_b64_tr_b16 v[164:165], v50 offset:17408\n" \
"v_mfma_f32_16x16x32_bf16 a[44:47], v[156:159], v[76:79], a[44:47]\n" \
"v_mfma_f32_16x16x32_bf16 a[40:43], v[144:147], v[72:75], a[40:43]\n" \
"ds_read_b64_tr_b16 v[166:167], v50 offset:21504\n" \
"v_mfma_f32_16x16x32_bf16 a[40:43], v[148:151], v[76:79], a[40:43]\n" \
"global_load_lds_dwordx4 v228, s[42:43] offset:-2048\n" \
"v_mfma_f32_16x16x32_bf16 a[36:39], v[136:139], v[72:75], a[36:39]\n" \
"ds_read_b64_tr_b16 v[168:169], v51 offset:1024\n" \
"v_mfma_f32_16x16x32_bf16 a[36:39], v[140:143], v[76:79], a[36:39]\n" \
"v_mfma_f32_16x16x32_bf16 a[32:35], v[128:131], v[72:75], a[32:35]\n" \
"ds_read_b64_tr_b16 v[170:171], v51 offset:5120\n" \
"v_mfma_f32_16x16x32_bf16 a[32:35], v[132:135], v[76:79], a[32:35]\n" \
"global_load_lds_dwordx4 v229, s[42:43] offset:-1024\n" \
"v_mfma_f32_16x16x32_bf16 a[64:67], v[128:131], v[80:83], a[64:67]\n" \
"s_add_u32 s42, s42, 0x180000\n" \
"s_addc_u32 s43, s43, 0\n" \
"ds_read_b64_tr_b16 v[172:173], v51 offset:17408\n" \
"v_mfma_f32_16x16x32_bf16 a[64:67], v[132:135], v[84:87], a[64:67]\n" \
"v_mfma_f32_16x16x32_bf16 a[68:71], v[136:139], v[80:83], a[68:71]\n" \
"s_add_u32 m0, s95, 20480\n" \
"ds_read_b64_tr_b16 v[174:175], v51 offset:21504\n" \
"v_mfma_f32_16x16x32_bf16 a[68:71], v[140:143], v[84:87], a[68:71]\n" \
"global_load_lds_dwordx4 v228, s[44:45] offset:-4096\n" \
"v_mfma_f32_16x16x32_bf16 a[72:75], v[144:147], v[80:83], a[72:75]\n" \
"ds_read_b64_tr_b16 v[176:177], v50 offset:1536\n" \
"v_mfma_f32_16x16x32_bf16 a[72:75], v[148:151], v[84:87], a[72:75]\n" \
"v_mfma_f32_16x16x32_bf16 a[76:79], v[152:155], v[80:83], a[76:79]\n" \
"ds_read_b64_tr_b16 v[178:179], v50 offset:5632\n" \
"v_mfma_f32_16x16x32_bf16 a[76:79], v[156:159], v[84:87], a[76:79]\n" \
"global_load_lds_dwordx4 v229, s[44:45] offset:-3072\n" \
"v_mfma_f32_16x16x32_bf16 a[108:111], v[152:155], v[88:91], a[108:111]\n" \
"s_add_u32 s44, s44, 0x400000\n" \
"Lpg1682_%=:\n" \
"Lst0x2_%=:\n" \
"s_cbranch_execz Lpg3849_%=\n" \
"s_addc_u32 s45, s45, 0\n" \
"ds_read_b64_tr_b16 v[180:181], v50 offset:17920\n" \
"v_mfma_f32_16x16x32_bf16 a[108:111], v[156:159], v[92:95], a[108:111]\n" \
"v_mfma_f32_16x16x32_bf16 a[104:107], v[144:147], v[88:91], a[104:107]\n" \
"ds_read_b64_tr_b16 v[182:183], v50 offset:22016\n" \
"v_mfma_f32_16x16x32_bf16 a[104:107], v[148:151], v[92:95], a[104:107]\n" \
"global_load_lds_dwordx4 v228, s[42:43] offset:-2048\n" \
"v_mfma_f32_16x16x32_bf16 a[100:103], v[136:139], v[88:91], a[100:103]\n" \
"ds_read_b64_tr_b16 v[184:185], v51 offset:1536\n" \
"v_mfma_f32_16x16x32_bf16 a[100:103], v[140:143], v[92:95], a[100:103]\n" \
"v_mfma_f32_16x16x32_bf16 a[96:99], v[128:131], v[88:91], a[96:99]\n" \
"ds_read_b64_tr_b16 v[186:187], v51 offset:5632\n" \
"v_mfma_f32_16x16x32_bf16 a[96:99], v[132:135], v[92:95], a[96:99]\n" \
"global_load_lds_dwordx4 v229, s[42:43] offset:-1024\n" \
"v_mfma_f32_16x16x32_bf16 a[128:131], v[128:131], v[96:99], a[128:131]\n" \
"s_add_u32 s42, s42, 0x400000\n" \
"s_addc_u32 s43, s43, 0\n" \
"ds_read_b64_tr_b16 v[188:189], v51 offset:17920\n" \
"v_mfma_f32_16x16x32_bf16 a[128:131], v[132:135], v[100:103], a[128:131]\n" \
"v_mfma_f32_16x16x32_bf16 a[132:135], v[136:139], v[96:99], a[132:135]\n" \
"ds_read_b64_tr_b16 v[190:191], v51 offset:22016\n" \
"v_mfma_f32_16x16x32_bf16 a[132:135], v[140:143], v[100:103], a[132:135]\n" \
"v_mfma_f32_16x16x32_bf16 a[136:139], v[144:147], v[96:99], a[136:139]\n" \
"v_mfma_f32_16x16x32_bf16 a[136:139], v[148:151], v[100:103], a[136:139]\n" \
"v_mfma_f32_16x16x32_bf16 a[140:143], v[152:155], v[96:99], a[140:143]\n" \
"v_mfma_f32_16x16x32_bf16 a[140:143], v[156:159], v[100:103], a[140:143]\n" \
"v_mfma_f32_16x16x32_bf16 a[172:175], v[152:155], v[104:107], a[172:175]\n" \
"s_mul_i32 s76, s73, 0x0\n" \
"s_add_u32 s77, s76, 0xffff8000\n" \
"s_sub_u32 s78, 0xffff8000, s76\n" \
"v_mfma_f32_16x16x32_bf16 a[172:175], v[156:159], v[108:111], a[172:175]\n" \
"v_add_u32 v48, s77, v48\n" \
"v_add_u32 v49, s77, v49\n" \
"v_mfma_f32_16x16x32_bf16 a[168:171], v[144:147], v[104:107], a[168:171]\n" \
"v_add_u32 v50, s78, v50\n" \
"v_add_u32 v51, s78, v51\n" \
"v_mfma_f32_16x16x32_bf16 a[168:171], v[148:151], v[108:111], a[168:171]\n" \
"v_mfma_f32_16x16x32_bf16 a[164:167], v[136:139], v[104:107], a[164:167]\n" \
"v_mfma_f32_16x16x32_bf16 a[164:167], v[140:143], v[108:111], a[164:167]\n" \
"v_mfma_f32_16x16x32_bf16 a[160:163], v[128:131], v[104:107], a[160:163]\n" \
"v_mfma_f32_16x16x32_bf16 a[160:163], v[132:135], v[108:111], a[160:163]\n" \
"s_waitcnt lgkmcnt(0)\n" \
"v_mfma_f32_16x16x32_bf16 a[192:195], v[128:131], v[112:115], a[192:195]\n" \
"v_mfma_f32_16x16x32_bf16 a[192:195], v[132:135], v[116:119], a[192:195]\n" \
"v_mfma_f32_16x16x32_bf16 a[196:199], v[136:139], v[112:115], a[196:199]\n" \
"v_mfma_f32_16x16x32_bf16 a[196:199], v[140:143], v[116:119], a[196:199]\n" \
"v_mfma_f32_16x16x32_bf16 a[200:203], v[144:147], v[112:115], a[200:203]\n" \
"v_mfma_f32_16x16x32_bf16 a[200:203], v[148:151], v[116:119], a[200:203]\n" \
"v_mfma_f32_16x16x32_bf16 a[204:207], v[152:155], v[112:115], a[204:207]\n" \
"v_mfma_f32_16x16x32_bf16 a[204:207], v[156:159], v[116:119], a[204:207]\n" \
"v_mfma_f32_16x16x32_bf16 a[236:239], v[152:155], v[120:123], a[236:239]\n" \
"v_mfma_f32_16x16x32_bf16 a[236:239], v[156:159], v[124:127], a[236:239]\n" \
"v_mfma_f32_16x16x32_bf16 a[232:235], v[144:147], v[120:123], a[232:235]\n" \
"v_mfma_f32_16x16x32_bf16 a[232:235], v[148:151], v[124:127], a[232:235]\n" \
"v_mfma_f32_16x16x32_bf16 a[228:231], v[136:139], v[120:123], a[228:231]\n" \
"v_mfma_f32_16x16x32_bf16 a[228:231], v[140:143], v[124:127], a[228:231]\n" \
"s_waitcnt vmcnt(10) lgkmcnt(0)\n" \
"v_mfma_f32_16x16x32_bf16 a[224:227], v[128:131], v[120:123], a[224:227]\n" \
"v_mfma_f32_16x16x32_bf16 a[224:227], v[132:135], v[124:127], a[224:227]\n" \
"s_barrier\n" \
"v_mfma_f32_16x16x32_bf16 a[16:19], v[64:67], v[160:163], a[16:19]\n" \
"s_add_u32 m0, s87, 135168\n" \
"ds_read_b64_tr_b16 v[128:129], v50 offset:0\n" \
"v_mfma_f32_16x16x32_bf16 a[16:19], v[68:71], v[164:167], a[16:19]\n" \
"global_load_lds_dwordx4 v224, s[48:49] offset:-4096\n" \
"v_mfma_f32_16x16x32_bf16 a[20:23], v[64:67], v[168:171], a[20:23]\n" \
"ds_read_b64_tr_b16 v[130:131], v50 offset:4096\n" \
"v_mfma_f32_16x16x32_bf16 a[20:23], v[68:71], v[172:175], a[20:23]\n" \
"v_mfma_f32_16x16x32_bf16 a[24:27], v[64:67], v[176:179], a[24:27]\n" \
"ds_read_b64_tr_b16 v[132:133], v50 offset:16384\n" \
"v_mfma_f32_16x16x32_bf16 a[24:27], v[68:71], v[180:183], a[24:27]\n" \
"global_load_lds_dwordx4 v225, s[48:49] offset:-3072\n" \
"v_mfma_f32_16x16x32_bf16 a[28:31], v[64:67], v[184:187], a[28:31]\n" \
"ds_read_b64_tr_b16 v[134:135], v50 offset:20480\n" \
"v_mfma_f32_16x16x32_bf16 a[28:31], v[68:71], v[188:191], a[28:31]\n" \
"v_mfma_f32_16x16x32_bf16 a[60:63], v[72:75], v[184:187], a[60:63]\n" \
"ds_read_b128 v[64:67], v48 offset:0\n" \
"v_mfma_f32_16x16x32_bf16 a[60:63], v[76:79], v[188:191], a[60:63]\n" \
"global_load_lds_dwordx4 v226, s[48:49] offset:-2048\n" \
"v_mfma_f32_16x16x32_bf16 a[56:59], v[72:75], v[176:179], a[56:59]\n" \
"ds_read_b64_tr_b16 v[136:137], v51 offset:0\n" \
"v_mfma_f32_16x16x32_bf16 a[56:59], v[76:79], v[180:183], a[56:59]\n" \
"v_mfma_f32_16x16x32_bf16 a[52:55], v[72:75], v[168:171], a[52:55]\n" \
"ds_read_b128 v[68:71], v49 offset:0\n" \
"v_mfma_f32_16x16x32_bf16 a[52:55], v[76:79], v[172:175], a[52:55]\n" \
"global_load_lds_dwordx4 v227, s[48:49] offset:-1024\n" \
"v_mfma_f32_16x16x32_bf16 a[48:51], v[72:75], v[160:163], a[48:51]\n" \
"s_add_u32 s48, s48, 0x180\n" \
"s_addc_u32 s49, s49, 0\n" \
"ds_read_b64_tr_b16 v[138:139], v51 offset:4096\n" \
"v_mfma_f32_16x16x32_bf16 a[48:51], v[76:79], v[164:167], a[48:51]\n" \
"v_mfma_f32_16x16x32_bf16 a[80:83], v[80:83], v[160:163], a[80:83]\n" \
"ds_read_b128 v[72:75], v48 offset:2048\n" \
"v_mfma_f32_16x16x32_bf16 a[80:83], v[84:87], v[164:167], a[80:83]\n" \
"global_load_lds_dwordx4 v224, s[46:47] offset:0\n" \
"v_mfma_f32_16x16x32_bf16 a[84:87], v[80:83], v[168:171], a[84:87]\n" \
"ds_read_b64_tr_b16 v[140:141], v51 offset:16384\n" \
"v_mfma_f32_16x16x32_bf16 a[84:87], v[84:87], v[172:175], a[84:87]\n" \
"v_mfma_f32_16x16x32_bf16 a[88:91], v[80:83], v[176:179], a[88:91]\n" \
"ds_read_b128 v[76:79], v49 offset:2048\n" \
"v_mfma_f32_16x16x32_bf16 a[88:91], v[84:87], v[180:183], a[88:91]\n" \
"global_load_lds_dwordx4 v225, s[46:47] offset:1024\n" \
"v_mfma_f32_16x16x32_bf16 a[92:95], v[80:83], v[184:187], a[92:95]\n" \
"ds_read_b64_tr_b16 v[142:143], v51 offset:20480\n" \
"v_mfma_f32_16x16x32_bf16 a[92:95], v[84:87], v[188:191], a[92:95]\n" \
"v_mfma_f32_16x16x32_bf16 a[124:127], v[88:91], v[184:187], a[124:127]\n" \
"ds_read_b128 v[80:83], v48 offset:4096\n" \
"v_mfma_f32_16x16x32_bf16 a[124:127], v[92:95], v[188:191], a[124:127]\n" \
"global_load_lds_dwordx4 v226, s[46:47] offset:2048\n" \
"v_mfma_f32_16x16x32_bf16 a[120:123], v[88:91], v[176:179], a[120:123]\n" \
"ds_read_b64_tr_b16 v[144:145], v50 offset:512\n" \
"v_mfma_f32_16x16x32_bf16 a[120:123], v[92:95], v[180:183], a[120:123]\n" \
"v_mfma_f32_16x16x32_bf16 a[116:119], v[88:91], v[168:171], a[116:119]\n" \
"ds_read_b128 v[84:87], v49 offset:4096\n" \
"v_mfma_f32_16x16x32_bf16 a[116:119], v[92:95], v[172:175], a[116:119]\n" \
"global_load_lds_dwordx4 v227, s[46:47] offset:3072\n" \
"v_mfma_f32_16x16x32_bf16 a[112:115], v[88:91], v[160:163], a[112:115]\n" \
"s_add_u32 s46, s46, 0x180\n" \
"s_addc_u32 s47, s47, 0\n" \
"ds_read_b64_tr_b16 v[146:147], v50 offset:4608\n" \
"v_mfma_f32_16x16x32_bf16 a[112:115], v[92:95], v[164:167], a[112:115]\n" \
"s_add_u32 s68, s67, 0x600000\n" \
"s_add_u32 s64, s12, s68\n" \
"s_addc_u32 s65, s13, 0\n" \
"v_mfma_f32_16x16x32_bf16 a[144:147], v[96:99], v[160:163], a[144:147]\n" \
"ds_read_b128 v[88:91], v48 offset:6144\n" \
"v_mfma_f32_16x16x32_bf16 a[144:147], v[100:103], v[164:167], a[144:147]\n" \
"global_load_dword v253, v248, s[64:65]\n" \
"v_mfma_f32_16x16x32_bf16 a[148:151], v[96:99], v[168:171], a[148:151]\n" \
"s_add_u32 s68, s67, 0x700000\n" \
"ds_read_b64_tr_b16 v[148:149], v50 offset:16896\n" \
"v_mfma_f32_16x16x32_bf16 a[148:151], v[100:103], v[172:175], a[148:151]\n" \
"s_add_u32 s64, s12, s68\n" \
"s_addc_u32 s65, s13, 0\n" \
"v_mfma_f32_16x16x32_bf16 a[152:155], v[96:99], v[176:179], a[152:155]\n" \
"ds_read_b128 v[92:95], v49 offset:6144\n" \
"v_mfma_f32_16x16x32_bf16 a[152:155], v[100:103], v[180:183], a[152:155]\n" \
"global_load_dword v253, v248, s[64:65]\n" \
"v_mfma_f32_16x16x32_bf16 a[156:159], v[96:99], v[184:187], a[156:159]\n" \
"s_lshr_b32 s68, s66, 2\n" \
"s_and_b32 s68, s68, 0xffff\n" \
"ds_read_b64_tr_b16 v[150:151], v50 offset:20992\n" \
"v_mfma_f32_16x16x32_bf16 a[156:159], v[100:103], v[188:191], a[156:159]\n" \
"s_add_u32 s68, s68, 65585152\n" \
"v_mfma_f32_16x16x32_bf16 a[188:191], v[104:107], v[184:187], a[188:191]\n" \
"ds_read_b128 v[96:99], v48 offset:8192\n" \
"v_mfma_f32_16x16x32_bf16 a[188:191], v[108:111], v[188:191], a[188:191]\n" \
"buffer_load_dword v253, v245, s[56:59], s68 offen sc1\n" \
"v_mfma_f32_16x16x32_bf16 a[184:187], v[104:107], v[176:179], a[184:187]\n" \
"ds_read_b64_tr_b16 v[152:153], v51 offset:512\n" \
"v_mfma_f32_16x16x32_bf16 a[184:187], v[108:111], v[180:183], a[184:187]\n" \
"v_mfma_f32_16x16x32_bf16 a[180:183], v[104:107], v[168:171], a[180:183]\n" \
"ds_read_b128 v[100:103], v49 offset:8192\n" \
"v_mfma_f32_16x16x32_bf16 a[180:183], v[108:111], v[172:175], a[180:183]\n" \
"v_mfma_f32_16x16x32_bf16 a[176:179], v[104:107], v[160:163], a[176:179]\n" \
"ds_read_b64_tr_b16 v[154:155], v51 offset:4608\n" \
"v_mfma_f32_16x16x32_bf16 a[176:179], v[108:111], v[164:167], a[176:179]\n" \
"v_mfma_f32_16x16x32_bf16 a[208:211], v[112:115], v[160:163], a[208:211]\n" \
"ds_read_b128 v[104:107], v48 offset:10240\n" \
"v_mfma_f32_16x16x32_bf16 a[208:211], v[116:119], v[164:167], a[208:211]\n" \
"v_mfma_f32_16x16x32_bf16 a[212:215], v[112:115], v[168:171], a[212:215]\n" \
"ds_read_b64_tr_b16 v[156:157], v51 offset:16896\n" \
"v_mfma_f32_16x16x32_bf16 a[212:215], v[116:119], v[172:175], a[212:215]\n" \
"v_mfma_f32_16x16x32_bf16 a[216:219], v[112:115], v[176:179], a[216:219]\n" \
"ds_read_b128 v[108:111], v49 offset:10240\n" \
"v_mfma_f32_16x16x32_bf16 a[216:219], v[116:119], v[180:183], a[216:219]\n" \
"v_mfma_f32_16x16x32_bf16 a[220:223], v[112:115], v[184:187], a[220:223]\n" \
"ds_read_b64_tr_b16 v[158:159], v51 offset:20992\n" \
"v_mfma_f32_16x16x32_bf16 a[220:223], v[116:119], v[188:191], a[220:223]\n" \
"v_mfma_f32_16x16x32_bf16 a[252:255], v[120:123], v[184:187], a[252:255]\n" \
"ds_read_b128 v[112:115], v48 offset:12288\n" \
"v_mfma_f32_16x16x32_bf16 a[252:255], v[124:127], v[188:191], a[252:255]\n" \
"v_mfma_f32_16x16x32_bf16 a[248:251], v[120:123], v[176:179], a[248:251]\n" \
"v_mfma_f32_16x16x32_bf16 a[248:251], v[124:127], v[180:183], a[248:251]\n" \
"v_mfma_f32_16x16x32_bf16 a[244:247], v[120:123], v[168:171], a[244:247]\n" \
"ds_read_b128 v[116:119], v49 offset:12288\n" \
"v_mfma_f32_16x16x32_bf16 a[244:247], v[124:127], v[172:175], a[244:247]\n" \
"v_mfma_f32_16x16x32_bf16 a[240:243], v[120:123], v[160:163], a[240:243]\n" \
"v_mfma_f32_16x16x32_bf16 a[240:243], v[124:127], v[164:167], a[240:243]\n" \
"s_waitcnt lgkmcnt(1)\n" \
"v_mfma_f32_16x16x32_bf16 a[0:3], v[128:131], v[64:67], a[0:3]\n" \
"ds_read_b128 v[120:123], v48 offset:14336\n" \
"v_mfma_f32_16x16x32_bf16 a[0:3], v[132:135], v[68:71], a[0:3]\n" \
"v_mfma_f32_16x16x32_bf16 a[4:7], v[136:139], v[64:67], a[4:7]\n" \
"ds_read_b128 v[124:127], v49 offset:14336\n" \
"s_add_u32 m0, s87, 69632\n" \
"v_mfma_f32_16x16x32_bf16 a[4:7], v[140:143], v[68:71], a[4:7]\n" \
"global_load_lds_dwordx4 v224, s[48:49] offset:-4096\n" \
"v_mfma_f32_16x16x32_bf16 a[8:11], v[144:147], v[64:67], a[8:11]\n" \
"ds_read_b64_tr_b16 v[160:161], v50 offset:1024\n" \
"v_mfma_f32_16x16x32_bf16 a[8:11], v[148:151], v[68:71], a[8:11]\n" \
"v_mfma_f32_16x16x32_bf16 a[12:15], v[152:155], v[64:67], a[12:15]\n" \
"ds_read_b64_tr_b16 v[162:163], v50 offset:5120\n" \
"v_mfma_f32_16x16x32_bf16 a[12:15], v[156:159], v[68:71], a[12:15]\n" \
"global_load_lds_dwordx4 v225, s[48:49] offset:-3072\n" \
"v_mfma_f32_16x16x32_bf16 a[44:47], v[152:155], v[72:75], a[44:47]\n" \
"ds_read_b64_tr_b16 v[164:165], v50 offset:17408\n" \
"v_mfma_f32_16x16x32_bf16 a[44:47], v[156:159], v[76:79], a[44:47]\n" \
"v_mfma_f32_16x16x32_bf16 a[40:43], v[144:147], v[72:75], a[40:43]\n" \
"ds_read_b64_tr_b16 v[166:167], v50 offset:21504\n" \
"v_mfma_f32_16x16x32_bf16 a[40:43], v[148:151], v[76:79], a[40:43]\n" \
"global_load_lds_dwordx4 v226, s[48:49] offset:-2048\n" \
"v_mfma_f32_16x16x32_bf16 a[36:39], v[136:139], v[72:75], a[36:39]\n" \
"ds_read_b64_tr_b16 v[168:169], v51 offset:1024\n" \
"v_mfma_f32_16x16x32_bf16 a[36:39], v[140:143], v[76:79], a[36:39]\n" \
"v_mfma_f32_16x16x32_bf16 a[32:35], v[128:131], v[72:75], a[32:35]\n" \
"ds_read_b64_tr_b16 v[170:171], v51 offset:5120\n" \
"v_mfma_f32_16x16x32_bf16 a[32:35], v[132:135], v[76:79], a[32:35]\n" \
"global_load_lds_dwordx4 v227, s[48:49] offset:-1024\n" \
"v_mfma_f32_16x16x32_bf16 a[64:67], v[128:131], v[80:83], a[64:67]\n" \
"s_add_u32 s48, s48, 0x400\n" \
"s_addc_u32 s49, s49, 0\n" \
"ds_read_b64_tr_b16 v[172:173], v51 offset:17408\n" \
"v_mfma_f32_16x16x32_bf16 a[64:67], v[132:135], v[84:87], a[64:67]\n" \
"v_mfma_f32_16x16x32_bf16 a[68:71], v[136:139], v[80:83], a[68:71]\n" \
"ds_read_b64_tr_b16 v[174:175], v51 offset:21504\n" \
"v_mfma_f32_16x16x32_bf16 a[68:71], v[140:143], v[84:87], a[68:71]\n" \
"global_load_lds_dwordx4 v224, s[46:47] offset:0\n" \
"v_mfma_f32_16x16x32_bf16 a[72:75], v[144:147], v[80:83], a[72:75]\n" \
"ds_read_b64_tr_b16 v[176:177], v50 offset:1536\n" \
"v_mfma_f32_16x16x32_bf16 a[72:75], v[148:151], v[84:87], a[72:75]\n" \
"v_mfma_f32_16x16x32_bf16 a[76:79], v[152:155], v[80:83], a[76:79]\n" \
"ds_read_b64_tr_b16 v[178:179], v50 offset:5632\n" \
"v_mfma_f32_16x16x32_bf16 a[76:79], v[156:159], v[84:87], a[76:79]\n" \
"global_load_lds_dwordx4 v225, s[46:47] offset:1024\n" \
"v_mfma_f32_16x16x32_bf16 a[108:111], v[152:155], v[88:91], a[108:111]\n" \
"ds_read_b64_tr_b16 v[180:181], v50 offset:17920\n" \
"v_mfma_f32_16x16x32_bf16 a[108:111], v[156:159], v[92:95], a[108:111]\n" \
"v_mfma_f32_16x16x32_bf16 a[104:107], v[144:147], v[88:91], a[104:107]\n" \
"ds_read_b64_tr_b16 v[182:183], v50 offset:22016\n" \
"v_mfma_f32_16x16x32_bf16 a[104:107], v[148:151], v[92:95], a[104:107]\n" \
"global_load_lds_dwordx4 v226, s[46:47] offset:2048\n" \
"v_mfma_f32_16x16x32_bf16 a[100:103], v[136:139], v[88:91], a[100:103]\n" \
"ds_read_b64_tr_b16 v[184:185], v51 offset:1536\n" \
"v_mfma_f32_16x16x32_bf16 a[100:103], v[140:143], v[92:95], a[100:103]\n" \
"v_mfma_f32_16x16x32_bf16 a[96:99], v[128:131], v[88:91], a[96:99]\n" \
"ds_read_b64_tr_b16 v[186:187], v51 offset:5632\n" \
"v_mfma_f32_16x16x32_bf16 a[96:99], v[132:135], v[92:95], a[96:99]\n" \
"global_load_lds_dwordx4 v227, s[46:47] offset:3072\n" \
"v_mfma_f32_16x16x32_bf16 a[128:131], v[128:131], v[96:99], a[128:131]\n" \
"s_add_u32 s46, s46, 0x400\n" \
"s_addc_u32 s47, s47, 0\n" \
"ds_read_b64_tr_b16 v[188:189], v51 offset:17920\n" \
"v_mfma_f32_16x16x32_bf16 a[128:131], v[132:135], v[100:103], a[128:131]\n" \
"v_mfma_f32_16x16x32_bf16 a[132:135], v[136:139], v[96:99], a[132:135]\n" \
"ds_read_b64_tr_b16 v[190:191], v51 offset:22016\n" \
"v_mfma_f32_16x16x32_bf16 a[132:135], v[140:143], v[100:103], a[132:135]\n" \
"v_mfma_f32_16x16x32_bf16 a[136:139], v[144:147], v[96:99], a[136:139]\n" \
"v_mfma_f32_16x16x32_bf16 a[136:139], v[148:151], v[100:103], a[136:139]\n" \
"v_mfma_f32_16x16x32_bf16 a[140:143], v[152:155], v[96:99], a[140:143]\n" \
"v_mfma_f32_16x16x32_bf16 a[140:143], v[156:159], v[100:103], a[140:143]\n" \
"v_mfma_f32_16x16x32_bf16 a[172:175], v[152:155], v[104:107], a[172:175]\n" \
"v_mfma_f32_16x16x32_bf16 a[172:175], v[156:159], v[108:111], a[172:175]\n" \
"v_mfma_f32_16x16x32_bf16 a[168:171], v[144:147], v[104:107], a[168:171]\n" \
"v_mfma_f32_16x16x32_bf16 a[168:171], v[148:151], v[108:111], a[168:171]\n" \
"v_mfma_f32_16x16x32_bf16 a[164:167], v[136:139], v[104:107], a[164:167]\n" \
"v_mfma_f32_16x16x32_bf16 a[164:167], v[140:143], v[108:111], a[164:167]\n" \
"v_mfma_f32_16x16x32_bf16 a[160:163], v[128:131], v[104:107], a[160:163]\n" \
"v_mfma_f32_16x16x32_bf16 a[160:163], v[132:135], v[108:111], a[160:163]\n" \
"s_waitcnt lgkmcnt(0)\n" \
"v_mfma_f32_16x16x32_bf16 a[192:195], v[128:131], v[112:115], a[192:195]\n" \
"v_mfma_f32_16x16x32_bf16 a[192:195], v[132:135], v[116:119], a[192:195]\n" \
"v_mfma_f32_16x16x32_bf16 a[196:199], v[136:139], v[112:115], a[196:199]\n" \
"v_mfma_f32_16x16x32_bf16 a[196:199], v[140:143], v[116:119], a[196:199]\n" \
"v_mfma_f32_16x16x32_bf16 a[200:203], v[144:147], v[112:115], a[200:203]\n" \
"v_mfma_f32_16x16x32_bf16 a[200:203], v[148:151], v[116:119], a[200:203]\n" \
"v_mfma_f32_16x16x32_bf16 a[204:207], v[152:155], v[112:115], a[204:207]\n" \
"v_mfma_f32_16x16x32_bf16 a[204:207], v[156:159], v[116:119], a[204:207]\n" \
"v_mfma_f32_16x16x32_bf16 a[236:239], v[152:155], v[120:123], a[236:239]\n" \
"v_mfma_f32_16x16x32_bf16 a[236:239], v[156:159], v[124:127], a[236:239]\n" \
"v_mfma_f32_16x16x32_bf16 a[232:235], v[144:147], v[120:123], a[232:235]\n" \
"v_mfma_f32_16x16x32_bf16 a[232:235], v[148:151], v[124:127], a[232:235]\n" \
"v_mfma_f32_16x16x32_bf16 a[228:231], v[136:139], v[120:123], a[228:231]\n" \
"v_mfma_f32_16x16x32_bf16 a[228:231], v[140:143], v[124:127], a[228:231]\n" \
"s_waitcnt vmcnt(11) lgkmcnt(0)\n" \
"v_mfma_f32_16x16x32_bf16 a[224:227], v[128:131], v[120:123], a[224:227]\n" \
"v_mfma_f32_16x16x32_bf16 a[224:227], v[132:135], v[124:127], a[224:227]\n" \
"s_barrier\n" \
"v_mfma_f32_16x16x32_bf16 a[16:19], v[64:67], v[160:163], a[16:19]\n" \
"s_add_u32 m0, s87, 102400\n" \
"v_mfma_f32_16x16x32_bf16 a[16:19], v[68:71], v[164:167], a[16:19]\n" \
"global_load_lds_dwordx4 v224, s[48:49] offset:-4096\n" \
"v_mfma_f32_16x16x32_bf16 a[20:23], v[64:67], v[168:171], a[20:23]\n" \
"ds_read_b64_tr_b16 v[0:1], v232 offset:0\n" \
"v_mfma_f32_16x16x32_bf16 a[20:23], v[68:71], v[172:175], a[20:23]\n" \
"v_accvgpr_read_b32 v234, a192\n" \
"v_accvgpr_read_b32 v235, a193\n" \
"v_mfma_f32_16x16x32_bf16 a[24:27], v[64:67], v[176:179], a[24:27]\n" \
"ds_read_b64_tr_b16 v[2:3], v232 offset:2048\n" \
"v_accvgpr_read_b32 v236, a194\n" \
"v_mfma_f32_16x16x32_bf16 a[24:27], v[68:71], v[180:183], a[24:27]\n" \
"global_load_lds_dwordx4 v225, s[48:49] offset:-3072\n" \
"v_accvgpr_read_b32 v237, a195\n" \
"v_mfma_f32_16x16x32_bf16 a[28:31], v[64:67], v[184:187], a[28:31]\n" \
"ds_read_b64_tr_b16 v[4:5], v232 offset:8192\n" \
"v_mul_f32 v234, s92, v234\n" \
"v_mfma_f32_16x16x32_bf16 a[28:31], v[68:71], v[188:191], a[28:31]\n" \
"v_mul_f32 v235, s92, v235\n" \
"v_mul_f32 v236, s92, v236\n" \
"v_mfma_f32_16x16x32_bf16 a[60:63], v[72:75], v[184:187], a[60:63]\n" \
"ds_read_b64_tr_b16 v[6:7], v232 offset:10240\n" \
"v_mul_f32 v237, s92, v237\n" \
"v_mfma_f32_16x16x32_bf16 a[60:63], v[76:79], v[188:191], a[60:63]\n" \
"global_load_lds_dwordx4 v226, s[48:49] offset:-2048\n" \
"v_cvt_pk_bf16_f32 v234, v234, v235\n" \
"v_mfma_f32_16x16x32_bf16 a[56:59], v[72:75], v[176:179], a[56:59]\n" \
"ds_read_b64_tr_b16 v[8:9], v233 offset:0\n" \
"v_cvt_pk_bf16_f32 v236, v236, v237\n" \
"v_mfma_f32_16x16x32_bf16 a[56:59], v[76:79], v[180:183], a[56:59]\n" \
"v_accvgpr_write_b32 a192, v234\n" \
"v_accvgpr_write_b32 a193, v236\n" \
"v_mfma_f32_16x16x32_bf16 a[52:55], v[72:75], v[168:171], a[52:55]\n" \
"ds_read_b64_tr_b16 v[10:11], v233 offset:2048\n" \
"v_accvgpr_read_b32 v234, a196\n" \
"v_mfma_f32_16x16x32_bf16 a[52:55], v[76:79], v[172:175], a[52:55]\n" \
"global_load_lds_dwordx4 v227, s[48:49] offset:-1024\n" \
"v_accvgpr_read_b32 v235, a197\n" \
"v_mfma_f32_16x16x32_bf16 a[48:51], v[72:75], v[160:163], a[48:51]\n" \
"s_add_u32 s48, s48, 0xfffffb00\n" \
"s_addc_u32 s49, s49, -1\n" \
"ds_read_b64_tr_b16 v[12:13], v233 offset:8192\n" \
"v_mfma_f32_16x16x32_bf16 a[48:51], v[76:79], v[164:167], a[48:51]\n" \
"v_accvgpr_read_b32 v236, a198\n" \
"v_accvgpr_read_b32 v237, a199\n" \
"v_mfma_f32_16x16x32_bf16 a[80:83], v[80:83], v[160:163], a[80:83]\n" \
"ds_read_b64_tr_b16 v[14:15], v233 offset:10240\n" \
"v_mul_f32 v234, s92, v234\n" \
"v_mfma_f32_16x16x32_bf16 a[80:83], v[84:87], v[164:167], a[80:83]\n" \
"global_load_lds_dwordx4 v224, s[46:47] offset:0\n" \
"v_mul_f32 v235, s92, v235\n" \
"v_mfma_f32_16x16x32_bf16 a[84:87], v[80:83], v[168:171], a[84:87]\n" \
"ds_read_b64_tr_b16 v[16:17], v232 offset:512\n" \
"v_mul_f32 v236, s92, v236\n" \
"v_mfma_f32_16x16x32_bf16 a[84:87], v[84:87], v[172:175], a[84:87]\n" \
"v_mul_f32 v237, s92, v237\n" \
"v_cvt_pk_bf16_f32 v234, v234, v235\n" \
"v_mfma_f32_16x16x32_bf16 a[88:91], v[80:83], v[176:179], a[88:91]\n" \
"ds_read_b64_tr_b16 v[18:19], v232 offset:2560\n" \
"v_cvt_pk_bf16_f32 v236, v236, v237\n" \
"v_mfma_f32_16x16x32_bf16 a[88:91], v[84:87], v[180:183], a[88:91]\n" \
"global_load_lds_dwordx4 v225, s[46:47] offset:1024\n" \
"v_accvgpr_write_b32 a194, v234\n" \
"v_mfma_f32_16x16x32_bf16 a[92:95], v[80:83], v[184:187], a[92:95]\n" \
"ds_read_b64_tr_b16 v[20:21], v232 offset:8704\n" \
"v_accvgpr_write_b32 a195, v236\n" \
"v_mfma_f32_16x16x32_bf16 a[92:95], v[84:87], v[188:191], a[92:95]\n" \
"v_accvgpr_read_b32 v234, a224\n" \
"v_accvgpr_read_b32 v235, a225\n" \
"v_mfma_f32_16x16x32_bf16 a[124:127], v[88:91], v[184:187], a[124:127]\n" \
"ds_read_b64_tr_b16 v[22:23], v232 offset:10752\n" \
"v_accvgpr_read_b32 v236, a226\n" \
"v_mfma_f32_16x16x32_bf16 a[124:127], v[92:95], v[188:191], a[124:127]\n" \
"global_load_lds_dwordx4 v226, s[46:47] offset:2048\n" \
"v_accvgpr_read_b32 v237, a227\n" \
"v_mfma_f32_16x16x32_bf16 a[120:123], v[88:91], v[176:179], a[120:123]\n" \
"ds_read_b64_tr_b16 v[24:25], v233 offset:512\n" \
"v_mul_f32 v234, s92, v234\n" \
"v_mfma_f32_16x16x32_bf16 a[120:123], v[92:95], v[180:183], a[120:123]\n" \
"v_mul_f32 v235, s92, v235\n" \
"v_mul_f32 v236, s92, v236\n" \
"v_mfma_f32_16x16x32_bf16 a[116:119], v[88:91], v[168:171], a[116:119]\n" \
"ds_read_b64_tr_b16 v[26:27], v233 offset:2560\n" \
"v_mul_f32 v237, s92, v237\n" \
"v_mfma_f32_16x16x32_bf16 a[116:119], v[92:95], v[172:175], a[116:119]\n" \
"global_load_lds_dwordx4 v227, s[46:47] offset:3072\n" \
"v_cvt_pk_bf16_f32 v234, v234, v235\n" \
"v_mfma_f32_16x16x32_bf16 a[112:115], v[88:91], v[160:163], a[112:115]\n" \
"s_add_u32 s46, s46, 0xfffffb00\n" \
"s_addc_u32 s47, s47, -1\n" \
"ds_read_b64_tr_b16 v[28:29], v233 offset:8704\n" \
"v_mfma_f32_16x16x32_bf16 a[112:115], v[92:95], v[164:167], a[112:115]\n" \
"v_cvt_pk_bf16_f32 v236, v236, v237\n" \
"v_accvgpr_write_b32 a224, v234\n" \
"v_mfma_f32_16x16x32_bf16 a[144:147], v[96:99], v[160:163], a[144:147]\n" \
"s_add_u32 m0, s95, 36864\n" \
"ds_read_b64_tr_b16 v[30:31], v233 offset:10752\n" \
"v_accvgpr_write_b32 a225, v236\n" \
"v_mfma_f32_16x16x32_bf16 a[144:147], v[100:103], v[164:167], a[144:147]\n" \
"global_load_lds_dwordx4 v228, s[44:45] offset:-4096\n" \
"v_accvgpr_read_b32 v234, a228\n" \
"v_mfma_f32_16x16x32_bf16 a[148:151], v[96:99], v[168:171], a[148:151]\n" \
"ds_read_b128 v[192:195], v230 offset:0\n" \
"v_accvgpr_read_b32 v235, a229\n" \
"v_mfma_f32_16x16x32_bf16 a[148:151], v[100:103], v[172:175], a[148:151]\n" \
"v_accvgpr_read_b32 v236, a230\n" \
"v_accvgpr_read_b32 v237, a231\n" \
"v_mfma_f32_16x16x32_bf16 a[152:155], v[96:99], v[176:179], a[152:155]\n" \
"ds_read_b128 v[196:199], v231 offset:0\n" \
"v_mul_f32 v234, s92, v234\n" \
"v_mfma_f32_16x16x32_bf16 a[152:155], v[100:103], v[180:183], a[152:155]\n" \
"global_load_lds_dwordx4 v229, s[44:45] offset:-3072\n" \
"v_mul_f32 v235, s92, v235\n" \
"v_mfma_f32_16x16x32_bf16 a[156:159], v[96:99], v[184:187], a[156:159]\n" \
"s_add_u32 s44, s44, 0xffb00000\n" \
"s_addc_u32 s45, s45, -1\n" \
"ds_read_b128 v[200:203], v230 offset:2048\n" \
"v_mfma_f32_16x16x32_bf16 a[156:159], v[100:103], v[188:191], a[156:159]\n" \
"v_mul_f32 v236, s92, v236\n" \
"v_mul_f32 v237, s92, v237\n" \
"v_mfma_f32_16x16x32_bf16 a[188:191], v[104:107], v[184:187], a[188:191]\n" \
"ds_read_b128 v[204:207], v231 offset:2048\n" \
"v_cvt_pk_bf16_f32 v234, v234, v235\n" \
"v_mfma_f32_16x16x32_bf16 a[188:191], v[108:111], v[188:191], a[188:191]\n" \
"global_load_lds_dwordx4 v228, s[42:43] offset:-2048\n" \
"v_cvt_pk_bf16_f32 v236, v236, v237\n" \
"v_mfma_f32_16x16x32_bf16 a[184:187], v[104:107], v[176:179], a[184:187]\n" \
"ds_read_b128 v[208:211], v230 offset:4096\n" \
"v_accvgpr_write_b32 a226, v234\n" \
"v_mfma_f32_16x16x32_bf16 a[184:187], v[108:111], v[180:183], a[184:187]\n" \
"v_accvgpr_write_b32 a227, v236\n" \
"v_accvgpr_read_b32 v234, a200\n" \
"v_mfma_f32_16x16x32_bf16 a[180:183], v[104:107], v[168:171], a[180:183]\n" \
"ds_read_b128 v[212:215], v231 offset:4096\n" \
"v_accvgpr_read_b32 v235, a201\n" \
"v_mfma_f32_16x16x32_bf16 a[180:183], v[108:111], v[172:175], a[180:183]\n" \
"global_load_lds_dwordx4 v229, s[42:43] offset:-1024\n" \
"v_accvgpr_read_b32 v236, a202\n" \
"v_mfma_f32_16x16x32_bf16 a[176:179], v[104:107], v[160:163], a[176:179]\n" \
"s_add_u32 s42, s42, 0xffb00000\n" \
"s_addc_u32 s43, s43, -1\n" \
"v_accvgpr_read_b32 v237, a203\n" \
"v_mfma_f32_16x16x32_bf16 a[176:179], v[108:111], v[164:167], a[176:179]\n" \
"v_mul_f32 v234, s92, v234\n" \
"v_mul_f32 v235, s92, v235\n" \
"v_mfma_f32_16x16x32_bf16 a[208:211], v[112:115], v[160:163], a[208:211]\n" \
"s_add_u32 m0, s95, 53248\n" \
"v_mul_f32 v236, s92, v236\n" \
"v_mul_f32 v237, s92, v237\n" \
"v_mfma_f32_16x16x32_bf16 a[208:211], v[116:119], v[164:167], a[208:211]\n" \
"global_load_lds_dwordx4 v228, s[44:45] offset:-4096\n" \
"v_cvt_pk_bf16_f32 v234, v234, v235\n" \
"v_mfma_f32_16x16x32_bf16 a[212:215], v[112:115], v[168:171], a[212:215]\n" \
"v_cvt_pk_bf16_f32 v236, v236, v237\n" \
"v_accvgpr_write_b32 a200, v234\n" \
"v_mfma_f32_16x16x32_bf16 a[212:215], v[116:119], v[172:175], a[212:215]\n" \
"v_accvgpr_write_b32 a201, v236\n" \
"v_accvgpr_read_b32 v234, a204\n" \
"v_mfma_f32_16x16x32_bf16 a[216:219], v[112:115], v[176:179], a[216:219]\n" \
"v_accvgpr_read_b32 v235, a205\n" \
"v_accvgpr_read_b32 v236, a206\n" \
"v_mfma_f32_16x16x32_bf16 a[216:219], v[116:119], v[180:183], a[216:219]\n" \
"global_load_lds_dwordx4 v229, s[44:45] offset:-3072\n" \
"v_accvgpr_read_b32 v237, a207\n" \
"v_mfma_f32_16x16x32_bf16 a[220:223], v[112:115], v[184:187], a[220:223]\n" \
"s_add_u32 s44, s44, 0x80000\n" \
"s_addc_u32 s45, s45, 0\n" \
"v_mul_f32 v234, s92, v234\n" \
"v_mfma_f32_16x16x32_bf16 a[220:223], v[116:119], v[188:191], a[220:223]\n" \
"v_mul_f32 v235, s92, v235\n" \
"v_mul_f32 v236, s92, v236\n" \
"v_mfma_f32_16x16x32_bf16 a[252:255], v[120:123], v[184:187], a[252:255]\n" \
"v_mul_f32 v237, s92, v237\n" \
"v_cvt_pk_bf16_f32 v234, v234, v235\n" \
"v_mfma_f32_16x16x32_bf16 a[252:255], v[124:127], v[188:191], a[252:255]\n" \
"global_load_lds_dwordx4 v228, s[42:43] offset:-2048\n" \
"v_cvt_pk_bf16_f32 v236, v236, v237\n" \
"v_mfma_f32_16x16x32_bf16 a[248:251], v[120:123], v[176:179], a[248:251]\n" \
"v_accvgpr_write_b32 a202, v234\n" \
"v_accvgpr_write_b32 a203, v236\n" \
"v_mfma_f32_16x16x32_bf16 a[248:251], v[124:127], v[180:183], a[248:251]\n" \
"v_accvgpr_read_b32 v234, a232\n" \
"v_accvgpr_read_b32 v235, a233\n" \
"v_mfma_f32_16x16x32_bf16 a[244:247], v[120:123], v[168:171], a[244:247]\n" \
"v_accvgpr_read_b32 v236, a234\n" \
"v_accvgpr_read_b32 v237, a235\n" \
"v_mfma_f32_16x16x32_bf16 a[244:247], v[124:127], v[172:175], a[244:247]\n" \
"global_load_lds_dwordx4 v229, s[42:43] offset:-1024\n" \
"v_mul_f32 v234, s92, v234\n" \
"v_mfma_f32_16x16x32_bf16 a[240:243], v[120:123], v[160:163], a[240:243]\n" \
"s_add_u32 s42, s42, 0x80000\n" \
"s_addc_u32 s43, s43, 0\n" \
"v_mul_f32 v235, s92, v235\n" \
"v_mfma_f32_16x16x32_bf16 a[240:243], v[124:127], v[164:167], a[240:243]\n" \
"s_waitcnt lgkmcnt(0)\n" \
"v_mul_f32 v236, s92, v236\n" \
"v_mul_f32 v237, s92, v237\n" \
"v_mfma_f32_16x16x32_bf16 v[64:67], v[0:3], v[192:195], 0\n" \
"ds_read_b128 v[216:219], v230 offset:6144\n" \
"v_cvt_pk_bf16_f32 v234, v234, v235\n" \
"v_mfma_f32_16x16x32_bf16 v[64:67], v[4:7], v[196:199], v[64:67]\n" \
"ds_read_b128 v[220:223], v231 offset:6144\n" \
"v_cvt_pk_bf16_f32 v236, v236, v237\n" \
"v_mfma_f32_16x16x32_bf16 v[68:71], v[8:11], v[192:195], 0\n" \
"s_add_u32 s90, s88, 0\n" \
"ds_read_b64_tr_b16 v[32:33], v232 offset:1024\n" \
"v_accvgpr_write_b32 a232, v234\n" \
"v_mfma_f32_16x16x32_bf16 v[68:71], v[12:15], v[196:199], v[68:71]\n" \
"buffer_store_dwordx4 a[192:195], v249, s[56:59], s90 offen offset:0 sc1\n" \
"v_accvgpr_write_b32 a233, v236\n" \
"v_accvgpr_read_b32 v234, a236\n" \
"v_accvgpr_read_b32 v235, a237\n" \
"v_accvgpr_read_b32 v236, a238\n" \
"v_mfma_f32_16x16x32_bf16 v[72:75], v[16:19], v[192:195], 0\n" \
"ds_read_b64_tr_b16 v[34:35], v232 offset:3072\n" \
"v_accvgpr_read_b32 v237, a239\n" \
"v_mfma_f32_16x16x32_bf16 v[72:75], v[20:23], v[196:199], v[72:75]\n" \
"ds_read_b64_tr_b16 v[36:37], v232 offset:9216\n" \
"v_mul_f32 v234, s92, v234\n" \
"v_mfma_f32_16x16x32_bf16 v[76:79], v[24:27], v[192:195], 0\n" \
"ds_read_b64_tr_b16 v[38:39], v232 offset:11264\n" \
"v_mul_f32 v235, s92, v235\n" \
"v_mfma_f32_16x16x32_bf16 v[76:79], v[28:31], v[196:199], v[76:79]\n" \
"buffer_store_dwordx4 a[224:227], v249, s[56:59], s90 offen offset:256 sc1\n" \
"v_mul_f32 v236, s92, v236\n" \
"v_mul_f32 v237, s92, v237\n" \
"v_cvt_pk_bf16_f32 v234, v234, v235\n" \
"v_cvt_pk_bf16_f32 v236, v236, v237\n" \
"v_mfma_f32_16x16x32_bf16 v[108:111], v[24:27], v[200:203], 0\n" \
"ds_read_b64_tr_b16 v[40:41], v233 offset:1024\n" \
"v_accvgpr_write_b32 a234, v234\n" \
"v_mfma_f32_16x16x32_bf16 v[108:111], v[28:31], v[204:207], v[108:111]\n" \
"ds_read_b64_tr_b16 v[42:43], v233 offset:3072\n" \
"v_accvgpr_write_b32 a235, v236\n" \
"v_mfma_f32_16x16x32_bf16 v[104:107], v[16:19], v[200:203], 0\n" \
"ds_read_b64_tr_b16 v[44:45], v233 offset:9216\n" \
"v_accvgpr_read_b32 v234, a208\n" \
"v_mfma_f32_16x16x32_bf16 v[104:107], v[20:23], v[204:207], v[104:107]\n" \
"v_accvgpr_read_b32 v235, a209\n" \
"v_accvgpr_read_b32 v236, a210\n" \
"v_mfma_f32_16x16x32_bf16 v[100:103], v[8:11], v[200:203], 0\n" \
"ds_read_b64_tr_b16 v[46:47], v233 offset:11264\n" \
"v_accvgpr_read_b32 v237, a211\n" \
"v_mfma_f32_16x16x32_bf16 v[100:103], v[12:15], v[204:207], v[100:103]\n" \
"ds_read_b64_tr_b16 v[48:49], v232 offset:1536\n" \
"v_cvt_pk_bf16_f32 v234, v234, v235\n" \
"v_mfma_f32_16x16x32_bf16 v[96:99], v[0:3], v[200:203], 0\n" \
"ds_read_b64_tr_b16 v[50:51], v232 offset:3584\n" \
"v_cvt_pk_bf16_f32 v236, v236, v237\n" \
"v_mfma_f32_16x16x32_bf16 v[96:99], v[4:7], v[204:207], v[96:99]\n" \
"v_accvgpr_write_b32 a208, v234\n" \
"v_accvgpr_write_b32 a209, v236\n" \
"v_mfma_f32_16x16x32_bf16 v[128:131], v[0:3], v[208:211], 0\n" \
"ds_read_b64_tr_b16 v[52:53], v232 offset:9728\n" \
"v_accvgpr_read_b32 v234, a240\n" \
"v_mfma_f32_16x16x32_bf16 v[128:131], v[4:7], v[212:215], v[128:131]\n" \
"ds_read_b64_tr_b16 v[54:55], v232 offset:11776\n" \
"v_accvgpr_read_b32 v235, a241\n" \
"v_mfma_f32_16x16x32_bf16 v[132:135], v[8:11], v[208:211], 0\n" \
"ds_read_b64_tr_b16 v[56:57], v233 offset:1536\n" \
"v_accvgpr_read_b32 v236, a242\n" \
"v_mfma_f32_16x16x32_bf16 v[132:135], v[12:15], v[212:215], v[132:135]\n" \
"v_accvgpr_read_b32 v237, a243\n" \
"v_cvt_pk_bf16_f32 v234, v234, v235\n" \
"Lpg2226_%=:\n" \
"Lst0x3_%=:\n" \
"s_cbranch_execz Lpg4398_%=\n" \
"v_mfma_f32_16x16x32_bf16 v[136:139], v[16:19], v[208:211], 0\n" \
"ds_read_b64_tr_b16 v[58:59], v233 offset:3584\n" \
"v_cvt_pk_bf16_f32 v236, v236, v237\n" \
"v_mfma_f32_16x16x32_bf16 v[136:139], v[20:23], v[212:215], v[136:139]\n" \
"ds_read_b64_tr_b16 v[60:61], v233 offset:9728\n" \
"v_accvgpr_write_b32 a210, v234\n" \
"v_mfma_f32_16x16x32_bf16 v[140:143], v[24:27], v[208:211], 0\n" \
"ds_read_b64_tr_b16 v[62:63], v233 offset:11776\n" \
"v_accvgpr_write_b32 a211, v236\n" \
"v_mfma_f32_16x16x32_bf16 v[140:143], v[28:31], v[212:215], v[140:143]\n" \
"s_waitcnt lgkmcnt(3)\n" \
"v_accvgpr_read_b32 v234, a212\n" \
"v_accvgpr_read_b32 v235, a213\n" \
"v_mfma_f32_16x16x32_bf16 v[172:175], v[24:27], v[216:219], 0\n" \
"v_add_u32 v230, 0xffff0000, v230\n" \
"v_add_u32 v231, 0xffff0000, v231\n" \
"v_mfma_f32_16x16x32_bf16 v[172:175], v[28:31], v[220:223], v[172:175]\n" \
"v_add_u32 v232, 0x4000, v232\n" \
"v_add_u32 v233, 0x4000, v233\n" \
"v_mfma_f32_16x16x32_bf16 v[168:171], v[16:19], v[216:219], 0\n" \
"v_accvgpr_read_b32 v236, a214\n" \
"v_accvgpr_read_b32 v237, a215\n" \
"v_mfma_f32_16x16x32_bf16 v[168:171], v[20:23], v[220:223], v[168:171]\n" \
"v_cvt_pk_bf16_f32 v234, v234, v235\n" \
"v_cvt_pk_bf16_f32 v236, v236, v237\n" \
"v_mfma_f32_16x16x32_bf16 v[164:167], v[8:11], v[216:219], 0\n" \
"v_accvgpr_write_b32 a212, v234\n" \
"v_accvgpr_write_b32 a213, v236\n" \
"v_mfma_f32_16x16x32_bf16 v[164:167], v[12:15], v[220:223], v[164:167]\n" \
"s_waitcnt vmcnt(18) lgkmcnt(0)\n" \
"v_accvgpr_read_b32 v234, a244\n" \
"v_accvgpr_read_b32 v235, a245\n" \
"v_mfma_f32_16x16x32_bf16 v[160:163], v[0:3], v[216:219], 0\n" \
"v_accvgpr_read_b32 v236, a246\n" \
"v_accvgpr_read_b32 v237, a247\n" \
"v_mfma_f32_16x16x32_bf16 v[160:163], v[4:7], v[220:223], v[160:163]\n" \
"s_barrier\n" \
"v_mfma_f32_16x16x32_bf16 v[80:83], v[32:35], v[192:195], 0\n" \
"ds_read_b64_tr_b16 v[0:1], v232 offset:0\n" \
"v_cvt_pk_bf16_f32 v234, v234, v235\n" \
"v_mfma_f32_16x16x32_bf16 v[80:83], v[36:39], v[196:199], v[80:83]\n" \
"ds_read_b64_tr_b16 v[2:3], v232 offset:2048\n" \
"v_cvt_pk_bf16_f32 v236, v236, v237\n" \
"v_mfma_f32_16x16x32_bf16 v[84:87], v[40:43], v[192:195], 0\n" \
"s_add_u32 m0, s87, 135168\n" \
"ds_read_b64_tr_b16 v[4:5], v232 offset:8192\n" \
"v_accvgpr_write_b32 a214, v234\n" \
"v_mfma_f32_16x16x32_bf16 v[84:87], v[44:47], v[196:199], v[84:87]\n" \
"global_load_lds_dwordx4 v224, s[48:49] offset:-4096\n" \
"v_accvgpr_write_b32 a215, v236\n" \
"v_mfma_f32_16x16x32_bf16 v[88:91], v[48:51], v[192:195], 0\n" \
"ds_read_b64_tr_b16 v[6:7], v232 offset:10240\n" \
"v_accvgpr_read_b32 v234, a216\n" \
"v_mfma_f32_16x16x32_bf16 v[88:91], v[52:55], v[196:199], v[88:91]\n" \
"ds_read_b64_tr_b16 v[8:9], v233 offset:0\n" \
"v_accvgpr_read_b32 v235, a217\n" \
"v_mfma_f32_16x16x32_bf16 v[92:95], v[56:59], v[192:195], 0\n" \
"ds_read_b64_tr_b16 v[10:11], v233 offset:2048\n" \
"v_accvgpr_read_b32 v236, a218\n" \
"v_mfma_f32_16x16x32_bf16 v[92:95], v[60:63], v[196:199], v[92:95]\n" \
"global_load_lds_dwordx4 v225, s[48:49] offset:-3072\n" \
"v_accvgpr_read_b32 v237, a219\n" \
"v_mfma_f32_16x16x32_bf16 v[124:127], v[56:59], v[200:203], 0\n" \
"ds_read_b128 v[192:195], v230 offset:0\n" \
"v_cvt_pk_bf16_f32 v234, v234, v235\n" \
"v_mfma_f32_16x16x32_bf16 v[124:127], v[60:63], v[204:207], v[124:127]\n" \
"ds_read_b128 v[196:199], v231 offset:0\n" \
"v_cvt_pk_bf16_f32 v236, v236, v237\n" \
"v_mfma_f32_16x16x32_bf16 v[120:123], v[48:51], v[200:203], 0\n" \
"ds_read_b64_tr_b16 v[12:13], v233 offset:8192\n" \
"v_accvgpr_write_b32 a216, v234\n" \
"v_mfma_f32_16x16x32_bf16 v[120:123], v[52:55], v[204:207], v[120:123]\n" \
"global_load_lds_dwordx4 v226, s[48:49] offset:-2048\n" \
"v_accvgpr_write_b32 a217, v236\n" \
"v_mfma_f32_16x16x32_bf16 v[116:119], v[40:43], v[200:203], 0\n" \
"ds_read_b64_tr_b16 v[14:15], v233 offset:10240\n" \
"v_accvgpr_read_b32 v234, a248\n" \
"v_mfma_f32_16x16x32_bf16 v[116:119], v[44:47], v[204:207], v[116:119]\n" \
"ds_read_b64_tr_b16 v[16:17], v232 offset:512\n" \
"v_accvgpr_read_b32 v235, a249\n" \
"v_mfma_f32_16x16x32_bf16 v[112:115], v[32:35], v[200:203], 0\n" \
"ds_read_b64_tr_b16 v[18:19], v232 offset:2560\n" \
"v_accvgpr_read_b32 v236, a250\n" \
"v_mfma_f32_16x16x32_bf16 v[112:115], v[36:39], v[204:207], v[112:115]\n" \
"global_load_lds_dwordx4 v227, s[48:49] offset:-1024\n" \
"v_accvgpr_read_b32 v237, a251\n" \
"v_mfma_f32_16x16x32_bf16 v[144:147], v[32:35], v[208:211], 0\n" \
"s_add_u32 s48, s48, 0x80\n" \
"s_addc_u32 s49, s49, 0\n" \
"ds_read_b128 v[200:203], v230 offset:2048\n" \
"v_mfma_f32_16x16x32_bf16 v[144:147], v[36:39], v[212:215], v[144:147]\n" \
"ds_read_b128 v[204:207], v231 offset:2048\n" \
"v_cvt_pk_bf16_f32 v234, v234, v235\n" \
"v_mfma_f32_16x16x32_bf16 v[148:151], v[40:43], v[208:211], 0\n" \
"ds_read_b64_tr_b16 v[20:21], v232 offset:8704\n" \
"v_cvt_pk_bf16_f32 v236, v236, v237\n" \
"v_mfma_f32_16x16x32_bf16 v[148:151], v[44:47], v[212:215], v[148:151]\n" \
"global_load_lds_dwordx4 v224, s[46:47] offset:0\n" \
"v_accvgpr_write_b32 a218, v234\n" \
"v_mfma_f32_16x16x32_bf16 v[152:155], v[48:51], v[208:211], 0\n" \
"ds_read_b64_tr_b16 v[22:23], v232 offset:10752\n" \
"v_accvgpr_write_b32 a219, v236\n" \
"v_mfma_f32_16x16x32_bf16 v[152:155], v[52:55], v[212:215], v[152:155]\n" \
"ds_read_b64_tr_b16 v[24:25], v233 offset:512\n" \
"v_accvgpr_read_b32 v234, a220\n" \
"v_mfma_f32_16x16x32_bf16 v[156:159], v[56:59], v[208:211], 0\n" \
"ds_read_b64_tr_b16 v[26:27], v233 offset:2560\n" \
"v_accvgpr_read_b32 v235, a221\n" \
"v_mfma_f32_16x16x32_bf16 v[156:159], v[60:63], v[212:215], v[156:159]\n" \
"global_load_lds_dwordx4 v225, s[46:47] offset:1024\n" \
"v_accvgpr_read_b32 v236, a222\n" \
"v_mfma_f32_16x16x32_bf16 v[188:191], v[56:59], v[216:219], 0\n" \
"ds_read_b128 v[208:211], v230 offset:4096\n" \
"v_accvgpr_read_b32 v237, a223\n" \
"v_mfma_f32_16x16x32_bf16 v[188:191], v[60:63], v[220:223], v[188:191]\n" \
"ds_read_b128 v[212:215], v231 offset:4096\n" \
"v_cvt_pk_bf16_f32 v234, v234, v235\n" \
"v_mfma_f32_16x16x32_bf16 v[184:187], v[48:51], v[216:219], 0\n" \
"ds_read_b64_tr_b16 v[28:29], v233 offset:8704\n" \
"v_cvt_pk_bf16_f32 v236, v236, v237\n" \
"v_mfma_f32_16x16x32_bf16 v[184:187], v[52:55], v[220:223], v[184:187]\n" \
"global_load_lds_dwordx4 v226, s[46:47] offset:2048\n" \
"v_accvgpr_write_b32 a220, v234\n" \
"v_mfma_f32_16x16x32_bf16 v[180:183], v[40:43], v[216:219], 0\n" \
"ds_read_b64_tr_b16 v[30:31], v233 offset:10752\n" \
"v_accvgpr_write_b32 a221, v236\n" \
"v_mfma_f32_16x16x32_bf16 v[180:183], v[44:47], v[220:223], v[180:183]\n" \
"v_accvgpr_read_b32 v234, a252\n" \
"v_accvgpr_read_b32 v235, a253\n" \
"v_mfma_f32_16x16x32_bf16 v[176:179], v[32:35], v[216:219], 0\n" \
"v_accvgpr_read_b32 v236, a254\n" \
"v_accvgpr_read_b32 v237, a255\n" \
"v_mfma_f32_16x16x32_bf16 v[176:179], v[36:39], v[220:223], v[176:179]\n" \
"s_waitcnt lgkmcnt(1)\n" \
"global_load_lds_dwordx4 v227, s[46:47] offset:3072\n" \
"v_cvt_pk_bf16_f32 v234, v234, v235\n" \
"v_mfma_f32_16x16x32_bf16 v[64:67], v[0:3], v[192:195], v[64:67]\n" \
"s_add_u32 s46, s46, 0x80\n" \
"s_addc_u32 s47, s47, 0\n" \
"ds_read_b128 v[216:219], v230 offset:6144\n" \
"v_mfma_f32_16x16x32_bf16 v[64:67], v[4:7], v[196:199], v[64:67]\n" \
"ds_read_b128 v[220:223], v231 offset:6144\n" \
"v_cvt_pk_bf16_f32 v236, v236, v237\n" \
"v_mfma_f32_16x16x32_bf16 v[68:71], v[8:11], v[192:195], v[68:71]\n" \
"ds_read_b64_tr_b16 v[32:33], v232 offset:1024\n" \
"v_accvgpr_write_b32 a222, v234\n" \
"v_mfma_f32_16x16x32_bf16 v[68:71], v[12:15], v[196:199], v[68:71]\n" \
"buffer_store_dwordx4 a[200:203], v249, s[56:59], s90 offen offset:2048 sc1\n" \
"v_accvgpr_write_b32 a223, v236\n" \
"v_accvgpr_read_b32 v234, a128\n" \
"v_accvgpr_read_b32 v235, a129\n" \
"v_accvgpr_read_b32 v236, a130\n" \
"v_mfma_f32_16x16x32_bf16 v[72:75], v[16:19], v[192:195], v[72:75]\n" \
"ds_read_b64_tr_b16 v[34:35], v232 offset:3072\n" \
"v_accvgpr_read_b32 v237, a131\n" \
"v_mfma_f32_16x16x32_bf16 v[72:75], v[20:23], v[196:199], v[72:75]\n" \
"s_waitcnt lgkmcnt(3)\n" \
"ds_read_b64_tr_b16 v[36:37], v232 offset:9216\n" \
"v_mul_f32 v234, s92, v234\n" \
"v_mfma_f32_16x16x32_bf16 v[76:79], v[24:27], v[192:195], v[76:79]\n" \
"ds_read_b64_tr_b16 v[38:39], v232 offset:11264\n" \
"v_mul_f32 v235, s92, v235\n" \
"v_mfma_f32_16x16x32_bf16 v[76:79], v[28:31], v[196:199], v[76:79]\n" \
"v_mul_f32 v236, s92, v236\n" \
"v_mul_f32 v237, s92, v237\n" \
"v_mfma_f32_16x16x32_bf16 v[108:111], v[24:27], v[200:203], v[108:111]\n" \
"ds_read_b64_tr_b16 v[40:41], v233 offset:1024\n" \
"v_cvt_pk_bf16_f32 v234, v234, v235\n" \
"v_mfma_f32_16x16x32_bf16 v[108:111], v[28:31], v[204:207], v[108:111]\n" \
"ds_read_b64_tr_b16 v[42:43], v233 offset:3072\n" \
"v_cvt_pk_bf16_f32 v236, v236, v237\n" \
"v_mfma_f32_16x16x32_bf16 v[104:107], v[16:19], v[200:203], v[104:107]\n" \
"ds_read_b64_tr_b16 v[44:45], v233 offset:9216\n" \
"v_accvgpr_write_b32 a128, v234\n" \
"v_mfma_f32_16x16x32_bf16 v[104:107], v[20:23], v[204:207], v[104:107]\n" \
"v_accvgpr_write_b32 a129, v236\n" \
"v_accvgpr_read_b32 v234, a132\n" \
"v_mfma_f32_16x16x32_bf16 v[100:103], v[8:11], v[200:203], v[100:103]\n" \
"ds_read_b64_tr_b16 v[46:47], v233 offset:11264\n" \
"v_accvgpr_read_b32 v235, a133\n" \
"v_mfma_f32_16x16x32_bf16 v[100:103], v[12:15], v[204:207], v[100:103]\n" \
"ds_read_b64_tr_b16 v[48:49], v232 offset:1536\n" \
"v_accvgpr_read_b32 v236, a134\n" \
"v_mfma_f32_16x16x32_bf16 v[96:99], v[0:3], v[200:203], v[96:99]\n" \
"s_add_u32 m0, s95, 4096\n" \
"ds_read_b64_tr_b16 v[50:51], v232 offset:3584\n" \
"v_accvgpr_read_b32 v237, a135\n" \
"v_mfma_f32_16x16x32_bf16 v[96:99], v[4:7], v[204:207], v[96:99]\n" \
"global_load_lds_dwordx4 v228, s[44:45] offset:-4096\n" \
"v_mul_f32 v234, s92, v234\n" \
"v_mfma_f32_16x16x32_bf16 v[128:131], v[0:3], v[208:211], v[128:131]\n" \
"ds_read_b64_tr_b16 v[52:53], v232 offset:9728\n" \
"v_mul_f32 v235, s92, v235\n" \
"v_mfma_f32_16x16x32_bf16 v[128:131], v[4:7], v[212:215], v[128:131]\n" \
"ds_read_b64_tr_b16 v[54:55], v232 offset:11776\n" \
"v_mul_f32 v236, s92, v236\n" \
"v_mfma_f32_16x16x32_bf16 v[132:135], v[8:11], v[208:211], v[132:135]\n" \
"ds_read_b64_tr_b16 v[56:57], v233 offset:1536\n" \
"v_mul_f32 v237, s92, v237\n" \
"v_mfma_f32_16x16x32_bf16 v[132:135], v[12:15], v[212:215], v[132:135]\n" \
"global_load_lds_dwordx4 v229, s[44:45] offset:-3072\n" \
"v_cvt_pk_bf16_f32 v234, v234, v235\n" \
"v_mfma_f32_16x16x32_bf16 v[136:139], v[16:19], v[208:211], v[136:139]\n" \
"s_add_u32 s44, s44, 0x100000\n" \
"s_addc_u32 s45, s45, 0\n" \
"ds_read_b64_tr_b16 v[58:59], v233 offset:3584\n" \
"v_mfma_f32_16x16x32_bf16 v[136:139], v[20:23], v[212:215], v[136:139]\n" \
"ds_read_b64_tr_b16 v[60:61], v233 offset:9728\n" \
"v_cvt_pk_bf16_f32 v236, v236, v237\n" \
"v_mfma_f32_16x16x32_bf16 v[140:143], v[24:27], v[208:211], v[140:143]\n" \
"ds_read_b64_tr_b16 v[62:63], v233 offset:11776\n" \
"v_accvgpr_write_b32 a130, v234\n" \
"v_mfma_f32_16x16x32_bf16 v[140:143], v[28:31], v[212:215], v[140:143]\n" \
"s_waitcnt lgkmcnt(3)\n" \
"global_load_lds_dwordx4 v228, s[42:43] offset:-2048\n" \
"v_accvgpr_write_b32 a131, v236\n" \
"v_mfma_f32_16x16x32_bf16 v[172:175], v[24:27], v[216:219], v[172:175]\n" \
"v_add_u32 v230, 0x8000, v230\n" \
"v_add_u32 v231, 0x8000, v231\n" \
"v_mfma_f32_16x16x32_bf16 v[172:175], v[28:31], v[220:223], v[172:175]\n" \
"v_add_u32 v232, 0x4000, v232\n" \
"v_add_u32 v233, 0x4000, v233\n" \
"v_mfma_f32_16x16x32_bf16 v[168:171], v[16:19], v[216:219], v[168:171]\n" \
"v_accvgpr_read_b32 v234, a160\n" \
"v_accvgpr_read_b32 v235, a161\n" \
"v_mfma_f32_16x16x32_bf16 v[168:171], v[20:23], v[220:223], v[168:171]\n" \
"global_load_lds_dwordx4 v229, s[42:43] offset:-1024\n" \
"v_accvgpr_read_b32 v236, a162\n" \
"v_mfma_f32_16x16x32_bf16 v[164:167], v[8:11], v[216:219], v[164:167]\n" \
"s_add_u32 s42, s42, 0x100000\n" \
"s_addc_u32 s43, s43, 0\n" \
"v_accvgpr_read_b32 v237, a163\n" \
"v_mfma_f32_16x16x32_bf16 v[164:167], v[12:15], v[220:223], v[164:167]\n" \
"s_waitcnt vmcnt(19) lgkmcnt(0)\n" \
"v_mul_f32 v234, s92, v234\n" \
"v_mul_f32 v235, s92, v235\n" \
"v_mfma_f32_16x16x32_bf16 v[160:163], v[0:3], v[216:219], v[160:163]\n" \
"v_mul_f32 v236, s92, v236\n" \
"v_mul_f32 v237, s92, v237\n" \
"v_mfma_f32_16x16x32_bf16 v[160:163], v[4:7], v[220:223], v[160:163]\n" \
"s_barrier\n" \
"v_mfma_f32_16x16x32_bf16 v[80:83], v[32:35], v[192:195], v[80:83]\n" \
"ds_read_b64_tr_b16 v[0:1], v232 offset:0\n" \
"v_cvt_pk_bf16_f32 v234, v234, v235\n" \
"v_mfma_f32_16x16x32_bf16 v[80:83], v[36:39], v[196:199], v[80:83]\n" \
"ds_read_b64_tr_b16 v[2:3], v232 offset:2048\n" \
"v_cvt_pk_bf16_f32 v236, v236, v237\n" \
"v_mfma_f32_16x16x32_bf16 v[84:87], v[40:43], v[192:195], v[84:87]\n" \
"s_add_u32 m0, s87, 69632\n" \
"ds_read_b64_tr_b16 v[4:5], v232 offset:8192\n" \
"v_accvgpr_write_b32 a160, v234\n" \
"v_mfma_f32_16x16x32_bf16 v[84:87], v[44:47], v[196:199], v[84:87]\n" \
"global_load_lds_dwordx4 v224, s[48:49] offset:-4096\n" \
"v_accvgpr_write_b32 a161, v236\n" \
"v_mfma_f32_16x16x32_bf16 v[88:91], v[48:51], v[192:195], v[88:91]\n" \
"ds_read_b64_tr_b16 v[6:7], v232 offset:10240\n" \
"v_accvgpr_read_b32 v234, a164\n" \
"v_mfma_f32_16x16x32_bf16 v[88:91], v[52:55], v[196:199], v[88:91]\n" \
"ds_read_b64_tr_b16 v[8:9], v233 offset:0\n" \
"v_accvgpr_read_b32 v235, a165\n" \
"v_mfma_f32_16x16x32_bf16 v[92:95], v[56:59], v[192:195], v[92:95]\n" \
"ds_read_b64_tr_b16 v[10:11], v233 offset:2048\n" \
"v_accvgpr_read_b32 v236, a166\n" \
"v_mfma_f32_16x16x32_bf16 v[92:95], v[60:63], v[196:199], v[92:95]\n" \
"global_load_lds_dwordx4 v225, s[48:49] offset:-3072\n" \
"v_accvgpr_read_b32 v237, a167\n" \
"v_mfma_f32_16x16x32_bf16 v[124:127], v[56:59], v[200:203], v[124:127]\n" \
"ds_read_b128 v[192:195], v230 offset:0\n" \
"v_mul_f32 v234, s92, v234\n" \
"v_mfma_f32_16x16x32_bf16 v[124:127], v[60:63], v[204:207], v[124:127]\n" \
"ds_read_b128 v[196:199], v231 offset:0\n" \
"v_mul_f32 v235, s92, v235\n" \
"v_mfma_f32_16x16x32_bf16 v[120:123], v[48:51], v[200:203], v[120:123]\n" \
"ds_read_b64_tr_b16 v[12:13], v233 offset:8192\n" \
"v_mul_f32 v236, s92, v236\n" \
"v_mfma_f32_16x16x32_bf16 v[120:123], v[52:55], v[204:207], v[120:123]\n" \
"global_load_lds_dwordx4 v226, s[48:49] offset:-2048\n" \
"v_mul_f32 v237, s92, v237\n" \
"v_mfma_f32_16x16x32_bf16 v[116:119], v[40:43], v[200:203], v[116:119]\n" \
"ds_read_b64_tr_b16 v[14:15], v233 offset:10240\n" \
"v_cvt_pk_bf16_f32 v234, v234, v235\n" \
"v_mfma_f32_16x16x32_bf16 v[116:119], v[44:47], v[204:207], v[116:119]\n" \
"ds_read_b64_tr_b16 v[16:17], v232 offset:512\n" \
"v_cvt_pk_bf16_f32 v236, v236, v237\n" \
"v_mfma_f32_16x16x32_bf16 v[112:115], v[32:35], v[200:203], v[112:115]\n" \
"ds_read_b64_tr_b16 v[18:19], v232 offset:2560\n" \
"v_accvgpr_write_b32 a162, v234\n" \
"v_mfma_f32_16x16x32_bf16 v[112:115], v[36:39], v[204:207], v[112:115]\n" \
"global_load_lds_dwordx4 v227, s[48:49] offset:-1024\n" \
"v_accvgpr_write_b32 a163, v236\n" \
"v_mfma_f32_16x16x32_bf16 v[144:147], v[32:35], v[208:211], v[144:147]\n" \
"s_add_u32 s48, s48, 0x100\n" \
"s_addc_u32 s49, s49, 0\n" \
"ds_read_b128 v[200:203], v230 offset:2048\n" \
"v_mfma_f32_16x16x32_bf16 v[144:147], v[36:39], v[212:215], v[144:147]\n" \
"ds_read_b128 v[204:207], v231 offset:2048\n" \
"v_accvgpr_read_b32 v234, a136\n" \
"v_mfma_f32_16x16x32_bf16 v[148:151], v[40:43], v[208:211], v[148:151]\n" \
"ds_read_b64_tr_b16 v[20:21], v232 offset:8704\n" \
"v_accvgpr_read_b32 v235, a137\n" \
"v_mfma_f32_16x16x32_bf16 v[148:151], v[44:47], v[212:215], v[148:151]\n" \
"global_load_lds_dwordx4 v224, s[46:47] offset:0\n" \
"v_accvgpr_read_b32 v236, a138\n" \
"v_mfma_f32_16x16x32_bf16 v[152:155], v[48:51], v[208:211], v[152:155]\n" \
"ds_read_b64_tr_b16 v[22:23], v232 offset:10752\n" \
"v_accvgpr_read_b32 v237, a139\n" \
"v_mfma_f32_16x16x32_bf16 v[152:155], v[52:55], v[212:215], v[152:155]\n" \
"ds_read_b64_tr_b16 v[24:25], v233 offset:512\n" \
"v_mul_f32 v234, s92, v234\n" \
"v_mfma_f32_16x16x32_bf16 v[156:159], v[56:59], v[208:211], v[156:159]\n" \
"ds_read_b64_tr_b16 v[26:27], v233 offset:2560\n" \
"v_mul_f32 v235, s92, v235\n" \
"v_mfma_f32_16x16x32_bf16 v[156:159], v[60:63], v[212:215], v[156:159]\n" \
"global_load_lds_dwordx4 v225, s[46:47] offset:1024\n" \
"v_mul_f32 v236, s92, v236\n" \
"v_mfma_f32_16x16x32_bf16 v[188:191], v[56:59], v[216:219], v[188:191]\n" \
"ds_read_b128 v[208:211], v230 offset:4096\n" \
"v_mul_f32 v237, s92, v237\n" \
"v_mfma_f32_16x16x32_bf16 v[188:191], v[60:63], v[220:223], v[188:191]\n" \
"ds_read_b128 v[212:215], v231 offset:4096\n" \
"v_cvt_pk_bf16_f32 v234, v234, v235\n" \
"v_mfma_f32_16x16x32_bf16 v[184:187], v[48:51], v[216:219], v[184:187]\n" \
"ds_read_b64_tr_b16 v[28:29], v233 offset:8704\n" \
"v_cvt_pk_bf16_f32 v236, v236, v237\n" \
"v_mfma_f32_16x16x32_bf16 v[184:187], v[52:55], v[220:223], v[184:187]\n" \
"global_load_lds_dwordx4 v226, s[46:47] offset:2048\n" \
"v_accvgpr_write_b32 a136, v234\n" \
"v_mfma_f32_16x16x32_bf16 v[180:183], v[40:43], v[216:219], v[180:183]\n" \
"ds_read_b64_tr_b16 v[30:31], v233 offset:10752\n" \
"v_accvgpr_write_b32 a137, v236\n" \
"v_mfma_f32_16x16x32_bf16 v[180:183], v[44:47], v[220:223], v[180:183]\n" \
"v_accvgpr_read_b32 v234, a140\n" \
"v_accvgpr_read_b32 v235, a141\n" \
"v_mfma_f32_16x16x32_bf16 v[176:179], v[32:35], v[216:219], v[176:179]\n" \
"v_accvgpr_read_b32 v236, a142\n" \
"v_accvgpr_read_b32 v237, a143\n" \
"v_mfma_f32_16x16x32_bf16 v[176:179], v[36:39], v[220:223], v[176:179]\n" \
"s_waitcnt lgkmcnt(1)\n" \
"global_load_lds_dwordx4 v227, s[46:47] offset:3072\n" \
"v_mul_f32 v234, s92, v234\n" \
"v_mfma_f32_16x16x32_bf16 v[64:67], v[0:3], v[192:195], v[64:67]\n" \
"s_add_u32 s46, s46, 0x100\n" \
"s_addc_u32 s47, s47, 0\n" \
"ds_read_b128 v[216:219], v230 offset:6144\n" \
"v_mfma_f32_16x16x32_bf16 v[64:67], v[4:7], v[196:199], v[64:67]\n" \
"ds_read_b128 v[220:223], v231 offset:6144\n" \
"v_mul_f32 v235, s92, v235\n" \
"v_mfma_f32_16x16x32_bf16 v[68:71], v[8:11], v[192:195], v[68:71]\n" \
"ds_read_b64_tr_b16 v[32:33], v232 offset:1024\n" \
"v_mul_f32 v236, s92, v236\n" \
"v_mfma_f32_16x16x32_bf16 v[68:71], v[12:15], v[196:199], v[68:71]\n" \
"buffer_store_dwordx4 a[232:235], v249, s[56:59], s90 offen offset:2304 sc1\n" \
"v_mul_f32 v237, s92, v237\n" \
"v_cvt_pk_bf16_f32 v234, v234, v235\n" \
"v_cvt_pk_bf16_f32 v236, v236, v237\n" \
"v_accvgpr_write_b32 a138, v234\n" \
"v_mfma_f32_16x16x32_bf16 v[72:75], v[16:19], v[192:195], v[72:75]\n" \
"ds_read_b64_tr_b16 v[34:35], v232 offset:3072\n" \
"v_accvgpr_write_b32 a139, v236\n" \
"v_mfma_f32_16x16x32_bf16 v[72:75], v[20:23], v[196:199], v[72:75]\n" \
"s_waitcnt lgkmcnt(3)\n" \
"ds_read_b64_tr_b16 v[36:37], v232 offset:9216\n" \
"v_accvgpr_read_b32 v234, a168\n" \
"v_mfma_f32_16x16x32_bf16 v[76:79], v[24:27], v[192:195], v[76:79]\n" \
"ds_read_b64_tr_b16 v[38:39], v232 offset:11264\n" \
"v_accvgpr_read_b32 v235, a169\n" \
"v_mfma_f32_16x16x32_bf16 v[76:79], v[28:31], v[196:199], v[76:79]\n" \
"v_accvgpr_read_b32 v236, a170\n" \
"v_accvgpr_read_b32 v237, a171\n" \
"v_mfma_f32_16x16x32_bf16 v[108:111], v[24:27], v[200:203], v[108:111]\n" \
"ds_read_b64_tr_b16 v[40:41], v233 offset:1024\n" \
"v_mul_f32 v234, s92, v234\n" \
"v_mfma_f32_16x16x32_bf16 v[108:111], v[28:31], v[204:207], v[108:111]\n" \
"ds_read_b64_tr_b16 v[42:43], v233 offset:3072\n" \
"v_mul_f32 v235, s92, v235\n" \
"v_mfma_f32_16x16x32_bf16 v[104:107], v[16:19], v[200:203], v[104:107]\n" \
"ds_read_b64_tr_b16 v[44:45], v233 offset:9216\n" \
"v_mul_f32 v236, s92, v236\n" \
"v_mfma_f32_16x16x32_bf16 v[104:107], v[20:23], v[204:207], v[104:107]\n" \
"v_mul_f32 v237, s92, v237\n" \
"v_cvt_pk_bf16_f32 v234, v234, v235\n" \
"v_mfma_f32_16x16x32_bf16 v[100:103], v[8:11], v[200:203], v[100:103]\n" \
"ds_read_b64_tr_b16 v[46:47], v233 offset:11264\n" \
"v_cvt_pk_bf16_f32 v236, v236, v237\n" \
"v_mfma_f32_16x16x32_bf16 v[100:103], v[12:15], v[204:207], v[100:103]\n" \
"ds_read_b64_tr_b16 v[48:49], v232 offset:1536\n" \
"v_accvgpr_write_b32 a168, v234\n" \
"v_mfma_f32_16x16x32_bf16 v[96:99], v[0:3], v[200:203], v[96:99]\n" \
"s_add_u32 m0, s95, 20480\n" \
"ds_read_b64_tr_b16 v[50:51], v232 offset:3584\n" \
"v_accvgpr_write_b32 a169, v236\n" \
"v_mfma_f32_16x16x32_bf16 v[96:99], v[4:7], v[204:207], v[96:99]\n" \
"global_load_lds_dwordx4 v228, s[44:45] offset:-4096\n" \
"v_accvgpr_read_b32 v234, a172\n" \
"v_mfma_f32_16x16x32_bf16 v[128:131], v[0:3], v[208:211], v[128:131]\n" \
"ds_read_b64_tr_b16 v[52:53], v232 offset:9728\n" \
"v_accvgpr_read_b32 v235, a173\n" \
"v_mfma_f32_16x16x32_bf16 v[128:131], v[4:7], v[212:215], v[128:131]\n" \
"ds_read_b64_tr_b16 v[54:55], v232 offset:11776\n" \
"v_accvgpr_read_b32 v236, a174\n" \
"v_mfma_f32_16x16x32_bf16 v[132:135], v[8:11], v[208:211], v[132:135]\n" \
"ds_read_b64_tr_b16 v[56:57], v233 offset:1536\n" \
"v_accvgpr_read_b32 v237, a175\n" \
"v_mfma_f32_16x16x32_bf16 v[132:135], v[12:15], v[212:215], v[132:135]\n" \
"global_load_lds_dwordx4 v229, s[44:45] offset:-3072\n" \
"v_mul_f32 v234, s92, v234\n" \
"v_mfma_f32_16x16x32_bf16 v[136:139], v[16:19], v[208:211], v[136:139]\n" \
"s_add_u32 s44, s44, 0x80000\n" \
"s_addc_u32 s45, s45, 0\n" \
"ds_read_b64_tr_b16 v[58:59], v233 offset:3584\n" \
"v_mfma_f32_16x16x32_bf16 v[136:139], v[20:23], v[212:215], v[136:139]\n" \
"ds_read_b64_tr_b16 v[60:61], v233 offset:9728\n" \
"v_mul_f32 v235, s92, v235\n" \
"v_mfma_f32_16x16x32_bf16 v[140:143], v[24:27], v[208:211], v[140:143]\n" \
"ds_read_b64_tr_b16 v[62:63], v233 offset:11776\n" \
"v_mul_f32 v236, s92, v236\n" \
"v_mfma_f32_16x16x32_bf16 v[140:143], v[28:31], v[212:215], v[140:143]\n" \
"s_waitcnt lgkmcnt(3)\n" \
"global_load_lds_dwordx4 v228, s[42:43] offset:-2048\n" \
"v_mul_f32 v237, s92, v237\n" \
"v_mfma_f32_16x16x32_bf16 v[172:175], v[24:27], v[216:219], v[172:175]\n" \
"v_add_u32 v230, 0x8000, v230\n" \
"v_add_u32 v231, 0x8000, v231\n" \
"v_mfma_f32_16x16x32_bf16 v[172:175], v[28:31], v[220:223], v[172:175]\n" \
"v_add_u32 v232, 0x4000, v232\n" \
"v_add_u32 v233, 0x4000, v233\n" \
"v_mfma_f32_16x16x32_bf16 v[168:171], v[16:19], v[216:219], v[168:171]\n" \
"v_cvt_pk_bf16_f32 v234, v234, v235\n" \
"v_cvt_pk_bf16_f32 v236, v236, v237\n" \
"v_mfma_f32_16x16x32_bf16 v[168:171], v[20:23], v[220:223], v[168:171]\n" \
"global_load_lds_dwordx4 v229, s[42:43] offset:-1024\n" \
"v_accvgpr_write_b32 a170, v234\n" \
"v_mfma_f32_16x16x32_bf16 v[164:167], v[8:11], v[216:219], v[164:167]\n" \
"s_add_u32 s42, s42, 0x80000\n" \
"s_addc_u32 s43, s43, 0\n" \
"v_accvgpr_write_b32 a171, v236\n" \
"v_mfma_f32_16x16x32_bf16 v[164:167], v[12:15], v[220:223], v[164:167]\n" \
"s_waitcnt vmcnt(18) lgkmcnt(0)\n" \
"v_accvgpr_read_b32 v234, a144\n" \
"v_accvgpr_read_b32 v235, a145\n" \
"v_mfma_f32_16x16x32_bf16 v[160:163], v[0:3], v[216:219], v[160:163]\n" \
"v_accvgpr_read_b32 v236, a146\n" \
"v_accvgpr_read_b32 v237, a147\n" \
"v_mfma_f32_16x16x32_bf16 v[160:163], v[4:7], v[220:223], v[160:163]\n" \
"s_barrier\n" \
"v_mfma_f32_16x16x32_bf16 v[80:83], v[32:35], v[192:195], v[80:83]\n" \
"ds_read_b64_tr_b16 v[0:1], v232 offset:0\n" \
"v_cvt_pk_bf16_f32 v234, v234, v235\n" \
"v_mfma_f32_16x16x32_bf16 v[80:83], v[36:39], v[196:199], v[80:83]\n" \
"ds_read_b64_tr_b16 v[2:3], v232 offset:2048\n" \
"v_cvt_pk_bf16_f32 v236, v236, v237\n" \
"v_mfma_f32_16x16x32_bf16 v[84:87], v[40:43], v[192:195], v[84:87]\n" \
"s_add_u32 m0, s87, 102400\n" \
"ds_read_b64_tr_b16 v[4:5], v232 offset:8192\n" \
"v_accvgpr_write_b32 a144, v234\n" \
"v_mfma_f32_16x16x32_bf16 v[84:87], v[44:47], v[196:199], v[84:87]\n" \
"global_load_lds_dwordx4 v224, s[48:49] offset:-4096\n" \
"v_accvgpr_write_b32 a145, v236\n" \
"v_mfma_f32_16x16x32_bf16 v[88:91], v[48:51], v[192:195], v[88:91]\n" \
"ds_read_b64_tr_b16 v[6:7], v232 offset:10240\n" \
"v_accvgpr_read_b32 v234, a176\n" \
"v_mfma_f32_16x16x32_bf16 v[88:91], v[52:55], v[196:199], v[88:91]\n" \
"ds_read_b64_tr_b16 v[8:9], v233 offset:0\n" \
"v_accvgpr_read_b32 v235, a177\n" \
"v_mfma_f32_16x16x32_bf16 v[92:95], v[56:59], v[192:195], v[92:95]\n" \
"ds_read_b64_tr_b16 v[10:11], v233 offset:2048\n" \
"v_accvgpr_read_b32 v236, a178\n" \
"v_mfma_f32_16x16x32_bf16 v[92:95], v[60:63], v[196:199], v[92:95]\n" \
"global_load_lds_dwordx4 v225, s[48:49] offset:-3072\n" \
"v_accvgpr_read_b32 v237, a179\n" \
"v_mfma_f32_16x16x32_bf16 v[124:127], v[56:59], v[200:203], v[124:127]\n" \
"ds_read_b128 v[192:195], v230 offset:0\n" \
"v_cvt_pk_bf16_f32 v234, v234, v235\n" \
"v_mfma_f32_16x16x32_bf16 v[124:127], v[60:63], v[204:207], v[124:127]\n" \
"ds_read_b128 v[196:199], v231 offset:0\n" \
"v_cvt_pk_bf16_f32 v236, v236, v237\n" \
"v_mfma_f32_16x16x32_bf16 v[120:123], v[48:51], v[200:203], v[120:123]\n" \
"ds_read_b64_tr_b16 v[12:13], v233 offset:8192\n" \
"v_accvgpr_write_b32 a146, v234\n" \
"v_mfma_f32_16x16x32_bf16 v[120:123], v[52:55], v[204:207], v[120:123]\n" \
"global_load_lds_dwordx4 v226, s[48:49] offset:-2048\n" \
"v_accvgpr_write_b32 a147, v236\n" \
"v_mfma_f32_16x16x32_bf16 v[116:119], v[40:43], v[200:203], v[116:119]\n" \
"ds_read_b64_tr_b16 v[14:15], v233 offset:10240\n" \
"v_accvgpr_read_b32 v234, a148\n" \
"v_mfma_f32_16x16x32_bf16 v[116:119], v[44:47], v[204:207], v[116:119]\n" \
"ds_read_b64_tr_b16 v[16:17], v232 offset:512\n" \
"v_accvgpr_read_b32 v235, a149\n" \
"v_mfma_f32_16x16x32_bf16 v[112:115], v[32:35], v[200:203], v[112:115]\n" \
"ds_read_b64_tr_b16 v[18:19], v232 offset:2560\n" \
"v_accvgpr_read_b32 v236, a150\n" \
"v_mfma_f32_16x16x32_bf16 v[112:115], v[36:39], v[204:207], v[112:115]\n" \
"global_load_lds_dwordx4 v227, s[48:49] offset:-1024\n" \
"v_accvgpr_read_b32 v237, a151\n" \
"v_mfma_f32_16x16x32_bf16 v[144:147], v[32:35], v[208:211], v[144:147]\n" \
"s_add_u32 s48, s48, 0x80\n" \
"s_addc_u32 s49, s49, 0\n" \
"ds_read_b128 v[200:203], v230 offset:2048\n" \
"v_mfma_f32_16x16x32_bf16 v[144:147], v[36:39], v[212:215], v[144:147]\n" \
"ds_read_b128 v[204:207], v231 offset:2048\n" \
"v_cvt_pk_bf16_f32 v234, v234, v235\n" \
"v_mfma_f32_16x16x32_bf16 v[148:151], v[40:43], v[208:211], v[148:151]\n" \
"ds_read_b64_tr_b16 v[20:21], v232 offset:8704\n" \
"v_cvt_pk_bf16_f32 v236, v236, v237\n" \
"v_mfma_f32_16x16x32_bf16 v[148:151], v[44:47], v[212:215], v[148:151]\n" \
"global_load_lds_dwordx4 v224, s[46:47] offset:0\n" \
"v_accvgpr_write_b32 a148, v234\n" \
"v_mfma_f32_16x16x32_bf16 v[152:155], v[48:51], v[208:211], v[152:155]\n" \
"ds_read_b64_tr_b16 v[22:23], v232 offset:10752\n" \
"v_accvgpr_write_b32 a149, v236\n" \
"v_mfma_f32_16x16x32_bf16 v[152:155], v[52:55], v[212:215], v[152:155]\n" \
"ds_read_b64_tr_b16 v[24:25], v233 offset:512\n" \
"v_accvgpr_read_b32 v234, a180\n" \
"v_mfma_f32_16x16x32_bf16 v[156:159], v[56:59], v[208:211], v[156:159]\n" \
"ds_read_b64_tr_b16 v[26:27], v233 offset:2560\n" \
"v_accvgpr_read_b32 v235, a181\n" \
"v_mfma_f32_16x16x32_bf16 v[156:159], v[60:63], v[212:215], v[156:159]\n" \
"global_load_lds_dwordx4 v225, s[46:47] offset:1024\n" \
"v_accvgpr_read_b32 v236, a182\n" \
"v_mfma_f32_16x16x32_bf16 v[188:191], v[56:59], v[216:219], v[188:191]\n" \
"ds_read_b128 v[208:211], v230 offset:4096\n" \
"v_accvgpr_read_b32 v237, a183\n" \
"v_mfma_f32_16x16x32_bf16 v[188:191], v[60:63], v[220:223], v[188:191]\n" \
"ds_read_b128 v[212:215], v231 offset:4096\n" \
"v_cvt_pk_bf16_f32 v234, v234, v235\n" \
"v_mfma_f32_16x16x32_bf16 v[184:187], v[48:51], v[216:219], v[184:187]\n" \
"ds_read_b64_tr_b16 v[28:29], v233 offset:8704\n" \
"v_cvt_pk_bf16_f32 v236, v236, v237\n" \
"v_mfma_f32_16x16x32_bf16 v[184:187], v[52:55], v[220:223], v[184:187]\n" \
"global_load_lds_dwordx4 v226, s[46:47] offset:2048\n" \
"v_accvgpr_write_b32 a150, v234\n" \
"v_mfma_f32_16x16x32_bf16 v[180:183], v[40:43], v[216:219], v[180:183]\n" \
"ds_read_b64_tr_b16 v[30:31], v233 offset:10752\n" \
"v_accvgpr_write_b32 a151, v236\n" \
"v_mfma_f32_16x16x32_bf16 v[180:183], v[44:47], v[220:223], v[180:183]\n" \
"v_accvgpr_read_b32 v234, a152\n" \
"v_accvgpr_read_b32 v235, a153\n" \
"v_mfma_f32_16x16x32_bf16 v[176:179], v[32:35], v[216:219], v[176:179]\n" \
"v_accvgpr_read_b32 v236, a154\n" \
"Lpg2766_%=:\n" \
"s_cbranch_execz Lpg4934_%=\n" \
"v_accvgpr_read_b32 v237, a155\n" \
"v_mfma_f32_16x16x32_bf16 v[176:179], v[36:39], v[220:223], v[176:179]\n" \
"s_waitcnt lgkmcnt(1)\n" \
"global_load_lds_dwordx4 v227, s[46:47] offset:3072\n" \
"v_cvt_pk_bf16_f32 v234, v234, v235\n" \
"v_mfma_f32_16x16x32_bf16 v[64:67], v[0:3], v[192:195], v[64:67]\n" \
"s_add_u32 s46, s46, 0x80\n" \
"s_addc_u32 s47, s47, 0\n" \
"ds_read_b128 v[216:219], v230 offset:6144\n" \
"v_mfma_f32_16x16x32_bf16 v[64:67], v[4:7], v[196:199], v[64:67]\n" \
"s_add_u32 s90, s89, 0\n" \
"ds_read_b128 v[220:223], v231 offset:6144\n" \
"v_cvt_pk_bf16_f32 v236, v236, v237\n" \
"v_mfma_f32_16x16x32_bf16 v[68:71], v[8:11], v[192:195], v[68:71]\n" \
"ds_read_b64_tr_b16 v[32:33], v232 offset:1024\n" \
"v_accvgpr_write_b32 a152, v234\n" \
"v_mfma_f32_16x16x32_bf16 v[68:71], v[12:15], v[196:199], v[68:71]\n" \
"buffer_store_dwordx4 a[208:211], v248, s[56:59], s90 offen offset:0 sc1\n" \
"v_accvgpr_write_b32 a153, v236\n" \
"v_accvgpr_read_b32 v234, a184\n" \
"v_accvgpr_read_b32 v235, a185\n" \
"v_accvgpr_read_b32 v236, a186\n" \
"v_mfma_f32_16x16x32_bf16 v[72:75], v[16:19], v[192:195], v[72:75]\n" \
"ds_read_b64_tr_b16 v[34:35], v232 offset:3072\n" \
"v_accvgpr_read_b32 v237, a187\n" \
"v_mfma_f32_16x16x32_bf16 v[72:75], v[20:23], v[196:199], v[72:75]\n" \
"s_waitcnt lgkmcnt(3)\n" \
"ds_read_b64_tr_b16 v[36:37], v232 offset:9216\n" \
"v_cvt_pk_bf16_f32 v234, v234, v235\n" \
"v_mfma_f32_16x16x32_bf16 v[76:79], v[24:27], v[192:195], v[76:79]\n" \
"ds_read_b64_tr_b16 v[38:39], v232 offset:11264\n" \
"v_cvt_pk_bf16_f32 v236, v236, v237\n" \
"v_mfma_f32_16x16x32_bf16 v[76:79], v[28:31], v[196:199], v[76:79]\n" \
"v_accvgpr_write_b32 a154, v234\n" \
"v_accvgpr_write_b32 a155, v236\n" \
"v_mfma_f32_16x16x32_bf16 v[108:111], v[24:27], v[200:203], v[108:111]\n" \
"ds_read_b64_tr_b16 v[40:41], v233 offset:1024\n" \
"v_accvgpr_read_b32 v234, a156\n" \
"v_mfma_f32_16x16x32_bf16 v[108:111], v[28:31], v[204:207], v[108:111]\n" \
"ds_read_b64_tr_b16 v[42:43], v233 offset:3072\n" \
"v_accvgpr_read_b32 v235, a157\n" \
"v_mfma_f32_16x16x32_bf16 v[104:107], v[16:19], v[200:203], v[104:107]\n" \
"ds_read_b64_tr_b16 v[44:45], v233 offset:9216\n" \
"v_accvgpr_read_b32 v236, a158\n" \
"v_mfma_f32_16x16x32_bf16 v[104:107], v[20:23], v[204:207], v[104:107]\n" \
"v_accvgpr_read_b32 v237, a159\n" \
"v_cvt_pk_bf16_f32 v234, v234, v235\n" \
"v_mfma_f32_16x16x32_bf16 v[100:103], v[8:11], v[200:203], v[100:103]\n" \
"ds_read_b64_tr_b16 v[46:47], v233 offset:11264\n" \
"v_cvt_pk_bf16_f32 v236, v236, v237\n" \
"v_mfma_f32_16x16x32_bf16 v[100:103], v[12:15], v[204:207], v[100:103]\n" \
"ds_read_b64_tr_b16 v[48:49], v232 offset:1536\n" \
"v_accvgpr_write_b32 a156, v234\n" \
"v_mfma_f32_16x16x32_bf16 v[96:99], v[0:3], v[200:203], v[96:99]\n" \
"s_add_u32 m0, s95, 36864\n" \
"ds_read_b64_tr_b16 v[50:51], v232 offset:3584\n" \
"v_accvgpr_write_b32 a157, v236\n" \
"v_mfma_f32_16x16x32_bf16 v[96:99], v[4:7], v[204:207], v[96:99]\n" \
"global_load_lds_dwordx4 v228, s[44:45] offset:-4096\n" \
"v_accvgpr_read_b32 v234, a188\n" \
"v_mfma_f32_16x16x32_bf16 v[128:131], v[0:3], v[208:211], v[128:131]\n" \
"ds_read_b64_tr_b16 v[52:53], v232 offset:9728\n" \
"v_accvgpr_read_b32 v235, a189\n" \
"v_mfma_f32_16x16x32_bf16 v[128:131], v[4:7], v[212:215], v[128:131]\n" \
"ds_read_b64_tr_b16 v[54:55], v232 offset:11776\n" \
"v_accvgpr_read_b32 v236, a190\n" \
"v_mfma_f32_16x16x32_bf16 v[132:135], v[8:11], v[208:211], v[132:135]\n" \
"ds_read_b64_tr_b16 v[56:57], v233 offset:1536\n" \
"v_accvgpr_read_b32 v237, a191\n" \
"v_mfma_f32_16x16x32_bf16 v[132:135], v[12:15], v[212:215], v[132:135]\n" \
"global_load_lds_dwordx4 v229, s[44:45] offset:-3072\n" \
"v_cvt_pk_bf16_f32 v234, v234, v235\n" \
"v_mfma_f32_16x16x32_bf16 v[136:139], v[16:19], v[208:211], v[136:139]\n" \
"s_add_u32 s44, s44, 0x80000\n" \
"s_addc_u32 s45, s45, 0\n" \
"ds_read_b64_tr_b16 v[58:59], v233 offset:3584\n" \
"v_mfma_f32_16x16x32_bf16 v[136:139], v[20:23], v[212:215], v[136:139]\n" \
"ds_read_b64_tr_b16 v[60:61], v233 offset:9728\n" \
"v_cvt_pk_bf16_f32 v236, v236, v237\n" \
"v_mfma_f32_16x16x32_bf16 v[140:143], v[24:27], v[208:211], v[140:143]\n" \
"ds_read_b64_tr_b16 v[62:63], v233 offset:11776\n" \
"v_accvgpr_write_b32 a158, v234\n" \
"v_mfma_f32_16x16x32_bf16 v[140:143], v[28:31], v[212:215], v[140:143]\n" \
"s_waitcnt lgkmcnt(3)\n" \
"global_load_lds_dwordx4 v228, s[42:43] offset:-2048\n" \
"v_accvgpr_write_b32 a159, v236\n" \
"v_mfma_f32_16x16x32_bf16 v[172:175], v[24:27], v[216:219], v[172:175]\n" \
"v_add_u32 v230, 0xffff0000, v230\n" \
"v_add_u32 v231, 0xffff0000, v231\n" \
"v_mfma_f32_16x16x32_bf16 v[172:175], v[28:31], v[220:223], v[172:175]\n" \
"v_add_u32 v232, 0xffff4000, v232\n" \
"v_add_u32 v233, 0xffff4000, v233\n" \
"v_mfma_f32_16x16x32_bf16 v[168:171], v[16:19], v[216:219], v[168:171]\n" \
"v_accvgpr_read_b32 v234, a64\n" \
"v_accvgpr_read_b32 v235, a65\n" \
"v_mfma_f32_16x16x32_bf16 v[168:171], v[20:23], v[220:223], v[168:171]\n" \
"global_load_lds_dwordx4 v229, s[42:43] offset:-1024\n" \
"v_accvgpr_read_b32 v236, a66\n" \
"v_mfma_f32_16x16x32_bf16 v[164:167], v[8:11], v[216:219], v[164:167]\n" \
"s_add_u32 s42, s42, 0x80000\n" \
"s_addc_u32 s43, s43, 0\n" \
"v_accvgpr_read_b32 v237, a67\n" \
"v_mfma_f32_16x16x32_bf16 v[164:167], v[12:15], v[220:223], v[164:167]\n" \
"s_waitcnt vmcnt(18) lgkmcnt(0)\n" \
"v_mul_f32 v234, s92, v234\n" \
"v_mul_f32 v235, s92, v235\n" \
"v_mfma_f32_16x16x32_bf16 v[160:163], v[0:3], v[216:219], v[160:163]\n" \
"v_mul_f32 v236, s92, v236\n" \
"v_mul_f32 v237, s92, v237\n" \
"v_mfma_f32_16x16x32_bf16 v[160:163], v[4:7], v[220:223], v[160:163]\n" \
"s_barrier\n" \
"v_mfma_f32_16x16x32_bf16 v[80:83], v[32:35], v[192:195], v[80:83]\n" \
"ds_read_b64_tr_b16 v[0:1], v232 offset:0\n" \
"v_cvt_pk_bf16_f32 v234, v234, v235\n" \
"v_mfma_f32_16x16x32_bf16 v[80:83], v[36:39], v[196:199], v[80:83]\n" \
"ds_read_b64_tr_b16 v[2:3], v232 offset:2048\n" \
"v_cvt_pk_bf16_f32 v236, v236, v237\n" \
"v_mfma_f32_16x16x32_bf16 v[84:87], v[40:43], v[192:195], v[84:87]\n" \
"s_add_u32 m0, s87, 135168\n" \
"ds_read_b64_tr_b16 v[4:5], v232 offset:8192\n" \
"v_accvgpr_write_b32 a64, v234\n" \
"v_mfma_f32_16x16x32_bf16 v[84:87], v[44:47], v[196:199], v[84:87]\n" \
"global_load_lds_dwordx4 v224, s[48:49] offset:-4096\n" \
"v_accvgpr_write_b32 a65, v236\n" \
"v_mfma_f32_16x16x32_bf16 v[88:91], v[48:51], v[192:195], v[88:91]\n" \
"ds_read_b64_tr_b16 v[6:7], v232 offset:10240\n" \
"v_accvgpr_read_b32 v234, a68\n" \
"v_mfma_f32_16x16x32_bf16 v[88:91], v[52:55], v[196:199], v[88:91]\n" \
"ds_read_b64_tr_b16 v[8:9], v233 offset:0\n" \
"v_accvgpr_read_b32 v235, a69\n" \
"v_mfma_f32_16x16x32_bf16 v[92:95], v[56:59], v[192:195], v[92:95]\n" \
"ds_read_b64_tr_b16 v[10:11], v233 offset:2048\n" \
"v_accvgpr_read_b32 v236, a70\n" \
"v_mfma_f32_16x16x32_bf16 v[92:95], v[60:63], v[196:199], v[92:95]\n" \
"global_load_lds_dwordx4 v225, s[48:49] offset:-3072\n" \
"v_accvgpr_read_b32 v237, a71\n" \
"v_mfma_f32_16x16x32_bf16 v[124:127], v[56:59], v[200:203], v[124:127]\n" \
"ds_read_b128 v[192:195], v230 offset:0\n" \
"v_mul_f32 v234, s92, v234\n" \
"v_mfma_f32_16x16x32_bf16 v[124:127], v[60:63], v[204:207], v[124:127]\n" \
"ds_read_b128 v[196:199], v231 offset:0\n" \
"v_mul_f32 v235, s92, v235\n" \
"v_mfma_f32_16x16x32_bf16 v[120:123], v[48:51], v[200:203], v[120:123]\n" \
"ds_read_b64_tr_b16 v[12:13], v233 offset:8192\n" \
"v_mul_f32 v236, s92, v236\n" \
"v_mfma_f32_16x16x32_bf16 v[120:123], v[52:55], v[204:207], v[120:123]\n" \
"global_load_lds_dwordx4 v226, s[48:49] offset:-2048\n" \
"v_mul_f32 v237, s92, v237\n" \
"v_mfma_f32_16x16x32_bf16 v[116:119], v[40:43], v[200:203], v[116:119]\n" \
"ds_read_b64_tr_b16 v[14:15], v233 offset:10240\n" \
"v_cvt_pk_bf16_f32 v234, v234, v235\n" \
"v_mfma_f32_16x16x32_bf16 v[116:119], v[44:47], v[204:207], v[116:119]\n" \
"ds_read_b64_tr_b16 v[16:17], v232 offset:512\n" \
"v_cvt_pk_bf16_f32 v236, v236, v237\n" \
"v_mfma_f32_16x16x32_bf16 v[112:115], v[32:35], v[200:203], v[112:115]\n" \
"ds_read_b64_tr_b16 v[18:19], v232 offset:2560\n" \
"v_accvgpr_write_b32 a66, v234\n" \
"v_mfma_f32_16x16x32_bf16 v[112:115], v[36:39], v[204:207], v[112:115]\n" \
"global_load_lds_dwordx4 v227, s[48:49] offset:-1024\n" \
"v_accvgpr_write_b32 a67, v236\n" \
"v_mfma_f32_16x16x32_bf16 v[144:147], v[32:35], v[208:211], v[144:147]\n" \
"s_add_u32 s48, s48, 0x80\n" \
"s_addc_u32 s49, s49, 0\n" \
"ds_read_b128 v[200:203], v230 offset:2048\n" \
"v_mfma_f32_16x16x32_bf16 v[144:147], v[36:39], v[212:215], v[144:147]\n" \
"ds_read_b128 v[204:207], v231 offset:2048\n" \
"v_accvgpr_read_b32 v234, a96\n" \
"v_mfma_f32_16x16x32_bf16 v[148:151], v[40:43], v[208:211], v[148:151]\n" \
"ds_read_b64_tr_b16 v[20:21], v232 offset:8704\n" \
"v_accvgpr_read_b32 v235, a97\n" \
"v_mfma_f32_16x16x32_bf16 v[148:151], v[44:47], v[212:215], v[148:151]\n" \
"global_load_lds_dwordx4 v224, s[46:47] offset:0\n" \
"v_accvgpr_read_b32 v236, a98\n" \
"v_mfma_f32_16x16x32_bf16 v[152:155], v[48:51], v[208:211], v[152:155]\n" \
"ds_read_b64_tr_b16 v[22:23], v232 offset:10752\n" \
"v_accvgpr_read_b32 v237, a99\n" \
"v_mfma_f32_16x16x32_bf16 v[152:155], v[52:55], v[212:215], v[152:155]\n" \
"ds_read_b64_tr_b16 v[24:25], v233 offset:512\n" \
"v_mul_f32 v234, s92, v234\n" \
"v_mfma_f32_16x16x32_bf16 v[156:159], v[56:59], v[208:211], v[156:159]\n" \
"ds_read_b64_tr_b16 v[26:27], v233 offset:2560\n" \
"v_mul_f32 v235, s92, v235\n" \
"v_mfma_f32_16x16x32_bf16 v[156:159], v[60:63], v[212:215], v[156:159]\n" \
"global_load_lds_dwordx4 v225, s[46:47] offset:1024\n" \
"v_mul_f32 v236, s92, v236\n" \
"v_mfma_f32_16x16x32_bf16 v[188:191], v[56:59], v[216:219], v[188:191]\n" \
"ds_read_b128 v[208:211], v230 offset:4096\n" \
"v_mul_f32 v237, s92, v237\n" \
"v_mfma_f32_16x16x32_bf16 v[188:191], v[60:63], v[220:223], v[188:191]\n" \
"ds_read_b128 v[212:215], v231 offset:4096\n" \
"v_cvt_pk_bf16_f32 v234, v234, v235\n" \
"v_mfma_f32_16x16x32_bf16 v[184:187], v[48:51], v[216:219], v[184:187]\n" \
"ds_read_b64_tr_b16 v[28:29], v233 offset:8704\n" \
"v_cvt_pk_bf16_f32 v236, v236, v237\n" \
"v_mfma_f32_16x16x32_bf16 v[184:187], v[52:55], v[220:223], v[184:187]\n" \
"global_load_lds_dwordx4 v226, s[46:47] offset:2048\n" \
"v_accvgpr_write_b32 a96, v234\n" \
"v_mfma_f32_16x16x32_bf16 v[180:183], v[40:43], v[216:219], v[180:183]\n" \
"ds_read_b64_tr_b16 v[30:31], v233 offset:10752\n" \
"v_accvgpr_write_b32 a97, v236\n" \
"v_mfma_f32_16x16x32_bf16 v[180:183], v[44:47], v[220:223], v[180:183]\n" \
"v_accvgpr_read_b32 v234, a100\n" \
"v_accvgpr_read_b32 v235, a101\n" \
"v_mfma_f32_16x16x32_bf16 v[176:179], v[32:35], v[216:219], v[176:179]\n" \
"v_accvgpr_read_b32 v236, a102\n" \
"v_accvgpr_read_b32 v237, a103\n" \
"v_mfma_f32_16x16x32_bf16 v[176:179], v[36:39], v[220:223], v[176:179]\n" \
"s_waitcnt lgkmcnt(1)\n" \
"global_load_lds_dwordx4 v227, s[46:47] offset:3072\n" \
"v_mul_f32 v234, s92, v234\n" \
"v_mfma_f32_16x16x32_bf16 v[64:67], v[0:3], v[192:195], v[64:67]\n" \
"s_add_u32 s46, s46, 0x80\n" \
"s_addc_u32 s47, s47, 0\n" \
"ds_read_b128 v[216:219], v230 offset:6144\n" \
"v_mfma_f32_16x16x32_bf16 v[64:67], v[4:7], v[196:199], v[64:67]\n" \
"ds_read_b128 v[220:223], v231 offset:6144\n" \
"v_mul_f32 v235, s92, v235\n" \
"v_mfma_f32_16x16x32_bf16 v[68:71], v[8:11], v[192:195], v[68:71]\n" \
"ds_read_b64_tr_b16 v[32:33], v232 offset:1024\n" \
"v_mul_f32 v236, s92, v236\n" \
"v_mfma_f32_16x16x32_bf16 v[68:71], v[12:15], v[196:199], v[68:71]\n" \
"buffer_store_dwordx4 a[212:215], v248, s[56:59], s90 offen offset:1024 sc1\n" \
"v_mul_f32 v237, s92, v237\n" \
"v_cvt_pk_bf16_f32 v234, v234, v235\n" \
"v_cvt_pk_bf16_f32 v236, v236, v237\n" \
"v_accvgpr_write_b32 a98, v234\n" \
"v_mfma_f32_16x16x32_bf16 v[72:75], v[16:19], v[192:195], v[72:75]\n" \
"ds_read_b64_tr_b16 v[34:35], v232 offset:3072\n" \
"v_accvgpr_write_b32 a99, v236\n" \
"v_mfma_f32_16x16x32_bf16 v[72:75], v[20:23], v[196:199], v[72:75]\n" \
"s_waitcnt lgkmcnt(3)\n" \
"ds_read_b64_tr_b16 v[36:37], v232 offset:9216\n" \
"v_accvgpr_read_b32 v234, a72\n" \
"v_mfma_f32_16x16x32_bf16 v[76:79], v[24:27], v[192:195], v[76:79]\n" \
"ds_read_b64_tr_b16 v[38:39], v232 offset:11264\n" \
"v_accvgpr_read_b32 v235, a73\n" \
"v_mfma_f32_16x16x32_bf16 v[76:79], v[28:31], v[196:199], v[76:79]\n" \
"buffer_store_dwordx4 a[216:219], v248, s[56:59], s90 offen offset:2048 sc1\n" \
"v_accvgpr_read_b32 v236, a74\n" \
"v_accvgpr_read_b32 v237, a75\n" \
"v_mul_f32 v234, s92, v234\n" \
"v_mul_f32 v235, s92, v235\n" \
"v_mfma_f32_16x16x32_bf16 v[108:111], v[24:27], v[200:203], v[108:111]\n" \
"ds_read_b64_tr_b16 v[40:41], v233 offset:1024\n" \
"v_mul_f32 v236, s92, v236\n" \
"v_mfma_f32_16x16x32_bf16 v[108:111], v[28:31], v[204:207], v[108:111]\n" \
"ds_read_b64_tr_b16 v[42:43], v233 offset:3072\n" \
"v_mul_f32 v237, s92, v237\n" \
"v_mfma_f32_16x16x32_bf16 v[104:107], v[16:19], v[200:203], v[104:107]\n" \
"ds_read_b64_tr_b16 v[44:45], v233 offset:9216\n" \
"v_cvt_pk_bf16_f32 v234, v234, v235\n" \
"v_mfma_f32_16x16x32_bf16 v[104:107], v[20:23], v[204:207], v[104:107]\n" \
"v_cvt_pk_bf16_f32 v236, v236, v237\n" \
"v_accvgpr_write_b32 a72, v234\n" \
"v_mfma_f32_16x16x32_bf16 v[100:103], v[8:11], v[200:203], v[100:103]\n" \
"ds_read_b64_tr_b16 v[46:47], v233 offset:11264\n" \
"v_accvgpr_write_b32 a73, v236\n" \
"v_mfma_f32_16x16x32_bf16 v[100:103], v[12:15], v[204:207], v[100:103]\n" \
"ds_read_b64_tr_b16 v[48:49], v232 offset:1536\n" \
"v_accvgpr_read_b32 v234, a76\n" \
"v_mfma_f32_16x16x32_bf16 v[96:99], v[0:3], v[200:203], v[96:99]\n" \
"s_add_u32 m0, s95, 53248\n" \
"ds_read_b64_tr_b16 v[50:51], v232 offset:3584\n" \
"v_accvgpr_read_b32 v235, a77\n" \
"v_mfma_f32_16x16x32_bf16 v[96:99], v[4:7], v[204:207], v[96:99]\n" \
"global_load_lds_dwordx4 v228, s[44:45] offset:-4096\n" \
"v_accvgpr_read_b32 v236, a78\n" \
"v_mfma_f32_16x16x32_bf16 v[128:131], v[0:3], v[208:211], v[128:131]\n" \
"ds_read_b64_tr_b16 v[52:53], v232 offset:9728\n" \
"v_accvgpr_read_b32 v237, a79\n" \
"v_mfma_f32_16x16x32_bf16 v[128:131], v[4:7], v[212:215], v[128:131]\n" \
"ds_read_b64_tr_b16 v[54:55], v232 offset:11776\n" \
"v_mul_f32 v234, s92, v234\n" \
"v_mfma_f32_16x16x32_bf16 v[132:135], v[8:11], v[208:211], v[132:135]\n" \
"ds_read_b64_tr_b16 v[56:57], v233 offset:1536\n" \
"v_mul_f32 v235, s92, v235\n" \
"v_mfma_f32_16x16x32_bf16 v[132:135], v[12:15], v[212:215], v[132:135]\n" \
"global_load_lds_dwordx4 v229, s[44:45] offset:-3072\n" \
"v_mul_f32 v236, s92, v236\n" \
"v_mfma_f32_16x16x32_bf16 v[136:139], v[16:19], v[208:211], v[136:139]\n" \
"s_add_u32 s44, s44, 0x80000\n" \
"s_addc_u32 s45, s45, 0\n" \
"ds_read_b64_tr_b16 v[58:59], v233 offset:3584\n" \
"v_mfma_f32_16x16x32_bf16 v[136:139], v[20:23], v[212:215], v[136:139]\n" \
"ds_read_b64_tr_b16 v[60:61], v233 offset:9728\n" \
"v_mul_f32 v237, s92, v237\n" \
"v_mfma_f32_16x16x32_bf16 v[140:143], v[24:27], v[208:211], v[140:143]\n" \
"ds_read_b64_tr_b16 v[62:63], v233 offset:11776\n" \
"v_cvt_pk_bf16_f32 v234, v234, v235\n" \
"v_mfma_f32_16x16x32_bf16 v[140:143], v[28:31], v[212:215], v[140:143]\n" \
"s_waitcnt lgkmcnt(3)\n" \
"global_load_lds_dwordx4 v228, s[42:43] offset:-2048\n" \
"v_cvt_pk_bf16_f32 v236, v236, v237\n" \
"v_mfma_f32_16x16x32_bf16 v[172:175], v[24:27], v[216:219], v[172:175]\n" \
"v_add_u32 v230, 0x8000, v230\n" \
"v_add_u32 v231, 0x8000, v231\n" \
"v_mfma_f32_16x16x32_bf16 v[172:175], v[28:31], v[220:223], v[172:175]\n" \
"v_add_u32 v232, 0x4000, v232\n" \
"v_add_u32 v233, 0x4000, v233\n" \
"v_mfma_f32_16x16x32_bf16 v[168:171], v[16:19], v[216:219], v[168:171]\n" \
"v_accvgpr_write_b32 a74, v234\n" \
"v_accvgpr_write_b32 a75, v236\n" \
"v_mfma_f32_16x16x32_bf16 v[168:171], v[20:23], v[220:223], v[168:171]\n" \
"global_load_lds_dwordx4 v229, s[42:43] offset:-1024\n" \
"v_accvgpr_read_b32 v234, a104\n" \
"v_mfma_f32_16x16x32_bf16 v[164:167], v[8:11], v[216:219], v[164:167]\n" \
"s_add_u32 s42, s42, 0x80000\n" \
"s_addc_u32 s43, s43, 0\n" \
"v_accvgpr_read_b32 v235, a105\n" \
"v_mfma_f32_16x16x32_bf16 v[164:167], v[12:15], v[220:223], v[164:167]\n" \
"s_waitcnt vmcnt(19) lgkmcnt(0)\n" \
"v_accvgpr_read_b32 v236, a106\n" \
"v_accvgpr_read_b32 v237, a107\n" \
"v_mfma_f32_16x16x32_bf16 v[160:163], v[0:3], v[216:219], v[160:163]\n" \
"v_mul_f32 v234, s92, v234\n" \
"v_mul_f32 v235, s92, v235\n" \
"v_mfma_f32_16x16x32_bf16 v[160:163], v[4:7], v[220:223], v[160:163]\n" \
"s_barrier\n" \
"v_mfma_f32_16x16x32_bf16 v[80:83], v[32:35], v[192:195], v[80:83]\n" \
"ds_read_b64_tr_b16 v[0:1], v232 offset:0\n" \
"v_mul_f32 v236, s92, v236\n" \
"v_mfma_f32_16x16x32_bf16 v[80:83], v[36:39], v[196:199], v[80:83]\n" \
"ds_read_b64_tr_b16 v[2:3], v232 offset:2048\n" \
"v_mul_f32 v237, s92, v237\n" \
"v_mfma_f32_16x16x32_bf16 v[84:87], v[40:43], v[192:195], v[84:87]\n" \
"s_add_u32 m0, s87, 69632\n" \
"ds_read_b64_tr_b16 v[4:5], v232 offset:8192\n" \
"v_cvt_pk_bf16_f32 v234, v234, v235\n" \
"v_mfma_f32_16x16x32_bf16 v[84:87], v[44:47], v[196:199], v[84:87]\n" \
"global_load_lds_dwordx4 v224, s[48:49] offset:-4096\n" \
"v_cvt_pk_bf16_f32 v236, v236, v237\n" \
"v_mfma_f32_16x16x32_bf16 v[88:91], v[48:51], v[192:195], v[88:91]\n" \
"ds_read_b64_tr_b16 v[6:7], v232 offset:10240\n" \
"v_accvgpr_write_b32 a104, v234\n" \
"v_mfma_f32_16x16x32_bf16 v[88:91], v[52:55], v[196:199], v[88:91]\n" \
"ds_read_b64_tr_b16 v[8:9], v233 offset:0\n" \
"v_accvgpr_write_b32 a105, v236\n" \
"v_mfma_f32_16x16x32_bf16 v[92:95], v[56:59], v[192:195], v[92:95]\n" \
"ds_read_b64_tr_b16 v[10:11], v233 offset:2048\n" \
"v_accvgpr_read_b32 v234, a108\n" \
"v_mfma_f32_16x16x32_bf16 v[92:95], v[60:63], v[196:199], v[92:95]\n" \
"global_load_lds_dwordx4 v225, s[48:49] offset:-3072\n" \
"v_accvgpr_read_b32 v235, a109\n" \
"v_mfma_f32_16x16x32_bf16 v[124:127], v[56:59], v[200:203], v[124:127]\n" \
"ds_read_b128 v[192:195], v230 offset:0\n" \
"v_accvgpr_read_b32 v236, a110\n" \
"v_mfma_f32_16x16x32_bf16 v[124:127], v[60:63], v[204:207], v[124:127]\n" \
"ds_read_b128 v[196:199], v231 offset:0\n" \
"v_accvgpr_read_b32 v237, a111\n" \
"v_mfma_f32_16x16x32_bf16 v[120:123], v[48:51], v[200:203], v[120:123]\n" \
"ds_read_b64_tr_b16 v[12:13], v233 offset:8192\n" \
"v_mul_f32 v234, s92, v234\n" \
"v_mfma_f32_16x16x32_bf16 v[120:123], v[52:55], v[204:207], v[120:123]\n" \
"global_load_lds_dwordx4 v226, s[48:49] offset:-2048\n" \
"v_mul_f32 v235, s92, v235\n" \
"v_mfma_f32_16x16x32_bf16 v[116:119], v[40:43], v[200:203], v[116:119]\n" \
"ds_read_b64_tr_b16 v[14:15], v233 offset:10240\n" \
"v_mul_f32 v236, s92, v236\n" \
"v_mfma_f32_16x16x32_bf16 v[116:119], v[44:47], v[204:207], v[116:119]\n" \
"ds_read_b64_tr_b16 v[16:17], v232 offset:512\n" \
"v_mul_f32 v237, s92, v237\n" \
"v_mfma_f32_16x16x32_bf16 v[112:115], v[32:35], v[200:203], v[112:115]\n" \
"ds_read_b64_tr_b16 v[18:19], v232 offset:2560\n" \
"v_cvt_pk_bf16_f32 v234, v234, v235\n" \
"v_mfma_f32_16x16x32_bf16 v[112:115], v[36:39], v[204:207], v[112:115]\n" \
"global_load_lds_dwordx4 v227, s[48:49] offset:-1024\n" \
"v_cvt_pk_bf16_f32 v236, v236, v237\n" \
"v_mfma_f32_16x16x32_bf16 v[144:147], v[32:35], v[208:211], v[144:147]\n" \
"s_add_u32 s48, s48, 0x80\n" \
"s_addc_u32 s49, s49, 0\n" \
"ds_read_b128 v[200:203], v230 offset:2048\n" \
"v_mfma_f32_16x16x32_bf16 v[144:147], v[36:39], v[212:215], v[144:147]\n" \
"ds_read_b128 v[204:207], v231 offset:2048\n" \
"v_accvgpr_write_b32 a106, v234\n" \
"v_mfma_f32_16x16x32_bf16 v[148:151], v[40:43], v[208:211], v[148:151]\n" \
"ds_read_b64_tr_b16 v[20:21], v232 offset:8704\n" \
"v_accvgpr_write_b32 a107, v236\n" \
"v_mfma_f32_16x16x32_bf16 v[148:151], v[44:47], v[212:215], v[148:151]\n" \
"global_load_lds_dwordx4 v224, s[46:47] offset:0\n" \
"v_accvgpr_read_b32 v234, a80\n" \
"v_mfma_f32_16x16x32_bf16 v[152:155], v[48:51], v[208:211], v[152:155]\n" \
"ds_read_b64_tr_b16 v[22:23], v232 offset:10752\n" \
"v_accvgpr_read_b32 v235, a81\n" \
"v_mfma_f32_16x16x32_bf16 v[152:155], v[52:55], v[212:215], v[152:155]\n" \
"ds_read_b64_tr_b16 v[24:25], v233 offset:512\n" \
"v_accvgpr_read_b32 v236, a82\n" \
"v_mfma_f32_16x16x32_bf16 v[156:159], v[56:59], v[208:211], v[156:159]\n" \
"ds_read_b64_tr_b16 v[26:27], v233 offset:2560\n" \
"v_accvgpr_read_b32 v237, a83\n" \
"v_mfma_f32_16x16x32_bf16 v[156:159], v[60:63], v[212:215], v[156:159]\n" \
"global_load_lds_dwordx4 v225, s[46:47] offset:1024\n" \
"v_cvt_pk_bf16_f32 v234, v234, v235\n" \
"v_mfma_f32_16x16x32_bf16 v[188:191], v[56:59], v[216:219], v[188:191]\n" \
"ds_read_b128 v[208:211], v230 offset:4096\n" \
"v_cvt_pk_bf16_f32 v236, v236, v237\n" \
"v_mfma_f32_16x16x32_bf16 v[188:191], v[60:63], v[220:223], v[188:191]\n" \
"ds_read_b128 v[212:215], v231 offset:4096\n" \
"v_accvgpr_write_b32 a80, v234\n" \
"v_mfma_f32_16x16x32_bf16 v[184:187], v[48:51], v[216:219], v[184:187]\n" \
"ds_read_b64_tr_b16 v[28:29], v233 offset:8704\n" \
"v_accvgpr_write_b32 a81, v236\n" \
"v_mfma_f32_16x16x32_bf16 v[184:187], v[52:55], v[220:223], v[184:187]\n" \
"global_load_lds_dwordx4 v226, s[46:47] offset:2048\n" \
"v_accvgpr_read_b32 v234, a112\n" \
"v_mfma_f32_16x16x32_bf16 v[180:183], v[40:43], v[216:219], v[180:183]\n" \
"ds_read_b64_tr_b16 v[30:31], v233 offset:10752\n" \
"v_accvgpr_read_b32 v235, a113\n" \
"v_mfma_f32_16x16x32_bf16 v[180:183], v[44:47], v[220:223], v[180:183]\n" \
"v_accvgpr_read_b32 v236, a114\n" \
"v_accvgpr_read_b32 v237, a115\n" \
"v_mfma_f32_16x16x32_bf16 v[176:179], v[32:35], v[216:219], v[176:179]\n" \
"v_cvt_pk_bf16_f32 v234, v234, v235\n" \
"v_cvt_pk_bf16_f32 v236, v236, v237\n" \
"v_mfma_f32_16x16x32_bf16 v[176:179], v[36:39], v[220:223], v[176:179]\n" \
"s_waitcnt lgkmcnt(1)\n" \
"global_load_lds_dwordx4 v227, s[46:47] offset:3072\n" \
"v_accvgpr_write_b32 a82, v234\n" \
"v_mfma_f32_16x16x32_bf16 v[64:67], v[0:3], v[192:195], v[64:67]\n" \
"s_add_u32 s46, s46, 0x80\n" \
"s_addc_u32 s47, s47, 0\n" \
"ds_read_b128 v[216:219], v230 offset:6144\n" \
"v_mfma_f32_16x16x32_bf16 v[64:67], v[4:7], v[196:199], v[64:67]\n" \
"ds_read_b128 v[220:223], v231 offset:6144\n" \
"v_accvgpr_write_b32 a83, v236\n" \
"v_mfma_f32_16x16x32_bf16 v[68:71], v[8:11], v[192:195], v[68:71]\n" \
"ds_read_b64_tr_b16 v[32:33], v232 offset:1024\n" \
"v_accvgpr_read_b32 v234, a84\n" \
"v_mfma_f32_16x16x32_bf16 v[68:71], v[12:15], v[196:199], v[68:71]\n" \
"buffer_store_dwordx4 a[220:223], v248, s[56:59], s90 offen offset:3072 sc1\n" \
"v_accvgpr_read_b32 v235, a85\n" \
"v_accvgpr_read_b32 v236, a86\n" \
"v_accvgpr_read_b32 v237, a87\n" \
"v_cvt_pk_bf16_f32 v234, v234, v235\n" \
"v_mfma_f32_16x16x32_bf16 v[72:75], v[16:19], v[192:195], v[72:75]\n" \
"ds_read_b64_tr_b16 v[34:35], v232 offset:3072\n" \
"v_cvt_pk_bf16_f32 v236, v236, v237\n" \
"v_mfma_f32_16x16x32_bf16 v[72:75], v[20:23], v[196:199], v[72:75]\n" \
"s_waitcnt lgkmcnt(3)\n" \
"ds_read_b64_tr_b16 v[36:37], v232 offset:9216\n" \
"v_accvgpr_write_b32 a84, v234\n" \
"v_mfma_f32_16x16x32_bf16 v[76:79], v[24:27], v[192:195], v[76:79]\n" \
"ds_read_b64_tr_b16 v[38:39], v232 offset:11264\n" \
"v_accvgpr_write_b32 a85, v236\n" \
"v_mfma_f32_16x16x32_bf16 v[76:79], v[28:31], v[196:199], v[76:79]\n" \
"v_accvgpr_read_b32 v234, a116\n" \
"v_accvgpr_read_b32 v235, a117\n" \
"v_mfma_f32_16x16x32_bf16 v[108:111], v[24:27], v[200:203], v[108:111]\n" \
"ds_read_b64_tr_b16 v[40:41], v233 offset:1024\n" \
"v_accvgpr_read_b32 v236, a118\n" \
"v_mfma_f32_16x16x32_bf16 v[108:111], v[28:31], v[204:207], v[108:111]\n" \
"ds_read_b64_tr_b16 v[42:43], v233 offset:3072\n" \
"v_accvgpr_read_b32 v237, a119\n" \
"v_mfma_f32_16x16x32_bf16 v[104:107], v[16:19], v[200:203], v[104:107]\n" \
"ds_read_b64_tr_b16 v[44:45], v233 offset:9216\n" \
"v_cvt_pk_bf16_f32 v234, v234, v235\n" \
"v_mfma_f32_16x16x32_bf16 v[104:107], v[20:23], v[204:207], v[104:107]\n" \
"v_cvt_pk_bf16_f32 v236, v236, v237\n" \
"v_accvgpr_write_b32 a86, v234\n" \
"v_mfma_f32_16x16x32_bf16 v[100:103], v[8:11], v[200:203], v[100:103]\n" \
"ds_read_b64_tr_b16 v[46:47], v233 offset:11264\n" \
"v_accvgpr_write_b32 a87, v236\n" \
"v_mfma_f32_16x16x32_bf16 v[100:103], v[12:15], v[204:207], v[100:103]\n" \
"ds_read_b64_tr_b16 v[48:49], v232 offset:1536\n" \
"v_accvgpr_read_b32 v234, a88\n" \
"v_mfma_f32_16x16x32_bf16 v[96:99], v[0:3], v[200:203], v[96:99]\n" \
"s_add_u32 m0, s95, 4096\n" \
"ds_read_b64_tr_b16 v[50:51], v232 offset:3584\n" \
"v_accvgpr_read_b32 v235, a89\n" \
"v_mfma_f32_16x16x32_bf16 v[96:99], v[4:7], v[204:207], v[96:99]\n" \
"global_load_lds_dwordx4 v228, s[44:45] offset:-4096\n" \
"v_accvgpr_read_b32 v236, a90\n" \
"v_mfma_f32_16x16x32_bf16 v[128:131], v[0:3], v[208:211], v[128:131]\n" \
"ds_read_b64_tr_b16 v[52:53], v232 offset:9728\n" \
"v_accvgpr_read_b32 v237, a91\n" \
"v_mfma_f32_16x16x32_bf16 v[128:131], v[4:7], v[212:215], v[128:131]\n" \
"ds_read_b64_tr_b16 v[54:55], v232 offset:11776\n" \
"v_cvt_pk_bf16_f32 v234, v234, v235\n" \
"v_mfma_f32_16x16x32_bf16 v[132:135], v[8:11], v[208:211], v[132:135]\n" \
"ds_read_b64_tr_b16 v[56:57], v233 offset:1536\n" \
"v_cvt_pk_bf16_f32 v236, v236, v237\n" \
"v_mfma_f32_16x16x32_bf16 v[132:135], v[12:15], v[212:215], v[132:135]\n" \
"global_load_lds_dwordx4 v229, s[44:45] offset:-3072\n" \
"v_accvgpr_write_b32 a88, v234\n" \
"v_mfma_f32_16x16x32_bf16 v[136:139], v[16:19], v[208:211], v[136:139]\n" \
"s_add_u32 s44, s44, 0x80000\n" \
"s_addc_u32 s45, s45, 0\n" \
"ds_read_b64_tr_b16 v[58:59], v233 offset:3584\n" \
"v_mfma_f32_16x16x32_bf16 v[136:139], v[20:23], v[212:215], v[136:139]\n" \
"ds_read_b64_tr_b16 v[60:61], v233 offset:9728\n" \
"v_accvgpr_write_b32 a89, v236\n" \
"v_mfma_f32_16x16x32_bf16 v[140:143], v[24:27], v[208:211], v[140:143]\n" \
"ds_read_b64_tr_b16 v[62:63], v233 offset:11776\n" \
"v_accvgpr_read_b32 v234, a120\n" \
"v_mfma_f32_16x16x32_bf16 v[140:143], v[28:31], v[212:215], v[140:143]\n" \
"s_waitcnt lgkmcnt(3)\n" \
"global_load_lds_dwordx4 v228, s[42:43] offset:-2048\n" \
"v_accvgpr_read_b32 v235, a121\n" \
"v_mfma_f32_16x16x32_bf16 v[172:175], v[24:27], v[216:219], v[172:175]\n" \
"v_add_u32 v230, 0x8000, v230\n" \
"v_add_u32 v231, 0x8000, v231\n" \
"v_mfma_f32_16x16x32_bf16 v[172:175], v[28:31], v[220:223], v[172:175]\n" \
"v_add_u32 v232, 0x4000, v232\n" \
"v_add_u32 v233, 0x4000, v233\n" \
"v_mfma_f32_16x16x32_bf16 v[168:171], v[16:19], v[216:219], v[168:171]\n" \
"v_accvgpr_read_b32 v236, a122\n" \
"v_accvgpr_read_b32 v237, a123\n" \
"v_mfma_f32_16x16x32_bf16 v[168:171], v[20:23], v[220:223], v[168:171]\n" \
"global_load_lds_dwordx4 v229, s[42:43] offset:-1024\n" \
"v_cvt_pk_bf16_f32 v234, v234, v235\n" \
"v_mfma_f32_16x16x32_bf16 v[164:167], v[8:11], v[216:219], v[164:167]\n" \
"s_add_u32 s42, s42, 0x80000\n" \
"s_addc_u32 s43, s43, 0\n" \
"v_cvt_pk_bf16_f32 v236, v236, v237\n" \
"v_mfma_f32_16x16x32_bf16 v[164:167], v[12:15], v[220:223], v[164:167]\n" \
"s_waitcnt vmcnt(19) lgkmcnt(0)\n" \
"v_accvgpr_write_b32 a90, v234\n" \
"v_accvgpr_write_b32 a91, v236\n" \
"v_mfma_f32_16x16x32_bf16 v[160:163], v[0:3], v[216:219], v[160:163]\n" \
"v_accvgpr_read_b32 v234, a92\n" \
"v_accvgpr_read_b32 v235, a93\n" \
"v_mfma_f32_16x16x32_bf16 v[160:163], v[4:7], v[220:223], v[160:163]\n" \
"s_barrier\n" \
"v_mfma_f32_16x16x32_bf16 v[80:83], v[32:35], v[192:195], v[80:83]\n" \
"ds_read_b64_tr_b16 v[0:1], v232 offset:0\n" \
"v_accvgpr_read_b32 v236, a94\n" \
"v_mfma_f32_16x16x32_bf16 v[80:83], v[36:39], v[196:199], v[80:83]\n" \
"ds_read_b64_tr_b16 v[2:3], v232 offset:2048\n" \
"v_accvgpr_read_b32 v237, a95\n" \
"v_mfma_f32_16x16x32_bf16 v[84:87], v[40:43], v[192:195], v[84:87]\n" \
"s_add_u32 m0, s87, 102400\n" \
"ds_read_b64_tr_b16 v[4:5], v232 offset:8192\n" \
"v_cvt_pk_bf16_f32 v234, v234, v235\n" \
"v_mfma_f32_16x16x32_bf16 v[84:87], v[44:47], v[196:199], v[84:87]\n" \
"global_load_lds_dwordx4 v224, s[48:49] offset:-4096\n" \
"v_cvt_pk_bf16_f32 v236, v236, v237\n" \
"v_mfma_f32_16x16x32_bf16 v[88:91], v[48:51], v[192:195], v[88:91]\n" \
"ds_read_b64_tr_b16 v[6:7], v232 offset:10240\n" \
"v_accvgpr_write_b32 a92, v234\n" \
"v_mfma_f32_16x16x32_bf16 v[88:91], v[52:55], v[196:199], v[88:91]\n" \
"ds_read_b64_tr_b16 v[8:9], v233 offset:0\n" \
"v_accvgpr_write_b32 a93, v236\n" \
"Lpg3308_%=:\n" \
"s_cbranch_execz Lpg5386_%=\n" \
"v_mfma_f32_16x16x32_bf16 v[92:95], v[56:59], v[192:195], v[92:95]\n" \
"ds_read_b64_tr_b16 v[10:11], v233 offset:2048\n" \
"v_accvgpr_read_b32 v234, a124\n" \
"v_mfma_f32_16x16x32_bf16 v[92:95], v[60:63], v[196:199], v[92:95]\n" \
"global_load_lds_dwordx4 v225, s[48:49] offset:-3072\n" \
"v_accvgpr_read_b32 v235, a125\n" \
"v_mfma_f32_16x16x32_bf16 v[124:127], v[56:59], v[200:203], v[124:127]\n" \
"ds_read_b128 v[192:195], v230 offset:0\n" \
"v_accvgpr_read_b32 v236, a126\n" \
"v_mfma_f32_16x16x32_bf16 v[124:127], v[60:63], v[204:207], v[124:127]\n" \
"ds_read_b128 v[196:199], v231 offset:0\n" \
"v_accvgpr_read_b32 v237, a127\n" \
"v_mfma_f32_16x16x32_bf16 v[120:123], v[48:51], v[200:203], v[120:123]\n" \
"ds_read_b64_tr_b16 v[12:13], v233 offset:8192\n" \
"v_cvt_pk_bf16_f32 v234, v234, v235\n" \
"v_mfma_f32_16x16x32_bf16 v[120:123], v[52:55], v[204:207], v[120:123]\n" \
"global_load_lds_dwordx4 v226, s[48:49] offset:-2048\n" \
"v_cvt_pk_bf16_f32 v236, v236, v237\n" \
"v_mfma_f32_16x16x32_bf16 v[116:119], v[40:43], v[200:203], v[116:119]\n" \
"ds_read_b64_tr_b16 v[14:15], v233 offset:10240\n" \
"v_accvgpr_write_b32 a94, v234\n" \
"v_mfma_f32_16x16x32_bf16 v[116:119], v[44:47], v[204:207], v[116:119]\n" \
"ds_read_b64_tr_b16 v[16:17], v232 offset:512\n" \
"v_accvgpr_write_b32 a95, v236\n" \
"v_mfma_f32_16x16x32_bf16 v[112:115], v[32:35], v[200:203], v[112:115]\n" \
"ds_read_b64_tr_b16 v[18:19], v232 offset:2560\n" \
"v_accvgpr_read_b32 v234, a0\n" \
"v_mfma_f32_16x16x32_bf16 v[112:115], v[36:39], v[204:207], v[112:115]\n" \
"global_load_lds_dwordx4 v227, s[48:49] offset:-1024\n" \
"v_accvgpr_read_b32 v235, a1\n" \
"v_mfma_f32_16x16x32_bf16 v[144:147], v[32:35], v[208:211], v[144:147]\n" \
"s_add_u32 s48, s48, 0x80\n" \
"s_addc_u32 s49, s49, 0\n" \
"ds_read_b128 v[200:203], v230 offset:2048\n" \
"v_mfma_f32_16x16x32_bf16 v[144:147], v[36:39], v[212:215], v[144:147]\n" \
"ds_read_b128 v[204:207], v231 offset:2048\n" \
"v_accvgpr_read_b32 v236, a2\n" \
"v_mfma_f32_16x16x32_bf16 v[148:151], v[40:43], v[208:211], v[148:151]\n" \
"ds_read_b64_tr_b16 v[20:21], v232 offset:8704\n" \
"v_accvgpr_read_b32 v237, a3\n" \
"v_mfma_f32_16x16x32_bf16 v[148:151], v[44:47], v[212:215], v[148:151]\n" \
"global_load_lds_dwordx4 v224, s[46:47] offset:0\n" \
"v_mul_f32 v234, s92, v234\n" \
"v_mfma_f32_16x16x32_bf16 v[152:155], v[48:51], v[208:211], v[152:155]\n" \
"ds_read_b64_tr_b16 v[22:23], v232 offset:10752\n" \
"v_mul_f32 v235, s92, v235\n" \
"v_mfma_f32_16x16x32_bf16 v[152:155], v[52:55], v[212:215], v[152:155]\n" \
"ds_read_b64_tr_b16 v[24:25], v233 offset:512\n" \
"v_mul_f32 v236, s92, v236\n" \
"v_mfma_f32_16x16x32_bf16 v[156:159], v[56:59], v[208:211], v[156:159]\n" \
"ds_read_b64_tr_b16 v[26:27], v233 offset:2560\n" \
"v_mul_f32 v237, s92, v237\n" \
"v_mfma_f32_16x16x32_bf16 v[156:159], v[60:63], v[212:215], v[156:159]\n" \
"global_load_lds_dwordx4 v225, s[46:47] offset:1024\n" \
"v_cvt_pk_bf16_f32 v234, v234, v235\n" \
"v_mfma_f32_16x16x32_bf16 v[188:191], v[56:59], v[216:219], v[188:191]\n" \
"ds_read_b128 v[208:211], v230 offset:4096\n" \
"v_cvt_pk_bf16_f32 v236, v236, v237\n" \
"v_mfma_f32_16x16x32_bf16 v[188:191], v[60:63], v[220:223], v[188:191]\n" \
"ds_read_b128 v[212:215], v231 offset:4096\n" \
"v_accvgpr_write_b32 a0, v234\n" \
"v_mfma_f32_16x16x32_bf16 v[184:187], v[48:51], v[216:219], v[184:187]\n" \
"ds_read_b64_tr_b16 v[28:29], v233 offset:8704\n" \
"v_accvgpr_write_b32 a1, v236\n" \
"v_mfma_f32_16x16x32_bf16 v[184:187], v[52:55], v[220:223], v[184:187]\n" \
"global_load_lds_dwordx4 v226, s[46:47] offset:2048\n" \
"v_accvgpr_read_b32 v234, a4\n" \
"v_mfma_f32_16x16x32_bf16 v[180:183], v[40:43], v[216:219], v[180:183]\n" \
"ds_read_b64_tr_b16 v[30:31], v233 offset:10752\n" \
"v_accvgpr_read_b32 v235, a5\n" \
"v_mfma_f32_16x16x32_bf16 v[180:183], v[44:47], v[220:223], v[180:183]\n" \
"v_accvgpr_read_b32 v236, a6\n" \
"v_accvgpr_read_b32 v237, a7\n" \
"v_mfma_f32_16x16x32_bf16 v[176:179], v[32:35], v[216:219], v[176:179]\n" \
"v_mul_f32 v234, s92, v234\n" \
"v_mul_f32 v235, s92, v235\n" \
"v_mfma_f32_16x16x32_bf16 v[176:179], v[36:39], v[220:223], v[176:179]\n" \
"s_waitcnt lgkmcnt(1)\n" \
"global_load_lds_dwordx4 v227, s[46:47] offset:3072\n" \
"v_mul_f32 v236, s92, v236\n" \
"v_mfma_f32_16x16x32_bf16 v[64:67], v[0:3], v[192:195], v[64:67]\n" \
"s_add_u32 s46, s46, 0x80\n" \
"s_addc_u32 s47, s47, 0\n" \
"ds_read_b128 v[216:219], v230 offset:6144\n" \
"v_mfma_f32_16x16x32_bf16 v[64:67], v[4:7], v[196:199], v[64:67]\n" \
"s_add_u32 s90, s88, 139264\n" \
"ds_read_b128 v[220:223], v231 offset:6144\n" \
"v_mul_f32 v237, s92, v237\n" \
"v_mfma_f32_16x16x32_bf16 v[68:71], v[8:11], v[192:195], v[68:71]\n" \
"ds_read_b64_tr_b16 v[32:33], v232 offset:1024\n" \
"v_cvt_pk_bf16_f32 v234, v234, v235\n" \
"v_mfma_f32_16x16x32_bf16 v[68:71], v[12:15], v[196:199], v[68:71]\n" \
"buffer_store_dwordx4 a[128:131], v249, s[56:59], s90 offen offset:0 sc1\n" \
"v_cvt_pk_bf16_f32 v236, v236, v237\n" \
"v_accvgpr_write_b32 a2, v234\n" \
"v_accvgpr_write_b32 a3, v236\n" \
"v_accvgpr_read_b32 v234, a32\n" \
"v_mfma_f32_16x16x32_bf16 v[72:75], v[16:19], v[192:195], v[72:75]\n" \
"ds_read_b64_tr_b16 v[34:35], v232 offset:3072\n" \
"v_accvgpr_read_b32 v235, a33\n" \
"v_mfma_f32_16x16x32_bf16 v[72:75], v[20:23], v[196:199], v[72:75]\n" \
"s_waitcnt lgkmcnt(3)\n" \
"ds_read_b64_tr_b16 v[36:37], v232 offset:9216\n" \
"v_accvgpr_read_b32 v236, a34\n" \
"v_mfma_f32_16x16x32_bf16 v[76:79], v[24:27], v[192:195], v[76:79]\n" \
"ds_read_b64_tr_b16 v[38:39], v232 offset:11264\n" \
"v_accvgpr_read_b32 v237, a35\n" \
"v_mfma_f32_16x16x32_bf16 v[76:79], v[28:31], v[196:199], v[76:79]\n" \
"v_mul_f32 v234, s92, v234\n" \
"v_mul_f32 v235, s92, v235\n" \
"v_mfma_f32_16x16x32_bf16 v[108:111], v[24:27], v[200:203], v[108:111]\n" \
"ds_read_b64_tr_b16 v[40:41], v233 offset:1024\n" \
"v_mul_f32 v236, s92, v236\n" \
"v_mfma_f32_16x16x32_bf16 v[108:111], v[28:31], v[204:207], v[108:111]\n" \
"ds_read_b64_tr_b16 v[42:43], v233 offset:3072\n" \
"v_mul_f32 v237, s92, v237\n" \
"v_mfma_f32_16x16x32_bf16 v[104:107], v[16:19], v[200:203], v[104:107]\n" \
"ds_read_b64_tr_b16 v[44:45], v233 offset:9216\n" \
"v_cvt_pk_bf16_f32 v234, v234, v235\n" \
"v_mfma_f32_16x16x32_bf16 v[104:107], v[20:23], v[204:207], v[104:107]\n" \
"v_cvt_pk_bf16_f32 v236, v236, v237\n" \
"v_accvgpr_write_b32 a32, v234\n" \
"v_mfma_f32_16x16x32_bf16 v[100:103], v[8:11], v[200:203], v[100:103]\n" \
"ds_read_b64_tr_b16 v[46:47], v233 offset:11264\n" \
"v_accvgpr_write_b32 a33, v236\n" \
"v_mfma_f32_16x16x32_bf16 v[100:103], v[12:15], v[204:207], v[100:103]\n" \
"ds_read_b64_tr_b16 v[48:49], v232 offset:1536\n" \
"v_accvgpr_read_b32 v234, a36\n" \
"v_mfma_f32_16x16x32_bf16 v[96:99], v[0:3], v[200:203], v[96:99]\n" \
"s_add_u32 m0, s95, 20480\n" \
"ds_read_b64_tr_b16 v[50:51], v232 offset:3584\n" \
"v_accvgpr_read_b32 v235, a37\n" \
"v_mfma_f32_16x16x32_bf16 v[96:99], v[4:7], v[204:207], v[96:99]\n" \
"global_load_lds_dwordx4 v228, s[44:45] offset:-4096\n" \
"v_accvgpr_read_b32 v236, a38\n" \
"v_mfma_f32_16x16x32_bf16 v[128:131], v[0:3], v[208:211], v[128:131]\n" \
"ds_read_b64_tr_b16 v[52:53], v232 offset:9728\n" \
"v_accvgpr_read_b32 v237, a39\n" \
"v_mfma_f32_16x16x32_bf16 v[128:131], v[4:7], v[212:215], v[128:131]\n" \
"ds_read_b64_tr_b16 v[54:55], v232 offset:11776\n" \
"v_mul_f32 v234, s92, v234\n" \
"v_mfma_f32_16x16x32_bf16 v[132:135], v[8:11], v[208:211], v[132:135]\n" \
"ds_read_b64_tr_b16 v[56:57], v233 offset:1536\n" \
"v_mul_f32 v235, s92, v235\n" \
"v_mfma_f32_16x16x32_bf16 v[132:135], v[12:15], v[212:215], v[132:135]\n" \
"global_load_lds_dwordx4 v229, s[44:45] offset:-3072\n" \
"v_mul_f32 v236, s92, v236\n" \
"v_mfma_f32_16x16x32_bf16 v[136:139], v[16:19], v[208:211], v[136:139]\n" \
"s_add_u32 s44, s44, 0x80000\n" \
"s_addc_u32 s45, s45, 0\n" \
"ds_read_b64_tr_b16 v[58:59], v233 offset:3584\n" \
"v_mfma_f32_16x16x32_bf16 v[136:139], v[20:23], v[212:215], v[136:139]\n" \
"ds_read_b64_tr_b16 v[60:61], v233 offset:9728\n" \
"v_mul_f32 v237, s92, v237\n" \
"v_mfma_f32_16x16x32_bf16 v[140:143], v[24:27], v[208:211], v[140:143]\n" \
"ds_read_b64_tr_b16 v[62:63], v233 offset:11776\n" \
"v_cvt_pk_bf16_f32 v234, v234, v235\n" \
"v_mfma_f32_16x16x32_bf16 v[140:143], v[28:31], v[212:215], v[140:143]\n" \
"s_waitcnt lgkmcnt(3)\n" \
"global_load_lds_dwordx4 v228, s[42:43] offset:-2048\n" \
"v_cvt_pk_bf16_f32 v236, v236, v237\n" \
"v_mfma_f32_16x16x32_bf16 v[172:175], v[24:27], v[216:219], v[172:175]\n" \
"v_add_u32 v230, 0xffff0000, v230\n" \
"v_add_u32 v231, 0xffff0000, v231\n" \
"v_mfma_f32_16x16x32_bf16 v[172:175], v[28:31], v[220:223], v[172:175]\n" \
"v_add_u32 v232, 0x4000, v232\n" \
"v_add_u32 v233, 0x4000, v233\n" \
"v_mfma_f32_16x16x32_bf16 v[168:171], v[16:19], v[216:219], v[168:171]\n" \
"v_accvgpr_write_b32 a34, v234\n" \
"v_accvgpr_write_b32 a35, v236\n" \
"v_mfma_f32_16x16x32_bf16 v[168:171], v[20:23], v[220:223], v[168:171]\n" \
"global_load_lds_dwordx4 v229, s[42:43] offset:-1024\n" \
"v_accvgpr_read_b32 v234, a8\n" \
"v_mfma_f32_16x16x32_bf16 v[164:167], v[8:11], v[216:219], v[164:167]\n" \
"s_add_u32 s42, s42, 0x80000\n" \
"s_addc_u32 s43, s43, 0\n" \
"v_accvgpr_read_b32 v235, a9\n" \
"v_mfma_f32_16x16x32_bf16 v[164:167], v[12:15], v[220:223], v[164:167]\n" \
"s_waitcnt vmcnt(18) lgkmcnt(0)\n" \
"v_accvgpr_read_b32 v236, a10\n" \
"v_accvgpr_read_b32 v237, a11\n" \
"v_mfma_f32_16x16x32_bf16 v[160:163], v[0:3], v[216:219], v[160:163]\n" \
"v_mul_f32 v234, s92, v234\n" \
"v_mul_f32 v235, s92, v235\n" \
"v_mfma_f32_16x16x32_bf16 v[160:163], v[4:7], v[220:223], v[160:163]\n" \
"s_barrier\n" \
"v_mfma_f32_16x16x32_bf16 v[80:83], v[32:35], v[192:195], v[80:83]\n" \
"ds_read_b64_tr_b16 v[0:1], v232 offset:0\n" \
"v_mul_f32 v236, s92, v236\n" \
"v_mfma_f32_16x16x32_bf16 v[80:83], v[36:39], v[196:199], v[80:83]\n" \
"ds_read_b64_tr_b16 v[2:3], v232 offset:2048\n" \
"v_mul_f32 v237, s92, v237\n" \
"v_mfma_f32_16x16x32_bf16 v[84:87], v[40:43], v[192:195], v[84:87]\n" \
"s_add_u32 m0, s87, 135168\n" \
"ds_read_b64_tr_b16 v[4:5], v232 offset:8192\n" \
"v_cvt_pk_bf16_f32 v234, v234, v235\n" \
"v_mfma_f32_16x16x32_bf16 v[84:87], v[44:47], v[196:199], v[84:87]\n" \
"global_load_lds_dwordx4 v224, s[48:49] offset:-4096\n" \
"v_cvt_pk_bf16_f32 v236, v236, v237\n" \
"v_mfma_f32_16x16x32_bf16 v[88:91], v[48:51], v[192:195], v[88:91]\n" \
"ds_read_b64_tr_b16 v[6:7], v232 offset:10240\n" \
"v_accvgpr_write_b32 a8, v234\n" \
"v_mfma_f32_16x16x32_bf16 v[88:91], v[52:55], v[196:199], v[88:91]\n" \
"ds_read_b64_tr_b16 v[8:9], v233 offset:0\n" \
"v_accvgpr_write_b32 a9, v236\n" \
"v_mfma_f32_16x16x32_bf16 v[92:95], v[56:59], v[192:195], v[92:95]\n" \
"ds_read_b64_tr_b16 v[10:11], v233 offset:2048\n" \
"v_accvgpr_read_b32 v234, a12\n" \
"v_mfma_f32_16x16x32_bf16 v[92:95], v[60:63], v[196:199], v[92:95]\n" \
"global_load_lds_dwordx4 v225, s[48:49] offset:-3072\n" \
"v_accvgpr_read_b32 v235, a13\n" \
"v_mfma_f32_16x16x32_bf16 v[124:127], v[56:59], v[200:203], v[124:127]\n" \
"ds_read_b128 v[192:195], v230 offset:0\n" \
"v_accvgpr_read_b32 v236, a14\n" \
"v_mfma_f32_16x16x32_bf16 v[124:127], v[60:63], v[204:207], v[124:127]\n" \
"ds_read_b128 v[196:199], v231 offset:0\n" \
"v_accvgpr_read_b32 v237, a15\n" \
"v_mfma_f32_16x16x32_bf16 v[120:123], v[48:51], v[200:203], v[120:123]\n" \
"ds_read_b64_tr_b16 v[12:13], v233 offset:8192\n" \
"v_mul_f32 v234, s92, v234\n" \
"v_mfma_f32_16x16x32_bf16 v[120:123], v[52:55], v[204:207], v[120:123]\n" \
"global_load_lds_dwordx4 v226, s[48:49] offset:-2048\n" \
"v_mul_f32 v235, s92, v235\n" \
"v_mfma_f32_16x16x32_bf16 v[116:119], v[40:43], v[200:203], v[116:119]\n" \
"ds_read_b64_tr_b16 v[14:15], v233 offset:10240\n" \
"v_mul_f32 v236, s92, v236\n" \
"v_mfma_f32_16x16x32_bf16 v[116:119], v[44:47], v[204:207], v[116:119]\n" \
"ds_read_b64_tr_b16 v[16:17], v232 offset:512\n" \
"v_mul_f32 v237, s92, v237\n" \
"v_mfma_f32_16x16x32_bf16 v[112:115], v[32:35], v[200:203], v[112:115]\n" \
"ds_read_b64_tr_b16 v[18:19], v232 offset:2560\n" \
"v_cvt_pk_bf16_f32 v234, v234, v235\n" \
"v_mfma_f32_16x16x32_bf16 v[112:115], v[36:39], v[204:207], v[112:115]\n" \
"global_load_lds_dwordx4 v227, s[48:49] offset:-1024\n" \
"v_cvt_pk_bf16_f32 v236, v236, v237\n" \
"v_mfma_f32_16x16x32_bf16 v[144:147], v[32:35], v[208:211], v[144:147]\n" \
"s_add_u32 s48, s48, 0x80\n" \
"s_addc_u32 s49, s49, 0\n" \
"ds_read_b128 v[200:203], v230 offset:2048\n" \
"v_mfma_f32_16x16x32_bf16 v[144:147], v[36:39], v[212:215], v[144:147]\n" \
"ds_read_b128 v[204:207], v231 offset:2048\n" \
"v_accvgpr_write_b32 a10, v234\n" \
"v_mfma_f32_16x16x32_bf16 v[148:151], v[40:43], v[208:211], v[148:151]\n" \
"ds_read_b64_tr_b16 v[20:21], v232 offset:8704\n" \
"v_accvgpr_write_b32 a11, v236\n" \
"v_mfma_f32_16x16x32_bf16 v[148:151], v[44:47], v[212:215], v[148:151]\n" \
"global_load_lds_dwordx4 v224, s[46:47] offset:0\n" \
"v_accvgpr_read_b32 v234, a40\n" \
"v_mfma_f32_16x16x32_bf16 v[152:155], v[48:51], v[208:211], v[152:155]\n" \
"ds_read_b64_tr_b16 v[22:23], v232 offset:10752\n" \
"v_accvgpr_read_b32 v235, a41\n" \
"v_mfma_f32_16x16x32_bf16 v[152:155], v[52:55], v[212:215], v[152:155]\n" \
"ds_read_b64_tr_b16 v[24:25], v233 offset:512\n" \
"v_accvgpr_read_b32 v236, a42\n" \
"v_mfma_f32_16x16x32_bf16 v[156:159], v[56:59], v[208:211], v[156:159]\n" \
"ds_read_b64_tr_b16 v[26:27], v233 offset:2560\n" \
"v_accvgpr_read_b32 v237, a43\n" \
"v_mfma_f32_16x16x32_bf16 v[156:159], v[60:63], v[212:215], v[156:159]\n" \
"global_load_lds_dwordx4 v225, s[46:47] offset:1024\n" \
"v_mul_f32 v234, s92, v234\n" \
"v_mfma_f32_16x16x32_bf16 v[188:191], v[56:59], v[216:219], v[188:191]\n" \
"ds_read_b128 v[208:211], v230 offset:4096\n" \
"v_mul_f32 v235, s92, v235\n" \
"v_mfma_f32_16x16x32_bf16 v[188:191], v[60:63], v[220:223], v[188:191]\n" \
"ds_read_b128 v[212:215], v231 offset:4096\n" \
"v_mul_f32 v236, s92, v236\n" \
"v_mfma_f32_16x16x32_bf16 v[184:187], v[48:51], v[216:219], v[184:187]\n" \
"ds_read_b64_tr_b16 v[28:29], v233 offset:8704\n" \
"v_mul_f32 v237, s92, v237\n" \
"v_mfma_f32_16x16x32_bf16 v[184:187], v[52:55], v[220:223], v[184:187]\n" \
"global_load_lds_dwordx4 v226, s[46:47] offset:2048\n" \
"v_cvt_pk_bf16_f32 v234, v234, v235\n" \
"v_mfma_f32_16x16x32_bf16 v[180:183], v[40:43], v[216:219], v[180:183]\n" \
"ds_read_b64_tr_b16 v[30:31], v233 offset:10752\n" \
"v_cvt_pk_bf16_f32 v236, v236, v237\n" \
"v_mfma_f32_16x16x32_bf16 v[180:183], v[44:47], v[220:223], v[180:183]\n" \
"v_accvgpr_write_b32 a40, v234\n" \
"v_accvgpr_write_b32 a41, v236\n" \
"v_mfma_f32_16x16x32_bf16 v[176:179], v[32:35], v[216:219], v[176:179]\n" \
"v_accvgpr_read_b32 v234, a44\n" \
"v_accvgpr_read_b32 v235, a45\n" \
"v_mfma_f32_16x16x32_bf16 v[176:179], v[36:39], v[220:223], v[176:179]\n" \
"s_waitcnt lgkmcnt(1)\n" \
"global_load_lds_dwordx4 v227, s[46:47] offset:3072\n" \
"v_accvgpr_read_b32 v236, a46\n" \
"v_mfma_f32_16x16x32_bf16 v[64:67], v[0:3], v[192:195], v[64:67]\n" \
"s_add_u32 s46, s46, 0x80\n" \
"s_addc_u32 s47, s47, 0\n" \
"ds_read_b128 v[216:219], v230 offset:6144\n" \
"v_mfma_f32_16x16x32_bf16 v[64:67], v[4:7], v[196:199], v[64:67]\n" \
"ds_read_b128 v[220:223], v231 offset:6144\n" \
"v_accvgpr_read_b32 v237, a47\n" \
"v_mfma_f32_16x16x32_bf16 v[68:71], v[8:11], v[192:195], v[68:71]\n" \
"ds_read_b64_tr_b16 v[32:33], v232 offset:1024\n" \
"v_mul_f32 v234, s92, v234\n" \
"v_mfma_f32_16x16x32_bf16 v[68:71], v[12:15], v[196:199], v[68:71]\n" \
"buffer_store_dwordx4 a[160:163], v249, s[56:59], s90 offen offset:256 sc1\n" \
"v_mul_f32 v235, s92, v235\n" \
"v_mul_f32 v236, s92, v236\n" \
"v_mul_f32 v237, s92, v237\n" \
"v_cvt_pk_bf16_f32 v234, v234, v235\n" \
"v_mfma_f32_16x16x32_bf16 v[72:75], v[16:19], v[192:195], v[72:75]\n" \
"ds_read_b64_tr_b16 v[34:35], v232 offset:3072\n" \
"v_cvt_pk_bf16_f32 v236, v236, v237\n" \
"v_mfma_f32_16x16x32_bf16 v[72:75], v[20:23], v[196:199], v[72:75]\n" \
"s_waitcnt lgkmcnt(3)\n" \
"ds_read_b64_tr_b16 v[36:37], v232 offset:9216\n" \
"v_accvgpr_write_b32 a42, v234\n" \
"v_mfma_f32_16x16x32_bf16 v[76:79], v[24:27], v[192:195], v[76:79]\n" \
"ds_read_b64_tr_b16 v[38:39], v232 offset:11264\n" \
"v_accvgpr_write_b32 a43, v236\n" \
"v_mfma_f32_16x16x32_bf16 v[76:79], v[28:31], v[196:199], v[76:79]\n" \
"v_accvgpr_read_b32 v234, a16\n" \
"v_accvgpr_read_b32 v235, a17\n" \
"v_mfma_f32_16x16x32_bf16 v[108:111], v[24:27], v[200:203], v[108:111]\n" \
"ds_read_b64_tr_b16 v[40:41], v233 offset:1024\n" \
"v_accvgpr_read_b32 v236, a18\n" \
"v_mfma_f32_16x16x32_bf16 v[108:111], v[28:31], v[204:207], v[108:111]\n" \
"ds_read_b64_tr_b16 v[42:43], v233 offset:3072\n" \
"v_accvgpr_read_b32 v237, a19\n" \
"v_mfma_f32_16x16x32_bf16 v[104:107], v[16:19], v[200:203], v[104:107]\n" \
"ds_read_b64_tr_b16 v[44:45], v233 offset:9216\n" \
"v_cvt_pk_bf16_f32 v234, v234, v235\n" \
"v_mfma_f32_16x16x32_bf16 v[104:107], v[20:23], v[204:207], v[104:107]\n" \
"v_cvt_pk_bf16_f32 v236, v236, v237\n" \
"v_accvgpr_write_b32 a16, v234\n" \
"v_mfma_f32_16x16x32_bf16 v[100:103], v[8:11], v[200:203], v[100:103]\n" \
"ds_read_b64_tr_b16 v[46:47], v233 offset:11264\n" \
"v_accvgpr_write_b32 a17, v236\n" \
"v_mfma_f32_16x16x32_bf16 v[100:103], v[12:15], v[204:207], v[100:103]\n" \
"ds_read_b64_tr_b16 v[48:49], v232 offset:1536\n" \
"v_accvgpr_read_b32 v234, a48\n" \
"v_mfma_f32_16x16x32_bf16 v[96:99], v[0:3], v[200:203], v[96:99]\n" \
"s_add_u32 m0, s95, 36864\n" \
"ds_read_b64_tr_b16 v[50:51], v232 offset:3584\n" \
"v_accvgpr_read_b32 v235, a49\n" \
"v_mfma_f32_16x16x32_bf16 v[96:99], v[4:7], v[204:207], v[96:99]\n" \
"global_load_lds_dwordx4 v228, s[44:45] offset:-4096\n" \
"v_accvgpr_read_b32 v236, a50\n" \
"v_mfma_f32_16x16x32_bf16 v[128:131], v[0:3], v[208:211], v[128:131]\n" \
"ds_read_b64_tr_b16 v[52:53], v232 offset:9728\n" \
"v_accvgpr_read_b32 v237, a51\n" \
"v_mfma_f32_16x16x32_bf16 v[128:131], v[4:7], v[212:215], v[128:131]\n" \
"ds_read_b64_tr_b16 v[54:55], v232 offset:11776\n" \
"v_cvt_pk_bf16_f32 v234, v234, v235\n" \
"v_mfma_f32_16x16x32_bf16 v[132:135], v[8:11], v[208:211], v[132:135]\n" \
"ds_read_b64_tr_b16 v[56:57], v233 offset:1536\n" \
"v_cvt_pk_bf16_f32 v236, v236, v237\n" \
"v_mfma_f32_16x16x32_bf16 v[132:135], v[12:15], v[212:215], v[132:135]\n" \
"global_load_lds_dwordx4 v229, s[44:45] offset:-3072\n" \
"v_accvgpr_write_b32 a18, v234\n" \
"v_mfma_f32_16x16x32_bf16 v[136:139], v[16:19], v[208:211], v[136:139]\n" \
"s_add_u32 s44, s44, 0x80000\n" \
"s_addc_u32 s45, s45, 0\n" \
"ds_read_b64_tr_b16 v[58:59], v233 offset:3584\n" \
"v_mfma_f32_16x16x32_bf16 v[136:139], v[20:23], v[212:215], v[136:139]\n" \
"ds_read_b64_tr_b16 v[60:61], v233 offset:9728\n" \
"v_accvgpr_write_b32 a19, v236\n" \
"v_mfma_f32_16x16x32_bf16 v[140:143], v[24:27], v[208:211], v[140:143]\n" \
"ds_read_b64_tr_b16 v[62:63], v233 offset:11776\n" \
"v_accvgpr_read_b32 v234, a20\n" \
"v_mfma_f32_16x16x32_bf16 v[140:143], v[28:31], v[212:215], v[140:143]\n" \
"s_waitcnt lgkmcnt(3)\n" \
"global_load_lds_dwordx4 v228, s[42:43] offset:-2048\n" \
"v_accvgpr_read_b32 v235, a21\n" \
"v_mfma_f32_16x16x32_bf16 v[172:175], v[24:27], v[216:219], v[172:175]\n" \
"v_add_u32 v230, 0x8000, v230\n" \
"v_add_u32 v231, 0x8000, v231\n" \
"v_mfma_f32_16x16x32_bf16 v[172:175], v[28:31], v[220:223], v[172:175]\n" \
"v_add_u32 v232, 0xffff4000, v232\n" \
"v_add_u32 v233, 0xffff4000, v233\n" \
"v_mfma_f32_16x16x32_bf16 v[168:171], v[16:19], v[216:219], v[168:171]\n" \
"v_accvgpr_read_b32 v236, a22\n" \
"v_accvgpr_read_b32 v237, a23\n" \
"v_mfma_f32_16x16x32_bf16 v[168:171], v[20:23], v[220:223], v[168:171]\n" \
"global_load_lds_dwordx4 v229, s[42:43] offset:-1024\n" \
"v_cvt_pk_bf16_f32 v234, v234, v235\n" \
"v_mfma_f32_16x16x32_bf16 v[164:167], v[8:11], v[216:219], v[164:167]\n" \
"s_add_u32 s42, s42, 0x80000\n" \
"s_addc_u32 s43, s43, 0\n" \
"v_cvt_pk_bf16_f32 v236, v236, v237\n" \
"v_mfma_f32_16x16x32_bf16 v[164:167], v[12:15], v[220:223], v[164:167]\n" \
"s_waitcnt vmcnt(18) lgkmcnt(0)\n" \
"v_accvgpr_write_b32 a20, v234\n" \
"v_accvgpr_write_b32 a21, v236\n" \
"v_mfma_f32_16x16x32_bf16 v[160:163], v[0:3], v[216:219], v[160:163]\n" \
"v_accvgpr_read_b32 v234, a52\n" \
"v_accvgpr_read_b32 v235, a53\n" \
"v_mfma_f32_16x16x32_bf16 v[160:163], v[4:7], v[220:223], v[160:163]\n" \
"s_barrier\n" \
"v_mfma_f32_16x16x32_bf16 v[80:83], v[32:35], v[192:195], v[80:83]\n" \
"ds_read_b64_tr_b16 v[0:1], v232 offset:0\n" \
"v_accvgpr_read_b32 v236, a54\n" \
"v_mfma_f32_16x16x32_bf16 v[80:83], v[36:39], v[196:199], v[80:83]\n" \
"ds_read_b64_tr_b16 v[2:3], v232 offset:2048\n" \
"v_accvgpr_read_b32 v237, a55\n" \
"v_mfma_f32_16x16x32_bf16 v[84:87], v[40:43], v[192:195], v[84:87]\n" \
"s_add_u32 m0, s87, 69632\n" \
"ds_read_b64_tr_b16 v[4:5], v232 offset:8192\n" \
"v_cvt_pk_bf16_f32 v234, v234, v235\n" \
"v_mfma_f32_16x16x32_bf16 v[84:87], v[44:47], v[196:199], v[84:87]\n" \
"global_load_lds_dwordx4 v224, s[48:49] offset:-4096\n" \
"v_cvt_pk_bf16_f32 v236, v236, v237\n" \
"v_mfma_f32_16x16x32_bf16 v[88:91], v[48:51], v[192:195], v[88:91]\n" \
"ds_read_b64_tr_b16 v[6:7], v232 offset:10240\n" \
"v_accvgpr_write_b32 a22, v234\n" \
"v_mfma_f32_16x16x32_bf16 v[88:91], v[52:55], v[196:199], v[88:91]\n" \
"ds_read_b64_tr_b16 v[8:9], v233 offset:0\n" \
"v_accvgpr_write_b32 a23, v236\n" \
"v_mfma_f32_16x16x32_bf16 v[92:95], v[56:59], v[192:195], v[92:95]\n" \
"ds_read_b64_tr_b16 v[10:11], v233 offset:2048\n" \
"v_accvgpr_read_b32 v234, a24\n" \
"v_mfma_f32_16x16x32_bf16 v[92:95], v[60:63], v[196:199], v[92:95]\n" \
"global_load_lds_dwordx4 v225, s[48:49] offset:-3072\n" \
"v_accvgpr_read_b32 v235, a25\n" \
"v_mfma_f32_16x16x32_bf16 v[124:127], v[56:59], v[200:203], v[124:127]\n" \
"ds_read_b128 v[192:195], v230 offset:0\n" \
"v_accvgpr_read_b32 v236, a26\n" \
"v_mfma_f32_16x16x32_bf16 v[124:127], v[60:63], v[204:207], v[124:127]\n" \
"ds_read_b128 v[196:199], v231 offset:0\n" \
"v_accvgpr_read_b32 v237, a27\n" \
"v_mfma_f32_16x16x32_bf16 v[120:123], v[48:51], v[200:203], v[120:123]\n" \
"ds_read_b64_tr_b16 v[12:13], v233 offset:8192\n" \
"v_cvt_pk_bf16_f32 v234, v234, v235\n" \
"v_mfma_f32_16x16x32_bf16 v[120:123], v[52:55], v[204:207], v[120:123]\n" \
"global_load_lds_dwordx4 v226, s[48:49] offset:-2048\n" \
"v_cvt_pk_bf16_f32 v236, v236, v237\n" \
"v_mfma_f32_16x16x32_bf16 v[116:119], v[40:43], v[200:203], v[116:119]\n" \
"ds_read_b64_tr_b16 v[14:15], v233 offset:10240\n" \
"v_accvgpr_write_b32 a24, v234\n" \
"v_mfma_f32_16x16x32_bf16 v[116:119], v[44:47], v[204:207], v[116:119]\n" \
"ds_read_b64_tr_b16 v[16:17], v232 offset:512\n" \
"v_accvgpr_write_b32 a25, v236\n" \
"v_mfma_f32_16x16x32_bf16 v[112:115], v[32:35], v[200:203], v[112:115]\n" \
"ds_read_b64_tr_b16 v[18:19], v232 offset:2560\n" \
"v_accvgpr_read_b32 v234, a56\n" \
"v_mfma_f32_16x16x32_bf16 v[112:115], v[36:39], v[204:207], v[112:115]\n" \
"global_load_lds_dwordx4 v227, s[48:49] offset:-1024\n" \
"v_accvgpr_read_b32 v235, a57\n" \
"v_mfma_f32_16x16x32_bf16 v[144:147], v[32:35], v[208:211], v[144:147]\n" \
"s_add_u32 s48, s48, 0x80\n" \
"s_addc_u32 s49, s49, 0\n" \
"ds_read_b128 v[200:203], v230 offset:2048\n" \
"v_mfma_f32_16x16x32_bf16 v[144:147], v[36:39], v[212:215], v[144:147]\n" \
"ds_read_b128 v[204:207], v231 offset:2048\n" \
"v_accvgpr_read_b32 v236, a58\n" \
"v_mfma_f32_16x16x32_bf16 v[148:151], v[40:43], v[208:211], v[148:151]\n" \
"ds_read_b64_tr_b16 v[20:21], v232 offset:8704\n" \
"v_accvgpr_read_b32 v237, a59\n" \
"v_mfma_f32_16x16x32_bf16 v[148:151], v[44:47], v[212:215], v[148:151]\n" \
"global_load_lds_dwordx4 v224, s[46:47] offset:0\n" \
"v_cvt_pk_bf16_f32 v234, v234, v235\n" \
"v_mfma_f32_16x16x32_bf16 v[152:155], v[48:51], v[208:211], v[152:155]\n" \
"ds_read_b64_tr_b16 v[22:23], v232 offset:10752\n" \
"v_cvt_pk_bf16_f32 v236, v236, v237\n" \
"v_mfma_f32_16x16x32_bf16 v[152:155], v[52:55], v[212:215], v[152:155]\n" \
"ds_read_b64_tr_b16 v[24:25], v233 offset:512\n" \
"v_accvgpr_write_b32 a26, v234\n" \
"v_mfma_f32_16x16x32_bf16 v[156:159], v[56:59], v[208:211], v[156:159]\n" \
"ds_read_b64_tr_b16 v[26:27], v233 offset:2560\n" \
"v_accvgpr_write_b32 a27, v236\n" \
"v_mfma_f32_16x16x32_bf16 v[156:159], v[60:63], v[212:215], v[156:159]\n" \
"global_load_lds_dwordx4 v225, s[46:47] offset:1024\n" \
"v_accvgpr_read_b32 v234, a28\n" \
"v_mfma_f32_16x16x32_bf16 v[188:191], v[56:59], v[216:219], v[188:191]\n" \
"ds_read_b128 v[208:211], v230 offset:4096\n" \
"v_accvgpr_read_b32 v235, a29\n" \
"v_mfma_f32_16x16x32_bf16 v[188:191], v[60:63], v[220:223], v[188:191]\n" \
"ds_read_b128 v[212:215], v231 offset:4096\n" \
"v_accvgpr_read_b32 v236, a30\n" \
"v_mfma_f32_16x16x32_bf16 v[184:187], v[48:51], v[216:219], v[184:187]\n" \
"ds_read_b64_tr_b16 v[28:29], v233 offset:8704\n" \
"v_accvgpr_read_b32 v237, a31\n" \
"v_mfma_f32_16x16x32_bf16 v[184:187], v[52:55], v[220:223], v[184:187]\n" \
"global_load_lds_dwordx4 v226, s[46:47] offset:2048\n" \
"v_cvt_pk_bf16_f32 v234, v234, v235\n" \
"v_mfma_f32_16x16x32_bf16 v[180:183], v[40:43], v[216:219], v[180:183]\n" \
"ds_read_b64_tr_b16 v[30:31], v233 offset:10752\n" \
"v_cvt_pk_bf16_f32 v236, v236, v237\n" \
"v_mfma_f32_16x16x32_bf16 v[180:183], v[44:47], v[220:223], v[180:183]\n" \
"v_accvgpr_write_b32 a28, v234\n" \
"v_accvgpr_write_b32 a29, v236\n" \
"v_mfma_f32_16x16x32_bf16 v[176:179], v[32:35], v[216:219], v[176:179]\n" \
"v_accvgpr_read_b32 v234, a60\n" \
"v_accvgpr_read_b32 v235, a61\n" \
"v_mfma_f32_16x16x32_bf16 v[176:179], v[36:39], v[220:223], v[176:179]\n" \
"s_waitcnt lgkmcnt(1)\n" \
"global_load_lds_dwordx4 v227, s[46:47] offset:3072\n" \
"v_accvgpr_read_b32 v236, a62\n" \
"v_mfma_f32_16x16x32_bf16 v[64:67], v[0:3], v[192:195], v[64:67]\n" \
"s_add_u32 s46, s46, 0x80\n" \
"s_addc_u32 s47, s47, 0\n" \
"ds_read_b128 v[216:219], v230 offset:6144\n" \
"v_mfma_f32_16x16x32_bf16 v[64:67], v[4:7], v[196:199], v[64:67]\n" \
"ds_read_b128 v[220:223], v231 offset:6144\n" \
"v_accvgpr_read_b32 v237, a63\n" \
"v_mfma_f32_16x16x32_bf16 v[68:71], v[8:11], v[192:195], v[68:71]\n" \
"ds_read_b64_tr_b16 v[32:33], v232 offset:1024\n" \
"v_cvt_pk_bf16_f32 v234, v234, v235\n" \
"v_mfma_f32_16x16x32_bf16 v[68:71], v[12:15], v[196:199], v[68:71]\n" \
"buffer_store_dwordx4 a[136:139], v249, s[56:59], s90 offen offset:2048 sc1\n" \
"v_cvt_pk_bf16_f32 v236, v236, v237\n" \
"v_accvgpr_write_b32 a30, v234\n" \
"v_accvgpr_write_b32 a31, v236\n" \
"v_mfma_f32_16x16x32_bf16 v[72:75], v[16:19], v[192:195], v[72:75]\n" \
"ds_read_b64_tr_b16 v[34:35], v232 offset:3072\n" \
"v_mfma_f32_16x16x32_bf16 v[72:75], v[20:23], v[196:199], v[72:75]\n" \
"s_waitcnt lgkmcnt(3)\n" \
"ds_read_b64_tr_b16 v[36:37], v232 offset:9216\n" \
"v_mfma_f32_16x16x32_bf16 v[76:79], v[24:27], v[192:195], v[76:79]\n" \
"ds_read_b64_tr_b16 v[38:39], v232 offset:11264\n" \
"v_mfma_f32_16x16x32_bf16 v[76:79], v[28:31], v[196:199], v[76:79]\n" \
"buffer_store_dwordx4 a[168:171], v249, s[56:59], s90 offen offset:2304 sc1\n" \
"v_mfma_f32_16x16x32_bf16 v[108:111], v[24:27], v[200:203], v[108:111]\n" \
"s_lshl_b32 s70, s63, 2\n" \
"ds_read_b64_tr_b16 v[40:41], v233 offset:1024\n" \
"v_mfma_f32_16x16x32_bf16 v[108:111], v[28:31], v[204:207], v[108:111]\n" \
"s_add_u32 s70, s70, s91\n" \
"ds_read_b64_tr_b16 v[42:43], v233 offset:3072\n" \
"v_mov_b32 v238, s60\n" \
"v_mfma_f32_16x16x32_bf16 v[104:107], v[16:19], v[200:203], v[104:107]\n" \
"ds_read_b64_tr_b16 v[44:45], v233 offset:9216\n" \
"v_lshlrev_b32 v239, 28, s61\n" \
"v_mfma_f32_16x16x32_bf16 v[104:107], v[20:23], v[204:207], v[104:107]\n" \
"buffer_store_dword v238, v239, s[56:59], s70 offen sc1\n" \
"v_mfma_f32_16x16x32_bf16 v[100:103], v[8:11], v[200:203], v[100:103]\n" \
"ds_read_b64_tr_b16 v[46:47], v233 offset:11264\n" \
"v_mfma_f32_16x16x32_bf16 v[100:103], v[12:15], v[204:207], v[100:103]\n" \
"ds_read_b64_tr_b16 v[48:49], v232 offset:1536\n" \
"v_mfma_f32_16x16x32_bf16 v[96:99], v[0:3], v[200:203], v[96:99]\n" \
"s_add_u32 m0, s95, 53248\n" \
"ds_read_b64_tr_b16 v[50:51], v232 offset:3584\n" \
"v_mfma_f32_16x16x32_bf16 v[96:99], v[4:7], v[204:207], v[96:99]\n" \
"global_load_lds_dwordx4 v228, s[44:45] offset:-4096\n" \
"v_mfma_f32_16x16x32_bf16 v[128:131], v[0:3], v[208:211], v[128:131]\n" \
"ds_read_b64_tr_b16 v[52:53], v232 offset:9728\n" \
"v_mfma_f32_16x16x32_bf16 v[128:131], v[4:7], v[212:215], v[128:131]\n" \
"ds_read_b64_tr_b16 v[54:55], v232 offset:11776\n" \
"v_mfma_f32_16x16x32_bf16 v[132:135], v[8:11], v[208:211], v[132:135]\n" \
"ds_read_b64_tr_b16 v[56:57], v233 offset:1536\n" \
"Lpg3849_%=:\n" \
"s_cbranch_execz Lpg5534_%=\n" \
"v_mfma_f32_16x16x32_bf16 v[132:135], v[12:15], v[212:215], v[132:135]\n" \
"global_load_lds_dwordx4 v229, s[44:45] offset:-3072\n" \
"v_mfma_f32_16x16x32_bf16 v[136:139], v[16:19], v[208:211], v[136:139]\n" \
"s_add_u32 s44, s44, 0x100000\n" \
"s_addc_u32 s45, s45, 0\n" \
"ds_read_b64_tr_b16 v[58:59], v233 offset:3584\n" \
"v_mfma_f32_16x16x32_bf16 v[136:139], v[20:23], v[212:215], v[136:139]\n" \
"ds_read_b64_tr_b16 v[60:61], v233 offset:9728\n" \
"v_mfma_f32_16x16x32_bf16 v[140:143], v[24:27], v[208:211], v[140:143]\n" \
"ds_read_b64_tr_b16 v[62:63], v233 offset:11776\n" \
"v_mfma_f32_16x16x32_bf16 v[140:143], v[28:31], v[212:215], v[140:143]\n" \
"s_waitcnt lgkmcnt(3)\n" \
"global_load_lds_dwordx4 v228, s[42:43] offset:-2048\n" \
"v_mfma_f32_16x16x32_bf16 v[172:175], v[24:27], v[216:219], v[172:175]\n" \
"v_add_u32 v230, 0x8000, v230\n" \
"v_add_u32 v231, 0x8000, v231\n" \
"v_mfma_f32_16x16x32_bf16 v[172:175], v[28:31], v[220:223], v[172:175]\n" \
"v_add_u32 v232, 0x4000, v232\n" \
"v_add_u32 v233, 0x4000, v233\n" \
"v_mfma_f32_16x16x32_bf16 v[168:171], v[16:19], v[216:219], v[168:171]\n" \
"v_mfma_f32_16x16x32_bf16 v[168:171], v[20:23], v[220:223], v[168:171]\n" \
"global_load_lds_dwordx4 v229, s[42:43] offset:-1024\n" \
"v_mfma_f32_16x16x32_bf16 v[164:167], v[8:11], v[216:219], v[164:167]\n" \
"s_add_u32 s42, s42, 0x100000\n" \
"s_addc_u32 s43, s43, 0\n" \
"v_mfma_f32_16x16x32_bf16 v[164:167], v[12:15], v[220:223], v[164:167]\n" \
"s_waitcnt vmcnt(20) lgkmcnt(0)\n" \
"v_mfma_f32_16x16x32_bf16 v[160:163], v[0:3], v[216:219], v[160:163]\n" \
"v_mfma_f32_16x16x32_bf16 v[160:163], v[4:7], v[220:223], v[160:163]\n" \
"s_barrier\n" \
"v_mfma_f32_16x16x32_bf16 v[80:83], v[32:35], v[192:195], v[80:83]\n" \
"ds_read_b64_tr_b16 v[0:1], v232 offset:0\n" \
"v_mfma_f32_16x16x32_bf16 v[80:83], v[36:39], v[196:199], v[80:83]\n" \
"ds_read_b64_tr_b16 v[2:3], v232 offset:2048\n" \
"v_mfma_f32_16x16x32_bf16 v[84:87], v[40:43], v[192:195], v[84:87]\n" \
"s_add_u32 m0, s87, 102400\n" \
"ds_read_b64_tr_b16 v[4:5], v232 offset:8192\n" \
"v_mfma_f32_16x16x32_bf16 v[84:87], v[44:47], v[196:199], v[84:87]\n" \
"global_load_lds_dwordx4 v224, s[48:49] offset:-4096\n" \
"v_mfma_f32_16x16x32_bf16 v[88:91], v[48:51], v[192:195], v[88:91]\n" \
"ds_read_b64_tr_b16 v[6:7], v232 offset:10240\n" \
"v_mfma_f32_16x16x32_bf16 v[88:91], v[52:55], v[196:199], v[88:91]\n" \
"ds_read_b64_tr_b16 v[8:9], v233 offset:0\n" \
"v_mfma_f32_16x16x32_bf16 v[92:95], v[56:59], v[192:195], v[92:95]\n" \
"ds_read_b64_tr_b16 v[10:11], v233 offset:2048\n" \
"v_mfma_f32_16x16x32_bf16 v[92:95], v[60:63], v[196:199], v[92:95]\n" \
"global_load_lds_dwordx4 v225, s[48:49] offset:-3072\n" \
"v_mfma_f32_16x16x32_bf16 v[124:127], v[56:59], v[200:203], v[124:127]\n" \
"ds_read_b128 v[192:195], v230 offset:0\n" \
"v_mfma_f32_16x16x32_bf16 v[124:127], v[60:63], v[204:207], v[124:127]\n" \
"ds_read_b128 v[196:199], v231 offset:0\n" \
"v_mfma_f32_16x16x32_bf16 v[120:123], v[48:51], v[200:203], v[120:123]\n" \
"ds_read_b64_tr_b16 v[12:13], v233 offset:8192\n" \
"v_mfma_f32_16x16x32_bf16 v[120:123], v[52:55], v[204:207], v[120:123]\n" \
"global_load_lds_dwordx4 v226, s[48:49] offset:-2048\n" \
"v_mfma_f32_16x16x32_bf16 v[116:119], v[40:43], v[200:203], v[116:119]\n" \
"ds_read_b64_tr_b16 v[14:15], v233 offset:10240\n" \
"v_mfma_f32_16x16x32_bf16 v[116:119], v[44:47], v[204:207], v[116:119]\n" \
"ds_read_b64_tr_b16 v[16:17], v232 offset:512\n" \
"v_mfma_f32_16x16x32_bf16 v[112:115], v[32:35], v[200:203], v[112:115]\n" \
"ds_read_b64_tr_b16 v[18:19], v232 offset:2560\n" \
"v_mfma_f32_16x16x32_bf16 v[112:115], v[36:39], v[204:207], v[112:115]\n" \
"global_load_lds_dwordx4 v227, s[48:49] offset:-1024\n" \
"v_mfma_f32_16x16x32_bf16 v[144:147], v[32:35], v[208:211], v[144:147]\n" \
"s_add_u32 s48, s48, 0x100\n" \
"s_addc_u32 s49, s49, 0\n" \
"ds_read_b128 v[200:203], v230 offset:2048\n" \
"v_mfma_f32_16x16x32_bf16 v[144:147], v[36:39], v[212:215], v[144:147]\n" \
"ds_read_b128 v[204:207], v231 offset:2048\n" \
"v_mfma_f32_16x16x32_bf16 v[148:151], v[40:43], v[208:211], v[148:151]\n" \
"ds_read_b64_tr_b16 v[20:21], v232 offset:8704\n" \
"v_mfma_f32_16x16x32_bf16 v[148:151], v[44:47], v[212:215], v[148:151]\n" \
"global_load_lds_dwordx4 v224, s[46:47] offset:0\n" \
"v_mfma_f32_16x16x32_bf16 v[152:155], v[48:51], v[208:211], v[152:155]\n" \
"ds_read_b64_tr_b16 v[22:23], v232 offset:10752\n" \
"v_mfma_f32_16x16x32_bf16 v[152:155], v[52:55], v[212:215], v[152:155]\n" \
"ds_read_b64_tr_b16 v[24:25], v233 offset:512\n" \
"v_mfma_f32_16x16x32_bf16 v[156:159], v[56:59], v[208:211], v[156:159]\n" \
"ds_read_b64_tr_b16 v[26:27], v233 offset:2560\n" \
"v_mfma_f32_16x16x32_bf16 v[156:159], v[60:63], v[212:215], v[156:159]\n" \
"global_load_lds_dwordx4 v225, s[46:47] offset:1024\n" \
"v_mfma_f32_16x16x32_bf16 v[188:191], v[56:59], v[216:219], v[188:191]\n" \
"ds_read_b128 v[208:211], v230 offset:4096\n" \
"v_mfma_f32_16x16x32_bf16 v[188:191], v[60:63], v[220:223], v[188:191]\n" \
"ds_read_b128 v[212:215], v231 offset:4096\n" \
"v_mfma_f32_16x16x32_bf16 v[184:187], v[48:51], v[216:219], v[184:187]\n" \
"ds_read_b64_tr_b16 v[28:29], v233 offset:8704\n" \
"v_mfma_f32_16x16x32_bf16 v[184:187], v[52:55], v[220:223], v[184:187]\n" \
"global_load_lds_dwordx4 v226, s[46:47] offset:2048\n" \
"v_mfma_f32_16x16x32_bf16 v[180:183], v[40:43], v[216:219], v[180:183]\n" \
"ds_read_b64_tr_b16 v[30:31], v233 offset:10752\n" \
"v_mfma_f32_16x16x32_bf16 v[180:183], v[44:47], v[220:223], v[180:183]\n" \
"v_mfma_f32_16x16x32_bf16 v[176:179], v[32:35], v[216:219], v[176:179]\n" \
"v_mfma_f32_16x16x32_bf16 v[176:179], v[36:39], v[220:223], v[176:179]\n" \
"s_waitcnt lgkmcnt(1)\n" \
"global_load_lds_dwordx4 v227, s[46:47] offset:3072\n" \
"v_mfma_f32_16x16x32_bf16 v[64:67], v[0:3], v[192:195], v[64:67]\n" \
"s_add_u32 s46, s46, 0x100\n" \
"s_addc_u32 s47, s47, 0\n" \
"ds_read_b128 v[216:219], v230 offset:6144\n" \
"v_mfma_f32_16x16x32_bf16 v[64:67], v[4:7], v[196:199], v[64:67]\n" \
"ds_read_b128 v[220:223], v231 offset:6144\n" \
"s_add_u32 s90, s89, 139264\n" \
"v_mfma_f32_16x16x32_bf16 v[68:71], v[8:11], v[192:195], v[68:71]\n" \
"ds_read_b64_tr_b16 v[32:33], v232 offset:1024\n" \
"v_mfma_f32_16x16x32_bf16 v[68:71], v[12:15], v[196:199], v[68:71]\n" \
"buffer_store_dwordx4 a[144:147], v248, s[56:59], s90 offen offset:0 sc1\n" \
"v_mfma_f32_16x16x32_bf16 v[72:75], v[16:19], v[192:195], v[72:75]\n" \
"ds_read_b64_tr_b16 v[34:35], v232 offset:3072\n" \
"v_mfma_f32_16x16x32_bf16 v[72:75], v[20:23], v[196:199], v[72:75]\n" \
"s_waitcnt lgkmcnt(3)\n" \
"ds_read_b64_tr_b16 v[36:37], v232 offset:9216\n" \
"v_mfma_f32_16x16x32_bf16 v[76:79], v[24:27], v[192:195], v[76:79]\n" \
"ds_read_b64_tr_b16 v[38:39], v232 offset:11264\n" \
"v_mfma_f32_16x16x32_bf16 v[76:79], v[28:31], v[196:199], v[76:79]\n" \
"v_mfma_f32_16x16x32_bf16 v[108:111], v[24:27], v[200:203], v[108:111]\n" \
"ds_read_b64_tr_b16 v[40:41], v233 offset:1024\n" \
"v_mfma_f32_16x16x32_bf16 v[108:111], v[28:31], v[204:207], v[108:111]\n" \
"ds_read_b64_tr_b16 v[42:43], v233 offset:3072\n" \
"v_mfma_f32_16x16x32_bf16 v[104:107], v[16:19], v[200:203], v[104:107]\n" \
"ds_read_b64_tr_b16 v[44:45], v233 offset:9216\n" \
"v_mfma_f32_16x16x32_bf16 v[104:107], v[20:23], v[204:207], v[104:107]\n" \
"v_mfma_f32_16x16x32_bf16 v[100:103], v[8:11], v[200:203], v[100:103]\n" \
"ds_read_b64_tr_b16 v[46:47], v233 offset:11264\n" \
"v_mfma_f32_16x16x32_bf16 v[100:103], v[12:15], v[204:207], v[100:103]\n" \
"ds_read_b64_tr_b16 v[48:49], v232 offset:1536\n" \
"v_mfma_f32_16x16x32_bf16 v[96:99], v[0:3], v[200:203], v[96:99]\n" \
"s_add_u32 m0, s95, 4096\n" \
"ds_read_b64_tr_b16 v[50:51], v232 offset:3584\n" \
"v_mfma_f32_16x16x32_bf16 v[96:99], v[4:7], v[204:207], v[96:99]\n" \
"global_load_lds_dwordx4 v228, s[44:45] offset:-4096\n" \
"v_mfma_f32_16x16x32_bf16 v[128:131], v[0:3], v[208:211], v[128:131]\n" \
"ds_read_b64_tr_b16 v[52:53], v232 offset:9728\n" \
"v_mfma_f32_16x16x32_bf16 v[128:131], v[4:7], v[212:215], v[128:131]\n" \
"ds_read_b64_tr_b16 v[54:55], v232 offset:11776\n" \
"v_mfma_f32_16x16x32_bf16 v[132:135], v[8:11], v[208:211], v[132:135]\n" \
"ds_read_b64_tr_b16 v[56:57], v233 offset:1536\n" \
"v_mfma_f32_16x16x32_bf16 v[132:135], v[12:15], v[212:215], v[132:135]\n" \
"global_load_lds_dwordx4 v229, s[44:45] offset:-3072\n" \
"v_mfma_f32_16x16x32_bf16 v[136:139], v[16:19], v[208:211], v[136:139]\n" \
"s_add_u32 s44, s44, 0x80000\n" \
"s_addc_u32 s45, s45, 0\n" \
"ds_read_b64_tr_b16 v[58:59], v233 offset:3584\n" \
"v_mfma_f32_16x16x32_bf16 v[136:139], v[20:23], v[212:215], v[136:139]\n" \
"ds_read_b64_tr_b16 v[60:61], v233 offset:9728\n" \
"v_mfma_f32_16x16x32_bf16 v[140:143], v[24:27], v[208:211], v[140:143]\n" \
"ds_read_b64_tr_b16 v[62:63], v233 offset:11776\n" \
"v_mfma_f32_16x16x32_bf16 v[140:143], v[28:31], v[212:215], v[140:143]\n" \
"s_waitcnt lgkmcnt(3)\n" \
"global_load_lds_dwordx4 v228, s[42:43] offset:-2048\n" \
"v_mfma_f32_16x16x32_bf16 v[172:175], v[24:27], v[216:219], v[172:175]\n" \
"v_add_u32 v230, 0xffff0000, v230\n" \
"v_add_u32 v231, 0xffff0000, v231\n" \
"v_mfma_f32_16x16x32_bf16 v[172:175], v[28:31], v[220:223], v[172:175]\n" \
"v_add_u32 v232, 0x4000, v232\n" \
"v_add_u32 v233, 0x4000, v233\n" \
"v_mfma_f32_16x16x32_bf16 v[168:171], v[16:19], v[216:219], v[168:171]\n" \
"v_mfma_f32_16x16x32_bf16 v[168:171], v[20:23], v[220:223], v[168:171]\n" \
"global_load_lds_dwordx4 v229, s[42:43] offset:-1024\n" \
"v_mfma_f32_16x16x32_bf16 v[164:167], v[8:11], v[216:219], v[164:167]\n" \
"s_add_u32 s42, s42, 0x80000\n" \
"s_addc_u32 s43, s43, 0\n" \
"v_mfma_f32_16x16x32_bf16 v[164:167], v[12:15], v[220:223], v[164:167]\n" \
"s_waitcnt vmcnt(20) lgkmcnt(0)\n" \
"v_mfma_f32_16x16x32_bf16 v[160:163], v[0:3], v[216:219], v[160:163]\n" \
"v_mfma_f32_16x16x32_bf16 v[160:163], v[4:7], v[220:223], v[160:163]\n" \
"s_barrier\n" \
"v_mfma_f32_16x16x32_bf16 v[80:83], v[32:35], v[192:195], v[80:83]\n" \
"ds_read_b64_tr_b16 v[0:1], v232 offset:0\n" \
"v_mfma_f32_16x16x32_bf16 v[80:83], v[36:39], v[196:199], v[80:83]\n" \
"ds_read_b64_tr_b16 v[2:3], v232 offset:2048\n" \
"v_mfma_f32_16x16x32_bf16 v[84:87], v[40:43], v[192:195], v[84:87]\n" \
"s_add_u32 m0, s87, 135168\n" \
"ds_read_b64_tr_b16 v[4:5], v232 offset:8192\n" \
"v_mfma_f32_16x16x32_bf16 v[84:87], v[44:47], v[196:199], v[84:87]\n" \
"global_load_lds_dwordx4 v224, s[48:49] offset:-4096\n" \
"v_mfma_f32_16x16x32_bf16 v[88:91], v[48:51], v[192:195], v[88:91]\n" \
"ds_read_b64_tr_b16 v[6:7], v232 offset:10240\n" \
"v_mfma_f32_16x16x32_bf16 v[88:91], v[52:55], v[196:199], v[88:91]\n" \
"ds_read_b64_tr_b16 v[8:9], v233 offset:0\n" \
"v_mfma_f32_16x16x32_bf16 v[92:95], v[56:59], v[192:195], v[92:95]\n" \
"ds_read_b64_tr_b16 v[10:11], v233 offset:2048\n" \
"v_mfma_f32_16x16x32_bf16 v[92:95], v[60:63], v[196:199], v[92:95]\n" \
"global_load_lds_dwordx4 v225, s[48:49] offset:-3072\n" \
"v_mfma_f32_16x16x32_bf16 v[124:127], v[56:59], v[200:203], v[124:127]\n" \
"ds_read_b128 v[192:195], v230 offset:0\n" \
"v_mfma_f32_16x16x32_bf16 v[124:127], v[60:63], v[204:207], v[124:127]\n" \
"ds_read_b128 v[196:199], v231 offset:0\n" \
"v_mfma_f32_16x16x32_bf16 v[120:123], v[48:51], v[200:203], v[120:123]\n" \
"ds_read_b64_tr_b16 v[12:13], v233 offset:8192\n" \
"v_mfma_f32_16x16x32_bf16 v[120:123], v[52:55], v[204:207], v[120:123]\n" \
"global_load_lds_dwordx4 v226, s[48:49] offset:-2048\n" \
"v_mfma_f32_16x16x32_bf16 v[116:119], v[40:43], v[200:203], v[116:119]\n" \
"ds_read_b64_tr_b16 v[14:15], v233 offset:10240\n" \
"v_mfma_f32_16x16x32_bf16 v[116:119], v[44:47], v[204:207], v[116:119]\n" \
"ds_read_b64_tr_b16 v[16:17], v232 offset:512\n" \
"v_mfma_f32_16x16x32_bf16 v[112:115], v[32:35], v[200:203], v[112:115]\n" \
"ds_read_b64_tr_b16 v[18:19], v232 offset:2560\n" \
"v_mfma_f32_16x16x32_bf16 v[112:115], v[36:39], v[204:207], v[112:115]\n" \
"global_load_lds_dwordx4 v227, s[48:49] offset:-1024\n" \
"v_mfma_f32_16x16x32_bf16 v[144:147], v[32:35], v[208:211], v[144:147]\n" \
"s_add_u32 s48, s48, 0x80\n" \
"s_addc_u32 s49, s49, 0\n" \
"ds_read_b128 v[200:203], v230 offset:2048\n" \
"v_mfma_f32_16x16x32_bf16 v[144:147], v[36:39], v[212:215], v[144:147]\n" \
"ds_read_b128 v[204:207], v231 offset:2048\n" \
"v_mfma_f32_16x16x32_bf16 v[148:151], v[40:43], v[208:211], v[148:151]\n" \
"ds_read_b64_tr_b16 v[20:21], v232 offset:8704\n" \
"v_mfma_f32_16x16x32_bf16 v[148:151], v[44:47], v[212:215], v[148:151]\n" \
"global_load_lds_dwordx4 v224, s[46:47] offset:0\n" \
"v_mfma_f32_16x16x32_bf16 v[152:155], v[48:51], v[208:211], v[152:155]\n" \
"ds_read_b64_tr_b16 v[22:23], v232 offset:10752\n" \
"v_mfma_f32_16x16x32_bf16 v[152:155], v[52:55], v[212:215], v[152:155]\n" \
"ds_read_b64_tr_b16 v[24:25], v233 offset:512\n" \
"v_mfma_f32_16x16x32_bf16 v[156:159], v[56:59], v[208:211], v[156:159]\n" \
"ds_read_b64_tr_b16 v[26:27], v233 offset:2560\n" \
"v_mfma_f32_16x16x32_bf16 v[156:159], v[60:63], v[212:215], v[156:159]\n" \
"global_load_lds_dwordx4 v225, s[46:47] offset:1024\n" \
"v_mfma_f32_16x16x32_bf16 v[188:191], v[56:59], v[216:219], v[188:191]\n" \
"ds_read_b128 v[208:211], v230 offset:4096\n" \
"v_mfma_f32_16x16x32_bf16 v[188:191], v[60:63], v[220:223], v[188:191]\n" \
"ds_read_b128 v[212:215], v231 offset:4096\n" \
"v_mfma_f32_16x16x32_bf16 v[184:187], v[48:51], v[216:219], v[184:187]\n" \
"ds_read_b64_tr_b16 v[28:29], v233 offset:8704\n" \
"v_mfma_f32_16x16x32_bf16 v[184:187], v[52:55], v[220:223], v[184:187]\n" \
"global_load_lds_dwordx4 v226, s[46:47] offset:2048\n" \
"v_mfma_f32_16x16x32_bf16 v[180:183], v[40:43], v[216:219], v[180:183]\n" \
"ds_read_b64_tr_b16 v[30:31], v233 offset:10752\n" \
"v_mfma_f32_16x16x32_bf16 v[180:183], v[44:47], v[220:223], v[180:183]\n" \
"v_mfma_f32_16x16x32_bf16 v[176:179], v[32:35], v[216:219], v[176:179]\n" \
"v_mfma_f32_16x16x32_bf16 v[176:179], v[36:39], v[220:223], v[176:179]\n" \
"s_waitcnt lgkmcnt(1)\n" \
"global_load_lds_dwordx4 v227, s[46:47] offset:3072\n" \
"v_mfma_f32_16x16x32_bf16 v[64:67], v[0:3], v[192:195], v[64:67]\n" \
"s_add_u32 s46, s46, 0x80\n" \
"s_addc_u32 s47, s47, 0\n" \
"ds_read_b128 v[216:219], v230 offset:6144\n" \
"v_mfma_f32_16x16x32_bf16 v[64:67], v[4:7], v[196:199], v[64:67]\n" \
"ds_read_b128 v[220:223], v231 offset:6144\n" \
"v_mfma_f32_16x16x32_bf16 v[68:71], v[8:11], v[192:195], v[68:71]\n" \
"ds_read_b64_tr_b16 v[32:33], v232 offset:1024\n" \
"v_mfma_f32_16x16x32_bf16 v[68:71], v[12:15], v[196:199], v[68:71]\n" \
"buffer_store_dwordx4 a[148:151], v248, s[56:59], s90 offen offset:1024 sc1\n" \
"v_mfma_f32_16x16x32_bf16 v[72:75], v[16:19], v[192:195], v[72:75]\n" \
"ds_read_b64_tr_b16 v[34:35], v232 offset:3072\n" \
"v_mfma_f32_16x16x32_bf16 v[72:75], v[20:23], v[196:199], v[72:75]\n" \
"s_waitcnt lgkmcnt(3)\n" \
"ds_read_b64_tr_b16 v[36:37], v232 offset:9216\n" \
"v_mfma_f32_16x16x32_bf16 v[76:79], v[24:27], v[192:195], v[76:79]\n" \
"ds_read_b64_tr_b16 v[38:39], v232 offset:11264\n" \
"v_mfma_f32_16x16x32_bf16 v[76:79], v[28:31], v[196:199], v[76:79]\n" \
"v_mfma_f32_16x16x32_bf16 v[108:111], v[24:27], v[200:203], v[108:111]\n" \
"ds_read_b64_tr_b16 v[40:41], v233 offset:1024\n" \
"v_mfma_f32_16x16x32_bf16 v[108:111], v[28:31], v[204:207], v[108:111]\n" \
"ds_read_b64_tr_b16 v[42:43], v233 offset:3072\n" \
"v_mfma_f32_16x16x32_bf16 v[104:107], v[16:19], v[200:203], v[104:107]\n" \
"ds_read_b64_tr_b16 v[44:45], v233 offset:9216\n" \
"v_mfma_f32_16x16x32_bf16 v[104:107], v[20:23], v[204:207], v[104:107]\n" \
"v_mfma_f32_16x16x32_bf16 v[100:103], v[8:11], v[200:203], v[100:103]\n" \
"ds_read_b64_tr_b16 v[46:47], v233 offset:11264\n" \
"v_mfma_f32_16x16x32_bf16 v[100:103], v[12:15], v[204:207], v[100:103]\n" \
"ds_read_b64_tr_b16 v[48:49], v232 offset:1536\n" \
"v_mfma_f32_16x16x32_bf16 v[96:99], v[0:3], v[200:203], v[96:99]\n" \
"s_add_u32 m0, s95, 20480\n" \
"ds_read_b64_tr_b16 v[50:51], v232 offset:3584\n" \
"v_mfma_f32_16x16x32_bf16 v[96:99], v[4:7], v[204:207], v[96:99]\n" \
"global_load_lds_dwordx4 v228, s[44:45] offset:-4096\n" \
"v_mfma_f32_16x16x32_bf16 v[128:131], v[0:3], v[208:211], v[128:131]\n" \
"ds_read_b64_tr_b16 v[52:53], v232 offset:9728\n" \
"v_mfma_f32_16x16x32_bf16 v[128:131], v[4:7], v[212:215], v[128:131]\n" \
"ds_read_b64_tr_b16 v[54:55], v232 offset:11776\n" \
"v_mfma_f32_16x16x32_bf16 v[132:135], v[8:11], v[208:211], v[132:135]\n" \
"ds_read_b64_tr_b16 v[56:57], v233 offset:1536\n" \
"v_mfma_f32_16x16x32_bf16 v[132:135], v[12:15], v[212:215], v[132:135]\n" \
"global_load_lds_dwordx4 v229, s[44:45] offset:-3072\n" \
"v_mfma_f32_16x16x32_bf16 v[136:139], v[16:19], v[208:211], v[136:139]\n" \
"s_add_u32 s44, s44, 0x80000\n" \
"s_addc_u32 s45, s45, 0\n" \
"ds_read_b64_tr_b16 v[58:59], v233 offset:3584\n" \
"v_mfma_f32_16x16x32_bf16 v[136:139], v[20:23], v[212:215], v[136:139]\n" \
"ds_read_b64_tr_b16 v[60:61], v233 offset:9728\n" \
"v_mfma_f32_16x16x32_bf16 v[140:143], v[24:27], v[208:211], v[140:143]\n" \
"ds_read_b64_tr_b16 v[62:63], v233 offset:11776\n" \
"v_mfma_f32_16x16x32_bf16 v[140:143], v[28:31], v[212:215], v[140:143]\n" \
"s_waitcnt lgkmcnt(3)\n" \
"global_load_lds_dwordx4 v228, s[42:43] offset:-2048\n" \
"v_mfma_f32_16x16x32_bf16 v[172:175], v[24:27], v[216:219], v[172:175]\n" \
"v_add_u32 v230, 0x8000, v230\n" \
"v_add_u32 v231, 0x8000, v231\n" \
"v_mfma_f32_16x16x32_bf16 v[172:175], v[28:31], v[220:223], v[172:175]\n" \
"v_add_u32 v232, 0x4000, v232\n" \
"v_add_u32 v233, 0x4000, v233\n" \
"v_mfma_f32_16x16x32_bf16 v[168:171], v[16:19], v[216:219], v[168:171]\n" \
"v_mfma_f32_16x16x32_bf16 v[168:171], v[20:23], v[220:223], v[168:171]\n" \
"global_load_lds_dwordx4 v229, s[42:43] offset:-1024\n" \
"v_mfma_f32_16x16x32_bf16 v[164:167], v[8:11], v[216:219], v[164:167]\n" \
"s_add_u32 s42, s42, 0x80000\n" \
"s_addc_u32 s43, s43, 0\n" \
"v_mfma_f32_16x16x32_bf16 v[164:167], v[12:15], v[220:223], v[164:167]\n" \
"s_waitcnt vmcnt(18) lgkmcnt(0)\n" \
"v_mfma_f32_16x16x32_bf16 v[160:163], v[0:3], v[216:219], v[160:163]\n" \
"v_mfma_f32_16x16x32_bf16 v[160:163], v[4:7], v[220:223], v[160:163]\n" \
"s_barrier\n" \
"v_mfma_f32_16x16x32_bf16 v[80:83], v[32:35], v[192:195], v[80:83]\n" \
"ds_read_b64_tr_b16 v[0:1], v232 offset:0\n" \
"v_mfma_f32_16x16x32_bf16 v[80:83], v[36:39], v[196:199], v[80:83]\n" \
"ds_read_b64_tr_b16 v[2:3], v232 offset:2048\n" \
"v_mfma_f32_16x16x32_bf16 v[84:87], v[40:43], v[192:195], v[84:87]\n" \
"s_add_u32 m0, s87, 69632\n" \
"ds_read_b64_tr_b16 v[4:5], v232 offset:8192\n" \
"v_mfma_f32_16x16x32_bf16 v[84:87], v[44:47], v[196:199], v[84:87]\n" \
"global_load_lds_dwordx4 v224, s[48:49] offset:-4096\n" \
"v_mfma_f32_16x16x32_bf16 v[88:91], v[48:51], v[192:195], v[88:91]\n" \
"ds_read_b64_tr_b16 v[6:7], v232 offset:10240\n" \
"v_mfma_f32_16x16x32_bf16 v[88:91], v[52:55], v[196:199], v[88:91]\n" \
"ds_read_b64_tr_b16 v[8:9], v233 offset:0\n" \
"v_mfma_f32_16x16x32_bf16 v[92:95], v[56:59], v[192:195], v[92:95]\n" \
"ds_read_b64_tr_b16 v[10:11], v233 offset:2048\n" \
"v_mfma_f32_16x16x32_bf16 v[92:95], v[60:63], v[196:199], v[92:95]\n" \
"global_load_lds_dwordx4 v225, s[48:49] offset:-3072\n" \
"v_mfma_f32_16x16x32_bf16 v[124:127], v[56:59], v[200:203], v[124:127]\n" \
"ds_read_b128 v[192:195], v230 offset:0\n" \
"v_mfma_f32_16x16x32_bf16 v[124:127], v[60:63], v[204:207], v[124:127]\n" \
"ds_read_b128 v[196:199], v231 offset:0\n" \
"v_mfma_f32_16x16x32_bf16 v[120:123], v[48:51], v[200:203], v[120:123]\n" \
"ds_read_b64_tr_b16 v[12:13], v233 offset:8192\n" \
"v_mfma_f32_16x16x32_bf16 v[120:123], v[52:55], v[204:207], v[120:123]\n" \
"global_load_lds_dwordx4 v226, s[48:49] offset:-2048\n" \
"v_mfma_f32_16x16x32_bf16 v[116:119], v[40:43], v[200:203], v[116:119]\n" \
"ds_read_b64_tr_b16 v[14:15], v233 offset:10240\n" \
"v_mfma_f32_16x16x32_bf16 v[116:119], v[44:47], v[204:207], v[116:119]\n" \
"ds_read_b64_tr_b16 v[16:17], v232 offset:512\n" \
"v_mfma_f32_16x16x32_bf16 v[112:115], v[32:35], v[200:203], v[112:115]\n" \
"ds_read_b64_tr_b16 v[18:19], v232 offset:2560\n" \
"v_mfma_f32_16x16x32_bf16 v[112:115], v[36:39], v[204:207], v[112:115]\n" \
"global_load_lds_dwordx4 v227, s[48:49] offset:-1024\n" \
"v_mfma_f32_16x16x32_bf16 v[144:147], v[32:35], v[208:211], v[144:147]\n" \
"s_add_u32 s48, s48, 0x80\n" \
"s_addc_u32 s49, s49, 0\n" \
"ds_read_b128 v[200:203], v230 offset:2048\n" \
"v_mfma_f32_16x16x32_bf16 v[144:147], v[36:39], v[212:215], v[144:147]\n" \
"ds_read_b128 v[204:207], v231 offset:2048\n" \
"v_mfma_f32_16x16x32_bf16 v[148:151], v[40:43], v[208:211], v[148:151]\n" \
"ds_read_b64_tr_b16 v[20:21], v232 offset:8704\n" \
"v_mfma_f32_16x16x32_bf16 v[148:151], v[44:47], v[212:215], v[148:151]\n" \
"global_load_lds_dwordx4 v224, s[46:47] offset:0\n" \
"v_mfma_f32_16x16x32_bf16 v[152:155], v[48:51], v[208:211], v[152:155]\n" \
"ds_read_b64_tr_b16 v[22:23], v232 offset:10752\n" \
"v_mfma_f32_16x16x32_bf16 v[152:155], v[52:55], v[212:215], v[152:155]\n" \
"ds_read_b64_tr_b16 v[24:25], v233 offset:512\n" \
"v_mfma_f32_16x16x32_bf16 v[156:159], v[56:59], v[208:211], v[156:159]\n" \
"ds_read_b64_tr_b16 v[26:27], v233 offset:2560\n" \
"v_mfma_f32_16x16x32_bf16 v[156:159], v[60:63], v[212:215], v[156:159]\n" \
"global_load_lds_dwordx4 v225, s[46:47] offset:1024\n" \
"v_mfma_f32_16x16x32_bf16 v[188:191], v[56:59], v[216:219], v[188:191]\n" \
"ds_read_b128 v[208:211], v230 offset:4096\n" \
"v_mfma_f32_16x16x32_bf16 v[188:191], v[60:63], v[220:223], v[188:191]\n" \
"ds_read_b128 v[212:215], v231 offset:4096\n" \
"v_mfma_f32_16x16x32_bf16 v[184:187], v[48:51], v[216:219], v[184:187]\n" \
"ds_read_b64_tr_b16 v[28:29], v233 offset:8704\n" \
"v_mfma_f32_16x16x32_bf16 v[184:187], v[52:55], v[220:223], v[184:187]\n" \
"global_load_lds_dwordx4 v226, s[46:47] offset:2048\n" \
"v_mfma_f32_16x16x32_bf16 v[180:183], v[40:43], v[216:219], v[180:183]\n" \
"ds_read_b64_tr_b16 v[30:31], v233 offset:10752\n" \
"v_mfma_f32_16x16x32_bf16 v[180:183], v[44:47], v[220:223], v[180:183]\n" \
"v_mfma_f32_16x16x32_bf16 v[176:179], v[32:35], v[216:219], v[176:179]\n" \
"v_mfma_f32_16x16x32_bf16 v[176:179], v[36:39], v[220:223], v[176:179]\n" \
"s_waitcnt lgkmcnt(1)\n" \
"global_load_lds_dwordx4 v227, s[46:47] offset:3072\n" \
"v_mfma_f32_16x16x32_bf16 v[64:67], v[0:3], v[192:195], v[64:67]\n" \
"s_add_u32 s46, s46, 0x80\n" \
"s_addc_u32 s47, s47, 0\n" \
"ds_read_b128 v[216:219], v230 offset:6144\n" \
"v_mfma_f32_16x16x32_bf16 v[64:67], v[4:7], v[196:199], v[64:67]\n" \
"s_mul_i32 s77, s62, 557824\n" \
"ds_read_b128 v[220:223], v231 offset:6144\n" \
"s_add_u32 s77, s77, s95\n" \
"v_mfma_f32_16x16x32_bf16 v[68:71], v[8:11], v[192:195], v[68:71]\n" \
"s_add_u32 s36, s28, s77\n" \
"s_mov_b32 s37, s29\n" \
"ds_read_b64_tr_b16 v[32:33], v232 offset:1024\n" \
"v_mfma_f32_16x16x32_bf16 v[68:71], v[12:15], v[196:199], v[68:71]\n" \
"buffer_store_dwordx4 a[152:155], v248, s[56:59], s90 offen offset:2048 sc1\n" \
"s_add_u32 s77, s77, 17850368\n" \
"s_add_u32 s38, s28, s77\n" \
"s_mov_b32 s39, s29\n" \
"s_and_b32 s74, s63, 1\n" \
"v_mfma_f32_16x16x32_bf16 v[72:75], v[16:19], v[192:195], v[72:75]\n" \
"s_lshl_b32 s74, s74, 15\n" \
"s_xor_b32 s74, s74, 0x18000\n" \
"ds_read_b64_tr_b16 v[34:35], v232 offset:3072\n" \
"v_mfma_f32_16x16x32_bf16 v[72:75], v[20:23], v[196:199], v[72:75]\n" \
"s_waitcnt lgkmcnt(3)\n" \
"ds_read_b64_tr_b16 v[36:37], v232 offset:9216\n" \
"v_add_u32 v247, s74, v248\n" \
"v_mfma_f32_16x16x32_bf16 v[76:79], v[24:27], v[192:195], v[76:79]\n" \
"s_add_u32 s74, s74, s95\n" \
"s_lshr_b32 s83, s61, 1\n" \
"ds_read_b64_tr_b16 v[38:39], v232 offset:11264\n" \
"v_mfma_f32_16x16x32_bf16 v[76:79], v[28:31], v[196:199], v[76:79]\n" \
"buffer_store_dwordx4 a[156:159], v248, s[56:59], s90 offen offset:3072 sc1\n" \
"s_lshl_b32 s75, s63, 2\n" \
"s_add_u32 s77, s83, s75\n" \
"s_mul_i32 s77, s77, 266240\n" \
"s_sub_u32 s78, s83, s75\n" \
"v_mfma_f32_16x16x32_bf16 v[108:111], v[24:27], v[200:203], v[108:111]\n" \
"s_add_u32 s78, s78, 60\n" \
"s_mul_i32 s78, s78, 266240\n" \
"ds_read_b64_tr_b16 v[40:41], v233 offset:1024\n" \
"v_mfma_f32_16x16x32_bf16 v[108:111], v[28:31], v[204:207], v[108:111]\n" \
"s_and_b32 s82, s61, 1\n" \
"ds_read_b64_tr_b16 v[42:43], v233 offset:3072\n" \
"v_mfma_f32_16x16x32_bf16 v[104:107], v[16:19], v[200:203], v[104:107]\n" \
"s_cmp_eq_u32 s82, 0\n" \
"s_cselect_b32 s16, 0, 0xff00\n" \
"ds_read_b64_tr_b16 v[44:45], v233 offset:9216\n" \
"v_mfma_f32_16x16x32_bf16 v[104:107], v[20:23], v[204:207], v[104:107]\n" \
"buffer_load_dword v244, v245, s[56:59], s91 offen sc1\n" \
"v_mfma_f32_16x16x32_bf16 v[100:103], v[8:11], v[200:203], v[100:103]\n" \
"s_mov_b32 s17, s16\n" \
"s_lshl_b32 s75, s82, 8\n" \
"ds_read_b64_tr_b16 v[46:47], v233 offset:11264\n" \
"v_mfma_f32_16x16x32_bf16 v[100:103], v[12:15], v[204:207], v[100:103]\n" \
"s_lshl_b32 s76, s62, 13\n" \
"s_add_u32 s75, s75, s76\n" \
"ds_read_b64_tr_b16 v[48:49], v232 offset:1536\n" \
"v_mfma_f32_16x16x32_bf16 v[96:99], v[0:3], v[200:203], v[96:99]\n" \
"s_add_u32 m0, s95, 36864\n" \
"s_add_u32 s75, s75, 35700736\n" \
"ds_read_b64_tr_b16 v[50:51], v232 offset:3584\n" \
"v_mfma_f32_16x16x32_bf16 v[96:99], v[4:7], v[204:207], v[96:99]\n" \
"s_add_u32 s77, s77, s75\n" \
"global_load_lds_dwordx4 v228, s[44:45] offset:-4096\n" \
"v_mfma_f32_16x16x32_bf16 v[128:131], v[0:3], v[208:211], v[128:131]\n" \
"s_add_u32 s78, s78, s75\n" \
"s_mul_i32 s83, s83, 9216\n" \
"ds_read_b64_tr_b16 v[52:53], v232 offset:9728\n" \
"v_mfma_f32_16x16x32_bf16 v[128:131], v[4:7], v[212:215], v[128:131]\n" \
"s_lshl_b32 s75, s82, 12\n" \
"s_add_u32 s83, s83, s75\n" \
"ds_read_b64_tr_b16 v[54:55], v232 offset:11776\n" \
"v_mfma_f32_16x16x32_bf16 v[132:135], v[8:11], v[208:211], v[132:135]\n" \
"s_add_u32 s83, s83, 4096\n" \
"s_lshl_b32 s82, s82, 14\n" \
"ds_read_b64_tr_b16 v[56:57], v233 offset:1536\n" \
"v_mfma_f32_16x16x32_bf16 v[132:135], v[12:15], v[212:215], v[132:135]\n" \
"global_load_lds_dwordx4 v229, s[44:45] offset:-3072\n" \
"v_mfma_f32_16x16x32_bf16 v[136:139], v[16:19], v[208:211], v[136:139]\n" \
"s_add_u32 s44, s44, 0x80000\n" \
"s_addc_u32 s45, s45, 0\n" \
"ds_read_b64_tr_b16 v[58:59], v233 offset:3584\n" \
"v_mfma_f32_16x16x32_bf16 v[136:139], v[20:23], v[212:215], v[136:139]\n" \
"s_cmp_eq_u32 s63, 7\n" \
"s_cselect_b32 s82, 0, s82\n" \
"ds_read_b64_tr_b16 v[60:61], v233 offset:9728\n" \
"v_mfma_f32_16x16x32_bf16 v[140:143], v[24:27], v[208:211], v[140:143]\n" \
"s_mov_b32 s75, s63\n" \
"s_sub_u32 s76, 7, s63\n" \
"ds_read_b64_tr_b16 v[62:63], v233 offset:11776\n" \
"v_mfma_f32_16x16x32_bf16 v[140:143], v[28:31], v[212:215], v[140:143]\n" \
"s_waitcnt lgkmcnt(3)\n" \
"global_load_lds_dwordx4 v228, s[42:43] offset:-2048\n" \
"s_mov_b32 s73, s95\n" \
"v_mfma_f32_16x16x32_bf16 v[172:175], v[24:27], v[216:219], v[172:175]\n" \
"s_movk_i32 s94, 0x207\n" \
"s_mov_b64 s[40:41], s[14:15]\n" \
"s_mov_b32 s80, 0\n" \
"v_mfma_f32_16x16x32_bf16 v[172:175], v[28:31], v[220:223], v[172:175]\n" \
"s_mov_b32 s81, 0\n" \
"v_add_u32 v230, 0x8000, v230\n" \
"v_add_u32 v231, 0x8000, v231\n" \
"v_mfma_f32_16x16x32_bf16 v[168:171], v[16:19], v[216:219], v[168:171]\n" \
"s_cmp_eq_u32 s63, 7\n" \
"s_cselect_b64 s[40:41], s[18:19], s[40:41]\n" \
"s_cselect_b32 s80, 12, s80\n" \
"v_mfma_f32_16x16x32_bf16 v[168:171], v[20:23], v[220:223], v[168:171]\n" \
"global_load_lds_dwordx4 v229, s[42:43] offset:-1024\n" \
"v_add_u32 v232, 0xffff4000, v232\n" \
"v_mfma_f32_16x16x32_bf16 v[164:167], v[8:11], v[216:219], v[164:167]\n" \
"s_add_u32 s42, s42, 0x80000\n" \
"s_addc_u32 s43, s43, 0\n" \
"v_add_u32 v233, 0xffff4000, v233\n" \
"v_mfma_f32_16x16x32_bf16 v[164:167], v[12:15], v[220:223], v[164:167]\n" \
"s_waitcnt vmcnt(20) lgkmcnt(0)\n" \
"v_mfma_f32_16x16x32_bf16 v[160:163], v[0:3], v[216:219], v[160:163]\n" \
"s_cmp_eq_u32 s63, 6\n" \
"s_cselect_b64 s[40:41], s[22:23], s[40:41]\n" \
"s_cselect_b32 s80, 12, s80\n" \
"v_mfma_f32_16x16x32_bf16 v[160:163], v[4:7], v[220:223], v[160:163]\n" \
"s_barrier\n" \
"v_mfma_f32_16x16x32_bf16 v[80:83], v[32:35], v[192:195], v[80:83]\n" \
"ds_read_b64_tr_b16 v[0:1], v232 offset:0\n" \
"v_mfma_f32_16x16x32_bf16 v[80:83], v[36:39], v[196:199], v[80:83]\n" \
"ds_read_b64_tr_b16 v[2:3], v232 offset:2048\n" \
"v_mfma_f32_16x16x32_bf16 v[84:87], v[40:43], v[192:195], v[84:87]\n" \
"s_add_u32 m0, s87, 102400\n" \
"ds_read_b64_tr_b16 v[4:5], v232 offset:8192\n" \
"v_mfma_f32_16x16x32_bf16 v[84:87], v[44:47], v[196:199], v[84:87]\n" \
"global_load_lds_dwordx4 v224, s[48:49] offset:-4096\n" \
"v_mfma_f32_16x16x32_bf16 v[88:91], v[48:51], v[192:195], v[88:91]\n" \
"ds_read_b64_tr_b16 v[6:7], v232 offset:10240\n" \
"v_mfma_f32_16x16x32_bf16 v[88:91], v[52:55], v[196:199], v[88:91]\n" \
"ds_read_b64_tr_b16 v[8:9], v233 offset:0\n" \
"v_mfma_f32_16x16x32_bf16 v[92:95], v[56:59], v[192:195], v[92:95]\n" \
"ds_read_b64_tr_b16 v[10:11], v233 offset:2048\n" \
"v_mfma_f32_16x16x32_bf16 v[92:95], v[60:63], v[196:199], v[92:95]\n" \
"global_load_lds_dwordx4 v225, s[48:49] offset:-3072\n" \
"v_mfma_f32_16x16x32_bf16 v[124:127], v[56:59], v[200:203], v[124:127]\n" \
"ds_read_b128 v[192:195], v230 offset:0\n" \
"v_mfma_f32_16x16x32_bf16 v[124:127], v[60:63], v[204:207], v[124:127]\n" \
"ds_read_b128 v[196:199], v231 offset:0\n" \
"v_mfma_f32_16x16x32_bf16 v[120:123], v[48:51], v[200:203], v[120:123]\n" \
"ds_read_b64_tr_b16 v[12:13], v233 offset:8192\n" \
"v_mfma_f32_16x16x32_bf16 v[120:123], v[52:55], v[204:207], v[120:123]\n" \
"global_load_lds_dwordx4 v226, s[48:49] offset:-2048\n" \
"v_mfma_f32_16x16x32_bf16 v[116:119], v[40:43], v[200:203], v[116:119]\n" \
"ds_read_b64_tr_b16 v[14:15], v233 offset:10240\n" \
"v_mfma_f32_16x16x32_bf16 v[116:119], v[44:47], v[204:207], v[116:119]\n" \
"ds_read_b64_tr_b16 v[16:17], v232 offset:512\n" \
"v_mfma_f32_16x16x32_bf16 v[112:115], v[32:35], v[200:203], v[112:115]\n" \
"ds_read_b64_tr_b16 v[18:19], v232 offset:2560\n" \
"v_mfma_f32_16x16x32_bf16 v[112:115], v[36:39], v[204:207], v[112:115]\n" \
"global_load_lds_dwordx4 v227, s[48:49] offset:-1024\n" \
"v_mfma_f32_16x16x32_bf16 v[144:147], v[32:35], v[208:211], v[144:147]\n" \
"s_add_u32 s48, s48, 0x80\n" \
"s_addc_u32 s49, s49, 0\n" \
"ds_read_b128 v[200:203], v230 offset:2048\n" \
"v_mfma_f32_16x16x32_bf16 v[144:147], v[36:39], v[212:215], v[144:147]\n" \
"ds_read_b128 v[204:207], v231 offset:2048\n" \
"v_mfma_f32_16x16x32_bf16 v[148:151], v[40:43], v[208:211], v[148:151]\n" \
"ds_read_b64_tr_b16 v[20:21], v232 offset:8704\n" \
"v_mfma_f32_16x16x32_bf16 v[148:151], v[44:47], v[212:215], v[148:151]\n" \
"global_load_lds_dwordx4 v224, s[46:47] offset:0\n" \
"v_mfma_f32_16x16x32_bf16 v[152:155], v[48:51], v[208:211], v[152:155]\n" \
"ds_read_b64_tr_b16 v[22:23], v232 offset:10752\n" \
"v_mfma_f32_16x16x32_bf16 v[152:155], v[52:55], v[212:215], v[152:155]\n" \
"ds_read_b64_tr_b16 v[24:25], v233 offset:512\n" \
"v_mfma_f32_16x16x32_bf16 v[156:159], v[56:59], v[208:211], v[156:159]\n" \
"ds_read_b64_tr_b16 v[26:27], v233 offset:2560\n" \
"v_mfma_f32_16x16x32_bf16 v[156:159], v[60:63], v[212:215], v[156:159]\n" \
"global_load_lds_dwordx4 v225, s[46:47] offset:1024\n" \
"v_mfma_f32_16x16x32_bf16 v[188:191], v[56:59], v[216:219], v[188:191]\n" \
"ds_read_b128 v[208:211], v230 offset:4096\n" \
"v_mfma_f32_16x16x32_bf16 v[188:191], v[60:63], v[220:223], v[188:191]\n" \
"ds_read_b128 v[212:215], v231 offset:4096\n" \
"v_mfma_f32_16x16x32_bf16 v[184:187], v[48:51], v[216:219], v[184:187]\n" \
"ds_read_b64_tr_b16 v[28:29], v233 offset:8704\n" \
"v_mfma_f32_16x16x32_bf16 v[184:187], v[52:55], v[220:223], v[184:187]\n" \
"Lpg4398_%=:\n" \
"s_cbranch_execz Lpg5573_%=\n" \
"global_load_lds_dwordx4 v226, s[46:47] offset:2048\n" \
"v_mfma_f32_16x16x32_bf16 v[180:183], v[40:43], v[216:219], v[180:183]\n" \
"ds_read_b64_tr_b16 v[30:31], v233 offset:10752\n" \
"v_mfma_f32_16x16x32_bf16 v[180:183], v[44:47], v[220:223], v[180:183]\n" \
"s_cmp_eq_u32 s63, 5\n" \
"s_cselect_b64 s[40:41], s[18:19], s[40:41]\n" \
"s_cselect_b32 s80, 4, s80\n" \
"v_mfma_f32_16x16x32_bf16 v[176:179], v[32:35], v[216:219], v[176:179]\n" \
"s_cmp_eq_u32 s63, 5\n" \
"s_cselect_b32 s81, 384, s81\n" \
"v_mfma_f32_16x16x32_bf16 v[176:179], v[36:39], v[220:223], v[176:179]\n" \
"s_waitcnt lgkmcnt(1)\n" \
"global_load_lds_dwordx4 v227, s[46:47] offset:3072\n" \
"v_mfma_f32_16x16x32_bf16 v[64:67], v[0:3], v[192:195], v[64:67]\n" \
"s_add_u32 s46, s46, 0x80\n" \
"s_addc_u32 s47, s47, 0\n" \
"ds_read_b128 v[216:219], v230 offset:6144\n" \
"v_mfma_f32_16x16x32_bf16 v[64:67], v[4:7], v[196:199], v[64:67]\n" \
"ds_read_b128 v[220:223], v231 offset:6144\n" \
"s_add_u32 s90, s88, 278528\n" \
"v_mfma_f32_16x16x32_bf16 v[68:71], v[8:11], v[192:195], v[68:71]\n" \
"ds_read_b64_tr_b16 v[32:33], v232 offset:1024\n" \
"v_mfma_f32_16x16x32_bf16 v[68:71], v[12:15], v[196:199], v[68:71]\n" \
"buffer_store_dwordx4 a[64:67], v249, s[56:59], s90 offen offset:0 sc1\n" \
"s_cmp_eq_u32 s63, 4\n" \
"s_cselect_b64 s[40:41], s[20:21], s[40:41]\n" \
"s_cselect_b32 s80, 8, s80\n" \
"v_mfma_f32_16x16x32_bf16 v[72:75], v[16:19], v[192:195], v[72:75]\n" \
"ds_read_b64_tr_b16 v[34:35], v232 offset:3072\n" \
"v_mfma_f32_16x16x32_bf16 v[72:75], v[20:23], v[196:199], v[72:75]\n" \
"s_waitcnt lgkmcnt(3)\n" \
"ds_read_b64_tr_b16 v[36:37], v232 offset:9216\n" \
"v_mfma_f32_16x16x32_bf16 v[76:79], v[24:27], v[192:195], v[76:79]\n" \
"ds_read_b128 a[64:67], v254\n" \
"ds_read_b64_tr_b16 v[38:39], v232 offset:11264\n" \
"v_mfma_f32_16x16x32_bf16 v[76:79], v[28:31], v[196:199], v[76:79]\n" \
"s_cmp_eq_u32 s63, 2\n" \
"s_cselect_b64 s[40:41], s[24:25], s[40:41]\n" \
"s_cselect_b32 s80, 4, s80\n" \
"v_mfma_f32_16x16x32_bf16 v[108:111], v[24:27], v[200:203], v[108:111]\n" \
"s_add_u32 s81, s81, s62\n" \
"s_lshl_b32 s81, s81, 2\n" \
"ds_read_b64_tr_b16 v[40:41], v233 offset:1024\n" \
"v_mfma_f32_16x16x32_bf16 v[108:111], v[28:31], v[204:207], v[108:111]\n" \
"s_add_u32 s81, s81, s61\n" \
"s_lshl_b32 s81, s81, 12\n" \
"ds_read_b64_tr_b16 v[42:43], v233 offset:3072\n" \
"v_mfma_f32_16x16x32_bf16 v[104:107], v[16:19], v[200:203], v[104:107]\n" \
"s_add_u32 s40, s40, s81\n" \
"s_addc_u32 s41, s41, 0\n" \
"ds_read_b64_tr_b16 v[44:45], v233 offset:9216\n" \
"v_mfma_f32_16x16x32_bf16 v[104:107], v[20:23], v[204:207], v[104:107]\n" \
"s_min_u32 s81, s80, 1\n" \
"s_lshl_b32 s81, s81, 2\n" \
"v_mul_u32_u24 v252, s81, v248\n" \
"v_mfma_f32_16x16x32_bf16 v[100:103], v[8:11], v[200:203], v[100:103]\n" \
"ds_read_b128 a[68:71], v254\n" \
"ds_read_b64_tr_b16 v[46:47], v233 offset:11264\n" \
"v_mfma_f32_16x16x32_bf16 v[100:103], v[12:15], v[204:207], v[100:103]\n" \
"ds_read_b64_tr_b16 v[48:49], v232 offset:1536\n" \
"v_mfma_f32_16x16x32_bf16 v[96:99], v[0:3], v[200:203], v[96:99]\n" \
"s_add_u32 m0, s95, 53248\n" \
"ds_read_b64_tr_b16 v[50:51], v232 offset:3584\n" \
"v_mfma_f32_16x16x32_bf16 v[96:99], v[4:7], v[204:207], v[96:99]\n" \
"global_load_lds_dwordx4 v228, s[44:45] offset:-4096\n" \
"v_mfma_f32_16x16x32_bf16 v[128:131], v[0:3], v[208:211], v[128:131]\n" \
"ds_read_b64_tr_b16 v[52:53], v232 offset:9728\n" \
"v_mfma_f32_16x16x32_bf16 v[128:131], v[4:7], v[212:215], v[128:131]\n" \
"ds_read_b64_tr_b16 v[54:55], v232 offset:11776\n" \
"v_mfma_f32_16x16x32_bf16 v[132:135], v[8:11], v[208:211], v[132:135]\n" \
"ds_read_b64_tr_b16 v[56:57], v233 offset:1536\n" \
"v_mfma_f32_16x16x32_bf16 v[132:135], v[12:15], v[212:215], v[132:135]\n" \
"global_load_lds_dwordx4 v229, s[44:45] offset:-3072\n" \
"v_mfma_f32_16x16x32_bf16 v[136:139], v[16:19], v[208:211], v[136:139]\n" \
"ds_read_b64_tr_b16 v[58:59], v233 offset:3584\n" \
"v_mfma_f32_16x16x32_bf16 v[136:139], v[20:23], v[212:215], v[136:139]\n" \
"ds_read_b64_tr_b16 v[60:61], v233 offset:9728\n" \
"v_mfma_f32_16x16x32_bf16 v[140:143], v[24:27], v[208:211], v[140:143]\n" \
"ds_read_b64_tr_b16 v[62:63], v233 offset:11776\n" \
"v_mfma_f32_16x16x32_bf16 v[140:143], v[28:31], v[212:215], v[140:143]\n" \
"s_waitcnt lgkmcnt(3)\n" \
"global_load_lds_dwordx4 v228, s[42:43] offset:-2048\n" \
"v_mfma_f32_16x16x32_bf16 v[172:175], v[24:27], v[216:219], v[172:175]\n" \
"v_add_u32 v230, 0xffff0000, v230\n" \
"v_add_u32 v231, 0xffff0000, v231\n" \
"v_mfma_f32_16x16x32_bf16 v[172:175], v[28:31], v[220:223], v[172:175]\n" \
"v_add_u32 v232, 0x4000, v232\n" \
"v_add_u32 v233, 0x4000, v233\n" \
"v_mfma_f32_16x16x32_bf16 v[168:171], v[16:19], v[216:219], v[168:171]\n" \
"v_mfma_f32_16x16x32_bf16 v[168:171], v[20:23], v[220:223], v[168:171]\n" \
"global_load_lds_dwordx4 v229, s[42:43] offset:-1024\n" \
"v_mfma_f32_16x16x32_bf16 v[164:167], v[8:11], v[216:219], v[164:167]\n" \
"v_mfma_f32_16x16x32_bf16 v[164:167], v[12:15], v[220:223], v[164:167]\n" \
"s_waitcnt vmcnt(20) lgkmcnt(0)\n" \
"v_mfma_f32_16x16x32_bf16 v[160:163], v[0:3], v[216:219], v[160:163]\n" \
"v_mfma_f32_16x16x32_bf16 v[160:163], v[4:7], v[220:223], v[160:163]\n" \
"s_barrier\n" \
"v_mfma_f32_16x16x32_bf16 v[80:83], v[32:35], v[192:195], v[80:83]\n" \
"s_waitcnt vmcnt(17)\n" \
"ds_read_b64_tr_b16 v[0:1], v232 offset:0\n" \
"v_cmp_ne_u32 vcc, s60, v244\n" \
"v_mfma_f32_16x16x32_bf16 v[80:83], v[36:39], v[196:199], v[80:83]\n" \
"ds_read_b64_tr_b16 v[2:3], v232 offset:2048\n" \
"v_mfma_f32_16x16x32_bf16 v[84:87], v[40:43], v[192:195], v[84:87]\n" \
"s_add_u32 m0, s87, 135168\n" \
"ds_read_b64_tr_b16 v[4:5], v232 offset:8192\n" \
"v_mfma_f32_16x16x32_bf16 v[84:87], v[44:47], v[196:199], v[84:87]\n" \
"s_and_b32 vcc_hi, vcc_lo, 255\n" \
"s_cbranch_scc1 Lpl1_%=\n" \
"Lpr1_%=:\n" \
"global_load_lds_dwordx4 v224, s[48:49] offset:-4096\n" \
"v_mfma_f32_16x16x32_bf16 v[88:91], v[48:51], v[192:195], v[88:91]\n" \
"s_add_u32 s79, s91, 12288\n" \
"ds_read_b64_tr_b16 v[6:7], v232 offset:10240\n" \
"v_mfma_f32_16x16x32_bf16 v[88:91], v[52:55], v[196:199], v[88:91]\n" \
"ds_read_b64_tr_b16 v[8:9], v233 offset:0\n" \
"v_mfma_f32_16x16x32_bf16 v[92:95], v[56:59], v[192:195], v[92:95]\n" \
"ds_read_b64_tr_b16 v[10:11], v233 offset:2048\n" \
"v_mfma_f32_16x16x32_bf16 v[92:95], v[60:63], v[196:199], v[92:95]\n" \
"global_load_lds_dwordx4 v225, s[48:49] offset:-3072\n" \
"v_mfma_f32_16x16x32_bf16 v[124:127], v[56:59], v[200:203], v[124:127]\n" \
"ds_read_b128 v[192:195], v230 offset:0\n" \
"v_mfma_f32_16x16x32_bf16 v[124:127], v[60:63], v[204:207], v[124:127]\n" \
"ds_read_b128 v[196:199], v231 offset:0\n" \
"v_mfma_f32_16x16x32_bf16 v[120:123], v[48:51], v[200:203], v[120:123]\n" \
"ds_read_b64_tr_b16 v[12:13], v233 offset:8192\n" \
"v_mfma_f32_16x16x32_bf16 v[120:123], v[52:55], v[204:207], v[120:123]\n" \
"global_load_lds_dwordx4 v226, s[48:49] offset:-2048\n" \
"v_mfma_f32_16x16x32_bf16 v[116:119], v[40:43], v[200:203], v[116:119]\n" \
"ds_read_b64_tr_b16 v[14:15], v233 offset:10240\n" \
"v_mfma_f32_16x16x32_bf16 v[116:119], v[44:47], v[204:207], v[116:119]\n" \
"ds_read_b64_tr_b16 v[16:17], v232 offset:512\n" \
"v_mfma_f32_16x16x32_bf16 v[112:115], v[32:35], v[200:203], v[112:115]\n" \
"ds_read_b64_tr_b16 v[18:19], v232 offset:2560\n" \
"v_mfma_f32_16x16x32_bf16 v[112:115], v[36:39], v[204:207], v[112:115]\n" \
"global_load_lds_dwordx4 v227, s[48:49] offset:-1024\n" \
"v_mfma_f32_16x16x32_bf16 v[144:147], v[32:35], v[208:211], v[144:147]\n" \
"ds_read_b128 v[200:203], v230 offset:2048\n" \
"v_mfma_f32_16x16x32_bf16 v[144:147], v[36:39], v[212:215], v[144:147]\n" \
"ds_read_b128 v[204:207], v231 offset:2048\n" \
"v_mfma_f32_16x16x32_bf16 v[148:151], v[40:43], v[208:211], v[148:151]\n" \
"ds_read_b64_tr_b16 v[20:21], v232 offset:8704\n" \
"v_mfma_f32_16x16x32_bf16 v[148:151], v[44:47], v[212:215], v[148:151]\n" \
"global_load_lds_dwordx4 v224, s[46:47] offset:0\n" \
"v_mfma_f32_16x16x32_bf16 v[152:155], v[48:51], v[208:211], v[152:155]\n" \
"ds_read_b64_tr_b16 v[22:23], v232 offset:10752\n" \
"v_mfma_f32_16x16x32_bf16 v[152:155], v[52:55], v[212:215], v[152:155]\n" \
"ds_read_b64_tr_b16 v[24:25], v233 offset:512\n" \
"v_mfma_f32_16x16x32_bf16 v[156:159], v[56:59], v[208:211], v[156:159]\n" \
"ds_read_b64_tr_b16 v[26:27], v233 offset:2560\n" \
"v_mfma_f32_16x16x32_bf16 v[156:159], v[60:63], v[212:215], v[156:159]\n" \
"global_load_lds_dwordx4 v225, s[46:47] offset:1024\n" \
"v_mfma_f32_16x16x32_bf16 v[188:191], v[56:59], v[216:219], v[188:191]\n" \
"ds_read_b128 v[208:211], v230 offset:4096\n" \
"v_mfma_f32_16x16x32_bf16 v[188:191], v[60:63], v[220:223], v[188:191]\n" \
"ds_read_b128 v[212:215], v231 offset:4096\n" \
"v_mfma_f32_16x16x32_bf16 v[184:187], v[48:51], v[216:219], v[184:187]\n" \
"ds_read_b64_tr_b16 v[28:29], v233 offset:8704\n" \
"v_mfma_f32_16x16x32_bf16 v[184:187], v[52:55], v[220:223], v[184:187]\n" \
"global_load_lds_dwordx4 v226, s[46:47] offset:2048\n" \
"v_mfma_f32_16x16x32_bf16 v[180:183], v[40:43], v[216:219], v[180:183]\n" \
"ds_read_b64_tr_b16 v[30:31], v233 offset:10752\n" \
"v_mfma_f32_16x16x32_bf16 v[180:183], v[44:47], v[220:223], v[180:183]\n" \
"v_mfma_f32_16x16x32_bf16 v[176:179], v[32:35], v[216:219], v[176:179]\n" \
"v_mfma_f32_16x16x32_bf16 v[176:179], v[36:39], v[220:223], v[176:179]\n" \
"s_waitcnt lgkmcnt(1)\n" \
"global_load_lds_dwordx4 v227, s[46:47] offset:3072\n" \
"v_mfma_f32_16x16x32_bf16 v[64:67], v[0:3], v[192:195], v[64:67]\n" \
"ds_read_b128 v[216:219], v230 offset:6144\n" \
"v_mfma_f32_16x16x32_bf16 v[64:67], v[4:7], v[196:199], v[64:67]\n" \
"ds_read_b128 v[220:223], v231 offset:6144\n" \
"v_mfma_f32_16x16x32_bf16 v[68:71], v[8:11], v[192:195], v[68:71]\n" \
"ds_read_b128 v[240:243], v254\n" \
"ds_read_b64_tr_b16 v[32:33], v232 offset:1024\n" \
"v_mfma_f32_16x16x32_bf16 v[68:71], v[12:15], v[196:199], v[68:71]\n" \
"buffer_store_dwordx4 a[96:99], v249, s[56:59], s90 offen offset:256 sc1\n" \
"v_and_b32 v238, 0xf0, v248\n" \
"v_mov_b32 v239, 0x3f803f80\n" \
"v_cmp_eq_u32 vcc, 32, v238\n" \
"v_cmp_eq_u32 s[64:65], 48, v238\n" \
"v_mfma_f32_16x16x32_bf16 v[72:75], v[16:19], v[192:195], v[72:75]\n" \
"ds_read_b64_tr_b16 v[34:35], v232 offset:3072\n" \
"v_mov_b32 v244, 0\n" \
"v_mfma_f32_16x16x32_bf16 v[72:75], v[20:23], v[196:199], v[72:75]\n" \
"s_waitcnt lgkmcnt(4)\n" \
"ds_read_b64_tr_b16 v[36:37], v232 offset:9216\n" \
"v_mov_b32 v245, 0\n" \
"v_mfma_f32_16x16x32_bf16 v[76:79], v[24:27], v[192:195], v[76:79]\n" \
"ds_read_b128 a[96:99], v254\n" \
"ds_read_b64_tr_b16 v[38:39], v232 offset:11264\n" \
"v_mfma_f32_16x16x32_bf16 v[76:79], v[28:31], v[196:199], v[76:79]\n" \
"buffer_store_dwordx4 a[72:75], v249, s[56:59], s90 offen offset:2048 sc1\n" \
"ds_read_b128 a[100:103], v254\n" \
"v_cndmask_b32 v238, 0, v239, vcc\n" \
"v_cndmask_b32 v239, 0, v239, s[64:65]\n" \
"v_mfma_f32_16x16x32_bf16 v[108:111], v[24:27], v[200:203], v[108:111]\n" \
"ds_read_b64_tr_b16 v[40:41], v233 offset:1024\n" \
"v_mfma_f32_16x16x32_bf16 v[108:111], v[28:31], v[204:207], v[108:111]\n" \
"ds_read_b64_tr_b16 v[42:43], v233 offset:3072\n" \
"v_mfma_f32_16x16x32_bf16 v[104:107], v[16:19], v[200:203], v[104:107]\n" \
"ds_read_b128 a[72:75], v254\n" \
"ds_read_b64_tr_b16 v[44:45], v233 offset:9216\n" \
"v_mfma_f32_16x16x32_bf16 v[104:107], v[20:23], v[204:207], v[104:107]\n" \
"ds_read_b128 a[76:79], v254\n" \
"v_mfma_f32_16x16x32_bf16 v[100:103], v[8:11], v[200:203], v[100:103]\n" \
"ds_read_b64_tr_b16 v[46:47], v233 offset:11264\n" \
"v_mfma_f32_16x16x32_bf16 v[100:103], v[12:15], v[204:207], v[100:103]\n" \
"ds_read_b64_tr_b16 v[48:49], v232 offset:1536\n" \
"v_mfma_f32_16x16x32_bf16 v[96:99], v[0:3], v[200:203], v[96:99]\n" \
"s_add_u32 m0, s95, 0\n" \
"ds_read_b64_tr_b16 v[50:51], v232 offset:3584\n" \
"v_mfma_f32_16x16x32_bf16 v[96:99], v[4:7], v[204:207], v[96:99]\n" \
"global_load_lds_dwordx4 v250, s[36:37] offset:0 sc1\n" \
"v_mfma_f32_16x16x32_bf16 v[128:131], v[0:3], v[208:211], v[128:131]\n" \
"ds_read_b64_tr_b16 v[52:53], v232 offset:9728\n" \
"v_mfma_f32_16x16x32_bf16 v[128:131], v[4:7], v[212:215], v[128:131]\n" \
"ds_read_b64_tr_b16 v[54:55], v232 offset:11776\n" \
"v_mfma_f32_16x16x32_bf16 v[132:135], v[8:11], v[208:211], v[132:135]\n" \
"ds_read_b64_tr_b16 v[56:57], v233 offset:1536\n" \
"v_mfma_f32_16x16x32_bf16 v[132:135], v[12:15], v[212:215], v[132:135]\n" \
"global_load_lds_dwordx4 v250, s[36:37] offset:1024 sc1\n" \
"v_mfma_f32_16x16x32_bf16 v[136:139], v[16:19], v[208:211], v[136:139]\n" \
"ds_read_b64_tr_b16 v[58:59], v233 offset:3584\n" \
"v_mfma_f32_16x16x32_bf16 v[136:139], v[20:23], v[212:215], v[136:139]\n" \
"ds_read_b64_tr_b16 v[60:61], v233 offset:9728\n" \
"v_mfma_f32_16x16x32_bf16 v[140:143], v[24:27], v[208:211], v[140:143]\n" \
"ds_read_b64_tr_b16 v[62:63], v233 offset:11776\n" \
"v_mfma_f32_16x16x32_bf16 v[140:143], v[28:31], v[212:215], v[140:143]\n" \
"s_waitcnt lgkmcnt(3)\n" \
"global_load_lds_dwordx4 v250, s[36:37] offset:2048 sc1\n" \
"v_mfma_f32_16x16x32_bf16 v[172:175], v[24:27], v[216:219], v[172:175]\n" \
"v_add_u32 v230, 0x8000, v230\n" \
"v_add_u32 v231, 0x8000, v231\n" \
"v_mfma_f32_16x16x32_bf16 v[172:175], v[28:31], v[220:223], v[172:175]\n" \
"v_add_u32 v232, 0x4000, v232\n" \
"v_add_u32 v233, 0x4000, v233\n" \
"v_mfma_f32_16x16x32_bf16 v[168:171], v[16:19], v[216:219], v[168:171]\n" \
"v_mfma_f32_16x16x32_bf16 v[168:171], v[20:23], v[220:223], v[168:171]\n" \
"global_load_lds_dwordx4 v250, s[36:37] offset:3072 sc1\n" \
"v_mfma_f32_16x16x32_bf16 v[164:167], v[8:11], v[216:219], v[164:167]\n" \
"s_add_u32 s36, s36, 17408\n" \
"v_mfma_f32_16x16x32_bf16 v[164:167], v[12:15], v[220:223], v[164:167]\n" \
"s_waitcnt vmcnt(19) lgkmcnt(0)\n" \
"v_mfma_f32_16x16x32_bf16 v[160:163], v[0:3], v[216:219], v[160:163]\n" \
"v_mfma_f32_16x16x32_bf16 v[160:163], v[4:7], v[220:223], v[160:163]\n" \
"s_barrier\n" \
"v_mfma_f32_16x16x32_bf16 v[80:83], v[32:35], v[192:195], v[80:83]\n" \
"ds_read_b64_tr_b16 v[0:1], v232 offset:0\n" \
"v_mfma_f32_16x16x32_bf16 v[80:83], v[36:39], v[196:199], v[80:83]\n" \
"ds_read_b64_tr_b16 v[2:3], v232 offset:2048\n" \
"v_mfma_f32_16x16x32_bf16 v[84:87], v[40:43], v[192:195], v[84:87]\n" \
"s_add_u32 m0, s95, 16384\n" \
"ds_read_b64_tr_b16 v[4:5], v232 offset:8192\n" \
"v_mfma_f32_16x16x32_bf16 v[84:87], v[44:47], v[196:199], v[84:87]\n" \
"global_load_lds_dwordx4 v250, s[36:37] offset:0 sc1\n" \
"v_mfma_f32_16x16x32_bf16 v[88:91], v[48:51], v[192:195], v[88:91]\n" \
"ds_read_b64_tr_b16 v[6:7], v232 offset:10240\n" \
"v_mfma_f32_16x16x32_bf16 v[88:91], v[52:55], v[196:199], v[88:91]\n" \
"ds_read_b64_tr_b16 v[8:9], v233 offset:0\n" \
"v_mfma_f32_16x16x32_bf16 v[92:95], v[56:59], v[192:195], v[92:95]\n" \
"ds_read_b64_tr_b16 v[10:11], v233 offset:2048\n" \
"v_mfma_f32_16x16x32_bf16 v[92:95], v[60:63], v[196:199], v[92:95]\n" \
"global_load_lds_dwordx4 v250, s[36:37] offset:1024 sc1\n" \
"v_mfma_f32_16x16x32_bf16 v[124:127], v[56:59], v[200:203], v[124:127]\n" \
"ds_read_b128 v[192:195], v230 offset:0\n" \
"v_mfma_f32_16x16x32_bf16 v[124:127], v[60:63], v[204:207], v[124:127]\n" \
"ds_read_b128 v[196:199], v231 offset:0\n" \
"v_mfma_f32_16x16x32_bf16 v[120:123], v[48:51], v[200:203], v[120:123]\n" \
"ds_read_b64_tr_b16 v[12:13], v233 offset:8192\n" \
"v_mfma_f32_16x16x32_bf16 v[120:123], v[52:55], v[204:207], v[120:123]\n" \
"global_load_lds_dwordx4 v250, s[36:37] offset:2048 sc1\n" \
"v_mfma_f32_16x16x32_bf16 v[116:119], v[40:43], v[200:203], v[116:119]\n" \
"ds_read_b64_tr_b16 v[14:15], v233 offset:10240\n" \
"v_mfma_f32_16x16x32_bf16 v[116:119], v[44:47], v[204:207], v[116:119]\n" \
"ds_read_b64_tr_b16 v[16:17], v232 offset:512\n" \
"v_mfma_f32_16x16x32_bf16 v[112:115], v[32:35], v[200:203], v[112:115]\n" \
"ds_read_b64_tr_b16 v[18:19], v232 offset:2560\n" \
"v_mfma_f32_16x16x32_bf16 v[112:115], v[36:39], v[204:207], v[112:115]\n" \
"global_load_lds_dwordx4 v250, s[36:37] offset:3072 sc1\n" \
"v_mfma_f32_16x16x32_bf16 v[144:147], v[32:35], v[208:211], v[144:147]\n" \
"ds_read_b128 v[200:203], v230 offset:2048\n" \
"s_add_u32 s36, s36, 17408\n" \
"v_mfma_f32_16x16x32_bf16 v[144:147], v[36:39], v[212:215], v[144:147]\n" \
"ds_read_b128 v[204:207], v231 offset:2048\n" \
"v_mfma_f32_16x16x32_bf16 v[148:151], v[40:43], v[208:211], v[148:151]\n" \
"ds_read_b64_tr_b16 v[20:21], v232 offset:8704\n" \
"v_mfma_f32_16x16x32_bf16 v[148:151], v[44:47], v[212:215], v[148:151]\n" \
"buffer_store_dwordx4 a[104:107], v249, s[56:59], s90 offen offset:2304 sc1\n" \
"v_mfma_f32_16x16x32_bf16 v[152:155], v[48:51], v[208:211], v[152:155]\n" \
"s_add_u32 s90, s89, 278528\n" \
"ds_read_b64_tr_b16 v[22:23], v232 offset:10752\n" \
"v_mfma_f32_16x16x32_bf16 v[152:155], v[52:55], v[212:215], v[152:155]\n" \
"ds_read_b64_tr_b16 v[24:25], v233 offset:512\n" \
"v_mfma_f32_16x16x32_bf16 v[156:159], v[56:59], v[208:211], v[156:159]\n" \
"ds_read_b128 a[104:107], v254\n" \
"ds_read_b64_tr_b16 v[26:27], v233 offset:2560\n" \
"v_mfma_f32_16x16x32_bf16 v[156:159], v[60:63], v[212:215], v[156:159]\n" \
"buffer_store_dwordx4 a[80:83], v248, s[56:59], s90 offen offset:0 sc1\n" \
"ds_read_b128 a[108:111], v254\n" \
"v_mfma_f32_16x16x32_bf16 v[188:191], v[56:59], v[216:219], v[188:191]\n" \
"ds_read_b128 v[208:211], v230 offset:4096\n" \
"v_mfma_f32_16x16x32_bf16 v[188:191], v[60:63], v[220:223], v[188:191]\n" \
"ds_read_b128 v[212:215], v231 offset:4096\n" \
"v_mfma_f32_16x16x32_bf16 v[184:187], v[48:51], v[216:219], v[184:187]\n" \
"ds_read_b128 a[80:83], v254\n" \
"ds_read_b64_tr_b16 v[28:29], v233 offset:8704\n" \
"v_mfma_f32_16x16x32_bf16 v[184:187], v[52:55], v[220:223], v[184:187]\n" \
"buffer_store_dwordx4 a[84:87], v248, s[56:59], s90 offen offset:1024 sc1\n" \
"ds_read_b128 a[112:115], v254\n" \
"v_mfma_f32_16x16x32_bf16 v[180:183], v[40:43], v[216:219], v[180:183]\n" \
"ds_read_b64_tr_b16 v[30:31], v233 offset:10752\n" \
"v_mfma_f32_16x16x32_bf16 v[180:183], v[44:47], v[220:223], v[180:183]\n" \
"v_mfma_f32_16x16x32_bf16 v[176:179], v[32:35], v[216:219], v[176:179]\n" \
"ds_read_b128 a[84:87], v254\n" \
"v_mfma_f32_16x16x32_bf16 v[176:179], v[36:39], v[220:223], v[176:179]\n" \
"s_waitcnt lgkmcnt(3)\n" \
"ds_read_b128 a[116:119], v254\n" \
"v_mfma_f32_16x16x32_bf16 v[64:67], v[0:3], v[192:195], v[64:67]\n" \
"ds_read_b128 v[216:219], v230 offset:6144\n" \
"v_mfma_f32_16x16x32_bf16 v[64:67], v[4:7], v[196:199], v[64:67]\n" \
"ds_read_b128 v[220:223], v231 offset:6144\n" \
"v_mfma_f32_16x16x32_bf16 v[68:71], v[8:11], v[192:195], v[68:71]\n" \
"ds_read_b64_tr_b16 v[32:33], v232 offset:1024\n" \
"v_mfma_f32_16x16x32_bf16 v[68:71], v[12:15], v[196:199], v[68:71]\n" \
"v_mfma_f32_16x16x32_bf16 v[72:75], v[16:19], v[192:195], v[72:75]\n" \
"ds_read_b64_tr_b16 v[34:35], v232 offset:3072\n" \
"v_mfma_f32_16x16x32_bf16 v[72:75], v[20:23], v[196:199], v[72:75]\n" \
"s_waitcnt lgkmcnt(3)\n" \
"ds_read_b64_tr_b16 v[36:37], v232 offset:9216\n" \
"v_mfma_f32_16x16x32_bf16 v[76:79], v[24:27], v[192:195], v[76:79]\n" \
"ds_read_b64_tr_b16 v[38:39], v232 offset:11264\n" \
"v_mfma_f32_16x16x32_bf16 v[76:79], v[28:31], v[196:199], v[76:79]\n" \
"v_mfma_f32_16x16x32_bf16 v[108:111], v[24:27], v[200:203], v[108:111]\n" \
"ds_read_b64_tr_b16 v[40:41], v233 offset:1024\n" \
"v_mfma_f32_16x16x32_bf16 v[108:111], v[28:31], v[204:207], v[108:111]\n" \
"ds_read_b64_tr_b16 v[42:43], v233 offset:3072\n" \
"v_mfma_f32_16x16x32_bf16 v[104:107], v[16:19], v[200:203], v[104:107]\n" \
"ds_read_b64_tr_b16 v[44:45], v233 offset:9216\n" \
"v_mfma_f32_16x16x32_bf16 v[104:107], v[20:23], v[204:207], v[104:107]\n" \
"v_mfma_f32_16x16x32_bf16 v[100:103], v[8:11], v[200:203], v[100:103]\n" \
"ds_read_b64_tr_b16 v[46:47], v233 offset:11264\n" \
"v_mfma_f32_16x16x32_bf16 v[100:103], v[12:15], v[204:207], v[100:103]\n" \
"ds_read_b64_tr_b16 v[48:49], v232 offset:1536\n" \
"v_mfma_f32_16x16x32_bf16 v[96:99], v[0:3], v[200:203], v[96:99]\n" \
"ds_read_b64_tr_b16 v[50:51], v232 offset:3584\n" \
"v_mfma_f32_16x16x32_bf16 v[96:99], v[4:7], v[204:207], v[96:99]\n" \
"v_mfma_f32_16x16x32_bf16 v[128:131], v[0:3], v[208:211], v[128:131]\n" \
"ds_read_b64_tr_b16 v[52:53], v232 offset:9728\n" \
"v_mfma_f32_16x16x32_bf16 v[128:131], v[4:7], v[212:215], v[128:131]\n" \
"ds_read_b64_tr_b16 v[54:55], v232 offset:11776\n" \
"v_mfma_f32_16x16x32_bf16 v[132:135], v[8:11], v[208:211], v[132:135]\n" \
"ds_read_b64_tr_b16 v[56:57], v233 offset:1536\n" \
"v_mfma_f32_16x16x32_bf16 v[132:135], v[12:15], v[212:215], v[132:135]\n" \
"v_mfma_f32_16x16x32_bf16 v[136:139], v[16:19], v[208:211], v[136:139]\n" \
"ds_read_b64_tr_b16 v[58:59], v233 offset:3584\n" \
"v_mfma_f32_16x16x32_bf16 v[136:139], v[20:23], v[212:215], v[136:139]\n" \
"ds_read_b64_tr_b16 v[60:61], v233 offset:9728\n" \
"v_mfma_f32_16x16x32_bf16 v[140:143], v[24:27], v[208:211], v[140:143]\n" \
"ds_read_b64_tr_b16 v[62:63], v233 offset:11776\n" \
"v_mfma_f32_16x16x32_bf16 v[140:143], v[28:31], v[212:215], v[140:143]\n" \
"s_waitcnt lgkmcnt(3)\n" \
"v_mfma_f32_16x16x32_bf16 v[172:175], v[24:27], v[216:219], v[172:175]\n" \
"v_add_u32 v230, 0x8000, v230\n" \
"v_add_u32 v231, 0x8000, v231\n" \
"v_mfma_f32_16x16x32_bf16 v[172:175], v[28:31], v[220:223], v[172:175]\n" \
"v_add_u32 v232, 0x4000, v232\n" \
"v_add_u32 v233, 0x4000, v233\n" \
"v_mfma_f32_16x16x32_bf16 v[168:171], v[16:19], v[216:219], v[168:171]\n" \
"v_mfma_f32_16x16x32_bf16 v[168:171], v[20:23], v[220:223], v[168:171]\n" \
"v_mfma_f32_16x16x32_bf16 v[164:167], v[8:11], v[216:219], v[164:167]\n" \
"v_mfma_f32_16x16x32_bf16 v[164:167], v[12:15], v[220:223], v[164:167]\n" \
"s_waitcnt vmcnt(13) lgkmcnt(0)\n" \
"v_mfma_f32_16x16x32_bf16 v[160:163], v[0:3], v[216:219], v[160:163]\n" \
"v_mfma_f32_16x16x32_bf16 v[160:163], v[4:7], v[220:223], v[160:163]\n" \
"s_barrier\n" \
"v_mfma_f32_16x16x32_bf16 v[80:83], v[32:35], v[192:195], v[80:83]\n" \
"ds_read_b64_tr_b16 v[0:1], v232 offset:0\n" \
"v_mfma_f32_16x16x32_bf16 v[80:83], v[36:39], v[196:199], v[80:83]\n" \
"ds_read_b64_tr_b16 v[2:3], v232 offset:2048\n" \
"v_mfma_f32_16x16x32_bf16 v[84:87], v[40:43], v[192:195], v[84:87]\n" \
"s_add_u32 m0, s95, 32768\n" \
"ds_read_b64_tr_b16 v[4:5], v232 offset:8192\n" \
"v_mfma_f32_16x16x32_bf16 v[84:87], v[44:47], v[196:199], v[84:87]\n" \
"global_load_lds_dwordx4 v250, s[36:37] offset:0 sc1\n" \
"v_mfma_f32_16x16x32_bf16 v[88:91], v[48:51], v[192:195], v[88:91]\n" \
"ds_read_b64_tr_b16 v[6:7], v232 offset:10240\n" \
"v_mfma_f32_16x16x32_bf16 v[88:91], v[52:55], v[196:199], v[88:91]\n" \
"ds_read_b64_tr_b16 v[8:9], v233 offset:0\n" \
"v_mfma_f32_16x16x32_bf16 v[92:95], v[56:59], v[192:195], v[92:95]\n" \
"ds_read_b64_tr_b16 v[10:11], v233 offset:2048\n" \
"v_mfma_f32_16x16x32_bf16 v[92:95], v[60:63], v[196:199], v[92:95]\n" \
"global_load_lds_dwordx4 v250, s[36:37] offset:1024 sc1\n" \
"v_mfma_f32_16x16x32_bf16 v[124:127], v[56:59], v[200:203], v[124:127]\n" \
"ds_read_b128 v[192:195], v230 offset:0\n" \
"v_mfma_f32_16x16x32_bf16 v[124:127], v[60:63], v[204:207], v[124:127]\n" \
"ds_read_b128 v[196:199], v231 offset:0\n" \
"v_mfma_f32_16x16x32_bf16 v[120:123], v[48:51], v[200:203], v[120:123]\n" \
"ds_read_b64_tr_b16 v[12:13], v233 offset:8192\n" \
"v_mfma_f32_16x16x32_bf16 v[120:123], v[52:55], v[204:207], v[120:123]\n" \
"global_load_lds_dwordx4 v250, s[36:37] offset:2048 sc1\n" \
"v_mfma_f32_16x16x32_bf16 v[116:119], v[40:43], v[200:203], v[116:119]\n" \
"ds_read_b64_tr_b16 v[14:15], v233 offset:10240\n" \
"v_mfma_f32_16x16x32_bf16 v[116:119], v[44:47], v[204:207], v[116:119]\n" \
"ds_read_b64_tr_b16 v[16:17], v232 offset:512\n" \
"v_mfma_f32_16x16x32_bf16 v[112:115], v[32:35], v[200:203], v[112:115]\n" \
"ds_read_b64_tr_b16 v[18:19], v232 offset:2560\n" \
"v_mfma_f32_16x16x32_bf16 v[112:115], v[36:39], v[204:207], v[112:115]\n" \
"global_load_lds_dwordx4 v250, s[36:37] offset:3072 sc1\n" \
"v_mfma_f32_16x16x32_bf16 v[144:147], v[32:35], v[208:211], v[144:147]\n" \
"ds_read_b128 v[200:203], v230 offset:2048\n" \
"s_add_u32 s36, s36, 17408\n" \
"v_mfma_f32_16x16x32_bf16 v[144:147], v[36:39], v[212:215], v[144:147]\n" \
"ds_read_b128 v[204:207], v231 offset:2048\n" \
"v_mfma_f32_16x16x32_bf16 v[148:151], v[40:43], v[208:211], v[148:151]\n" \
"ds_read_b64_tr_b16 v[20:21], v232 offset:8704\n" \
"v_mfma_f32_16x16x32_bf16 v[148:151], v[44:47], v[212:215], v[148:151]\n" \
"buffer_store_dwordx4 a[88:91], v248, s[56:59], s90 offen offset:2048 sc1\n" \
"v_mfma_f32_16x16x32_bf16 v[152:155], v[48:51], v[208:211], v[152:155]\n" \
"ds_read_b64_tr_b16 v[22:23], v232 offset:10752\n" \
"v_mfma_f32_16x16x32_bf16 v[152:155], v[52:55], v[212:215], v[152:155]\n" \
"ds_read_b64_tr_b16 v[24:25], v233 offset:512\n" \
"v_mfma_f32_16x16x32_bf16 v[156:159], v[56:59], v[208:211], v[156:159]\n" \
"ds_read_b128 a[88:91], v254\n" \
"ds_read_b64_tr_b16 v[26:27], v233 offset:2560\n" \
"v_mfma_f32_16x16x32_bf16 v[156:159], v[60:63], v[212:215], v[156:159]\n" \
"buffer_store_dwordx4 a[92:95], v248, s[56:59], s90 offen offset:3072 sc1\n" \
"v_mfma_f32_16x16x32_bf16 v[188:191], v[56:59], v[216:219], v[188:191]\n" \
"ds_read_b128 v[208:211], v230 offset:4096\n" \
"s_add_u32 s90, s88, 417792\n" \
"v_mfma_f32_16x16x32_bf16 v[188:191], v[60:63], v[220:223], v[188:191]\n" \
"ds_read_b128 v[212:215], v231 offset:4096\n" \
"v_mfma_f32_16x16x32_bf16 v[184:187], v[48:51], v[216:219], v[184:187]\n" \
"ds_read_b128 a[92:95], v254\n" \
"ds_read_b64_tr_b16 v[28:29], v233 offset:8704\n" \
"v_mfma_f32_16x16x32_bf16 v[184:187], v[52:55], v[220:223], v[184:187]\n" \
"buffer_store_dwordx4 a[0:3], v249, s[56:59], s90 offen offset:0 sc1\n" \
"v_accvgpr_write_b32 a120, v238\n" \
"v_accvgpr_write_b32 a121, v238\n" \
"v_accvgpr_write_b32 a122, v238\n" \
"v_accvgpr_write_b32 a123, v238\n" \
"v_mfma_f32_16x16x32_bf16 v[180:183], v[40:43], v[216:219], v[180:183]\n" \
"ds_read_b64_tr_b16 v[30:31], v233 offset:10752\n" \
"v_accvgpr_write_b32 a124, v239\n" \
"v_mfma_f32_16x16x32_bf16 v[180:183], v[44:47], v[220:223], v[180:183]\n" \
"v_accvgpr_write_b32 a125, v239\n" \
"v_accvgpr_write_b32 a126, v239\n" \
"v_mfma_f32_16x16x32_bf16 v[176:179], v[32:35], v[216:219], v[176:179]\n" \
"ds_read_b128 a[0:3], v254\n" \
"v_accvgpr_write_b32 a127, v239\n" \
"v_mfma_f32_16x16x32_bf16 v[176:179], v[36:39], v[220:223], v[176:179]\n" \
"s_waitcnt lgkmcnt(2)\n" \
"ds_read_b128 a[4:7], v254\n" \
"v_mfma_f32_16x16x32_bf16 v[64:67], v[0:3], v[192:195], v[64:67]\n" \
"ds_read_b128 v[216:219], v230 offset:6144\n" \
"v_mfma_f32_16x16x32_bf16 v[64:67], v[4:7], v[196:199], v[64:67]\n" \
"ds_read_b128 v[220:223], v231 offset:6144\n" \
"v_mfma_f32_16x16x32_bf16 v[68:71], v[8:11], v[192:195], v[68:71]\n" \
"ds_read_b64_tr_b16 v[32:33], v232 offset:1024\n" \
"v_mfma_f32_16x16x32_bf16 v[68:71], v[12:15], v[196:199], v[68:71]\n" \
"v_mfma_f32_16x16x32_bf16 v[72:75], v[16:19], v[192:195], v[72:75]\n" \
"ds_read_b64_tr_b16 v[34:35], v232 offset:3072\n" \
"v_mfma_f32_16x16x32_bf16 v[72:75], v[20:23], v[196:199], v[72:75]\n" \
"s_waitcnt lgkmcnt(3)\n" \
"ds_read_b64_tr_b16 v[36:37], v232 offset:9216\n" \
"v_mfma_f32_16x16x32_bf16 v[76:79], v[24:27], v[192:195], v[76:79]\n" \
"ds_read_b64_tr_b16 v[38:39], v232 offset:11264\n" \
"v_cvt_pk_bf16_f32 v64, v64, v65\n" \
"v_mfma_f32_16x16x32_bf16 v[76:79], v[28:31], v[196:199], v[76:79]\n" \
"v_cvt_pk_bf16_f32 v65, v66, v67\n" \
"v_cvt_pk_bf16_f32 v66, v68, v69\n" \
"v_cvt_pk_bf16_f32 v67, v70, v71\n" \
"v_accvgpr_write_b32 a128, v64\n" \
"v_accvgpr_write_b32 a129, v65\n" \
"v_mfma_f32_16x16x32_bf16 v[96:99], v[0:3], v[200:203], v[96:99]\n" \
"ds_read_b64_tr_b16 v[40:41], v233 offset:1024\n" \
"v_accvgpr_write_b32 a130, v66\n" \
"v_mfma_f32_16x16x32_bf16 v[96:99], v[4:7], v[204:207], v[96:99]\n" \
"ds_read_b64_tr_b16 v[42:43], v233 offset:3072\n" \
"v_accvgpr_write_b32 a131, v67\n" \
"v_mfma_f32_16x16x32_bf16 v[100:103], v[8:11], v[200:203], v[100:103]\n" \
"ds_read_b64_tr_b16 v[44:45], v233 offset:9216\n" \
"v_cvt_pk_bf16_f32 v72, v72, v73\n" \
"v_mfma_f32_16x16x32_bf16 v[100:103], v[12:15], v[204:207], v[100:103]\n" \
"v_cvt_pk_bf16_f32 v73, v74, v75\n" \
"v_cvt_pk_bf16_f32 v74, v76, v77\n" \
"v_cvt_pk_bf16_f32 v75, v78, v79\n" \
"v_accvgpr_write_b32 a132, v72\n" \
"v_accvgpr_write_b32 a133, v73\n" \
"v_mfma_f32_16x16x32_bf16 v[104:107], v[16:19], v[200:203], v[104:107]\n" \
"ds_read_b64_tr_b16 v[46:47], v233 offset:11264\n" \
"v_accvgpr_write_b32 a134, v74\n" \
"v_mfma_f32_16x16x32_bf16 v[104:107], v[20:23], v[204:207], v[104:107]\n" \
"ds_read_b64_tr_b16 v[48:49], v232 offset:1536\n" \
"v_accvgpr_write_b32 a135, v75\n" \
"v_mfma_f32_16x16x32_bf16 v[108:111], v[24:27], v[200:203], v[108:111]\n" \
"ds_read_b64_tr_b16 v[50:51], v232 offset:3584\n" \
"v_cvt_pk_bf16_f32 v96, v96, v97\n" \
"v_mfma_f32_16x16x32_bf16 v[108:111], v[28:31], v[204:207], v[108:111]\n" \
"v_cvt_pk_bf16_f32 v97, v98, v99\n" \
"v_cvt_pk_bf16_f32 v98, v100, v101\n" \
"v_cvt_pk_bf16_f32 v99, v102, v103\n" \
"v_accvgpr_write_b32 a144, v96\n" \
"v_accvgpr_write_b32 a145, v97\n" \
"v_mfma_f32_16x16x32_bf16 v[128:131], v[0:3], v[208:211], v[128:131]\n" \
"ds_read_b64_tr_b16 v[52:53], v232 offset:9728\n" \
"v_accvgpr_write_b32 a146, v98\n" \
"v_mfma_f32_16x16x32_bf16 v[128:131], v[4:7], v[212:215], v[128:131]\n" \
"ds_read_b64_tr_b16 v[54:55], v232 offset:11776\n" \
"v_accvgpr_write_b32 a147, v99\n" \
"v_mfma_f32_16x16x32_bf16 v[132:135], v[8:11], v[208:211], v[132:135]\n" \
"ds_read_b64_tr_b16 v[56:57], v233 offset:1536\n" \
"v_cvt_pk_bf16_f32 v104, v104, v105\n" \
"v_mfma_f32_16x16x32_bf16 v[132:135], v[12:15], v[212:215], v[132:135]\n" \
"v_cvt_pk_bf16_f32 v105, v106, v107\n" \
"v_cvt_pk_bf16_f32 v106, v108, v109\n" \
"v_cvt_pk_bf16_f32 v107, v110, v111\n" \
"v_accvgpr_write_b32 a148, v104\n" \
"v_accvgpr_write_b32 a149, v105\n" \
"v_mfma_f32_16x16x32_bf16 v[136:139], v[16:19], v[208:211], v[136:139]\n" \
"ds_read_b64_tr_b16 v[58:59], v233 offset:3584\n" \
"v_accvgpr_write_b32 a150, v106\n" \
"v_mfma_f32_16x16x32_bf16 v[136:139], v[20:23], v[212:215], v[136:139]\n" \
"ds_read_b64_tr_b16 v[60:61], v233 offset:9728\n" \
"v_accvgpr_write_b32 a151, v107\n" \
"v_mfma_f32_16x16x32_bf16 v[140:143], v[24:27], v[208:211], v[140:143]\n" \
"ds_read_b64_tr_b16 v[62:63], v233 offset:11776\n" \
"v_cvt_pk_bf16_f32 v128, v128, v129\n" \
"v_mfma_f32_16x16x32_bf16 v[140:143], v[28:31], v[212:215], v[140:143]\n" \
"s_waitcnt lgkmcnt(3)\n" \
"v_cvt_pk_bf16_f32 v129, v130, v131\n" \
"v_cvt_pk_bf16_f32 v130, v132, v133\n" \
"v_cvt_pk_bf16_f32 v131, v134, v135\n" \
"v_mfma_f32_16x16x32_bf16 v[160:163], v[0:3], v[216:219], v[160:163]\n" \
"v_accvgpr_write_b32 a160, v128\n" \
"v_accvgpr_write_b32 a161, v129\n" \
"Lpg4934_%=:\n" \
"s_cbranch_execz Lpg6171_%=\n" \
"v_mfma_f32_16x16x32_bf16 v[160:163], v[4:7], v[220:223], v[160:163]\n" \
"v_accvgpr_write_b32 a162, v130\n" \
"v_accvgpr_write_b32 a163, v131\n" \
"v_mfma_f32_16x16x32_bf16 v[164:167], v[8:11], v[216:219], v[164:167]\n" \
"v_cvt_pk_bf16_f32 v136, v136, v137\n" \
"v_cvt_pk_bf16_f32 v137, v138, v139\n" \
"v_mfma_f32_16x16x32_bf16 v[164:167], v[12:15], v[220:223], v[164:167]\n" \
"v_cvt_pk_bf16_f32 v138, v140, v141\n" \
"v_cvt_pk_bf16_f32 v139, v142, v143\n" \
"v_mfma_f32_16x16x32_bf16 v[168:171], v[16:19], v[216:219], v[168:171]\n" \
"v_accvgpr_write_b32 a164, v136\n" \
"v_accvgpr_write_b32 a165, v137\n" \
"v_mfma_f32_16x16x32_bf16 v[168:171], v[20:23], v[220:223], v[168:171]\n" \
"s_waitcnt vmcnt(10) lgkmcnt(0)\n" \
"v_accvgpr_write_b32 a166, v138\n" \
"v_accvgpr_write_b32 a167, v139\n" \
"v_mfma_f32_16x16x32_bf16 v[172:175], v[24:27], v[216:219], v[172:175]\n" \
"v_cvt_pk_bf16_f32 v160, v160, v161\n" \
"v_cvt_pk_bf16_f32 v161, v162, v163\n" \
"v_mfma_f32_16x16x32_bf16 v[172:175], v[28:31], v[220:223], v[172:175]\n" \
"s_barrier\n" \
"v_cvt_pk_bf16_f32 v162, v164, v165\n" \
"v_cvt_pk_bf16_f32 v163, v166, v167\n" \
"v_accvgpr_write_b32 a176, v160\n" \
"v_accvgpr_write_b32 a177, v161\n" \
"v_mfma_f32_16x16x32_bf16 v[80:83], v[32:35], v[192:195], v[80:83]\n" \
"s_mov_b32 m0, s74\n" \
"v_accvgpr_write_b32 a178, v162\n" \
"v_accvgpr_write_b32 a179, v163\n" \
"v_mfma_f32_16x16x32_bf16 v[80:83], v[36:39], v[196:199], v[80:83]\n" \
"ds_read_b128 a[192:195], v249 offset:0\n" \
"global_load_lds_dwordx4 v250, s[38:39] offset:0 sc1\n" \
"v_mfma_f32_16x16x32_bf16 v[84:87], v[40:43], v[192:195], v[84:87]\n" \
"ds_read_b128 v[0:3], v254\n" \
"v_cvt_pk_bf16_f32 v168, v168, v169\n" \
"v_mfma_f32_16x16x32_bf16 v[84:87], v[44:47], v[196:199], v[84:87]\n" \
"ds_read_b128 a[196:199], v249 offset:2048\n" \
"global_load_lds_dwordx4 v250, s[38:39] offset:1024 sc1\n" \
"v_cvt_pk_bf16_f32 v169, v170, v171\n" \
"v_cvt_pk_bf16_f32 v170, v172, v173\n" \
"v_cvt_pk_bf16_f32 v171, v174, v175\n" \
"v_accvgpr_write_b32 a180, v168\n" \
"v_accvgpr_write_b32 a181, v169\n" \
"v_accvgpr_write_b32 a182, v170\n" \
"v_mfma_f32_16x16x32_bf16 v[88:91], v[48:51], v[192:195], v[88:91]\n" \
"ds_read_b128 v[4:7], v254\n" \
"v_accvgpr_write_b32 a183, v171\n" \
"v_mfma_f32_16x16x32_bf16 v[88:91], v[52:55], v[196:199], v[88:91]\n" \
"ds_read_b128 a[200:203], v249 offset:4096\n" \
"global_load_lds_dwordx4 v250, s[38:39] offset:2048 sc1\n" \
"v_mfma_f32_16x16x32_bf16 v[92:95], v[56:59], v[192:195], v[92:95]\n" \
"v_cvt_pk_bf16_f32 v80, v80, v81\n" \
"v_cvt_pk_bf16_f32 v81, v82, v83\n" \
"v_mfma_f32_16x16x32_bf16 v[92:95], v[60:63], v[196:199], v[92:95]\n" \
"ds_read_b128 a[204:207], v249 offset:6144\n" \
"global_load_lds_dwordx4 v250, s[38:39] offset:3072 sc1\n" \
"v_cvt_pk_bf16_f32 v82, v84, v85\n" \
"v_cvt_pk_bf16_f32 v83, v86, v87\n" \
"v_accvgpr_write_b32 a136, v80\n" \
"v_accvgpr_write_b32 a137, v81\n" \
"v_accvgpr_write_b32 a138, v82\n" \
"v_mfma_f32_16x16x32_bf16 v[112:115], v[32:35], v[200:203], v[112:115]\n" \
"s_add_u32 s38, s38, 17408\n" \
"v_accvgpr_write_b32 a139, v83\n" \
"s_add_u32 m0, m0, 0x4000\n" \
"v_mfma_f32_16x16x32_bf16 v[112:115], v[36:39], v[204:207], v[112:115]\n" \
"ds_read_b128 a[208:211], v249 offset:256\n" \
"global_load_lds_dwordx4 v250, s[38:39] offset:0 sc1\n" \
"v_mfma_f32_16x16x32_bf16 v[116:119], v[40:43], v[200:203], v[116:119]\n" \
"v_cvt_pk_bf16_f32 v88, v88, v89\n" \
"v_cvt_pk_bf16_f32 v89, v90, v91\n" \
"v_mfma_f32_16x16x32_bf16 v[116:119], v[44:47], v[204:207], v[116:119]\n" \
"ds_read_b128 a[212:215], v249 offset:2304\n" \
"global_load_lds_dwordx4 v250, s[38:39] offset:1024 sc1\n" \
"v_cvt_pk_bf16_f32 v90, v92, v93\n" \
"v_cvt_pk_bf16_f32 v91, v94, v95\n" \
"v_accvgpr_write_b32 a140, v88\n" \
"v_accvgpr_write_b32 a141, v89\n" \
"v_mfma_f32_16x16x32_bf16 v[120:123], v[48:51], v[200:203], v[120:123]\n" \
"v_accvgpr_write_b32 a142, v90\n" \
"v_accvgpr_write_b32 a143, v91\n" \
"v_mfma_f32_16x16x32_bf16 v[120:123], v[52:55], v[204:207], v[120:123]\n" \
"ds_read_b128 a[216:219], v249 offset:4352\n" \
"global_load_lds_dwordx4 v250, s[38:39] offset:2048 sc1\n" \
"v_mfma_f32_16x16x32_bf16 v[124:127], v[56:59], v[200:203], v[124:127]\n" \
"v_cvt_pk_bf16_f32 v112, v112, v113\n" \
"v_cvt_pk_bf16_f32 v113, v114, v115\n" \
"v_mfma_f32_16x16x32_bf16 v[124:127], v[60:63], v[204:207], v[124:127]\n" \
"ds_read_b128 a[220:223], v249 offset:6400\n" \
"global_load_lds_dwordx4 v250, s[38:39] offset:3072 sc1\n" \
"v_cvt_pk_bf16_f32 v114, v116, v117\n" \
"v_cvt_pk_bf16_f32 v115, v118, v119\n" \
"v_mfma_f32_16x16x32_bf16 v[144:147], v[32:35], v[208:211], v[144:147]\n" \
"s_add_u32 s38, s38, 17408\n" \
"v_accvgpr_write_b32 a152, v112\n" \
"s_xor_b32 s74, s74, 0x8000\n" \
"v_mfma_f32_16x16x32_bf16 v[144:147], v[36:39], v[212:215], v[144:147]\n" \
"ds_read_b128 a[224:227], v249 offset:8192\n" \
"v_accvgpr_write_b32 a153, v113\n" \
"v_mfma_f32_16x16x32_bf16 v[148:151], v[40:43], v[208:211], v[148:151]\n" \
"v_accvgpr_write_b32 a154, v114\n" \
"v_accvgpr_write_b32 a155, v115\n" \
"v_mfma_f32_16x16x32_bf16 v[148:151], v[44:47], v[212:215], v[148:151]\n" \
"buffer_store_dwordx4 a[32:35], v249, s[56:59], s90 offen offset:256 sc1\n" \
"ds_read_b128 a[228:231], v249 offset:10240\n" \
"v_cvt_pk_bf16_f32 v120, v120, v121\n" \
"v_cvt_pk_bf16_f32 v121, v122, v123\n" \
"v_cvt_pk_bf16_f32 v122, v124, v125\n" \
"v_mfma_f32_16x16x32_bf16 v[152:155], v[48:51], v[208:211], v[152:155]\n" \
"v_cvt_pk_bf16_f32 v123, v126, v127\n" \
"v_accvgpr_write_b32 a156, v120\n" \
"v_mfma_f32_16x16x32_bf16 v[152:155], v[52:55], v[212:215], v[152:155]\n" \
"ds_read_b128 a[232:235], v249 offset:12288\n" \
"v_accvgpr_write_b32 a157, v121\n" \
"v_mfma_f32_16x16x32_bf16 v[156:159], v[56:59], v[208:211], v[156:159]\n" \
"ds_read_b128 a[32:35], v254\n" \
"v_accvgpr_write_b32 a158, v122\n" \
"v_mfma_f32_16x16x32_bf16 v[156:159], v[60:63], v[212:215], v[156:159]\n" \
"ds_read_b128 a[236:239], v249 offset:14336\n" \
"v_accvgpr_write_b32 a159, v123\n" \
"v_mfma_f32_16x16x32_bf16 v[176:179], v[32:35], v[216:219], v[176:179]\n" \
"ds_read_b128 a[240:243], v249 offset:8448\n" \
"v_cvt_pk_bf16_f32 v144, v144, v145\n" \
"v_mfma_f32_16x16x32_bf16 v[176:179], v[36:39], v[220:223], v[176:179]\n" \
"ds_read_b128 a[244:247], v249 offset:10496\n" \
"v_cvt_pk_bf16_f32 v145, v146, v147\n" \
"v_mfma_f32_16x16x32_bf16 v[180:183], v[40:43], v[216:219], v[180:183]\n" \
"ds_read_b128 a[248:251], v249 offset:12544\n" \
"v_cvt_pk_bf16_f32 v146, v148, v149\n" \
"v_mfma_f32_16x16x32_bf16 v[180:183], v[44:47], v[220:223], v[180:183]\n" \
"ds_read_b128 a[252:255], v249 offset:14592\n" \
"v_cvt_pk_bf16_f32 v147, v150, v151\n" \
"v_mfma_f32_16x16x32_bf16 v[184:187], v[48:51], v[216:219], v[184:187]\n" \
"ds_read_b128 a[36:39], v254\n" \
"v_accvgpr_write_b32 a168, v144\n" \
"v_mfma_f32_16x16x32_bf16 v[184:187], v[52:55], v[220:223], v[184:187]\n" \
"ds_read_b128 v[32:35], v254\n" \
"v_accvgpr_write_b32 a169, v145\n" \
"v_mfma_f32_16x16x32_bf16 v[188:191], v[56:59], v[216:219], v[188:191]\n" \
"ds_read_b128 v[36:39], v254\n" \
"v_accvgpr_write_b32 a170, v146\n" \
"v_mfma_f32_16x16x32_bf16 v[188:191], v[60:63], v[220:223], v[188:191]\n" \
"s_waitcnt lgkmcnt(4)\n" \
"buffer_store_dwordx4 a[8:11], v249, s[56:59], s90 offen offset:2048 sc1\n" \
"v_accvgpr_write_b32 a171, v147\n" \
"s_cmp_eq_u32 s63, 0\n" \
"s_cbranch_scc1 Lmask0_%=\n" \
"Lmask0r_%=:\n" \
"v_mfma_f32_16x16x32_bf16 v[0:3], a[192:195], a[128:131], v[0:3]\n" \
"s_lshl_b32 s70, s95, 1\n" \
"v_cvt_pk_bf16_f32 v152, v152, v153\n" \
"v_cvt_pk_bf16_f32 v153, v154, v155\n" \
"v_mfma_f32_16x16x32_bf16 v[0:3], a[196:199], a[132:135], v[0:3]\n" \
"s_add_u32 s70, s70, 131072\n" \
"v_cvt_pk_bf16_f32 v154, v156, v157\n" \
"v_cvt_pk_bf16_f32 v155, v158, v159\n" \
"v_mfma_f32_16x16x32_bf16 v[0:3], a[200:203], a[136:139], v[0:3]\n" \
"ds_read_b128 a[8:11], v254\n" \
"v_accvgpr_write_b32 a172, v152\n" \
"v_mfma_f32_16x16x32_bf16 v[0:3], a[204:207], a[140:143], v[0:3]\n" \
"ds_read_b128 a[12:15], v254\n" \
"v_accvgpr_write_b32 a173, v153\n" \
"v_mov_b32 v201, 0xf149f2ca\n" \
"v_mfma_f32_16x16x32_bf16 v[8:11], a[192:195], a[144:147], 0\n" \
"v_accvgpr_write_b32 a174, v154\n" \
"v_accvgpr_write_b32 a175, v155\n" \
"v_mfma_f32_16x16x32_bf16 v[8:11], a[196:199], a[148:151], v[8:11]\n" \
"v_cvt_pk_bf16_f32 v176, v176, v177\n" \
"v_cvt_pk_bf16_f32 v177, v178, v179\n" \
"v_mfma_f32_16x16x32_bf16 v[8:11], a[200:203], a[152:155], v[8:11]\n" \
"v_cvt_pk_bf16_f32 v178, v180, v181\n" \
"v_cvt_pk_bf16_f32 v179, v182, v183\n" \
"v_mfma_f32_16x16x32_bf16 v[8:11], a[204:207], a[156:159], v[8:11]\n" \
"v_accvgpr_write_b32 a184, v176\n" \
"v_accvgpr_write_b32 a185, v177\n" \
"v_max3_f32 v192, v0, v1, v2\n" \
"v_max3_f32 v192, v192, v3, v201\n" \
"v_mfma_f32_16x16x32_bf16 v[16:19], a[192:195], a[160:163], 0\n" \
"v_accvgpr_write_b32 a186, v178\n" \
"v_accvgpr_write_b32 a187, v179\n" \
"v_mfma_f32_16x16x32_bf16 v[16:19], a[196:199], a[164:167], v[16:19]\n" \
"v_cvt_pk_bf16_f32 v184, v184, v185\n" \
"v_cvt_pk_bf16_f32 v185, v186, v187\n" \
"v_mfma_f32_16x16x32_bf16 v[16:19], a[200:203], a[168:171], v[16:19]\n" \
"v_cvt_pk_bf16_f32 v186, v188, v189\n" \
"v_cvt_pk_bf16_f32 v187, v190, v191\n" \
"v_mfma_f32_16x16x32_bf16 v[16:19], a[204:207], a[172:175], v[16:19]\n" \
"v_max3_f32 v193, v8, v9, v10\n" \
"v_accvgpr_write_b32 a188, v184\n" \
"v_accvgpr_write_b32 a189, v185\n" \
"v_mfma_f32_16x16x32_bf16 v[24:27], a[192:195], a[176:179], 0\n" \
"v_accvgpr_write_b32 a190, v186\n" \
"v_accvgpr_write_b32 a191, v187\n" \
"v_mfma_f32_16x16x32_bf16 v[24:27], a[196:199], a[180:183], v[24:27]\n" \
"v_add_u32 v200, s70, v248\n" \
"v_max3_f32 v193, v193, v11, v201\n" \
"v_mfma_f32_16x16x32_bf16 v[24:27], a[200:203], a[184:187], v[24:27]\n" \
"ds_read_b128 v[128:131], v254\n" \
"ds_read_b128 v[132:135], v254\n" \
"v_max3_f32 v194, v16, v17, v18\n" \
"v_max3_f32 v194, v194, v19, v201\n" \
"v_mfma_f32_16x16x32_bf16 v[24:27], a[204:207], a[188:191], v[24:27]\n" \
"s_waitcnt lgkmcnt(2)\n" \
"ds_read_b128 v[136:139], v254\n" \
"ds_read_b128 v[140:143], v254\n" \
"v_mfma_f32_16x16x32_bf16 v[4:7], a[208:211], a[128:131], v[4:7]\n" \
"ds_read_b128 v[144:147], v254\n" \
"ds_read_b128 v[148:151], v254\n" \
"v_mfma_f32_16x16x32_bf16 v[4:7], a[212:215], a[132:135], v[4:7]\n" \
"ds_read_b128 a[192:195], v249 offset:16384\n" \
"ds_read_b128 v[152:155], v254\n" \
"v_mfma_f32_16x16x32_bf16 v[4:7], a[216:219], a[136:139], v[4:7]\n" \
"ds_read_b128 v[156:159], v254\n" \
"ds_read_b128 v[160:163], v254\n" \
"v_max3_f32 v195, v24, v25, v26\n" \
"v_max3_f32 v195, v195, v27, v201\n" \
"v_mov_b32 v196, v192\n" \
"v_mov_b32 v197, v193\n" \
"v_mov_b32 v198, v194\n" \
"v_mov_b32 v199, v195\n" \
"v_permlane16_swap_b32 v196, v192\n" \
"v_permlane16_swap_b32 v197, v193\n" \
"v_permlane16_swap_b32 v198, v194\n" \
"v_permlane16_swap_b32 v199, v195\n" \
"v_max_f32 v192, v192, v196\n" \
"v_max_f32 v193, v193, v197\n" \
"v_mfma_f32_16x16x32_bf16 v[4:7], a[220:223], a[140:143], v[4:7]\n" \
"ds_read_b128 a[196:199], v249 offset:18432\n" \
"ds_read_b128 v[164:167], v254\n" \
"v_max_f32 v194, v194, v198\n" \
"v_max_f32 v195, v195, v199\n" \
"v_mov_b32 v196, v192\n" \
"v_mov_b32 v197, v193\n" \
"v_mov_b32 v198, v194\n" \
"v_mov_b32 v199, v195\n" \
"v_permlane32_swap_b32 v196, v192\n" \
"v_permlane32_swap_b32 v197, v193\n" \
"v_permlane32_swap_b32 v198, v194\n" \
"v_permlane32_swap_b32 v199, v195\n" \
"v_max_f32 v192, v192, v196\n" \
"v_max_f32 v193, v193, v197\n" \
"v_mfma_f32_16x16x32_bf16 v[12:15], a[216:219], a[152:155], 0\n" \
"ds_read_b128 v[168:171], v254\n" \
"ds_read_b128 v[172:175], v254\n" \
"v_max_f32 v194, v194, v198\n" \
"v_max_f32 v195, v195, v199\n" \
"v_sub_f32 v224, 0, v192\n" \
"v_sub_f32 v225, 0, v192\n" \
"v_sub_f32 v226, 0, v192\n" \
"v_sub_f32 v227, 0, v192\n" \
"v_add_f32 v32, v32, v224\n" \
"v_mfma_f32_16x16x32_bf16 v[12:15], a[220:223], a[156:159], v[12:15]\n" \
"ds_read_b128 a[200:203], v249 offset:20480\n" \
"ds_read_b128 v[176:179], v254\n" \
"v_mfma_f32_16x16x32_bf16 v[12:15], a[208:211], a[144:147], v[12:15]\n" \
"ds_read_b128 v[180:183], v254\n" \
"ds_read_b128 v[184:187], v254\n" \
"v_mfma_f32_16x16x32_bf16 v[12:15], a[212:215], a[148:151], v[12:15]\n" \
"ds_read_b128 a[204:207], v249 offset:22528\n" \
"buffer_store_dwordx4 a[40:43], v249, s[56:59], s90 offen offset:2304 sc1\n" \
"ds_write_b128 v200, a[160:163] offset:0\n" \
"s_add_u32 s90, s89, 417792\n" \
"v_mfma_f32_16x16x32_bf16 v[20:23], a[208:211], a[160:163], 0\n" \
"v_add_f32 v33, v33, v224\n" \
"v_add_f32 v34, v34, v224\n" \
"v_mfma_f32_16x16x32_bf16 v[20:23], a[212:215], a[164:167], v[20:23]\n" \
"v_add_f32 v35, v35, v224\n" \
"v_sub_f32 v228, 0, v193\n" \
"v_mfma_f32_16x16x32_bf16 v[20:23], a[216:219], a[168:171], v[20:23]\n" \
"ds_read_b128 a[40:43], v254\n" \
"ds_read_b128 v[188:191], v254\n" \
"v_mfma_f32_16x16x32_bf16 v[20:23], a[220:223], a[172:175], v[20:23]\n" \
"buffer_store_dwordx4 a[16:19], v248, s[56:59], s90 offen offset:0 sc1\n" \
"ds_write_b128 v200, a[164:167] offset:1024\n" \
"v_sub_f32 v229, 0, v193\n" \
"v_mfma_f32_16x16x32_bf16 v[28:31], a[216:219], a[184:187], 0\n" \
"v_sub_f32 v230, 0, v193\n" \
"v_sub_f32 v231, 0, v193\n" \
"v_mfma_f32_16x16x32_bf16 v[28:31], a[220:223], a[188:191], v[28:31]\n" \
"v_sub_f32 v232, 0, v194\n" \
"v_sub_f32 v233, 0, v194\n" \
"v_mfma_f32_16x16x32_bf16 v[28:31], a[208:211], a[176:179], v[28:31]\n" \
"buffer_store_dwordx4 a[20:23], v248, s[56:59], s90 offen offset:1024 sc1\n" \
"ds_write_b128 v200, a[168:171] offset:2048\n" \
"v_sub_f32 v234, 0, v194\n" \
"v_mfma_f32_16x16x32_bf16 v[28:31], a[212:215], a[180:183], v[28:31]\n" \
"v_sub_f32 v235, 0, v194\n" \
"v_sub_f32 v236, 0, v195\n" \
"v_mfma_f32_16x16x32_bf16 v[32:35], a[224:227], a[128:131], v[32:35]\n" \
"v_sub_f32 v237, 0, v195\n" \
"v_sub_f32 v238, 0, v195\n" \
"v_mfma_f32_16x16x32_bf16 v[32:35], a[228:231], a[132:135], v[32:35]\n" \
"ds_read_b128 a[208:211], v249 offset:16640\n" \
"v_sub_f32 v239, 0, v195\n" \
"v_mfma_f32_16x16x32_bf16 v[32:35], a[232:235], a[136:139], v[32:35]\n" \
"ds_read_b128 a[44:47], v254\n" \
"v_add_f32 v36, v36, v224\n" \
"v_mfma_f32_16x16x32_bf16 v[32:35], a[236:239], a[140:143], v[32:35]\n" \
"ds_read_b128 a[212:215], v249 offset:18688\n" \
"ds_read_b128 a[16:19], v254\n" \
"v_mfma_f32_16x16x32_bf16 v[40:43], a[232:235], a[152:155], v[228:231]\n" \
"ds_read_b128 a[48:51], v254\n" \
"v_add_f32 v37, v37, v224\n" \
"v_mfma_f32_16x16x32_bf16 v[40:43], a[236:239], a[156:159], v[40:43]\n" \
"ds_read_b128 a[216:219], v249 offset:20736\n" \
"v_add_f32 v38, v38, v224\n" \
"v_mfma_f32_16x16x32_bf16 v[40:43], a[224:227], a[144:147], v[40:43]\n" \
"ds_read_b128 a[20:23], v254\n" \
"v_add_f32 v39, v39, v224\n" \
"v_mfma_f32_16x16x32_bf16 v[40:43], a[228:231], a[148:151], v[40:43]\n" \
"buffer_store_dwordx4 a[24:27], v248, s[56:59], s90 offen offset:2048 sc1\n" \
"ds_read_b128 a[220:223], v249 offset:22784\n" \
"ds_write_b128 v200, a[172:175] offset:3072\n" \
"v_pk_add_f32 v[0:1], v[0:1], v[224:225]\n" \
"v_pk_add_f32 v[2:3], v[2:3], v[224:225]\n" \
"v_pk_add_f32 v[4:5], v[4:5], v[224:225]\n" \
"v_pk_add_f32 v[6:7], v[6:7], v[224:225]\n" \
"v_exp_f32 v0, v0\n" \
"v_exp_f32 v1, v1\n" \
"v_exp_f32 v2, v2\n" \
"v_cvt_pk_bf16_f32 v0, v0, v1\n" \
"v_exp_f32 v3, v3\n" \
"v_exp_f32 v4, v4\n" \
"v_cvt_pk_bf16_f32 v1, v2, v3\n" \
"v_mfma_f32_16x16x32_bf16 v[48:51], a[224:227], a[160:163], v[232:235]\n" \
"v_exp_f32 v5, v5\n" \
"v_mfma_f32_16x16x32_bf16 v[48:51], a[228:231], a[164:167], v[48:51]\n" \
"v_exp_f32 v6, v6\n" \
"v_mfma_f32_16x16x32_bf16 v[48:51], a[232:235], a[168:171], v[48:51]\n" \
"ds_read_b128 a[52:55], v254\n" \
"v_cvt_pk_bf16_f32 v2, v4, v5\n" \
"v_mfma_f32_16x16x32_bf16 v[48:51], a[236:239], a[172:175], v[48:51]\n" \
"buffer_store_dwordx4 a[28:31], v248, s[56:59], s90 offen offset:3072 sc1\n" \
"ds_write_b128 v200, a[176:179] offset:4096\n" \
"v_exp_f32 v7, v7\n" \
"v_pk_add_f32 v[8:9], v[8:9], v[228:229]\n" \
"v_pk_add_f32 v[10:11], v[10:11], v[228:229]\n" \
"v_pk_add_f32 v[12:13], v[12:13], v[228:229]\n" \
"v_pk_add_f32 v[14:15], v[14:15], v[228:229]\n" \
"v_exp_f32 v8, v8\n" \
"v_cvt_pk_bf16_f32 v3, v6, v7\n" \
"v_exp_f32 v9, v9\n" \
"v_exp_f32 v10, v10\n" \
"v_cvt_pk_bf16_f32 v8, v8, v9\n" \
"v_exp_f32 v11, v11\n" \
"v_mfma_f32_16x16x32_bf16 v[56:59], a[232:235], a[184:187], v[236:239]\n" \
"global_load_dword v253, v252, s[40:41]\n" \
"v_mfma_f32_16x16x32_bf16 v[56:59], a[236:239], a[188:191], v[56:59]\n" \
"s_sub_u32 s80, s80, 1\n" \
"v_exp_f32 v12, v12\n" \
"v_mfma_f32_16x16x32_bf16 v[56:59], a[224:227], a[176:179], v[56:59]\n" \
"s_cmp_gt_i32 s80, 0\n" \
"ds_write_b128 v200, a[180:183] offset:5120\n" \
"v_cvt_pk_bf16_f32 v9, v10, v11\n" \
"v_exp_f32 v13, v13\n" \
"v_exp_f32 v14, v14\n" \
"v_cvt_pk_bf16_f32 v10, v12, v13\n" \
"v_exp_f32 v15, v15\n" \
"v_pk_add_f32 v[16:17], v[16:17], v[232:233]\n" \
"v_pk_add_f32 v[18:19], v[18:19], v[232:233]\n" \
"v_pk_add_f32 v[20:21], v[20:21], v[232:233]\n" \
"v_pk_add_f32 v[22:23], v[22:23], v[232:233]\n" \
"v_exp_f32 v16, v16\n" \
"v_cvt_pk_bf16_f32 v11, v14, v15\n" \
"v_mfma_f32_16x16x32_bf16 v[56:59], a[228:231], a[180:183], v[56:59]\n" \
"s_cselect_b32 s81, 0x80000, 0\n" \
"v_exp_f32 v17, v17\n" \
"v_mfma_f32_16x16x32_bf16 v[36:39], a[240:243], a[128:131], v[36:39]\n" \
"s_add_u32 s40, s40, s81\n" \
"v_exp_f32 v18, v18\n" \
"v_mfma_f32_16x16x32_bf16 v[36:39], a[244:247], a[132:135], v[36:39]\n" \
"ds_read_b128 a[224:227], v249 offset:24576\n" \
"v_cvt_pk_bf16_f32 v16, v16, v17\n" \
"v_mfma_f32_16x16x32_bf16 v[240:243], a[120:123], v[0:3], v[240:243]\n" \
"v_exp_f32 v19, v19\n" \
"v_mfma_f32_16x16x32_bf16 v[240:243], a[124:127], v[8:11], v[240:243]\n" \
"s_addc_u32 s41, s41, 0\n" \
"v_exp_f32 v20, v20\n" \
"v_mfma_f32_16x16x32_bf16 v[36:39], a[248:251], a[136:139], v[36:39]\n" \
"s_add_u32 m0, s95, 49152\n" \
"ds_read_b128 a[24:27], v254\n" \
"v_cvt_pk_bf16_f32 v17, v18, v19\n" \
"v_mfma_f32_16x16x32_bf16 v[36:39], a[252:255], a[140:143], v[36:39]\n" \
"ds_read_b128 a[228:231], v249 offset:26624\n" \
"global_load_lds_dwordx4 v250, s[36:37] offset:0 sc1\n" \
"ds_read_b128 a[56:59], v254\n" \
"v_exp_f32 v21, v21\n" \
"v_exp_f32 v22, v22\n" \
"v_mfma_f32_16x16x32_bf16 v[44:47], a[248:251], a[152:155], v[228:231]\n" \
"ds_read_b128 a[28:31], v254\n" \
"v_cvt_pk_bf16_f32 v18, v20, v21\n" \
"v_mfma_f32_16x16x32_bf16 v[44:47], a[252:255], a[156:159], v[44:47]\n" \
"ds_read_b128 a[232:235], v249 offset:28672\n" \
"global_load_lds_dwordx4 v250, s[36:37] offset:1024 sc1\n" \
"v_mfma_f32_16x16x32_bf16 v[44:47], a[240:243], a[144:147], v[44:47]\n" \
"ds_read_b128 a[60:63], v254\n" \
"global_load_lds_dwordx4 v250, s[36:37] offset:2048 sc1\n" \
"v_mfma_f32_16x16x32_bf16 v[44:47], a[244:247], a[148:151], v[44:47]\n" \
"ds_read_b128 a[236:239], v249 offset:30720\n" \
"global_load_lds_dwordx4 v250, s[36:37] offset:3072 sc1\n" \
"ds_write_b128 v200, a[184:187] offset:6144\n" \
"v_exp_f32 v23, v23\n" \
"v_pk_add_f32 v[24:25], v[24:25], v[236:237]\n" \
"v_pk_add_f32 v[26:27], v[26:27], v[236:237]\n" \
"v_pk_add_f32 v[28:29], v[28:29], v[236:237]\n" \
"v_pk_add_f32 v[30:31], v[30:31], v[236:237]\n" \
"v_exp_f32 v24, v24\n" \
"v_cvt_pk_bf16_f32 v19, v22, v23\n" \
"v_exp_f32 v25, v25\n" \
"v_exp_f32 v26, v26\n" \
"v_cvt_pk_bf16_f32 v24, v24, v25\n" \
"v_mfma_f32_16x16x32_bf16 v[242:245], a[120:123], v[16:19], v[242:245]\n" \
"v_exp_f32 v27, v27\n" \
"v_mfma_f32_16x16x32_bf16 v[52:55], a[240:243], a[160:163], v[232:235]\n" \
"s_add_u32 s36, s36, 17408\n" \
"v_mov_b32 v202, s60\n" \
"v_lshlrev_b32 v203, 28, s61\n" \
"v_mfma_f32_16x16x32_bf16 v[52:55], a[244:247], a[164:167], v[52:55]\n" \
"s_mov_b32 s42, s60\n" \
"v_mov_b32 v246, v249\n" \
"v_mov_b32 v254, 0xff800000\n" \
"v_mfma_f32_16x16x32_bf16 v[52:55], a[248:251], a[168:171], v[52:55]\n" \
"s_mov_b32 s43, s60\n" \
"v_exp_f32 v28, v28\n" \
"v_mfma_f32_16x16x32_bf16 v[52:55], a[252:255], a[172:175], v[52:55]\n" \
"s_mov_b64 s[8:9], 0\n" \
"ds_write_b128 v200, a[188:191] offset:7168\n" \
"v_cvt_pk_bf16_f32 v25, v26, v27\n" \
"v_exp_f32 v29, v29\n" \
"v_mfma_f32_16x16x32_bf16 v[60:63], a[248:251], a[184:187], v[236:239]\n" \
"s_mov_b64 s[10:11], -1\n" \
"v_exp_f32 v30, v30\n" \
"v_cvt_pk_bf16_f32 v26, v28, v29\n" \
"v_mfma_f32_16x16x32_bf16 v[60:63], a[252:255], a[188:191], v[60:63]\n" \
"s_mov_b64 s[92:93], 0\n" \
"v_exp_f32 v31, v31\n" \
"v_mfma_f32_16x16x32_bf16 v[60:63], a[240:243], a[176:179], v[60:63]\n" \
"v_cvt_pk_bf16_f32 v27, v30, v31\n" \
"s_lshl_b32 s84, s63, 2\n" \
"v_mfma_f32_16x16x32_bf16 v[60:63], a[244:247], a[180:183], v[60:63]\n" \
"v_mfma_f32_16x16x32_bf16 v[242:245], a[124:127], v[24:27], v[242:245]\n" \
"s_add_u32 s84, s84, s79\n" \
"ds_read_b128 a[240:243], v249 offset:24832\n" \
"ds_read_b128 a[244:247], v249 offset:26880\n" \
"ds_read_b128 a[248:251], v249 offset:28928\n" \
"s_cmp_eq_u32 s63, 0\n" \
"s_waitcnt vmcnt(16) lgkmcnt(3)\n" \
"s_barrier\n" \
"s_memrealtime s[12:13]\n" \
"s_cbranch_scc1 LA1l_%=\n" \
"s_branch LA1_%=\n" \
"Lpg5386_%=:\n" \
"s_cbranch_execz Lpg6288_%=\n" \
"Lloop_%=:\n" \
"v_mfma_f32_16x16x32_bf16 a[0:3], a[192:195], v[64:67], a[0:3]\n" \
"v_exp_f32 v0, v0\n" \
"v_mfma_f32_16x16x32_bf16 a[0:3], a[196:199], v[96:99], a[0:3]\n" \
"v_cvt_pk_bf16_f32 v123, v126, v127\n" \
"v_add_f32 v242, v242, v0\n" \
"v_exp_f32 v1, v1\n" \
"v_mfma_f32_16x16x32_bf16 a[32:35], a[192:195], v[72:75], a[32:35]\n" \
"s_add_u32 m0, s73, 0x4000\n" \
"ds_read_b128 a[244:247], v247 offset:30720\n" \
"v_add_f32 v242, v242, v1\n" \
"v_mfma_f32_16x16x32_bf16 a[32:35], a[196:199], v[104:107], a[32:35]\n" \
"ds_read_b128 a[248:251], v247 offset:23552\n" \
"global_load_lds_dwordx4 v250, s[36:37] offset:0 sc1\n" \
"v_mfma_f32_16x16x32_bf16 v[128:131], a[192:195], v[80:83], v[128:131]\n" \
"ds_read_b128 a[252:255], v247 offset:31744\n" \
"v_exp_f32 v2, v2\n" \
"v_mfma_f32_16x16x32_bf16 v[128:131], a[196:199], v[112:115], v[128:131]\n" \
"v_add_f32 v242, v242, v2\n" \
"v_cvt_pk_bf16_f32 v0, v0, v1\n" \
"v_mfma_f32_16x16x32_bf16 v[160:163], a[192:195], v[88:91], v[160:163]\n" \
"v_exp_f32 v3, v3\n" \
"v_mfma_f32_16x16x32_bf16 v[160:163], a[196:199], v[120:123], v[160:163]\n" \
"ds_read_b128 a[192:195], v246 offset:16384\n" \
"v_xor_b32 v247, 0x8000, v247\n" \
"v_add_f32 v242, v242, v3\n" \
"v_mfma_f32_16x16x32_bf16 v[164:167], a[200:203], v[88:91], v[164:167]\n" \
"v_exp_f32 v4, v4\n" \
"v_mfma_f32_16x16x32_bf16 v[164:167], a[204:207], v[120:123], v[164:167]\n" \
"v_add_f32 v242, v242, v4\n" \
"v_cvt_pk_bf16_f32 v1, v2, v3\n" \
"v_mfma_f32_16x16x32_bf16 v[132:135], a[200:203], v[80:83], v[132:135]\n" \
"v_exp_f32 v5, v5\n" \
"v_mfma_f32_16x16x32_bf16 v[132:135], a[204:207], v[112:115], v[132:135]\n" \
"ds_read_b128 a[196:199], v246 offset:18432\n" \
"v_add_f32 v242, v242, v5\n" \
"v_mfma_f32_16x16x32_bf16 a[36:39], a[200:203], v[72:75], a[36:39]\n" \
"v_exp_f32 v6, v6\n" \
"v_mfma_f32_16x16x32_bf16 a[36:39], a[204:207], v[104:107], a[36:39]\n" \
"v_add_f32 v242, v242, v6\n" \
"v_cvt_pk_bf16_f32 v2, v4, v5\n" \
"v_mfma_f32_16x16x32_bf16 a[4:7], a[200:203], v[64:67], a[4:7]\n" \
"v_exp_f32 v7, v7\n" \
"v_mfma_f32_16x16x32_bf16 a[4:7], a[204:207], v[96:99], a[4:7]\n" \
"ds_read_b128 a[200:203], v246 offset:20480\n" \
"global_load_lds_dwordx4 v250, s[36:37] offset:1024 sc1\n" \
"v_add_f32 v242, v242, v7\n" \
"v_mfma_f32_16x16x32_bf16 a[8:11], a[208:211], v[64:67], a[8:11]\n" \
"v_exp_f32 v8, v8\n" \
"v_mfma_f32_16x16x32_bf16 a[8:11], a[212:215], v[96:99], a[8:11]\n" \
"v_add_f32 v243, v243, v8\n" \
"v_cvt_pk_bf16_f32 v3, v6, v7\n" \
"v_mfma_f32_16x16x32_bf16 a[40:43], a[208:211], v[72:75], a[40:43]\n" \
"v_exp_f32 v9, v9\n" \
"v_mfma_f32_16x16x32_bf16 a[40:43], a[212:215], v[104:107], a[40:43]\n" \
"ds_read_b128 a[204:207], v246 offset:22528\n" \
"v_add_f32 v243, v243, v9\n" \
"v_mfma_f32_16x16x32_bf16 v[136:139], a[208:211], v[80:83], v[136:139]\n" \
"v_exp_f32 v10, v10\n" \
"v_mfma_f32_16x16x32_bf16 v[136:139], a[212:215], v[112:115], v[136:139]\n" \
"v_add_f32 v243, v243, v10\n" \
"v_cvt_pk_bf16_f32 v8, v8, v9\n" \
"v_mfma_f32_16x16x32_bf16 v[168:171], a[208:211], v[88:91], v[168:171]\n" \
"v_exp_f32 v11, v11\n" \
"v_mfma_f32_16x16x32_bf16 v[168:171], a[212:215], v[120:123], v[168:171]\n" \
"ds_read_b128 a[208:211], v246 offset:16640\n" \
"v_add_f32 v243, v243, v11\n" \
"v_mfma_f32_16x16x32_bf16 v[172:175], a[216:219], v[88:91], v[172:175]\n" \
"v_exp_f32 v12, v12\n" \
"v_mfma_f32_16x16x32_bf16 v[172:175], a[220:223], v[120:123], v[172:175]\n" \
"v_add_f32 v243, v243, v12\n" \
"v_cvt_pk_bf16_f32 v9, v10, v11\n" \
"v_mfma_f32_16x16x32_bf16 v[140:143], a[216:219], v[80:83], v[140:143]\n" \
"v_exp_f32 v13, v13\n" \
"v_mfma_f32_16x16x32_bf16 v[140:143], a[220:223], v[112:115], v[140:143]\n" \
"ds_read_b128 a[212:215], v246 offset:18688\n" \
"global_load_lds_dwordx4 v250, s[36:37] offset:2048 sc1\n" \
"v_add_f32 v243, v243, v13\n" \
"v_mfma_f32_16x16x32_bf16 a[44:47], a[216:219], v[72:75], a[44:47]\n" \
"v_exp_f32 v14, v14\n" \
"v_mfma_f32_16x16x32_bf16 a[44:47], a[220:223], v[104:107], a[44:47]\n" \
"v_add_f32 v243, v243, v14\n" \
"v_cvt_pk_bf16_f32 v10, v12, v13\n" \
"v_mfma_f32_16x16x32_bf16 a[12:15], a[216:219], v[64:67], a[12:15]\n" \
"s_waitcnt lgkmcnt(6)\n" \
"v_exp_f32 v15, v15\n" \
"v_mfma_f32_16x16x32_bf16 a[12:15], a[220:223], v[96:99], a[12:15]\n" \
"ds_read_b128 a[216:219], v246 offset:20736\n" \
"v_add_f32 v243, v243, v15\n" \
"v_mfma_f32_16x16x32_bf16 a[16:19], a[224:227], v[64:67], a[16:19]\n" \
"v_exp_f32 v16, v16\n" \
"v_mfma_f32_16x16x32_bf16 a[16:19], a[228:231], v[96:99], a[16:19]\n" \
"v_add_f32 v244, v244, v16\n" \
"v_cvt_pk_bf16_f32 v11, v14, v15\n" \
"v_mfma_f32_16x16x32_bf16 a[48:51], a[224:227], v[72:75], a[48:51]\n" \
"v_exp_f32 v17, v17\n" \
"v_mfma_f32_16x16x32_bf16 a[48:51], a[228:231], v[104:107], a[48:51]\n" \
"ds_read_b128 a[220:223], v246 offset:22784\n" \
"global_load_lds_dwordx4 v250, s[36:37] offset:3072 sc1\n" \
"v_add_f32 v244, v244, v17\n" \
"v_mfma_f32_16x16x32_bf16 v[144:147], a[224:227], v[80:83], v[144:147]\n" \
"s_add_u32 s36, s36, 17408\n" \
"v_exp_f32 v18, v18\n" \
"v_mfma_f32_16x16x32_bf16 v[144:147], a[228:231], v[112:115], v[144:147]\n" \
"s_xor_b32 s73, s73, 0x8000\n" \
"v_add_f32 v244, v244, v18\n" \
"v_cvt_pk_bf16_f32 v16, v16, v17\n" \
"v_mfma_f32_16x16x32_bf16 v[176:179], a[224:227], v[88:91], v[176:179]\n" \
"s_xor_b64 s[52:53], s[44:45], s[42:43]\n" \
"v_exp_f32 v19, v19\n" \
"v_mfma_f32_16x16x32_bf16 v[176:179], a[228:231], v[120:123], v[176:179]\n" \
"s_xor_b64 s[54:55], s[46:47], s[42:43]\n" \
"ds_read_b128 a[224:227], v246 offset:24576\n" \
"v_add_f32 v244, v244, v19\n" \
"v_mfma_f32_16x16x32_bf16 v[180:183], a[232:235], v[88:91], v[180:183]\n" \
"s_or_b64 s[52:53], s[52:53], s[54:55]\n" \
"v_exp_f32 v20, v20\n" \
"v_mfma_f32_16x16x32_bf16 v[180:183], a[236:239], v[120:123], v[180:183]\n" \
"s_xor_b64 s[54:55], s[48:49], s[42:43]\n" \
"v_add_f32 v244, v244, v20\n" \
"v_cvt_pk_bf16_f32 v17, v18, v19\n" \
"v_mfma_f32_16x16x32_bf16 v[148:151], a[232:235], v[80:83], v[148:151]\n" \
"s_or_b64 s[52:53], s[52:53], s[54:55]\n" \
"v_exp_f32 v21, v21\n" \
"v_mfma_f32_16x16x32_bf16 v[148:151], a[236:239], v[112:115], v[148:151]\n" \
"s_xor_b64 s[54:55], s[50:51], s[42:43]\n" \
"ds_read_b128 a[228:231], v246 offset:26624\n" \
"v_add_f32 v244, v244, v21\n" \
"v_exp_f32 v22, v22\n" \
"v_mfma_f32_16x16x32_bf16 a[52:55], a[232:235], v[72:75], a[52:55]\n" \
"s_or_b64 s[52:53], s[52:53], s[54:55]\n" \
"v_add_f32 v244, v244, v22\n" \
"v_cvt_pk_bf16_f32 v18, v20, v21\n" \
"v_mfma_f32_16x16x32_bf16 a[52:55], a[236:239], v[104:107], a[52:55]\n" \
"s_and_b64 s[52:53], s[52:53], s[8:9]\n" \
"s_cbranch_scc1 Lrs0_%=\n" \
"Lrr0_%=:\n" \
"v_mfma_f32_16x16x32_bf16 a[20:23], a[232:235], v[64:67], a[20:23]\n" \
"s_mov_b64 s[8:9], s[10:11]\n" \
"v_exp_f32 v23, v23\n" \
"v_mfma_f32_16x16x32_bf16 a[20:23], a[236:239], v[96:99], a[20:23]\n" \
"ds_read_b128 a[232:235], v246 offset:28672\n" \
"v_add_f32 v244, v244, v23\n" \
"v_mfma_f32_16x16x32_bf16 a[24:27], a[240:243], v[64:67], a[24:27]\n" \
"v_exp_f32 v24, v24\n" \
"v_mfma_f32_16x16x32_bf16 a[24:27], a[244:247], v[96:99], a[24:27]\n" \
"v_add_f32 v245, v245, v24\n" \
"Lpg5534_%=:\n" \
"s_cbranch_execz Lpg6770_%=\n" \
"v_cvt_pk_bf16_f32 v19, v22, v23\n" \
"v_mfma_f32_16x16x32_bf16 a[56:59], a[240:243], v[72:75], a[56:59]\n" \
"v_exp_f32 v25, v25\n" \
"v_mfma_f32_16x16x32_bf16 a[56:59], a[244:247], v[104:107], a[56:59]\n" \
"ds_read_b128 a[236:239], v246 offset:30720\n" \
"v_add_f32 v245, v245, v25\n" \
"v_mfma_f32_16x16x32_bf16 v[152:155], a[240:243], v[80:83], v[152:155]\n" \
"v_exp_f32 v26, v26\n" \
"v_mfma_f32_16x16x32_bf16 v[152:155], a[244:247], v[112:115], v[152:155]\n" \
"v_add_f32 v245, v245, v26\n" \
"v_cvt_pk_bf16_f32 v24, v24, v25\n" \
"v_mfma_f32_16x16x32_bf16 v[184:187], a[240:243], v[88:91], v[184:187]\n" \
"v_exp_f32 v27, v27\n" \
"v_mfma_f32_16x16x32_bf16 v[184:187], a[244:247], v[120:123], v[184:187]\n" \
"ds_read_b128 a[240:243], v246 offset:24832\n" \
"v_add_f32 v245, v245, v27\n" \
"v_mfma_f32_16x16x32_bf16 v[188:191], a[248:251], v[88:91], v[188:191]\n" \
"v_exp_f32 v28, v28\n" \
"v_mfma_f32_16x16x32_bf16 v[188:191], a[252:255], v[120:123], v[188:191]\n" \
"v_add_f32 v245, v245, v28\n" \
"v_cvt_pk_bf16_f32 v25, v26, v27\n" \
"v_mfma_f32_16x16x32_bf16 v[156:159], a[248:251], v[80:83], v[156:159]\n" \
"v_exp_f32 v29, v29\n" \
"v_mfma_f32_16x16x32_bf16 v[156:159], a[252:255], v[112:115], v[156:159]\n" \
"ds_read_b128 a[244:247], v246 offset:26880\n" \
"v_add_f32 v245, v245, v29\n" \
"v_mfma_f32_16x16x32_bf16 a[60:63], a[248:251], v[72:75], a[60:63]\n" \
"v_exp_f32 v30, v30\n" \
"v_mfma_f32_16x16x32_bf16 a[60:63], a[252:255], v[104:107], a[60:63]\n" \
"v_add_f32 v245, v245, v30\n" \
"v_cvt_pk_bf16_f32 v26, v28, v29\n" \
"v_mfma_f32_16x16x32_bf16 a[28:31], a[248:251], v[64:67], a[28:31]\n" \
"s_waitcnt lgkmcnt(6)\n" \
"v_exp_f32 v31, v31\n" \
"v_mfma_f32_16x16x32_bf16 a[28:31], a[252:255], v[96:99], a[28:31]\n" \
"ds_read_b128 a[248:251], v246 offset:28928\n" \
"v_add_f32 v245, v245, v31\n" \
"LA1_%=:\n" \
"Lpg5573_%=:\n" \
"s_cbranch_execz Lpg7025_%=\n" \
"v_mfma_f32_16x16x32_bf16 v[64:67], a[192:195], a[128:131], v[224:227]\n" \
"v_exp_f32 v32, v32\n" \
"v_mfma_f32_16x16x32_bf16 v[64:67], a[196:199], a[132:135], v[64:67]\n" \
"v_cvt_pk_bf16_f32 v27, v30, v31\n" \
"v_add_f32 v242, v242, v32\n" \
"v_mfma_f32_16x16x32_bf16 v[64:67], a[200:203], a[136:139], v[64:67]\n" \
"s_mov_b32 m0, s74\n" \
"v_exp_f32 v33, v33\n" \
"v_mfma_f32_16x16x32_bf16 v[64:67], a[204:207], a[140:143], v[64:67]\n" \
"ds_read_b128 a[252:255], v246 offset:30976\n" \
"global_load_lds_dwordx4 v250, s[38:39] offset:0 sc1\n" \
"v_add_f32 v242, v242, v33\n" \
"v_mfma_f32_16x16x32_bf16 v[72:75], a[200:203], a[152:155], v[228:231]\n" \
"v_exp_f32 v34, v34\n" \
"v_mfma_f32_16x16x32_bf16 v[72:75], a[204:207], a[156:159], v[72:75]\n" \
"v_add_f32 v242, v242, v34\n" \
"v_cvt_pk_bf16_f32 v32, v32, v33\n" \
"v_mfma_f32_16x16x32_bf16 v[72:75], a[192:195], a[144:147], v[72:75]\n" \
"v_exp_f32 v35, v35\n" \
"v_mfma_f32_16x16x32_bf16 v[72:75], a[196:199], a[148:151], v[72:75]\n" \
"v_add_f32 v242, v242, v35\n" \
"v_exp_f32 v36, v36\n" \
"v_mfma_f32_16x16x32_bf16 v[80:83], a[192:195], a[160:163], v[232:235]\n" \
"v_add_f32 v242, v242, v36\n" \
"v_cvt_pk_bf16_f32 v33, v34, v35\n" \
"v_mfma_f32_16x16x32_bf16 v[80:83], a[196:199], a[164:167], v[80:83]\n" \
"v_exp_f32 v37, v37\n" \
"v_mfma_f32_16x16x32_bf16 v[80:83], a[200:203], a[168:171], v[80:83]\n" \
"v_add_f32 v242, v242, v37\n" \
"v_mfma_f32_16x16x32_bf16 v[80:83], a[204:207], a[172:175], v[80:83]\n" \
"v_exp_f32 v38, v38\n" \
"v_mfma_f32_16x16x32_bf16 v[88:91], a[200:203], a[184:187], v[236:239]\n" \
"v_add_f32 v242, v242, v38\n" \
"v_cvt_pk_bf16_f32 v34, v36, v37\n" \
"v_mfma_f32_16x16x32_bf16 v[88:91], a[204:207], a[188:191], v[88:91]\n" \
"v_exp_f32 v39, v39\n" \
"v_mfma_f32_16x16x32_bf16 v[88:91], a[192:195], a[176:179], v[88:91]\n" \
"v_add_f32 v242, v242, v39\n" \
"v_mfma_f32_16x16x32_bf16 v[88:91], a[196:199], a[180:183], v[88:91]\n" \
"ds_read_b128 a[192:195], v247 offset:0\n" \
"v_xor_b32 v246, 0x8000, v246\n" \
"v_mfma_f32_16x16x32_bf16 v[92:95], a[208:211], a[176:179], v[236:239]\n" \
"v_exp_f32 v40, v40\n" \
"v_mfma_f32_16x16x32_bf16 v[92:95], a[212:215], a[180:183], v[92:95]\n" \
"v_add_f32 v243, v243, v40\n" \
"v_cvt_pk_bf16_f32 v35, v38, v39\n" \
"v_mfma_f32_16x16x32_bf16 v[92:95], a[216:219], a[184:187], v[92:95]\n" \
"v_exp_f32 v41, v41\n" \
"v_mfma_f32_16x16x32_bf16 v[92:95], a[220:223], a[188:191], v[92:95]\n" \
"ds_read_b128 a[196:199], v247 offset:8192\n" \
"global_load_lds_dwordx4 v250, s[38:39] offset:1024 sc1\n" \
"v_add_f32 v243, v243, v41\n" \
"v_mfma_f32_16x16x32_bf16 v[84:87], a[216:219], a[168:171], v[232:235]\n" \
"v_exp_f32 v42, v42\n" \
"v_mfma_f32_16x16x32_bf16 v[84:87], a[220:223], a[172:175], v[84:87]\n" \
"v_add_f32 v243, v243, v42\n" \
"v_cvt_pk_bf16_f32 v40, v40, v41\n" \
"v_mfma_f32_16x16x32_bf16 v[84:87], a[208:211], a[160:163], v[84:87]\n" \
"v_exp_f32 v43, v43\n" \
"v_mfma_f32_16x16x32_bf16 v[84:87], a[212:215], a[164:167], v[84:87]\n" \
"ds_read_b128 a[200:203], v247 offset:1024\n" \
"v_add_f32 v243, v243, v43\n" \
"v_mfma_f32_16x16x32_bf16 v[76:79], a[208:211], a[144:147], v[228:231]\n" \
"v_exp_f32 v44, v44\n" \
"v_mfma_f32_16x16x32_bf16 v[76:79], a[212:215], a[148:151], v[76:79]\n" \
"v_add_f32 v243, v243, v44\n" \
"v_cvt_pk_bf16_f32 v41, v42, v43\n" \
"v_mfma_f32_16x16x32_bf16 v[76:79], a[216:219], a[152:155], v[76:79]\n" \
"v_exp_f32 v45, v45\n" \
"v_mfma_f32_16x16x32_bf16 v[76:79], a[220:223], a[156:159], v[76:79]\n" \
"ds_read_b128 a[204:207], v247 offset:9216\n" \
"global_load_lds_dwordx4 v250, s[38:39] offset:2048 sc1\n" \
"v_add_f32 v243, v243, v45\n" \
"v_mfma_f32_16x16x32_bf16 v[68:71], a[216:219], a[136:139], v[224:227]\n" \
"v_exp_f32 v46, v46\n" \
"v_mfma_f32_16x16x32_bf16 v[68:71], a[220:223], a[140:143], v[68:71]\n" \
"v_add_f32 v243, v243, v46\n" \
"v_cvt_pk_bf16_f32 v42, v44, v45\n" \
"v_mfma_f32_16x16x32_bf16 v[68:71], a[208:211], a[128:131], v[68:71]\n" \
"s_waitcnt lgkmcnt(4)\n" \
"v_exp_f32 v47, v47\n" \
"v_mfma_f32_16x16x32_bf16 v[68:71], a[212:215], a[132:135], v[68:71]\n" \
"ds_read_b128 a[208:211], v247 offset:2048\n" \
"v_add_f32 v243, v243, v47\n" \
"v_mfma_f32_16x16x32_bf16 v[96:99], a[224:227], a[128:131], v[224:227]\n" \
"v_exp_f32 v48, v48\n" \
"v_mfma_f32_16x16x32_bf16 v[96:99], a[228:231], a[132:135], v[96:99]\n" \
"v_add_f32 v244, v244, v48\n" \
"v_cvt_pk_bf16_f32 v43, v46, v47\n" \
"v_mfma_f32_16x16x32_bf16 v[96:99], a[232:235], a[136:139], v[96:99]\n" \
"v_exp_f32 v49, v49\n" \
"v_mfma_f32_16x16x32_bf16 v[96:99], a[236:239], a[140:143], v[96:99]\n" \
"ds_read_b128 a[212:215], v247 offset:10240\n" \
"v_add_f32 v244, v244, v49\n" \
"v_mfma_f32_16x16x32_bf16 v[104:107], a[232:235], a[152:155], v[228:231]\n" \
"v_exp_f32 v50, v50\n" \
"v_mfma_f32_16x16x32_bf16 v[104:107], a[236:239], a[156:159], v[104:107]\n" \
"v_add_f32 v244, v244, v50\n" \
"v_cvt_pk_bf16_f32 v48, v48, v49\n" \
"v_mfma_f32_16x16x32_bf16 v[104:107], a[224:227], a[144:147], v[104:107]\n" \
"v_exp_f32 v51, v51\n" \
"v_mfma_f32_16x16x32_bf16 v[104:107], a[228:231], a[148:151], v[104:107]\n" \
"ds_read_b128 a[216:219], v247 offset:3072\n" \
"global_load_lds_dwordx4 v250, s[38:39] offset:3072 sc1\n" \
"v_add_f32 v244, v244, v51\n" \
"v_mfma_f32_16x16x32_bf16 v[112:115], a[224:227], a[160:163], v[232:235]\n" \
"s_add_u32 s38, s38, 17408\n" \
"v_exp_f32 v52, v52\n" \
"v_mfma_f32_16x16x32_bf16 v[112:115], a[228:231], a[164:167], v[112:115]\n" \
"v_add_f32 v244, v244, v52\n" \
"v_cvt_pk_bf16_f32 v49, v50, v51\n" \
"v_mfma_f32_16x16x32_bf16 v[112:115], a[232:235], a[168:171], v[112:115]\n" \
"v_exp_f32 v53, v53\n" \
"v_mfma_f32_16x16x32_bf16 v[112:115], a[236:239], a[172:175], v[112:115]\n" \
"ds_read_b128 a[220:223], v247 offset:11264\n" \
"v_add_f32 v244, v244, v53\n" \
"v_exp_f32 v54, v54\n" \
"v_mfma_f32_16x16x32_bf16 v[120:123], a[232:235], a[184:187], v[236:239]\n" \
"global_load_dword v253, v252, s[40:41]\n" \
"v_mfma_f32_16x16x32_bf16 v[120:123], a[236:239], a[188:191], v[120:123]\n" \
"s_sub_u32 s80, s80, 1\n" \
"v_add_f32 v244, v244, v54\n" \
"v_cvt_pk_bf16_f32 v50, v52, v53\n" \
"v_mfma_f32_16x16x32_bf16 v[120:123], a[224:227], a[176:179], v[120:123]\n" \
"s_cmp_gt_i32 s80, 0\n" \
"v_exp_f32 v55, v55\n" \
"v_mfma_f32_16x16x32_bf16 v[120:123], a[228:231], a[180:183], v[120:123]\n" \
"s_cselect_b32 s81, 0x80000, 0\n" \
"ds_read_b128 a[224:227], v247 offset:4096\n" \
"v_add_f32 v244, v244, v55\n" \
"v_mfma_f32_16x16x32_bf16 v[124:127], a[240:243], a[176:179], v[236:239]\n" \
"s_add_u32 s40, s40, s81\n" \
"v_exp_f32 v56, v56\n" \
"v_mfma_f32_16x16x32_bf16 v[124:127], a[244:247], a[180:183], v[124:127]\n" \
"s_addc_u32 s41, s41, 0\n" \
"v_add_f32 v245, v245, v56\n" \
"v_cvt_pk_bf16_f32 v51, v54, v55\n" \
"v_mfma_f32_16x16x32_bf16 v[124:127], a[248:251], a[184:187], v[124:127]\n" \
"v_exp_f32 v57, v57\n" \
"v_mfma_f32_16x16x32_bf16 v[124:127], a[252:255], a[188:191], v[124:127]\n" \
"ds_read_b128 a[228:231], v247 offset:12288\n" \
"v_add_f32 v245, v245, v57\n" \
"v_mfma_f32_16x16x32_bf16 v[116:119], a[248:251], a[168:171], v[232:235]\n" \
"v_exp_f32 v58, v58\n" \
"v_mfma_f32_16x16x32_bf16 v[116:119], a[252:255], a[172:175], v[116:119]\n" \
"v_add_f32 v245, v245, v58\n" \
"v_cvt_pk_bf16_f32 v56, v56, v57\n" \
"v_mfma_f32_16x16x32_bf16 v[116:119], a[240:243], a[160:163], v[116:119]\n" \
"v_exp_f32 v59, v59\n" \
"v_mfma_f32_16x16x32_bf16 v[116:119], a[244:247], a[164:167], v[116:119]\n" \
"ds_read_b128 a[232:235], v247 offset:5120\n" \
"v_add_f32 v245, v245, v59\n" \
"v_mfma_f32_16x16x32_bf16 v[108:111], a[240:243], a[144:147], v[228:231]\n" \
"v_exp_f32 v60, v60\n" \
"v_mfma_f32_16x16x32_bf16 v[108:111], a[244:247], a[148:151], v[108:111]\n" \
"v_add_f32 v245, v245, v60\n" \
"v_cvt_pk_bf16_f32 v57, v58, v59\n" \
"v_mfma_f32_16x16x32_bf16 v[108:111], a[248:251], a[152:155], v[108:111]\n" \
"v_exp_f32 v61, v61\n" \
"v_mfma_f32_16x16x32_bf16 v[108:111], a[252:255], a[156:159], v[108:111]\n" \
"ds_read_b128 a[236:239], v247 offset:13312\n" \
"v_add_f32 v245, v245, v61\n" \
"v_mfma_f32_16x16x32_bf16 v[100:103], a[248:251], a[136:139], v[224:227]\n" \
"v_exp_f32 v62, v62\n" \
"v_mfma_f32_16x16x32_bf16 v[100:103], a[252:255], a[140:143], v[100:103]\n" \
"v_add_f32 v245, v245, v62\n" \
"v_cvt_pk_bf16_f32 v58, v60, v61\n" \
"v_mfma_f32_16x16x32_bf16 v[100:103], a[240:243], a[128:131], v[100:103]\n" \
"s_waitcnt vmcnt(10) lgkmcnt(4)\n" \
"v_exp_f32 v63, v63\n" \
"v_mfma_f32_16x16x32_bf16 v[100:103], a[244:247], a[132:135], v[100:103]\n" \
"s_barrier\n" \
"ds_read_b128 a[240:243], v247 offset:6144\n" \
"v_add_f32 v245, v245, v63\n" \
"v_mfma_f32_16x16x32_bf16 a[0:3], a[192:195], v[0:3], a[0:3]\n" \
"buffer_store_dword v202, v203, s[56:59], s84 offen sc1\n" \
"v_mfma_f32_16x16x32_bf16 a[0:3], a[196:199], v[32:35], a[0:3]\n" \
"s_mov_b32 s84, 0x20000000\n" \
"v_cvt_pk_bf16_f32 v59, v62, v63\n" \
"v_exp_f32 v64, v64\n" \
"v_mfma_f32_16x16x32_bf16 a[32:35], a[192:195], v[8:11], a[32:35]\n" \
"s_mov_b32 m0, s73\n" \
"ds_read_b128 a[244:247], v247 offset:14336\n" \
"v_add_f32 v242, v242, v64\n" \
"v_mfma_f32_16x16x32_bf16 a[32:35], a[196:199], v[40:43], a[32:35]\n" \
"ds_read_b128 a[248:251], v247 offset:7168\n" \
"global_load_lds_dwordx4 v250, s[36:37] offset:0 sc1\n" \
"v_exp_f32 v65, v65\n" \
"v_mfma_f32_16x16x32_bf16 v[128:131], a[192:195], v[16:19], v[128:131]\n" \
"ds_read_b128 a[252:255], v247 offset:15360\n" \
"v_add_f32 v242, v242, v65\n" \
"v_exp_f32 v66, v66\n" \
"v_mfma_f32_16x16x32_bf16 v[128:131], a[196:199], v[48:51], v[128:131]\n" \
"v_add_f32 v242, v242, v66\n" \
"v_cvt_pk_bf16_f32 v64, v64, v65\n" \
"v_mfma_f32_16x16x32_bf16 v[160:163], a[192:195], v[24:27], v[160:163]\n" \
"v_exp_f32 v67, v67\n" \
"v_mfma_f32_16x16x32_bf16 v[160:163], a[196:199], v[56:59], v[160:163]\n" \
"ds_read_b128 a[192:195], v246 offset:0\n" \
"v_add_f32 v242, v242, v67\n" \
"v_mfma_f32_16x16x32_bf16 v[164:167], a[200:203], v[24:27], v[164:167]\n" \
"v_exp_f32 v68, v68\n" \
"v_mfma_f32_16x16x32_bf16 v[164:167], a[204:207], v[56:59], v[164:167]\n" \
"v_add_f32 v242, v242, v68\n" \
"v_cvt_pk_bf16_f32 v65, v66, v67\n" \
"v_mfma_f32_16x16x32_bf16 v[132:135], a[200:203], v[16:19], v[132:135]\n" \
"v_exp_f32 v69, v69\n" \
"v_mfma_f32_16x16x32_bf16 v[132:135], a[204:207], v[48:51], v[132:135]\n" \
"ds_read_b128 a[196:199], v246 offset:2048\n" \
"v_add_f32 v242, v242, v69\n" \
"v_mfma_f32_16x16x32_bf16 a[36:39], a[200:203], v[8:11], a[36:39]\n" \
"v_exp_f32 v70, v70\n" \
"v_mfma_f32_16x16x32_bf16 a[36:39], a[204:207], v[40:43], a[36:39]\n" \
"v_add_f32 v242, v242, v70\n" \
"v_cvt_pk_bf16_f32 v66, v68, v69\n" \
"v_mfma_f32_16x16x32_bf16 a[4:7], a[200:203], v[0:3], a[4:7]\n" \
"v_exp_f32 v71, v71\n" \
"v_mfma_f32_16x16x32_bf16 a[4:7], a[204:207], v[32:35], a[4:7]\n" \
"ds_read_b128 a[200:203], v246 offset:4096\n" \
"global_load_lds_dwordx4 v250, s[36:37] offset:1024 sc1\n" \
"v_add_f32 v242, v242, v71\n" \
"v_mfma_f32_16x16x32_bf16 a[8:11], a[208:211], v[0:3], a[8:11]\n" \
"v_exp_f32 v72, v72\n" \
"v_mfma_f32_16x16x32_bf16 a[8:11], a[212:215], v[32:35], a[8:11]\n" \
"v_add_f32 v243, v243, v72\n" \
"v_cvt_pk_bf16_f32 v67, v70, v71\n" \
"v_mfma_f32_16x16x32_bf16 a[40:43], a[208:211], v[8:11], a[40:43]\n" \
"v_exp_f32 v73, v73\n" \
"v_mfma_f32_16x16x32_bf16 a[40:43], a[212:215], v[40:43], a[40:43]\n" \
"ds_read_b128 a[204:207], v246 offset:6144\n" \
"v_add_f32 v243, v243, v73\n" \
"v_mfma_f32_16x16x32_bf16 v[136:139], a[208:211], v[16:19], v[136:139]\n" \
"v_exp_f32 v74, v74\n" \
"v_mfma_f32_16x16x32_bf16 v[136:139], a[212:215], v[48:51], v[136:139]\n" \
"v_add_f32 v243, v243, v74\n" \
"v_cvt_pk_bf16_f32 v72, v72, v73\n" \
"v_mfma_f32_16x16x32_bf16 v[168:171], a[208:211], v[24:27], v[168:171]\n" \
"v_exp_f32 v75, v75\n" \
"v_mfma_f32_16x16x32_bf16 v[168:171], a[212:215], v[56:59], v[168:171]\n" \
"ds_read_b128 a[208:211], v246 offset:256\n" \
"v_add_f32 v243, v243, v75\n" \
"v_mfma_f32_16x16x32_bf16 v[172:175], a[216:219], v[24:27], v[172:175]\n" \
"v_exp_f32 v76, v76\n" \
"v_mfma_f32_16x16x32_bf16 v[172:175], a[220:223], v[56:59], v[172:175]\n" \
"v_add_f32 v243, v243, v76\n" \
"v_cvt_pk_bf16_f32 v73, v74, v75\n" \
"v_mfma_f32_16x16x32_bf16 v[140:143], a[216:219], v[16:19], v[140:143]\n" \
"v_exp_f32 v77, v77\n" \
"v_mfma_f32_16x16x32_bf16 v[140:143], a[220:223], v[48:51], v[140:143]\n" \
"ds_read_b128 a[212:215], v246 offset:2304\n" \
"global_load_lds_dwordx4 v250, s[36:37] offset:2048 sc1\n" \
"v_add_f32 v243, v243, v77\n" \
"v_mfma_f32_16x16x32_bf16 a[44:47], a[216:219], v[8:11], a[44:47]\n" \
"v_exp_f32 v78, v78\n" \
"v_mfma_f32_16x16x32_bf16 a[44:47], a[220:223], v[40:43], a[44:47]\n" \
"v_add_f32 v243, v243, v78\n" \
"v_cvt_pk_bf16_f32 v74, v76, v77\n" \
"v_mfma_f32_16x16x32_bf16 a[12:15], a[216:219], v[0:3], a[12:15]\n" \
"s_waitcnt lgkmcnt(6)\n" \
"v_exp_f32 v79, v79\n" \
"v_mfma_f32_16x16x32_bf16 a[12:15], a[220:223], v[32:35], a[12:15]\n" \
"ds_read_b128 a[216:219], v246 offset:4352\n" \
"v_add_f32 v243, v243, v79\n" \
"v_mfma_f32_16x16x32_bf16 a[16:19], a[224:227], v[0:3], a[16:19]\n" \
"v_exp_f32 v80, v80\n" \
"v_mfma_f32_16x16x32_bf16 a[16:19], a[228:231], v[32:35], a[16:19]\n" \
"v_add_f32 v244, v244, v80\n" \
"v_cvt_pk_bf16_f32 v75, v78, v79\n" \
"v_mfma_f32_16x16x32_bf16 a[48:51], a[224:227], v[8:11], a[48:51]\n" \
"v_exp_f32 v81, v81\n" \
"v_mfma_f32_16x16x32_bf16 a[48:51], a[228:231], v[40:43], a[48:51]\n" \
"ds_read_b128 a[220:223], v246 offset:6400\n" \
"v_add_f32 v244, v244, v81\n" \
"v_mfma_f32_16x16x32_bf16 v[144:147], a[224:227], v[16:19], v[144:147]\n" \
"v_exp_f32 v82, v82\n" \
"v_mfma_f32_16x16x32_bf16 v[144:147], a[228:231], v[48:51], v[144:147]\n" \
"v_add_f32 v244, v244, v82\n" \
"v_cvt_pk_bf16_f32 v80, v80, v81\n" \
"v_mfma_f32_16x16x32_bf16 v[176:179], a[224:227], v[24:27], v[176:179]\n" \
"v_exp_f32 v83, v83\n" \
"v_mfma_f32_16x16x32_bf16 v[176:179], a[228:231], v[56:59], v[176:179]\n" \
"ds_read_b128 a[224:227], v246 offset:8192\n" \
"global_load_lds_dwordx4 v250, s[36:37] offset:3072 sc1\n" \
"v_add_f32 v244, v244, v83\n" \
"v_mfma_f32_16x16x32_bf16 v[180:183], a[232:235], v[24:27], v[180:183]\n" \
"s_add_u32 s36, s36, 17408\n" \
"v_exp_f32 v84, v84\n" \
"v_mfma_f32_16x16x32_bf16 v[180:183], a[236:239], v[56:59], v[180:183]\n" \
"v_add_f32 v244, v244, v84\n" \
"v_cvt_pk_bf16_f32 v81, v82, v83\n" \
"v_mfma_f32_16x16x32_bf16 v[148:151], a[232:235], v[16:19], v[148:151]\n" \
"v_exp_f32 v85, v85\n" \
"v_mfma_f32_16x16x32_bf16 v[148:151], a[236:239], v[48:51], v[148:151]\n" \
"ds_read_b128 a[228:231], v246 offset:10240\n" \
"v_add_f32 v244, v244, v85\n" \
"v_mfma_f32_16x16x32_bf16 a[52:55], a[232:235], v[8:11], a[52:55]\n" \
"v_exp_f32 v86, v86\n" \
"v_mfma_f32_16x16x32_bf16 a[52:55], a[236:239], v[40:43], a[52:55]\n" \
"v_add_f32 v244, v244, v86\n" \
"v_cvt_pk_bf16_f32 v82, v84, v85\n" \
"v_mfma_f32_16x16x32_bf16 a[20:23], a[232:235], v[0:3], a[20:23]\n" \
"v_exp_f32 v87, v87\n" \
"v_mfma_f32_16x16x32_bf16 a[20:23], a[236:239], v[32:35], a[20:23]\n" \
"ds_read_b128 a[232:235], v246 offset:12288\n" \
"v_add_f32 v244, v244, v87\n" \
"v_mfma_f32_16x16x32_bf16 a[24:27], a[240:243], v[0:3], a[24:27]\n" \
"v_exp_f32 v88, v88\n" \
"v_mfma_f32_16x16x32_bf16 a[24:27], a[244:247], v[32:35], a[24:27]\n" \
"v_add_f32 v245, v245, v88\n" \
"v_cvt_pk_bf16_f32 v83, v86, v87\n" \
"v_mfma_f32_16x16x32_bf16 a[56:59], a[240:243], v[8:11], a[56:59]\n" \
"v_exp_f32 v89, v89\n" \
"v_mfma_f32_16x16x32_bf16 a[56:59], a[244:247], v[40:43], a[56:59]\n" \
"ds_read_b128 a[236:239], v246 offset:14336\n" \
"v_add_f32 v245, v245, v89\n" \
"v_mfma_f32_16x16x32_bf16 v[152:155], a[240:243], v[16:19], v[152:155]\n" \
"v_exp_f32 v90, v90\n" \
"v_mfma_f32_16x16x32_bf16 v[152:155], a[244:247], v[48:51], v[152:155]\n" \
"v_add_f32 v245, v245, v90\n" \
"v_cvt_pk_bf16_f32 v88, v88, v89\n" \
"v_mfma_f32_16x16x32_bf16 v[184:187], a[240:243], v[24:27], v[184:187]\n" \
"v_exp_f32 v91, v91\n" \
"v_mfma_f32_16x16x32_bf16 v[184:187], a[244:247], v[56:59], v[184:187]\n" \
"ds_read_b128 a[240:243], v246 offset:8448\n" \
"v_add_f32 v245, v245, v91\n" \
"v_mfma_f32_16x16x32_bf16 v[188:191], a[248:251], v[24:27], v[188:191]\n" \
"v_exp_f32 v92, v92\n" \
"v_mfma_f32_16x16x32_bf16 v[188:191], a[252:255], v[56:59], v[188:191]\n" \
"v_add_f32 v245, v245, v92\n" \
"v_cvt_pk_bf16_f32 v89, v90, v91\n" \
"v_mfma_f32_16x16x32_bf16 v[156:159], a[248:251], v[16:19], v[156:159]\n" \
"v_exp_f32 v93, v93\n" \
"v_mfma_f32_16x16x32_bf16 v[156:159], a[252:255], v[48:51], v[156:159]\n" \
"ds_read_b128 a[244:247], v246 offset:10496\n" \
"v_add_f32 v245, v245, v93\n" \
"v_mfma_f32_16x16x32_bf16 a[60:63], a[248:251], v[8:11], a[60:63]\n" \
"v_exp_f32 v94, v94\n" \
"v_mfma_f32_16x16x32_bf16 a[60:63], a[252:255], v[40:43], a[60:63]\n" \
"v_add_f32 v245, v245, v94\n" \
"v_cvt_pk_bf16_f32 v90, v92, v93\n" \
"v_mfma_f32_16x16x32_bf16 a[28:31], a[248:251], v[0:3], a[28:31]\n" \
"s_waitcnt lgkmcnt(6)\n" \
"v_exp_f32 v95, v95\n" \
"v_mfma_f32_16x16x32_bf16 a[28:31], a[252:255], v[32:35], a[28:31]\n" \
"ds_read_b128 a[248:251], v246 offset:12544\n" \
"v_add_f32 v245, v245, v95\n" \
"v_mfma_f32_16x16x32_bf16 v[0:3], a[192:195], a[128:131], v[224:227]\n" \
"v_exp_f32 v96, v96\n" \
"v_mfma_f32_16x16x32_bf16 v[0:3], a[196:199], a[132:135], v[0:3]\n" \
"v_cvt_pk_bf16_f32 v91, v94, v95\n" \
"v_add_f32 v242, v242, v96\n" \
"v_mfma_f32_16x16x32_bf16 v[0:3], a[200:203], a[136:139], v[0:3]\n" \
"s_add_u32 m0, s74, 0x4000\n" \
"v_exp_f32 v97, v97\n" \
"v_mfma_f32_16x16x32_bf16 v[0:3], a[204:207], a[140:143], v[0:3]\n" \
"ds_read_b128 a[252:255], v246 offset:14592\n" \
"global_load_lds_dwordx4 v250, s[38:39] offset:0 sc1\n" \
"v_add_f32 v242, v242, v97\n" \
"v_mfma_f32_16x16x32_bf16 v[8:11], a[200:203], a[152:155], v[228:231]\n" \
"v_exp_f32 v98, v98\n" \
"v_mfma_f32_16x16x32_bf16 v[8:11], a[204:207], a[156:159], v[8:11]\n" \
"v_add_f32 v242, v242, v98\n" \
"v_cvt_pk_bf16_f32 v96, v96, v97\n" \
"v_mfma_f32_16x16x32_bf16 v[8:11], a[192:195], a[144:147], v[8:11]\n" \
"v_exp_f32 v99, v99\n" \
"v_mfma_f32_16x16x32_bf16 v[8:11], a[196:199], a[148:151], v[8:11]\n" \
"v_add_f32 v242, v242, v99\n" \
"v_exp_f32 v100, v100\n" \
"v_mfma_f32_16x16x32_bf16 v[16:19], a[192:195], a[160:163], v[232:235]\n" \
"v_add_f32 v242, v242, v100\n" \
"v_cvt_pk_bf16_f32 v97, v98, v99\n" \
"v_mfma_f32_16x16x32_bf16 v[16:19], a[196:199], a[164:167], v[16:19]\n" \
"v_exp_f32 v101, v101\n" \
"v_mfma_f32_16x16x32_bf16 v[16:19], a[200:203], a[168:171], v[16:19]\n" \
"v_add_f32 v242, v242, v101\n" \
"v_mfma_f32_16x16x32_bf16 v[16:19], a[204:207], a[172:175], v[16:19]\n" \
"v_exp_f32 v102, v102\n" \
"v_mfma_f32_16x16x32_bf16 v[24:27], a[200:203], a[184:187], v[236:239]\n" \
"v_add_f32 v242, v242, v102\n" \
"v_cvt_pk_bf16_f32 v98, v100, v101\n" \
"v_mfma_f32_16x16x32_bf16 v[24:27], a[204:207], a[188:191], v[24:27]\n" \
"v_exp_f32 v103, v103\n" \
"v_mfma_f32_16x16x32_bf16 v[24:27], a[192:195], a[176:179], v[24:27]\n" \
"v_add_f32 v242, v242, v103\n" \
"v_mfma_f32_16x16x32_bf16 v[24:27], a[196:199], a[180:183], v[24:27]\n" \
"ds_read_b128 a[192:195], v247 offset:16384\n" \
"global_load_lds_dwordx4 v250, s[38:39] offset:1024 sc1\n" \
"v_mfma_f32_16x16x32_bf16 v[28:31], a[208:211], a[176:179], v[236:239]\n" \
"v_exp_f32 v104, v104\n" \
"v_mfma_f32_16x16x32_bf16 v[28:31], a[212:215], a[180:183], v[28:31]\n" \
"v_add_f32 v243, v243, v104\n" \
"v_cvt_pk_bf16_f32 v99, v102, v103\n" \
"v_mfma_f32_16x16x32_bf16 v[28:31], a[216:219], a[184:187], v[28:31]\n" \
"v_exp_f32 v105, v105\n" \
"v_mfma_f32_16x16x32_bf16 v[28:31], a[220:223], a[188:191], v[28:31]\n" \
"ds_read_b128 a[196:199], v247 offset:24576\n" \
"v_add_f32 v243, v243, v105\n" \
"v_mfma_f32_16x16x32_bf16 v[20:23], a[216:219], a[168:171], v[232:235]\n" \
"v_exp_f32 v106, v106\n" \
"v_mfma_f32_16x16x32_bf16 v[20:23], a[220:223], a[172:175], v[20:23]\n" \
"v_add_f32 v243, v243, v106\n" \
"v_cvt_pk_bf16_f32 v104, v104, v105\n" \
"v_mfma_f32_16x16x32_bf16 v[20:23], a[208:211], a[160:163], v[20:23]\n" \
"v_exp_f32 v107, v107\n" \
"v_mfma_f32_16x16x32_bf16 v[20:23], a[212:215], a[164:167], v[20:23]\n" \
"ds_read_b128 a[200:203], v247 offset:17408\n" \
"v_add_f32 v243, v243, v107\n" \
"v_mfma_f32_16x16x32_bf16 v[12:15], a[208:211], a[144:147], v[228:231]\n" \
"v_exp_f32 v108, v108\n" \
"v_mfma_f32_16x16x32_bf16 v[12:15], a[212:215], a[148:151], v[12:15]\n" \
"v_add_f32 v243, v243, v108\n" \
"v_cvt_pk_bf16_f32 v105, v106, v107\n" \
"v_mfma_f32_16x16x32_bf16 v[12:15], a[216:219], a[152:155], v[12:15]\n" \
"v_exp_f32 v109, v109\n" \
"v_mfma_f32_16x16x32_bf16 v[12:15], a[220:223], a[156:159], v[12:15]\n" \
"ds_read_b128 a[204:207], v247 offset:25600\n" \
"global_load_lds_dwordx4 v250, s[38:39] offset:2048 sc1\n" \
"v_add_f32 v243, v243, v109\n" \
"v_mfma_f32_16x16x32_bf16 v[4:7], a[216:219], a[136:139], v[224:227]\n" \
"v_exp_f32 v110, v110\n" \
"v_mfma_f32_16x16x32_bf16 v[4:7], a[220:223], a[140:143], v[4:7]\n" \
"v_add_f32 v243, v243, v110\n" \
"v_cvt_pk_bf16_f32 v106, v108, v109\n" \
"v_mfma_f32_16x16x32_bf16 v[4:7], a[208:211], a[128:131], v[4:7]\n" \
"s_waitcnt lgkmcnt(4)\n" \
"v_exp_f32 v111, v111\n" \
"v_mfma_f32_16x16x32_bf16 v[4:7], a[212:215], a[132:135], v[4:7]\n" \
"ds_read_b128 a[208:211], v247 offset:18432\n" \
"v_add_f32 v243, v243, v111\n" \
"v_mfma_f32_16x16x32_bf16 v[32:35], a[224:227], a[128:131], v[224:227]\n" \
"v_exp_f32 v112, v112\n" \
"v_mfma_f32_16x16x32_bf16 v[32:35], a[228:231], a[132:135], v[32:35]\n" \
"v_add_f32 v244, v244, v112\n" \
"v_cvt_pk_bf16_f32 v107, v110, v111\n" \
"v_mfma_f32_16x16x32_bf16 v[32:35], a[232:235], a[136:139], v[32:35]\n" \
"v_exp_f32 v113, v113\n" \
"v_mfma_f32_16x16x32_bf16 v[32:35], a[236:239], a[140:143], v[32:35]\n" \
"ds_read_b128 a[212:215], v247 offset:26624\n" \
"v_add_f32 v244, v244, v113\n" \
"v_mfma_f32_16x16x32_bf16 v[40:43], a[232:235], a[152:155], v[228:231]\n" \
"v_exp_f32 v114, v114\n" \
"v_mfma_f32_16x16x32_bf16 v[40:43], a[236:239], a[156:159], v[40:43]\n" \
"v_add_f32 v244, v244, v114\n" \
"v_cvt_pk_bf16_f32 v112, v112, v113\n" \
"v_mfma_f32_16x16x32_bf16 v[40:43], a[224:227], a[144:147], v[40:43]\n" \
"v_exp_f32 v115, v115\n" \
"v_mfma_f32_16x16x32_bf16 v[40:43], a[228:231], a[148:151], v[40:43]\n" \
"ds_read_b128 a[216:219], v247 offset:19456\n" \
"global_load_lds_dwordx4 v250, s[38:39] offset:3072 sc1\n" \
"v_add_f32 v244, v244, v115\n" \
"v_exp_f32 v116, v116\n" \
"v_mfma_f32_16x16x32_bf16 v[48:51], a[224:227], a[160:163], v[232:235]\n" \
"s_add_u32 s38, s38, 17408\n" \
"v_add_f32 v244, v244, v116\n" \
"v_cvt_pk_bf16_f32 v113, v114, v115\n" \
"v_mfma_f32_16x16x32_bf16 v[48:51], a[228:231], a[164:167], v[48:51]\n" \
"global_load_dword v253, v252, s[40:41]\n" \
"v_mfma_f32_16x16x32_bf16 v[48:51], a[232:235], a[168:171], v[48:51]\n" \
"s_sub_u32 s80, s80, 1\n" \
"v_exp_f32 v117, v117\n" \
"v_mfma_f32_16x16x32_bf16 v[48:51], a[236:239], a[172:175], v[48:51]\n" \
"s_cmp_gt_i32 s80, 0\n" \
"ds_read_b128 a[220:223], v247 offset:27648\n" \
"v_add_f32 v244, v244, v117\n" \
"v_mfma_f32_16x16x32_bf16 v[56:59], a[232:235], a[184:187], v[236:239]\n" \
"s_cselect_b32 s81, 0x80000, 0\n" \
"v_exp_f32 v118, v118\n" \
"v_mfma_f32_16x16x32_bf16 v[56:59], a[236:239], a[188:191], v[56:59]\n" \
"s_add_u32 s40, s40, s81\n" \
"v_add_f32 v244, v244, v118\n" \
"v_cvt_pk_bf16_f32 v114, v116, v117\n" \
"v_mfma_f32_16x16x32_bf16 v[56:59], a[224:227], a[176:179], v[56:59]\n" \
"s_addc_u32 s41, s41, 0\n" \
"v_exp_f32 v119, v119\n" \
"v_mfma_f32_16x16x32_bf16 v[56:59], a[228:231], a[180:183], v[56:59]\n" \
"s_load_dwordx8 s[44:51], s[56:57], s79 glc\n" \
"ds_read_b128 a[224:227], v247 offset:20480\n" \
"v_add_f32 v244, v244, v119\n" \
"v_mfma_f32_16x16x32_bf16 v[60:63], a[240:243], a[176:179], v[236:239]\n" \
"s_xor_b32 s74, s74, 0x8000\n" \
"v_exp_f32 v120, v120\n" \
"v_mfma_f32_16x16x32_bf16 v[60:63], a[244:247], a[180:183], v[60:63]\n" \
"s_sub_u32 s75, s75, 1\n" \
"v_add_f32 v245, v245, v120\n" \
"v_cvt_pk_bf16_f32 v115, v118, v119\n" \
"v_mfma_f32_16x16x32_bf16 v[60:63], a[248:251], a[184:187], v[60:63]\n" \
"s_cmp_lg_u32 s75, 0\n" \
"v_exp_f32 v121, v121\n" \
"v_mfma_f32_16x16x32_bf16 v[60:63], a[252:255], a[188:191], v[60:63]\n" \
"ds_read_b128 a[228:231], v247 offset:28672\n" \
"v_add_f32 v245, v245, v121\n" \
"v_mfma_f32_16x16x32_bf16 v[52:55], a[248:251], a[168:171], v[232:235]\n" \
"v_exp_f32 v122, v122\n" \
"v_mfma_f32_16x16x32_bf16 v[52:55], a[252:255], a[172:175], v[52:55]\n" \
"v_add_f32 v245, v245, v122\n" \
"v_cvt_pk_bf16_f32 v120, v120, v121\n" \
"v_mfma_f32_16x16x32_bf16 v[52:55], a[240:243], a[160:163], v[52:55]\n" \
"v_exp_f32 v123, v123\n" \
"v_mfma_f32_16x16x32_bf16 v[52:55], a[244:247], a[164:167], v[52:55]\n" \
"ds_read_b128 a[232:235], v247 offset:21504\n" \
"v_add_f32 v245, v245, v123\n" \
"v_mfma_f32_16x16x32_bf16 v[44:47], a[240:243], a[144:147], v[228:231]\n" \
"v_exp_f32 v124, v124\n" \
"v_mfma_f32_16x16x32_bf16 v[44:47], a[244:247], a[148:151], v[44:47]\n" \
"v_add_f32 v245, v245, v124\n" \
"v_cvt_pk_bf16_f32 v121, v122, v123\n" \
"v_mfma_f32_16x16x32_bf16 v[44:47], a[248:251], a[152:155], v[44:47]\n" \
"v_exp_f32 v125, v125\n" \
"v_mfma_f32_16x16x32_bf16 v[44:47], a[252:255], a[156:159], v[44:47]\n" \
"ds_read_b128 a[236:239], v247 offset:29696\n" \
"v_add_f32 v245, v245, v125\n" \
"v_mfma_f32_16x16x32_bf16 v[36:39], a[248:251], a[136:139], v[224:227]\n" \
"v_exp_f32 v126, v126\n" \
"v_mfma_f32_16x16x32_bf16 v[36:39], a[252:255], a[140:143], v[36:39]\n" \
"v_add_f32 v245, v245, v126\n" \
"v_cvt_pk_bf16_f32 v122, v124, v125\n" \
"v_mfma_f32_16x16x32_bf16 v[36:39], a[240:243], a[128:131], v[36:39]\n" \
"s_waitcnt vmcnt(11) lgkmcnt(4)\n" \
"v_exp_f32 v127, v127\n" \
"v_mfma_f32_16x16x32_bf16 v[36:39], a[244:247], a[132:135], v[36:39]\n" \
"s_barrier\n" \
"ds_read_b128 a[240:243], v247 offset:22528\n" \
"v_add_f32 v245, v245, v127\n" \
"s_cbranch_scc1 Lloop_%=\n" \
"v_mfma_f32_16x16x32_bf16 a[0:3], a[192:195], v[64:67], a[0:3]\n" \
"v_exp_f32 v8, v8\n" \
"v_mfma_f32_16x16x32_bf16 a[0:3], a[196:199], v[96:99], a[0:3]\n" \
"v_cvt_pk_bf16_f32 v123, v126, v127\n" \
"v_exp_f32 v9, v9\n" \
"v_mfma_f32_16x16x32_bf16 a[32:35], a[192:195], v[72:75], a[32:35]\n" \
"s_add_u32 m0, s73, 0x4000\n" \
"ds_read_b128 a[244:247], v247 offset:30720\n" \
"v_exp_f32 v10, v10\n" \
"v_mfma_f32_16x16x32_bf16 a[32:35], a[196:199], v[104:107], a[32:35]\n" \
"ds_read_b128 a[248:251], v247 offset:23552\n" \
"global_load_lds_dwordx4 v250, s[36:37] offset:0 sc1\n" \
"v_mfma_f32_16x16x32_bf16 v[128:131], a[192:195], v[80:83], v[128:131]\n" \
"ds_read_b128 a[252:255], v247 offset:31744\n" \
"v_cvt_pk_bf16_f32 v8, v8, v9\n" \
"v_mfma_f32_16x16x32_bf16 v[128:131], a[196:199], v[112:115], v[128:131]\n" \
"v_exp_f32 v11, v11\n" \
"v_mfma_f32_16x16x32_bf16 v[160:163], a[192:195], v[88:91], v[160:163]\n" \
"v_exp_f32 v12, v12\n" \
"v_mfma_f32_16x16x32_bf16 v[160:163], a[196:199], v[120:123], v[160:163]\n" \
"ds_read_b128 a[192:195], v246 offset:16384\n" \
"v_xor_b32 v247, 0x8000, v247\n" \
"v_cvt_pk_bf16_f32 v9, v10, v11\n" \
"v_mfma_f32_16x16x32_bf16 v[164:167], a[200:203], v[88:91], v[164:167]\n" \
"v_exp_f32 v13, v13\n" \
"v_mfma_f32_16x16x32_bf16 v[164:167], a[204:207], v[120:123], v[164:167]\n" \
"v_exp_f32 v14, v14\n" \
"v_cvt_pk_bf16_f32 v10, v12, v13\n" \
"v_mfma_f32_16x16x32_bf16 v[132:135], a[200:203], v[80:83], v[132:135]\n" \
"v_exp_f32 v15, v15\n" \
"v_mfma_f32_16x16x32_bf16 v[132:135], a[204:207], v[112:115], v[132:135]\n" \
"ds_read_b128 a[196:199], v246 offset:18432\n" \
"v_cmp_gt_i32 vcc, 0, v251\n" \
"v_cmp_gt_i32 s[64:65], 1, v251\n" \
"v_mfma_f32_16x16x32_bf16 a[36:39], a[200:203], v[72:75], a[36:39]\n" \
"v_cmp_gt_i32 s[66:67], 2, v251\n" \
"v_cndmask_b32 v0, v0, v254, vcc\n" \
"v_mfma_f32_16x16x32_bf16 a[36:39], a[204:207], v[104:107], a[36:39]\n" \
"v_cmp_gt_i32 vcc, 3, v251\n" \
"v_cndmask_b32 v1, v1, v254, s[64:65]\n" \
"v_mfma_f32_16x16x32_bf16 a[4:7], a[200:203], v[64:67], a[4:7]\n" \
"v_cmp_gt_i32 s[64:65], 16, v251\n" \
"v_cndmask_b32 v2, v2, v254, s[66:67]\n" \
"v_mfma_f32_16x16x32_bf16 a[4:7], a[204:207], v[96:99], a[4:7]\n" \
"ds_read_b128 a[200:203], v246 offset:20480\n" \
"global_load_lds_dwordx4 v250, s[36:37] offset:1024 sc1\n" \
"v_mfma_f32_16x16x32_bf16 a[8:11], a[208:211], v[64:67], a[8:11]\n" \
"v_cmp_gt_i32 s[66:67], 17, v251\n" \
"v_cndmask_b32 v3, v3, v254, vcc\n" \
"v_mfma_f32_16x16x32_bf16 a[8:11], a[212:215], v[96:99], a[8:11]\n" \
"v_cmp_gt_i32 vcc, 18, v251\n" \
"v_cndmask_b32 v4, v4, v254, s[64:65]\n" \
"v_mfma_f32_16x16x32_bf16 a[40:43], a[208:211], v[72:75], a[40:43]\n" \
"v_cmp_gt_i32 s[64:65], 19, v251\n" \
"v_cndmask_b32 v5, v5, v254, s[66:67]\n" \
"v_mfma_f32_16x16x32_bf16 a[40:43], a[212:215], v[104:107], a[40:43]\n" \
"ds_read_b128 a[204:207], v246 offset:22528\n" \
"v_cmp_gt_i32 s[66:67], 32, v251\n" \
"v_mfma_f32_16x16x32_bf16 v[136:139], a[208:211], v[80:83], v[136:139]\n" \
"v_cndmask_b32 v6, v6, v254, vcc\n" \
"v_cmp_gt_i32 vcc, 33, v251\n" \
"v_mfma_f32_16x16x32_bf16 v[136:139], a[212:215], v[112:115], v[136:139]\n" \
"v_cndmask_b32 v7, v7, v254, s[64:65]\n" \
"v_cmp_gt_i32 s[64:65], 34, v251\n" \
"v_mfma_f32_16x16x32_bf16 v[168:171], a[208:211], v[88:91], v[168:171]\n" \
"v_cndmask_b32 v32, v32, v254, s[66:67]\n" \
"v_cmp_gt_i32 s[66:67], 35, v251\n" \
"v_mfma_f32_16x16x32_bf16 v[168:171], a[212:215], v[120:123], v[168:171]\n" \
"ds_read_b128 a[208:211], v246 offset:16640\n" \
"v_cndmask_b32 v33, v33, v254, vcc\n" \
"v_mfma_f32_16x16x32_bf16 v[172:175], a[216:219], v[88:91], v[172:175]\n" \
"v_cmp_gt_i32 vcc, 48, v251\n" \
"v_cndmask_b32 v34, v34, v254, s[64:65]\n" \
"Lpg6171_%=:\n" \
"s_cbranch_execz Lpg7550_%=\n" \
"v_mfma_f32_16x16x32_bf16 v[172:175], a[220:223], v[120:123], v[172:175]\n" \
"v_cmp_gt_i32 s[64:65], 49, v251\n" \
"v_cndmask_b32 v35, v35, v254, s[66:67]\n" \
"v_mfma_f32_16x16x32_bf16 v[140:143], a[216:219], v[80:83], v[140:143]\n" \
"v_cmp_gt_i32 s[66:67], 50, v251\n" \
"v_cndmask_b32 v36, v36, v254, vcc\n" \
"v_mfma_f32_16x16x32_bf16 v[140:143], a[220:223], v[112:115], v[140:143]\n" \
"ds_read_b128 a[212:215], v246 offset:18688\n" \
"global_load_lds_dwordx4 v250, s[36:37] offset:2048 sc1\n" \
"v_mfma_f32_16x16x32_bf16 a[44:47], a[216:219], v[72:75], a[44:47]\n" \
"v_cmp_gt_i32 vcc, 51, v251\n" \
"v_cndmask_b32 v37, v37, v254, s[64:65]\n" \
"v_mfma_f32_16x16x32_bf16 a[44:47], a[220:223], v[104:107], a[44:47]\n" \
"v_cndmask_b32 v38, v38, v254, s[66:67]\n" \
"v_cndmask_b32 v39, v39, v254, vcc\n" \
"v_mfma_f32_16x16x32_bf16 a[12:15], a[216:219], v[64:67], a[12:15]\n" \
"s_waitcnt lgkmcnt(6)\n" \
"v_exp_f32 v0, v0\n" \
"v_mfma_f32_16x16x32_bf16 a[12:15], a[220:223], v[96:99], a[12:15]\n" \
"ds_read_b128 a[216:219], v246 offset:20736\n" \
"v_cvt_pk_bf16_f32 v11, v14, v15\n" \
"v_mfma_f32_16x16x32_bf16 a[16:19], a[224:227], v[64:67], a[16:19]\n" \
"v_exp_f32 v1, v1\n" \
"v_mfma_f32_16x16x32_bf16 a[16:19], a[228:231], v[96:99], a[16:19]\n" \
"v_exp_f32 v2, v2\n" \
"v_cvt_pk_bf16_f32 v0, v0, v1\n" \
"v_mfma_f32_16x16x32_bf16 a[48:51], a[224:227], v[72:75], a[48:51]\n" \
"v_exp_f32 v3, v3\n" \
"v_mfma_f32_16x16x32_bf16 a[48:51], a[228:231], v[104:107], a[48:51]\n" \
"ds_read_b128 a[220:223], v246 offset:22784\n" \
"global_load_lds_dwordx4 v250, s[36:37] offset:3072 sc1\n" \
"v_mfma_f32_16x16x32_bf16 v[144:147], a[224:227], v[80:83], v[144:147]\n" \
"s_add_u32 s36, s36, 17408\n" \
"v_exp_f32 v4, v4\n" \
"v_mfma_f32_16x16x32_bf16 v[144:147], a[228:231], v[112:115], v[144:147]\n" \
"s_xor_b32 s73, s73, 0x8000\n" \
"v_cvt_pk_bf16_f32 v1, v2, v3\n" \
"v_exp_f32 v5, v5\n" \
"v_mfma_f32_16x16x32_bf16 v[176:179], a[224:227], v[88:91], v[176:179]\n" \
"s_xor_b64 s[52:53], s[44:45], s[42:43]\n" \
"v_exp_f32 v6, v6\n" \
"v_mfma_f32_16x16x32_bf16 v[176:179], a[228:231], v[120:123], v[176:179]\n" \
"s_xor_b64 s[54:55], s[46:47], s[42:43]\n" \
"ds_read_b128 a[224:227], v246 offset:24576\n" \
"v_cvt_pk_bf16_f32 v2, v4, v5\n" \
"v_mfma_f32_16x16x32_bf16 v[180:183], a[232:235], v[88:91], v[180:183]\n" \
"s_or_b64 s[52:53], s[52:53], s[54:55]\n" \
"v_exp_f32 v7, v7\n" \
"v_mfma_f32_16x16x32_bf16 v[180:183], a[236:239], v[120:123], v[180:183]\n" \
"s_xor_b64 s[54:55], s[48:49], s[42:43]\n" \
"v_exp_f32 v16, v16\n" \
"v_cvt_pk_bf16_f32 v3, v6, v7\n" \
"v_mfma_f32_16x16x32_bf16 v[148:151], a[232:235], v[80:83], v[148:151]\n" \
"s_or_b64 s[52:53], s[52:53], s[54:55]\n" \
"v_exp_f32 v17, v17\n" \
"v_mfma_f32_16x16x32_bf16 v[148:151], a[236:239], v[112:115], v[148:151]\n" \
"ds_read_b128 a[228:231], v246 offset:26624\n" \
"s_xor_b64 s[54:55], s[50:51], s[42:43]\n" \
"v_mfma_f32_16x16x32_bf16 a[52:55], a[232:235], v[72:75], a[52:55]\n" \
"s_or_b64 s[52:53], s[52:53], s[54:55]\n" \
"v_exp_f32 v18, v18\n" \
"v_mfma_f32_16x16x32_bf16 a[52:55], a[236:239], v[104:107], a[52:55]\n" \
"v_cvt_pk_bf16_f32 v16, v16, v17\n" \
"v_mfma_f32_16x16x32_bf16 v[240:243], a[124:127], v[8:11], v[240:243]\n" \
"v_exp_f32 v19, v19\n" \
"v_mfma_f32_16x16x32_bf16 v[240:243], a[120:123], v[0:3], v[240:243]\n" \
"s_and_b64 s[52:53], s[52:53], s[8:9]\n" \
"s_cbranch_scc1 Lrs1_%=\n" \
"Lrr1_%=:\n" \
"v_mfma_f32_16x16x32_bf16 a[20:23], a[232:235], v[64:67], a[20:23]\n" \
"s_mov_b64 s[8:9], s[10:11]\n" \
"v_exp_f32 v20, v20\n" \
"v_mfma_f32_16x16x32_bf16 a[20:23], a[236:239], v[96:99], a[20:23]\n" \
"ds_read_b128 a[232:235], v246 offset:28672\n" \
"v_cvt_pk_bf16_f32 v17, v18, v19\n" \
"v_mfma_f32_16x16x32_bf16 a[24:27], a[240:243], v[64:67], a[24:27]\n" \
"v_exp_f32 v21, v21\n" \
"v_mfma_f32_16x16x32_bf16 a[24:27], a[244:247], v[96:99], a[24:27]\n" \
"v_exp_f32 v22, v22\n" \
"v_cvt_pk_bf16_f32 v18, v20, v21\n" \
"v_mfma_f32_16x16x32_bf16 a[56:59], a[240:243], v[72:75], a[56:59]\n" \
"v_exp_f32 v23, v23\n" \
"v_mfma_f32_16x16x32_bf16 a[56:59], a[244:247], v[104:107], a[56:59]\n" \
"ds_read_b128 a[236:239], v246 offset:30720\n" \
"v_exp_f32 v24, v24\n" \
"v_mfma_f32_16x16x32_bf16 v[152:155], a[240:243], v[80:83], v[152:155]\n" \
"v_cvt_pk_bf16_f32 v19, v22, v23\n" \
"v_mfma_f32_16x16x32_bf16 v[152:155], a[244:247], v[112:115], v[152:155]\n" \
"v_exp_f32 v25, v25\n" \
"v_mfma_f32_16x16x32_bf16 v[184:187], a[240:243], v[88:91], v[184:187]\n" \
"v_exp_f32 v26, v26\n" \
"v_mfma_f32_16x16x32_bf16 v[184:187], a[244:247], v[120:123], v[184:187]\n" \
"ds_read_b128 a[240:243], v246 offset:24832\n" \
"v_cvt_pk_bf16_f32 v24, v24, v25\n" \
"v_mfma_f32_16x16x32_bf16 v[242:245], a[120:123], v[16:19], v[242:245]\n" \
"v_exp_f32 v27, v27\n" \
"v_mfma_f32_16x16x32_bf16 v[188:191], a[248:251], v[88:91], v[188:191]\n" \
"v_exp_f32 v28, v28\n" \
"v_mfma_f32_16x16x32_bf16 v[188:191], a[252:255], v[120:123], v[188:191]\n" \
"v_cvt_pk_bf16_f32 v25, v26, v27\n" \
"v_exp_f32 v29, v29\n" \
"v_mfma_f32_16x16x32_bf16 v[156:159], a[248:251], v[80:83], v[156:159]\n" \
"v_exp_f32 v30, v30\n" \
"v_mfma_f32_16x16x32_bf16 v[156:159], a[252:255], v[112:115], v[156:159]\n" \
"ds_read_b128 a[244:247], v246 offset:26880\n" \
"v_cvt_pk_bf16_f32 v26, v28, v29\n" \
"v_mfma_f32_16x16x32_bf16 a[60:63], a[248:251], v[72:75], a[60:63]\n" \
"v_exp_f32 v31, v31\n" \
"v_mfma_f32_16x16x32_bf16 a[60:63], a[252:255], v[104:107], a[60:63]\n" \
"v_cvt_pk_bf16_f32 v27, v30, v31\n" \
"v_mfma_f32_16x16x32_bf16 a[28:31], a[248:251], v[64:67], a[28:31]\n" \
"s_waitcnt lgkmcnt(6)\n" \
"v_mfma_f32_16x16x32_bf16 v[242:245], a[124:127], v[24:27], v[242:245]\n" \
"v_mfma_f32_16x16x32_bf16 a[28:31], a[252:255], v[96:99], a[28:31]\n" \
"ds_read_b128 a[248:251], v246 offset:28928\n" \
"LA1l_%=:\n" \
"Lpg6288_%=:\n" \
"s_cbranch_execz Lpg13428_%=\n" \
"s_getpc_b64 s[40:41]\n" \
"Lpd_%=:\n" \
"s_lshl_b32 s80, s62, 2\n" \
"s_add_u32 s80, s80, s61\n" \
"s_and_b32 s80, s80, 15\n" \
"s_lshl_b32 s80, s80, 12\n" \
"s_mov_b32 s81, Lcode_end_%=-Lpd_%=-4\n" \
"v_lshl_add_u32 v252, v248, 2, s80\n" \
"v_min_u32 v252, s81, v252\n" \
"s_waitcnt lgkmcnt(0)\n" \
"v_mfma_f32_16x16x32_bf16 v[72:75], a[192:195], a[144:147], v[228:231]\n" \
"v_exp_f32 v32, v32\n" \
"v_mfma_f32_16x16x32_bf16 v[72:75], a[196:199], a[148:151], v[72:75]\n" \
"v_cvt_pk_bf16_f32 v27, v30, v31\n" \
"v_mfma_f32_16x16x32_bf16 v[72:75], a[200:203], a[152:155], v[72:75]\n" \
"s_mov_b32 m0, s74\n" \
"v_exp_f32 v33, v33\n" \
"v_mfma_f32_16x16x32_bf16 v[72:75], a[204:207], a[156:159], v[72:75]\n" \
"ds_read_b128 a[252:255], v246 offset:30976\n" \
"global_load_lds_dwordx4 v250, s[38:39] offset:0 sc1\n" \
"v_mfma_f32_16x16x32_bf16 v[80:83], a[200:203], a[168:171], v[232:235]\n" \
"s_lshl_b32 s70, s95, 1\n" \
"v_exp_f32 v34, v34\n" \
"v_mfma_f32_16x16x32_bf16 v[80:83], a[204:207], a[172:175], v[80:83]\n" \
"s_xor_b32 s70, s70, 139264\n" \
"v_cvt_pk_bf16_f32 v32, v32, v33\n" \
"v_add_u32 v64, s70, v248\n" \
"v_mfma_f32_16x16x32_bf16 v[80:83], a[192:195], a[160:163], v[80:83]\n" \
"ds_read_b128 v[192:195], v64 offset:0\n" \
"v_exp_f32 v35, v35\n" \
"v_mfma_f32_16x16x32_bf16 v[80:83], a[196:199], a[164:167], v[80:83]\n" \
"ds_read_b128 v[196:199], v64 offset:1024\n" \
"v_exp_f32 v36, v36\n" \
"v_mfma_f32_16x16x32_bf16 v[88:91], a[192:195], a[176:179], v[236:239]\n" \
"ds_read_b128 v[200:203], v64 offset:2048\n" \
"v_cvt_pk_bf16_f32 v33, v34, v35\n" \
"v_mfma_f32_16x16x32_bf16 v[88:91], a[196:199], a[180:183], v[88:91]\n" \
"ds_read_b128 v[204:207], v64 offset:3072\n" \
"v_exp_f32 v37, v37\n" \
"v_exp_f32 v38, v38\n" \
"v_mfma_f32_16x16x32_bf16 v[88:91], a[200:203], a[184:187], v[88:91]\n" \
"ds_read_b128 v[208:211], v64 offset:4096\n" \
"v_cvt_pk_bf16_f32 v34, v36, v37\n" \
"v_mfma_f32_16x16x32_bf16 v[88:91], a[204:207], a[188:191], v[88:91]\n" \
"ds_read_b128 a[192:195], v247 offset:0\n" \
"v_exp_f32 v39, v39\n" \
"v_mfma_f32_16x16x32_bf16 v[92:95], a[216:219], a[184:187], v[236:239]\n" \
"ds_read_b128 v[212:215], v64 offset:5120\n" \
"global_load_lds_dwordx4 v250, s[38:39] offset:1024 sc1\n" \
"v_mfma_f32_16x16x32_bf16 v[92:95], a[220:223], a[188:191], v[92:95]\n" \
"ds_read_b128 v[216:219], v64 offset:6144\n" \
"v_exp_f32 v40, v40\n" \
"v_mfma_f32_16x16x32_bf16 v[92:95], a[208:211], a[176:179], v[92:95]\n" \
"ds_read_b128 a[196:199], v247 offset:8192\n" \
"v_cvt_pk_bf16_f32 v35, v38, v39\n" \
"v_mfma_f32_16x16x32_bf16 v[92:95], a[212:215], a[180:183], v[92:95]\n" \
"ds_read_b128 v[220:223], v64 offset:7168\n" \
"v_exp_f32 v41, v41\n" \
"v_mfma_f32_16x16x32_bf16 v[84:87], a[208:211], a[160:163], v[232:235]\n" \
"ds_write_b128 v64, v[232:235]\n" \
"v_exp_f32 v42, v42\n" \
"v_mfma_f32_16x16x32_bf16 v[84:87], a[212:215], a[164:167], v[84:87]\n" \
"ds_read_b128 a[200:203], v247 offset:1024\n" \
"v_cvt_pk_bf16_f32 v40, v40, v41\n" \
"v_mfma_f32_16x16x32_bf16 v[84:87], a[216:219], a[168:171], v[84:87]\n" \
"v_exp_f32 v43, v43\n" \
"v_mfma_f32_16x16x32_bf16 v[84:87], a[220:223], a[172:175], v[84:87]\n" \
"global_load_lds_dwordx4 v250, s[38:39] offset:2048 sc1\n" \
"ds_write_b128 v64, v[236:239] offset:1024\n" \
"v_exp_f32 v44, v44\n" \
"v_mfma_f32_16x16x32_bf16 v[76:79], a[216:219], a[152:155], v[228:231]\n" \
"ds_read_b128 a[204:207], v247 offset:9216\n" \
"v_xor_b32 v65, 0x2000, v64\n" \
"v_mfma_f32_16x16x32_bf16 v[76:79], a[220:223], a[156:159], v[76:79]\n" \
"v_xor_b32 v246, 0x8000, v246\n" \
"v_add_u32 v246, s82, v246\n" \
"v_mfma_f32_16x16x32_bf16 v[76:79], a[208:211], a[144:147], v[76:79]\n" \
"s_waitcnt lgkmcnt(4)\n" \
"v_mov_b32 v66, s60\n" \
"v_lshlrev_b32 v67, 28, s61\n" \
"v_mfma_f32_16x16x32_bf16 v[76:79], a[212:215], a[148:151], v[76:79]\n" \
"ds_read_b128 a[208:211], v247 offset:2048\n" \
"v_cvt_pk_bf16_f32 v41, v42, v43\n" \
"v_mfma_f32_16x16x32_bf16 v[104:107], a[224:227], a[144:147], v[228:231]\n" \
"v_exp_f32 v45, v45\n" \
"v_mfma_f32_16x16x32_bf16 v[104:107], a[228:231], a[148:151], v[104:107]\n" \
"v_exp_f32 v46, v46\n" \
"v_mfma_f32_16x16x32_bf16 v[104:107], a[232:235], a[152:155], v[104:107]\n" \
"ds_read_b128 a[212:215], v247 offset:10240\n" \
"v_cvt_pk_bf16_f32 v42, v44, v45\n" \
"v_mfma_f32_16x16x32_bf16 v[104:107], a[236:239], a[156:159], v[104:107]\n" \
"v_exp_f32 v47, v47\n" \
"v_mfma_f32_16x16x32_bf16 v[112:115], a[232:235], a[168:171], v[232:235]\n" \
"v_exp_f32 v48, v48\n" \
"v_cvt_pk_bf16_f32 v43, v46, v47\n" \
"v_mfma_f32_16x16x32_bf16 v[112:115], a[236:239], a[172:175], v[112:115]\n" \
"ds_read_b128 a[216:219], v247 offset:3072\n" \
"global_load_lds_dwordx4 v250, s[38:39] offset:3072 sc1\n" \
"v_mfma_f32_16x16x32_bf16 v[112:115], a[224:227], a[160:163], v[112:115]\n" \
"v_exp_f32 v49, v49\n" \
"v_mfma_f32_16x16x32_bf16 v[112:115], a[228:231], a[164:167], v[112:115]\n" \
"v_exp_f32 v50, v50\n" \
"v_cvt_pk_bf16_f32 v48, v48, v49\n" \
"v_mfma_f32_16x16x32_bf16 v[240:243], a[120:123], v[32:35], v[240:243]\n" \
"v_exp_f32 v51, v51\n" \
"v_mfma_f32_16x16x32_bf16 v[240:243], a[124:127], v[40:43], v[240:243]\n" \
"s_add_u32 s38, s38, 17408\n" \
"v_exp_f32 v52, v52\n" \
"v_mfma_f32_16x16x32_bf16 v[120:123], a[224:227], a[176:179], v[236:239]\n" \
"ds_read_b128 a[220:223], v247 offset:11264\n" \
"v_cvt_pk_bf16_f32 v49, v50, v51\n" \
"v_mfma_f32_16x16x32_bf16 v[120:123], a[228:231], a[180:183], v[120:123]\n" \
"v_exp_f32 v53, v53\n" \
"v_mfma_f32_16x16x32_bf16 v[120:123], a[232:235], a[184:187], v[120:123]\n" \
"v_exp_f32 v54, v54\n" \
"v_mfma_f32_16x16x32_bf16 v[120:123], a[236:239], a[188:191], v[120:123]\n" \
"ds_read_b128 a[224:227], v247 offset:4096\n" \
"v_cvt_pk_bf16_f32 v50, v52, v53\n" \
"v_exp_f32 v55, v55\n" \
"v_mfma_f32_16x16x32_bf16 v[124:127], a[248:251], a[184:187], v[236:239]\n" \
"global_load_dword v253, v252, s[40:41]\n" \
"v_mfma_f32_16x16x32_bf16 v[124:127], a[252:255], a[188:191], v[124:127]\n" \
"v_exp_f32 v56, v56\n" \
"v_mfma_f32_16x16x32_bf16 v[124:127], a[240:243], a[176:179], v[124:127]\n" \
"ds_read_b128 a[228:231], v247 offset:12288\n" \
"v_cvt_pk_bf16_f32 v51, v54, v55\n" \
"v_mfma_f32_16x16x32_bf16 v[124:127], a[244:247], a[180:183], v[124:127]\n" \
"v_exp_f32 v57, v57\n" \
"v_exp_f32 v58, v58\n" \
"v_cvt_pk_bf16_f32 v56, v56, v57\n" \
"v_mfma_f32_16x16x32_bf16 v[242:245], a[120:123], v[48:51], v[242:245]\n" \
"v_exp_f32 v59, v59\n" \
"v_mfma_f32_16x16x32_bf16 v[116:119], a[240:243], a[160:163], v[232:235]\n" \
"v_exp_f32 v60, v60\n" \
"v_mfma_f32_16x16x32_bf16 v[116:119], a[244:247], a[164:167], v[116:119]\n" \
"ds_read_b128 a[232:235], v247 offset:5120\n" \
"v_cvt_pk_bf16_f32 v57, v58, v59\n" \
"v_mfma_f32_16x16x32_bf16 v[116:119], a[248:251], a[168:171], v[116:119]\n" \
"v_exp_f32 v61, v61\n" \
"v_mfma_f32_16x16x32_bf16 v[116:119], a[252:255], a[172:175], v[116:119]\n" \
"v_exp_f32 v62, v62\n" \
"v_cvt_pk_bf16_f32 v58, v60, v61\n" \
"v_mfma_f32_16x16x32_bf16 v[108:111], a[248:251], a[152:155], v[228:231]\n" \
"ds_read_b128 a[236:239], v247 offset:13312\n" \
"v_exp_f32 v63, v63\n" \
"v_mfma_f32_16x16x32_bf16 v[108:111], a[252:255], a[156:159], v[108:111]\n" \
"v_cvt_pk_bf16_f32 v59, v62, v63\n" \
"v_mfma_f32_16x16x32_bf16 v[108:111], a[240:243], a[144:147], v[108:111]\n" \
"s_waitcnt vmcnt(5) lgkmcnt(4)\n" \
"v_mfma_f32_16x16x32_bf16 v[242:245], a[124:127], v[56:59], v[242:245]\n" \
"v_mfma_f32_16x16x32_bf16 v[108:111], a[244:247], a[148:151], v[108:111]\n" \
"s_barrier\n" \
"ds_read_b128 a[240:243], v247 offset:6144\n" \
"v_mfma_f32_16x16x32_bf16 a[0:3], a[192:195], v[0:3], a[0:3]\n" \
"buffer_store_dword v66, v67, s[56:59], s84 offen sc1\n" \
"v_mfma_f32_16x16x32_bf16 a[0:3], a[196:199], v[32:35], a[0:3]\n" \
"s_add_u32 s73, s73, s95\n" \
"v_cvt_pk_bf16_f32 v59, v62, v63\n" \
"v_exp_f32 v80, v80\n" \
"v_mfma_f32_16x16x32_bf16 a[32:35], a[192:195], v[8:11], a[32:35]\n" \
"s_add_u32 s36, s36, s83\n" \
"ds_read_b128 a[244:247], v247 offset:14336\n" \
"v_exp_f32 v81, v81\n" \
"v_mfma_f32_16x16x32_bf16 a[32:35], a[196:199], v[40:43], a[32:35]\n" \
"s_add_u32 s73, s73, 4096\n" \
"ds_read_b128 a[248:251], v247 offset:7168\n" \
"v_exp_f32 v82, v82\n" \
"v_mfma_f32_16x16x32_bf16 v[128:131], a[192:195], v[16:19], v[128:131]\n" \
"ds_read_b128 a[252:255], v247 offset:15360\n" \
"v_cvt_pk_bf16_f32 v80, v80, v81\n" \
"v_mfma_f32_16x16x32_bf16 v[128:131], a[196:199], v[48:51], v[128:131]\n" \
"ds_read_b128 v[224:227], v65\n" \
"v_exp_f32 v83, v83\n" \
"v_mfma_f32_16x16x32_bf16 v[160:163], a[192:195], v[24:27], v[160:163]\n" \
"s_mov_b32 m0, s73\n" \
"ds_read_b128 v[228:231], v65 offset:1024\n" \
"v_xor_b32 v252, 0x8000, v246\n" \
"v_mfma_f32_16x16x32_bf16 v[160:163], a[196:199], v[56:59], v[160:163]\n" \
"ds_read_b128 a[192:195], v246 offset:0\n" \
"global_load_lds_dwordx4 v250, s[36:37] offset:-4096 sc1\n" \
"v_mfma_f32_16x16x32_bf16 v[164:167], a[200:203], v[24:27], v[164:167]\n" \
"v_exp_f32 v84, v84\n" \
"v_mfma_f32_16x16x32_bf16 v[164:167], a[204:207], v[56:59], v[164:167]\n" \
"v_cvt_pk_bf16_f32 v81, v82, v83\n" \
"v_exp_f32 v85, v85\n" \
"v_mfma_f32_16x16x32_bf16 v[132:135], a[200:203], v[16:19], v[132:135]\n" \
"v_exp_f32 v86, v86\n" \
"v_mfma_f32_16x16x32_bf16 v[132:135], a[204:207], v[48:51], v[132:135]\n" \
"ds_read_b128 a[196:199], v246 offset:2048\n" \
"global_load_lds_dwordx4 v250, s[36:37] offset:-3072 sc1\n" \
"v_mfma_f32_16x16x32_bf16 a[36:39], a[200:203], v[8:11], a[36:39]\n" \
"v_cvt_pk_bf16_f32 v82, v84, v85\n" \
"v_mfma_f32_16x16x32_bf16 a[36:39], a[204:207], v[40:43], a[36:39]\n" \
"v_exp_f32 v87, v87\n" \
"v_mfma_f32_16x16x32_bf16 a[4:7], a[200:203], v[0:3], a[4:7]\n" \
"v_cmp_gt_i32 vcc, 0, v251\n" \
"v_cmp_gt_i32 s[64:65], 1, v251\n" \
"v_mfma_f32_16x16x32_bf16 a[4:7], a[204:207], v[32:35], a[4:7]\n" \
"ds_read_b128 a[200:203], v246 offset:4096\n" \
"global_load_lds_dwordx4 v250, s[36:37] offset:-2048 sc1\n" \
"v_mfma_f32_16x16x32_bf16 a[8:11], a[208:211], v[0:3], a[8:11]\n" \
"v_cmp_gt_i32 s[66:67], 2, v251\n" \
"v_cndmask_b32 v72, v72, v254, vcc\n" \
"v_mfma_f32_16x16x32_bf16 a[8:11], a[212:215], v[32:35], a[8:11]\n" \
"v_cmp_gt_i32 vcc, 3, v251\n" \
"v_cndmask_b32 v73, v73, v254, s[64:65]\n" \
"v_mfma_f32_16x16x32_bf16 a[40:43], a[208:211], v[8:11], a[40:43]\n" \
"v_cmp_gt_i32 s[64:65], 16, v251\n" \
"v_cndmask_b32 v74, v74, v254, s[66:67]\n" \
"v_mfma_f32_16x16x32_bf16 a[40:43], a[212:215], v[40:43], a[40:43]\n" \
"ds_read_b128 a[204:207], v246 offset:6144\n" \
"global_load_lds_dwordx4 v250, s[36:37] offset:-1024 sc1\n" \
"v_mfma_f32_16x16x32_bf16 v[136:139], a[208:211], v[16:19], v[136:139]\n" \
"v_cmp_gt_i32 s[66:67], 17, v251\n" \
"v_cndmask_b32 v75, v75, v254, vcc\n" \
"v_mfma_f32_16x16x32_bf16 v[136:139], a[212:215], v[48:51], v[136:139]\n" \
"v_cmp_gt_i32 vcc, 18, v251\n" \
"v_cndmask_b32 v76, v76, v254, s[64:65]\n" \
"v_mfma_f32_16x16x32_bf16 v[168:171], a[208:211], v[24:27], v[168:171]\n" \
"v_cmp_gt_i32 s[64:65], 19, v251\n" \
"v_cndmask_b32 v77, v77, v254, s[66:67]\n" \
"v_mfma_f32_16x16x32_bf16 v[168:171], a[212:215], v[56:59], v[168:171]\n" \
"ds_read_b128 a[208:211], v246 offset:256\n" \
"global_load_lds_dwordx4 v250, s[36:37] offset:0 sc1\n" \
"v_mfma_f32_16x16x32_bf16 v[172:175], a[216:219], v[24:27], v[172:175]\n" \
"v_cmp_gt_i32 s[66:67], 32, v251\n" \
"v_cndmask_b32 v78, v78, v254, vcc\n" \
"v_mfma_f32_16x16x32_bf16 v[172:175], a[220:223], v[56:59], v[172:175]\n" \
"v_cmp_gt_i32 vcc, 33, v251\n" \
"v_cndmask_b32 v79, v79, v254, s[64:65]\n" \
"v_mfma_f32_16x16x32_bf16 v[140:143], a[216:219], v[16:19], v[140:143]\n" \
"v_cmp_gt_i32 s[64:65], 34, v251\n" \
"v_cndmask_b32 v104, v104, v254, s[66:67]\n" \
"v_mfma_f32_16x16x32_bf16 v[140:143], a[220:223], v[48:51], v[140:143]\n" \
"ds_read_b128 a[212:215], v246 offset:2304\n" \
"global_load_lds_dwordx4 v250, s[36:37] offset:1024 sc1\n" \
"v_mfma_f32_16x16x32_bf16 a[44:47], a[216:219], v[8:11], a[44:47]\n" \
"v_cmp_gt_i32 s[66:67], 35, v251\n" \
"v_cndmask_b32 v105, v105, v254, vcc\n" \
"v_mfma_f32_16x16x32_bf16 a[44:47], a[220:223], v[40:43], a[44:47]\n" \
"v_cmp_gt_i32 vcc, 48, v251\n" \
"v_cndmask_b32 v106, v106, v254, s[64:65]\n" \
"v_mfma_f32_16x16x32_bf16 a[12:15], a[216:219], v[0:3], a[12:15]\n" \
"s_waitcnt lgkmcnt(6)\n" \
"v_cmp_gt_i32 s[64:65], 49, v251\n" \
"v_cndmask_b32 v107, v107, v254, s[66:67]\n" \
"v_mfma_f32_16x16x32_bf16 a[12:15], a[220:223], v[32:35], a[12:15]\n" \
"ds_read_b128 a[216:219], v246 offset:4352\n" \
"global_load_lds_dwordx4 v250, s[36:37] offset:2048 sc1\n" \
"v_mfma_f32_16x16x32_bf16 a[16:19], a[224:227], v[0:3], a[16:19]\n" \
"v_cmp_gt_i32 s[66:67], 50, v251\n" \
"v_cndmask_b32 v108, v108, v254, vcc\n" \
"v_mfma_f32_16x16x32_bf16 a[16:19], a[228:231], v[32:35], a[16:19]\n" \
"v_cmp_gt_i32 vcc, 51, v251\n" \
"v_cndmask_b32 v109, v109, v254, s[64:65]\n" \
"v_mfma_f32_16x16x32_bf16 a[48:51], a[224:227], v[8:11], a[48:51]\n" \
"v_cndmask_b32 v110, v110, v254, s[66:67]\n" \
"v_cndmask_b32 v111, v111, v254, vcc\n" \
"v_mfma_f32_16x16x32_bf16 a[48:51], a[228:231], v[40:43], a[48:51]\n" \
"ds_read_b128 a[220:223], v246 offset:6400\n" \
"global_load_lds_dwordx4 v250, s[36:37] offset:3072 sc1\n" \
"v_mfma_f32_16x16x32_bf16 v[144:147], a[224:227], v[16:19], v[144:147]\n" \
"s_add_u32 s36, s36, 34816\n" \
"v_exp_f32 v72, v72\n" \
"v_mfma_f32_16x16x32_bf16 v[144:147], a[228:231], v[48:51], v[144:147]\n" \
"v_cvt_pk_bf16_f32 v83, v86, v87\n" \
"v_mfma_f32_16x16x32_bf16 v[176:179], a[224:227], v[24:27], v[176:179]\n" \
"s_add_u32 m0, s74, 0x4000\n" \
"v_exp_f32 v73, v73\n" \
"v_mfma_f32_16x16x32_bf16 v[176:179], a[228:231], v[56:59], v[176:179]\n" \
"ds_read_b128 a[224:227], v246 offset:8192\n" \
"global_load_lds_dwordx4 v250, s[38:39] offset:0 sc1\n" \
"v_mfma_f32_16x16x32_bf16 v[180:183], a[232:235], v[24:27], v[180:183]\n" \
"v_exp_f32 v74, v74\n" \
"v_mfma_f32_16x16x32_bf16 v[180:183], a[236:239], v[56:59], v[180:183]\n" \
"v_cvt_pk_bf16_f32 v72, v72, v73\n" \
"v_mfma_f32_16x16x32_bf16 v[242:245], a[120:123], v[80:83], v[242:245]\n" \
"v_exp_f32 v75, v75\n" \
"v_mfma_f32_16x16x32_bf16 v[148:151], a[232:235], v[16:19], v[148:151]\n" \
"v_exp_f32 v76, v76\n" \
"v_mfma_f32_16x16x32_bf16 v[148:151], a[236:239], v[48:51], v[148:151]\n" \
"ds_read_b128 a[228:231], v246 offset:10240\n" \
"global_load_lds_dwordx4 v250, s[38:39] offset:1024 sc1\n" \
"v_mfma_f32_16x16x32_bf16 a[52:55], a[232:235], v[8:11], a[52:55]\n" \
"v_cvt_pk_bf16_f32 v73, v74, v75\n" \
"v_mfma_f32_16x16x32_bf16 a[52:55], a[236:239], v[40:43], a[52:55]\n" \
"v_exp_f32 v77, v77\n" \
"v_mfma_f32_16x16x32_bf16 a[20:23], a[232:235], v[0:3], a[20:23]\n" \
"v_exp_f32 v78, v78\n" \
"v_mfma_f32_16x16x32_bf16 a[20:23], a[236:239], v[32:35], a[20:23]\n" \
"ds_read_b128 a[232:235], v246 offset:12288\n" \
"v_cvt_pk_bf16_f32 v74, v76, v77\n" \
"v_mfma_f32_16x16x32_bf16 a[24:27], a[240:243], v[0:3], a[24:27]\n" \
"global_load_lds_dwordx4 v250, s[38:39] offset:2048 sc1\n" \
"global_load_lds_dwordx4 v250, s[38:39] offset:3072 sc1\n" \
"v_mfma_f32_16x16x32_bf16 a[24:27], a[244:247], v[32:35], a[24:27]\n" \
"v_exp_f32 v79, v79\n" \
"v_mfma_f32_16x16x32_bf16 a[56:59], a[240:243], v[8:11], a[56:59]\n" \
"s_add_u32 s38, s38, 17408\n" \
"v_exp_f32 v88, v88\n" \
"v_mfma_f32_16x16x32_bf16 a[56:59], a[244:247], v[40:43], a[56:59]\n" \
"s_add_u32 s38, s38, s83\n" \
"ds_read_b128 a[236:239], v246 offset:14336\n" \
"v_cvt_pk_bf16_f32 v75, v78, v79\n" \
"v_mfma_f32_16x16x32_bf16 v[152:155], a[240:243], v[16:19], v[152:155]\n" \
"s_add_u32 s74, s74, s95\n" \
"v_exp_f32 v89, v89\n" \
"v_mfma_f32_16x16x32_bf16 v[152:155], a[244:247], v[48:51], v[152:155]\n" \
"v_exp_f32 v90, v90\n" \
"v_cvt_pk_bf16_f32 v88, v88, v89\n" \
"v_mfma_f32_16x16x32_bf16 v[240:243], a[124:127], v[72:75], v[240:243]\n" \
"s_add_u32 s74, s74, 4096\n" \
"v_exp_f32 v91, v91\n" \
"v_mfma_f32_16x16x32_bf16 v[184:187], a[240:243], v[24:27], v[184:187]\n" \
"s_cmp_eq_u32 s63, 7\n" \
"v_exp_f32 v92, v92\n" \
"v_mfma_f32_16x16x32_bf16 v[184:187], a[244:247], v[56:59], v[184:187]\n" \
"ds_read_b128 a[240:243], v246 offset:8448\n" \
"v_cvt_pk_bf16_f32 v89, v90, v91\n" \
"v_mfma_f32_16x16x32_bf16 v[188:191], a[248:251], v[24:27], v[188:191]\n" \
"v_exp_f32 v93, v93\n" \
"v_mfma_f32_16x16x32_bf16 v[188:191], a[252:255], v[56:59], v[188:191]\n" \
"v_exp_f32 v94, v94\n" \
"v_mfma_f32_16x16x32_bf16 v[156:159], a[248:251], v[16:19], v[156:159]\n" \
"v_cvt_pk_bf16_f32 v90, v92, v93\n" \
"v_mfma_f32_16x16x32_bf16 v[156:159], a[252:255], v[48:51], v[156:159]\n" \
"ds_read_b128 a[244:247], v246 offset:10496\n" \
"v_mfma_f32_16x16x32_bf16 a[60:63], a[248:251], v[8:11], a[60:63]\n" \
"v_exp_f32 v95, v95\n" \
"v_mfma_f32_16x16x32_bf16 a[60:63], a[252:255], v[40:43], a[60:63]\n" \
"v_cvt_pk_bf16_f32 v91, v94, v95\n" \
"v_mfma_f32_16x16x32_bf16 a[28:31], a[248:251], v[0:3], a[28:31]\n" \
"s_waitcnt lgkmcnt(6)\n" \
"v_mfma_f32_16x16x32_bf16 v[242:245], a[124:127], v[88:91], v[242:245]\n" \
"v_mfma_f32_16x16x32_bf16 a[28:31], a[252:255], v[32:35], a[28:31]\n" \
"ds_read_b128 a[248:251], v246 offset:12544\n" \
"s_cbranch_scc1 LF7_%=\n" \
"v_mfma_f32_16x16x32_bf16 v[0:3], a[192:195], v[192:195], v[224:227]\n" \
"s_load_dwordx8 s[44:51], s[56:57], s79 glc\n" \
"v_exp_f32 v104, v104\n" \
"v_mfma_f32_16x16x32_bf16 v[0:3], a[196:199], v[196:199], v[0:3]\n" \
"v_cvt_pk_bf16_f32 v91, v94, v95\n" \
"v_mfma_f32_16x16x32_bf16 v[0:3], a[200:203], v[200:203], v[0:3]\n" \
"v_exp_f32 v105, v105\n" \
"v_mfma_f32_16x16x32_bf16 v[0:3], a[204:207], v[204:207], v[0:3]\n" \
"ds_read_b128 a[252:255], v246 offset:14592\n" \
"v_mov_b32 v78, v242\n" \
"v_mfma_f32_16x16x32_bf16 v[8:11], a[200:203], v[216:219], v[228:231]\n" \
"v_exp_f32 v106, v106\n" \
"v_mfma_f32_16x16x32_bf16 v[8:11], a[204:207], v[220:223], v[8:11]\n" \
"v_cvt_pk_bf16_f32 v104, v104, v105\n" \
"v_mov_b32 v76, v78\n" \
"v_mfma_f32_16x16x32_bf16 v[8:11], a[192:195], v[208:211], v[8:11]\n" \
"v_exp_f32 v107, v107\n" \
"v_mfma_f32_16x16x32_bf16 v[8:11], a[196:199], v[212:215], v[8:11]\n" \
"v_exp_f32 v108, v108\n" \
"v_mfma_f32_16x16x32_bf16 v[16:19], a[192:195], a[160:163], v[232:235]\n" \
"v_cvt_pk_bf16_f32 v105, v106, v107\n" \
"v_mfma_f32_16x16x32_bf16 v[16:19], a[196:199], a[164:167], v[16:19]\n" \
"v_exp_f32 v109, v109\n" \
"v_mfma_f32_16x16x32_bf16 v[16:19], a[200:203], a[168:171], v[16:19]\n" \
"v_exp_f32 v110, v110\n" \
"v_mfma_f32_16x16x32_bf16 v[16:19], a[204:207], a[172:175], v[16:19]\n" \
"v_cvt_pk_bf16_f32 v106, v108, v109\n" \
"v_permlane16_swap_b32 v76, v78\n" \
"v_mfma_f32_16x16x32_bf16 v[24:27], a[200:203], a[184:187], v[236:239]\n" \
"v_exp_f32 v111, v111\n" \
"v_mfma_f32_16x16x32_bf16 v[24:27], a[204:207], a[188:191], v[24:27]\n" \
"v_exp_f32 v112, v112\n" \
"v_mfma_f32_16x16x32_bf16 v[24:27], a[192:195], a[176:179], v[24:27]\n" \
"v_cvt_pk_bf16_f32 v107, v110, v111\n" \
"v_add_f32 v78, v78, v76\n" \
"v_mfma_f32_16x16x32_bf16 v[24:27], a[196:199], a[180:183], v[24:27]\n" \
"ds_read_b128 a[192:195], v247 offset:16384\n" \
"v_mov_b32 v76, v78\n" \
"v_mfma_f32_16x16x32_bf16 v[28:31], a[208:211], a[176:179], v[236:239]\n" \
"v_exp_f32 v113, v113\n" \
"v_mfma_f32_16x16x32_bf16 v[28:31], a[212:215], a[180:183], v[28:31]\n" \
"v_exp_f32 v114, v114\n" \
"v_mfma_f32_16x16x32_bf16 v[28:31], a[216:219], a[184:187], v[28:31]\n" \
"v_cvt_pk_bf16_f32 v112, v112, v113\n" \
"v_mfma_f32_16x16x32_bf16 v[28:31], a[220:223], a[188:191], v[28:31]\n" \
"v_permlane32_swap_b32 v76, v78\n" \
"v_mfma_f32_16x16x32_bf16 v[240:243], a[124:127], v[104:107], v[240:243]\n" \
"ds_read_b128 a[196:199], v247 offset:24576\n" \
"v_exp_f32 v115, v115\n" \
"v_mfma_f32_16x16x32_bf16 v[20:23], a[216:219], a[168:171], v[232:235]\n" \
"v_exp_f32 v116, v116\n" \
"v_mfma_f32_16x16x32_bf16 v[20:23], a[220:223], a[172:175], v[20:23]\n" \
"v_cvt_pk_bf16_f32 v113, v114, v115\n" \
"v_add_f32 v78, v78, v76\n" \
"v_mfma_f32_16x16x32_bf16 v[20:23], a[208:211], a[160:163], v[20:23]\n" \
"v_exp_f32 v117, v117\n" \
"v_mfma_f32_16x16x32_bf16 v[20:23], a[212:215], a[164:167], v[20:23]\n" \
"ds_read_b128 a[200:203], v247 offset:17408\n" \
"v_rcp_f32 v76, v78\n" \
"v_mfma_f32_16x16x32_bf16 v[12:15], a[208:211], v[208:211], v[228:231]\n" \
"v_exp_f32 v118, v118\n" \
"v_mfma_f32_16x16x32_bf16 v[12:15], a[212:215], v[212:215], v[12:15]\n" \
"v_cvt_pk_bf16_f32 v114, v116, v117\n" \
"v_mul_f32 v78, 0x4d800000, v78\n" \
"v_mfma_f32_16x16x32_bf16 v[12:15], a[216:219], v[216:219], v[12:15]\n" \
"v_exp_f32 v119, v119\n" \
"v_mfma_f32_16x16x32_bf16 v[12:15], a[220:223], v[220:223], v[12:15]\n" \
"ds_read_b128 a[204:207], v247 offset:25600\n" \
"v_add_u32 v77, 0x1c90080, v249\n" \
"v_mfma_f32_16x16x32_bf16 v[4:7], a[216:219], v[200:203], v[224:227]\n" \
"v_exp_f32 v120, v120\n" \
"v_mfma_f32_16x16x32_bf16 v[4:7], a[220:223], v[204:207], v[4:7]\n" \
"v_cvt_pk_bf16_f32 v115, v118, v119\n" \
"v_cndmask_b32 v77, v249, v77, s[16:17]\n" \
"v_mfma_f32_16x16x32_bf16 v[4:7], a[208:211], v[192:195], v[4:7]\n" \
"s_waitcnt lgkmcnt(4)\n" \
"v_exp_f32 v121, v121\n" \
"v_mfma_f32_16x16x32_bf16 v[4:7], a[212:215], v[196:199], v[4:7]\n" \
"ds_read_b128 a[208:211], v247 offset:18432\n" \
"v_accvgpr_read_b32 v64, a0\n" \
"v_mfma_f32_16x16x32_bf16 v[32:35], a[224:227], v[192:195], v[224:227]\n" \
"v_exp_f32 v122, v122\n" \
"v_mfma_f32_16x16x32_bf16 v[32:35], a[228:231], v[196:199], v[32:35]\n" \
"v_cvt_pk_bf16_f32 v120, v120, v121\n" \
"v_accvgpr_read_b32 v65, a1\n" \
"v_mfma_f32_16x16x32_bf16 v[32:35], a[232:235], v[200:203], v[32:35]\n" \
"v_accvgpr_read_b32 v66, a2\n" \
"v_accvgpr_read_b32 v67, a3\n" \
"v_mfma_f32_16x16x32_bf16 v[32:35], a[236:239], v[204:207], v[32:35]\n" \
"ds_read_b128 a[212:215], v247 offset:26624\n" \
"v_accvgpr_read_b32 v68, a4\n" \
"v_mfma_f32_16x16x32_bf16 v[242:245], a[120:123], v[112:115], v[242:245]\n" \
"v_exp_f32 v123, v123\n" \
"v_mfma_f32_16x16x32_bf16 v[40:43], a[232:235], v[216:219], v[228:231]\n" \
"v_accvgpr_read_b32 v69, a5\n" \
"v_accvgpr_read_b32 v70, a6\n" \
"v_accvgpr_read_b32 v71, a7\n" \
"v_exp_f32 v124, v124\n" \
"v_pk_mul_f32 v[64:65], v[64:65], v[76:77] op_sel_hi:[1,0]\n" \
"v_pk_mul_f32 v[66:67], v[66:67], v[76:77] op_sel_hi:[1,0]\n" \
"v_pk_mul_f32 v[68:69], v[68:69], v[76:77] op_sel_hi:[1,0]\n" \
"v_pk_mul_f32 v[70:71], v[70:71], v[76:77] op_sel_hi:[1,0]\n" \
"v_mfma_f32_16x16x32_bf16 v[40:43], a[236:239], v[220:223], v[40:43]\n" \
"v_cvt_pk_bf16_f32 v121, v122, v123\n" \
"v_cvt_pk_bf16_f32 v96, v64, v65\n" \
"v_mfma_f32_16x16x32_bf16 v[40:43], a[224:227], v[208:211], v[40:43]\n" \
"v_exp_f32 v125, v125\n" \
"v_mfma_f32_16x16x32_bf16 v[40:43], a[228:231], v[212:215], v[40:43]\n" \
"ds_read_b128 a[216:219], v247 offset:19456\n" \
"v_cvt_pk_bf16_f32 v97, v66, v67\n" \
"v_cvt_pk_bf16_f32 v98, v68, v69\n" \
"v_cvt_pk_bf16_f32 v99, v70, v71\n" \
"v_pk_mul_f32 v[64:65], v[64:65], v[66:67]\n" \
"v_pk_fma_f32 v[64:65], v[68:69], v[70:71], v[64:65]\n" \
"v_mfma_f32_16x16x32_bf16 v[48:51], a[224:227], a[160:163], v[232:235]\n" \
"v_exp_f32 v126, v126\n" \
"v_mfma_f32_16x16x32_bf16 v[48:51], a[228:231], a[164:167], v[48:51]\n" \
"v_cvt_pk_bf16_f32 v122, v124, v125\n" \
"v_fma_f32 v78, v64, v65, v78\n" \
"v_mfma_f32_16x16x32_bf16 v[48:51], a[232:235], a[168:171], v[48:51]\n" \
"v_exp_f32 v127, v127\n" \
"v_mfma_f32_16x16x32_bf16 v[48:51], a[236:239], a[172:175], v[48:51]\n" \
"buffer_store_dwordx4 v[96:99], v77, s[56:59], s77 offen offset:0 sc1\n" \
"ds_read_b128 a[220:223], v247 offset:27648\n" \
"v_cvt_pk_bf16_f32 v123, v126, v127\n" \
"v_accvgpr_read_b32 v64, a8\n" \
"v_accvgpr_read_b32 v65, a9\n" \
"v_mfma_f32_16x16x32_bf16 v[56:59], a[232:235], a[184:187], v[236:239]\n" \
"s_xor_b64 s[52:53], s[44:45], s[42:43]\n" \
"v_accvgpr_read_b32 v66, a10\n" \
"v_accvgpr_read_b32 v67, a11\n" \
"v_mfma_f32_16x16x32_bf16 v[56:59], a[236:239], a[188:191], v[56:59]\n" \
"s_xor_b64 s[54:55], s[46:47], s[42:43]\n" \
"v_accvgpr_read_b32 v68, a12\n" \
"v_accvgpr_read_b32 v69, a13\n" \
"v_accvgpr_read_b32 v70, a14\n" \
"v_accvgpr_read_b32 v71, a15\n" \
"v_pk_mul_f32 v[64:65], v[64:65], v[76:77] op_sel_hi:[1,0]\n" \
"v_pk_mul_f32 v[66:67], v[66:67], v[76:77] op_sel_hi:[1,0]\n" \
"v_pk_mul_f32 v[68:69], v[68:69], v[76:77] op_sel_hi:[1,0]\n" \
"v_pk_mul_f32 v[70:71], v[70:71], v[76:77] op_sel_hi:[1,0]\n" \
"v_cvt_pk_bf16_f32 v100, v64, v65\n" \
"v_cvt_pk_bf16_f32 v101, v66, v67\n" \
"v_cvt_pk_bf16_f32 v102, v68, v69\n" \
"Lpg6770_%=:\n" \
"s_cbranch_execz Lcb0_%=\n" \
"v_cvt_pk_bf16_f32 v103, v70, v71\n" \
"v_pk_mul_f32 v[64:65], v[64:65], v[66:67]\n" \
"v_pk_fma_f32 v[64:65], v[68:69], v[70:71], v[64:65]\n" \
"v_mfma_f32_16x16x32_bf16 v[56:59], a[224:227], a[176:179], v[56:59]\n" \
"v_fma_f32 v78, v64, v65, v78\n" \
"s_or_b64 s[52:53], s[52:53], s[54:55]\n" \
"v_mfma_f32_16x16x32_bf16 v[56:59], a[228:231], a[180:183], v[56:59]\n" \
"ds_read_b128 a[224:227], v247 offset:20480\n" \
"v_mfma_f32_16x16x32_bf16 v[242:245], a[124:127], v[120:123], v[242:245]\n" \
"buffer_store_dwordx4 v[100:103], v77, s[56:59], s77 offen offset:2048 sc1\n" \
"s_xor_b64 s[54:55], s[48:49], s[42:43]\n" \
"s_add_u32 s77, s77, 0x1000\n" \
"v_accvgpr_read_b32 v64, a16\n" \
"v_accvgpr_read_b32 v65, a17\n" \
"v_mfma_f32_16x16x32_bf16 v[60:63], a[240:243], a[176:179], v[236:239]\n" \
"s_or_b64 s[52:53], s[52:53], s[54:55]\n" \
"v_accvgpr_read_b32 v66, a18\n" \
"v_accvgpr_read_b32 v67, a19\n" \
"v_mfma_f32_16x16x32_bf16 v[60:63], a[244:247], a[180:183], v[60:63]\n" \
"s_xor_b64 s[54:55], s[50:51], s[42:43]\n" \
"v_accvgpr_read_b32 v68, a20\n" \
"v_accvgpr_read_b32 v69, a21\n" \
"v_accvgpr_read_b32 v70, a22\n" \
"v_accvgpr_read_b32 v71, a23\n" \
"v_pk_mul_f32 v[64:65], v[64:65], v[76:77] op_sel_hi:[1,0]\n" \
"v_pk_mul_f32 v[66:67], v[66:67], v[76:77] op_sel_hi:[1,0]\n" \
"v_pk_mul_f32 v[68:69], v[68:69], v[76:77] op_sel_hi:[1,0]\n" \
"v_pk_mul_f32 v[70:71], v[70:71], v[76:77] op_sel_hi:[1,0]\n" \
"v_mfma_f32_16x16x32_bf16 v[60:63], a[248:251], a[184:187], v[60:63]\n" \
"v_cvt_pk_bf16_f32 v96, v64, v65\n" \
"v_cvt_pk_bf16_f32 v97, v66, v67\n" \
"v_cvt_pk_bf16_f32 v98, v68, v69\n" \
"v_cvt_pk_bf16_f32 v99, v70, v71\n" \
"s_or_b64 s[52:53], s[52:53], s[54:55]\n" \
"v_pk_mul_f32 v[64:65], v[64:65], v[66:67]\n" \
"v_pk_fma_f32 v[64:65], v[68:69], v[70:71], v[64:65]\n" \
"v_mfma_f32_16x16x32_bf16 v[60:63], a[252:255], a[188:191], v[60:63]\n" \
"ds_read_b128 a[228:231], v247 offset:28672\n" \
"v_fma_f32 v78, v64, v65, v78\n" \
"v_mfma_f32_16x16x32_bf16 v[52:55], a[248:251], a[168:171], v[232:235]\n" \
"buffer_store_dwordx4 v[96:99], v77, s[56:59], s77 offen offset:0 sc1\n" \
"s_and_b64 s[52:53], s[52:53], s[8:9]\n" \
"s_cbranch_scc1 Lrs2_%=\n" \
"Lrr2_%=:\n" \
"v_mfma_f32_16x16x32_bf16 v[52:55], a[252:255], a[172:175], v[52:55]\n" \
"s_mov_b64 s[8:9], s[10:11]\n" \
"v_accvgpr_read_b32 v64, a24\n" \
"v_accvgpr_read_b32 v65, a25\n" \
"v_mfma_f32_16x16x32_bf16 v[52:55], a[240:243], a[160:163], v[52:55]\n" \
"v_accvgpr_read_b32 v66, a26\n" \
"v_accvgpr_read_b32 v67, a27\n" \
"v_mfma_f32_16x16x32_bf16 v[52:55], a[244:247], a[164:167], v[52:55]\n" \
"ds_read_b128 a[232:235], v247 offset:21504\n" \
"v_accvgpr_read_b32 v68, a28\n" \
"v_accvgpr_read_b32 v69, a29\n" \
"v_accvgpr_read_b32 v70, a30\n" \
"v_accvgpr_read_b32 v71, a31\n" \
"v_pk_mul_f32 v[64:65], v[64:65], v[76:77] op_sel_hi:[1,0]\n" \
"v_pk_mul_f32 v[66:67], v[66:67], v[76:77] op_sel_hi:[1,0]\n" \
"v_pk_mul_f32 v[68:69], v[68:69], v[76:77] op_sel_hi:[1,0]\n" \
"v_pk_mul_f32 v[70:71], v[70:71], v[76:77] op_sel_hi:[1,0]\n" \
"v_mfma_f32_16x16x32_bf16 v[44:47], a[240:243], v[208:211], v[228:231]\n" \
"v_cvt_pk_bf16_f32 v100, v64, v65\n" \
"v_cvt_pk_bf16_f32 v101, v66, v67\n" \
"v_cvt_pk_bf16_f32 v102, v68, v69\n" \
"v_cvt_pk_bf16_f32 v103, v70, v71\n" \
"v_pk_mul_f32 v[64:65], v[64:65], v[66:67]\n" \
"v_pk_fma_f32 v[64:65], v[68:69], v[70:71], v[64:65]\n" \
"v_mfma_f32_16x16x32_bf16 v[44:47], a[244:247], v[212:215], v[44:47]\n" \
"v_fma_f32 v78, v64, v65, v78\n" \
"v_mfma_f32_16x16x32_bf16 v[44:47], a[248:251], v[216:219], v[44:47]\n" \
"v_mfma_f32_16x16x32_bf16 v[44:47], a[252:255], v[220:223], v[44:47]\n" \
"buffer_store_dwordx4 v[100:103], v77, s[56:59], s77 offen offset:2048 sc1\n" \
"ds_read_b128 a[236:239], v247 offset:29696\n" \
"v_cmp_class_f32 vcc, v78, s94\n" \
"s_sub_u32 s77, s77, 4096\n" \
"v_mfma_f32_16x16x32_bf16 v[36:39], a[248:251], v[200:203], v[224:227]\n" \
"s_or_b64 s[92:93], s[92:93], vcc\n" \
"v_mfma_f32_16x16x32_bf16 v[36:39], a[252:255], v[204:207], v[36:39]\n" \
"v_mfma_f32_16x16x32_bf16 v[36:39], a[240:243], v[192:195], v[36:39]\n" \
"s_waitcnt vmcnt(8) lgkmcnt(4)\n" \
"v_mfma_f32_16x16x32_bf16 v[36:39], a[244:247], v[196:199], v[36:39]\n" \
"s_barrier\n" \
"ds_read_b128 a[240:243], v247 offset:22528\n" \
"v_mfma_f32_16x16x32_bf16 a[32:35], a[192:195], v[72:75], a[32:35]\n" \
"v_exp_f32 v0, v0\n" \
"v_mfma_f32_16x16x32_bf16 a[32:35], a[196:199], v[104:107], a[32:35]\n" \
"v_cvt_pk_bf16_f32 v123, v126, v127\n" \
"v_exp_f32 v1, v1\n" \
"v_exp_f32 v2, v2\n" \
"v_cvt_pk_bf16_f32 v0, v0, v1\n" \
"v_exp_f32 v3, v3\n" \
"v_exp_f32 v4, v4\n" \
"v_mfma_f32_16x16x32_bf16 v[128:131], a[192:195], v[80:83], v[128:131]\n" \
"s_xor_b32 m0, s73, 0x8000\n" \
"ds_read_b128 a[244:247], v247 offset:30720\n" \
"v_cvt_pk_bf16_f32 v1, v2, v3\n" \
"v_mfma_f32_16x16x32_bf16 v[128:131], a[196:199], v[112:115], v[128:131]\n" \
"ds_read_b128 a[248:251], v247 offset:23552\n" \
"global_load_lds_dwordx4 v250, s[36:37] offset:-4096 sc1\n" \
"v_mfma_f32_16x16x32_bf16 v[160:163], a[192:195], v[88:91], v[160:163]\n" \
"ds_read_b128 a[252:255], v247 offset:31744\n" \
"global_load_lds_dwordx4 v250, s[36:37] offset:-3072 sc1\n" \
"v_mfma_f32_16x16x32_bf16 v[160:163], a[196:199], v[120:123], v[160:163]\n" \
"s_add_u32 s70, s82, 0x10000\n" \
"ds_read_b128 a[192:195], v252 offset:0\n" \
"v_add_u32 v247, s70, v248\n" \
"v_mfma_f32_16x16x32_bf16 v[164:167], a[200:203], v[88:91], v[164:167]\n" \
"v_accvgpr_mov_b32 a0, a120\n" \
"v_accvgpr_mov_b32 a1, a121\n" \
"v_mfma_f32_16x16x32_bf16 v[164:167], a[204:207], v[120:123], v[164:167]\n" \
"v_accvgpr_mov_b32 a2, a122\n" \
"v_accvgpr_mov_b32 a3, a123\n" \
"v_mfma_f32_16x16x32_bf16 v[132:135], a[200:203], v[80:83], v[132:135]\n" \
"ds_read_b128 a[196:199], v252 offset:2048\n" \
"v_accvgpr_mov_b32 a4, a124\n" \
"v_mfma_f32_16x16x32_bf16 v[132:135], a[204:207], v[112:115], v[132:135]\n" \
"v_accvgpr_mov_b32 a5, a125\n" \
"v_accvgpr_mov_b32 a6, a126\n" \
"v_mfma_f32_16x16x32_bf16 a[36:39], a[200:203], v[72:75], a[36:39]\n" \
"v_accvgpr_mov_b32 a7, a127\n" \
"v_mov_b32 v64, 0x30000\n" \
"v_mfma_f32_16x16x32_bf16 a[36:39], a[204:207], v[104:107], a[36:39]\n" \
"ds_read_b128 a[200:203], v252 offset:4096\n" \
"global_load_lds_dwordx4 v250, s[36:37] offset:-2048 sc1\n" \
"v_mfma_f32_16x16x32_bf16 a[40:43], a[208:211], v[72:75], a[40:43]\n" \
"v_exp_f32 v5, v5\n" \
"v_mfma_f32_16x16x32_bf16 a[40:43], a[212:215], v[104:107], a[40:43]\n" \
"v_exp_f32 v6, v6\n" \
"v_mfma_f32_16x16x32_bf16 v[136:139], a[208:211], v[80:83], v[136:139]\n" \
"ds_read_b128 a[204:207], v252 offset:6144\n" \
"v_cvt_pk_bf16_f32 v2, v4, v5\n" \
"v_mfma_f32_16x16x32_bf16 v[136:139], a[212:215], v[112:115], v[136:139]\n" \
"v_exp_f32 v7, v7\n" \
"v_mfma_f32_16x16x32_bf16 v[168:171], a[208:211], v[88:91], v[168:171]\n" \
"v_exp_f32 v8, v8\n" \
"v_mfma_f32_16x16x32_bf16 v[168:171], a[212:215], v[120:123], v[168:171]\n" \
"ds_read_b128 a[208:211], v252 offset:256\n" \
"global_load_lds_dwordx4 v250, s[36:37] offset:-1024 sc1\n" \
"v_cvt_pk_bf16_f32 v3, v6, v7\n" \
"v_mfma_f32_16x16x32_bf16 v[172:175], a[216:219], v[88:91], v[172:175]\n" \
"v_exp_f32 v9, v9\n" \
"v_mfma_f32_16x16x32_bf16 v[172:175], a[220:223], v[120:123], v[172:175]\n" \
"v_exp_f32 v10, v10\n" \
"v_cvt_pk_bf16_f32 v8, v8, v9\n" \
"v_mfma_f32_16x16x32_bf16 v[140:143], a[216:219], v[80:83], v[140:143]\n" \
"ds_read_b128 a[212:215], v252 offset:2304\n" \
"global_load_lds_dwordx4 v250, s[36:37] offset:0 sc1\n" \
"v_mfma_f32_16x16x32_bf16 v[140:143], a[220:223], v[112:115], v[140:143]\n" \
"v_exp_f32 v11, v11\n" \
"v_mfma_f32_16x16x32_bf16 a[44:47], a[216:219], v[72:75], a[44:47]\n" \
"s_waitcnt lgkmcnt(6)\n" \
"v_exp_f32 v12, v12\n" \
"v_mfma_f32_16x16x32_bf16 a[44:47], a[220:223], v[104:107], a[44:47]\n" \
"ds_read_b128 a[216:219], v252 offset:4352\n" \
"v_cvt_pk_bf16_f32 v9, v10, v11\n" \
"v_mfma_f32_16x16x32_bf16 a[48:51], a[224:227], v[72:75], a[48:51]\n" \
"v_exp_f32 v13, v13\n" \
"v_mfma_f32_16x16x32_bf16 a[48:51], a[228:231], v[104:107], a[48:51]\n" \
"v_exp_f32 v14, v14\n" \
"v_cvt_pk_bf16_f32 v10, v12, v13\n" \
"v_mfma_f32_16x16x32_bf16 v[144:147], a[224:227], v[80:83], v[144:147]\n" \
"ds_read_b128 a[220:223], v252 offset:6400\n" \
"global_load_lds_dwordx4 v250, s[36:37] offset:1024 sc1\n" \
"v_mfma_f32_16x16x32_bf16 v[144:147], a[228:231], v[112:115], v[144:147]\n" \
"v_exp_f32 v15, v15\n" \
"v_mfma_f32_16x16x32_bf16 v[176:179], a[224:227], v[88:91], v[176:179]\n" \
"v_exp_f32 v16, v16\n" \
"v_mfma_f32_16x16x32_bf16 v[176:179], a[228:231], v[120:123], v[176:179]\n" \
"ds_read_b128 a[224:227], v252 offset:8192\n" \
"global_load_lds_dwordx4 v250, s[36:37] offset:2048 sc1\n" \
"v_cvt_pk_bf16_f32 v11, v14, v15\n" \
"v_mfma_f32_16x16x32_bf16 v[180:183], a[232:235], v[88:91], v[180:183]\n" \
"v_exp_f32 v17, v17\n" \
"v_mfma_f32_16x16x32_bf16 v[180:183], a[236:239], v[120:123], v[180:183]\n" \
"v_exp_f32 v18, v18\n" \
"v_cvt_pk_bf16_f32 v16, v16, v17\n" \
"v_mfma_f32_16x16x32_bf16 v[238:241], a[120:123], v[0:3], v[238:241]\n" \
"v_exp_f32 v19, v19\n" \
"v_mfma_f32_16x16x32_bf16 v[238:241], a[124:127], v[8:11], v[238:241]\n" \
"v_exp_f32 v20, v20\n" \
"v_cvt_pk_bf16_f32 v17, v18, v19\n" \
"v_mfma_f32_16x16x32_bf16 v[148:151], a[232:235], v[80:83], v[148:151]\n" \
"ds_read_b128 a[228:231], v252 offset:10240\n" \
"global_load_lds_dwordx4 v250, s[36:37] offset:3072 sc1\n" \
"v_mfma_f32_16x16x32_bf16 v[148:151], a[236:239], v[112:115], v[148:151]\n" \
"v_exp_f32 v21, v21\n" \
"v_mfma_f32_16x16x32_bf16 a[52:55], a[232:235], v[72:75], a[52:55]\n" \
"v_exp_f32 v22, v22\n" \
"v_mfma_f32_16x16x32_bf16 a[52:55], a[236:239], v[104:107], a[52:55]\n" \
"s_add_u32 s36, s36, 34816\n" \
"ds_read_b128 a[232:235], v252 offset:12288\n" \
"v_cvt_pk_bf16_f32 v18, v20, v21\n" \
"v_mfma_f32_16x16x32_bf16 a[56:59], a[240:243], v[72:75], a[56:59]\n" \
"v_exp_f32 v23, v23\n" \
"v_mfma_f32_16x16x32_bf16 a[56:59], a[244:247], v[104:107], a[56:59]\n" \
"v_exp_f32 v24, v24\n" \
"v_mfma_f32_16x16x32_bf16 v[152:155], a[240:243], v[80:83], v[152:155]\n" \
"ds_read_b128 a[236:239], v252 offset:14336\n" \
"v_cvt_pk_bf16_f32 v19, v22, v23\n" \
"v_mfma_f32_16x16x32_bf16 v[152:155], a[244:247], v[112:115], v[152:155]\n" \
"v_exp_f32 v25, v25\n" \
"v_exp_f32 v26, v26\n" \
"v_cvt_pk_bf16_f32 v24, v24, v25\n" \
"v_mfma_f32_16x16x32_bf16 v[242:245], a[120:123], v[16:19], v[242:245]\n" \
"v_exp_f32 v27, v27\n" \
"v_mfma_f32_16x16x32_bf16 v[184:187], a[240:243], v[88:91], v[184:187]\n" \
"s_load_dwordx8 s[44:51], s[56:57], s79 glc\n" \
"v_exp_f32 v28, v28\n" \
"v_mfma_f32_16x16x32_bf16 v[184:187], a[244:247], v[120:123], v[184:187]\n" \
"ds_read_b128 a[240:243], v252 offset:8448\n" \
"v_cvt_pk_bf16_f32 v25, v26, v27\n" \
"v_exp_f32 v29, v29\n" \
"v_mfma_f32_16x16x32_bf16 v[188:191], a[248:251], v[88:91], v[188:191]\n" \
"v_exp_f32 v30, v30\n" \
"v_mfma_f32_16x16x32_bf16 v[188:191], a[252:255], v[120:123], v[188:191]\n" \
"v_cvt_pk_bf16_f32 v26, v28, v29\n" \
"v_exp_f32 v31, v31\n" \
"v_mfma_f32_16x16x32_bf16 v[156:159], a[248:251], v[80:83], v[156:159]\n" \
"ds_read_b128 a[244:247], v252 offset:10496\n" \
"v_cvt_pk_bf16_f32 v27, v30, v31\n" \
"v_mfma_f32_16x16x32_bf16 v[156:159], a[252:255], v[112:115], v[156:159]\n" \
"v_mfma_f32_16x16x32_bf16 v[242:245], a[124:127], v[24:27], v[242:245]\n" \
"v_mfma_f32_16x16x32_bf16 a[60:63], a[248:251], v[72:75], a[60:63]\n" \
"s_waitcnt vmcnt(12) lgkmcnt(6)\n" \
"v_mfma_f32_16x16x32_bf16 a[60:63], a[252:255], v[104:107], a[60:63]\n" \
"s_barrier\n" \
"ds_read_b128 a[248:251], v252 offset:12544\n" \
"ds_read_b128 a[120:123], v64\n" \
"ds_read_b128 a[124:127], v64\n" \
"s_mov_b64 exec, 0\n" \
"s_cmp_eq_u32 s61, 0\n" \
"s_cbranch_scc1 Lst1x0_%=\n" \
"s_cmp_eq_u32 s61, 1\n" \
"s_cbranch_scc1 Lst1x1_%=\n" \
"s_cmp_eq_u32 s61, 2\n" \
"s_cbranch_scc1 Lst1x2_%=\n" \
"s_branch Lst1x3_%=\n" \
"Lcb1_%=:\n" \
"s_mov_b64 exec, -1\n" \
"s_waitcnt lgkmcnt(0)\n" \
"s_cmp_lg_u32 s63, 0\n" \
"s_cbranch_scc1 LA1g_%=\n" \
"s_xor_b64 s[52:53], s[44:45], s[42:43]\n" \
"s_xor_b64 s[54:55], s[46:47], s[42:43]\n" \
"s_or_b64 s[52:53], s[52:53], s[54:55]\n" \
"s_xor_b64 s[54:55], s[48:49], s[42:43]\n" \
"s_or_b64 s[52:53], s[52:53], s[54:55]\n" \
"s_xor_b64 s[54:55], s[50:51], s[42:43]\n" \
"s_or_b64 s[52:53], s[52:53], s[54:55]\n" \
"s_and_b64 s[52:53], s[52:53], s[8:9]\n" \
"s_cbranch_scc1 Lrs3_%=\n" \
"Lrr3_%=:\n" \
"s_branch LA1g_%=\n" \
"Lpg7025_%=:\n" \
"s_cbranch_execz Lcb0_%=\n" \
"Lloop2_%=:\n" \
"v_mfma_f32_16x16x32_bf16 a[64:67], a[192:195], v[0:3], a[64:67]\n" \
"v_exp_f32 v64, v64\n" \
"v_mfma_f32_16x16x32_bf16 a[64:67], a[196:199], v[32:35], a[64:67]\n" \
"v_cvt_pk_bf16_f32 v59, v62, v63\n" \
"v_add_f32 v240, v240, v64\n" \
"v_exp_f32 v65, v65\n" \
"v_mfma_f32_16x16x32_bf16 a[96:99], a[192:195], v[8:11], a[96:99]\n" \
"s_mov_b32 m0, s73\n" \
"ds_read_b128 a[244:247], v247 offset:14336\n" \
"v_add_f32 v240, v240, v65\n" \
"v_mfma_f32_16x16x32_bf16 a[96:99], a[196:199], v[40:43], a[96:99]\n" \
"ds_read_b128 a[248:251], v247 offset:7168\n" \
"global_load_lds_dwordx4 v250, s[36:37] offset:-4096 sc1\n" \
"v_mfma_f32_16x16x32_bf16 v[128:131], a[192:195], v[16:19], v[128:131]\n" \
"ds_read_b128 a[252:255], v247 offset:15360\n" \
"v_exp_f32 v66, v66\n" \
"v_mfma_f32_16x16x32_bf16 v[128:131], a[196:199], v[48:51], v[128:131]\n" \
"v_add_f32 v240, v240, v66\n" \
"v_cvt_pk_bf16_f32 v64, v64, v65\n" \
"v_mfma_f32_16x16x32_bf16 v[160:163], a[192:195], v[24:27], v[160:163]\n" \
"v_exp_f32 v67, v67\n" \
"v_mfma_f32_16x16x32_bf16 v[160:163], a[196:199], v[56:59], v[160:163]\n" \
"ds_read_b128 a[192:195], v246 offset:0\n" \
"global_load_lds_dwordx4 v250, s[36:37] offset:-3072 sc1\n" \
"v_add_f32 v240, v240, v67\n" \
"v_mfma_f32_16x16x32_bf16 v[164:167], a[200:203], v[24:27], v[164:167]\n" \
"v_exp_f32 v68, v68\n" \
"v_mfma_f32_16x16x32_bf16 v[164:167], a[204:207], v[56:59], v[164:167]\n" \
"v_add_f32 v240, v240, v68\n" \
"v_cvt_pk_bf16_f32 v65, v66, v67\n" \
"v_mfma_f32_16x16x32_bf16 v[132:135], a[200:203], v[16:19], v[132:135]\n" \
"v_exp_f32 v69, v69\n" \
"v_mfma_f32_16x16x32_bf16 v[132:135], a[204:207], v[48:51], v[132:135]\n" \
"ds_read_b128 a[196:199], v246 offset:2048\n" \
"v_add_f32 v240, v240, v69\n" \
"v_mfma_f32_16x16x32_bf16 a[100:103], a[200:203], v[8:11], a[100:103]\n" \
"v_exp_f32 v70, v70\n" \
"v_mfma_f32_16x16x32_bf16 a[100:103], a[204:207], v[40:43], a[100:103]\n" \
"v_add_f32 v240, v240, v70\n" \
"v_cvt_pk_bf16_f32 v66, v68, v69\n" \
"v_mfma_f32_16x16x32_bf16 a[68:71], a[200:203], v[0:3], a[68:71]\n" \
"v_exp_f32 v71, v71\n" \
"v_mfma_f32_16x16x32_bf16 a[68:71], a[204:207], v[32:35], a[68:71]\n" \
"ds_read_b128 a[200:203], v246 offset:4096\n" \
"global_load_lds_dwordx4 v250, s[36:37] offset:-2048 sc1\n" \
"v_add_f32 v240, v240, v71\n" \
"v_mfma_f32_16x16x32_bf16 a[72:75], a[208:211], v[0:3], a[72:75]\n" \
"v_exp_f32 v72, v72\n" \
"v_mfma_f32_16x16x32_bf16 a[72:75], a[212:215], v[32:35], a[72:75]\n" \
"v_add_f32 v241, v241, v72\n" \
"v_cvt_pk_bf16_f32 v67, v70, v71\n" \
"v_mfma_f32_16x16x32_bf16 a[104:107], a[208:211], v[8:11], a[104:107]\n" \
"v_exp_f32 v73, v73\n" \
"v_mfma_f32_16x16x32_bf16 a[104:107], a[212:215], v[40:43], a[104:107]\n" \
"ds_read_b128 a[204:207], v246 offset:6144\n" \
"global_load_lds_dwordx4 v250, s[36:37] offset:-1024 sc1\n" \
"v_add_f32 v241, v241, v73\n" \
"v_mfma_f32_16x16x32_bf16 v[136:139], a[208:211], v[16:19], v[136:139]\n" \
"v_exp_f32 v74, v74\n" \
"v_mfma_f32_16x16x32_bf16 v[136:139], a[212:215], v[48:51], v[136:139]\n" \
"v_add_f32 v241, v241, v74\n" \
"v_cvt_pk_bf16_f32 v72, v72, v73\n" \
"v_mfma_f32_16x16x32_bf16 v[168:171], a[208:211], v[24:27], v[168:171]\n" \
"v_exp_f32 v75, v75\n" \
"v_mfma_f32_16x16x32_bf16 v[168:171], a[212:215], v[56:59], v[168:171]\n" \
"ds_read_b128 a[208:211], v246 offset:256\n" \
"v_add_f32 v241, v241, v75\n" \
"v_mfma_f32_16x16x32_bf16 v[172:175], a[216:219], v[24:27], v[172:175]\n" \
"v_exp_f32 v76, v76\n" \
"v_mfma_f32_16x16x32_bf16 v[172:175], a[220:223], v[56:59], v[172:175]\n" \
"v_add_f32 v241, v241, v76\n" \
"v_cvt_pk_bf16_f32 v73, v74, v75\n" \
"v_mfma_f32_16x16x32_bf16 v[140:143], a[216:219], v[16:19], v[140:143]\n" \
"v_exp_f32 v77, v77\n" \
"v_mfma_f32_16x16x32_bf16 v[140:143], a[220:223], v[48:51], v[140:143]\n" \
"ds_read_b128 a[212:215], v246 offset:2304\n" \
"global_load_lds_dwordx4 v250, s[36:37] offset:0 sc1\n" \
"v_add_f32 v241, v241, v77\n" \
"v_mfma_f32_16x16x32_bf16 a[108:111], a[216:219], v[8:11], a[108:111]\n" \
"v_exp_f32 v78, v78\n" \
"v_mfma_f32_16x16x32_bf16 a[108:111], a[220:223], v[40:43], a[108:111]\n" \
"v_add_f32 v241, v241, v78\n" \
"v_cvt_pk_bf16_f32 v74, v76, v77\n" \
"v_mfma_f32_16x16x32_bf16 a[76:79], a[216:219], v[0:3], a[76:79]\n" \
"s_waitcnt lgkmcnt(6)\n" \
"v_exp_f32 v79, v79\n" \
"v_mfma_f32_16x16x32_bf16 a[76:79], a[220:223], v[32:35], a[76:79]\n" \
"ds_read_b128 a[216:219], v246 offset:4352\n" \
"global_load_lds_dwordx4 v250, s[36:37] offset:1024 sc1\n" \
"v_add_f32 v241, v241, v79\n" \
"v_mfma_f32_16x16x32_bf16 a[80:83], a[224:227], v[0:3], a[80:83]\n" \
"v_exp_f32 v80, v80\n" \
"v_mfma_f32_16x16x32_bf16 a[80:83], a[228:231], v[32:35], a[80:83]\n" \
"v_add_f32 v244, v244, v80\n" \
"v_cvt_pk_bf16_f32 v75, v78, v79\n" \
"v_mfma_f32_16x16x32_bf16 a[112:115], a[224:227], v[8:11], a[112:115]\n" \
"v_exp_f32 v81, v81\n" \
"v_mfma_f32_16x16x32_bf16 a[112:115], a[228:231], v[40:43], a[112:115]\n" \
"ds_read_b128 a[220:223], v246 offset:6400\n" \
"v_add_f32 v244, v244, v81\n" \
"v_mfma_f32_16x16x32_bf16 v[144:147], a[224:227], v[16:19], v[144:147]\n" \
"v_exp_f32 v82, v82\n" \
"v_mfma_f32_16x16x32_bf16 v[144:147], a[228:231], v[48:51], v[144:147]\n" \
"v_add_f32 v244, v244, v82\n" \
"v_cvt_pk_bf16_f32 v80, v80, v81\n" \
"v_mfma_f32_16x16x32_bf16 v[176:179], a[224:227], v[24:27], v[176:179]\n" \
"v_exp_f32 v83, v83\n" \
"v_mfma_f32_16x16x32_bf16 v[176:179], a[228:231], v[56:59], v[176:179]\n" \
"ds_read_b128 a[224:227], v246 offset:8192\n" \
"global_load_lds_dwordx4 v250, s[36:37] offset:2048 sc1\n" \
"v_add_f32 v244, v244, v83\n" \
"v_mfma_f32_16x16x32_bf16 v[180:183], a[232:235], v[24:27], v[180:183]\n" \
"v_exp_f32 v84, v84\n" \
"v_mfma_f32_16x16x32_bf16 v[180:183], a[236:239], v[56:59], v[180:183]\n" \
"v_add_f32 v244, v244, v84\n" \
"v_cvt_pk_bf16_f32 v81, v82, v83\n" \
"v_mfma_f32_16x16x32_bf16 v[148:151], a[232:235], v[16:19], v[148:151]\n" \
"v_exp_f32 v85, v85\n" \
"v_mfma_f32_16x16x32_bf16 v[148:151], a[236:239], v[48:51], v[148:151]\n" \
"ds_read_b128 a[228:231], v246 offset:10240\n" \
"global_load_lds_dwordx4 v250, s[36:37] offset:3072 sc1\n" \
"v_add_f32 v244, v244, v85\n" \
"v_mfma_f32_16x16x32_bf16 a[116:119], a[232:235], v[8:11], a[116:119]\n" \
"s_add_u32 s36, s36, 34816\n" \
"v_exp_f32 v86, v86\n" \
"v_mfma_f32_16x16x32_bf16 a[116:119], a[236:239], v[40:43], a[116:119]\n" \
"v_add_f32 v244, v244, v86\n" \
"v_cvt_pk_bf16_f32 v82, v84, v85\n" \
"v_mfma_f32_16x16x32_bf16 a[84:87], a[232:235], v[0:3], a[84:87]\n" \
"v_exp_f32 v87, v87\n" \
"v_mfma_f32_16x16x32_bf16 a[84:87], a[236:239], v[32:35], a[84:87]\n" \
"ds_read_b128 a[232:235], v246 offset:12288\n" \
"v_add_f32 v244, v244, v87\n" \
"v_mfma_f32_16x16x32_bf16 a[88:91], a[240:243], v[0:3], a[88:91]\n" \
"v_exp_f32 v88, v88\n" \
"v_mfma_f32_16x16x32_bf16 a[88:91], a[244:247], v[32:35], a[88:91]\n" \
"v_add_f32 v245, v245, v88\n" \
"v_cvt_pk_bf16_f32 v83, v86, v87\n" \
"v_mfma_f32_16x16x32_bf16 a[120:123], a[240:243], v[8:11], a[120:123]\n" \
"v_exp_f32 v89, v89\n" \
"v_mfma_f32_16x16x32_bf16 a[120:123], a[244:247], v[40:43], a[120:123]\n" \
"ds_read_b128 a[236:239], v246 offset:14336\n" \
"v_add_f32 v245, v245, v89\n" \
"v_mfma_f32_16x16x32_bf16 v[152:155], a[240:243], v[16:19], v[152:155]\n" \
"v_exp_f32 v90, v90\n" \
"v_mfma_f32_16x16x32_bf16 v[152:155], a[244:247], v[48:51], v[152:155]\n" \
"v_add_f32 v245, v245, v90\n" \
"v_cvt_pk_bf16_f32 v88, v88, v89\n" \
"v_mfma_f32_16x16x32_bf16 v[184:187], a[240:243], v[24:27], v[184:187]\n" \
"v_exp_f32 v91, v91\n" \
"v_mfma_f32_16x16x32_bf16 v[184:187], a[244:247], v[56:59], v[184:187]\n" \
"ds_read_b128 a[240:243], v246 offset:8448\n" \
"v_add_f32 v245, v245, v91\n" \
"v_mfma_f32_16x16x32_bf16 v[188:191], a[248:251], v[24:27], v[188:191]\n" \
"v_exp_f32 v92, v92\n" \
"v_mfma_f32_16x16x32_bf16 v[188:191], a[252:255], v[56:59], v[188:191]\n" \
"v_add_f32 v245, v245, v92\n" \
"v_cvt_pk_bf16_f32 v89, v90, v91\n" \
"v_mfma_f32_16x16x32_bf16 v[156:159], a[248:251], v[16:19], v[156:159]\n" \
"v_exp_f32 v93, v93\n" \
"v_mfma_f32_16x16x32_bf16 v[156:159], a[252:255], v[48:51], v[156:159]\n" \
"ds_read_b128 a[244:247], v246 offset:10496\n" \
"v_add_f32 v245, v245, v93\n" \
"v_mfma_f32_16x16x32_bf16 a[124:127], a[248:251], v[8:11], a[124:127]\n" \
"v_exp_f32 v94, v94\n" \
"v_mfma_f32_16x16x32_bf16 a[124:127], a[252:255], v[40:43], a[124:127]\n" \
"v_add_f32 v245, v245, v94\n" \
"v_cvt_pk_bf16_f32 v90, v92, v93\n" \
"v_mfma_f32_16x16x32_bf16 a[92:95], a[248:251], v[0:3], a[92:95]\n" \
"s_waitcnt vmcnt(8) lgkmcnt(6)\n" \
"v_exp_f32 v95, v95\n" \
"v_mfma_f32_16x16x32_bf16 a[92:95], a[252:255], v[32:35], a[92:95]\n" \
"s_barrier\n" \
"ds_read_b128 a[248:251], v246 offset:12544\n" \
"v_add_f32 v245, v245, v95\n" \
"v_mfma_f32_16x16x32_bf16 v[0:3], a[192:195], v[192:195], v[224:227]\n" \
"v_exp_f32 v96, v96\n" \
"v_mfma_f32_16x16x32_bf16 v[0:3], a[196:199], v[196:199], v[0:3]\n" \
"v_cvt_pk_bf16_f32 v91, v94, v95\n" \
"v_add_f32 v240, v240, v96\n" \
"v_mfma_f32_16x16x32_bf16 v[0:3], a[200:203], v[200:203], v[0:3]\n" \
"s_mov_b32 m0, s74\n" \
"v_exp_f32 v97, v97\n" \
"v_mfma_f32_16x16x32_bf16 v[0:3], a[204:207], v[204:207], v[0:3]\n" \
"ds_read_b128 a[252:255], v246 offset:14592\n" \
"v_add_f32 v240, v240, v97\n" \
"v_mfma_f32_16x16x32_bf16 v[8:11], a[200:203], v[216:219], v[228:231]\n" \
"global_load_lds_dwordx4 v250, s[38:39] offset:-4096 sc1\n" \
"global_load_lds_dwordx4 v250, s[38:39] offset:-3072 sc1\n" \
"v_mfma_f32_16x16x32_bf16 v[8:11], a[204:207], v[220:223], v[8:11]\n" \
"v_exp_f32 v98, v98\n" \
"v_mfma_f32_16x16x32_bf16 v[8:11], a[192:195], v[208:211], v[8:11]\n" \
"v_add_f32 v240, v240, v98\n" \
"v_cvt_pk_bf16_f32 v96, v96, v97\n" \
"v_mfma_f32_16x16x32_bf16 v[8:11], a[196:199], v[212:215], v[8:11]\n" \
"v_exp_f32 v99, v99\n" \
"v_mfma_f32_16x16x32_bf16 v[16:19], a[192:195], a[160:163], v[232:235]\n" \
"v_add_f32 v240, v240, v99\n" \
"v_exp_f32 v100, v100\n" \
"v_mfma_f32_16x16x32_bf16 v[16:19], a[196:199], a[164:167], v[16:19]\n" \
"v_add_f32 v240, v240, v100\n" \
"v_cvt_pk_bf16_f32 v97, v98, v99\n" \
"v_mfma_f32_16x16x32_bf16 v[16:19], a[200:203], a[168:171], v[16:19]\n" \
"v_exp_f32 v101, v101\n" \
"v_mfma_f32_16x16x32_bf16 v[16:19], a[204:207], a[172:175], v[16:19]\n" \
"v_add_f32 v240, v240, v101\n" \
"v_exp_f32 v102, v102\n" \
"v_mfma_f32_16x16x32_bf16 v[24:27], a[200:203], a[184:187], v[236:239]\n" \
"v_add_f32 v240, v240, v102\n" \
"v_cvt_pk_bf16_f32 v98, v100, v101\n" \
"v_mfma_f32_16x16x32_bf16 v[24:27], a[204:207], a[188:191], v[24:27]\n" \
"v_exp_f32 v103, v103\n" \
"v_mfma_f32_16x16x32_bf16 v[24:27], a[192:195], a[176:179], v[24:27]\n" \
"v_add_f32 v240, v240, v103\n" \
"v_mfma_f32_16x16x32_bf16 v[24:27], a[196:199], a[180:183], v[24:27]\n" \
"ds_read_b128 a[192:195], v247 offset:32768\n" \
"global_load_lds_dwordx4 v250, s[38:39] offset:-2048 sc1\n" \
"v_mfma_f32_16x16x32_bf16 v[28:31], a[208:211], a[176:179], v[236:239]\n" \
"v_exp_f32 v104, v104\n" \
"v_mfma_f32_16x16x32_bf16 v[28:31], a[212:215], a[180:183], v[28:31]\n" \
"v_add_f32 v241, v241, v104\n" \
"v_cvt_pk_bf16_f32 v99, v102, v103\n" \
"v_mfma_f32_16x16x32_bf16 v[28:31], a[216:219], a[184:187], v[28:31]\n" \
"v_exp_f32 v105, v105\n" \
"v_mfma_f32_16x16x32_bf16 v[28:31], a[220:223], a[188:191], v[28:31]\n" \
"ds_read_b128 a[196:199], v247 offset:40960\n" \
"global_load_lds_dwordx4 v250, s[38:39] offset:-1024 sc1\n" \
"v_add_f32 v241, v241, v105\n" \
"v_mfma_f32_16x16x32_bf16 v[20:23], a[216:219], a[168:171], v[232:235]\n" \
"v_exp_f32 v106, v106\n" \
"v_mfma_f32_16x16x32_bf16 v[20:23], a[220:223], a[172:175], v[20:23]\n" \
"v_add_f32 v241, v241, v106\n" \
"v_cvt_pk_bf16_f32 v104, v104, v105\n" \
"v_mfma_f32_16x16x32_bf16 v[20:23], a[208:211], a[160:163], v[20:23]\n" \
"v_exp_f32 v107, v107\n" \
"v_mfma_f32_16x16x32_bf16 v[20:23], a[212:215], a[164:167], v[20:23]\n" \
"ds_read_b128 a[200:203], v247 offset:33792\n" \
"v_add_f32 v241, v241, v107\n" \
"v_mfma_f32_16x16x32_bf16 v[12:15], a[208:211], v[208:211], v[228:231]\n" \
"v_exp_f32 v108, v108\n" \
"v_mfma_f32_16x16x32_bf16 v[12:15], a[212:215], v[212:215], v[12:15]\n" \
"v_add_f32 v241, v241, v108\n" \
"v_cvt_pk_bf16_f32 v105, v106, v107\n" \
"v_mfma_f32_16x16x32_bf16 v[12:15], a[216:219], v[216:219], v[12:15]\n" \
"v_exp_f32 v109, v109\n" \
"v_mfma_f32_16x16x32_bf16 v[12:15], a[220:223], v[220:223], v[12:15]\n" \
"ds_read_b128 a[204:207], v247 offset:41984\n" \
"global_load_lds_dwordx4 v250, s[38:39] offset:0 sc1\n" \
"v_add_f32 v241, v241, v109\n" \
"v_mfma_f32_16x16x32_bf16 v[4:7], a[216:219], v[200:203], v[224:227]\n" \
"v_exp_f32 v110, v110\n" \
"v_mfma_f32_16x16x32_bf16 v[4:7], a[220:223], v[204:207], v[4:7]\n" \
"v_add_f32 v241, v241, v110\n" \
"v_cvt_pk_bf16_f32 v106, v108, v109\n" \
"v_mfma_f32_16x16x32_bf16 v[4:7], a[208:211], v[192:195], v[4:7]\n" \
"s_waitcnt lgkmcnt(4)\n" \
"v_exp_f32 v111, v111\n" \
"v_mfma_f32_16x16x32_bf16 v[4:7], a[212:215], v[196:199], v[4:7]\n" \
"ds_read_b128 a[208:211], v247 offset:34816\n" \
"global_load_lds_dwordx4 v250, s[38:39] offset:1024 sc1\n" \
"v_add_f32 v241, v241, v111\n" \
"v_mfma_f32_16x16x32_bf16 v[32:35], a[224:227], v[192:195], v[224:227]\n" \
"v_exp_f32 v112, v112\n" \
"v_mfma_f32_16x16x32_bf16 v[32:35], a[228:231], v[196:199], v[32:35]\n" \
"v_add_f32 v244, v244, v112\n" \
"v_cvt_pk_bf16_f32 v107, v110, v111\n" \
"v_mfma_f32_16x16x32_bf16 v[32:35], a[232:235], v[200:203], v[32:35]\n" \
"v_exp_f32 v113, v113\n" \
"v_mfma_f32_16x16x32_bf16 v[32:35], a[236:239], v[204:207], v[32:35]\n" \
"ds_read_b128 a[212:215], v247 offset:43008\n" \
"v_add_f32 v244, v244, v113\n" \
"v_mfma_f32_16x16x32_bf16 v[40:43], a[232:235], v[216:219], v[228:231]\n" \
"v_exp_f32 v114, v114\n" \
"v_mfma_f32_16x16x32_bf16 v[40:43], a[236:239], v[220:223], v[40:43]\n" \
"v_add_f32 v244, v244, v114\n" \
"v_cvt_pk_bf16_f32 v112, v112, v113\n" \
"v_mfma_f32_16x16x32_bf16 v[40:43], a[224:227], v[208:211], v[40:43]\n" \
"v_exp_f32 v115, v115\n" \
"v_mfma_f32_16x16x32_bf16 v[40:43], a[228:231], v[212:215], v[40:43]\n" \
"ds_read_b128 a[216:219], v247 offset:35840\n" \
"global_load_lds_dwordx4 v250, s[38:39] offset:2048 sc1\n" \
"v_add_f32 v244, v244, v115\n" \
"v_mfma_f32_16x16x32_bf16 v[48:51], a[224:227], a[160:163], v[232:235]\n" \
"v_exp_f32 v116, v116\n" \
"v_mfma_f32_16x16x32_bf16 v[48:51], a[228:231], a[164:167], v[48:51]\n" \
"v_add_f32 v244, v244, v116\n" \
"v_cvt_pk_bf16_f32 v113, v114, v115\n" \
"v_mfma_f32_16x16x32_bf16 v[48:51], a[232:235], a[168:171], v[48:51]\n" \
"v_exp_f32 v117, v117\n" \
"v_mfma_f32_16x16x32_bf16 v[48:51], a[236:239], a[172:175], v[48:51]\n" \
"ds_read_b128 a[220:223], v247 offset:44032\n" \
"global_load_lds_dwordx4 v250, s[38:39] offset:3072 sc1\n" \
"v_add_f32 v244, v244, v117\n" \
"v_mfma_f32_16x16x32_bf16 v[56:59], a[232:235], a[184:187], v[236:239]\n" \
"s_add_u32 s38, s38, 34816\n" \
"v_exp_f32 v118, v118\n" \
"v_mfma_f32_16x16x32_bf16 v[56:59], a[236:239], a[188:191], v[56:59]\n" \
"v_add_f32 v244, v244, v118\n" \
"v_cvt_pk_bf16_f32 v114, v116, v117\n" \
"v_mfma_f32_16x16x32_bf16 v[56:59], a[224:227], a[176:179], v[56:59]\n" \
"v_exp_f32 v119, v119\n" \
"v_mfma_f32_16x16x32_bf16 v[56:59], a[228:231], a[180:183], v[56:59]\n" \
"ds_read_b128 a[224:227], v247 offset:36864\n" \
"v_add_f32 v244, v244, v119\n" \
"v_mfma_f32_16x16x32_bf16 v[60:63], a[240:243], a[176:179], v[236:239]\n" \
"v_exp_f32 v120, v120\n" \
"v_mfma_f32_16x16x32_bf16 v[60:63], a[244:247], a[180:183], v[60:63]\n" \
"v_add_f32 v245, v245, v120\n" \
"v_cvt_pk_bf16_f32 v115, v118, v119\n" \
"v_mfma_f32_16x16x32_bf16 v[60:63], a[248:251], a[184:187], v[60:63]\n" \
"v_exp_f32 v121, v121\n" \
"v_mfma_f32_16x16x32_bf16 v[60:63], a[252:255], a[188:191], v[60:63]\n" \
"ds_read_b128 a[228:231], v247 offset:45056\n" \
"v_add_f32 v245, v245, v121\n" \
"v_mfma_f32_16x16x32_bf16 v[52:55], a[248:251], a[168:171], v[232:235]\n" \
"v_exp_f32 v122, v122\n" \
"v_mfma_f32_16x16x32_bf16 v[52:55], a[252:255], a[172:175], v[52:55]\n" \
"v_add_f32 v245, v245, v122\n" \
"v_cvt_pk_bf16_f32 v120, v120, v121\n" \
"v_mfma_f32_16x16x32_bf16 v[52:55], a[240:243], a[160:163], v[52:55]\n" \
"v_exp_f32 v123, v123\n" \
"v_mfma_f32_16x16x32_bf16 v[52:55], a[244:247], a[164:167], v[52:55]\n" \
"ds_read_b128 a[232:235], v247 offset:37888\n" \
"v_add_f32 v245, v245, v123\n" \
"v_mfma_f32_16x16x32_bf16 v[44:47], a[240:243], v[208:211], v[228:231]\n" \
"v_exp_f32 v124, v124\n" \
"v_mfma_f32_16x16x32_bf16 v[44:47], a[244:247], v[212:215], v[44:47]\n" \
"v_add_f32 v245, v245, v124\n" \
"v_cvt_pk_bf16_f32 v121, v122, v123\n" \
"v_mfma_f32_16x16x32_bf16 v[44:47], a[248:251], v[216:219], v[44:47]\n" \
"v_exp_f32 v125, v125\n" \
"v_mfma_f32_16x16x32_bf16 v[44:47], a[252:255], v[220:223], v[44:47]\n" \
"ds_read_b128 a[236:239], v247 offset:46080\n" \
"v_add_f32 v245, v245, v125\n" \
"v_mfma_f32_16x16x32_bf16 v[36:39], a[248:251], v[200:203], v[224:227]\n" \
"v_exp_f32 v126, v126\n" \
"v_mfma_f32_16x16x32_bf16 v[36:39], a[252:255], v[204:207], v[36:39]\n" \
"v_add_f32 v245, v245, v126\n" \
"v_cvt_pk_bf16_f32 v122, v124, v125\n" \
"v_mfma_f32_16x16x32_bf16 v[36:39], a[240:243], v[192:195], v[36:39]\n" \
"s_waitcnt vmcnt(8) lgkmcnt(4)\n" \
"v_exp_f32 v127, v127\n" \
"v_mfma_f32_16x16x32_bf16 v[36:39], a[244:247], v[196:199], v[36:39]\n" \
"s_barrier\n" \
"ds_read_b128 a[240:243], v247 offset:38912\n" \
"v_add_f32 v245, v245, v127\n" \
"v_mfma_f32_16x16x32_bf16 a[64:67], a[192:195], v[64:67], a[64:67]\n" \
"v_exp_f32 v0, v0\n" \
"v_mfma_f32_16x16x32_bf16 a[64:67], a[196:199], v[96:99], a[64:67]\n" \
"v_cvt_pk_bf16_f32 v123, v126, v127\n" \
"v_add_f32 v240, v240, v0\n" \
"v_exp_f32 v1, v1\n" \
"v_mfma_f32_16x16x32_bf16 a[96:99], a[192:195], v[72:75], a[96:99]\n" \
"s_xor_b32 m0, s73, 0x8000\n" \
"ds_read_b128 a[244:247], v247 offset:47104\n" \
"v_add_f32 v240, v240, v1\n" \
"v_mfma_f32_16x16x32_bf16 a[96:99], a[196:199], v[104:107], a[96:99]\n" \
"ds_read_b128 a[248:251], v247 offset:39936\n" \
"global_load_lds_dwordx4 v250, s[36:37] offset:-4096 sc1\n" \
"v_mfma_f32_16x16x32_bf16 v[128:131], a[192:195], v[80:83], v[128:131]\n" \
"ds_read_b128 a[252:255], v247 offset:48128\n" \
"v_exp_f32 v2, v2\n" \
"v_mfma_f32_16x16x32_bf16 v[128:131], a[196:199], v[112:115], v[128:131]\n" \
"v_add_f32 v240, v240, v2\n" \
"v_cvt_pk_bf16_f32 v0, v0, v1\n" \
"v_mfma_f32_16x16x32_bf16 v[160:163], a[192:195], v[88:91], v[160:163]\n" \
"v_exp_f32 v3, v3\n" \
"v_mfma_f32_16x16x32_bf16 v[160:163], a[196:199], v[120:123], v[160:163]\n" \
"ds_read_b128 a[192:195], v252 offset:0\n" \
"global_load_lds_dwordx4 v250, s[36:37] offset:-3072 sc1\n" \
"Lpg7397_%=:\n" \
"Lst1x0_%=:\n" \
"s_cbranch_execz Lpg9734_%=\n" \
"v_add_f32 v240, v240, v3\n" \
"v_mfma_f32_16x16x32_bf16 v[164:167], a[200:203], v[88:91], v[164:167]\n" \
"v_exp_f32 v4, v4\n" \
"v_mfma_f32_16x16x32_bf16 v[164:167], a[204:207], v[120:123], v[164:167]\n" \
"v_add_f32 v240, v240, v4\n" \
"v_cvt_pk_bf16_f32 v1, v2, v3\n" \
"v_mfma_f32_16x16x32_bf16 v[132:135], a[200:203], v[80:83], v[132:135]\n" \
"v_exp_f32 v5, v5\n" \
"v_mfma_f32_16x16x32_bf16 v[132:135], a[204:207], v[112:115], v[132:135]\n" \
"ds_read_b128 a[196:199], v252 offset:2048\n" \
"v_add_f32 v240, v240, v5\n" \
"v_mfma_f32_16x16x32_bf16 a[100:103], a[200:203], v[72:75], a[100:103]\n" \
"v_exp_f32 v6, v6\n" \
"v_mfma_f32_16x16x32_bf16 a[100:103], a[204:207], v[104:107], a[100:103]\n" \
"v_add_f32 v240, v240, v6\n" \
"v_cvt_pk_bf16_f32 v2, v4, v5\n" \
"v_mfma_f32_16x16x32_bf16 a[68:71], a[200:203], v[64:67], a[68:71]\n" \
"v_exp_f32 v7, v7\n" \
"v_mfma_f32_16x16x32_bf16 a[68:71], a[204:207], v[96:99], a[68:71]\n" \
"ds_read_b128 a[200:203], v252 offset:4096\n" \
"global_load_lds_dwordx4 v250, s[36:37] offset:-2048 sc1\n" \
"v_add_f32 v240, v240, v7\n" \
"v_mfma_f32_16x16x32_bf16 a[72:75], a[208:211], v[64:67], a[72:75]\n" \
"v_exp_f32 v8, v8\n" \
"v_mfma_f32_16x16x32_bf16 a[72:75], a[212:215], v[96:99], a[72:75]\n" \
"v_add_f32 v241, v241, v8\n" \
"v_cvt_pk_bf16_f32 v3, v6, v7\n" \
"v_mfma_f32_16x16x32_bf16 a[104:107], a[208:211], v[72:75], a[104:107]\n" \
"v_exp_f32 v9, v9\n" \
"v_mfma_f32_16x16x32_bf16 a[104:107], a[212:215], v[104:107], a[104:107]\n" \
"ds_read_b128 a[204:207], v252 offset:6144\n" \
"global_load_lds_dwordx4 v250, s[36:37] offset:-1024 sc1\n" \
"v_add_f32 v241, v241, v9\n" \
"v_mfma_f32_16x16x32_bf16 v[136:139], a[208:211], v[80:83], v[136:139]\n" \
"v_exp_f32 v10, v10\n" \
"v_mfma_f32_16x16x32_bf16 v[136:139], a[212:215], v[112:115], v[136:139]\n" \
"v_add_f32 v241, v241, v10\n" \
"v_cvt_pk_bf16_f32 v8, v8, v9\n" \
"v_mfma_f32_16x16x32_bf16 v[168:171], a[208:211], v[88:91], v[168:171]\n" \
"v_exp_f32 v11, v11\n" \
"v_mfma_f32_16x16x32_bf16 v[168:171], a[212:215], v[120:123], v[168:171]\n" \
"ds_read_b128 a[208:211], v252 offset:256\n" \
"v_add_f32 v241, v241, v11\n" \
"v_mfma_f32_16x16x32_bf16 v[172:175], a[216:219], v[88:91], v[172:175]\n" \
"v_exp_f32 v12, v12\n" \
"v_mfma_f32_16x16x32_bf16 v[172:175], a[220:223], v[120:123], v[172:175]\n" \
"v_add_f32 v241, v241, v12\n" \
"v_cvt_pk_bf16_f32 v9, v10, v11\n" \
"v_mfma_f32_16x16x32_bf16 v[140:143], a[216:219], v[80:83], v[140:143]\n" \
"v_exp_f32 v13, v13\n" \
"v_mfma_f32_16x16x32_bf16 v[140:143], a[220:223], v[112:115], v[140:143]\n" \
"ds_read_b128 a[212:215], v252 offset:2304\n" \
"global_load_lds_dwordx4 v250, s[36:37] offset:0 sc1\n" \
"v_add_f32 v241, v241, v13\n" \
"v_mfma_f32_16x16x32_bf16 a[108:111], a[216:219], v[72:75], a[108:111]\n" \
"v_exp_f32 v14, v14\n" \
"v_mfma_f32_16x16x32_bf16 a[108:111], a[220:223], v[104:107], a[108:111]\n" \
"v_add_f32 v241, v241, v14\n" \
"v_cvt_pk_bf16_f32 v10, v12, v13\n" \
"v_mfma_f32_16x16x32_bf16 a[76:79], a[216:219], v[64:67], a[76:79]\n" \
"s_waitcnt lgkmcnt(6)\n" \
"v_exp_f32 v15, v15\n" \
"v_mfma_f32_16x16x32_bf16 a[76:79], a[220:223], v[96:99], a[76:79]\n" \
"ds_read_b128 a[216:219], v252 offset:4352\n" \
"global_load_lds_dwordx4 v250, s[36:37] offset:1024 sc1\n" \
"v_add_f32 v241, v241, v15\n" \
"v_mfma_f32_16x16x32_bf16 a[80:83], a[224:227], v[64:67], a[80:83]\n" \
"v_exp_f32 v16, v16\n" \
"v_mfma_f32_16x16x32_bf16 a[80:83], a[228:231], v[96:99], a[80:83]\n" \
"v_add_f32 v244, v244, v16\n" \
"v_cvt_pk_bf16_f32 v11, v14, v15\n" \
"v_mfma_f32_16x16x32_bf16 a[112:115], a[224:227], v[72:75], a[112:115]\n" \
"v_exp_f32 v17, v17\n" \
"v_mfma_f32_16x16x32_bf16 a[112:115], a[228:231], v[104:107], a[112:115]\n" \
"ds_read_b128 a[220:223], v252 offset:6400\n" \
"v_add_f32 v244, v244, v17\n" \
"v_mfma_f32_16x16x32_bf16 v[144:147], a[224:227], v[80:83], v[144:147]\n" \
"v_exp_f32 v18, v18\n" \
"v_mfma_f32_16x16x32_bf16 v[144:147], a[228:231], v[112:115], v[144:147]\n" \
"v_add_f32 v244, v244, v18\n" \
"v_cvt_pk_bf16_f32 v16, v16, v17\n" \
"v_mfma_f32_16x16x32_bf16 v[176:179], a[224:227], v[88:91], v[176:179]\n" \
"v_exp_f32 v19, v19\n" \
"v_mfma_f32_16x16x32_bf16 v[176:179], a[228:231], v[120:123], v[176:179]\n" \
"ds_read_b128 a[224:227], v252 offset:8192\n" \
"global_load_lds_dwordx4 v250, s[36:37] offset:2048 sc1\n" \
"v_add_f32 v244, v244, v19\n" \
"v_mfma_f32_16x16x32_bf16 v[180:183], a[232:235], v[88:91], v[180:183]\n" \
"v_exp_f32 v20, v20\n" \
"v_mfma_f32_16x16x32_bf16 v[180:183], a[236:239], v[120:123], v[180:183]\n" \
"v_add_f32 v244, v244, v20\n" \
"v_cvt_pk_bf16_f32 v17, v18, v19\n" \
"v_mfma_f32_16x16x32_bf16 v[148:151], a[232:235], v[80:83], v[148:151]\n" \
"v_exp_f32 v21, v21\n" \
"v_mfma_f32_16x16x32_bf16 v[148:151], a[236:239], v[112:115], v[148:151]\n" \
"ds_read_b128 a[228:231], v252 offset:10240\n" \
"global_load_lds_dwordx4 v250, s[36:37] offset:3072 sc1\n" \
"v_add_f32 v244, v244, v21\n" \
"v_mfma_f32_16x16x32_bf16 a[116:119], a[232:235], v[72:75], a[116:119]\n" \
"s_add_u32 s36, s36, 34816\n" \
"v_exp_f32 v22, v22\n" \
"v_mfma_f32_16x16x32_bf16 a[116:119], a[236:239], v[104:107], a[116:119]\n" \
"v_add_f32 v244, v244, v22\n" \
"v_cvt_pk_bf16_f32 v18, v20, v21\n" \
"v_mfma_f32_16x16x32_bf16 a[84:87], a[232:235], v[64:67], a[84:87]\n" \
"v_exp_f32 v23, v23\n" \
"v_mfma_f32_16x16x32_bf16 a[84:87], a[236:239], v[96:99], a[84:87]\n" \
"ds_read_b128 a[232:235], v252 offset:12288\n" \
"v_add_f32 v244, v244, v23\n" \
"v_mfma_f32_16x16x32_bf16 a[88:91], a[240:243], v[64:67], a[88:91]\n" \
"v_exp_f32 v24, v24\n" \
"v_mfma_f32_16x16x32_bf16 a[88:91], a[244:247], v[96:99], a[88:91]\n" \
"v_add_f32 v245, v245, v24\n" \
"v_cvt_pk_bf16_f32 v19, v22, v23\n" \
"v_mfma_f32_16x16x32_bf16 a[120:123], a[240:243], v[72:75], a[120:123]\n" \
"v_exp_f32 v25, v25\n" \
"v_mfma_f32_16x16x32_bf16 a[120:123], a[244:247], v[104:107], a[120:123]\n" \
"ds_read_b128 a[236:239], v252 offset:14336\n" \
"v_add_f32 v245, v245, v25\n" \
"v_mfma_f32_16x16x32_bf16 v[152:155], a[240:243], v[80:83], v[152:155]\n" \
"v_exp_f32 v26, v26\n" \
"v_mfma_f32_16x16x32_bf16 v[152:155], a[244:247], v[112:115], v[152:155]\n" \
"v_add_f32 v245, v245, v26\n" \
"v_cvt_pk_bf16_f32 v24, v24, v25\n" \
"v_mfma_f32_16x16x32_bf16 v[184:187], a[240:243], v[88:91], v[184:187]\n" \
"v_exp_f32 v27, v27\n" \
"v_mfma_f32_16x16x32_bf16 v[184:187], a[244:247], v[120:123], v[184:187]\n" \
"ds_read_b128 a[240:243], v252 offset:8448\n" \
"v_add_f32 v245, v245, v27\n" \
"v_mfma_f32_16x16x32_bf16 v[188:191], a[248:251], v[88:91], v[188:191]\n" \
"v_exp_f32 v28, v28\n" \
"v_mfma_f32_16x16x32_bf16 v[188:191], a[252:255], v[120:123], v[188:191]\n" \
"v_add_f32 v245, v245, v28\n" \
"v_cvt_pk_bf16_f32 v25, v26, v27\n" \
"v_mfma_f32_16x16x32_bf16 v[156:159], a[248:251], v[80:83], v[156:159]\n" \
"v_exp_f32 v29, v29\n" \
"v_mfma_f32_16x16x32_bf16 v[156:159], a[252:255], v[112:115], v[156:159]\n" \
"ds_read_b128 a[244:247], v252 offset:10496\n" \
"v_add_f32 v245, v245, v29\n" \
"v_mfma_f32_16x16x32_bf16 a[124:127], a[248:251], v[72:75], a[124:127]\n" \
"v_exp_f32 v30, v30\n" \
"v_mfma_f32_16x16x32_bf16 a[124:127], a[252:255], v[104:107], a[124:127]\n" \
"v_add_f32 v245, v245, v30\n" \
"v_cvt_pk_bf16_f32 v26, v28, v29\n" \
"v_mfma_f32_16x16x32_bf16 a[92:95], a[248:251], v[64:67], a[92:95]\n" \
"s_waitcnt vmcnt(8) lgkmcnt(6)\n" \
"v_exp_f32 v31, v31\n" \
"v_mfma_f32_16x16x32_bf16 a[92:95], a[252:255], v[96:99], a[92:95]\n" \
"s_barrier\n" \
"ds_read_b128 a[248:251], v252 offset:12544\n" \
"v_add_f32 v245, v245, v31\n" \
"LA1g_%=:\n" \
"Lpg7550_%=:\n" \
"s_cbranch_execz Lcb0_%=\n" \
"v_mfma_f32_16x16x32_bf16 v[64:67], a[192:195], v[192:195], v[224:227]\n" \
"v_exp_f32 v32, v32\n" \
"v_mfma_f32_16x16x32_bf16 v[64:67], a[196:199], v[196:199], v[64:67]\n" \
"v_cvt_pk_bf16_f32 v27, v30, v31\n" \
"v_add_f32 v240, v240, v32\n" \
"v_mfma_f32_16x16x32_bf16 v[64:67], a[200:203], v[200:203], v[64:67]\n" \
"s_add_u32 m0, s74, 0x8000\n" \
"v_exp_f32 v33, v33\n" \
"v_mfma_f32_16x16x32_bf16 v[64:67], a[204:207], v[204:207], v[64:67]\n" \
"ds_read_b128 a[252:255], v252 offset:14592\n" \
"v_add_f32 v240, v240, v33\n" \
"v_mfma_f32_16x16x32_bf16 v[72:75], a[200:203], v[216:219], v[228:231]\n" \
"global_load_lds_dwordx4 v250, s[38:39] offset:-4096 sc1\n" \
"global_load_lds_dwordx4 v250, s[38:39] offset:-3072 sc1\n" \
"v_mfma_f32_16x16x32_bf16 v[72:75], a[204:207], v[220:223], v[72:75]\n" \
"v_exp_f32 v34, v34\n" \
"v_mfma_f32_16x16x32_bf16 v[72:75], a[192:195], v[208:211], v[72:75]\n" \
"v_add_f32 v240, v240, v34\n" \
"v_cvt_pk_bf16_f32 v32, v32, v33\n" \
"v_mfma_f32_16x16x32_bf16 v[72:75], a[196:199], v[212:215], v[72:75]\n" \
"v_exp_f32 v35, v35\n" \
"v_mfma_f32_16x16x32_bf16 v[80:83], a[192:195], a[160:163], v[232:235]\n" \
"v_add_f32 v240, v240, v35\n" \
"v_exp_f32 v36, v36\n" \
"v_mfma_f32_16x16x32_bf16 v[80:83], a[196:199], a[164:167], v[80:83]\n" \
"v_add_f32 v240, v240, v36\n" \
"v_cvt_pk_bf16_f32 v33, v34, v35\n" \
"v_mfma_f32_16x16x32_bf16 v[80:83], a[200:203], a[168:171], v[80:83]\n" \
"v_exp_f32 v37, v37\n" \
"v_mfma_f32_16x16x32_bf16 v[80:83], a[204:207], a[172:175], v[80:83]\n" \
"v_add_f32 v240, v240, v37\n" \
"v_exp_f32 v38, v38\n" \
"v_mfma_f32_16x16x32_bf16 v[88:91], a[200:203], a[184:187], v[236:239]\n" \
"v_add_f32 v240, v240, v38\n" \
"v_cvt_pk_bf16_f32 v34, v36, v37\n" \
"v_mfma_f32_16x16x32_bf16 v[88:91], a[204:207], a[188:191], v[88:91]\n" \
"v_exp_f32 v39, v39\n" \
"v_mfma_f32_16x16x32_bf16 v[88:91], a[192:195], a[176:179], v[88:91]\n" \
"v_add_f32 v240, v240, v39\n" \
"v_mfma_f32_16x16x32_bf16 v[88:91], a[196:199], a[180:183], v[88:91]\n" \
"ds_read_b128 a[192:195], v247 offset:0\n" \
"global_load_lds_dwordx4 v250, s[38:39] offset:-2048 sc1\n" \
"v_mfma_f32_16x16x32_bf16 v[92:95], a[208:211], a[176:179], v[236:239]\n" \
"v_exp_f32 v40, v40\n" \
"v_mfma_f32_16x16x32_bf16 v[92:95], a[212:215], a[180:183], v[92:95]\n" \
"v_add_f32 v241, v241, v40\n" \
"v_cvt_pk_bf16_f32 v35, v38, v39\n" \
"v_mfma_f32_16x16x32_bf16 v[92:95], a[216:219], a[184:187], v[92:95]\n" \
"v_exp_f32 v41, v41\n" \
"v_mfma_f32_16x16x32_bf16 v[92:95], a[220:223], a[188:191], v[92:95]\n" \
"ds_read_b128 a[196:199], v247 offset:8192\n" \
"global_load_lds_dwordx4 v250, s[38:39] offset:-1024 sc1\n" \
"v_add_f32 v241, v241, v41\n" \
"v_mfma_f32_16x16x32_bf16 v[84:87], a[216:219], a[168:171], v[232:235]\n" \
"v_exp_f32 v42, v42\n" \
"v_mfma_f32_16x16x32_bf16 v[84:87], a[220:223], a[172:175], v[84:87]\n" \
"v_add_f32 v241, v241, v42\n" \
"v_cvt_pk_bf16_f32 v40, v40, v41\n" \
"v_mfma_f32_16x16x32_bf16 v[84:87], a[208:211], a[160:163], v[84:87]\n" \
"v_exp_f32 v43, v43\n" \
"v_mfma_f32_16x16x32_bf16 v[84:87], a[212:215], a[164:167], v[84:87]\n" \
"ds_read_b128 a[200:203], v247 offset:1024\n" \
"v_add_f32 v241, v241, v43\n" \
"v_mfma_f32_16x16x32_bf16 v[76:79], a[208:211], v[208:211], v[228:231]\n" \
"v_exp_f32 v44, v44\n" \
"v_mfma_f32_16x16x32_bf16 v[76:79], a[212:215], v[212:215], v[76:79]\n" \
"v_add_f32 v241, v241, v44\n" \
"v_cvt_pk_bf16_f32 v41, v42, v43\n" \
"v_mfma_f32_16x16x32_bf16 v[76:79], a[216:219], v[216:219], v[76:79]\n" \
"v_exp_f32 v45, v45\n" \
"v_mfma_f32_16x16x32_bf16 v[76:79], a[220:223], v[220:223], v[76:79]\n" \
"ds_read_b128 a[204:207], v247 offset:9216\n" \
"global_load_lds_dwordx4 v250, s[38:39] offset:0 sc1\n" \
"v_add_f32 v241, v241, v45\n" \
"v_mfma_f32_16x16x32_bf16 v[68:71], a[216:219], v[200:203], v[224:227]\n" \
"v_exp_f32 v46, v46\n" \
"v_mfma_f32_16x16x32_bf16 v[68:71], a[220:223], v[204:207], v[68:71]\n" \
"v_add_f32 v241, v241, v46\n" \
"v_cvt_pk_bf16_f32 v42, v44, v45\n" \
"v_mfma_f32_16x16x32_bf16 v[68:71], a[208:211], v[192:195], v[68:71]\n" \
"s_waitcnt lgkmcnt(4)\n" \
"v_exp_f32 v47, v47\n" \
"v_mfma_f32_16x16x32_bf16 v[68:71], a[212:215], v[196:199], v[68:71]\n" \
"ds_read_b128 a[208:211], v247 offset:2048\n" \
"global_load_lds_dwordx4 v250, s[38:39] offset:1024 sc1\n" \
"v_add_f32 v241, v241, v47\n" \
"v_mfma_f32_16x16x32_bf16 v[96:99], a[224:227], v[192:195], v[224:227]\n" \
"v_exp_f32 v48, v48\n" \
"v_mfma_f32_16x16x32_bf16 v[96:99], a[228:231], v[196:199], v[96:99]\n" \
"v_add_f32 v244, v244, v48\n" \
"v_cvt_pk_bf16_f32 v43, v46, v47\n" \
"v_mfma_f32_16x16x32_bf16 v[96:99], a[232:235], v[200:203], v[96:99]\n" \
"v_exp_f32 v49, v49\n" \
"v_mfma_f32_16x16x32_bf16 v[96:99], a[236:239], v[204:207], v[96:99]\n" \
"ds_read_b128 a[212:215], v247 offset:10240\n" \
"v_add_f32 v244, v244, v49\n" \
"v_mfma_f32_16x16x32_bf16 v[104:107], a[232:235], v[216:219], v[228:231]\n" \
"v_exp_f32 v50, v50\n" \
"v_mfma_f32_16x16x32_bf16 v[104:107], a[236:239], v[220:223], v[104:107]\n" \
"v_add_f32 v244, v244, v50\n" \
"v_cvt_pk_bf16_f32 v48, v48, v49\n" \
"v_mfma_f32_16x16x32_bf16 v[104:107], a[224:227], v[208:211], v[104:107]\n" \
"v_exp_f32 v51, v51\n" \
"v_mfma_f32_16x16x32_bf16 v[104:107], a[228:231], v[212:215], v[104:107]\n" \
"ds_read_b128 a[216:219], v247 offset:3072\n" \
"global_load_lds_dwordx4 v250, s[38:39] offset:2048 sc1\n" \
"v_add_f32 v244, v244, v51\n" \
"v_mfma_f32_16x16x32_bf16 v[112:115], a[224:227], a[160:163], v[232:235]\n" \
"v_exp_f32 v52, v52\n" \
"v_mfma_f32_16x16x32_bf16 v[112:115], a[228:231], a[164:167], v[112:115]\n" \
"v_add_f32 v244, v244, v52\n" \
"v_cvt_pk_bf16_f32 v49, v50, v51\n" \
"v_mfma_f32_16x16x32_bf16 v[112:115], a[232:235], a[168:171], v[112:115]\n" \
"v_exp_f32 v53, v53\n" \
"v_mfma_f32_16x16x32_bf16 v[112:115], a[236:239], a[172:175], v[112:115]\n" \
"ds_read_b128 a[220:223], v247 offset:11264\n" \
"global_load_lds_dwordx4 v250, s[38:39] offset:3072 sc1\n" \
"v_add_f32 v244, v244, v53\n" \
"v_mfma_f32_16x16x32_bf16 v[120:123], a[232:235], a[184:187], v[236:239]\n" \
"s_add_u32 s38, s38, 34816\n" \
"v_exp_f32 v54, v54\n" \
"v_mfma_f32_16x16x32_bf16 v[120:123], a[236:239], a[188:191], v[120:123]\n" \
"s_sub_u32 s76, s76, 1\n" \
"v_add_f32 v244, v244, v54\n" \
"v_cvt_pk_bf16_f32 v50, v52, v53\n" \
"v_mfma_f32_16x16x32_bf16 v[120:123], a[224:227], a[176:179], v[120:123]\n" \
"s_cmp_lg_u32 s76, 0\n" \
"v_exp_f32 v55, v55\n" \
"v_mfma_f32_16x16x32_bf16 v[120:123], a[228:231], a[180:183], v[120:123]\n" \
"ds_read_b128 a[224:227], v247 offset:4096\n" \
"v_add_f32 v244, v244, v55\n" \
"v_mfma_f32_16x16x32_bf16 v[124:127], a[240:243], a[176:179], v[236:239]\n" \
"v_exp_f32 v56, v56\n" \
"v_mfma_f32_16x16x32_bf16 v[124:127], a[244:247], a[180:183], v[124:127]\n" \
"v_add_f32 v245, v245, v56\n" \
"v_cvt_pk_bf16_f32 v51, v54, v55\n" \
"v_mfma_f32_16x16x32_bf16 v[124:127], a[248:251], a[184:187], v[124:127]\n" \
"v_exp_f32 v57, v57\n" \
"v_mfma_f32_16x16x32_bf16 v[124:127], a[252:255], a[188:191], v[124:127]\n" \
"ds_read_b128 a[228:231], v247 offset:12288\n" \
"v_add_f32 v245, v245, v57\n" \
"v_mfma_f32_16x16x32_bf16 v[116:119], a[248:251], a[168:171], v[232:235]\n" \
"v_exp_f32 v58, v58\n" \
"v_mfma_f32_16x16x32_bf16 v[116:119], a[252:255], a[172:175], v[116:119]\n" \
"v_add_f32 v245, v245, v58\n" \
"v_cvt_pk_bf16_f32 v56, v56, v57\n" \
"v_mfma_f32_16x16x32_bf16 v[116:119], a[240:243], a[160:163], v[116:119]\n" \
"v_exp_f32 v59, v59\n" \
"v_mfma_f32_16x16x32_bf16 v[116:119], a[244:247], a[164:167], v[116:119]\n" \
"ds_read_b128 a[232:235], v247 offset:5120\n" \
"v_add_f32 v245, v245, v59\n" \
"v_mfma_f32_16x16x32_bf16 v[108:111], a[240:243], v[208:211], v[228:231]\n" \
"v_exp_f32 v60, v60\n" \
"v_mfma_f32_16x16x32_bf16 v[108:111], a[244:247], v[212:215], v[108:111]\n" \
"v_add_f32 v245, v245, v60\n" \
"v_cvt_pk_bf16_f32 v57, v58, v59\n" \
"v_mfma_f32_16x16x32_bf16 v[108:111], a[248:251], v[216:219], v[108:111]\n" \
"v_exp_f32 v61, v61\n" \
"v_mfma_f32_16x16x32_bf16 v[108:111], a[252:255], v[220:223], v[108:111]\n" \
"ds_read_b128 a[236:239], v247 offset:13312\n" \
"v_add_f32 v245, v245, v61\n" \
"v_mfma_f32_16x16x32_bf16 v[100:103], a[248:251], v[200:203], v[224:227]\n" \
"v_exp_f32 v62, v62\n" \
"v_mfma_f32_16x16x32_bf16 v[100:103], a[252:255], v[204:207], v[100:103]\n" \
"v_add_f32 v245, v245, v62\n" \
"v_cvt_pk_bf16_f32 v58, v60, v61\n" \
"v_mfma_f32_16x16x32_bf16 v[100:103], a[240:243], v[192:195], v[100:103]\n" \
"s_waitcnt vmcnt(8) lgkmcnt(4)\n" \
"v_exp_f32 v63, v63\n" \
"v_mfma_f32_16x16x32_bf16 v[100:103], a[244:247], v[196:199], v[100:103]\n" \
"s_barrier\n" \
"ds_read_b128 a[240:243], v247 offset:6144\n" \
"v_add_f32 v245, v245, v63\n" \
"s_cbranch_scc1 Lloop2_%=\n" \
"v_mfma_f32_16x16x32_bf16 a[64:67], a[192:195], v[0:3], a[64:67]\n" \
"s_bfe_u32 s88, %1, 0x20001\n" \
"v_subrev_u32 v246, s82, v246\n" \
"s_lshl_b32 s88, s88, 2\n" \
"v_mfma_f32_16x16x32_bf16 a[64:67], a[196:199], v[32:35], a[64:67]\n" \
"s_memrealtime s[52:53]\n" \
"v_cvt_pk_bf16_f32 v59, v62, v63\n" \
"s_lshr_b32 s70, %1, 6\n" \
"v_mfma_f32_16x16x32_bf16 a[96:99], a[192:195], v[8:11], a[96:99]\n" \
"s_add_u32 s88, s88, s70\n" \
"ds_read_b128 a[244:247], v247 offset:14336\n" \
"v_mov_b32 v216, v243\n" \
"v_mfma_f32_16x16x32_bf16 a[96:99], a[196:199], v[40:43], a[96:99]\n" \
"s_lshr_b32 s71, s22, 7\n" \
"ds_read_b128 a[248:251], v247 offset:7168\n" \
"v_exp_f32 v64, v64\n" \
"v_mfma_f32_16x16x32_bf16 v[128:131], a[192:195], v[16:19], v[128:131]\n" \
"s_sub_u32 s71, s63, s71\n" \
"ds_read_b128 a[252:255], v247 offset:15360\n" \
"v_mov_b32 v214, v216\n" \
"v_mfma_f32_16x16x32_bf16 v[128:131], a[196:199], v[48:51], v[128:131]\n" \
"s_add_u32 s71, s71, 6\n" \
"v_exp_f32 v65, v65\n" \
"v_mfma_f32_16x16x32_bf16 v[160:163], a[192:195], v[24:27], v[160:163]\n" \
"s_and_b32 s71, s71, 7\n" \
"v_exp_f32 v66, v66\n" \
"v_mfma_f32_16x16x32_bf16 v[160:163], a[196:199], v[56:59], v[160:163]\n" \
"s_and_b32 s70, %1, 1\n" \
"ds_read_b128 a[192:195], v246 offset:0\n" \
"v_cvt_pk_bf16_f32 v64, v64, v65\n" \
"v_permlane16_swap_b32 v214, v216\n" \
"v_add_f32 v216, v216, v214\n" \
"v_mov_b32 v214, v216\n" \
"v_mfma_f32_16x16x32_bf16 v[164:167], a[200:203], v[24:27], v[164:167]\n" \
"s_lshl_b32 s70, s70, 3\n" \
"v_exp_f32 v67, v67\n" \
"v_mfma_f32_16x16x32_bf16 v[164:167], a[204:207], v[56:59], v[164:167]\n" \
"s_waitcnt lgkmcnt(0)\n" \
"v_exp_f32 v68, v68\n" \
"v_cvt_pk_bf16_f32 v65, v66, v67\n" \
"v_permlane32_swap_b32 v214, v216\n" \
"v_add_f32 v216, v216, v214\n" \
"v_rcp_f32 v214, v216\n" \
"v_mul_f32 v216, 0x4d800000, v216\n" \
"v_add_u32 v215, 0x1c90080, v249\n" \
"v_cndmask_b32 v215, v249, v215, s[16:17]\n" \
"s_add_u32 s77, s77, 532480\n" \
"v_accvgpr_read_b32 v192, a32\n" \
"v_mfma_f32_16x16x32_bf16 v[132:135], a[200:203], v[16:19], v[132:135]\n" \
"s_barrier\n" \
"v_mfma_f32_16x16x32_bf16 v[132:135], a[204:207], v[48:51], v[132:135]\n" \
"s_and_b32 s54, %1, 7\n" \
"ds_read_b128 a[196:199], v246 offset:2048\n" \
"s_mov_b32 m0, s74\n" \
"v_mfma_f32_16x16x32_bf16 a[100:103], a[200:203], v[8:11], a[100:103]\n" \
"s_lshl_b32 s54, s54, 2\n" \
"v_exp_f32 v69, v69\n" \
"v_mfma_f32_16x16x32_bf16 a[100:103], a[204:207], v[40:43], a[100:103]\n" \
"s_lshr_b32 s55, %1, 6\n" \
"v_exp_f32 v70, v70\n" \
"v_mfma_f32_16x16x32_bf16 a[68:71], a[200:203], v[0:3], a[68:71]\n" \
"s_add_u32 s54, s54, s55\n" \
"v_cvt_pk_bf16_f32 v66, v68, v69\n" \
"v_accvgpr_read_b32 v193, a33\n" \
"v_mfma_f32_16x16x32_bf16 a[68:71], a[204:207], v[32:35], a[68:71]\n" \
"s_lshl_b32 s54, s54, 3\n" \
"ds_read_b128 a[200:203], v246 offset:4096\n" \
"global_load_lds_dwordx4 v250, s[38:39] offset:-4096 sc1\n" \
"global_load_lds_dwordx4 v250, s[38:39] offset:-3072 sc1\n" \
"v_mfma_f32_16x16x32_bf16 a[72:75], a[208:211], v[0:3], a[72:75]\n" \
"s_add_u32 s54, s54, 65593856\n" \
"v_exp_f32 v71, v71\n" \
"v_mfma_f32_16x16x32_bf16 a[72:75], a[212:215], v[32:35], a[72:75]\n" \
"s_sub_u32 s53, s52, s12\n" \
"v_exp_f32 v72, v72\n" \
"v_mfma_f32_16x16x32_bf16 a[104:107], a[208:211], v[8:11], a[104:107]\n" \
"s_mul_i32 s53, s53, 3\n" \
"v_cvt_pk_bf16_f32 v67, v70, v71\n" \
"v_accvgpr_read_b32 v194, a34\n" \
"v_mfma_f32_16x16x32_bf16 a[104:107], a[212:215], v[40:43], a[104:107]\n" \
"s_lshl_b32 s52, s52, 1\n" \
"ds_read_b128 a[204:207], v246 offset:6144\n" \
"global_load_lds_dwordx4 v250, s[38:39] offset:-2048 sc1\n" \
"v_mfma_f32_16x16x32_bf16 v[136:139], a[208:211], v[16:19], v[136:139]\n" \
"s_add_u32 s52, s52, s53\n" \
"v_mov_b32 v211, s52\n" \
"v_xor_b32 v210, s60, v211\n" \
"v_mfma_f32_16x16x32_bf16 v[136:139], a[212:215], v[48:51], v[136:139]\n" \
"s_add_u32 s70, s70, s71\n" \
"v_exp_f32 v73, v73\n" \
"v_mfma_f32_16x16x32_bf16 v[168:171], a[208:211], v[24:27], v[168:171]\n" \
"s_lshl_b32 s87, s70, 12\n" \
"v_exp_f32 v74, v74\n" \
"v_mfma_f32_16x16x32_bf16 v[168:171], a[212:215], v[56:59], v[168:171]\n" \
"s_lshl_b32 s70, s70, 7\n" \
"ds_read_b128 a[208:211], v246 offset:256\n" \
"v_lshlrev_b32 v212, 28, s61\n" \
"v_mfma_f32_16x16x32_bf16 v[172:175], a[216:219], v[24:27], v[172:175]\n" \
"s_cmp_eq_u32 s63, 3\n" \
"s_cselect_b32 s54, s54, 0x20000000\n" \
"v_cvt_pk_bf16_f32 v72, v72, v73\n" \
"v_mfma_f32_16x16x32_bf16 v[172:175], a[220:223], v[56:59], v[172:175]\n" \
"s_mul_i32 s84, s61, 6144\n" \
"v_exp_f32 v75, v75\n" \
"v_mfma_f32_16x16x32_bf16 v[140:143], a[216:219], v[16:19], v[140:143]\n" \
"s_lshl_b32 s71, s84, 4\n" \
"v_exp_f32 v76, v76\n" \
"v_mfma_f32_16x16x32_bf16 v[140:143], a[220:223], v[48:51], v[140:143]\n" \
"s_add_u32 s70, s70, s71\n" \
"ds_read_b128 a[212:215], v246 offset:2304\n" \
"global_load_lds_dwordx4 v250, s[38:39] offset:-1024 sc1\n" \
"v_cvt_pk_bf16_f32 v73, v74, v75\n" \
"v_mfma_f32_16x16x32_bf16 a[108:111], a[216:219], v[8:11], a[108:111]\n" \
"s_add_u32 s70, s70, 4096\n" \
"v_exp_f32 v77, v77\n" \
"v_mfma_f32_16x16x32_bf16 a[108:111], a[220:223], v[40:43], a[108:111]\n" \
"s_add_u32 s48, s22, s70\n" \
"s_addc_u32 s49, s23, 0\n" \
"v_accvgpr_read_b32 v195, a35\n" \
"v_mfma_f32_16x16x32_bf16 a[76:79], a[216:219], v[0:3], a[76:79]\n" \
"s_waitcnt lgkmcnt(6)\n" \
"global_load_lds_dwordx4 v250, s[38:39] offset:0 sc1\n" \
"global_load_lds_dwordx4 v250, s[38:39] offset:1024 sc1\n" \
"v_mfma_f32_16x16x32_bf16 a[76:79], a[220:223], v[32:35], a[76:79]\n" \
"s_add_u32 s50, s48, 23552\n" \
"s_addc_u32 s51, s49, 0\n" \
"ds_read_b128 a[216:219], v246 offset:4352\n" \
"v_mfma_f32_16x16x32_bf16 a[80:83], a[224:227], v[0:3], a[80:83]\n" \
"s_lshr_b32 s89, s22, 7\n" \
"v_exp_f32 v78, v78\n" \
"v_mfma_f32_16x16x32_bf16 a[80:83], a[228:231], v[32:35], a[80:83]\n" \
"s_sub_u32 s89, 3, s89\n" \
"v_cvt_pk_bf16_f32 v74, v76, v77\n" \
"v_accvgpr_read_b32 v196, a36\n" \
"v_accvgpr_read_b32 v197, a37\n" \
"v_accvgpr_read_b32 v198, a38\n" \
"v_accvgpr_read_b32 v199, a39\n" \
"v_pk_mul_f32 v[192:193], v[192:193], v[214:215] op_sel_hi:[1,0]\n" \
"v_pk_mul_f32 v[194:195], v[194:195], v[214:215] op_sel_hi:[1,0]\n" \
"v_pk_mul_f32 v[196:197], v[196:197], v[214:215] op_sel_hi:[1,0]\n" \
"v_pk_mul_f32 v[198:199], v[198:199], v[214:215] op_sel_hi:[1,0]\n" \
"v_mfma_f32_16x16x32_bf16 a[112:115], a[224:227], v[8:11], a[112:115]\n" \
"s_and_b32 s89, s89, 7\n" \
"v_exp_f32 v79, v79\n" \
"v_mfma_f32_16x16x32_bf16 a[112:115], a[228:231], v[40:43], a[112:115]\n" \
"s_and_b32 s90, %1, 1\n" \
"ds_read_b128 a[220:223], v246 offset:6400\n" \
"global_load_lds_dwordx4 v250, s[38:39] offset:2048 sc1\n" \
"v_mfma_f32_16x16x32_bf16 v[144:147], a[224:227], v[16:19], v[144:147]\n" \
"s_lshl_b32 s90, s90, 3\n" \
"v_exp_f32 v80, v80\n" \
"v_mfma_f32_16x16x32_bf16 v[144:147], a[228:231], v[48:51], v[144:147]\n" \
"s_add_u32 s89, s89, s90\n" \
"v_cvt_pk_bf16_f32 v75, v78, v79\n" \
"v_cvt_pk_bf16_f32 v200, v192, v193\n" \
"v_cvt_pk_bf16_f32 v201, v194, v195\n" \
"v_cvt_pk_bf16_f32 v202, v196, v197\n" \
"v_cvt_pk_bf16_f32 v203, v198, v199\n" \
"v_pk_mul_f32 v[192:193], v[192:193], v[194:195]\n" \
"v_pk_fma_f32 v[192:193], v[196:197], v[198:199], v[192:193]\n" \
"v_mfma_f32_16x16x32_bf16 v[176:179], a[224:227], v[24:27], v[176:179]\n" \
"s_lshl_b32 s89, s89, 7\n" \
"v_exp_f32 v81, v81\n" \
"v_mfma_f32_16x16x32_bf16 v[176:179], a[228:231], v[56:59], v[176:179]\n" \
"s_lshr_b32 s90, %1, 3\n" \
"ds_read_b128 a[224:227], v246 offset:8192\n" \
"global_load_lds_dwordx4 v250, s[38:39] offset:3072 sc1\n" \
"v_mfma_f32_16x16x32_bf16 v[180:183], a[232:235], v[24:27], v[180:183]\n" \
"s_lshl_b32 s90, s90, 2\n" \
"v_exp_f32 v82, v82\n" \
"v_mfma_f32_16x16x32_bf16 v[180:183], a[236:239], v[56:59], v[180:183]\n" \
"v_fma_f32 v216, v192, v193, v216\n" \
"v_cvt_pk_bf16_f32 v80, v80, v81\n" \
"v_mfma_f32_16x16x32_bf16 v[238:241], a[0:3], v[64:67], v[238:241]\n" \
"s_add_u32 s90, s90, s61\n" \
"v_exp_f32 v83, v83\n" \
"v_mfma_f32_16x16x32_bf16 v[238:241], a[4:7], v[72:75], v[238:241]\n" \
"buffer_store_dwordx2 v[210:211], v212, s[56:59], s54 offen sc1\n" \
"v_mfma_f32_16x16x32_bf16 v[148:151], a[232:235], v[16:19], v[148:151]\n" \
"s_mul_i32 s90, s90, 24576\n" \
"v_exp_f32 v84, v84\n" \
"v_mfma_f32_16x16x32_bf16 v[148:151], a[236:239], v[48:51], v[148:151]\n" \
"s_add_u32 s89, s89, s90\n" \
"ds_read_b128 a[228:231], v246 offset:10240\n" \
"buffer_store_dwordx4 v[200:203], v215, s[56:59], s77 offen offset:0 sc1\n" \
"v_cvt_pk_bf16_f32 v81, v82, v83\n" \
"v_accvgpr_read_b32 v192, a40\n" \
"v_accvgpr_read_b32 v193, a41\n" \
"v_accvgpr_read_b32 v194, a42\n" \
"v_mfma_f32_16x16x32_bf16 a[116:119], a[232:235], v[8:11], a[116:119]\n" \
"s_lshl_b32 s70, s88, 18\n" \
"v_exp_f32 v85, v85\n" \
"v_mfma_f32_16x16x32_bf16 a[116:119], a[236:239], v[40:43], a[116:119]\n" \
"s_lshl_b32 s71, s61, 16\n" \
"v_accvgpr_read_b32 v195, a43\n" \
"v_accvgpr_read_b32 v196, a44\n" \
"v_accvgpr_read_b32 v197, a45\n" \
"v_accvgpr_read_b32 v198, a46\n" \
"v_accvgpr_read_b32 v199, a47\n" \
"v_exp_f32 v86, v86\n" \
"v_pk_mul_f32 v[192:193], v[192:193], v[214:215] op_sel_hi:[1,0]\n" \
"v_pk_mul_f32 v[194:195], v[194:195], v[214:215] op_sel_hi:[1,0]\n" \
"v_pk_mul_f32 v[196:197], v[196:197], v[214:215] op_sel_hi:[1,0]\n" \
"v_pk_mul_f32 v[198:199], v[198:199], v[214:215] op_sel_hi:[1,0]\n" \
"v_cvt_pk_bf16_f32 v204, v192, v193\n" \
"v_cvt_pk_bf16_f32 v205, v194, v195\n" \
"v_cvt_pk_bf16_f32 v206, v196, v197\n" \
"v_cvt_pk_bf16_f32 v207, v198, v199\n" \
"v_pk_mul_f32 v[192:193], v[192:193], v[194:195]\n" \
"v_pk_fma_f32 v[192:193], v[196:197], v[198:199], v[192:193]\n" \
"v_mfma_f32_16x16x32_bf16 a[84:87], a[232:235], v[0:3], a[84:87]\n" \
"s_add_u32 s71, s71, s70\n" \
"v_cvt_pk_bf16_f32 v82, v84, v85\n" \
"v_fma_f32 v216, v192, v193, v216\n" \
"v_mfma_f32_16x16x32_bf16 a[84:87], a[236:239], v[32:35], a[84:87]\n" \
"s_add_u32 s42, s20, s71\n" \
"s_addc_u32 s43, s21, 0\n" \
"ds_read_b128 a[232:235], v246 offset:12288\n" \
"buffer_store_dwordx4 v[204:207], v215, s[56:59], s77 offen offset:2048 sc1\n" \
"s_add_u32 s77, s77, 0x1000\n" \
"v_accvgpr_read_b32 v192, a48\n" \
"v_accvgpr_read_b32 v193, a49\n" \
"v_accvgpr_read_b32 v194, a50\n" \
"v_mfma_f32_16x16x32_bf16 a[88:91], a[240:243], v[0:3], a[88:91]\n" \
"s_add_u32 s44, s42, 31744\n" \
"s_addc_u32 s45, s43, 0\n" \
"v_accvgpr_read_b32 v195, a51\n" \
"v_mfma_f32_16x16x32_bf16 a[88:91], a[244:247], v[32:35], a[88:91]\n" \
"s_max_u32 s70, s70, 32768\n" \
"v_accvgpr_read_b32 v196, a52\n" \
"v_accvgpr_read_b32 v197, a53\n" \
"v_accvgpr_read_b32 v198, a54\n" \
"v_accvgpr_read_b32 v199, a55\n" \
"v_exp_f32 v87, v87\n" \
"v_pk_mul_f32 v[192:193], v[192:193], v[214:215] op_sel_hi:[1,0]\n" \
"v_pk_mul_f32 v[194:195], v[194:195], v[214:215] op_sel_hi:[1,0]\n" \
"v_pk_mul_f32 v[196:197], v[196:197], v[214:215] op_sel_hi:[1,0]\n" \
"v_pk_mul_f32 v[198:199], v[198:199], v[214:215] op_sel_hi:[1,0]\n" \
"v_cvt_pk_bf16_f32 v200, v192, v193\n" \
"v_cvt_pk_bf16_f32 v201, v194, v195\n" \
"v_cvt_pk_bf16_f32 v202, v196, v197\n" \
"v_cvt_pk_bf16_f32 v203, v198, v199\n" \
"v_pk_mul_f32 v[192:193], v[192:193], v[194:195]\n" \
"v_pk_fma_f32 v[192:193], v[196:197], v[198:199], v[192:193]\n" \
"v_fma_f32 v216, v192, v193, v216\n" \
"v_mfma_f32_16x16x32_bf16 a[120:123], a[240:243], v[8:11], a[120:123]\n" \
"s_sub_u32 s70, s70, 32768\n" \
"v_exp_f32 v88, v88\n" \
"v_mfma_f32_16x16x32_bf16 a[120:123], a[244:247], v[40:43], a[120:123]\n" \
"s_and_b32 s86, s61, 1\n" \
"ds_read_b128 a[236:239], v246 offset:14336\n" \
"buffer_store_dwordx4 v[200:203], v215, s[56:59], s77 offen offset:0 sc1\n" \
"v_cvt_pk_bf16_f32 v83, v86, v87\n" \
"v_accvgpr_read_b32 v192, a56\n" \
"v_accvgpr_read_b32 v193, a57\n" \
"v_accvgpr_read_b32 v194, a58\n" \
"v_mfma_f32_16x16x32_bf16 v[152:155], a[240:243], v[16:19], v[152:155]\n" \
"s_lshl_b32 s86, s86, 2\n" \
"v_exp_f32 v89, v89\n" \
"v_mfma_f32_16x16x32_bf16 v[152:155], a[244:247], v[48:51], v[152:155]\n" \
"v_accvgpr_read_b32 v195, a59\n" \
"v_accvgpr_read_b32 v196, a60\n" \
"v_exp_f32 v90, v90\n" \
"v_cvt_pk_bf16_f32 v88, v88, v89\n" \
"v_mfma_f32_16x16x32_bf16 v[242:245], a[0:3], v[80:83], v[242:245]\n" \
"s_lshr_b32 s71, s61, 1\n" \
"v_exp_f32 v91, v91\n" \
"v_mfma_f32_16x16x32_bf16 v[184:187], a[240:243], v[24:27], v[184:187]\n" \
"s_lshl_b32 s71, s71, 11\n" \
"v_exp_f32 v92, v92\n" \
"v_mfma_f32_16x16x32_bf16 v[184:187], a[244:247], v[56:59], v[184:187]\n" \
"s_add_u32 s70, s70, s71\n" \
"ds_read_b128 a[240:243], v246 offset:8448\n" \
"v_cvt_pk_bf16_f32 v89, v90, v91\n" \
"v_accvgpr_read_b32 v197, a61\n" \
"v_accvgpr_read_b32 v198, a62\n" \
"v_accvgpr_read_b32 v199, a63\n" \
"v_pk_mul_f32 v[192:193], v[192:193], v[214:215] op_sel_hi:[1,0]\n" \
"v_pk_mul_f32 v[194:195], v[194:195], v[214:215] op_sel_hi:[1,0]\n" \
"v_pk_mul_f32 v[196:197], v[196:197], v[214:215] op_sel_hi:[1,0]\n" \
"v_pk_mul_f32 v[198:199], v[198:199], v[214:215] op_sel_hi:[1,0]\n" \
"v_cvt_pk_bf16_f32 v204, v192, v193\n" \
"v_cvt_pk_bf16_f32 v205, v194, v195\n" \
"v_cvt_pk_bf16_f32 v206, v196, v197\n" \
"v_cvt_pk_bf16_f32 v207, v198, v199\n" \
"v_pk_mul_f32 v[192:193], v[192:193], v[194:195]\n" \
"v_pk_fma_f32 v[192:193], v[196:197], v[198:199], v[192:193]\n" \
"v_mfma_f32_16x16x32_bf16 v[188:191], a[248:251], v[24:27], v[188:191]\n" \
"s_mov_b32 s46, s70\n" \
"v_exp_f32 v93, v93\n" \
"v_mfma_f32_16x16x32_bf16 v[188:191], a[252:255], v[56:59], v[188:191]\n" \
"s_lshl_b32 s85, s61, 10\n" \
"v_exp_f32 v94, v94\n" \
"v_mfma_f32_16x16x32_bf16 v[156:159], a[248:251], v[16:19], v[156:159]\n" \
"s_mov_b64 s[12:13], s[20:21]\n" \
"v_cvt_pk_bf16_f32 v90, v92, v93\n" \
"v_fma_f32 v216, v192, v193, v216\n" \
"v_mfma_f32_16x16x32_bf16 v[156:159], a[252:255], v[48:51], v[156:159]\n" \
"buffer_store_dwordx4 v[204:207], v215, s[56:59], s77 offen offset:2048 sc1\n" \
"ds_read_b128 a[244:247], v246 offset:10496\n" \
"Lpg8025_%=:\n" \
"Lst1x1_%=:\n" \
"s_cbranch_execz Lpg10301_%=\n" \
"s_and_b32 s13, s13, 0xffff\n" \
"v_cmp_class_f32 vcc, v216, s94\n" \
"s_sub_u32 s77, s77, 536576\n" \
"v_mfma_f32_16x16x32_bf16 a[124:127], a[248:251], v[8:11], a[124:127]\n" \
"s_mov_b32 s14, 4194304\n" \
"v_exp_f32 v95, v95\n" \
"v_mfma_f32_16x16x32_bf16 a[124:127], a[252:255], v[40:43], a[124:127]\n" \
"s_mov_b32 s15, 0x20000\n" \
"v_cvt_pk_bf16_f32 v91, v94, v95\n" \
"s_or_b64 s[92:93], s[92:93], vcc\n" \
"v_mfma_f32_16x16x32_bf16 a[92:95], a[248:251], v[0:3], a[92:95]\n" \
"s_waitcnt vmcnt(13) lgkmcnt(6)\n" \
"v_mfma_f32_16x16x32_bf16 v[242:245], a[4:7], v[88:91], v[242:245]\n" \
"v_mfma_f32_16x16x32_bf16 a[92:95], a[252:255], v[32:35], a[92:95]\n" \
"s_barrier\n" \
"ds_read_b128 a[248:251], v246 offset:12544\n" \
"v_mfma_f32_16x16x32_bf16 v[24:27], a[192:195], a[176:179], v[236:239]\n" \
"ds_read_b128 v[192:195], v246 offset:16384\n" \
"v_bfe_u32 v69, v248, 8, 1\n" \
"v_mfma_f32_16x16x32_bf16 v[24:27], a[196:199], a[180:183], v[24:27]\n" \
"ds_read_b128 v[196:199], v246 offset:18432\n" \
"v_cvt_pk_bf16_f32 v91, v94, v95\n" \
"v_mfma_f32_16x16x32_bf16 v[24:27], a[200:203], a[184:187], v[24:27]\n" \
"ds_read_b128 v[200:203], v246 offset:20480\n" \
"v_bfe_u32 v70, v248, 6, 2\n" \
"v_mfma_f32_16x16x32_bf16 v[24:27], a[204:207], a[188:191], v[24:27]\n" \
"ds_read_b128 a[252:255], v246 offset:14592\n" \
"v_lshl_add_u32 v70, v69, 3, v70\n" \
"v_exp_f32 v96, v96\n" \
"v_exp_f32 v97, v97\n" \
"v_exp_f32 v98, v98\n" \
"v_cvt_pk_bf16_f32 v96, v96, v97\n" \
"v_exp_f32 v99, v99\n" \
"v_exp_f32 v100, v100\n" \
"v_cvt_pk_bf16_f32 v97, v98, v99\n" \
"v_exp_f32 v101, v101\n" \
"v_exp_f32 v102, v102\n" \
"v_cvt_pk_bf16_f32 v98, v100, v101\n" \
"v_mfma_f32_16x16x32_bf16 v[16:19], a[200:203], a[168:171], v[232:235]\n" \
"ds_read_b128 v[204:207], v246 offset:22528\n" \
"v_mul_u32_u24 v70, 6144, v70\n" \
"v_mfma_f32_16x16x32_bf16 v[16:19], a[204:207], a[172:175], v[16:19]\n" \
"ds_read_b128 v[208:211], v246 offset:16640\n" \
"v_lshlrev_b32 v69, 5, v69\n" \
"v_mfma_f32_16x16x32_bf16 v[16:19], a[192:195], a[160:163], v[16:19]\n" \
"ds_read_b128 v[212:215], v246 offset:18688\n" \
"v_and_b32 v71, 0x30, v248\n" \
"v_mfma_f32_16x16x32_bf16 v[16:19], a[196:199], a[164:167], v[16:19]\n" \
"ds_read_b128 v[216:219], v246 offset:20736\n" \
"v_xor_b32 v69, v69, v71\n" \
"v_exp_f32 v103, v103\n" \
"v_exp_f32 v104, v104\n" \
"v_cvt_pk_bf16_f32 v99, v102, v103\n" \
"v_exp_f32 v105, v105\n" \
"v_exp_f32 v106, v106\n" \
"v_mfma_f32_16x16x32_bf16 v[20:23], a[208:211], a[160:163], v[232:235]\n" \
"ds_read_b128 v[220:223], v246 offset:22784\n" \
"v_lshrrev_b32 v71, 9, v248\n" \
"v_mfma_f32_16x16x32_bf16 v[20:23], a[212:215], a[164:167], v[20:23]\n" \
"ds_read_b128 a[192:195], v247 offset:32768\n" \
"v_lshl_add_u32 v70, v71, 6, v70\n" \
"v_mfma_f32_16x16x32_bf16 v[20:23], a[216:219], a[168:171], v[20:23]\n" \
"ds_read_b128 a[196:199], v247 offset:40960\n" \
"v_add_u32 v253, v69, v70\n" \
"v_mfma_f32_16x16x32_bf16 v[20:23], a[220:223], a[172:175], v[20:23]\n" \
"ds_read_b128 a[200:203], v247 offset:33792\n" \
"v_bfe_u32 v84, v248, 7, 2\n" \
"v_mfma_f32_16x16x32_bf16 v[28:31], a[216:219], a[184:187], v[236:239]\n" \
"ds_read_b128 a[204:207], v247 offset:41984\n" \
"v_bfe_u32 v85, v248, 5, 2\n" \
"v_mfma_f32_16x16x32_bf16 v[28:31], a[220:223], a[188:191], v[28:31]\n" \
"v_min_u32 v85, 2, v85\n" \
"v_mad_u32_u24 v84, v84, 3, v85\n" \
"v_mfma_f32_16x16x32_bf16 v[28:31], a[208:211], a[176:179], v[28:31]\n" \
"v_and_b32 v85, 16, v248\n" \
"v_lshl_add_u32 v85, v85, 2, s89\n" \
"v_mfma_f32_16x16x32_bf16 v[28:31], a[212:215], a[180:183], v[28:31]\n" \
"s_waitcnt lgkmcnt(2)\n" \
"ds_read_b128 a[208:211], v247 offset:34816\n" \
"v_lshl_add_u32 v84, v84, 11, v85\n" \
"v_mfma_f32_16x16x32_bf16 v[8:11], v[192:195], a[176:179], v[236:239]\n" \
"s_add_u32 m0, s84, 143360\n" \
"ds_read_b128 a[212:215], v247 offset:43008\n" \
"v_cvt_pk_bf16_f32 v104, v104, v105\n" \
"v_mfma_f32_16x16x32_bf16 v[8:11], v[196:199], a[180:183], v[8:11]\n" \
"ds_read_b128 a[216:219], v247 offset:35840\n" \
"global_load_lds_dwordx4 v253, s[48:49] offset:-4096\n" \
"v_mfma_f32_16x16x32_bf16 v[8:11], v[200:203], a[184:187], v[8:11]\n" \
"ds_read_b128 a[220:223], v247 offset:44032\n" \
"v_exp_f32 v107, v107\n" \
"v_mfma_f32_16x16x32_bf16 v[8:11], v[204:207], a[188:191], v[8:11]\n" \
"ds_read_b128 v[192:195], v246 offset:24576\n" \
"v_exp_f32 v108, v108\n" \
"v_mfma_f32_16x16x32_bf16 v[56:59], a[232:235], a[184:187], v[236:239]\n" \
"ds_read_b128 v[196:199], v246 offset:26624\n" \
"global_load_lds_dwordx4 v253, s[48:49] offset:-2048\n" \
"v_mfma_f32_16x16x32_bf16 v[56:59], a[236:239], a[188:191], v[56:59]\n" \
"ds_read_b128 v[200:203], v246 offset:28672\n" \
"v_cvt_pk_bf16_f32 v105, v106, v107\n" \
"v_mfma_f32_16x16x32_bf16 v[56:59], a[224:227], a[176:179], v[56:59]\n" \
"ds_read_b128 v[204:207], v246 offset:30720\n" \
"global_load_lds_dwordx4 v253, s[48:49] offset:0\n" \
"v_mfma_f32_16x16x32_bf16 v[56:59], a[228:231], a[180:183], v[56:59]\n" \
"v_exp_f32 v109, v109\n" \
"v_exp_f32 v110, v110\n" \
"v_mfma_f32_16x16x32_bf16 v[48:51], a[224:227], a[160:163], v[232:235]\n" \
"s_add_u32 s48, s48, 0x120000\n" \
"s_addc_u32 s49, s49, 0\n" \
"v_cvt_pk_bf16_f32 v106, v108, v109\n" \
"v_mfma_f32_16x16x32_bf16 v[48:51], a[228:231], a[164:167], v[48:51]\n" \
"v_exp_f32 v111, v111\n" \
"v_mfma_f32_16x16x32_bf16 v[48:51], a[232:235], a[168:171], v[48:51]\n" \
"v_exp_f32 v112, v112\n" \
"v_mfma_f32_16x16x32_bf16 v[48:51], a[236:239], a[172:175], v[48:51]\n" \
"ds_read_b128 a[224:227], v247 offset:36864\n" \
"global_load_lds_dwordx4 v253, s[50:51] offset:-3072\n" \
"v_mfma_f32_16x16x32_bf16 v[12:15], v[216:219], a[184:187], v[236:239]\n" \
"ds_read_b128 a[228:231], v247 offset:45056\n" \
"v_cvt_pk_bf16_f32 v107, v110, v111\n" \
"v_mfma_f32_16x16x32_bf16 v[12:15], v[220:223], a[188:191], v[12:15]\n" \
"ds_read_b128 a[232:235], v247 offset:37888\n" \
"global_load_lds_dwordx4 v253, s[50:51] offset:-1024\n" \
"v_mfma_f32_16x16x32_bf16 v[12:15], v[208:211], a[176:179], v[12:15]\n" \
"ds_read_b128 a[236:239], v247 offset:46080\n" \
"v_exp_f32 v113, v113\n" \
"v_mfma_f32_16x16x32_bf16 v[12:15], v[212:215], a[180:183], v[12:15]\n" \
"ds_read_b128 v[208:211], v246 offset:24832\n" \
"v_exp_f32 v114, v114\n" \
"v_mfma_f32_16x16x32_bf16 v[60:63], a[240:243], a[176:179], v[236:239]\n" \
"ds_read_b128 v[212:215], v246 offset:26880\n" \
"global_load_lds_dwordx4 v253, s[50:51] offset:1024\n" \
"v_mfma_f32_16x16x32_bf16 v[60:63], a[244:247], a[180:183], v[60:63]\n" \
"s_add_u32 s50, s50, 0x120000\n" \
"s_addc_u32 s51, s51, 0\n" \
"ds_read_b128 v[216:219], v246 offset:28928\n" \
"v_mfma_f32_16x16x32_bf16 v[60:63], a[248:251], a[184:187], v[60:63]\n" \
"ds_read_b128 v[220:223], v246 offset:30976\n" \
"v_cvt_pk_bf16_f32 v112, v112, v113\n" \
"v_mfma_f32_16x16x32_bf16 v[60:63], a[252:255], a[188:191], v[60:63]\n" \
"global_load_dword v85, v84, s[22:23]\n" \
"v_mfma_f32_16x16x32_bf16 v[52:55], a[248:251], a[168:171], v[232:235]\n" \
"v_exp_f32 v115, v115\n" \
"v_mfma_f32_16x16x32_bf16 v[52:55], a[252:255], a[172:175], v[52:55]\n" \
"v_exp_f32 v116, v116\n" \
"v_cvt_pk_bf16_f32 v113, v114, v115\n" \
"v_mfma_f32_16x16x32_bf16 v[52:55], a[240:243], a[160:163], v[52:55]\n" \
"v_exp_f32 v117, v117\n" \
"v_mfma_f32_16x16x32_bf16 v[52:55], a[244:247], a[164:167], v[52:55]\n" \
"s_waitcnt lgkmcnt(0)\n" \
"ds_read_b128 a[240:243], v247 offset:38912\n" \
"v_exp_f32 v118, v118\n" \
"v_mfma_f32_16x16x32_bf16 v[40:43], v[192:195], a[176:179], v[236:239]\n" \
"ds_read_b128 a[244:247], v247 offset:47104\n" \
"v_cvt_pk_bf16_f32 v114, v116, v117\n" \
"v_mfma_f32_16x16x32_bf16 v[40:43], v[196:199], a[180:183], v[40:43]\n" \
"ds_read_b128 a[248:251], v247 offset:39936\n" \
"v_exp_f32 v119, v119\n" \
"v_exp_f32 v120, v120\n" \
"v_mfma_f32_16x16x32_bf16 v[40:43], v[200:203], a[184:187], v[40:43]\n" \
"ds_read_b128 a[252:255], v247 offset:48128\n" \
"v_cvt_pk_bf16_f32 v115, v118, v119\n" \
"v_mfma_f32_16x16x32_bf16 v[40:43], v[204:207], a[188:191], v[40:43]\n" \
"v_exp_f32 v121, v121\n" \
"v_exp_f32 v122, v122\n" \
"v_cvt_pk_bf16_f32 v120, v120, v121\n" \
"v_mfma_f32_16x16x32_bf16 v[44:47], v[216:219], a[184:187], v[236:239]\n" \
"v_exp_f32 v123, v123\n" \
"v_mfma_f32_16x16x32_bf16 v[44:47], v[220:223], a[188:191], v[44:47]\n" \
"s_waitcnt vmcnt(12) lgkmcnt(0)\n" \
"v_exp_f32 v124, v124\n" \
"v_mfma_f32_16x16x32_bf16 v[44:47], v[208:211], a[176:179], v[44:47]\n" \
"v_add_u32 v247, 0x10000, v248\n" \
"v_cvt_pk_bf16_f32 v121, v122, v123\n" \
"v_mfma_f32_16x16x32_bf16 v[44:47], v[212:215], a[180:183], v[44:47]\n" \
"s_barrier\n" \
"v_exp_f32 v125, v125\n" \
"v_exp_f32 v126, v126\n" \
"v_cvt_pk_bf16_f32 v122, v124, v125\n" \
"v_mfma_f32_16x16x32_bf16 a[64:67], a[192:195], v[64:67], a[64:67]\n" \
"ds_read_b128 v[192:195], v247 offset:16384\n" \
"v_exp_f32 v127, v127\n" \
"v_mfma_f32_16x16x32_bf16 a[64:67], a[196:199], v[96:99], a[64:67]\n" \
"v_cvt_pk_bf16_f32 v123, v126, v127\n" \
"v_mfma_f32_16x16x32_bf16 a[96:99], a[192:195], v[72:75], a[96:99]\n" \
"s_lshl_b32 s70, s61, 14\n" \
"v_add_u32 v124, s70, v248\n" \
"v_xor_b32 v125, 0x4000, v124\n" \
"v_mfma_f32_16x16x32_bf16 a[96:99], a[196:199], v[104:107], a[96:99]\n" \
"s_lshl_b32 s70, s61, 9\n" \
"v_cmp_gt_i32 vcc, 0, v251\n" \
"v_cmp_gt_i32 s[64:65], 1, v251\n" \
"v_cmp_gt_i32 s[66:67], 2, v251\n" \
"v_cndmask_b32 v8, v8, v254, vcc\n" \
"v_mfma_f32_16x16x32_bf16 v[238:241], a[0:3], v[96:99], v[238:241]\n" \
"v_cndmask_b32 v16, v16, v254, vcc\n" \
"v_cmp_gt_i32 vcc, 3, v251\n" \
"v_mfma_f32_16x16x32_bf16 v[238:241], a[4:7], v[104:107], v[238:241]\n" \
"v_cndmask_b32 v9, v9, v254, s[64:65]\n" \
"v_cndmask_b32 v17, v17, v254, s[64:65]\n" \
"v_mfma_f32_16x16x32_bf16 v[128:131], a[192:195], v[80:83], v[128:131]\n" \
"s_add_u32 s70, s70, 110592\n" \
"ds_read_b128 v[196:199], v247 offset:24576\n" \
"v_lshrrev_b32 v126, 1, v248\n" \
"v_mfma_f32_16x16x32_bf16 v[128:131], a[196:199], v[112:115], v[128:131]\n" \
"v_add_u32 v126, s70, v126\n" \
"v_xor_b32 v127, 0x200, v126\n" \
"v_cmp_gt_i32 s[64:65], 16, v251\n" \
"v_cndmask_b32 v10, v10, v254, s[66:67]\n" \
"v_cndmask_b32 v18, v18, v254, s[66:67]\n" \
"v_cmp_gt_i32 s[66:67], 17, v251\n" \
"v_cndmask_b32 v11, v11, v254, vcc\n" \
"v_cndmask_b32 v19, v19, v254, vcc\n" \
"v_mfma_f32_16x16x32_bf16 v[160:163], a[192:195], v[88:91], v[160:163]\n" \
"v_lshrrev_b32 v71, 7, v248\n" \
"v_and_b32 v76, 0x70, v248\n" \
"v_mfma_f32_16x16x32_bf16 v[160:163], a[196:199], v[120:123], v[160:163]\n" \
"ds_read_b128 a[192:195], v247 offset:0\n" \
"v_lshlrev_b32 v77, 4, v71\n" \
"v_cmp_gt_i32 vcc, 18, v251\n" \
"v_cndmask_b32 v12, v12, v254, s[64:65]\n" \
"v_mfma_f32_16x16x32_bf16 v[242:245], a[0:3], v[112:115], v[242:245]\n" \
"v_cndmask_b32 v20, v20, v254, s[64:65]\n" \
"v_cmp_gt_i32 s[64:65], 19, v251\n" \
"v_mfma_f32_16x16x32_bf16 v[242:245], a[4:7], v[120:123], v[242:245]\n" \
"v_cndmask_b32 v13, v13, v254, s[66:67]\n" \
"v_cndmask_b32 v21, v21, v254, s[66:67]\n" \
"v_cmp_gt_i32 s[66:67], 32, v251\n" \
"v_cndmask_b32 v14, v14, v254, vcc\n" \
"v_mfma_f32_16x16x32_bf16 v[164:167], a[200:203], v[88:91], v[164:167]\n" \
"ds_read_b128 v[200:203], v247 offset:17408\n" \
"v_xor_b32 v77, v77, v76\n" \
"v_mfma_f32_16x16x32_bf16 v[164:167], a[204:207], v[120:123], v[164:167]\n" \
"ds_write_b128 v124, a[64:67] offset:0\n" \
"v_lshl_add_u32 v252, v71, 12, v77\n" \
"v_cndmask_b32 v22, v22, v254, vcc\n" \
"v_cmp_gt_i32 vcc, 33, v251\n" \
"v_cndmask_b32 v15, v15, v254, s[64:65]\n" \
"v_cndmask_b32 v23, v23, v254, s[64:65]\n" \
"v_cmp_gt_i32 s[64:65], 34, v251\n" \
"v_cndmask_b32 v40, v40, v254, s[66:67]\n" \
"v_cndmask_b32 v48, v48, v254, s[66:67]\n" \
"v_cmp_gt_i32 s[66:67], 35, v251\n" \
"v_cndmask_b32 v41, v41, v254, vcc\n" \
"v_cndmask_b32 v49, v49, v254, vcc\n" \
"v_mfma_f32_16x16x32_bf16 v[132:135], a[200:203], v[80:83], v[132:135]\n" \
"v_and_or_b32 v71, v71, 3, s86\n" \
"v_lshlrev_b32 v77, 4, v71\n" \
"v_mfma_f32_16x16x32_bf16 v[132:135], a[204:207], v[112:115], v[132:135]\n" \
"ds_read_b128 a[196:199], v247 offset:8192\n" \
"v_xor_b32 v77, v77, v76\n" \
"v_cmp_gt_i32 vcc, 48, v251\n" \
"v_cndmask_b32 v42, v42, v254, s[64:65]\n" \
"v_mfma_f32_16x16x32_bf16 a[100:103], a[200:203], v[72:75], a[100:103]\n" \
"ds_read_b128 v[204:207], v247 offset:25600\n" \
"v_lshl_add_u32 v246, v71, 12, v77\n" \
"v_mfma_f32_16x16x32_bf16 a[100:103], a[204:207], v[104:107], a[100:103]\n" \
"ds_write_b128 v124, a[96:99] offset:8192\n" \
"v_cndmask_b32 v50, v50, v254, s[64:65]\n" \
"v_mfma_f32_16x16x32_bf16 a[68:71], a[200:203], v[64:67], a[68:71]\n" \
"v_lshrrev_b32 v71, 9, v248\n" \
"v_lshl_add_u32 v246, v71, 30, v246\n" \
"v_mfma_f32_16x16x32_bf16 a[68:71], a[204:207], v[96:99], a[68:71]\n" \
"s_add_u32 m0, s95, 118784\n" \
"ds_read_b128 a[200:203], v247 offset:1024\n" \
"v_cmp_gt_i32 s[64:65], 49, v251\n" \
"v_mfma_f32_16x16x32_bf16 a[72:75], a[208:211], v[64:67], a[72:75]\n" \
"ds_read_b128 v[208:211], v247 offset:18432\n" \
"global_load_lds_dwordx4 v252, s[42:43] offset:0\n" \
"v_mfma_f32_16x16x32_bf16 a[72:75], a[212:215], v[96:99], a[72:75]\n" \
"ds_write_b128 v124, a[100:103] offset:9216\n" \
"global_load_lds_dwordx4 v252, s[42:43] offset:2048\n" \
"v_mfma_f32_16x16x32_bf16 a[104:107], a[208:211], v[72:75], a[104:107]\n" \
"v_cndmask_b32 v43, v43, v254, s[66:67]\n" \
"v_cndmask_b32 v51, v51, v254, s[66:67]\n" \
"v_mfma_f32_16x16x32_bf16 a[104:107], a[212:215], v[104:107], a[104:107]\n" \
"ds_read_b128 a[204:207], v247 offset:9216\n" \
"ds_write_b64 v126, v[240:241]\n" \
"v_mfma_f32_16x16x32_bf16 v[136:139], a[208:211], v[80:83], v[136:139]\n" \
"s_add_u32 s42, s42, 0x180\n" \
"s_addc_u32 s43, s43, 0\n" \
"ds_read_b128 v[212:215], v247 offset:26624\n" \
"v_mfma_f32_16x16x32_bf16 v[136:139], a[212:215], v[112:115], v[136:139]\n" \
"ds_write_b128 v124, a[68:71] offset:1024\n" \
"global_load_lds_dwordx4 v252, s[44:45] offset:1024\n" \
"v_mfma_f32_16x16x32_bf16 v[168:171], a[208:211], v[88:91], v[168:171]\n" \
"v_cmp_gt_i32 s[66:67], 50, v251\n" \
"v_cndmask_b32 v44, v44, v254, vcc\n" \
"v_mfma_f32_16x16x32_bf16 v[168:171], a[212:215], v[120:123], v[168:171]\n" \
"ds_read_b128 a[208:211], v247 offset:2048\n" \
"v_cndmask_b32 v52, v52, v254, vcc\n" \
"v_mfma_f32_16x16x32_bf16 v[172:175], a[216:219], v[88:91], v[172:175]\n" \
"ds_read_b128 v[216:219], v247 offset:19456\n" \
"v_cmp_gt_i32 vcc, 51, v251\n" \
"v_mfma_f32_16x16x32_bf16 v[172:175], a[220:223], v[120:123], v[172:175]\n" \
"ds_write_b128 v124, a[72:75] offset:2048\n" \
"global_load_lds_dwordx4 v252, s[44:45] offset:3072\n" \
"v_mfma_f32_16x16x32_bf16 v[140:143], a[216:219], v[80:83], v[140:143]\n" \
"s_add_u32 s44, s44, 0x180\n" \
"s_addc_u32 s45, s45, 0\n" \
"v_cndmask_b32 v45, v45, v254, s[64:65]\n" \
"v_mfma_f32_16x16x32_bf16 v[140:143], a[220:223], v[112:115], v[140:143]\n" \
"s_add_u32 m0, s85, 135168\n" \
"ds_read_b128 a[212:215], v247 offset:10240\n" \
"v_cndmask_b32 v53, v53, v254, s[64:65]\n" \
"v_mfma_f32_16x16x32_bf16 a[108:111], a[216:219], v[72:75], a[108:111]\n" \
"ds_read_b128 v[220:223], v247 offset:27648\n" \
"v_cndmask_b32 v46, v46, v254, s[66:67]\n" \
"v_mfma_f32_16x16x32_bf16 a[108:111], a[220:223], v[104:107], a[108:111]\n" \
"ds_write_b128 v124, a[104:107] offset:10240\n" \
"v_cndmask_b32 v54, v54, v254, s[66:67]\n" \
"v_mfma_f32_16x16x32_bf16 a[76:79], a[216:219], v[64:67], a[76:79]\n" \
"buffer_load_dwordx4 v246, s[12:15], s46 offen lds\n" \
"v_mfma_f32_16x16x32_bf16 a[76:79], a[220:223], v[96:99], a[76:79]\n" \
"s_add_u32 s46, s46, 0x180\n" \
"ds_read_b128 a[216:219], v247 offset:3072\n" \
"v_cndmask_b32 v47, v47, v254, vcc\n" \
"v_mfma_f32_16x16x32_bf16 a[80:83], a[224:227], v[64:67], a[80:83]\n" \
"ds_read_b128 v[224:227], v247 offset:20480\n" \
"v_cndmask_b32 v55, v55, v254, vcc\n" \
"v_mfma_f32_16x16x32_bf16 a[80:83], a[228:231], v[96:99], a[80:83]\n" \
"ds_write_b128 v124, a[108:111] offset:11264\n" \
"v_exp_f32 v8, v8\n" \
"v_exp_f32 v9, v9\n" \
"v_exp_f32 v10, v10\n" \
"v_cvt_pk_bf16_f32 v8, v8, v9\n" \
"v_mfma_f32_16x16x32_bf16 a[112:115], a[224:227], v[72:75], a[112:115]\n" \
"v_exp_f32 v11, v11\n" \
"v_mfma_f32_16x16x32_bf16 a[112:115], a[228:231], v[104:107], a[112:115]\n" \
"ds_read_b128 a[220:223], v247 offset:11264\n" \
"v_exp_f32 v12, v12\n" \
"v_mfma_f32_16x16x32_bf16 v[144:147], a[224:227], v[80:83], v[144:147]\n" \
"ds_read_b128 v[228:231], v247 offset:28672\n" \
"v_cvt_pk_bf16_f32 v9, v10, v11\n" \
"v_mfma_f32_16x16x32_bf16 v[144:147], a[228:231], v[112:115], v[144:147]\n" \
"ds_write_b128 v124, a[76:79] offset:3072\n" \
"v_exp_f32 v13, v13\n" \
"v_exp_f32 v14, v14\n" \
"v_cvt_pk_bf16_f32 v10, v12, v13\n" \
"v_mfma_f32_16x16x32_bf16 v[176:179], a[224:227], v[88:91], v[176:179]\n" \
"v_exp_f32 v15, v15\n" \
"v_mfma_f32_16x16x32_bf16 v[176:179], a[228:231], v[120:123], v[176:179]\n" \
"ds_read_b128 a[224:227], v247 offset:4096\n" \
"v_exp_f32 v40, v40\n" \
"v_mfma_f32_16x16x32_bf16 v[180:183], a[232:235], v[88:91], v[180:183]\n" \
"ds_read_b128 v[232:235], v247 offset:21504\n" \
"v_cvt_pk_bf16_f32 v11, v14, v15\n" \
"v_mfma_f32_16x16x32_bf16 v[180:183], a[236:239], v[120:123], v[180:183]\n" \
"ds_write_b128 v124, a[80:83] offset:4096\n" \
"v_exp_f32 v41, v41\n" \
"v_exp_f32 v42, v42\n" \
"v_cvt_pk_bf16_f32 v40, v40, v41\n" \
"v_mfma_f32_16x16x32_bf16 v[148:151], a[232:235], v[80:83], v[148:151]\n" \
"v_exp_f32 v43, v43\n" \
"v_mfma_f32_16x16x32_bf16 v[148:151], a[236:239], v[112:115], v[148:151]\n" \
"ds_read_b128 a[228:231], v247 offset:12288\n" \
"v_exp_f32 v44, v44\n" \
"v_mfma_f32_16x16x32_bf16 a[116:119], a[232:235], v[72:75], a[116:119]\n" \
"ds_read_b128 v[236:239], v247 offset:29696\n" \
"v_cvt_pk_bf16_f32 v41, v42, v43\n" \
"v_mfma_f32_16x16x32_bf16 a[116:119], a[236:239], v[104:107], a[116:119]\n" \
"ds_write_b128 v124, a[112:115] offset:12288\n" \
"v_exp_f32 v45, v45\n" \
"v_exp_f32 v46, v46\n" \
"v_cvt_pk_bf16_f32 v42, v44, v45\n" \
"v_mfma_f32_16x16x32_bf16 a[84:87], a[232:235], v[64:67], a[84:87]\n" \
"v_exp_f32 v47, v47\n" \
"v_mfma_f32_16x16x32_bf16 a[84:87], a[236:239], v[96:99], a[84:87]\n" \
"ds_read_b128 a[232:235], v247 offset:5120\n" \
"v_exp_f32 v24, v24\n" \
"v_mfma_f32_16x16x32_bf16 a[88:91], a[240:243], v[64:67], a[88:91]\n" \
"ds_read_b128 v[0:3], v247 offset:22528\n" \
"v_cvt_pk_bf16_f32 v43, v46, v47\n" \
"v_mfma_f32_16x16x32_bf16 a[88:91], a[244:247], v[96:99], a[88:91]\n" \
"ds_write_b128 v124, a[116:119] offset:13312\n" \
"v_exp_f32 v25, v25\n" \
"v_exp_f32 v26, v26\n" \
"v_cvt_pk_bf16_f32 v24, v24, v25\n" \
"v_mfma_f32_16x16x32_bf16 v[242:245], a[4:7], v[8:11], v[242:245]\n" \
"v_exp_f32 v27, v27\n" \
"v_mfma_f32_16x16x32_bf16 v[242:245], a[4:7], v[40:43], v[242:245]\n" \
"v_exp_f32 v28, v28\n" \
"v_cvt_pk_bf16_f32 v25, v26, v27\n" \
"v_mfma_f32_16x16x32_bf16 a[120:123], a[240:243], v[72:75], a[120:123]\n" \
"v_exp_f32 v29, v29\n" \
"v_mfma_f32_16x16x32_bf16 a[120:123], a[244:247], v[104:107], a[120:123]\n" \
"ds_read_b128 a[236:239], v247 offset:13312\n" \
"v_exp_f32 v30, v30\n" \
"v_mfma_f32_16x16x32_bf16 v[152:155], a[240:243], v[80:83], v[152:155]\n" \
"ds_read_b128 v[4:7], v247 offset:30720\n" \
"v_cvt_pk_bf16_f32 v26, v28, v29\n" \
"v_mfma_f32_16x16x32_bf16 v[152:155], a[244:247], v[112:115], v[152:155]\n" \
"ds_write_b128 v124, a[84:87] offset:5120\n" \
"v_exp_f32 v31, v31\n" \
"v_exp_f32 v56, v56\n" \
"v_cvt_pk_bf16_f32 v27, v30, v31\n" \
"v_mfma_f32_16x16x32_bf16 v[184:187], a[240:243], v[88:91], v[184:187]\n" \
"v_exp_f32 v57, v57\n" \
"v_mfma_f32_16x16x32_bf16 v[184:187], a[244:247], v[120:123], v[184:187]\n" \
"ds_read_b128 a[240:243], v247 offset:6144\n" \
"v_exp_f32 v58, v58\n" \
"v_mfma_f32_16x16x32_bf16 a[92:95], a[248:251], v[64:67], a[92:95]\n" \
"ds_read_b128 v[32:35], v247 offset:23552\n" \
"v_cvt_pk_bf16_f32 v56, v56, v57\n" \
"v_mfma_f32_16x16x32_bf16 a[92:95], a[252:255], v[96:99], a[92:95]\n" \
"ds_write_b128 v124, a[88:91] offset:6144\n" \
"v_exp_f32 v59, v59\n" \
"v_exp_f32 v60, v60\n" \
"v_cvt_pk_bf16_f32 v57, v58, v59\n" \
"v_mfma_f32_16x16x32_bf16 a[124:127], a[248:251], v[72:75], a[124:127]\n" \
"v_exp_f32 v61, v61\n" \
"v_mfma_f32_16x16x32_bf16 a[124:127], a[252:255], v[104:107], a[124:127]\n" \
"ds_read_b128 a[244:247], v247 offset:14336\n" \
"v_exp_f32 v62, v62\n" \
"v_mfma_f32_16x16x32_bf16 v[156:159], a[248:251], v[80:83], v[156:159]\n" \
"ds_read_b128 v[36:39], v247 offset:31744\n" \
"v_cvt_pk_bf16_f32 v58, v60, v61\n" \
"v_mfma_f32_16x16x32_bf16 v[156:159], a[252:255], v[112:115], v[156:159]\n" \
"ds_write_b128 v124, a[120:123] offset:14336\n" \
"v_exp_f32 v63, v63\n" \
"v_mfma_f32_16x16x32_bf16 v[188:191], a[248:251], v[88:91], v[188:191]\n" \
"v_exp_f32 v16, v16\n" \
"v_mfma_f32_16x16x32_bf16 v[188:191], a[252:255], v[120:123], v[188:191]\n" \
"s_waitcnt lgkmcnt(3)\n" \
"ds_read_b128 a[248:251], v247 offset:7168\n" \
"ds_write_b128 v124, a[92:95] offset:7168\n" \
"v_cvt_pk_bf16_f32 v59, v62, v63\n" \
"v_exp_f32 v17, v17\n" \
"v_mfma_f32_16x16x32_bf16 v[160:163], v[192:195], v[8:11], v[160:163]\n" \
"v_exp_f32 v18, v18\n" \
"v_mfma_f32_16x16x32_bf16 v[160:163], v[196:199], v[40:43], v[160:163]\n" \
"ds_read_b128 a[252:255], v247 offset:15360\n" \
"v_cvt_pk_bf16_f32 v16, v16, v17\n" \
"v_mfma_f32_16x16x32_bf16 v[242:245], a[4:7], v[24:27], v[242:245]\n" \
"v_exp_f32 v19, v19\n" \
"v_mfma_f32_16x16x32_bf16 v[242:245], a[4:7], v[56:59], v[242:245]\n" \
"v_exp_f32 v20, v20\n" \
"v_cvt_pk_bf16_f32 v17, v18, v19\n" \
"v_exp_f32 v21, v21\n" \
"v_mfma_f32_16x16x32_bf16 v[164:167], v[200:203], v[8:11], v[164:167]\n" \
"v_exp_f32 v22, v22\n" \
"v_mfma_f32_16x16x32_bf16 v[164:167], v[204:207], v[40:43], v[164:167]\n" \
"ds_write_b128 v124, a[124:127] offset:15360\n" \
"v_cvt_pk_bf16_f32 v18, v20, v21\n" \
"v_exp_f32 v23, v23\n" \
"v_mfma_f32_16x16x32_bf16 v[168:171], v[208:211], v[8:11], v[168:171]\n" \
"v_exp_f32 v48, v48\n" \
"v_mfma_f32_16x16x32_bf16 v[168:171], v[212:215], v[40:43], v[168:171]\n" \
"s_waitcnt vmcnt(0) lgkmcnt(0)\n" \
"v_cvt_pk_bf16_f32 v19, v22, v23\n" \
"v_exp_f32 v49, v49\n" \
"v_mfma_f32_16x16x32_bf16 v[172:175], v[216:219], v[8:11], v[172:175]\n" \
"v_exp_f32 v50, v50\n" \
"v_mfma_f32_16x16x32_bf16 v[172:175], v[220:223], v[40:43], v[172:175]\n" \
"s_barrier\n" \
"v_cvt_pk_bf16_f32 v48, v48, v49\n" \
"v_mfma_f32_16x16x32_bf16 v[242:245], a[0:3], v[16:19], v[242:245]\n" \
"v_exp_f32 v51, v51\n" \
"v_exp_f32 v52, v52\n" \
"v_mfma_f32_16x16x32_bf16 v[176:179], v[224:227], v[8:11], v[176:179]\n" \
"s_add_u32 m0, s84, 90112\n" \
"ds_read_b64 v[240:241], v127\n" \
"v_cvt_pk_bf16_f32 v49, v50, v51\n" \
"v_mfma_f32_16x16x32_bf16 v[176:179], v[228:231], v[40:43], v[176:179]\n" \
"ds_read_b128 v[64:67], v125 offset:0\n" \
"global_load_lds_dwordx4 v253, s[48:49] offset:-4096\n" \
"v_exp_f32 v53, v53\n" \
"v_exp_f32 v54, v54\n" \
"v_mfma_f32_16x16x32_bf16 v[180:183], v[232:235], v[8:11], v[180:183]\n" \
"ds_read_b128 v[68:71], v125 offset:1024\n" \
"global_load_lds_dwordx4 v253, s[48:49] offset:-2048\n" \
"v_mfma_f32_16x16x32_bf16 v[180:183], v[236:239], v[40:43], v[180:183]\n" \
"ds_read_b128 v[72:75], v125 offset:2048\n" \
"v_cvt_pk_bf16_f32 v50, v52, v53\n" \
"v_mfma_f32_16x16x32_bf16 v[184:187], v[0:3], v[8:11], v[184:187]\n" \
"ds_read_b128 v[76:79], v125 offset:3072\n" \
"global_load_lds_dwordx4 v253, s[48:49] offset:0\n" \
"v_mfma_f32_16x16x32_bf16 v[184:187], v[4:7], v[40:43], v[184:187]\n" \
"s_add_u32 s48, s48, 0x300000\n" \
"s_addc_u32 s49, s49, 0\n" \
"ds_read_b128 v[80:83], v125 offset:4096\n" \
"v_mfma_f32_16x16x32_bf16 v[188:191], v[32:35], v[8:11], v[188:191]\n" \
"ds_read_b128 v[84:87], v125 offset:5120\n" \
"global_load_lds_dwordx4 v253, s[50:51] offset:-3072\n" \
"v_mfma_f32_16x16x32_bf16 v[188:191], v[36:39], v[40:43], v[188:191]\n" \
"s_waitcnt lgkmcnt(5)\n" \
"ds_read_b128 v[88:91], v125 offset:6144\n" \
"v_exp_f32 v55, v55\n" \
"v_mfma_f32_16x16x32_bf16 v[160:163], a[192:195], v[24:27], v[160:163]\n" \
"ds_read_b128 v[92:95], v125 offset:7168\n" \
"global_load_lds_dwordx4 v253, s[50:51] offset:-1024\n" \
"v_mfma_f32_16x16x32_bf16 v[160:163], a[196:199], v[56:59], v[160:163]\n" \
"s_movk_i32 s89, 0xc0\n" \
"ds_read_b128 v[96:99], v125 offset:8192\n" \
"global_load_lds_dwordx4 v253, s[50:51] offset:1024\n" \
"v_mfma_f32_16x16x32_bf16 v[164:167], a[200:203], v[24:27], v[164:167]\n" \
"s_add_u32 s50, s50, 0x300000\n" \
"s_addc_u32 s51, s51, 0\n" \
"ds_read_b128 v[100:103], v125 offset:9216\n" \
"v_mfma_f32_16x16x32_bf16 v[164:167], a[204:207], v[56:59], v[164:167]\n" \
"s_add_u32 m0, s95, 65536\n" \
"s_movk_i32 s90, 288\n" \
"ds_read_b128 v[104:107], v125 offset:10240\n" \
"v_lshrrev_b32 v211, 4, v248\n" \
"v_lshrrev_b32 v214, 8, v248\n" \
"v_and_b32 v215, 7, v211\n" \
"v_xor_b32 v214, v214, v215\n" \
"v_pk_add_f32 v[128:129], v[128:129], v[64:65]\n" \
"v_pk_add_f32 v[130:131], v[130:131], v[66:67]\n" \
"s_waitcnt lgkmcnt(9)\n" \
"v_pk_add_f32 v[132:133], v[132:133], v[68:69]\n" \
"v_pk_add_f32 v[134:135], v[134:135], v[70:71]\n" \
"s_waitcnt lgkmcnt(8)\n" \
"v_pk_add_f32 v[136:137], v[136:137], v[72:73]\n" \
"v_pk_add_f32 v[138:139], v[138:139], v[74:75]\n" \
"s_waitcnt lgkmcnt(7)\n" \
"v_pk_add_f32 v[140:141], v[140:141], v[76:77]\n" \
"v_pk_add_f32 v[142:143], v[142:143], v[78:79]\n" \
"s_waitcnt lgkmcnt(6)\n" \
"v_pk_add_f32 v[144:145], v[144:145], v[80:81]\n" \
"v_pk_add_f32 v[146:147], v[146:147], v[82:83]\n" \
"s_waitcnt lgkmcnt(5)\n" \
"v_pk_add_f32 v[148:149], v[148:149], v[84:85]\n" \
"v_pk_add_f32 v[150:151], v[150:151], v[86:87]\n" \
"s_waitcnt lgkmcnt(4)\n" \
"v_pk_add_f32 v[152:153], v[152:153], v[88:89]\n" \
"v_pk_add_f32 v[154:155], v[154:155], v[90:91]\n" \
"s_waitcnt lgkmcnt(3)\n" \
"v_pk_add_f32 v[156:157], v[156:157], v[92:93]\n" \
"v_pk_add_f32 v[158:159], v[158:159], v[94:95]\n" \
"v_mfma_f32_16x16x32_bf16 v[168:171], a[208:211], v[24:27], v[168:171]\n" \
"s_lshr_b32 s71, s61, 1\n" \
"ds_read_b128 v[108:111], v125 offset:11264\n" \
"global_load_lds_dwordx4 v252, s[42:43] offset:0\n" \
"v_mfma_f32_16x16x32_bf16 v[168:171], a[212:215], v[56:59], v[168:171]\n" \
"s_lshl_b32 s70, s71, 11\n" \
"ds_read_b128 v[112:115], v125 offset:12288\n" \
"v_cvt_pk_bf16_f32 v51, v54, v55\n" \
"v_mfma_f32_16x16x32_bf16 v[172:175], a[216:219], v[24:27], v[172:175]\n" \
"s_waitcnt lgkmcnt(4)\n" \
"s_add_u32 s70, s70, 118784\n" \
"ds_read_b128 v[116:119], v125 offset:13312\n" \
"global_load_lds_dwordx4 v252, s[42:43] offset:2048\n" \
"v_mfma_f32_16x16x32_bf16 v[242:245], a[0:3], v[48:51], v[242:245]\n" \
"v_and_b32 v216, 15, v211\n" \
"v_lshlrev_b32 v213, 7, v216\n" \
"v_lshl_add_u32 v213, v214, 4, v213\n" \
"v_mfma_f32_16x16x32_bf16 v[172:175], a[220:223], v[56:59], v[172:175]\n" \
"s_add_u32 s42, s42, 0x400\n" \
"s_addc_u32 s43, s43, 0\n" \
"ds_read_b128 v[120:123], v125 offset:14336\n" \
"v_add_u32 v205, s70, v213\n" \
"v_xor_b32 v206, 64, v205\n" \
"v_lshrrev_b32 v216, 2, v216\n" \
"v_lshl_add_u32 v216, v216, 9, v213\n" \
"v_add_u32 v207, 135168, v216\n" \
"v_xor_b32 v208, 64, v207\n" \
"v_pk_add_f32 v[160:161], v[160:161], v[96:97]\n" \
"v_pk_add_f32 v[162:163], v[162:163], v[98:99]\n" \
"v_mfma_f32_16x16x32_bf16 v[176:179], a[224:227], v[24:27], v[176:179]\n" \
"s_and_b32 s70, s61, 1\n" \
"ds_read_b128 v[124:127], v125 offset:15360\n" \
"s_lshl_b32 s70, s70, 9\n" \
"v_mfma_f32_16x16x32_bf16 v[176:179], a[228:231], v[56:59], v[176:179]\n" \
"s_waitcnt lgkmcnt(6)\n" \
"global_load_lds_dwordx4 v252, s[44:45] offset:1024\n" \
"global_load_lds_dwordx4 v252, s[44:45] offset:3072\n" \
"v_mfma_f32_16x16x32_bf16 v[180:183], a[232:235], v[24:27], v[180:183]\n" \
"s_add_u32 s70, s70, 139264\n" \
"s_add_u32 s44, s44, 0x400\n" \
"s_addc_u32 s45, s45, 0\n" \
"v_mfma_f32_16x16x32_bf16 v[180:183], a[236:239], v[56:59], v[180:183]\n" \
"s_waitcnt lgkmcnt(0)\n" \
"s_lshl_b32 s71, s71, 5\n" \
"v_and_b32 v213, 3, v211\n" \
"v_lshlrev_b32 v213, 3, v213\n" \
"v_mfma_f32_16x16x32_bf16 v[184:187], a[240:243], v[24:27], v[184:187]\n" \
"s_add_u32 m0, s85, 81920\n" \
"v_and_or_b32 v213, v248, s89, v213\n" \
"v_bfe_u32 v214, v211, 4, 1\n" \
"v_mfma_f32_16x16x32_bf16 v[184:187], a[244:247], v[56:59], v[184:187]\n" \
"s_barrier\n" \
"v_mad_u32_u24 v213, v214, s90, v213\n" \
"v_lshrrev_b32 v214, 5, v211\n" \
"v_mul_u32_u24 v214, 6144, v214\n" \
"v_add3_u32 v213, v213, v214, s70\n" \
"v_pk_add_f32 v[164:165], v[164:165], v[100:101]\n" \
"v_pk_add_f32 v[166:167], v[166:167], v[102:103]\n" \
"v_pk_add_f32 v[168:169], v[168:169], v[104:105]\n" \
"v_pk_add_f32 v[170:171], v[170:171], v[106:107]\n" \
"v_pk_add_f32 v[172:173], v[172:173], v[108:109]\n" \
"v_pk_add_f32 v[174:175], v[174:175], v[110:111]\n" \
"v_mfma_f32_16x16x32_bf16 v[188:191], a[248:251], v[24:27], v[188:191]\n" \
"v_xor_b32 v209, s71, v213\n" \
"v_xor_b32 v210, 32, v209\n" \
"v_mfma_f32_16x16x32_bf16 v[188:191], a[252:255], v[56:59], v[188:191]\n" \
"ds_read_b128 a[64:67], v207 offset:0\n" \
"Lpg8622_%=:\n" \
"Lst1x2_%=:\n" \
"s_cbranch_execz Lpg10884_%=\n" \
"ds_read_b64_tr_b16 a[96:97], v209 offset:2048\n" \
"v_mfma_f32_16x16x32_bf16 v[128:131], a[192:195], v[16:19], v[128:131]\n" \
"ds_read_b64_tr_b16 a[98:99], v209 offset:3072\n" \
"v_mfma_f32_16x16x32_bf16 v[128:131], a[196:199], v[48:51], v[128:131]\n" \
"ds_read_b128 a[68:71], v208 offset:0\n" \
"v_mfma_f32_16x16x32_bf16 v[132:135], a[200:203], v[16:19], v[132:135]\n" \
"ds_read_b64_tr_b16 a[100:101], v209 offset:14336\n" \
"ds_read_b64_tr_b16 a[102:103], v209 offset:15360\n" \
"buffer_load_dwordx4 v246, s[12:15], s46 offen lds\n" \
"v_pk_add_f32 v[176:177], v[176:177], v[112:113]\n" \
"v_pk_add_f32 v[178:179], v[178:179], v[114:115]\n" \
"v_pk_add_f32 v[180:181], v[180:181], v[116:117]\n" \
"v_pk_add_f32 v[182:183], v[182:183], v[118:119]\n" \
"v_pk_add_f32 v[184:185], v[184:185], v[120:121]\n" \
"v_pk_add_f32 v[186:187], v[186:187], v[122:123]\n" \
"v_pk_add_f32 v[188:189], v[188:189], v[124:125]\n" \
"v_pk_add_f32 v[190:191], v[190:191], v[126:127]\n" \
"v_mfma_f32_16x16x32_bf16 v[132:135], a[204:207], v[48:51], v[132:135]\n" \
"ds_read_b128 a[72:75], v205 offset:0\n" \
"s_add_u32 s46, s46, 0x400\n" \
"v_mfma_f32_16x16x32_bf16 v[136:139], a[208:211], v[16:19], v[136:139]\n" \
"s_add_u32 m0, s95, 0\n" \
"ds_read_b64_tr_b16 a[104:105], v209 offset:0\n" \
"v_mfma_f32_16x16x32_bf16 v[136:139], a[212:215], v[48:51], v[136:139]\n" \
"ds_read_b128 a[76:79], v206 offset:0\n" \
"ds_read_b64_tr_b16 a[106:107], v209 offset:1024\n" \
"v_mfma_f32_16x16x32_bf16 v[140:143], a[216:219], v[16:19], v[140:143]\n" \
"ds_read_b64_tr_b16 a[108:109], v209 offset:12288\n" \
"global_load_lds_dwordx4 v252, s[42:43] offset:0\n" \
"v_mfma_f32_16x16x32_bf16 v[140:143], a[220:223], v[48:51], v[140:143]\n" \
"ds_read_b64_tr_b16 a[110:111], v209 offset:13312\n" \
"global_load_lds_dwordx4 v252, s[42:43] offset:2048\n" \
"v_mfma_f32_16x16x32_bf16 v[144:147], a[224:227], v[16:19], v[144:147]\n" \
"ds_read_b128 a[80:83], v205 offset:4096\n" \
"ds_read_b64_tr_b16 a[112:113], v210 offset:0\n" \
"v_mfma_f32_16x16x32_bf16 v[144:147], a[228:231], v[48:51], v[144:147]\n" \
"s_add_u32 s42, s42, 0xfffffb00\n" \
"s_addc_u32 s43, s43, -1\n" \
"ds_read_b64_tr_b16 a[114:115], v210 offset:1024\n" \
"v_mfma_f32_16x16x32_bf16 v[148:151], a[232:235], v[16:19], v[148:151]\n" \
"ds_read_b64_tr_b16 a[116:117], v210 offset:12288\n" \
"global_load_lds_dwordx4 v252, s[44:45] offset:1024\n" \
"v_mfma_f32_16x16x32_bf16 v[148:151], a[236:239], v[48:51], v[148:151]\n" \
"ds_read_b128 a[84:87], v206 offset:4096\n" \
"ds_read_b64_tr_b16 a[118:119], v210 offset:13312\n" \
"v_mfma_f32_16x16x32_bf16 v[152:155], a[240:243], v[16:19], v[152:155]\n" \
"ds_read_b128 a[88:91], v205 offset:8192\n" \
"global_load_lds_dwordx4 v252, s[44:45] offset:3072\n" \
"v_mfma_f32_16x16x32_bf16 v[152:155], a[244:247], v[48:51], v[152:155]\n" \
"s_add_u32 s44, s44, 0xfffffb00\n" \
"s_addc_u32 s45, s45, -1\n" \
"ds_read_b128 a[92:95], v206 offset:8192\n" \
"v_mfma_f32_16x16x32_bf16 v[156:159], a[248:251], v[16:19], v[156:159]\n" \
"s_add_u32 m0, s85, 16384\n" \
"v_add_f32 v244, v244, v240\n" \
"v_add_f32 v245, v245, v241\n" \
"v_mfma_f32_16x16x32_bf16 v[156:159], a[252:255], v[48:51], v[156:159]\n" \
"s_waitcnt lgkmcnt(4)\n" \
"buffer_load_dwordx4 v246, s[12:15], s46 offen lds\n" \
"Ljoin_%=:\n" \
"v_mfma_f32_16x16x32_bf16 v[32:35], a[96:99], a[64:67], 0\n" \
"s_add_u32 s46, s46, 0xfffffb00\n" \
"ds_read_b128 a[192:195], v205 offset:12288\n" \
"s_add_u32 m0, s84, 24576\n" \
"v_mfma_f32_16x16x32_bf16 v[32:35], a[100:103], a[68:71], v[32:35]\n" \
"global_load_lds_dwordx4 v253, s[48:49] offset:-4096\n" \
"v_mfma_f32_16x16x32_bf16 v[40:43], a[96:99], a[72:75], 0\n" \
"ds_read_b128 a[196:199], v206 offset:12288\n" \
"v_mfma_f32_16x16x32_bf16 v[40:43], a[100:103], a[76:79], v[40:43]\n" \
"ds_read_b64_tr_b16 a[208:209], v209 offset:4096\n" \
"v_mov_b32 v232, v244\n" \
"v_mfma_f32_16x16x32_bf16 v[0:3], a[104:107], a[72:75], 0\n" \
"ds_read_b64_tr_b16 a[210:211], v209 offset:5120\n" \
"global_load_lds_dwordx4 v253, s[48:49] offset:-2048\n" \
"v_mfma_f32_16x16x32_bf16 v[0:3], a[108:111], a[76:79], v[0:3]\n" \
"s_waitcnt lgkmcnt(3)\n" \
"v_permlane16_swap_b32 v232, v244\n" \
"v_mfma_f32_16x16x32_bf16 v[4:7], a[112:115], a[72:75], 0\n" \
"ds_read_b64_tr_b16 a[212:213], v209 offset:16384\n" \
"v_add_f32 v244, v244, v232\n" \
"v_mfma_f32_16x16x32_bf16 v[4:7], a[116:119], a[76:79], v[4:7]\n" \
"ds_read_b64_tr_b16 a[214:215], v209 offset:17408\n" \
"global_load_lds_dwordx4 v253, s[48:49] offset:0\n" \
"v_mfma_f32_16x16x32_bf16 v[12:15], a[112:115], a[80:83], 0\n" \
"s_add_u32 s48, s48, 0xffc40000\n" \
"s_addc_u32 s49, s49, -1\n" \
"ds_read_b64_tr_b16 a[216:217], v210 offset:4096\n" \
"v_mfma_f32_16x16x32_bf16 v[12:15], a[116:119], a[84:87], v[12:15]\n" \
"v_mov_b32 v232, v244\n" \
"v_mfma_f32_16x16x32_bf16 v[8:11], a[104:107], a[80:83], 0\n" \
"ds_read_b64_tr_b16 a[218:219], v210 offset:5120\n" \
"global_load_lds_dwordx4 v253, s[50:51] offset:-3072\n" \
"v_mfma_f32_16x16x32_bf16 v[8:11], a[108:111], a[84:87], v[8:11]\n" \
"ds_read_b64_tr_b16 a[220:221], v210 offset:16384\n" \
"v_mfma_f32_16x16x32_bf16 v[48:51], a[96:99], a[80:83], 0\n" \
"ds_read_b64_tr_b16 a[222:223], v210 offset:17408\n" \
"v_mfma_f32_16x16x32_bf16 v[48:51], a[100:103], a[84:87], v[48:51]\n" \
"global_load_lds_dwordx4 v253, s[50:51] offset:-1024\n" \
"v_mfma_f32_16x16x32_bf16 v[56:59], a[96:99], a[88:91], 0\n" \
"ds_read_b64_tr_b16 a[200:201], v210 offset:2048\n" \
"v_mfma_f32_16x16x32_bf16 v[56:59], a[100:103], a[92:95], v[56:59]\n" \
"ds_read_b64_tr_b16 a[202:203], v210 offset:3072\n" \
"v_mfma_f32_16x16x32_bf16 v[16:19], a[104:107], a[88:91], 0\n" \
"ds_read_b64_tr_b16 a[204:205], v210 offset:14336\n" \
"global_load_lds_dwordx4 v253, s[50:51] offset:1024\n" \
"v_mfma_f32_16x16x32_bf16 v[16:19], a[108:111], a[92:95], v[16:19]\n" \
"s_add_u32 s50, s50, 0xffc40000\n" \
"s_addc_u32 s51, s51, -1\n" \
"v_mfma_f32_16x16x32_bf16 v[20:23], a[112:115], a[88:91], 0\n" \
"ds_read_b64_tr_b16 a[206:207], v210 offset:15360\n" \
"v_mfma_f32_16x16x32_bf16 v[20:23], a[116:119], a[92:95], v[20:23]\n" \
"s_waitcnt lgkmcnt(3)\n" \
"v_add_u32 v207, 0xffff3000, v207\n" \
"v_add_u32 v208, 0xffff3000, v208\n" \
"v_mfma_f32_16x16x32_bf16 v[28:31], a[112:115], a[192:195], 0\n" \
"v_add_u32 v205, 0xffff3000, v205\n" \
"v_add_u32 v206, 0xffff3000, v206\n" \
"v_mfma_f32_16x16x32_bf16 v[28:31], a[116:119], a[196:199], v[28:31]\n" \
"v_add_u32 v209, 0xffff3000, v209\n" \
"v_add_u32 v210, 0xffff3000, v210\n" \
"v_mfma_f32_16x16x32_bf16 v[24:27], a[104:107], a[192:195], 0\n" \
"v_permlane32_swap_b32 v232, v244\n" \
"v_mfma_f32_16x16x32_bf16 v[24:27], a[108:111], a[196:199], v[24:27]\n" \
"s_waitcnt vmcnt(11) lgkmcnt(0)\n" \
"v_add_f32 v244, v244, v232\n" \
"v_mfma_f32_16x16x32_bf16 v[64:67], a[96:99], a[192:195], 0\n" \
"v_rcp_f32 v232, v244\n" \
"v_mfma_f32_16x16x32_bf16 v[64:67], a[100:103], a[196:199], v[64:67]\n" \
"s_barrier\n" \
"v_mul_f32 v244, 0x4d800000, v244\n" \
"v_mfma_f32_16x16x32_bf16 v[72:75], a[64:67], a[208:211], 0\n" \
"ds_read_b64_tr_b16 a[96:97], v209 offset:2048\n" \
"v_add_u32 v233, 0x1c90080, v249\n" \
"v_mfma_f32_16x16x32_bf16 v[72:75], a[68:71], a[212:215], v[72:75]\n" \
"ds_read_b64_tr_b16 a[98:99], v209 offset:3072\n" \
"v_cndmask_b32 v233, v249, v233, s[16:17]\n" \
"v_mfma_f32_16x16x32_bf16 v[80:83], a[72:75], a[208:211], 0\n" \
"s_add_u32 m0, s95, 118784\n" \
"ds_read_b128 a[64:67], v207 offset:0\n" \
"ds_read_b64_tr_b16 a[100:101], v209 offset:14336\n" \
"v_mfma_f32_16x16x32_bf16 v[80:83], a[76:79], a[212:215], v[80:83]\n" \
"global_load_lds_dwordx4 v252, s[42:43] offset:0\n" \
"v_mul_f32 v128, v128, v232\n" \
"v_mfma_f32_16x16x32_bf16 v[84:87], a[72:75], a[216:219], 0\n" \
"ds_read_b128 a[68:71], v208 offset:0\n" \
"ds_read_b64_tr_b16 a[102:103], v209 offset:15360\n" \
"v_mfma_f32_16x16x32_bf16 v[84:87], a[76:79], a[220:223], v[84:87]\n" \
"ds_read_b64_tr_b16 a[104:105], v209 offset:0\n" \
"v_mul_f32 v129, v129, v232\n" \
"v_mfma_f32_16x16x32_bf16 v[44:47], a[200:203], a[72:75], 0\n" \
"ds_read_b64_tr_b16 a[106:107], v209 offset:1024\n" \
"v_mul_f32 v130, v130, v232\n" \
"v_mfma_f32_16x16x32_bf16 v[44:47], a[204:207], a[76:79], v[44:47]\n" \
"global_load_lds_dwordx4 v252, s[42:43] offset:2048\n" \
"v_mul_f32 v131, v131, v232\n" \
"v_mfma_f32_16x16x32_bf16 v[52:55], a[200:203], a[80:83], 0\n" \
"s_add_u32 s42, s42, 0x80\n" \
"s_addc_u32 s43, s43, 0\n" \
"ds_read_b128 a[72:75], v205 offset:0\n" \
"v_mfma_f32_16x16x32_bf16 v[52:55], a[204:207], a[84:87], v[52:55]\n" \
"ds_read_b128 a[76:79], v206 offset:0\n" \
"v_mul_f32 v132, v132, v232\n" \
"v_mfma_f32_16x16x32_bf16 v[92:95], a[80:83], a[216:219], 0\n" \
"ds_read_b64_tr_b16 a[108:109], v209 offset:12288\n" \
"v_mul_f32 v133, v133, v232\n" \
"v_mfma_f32_16x16x32_bf16 v[92:95], a[84:87], a[220:223], v[92:95]\n" \
"global_load_lds_dwordx4 v252, s[44:45] offset:1024\n" \
"v_mul_f32 v134, v134, v232\n" \
"v_mfma_f32_16x16x32_bf16 v[88:91], a[80:83], a[208:211], 0\n" \
"ds_read_b64_tr_b16 a[110:111], v209 offset:13312\n" \
"v_mul_f32 v135, v135, v232\n" \
"v_mfma_f32_16x16x32_bf16 v[88:91], a[84:87], a[212:215], v[88:91]\n" \
"ds_read_b64_tr_b16 a[112:113], v210 offset:0\n" \
"v_fma_f32 v244, v128, v129, v244\n" \
"v_mfma_f32_16x16x32_bf16 v[96:99], a[88:91], a[208:211], 0\n" \
"ds_read_b128 a[80:83], v205 offset:4096\n" \
"v_cvt_pk_bf16_f32 v128, v128, v129\n" \
"v_mfma_f32_16x16x32_bf16 v[96:99], a[92:95], a[212:215], v[96:99]\n" \
"global_load_lds_dwordx4 v252, s[44:45] offset:3072\n" \
"v_fma_f32 v244, v130, v131, v244\n" \
"v_mfma_f32_16x16x32_bf16 v[100:103], a[88:91], a[216:219], 0\n" \
"s_add_u32 s44, s44, 0x80\n" \
"s_addc_u32 s45, s45, 0\n" \
"ds_read_b128 a[84:87], v206 offset:4096\n" \
"v_mfma_f32_16x16x32_bf16 v[100:103], a[92:95], a[220:223], v[100:103]\n" \
"ds_read_b64_tr_b16 a[114:115], v210 offset:1024\n" \
"v_cvt_pk_bf16_f32 v129, v130, v131\n" \
"v_mfma_f32_16x16x32_bf16 v[60:63], a[200:203], a[88:91], 0\n" \
"s_add_u32 m0, s85, 135168\n" \
"ds_read_b64_tr_b16 a[116:117], v210 offset:12288\n" \
"v_fma_f32 v244, v132, v133, v244\n" \
"v_mfma_f32_16x16x32_bf16 v[60:63], a[204:207], a[92:95], v[60:63]\n" \
"buffer_load_dwordx4 v246, s[12:15], s46 offen lds\n" \
"v_mfma_f32_16x16x32_bf16 v[68:71], a[200:203], a[192:195], 0\n" \
"s_add_u32 s46, s46, 0x80\n" \
"ds_read_b128 a[88:91], v205 offset:8192\n" \
"v_cvt_pk_bf16_f32 v130, v132, v133\n" \
"v_mfma_f32_16x16x32_bf16 v[68:71], a[204:207], a[196:199], v[68:71]\n" \
"ds_read_b128 a[92:95], v206 offset:8192\n" \
"v_fma_f32 v244, v134, v135, v244\n" \
"v_mfma_f32_16x16x32_bf16 v[108:111], a[192:195], a[216:219], 0\n" \
"s_add_u32 m0, s84, 143360\n" \
"ds_read_b64_tr_b16 a[118:119], v210 offset:13312\n" \
"v_cvt_pk_bf16_f32 v131, v134, v135\n" \
"v_mfma_f32_16x16x32_bf16 v[108:111], a[196:199], a[220:223], v[108:111]\n" \
"buffer_store_dwordx4 v[128:131], v233, s[56:59], s78 offen offset:0 sc1\n" \
"global_load_lds_dwordx4 v253, s[48:49] offset:-4096\n" \
"v_mul_f32 v136, v136, v232\n" \
"v_mul_f32 v137, v137, v232\n" \
"v_mul_f32 v138, v138, v232\n" \
"v_mfma_f32_16x16x32_bf16 v[104:107], a[192:195], a[208:211], 0\n" \
"v_mul_f32 v139, v139, v232\n" \
"v_mul_f32 v140, v140, v232\n" \
"v_mfma_f32_16x16x32_bf16 v[104:107], a[196:199], a[212:215], v[104:107]\n" \
"s_waitcnt lgkmcnt(2)\n" \
"v_mul_f32 v141, v141, v232\n" \
"v_mul_f32 v142, v142, v232\n" \
"v_mfma_f32_16x16x32_bf16 v[32:35], a[96:99], a[64:67], v[32:35]\n" \
"ds_read_b128 a[192:195], v205 offset:12288\n" \
"v_mul_f32 v143, v143, v232\n" \
"v_mfma_f32_16x16x32_bf16 v[32:35], a[100:103], a[68:71], v[32:35]\n" \
"global_load_lds_dwordx4 v253, s[48:49] offset:-2048\n" \
"v_fma_f32 v244, v136, v137, v244\n" \
"v_mfma_f32_16x16x32_bf16 v[40:43], a[96:99], a[72:75], v[40:43]\n" \
"ds_read_b128 a[196:199], v206 offset:12288\n" \
"v_cvt_pk_bf16_f32 v136, v136, v137\n" \
"v_mfma_f32_16x16x32_bf16 v[40:43], a[100:103], a[76:79], v[40:43]\n" \
"ds_read_b64_tr_b16 a[208:209], v209 offset:4096\n" \
"v_fma_f32 v244, v138, v139, v244\n" \
"v_mfma_f32_16x16x32_bf16 v[0:3], a[104:107], a[72:75], v[0:3]\n" \
"ds_read_b64_tr_b16 a[210:211], v209 offset:5120\n" \
"v_cvt_pk_bf16_f32 v137, v138, v139\n" \
"v_mfma_f32_16x16x32_bf16 v[0:3], a[108:111], a[76:79], v[0:3]\n" \
"s_waitcnt lgkmcnt(3)\n" \
"global_load_lds_dwordx4 v253, s[48:49] offset:0\n" \
"v_fma_f32 v244, v140, v141, v244\n" \
"v_mfma_f32_16x16x32_bf16 v[4:7], a[112:115], a[72:75], v[4:7]\n" \
"s_add_u32 s48, s48, 0x60000\n" \
"s_addc_u32 s49, s49, 0\n" \
"ds_read_b64_tr_b16 a[212:213], v209 offset:16384\n" \
"v_mfma_f32_16x16x32_bf16 v[4:7], a[116:119], a[76:79], v[4:7]\n" \
"ds_read_b64_tr_b16 a[214:215], v209 offset:17408\n" \
"v_cvt_pk_bf16_f32 v138, v140, v141\n" \
"v_mfma_f32_16x16x32_bf16 v[12:15], a[112:115], a[80:83], v[12:15]\n" \
"ds_read_b64_tr_b16 a[216:217], v210 offset:4096\n" \
"v_fma_f32 v244, v142, v143, v244\n" \
"v_mfma_f32_16x16x32_bf16 v[12:15], a[116:119], a[84:87], v[12:15]\n" \
"global_load_lds_dwordx4 v253, s[50:51] offset:-3072\n" \
"v_cvt_pk_bf16_f32 v139, v142, v143\n" \
"v_mfma_f32_16x16x32_bf16 v[8:11], a[104:107], a[80:83], v[8:11]\n" \
"ds_read_b64_tr_b16 a[218:219], v210 offset:5120\n" \
"v_mfma_f32_16x16x32_bf16 v[8:11], a[108:111], a[84:87], v[8:11]\n" \
"buffer_store_dwordx4 v[136:139], v233, s[56:59], s78 offen offset:2048 sc1\n" \
"ds_read_b64_tr_b16 a[220:221], v210 offset:16384\n" \
"s_add_u32 s78, s78, 0x1000\n" \
"v_mul_f32 v144, v144, v232\n" \
"v_mul_f32 v145, v145, v232\n" \
"v_mfma_f32_16x16x32_bf16 v[48:51], a[96:99], a[80:83], v[48:51]\n" \
"ds_read_b64_tr_b16 a[222:223], v210 offset:17408\n" \
"v_mul_f32 v146, v146, v232\n" \
"v_mfma_f32_16x16x32_bf16 v[48:51], a[100:103], a[84:87], v[48:51]\n" \
"global_load_lds_dwordx4 v253, s[50:51] offset:-1024\n" \
"v_mul_f32 v147, v147, v232\n" \
"v_mfma_f32_16x16x32_bf16 v[56:59], a[96:99], a[88:91], v[56:59]\n" \
"ds_read_b64_tr_b16 a[200:201], v210 offset:2048\n" \
"v_mul_f32 v148, v148, v232\n" \
"v_mfma_f32_16x16x32_bf16 v[56:59], a[100:103], a[92:95], v[56:59]\n" \
"ds_read_b64_tr_b16 a[202:203], v210 offset:3072\n" \
"v_mul_f32 v149, v149, v232\n" \
"v_mfma_f32_16x16x32_bf16 v[16:19], a[104:107], a[88:91], v[16:19]\n" \
"ds_read_b64_tr_b16 a[204:205], v210 offset:14336\n" \
"v_mul_f32 v150, v150, v232\n" \
"v_mfma_f32_16x16x32_bf16 v[16:19], a[108:111], a[92:95], v[16:19]\n" \
"global_load_lds_dwordx4 v253, s[50:51] offset:1024\n" \
"v_mul_f32 v151, v151, v232\n" \
"v_mfma_f32_16x16x32_bf16 v[20:23], a[112:115], a[88:91], v[20:23]\n" \
"s_add_u32 s50, s50, 0x60000\n" \
"s_addc_u32 s51, s51, 0\n" \
"ds_read_b64_tr_b16 a[206:207], v210 offset:15360\n" \
"v_mfma_f32_16x16x32_bf16 v[20:23], a[116:119], a[92:95], v[20:23]\n" \
"s_waitcnt lgkmcnt(3)\n" \
"v_add_u32 v207, 0xffff0000, v207\n" \
"v_add_u32 v208, 0xffff0000, v208\n" \
"v_mfma_f32_16x16x32_bf16 v[28:31], a[112:115], a[192:195], v[28:31]\n" \
"v_add_u32 v205, 0xffff0000, v205\n" \
"v_add_u32 v206, 0xffff0000, v206\n" \
"v_mfma_f32_16x16x32_bf16 v[28:31], a[116:119], a[196:199], v[28:31]\n" \
"v_add_u32 v209, 0xffff0000, v209\n" \
"v_add_u32 v210, 0xffff0000, v210\n" \
"v_mfma_f32_16x16x32_bf16 v[24:27], a[104:107], a[192:195], v[24:27]\n" \
"v_fma_f32 v244, v144, v145, v244\n" \
"v_cvt_pk_bf16_f32 v144, v144, v145\n" \
"v_mfma_f32_16x16x32_bf16 v[24:27], a[108:111], a[196:199], v[24:27]\n" \
"s_waitcnt vmcnt(13) lgkmcnt(0)\n" \
"v_fma_f32 v244, v146, v147, v244\n" \
"v_cvt_pk_bf16_f32 v145, v146, v147\n" \
"v_mfma_f32_16x16x32_bf16 v[64:67], a[96:99], a[192:195], v[64:67]\n" \
"v_fma_f32 v244, v148, v149, v244\n" \
"v_cvt_pk_bf16_f32 v146, v148, v149\n" \
"v_mfma_f32_16x16x32_bf16 v[64:67], a[100:103], a[196:199], v[64:67]\n" \
"s_barrier\n" \
"v_fma_f32 v244, v150, v151, v244\n" \
"v_mfma_f32_16x16x32_bf16 v[72:75], a[64:67], a[208:211], v[72:75]\n" \
"ds_read_b64_tr_b16 a[96:97], v209 offset:2048\n" \
"v_cvt_pk_bf16_f32 v147, v150, v151\n" \
"v_mfma_f32_16x16x32_bf16 v[72:75], a[68:71], a[212:215], v[72:75]\n" \
"buffer_store_dwordx4 v[144:147], v233, s[56:59], s78 offen offset:0 sc1\n" \
"ds_read_b64_tr_b16 a[98:99], v209 offset:3072\n" \
"v_mul_f32 v152, v152, v232\n" \
"v_mul_f32 v153, v153, v232\n" \
"v_mul_f32 v154, v154, v232\n" \
"v_mfma_f32_16x16x32_bf16 v[80:83], a[72:75], a[208:211], v[80:83]\n" \
"s_add_u32 m0, s95, 65536\n" \
"ds_read_b128 a[64:67], v207 offset:0\n" \
"ds_read_b64_tr_b16 a[100:101], v209 offset:14336\n" \
"v_mfma_f32_16x16x32_bf16 v[80:83], a[76:79], a[212:215], v[80:83]\n" \
"global_load_lds_dwordx4 v252, s[42:43] offset:0\n" \
"v_mul_f32 v155, v155, v232\n" \
"v_mfma_f32_16x16x32_bf16 v[84:87], a[72:75], a[216:219], v[84:87]\n" \
"ds_read_b128 a[68:71], v208 offset:0\n" \
"ds_read_b64_tr_b16 a[102:103], v209 offset:15360\n" \
"v_mfma_f32_16x16x32_bf16 v[84:87], a[76:79], a[220:223], v[84:87]\n" \
"ds_read_b64_tr_b16 a[104:105], v209 offset:0\n" \
"v_mul_f32 v156, v156, v232\n" \
"v_mfma_f32_16x16x32_bf16 v[44:47], a[200:203], a[72:75], v[44:47]\n" \
"ds_read_b64_tr_b16 a[106:107], v209 offset:1024\n" \
"v_mul_f32 v157, v157, v232\n" \
"v_mfma_f32_16x16x32_bf16 v[44:47], a[204:207], a[76:79], v[44:47]\n" \
"global_load_lds_dwordx4 v252, s[42:43] offset:2048\n" \
"v_mul_f32 v158, v158, v232\n" \
"v_mfma_f32_16x16x32_bf16 v[52:55], a[200:203], a[80:83], v[52:55]\n" \
"s_add_u32 s42, s42, 0x100\n" \
"s_addc_u32 s43, s43, 0\n" \
"ds_read_b128 a[72:75], v205 offset:0\n" \
"v_mfma_f32_16x16x32_bf16 v[52:55], a[204:207], a[84:87], v[52:55]\n" \
"ds_read_b128 a[76:79], v206 offset:0\n" \
"v_mul_f32 v159, v159, v232\n" \
"v_mfma_f32_16x16x32_bf16 v[92:95], a[80:83], a[216:219], v[92:95]\n" \
"ds_read_b64_tr_b16 a[108:109], v209 offset:12288\n" \
"v_fma_f32 v244, v152, v153, v244\n" \
"v_mfma_f32_16x16x32_bf16 v[92:95], a[84:87], a[220:223], v[92:95]\n" \
"global_load_lds_dwordx4 v252, s[44:45] offset:1024\n" \
"v_cvt_pk_bf16_f32 v152, v152, v153\n" \
"v_mfma_f32_16x16x32_bf16 v[88:91], a[80:83], a[208:211], v[88:91]\n" \
"ds_read_b64_tr_b16 a[110:111], v209 offset:13312\n" \
"v_fma_f32 v244, v154, v155, v244\n" \
"v_mfma_f32_16x16x32_bf16 v[88:91], a[84:87], a[212:215], v[88:91]\n" \
"ds_read_b64_tr_b16 a[112:113], v210 offset:0\n" \
"v_cvt_pk_bf16_f32 v153, v154, v155\n" \
"v_mfma_f32_16x16x32_bf16 v[96:99], a[88:91], a[208:211], v[96:99]\n" \
"ds_read_b128 a[80:83], v205 offset:4096\n" \
"v_fma_f32 v244, v156, v157, v244\n" \
"v_mfma_f32_16x16x32_bf16 v[96:99], a[92:95], a[212:215], v[96:99]\n" \
"global_load_lds_dwordx4 v252, s[44:45] offset:3072\n" \
"v_cvt_pk_bf16_f32 v154, v156, v157\n" \
"v_mfma_f32_16x16x32_bf16 v[100:103], a[88:91], a[216:219], v[100:103]\n" \
"s_add_u32 s44, s44, 0x100\n" \
"s_addc_u32 s45, s45, 0\n" \
"ds_read_b128 a[84:87], v206 offset:4096\n" \
"v_mfma_f32_16x16x32_bf16 v[100:103], a[92:95], a[220:223], v[100:103]\n" \
"ds_read_b64_tr_b16 a[114:115], v210 offset:1024\n" \
"v_fma_f32 v244, v158, v159, v244\n" \
"v_mfma_f32_16x16x32_bf16 v[60:63], a[200:203], a[88:91], v[60:63]\n" \
"s_add_u32 m0, s85, 81920\n" \
"ds_read_b64_tr_b16 a[116:117], v210 offset:12288\n" \
"v_cvt_pk_bf16_f32 v155, v158, v159\n" \
"v_mfma_f32_16x16x32_bf16 v[60:63], a[204:207], a[92:95], v[60:63]\n" \
"buffer_store_dwordx4 v[152:155], v233, s[56:59], s78 offen offset:2048 sc1\n" \
"buffer_load_dwordx4 v246, s[12:15], s46 offen lds\n" \
"v_cmp_class_f32 vcc, v244, s94\n" \
"s_sub_u32 s78, s78, 4096\n" \
"v_mfma_f32_16x16x32_bf16 v[68:71], a[200:203], a[192:195], v[68:71]\n" \
"s_add_u32 s46, s46, 0x100\n" \
"ds_read_b128 a[88:91], v205 offset:8192\n" \
"s_or_b64 s[92:93], s[92:93], vcc\n" \
"v_mfma_f32_16x16x32_bf16 v[68:71], a[204:207], a[196:199], v[68:71]\n" \
"ds_read_b128 a[92:95], v206 offset:8192\n" \
"v_mov_b32 v234, v245\n" \
"v_mfma_f32_16x16x32_bf16 v[108:111], a[192:195], a[216:219], v[108:111]\n" \
"s_add_u32 m0, s84, 90112\n" \
"ds_read_b64_tr_b16 a[118:119], v210 offset:13312\n" \
"v_mfma_f32_16x16x32_bf16 v[108:111], a[196:199], a[220:223], v[108:111]\n" \
"global_load_lds_dwordx4 v253, s[48:49] offset:-4096\n" \
"v_mfma_f32_16x16x32_bf16 v[104:107], a[192:195], a[208:211], v[104:107]\n" \
"v_permlane16_swap_b32 v234, v245\n" \
"v_mfma_f32_16x16x32_bf16 v[104:107], a[196:199], a[212:215], v[104:107]\n" \
"s_waitcnt lgkmcnt(2)\n" \
"v_add_f32 v245, v245, v234\n" \
"v_mov_b32 v234, v245\n" \
"v_mfma_f32_16x16x32_bf16 v[32:35], a[96:99], a[64:67], v[32:35]\n" \
"ds_read_b128 a[192:195], v205 offset:12288\n" \
"v_mfma_f32_16x16x32_bf16 v[32:35], a[100:103], a[68:71], v[32:35]\n" \
"global_load_lds_dwordx4 v253, s[48:49] offset:-2048\n" \
"v_permlane32_swap_b32 v234, v245\n" \
"v_mfma_f32_16x16x32_bf16 v[40:43], a[96:99], a[72:75], v[40:43]\n" \
"ds_read_b128 a[196:199], v206 offset:12288\n" \
"v_add_f32 v245, v245, v234\n" \
"v_mfma_f32_16x16x32_bf16 v[40:43], a[100:103], a[76:79], v[40:43]\n" \
"ds_read_b64_tr_b16 a[208:209], v209 offset:4096\n" \
"v_rcp_f32 v234, v245\n" \
"v_mfma_f32_16x16x32_bf16 v[0:3], a[104:107], a[72:75], v[0:3]\n" \
"ds_read_b64_tr_b16 a[210:211], v209 offset:5120\n" \
"v_mul_f32 v245, 0x4d800000, v245\n" \
"v_mfma_f32_16x16x32_bf16 v[0:3], a[108:111], a[76:79], v[0:3]\n" \
"s_waitcnt lgkmcnt(3)\n" \
"global_load_lds_dwordx4 v253, s[48:49] offset:0\n" \
"v_add_u32 v235, 0x1c90080, v249\n" \
"v_mfma_f32_16x16x32_bf16 v[4:7], a[112:115], a[72:75], v[4:7]\n" \
"s_add_u32 s48, s48, 0xc0000\n" \
"s_addc_u32 s49, s49, 0\n" \
"ds_read_b64_tr_b16 a[212:213], v209 offset:16384\n" \
"v_mfma_f32_16x16x32_bf16 v[4:7], a[116:119], a[76:79], v[4:7]\n" \
"ds_read_b64_tr_b16 a[214:215], v209 offset:17408\n" \
"v_cndmask_b32 v235, v249, v235, s[16:17]\n" \
"v_mfma_f32_16x16x32_bf16 v[12:15], a[112:115], a[80:83], v[12:15]\n" \
"s_add_u32 s78, s78, 532480\n" \
"ds_read_b64_tr_b16 a[216:217], v210 offset:4096\n" \
"v_mul_f32 v160, v160, v234\n" \
"v_mfma_f32_16x16x32_bf16 v[12:15], a[116:119], a[84:87], v[12:15]\n" \
"global_load_lds_dwordx4 v253, s[50:51] offset:-3072\n" \
"v_mul_f32 v161, v161, v234\n" \
"v_mfma_f32_16x16x32_bf16 v[8:11], a[104:107], a[80:83], v[8:11]\n" \
"ds_read_b64_tr_b16 a[218:219], v210 offset:5120\n" \
"v_mul_f32 v162, v162, v234\n" \
"v_mfma_f32_16x16x32_bf16 v[8:11], a[108:111], a[84:87], v[8:11]\n" \
"ds_read_b64_tr_b16 a[220:221], v210 offset:16384\n" \
"v_mul_f32 v163, v163, v234\n" \
"v_mfma_f32_16x16x32_bf16 v[48:51], a[96:99], a[80:83], v[48:51]\n" \
"ds_read_b64_tr_b16 a[222:223], v210 offset:17408\n" \
"v_mul_f32 v164, v164, v234\n" \
"v_mfma_f32_16x16x32_bf16 v[48:51], a[100:103], a[84:87], v[48:51]\n" \
"global_load_lds_dwordx4 v253, s[50:51] offset:-1024\n" \
"v_mul_f32 v165, v165, v234\n" \
"v_mfma_f32_16x16x32_bf16 v[56:59], a[96:99], a[88:91], v[56:59]\n" \
"ds_read_b64_tr_b16 a[200:201], v210 offset:2048\n" \
"v_mul_f32 v166, v166, v234\n" \
"v_mfma_f32_16x16x32_bf16 v[56:59], a[100:103], a[92:95], v[56:59]\n" \
"ds_read_b64_tr_b16 a[202:203], v210 offset:3072\n" \
"v_mul_f32 v167, v167, v234\n" \
"v_mfma_f32_16x16x32_bf16 v[16:19], a[104:107], a[88:91], v[16:19]\n" \
"ds_read_b64_tr_b16 a[204:205], v210 offset:14336\n" \
"v_fma_f32 v245, v160, v161, v245\n" \
"v_mfma_f32_16x16x32_bf16 v[16:19], a[108:111], a[92:95], v[16:19]\n" \
"global_load_lds_dwordx4 v253, s[50:51] offset:1024\n" \
"v_cvt_pk_bf16_f32 v160, v160, v161\n" \
"v_mfma_f32_16x16x32_bf16 v[20:23], a[112:115], a[88:91], v[20:23]\n" \
"s_add_u32 s50, s50, 0xc0000\n" \
"s_addc_u32 s51, s51, 0\n" \
"ds_read_b64_tr_b16 a[206:207], v210 offset:15360\n" \
"v_mfma_f32_16x16x32_bf16 v[20:23], a[116:119], a[92:95], v[20:23]\n" \
"s_waitcnt lgkmcnt(3)\n" \
"v_add_u32 v207, 0x1d000, v207\n" \
"v_add_u32 v208, 0x1d000, v208\n" \
"v_mfma_f32_16x16x32_bf16 v[28:31], a[112:115], a[192:195], v[28:31]\n" \
"v_add_u32 v205, 0x1d000, v205\n" \
"v_add_u32 v206, 0x1d000, v206\n" \
"v_mfma_f32_16x16x32_bf16 v[28:31], a[116:119], a[196:199], v[28:31]\n" \
"v_add_u32 v209, 0x1d000, v209\n" \
"v_add_u32 v210, 0x1d000, v210\n" \
"v_mfma_f32_16x16x32_bf16 v[24:27], a[104:107], a[192:195], v[24:27]\n" \
"v_fma_f32 v245, v162, v163, v245\n" \
"v_cvt_pk_bf16_f32 v161, v162, v163\n" \
"v_mfma_f32_16x16x32_bf16 v[24:27], a[108:111], a[196:199], v[24:27]\n" \
"s_waitcnt vmcnt(13) lgkmcnt(0)\n" \
"v_fma_f32 v245, v164, v165, v245\n" \
"v_cvt_pk_bf16_f32 v162, v164, v165\n" \
"v_mfma_f32_16x16x32_bf16 v[64:67], a[96:99], a[192:195], v[64:67]\n" \
"v_fma_f32 v245, v166, v167, v245\n" \
"v_cvt_pk_bf16_f32 v163, v166, v167\n" \
"v_mfma_f32_16x16x32_bf16 v[64:67], a[100:103], a[196:199], v[64:67]\n" \
"s_barrier\n" \
"buffer_store_dwordx4 v[160:163], v235, s[56:59], s78 offen offset:0 sc1\n" \
"v_mul_f32 v168, v168, v234\n" \
"v_mul_f32 v169, v169, v234\n" \
"v_mul_f32 v170, v170, v234\n" \
"v_mul_f32 v171, v171, v234\n" \
"v_mfma_f32_16x16x32_bf16 v[72:75], a[64:67], a[208:211], v[72:75]\n" \
"ds_read_b64_tr_b16 a[96:97], v209 offset:2048\n" \
"v_mul_f32 v172, v172, v234\n" \
"v_mfma_f32_16x16x32_bf16 v[72:75], a[68:71], a[212:215], v[72:75]\n" \
"ds_read_b64_tr_b16 a[98:99], v209 offset:3072\n" \
"v_mul_f32 v173, v173, v234\n" \
"v_mfma_f32_16x16x32_bf16 v[80:83], a[72:75], a[208:211], v[80:83]\n" \
"s_add_u32 m0, s95, 0\n" \
"ds_read_b128 a[64:67], v207 offset:0\n" \
"ds_read_b64_tr_b16 a[100:101], v209 offset:14336\n" \
"v_mfma_f32_16x16x32_bf16 v[80:83], a[76:79], a[212:215], v[80:83]\n" \
"global_load_lds_dwordx4 v252, s[42:43] offset:0\n" \
"v_mul_f32 v174, v174, v234\n" \
"v_mfma_f32_16x16x32_bf16 v[84:87], a[72:75], a[216:219], v[84:87]\n" \
"ds_read_b128 a[68:71], v208 offset:0\n" \
"ds_read_b64_tr_b16 a[102:103], v209 offset:15360\n" \
"v_mfma_f32_16x16x32_bf16 v[84:87], a[76:79], a[220:223], v[84:87]\n" \
"ds_read_b64_tr_b16 a[104:105], v209 offset:0\n" \
"v_mul_f32 v175, v175, v234\n" \
"v_mfma_f32_16x16x32_bf16 v[44:47], a[200:203], a[72:75], v[44:47]\n" \
"ds_read_b64_tr_b16 a[106:107], v209 offset:1024\n" \
"v_fma_f32 v245, v168, v169, v245\n" \
"v_mfma_f32_16x16x32_bf16 v[44:47], a[204:207], a[76:79], v[44:47]\n" \
"global_load_lds_dwordx4 v252, s[42:43] offset:2048\n" \
"v_cvt_pk_bf16_f32 v168, v168, v169\n" \
"v_mfma_f32_16x16x32_bf16 v[52:55], a[200:203], a[80:83], v[52:55]\n" \
"s_add_u32 s42, s42, 0x80\n" \
"s_addc_u32 s43, s43, 0\n" \
"ds_read_b128 a[72:75], v205 offset:0\n" \
"v_mfma_f32_16x16x32_bf16 v[52:55], a[204:207], a[84:87], v[52:55]\n" \
"ds_read_b128 a[76:79], v206 offset:0\n" \
"v_fma_f32 v245, v170, v171, v245\n" \
"v_mfma_f32_16x16x32_bf16 v[92:95], a[80:83], a[216:219], v[92:95]\n" \
"ds_read_b64_tr_b16 a[108:109], v209 offset:12288\n" \
"v_cvt_pk_bf16_f32 v169, v170, v171\n" \
"v_mfma_f32_16x16x32_bf16 v[92:95], a[84:87], a[220:223], v[92:95]\n" \
"global_load_lds_dwordx4 v252, s[44:45] offset:1024\n" \
"v_fma_f32 v245, v172, v173, v245\n" \
"v_mfma_f32_16x16x32_bf16 v[88:91], a[80:83], a[208:211], v[88:91]\n" \
"ds_read_b64_tr_b16 a[110:111], v209 offset:13312\n" \
"v_cvt_pk_bf16_f32 v170, v172, v173\n" \
"v_mfma_f32_16x16x32_bf16 v[88:91], a[84:87], a[212:215], v[88:91]\n" \
"ds_read_b64_tr_b16 a[112:113], v210 offset:0\n" \
"v_fma_f32 v245, v174, v175, v245\n" \
"v_mfma_f32_16x16x32_bf16 v[96:99], a[88:91], a[208:211], v[96:99]\n" \
"ds_read_b128 a[80:83], v205 offset:4096\n" \
"v_cvt_pk_bf16_f32 v171, v174, v175\n" \
"v_mfma_f32_16x16x32_bf16 v[96:99], a[92:95], a[212:215], v[96:99]\n" \
"buffer_store_dwordx4 v[168:171], v235, s[56:59], s78 offen offset:2048 sc1\n" \
"global_load_lds_dwordx4 v252, s[44:45] offset:3072\n" \
"s_add_u32 s78, s78, 0x1000\n" \
"v_mul_f32 v176, v176, v234\n" \
"v_mul_f32 v177, v177, v234\n" \
"v_mfma_f32_16x16x32_bf16 v[100:103], a[88:91], a[216:219], v[100:103]\n" \
"s_add_u32 s44, s44, 0x80\n" \
"s_addc_u32 s45, s45, 0\n" \
"ds_read_b128 a[84:87], v206 offset:4096\n" \
"v_mfma_f32_16x16x32_bf16 v[100:103], a[92:95], a[220:223], v[100:103]\n" \
"ds_read_b64_tr_b16 a[114:115], v210 offset:1024\n" \
"v_mul_f32 v178, v178, v234\n" \
"v_mfma_f32_16x16x32_bf16 v[60:63], a[200:203], a[88:91], v[60:63]\n" \
"s_add_u32 m0, s85, 16384\n" \
"ds_read_b64_tr_b16 a[116:117], v210 offset:12288\n" \
"v_mul_f32 v179, v179, v234\n" \
"v_mfma_f32_16x16x32_bf16 v[60:63], a[204:207], a[92:95], v[60:63]\n" \
"buffer_load_dwordx4 v246, s[12:15], s46 offen lds\n" \
"v_mfma_f32_16x16x32_bf16 v[68:71], a[200:203], a[192:195], v[68:71]\n" \
"s_add_u32 s46, s46, 0x80\n" \
"ds_read_b128 a[88:91], v205 offset:8192\n" \
"v_mul_f32 v180, v180, v234\n" \
"v_mfma_f32_16x16x32_bf16 v[68:71], a[204:207], a[196:199], v[68:71]\n" \
"ds_read_b128 a[92:95], v206 offset:8192\n" \
"v_mul_f32 v181, v181, v234\n" \
"v_mfma_f32_16x16x32_bf16 v[108:111], a[192:195], a[216:219], v[108:111]\n" \
"s_add_u32 m0, s84, 24576\n" \
"ds_read_b64_tr_b16 a[118:119], v210 offset:13312\n" \
"v_mul_f32 v182, v182, v234\n" \
"v_mfma_f32_16x16x32_bf16 v[108:111], a[196:199], a[220:223], v[108:111]\n" \
"global_load_lds_dwordx4 v253, s[48:49] offset:-4096\n" \
"v_mul_f32 v183, v183, v234\n" \
"v_mfma_f32_16x16x32_bf16 v[104:107], a[192:195], a[208:211], v[104:107]\n" \
"v_fma_f32 v245, v176, v177, v245\n" \
"v_cvt_pk_bf16_f32 v176, v176, v177\n" \
"v_mfma_f32_16x16x32_bf16 v[104:107], a[196:199], a[212:215], v[104:107]\n" \
"s_waitcnt lgkmcnt(2)\n" \
"v_fma_f32 v245, v178, v179, v245\n" \
"v_cvt_pk_bf16_f32 v177, v178, v179\n" \
"v_mfma_f32_16x16x32_bf16 v[32:35], a[96:99], a[64:67], v[32:35]\n" \
"ds_read_b128 a[192:195], v205 offset:12288\n" \
"Lpg9188_%=:\n" \
"Lst1x3_%=:\n" \
"s_cbranch_execz Lpg11445_%=\n" \
"v_fma_f32 v245, v180, v181, v245\n" \
"v_mfma_f32_16x16x32_bf16 v[32:35], a[100:103], a[68:71], v[32:35]\n" \
"global_load_lds_dwordx4 v253, s[48:49] offset:-2048\n" \
"v_cvt_pk_bf16_f32 v178, v180, v181\n" \
"v_mfma_f32_16x16x32_bf16 v[40:43], a[96:99], a[72:75], v[40:43]\n" \
"ds_read_b128 a[196:199], v206 offset:12288\n" \
"v_fma_f32 v245, v182, v183, v245\n" \
"v_mfma_f32_16x16x32_bf16 v[40:43], a[100:103], a[76:79], v[40:43]\n" \
"ds_read_b64_tr_b16 a[208:209], v209 offset:4096\n" \
"v_cvt_pk_bf16_f32 v179, v182, v183\n" \
"v_mfma_f32_16x16x32_bf16 v[0:3], a[104:107], a[72:75], v[0:3]\n" \
"ds_read_b64_tr_b16 a[210:211], v209 offset:5120\n" \
"v_mfma_f32_16x16x32_bf16 v[0:3], a[108:111], a[76:79], v[0:3]\n" \
"s_waitcnt lgkmcnt(3)\n" \
"buffer_store_dwordx4 v[176:179], v235, s[56:59], s78 offen offset:0 sc1\n" \
"global_load_lds_dwordx4 v253, s[48:49] offset:0\n" \
"v_mul_f32 v184, v184, v234\n" \
"v_mul_f32 v185, v185, v234\n" \
"v_mul_f32 v186, v186, v234\n" \
"v_mfma_f32_16x16x32_bf16 v[4:7], a[112:115], a[72:75], v[4:7]\n" \
"s_add_u32 s48, s48, 0x60000\n" \
"s_addc_u32 s49, s49, 0\n" \
"ds_read_b64_tr_b16 a[212:213], v209 offset:16384\n" \
"v_mfma_f32_16x16x32_bf16 v[4:7], a[116:119], a[76:79], v[4:7]\n" \
"ds_read_b64_tr_b16 a[214:215], v209 offset:17408\n" \
"v_mul_f32 v187, v187, v234\n" \
"v_mfma_f32_16x16x32_bf16 v[12:15], a[112:115], a[80:83], v[12:15]\n" \
"ds_read_b64_tr_b16 a[216:217], v210 offset:4096\n" \
"v_mul_f32 v188, v188, v234\n" \
"v_mfma_f32_16x16x32_bf16 v[12:15], a[116:119], a[84:87], v[12:15]\n" \
"global_load_lds_dwordx4 v253, s[50:51] offset:-3072\n" \
"v_mul_f32 v189, v189, v234\n" \
"v_mfma_f32_16x16x32_bf16 v[8:11], a[104:107], a[80:83], v[8:11]\n" \
"ds_read_b64_tr_b16 a[218:219], v210 offset:5120\n" \
"v_mul_f32 v190, v190, v234\n" \
"v_mfma_f32_16x16x32_bf16 v[8:11], a[108:111], a[84:87], v[8:11]\n" \
"ds_read_b64_tr_b16 a[220:221], v210 offset:16384\n" \
"v_mul_f32 v191, v191, v234\n" \
"v_mfma_f32_16x16x32_bf16 v[48:51], a[96:99], a[80:83], v[48:51]\n" \
"ds_read_b64_tr_b16 a[222:223], v210 offset:17408\n" \
"v_fma_f32 v245, v184, v185, v245\n" \
"v_mfma_f32_16x16x32_bf16 v[48:51], a[100:103], a[84:87], v[48:51]\n" \
"global_load_lds_dwordx4 v253, s[50:51] offset:-1024\n" \
"v_cvt_pk_bf16_f32 v184, v184, v185\n" \
"v_mfma_f32_16x16x32_bf16 v[56:59], a[96:99], a[88:91], v[56:59]\n" \
"ds_read_b64_tr_b16 a[200:201], v210 offset:2048\n" \
"v_fma_f32 v245, v186, v187, v245\n" \
"v_mfma_f32_16x16x32_bf16 v[56:59], a[100:103], a[92:95], v[56:59]\n" \
"ds_read_b64_tr_b16 a[202:203], v210 offset:3072\n" \
"v_cvt_pk_bf16_f32 v185, v186, v187\n" \
"v_mfma_f32_16x16x32_bf16 v[16:19], a[104:107], a[88:91], v[16:19]\n" \
"ds_read_b64_tr_b16 a[204:205], v210 offset:14336\n" \
"v_fma_f32 v245, v188, v189, v245\n" \
"v_mfma_f32_16x16x32_bf16 v[16:19], a[108:111], a[92:95], v[16:19]\n" \
"global_load_lds_dwordx4 v253, s[50:51] offset:1024\n" \
"v_cvt_pk_bf16_f32 v186, v188, v189\n" \
"v_mfma_f32_16x16x32_bf16 v[20:23], a[112:115], a[88:91], v[20:23]\n" \
"s_add_u32 s50, s50, 0x60000\n" \
"s_addc_u32 s51, s51, 0\n" \
"ds_read_b64_tr_b16 a[206:207], v210 offset:15360\n" \
"v_mfma_f32_16x16x32_bf16 v[20:23], a[116:119], a[92:95], v[20:23]\n" \
"s_waitcnt lgkmcnt(3)\n" \
"v_add_u32 v207, 0xffff3000, v207\n" \
"v_add_u32 v208, 0xffff3000, v208\n" \
"v_mfma_f32_16x16x32_bf16 v[28:31], a[112:115], a[192:195], v[28:31]\n" \
"v_add_u32 v205, 0xffff3000, v205\n" \
"v_add_u32 v206, 0xffff3000, v206\n" \
"v_mfma_f32_16x16x32_bf16 v[28:31], a[116:119], a[196:199], v[28:31]\n" \
"v_add_u32 v209, 0xffff3000, v209\n" \
"v_add_u32 v210, 0xffff3000, v210\n" \
"v_mfma_f32_16x16x32_bf16 v[24:27], a[104:107], a[192:195], v[24:27]\n" \
"v_fma_f32 v245, v190, v191, v245\n" \
"v_cvt_pk_bf16_f32 v187, v190, v191\n" \
"v_mfma_f32_16x16x32_bf16 v[24:27], a[108:111], a[196:199], v[24:27]\n" \
"s_waitcnt vmcnt(14) lgkmcnt(0)\n" \
"buffer_store_dwordx4 v[184:187], v235, s[56:59], s78 offen offset:2048 sc1\n" \
"v_cmp_class_f32 vcc, v245, s94\n" \
"s_sub_u32 s78, s78, 536576\n" \
"v_mfma_f32_16x16x32_bf16 v[64:67], a[96:99], a[192:195], v[64:67]\n" \
"s_or_b64 s[92:93], s[92:93], vcc\n" \
"s_cbranch_scc1 Lexact_%=\n" \
"Lexr_%=:\n" \
"v_mfma_f32_16x16x32_bf16 v[64:67], a[100:103], a[196:199], v[64:67]\n" \
"s_barrier\n" \
"v_mfma_f32_16x16x32_bf16 v[72:75], a[64:67], a[208:211], v[72:75]\n" \
"ds_read_b64_tr_b16 a[96:97], v209 offset:2048\n" \
"v_mfma_f32_16x16x32_bf16 v[72:75], a[68:71], a[212:215], v[72:75]\n" \
"ds_read_b64_tr_b16 a[98:99], v209 offset:3072\n" \
"v_mfma_f32_16x16x32_bf16 v[80:83], a[72:75], a[208:211], v[80:83]\n" \
"s_add_u32 m0, s95, 118784\n" \
"ds_read_b128 a[64:67], v207 offset:0\n" \
"ds_read_b64_tr_b16 a[100:101], v209 offset:14336\n" \
"v_mfma_f32_16x16x32_bf16 v[80:83], a[76:79], a[212:215], v[80:83]\n" \
"global_load_lds_dwordx4 v252, s[42:43] offset:0\n" \
"v_mfma_f32_16x16x32_bf16 v[84:87], a[72:75], a[216:219], v[84:87]\n" \
"ds_read_b128 a[68:71], v208 offset:0\n" \
"ds_read_b64_tr_b16 a[102:103], v209 offset:15360\n" \
"v_mfma_f32_16x16x32_bf16 v[84:87], a[76:79], a[220:223], v[84:87]\n" \
"ds_read_b64_tr_b16 a[104:105], v209 offset:0\n" \
"v_mfma_f32_16x16x32_bf16 v[44:47], a[200:203], a[72:75], v[44:47]\n" \
"ds_read_b64_tr_b16 a[106:107], v209 offset:1024\n" \
"v_mfma_f32_16x16x32_bf16 v[44:47], a[204:207], a[76:79], v[44:47]\n" \
"global_load_lds_dwordx4 v252, s[42:43] offset:2048\n" \
"v_mfma_f32_16x16x32_bf16 v[52:55], a[200:203], a[80:83], v[52:55]\n" \
"s_add_u32 s42, s42, 0x80\n" \
"s_addc_u32 s43, s43, 0\n" \
"ds_read_b128 a[72:75], v205 offset:0\n" \
"v_mfma_f32_16x16x32_bf16 v[52:55], a[204:207], a[84:87], v[52:55]\n" \
"ds_read_b128 a[76:79], v206 offset:0\n" \
"v_mfma_f32_16x16x32_bf16 v[92:95], a[80:83], a[216:219], v[92:95]\n" \
"ds_read_b64_tr_b16 a[108:109], v209 offset:12288\n" \
"v_mfma_f32_16x16x32_bf16 v[92:95], a[84:87], a[220:223], v[92:95]\n" \
"global_load_lds_dwordx4 v252, s[44:45] offset:1024\n" \
"v_mfma_f32_16x16x32_bf16 v[88:91], a[80:83], a[208:211], v[88:91]\n" \
"ds_read_b64_tr_b16 a[110:111], v209 offset:13312\n" \
"v_mfma_f32_16x16x32_bf16 v[88:91], a[84:87], a[212:215], v[88:91]\n" \
"ds_read_b64_tr_b16 a[112:113], v210 offset:0\n" \
"v_mfma_f32_16x16x32_bf16 v[96:99], a[88:91], a[208:211], v[96:99]\n" \
"ds_read_b128 a[80:83], v205 offset:4096\n" \
"v_mfma_f32_16x16x32_bf16 v[96:99], a[92:95], a[212:215], v[96:99]\n" \
"global_load_lds_dwordx4 v252, s[44:45] offset:3072\n" \
"v_mfma_f32_16x16x32_bf16 v[100:103], a[88:91], a[216:219], v[100:103]\n" \
"s_add_u32 s44, s44, 0x80\n" \
"s_addc_u32 s45, s45, 0\n" \
"ds_read_b128 a[84:87], v206 offset:4096\n" \
"v_mfma_f32_16x16x32_bf16 v[100:103], a[92:95], a[220:223], v[100:103]\n" \
"ds_read_b64_tr_b16 a[114:115], v210 offset:1024\n" \
"v_mfma_f32_16x16x32_bf16 v[60:63], a[200:203], a[88:91], v[60:63]\n" \
"s_add_u32 m0, s85, 135168\n" \
"ds_read_b64_tr_b16 a[116:117], v210 offset:12288\n" \
"v_mfma_f32_16x16x32_bf16 v[60:63], a[204:207], a[92:95], v[60:63]\n" \
"buffer_load_dwordx4 v246, s[12:15], s46 offen lds\n" \
"v_mfma_f32_16x16x32_bf16 v[68:71], a[200:203], a[192:195], v[68:71]\n" \
"ds_read_b128 a[88:91], v205 offset:8192\n" \
"s_add_u32 s46, s46, 0x80\n" \
"v_mfma_f32_16x16x32_bf16 v[68:71], a[204:207], a[196:199], v[68:71]\n" \
"ds_read_b128 a[92:95], v206 offset:8192\n" \
"v_mfma_f32_16x16x32_bf16 v[108:111], a[192:195], a[216:219], v[108:111]\n" \
"s_add_u32 m0, s84, 143360\n" \
"ds_read_b64_tr_b16 a[118:119], v210 offset:13312\n" \
"v_mfma_f32_16x16x32_bf16 v[108:111], a[196:199], a[220:223], v[108:111]\n" \
"global_load_lds_dwordx4 v253, s[48:49] offset:-4096\n" \
"v_mfma_f32_16x16x32_bf16 v[104:107], a[192:195], a[208:211], v[104:107]\n" \
"v_mfma_f32_16x16x32_bf16 v[104:107], a[196:199], a[212:215], v[104:107]\n" \
"s_waitcnt lgkmcnt(2)\n" \
"v_mfma_f32_16x16x32_bf16 v[32:35], a[96:99], a[64:67], v[32:35]\n" \
"ds_read_b128 a[192:195], v205 offset:12288\n" \
"v_mfma_f32_16x16x32_bf16 v[32:35], a[100:103], a[68:71], v[32:35]\n" \
"global_load_lds_dwordx4 v253, s[48:49] offset:-2048\n" \
"v_mfma_f32_16x16x32_bf16 v[40:43], a[96:99], a[72:75], v[40:43]\n" \
"ds_read_b128 a[196:199], v206 offset:12288\n" \
"v_mfma_f32_16x16x32_bf16 v[40:43], a[100:103], a[76:79], v[40:43]\n" \
"ds_read_b64_tr_b16 a[208:209], v209 offset:4096\n" \
"v_mfma_f32_16x16x32_bf16 v[0:3], a[104:107], a[72:75], v[0:3]\n" \
"ds_read_b64_tr_b16 a[210:211], v209 offset:5120\n" \
"v_mfma_f32_16x16x32_bf16 v[0:3], a[108:111], a[76:79], v[0:3]\n" \
"s_waitcnt lgkmcnt(3)\n" \
"global_load_lds_dwordx4 v253, s[48:49] offset:0\n" \
"v_mfma_f32_16x16x32_bf16 v[4:7], a[112:115], a[72:75], v[4:7]\n" \
"s_add_u32 s48, s48, 0x60000\n" \
"s_addc_u32 s49, s49, 0\n" \
"ds_read_b64_tr_b16 a[212:213], v209 offset:16384\n" \
"v_mfma_f32_16x16x32_bf16 v[4:7], a[116:119], a[76:79], v[4:7]\n" \
"ds_read_b64_tr_b16 a[214:215], v209 offset:17408\n" \
"v_mfma_f32_16x16x32_bf16 v[12:15], a[112:115], a[80:83], v[12:15]\n" \
"ds_read_b64_tr_b16 a[216:217], v210 offset:4096\n" \
"v_mfma_f32_16x16x32_bf16 v[12:15], a[116:119], a[84:87], v[12:15]\n" \
"global_load_lds_dwordx4 v253, s[50:51] offset:-3072\n" \
"v_mfma_f32_16x16x32_bf16 v[8:11], a[104:107], a[80:83], v[8:11]\n" \
"ds_read_b64_tr_b16 a[218:219], v210 offset:5120\n" \
"v_mfma_f32_16x16x32_bf16 v[8:11], a[108:111], a[84:87], v[8:11]\n" \
"ds_read_b64_tr_b16 a[220:221], v210 offset:16384\n" \
"v_mfma_f32_16x16x32_bf16 v[48:51], a[96:99], a[80:83], v[48:51]\n" \
"ds_read_b64_tr_b16 a[222:223], v210 offset:17408\n" \
"v_mfma_f32_16x16x32_bf16 v[48:51], a[100:103], a[84:87], v[48:51]\n" \
"global_load_lds_dwordx4 v253, s[50:51] offset:-1024\n" \
"v_mfma_f32_16x16x32_bf16 v[56:59], a[96:99], a[88:91], v[56:59]\n" \
"ds_read_b64_tr_b16 a[200:201], v210 offset:2048\n" \
"v_mfma_f32_16x16x32_bf16 v[56:59], a[100:103], a[92:95], v[56:59]\n" \
"ds_read_b64_tr_b16 a[202:203], v210 offset:3072\n" \
"v_mfma_f32_16x16x32_bf16 v[16:19], a[104:107], a[88:91], v[16:19]\n" \
"ds_read_b64_tr_b16 a[204:205], v210 offset:14336\n" \
"v_mfma_f32_16x16x32_bf16 v[16:19], a[108:111], a[92:95], v[16:19]\n" \
"global_load_lds_dwordx4 v253, s[50:51] offset:1024\n" \
"v_mfma_f32_16x16x32_bf16 v[20:23], a[112:115], a[88:91], v[20:23]\n" \
"s_add_u32 s50, s50, 0x60000\n" \
"s_addc_u32 s51, s51, 0\n" \
"ds_read_b64_tr_b16 a[206:207], v210 offset:15360\n" \
"v_mfma_f32_16x16x32_bf16 v[20:23], a[116:119], a[92:95], v[20:23]\n" \
"s_waitcnt lgkmcnt(3)\n" \
"v_add_u32 v207, 0xffff0000, v207\n" \
"v_add_u32 v208, 0xffff0000, v208\n" \
"v_mfma_f32_16x16x32_bf16 v[28:31], a[112:115], a[192:195], v[28:31]\n" \
"v_add_u32 v205, 0xffff0000, v205\n" \
"v_add_u32 v206, 0xffff0000, v206\n" \
"v_mfma_f32_16x16x32_bf16 v[28:31], a[116:119], a[196:199], v[28:31]\n" \
"v_add_u32 v209, 0xffff0000, v209\n" \
"v_add_u32 v210, 0xffff0000, v210\n" \
"v_mfma_f32_16x16x32_bf16 v[24:27], a[104:107], a[192:195], v[24:27]\n" \
"v_mfma_f32_16x16x32_bf16 v[24:27], a[108:111], a[196:199], v[24:27]\n" \
"s_waitcnt vmcnt(11) lgkmcnt(0)\n" \
"s_lshl_b32 s70, s63, 7\n" \
"s_lshl_b32 s71, s62, 2\n" \
"v_mfma_f32_16x16x32_bf16 v[64:67], a[96:99], a[192:195], v[64:67]\n" \
"s_add_u32 s71, s71, 65589248\n" \
"s_add_u32 s72, s71, s70\n" \
"s_sub_u32 s71, s71, s70\n" \
"v_mfma_f32_16x16x32_bf16 v[64:67], a[100:103], a[196:199], v[64:67]\n" \
"s_barrier\n" \
"v_mfma_f32_16x16x32_bf16 v[72:75], a[64:67], a[208:211], v[72:75]\n" \
"s_add_u32 s71, s71, 1920\n" \
"ds_read_b64_tr_b16 a[96:97], v209 offset:2048\n" \
"v_mov_b32 v236, s60\n" \
"v_mfma_f32_16x16x32_bf16 v[72:75], a[68:71], a[212:215], v[72:75]\n" \
"ds_read_b64_tr_b16 a[98:99], v209 offset:3072\n" \
"v_lshlrev_b32 v237, 28, s61\n" \
"v_mfma_f32_16x16x32_bf16 v[80:83], a[72:75], a[208:211], v[80:83]\n" \
"s_add_u32 m0, s95, 65536\n" \
"ds_read_b128 a[64:67], v207 offset:0\n" \
"ds_read_b64_tr_b16 a[100:101], v209 offset:14336\n" \
"v_mfma_f32_16x16x32_bf16 v[80:83], a[76:79], a[212:215], v[80:83]\n" \
"buffer_store_dword v236, v237, s[56:59], s72 offen sc1\n" \
"global_load_lds_dwordx4 v252, s[42:43] offset:0\n" \
"v_mfma_f32_16x16x32_bf16 v[84:87], a[72:75], a[216:219], v[84:87]\n" \
"ds_read_b128 a[68:71], v208 offset:0\n" \
"ds_read_b64_tr_b16 a[102:103], v209 offset:15360\n" \
"v_mfma_f32_16x16x32_bf16 v[84:87], a[76:79], a[220:223], v[84:87]\n" \
"buffer_store_dword v236, v237, s[56:59], s71 offen sc1\n" \
"ds_read_b64_tr_b16 a[104:105], v209 offset:0\n" \
"v_mfma_f32_16x16x32_bf16 v[44:47], a[200:203], a[72:75], v[44:47]\n" \
"ds_read_b64_tr_b16 a[106:107], v209 offset:1024\n" \
"v_mfma_f32_16x16x32_bf16 v[44:47], a[204:207], a[76:79], v[44:47]\n" \
"global_load_lds_dwordx4 v252, s[42:43] offset:2048\n" \
"v_mfma_f32_16x16x32_bf16 v[52:55], a[200:203], a[80:83], v[52:55]\n" \
"s_add_u32 s42, s42, 0x80\n" \
"s_addc_u32 s43, s43, 0\n" \
"ds_read_b128 a[72:75], v205 offset:0\n" \
"v_mfma_f32_16x16x32_bf16 v[52:55], a[204:207], a[84:87], v[52:55]\n" \
"ds_read_b128 a[76:79], v206 offset:0\n" \
"v_mfma_f32_16x16x32_bf16 v[92:95], a[80:83], a[216:219], v[92:95]\n" \
"ds_read_b64_tr_b16 a[108:109], v209 offset:12288\n" \
"v_mfma_f32_16x16x32_bf16 v[92:95], a[84:87], a[220:223], v[92:95]\n" \
"global_load_lds_dwordx4 v252, s[44:45] offset:1024\n" \
"v_mfma_f32_16x16x32_bf16 v[88:91], a[80:83], a[208:211], v[88:91]\n" \
"ds_read_b64_tr_b16 a[110:111], v209 offset:13312\n" \
"v_mfma_f32_16x16x32_bf16 v[88:91], a[84:87], a[212:215], v[88:91]\n" \
"ds_read_b64_tr_b16 a[112:113], v210 offset:0\n" \
"v_mfma_f32_16x16x32_bf16 v[96:99], a[88:91], a[208:211], v[96:99]\n" \
"ds_read_b128 a[80:83], v205 offset:4096\n" \
"v_mfma_f32_16x16x32_bf16 v[96:99], a[92:95], a[212:215], v[96:99]\n" \
"global_load_lds_dwordx4 v252, s[44:45] offset:3072\n" \
"v_mfma_f32_16x16x32_bf16 v[100:103], a[88:91], a[216:219], v[100:103]\n" \
"s_add_u32 s44, s44, 0x80\n" \
"s_addc_u32 s45, s45, 0\n" \
"ds_read_b128 a[84:87], v206 offset:4096\n" \
"v_mfma_f32_16x16x32_bf16 v[100:103], a[92:95], a[220:223], v[100:103]\n" \
"ds_read_b64_tr_b16 a[114:115], v210 offset:1024\n" \
"v_mfma_f32_16x16x32_bf16 v[60:63], a[200:203], a[88:91], v[60:63]\n" \
"s_add_u32 m0, s85, 81920\n" \
"ds_read_b64_tr_b16 a[116:117], v210 offset:12288\n" \
"v_mfma_f32_16x16x32_bf16 v[60:63], a[204:207], a[92:95], v[60:63]\n" \
"buffer_load_dwordx4 v246, s[12:15], s46 offen lds\n" \
"v_mfma_f32_16x16x32_bf16 v[68:71], a[200:203], a[192:195], v[68:71]\n" \
"ds_read_b128 a[88:91], v205 offset:8192\n" \
"s_add_u32 s46, s46, 0x80\n" \
"v_mfma_f32_16x16x32_bf16 v[68:71], a[204:207], a[196:199], v[68:71]\n" \
"ds_read_b128 a[92:95], v206 offset:8192\n" \
"v_mfma_f32_16x16x32_bf16 v[108:111], a[192:195], a[216:219], v[108:111]\n" \
"s_add_u32 m0, s84, 90112\n" \
"ds_read_b64_tr_b16 a[118:119], v210 offset:13312\n" \
"v_mfma_f32_16x16x32_bf16 v[108:111], a[196:199], a[220:223], v[108:111]\n" \
"global_load_lds_dwordx4 v253, s[48:49] offset:-4096\n" \
"v_mfma_f32_16x16x32_bf16 v[104:107], a[192:195], a[208:211], v[104:107]\n" \
"v_mfma_f32_16x16x32_bf16 v[104:107], a[196:199], a[212:215], v[104:107]\n" \
"s_waitcnt lgkmcnt(2)\n" \
"v_mfma_f32_16x16x32_bf16 v[32:35], a[96:99], a[64:67], v[32:35]\n" \
"s_lshr_b32 s89, s22, 7\n" \
"ds_read_b128 a[192:195], v205 offset:12288\n" \
"s_sub_u32 s89, 3, s89\n" \
"v_mfma_f32_16x16x32_bf16 v[32:35], a[100:103], a[68:71], v[32:35]\n" \
"global_load_lds_dwordx4 v253, s[48:49] offset:-2048\n" \
"v_bfe_u32 v203, v248, 7, 2\n" \
"v_mfma_f32_16x16x32_bf16 v[40:43], a[96:99], a[72:75], v[40:43]\n" \
"s_and_b32 s89, s89, 7\n" \
"ds_read_b128 a[196:199], v206 offset:12288\n" \
"s_and_b32 s90, %1, 1\n" \
"v_mfma_f32_16x16x32_bf16 v[40:43], a[100:103], a[76:79], v[40:43]\n" \
"ds_read_b64_tr_b16 a[208:209], v209 offset:4096\n" \
"v_bfe_u32 v204, v248, 5, 2\n" \
"v_mfma_f32_16x16x32_bf16 v[0:3], a[104:107], a[72:75], v[0:3]\n" \
"s_lshl_b32 s90, s90, 3\n" \
"s_add_u32 s89, s89, s90\n" \
"ds_read_b64_tr_b16 a[210:211], v209 offset:5120\n" \
"v_mfma_f32_16x16x32_bf16 v[0:3], a[108:111], a[76:79], v[0:3]\n" \
"s_waitcnt lgkmcnt(3)\n" \
"global_load_lds_dwordx4 v253, s[48:49] offset:0\n" \
"v_min_u32 v204, 2, v204\n" \
"v_mfma_f32_16x16x32_bf16 v[4:7], a[112:115], a[72:75], v[4:7]\n" \
"s_add_u32 s48, s48, 0x60000\n" \
"s_addc_u32 s49, s49, 0\n" \
"ds_read_b64_tr_b16 a[212:213], v209 offset:16384\n" \
"v_mfma_f32_16x16x32_bf16 v[4:7], a[116:119], a[76:79], v[4:7]\n" \
"s_lshl_b32 s89, s89, 7\n" \
"s_lshr_b32 s90, %1, 3\n" \
"ds_read_b64_tr_b16 a[214:215], v209 offset:17408\n" \
"v_mfma_f32_16x16x32_bf16 v[12:15], a[112:115], a[80:83], v[12:15]\n" \
"ds_read_b64_tr_b16 a[216:217], v210 offset:4096\n" \
"v_mad_u32_u24 v203, v203, 3, v204\n" \
"v_mfma_f32_16x16x32_bf16 v[12:15], a[116:119], a[84:87], v[12:15]\n" \
"s_lshl_b32 s90, s90, 2\n" \
"global_load_lds_dwordx4 v253, s[50:51] offset:-3072\n" \
"v_mfma_f32_16x16x32_bf16 v[8:11], a[104:107], a[80:83], v[8:11]\n" \
"s_add_u32 s90, s90, s61\n" \
"ds_read_b64_tr_b16 a[218:219], v210 offset:5120\n" \
"v_and_b32 v204, 16, v248\n" \
"v_mfma_f32_16x16x32_bf16 v[8:11], a[108:111], a[84:87], v[8:11]\n" \
"s_add_u32 s90, s90, 128\n" \
"s_mul_i32 s90, s90, 24576\n" \
"ds_read_b64_tr_b16 a[220:221], v210 offset:16384\n" \
"v_mfma_f32_16x16x32_bf16 v[48:51], a[96:99], a[80:83], v[48:51]\n" \
"s_add_u32 s89, s89, s90\n" \
"ds_read_b64_tr_b16 a[222:223], v210 offset:17408\n" \
"v_lshl_add_u32 v204, v204, 2, s89\n" \
"v_mfma_f32_16x16x32_bf16 v[48:51], a[100:103], a[84:87], v[48:51]\n" \
"global_load_lds_dwordx4 v253, s[50:51] offset:-1024\n" \
"v_lshl_add_u32 v203, v203, 11, v204\n" \
"v_mfma_f32_16x16x32_bf16 v[56:59], a[96:99], a[88:91], v[56:59]\n" \
"ds_read_b64_tr_b16 a[200:201], v210 offset:2048\n" \
"v_mfma_f32_16x16x32_bf16 v[56:59], a[100:103], a[92:95], v[56:59]\n" \
"ds_read_b64_tr_b16 a[202:203], v210 offset:3072\n" \
"v_mfma_f32_16x16x32_bf16 v[16:19], a[104:107], a[88:91], v[16:19]\n" \
"ds_read_b64_tr_b16 a[204:205], v210 offset:14336\n" \
"v_mfma_f32_16x16x32_bf16 v[16:19], a[108:111], a[92:95], v[16:19]\n" \
"global_load_lds_dwordx4 v253, s[50:51] offset:1024\n" \
"v_mfma_f32_16x16x32_bf16 v[20:23], a[112:115], a[88:91], v[20:23]\n" \
"s_add_u32 s50, s50, 0x60000\n" \
"s_addc_u32 s51, s51, 0\n" \
"ds_read_b64_tr_b16 a[206:207], v210 offset:15360\n" \
"v_mfma_f32_16x16x32_bf16 v[20:23], a[116:119], a[92:95], v[20:23]\n" \
"s_waitcnt lgkmcnt(3)\n" \
"v_add_u32 v207, 0x1d000, v207\n" \
"v_add_u32 v208, 0x1d000, v208\n" \
"v_mfma_f32_16x16x32_bf16 v[28:31], a[112:115], a[192:195], v[28:31]\n" \
"v_add_u32 v205, 0x1d000, v205\n" \
"v_add_u32 v206, 0x1d000, v206\n" \
"v_mfma_f32_16x16x32_bf16 v[28:31], a[116:119], a[196:199], v[28:31]\n" \
"v_add_u32 v209, 0x1d000, v209\n" \
"v_add_u32 v210, 0x1d000, v210\n" \
"v_mfma_f32_16x16x32_bf16 v[24:27], a[104:107], a[192:195], v[24:27]\n" \
"v_mfma_f32_16x16x32_bf16 v[24:27], a[108:111], a[196:199], v[24:27]\n" \
"s_waitcnt vmcnt(13) lgkmcnt(0)\n" \
"v_mfma_f32_16x16x32_bf16 v[64:67], a[96:99], a[192:195], v[64:67]\n" \
"v_mfma_f32_16x16x32_bf16 v[64:67], a[100:103], a[196:199], v[64:67]\n" \
"s_barrier\n" \
"v_mfma_f32_16x16x32_bf16 v[72:75], a[64:67], a[208:211], v[72:75]\n" \
"ds_read_b64_tr_b16 a[96:97], v209 offset:2048\n" \
"v_mfma_f32_16x16x32_bf16 v[72:75], a[68:71], a[212:215], v[72:75]\n" \
"ds_read_b64_tr_b16 a[98:99], v209 offset:3072\n" \
"v_mfma_f32_16x16x32_bf16 v[80:83], a[72:75], a[208:211], v[80:83]\n" \
"s_add_u32 m0, s95, 0\n" \
"ds_read_b128 a[64:67], v207 offset:0\n" \
"ds_read_b64_tr_b16 a[100:101], v209 offset:14336\n" \
"v_mfma_f32_16x16x32_bf16 v[80:83], a[76:79], a[212:215], v[80:83]\n" \
"global_load_lds_dwordx4 v252, s[42:43] offset:0\n" \
"v_mfma_f32_16x16x32_bf16 v[84:87], a[72:75], a[216:219], v[84:87]\n" \
"ds_read_b128 a[68:71], v208 offset:0\n" \
"ds_read_b64_tr_b16 a[102:103], v209 offset:15360\n" \
"v_mfma_f32_16x16x32_bf16 v[84:87], a[76:79], a[220:223], v[84:87]\n" \
"ds_read_b64_tr_b16 a[104:105], v209 offset:0\n" \
"v_mfma_f32_16x16x32_bf16 v[44:47], a[200:203], a[72:75], v[44:47]\n" \
"ds_read_b64_tr_b16 a[106:107], v209 offset:1024\n" \
"v_mfma_f32_16x16x32_bf16 v[44:47], a[204:207], a[76:79], v[44:47]\n" \
"global_load_lds_dwordx4 v252, s[42:43] offset:2048\n" \
"v_mfma_f32_16x16x32_bf16 v[52:55], a[200:203], a[80:83], v[52:55]\n" \
"s_add_u32 s42, s42, 0x80\n" \
"s_addc_u32 s43, s43, 0\n" \
"ds_read_b128 a[72:75], v205 offset:0\n" \
"v_mfma_f32_16x16x32_bf16 v[52:55], a[204:207], a[84:87], v[52:55]\n" \
"ds_read_b128 a[76:79], v206 offset:0\n" \
"v_mfma_f32_16x16x32_bf16 v[92:95], a[80:83], a[216:219], v[92:95]\n" \
"ds_read_b64_tr_b16 a[108:109], v209 offset:12288\n" \
"v_mfma_f32_16x16x32_bf16 v[92:95], a[84:87], a[220:223], v[92:95]\n" \
"global_load_lds_dwordx4 v252, s[44:45] offset:1024\n" \
"v_mfma_f32_16x16x32_bf16 v[88:91], a[80:83], a[208:211], v[88:91]\n" \
"ds_read_b64_tr_b16 a[110:111], v209 offset:13312\n" \
"v_mfma_f32_16x16x32_bf16 v[88:91], a[84:87], a[212:215], v[88:91]\n" \
"ds_read_b64_tr_b16 a[112:113], v210 offset:0\n" \
"v_mfma_f32_16x16x32_bf16 v[96:99], a[88:91], a[208:211], v[96:99]\n" \
"ds_read_b128 a[80:83], v205 offset:4096\n" \
"v_mfma_f32_16x16x32_bf16 v[96:99], a[92:95], a[212:215], v[96:99]\n" \
"global_load_lds_dwordx4 v252, s[44:45] offset:3072\n" \
"v_mfma_f32_16x16x32_bf16 v[100:103], a[88:91], a[216:219], v[100:103]\n" \
"s_add_u32 s44, s44, 0x80\n" \
"s_addc_u32 s45, s45, 0\n" \
"ds_read_b128 a[84:87], v206 offset:4096\n" \
"v_mfma_f32_16x16x32_bf16 v[100:103], a[92:95], a[220:223], v[100:103]\n" \
"ds_read_b64_tr_b16 a[114:115], v210 offset:1024\n" \
"v_mfma_f32_16x16x32_bf16 v[60:63], a[200:203], a[88:91], v[60:63]\n" \
"s_add_u32 m0, s85, 16384\n" \
"ds_read_b64_tr_b16 a[116:117], v210 offset:12288\n" \
"v_mfma_f32_16x16x32_bf16 v[60:63], a[204:207], a[92:95], v[60:63]\n" \
"buffer_load_dwordx4 v246, s[12:15], s46 offen lds\n" \
"v_mfma_f32_16x16x32_bf16 v[68:71], a[200:203], a[192:195], v[68:71]\n" \
"ds_read_b128 a[88:91], v205 offset:8192\n" \
"s_add_u32 s46, s46, 0x80\n" \
"v_mfma_f32_16x16x32_bf16 v[68:71], a[204:207], a[196:199], v[68:71]\n" \
"ds_read_b128 a[92:95], v206 offset:8192\n" \
"v_mfma_f32_16x16x32_bf16 v[108:111], a[192:195], a[216:219], v[108:111]\n" \
"s_add_u32 m0, s84, 24576\n" \
"ds_read_b64_tr_b16 a[118:119], v210 offset:13312\n" \
"v_mfma_f32_16x16x32_bf16 v[108:111], a[196:199], a[220:223], v[108:111]\n" \
"global_load_lds_dwordx4 v253, s[48:49] offset:-4096\n" \
"v_mfma_f32_16x16x32_bf16 v[104:107], a[192:195], a[208:211], v[104:107]\n" \
"v_mfma_f32_16x16x32_bf16 v[104:107], a[196:199], a[212:215], v[104:107]\n" \
"s_waitcnt lgkmcnt(2)\n" \
"v_mfma_f32_16x16x32_bf16 v[32:35], a[96:99], a[64:67], v[32:35]\n" \
"s_mov_b32 s30, 65593856\n" \
"ds_read_b128 a[192:195], v205 offset:12288\n" \
"v_lshrrev_b32 v228, 1, v248\n" \
"v_mfma_f32_16x16x32_bf16 v[32:35], a[100:103], a[68:71], v[32:35]\n" \
"global_load_lds_dwordx4 v253, s[48:49] offset:-2048\n" \
"v_mfma_f32_16x16x32_bf16 v[40:43], a[96:99], a[72:75], v[40:43]\n" \
"ds_read_b128 a[196:199], v206 offset:12288\n" \
"v_mfma_f32_16x16x32_bf16 v[40:43], a[100:103], a[76:79], v[40:43]\n" \
"ds_read_b64_tr_b16 a[208:209], v209 offset:4096\n" \
"v_mfma_f32_16x16x32_bf16 v[0:3], a[104:107], a[72:75], v[0:3]\n" \
"ds_read_b64_tr_b16 a[210:211], v209 offset:5120\n" \
"v_mfma_f32_16x16x32_bf16 v[0:3], a[108:111], a[76:79], v[0:3]\n" \
"s_waitcnt lgkmcnt(3)\n" \
"global_load_lds_dwordx4 v253, s[48:49] offset:0\n" \
"v_mfma_f32_16x16x32_bf16 v[4:7], a[112:115], a[72:75], v[4:7]\n" \
"s_add_u32 s48, s48, 0x60000\n" \
"s_addc_u32 s49, s49, 0\n" \
"ds_read_b64_tr_b16 a[212:213], v209 offset:16384\n" \
"v_mfma_f32_16x16x32_bf16 v[4:7], a[116:119], a[76:79], v[4:7]\n" \
"ds_read_b64_tr_b16 a[214:215], v209 offset:17408\n" \
"v_mfma_f32_16x16x32_bf16 v[12:15], a[112:115], a[80:83], v[12:15]\n" \
"ds_read_b64_tr_b16 a[216:217], v210 offset:4096\n" \
"v_mfma_f32_16x16x32_bf16 v[12:15], a[116:119], a[84:87], v[12:15]\n" \
"global_load_lds_dwordx4 v253, s[50:51] offset:-3072\n" \
"v_mfma_f32_16x16x32_bf16 v[8:11], a[104:107], a[80:83], v[8:11]\n" \
"ds_read_b64_tr_b16 a[218:219], v210 offset:5120\n" \
"v_mfma_f32_16x16x32_bf16 v[8:11], a[108:111], a[84:87], v[8:11]\n" \
"ds_read_b64_tr_b16 a[220:221], v210 offset:16384\n" \
"v_mfma_f32_16x16x32_bf16 v[48:51], a[96:99], a[80:83], v[48:51]\n" \
"ds_read_b64_tr_b16 a[222:223], v210 offset:17408\n" \
"v_mfma_f32_16x16x32_bf16 v[48:51], a[100:103], a[84:87], v[48:51]\n" \
"global_load_lds_dwordx4 v253, s[50:51] offset:-1024\n" \
"v_mfma_f32_16x16x32_bf16 v[56:59], a[96:99], a[88:91], v[56:59]\n" \
"ds_read_b64_tr_b16 a[200:201], v210 offset:2048\n" \
"v_mfma_f32_16x16x32_bf16 v[56:59], a[100:103], a[92:95], v[56:59]\n" \
"ds_read_b64_tr_b16 a[202:203], v210 offset:3072\n" \
"v_mfma_f32_16x16x32_bf16 v[16:19], a[104:107], a[88:91], v[16:19]\n" \
"ds_read_b64_tr_b16 a[204:205], v210 offset:14336\n" \
"v_mfma_f32_16x16x32_bf16 v[16:19], a[108:111], a[92:95], v[16:19]\n" \
"global_load_lds_dwordx4 v253, s[50:51] offset:1024\n" \
"v_mfma_f32_16x16x32_bf16 v[20:23], a[112:115], a[88:91], v[20:23]\n" \
"s_add_u32 s50, s50, 0x60000\n" \
"s_addc_u32 s51, s51, 0\n" \
"ds_read_b64_tr_b16 a[206:207], v210 offset:15360\n" \
"v_mfma_f32_16x16x32_bf16 v[20:23], a[116:119], a[92:95], v[20:23]\n" \
"s_waitcnt lgkmcnt(3)\n" \
"v_add_u32 v207, 0xffff3000, v207\n" \
"v_add_u32 v208, 0xffff3000, v208\n" \
"v_mfma_f32_16x16x32_bf16 v[28:31], a[112:115], a[192:195], v[28:31]\n" \
"v_add_u32 v205, 0xffff3000, v205\n" \
"v_add_u32 v206, 0xffff3000, v206\n" \
"v_mfma_f32_16x16x32_bf16 v[28:31], a[116:119], a[196:199], v[28:31]\n" \
"global_load_dword v204, v203, s[22:23]\n" \
"v_mfma_f32_16x16x32_bf16 v[24:27], a[104:107], a[192:195], v[24:27]\n" \
"v_add_u32 v209, 0xffff3000, v209\n" \
"v_add_u32 v210, 0xffff3000, v210\n" \
"v_mfma_f32_16x16x32_bf16 v[24:27], a[108:111], a[196:199], v[24:27]\n" \
"s_waitcnt vmcnt(12) lgkmcnt(0)\n" \
"v_mfma_f32_16x16x32_bf16 v[64:67], a[96:99], a[192:195], v[64:67]\n" \
"v_mfma_f32_16x16x32_bf16 v[64:67], a[100:103], a[196:199], v[64:67]\n" \
"s_barrier\n" \
"v_mfma_f32_16x16x32_bf16 v[72:75], a[64:67], a[208:211], v[72:75]\n" \
"ds_read_b64_tr_b16 a[96:97], v209 offset:2048\n" \
"v_mfma_f32_16x16x32_bf16 v[72:75], a[68:71], a[212:215], v[72:75]\n" \
"ds_read_b64_tr_b16 a[98:99], v209 offset:3072\n" \
"v_mfma_f32_16x16x32_bf16 v[80:83], a[72:75], a[208:211], v[80:83]\n" \
"s_add_u32 m0, s95, 118784\n" \
"ds_read_b128 a[64:67], v207 offset:0\n" \
"ds_read_b64_tr_b16 a[100:101], v209 offset:14336\n" \
"v_mfma_f32_16x16x32_bf16 v[80:83], a[76:79], a[212:215], v[80:83]\n" \
"global_load_lds_dwordx4 v252, s[42:43] offset:0\n" \
"v_mfma_f32_16x16x32_bf16 v[84:87], a[72:75], a[216:219], v[84:87]\n" \
"ds_read_b128 a[68:71], v208 offset:0\n" \
"ds_read_b64_tr_b16 a[102:103], v209 offset:15360\n" \
"v_mfma_f32_16x16x32_bf16 v[84:87], a[76:79], a[220:223], v[84:87]\n" \
"ds_read_b64_tr_b16 a[104:105], v209 offset:0\n" \
"v_mfma_f32_16x16x32_bf16 v[44:47], a[200:203], a[72:75], v[44:47]\n" \
"ds_read_b64_tr_b16 a[106:107], v209 offset:1024\n" \
"v_mfma_f32_16x16x32_bf16 v[44:47], a[204:207], a[76:79], v[44:47]\n" \
"global_load_lds_dwordx4 v252, s[42:43] offset:2048\n" \
"v_mfma_f32_16x16x32_bf16 v[52:55], a[200:203], a[80:83], v[52:55]\n" \
"s_add_u32 s42, s42, 0x80\n" \
"s_addc_u32 s43, s43, 0\n" \
"ds_read_b128 a[72:75], v205 offset:0\n" \
"v_mfma_f32_16x16x32_bf16 v[52:55], a[204:207], a[84:87], v[52:55]\n" \
"ds_read_b128 a[76:79], v206 offset:0\n" \
"v_mfma_f32_16x16x32_bf16 v[92:95], a[80:83], a[216:219], v[92:95]\n" \
"ds_read_b64_tr_b16 a[108:109], v209 offset:12288\n" \
"v_mfma_f32_16x16x32_bf16 v[92:95], a[84:87], a[220:223], v[92:95]\n" \
"global_load_lds_dwordx4 v252, s[44:45] offset:1024\n" \
"v_mfma_f32_16x16x32_bf16 v[88:91], a[80:83], a[208:211], v[88:91]\n" \
"ds_read_b64_tr_b16 a[110:111], v209 offset:13312\n" \
"v_mfma_f32_16x16x32_bf16 v[88:91], a[84:87], a[212:215], v[88:91]\n" \
"ds_read_b64_tr_b16 a[112:113], v210 offset:0\n" \
"v_mfma_f32_16x16x32_bf16 v[96:99], a[88:91], a[208:211], v[96:99]\n" \
"ds_read_b128 a[80:83], v205 offset:4096\n" \
"v_mfma_f32_16x16x32_bf16 v[96:99], a[92:95], a[212:215], v[96:99]\n" \
"global_load_lds_dwordx4 v252, s[44:45] offset:3072\n" \
"v_mfma_f32_16x16x32_bf16 v[100:103], a[88:91], a[216:219], v[100:103]\n" \
"s_add_u32 s44, s44, 0x80\n" \
"s_addc_u32 s45, s45, 0\n" \
"ds_read_b128 a[84:87], v206 offset:4096\n" \
"v_mfma_f32_16x16x32_bf16 v[100:103], a[92:95], a[220:223], v[100:103]\n" \
"ds_read_b64_tr_b16 a[114:115], v210 offset:1024\n" \
"v_mfma_f32_16x16x32_bf16 v[60:63], a[200:203], a[88:91], v[60:63]\n" \
"s_add_u32 m0, s85, 135168\n" \
"ds_read_b64_tr_b16 a[116:117], v210 offset:12288\n" \
"v_mfma_f32_16x16x32_bf16 v[60:63], a[204:207], a[92:95], v[60:63]\n" \
"buffer_load_dwordx4 v246, s[12:15], s46 offen lds\n" \
"v_mfma_f32_16x16x32_bf16 v[68:71], a[200:203], a[192:195], v[68:71]\n" \
"ds_read_b128 a[88:91], v205 offset:8192\n" \
"s_add_u32 s46, s46, 0x80\n" \
"v_mfma_f32_16x16x32_bf16 v[68:71], a[204:207], a[196:199], v[68:71]\n" \
"ds_read_b128 a[92:95], v206 offset:8192\n" \
"v_mfma_f32_16x16x32_bf16 v[108:111], a[192:195], a[216:219], v[108:111]\n" \
"s_add_u32 m0, s84, 143360\n" \
"ds_read_b64_tr_b16 a[118:119], v210 offset:13312\n" \
"v_mfma_f32_16x16x32_bf16 v[108:111], a[196:199], a[220:223], v[108:111]\n" \
"global_load_lds_dwordx4 v253, s[48:49] offset:-4096\n" \
"v_mfma_f32_16x16x32_bf16 v[104:107], a[192:195], a[208:211], v[104:107]\n" \
"v_mfma_f32_16x16x32_bf16 v[104:107], a[196:199], a[212:215], v[104:107]\n" \
"s_waitcnt lgkmcnt(2)\n" \
"v_mfma_f32_16x16x32_bf16 v[32:35], a[96:99], a[64:67], v[32:35]\n" \
"ds_read_b128 a[192:195], v205 offset:12288\n" \
"v_mfma_f32_16x16x32_bf16 v[32:35], a[100:103], a[68:71], v[32:35]\n" \
"global_load_lds_dwordx4 v253, s[48:49] offset:-2048\n" \
"v_mfma_f32_16x16x32_bf16 v[40:43], a[96:99], a[72:75], v[40:43]\n" \
"ds_read_b128 a[196:199], v206 offset:12288\n" \
"v_mfma_f32_16x16x32_bf16 v[40:43], a[100:103], a[76:79], v[40:43]\n" \
"Lpg9734_%=:\n" \
"s_cbranch_execz Lpg12014_%=\n" \
"ds_read_b64_tr_b16 a[208:209], v209 offset:4096\n" \
"v_mfma_f32_16x16x32_bf16 v[0:3], a[104:107], a[72:75], v[0:3]\n" \
"ds_read_b64_tr_b16 a[210:211], v209 offset:5120\n" \
"v_mfma_f32_16x16x32_bf16 v[0:3], a[108:111], a[76:79], v[0:3]\n" \
"s_waitcnt lgkmcnt(3)\n" \
"global_load_lds_dwordx4 v253, s[48:49] offset:0\n" \
"v_mfma_f32_16x16x32_bf16 v[4:7], a[112:115], a[72:75], v[4:7]\n" \
"s_add_u32 s48, s48, 0x60000\n" \
"s_addc_u32 s49, s49, 0\n" \
"ds_read_b64_tr_b16 a[212:213], v209 offset:16384\n" \
"v_mfma_f32_16x16x32_bf16 v[4:7], a[116:119], a[76:79], v[4:7]\n" \
"ds_read_b64_tr_b16 a[214:215], v209 offset:17408\n" \
"v_mfma_f32_16x16x32_bf16 v[12:15], a[112:115], a[80:83], v[12:15]\n" \
"ds_read_b64_tr_b16 a[216:217], v210 offset:4096\n" \
"v_mfma_f32_16x16x32_bf16 v[12:15], a[116:119], a[84:87], v[12:15]\n" \
"global_load_lds_dwordx4 v253, s[50:51] offset:-3072\n" \
"v_mfma_f32_16x16x32_bf16 v[8:11], a[104:107], a[80:83], v[8:11]\n" \
"ds_read_b64_tr_b16 a[218:219], v210 offset:5120\n" \
"v_mfma_f32_16x16x32_bf16 v[8:11], a[108:111], a[84:87], v[8:11]\n" \
"ds_read_b64_tr_b16 a[220:221], v210 offset:16384\n" \
"v_mfma_f32_16x16x32_bf16 v[48:51], a[96:99], a[80:83], v[48:51]\n" \
"ds_read_b64_tr_b16 a[222:223], v210 offset:17408\n" \
"v_mfma_f32_16x16x32_bf16 v[48:51], a[100:103], a[84:87], v[48:51]\n" \
"global_load_lds_dwordx4 v253, s[50:51] offset:-1024\n" \
"v_mfma_f32_16x16x32_bf16 v[56:59], a[96:99], a[88:91], v[56:59]\n" \
"ds_read_b64_tr_b16 a[200:201], v210 offset:2048\n" \
"v_mfma_f32_16x16x32_bf16 v[56:59], a[100:103], a[92:95], v[56:59]\n" \
"ds_read_b64_tr_b16 a[202:203], v210 offset:3072\n" \
"v_mfma_f32_16x16x32_bf16 v[16:19], a[104:107], a[88:91], v[16:19]\n" \
"ds_read_b64_tr_b16 a[204:205], v210 offset:14336\n" \
"v_mfma_f32_16x16x32_bf16 v[16:19], a[108:111], a[92:95], v[16:19]\n" \
"global_load_lds_dwordx4 v253, s[50:51] offset:1024\n" \
"v_mfma_f32_16x16x32_bf16 v[20:23], a[112:115], a[88:91], v[20:23]\n" \
"s_add_u32 s50, s50, 0x60000\n" \
"s_addc_u32 s51, s51, 0\n" \
"ds_read_b64_tr_b16 a[206:207], v210 offset:15360\n" \
"v_mfma_f32_16x16x32_bf16 v[20:23], a[116:119], a[92:95], v[20:23]\n" \
"s_waitcnt lgkmcnt(3)\n" \
"v_add_u32 v207, 0xffff0000, v207\n" \
"v_add_u32 v208, 0xffff0000, v208\n" \
"v_mfma_f32_16x16x32_bf16 v[28:31], a[112:115], a[192:195], v[28:31]\n" \
"v_add_u32 v205, 0xffff0000, v205\n" \
"v_add_u32 v206, 0xffff0000, v206\n" \
"v_mfma_f32_16x16x32_bf16 v[28:31], a[116:119], a[196:199], v[28:31]\n" \
"buffer_load_dwordx2 v[226:227], v228, s[56:59], s30 offen sc1\n" \
"v_mfma_f32_16x16x32_bf16 v[24:27], a[104:107], a[192:195], v[24:27]\n" \
"v_add_u32 v209, 0xffff0000, v209\n" \
"v_add_u32 v210, 0xffff0000, v210\n" \
"v_mfma_f32_16x16x32_bf16 v[24:27], a[108:111], a[196:199], v[24:27]\n" \
"s_waitcnt vmcnt(13) lgkmcnt(0)\n" \
"v_mfma_f32_16x16x32_bf16 v[64:67], a[96:99], a[192:195], v[64:67]\n" \
"v_mfma_f32_16x16x32_bf16 v[64:67], a[100:103], a[196:199], v[64:67]\n" \
"s_barrier\n" \
"v_mfma_f32_16x16x32_bf16 v[72:75], a[64:67], a[208:211], v[72:75]\n" \
"ds_read_b64_tr_b16 a[96:97], v209 offset:2048\n" \
"v_mfma_f32_16x16x32_bf16 v[72:75], a[68:71], a[212:215], v[72:75]\n" \
"ds_read_b64_tr_b16 a[98:99], v209 offset:3072\n" \
"v_mfma_f32_16x16x32_bf16 v[80:83], a[72:75], a[208:211], v[80:83]\n" \
"s_add_u32 m0, s95, 65536\n" \
"ds_read_b128 a[64:67], v207 offset:0\n" \
"ds_read_b64_tr_b16 a[100:101], v209 offset:14336\n" \
"v_mfma_f32_16x16x32_bf16 v[80:83], a[76:79], a[212:215], v[80:83]\n" \
"global_load_lds_dwordx4 v252, s[42:43] offset:0\n" \
"v_mfma_f32_16x16x32_bf16 v[84:87], a[72:75], a[216:219], v[84:87]\n" \
"ds_read_b128 a[68:71], v208 offset:0\n" \
"ds_read_b64_tr_b16 a[102:103], v209 offset:15360\n" \
"v_mfma_f32_16x16x32_bf16 v[84:87], a[76:79], a[220:223], v[84:87]\n" \
"ds_read_b64_tr_b16 a[104:105], v209 offset:0\n" \
"v_mfma_f32_16x16x32_bf16 v[44:47], a[200:203], a[72:75], v[44:47]\n" \
"ds_read_b64_tr_b16 a[106:107], v209 offset:1024\n" \
"v_mfma_f32_16x16x32_bf16 v[44:47], a[204:207], a[76:79], v[44:47]\n" \
"global_load_lds_dwordx4 v252, s[42:43] offset:2048\n" \
"v_mfma_f32_16x16x32_bf16 v[52:55], a[200:203], a[80:83], v[52:55]\n" \
"s_add_u32 s42, s42, 0x80\n" \
"s_addc_u32 s43, s43, 0\n" \
"ds_read_b128 a[72:75], v205 offset:0\n" \
"v_mfma_f32_16x16x32_bf16 v[52:55], a[204:207], a[84:87], v[52:55]\n" \
"ds_read_b128 a[76:79], v206 offset:0\n" \
"v_mfma_f32_16x16x32_bf16 v[92:95], a[80:83], a[216:219], v[92:95]\n" \
"ds_read_b64_tr_b16 a[108:109], v209 offset:12288\n" \
"v_mfma_f32_16x16x32_bf16 v[92:95], a[84:87], a[220:223], v[92:95]\n" \
"global_load_lds_dwordx4 v252, s[44:45] offset:1024\n" \
"v_mfma_f32_16x16x32_bf16 v[88:91], a[80:83], a[208:211], v[88:91]\n" \
"ds_read_b64_tr_b16 a[110:111], v209 offset:13312\n" \
"v_mfma_f32_16x16x32_bf16 v[88:91], a[84:87], a[212:215], v[88:91]\n" \
"ds_read_b64_tr_b16 a[112:113], v210 offset:0\n" \
"v_mfma_f32_16x16x32_bf16 v[96:99], a[88:91], a[208:211], v[96:99]\n" \
"ds_read_b128 a[80:83], v205 offset:4096\n" \
"v_mfma_f32_16x16x32_bf16 v[96:99], a[92:95], a[212:215], v[96:99]\n" \
"global_load_lds_dwordx4 v252, s[44:45] offset:3072\n" \
"v_mfma_f32_16x16x32_bf16 v[100:103], a[88:91], a[216:219], v[100:103]\n" \
"s_add_u32 s44, s44, 0x80\n" \
"s_addc_u32 s45, s45, 0\n" \
"ds_read_b128 a[84:87], v206 offset:4096\n" \
"v_mfma_f32_16x16x32_bf16 v[100:103], a[92:95], a[220:223], v[100:103]\n" \
"ds_read_b64_tr_b16 a[114:115], v210 offset:1024\n" \
"v_mfma_f32_16x16x32_bf16 v[60:63], a[200:203], a[88:91], v[60:63]\n" \
"s_add_u32 m0, s85, 81920\n" \
"ds_read_b64_tr_b16 a[116:117], v210 offset:12288\n" \
"v_mfma_f32_16x16x32_bf16 v[60:63], a[204:207], a[92:95], v[60:63]\n" \
"buffer_load_dwordx4 v246, s[12:15], s46 offen lds\n" \
"v_mfma_f32_16x16x32_bf16 v[68:71], a[200:203], a[192:195], v[68:71]\n" \
"ds_read_b128 a[88:91], v205 offset:8192\n" \
"s_add_u32 s46, s46, 0x80\n" \
"v_mfma_f32_16x16x32_bf16 v[68:71], a[204:207], a[196:199], v[68:71]\n" \
"ds_read_b128 a[92:95], v206 offset:8192\n" \
"v_mfma_f32_16x16x32_bf16 v[108:111], a[192:195], a[216:219], v[108:111]\n" \
"s_add_u32 m0, s84, 90112\n" \
"ds_read_b64_tr_b16 a[118:119], v210 offset:13312\n" \
"v_mfma_f32_16x16x32_bf16 v[108:111], a[196:199], a[220:223], v[108:111]\n" \
"global_load_lds_dwordx4 v253, s[48:49] offset:-4096\n" \
"v_mfma_f32_16x16x32_bf16 v[104:107], a[192:195], a[208:211], v[104:107]\n" \
"v_mfma_f32_16x16x32_bf16 v[104:107], a[196:199], a[212:215], v[104:107]\n" \
"s_waitcnt lgkmcnt(2)\n" \
"v_mfma_f32_16x16x32_bf16 v[32:35], a[96:99], a[64:67], v[32:35]\n" \
"ds_read_b128 a[192:195], v205 offset:12288\n" \
"v_mfma_f32_16x16x32_bf16 v[32:35], a[100:103], a[68:71], v[32:35]\n" \
"global_load_lds_dwordx4 v253, s[48:49] offset:-2048\n" \
"v_mfma_f32_16x16x32_bf16 v[40:43], a[96:99], a[72:75], v[40:43]\n" \
"ds_read_b128 a[196:199], v206 offset:12288\n" \
"v_mfma_f32_16x16x32_bf16 v[40:43], a[100:103], a[76:79], v[40:43]\n" \
"ds_read_b64_tr_b16 a[208:209], v209 offset:4096\n" \
"v_mfma_f32_16x16x32_bf16 v[0:3], a[104:107], a[72:75], v[0:3]\n" \
"ds_read_b64_tr_b16 a[210:211], v209 offset:5120\n" \
"v_mfma_f32_16x16x32_bf16 v[0:3], a[108:111], a[76:79], v[0:3]\n" \
"s_waitcnt lgkmcnt(3)\n" \
"global_load_lds_dwordx4 v253, s[48:49] offset:0\n" \
"v_mfma_f32_16x16x32_bf16 v[4:7], a[112:115], a[72:75], v[4:7]\n" \
"s_add_u32 s48, s48, 0x60000\n" \
"s_addc_u32 s49, s49, 0\n" \
"ds_read_b64_tr_b16 a[212:213], v209 offset:16384\n" \
"v_mfma_f32_16x16x32_bf16 v[4:7], a[116:119], a[76:79], v[4:7]\n" \
"ds_read_b64_tr_b16 a[214:215], v209 offset:17408\n" \
"v_mfma_f32_16x16x32_bf16 v[12:15], a[112:115], a[80:83], v[12:15]\n" \
"ds_read_b64_tr_b16 a[216:217], v210 offset:4096\n" \
"v_mfma_f32_16x16x32_bf16 v[12:15], a[116:119], a[84:87], v[12:15]\n" \
"global_load_lds_dwordx4 v253, s[50:51] offset:-3072\n" \
"v_mfma_f32_16x16x32_bf16 v[8:11], a[104:107], a[80:83], v[8:11]\n" \
"ds_read_b64_tr_b16 a[218:219], v210 offset:5120\n" \
"v_mfma_f32_16x16x32_bf16 v[8:11], a[108:111], a[84:87], v[8:11]\n" \
"ds_read_b64_tr_b16 a[220:221], v210 offset:16384\n" \
"v_mfma_f32_16x16x32_bf16 v[48:51], a[96:99], a[80:83], v[48:51]\n" \
"ds_read_b64_tr_b16 a[222:223], v210 offset:17408\n" \
"v_mfma_f32_16x16x32_bf16 v[48:51], a[100:103], a[84:87], v[48:51]\n" \
"global_load_lds_dwordx4 v253, s[50:51] offset:-1024\n" \
"v_mfma_f32_16x16x32_bf16 v[56:59], a[96:99], a[88:91], v[56:59]\n" \
"ds_read_b64_tr_b16 a[200:201], v210 offset:2048\n" \
"v_mfma_f32_16x16x32_bf16 v[56:59], a[100:103], a[92:95], v[56:59]\n" \
"ds_read_b64_tr_b16 a[202:203], v210 offset:3072\n" \
"v_mfma_f32_16x16x32_bf16 v[16:19], a[104:107], a[88:91], v[16:19]\n" \
"ds_read_b64_tr_b16 a[204:205], v210 offset:14336\n" \
"v_mfma_f32_16x16x32_bf16 v[16:19], a[108:111], a[92:95], v[16:19]\n" \
"global_load_lds_dwordx4 v253, s[50:51] offset:1024\n" \
"v_mfma_f32_16x16x32_bf16 v[20:23], a[112:115], a[88:91], v[20:23]\n" \
"s_add_u32 s50, s50, 0x60000\n" \
"s_addc_u32 s51, s51, 0\n" \
"ds_read_b64_tr_b16 a[206:207], v210 offset:15360\n" \
"v_mfma_f32_16x16x32_bf16 v[20:23], a[116:119], a[92:95], v[20:23]\n" \
"s_waitcnt lgkmcnt(3)\n" \
"v_add_u32 v207, 0x1d000, v207\n" \
"v_add_u32 v208, 0x1d000, v208\n" \
"v_mfma_f32_16x16x32_bf16 v[28:31], a[112:115], a[192:195], v[28:31]\n" \
"v_add_u32 v205, 0x1d000, v205\n" \
"v_add_u32 v206, 0x1d000, v206\n" \
"v_mfma_f32_16x16x32_bf16 v[28:31], a[116:119], a[196:199], v[28:31]\n" \
"v_add_u32 v209, 0x1d000, v209\n" \
"v_add_u32 v210, 0x1d000, v210\n" \
"v_mfma_f32_16x16x32_bf16 v[24:27], a[104:107], a[192:195], v[24:27]\n" \
"v_mfma_f32_16x16x32_bf16 v[24:27], a[108:111], a[196:199], v[24:27]\n" \
"s_waitcnt vmcnt(12) lgkmcnt(0)\n" \
"v_mfma_f32_16x16x32_bf16 v[64:67], a[96:99], a[192:195], v[64:67]\n" \
"v_mfma_f32_16x16x32_bf16 v[64:67], a[100:103], a[196:199], v[64:67]\n" \
"s_barrier\n" \
"v_mfma_f32_16x16x32_bf16 v[72:75], a[64:67], a[208:211], v[72:75]\n" \
"ds_read_b64_tr_b16 a[96:97], v209 offset:2048\n" \
"s_waitcnt vmcnt(11)\n" \
"v_mfma_f32_16x16x32_bf16 v[72:75], a[68:71], a[212:215], v[72:75]\n" \
"ds_read_b64_tr_b16 a[98:99], v209 offset:3072\n" \
"v_mfma_f32_16x16x32_bf16 v[80:83], a[72:75], a[208:211], v[80:83]\n" \
"s_add_u32 m0, s95, 0\n" \
"ds_read_b128 a[64:67], v207 offset:0\n" \
"ds_read_b64_tr_b16 a[100:101], v209 offset:14336\n" \
"v_mfma_f32_16x16x32_bf16 v[80:83], a[76:79], a[212:215], v[80:83]\n" \
"global_load_lds_dwordx4 v252, s[42:43] offset:0\n" \
"v_mfma_f32_16x16x32_bf16 v[84:87], a[72:75], a[216:219], v[84:87]\n" \
"ds_read_b128 a[68:71], v208 offset:0\n" \
"ds_read_b64_tr_b16 a[102:103], v209 offset:15360\n" \
"v_mfma_f32_16x16x32_bf16 v[84:87], a[76:79], a[220:223], v[84:87]\n" \
"ds_read_b64_tr_b16 a[104:105], v209 offset:0\n" \
"v_mfma_f32_16x16x32_bf16 v[44:47], a[200:203], a[72:75], v[44:47]\n" \
"ds_read_b64_tr_b16 a[106:107], v209 offset:1024\n" \
"v_mfma_f32_16x16x32_bf16 v[44:47], a[204:207], a[76:79], v[44:47]\n" \
"global_load_lds_dwordx4 v252, s[42:43] offset:2048\n" \
"v_mfma_f32_16x16x32_bf16 v[52:55], a[200:203], a[80:83], v[52:55]\n" \
"s_add_u32 s42, s42, 0x100\n" \
"s_addc_u32 s43, s43, 0\n" \
"ds_read_b128 a[72:75], v205 offset:0\n" \
"v_mfma_f32_16x16x32_bf16 v[52:55], a[204:207], a[84:87], v[52:55]\n" \
"ds_read_b128 a[76:79], v206 offset:0\n" \
"v_mfma_f32_16x16x32_bf16 v[92:95], a[80:83], a[216:219], v[92:95]\n" \
"ds_read_b64_tr_b16 a[108:109], v209 offset:12288\n" \
"v_mfma_f32_16x16x32_bf16 v[92:95], a[84:87], a[220:223], v[92:95]\n" \
"global_load_lds_dwordx4 v252, s[44:45] offset:1024\n" \
"v_mfma_f32_16x16x32_bf16 v[88:91], a[80:83], a[208:211], v[88:91]\n" \
"ds_read_b64_tr_b16 a[110:111], v209 offset:13312\n" \
"v_mfma_f32_16x16x32_bf16 v[88:91], a[84:87], a[212:215], v[88:91]\n" \
"ds_read_b64_tr_b16 a[112:113], v210 offset:0\n" \
"v_mfma_f32_16x16x32_bf16 v[96:99], a[88:91], a[208:211], v[96:99]\n" \
"ds_read_b128 a[80:83], v205 offset:4096\n" \
"v_mfma_f32_16x16x32_bf16 v[96:99], a[92:95], a[212:215], v[96:99]\n" \
"global_load_lds_dwordx4 v252, s[44:45] offset:3072\n" \
"v_mfma_f32_16x16x32_bf16 v[100:103], a[88:91], a[216:219], v[100:103]\n" \
"s_add_u32 s44, s44, 0x100\n" \
"s_addc_u32 s45, s45, 0\n" \
"ds_read_b128 a[84:87], v206 offset:4096\n" \
"v_mfma_f32_16x16x32_bf16 v[100:103], a[92:95], a[220:223], v[100:103]\n" \
"ds_read_b64_tr_b16 a[114:115], v210 offset:1024\n" \
"v_mfma_f32_16x16x32_bf16 v[60:63], a[200:203], a[88:91], v[60:63]\n" \
"s_add_u32 m0, s85, 16384\n" \
"ds_read_b64_tr_b16 a[116:117], v210 offset:12288\n" \
"v_mfma_f32_16x16x32_bf16 v[60:63], a[204:207], a[92:95], v[60:63]\n" \
"buffer_load_dwordx4 v246, s[12:15], s46 offen lds\n" \
"v_mfma_f32_16x16x32_bf16 v[68:71], a[200:203], a[192:195], v[68:71]\n" \
"ds_read_b128 a[88:91], v205 offset:8192\n" \
"s_add_u32 s46, s46, 0x100\n" \
"v_mfma_f32_16x16x32_bf16 v[68:71], a[204:207], a[196:199], v[68:71]\n" \
"ds_read_b128 a[92:95], v206 offset:8192\n" \
"v_mfma_f32_16x16x32_bf16 v[108:111], a[192:195], a[216:219], v[108:111]\n" \
"s_add_u32 m0, s84, 24576\n" \
"ds_read_b64_tr_b16 a[118:119], v210 offset:13312\n" \
"v_mfma_f32_16x16x32_bf16 v[108:111], a[196:199], a[220:223], v[108:111]\n" \
"global_load_lds_dwordx4 v253, s[48:49] offset:-4096\n" \
"v_mfma_f32_16x16x32_bf16 v[104:107], a[192:195], a[208:211], v[104:107]\n" \
"v_xor_b32 v226, v226, v227\n" \
"v_cmp_ne_u32 vcc, s60, v226\n" \
"v_mfma_f32_16x16x32_bf16 v[104:107], a[196:199], a[212:215], v[104:107]\n" \
"s_waitcnt lgkmcnt(2)\n" \
"v_mfma_f32_16x16x32_bf16 v[32:35], a[96:99], a[64:67], v[32:35]\n" \
"ds_read_b128 a[192:195], v205 offset:12288\n" \
"v_mfma_f32_16x16x32_bf16 v[32:35], a[100:103], a[68:71], v[32:35]\n" \
"global_load_lds_dwordx4 v253, s[48:49] offset:-2048\n" \
"v_mfma_f32_16x16x32_bf16 v[40:43], a[96:99], a[72:75], v[40:43]\n" \
"ds_read_b128 a[196:199], v206 offset:12288\n" \
"v_mfma_f32_16x16x32_bf16 v[40:43], a[100:103], a[76:79], v[40:43]\n" \
"ds_read_b64_tr_b16 a[208:209], v209 offset:4096\n" \
"v_mfma_f32_16x16x32_bf16 v[0:3], a[104:107], a[72:75], v[0:3]\n" \
"ds_read_b64_tr_b16 a[210:211], v209 offset:5120\n" \
"v_mfma_f32_16x16x32_bf16 v[0:3], a[108:111], a[76:79], v[0:3]\n" \
"s_waitcnt lgkmcnt(3)\n" \
"global_load_lds_dwordx4 v253, s[48:49] offset:0\n" \
"v_mfma_f32_16x16x32_bf16 v[4:7], a[112:115], a[72:75], v[4:7]\n" \
"s_add_u32 s48, s48, 0xc0000\n" \
"s_addc_u32 s49, s49, 0\n" \
"ds_read_b64_tr_b16 a[212:213], v209 offset:16384\n" \
"v_mfma_f32_16x16x32_bf16 v[4:7], a[116:119], a[76:79], v[4:7]\n" \
"ds_read_b64_tr_b16 a[214:215], v209 offset:17408\n" \
"v_mfma_f32_16x16x32_bf16 v[12:15], a[112:115], a[80:83], v[12:15]\n" \
"ds_read_b64_tr_b16 a[216:217], v210 offset:4096\n" \
"v_mfma_f32_16x16x32_bf16 v[12:15], a[116:119], a[84:87], v[12:15]\n" \
"global_load_lds_dwordx4 v253, s[50:51] offset:-3072\n" \
"v_mfma_f32_16x16x32_bf16 v[8:11], a[104:107], a[80:83], v[8:11]\n" \
"ds_read_b64_tr_b16 a[218:219], v210 offset:5120\n" \
"v_mfma_f32_16x16x32_bf16 v[8:11], a[108:111], a[84:87], v[8:11]\n" \
"ds_read_b64_tr_b16 a[220:221], v210 offset:16384\n" \
"v_mfma_f32_16x16x32_bf16 v[48:51], a[96:99], a[80:83], v[48:51]\n" \
"ds_read_b64_tr_b16 a[222:223], v210 offset:17408\n" \
"v_mfma_f32_16x16x32_bf16 v[48:51], a[100:103], a[84:87], v[48:51]\n" \
"global_load_lds_dwordx4 v253, s[50:51] offset:-1024\n" \
"v_mfma_f32_16x16x32_bf16 v[56:59], a[96:99], a[88:91], v[56:59]\n" \
"ds_read_b64_tr_b16 a[200:201], v210 offset:2048\n" \
"v_mfma_f32_16x16x32_bf16 v[56:59], a[100:103], a[92:95], v[56:59]\n" \
"ds_read_b64_tr_b16 a[202:203], v210 offset:3072\n" \
"v_mfma_f32_16x16x32_bf16 v[16:19], a[104:107], a[88:91], v[16:19]\n" \
"ds_read_b64_tr_b16 a[204:205], v210 offset:14336\n" \
"v_mfma_f32_16x16x32_bf16 v[16:19], a[108:111], a[92:95], v[16:19]\n" \
"global_load_lds_dwordx4 v253, s[50:51] offset:1024\n" \
"v_mfma_f32_16x16x32_bf16 v[20:23], a[112:115], a[88:91], v[20:23]\n" \
"s_add_u32 s50, s50, 0xc0000\n" \
"s_addc_u32 s51, s51, 0\n" \
"ds_read_b64_tr_b16 a[206:207], v210 offset:15360\n" \
"v_mfma_f32_16x16x32_bf16 v[20:23], a[116:119], a[92:95], v[20:23]\n" \
"s_waitcnt lgkmcnt(3)\n" \
"v_add_u32 v207, 0xffff3000, v207\n" \
"v_add_u32 v208, 0xffff3000, v208\n" \
"v_mfma_f32_16x16x32_bf16 v[28:31], a[112:115], a[192:195], v[28:31]\n" \
"v_add_u32 v205, 0xffff3000, v205\n" \
"v_add_u32 v206, 0xffff3000, v206\n" \
"v_mfma_f32_16x16x32_bf16 v[28:31], a[116:119], a[196:199], v[28:31]\n" \
"v_add_u32 v209, 0xffff3000, v209\n" \
"v_add_u32 v210, 0xffff3000, v210\n" \
"v_mfma_f32_16x16x32_bf16 v[24:27], a[104:107], a[192:195], v[24:27]\n" \
"s_and_b32 vcc_hi, vcc_lo, -1\n" \
"s_cbranch_scc1 Lpl3_%=\n" \
"Lpr3_%=:\n" \
"v_mfma_f32_16x16x32_bf16 v[24:27], a[108:111], a[196:199], v[24:27]\n" \
"s_waitcnt vmcnt(11) lgkmcnt(0)\n" \
"v_readfirstlane_b32 s71, v227\n" \
"s_and_b32 s70, %1, 7\n" \
"v_mfma_f32_16x16x32_bf16 v[64:67], a[96:99], a[192:195], v[64:67]\n" \
"s_lshl_b32 s78, s70, 2\n" \
"v_mfma_f32_16x16x32_bf16 v[64:67], a[100:103], a[196:199], v[64:67]\n" \
"s_barrier\n" \
"v_mfma_f32_16x16x32_bf16 v[72:75], a[64:67], a[208:211], v[72:75]\n" \
"ds_read_b64_tr_b16 a[96:97], v209 offset:2048\n" \
"v_subrev_u32 v227, s71, v227\n" \
"v_mfma_f32_16x16x32_bf16 v[72:75], a[68:71], a[212:215], v[72:75]\n" \
"ds_read_b64_tr_b16 a[98:99], v209 offset:3072\n" \
"v_mfma_f32_16x16x32_bf16 v[80:83], a[72:75], a[208:211], v[80:83]\n" \
"s_add_u32 m0, s95, 118784\n" \
"ds_read_b128 a[64:67], v207 offset:0\n" \
"ds_read_b64_tr_b16 a[100:101], v209 offset:14336\n" \
"v_mfma_f32_16x16x32_bf16 v[80:83], a[76:79], a[212:215], v[80:83]\n" \
"global_load_lds_dwordx4 v252, s[42:43] offset:0\n" \
"v_add_u32_dpp v227, v227, v227 quad_perm:[1,0,3,2] row_mask:0xf bank_mask:0xf\n" \
"v_mfma_f32_16x16x32_bf16 v[84:87], a[72:75], a[216:219], v[84:87]\n" \
"ds_read_b128 a[68:71], v208 offset:0\n" \
"ds_read_b64_tr_b16 a[102:103], v209 offset:15360\n" \
"v_mfma_f32_16x16x32_bf16 v[84:87], a[76:79], a[220:223], v[84:87]\n" \
"ds_read_b64_tr_b16 a[104:105], v209 offset:0\n" \
"v_add_u32_dpp v227, v227, v227 quad_perm:[2,3,0,1] row_mask:0xf bank_mask:0xf\n" \
"v_mfma_f32_16x16x32_bf16 v[44:47], a[200:203], a[72:75], v[44:47]\n" \
"ds_read_b64_tr_b16 a[106:107], v209 offset:1024\n" \
"v_readlane_b32 s71, v227, s78\n" \
"v_mfma_f32_16x16x32_bf16 v[44:47], a[204:207], a[76:79], v[44:47]\n" \
"global_load_lds_dwordx4 v252, s[42:43] offset:2048\n" \
"v_lshrrev_b32 v226, 6, v248\n" \
"v_mfma_f32_16x16x32_bf16 v[52:55], a[200:203], a[80:83], v[52:55]\n" \
"s_add_u32 s42, s42, 0x80\n" \
"s_addc_u32 s43, s43, 0\n" \
"ds_read_b128 a[72:75], v205 offset:0\n" \
"v_mfma_f32_16x16x32_bf16 v[52:55], a[204:207], a[84:87], v[52:55]\n" \
"ds_read_b128 a[76:79], v206 offset:0\n" \
"v_subrev_u32 v227, s71, v227\n" \
"v_mfma_f32_16x16x32_bf16 v[92:95], a[80:83], a[216:219], v[92:95]\n" \
"ds_read_b64_tr_b16 a[108:109], v209 offset:12288\n" \
"v_lshl_add_u32 v227, v227, 3, v226\n" \
"v_mfma_f32_16x16x32_bf16 v[92:95], a[84:87], a[220:223], v[92:95]\n" \
"global_load_lds_dwordx4 v252, s[44:45] offset:1024\n" \
"v_cmp_gt_i32 vcc, s70, v227\n" \
"v_mfma_f32_16x16x32_bf16 v[88:91], a[80:83], a[208:211], v[88:91]\n" \
"ds_read_b64_tr_b16 a[110:111], v209 offset:13312\n" \
"v_mfma_f32_16x16x32_bf16 v[88:91], a[84:87], a[212:215], v[88:91]\n" \
"s_bcnt1_i32_b32 s70, vcc_lo\n" \
"s_lshr_b32 s70, s70, 2\n" \
"ds_read_b64_tr_b16 a[112:113], v210 offset:0\n" \
"v_mfma_f32_16x16x32_bf16 v[96:99], a[88:91], a[208:211], v[96:99]\n" \
"s_lshr_b32 s78, s70, 2\n" \
"ds_read_b128 a[80:83], v205 offset:4096\n" \
"s_mul_i32 s71, s78, 7\n" \
"v_mfma_f32_16x16x32_bf16 v[96:99], a[92:95], a[212:215], v[96:99]\n" \
"s_xor_b32 s70, s70, s71\n" \
"global_load_lds_dwordx4 v252, s[44:45] offset:3072\n" \
"v_mfma_f32_16x16x32_bf16 v[100:103], a[88:91], a[216:219], v[100:103]\n" \
"s_add_u32 s44, s44, 0x80\n" \
"s_addc_u32 s45, s45, 0\n" \
"ds_read_b128 a[84:87], v206 offset:4096\n" \
"v_mfma_f32_16x16x32_bf16 v[100:103], a[92:95], a[220:223], v[100:103]\n" \
"s_xor_b32 s78, s78, 1\n" \
"s_lshl_b32 s65, s70, 2\n" \
"ds_read_b64_tr_b16 a[114:115], v210 offset:1024\n" \
"v_mfma_f32_16x16x32_bf16 v[60:63], a[200:203], a[88:91], v[60:63]\n" \
"s_add_u32 m0, s85, 135168\n" \
"s_lshr_b32 s64, 0x9764, s65\n" \
"ds_read_b64_tr_b16 a[116:117], v210 offset:12288\n" \
"v_mfma_f32_16x16x32_bf16 v[60:63], a[204:207], a[92:95], v[60:63]\n" \
"buffer_load_dwordx4 v246, s[12:15], s46 offen lds\n" \
"v_mfma_f32_16x16x32_bf16 v[68:71], a[200:203], a[192:195], v[68:71]\n" \
"s_add_u32 s46, s46, 0x80\n" \
"ds_read_b128 a[88:91], v205 offset:8192\n" \
"s_and_b32 s64, s64, 15\n" \
"v_mfma_f32_16x16x32_bf16 v[68:71], a[204:207], a[196:199], v[68:71]\n" \
"s_add_u32 s64, s64, 32\n" \
"ds_read_b128 a[92:95], v206 offset:8192\n" \
"s_lshr_b32 s70, %1, 6\n" \
"v_mfma_f32_16x16x32_bf16 v[108:111], a[192:195], a[216:219], v[108:111]\n" \
"s_add_u32 m0, s84, 143360\n" \
"s_add_u32 s65, s65, s70\n" \
"ds_read_b64_tr_b16 a[118:119], v210 offset:13312\n" \
"v_mfma_f32_16x16x32_bf16 v[108:111], a[196:199], a[220:223], v[108:111]\n" \
"s_and_b32 s30, %1, 1\n" \
"global_load_lds_dwordx4 v253, s[48:49] offset:-4096\n" \
"v_mfma_f32_16x16x32_bf16 v[104:107], a[192:195], a[208:211], v[104:107]\n" \
"s_lshl_b32 s30, s30, 3\n" \
"s_add_u32 s30, s30, s63\n" \
"s_lshl_b32 s30, s30, 2\n" \
"v_mfma_f32_16x16x32_bf16 v[104:107], a[196:199], a[212:215], v[104:107]\n" \
"s_waitcnt lgkmcnt(2)\n" \
"s_lshl_b32 s70, s88, 7\n" \
"s_add_u32 s30, s30, s70\n" \
"v_mfma_f32_16x16x32_bf16 v[32:35], a[96:99], a[64:67], v[32:35]\n" \
"s_add_u32 s30, s30, 65591296\n" \
"ds_read_b128 a[192:195], v205 offset:12288\n" \
"v_lshrrev_b32 v221, 2, v248\n" \
"v_mfma_f32_16x16x32_bf16 v[32:35], a[100:103], a[68:71], v[32:35]\n" \
"s_lshl_b32 s81, s65, 7\n" \
"global_load_lds_dwordx4 v253, s[48:49] offset:-2048\n" \
"v_mfma_f32_16x16x32_bf16 v[40:43], a[96:99], a[72:75], v[40:43]\n" \
"s_add_u32 s82, s81, 65591296\n" \
"ds_read_b128 a[196:199], v206 offset:12288\n" \
"s_add_u32 s81, s81, 65589248\n" \
"v_mfma_f32_16x16x32_bf16 v[40:43], a[100:103], a[76:79], v[40:43]\n" \
"s_mov_b64 s[52:53], s[26:27]\n" \
"s_and_b32 s53, s53, 0xffff\n" \
"ds_read_b64_tr_b16 a[208:209], v209 offset:4096\n" \
"v_mfma_f32_16x16x32_bf16 v[0:3], a[104:107], a[72:75], v[0:3]\n" \
"s_mov_b32 s54, 8388608\n" \
"s_mov_b32 s55, 0x20000\n" \
"ds_read_b64_tr_b16 a[210:211], v209 offset:5120\n" \
"v_mfma_f32_16x16x32_bf16 v[0:3], a[108:111], a[76:79], v[0:3]\n" \
"s_waitcnt lgkmcnt(3)\n" \
"global_load_lds_dwordx4 v253, s[48:49] offset:0\n" \
"s_lshr_b32 s83, s63, 1\n" \
"v_mfma_f32_16x16x32_bf16 v[4:7], a[112:115], a[72:75], v[4:7]\n" \
"s_add_u32 s48, s48, 0x60000\n" \
"s_addc_u32 s49, s49, 0\n" \
"ds_read_b64_tr_b16 a[212:213], v209 offset:16384\n" \
"v_mfma_f32_16x16x32_bf16 v[4:7], a[116:119], a[76:79], v[4:7]\n" \
"s_add_u32 s83, s83, 2\n" \
"s_lshr_b32 s70, s18, 8\n" \
"ds_read_b64_tr_b16 a[214:215], v209 offset:17408\n" \
"v_mfma_f32_16x16x32_bf16 v[12:15], a[112:115], a[80:83], v[12:15]\n" \
"s_sub_u32 s83, s83, s70\n" \
"s_and_b32 s83, s83, 3\n" \
"ds_read_b64_tr_b16 a[216:217], v210 offset:4096\n" \
"v_mfma_f32_16x16x32_bf16 v[12:15], a[116:119], a[84:87], v[12:15]\n" \
"s_and_b32 s70, s63, 1\n" \
"global_load_lds_dwordx4 v253, s[50:51] offset:-3072\n" \
"v_mfma_f32_16x16x32_bf16 v[8:11], a[104:107], a[80:83], v[8:11]\n" \
"s_lshl_b32 s70, s70, 2\n" \
"s_or_b32 s83, s83, s70\n" \
"ds_read_b64_tr_b16 a[218:219], v210 offset:5120\n" \
"v_mfma_f32_16x16x32_bf16 v[8:11], a[108:111], a[84:87], v[8:11]\n" \
"s_mul_i32 s75, s78, s64\n" \
"ds_read_b64_tr_b16 a[220:221], v210 offset:16384\n" \
"v_lshrrev_b32 v228, 4, v248\n" \
"v_mfma_f32_16x16x32_bf16 v[48:51], a[96:99], a[80:83], v[48:51]\n" \
"s_lshl_b32 s70, s65, 2\n" \
"s_add_u32 s70, s70, s61\n" \
"ds_read_b64_tr_b16 a[222:223], v210 offset:17408\n" \
"v_mfma_f32_16x16x32_bf16 v[48:51], a[100:103], a[84:87], v[48:51]\n" \
"s_mul_i32 s73, s70, 69632\n" \
"global_load_lds_dwordx4 v253, s[50:51] offset:-1024\n" \
"v_mfma_f32_16x16x32_bf16 v[56:59], a[96:99], a[88:91], v[56:59]\n" \
"s_add_u32 s73, s73, 52740096\n" \
"s_add_u32 s73, s73, s28\n" \
"ds_read_b64_tr_b16 a[200:201], v210 offset:2048\n" \
"v_mfma_f32_16x16x32_bf16 v[56:59], a[100:103], a[92:95], v[56:59]\n" \
"s_mul_i32 s70, s70, 266240\n" \
"s_lshl_b32 s71, s75, 12\n" \
"ds_read_b64_tr_b16 a[202:203], v210 offset:3072\n" \
"v_mfma_f32_16x16x32_bf16 v[16:19], a[104:107], a[88:91], v[16:19]\n" \
"s_add_u32 s70, s70, s71\n" \
"s_add_u32 s70, s70, 35700736\n" \
"ds_read_b64_tr_b16 a[204:205], v210 offset:14336\n" \
"v_mfma_f32_16x16x32_bf16 v[16:19], a[108:111], a[92:95], v[16:19]\n" \
"s_add_u32 s36, s28, s70\n" \
"global_load_lds_dwordx4 v253, s[50:51] offset:1024\n" \
"v_mfma_f32_16x16x32_bf16 v[20:23], a[112:115], a[88:91], v[20:23]\n" \
"s_add_u32 s50, s50, 0x60000\n" \
"s_addc_u32 s51, s51, 0\n" \
"ds_read_b64_tr_b16 a[206:207], v210 offset:15360\n" \
"v_mfma_f32_16x16x32_bf16 v[20:23], a[116:119], a[92:95], v[20:23]\n" \
"s_waitcnt lgkmcnt(3)\n" \
"v_add_u32 v207, 0xffff0000, v207\n" \
"v_add_u32 v208, 0xffff0000, v208\n" \
"v_mfma_f32_16x16x32_bf16 v[28:31], a[112:115], a[192:195], v[28:31]\n" \
"s_mov_b32 s37, s29\n" \
"v_add_u32 v205, 0xffff0000, v205\n" \
"v_add_u32 v206, 0xffff0000, v206\n" \
"v_mfma_f32_16x16x32_bf16 v[28:31], a[116:119], a[196:199], v[28:31]\n" \
"s_lshr_b32 s74, s61, 1\n" \
"v_add_u32 v209, 0xffff0000, v209\n" \
"v_add_u32 v210, 0xffff0000, v210\n" \
"v_mfma_f32_16x16x32_bf16 v[24:27], a[104:107], a[192:195], v[24:27]\n" \
"s_lshl_b32 s74, s74, 16\n" \
"s_and_b32 s70, s61, 1\n" \
"s_lshl_b32 s70, s70, 14\n" \
"v_mfma_f32_16x16x32_bf16 v[24:27], a[108:111], a[196:199], v[24:27]\n" \
"s_waitcnt vmcnt(11) lgkmcnt(0)\n" \
"s_add_u32 s74, s74, s70\n" \
"s_lshl_b32 s71, s83, 8\n" \
"v_mfma_f32_16x16x32_bf16 v[64:67], a[96:99], a[192:195], v[64:67]\n" \
"s_add_u32 s74, s74, s71\n" \
"s_add_u32 s40, s24, s74\n" \
"s_addc_u32 s41, s25, 0\n" \
"v_mfma_f32_16x16x32_bf16 v[64:67], a[100:103], a[196:199], v[64:67]\n" \
"s_barrier\n" \
"v_mfma_f32_16x16x32_bf16 v[72:75], a[64:67], a[208:211], v[72:75]\n" \
"s_lshl_b32 s70, s75, 17\n" \
"s_add_u32 s70, s70, s74\n" \
"ds_read_b64_tr_b16 a[96:97], v209 offset:2048\n" \
"v_mfma_f32_16x16x32_bf16 v[72:75], a[68:71], a[212:215], v[72:75]\n" \
"s_add_u32 s38, s18, s70\n" \
"s_addc_u32 s39, s19, 0\n" \
"ds_read_b64_tr_b16 a[98:99], v209 offset:3072\n" \
"v_mfma_f32_16x16x32_bf16 v[80:83], a[72:75], a[208:211], v[80:83]\n" \
"s_add_u32 m0, s95, 65536\n" \
"ds_read_b128 a[64:67], v207 offset:0\n" \
"ds_read_b64_tr_b16 a[100:101], v209 offset:14336\n" \
"v_mfma_f32_16x16x32_bf16 v[80:83], a[76:79], a[212:215], v[80:83]\n" \
"global_load_lds_dwordx4 v252, s[42:43] offset:0\n" \
"v_bfe_u32 v229, v228, 2, 3\n" \
"v_mfma_f32_16x16x32_bf16 v[84:87], a[72:75], a[216:219], v[84:87]\n" \
"ds_read_b128 a[68:71], v208 offset:0\n" \
"ds_read_b64_tr_b16 a[102:103], v209 offset:15360\n" \
"v_mfma_f32_16x16x32_bf16 v[84:87], a[76:79], a[220:223], v[84:87]\n" \
"ds_read_b64_tr_b16 a[104:105], v209 offset:0\n" \
"v_lshlrev_b32 v229, 11, v229\n" \
"v_mfma_f32_16x16x32_bf16 v[44:47], a[200:203], a[72:75], v[44:47]\n" \
"ds_read_b64_tr_b16 a[106:107], v209 offset:1024\n" \
"v_lshrrev_b32 v230, 5, v228\n" \
"v_mfma_f32_16x16x32_bf16 v[44:47], a[204:207], a[76:79], v[44:47]\n" \
"global_load_lds_dwordx4 v252, s[42:43] offset:2048\n" \
"v_lshl_add_u32 v229, v230, 6, v229\n" \
"v_mfma_f32_16x16x32_bf16 v[52:55], a[200:203], a[80:83], v[52:55]\n" \
"s_add_u32 s42, s42, 0x80\n" \
"s_addc_u32 s43, s43, 0\n" \
"ds_read_b128 a[72:75], v205 offset:0\n" \
"v_mfma_f32_16x16x32_bf16 v[52:55], a[204:207], a[84:87], v[52:55]\n" \
"ds_read_b128 a[76:79], v206 offset:0\n" \
"v_and_b32 v230, 3, v228\n" \
"v_mfma_f32_16x16x32_bf16 v[92:95], a[80:83], a[216:219], v[92:95]\n" \
"ds_read_b64_tr_b16 a[108:109], v209 offset:12288\n" \
"v_bfe_u32 v231, v228, 4, 1\n" \
"v_mfma_f32_16x16x32_bf16 v[92:95], a[84:87], a[220:223], v[92:95]\n" \
"global_load_lds_dwordx4 v252, s[44:45] offset:1024\n" \
"v_lshlrev_b32 v231, 1, v231\n" \
"v_mfma_f32_16x16x32_bf16 v[88:91], a[80:83], a[208:211], v[88:91]\n" \
"ds_read_b64_tr_b16 a[110:111], v209 offset:13312\n" \
"v_xor_b32 v230, v230, v231\n" \
"v_mfma_f32_16x16x32_bf16 v[88:91], a[84:87], a[212:215], v[88:91]\n" \
"ds_read_b64_tr_b16 a[112:113], v210 offset:0\n" \
"v_lshl_add_u32 v229, v230, 4, v229\n" \
"v_mfma_f32_16x16x32_bf16 v[96:99], a[88:91], a[208:211], v[96:99]\n" \
"ds_read_b128 a[80:83], v205 offset:4096\n" \
"v_add_u32 v216, 4096, v229\n" \
"v_mfma_f32_16x16x32_bf16 v[96:99], a[92:95], a[212:215], v[96:99]\n" \
"global_load_lds_dwordx4 v252, s[44:45] offset:3072\n" \
"v_add_u32 v217, 3200, v229\n" \
"v_mfma_f32_16x16x32_bf16 v[100:103], a[88:91], a[216:219], v[100:103]\n" \
"s_add_u32 s44, s44, 0x80\n" \
"s_addc_u32 s45, s45, 0\n" \
"ds_read_b128 a[84:87], v206 offset:4096\n" \
"v_mfma_f32_16x16x32_bf16 v[100:103], a[92:95], a[220:223], v[100:103]\n" \
"ds_read_b64_tr_b16 a[114:115], v210 offset:1024\n" \
"v_add_u32 v218, 34816, v229\n" \
"v_mfma_f32_16x16x32_bf16 v[60:63], a[200:203], a[88:91], v[60:63]\n" \
"s_add_u32 m0, s85, 81920\n" \
"ds_read_b64_tr_b16 a[116:117], v210 offset:12288\n" \
"v_add_u32 v219, 33920, v229\n" \
"v_mfma_f32_16x16x32_bf16 v[60:63], a[204:207], a[92:95], v[60:63]\n" \
"buffer_load_dwordx4 v246, s[12:15], s46 offen lds\n" \
"v_mfma_f32_16x16x32_bf16 v[68:71], a[200:203], a[192:195], v[68:71]\n" \
"s_add_u32 s46, s46, 0x80\n" \
"ds_read_b128 a[88:91], v205 offset:8192\n" \
"s_mov_b32 s75, 65536\n" \
"v_mfma_f32_16x16x32_bf16 v[68:71], a[204:207], a[196:199], v[68:71]\n" \
"s_lshr_b32 s70, s61, 1\n" \
"ds_read_b128 a[92:95], v206 offset:8192\n" \
"s_lshl_b32 s71, s70, 13\n" \
"v_mfma_f32_16x16x32_bf16 v[108:111], a[192:195], a[216:219], v[108:111]\n" \
"s_add_u32 m0, s84, 90112\n" \
"ds_read_b64_tr_b16 a[118:119], v210 offset:13312\n" \
"v_add_u32 v211, s71, v249\n" \
"v_mfma_f32_16x16x32_bf16 v[108:111], a[196:199], a[220:223], v[108:111]\n" \
"s_lshl_b32 s71, s65, 1\n" \
"Lpg10301_%=:\n" \
"s_cbranch_execz Lpg12366_%=\n" \
"global_load_lds_dwordx4 v253, s[48:49] offset:-4096\n" \
"v_mfma_f32_16x16x32_bf16 v[104:107], a[192:195], a[208:211], v[104:107]\n" \
"s_add_u32 s70, s70, s71\n" \
"s_lshl_b32 s77, s70, 18\n" \
"s_lshl_b32 s71, s83, 8\n" \
"v_mfma_f32_16x16x32_bf16 v[104:107], a[196:199], a[212:215], v[104:107]\n" \
"s_waitcnt lgkmcnt(2)\n" \
"s_add_u32 s77, s77, s71\n" \
"s_lshl_b32 s71, s78, 11\n" \
"v_mfma_f32_16x16x32_bf16 v[32:35], a[96:99], a[64:67], v[32:35]\n" \
"s_add_u32 s77, s77, s71\n" \
"ds_read_b128 a[192:195], v205 offset:12288\n" \
"s_and_b32 s70, s61, 1\n" \
"v_mfma_f32_16x16x32_bf16 v[32:35], a[100:103], a[68:71], v[32:35]\n" \
"s_lshl_b32 s71, s70, 7\n" \
"global_load_lds_dwordx4 v253, s[48:49] offset:-2048\n" \
"v_mfma_f32_16x16x32_bf16 v[40:43], a[96:99], a[72:75], v[40:43]\n" \
"s_add_u32 s77, s77, s71\n" \
"ds_read_b128 a[196:199], v206 offset:12288\n" \
"v_and_b32 v229, 3, v228\n" \
"v_mfma_f32_16x16x32_bf16 v[40:43], a[100:103], a[76:79], v[40:43]\n" \
"ds_read_b64_tr_b16 a[208:209], v209 offset:4096\n" \
"v_lshlrev_b32 v229, 3, v229\n" \
"v_mfma_f32_16x16x32_bf16 v[0:3], a[104:107], a[72:75], v[0:3]\n" \
"ds_read_b64_tr_b16 a[210:211], v209 offset:5120\n" \
"v_bfe_u32 v230, v228, 2, 2\n" \
"v_mfma_f32_16x16x32_bf16 v[0:3], a[108:111], a[76:79], v[0:3]\n" \
"s_waitcnt lgkmcnt(3)\n" \
"global_load_lds_dwordx4 v253, s[48:49] offset:0\n" \
"v_lshl_add_u32 v229, v230, 6, v229\n" \
"v_mfma_f32_16x16x32_bf16 v[4:7], a[112:115], a[72:75], v[4:7]\n" \
"s_add_u32 s48, s48, 0x60000\n" \
"s_addc_u32 s49, s49, 0\n" \
"ds_read_b64_tr_b16 a[212:213], v209 offset:16384\n" \
"v_mfma_f32_16x16x32_bf16 v[4:7], a[116:119], a[76:79], v[4:7]\n" \
"ds_read_b64_tr_b16 a[214:215], v209 offset:17408\n" \
"v_bfe_u32 v230, v228, 4, 1\n" \
"v_mfma_f32_16x16x32_bf16 v[12:15], a[112:115], a[80:83], v[12:15]\n" \
"ds_read_b64_tr_b16 a[216:217], v210 offset:4096\n" \
"v_lshl_add_u32 v229, v230, 5, v229\n" \
"v_mfma_f32_16x16x32_bf16 v[12:15], a[116:119], a[84:87], v[12:15]\n" \
"global_load_lds_dwordx4 v253, s[50:51] offset:-3072\n" \
"v_lshl_add_u32 v229, v230, 8, v229\n" \
"v_mfma_f32_16x16x32_bf16 v[8:11], a[104:107], a[80:83], v[8:11]\n" \
"ds_read_b64_tr_b16 a[218:219], v210 offset:5120\n" \
"v_lshrrev_b32 v230, 5, v228\n" \
"v_mfma_f32_16x16x32_bf16 v[8:11], a[108:111], a[84:87], v[8:11]\n" \
"ds_read_b64_tr_b16 a[220:221], v210 offset:16384\n" \
"v_lshl_add_u32 v229, v230, 12, v229\n" \
"v_mfma_f32_16x16x32_bf16 v[48:51], a[96:99], a[80:83], v[48:51]\n" \
"s_lshl_b32 s71, s70, 10\n" \
"ds_read_b64_tr_b16 a[222:223], v210 offset:17408\n" \
"v_add_u32 v212, s71, v229\n" \
"v_mfma_f32_16x16x32_bf16 v[48:51], a[100:103], a[84:87], v[48:51]\n" \
"s_lshl_b32 s70, s65, 3\n" \
"global_load_lds_dwordx4 v253, s[50:51] offset:-1024\n" \
"v_mfma_f32_16x16x32_bf16 v[56:59], a[96:99], a[88:91], v[56:59]\n" \
"s_add_u32 s70, s70, s83\n" \
"s_lshl_b32 s76, s70, 16\n" \
"ds_read_b64_tr_b16 a[200:201], v210 offset:2048\n" \
"v_mfma_f32_16x16x32_bf16 v[56:59], a[100:103], a[92:95], v[56:59]\n" \
"s_lshl_b32 s71, s61, 14\n" \
"s_add_u32 s76, s76, s71\n" \
"ds_read_b64_tr_b16 a[202:203], v210 offset:3072\n" \
"v_mfma_f32_16x16x32_bf16 v[16:19], a[104:107], a[88:91], v[16:19]\n" \
"s_add_u32 s76, s76, 57196544\n" \
"s_lshl_b32 s79, s70, 7\n" \
"ds_read_b64_tr_b16 a[204:205], v210 offset:14336\n" \
"v_mfma_f32_16x16x32_bf16 v[16:19], a[108:111], a[92:95], v[16:19]\n" \
"s_add_u32 s79, s79, 65601536\n" \
"global_load_lds_dwordx4 v253, s[50:51] offset:1024\n" \
"v_mfma_f32_16x16x32_bf16 v[20:23], a[112:115], a[88:91], v[20:23]\n" \
"s_add_u32 s50, s50, 0x60000\n" \
"s_addc_u32 s51, s51, 0\n" \
"ds_read_b64_tr_b16 a[206:207], v210 offset:15360\n" \
"v_mfma_f32_16x16x32_bf16 v[20:23], a[116:119], a[92:95], v[20:23]\n" \
"s_waitcnt lgkmcnt(3)\n" \
"v_add_u32 v207, 0x1d000, v207\n" \
"v_add_u32 v208, 0x1d000, v208\n" \
"v_mfma_f32_16x16x32_bf16 v[28:31], a[112:115], a[192:195], v[28:31]\n" \
"s_lshr_b32 s70, s64, 1\n" \
"v_add_u32 v205, 0x1d000, v205\n" \
"v_add_u32 v206, 0x1d000, v206\n" \
"v_mfma_f32_16x16x32_bf16 v[28:31], a[116:119], a[196:199], v[28:31]\n" \
"s_lshl_b32 s70, -1, s70\n" \
"v_add_u32 v209, 0x1d000, v209\n" \
"v_add_u32 v210, 0x1d000, v210\n" \
"v_mfma_f32_16x16x32_bf16 v[24:27], a[104:107], a[192:195], v[24:27]\n" \
"s_add_u32 s71, s64, 1\n" \
"s_lshr_b32 s71, s71, 1\n" \
"s_bfm_b32 s71, s71, 0\n" \
"v_mfma_f32_16x16x32_bf16 v[24:27], a[108:111], a[196:199], v[24:27]\n" \
"s_waitcnt vmcnt(11) lgkmcnt(0)\n" \
"s_cmp_eq_u32 s78, 0\n" \
"s_cselect_b32 s80, s71, s70\n" \
"v_mfma_f32_16x16x32_bf16 v[64:67], a[96:99], a[192:195], v[64:67]\n" \
"s_mov_b32 s16, 0xff000000\n" \
"s_mov_b32 s17, 0\n" \
"v_add_u32 v222, 0x1c90080, v250\n" \
"v_mfma_f32_16x16x32_bf16 v[64:67], a[100:103], a[196:199], v[64:67]\n" \
"s_barrier\n" \
"v_mfma_f32_16x16x32_bf16 v[72:75], a[64:67], a[208:211], v[72:75]\n" \
"ds_read_b64_tr_b16 a[96:97], v209 offset:2048\n" \
"v_cndmask_b32 v222, v250, v222, s[16:17]\n" \
"v_mfma_f32_16x16x32_bf16 v[72:75], a[68:71], a[212:215], v[72:75]\n" \
"s_mov_b32 s74, 98304\n" \
"ds_read_b64_tr_b16 a[98:99], v209 offset:3072\n" \
"v_mfma_f32_16x16x32_bf16 v[80:83], a[72:75], a[208:211], v[80:83]\n" \
"s_add_u32 m0, s95, 0\n" \
"ds_read_b128 a[64:67], v207 offset:0\n" \
"ds_read_b64_tr_b16 a[100:101], v209 offset:14336\n" \
"v_mfma_f32_16x16x32_bf16 v[80:83], a[76:79], a[212:215], v[80:83]\n" \
"global_load_lds_dwordx4 v252, s[42:43] offset:0\n" \
"v_mfma_f32_16x16x32_bf16 v[84:87], a[72:75], a[216:219], v[84:87]\n" \
"ds_read_b128 a[68:71], v208 offset:0\n" \
"ds_read_b64_tr_b16 a[102:103], v209 offset:15360\n" \
"v_mfma_f32_16x16x32_bf16 v[84:87], a[76:79], a[220:223], v[84:87]\n" \
"s_sub_u32 s74, s74, 32768\n" \
"s_cselect_b32 s74, 131072, s74\n" \
"ds_read_b64_tr_b16 a[104:105], v209 offset:0\n" \
"v_mfma_f32_16x16x32_bf16 v[44:47], a[200:203], a[72:75], v[44:47]\n" \
"ds_read_b64_tr_b16 a[106:107], v209 offset:1024\n" \
"v_add_u32 v213, s74, v211\n" \
"v_mfma_f32_16x16x32_bf16 v[44:47], a[204:207], a[76:79], v[44:47]\n" \
"global_load_lds_dwordx4 v252, s[42:43] offset:2048\n" \
"v_add_u32 v214, s74, v212\n" \
"v_mfma_f32_16x16x32_bf16 v[52:55], a[200:203], a[80:83], v[52:55]\n" \
"s_add_u32 s42, s42, 0x80\n" \
"s_addc_u32 s43, s43, 0\n" \
"ds_read_b128 a[72:75], v205 offset:0\n" \
"v_mfma_f32_16x16x32_bf16 v[52:55], a[204:207], a[84:87], v[52:55]\n" \
"ds_read_b128 a[76:79], v206 offset:0\n" \
"v_xor_b32 v215, 32, v214\n" \
"v_mfma_f32_16x16x32_bf16 v[92:95], a[80:83], a[216:219], v[92:95]\n" \
"s_and_b32 s89, s61, 1\n" \
"s_lshl_b32 s89, s89, 11\n" \
"ds_read_b64_tr_b16 a[108:109], v209 offset:12288\n" \
"v_mfma_f32_16x16x32_bf16 v[92:95], a[84:87], a[220:223], v[92:95]\n" \
"s_add_u32 s87, s87, s89\n" \
"global_load_lds_dwordx4 v252, s[44:45] offset:1024\n" \
"v_mfma_f32_16x16x32_bf16 v[88:91], a[80:83], a[208:211], v[88:91]\n" \
"s_add_u32 s87, s87, 52740096\n" \
"ds_read_b64_tr_b16 a[110:111], v209 offset:13312\n" \
"v_mfma_f32_16x16x32_bf16 v[88:91], a[84:87], a[212:215], v[88:91]\n" \
"s_cmp_eq_u32 s88, 0\n" \
"s_cselect_b32 s86, -1, 0\n" \
"ds_read_b64_tr_b16 a[112:113], v210 offset:0\n" \
"v_mfma_f32_16x16x32_bf16 v[96:99], a[88:91], a[208:211], v[96:99]\n" \
"s_mul_i32 s89, s88, 278528\n" \
"ds_read_b128 a[80:83], v205 offset:4096\n" \
"s_add_u32 s87, s87, s89\n" \
"v_mfma_f32_16x16x32_bf16 v[96:99], a[92:95], a[212:215], v[96:99]\n" \
"s_lshr_b32 s89, s61, 1\n" \
"global_load_lds_dwordx4 v252, s[44:45] offset:3072\n" \
"v_mfma_f32_16x16x32_bf16 v[100:103], a[88:91], a[216:219], v[100:103]\n" \
"s_add_u32 s44, s44, 0x80\n" \
"s_addc_u32 s45, s45, 0\n" \
"ds_read_b128 a[84:87], v206 offset:4096\n" \
"v_mfma_f32_16x16x32_bf16 v[100:103], a[92:95], a[220:223], v[100:103]\n" \
"s_lshl_b32 s89, s89, 8\n" \
"s_add_u32 s87, s87, s89\n" \
"ds_read_b64_tr_b16 a[114:115], v210 offset:1024\n" \
"v_mfma_f32_16x16x32_bf16 v[60:63], a[200:203], a[88:91], v[60:63]\n" \
"s_add_u32 m0, s85, 16384\n" \
"s_lshl_b32 s89, s61, 4\n" \
"ds_read_b64_tr_b16 a[116:117], v210 offset:12288\n" \
"v_mfma_f32_16x16x32_bf16 v[60:63], a[204:207], a[92:95], v[60:63]\n" \
"buffer_load_dwordx4 v246, s[12:15], s46 offen lds\n" \
"v_mfma_f32_16x16x32_bf16 v[68:71], a[200:203], a[192:195], v[68:71]\n" \
"s_add_u32 s46, s46, 0x80\n" \
"ds_read_b128 a[88:91], v205 offset:8192\n" \
"v_subrev_u32 v147, s89, v251\n" \
"v_mfma_f32_16x16x32_bf16 v[68:71], a[204:207], a[196:199], v[68:71]\n" \
"s_lshr_b32 s89, s61, 1\n" \
"ds_read_b128 a[92:95], v206 offset:8192\n" \
"s_lshl_b32 s90, s89, 9\n" \
"v_mfma_f32_16x16x32_bf16 v[108:111], a[192:195], a[216:219], v[108:111]\n" \
"s_add_u32 m0, s84, 24576\n" \
"s_add_u32 s90, s90, 511\n" \
"ds_read_b64_tr_b16 a[118:119], v210 offset:13312\n" \
"v_mfma_f32_16x16x32_bf16 v[108:111], a[196:199], a[220:223], v[108:111]\n" \
"global_load_lds_dwordx4 v253, s[48:49] offset:-4096\n" \
"v_sub_u32 v145, s90, v248\n" \
"v_mfma_f32_16x16x32_bf16 v[104:107], a[192:195], a[208:211], v[104:107]\n" \
"s_lshl_b32 s90, s89, 3\n" \
"s_not_b32 s90, s90\n" \
"v_sub_u32 v146, s90, v147\n" \
"v_mfma_f32_16x16x32_bf16 v[104:107], a[196:199], a[212:215], v[104:107]\n" \
"s_waitcnt lgkmcnt(2)\n" \
"v_subrev_u32 v152, 0, v147\n" \
"v_sub_u32 v144, 7, v147\n" \
"v_mfma_f32_16x16x32_bf16 v[32:35], a[96:99], a[64:67], v[32:35]\n" \
"ds_read_b128 a[192:195], v205 offset:12288\n" \
"v_or_b32 v152, v152, v144\n" \
"v_mfma_f32_16x16x32_bf16 v[32:35], a[100:103], a[68:71], v[32:35]\n" \
"global_load_lds_dwordx4 v253, s[48:49] offset:-2048\n" \
"v_ashrrev_i32 v152, 31, v152\n" \
"v_mfma_f32_16x16x32_bf16 v[40:43], a[96:99], a[72:75], v[40:43]\n" \
"ds_read_b128 a[196:199], v206 offset:12288\n" \
"v_and_b32 v152, 0xff800000, v152\n" \
"v_mfma_f32_16x16x32_bf16 v[40:43], a[100:103], a[76:79], v[40:43]\n" \
"ds_read_b64_tr_b16 a[208:209], v209 offset:4096\n" \
"v_mov_b32 v160, v152\n" \
"v_mfma_f32_16x16x32_bf16 v[0:3], a[104:107], a[72:75], v[0:3]\n" \
"ds_read_b64_tr_b16 a[210:211], v209 offset:5120\n" \
"v_mov_b32 v168, v152\n" \
"v_mfma_f32_16x16x32_bf16 v[0:3], a[108:111], a[76:79], v[0:3]\n" \
"s_waitcnt lgkmcnt(3)\n" \
"global_load_lds_dwordx4 v253, s[48:49] offset:0\n" \
"v_mov_b32 v176, v152\n" \
"v_mfma_f32_16x16x32_bf16 v[4:7], a[112:115], a[72:75], v[4:7]\n" \
"s_add_u32 s48, s48, 0x60000\n" \
"s_addc_u32 s49, s49, 0\n" \
"ds_read_b64_tr_b16 a[212:213], v209 offset:16384\n" \
"v_mfma_f32_16x16x32_bf16 v[4:7], a[116:119], a[76:79], v[4:7]\n" \
"ds_read_b64_tr_b16 a[214:215], v209 offset:17408\n" \
"v_sub_u32 v156, -9, v147\n" \
"v_mfma_f32_16x16x32_bf16 v[12:15], a[112:115], a[80:83], v[12:15]\n" \
"ds_read_b64_tr_b16 a[216:217], v210 offset:4096\n" \
"v_ashrrev_i32 v156, 31, v156\n" \
"v_mfma_f32_16x16x32_bf16 v[12:15], a[116:119], a[84:87], v[12:15]\n" \
"global_load_lds_dwordx4 v253, s[50:51] offset:-3072\n" \
"v_and_b32 v156, 0xff800000, v156\n" \
"v_mfma_f32_16x16x32_bf16 v[8:11], a[104:107], a[80:83], v[8:11]\n" \
"ds_read_b64_tr_b16 a[218:219], v210 offset:5120\n" \
"v_mov_b32 v164, v156\n" \
"v_mfma_f32_16x16x32_bf16 v[8:11], a[108:111], a[84:87], v[8:11]\n" \
"ds_read_b64_tr_b16 a[220:221], v210 offset:16384\n" \
"v_mov_b32 v172, v156\n" \
"v_mfma_f32_16x16x32_bf16 v[48:51], a[96:99], a[80:83], v[48:51]\n" \
"ds_read_b64_tr_b16 a[222:223], v210 offset:17408\n" \
"v_add_u32 v148, 0, v146\n" \
"v_mfma_f32_16x16x32_bf16 v[48:51], a[100:103], a[84:87], v[48:51]\n" \
"global_load_lds_dwordx4 v253, s[50:51] offset:-1024\n" \
"v_or3_b32 v148, v148, v145, s86\n" \
"v_mfma_f32_16x16x32_bf16 v[56:59], a[96:99], a[88:91], v[56:59]\n" \
"ds_read_b64_tr_b16 a[200:201], v210 offset:2048\n" \
"v_ashrrev_i32 v148, 31, v148\n" \
"v_mfma_f32_16x16x32_bf16 v[56:59], a[100:103], a[92:95], v[56:59]\n" \
"ds_read_b64_tr_b16 a[202:203], v210 offset:3072\n" \
"v_and_b32 v148, 0xff800000, v148\n" \
"v_mfma_f32_16x16x32_bf16 v[16:19], a[104:107], a[88:91], v[16:19]\n" \
"ds_read_b64_tr_b16 a[204:205], v210 offset:14336\n" \
"v_subrev_u32 v153, 1, v147\n" \
"v_mfma_f32_16x16x32_bf16 v[16:19], a[108:111], a[92:95], v[16:19]\n" \
"global_load_lds_dwordx4 v253, s[50:51] offset:1024\n" \
"v_sub_u32 v144, 8, v147\n" \
"v_mfma_f32_16x16x32_bf16 v[20:23], a[112:115], a[88:91], v[20:23]\n" \
"s_add_u32 s50, s50, 0x60000\n" \
"s_addc_u32 s51, s51, 0\n" \
"ds_read_b64_tr_b16 a[206:207], v210 offset:15360\n" \
"v_mfma_f32_16x16x32_bf16 v[20:23], a[116:119], a[92:95], v[20:23]\n" \
"s_waitcnt lgkmcnt(3)\n" \
"v_add_u32 v207, 0xffff3000, v207\n" \
"v_add_u32 v208, 0xffff3000, v208\n" \
"v_mfma_f32_16x16x32_bf16 v[28:31], a[112:115], a[192:195], v[28:31]\n" \
"v_add_u32 v205, 0xffff3000, v205\n" \
"v_add_u32 v206, 0xffff3000, v206\n" \
"v_mfma_f32_16x16x32_bf16 v[28:31], a[116:119], a[196:199], v[28:31]\n" \
"buffer_load_dword v220, v221, s[56:59], s81 offen sc1\n" \
"v_mfma_f32_16x16x32_bf16 v[24:27], a[104:107], a[192:195], v[24:27]\n" \
"v_add_u32 v209, 0xffff3000, v209\n" \
"v_add_u32 v210, 0xffff3000, v210\n" \
"v_mfma_f32_16x16x32_bf16 v[24:27], a[108:111], a[196:199], v[24:27]\n" \
"s_waitcnt vmcnt(12) lgkmcnt(0)\n" \
"v_or_b32 v153, v153, v144\n" \
"v_ashrrev_i32 v153, 31, v153\n" \
"v_mfma_f32_16x16x32_bf16 v[64:67], a[96:99], a[192:195], v[64:67]\n" \
"v_and_b32 v153, 0xff800000, v153\n" \
"v_mov_b32 v161, v153\n" \
"v_mfma_f32_16x16x32_bf16 v[64:67], a[100:103], a[196:199], v[64:67]\n" \
"s_barrier\n" \
"v_mfma_f32_16x16x32_bf16 v[72:75], a[64:67], a[208:211], v[72:75]\n" \
"ds_read_b64_tr_b16 a[96:97], v209 offset:2048\n" \
"v_mov_b32 v169, v153\n" \
"v_mfma_f32_16x16x32_bf16 v[72:75], a[68:71], a[212:215], v[72:75]\n" \
"ds_read_b64_tr_b16 a[98:99], v209 offset:3072\n" \
"v_mov_b32 v177, v153\n" \
"v_mfma_f32_16x16x32_bf16 v[80:83], a[72:75], a[208:211], v[80:83]\n" \
"s_add_u32 m0, s95, 118784\n" \
"ds_read_b128 a[64:67], v207 offset:0\n" \
"ds_read_b64_tr_b16 a[100:101], v209 offset:14336\n" \
"v_mfma_f32_16x16x32_bf16 v[80:83], a[76:79], a[212:215], v[80:83]\n" \
"global_load_lds_dwordx4 v252, s[42:43] offset:0\n" \
"v_sub_u32 v157, -8, v147\n" \
"v_mfma_f32_16x16x32_bf16 v[84:87], a[72:75], a[216:219], v[84:87]\n" \
"ds_read_b128 a[68:71], v208 offset:0\n" \
"ds_read_b64_tr_b16 a[102:103], v209 offset:15360\n" \
"v_mfma_f32_16x16x32_bf16 v[84:87], a[76:79], a[220:223], v[84:87]\n" \
"ds_read_b64_tr_b16 a[104:105], v209 offset:0\n" \
"v_ashrrev_i32 v157, 31, v157\n" \
"v_mfma_f32_16x16x32_bf16 v[44:47], a[200:203], a[72:75], v[44:47]\n" \
"ds_read_b64_tr_b16 a[106:107], v209 offset:1024\n" \
"v_and_b32 v157, 0xff800000, v157\n" \
"v_mfma_f32_16x16x32_bf16 v[44:47], a[204:207], a[76:79], v[44:47]\n" \
"global_load_lds_dwordx4 v252, s[42:43] offset:2048\n" \
"v_mov_b32 v165, v157\n" \
"v_mfma_f32_16x16x32_bf16 v[52:55], a[200:203], a[80:83], v[52:55]\n" \
"ds_read_b128 a[72:75], v205 offset:0\n" \
"v_mov_b32 v173, v157\n" \
"v_mfma_f32_16x16x32_bf16 v[52:55], a[204:207], a[84:87], v[52:55]\n" \
"ds_read_b128 a[76:79], v206 offset:0\n" \
"v_add_u32 v149, 1, v146\n" \
"v_mfma_f32_16x16x32_bf16 v[92:95], a[80:83], a[216:219], v[92:95]\n" \
"ds_read_b64_tr_b16 a[108:109], v209 offset:12288\n" \
"v_or3_b32 v149, v149, v145, s86\n" \
"v_mfma_f32_16x16x32_bf16 v[92:95], a[84:87], a[220:223], v[92:95]\n" \
"global_load_lds_dwordx4 v252, s[44:45] offset:1024\n" \
"v_ashrrev_i32 v149, 31, v149\n" \
"v_mfma_f32_16x16x32_bf16 v[88:91], a[80:83], a[208:211], v[88:91]\n" \
"ds_read_b64_tr_b16 a[110:111], v209 offset:13312\n" \
"v_and_b32 v149, 0xff800000, v149\n" \
"v_mfma_f32_16x16x32_bf16 v[88:91], a[84:87], a[212:215], v[88:91]\n" \
"ds_read_b64_tr_b16 a[112:113], v210 offset:0\n" \
"v_subrev_u32 v154, 2, v147\n" \
"v_mfma_f32_16x16x32_bf16 v[96:99], a[88:91], a[208:211], v[96:99]\n" \
"ds_read_b128 a[80:83], v205 offset:4096\n" \
"v_sub_u32 v144, 9, v147\n" \
"v_mfma_f32_16x16x32_bf16 v[96:99], a[92:95], a[212:215], v[96:99]\n" \
"global_load_lds_dwordx4 v252, s[44:45] offset:3072\n" \
"v_or_b32 v154, v154, v144\n" \
"v_mfma_f32_16x16x32_bf16 v[100:103], a[88:91], a[216:219], v[100:103]\n" \
"ds_read_b128 a[84:87], v206 offset:4096\n" \
"v_ashrrev_i32 v154, 31, v154\n" \
"v_mfma_f32_16x16x32_bf16 v[100:103], a[92:95], a[220:223], v[100:103]\n" \
"ds_read_b64_tr_b16 a[114:115], v210 offset:1024\n" \
"v_and_b32 v154, 0xff800000, v154\n" \
"v_mfma_f32_16x16x32_bf16 v[60:63], a[200:203], a[88:91], v[60:63]\n" \
"s_add_u32 m0, s85, 135168\n" \
"ds_read_b64_tr_b16 a[116:117], v210 offset:12288\n" \
"v_mov_b32 v162, v154\n" \
"v_mfma_f32_16x16x32_bf16 v[60:63], a[204:207], a[92:95], v[60:63]\n" \
"buffer_load_dwordx4 v246, s[12:15], s46 offen lds\n" \
"v_mfma_f32_16x16x32_bf16 v[68:71], a[200:203], a[192:195], v[68:71]\n" \
"ds_read_b128 a[88:91], v205 offset:8192\n" \
"v_mov_b32 v170, v154\n" \
"v_mfma_f32_16x16x32_bf16 v[68:71], a[204:207], a[196:199], v[68:71]\n" \
"ds_read_b128 a[92:95], v206 offset:8192\n" \
"v_mov_b32 v178, v154\n" \
"v_mfma_f32_16x16x32_bf16 v[108:111], a[192:195], a[216:219], v[108:111]\n" \
"s_add_u32 m0, s84, 143360\n" \
"ds_read_b64_tr_b16 a[118:119], v210 offset:13312\n" \
"v_sub_u32 v158, -7, v147\n" \
"v_mfma_f32_16x16x32_bf16 v[108:111], a[196:199], a[220:223], v[108:111]\n" \
"global_load_lds_dwordx4 v253, s[48:49] offset:-4096\n" \
"v_ashrrev_i32 v158, 31, v158\n" \
"v_mfma_f32_16x16x32_bf16 v[104:107], a[192:195], a[208:211], v[104:107]\n" \
"v_and_b32 v158, 0xff800000, v158\n" \
"v_mov_b32 v166, v158\n" \
"v_mfma_f32_16x16x32_bf16 v[104:107], a[196:199], a[212:215], v[104:107]\n" \
"s_waitcnt lgkmcnt(2)\n" \
"v_mov_b32 v174, v158\n" \
"v_add_u32 v150, 2, v146\n" \
"v_mfma_f32_16x16x32_bf16 v[32:35], a[96:99], a[64:67], v[32:35]\n" \
"ds_read_b128 a[192:195], v205 offset:12288\n" \
"v_or3_b32 v150, v150, v145, s86\n" \
"v_mfma_f32_16x16x32_bf16 v[32:35], a[100:103], a[68:71], v[32:35]\n" \
"global_load_lds_dwordx4 v253, s[48:49] offset:-2048\n" \
"v_ashrrev_i32 v150, 31, v150\n" \
"v_mfma_f32_16x16x32_bf16 v[40:43], a[96:99], a[72:75], v[40:43]\n" \
"ds_read_b128 a[196:199], v206 offset:12288\n" \
"v_and_b32 v150, 0xff800000, v150\n" \
"v_mfma_f32_16x16x32_bf16 v[40:43], a[100:103], a[76:79], v[40:43]\n" \
"ds_read_b64_tr_b16 a[208:209], v209 offset:4096\n" \
"v_subrev_u32 v155, 3, v147\n" \
"v_mfma_f32_16x16x32_bf16 v[0:3], a[104:107], a[72:75], v[0:3]\n" \
"ds_read_b64_tr_b16 a[210:211], v209 offset:5120\n" \
"v_sub_u32 v144, 10, v147\n" \
"v_mfma_f32_16x16x32_bf16 v[0:3], a[108:111], a[76:79], v[0:3]\n" \
"s_waitcnt lgkmcnt(3)\n" \
"global_load_lds_dwordx4 v253, s[48:49] offset:0\n" \
"v_or_b32 v155, v155, v144\n" \
"v_mfma_f32_16x16x32_bf16 v[4:7], a[112:115], a[72:75], v[4:7]\n" \
"ds_read_b64_tr_b16 a[212:213], v209 offset:16384\n" \
"v_ashrrev_i32 v155, 31, v155\n" \
"v_mfma_f32_16x16x32_bf16 v[4:7], a[116:119], a[76:79], v[4:7]\n" \
"ds_read_b64_tr_b16 a[214:215], v209 offset:17408\n" \
"v_and_b32 v155, 0xff800000, v155\n" \
"v_mfma_f32_16x16x32_bf16 v[12:15], a[112:115], a[80:83], v[12:15]\n" \
"ds_read_b64_tr_b16 a[216:217], v210 offset:4096\n" \
"v_mov_b32 v163, v155\n" \
"v_mfma_f32_16x16x32_bf16 v[12:15], a[116:119], a[84:87], v[12:15]\n" \
"global_load_lds_dwordx4 v253, s[50:51] offset:-3072\n" \
"v_mov_b32 v171, v155\n" \
"v_mfma_f32_16x16x32_bf16 v[8:11], a[104:107], a[80:83], v[8:11]\n" \
"ds_read_b64_tr_b16 a[218:219], v210 offset:5120\n" \
"v_mov_b32 v179, v155\n" \
"v_mfma_f32_16x16x32_bf16 v[8:11], a[108:111], a[84:87], v[8:11]\n" \
"ds_read_b64_tr_b16 a[220:221], v210 offset:16384\n" \
"v_sub_u32 v159, -6, v147\n" \
"v_mfma_f32_16x16x32_bf16 v[48:51], a[96:99], a[80:83], v[48:51]\n" \
"ds_read_b64_tr_b16 a[222:223], v210 offset:17408\n" \
"v_ashrrev_i32 v159, 31, v159\n" \
"v_mfma_f32_16x16x32_bf16 v[48:51], a[100:103], a[84:87], v[48:51]\n" \
"global_load_lds_dwordx4 v253, s[50:51] offset:-1024\n" \
"v_and_b32 v159, 0xff800000, v159\n" \
"v_mfma_f32_16x16x32_bf16 v[56:59], a[96:99], a[88:91], v[56:59]\n" \
"ds_read_b64_tr_b16 a[200:201], v210 offset:2048\n" \
"v_mov_b32 v167, v159\n" \
"v_mfma_f32_16x16x32_bf16 v[56:59], a[100:103], a[92:95], v[56:59]\n" \
"ds_read_b64_tr_b16 a[202:203], v210 offset:3072\n" \
"v_mov_b32 v175, v159\n" \
"v_mfma_f32_16x16x32_bf16 v[16:19], a[104:107], a[88:91], v[16:19]\n" \
"ds_read_b64_tr_b16 a[204:205], v210 offset:14336\n" \
"v_add_u32 v151, 3, v146\n" \
"v_mfma_f32_16x16x32_bf16 v[16:19], a[108:111], a[92:95], v[16:19]\n" \
"global_load_lds_dwordx4 v253, s[50:51] offset:1024\n" \
"v_or3_b32 v151, v151, v145, s86\n" \
"v_mfma_f32_16x16x32_bf16 v[20:23], a[112:115], a[88:91], v[20:23]\n" \
"ds_read_b64_tr_b16 a[206:207], v210 offset:15360\n" \
"v_ashrrev_i32 v151, 31, v151\n" \
"v_mfma_f32_16x16x32_bf16 v[20:23], a[116:119], a[92:95], v[20:23]\n" \
"s_waitcnt lgkmcnt(3)\n" \
"v_add_u32 v207, 0xffff0000, v207\n" \
"v_add_u32 v208, 0xffff0000, v208\n" \
"v_mfma_f32_16x16x32_bf16 v[28:31], a[112:115], a[192:195], v[28:31]\n" \
"v_add_u32 v205, 0xffff0000, v205\n" \
"v_add_u32 v206, 0xffff0000, v206\n" \
"v_mfma_f32_16x16x32_bf16 v[28:31], a[116:119], a[196:199], v[28:31]\n" \
"v_add_u32 v209, 0xffff0000, v209\n" \
"v_add_u32 v210, 0xffff0000, v210\n" \
"v_mfma_f32_16x16x32_bf16 v[24:27], a[104:107], a[192:195], v[24:27]\n" \
"v_and_b32 v151, 0xff800000, v151\n" \
"v_mov_b32 v144, 0x3e8293ee\n" \
"v_mfma_f32_16x16x32_bf16 v[24:27], a[108:111], a[196:199], v[24:27]\n" \
"s_waitcnt vmcnt(12) lgkmcnt(0)\n" \
"s_add_u32 s90, s95, 117760\n" \
"v_add_u32 v223, s90, v248\n" \
"v_mfma_f32_16x16x32_bf16 v[64:67], a[96:99], a[192:195], v[64:67]\n" \
"s_cmp_lg_u32 s89, 0\n" \
"s_cselect_b64 s[92:93], -1, 0\n" \
"v_xor_b32 v145, 0x2000, v223\n" \
"v_mfma_f32_16x16x32_bf16 v[64:67], a[100:103], a[196:199], v[64:67]\n" \
"s_barrier\n" \
"v_mfma_f32_16x16x32_bf16 v[72:75], a[64:67], a[208:211], v[72:75]\n" \
"s_waitcnt vmcnt(11)\n" \
"ds_read_b64_tr_b16 a[96:97], v209 offset:2048\n" \
"v_cmp_ne_u32 vcc, s60, v220\n" \
"v_mfma_f32_16x16x32_bf16 v[72:75], a[68:71], a[212:215], v[72:75]\n" \
"s_cmp_eq_u32 s89, 0\n" \
"s_cselect_b32 s16, 0, 0xff00\n" \
"ds_read_b64_tr_b16 a[98:99], v209 offset:3072\n" \
"v_mfma_f32_16x16x32_bf16 v[80:83], a[72:75], a[208:211], v[80:83]\n" \
"s_mov_b32 s17, s16\n" \
"ds_read_b128 a[64:67], v207 offset:0\n" \
"ds_read_b64_tr_b16 a[100:101], v209 offset:14336\n" \
"v_mfma_f32_16x16x32_bf16 v[80:83], a[76:79], a[212:215], v[80:83]\n" \
"s_and_b32 vcc_hi, vcc_lo, s80\n" \
"s_cbranch_scc1 Lpl2_%=\n" \
"Lpr2_%=:\n" \
"v_mfma_f32_16x16x32_bf16 v[84:87], a[72:75], a[216:219], v[84:87]\n" \
"s_add_u32 m0, s75, s95\n" \
"ds_read_b128 a[68:71], v208 offset:0\n" \
"ds_read_b64_tr_b16 a[102:103], v209 offset:15360\n" \
"v_mfma_f32_16x16x32_bf16 v[84:87], a[76:79], a[220:223], v[84:87]\n" \
"ds_read_b64_tr_b16 a[104:105], v209 offset:0\n" \
"global_load_lds_dwordx4 v222, s[36:37] offset:0 sc1\n" \
"v_mfma_f32_16x16x32_bf16 v[44:47], a[200:203], a[72:75], v[44:47]\n" \
"ds_read_b64_tr_b16 a[106:107], v209 offset:1024\n" \
"v_add_u32 v146, 0x1c90080, v249\n" \
"v_mfma_f32_16x16x32_bf16 v[44:47], a[204:207], a[76:79], v[44:47]\n" \
"v_cndmask_b32 v146, v249, v146, s[16:17]\n" \
"v_mfma_f32_16x16x32_bf16 v[52:55], a[200:203], a[80:83], v[52:55]\n" \
"ds_read_b128 a[72:75], v205 offset:0\n" \
"v_mfma_f32_16x16x32_bf16 v[52:55], a[204:207], a[84:87], v[52:55]\n" \
"ds_read_b128 a[76:79], v206 offset:0\n" \
"global_load_lds_dwordx4 v222, s[36:37] offset:1024 sc1\n" \
"v_mfma_f32_16x16x32_bf16 v[92:95], a[80:83], a[216:219], v[92:95]\n" \
"ds_read_b64_tr_b16 a[108:109], v209 offset:12288\n" \
"v_mfma_f32_16x16x32_bf16 v[92:95], a[84:87], a[220:223], v[92:95]\n" \
"v_mfma_f32_16x16x32_bf16 v[88:91], a[80:83], a[208:211], v[88:91]\n" \
"ds_read_b64_tr_b16 a[110:111], v209 offset:13312\n" \
"v_mfma_f32_16x16x32_bf16 v[88:91], a[84:87], a[212:215], v[88:91]\n" \
"ds_read_b64_tr_b16 a[112:113], v210 offset:0\n" \
"global_load_lds_dwordx4 v222, s[36:37] offset:2048 sc1\n" \
"v_mfma_f32_16x16x32_bf16 v[96:99], a[88:91], a[208:211], v[96:99]\n" \
"ds_read_b128 a[80:83], v205 offset:4096\n" \
"v_mfma_f32_16x16x32_bf16 v[96:99], a[92:95], a[212:215], v[96:99]\n" \
"v_mfma_f32_16x16x32_bf16 v[100:103], a[88:91], a[216:219], v[100:103]\n" \
"ds_read_b128 a[84:87], v206 offset:4096\n" \
"v_mfma_f32_16x16x32_bf16 v[100:103], a[92:95], a[220:223], v[100:103]\n" \
"ds_read_b64_tr_b16 a[114:115], v210 offset:1024\n" \
"global_load_lds_dwordx4 v222, s[36:37] offset:3072 sc1\n" \
"v_mfma_f32_16x16x32_bf16 v[60:63], a[200:203], a[88:91], v[60:63]\n" \
"s_add_u32 s36, s36, 0x1000\n" \
"ds_read_b64_tr_b16 a[116:117], v210 offset:12288\n" \
"v_mfma_f32_16x16x32_bf16 v[60:63], a[204:207], a[92:95], v[60:63]\n" \
"v_mfma_f32_16x16x32_bf16 v[68:71], a[200:203], a[192:195], v[68:71]\n" \
"ds_read_b128 a[88:91], v205 offset:8192\n" \
"s_add_u32 m0, m0, 0x5000\n" \
"v_mfma_f32_16x16x32_bf16 v[68:71], a[204:207], a[196:199], v[68:71]\n" \
"ds_read_b128 a[92:95], v206 offset:8192\n" \
"global_load_lds_dwordx4 v216, s[38:39] offset:-4096\n" \
"v_mfma_f32_16x16x32_bf16 v[108:111], a[192:195], a[216:219], v[108:111]\n" \
"ds_read_b64_tr_b16 a[118:119], v210 offset:13312\n" \
"v_mfma_f32_16x16x32_bf16 v[108:111], a[196:199], a[220:223], v[108:111]\n" \
"v_mfma_f32_16x16x32_bf16 v[104:107], a[192:195], a[208:211], v[104:107]\n" \
"v_mfma_f32_16x16x32_bf16 v[104:107], a[196:199], a[212:215], v[104:107]\n" \
"s_waitcnt lgkmcnt(2)\n" \
"global_load_lds_dwordx4 v217, s[38:39] offset:-3072\n" \
"v_mfma_f32_16x16x32_bf16 v[32:35], a[96:99], a[64:67], v[32:35]\n" \
"ds_read_b128 a[192:195], v205 offset:12288\n" \
"v_mfma_f32_16x16x32_bf16 v[32:35], a[100:103], a[68:71], v[32:35]\n" \
"v_mfma_f32_16x16x32_bf16 v[40:43], a[96:99], a[72:75], v[40:43]\n" \
"ds_read_b128 a[196:199], v206 offset:12288\n" \
"v_mfma_f32_16x16x32_bf16 v[40:43], a[100:103], a[76:79], v[40:43]\n" \
"ds_read_b64_tr_b16 a[208:209], v209 offset:4096\n" \
"global_load_lds_dwordx4 v218, s[38:39] offset:-2048\n" \
"v_mfma_f32_16x16x32_bf16 v[0:3], a[104:107], a[72:75], v[0:3]\n" \
"ds_read_b64_tr_b16 a[210:211], v209 offset:5120\n" \
"v_mfma_f32_16x16x32_bf16 v[0:3], a[108:111], a[76:79], v[0:3]\n" \
"s_waitcnt lgkmcnt(3)\n" \
"v_mfma_f32_16x16x32_bf16 v[4:7], a[112:115], a[72:75], v[4:7]\n" \
"ds_read_b64_tr_b16 a[212:213], v209 offset:16384\n" \
"v_mfma_f32_16x16x32_bf16 v[4:7], a[116:119], a[76:79], v[4:7]\n" \
"ds_read_b64_tr_b16 a[214:215], v209 offset:17408\n" \
"global_load_lds_dwordx4 v219, s[38:39] offset:-1024\n" \
"v_mfma_f32_16x16x32_bf16 v[12:15], a[112:115], a[80:83], v[12:15]\n" \
"s_add_u32 s38, s38, 0x20000\n" \
"s_addc_u32 s39, s39, 0\n" \
"ds_read_b64_tr_b16 a[216:217], v210 offset:4096\n" \
"v_mfma_f32_16x16x32_bf16 v[12:15], a[116:119], a[84:87], v[12:15]\n" \
"s_sub_u32 s75, s75, 32768\n" \
"s_cselect_b32 s75, 131072, s75\n" \
"v_mfma_f32_16x16x32_bf16 v[8:11], a[104:107], a[80:83], v[8:11]\n" \
"ds_read_b64_tr_b16 a[218:219], v210 offset:5120\n" \
"v_mfma_f32_16x16x32_bf16 v[8:11], a[108:111], a[84:87], v[8:11]\n" \
"ds_read_b64_tr_b16 a[220:221], v210 offset:16384\n" \
"v_mfma_f32_16x16x32_bf16 v[48:51], a[96:99], a[80:83], v[48:51]\n" \
"ds_read_b64_tr_b16 a[222:223], v210 offset:17408\n" \
"v_mfma_f32_16x16x32_bf16 v[48:51], a[100:103], a[84:87], v[48:51]\n" \
"v_mfma_f32_16x16x32_bf16 v[56:59], a[96:99], a[88:91], v[56:59]\n" \
"ds_read_b64_tr_b16 a[200:201], v210 offset:2048\n" \
"v_mfma_f32_16x16x32_bf16 v[56:59], a[100:103], a[92:95], v[56:59]\n" \
"ds_read_b64_tr_b16 a[202:203], v210 offset:3072\n" \
"v_mfma_f32_16x16x32_bf16 v[16:19], a[104:107], a[88:91], v[16:19]\n" \
"ds_read_b64_tr_b16 a[204:205], v210 offset:14336\n" \
"v_mfma_f32_16x16x32_bf16 v[16:19], a[108:111], a[92:95], v[16:19]\n" \
"v_mfma_f32_16x16x32_bf16 v[20:23], a[112:115], a[88:91], v[20:23]\n" \
"ds_read_b64_tr_b16 a[206:207], v210 offset:15360\n" \
"v_mfma_f32_16x16x32_bf16 v[20:23], a[116:119], a[92:95], v[20:23]\n" \
"s_waitcnt lgkmcnt(3)\n" \
"v_add_u32 v207, 0x1d000, v207\n" \
"v_add_u32 v208, 0x1d000, v208\n" \
"v_mfma_f32_16x16x32_bf16 v[28:31], a[112:115], a[192:195], v[28:31]\n" \
"v_add_u32 v205, 0x1d000, v205\n" \
"v_add_u32 v206, 0x1d000, v206\n" \
"v_mfma_f32_16x16x32_bf16 v[28:31], a[116:119], a[196:199], v[28:31]\n" \
"v_add_u32 v209, 0x1d000, v209\n" \
"v_add_u32 v210, 0x1d000, v210\n" \
"v_mfma_f32_16x16x32_bf16 v[24:27], a[104:107], a[192:195], v[24:27]\n" \
"v_mfma_f32_16x16x32_bf16 v[24:27], a[108:111], a[196:199], v[24:27]\n" \
"s_waitcnt vmcnt(8) lgkmcnt(0)\n" \
"v_mfma_f32_16x16x32_bf16 v[64:67], a[96:99], a[192:195], v[64:67]\n" \
"v_mfma_f32_16x16x32_bf16 v[64:67], a[100:103], a[196:199], v[64:67]\n" \
"s_barrier\n" \
"v_mfma_f32_16x16x32_bf16 v[72:75], a[64:67], a[208:211], v[72:75]\n" \
"ds_read_b64_tr_b16 a[96:97], v209 offset:2048\n" \
"v_mfma_f32_16x16x32_bf16 v[72:75], a[68:71], a[212:215], v[72:75]\n" \
"ds_read_b64_tr_b16 a[98:99], v209 offset:3072\n" \
"v_mfma_f32_16x16x32_bf16 v[80:83], a[72:75], a[208:211], v[80:83]\n" \
"s_add_u32 m0, s75, s95\n" \
"ds_read_b128 a[64:67], v207 offset:0\n" \
"ds_read_b64_tr_b16 a[100:101], v209 offset:14336\n" \
"v_mfma_f32_16x16x32_bf16 v[80:83], a[76:79], a[212:215], v[80:83]\n" \
"global_load_lds_dwordx4 v222, s[36:37] offset:0 sc1\n" \
"v_mfma_f32_16x16x32_bf16 v[84:87], a[72:75], a[216:219], v[84:87]\n" \
"ds_read_b128 a[68:71], v208 offset:0\n" \
"ds_read_b64_tr_b16 a[102:103], v209 offset:15360\n" \
"v_mfma_f32_16x16x32_bf16 v[84:87], a[76:79], a[220:223], v[84:87]\n" \
"ds_read_b64_tr_b16 a[104:105], v209 offset:0\n" \
"v_mfma_f32_16x16x32_bf16 v[44:47], a[200:203], a[72:75], v[44:47]\n" \
"ds_read_b64_tr_b16 a[106:107], v209 offset:1024\n" \
"v_mfma_f32_16x16x32_bf16 v[44:47], a[204:207], a[76:79], v[44:47]\n" \
"global_load_lds_dwordx4 v222, s[36:37] offset:1024 sc1\n" \
"v_mfma_f32_16x16x32_bf16 v[52:55], a[200:203], a[80:83], v[52:55]\n" \
"ds_read_b128 a[72:75], v205 offset:0\n" \
"v_mfma_f32_16x16x32_bf16 v[52:55], a[204:207], a[84:87], v[52:55]\n" \
"ds_read_b128 a[76:79], v206 offset:0\n" \
"v_mfma_f32_16x16x32_bf16 v[92:95], a[80:83], a[216:219], v[92:95]\n" \
"ds_read_b64_tr_b16 a[108:109], v209 offset:12288\n" \
"v_mfma_f32_16x16x32_bf16 v[92:95], a[84:87], a[220:223], v[92:95]\n" \
"Lpg10884_%=:\n" \
"s_cbranch_execz Lpg12454_%=\n" \
"global_load_lds_dwordx4 v222, s[36:37] offset:2048 sc1\n" \
"v_mfma_f32_16x16x32_bf16 v[88:91], a[80:83], a[208:211], v[88:91]\n" \
"ds_read_b64_tr_b16 a[110:111], v209 offset:13312\n" \
"v_mfma_f32_16x16x32_bf16 v[88:91], a[84:87], a[212:215], v[88:91]\n" \
"ds_read_b64_tr_b16 a[112:113], v210 offset:0\n" \
"v_mfma_f32_16x16x32_bf16 v[96:99], a[88:91], a[208:211], v[96:99]\n" \
"ds_read_b128 a[80:83], v205 offset:4096\n" \
"v_mfma_f32_16x16x32_bf16 v[96:99], a[92:95], a[212:215], v[96:99]\n" \
"global_load_lds_dwordx4 v222, s[36:37] offset:3072 sc1\n" \
"v_mfma_f32_16x16x32_bf16 v[100:103], a[88:91], a[216:219], v[100:103]\n" \
"ds_read_b128 a[84:87], v206 offset:4096\n" \
"s_add_u32 s36, s36, 0x1000\n" \
"v_mfma_f32_16x16x32_bf16 v[100:103], a[92:95], a[220:223], v[100:103]\n" \
"ds_read_b64_tr_b16 a[114:115], v210 offset:1024\n" \
"v_mfma_f32_16x16x32_bf16 v[60:63], a[200:203], a[88:91], v[60:63]\n" \
"s_add_u32 m0, m0, 0x5000\n" \
"ds_read_b64_tr_b16 a[116:117], v210 offset:12288\n" \
"v_mfma_f32_16x16x32_bf16 v[60:63], a[204:207], a[92:95], v[60:63]\n" \
"global_load_lds_dwordx4 v216, s[38:39] offset:-4096\n" \
"v_mfma_f32_16x16x32_bf16 v[68:71], a[200:203], a[192:195], v[68:71]\n" \
"ds_read_b128 a[88:91], v205 offset:8192\n" \
"v_mfma_f32_16x16x32_bf16 v[68:71], a[204:207], a[196:199], v[68:71]\n" \
"ds_read_b128 a[92:95], v206 offset:8192\n" \
"v_mfma_f32_16x16x32_bf16 v[108:111], a[192:195], a[216:219], v[108:111]\n" \
"ds_read_b64_tr_b16 a[118:119], v210 offset:13312\n" \
"v_mfma_f32_16x16x32_bf16 v[108:111], a[196:199], a[220:223], v[108:111]\n" \
"global_load_lds_dwordx4 v217, s[38:39] offset:-3072\n" \
"v_mfma_f32_16x16x32_bf16 v[104:107], a[192:195], a[208:211], v[104:107]\n" \
"v_mfma_f32_16x16x32_bf16 v[104:107], a[196:199], a[212:215], v[104:107]\n" \
"s_waitcnt lgkmcnt(2)\n" \
"v_mfma_f32_16x16x32_bf16 v[32:35], a[96:99], a[64:67], v[32:35]\n" \
"ds_read_b128 a[192:195], v205 offset:12288\n" \
"v_mfma_f32_16x16x32_bf16 v[32:35], a[100:103], a[68:71], v[32:35]\n" \
"global_load_lds_dwordx4 v218, s[38:39] offset:-2048\n" \
"v_mfma_f32_16x16x32_bf16 v[40:43], a[96:99], a[72:75], v[40:43]\n" \
"ds_read_b128 a[196:199], v206 offset:12288\n" \
"v_mfma_f32_16x16x32_bf16 v[40:43], a[100:103], a[76:79], v[40:43]\n" \
"ds_read_b64_tr_b16 a[208:209], v209 offset:4096\n" \
"v_mfma_f32_16x16x32_bf16 v[0:3], a[104:107], a[72:75], v[0:3]\n" \
"ds_read_b64_tr_b16 a[210:211], v209 offset:5120\n" \
"v_mfma_f32_16x16x32_bf16 v[0:3], a[108:111], a[76:79], v[0:3]\n" \
"s_waitcnt lgkmcnt(3)\n" \
"global_load_lds_dwordx4 v219, s[38:39] offset:-1024\n" \
"v_mfma_f32_16x16x32_bf16 v[4:7], a[112:115], a[72:75], v[4:7]\n" \
"s_add_u32 s38, s38, 0x20000\n" \
"s_addc_u32 s39, s39, 0\n" \
"ds_read_b64_tr_b16 a[212:213], v209 offset:16384\n" \
"v_mfma_f32_16x16x32_bf16 v[4:7], a[116:119], a[76:79], v[4:7]\n" \
"s_sub_u32 s75, s75, 32768\n" \
"s_cselect_b32 s75, 131072, s75\n" \
"ds_read_b64_tr_b16 a[214:215], v209 offset:17408\n" \
"v_mfma_f32_16x16x32_bf16 v[48:51], a[96:99], a[80:83], v[48:51]\n" \
"s_add_u32 m0, s75, s95\n" \
"ds_read_b64_tr_b16 a[216:217], v210 offset:4096\n" \
"v_mfma_f32_16x16x32_bf16 v[48:51], a[100:103], a[84:87], v[48:51]\n" \
"global_load_lds_dwordx4 v222, s[36:37] offset:0 sc1\n" \
"v_mfma_f32_16x16x32_bf16 v[8:11], a[104:107], a[80:83], v[8:11]\n" \
"ds_read_b64_tr_b16 a[218:219], v210 offset:5120\n" \
"v_cvt_pk_bf16_f32 v112, v0, v1\n" \
"v_mfma_f32_16x16x32_bf16 v[8:11], a[108:111], a[84:87], v[8:11]\n" \
"ds_read_b64_tr_b16 a[220:221], v210 offset:16384\n" \
"v_cvt_pk_bf16_f32 v113, v2, v3\n" \
"v_mfma_f32_16x16x32_bf16 v[12:15], a[112:115], a[80:83], v[12:15]\n" \
"ds_read_b64_tr_b16 a[222:223], v210 offset:17408\n" \
"v_cvt_pk_bf16_f32 v114, v4, v5\n" \
"v_mfma_f32_16x16x32_bf16 v[12:15], a[116:119], a[84:87], v[12:15]\n" \
"global_load_lds_dwordx4 v222, s[36:37] offset:1024 sc1\n" \
"v_cvt_pk_bf16_f32 v115, v6, v7\n" \
"v_mfma_f32_16x16x32_bf16 v[56:59], a[96:99], a[88:91], v[56:59]\n" \
"ds_read_b64_tr_b16 a[200:201], v210 offset:2048\n" \
"v_mfma_f32_16x16x32_bf16 v[56:59], a[100:103], a[92:95], v[56:59]\n" \
"ds_read_b64_tr_b16 a[202:203], v210 offset:3072\n" \
"v_mfma_f32_16x16x32_bf16 v[16:19], a[104:107], a[88:91], v[16:19]\n" \
"ds_read_b64_tr_b16 a[204:205], v210 offset:14336\n" \
"v_cvt_pk_bf16_f32 v116, v8, v9\n" \
"v_mfma_f32_16x16x32_bf16 v[16:19], a[108:111], a[92:95], v[16:19]\n" \
"global_load_lds_dwordx4 v222, s[36:37] offset:2048 sc1\n" \
"v_cvt_pk_bf16_f32 v117, v10, v11\n" \
"v_mfma_f32_16x16x32_bf16 v[20:23], a[112:115], a[88:91], v[20:23]\n" \
"ds_read_b64_tr_b16 a[206:207], v210 offset:15360\n" \
"v_cvt_pk_bf16_f32 v118, v12, v13\n" \
"v_mfma_f32_16x16x32_bf16 v[20:23], a[116:119], a[92:95], v[20:23]\n" \
"s_waitcnt lgkmcnt(3)\n" \
"v_cvt_pk_bf16_f32 v119, v14, v15\n" \
"v_mfma_f32_16x16x32_bf16 v[64:67], a[96:99], a[192:195], v[64:67]\n" \
"v_mfma_f32_16x16x32_bf16 v[64:67], a[100:103], a[196:199], v[64:67]\n" \
"global_load_lds_dwordx4 v222, s[36:37] offset:3072 sc1\n" \
"v_mfma_f32_16x16x32_bf16 v[24:27], a[104:107], a[192:195], v[24:27]\n" \
"v_cvt_pk_bf16_f32 v120, v16, v17\n" \
"v_cvt_pk_bf16_f32 v121, v18, v19\n" \
"v_mfma_f32_16x16x32_bf16 v[24:27], a[108:111], a[196:199], v[24:27]\n" \
"s_waitcnt vmcnt(12) lgkmcnt(0)\n" \
"v_cvt_pk_bf16_f32 v122, v20, v21\n" \
"v_cvt_pk_bf16_f32 v123, v22, v23\n" \
"v_mfma_f32_16x16x32_bf16 v[28:31], a[112:115], a[192:195], v[28:31]\n" \
"v_mfma_f32_16x16x32_bf16 v[28:31], a[116:119], a[196:199], v[28:31]\n" \
"s_barrier\n" \
"v_mfma_f32_16x16x32_bf16 v[72:75], a[64:67], a[208:211], v[72:75]\n" \
"ds_read_b64_tr_b16 a[32:33], v214 offset:16384\n" \
"v_mfma_f32_16x16x32_bf16 v[72:75], a[68:71], a[212:215], v[72:75]\n" \
"v_mfma_f32_16x16x32_bf16 v[44:47], a[200:203], a[72:75], v[44:47]\n" \
"s_add_u32 s36, s36, 0x1000\n" \
"s_add_u32 m0, m0, 0x5000\n" \
"ds_read_b64_tr_b16 a[34:35], v214 offset:18432\n" \
"v_mfma_f32_16x16x32_bf16 v[44:47], a[204:207], a[76:79], v[44:47]\n" \
"ds_read_b64_tr_b16 a[36:37], v214 offset:24576\n" \
"global_load_lds_dwordx4 v216, s[38:39] offset:-4096\n" \
"v_mfma_f32_16x16x32_bf16 v[80:83], a[72:75], a[208:211], v[80:83]\n" \
"v_cvt_pk_bf16_f32 v124, v24, v25\n" \
"v_cvt_pk_bf16_f32 v125, v26, v27\n" \
"v_mfma_f32_16x16x32_bf16 v[80:83], a[76:79], a[212:215], v[80:83]\n" \
"ds_read_b64_tr_b16 a[38:39], v214 offset:26624\n" \
"v_cvt_pk_bf16_f32 v126, v28, v29\n" \
"v_mfma_f32_16x16x32_bf16 v[84:87], a[72:75], a[216:219], v[84:87]\n" \
"ds_read_b128 a[0:3], v213 offset:0\n" \
"v_cvt_pk_bf16_f32 v127, v30, v31\n" \
"v_mfma_f32_16x16x32_bf16 v[84:87], a[76:79], a[220:223], v[84:87]\n" \
"global_load_lds_dwordx4 v217, s[38:39] offset:-3072\n" \
"v_cvt_pk_bf16_f32 v128, v32, v33\n" \
"v_mfma_f32_16x16x32_bf16 v[52:55], a[200:203], a[80:83], v[52:55]\n" \
"ds_read_b128 a[4:7], v213 offset:2048\n" \
"v_cvt_pk_bf16_f32 v129, v34, v35\n" \
"v_mfma_f32_16x16x32_bf16 v[52:55], a[204:207], a[84:87], v[52:55]\n" \
"ds_read_b64_tr_b16 a[40:41], v215 offset:16384\n" \
"v_cvt_pk_bf16_f32 v130, v72, v73\n" \
"v_mfma_f32_16x16x32_bf16 v[88:91], a[80:83], a[208:211], v[88:91]\n" \
"v_cvt_pk_bf16_f32 v131, v74, v75\n" \
"v_mfma_f32_16x16x32_bf16 v[88:91], a[84:87], a[212:215], v[88:91]\n" \
"ds_read_b64_tr_b16 a[42:43], v215 offset:18432\n" \
"global_load_lds_dwordx4 v218, s[38:39] offset:-2048\n" \
"v_mfma_f32_16x16x32_bf16 v[92:95], a[80:83], a[216:219], v[92:95]\n" \
"ds_read_b64_tr_b16 a[44:45], v215 offset:24576\n" \
"v_mfma_f32_16x16x32_bf16 v[92:95], a[84:87], a[220:223], v[92:95]\n" \
"ds_write_b128 v145, v[128:131]\n" \
"v_cvt_pk_bf16_f32 v132, v40, v41\n" \
"v_mfma_f32_16x16x32_bf16 v[60:63], a[200:203], a[88:91], v[60:63]\n" \
"ds_read_b64_tr_b16 a[46:47], v215 offset:26624\n" \
"v_cvt_pk_bf16_f32 v133, v42, v43\n" \
"v_mfma_f32_16x16x32_bf16 v[60:63], a[204:207], a[92:95], v[60:63]\n" \
"ds_read_b128 a[8:11], v213 offset:256\n" \
"global_load_lds_dwordx4 v219, s[38:39] offset:-1024\n" \
"v_mfma_f32_16x16x32_bf16 v[96:99], a[88:91], a[208:211], v[96:99]\n" \
"s_add_u32 s38, s38, 0x20000\n" \
"s_addc_u32 s39, s39, 0\n" \
"v_cvt_pk_bf16_f32 v134, v44, v45\n" \
"v_mfma_f32_16x16x32_bf16 v[96:99], a[92:95], a[212:215], v[96:99]\n" \
"s_sub_u32 s75, s75, 32768\n" \
"s_cselect_b32 s75, 131072, s75\n" \
"ds_read_b128 a[12:15], v213 offset:2304\n" \
"v_mfma_f32_16x16x32_bf16 v[100:103], a[88:91], a[216:219], v[100:103]\n" \
"s_add_u32 m0, s75, s95\n" \
"ds_read_b128 a[16:19], v213 offset:4096\n" \
"v_cvt_pk_bf16_f32 v135, v46, v47\n" \
"v_mfma_f32_16x16x32_bf16 v[100:103], a[92:95], a[220:223], v[100:103]\n" \
"global_load_lds_dwordx4 v222, s[36:37] offset:0 sc1\n" \
"v_cvt_pk_bf16_f32 v136, v48, v49\n" \
"v_mfma_f32_16x16x32_bf16 v[68:71], a[200:203], a[192:195], v[68:71]\n" \
"ds_read_b128 a[20:23], v213 offset:6144\n" \
"v_cvt_pk_bf16_f32 v137, v50, v51\n" \
"v_mfma_f32_16x16x32_bf16 v[68:71], a[204:207], a[196:199], v[68:71]\n" \
"v_cvt_pk_bf16_f32 v138, v52, v53\n" \
"v_cvt_pk_bf16_f32 v139, v54, v55\n" \
"v_mfma_f32_16x16x32_bf16 v[104:107], a[192:195], a[208:211], v[104:107]\n" \
"v_cvt_pk_bf16_f32 v140, v56, v57\n" \
"v_cvt_pk_bf16_f32 v141, v58, v59\n" \
"v_mfma_f32_16x16x32_bf16 v[104:107], a[196:199], a[212:215], v[104:107]\n" \
"global_load_lds_dwordx4 v222, s[36:37] offset:1024 sc1\n" \
"v_cvt_pk_bf16_f32 v142, v60, v61\n" \
"v_mfma_f32_16x16x32_bf16 v[108:111], a[192:195], a[216:219], v[108:111]\n" \
"v_cvt_pk_bf16_f32 v143, v62, v63\n" \
"v_mfma_f32_16x16x32_bf16 v[108:111], a[196:199], a[220:223], v[108:111]\n" \
"s_waitcnt lgkmcnt(0)\n" \
"v_mfma_f32_16x16x32_bf16 v[0:3], a[32:35], a[0:3], 0\n" \
"ds_read_b128 a[24:27], v213 offset:4352\n" \
"v_mfma_f32_16x16x32_bf16 v[0:3], a[36:39], a[4:7], v[0:3]\n" \
"ds_read_b128 a[28:31], v213 offset:6400\n" \
"global_load_lds_dwordx4 v222, s[36:37] offset:2048 sc1\n" \
"v_mfma_f32_16x16x32_bf16 v[4:7], a[40:43], a[0:3], 0\n" \
"s_sub_u32 s74, s74, 32768\n" \
"s_cselect_b32 s74, 131072, s74\n" \
"ds_read_b64_tr_b16 a[48:49], v214 offset:16896\n" \
"v_mfma_f32_16x16x32_bf16 v[4:7], a[44:47], a[4:7], v[4:7]\n" \
"ds_read_b64_tr_b16 a[50:51], v214 offset:18944\n" \
"v_cvt_pk_bf16_f32 v64, v64, v65\n" \
"v_mfma_f32_16x16x32_bf16 v[20:23], a[40:43], a[8:11], 0\n" \
"ds_read_b64_tr_b16 a[52:53], v214 offset:25088\n" \
"global_load_lds_dwordx4 v222, s[36:37] offset:3072 sc1\n" \
"v_mfma_f32_16x16x32_bf16 v[20:23], a[44:47], a[12:15], v[20:23]\n" \
"s_add_u32 s36, s36, 0x1000\n" \
"ds_read_b64_tr_b16 a[54:55], v214 offset:27136\n" \
"v_cvt_pk_bf16_f32 v65, v66, v67\n" \
"v_mfma_f32_16x16x32_bf16 v[16:19], a[32:35], a[8:11], 0\n" \
"s_add_u32 m0, m0, 0x5000\n" \
"ds_read_b64_tr_b16 a[56:57], v215 offset:16896\n" \
"v_cvt_pk_bf16_f32 v66, v68, v69\n" \
"v_mfma_f32_16x16x32_bf16 v[16:19], a[36:39], a[12:15], v[16:19]\n" \
"ds_read_b64_tr_b16 a[58:59], v215 offset:18944\n" \
"global_load_lds_dwordx4 v216, s[38:39] offset:-4096\n" \
"v_mfma_f32_16x16x32_bf16 v[32:35], a[32:35], a[16:19], 0\n" \
"ds_read_b64_tr_b16 a[60:61], v215 offset:25088\n" \
"v_cvt_pk_bf16_f32 v67, v70, v71\n" \
"v_mfma_f32_16x16x32_bf16 v[32:35], a[36:39], a[20:23], v[32:35]\n" \
"ds_read_b64_tr_b16 a[62:63], v215 offset:27136\n" \
"v_cvt_pk_bf16_f32 v68, v72, v73\n" \
"v_mfma_f32_16x16x32_bf16 v[36:39], a[40:43], a[16:19], 0\n" \
"global_load_lds_dwordx4 v217, s[38:39] offset:-3072\n" \
"v_add_u32 v213, s74, v211\n" \
"v_mfma_f32_16x16x32_bf16 v[36:39], a[44:47], a[20:23], v[36:39]\n" \
"s_waitcnt lgkmcnt(3)\n" \
"v_add_u32 v214, s74, v212\n" \
"v_xor_b32 v215, 32, v214\n" \
"v_mfma_f32_16x16x32_bf16 v[52:55], a[40:43], a[24:27], 0\n" \
"v_cvt_pk_bf16_f32 v69, v74, v75\n" \
"v_cvt_pk_bf16_f32 v70, v80, v81\n" \
"v_mfma_f32_16x16x32_bf16 v[52:55], a[44:47], a[28:31], v[52:55]\n" \
"s_waitcnt vmcnt(14) lgkmcnt(0)\n" \
"global_load_lds_dwordx4 v218, s[38:39] offset:-2048\n" \
"v_cvt_pk_bf16_f32 v71, v82, v83\n" \
"v_mfma_f32_16x16x32_bf16 v[48:51], a[32:35], a[24:27], 0\n" \
"global_load_lds_dwordx4 v219, s[38:39] offset:-1024\n" \
"v_cvt_pk_bf16_f32 v194, v84, v85\n" \
"v_mfma_f32_16x16x32_bf16 v[48:51], a[36:39], a[28:31], v[48:51]\n" \
"s_barrier\n" \
"v_cvt_pk_bf16_f32 v195, v86, v87\n" \
"v_mfma_f32_16x16x32_bf16 v[8:11], a[48:51], a[0:3], 0\n" \
"ds_read_b64 v[130:131], v223\n" \
"ds_read_b64_tr_b16 a[32:33], v214 offset:16384\n" \
"v_mfma_f32_16x16x32_bf16 v[8:11], a[52:55], a[4:7], v[8:11]\n" \
"ds_read_b64 v[192:193], v223 offset:8\n" \
"ds_read_b64_tr_b16 a[34:35], v214 offset:18432\n" \
"v_mfma_f32_16x16x32_bf16 v[12:15], a[56:59], a[0:3], 0\n" \
"s_add_u32 s38, s38, 0x20000\n" \
"s_addc_u32 s39, s39, 0\n" \
"ds_read_b64_tr_b16 a[36:37], v214 offset:24576\n" \
"v_mfma_f32_16x16x32_bf16 v[12:15], a[60:63], a[4:7], v[12:15]\n" \
"s_sub_u32 s75, s75, 32768\n" \
"s_cselect_b32 s75, 131072, s75\n" \
"ds_read_b128 a[0:3], v213 offset:0\n" \
"v_mfma_f32_16x16x32_bf16 v[28:31], a[56:59], a[8:11], 0\n" \
"s_add_u32 m0, s75, s95\n" \
"ds_read_b128 a[4:7], v213 offset:2048\n" \
"v_cvt_pk_bf16_f32 v72, v88, v89\n" \
"v_mfma_f32_16x16x32_bf16 v[28:31], a[60:63], a[12:15], v[28:31]\n" \
"ds_read_b64_tr_b16 a[38:39], v214 offset:26624\n" \
"global_load_lds_dwordx4 v222, s[36:37] offset:0 sc1\n" \
"v_mfma_f32_16x16x32_bf16 v[24:27], a[48:51], a[8:11], 0\n" \
"ds_read_b64_tr_b16 a[40:41], v215 offset:16384\n" \
"v_cvt_pk_bf16_f32 v73, v90, v91\n" \
"v_mfma_f32_16x16x32_bf16 v[24:27], a[52:55], a[12:15], v[24:27]\n" \
"s_waitcnt lgkmcnt(4)\n" \
"ds_read_b128 a[8:11], v213 offset:256\n" \
"global_load_lds_dwordx4 v222, s[36:37] offset:1024 sc1\n" \
"v_mfma_f32_16x16x32_bf16 v[40:43], a[48:51], a[16:19], 0\n" \
"ds_read_b128 a[12:15], v213 offset:2304\n" \
"v_cvt_pk_bf16_f32 v196, v92, v93\n" \
"v_mfma_f32_16x16x32_bf16 v[40:43], a[52:55], a[20:23], v[40:43]\n" \
"ds_read_b64_tr_b16 a[42:43], v215 offset:18432\n" \
"v_cvt_pk_bf16_f32 v197, v94, v95\n" \
"v_mfma_f32_16x16x32_bf16 v[44:47], a[56:59], a[16:19], 0\n" \
"ds_read_b64_tr_b16 a[44:45], v215 offset:24576\n" \
"global_load_lds_dwordx4 v222, s[36:37] offset:2048 sc1\n" \
"v_mfma_f32_16x16x32_bf16 v[44:47], a[60:63], a[20:23], v[44:47]\n" \
"ds_read_b128 a[16:19], v213 offset:4096\n" \
"v_cvt_pk_bf16_f32 v74, v96, v97\n" \
"v_mfma_f32_16x16x32_bf16 v[60:63], a[56:59], a[24:27], 0\n" \
"ds_read_b128 a[20:23], v213 offset:6144\n" \
"global_load_lds_dwordx4 v222, s[36:37] offset:3072 sc1\n" \
"v_mfma_f32_16x16x32_bf16 v[60:63], a[60:63], a[28:31], v[60:63]\n" \
"ds_read_b64_tr_b16 a[46:47], v215 offset:26624\n" \
"v_cvt_pk_bf16_f32 v75, v98, v99\n" \
"v_mfma_f32_16x16x32_bf16 v[56:59], a[48:51], a[24:27], 0\n" \
"v_cvt_pk_bf16_f32 v198, v100, v101\n" \
"v_cvt_pk_bf16_f32 v199, v102, v103\n" \
"v_mfma_f32_16x16x32_bf16 v[56:59], a[52:55], a[28:31], v[56:59]\n" \
"s_waitcnt lgkmcnt(3)\n" \
"v_cvt_pk_bf16_f32 v76, v104, v105\n" \
"v_cvt_pk_bf16_f32 v77, v106, v107\n" \
"v_mfma_f32_16x16x32_bf16 v[0:3], a[32:35], a[0:3], v[0:3]\n" \
"v_cvt_pk_bf16_f32 v200, v108, v109\n" \
"v_cvt_pk_bf16_f32 v201, v110, v111\n" \
"v_mfma_f32_16x16x32_bf16 v[176:179], v[64:67], v[124:127], v[176:179]\n" \
"v_mfma_f32_16x16x32_bf16 v[172:175], v[140:143], v[124:127], v[172:175]\n" \
"s_add_u32 s36, s36, 0x1000\n" \
"ds_read_b128 a[24:27], v213 offset:4352\n" \
"s_add_u32 m0, m0, 0x5000\n" \
"v_mfma_f32_16x16x32_bf16 v[0:3], a[36:39], a[4:7], v[0:3]\n" \
"s_waitcnt lgkmcnt(1)\n" \
"ds_read_b128 a[28:31], v213 offset:6400\n" \
"global_load_lds_dwordx4 v216, s[38:39] offset:-4096\n" \
"v_mfma_f32_16x16x32_bf16 v[168:171], v[140:143], v[120:123], v[168:171]\n" \
"v_mfma_f32_16x16x32_bf16 v[164:167], v[136:139], v[120:123], v[164:167]\n" \
"v_mfma_f32_16x16x32_bf16 v[160:163], v[136:139], v[116:119], v[160:163]\n" \
"v_mfma_f32_16x16x32_bf16 v[156:159], v[132:135], v[116:119], v[156:159]\n" \
"v_mfma_f32_16x16x32_bf16 v[152:155], v[132:135], v[112:115], v[152:155]\n" \
"v_mfma_f32_16x16x32_bf16 v[148:151], v[128:131], v[112:115], v[148:151]\n" \
"v_mfma_f32_16x16x32_bf16 v[4:7], a[40:43], a[0:3], v[4:7]\n" \
"s_sub_u32 s74, s74, 32768\n" \
"s_cselect_b32 s74, 131072, s74\n" \
"ds_read_b64_tr_b16 a[48:49], v214 offset:16896\n" \
"v_mfma_f32_16x16x32_bf16 v[4:7], a[44:47], a[4:7], v[4:7]\n" \
"ds_read_b64_tr_b16 a[50:51], v214 offset:18944\n" \
"v_max3_f32 v183, v172, v173, v174\n" \
"v_max3_f32 v182, v164, v165, v166\n" \
"v_max3_f32 v181, v156, v157, v158\n" \
"v_max3_f32 v180, v148, v149, v150\n" \
"v_max3_f32 v183, v183, v175, v176\n" \
"v_max3_f32 v182, v182, v167, v168\n" \
"v_mfma_f32_16x16x32_bf16 v[20:23], a[40:43], a[8:11], v[20:23]\n" \
"ds_read_b64_tr_b16 a[52:53], v214 offset:25088\n" \
"global_load_lds_dwordx4 v217, s[38:39] offset:-3072\n" \
"v_mfma_f32_16x16x32_bf16 v[20:23], a[44:47], a[12:15], v[20:23]\n" \
"ds_read_b64_tr_b16 a[54:55], v214 offset:27136\n" \
"v_max3_f32 v181, v181, v159, v160\n" \
"v_max3_f32 v180, v180, v151, v152\n" \
"v_max3_f32 v183, v183, v177, v178\n" \
"v_max3_f32 v182, v182, v169, v170\n" \
"v_max3_f32 v181, v181, v161, v162\n" \
"v_max3_f32 v180, v180, v153, v154\n" \
"v_mfma_f32_16x16x32_bf16 v[16:19], a[32:35], a[8:11], v[16:19]\n" \
"ds_read_b64_tr_b16 a[56:57], v215 offset:16896\n" \
"v_max_f32 v183, v183, v179\n" \
"v_mfma_f32_16x16x32_bf16 v[16:19], a[36:39], a[12:15], v[16:19]\n" \
"ds_read_b64_tr_b16 a[58:59], v215 offset:18944\n" \
"global_load_lds_dwordx4 v218, s[38:39] offset:-2048\n" \
"v_max_f32 v182, v182, v171\n" \
"v_max_f32 v181, v181, v163\n" \
"v_max_f32 v180, v180, v155\n" \
"v_mov_b32 v187, v183\n" \
"v_mov_b32 v186, v182\n" \
"v_mov_b32 v185, v181\n" \
"v_mfma_f32_16x16x32_bf16 v[32:35], a[32:35], a[16:19], v[32:35]\n" \
"ds_read_b64_tr_b16 a[60:61], v215 offset:25088\n" \
"v_mov_b32 v184, v180\n" \
"v_mfma_f32_16x16x32_bf16 v[32:35], a[36:39], a[20:23], v[32:35]\n" \
"ds_read_b64_tr_b16 a[62:63], v215 offset:27136\n" \
"v_permlane16_swap_b32 v187, v183\n" \
"v_permlane16_swap_b32 v186, v182\n" \
"v_permlane16_swap_b32 v185, v181\n" \
"v_permlane16_swap_b32 v184, v180\n" \
"v_max_f32 v183, v183, v187\n" \
"v_max_f32 v182, v182, v186\n" \
"v_mfma_f32_16x16x32_bf16 v[36:39], a[40:43], a[16:19], v[36:39]\n" \
"global_load_lds_dwordx4 v219, s[38:39] offset:-1024\n" \
"v_add_u32 v213, s74, v211\n" \
"v_mfma_f32_16x16x32_bf16 v[36:39], a[44:47], a[20:23], v[36:39]\n" \
"s_waitcnt lgkmcnt(3)\n" \
"v_max_f32 v181, v181, v185\n" \
"v_max_f32 v180, v180, v184\n" \
"v_mov_b32 v187, v183\n" \
"v_mov_b32 v186, v182\n" \
"v_mov_b32 v185, v181\n" \
"v_mov_b32 v184, v180\n" \
"s_add_u32 s38, s38, 0x20000\n" \
"s_addc_u32 s39, s39, 0\n" \
"v_mfma_f32_16x16x32_bf16 v[52:55], a[40:43], a[24:27], v[52:55]\n" \
"s_sub_u32 s75, s75, 32768\n" \
"s_cselect_b32 s75, 131072, s75\n" \
"s_add_u32 m0, s75, s95\n" \
"v_mfma_f32_16x16x32_bf16 v[52:55], a[44:47], a[28:31], v[52:55]\n" \
"s_waitcnt vmcnt(16) lgkmcnt(0)\n" \
"global_load_lds_dwordx4 v222, s[36:37] offset:0 sc1\n" \
"v_add_u32 v214, s74, v212\n" \
"v_permlane32_swap_b32 v187, v183\n" \
"v_permlane32_swap_b32 v186, v182\n" \
"v_permlane32_swap_b32 v185, v181\n" \
"v_permlane32_swap_b32 v184, v180\n" \
"v_max_f32 v183, v183, v187\n" \
"v_max_f32 v182, v182, v186\n" \
"v_mfma_f32_16x16x32_bf16 v[48:51], a[32:35], a[24:27], v[48:51]\n" \
"v_xor_b32 v215, 32, v214\n" \
"v_max_f32 v181, v181, v185\n" \
"v_mfma_f32_16x16x32_bf16 v[48:51], a[36:39], a[28:31], v[48:51]\n" \
"s_barrier\n" \
"v_max_f32 v180, v180, v184\n" \
"v_mul_f32 v183, v183, v144\n" \
"v_mul_f32 v182, v182, v144\n" \
"v_mfma_f32_16x16x32_bf16 v[8:11], a[48:51], a[0:3], v[8:11]\n" \
"ds_read_b64_tr_b16 a[32:33], v214 offset:16384\n" \
"v_mul_f32 v181, v181, v144\n" \
"v_mfma_f32_16x16x32_bf16 v[8:11], a[52:55], a[4:7], v[8:11]\n" \
"ds_read_b64_tr_b16 a[34:35], v214 offset:18432\n" \
"global_load_lds_dwordx4 v222, s[36:37] offset:1024 sc1\n" \
"v_mul_f32 v180, v180, v144\n" \
"v_fma_f32 v172, v172, v144, -v183\n" \
"v_mfma_f32_16x16x32_bf16 v[12:15], a[56:59], a[0:3], v[12:15]\n" \
"ds_read_b64_tr_b16 a[36:37], v214 offset:24576\n" \
"v_fma_f32 v164, v164, v144, -v182\n" \
"v_mfma_f32_16x16x32_bf16 v[12:15], a[60:63], a[4:7], v[12:15]\n" \
"ds_read_b128 a[0:3], v213 offset:0\n" \
"v_fma_f32 v156, v156, v144, -v181\n" \
"v_mfma_f32_16x16x32_bf16 v[28:31], a[56:59], a[8:11], v[28:31]\n" \
"ds_read_b128 a[4:7], v213 offset:2048\n" \
"global_load_lds_dwordx4 v222, s[36:37] offset:2048 sc1\n" \
"v_mfma_f32_16x16x32_bf16 v[28:31], a[60:63], a[12:15], v[28:31]\n" \
"ds_read_b64_tr_b16 a[38:39], v214 offset:26624\n" \
"v_exp_f32 v172, v172\n" \
"v_mfma_f32_16x16x32_bf16 v[24:27], a[48:51], a[8:11], v[24:27]\n" \
"ds_read_b64_tr_b16 a[40:41], v215 offset:16384\n" \
"v_fma_f32 v148, v148, v144, -v180\n" \
"v_mfma_f32_16x16x32_bf16 v[24:27], a[52:55], a[12:15], v[24:27]\n" \
"ds_read_b128 a[8:11], v213 offset:256\n" \
"global_load_lds_dwordx4 v222, s[36:37] offset:3072 sc1\n" \
"v_exp_f32 v164, v164\n" \
"v_mfma_f32_16x16x32_bf16 v[40:43], a[48:51], a[16:19], v[40:43]\n" \
"s_add_u32 s36, s36, 0x1000\n" \
"ds_read_b128 a[12:15], v213 offset:2304\n" \
"v_fma_f32 v173, v173, v144, -v183\n" \
"v_mfma_f32_16x16x32_bf16 v[40:43], a[52:55], a[20:23], v[40:43]\n" \
"s_add_u32 m0, m0, 0x5000\n" \
"ds_read_b64_tr_b16 a[42:43], v215 offset:18432\n" \
"v_exp_f32 v156, v156\n" \
"v_fma_f32 v165, v165, v144, -v182\n" \
"v_mfma_f32_16x16x32_bf16 v[44:47], a[56:59], a[16:19], v[44:47]\n" \
"ds_read_b64_tr_b16 a[44:45], v215 offset:24576\n" \
"global_load_lds_dwordx4 v216, s[38:39] offset:-4096\n" \
"v_mfma_f32_16x16x32_bf16 v[44:47], a[60:63], a[20:23], v[44:47]\n" \
"ds_read_b128 a[16:19], v213 offset:4096\n" \
"v_exp_f32 v148, v148\n" \
"v_mfma_f32_16x16x32_bf16 v[60:63], a[56:59], a[24:27], v[60:63]\n" \
"ds_read_b128 a[20:23], v213 offset:6144\n" \
"global_load_lds_dwordx4 v217, s[38:39] offset:-3072\n" \
"v_mfma_f32_16x16x32_bf16 v[60:63], a[60:63], a[28:31], v[60:63]\n" \
"ds_read_b64_tr_b16 a[46:47], v215 offset:26624\n" \
"v_fma_f32 v157, v157, v144, -v181\n" \
"v_mfma_f32_16x16x32_bf16 v[56:59], a[48:51], a[24:27], v[56:59]\n" \
"v_exp_f32 v173, v173\n" \
"v_mfma_f32_16x16x32_bf16 v[56:59], a[52:55], a[28:31], v[56:59]\n" \
"s_waitcnt lgkmcnt(3)\n" \
"v_fma_f32 v149, v149, v144, -v180\n" \
"v_exp_f32 v165, v165\n" \
"v_mfma_f32_16x16x32_bf16 v[0:3], a[32:35], a[0:3], v[0:3]\n" \
"ds_read_b128 a[24:27], v213 offset:4352\n" \
"v_fma_f32 v174, v174, v144, -v183\n" \
"v_mfma_f32_16x16x32_bf16 v[0:3], a[36:39], a[4:7], v[0:3]\n" \
"s_waitcnt lgkmcnt(1)\n" \
"ds_read_b128 a[28:31], v213 offset:6400\n" \
"global_load_lds_dwordx4 v218, s[38:39] offset:-2048\n" \
"v_exp_f32 v157, v157\n" \
"v_fma_f32 v166, v166, v144, -v182\n" \
"v_mfma_f32_16x16x32_bf16 v[4:7], a[40:43], a[0:3], v[4:7]\n" \
"s_sub_u32 s74, s74, 32768\n" \
"s_cselect_b32 s74, 131072, s74\n" \
"ds_read_b64_tr_b16 a[48:49], v214 offset:16896\n" \
"v_mfma_f32_16x16x32_bf16 v[4:7], a[44:47], a[4:7], v[4:7]\n" \
"ds_read_b64_tr_b16 a[50:51], v214 offset:18944\n" \
"global_load_lds_dwordx4 v219, s[38:39] offset:-1024\n" \
"v_exp_f32 v149, v149\n" \
"v_fma_f32 v158, v158, v144, -v181\n" \
"v_mfma_f32_16x16x32_bf16 v[20:23], a[40:43], a[8:11], v[20:23]\n" \
"s_add_u32 s38, s38, 0x20000\n" \
"s_addc_u32 s39, s39, 0\n" \
"ds_read_b64_tr_b16 a[52:53], v214 offset:25088\n" \
"v_mfma_f32_16x16x32_bf16 v[20:23], a[44:47], a[12:15], v[20:23]\n" \
"s_sub_u32 s75, s75, 32768\n" \
"s_cselect_b32 s75, 131072, s75\n" \
"ds_read_b64_tr_b16 a[54:55], v214 offset:27136\n" \
"v_exp_f32 v174, v174\n" \
"v_mfma_f32_16x16x32_bf16 v[16:19], a[32:35], a[8:11], v[16:19]\n" \
"s_add_u32 m0, s75, s95\n" \
"ds_read_b64_tr_b16 a[56:57], v215 offset:16896\n" \
"v_fma_f32 v150, v150, v144, -v180\n" \
"v_mfma_f32_16x16x32_bf16 v[16:19], a[36:39], a[12:15], v[16:19]\n" \
"ds_read_b64_tr_b16 a[58:59], v215 offset:18944\n" \
"global_load_lds_dwordx4 v222, s[36:37] offset:0 sc1\n" \
"v_exp_f32 v166, v166\n" \
"v_mfma_f32_16x16x32_bf16 v[32:35], a[32:35], a[16:19], v[32:35]\n" \
"ds_read_b64_tr_b16 a[60:61], v215 offset:25088\n" \
"v_fma_f32 v175, v175, v144, -v183\n" \
"v_mfma_f32_16x16x32_bf16 v[32:35], a[36:39], a[20:23], v[32:35]\n" \
"ds_read_b64_tr_b16 a[62:63], v215 offset:27136\n" \
"v_exp_f32 v158, v158\n" \
"v_fma_f32 v167, v167, v144, -v182\n" \
"v_mfma_f32_16x16x32_bf16 v[36:39], a[40:43], a[16:19], v[36:39]\n" \
"global_load_lds_dwordx4 v222, s[36:37] offset:1024 sc1\n" \
"v_add_u32 v213, s74, v211\n" \
"v_mfma_f32_16x16x32_bf16 v[36:39], a[44:47], a[20:23], v[36:39]\n" \
"s_waitcnt lgkmcnt(3)\n" \
"v_add_u32 v214, s74, v212\n" \
"v_xor_b32 v215, 32, v214\n" \
"v_mfma_f32_16x16x32_bf16 v[52:55], a[40:43], a[24:27], v[52:55]\n" \
"v_exp_f32 v150, v150\n" \
"v_mfma_f32_16x16x32_bf16 v[52:55], a[44:47], a[28:31], v[52:55]\n" \
"s_waitcnt vmcnt(18) lgkmcnt(0)\n" \
"global_load_lds_dwordx4 v222, s[36:37] offset:2048 sc1\n" \
"v_fma_f32 v159, v159, v144, -v181\n" \
"v_mfma_f32_16x16x32_bf16 v[48:51], a[32:35], a[24:27], v[48:51]\n" \
"v_exp_f32 v175, v175\n" \
"v_mfma_f32_16x16x32_bf16 v[48:51], a[36:39], a[28:31], v[48:51]\n" \
"s_barrier\n" \
"v_fma_f32 v151, v151, v144, -v180\n" \
"v_exp_f32 v167, v167\n" \
"v_mfma_f32_16x16x32_bf16 v[8:11], a[48:51], a[0:3], v[8:11]\n" \
"ds_read_b64_tr_b16 a[32:33], v214 offset:16384\n" \
"v_fma_f32 v176, v176, v144, -v183\n" \
"v_mfma_f32_16x16x32_bf16 v[8:11], a[52:55], a[4:7], v[8:11]\n" \
"ds_read_b64_tr_b16 a[34:35], v214 offset:18432\n" \
"global_load_lds_dwordx4 v222, s[36:37] offset:3072 sc1\n" \
"v_exp_f32 v159, v159\n" \
"v_mfma_f32_16x16x32_bf16 v[12:15], a[56:59], a[0:3], v[12:15]\n" \
"s_add_u32 s36, s36, 0x1000\n" \
"ds_read_b64_tr_b16 a[36:37], v214 offset:24576\n" \
"v_fma_f32 v168, v168, v144, -v182\n" \
"v_mfma_f32_16x16x32_bf16 v[12:15], a[60:63], a[4:7], v[12:15]\n" \
"s_add_u32 m0, m0, 0x5000\n" \
"ds_read_b128 a[0:3], v213 offset:0\n" \
"v_exp_f32 v151, v151\n" \
"v_fma_f32 v160, v160, v144, -v181\n" \
"v_mfma_f32_16x16x32_bf16 v[28:31], a[56:59], a[8:11], v[28:31]\n" \
"ds_read_b128 a[4:7], v213 offset:2048\n" \
"global_load_lds_dwordx4 v216, s[38:39] offset:-4096\n" \
"v_mfma_f32_16x16x32_bf16 v[28:31], a[60:63], a[12:15], v[28:31]\n" \
"ds_read_b64_tr_b16 a[38:39], v214 offset:26624\n" \
"v_exp_f32 v176, v176\n" \
"v_mfma_f32_16x16x32_bf16 v[24:27], a[48:51], a[8:11], v[24:27]\n" \
"ds_read_b64_tr_b16 a[40:41], v215 offset:16384\n" \
"v_fma_f32 v152, v152, v144, -v180\n" \
"v_mfma_f32_16x16x32_bf16 v[24:27], a[52:55], a[12:15], v[24:27]\n" \
"ds_read_b128 a[8:11], v213 offset:256\n" \
"global_load_lds_dwordx4 v217, s[38:39] offset:-3072\n" \
"v_exp_f32 v168, v168\n" \
"v_mfma_f32_16x16x32_bf16 v[40:43], a[48:51], a[16:19], v[40:43]\n" \
"ds_read_b128 a[12:15], v213 offset:2304\n" \
"v_fma_f32 v177, v177, v144, -v183\n" \
"v_mfma_f32_16x16x32_bf16 v[40:43], a[52:55], a[20:23], v[40:43]\n" \
"ds_read_b64_tr_b16 a[42:43], v215 offset:18432\n" \
"v_exp_f32 v160, v160\n" \
"v_fma_f32 v169, v169, v144, -v182\n" \
"v_mfma_f32_16x16x32_bf16 v[44:47], a[56:59], a[16:19], v[44:47]\n" \
"ds_read_b64_tr_b16 a[44:45], v215 offset:24576\n" \
"global_load_lds_dwordx4 v218, s[38:39] offset:-2048\n" \
"v_mfma_f32_16x16x32_bf16 v[44:47], a[60:63], a[20:23], v[44:47]\n" \
"ds_read_b128 a[16:19], v213 offset:4096\n" \
"v_exp_f32 v152, v152\n" \
"v_fma_f32 v161, v161, v144, -v181\n" \
"v_mfma_f32_16x16x32_bf16 v[60:63], a[56:59], a[24:27], v[60:63]\n" \
"ds_read_b128 a[20:23], v213 offset:6144\n" \
"global_load_lds_dwordx4 v219, s[38:39] offset:-1024\n" \
"v_mfma_f32_16x16x32_bf16 v[60:63], a[60:63], a[28:31], v[60:63]\n" \
"s_add_u32 s38, s38, 0x20000\n" \
"s_addc_u32 s39, s39, 0\n" \
"ds_read_b64_tr_b16 a[46:47], v215 offset:26624\n" \
"v_exp_f32 v177, v177\n" \
"v_mfma_f32_16x16x32_bf16 v[56:59], a[48:51], a[24:27], v[56:59]\n" \
"s_sub_u32 s75, s75, 32768\n" \
"s_cselect_b32 s75, 131072, s75\n" \
"v_fma_f32 v153, v153, v144, -v180\n" \
"v_mfma_f32_16x16x32_bf16 v[56:59], a[52:55], a[28:31], v[56:59]\n" \
"s_waitcnt lgkmcnt(3)\n" \
"v_exp_f32 v169, v169\n" \
"v_mfma_f32_16x16x32_bf16 v[0:3], a[32:35], a[0:3], v[0:3]\n" \
"s_add_u32 m0, s75, s95\n" \
"ds_read_b128 a[24:27], v213 offset:4352\n" \
"v_fma_f32 v178, v178, v144, -v183\n" \
"v_mfma_f32_16x16x32_bf16 v[0:3], a[36:39], a[4:7], v[0:3]\n" \
"s_waitcnt lgkmcnt(1)\n" \
"ds_read_b128 a[28:31], v213 offset:6400\n" \
"global_load_lds_dwordx4 v222, s[36:37] offset:0 sc1\n" \
"v_mfma_f32_16x16x32_bf16 v[4:7], a[40:43], a[0:3], v[4:7]\n" \
"s_sub_u32 s74, s74, 32768\n" \
"s_cselect_b32 s74, 131072, s74\n" \
"Lpg11445_%=:\n" \
"s_cbranch_execz Lpg12577_%=\n" \
"ds_read_b64_tr_b16 a[48:49], v214 offset:16896\n" \
"v_mfma_f32_16x16x32_bf16 v[4:7], a[44:47], a[4:7], v[4:7]\n" \
"ds_read_b64_tr_b16 a[50:51], v214 offset:18944\n" \
"v_exp_f32 v161, v161\n" \
"v_mfma_f32_16x16x32_bf16 v[20:23], a[40:43], a[8:11], v[20:23]\n" \
"ds_read_b64_tr_b16 a[52:53], v214 offset:25088\n" \
"v_fma_f32 v170, v170, v144, -v182\n" \
"v_mfma_f32_16x16x32_bf16 v[20:23], a[44:47], a[12:15], v[20:23]\n" \
"ds_read_b64_tr_b16 a[54:55], v214 offset:27136\n" \
"global_load_lds_dwordx4 v222, s[36:37] offset:1024 sc1\n" \
"v_exp_f32 v153, v153\n" \
"v_mfma_f32_16x16x32_bf16 v[16:19], a[32:35], a[8:11], v[16:19]\n" \
"ds_read_b64_tr_b16 a[56:57], v215 offset:16896\n" \
"v_fma_f32 v162, v162, v144, -v181\n" \
"v_mfma_f32_16x16x32_bf16 v[16:19], a[36:39], a[12:15], v[16:19]\n" \
"ds_read_b64_tr_b16 a[58:59], v215 offset:18944\n" \
"v_exp_f32 v178, v178\n" \
"v_mfma_f32_16x16x32_bf16 v[32:35], a[32:35], a[16:19], v[32:35]\n" \
"ds_read_b64_tr_b16 a[60:61], v215 offset:25088\n" \
"v_fma_f32 v154, v154, v144, -v180\n" \
"v_mfma_f32_16x16x32_bf16 v[32:35], a[36:39], a[20:23], v[32:35]\n" \
"ds_read_b64_tr_b16 a[62:63], v215 offset:27136\n" \
"global_load_lds_dwordx4 v222, s[36:37] offset:2048 sc1\n" \
"v_exp_f32 v170, v170\n" \
"v_mfma_f32_16x16x32_bf16 v[36:39], a[40:43], a[16:19], v[36:39]\n" \
"v_add_u32 v213, s74, v211\n" \
"v_add_u32 v214, s74, v212\n" \
"v_mfma_f32_16x16x32_bf16 v[36:39], a[44:47], a[20:23], v[36:39]\n" \
"s_waitcnt lgkmcnt(3)\n" \
"v_xor_b32 v215, 32, v214\n" \
"v_fma_f32 v179, v179, v144, -v183\n" \
"v_mfma_f32_16x16x32_bf16 v[52:55], a[40:43], a[24:27], v[52:55]\n" \
"v_exp_f32 v162, v162\n" \
"v_mfma_f32_16x16x32_bf16 v[52:55], a[44:47], a[28:31], v[52:55]\n" \
"s_waitcnt vmcnt(19) lgkmcnt(0)\n" \
"global_load_lds_dwordx4 v222, s[36:37] offset:3072 sc1\n" \
"v_fma_f32 v171, v171, v144, -v182\n" \
"v_mfma_f32_16x16x32_bf16 v[48:51], a[32:35], a[24:27], v[48:51]\n" \
"s_add_u32 s36, s36, 0x1000\n" \
"v_exp_f32 v154, v154\n" \
"v_mfma_f32_16x16x32_bf16 v[48:51], a[36:39], a[28:31], v[48:51]\n" \
"s_barrier\n" \
"v_fma_f32 v163, v163, v144, -v181\n" \
"v_exp_f32 v179, v179\n" \
"v_mfma_f32_16x16x32_bf16 v[8:11], a[48:51], a[0:3], v[8:11]\n" \
"s_add_u32 m0, m0, 0x5000\n" \
"ds_read_b64_tr_b16 a[32:33], v214 offset:16384\n" \
"v_fma_f32 v155, v155, v144, -v180\n" \
"v_mfma_f32_16x16x32_bf16 v[8:11], a[52:55], a[4:7], v[8:11]\n" \
"ds_read_b64_tr_b16 a[34:35], v214 offset:18432\n" \
"global_load_lds_dwordx4 v216, s[38:39] offset:-4096\n" \
"v_exp_f32 v171, v171\n" \
"v_exp_f32 v163, v163\n" \
"v_exp_f32 v155, v155\n" \
"v_mov_b32 v188, 0x3f803f80\n" \
"v_mfma_f32_16x16x32_bf16 v[12:15], a[56:59], a[0:3], v[12:15]\n" \
"ds_read_b64_tr_b16 a[36:37], v214 offset:24576\n" \
"v_mov_b32 v189, 0x3f803f80\n" \
"v_mfma_f32_16x16x32_bf16 v[12:15], a[60:63], a[4:7], v[12:15]\n" \
"ds_read_b128 a[0:3], v213 offset:0\n" \
"v_mov_b32 v190, 0x3f803f80\n" \
"v_mov_b32 v191, 0x3f803f80\n" \
"v_mfma_f32_16x16x32_bf16 v[28:31], a[56:59], a[8:11], v[28:31]\n" \
"ds_read_b128 a[4:7], v213 offset:2048\n" \
"v_cvt_pk_bf16_f32 v172, v172, v173\n" \
"v_mfma_f32_16x16x32_bf16 v[28:31], a[60:63], a[12:15], v[28:31]\n" \
"ds_read_b64_tr_b16 a[38:39], v214 offset:26624\n" \
"global_load_lds_dwordx4 v217, s[38:39] offset:-3072\n" \
"v_cvt_pk_bf16_f32 v173, v174, v175\n" \
"v_mfma_f32_16x16x32_bf16 v[24:27], a[48:51], a[8:11], v[24:27]\n" \
"ds_read_b64_tr_b16 a[40:41], v215 offset:16384\n" \
"v_cvt_pk_bf16_f32 v174, v176, v177\n" \
"v_mfma_f32_16x16x32_bf16 v[24:27], a[52:55], a[12:15], v[24:27]\n" \
"ds_read_b128 a[8:11], v213 offset:256\n" \
"v_cvt_pk_bf16_f32 v175, v178, v179\n" \
"v_mfma_f32_16x16x32_bf16 v[40:43], a[48:51], a[16:19], v[40:43]\n" \
"ds_read_b128 a[12:15], v213 offset:2304\n" \
"v_cvt_pk_bf16_f32 v164, v164, v165\n" \
"v_mfma_f32_16x16x32_bf16 v[40:43], a[52:55], a[20:23], v[40:43]\n" \
"ds_read_b64_tr_b16 a[42:43], v215 offset:18432\n" \
"global_load_lds_dwordx4 v218, s[38:39] offset:-2048\n" \
"v_mfma_f32_16x16x32_bf16 v[44:47], a[56:59], a[16:19], v[44:47]\n" \
"ds_read_b64_tr_b16 a[44:45], v215 offset:24576\n" \
"v_cvt_pk_bf16_f32 v165, v166, v167\n" \
"v_mfma_f32_16x16x32_bf16 v[44:47], a[60:63], a[20:23], v[44:47]\n" \
"ds_read_b128 a[16:19], v213 offset:4096\n" \
"v_cvt_pk_bf16_f32 v166, v168, v169\n" \
"v_mfma_f32_16x16x32_bf16 v[60:63], a[56:59], a[24:27], v[60:63]\n" \
"ds_read_b128 a[20:23], v213 offset:6144\n" \
"global_load_lds_dwordx4 v219, s[38:39] offset:-1024\n" \
"v_mfma_f32_16x16x32_bf16 v[60:63], a[60:63], a[28:31], v[60:63]\n" \
"s_add_u32 s38, s38, 0x20000\n" \
"s_addc_u32 s39, s39, 0\n" \
"ds_read_b64_tr_b16 a[46:47], v215 offset:26624\n" \
"v_mfma_f32_16x16x32_bf16 v[56:59], a[48:51], a[24:27], v[56:59]\n" \
"s_sub_u32 s75, s75, 32768\n" \
"s_cselect_b32 s75, 131072, s75\n" \
"v_cvt_pk_bf16_f32 v167, v170, v171\n" \
"v_mfma_f32_16x16x32_bf16 v[56:59], a[52:55], a[28:31], v[56:59]\n" \
"s_waitcnt lgkmcnt(3)\n" \
"v_cvt_pk_bf16_f32 v156, v156, v157\n" \
"v_cvt_pk_bf16_f32 v157, v158, v159\n" \
"v_mfma_f32_16x16x32_bf16 v[0:3], a[32:35], a[0:3], v[0:3]\n" \
"s_add_u32 m0, s75, s95\n" \
"ds_read_b128 a[24:27], v213 offset:4352\n" \
"v_cvt_pk_bf16_f32 v158, v160, v161\n" \
"v_mfma_f32_16x16x32_bf16 v[0:3], a[36:39], a[4:7], v[0:3]\n" \
"s_waitcnt lgkmcnt(1)\n" \
"ds_read_b128 a[28:31], v213 offset:6400\n" \
"global_load_lds_dwordx4 v222, s[36:37] offset:0 sc1\n" \
"v_mfma_f32_16x16x32_bf16 v[4:7], a[40:43], a[0:3], v[4:7]\n" \
"s_sub_u32 s74, s74, 32768\n" \
"s_cselect_b32 s74, 131072, s74\n" \
"ds_read_b64_tr_b16 a[48:49], v214 offset:16896\n" \
"v_mfma_f32_16x16x32_bf16 v[4:7], a[44:47], a[4:7], v[4:7]\n" \
"ds_read_b64_tr_b16 a[50:51], v214 offset:18944\n" \
"v_cvt_pk_bf16_f32 v159, v162, v163\n" \
"v_mfma_f32_16x16x32_bf16 v[20:23], a[40:43], a[8:11], v[20:23]\n" \
"ds_read_b64_tr_b16 a[52:53], v214 offset:25088\n" \
"v_cvt_pk_bf16_f32 v148, v148, v149\n" \
"v_mfma_f32_16x16x32_bf16 v[20:23], a[44:47], a[12:15], v[20:23]\n" \
"ds_read_b64_tr_b16 a[54:55], v214 offset:27136\n" \
"global_load_lds_dwordx4 v222, s[36:37] offset:1024 sc1\n" \
"v_mfma_f32_16x16x32_bf16 v[16:19], a[32:35], a[8:11], v[16:19]\n" \
"ds_read_b64_tr_b16 a[56:57], v215 offset:16896\n" \
"v_cvt_pk_bf16_f32 v149, v150, v151\n" \
"v_mfma_f32_16x16x32_bf16 v[16:19], a[36:39], a[12:15], v[16:19]\n" \
"ds_read_b64_tr_b16 a[58:59], v215 offset:18944\n" \
"v_cvt_pk_bf16_f32 v150, v152, v153\n" \
"v_mfma_f32_16x16x32_bf16 v[32:35], a[32:35], a[16:19], v[32:35]\n" \
"ds_read_b64_tr_b16 a[60:61], v215 offset:25088\n" \
"v_cvt_pk_bf16_f32 v151, v154, v155\n" \
"v_mfma_f32_16x16x32_bf16 v[32:35], a[36:39], a[20:23], v[32:35]\n" \
"ds_read_b64_tr_b16 a[62:63], v215 offset:27136\n" \
"global_load_lds_dwordx4 v222, s[36:37] offset:2048 sc1\n" \
"v_mfma_f32_16x16x32_bf16 v[176:179], v[188:191], v[172:175], 0\n" \
"v_mfma_f32_16x16x32_bf16 v[136:139], v[74:77], v[172:175], 0\n" \
"v_mfma_f32_16x16x32_bf16 v[140:143], v[198:201], v[172:175], 0\n" \
"v_mfma_f32_16x16x32_bf16 v[168:171], v[188:191], v[164:167], 0\n" \
"v_mfma_f32_16x16x32_bf16 v[128:131], v[72:75], v[164:167], 0\n" \
"v_mfma_f32_16x16x32_bf16 v[132:135], v[196:199], v[164:167], 0\n" \
"v_mfma_f32_16x16x32_bf16 v[36:39], a[40:43], a[16:19], v[36:39]\n" \
"v_mfma_f32_16x16x32_bf16 v[160:163], v[188:191], v[156:159], 0\n" \
"v_mfma_f32_16x16x32_bf16 v[120:123], v[70:73], v[156:159], 0\n" \
"v_mfma_f32_16x16x32_bf16 v[124:127], v[194:197], v[156:159], 0\n" \
"v_mfma_f32_16x16x32_bf16 v[152:155], v[188:191], v[148:151], 0\n" \
"v_mfma_f32_16x16x32_bf16 v[112:115], v[68:71], v[148:151], 0\n" \
"v_mfma_f32_16x16x32_bf16 v[116:119], v[192:195], v[148:151], 0\n" \
"v_add_u32 v213, s74, v211\n" \
"v_add_u32 v214, s74, v212\n" \
"v_mfma_f32_16x16x32_bf16 v[36:39], a[44:47], a[20:23], v[36:39]\n" \
"s_waitcnt lgkmcnt(3)\n" \
"v_xor_b32 v215, 32, v214\n" \
"v_rcp_f32 v187, v176\n" \
"v_rcp_f32 v186, v168\n" \
"v_rcp_f32 v185, v160\n" \
"v_mfma_f32_16x16x32_bf16 v[52:55], a[40:43], a[24:27], v[52:55]\n" \
"v_rcp_f32 v184, v152\n" \
"v_mfma_f32_16x16x32_bf16 v[52:55], a[44:47], a[28:31], v[52:55]\n" \
"s_waitcnt vmcnt(19) lgkmcnt(0)\n" \
"global_load_lds_dwordx4 v222, s[36:37] offset:3072 sc1\n" \
"v_mul_f32 v136, v136, v187\n" \
"v_mul_f32 v137, v137, v187\n" \
"v_mfma_f32_16x16x32_bf16 v[48:51], a[32:35], a[24:27], v[48:51]\n" \
"s_add_u32 s36, s36, 0x1000\n" \
"v_mul_f32 v138, v138, v187\n" \
"v_mul_f32 v139, v139, v187\n" \
"v_mfma_f32_16x16x32_bf16 v[48:51], a[36:39], a[28:31], v[48:51]\n" \
"s_barrier\n" \
"v_mul_f32 v140, v140, v187\n" \
"v_mfma_f32_16x16x32_bf16 v[8:11], a[48:51], a[0:3], v[8:11]\n" \
"s_add_u32 m0, m0, 0x5000\n" \
"ds_read_b64_tr_b16 a[32:33], v214 offset:16384\n" \
"v_mul_f32 v141, v141, v187\n" \
"v_mfma_f32_16x16x32_bf16 v[8:11], a[52:55], a[4:7], v[8:11]\n" \
"ds_read_b64_tr_b16 a[34:35], v214 offset:18432\n" \
"global_load_lds_dwordx4 v216, s[38:39] offset:-4096\n" \
"v_mul_f32 v142, v142, v187\n" \
"v_mul_f32 v143, v143, v187\n" \
"v_mfma_f32_16x16x32_bf16 v[12:15], a[56:59], a[0:3], v[12:15]\n" \
"ds_read_b64_tr_b16 a[36:37], v214 offset:24576\n" \
"v_mul_f32 v128, v128, v186\n" \
"v_mfma_f32_16x16x32_bf16 v[12:15], a[60:63], a[4:7], v[12:15]\n" \
"ds_read_b128 a[0:3], v213 offset:0\n" \
"v_mul_f32 v129, v129, v186\n" \
"v_mul_f32 v130, v130, v186\n" \
"v_mfma_f32_16x16x32_bf16 v[28:31], a[56:59], a[8:11], v[28:31]\n" \
"ds_read_b128 a[4:7], v213 offset:2048\n" \
"v_mul_f32 v131, v131, v186\n" \
"v_mfma_f32_16x16x32_bf16 v[28:31], a[60:63], a[12:15], v[28:31]\n" \
"ds_read_b64_tr_b16 a[38:39], v214 offset:26624\n" \
"global_load_lds_dwordx4 v217, s[38:39] offset:-3072\n" \
"v_mul_f32 v132, v132, v186\n" \
"v_mul_f32 v133, v133, v186\n" \
"v_mfma_f32_16x16x32_bf16 v[24:27], a[48:51], a[8:11], v[24:27]\n" \
"ds_read_b64_tr_b16 a[40:41], v215 offset:16384\n" \
"v_mul_f32 v134, v134, v186\n" \
"v_mfma_f32_16x16x32_bf16 v[24:27], a[52:55], a[12:15], v[24:27]\n" \
"ds_read_b128 a[8:11], v213 offset:256\n" \
"v_mul_f32 v135, v135, v186\n" \
"v_mul_f32 v120, v120, v185\n" \
"v_mfma_f32_16x16x32_bf16 v[40:43], a[48:51], a[16:19], v[40:43]\n" \
"ds_read_b128 a[12:15], v213 offset:2304\n" \
"v_mul_f32 v121, v121, v185\n" \
"v_mfma_f32_16x16x32_bf16 v[40:43], a[52:55], a[20:23], v[40:43]\n" \
"ds_read_b64_tr_b16 a[42:43], v215 offset:18432\n" \
"global_load_lds_dwordx4 v218, s[38:39] offset:-2048\n" \
"v_mul_f32 v122, v122, v185\n" \
"v_mul_f32 v123, v123, v185\n" \
"v_mfma_f32_16x16x32_bf16 v[44:47], a[56:59], a[16:19], v[44:47]\n" \
"ds_read_b64_tr_b16 a[44:45], v215 offset:24576\n" \
"v_mul_f32 v124, v124, v185\n" \
"v_mfma_f32_16x16x32_bf16 v[44:47], a[60:63], a[20:23], v[44:47]\n" \
"ds_read_b128 a[16:19], v213 offset:4096\n" \
"v_mul_f32 v125, v125, v185\n" \
"v_mul_f32 v126, v126, v185\n" \
"v_mfma_f32_16x16x32_bf16 v[60:63], a[56:59], a[24:27], v[60:63]\n" \
"ds_read_b128 a[20:23], v213 offset:6144\n" \
"global_load_lds_dwordx4 v219, s[38:39] offset:-1024\n" \
"v_mfma_f32_16x16x32_bf16 v[60:63], a[60:63], a[28:31], v[60:63]\n" \
"s_add_u32 s38, s38, 0x20000\n" \
"s_addc_u32 s39, s39, 0\n" \
"ds_read_b64_tr_b16 a[46:47], v215 offset:26624\n" \
"v_mul_f32 v127, v127, v185\n" \
"v_mul_f32 v112, v112, v184\n" \
"v_mul_f32 v113, v113, v184\n" \
"v_mfma_f32_16x16x32_bf16 v[56:59], a[48:51], a[24:27], v[56:59]\n" \
"s_sub_u32 s75, s75, 32768\n" \
"s_cselect_b32 s75, 131072, s75\n" \
"v_mul_f32 v114, v114, v184\n" \
"v_mfma_f32_16x16x32_bf16 v[56:59], a[52:55], a[28:31], v[56:59]\n" \
"s_waitcnt lgkmcnt(3)\n" \
"v_mul_f32 v115, v115, v184\n" \
"v_mul_f32 v116, v116, v184\n" \
"v_mfma_f32_16x16x32_bf16 v[0:3], a[32:35], a[0:3], v[0:3]\n" \
"s_add_u32 m0, s75, s95\n" \
"ds_read_b128 a[24:27], v213 offset:4352\n" \
"v_mul_f32 v117, v117, v184\n" \
"v_mfma_f32_16x16x32_bf16 v[0:3], a[36:39], a[4:7], v[0:3]\n" \
"s_waitcnt lgkmcnt(1)\n" \
"ds_read_b128 a[28:31], v213 offset:6400\n" \
"global_load_lds_dwordx4 v222, s[36:37] offset:0 sc1\n" \
"v_mul_f32 v118, v118, v184\n" \
"v_mul_f32 v119, v119, v184\n" \
"v_mfma_f32_16x16x32_bf16 v[4:7], a[40:43], a[0:3], v[4:7]\n" \
"s_sub_u32 s74, s74, 32768\n" \
"s_cselect_b32 s74, 131072, s74\n" \
"ds_read_b64_tr_b16 a[48:49], v214 offset:16896\n" \
"v_mfma_f32_16x16x32_bf16 v[4:7], a[44:47], a[4:7], v[4:7]\n" \
"ds_read_b64_tr_b16 a[50:51], v214 offset:18944\n" \
"v_cvt_pk_bf16_f32 v136, v136, v137\n" \
"v_cvt_pk_bf16_f32 v137, v138, v139\n" \
"v_cvt_pk_bf16_f32 v138, v140, v141\n" \
"v_mfma_f32_16x16x32_bf16 v[20:23], a[40:43], a[8:11], v[20:23]\n" \
"ds_read_b64_tr_b16 a[52:53], v214 offset:25088\n" \
"v_cvt_pk_bf16_f32 v139, v142, v143\n" \
"v_mfma_f32_16x16x32_bf16 v[20:23], a[44:47], a[12:15], v[20:23]\n" \
"ds_read_b64_tr_b16 a[54:55], v214 offset:27136\n" \
"global_load_lds_dwordx4 v222, s[36:37] offset:1024 sc1\n" \
"v_cndmask_b32 v140, v136, v138, s[92:93]\n" \
"v_mfma_f32_16x16x32_bf16 v[16:19], a[32:35], a[8:11], v[16:19]\n" \
"ds_read_b64_tr_b16 a[56:57], v215 offset:16896\n" \
"v_cndmask_b32 v141, v137, v139, s[92:93]\n" \
"v_mfma_f32_16x16x32_bf16 v[16:19], a[36:39], a[12:15], v[16:19]\n" \
"ds_read_b64_tr_b16 a[58:59], v215 offset:18944\n" \
"v_cndmask_b32 v142, v138, v136, s[92:93]\n" \
"v_mfma_f32_16x16x32_bf16 v[32:35], a[32:35], a[16:19], v[32:35]\n" \
"ds_read_b64_tr_b16 a[60:61], v215 offset:25088\n" \
"v_cndmask_b32 v143, v139, v137, s[92:93]\n" \
"v_mfma_f32_16x16x32_bf16 v[32:35], a[36:39], a[20:23], v[32:35]\n" \
"s_add_u32 s71, s87, 208896\n" \
"ds_read_b64_tr_b16 a[62:63], v215 offset:27136\n" \
"global_load_lds_dwordx4 v222, s[36:37] offset:2048 sc1\n" \
"v_mfma_f32_16x16x32_bf16 v[36:39], a[40:43], a[16:19], v[36:39]\n" \
"v_add_u32 v213, s74, v211\n" \
"v_add_u32 v214, s74, v212\n" \
"v_mfma_f32_16x16x32_bf16 v[36:39], a[44:47], a[20:23], v[36:39]\n" \
"s_waitcnt lgkmcnt(3)\n" \
"buffer_store_dwordx4 v[140:143], v146, s[56:59], s71 offen sc1\n" \
"v_xor_b32 v215, 32, v214\n" \
"v_cvt_pk_bf16_f32 v128, v128, v129\n" \
"v_cvt_pk_bf16_f32 v129, v130, v131\n" \
"v_cvt_pk_bf16_f32 v130, v132, v133\n" \
"v_cvt_pk_bf16_f32 v131, v134, v135\n" \
"v_mfma_f32_16x16x32_bf16 v[52:55], a[40:43], a[24:27], v[52:55]\n" \
"v_cndmask_b32 v132, v128, v130, s[92:93]\n" \
"v_cndmask_b32 v133, v129, v131, s[92:93]\n" \
"v_mfma_f32_16x16x32_bf16 v[52:55], a[44:47], a[28:31], v[52:55]\n" \
"s_waitcnt vmcnt(20) lgkmcnt(0)\n" \
"global_load_lds_dwordx4 v222, s[36:37] offset:3072 sc1\n" \
"v_cndmask_b32 v134, v130, v128, s[92:93]\n" \
"v_mfma_f32_16x16x32_bf16 v[48:51], a[32:35], a[24:27], v[48:51]\n" \
"s_add_u32 s36, s36, 0x1000\n" \
"v_cndmask_b32 v135, v131, v129, s[92:93]\n" \
"s_add_u32 s71, s87, 139264\n" \
"v_mfma_f32_16x16x32_bf16 v[48:51], a[36:39], a[28:31], v[48:51]\n" \
"s_barrier\n" \
"buffer_store_dwordx4 v[132:135], v146, s[56:59], s71 offen sc1\n" \
"v_cvt_pk_bf16_f32 v120, v120, v121\n" \
"v_cvt_pk_bf16_f32 v121, v122, v123\n" \
"v_cvt_pk_bf16_f32 v122, v124, v125\n" \
"v_cvt_pk_bf16_f32 v123, v126, v127\n" \
"v_mfma_f32_16x16x32_bf16 v[8:11], a[48:51], a[0:3], v[8:11]\n" \
"s_add_u32 m0, m0, 0x5000\n" \
"ds_read_b64_tr_b16 a[32:33], v214 offset:16384\n" \
"v_cndmask_b32 v124, v120, v122, s[92:93]\n" \
"v_mfma_f32_16x16x32_bf16 v[8:11], a[52:55], a[4:7], v[8:11]\n" \
"ds_read_b64_tr_b16 a[34:35], v214 offset:18432\n" \
"global_load_lds_dwordx4 v216, s[38:39] offset:-4096\n" \
"v_mfma_f32_16x16x32_bf16 v[12:15], a[56:59], a[0:3], v[12:15]\n" \
"ds_read_b64_tr_b16 a[36:37], v214 offset:24576\n" \
"v_cndmask_b32 v125, v121, v123, s[92:93]\n" \
"v_mfma_f32_16x16x32_bf16 v[12:15], a[60:63], a[4:7], v[12:15]\n" \
"ds_read_b128 a[0:3], v213 offset:0\n" \
"v_cndmask_b32 v126, v122, v120, s[92:93]\n" \
"v_mfma_f32_16x16x32_bf16 v[28:31], a[56:59], a[8:11], v[28:31]\n" \
"ds_read_b128 a[4:7], v213 offset:2048\n" \
"v_cndmask_b32 v127, v123, v121, s[92:93]\n" \
"v_mfma_f32_16x16x32_bf16 v[28:31], a[60:63], a[12:15], v[28:31]\n" \
"s_add_u32 s71, s87, 69632\n" \
"ds_read_b64_tr_b16 a[38:39], v214 offset:26624\n" \
"global_load_lds_dwordx4 v217, s[38:39] offset:-3072\n" \
"v_mfma_f32_16x16x32_bf16 v[24:27], a[48:51], a[8:11], v[24:27]\n" \
"ds_read_b64_tr_b16 a[40:41], v215 offset:16384\n" \
"buffer_store_dwordx4 v[124:127], v146, s[56:59], s71 offen sc1\n" \
"v_cvt_pk_bf16_f32 v112, v112, v113\n" \
"v_cvt_pk_bf16_f32 v113, v114, v115\n" \
"v_cvt_pk_bf16_f32 v114, v116, v117\n" \
"v_cvt_pk_bf16_f32 v115, v118, v119\n" \
"v_mfma_f32_16x16x32_bf16 v[24:27], a[52:55], a[12:15], v[24:27]\n" \
"ds_read_b128 a[8:11], v213 offset:256\n" \
"v_cndmask_b32 v116, v112, v114, s[92:93]\n" \
"v_mfma_f32_16x16x32_bf16 v[40:43], a[48:51], a[16:19], v[40:43]\n" \
"ds_read_b128 a[12:15], v213 offset:2304\n" \
"v_cndmask_b32 v117, v113, v115, s[92:93]\n" \
"v_mfma_f32_16x16x32_bf16 v[40:43], a[52:55], a[20:23], v[40:43]\n" \
"ds_read_b64_tr_b16 a[42:43], v215 offset:18432\n" \
"global_load_lds_dwordx4 v218, s[38:39] offset:-2048\n" \
"v_mfma_f32_16x16x32_bf16 v[44:47], a[56:59], a[16:19], v[44:47]\n" \
"ds_read_b64_tr_b16 a[44:45], v215 offset:24576\n" \
"v_cndmask_b32 v118, v114, v112, s[92:93]\n" \
"v_mfma_f32_16x16x32_bf16 v[44:47], a[60:63], a[20:23], v[44:47]\n" \
"ds_read_b128 a[16:19], v213 offset:4096\n" \
"v_cndmask_b32 v119, v115, v113, s[92:93]\n" \
"v_mfma_f32_16x16x32_bf16 v[60:63], a[56:59], a[24:27], v[60:63]\n" \
"s_add_u32 s71, s87, 0\n" \
"ds_read_b128 a[20:23], v213 offset:6144\n" \
"global_load_lds_dwordx4 v219, s[38:39] offset:-1024\n" \
"v_mfma_f32_16x16x32_bf16 v[60:63], a[60:63], a[28:31], v[60:63]\n" \
"buffer_store_dwordx4 v[116:119], v146, s[56:59], s71 offen sc1\n" \
"s_add_u32 s38, s38, 0x20000\n" \
"s_addc_u32 s39, s39, 0\n" \
"ds_read_b64_tr_b16 a[46:47], v215 offset:26624\n" \
"v_mfma_f32_16x16x32_bf16 v[56:59], a[48:51], a[24:27], v[56:59]\n" \
"s_sub_u32 s75, s75, 32768\n" \
"s_cselect_b32 s75, 131072, s75\n" \
"v_mfma_f32_16x16x32_bf16 v[56:59], a[52:55], a[28:31], v[56:59]\n" \
"s_waitcnt lgkmcnt(3)\n" \
"v_mfma_f32_16x16x32_bf16 v[0:3], a[32:35], a[0:3], v[0:3]\n" \
"s_add_u32 m0, s75, s95\n" \
"ds_read_b128 a[24:27], v213 offset:4352\n" \
"v_mov_b32 v224, s60\n" \
"v_mfma_f32_16x16x32_bf16 v[0:3], a[36:39], a[4:7], v[0:3]\n" \
"s_waitcnt lgkmcnt(1)\n" \
"ds_read_b128 a[28:31], v213 offset:6400\n" \
"global_load_lds_dwordx4 v222, s[36:37] offset:0 sc1\n" \
"v_mfma_f32_16x16x32_bf16 v[4:7], a[40:43], a[0:3], v[4:7]\n" \
"s_sub_u32 s74, s74, 32768\n" \
"s_cselect_b32 s74, 131072, s74\n" \
"ds_read_b64_tr_b16 a[48:49], v214 offset:16896\n" \
"v_mfma_f32_16x16x32_bf16 v[4:7], a[44:47], a[4:7], v[4:7]\n" \
"ds_read_b64_tr_b16 a[50:51], v214 offset:18944\n" \
"v_lshlrev_b32 v225, 28, s61\n" \
"v_mfma_f32_16x16x32_bf16 v[20:23], a[40:43], a[8:11], v[20:23]\n" \
"ds_read_b64_tr_b16 a[52:53], v214 offset:25088\n" \
"v_mfma_f32_16x16x32_bf16 v[20:23], a[44:47], a[12:15], v[20:23]\n" \
"ds_read_b64_tr_b16 a[54:55], v214 offset:27136\n" \
"global_load_lds_dwordx4 v222, s[36:37] offset:1024 sc1\n" \
"v_mfma_f32_16x16x32_bf16 v[16:19], a[32:35], a[8:11], v[16:19]\n" \
"ds_read_b64_tr_b16 a[56:57], v215 offset:16896\n" \
"v_mfma_f32_16x16x32_bf16 v[16:19], a[36:39], a[12:15], v[16:19]\n" \
"ds_read_b64_tr_b16 a[58:59], v215 offset:18944\n" \
"v_mfma_f32_16x16x32_bf16 v[32:35], a[32:35], a[16:19], v[32:35]\n" \
"ds_read_b64_tr_b16 a[60:61], v215 offset:25088\n" \
"v_mfma_f32_16x16x32_bf16 v[32:35], a[36:39], a[20:23], v[32:35]\n" \
"ds_read_b64_tr_b16 a[62:63], v215 offset:27136\n" \
"global_load_lds_dwordx4 v222, s[36:37] offset:2048 sc1\n" \
"v_mfma_f32_16x16x32_bf16 v[36:39], a[40:43], a[16:19], v[36:39]\n" \
"v_add_u32 v213, s74, v211\n" \
"v_add_u32 v214, s74, v212\n" \
"v_mfma_f32_16x16x32_bf16 v[36:39], a[44:47], a[20:23], v[36:39]\n" \
"s_waitcnt lgkmcnt(3)\n" \
"v_xor_b32 v215, 32, v214\n" \
"v_mfma_f32_16x16x32_bf16 v[52:55], a[40:43], a[24:27], v[52:55]\n" \
"v_mfma_f32_16x16x32_bf16 v[52:55], a[44:47], a[28:31], v[52:55]\n" \
"s_waitcnt vmcnt(23) lgkmcnt(0)\n" \
"global_load_lds_dwordx4 v222, s[36:37] offset:3072 sc1\n" \
"v_mfma_f32_16x16x32_bf16 v[48:51], a[32:35], a[24:27], v[48:51]\n" \
"s_add_u32 s36, s36, 0x1000\n" \
"v_mfma_f32_16x16x32_bf16 v[48:51], a[36:39], a[28:31], v[48:51]\n" \
"s_barrier\n" \
"v_mfma_f32_16x16x32_bf16 v[8:11], a[48:51], a[0:3], v[8:11]\n" \
"s_add_u32 m0, m0, 0x5000\n" \
"ds_read_b64_tr_b16 a[32:33], v214 offset:16384\n" \
"v_mfma_f32_16x16x32_bf16 v[8:11], a[52:55], a[4:7], v[8:11]\n" \
"ds_read_b64_tr_b16 a[34:35], v214 offset:18432\n" \
"global_load_lds_dwordx4 v216, s[38:39] offset:-4096\n" \
"v_mfma_f32_16x16x32_bf16 v[12:15], a[56:59], a[0:3], v[12:15]\n" \
"ds_read_b64_tr_b16 a[36:37], v214 offset:24576\n" \
"v_mfma_f32_16x16x32_bf16 v[12:15], a[60:63], a[4:7], v[12:15]\n" \
"ds_read_b128 a[0:3], v213 offset:0\n" \
"v_mfma_f32_16x16x32_bf16 v[28:31], a[56:59], a[8:11], v[28:31]\n" \
"ds_read_b128 a[4:7], v213 offset:2048\n" \
"v_mfma_f32_16x16x32_bf16 v[28:31], a[60:63], a[12:15], v[28:31]\n" \
"ds_read_b64_tr_b16 a[38:39], v214 offset:26624\n" \
"global_load_lds_dwordx4 v217, s[38:39] offset:-3072\n" \
"v_mfma_f32_16x16x32_bf16 v[24:27], a[48:51], a[8:11], v[24:27]\n" \
"ds_read_b64_tr_b16 a[40:41], v215 offset:16384\n" \
"v_mfma_f32_16x16x32_bf16 v[24:27], a[52:55], a[12:15], v[24:27]\n" \
"ds_read_b128 a[8:11], v213 offset:256\n" \
"v_mfma_f32_16x16x32_bf16 v[40:43], a[48:51], a[16:19], v[40:43]\n" \
"ds_read_b128 a[12:15], v213 offset:2304\n" \
"v_mfma_f32_16x16x32_bf16 v[40:43], a[52:55], a[20:23], v[40:43]\n" \
"ds_read_b64_tr_b16 a[42:43], v215 offset:18432\n" \
"global_load_lds_dwordx4 v218, s[38:39] offset:-2048\n" \
"v_mfma_f32_16x16x32_bf16 v[44:47], a[56:59], a[16:19], v[44:47]\n" \
"ds_read_b64_tr_b16 a[44:45], v215 offset:24576\n" \
"v_mfma_f32_16x16x32_bf16 v[44:47], a[60:63], a[20:23], v[44:47]\n" \
"ds_read_b128 a[16:19], v213 offset:4096\n" \
"v_mfma_f32_16x16x32_bf16 v[60:63], a[56:59], a[24:27], v[60:63]\n" \
"ds_read_b128 a[20:23], v213 offset:6144\n" \
"global_load_lds_dwordx4 v219, s[38:39] offset:-1024\n" \
"v_mfma_f32_16x16x32_bf16 v[60:63], a[60:63], a[28:31], v[60:63]\n" \
"s_add_u32 s38, s38, 0x20000\n" \
"s_addc_u32 s39, s39, 0\n" \
"ds_read_b64_tr_b16 a[46:47], v215 offset:26624\n" \
"v_mfma_f32_16x16x32_bf16 v[56:59], a[48:51], a[24:27], v[56:59]\n" \
"s_sub_u32 s75, s75, 32768\n" \
"s_cselect_b32 s75, 131072, s75\n" \
"v_mfma_f32_16x16x32_bf16 v[56:59], a[52:55], a[28:31], v[56:59]\n" \
"s_waitcnt lgkmcnt(3)\n" \
"v_mfma_f32_16x16x32_bf16 v[0:3], a[32:35], a[0:3], v[0:3]\n" \
"ds_read_b128 a[24:27], v213 offset:4352\n" \
"s_add_u32 m0, s75, s95\n" \
"v_mfma_f32_16x16x32_bf16 v[0:3], a[36:39], a[4:7], v[0:3]\n" \
"s_waitcnt lgkmcnt(1)\n" \
"ds_read_b128 a[28:31], v213 offset:6400\n" \
"global_load_lds_dwordx4 v222, s[36:37] offset:0 sc1\n" \
"v_mfma_f32_16x16x32_bf16 v[4:7], a[40:43], a[0:3], v[4:7]\n" \
"s_sub_u32 s74, s74, 32768\n" \
"s_cselect_b32 s74, 131072, s74\n" \
"ds_read_b64_tr_b16 a[48:49], v214 offset:16896\n" \
"v_mfma_f32_16x16x32_bf16 v[4:7], a[44:47], a[4:7], v[4:7]\n" \
"ds_read_b64_tr_b16 a[50:51], v214 offset:18944\n" \
"v_mfma_f32_16x16x32_bf16 v[20:23], a[40:43], a[8:11], v[20:23]\n" \
"ds_read_b64_tr_b16 a[52:53], v214 offset:25088\n" \
"v_mfma_f32_16x16x32_bf16 v[20:23], a[44:47], a[12:15], v[20:23]\n" \
"ds_read_b64_tr_b16 a[54:55], v214 offset:27136\n" \
"global_load_lds_dwordx4 v222, s[36:37] offset:1024 sc1\n" \
"v_mfma_f32_16x16x32_bf16 v[16:19], a[32:35], a[8:11], v[16:19]\n" \
"ds_read_b64_tr_b16 a[56:57], v215 offset:16896\n" \
"v_mfma_f32_16x16x32_bf16 v[16:19], a[36:39], a[12:15], v[16:19]\n" \
"ds_read_b64_tr_b16 a[58:59], v215 offset:18944\n" \
"v_mfma_f32_16x16x32_bf16 v[32:35], a[32:35], a[16:19], v[32:35]\n" \
"ds_read_b64_tr_b16 a[60:61], v215 offset:25088\n" \
"v_mfma_f32_16x16x32_bf16 v[32:35], a[36:39], a[20:23], v[32:35]\n" \
"ds_read_b64_tr_b16 a[62:63], v215 offset:27136\n" \
"global_load_lds_dwordx4 v222, s[36:37] offset:2048 sc1\n" \
"v_mfma_f32_16x16x32_bf16 v[36:39], a[40:43], a[16:19], v[36:39]\n" \
"v_add_u32 v213, s74, v211\n" \
"v_add_u32 v214, s74, v212\n" \
"v_mfma_f32_16x16x32_bf16 v[36:39], a[44:47], a[20:23], v[36:39]\n" \
"s_waitcnt lgkmcnt(3)\n" \
"v_xor_b32 v215, 32, v214\n" \
"v_mfma_f32_16x16x32_bf16 v[52:55], a[40:43], a[24:27], v[52:55]\n" \
"v_mfma_f32_16x16x32_bf16 v[52:55], a[44:47], a[28:31], v[52:55]\n" \
"s_waitcnt vmcnt(11) lgkmcnt(0)\n" \
"global_load_lds_dwordx4 v222, s[36:37] offset:3072 sc1\n" \
"v_mfma_f32_16x16x32_bf16 v[48:51], a[32:35], a[24:27], v[48:51]\n" \
"s_add_u32 s36, s36, 0x1000\n" \
"v_mfma_f32_16x16x32_bf16 v[48:51], a[36:39], a[28:31], v[48:51]\n" \
"s_barrier\n" \
"v_mfma_f32_16x16x32_bf16 v[8:11], a[48:51], a[0:3], v[8:11]\n" \
"s_add_u32 m0, m0, 0x5000\n" \
"ds_read_b64_tr_b16 a[32:33], v214 offset:16384\n" \
"v_mfma_f32_16x16x32_bf16 v[8:11], a[52:55], a[4:7], v[8:11]\n" \
"ds_read_b64_tr_b16 a[34:35], v214 offset:18432\n" \
"global_load_lds_dwordx4 v216, s[38:39] offset:-4096\n" \
"v_mfma_f32_16x16x32_bf16 v[12:15], a[56:59], a[0:3], v[12:15]\n" \
"ds_read_b64_tr_b16 a[36:37], v214 offset:24576\n" \
"v_mfma_f32_16x16x32_bf16 v[12:15], a[60:63], a[4:7], v[12:15]\n" \
"buffer_store_dword v224, v225, s[56:59], s30 offen sc1\n" \
"ds_read_b128 a[0:3], v213 offset:0\n" \
"v_mfma_f32_16x16x32_bf16 v[28:31], a[56:59], a[8:11], v[28:31]\n" \
"ds_read_b128 a[4:7], v213 offset:2048\n" \
"v_mfma_f32_16x16x32_bf16 v[28:31], a[60:63], a[12:15], v[28:31]\n" \
"ds_read_b64_tr_b16 a[38:39], v214 offset:26624\n" \
"global_load_lds_dwordx4 v217, s[38:39] offset:-3072\n" \
"v_mfma_f32_16x16x32_bf16 v[24:27], a[48:51], a[8:11], v[24:27]\n" \
"ds_read_b64_tr_b16 a[40:41], v215 offset:16384\n" \
"v_mfma_f32_16x16x32_bf16 v[24:27], a[52:55], a[12:15], v[24:27]\n" \
"ds_read_b128 a[8:11], v213 offset:256\n" \
"v_mfma_f32_16x16x32_bf16 v[40:43], a[48:51], a[16:19], v[40:43]\n" \
"ds_read_b128 a[12:15], v213 offset:2304\n" \
"v_mfma_f32_16x16x32_bf16 v[40:43], a[52:55], a[20:23], v[40:43]\n" \
"ds_read_b64_tr_b16 a[42:43], v215 offset:18432\n" \
"global_load_lds_dwordx4 v218, s[38:39] offset:-2048\n" \
"v_mfma_f32_16x16x32_bf16 v[44:47], a[56:59], a[16:19], v[44:47]\n" \
"ds_read_b64_tr_b16 a[44:45], v215 offset:24576\n" \
"v_mfma_f32_16x16x32_bf16 v[44:47], a[60:63], a[20:23], v[44:47]\n" \
"ds_read_b128 a[16:19], v213 offset:4096\n" \
"v_mfma_f32_16x16x32_bf16 v[60:63], a[56:59], a[24:27], v[60:63]\n" \
"ds_read_b128 a[20:23], v213 offset:6144\n" \
"global_load_lds_dwordx4 v219, s[38:39] offset:-1024\n" \
"v_mfma_f32_16x16x32_bf16 v[60:63], a[60:63], a[28:31], v[60:63]\n" \
"s_add_u32 s38, s38, 0x20000\n" \
"s_addc_u32 s39, s39, 0\n" \
"ds_read_b64_tr_b16 a[46:47], v215 offset:26624\n" \
"v_mfma_f32_16x16x32_bf16 v[56:59], a[48:51], a[24:27], v[56:59]\n" \
"s_sub_u32 s75, s75, 32768\n" \
"s_cselect_b32 s75, 131072, s75\n" \
"v_mfma_f32_16x16x32_bf16 v[56:59], a[52:55], a[28:31], v[56:59]\n" \
"s_waitcnt lgkmcnt(3)\n" \
"s_cmp_eq_u32 s78, 0\n" \
"s_cbranch_scc0 Lrole1_%=\n" \
"s_sub_u32 s72, s64, 14\n" \
"s_call_b64 s[68:69], Lo0_%=\n" \
"buffer_load_dword v220, off, s[56:59], s79 sc1\n" \
"s_mov_b32 s72, 2\n" \
"s_call_b64 s[68:69], Lo0_%=\n" \
"v_mfma_f32_16x16x32_bf16 v[0:3], a[32:35], a[0:3], v[0:3]\n" \
"s_lshl_b32 s71, s61, 4\n" \
"ds_read_b128 a[24:27], v213 offset:4352\n" \
"v_subrev_u32 v72, s71, v251\n" \
"v_mfma_f32_16x16x32_bf16 v[0:3], a[36:39], a[4:7], v[0:3]\n" \
"s_waitcnt lgkmcnt(1)\n" \
"ds_read_b128 a[28:31], v213 offset:6400\n" \
"v_mov_b32 v73, 0x3f80\n" \
"v_mfma_f32_16x16x32_bf16 v[4:7], a[40:43], a[0:3], v[4:7]\n" \
"s_sub_u32 s74, s74, 32768\n" \
"s_cselect_b32 s74, 131072, s74\n" \
"ds_read_b64_tr_b16 a[48:49], v214 offset:16896\n" \
"v_mfma_f32_16x16x32_bf16 v[4:7], a[44:47], a[4:7], v[4:7]\n" \
"ds_read_b64_tr_b16 a[50:51], v214 offset:18944\n" \
"v_mov_b32 v74, 0x3f800000\n" \
"v_mfma_f32_16x16x32_bf16 v[20:23], a[40:43], a[8:11], v[20:23]\n" \
"ds_read_b64_tr_b16 a[52:53], v214 offset:25088\n" \
"v_cmp_eq_u32 vcc, 0, v72\n" \
"v_mfma_f32_16x16x32_bf16 v[20:23], a[44:47], a[12:15], v[20:23]\n" \
"ds_read_b64_tr_b16 a[54:55], v214 offset:27136\n" \
"v_cmp_eq_u32 s[66:67], 1, v72\n" \
"v_mfma_f32_16x16x32_bf16 v[16:19], a[32:35], a[8:11], v[16:19]\n" \
"ds_read_b64_tr_b16 a[56:57], v215 offset:16896\n" \
"v_mov_b32 v66, 0\n" \
"v_mfma_f32_16x16x32_bf16 v[16:19], a[36:39], a[12:15], v[16:19]\n" \
"ds_read_b64_tr_b16 a[58:59], v215 offset:18944\n" \
"v_mov_b32 v68, 0\n" \
"v_mfma_f32_16x16x32_bf16 v[32:35], a[32:35], a[16:19], v[32:35]\n" \
"ds_read_b64_tr_b16 a[60:61], v215 offset:25088\n" \
"v_cndmask_b32 v64, 0, v73, vcc\n" \
"v_mfma_f32_16x16x32_bf16 v[32:35], a[36:39], a[20:23], v[32:35]\n" \
"ds_read_b64_tr_b16 a[62:63], v215 offset:27136\n" \
"v_cndmask_b32 v64, v64, v74, s[66:67]\n" \
"v_mfma_f32_16x16x32_bf16 v[36:39], a[40:43], a[16:19], v[36:39]\n" \
"v_add_u32 v213, s74, v211\n" \
"v_add_u32 v214, s74, v212\n" \
"v_mfma_f32_16x16x32_bf16 v[36:39], a[44:47], a[20:23], v[36:39]\n" \
"s_waitcnt lgkmcnt(3)\n" \
"Lpg12014_%=:\n" \
"s_cbranch_execz Lpg13127_%=\n" \
"v_xor_b32 v215, 32, v214\n" \
"v_mov_b32 v70, v64\n" \
"v_mfma_f32_16x16x32_bf16 v[52:55], a[40:43], a[24:27], v[52:55]\n" \
"v_cmp_eq_u32 vcc, 2, v72\n" \
"v_cmp_eq_u32 s[66:67], 3, v72\n" \
"v_mfma_f32_16x16x32_bf16 v[52:55], a[44:47], a[28:31], v[52:55]\n" \
"s_waitcnt vmcnt(17) lgkmcnt(0)\n" \
"v_mov_b32 v67, 0\n" \
"v_mov_b32 v69, 0\n" \
"v_mfma_f32_16x16x32_bf16 v[48:51], a[32:35], a[24:27], v[48:51]\n" \
"v_cndmask_b32 v65, 0, v73, vcc\n" \
"v_cndmask_b32 v65, v65, v74, s[66:67]\n" \
"v_mfma_f32_16x16x32_bf16 v[48:51], a[36:39], a[28:31], v[48:51]\n" \
"s_barrier\n" \
"v_mov_b32 v71, v65\n" \
"v_mfma_f32_16x16x32_bf16 v[8:11], a[48:51], a[0:3], v[8:11]\n" \
"s_lshl_b32 s83, s95, 1\n" \
"s_add_u32 s83, s83, s74\n" \
"ds_read_b64_tr_b16 a[32:33], v214 offset:16384\n" \
"v_mfma_f32_16x16x32_bf16 v[8:11], a[52:55], a[4:7], v[8:11]\n" \
"s_waitcnt vmcnt(16)\n" \
"ds_read_b64_tr_b16 a[34:35], v214 offset:18432\n" \
"v_cmp_ne_u32 vcc, s60, v220\n" \
"v_mfma_f32_16x16x32_bf16 v[12:15], a[56:59], a[0:3], v[12:15]\n" \
"ds_read_b64_tr_b16 a[36:37], v214 offset:24576\n" \
"v_mfma_f32_16x16x32_bf16 v[12:15], a[60:63], a[4:7], v[12:15]\n" \
"s_and_b32 vcc_hi, vcc_lo, 1\n" \
"s_cbranch_scc1 Lpl4_%=\n" \
"Lpr4_%=:\n" \
"ds_read_b128 a[0:3], v213 offset:0\n" \
"v_mfma_f32_16x16x32_bf16 v[28:31], a[56:59], a[8:11], v[28:31]\n" \
"ds_read_b128 a[4:7], v213 offset:2048\n" \
"v_mfma_f32_16x16x32_bf16 v[28:31], a[60:63], a[12:15], v[28:31]\n" \
"s_add_u32 s71, s76, 0\n" \
"ds_read_b64_tr_b16 a[38:39], v214 offset:26624\n" \
"buffer_load_dwordx4 v[176:179], v248, s[56:59], s71 offen offset:0 sc1\n" \
"v_mfma_f32_16x16x32_bf16 v[24:27], a[48:51], a[8:11], v[24:27]\n" \
"ds_read_b64_tr_b16 a[40:41], v215 offset:16384\n" \
"v_mfma_f32_16x16x32_bf16 v[24:27], a[52:55], a[12:15], v[24:27]\n" \
"buffer_load_dwordx4 v[180:183], v248, s[56:59], s71 offen offset:1024 sc1\n" \
"ds_read_b128 a[8:11], v213 offset:256\n" \
"v_mfma_f32_16x16x32_bf16 v[40:43], a[48:51], a[16:19], v[40:43]\n" \
"ds_read_b128 a[12:15], v213 offset:2304\n" \
"v_mfma_f32_16x16x32_bf16 v[40:43], a[52:55], a[20:23], v[40:43]\n" \
"ds_read_b64_tr_b16 a[42:43], v215 offset:18432\n" \
"v_mfma_f32_16x16x32_bf16 v[44:47], a[56:59], a[16:19], v[44:47]\n" \
"ds_read_b64_tr_b16 a[44:45], v215 offset:24576\n" \
"v_mfma_f32_16x16x32_bf16 v[44:47], a[60:63], a[20:23], v[44:47]\n" \
"ds_read_b128 a[16:19], v213 offset:4096\n" \
"v_mfma_f32_16x16x32_bf16 v[60:63], a[56:59], a[24:27], v[60:63]\n" \
"ds_read_b128 a[20:23], v213 offset:6144\n" \
"v_mfma_f32_16x16x32_bf16 v[60:63], a[60:63], a[28:31], v[60:63]\n" \
"ds_read_b64_tr_b16 a[46:47], v215 offset:26624\n" \
"v_mfma_f32_16x16x32_bf16 v[56:59], a[48:51], a[24:27], v[56:59]\n" \
"buffer_load_dwordx4 v[184:187], v248, s[56:59], s71 offen offset:2048 sc1\n" \
"v_mfma_f32_16x16x32_bf16 v[56:59], a[52:55], a[28:31], v[56:59]\n" \
"s_waitcnt lgkmcnt(3)\n" \
"buffer_load_dwordx4 v[188:191], v248, s[56:59], s71 offen offset:3072 sc1\n" \
"v_mfma_f32_16x16x32_bf16 v[0:3], a[32:35], a[0:3], v[0:3]\n" \
"ds_read_b128 a[24:27], v213 offset:4352\n" \
"s_add_u32 s71, s76, 4096\n" \
"v_mfma_f32_16x16x32_bf16 v[0:3], a[36:39], a[4:7], v[0:3]\n" \
"s_waitcnt lgkmcnt(1)\n" \
"buffer_load_dwordx4 v[192:195], v248, s[56:59], s71 offen offset:0 sc1\n" \
"ds_read_b128 a[28:31], v213 offset:6400\n" \
"v_mfma_f32_16x16x32_bf16 v[4:7], a[40:43], a[0:3], v[4:7]\n" \
"s_sub_u32 s74, s74, 32768\n" \
"s_cselect_b32 s74, 131072, s74\n" \
"ds_read_b64_tr_b16 a[48:49], v214 offset:16896\n" \
"v_mfma_f32_16x16x32_bf16 v[4:7], a[44:47], a[4:7], v[4:7]\n" \
"ds_read_b64_tr_b16 a[50:51], v214 offset:18944\n" \
"v_lshrrev_b32 v132, 4, v248\n" \
"v_mfma_f32_16x16x32_bf16 v[20:23], a[40:43], a[8:11], v[20:23]\n" \
"ds_read_b64_tr_b16 a[52:53], v214 offset:25088\n" \
"v_and_b32 v133, 15, v132\n" \
"v_mfma_f32_16x16x32_bf16 v[20:23], a[44:47], a[12:15], v[20:23]\n" \
"ds_read_b64_tr_b16 a[54:55], v214 offset:27136\n" \
"v_mul_u32_u24 v128, 144, v133\n" \
"v_mfma_f32_16x16x32_bf16 v[16:19], a[32:35], a[8:11], v[16:19]\n" \
"ds_read_b64_tr_b16 a[56:57], v215 offset:16896\n" \
"v_lshrrev_b32 v133, 4, v132\n" \
"v_mfma_f32_16x16x32_bf16 v[16:19], a[36:39], a[12:15], v[16:19]\n" \
"ds_read_b64_tr_b16 a[58:59], v215 offset:18944\n" \
"v_lshl_add_u32 v128, v133, 3, v128\n" \
"v_mfma_f32_16x16x32_bf16 v[32:35], a[32:35], a[16:19], v[32:35]\n" \
"ds_read_b64_tr_b16 a[60:61], v215 offset:25088\n" \
"v_add_u32 v128, s83, v128\n" \
"v_mfma_f32_16x16x32_bf16 v[32:35], a[36:39], a[20:23], v[32:35]\n" \
"ds_read_b64_tr_b16 a[62:63], v215 offset:27136\n" \
"v_lshrrev_b32 v133, 3, v132\n" \
"v_mfma_f32_16x16x32_bf16 v[36:39], a[40:43], a[16:19], v[36:39]\n" \
"v_add_u32 v213, s74, v211\n" \
"v_add_u32 v214, s74, v212\n" \
"v_mfma_f32_16x16x32_bf16 v[36:39], a[44:47], a[20:23], v[36:39]\n" \
"s_waitcnt lgkmcnt(3)\n" \
"v_xor_b32 v215, 32, v214\n" \
"v_and_b32 v134, 7, v132\n" \
"v_mfma_f32_16x16x32_bf16 v[52:55], a[40:43], a[24:27], v[52:55]\n" \
"buffer_load_dwordx4 v[196:199], v248, s[56:59], s71 offen offset:1024 sc1\n" \
"v_mfma_f32_16x16x32_bf16 v[52:55], a[44:47], a[28:31], v[52:55]\n" \
"s_waitcnt vmcnt(6) lgkmcnt(0)\n" \
"buffer_load_dwordx4 v[200:203], v248, s[56:59], s71 offen offset:2048 sc1\n" \
"v_mfma_f32_16x16x32_bf16 v[48:51], a[32:35], a[24:27], v[48:51]\n" \
"buffer_load_dwordx4 v[204:207], v248, s[56:59], s71 offen offset:3072 sc1\n" \
"v_mfma_f32_16x16x32_bf16 v[48:51], a[36:39], a[28:31], v[48:51]\n" \
"s_barrier\n" \
"v_mfma_f32_16x16x32_bf16 v[8:11], a[48:51], a[0:3], v[8:11]\n" \
"s_mov_b32 s70, 0x20000000\n" \
"ds_read_b128 a[192:195], v213 offset:0\n" \
"ds_read_b64_tr_b16 a[128:129], v214 offset:16384\n" \
"v_mfma_f32_16x16x32_bf16 v[8:11], a[52:55], a[4:7], v[8:11]\n" \
"s_sub_u32 s74, s74, 32768\n" \
"s_cselect_b32 s74, 131072, s74\n" \
"ds_read_b64_tr_b16 a[130:131], v214 offset:18432\n" \
"v_mfma_f32_16x16x32_bf16 v[12:15], a[56:59], a[0:3], v[12:15]\n" \
"ds_read_b128 a[196:199], v213 offset:2048\n" \
"ds_read_b64_tr_b16 a[132:133], v214 offset:24576\n" \
"v_mfma_f32_16x16x32_bf16 v[12:15], a[60:63], a[4:7], v[12:15]\n" \
"ds_read_b64_tr_b16 a[134:135], v214 offset:26624\n" \
"v_add_u32 v226, s74, v211\n" \
"v_mfma_f32_16x16x32_bf16 v[28:31], a[56:59], a[8:11], v[28:31]\n" \
"ds_read_b128 a[200:203], v213 offset:256\n" \
"ds_read_b64_tr_b16 a[136:137], v215 offset:16384\n" \
"v_mfma_f32_16x16x32_bf16 v[28:31], a[60:63], a[12:15], v[28:31]\n" \
"ds_read_b64_tr_b16 a[138:139], v215 offset:18432\n" \
"v_add_u32 v227, s74, v212\n" \
"v_mfma_f32_16x16x32_bf16 v[24:27], a[48:51], a[8:11], v[24:27]\n" \
"ds_read_b128 a[204:207], v213 offset:2304\n" \
"ds_read_b64_tr_b16 a[140:141], v215 offset:24576\n" \
"v_mfma_f32_16x16x32_bf16 v[24:27], a[52:55], a[12:15], v[24:27]\n" \
"ds_read_b64_tr_b16 a[142:143], v215 offset:26624\n" \
"v_xor_b32 v228, 32, v227\n" \
"v_mfma_f32_16x16x32_bf16 v[40:43], a[48:51], a[16:19], v[40:43]\n" \
"s_cmp_eq_u32 s79, 65601536\n" \
"s_cselect_b32 s70, 65593344, s70\n" \
"ds_read_b64_tr_b16 a[144:145], v214 offset:16896\n" \
"v_mfma_f32_16x16x32_bf16 v[40:43], a[52:55], a[20:23], v[40:43]\n" \
"buffer_store_dword v224, v225, s[56:59], s70 offen sc1\n" \
"ds_read_b64_tr_b16 a[146:147], v214 offset:18944\n" \
"v_mfma_f32_16x16x32_bf16 v[44:47], a[56:59], a[16:19], v[44:47]\n" \
"ds_read_b64_tr_b16 a[148:149], v214 offset:25088\n" \
"v_mul_u32_u24 v129, 144, v133\n" \
"v_mfma_f32_16x16x32_bf16 v[44:47], a[60:63], a[20:23], v[44:47]\n" \
"ds_read_b64_tr_b16 a[150:151], v214 offset:27136\n" \
"v_lshl_add_u32 v129, v134, 4, v129\n" \
"v_mfma_f32_16x16x32_bf16 v[60:63], a[56:59], a[24:27], v[60:63]\n" \
"ds_read_b64_tr_b16 a[152:153], v215 offset:16896\n" \
"v_add_u32 v129, s83, v129\n" \
"v_mfma_f32_16x16x32_bf16 v[60:63], a[60:63], a[28:31], v[60:63]\n" \
"ds_read_b64_tr_b16 a[154:155], v215 offset:18944\n" \
"v_lshlrev_b32 v134, 4, v134\n" \
"v_mfma_f32_16x16x32_bf16 v[56:59], a[48:51], a[24:27], v[56:59]\n" \
"ds_read_b64_tr_b16 a[156:157], v215 offset:25088\n" \
"v_lshl_add_u32 v130, v133, 12, v134\n" \
"v_mfma_f32_16x16x32_bf16 v[56:59], a[52:55], a[28:31], v[56:59]\n" \
"s_waitcnt lgkmcnt(4)\n" \
"ds_read_b64_tr_b16 a[158:159], v215 offset:27136\n" \
"v_mfma_f32_16x16x32_bf16 v[0:3], a[128:131], a[192:195], v[0:3]\n" \
"ds_read_b128 a[224:227], v226 offset:0\n" \
"ds_read_b64_tr_b16 a[160:161], v227 offset:16384\n" \
"v_mfma_f32_16x16x32_bf16 v[0:3], a[132:135], a[196:199], v[0:3]\n" \
"ds_read_b64_tr_b16 a[162:163], v227 offset:18432\n" \
"v_mfma_f32_16x16x32_bf16 v[16:19], a[128:131], a[200:203], v[16:19]\n" \
"ds_read_b64_tr_b16 a[164:165], v227 offset:24576\n" \
"v_mfma_f32_16x16x32_bf16 v[16:19], a[132:135], a[204:207], v[16:19]\n" \
"ds_read_b128 a[228:231], v226 offset:2048\n" \
"ds_read_b64_tr_b16 a[166:167], v227 offset:26624\n" \
"v_mfma_f32_16x16x32_bf16 v[20:23], a[136:139], a[200:203], v[20:23]\n" \
"ds_read_b64_tr_b16 a[168:169], v228 offset:16384\n" \
"v_mfma_f32_16x16x32_bf16 v[20:23], a[140:143], a[204:207], v[20:23]\n" \
"ds_read_b64_tr_b16 a[170:171], v228 offset:18432\n" \
"v_mfma_f32_16x16x32_bf16 v[4:7], a[136:139], a[192:195], v[4:7]\n" \
"ds_read_b128 a[232:235], v226 offset:256\n" \
"ds_read_b64_tr_b16 a[172:173], v228 offset:24576\n" \
"v_mfma_f32_16x16x32_bf16 v[4:7], a[140:143], a[196:199], v[4:7]\n" \
"s_waitcnt lgkmcnt(6)\n" \
"ds_read_b64_tr_b16 a[174:175], v228 offset:26624\n" \
"v_mfma_f32_16x16x32_bf16 v[8:11], a[144:147], a[192:195], v[8:11]\n" \
"ds_read_b64_tr_b16 a[176:177], v227 offset:16896\n" \
"v_mfma_f32_16x16x32_bf16 v[8:11], a[148:151], a[196:199], v[8:11]\n" \
"ds_read_b128 a[236:239], v226 offset:2304\n" \
"ds_read_b64_tr_b16 a[178:179], v227 offset:18944\n" \
"v_mfma_f32_16x16x32_bf16 v[24:27], a[144:147], a[200:203], v[24:27]\n" \
"ds_read_b64_tr_b16 a[180:181], v227 offset:25088\n" \
"v_mfma_f32_16x16x32_bf16 v[24:27], a[148:151], a[204:207], v[24:27]\n" \
"ds_read_b64_tr_b16 a[182:183], v227 offset:27136\n" \
"v_mfma_f32_16x16x32_bf16 v[28:31], a[152:155], a[200:203], v[28:31]\n" \
"ds_read_b64_tr_b16 a[184:185], v228 offset:16896\n" \
"v_mfma_f32_16x16x32_bf16 v[28:31], a[156:159], a[204:207], v[28:31]\n" \
"ds_read_b64_tr_b16 a[186:187], v228 offset:18944\n" \
"v_mfma_f32_16x16x32_bf16 v[12:15], a[152:155], a[192:195], v[12:15]\n" \
"ds_read_b64_tr_b16 a[188:189], v228 offset:25088\n" \
"v_mfma_f32_16x16x32_bf16 v[12:15], a[156:159], a[196:199], v[12:15]\n" \
"s_waitcnt lgkmcnt(4)\n" \
"ds_read_b64_tr_b16 a[190:191], v228 offset:27136\n" \
"v_mfma_f32_16x16x32_bf16 v[0:3], a[160:163], a[224:227], v[0:3]\n" \
"s_waitcnt vmcnt(1)\n" \
"ds_read_b128 a[208:211], v213 offset:4096\n" \
"v_mfma_f32_16x16x32_bf16 v[0:3], a[164:167], a[228:231], v[0:3]\n" \
"ds_read_b128 a[212:215], v213 offset:6144\n" \
"v_mfma_f32_16x16x32_bf16 v[0:3], v[64:67], v[176:179], v[0:3]\n" \
"ds_read_b128 a[240:243], v226 offset:4096\n" \
"v_mfma_f32_16x16x32_bf16 v[16:19], a[160:163], a[232:235], v[16:19]\n" \
"ds_read_b128 a[244:247], v226 offset:6144\n" \
"v_mfma_f32_16x16x32_bf16 v[16:19], a[164:167], a[236:239], v[16:19]\n" \
"ds_read_b128 a[216:219], v213 offset:4352\n" \
"v_mfma_f32_16x16x32_bf16 v[16:19], v[64:67], v[184:187], v[16:19]\n" \
"ds_read_b128 a[220:223], v213 offset:6400\n" \
"v_cvt_pk_bf16_f32 v0, v0, v1\n" \
"v_mfma_f32_16x16x32_bf16 v[20:23], a[168:171], a[232:235], v[20:23]\n" \
"ds_read_b128 a[248:251], v226 offset:4352\n" \
"v_cvt_pk_bf16_f32 v1, v2, v3\n" \
"v_mfma_f32_16x16x32_bf16 v[20:23], a[172:175], a[236:239], v[20:23]\n" \
"ds_read_b128 a[252:255], v226 offset:6400\n" \
"v_mfma_f32_16x16x32_bf16 v[20:23], v[68:71], v[184:187], v[20:23]\n" \
"ds_write_b64 v128, v[0:1] offset:0\n" \
"v_mfma_f32_16x16x32_bf16 v[4:7], a[168:171], a[224:227], v[4:7]\n" \
"v_cvt_pk_bf16_f32 v16, v16, v17\n" \
"v_cvt_pk_bf16_f32 v17, v18, v19\n" \
"v_mfma_f32_16x16x32_bf16 v[4:7], a[172:175], a[228:231], v[4:7]\n" \
"ds_write_b64 v128, v[16:17] offset:2304\n" \
"v_mfma_f32_16x16x32_bf16 v[4:7], v[68:71], v[176:179], v[4:7]\n" \
"s_waitcnt lgkmcnt(3)\n" \
"v_cvt_pk_bf16_f32 v20, v20, v21\n" \
"v_cvt_pk_bf16_f32 v21, v22, v23\n" \
"v_mfma_f32_16x16x32_bf16 v[8:11], a[176:179], a[224:227], v[8:11]\n" \
"ds_write_b64 v128, v[20:21] offset:2336\n" \
"v_mfma_f32_16x16x32_bf16 v[8:11], a[180:183], a[228:231], v[8:11]\n" \
"v_mfma_f32_16x16x32_bf16 v[8:11], v[64:67], v[180:183], v[8:11]\n" \
"v_cvt_pk_bf16_f32 v4, v4, v5\n" \
"v_cvt_pk_bf16_f32 v5, v6, v7\n" \
"v_mfma_f32_16x16x32_bf16 v[24:27], a[176:179], a[232:235], v[24:27]\n" \
"ds_write_b64 v128, v[4:5] offset:32\n" \
"v_mfma_f32_16x16x32_bf16 v[24:27], a[180:183], a[236:239], v[24:27]\n" \
"v_mfma_f32_16x16x32_bf16 v[24:27], v[64:67], v[188:191], v[24:27]\n" \
"v_cvt_pk_bf16_f32 v8, v8, v9\n" \
"v_cvt_pk_bf16_f32 v9, v10, v11\n" \
"v_mfma_f32_16x16x32_bf16 v[28:31], a[184:187], a[232:235], v[28:31]\n" \
"ds_write_b64 v128, v[8:9] offset:64\n" \
"v_mfma_f32_16x16x32_bf16 v[28:31], a[188:191], a[236:239], v[28:31]\n" \
"v_mfma_f32_16x16x32_bf16 v[28:31], v[68:71], v[188:191], v[28:31]\n" \
"v_cvt_pk_bf16_f32 v24, v24, v25\n" \
"v_cvt_pk_bf16_f32 v25, v26, v27\n" \
"v_mfma_f32_16x16x32_bf16 v[12:15], a[184:187], a[224:227], v[12:15]\n" \
"ds_write_b64 v128, v[24:25] offset:2368\n" \
"v_mfma_f32_16x16x32_bf16 v[12:15], a[188:191], a[228:231], v[12:15]\n" \
"v_mfma_f32_16x16x32_bf16 v[12:15], v[68:71], v[180:183], v[12:15]\n" \
"v_cvt_pk_bf16_f32 v28, v28, v29\n" \
"v_cvt_pk_bf16_f32 v29, v30, v31\n" \
"v_mfma_f32_16x16x32_bf16 v[32:35], a[128:131], a[208:211], v[32:35]\n" \
"ds_write_b64 v128, v[28:29] offset:2400\n" \
"v_mfma_f32_16x16x32_bf16 v[32:35], a[132:135], a[212:215], v[32:35]\n" \
"v_mfma_f32_16x16x32_bf16 v[32:35], a[160:163], a[240:243], v[32:35]\n" \
"v_cvt_pk_bf16_f32 v12, v12, v13\n" \
"v_cvt_pk_bf16_f32 v13, v14, v15\n" \
"v_mfma_f32_16x16x32_bf16 v[32:35], a[164:167], a[244:247], v[32:35]\n" \
"ds_write_b64 v128, v[12:13] offset:96\n" \
"v_mfma_f32_16x16x32_bf16 v[32:35], v[64:67], v[192:195], v[32:35]\n" \
"ds_read_b128 v[140:143], v129 offset:0\n" \
"v_mfma_f32_16x16x32_bf16 v[36:39], a[168:171], a[240:243], v[36:39]\n" \
"ds_read_b128 v[144:147], v129 offset:1152\n" \
"v_mfma_f32_16x16x32_bf16 v[36:39], a[172:175], a[244:247], v[36:39]\n" \
"ds_read_b128 v[148:151], v129 offset:2304\n" \
"v_mfma_f32_16x16x32_bf16 v[36:39], a[136:139], a[208:211], v[36:39]\n" \
"ds_read_b128 v[152:155], v129 offset:3456\n" \
"v_mfma_f32_16x16x32_bf16 v[36:39], a[140:143], a[212:215], v[36:39]\n" \
"v_cvt_pk_bf16_f32 v32, v32, v33\n" \
"v_cvt_pk_bf16_f32 v33, v34, v35\n" \
"v_mfma_f32_16x16x32_bf16 v[36:39], v[68:71], v[192:195], v[36:39]\n" \
"ds_write_b64 v128, v[32:33] offset:0\n" \
"v_mfma_f32_16x16x32_bf16 v[40:43], a[144:147], a[208:211], v[40:43]\n" \
"v_mfma_f32_16x16x32_bf16 v[40:43], a[148:151], a[212:215], v[40:43]\n" \
"s_add_u32 s70, s77, 0\n" \
"s_waitcnt lgkmcnt(4)\n" \
"v_mfma_f32_16x16x32_bf16 v[40:43], a[176:179], a[240:243], v[40:43]\n" \
"v_cvt_pk_bf16_f32 v36, v36, v37\n" \
"v_cvt_pk_bf16_f32 v37, v38, v39\n" \
"v_mfma_f32_16x16x32_bf16 v[40:43], a[180:183], a[244:247], v[40:43]\n" \
"buffer_store_dwordx4 v[140:143], v130, s[52:55], s70 offen sc1\n" \
"ds_write_b64 v128, v[36:37] offset:32\n" \
"v_mfma_f32_16x16x32_bf16 v[40:43], v[64:67], v[196:199], v[40:43]\n" \
"v_mfma_f32_16x16x32_bf16 v[44:47], a[184:187], a[240:243], v[44:47]\n" \
"v_mfma_f32_16x16x32_bf16 v[44:47], a[188:191], a[244:247], v[44:47]\n" \
"v_mfma_f32_16x16x32_bf16 v[44:47], a[152:155], a[208:211], v[44:47]\n" \
"s_add_u32 s70, s77, 32768\n" \
"v_cvt_pk_bf16_f32 v40, v40, v41\n" \
"v_cvt_pk_bf16_f32 v41, v42, v43\n" \
"v_mfma_f32_16x16x32_bf16 v[44:47], a[156:159], a[212:215], v[44:47]\n" \
"s_waitcnt lgkmcnt(4)\n" \
"buffer_store_dwordx4 v[144:147], v130, s[52:55], s70 offen sc1\n" \
"ds_write_b64 v128, v[40:41] offset:64\n" \
"v_mfma_f32_16x16x32_bf16 v[44:47], v[68:71], v[196:199], v[44:47]\n" \
"v_mfma_f32_16x16x32_bf16 v[48:51], a[128:131], a[216:219], v[48:51]\n" \
"v_mfma_f32_16x16x32_bf16 v[48:51], a[132:135], a[220:223], v[48:51]\n" \
"v_mfma_f32_16x16x32_bf16 v[48:51], a[160:163], a[248:251], v[48:51]\n" \
"s_add_u32 s70, s77, 65536\n" \
"v_cvt_pk_bf16_f32 v44, v44, v45\n" \
"v_cvt_pk_bf16_f32 v45, v46, v47\n" \
"v_mfma_f32_16x16x32_bf16 v[48:51], a[164:167], a[252:255], v[48:51]\n" \
"s_waitcnt lgkmcnt(4)\n" \
"buffer_store_dwordx4 v[148:151], v130, s[52:55], s70 offen sc1\n" \
"ds_write_b64 v128, v[44:45] offset:96\n" \
"v_mfma_f32_16x16x32_bf16 v[48:51], v[64:67], v[200:203], v[48:51]\n" \
"ds_read_b128 v[156:159], v129 offset:0\n" \
"v_mfma_f32_16x16x32_bf16 v[52:55], a[168:171], a[248:251], v[52:55]\n" \
"ds_read_b128 v[160:163], v129 offset:1152\n" \
"v_mfma_f32_16x16x32_bf16 v[52:55], a[172:175], a[252:255], v[52:55]\n" \
"v_mfma_f32_16x16x32_bf16 v[52:55], a[136:139], a[216:219], v[52:55]\n" \
"s_add_u32 s70, s77, 98304\n" \
"v_cvt_pk_bf16_f32 v48, v48, v49\n" \
"v_cvt_pk_bf16_f32 v49, v50, v51\n" \
"v_mfma_f32_16x16x32_bf16 v[52:55], a[140:143], a[220:223], v[52:55]\n" \
"s_waitcnt lgkmcnt(6)\n" \
"buffer_store_dwordx4 v[152:155], v130, s[52:55], s70 offen sc1\n" \
"ds_write_b64 v128, v[48:49] offset:2304\n" \
"v_mfma_f32_16x16x32_bf16 v[52:55], v[68:71], v[200:203], v[52:55]\n" \
"v_mfma_f32_16x16x32_bf16 v[56:59], a[144:147], a[216:219], v[56:59]\n" \
"v_mfma_f32_16x16x32_bf16 v[56:59], a[148:151], a[220:223], v[56:59]\n" \
"v_mfma_f32_16x16x32_bf16 v[56:59], a[176:179], a[248:251], v[56:59]\n" \
"s_add_u32 s70, s77, 131072\n" \
"v_cvt_pk_bf16_f32 v52, v52, v53\n" \
"v_cvt_pk_bf16_f32 v53, v54, v55\n" \
"v_mfma_f32_16x16x32_bf16 v[56:59], a[180:183], a[252:255], v[56:59]\n" \
"s_waitcnt lgkmcnt(2)\n" \
"buffer_store_dwordx4 v[156:159], v130, s[52:55], s70 offen sc1\n" \
"ds_write_b64 v128, v[52:53] offset:2336\n" \
"v_mfma_f32_16x16x32_bf16 v[56:59], v[64:67], v[204:207], v[56:59]\n" \
"v_mfma_f32_16x16x32_bf16 v[60:63], a[184:187], a[248:251], v[60:63]\n" \
"v_mfma_f32_16x16x32_bf16 v[60:63], a[188:191], a[252:255], v[60:63]\n" \
"v_mfma_f32_16x16x32_bf16 v[60:63], a[152:155], a[216:219], v[60:63]\n" \
"s_add_u32 s70, s77, 163840\n" \
"v_cvt_pk_bf16_f32 v56, v56, v57\n" \
"v_cvt_pk_bf16_f32 v57, v58, v59\n" \
"v_mfma_f32_16x16x32_bf16 v[60:63], a[156:159], a[220:223], v[60:63]\n" \
"s_waitcnt lgkmcnt(2)\n" \
"buffer_store_dwordx4 v[160:163], v130, s[52:55], s70 offen sc1\n" \
"ds_write_b64 v128, v[56:57] offset:2368\n" \
"v_mfma_f32_16x16x32_bf16 v[60:63], v[68:71], v[204:207], v[60:63]\n" \
"s_nop 7\n" \
"v_cvt_pk_bf16_f32 v60, v60, v61\n" \
"v_cvt_pk_bf16_f32 v61, v62, v63\n" \
"ds_write_b64 v128, v[60:61] offset:2400\n" \
"ds_read_b128 v[164:167], v129 offset:2304\n" \
"ds_read_b128 v[168:171], v129 offset:3456\n" \
"s_add_u32 s70, s77, 196608\n" \
"s_waitcnt lgkmcnt(1)\n" \
"buffer_store_dwordx4 v[164:167], v130, s[52:55], s70 offen sc1\n" \
"s_add_u32 s70, s77, 229376\n" \
"s_waitcnt lgkmcnt(0)\n" \
"buffer_store_dwordx4 v[168:171], v130, s[52:55], s70 offen sc1\n" \
"s_endpgm\n" \
"Lpg12366_%=:\n" \
"s_cbranch_execz Lpg13339_%=\n" \
"Lo0_%=:\n" \
"v_mfma_f32_16x16x32_bf16 v[0:3], a[32:35], a[0:3], v[0:3]\n" \
"ds_read_b128 a[24:27], v213 offset:4352\n" \
"s_add_u32 m0, s75, s95\n" \
"v_mfma_f32_16x16x32_bf16 v[0:3], a[36:39], a[4:7], v[0:3]\n" \
"s_waitcnt lgkmcnt(1)\n" \
"ds_read_b128 a[28:31], v213 offset:6400\n" \
"global_load_lds_dwordx4 v222, s[36:37] offset:0 sc1\n" \
"v_mfma_f32_16x16x32_bf16 v[4:7], a[40:43], a[0:3], v[4:7]\n" \
"s_sub_u32 s74, s74, 32768\n" \
"s_cselect_b32 s74, 131072, s74\n" \
"ds_read_b64_tr_b16 a[48:49], v214 offset:16896\n" \
"v_mfma_f32_16x16x32_bf16 v[4:7], a[44:47], a[4:7], v[4:7]\n" \
"ds_read_b64_tr_b16 a[50:51], v214 offset:18944\n" \
"v_mfma_f32_16x16x32_bf16 v[20:23], a[40:43], a[8:11], v[20:23]\n" \
"ds_read_b64_tr_b16 a[52:53], v214 offset:25088\n" \
"v_mfma_f32_16x16x32_bf16 v[20:23], a[44:47], a[12:15], v[20:23]\n" \
"ds_read_b64_tr_b16 a[54:55], v214 offset:27136\n" \
"global_load_lds_dwordx4 v222, s[36:37] offset:1024 sc1\n" \
"v_mfma_f32_16x16x32_bf16 v[16:19], a[32:35], a[8:11], v[16:19]\n" \
"ds_read_b64_tr_b16 a[56:57], v215 offset:16896\n" \
"v_mfma_f32_16x16x32_bf16 v[16:19], a[36:39], a[12:15], v[16:19]\n" \
"ds_read_b64_tr_b16 a[58:59], v215 offset:18944\n" \
"v_mfma_f32_16x16x32_bf16 v[32:35], a[32:35], a[16:19], v[32:35]\n" \
"ds_read_b64_tr_b16 a[60:61], v215 offset:25088\n" \
"v_mfma_f32_16x16x32_bf16 v[32:35], a[36:39], a[20:23], v[32:35]\n" \
"ds_read_b64_tr_b16 a[62:63], v215 offset:27136\n" \
"global_load_lds_dwordx4 v222, s[36:37] offset:2048 sc1\n" \
"v_mfma_f32_16x16x32_bf16 v[36:39], a[40:43], a[16:19], v[36:39]\n" \
"v_add_u32 v213, s74, v211\n" \
"v_add_u32 v214, s74, v212\n" \
"v_mfma_f32_16x16x32_bf16 v[36:39], a[44:47], a[20:23], v[36:39]\n" \
"s_waitcnt lgkmcnt(3)\n" \
"v_xor_b32 v215, 32, v214\n" \
"v_mfma_f32_16x16x32_bf16 v[52:55], a[40:43], a[24:27], v[52:55]\n" \
"v_mfma_f32_16x16x32_bf16 v[52:55], a[44:47], a[28:31], v[52:55]\n" \
"s_waitcnt vmcnt(19) lgkmcnt(0)\n" \
"global_load_lds_dwordx4 v222, s[36:37] offset:3072 sc1\n" \
"v_mfma_f32_16x16x32_bf16 v[48:51], a[32:35], a[24:27], v[48:51]\n" \
"s_add_u32 s36, s36, 0x1000\n" \
"v_mfma_f32_16x16x32_bf16 v[48:51], a[36:39], a[28:31], v[48:51]\n" \
"s_barrier\n" \
"v_mfma_f32_16x16x32_bf16 v[8:11], a[48:51], a[0:3], v[8:11]\n" \
"s_add_u32 m0, m0, 0x5000\n" \
"ds_read_b64_tr_b16 a[32:33], v214 offset:16384\n" \
"v_mfma_f32_16x16x32_bf16 v[8:11], a[52:55], a[4:7], v[8:11]\n" \
"ds_read_b64_tr_b16 a[34:35], v214 offset:18432\n" \
"global_load_lds_dwordx4 v216, s[38:39] offset:-4096\n" \
"v_mfma_f32_16x16x32_bf16 v[12:15], a[56:59], a[0:3], v[12:15]\n" \
"ds_read_b64_tr_b16 a[36:37], v214 offset:24576\n" \
"v_mfma_f32_16x16x32_bf16 v[12:15], a[60:63], a[4:7], v[12:15]\n" \
"ds_read_b128 a[0:3], v213 offset:0\n" \
"v_mfma_f32_16x16x32_bf16 v[28:31], a[56:59], a[8:11], v[28:31]\n" \
"ds_read_b128 a[4:7], v213 offset:2048\n" \
"v_mfma_f32_16x16x32_bf16 v[28:31], a[60:63], a[12:15], v[28:31]\n" \
"ds_read_b64_tr_b16 a[38:39], v214 offset:26624\n" \
"global_load_lds_dwordx4 v217, s[38:39] offset:-3072\n" \
"v_mfma_f32_16x16x32_bf16 v[24:27], a[48:51], a[8:11], v[24:27]\n" \
"ds_read_b64_tr_b16 a[40:41], v215 offset:16384\n" \
"v_mfma_f32_16x16x32_bf16 v[24:27], a[52:55], a[12:15], v[24:27]\n" \
"ds_read_b128 a[8:11], v213 offset:256\n" \
"v_mfma_f32_16x16x32_bf16 v[40:43], a[48:51], a[16:19], v[40:43]\n" \
"ds_read_b128 a[12:15], v213 offset:2304\n" \
"v_mfma_f32_16x16x32_bf16 v[40:43], a[52:55], a[20:23], v[40:43]\n" \
"ds_read_b64_tr_b16 a[42:43], v215 offset:18432\n" \
"global_load_lds_dwordx4 v218, s[38:39] offset:-2048\n" \
"v_mfma_f32_16x16x32_bf16 v[44:47], a[56:59], a[16:19], v[44:47]\n" \
"ds_read_b64_tr_b16 a[44:45], v215 offset:24576\n" \
"v_mfma_f32_16x16x32_bf16 v[44:47], a[60:63], a[20:23], v[44:47]\n" \
"ds_read_b128 a[16:19], v213 offset:4096\n" \
"global_load_lds_dwordx4 v219, s[38:39] offset:-1024\n" \
"v_mfma_f32_16x16x32_bf16 v[60:63], a[56:59], a[24:27], v[60:63]\n" \
"s_add_u32 s38, s38, 0x20000\n" \
"s_addc_u32 s39, s39, 0\n" \
"ds_read_b128 a[20:23], v213 offset:6144\n" \
"v_mfma_f32_16x16x32_bf16 v[60:63], a[60:63], a[28:31], v[60:63]\n" \
"s_sub_u32 s75, s75, 32768\n" \
"s_cselect_b32 s75, 131072, s75\n" \
"ds_read_b64_tr_b16 a[46:47], v215 offset:26624\n" \
"v_mfma_f32_16x16x32_bf16 v[56:59], a[48:51], a[24:27], v[56:59]\n" \
"s_sub_u32 s72, s72, 1\n" \
"s_cmp_lg_u32 s72, 0\n" \
"v_mfma_f32_16x16x32_bf16 v[56:59], a[52:55], a[28:31], v[56:59]\n" \
"s_waitcnt lgkmcnt(3)\n" \
"s_cbranch_scc1 Lo0_%=\n" \
"s_setpc_b64 s[68:69]\n" \
"Lrole1_%=:\n" \
"Lpg12454_%=:\n" \
"s_cbranch_execz Lcb1_%=\n" \
"s_sub_u32 s72, 50, s64\n" \
"s_call_b64 s[68:69], Lo0_%=\n" \
"buffer_load_dword v220, v221, s[56:59], s82 offen sc1\n" \
"s_mov_b32 s72, 2\n" \
"s_call_b64 s[68:69], Lo0_%=\n" \
"s_waitcnt vmcnt(16)\n" \
"v_cmp_ne_u32 vcc, s60, v220\n" \
"s_nop 1\n" \
"s_and_b32 vcc_hi, vcc_lo, 65535\n" \
"s_cbranch_scc1 Lpl5_%=\n" \
"Lpr5_%=:\n" \
"s_mov_b32 s36, s73\n" \
"s_mov_b64 s[38:39], s[40:41]\n" \
"s_mov_b32 s72, 4\n" \
"s_call_b64 s[68:69], Lo0_%=\n" \
"v_mfma_f32_16x16x32_bf16 v[64:67], a[32:35], a[0:3], 0\n" \
"ds_read_b128 a[24:27], v213 offset:4352\n" \
"s_add_u32 m0, s75, s95\n" \
"v_mfma_f32_16x16x32_bf16 v[64:67], a[36:39], a[4:7], v[64:67]\n" \
"s_waitcnt lgkmcnt(1)\n" \
"ds_read_b128 a[28:31], v213 offset:6400\n" \
"global_load_lds_dwordx4 v222, s[36:37] offset:0 sc1\n" \
"v_mfma_f32_16x16x32_bf16 v[68:71], a[40:43], a[0:3], 0\n" \
"s_sub_u32 s74, s74, 32768\n" \
"s_cselect_b32 s74, 131072, s74\n" \
"ds_read_b64_tr_b16 a[48:49], v214 offset:16896\n" \
"v_mfma_f32_16x16x32_bf16 v[68:71], a[44:47], a[4:7], v[68:71]\n" \
"ds_read_b64_tr_b16 a[50:51], v214 offset:18944\n" \
"v_cvt_pk_bf16_f32 v0, v0, v1\n" \
"v_mfma_f32_16x16x32_bf16 v[84:87], a[40:43], a[8:11], 0\n" \
"ds_read_b64_tr_b16 a[52:53], v214 offset:25088\n" \
"v_cvt_pk_bf16_f32 v1, v2, v3\n" \
"v_mfma_f32_16x16x32_bf16 v[84:87], a[44:47], a[12:15], v[84:87]\n" \
"ds_read_b64_tr_b16 a[54:55], v214 offset:27136\n" \
"global_load_lds_dwordx4 v222, s[36:37] offset:1024 sc1\n" \
"v_mfma_f32_16x16x32_bf16 v[80:83], a[32:35], a[8:11], 0\n" \
"ds_read_b64_tr_b16 a[56:57], v215 offset:16896\n" \
"v_cvt_pk_bf16_f32 v2, v4, v5\n" \
"v_mfma_f32_16x16x32_bf16 v[80:83], a[36:39], a[12:15], v[80:83]\n" \
"ds_read_b64_tr_b16 a[58:59], v215 offset:18944\n" \
"v_cvt_pk_bf16_f32 v3, v6, v7\n" \
"v_mfma_f32_16x16x32_bf16 v[96:99], a[32:35], a[16:19], 0\n" \
"s_add_u32 s71, s76, 0\n" \
"ds_read_b64_tr_b16 a[60:61], v215 offset:25088\n" \
"v_mfma_f32_16x16x32_bf16 v[96:99], a[36:39], a[20:23], v[96:99]\n" \
"ds_read_b64_tr_b16 a[62:63], v215 offset:27136\n" \
"global_load_lds_dwordx4 v222, s[36:37] offset:2048 sc1\n" \
"v_mfma_f32_16x16x32_bf16 v[100:103], a[40:43], a[16:19], 0\n" \
"v_add_u32 v213, s74, v211\n" \
"v_add_u32 v214, s74, v212\n" \
"v_mfma_f32_16x16x32_bf16 v[100:103], a[44:47], a[20:23], v[100:103]\n" \
"s_waitcnt lgkmcnt(3)\n" \
"v_xor_b32 v215, 32, v214\n" \
"v_mfma_f32_16x16x32_bf16 v[116:119], a[40:43], a[24:27], 0\n" \
"v_mfma_f32_16x16x32_bf16 v[116:119], a[44:47], a[28:31], v[116:119]\n" \
"s_waitcnt vmcnt(19) lgkmcnt(0)\n" \
"global_load_lds_dwordx4 v222, s[36:37] offset:3072 sc1\n" \
"v_mfma_f32_16x16x32_bf16 v[112:115], a[32:35], a[24:27], 0\n" \
"s_add_u32 s36, s36, 0x1000\n" \
"v_mfma_f32_16x16x32_bf16 v[112:115], a[36:39], a[28:31], v[112:115]\n" \
"s_barrier\n" \
"v_mfma_f32_16x16x32_bf16 v[72:75], a[48:51], a[0:3], 0\n" \
"s_add_u32 m0, m0, 0x5000\n" \
"ds_read_b64_tr_b16 a[32:33], v214 offset:16384\n" \
"v_mfma_f32_16x16x32_bf16 v[72:75], a[52:55], a[4:7], v[72:75]\n" \
"ds_read_b64_tr_b16 a[34:35], v214 offset:18432\n" \
"global_load_lds_dwordx4 v216, s[38:39] offset:-4096\n" \
"v_mfma_f32_16x16x32_bf16 v[76:79], a[56:59], a[0:3], 0\n" \
"ds_read_b64_tr_b16 a[36:37], v214 offset:24576\n" \
"v_mfma_f32_16x16x32_bf16 v[76:79], a[60:63], a[4:7], v[76:79]\n" \
"ds_read_b128 a[0:3], v213 offset:0\n" \
"v_mfma_f32_16x16x32_bf16 v[92:95], a[56:59], a[8:11], 0\n" \
"ds_read_b128 a[4:7], v213 offset:2048\n" \
"v_mfma_f32_16x16x32_bf16 v[92:95], a[60:63], a[12:15], v[92:95]\n" \
"ds_read_b64_tr_b16 a[38:39], v214 offset:26624\n" \
"global_load_lds_dwordx4 v217, s[38:39] offset:-3072\n" \
"v_mfma_f32_16x16x32_bf16 v[88:91], a[48:51], a[8:11], 0\n" \
"ds_read_b64_tr_b16 a[40:41], v215 offset:16384\n" \
"v_mfma_f32_16x16x32_bf16 v[88:91], a[52:55], a[12:15], v[88:91]\n" \
"buffer_store_dwordx4 v[0:3], v248, s[56:59], s71 offen offset:0 sc1\n" \
"ds_read_b128 a[8:11], v213 offset:256\n" \
"v_cvt_pk_bf16_f32 v4, v8, v9\n" \
"v_cvt_pk_bf16_f32 v5, v10, v11\n" \
"v_cvt_pk_bf16_f32 v6, v12, v13\n" \
"v_mfma_f32_16x16x32_bf16 v[104:107], a[48:51], a[16:19], 0\n" \
"ds_read_b128 a[12:15], v213 offset:2304\n" \
"v_cvt_pk_bf16_f32 v7, v14, v15\n" \
"v_mfma_f32_16x16x32_bf16 v[104:107], a[52:55], a[20:23], v[104:107]\n" \
"ds_read_b64_tr_b16 a[42:43], v215 offset:18432\n" \
"global_load_lds_dwordx4 v218, s[38:39] offset:-2048\n" \
"v_mfma_f32_16x16x32_bf16 v[108:111], a[56:59], a[16:19], 0\n" \
"ds_read_b64_tr_b16 a[44:45], v215 offset:24576\n" \
"v_mfma_f32_16x16x32_bf16 v[108:111], a[60:63], a[20:23], v[108:111]\n" \
"buffer_store_dwordx4 v[4:7], v248, s[56:59], s71 offen offset:1024 sc1\n" \
"ds_read_b128 a[16:19], v213 offset:4096\n" \
"v_cvt_pk_bf16_f32 v8, v16, v17\n" \
"v_cvt_pk_bf16_f32 v9, v18, v19\n" \
"v_cvt_pk_bf16_f32 v10, v20, v21\n" \
"v_mfma_f32_16x16x32_bf16 v[124:127], a[56:59], a[24:27], 0\n" \
"ds_read_b128 a[20:23], v213 offset:6144\n" \
"global_load_lds_dwordx4 v219, s[38:39] offset:-1024\n" \
"v_mfma_f32_16x16x32_bf16 v[124:127], a[60:63], a[28:31], v[124:127]\n" \
"s_add_u32 s38, s38, 0x20000\n" \
"s_addc_u32 s39, s39, 0\n" \
"ds_read_b64_tr_b16 a[46:47], v215 offset:26624\n" \
"v_mfma_f32_16x16x32_bf16 v[120:123], a[48:51], a[24:27], 0\n" \
"s_sub_u32 s75, s75, 32768\n" \
"s_cselect_b32 s75, 131072, s75\n" \
"v_cvt_pk_bf16_f32 v11, v22, v23\n" \
"v_mfma_f32_16x16x32_bf16 v[120:123], a[52:55], a[28:31], v[120:123]\n" \
"s_waitcnt lgkmcnt(3)\n" \
"buffer_store_dwordx4 v[8:11], v248, s[56:59], s71 offen offset:2048 sc1\n" \
"v_cvt_pk_bf16_f32 v12, v24, v25\n" \
"v_cvt_pk_bf16_f32 v13, v26, v27\n" \
"v_cvt_pk_bf16_f32 v14, v28, v29\n" \
"v_cvt_pk_bf16_f32 v15, v30, v31\n" \
"v_mfma_f32_16x16x32_bf16 v[64:67], a[32:35], a[0:3], v[64:67]\n" \
"ds_read_b128 a[24:27], v213 offset:4352\n" \
"s_add_u32 m0, s75, s95\n" \
"v_mfma_f32_16x16x32_bf16 v[64:67], a[36:39], a[4:7], v[64:67]\n" \
"s_waitcnt lgkmcnt(1)\n" \
"ds_read_b128 a[28:31], v213 offset:6400\n" \
"Lpg12577_%=:\n" \
"s_cbranch_execz Lcb1_%=\n" \
"global_load_lds_dwordx4 v222, s[36:37] offset:0 sc1\n" \
"v_mfma_f32_16x16x32_bf16 v[68:71], a[40:43], a[0:3], v[68:71]\n" \
"s_sub_u32 s74, s74, 32768\n" \
"s_cselect_b32 s74, 131072, s74\n" \
"ds_read_b64_tr_b16 a[48:49], v214 offset:16896\n" \
"v_mfma_f32_16x16x32_bf16 v[68:71], a[44:47], a[4:7], v[68:71]\n" \
"ds_read_b64_tr_b16 a[50:51], v214 offset:18944\n" \
"v_mfma_f32_16x16x32_bf16 v[84:87], a[40:43], a[8:11], v[84:87]\n" \
"ds_read_b64_tr_b16 a[52:53], v214 offset:25088\n" \
"v_mfma_f32_16x16x32_bf16 v[84:87], a[44:47], a[12:15], v[84:87]\n" \
"ds_read_b64_tr_b16 a[54:55], v214 offset:27136\n" \
"global_load_lds_dwordx4 v222, s[36:37] offset:1024 sc1\n" \
"v_mfma_f32_16x16x32_bf16 v[80:83], a[32:35], a[8:11], v[80:83]\n" \
"ds_read_b64_tr_b16 a[56:57], v215 offset:16896\n" \
"v_mfma_f32_16x16x32_bf16 v[80:83], a[36:39], a[12:15], v[80:83]\n" \
"ds_read_b64_tr_b16 a[58:59], v215 offset:18944\n" \
"v_mfma_f32_16x16x32_bf16 v[96:99], a[32:35], a[16:19], v[96:99]\n" \
"ds_read_b64_tr_b16 a[60:61], v215 offset:25088\n" \
"v_mfma_f32_16x16x32_bf16 v[96:99], a[36:39], a[20:23], v[96:99]\n" \
"ds_read_b64_tr_b16 a[62:63], v215 offset:27136\n" \
"global_load_lds_dwordx4 v222, s[36:37] offset:2048 sc1\n" \
"v_mfma_f32_16x16x32_bf16 v[100:103], a[40:43], a[16:19], v[100:103]\n" \
"v_add_u32 v213, s74, v211\n" \
"v_add_u32 v214, s74, v212\n" \
"v_mfma_f32_16x16x32_bf16 v[100:103], a[44:47], a[20:23], v[100:103]\n" \
"s_waitcnt lgkmcnt(3)\n" \
"v_xor_b32 v215, 32, v214\n" \
"v_mfma_f32_16x16x32_bf16 v[116:119], a[40:43], a[24:27], v[116:119]\n" \
"v_mfma_f32_16x16x32_bf16 v[116:119], a[44:47], a[28:31], v[116:119]\n" \
"s_waitcnt vmcnt(22) lgkmcnt(0)\n" \
"global_load_lds_dwordx4 v222, s[36:37] offset:3072 sc1\n" \
"v_mfma_f32_16x16x32_bf16 v[112:115], a[32:35], a[24:27], v[112:115]\n" \
"s_add_u32 s36, s36, 0x1000\n" \
"v_mfma_f32_16x16x32_bf16 v[112:115], a[36:39], a[28:31], v[112:115]\n" \
"s_barrier\n" \
"v_mfma_f32_16x16x32_bf16 v[72:75], a[48:51], a[0:3], v[72:75]\n" \
"s_add_u32 m0, m0, 0x5000\n" \
"ds_read_b64_tr_b16 a[32:33], v214 offset:16384\n" \
"v_mfma_f32_16x16x32_bf16 v[72:75], a[52:55], a[4:7], v[72:75]\n" \
"ds_read_b64_tr_b16 a[34:35], v214 offset:18432\n" \
"global_load_lds_dwordx4 v216, s[38:39] offset:-4096\n" \
"v_mfma_f32_16x16x32_bf16 v[76:79], a[56:59], a[0:3], v[76:79]\n" \
"ds_read_b64_tr_b16 a[36:37], v214 offset:24576\n" \
"v_mfma_f32_16x16x32_bf16 v[76:79], a[60:63], a[4:7], v[76:79]\n" \
"ds_read_b128 a[0:3], v213 offset:0\n" \
"v_mfma_f32_16x16x32_bf16 v[92:95], a[56:59], a[8:11], v[92:95]\n" \
"ds_read_b128 a[4:7], v213 offset:2048\n" \
"v_mfma_f32_16x16x32_bf16 v[92:95], a[60:63], a[12:15], v[92:95]\n" \
"ds_read_b64_tr_b16 a[38:39], v214 offset:26624\n" \
"global_load_lds_dwordx4 v217, s[38:39] offset:-3072\n" \
"v_mfma_f32_16x16x32_bf16 v[88:91], a[48:51], a[8:11], v[88:91]\n" \
"ds_read_b64_tr_b16 a[40:41], v215 offset:16384\n" \
"v_mfma_f32_16x16x32_bf16 v[88:91], a[52:55], a[12:15], v[88:91]\n" \
"buffer_store_dwordx4 v[12:15], v248, s[56:59], s71 offen offset:3072 sc1\n" \
"ds_read_b128 a[8:11], v213 offset:256\n" \
"v_cvt_pk_bf16_f32 v16, v32, v33\n" \
"v_cvt_pk_bf16_f32 v17, v34, v35\n" \
"v_cvt_pk_bf16_f32 v18, v36, v37\n" \
"v_mfma_f32_16x16x32_bf16 v[104:107], a[48:51], a[16:19], v[104:107]\n" \
"ds_read_b128 a[12:15], v213 offset:2304\n" \
"v_cvt_pk_bf16_f32 v19, v38, v39\n" \
"v_mfma_f32_16x16x32_bf16 v[104:107], a[52:55], a[20:23], v[104:107]\n" \
"s_add_u32 s71, s76, 4096\n" \
"ds_read_b64_tr_b16 a[42:43], v215 offset:18432\n" \
"global_load_lds_dwordx4 v218, s[38:39] offset:-2048\n" \
"v_mfma_f32_16x16x32_bf16 v[108:111], a[56:59], a[16:19], v[108:111]\n" \
"ds_read_b64_tr_b16 a[44:45], v215 offset:24576\n" \
"v_mfma_f32_16x16x32_bf16 v[108:111], a[60:63], a[20:23], v[108:111]\n" \
"buffer_store_dwordx4 v[16:19], v248, s[56:59], s71 offen offset:0 sc1\n" \
"ds_read_b128 a[16:19], v213 offset:4096\n" \
"v_cvt_pk_bf16_f32 v20, v40, v41\n" \
"v_cvt_pk_bf16_f32 v21, v42, v43\n" \
"v_cvt_pk_bf16_f32 v22, v44, v45\n" \
"v_mfma_f32_16x16x32_bf16 v[124:127], a[56:59], a[24:27], v[124:127]\n" \
"ds_read_b128 a[20:23], v213 offset:6144\n" \
"global_load_lds_dwordx4 v219, s[38:39] offset:-1024\n" \
"v_mfma_f32_16x16x32_bf16 v[124:127], a[60:63], a[28:31], v[124:127]\n" \
"s_add_u32 s38, s38, 0x20000\n" \
"s_addc_u32 s39, s39, 0\n" \
"ds_read_b64_tr_b16 a[46:47], v215 offset:26624\n" \
"v_mfma_f32_16x16x32_bf16 v[120:123], a[48:51], a[24:27], v[120:123]\n" \
"s_sub_u32 s75, s75, 32768\n" \
"s_cselect_b32 s75, 131072, s75\n" \
"v_cvt_pk_bf16_f32 v23, v46, v47\n" \
"v_mfma_f32_16x16x32_bf16 v[120:123], a[52:55], a[28:31], v[120:123]\n" \
"s_waitcnt lgkmcnt(3)\n" \
"buffer_store_dwordx4 v[20:23], v248, s[56:59], s71 offen offset:1024 sc1\n" \
"v_cvt_pk_bf16_f32 v24, v48, v49\n" \
"v_cvt_pk_bf16_f32 v25, v50, v51\n" \
"v_cvt_pk_bf16_f32 v26, v52, v53\n" \
"v_cvt_pk_bf16_f32 v27, v54, v55\n" \
"v_mfma_f32_16x16x32_bf16 v[64:67], a[32:35], a[0:3], v[64:67]\n" \
"ds_read_b128 a[24:27], v213 offset:4352\n" \
"s_add_u32 m0, s75, s95\n" \
"v_mfma_f32_16x16x32_bf16 v[64:67], a[36:39], a[4:7], v[64:67]\n" \
"s_waitcnt lgkmcnt(1)\n" \
"ds_read_b128 a[28:31], v213 offset:6400\n" \
"global_load_lds_dwordx4 v222, s[36:37] offset:0 sc1\n" \
"v_mfma_f32_16x16x32_bf16 v[68:71], a[40:43], a[0:3], v[68:71]\n" \
"s_sub_u32 s74, s74, 32768\n" \
"s_cselect_b32 s74, 131072, s74\n" \
"ds_read_b64_tr_b16 a[48:49], v214 offset:16896\n" \
"v_mfma_f32_16x16x32_bf16 v[68:71], a[44:47], a[4:7], v[68:71]\n" \
"ds_read_b64_tr_b16 a[50:51], v214 offset:18944\n" \
"v_mfma_f32_16x16x32_bf16 v[84:87], a[40:43], a[8:11], v[84:87]\n" \
"ds_read_b64_tr_b16 a[52:53], v214 offset:25088\n" \
"v_mfma_f32_16x16x32_bf16 v[84:87], a[44:47], a[12:15], v[84:87]\n" \
"ds_read_b64_tr_b16 a[54:55], v214 offset:27136\n" \
"global_load_lds_dwordx4 v222, s[36:37] offset:1024 sc1\n" \
"v_mfma_f32_16x16x32_bf16 v[80:83], a[32:35], a[8:11], v[80:83]\n" \
"ds_read_b64_tr_b16 a[56:57], v215 offset:16896\n" \
"v_mfma_f32_16x16x32_bf16 v[80:83], a[36:39], a[12:15], v[80:83]\n" \
"ds_read_b64_tr_b16 a[58:59], v215 offset:18944\n" \
"v_mfma_f32_16x16x32_bf16 v[96:99], a[32:35], a[16:19], v[96:99]\n" \
"ds_read_b64_tr_b16 a[60:61], v215 offset:25088\n" \
"v_mfma_f32_16x16x32_bf16 v[96:99], a[36:39], a[20:23], v[96:99]\n" \
"ds_read_b64_tr_b16 a[62:63], v215 offset:27136\n" \
"global_load_lds_dwordx4 v222, s[36:37] offset:2048 sc1\n" \
"v_mfma_f32_16x16x32_bf16 v[100:103], a[40:43], a[16:19], v[100:103]\n" \
"v_add_u32 v213, s74, v211\n" \
"v_add_u32 v214, s74, v212\n" \
"v_mfma_f32_16x16x32_bf16 v[100:103], a[44:47], a[20:23], v[100:103]\n" \
"s_waitcnt lgkmcnt(3)\n" \
"v_xor_b32 v215, 32, v214\n" \
"v_mfma_f32_16x16x32_bf16 v[116:119], a[40:43], a[24:27], v[116:119]\n" \
"v_mfma_f32_16x16x32_bf16 v[116:119], a[44:47], a[28:31], v[116:119]\n" \
"s_waitcnt vmcnt(25) lgkmcnt(0)\n" \
"global_load_lds_dwordx4 v222, s[36:37] offset:3072 sc1\n" \
"v_mfma_f32_16x16x32_bf16 v[112:115], a[32:35], a[24:27], v[112:115]\n" \
"s_add_u32 s36, s36, 0x1000\n" \
"v_mfma_f32_16x16x32_bf16 v[112:115], a[36:39], a[28:31], v[112:115]\n" \
"s_barrier\n" \
"v_mfma_f32_16x16x32_bf16 v[72:75], a[48:51], a[0:3], v[72:75]\n" \
"s_add_u32 m0, m0, 0x5000\n" \
"ds_read_b64_tr_b16 a[32:33], v214 offset:16384\n" \
"v_mfma_f32_16x16x32_bf16 v[72:75], a[52:55], a[4:7], v[72:75]\n" \
"ds_read_b64_tr_b16 a[34:35], v214 offset:18432\n" \
"global_load_lds_dwordx4 v216, s[38:39] offset:-4096\n" \
"v_mfma_f32_16x16x32_bf16 v[76:79], a[56:59], a[0:3], v[76:79]\n" \
"ds_read_b64_tr_b16 a[36:37], v214 offset:24576\n" \
"v_mfma_f32_16x16x32_bf16 v[76:79], a[60:63], a[4:7], v[76:79]\n" \
"ds_read_b128 a[0:3], v213 offset:0\n" \
"v_mfma_f32_16x16x32_bf16 v[92:95], a[56:59], a[8:11], v[92:95]\n" \
"ds_read_b128 a[4:7], v213 offset:2048\n" \
"v_mfma_f32_16x16x32_bf16 v[92:95], a[60:63], a[12:15], v[92:95]\n" \
"ds_read_b64_tr_b16 a[38:39], v214 offset:26624\n" \
"global_load_lds_dwordx4 v217, s[38:39] offset:-3072\n" \
"v_mfma_f32_16x16x32_bf16 v[88:91], a[48:51], a[8:11], v[88:91]\n" \
"ds_read_b64_tr_b16 a[40:41], v215 offset:16384\n" \
"v_mfma_f32_16x16x32_bf16 v[88:91], a[52:55], a[12:15], v[88:91]\n" \
"buffer_store_dwordx4 v[24:27], v248, s[56:59], s71 offen offset:2048 sc1\n" \
"ds_read_b128 a[8:11], v213 offset:256\n" \
"v_cvt_pk_bf16_f32 v28, v56, v57\n" \
"v_cvt_pk_bf16_f32 v29, v58, v59\n" \
"v_cvt_pk_bf16_f32 v30, v60, v61\n" \
"v_mfma_f32_16x16x32_bf16 v[104:107], a[48:51], a[16:19], v[104:107]\n" \
"ds_read_b128 a[12:15], v213 offset:2304\n" \
"v_cvt_pk_bf16_f32 v31, v62, v63\n" \
"v_mfma_f32_16x16x32_bf16 v[104:107], a[52:55], a[20:23], v[104:107]\n" \
"ds_read_b64_tr_b16 a[42:43], v215 offset:18432\n" \
"global_load_lds_dwordx4 v218, s[38:39] offset:-2048\n" \
"v_mfma_f32_16x16x32_bf16 v[108:111], a[56:59], a[16:19], v[108:111]\n" \
"ds_read_b64_tr_b16 a[44:45], v215 offset:24576\n" \
"v_mfma_f32_16x16x32_bf16 v[108:111], a[60:63], a[20:23], v[108:111]\n" \
"buffer_store_dwordx4 v[28:31], v248, s[56:59], s71 offen offset:3072 sc1\n" \
"ds_read_b128 a[16:19], v213 offset:4096\n" \
"v_mfma_f32_16x16x32_bf16 v[124:127], a[56:59], a[24:27], v[124:127]\n" \
"ds_read_b128 a[20:23], v213 offset:6144\n" \
"global_load_lds_dwordx4 v219, s[38:39] offset:-1024\n" \
"v_mfma_f32_16x16x32_bf16 v[124:127], a[60:63], a[28:31], v[124:127]\n" \
"s_add_u32 s38, s38, 0x20000\n" \
"s_addc_u32 s39, s39, 0\n" \
"ds_read_b64_tr_b16 a[46:47], v215 offset:26624\n" \
"v_mfma_f32_16x16x32_bf16 v[120:123], a[48:51], a[24:27], v[120:123]\n" \
"s_sub_u32 s75, s75, 32768\n" \
"s_cselect_b32 s75, 131072, s75\n" \
"v_mfma_f32_16x16x32_bf16 v[120:123], a[52:55], a[28:31], v[120:123]\n" \
"s_waitcnt lgkmcnt(3)\n" \
"v_mfma_f32_16x16x32_bf16 v[64:67], a[32:35], a[0:3], v[64:67]\n" \
"ds_read_b128 a[24:27], v213 offset:4352\n" \
"s_add_u32 m0, s75, s95\n" \
"v_mfma_f32_16x16x32_bf16 v[64:67], a[36:39], a[4:7], v[64:67]\n" \
"s_waitcnt lgkmcnt(1)\n" \
"ds_read_b128 a[28:31], v213 offset:6400\n" \
"global_load_lds_dwordx4 v222, s[36:37] offset:0 sc1\n" \
"v_mfma_f32_16x16x32_bf16 v[68:71], a[40:43], a[0:3], v[68:71]\n" \
"s_sub_u32 s74, s74, 32768\n" \
"s_cselect_b32 s74, 131072, s74\n" \
"ds_read_b64_tr_b16 a[48:49], v214 offset:16896\n" \
"v_mfma_f32_16x16x32_bf16 v[68:71], a[44:47], a[4:7], v[68:71]\n" \
"ds_read_b64_tr_b16 a[50:51], v214 offset:18944\n" \
"v_mfma_f32_16x16x32_bf16 v[84:87], a[40:43], a[8:11], v[84:87]\n" \
"ds_read_b64_tr_b16 a[52:53], v214 offset:25088\n" \
"v_mfma_f32_16x16x32_bf16 v[84:87], a[44:47], a[12:15], v[84:87]\n" \
"ds_read_b64_tr_b16 a[54:55], v214 offset:27136\n" \
"global_load_lds_dwordx4 v222, s[36:37] offset:1024 sc1\n" \
"v_mfma_f32_16x16x32_bf16 v[80:83], a[32:35], a[8:11], v[80:83]\n" \
"ds_read_b64_tr_b16 a[56:57], v215 offset:16896\n" \
"v_mfma_f32_16x16x32_bf16 v[80:83], a[36:39], a[12:15], v[80:83]\n" \
"ds_read_b64_tr_b16 a[58:59], v215 offset:18944\n" \
"v_mfma_f32_16x16x32_bf16 v[96:99], a[32:35], a[16:19], v[96:99]\n" \
"ds_read_b64_tr_b16 a[60:61], v215 offset:25088\n" \
"v_mfma_f32_16x16x32_bf16 v[96:99], a[36:39], a[20:23], v[96:99]\n" \
"ds_read_b64_tr_b16 a[62:63], v215 offset:27136\n" \
"global_load_lds_dwordx4 v222, s[36:37] offset:2048 sc1\n" \
"v_mfma_f32_16x16x32_bf16 v[100:103], a[40:43], a[16:19], v[100:103]\n" \
"v_add_u32 v213, s74, v211\n" \
"v_add_u32 v214, s74, v212\n" \
"v_mfma_f32_16x16x32_bf16 v[100:103], a[44:47], a[20:23], v[100:103]\n" \
"s_waitcnt lgkmcnt(3)\n" \
"v_xor_b32 v215, 32, v214\n" \
"v_mfma_f32_16x16x32_bf16 v[116:119], a[40:43], a[24:27], v[116:119]\n" \
"v_mfma_f32_16x16x32_bf16 v[116:119], a[44:47], a[28:31], v[116:119]\n" \
"s_waitcnt vmcnt(25) lgkmcnt(0)\n" \
"global_load_lds_dwordx4 v222, s[36:37] offset:3072 sc1\n" \
"v_mfma_f32_16x16x32_bf16 v[112:115], a[32:35], a[24:27], v[112:115]\n" \
"s_add_u32 s36, s36, 0x1000\n" \
"v_mfma_f32_16x16x32_bf16 v[112:115], a[36:39], a[28:31], v[112:115]\n" \
"s_barrier\n" \
"v_mfma_f32_16x16x32_bf16 v[72:75], a[48:51], a[0:3], v[72:75]\n" \
"s_add_u32 m0, m0, 0x5000\n" \
"ds_read_b64_tr_b16 a[32:33], v214 offset:16384\n" \
"v_mfma_f32_16x16x32_bf16 v[72:75], a[52:55], a[4:7], v[72:75]\n" \
"ds_read_b64_tr_b16 a[34:35], v214 offset:18432\n" \
"global_load_lds_dwordx4 v216, s[38:39] offset:-4096\n" \
"v_mfma_f32_16x16x32_bf16 v[76:79], a[56:59], a[0:3], v[76:79]\n" \
"ds_read_b64_tr_b16 a[36:37], v214 offset:24576\n" \
"v_mfma_f32_16x16x32_bf16 v[76:79], a[60:63], a[4:7], v[76:79]\n" \
"ds_read_b128 a[0:3], v213 offset:0\n" \
"v_mfma_f32_16x16x32_bf16 v[92:95], a[56:59], a[8:11], v[92:95]\n" \
"ds_read_b128 a[4:7], v213 offset:2048\n" \
"v_mfma_f32_16x16x32_bf16 v[92:95], a[60:63], a[12:15], v[92:95]\n" \
"ds_read_b64_tr_b16 a[38:39], v214 offset:26624\n" \
"global_load_lds_dwordx4 v217, s[38:39] offset:-3072\n" \
"v_mfma_f32_16x16x32_bf16 v[88:91], a[48:51], a[8:11], v[88:91]\n" \
"ds_read_b64_tr_b16 a[40:41], v215 offset:16384\n" \
"v_mfma_f32_16x16x32_bf16 v[88:91], a[52:55], a[12:15], v[88:91]\n" \
"ds_read_b128 a[8:11], v213 offset:256\n" \
"v_mfma_f32_16x16x32_bf16 v[104:107], a[48:51], a[16:19], v[104:107]\n" \
"ds_read_b128 a[12:15], v213 offset:2304\n" \
"v_mfma_f32_16x16x32_bf16 v[104:107], a[52:55], a[20:23], v[104:107]\n" \
"ds_read_b64_tr_b16 a[42:43], v215 offset:18432\n" \
"global_load_lds_dwordx4 v218, s[38:39] offset:-2048\n" \
"v_mfma_f32_16x16x32_bf16 v[108:111], a[56:59], a[16:19], v[108:111]\n" \
"ds_read_b64_tr_b16 a[44:45], v215 offset:24576\n" \
"v_mfma_f32_16x16x32_bf16 v[108:111], a[60:63], a[20:23], v[108:111]\n" \
"ds_read_b128 a[16:19], v213 offset:4096\n" \
"v_mfma_f32_16x16x32_bf16 v[124:127], a[56:59], a[24:27], v[124:127]\n" \
"ds_read_b128 a[20:23], v213 offset:6144\n" \
"global_load_lds_dwordx4 v219, s[38:39] offset:-1024\n" \
"v_mfma_f32_16x16x32_bf16 v[124:127], a[60:63], a[28:31], v[124:127]\n" \
"s_add_u32 s38, s38, 0x20000\n" \
"s_addc_u32 s39, s39, 0\n" \
"ds_read_b64_tr_b16 a[46:47], v215 offset:26624\n" \
"v_mfma_f32_16x16x32_bf16 v[120:123], a[48:51], a[24:27], v[120:123]\n" \
"s_sub_u32 s75, s75, 32768\n" \
"s_cselect_b32 s75, 131072, s75\n" \
"v_mfma_f32_16x16x32_bf16 v[120:123], a[52:55], a[28:31], v[120:123]\n" \
"s_waitcnt lgkmcnt(3)\n" \
"v_mfma_f32_16x16x32_bf16 v[64:67], a[32:35], a[0:3], v[64:67]\n" \
"ds_read_b128 a[24:27], v213 offset:4352\n" \
"s_add_u32 m0, s75, s95\n" \
"v_mfma_f32_16x16x32_bf16 v[64:67], a[36:39], a[4:7], v[64:67]\n" \
"s_waitcnt lgkmcnt(1)\n" \
"ds_read_b128 a[28:31], v213 offset:6400\n" \
"global_load_lds_dwordx4 v222, s[36:37] offset:0 sc1\n" \
"v_mfma_f32_16x16x32_bf16 v[68:71], a[40:43], a[0:3], v[68:71]\n" \
"s_sub_u32 s74, s74, 32768\n" \
"s_cselect_b32 s74, 131072, s74\n" \
"ds_read_b64_tr_b16 a[48:49], v214 offset:16896\n" \
"v_mfma_f32_16x16x32_bf16 v[68:71], a[44:47], a[4:7], v[68:71]\n" \
"ds_read_b64_tr_b16 a[50:51], v214 offset:18944\n" \
"v_mfma_f32_16x16x32_bf16 v[84:87], a[40:43], a[8:11], v[84:87]\n" \
"ds_read_b64_tr_b16 a[52:53], v214 offset:25088\n" \
"v_mfma_f32_16x16x32_bf16 v[84:87], a[44:47], a[12:15], v[84:87]\n" \
"ds_read_b64_tr_b16 a[54:55], v214 offset:27136\n" \
"global_load_lds_dwordx4 v222, s[36:37] offset:1024 sc1\n" \
"v_mfma_f32_16x16x32_bf16 v[80:83], a[32:35], a[8:11], v[80:83]\n" \
"ds_read_b64_tr_b16 a[56:57], v215 offset:16896\n" \
"v_mfma_f32_16x16x32_bf16 v[80:83], a[36:39], a[12:15], v[80:83]\n" \
"ds_read_b64_tr_b16 a[58:59], v215 offset:18944\n" \
"v_mfma_f32_16x16x32_bf16 v[96:99], a[32:35], a[16:19], v[96:99]\n" \
"ds_read_b64_tr_b16 a[60:61], v215 offset:25088\n" \
"v_mfma_f32_16x16x32_bf16 v[96:99], a[36:39], a[20:23], v[96:99]\n" \
"ds_read_b64_tr_b16 a[62:63], v215 offset:27136\n" \
"global_load_lds_dwordx4 v222, s[36:37] offset:2048 sc1\n" \
"v_mfma_f32_16x16x32_bf16 v[100:103], a[40:43], a[16:19], v[100:103]\n" \
"v_add_u32 v213, s74, v211\n" \
"v_add_u32 v214, s74, v212\n" \
"v_mfma_f32_16x16x32_bf16 v[100:103], a[44:47], a[20:23], v[100:103]\n" \
"s_waitcnt lgkmcnt(3)\n" \
"v_xor_b32 v215, 32, v214\n" \
"v_mfma_f32_16x16x32_bf16 v[116:119], a[40:43], a[24:27], v[116:119]\n" \
"v_mfma_f32_16x16x32_bf16 v[116:119], a[44:47], a[28:31], v[116:119]\n" \
"s_waitcnt vmcnt(22) lgkmcnt(0)\n" \
"global_load_lds_dwordx4 v222, s[36:37] offset:3072 sc1\n" \
"v_mfma_f32_16x16x32_bf16 v[112:115], a[32:35], a[24:27], v[112:115]\n" \
"s_add_u32 s36, s36, 0x1000\n" \
"v_mfma_f32_16x16x32_bf16 v[112:115], a[36:39], a[28:31], v[112:115]\n" \
"s_barrier\n" \
"v_mfma_f32_16x16x32_bf16 v[72:75], a[48:51], a[0:3], v[72:75]\n" \
"s_add_u32 m0, m0, 0x5000\n" \
"ds_read_b64_tr_b16 a[32:33], v214 offset:16384\n" \
"v_mfma_f32_16x16x32_bf16 v[72:75], a[52:55], a[4:7], v[72:75]\n" \
"ds_read_b64_tr_b16 a[34:35], v214 offset:18432\n" \
"global_load_lds_dwordx4 v216, s[38:39] offset:-4096\n" \
"v_mfma_f32_16x16x32_bf16 v[76:79], a[56:59], a[0:3], v[76:79]\n" \
"ds_read_b64_tr_b16 a[36:37], v214 offset:24576\n" \
"v_mfma_f32_16x16x32_bf16 v[76:79], a[60:63], a[4:7], v[76:79]\n" \
"ds_read_b128 a[0:3], v213 offset:0\n" \
"v_mfma_f32_16x16x32_bf16 v[92:95], a[56:59], a[8:11], v[92:95]\n" \
"ds_read_b128 a[4:7], v213 offset:2048\n" \
"v_mfma_f32_16x16x32_bf16 v[92:95], a[60:63], a[12:15], v[92:95]\n" \
"ds_read_b64_tr_b16 a[38:39], v214 offset:26624\n" \
"global_load_lds_dwordx4 v217, s[38:39] offset:-3072\n" \
"v_mfma_f32_16x16x32_bf16 v[88:91], a[48:51], a[8:11], v[88:91]\n" \
"ds_read_b64_tr_b16 a[40:41], v215 offset:16384\n" \
"v_mfma_f32_16x16x32_bf16 v[88:91], a[52:55], a[12:15], v[88:91]\n" \
"ds_read_b128 a[8:11], v213 offset:256\n" \
"v_mfma_f32_16x16x32_bf16 v[104:107], a[48:51], a[16:19], v[104:107]\n" \
"ds_read_b128 a[12:15], v213 offset:2304\n" \
"v_mfma_f32_16x16x32_bf16 v[104:107], a[52:55], a[20:23], v[104:107]\n" \
"ds_read_b64_tr_b16 a[42:43], v215 offset:18432\n" \
"global_load_lds_dwordx4 v218, s[38:39] offset:-2048\n" \
"v_mfma_f32_16x16x32_bf16 v[108:111], a[56:59], a[16:19], v[108:111]\n" \
"ds_read_b64_tr_b16 a[44:45], v215 offset:24576\n" \
"v_mfma_f32_16x16x32_bf16 v[108:111], a[60:63], a[20:23], v[108:111]\n" \
"ds_read_b128 a[16:19], v213 offset:4096\n" \
"v_mfma_f32_16x16x32_bf16 v[124:127], a[56:59], a[24:27], v[124:127]\n" \
"ds_read_b128 a[20:23], v213 offset:6144\n" \
"global_load_lds_dwordx4 v219, s[38:39] offset:-1024\n" \
"v_mfma_f32_16x16x32_bf16 v[124:127], a[60:63], a[28:31], v[124:127]\n" \
"s_add_u32 s38, s38, 0x20000\n" \
"s_addc_u32 s39, s39, 0\n" \
"ds_read_b64_tr_b16 a[46:47], v215 offset:26624\n" \
"v_mfma_f32_16x16x32_bf16 v[120:123], a[48:51], a[24:27], v[120:123]\n" \
"s_sub_u32 s75, s75, 32768\n" \
"s_cselect_b32 s75, 131072, s75\n" \
"v_mfma_f32_16x16x32_bf16 v[120:123], a[52:55], a[28:31], v[120:123]\n" \
"s_waitcnt lgkmcnt(3)\n" \
"v_mfma_f32_16x16x32_bf16 v[64:67], a[32:35], a[0:3], v[64:67]\n" \
"ds_read_b128 a[24:27], v213 offset:4352\n" \
"s_add_u32 m0, s75, s95\n" \
"v_mfma_f32_16x16x32_bf16 v[64:67], a[36:39], a[4:7], v[64:67]\n" \
"s_waitcnt lgkmcnt(1)\n" \
"ds_read_b128 a[28:31], v213 offset:6400\n" \
"global_load_lds_dwordx4 v222, s[36:37] offset:0 sc1\n" \
"v_mfma_f32_16x16x32_bf16 v[68:71], a[40:43], a[0:3], v[68:71]\n" \
"s_sub_u32 s74, s74, 32768\n" \
"s_cselect_b32 s74, 131072, s74\n" \
"ds_read_b64_tr_b16 a[48:49], v214 offset:16896\n" \
"v_mfma_f32_16x16x32_bf16 v[68:71], a[44:47], a[4:7], v[68:71]\n" \
"ds_read_b64_tr_b16 a[50:51], v214 offset:18944\n" \
"v_mfma_f32_16x16x32_bf16 v[84:87], a[40:43], a[8:11], v[84:87]\n" \
"ds_read_b64_tr_b16 a[52:53], v214 offset:25088\n" \
"v_mfma_f32_16x16x32_bf16 v[84:87], a[44:47], a[12:15], v[84:87]\n" \
"ds_read_b64_tr_b16 a[54:55], v214 offset:27136\n" \
"global_load_lds_dwordx4 v222, s[36:37] offset:1024 sc1\n" \
"v_mfma_f32_16x16x32_bf16 v[80:83], a[32:35], a[8:11], v[80:83]\n" \
"ds_read_b64_tr_b16 a[56:57], v215 offset:16896\n" \
"v_mfma_f32_16x16x32_bf16 v[80:83], a[36:39], a[12:15], v[80:83]\n" \
"ds_read_b64_tr_b16 a[58:59], v215 offset:18944\n" \
"v_mfma_f32_16x16x32_bf16 v[96:99], a[32:35], a[16:19], v[96:99]\n" \
"ds_read_b64_tr_b16 a[60:61], v215 offset:25088\n" \
"v_mfma_f32_16x16x32_bf16 v[96:99], a[36:39], a[20:23], v[96:99]\n" \
"ds_read_b64_tr_b16 a[62:63], v215 offset:27136\n" \
"global_load_lds_dwordx4 v222, s[36:37] offset:2048 sc1\n" \
"v_mfma_f32_16x16x32_bf16 v[100:103], a[40:43], a[16:19], v[100:103]\n" \
"v_add_u32 v213, s74, v211\n" \
"v_add_u32 v214, s74, v212\n" \
"v_mfma_f32_16x16x32_bf16 v[100:103], a[44:47], a[20:23], v[100:103]\n" \
"s_waitcnt lgkmcnt(3)\n" \
"v_xor_b32 v215, 32, v214\n" \
"v_mfma_f32_16x16x32_bf16 v[116:119], a[40:43], a[24:27], v[116:119]\n" \
"v_mfma_f32_16x16x32_bf16 v[116:119], a[44:47], a[28:31], v[116:119]\n" \
"s_waitcnt vmcnt(19) lgkmcnt(0)\n" \
"global_load_lds_dwordx4 v222, s[36:37] offset:3072 sc1\n" \
"v_mfma_f32_16x16x32_bf16 v[112:115], a[32:35], a[24:27], v[112:115]\n" \
"s_add_u32 s36, s36, 0x1000\n" \
"v_mfma_f32_16x16x32_bf16 v[112:115], a[36:39], a[28:31], v[112:115]\n" \
"s_barrier\n" \
"v_mfma_f32_16x16x32_bf16 v[72:75], a[48:51], a[0:3], v[72:75]\n" \
"s_add_u32 m0, m0, 0x5000\n" \
"ds_read_b64_tr_b16 a[32:33], v214 offset:16384\n" \
"v_mfma_f32_16x16x32_bf16 v[72:75], a[52:55], a[4:7], v[72:75]\n" \
"ds_read_b64_tr_b16 a[34:35], v214 offset:18432\n" \
"global_load_lds_dwordx4 v216, s[38:39] offset:-4096\n" \
"v_mfma_f32_16x16x32_bf16 v[76:79], a[56:59], a[0:3], v[76:79]\n" \
"ds_read_b64_tr_b16 a[36:37], v214 offset:24576\n" \
"v_mfma_f32_16x16x32_bf16 v[76:79], a[60:63], a[4:7], v[76:79]\n" \
"buffer_store_dword v224, v225, s[56:59], s79 offen sc1\n" \
"ds_read_b128 a[0:3], v213 offset:0\n" \
"v_mfma_f32_16x16x32_bf16 v[92:95], a[56:59], a[8:11], v[92:95]\n" \
"ds_read_b128 a[4:7], v213 offset:2048\n" \
"v_mfma_f32_16x16x32_bf16 v[92:95], a[60:63], a[12:15], v[92:95]\n" \
"ds_read_b64_tr_b16 a[38:39], v214 offset:26624\n" \
"global_load_lds_dwordx4 v217, s[38:39] offset:-3072\n" \
"v_mfma_f32_16x16x32_bf16 v[88:91], a[48:51], a[8:11], v[88:91]\n" \
"ds_read_b64_tr_b16 a[40:41], v215 offset:16384\n" \
"v_mfma_f32_16x16x32_bf16 v[88:91], a[52:55], a[12:15], v[88:91]\n" \
"ds_read_b128 a[8:11], v213 offset:256\n" \
"v_mfma_f32_16x16x32_bf16 v[104:107], a[48:51], a[16:19], v[104:107]\n" \
"ds_read_b128 a[12:15], v213 offset:2304\n" \
"v_mfma_f32_16x16x32_bf16 v[104:107], a[52:55], a[20:23], v[104:107]\n" \
"ds_read_b64_tr_b16 a[42:43], v215 offset:18432\n" \
"global_load_lds_dwordx4 v218, s[38:39] offset:-2048\n" \
"v_mfma_f32_16x16x32_bf16 v[108:111], a[56:59], a[16:19], v[108:111]\n" \
"ds_read_b64_tr_b16 a[44:45], v215 offset:24576\n" \
"v_mfma_f32_16x16x32_bf16 v[108:111], a[60:63], a[20:23], v[108:111]\n" \
"ds_read_b128 a[16:19], v213 offset:4096\n" \
"v_mfma_f32_16x16x32_bf16 v[124:127], a[56:59], a[24:27], v[124:127]\n" \
"ds_read_b128 a[20:23], v213 offset:6144\n" \
"global_load_lds_dwordx4 v219, s[38:39] offset:-1024\n" \
"v_mfma_f32_16x16x32_bf16 v[124:127], a[60:63], a[28:31], v[124:127]\n" \
"s_add_u32 s38, s38, 0x20000\n" \
"s_addc_u32 s39, s39, 0\n" \
"ds_read_b64_tr_b16 a[46:47], v215 offset:26624\n" \
"v_mfma_f32_16x16x32_bf16 v[120:123], a[48:51], a[24:27], v[120:123]\n" \
"s_sub_u32 s75, s75, 32768\n" \
"s_cselect_b32 s75, 131072, s75\n" \
"v_mfma_f32_16x16x32_bf16 v[120:123], a[52:55], a[28:31], v[120:123]\n" \
"s_waitcnt lgkmcnt(3)\n" \
"s_mov_b32 s72, 6\n" \
"s_call_b64 s[68:69], Lo64_%=\n" \
"v_mfma_f32_16x16x32_bf16 v[64:67], a[32:35], a[0:3], v[64:67]\n" \
"ds_read_b128 a[24:27], v213 offset:4352\n" \
"v_mfma_f32_16x16x32_bf16 v[64:67], a[36:39], a[4:7], v[64:67]\n" \
"s_waitcnt lgkmcnt(1)\n" \
"ds_read_b128 a[28:31], v213 offset:6400\n" \
"v_mfma_f32_16x16x32_bf16 v[68:71], a[40:43], a[0:3], v[68:71]\n" \
"s_sub_u32 s74, s74, 32768\n" \
"s_cselect_b32 s74, 131072, s74\n" \
"ds_read_b64_tr_b16 a[48:49], v214 offset:16896\n" \
"v_mfma_f32_16x16x32_bf16 v[68:71], a[44:47], a[4:7], v[68:71]\n" \
"ds_read_b64_tr_b16 a[50:51], v214 offset:18944\n" \
"v_mfma_f32_16x16x32_bf16 v[84:87], a[40:43], a[8:11], v[84:87]\n" \
"ds_read_b64_tr_b16 a[52:53], v214 offset:25088\n" \
"v_mfma_f32_16x16x32_bf16 v[84:87], a[44:47], a[12:15], v[84:87]\n" \
"ds_read_b64_tr_b16 a[54:55], v214 offset:27136\n" \
"v_mfma_f32_16x16x32_bf16 v[80:83], a[32:35], a[8:11], v[80:83]\n" \
"ds_read_b64_tr_b16 a[56:57], v215 offset:16896\n" \
"v_mfma_f32_16x16x32_bf16 v[80:83], a[36:39], a[12:15], v[80:83]\n" \
"ds_read_b64_tr_b16 a[58:59], v215 offset:18944\n" \
"v_mfma_f32_16x16x32_bf16 v[96:99], a[32:35], a[16:19], v[96:99]\n" \
"ds_read_b64_tr_b16 a[60:61], v215 offset:25088\n" \
"v_mfma_f32_16x16x32_bf16 v[96:99], a[36:39], a[20:23], v[96:99]\n" \
"ds_read_b64_tr_b16 a[62:63], v215 offset:27136\n" \
"v_mfma_f32_16x16x32_bf16 v[100:103], a[40:43], a[16:19], v[100:103]\n" \
"v_add_u32 v213, s74, v211\n" \
"v_add_u32 v214, s74, v212\n" \
"v_mfma_f32_16x16x32_bf16 v[100:103], a[44:47], a[20:23], v[100:103]\n" \
"s_waitcnt lgkmcnt(3)\n" \
"v_xor_b32 v215, 32, v214\n" \
"v_mfma_f32_16x16x32_bf16 v[116:119], a[40:43], a[24:27], v[116:119]\n" \
"v_mfma_f32_16x16x32_bf16 v[116:119], a[44:47], a[28:31], v[116:119]\n" \
"s_waitcnt vmcnt(16) lgkmcnt(0)\n" \
"v_mfma_f32_16x16x32_bf16 v[112:115], a[32:35], a[24:27], v[112:115]\n" \
"v_mfma_f32_16x16x32_bf16 v[112:115], a[36:39], a[28:31], v[112:115]\n" \
"s_barrier\n" \
"v_mfma_f32_16x16x32_bf16 v[72:75], a[48:51], a[0:3], v[72:75]\n" \
"s_lshl_b32 s83, s95, 1\n" \
"s_add_u32 s83, s83, s74\n" \
"ds_read_b64_tr_b16 a[32:33], v214 offset:16384\n" \
"v_mfma_f32_16x16x32_bf16 v[72:75], a[52:55], a[4:7], v[72:75]\n" \
"ds_read_b64_tr_b16 a[34:35], v214 offset:18432\n" \
"v_mfma_f32_16x16x32_bf16 v[76:79], a[56:59], a[0:3], v[76:79]\n" \
"ds_read_b64_tr_b16 a[36:37], v214 offset:24576\n" \
"v_mfma_f32_16x16x32_bf16 v[76:79], a[60:63], a[4:7], v[76:79]\n" \
"ds_read_b128 a[0:3], v213 offset:0\n" \
"v_mfma_f32_16x16x32_bf16 v[92:95], a[56:59], a[8:11], v[92:95]\n" \
"ds_read_b128 a[4:7], v213 offset:2048\n" \
"v_mfma_f32_16x16x32_bf16 v[92:95], a[60:63], a[12:15], v[92:95]\n" \
"ds_read_b64_tr_b16 a[38:39], v214 offset:26624\n" \
"v_mfma_f32_16x16x32_bf16 v[88:91], a[48:51], a[8:11], v[88:91]\n" \
"ds_read_b64_tr_b16 a[40:41], v215 offset:16384\n" \
"v_mfma_f32_16x16x32_bf16 v[88:91], a[52:55], a[12:15], v[88:91]\n" \
"ds_read_b128 a[8:11], v213 offset:256\n" \
"v_mfma_f32_16x16x32_bf16 v[104:107], a[48:51], a[16:19], v[104:107]\n" \
"ds_read_b128 a[12:15], v213 offset:2304\n" \
"v_mfma_f32_16x16x32_bf16 v[104:107], a[52:55], a[20:23], v[104:107]\n" \
"ds_read_b64_tr_b16 a[42:43], v215 offset:18432\n" \
"v_mfma_f32_16x16x32_bf16 v[108:111], a[56:59], a[16:19], v[108:111]\n" \
"ds_read_b64_tr_b16 a[44:45], v215 offset:24576\n" \
"v_mfma_f32_16x16x32_bf16 v[108:111], a[60:63], a[20:23], v[108:111]\n" \
"ds_read_b128 a[16:19], v213 offset:4096\n" \
"v_mfma_f32_16x16x32_bf16 v[124:127], a[56:59], a[24:27], v[124:127]\n" \
"ds_read_b128 a[20:23], v213 offset:6144\n" \
"v_mfma_f32_16x16x32_bf16 v[124:127], a[60:63], a[28:31], v[124:127]\n" \
"ds_read_b64_tr_b16 a[46:47], v215 offset:26624\n" \
"v_mfma_f32_16x16x32_bf16 v[120:123], a[48:51], a[24:27], v[120:123]\n" \
"v_mfma_f32_16x16x32_bf16 v[120:123], a[52:55], a[28:31], v[120:123]\n" \
"s_waitcnt lgkmcnt(3)\n" \
"v_mfma_f32_16x16x32_bf16 v[64:67], a[32:35], a[0:3], v[64:67]\n" \
"ds_read_b128 a[24:27], v213 offset:4352\n" \
"v_mfma_f32_16x16x32_bf16 v[64:67], a[36:39], a[4:7], v[64:67]\n" \
"s_waitcnt lgkmcnt(1)\n" \
"ds_read_b128 a[28:31], v213 offset:6400\n" \
"v_lshrrev_b32 v132, 4, v248\n" \
"v_mfma_f32_16x16x32_bf16 v[68:71], a[40:43], a[0:3], v[68:71]\n" \
"s_sub_u32 s74, s74, 32768\n" \
"s_cselect_b32 s74, 131072, s74\n" \
"ds_read_b64_tr_b16 a[48:49], v214 offset:16896\n" \
"v_mfma_f32_16x16x32_bf16 v[68:71], a[44:47], a[4:7], v[68:71]\n" \
"ds_read_b64_tr_b16 a[50:51], v214 offset:18944\n" \
"v_and_b32 v133, 15, v132\n" \
"v_mfma_f32_16x16x32_bf16 v[84:87], a[40:43], a[8:11], v[84:87]\n" \
"ds_read_b64_tr_b16 a[52:53], v214 offset:25088\n" \
"v_mul_u32_u24 v128, 144, v133\n" \
"v_mfma_f32_16x16x32_bf16 v[84:87], a[44:47], a[12:15], v[84:87]\n" \
"ds_read_b64_tr_b16 a[54:55], v214 offset:27136\n" \
"v_lshrrev_b32 v133, 4, v132\n" \
"v_mfma_f32_16x16x32_bf16 v[80:83], a[32:35], a[8:11], v[80:83]\n" \
"ds_read_b64_tr_b16 a[56:57], v215 offset:16896\n" \
"v_lshl_add_u32 v128, v133, 3, v128\n" \
"v_mfma_f32_16x16x32_bf16 v[80:83], a[36:39], a[12:15], v[80:83]\n" \
"ds_read_b64_tr_b16 a[58:59], v215 offset:18944\n" \
"v_add_u32 v128, s83, v128\n" \
"v_mfma_f32_16x16x32_bf16 v[96:99], a[32:35], a[16:19], v[96:99]\n" \
"ds_read_b64_tr_b16 a[60:61], v215 offset:25088\n" \
"v_lshrrev_b32 v133, 3, v132\n" \
"v_mfma_f32_16x16x32_bf16 v[96:99], a[36:39], a[20:23], v[96:99]\n" \
"ds_read_b64_tr_b16 a[62:63], v215 offset:27136\n" \
"v_and_b32 v134, 7, v132\n" \
"v_mfma_f32_16x16x32_bf16 v[100:103], a[40:43], a[16:19], v[100:103]\n" \
"v_add_u32 v213, s74, v211\n" \
"v_add_u32 v214, s74, v212\n" \
"v_mfma_f32_16x16x32_bf16 v[100:103], a[44:47], a[20:23], v[100:103]\n" \
"s_waitcnt lgkmcnt(3)\n" \
"v_xor_b32 v215, 32, v214\n" \
"v_mul_u32_u24 v129, 144, v133\n" \
"v_mfma_f32_16x16x32_bf16 v[116:119], a[40:43], a[24:27], v[116:119]\n" \
"v_lshl_add_u32 v129, v134, 4, v129\n" \
"v_add_u32 v129, s83, v129\n" \
"v_mfma_f32_16x16x32_bf16 v[116:119], a[44:47], a[28:31], v[116:119]\n" \
"s_waitcnt vmcnt(0) lgkmcnt(0)\n" \
"v_lshlrev_b32 v134, 4, v134\n" \
"v_lshl_add_u32 v130, v133, 13, v134\n" \
"v_mfma_f32_16x16x32_bf16 v[112:115], a[32:35], a[24:27], v[112:115]\n" \
"v_mfma_f32_16x16x32_bf16 v[112:115], a[36:39], a[28:31], v[112:115]\n" \
"s_barrier\n" \
"v_mfma_f32_16x16x32_bf16 v[72:75], a[48:51], a[0:3], v[72:75]\n" \
"ds_read_b128 a[192:195], v213 offset:0\n" \
"ds_read_b64_tr_b16 a[128:129], v214 offset:16384\n" \
"v_mfma_f32_16x16x32_bf16 v[72:75], a[52:55], a[4:7], v[72:75]\n" \
"s_sub_u32 s74, s74, 32768\n" \
"s_cselect_b32 s74, 131072, s74\n" \
"ds_read_b64_tr_b16 a[130:131], v214 offset:18432\n" \
"v_mfma_f32_16x16x32_bf16 v[76:79], a[56:59], a[0:3], v[76:79]\n" \
"ds_read_b128 a[196:199], v213 offset:2048\n" \
"Lpg13127_%=:\n" \
"s_cbranch_execz Lcb1_%=\n" \
"ds_read_b64_tr_b16 a[132:133], v214 offset:24576\n" \
"v_mfma_f32_16x16x32_bf16 v[76:79], a[60:63], a[4:7], v[76:79]\n" \
"ds_read_b64_tr_b16 a[134:135], v214 offset:26624\n" \
"v_add_u32 v226, s74, v211\n" \
"v_mfma_f32_16x16x32_bf16 v[92:95], a[56:59], a[8:11], v[92:95]\n" \
"ds_read_b128 a[200:203], v213 offset:256\n" \
"ds_read_b64_tr_b16 a[136:137], v215 offset:16384\n" \
"v_mfma_f32_16x16x32_bf16 v[92:95], a[60:63], a[12:15], v[92:95]\n" \
"ds_read_b64_tr_b16 a[138:139], v215 offset:18432\n" \
"v_add_u32 v227, s74, v212\n" \
"v_mfma_f32_16x16x32_bf16 v[88:91], a[48:51], a[8:11], v[88:91]\n" \
"ds_read_b128 a[204:207], v213 offset:2304\n" \
"ds_read_b64_tr_b16 a[140:141], v215 offset:24576\n" \
"v_mfma_f32_16x16x32_bf16 v[88:91], a[52:55], a[12:15], v[88:91]\n" \
"ds_read_b64_tr_b16 a[142:143], v215 offset:26624\n" \
"v_xor_b32 v228, 32, v227\n" \
"v_mfma_f32_16x16x32_bf16 v[104:107], a[48:51], a[16:19], v[104:107]\n" \
"ds_read_b64_tr_b16 a[144:145], v214 offset:16896\n" \
"v_mfma_f32_16x16x32_bf16 v[104:107], a[52:55], a[20:23], v[104:107]\n" \
"ds_read_b64_tr_b16 a[146:147], v214 offset:18944\n" \
"v_mfma_f32_16x16x32_bf16 v[108:111], a[56:59], a[16:19], v[108:111]\n" \
"ds_read_b64_tr_b16 a[148:149], v214 offset:25088\n" \
"v_mfma_f32_16x16x32_bf16 v[108:111], a[60:63], a[20:23], v[108:111]\n" \
"ds_read_b64_tr_b16 a[150:151], v214 offset:27136\n" \
"v_mfma_f32_16x16x32_bf16 v[124:127], a[56:59], a[24:27], v[124:127]\n" \
"ds_read_b64_tr_b16 a[152:153], v215 offset:16896\n" \
"v_mfma_f32_16x16x32_bf16 v[124:127], a[60:63], a[28:31], v[124:127]\n" \
"ds_read_b64_tr_b16 a[154:155], v215 offset:18944\n" \
"v_mfma_f32_16x16x32_bf16 v[120:123], a[48:51], a[24:27], v[120:123]\n" \
"ds_read_b64_tr_b16 a[156:157], v215 offset:25088\n" \
"v_mfma_f32_16x16x32_bf16 v[120:123], a[52:55], a[28:31], v[120:123]\n" \
"s_waitcnt lgkmcnt(4)\n" \
"ds_read_b64_tr_b16 a[158:159], v215 offset:27136\n" \
"v_mfma_f32_16x16x32_bf16 v[64:67], a[128:131], a[192:195], v[64:67]\n" \
"ds_read_b128 a[224:227], v226 offset:0\n" \
"ds_read_b64_tr_b16 a[160:161], v227 offset:16384\n" \
"v_mfma_f32_16x16x32_bf16 v[64:67], a[132:135], a[196:199], v[64:67]\n" \
"ds_read_b64_tr_b16 a[162:163], v227 offset:18432\n" \
"v_mfma_f32_16x16x32_bf16 v[80:83], a[128:131], a[200:203], v[80:83]\n" \
"ds_read_b64_tr_b16 a[164:165], v227 offset:24576\n" \
"v_mfma_f32_16x16x32_bf16 v[80:83], a[132:135], a[204:207], v[80:83]\n" \
"ds_read_b128 a[228:231], v226 offset:2048\n" \
"ds_read_b64_tr_b16 a[166:167], v227 offset:26624\n" \
"v_mfma_f32_16x16x32_bf16 v[84:87], a[136:139], a[200:203], v[84:87]\n" \
"ds_read_b64_tr_b16 a[168:169], v228 offset:16384\n" \
"v_mfma_f32_16x16x32_bf16 v[84:87], a[140:143], a[204:207], v[84:87]\n" \
"ds_read_b64_tr_b16 a[170:171], v228 offset:18432\n" \
"v_mfma_f32_16x16x32_bf16 v[68:71], a[136:139], a[192:195], v[68:71]\n" \
"ds_read_b128 a[232:235], v226 offset:256\n" \
"ds_read_b64_tr_b16 a[172:173], v228 offset:24576\n" \
"v_mfma_f32_16x16x32_bf16 v[68:71], a[140:143], a[196:199], v[68:71]\n" \
"s_waitcnt lgkmcnt(6)\n" \
"ds_read_b64_tr_b16 a[174:175], v228 offset:26624\n" \
"v_mfma_f32_16x16x32_bf16 v[72:75], a[144:147], a[192:195], v[72:75]\n" \
"ds_read_b64_tr_b16 a[176:177], v227 offset:16896\n" \
"v_mfma_f32_16x16x32_bf16 v[72:75], a[148:151], a[196:199], v[72:75]\n" \
"ds_read_b128 a[236:239], v226 offset:2304\n" \
"ds_read_b64_tr_b16 a[178:179], v227 offset:18944\n" \
"v_mfma_f32_16x16x32_bf16 v[88:91], a[144:147], a[200:203], v[88:91]\n" \
"ds_read_b64_tr_b16 a[180:181], v227 offset:25088\n" \
"v_mfma_f32_16x16x32_bf16 v[88:91], a[148:151], a[204:207], v[88:91]\n" \
"ds_read_b64_tr_b16 a[182:183], v227 offset:27136\n" \
"v_mfma_f32_16x16x32_bf16 v[92:95], a[152:155], a[200:203], v[92:95]\n" \
"ds_read_b64_tr_b16 a[184:185], v228 offset:16896\n" \
"v_mfma_f32_16x16x32_bf16 v[92:95], a[156:159], a[204:207], v[92:95]\n" \
"ds_read_b64_tr_b16 a[186:187], v228 offset:18944\n" \
"v_mfma_f32_16x16x32_bf16 v[76:79], a[152:155], a[192:195], v[76:79]\n" \
"ds_read_b64_tr_b16 a[188:189], v228 offset:25088\n" \
"v_mfma_f32_16x16x32_bf16 v[76:79], a[156:159], a[196:199], v[76:79]\n" \
"s_waitcnt lgkmcnt(4)\n" \
"ds_read_b64_tr_b16 a[190:191], v228 offset:27136\n" \
"v_mfma_f32_16x16x32_bf16 v[64:67], a[160:163], a[224:227], v[64:67]\n" \
"ds_read_b128 a[208:211], v213 offset:4096\n" \
"v_mfma_f32_16x16x32_bf16 v[64:67], a[164:167], a[228:231], v[64:67]\n" \
"ds_read_b128 a[212:215], v213 offset:6144\n" \
"v_mfma_f32_16x16x32_bf16 v[80:83], a[160:163], a[232:235], v[80:83]\n" \
"ds_read_b128 a[240:243], v226 offset:4096\n" \
"v_mfma_f32_16x16x32_bf16 v[80:83], a[164:167], a[236:239], v[80:83]\n" \
"ds_read_b128 a[244:247], v226 offset:6144\n" \
"v_mfma_f32_16x16x32_bf16 v[84:87], a[168:171], a[232:235], v[84:87]\n" \
"ds_read_b128 a[216:219], v213 offset:4352\n" \
"v_cvt_pk_bf16_f32 v64, v64, v65\n" \
"v_mfma_f32_16x16x32_bf16 v[84:87], a[172:175], a[236:239], v[84:87]\n" \
"ds_read_b128 a[220:223], v213 offset:6400\n" \
"v_cvt_pk_bf16_f32 v65, v66, v67\n" \
"v_mfma_f32_16x16x32_bf16 v[68:71], a[168:171], a[224:227], v[68:71]\n" \
"ds_read_b128 a[248:251], v226 offset:4352\n" \
"v_mfma_f32_16x16x32_bf16 v[68:71], a[172:175], a[228:231], v[68:71]\n" \
"s_waitcnt lgkmcnt(4)\n" \
"ds_read_b128 a[252:255], v226 offset:6400\n" \
"v_mfma_f32_16x16x32_bf16 v[72:75], a[176:179], a[224:227], v[72:75]\n" \
"ds_write_b64 v128, v[64:65] offset:0\n" \
"v_mfma_f32_16x16x32_bf16 v[72:75], a[180:183], a[228:231], v[72:75]\n" \
"v_cvt_pk_bf16_f32 v80, v80, v81\n" \
"v_cvt_pk_bf16_f32 v81, v82, v83\n" \
"v_mfma_f32_16x16x32_bf16 v[88:91], a[176:179], a[232:235], v[88:91]\n" \
"ds_write_b64 v128, v[80:81] offset:2304\n" \
"v_mfma_f32_16x16x32_bf16 v[88:91], a[180:183], a[236:239], v[88:91]\n" \
"v_cvt_pk_bf16_f32 v84, v84, v85\n" \
"v_cvt_pk_bf16_f32 v85, v86, v87\n" \
"v_mfma_f32_16x16x32_bf16 v[92:95], a[184:187], a[232:235], v[92:95]\n" \
"ds_write_b64 v128, v[84:85] offset:2336\n" \
"v_mfma_f32_16x16x32_bf16 v[92:95], a[188:191], a[236:239], v[92:95]\n" \
"v_cvt_pk_bf16_f32 v68, v68, v69\n" \
"v_cvt_pk_bf16_f32 v69, v70, v71\n" \
"v_mfma_f32_16x16x32_bf16 v[76:79], a[184:187], a[224:227], v[76:79]\n" \
"ds_write_b64 v128, v[68:69] offset:32\n" \
"v_mfma_f32_16x16x32_bf16 v[76:79], a[188:191], a[228:231], v[76:79]\n" \
"v_cvt_pk_bf16_f32 v72, v72, v73\n" \
"v_cvt_pk_bf16_f32 v73, v74, v75\n" \
"v_mfma_f32_16x16x32_bf16 v[96:99], a[128:131], a[208:211], v[96:99]\n" \
"ds_write_b64 v128, v[72:73] offset:64\n" \
"v_mfma_f32_16x16x32_bf16 v[96:99], a[132:135], a[212:215], v[96:99]\n" \
"s_waitcnt lgkmcnt(2)\n" \
"v_cvt_pk_bf16_f32 v88, v88, v89\n" \
"v_cvt_pk_bf16_f32 v89, v90, v91\n" \
"v_mfma_f32_16x16x32_bf16 v[96:99], a[160:163], a[240:243], v[96:99]\n" \
"ds_write_b64 v128, v[88:89] offset:2368\n" \
"v_mfma_f32_16x16x32_bf16 v[96:99], a[164:167], a[244:247], v[96:99]\n" \
"v_cvt_pk_bf16_f32 v92, v92, v93\n" \
"v_cvt_pk_bf16_f32 v93, v94, v95\n" \
"v_mfma_f32_16x16x32_bf16 v[100:103], a[168:171], a[240:243], v[100:103]\n" \
"ds_write_b64 v128, v[92:93] offset:2400\n" \
"v_mfma_f32_16x16x32_bf16 v[100:103], a[172:175], a[244:247], v[100:103]\n" \
"v_cvt_pk_bf16_f32 v76, v76, v77\n" \
"v_cvt_pk_bf16_f32 v77, v78, v79\n" \
"v_mfma_f32_16x16x32_bf16 v[100:103], a[136:139], a[208:211], v[100:103]\n" \
"ds_write_b64 v128, v[76:77] offset:96\n" \
"v_mfma_f32_16x16x32_bf16 v[100:103], a[140:143], a[212:215], v[100:103]\n" \
"ds_read_b128 v[140:143], v129 offset:0\n" \
"v_mfma_f32_16x16x32_bf16 v[104:107], a[144:147], a[208:211], v[104:107]\n" \
"ds_read_b128 v[144:147], v129 offset:1152\n" \
"v_mfma_f32_16x16x32_bf16 v[104:107], a[148:151], a[212:215], v[104:107]\n" \
"ds_read_b128 v[148:151], v129 offset:2304\n" \
"v_mfma_f32_16x16x32_bf16 v[104:107], a[176:179], a[240:243], v[104:107]\n" \
"ds_read_b128 v[152:155], v129 offset:3456\n" \
"v_mfma_f32_16x16x32_bf16 v[104:107], a[180:183], a[244:247], v[104:107]\n" \
"v_cvt_pk_bf16_f32 v96, v96, v97\n" \
"v_cvt_pk_bf16_f32 v97, v98, v99\n" \
"v_mfma_f32_16x16x32_bf16 v[108:111], a[184:187], a[240:243], v[108:111]\n" \
"ds_write_b64 v128, v[96:97] offset:0\n" \
"v_mfma_f32_16x16x32_bf16 v[108:111], a[188:191], a[244:247], v[108:111]\n" \
"v_cvt_pk_bf16_f32 v100, v100, v101\n" \
"v_cvt_pk_bf16_f32 v101, v102, v103\n" \
"v_mfma_f32_16x16x32_bf16 v[108:111], a[152:155], a[208:211], v[108:111]\n" \
"s_add_u32 s70, s77, 0\n" \
"ds_write_b64 v128, v[100:101] offset:32\n" \
"v_mfma_f32_16x16x32_bf16 v[108:111], a[156:159], a[212:215], v[108:111]\n" \
"s_waitcnt lgkmcnt(5)\n" \
"v_cvt_pk_bf16_f32 v104, v104, v105\n" \
"v_cvt_pk_bf16_f32 v105, v106, v107\n" \
"v_mfma_f32_16x16x32_bf16 v[112:115], a[128:131], a[216:219], v[112:115]\n" \
"buffer_store_dwordx4 v[140:143], v130, s[52:55], s70 offen sc1\n" \
"ds_write_b64 v128, v[104:105] offset:64\n" \
"v_mfma_f32_16x16x32_bf16 v[112:115], a[132:135], a[220:223], v[112:115]\n" \
"v_mfma_f32_16x16x32_bf16 v[112:115], a[160:163], a[248:251], v[112:115]\n" \
"v_cvt_pk_bf16_f32 v108, v108, v109\n" \
"v_cvt_pk_bf16_f32 v109, v110, v111\n" \
"v_mfma_f32_16x16x32_bf16 v[112:115], a[164:167], a[252:255], v[112:115]\n" \
"ds_write_b64 v128, v[108:109] offset:96\n" \
"v_mfma_f32_16x16x32_bf16 v[116:119], a[168:171], a[248:251], v[116:119]\n" \
"s_add_u32 s70, s77, 65536\n" \
"ds_read_b128 v[156:159], v129 offset:0\n" \
"s_waitcnt lgkmcnt(7)\n" \
"v_mfma_f32_16x16x32_bf16 v[116:119], a[172:175], a[252:255], v[116:119]\n" \
"buffer_store_dwordx4 v[144:147], v130, s[52:55], s70 offen sc1\n" \
"ds_read_b128 v[160:163], v129 offset:1152\n" \
"v_mfma_f32_16x16x32_bf16 v[116:119], a[136:139], a[216:219], v[116:119]\n" \
"v_cvt_pk_bf16_f32 v112, v112, v113\n" \
"v_cvt_pk_bf16_f32 v113, v114, v115\n" \
"v_mfma_f32_16x16x32_bf16 v[116:119], a[140:143], a[220:223], v[116:119]\n" \
"ds_write_b64 v128, v[112:113] offset:2304\n" \
"v_mfma_f32_16x16x32_bf16 v[120:123], a[144:147], a[216:219], v[120:123]\n" \
"v_mfma_f32_16x16x32_bf16 v[120:123], a[148:151], a[220:223], v[120:123]\n" \
"s_add_u32 s70, s77, 4096\n" \
"s_waitcnt lgkmcnt(8)\n" \
"v_mfma_f32_16x16x32_bf16 v[120:123], a[176:179], a[248:251], v[120:123]\n" \
"buffer_store_dwordx4 v[148:151], v130, s[52:55], s70 offen sc1\n" \
"v_cvt_pk_bf16_f32 v116, v116, v117\n" \
"v_cvt_pk_bf16_f32 v117, v118, v119\n" \
"v_mfma_f32_16x16x32_bf16 v[120:123], a[180:183], a[252:255], v[120:123]\n" \
"ds_write_b64 v128, v[116:117] offset:2336\n" \
"v_mfma_f32_16x16x32_bf16 v[124:127], a[184:187], a[248:251], v[124:127]\n" \
"v_mfma_f32_16x16x32_bf16 v[124:127], a[188:191], a[252:255], v[124:127]\n" \
"v_mfma_f32_16x16x32_bf16 v[124:127], a[152:155], a[216:219], v[124:127]\n" \
"s_add_u32 s70, s77, 69632\n" \
"v_cvt_pk_bf16_f32 v120, v120, v121\n" \
"v_cvt_pk_bf16_f32 v121, v122, v123\n" \
"v_mfma_f32_16x16x32_bf16 v[124:127], a[156:159], a[220:223], v[124:127]\n" \
"s_waitcnt lgkmcnt(8)\n" \
"ds_write_b64 v128, v[120:121] offset:2368\n" \
"buffer_store_dwordx4 v[152:155], v130, s[52:55], s70 offen sc1\n" \
"s_add_u32 s70, s77, 131072\n" \
"s_waitcnt lgkmcnt(4)\n" \
"s_nop 2\n" \
"v_cvt_pk_bf16_f32 v124, v124, v125\n" \
"v_cvt_pk_bf16_f32 v125, v126, v127\n" \
"ds_write_b64 v128, v[124:125] offset:2400\n" \
"ds_read_b128 v[164:167], v129 offset:2304\n" \
"ds_read_b128 v[168:171], v129 offset:3456\n" \
"buffer_store_dwordx4 v[156:159], v130, s[52:55], s70 offen sc1\n" \
"s_add_u32 s70, s77, 196608\n" \
"s_waitcnt lgkmcnt(6)\n" \
"buffer_store_dwordx4 v[160:163], v130, s[52:55], s70 offen sc1\n" \
"s_add_u32 s70, s77, 135168\n" \
"s_waitcnt lgkmcnt(1)\n" \
"buffer_store_dwordx4 v[164:167], v130, s[52:55], s70 offen sc1\n" \
"s_add_u32 s70, s77, 200704\n" \
"s_waitcnt lgkmcnt(0)\n" \
"buffer_store_dwordx4 v[168:171], v130, s[52:55], s70 offen sc1\n" \
"s_endpgm\n" \
"Lpg13339_%=:\n" \
"s_cbranch_execz Lcb1_%=\n" \
"Lo64_%=:\n" \
"v_mfma_f32_16x16x32_bf16 v[64:67], a[32:35], a[0:3], v[64:67]\n" \
"ds_read_b128 a[24:27], v213 offset:4352\n" \
"s_add_u32 m0, s75, s95\n" \
"v_mfma_f32_16x16x32_bf16 v[64:67], a[36:39], a[4:7], v[64:67]\n" \
"s_waitcnt lgkmcnt(1)\n" \
"ds_read_b128 a[28:31], v213 offset:6400\n" \
"global_load_lds_dwordx4 v222, s[36:37] offset:0 sc1\n" \
"v_mfma_f32_16x16x32_bf16 v[68:71], a[40:43], a[0:3], v[68:71]\n" \
"s_sub_u32 s74, s74, 32768\n" \
"s_cselect_b32 s74, 131072, s74\n" \
"ds_read_b64_tr_b16 a[48:49], v214 offset:16896\n" \
"v_mfma_f32_16x16x32_bf16 v[68:71], a[44:47], a[4:7], v[68:71]\n" \
"ds_read_b64_tr_b16 a[50:51], v214 offset:18944\n" \
"v_mfma_f32_16x16x32_bf16 v[84:87], a[40:43], a[8:11], v[84:87]\n" \
"ds_read_b64_tr_b16 a[52:53], v214 offset:25088\n" \
"v_mfma_f32_16x16x32_bf16 v[84:87], a[44:47], a[12:15], v[84:87]\n" \
"ds_read_b64_tr_b16 a[54:55], v214 offset:27136\n" \
"global_load_lds_dwordx4 v222, s[36:37] offset:1024 sc1\n" \
"v_mfma_f32_16x16x32_bf16 v[80:83], a[32:35], a[8:11], v[80:83]\n" \
"ds_read_b64_tr_b16 a[56:57], v215 offset:16896\n" \
"v_mfma_f32_16x16x32_bf16 v[80:83], a[36:39], a[12:15], v[80:83]\n" \
"ds_read_b64_tr_b16 a[58:59], v215 offset:18944\n" \
"v_mfma_f32_16x16x32_bf16 v[96:99], a[32:35], a[16:19], v[96:99]\n" \
"ds_read_b64_tr_b16 a[60:61], v215 offset:25088\n" \
"v_mfma_f32_16x16x32_bf16 v[96:99], a[36:39], a[20:23], v[96:99]\n" \
"ds_read_b64_tr_b16 a[62:63], v215 offset:27136\n" \
"global_load_lds_dwordx4 v222, s[36:37] offset:2048 sc1\n" \
"v_mfma_f32_16x16x32_bf16 v[100:103], a[40:43], a[16:19], v[100:103]\n" \
"v_add_u32 v213, s74, v211\n" \
"v_add_u32 v214, s74, v212\n" \
"v_mfma_f32_16x16x32_bf16 v[100:103], a[44:47], a[20:23], v[100:103]\n" \
"s_waitcnt lgkmcnt(3)\n" \
"v_xor_b32 v215, 32, v214\n" \
"v_mfma_f32_16x16x32_bf16 v[116:119], a[40:43], a[24:27], v[116:119]\n" \
"v_mfma_f32_16x16x32_bf16 v[116:119], a[44:47], a[28:31], v[116:119]\n" \
"s_waitcnt vmcnt(19) lgkmcnt(0)\n" \
"global_load_lds_dwordx4 v222, s[36:37] offset:3072 sc1\n" \
"v_mfma_f32_16x16x32_bf16 v[112:115], a[32:35], a[24:27], v[112:115]\n" \
"s_add_u32 s36, s36, 0x1000\n" \
"v_mfma_f32_16x16x32_bf16 v[112:115], a[36:39], a[28:31], v[112:115]\n" \
"s_barrier\n" \
"v_mfma_f32_16x16x32_bf16 v[72:75], a[48:51], a[0:3], v[72:75]\n" \
"s_add_u32 m0, m0, 0x5000\n" \
"ds_read_b64_tr_b16 a[32:33], v214 offset:16384\n" \
"v_mfma_f32_16x16x32_bf16 v[72:75], a[52:55], a[4:7], v[72:75]\n" \
"ds_read_b64_tr_b16 a[34:35], v214 offset:18432\n" \
"global_load_lds_dwordx4 v216, s[38:39] offset:-4096\n" \
"v_mfma_f32_16x16x32_bf16 v[76:79], a[56:59], a[0:3], v[76:79]\n" \
"ds_read_b64_tr_b16 a[36:37], v214 offset:24576\n" \
"v_mfma_f32_16x16x32_bf16 v[76:79], a[60:63], a[4:7], v[76:79]\n" \
"ds_read_b128 a[0:3], v213 offset:0\n" \
"v_mfma_f32_16x16x32_bf16 v[92:95], a[56:59], a[8:11], v[92:95]\n" \
"ds_read_b128 a[4:7], v213 offset:2048\n" \
"v_mfma_f32_16x16x32_bf16 v[92:95], a[60:63], a[12:15], v[92:95]\n" \
"ds_read_b64_tr_b16 a[38:39], v214 offset:26624\n" \
"global_load_lds_dwordx4 v217, s[38:39] offset:-3072\n" \
"v_mfma_f32_16x16x32_bf16 v[88:91], a[48:51], a[8:11], v[88:91]\n" \
"ds_read_b64_tr_b16 a[40:41], v215 offset:16384\n" \
"v_mfma_f32_16x16x32_bf16 v[88:91], a[52:55], a[12:15], v[88:91]\n" \
"ds_read_b128 a[8:11], v213 offset:256\n" \
"v_mfma_f32_16x16x32_bf16 v[104:107], a[48:51], a[16:19], v[104:107]\n" \
"ds_read_b128 a[12:15], v213 offset:2304\n" \
"v_mfma_f32_16x16x32_bf16 v[104:107], a[52:55], a[20:23], v[104:107]\n" \
"ds_read_b64_tr_b16 a[42:43], v215 offset:18432\n" \
"global_load_lds_dwordx4 v218, s[38:39] offset:-2048\n" \
"v_mfma_f32_16x16x32_bf16 v[108:111], a[56:59], a[16:19], v[108:111]\n" \
"ds_read_b64_tr_b16 a[44:45], v215 offset:24576\n" \
"v_mfma_f32_16x16x32_bf16 v[108:111], a[60:63], a[20:23], v[108:111]\n" \
"ds_read_b128 a[16:19], v213 offset:4096\n" \
"global_load_lds_dwordx4 v219, s[38:39] offset:-1024\n" \
"v_mfma_f32_16x16x32_bf16 v[124:127], a[56:59], a[24:27], v[124:127]\n" \
"s_add_u32 s38, s38, 0x20000\n" \
"s_addc_u32 s39, s39, 0\n" \
"ds_read_b128 a[20:23], v213 offset:6144\n" \
"v_mfma_f32_16x16x32_bf16 v[124:127], a[60:63], a[28:31], v[124:127]\n" \
"s_sub_u32 s75, s75, 32768\n" \
"s_cselect_b32 s75, 131072, s75\n" \
"ds_read_b64_tr_b16 a[46:47], v215 offset:26624\n" \
"v_mfma_f32_16x16x32_bf16 v[120:123], a[48:51], a[24:27], v[120:123]\n" \
"s_sub_u32 s72, s72, 1\n" \
"s_cmp_lg_u32 s72, 0\n" \
"v_mfma_f32_16x16x32_bf16 v[120:123], a[52:55], a[28:31], v[120:123]\n" \
"s_waitcnt lgkmcnt(3)\n" \
"s_cbranch_scc1 Lo64_%=\n" \
"s_setpc_b64 s[68:69]\n" \
"Lhot_end_%=:\n" \
"Lmask0_%=:\n" \
"Lpg13428_%=:\n" \
"s_cbranch_execz Lcb0_%=\n" \
"s_waitcnt lgkmcnt(0)\n" \
"v_subrev_u32 v0, 0, v251\n" \
"v_subrev_u32 v1, 1, v251\n" \
"v_subrev_u32 v2, 2, v251\n" \
"v_subrev_u32 v3, 3, v251\n" \
"v_subrev_u32 v4, 16, v251\n" \
"v_subrev_u32 v5, 17, v251\n" \
"v_subrev_u32 v6, 18, v251\n" \
"v_subrev_u32 v7, 19, v251\n" \
"v_subrev_u32 v32, 32, v251\n" \
"v_subrev_u32 v33, 33, v251\n" \
"v_subrev_u32 v34, 34, v251\n" \
"v_subrev_u32 v35, 35, v251\n" \
"v_subrev_u32 v36, 48, v251\n" \
"v_subrev_u32 v37, 49, v251\n" \
"v_subrev_u32 v38, 50, v251\n" \
"v_subrev_u32 v39, 51, v251\n" \
"v_ashrrev_i32 v0, 31, v0\n" \
"v_ashrrev_i32 v1, 31, v1\n" \
"v_ashrrev_i32 v2, 31, v2\n" \
"v_ashrrev_i32 v3, 31, v3\n" \
"v_ashrrev_i32 v4, 31, v4\n" \
"v_ashrrev_i32 v5, 31, v5\n" \
"v_ashrrev_i32 v6, 31, v6\n" \
"v_ashrrev_i32 v7, 31, v7\n" \
"v_ashrrev_i32 v32, 31, v32\n" \
"v_ashrrev_i32 v33, 31, v33\n" \
"v_ashrrev_i32 v34, 31, v34\n" \
"v_ashrrev_i32 v35, 31, v35\n" \
"v_ashrrev_i32 v36, 31, v36\n" \
"v_ashrrev_i32 v37, 31, v37\n" \
"v_ashrrev_i32 v38, 31, v38\n" \
"v_ashrrev_i32 v39, 31, v39\n" \
"v_and_b32 v0, 0xff800000, v0\n" \
"v_and_b32 v1, 0xff800000, v1\n" \
"v_and_b32 v2, 0xff800000, v2\n" \
"v_and_b32 v3, 0xff800000, v3\n" \
"v_and_b32 v4, 0xff800000, v4\n" \
"v_and_b32 v5, 0xff800000, v5\n" \
"v_and_b32 v6, 0xff800000, v6\n" \
"v_and_b32 v7, 0xff800000, v7\n" \
"v_and_b32 v32, 0xff800000, v32\n" \
"v_and_b32 v33, 0xff800000, v33\n" \
"v_and_b32 v34, 0xff800000, v34\n" \
"v_and_b32 v35, 0xff800000, v35\n" \
"v_and_b32 v36, 0xff800000, v36\n" \
"v_and_b32 v37, 0xff800000, v37\n" \
"v_and_b32 v38, 0xff800000, v38\n" \
"v_and_b32 v39, 0xff800000, v39\n" \
"s_branch Lmask0r_%=\n" \
"Lpl1_%=:\n" \
"s_mov_b32 s31, 0\n" \
"Lpk1_%=:\n" \
"s_sleep 2\n" \
"buffer_load_dword v244, v245, s[56:59], s91 offen sc1\n" \
"s_waitcnt vmcnt(0)\n" \
"v_cmp_ne_u32 vcc, s60, v244\n" \
"s_nop 1\n" \
"s_and_b32 vcc_hi, vcc_lo, 255\n" \
"s_cbranch_scc0 Lpr1_%=\n" \
"s_add_u32 s31, s31, 1\n" \
"s_cmp_lt_u32 s31, 0x40000\n" \
"s_cbranch_scc1 Lpk1_%=\n" \
"s_branch Lpr1_%=\n" \
"Lrs0_%=:\n" \
"s_call_b64 s[68:69], Lrest_%=\n" \
"s_branch Lrr0_%=\n" \
"Lrest_%=:\n" \
"s_mov_b32 s31, 0\n" \
"s_waitcnt vmcnt(0)\n" \
"Lrk_%=:\n" \
"v_lshrrev_b32 v253, 2, v248\n" \
"s_sleep 2\n" \
"buffer_load_dword v253, v253, s[56:59], s79 offen sc1\n" \
"s_waitcnt vmcnt(0)\n" \
"v_cmp_ne_u32 s[52:53], s60, v253\n" \
"s_add_u32 s31, s31, 1\n" \
"s_cmp_lt_u32 s31, 0x40000\n" \
"s_cselect_b32 s54, 0xff, 0\n" \
"s_and_b32 s52, s52, s54\n" \
"s_cmp_lg_u32 s52, 0\n" \
"s_cbranch_scc1 Lrk_%=\n" \
"s_mov_b64 s[10:11], 0\n" \
"s_setpc_b64 s[68:69]\n" \
"Lrs1_%=:\n" \
"s_call_b64 s[68:69], Lrest_%=\n" \
"s_branch Lrr1_%=\n" \
"Lrs2_%=:\n" \
"s_call_b64 s[68:69], Lrest_%=\n" \
"s_branch Lrr2_%=\n" \
"Lrs3_%=:\n" \
"s_call_b64 s[68:69], Lrest_%=\n" \
"s_branch Lrr3_%=\n" \
"LF7_%=:\n" \
"v_mfma_f32_16x16x32_bf16 v[24:27], a[192:195], a[176:179], v[236:239]\n" \
"s_waitcnt vmcnt(13)\n" \
"ds_read_b128 v[192:195], v246 offset:16384\n" \
"s_bfe_u32 s88, %1, 0x20001\n" \
"v_mfma_f32_16x16x32_bf16 v[24:27], a[196:199], a[180:183], v[24:27]\n" \
"s_lshl_b32 s88, s88, 2\n" \
"ds_read_b128 v[196:199], v246 offset:18432\n" \
"v_cvt_pk_bf16_f32 v91, v94, v95\n" \
"v_mfma_f32_16x16x32_bf16 v[24:27], a[200:203], a[184:187], v[24:27]\n" \
"s_lshr_b32 s70, %1, 6\n" \
"ds_read_b128 v[200:203], v246 offset:20480\n" \
"v_mov_b32 v66, v242\n" \
"v_mov_b32 v64, v66\n" \
"v_exp_f32 v104, v104\n" \
"v_exp_f32 v105, v105\n" \
"v_exp_f32 v106, v106\n" \
"v_mfma_f32_16x16x32_bf16 v[24:27], a[204:207], a[188:191], v[24:27]\n" \
"s_add_u32 s88, s88, s70\n" \
"ds_read_b128 a[252:255], v246 offset:14592\n" \
"v_bfe_u32 v69, v248, 8, 1\n" \
"v_bfe_u32 v70, v248, 6, 2\n" \
"v_mfma_f32_16x16x32_bf16 v[16:19], a[200:203], a[168:171], v[232:235]\n" \
"s_lshr_b32 s71, s22, 7\n" \
"ds_read_b128 v[204:207], v246 offset:22528\n" \
"v_cvt_pk_bf16_f32 v104, v104, v105\n" \
"v_mfma_f32_16x16x32_bf16 v[16:19], a[204:207], a[172:175], v[16:19]\n" \
"s_sub_u32 s71, s63, s71\n" \
"ds_read_b128 v[208:211], v246 offset:16640\n" \
"v_permlane16_swap_b32 v64, v66\n" \
"v_add_f32 v66, v66, v64\n" \
"v_mov_b32 v64, v66\n" \
"v_exp_f32 v107, v107\n" \
"v_exp_f32 v108, v108\n" \
"v_mfma_f32_16x16x32_bf16 v[16:19], a[192:195], a[160:163], v[16:19]\n" \
"s_add_u32 s71, s71, 6\n" \
"ds_read_b128 v[212:215], v246 offset:18688\n" \
"v_cvt_pk_bf16_f32 v105, v106, v107\n" \
"v_mfma_f32_16x16x32_bf16 v[16:19], a[196:199], a[164:167], v[16:19]\n" \
"s_and_b32 s71, s71, 7\n" \
"ds_read_b128 v[216:219], v246 offset:20736\n" \
"v_lshl_add_u32 v70, v69, 3, v70\n" \
"v_mul_u32_u24 v70, 6144, v70\n" \
"v_permlane32_swap_b32 v64, v66\n" \
"v_add_f32 v66, v66, v64\n" \
"v_rcp_f32 v64, v66\n" \
"v_mul_f32 v66, 0x4d800000, v66\n" \
"v_add_u32 v65, 0x1c90080, v249\n" \
"v_cndmask_b32 v65, v249, v65, s[16:17]\n" \
"v_accvgpr_read_b32 v96, a0\n" \
"v_accvgpr_read_b32 v97, a1\n" \
"v_accvgpr_read_b32 v98, a2\n" \
"v_accvgpr_read_b32 v99, a3\n" \
"v_accvgpr_read_b32 v100, a4\n" \
"v_accvgpr_read_b32 v101, a5\n" \
"v_accvgpr_read_b32 v102, a6\n" \
"v_accvgpr_read_b32 v103, a7\n" \
"v_exp_f32 v109, v109\n" \
"v_exp_f32 v110, v110\n" \
"v_pk_mul_f32 v[96:97], v[96:97], v[64:65] op_sel_hi:[1,0]\n" \
"v_pk_mul_f32 v[98:99], v[98:99], v[64:65] op_sel_hi:[1,0]\n" \
"v_pk_mul_f32 v[100:101], v[100:101], v[64:65] op_sel_hi:[1,0]\n" \
"v_pk_mul_f32 v[102:103], v[102:103], v[64:65] op_sel_hi:[1,0]\n" \
"v_cvt_pk_bf16_f32 v224, v96, v97\n" \
"v_cvt_pk_bf16_f32 v225, v98, v99\n" \
"v_mfma_f32_16x16x32_bf16 v[20:23], a[208:211], a[160:163], v[232:235]\n" \
"s_and_b32 s70, %1, 1\n" \
"ds_read_b128 v[220:223], v246 offset:22784\n" \
"s_lshl_b32 s70, s70, 3\n" \
"v_mfma_f32_16x16x32_bf16 v[20:23], a[212:215], a[164:167], v[20:23]\n" \
"s_add_u32 s70, s70, s71\n" \
"ds_read_b128 a[192:195], v247 offset:16384\n" \
"v_cvt_pk_bf16_f32 v226, v100, v101\n" \
"v_cvt_pk_bf16_f32 v227, v102, v103\n" \
"v_lshlrev_b32 v69, 5, v69\n" \
"v_and_b32 v71, 0x30, v248\n" \
"v_pk_mul_f32 v[96:97], v[96:97], v[98:99]\n" \
"v_pk_fma_f32 v[96:97], v[100:101], v[102:103], v[96:97]\n" \
"v_fma_f32 v66, v96, v97, v66\n" \
"v_mfma_f32_16x16x32_bf16 v[20:23], a[216:219], a[168:171], v[20:23]\n" \
"s_lshl_b32 s87, s70, 12\n" \
"ds_read_b128 a[196:199], v247 offset:24576\n" \
"v_cvt_pk_bf16_f32 v106, v108, v109\n" \
"v_mfma_f32_16x16x32_bf16 v[20:23], a[220:223], a[172:175], v[20:23]\n" \
"s_lshl_b32 s70, s70, 7\n" \
"ds_read_b128 a[200:203], v247 offset:17408\n" \
"buffer_store_dwordx4 v[224:227], v65, s[56:59], s77 offen offset:0 sc1\n" \
"v_exp_f32 v111, v111\n" \
"v_exp_f32 v112, v112\n" \
"v_accvgpr_read_b32 v96, a8\n" \
"v_accvgpr_read_b32 v97, a9\n" \
"v_mfma_f32_16x16x32_bf16 v[28:31], a[216:219], a[184:187], v[236:239]\n" \
"s_mul_i32 s84, s61, 6144\n" \
"ds_read_b128 a[204:207], v247 offset:25600\n" \
"v_cvt_pk_bf16_f32 v107, v110, v111\n" \
"v_mfma_f32_16x16x32_bf16 v[28:31], a[220:223], a[188:191], v[28:31]\n" \
"s_lshl_b32 s71, s84, 4\n" \
"v_xor_b32 v69, v69, v71\n" \
"v_lshrrev_b32 v71, 9, v248\n" \
"v_mfma_f32_16x16x32_bf16 v[28:31], a[208:211], a[176:179], v[28:31]\n" \
"s_add_u32 s70, s70, s71\n" \
"s_add_u32 s70, s70, 4096\n" \
"v_lshl_add_u32 v70, v71, 6, v70\n" \
"v_mfma_f32_16x16x32_bf16 v[28:31], a[212:215], a[180:183], v[28:31]\n" \
"s_waitcnt lgkmcnt(2)\n" \
"ds_read_b128 a[208:211], v247 offset:18432\n" \
"v_add_u32 v253, v69, v70\n" \
"v_accvgpr_read_b32 v98, a10\n" \
"v_accvgpr_read_b32 v99, a11\n" \
"v_accvgpr_read_b32 v100, a12\n" \
"v_accvgpr_read_b32 v101, a13\n" \
"v_accvgpr_read_b32 v102, a14\n" \
"v_accvgpr_read_b32 v103, a15\n" \
"v_exp_f32 v113, v113\n" \
"v_exp_f32 v114, v114\n" \
"v_pk_mul_f32 v[96:97], v[96:97], v[64:65] op_sel_hi:[1,0]\n" \
"v_pk_mul_f32 v[98:99], v[98:99], v[64:65] op_sel_hi:[1,0]\n" \
"v_pk_mul_f32 v[100:101], v[100:101], v[64:65] op_sel_hi:[1,0]\n" \
"v_pk_mul_f32 v[102:103], v[102:103], v[64:65] op_sel_hi:[1,0]\n" \
"v_cvt_pk_bf16_f32 v228, v96, v97\n" \
"v_cvt_pk_bf16_f32 v229, v98, v99\n" \
"v_cvt_pk_bf16_f32 v230, v100, v101\n" \
"v_cvt_pk_bf16_f32 v231, v102, v103\n" \
"v_pk_mul_f32 v[96:97], v[96:97], v[98:99]\n" \
"v_pk_fma_f32 v[96:97], v[100:101], v[102:103], v[96:97]\n" \
"v_fma_f32 v66, v96, v97, v66\n" \
"v_mfma_f32_16x16x32_bf16 v[8:11], v[192:195], a[176:179], v[236:239]\n" \
"s_add_u32 s48, s22, s70\n" \
"s_addc_u32 s49, s23, 0\n" \
"ds_read_b128 a[212:215], v247 offset:26624\n" \
"v_mfma_f32_16x16x32_bf16 v[8:11], v[196:199], a[180:183], v[8:11]\n" \
"s_add_u32 s50, s48, 23552\n" \
"s_addc_u32 s51, s49, 0\n" \
"ds_read_b128 a[216:219], v247 offset:19456\n" \
"v_mfma_f32_16x16x32_bf16 v[8:11], v[200:203], a[184:187], v[8:11]\n" \
"s_lshr_b32 s89, s22, 7\n" \
"ds_read_b128 a[220:223], v247 offset:27648\n" \
"v_cvt_pk_bf16_f32 v112, v112, v113\n" \
"v_mfma_f32_16x16x32_bf16 v[8:11], v[204:207], a[188:191], v[8:11]\n" \
"s_sub_u32 s89, 3, s89\n" \
"ds_read_b128 v[192:195], v246 offset:24576\n" \
"v_bfe_u32 v84, v248, 7, 2\n" \
"s_and_b32 s89, s89, 7\n" \
"v_mfma_f32_16x16x32_bf16 v[56:59], a[232:235], a[184:187], v[236:239]\n" \
"s_and_b32 s90, %1, 1\n" \
"ds_read_b128 v[196:199], v246 offset:26624\n" \
"v_bfe_u32 v85, v248, 5, 2\n" \
"v_mfma_f32_16x16x32_bf16 v[56:59], a[236:239], a[188:191], v[56:59]\n" \
"s_lshl_b32 s90, s90, 3\n" \
"ds_read_b128 v[200:203], v246 offset:28672\n" \
"v_exp_f32 v115, v115\n" \
"v_mfma_f32_16x16x32_bf16 v[56:59], a[224:227], a[176:179], v[56:59]\n" \
"s_add_u32 s89, s89, s90\n" \
"ds_read_b128 v[204:207], v246 offset:30720\n" \
"v_min_u32 v85, 2, v85\n" \
"v_mfma_f32_16x16x32_bf16 v[56:59], a[228:231], a[180:183], v[56:59]\n" \
"s_lshl_b32 s89, s89, 7\n" \
"s_lshr_b32 s90, %1, 3\n" \
"v_mad_u32_u24 v84, v84, 3, v85\n" \
"s_lshl_b32 s90, s90, 2\n" \
"v_mfma_f32_16x16x32_bf16 v[48:51], a[224:227], a[160:163], v[232:235]\n" \
"s_add_u32 s90, s90, s61\n" \
"v_and_b32 v85, 16, v248\n" \
"s_mul_i32 s90, s90, 24576\n" \
"v_mfma_f32_16x16x32_bf16 v[48:51], a[228:231], a[164:167], v[48:51]\n" \
"s_add_u32 s89, s89, s90\n" \
"v_lshl_add_u32 v85, v85, 2, s89\n" \
"v_lshl_add_u32 v84, v84, 11, v85\n" \
"v_mfma_f32_16x16x32_bf16 v[48:51], a[232:235], a[168:171], v[48:51]\n" \
"s_add_u32 m0, s84, 143360\n" \
"v_exp_f32 v116, v116\n" \
"v_mfma_f32_16x16x32_bf16 v[48:51], a[236:239], a[172:175], v[48:51]\n" \
"ds_read_b128 a[224:227], v247 offset:20480\n" \
"global_load_lds_dwordx4 v253, s[48:49] offset:-4096\n" \
"v_mfma_f32_16x16x32_bf16 v[12:15], v[216:219], a[184:187], v[236:239]\n" \
"ds_read_b128 a[228:231], v247 offset:28672\n" \
"v_cvt_pk_bf16_f32 v113, v114, v115\n" \
"v_mfma_f32_16x16x32_bf16 v[12:15], v[220:223], a[188:191], v[12:15]\n" \
"ds_read_b128 a[232:235], v247 offset:21504\n" \
"v_exp_f32 v117, v117\n" \
"v_mfma_f32_16x16x32_bf16 v[12:15], v[208:211], a[176:179], v[12:15]\n" \
"ds_read_b128 a[236:239], v247 offset:29696\n" \
"global_load_lds_dwordx4 v253, s[48:49] offset:-2048\n" \
"v_mfma_f32_16x16x32_bf16 v[12:15], v[212:215], a[180:183], v[12:15]\n" \
"ds_read_b128 v[208:211], v246 offset:24832\n" \
"v_exp_f32 v118, v118\n" \
"v_mfma_f32_16x16x32_bf16 v[60:63], a[240:243], a[176:179], v[236:239]\n" \
"ds_read_b128 v[212:215], v246 offset:26880\n" \
"v_cvt_pk_bf16_f32 v114, v116, v117\n" \
"v_mfma_f32_16x16x32_bf16 v[60:63], a[244:247], a[180:183], v[60:63]\n" \
"ds_read_b128 v[216:219], v246 offset:28928\n" \
"global_load_lds_dwordx4 v253, s[48:49] offset:0\n" \
"v_mfma_f32_16x16x32_bf16 v[60:63], a[248:251], a[184:187], v[60:63]\n" \
"s_add_u32 s48, s48, 0x120000\n" \
"s_addc_u32 s49, s49, 0\n" \
"ds_read_b128 v[220:223], v246 offset:30976\n" \
"v_mfma_f32_16x16x32_bf16 v[60:63], a[252:255], a[188:191], v[60:63]\n" \
"buffer_store_dwordx4 v[228:231], v65, s[56:59], s77 offen offset:2048 sc1\n" \
"v_exp_f32 v119, v119\n" \
"v_exp_f32 v120, v120\n" \
"v_cvt_pk_bf16_f32 v115, v118, v119\n" \
"v_mfma_f32_16x16x32_bf16 v[52:55], a[248:251], a[168:171], v[232:235]\n" \
"global_load_lds_dwordx4 v253, s[50:51] offset:-3072\n" \
"global_load_lds_dwordx4 v253, s[50:51] offset:-1024\n" \
"v_mfma_f32_16x16x32_bf16 v[52:55], a[252:255], a[172:175], v[52:55]\n" \
"s_add_u32 s77, s77, 0x1000\n" \
"v_exp_f32 v121, v121\n" \
"v_mfma_f32_16x16x32_bf16 v[52:55], a[240:243], a[160:163], v[52:55]\n" \
"v_exp_f32 v122, v122\n" \
"v_mfma_f32_16x16x32_bf16 v[52:55], a[244:247], a[164:167], v[52:55]\n" \
"s_waitcnt lgkmcnt(0)\n" \
"ds_read_b128 a[240:243], v247 offset:22528\n" \
"v_accvgpr_read_b32 v96, a16\n" \
"v_accvgpr_read_b32 v97, a17\n" \
"v_accvgpr_read_b32 v98, a18\n" \
"v_accvgpr_read_b32 v99, a19\n" \
"v_accvgpr_read_b32 v100, a20\n" \
"v_accvgpr_read_b32 v101, a21\n" \
"v_accvgpr_read_b32 v102, a22\n" \
"v_accvgpr_read_b32 v103, a23\n" \
"v_cvt_pk_bf16_f32 v120, v120, v121\n" \
"v_exp_f32 v123, v123\n" \
"v_pk_mul_f32 v[96:97], v[96:97], v[64:65] op_sel_hi:[1,0]\n" \
"v_pk_mul_f32 v[98:99], v[98:99], v[64:65] op_sel_hi:[1,0]\n" \
"v_pk_mul_f32 v[100:101], v[100:101], v[64:65] op_sel_hi:[1,0]\n" \
"v_pk_mul_f32 v[102:103], v[102:103], v[64:65] op_sel_hi:[1,0]\n" \
"v_cvt_pk_bf16_f32 v224, v96, v97\n" \
"v_cvt_pk_bf16_f32 v225, v98, v99\n" \
"v_cvt_pk_bf16_f32 v226, v100, v101\n" \
"v_cvt_pk_bf16_f32 v227, v102, v103\n" \
"v_pk_mul_f32 v[96:97], v[96:97], v[98:99]\n" \
"v_pk_fma_f32 v[96:97], v[100:101], v[102:103], v[96:97]\n" \
"v_fma_f32 v66, v96, v97, v66\n" \
"v_mfma_f32_16x16x32_bf16 v[40:43], v[192:195], a[176:179], v[236:239]\n" \
"buffer_store_dwordx4 v[224:227], v65, s[56:59], s77 offen offset:0 sc1\n" \
"ds_read_b128 a[244:247], v247 offset:30720\n" \
"v_accvgpr_read_b32 v96, a24\n" \
"v_accvgpr_read_b32 v97, a25\n" \
"v_accvgpr_read_b32 v98, a26\n" \
"v_accvgpr_read_b32 v99, a27\n" \
"v_accvgpr_read_b32 v100, a28\n" \
"v_accvgpr_read_b32 v101, a29\n" \
"v_accvgpr_read_b32 v102, a30\n" \
"v_accvgpr_read_b32 v103, a31\n" \
"v_exp_f32 v124, v124\n" \
"v_pk_mul_f32 v[96:97], v[96:97], v[64:65] op_sel_hi:[1,0]\n" \
"v_pk_mul_f32 v[98:99], v[98:99], v[64:65] op_sel_hi:[1,0]\n" \
"v_pk_mul_f32 v[100:101], v[100:101], v[64:65] op_sel_hi:[1,0]\n" \
"v_pk_mul_f32 v[102:103], v[102:103], v[64:65] op_sel_hi:[1,0]\n" \
"v_mfma_f32_16x16x32_bf16 v[40:43], v[196:199], a[180:183], v[40:43]\n" \
"ds_read_b128 a[248:251], v247 offset:23552\n" \
"v_cvt_pk_bf16_f32 v121, v122, v123\n" \
"v_mfma_f32_16x16x32_bf16 v[40:43], v[200:203], a[184:187], v[40:43]\n" \
"ds_read_b128 a[252:255], v247 offset:31744\n" \
"global_load_lds_dwordx4 v253, s[50:51] offset:1024\n" \
"v_mfma_f32_16x16x32_bf16 v[40:43], v[204:207], a[188:191], v[40:43]\n" \
"s_add_u32 s50, s50, 0x120000\n" \
"s_addc_u32 s51, s51, 0\n" \
"v_exp_f32 v125, v125\n" \
"v_mfma_f32_16x16x32_bf16 v[44:47], v[216:219], a[184:187], v[236:239]\n" \
"v_exp_f32 v126, v126\n" \
"v_mfma_f32_16x16x32_bf16 v[44:47], v[220:223], a[188:191], v[44:47]\n" \
"s_waitcnt vmcnt(9) lgkmcnt(0)\n" \
"global_load_dword v85, v84, s[22:23]\n" \
"v_mfma_f32_16x16x32_bf16 v[44:47], v[208:211], a[176:179], v[44:47]\n" \
"v_add_u32 v247, 0x10000, v248\n" \
"v_cvt_pk_bf16_f32 v122, v124, v125\n" \
"v_mfma_f32_16x16x32_bf16 v[44:47], v[212:215], a[180:183], v[44:47]\n" \
"s_barrier\n" \
"v_cvt_pk_bf16_f32 v228, v96, v97\n" \
"v_cvt_pk_bf16_f32 v229, v98, v99\n" \
"v_cvt_pk_bf16_f32 v230, v100, v101\n" \
"v_cvt_pk_bf16_f32 v231, v102, v103\n" \
"v_pk_mul_f32 v[96:97], v[96:97], v[98:99]\n" \
"v_pk_fma_f32 v[96:97], v[100:101], v[102:103], v[96:97]\n" \
"v_mfma_f32_16x16x32_bf16 a[32:35], a[192:195], v[72:75], a[32:35]\n" \
"v_exp_f32 v127, v127\n" \
"v_mfma_f32_16x16x32_bf16 a[32:35], a[196:199], v[104:107], a[32:35]\n" \
"v_cvt_pk_bf16_f32 v123, v126, v127\n" \
"v_fma_f32 v66, v96, v97, v66\n" \
"v_mfma_f32_16x16x32_bf16 v[128:131], a[192:195], v[80:83], v[128:131]\n" \
"ds_read_b128 v[192:195], v247 offset:16384\n" \
"v_cmp_gt_i32 vcc, 0, v251\n" \
"v_mfma_f32_16x16x32_bf16 v[128:131], a[196:199], v[112:115], v[128:131]\n" \
"buffer_store_dwordx4 v[228:231], v65, s[56:59], s77 offen offset:2048 sc1\n" \
"v_lshrrev_b32 v71, 7, v248\n" \
"v_and_b32 v76, 0x70, v248\n" \
"v_cmp_class_f32 s[54:55], v66, s94\n" \
"s_sub_u32 s77, s77, 4096\n" \
"v_mfma_f32_16x16x32_bf16 v[160:163], a[192:195], v[88:91], v[160:163]\n" \
"s_lshl_b32 s70, s88, 18\n" \
"v_cmp_gt_i32 s[64:65], 1, v251\n" \
"v_cmp_gt_i32 s[66:67], 2, v251\n" \
"v_mfma_f32_16x16x32_bf16 v[160:163], a[196:199], v[120:123], v[160:163]\n" \
"s_lshl_b32 s71, s61, 16\n" \
"ds_read_b128 a[192:195], v247 offset:0\n" \
"v_cndmask_b32 v8, v8, v254, vcc\n" \
"v_mfma_f32_16x16x32_bf16 v[240:243], a[124:127], v[104:107], v[240:243]\n" \
"s_add_u32 s71, s71, s70\n" \
"ds_read_b128 v[196:199], v247 offset:24576\n" \
"v_cndmask_b32 v16, v16, v254, vcc\n" \
"v_mfma_f32_16x16x32_bf16 v[164:167], a[200:203], v[88:91], v[164:167]\n" \
"s_or_b64 s[92:93], s[92:93], s[54:55]\n" \
"v_lshlrev_b32 v77, 4, v71\n" \
"v_xor_b32 v77, v77, v76\n" \
"v_mfma_f32_16x16x32_bf16 v[164:167], a[204:207], v[120:123], v[164:167]\n" \
"s_add_u32 s42, s20, s71\n" \
"s_addc_u32 s43, s21, 0\n" \
"ds_read_b128 a[196:199], v247 offset:8192\n" \
"v_cmp_gt_i32 vcc, 3, v251\n" \
"v_cndmask_b32 v9, v9, v254, s[64:65]\n" \
"v_cndmask_b32 v17, v17, v254, s[64:65]\n" \
"v_cmp_gt_i32 s[64:65], 16, v251\n" \
"v_cndmask_b32 v10, v10, v254, s[66:67]\n" \
"v_cndmask_b32 v18, v18, v254, s[66:67]\n" \
"v_cmp_gt_i32 s[66:67], 17, v251\n" \
"v_mfma_f32_16x16x32_bf16 v[132:135], a[200:203], v[80:83], v[132:135]\n" \
"ds_read_b128 v[200:203], v247 offset:17408\n" \
"v_lshl_add_u32 v252, v71, 12, v77\n" \
"v_mfma_f32_16x16x32_bf16 v[132:135], a[204:207], v[112:115], v[132:135]\n" \
"s_add_u32 s44, s42, 31744\n" \
"s_addc_u32 s45, s43, 0\n" \
"v_cndmask_b32 v11, v11, v254, vcc\n" \
"v_mfma_f32_16x16x32_bf16 a[36:39], a[200:203], v[72:75], a[36:39]\n" \
"s_max_u32 s70, s70, 32768\n" \
"v_cndmask_b32 v19, v19, v254, vcc\n" \
"v_cmp_gt_i32 vcc, 18, v251\n" \
"v_mfma_f32_16x16x32_bf16 a[36:39], a[204:207], v[104:107], a[36:39]\n" \
"s_sub_u32 s70, s70, 32768\n" \
"ds_read_b128 a[200:203], v247 offset:1024\n" \
"v_cndmask_b32 v12, v12, v254, s[64:65]\n" \
"v_mfma_f32_16x16x32_bf16 v[242:245], a[120:123], v[112:115], v[242:245]\n" \
"s_and_b32 s86, s61, 1\n" \
"ds_read_b128 v[204:207], v247 offset:25600\n" \
"v_cndmask_b32 v20, v20, v254, s[64:65]\n" \
"v_mfma_f32_16x16x32_bf16 a[40:43], a[208:211], v[72:75], a[40:43]\n" \
"s_lshl_b32 s86, s86, 2\n" \
"v_cmp_gt_i32 s[64:65], 19, v251\n" \
"v_and_or_b32 v71, v71, 3, s86\n" \
"v_mfma_f32_16x16x32_bf16 v[242:245], a[124:127], v[120:123], v[242:245]\n" \
"v_cndmask_b32 v13, v13, v254, s[66:67]\n" \
"v_cndmask_b32 v21, v21, v254, s[66:67]\n" \
"v_cmp_gt_i32 s[66:67], 32, v251\n" \
"v_mfma_f32_16x16x32_bf16 a[40:43], a[212:215], v[104:107], a[40:43]\n" \
"s_lshr_b32 s71, s61, 1\n" \
"ds_read_b128 a[204:207], v247 offset:9216\n" \
"v_cndmask_b32 v14, v14, v254, vcc\n" \
"v_cndmask_b32 v22, v22, v254, vcc\n" \
"v_cmp_gt_i32 vcc, 33, v251\n" \
"v_cndmask_b32 v15, v15, v254, s[64:65]\n" \
"v_cndmask_b32 v23, v23, v254, s[64:65]\n" \
"v_cmp_gt_i32 s[64:65], 34, v251\n" \
"v_cndmask_b32 v40, v40, v254, s[66:67]\n" \
"v_cndmask_b32 v48, v48, v254, s[66:67]\n" \
"v_cmp_gt_i32 s[66:67], 35, v251\n" \
"v_cndmask_b32 v41, v41, v254, vcc\n" \
"v_mfma_f32_16x16x32_bf16 v[136:139], a[208:211], v[80:83], v[136:139]\n" \
"s_lshl_b32 s71, s71, 11\n" \
"ds_read_b128 v[208:211], v247 offset:18432\n" \
"v_lshlrev_b32 v77, 4, v71\n" \
"v_mfma_f32_16x16x32_bf16 v[136:139], a[212:215], v[112:115], v[136:139]\n" \
"s_add_u32 s70, s70, s71\n" \
"v_mov_b32 v66, v243\n" \
"v_cndmask_b32 v49, v49, v254, vcc\n" \
"v_cmp_gt_i32 vcc, 48, v251\n" \
"v_cndmask_b32 v42, v42, v254, s[64:65]\n" \
"v_cndmask_b32 v50, v50, v254, s[64:65]\n" \
"v_cmp_gt_i32 s[64:65], 49, v251\n" \
"v_cndmask_b32 v43, v43, v254, s[66:67]\n" \
"v_cndmask_b32 v51, v51, v254, s[66:67]\n" \
"v_cmp_gt_i32 s[66:67], 50, v251\n" \
"v_cndmask_b32 v44, v44, v254, vcc\n" \
"v_cndmask_b32 v52, v52, v254, vcc\n" \
"v_xor_b32 v77, v77, v76\n" \
"v_mfma_f32_16x16x32_bf16 v[168:171], a[208:211], v[88:91], v[168:171]\n" \
"s_mov_b32 s46, s70\n" \
"v_cmp_gt_i32 vcc, 51, v251\n" \
"v_lshl_add_u32 v246, v71, 12, v77\n" \
"v_mfma_f32_16x16x32_bf16 v[168:171], a[212:215], v[120:123], v[168:171]\n" \
"s_lshl_b32 s85, s61, 10\n" \
"ds_read_b128 a[208:211], v247 offset:2048\n" \
"s_mov_b64 s[12:13], s[20:21]\n" \
"ds_read_b128 v[212:215], v247 offset:26624\n" \
"v_cndmask_b32 v45, v45, v254, s[64:65]\n" \
"v_cndmask_b32 v53, v53, v254, s[64:65]\n" \
"v_mfma_f32_16x16x32_bf16 v[172:175], a[216:219], v[88:91], v[172:175]\n" \
"s_and_b32 s13, s13, 0xffff\n" \
"v_cndmask_b32 v46, v46, v254, s[66:67]\n" \
"v_cndmask_b32 v54, v54, v254, s[66:67]\n" \
"v_mfma_f32_16x16x32_bf16 v[172:175], a[220:223], v[120:123], v[172:175]\n" \
"s_mov_b32 s14, 4194304\n" \
"ds_read_b128 a[212:215], v247 offset:10240\n" \
"v_cndmask_b32 v47, v47, v254, vcc\n" \
"v_mfma_f32_16x16x32_bf16 v[140:143], a[216:219], v[80:83], v[140:143]\n" \
"s_mov_b32 s15, 0x20000\n" \
"ds_read_b128 v[216:219], v247 offset:19456\n" \
"v_cndmask_b32 v55, v55, v254, vcc\n" \
"v_mfma_f32_16x16x32_bf16 v[140:143], a[220:223], v[112:115], v[140:143]\n" \
"v_lshrrev_b32 v71, 9, v248\n" \
"v_lshl_add_u32 v246, v71, 30, v246\n" \
"v_exp_f32 v8, v8\n" \
"v_exp_f32 v9, v9\n" \
"v_mfma_f32_16x16x32_bf16 a[44:47], a[216:219], v[72:75], a[44:47]\n" \
"s_add_u32 m0, s95, 118784\n" \
"v_exp_f32 v10, v10\n" \
"v_mfma_f32_16x16x32_bf16 a[44:47], a[220:223], v[104:107], a[44:47]\n" \
"ds_read_b128 a[216:219], v247 offset:3072\n" \
"global_load_lds_dwordx4 v252, s[42:43] offset:0\n" \
"ds_read_b128 v[220:223], v247 offset:27648\n" \
"v_cvt_pk_bf16_f32 v8, v8, v9\n" \
"v_mfma_f32_16x16x32_bf16 a[48:51], a[224:227], v[72:75], a[48:51]\n" \
"v_exp_f32 v11, v11\n" \
"v_mfma_f32_16x16x32_bf16 a[48:51], a[228:231], v[104:107], a[48:51]\n" \
"ds_read_b128 a[220:223], v247 offset:11264\n" \
"v_exp_f32 v12, v12\n" \
"v_mfma_f32_16x16x32_bf16 v[144:147], a[224:227], v[80:83], v[144:147]\n" \
"ds_read_b128 v[224:227], v247 offset:20480\n" \
"v_cvt_pk_bf16_f32 v9, v10, v11\n" \
"v_mfma_f32_16x16x32_bf16 v[144:147], a[228:231], v[112:115], v[144:147]\n" \
"v_exp_f32 v13, v13\n" \
"v_mfma_f32_16x16x32_bf16 v[176:179], a[224:227], v[88:91], v[176:179]\n" \
"v_exp_f32 v14, v14\n" \
"v_mfma_f32_16x16x32_bf16 v[176:179], a[228:231], v[120:123], v[176:179]\n" \
"ds_read_b128 a[224:227], v247 offset:4096\n" \
"global_load_lds_dwordx4 v252, s[42:43] offset:2048\n" \
"ds_read_b128 v[228:231], v247 offset:28672\n" \
"v_mfma_f32_16x16x32_bf16 v[180:183], a[232:235], v[88:91], v[180:183]\n" \
"s_add_u32 s42, s42, 0x180\n" \
"s_addc_u32 s43, s43, 0\n" \
"v_cvt_pk_bf16_f32 v10, v12, v13\n" \
"v_mfma_f32_16x16x32_bf16 v[180:183], a[236:239], v[120:123], v[180:183]\n" \
"ds_read_b128 a[228:231], v247 offset:12288\n" \
"v_exp_f32 v15, v15\n" \
"v_exp_f32 v40, v40\n" \
"v_mfma_f32_16x16x32_bf16 v[148:151], a[232:235], v[80:83], v[148:151]\n" \
"ds_read_b128 v[232:235], v247 offset:21504\n" \
"v_cvt_pk_bf16_f32 v11, v14, v15\n" \
"v_mfma_f32_16x16x32_bf16 v[148:151], a[236:239], v[112:115], v[148:151]\n" \
"v_exp_f32 v41, v41\n" \
"v_mfma_f32_16x16x32_bf16 a[52:55], a[232:235], v[72:75], a[52:55]\n" \
"v_exp_f32 v42, v42\n" \
"v_mfma_f32_16x16x32_bf16 a[52:55], a[236:239], v[104:107], a[52:55]\n" \
"ds_read_b128 a[232:235], v247 offset:5120\n" \
"global_load_lds_dwordx4 v252, s[44:45] offset:1024\n" \
"ds_read_b128 v[236:239], v247 offset:29696\n" \
"v_cvt_pk_bf16_f32 v40, v40, v41\n" \
"v_mfma_f32_16x16x32_bf16 a[56:59], a[240:243], v[72:75], a[56:59]\n" \
"v_exp_f32 v43, v43\n" \
"v_mfma_f32_16x16x32_bf16 a[56:59], a[244:247], v[104:107], a[56:59]\n" \
"ds_read_b128 a[236:239], v247 offset:13312\n" \
"v_exp_f32 v44, v44\n" \
"v_mfma_f32_16x16x32_bf16 v[152:155], a[240:243], v[80:83], v[152:155]\n" \
"ds_read_b128 v[0:3], v247 offset:22528\n" \
"v_cvt_pk_bf16_f32 v41, v42, v43\n" \
"v_mfma_f32_16x16x32_bf16 v[152:155], a[244:247], v[112:115], v[152:155]\n" \
"v_exp_f32 v45, v45\n" \
"v_mfma_f32_16x16x32_bf16 v[184:187], a[240:243], v[88:91], v[184:187]\n" \
"v_exp_f32 v46, v46\n" \
"v_mfma_f32_16x16x32_bf16 v[184:187], a[244:247], v[120:123], v[184:187]\n" \
"ds_read_b128 a[240:243], v247 offset:6144\n" \
"global_load_lds_dwordx4 v252, s[44:45] offset:3072\n" \
"ds_read_b128 v[4:7], v247 offset:30720\n" \
"v_mfma_f32_16x16x32_bf16 a[60:63], a[248:251], v[72:75], a[60:63]\n" \
"s_add_u32 s44, s44, 0x180\n" \
"s_addc_u32 s45, s45, 0\n" \
"v_cvt_pk_bf16_f32 v42, v44, v45\n" \
"v_mfma_f32_16x16x32_bf16 a[60:63], a[252:255], v[104:107], a[60:63]\n" \
"s_add_u32 m0, s85, 135168\n" \
"ds_read_b128 a[244:247], v247 offset:14336\n" \
"v_exp_f32 v47, v47\n" \
"v_exp_f32 v24, v24\n" \
"v_mfma_f32_16x16x32_bf16 v[156:159], a[248:251], v[80:83], v[156:159]\n" \
"ds_read_b128 v[32:35], v247 offset:23552\n" \
"v_cvt_pk_bf16_f32 v43, v46, v47\n" \
"v_mfma_f32_16x16x32_bf16 v[156:159], a[252:255], v[112:115], v[156:159]\n" \
"buffer_load_dwordx4 v246, s[12:15], s46 offen lds\n" \
"v_exp_f32 v25, v25\n" \
"v_mfma_f32_16x16x32_bf16 v[188:191], a[248:251], v[88:91], v[188:191]\n" \
"s_add_u32 s46, s46, 0x180\n" \
"v_exp_f32 v26, v26\n" \
"v_mfma_f32_16x16x32_bf16 v[188:191], a[252:255], v[120:123], v[188:191]\n" \
"s_waitcnt lgkmcnt(2)\n" \
"ds_read_b128 a[248:251], v247 offset:7168\n" \
"v_cvt_pk_bf16_f32 v24, v24, v25\n" \
"v_mfma_f32_16x16x32_bf16 v[242:245], a[124:127], v[8:11], v[242:245]\n" \
"v_exp_f32 v27, v27\n" \
"v_mfma_f32_16x16x32_bf16 v[242:245], a[124:127], v[40:43], v[242:245]\n" \
"ds_read_b128 v[36:39], v247 offset:31744\n" \
"v_exp_f32 v28, v28\n" \
"v_mfma_f32_16x16x32_bf16 v[160:163], v[192:195], v[8:11], v[160:163]\n" \
"v_cvt_pk_bf16_f32 v25, v26, v27\n" \
"v_exp_f32 v29, v29\n" \
"v_exp_f32 v30, v30\n" \
"v_cvt_pk_bf16_f32 v26, v28, v29\n" \
"v_exp_f32 v31, v31\n" \
"v_exp_f32 v56, v56\n" \
"v_cvt_pk_bf16_f32 v27, v30, v31\n" \
"v_exp_f32 v57, v57\n" \
"v_exp_f32 v58, v58\n" \
"v_cvt_pk_bf16_f32 v56, v56, v57\n" \
"v_mfma_f32_16x16x32_bf16 v[160:163], v[196:199], v[40:43], v[160:163]\n" \
"ds_read_b128 a[252:255], v247 offset:15360\n" \
"v_exp_f32 v59, v59\n" \
"v_mfma_f32_16x16x32_bf16 v[164:167], v[200:203], v[8:11], v[164:167]\n" \
"v_exp_f32 v60, v60\n" \
"v_mfma_f32_16x16x32_bf16 v[164:167], v[204:207], v[40:43], v[164:167]\n" \
"v_mov_b32 v64, v66\n" \
"v_cvt_pk_bf16_f32 v57, v58, v59\n" \
"v_exp_f32 v61, v61\n" \
"v_mfma_f32_16x16x32_bf16 v[168:171], v[208:211], v[8:11], v[168:171]\n" \
"v_exp_f32 v62, v62\n" \
"v_mfma_f32_16x16x32_bf16 v[168:171], v[212:215], v[40:43], v[168:171]\n" \
"s_waitcnt vmcnt(0) lgkmcnt(0)\n" \
"v_cvt_pk_bf16_f32 v58, v60, v61\n" \
"v_exp_f32 v63, v63\n" \
"v_permlane16_swap_b32 v64, v66\n" \
"v_add_f32 v66, v66, v64\n" \
"v_mov_b32 v64, v66\n" \
"v_mfma_f32_16x16x32_bf16 v[172:175], v[216:219], v[8:11], v[172:175]\n" \
"v_exp_f32 v16, v16\n" \
"v_mfma_f32_16x16x32_bf16 v[172:175], v[220:223], v[40:43], v[172:175]\n" \
"s_barrier\n" \
"v_cvt_pk_bf16_f32 v59, v62, v63\n" \
"v_exp_f32 v17, v17\n" \
"v_permlane32_swap_b32 v64, v66\n" \
"v_add_f32 v66, v66, v64\n" \
"v_rcp_f32 v64, v66\n" \
"v_mul_f32 v66, 0x4d800000, v66\n" \
"v_add_u32 v65, 0x1c90080, v249\n" \
"v_cndmask_b32 v65, v249, v65, s[16:17]\n" \
"v_mfma_f32_16x16x32_bf16 v[176:179], v[224:227], v[8:11], v[176:179]\n" \
"s_add_u32 m0, s84, 90112\n" \
"v_exp_f32 v18, v18\n" \
"v_mfma_f32_16x16x32_bf16 v[176:179], v[228:231], v[40:43], v[176:179]\n" \
"global_load_lds_dwordx4 v253, s[48:49] offset:-4096\n" \
"global_load_lds_dwordx4 v253, s[48:49] offset:-2048\n" \
"v_cvt_pk_bf16_f32 v16, v16, v17\n" \
"v_mfma_f32_16x16x32_bf16 v[242:245], a[124:127], v[24:27], v[242:245]\n" \
"v_exp_f32 v19, v19\n" \
"v_mfma_f32_16x16x32_bf16 v[242:245], a[124:127], v[56:59], v[242:245]\n" \
"v_exp_f32 v20, v20\n" \
"v_cvt_pk_bf16_f32 v17, v18, v19\n" \
"v_mfma_f32_16x16x32_bf16 v[180:183], v[232:235], v[8:11], v[180:183]\n" \
"s_add_u32 s77, s77, 532480\n" \
"v_exp_f32 v21, v21\n" \
"v_mfma_f32_16x16x32_bf16 v[180:183], v[236:239], v[40:43], v[180:183]\n" \
"v_accvgpr_read_b32 v96, a32\n" \
"v_accvgpr_read_b32 v97, a33\n" \
"v_accvgpr_read_b32 v98, a34\n" \
"v_accvgpr_read_b32 v99, a35\n" \
"v_accvgpr_read_b32 v100, a36\n" \
"v_accvgpr_read_b32 v101, a37\n" \
"v_accvgpr_read_b32 v102, a38\n" \
"v_accvgpr_read_b32 v103, a39\n" \
"v_exp_f32 v22, v22\n" \
"v_pk_mul_f32 v[96:97], v[96:97], v[64:65] op_sel_hi:[1,0]\n" \
"v_pk_mul_f32 v[98:99], v[98:99], v[64:65] op_sel_hi:[1,0]\n" \
"v_pk_mul_f32 v[100:101], v[100:101], v[64:65] op_sel_hi:[1,0]\n" \
"v_pk_mul_f32 v[102:103], v[102:103], v[64:65] op_sel_hi:[1,0]\n" \
"v_cvt_pk_bf16_f32 v104, v96, v97\n" \
"v_cvt_pk_bf16_f32 v105, v98, v99\n" \
"v_cvt_pk_bf16_f32 v106, v100, v101\n" \
"v_cvt_pk_bf16_f32 v107, v102, v103\n" \
"v_pk_mul_f32 v[96:97], v[96:97], v[98:99]\n" \
"v_pk_fma_f32 v[96:97], v[100:101], v[102:103], v[96:97]\n" \
"v_fma_f32 v66, v96, v97, v66\n" \
"v_mfma_f32_16x16x32_bf16 v[184:187], v[0:3], v[8:11], v[184:187]\n" \
"global_load_lds_dwordx4 v253, s[48:49] offset:0\n" \
"v_cvt_pk_bf16_f32 v18, v20, v21\n" \
"v_mfma_f32_16x16x32_bf16 v[184:187], v[4:7], v[40:43], v[184:187]\n" \
"s_add_u32 s48, s48, 0x300000\n" \
"s_addc_u32 s49, s49, 0\n" \
"v_exp_f32 v23, v23\n" \
"v_exp_f32 v48, v48\n" \
"v_mfma_f32_16x16x32_bf16 v[188:191], v[32:35], v[8:11], v[188:191]\n" \
"global_load_lds_dwordx4 v253, s[50:51] offset:-3072\n" \
"v_cvt_pk_bf16_f32 v19, v22, v23\n" \
"v_mfma_f32_16x16x32_bf16 v[188:191], v[36:39], v[40:43], v[188:191]\n" \
"buffer_store_dwordx4 v[104:107], v65, s[56:59], s77 offen offset:0 sc1\n" \
"v_accvgpr_read_b32 v96, a40\n" \
"v_accvgpr_read_b32 v97, a41\n" \
"v_accvgpr_read_b32 v98, a42\n" \
"v_accvgpr_read_b32 v99, a43\n" \
"v_accvgpr_read_b32 v100, a44\n" \
"v_accvgpr_read_b32 v101, a45\n" \
"v_accvgpr_read_b32 v102, a46\n" \
"v_accvgpr_read_b32 v103, a47\n" \
"v_exp_f32 v49, v49\n" \
"v_pk_mul_f32 v[96:97], v[96:97], v[64:65] op_sel_hi:[1,0]\n" \
"v_pk_mul_f32 v[98:99], v[98:99], v[64:65] op_sel_hi:[1,0]\n" \
"v_pk_mul_f32 v[100:101], v[100:101], v[64:65] op_sel_hi:[1,0]\n" \
"v_pk_mul_f32 v[102:103], v[102:103], v[64:65] op_sel_hi:[1,0]\n" \
"v_cvt_pk_bf16_f32 v108, v96, v97\n" \
"v_cvt_pk_bf16_f32 v109, v98, v99\n" \
"v_cvt_pk_bf16_f32 v110, v100, v101\n" \
"v_cvt_pk_bf16_f32 v111, v102, v103\n" \
"v_pk_mul_f32 v[96:97], v[96:97], v[98:99]\n" \
"v_pk_fma_f32 v[96:97], v[100:101], v[102:103], v[96:97]\n" \
"v_mfma_f32_16x16x32_bf16 v[160:163], a[192:195], v[24:27], v[160:163]\n" \
"global_load_lds_dwordx4 v253, s[50:51] offset:-1024\n" \
"global_load_lds_dwordx4 v253, s[50:51] offset:1024\n" \
"v_mfma_f32_16x16x32_bf16 v[160:163], a[196:199], v[56:59], v[160:163]\n" \
"s_movk_i32 s89, 0xc0\n" \
"v_exp_f32 v50, v50\n" \
"v_cvt_pk_bf16_f32 v48, v48, v49\n" \
"v_mfma_f32_16x16x32_bf16 v[242:245], a[120:123], v[16:19], v[242:245]\n" \
"s_movk_i32 s90, 288\n" \
"v_exp_f32 v51, v51\n" \
"v_lshrrev_b32 v211, 4, v248\n" \
"v_mfma_f32_16x16x32_bf16 v[164:167], a[200:203], v[24:27], v[164:167]\n" \
"s_add_u32 s50, s50, 0x300000\n" \
"s_addc_u32 s51, s51, 0\n" \
"v_lshrrev_b32 v214, 8, v248\n" \
"v_mfma_f32_16x16x32_bf16 v[164:167], a[204:207], v[56:59], v[164:167]\n" \
"s_add_u32 m0, s95, 65536\n" \
"v_and_b32 v215, 7, v211\n" \
"v_xor_b32 v214, v214, v215\n" \
"v_exp_f32 v52, v52\n" \
"v_cvt_pk_bf16_f32 v49, v50, v51\n" \
"v_exp_f32 v53, v53\n" \
"v_exp_f32 v54, v54\n" \
"v_cvt_pk_bf16_f32 v50, v52, v53\n" \
"v_exp_f32 v55, v55\n" \
"v_mfma_f32_16x16x32_bf16 v[168:171], a[208:211], v[24:27], v[168:171]\n" \
"global_load_lds_dwordx4 v252, s[42:43] offset:0\n" \
"global_load_lds_dwordx4 v252, s[42:43] offset:2048\n" \
"v_mfma_f32_16x16x32_bf16 v[168:171], a[212:215], v[56:59], v[168:171]\n" \
"s_lshr_b32 s71, s61, 1\n" \
"v_cvt_pk_bf16_f32 v51, v54, v55\n" \
"s_lshl_b32 s70, s71, 11\n" \
"v_mfma_f32_16x16x32_bf16 v[172:175], a[216:219], v[24:27], v[172:175]\n" \
"s_add_u32 s70, s70, 118784\n" \
"v_and_b32 v216, 15, v211\n" \
"v_lshlrev_b32 v213, 7, v216\n" \
"v_mfma_f32_16x16x32_bf16 v[172:175], a[220:223], v[56:59], v[172:175]\n" \
"v_fma_f32 v66, v96, v97, v66\n" \
"v_lshl_add_u32 v213, v214, 4, v213\n" \
"v_mfma_f32_16x16x32_bf16 v[242:245], a[120:123], v[48:51], v[242:245]\n" \
"s_add_u32 s42, s42, 0x400\n" \
"s_addc_u32 s43, s43, 0\n" \
"v_add_u32 v205, s70, v213\n" \
"v_mfma_f32_16x16x32_bf16 v[176:179], a[224:227], v[24:27], v[176:179]\n" \
"global_load_lds_dwordx4 v252, s[44:45] offset:1024\n" \
"global_load_lds_dwordx4 v252, s[44:45] offset:3072\n" \
"v_mfma_f32_16x16x32_bf16 v[176:179], a[228:231], v[56:59], v[176:179]\n" \
"v_xor_b32 v206, 64, v205\n" \
"v_lshrrev_b32 v216, 2, v216\n" \
"v_mfma_f32_16x16x32_bf16 v[180:183], a[232:235], v[24:27], v[180:183]\n" \
"s_add_u32 s44, s44, 0x400\n" \
"s_addc_u32 s45, s45, 0\n" \
"v_lshl_add_u32 v216, v216, 9, v213\n" \
"v_mfma_f32_16x16x32_bf16 v[180:183], a[236:239], v[56:59], v[180:183]\n" \
"s_add_u32 m0, s85, 81920\n" \
"v_add_u32 v207, 135168, v216\n" \
"v_xor_b32 v208, 64, v207\n" \
"v_mfma_f32_16x16x32_bf16 v[184:187], a[240:243], v[24:27], v[184:187]\n" \
"s_and_b32 s70, s61, 1\n" \
"s_lshl_b32 s70, s70, 9\n" \
"s_add_u32 s70, s70, 139264\n" \
"v_mfma_f32_16x16x32_bf16 v[184:187], a[244:247], v[56:59], v[184:187]\n" \
"s_lshl_b32 s71, s71, 5\n" \
"v_and_b32 v213, 3, v211\n" \
"v_lshlrev_b32 v213, 3, v213\n" \
"v_and_or_b32 v213, v248, s89, v213\n" \
"v_bfe_u32 v214, v211, 4, 1\n" \
"v_mfma_f32_16x16x32_bf16 v[188:191], a[248:251], v[24:27], v[188:191]\n" \
"buffer_store_dwordx4 v[108:111], v65, s[56:59], s77 offen offset:2048 sc1\n" \
"v_mad_u32_u24 v213, v214, s90, v213\n" \
"v_lshrrev_b32 v214, 5, v211\n" \
"v_mul_u32_u24 v214, 6144, v214\n" \
"v_add3_u32 v213, v213, v214, s70\n" \
"v_xor_b32 v209, s71, v213\n" \
"v_xor_b32 v210, 32, v209\n" \
"v_mfma_f32_16x16x32_bf16 v[188:191], a[252:255], v[56:59], v[188:191]\n" \
"s_add_u32 s77, s77, 0x1000\n" \
"ds_read_b128 a[64:67], v207 offset:0\n" \
"ds_read_b64_tr_b16 a[96:97], v209 offset:2048\n" \
"v_accvgpr_read_b32 v96, a48\n" \
"v_accvgpr_read_b32 v97, a49\n" \
"v_accvgpr_read_b32 v98, a50\n" \
"v_accvgpr_read_b32 v99, a51\n" \
"v_accvgpr_read_b32 v100, a52\n" \
"v_accvgpr_read_b32 v101, a53\n" \
"v_accvgpr_read_b32 v102, a54\n" \
"v_accvgpr_read_b32 v103, a55\n" \
"v_pk_mul_f32 v[96:97], v[96:97], v[64:65] op_sel_hi:[1,0]\n" \
"v_pk_mul_f32 v[98:99], v[98:99], v[64:65] op_sel_hi:[1,0]\n" \
"v_pk_mul_f32 v[100:101], v[100:101], v[64:65] op_sel_hi:[1,0]\n" \
"v_pk_mul_f32 v[102:103], v[102:103], v[64:65] op_sel_hi:[1,0]\n" \
"v_cvt_pk_bf16_f32 v104, v96, v97\n" \
"v_cvt_pk_bf16_f32 v105, v98, v99\n" \
"v_cvt_pk_bf16_f32 v106, v100, v101\n" \
"v_cvt_pk_bf16_f32 v107, v102, v103\n" \
"v_pk_mul_f32 v[96:97], v[96:97], v[98:99]\n" \
"v_pk_fma_f32 v[96:97], v[100:101], v[102:103], v[96:97]\n" \
"v_mfma_f32_16x16x32_bf16 v[128:131], a[192:195], v[16:19], v[128:131]\n" \
"ds_read_b64_tr_b16 a[98:99], v209 offset:3072\n" \
"v_fma_f32 v66, v96, v97, v66\n" \
"v_mfma_f32_16x16x32_bf16 v[128:131], a[196:199], v[48:51], v[128:131]\n" \
"buffer_store_dwordx4 v[104:107], v65, s[56:59], s77 offen offset:0 sc1\n" \
"ds_read_b128 a[68:71], v208 offset:0\n" \
"v_accvgpr_read_b32 v96, a56\n" \
"v_accvgpr_read_b32 v97, a57\n" \
"v_accvgpr_read_b32 v98, a58\n" \
"v_accvgpr_read_b32 v99, a59\n" \
"v_accvgpr_read_b32 v100, a60\n" \
"v_accvgpr_read_b32 v101, a61\n" \
"v_accvgpr_read_b32 v102, a62\n" \
"v_accvgpr_read_b32 v103, a63\n" \
"v_pk_mul_f32 v[96:97], v[96:97], v[64:65] op_sel_hi:[1,0]\n" \
"v_pk_mul_f32 v[98:99], v[98:99], v[64:65] op_sel_hi:[1,0]\n" \
"v_pk_mul_f32 v[100:101], v[100:101], v[64:65] op_sel_hi:[1,0]\n" \
"v_pk_mul_f32 v[102:103], v[102:103], v[64:65] op_sel_hi:[1,0]\n" \
"v_cvt_pk_bf16_f32 v108, v96, v97\n" \
"v_cvt_pk_bf16_f32 v109, v98, v99\n" \
"v_cvt_pk_bf16_f32 v110, v100, v101\n" \
"v_cvt_pk_bf16_f32 v111, v102, v103\n" \
"v_pk_mul_f32 v[96:97], v[96:97], v[98:99]\n" \
"v_pk_fma_f32 v[96:97], v[100:101], v[102:103], v[96:97]\n" \
"v_fma_f32 v66, v96, v97, v66\n" \
"v_mfma_f32_16x16x32_bf16 v[132:135], a[200:203], v[16:19], v[132:135]\n" \
"buffer_store_dwordx4 v[108:111], v65, s[56:59], s77 offen offset:2048 sc1\n" \
"ds_read_b64_tr_b16 a[100:101], v209 offset:14336\n" \
"buffer_load_dwordx4 v246, s[12:15], s46 offen lds\n" \
"ds_read_b64_tr_b16 a[102:103], v209 offset:15360\n" \
"v_mfma_f32_16x16x32_bf16 v[132:135], a[204:207], v[48:51], v[132:135]\n" \
"s_add_u32 s46, s46, 0x400\n" \
"ds_read_b128 a[72:75], v205 offset:0\n" \
"v_cmp_class_f32 s[54:55], v66, s94\n" \
"v_mfma_f32_16x16x32_bf16 v[136:139], a[208:211], v[16:19], v[136:139]\n" \
"s_add_u32 m0, s95, 0\n" \
"s_sub_u32 s77, s77, 536576\n" \
"ds_read_b64_tr_b16 a[104:105], v209 offset:0\n" \
"v_mfma_f32_16x16x32_bf16 v[136:139], a[212:215], v[48:51], v[136:139]\n" \
"s_or_b64 s[92:93], s[92:93], s[54:55]\n" \
"ds_read_b128 a[76:79], v206 offset:0\n" \
"ds_read_b64_tr_b16 a[106:107], v209 offset:1024\n" \
"v_mfma_f32_16x16x32_bf16 v[140:143], a[216:219], v[16:19], v[140:143]\n" \
"ds_read_b64_tr_b16 a[108:109], v209 offset:12288\n" \
"global_load_lds_dwordx4 v252, s[42:43] offset:0\n" \
"v_mfma_f32_16x16x32_bf16 v[140:143], a[220:223], v[48:51], v[140:143]\n" \
"ds_read_b64_tr_b16 a[110:111], v209 offset:13312\n" \
"global_load_lds_dwordx4 v252, s[42:43] offset:2048\n" \
"v_mfma_f32_16x16x32_bf16 v[144:147], a[224:227], v[16:19], v[144:147]\n" \
"ds_read_b128 a[80:83], v205 offset:4096\n" \
"ds_read_b64_tr_b16 a[112:113], v210 offset:0\n" \
"v_mfma_f32_16x16x32_bf16 v[144:147], a[228:231], v[48:51], v[144:147]\n" \
"s_add_u32 s42, s42, 0xfffffb00\n" \
"s_addc_u32 s43, s43, -1\n" \
"ds_read_b64_tr_b16 a[114:115], v210 offset:1024\n" \
"v_mfma_f32_16x16x32_bf16 v[148:151], a[232:235], v[16:19], v[148:151]\n" \
"ds_read_b64_tr_b16 a[116:117], v210 offset:12288\n" \
"global_load_lds_dwordx4 v252, s[44:45] offset:1024\n" \
"v_mfma_f32_16x16x32_bf16 v[148:151], a[236:239], v[48:51], v[148:151]\n" \
"ds_read_b128 a[84:87], v206 offset:4096\n" \
"ds_read_b64_tr_b16 a[118:119], v210 offset:13312\n" \
"v_mfma_f32_16x16x32_bf16 v[152:155], a[240:243], v[16:19], v[152:155]\n" \
"ds_read_b128 a[88:91], v205 offset:8192\n" \
"global_load_lds_dwordx4 v252, s[44:45] offset:3072\n" \
"v_mfma_f32_16x16x32_bf16 v[152:155], a[244:247], v[48:51], v[152:155]\n" \
"s_add_u32 s44, s44, 0xfffffb00\n" \
"s_addc_u32 s45, s45, -1\n" \
"ds_read_b128 a[92:95], v206 offset:8192\n" \
"v_mfma_f32_16x16x32_bf16 v[156:159], a[248:251], v[16:19], v[156:159]\n" \
"s_add_u32 m0, s85, 16384\n" \
"v_mfma_f32_16x16x32_bf16 v[156:159], a[252:255], v[48:51], v[156:159]\n" \
"s_waitcnt lgkmcnt(4)\n" \
"buffer_load_dwordx4 v246, s[12:15], s46 offen lds\n" \
"s_branch Ljoin_%=\n" \
"Lexact_%=:\n" \
"v_mov_b32 v190, 0xff800000\n" \
"v_add_u32 v191, 0x1c90080, v249\n" \
"v_cndmask_b32 v191, v249, v191, s[16:17]\n" \
"s_and_b32 s80, s61, 2\n" \
"s_lshl_b32 s80, s80, 4\n" \
"v_subrev_u32 v186, s80, v251\n" \
"v_accvgpr_read_b32 v160, a128\n" \
"v_accvgpr_read_b32 v161, a129\n" \
"v_accvgpr_read_b32 v162, a130\n" \
"v_accvgpr_read_b32 v163, a131\n" \
"v_accvgpr_read_b32 v164, a132\n" \
"v_accvgpr_read_b32 v165, a133\n" \
"v_accvgpr_read_b32 v166, a134\n" \
"v_accvgpr_read_b32 v167, a135\n" \
"v_accvgpr_read_b32 v168, a136\n" \
"v_accvgpr_read_b32 v169, a137\n" \
"v_accvgpr_read_b32 v170, a138\n" \
"v_accvgpr_read_b32 v171, a139\n" \
"v_accvgpr_read_b32 v172, a140\n" \
"v_accvgpr_read_b32 v173, a141\n" \
"v_accvgpr_read_b32 v174, a142\n" \
"v_accvgpr_read_b32 v175, a143\n" \
"s_lshl_b32 s75, s63, 2\n" \
"s_lshr_b32 s80, s61, 1\n" \
"s_add_u32 s75, s75, s80\n" \
"s_add_u32 s75, s75, 0\n" \
"s_add_u32 s76, s77, 0\n" \
"s_call_b64 s[68:69], Lex1_%=\n" \
"v_accvgpr_read_b32 v160, a144\n" \
"v_accvgpr_read_b32 v161, a145\n" \
"v_accvgpr_read_b32 v162, a146\n" \
"v_accvgpr_read_b32 v163, a147\n" \
"v_accvgpr_read_b32 v164, a148\n" \
"v_accvgpr_read_b32 v165, a149\n" \
"v_accvgpr_read_b32 v166, a150\n" \
"v_accvgpr_read_b32 v167, a151\n" \
"v_accvgpr_read_b32 v168, a152\n" \
"v_accvgpr_read_b32 v169, a153\n" \
"v_accvgpr_read_b32 v170, a154\n" \
"v_accvgpr_read_b32 v171, a155\n" \
"v_accvgpr_read_b32 v172, a156\n" \
"v_accvgpr_read_b32 v173, a157\n" \
"v_accvgpr_read_b32 v174, a158\n" \
"v_accvgpr_read_b32 v175, a159\n" \
"s_lshl_b32 s75, s63, 2\n" \
"s_lshr_b32 s80, s61, 1\n" \
"s_add_u32 s75, s75, s80\n" \
"s_add_u32 s75, s75, 2\n" \
"s_add_u32 s76, s77, 532480\n" \
"s_call_b64 s[68:69], Lex1_%=\n" \
"v_accvgpr_read_b32 v160, a160\n" \
"v_accvgpr_read_b32 v161, a161\n" \
"v_accvgpr_read_b32 v162, a162\n" \
"v_accvgpr_read_b32 v163, a163\n" \
"v_accvgpr_read_b32 v164, a164\n" \
"v_accvgpr_read_b32 v165, a165\n" \
"v_accvgpr_read_b32 v166, a166\n" \
"v_accvgpr_read_b32 v167, a167\n" \
"v_accvgpr_read_b32 v168, a168\n" \
"v_accvgpr_read_b32 v169, a169\n" \
"v_accvgpr_read_b32 v170, a170\n" \
"v_accvgpr_read_b32 v171, a171\n" \
"v_accvgpr_read_b32 v172, a172\n" \
"v_accvgpr_read_b32 v173, a173\n" \
"v_accvgpr_read_b32 v174, a174\n" \
"v_accvgpr_read_b32 v175, a175\n" \
"s_lshl_b32 s75, s63, 2\n" \
"s_sub_u32 s75, 60, s75\n" \
"s_lshr_b32 s80, s61, 1\n" \
"s_add_u32 s75, s75, s80\n" \
"s_add_u32 s75, s75, 0\n" \
"s_add_u32 s76, s78, 0\n" \
"s_call_b64 s[68:69], Lex1_%=\n" \
"v_accvgpr_read_b32 v160, a176\n" \
"v_accvgpr_read_b32 v161, a177\n" \
"v_accvgpr_read_b32 v162, a178\n" \
"v_accvgpr_read_b32 v163, a179\n" \
"v_accvgpr_read_b32 v164, a180\n" \
"v_accvgpr_read_b32 v165, a181\n" \
"v_accvgpr_read_b32 v166, a182\n" \
"v_accvgpr_read_b32 v167, a183\n" \
"v_accvgpr_read_b32 v168, a184\n" \
"v_accvgpr_read_b32 v169, a185\n" \
"v_accvgpr_read_b32 v170, a186\n" \
"v_accvgpr_read_b32 v171, a187\n" \
"v_accvgpr_read_b32 v172, a188\n" \
"v_accvgpr_read_b32 v173, a189\n" \
"v_accvgpr_read_b32 v174, a190\n" \
"v_accvgpr_read_b32 v175, a191\n" \
"s_lshl_b32 s75, s63, 2\n" \
"s_sub_u32 s75, 60, s75\n" \
"s_lshr_b32 s80, s61, 1\n" \
"s_add_u32 s75, s75, s80\n" \
"s_add_u32 s75, s75, 2\n" \
"s_add_u32 s76, s78, 532480\n" \
"s_call_b64 s[68:69], Lex1_%=\n" \
"s_branch Lexr_%=\n" \
"Lex1_%=:\n" \
"v_mov_b32 v128, 0\n" \
"v_mov_b32 v129, 0\n" \
"v_mov_b32 v130, 0\n" \
"v_mov_b32 v131, 0\n" \
"v_mov_b32 v132, 0\n" \
"v_mov_b32 v133, 0\n" \
"v_mov_b32 v134, 0\n" \
"v_mov_b32 v135, 0\n" \
"v_mov_b32 v136, 0\n" \
"v_mov_b32 v137, 0\n" \
"v_mov_b32 v138, 0\n" \
"v_mov_b32 v139, 0\n" \
"v_mov_b32 v140, 0\n" \
"v_mov_b32 v141, 0\n" \
"v_mov_b32 v142, 0\n" \
"v_mov_b32 v143, 0\n" \
"v_mov_b32 v144, 0\n" \
"v_mov_b32 v145, 0\n" \
"v_mov_b32 v146, 0\n" \
"v_mov_b32 v147, 0\n" \
"v_mov_b32 v148, 0\n" \
"v_mov_b32 v149, 0\n" \
"v_mov_b32 v150, 0\n" \
"v_mov_b32 v151, 0\n" \
"v_mov_b32 v152, 0\n" \
"v_mov_b32 v153, 0\n" \
"v_mov_b32 v154, 0\n" \
"v_mov_b32 v155, 0\n" \
"v_mov_b32 v156, 0\n" \
"v_mov_b32 v157, 0\n" \
"v_mov_b32 v158, 0\n" \
"v_mov_b32 v159, 0\n" \
"v_mov_b32 v184, 0xf149f2ca\n" \
"v_mov_b32 v185, 0\n" \
"s_mov_b32 s73, 0\n" \
"s_mul_i32 s74, s62, 557824\n" \
"Lex2_%=:\n" \
"s_add_u32 s80, s74, 4096\n" \
"buffer_load_dwordx4 a[0:3], v249, s[56:59], s74 offen offset:0 sc1\n" \
"buffer_load_dwordx4 a[4:7], v249, s[56:59], s74 offen offset:2048 sc1\n" \
"buffer_load_dwordx4 a[8:11], v249, s[56:59], s80 offen offset:0 sc1\n" \
"buffer_load_dwordx4 a[12:15], v249, s[56:59], s80 offen offset:2048 sc1\n" \
"buffer_load_dwordx4 a[16:19], v249, s[56:59], s74 offen offset:256 sc1\n" \
"buffer_load_dwordx4 a[20:23], v249, s[56:59], s74 offen offset:2304 sc1\n" \
"buffer_load_dwordx4 a[24:27], v249, s[56:59], s80 offen offset:256 sc1\n" \
"buffer_load_dwordx4 a[28:31], v249, s[56:59], s80 offen offset:2304 sc1\n" \
"s_add_u32 s80, s74, 17850368\n" \
"s_add_u32 s81, s80, 4096\n" \
"buffer_load_dwordx4 a[32:35], v248, s[56:59], s80 offen offset:0 sc1\n" \
"buffer_load_dwordx4 a[36:39], v248, s[56:59], s80 offen offset:1024 sc1\n" \
"buffer_load_dwordx4 a[40:43], v248, s[56:59], s80 offen offset:2048 sc1\n" \
"buffer_load_dwordx4 a[44:47], v248, s[56:59], s80 offen offset:3072 sc1\n" \
"buffer_load_dwordx4 a[48:51], v248, s[56:59], s81 offen offset:0 sc1\n" \
"buffer_load_dwordx4 a[52:55], v248, s[56:59], s81 offen offset:1024 sc1\n" \
"buffer_load_dwordx4 a[56:59], v248, s[56:59], s81 offen offset:2048 sc1\n" \
"buffer_load_dwordx4 a[60:63], v248, s[56:59], s81 offen offset:3072 sc1\n" \
"s_waitcnt vmcnt(0)\n" \
"v_mfma_f32_16x16x32_bf16 v[176:179], a[0:3], v[160:163], 0\n" \
"v_mfma_f32_16x16x32_bf16 v[176:179], a[4:7], v[164:167], v[176:179]\n" \
"v_mfma_f32_16x16x32_bf16 v[176:179], a[8:11], v[168:171], v[176:179]\n" \
"v_mfma_f32_16x16x32_bf16 v[176:179], a[12:15], v[172:175], v[176:179]\n" \
"v_mfma_f32_16x16x32_bf16 v[180:183], a[16:19], v[160:163], 0\n" \
"v_mfma_f32_16x16x32_bf16 v[180:183], a[20:23], v[164:167], v[180:183]\n" \
"v_mfma_f32_16x16x32_bf16 v[180:183], a[24:27], v[168:171], v[180:183]\n" \
"v_mfma_f32_16x16x32_bf16 v[180:183], a[28:31], v[172:175], v[180:183]\n" \
"s_nop 7\n" \
"s_nop 1\n" \
"s_cmp_eq_u32 s73, s75\n" \
"s_cbranch_scc0 Lex3_%=\n" \
"v_cmp_gt_i32 vcc, 0, v186\n" \
"s_nop 1\n" \
"v_cndmask_b32 v176, v176, v190, vcc\n" \
"v_cmp_gt_i32 vcc, 1, v186\n" \
"s_nop 1\n" \
"v_cndmask_b32 v177, v177, v190, vcc\n" \
"v_cmp_gt_i32 vcc, 2, v186\n" \
"s_nop 1\n" \
"v_cndmask_b32 v178, v178, v190, vcc\n" \
"v_cmp_gt_i32 vcc, 3, v186\n" \
"s_nop 1\n" \
"v_cndmask_b32 v179, v179, v190, vcc\n" \
"v_cmp_gt_i32 vcc, 16, v186\n" \
"s_nop 1\n" \
"v_cndmask_b32 v180, v180, v190, vcc\n" \
"v_cmp_gt_i32 vcc, 17, v186\n" \
"s_nop 1\n" \
"v_cndmask_b32 v181, v181, v190, vcc\n" \
"v_cmp_gt_i32 vcc, 18, v186\n" \
"s_nop 1\n" \
"v_cndmask_b32 v182, v182, v190, vcc\n" \
"v_cmp_gt_i32 vcc, 19, v186\n" \
"s_nop 1\n" \
"v_cndmask_b32 v183, v183, v190, vcc\n" \
"Lex3_%=:\n" \
"v_max3_f32 v187, v176, v177, v178\n" \
"v_max3_f32 v187, v187, v179, v180\n" \
"v_max3_f32 v187, v187, v181, v182\n" \
"v_max3_f32 v187, v187, v183, v184\n" \
"v_mov_b32 v188, v187\n" \
"s_nop 1\n" \
"v_permlane16_swap_b32 v188, v187\n" \
"v_max_f32 v187, v187, v188\n" \
"v_mov_b32 v188, v187\n" \
"s_nop 1\n" \
"v_permlane32_swap_b32 v188, v187\n" \
"v_max_f32 v187, v187, v188\n" \
"v_sub_f32 v189, v184, v187\n" \
"v_exp_f32 v189, v189\n" \
"v_mov_b32 v184, v187\n" \
"v_sub_f32 v176, v176, v187\n" \
"v_sub_f32 v177, v177, v187\n" \
"v_sub_f32 v178, v178, v187\n" \
"v_sub_f32 v179, v179, v187\n" \
"v_sub_f32 v180, v180, v187\n" \
"v_sub_f32 v181, v181, v187\n" \
"v_sub_f32 v182, v182, v187\n" \
"v_sub_f32 v183, v183, v187\n" \
"v_exp_f32 v176, v176\n" \
"v_exp_f32 v177, v177\n" \
"v_exp_f32 v178, v178\n" \
"v_exp_f32 v179, v179\n" \
"v_exp_f32 v180, v180\n" \
"v_exp_f32 v181, v181\n" \
"v_exp_f32 v182, v182\n" \
"v_exp_f32 v183, v183\n" \
"v_mul_f32 v185, v185, v189\n" \
"v_add_f32 v185, v185, v176\n" \
"v_add_f32 v185, v185, v177\n" \
"v_add_f32 v185, v185, v178\n" \
"v_add_f32 v185, v185, v179\n" \
"v_add_f32 v185, v185, v180\n" \
"v_add_f32 v185, v185, v181\n" \
"v_add_f32 v185, v185, v182\n" \
"v_add_f32 v185, v185, v183\n" \
"v_cvt_pk_bf16_f32 v112, v176, v177\n" \
"v_cvt_pk_bf16_f32 v113, v178, v179\n" \
"v_cvt_pk_bf16_f32 v114, v180, v181\n" \
"v_cvt_pk_bf16_f32 v115, v182, v183\n" \
"v_mul_f32 v128, v128, v189\n" \
"v_mul_f32 v129, v129, v189\n" \
"v_mul_f32 v130, v130, v189\n" \
"v_mul_f32 v131, v131, v189\n" \
"v_mul_f32 v132, v132, v189\n" \
"v_mul_f32 v133, v133, v189\n" \
"v_mul_f32 v134, v134, v189\n" \
"v_mul_f32 v135, v135, v189\n" \
"v_mul_f32 v136, v136, v189\n" \
"v_mul_f32 v137, v137, v189\n" \
"v_mul_f32 v138, v138, v189\n" \
"v_mul_f32 v139, v139, v189\n" \
"v_mul_f32 v140, v140, v189\n" \
"v_mul_f32 v141, v141, v189\n" \
"v_mul_f32 v142, v142, v189\n" \
"v_mul_f32 v143, v143, v189\n" \
"v_mul_f32 v144, v144, v189\n" \
"v_mul_f32 v145, v145, v189\n" \
"v_mul_f32 v146, v146, v189\n" \
"v_mul_f32 v147, v147, v189\n" \
"v_mul_f32 v148, v148, v189\n" \
"v_mul_f32 v149, v149, v189\n" \
"v_mul_f32 v150, v150, v189\n" \
"v_mul_f32 v151, v151, v189\n" \
"v_mul_f32 v152, v152, v189\n" \
"v_mul_f32 v153, v153, v189\n" \
"v_mul_f32 v154, v154, v189\n" \
"v_mul_f32 v155, v155, v189\n" \
"v_mul_f32 v156, v156, v189\n" \
"v_mul_f32 v157, v157, v189\n" \
"v_mul_f32 v158, v158, v189\n" \
"v_mul_f32 v159, v159, v189\n" \
"s_nop 1\n" \
"v_mfma_f32_16x16x32_bf16 v[128:131], a[32:35], v[112:115], v[128:131]\n" \
"v_mfma_f32_16x16x32_bf16 v[132:135], a[36:39], v[112:115], v[132:135]\n" \
"v_mfma_f32_16x16x32_bf16 v[136:139], a[40:43], v[112:115], v[136:139]\n" \
"v_mfma_f32_16x16x32_bf16 v[140:143], a[44:47], v[112:115], v[140:143]\n" \
"v_mfma_f32_16x16x32_bf16 v[144:147], a[48:51], v[112:115], v[144:147]\n" \
"v_mfma_f32_16x16x32_bf16 v[148:151], a[52:55], v[112:115], v[148:151]\n" \
"v_mfma_f32_16x16x32_bf16 v[152:155], a[56:59], v[112:115], v[152:155]\n" \
"v_mfma_f32_16x16x32_bf16 v[156:159], a[60:63], v[112:115], v[156:159]\n" \
"s_nop 7\n" \
"s_nop 1\n" \
"s_and_b32 s80, s73, 1\n" \
"s_mul_i32 s80, s80, 1024\n" \
"s_add_u32 s74, s74, s80\n" \
"s_add_u32 s74, s74, 8192\n" \
"s_add_u32 s73, s73, 1\n" \
"s_cmp_le_u32 s73, s75\n" \
"s_cbranch_scc1 Lex2_%=\n" \
"v_mov_b32 v187, v185\n" \
"s_nop 1\n" \
"v_permlane16_swap_b32 v187, v185\n" \
"v_add_f32 v185, v185, v187\n" \
"v_mov_b32 v187, v185\n" \
"s_nop 1\n" \
"v_permlane32_swap_b32 v187, v185\n" \
"v_add_f32 v185, v185, v187\n" \
"v_rcp_f32 v185, v185\n" \
"s_nop 0\n" \
"v_mul_f32 v128, v128, v185\n" \
"v_mul_f32 v129, v129, v185\n" \
"v_mul_f32 v130, v130, v185\n" \
"v_mul_f32 v131, v131, v185\n" \
"v_mul_f32 v132, v132, v185\n" \
"v_mul_f32 v133, v133, v185\n" \
"v_mul_f32 v134, v134, v185\n" \
"v_mul_f32 v135, v135, v185\n" \
"v_mul_f32 v136, v136, v185\n" \
"v_mul_f32 v137, v137, v185\n" \
"v_mul_f32 v138, v138, v185\n" \
"v_mul_f32 v139, v139, v185\n" \
"v_mul_f32 v140, v140, v185\n" \
"v_mul_f32 v141, v141, v185\n" \
"v_mul_f32 v142, v142, v185\n" \
"v_mul_f32 v143, v143, v185\n" \
"v_mul_f32 v144, v144, v185\n" \
"v_mul_f32 v145, v145, v185\n" \
"v_mul_f32 v146, v146, v185\n" \
"v_mul_f32 v147, v147, v185\n" \
"v_mul_f32 v148, v148, v185\n" \
"v_mul_f32 v149, v149, v185\n" \
"v_mul_f32 v150, v150, v185\n" \
"v_mul_f32 v151, v151, v185\n" \
"v_mul_f32 v152, v152, v185\n" \
"v_mul_f32 v153, v153, v185\n" \
"v_mul_f32 v154, v154, v185\n" \
"v_mul_f32 v155, v155, v185\n" \
"v_mul_f32 v156, v156, v185\n" \
"v_mul_f32 v157, v157, v185\n" \
"v_mul_f32 v158, v158, v185\n" \
"v_mul_f32 v159, v159, v185\n" \
"s_add_u32 s80, s76, 4096\n" \
"v_cvt_pk_bf16_f32 v112, v128, v129\n" \
"v_cvt_pk_bf16_f32 v113, v130, v131\n" \
"v_cvt_pk_bf16_f32 v114, v132, v133\n" \
"v_cvt_pk_bf16_f32 v115, v134, v135\n" \
"buffer_store_dwordx4 v[112:115], v191, s[56:59], s76 offen offset:0 sc1\n" \
"v_cvt_pk_bf16_f32 v116, v136, v137\n" \
"v_cvt_pk_bf16_f32 v117, v138, v139\n" \
"v_cvt_pk_bf16_f32 v118, v140, v141\n" \
"v_cvt_pk_bf16_f32 v119, v142, v143\n" \
"buffer_store_dwordx4 v[116:119], v191, s[56:59], s76 offen offset:2048 sc1\n" \
"v_cvt_pk_bf16_f32 v120, v144, v145\n" \
"v_cvt_pk_bf16_f32 v121, v146, v147\n" \
"v_cvt_pk_bf16_f32 v122, v148, v149\n" \
"v_cvt_pk_bf16_f32 v123, v150, v151\n" \
"buffer_store_dwordx4 v[120:123], v191, s[56:59], s80 offen offset:0 sc1\n" \
"v_cvt_pk_bf16_f32 v124, v152, v153\n" \
"v_cvt_pk_bf16_f32 v125, v154, v155\n" \
"v_cvt_pk_bf16_f32 v126, v156, v157\n" \
"v_cvt_pk_bf16_f32 v127, v158, v159\n" \
"buffer_store_dwordx4 v[124:127], v191, s[56:59], s80 offen offset:2048 sc1\n" \
"s_waitcnt vmcnt(0)\n" \
"s_setpc_b64 s[68:69]\n" \
"Lpl2_%=:\n" \
"s_mov_b32 s31, 0\n" \
"Lpk2_%=:\n" \
"s_sleep 2\n" \
"buffer_load_dword v220, v221, s[56:59], s81 offen sc1\n" \
"s_waitcnt vmcnt(0)\n" \
"v_cmp_ne_u32 vcc, s60, v220\n" \
"s_nop 1\n" \
"s_and_b32 vcc_hi, vcc_lo, s80\n" \
"s_cbranch_scc0 Lpr2_%=\n" \
"s_add_u32 s31, s31, 1\n" \
"s_cmp_lt_u32 s31, 0x40000\n" \
"s_cbranch_scc1 Lpk2_%=\n" \
"s_branch Lpr2_%=\n" \
"Lpl3_%=:\n" \
"s_mov_b32 s31, 0\n" \
"Lpk3_%=:\n" \
"s_sleep 2\n" \
"buffer_load_dwordx2 v[226:227], v228, s[56:59], s30 offen sc1\n" \
"s_waitcnt vmcnt(0)\n" \
"v_xor_b32 v226, v226, v227\n" \
"v_cmp_ne_u32 vcc, s60, v226\n" \
"s_nop 1\n" \
"s_and_b32 vcc_hi, vcc_lo, -1\n" \
"s_cbranch_scc0 Lpr3_%=\n" \
"s_add_u32 s31, s31, 1\n" \
"s_cmp_lt_u32 s31, 0x40000\n" \
"s_cbranch_scc1 Lpk3_%=\n" \
"s_branch Lpr3_%=\n" \
"Lpl4_%=:\n" \
"s_mov_b32 s31, 0\n" \
"Lpk4_%=:\n" \
"s_sleep 2\n" \
"buffer_load_dword v220, off, s[56:59], s79 sc1\n" \
"s_waitcnt vmcnt(0)\n" \
"v_cmp_ne_u32 vcc, s60, v220\n" \
"s_nop 1\n" \
"s_and_b32 vcc_hi, vcc_lo, 1\n" \
"s_cbranch_scc0 Lpr4_%=\n" \
"s_add_u32 s31, s31, 1\n" \
"s_cmp_lt_u32 s31, 0x40000\n" \
"s_cbranch_scc1 Lpk4_%=\n" \
"s_branch Lpr4_%=\n" \
"Lpl5_%=:\n" \
"s_mov_b32 s31, 0\n" \
"Lpk5_%=:\n" \
"s_sleep 2\n" \
"buffer_load_dword v220, v221, s[56:59], s82 offen sc1\n" \
"s_waitcnt vmcnt(0)\n" \
"v_cmp_ne_u32 vcc, s60, v220\n" \
"s_nop 1\n" \
"s_and_b32 vcc_hi, vcc_lo, 65535\n" \
"s_cbranch_scc0 Lpr5_%=\n" \
"s_add_u32 s31, s31, 1\n" \
"s_cmp_lt_u32 s31, 0x40000\n" \
"s_cbranch_scc1 Lpk5_%=\n" \
"s_branch Lpr5_%=\n" \
"Lcode_end_%=:\n" \
".p2align 12\n" \
""
#define MK_ASM_CLOBBERS "v1", "v2", "v3", "v4", "v5", "v6", "v7", "v8", "v9", "v10", "v11", "v12", "v13", "v14", "v15", "v16", "v17", "v18", "v19", "v20", "v21", "v22", "v23", "v24", "v25", "v26", "v27", "v28", "v29", "v30", "v31", "v32", "v33", "v34", "v35", "v36", "v37", "v38", "v39", "v40", "v41", "v42", "v43", "v44", "v45", "v46", "v47", "v48", "v49", "v50", "v51", "v52", "v53", "v54", "v55", "v56", "v57", "v58", "v59", "v60", "v61", "v62", "v63", "v64", "v65", "v66", "v67", "v68", "v69", "v70", "v71", "v72", "v73", "v74", "v75", "v76", "v77", "v78", "v79", "v80", "v81", "v82", "v83", "v84", "v85", "v86", "v87", "v88", "v89", "v90", "v91", "v92", "v93", "v94", "v95", "v96", "v97", "v98", "v99", "v100", "v101", "v102", "v103", "v104", "v105", "v106", "v107", "v108", "v109", "v110", "v111", "v112", "v113", "v114", "v115", "v116", "v117", "v118", "v119", "v120", "v121", "v122", "v123", "v124", "v125", "v126", "v127", "v128", "v129", "v130", "v131", "v132", "v133", "v134", "v135", "v136", "v137", "v138", "v139", "v140", "v141", "v142", "v143", "v144", "v145", "v146", "v147", "v148", "v149", "v150", "v151", "v152", "v153", "v154", "v155", "v156", "v157", "v158", "v159", "v160", "v161", "v162", "v163", "v164", "v165", "v166", "v167", "v168", "v169", "v170", "v171", "v172", "v173", "v174", "v175", "v176", "v177", "v178", "v179", "v180", "v181", "v182", "v183", "v184", "v185", "v186", "v187", "v188", "v189", "v190", "v191", "v192", "v193", "v194", "v195", "v196", "v197", "v198", "v199", "v200", "v201", "v202", "v203", "v204", "v205", "v206", "v207", "v208", "v209", "v210", "v211", "v212", "v213", "v214", "v215", "v216", "v217", "v218", "v219", "v220", "v221", "v222", "v223", "v224", "v225", "v226", "v227", "v228", "v229", "v230", "v231", "v232", "v233", "v234", "v235", "v236", "v237", "v238", "v239", "v240", "v241", "v242", "v243", "v244", "v245", "v246", "v247", "v248", "v249", "v250", "v251", "v252", "v253", "v254", "v255", "a0", "a1", "a2", "a3", "a4", "a5", "a6", "a7", "a8", "a9", "a10", "a11", "a12", "a13", "a14", "a15", "a16", "a17", "a18", "a19", "a20", "a21", "a22", "a23", "a24", "a25", "a26", "a27", "a28", "a29", "a30", "a31", "a32", "a33", "a34", "a35", "a36", "a37", "a38", "a39", "a40", "a41", "a42", "a43", "a44", "a45", "a46", "a47", "a48", "a49", "a50", "a51", "a52", "a53", "a54", "a55", "a56", "a57", "a58", "a59", "a60", "a61", "a62", "a63", "a64", "a65", "a66", "a67", "a68", "a69", "a70", "a71", "a72", "a73", "a74", "a75", "a76", "a77", "a78", "a79", "a80", "a81", "a82", "a83", "a84", "a85", "a86", "a87", "a88", "a89", "a90", "a91", "a92", "a93", "a94", "a95", "a96", "a97", "a98", "a99", "a100", "a101", "a102", "a103", "a104", "a105", "a106", "a107", "a108", "a109", "a110", "a111", "a112", "a113", "a114", "a115", "a116", "a117", "a118", "a119", "a120", "a121", "a122", "a123", "a124", "a125", "a126", "a127", "a128", "a129", "a130", "a131", "a132", "a133", "a134", "a135", "a136", "a137", "a138", "a139", "a140", "a141", "a142", "a143", "a144", "a145", "a146", "a147", "a148", "a149", "a150", "a151", "a152", "a153", "a154", "a155", "a156", "a157", "a158", "a159", "a160", "a161", "a162", "a163", "a164", "a165", "a166", "a167", "a168", "a169", "a170", "a171", "a172", "a173", "a174", "a175", "a176", "a177", "a178", "a179", "a180", "a181", "a182", "a183", "a184", "a185", "a186", "a187", "a188", "a189", "a190", "a191", "a192", "a193", "a194", "a195", "a196", "a197", "a198", "a199", "a200", "a201", "a202", "a203", "a204", "a205", "a206", "a207", "a208", "a209", "a210", "a211", "a212", "a213", "a214", "a215", "a216", "a217", "a218", "a219", "a220", "a221", "a222", "a223", "a224", "a225", "a226", "a227", "a228", "a229", "a230", "a231", "a232", "a233", "a234", "a235", "a236", "a237", "a238", "a239", "a240", "a241", "a242", "a243", "a244", "a245", "a246", "a247", "a248", "a249", "a250", "a251", "a252", "a253", "a254", "a255", "s8", "s9", "s10", "s11", "s12", "s13", "s14", "s15", "s16", "s17", "s18", "s19", "s20", "s21", "s22", "s23", "s24", "s25", "s26", "s27", "s28", "s29", "s30", "s31", "s36", "s37", "s38", "s39", "s40", "s41", "s42", "s43", "s44", "s45", "s46", "s47", "s48", "s49", "s50", "s51", "s52", "s53", "s54", "s55", "s56", "s57", "s58", "s59", "s60", "s61", "s62", "s63", "s64", "s65", "s66", "s67", "s68", "s69", "s70", "s71", "s72", "s73", "s74", "s75", "s76", "s77", "s78", "s79", "s80", "s81", "s82", "s83", "s84", "s85", "s86", "s87", "s88", "s89", "s90", "s91", "s92", "s93", "s94", "s95", "vcc", "scc", "memory"
namespace mk {
constexpr int LDS_BYTES = 163840;
struct Args { const void* in[9]; void* out; void* ws; };
__global__ __launch_bounds__(256, 1) void mega_k(const Args a) {
    extern __shared__ __attribute__((aligned(16))) unsigned char shm[];
    const unsigned ldsb = (unsigned)(size_t)(LAS unsigned char*)shm;
    asm volatile(MK_ASM_BODY
                 :
                 : "s"(__builtin_amdgcn_kernarg_segment_ptr()), "s"(blockIdx.x), "v"(threadIdx.x), "s"(ldsb)
                 : MK_ASM_CLOBBERS);
}
}
extern "C" void kernel_launch(void* const* d_in, const int* in_sizes, int n_in, void* d_out, int out_size, void* d_ws, size_t ws_size, hipStream_t stream) {
    (void)hipFuncSetAttribute((const void*)mk::mega_k, hipFuncAttributeMaxDynamicSharedMemorySize, mk::LDS_BYTES);
    mk::Args a;
    for (int i = 0; i < 9; ++i) a.in[i] = d_in[i];
    a.out = d_out; a.ws = (char*)d_ws + ((ws_size - WS_END) & ~(size_t)0x1fffff);
    mk::mega_k<<<256, 256, mk::LDS_BYTES, stream>>>(a);
}
```

```cpp
#include <hip/hip_runtime.h>
#include <stdint.h>
#define LAS __attribute__((address_space(3)))
constexpr size_t LAY_TS = 17408, LAY_HS = 557824, LAY_CRS = 266240, LAY_ORS = 69632, WS_K = 0, WS_V = 17850368, WS_CTX = 35700736, WS_OB = 52740096, WS_FL = 65585152, LAY_EXD = 29950080, WS_END = 87147648;
#define MK_ASM_BODY \
"s_load_dwordx16 s[8:23], %0, 0x0\n" \
"s_load_dwordx4 s[24:27], %0, 0x40\n" \
"s_load_dwordx2 s[28:29], %0, 0x50\n" \
"v_readfirstlane_b32 s61, %2\n" \
"s_lshr_b32 s61, s61, 6\n" \
"v_mbcnt_lo_u32_b32 v52, -1, 0\n" \
"v_mbcnt_hi_u32_b32 v52, -1, v52\n" \
"v_lshlrev_b32 v248, 4, v52\n" \
"s_getpc_b64 s[64:65]\n" \
"Lpc_%=:\n" \
"s_lshr_b32 s70, %1, 3\n" \
"s_lshl_b32 s70, s70, 2\n" \
"s_add_u32 s70, s70, s61\n" \
"s_and_b32 s71, s70, 127\n" \
"s_lshl_b32 s71, s71, 10\n" \
"v_lshl_add_u32 v53, v248, 0, s71\n" \
"s_mov_b32 s71, Lcode_end_%=-Lpc_%=-4\n" \
"v_min_u32 v53, s71, v53\n" \
"global_load_dword v253, v53, s[64:65]\n" \
"s_and_b32 s62, %1, 7\n" \
"s_lshl_b32 s62, s62, 2\n" \
"s_lshr_b32 s70, %1, 6\n" \
"s_add_u32 s62, s62, s70\n" \
"s_bfe_u32 s63, %1, 0x30003\n" \
"s_lshl_b32 s87, s61, 13\n" \
"v_bfe_u32 v53, v52, 4, 1\n" \
"v_bfe_u32 v54, v52, 2, 2\n" \
"v_lshl_add_u32 v54, v53, 3, v54\n" \
"s_lshl_b32 s71, s61, 4\n" \
"v_add_u32 v54, s71, v54\n" \
"v_lshlrev_b32 v54, 13, v54\n" \
"v_lshlrev_b32 v53, 1, v53\n" \
"v_and_b32 v55, 3, v52\n" \
"v_xor_b32 v53, v53, v55\n" \
"v_lshl_add_u32 v54, v53, 4, v54\n" \
"v_lshrrev_b32 v53, 5, v52\n" \
"v_lshl_add_u32 v54, v53, 6, v54\n" \
"v_add_u32 v40, 4096, v54\n" \
"v_add_u32 v41, 3072, v54\n" \
"v_add_u32 v42, 2176, v54\n" \
"v_add_u32 v43, 1152, v54\n" \
"v_add_u32 v44, 32768, v54\n" \
"v_add_u32 v45, 31744, v54\n" \
"v_add_u32 v46, 30848, v54\n" \
"v_add_u32 v47, 29824, v54\n" \
"v_lshrrev_b32 v55, 3, v52\n" \
"v_and_b32 v56, 7, v52\n" \
"v_xor_b32 v56, v56, v55\n" \
"v_lshlrev_b32 v56, 4, v56\n" \
"v_lshl_add_u32 v57, v55, 10, v56\n" \
"v_add_u32 v32, 528384, v57\n" \
"v_add_u32 v33, 535552, v57\n" \
"v_add_u32 v34, 542720, v57\n" \
"v_add_u32 v35, 549888, v57\n" \
"v_add_u32 v36, 0, v57\n" \
"v_add_u32 v37, 7168, v57\n" \
"v_add_u32 v38, 14336, v57\n" \
"v_add_u32 v39, 21504, v57\n" \
"s_lshl_b32 s88, s62, 8\n" \
"s_lshl_b32 s70, s63, 16\n" \
"s_and_b32 s71, s87, 0x4000\n" \
"s_lshl_b32 s71, s71, 1\n" \
"s_add_u32 s70, s70, s71\n" \
"s_andn2_b32 s71, 1, s61\n" \
"s_lshl_b32 s71, s71, 20\n" \
"s_add_u32 s70, s70, s71\n" \
"s_add_u32 s73, s63, s62\n" \
"s_and_b32 s73, s73, 1\n" \
"s_mov_b32 s74, 128\n" \
"s_cmp_lg_u32 s73, 0\n" \
"s_cselect_b64 exec, -1, 0\n" \
"v_swap_b32 v32, v40\n" \
"v_swap_b32 v33, v41\n" \
"v_swap_b32 v34, v42\n" \
"v_swap_b32 v35, v43\n" \
"v_swap_b32 v36, v44\n" \
"v_swap_b32 v37, v45\n" \
"v_swap_b32 v38, v46\n" \
"v_swap_b32 v39, v47\n" \
"s_mov_b64 exec, -1\n" \
"s_cselect_b32 s76, s70, s88\n" \
"s_cselect_b32 s77, s88, s70\n" \
"s_cselect_b32 s75, 524288, s74\n" \
"s_cselect_b32 s74, s74, 524288\n" \
"s_waitcnt lgkmcnt(0)\n" \
"s_cselect_b64 s[52:53], s[10:11], s[14:15]\n" \
"s_cselect_b64 s[54:55], s[14:15], s[10:11]\n" \
"s_cselect_b64 s[50:51], s[10:11], s[16:17]\n" \
"s_cselect_b64 s[40:41], s[16:17], s[10:11]\n" \
"s_add_u32 s52, s52, s76\n" \
"s_addc_u32 s53, s53, 0\n" \
"s_add_u32 s50, s50, s76\n" \
"s_addc_u32 s51, s51, 0\n" \
"s_add_u32 m0, s87, 36864\n" \
"s_nop 0\n" \
"global_load_lds_dwordx4 v40, s[52:53] offset:-4096\n" \
"s_add_u32 s54, s54, s77\n" \
"s_addc_u32 s55, s55, 0\n" \
"s_add_u32 s40, s40, s77\n" \
"s_addc_u32 s41, s41, 0\n" \
"global_load_lds_dwordx4 v41, s[50:51] offset:-3072\n" \
"global_load_lds_dwordx4 v42, s[52:53] offset:-2048\n" \
"global_load_lds_dwordx4 v43, s[50:51] offset:-1024\n" \
"global_load_lds_dwordx4 v44, s[52:53] offset:0\n" \
"global_load_lds_dwordx4 v45, s[50:51] offset:1024\n" \
"global_load_lds_dwordx4 v46, s[52:53] offset:2048\n" \
"global_load_lds_dwordx4 v47, s[50:51] offset:3072\n" \
"s_add_u32 s52, s52, s74\n" \
"s_addc_u32 s53, s53, 0\n" \
"s_add_u32 s50, s50, s74\n" \
"s_addc_u32 s51, s51, 0\n" \
"s_add_u32 m0, s87, 4096\n" \
"s_nop 0\n" \
"global_load_lds_dwordx4 v32, s[54:55] offset:-4096\n" \
"global_load_lds_dwordx4 v33, s[40:41] offset:-3072\n" \
"global_load_lds_dwordx4 v34, s[54:55] offset:-2048\n" \
"global_load_lds_dwordx4 v35, s[40:41] offset:-1024\n" \
"global_load_lds_dwordx4 v36, s[54:55] offset:0\n" \
"global_load_lds_dwordx4 v37, s[40:41] offset:1024\n" \
"global_load_lds_dwordx4 v38, s[54:55] offset:2048\n" \
"global_load_lds_dwordx4 v39, s[40:41] offset:3072\n" \
"s_add_u32 s54, s54, s75\n" \
"s_addc_u32 s55, s55, 0\n" \
"s_add_u32 s40, s40, s75\n" \
"s_addc_u32 s41, s41, 0\n" \
"s_mov_b64 s[56:57], s[28:29]\n" \
"s_and_b32 s57, s57, 0xffff\n" \
"s_mov_b32 s58, 87147648\n" \
"s_mov_b32 s59, 0x20000\n" \
"s_lshl_b32 s66, %1, 2\n" \
"s_add_u32 s66, s66, s61\n" \
"s_lshl_b32 s66, s66, 10\n" \
"s_lshr_b32 s67, %1, 3\n" \
"s_lshl_b32 s67, s67, 2\n" \
"s_add_u32 s67, s67, s61\n" \
"s_lshl_b32 s67, s67, 13\n" \
"s_and_b32 s64, %1, 7\n" \
"s_lshl_b32 s64, s64, 10\n" \
"s_add_u32 s67, s67, s64\n" \
"s_lshl_b32 s89, s63, 18\n" \
"s_lshl_b32 s95, s61, 12\n" \
"v_add_u32 v228, 4096, v54\n" \
"v_add_u32 v229, 3200, v54\n" \
"v_lshl_add_u32 v57, v55, 11, v56\n" \
"v_add_u32 v224, 0, v57\n" \
"v_add_u32 v225, 15360, v57\n" \
"v_add_u32 v226, 129024, v57\n" \
"v_add_u32 v227, 144384, v57\n" \
"s_lshl_b32 s70, s61, 15\n" \
"s_add_u32 s70, s70, 0\n" \
"s_add_u32 s71, s70, s89\n" \
"s_add_u32 s71, s71, 4096\n" \
"s_add_u32 s48, s8, s71\n" \
"s_addc_u32 s49, s9, 0\n" \
"s_sub_u32 s71, s70, s89\n" \
"s_add_u32 s71, s71, 3932160\n" \
"s_add_u32 s46, s8, s71\n" \
"s_addc_u32 s47, s9, 0\n" \
"s_add_u32 s70, s88, 0\n" \
"s_add_u32 s44, s12, s70\n" \
"s_addc_u32 s45, s13, 0\n" \
"s_add_u32 s42, s44, 30720\n" \
"s_addc_u32 s43, s45, 0\n" \
"s_lshr_b32 s70, s28, 31\n" \
"s_lshl_b32 s70, s70, 30\n" \
"s_sub_u32 s28, s28, s70\n" \
"v_add_u32 v250, s70, v248\n" \
"v_lshlrev_b32 v245, 2, v52\n" \
"v_mov_b32 v254, 0x30000\n" \
"v_lshrrev_b32 v55, 4, v52\n" \
"v_and_b32 v249, 0xf0, v248\n" \
"v_lshl_add_u32 v249, v55, 9, v249\n" \
"v_and_b32 v53, 15, v52\n" \
"s_lshl_b32 s70, s61, 4\n" \
"v_lshlrev_b32 v54, 2, v55\n" \
"v_sub_u32 v251, v53, v54\n" \
"v_add_u32 v251, s70, v251\n" \
"v_and_b32 v54, 7, v52\n" \
"v_xor_b32 v54, v54, v55\n" \
"v_lshlrev_b32 v53, 7, v53\n" \
"s_add_u32 s71, s87, 131072\n" \
"s_lshr_b32 s70, s61, 1\n" \
"s_lshl_b32 s70, s70, 14\n" \
"s_lshl_b32 s76, s73, 15\n" \
"s_add_u32 s70, s70, s76\n" \
"v_xor_b32 v56, 0, v54\n" \
"v_lshl_add_u32 v56, v56, 4, v53\n" \
"v_add_u32 v230, s71, v56\n" \
"v_add_u32 v48, s70, v56\n" \
"v_xor_b32 v56, 4, v54\n" \
"v_lshl_add_u32 v56, v56, 4, v53\n" \
"v_add_u32 v231, s71, v56\n" \
"v_add_u32 v49, s70, v56\n" \
"v_and_b32 v53, 3, v52\n" \
"v_lshlrev_b32 v53, 3, v53\n" \
"v_bfe_u32 v54, v52, 2, 2\n" \
"v_lshl_add_u32 v53, v54, 6, v53\n" \
"v_bfe_u32 v54, v52, 4, 1\n" \
"v_lshl_add_u32 v53, v54, 8, v53\n" \
"v_lshl_add_u32 v53, v54, 5, v53\n" \
"v_lshrrev_b32 v54, 5, v52\n" \
"s_and_b32 s70, s61, 1\n" \
"s_lshl_b32 s70, s70, 11\n" \
"s_xor_b32 s76, s76, 0x8000\n" \
"s_add_u32 s70, s70, s76\n" \
"v_lshl_add_u32 v56, v54, 13, v53\n" \
"v_add_u32 v50, s70, v56\n" \
"v_xor_b32 v51, 32, v50\n" \
"v_lshl_add_u32 v232, v54, 12, v53\n" \
"v_xor_b32 v233, 32, v232\n" \
"s_mov_b64 exec, 0\n" \
"s_cmp_eq_u32 s61, 0\n" \
"s_cbranch_scc1 Lst0x0_%=\n" \
"s_cmp_eq_u32 s61, 1\n" \
"s_cbranch_scc1 Lst0x1_%=\n" \
"s_cmp_eq_u32 s61, 2\n" \
"s_cbranch_scc1 Lst0x2_%=\n" \
"s_branch Lst0x3_%=\n" \
"Lcb0_%=:\n" \
"s_mov_b64 exec, -1\n" \
"s_add_u32 m0, s87, 135168\n" \
"s_nop 0\n" \
"global_load_lds_dwordx4 v40, s[52:53] offset:-4096\n" \
"global_load_lds_dwordx4 v41, s[50:51] offset:-3072\n" \
"global_load_lds_dwordx4 v42, s[52:53] offset:-2048\n" \
"global_load_lds_dwordx4 v43, s[50:51] offset:-1024\n" \
"global_load_lds_dwordx4 v44, s[52:53] offset:0\n" \
"global_load_lds_dwordx4 v45, s[50:51] offset:1024\n" \
"global_load_lds_dwordx4 v46, s[52:53] offset:2048\n" \
"global_load_lds_dwordx4 v47, s[50:51] offset:3072\n" \
"s_add_u32 s52, s52, s74\n" \
"s_addc_u32 s53, s53, 0\n" \
"s_add_u32 s50, s50, s74\n" \
"s_addc_u32 s51, s51, 0\n" \
"s_waitcnt vmcnt(8)\n" \
"s_barrier\n" \
"ds_read_b128 v[64:67], v48 offset:0\n" \
"ds_read_b64_tr_b16 v[128:129], v50 offset:0\n" \
"ds_read_b64_tr_b16 v[130:131], v50 offset:4096\n" \
"ds_read_b128 v[68:71], v49 offset:0\n" \
"ds_read_b64_tr_b16 v[132:133], v50 offset:16384\n" \
"ds_read_b64_tr_b16 v[134:135], v50 offset:20480\n" \
"ds_read_b128 v[72:75], v48 offset:2048\n" \
"ds_read_b64_tr_b16 v[136:137], v51 offset:0\n" \
"ds_read_b64_tr_b16 v[138:139], v51 offset:4096\n" \
"ds_read_b128 v[76:79], v49 offset:2048\n" \
"ds_read_b64_tr_b16 v[140:141], v51 offset:16384\n" \
"ds_read_b64_tr_b16 v[142:143], v51 offset:20480\n" \
"ds_read_b64_tr_b16 v[144:145], v50 offset:512\n" \
"ds_read_b64_tr_b16 v[146:147], v50 offset:4608\n" \
"ds_read_b64_tr_b16 v[148:149], v50 offset:16896\n" \
"ds_read_b64_tr_b16 v[150:151], v50 offset:20992\n" \
"ds_read_b64_tr_b16 v[152:153], v51 offset:512\n" \
"ds_read_b64_tr_b16 v[154:155], v51 offset:4608\n" \
"ds_read_b64_tr_b16 v[156:157], v51 offset:16896\n" \
"ds_read_b64_tr_b16 v[158:159], v51 offset:20992\n" \
"s_waitcnt lgkmcnt(14)\n" \
"v_mfma_f32_16x16x32_bf16 a[0:3], v[128:131], v[64:67], 0\n" \
"ds_read_b128 v[120:123], v48 offset:14336\n" \
"s_add_u32 m0, s87, 69632\n" \
"v_mfma_f32_16x16x32_bf16 a[0:3], v[132:135], v[68:71], a[0:3]\n" \
"s_waitcnt lgkmcnt(9)\n" \
"ds_read_b128 v[80:83], v48 offset:4096\n" \
"global_load_lds_dwordx4 v32, s[54:55] offset:-4096\n" \
"v_mfma_f32_16x16x32_bf16 a[4:7], v[136:139], v[64:67], 0\n" \
"ds_read_b128 v[124:127], v49 offset:14336\n" \
"v_mfma_f32_16x16x32_bf16 a[4:7], v[140:143], v[68:71], a[4:7]\n" \
"s_waitcnt lgkmcnt(7)\n" \
"ds_read_b128 v[84:87], v49 offset:4096\n" \
"v_mfma_f32_16x16x32_bf16 a[8:11], v[144:147], v[64:67], 0\n" \
"ds_read_b64_tr_b16 v[160:161], v50 offset:1024\n" \
"global_load_lds_dwordx4 v33, s[40:41] offset:-3072\n" \
"v_mfma_f32_16x16x32_bf16 a[8:11], v[148:151], v[68:71], a[8:11]\n" \
"s_waitcnt lgkmcnt(4)\n" \
"ds_read_b128 v[88:91], v48 offset:6144\n" \
"v_mfma_f32_16x16x32_bf16 a[12:15], v[152:155], v[64:67], 0\n" \
"ds_read_b64_tr_b16 v[162:163], v50 offset:5120\n" \
"v_mfma_f32_16x16x32_bf16 a[12:15], v[156:159], v[68:71], a[12:15]\n" \
"ds_read_b128 v[92:95], v49 offset:6144\n" \
"global_load_lds_dwordx4 v34, s[54:55] offset:-2048\n" \
"v_mfma_f32_16x16x32_bf16 a[44:47], v[152:155], v[72:75], 0\n" \
"ds_read_b64_tr_b16 v[164:165], v50 offset:17408\n" \
"v_mfma_f32_16x16x32_bf16 a[44:47], v[156:159], v[76:79], a[44:47]\n" \
"ds_read_b128 v[96:99], v48 offset:8192\n" \
"v_mfma_f32_16x16x32_bf16 a[40:43], v[144:147], v[72:75], 0\n" \
"ds_read_b64_tr_b16 v[166:167], v50 offset:21504\n" \
"global_load_lds_dwordx4 v35, s[40:41] offset:-1024\n" \
"v_mfma_f32_16x16x32_bf16 a[40:43], v[148:151], v[76:79], a[40:43]\n" \
"ds_read_b128 v[100:103], v49 offset:8192\n" \
"v_mfma_f32_16x16x32_bf16 a[36:39], v[136:139], v[72:75], 0\n" \
"ds_read_b64_tr_b16 v[168:169], v51 offset:1024\n" \
"v_mfma_f32_16x16x32_bf16 a[36:39], v[140:143], v[76:79], a[36:39]\n" \
"ds_read_b128 v[104:107], v48 offset:10240\n" \
"global_load_lds_dwordx4 v36, s[54:55] offset:0\n" \
"v_mfma_f32_16x16x32_bf16 a[32:35], v[128:131], v[72:75], 0\n" \
"ds_read_b64_tr_b16 v[170:171], v51 offset:5120\n" \
"v_mfma_f32_16x16x32_bf16 a[32:35], v[132:135], v[76:79], a[32:35]\n" \
"s_waitcnt lgkmcnt(4)\n" \
"ds_read_b128 v[108:111], v49 offset:10240\n" \
"v_mfma_f32_16x16x32_bf16 a[64:67], v[128:131], v[80:83], 0\n" \
"ds_read_b64_tr_b16 v[172:173], v51 offset:17408\n" \
"global_load_lds_dwordx4 v37, s[40:41] offset:1024\n" \
"v_mfma_f32_16x16x32_bf16 a[64:67], v[132:135], v[84:87], a[64:67]\n" \
"ds_read_b128 v[112:115], v48 offset:12288\n" \
"v_mfma_f32_16x16x32_bf16 a[68:71], v[136:139], v[80:83], 0\n" \
"ds_read_b64_tr_b16 v[174:175], v51 offset:21504\n" \
"v_mfma_f32_16x16x32_bf16 a[68:71], v[140:143], v[84:87], a[68:71]\n" \
"ds_read_b128 v[116:119], v49 offset:12288\n" \
"global_load_lds_dwordx4 v38, s[54:55] offset:2048\n" \
"v_mfma_f32_16x16x32_bf16 a[72:75], v[144:147], v[80:83], 0\n" \
"ds_read_b64_tr_b16 v[176:177], v50 offset:1536\n" \
"v_mfma_f32_16x16x32_bf16 a[72:75], v[148:151], v[84:87], a[72:75]\n" \
"global_load_lds_dwordx4 v39, s[40:41] offset:3072\n" \
"v_mfma_f32_16x16x32_bf16 a[76:79], v[152:155], v[80:83], 0\n" \
"s_add_u32 s54, s54, s75\n" \
"s_addc_u32 s55, s55, 0\n" \
"ds_read_b64_tr_b16 v[178:179], v50 offset:5632\n" \
"v_mfma_f32_16x16x32_bf16 a[76:79], v[156:159], v[84:87], a[76:79]\n" \
"s_add_u32 s40, s40, s75\n" \
"s_addc_u32 s41, s41, 0\n" \
"s_mov_b32 s90, 65593344\n" \
"v_mfma_f32_16x16x32_bf16 a[108:111], v[152:155], v[88:91], 0\n" \
"ds_read_b64_tr_b16 v[180:181], v50 offset:17920\n" \
"v_mfma_f32_16x16x32_bf16 a[108:111], v[156:159], v[92:95], a[108:111]\n" \
"buffer_load_dword v244, off, s[56:59], s90 sc1\n" \
"v_mfma_f32_16x16x32_bf16 a[104:107], v[144:147], v[88:91], 0\n" \
"ds_read_b64_tr_b16 v[182:183], v50 offset:22016\n" \
"v_mfma_f32_16x16x32_bf16 a[104:107], v[148:151], v[92:95], a[104:107]\n" \
"s_add_u32 m0, s87, 102400\n" \
"v_mfma_f32_16x16x32_bf16 a[100:103], v[136:139], v[88:91], 0\n" \
"ds_read_b64_tr_b16 v[184:185], v51 offset:1536\n" \
"global_load_lds_dwordx4 v40, s[52:53] offset:-4096\n" \
"v_mfma_f32_16x16x32_bf16 a[100:103], v[140:143], v[92:95], a[100:103]\n" \
"v_mfma_f32_16x16x32_bf16 a[96:99], v[128:131], v[88:91], 0\n" \
"ds_read_b64_tr_b16 v[186:187], v51 offset:5632\n" \
"v_mfma_f32_16x16x32_bf16 a[96:99], v[132:135], v[92:95], a[96:99]\n" \
"s_waitcnt lgkmcnt(2)\n" \
"global_load_lds_dwordx4 v41, s[50:51] offset:-3072\n" \
"v_mfma_f32_16x16x32_bf16 a[128:131], v[128:131], v[96:99], 0\n" \
"ds_read_b64_tr_b16 v[188:189], v51 offset:17920\n" \
"v_mfma_f32_16x16x32_bf16 a[128:131], v[132:135], v[100:103], a[128:131]\n" \
"v_mfma_f32_16x16x32_bf16 a[132:135], v[136:139], v[96:99], 0\n" \
"ds_read_b64_tr_b16 v[190:191], v51 offset:22016\n" \
"global_load_lds_dwordx4 v42, s[52:53] offset:-2048\n" \
"v_mfma_f32_16x16x32_bf16 a[132:135], v[140:143], v[100:103], a[132:135]\n" \
"v_mfma_f32_16x16x32_bf16 a[136:139], v[144:147], v[96:99], 0\n" \
"v_mfma_f32_16x16x32_bf16 a[136:139], v[148:151], v[100:103], a[136:139]\n" \
"global_load_lds_dwordx4 v43, s[50:51] offset:-1024\n" \
"v_mfma_f32_16x16x32_bf16 a[140:143], v[152:155], v[96:99], 0\n" \
"v_mfma_f32_16x16x32_bf16 a[140:143], v[156:159], v[100:103], a[140:143]\n" \
"global_load_lds_dwordx4 v44, s[52:53] offset:0\n" \
"v_mfma_f32_16x16x32_bf16 a[172:175], v[152:155], v[104:107], 0\n" \
"s_mul_i32 s76, s73, 0x8000\n" \
"s_add_u32 s77, s76, 0x10000\n" \
"s_sub_u32 s78, 0x18000, s76\n" \
"v_mfma_f32_16x16x32_bf16 a[172:175], v[156:159], v[108:111], a[172:175]\n" \
"v_add_u32 v48, s77, v48\n" \
"v_add_u32 v49, s77, v49\n" \
"v_mfma_f32_16x16x32_bf16 a[168:171], v[144:147], v[104:107], 0\n" \
"v_add_u32 v50, s78, v50\n" \
"v_add_u32 v51, s78, v51\n" \
"v_mfma_f32_16x16x32_bf16 a[168:171], v[148:151], v[108:111], a[168:171]\n" \
"global_load_lds_dwordx4 v45, s[50:51] offset:1024\n" \
"v_mfma_f32_16x16x32_bf16 a[164:167], v[136:139], v[104:107], 0\n" \
"v_mfma_f32_16x16x32_bf16 a[164:167], v[140:143], v[108:111], a[164:167]\n" \
"v_mfma_f32_16x16x32_bf16 a[160:163], v[128:131], v[104:107], 0\n" \
"global_load_lds_dwordx4 v46, s[52:53] offset:2048\n" \
"v_mfma_f32_16x16x32_bf16 a[160:163], v[132:135], v[108:111], a[160:163]\n" \
"v_mfma_f32_16x16x32_bf16 a[192:195], v[128:131], v[112:115], 0\n" \
"v_mfma_f32_16x16x32_bf16 a[192:195], v[132:135], v[116:119], a[192:195]\n" \
"global_load_lds_dwordx4 v47, s[50:51] offset:3072\n" \
"v_mfma_f32_16x16x32_bf16 a[196:199], v[136:139], v[112:115], 0\n" \
"s_add_u32 s52, s52, s74\n" \
"s_addc_u32 s53, s53, 0\n" \
"v_mfma_f32_16x16x32_bf16 a[196:199], v[140:143], v[116:119], a[196:199]\n" \
"s_add_u32 s50, s50, s74\n" \
"s_addc_u32 s51, s51, 0\n" \
"v_mfma_f32_16x16x32_bf16 a[200:203], v[144:147], v[112:115], 0\n" \
"v_mfma_f32_16x16x32_bf16 a[200:203], v[148:151], v[116:119], a[200:203]\n" \
"v_mfma_f32_16x16x32_bf16 a[204:207], v[152:155], v[112:115], 0\n" \
"v_mfma_f32_16x16x32_bf16 a[204:207], v[156:159], v[116:119], a[204:207]\n" \
"v_mfma_f32_16x16x32_bf16 a[236:239], v[152:155], v[120:123], 0\n" \
"v_mfma_f32_16x16x32_bf16 a[236:239], v[156:159], v[124:127], a[236:239]\n" \
"v_mfma_f32_16x16x32_bf16 a[232:235], v[144:147], v[120:123], 0\n" \
"v_mfma_f32_16x16x32_bf16 a[232:235], v[148:151], v[124:127], a[232:235]\n" \
"v_mfma_f32_16x16x32_bf16 a[228:231], v[136:139], v[120:123], 0\n" \
"v_mfma_f32_16x16x32_bf16 a[228:231], v[140:143], v[124:127], a[228:231]\n" \
"s_waitcnt vmcnt(9) lgkmcnt(0)\n" \
"v_mfma_f32_16x16x32_bf16 a[224:227], v[128:131], v[120:123], 0\n" \
"v_mfma_f32_16x16x32_bf16 a[224:227], v[132:135], v[124:127], a[224:227]\n" \
"s_barrier\n" \
"v_mfma_f32_16x16x32_bf16 a[16:19], v[64:67], v[160:163], 0\n" \
"s_add_u32 m0, s87, 36864\n" \
"ds_read_b64_tr_b16 v[128:129], v50 offset:0\n" \
"v_mfma_f32_16x16x32_bf16 a[16:19], v[68:71], v[164:167], a[16:19]\n" \
"global_load_lds_dwordx4 v32, s[54:55] offset:-4096\n" \
"v_mfma_f32_16x16x32_bf16 a[20:23], v[64:67], v[168:171], 0\n" \
"ds_read_b64_tr_b16 v[130:131], v50 offset:4096\n" \
"v_mfma_f32_16x16x32_bf16 a[20:23], v[68:71], v[172:175], a[20:23]\n" \
"v_mfma_f32_16x16x32_bf16 a[24:27], v[64:67], v[176:179], 0\n" \
"ds_read_b64_tr_b16 v[132:133], v50 offset:16384\n" \
"v_mfma_f32_16x16x32_bf16 a[24:27], v[68:71], v[180:183], a[24:27]\n" \
"global_load_lds_dwordx4 v33, s[40:41] offset:-3072\n" \
"v_mfma_f32_16x16x32_bf16 a[28:31], v[64:67], v[184:187], 0\n" \
"ds_read_b64_tr_b16 v[134:135], v50 offset:20480\n" \
"v_mfma_f32_16x16x32_bf16 a[28:31], v[68:71], v[188:191], a[28:31]\n" \
"v_mfma_f32_16x16x32_bf16 a[60:63], v[72:75], v[184:187], 0\n" \
"ds_read_b128 v[64:67], v48 offset:0\n" \
"v_mfma_f32_16x16x32_bf16 a[60:63], v[76:79], v[188:191], a[60:63]\n" \
"global_load_lds_dwordx4 v34, s[54:55] offset:-2048\n" \
"v_mfma_f32_16x16x32_bf16 a[56:59], v[72:75], v[176:179], 0\n" \
"ds_read_b64_tr_b16 v[136:137], v51 offset:0\n" \
"v_mfma_f32_16x16x32_bf16 a[56:59], v[76:79], v[180:183], a[56:59]\n" \
"v_mfma_f32_16x16x32_bf16 a[52:55], v[72:75], v[168:171], 0\n" \
"ds_read_b128 v[68:71], v49 offset:0\n" \
"v_mfma_f32_16x16x32_bf16 a[52:55], v[76:79], v[172:175], a[52:55]\n" \
"global_load_lds_dwordx4 v35, s[40:41] offset:-1024\n" \
"v_mfma_f32_16x16x32_bf16 a[48:51], v[72:75], v[160:163], 0\n" \
"ds_read_b64_tr_b16 v[138:139], v51 offset:4096\n" \
"v_mfma_f32_16x16x32_bf16 a[48:51], v[76:79], v[164:167], a[48:51]\n" \
"v_mfma_f32_16x16x32_bf16 a[80:83], v[80:83], v[160:163], 0\n" \
"ds_read_b128 v[72:75], v48 offset:2048\n" \
"v_mfma_f32_16x16x32_bf16 a[80:83], v[84:87], v[164:167], a[80:83]\n" \
"global_load_lds_dwordx4 v36, s[54:55] offset:0\n" \
"v_mfma_f32_16x16x32_bf16 a[84:87], v[80:83], v[168:171], 0\n" \
"ds_read_b64_tr_b16 v[140:141], v51 offset:16384\n" \
"v_mfma_f32_16x16x32_bf16 a[84:87], v[84:87], v[172:175], a[84:87]\n" \
"v_mfma_f32_16x16x32_bf16 a[88:91], v[80:83], v[176:179], 0\n" \
"ds_read_b128 v[76:79], v49 offset:2048\n" \
"v_mfma_f32_16x16x32_bf16 a[88:91], v[84:87], v[180:183], a[88:91]\n" \
"global_load_lds_dwordx4 v37, s[40:41] offset:1024\n" \
"v_mfma_f32_16x16x32_bf16 a[92:95], v[80:83], v[184:187], 0\n" \
"ds_read_b64_tr_b16 v[142:143], v51 offset:20480\n" \
"v_mfma_f32_16x16x32_bf16 a[92:95], v[84:87], v[188:191], a[92:95]\n" \
"v_mfma_f32_16x16x32_bf16 a[124:127], v[88:91], v[184:187], 0\n" \
"ds_read_b128 v[80:83], v48 offset:4096\n" \
"v_mfma_f32_16x16x32_bf16 a[124:127], v[92:95], v[188:191], a[124:127]\n" \
"global_load_lds_dwordx4 v38, s[54:55] offset:2048\n" \
"v_mfma_f32_16x16x32_bf16 a[120:123], v[88:91], v[176:179], 0\n" \
"ds_read_b64_tr_b16 v[144:145], v50 offset:512\n" \
"v_mfma_f32_16x16x32_bf16 a[120:123], v[92:95], v[180:183], a[120:123]\n" \
"v_mfma_f32_16x16x32_bf16 a[116:119], v[88:91], v[168:171], 0\n" \
"ds_read_b128 v[84:87], v49 offset:4096\n" \
"global_load_lds_dwordx4 v39, s[40:41] offset:3072\n" \
"v_mfma_f32_16x16x32_bf16 a[116:119], v[92:95], v[172:175], a[116:119]\n" \
"s_add_u32 s54, s54, s75\n" \
"s_addc_u32 s55, s55, 0\n" \
"v_mfma_f32_16x16x32_bf16 a[112:115], v[88:91], v[160:163], 0\n" \
"s_add_u32 s40, s40, s75\n" \
"s_addc_u32 s41, s41, 0\n" \
"ds_read_b64_tr_b16 v[146:147], v50 offset:4608\n" \
"v_mfma_f32_16x16x32_bf16 a[112:115], v[92:95], v[164:167], a[112:115]\n" \
"s_add_u32 s68, s66, 0x0\n" \
"s_add_u32 s64, s10, s68\n" \
"s_addc_u32 s65, s11, 0\n" \
"v_mfma_f32_16x16x32_bf16 a[144:147], v[96:99], v[160:163], 0\n" \
"ds_read_b128 v[88:91], v48 offset:6144\n" \
"v_mfma_f32_16x16x32_bf16 a[144:147], v[100:103], v[164:167], a[144:147]\n" \
"global_load_dword v253, v248, s[64:65]\n" \
"v_mfma_f32_16x16x32_bf16 a[148:151], v[96:99], v[168:171], 0\n" \
"ds_read_b64_tr_b16 v[148:149], v50 offset:16896\n" \
"v_mfma_f32_16x16x32_bf16 a[148:151], v[100:103], v[172:175], a[148:151]\n" \
"v_mfma_f32_16x16x32_bf16 a[152:155], v[96:99], v[176:179], 0\n" \
"ds_read_b128 v[92:95], v49 offset:6144\n" \
"v_mfma_f32_16x16x32_bf16 a[152:155], v[100:103], v[180:183], a[152:155]\n" \
"v_mfma_f32_16x16x32_bf16 a[156:159], v[96:99], v[184:187], 0\n" \
"ds_read_b64_tr_b16 v[150:151], v50 offset:20992\n" \
"v_mfma_f32_16x16x32_bf16 a[156:159], v[100:103], v[188:191], a[156:159]\n" \
"v_mfma_f32_16x16x32_bf16 a[188:191], v[104:107], v[184:187], 0\n" \
"ds_read_b128 v[96:99], v48 offset:8192\n" \
"v_mfma_f32_16x16x32_bf16 a[188:191], v[108:111], v[188:191], a[188:191]\n" \
"v_mfma_f32_16x16x32_bf16 a[184:187], v[104:107], v[176:179], 0\n" \
"ds_read_b64_tr_b16 v[152:153], v51 offset:512\n" \
"v_mfma_f32_16x16x32_bf16 a[184:187], v[108:111], v[180:183], a[184:187]\n" \
"v_mfma_f32_16x16x32_bf16 a[180:183], v[104:107], v[168:171], 0\n" \
"ds_read_b128 v[100:103], v49 offset:8192\n" \
"v_mfma_f32_16x16x32_bf16 a[180:183], v[108:111], v[172:175], a[180:183]\n" \
"v_mfma_f32_16x16x32_bf16 a[176:179], v[104:107], v[160:163], 0\n" \
"ds_read_b64_tr_b16 v[154:155], v51 offset:4608\n" \
"v_mfma_f32_16x16x32_bf16 a[176:179], v[108:111], v[164:167], a[176:179]\n" \
"v_mfma_f32_16x16x32_bf16 a[208:211], v[112:115], v[160:163], 0\n" \
"ds_read_b128 v[104:107], v48 offset:10240\n" \
"v_mfma_f32_16x16x32_bf16 a[208:211], v[116:119], v[164:167], a[208:211]\n" \
"v_mfma_f32_16x16x32_bf16 a[212:215], v[112:115], v[168:171], 0\n" \
"ds_read_b64_tr_b16 v[156:157], v51 offset:16896\n" \
"v_mfma_f32_16x16x32_bf16 a[212:215], v[116:119], v[172:175], a[212:215]\n" \
"v_mfma_f32_16x16x32_bf16 a[216:219], v[112:115], v[176:179], 0\n" \
"ds_read_b128 v[108:111], v49 offset:10240\n" \
"v_mfma_f32_16x16x32_bf16 a[216:219], v[116:119], v[180:183], a[216:219]\n" \
"v_mfma_f32_16x16x32_bf16 a[220:223], v[112:115], v[184:187], 0\n" \
"ds_read_b64_tr_b16 v[158:159], v51 offset:20992\n" \
"v_mfma_f32_16x16x32_bf16 a[220:223], v[116:119], v[188:191], a[220:223]\n" \
"v_mfma_f32_16x16x32_bf16 a[252:255], v[120:123], v[184:187], 0\n" \
"ds_read_b128 v[112:115], v48 offset:12288\n" \
"v_mfma_f32_16x16x32_bf16 a[252:255], v[124:127], v[188:191], a[252:255]\n" \
"v_mfma_f32_16x16x32_bf16 a[248:251], v[120:123], v[176:179], 0\n" \
"v_mfma_f32_16x16x32_bf16 a[248:251], v[124:127], v[180:183], a[248:251]\n" \
"v_mfma_f32_16x16x32_bf16 a[244:247], v[120:123], v[168:171], 0\n" \
"ds_read_b128 v[116:119], v49 offset:12288\n" \
"v_mfma_f32_16x16x32_bf16 a[244:247], v[124:127], v[172:175], a[244:247]\n" \
"v_mfma_f32_16x16x32_bf16 a[240:243], v[120:123], v[160:163], 0\n" \
"v_mfma_f32_16x16x32_bf16 a[240:243], v[124:127], v[164:167], a[240:243]\n" \
"s_waitcnt lgkmcnt(1)\n" \
"v_mfma_f32_16x16x32_bf16 a[0:3], v[128:131], v[64:67], a[0:3]\n" \
"ds_read_b128 v[120:123], v48 offset:14336\n" \
"v_mfma_f32_16x16x32_bf16 a[0:3], v[132:135], v[68:71], a[0:3]\n" \
"v_mfma_f32_16x16x32_bf16 a[4:7], v[136:139], v[64:67], a[4:7]\n" \
"ds_read_b128 v[124:127], v49 offset:14336\n" \
"v_mfma_f32_16x16x32_bf16 a[4:7], v[140:143], v[68:71], a[4:7]\n" \
"s_add_u32 s68, s66, 0x100000\n" \
"v_mfma_f32_16x16x32_bf16 a[8:11], v[144:147], v[64:67], a[8:11]\n" \
"s_add_u32 s64, s10, s68\n" \
"s_addc_u32 s65, s11, 0\n" \
"ds_read_b64_tr_b16 v[160:161], v50 offset:1024\n" \
"v_mfma_f32_16x16x32_bf16 a[8:11], v[148:151], v[68:71], a[8:11]\n" \
"global_load_dword v253, v248, s[64:65]\n" \
"v_mfma_f32_16x16x32_bf16 a[12:15], v[152:155], v[64:67], a[12:15]\n" \
"s_add_u32 m0, s87, 4096\n" \
"ds_read_b64_tr_b16 v[162:163], v50 offset:5120\n" \
"v_mfma_f32_16x16x32_bf16 a[12:15], v[156:159], v[68:71], a[12:15]\n" \
"global_load_lds_dwordx4 v40, s[52:53] offset:-4096\n" \
"v_mfma_f32_16x16x32_bf16 a[44:47], v[152:155], v[72:75], a[44:47]\n" \
"ds_read_b64_tr_b16 v[164:165], v50 offset:17408\n" \
"v_mfma_f32_16x16x32_bf16 a[44:47], v[156:159], v[76:79], a[44:47]\n" \
"v_mfma_f32_16x16x32_bf16 a[40:43], v[144:147], v[72:75], a[40:43]\n" \
"ds_read_b64_tr_b16 v[166:167], v50 offset:21504\n" \
"v_mfma_f32_16x16x32_bf16 a[40:43], v[148:151], v[76:79], a[40:43]\n" \
"global_load_lds_dwordx4 v41, s[50:51] offset:-3072\n" \
"v_mfma_f32_16x16x32_bf16 a[36:39], v[136:139], v[72:75], a[36:39]\n" \
"ds_read_b64_tr_b16 v[168:169], v51 offset:1024\n" \
"v_mfma_f32_16x16x32_bf16 a[36:39], v[140:143], v[76:79], a[36:39]\n" \
"v_mfma_f32_16x16x32_bf16 a[32:35], v[128:131], v[72:75], a[32:35]\n" \
"ds_read_b64_tr_b16 v[170:171], v51 offset:5120\n" \
"v_mfma_f32_16x16x32_bf16 a[32:35], v[132:135], v[76:79], a[32:35]\n" \
"global_load_lds_dwordx4 v42, s[52:53] offset:-2048\n" \
"v_mfma_f32_16x16x32_bf16 a[64:67], v[128:131], v[80:83], a[64:67]\n" \
"ds_read_b64_tr_b16 v[172:173], v51 offset:17408\n" \
"v_mfma_f32_16x16x32_bf16 a[64:67], v[132:135], v[84:87], a[64:67]\n" \
"v_mfma_f32_16x16x32_bf16 a[68:71], v[136:139], v[80:83], a[68:71]\n" \
"ds_read_b64_tr_b16 v[174:175], v51 offset:21504\n" \
"v_mfma_f32_16x16x32_bf16 a[68:71], v[140:143], v[84:87], a[68:71]\n" \
"global_load_lds_dwordx4 v43, s[50:51] offset:-1024\n" \
"v_mfma_f32_16x16x32_bf16 a[72:75], v[144:147], v[80:83], a[72:75]\n" \
"ds_read_b64_tr_b16 v[176:177], v50 offset:1536\n" \
"v_mfma_f32_16x16x32_bf16 a[72:75], v[148:151], v[84:87], a[72:75]\n" \
"v_mfma_f32_16x16x32_bf16 a[76:79], v[152:155], v[80:83], a[76:79]\n" \
"ds_read_b64_tr_b16 v[178:179], v50 offset:5632\n" \
"v_mfma_f32_16x16x32_bf16 a[76:79], v[156:159], v[84:87], a[76:79]\n" \
"global_load_lds_dwordx4 v44, s[52:53] offset:0\n" \
"v_mfma_f32_16x16x32_bf16 a[108:111], v[152:155], v[88:91], a[108:111]\n" \
"ds_read_b64_tr_b16 v[180:181], v50 offset:17920\n" \
"v_mfma_f32_16x16x32_bf16 a[108:111], v[156:159], v[92:95], a[108:111]\n" \
"v_mfma_f32_16x16x32_bf16 a[104:107], v[144:147], v[88:91], a[104:107]\n" \
"ds_read_b64_tr_b16 v[182:183], v50 offset:22016\n" \
"v_mfma_f32_16x16x32_bf16 a[104:107], v[148:151], v[92:95], a[104:107]\n" \
"global_load_lds_dwordx4 v45, s[50:51] offset:1024\n" \
"v_mfma_f32_16x16x32_bf16 a[100:103], v[136:139], v[88:91], a[100:103]\n" \
"ds_read_b64_tr_b16 v[184:185], v51 offset:1536\n" \
"v_mfma_f32_16x16x32_bf16 a[100:103], v[140:143], v[92:95], a[100:103]\n" \
"v_mfma_f32_16x16x32_bf16 a[96:99], v[128:131], v[88:91], a[96:99]\n" \
"ds_read_b64_tr_b16 v[186:187], v51 offset:5632\n" \
"v_mfma_f32_16x16x32_bf16 a[96:99], v[132:135], v[92:95], a[96:99]\n" \
"global_load_lds_dwordx4 v46, s[52:53] offset:2048\n" \
"v_mfma_f32_16x16x32_bf16 a[128:131], v[128:131], v[96:99], a[128:131]\n" \
"ds_read_b64_tr_b16 v[188:189], v51 offset:17920\n" \
"v_mfma_f32_16x16x32_bf16 a[128:131], v[132:135], v[100:103], a[128:131]\n" \
"v_mfma_f32_16x16x32_bf16 a[132:135], v[136:139], v[96:99], a[132:135]\n" \
"ds_read_b64_tr_b16 v[190:191], v51 offset:22016\n" \
"v_mfma_f32_16x16x32_bf16 a[132:135], v[140:143], v[100:103], a[132:135]\n" \
"global_load_lds_dwordx4 v47, s[50:51] offset:3072\n" \
"v_mfma_f32_16x16x32_bf16 a[136:139], v[144:147], v[96:99], a[136:139]\n" \
"s_add_u32 s52, s52, s74\n" \
"s_addc_u32 s53, s53, 0\n" \
"v_mfma_f32_16x16x32_bf16 a[136:139], v[148:151], v[100:103], a[136:139]\n" \
"s_add_u32 s50, s50, s74\n" \
"s_addc_u32 s51, s51, 0\n" \
"v_mfma_f32_16x16x32_bf16 a[140:143], v[152:155], v[96:99], a[140:143]\n" \
"v_mfma_f32_16x16x32_bf16 a[140:143], v[156:159], v[100:103], a[140:143]\n" \
"v_mfma_f32_16x16x32_bf16 a[172:175], v[152:155], v[104:107], a[172:175]\n" \
"s_mul_i32 s76, s73, 0x0\n" \
"s_add_u32 s77, s76, 0xffff8000\n" \
"s_sub_u32 s78, 0xffff8000, s76\n" \
"v_mfma_f32_16x16x32_bf16 a[172:175], v[156:159], v[108:111], a[172:175]\n" \
"v_add_u32 v48, s77, v48\n" \
"v_add_u32 v49, s77, v49\n" \
"v_mfma_f32_16x16x32_bf16 a[168:171], v[144:147], v[104:107], a[168:171]\n" \
"v_add_u32 v50, s78, v50\n" \
"v_add_u32 v51, s78, v51\n" \
"v_mfma_f32_16x16x32_bf16 a[168:171], v[148:151], v[108:111], a[168:171]\n" \
"v_mfma_f32_16x16x32_bf16 a[164:167], v[136:139], v[104:107], a[164:167]\n" \
"v_mfma_f32_16x16x32_bf16 a[164:167], v[140:143], v[108:111], a[164:167]\n" \
"v_mfma_f32_16x16x32_bf16 a[160:163], v[128:131], v[104:107], a[160:163]\n" \
"v_mfma_f32_16x16x32_bf16 a[160:163], v[132:135], v[108:111], a[160:163]\n" \
"s_waitcnt lgkmcnt(0)\n" \
"v_mfma_f32_16x16x32_bf16 a[192:195], v[128:131], v[112:115], a[192:195]\n" \
"v_mfma_f32_16x16x32_bf16 a[192:195], v[132:135], v[116:119], a[192:195]\n" \
"v_mfma_f32_16x16x32_bf16 a[196:199], v[136:139], v[112:115], a[196:199]\n" \
"v_mfma_f32_16x16x32_bf16 a[196:199], v[140:143], v[116:119], a[196:199]\n" \
"v_mfma_f32_16x16x32_bf16 a[200:203], v[144:147], v[112:115], a[200:203]\n" \
"v_mfma_f32_16x16x32_bf16 a[200:203], v[148:151], v[116:119], a[200:203]\n" \
"v_mfma_f32_16x16x32_bf16 a[204:207], v[152:155], v[112:115], a[204:207]\n" \
"v_mfma_f32_16x16x32_bf16 a[204:207], v[156:159], v[116:119], a[204:207]\n" \
"v_mfma_f32_16x16x32_bf16 a[236:239], v[152:155], v[120:123], a[236:239]\n" \
"v_mfma_f32_16x16x32_bf16 a[236:239], v[156:159], v[124:127], a[236:239]\n" \
"v_mfma_f32_16x16x32_bf16 a[232:235], v[144:147], v[120:123], a[232:235]\n" \
"v_mfma_f32_16x16x32_bf16 a[232:235], v[148:151], v[124:127], a[232:235]\n" \
"v_mfma_f32_16x16x32_bf16 a[228:231], v[136:139], v[120:123], a[228:231]\n" \
"v_mfma_f32_16x16x32_bf16 a[228:231], v[140:143], v[124:127], a[228:231]\n" \
"Lpg608_%=:\n" \
"Lst0x0_%=:\n" \
"s_cbranch_execz Lpg2766_%=\n" \
"s_waitcnt vmcnt(10) lgkmcnt(0)\n" \
"v_mfma_f32_16x16x32_bf16 a[224:227], v[128:131], v[120:123], a[224:227]\n" \
"v_mfma_f32_16x16x32_bf16 a[224:227], v[132:135], v[124:127], a[224:227]\n" \
"s_barrier\n" \
"v_mfma_f32_16x16x32_bf16 a[16:19], v[64:67], v[160:163], a[16:19]\n" \
"s_add_u32 m0, s87, 135168\n" \
"ds_read_b64_tr_b16 v[128:129], v50 offset:0\n" \
"v_readfirstlane_b32 s60, v244\n" \
"v_mfma_f32_16x16x32_bf16 a[16:19], v[68:71], v[164:167], a[16:19]\n" \
"s_add_u32 s60, s60, 0x9e3779b9\n" \
"global_load_lds_dwordx4 v32, s[54:55] offset:-4096\n" \
"v_mfma_f32_16x16x32_bf16 a[20:23], v[64:67], v[168:171], a[20:23]\n" \
"s_mul_i32 s88, s62, 557824\n" \
"s_mul_i32 s89, s63, 17408\n" \
"ds_read_b64_tr_b16 v[130:131], v50 offset:4096\n" \
"v_mfma_f32_16x16x32_bf16 a[20:23], v[68:71], v[172:175], a[20:23]\n" \
"s_add_u32 s88, s88, s89\n" \
"s_add_u32 s88, s88, s95\n" \
"s_add_u32 s89, s88, 17850368\n" \
"v_mfma_f32_16x16x32_bf16 a[24:27], v[64:67], v[176:179], a[24:27]\n" \
"s_lshl_b32 s91, s62, 7\n" \
"s_add_u32 s91, s91, 65585152\n" \
"ds_read_b64_tr_b16 v[132:133], v50 offset:16384\n" \
"v_mfma_f32_16x16x32_bf16 a[24:27], v[68:71], v[180:183], a[24:27]\n" \
"s_mov_b32 s92, 0x3e0293ee\n" \
"global_load_lds_dwordx4 v33, s[40:41] offset:-3072\n" \
"v_mfma_f32_16x16x32_bf16 a[28:31], v[64:67], v[184:187], a[28:31]\n" \
"ds_read_b64_tr_b16 v[134:135], v50 offset:20480\n" \
"v_mfma_f32_16x16x32_bf16 a[28:31], v[68:71], v[188:191], a[28:31]\n" \
"v_mfma_f32_16x16x32_bf16 a[60:63], v[72:75], v[184:187], a[60:63]\n" \
"ds_read_b128 v[64:67], v48 offset:0\n" \
"v_mfma_f32_16x16x32_bf16 a[60:63], v[76:79], v[188:191], a[60:63]\n" \
"global_load_lds_dwordx4 v34, s[54:55] offset:-2048\n" \
"v_mfma_f32_16x16x32_bf16 a[56:59], v[72:75], v[176:179], a[56:59]\n" \
"ds_read_b64_tr_b16 v[136:137], v51 offset:0\n" \
"v_mfma_f32_16x16x32_bf16 a[56:59], v[76:79], v[180:183], a[56:59]\n" \
"v_mfma_f32_16x16x32_bf16 a[52:55], v[72:75], v[168:171], a[52:55]\n" \
"ds_read_b128 v[68:71], v49 offset:0\n" \
"v_mfma_f32_16x16x32_bf16 a[52:55], v[76:79], v[172:175], a[52:55]\n" \
"global_load_lds_dwordx4 v35, s[40:41] offset:-1024\n" \
"v_mfma_f32_16x16x32_bf16 a[48:51], v[72:75], v[160:163], a[48:51]\n" \
"ds_read_b64_tr_b16 v[138:139], v51 offset:4096\n" \
"v_mfma_f32_16x16x32_bf16 a[48:51], v[76:79], v[164:167], a[48:51]\n" \
"v_mfma_f32_16x16x32_bf16 a[80:83], v[80:83], v[160:163], a[80:83]\n" \
"ds_read_b128 v[72:75], v48 offset:2048\n" \
"v_mfma_f32_16x16x32_bf16 a[80:83], v[84:87], v[164:167], a[80:83]\n" \
"global_load_lds_dwordx4 v36, s[54:55] offset:0\n" \
"v_mfma_f32_16x16x32_bf16 a[84:87], v[80:83], v[168:171], a[84:87]\n" \
"ds_read_b64_tr_b16 v[140:141], v51 offset:16384\n" \
"v_mfma_f32_16x16x32_bf16 a[84:87], v[84:87], v[172:175], a[84:87]\n" \
"v_mfma_f32_16x16x32_bf16 a[88:91], v[80:83], v[176:179], a[88:91]\n" \
"ds_read_b128 v[76:79], v49 offset:2048\n" \
"v_mfma_f32_16x16x32_bf16 a[88:91], v[84:87], v[180:183], a[88:91]\n" \
"global_load_lds_dwordx4 v37, s[40:41] offset:1024\n" \
"v_mfma_f32_16x16x32_bf16 a[92:95], v[80:83], v[184:187], a[92:95]\n" \
"ds_read_b64_tr_b16 v[142:143], v51 offset:20480\n" \
"v_mfma_f32_16x16x32_bf16 a[92:95], v[84:87], v[188:191], a[92:95]\n" \
"v_mfma_f32_16x16x32_bf16 a[124:127], v[88:91], v[184:187], a[124:127]\n" \
"ds_read_b128 v[80:83], v48 offset:4096\n" \
"v_mfma_f32_16x16x32_bf16 a[124:127], v[92:95], v[188:191], a[124:127]\n" \
"global_load_lds_dwordx4 v38, s[54:55] offset:2048\n" \
"v_mfma_f32_16x16x32_bf16 a[120:123], v[88:91], v[176:179], a[120:123]\n" \
"ds_read_b64_tr_b16 v[144:145], v50 offset:512\n" \
"v_mfma_f32_16x16x32_bf16 a[120:123], v[92:95], v[180:183], a[120:123]\n" \
"v_mfma_f32_16x16x32_bf16 a[116:119], v[88:91], v[168:171], a[116:119]\n" \
"ds_read_b128 v[84:87], v49 offset:4096\n" \
"global_load_lds_dwordx4 v39, s[40:41] offset:3072\n" \
"v_mfma_f32_16x16x32_bf16 a[116:119], v[92:95], v[172:175], a[116:119]\n" \
"s_add_u32 s54, s54, s75\n" \
"s_addc_u32 s55, s55, 0\n" \
"v_mfma_f32_16x16x32_bf16 a[112:115], v[88:91], v[160:163], a[112:115]\n" \
"s_add_u32 s40, s40, s75\n" \
"s_addc_u32 s41, s41, 0\n" \
"ds_read_b64_tr_b16 v[146:147], v50 offset:4608\n" \
"v_mfma_f32_16x16x32_bf16 a[112:115], v[92:95], v[164:167], a[112:115]\n" \
"s_add_u32 s68, s67, 0x200000\n" \
"s_add_u32 s64, s14, s68\n" \
"s_addc_u32 s65, s15, 0\n" \
"v_mfma_f32_16x16x32_bf16 a[144:147], v[96:99], v[160:163], a[144:147]\n" \
"ds_read_b128 v[88:91], v48 offset:6144\n" \
"v_mfma_f32_16x16x32_bf16 a[144:147], v[100:103], v[164:167], a[144:147]\n" \
"global_load_dword v253, v248, s[64:65]\n" \
"v_mfma_f32_16x16x32_bf16 a[148:151], v[96:99], v[168:171], a[148:151]\n" \
"s_add_u32 s68, s67, 0x200000\n" \
"ds_read_b64_tr_b16 v[148:149], v50 offset:16896\n" \
"v_mfma_f32_16x16x32_bf16 a[148:151], v[100:103], v[172:175], a[148:151]\n" \
"s_add_u32 s64, s16, s68\n" \
"s_addc_u32 s65, s17, 0\n" \
"v_mfma_f32_16x16x32_bf16 a[152:155], v[96:99], v[176:179], a[152:155]\n" \
"ds_read_b128 v[92:95], v49 offset:6144\n" \
"v_mfma_f32_16x16x32_bf16 a[152:155], v[100:103], v[180:183], a[152:155]\n" \
"global_load_dword v253, v248, s[64:65]\n" \
"v_mfma_f32_16x16x32_bf16 a[156:159], v[96:99], v[184:187], a[156:159]\n" \
"ds_read_b64_tr_b16 v[150:151], v50 offset:20992\n" \
"v_mfma_f32_16x16x32_bf16 a[156:159], v[100:103], v[188:191], a[156:159]\n" \
"v_mfma_f32_16x16x32_bf16 a[188:191], v[104:107], v[184:187], a[188:191]\n" \
"ds_read_b128 v[96:99], v48 offset:8192\n" \
"v_mfma_f32_16x16x32_bf16 a[188:191], v[108:111], v[188:191], a[188:191]\n" \
"v_mfma_f32_16x16x32_bf16 a[184:187], v[104:107], v[176:179], a[184:187]\n" \
"ds_read_b64_tr_b16 v[152:153], v51 offset:512\n" \
"v_mfma_f32_16x16x32_bf16 a[184:187], v[108:111], v[180:183], a[184:187]\n" \
"v_mfma_f32_16x16x32_bf16 a[180:183], v[104:107], v[168:171], a[180:183]\n" \
"ds_read_b128 v[100:103], v49 offset:8192\n" \
"v_mfma_f32_16x16x32_bf16 a[180:183], v[108:111], v[172:175], a[180:183]\n" \
"v_mfma_f32_16x16x32_bf16 a[176:179], v[104:107], v[160:163], a[176:179]\n" \
"ds_read_b64_tr_b16 v[154:155], v51 offset:4608\n" \
"v_mfma_f32_16x16x32_bf16 a[176:179], v[108:111], v[164:167], a[176:179]\n" \
"v_mfma_f32_16x16x32_bf16 a[208:211], v[112:115], v[160:163], a[208:211]\n" \
"ds_read_b128 v[104:107], v48 offset:10240\n" \
"v_mfma_f32_16x16x32_bf16 a[208:211], v[116:119], v[164:167], a[208:211]\n" \
"v_mfma_f32_16x16x32_bf16 a[212:215], v[112:115], v[168:171], a[212:215]\n" \
"ds_read_b64_tr_b16 v[156:157], v51 offset:16896\n" \
"v_mfma_f32_16x16x32_bf16 a[212:215], v[116:119], v[172:175], a[212:215]\n" \
"v_mfma_f32_16x16x32_bf16 a[216:219], v[112:115], v[176:179], a[216:219]\n" \
"ds_read_b128 v[108:111], v49 offset:10240\n" \
"v_mfma_f32_16x16x32_bf16 a[216:219], v[116:119], v[180:183], a[216:219]\n" \
"v_mfma_f32_16x16x32_bf16 a[220:223], v[112:115], v[184:187], a[220:223]\n" \
"ds_read_b64_tr_b16 v[158:159], v51 offset:20992\n" \
"v_mfma_f32_16x16x32_bf16 a[220:223], v[116:119], v[188:191], a[220:223]\n" \
"v_mfma_f32_16x16x32_bf16 a[252:255], v[120:123], v[184:187], a[252:255]\n" \
"ds_read_b128 v[112:115], v48 offset:12288\n" \
"v_mfma_f32_16x16x32_bf16 a[252:255], v[124:127], v[188:191], a[252:255]\n" \
"v_mfma_f32_16x16x32_bf16 a[248:251], v[120:123], v[176:179], a[248:251]\n" \
"v_mfma_f32_16x16x32_bf16 a[248:251], v[124:127], v[180:183], a[248:251]\n" \
"v_mfma_f32_16x16x32_bf16 a[244:247], v[120:123], v[168:171], a[244:247]\n" \
"ds_read_b128 v[116:119], v49 offset:12288\n" \
"v_mfma_f32_16x16x32_bf16 a[244:247], v[124:127], v[172:175], a[244:247]\n" \
"v_mfma_f32_16x16x32_bf16 a[240:243], v[120:123], v[160:163], a[240:243]\n" \
"v_mfma_f32_16x16x32_bf16 a[240:243], v[124:127], v[164:167], a[240:243]\n" \
"s_waitcnt lgkmcnt(1)\n" \
"v_mfma_f32_16x16x32_bf16 a[0:3], v[128:131], v[64:67], a[0:3]\n" \
"ds_read_b128 v[120:123], v48 offset:14336\n" \
"v_mfma_f32_16x16x32_bf16 a[0:3], v[132:135], v[68:71], a[0:3]\n" \
"v_mfma_f32_16x16x32_bf16 a[4:7], v[136:139], v[64:67], a[4:7]\n" \
"ds_read_b128 v[124:127], v49 offset:14336\n" \
"v_mfma_f32_16x16x32_bf16 a[4:7], v[140:143], v[68:71], a[4:7]\n" \
"s_add_u32 s68, s67, 0x300000\n" \
"v_mfma_f32_16x16x32_bf16 a[8:11], v[144:147], v[64:67], a[8:11]\n" \
"s_add_u32 s64, s14, s68\n" \
"s_addc_u32 s65, s15, 0\n" \
"ds_read_b64_tr_b16 v[160:161], v50 offset:1024\n" \
"v_mfma_f32_16x16x32_bf16 a[8:11], v[148:151], v[68:71], a[8:11]\n" \
"global_load_dword v253, v248, s[64:65]\n" \
"v_mfma_f32_16x16x32_bf16 a[12:15], v[152:155], v[64:67], a[12:15]\n" \
"s_add_u32 m0, s87, 69632\n" \
"ds_read_b64_tr_b16 v[162:163], v50 offset:5120\n" \
"v_mfma_f32_16x16x32_bf16 a[12:15], v[156:159], v[68:71], a[12:15]\n" \
"global_load_lds_dwordx4 v40, s[52:53] offset:-4096\n" \
"v_mfma_f32_16x16x32_bf16 a[44:47], v[152:155], v[72:75], a[44:47]\n" \
"ds_read_b64_tr_b16 v[164:165], v50 offset:17408\n" \
"v_mfma_f32_16x16x32_bf16 a[44:47], v[156:159], v[76:79], a[44:47]\n" \
"v_mfma_f32_16x16x32_bf16 a[40:43], v[144:147], v[72:75], a[40:43]\n" \
"ds_read_b64_tr_b16 v[166:167], v50 offset:21504\n" \
"v_mfma_f32_16x16x32_bf16 a[40:43], v[148:151], v[76:79], a[40:43]\n" \
"global_load_lds_dwordx4 v41, s[50:51] offset:-3072\n" \
"v_mfma_f32_16x16x32_bf16 a[36:39], v[136:139], v[72:75], a[36:39]\n" \
"ds_read_b64_tr_b16 v[168:169], v51 offset:1024\n" \
"v_mfma_f32_16x16x32_bf16 a[36:39], v[140:143], v[76:79], a[36:39]\n" \
"v_mfma_f32_16x16x32_bf16 a[32:35], v[128:131], v[72:75], a[32:35]\n" \
"ds_read_b64_tr_b16 v[170:171], v51 offset:5120\n" \
"v_mfma_f32_16x16x32_bf16 a[32:35], v[132:135], v[76:79], a[32:35]\n" \
"global_load_lds_dwordx4 v42, s[52:53] offset:-2048\n" \
"v_mfma_f32_16x16x32_bf16 a[64:67], v[128:131], v[80:83], a[64:67]\n" \
"ds_read_b64_tr_b16 v[172:173], v51 offset:17408\n" \
"v_mfma_f32_16x16x32_bf16 a[64:67], v[132:135], v[84:87], a[64:67]\n" \
"v_mfma_f32_16x16x32_bf16 a[68:71], v[136:139], v[80:83], a[68:71]\n" \
"ds_read_b64_tr_b16 v[174:175], v51 offset:21504\n" \
"v_mfma_f32_16x16x32_bf16 a[68:71], v[140:143], v[84:87], a[68:71]\n" \
"global_load_lds_dwordx4 v43, s[50:51] offset:-1024\n" \
"v_mfma_f32_16x16x32_bf16 a[72:75], v[144:147], v[80:83], a[72:75]\n" \
"ds_read_b64_tr_b16 v[176:177], v50 offset:1536\n" \
"v_mfma_f32_16x16x32_bf16 a[72:75], v[148:151], v[84:87], a[72:75]\n" \
"v_mfma_f32_16x16x32_bf16 a[76:79], v[152:155], v[80:83], a[76:79]\n" \
"ds_read_b64_tr_b16 v[178:179], v50 offset:5632\n" \
"v_mfma_f32_16x16x32_bf16 a[76:79], v[156:159], v[84:87], a[76:79]\n" \
"global_load_lds_dwordx4 v44, s[52:53] offset:0\n" \
"v_mfma_f32_16x16x32_bf16 a[108:111], v[152:155], v[88:91], a[108:111]\n" \
"ds_read_b64_tr_b16 v[180:181], v50 offset:17920\n" \
"v_mfma_f32_16x16x32_bf16 a[108:111], v[156:159], v[92:95], a[108:111]\n" \
"v_mfma_f32_16x16x32_bf16 a[104:107], v[144:147], v[88:91], a[104:107]\n" \
"ds_read_b64_tr_b16 v[182:183], v50 offset:22016\n" \
"v_mfma_f32_16x16x32_bf16 a[104:107], v[148:151], v[92:95], a[104:107]\n" \
"global_load_lds_dwordx4 v45, s[50:51] offset:1024\n" \
"v_mfma_f32_16x16x32_bf16 a[100:103], v[136:139], v[88:91], a[100:103]\n" \
"ds_read_b64_tr_b16 v[184:185], v51 offset:1536\n" \
"v_mfma_f32_16x16x32_bf16 a[100:103], v[140:143], v[92:95], a[100:103]\n" \
"v_mfma_f32_16x16x32_bf16 a[96:99], v[128:131], v[88:91], a[96:99]\n" \
"ds_read_b64_tr_b16 v[186:187], v51 offset:5632\n" \
"v_mfma_f32_16x16x32_bf16 a[96:99], v[132:135], v[92:95], a[96:99]\n" \
"global_load_lds_dwordx4 v46, s[52:53] offset:2048\n" \
"v_mfma_f32_16x16x32_bf16 a[128:131], v[128:131], v[96:99], a[128:131]\n" \
"ds_read_b64_tr_b16 v[188:189], v51 offset:17920\n" \
"v_mfma_f32_16x16x32_bf16 a[128:131], v[132:135], v[100:103], a[128:131]\n" \
"v_mfma_f32_16x16x32_bf16 a[132:135], v[136:139], v[96:99], a[132:135]\n" \
"ds_read_b64_tr_b16 v[190:191], v51 offset:22016\n" \
"v_mfma_f32_16x16x32_bf16 a[132:135], v[140:143], v[100:103], a[132:135]\n" \
"global_load_lds_dwordx4 v47, s[50:51] offset:3072\n" \
"v_mfma_f32_16x16x32_bf16 a[136:139], v[144:147], v[96:99], a[136:139]\n" \
"s_add_u32 s52, s52, s74\n" \
"s_addc_u32 s53, s53, 0\n" \
"v_mfma_f32_16x16x32_bf16 a[136:139], v[148:151], v[100:103], a[136:139]\n" \
"s_add_u32 s50, s50, s74\n" \
"s_addc_u32 s51, s51, 0\n" \
"v_mfma_f32_16x16x32_bf16 a[140:143], v[152:155], v[96:99], a[140:143]\n" \
"v_mfma_f32_16x16x32_bf16 a[140:143], v[156:159], v[100:103], a[140:143]\n" \
"v_mfma_f32_16x16x32_bf16 a[172:175], v[152:155], v[104:107], a[172:175]\n" \
"s_mul_i32 s76, s73, 0xfffd0000\n" \
"s_add_u32 s77, s76, 0x18000\n" \
"s_sub_u32 s78, 0xfffe8000, s76\n" \
"v_mfma_f32_16x16x32_bf16 a[172:175], v[156:159], v[108:111], a[172:175]\n" \
"v_add_u32 v48, s77, v48\n" \
"v_add_u32 v49, s77, v49\n" \
"v_mfma_f32_16x16x32_bf16 a[168:171], v[144:147], v[104:107], a[168:171]\n" \
"v_add_u32 v50, s78, v50\n" \
"v_add_u32 v51, s78, v51\n" \
"v_mfma_f32_16x16x32_bf16 a[168:171], v[148:151], v[108:111], a[168:171]\n" \
"v_mfma_f32_16x16x32_bf16 a[164:167], v[136:139], v[104:107], a[164:167]\n" \
"v_mfma_f32_16x16x32_bf16 a[164:167], v[140:143], v[108:111], a[164:167]\n" \
"v_mfma_f32_16x16x32_bf16 a[160:163], v[128:131], v[104:107], a[160:163]\n" \
"v_mfma_f32_16x16x32_bf16 a[160:163], v[132:135], v[108:111], a[160:163]\n" \
"s_waitcnt lgkmcnt(0)\n" \
"v_mfma_f32_16x16x32_bf16 a[192:195], v[128:131], v[112:115], a[192:195]\n" \
"v_mfma_f32_16x16x32_bf16 a[192:195], v[132:135], v[116:119], a[192:195]\n" \
"v_mfma_f32_16x16x32_bf16 a[196:199], v[136:139], v[112:115], a[196:199]\n" \
"v_mfma_f32_16x16x32_bf16 a[196:199], v[140:143], v[116:119], a[196:199]\n" \
"v_mfma_f32_16x16x32_bf16 a[200:203], v[144:147], v[112:115], a[200:203]\n" \
"v_mfma_f32_16x16x32_bf16 a[200:203], v[148:151], v[116:119], a[200:203]\n" \
"v_mfma_f32_16x16x32_bf16 a[204:207], v[152:155], v[112:115], a[204:207]\n" \
"v_mfma_f32_16x16x32_bf16 a[204:207], v[156:159], v[116:119], a[204:207]\n" \
"v_mfma_f32_16x16x32_bf16 a[236:239], v[152:155], v[120:123], a[236:239]\n" \
"v_mfma_f32_16x16x32_bf16 a[236:239], v[156:159], v[124:127], a[236:239]\n" \
"v_mfma_f32_16x16x32_bf16 a[232:235], v[144:147], v[120:123], a[232:235]\n" \
"v_mfma_f32_16x16x32_bf16 a[232:235], v[148:151], v[124:127], a[232:235]\n" \
"v_mfma_f32_16x16x32_bf16 a[228:231], v[136:139], v[120:123], a[228:231]\n" \
"v_mfma_f32_16x16x32_bf16 a[228:231], v[140:143], v[124:127], a[228:231]\n" \
"s_waitcnt vmcnt(11) lgkmcnt(0)\n" \
"v_mfma_f32_16x16x32_bf16 a[224:227], v[128:131], v[120:123], a[224:227]\n" \
"v_mfma_f32_16x16x32_bf16 a[224:227], v[132:135], v[124:127], a[224:227]\n" \
"s_barrier\n" \
"v_mfma_f32_16x16x32_bf16 a[16:19], v[64:67], v[160:163], a[16:19]\n" \
"s_add_u32 m0, s87, 102400\n" \
"ds_read_b64_tr_b16 v[128:129], v50 offset:0\n" \
"v_mfma_f32_16x16x32_bf16 a[16:19], v[68:71], v[164:167], a[16:19]\n" \
"global_load_lds_dwordx4 v32, s[54:55] offset:-4096\n" \
"v_mfma_f32_16x16x32_bf16 a[20:23], v[64:67], v[168:171], a[20:23]\n" \
"ds_read_b64_tr_b16 v[130:131], v50 offset:4096\n" \
"v_mfma_f32_16x16x32_bf16 a[20:23], v[68:71], v[172:175], a[20:23]\n" \
"v_mfma_f32_16x16x32_bf16 a[24:27], v[64:67], v[176:179], a[24:27]\n" \
"ds_read_b64_tr_b16 v[132:133], v50 offset:16384\n" \
"v_mfma_f32_16x16x32_bf16 a[24:27], v[68:71], v[180:183], a[24:27]\n" \
"global_load_lds_dwordx4 v33, s[40:41] offset:-3072\n" \
"v_mfma_f32_16x16x32_bf16 a[28:31], v[64:67], v[184:187], a[28:31]\n" \
"ds_read_b64_tr_b16 v[134:135], v50 offset:20480\n" \
"v_mfma_f32_16x16x32_bf16 a[28:31], v[68:71], v[188:191], a[28:31]\n" \
"v_mfma_f32_16x16x32_bf16 a[60:63], v[72:75], v[184:187], a[60:63]\n" \
"ds_read_b128 v[64:67], v48 offset:0\n" \
"v_mfma_f32_16x16x32_bf16 a[60:63], v[76:79], v[188:191], a[60:63]\n" \
"global_load_lds_dwordx4 v34, s[54:55] offset:-2048\n" \
"v_mfma_f32_16x16x32_bf16 a[56:59], v[72:75], v[176:179], a[56:59]\n" \
"ds_read_b64_tr_b16 v[136:137], v51 offset:0\n" \
"v_mfma_f32_16x16x32_bf16 a[56:59], v[76:79], v[180:183], a[56:59]\n" \
"v_mfma_f32_16x16x32_bf16 a[52:55], v[72:75], v[168:171], a[52:55]\n" \
"ds_read_b128 v[68:71], v49 offset:0\n" \
"v_mfma_f32_16x16x32_bf16 a[52:55], v[76:79], v[172:175], a[52:55]\n" \
"global_load_lds_dwordx4 v35, s[40:41] offset:-1024\n" \
"v_mfma_f32_16x16x32_bf16 a[48:51], v[72:75], v[160:163], a[48:51]\n" \
"ds_read_b64_tr_b16 v[138:139], v51 offset:4096\n" \
"v_mfma_f32_16x16x32_bf16 a[48:51], v[76:79], v[164:167], a[48:51]\n" \
"v_mfma_f32_16x16x32_bf16 a[80:83], v[80:83], v[160:163], a[80:83]\n" \
"ds_read_b128 v[72:75], v48 offset:2048\n" \
"v_mfma_f32_16x16x32_bf16 a[80:83], v[84:87], v[164:167], a[80:83]\n" \
"global_load_lds_dwordx4 v36, s[54:55] offset:0\n" \
"v_mfma_f32_16x16x32_bf16 a[84:87], v[80:83], v[168:171], a[84:87]\n" \
"ds_read_b64_tr_b16 v[140:141], v51 offset:16384\n" \
"v_mfma_f32_16x16x32_bf16 a[84:87], v[84:87], v[172:175], a[84:87]\n" \
"v_mfma_f32_16x16x32_bf16 a[88:91], v[80:83], v[176:179], a[88:91]\n" \
"ds_read_b128 v[76:79], v49 offset:2048\n" \
"v_mfma_f32_16x16x32_bf16 a[88:91], v[84:87], v[180:183], a[88:91]\n" \
"global_load_lds_dwordx4 v37, s[40:41] offset:1024\n" \
"v_mfma_f32_16x16x32_bf16 a[92:95], v[80:83], v[184:187], a[92:95]\n" \
"ds_read_b64_tr_b16 v[142:143], v51 offset:20480\n" \
"v_mfma_f32_16x16x32_bf16 a[92:95], v[84:87], v[188:191], a[92:95]\n" \
"v_mfma_f32_16x16x32_bf16 a[124:127], v[88:91], v[184:187], a[124:127]\n" \
"ds_read_b128 v[80:83], v48 offset:4096\n" \
"v_mfma_f32_16x16x32_bf16 a[124:127], v[92:95], v[188:191], a[124:127]\n" \
"global_load_lds_dwordx4 v38, s[54:55] offset:2048\n" \
"v_mfma_f32_16x16x32_bf16 a[120:123], v[88:91], v[176:179], a[120:123]\n" \
"ds_read_b64_tr_b16 v[144:145], v50 offset:512\n" \
"v_mfma_f32_16x16x32_bf16 a[120:123], v[92:95], v[180:183], a[120:123]\n" \
"v_mfma_f32_16x16x32_bf16 a[116:119], v[88:91], v[168:171], a[116:119]\n" \
"ds_read_b128 v[84:87], v49 offset:4096\n" \
"global_load_lds_dwordx4 v39, s[40:41] offset:3072\n" \
"v_mfma_f32_16x16x32_bf16 a[116:119], v[92:95], v[172:175], a[116:119]\n" \
"s_add_u32 s54, s54, s75\n" \
"s_addc_u32 s55, s55, 0\n" \
"v_mfma_f32_16x16x32_bf16 a[112:115], v[88:91], v[160:163], a[112:115]\n" \
"s_add_u32 s40, s40, s75\n" \
"s_addc_u32 s41, s41, 0\n" \
"ds_read_b64_tr_b16 v[146:147], v50 offset:4608\n" \
"v_mfma_f32_16x16x32_bf16 a[112:115], v[92:95], v[164:167], a[112:115]\n" \
"s_add_u32 s68, s67, 0x300000\n" \
"s_add_u32 s64, s16, s68\n" \
"s_addc_u32 s65, s17, 0\n" \
"v_mfma_f32_16x16x32_bf16 a[144:147], v[96:99], v[160:163], a[144:147]\n" \
"ds_read_b128 v[88:91], v48 offset:6144\n" \
"v_mfma_f32_16x16x32_bf16 a[144:147], v[100:103], v[164:167], a[144:147]\n" \
"global_load_dword v253, v248, s[64:65]\n" \
"v_mfma_f32_16x16x32_bf16 a[148:151], v[96:99], v[168:171], a[148:151]\n" \
"s_add_u32 s68, s66, 0x0\n" \
"ds_read_b64_tr_b16 v[148:149], v50 offset:16896\n" \
"v_mfma_f32_16x16x32_bf16 a[148:151], v[100:103], v[172:175], a[148:151]\n" \
"s_add_u32 s64, s8, s68\n" \
"s_addc_u32 s65, s9, 0\n" \
"v_mfma_f32_16x16x32_bf16 a[152:155], v[96:99], v[176:179], a[152:155]\n" \
"ds_read_b128 v[92:95], v49 offset:6144\n" \
"v_mfma_f32_16x16x32_bf16 a[152:155], v[100:103], v[180:183], a[152:155]\n" \
"global_load_dword v253, v248, s[64:65]\n" \
"v_mfma_f32_16x16x32_bf16 a[156:159], v[96:99], v[184:187], a[156:159]\n" \
"ds_read_b64_tr_b16 v[150:151], v50 offset:20992\n" \
"v_mfma_f32_16x16x32_bf16 a[156:159], v[100:103], v[188:191], a[156:159]\n" \
"v_mfma_f32_16x16x32_bf16 a[188:191], v[104:107], v[184:187], a[188:191]\n" \
"ds_read_b128 v[96:99], v48 offset:8192\n" \
"v_mfma_f32_16x16x32_bf16 a[188:191], v[108:111], v[188:191], a[188:191]\n" \
"v_mfma_f32_16x16x32_bf16 a[184:187], v[104:107], v[176:179], a[184:187]\n" \
"ds_read_b64_tr_b16 v[152:153], v51 offset:512\n" \
"v_mfma_f32_16x16x32_bf16 a[184:187], v[108:111], v[180:183], a[184:187]\n" \
"v_mfma_f32_16x16x32_bf16 a[180:183], v[104:107], v[168:171], a[180:183]\n" \
"ds_read_b128 v[100:103], v49 offset:8192\n" \
"v_mfma_f32_16x16x32_bf16 a[180:183], v[108:111], v[172:175], a[180:183]\n" \
"v_mfma_f32_16x16x32_bf16 a[176:179], v[104:107], v[160:163], a[176:179]\n" \
"ds_read_b64_tr_b16 v[154:155], v51 offset:4608\n" \
"v_mfma_f32_16x16x32_bf16 a[176:179], v[108:111], v[164:167], a[176:179]\n" \
"v_mfma_f32_16x16x32_bf16 a[208:211], v[112:115], v[160:163], a[208:211]\n" \
"ds_read_b128 v[104:107], v48 offset:10240\n" \
"v_mfma_f32_16x16x32_bf16 a[208:211], v[116:119], v[164:167], a[208:211]\n" \
"v_mfma_f32_16x16x32_bf16 a[212:215], v[112:115], v[168:171], a[212:215]\n" \
"ds_read_b64_tr_b16 v[156:157], v51 offset:16896\n" \
"v_mfma_f32_16x16x32_bf16 a[212:215], v[116:119], v[172:175], a[212:215]\n" \
"v_mfma_f32_16x16x32_bf16 a[216:219], v[112:115], v[176:179], a[216:219]\n" \
"ds_read_b128 v[108:111], v49 offset:10240\n" \
"v_mfma_f32_16x16x32_bf16 a[216:219], v[116:119], v[180:183], a[216:219]\n" \
"v_mfma_f32_16x16x32_bf16 a[220:223], v[112:115], v[184:187], a[220:223]\n" \
"ds_read_b64_tr_b16 v[158:159], v51 offset:20992\n" \
"v_mfma_f32_16x16x32_bf16 a[220:223], v[116:119], v[188:191], a[220:223]\n" \
"v_mfma_f32_16x16x32_bf16 a[252:255], v[120:123], v[184:187], a[252:255]\n" \
"ds_read_b128 v[112:115], v48 offset:12288\n" \
"v_mfma_f32_16x16x32_bf16 a[252:255], v[124:127], v[188:191], a[252:255]\n" \
"v_mfma_f32_16x16x32_bf16 a[248:251], v[120:123], v[176:179], a[248:251]\n" \
"v_mfma_f32_16x16x32_bf16 a[248:251], v[124:127], v[180:183], a[248:251]\n" \
"v_mfma_f32_16x16x32_bf16 a[244:247], v[120:123], v[168:171], a[244:247]\n" \
"ds_read_b128 v[116:119], v49 offset:12288\n" \
"v_mfma_f32_16x16x32_bf16 a[244:247], v[124:127], v[172:175], a[244:247]\n" \
"v_mfma_f32_16x16x32_bf16 a[240:243], v[120:123], v[160:163], a[240:243]\n" \
"v_mfma_f32_16x16x32_bf16 a[240:243], v[124:127], v[164:167], a[240:243]\n" \
"s_waitcnt lgkmcnt(1)\n" \
"v_mfma_f32_16x16x32_bf16 a[0:3], v[128:131], v[64:67], a[0:3]\n" \
"ds_read_b128 v[120:123], v48 offset:14336\n" \
"v_mfma_f32_16x16x32_bf16 a[0:3], v[132:135], v[68:71], a[0:3]\n" \
"v_mfma_f32_16x16x32_bf16 a[4:7], v[136:139], v[64:67], a[4:7]\n" \
"ds_read_b128 v[124:127], v49 offset:14336\n" \
"v_mfma_f32_16x16x32_bf16 a[4:7], v[140:143], v[68:71], a[4:7]\n" \
"s_add_u32 s68, s66, 0x100000\n" \
"v_mfma_f32_16x16x32_bf16 a[8:11], v[144:147], v[64:67], a[8:11]\n" \
"s_add_u32 s64, s8, s68\n" \
"s_addc_u32 s65, s9, 0\n" \
"ds_read_b64_tr_b16 v[160:161], v50 offset:1024\n" \
"v_mfma_f32_16x16x32_bf16 a[8:11], v[148:151], v[68:71], a[8:11]\n" \
"global_load_dword v253, v248, s[64:65]\n" \
"v_mfma_f32_16x16x32_bf16 a[12:15], v[152:155], v[64:67], a[12:15]\n" \
"s_add_u32 m0, s87, 36864\n" \
"ds_read_b64_tr_b16 v[162:163], v50 offset:5120\n" \
"v_mfma_f32_16x16x32_bf16 a[12:15], v[156:159], v[68:71], a[12:15]\n" \
"global_load_lds_dwordx4 v40, s[52:53] offset:-4096\n" \
"v_mfma_f32_16x16x32_bf16 a[44:47], v[152:155], v[72:75], a[44:47]\n" \
"ds_read_b64_tr_b16 v[164:165], v50 offset:17408\n" \
"v_mfma_f32_16x16x32_bf16 a[44:47], v[156:159], v[76:79], a[44:47]\n" \
"v_mfma_f32_16x16x32_bf16 a[40:43], v[144:147], v[72:75], a[40:43]\n" \
"ds_read_b64_tr_b16 v[166:167], v50 offset:21504\n" \
"v_mfma_f32_16x16x32_bf16 a[40:43], v[148:151], v[76:79], a[40:43]\n" \
"global_load_lds_dwordx4 v41, s[50:51] offset:-3072\n" \
"v_mfma_f32_16x16x32_bf16 a[36:39], v[136:139], v[72:75], a[36:39]\n" \
"ds_read_b64_tr_b16 v[168:169], v51 offset:1024\n" \
"v_mfma_f32_16x16x32_bf16 a[36:39], v[140:143], v[76:79], a[36:39]\n" \
"v_mfma_f32_16x16x32_bf16 a[32:35], v[128:131], v[72:75], a[32:35]\n" \
"ds_read_b64_tr_b16 v[170:171], v51 offset:5120\n" \
"v_mfma_f32_16x16x32_bf16 a[32:35], v[132:135], v[76:79], a[32:35]\n" \
"global_load_lds_dwordx4 v42, s[52:53] offset:-2048\n" \
"v_mfma_f32_16x16x32_bf16 a[64:67], v[128:131], v[80:83], a[64:67]\n" \
"ds_read_b64_tr_b16 v[172:173], v51 offset:17408\n" \
"v_mfma_f32_16x16x32_bf16 a[64:67], v[132:135], v[84:87], a[64:67]\n" \
"v_mfma_f32_16x16x32_bf16 a[68:71], v[136:139], v[80:83], a[68:71]\n" \
"ds_read_b64_tr_b16 v[174:175], v51 offset:21504\n" \
"v_mfma_f32_16x16x32_bf16 a[68:71], v[140:143], v[84:87], a[68:71]\n" \
"global_load_lds_dwordx4 v43, s[50:51] offset:-1024\n" \
"v_mfma_f32_16x16x32_bf16 a[72:75], v[144:147], v[80:83], a[72:75]\n" \
"ds_read_b64_tr_b16 v[176:177], v50 offset:1536\n" \
"v_mfma_f32_16x16x32_bf16 a[72:75], v[148:151], v[84:87], a[72:75]\n" \
"v_mfma_f32_16x16x32_bf16 a[76:79], v[152:155], v[80:83], a[76:79]\n" \
"ds_read_b64_tr_b16 v[178:179], v50 offset:5632\n" \
"v_mfma_f32_16x16x32_bf16 a[76:79], v[156:159], v[84:87], a[76:79]\n" \
"global_load_lds_dwordx4 v44, s[52:53] offset:0\n" \
"v_mfma_f32_16x16x32_bf16 a[108:111], v[152:155], v[88:91], a[108:111]\n" \
"ds_read_b64_tr_b16 v[180:181], v50 offset:17920\n" \
"v_mfma_f32_16x16x32_bf16 a[108:111], v[156:159], v[92:95], a[108:111]\n" \
"v_mfma_f32_16x16x32_bf16 a[104:107], v[144:147], v[88:91], a[104:107]\n" \
"ds_read_b64_tr_b16 v[182:183], v50 offset:22016\n" \
"v_mfma_f32_16x16x32_bf16 a[104:107], v[148:151], v[92:95], a[104:107]\n" \
"global_load_lds_dwordx4 v45, s[50:51] offset:1024\n" \
"v_mfma_f32_16x16x32_bf16 a[100:103], v[136:139], v[88:91], a[100:103]\n" \
"ds_read_b64_tr_b16 v[184:185], v51 offset:1536\n" \
"v_mfma_f32_16x16x32_bf16 a[100:103], v[140:143], v[92:95], a[100:103]\n" \
"v_mfma_f32_16x16x32_bf16 a[96:99], v[128:131], v[88:91], a[96:99]\n" \
"ds_read_b64_tr_b16 v[186:187], v51 offset:5632\n" \
"v_mfma_f32_16x16x32_bf16 a[96:99], v[132:135], v[92:95], a[96:99]\n" \
"global_load_lds_dwordx4 v46, s[52:53] offset:2048\n" \
"v_mfma_f32_16x16x32_bf16 a[128:131], v[128:131], v[96:99], a[128:131]\n" \
"ds_read_b64_tr_b16 v[188:189], v51 offset:17920\n" \
"v_mfma_f32_16x16x32_bf16 a[128:131], v[132:135], v[100:103], a[128:131]\n" \
"v_mfma_f32_16x16x32_bf16 a[132:135], v[136:139], v[96:99], a[132:135]\n" \
"ds_read_b64_tr_b16 v[190:191], v51 offset:22016\n" \
"v_mfma_f32_16x16x32_bf16 a[132:135], v[140:143], v[100:103], a[132:135]\n" \
"global_load_lds_dwordx4 v47, s[50:51] offset:3072\n" \
"v_mfma_f32_16x16x32_bf16 a[136:139], v[144:147], v[96:99], a[136:139]\n" \
"s_add_u32 s52, s52, s74\n" \
"s_addc_u32 s53, s53, 0\n" \
"v_mfma_f32_16x16x32_bf16 a[136:139], v[148:151], v[100:103], a[136:139]\n" \
"s_add_u32 s50, s50, s74\n" \
"s_addc_u32 s51, s51, 0\n" \
"v_mfma_f32_16x16x32_bf16 a[140:143], v[152:155], v[96:99], a[140:143]\n" \
"v_mfma_f32_16x16x32_bf16 a[140:143], v[156:159], v[100:103], a[140:143]\n" \
"v_mfma_f32_16x16x32_bf16 a[172:175], v[152:155], v[104:107], a[172:175]\n" \
"s_mul_i32 s76, s73, 0x18000\n" \
"s_add_u32 s77, s76, 0xffff8000\n" \
"s_sub_u32 s78, 0x10000, s76\n" \
"v_mfma_f32_16x16x32_bf16 a[172:175], v[156:159], v[108:111], a[172:175]\n" \
"v_add_u32 v48, s77, v48\n" \
"v_add_u32 v49, s77, v49\n" \
"v_mfma_f32_16x16x32_bf16 a[168:171], v[144:147], v[104:107], a[168:171]\n" \
"v_add_u32 v50, s78, v50\n" \
"v_add_u32 v51, s78, v51\n" \
"v_mfma_f32_16x16x32_bf16 a[168:171], v[148:151], v[108:111], a[168:171]\n" \
"v_mfma_f32_16x16x32_bf16 a[164:167], v[136:139], v[104:107], a[164:167]\n" \
"v_mfma_f32_16x16x32_bf16 a[164:167], v[140:143], v[108:111], a[164:167]\n" \
"v_mfma_f32_16x16x32_bf16 a[160:163], v[128:131], v[104:107], a[160:163]\n" \
"v_mfma_f32_16x16x32_bf16 a[160:163], v[132:135], v[108:111], a[160:163]\n" \
"s_waitcnt lgkmcnt(0)\n" \
"v_mfma_f32_16x16x32_bf16 a[192:195], v[128:131], v[112:115], a[192:195]\n" \
"v_mfma_f32_16x16x32_bf16 a[192:195], v[132:135], v[116:119], a[192:195]\n" \
"v_mfma_f32_16x16x32_bf16 a[196:199], v[136:139], v[112:115], a[196:199]\n" \
"v_mfma_f32_16x16x32_bf16 a[196:199], v[140:143], v[116:119], a[196:199]\n" \
"v_mfma_f32_16x16x32_bf16 a[200:203], v[144:147], v[112:115], a[200:203]\n" \
"v_mfma_f32_16x16x32_bf16 a[200:203], v[148:151], v[116:119], a[200:203]\n" \
"v_mfma_f32_16x16x32_bf16 a[204:207], v[152:155], v[112:115], a[204:207]\n" \
"v_mfma_f32_16x16x32_bf16 a[204:207], v[156:159], v[116:119], a[204:207]\n" \
"v_mfma_f32_16x16x32_bf16 a[236:239], v[152:155], v[120:123], a[236:239]\n" \
"v_mfma_f32_16x16x32_bf16 a[236:239], v[156:159], v[124:127], a[236:239]\n" \
"v_mfma_f32_16x16x32_bf16 a[232:235], v[144:147], v[120:123], a[232:235]\n" \
"v_mfma_f32_16x16x32_bf16 a[232:235], v[148:151], v[124:127], a[232:235]\n" \
"v_mfma_f32_16x16x32_bf16 a[228:231], v[136:139], v[120:123], a[228:231]\n" \
"v_mfma_f32_16x16x32_bf16 a[228:231], v[140:143], v[124:127], a[228:231]\n" \
"s_waitcnt vmcnt(11) lgkmcnt(0)\n" \
"v_mfma_f32_16x16x32_bf16 a[224:227], v[128:131], v[120:123], a[224:227]\n" \
"v_mfma_f32_16x16x32_bf16 a[224:227], v[132:135], v[124:127], a[224:227]\n" \
"s_barrier\n" \
"v_mfma_f32_16x16x32_bf16 a[16:19], v[64:67], v[160:163], a[16:19]\n" \
"s_add_u32 m0, s87, 4096\n" \
"ds_read_b64_tr_b16 v[128:129], v50 offset:0\n" \
"v_mfma_f32_16x16x32_bf16 a[16:19], v[68:71], v[164:167], a[16:19]\n" \
"global_load_lds_dwordx4 v32, s[54:55] offset:-4096\n" \
"v_mfma_f32_16x16x32_bf16 a[20:23], v[64:67], v[168:171], a[20:23]\n" \
"ds_read_b64_tr_b16 v[130:131], v50 offset:4096\n" \
"v_mfma_f32_16x16x32_bf16 a[20:23], v[68:71], v[172:175], a[20:23]\n" \
"v_mfma_f32_16x16x32_bf16 a[24:27], v[64:67], v[176:179], a[24:27]\n" \
"ds_read_b64_tr_b16 v[132:133], v50 offset:16384\n" \
"v_mfma_f32_16x16x32_bf16 a[24:27], v[68:71], v[180:183], a[24:27]\n" \
"global_load_lds_dwordx4 v33, s[40:41] offset:-3072\n" \
"v_mfma_f32_16x16x32_bf16 a[28:31], v[64:67], v[184:187], a[28:31]\n" \
"ds_read_b64_tr_b16 v[134:135], v50 offset:20480\n" \
"v_mfma_f32_16x16x32_bf16 a[28:31], v[68:71], v[188:191], a[28:31]\n" \
"v_mfma_f32_16x16x32_bf16 a[60:63], v[72:75], v[184:187], a[60:63]\n" \
"ds_read_b128 v[64:67], v48 offset:0\n" \
"v_mfma_f32_16x16x32_bf16 a[60:63], v[76:79], v[188:191], a[60:63]\n" \
"global_load_lds_dwordx4 v34, s[54:55] offset:-2048\n" \
"v_mfma_f32_16x16x32_bf16 a[56:59], v[72:75], v[176:179], a[56:59]\n" \
"ds_read_b64_tr_b16 v[136:137], v51 offset:0\n" \
"v_mfma_f32_16x16x32_bf16 a[56:59], v[76:79], v[180:183], a[56:59]\n" \
"v_mfma_f32_16x16x32_bf16 a[52:55], v[72:75], v[168:171], a[52:55]\n" \
"ds_read_b128 v[68:71], v49 offset:0\n" \
"v_mfma_f32_16x16x32_bf16 a[52:55], v[76:79], v[172:175], a[52:55]\n" \
"global_load_lds_dwordx4 v35, s[40:41] offset:-1024\n" \
"v_mfma_f32_16x16x32_bf16 a[48:51], v[72:75], v[160:163], a[48:51]\n" \
"ds_read_b64_tr_b16 v[138:139], v51 offset:4096\n" \
"v_mfma_f32_16x16x32_bf16 a[48:51], v[76:79], v[164:167], a[48:51]\n" \
"v_mfma_f32_16x16x32_bf16 a[80:83], v[80:83], v[160:163], a[80:83]\n" \
"ds_read_b128 v[72:75], v48 offset:2048\n" \
"v_mfma_f32_16x16x32_bf16 a[80:83], v[84:87], v[164:167], a[80:83]\n" \
"global_load_lds_dwordx4 v36, s[54:55] offset:0\n" \
"v_mfma_f32_16x16x32_bf16 a[84:87], v[80:83], v[168:171], a[84:87]\n" \
"ds_read_b64_tr_b16 v[140:141], v51 offset:16384\n" \
"v_mfma_f32_16x16x32_bf16 a[84:87], v[84:87], v[172:175], a[84:87]\n" \
"v_mfma_f32_16x16x32_bf16 a[88:91], v[80:83], v[176:179], a[88:91]\n" \
"ds_read_b128 v[76:79], v49 offset:2048\n" \
"v_mfma_f32_16x16x32_bf16 a[88:91], v[84:87], v[180:183], a[88:91]\n" \
"global_load_lds_dwordx4 v37, s[40:41] offset:1024\n" \
"v_mfma_f32_16x16x32_bf16 a[92:95], v[80:83], v[184:187], a[92:95]\n" \
"ds_read_b64_tr_b16 v[142:143], v51 offset:20480\n" \
"v_mfma_f32_16x16x32_bf16 a[92:95], v[84:87], v[188:191], a[92:95]\n" \
"v_mfma_f32_16x16x32_bf16 a[124:127], v[88:91], v[184:187], a[124:127]\n" \
"ds_read_b128 v[80:83], v48 offset:4096\n" \
"v_mfma_f32_16x16x32_bf16 a[124:127], v[92:95], v[188:191], a[124:127]\n" \
"global_load_lds_dwordx4 v38, s[54:55] offset:2048\n" \
"v_mfma_f32_16x16x32_bf16 a[120:123], v[88:91], v[176:179], a[120:123]\n" \
"ds_read_b64_tr_b16 v[144:145], v50 offset:512\n" \
"v_mfma_f32_16x16x32_bf16 a[120:123], v[92:95], v[180:183], a[120:123]\n" \
"v_mfma_f32_16x16x32_bf16 a[116:119], v[88:91], v[168:171], a[116:119]\n" \
"ds_read_b128 v[84:87], v49 offset:4096\n" \
"global_load_lds_dwordx4 v39, s[40:41] offset:3072\n" \
"v_mfma_f32_16x16x32_bf16 a[116:119], v[92:95], v[172:175], a[116:119]\n" \
"s_add_u32 s54, s54, s75\n" \
"s_addc_u32 s55, s55, 0\n" \
"v_mfma_f32_16x16x32_bf16 a[112:115], v[88:91], v[160:163], a[112:115]\n" \
"s_add_u32 s40, s40, s75\n" \
"s_addc_u32 s41, s41, 0\n" \
"ds_read_b64_tr_b16 v[146:147], v50 offset:4608\n" \
"v_mfma_f32_16x16x32_bf16 a[112:115], v[92:95], v[164:167], a[112:115]\n" \
"s_add_u32 s68, s66, 0x200000\n" \
"s_add_u32 s64, s8, s68\n" \
"s_addc_u32 s65, s9, 0\n" \
"v_mfma_f32_16x16x32_bf16 a[144:147], v[96:99], v[160:163], a[144:147]\n" \
"ds_read_b128 v[88:91], v48 offset:6144\n" \
"v_mfma_f32_16x16x32_bf16 a[144:147], v[100:103], v[164:167], a[144:147]\n" \
"global_load_dword v253, v248, s[64:65]\n" \
"v_mfma_f32_16x16x32_bf16 a[148:151], v[96:99], v[168:171], a[148:151]\n" \
"s_add_u32 s68, s67, 0x0\n" \
"ds_read_b64_tr_b16 v[148:149], v50 offset:16896\n" \
"v_mfma_f32_16x16x32_bf16 a[148:151], v[100:103], v[172:175], a[148:151]\n" \
"s_add_u32 s64, s12, s68\n" \
"s_addc_u32 s65, s13, 0\n" \
"v_mfma_f32_16x16x32_bf16 a[152:155], v[96:99], v[176:179], a[152:155]\n" \
"ds_read_b128 v[92:95], v49 offset:6144\n" \
"v_mfma_f32_16x16x32_bf16 a[152:155], v[100:103], v[180:183], a[152:155]\n" \
"Lpg1150_%=:\n" \
"Lst0x1_%=:\n" \
"s_cbranch_execz Lpg3308_%=\n" \
"global_load_dword v253, v248, s[64:65]\n" \
"v_mfma_f32_16x16x32_bf16 a[156:159], v[96:99], v[184:187], a[156:159]\n" \
"ds_read_b64_tr_b16 v[150:151], v50 offset:20992\n" \
"v_mfma_f32_16x16x32_bf16 a[156:159], v[100:103], v[188:191], a[156:159]\n" \
"v_mfma_f32_16x16x32_bf16 a[188:191], v[104:107], v[184:187], a[188:191]\n" \
"ds_read_b128 v[96:99], v48 offset:8192\n" \
"v_mfma_f32_16x16x32_bf16 a[188:191], v[108:111], v[188:191], a[188:191]\n" \
"v_mfma_f32_16x16x32_bf16 a[184:187], v[104:107], v[176:179], a[184:187]\n" \
"ds_read_b64_tr_b16 v[152:153], v51 offset:512\n" \
"v_mfma_f32_16x16x32_bf16 a[184:187], v[108:111], v[180:183], a[184:187]\n" \
"v_mfma_f32_16x16x32_bf16 a[180:183], v[104:107], v[168:171], a[180:183]\n" \
"ds_read_b128 v[100:103], v49 offset:8192\n" \
"v_mfma_f32_16x16x32_bf16 a[180:183], v[108:111], v[172:175], a[180:183]\n" \
"v_mfma_f32_16x16x32_bf16 a[176:179], v[104:107], v[160:163], a[176:179]\n" \
"ds_read_b64_tr_b16 v[154:155], v51 offset:4608\n" \
"v_mfma_f32_16x16x32_bf16 a[176:179], v[108:111], v[164:167], a[176:179]\n" \
"v_mfma_f32_16x16x32_bf16 a[208:211], v[112:115], v[160:163], a[208:211]\n" \
"ds_read_b128 v[104:107], v48 offset:10240\n" \
"v_mfma_f32_16x16x32_bf16 a[208:211], v[116:119], v[164:167], a[208:211]\n" \
"v_mfma_f32_16x16x32_bf16 a[212:215], v[112:115], v[168:171], a[212:215]\n" \
"ds_read_b64_tr_b16 v[156:157], v51 offset:16896\n" \
"v_mfma_f32_16x16x32_bf16 a[212:215], v[116:119], v[172:175], a[212:215]\n" \
"v_mfma_f32_16x16x32_bf16 a[216:219], v[112:115], v[176:179], a[216:219]\n" \
"ds_read_b128 v[108:111], v49 offset:10240\n" \
"v_mfma_f32_16x16x32_bf16 a[216:219], v[116:119], v[180:183], a[216:219]\n" \
"v_mfma_f32_16x16x32_bf16 a[220:223], v[112:115], v[184:187], a[220:223]\n" \
"ds_read_b64_tr_b16 v[158:159], v51 offset:20992\n" \
"v_mfma_f32_16x16x32_bf16 a[220:223], v[116:119], v[188:191], a[220:223]\n" \
"v_mfma_f32_16x16x32_bf16 a[252:255], v[120:123], v[184:187], a[252:255]\n" \
"ds_read_b128 v[112:115], v48 offset:12288\n" \
"v_mfma_f32_16x16x32_bf16 a[252:255], v[124:127], v[188:191], a[252:255]\n" \
"v_mfma_f32_16x16x32_bf16 a[248:251], v[120:123], v[176:179], a[248:251]\n" \
"v_mfma_f32_16x16x32_bf16 a[248:251], v[124:127], v[180:183], a[248:251]\n" \
"v_mfma_f32_16x16x32_bf16 a[244:247], v[120:123], v[168:171], a[244:247]\n" \
"ds_read_b128 v[116:119], v49 offset:12288\n" \
"v_mfma_f32_16x16x32_bf16 a[244:247], v[124:127], v[172:175], a[244:247]\n" \
"v_mfma_f32_16x16x32_bf16 a[240:243], v[120:123], v[160:163], a[240:243]\n" \
"v_mfma_f32_16x16x32_bf16 a[240:243], v[124:127], v[164:167], a[240:243]\n" \
"s_waitcnt lgkmcnt(1)\n" \
"v_mfma_f32_16x16x32_bf16 a[0:3], v[128:131], v[64:67], a[0:3]\n" \
"ds_read_b128 v[120:123], v48 offset:14336\n" \
"v_mfma_f32_16x16x32_bf16 a[0:3], v[132:135], v[68:71], a[0:3]\n" \
"v_mfma_f32_16x16x32_bf16 a[4:7], v[136:139], v[64:67], a[4:7]\n" \
"ds_read_b128 v[124:127], v49 offset:14336\n" \
"v_mfma_f32_16x16x32_bf16 a[4:7], v[140:143], v[68:71], a[4:7]\n" \
"s_add_u32 s68, s66, 0x300000\n" \
"v_mfma_f32_16x16x32_bf16 a[8:11], v[144:147], v[64:67], a[8:11]\n" \
"s_add_u32 s64, s8, s68\n" \
"s_addc_u32 s65, s9, 0\n" \
"ds_read_b64_tr_b16 v[160:161], v50 offset:1024\n" \
"v_mfma_f32_16x16x32_bf16 a[8:11], v[148:151], v[68:71], a[8:11]\n" \
"global_load_dword v253, v248, s[64:65]\n" \
"v_mfma_f32_16x16x32_bf16 a[12:15], v[152:155], v[64:67], a[12:15]\n" \
"s_add_u32 m0, s87, 135168\n" \
"ds_read_b64_tr_b16 v[162:163], v50 offset:5120\n" \
"v_mfma_f32_16x16x32_bf16 a[12:15], v[156:159], v[68:71], a[12:15]\n" \
"global_load_lds_dwordx4 v40, s[52:53] offset:-4096\n" \
"v_mfma_f32_16x16x32_bf16 a[44:47], v[152:155], v[72:75], a[44:47]\n" \
"ds_read_b64_tr_b16 v[164:165], v50 offset:17408\n" \
"v_mfma_f32_16x16x32_bf16 a[44:47], v[156:159], v[76:79], a[44:47]\n" \
"v_mfma_f32_16x16x32_bf16 a[40:43], v[144:147], v[72:75], a[40:43]\n" \
"ds_read_b64_tr_b16 v[166:167], v50 offset:21504\n" \
"v_mfma_f32_16x16x32_bf16 a[40:43], v[148:151], v[76:79], a[40:43]\n" \
"global_load_lds_dwordx4 v41, s[50:51] offset:-3072\n" \
"v_mfma_f32_16x16x32_bf16 a[36:39], v[136:139], v[72:75], a[36:39]\n" \
"ds_read_b64_tr_b16 v[168:169], v51 offset:1024\n" \
"v_mfma_f32_16x16x32_bf16 a[36:39], v[140:143], v[76:79], a[36:39]\n" \
"v_mfma_f32_16x16x32_bf16 a[32:35], v[128:131], v[72:75], a[32:35]\n" \
"ds_read_b64_tr_b16 v[170:171], v51 offset:5120\n" \
"v_mfma_f32_16x16x32_bf16 a[32:35], v[132:135], v[76:79], a[32:35]\n" \
"global_load_lds_dwordx4 v42, s[52:53] offset:-2048\n" \
"v_mfma_f32_16x16x32_bf16 a[64:67], v[128:131], v[80:83], a[64:67]\n" \
"ds_read_b64_tr_b16 v[172:173], v51 offset:17408\n" \
"v_mfma_f32_16x16x32_bf16 a[64:67], v[132:135], v[84:87], a[64:67]\n" \
"v_mfma_f32_16x16x32_bf16 a[68:71], v[136:139], v[80:83], a[68:71]\n" \
"ds_read_b64_tr_b16 v[174:175], v51 offset:21504\n" \
"v_mfma_f32_16x16x32_bf16 a[68:71], v[140:143], v[84:87], a[68:71]\n" \
"global_load_lds_dwordx4 v43, s[50:51] offset:-1024\n" \
"v_mfma_f32_16x16x32_bf16 a[72:75], v[144:147], v[80:83], a[72:75]\n" \
"ds_read_b64_tr_b16 v[176:177], v50 offset:1536\n" \
"v_mfma_f32_16x16x32_bf16 a[72:75], v[148:151], v[84:87], a[72:75]\n" \
"v_mfma_f32_16x16x32_bf16 a[76:79], v[152:155], v[80:83], a[76:79]\n" \
"ds_read_b64_tr_b16 v[178:179], v50 offset:5632\n" \
"v_mfma_f32_16x16x32_bf16 a[76:79], v[156:159], v[84:87], a[76:79]\n" \
"global_load_lds_dwordx4 v44, s[52:53] offset:0\n" \
"v_mfma_f32_16x16x32_bf16 a[108:111], v[152:155], v[88:91], a[108:111]\n" \
"ds_read_b64_tr_b16 v[180:181], v50 offset:17920\n" \
"v_mfma_f32_16x16x32_bf16 a[108:111], v[156:159], v[92:95], a[108:111]\n" \
"v_mfma_f32_16x16x32_bf16 a[104:107], v[144:147], v[88:91], a[104:107]\n" \
"ds_read_b64_tr_b16 v[182:183], v50 offset:22016\n" \
"v_mfma_f32_16x16x32_bf16 a[104:107], v[148:151], v[92:95], a[104:107]\n" \
"global_load_lds_dwordx4 v45, s[50:51] offset:1024\n" \
"v_mfma_f32_16x16x32_bf16 a[100:103], v[136:139], v[88:91], a[100:103]\n" \
"ds_read_b64_tr_b16 v[184:185], v51 offset:1536\n" \
"v_mfma_f32_16x16x32_bf16 a[100:103], v[140:143], v[92:95], a[100:103]\n" \
"v_mfma_f32_16x16x32_bf16 a[96:99], v[128:131], v[88:91], a[96:99]\n" \
"ds_read_b64_tr_b16 v[186:187], v51 offset:5632\n" \
"v_mfma_f32_16x16x32_bf16 a[96:99], v[132:135], v[92:95], a[96:99]\n" \
"global_load_lds_dwordx4 v46, s[52:53] offset:2048\n" \
"v_mfma_f32_16x16x32_bf16 a[128:131], v[128:131], v[96:99], a[128:131]\n" \
"ds_read_b64_tr_b16 v[188:189], v51 offset:17920\n" \
"v_mfma_f32_16x16x32_bf16 a[128:131], v[132:135], v[100:103], a[128:131]\n" \
"v_mfma_f32_16x16x32_bf16 a[132:135], v[136:139], v[96:99], a[132:135]\n" \
"ds_read_b64_tr_b16 v[190:191], v51 offset:22016\n" \
"v_mfma_f32_16x16x32_bf16 a[132:135], v[140:143], v[100:103], a[132:135]\n" \
"global_load_lds_dwordx4 v47, s[50:51] offset:3072\n" \
"v_mfma_f32_16x16x32_bf16 a[136:139], v[144:147], v[96:99], a[136:139]\n" \
"s_add_u32 s52, s52, s74\n" \
"s_addc_u32 s53, s53, 0\n" \
"v_mfma_f32_16x16x32_bf16 a[136:139], v[148:151], v[100:103], a[136:139]\n" \
"s_add_u32 s50, s50, s74\n" \
"s_addc_u32 s51, s51, 0\n" \
"v_mfma_f32_16x16x32_bf16 a[140:143], v[152:155], v[96:99], a[140:143]\n" \
"v_mfma_f32_16x16x32_bf16 a[140:143], v[156:159], v[100:103], a[140:143]\n" \
"v_mfma_f32_16x16x32_bf16 a[172:175], v[152:155], v[104:107], a[172:175]\n" \
"s_mul_i32 s76, s73, 0x10000\n" \
"s_add_u32 s77, s76, 0xfffe8000\n" \
"s_sub_u32 s78, 0xffff8000, s76\n" \
"v_mfma_f32_16x16x32_bf16 a[172:175], v[156:159], v[108:111], a[172:175]\n" \
"v_add_u32 v48, s77, v48\n" \
"v_add_u32 v49, s77, v49\n" \
"v_mfma_f32_16x16x32_bf16 a[168:171], v[144:147], v[104:107], a[168:171]\n" \
"v_add_u32 v50, s78, v50\n" \
"v_add_u32 v51, s78, v51\n" \
"v_mfma_f32_16x16x32_bf16 a[168:171], v[148:151], v[108:111], a[168:171]\n" \
"v_mfma_f32_16x16x32_bf16 a[164:167], v[136:139], v[104:107], a[164:167]\n" \
"v_mfma_f32_16x16x32_bf16 a[164:167], v[140:143], v[108:111], a[164:167]\n" \
"v_mfma_f32_16x16x32_bf16 a[160:163], v[128:131], v[104:107], a[160:163]\n" \
"v_mfma_f32_16x16x32_bf16 a[160:163], v[132:135], v[108:111], a[160:163]\n" \
"s_waitcnt lgkmcnt(0)\n" \
"v_mfma_f32_16x16x32_bf16 a[192:195], v[128:131], v[112:115], a[192:195]\n" \
"v_mfma_f32_16x16x32_bf16 a[192:195], v[132:135], v[116:119], a[192:195]\n" \
"v_mfma_f32_16x16x32_bf16 a[196:199], v[136:139], v[112:115], a[196:199]\n" \
"v_mfma_f32_16x16x32_bf16 a[196:199], v[140:143], v[116:119], a[196:199]\n" \
"v_mfma_f32_16x16x32_bf16 a[200:203], v[144:147], v[112:115], a[200:203]\n" \
"v_mfma_f32_16x16x32_bf16 a[200:203], v[148:151], v[116:119], a[200:203]\n" \
"v_mfma_f32_16x16x32_bf16 a[204:207], v[152:155], v[112:115], a[204:207]\n" \
"v_mfma_f32_16x16x32_bf16 a[204:207], v[156:159], v[116:119], a[204:207]\n" \
"v_mfma_f32_16x16x32_bf16 a[236:239], v[152:155], v[120:123], a[236:239]\n" \
"v_mfma_f32_16x16x32_bf16 a[236:239], v[156:159], v[124:127], a[236:239]\n" \
"v_mfma_f32_16x16x32_bf16 a[232:235], v[144:147], v[120:123], a[232:235]\n" \
"v_mfma_f32_16x16x32_bf16 a[232:235], v[148:151], v[124:127], a[232:235]\n" \
"v_mfma_f32_16x16x32_bf16 a[228:231], v[136:139], v[120:123], a[228:231]\n" \
"v_mfma_f32_16x16x32_bf16 a[228:231], v[140:143], v[124:127], a[228:231]\n" \
"s_waitcnt vmcnt(11) lgkmcnt(0)\n" \
"v_mfma_f32_16x16x32_bf16 a[224:227], v[128:131], v[120:123], a[224:227]\n" \
"v_mfma_f32_16x16x32_bf16 a[224:227], v[132:135], v[124:127], a[224:227]\n" \
"s_barrier\n" \
"v_mfma_f32_16x16x32_bf16 a[16:19], v[64:67], v[160:163], a[16:19]\n" \
"s_add_u32 m0, s87, 69632\n" \
"ds_read_b64_tr_b16 v[128:129], v50 offset:0\n" \
"v_mfma_f32_16x16x32_bf16 a[16:19], v[68:71], v[164:167], a[16:19]\n" \
"global_load_lds_dwordx4 v32, s[54:55] offset:-4096\n" \
"v_mfma_f32_16x16x32_bf16 a[20:23], v[64:67], v[168:171], a[20:23]\n" \
"ds_read_b64_tr_b16 v[130:131], v50 offset:4096\n" \
"v_mfma_f32_16x16x32_bf16 a[20:23], v[68:71], v[172:175], a[20:23]\n" \
"v_mfma_f32_16x16x32_bf16 a[24:27], v[64:67], v[176:179], a[24:27]\n" \
"ds_read_b64_tr_b16 v[132:133], v50 offset:16384\n" \
"v_mfma_f32_16x16x32_bf16 a[24:27], v[68:71], v[180:183], a[24:27]\n" \
"global_load_lds_dwordx4 v33, s[40:41] offset:-3072\n" \
"v_mfma_f32_16x16x32_bf16 a[28:31], v[64:67], v[184:187], a[28:31]\n" \
"ds_read_b64_tr_b16 v[134:135], v50 offset:20480\n" \
"v_mfma_f32_16x16x32_bf16 a[28:31], v[68:71], v[188:191], a[28:31]\n" \
"v_mfma_f32_16x16x32_bf16 a[60:63], v[72:75], v[184:187], a[60:63]\n" \
"ds_read_b128 v[64:67], v48 offset:0\n" \
"v_mfma_f32_16x16x32_bf16 a[60:63], v[76:79], v[188:191], a[60:63]\n" \
"global_load_lds_dwordx4 v34, s[54:55] offset:-2048\n" \
"v_mfma_f32_16x16x32_bf16 a[56:59], v[72:75], v[176:179], a[56:59]\n" \
"ds_read_b64_tr_b16 v[136:137], v51 offset:0\n" \
"v_mfma_f32_16x16x32_bf16 a[56:59], v[76:79], v[180:183], a[56:59]\n" \
"v_mfma_f32_16x16x32_bf16 a[52:55], v[72:75], v[168:171], a[52:55]\n" \
"ds_read_b128 v[68:71], v49 offset:0\n" \
"v_mfma_f32_16x16x32_bf16 a[52:55], v[76:79], v[172:175], a[52:55]\n" \
"global_load_lds_dwordx4 v35, s[40:41] offset:-1024\n" \
"v_mfma_f32_16x16x32_bf16 a[48:51], v[72:75], v[160:163], a[48:51]\n" \
"ds_read_b64_tr_b16 v[138:139], v51 offset:4096\n" \
"v_mfma_f32_16x16x32_bf16 a[48:51], v[76:79], v[164:167], a[48:51]\n" \
"v_mfma_f32_16x16x32_bf16 a[80:83], v[80:83], v[160:163], a[80:83]\n" \
"ds_read_b128 v[72:75], v48 offset:2048\n" \
"v_mfma_f32_16x16x32_bf16 a[80:83], v[84:87], v[164:167], a[80:83]\n" \
"global_load_lds_dwordx4 v36, s[54:55] offset:0\n" \
"v_mfma_f32_16x16x32_bf16 a[84:87], v[80:83], v[168:171], a[84:87]\n" \
"ds_read_b64_tr_b16 v[140:141], v51 offset:16384\n" \
"v_mfma_f32_16x16x32_bf16 a[84:87], v[84:87], v[172:175], a[84:87]\n" \
"v_mfma_f32_16x16x32_bf16 a[88:91], v[80:83], v[176:179], a[88:91]\n" \
"ds_read_b128 v[76:79], v49 offset:2048\n" \
"v_mfma_f32_16x16x32_bf16 a[88:91], v[84:87], v[180:183], a[88:91]\n" \
"global_load_lds_dwordx4 v37, s[40:41] offset:1024\n" \
"v_mfma_f32_16x16x32_bf16 a[92:95], v[80:83], v[184:187], a[92:95]\n" \
"ds_read_b64_tr_b16 v[142:143], v51 offset:20480\n" \
"v_mfma_f32_16x16x32_bf16 a[92:95], v[84:87], v[188:191], a[92:95]\n" \
"v_mfma_f32_16x16x32_bf16 a[124:127], v[88:91], v[184:187], a[124:127]\n" \
"ds_read_b128 v[80:83], v48 offset:4096\n" \
"v_mfma_f32_16x16x32_bf16 a[124:127], v[92:95], v[188:191], a[124:127]\n" \
"global_load_lds_dwordx4 v38, s[54:55] offset:2048\n" \
"v_mfma_f32_16x16x32_bf16 a[120:123], v[88:91], v[176:179], a[120:123]\n" \
"ds_read_b64_tr_b16 v[144:145], v50 offset:512\n" \
"v_mfma_f32_16x16x32_bf16 a[120:123], v[92:95], v[180:183], a[120:123]\n" \
"v_mfma_f32_16x16x32_bf16 a[116:119], v[88:91], v[168:171], a[116:119]\n" \
"ds_read_b128 v[84:87], v49 offset:4096\n" \
"global_load_lds_dwordx4 v39, s[40:41] offset:3072\n" \
"v_mfma_f32_16x16x32_bf16 a[116:119], v[92:95], v[172:175], a[116:119]\n" \
"s_add_u32 s54, s54, s75\n" \
"s_addc_u32 s55, s55, 0\n" \
"v_mfma_f32_16x16x32_bf16 a[112:115], v[88:91], v[160:163], a[112:115]\n" \
"s_add_u32 s40, s40, s75\n" \
"s_addc_u32 s41, s41, 0\n" \
"ds_read_b64_tr_b16 v[146:147], v50 offset:4608\n" \
"v_mfma_f32_16x16x32_bf16 a[112:115], v[92:95], v[164:167], a[112:115]\n" \
"s_add_u32 s68, s67, 0x100000\n" \
"s_add_u32 s64, s12, s68\n" \
"s_addc_u32 s65, s13, 0\n" \
"v_mfma_f32_16x16x32_bf16 a[144:147], v[96:99], v[160:163], a[144:147]\n" \
"ds_read_b128 v[88:91], v48 offset:6144\n" \
"v_mfma_f32_16x16x32_bf16 a[144:147], v[100:103], v[164:167], a[144:147]\n" \
"global_load_dword v253, v248, s[64:65]\n" \
"v_mfma_f32_16x16x32_bf16 a[148:151], v[96:99], v[168:171], a[148:151]\n" \
"s_add_u32 s68, s67, 0x500000\n" \
"ds_read_b64_tr_b16 v[148:149], v50 offset:16896\n" \
"v_mfma_f32_16x16x32_bf16 a[148:151], v[100:103], v[172:175], a[148:151]\n" \
"s_add_u32 s64, s12, s68\n" \
"s_addc_u32 s65, s13, 0\n" \
"v_mfma_f32_16x16x32_bf16 a[152:155], v[96:99], v[176:179], a[152:155]\n" \
"ds_read_b128 v[92:95], v49 offset:6144\n" \
"v_mfma_f32_16x16x32_bf16 a[152:155], v[100:103], v[180:183], a[152:155]\n" \
"global_load_dword v253, v248, s[64:65]\n" \
"v_mfma_f32_16x16x32_bf16 a[156:159], v[96:99], v[184:187], a[156:159]\n" \
"ds_read_b64_tr_b16 v[150:151], v50 offset:20992\n" \
"v_mfma_f32_16x16x32_bf16 a[156:159], v[100:103], v[188:191], a[156:159]\n" \
"v_mfma_f32_16x16x32_bf16 a[188:191], v[104:107], v[184:187], a[188:191]\n" \
"ds_read_b128 v[96:99], v48 offset:8192\n" \
"v_mfma_f32_16x16x32_bf16 a[188:191], v[108:111], v[188:191], a[188:191]\n" \
"v_mfma_f32_16x16x32_bf16 a[184:187], v[104:107], v[176:179], a[184:187]\n" \
"ds_read_b64_tr_b16 v[152:153], v51 offset:512\n" \
"v_mfma_f32_16x16x32_bf16 a[184:187], v[108:111], v[180:183], a[184:187]\n" \
"v_mfma_f32_16x16x32_bf16 a[180:183], v[104:107], v[168:171], a[180:183]\n" \
"ds_read_b128 v[100:103], v49 offset:8192\n" \
"v_mfma_f32_16x16x32_bf16 a[180:183], v[108:111], v[172:175], a[180:183]\n" \
"v_mfma_f32_16x16x32_bf16 a[176:179], v[104:107], v[160:163], a[176:179]\n" \
"ds_read_b64_tr_b16 v[154:155], v51 offset:4608\n" \
"v_mfma_f32_16x16x32_bf16 a[176:179], v[108:111], v[164:167], a[176:179]\n" \
"v_mfma_f32_16x16x32_bf16 a[208:211], v[112:115], v[160:163], a[208:211]\n" \
"ds_read_b128 v[104:107], v48 offset:10240\n" \
"v_mfma_f32_16x16x32_bf16 a[208:211], v[116:119], v[164:167], a[208:211]\n" \
"v_mfma_f32_16x16x32_bf16 a[212:215], v[112:115], v[168:171], a[212:215]\n" \
"ds_read_b64_tr_b16 v[156:157], v51 offset:16896\n" \
"v_mfma_f32_16x16x32_bf16 a[212:215], v[116:119], v[172:175], a[212:215]\n" \
"v_mfma_f32_16x16x32_bf16 a[216:219], v[112:115], v[176:179], a[216:219]\n" \
"ds_read_b128 v[108:111], v49 offset:10240\n" \
"v_mfma_f32_16x16x32_bf16 a[216:219], v[116:119], v[180:183], a[216:219]\n" \
"v_mfma_f32_16x16x32_bf16 a[220:223], v[112:115], v[184:187], a[220:223]\n" \
"ds_read_b64_tr_b16 v[158:159], v51 offset:20992\n" \
"v_mfma_f32_16x16x32_bf16 a[220:223], v[116:119], v[188:191], a[220:223]\n" \
"v_mfma_f32_16x16x32_bf16 a[252:255], v[120:123], v[184:187], a[252:255]\n" \
"ds_read_b128 v[112:115], v48 offset:12288\n" \
"v_mfma_f32_16x16x32_bf16 a[252:255], v[124:127], v[188:191], a[252:255]\n" \
"v_mfma_f32_16x16x32_bf16 a[248:251], v[120:123], v[176:179], a[248:251]\n" \
"v_mfma_f32_16x16x32_bf16 a[248:251], v[124:127], v[180:183], a[248:251]\n" \
"v_mfma_f32_16x16x32_bf16 a[244:247], v[120:123], v[168:171], a[244:247]\n" \
"ds_read_b128 v[116:119], v49 offset:12288\n" \
"v_mfma_f32_16x16x32_bf16 a[244:247], v[124:127], v[172:175], a[244:247]\n" \
"v_mfma_f32_16x16x32_bf16 a[240:243], v[120:123], v[160:163], a[240:243]\n" \
"v_mfma_f32_16x16x32_bf16 a[240:243], v[124:127], v[164:167], a[240:243]\n" \
"s_waitcnt lgkmcnt(1)\n" \
"v_mfma_f32_16x16x32_bf16 a[0:3], v[128:131], v[64:67], a[0:3]\n" \
"ds_read_b128 v[120:123], v48 offset:14336\n" \
"v_mfma_f32_16x16x32_bf16 a[0:3], v[132:135], v[68:71], a[0:3]\n" \
"v_mfma_f32_16x16x32_bf16 a[4:7], v[136:139], v[64:67], a[4:7]\n" \
"ds_read_b128 v[124:127], v49 offset:14336\n" \
"v_mfma_f32_16x16x32_bf16 a[4:7], v[140:143], v[68:71], a[4:7]\n" \
"s_add_u32 s68, s67, 0x200000\n" \
"v_mfma_f32_16x16x32_bf16 a[8:11], v[144:147], v[64:67], a[8:11]\n" \
"s_add_u32 s64, s12, s68\n" \
"s_addc_u32 s65, s13, 0\n" \
"ds_read_b64_tr_b16 v[160:161], v50 offset:1024\n" \
"v_mfma_f32_16x16x32_bf16 a[8:11], v[148:151], v[68:71], a[8:11]\n" \
"global_load_dword v253, v248, s[64:65]\n" \
"v_mfma_f32_16x16x32_bf16 a[12:15], v[152:155], v[64:67], a[12:15]\n" \
"s_add_u32 m0, s87, 102400\n" \
"ds_read_b64_tr_b16 v[162:163], v50 offset:5120\n" \
"v_mfma_f32_16x16x32_bf16 a[12:15], v[156:159], v[68:71], a[12:15]\n" \
"global_load_lds_dwordx4 v40, s[52:53] offset:-4096\n" \
"v_mfma_f32_16x16x32_bf16 a[44:47], v[152:155], v[72:75], a[44:47]\n" \
"ds_read_b64_tr_b16 v[164:165], v50 offset:17408\n" \
"v_mfma_f32_16x16x32_bf16 a[44:47], v[156:159], v[76:79], a[44:47]\n" \
"v_mfma_f32_16x16x32_bf16 a[40:43], v[144:147], v[72:75], a[40:43]\n" \
"ds_read_b64_tr_b16 v[166:167], v50 offset:21504\n" \
"v_mfma_f32_16x16x32_bf16 a[40:43], v[148:151], v[76:79], a[40:43]\n" \
"global_load_lds_dwordx4 v41, s[50:51] offset:-3072\n" \
"v_mfma_f32_16x16x32_bf16 a[36:39], v[136:139], v[72:75], a[36:39]\n" \
"ds_read_b64_tr_b16 v[168:169], v51 offset:1024\n" \
"v_mfma_f32_16x16x32_bf16 a[36:39], v[140:143], v[76:79], a[36:39]\n" \
"v_mfma_f32_16x16x32_bf16 a[32:35], v[128:131], v[72:75], a[32:35]\n" \
"ds_read_b64_tr_b16 v[170:171], v51 offset:5120\n" \
"v_mfma_f32_16x16x32_bf16 a[32:35], v[132:135], v[76:79], a[32:35]\n" \
"global_load_lds_dwordx4 v42, s[52:53] offset:-2048\n" \
"v_mfma_f32_16x16x32_bf16 a[64:67], v[128:131], v[80:83], a[64:67]\n" \
"ds_read_b64_tr_b16 v[172:173], v51 offset:17408\n" \
"v_mfma_f32_16x16x32_bf16 a[64:67], v[132:135], v[84:87], a[64:67]\n" \
"v_mfma_f32_16x16x32_bf16 a[68:71], v[136:139], v[80:83], a[68:71]\n" \
"ds_read_b64_tr_b16 v[174:175], v51 offset:21504\n" \
"v_mfma_f32_16x16x32_bf16 a[68:71], v[140:143], v[84:87], a[68:71]\n" \
"global_load_lds_dwordx4 v43, s[50:51] offset:-1024\n" \
"v_mfma_f32_16x16x32_bf16 a[72:75], v[144:147], v[80:83], a[72:75]\n" \
"ds_read_b64_tr_b16 v[176:177], v50 offset:1536\n" \
"v_mfma_f32_16x16x32_bf16 a[72:75], v[148:151], v[84:87], a[72:75]\n" \
"v_mfma_f32_16x16x32_bf16 a[76:79], v[152:155], v[80:83], a[76:79]\n" \
"ds_read_b64_tr_b16 v[178:179], v50 offset:5632\n" \
"v_mfma_f32_16x16x32_bf16 a[76:79], v[156:159], v[84:87], a[76:79]\n" \
"global_load_lds_dwordx4 v44, s[52:53] offset:0\n" \
"v_mfma_f32_16x16x32_bf16 a[108:111], v[152:155], v[88:91], a[108:111]\n" \
"ds_read_b64_tr_b16 v[180:181], v50 offset:17920\n" \
"v_mfma_f32_16x16x32_bf16 a[108:111], v[156:159], v[92:95], a[108:111]\n" \
"v_mfma_f32_16x16x32_bf16 a[104:107], v[144:147], v[88:91], a[104:107]\n" \
"ds_read_b64_tr_b16 v[182:183], v50 offset:22016\n" \
"v_mfma_f32_16x16x32_bf16 a[104:107], v[148:151], v[92:95], a[104:107]\n" \
"global_load_lds_dwordx4 v45, s[50:51] offset:1024\n" \
"v_mfma_f32_16x16x32_bf16 a[100:103], v[136:139], v[88:91], a[100:103]\n" \
"ds_read_b64_tr_b16 v[184:185], v51 offset:1536\n" \
"v_mfma_f32_16x16x32_bf16 a[100:103], v[140:143], v[92:95], a[100:103]\n" \
"v_mfma_f32_16x16x32_bf16 a[96:99], v[128:131], v[88:91], a[96:99]\n" \
"ds_read_b64_tr_b16 v[186:187], v51 offset:5632\n" \
"v_mfma_f32_16x16x32_bf16 a[96:99], v[132:135], v[92:95], a[96:99]\n" \
"global_load_lds_dwordx4 v46, s[52:53] offset:2048\n" \
"v_mfma_f32_16x16x32_bf16 a[128:131], v[128:131], v[96:99], a[128:131]\n" \
"ds_read_b64_tr_b16 v[188:189], v51 offset:17920\n" \
"v_mfma_f32_16x16x32_bf16 a[128:131], v[132:135], v[100:103], a[128:131]\n" \
"v_mfma_f32_16x16x32_bf16 a[132:135], v[136:139], v[96:99], a[132:135]\n" \
"ds_read_b64_tr_b16 v[190:191], v51 offset:22016\n" \
"v_mfma_f32_16x16x32_bf16 a[132:135], v[140:143], v[100:103], a[132:135]\n" \
"global_load_lds_dwordx4 v47, s[50:51] offset:3072\n" \
"v_mfma_f32_16x16x32_bf16 a[136:139], v[144:147], v[96:99], a[136:139]\n" \
"v_mfma_f32_16x16x32_bf16 a[136:139], v[148:151], v[100:103], a[136:139]\n" \
"v_mfma_f32_16x16x32_bf16 a[140:143], v[152:155], v[96:99], a[140:143]\n" \
"v_mfma_f32_16x16x32_bf16 a[140:143], v[156:159], v[100:103], a[140:143]\n" \
"v_mfma_f32_16x16x32_bf16 a[172:175], v[152:155], v[104:107], a[172:175]\n" \
"s_mul_i32 s76, s73, 0x8000\n" \
"s_add_u32 s77, s76, 0x10000\n" \
"s_sub_u32 s78, 0x18000, s76\n" \
"v_mfma_f32_16x16x32_bf16 a[172:175], v[156:159], v[108:111], a[172:175]\n" \
"v_add_u32 v48, s77, v48\n" \
"v_add_u32 v49, s77, v49\n" \
"v_mfma_f32_16x16x32_bf16 a[168:171], v[144:147], v[104:107], a[168:171]\n" \
"v_add_u32 v50, s78, v50\n" \
"v_add_u32 v51, s78, v51\n" \
"v_mfma_f32_16x16x32_bf16 a[168:171], v[148:151], v[108:111], a[168:171]\n" \
"v_mfma_f32_16x16x32_bf16 a[164:167], v[136:139], v[104:107], a[164:167]\n" \
"v_mfma_f32_16x16x32_bf16 a[164:167], v[140:143], v[108:111], a[164:167]\n" \
"v_mfma_f32_16x16x32_bf16 a[160:163], v[128:131], v[104:107], a[160:163]\n" \
"v_mfma_f32_16x16x32_bf16 a[160:163], v[132:135], v[108:111], a[160:163]\n" \
"s_waitcnt lgkmcnt(0)\n" \
"v_mfma_f32_16x16x32_bf16 a[192:195], v[128:131], v[112:115], a[192:195]\n" \
"v_mfma_f32_16x16x32_bf16 a[192:195], v[132:135], v[116:119], a[192:195]\n" \
"v_mfma_f32_16x16x32_bf16 a[196:199], v[136:139], v[112:115], a[196:199]\n" \
"v_mfma_f32_16x16x32_bf16 a[196:199], v[140:143], v[116:119], a[196:199]\n" \
"v_mfma_f32_16x16x32_bf16 a[200:203], v[144:147], v[112:115], a[200:203]\n" \
"v_mfma_f32_16x16x32_bf16 a[200:203], v[148:151], v[116:119], a[200:203]\n" \
"v_mfma_f32_16x16x32_bf16 a[204:207], v[152:155], v[112:115], a[204:207]\n" \
"v_mfma_f32_16x16x32_bf16 a[204:207], v[156:159], v[116:119], a[204:207]\n" \
"v_mfma_f32_16x16x32_bf16 a[236:239], v[152:155], v[120:123], a[236:239]\n" \
"v_mfma_f32_16x16x32_bf16 a[236:239], v[156:159], v[124:127], a[236:239]\n" \
"v_mfma_f32_16x16x32_bf16 a[232:235], v[144:147], v[120:123], a[232:235]\n" \
"v_mfma_f32_16x16x32_bf16 a[232:235], v[148:151], v[124:127], a[232:235]\n" \
"v_mfma_f32_16x16x32_bf16 a[228:231], v[136:139], v[120:123], a[228:231]\n" \
"v_mfma_f32_16x16x32_bf16 a[228:231], v[140:143], v[124:127], a[228:231]\n" \
"s_waitcnt vmcnt(11) lgkmcnt(0)\n" \
"v_mfma_f32_16x16x32_bf16 a[224:227], v[128:131], v[120:123], a[224:227]\n" \
"v_mfma_f32_16x16x32_bf16 a[224:227], v[132:135], v[124:127], a[224:227]\n" \
"s_barrier\n" \
"v_mfma_f32_16x16x32_bf16 a[16:19], v[64:67], v[160:163], a[16:19]\n" \
"s_add_u32 m0, s87, 36864\n" \
"ds_read_b64_tr_b16 v[128:129], v50 offset:0\n" \
"v_mfma_f32_16x16x32_bf16 a[16:19], v[68:71], v[164:167], a[16:19]\n" \
"global_load_lds_dwordx4 v32, s[54:55] offset:-4096\n" \
"v_mfma_f32_16x16x32_bf16 a[20:23], v[64:67], v[168:171], a[20:23]\n" \
"ds_read_b64_tr_b16 v[130:131], v50 offset:4096\n" \
"v_mfma_f32_16x16x32_bf16 a[20:23], v[68:71], v[172:175], a[20:23]\n" \
"v_mfma_f32_16x16x32_bf16 a[24:27], v[64:67], v[176:179], a[24:27]\n" \
"ds_read_b64_tr_b16 v[132:133], v50 offset:16384\n" \
"v_mfma_f32_16x16x32_bf16 a[24:27], v[68:71], v[180:183], a[24:27]\n" \
"global_load_lds_dwordx4 v33, s[40:41] offset:-3072\n" \
"v_mfma_f32_16x16x32_bf16 a[28:31], v[64:67], v[184:187], a[28:31]\n" \
"ds_read_b64_tr_b16 v[134:135], v50 offset:20480\n" \
"v_mfma_f32_16x16x32_bf16 a[28:31], v[68:71], v[188:191], a[28:31]\n" \
"v_mfma_f32_16x16x32_bf16 a[60:63], v[72:75], v[184:187], a[60:63]\n" \
"ds_read_b128 v[64:67], v48 offset:0\n" \
"v_mfma_f32_16x16x32_bf16 a[60:63], v[76:79], v[188:191], a[60:63]\n" \
"global_load_lds_dwordx4 v34, s[54:55] offset:-2048\n" \
"v_mfma_f32_16x16x32_bf16 a[56:59], v[72:75], v[176:179], a[56:59]\n" \
"ds_read_b64_tr_b16 v[136:137], v51 offset:0\n" \
"v_mfma_f32_16x16x32_bf16 a[56:59], v[76:79], v[180:183], a[56:59]\n" \
"v_mfma_f32_16x16x32_bf16 a[52:55], v[72:75], v[168:171], a[52:55]\n" \
"ds_read_b128 v[68:71], v49 offset:0\n" \
"v_mfma_f32_16x16x32_bf16 a[52:55], v[76:79], v[172:175], a[52:55]\n" \
"global_load_lds_dwordx4 v35, s[40:41] offset:-1024\n" \
"v_mfma_f32_16x16x32_bf16 a[48:51], v[72:75], v[160:163], a[48:51]\n" \
"ds_read_b64_tr_b16 v[138:139], v51 offset:4096\n" \
"v_mfma_f32_16x16x32_bf16 a[48:51], v[76:79], v[164:167], a[48:51]\n" \
"v_mfma_f32_16x16x32_bf16 a[80:83], v[80:83], v[160:163], a[80:83]\n" \
"ds_read_b128 v[72:75], v48 offset:2048\n" \
"v_mfma_f32_16x16x32_bf16 a[80:83], v[84:87], v[164:167], a[80:83]\n" \
"global_load_lds_dwordx4 v36, s[54:55] offset:0\n" \
"v_mfma_f32_16x16x32_bf16 a[84:87], v[80:83], v[168:171], a[84:87]\n" \
"ds_read_b64_tr_b16 v[140:141], v51 offset:16384\n" \
"v_mfma_f32_16x16x32_bf16 a[84:87], v[84:87], v[172:175], a[84:87]\n" \
"v_mfma_f32_16x16x32_bf16 a[88:91], v[80:83], v[176:179], a[88:91]\n" \
"ds_read_b128 v[76:79], v49 offset:2048\n" \
"v_mfma_f32_16x16x32_bf16 a[88:91], v[84:87], v[180:183], a[88:91]\n" \
"global_load_lds_dwordx4 v37, s[40:41] offset:1024\n" \
"v_mfma_f32_16x16x32_bf16 a[92:95], v[80:83], v[184:187], a[92:95]\n" \
"ds_read_b64_tr_b16 v[142:143], v51 offset:20480\n" \
"v_mfma_f32_16x16x32_bf16 a[92:95], v[84:87], v[188:191], a[92:95]\n" \
"v_mfma_f32_16x16x32_bf16 a[124:127], v[88:91], v[184:187], a[124:127]\n" \
"ds_read_b128 v[80:83], v48 offset:4096\n" \
"v_mfma_f32_16x16x32_bf16 a[124:127], v[92:95], v[188:191], a[124:127]\n" \
"global_load_lds_dwordx4 v38, s[54:55] offset:2048\n" \
"v_mfma_f32_16x16x32_bf16 a[120:123], v[88:91], v[176:179], a[120:123]\n" \
"ds_read_b64_tr_b16 v[144:145], v50 offset:512\n" \
"v_mfma_f32_16x16x32_bf16 a[120:123], v[92:95], v[180:183], a[120:123]\n" \
"v_mfma_f32_16x16x32_bf16 a[116:119], v[88:91], v[168:171], a[116:119]\n" \
"ds_read_b128 v[84:87], v49 offset:4096\n" \
"v_mfma_f32_16x16x32_bf16 a[116:119], v[92:95], v[172:175], a[116:119]\n" \
"global_load_lds_dwordx4 v39, s[40:41] offset:3072\n" \
"s_add_u32 s68, s67, 0x300000\n" \
"v_mfma_f32_16x16x32_bf16 a[112:115], v[88:91], v[160:163], a[112:115]\n" \
"s_add_u32 s64, s12, s68\n" \
"s_addc_u32 s65, s13, 0\n" \
"ds_read_b64_tr_b16 v[146:147], v50 offset:4608\n" \
"v_mfma_f32_16x16x32_bf16 a[112:115], v[92:95], v[164:167], a[112:115]\n" \
"v_mfma_f32_16x16x32_bf16 a[144:147], v[96:99], v[160:163], a[144:147]\n" \
"ds_read_b128 v[88:91], v48 offset:6144\n" \
"v_mfma_f32_16x16x32_bf16 a[144:147], v[100:103], v[164:167], a[144:147]\n" \
"global_load_dword v253, v248, s[64:65]\n" \
"v_mfma_f32_16x16x32_bf16 a[148:151], v[96:99], v[168:171], a[148:151]\n" \
"s_add_u32 s68, s67, 0x400000\n" \
"ds_read_b64_tr_b16 v[148:149], v50 offset:16896\n" \
"v_mfma_f32_16x16x32_bf16 a[148:151], v[100:103], v[172:175], a[148:151]\n" \
"s_add_u32 s64, s12, s68\n" \
"s_addc_u32 s65, s13, 0\n" \
"v_mfma_f32_16x16x32_bf16 a[152:155], v[96:99], v[176:179], a[152:155]\n" \
"ds_read_b128 v[92:95], v49 offset:6144\n" \
"v_mfma_f32_16x16x32_bf16 a[152:155], v[100:103], v[180:183], a[152:155]\n" \
"global_load_dword v253, v248, s[64:65]\n" \
"v_mfma_f32_16x16x32_bf16 a[156:159], v[96:99], v[184:187], a[156:159]\n" \
"ds_read_b64_tr_b16 v[150:151], v50 offset:20992\n" \
"v_mfma_f32_16x16x32_bf16 a[156:159], v[100:103], v[188:191], a[156:159]\n" \
"v_mfma_f32_16x16x32_bf16 a[188:191], v[104:107], v[184:187], a[188:191]\n" \
"ds_read_b128 v[96:99], v48 offset:8192\n" \
"v_mfma_f32_16x16x32_bf16 a[188:191], v[108:111], v[188:191], a[188:191]\n" \
"v_mfma_f32_16x16x32_bf16 a[184:187], v[104:107], v[176:179], a[184:187]\n" \
"ds_read_b64_tr_b16 v[152:153], v51 offset:512\n" \
"v_mfma_f32_16x16x32_bf16 a[184:187], v[108:111], v[180:183], a[184:187]\n" \
"v_mfma_f32_16x16x32_bf16 a[180:183], v[104:107], v[168:171], a[180:183]\n" \
"ds_read_b128 v[100:103], v49 offset:8192\n" \
"v_mfma_f32_16x16x32_bf16 a[180:183], v[108:111], v[172:175], a[180:183]\n" \
"v_mfma_f32_16x16x32_bf16 a[176:179], v[104:107], v[160:163], a[176:179]\n" \
"ds_read_b64_tr_b16 v[154:155], v51 offset:4608\n" \
"v_mfma_f32_16x16x32_bf16 a[176:179], v[108:111], v[164:167], a[176:179]\n" \
"v_mfma_f32_16x16x32_bf16 a[208:211], v[112:115], v[160:163], a[208:211]\n" \
"ds_read_b128 v[104:107], v48 offset:10240\n" \
"v_mfma_f32_16x16x32_bf16 a[208:211], v[116:119], v[164:167], a[208:211]\n" \
"v_mfma_f32_16x16x32_bf16 a[212:215], v[112:115], v[168:171], a[212:215]\n" \
"ds_read_b64_tr_b16 v[156:157], v51 offset:16896\n" \
"v_mfma_f32_16x16x32_bf16 a[212:215], v[116:119], v[172:175], a[212:215]\n" \
"v_mfma_f32_16x16x32_bf16 a[216:219], v[112:115], v[176:179], a[216:219]\n" \
"ds_read_b128 v[108:111], v49 offset:10240\n" \
"v_mfma_f32_16x16x32_bf16 a[216:219], v[116:119], v[180:183], a[216:219]\n" \
"v_mfma_f32_16x16x32_bf16 a[220:223], v[112:115], v[184:187], a[220:223]\n" \
"ds_read_b64_tr_b16 v[158:159], v51 offset:20992\n" \
"v_mfma_f32_16x16x32_bf16 a[220:223], v[116:119], v[188:191], a[220:223]\n" \
"v_mfma_f32_16x16x32_bf16 a[252:255], v[120:123], v[184:187], a[252:255]\n" \
"ds_read_b128 v[112:115], v48 offset:12288\n" \
"v_mfma_f32_16x16x32_bf16 a[252:255], v[124:127], v[188:191], a[252:255]\n" \
"v_mfma_f32_16x16x32_bf16 a[248:251], v[120:123], v[176:179], a[248:251]\n" \
"v_mfma_f32_16x16x32_bf16 a[248:251], v[124:127], v[180:183], a[248:251]\n" \
"v_mfma_f32_16x16x32_bf16 a[244:247], v[120:123], v[168:171], a[244:247]\n" \
"ds_read_b128 v[116:119], v49 offset:12288\n" \
"v_mfma_f32_16x16x32_bf16 a[244:247], v[124:127], v[172:175], a[244:247]\n" \
"v_mfma_f32_16x16x32_bf16 a[240:243], v[120:123], v[160:163], a[240:243]\n" \
"v_mfma_f32_16x16x32_bf16 a[240:243], v[124:127], v[164:167], a[240:243]\n" \
"s_waitcnt lgkmcnt(1)\n" \
"v_mfma_f32_16x16x32_bf16 a[0:3], v[128:131], v[64:67], a[0:3]\n" \
"ds_read_b128 v[120:123], v48 offset:14336\n" \
"v_mfma_f32_16x16x32_bf16 a[0:3], v[132:135], v[68:71], a[0:3]\n" \
"v_mfma_f32_16x16x32_bf16 a[4:7], v[136:139], v[64:67], a[4:7]\n" \
"ds_read_b128 v[124:127], v49 offset:14336\n" \
"s_add_u32 m0, s95, 4096\n" \
"v_mfma_f32_16x16x32_bf16 a[4:7], v[140:143], v[68:71], a[4:7]\n" \
"global_load_lds_dwordx4 v228, s[44:45] offset:-4096\n" \
"v_mfma_f32_16x16x32_bf16 a[8:11], v[144:147], v[64:67], a[8:11]\n" \
"ds_read_b64_tr_b16 v[160:161], v50 offset:1024\n" \
"v_mfma_f32_16x16x32_bf16 a[8:11], v[148:151], v[68:71], a[8:11]\n" \
"v_mfma_f32_16x16x32_bf16 a[12:15], v[152:155], v[64:67], a[12:15]\n" \
"ds_read_b64_tr_b16 v[162:163], v50 offset:5120\n" \
"v_mfma_f32_16x16x32_bf16 a[12:15], v[156:159], v[68:71], a[12:15]\n" \
"global_load_lds_dwordx4 v229, s[44:45] offset:-3072\n" \
"v_mfma_f32_16x16x32_bf16 a[44:47], v[152:155], v[72:75], a[44:47]\n" \
"s_add_u32 s44, s44, 0x180000\n" \
"s_addc_u32 s45, s45, 0\n" \
"ds_read_b64_tr_b16 v[164:165], v50 offset:17408\n" \
"v_mfma_f32_16x16x32_bf16 a[44:47], v[156:159], v[76:79], a[44:47]\n" \
"v_mfma_f32_16x16x32_bf16 a[40:43], v[144:147], v[72:75], a[40:43]\n" \
"ds_read_b64_tr_b16 v[166:167], v50 offset:21504\n" \
"v_mfma_f32_16x16x32_bf16 a[40:43], v[148:151], v[76:79], a[40:43]\n" \
"global_load_lds_dwordx4 v228, s[42:43] offset:-2048\n" \
"v_mfma_f32_16x16x32_bf16 a[36:39], v[136:139], v[72:75], a[36:39]\n" \
"ds_read_b64_tr_b16 v[168:169], v51 offset:1024\n" \
"v_mfma_f32_16x16x32_bf16 a[36:39], v[140:143], v[76:79], a[36:39]\n" \
"v_mfma_f32_16x16x32_bf16 a[32:35], v[128:131], v[72:75], a[32:35]\n" \
"ds_read_b64_tr_b16 v[170:171], v51 offset:5120\n" \
"v_mfma_f32_16x16x32_bf16 a[32:35], v[132:135], v[76:79], a[32:35]\n" \
"global_load_lds_dwordx4 v229, s[42:43] offset:-1024\n" \
"v_mfma_f32_16x16x32_bf16 a[64:67], v[128:131], v[80:83], a[64:67]\n" \
"s_add_u32 s42, s42, 0x180000\n" \
"s_addc_u32 s43, s43, 0\n" \
"ds_read_b64_tr_b16 v[172:173], v51 offset:17408\n" \
"v_mfma_f32_16x16x32_bf16 a[64:67], v[132:135], v[84:87], a[64:67]\n" \
"v_mfma_f32_16x16x32_bf16 a[68:71], v[136:139], v[80:83], a[68:71]\n" \
"s_add_u32 m0, s95, 20480\n" \
"ds_read_b64_tr_b16 v[174:175], v51 offset:21504\n" \
"v_mfma_f32_16x16x32_bf16 a[68:71], v[140:143], v[84:87], a[68:71]\n" \
"global_load_lds_dwordx4 v228, s[44:45] offset:-4096\n" \
"v_mfma_f32_16x16x32_bf16 a[72:75], v[144:147], v[80:83], a[72:75]\n" \
"ds_read_b64_tr_b16 v[176:177], v50 offset:1536\n" \
"v_mfma_f32_16x16x32_bf16 a[72:75], v[148:151], v[84:87], a[72:75]\n" \
"v_mfma_f32_16x16x32_bf16 a[76:79], v[152:155], v[80:83], a[76:79]\n" \
"ds_read_b64_tr_b16 v[178:179], v50 offset:5632\n" \
"v_mfma_f32_16x16x32_bf16 a[76:79], v[156:159], v[84:87], a[76:79]\n" \
"global_load_lds_dwordx4 v229, s[44:45] offset:-3072\n" \
"v_mfma_f32_16x16x32_bf16 a[108:111], v[152:155], v[88:91], a[108:111]\n" \
"s_add_u32 s44, s44, 0x400000\n" \
"Lpg1682_%=:\n" \
"Lst0x2_%=:\n" \
"s_cbranch_execz Lpg3849_%=\n" \
"s_addc_u32 s45, s45, 0\n" \
"ds_read_b64_tr_b16 v[180:181], v50 offset:17920\n" \
"v_mfma_f32_16x16x32_bf16 a[108:111], v[156:159], v[92:95], a[108:111]\n" \
"v_mfma_f32_16x16x32_bf16 a[104:107], v[144:147], v[88:91], a[104:107]\n" \
"ds_read_b64_tr_b16 v[182:183], v50 offset:22016\n" \
"v_mfma_f32_16x16x32_bf16 a[104:107], v[148:151], v[92:95], a[104:107]\n" \
"global_load_lds_dwordx4 v228, s[42:43] offset:-2048\n" \
"v_mfma_f32_16x16x32_bf16 a[100:103], v[136:139], v[88:91], a[100:103]\n" \
"ds_read_b64_tr_b16 v[184:185], v51 offset:1536\n" \
"v_mfma_f32_16x16x32_bf16 a[100:103], v[140:143], v[92:95], a[100:103]\n" \
"v_mfma_f32_16x16x32_bf16 a[96:99], v[128:131], v[88:91], a[96:99]\n" \
"ds_read_b64_tr_b16 v[186:187], v51 offset:5632\n" \
"v_mfma_f32_16x16x32_bf16 a[96:99], v[132:135], v[92:95], a[96:99]\n" \
"global_load_lds_dwordx4 v229, s[42:43] offset:-1024\n" \
"v_mfma_f32_16x16x32_bf16 a[128:131], v[128:131], v[96:99], a[128:131]\n" \
"s_add_u32 s42, s42, 0x400000\n" \
"s_addc_u32 s43, s43, 0\n" \
"ds_read_b64_tr_b16 v[188:189], v51 offset:17920\n" \
"v_mfma_f32_16x16x32_bf16 a[128:131], v[132:135], v[100:103], a[128:131]\n" \
"v_mfma_f32_16x16x32_bf16 a[132:135], v[136:139], v[96:99], a[132:135]\n" \
"ds_read_b64_tr_b16 v[190:191], v51 offset:22016\n" \
"v_mfma_f32_16x16x32_bf16 a[132:135], v[140:143], v[100:103], a[132:135]\n" \
"v_mfma_f32_16x16x32_bf16 a[136:139], v[144:147], v[96:99], a[136:139]\n" \
"v_mfma_f32_16x16x32_bf16 a[136:139], v[148:151], v[100:103], a[136:139]\n" \
"v_mfma_f32_16x16x32_bf16 a[140:143], v[152:155], v[96:99], a[140:143]\n" \
"v_mfma_f32_16x16x32_bf16 a[140:143], v[156:159], v[100:103], a[140:143]\n" \
"v_mfma_f32_16x16x32_bf16 a[172:175], v[152:155], v[104:107], a[172:175]\n" \
"s_mul_i32 s76, s73, 0x0\n" \
"s_add_u32 s77, s76, 0xffff8000\n" \
"s_sub_u32 s78, 0xffff8000, s76\n" \
"v_mfma_f32_16x16x32_bf16 a[172:175], v[156:159], v[108:111], a[172:175]\n" \
"v_add_u32 v48, s77, v48\n" \
"v_add_u32 v49, s77, v49\n" \
"v_mfma_f32_16x16x32_bf16 a[168:171], v[144:147], v[104:107], a[168:171]\n" \
"v_add_u32 v50, s78, v50\n" \
"v_add_u32 v51, s78, v51\n" \
"v_mfma_f32_16x16x32_bf16 a[168:171], v[148:151], v[108:111], a[168:171]\n" \
"v_mfma_f32_16x16x32_bf16 a[164:167], v[136:139], v[104:107], a[164:167]\n" \
"v_mfma_f32_16x16x32_bf16 a[164:167], v[140:143], v[108:111], a[164:167]\n" \
"v_mfma_f32_16x16x32_bf16 a[160:163], v[128:131], v[104:107], a[160:163]\n" \
"v_mfma_f32_16x16x32_bf16 a[160:163], v[132:135], v[108:111], a[160:163]\n" \
"s_waitcnt lgkmcnt(0)\n" \
"v_mfma_f32_16x16x32_bf16 a[192:195], v[128:131], v[112:115], a[192:195]\n" \
"v_mfma_f32_16x16x32_bf16 a[192:195], v[132:135], v[116:119], a[192:195]\n" \
"v_mfma_f32_16x16x32_bf16 a[196:199], v[136:139], v[112:115], a[196:199]\n" \
"v_mfma_f32_16x16x32_bf16 a[196:199], v[140:143], v[116:119], a[196:199]\n" \
"v_mfma_f32_16x16x32_bf16 a[200:203], v[144:147], v[112:115], a[200:203]\n" \
"v_mfma_f32_16x16x32_bf16 a[200:203], v[148:151], v[116:119], a[200:203]\n" \
"v_mfma_f32_16x16x32_bf16 a[204:207], v[152:155], v[112:115], a[204:207]\n" \
"v_mfma_f32_16x16x32_bf16 a[204:207], v[156:159], v[116:119], a[204:207]\n" \
"v_mfma_f32_16x16x32_bf16 a[236:239], v[152:155], v[120:123], a[236:239]\n" \
"v_mfma_f32_16x16x32_bf16 a[236:239], v[156:159], v[124:127], a[236:239]\n" \
"v_mfma_f32_16x16x32_bf16 a[232:235], v[144:147], v[120:123], a[232:235]\n" \
"v_mfma_f32_16x16x32_bf16 a[232:235], v[148:151], v[124:127], a[232:235]\n" \
"v_mfma_f32_16x16x32_bf16 a[228:231], v[136:139], v[120:123], a[228:231]\n" \
"v_mfma_f32_16x16x32_bf16 a[228:231], v[140:143], v[124:127], a[228:231]\n" \
"s_waitcnt vmcnt(10) lgkmcnt(0)\n" \
"v_mfma_f32_16x16x32_bf16 a[224:227], v[128:131], v[120:123], a[224:227]\n" \
"v_mfma_f32_16x16x32_bf16 a[224:227], v[132:135], v[124:127], a[224:227]\n" \
"s_barrier\n" \
"v_mfma_f32_16x16x32_bf16 a[16:19], v[64:67], v[160:163], a[16:19]\n" \
"s_add_u32 m0, s87, 135168\n" \
"ds_read_b64_tr_b16 v[128:129], v50 offset:0\n" \
"v_mfma_f32_16x16x32_bf16 a[16:19], v[68:71], v[164:167], a[16:19]\n" \
"global_load_lds_dwordx4 v224, s[48:49] offset:-4096\n" \
"v_mfma_f32_16x16x32_bf16 a[20:23], v[64:67], v[168:171], a[20:23]\n" \
"ds_read_b64_tr_b16 v[130:131], v50 offset:4096\n" \
"v_mfma_f32_16x16x32_bf16 a[20:23], v[68:71], v[172:175], a[20:23]\n" \
"v_mfma_f32_16x16x32_bf16 a[24:27], v[64:67], v[176:179], a[24:27]\n" \
"ds_read_b64_tr_b16 v[132:133], v50 offset:16384\n" \
"v_mfma_f32_16x16x32_bf16 a[24:27], v[68:71], v[180:183], a[24:27]\n" \
"global_load_lds_dwordx4 v225, s[48:49] offset:-3072\n" \
"v_mfma_f32_16x16x32_bf16 a[28:31], v[64:67], v[184:187], a[28:31]\n" \
"ds_read_b64_tr_b16 v[134:135], v50 offset:20480\n" \
"v_mfma_f32_16x16x32_bf16 a[28:31], v[68:71], v[188:191], a[28:31]\n" \
"v_mfma_f32_16x16x32_bf16 a[60:63], v[72:75], v[184:187], a[60:63]\n" \
"ds_read_b128 v[64:67], v48 offset:0\n" \
"v_mfma_f32_16x16x32_bf16 a[60:63], v[76:79], v[188:191], a[60:63]\n" \
"global_load_lds_dwordx4 v226, s[48:49] offset:-2048\n" \
"v_mfma_f32_16x16x32_bf16 a[56:59], v[72:75], v[176:179], a[56:59]\n" \
"ds_read_b64_tr_b16 v[136:137], v51 offset:0\n" \
"v_mfma_f32_16x16x32_bf16 a[56:59], v[76:79], v[180:183], a[56:59]\n" \
"v_mfma_f32_16x16x32_bf16 a[52:55], v[72:75], v[168:171], a[52:55]\n" \
"ds_read_b128 v[68:71], v49 offset:0\n" \
"v_mfma_f32_16x16x32_bf16 a[52:55], v[76:79], v[172:175], a[52:55]\n" \
"global_load_lds_dwordx4 v227, s[48:49] offset:-1024\n" \
"v_mfma_f32_16x16x32_bf16 a[48:51], v[72:75], v[160:163], a[48:51]\n" \
"s_add_u32 s48, s48, 0x180\n" \
"s_addc_u32 s49, s49, 0\n" \
"ds_read_b64_tr_b16 v[138:139], v51 offset:4096\n" \
"v_mfma_f32_16x16x32_bf16 a[48:51], v[76:79], v[164:167], a[48:51]\n" \
"v_mfma_f32_16x16x32_bf16 a[80:83], v[80:83], v[160:163], a[80:83]\n" \
"ds_read_b128 v[72:75], v48 offset:2048\n" \
"v_mfma_f32_16x16x32_bf16 a[80:83], v[84:87], v[164:167], a[80:83]\n" \
"global_load_lds_dwordx4 v224, s[46:47] offset:0\n" \
"v_mfma_f32_16x16x32_bf16 a[84:87], v[80:83], v[168:171], a[84:87]\n" \
"ds_read_b64_tr_b16 v[140:141], v51 offset:16384\n" \
"v_mfma_f32_16x16x32_bf16 a[84:87], v[84:87], v[172:175], a[84:87]\n" \
"v_mfma_f32_16x16x32_bf16 a[88:91], v[80:83], v[176:179], a[88:91]\n" \
"ds_read_b128 v[76:79], v49 offset:2048\n" \
"v_mfma_f32_16x16x32_bf16 a[88:91], v[84:87], v[180:183], a[88:91]\n" \
"global_load_lds_dwordx4 v225, s[46:47] offset:1024\n" \
"v_mfma_f32_16x16x32_bf16 a[92:95], v[80:83], v[184:187], a[92:95]\n" \
"ds_read_b64_tr_b16 v[142:143], v51 offset:20480\n" \
"v_mfma_f32_16x16x32_bf16 a[92:95], v[84:87], v[188:191], a[92:95]\n" \
"v_mfma_f32_16x16x32_bf16 a[124:127], v[88:91], v[184:187], a[124:127]\n" \
"ds_read_b128 v[80:83], v48 offset:4096\n" \
"v_mfma_f32_16x16x32_bf16 a[124:127], v[92:95], v[188:191], a[124:127]\n" \
"global_load_lds_dwordx4 v226, s[46:47] offset:2048\n" \
"v_mfma_f32_16x16x32_bf16 a[120:123], v[88:91], v[176:179], a[120:123]\n" \
"ds_read_b64_tr_b16 v[144:145], v50 offset:512\n" \
"v_mfma_f32_16x16x32_bf16 a[120:123], v[92:95], v[180:183], a[120:123]\n" \
"v_mfma_f32_16x16x32_bf16 a[116:119], v[88:91], v[168:171], a[116:119]\n" \
"ds_read_b128 v[84:87], v49 offset:4096\n" \
"v_mfma_f32_16x16x32_bf16 a[116:119], v[92:95], v[172:175], a[116:119]\n" \
"global_load_lds_dwordx4 v227, s[46:47] offset:3072\n" \
"v_mfma_f32_16x16x32_bf16 a[112:115], v[88:91], v[160:163], a[112:115]\n" \
"s_add_u32 s46, s46, 0x180\n" \
"s_addc_u32 s47, s47, 0\n" \
"ds_read_b64_tr_b16 v[146:147], v50 offset:4608\n" \
"v_mfma_f32_16x16x32_bf16 a[112:115], v[92:95], v[164:167], a[112:115]\n" \
"s_add_u32 s68, s67, 0x600000\n" \
"s_add_u32 s64, s12, s68\n" \
"s_addc_u32 s65, s13, 0\n" \
"v_mfma_f32_16x16x32_bf16 a[144:147], v[96:99], v[160:163], a[144:147]\n" \
"ds_read_b128 v[88:91], v48 offset:6144\n" \
"v_mfma_f32_16x16x32_bf16 a[144:147], v[100:103], v[164:167], a[144:147]\n" \
"global_load_dword v253, v248, s[64:65]\n" \
"v_mfma_f32_16x16x32_bf16 a[148:151], v[96:99], v[168:171], a[148:151]\n" \
"s_add_u32 s68, s67, 0x700000\n" \
"ds_read_b64_tr_b16 v[148:149], v50 offset:16896\n" \
"v_mfma_f32_16x16x32_bf16 a[148:151], v[100:103], v[172:175], a[148:151]\n" \
"s_add_u32 s64, s12, s68\n" \
"s_addc_u32 s65, s13, 0\n" \
"v_mfma_f32_16x16x32_bf16 a[152:155], v[96:99], v[176:179], a[152:155]\n" \
"ds_read_b128 v[92:95], v49 offset:6144\n" \
"v_mfma_f32_16x16x32_bf16 a[152:155], v[100:103], v[180:183], a[152:155]\n" \
"global_load_dword v253, v248, s[64:65]\n" \
"v_mfma_f32_16x16x32_bf16 a[156:159], v[96:99], v[184:187], a[156:159]\n" \
"s_lshr_b32 s68, s66, 2\n" \
"s_and_b32 s68, s68, 0xffff\n" \
"ds_read_b64_tr_b16 v[150:151], v50 offset:20992\n" \
"v_mfma_f32_16x16x32_bf16 a[156:159], v[100:103], v[188:191], a[156:159]\n" \
"s_add_u32 s68, s68, 65585152\n" \
"v_mfma_f32_16x16x32_bf16 a[188:191], v[104:107], v[184:187], a[188:191]\n" \
"ds_read_b128 v[96:99], v48 offset:8192\n" \
"v_mfma_f32_16x16x32_bf16 a[188:191], v[108:111], v[188:191], a[188:191]\n" \
"buffer_load_dword v253, v245, s[56:59], s68 offen sc1\n" \
"v_mfma_f32_16x16x32_bf16 a[184:187], v[104:107], v[176:179], a[184:187]\n" \
"ds_read_b64_tr_b16 v[152:153], v51 offset:512\n" \
"v_mfma_f32_16x16x32_bf16 a[184:187], v[108:111], v[180:183], a[184:187]\n" \
"v_mfma_f32_16x16x32_bf16 a[180:183], v[104:107], v[168:171], a[180:183]\n" \
"ds_read_b128 v[100:103], v49 offset:8192\n" \
"v_mfma_f32_16x16x32_bf16 a[180:183], v[108:111], v[172:175], a[180:183]\n" \
"v_mfma_f32_16x16x32_bf16 a[176:179], v[104:107], v[160:163], a[176:179]\n" \
"ds_read_b64_tr_b16 v[154:155], v51 offset:4608\n" \
"v_mfma_f32_16x16x32_bf16 a[176:179], v[108:111], v[164:167], a[176:179]\n" \
"v_mfma_f32_16x16x32_bf16 a[208:211], v[112:115], v[160:163], a[208:211]\n" \
"ds_read_b128 v[104:107], v48 offset:10240\n" \
"v_mfma_f32_16x16x32_bf16 a[208:211], v[116:119], v[164:167], a[208:211]\n" \
"v_mfma_f32_16x16x32_bf16 a[212:215], v[112:115], v[168:171], a[212:215]\n" \
"ds_read_b64_tr_b16 v[156:157], v51 offset:16896\n" \
"v_mfma_f32_16x16x32_bf16 a[212:215], v[116:119], v[172:175], a[212:215]\n" \
"v_mfma_f32_16x16x32_bf16 a[216:219], v[112:115], v[176:179], a[216:219]\n" \
"ds_read_b128 v[108:111], v49 offset:10240\n" \
"v_mfma_f32_16x16x32_bf16 a[216:219], v[116:119], v[180:183], a[216:219]\n" \
"v_mfma_f32_16x16x32_bf16 a[220:223], v[112:115], v[184:187], a[220:223]\n" \
"ds_read_b64_tr_b16 v[158:159], v51 offset:20992\n" \
"v_mfma_f32_16x16x32_bf16 a[220:223], v[116:119], v[188:191], a[220:223]\n" \
"v_mfma_f32_16x16x32_bf16 a[252:255], v[120:123], v[184:187], a[252:255]\n" \
"ds_read_b128 v[112:115], v48 offset:12288\n" \
"v_mfma_f32_16x16x32_bf16 a[252:255], v[124:127], v[188:191], a[252:255]\n" \
"v_mfma_f32_16x16x32_bf16 a[248:251], v[120:123], v[176:179], a[248:251]\n" \
"v_mfma_f32_16x16x32_bf16 a[248:251], v[124:127], v[180:183], a[248:251]\n" \
"v_mfma_f32_16x16x32_bf16 a[244:247], v[120:123], v[168:171], a[244:247]\n" \
"ds_read_b128 v[116:119], v49 offset:12288\n" \
"v_mfma_f32_16x16x32_bf16 a[244:247], v[124:127], v[172:175], a[244:247]\n" \
"v_mfma_f32_16x16x32_bf16 a[240:243], v[120:123], v[160:163], a[240:243]\n" \
"v_mfma_f32_16x16x32_bf16 a[240:243], v[124:127], v[164:167], a[240:243]\n" \
"s_waitcnt lgkmcnt(1)\n" \
"v_mfma_f32_16x16x32_bf16 a[0:3], v[128:131], v[64:67], a[0:3]\n" \
"ds_read_b128 v[120:123], v48 offset:14336\n" \
"v_mfma_f32_16x16x32_bf16 a[0:3], v[132:135], v[68:71], a[0:3]\n" \
"v_mfma_f32_16x16x32_bf16 a[4:7], v[136:139], v[64:67], a[4:7]\n" \
"ds_read_b128 v[124:127], v49 offset:14336\n" \
"s_add_u32 m0, s87, 69632\n" \
"v_mfma_f32_16x16x32_bf16 a[4:7], v[140:143], v[68:71], a[4:7]\n" \
"global_load_lds_dwordx4 v224, s[48:49] offset:-4096\n" \
"v_mfma_f32_16x16x32_bf16 a[8:11], v[144:147], v[64:67], a[8:11]\n" \
"ds_read_b64_tr_b16 v[160:161], v50 offset:1024\n" \
"v_mfma_f32_16x16x32_bf16 a[8:11], v[148:151], v[68:71], a[8:11]\n" \
"v_mfma_f32_16x16x32_bf16 a[12:15], v[152:155], v[64:67], a[12:15]\n" \
"ds_read_b64_tr_b16 v[162:163], v50 offset:5120\n" \
"v_mfma_f32_16x16x32_bf16 a[12:15], v[156:159], v[68:71], a[12:15]\n" \
"global_load_lds_dwordx4 v225, s[48:49] offset:-3072\n" \
"v_mfma_f32_16x16x32_bf16 a[44:47], v[152:155], v[72:75], a[44:47]\n" \
"ds_read_b64_tr_b16 v[164:165], v50 offset:17408\n" \
"v_mfma_f32_16x16x32_bf16 a[44:47], v[156:159], v[76:79], a[44:47]\n" \
"v_mfma_f32_16x16x32_bf16 a[40:43], v[144:147], v[72:75], a[40:43]\n" \
"ds_read_b64_tr_b16 v[166:167], v50 offset:21504\n" \
"v_mfma_f32_16x16x32_bf16 a[40:43], v[148:151], v[76:79], a[40:43]\n" \
"global_load_lds_dwordx4 v226, s[48:49] offset:-2048\n" \
"v_mfma_f32_16x16x32_bf16 a[36:39], v[136:139], v[72:75], a[36:39]\n" \
"ds_read_b64_tr_b16 v[168:169], v51 offset:1024\n" \
"v_mfma_f32_16x16x32_bf16 a[36:39], v[140:143], v[76:79], a[36:39]\n" \
"v_mfma_f32_16x16x32_bf16 a[32:35], v[128:131], v[72:75], a[32:35]\n" \
"ds_read_b64_tr_b16 v[170:171], v51 offset:5120\n" \
"v_mfma_f32_16x16x32_bf16 a[32:35], v[132:135], v[76:79], a[32:35]\n" \
"global_load_lds_dwordx4 v227, s[48:49] offset:-1024\n" \
"v_mfma_f32_16x16x32_bf16 a[64:67], v[128:131], v[80:83], a[64:67]\n" \
"s_add_u32 s48, s48, 0x400\n" \
"s_addc_u32 s49, s49, 0\n" \
"ds_read_b64_tr_b16 v[172:173], v51 offset:17408\n" \
"v_mfma_f32_16x16x32_bf16 a[64:67], v[132:135], v[84:87], a[64:67]\n" \
"v_mfma_f32_16x16x32_bf16 a[68:71], v[136:139], v[80:83], a[68:71]\n" \
"ds_read_b64_tr_b16 v[174:175], v51 offset:21504\n" \
"v_mfma_f32_16x16x32_bf16 a[68:71], v[140:143], v[84:87], a[68:71]\n" \
"global_load_lds_dwordx4 v224, s[46:47] offset:0\n" \
"v_mfma_f32_16x16x32_bf16 a[72:75], v[144:147], v[80:83], a[72:75]\n" \
"ds_read_b64_tr_b16 v[176:177], v50 offset:1536\n" \
"v_mfma_f32_16x16x32_bf16 a[72:75], v[148:151], v[84:87], a[72:75]\n" \
"v_mfma_f32_16x16x32_bf16 a[76:79], v[152:155], v[80:83], a[76:79]\n" \
"ds_read_b64_tr_b16 v[178:179], v50 offset:5632\n" \
"v_mfma_f32_16x16x32_bf16 a[76:79], v[156:159], v[84:87], a[76:79]\n" \
"global_load_lds_dwordx4 v225, s[46:47] offset:1024\n" \
"v_mfma_f32_16x16x32_bf16 a[108:111], v[152:155], v[88:91], a[108:111]\n" \
"ds_read_b64_tr_b16 v[180:181], v50 offset:17920\n" \
"v_mfma_f32_16x16x32_bf16 a[108:111], v[156:159], v[92:95], a[108:111]\n" \
"v_mfma_f32_16x16x32_bf16 a[104:107], v[144:147], v[88:91], a[104:107]\n" \
"ds_read_b64_tr_b16 v[182:183], v50 offset:22016\n" \
"v_mfma_f32_16x16x32_bf16 a[104:107], v[148:151], v[92:95], a[104:107]\n" \
"global_load_lds_dwordx4 v226, s[46:47] offset:2048\n" \
"v_mfma_f32_16x16x32_bf16 a[100:103], v[136:139], v[88:91], a[100:103]\n" \
"ds_read_b64_tr_b16 v[184:185], v51 offset:1536\n" \
"v_mfma_f32_16x16x32_bf16 a[100:103], v[140:143], v[92:95], a[100:103]\n" \
"v_mfma_f32_16x16x32_bf16 a[96:99], v[128:131], v[88:91], a[96:99]\n" \
"ds_read_b64_tr_b16 v[186:187], v51 offset:5632\n" \
"v_mfma_f32_16x16x32_bf16 a[96:99], v[132:135], v[92:95], a[96:99]\n" \
"global_load_lds_dwordx4 v227, s[46:47] offset:3072\n" \
"v_mfma_f32_16x16x32_bf16 a[128:131], v[128:131], v[96:99], a[128:131]\n" \
"s_add_u32 s46, s46, 0x400\n" \
"s_addc_u32 s47, s47, 0\n" \
"ds_read_b64_tr_b16 v[188:189], v51 offset:17920\n" \
"v_mfma_f32_16x16x32_bf16 a[128:131], v[132:135], v[100:103], a[128:131]\n" \
"v_mfma_f32_16x16x32_bf16 a[132:135], v[136:139], v[96:99], a[132:135]\n" \
"ds_read_b64_tr_b16 v[190:191], v51 offset:22016\n" \
"v_mfma_f32_16x16x32_bf16 a[132:135], v[140:143], v[100:103], a[132:135]\n" \
"v_mfma_f32_16x16x32_bf16 a[136:139], v[144:147], v[96:99], a[136:139]\n" \
"v_mfma_f32_16x16x32_bf16 a[136:139], v[148:151], v[100:103], a[136:139]\n" \
"v_mfma_f32_16x16x32_bf16 a[140:143], v[152:155], v[96:99], a[140:143]\n" \
"v_mfma_f32_16x16x32_bf16 a[140:143], v[156:159], v[100:103], a[140:143]\n" \
"v_mfma_f32_16x16x32_bf16 a[172:175], v[152:155], v[104:107], a[172:175]\n" \
"v_mfma_f32_16x16x32_bf16 a[172:175], v[156:159], v[108:111], a[172:175]\n" \
"v_mfma_f32_16x16x32_bf16 a[168:171], v[144:147], v[104:107], a[168:171]\n" \
"v_mfma_f32_16x16x32_bf16 a[168:171], v[148:151], v[108:111], a[168:171]\n" \
"v_mfma_f32_16x16x32_bf16 a[164:167], v[136:139], v[104:107], a[164:167]\n" \
"v_mfma_f32_16x16x32_bf16 a[164:167], v[140:143], v[108:111], a[164:167]\n" \
"v_mfma_f32_16x16x32_bf16 a[160:163], v[128:131], v[104:107], a[160:163]\n" \
"v_mfma_f32_16x16x32_bf16 a[160:163], v[132:135], v[108:111], a[160:163]\n" \
"s_waitcnt lgkmcnt(0)\n" \
"v_mfma_f32_16x16x32_bf16 a[192:195], v[128:131], v[112:115], a[192:195]\n" \
"v_mfma_f32_16x16x32_bf16 a[192:195], v[132:135], v[116:119], a[192:195]\n" \
"v_mfma_f32_16x16x32_bf16 a[196:199], v[136:139], v[112:115], a[196:199]\n" \
"v_mfma_f32_16x16x32_bf16 a[196:199], v[140:143], v[116:119], a[196:199]\n" \
"v_mfma_f32_16x16x32_bf16 a[200:203], v[144:147], v[112:115], a[200:203]\n" \
"v_mfma_f32_16x16x32_bf16 a[200:203], v[148:151], v[116:119], a[200:203]\n" \
"v_mfma_f32_16x16x32_bf16 a[204:207], v[152:155], v[112:115], a[204:207]\n" \
"v_mfma_f32_16x16x32_bf16 a[204:207], v[156:159], v[116:119], a[204:207]\n" \
"v_mfma_f32_16x16x32_bf16 a[236:239], v[152:155], v[120:123], a[236:239]\n" \
"v_mfma_f32_16x16x32_bf16 a[236:239], v[156:159], v[124:127], a[236:239]\n" \
"v_mfma_f32_16x16x32_bf16 a[232:235], v[144:147], v[120:123], a[232:235]\n" \
"v_mfma_f32_16x16x32_bf16 a[232:235], v[148:151], v[124:127], a[232:235]\n" \
"v_mfma_f32_16x16x32_bf16 a[228:231], v[136:139], v[120:123], a[228:231]\n" \
"v_mfma_f32_16x16x32_bf16 a[228:231], v[140:143], v[124:127], a[228:231]\n" \
"s_waitcnt vmcnt(11) lgkmcnt(0)\n" \
"v_mfma_f32_16x16x32_bf16 a[224:227], v[128:131], v[120:123], a[224:227]\n" \
"v_mfma_f32_16x16x32_bf16 a[224:227], v[132:135], v[124:127], a[224:227]\n" \
"s_barrier\n" \
"v_mfma_f32_16x16x32_bf16 a[16:19], v[64:67], v[160:163], a[16:19]\n" \
"s_add_u32 m0, s87, 102400\n" \
"v_mfma_f32_16x16x32_bf16 a[16:19], v[68:71], v[164:167], a[16:19]\n" \
"global_load_lds_dwordx4 v224, s[48:49] offset:-4096\n" \
"v_mfma_f32_16x16x32_bf16 a[20:23], v[64:67], v[168:171], a[20:23]\n" \
"ds_read_b64_tr_b16 v[0:1], v232 offset:0\n" \
"v_mfma_f32_16x16x32_bf16 a[20:23], v[68:71], v[172:175], a[20:23]\n" \
"v_accvgpr_read_b32 v234, a192\n" \
"v_accvgpr_read_b32 v235, a193\n" \
"v_mfma_f32_16x16x32_bf16 a[24:27], v[64:67], v[176:179], a[24:27]\n" \
"ds_read_b64_tr_b16 v[2:3], v232 offset:2048\n" \
"v_accvgpr_read_b32 v236, a194\n" \
"v_mfma_f32_16x16x32_bf16 a[24:27], v[68:71], v[180:183], a[24:27]\n" \
"global_load_lds_dwordx4 v225, s[48:49] offset:-3072\n" \
"v_accvgpr_read_b32 v237, a195\n" \
"v_mfma_f32_16x16x32_bf16 a[28:31], v[64:67], v[184:187], a[28:31]\n" \
"ds_read_b64_tr_b16 v[4:5], v232 offset:8192\n" \
"v_mul_f32 v234, s92, v234\n" \
"v_mfma_f32_16x16x32_bf16 a[28:31], v[68:71], v[188:191], a[28:31]\n" \
"v_mul_f32 v235, s92, v235\n" \
"v_mul_f32 v236, s92, v236\n" \
"v_mfma_f32_16x16x32_bf16 a[60:63], v[72:75], v[184:187], a[60:63]\n" \
"ds_read_b64_tr_b16 v[6:7], v232 offset:10240\n" \
"v_mul_f32 v237, s92, v237\n" \
"v_mfma_f32_16x16x32_bf16 a[60:63], v[76:79], v[188:191], a[60:63]\n" \
"global_load_lds_dwordx4 v226, s[48:49] offset:-2048\n" \
"v_cvt_pk_bf16_f32 v234, v234, v235\n" \
"v_mfma_f32_16x16x32_bf16 a[56:59], v[72:75], v[176:179], a[56:59]\n" \
"ds_read_b64_tr_b16 v[8:9], v233 offset:0\n" \
"v_cvt_pk_bf16_f32 v236, v236, v237\n" \
"v_mfma_f32_16x16x32_bf16 a[56:59], v[76:79], v[180:183], a[56:59]\n" \
"v_accvgpr_write_b32 a192, v234\n" \
"v_accvgpr_write_b32 a193, v236\n" \
"v_mfma_f32_16x16x32_bf16 a[52:55], v[72:75], v[168:171], a[52:55]\n" \
"ds_read_b64_tr_b16 v[10:11], v233 offset:2048\n" \
"v_accvgpr_read_b32 v234, a196\n" \
"v_mfma_f32_16x16x32_bf16 a[52:55], v[76:79], v[172:175], a[52:55]\n" \
"global_load_lds_dwordx4 v227, s[48:49] offset:-1024\n" \
"v_accvgpr_read_b32 v235, a197\n" \
"v_mfma_f32_16x16x32_bf16 a[48:51], v[72:75], v[160:163], a[48:51]\n" \
"s_add_u32 s48, s48, 0xfffffb00\n" \
"s_addc_u32 s49, s49, -1\n" \
"ds_read_b64_tr_b16 v[12:13], v233 offset:8192\n" \
"v_mfma_f32_16x16x32_bf16 a[48:51], v[76:79], v[164:167], a[48:51]\n" \
"v_accvgpr_read_b32 v236, a198\n" \
"v_accvgpr_read_b32 v237, a199\n" \
"v_mfma_f32_16x16x32_bf16 a[80:83], v[80:83], v[160:163], a[80:83]\n" \
"ds_read_b64_tr_b16 v[14:15], v233 offset:10240\n" \
"v_mul_f32 v234, s92, v234\n" \
"v_mfma_f32_16x16x32_bf16 a[80:83], v[84:87], v[164:167], a[80:83]\n" \
"global_load_lds_dwordx4 v224, s[46:47] offset:0\n" \
"v_mul_f32 v235, s92, v235\n" \
"v_mfma_f32_16x16x32_bf16 a[84:87], v[80:83], v[168:171], a[84:87]\n" \
"ds_read_b64_tr_b16 v[16:17], v232 offset:512\n" \
"v_mul_f32 v236, s92, v236\n" \
"v_mfma_f32_16x16x32_bf16 a[84:87], v[84:87], v[172:175], a[84:87]\n" \
"v_mul_f32 v237, s92, v237\n" \
"v_cvt_pk_bf16_f32 v234, v234, v235\n" \
"v_mfma_f32_16x16x32_bf16 a[88:91], v[80:83], v[176:179], a[88:91]\n" \
"ds_read_b64_tr_b16 v[18:19], v232 offset:2560\n" \
"v_cvt_pk_bf16_f32 v236, v236, v237\n" \
"v_mfma_f32_16x16x32_bf16 a[88:91], v[84:87], v[180:183], a[88:91]\n" \
"global_load_lds_dwordx4 v225, s[46:47] offset:1024\n" \
"v_accvgpr_write_b32 a194, v234\n" \
"v_mfma_f32_16x16x32_bf16 a[92:95], v[80:83], v[184:187], a[92:95]\n" \
"ds_read_b64_tr_b16 v[20:21], v232 offset:8704\n" \
"v_accvgpr_write_b32 a195, v236\n" \
"v_mfma_f32_16x16x32_bf16 a[92:95], v[84:87], v[188:191], a[92:95]\n" \
"v_accvgpr_read_b32 v234, a224\n" \
"v_accvgpr_read_b32 v235, a225\n" \
"v_mfma_f32_16x16x32_bf16 a[124:127], v[88:91], v[184:187], a[124:127]\n" \
"ds_read_b64_tr_b16 v[22:23], v232 offset:10752\n" \
"v_accvgpr_read_b32 v236, a226\n" \
"v_mfma_f32_16x16x32_bf16 a[124:127], v[92:95], v[188:191], a[124:127]\n" \
"global_load_lds_dwordx4 v226, s[46:47] offset:2048\n" \
"v_accvgpr_read_b32 v237, a227\n" \
"v_mfma_f32_16x16x32_bf16 a[120:123], v[88:91], v[176:179], a[120:123]\n" \
"ds_read_b64_tr_b16 v[24:25], v233 offset:512\n" \
"v_mul_f32 v234, s92, v234\n" \
"v_mfma_f32_16x16x32_bf16 a[120:123], v[92:95], v[180:183], a[120:123]\n" \
"v_mul_f32 v235, s92, v235\n" \
"v_mul_f32 v236, s92, v236\n" \
"v_mfma_f32_16x16x32_bf16 a[116:119], v[88:91], v[168:171], a[116:119]\n" \
"ds_read_b64_tr_b16 v[26:27], v233 offset:2560\n" \
"v_mul_f32 v237, s92, v237\n" \
"v_mfma_f32_16x16x32_bf16 a[116:119], v[92:95], v[172:175], a[116:119]\n" \
"global_load_lds_dwordx4 v227, s[46:47] offset:3072\n" \
"v_cvt_pk_bf16_f32 v234, v234, v235\n" \
"v_mfma_f32_16x16x32_bf16 a[112:115], v[88:91], v[160:163], a[112:115]\n" \
"s_add_u32 s46, s46, 0xfffffb00\n" \
"s_addc_u32 s47, s47, -1\n" \
"ds_read_b64_tr_b16 v[28:29], v233 offset:8704\n" \
"v_mfma_f32_16x16x32_bf16 a[112:115], v[92:95], v[164:167], a[112:115]\n" \
"v_cvt_pk_bf16_f32 v236, v236, v237\n" \
"v_accvgpr_write_b32 a224, v234\n" \
"v_mfma_f32_16x16x32_bf16 a[144:147], v[96:99], v[160:163], a[144:147]\n" \
"s_add_u32 m0, s95, 36864\n" \
"ds_read_b64_tr_b16 v[30:31], v233 offset:10752\n" \
"v_accvgpr_write_b32 a225, v236\n" \
"v_mfma_f32_16x16x32_bf16 a[144:147], v[100:103], v[164:167], a[144:147]\n" \
"global_load_lds_dwordx4 v228, s[44:45] offset:-4096\n" \
"v_accvgpr_read_b32 v234, a228\n" \
"v_mfma_f32_16x16x32_bf16 a[148:151], v[96:99], v[168:171], a[148:151]\n" \
"ds_read_b128 v[192:195], v230 offset:0\n" \
"v_accvgpr_read_b32 v235, a229\n" \
"v_mfma_f32_16x16x32_bf16 a[148:151], v[100:103], v[172:175], a[148:151]\n" \
"v_accvgpr_read_b32 v236, a230\n" \
"v_accvgpr_read_b32 v237, a231\n" \
"v_mfma_f32_16x16x32_bf16 a[152:155], v[96:99], v[176:179], a[152:155]\n" \
"ds_read_b128 v[196:199], v231 offset:0\n" \
"v_mul_f32 v234, s92, v234\n" \
"v_mfma_f32_16x16x32_bf16 a[152:155], v[100:103], v[180:183], a[152:155]\n" \
"global_load_lds_dwordx4 v229, s[44:45] offset:-3072\n" \
"v_mul_f32 v235, s92, v235\n" \
"v_mfma_f32_16x16x32_bf16 a[156:159], v[96:99], v[184:187], a[156:159]\n" \
"s_add_u32 s44, s44, 0xffb00000\n" \
"s_addc_u32 s45, s45, -1\n" \
"ds_read_b128 v[200:203], v230 offset:2048\n" \
"v_mfma_f32_16x16x32_bf16 a[156:159], v[100:103], v[188:191], a[156:159]\n" \
"v_mul_f32 v236, s92, v236\n" \
"v_mul_f32 v237, s92, v237\n" \
"v_mfma_f32_16x16x32_bf16 a[188:191], v[104:107], v[184:187], a[188:191]\n" \
"ds_read_b128 v[204:207], v231 offset:2048\n" \
"v_cvt_pk_bf16_f32 v234, v234, v235\n" \
"v_mfma_f32_16x16x32_bf16 a[188:191], v[108:111], v[188:191], a[188:191]\n" \
"global_load_lds_dwordx4 v228, s[42:43] offset:-2048\n" \
"v_cvt_pk_bf16_f32 v236, v236, v237\n" \
"v_mfma_f32_16x16x32_bf16 a[184:187], v[104:107], v[176:179], a[184:187]\n" \
"ds_read_b128 v[208:211], v230 offset:4096\n" \
"v_accvgpr_write_b32 a226, v234\n" \
"v_mfma_f32_16x16x32_bf16 a[184:187], v[108:111], v[180:183], a[184:187]\n" \
"v_accvgpr_write_b32 a227, v236\n" \
"v_accvgpr_read_b32 v234, a200\n" \
"v_mfma_f32_16x16x32_bf16 a[180:183], v[104:107], v[168:171], a[180:183]\n" \
"ds_read_b128 v[212:215], v231 offset:4096\n" \
"v_accvgpr_read_b32 v235, a201\n" \
"v_mfma_f32_16x16x32_bf16 a[180:183], v[108:111], v[172:175], a[180:183]\n" \
"global_load_lds_dwordx4 v229, s[42:43] offset:-1024\n" \
"v_accvgpr_read_b32 v236, a202\n" \
"v_mfma_f32_16x16x32_bf16 a[176:179], v[104:107], v[160:163], a[176:179]\n" \
"s_add_u32 s42, s42, 0xffb00000\n" \
"s_addc_u32 s43, s43, -1\n" \
"v_accvgpr_read_b32 v237, a203\n" \
"v_mfma_f32_16x16x32_bf16 a[176:179], v[108:111], v[164:167], a[176:179]\n" \
"v_mul_f32 v234, s92, v234\n" \
"v_mul_f32 v235, s92, v235\n" \
"v_mfma_f32_16x16x32_bf16 a[208:211], v[112:115], v[160:163], a[208:211]\n" \
"s_add_u32 m0, s95, 53248\n" \
"v_mul_f32 v236, s92, v236\n" \
"v_mul_f32 v237, s92, v237\n" \
"v_mfma_f32_16x16x32_bf16 a[208:211], v[116:119], v[164:167], a[208:211]\n" \
"global_load_lds_dwordx4 v228, s[44:45] offset:-4096\n" \
"v_cvt_pk_bf16_f32 v234, v234, v235\n" \
"v_mfma_f32_16x16x32_bf16 a[212:215], v[112:115], v[168:171], a[212:215]\n" \
"v_cvt_pk_bf16_f32 v236, v236, v237\n" \
"v_accvgpr_write_b32 a200, v234\n" \
"v_mfma_f32_16x16x32_bf16 a[212:215], v[116:119], v[172:175], a[212:215]\n" \
"v_accvgpr_write_b32 a201, v236\n" \
"v_accvgpr_read_b32 v234, a204\n" \
"v_mfma_f32_16x16x32_bf16 a[216:219], v[112:115], v[176:179], a[216:219]\n" \
"v_accvgpr_read_b32 v235, a205\n" \
"v_accvgpr_read_b32 v236, a206\n" \
"v_mfma_f32_16x16x32_bf16 a[216:219], v[116:119], v[180:183], a[216:219]\n" \
"global_load_lds_dwordx4 v229, s[44:45] offset:-3072\n" \
"v_accvgpr_read_b32 v237, a207\n" \
"v_mfma_f32_16x16x32_bf16 a[220:223], v[112:115], v[184:187], a[220:223]\n" \
"s_add_u32 s44, s44, 0x80000\n" \
"s_addc_u32 s45, s45, 0\n" \
"v_mul_f32 v234, s92, v234\n" \
"v_mfma_f32_16x16x32_bf16 a[220:223], v[116:119], v[188:191], a[220:223]\n" \
"v_mul_f32 v235, s92, v235\n" \
"v_mul_f32 v236, s92, v236\n" \
"v_mfma_f32_16x16x32_bf16 a[252:255], v[120:123], v[184:187], a[252:255]\n" \
"v_mul_f32 v237, s92, v237\n" \
"v_cvt_pk_bf16_f32 v234, v234, v235\n" \
"v_mfma_f32_16x16x32_bf16 a[252:255], v[124:127], v[188:191], a[252:255]\n" \
"global_load_lds_dwordx4 v228, s[42:43] offset:-2048\n" \
"v_cvt_pk_bf16_f32 v236, v236, v237\n" \
"v_mfma_f32_16x16x32_bf16 a[248:251], v[120:123], v[176:179], a[248:251]\n" \
"v_accvgpr_write_b32 a202, v234\n" \
"v_accvgpr_write_b32 a203, v236\n" \
"v_mfma_f32_16x16x32_bf16 a[248:251], v[124:127], v[180:183], a[248:251]\n" \
"v_accvgpr_read_b32 v234, a232\n" \
"v_accvgpr_read_b32 v235, a233\n" \
"v_mfma_f32_16x16x32_bf16 a[244:247], v[120:123], v[168:171], a[244:247]\n" \
"v_accvgpr_read_b32 v236, a234\n" \
"v_accvgpr_read_b32 v237, a235\n" \
"v_mfma_f32_16x16x32_bf16 a[244:247], v[124:127], v[172:175], a[244:247]\n" \
"global_load_lds_dwordx4 v229, s[42:43] offset:-1024\n" \
"v_mul_f32 v234, s92, v234\n" \
"v_mfma_f32_16x16x32_bf16 a[240:243], v[120:123], v[160:163], a[240:243]\n" \
"s_add_u32 s42, s42, 0x80000\n" \
"s_addc_u32 s43, s43, 0\n" \
"v_mul_f32 v235, s92, v235\n" \
"v_mfma_f32_16x16x32_bf16 a[240:243], v[124:127], v[164:167], a[240:243]\n" \
"s_waitcnt lgkmcnt(0)\n" \
"v_mul_f32 v236, s92, v236\n" \
"v_mul_f32 v237, s92, v237\n" \
"v_mfma_f32_16x16x32_bf16 v[64:67], v[0:3], v[192:195], 0\n" \
"ds_read_b128 v[216:219], v230 offset:6144\n" \
"v_cvt_pk_bf16_f32 v234, v234, v235\n" \
"v_mfma_f32_16x16x32_bf16 v[64:67], v[4:7], v[196:199], v[64:67]\n" \
"ds_read_b128 v[220:223], v231 offset:6144\n" \
"v_cvt_pk_bf16_f32 v236, v236, v237\n" \
"v_mfma_f32_16x16x32_bf16 v[68:71], v[8:11], v[192:195], 0\n" \
"s_add_u32 s90, s88, 0\n" \
"ds_read_b64_tr_b16 v[32:33], v232 offset:1024\n" \
"v_accvgpr_write_b32 a232, v234\n" \
"v_mfma_f32_16x16x32_bf16 v[68:71], v[12:15], v[196:199], v[68:71]\n" \
"buffer_store_dwordx4 a[192:195], v249, s[56:59], s90 offen offset:0 sc1\n" \
"v_accvgpr_write_b32 a233, v236\n" \
"v_accvgpr_read_b32 v234, a236\n" \
"v_accvgpr_read_b32 v235, a237\n" \
"v_accvgpr_read_b32 v236, a238\n" \
"v_mfma_f32_16x16x32_bf16 v[72:75], v[16:19], v[192:195], 0\n" \
"ds_read_b64_tr_b16 v[34:35], v232 offset:3072\n" \
"v_accvgpr_read_b32 v237, a239\n" \
"v_mfma_f32_16x16x32_bf16 v[72:75], v[20:23], v[196:199], v[72:75]\n" \
"ds_read_b64_tr_b16 v[36:37], v232 offset:9216\n" \
"v_mul_f32 v234, s92, v234\n" \
"v_mfma_f32_16x16x32_bf16 v[76:79], v[24:27], v[192:195], 0\n" \
"ds_read_b64_tr_b16 v[38:39], v232 offset:11264\n" \
"v_mul_f32 v235, s92, v235\n" \
"v_mfma_f32_16x16x32_bf16 v[76:79], v[28:31], v[196:199], v[76:79]\n" \
"buffer_store_dwordx4 a[224:227], v249, s[56:59], s90 offen offset:256 sc1\n" \
"v_mul_f32 v236, s92, v236\n" \
"v_mul_f32 v237, s92, v237\n" \
"v_cvt_pk_bf16_f32 v234, v234, v235\n" \
"v_cvt_pk_bf16_f32 v236, v236, v237\n" \
"v_mfma_f32_16x16x32_bf16 v[108:111], v[24:27], v[200:203], 0\n" \
"ds_read_b64_tr_b16 v[40:41], v233 offset:1024\n" \
"v_accvgpr_write_b32 a234, v234\n" \
"v_mfma_f32_16x16x32_bf16 v[108:111], v[28:31], v[204:207], v[108:111]\n" \
"ds_read_b64_tr_b16 v[42:43], v233 offset:3072\n" \
"v_accvgpr_write_b32 a235, v236\n" \
"v_mfma_f32_16x16x32_bf16 v[104:107], v[16:19], v[200:203], 0\n" \
"ds_read_b64_tr_b16 v[44:45], v233 offset:9216\n" \
"v_accvgpr_read_b32 v234, a208\n" \
"v_mfma_f32_16x16x32_bf16 v[104:107], v[20:23], v[204:207], v[104:107]\n" \
"v_accvgpr_read_b32 v235, a209\n" \
"v_accvgpr_read_b32 v236, a210\n" \
"v_mfma_f32_16x16x32_bf16 v[100:103], v[8:11], v[200:203], 0\n" \
"ds_read_b64_tr_b16 v[46:47], v233 offset:11264\n" \
"v_accvgpr_read_b32 v237, a211\n" \
"v_mfma_f32_16x16x32_bf16 v[100:103], v[12:15], v[204:207], v[100:103]\n" \
"ds_read_b64_tr_b16 v[48:49], v232 offset:1536\n" \
"v_cvt_pk_bf16_f32 v234, v234, v235\n" \
"v_mfma_f32_16x16x32_bf16 v[96:99], v[0:3], v[200:203], 0\n" \
"ds_read_b64_tr_b16 v[50:51], v232 offset:3584\n" \
"v_cvt_pk_bf16_f32 v236, v236, v237\n" \
"v_mfma_f32_16x16x32_bf16 v[96:99], v[4:7], v[204:207], v[96:99]\n" \
"v_accvgpr_write_b32 a208, v234\n" \
"v_accvgpr_write_b32 a209, v236\n" \
"v_mfma_f32_16x16x32_bf16 v[128:131], v[0:3], v[208:211], 0\n" \
"ds_read_b64_tr_b16 v[52:53], v232 offset:9728\n" \
"v_accvgpr_read_b32 v234, a240\n" \
"v_mfma_f32_16x16x32_bf16 v[128:131], v[4:7], v[212:215], v[128:131]\n" \
"ds_read_b64_tr_b16 v[54:55], v232 offset:11776\n" \
"v_accvgpr_read_b32 v235, a241\n" \
"v_mfma_f32_16x16x32_bf16 v[132:135], v[8:11], v[208:211], 0\n" \
"ds_read_b64_tr_b16 v[56:57], v233 offset:1536\n" \
"v_accvgpr_read_b32 v236, a242\n" \
"v_mfma_f32_16x16x32_bf16 v[132:135], v[12:15], v[212:215], v[132:135]\n" \
"v_accvgpr_read_b32 v237, a243\n" \
"v_cvt_pk_bf16_f32 v234, v234, v235\n" \
"Lpg2226_%=:\n" \
"Lst0x3_%=:\n" \
"s_cbranch_execz Lpg4398_%=\n" \
"v_mfma_f32_16x16x32_bf16 v[136:139], v[16:19], v[208:211], 0\n" \
"ds_read_b64_tr_b16 v[58:59], v233 offset:3584\n" \
"v_cvt_pk_bf16_f32 v236, v236, v237\n" \
"v_mfma_f32_16x16x32_bf16 v[136:139], v[20:23], v[212:215], v[136:139]\n" \
"ds_read_b64_tr_b16 v[60:61], v233 offset:9728\n" \
"v_accvgpr_write_b32 a210, v234\n" \
"v_mfma_f32_16x16x32_bf16 v[140:143], v[24:27], v[208:211], 0\n" \
"ds_read_b64_tr_b16 v[62:63], v233 offset:11776\n" \
"v_accvgpr_write_b32 a211, v236\n" \
"v_mfma_f32_16x16x32_bf16 v[140:143], v[28:31], v[212:215], v[140:143]\n" \
"s_waitcnt lgkmcnt(3)\n" \
"v_accvgpr_read_b32 v234, a212\n" \
"v_accvgpr_read_b32 v235, a213\n" \
"v_mfma_f32_16x16x32_bf16 v[172:175], v[24:27], v[216:219], 0\n" \
"v_add_u32 v230, 0xffff0000, v230\n" \
"v_add_u32 v231, 0xffff0000, v231\n" \
"v_mfma_f32_16x16x32_bf16 v[172:175], v[28:31], v[220:223], v[172:175]\n" \
"v_add_u32 v232, 0x4000, v232\n" \
"v_add_u32 v233, 0x4000, v233\n" \
"v_mfma_f32_16x16x32_bf16 v[168:171], v[16:19], v[216:219], 0\n" \
"v_accvgpr_read_b32 v236, a214\n" \
"v_accvgpr_read_b32 v237, a215\n" \
"v_mfma_f32_16x16x32_bf16 v[168:171], v[20:23], v[220:223], v[168:171]\n" \
"v_cvt_pk_bf16_f32 v234, v234, v235\n" \
"v_cvt_pk_bf16_f32 v236, v236, v237\n" \
"v_mfma_f32_16x16x32_bf16 v[164:167], v[8:11], v[216:219], 0\n" \
"v_accvgpr_write_b32 a212, v234\n" \
"v_accvgpr_write_b32 a213, v236\n" \
"v_mfma_f32_16x16x32_bf16 v[164:167], v[12:15], v[220:223], v[164:167]\n" \
"s_waitcnt vmcnt(18) lgkmcnt(0)\n" \
"v_accvgpr_read_b32 v234, a244\n" \
"v_accvgpr_read_b32 v235, a245\n" \
"v_mfma_f32_16x16x32_bf16 v[160:163], v[0:3], v[216:219], 0\n" \
"v_accvgpr_read_b32 v236, a246\n" \
"v_accvgpr_read_b32 v237, a247\n" \
"v_mfma_f32_16x16x32_bf16 v[160:163], v[4:7], v[220:223], v[160:163]\n" \
"s_barrier\n" \
"v_mfma_f32_16x16x32_bf16 v[80:83], v[32:35], v[192:195], 0\n" \
"ds_read_b64_tr_b16 v[0:1], v232 offset:0\n" \
"v_cvt_pk_bf16_f32 v234, v234, v235\n" \
"v_mfma_f32_16x16x32_bf16 v[80:83], v[36:39], v[196:199], v[80:83]\n" \
"ds_read_b64_tr_b16 v[2:3], v232 offset:2048\n" \
"v_cvt_pk_bf16_f32 v236, v236, v237\n" \
"v_mfma_f32_16x16x32_bf16 v[84:87], v[40:43], v[192:195], 0\n" \
"s_add_u32 m0, s87, 135168\n" \
"ds_read_b64_tr_b16 v[4:5], v232 offset:8192\n" \
"v_accvgpr_write_b32 a214, v234\n" \
"v_mfma_f32_16x16x32_bf16 v[84:87], v[44:47], v[196:199], v[84:87]\n" \
"global_load_lds_dwordx4 v224, s[48:49] offset:-4096\n" \
"v_accvgpr_write_b32 a215, v236\n" \
"v_mfma_f32_16x16x32_bf16 v[88:91], v[48:51], v[192:195], 0\n" \
"ds_read_b64_tr_b16 v[6:7], v232 offset:10240\n" \
"v_accvgpr_read_b32 v234, a216\n" \
"v_mfma_f32_16x16x32_bf16 v[88:91], v[52:55], v[196:199], v[88:91]\n" \
"ds_read_b64_tr_b16 v[8:9], v233 offset:0\n" \
"v_accvgpr_read_b32 v235, a217\n" \
"v_mfma_f32_16x16x32_bf16 v[92:95], v[56:59], v[192:195], 0\n" \
"ds_read_b64_tr_b16 v[10:11], v233 offset:2048\n" \
"v_accvgpr_read_b32 v236, a218\n" \
"v_mfma_f32_16x16x32_bf16 v[92:95], v[60:63], v[196:199], v[92:95]\n" \
"global_load_lds_dwordx4 v225, s[48:49] offset:-3072\n" \
"v_accvgpr_read_b32 v237, a219\n" \
"v_mfma_f32_16x16x32_bf16 v[124:127], v[56:59], v[200:203], 0\n" \
"ds_read_b128 v[192:195], v230 offset:0\n" \
"v_cvt_pk_bf16_f32 v234, v234, v235\n" \
"v_mfma_f32_16x16x32_bf16 v[124:127], v[60:63], v[204:207], v[124:127]\n" \
"ds_read_b128 v[196:199], v231 offset:0\n" \
"v_cvt_pk_bf16_f32 v236, v236, v237\n" \
"v_mfma_f32_16x16x32_bf16 v[120:123], v[48:51], v[200:203], 0\n" \
"ds_read_b64_tr_b16 v[12:13], v233 offset:8192\n" \
"v_accvgpr_write_b32 a216, v234\n" \
"v_mfma_f32_16x16x32_bf16 v[120:123], v[52:55], v[204:207], v[120:123]\n" \
"global_load_lds_dwordx4 v226, s[48:49] offset:-2048\n" \
"v_accvgpr_write_b32 a217, v236\n" \
"v_mfma_f32_16x16x32_bf16 v[116:119], v[40:43], v[200:203], 0\n" \
"ds_read_b64_tr_b16 v[14:15], v233 offset:10240\n" \
"v_accvgpr_read_b32 v234, a248\n" \
"v_mfma_f32_16x16x32_bf16 v[116:119], v[44:47], v[204:207], v[116:119]\n" \
"ds_read_b64_tr_b16 v[16:17], v232 offset:512\n" \
"v_accvgpr_read_b32 v235, a249\n" \
"v_mfma_f32_16x16x32_bf16 v[112:115], v[32:35], v[200:203], 0\n" \
"ds_read_b64_tr_b16 v[18:19], v232 offset:2560\n" \
"v_accvgpr_read_b32 v236, a250\n" \
"v_mfma_f32_16x16x32_bf16 v[112:115], v[36:39], v[204:207], v[112:115]\n" \
"global_load_lds_dwordx4 v227, s[48:49] offset:-1024\n" \
"v_accvgpr_read_b32 v237, a251\n" \
"v_mfma_f32_16x16x32_bf16 v[144:147], v[32:35], v[208:211], 0\n" \
"s_add_u32 s48, s48, 0x80\n" \
"s_addc_u32 s49, s49, 0\n" \
"ds_read_b128 v[200:203], v230 offset:2048\n" \
"v_mfma_f32_16x16x32_bf16 v[144:147], v[36:39], v[212:215], v[144:147]\n" \
"ds_read_b128 v[204:207], v231 offset:2048\n" \
"v_cvt_pk_bf16_f32 v234, v234, v235\n" \
"v_mfma_f32_16x16x32_bf16 v[148:151], v[40:43], v[208:211], 0\n" \
"ds_read_b64_tr_b16 v[20:21], v232 offset:8704\n" \
"v_cvt_pk_bf16_f32 v236, v236, v237\n" \
"v_mfma_f32_16x16x32_bf16 v[148:151], v[44:47], v[212:215], v[148:151]\n" \
"global_load_lds_dwordx4 v224, s[46:47] offset:0\n" \
"v_accvgpr_write_b32 a218, v234\n" \
"v_mfma_f32_16x16x32_bf16 v[152:155], v[48:51], v[208:211], 0\n" \
"ds_read_b64_tr_b16 v[22:23], v232 offset:10752\n" \
"v_accvgpr_write_b32 a219, v236\n" \
"v_mfma_f32_16x16x32_bf16 v[152:155], v[52:55], v[212:215], v[152:155]\n" \
"ds_read_b64_tr_b16 v[24:25], v233 offset:512\n" \
"v_accvgpr_read_b32 v234, a220\n" \
"v_mfma_f32_16x16x32_bf16 v[156:159], v[56:59], v[208:211], 0\n" \
"ds_read_b64_tr_b16 v[26:27], v233 offset:2560\n" \
"v_accvgpr_read_b32 v235, a221\n" \
"v_mfma_f32_16x16x32_bf16 v[156:159], v[60:63], v[212:215], v[156:159]\n" \
"global_load_lds_dwordx4 v225, s[46:47] offset:1024\n" \
"v_accvgpr_read_b32 v236, a222\n" \
"v_mfma_f32_16x16x32_bf16 v[188:191], v[56:59], v[216:219], 0\n" \
"ds_read_b128 v[208:211], v230 offset:4096\n" \
"v_accvgpr_read_b32 v237, a223\n" \
"v_mfma_f32_16x16x32_bf16 v[188:191], v[60:63], v[220:223], v[188:191]\n" \
"ds_read_b128 v[212:215], v231 offset:4096\n" \
"v_cvt_pk_bf16_f32 v234, v234, v235\n" \
"v_mfma_f32_16x16x32_bf16 v[184:187], v[48:51], v[216:219], 0\n" \
"ds_read_b64_tr_b16 v[28:29], v233 offset:8704\n" \
"v_cvt_pk_bf16_f32 v236, v236, v237\n" \
"v_mfma_f32_16x16x32_bf16 v[184:187], v[52:55], v[220:223], v[184:187]\n" \
"global_load_lds_dwordx4 v226, s[46:47] offset:2048\n" \
"v_accvgpr_write_b32 a220, v234\n" \
"v_mfma_f32_16x16x32_bf16 v[180:183], v[40:43], v[216:219], 0\n" \
"ds_read_b64_tr_b16 v[30:31], v233 offset:10752\n" \
"v_accvgpr_write_b32 a221, v236\n" \
"v_mfma_f32_16x16x32_bf16 v[180:183], v[44:47], v[220:223], v[180:183]\n" \
"v_accvgpr_read_b32 v234, a252\n" \
"v_accvgpr_read_b32 v235, a253\n" \
"v_mfma_f32_16x16x32_bf16 v[176:179], v[32:35], v[216:219], 0\n" \
"v_accvgpr_read_b32 v236, a254\n" \
"v_accvgpr_read_b32 v237, a255\n" \
"v_mfma_f32_16x16x32_bf16 v[176:179], v[36:39], v[220:223], v[176:179]\n" \
"s_waitcnt lgkmcnt(1)\n" \
"global_load_lds_dwordx4 v227, s[46:47] offset:3072\n" \
"v_cvt_pk_bf16_f32 v234, v234, v235\n" \
"v_mfma_f32_16x16x32_bf16 v[64:67], v[0:3], v[192:195], v[64:67]\n" \
"s_add_u32 s46, s46, 0x80\n" \
"s_addc_u32 s47, s47, 0\n" \
"ds_read_b128 v[216:219], v230 offset:6144\n" \
"v_mfma_f32_16x16x32_bf16 v[64:67], v[4:7], v[196:199], v[64:67]\n" \
"ds_read_b128 v[220:223], v231 offset:6144\n" \
"v_cvt_pk_bf16_f32 v236, v236, v237\n" \
"v_mfma_f32_16x16x32_bf16 v[68:71], v[8:11], v[192:195], v[68:71]\n" \
"ds_read_b64_tr_b16 v[32:33], v232 offset:1024\n" \
"v_accvgpr_write_b32 a222, v234\n" \
"v_mfma_f32_16x16x32_bf16 v[68:71], v[12:15], v[196:199], v[68:71]\n" \
"buffer_store_dwordx4 a[200:203], v249, s[56:59], s90 offen offset:2048 sc1\n" \
"v_accvgpr_write_b32 a223, v236\n" \
"v_accvgpr_read_b32 v234, a128\n" \
"v_accvgpr_read_b32 v235, a129\n" \
"v_accvgpr_read_b32 v236, a130\n" \
"v_mfma_f32_16x16x32_bf16 v[72:75], v[16:19], v[192:195], v[72:75]\n" \
"ds_read_b64_tr_b16 v[34:35], v232 offset:3072\n" \
"v_accvgpr_read_b32 v237, a131\n" \
"v_mfma_f32_16x16x32_bf16 v[72:75], v[20:23], v[196:199], v[72:75]\n" \
"s_waitcnt lgkmcnt(3)\n" \
"ds_read_b64_tr_b16 v[36:37], v232 offset:9216\n" \
"v_mul_f32 v234, s92, v234\n" \
"v_mfma_f32_16x16x32_bf16 v[76:79], v[24:27], v[192:195], v[76:79]\n" \
"ds_read_b64_tr_b16 v[38:39], v232 offset:11264\n" \
"v_mul_f32 v235, s92, v235\n" \
"v_mfma_f32_16x16x32_bf16 v[76:79], v[28:31], v[196:199], v[76:79]\n" \
"v_mul_f32 v236, s92, v236\n" \
"v_mul_f32 v237, s92, v237\n" \
"v_mfma_f32_16x16x32_bf16 v[108:111], v[24:27], v[200:203], v[108:111]\n" \
"ds_read_b64_tr_b16 v[40:41], v233 offset:1024\n" \
"v_cvt_pk_bf16_f32 v234, v234, v235\n" \
"v_mfma_f32_16x16x32_bf16 v[108:111], v[28:31], v[204:207], v[108:111]\n" \
"ds_read_b64_tr_b16 v[42:43], v233 offset:3072\n" \
"v_cvt_pk_bf16_f32 v236, v236, v237\n" \
"v_mfma_f32_16x16x32_bf16 v[104:107], v[16:19], v[200:203], v[104:107]\n" \
"ds_read_b64_tr_b16 v[44:45], v233 offset:9216\n" \
"v_accvgpr_write_b32 a128, v234\n" \
"v_mfma_f32_16x16x32_bf16 v[104:107], v[20:23], v[204:207], v[104:107]\n" \
"v_accvgpr_write_b32 a129, v236\n" \
"v_accvgpr_read_b32 v234, a132\n" \
"v_mfma_f32_16x16x32_bf16 v[100:103], v[8:11], v[200:203], v[100:103]\n" \
"ds_read_b64_tr_b16 v[46:47], v233 offset:11264\n" \
"v_accvgpr_read_b32 v235, a133\n" \
"v_mfma_f32_16x16x32_bf16 v[100:103], v[12:15], v[204:207], v[100:103]\n" \
"ds_read_b64_tr_b16 v[48:49], v232 offset:1536\n" \
"v_accvgpr_read_b32 v236, a134\n" \
"v_mfma_f32_16x16x32_bf16 v[96:99], v[0:3], v[200:203], v[96:99]\n" \
"s_add_u32 m0, s95, 4096\n" \
"ds_read_b64_tr_b16 v[50:51], v232 offset:3584\n" \
"v_accvgpr_read_b32 v237, a135\n" \
"v_mfma_f32_16x16x32_bf16 v[96:99], v[4:7], v[204:207], v[96:99]\n" \
"global_load_lds_dwordx4 v228, s[44:45] offset:-4096\n" \
"v_mul_f32 v234, s92, v234\n" \
"v_mfma_f32_16x16x32_bf16 v[128:131], v[0:3], v[208:211], v[128:131]\n" \
"ds_read_b64_tr_b16 v[52:53], v232 offset:9728\n" \
"v_mul_f32 v235, s92, v235\n" \
"v_mfma_f32_16x16x32_bf16 v[128:131], v[4:7], v[212:215], v[128:131]\n" \
"ds_read_b64_tr_b16 v[54:55], v232 offset:11776\n" \
"v_mul_f32 v236, s92, v236\n" \
"v_mfma_f32_16x16x32_bf16 v[132:135], v[8:11], v[208:211], v[132:135]\n" \
"ds_read_b64_tr_b16 v[56:57], v233 offset:1536\n" \
"v_mul_f32 v237, s92, v237\n" \
"v_mfma_f32_16x16x32_bf16 v[132:135], v[12:15], v[212:215], v[132:135]\n" \
"global_load_lds_dwordx4 v229, s[44:45] offset:-3072\n" \
"v_cvt_pk_bf16_f32 v234, v234, v235\n" \
"v_mfma_f32_16x16x32_bf16 v[136:139], v[16:19], v[208:211], v[136:139]\n" \
"s_add_u32 s44, s44, 0x100000\n" \
"s_addc_u32 s45, s45, 0\n" \
"ds_read_b64_tr_b16 v[58:59], v233 offset:3584\n" \
"v_mfma_f32_16x16x32_bf16 v[136:139], v[20:23], v[212:215], v[136:139]\n" \
"ds_read_b64_tr_b16 v[60:61], v233 offset:9728\n" \
"v_cvt_pk_bf16_f32 v236, v236, v237\n" \
"v_mfma_f32_16x16x32_bf16 v[140:143], v[24:27], v[208:211], v[140:143]\n" \
"ds_read_b64_tr_b16 v[62:63], v233 offset:11776\n" \
"v_accvgpr_write_b32 a130, v234\n" \
"v_mfma_f32_16x16x32_bf16 v[140:143], v[28:31], v[212:215], v[140:143]\n" \
"s_waitcnt lgkmcnt(3)\n" \
"global_load_lds_dwordx4 v228, s[42:43] offset:-2048\n" \
"v_accvgpr_write_b32 a131, v236\n" \
"v_mfma_f32_16x16x32_bf16 v[172:175], v[24:27], v[216:219], v[172:175]\n" \
"v_add_u32 v230, 0x8000, v230\n" \
"v_add_u32 v231, 0x8000, v231\n" \
"v_mfma_f32_16x16x32_bf16 v[172:175], v[28:31], v[220:223], v[172:175]\n" \
"v_add_u32 v232, 0x4000, v232\n" \
"v_add_u32 v233, 0x4000, v233\n" \
"v_mfma_f32_16x16x32_bf16 v[168:171], v[16:19], v[216:219], v[168:171]\n" \
"v_accvgpr_read_b32 v234, a160\n" \
"v_accvgpr_read_b32 v235, a161\n" \
"v_mfma_f32_16x16x32_bf16 v[168:171], v[20:23], v[220:223], v[168:171]\n" \
"global_load_lds_dwordx4 v229, s[42:43] offset:-1024\n" \
"v_accvgpr_read_b32 v236, a162\n" \
"v_mfma_f32_16x16x32_bf16 v[164:167], v[8:11], v[216:219], v[164:167]\n" \
"s_add_u32 s42, s42, 0x100000\n" \
"s_addc_u32 s43, s43, 0\n" \
"v_accvgpr_read_b32 v237, a163\n" \
"v_mfma_f32_16x16x32_bf16 v[164:167], v[12:15], v[220:223], v[164:167]\n" \
"s_waitcnt vmcnt(19) lgkmcnt(0)\n" \
"v_mul_f32 v234, s92, v234\n" \
"v_mul_f32 v235, s92, v235\n" \
"v_mfma_f32_16x16x32_bf16 v[160:163], v[0:3], v[216:219], v[160:163]\n" \
"v_mul_f32 v236, s92, v236\n" \
"v_mul_f32 v237, s92, v237\n" \
"v_mfma_f32_16x16x32_bf16 v[160:163], v[4:7], v[220:223], v[160:163]\n" \
"s_barrier\n" \
"v_mfma_f32_16x16x32_bf16 v[80:83], v[32:35], v[192:195], v[80:83]\n" \
"ds_read_b64_tr_b16 v[0:1], v232 offset:0\n" \
"v_cvt_pk_bf16_f32 v234, v234, v235\n" \
"v_mfma_f32_16x16x32_bf16 v[80:83], v[36:39], v[196:199], v[80:83]\n" \
"ds_read_b64_tr_b16 v[2:3], v232 offset:2048\n" \
"v_cvt_pk_bf16_f32 v236, v236, v237\n" \
"v_mfma_f32_16x16x32_bf16 v[84:87], v[40:43], v[192:195], v[84:87]\n" \
"s_add_u32 m0, s87, 69632\n" \
"ds_read_b64_tr_b16 v[4:5], v232 offset:8192\n" \
"v_accvgpr_write_b32 a160, v234\n" \
"v_mfma_f32_16x16x32_bf16 v[84:87], v[44:47], v[196:199], v[84:87]\n" \
"global_load_lds_dwordx4 v224, s[48:49] offset:-4096\n" \
"v_accvgpr_write_b32 a161, v236\n" \
"v_mfma_f32_16x16x32_bf16 v[88:91], v[48:51], v[192:195], v[88:91]\n" \
"ds_read_b64_tr_b16 v[6:7], v232 offset:10240\n" \
"v_accvgpr_read_b32 v234, a164\n" \
"v_mfma_f32_16x16x32_bf16 v[88:91], v[52:55], v[196:199], v[88:91]\n" \
"ds_read_b64_tr_b16 v[8:9], v233 offset:0\n" \
"v_accvgpr_read_b32 v235, a165\n" \
"v_mfma_f32_16x16x32_bf16 v[92:95], v[56:59], v[192:195], v[92:95]\n" \
"ds_read_b64_tr_b16 v[10:11], v233 offset:2048\n" \
"v_accvgpr_read_b32 v236, a166\n" \
"v_mfma_f32_16x16x32_bf16 v[92:95], v[60:63], v[196:199], v[92:95]\n" \
"global_load_lds_dwordx4 v225, s[48:49] offset:-3072\n" \
"v_accvgpr_read_b32 v237, a167\n" \
"v_mfma_f32_16x16x32_bf16 v[124:127], v[56:59], v[200:203], v[124:127]\n" \
"ds_read_b128 v[192:195], v230 offset:0\n" \
"v_mul_f32 v234, s92, v234\n" \
"v_mfma_f32_16x16x32_bf16 v[124:127], v[60:63], v[204:207], v[124:127]\n" \
"ds_read_b128 v[196:199], v231 offset:0\n" \
"v_mul_f32 v235, s92, v235\n" \
"v_mfma_f32_16x16x32_bf16 v[120:123], v[48:51], v[200:203], v[120:123]\n" \
"ds_read_b64_tr_b16 v[12:13], v233 offset:8192\n" \
"v_mul_f32 v236, s92, v236\n" \
"v_mfma_f32_16x16x32_bf16 v[120:123], v[52:55], v[204:207], v[120:123]\n" \
"global_load_lds_dwordx4 v226, s[48:49] offset:-2048\n" \
"v_mul_f32 v237, s92, v237\n" \
"v_mfma_f32_16x16x32_bf16 v[116:119], v[40:43], v[200:203], v[116:119]\n" \
"ds_read_b64_tr_b16 v[14:15], v233 offset:10240\n" \
"v_cvt_pk_bf16_f32 v234, v234, v235\n" \
"v_mfma_f32_16x16x32_bf16 v[116:119], v[44:47], v[204:207], v[116:119]\n" \
"ds_read_b64_tr_b16 v[16:17], v232 offset:512\n" \
"v_cvt_pk_bf16_f32 v236, v236, v237\n" \
"v_mfma_f32_16x16x32_bf16 v[112:115], v[32:35], v[200:203], v[112:115]\n" \
"ds_read_b64_tr_b16 v[18:19], v232 offset:2560\n" \
"v_accvgpr_write_b32 a162, v234\n" \
"v_mfma_f32_16x16x32_bf16 v[112:115], v[36:39], v[204:207], v[112:115]\n" \
"global_load_lds_dwordx4 v227, s[48:49] offset:-1024\n" \
"v_accvgpr_write_b32 a163, v236\n" \
"v_mfma_f32_16x16x32_bf16 v[144:147], v[32:35], v[208:211], v[144:147]\n" \
"s_add_u32 s48, s48, 0x100\n" \
"s_addc_u32 s49, s49, 0\n" \
"ds_read_b128 v[200:203], v230 offset:2048\n" \
"v_mfma_f32_16x16x32_bf16 v[144:147], v[36:39], v[212:215], v[144:147]\n" \
"ds_read_b128 v[204:207], v231 offset:2048\n" \
"v_accvgpr_read_b32 v234, a136\n" \
"v_mfma_f32_16x16x32_bf16 v[148:151], v[40:43], v[208:211], v[148:151]\n" \
"ds_read_b64_tr_b16 v[20:21], v232 offset:8704\n" \
"v_accvgpr_read_b32 v235, a137\n" \
"v_mfma_f32_16x16x32_bf16 v[148:151], v[44:47], v[212:215], v[148:151]\n" \
"global_load_lds_dwordx4 v224, s[46:47] offset:0\n" \
"v_accvgpr_read_b32 v236, a138\n" \
"v_mfma_f32_16x16x32_bf16 v[152:155], v[48:51], v[208:211], v[152:155]\n" \
"ds_read_b64_tr_b16 v[22:23], v232 offset:10752\n" \
"v_accvgpr_read_b32 v237, a139\n" \
"v_mfma_f32_16x16x32_bf16 v[152:155], v[52:55], v[212:215], v[152:155]\n" \
"ds_read_b64_tr_b16 v[24:25], v233 offset:512\n" \
"v_mul_f32 v234, s92, v234\n" \
"v_mfma_f32_16x16x32_bf16 v[156:159], v[56:59], v[208:211], v[156:159]\n" \
"ds_read_b64_tr_b16 v[26:27], v233 offset:2560\n" \
"v_mul_f32 v235, s92, v235\n" \
"v_mfma_f32_16x16x32_bf16 v[156:159], v[60:63], v[212:215], v[156:159]\n" \
"global_load_lds_dwordx4 v225, s[46:47] offset:1024\n" \
"v_mul_f32 v236, s92, v236\n" \
"v_mfma_f32_16x16x32_bf16 v[188:191], v[56:59], v[216:219], v[188:191]\n" \
"ds_read_b128 v[208:211], v230 offset:4096\n" \
"v_mul_f32 v237, s92, v237\n" \
"v_mfma_f32_16x16x32_bf16 v[188:191], v[60:63], v[220:223], v[188:191]\n" \
"ds_read_b128 v[212:215], v231 offset:4096\n" \
"v_cvt_pk_bf16_f32 v234, v234, v235\n" \
"v_mfma_f32_16x16x32_bf16 v[184:187], v[48:51], v[216:219], v[184:187]\n" \
"ds_read_b64_tr_b16 v[28:29], v233 offset:8704\n" \
"v_cvt_pk_bf16_f32 v236, v236, v237\n" \
"v_mfma_f32_16x16x32_bf16 v[184:187], v[52:55], v[220:223], v[184:187]\n" \
"global_load_lds_dwordx4 v226, s[46:47] offset:2048\n" \
"v_accvgpr_write_b32 a136, v234\n" \
"v_mfma_f32_16x16x32_bf16 v[180:183], v[40:43], v[216:219], v[180:183]\n" \
"ds_read_b64_tr_b16 v[30:31], v233 offset:10752\n" \
"v_accvgpr_write_b32 a137, v236\n" \
"v_mfma_f32_16x16x32_bf16 v[180:183], v[44:47], v[220:223], v[180:183]\n" \
"v_accvgpr_read_b32 v234, a140\n" \
"v_accvgpr_read_b32 v235, a141\n" \
"v_mfma_f32_16x16x32_bf16 v[176:179], v[32:35], v[216:219], v[176:179]\n" \
"v_accvgpr_read_b32 v236, a142\n" \
"v_accvgpr_read_b32 v237, a143\n" \
"v_mfma_f32_16x16x32_bf16 v[176:179], v[36:39], v[220:223], v[176:179]\n" \
"s_waitcnt lgkmcnt(1)\n" \
"global_load_lds_dwordx4 v227, s[46:47] offset:3072\n" \
"v_mul_f32 v234, s92, v234\n" \
"v_mfma_f32_16x16x32_bf16 v[64:67], v[0:3], v[192:195], v[64:67]\n" \
"s_add_u32 s46, s46, 0x100\n" \
"s_addc_u32 s47, s47, 0\n" \
"ds_read_b128 v[216:219], v230 offset:6144\n" \
"v_mfma_f32_16x16x32_bf16 v[64:67], v[4:7], v[196:199], v[64:67]\n" \
"ds_read_b128 v[220:223], v231 offset:6144\n" \
"v_mul_f32 v235, s92, v235\n" \
"v_mfma_f32_16x16x32_bf16 v[68:71], v[8:11], v[192:195], v[68:71]\n" \
"ds_read_b64_tr_b16 v[32:33], v232 offset:1024\n" \
"v_mul_f32 v236, s92, v236\n" \
"v_mfma_f32_16x16x32_bf16 v[68:71], v[12:15], v[196:199], v[68:71]\n" \
"buffer_store_dwordx4 a[232:235], v249, s[56:59], s90 offen offset:2304 sc1\n" \
"v_mul_f32 v237, s92, v237\n" \
"v_cvt_pk_bf16_f32 v234, v234, v235\n" \
"v_cvt_pk_bf16_f32 v236, v236, v237\n" \
"v_accvgpr_write_b32 a138, v234\n" \
"v_mfma_f32_16x16x32_bf16 v[72:75], v[16:19], v[192:195], v[72:75]\n" \
"ds_read_b64_tr_b16 v[34:35], v232 offset:3072\n" \
"v_accvgpr_write_b32 a139, v236\n" \
"v_mfma_f32_16x16x32_bf16 v[72:75], v[20:23], v[196:199], v[72:75]\n" \
"s_waitcnt lgkmcnt(3)\n" \
"ds_read_b64_tr_b16 v[36:37], v232 offset:9216\n" \
"v_accvgpr_read_b32 v234, a168\n" \
"v_mfma_f32_16x16x32_bf16 v[76:79], v[24:27], v[192:195], v[76:79]\n" \
"ds_read_b64_tr_b16 v[38:39], v232 offset:11264\n" \
"v_accvgpr_read_b32 v235, a169\n" \
"v_mfma_f32_16x16x32_bf16 v[76:79], v[28:31], v[196:199], v[76:79]\n" \
"v_accvgpr_read_b32 v236, a170\n" \
"v_accvgpr_read_b32 v237, a171\n" \
"v_mfma_f32_16x16x32_bf16 v[108:111], v[24:27], v[200:203], v[108:111]\n" \
"ds_read_b64_tr_b16 v[40:41], v233 offset:1024\n" \
"v_mul_f32 v234, s92, v234\n" \
"v_mfma_f32_16x16x32_bf16 v[108:111], v[28:31], v[204:207], v[108:111]\n" \
"ds_read_b64_tr_b16 v[42:43], v233 offset:3072\n" \
"v_mul_f32 v235, s92, v235\n" \
"v_mfma_f32_16x16x32_bf16 v[104:107], v[16:19], v[200:203], v[104:107]\n" \
"ds_read_b64_tr_b16 v[44:45], v233 offset:9216\n" \
"v_mul_f32 v236, s92, v236\n" \
"v_mfma_f32_16x16x32_bf16 v[104:107], v[20:23], v[204:207], v[104:107]\n" \
"v_mul_f32 v237, s92, v237\n" \
"v_cvt_pk_bf16_f32 v234, v234, v235\n" \
"v_mfma_f32_16x16x32_bf16 v[100:103], v[8:11], v[200:203], v[100:103]\n" \
"ds_read_b64_tr_b16 v[46:47], v233 offset:11264\n" \
"v_cvt_pk_bf16_f32 v236, v236, v237\n" \
"v_mfma_f32_16x16x32_bf16 v[100:103], v[12:15], v[204:207], v[100:103]\n" \
"ds_read_b64_tr_b16 v[48:49], v232 offset:1536\n" \
"v_accvgpr_write_b32 a168, v234\n" \
"v_mfma_f32_16x16x32_bf16 v[96:99], v[0:3], v[200:203], v[96:99]\n" \
"s_add_u32 m0, s95, 20480\n" \
"ds_read_b64_tr_b16 v[50:51], v232 offset:3584\n" \
"v_accvgpr_write_b32 a169, v236\n" \
"v_mfma_f32_16x16x32_bf16 v[96:99], v[4:7], v[204:207], v[96:99]\n" \
"global_load_lds_dwordx4 v228, s[44:45] offset:-4096\n" \
"v_accvgpr_read_b32 v234, a172\n" \
"v_mfma_f32_16x16x32_bf16 v[128:131], v[0:3], v[208:211], v[128:131]\n" \
"ds_read_b64_tr_b16 v[52:53], v232 offset:9728\n" \
"v_accvgpr_read_b32 v235, a173\n" \
"v_mfma_f32_16x16x32_bf16 v[128:131], v[4:7], v[212:215], v[128:131]\n" \
"ds_read_b64_tr_b16 v[54:55], v232 offset:11776\n" \
"v_accvgpr_read_b32 v236, a174\n" \
"v_mfma_f32_16x16x32_bf16 v[132:135], v[8:11], v[208:211], v[132:135]\n" \
"ds_read_b64_tr_b16 v[56:57], v233 offset:1536\n" \
"v_accvgpr_read_b32 v237, a175\n" \
"v_mfma_f32_16x16x32_bf16 v[132:135], v[12:15], v[212:215], v[132:135]\n" \
"global_load_lds_dwordx4 v229, s[44:45] offset:-3072\n" \
"v_mul_f32 v234, s92, v234\n" \
"v_mfma_f32_16x16x32_bf16 v[136:139], v[16:19], v[208:211], v[136:139]\n" \
"s_add_u32 s44, s44, 0x80000\n" \
"s_addc_u32 s45, s45, 0\n" \
"ds_read_b64_tr_b16 v[58:59], v233 offset:3584\n" \
"v_mfma_f32_16x16x32_bf16 v[136:139], v[20:23], v[212:215], v[136:139]\n" \
"ds_read_b64_tr_b16 v[60:61], v233 offset:9728\n" \
"v_mul_f32 v235, s92, v235\n" \
"v_mfma_f32_16x16x32_bf16 v[140:143], v[24:27], v[208:211], v[140:143]\n" \
"ds_read_b64_tr_b16 v[62:63], v233 offset:11776\n" \
"v_mul_f32 v236, s92, v236\n" \
"v_mfma_f32_16x16x32_bf16 v[140:143], v[28:31], v[212:215], v[140:143]\n" \
"s_waitcnt lgkmcnt(3)\n" \
"global_load_lds_dwordx4 v228, s[42:43] offset:-2048\n" \
"v_mul_f32 v237, s92, v237\n" \
"v_mfma_f32_16x16x32_bf16 v[172:175], v[24:27], v[216:219], v[172:175]\n" \
"v_add_u32 v230, 0x8000, v230\n" \
"v_add_u32 v231, 0x8000, v231\n" \
"v_mfma_f32_16x16x32_bf16 v[172:175], v[28:31], v[220:223], v[172:175]\n" \
"v_add_u32 v232, 0x4000, v232\n" \
"v_add_u32 v233, 0x4000, v233\n" \
"v_mfma_f32_16x16x32_bf16 v[168:171], v[16:19], v[216:219], v[168:171]\n" \
"v_cvt_pk_bf16_f32 v234, v234, v235\n" \
"v_cvt_pk_bf16_f32 v236, v236, v237\n" \
"v_mfma_f32_16x16x32_bf16 v[168:171], v[20:23], v[220:223], v[168:171]\n" \
"global_load_lds_dwordx4 v229, s[42:43] offset:-1024\n" \
"v_accvgpr_write_b32 a170, v234\n" \
"v_mfma_f32_16x16x32_bf16 v[164:167], v[8:11], v[216:219], v[164:167]\n" \
"s_add_u32 s42, s42, 0x80000\n" \
"s_addc_u32 s43, s43, 0\n" \
"v_accvgpr_write_b32 a171, v236\n" \
"v_mfma_f32_16x16x32_bf16 v[164:167], v[12:15], v[220:223], v[164:167]\n" \
"s_waitcnt vmcnt(18) lgkmcnt(0)\n" \
"v_accvgpr_read_b32 v234, a144\n" \
"v_accvgpr_read_b32 v235, a145\n" \
"v_mfma_f32_16x16x32_bf16 v[160:163], v[0:3], v[216:219], v[160:163]\n" \
"v_accvgpr_read_b32 v236, a146\n" \
"v_accvgpr_read_b32 v237, a147\n" \
"v_mfma_f32_16x16x32_bf16 v[160:163], v[4:7], v[220:223], v[160:163]\n" \
"s_barrier\n" \
"v_mfma_f32_16x16x32_bf16 v[80:83], v[32:35], v[192:195], v[80:83]\n" \
"ds_read_b64_tr_b16 v[0:1], v232 offset:0\n" \
"v_cvt_pk_bf16_f32 v234, v234, v235\n" \
"v_mfma_f32_16x16x32_bf16 v[80:83], v[36:39], v[196:199], v[80:83]\n" \
"ds_read_b64_tr_b16 v[2:3], v232 offset:2048\n" \
"v_cvt_pk_bf16_f32 v236, v236, v237\n" \
"v_mfma_f32_16x16x32_bf16 v[84:87], v[40:43], v[192:195], v[84:87]\n" \
"s_add_u32 m0, s87, 102400\n" \
"ds_read_b64_tr_b16 v[4:5], v232 offset:8192\n" \
"v_accvgpr_write_b32 a144, v234\n" \
"v_mfma_f32_16x16x32_bf16 v[84:87], v[44:47], v[196:199], v[84:87]\n" \
"global_load_lds_dwordx4 v224, s[48:49] offset:-4096\n" \
"v_accvgpr_write_b32 a145, v236\n" \
"v_mfma_f32_16x16x32_bf16 v[88:91], v[48:51], v[192:195], v[88:91]\n" \
"ds_read_b64_tr_b16 v[6:7], v232 offset:10240\n" \
"v_accvgpr_read_b32 v234, a176\n" \
"v_mfma_f32_16x16x32_bf16 v[88:91], v[52:55], v[196:199], v[88:91]\n" \
"ds_read_b64_tr_b16 v[8:9], v233 offset:0\n" \
"v_accvgpr_read_b32 v235, a177\n" \
"v_mfma_f32_16x16x32_bf16 v[92:95], v[56:59], v[192:195], v[92:95]\n" \
"ds_read_b64_tr_b16 v[10:11], v233 offset:2048\n" \
"v_accvgpr_read_b32 v236, a178\n" \
"v_mfma_f32_16x16x32_bf16 v[92:95], v[60:63], v[196:199], v[92:95]\n" \
"global_load_lds_dwordx4 v225, s[48:49] offset:-3072\n" \
"v_accvgpr_read_b32 v237, a179\n" \
"v_mfma_f32_16x16x32_bf16 v[124:127], v[56:59], v[200:203], v[124:127]\n" \
"ds_read_b128 v[192:195], v230 offset:0\n" \
"v_cvt_pk_bf16_f32 v234, v234, v235\n" \
"v_mfma_f32_16x16x32_bf16 v[124:127], v[60:63], v[204:207], v[124:127]\n" \
"ds_read_b128 v[196:199], v231 offset:0\n" \
"v_cvt_pk_bf16_f32 v236, v236, v237\n" \
"v_mfma_f32_16x16x32_bf16 v[120:123], v[48:51], v[200:203], v[120:123]\n" \
"ds_read_b64_tr_b16 v[12:13], v233 offset:8192\n" \
"v_accvgpr_write_b32 a146, v234\n" \
"v_mfma_f32_16x16x32_bf16 v[120:123], v[52:55], v[204:207], v[120:123]\n" \
"global_load_lds_dwordx4 v226, s[48:49] offset:-2048\n" \
"v_accvgpr_write_b32 a147, v236\n" \
"v_mfma_f32_16x16x32_bf16 v[116:119], v[40:43], v[200:203], v[116:119]\n" \
"ds_read_b64_tr_b16 v[14:15], v233 offset:10240\n" \
"v_accvgpr_read_b32 v234, a148\n" \
"v_mfma_f32_16x16x32_bf16 v[116:119], v[44:47], v[204:207], v[116:119]\n" \
"ds_read_b64_tr_b16 v[16:17], v232 offset:512\n" \
"v_accvgpr_read_b32 v235, a149\n" \
"v_mfma_f32_16x16x32_bf16 v[112:115], v[32:35], v[200:203], v[112:115]\n" \
"ds_read_b64_tr_b16 v[18:19], v232 offset:2560\n" \
"v_accvgpr_read_b32 v236, a150\n" \
"v_mfma_f32_16x16x32_bf16 v[112:115], v[36:39], v[204:207], v[112:115]\n" \
"global_load_lds_dwordx4 v227, s[48:49] offset:-1024\n" \
"v_accvgpr_read_b32 v237, a151\n" \
"v_mfma_f32_16x16x32_bf16 v[144:147], v[32:35], v[208:211], v[144:147]\n" \
"s_add_u32 s48, s48, 0x80\n" \
"s_addc_u32 s49, s49, 0\n" \
"ds_read_b128 v[200:203], v230 offset:2048\n" \
"v_mfma_f32_16x16x32_bf16 v[144:147], v[36:39], v[212:215], v[144:147]\n" \
"ds_read_b128 v[204:207], v231 offset:2048\n" \
"v_cvt_pk_bf16_f32 v234, v234, v235\n" \
"v_mfma_f32_16x16x32_bf16 v[148:151], v[40:43], v[208:211], v[148:151]\n" \
"ds_read_b64_tr_b16 v[20:21], v232 offset:8704\n" \
"v_cvt_pk_bf16_f32 v236, v236, v237\n" \
"v_mfma_f32_16x16x32_bf16 v[148:151], v[44:47], v[212:215], v[148:151]\n" \
"global_load_lds_dwordx4 v224, s[46:47] offset:0\n" \
"v_accvgpr_write_b32 a148, v234\n" \
"v_mfma_f32_16x16x32_bf16 v[152:155], v[48:51], v[208:211], v[152:155]\n" \
"ds_read_b64_tr_b16 v[22:23], v232 offset:10752\n" \
"v_accvgpr_write_b32 a149, v236\n" \
"v_mfma_f32_16x16x32_bf16 v[152:155], v[52:55], v[212:215], v[152:155]\n" \
"ds_read_b64_tr_b16 v[24:25], v233 offset:512\n" \
"v_accvgpr_read_b32 v234, a180\n" \
"v_mfma_f32_16x16x32_bf16 v[156:159], v[56:59], v[208:211], v[156:159]\n" \
"ds_read_b64_tr_b16 v[26:27], v233 offset:2560\n" \
"v_accvgpr_read_b32 v235, a181\n" \
"v_mfma_f32_16x16x32_bf16 v[156:159], v[60:63], v[212:215], v[156:159]\n" \
"global_load_lds_dwordx4 v225, s[46:47] offset:1024\n" \
"v_accvgpr_read_b32 v236, a182\n" \
"v_mfma_f32_16x16x32_bf16 v[188:191], v[56:59], v[216:219], v[188:191]\n" \
"ds_read_b128 v[208:211], v230 offset:4096\n" \
"v_accvgpr_read_b32 v237, a183\n" \
"v_mfma_f32_16x16x32_bf16 v[188:191], v[60:63], v[220:223], v[188:191]\n" \
"ds_read_b128 v[212:215], v231 offset:4096\n" \
"v_cvt_pk_bf16_f32 v234, v234, v235\n" \
"v_mfma_f32_16x16x32_bf16 v[184:187], v[48:51], v[216:219], v[184:187]\n" \
"ds_read_b64_tr_b16 v[28:29], v233 offset:8704\n" \
"v_cvt_pk_bf16_f32 v236, v236, v237\n" \
"v_mfma_f32_16x16x32_bf16 v[184:187], v[52:55], v[220:223], v[184:187]\n" \
"global_load_lds_dwordx4 v226, s[46:47] offset:2048\n" \
"v_accvgpr_write_b32 a150, v234\n" \
"v_mfma_f32_16x16x32_bf16 v[180:183], v[40:43], v[216:219], v[180:183]\n" \
"ds_read_b64_tr_b16 v[30:31], v233 offset:10752\n" \
"v_accvgpr_write_b32 a151, v236\n" \
"v_mfma_f32_16x16x32_bf16 v[180:183], v[44:47], v[220:223], v[180:183]\n" \
"v_accvgpr_read_b32 v234, a152\n" \
"v_accvgpr_read_b32 v235, a153\n" \
"v_mfma_f32_16x16x32_bf16 v[176:179], v[32:35], v[216:219], v[176:179]\n" \
"v_accvgpr_read_b32 v236, a154\n" \
"Lpg2766_%=:\n" \
"s_cbranch_execz Lpg4934_%=\n" \
"v_accvgpr_read_b32 v237, a155\n" \
"v_mfma_f32_16x16x32_bf16 v[176:179], v[36:39], v[220:223], v[176:179]\n" \
"s_waitcnt lgkmcnt(1)\n" \
"global_load_lds_dwordx4 v227, s[46:47] offset:3072\n" \
"v_cvt_pk_bf16_f32 v234, v234, v235\n" \
"v_mfma_f32_16x16x32_bf16 v[64:67], v[0:3], v[192:195], v[64:67]\n" \
"s_add_u32 s46, s46, 0x80\n" \
"s_addc_u32 s47, s47, 0\n" \
"ds_read_b128 v[216:219], v230 offset:6144\n" \
"v_mfma_f32_16x16x32_bf16 v[64:67], v[4:7], v[196:199], v[64:67]\n" \
"s_add_u32 s90, s89, 0\n" \
"ds_read_b128 v[220:223], v231 offset:6144\n" \
"v_cvt_pk_bf16_f32 v236, v236, v237\n" \
"v_mfma_f32_16x16x32_bf16 v[68:71], v[8:11], v[192:195], v[68:71]\n" \
"ds_read_b64_tr_b16 v[32:33], v232 offset:1024\n" \
"v_accvgpr_write_b32 a152, v234\n" \
"v_mfma_f32_16x16x32_bf16 v[68:71], v[12:15], v[196:199], v[68:71]\n" \
"buffer_store_dwordx4 a[208:211], v248, s[56:59], s90 offen offset:0 sc1\n" \
"v_accvgpr_write_b32 a153, v236\n" \
"v_accvgpr_read_b32 v234, a184\n" \
"v_accvgpr_read_b32 v235, a185\n" \
"v_accvgpr_read_b32 v236, a186\n" \
"v_mfma_f32_16x16x32_bf16 v[72:75], v[16:19], v[192:195], v[72:75]\n" \
"ds_read_b64_tr_b16 v[34:35], v232 offset:3072\n" \
"v_accvgpr_read_b32 v237, a187\n" \
"v_mfma_f32_16x16x32_bf16 v[72:75], v[20:23], v[196:199], v[72:75]\n" \
"s_waitcnt lgkmcnt(3)\n" \
"ds_read_b64_tr_b16 v[36:37], v232 offset:9216\n" \
"v_cvt_pk_bf16_f32 v234, v234, v235\n" \
"v_mfma_f32_16x16x32_bf16 v[76:79], v[24:27], v[192:195], v[76:79]\n" \
"ds_read_b64_tr_b16 v[38:39], v232 offset:11264\n" \
"v_cvt_pk_bf16_f32 v236, v236, v237\n" \
"v_mfma_f32_16x16x32_bf16 v[76:79], v[28:31], v[196:199], v[76:79]\n" \
"v_accvgpr_write_b32 a154, v234\n" \
"v_accvgpr_write_b32 a155, v236\n" \
"v_mfma_f32_16x16x32_bf16 v[108:111], v[24:27], v[200:203], v[108:111]\n" \
"ds_read_b64_tr_b16 v[40:41], v233 offset:1024\n" \
"v_accvgpr_read_b32 v234, a156\n" \
"v_mfma_f32_16x16x32_bf16 v[108:111], v[28:31], v[204:207], v[108:111]\n" \
"ds_read_b64_tr_b16 v[42:43], v233 offset:3072\n" \
"v_accvgpr_read_b32 v235, a157\n" \
"v_mfma_f32_16x16x32_bf16 v[104:107], v[16:19], v[200:203], v[104:107]\n" \
"ds_read_b64_tr_b16 v[44:45], v233 offset:9216\n" \
"v_accvgpr_read_b32 v236, a158\n" \
"v_mfma_f32_16x16x32_bf16 v[104:107], v[20:23], v[204:207], v[104:107]\n" \
"v_accvgpr_read_b32 v237, a159\n" \
"v_cvt_pk_bf16_f32 v234, v234, v235\n" \
"v_mfma_f32_16x16x32_bf16 v[100:103], v[8:11], v[200:203], v[100:103]\n" \
"ds_read_b64_tr_b16 v[46:47], v233 offset:11264\n" \
"v_cvt_pk_bf16_f32 v236, v236, v237\n" \
"v_mfma_f32_16x16x32_bf16 v[100:103], v[12:15], v[204:207], v[100:103]\n" \
"ds_read_b64_tr_b16 v[48:49], v232 offset:1536\n" \
"v_accvgpr_write_b32 a156, v234\n" \
"v_mfma_f32_16x16x32_bf16 v[96:99], v[0:3], v[200:203], v[96:99]\n" \
"s_add_u32 m0, s95, 36864\n" \
"ds_read_b64_tr_b16 v[50:51], v232 offset:3584\n" \
"v_accvgpr_write_b32 a157, v236\n" \
"v_mfma_f32_16x16x32_bf16 v[96:99], v[4:7], v[204:207], v[96:99]\n" \
"global_load_lds_dwordx4 v228, s[44:45] offset:-4096\n" \
"v_accvgpr_read_b32 v234, a188\n" \
"v_mfma_f32_16x16x32_bf16 v[128:131], v[0:3], v[208:211], v[128:131]\n" \
"ds_read_b64_tr_b16 v[52:53], v232 offset:9728\n" \
"v_accvgpr_read_b32 v235, a189\n" \
"v_mfma_f32_16x16x32_bf16 v[128:131], v[4:7], v[212:215], v[128:131]\n" \
"ds_read_b64_tr_b16 v[54:55], v232 offset:11776\n" \
"v_accvgpr_read_b32 v236, a190\n" \
"v_mfma_f32_16x16x32_bf16 v[132:135], v[8:11], v[208:211], v[132:135]\n" \
"ds_read_b64_tr_b16 v[56:57], v233 offset:1536\n" \
"v_accvgpr_read_b32 v237, a191\n" \
"v_mfma_f32_16x16x32_bf16 v[132:135], v[12:15], v[212:215], v[132:135]\n" \
"global_load_lds_dwordx4 v229, s[44:45] offset:-3072\n" \
"v_cvt_pk_bf16_f32 v234, v234, v235\n" \
"v_mfma_f32_16x16x32_bf16 v[136:139], v[16:19], v[208:211], v[136:139]\n" \
"s_add_u32 s44, s44, 0x80000\n" \
"s_addc_u32 s45, s45, 0\n" \
"ds_read_b64_tr_b16 v[58:59], v233 offset:3584\n" \
"v_mfma_f32_16x16x32_bf16 v[136:139], v[20:23], v[212:215], v[136:139]\n" \
"ds_read_b64_tr_b16 v[60:61], v233 offset:9728\n" \
"v_cvt_pk_bf16_f32 v236, v236, v237\n" \
"v_mfma_f32_16x16x32_bf16 v[140:143], v[24:27], v[208:211], v[140:143]\n" \
"ds_read_b64_tr_b16 v[62:63], v233 offset:11776\n" \
"v_accvgpr_write_b32 a158, v234\n" \
"v_mfma_f32_16x16x32_bf16 v[140:143], v[28:31], v[212:215], v[140:143]\n" \
"s_waitcnt lgkmcnt(3)\n" \
"global_load_lds_dwordx4 v228, s[42:43] offset:-2048\n" \
"v_accvgpr_write_b32 a159, v236\n" \
"v_mfma_f32_16x16x32_bf16 v[172:175], v[24:27], v[216:219], v[172:175]\n" \
"v_add_u32 v230, 0xffff0000, v230\n" \
"v_add_u32 v231, 0xffff0000, v231\n" \
"v_mfma_f32_16x16x32_bf16 v[172:175], v[28:31], v[220:223], v[172:175]\n" \
"v_add_u32 v232, 0xffff4000, v232\n" \
"v_add_u32 v233, 0xffff4000, v233\n" \
"v_mfma_f32_16x16x32_bf16 v[168:171], v[16:19], v[216:219], v[168:171]\n" \
"v_accvgpr_read_b32 v234, a64\n" \
"v_accvgpr_read_b32 v235, a65\n" \
"v_mfma_f32_16x16x32_bf16 v[168:171], v[20:23], v[220:223], v[168:171]\n" \
"global_load_lds_dwordx4 v229, s[42:43] offset:-1024\n" \
"v_accvgpr_read_b32 v236, a66\n" \
"v_mfma_f32_16x16x32_bf16 v[164:167], v[8:11], v[216:219], v[164:167]\n" \
"s_add_u32 s42, s42, 0x80000\n" \
"s_addc_u32 s43, s43, 0\n" \
"v_accvgpr_read_b32 v237, a67\n" \
"v_mfma_f32_16x16x32_bf16 v[164:167], v[12:15], v[220:223], v[164:167]\n" \
"s_waitcnt vmcnt(18) lgkmcnt(0)\n" \
"v_mul_f32 v234, s92, v234\n" \
"v_mul_f32 v235, s92, v235\n" \
"v_mfma_f32_16x16x32_bf16 v[160:163], v[0:3], v[216:219], v[160:163]\n" \
"v_mul_f32 v236, s92, v236\n" \
"v_mul_f32 v237, s92, v237\n" \
"v_mfma_f32_16x16x32_bf16 v[160:163], v[4:7], v[220:223], v[160:163]\n" \
"s_barrier\n" \
"v_mfma_f32_16x16x32_bf16 v[80:83], v[32:35], v[192:195], v[80:83]\n" \
"ds_read_b64_tr_b16 v[0:1], v232 offset:0\n" \
"v_cvt_pk_bf16_f32 v234, v234, v235\n" \
"v_mfma_f32_16x16x32_bf16 v[80:83], v[36:39], v[196:199], v[80:83]\n" \
"ds_read_b64_tr_b16 v[2:3], v232 offset:2048\n" \
"v_cvt_pk_bf16_f32 v236, v236, v237\n" \
"v_mfma_f32_16x16x32_bf16 v[84:87], v[40:43], v[192:195], v[84:87]\n" \
"s_add_u32 m0, s87, 135168\n" \
"ds_read_b64_tr_b16 v[4:5], v232 offset:8192\n" \
"v_accvgpr_write_b32 a64, v234\n" \
"v_mfma_f32_16x16x32_bf16 v[84:87], v[44:47], v[196:199], v[84:87]\n" \
"global_load_lds_dwordx4 v224, s[48:49] offset:-4096\n" \
"v_accvgpr_write_b32 a65, v236\n" \
"v_mfma_f32_16x16x32_bf16 v[88:91], v[48:51], v[192:195], v[88:91]\n" \
"ds_read_b64_tr_b16 v[6:7], v232 offset:10240\n" \
"v_accvgpr_read_b32 v234, a68\n" \
"v_mfma_f32_16x16x32_bf16 v[88:91], v[52:55], v[196:199], v[88:91]\n" \
"ds_read_b64_tr_b16 v[8:9], v233 offset:0\n" \
"v_accvgpr_read_b32 v235, a69\n" \
"v_mfma_f32_16x16x32_bf16 v[92:95], v[56:59], v[192:195], v[92:95]\n" \
"ds_read_b64_tr_b16 v[10:11], v233 offset:2048\n" \
"v_accvgpr_read_b32 v236, a70\n" \
"v_mfma_f32_16x16x32_bf16 v[92:95], v[60:63], v[196:199], v[92:95]\n" \
"global_load_lds_dwordx4 v225, s[48:49] offset:-3072\n" \
"v_accvgpr_read_b32 v237, a71\n" \
"v_mfma_f32_16x16x32_bf16 v[124:127], v[56:59], v[200:203], v[124:127]\n" \
"ds_read_b128 v[192:195], v230 offset:0\n" \
"v_mul_f32 v234, s92, v234\n" \
"v_mfma_f32_16x16x32_bf16 v[124:127], v[60:63], v[204:207], v[124:127]\n" \
"ds_read_b128 v[196:199], v231 offset:0\n" \
"v_mul_f32 v235, s92, v235\n" \
"v_mfma_f32_16x16x32_bf16 v[120:123], v[48:51], v[200:203], v[120:123]\n" \
"ds_read_b64_tr_b16 v[12:13], v233 offset:8192\n" \
"v_mul_f32 v236, s92, v236\n" \
"v_mfma_f32_16x16x32_bf16 v[120:123], v[52:55], v[204:207], v[120:123]\n" \
"global_load_lds_dwordx4 v226, s[48:49] offset:-2048\n" \
"v_mul_f32 v237, s92, v237\n" \
"v_mfma_f32_16x16x32_bf16 v[116:119], v[40:43], v[200:203], v[116:119]\n" \
"ds_read_b64_tr_b16 v[14:15], v233 offset:10240\n" \
"v_cvt_pk_bf16_f32 v234, v234, v235\n" \
"v_mfma_f32_16x16x32_bf16 v[116:119], v[44:47], v[204:207], v[116:119]\n" \
"ds_read_b64_tr_b16 v[16:17], v232 offset:512\n" \
"v_cvt_pk_bf16_f32 v236, v236, v237\n" \
"v_mfma_f32_16x16x32_bf16 v[112:115], v[32:35], v[200:203], v[112:115]\n" \
"ds_read_b64_tr_b16 v[18:19], v232 offset:2560\n" \
"v_accvgpr_write_b32 a66, v234\n" \
"v_mfma_f32_16x16x32_bf16 v[112:115], v[36:39], v[204:207], v[112:115]\n" \
"global_load_lds_dwordx4 v227, s[48:49] offset:-1024\n" \
"v_accvgpr_write_b32 a67, v236\n" \
"v_mfma_f32_16x16x32_bf16 v[144:147], v[32:35], v[208:211], v[144:147]\n" \
"s_add_u32 s48, s48, 0x80\n" \
"s_addc_u32 s49, s49, 0\n" \
"ds_read_b128 v[200:203], v230 offset:2048\n" \
"v_mfma_f32_16x16x32_bf16 v[144:147], v[36:39], v[212:215], v[144:147]\n" \
"ds_read_b128 v[204:207], v231 offset:2048\n" \
"v_accvgpr_read_b32 v234, a96\n" \
"v_mfma_f32_16x16x32_bf16 v[148:151], v[40:43], v[208:211], v[148:151]\n" \
"ds_read_b64_tr_b16 v[20:21], v232 offset:8704\n" \
"v_accvgpr_read_b32 v235, a97\n" \
"v_mfma_f32_16x16x32_bf16 v[148:151], v[44:47], v[212:215], v[148:151]\n" \
"global_load_lds_dwordx4 v224, s[46:47] offset:0\n" \
"v_accvgpr_read_b32 v236, a98\n" \
"v_mfma_f32_16x16x32_bf16 v[152:155], v[48:51], v[208:211], v[152:155]\n" \
"ds_read_b64_tr_b16 v[22:23], v232 offset:10752\n" \
"v_accvgpr_read_b32 v237, a99\n" \
"v_mfma_f32_16x16x32_bf16 v[152:155], v[52:55], v[212:215], v[152:155]\n" \
"ds_read_b64_tr_b16 v[24:25], v233 offset:512\n" \
"v_mul_f32 v234, s92, v234\n" \
"v_mfma_f32_16x16x32_bf16 v[156:159], v[56:59], v[208:211], v[156:159]\n" \
"ds_read_b64_tr_b16 v[26:27], v233 offset:2560\n" \
"v_mul_f32 v235, s92, v235\n" \
"v_mfma_f32_16x16x32_bf16 v[156:159], v[60:63], v[212:215], v[156:159]\n" \
"global_load_lds_dwordx4 v225, s[46:47] offset:1024\n" \
"v_mul_f32 v236, s92, v236\n" \
"v_mfma_f32_16x16x32_bf16 v[188:191], v[56:59], v[216:219], v[188:191]\n" \
"ds_read_b128 v[208:211], v230 offset:4096\n" \
"v_mul_f32 v237, s92, v237\n" \
"v_mfma_f32_16x16x32_bf16 v[188:191], v[60:63], v[220:223], v[188:191]\n" \
"ds_read_b128 v[212:215], v231 offset:4096\n" \
"v_cvt_pk_bf16_f32 v234, v234, v235\n" \
"v_mfma_f32_16x16x32_bf16 v[184:187], v[48:51], v[216:219], v[184:187]\n" \
"ds_read_b64_tr_b16 v[28:29], v233 offset:8704\n" \
"v_cvt_pk_bf16_f32 v236, v236, v237\n" \
"v_mfma_f32_16x16x32_bf16 v[184:187], v[52:55], v[220:223], v[184:187]\n" \
"global_load_lds_dwordx4 v226, s[46:47] offset:2048\n" \
"v_accvgpr_write_b32 a96, v234\n" \
"v_mfma_f32_16x16x32_bf16 v[180:183], v[40:43], v[216:219], v[180:183]\n" \
"ds_read_b64_tr_b16 v[30:31], v233 offset:10752\n" \
"v_accvgpr_write_b32 a97, v236\n" \
"v_mfma_f32_16x16x32_bf16 v[180:183], v[44:47], v[220:223], v[180:183]\n" \
"v_accvgpr_read_b32 v234, a100\n" \
"v_accvgpr_read_b32 v235, a101\n" \
"v_mfma_f32_16x16x32_bf16 v[176:179], v[32:35], v[216:219], v[176:179]\n" \
"v_accvgpr_read_b32 v236, a102\n" \
"v_accvgpr_read_b32 v237, a103\n" \
"v_mfma_f32_16x16x32_bf16 v[176:179], v[36:39], v[220:223], v[176:179]\n" \
"s_waitcnt lgkmcnt(1)\n" \
"global_load_lds_dwordx4 v227, s[46:47] offset:3072\n" \
"v_mul_f32 v234, s92, v234\n" \
"v_mfma_f32_16x16x32_bf16 v[64:67], v[0:3], v[192:195], v[64:67]\n" \
"s_add_u32 s46, s46, 0x80\n" \
"s_addc_u32 s47, s47, 0\n" \
"ds_read_b128 v[216:219], v230 offset:6144\n" \
"v_mfma_f32_16x16x32_bf16 v[64:67], v[4:7], v[196:199], v[64:67]\n" \
"ds_read_b128 v[220:223], v231 offset:6144\n" \
"v_mul_f32 v235, s92, v235\n" \
"v_mfma_f32_16x16x32_bf16 v[68:71], v[8:11], v[192:195], v[68:71]\n" \
"ds_read_b64_tr_b16 v[32:33], v232 offset:1024\n" \
"v_mul_f32 v236, s92, v236\n" \
"v_mfma_f32_16x16x32_bf16 v[68:71], v[12:15], v[196:199], v[68:71]\n" \
"buffer_store_dwordx4 a[212:215], v248, s[56:59], s90 offen offset:1024 sc1\n" \
"v_mul_f32 v237, s92, v237\n" \
"v_cvt_pk_bf16_f32 v234, v234, v235\n" \
"v_cvt_pk_bf16_f32 v236, v236, v237\n" \
"v_accvgpr_write_b32 a98, v234\n" \
"v_mfma_f32_16x16x32_bf16 v[72:75], v[16:19], v[192:195], v[72:75]\n" \
"ds_read_b64_tr_b16 v[34:35], v232 offset:3072\n" \
"v_accvgpr_write_b32 a99, v236\n" \
"v_mfma_f32_16x16x32_bf16 v[72:75], v[20:23], v[196:199], v[72:75]\n" \
"s_waitcnt lgkmcnt(3)\n" \
"ds_read_b64_tr_b16 v[36:37], v232 offset:9216\n" \
"v_accvgpr_read_b32 v234, a72\n" \
"v_mfma_f32_16x16x32_bf16 v[76:79], v[24:27], v[192:195], v[76:79]\n" \
"ds_read_b64_tr_b16 v[38:39], v232 offset:11264\n" \
"v_accvgpr_read_b32 v235, a73\n" \
"v_mfma_f32_16x16x32_bf16 v[76:79], v[28:31], v[196:199], v[76:79]\n" \
"buffer_store_dwordx4 a[216:219], v248, s[56:59], s90 offen offset:2048 sc1\n" \
"v_accvgpr_read_b32 v236, a74\n" \
"v_accvgpr_read_b32 v237, a75\n" \
"v_mul_f32 v234, s92, v234\n" \
"v_mul_f32 v235, s92, v235\n" \
"v_mfma_f32_16x16x32_bf16 v[108:111], v[24:27], v[200:203], v[108:111]\n" \
"ds_read_b64_tr_b16 v[40:41], v233 offset:1024\n" \
"v_mul_f32 v236, s92, v236\n" \
"v_mfma_f32_16x16x32_bf16 v[108:111], v[28:31], v[204:207], v[108:111]\n" \
"ds_read_b64_tr_b16 v[42:43], v233 offset:3072\n" \
"v_mul_f32 v237, s92, v237\n" \
"v_mfma_f32_16x16x32_bf16 v[104:107], v[16:19], v[200:203], v[104:107]\n" \
"ds_read_b64_tr_b16 v[44:45], v233 offset:9216\n" \
"v_cvt_pk_bf16_f32 v234, v234, v235\n" \
"v_mfma_f32_16x16x32_bf16 v[104:107], v[20:23], v[204:207], v[104:107]\n" \
"v_cvt_pk_bf16_f32 v236, v236, v237\n" \
"v_accvgpr_write_b32 a72, v234\n" \
"v_mfma_f32_16x16x32_bf16 v[100:103], v[8:11], v[200:203], v[100:103]\n" \
"ds_read_b64_tr_b16 v[46:47], v233 offset:11264\n" \
"v_accvgpr_write_b32 a73, v236\n" \
"v_mfma_f32_16x16x32_bf16 v[100:103], v[12:15], v[204:207], v[100:103]\n" \
"ds_read_b64_tr_b16 v[48:49], v232 offset:1536\n" \
"v_accvgpr_read_b32 v234, a76\n" \
"v_mfma_f32_16x16x32_bf16 v[96:99], v[0:3], v[200:203], v[96:99]\n" \
"s_add_u32 m0, s95, 53248\n" \
"ds_read_b64_tr_b16 v[50:51], v232 offset:3584\n" \
"v_accvgpr_read_b32 v235, a77\n" \
"v_mfma_f32_16x16x32_bf16 v[96:99], v[4:7], v[204:207], v[96:99]\n" \
"global_load_lds_dwordx4 v228, s[44:45] offset:-4096\n" \
"v_accvgpr_read_b32 v236, a78\n" \
"v_mfma_f32_16x16x32_bf16 v[128:131], v[0:3], v[208:211], v[128:131]\n" \
"ds_read_b64_tr_b16 v[52:53], v232 offset:9728\n" \
"v_accvgpr_read_b32 v237, a79\n" \
"v_mfma_f32_16x16x32_bf16 v[128:131], v[4:7], v[212:215], v[128:131]\n" \
"ds_read_b64_tr_b16 v[54:55], v232 offset:11776\n" \
"v_mul_f32 v234, s92, v234\n" \
"v_mfma_f32_16x16x32_bf16 v[132:135], v[8:11], v[208:211], v[132:135]\n" \
"ds_read_b64_tr_b16 v[56:57], v233 offset:1536\n" \
"v_mul_f32 v235, s92, v235\n" \
"v_mfma_f32_16x16x32_bf16 v[132:135], v[12:15], v[212:215], v[132:135]\n" \
"global_load_lds_dwordx4 v229, s[44:45] offset:-3072\n" \
"v_mul_f32 v236, s92, v236\n" \
"v_mfma_f32_16x16x32_bf16 v[136:139], v[16:19], v[208:211], v[136:139]\n" \
"s_add_u32 s44, s44, 0x80000\n" \
"s_addc_u32 s45, s45, 0\n" \
"ds_read_b64_tr_b16 v[58:59], v233 offset:3584\n" \
"v_mfma_f32_16x16x32_bf16 v[136:139], v[20:23], v[212:215], v[136:139]\n" \
"ds_read_b64_tr_b16 v[60:61], v233 offset:9728\n" \
"v_mul_f32 v237, s92, v237\n" \
"v_mfma_f32_16x16x32_bf16 v[140:143], v[24:27], v[208:211], v[140:143]\n" \
"ds_read_b64_tr_b16 v[62:63], v233 offset:11776\n" \
"v_cvt_pk_bf16_f32 v234, v234, v235\n" \
"v_mfma_f32_16x16x32_bf16 v[140:143], v[28:31], v[212:215], v[140:143]\n" \
"s_waitcnt lgkmcnt(3)\n" \
"global_load_lds_dwordx4 v228, s[42:43] offset:-2048\n" \
"v_cvt_pk_bf16_f32 v236, v236, v237\n" \
"v_mfma_f32_16x16x32_bf16 v[172:175], v[24:27], v[216:219], v[172:175]\n" \
"v_add_u32 v230, 0x8000, v230\n" \
"v_add_u32 v231, 0x8000, v231\n" \
"v_mfma_f32_16x16x32_bf16 v[172:175], v[28:31], v[220:223], v[172:175]\n" \
"v_add_u32 v232, 0x4000, v232\n" \
"v_add_u32 v233, 0x4000, v233\n" \
"v_mfma_f32_16x16x32_bf16 v[168:171], v[16:19], v[216:219], v[168:171]\n" \
"v_accvgpr_write_b32 a74, v234\n" \
"v_accvgpr_write_b32 a75, v236\n" \
"v_mfma_f32_16x16x32_bf16 v[168:171], v[20:23], v[220:223], v[168:171]\n" \
"global_load_lds_dwordx4 v229, s[42:43] offset:-1024\n" \
"v_accvgpr_read_b32 v234, a104\n" \
"v_mfma_f32_16x16x32_bf16 v[164:167], v[8:11], v[216:219], v[164:167]\n" \
"s_add_u32 s42, s42, 0x80000\n" \
"s_addc_u32 s43, s43, 0\n" \
"v_accvgpr_read_b32 v235, a105\n" \
"v_mfma_f32_16x16x32_bf16 v[164:167], v[12:15], v[220:223], v[164:167]\n" \
"s_waitcnt vmcnt(19) lgkmcnt(0)\n" \
"v_accvgpr_read_b32 v236, a106\n" \
"v_accvgpr_read_b32 v237, a107\n" \
"v_mfma_f32_16x16x32_bf16 v[160:163], v[0:3], v[216:219], v[160:163]\n" \
"v_mul_f32 v234, s92, v234\n" \
"v_mul_f32 v235, s92, v235\n" \
"v_mfma_f32_16x16x32_bf16 v[160:163], v[4:7], v[220:223], v[160:163]\n" \
"s_barrier\n" \
"v_mfma_f32_16x16x32_bf16 v[80:83], v[32:35], v[192:195], v[80:83]\n" \
"ds_read_b64_tr_b16 v[0:1], v232 offset:0\n" \
"v_mul_f32 v236, s92, v236\n" \
"v_mfma_f32_16x16x32_bf16 v[80:83], v[36:39], v[196:199], v[80:83]\n" \
"ds_read_b64_tr_b16 v[2:3], v232 offset:2048\n" \
"v_mul_f32 v237, s92, v237\n" \
"v_mfma_f32_16x16x32_bf16 v[84:87], v[40:43], v[192:195], v[84:87]\n" \
"s_add_u32 m0, s87, 69632\n" \
"ds_read_b64_tr_b16 v[4:5], v232 offset:8192\n" \
"v_cvt_pk_bf16_f32 v234, v234, v235\n" \
"v_mfma_f32_16x16x32_bf16 v[84:87], v[44:47], v[196:199], v[84:87]\n" \
"global_load_lds_dwordx4 v224, s[48:49] offset:-4096\n" \
"v_cvt_pk_bf16_f32 v236, v236, v237\n" \
"v_mfma_f32_16x16x32_bf16 v[88:91], v[48:51], v[192:195], v[88:91]\n" \
"ds_read_b64_tr_b16 v[6:7], v232 offset:10240\n" \
"v_accvgpr_write_b32 a104, v234\n" \
"v_mfma_f32_16x16x32_bf16 v[88:91], v[52:55], v[196:199], v[88:91]\n" \
"ds_read_b64_tr_b16 v[8:9], v233 offset:0\n" \
"v_accvgpr_write_b32 a105, v236\n" \
"v_mfma_f32_16x16x32_bf16 v[92:95], v[56:59], v[192:195], v[92:95]\n" \
"ds_read_b64_tr_b16 v[10:11], v233 offset:2048\n" \
"v_accvgpr_read_b32 v234, a108\n" \
"v_mfma_f32_16x16x32_bf16 v[92:95], v[60:63], v[196:199], v[92:95]\n" \
"global_load_lds_dwordx4 v225, s[48:49] offset:-3072\n" \
"v_accvgpr_read_b32 v235, a109\n" \
"v_mfma_f32_16x16x32_bf16 v[124:127], v[56:59], v[200:203], v[124:127]\n" \
"ds_read_b128 v[192:195], v230 offset:0\n" \
"v_accvgpr_read_b32 v236, a110\n" \
"v_mfma_f32_16x16x32_bf16 v[124:127], v[60:63], v[204:207], v[124:127]\n" \
"ds_read_b128 v[196:199], v231 offset:0\n" \
"v_accvgpr_read_b32 v237, a111\n" \
"v_mfma_f32_16x16x32_bf16 v[120:123], v[48:51], v[200:203], v[120:123]\n" \
"ds_read_b64_tr_b16 v[12:13], v233 offset:8192\n" \
"v_mul_f32 v234, s92, v234\n" \
"v_mfma_f32_16x16x32_bf16 v[120:123], v[52:55], v[204:207], v[120:123]\n" \
"global_load_lds_dwordx4 v226, s[48:49] offset:-2048\n" \
"v_mul_f32 v235, s92, v235\n" \
"v_mfma_f32_16x16x32_bf16 v[116:119], v[40:43], v[200:203], v[116:119]\n" \
"ds_read_b64_tr_b16 v[14:15], v233 offset:10240\n" \
"v_mul_f32 v236, s92, v236\n" \
"v_mfma_f32_16x16x32_bf16 v[116:119], v[44:47], v[204:207], v[116:119]\n" \
"ds_read_b64_tr_b16 v[16:17], v232 offset:512\n" \
"v_mul_f32 v237, s92, v237\n" \
"v_mfma_f32_16x16x32_bf16 v[112:115], v[32:35], v[200:203], v[112:115]\n" \
"ds_read_b64_tr_b16 v[18:19], v232 offset:2560\n" \
"v_cvt_pk_bf16_f32 v234, v234, v235\n" \
"v_mfma_f32_16x16x32_bf16 v[112:115], v[36:39], v[204:207], v[112:115]\n" \
"global_load_lds_dwordx4 v227, s[48:49] offset:-1024\n" \
"v_cvt_pk_bf16_f32 v236, v236, v237\n" \
"v_mfma_f32_16x16x32_bf16 v[144:147], v[32:35], v[208:211], v[144:147]\n" \
"s_add_u32 s48, s48, 0x80\n" \
"s_addc_u32 s49, s49, 0\n" \
"ds_read_b128 v[200:203], v230 offset:2048\n" \
"v_mfma_f32_16x16x32_bf16 v[144:147], v[36:39], v[212:215], v[144:147]\n" \
"ds_read_b128 v[204:207], v231 offset:2048\n" \
"v_accvgpr_write_b32 a106, v234\n" \
"v_mfma_f32_16x16x32_bf16 v[148:151], v[40:43], v[208:211], v[148:151]\n" \
"ds_read_b64_tr_b16 v[20:21], v232 offset:8704\n" \
"v_accvgpr_write_b32 a107, v236\n" \
"v_mfma_f32_16x16x32_bf16 v[148:151], v[44:47], v[212:215], v[148:151]\n" \
"global_load_lds_dwordx4 v224, s[46:47] offset:0\n" \
"v_accvgpr_read_b32 v234, a80\n" \
"v_mfma_f32_16x16x32_bf16 v[152:155], v[48:51], v[208:211], v[152:155]\n" \
"ds_read_b64_tr_b16 v[22:23], v232 offset:10752\n" \
"v_accvgpr_read_b32 v235, a81\n" \
"v_mfma_f32_16x16x32_bf16 v[152:155], v[52:55], v[212:215], v[152:155]\n" \
"ds_read_b64_tr_b16 v[24:25], v233 offset:512\n" \
"v_accvgpr_read_b32 v236, a82\n" \
"v_mfma_f32_16x16x32_bf16 v[156:159], v[56:59], v[208:211], v[156:159]\n" \
"ds_read_b64_tr_b16 v[26:27], v233 offset:2560\n" \
"v_accvgpr_read_b32 v237, a83\n" \
"v_mfma_f32_16x16x32_bf16 v[156:159], v[60:63], v[212:215], v[156:159]\n" \
"global_load_lds_dwordx4 v225, s[46:47] offset:1024\n" \
"v_cvt_pk_bf16_f32 v234, v234, v235\n" \
"v_mfma_f32_16x16x32_bf16 v[188:191], v[56:59], v[216:219], v[188:191]\n" \
"ds_read_b128 v[208:211], v230 offset:4096\n" \
"v_cvt_pk_bf16_f32 v236, v236, v237\n" \
"v_mfma_f32_16x16x32_bf16 v[188:191], v[60:63], v[220:223], v[188:191]\n" \
"ds_read_b128 v[212:215], v231 offset:4096\n" \
"v_accvgpr_write_b32 a80, v234\n" \
"v_mfma_f32_16x16x32_bf16 v[184:187], v[48:51], v[216:219], v[184:187]\n" \
"ds_read_b64_tr_b16 v[28:29], v233 offset:8704\n" \
"v_accvgpr_write_b32 a81, v236\n" \
"v_mfma_f32_16x16x32_bf16 v[184:187], v[52:55], v[220:223], v[184:187]\n" \
"global_load_lds_dwordx4 v226, s[46:47] offset:2048\n" \
"v_accvgpr_read_b32 v234, a112\n" \
"v_mfma_f32_16x16x32_bf16 v[180:183], v[40:43], v[216:219], v[180:183]\n" \
"ds_read_b64_tr_b16 v[30:31], v233 offset:10752\n" \
"v_accvgpr_read_b32 v235, a113\n" \
"v_mfma_f32_16x16x32_bf16 v[180:183], v[44:47], v[220:223], v[180:183]\n" \
"v_accvgpr_read_b32 v236, a114\n" \
"v_accvgpr_read_b32 v237, a115\n" \
"v_mfma_f32_16x16x32_bf16 v[176:179], v[32:35], v[216:219], v[176:179]\n" \
"v_cvt_pk_bf16_f32 v234, v234, v235\n" \
"v_cvt_pk_bf16_f32 v236, v236, v237\n" \
"v_mfma_f32_16x16x32_bf16 v[176:179], v[36:39], v[220:223], v[176:179]\n" \
"s_waitcnt lgkmcnt(1)\n" \
"global_load_lds_dwordx4 v227, s[46:47] offset:3072\n" \
"v_accvgpr_write_b32 a82, v234\n" \
"v_mfma_f32_16x16x32_bf16 v[64:67], v[0:3], v[192:195], v[64:67]\n" \
"s_add_u32 s46, s46, 0x80\n" \
"s_addc_u32 s47, s47, 0\n" \
"ds_read_b128 v[216:219], v230 offset:6144\n" \
"v_mfma_f32_16x16x32_bf16 v[64:67], v[4:7], v[196:199], v[64:67]\n" \
"ds_read_b128 v[220:223], v231 offset:6144\n" \
"v_accvgpr_write_b32 a83, v236\n" \
"v_mfma_f32_16x16x32_bf16 v[68:71], v[8:11], v[192:195], v[68:71]\n" \
"ds_read_b64_tr_b16 v[32:33], v232 offset:1024\n" \
"v_accvgpr_read_b32 v234, a84\n" \
"v_mfma_f32_16x16x32_bf16 v[68:71], v[12:15], v[196:199], v[68:71]\n" \
"buffer_store_dwordx4 a[220:223], v248, s[56:59], s90 offen offset:3072 sc1\n" \
"v_accvgpr_read_b32 v235, a85\n" \
"v_accvgpr_read_b32 v236, a86\n" \
"v_accvgpr_read_b32 v237, a87\n" \
"v_cvt_pk_bf16_f32 v234, v234, v235\n" \
"v_mfma_f32_16x16x32_bf16 v[72:75], v[16:19], v[192:195], v[72:75]\n" \
"ds_read_b64_tr_b16 v[34:35], v232 offset:3072\n" \
"v_cvt_pk_bf16_f32 v236, v236, v237\n" \
"v_mfma_f32_16x16x32_bf16 v[72:75], v[20:23], v[196:199], v[72:75]\n" \
"s_waitcnt lgkmcnt(3)\n" \
"ds_read_b64_tr_b16 v[36:37], v232 offset:9216\n" \
"v_accvgpr_write_b32 a84, v234\n" \
"v_mfma_f32_16x16x32_bf16 v[76:79], v[24:27], v[192:195], v[76:79]\n" \
"ds_read_b64_tr_b16 v[38:39], v232 offset:11264\n" \
"v_accvgpr_write_b32 a85, v236\n" \
"v_mfma_f32_16x16x32_bf16 v[76:79], v[28:31], v[196:199], v[76:79]\n" \
"v_accvgpr_read_b32 v234, a116\n" \
"v_accvgpr_read_b32 v235, a117\n" \
"v_mfma_f32_16x16x32_bf16 v[108:111], v[24:27], v[200:203], v[108:111]\n" \
"ds_read_b64_tr_b16 v[40:41], v233 offset:1024\n" \
"v_accvgpr_read_b32 v236, a118\n" \
"v_mfma_f32_16x16x32_bf16 v[108:111], v[28:31], v[204:207], v[108:111]\n" \
"ds_read_b64_tr_b16 v[42:43], v233 offset:3072\n" \
"v_accvgpr_read_b32 v237, a119\n" \
"v_mfma_f32_16x16x32_bf16 v[104:107], v[16:19], v[200:203], v[104:107]\n" \
"ds_read_b64_tr_b16 v[44:45], v233 offset:9216\n" \
"v_cvt_pk_bf16_f32 v234, v234, v235\n" \
"v_mfma_f32_16x16x32_bf16 v[104:107], v[20:23], v[204:207], v[104:107]\n" \
"v_cvt_pk_bf16_f32 v236, v236, v237\n" \
"v_accvgpr_write_b32 a86, v234\n" \
"v_mfma_f32_16x16x32_bf16 v[100:103], v[8:11], v[200:203], v[100:103]\n" \
"ds_read_b64_tr_b16 v[46:47], v233 offset:11264\n" \
"v_accvgpr_write_b32 a87, v236\n" \
"v_mfma_f32_16x16x32_bf16 v[100:103], v[12:15], v[204:207], v[100:103]\n" \
"ds_read_b64_tr_b16 v[48:49], v232 offset:1536\n" \
"v_accvgpr_read_b32 v234, a88\n" \
"v_mfma_f32_16x16x32_bf16 v[96:99], v[0:3], v[200:203], v[96:99]\n" \
"s_add_u32 m0, s95, 4096\n" \
"ds_read_b64_tr_b16 v[50:51], v232 offset:3584\n" \
"v_accvgpr_read_b32 v235, a89\n" \
"v_mfma_f32_16x16x32_bf16 v[96:99], v[4:7], v[204:207], v[96:99]\n" \
"global_load_lds_dwordx4 v228, s[44:45] offset:-4096\n" \
"v_accvgpr_read_b32 v236, a90\n" \
"v_mfma_f32_16x16x32_bf16 v[128:131], v[0:3], v[208:211], v[128:131]\n" \
"ds_read_b64_tr_b16 v[52:53], v232 offset:9728\n" \
"v_accvgpr_read_b32 v237, a91\n" \
"v_mfma_f32_16x16x32_bf16 v[128:131], v[4:7], v[212:215], v[128:131]\n" \
"ds_read_b64_tr_b16 v[54:55], v232 offset:11776\n" \
"v_cvt_pk_bf16_f32 v234, v234, v235\n" \
"v_mfma_f32_16x16x32_bf16 v[132:135], v[8:11], v[208:211], v[132:135]\n" \
"ds_read_b64_tr_b16 v[56:57], v233 offset:1536\n" \
"v_cvt_pk_bf16_f32 v236, v236, v237\n" \
"v_mfma_f32_16x16x32_bf16 v[132:135], v[12:15], v[212:215], v[132:135]\n" \
"global_load_lds_dwordx4 v229, s[44:45] offset:-3072\n" \
"v_accvgpr_write_b32 a88, v234\n" \
"v_mfma_f32_16x16x32_bf16 v[136:139], v[16:19], v[208:211], v[136:139]\n" \
"s_add_u32 s44, s44, 0x80000\n" \
"s_addc_u32 s45, s45, 0\n" \
"ds_read_b64_tr_b16 v[58:59], v233 offset:3584\n" \
"v_mfma_f32_16x16x32_bf16 v[136:139], v[20:23], v[212:215], v[136:139]\n" \
"ds_read_b64_tr_b16 v[60:61], v233 offset:9728\n" \
"v_accvgpr_write_b32 a89, v236\n" \
"v_mfma_f32_16x16x32_bf16 v[140:143], v[24:27], v[208:211], v[140:143]\n" \
"ds_read_b64_tr_b16 v[62:63], v233 offset:11776\n" \
"v_accvgpr_read_b32 v234, a120\n" \
"v_mfma_f32_16x16x32_bf16 v[140:143], v[28:31], v[212:215], v[140:143]\n" \
"s_waitcnt lgkmcnt(3)\n" \
"global_load_lds_dwordx4 v228, s[42:43] offset:-2048\n" \
"v_accvgpr_read_b32 v235, a121\n" \
"v_mfma_f32_16x16x32_bf16 v[172:175], v[24:27], v[216:219], v[172:175]\n" \
"v_add_u32 v230, 0x8000, v230\n" \
"v_add_u32 v231, 0x8000, v231\n" \
"v_mfma_f32_16x16x32_bf16 v[172:175], v[28:31], v[220:223], v[172:175]\n" \
"v_add_u32 v232, 0x4000, v232\n" \
"v_add_u32 v233, 0x4000, v233\n" \
"v_mfma_f32_16x16x32_bf16 v[168:171], v[16:19], v[216:219], v[168:171]\n" \
"v_accvgpr_read_b32 v236, a122\n" \
"v_accvgpr_read_b32 v237, a123\n" \
"v_mfma_f32_16x16x32_bf16 v[168:171], v[20:23], v[220:223], v[168:171]\n" \
"global_load_lds_dwordx4 v229, s[42:43] offset:-1024\n" \
"v_cvt_pk_bf16_f32 v234, v234, v235\n" \
"v_mfma_f32_16x16x32_bf16 v[164:167], v[8:11], v[216:219], v[164:167]\n" \
"s_add_u32 s42, s42, 0x80000\n" \
"s_addc_u32 s43, s43, 0\n" \
"v_cvt_pk_bf16_f32 v236, v236, v237\n" \
"v_mfma_f32_16x16x32_bf16 v[164:167], v[12:15], v[220:223], v[164:167]\n" \
"s_waitcnt vmcnt(19) lgkmcnt(0)\n" \
"v_accvgpr_write_b32 a90, v234\n" \
"v_accvgpr_write_b32 a91, v236\n" \
"v_mfma_f32_16x16x32_bf16 v[160:163], v[0:3], v[216:219], v[160:163]\n" \
"v_accvgpr_read_b32 v234, a92\n" \
"v_accvgpr_read_b32 v235, a93\n" \
"v_mfma_f32_16x16x32_bf16 v[160:163], v[4:7], v[220:223], v[160:163]\n" \
"s_barrier\n" \
"v_mfma_f32_16x16x32_bf16 v[80:83], v[32:35], v[192:195], v[80:83]\n" \
"ds_read_b64_tr_b16 v[0:1], v232 offset:0\n" \
"v_accvgpr_read_b32 v236, a94\n" \
"v_mfma_f32_16x16x32_bf16 v[80:83], v[36:39], v[196:199], v[80:83]\n" \
"ds_read_b64_tr_b16 v[2:3], v232 offset:2048\n" \
"v_accvgpr_read_b32 v237, a95\n" \
"v_mfma_f32_16x16x32_bf16 v[84:87], v[40:43], v[192:195], v[84:87]\n" \
"s_add_u32 m0, s87, 102400\n" \
"ds_read_b64_tr_b16 v[4:5], v232 offset:8192\n" \
"v_cvt_pk_bf16_f32 v234, v234, v235\n" \
"v_mfma_f32_16x16x32_bf16 v[84:87], v[44:47], v[196:199], v[84:87]\n" \
"global_load_lds_dwordx4 v224, s[48:49] offset:-4096\n" \
"v_cvt_pk_bf16_f32 v236, v236, v237\n" \
"v_mfma_f32_16x16x32_bf16 v[88:91], v[48:51], v[192:195], v[88:91]\n" \
"ds_read_b64_tr_b16 v[6:7], v232 offset:10240\n" \
"v_accvgpr_write_b32 a92, v234\n" \
"v_mfma_f32_16x16x32_bf16 v[88:91], v[52:55], v[196:199], v[88:91]\n" \
"ds_read_b64_tr_b16 v[8:9], v233 offset:0\n" \
"v_accvgpr_write_b32 a93, v236\n" \
"Lpg3308_%=:\n" \
"s_cbranch_execz Lpg5386_%=\n" \
"v_mfma_f32_16x16x32_bf16 v[92:95], v[56:59], v[192:195], v[92:95]\n" \
"ds_read_b64_tr_b16 v[10:11], v233 offset:2048\n" \
"v_accvgpr_read_b32 v234, a124\n" \
"v_mfma_f32_16x16x32_bf16 v[92:95], v[60:63], v[196:199], v[92:95]\n" \
"global_load_lds_dwordx4 v225, s[48:49] offset:-3072\n" \
"v_accvgpr_read_b32 v235, a125\n" \
"v_mfma_f32_16x16x32_bf16 v[124:127], v[56:59], v[200:203], v[124:127]\n" \
"ds_read_b128 v[192:195], v230 offset:0\n" \
"v_accvgpr_read_b32 v236, a126\n" \
"v_mfma_f32_16x16x32_bf16 v[124:127], v[60:63], v[204:207], v[124:127]\n" \
"ds_read_b128 v[196:199], v231 offset:0\n" \
"v_accvgpr_read_b32 v237, a127\n" \
"v_mfma_f32_16x16x32_bf16 v[120:123], v[48:51], v[200:203], v[120:123]\n" \
"ds_read_b64_tr_b16 v[12:13], v233 offset:8192\n" \
"v_cvt_pk_bf16_f32 v234, v234, v235\n" \
"v_mfma_f32_16x16x32_bf16 v[120:123], v[52:55], v[204:207], v[120:123]\n" \
"global_load_lds_dwordx4 v226, s[48:49] offset:-2048\n" \
"v_cvt_pk_bf16_f32 v236, v236, v237\n" \
"v_mfma_f32_16x16x32_bf16 v[116:119], v[40:43], v[200:203], v[116:119]\n" \
"ds_read_b64_tr_b16 v[14:15], v233 offset:10240\n" \
"v_accvgpr_write_b32 a94, v234\n" \
"v_mfma_f32_16x16x32_bf16 v[116:119], v[44:47], v[204:207], v[116:119]\n" \
"ds_read_b64_tr_b16 v[16:17], v232 offset:512\n" \
"v_accvgpr_write_b32 a95, v236\n" \
"v_mfma_f32_16x16x32_bf16 v[112:115], v[32:35], v[200:203], v[112:115]\n" \
"ds_read_b64_tr_b16 v[18:19], v232 offset:2560\n" \
"v_accvgpr_read_b32 v234, a0\n" \
"v_mfma_f32_16x16x32_bf16 v[112:115], v[36:39], v[204:207], v[112:115]\n" \
"global_load_lds_dwordx4 v227, s[48:49] offset:-1024\n" \
"v_accvgpr_read_b32 v235, a1\n" \
"v_mfma_f32_16x16x32_bf16 v[144:147], v[32:35], v[208:211], v[144:147]\n" \
"s_add_u32 s48, s48, 0x80\n" \
"s_addc_u32 s49, s49, 0\n" \
"ds_read_b128 v[200:203], v230 offset:2048\n" \
"v_mfma_f32_16x16x32_bf16 v[144:147], v[36:39], v[212:215], v[144:147]\n" \
"ds_read_b128 v[204:207], v231 offset:2048\n" \
"v_accvgpr_read_b32 v236, a2\n" \
"v_mfma_f32_16x16x32_bf16 v[148:151], v[40:43], v[208:211], v[148:151]\n" \
"ds_read_b64_tr_b16 v[20:21], v232 offset:8704\n" \
"v_accvgpr_read_b32 v237, a3\n" \
"v_mfma_f32_16x16x32_bf16 v[148:151], v[44:47], v[212:215], v[148:151]\n" \
"global_load_lds_dwordx4 v224, s[46:47] offset:0\n" \
"v_mul_f32 v234, s92, v234\n" \
"v_mfma_f32_16x16x32_bf16 v[152:155], v[48:51], v[208:211], v[152:155]\n" \
"ds_read_b64_tr_b16 v[22:23], v232 offset:10752\n" \
"v_mul_f32 v235, s92, v235\n" \
"v_mfma_f32_16x16x32_bf16 v[152:155], v[52:55], v[212:215], v[152:155]\n" \
"ds_read_b64_tr_b16 v[24:25], v233 offset:512\n" \
"v_mul_f32 v236, s92, v236\n" \
"v_mfma_f32_16x16x32_bf16 v[156:159], v[56:59], v[208:211], v[156:159]\n" \
"ds_read_b64_tr_b16 v[26:27], v233 offset:2560\n" \
"v_mul_f32 v237, s92, v237\n" \
"v_mfma_f32_16x16x32_bf16 v[156:159], v[60:63], v[212:215], v[156:159]\n" \
"global_load_lds_dwordx4 v225, s[46:47] offset:1024\n" \
"v_cvt_pk_bf16_f32 v234, v234, v235\n" \
"v_mfma_f32_16x16x32_bf16 v[188:191], v[56:59], v[216:219], v[188:191]\n" \
"ds_read_b128 v[208:211], v230 offset:4096\n" \
"v_cvt_pk_bf16_f32 v236, v236, v237\n" \
"v_mfma_f32_16x16x32_bf16 v[188:191], v[60:63], v[220:223], v[188:191]\n" \
"ds_read_b128 v[212:215], v231 offset:4096\n" \
"v_accvgpr_write_b32 a0, v234\n" \
"v_mfma_f32_16x16x32_bf16 v[184:187], v[48:51], v[216:219], v[184:187]\n" \
"ds_read_b64_tr_b16 v[28:29], v233 offset:8704\n" \
"v_accvgpr_write_b32 a1, v236\n" \
"v_mfma_f32_16x16x32_bf16 v[184:187], v[52:55], v[220:223], v[184:187]\n" \
"global_load_lds_dwordx4 v226, s[46:47] offset:2048\n" \
"v_accvgpr_read_b32 v234, a4\n" \
"v_mfma_f32_16x16x32_bf16 v[180:183], v[40:43], v[216:219], v[180:183]\n" \
"ds_read_b64_tr_b16 v[30:31], v233 offset:10752\n" \
"v_accvgpr_read_b32 v235, a5\n" \
"v_mfma_f32_16x16x32_bf16 v[180:183], v[44:47], v[220:223], v[180:183]\n" \
"v_accvgpr_read_b32 v236, a6\n" \
"v_accvgpr_read_b32 v237, a7\n" \
"v_mfma_f32_16x16x32_bf16 v[176:179], v[32:35], v[216:219], v[176:179]\n" \
"v_mul_f32 v234, s92, v234\n" \
"v_mul_f32 v235, s92, v235\n" \
"v_mfma_f32_16x16x32_bf16 v[176:179], v[36:39], v[220:223], v[176:179]\n" \
"s_waitcnt lgkmcnt(1)\n" \
"global_load_lds_dwordx4 v227, s[46:47] offset:3072\n" \
"v_mul_f32 v236, s92, v236\n" \
"v_mfma_f32_16x16x32_bf16 v[64:67], v[0:3], v[192:195], v[64:67]\n" \
"s_add_u32 s46, s46, 0x80\n" \
"s_addc_u32 s47, s47, 0\n" \
"ds_read_b128 v[216:219], v230 offset:6144\n" \
"v_mfma_f32_16x16x32_bf16 v[64:67], v[4:7], v[196:199], v[64:67]\n" \
"s_add_u32 s90, s88, 139264\n" \
"ds_read_b128 v[220:223], v231 offset:6144\n" \
"v_mul_f32 v237, s92, v237\n" \
"v_mfma_f32_16x16x32_bf16 v[68:71], v[8:11], v[192:195], v[68:71]\n" \
"ds_read_b64_tr_b16 v[32:33], v232 offset:1024\n" \
"v_cvt_pk_bf16_f32 v234, v234, v235\n" \
"v_mfma_f32_16x16x32_bf16 v[68:71], v[12:15], v[196:199], v[68:71]\n" \
"buffer_store_dwordx4 a[128:131], v249, s[56:59], s90 offen offset:0 sc1\n" \
"v_cvt_pk_bf16_f32 v236, v236, v237\n" \
"v_accvgpr_write_b32 a2, v234\n" \
"v_accvgpr_write_b32 a3, v236\n" \
"v_accvgpr_read_b32 v234, a32\n" \
"v_mfma_f32_16x16x32_bf16 v[72:75], v[16:19], v[192:195], v[72:75]\n" \
"ds_read_b64_tr_b16 v[34:35], v232 offset:3072\n" \
"v_accvgpr_read_b32 v235, a33\n" \
"v_mfma_f32_16x16x32_bf16 v[72:75], v[20:23], v[196:199], v[72:75]\n" \
"s_waitcnt lgkmcnt(3)\n" \
"ds_read_b64_tr_b16 v[36:37], v232 offset:9216\n" \
"v_accvgpr_read_b32 v236, a34\n" \
"v_mfma_f32_16x16x32_bf16 v[76:79], v[24:27], v[192:195], v[76:79]\n" \
"ds_read_b64_tr_b16 v[38:39], v232 offset:11264\n" \
"v_accvgpr_read_b32 v237, a35\n" \
"v_mfma_f32_16x16x32_bf16 v[76:79], v[28:31], v[196:199], v[76:79]\n" \
"v_mul_f32 v234, s92, v234\n" \
"v_mul_f32 v235, s92, v235\n" \
"v_mfma_f32_16x16x32_bf16 v[108:111], v[24:27], v[200:203], v[108:111]\n" \
"ds_read_b64_tr_b16 v[40:41], v233 offset:1024\n" \
"v_mul_f32 v236, s92, v236\n" \
"v_mfma_f32_16x16x32_bf16 v[108:111], v[28:31], v[204:207], v[108:111]\n" \
"ds_read_b64_tr_b16 v[42:43], v233 offset:3072\n" \
"v_mul_f32 v237, s92, v237\n" \
"v_mfma_f32_16x16x32_bf16 v[104:107], v[16:19], v[200:203], v[104:107]\n" \
"ds_read_b64_tr_b16 v[44:45], v233 offset:9216\n" \
"v_cvt_pk_bf16_f32 v234, v234, v235\n" \
"v_mfma_f32_16x16x32_bf16 v[104:107], v[20:23], v[204:207], v[104:107]\n" \
"v_cvt_pk_bf16_f32 v236, v236, v237\n" \
"v_accvgpr_write_b32 a32, v234\n" \
"v_mfma_f32_16x16x32_bf16 v[100:103], v[8:11], v[200:203], v[100:103]\n" \
"ds_read_b64_tr_b16 v[46:47], v233 offset:11264\n" \
"v_accvgpr_write_b32 a33, v236\n" \
"v_mfma_f32_16x16x32_bf16 v[100:103], v[12:15], v[204:207], v[100:103]\n" \
"ds_read_b64_tr_b16 v[48:49], v232 offset:1536\n" \
"v_accvgpr_read_b32 v234, a36\n" \
"v_mfma_f32_16x16x32_bf16 v[96:99], v[0:3], v[200:203], v[96:99]\n" \
"s_add_u32 m0, s95, 20480\n" \
"ds_read_b64_tr_b16 v[50:51], v232 offset:3584\n" \
"v_accvgpr_read_b32 v235, a37\n" \
"v_mfma_f32_16x16x32_bf16 v[96:99], v[4:7], v[204:207], v[96:99]\n" \
"global_load_lds_dwordx4 v228, s[44:45] offset:-4096\n" \
"v_accvgpr_read_b32 v236, a38\n" \
"v_mfma_f32_16x16x32_bf16 v[128:131], v[0:3], v[208:211], v[128:131]\n" \
"ds_read_b64_tr_b16 v[52:53], v232 offset:9728\n" \
"v_accvgpr_read_b32 v237, a39\n" \
"v_mfma_f32_16x16x32_bf16 v[128:131], v[4:7], v[212:215], v[128:131]\n" \
"ds_read_b64_tr_b16 v[54:55], v232 offset:11776\n" \
"v_mul_f32 v234, s92, v234\n" \
"v_mfma_f32_16x16x32_bf16 v[132:135], v[8:11], v[208:211], v[132:135]\n" \
"ds_read_b64_tr_b16 v[56:57], v233 offset:1536\n" \
"v_mul_f32 v235, s92, v235\n" \
"v_mfma_f32_16x16x32_bf16 v[132:135], v[12:15], v[212:215], v[132:135]\n" \
"global_load_lds_dwordx4 v229, s[44:45] offset:-3072\n" \
"v_mul_f32 v236, s92, v236\n" \
"v_mfma_f32_16x16x32_bf16 v[136:139], v[16:19], v[208:211], v[136:139]\n" \
"s_add_u32 s44, s44, 0x80000\n" \
"s_addc_u32 s45, s45, 0\n" \
"ds_read_b64_tr_b16 v[58:59], v233 offset:3584\n" \
"v_mfma_f32_16x16x32_bf16 v[136:139], v[20:23], v[212:215], v[136:139]\n" \
"ds_read_b64_tr_b16 v[60:61], v233 offset:9728\n" \
"v_mul_f32 v237, s92, v237\n" \
"v_mfma_f32_16x16x32_bf16 v[140:143], v[24:27], v[208:211], v[140:143]\n" \
"ds_read_b64_tr_b16 v[62:63], v233 offset:11776\n" \
"v_cvt_pk_bf16_f32 v234, v234, v235\n" \
"v_mfma_f32_16x16x32_bf16 v[140:143], v[28:31], v[212:215], v[140:143]\n" \
"s_waitcnt lgkmcnt(3)\n" \
"global_load_lds_dwordx4 v228, s[42:43] offset:-2048\n" \
"v_cvt_pk_bf16_f32 v236, v236, v237\n" \
"v_mfma_f32_16x16x32_bf16 v[172:175], v[24:27], v[216:219], v[172:175]\n" \
"v_add_u32 v230, 0xffff0000, v230\n" \
"v_add_u32 v231, 0xffff0000, v231\n" \
"v_mfma_f32_16x16x32_bf16 v[172:175], v[28:31], v[220:223], v[172:175]\n" \
"v_add_u32 v232, 0x4000, v232\n" \
"v_add_u32 v233, 0x4000, v233\n" \
"v_mfma_f32_16x16x32_bf16 v[168:171], v[16:19], v[216:219], v[168:171]\n" \
"v_accvgpr_write_b32 a34, v234\n" \
"v_accvgpr_write_b32 a35, v236\n" \
"v_mfma_f32_16x16x32_bf16 v[168:171], v[20:23], v[220:223], v[168:171]\n" \
"global_load_lds_dwordx4 v229, s[42:43] offset:-1024\n" \
"v_accvgpr_read_b32 v234, a8\n" \
"v_mfma_f32_16x16x32_bf16 v[164:167], v[8:11], v[216:219], v[164:167]\n" \
"s_add_u32 s42, s42, 0x80000\n" \
"s_addc_u32 s43, s43, 0\n" \
"v_accvgpr_read_b32 v235, a9\n" \
"v_mfma_f32_16x16x32_bf16 v[164:167], v[12:15], v[220:223], v[164:167]\n" \
"s_waitcnt vmcnt(18) lgkmcnt(0)\n" \
"v_accvgpr_read_b32 v236, a10\n" \
"v_accvgpr_read_b32 v237, a11\n" \
"v_mfma_f32_16x16x32_bf16 v[160:163], v[0:3], v[216:219], v[160:163]\n" \
"v_mul_f32 v234, s92, v234\n" \
"v_mul_f32 v235, s92, v235\n" \
"v_mfma_f32_16x16x32_bf16 v[160:163], v[4:7], v[220:223], v[160:163]\n" \
"s_barrier\n" \
"v_mfma_f32_16x16x32_bf16 v[80:83], v[32:35], v[192:195], v[80:83]\n" \
"ds_read_b64_tr_b16 v[0:1], v232 offset:0\n" \
"v_mul_f32 v236, s92, v236\n" \
"v_mfma_f32_16x16x32_bf16 v[80:83], v[36:39], v[196:199], v[80:83]\n" \
"ds_read_b64_tr_b16 v[2:3], v232 offset:2048\n" \
"v_mul_f32 v237, s92, v237\n" \
"v_mfma_f32_16x16x32_bf16 v[84:87], v[40:43], v[192:195], v[84:87]\n" \
"s_add_u32 m0, s87, 135168\n" \
"ds_read_b64_tr_b16 v[4:5], v232 offset:8192\n" \
"v_cvt_pk_bf16_f32 v234, v234, v235\n" \
"v_mfma_f32_16x16x32_bf16 v[84:87], v[44:47], v[196:199], v[84:87]\n" \
"global_load_lds_dwordx4 v224, s[48:49] offset:-4096\n" \
"v_cvt_pk_bf16_f32 v236, v236, v237\n" \
"v_mfma_f32_16x16x32_bf16 v[88:91], v[48:51], v[192:195], v[88:91]\n" \
"ds_read_b64_tr_b16 v[6:7], v232 offset:10240\n" \
"v_accvgpr_write_b32 a8, v234\n" \
"v_mfma_f32_16x16x32_bf16 v[88:91], v[52:55], v[196:199], v[88:91]\n" \
"ds_read_b64_tr_b16 v[8:9], v233 offset:0\n" \
"v_accvgpr_write_b32 a9, v236\n" \
"v_mfma_f32_16x16x32_bf16 v[92:95], v[56:59], v[192:195], v[92:95]\n" \
"ds_read_b64_tr_b16 v[10:11], v233 offset:2048\n" \
"v_accvgpr_read_b32 v234, a12\n" \
"v_mfma_f32_16x16x32_bf16 v[92:95], v[60:63], v[196:199], v[92:95]\n" \
"global_load_lds_dwordx4 v225, s[48:49] offset:-3072\n" \
"v_accvgpr_read_b32 v235, a13\n" \
"v_mfma_f32_16x16x32_bf16 v[124:127], v[56:59], v[200:203], v[124:127]\n" \
"ds_read_b128 v[192:195], v230 offset:0\n" \
"v_accvgpr_read_b32 v236, a14\n" \
"v_mfma_f32_16x16x32_bf16 v[124:127], v[60:63], v[204:207], v[124:127]\n" \
"ds_read_b128 v[196:199], v231 offset:0\n" \
"v_accvgpr_read_b32 v237, a15\n" \
"v_mfma_f32_16x16x32_bf16 v[120:123], v[48:51], v[200:203], v[120:123]\n" \
"ds_read_b64_tr_b16 v[12:13], v233 offset:8192\n" \
"v_mul_f32 v234, s92, v234\n" \
"v_mfma_f32_16x16x32_bf16 v[120:123], v[52:55], v[204:207], v[120:123]\n" \
"global_load_lds_dwordx4 v226, s[48:49] offset:-2048\n" \
"v_mul_f32 v235, s92, v235\n" \
"v_mfma_f32_16x16x32_bf16 v[116:119], v[40:43], v[200:203], v[116:119]\n" \
"ds_read_b64_tr_b16 v[14:15], v233 offset:10240\n" \
"v_mul_f32 v236, s92, v236\n" \
"v_mfma_f32_16x16x32_bf16 v[116:119], v[44:47], v[204:207], v[116:119]\n" \
"ds_read_b64_tr_b16 v[16:17], v232 offset:512\n" \
"v_mul_f32 v237, s92, v237\n" \
"v_mfma_f32_16x16x32_bf16 v[112:115], v[32:35], v[200:203], v[112:115]\n" \
"ds_read_b64_tr_b16 v[18:19], v232 offset:2560\n" \
"v_cvt_pk_bf16_f32 v234, v234, v235\n" \
"v_mfma_f32_16x16x32_bf16 v[112:115], v[36:39], v[204:207], v[112:115]\n" \
"global_load_lds_dwordx4 v227, s[48:49] offset:-1024\n" \
"v_cvt_pk_bf16_f32 v236, v236, v237\n" \
"v_mfma_f32_16x16x32_bf16 v[144:147], v[32:35], v[208:211], v[144:147]\n" \
"s_add_u32 s48, s48, 0x80\n" \
"s_addc_u32 s49, s49, 0\n" \
"ds_read_b128 v[200:203], v230 offset:2048\n" \
"v_mfma_f32_16x16x32_bf16 v[144:147], v[36:39], v[212:215], v[144:147]\n" \
"ds_read_b128 v[204:207], v231 offset:2048\n" \
"v_accvgpr_write_b32 a10, v234\n" \
"v_mfma_f32_16x16x32_bf16 v[148:151], v[40:43], v[208:211], v[148:151]\n" \
"ds_read_b64_tr_b16 v[20:21], v232 offset:8704\n" \
"v_accvgpr_write_b32 a11, v236\n" \
"v_mfma_f32_16x16x32_bf16 v[148:151], v[44:47], v[212:215], v[148:151]\n" \
"global_load_lds_dwordx4 v224, s[46:47] offset:0\n" \
"v_accvgpr_read_b32 v234, a40\n" \
"v_mfma_f32_16x16x32_bf16 v[152:155], v[48:51], v[208:211], v[152:155]\n" \
"ds_read_b64_tr_b16 v[22:23], v232 offset:10752\n" \
"v_accvgpr_read_b32 v235, a41\n" \
"v_mfma_f32_16x16x32_bf16 v[152:155], v[52:55], v[212:215], v[152:155]\n" \
"ds_read_b64_tr_b16 v[24:25], v233 offset:512\n" \
"v_accvgpr_read_b32 v236, a42\n" \
"v_mfma_f32_16x16x32_bf16 v[156:159], v[56:59], v[208:211], v[156:159]\n" \
"ds_read_b64_tr_b16 v[26:27], v233 offset:2560\n" \
"v_accvgpr_read_b32 v237, a43\n" \
"v_mfma_f32_16x16x32_bf16 v[156:159], v[60:63], v[212:215], v[156:159]\n" \
"global_load_lds_dwordx4 v225, s[46:47] offset:1024\n" \
"v_mul_f32 v234, s92, v234\n" \
"v_mfma_f32_16x16x32_bf16 v[188:191], v[56:59], v[216:219], v[188:191]\n" \
"ds_read_b128 v[208:211], v230 offset:4096\n" \
"v_mul_f32 v235, s92, v235\n" \
"v_mfma_f32_16x16x32_bf16 v[188:191], v[60:63], v[220:223], v[188:191]\n" \
"ds_read_b128 v[212:215], v231 offset:4096\n" \
"v_mul_f32 v236, s92, v236\n" \
"v_mfma_f32_16x16x32_bf16 v[184:187], v[48:51], v[216:219], v[184:187]\n" \
"ds_read_b64_tr_b16 v[28:29], v233 offset:8704\n" \
"v_mul_f32 v237, s92, v237\n" \
"v_mfma_f32_16x16x32_bf16 v[184:187], v[52:55], v[220:223], v[184:187]\n" \
"global_load_lds_dwordx4 v226, s[46:47] offset:2048\n" \
"v_cvt_pk_bf16_f32 v234, v234, v235\n" \
"v_mfma_f32_16x16x32_bf16 v[180:183], v[40:43], v[216:219], v[180:183]\n" \
"ds_read_b64_tr_b16 v[30:31], v233 offset:10752\n" \
"v_cvt_pk_bf16_f32 v236, v236, v237\n" \
"v_mfma_f32_16x16x32_bf16 v[180:183], v[44:47], v[220:223], v[180:183]\n" \
"v_accvgpr_write_b32 a40, v234\n" \
"v_accvgpr_write_b32 a41, v236\n" \
"v_mfma_f32_16x16x32_bf16 v[176:179], v[32:35], v[216:219], v[176:179]\n" \
"v_accvgpr_read_b32 v234, a44\n" \
"v_accvgpr_read_b32 v235, a45\n" \
"v_mfma_f32_16x16x32_bf16 v[176:179], v[36:39], v[220:223], v[176:179]\n" \
"s_waitcnt lgkmcnt(1)\n" \
"global_load_lds_dwordx4 v227, s[46:47] offset:3072\n" \
"v_accvgpr_read_b32 v236, a46\n" \
"v_mfma_f32_16x16x32_bf16 v[64:67], v[0:3], v[192:195], v[64:67]\n" \
"s_add_u32 s46, s46, 0x80\n" \
"s_addc_u32 s47, s47, 0\n" \
"ds_read_b128 v[216:219], v230 offset:6144\n" \
"v_mfma_f32_16x16x32_bf16 v[64:67], v[4:7], v[196:199], v[64:67]\n" \
"ds_read_b128 v[220:223], v231 offset:6144\n" \
"v_accvgpr_read_b32 v237, a47\n" \
"v_mfma_f32_16x16x32_bf16 v[68:71], v[8:11], v[192:195], v[68:71]\n" \
"ds_read_b64_tr_b16 v[32:33], v232 offset:1024\n" \
"v_mul_f32 v234, s92, v234\n" \
"v_mfma_f32_16x16x32_bf16 v[68:71], v[12:15], v[196:199], v[68:71]\n" \
"buffer_store_dwordx4 a[160:163], v249, s[56:59], s90 offen offset:256 sc1\n" \
"v_mul_f32 v235, s92, v235\n" \
"v_mul_f32 v236, s92, v236\n" \
"v_mul_f32 v237, s92, v237\n" \
"v_cvt_pk_bf16_f32 v234, v234, v235\n" \
"v_mfma_f32_16x16x32_bf16 v[72:75], v[16:19], v[192:195], v[72:75]\n" \
"ds_read_b64_tr_b16 v[34:35], v232 offset:3072\n" \
"v_cvt_pk_bf16_f32 v236, v236, v237\n" \
"v_mfma_f32_16x16x32_bf16 v[72:75], v[20:23], v[196:199], v[72:75]\n" \
"s_waitcnt lgkmcnt(3)\n" \
"ds_read_b64_tr_b16 v[36:37], v232 offset:9216\n" \
"v_accvgpr_write_b32 a42, v234\n" \
"v_mfma_f32_16x16x32_bf16 v[76:79], v[24:27], v[192:195], v[76:79]\n" \
"ds_read_b64_tr_b16 v[38:39], v232 offset:11264\n" \
"v_accvgpr_write_b32 a43, v236\n" \
"v_mfma_f32_16x16x32_bf16 v[76:79], v[28:31], v[196:199], v[76:79]\n" \
"v_accvgpr_read_b32 v234, a16\n" \
"v_accvgpr_read_b32 v235, a17\n" \
"v_mfma_f32_16x16x32_bf16 v[108:111], v[24:27], v[200:203], v[108:111]\n" \
"ds_read_b64_tr_b16 v[40:41], v233 offset:1024\n" \
"v_accvgpr_read_b32 v236, a18\n" \
"v_mfma_f32_16x16x32_bf16 v[108:111], v[28:31], v[204:207], v[108:111]\n" \
"ds_read_b64_tr_b16 v[42:43], v233 offset:3072\n" \
"v_accvgpr_read_b32 v237, a19\n" \
"v_mfma_f32_16x16x32_bf16 v[104:107], v[16:19], v[200:203], v[104:107]\n" \
"ds_read_b64_tr_b16 v[44:45], v233 offset:9216\n" \
"v_cvt_pk_bf16_f32 v234, v234, v235\n" \
"v_mfma_f32_16x16x32_bf16 v[104:107], v[20:23], v[204:207], v[104:107]\n" \
"v_cvt_pk_bf16_f32 v236, v236, v237\n" \
"v_accvgpr_write_b32 a16, v234\n" \
"v_mfma_f32_16x16x32_bf16 v[100:103], v[8:11], v[200:203], v[100:103]\n" \
"ds_read_b64_tr_b16 v[46:47], v233 offset:11264\n" \
"v_accvgpr_write_b32 a17, v236\n" \
"v_mfma_f32_16x16x32_bf16 v[100:103], v[12:15], v[204:207], v[100:103]\n" \
"ds_read_b64_tr_b16 v[48:49], v232 offset:1536\n" \
"v_accvgpr_read_b32 v234, a48\n" \
"v_mfma_f32_16x16x32_bf16 v[96:99], v[0:3], v[200:203], v[96:99]\n" \
"s_add_u32 m0, s95, 36864\n" \
"ds_read_b64_tr_b16 v[50:51], v232 offset:3584\n" \
"v_accvgpr_read_b32 v235, a49\n" \
"v_mfma_f32_16x16x32_bf16 v[96:99], v[4:7], v[204:207], v[96:99]\n" \
"global_load_lds_dwordx4 v228, s[44:45] offset:-4096\n" \
"v_accvgpr_read_b32 v236, a50\n" \
"v_mfma_f32_16x16x32_bf16 v[128:131], v[0:3], v[208:211], v[128:131]\n" \
"ds_read_b64_tr_b16 v[52:53], v232 offset:9728\n" \
"v_accvgpr_read_b32 v237, a51\n" \
"v_mfma_f32_16x16x32_bf16 v[128:131], v[4:7], v[212:215], v[128:131]\n" \
"ds_read_b64_tr_b16 v[54:55], v232 offset:11776\n" \
"v_cvt_pk_bf16_f32 v234, v234, v235\n" \
"v_mfma_f32_16x16x32_bf16 v[132:135], v[8:11], v[208:211], v[132:135]\n" \
"ds_read_b64_tr_b16 v[56:57], v233 offset:1536\n" \
"v_cvt_pk_bf16_f32 v236, v236, v237\n" \
"v_mfma_f32_16x16x32_bf16 v[132:135], v[12:15], v[212:215], v[132:135]\n" \
"global_load_lds_dwordx4 v229, s[44:45] offset:-3072\n" \
"v_accvgpr_write_b32 a18, v234\n" \
"v_mfma_f32_16x16x32_bf16 v[136:139], v[16:19], v[208:211], v[136:139]\n" \
"s_add_u32 s44, s44, 0x80000\n" \
"s_addc_u32 s45, s45, 0\n" \
"ds_read_b64_tr_b16 v[58:59], v233 offset:3584\n" \
"v_mfma_f32_16x16x32_bf16 v[136:139], v[20:23], v[212:215], v[136:139]\n" \
"ds_read_b64_tr_b16 v[60:61], v233 offset:9728\n" \
"v_accvgpr_write_b32 a19, v236\n" \
"v_mfma_f32_16x16x32_bf16 v[140:143], v[24:27], v[208:211], v[140:143]\n" \
"ds_read_b64_tr_b16 v[62:63], v233 offset:11776\n" \
"v_accvgpr_read_b32 v234, a20\n" \
"v_mfma_f32_16x16x32_bf16 v[140:143], v[28:31], v[212:215], v[140:143]\n" \
"s_waitcnt lgkmcnt(3)\n" \
"global_load_lds_dwordx4 v228, s[42:43] offset:-2048\n" \
"v_accvgpr_read_b32 v235, a21\n" \
"v_mfma_f32_16x16x32_bf16 v[172:175], v[24:27], v[216:219], v[172:175]\n" \
"v_add_u32 v230, 0x8000, v230\n" \
"v_add_u32 v231, 0x8000, v231\n" \
"v_mfma_f32_16x16x32_bf16 v[172:175], v[28:31], v[220:223], v[172:175]\n" \
"v_add_u32 v232, 0xffff4000, v232\n" \
"v_add_u32 v233, 0xffff4000, v233\n" \
"v_mfma_f32_16x16x32_bf16 v[168:171], v[16:19], v[216:219], v[168:171]\n" \
"v_accvgpr_read_b32 v236, a22\n" \
"v_accvgpr_read_b32 v237, a23\n" \
"v_mfma_f32_16x16x32_bf16 v[168:171], v[20:23], v[220:223], v[168:171]\n" \
"global_load_lds_dwordx4 v229, s[42:43] offset:-1024\n" \
"v_cvt_pk_bf16_f32 v234, v234, v235\n" \
"v_mfma_f32_16x16x32_bf16 v[164:167], v[8:11], v[216:219], v[164:167]\n" \
"s_add_u32 s42, s42, 0x80000\n" \
"s_addc_u32 s43, s43, 0\n" \
"v_cvt_pk_bf16_f32 v236, v236, v237\n" \
"v_mfma_f32_16x16x32_bf16 v[164:167], v[12:15], v[220:223], v[164:167]\n" \
"s_waitcnt vmcnt(18) lgkmcnt(0)\n" \
"v_accvgpr_write_b32 a20, v234\n" \
"v_accvgpr_write_b32 a21, v236\n" \
"v_mfma_f32_16x16x32_bf16 v[160:163], v[0:3], v[216:219], v[160:163]\n" \
"v_accvgpr_read_b32 v234, a52\n" \
"v_accvgpr_read_b32 v235, a53\n" \
"v_mfma_f32_16x16x32_bf16 v[160:163], v[4:7], v[220:223], v[160:163]\n" \
"s_barrier\n" \
"v_mfma_f32_16x16x32_bf16 v[80:83], v[32:35], v[192:195], v[80:83]\n" \
"ds_read_b64_tr_b16 v[0:1], v232 offset:0\n" \
"v_accvgpr_read_b32 v236, a54\n" \
"v_mfma_f32_16x16x32_bf16 v[80:83], v[36:39], v[196:199], v[80:83]\n" \
"ds_read_b64_tr_b16 v[2:3], v232 offset:2048\n" \
"v_accvgpr_read_b32 v237, a55\n" \
"v_mfma_f32_16x16x32_bf16 v[84:87], v[40:43], v[192:195], v[84:87]\n" \
"s_add_u32 m0, s87, 69632\n" \
"ds_read_b64_tr_b16 v[4:5], v232 offset:8192\n" \
"v_cvt_pk_bf16_f32 v234, v234, v235\n" \
"v_mfma_f32_16x16x32_bf16 v[84:87], v[44:47], v[196:199], v[84:87]\n" \
"global_load_lds_dwordx4 v224, s[48:49] offset:-4096\n" \
"v_cvt_pk_bf16_f32 v236, v236, v237\n" \
"v_mfma_f32_16x16x32_bf16 v[88:91], v[48:51], v[192:195], v[88:91]\n" \
"ds_read_b64_tr_b16 v[6:7], v232 offset:10240\n" \
"v_accvgpr_write_b32 a22, v234\n" \
"v_mfma_f32_16x16x32_bf16 v[88:91], v[52:55], v[196:199], v[88:91]\n" \
"ds_read_b64_tr_b16 v[8:9], v233 offset:0\n" \
"v_accvgpr_write_b32 a23, v236\n" \
"v_mfma_f32_16x16x32_bf16 v[92:95], v[56:59], v[192:195], v[92:95]\n" \
"ds_read_b64_tr_b16 v[10:11], v233 offset:2048\n" \
"v_accvgpr_read_b32 v234, a24\n" \
"v_mfma_f32_16x16x32_bf16 v[92:95], v[60:63], v[196:199], v[92:95]\n" \
"global_load_lds_dwordx4 v225, s[48:49] offset:-3072\n" \
"v_accvgpr_read_b32 v235, a25\n" \
"v_mfma_f32_16x16x32_bf16 v[124:127], v[56:59], v[200:203], v[124:127]\n" \
"ds_read_b128 v[192:195], v230 offset:0\n" \
"v_accvgpr_read_b32 v236, a26\n" \
"v_mfma_f32_16x16x32_bf16 v[124:127], v[60:63], v[204:207], v[124:127]\n" \
"ds_read_b128 v[196:199], v231 offset:0\n" \
"v_accvgpr_read_b32 v237, a27\n" \
"v_mfma_f32_16x16x32_bf16 v[120:123], v[48:51], v[200:203], v[120:123]\n" \
"ds_read_b64_tr_b16 v[12:13], v233 offset:8192\n" \
"v_cvt_pk_bf16_f32 v234, v234, v235\n" \
"v_mfma_f32_16x16x32_bf16 v[120:123], v[52:55], v[204:207], v[120:123]\n" \
"global_load_lds_dwordx4 v226, s[48:49] offset:-2048\n" \
"v_cvt_pk_bf16_f32 v236, v236, v237\n" \
"v_mfma_f32_16x16x32_bf16 v[116:119], v[40:43], v[200:203], v[116:119]\n" \
"ds_read_b64_tr_b16 v[14:15], v233 offset:10240\n" \
"v_accvgpr_write_b32 a24, v234\n" \
"v_mfma_f32_16x16x32_bf16 v[116:119], v[44:47], v[204:207], v[116:119]\n" \
"ds_read_b64_tr_b16 v[16:17], v232 offset:512\n" \
"v_accvgpr_write_b32 a25, v236\n" \
"v_mfma_f32_16x16x32_bf16 v[112:115], v[32:35], v[200:203], v[112:115]\n" \
"ds_read_b64_tr_b16 v[18:19], v232 offset:2560\n" \
"v_accvgpr_read_b32 v234, a56\n" \
"v_mfma_f32_16x16x32_bf16 v[112:115], v[36:39], v[204:207], v[112:115]\n" \
"global_load_lds_dwordx4 v227, s[48:49] offset:-1024\n" \
"v_accvgpr_read_b32 v235, a57\n" \
"v_mfma_f32_16x16x32_bf16 v[144:147], v[32:35], v[208:211], v[144:147]\n" \
"s_add_u32 s48, s48, 0x80\n" \
"s_addc_u32 s49, s49, 0\n" \
"ds_read_b128 v[200:203], v230 offset:2048\n" \
"v_mfma_f32_16x16x32_bf16 v[144:147], v[36:39], v[212:215], v[144:147]\n" \
"ds_read_b128 v[204:207], v231 offset:2048\n" \
"v_accvgpr_read_b32 v236, a58\n" \
"v_mfma_f32_16x16x32_bf16 v[148:151], v[40:43], v[208:211], v[148:151]\n" \
"ds_read_b64_tr_b16 v[20:21], v232 offset:8704\n" \
"v_accvgpr_read_b32 v237, a59\n" \
"v_mfma_f32_16x16x32_bf16 v[148:151], v[44:47], v[212:215], v[148:151]\n" \
"global_load_lds_dwordx4 v224, s[46:47] offset:0\n" \
"v_cvt_pk_bf16_f32 v234, v234, v235\n" \
"v_mfma_f32_16x16x32_bf16 v[152:155], v[48:51], v[208:211], v[152:155]\n" \
"ds_read_b64_tr_b16 v[22:23], v232 offset:10752\n" \
"v_cvt_pk_bf16_f32 v236, v236, v237\n" \
"v_mfma_f32_16x16x32_bf16 v[152:155], v[52:55], v[212:215], v[152:155]\n" \
"ds_read_b64_tr_b16 v[24:25], v233 offset:512\n" \
"v_accvgpr_write_b32 a26, v234\n" \
"v_mfma_f32_16x16x32_bf16 v[156:159], v[56:59], v[208:211], v[156:159]\n" \
"ds_read_b64_tr_b16 v[26:27], v233 offset:2560\n" \
"v_accvgpr_write_b32 a27, v236\n" \
"v_mfma_f32_16x16x32_bf16 v[156:159], v[60:63], v[212:215], v[156:159]\n" \
"global_load_lds_dwordx4 v225, s[46:47] offset:1024\n" \
"v_accvgpr_read_b32 v234, a28\n" \
"v_mfma_f32_16x16x32_bf16 v[188:191], v[56:59], v[216:219], v[188:191]\n" \
"ds_read_b128 v[208:211], v230 offset:4096\n" \
"v_accvgpr_read_b32 v235, a29\n" \
"v_mfma_f32_16x16x32_bf16 v[188:191], v[60:63], v[220:223], v[188:191]\n" \
"ds_read_b128 v[212:215], v231 offset:4096\n" \
"v_accvgpr_read_b32 v236, a30\n" \
"v_mfma_f32_16x16x32_bf16 v[184:187], v[48:51], v[216:219], v[184:187]\n" \
"ds_read_b64_tr_b16 v[28:29], v233 offset:8704\n" \
"v_accvgpr_read_b32 v237, a31\n" \
"v_mfma_f32_16x16x32_bf16 v[184:187], v[52:55], v[220:223], v[184:187]\n" \
"global_load_lds_dwordx4 v226, s[46:47] offset:2048\n" \
"v_cvt_pk_bf16_f32 v234, v234, v235\n" \
"v_mfma_f32_16x16x32_bf16 v[180:183], v[40:43], v[216:219], v[180:183]\n" \
"ds_read_b64_tr_b16 v[30:31], v233 offset:10752\n" \
"v_cvt_pk_bf16_f32 v236, v236, v237\n" \
"v_mfma_f32_16x16x32_bf16 v[180:183], v[44:47], v[220:223], v[180:183]\n" \
"v_accvgpr_write_b32 a28, v234\n" \
"v_accvgpr_write_b32 a29, v236\n" \
"v_mfma_f32_16x16x32_bf16 v[176:179], v[32:35], v[216:219], v[176:179]\n" \
"v_accvgpr_read_b32 v234, a60\n" \
"v_accvgpr_read_b32 v235, a61\n" \
"v_mfma_f32_16x16x32_bf16 v[176:179], v[36:39], v[220:223], v[176:179]\n" \
"s_waitcnt lgkmcnt(1)\n" \
"global_load_lds_dwordx4 v227, s[46:47] offset:3072\n" \
"v_accvgpr_read_b32 v236, a62\n" \
"v_mfma_f32_16x16x32_bf16 v[64:67], v[0:3], v[192:195], v[64:67]\n" \
"s_add_u32 s46, s46, 0x80\n" \
"s_addc_u32 s47, s47, 0\n" \
"ds_read_b128 v[216:219], v230 offset:6144\n" \
"v_mfma_f32_16x16x32_bf16 v[64:67], v[4:7], v[196:199], v[64:67]\n" \
"ds_read_b128 v[220:223], v231 offset:6144\n" \
"v_accvgpr_read_b32 v237, a63\n" \
"v_mfma_f32_16x16x32_bf16 v[68:71], v[8:11], v[192:195], v[68:71]\n" \
"ds_read_b64_tr_b16 v[32:33], v232 offset:1024\n" \
"v_cvt_pk_bf16_f32 v234, v234, v235\n" \
"v_mfma_f32_16x16x32_bf16 v[68:71], v[12:15], v[196:199], v[68:71]\n" \
"buffer_store_dwordx4 a[136:139], v249, s[56:59], s90 offen offset:2048 sc1\n" \
"v_cvt_pk_bf16_f32 v236, v236, v237\n" \
"v_accvgpr_write_b32 a30, v234\n" \
"v_accvgpr_write_b32 a31, v236\n" \
"v_mfma_f32_16x16x32_bf16 v[72:75], v[16:19], v[192:195], v[72:75]\n" \
"ds_read_b64_tr_b16 v[34:35], v232 offset:3072\n" \
"v_mfma_f32_16x16x32_bf16 v[72:75], v[20:23], v[196:199], v[72:75]\n" \
"s_waitcnt lgkmcnt(3)\n" \
"ds_read_b64_tr_b16 v[36:37], v232 offset:9216\n" \
"v_mfma_f32_16x16x32_bf16 v[76:79], v[24:27], v[192:195], v[76:79]\n" \
"ds_read_b64_tr_b16 v[38:39], v232 offset:11264\n" \
"v_mfma_f32_16x16x32_bf16 v[76:79], v[28:31], v[196:199], v[76:79]\n" \
"buffer_store_dwordx4 a[168:171], v249, s[56:59], s90 offen offset:2304 sc1\n" \
"v_mfma_f32_16x16x32_bf16 v[108:111], v[24:27], v[200:203], v[108:111]\n" \
"s_lshl_b32 s70, s63, 2\n" \
"ds_read_b64_tr_b16 v[40:41], v233 offset:1024\n" \
"v_mfma_f32_16x16x32_bf16 v[108:111], v[28:31], v[204:207], v[108:111]\n" \
"s_add_u32 s70, s70, s91\n" \
"ds_read_b64_tr_b16 v[42:43], v233 offset:3072\n" \
"v_mov_b32 v238, s60\n" \
"v_mfma_f32_16x16x32_bf16 v[104:107], v[16:19], v[200:203], v[104:107]\n" \
"ds_read_b64_tr_b16 v[44:45], v233 offset:9216\n" \
"v_lshlrev_b32 v239, 28, s61\n" \
"v_mfma_f32_16x16x32_bf16 v[104:107], v[20:23], v[204:207], v[104:107]\n" \
"buffer_store_dword v238, v239, s[56:59], s70 offen sc1\n" \
"v_mfma_f32_16x16x32_bf16 v[100:103], v[8:11], v[200:203], v[100:103]\n" \
"ds_read_b64_tr_b16 v[46:47], v233 offset:11264\n" \
"v_mfma_f32_16x16x32_bf16 v[100:103], v[12:15], v[204:207], v[100:103]\n" \
"ds_read_b64_tr_b16 v[48:49], v232 offset:1536\n" \
"v_mfma_f32_16x16x32_bf16 v[96:99], v[0:3], v[200:203], v[96:99]\n" \
"s_add_u32 m0, s95, 53248\n" \
"ds_read_b64_tr_b16 v[50:51], v232 offset:3584\n" \
"v_mfma_f32_16x16x32_bf16 v[96:99], v[4:7], v[204:207], v[96:99]\n" \
"global_load_lds_dwordx4 v228, s[44:45] offset:-4096\n" \
"v_mfma_f32_16x16x32_bf16 v[128:131], v[0:3], v[208:211], v[128:131]\n" \
"ds_read_b64_tr_b16 v[52:53], v232 offset:9728\n" \
"v_mfma_f32_16x16x32_bf16 v[128:131], v[4:7], v[212:215], v[128:131]\n" \
"ds_read_b64_tr_b16 v[54:55], v232 offset:11776\n" \
"v_mfma_f32_16x16x32_bf16 v[132:135], v[8:11], v[208:211], v[132:135]\n" \
"ds_read_b64_tr_b16 v[56:57], v233 offset:1536\n" \
"Lpg3849_%=:\n" \
"s_cbranch_execz Lpg5534_%=\n" \
"v_mfma_f32_16x16x32_bf16 v[132:135], v[12:15], v[212:215], v[132:135]\n" \
"global_load_lds_dwordx4 v229, s[44:45] offset:-3072\n" \
"v_mfma_f32_16x16x32_bf16 v[136:139], v[16:19], v[208:211], v[136:139]\n" \
"s_add_u32 s44, s44, 0x100000\n" \
"s_addc_u32 s45, s45, 0\n" \
"ds_read_b64_tr_b16 v[58:59], v233 offset:3584\n" \
"v_mfma_f32_16x16x32_bf16 v[136:139], v[20:23], v[212:215], v[136:139]\n" \
"ds_read_b64_tr_b16 v[60:61], v233 offset:9728\n" \
"v_mfma_f32_16x16x32_bf16 v[140:143], v[24:27], v[208:211], v[140:143]\n" \
"ds_read_b64_tr_b16 v[62:63], v233 offset:11776\n" \
"v_mfma_f32_16x16x32_bf16 v[140:143], v[28:31], v[212:215], v[140:143]\n" \
"s_waitcnt lgkmcnt(3)\n" \
"global_load_lds_dwordx4 v228, s[42:43] offset:-2048\n" \
"v_mfma_f32_16x16x32_bf16 v[172:175], v[24:27], v[216:219], v[172:175]\n" \
"v_add_u32 v230, 0x8000, v230\n" \
"v_add_u32 v231, 0x8000, v231\n" \
"v_mfma_f32_16x16x32_bf16 v[172:175], v[28:31], v[220:223], v[172:175]\n" \
"v_add_u32 v232, 0x4000, v232\n" \
"v_add_u32 v233, 0x4000, v233\n" \
"v_mfma_f32_16x16x32_bf16 v[168:171], v[16:19], v[216:219], v[168:171]\n" \
"v_mfma_f32_16x16x32_bf16 v[168:171], v[20:23], v[220:223], v[168:171]\n" \
"global_load_lds_dwordx4 v229, s[42:43] offset:-1024\n" \
"v_mfma_f32_16x16x32_bf16 v[164:167], v[8:11], v[216:219], v[164:167]\n" \
"s_add_u32 s42, s42, 0x100000\n" \
"s_addc_u32 s43, s43, 0\n" \
"v_mfma_f32_16x16x32_bf16 v[164:167], v[12:15], v[220:223], v[164:167]\n" \
"s_waitcnt vmcnt(20) lgkmcnt(0)\n" \
"v_mfma_f32_16x16x32_bf16 v[160:163], v[0:3], v[216:219], v[160:163]\n" \
"v_mfma_f32_16x16x32_bf16 v[160:163], v[4:7], v[220:223], v[160:163]\n" \
"s_barrier\n" \
"v_mfma_f32_16x16x32_bf16 v[80:83], v[32:35], v[192:195], v[80:83]\n" \
"ds_read_b64_tr_b16 v[0:1], v232 offset:0\n" \
"v_mfma_f32_16x16x32_bf16 v[80:83], v[36:39], v[196:199], v[80:83]\n" \
"ds_read_b64_tr_b16 v[2:3], v232 offset:2048\n" \
"v_mfma_f32_16x16x32_bf16 v[84:87], v[40:43], v[192:195], v[84:87]\n" \
"s_add_u32 m0, s87, 102400\n" \
"ds_read_b64_tr_b16 v[4:5], v232 offset:8192\n" \
"v_mfma_f32_16x16x32_bf16 v[84:87], v[44:47], v[196:199], v[84:87]\n" \
"global_load_lds_dwordx4 v224, s[48:49] offset:-4096\n" \
"v_mfma_f32_16x16x32_bf16 v[88:91], v[48:51], v[192:195], v[88:91]\n" \
"ds_read_b64_tr_b16 v[6:7], v232 offset:10240\n" \
"v_mfma_f32_16x16x32_bf16 v[88:91], v[52:55], v[196:199], v[88:91]\n" \
"ds_read_b64_tr_b16 v[8:9], v233 offset:0\n" \
"v_mfma_f32_16x16x32_bf16 v[92:95], v[56:59], v[192:195], v[92:95]\n" \
"ds_read_b64_tr_b16 v[10:11], v233 offset:2048\n" \
"v_mfma_f32_16x16x32_bf16 v[92:95], v[60:63], v[196:199], v[92:95]\n" \
"global_load_lds_dwordx4 v225, s[48:49] offset:-3072\n" \
"v_mfma_f32_16x16x32_bf16 v[124:127], v[56:59], v[200:203], v[124:127]\n" \
"ds_read_b128 v[192:195], v230 offset:0\n" \
"v_mfma_f32_16x16x32_bf16 v[124:127], v[60:63], v[204:207], v[124:127]\n" \
"ds_read_b128 v[196:199], v231 offset:0\n" \
"v_mfma_f32_16x16x32_bf16 v[120:123], v[48:51], v[200:203], v[120:123]\n" \
"ds_read_b64_tr_b16 v[12:13], v233 offset:8192\n" \
"v_mfma_f32_16x16x32_bf16 v[120:123], v[52:55], v[204:207], v[120:123]\n" \
"global_load_lds_dwordx4 v226, s[48:49] offset:-2048\n" \
"v_mfma_f32_16x16x32_bf16 v[116:119], v[40:43], v[200:203], v[116:119]\n" \
"ds_read_b64_tr_b16 v[14:15], v233 offset:10240\n" \
"v_mfma_f32_16x16x32_bf16 v[116:119], v[44:47], v[204:207], v[116:119]\n" \
"ds_read_b64_tr_b16 v[16:17], v232 offset:512\n" \
"v_mfma_f32_16x16x32_bf16 v[112:115], v[32:35], v[200:203], v[112:115]\n" \
"ds_read_b64_tr_b16 v[18:19], v232 offset:2560\n" \
"v_mfma_f32_16x16x32_bf16 v[112:115], v[36:39], v[204:207], v[112:115]\n" \
"global_load_lds_dwordx4 v227, s[48:49] offset:-1024\n" \
"v_mfma_f32_16x16x32_bf16 v[144:147], v[32:35], v[208:211], v[144:147]\n" \
"s_add_u32 s48, s48, 0x100\n" \
"s_addc_u32 s49, s49, 0\n" \
"ds_read_b128 v[200:203], v230 offset:2048\n" \
"v_mfma_f32_16x16x32_bf16 v[144:147], v[36:39], v[212:215], v[144:147]\n" \
"ds_read_b128 v[204:207], v231 offset:2048\n" \
"v_mfma_f32_16x16x32_bf16 v[148:151], v[40:43], v[208:211], v[148:151]\n" \
"ds_read_b64_tr_b16 v[20:21], v232 offset:8704\n" \
"v_mfma_f32_16x16x32_bf16 v[148:151], v[44:47], v[212:215], v[148:151]\n" \
"global_load_lds_dwordx4 v224, s[46:47] offset:0\n" \
"v_mfma_f32_16x16x32_bf16 v[152:155], v[48:51], v[208:211], v[152:155]\n" \
"ds_read_b64_tr_b16 v[22:23], v232 offset:10752\n" \
"v_mfma_f32_16x16x32_bf16 v[152:155], v[52:55], v[212:215], v[152:155]\n" \
"ds_read_b64_tr_b16 v[24:25], v233 offset:512\n" \
"v_mfma_f32_16x16x32_bf16 v[156:159], v[56:59], v[208:211], v[156:159]\n" \
"ds_read_b64_tr_b16 v[26:27], v233 offset:2560\n" \
"v_mfma_f32_16x16x32_bf16 v[156:159], v[60:63], v[212:215], v[156:159]\n" \
"global_load_lds_dwordx4 v225, s[46:47] offset:1024\n" \
"v_mfma_f32_16x16x32_bf16 v[188:191], v[56:59], v[216:219], v[188:191]\n" \
"ds_read_b128 v[208:211], v230 offset:4096\n" \
"v_mfma_f32_16x16x32_bf16 v[188:191], v[60:63], v[220:223], v[188:191]\n" \
"ds_read_b128 v[212:215], v231 offset:4096\n" \
"v_mfma_f32_16x16x32_bf16 v[184:187], v[48:51], v[216:219], v[184:187]\n" \
"ds_read_b64_tr_b16 v[28:29], v233 offset:8704\n" \
"v_mfma_f32_16x16x32_bf16 v[184:187], v[52:55], v[220:223], v[184:187]\n" \
"global_load_lds_dwordx4 v226, s[46:47] offset:2048\n" \
"v_mfma_f32_16x16x32_bf16 v[180:183], v[40:43], v[216:219], v[180:183]\n" \
"ds_read_b64_tr_b16 v[30:31], v233 offset:10752\n" \
"v_mfma_f32_16x16x32_bf16 v[180:183], v[44:47], v[220:223], v[180:183]\n" \
"v_mfma_f32_16x16x32_bf16 v[176:179], v[32:35], v[216:219], v[176:179]\n" \
"v_mfma_f32_16x16x32_bf16 v[176:179], v[36:39], v[220:223], v[176:179]\n" \
"s_waitcnt lgkmcnt(1)\n" \
"global_load_lds_dwordx4 v227, s[46:47] offset:3072\n" \
"v_mfma_f32_16x16x32_bf16 v[64:67], v[0:3], v[192:195], v[64:67]\n" \
"s_add_u32 s46, s46, 0x100\n" \
"s_addc_u32 s47, s47, 0\n" \
"ds_read_b128 v[216:219], v230 offset:6144\n" \
"v_mfma_f32_16x16x32_bf16 v[64:67], v[4:7], v[196:199], v[64:67]\n" \
"ds_read_b128 v[220:223], v231 offset:6144\n" \
"s_add_u32 s90, s89, 139264\n" \
"v_mfma_f32_16x16x32_bf16 v[68:71], v[8:11], v[192:195], v[68:71]\n" \
"ds_read_b64_tr_b16 v[32:33], v232 offset:1024\n" \
"v_mfma_f32_16x16x32_bf16 v[68:71], v[12:15], v[196:199], v[68:71]\n" \
"buffer_store_dwordx4 a[144:147], v248, s[56:59], s90 offen offset:0 sc1\n" \
"v_mfma_f32_16x16x32_bf16 v[72:75], v[16:19], v[192:195], v[72:75]\n" \
"ds_read_b64_tr_b16 v[34:35], v232 offset:3072\n" \
"v_mfma_f32_16x16x32_bf16 v[72:75], v[20:23], v[196:199], v[72:75]\n" \
"s_waitcnt lgkmcnt(3)\n" \
"ds_read_b64_tr_b16 v[36:37], v232 offset:9216\n" \
"v_mfma_f32_16x16x32_bf16 v[76:79], v[24:27], v[192:195], v[76:79]\n" \
"ds_read_b64_tr_b16 v[38:39], v232 offset:11264\n" \
"v_mfma_f32_16x16x32_bf16 v[76:79], v[28:31], v[196:199], v[76:79]\n" \
"v_mfma_f32_16x16x32_bf16 v[108:111], v[24:27], v[200:203], v[108:111]\n" \
"ds_read_b64_tr_b16 v[40:41], v233 offset:1024\n" \
"v_mfma_f32_16x16x32_bf16 v[108:111], v[28:31], v[204:207], v[108:111]\n" \
"ds_read_b64_tr_b16 v[42:43], v233 offset:3072\n" \
"v_mfma_f32_16x16x32_bf16 v[104:107], v[16:19], v[200:203], v[104:107]\n" \
"ds_read_b64_tr_b16 v[44:45], v233 offset:9216\n" \
"v_mfma_f32_16x16x32_bf16 v[104:107], v[20:23], v[204:207], v[104:107]\n" \
"v_mfma_f32_16x16x32_bf16 v[100:103], v[8:11], v[200:203], v[100:103]\n" \
"ds_read_b64_tr_b16 v[46:47], v233 offset:11264\n" \
"v_mfma_f32_16x16x32_bf16 v[100:103], v[12:15], v[204:207], v[100:103]\n" \
"ds_read_b64_tr_b16 v[48:49], v232 offset:1536\n" \
"v_mfma_f32_16x16x32_bf16 v[96:99], v[0:3], v[200:203], v[96:99]\n" \
"s_add_u32 m0, s95, 4096\n" \
"ds_read_b64_tr_b16 v[50:51], v232 offset:3584\n" \
"v_mfma_f32_16x16x32_bf16 v[96:99], v[4:7], v[204:207], v[96:99]\n" \
"global_load_lds_dwordx4 v228, s[44:45] offset:-4096\n" \
"v_mfma_f32_16x16x32_bf16 v[128:131], v[0:3], v[208:211], v[128:131]\n" \
"ds_read_b64_tr_b16 v[52:53], v232 offset:9728\n" \
"v_mfma_f32_16x16x32_bf16 v[128:131], v[4:7], v[212:215], v[128:131]\n" \
"ds_read_b64_tr_b16 v[54:55], v232 offset:11776\n" \
"v_mfma_f32_16x16x32_bf16 v[132:135], v[8:11], v[208:211], v[132:135]\n" \
"ds_read_b64_tr_b16 v[56:57], v233 offset:1536\n" \
"v_mfma_f32_16x16x32_bf16 v[132:135], v[12:15], v[212:215], v[132:135]\n" \
"global_load_lds_dwordx4 v229, s[44:45] offset:-3072\n" \
"v_mfma_f32_16x16x32_bf16 v[136:139], v[16:19], v[208:211], v[136:139]\n" \
"s_add_u32 s44, s44, 0x80000\n" \
"s_addc_u32 s45, s45, 0\n" \
"ds_read_b64_tr_b16 v[58:59], v233 offset:3584\n" \
"v_mfma_f32_16x16x32_bf16 v[136:139], v[20:23], v[212:215], v[136:139]\n" \
"ds_read_b64_tr_b16 v[60:61], v233 offset:9728\n" \
"v_mfma_f32_16x16x32_bf16 v[140:143], v[24:27], v[208:211], v[140:143]\n" \
"ds_read_b64_tr_b16 v[62:63], v233 offset:11776\n" \
"v_mfma_f32_16x16x32_bf16 v[140:143], v[28:31], v[212:215], v[140:143]\n" \
"s_waitcnt lgkmcnt(3)\n" \
"global_load_lds_dwordx4 v228, s[42:43] offset:-2048\n" \
"v_mfma_f32_16x16x32_bf16 v[172:175], v[24:27], v[216:219], v[172:175]\n" \
"v_add_u32 v230, 0xffff0000, v230\n" \
"v_add_u32 v231, 0xffff0000, v231\n" \
"v_mfma_f32_16x16x32_bf16 v[172:175], v[28:31], v[220:223], v[172:175]\n" \
"v_add_u32 v232, 0x4000, v232\n" \
"v_add_u32 v233, 0x4000, v233\n" \
"v_mfma_f32_16x16x32_bf16 v[168:171], v[16:19], v[216:219], v[168:171]\n" \
"v_mfma_f32_16x16x32_bf16 v[168:171], v[20:23], v[220:223], v[168:171]\n" \
"global_load_lds_dwordx4 v229, s[42:43] offset:-1024\n" \
"v_mfma_f32_16x16x32_bf16 v[164:167], v[8:11], v[216:219], v[164:167]\n" \
"s_add_u32 s42, s42, 0x80000\n" \
"s_addc_u32 s43, s43, 0\n" \
"v_mfma_f32_16x16x32_bf16 v[164:167], v[12:15], v[220:223], v[164:167]\n" \
"s_waitcnt vmcnt(20) lgkmcnt(0)\n" \
"v_mfma_f32_16x16x32_bf16 v[160:163], v[0:3], v[216:219], v[160:163]\n" \
"v_mfma_f32_16x16x32_bf16 v[160:163], v[4:7], v[220:223], v[160:163]\n" \
"s_barrier\n" \
"v_mfma_f32_16x16x32_bf16 v[80:83], v[32:35], v[192:195], v[80:83]\n" \
"ds_read_b64_tr_b16 v[0:1], v232 offset:0\n" \
"v_mfma_f32_16x16x32_bf16 v[80:83], v[36:39], v[196:199], v[80:83]\n" \
"ds_read_b64_tr_b16 v[2:3], v232 offset:2048\n" \
"v_mfma_f32_16x16x32_bf16 v[84:87], v[40:43], v[192:195], v[84:87]\n" \
"s_add_u32 m0, s87, 135168\n" \
"ds_read_b64_tr_b16 v[4:5], v232 offset:8192\n" \
"v_mfma_f32_16x16x32_bf16 v[84:87], v[44:47], v[196:199], v[84:87]\n" \
"global_load_lds_dwordx4 v224, s[48:49] offset:-4096\n" \
"v_mfma_f32_16x16x32_bf16 v[88:91], v[48:51], v[192:195], v[88:91]\n" \
"ds_read_b64_tr_b16 v[6:7], v232 offset:10240\n" \
"v_mfma_f32_16x16x32_bf16 v[88:91], v[52:55], v[196:199], v[88:91]\n" \
"ds_read_b64_tr_b16 v[8:9], v233 offset:0\n" \
"v_mfma_f32_16x16x32_bf16 v[92:95], v[56:59], v[192:195], v[92:95]\n" \
"ds_read_b64_tr_b16 v[10:11], v233 offset:2048\n" \
"v_mfma_f32_16x16x32_bf16 v[92:95], v[60:63], v[196:199], v[92:95]\n" \
"global_load_lds_dwordx4 v225, s[48:49] offset:-3072\n" \
"v_mfma_f32_16x16x32_bf16 v[124:127], v[56:59], v[200:203], v[124:127]\n" \
"ds_read_b128 v[192:195], v230 offset:0\n" \
"v_mfma_f32_16x16x32_bf16 v[124:127], v[60:63], v[204:207], v[124:127]\n" \
"ds_read_b128 v[196:199], v231 offset:0\n" \
"v_mfma_f32_16x16x32_bf16 v[120:123], v[48:51], v[200:203], v[120:123]\n" \
"ds_read_b64_tr_b16 v[12:13], v233 offset:8192\n" \
"v_mfma_f32_16x16x32_bf16 v[120:123], v[52:55], v[204:207], v[120:123]\n" \
"global_load_lds_dwordx4 v226, s[48:49] offset:-2048\n" \
"v_mfma_f32_16x16x32_bf16 v[116:119], v[40:43], v[200:203], v[116:119]\n" \
"ds_read_b64_tr_b16 v[14:15], v233 offset:10240\n" \
"v_mfma_f32_16x16x32_bf16 v[116:119], v[44:47], v[204:207], v[116:119]\n" \
"ds_read_b64_tr_b16 v[16:17], v232 offset:512\n" \
"v_mfma_f32_16x16x32_bf16 v[112:115], v[32:35], v[200:203], v[112:115]\n" \
"ds_read_b64_tr_b16 v[18:19], v232 offset:2560\n" \
"v_mfma_f32_16x16x32_bf16 v[112:115], v[36:39], v[204:207], v[112:115]\n" \
"global_load_lds_dwordx4 v227, s[48:49] offset:-1024\n" \
"v_mfma_f32_16x16x32_bf16 v[144:147], v[32:35], v[208:211], v[144:147]\n" \
"s_add_u32 s48, s48, 0x80\n" \
"s_addc_u32 s49, s49, 0\n" \
"ds_read_b128 v[200:203], v230 offset:2048\n" \
"v_mfma_f32_16x16x32_bf16 v[144:147], v[36:39], v[212:215], v[144:147]\n" \
"ds_read_b128 v[204:207], v231 offset:2048\n" \
"v_mfma_f32_16x16x32_bf16 v[148:151], v[40:43], v[208:211], v[148:151]\n" \
"ds_read_b64_tr_b16 v[20:21], v232 offset:8704\n" \
"v_mfma_f32_16x16x32_bf16 v[148:151], v[44:47], v[212:215], v[148:151]\n" \
"global_load_lds_dwordx4 v224, s[46:47] offset:0\n" \
"v_mfma_f32_16x16x32_bf16 v[152:155], v[48:51], v[208:211], v[152:155]\n" \
"ds_read_b64_tr_b16 v[22:23], v232 offset:10752\n" \
"v_mfma_f32_16x16x32_bf16 v[152:155], v[52:55], v[212:215], v[152:155]\n" \
"ds_read_b64_tr_b16 v[24:25], v233 offset:512\n" \
"v_mfma_f32_16x16x32_bf16 v[156:159], v[56:59], v[208:211], v[156:159]\n" \
"ds_read_b64_tr_b16 v[26:27], v233 offset:2560\n" \
"v_mfma_f32_16x16x32_bf16 v[156:159], v[60:63], v[212:215], v[156:159]\n" \
"global_load_lds_dwordx4 v225, s[46:47] offset:1024\n" \
"v_mfma_f32_16x16x32_bf16 v[188:191], v[56:59], v[216:219], v[188:191]\n" \
"ds_read_b128 v[208:211], v230 offset:4096\n" \
"v_mfma_f32_16x16x32_bf16 v[188:191], v[60:63], v[220:223], v[188:191]\n" \
"ds_read_b128 v[212:215], v231 offset:4096\n" \
"v_mfma_f32_16x16x32_bf16 v[184:187], v[48:51], v[216:219], v[184:187]\n" \
"ds_read_b64_tr_b16 v[28:29], v233 offset:8704\n" \
"v_mfma_f32_16x16x32_bf16 v[184:187], v[52:55], v[220:223], v[184:187]\n" \
"global_load_lds_dwordx4 v226, s[46:47] offset:2048\n" \
"v_mfma_f32_16x16x32_bf16 v[180:183], v[40:43], v[216:219], v[180:183]\n" \
"ds_read_b64_tr_b16 v[30:31], v233 offset:10752\n" \
"v_mfma_f32_16x16x32_bf16 v[180:183], v[44:47], v[220:223], v[180:183]\n" \
"v_mfma_f32_16x16x32_bf16 v[176:179], v[32:35], v[216:219], v[176:179]\n" \
"v_mfma_f32_16x16x32_bf16 v[176:179], v[36:39], v[220:223], v[176:179]\n" \
"s_waitcnt lgkmcnt(1)\n" \
"global_load_lds_dwordx4 v227, s[46:47] offset:3072\n" \
"v_mfma_f32_16x16x32_bf16 v[64:67], v[0:3], v[192:195], v[64:67]\n" \
"s_add_u32 s46, s46, 0x80\n" \
"s_addc_u32 s47, s47, 0\n" \
"ds_read_b128 v[216:219], v230 offset:6144\n" \
"v_mfma_f32_16x16x32_bf16 v[64:67], v[4:7], v[196:199], v[64:67]\n" \
"ds_read_b128 v[220:223], v231 offset:6144\n" \
"v_mfma_f32_16x16x32_bf16 v[68:71], v[8:11], v[192:195], v[68:71]\n" \
"ds_read_b64_tr_b16 v[32:33], v232 offset:1024\n" \
"v_mfma_f32_16x16x32_bf16 v[68:71], v[12:15], v[196:199], v[68:71]\n" \
"buffer_store_dwordx4 a[148:151], v248, s[56:59], s90 offen offset:1024 sc1\n" \
"v_mfma_f32_16x16x32_bf16 v[72:75], v[16:19], v[192:195], v[72:75]\n" \
"ds_read_b64_tr_b16 v[34:35], v232 offset:3072\n" \
"v_mfma_f32_16x16x32_bf16 v[72:75], v[20:23], v[196:199], v[72:75]\n" \
"s_waitcnt lgkmcnt(3)\n" \
"ds_read_b64_tr_b16 v[36:37], v232 offset:9216\n" \
"v_mfma_f32_16x16x32_bf16 v[76:79], v[24:27], v[192:195], v[76:79]\n" \
"ds_read_b64_tr_b16 v[38:39], v232 offset:11264\n" \
"v_mfma_f32_16x16x32_bf16 v[76:79], v[28:31], v[196:199], v[76:79]\n" \
"v_mfma_f32_16x16x32_bf16 v[108:111], v[24:27], v[200:203], v[108:111]\n" \
"ds_read_b64_tr_b16 v[40:41], v233 offset:1024\n" \
"v_mfma_f32_16x16x32_bf16 v[108:111], v[28:31], v[204:207], v[108:111]\n" \
"ds_read_b64_tr_b16 v[42:43], v233 offset:3072\n" \
"v_mfma_f32_16x16x32_bf16 v[104:107], v[16:19], v[200:203], v[104:107]\n" \
"ds_read_b64_tr_b16 v[44:45], v233 offset:9216\n" \
"v_mfma_f32_16x16x32_bf16 v[104:107], v[20:23], v[204:207], v[104:107]\n" \
"v_mfma_f32_16x16x32_bf16 v[100:103], v[8:11], v[200:203], v[100:103]\n" \
"ds_read_b64_tr_b16 v[46:47], v233 offset:11264\n" \
"v_mfma_f32_16x16x32_bf16 v[100:103], v[12:15], v[204:207], v[100:103]\n" \
"ds_read_b64_tr_b16 v[48:49], v232 offset:1536\n" \
"v_mfma_f32_16x16x32_bf16 v[96:99], v[0:3], v[200:203], v[96:99]\n" \
"s_add_u32 m0, s95, 20480\n" \
"ds_read_b64_tr_b16 v[50:51], v232 offset:3584\n" \
"v_mfma_f32_16x16x32_bf16 v[96:99], v[4:7], v[204:207], v[96:99]\n" \
"global_load_lds_dwordx4 v228, s[44:45] offset:-4096\n" \
"v_mfma_f32_16x16x32_bf16 v[128:131], v[0:3], v[208:211], v[128:131]\n" \
"ds_read_b64_tr_b16 v[52:53], v232 offset:9728\n" \
"v_mfma_f32_16x16x32_bf16 v[128:131], v[4:7], v[212:215], v[128:131]\n" \
"ds_read_b64_tr_b16 v[54:55], v232 offset:11776\n" \
"v_mfma_f32_16x16x32_bf16 v[132:135], v[8:11], v[208:211], v[132:135]\n" \
"ds_read_b64_tr_b16 v[56:57], v233 offset:1536\n" \
"v_mfma_f32_16x16x32_bf16 v[132:135], v[12:15], v[212:215], v[132:135]\n" \
"global_load_lds_dwordx4 v229, s[44:45] offset:-3072\n" \
"v_mfma_f32_16x16x32_bf16 v[136:139], v[16:19], v[208:211], v[136:139]\n" \
"s_add_u32 s44, s44, 0x80000\n" \
"s_addc_u32 s45, s45, 0\n" \
"ds_read_b64_tr_b16 v[58:59], v233 offset:3584\n" \
"v_mfma_f32_16x16x32_bf16 v[136:139], v[20:23], v[212:215], v[136:139]\n" \
"ds_read_b64_tr_b16 v[60:61], v233 offset:9728\n" \
"v_mfma_f32_16x16x32_bf16 v[140:143], v[24:27], v[208:211], v[140:143]\n" \
"ds_read_b64_tr_b16 v[62:63], v233 offset:11776\n" \
"v_mfma_f32_16x16x32_bf16 v[140:143], v[28:31], v[212:215], v[140:143]\n" \
"s_waitcnt lgkmcnt(3)\n" \
"global_load_lds_dwordx4 v228, s[42:43] offset:-2048\n" \
"v_mfma_f32_16x16x32_bf16 v[172:175], v[24:27], v[216:219], v[172:175]\n" \
"v_add_u32 v230, 0x8000, v230\n" \
"v_add_u32 v231, 0x8000, v231\n" \
"v_mfma_f32_16x16x32_bf16 v[172:175], v[28:31], v[220:223], v[172:175]\n" \
"v_add_u32 v232, 0x4000, v232\n" \
"v_add_u32 v233, 0x4000, v233\n" \
"v_mfma_f32_16x16x32_bf16 v[168:171], v[16:19], v[216:219], v[168:171]\n" \
"v_mfma_f32_16x16x32_bf16 v[168:171], v[20:23], v[220:223], v[168:171]\n" \
"global_load_lds_dwordx4 v229, s[42:43] offset:-1024\n" \
"v_mfma_f32_16x16x32_bf16 v[164:167], v[8:11], v[216:219], v[164:167]\n" \
"s_add_u32 s42, s42, 0x80000\n" \
"s_addc_u32 s43, s43, 0\n" \
"v_mfma_f32_16x16x32_bf16 v[164:167], v[12:15], v[220:223], v[164:167]\n" \
"s_waitcnt vmcnt(18) lgkmcnt(0)\n" \
"v_mfma_f32_16x16x32_bf16 v[160:163], v[0:3], v[216:219], v[160:163]\n" \
"v_mfma_f32_16x16x32_bf16 v[160:163], v[4:7], v[220:223], v[160:163]\n" \
"s_barrier\n" \
"v_mfma_f32_16x16x32_bf16 v[80:83], v[32:35], v[192:195], v[80:83]\n" \
"ds_read_b64_tr_b16 v[0:1], v232 offset:0\n" \
"v_mfma_f32_16x16x32_bf16 v[80:83], v[36:39], v[196:199], v[80:83]\n" \
"ds_read_b64_tr_b16 v[2:3], v232 offset:2048\n" \
"v_mfma_f32_16x16x32_bf16 v[84:87], v[40:43], v[192:195], v[84:87]\n" \
"s_add_u32 m0, s87, 69632\n" \
"ds_read_b64_tr_b16 v[4:5], v232 offset:8192\n" \
"v_mfma_f32_16x16x32_bf16 v[84:87], v[44:47], v[196:199], v[84:87]\n" \
"global_load_lds_dwordx4 v224, s[48:49] offset:-4096\n" \
"v_mfma_f32_16x16x32_bf16 v[88:91], v[48:51], v[192:195], v[88:91]\n" \
"ds_read_b64_tr_b16 v[6:7], v232 offset:10240\n" \
"v_mfma_f32_16x16x32_bf16 v[88:91], v[52:55], v[196:199], v[88:91]\n" \
"ds_read_b64_tr_b16 v[8:9], v233 offset:0\n" \
"v_mfma_f32_16x16x32_bf16 v[92:95], v[56:59], v[192:195], v[92:95]\n" \
"ds_read_b64_tr_b16 v[10:11], v233 offset:2048\n" \
"v_mfma_f32_16x16x32_bf16 v[92:95], v[60:63], v[196:199], v[92:95]\n" \
"global_load_lds_dwordx4 v225, s[48:49] offset:-3072\n" \
"v_mfma_f32_16x16x32_bf16 v[124:127], v[56:59], v[200:203], v[124:127]\n" \
"ds_read_b128 v[192:195], v230 offset:0\n" \
"v_mfma_f32_16x16x32_bf16 v[124:127], v[60:63], v[204:207], v[124:127]\n" \
"ds_read_b128 v[196:199], v231 offset:0\n" \
"v_mfma_f32_16x16x32_bf16 v[120:123], v[48:51], v[200:203], v[120:123]\n" \
"ds_read_b64_tr_b16 v[12:13], v233 offset:8192\n" \
"v_mfma_f32_16x16x32_bf16 v[120:123], v[52:55], v[204:207], v[120:123]\n" \
"global_load_lds_dwordx4 v226, s[48:49] offset:-2048\n" \
"v_mfma_f32_16x16x32_bf16 v[116:119], v[40:43], v[200:203], v[116:119]\n" \
"ds_read_b64_tr_b16 v[14:15], v233 offset:10240\n" \
"v_mfma_f32_16x16x32_bf16 v[116:119], v[44:47], v[204:207], v[116:119]\n" \
"ds_read_b64_tr_b16 v[16:17], v232 offset:512\n" \
"v_mfma_f32_16x16x32_bf16 v[112:115], v[32:35], v[200:203], v[112:115]\n" \
"ds_read_b64_tr_b16 v[18:19], v232 offset:2560\n" \
"v_mfma_f32_16x16x32_bf16 v[112:115], v[36:39], v[204:207], v[112:115]\n" \
"global_load_lds_dwordx4 v227, s[48:49] offset:-1024\n" \
"v_mfma_f32_16x16x32_bf16 v[144:147], v[32:35], v[208:211], v[144:147]\n" \
"s_add_u32 s48, s48, 0x80\n" \
"s_addc_u32 s49, s49, 0\n" \
"ds_read_b128 v[200:203], v230 offset:2048\n" \
"v_mfma_f32_16x16x32_bf16 v[144:147], v[36:39], v[212:215], v[144:147]\n" \
"ds_read_b128 v[204:207], v231 offset:2048\n" \
"v_mfma_f32_16x16x32_bf16 v[148:151], v[40:43], v[208:211], v[148:151]\n" \
"ds_read_b64_tr_b16 v[20:21], v232 offset:8704\n" \
"v_mfma_f32_16x16x32_bf16 v[148:151], v[44:47], v[212:215], v[148:151]\n" \
"global_load_lds_dwordx4 v224, s[46:47] offset:0\n" \
"v_mfma_f32_16x16x32_bf16 v[152:155], v[48:51], v[208:211], v[152:155]\n" \
"ds_read_b64_tr_b16 v[22:23], v232 offset:10752\n" \
"v_mfma_f32_16x16x32_bf16 v[152:155], v[52:55], v[212:215], v[152:155]\n" \
"ds_read_b64_tr_b16 v[24:25], v233 offset:512\n" \
"v_mfma_f32_16x16x32_bf16 v[156:159], v[56:59], v[208:211], v[156:159]\n" \
"ds_read_b64_tr_b16 v[26:27], v233 offset:2560\n" \
"v_mfma_f32_16x16x32_bf16 v[156:159], v[60:63], v[212:215], v[156:159]\n" \
"global_load_lds_dwordx4 v225, s[46:47] offset:1024\n" \
"v_mfma_f32_16x16x32_bf16 v[188:191], v[56:59], v[216:219], v[188:191]\n" \
"ds_read_b128 v[208:211], v230 offset:4096\n" \
"v_mfma_f32_16x16x32_bf16 v[188:191], v[60:63], v[220:223], v[188:191]\n" \
"ds_read_b128 v[212:215], v231 offset:4096\n" \
"v_mfma_f32_16x16x32_bf16 v[184:187], v[48:51], v[216:219], v[184:187]\n" \
"ds_read_b64_tr_b16 v[28:29], v233 offset:8704\n" \
"v_mfma_f32_16x16x32_bf16 v[184:187], v[52:55], v[220:223], v[184:187]\n" \
"global_load_lds_dwordx4 v226, s[46:47] offset:2048\n" \
"v_mfma_f32_16x16x32_bf16 v[180:183], v[40:43], v[216:219], v[180:183]\n" \
"ds_read_b64_tr_b16 v[30:31], v233 offset:10752\n" \
"v_mfma_f32_16x16x32_bf16 v[180:183], v[44:47], v[220:223], v[180:183]\n" \
"v_mfma_f32_16x16x32_bf16 v[176:179], v[32:35], v[216:219], v[176:179]\n" \
"v_mfma_f32_16x16x32_bf16 v[176:179], v[36:39], v[220:223], v[176:179]\n" \
"s_waitcnt lgkmcnt(1)\n" \
"global_load_lds_dwordx4 v227, s[46:47] offset:3072\n" \
"v_mfma_f32_16x16x32_bf16 v[64:67], v[0:3], v[192:195], v[64:67]\n" \
"s_add_u32 s46, s46, 0x80\n" \
"s_addc_u32 s47, s47, 0\n" \
"ds_read_b128 v[216:219], v230 offset:6144\n" \
"v_mfma_f32_16x16x32_bf16 v[64:67], v[4:7], v[196:199], v[64:67]\n" \
"s_mul_i32 s77, s62, 557824\n" \
"ds_read_b128 v[220:223], v231 offset:6144\n" \
"s_add_u32 s77, s77, s95\n" \
"v_mfma_f32_16x16x32_bf16 v[68:71], v[8:11], v[192:195], v[68:71]\n" \
"s_add_u32 s36, s28, s77\n" \
"s_mov_b32 s37, s29\n" \
"ds_read_b64_tr_b16 v[32:33], v232 offset:1024\n" \
"v_mfma_f32_16x16x32_bf16 v[68:71], v[12:15], v[196:199], v[68:71]\n" \
"buffer_store_dwordx4 a[152:155], v248, s[56:59], s90 offen offset:2048 sc1\n" \
"s_add_u32 s77, s77, 17850368\n" \
"s_add_u32 s38, s28, s77\n" \
"s_mov_b32 s39, s29\n" \
"s_and_b32 s74, s63, 1\n" \
"v_mfma_f32_16x16x32_bf16 v[72:75], v[16:19], v[192:195], v[72:75]\n" \
"s_lshl_b32 s74, s74, 15\n" \
"s_xor_b32 s74, s74, 0x18000\n" \
"ds_read_b64_tr_b16 v[34:35], v232 offset:3072\n" \
"v_mfma_f32_16x16x32_bf16 v[72:75], v[20:23], v[196:199], v[72:75]\n" \
"s_waitcnt lgkmcnt(3)\n" \
"ds_read_b64_tr_b16 v[36:37], v232 offset:9216\n" \
"v_add_u32 v247, s74, v248\n" \
"v_mfma_f32_16x16x32_bf16 v[76:79], v[24:27], v[192:195], v[76:79]\n" \
"s_add_u32 s74, s74, s95\n" \
"s_lshr_b32 s83, s61, 1\n" \
"ds_read_b64_tr_b16 v[38:39], v232 offset:11264\n" \
"v_mfma_f32_16x16x32_bf16 v[76:79], v[28:31], v[196:199], v[76:79]\n" \
"buffer_store_dwordx4 a[156:159], v248, s[56:59], s90 offen offset:3072 sc1\n" \
"s_lshl_b32 s75, s63, 2\n" \
"s_add_u32 s77, s83, s75\n" \
"s_mul_i32 s77, s77, 266240\n" \
"s_sub_u32 s78, s83, s75\n" \
"v_mfma_f32_16x16x32_bf16 v[108:111], v[24:27], v[200:203], v[108:111]\n" \
"s_add_u32 s78, s78, 60\n" \
"s_mul_i32 s78, s78, 266240\n" \
"ds_read_b64_tr_b16 v[40:41], v233 offset:1024\n" \
"v_mfma_f32_16x16x32_bf16 v[108:111], v[28:31], v[204:207], v[108:111]\n" \
"s_and_b32 s82, s61, 1\n" \
"ds_read_b64_tr_b16 v[42:43], v233 offset:3072\n" \
"v_mfma_f32_16x16x32_bf16 v[104:107], v[16:19], v[200:203], v[104:107]\n" \
"s_cmp_eq_u32 s82, 0\n" \
"s_cselect_b32 s16, 0, 0xff00\n" \
"ds_read_b64_tr_b16 v[44:45], v233 offset:9216\n" \
"v_mfma_f32_16x16x32_bf16 v[104:107], v[20:23], v[204:207], v[104:107]\n" \
"buffer_load_dword v244, v245, s[56:59], s91 offen sc1\n" \
"v_mfma_f32_16x16x32_bf16 v[100:103], v[8:11], v[200:203], v[100:103]\n" \
"s_mov_b32 s17, s16\n" \
"s_lshl_b32 s75, s82, 8\n" \
"ds_read_b64_tr_b16 v[46:47], v233 offset:11264\n" \
"v_mfma_f32_16x16x32_bf16 v[100:103], v[12:15], v[204:207], v[100:103]\n" \
"s_lshl_b32 s76, s62, 13\n" \
"s_add_u32 s75, s75, s76\n" \
"ds_read_b64_tr_b16 v[48:49], v232 offset:1536\n" \
"v_mfma_f32_16x16x32_bf16 v[96:99], v[0:3], v[200:203], v[96:99]\n" \
"s_add_u32 m0, s95, 36864\n" \
"s_add_u32 s75, s75, 35700736\n" \
"ds_read_b64_tr_b16 v[50:51], v232 offset:3584\n" \
"v_mfma_f32_16x16x32_bf16 v[96:99], v[4:7], v[204:207], v[96:99]\n" \
"s_add_u32 s77, s77, s75\n" \
"global_load_lds_dwordx4 v228, s[44:45] offset:-4096\n" \
"v_mfma_f32_16x16x32_bf16 v[128:131], v[0:3], v[208:211], v[128:131]\n" \
"s_add_u32 s78, s78, s75\n" \
"s_mul_i32 s83, s83, 9216\n" \
"ds_read_b64_tr_b16 v[52:53], v232 offset:9728\n" \
"v_mfma_f32_16x16x32_bf16 v[128:131], v[4:7], v[212:215], v[128:131]\n" \
"s_lshl_b32 s75, s82, 12\n" \
"s_add_u32 s83, s83, s75\n" \
"ds_read_b64_tr_b16 v[54:55], v232 offset:11776\n" \
"v_mfma_f32_16x16x32_bf16 v[132:135], v[8:11], v[208:211], v[132:135]\n" \
"s_add_u32 s83, s83, 4096\n" \
"s_lshl_b32 s82, s82, 14\n" \
"ds_read_b64_tr_b16 v[56:57], v233 offset:1536\n" \
"v_mfma_f32_16x16x32_bf16 v[132:135], v[12:15], v[212:215], v[132:135]\n" \
"global_load_lds_dwordx4 v229, s[44:45] offset:-3072\n" \
"v_mfma_f32_16x16x32_bf16 v[136:139], v[16:19], v[208:211], v[136:139]\n" \
"s_add_u32 s44, s44, 0x80000\n" \
"s_addc_u32 s45, s45, 0\n" \
"ds_read_b64_tr_b16 v[58:59], v233 offset:3584\n" \
"v_mfma_f32_16x16x32_bf16 v[136:139], v[20:23], v[212:215], v[136:139]\n" \
"s_cmp_eq_u32 s63, 7\n" \
"s_cselect_b32 s82, 0, s82\n" \
"ds_read_b64_tr_b16 v[60:61], v233 offset:9728\n" \
"v_mfma_f32_16x16x32_bf16 v[140:143], v[24:27], v[208:211], v[140:143]\n" \
"s_mov_b32 s75, s63\n" \
"s_sub_u32 s76, 7, s63\n" \
"ds_read_b64_tr_b16 v[62:63], v233 offset:11776\n" \
"v_mfma_f32_16x16x32_bf16 v[140:143], v[28:31], v[212:215], v[140:143]\n" \
"s_waitcnt lgkmcnt(3)\n" \
"global_load_lds_dwordx4 v228, s[42:43] offset:-2048\n" \
"s_mov_b32 s73, s95\n" \
"v_mfma_f32_16x16x32_bf16 v[172:175], v[24:27], v[216:219], v[172:175]\n" \
"s_movk_i32 s94, 0x207\n" \
"s_mov_b64 s[40:41], s[14:15]\n" \
"s_mov_b32 s80, 0\n" \
"v_mfma_f32_16x16x32_bf16 v[172:175], v[28:31], v[220:223], v[172:175]\n" \
"s_mov_b32 s81, 0\n" \
"v_add_u32 v230, 0x8000, v230\n" \
"v_add_u32 v231, 0x8000, v231\n" \
"v_mfma_f32_16x16x32_bf16 v[168:171], v[16:19], v[216:219], v[168:171]\n" \
"s_cmp_eq_u32 s63, 7\n" \
"s_cselect_b64 s[40:41], s[18:19], s[40:41]\n" \
"s_cselect_b32 s80, 12, s80\n" \
"v_mfma_f32_16x16x32_bf16 v[168:171], v[20:23], v[220:223], v[168:171]\n" \
"global_load_lds_dwordx4 v229, s[42:43] offset:-1024\n" \
"v_add_u32 v232, 0xffff4000, v232\n" \
"v_mfma_f32_16x16x32_bf16 v[164:167], v[8:11], v[216:219], v[164:167]\n" \
"s_add_u32 s42, s42, 0x80000\n" \
"s_addc_u32 s43, s43, 0\n" \
"v_add_u32 v233, 0xffff4000, v233\n" \
"v_mfma_f32_16x16x32_bf16 v[164:167], v[12:15], v[220:223], v[164:167]\n" \
"s_waitcnt vmcnt(20) lgkmcnt(0)\n" \
"v_mfma_f32_16x16x32_bf16 v[160:163], v[0:3], v[216:219], v[160:163]\n" \
"s_cmp_eq_u32 s63, 6\n" \
"s_cselect_b64 s[40:41], s[22:23], s[40:41]\n" \
"s_cselect_b32 s80, 12, s80\n" \
"v_mfma_f32_16x16x32_bf16 v[160:163], v[4:7], v[220:223], v[160:163]\n" \
"s_barrier\n" \
"v_mfma_f32_16x16x32_bf16 v[80:83], v[32:35], v[192:195], v[80:83]\n" \
"ds_read_b64_tr_b16 v[0:1], v232 offset:0\n" \
"v_mfma_f32_16x16x32_bf16 v[80:83], v[36:39], v[196:199], v[80:83]\n" \
"ds_read_b64_tr_b16 v[2:3], v232 offset:2048\n" \
"v_mfma_f32_16x16x32_bf16 v[84:87], v[40:43], v[192:195], v[84:87]\n" \
"s_add_u32 m0, s87, 102400\n" \
"ds_read_b64_tr_b16 v[4:5], v232 offset:8192\n" \
"v_mfma_f32_16x16x32_bf16 v[84:87], v[44:47], v[196:199], v[84:87]\n" \
"global_load_lds_dwordx4 v224, s[48:49] offset:-4096\n" \
"v_mfma_f32_16x16x32_bf16 v[88:91], v[48:51], v[192:195], v[88:91]\n" \
"ds_read_b64_tr_b16 v[6:7], v232 offset:10240\n" \
"v_mfma_f32_16x16x32_bf16 v[88:91], v[52:55], v[196:199], v[88:91]\n" \
"ds_read_b64_tr_b16 v[8:9], v233 offset:0\n" \
"v_mfma_f32_16x16x32_bf16 v[92:95], v[56:59], v[192:195], v[92:95]\n" \
"ds_read_b64_tr_b16 v[10:11], v233 offset:2048\n" \
"v_mfma_f32_16x16x32_bf16 v[92:95], v[60:63], v[196:199], v[92:95]\n" \
"global_load_lds_dwordx4 v225, s[48:49] offset:-3072\n" \
"v_mfma_f32_16x16x32_bf16 v[124:127], v[56:59], v[200:203], v[124:127]\n" \
"ds_read_b128 v[192:195], v230 offset:0\n" \
"v_mfma_f32_16x16x32_bf16 v[124:127], v[60:63], v[204:207], v[124:127]\n" \
"ds_read_b128 v[196:199], v231 offset:0\n" \
"v_mfma_f32_16x16x32_bf16 v[120:123], v[48:51], v[200:203], v[120:123]\n" \
"ds_read_b64_tr_b16 v[12:13], v233 offset:8192\n" \
"v_mfma_f32_16x16x32_bf16 v[120:123], v[52:55], v[204:207], v[120:123]\n" \
"global_load_lds_dwordx4 v226, s[48:49] offset:-2048\n" \
"v_mfma_f32_16x16x32_bf16 v[116:119], v[40:43], v[200:203], v[116:119]\n" \
"ds_read_b64_tr_b16 v[14:15], v233 offset:10240\n" \
"v_mfma_f32_16x16x32_bf16 v[116:119], v[44:47], v[204:207], v[116:119]\n" \
"ds_read_b64_tr_b16 v[16:17], v232 offset:512\n" \
"v_mfma_f32_16x16x32_bf16 v[112:115], v[32:35], v[200:203], v[112:115]\n" \
"ds_read_b64_tr_b16 v[18:19], v232 offset:2560\n" \
"v_mfma_f32_16x16x32_bf16 v[112:115], v[36:39], v[204:207], v[112:115]\n" \
"global_load_lds_dwordx4 v227, s[48:49] offset:-1024\n" \
"v_mfma_f32_16x16x32_bf16 v[144:147], v[32:35], v[208:211], v[144:147]\n" \
"s_add_u32 s48, s48, 0x80\n" \
"s_addc_u32 s49, s49, 0\n" \
"ds_read_b128 v[200:203], v230 offset:2048\n" \
"v_mfma_f32_16x16x32_bf16 v[144:147], v[36:39], v[212:215], v[144:147]\n" \
"ds_read_b128 v[204:207], v231 offset:2048\n" \
"v_mfma_f32_16x16x32_bf16 v[148:151], v[40:43], v[208:211], v[148:151]\n" \
"ds_read_b64_tr_b16 v[20:21], v232 offset:8704\n" \
"v_mfma_f32_16x16x32_bf16 v[148:151], v[44:47], v[212:215], v[148:151]\n" \
"global_load_lds_dwordx4 v224, s[46:47] offset:0\n" \
"v_mfma_f32_16x16x32_bf16 v[152:155], v[48:51], v[208:211], v[152:155]\n" \
"ds_read_b64_tr_b16 v[22:23], v232 offset:10752\n" \
"v_mfma_f32_16x16x32_bf16 v[152:155], v[52:55], v[212:215], v[152:155]\n" \
"ds_read_b64_tr_b16 v[24:25], v233 offset:512\n" \
"v_mfma_f32_16x16x32_bf16 v[156:159], v[56:59], v[208:211], v[156:159]\n" \
"ds_read_b64_tr_b16 v[26:27], v233 offset:2560\n" \
"v_mfma_f32_16x16x32_bf16 v[156:159], v[60:63], v[212:215], v[156:159]\n" \
"global_load_lds_dwordx4 v225, s[46:47] offset:1024\n" \
"v_mfma_f32_16x16x32_bf16 v[188:191], v[56:59], v[216:219], v[188:191]\n" \
"ds_read_b128 v[208:211], v230 offset:4096\n" \
"v_mfma_f32_16x16x32_bf16 v[188:191], v[60:63], v[220:223], v[188:191]\n" \
"ds_read_b128 v[212:215], v231 offset:4096\n" \
"v_mfma_f32_16x16x32_bf16 v[184:187], v[48:51], v[216:219], v[184:187]\n" \
"ds_read_b64_tr_b16 v[28:29], v233 offset:8704\n" \
"v_mfma_f32_16x16x32_bf16 v[184:187], v[52:55], v[220:223], v[184:187]\n" \
"Lpg4398_%=:\n" \
"s_cbranch_execz Lpg5573_%=\n" \
"global_load_lds_dwordx4 v226, s[46:47] offset:2048\n" \
"v_mfma_f32_16x16x32_bf16 v[180:183], v[40:43], v[216:219], v[180:183]\n" \
"ds_read_b64_tr_b16 v[30:31], v233 offset:10752\n" \
"v_mfma_f32_16x16x32_bf16 v[180:183], v[44:47], v[220:223], v[180:183]\n" \
"s_cmp_eq_u32 s63, 5\n" \
"s_cselect_b64 s[40:41], s[18:19], s[40:41]\n" \
"s_cselect_b32 s80, 4, s80\n" \
"v_mfma_f32_16x16x32_bf16 v[176:179], v[32:35], v[216:219], v[176:179]\n" \
"s_cmp_eq_u32 s63, 5\n" \
"s_cselect_b32 s81, 384, s81\n" \
"v_mfma_f32_16x16x32_bf16 v[176:179], v[36:39], v[220:223], v[176:179]\n" \
"s_waitcnt lgkmcnt(1)\n" \
"global_load_lds_dwordx4 v227, s[46:47] offset:3072\n" \
"v_mfma_f32_16x16x32_bf16 v[64:67], v[0:3], v[192:195], v[64:67]\n" \
"s_add_u32 s46, s46, 0x80\n" \
"s_addc_u32 s47, s47, 0\n" \
"ds_read_b128 v[216:219], v230 offset:6144\n" \
"v_mfma_f32_16x16x32_bf16 v[64:67], v[4:7], v[196:199], v[64:67]\n" \
"ds_read_b128 v[220:223], v231 offset:6144\n" \
"s_add_u32 s90, s88, 278528\n" \
"v_mfma_f32_16x16x32_bf16 v[68:71], v[8:11], v[192:195], v[68:71]\n" \
"ds_read_b64_tr_b16 v[32:33], v232 offset:1024\n" \
"v_mfma_f32_16x16x32_bf16 v[68:71], v[12:15], v[196:199], v[68:71]\n" \
"buffer_store_dwordx4 a[64:67], v249, s[56:59], s90 offen offset:0 sc1\n" \
"s_cmp_eq_u32 s63, 4\n" \
"s_cselect_b64 s[40:41], s[20:21], s[40:41]\n" \
"s_cselect_b32 s80, 8, s80\n" \
"v_mfma_f32_16x16x32_bf16 v[72:75], v[16:19], v[192:195], v[72:75]\n" \
"ds_read_b64_tr_b16 v[34:35], v232 offset:3072\n" \
"v_mfma_f32_16x16x32_bf16 v[72:75], v[20:23], v[196:199], v[72:75]\n" \
"s_waitcnt lgkmcnt(3)\n" \
"ds_read_b64_tr_b16 v[36:37], v232 offset:9216\n" \
"v_mfma_f32_16x16x32_bf16 v[76:79], v[24:27], v[192:195], v[76:79]\n" \
"ds_read_b128 a[64:67], v254\n" \
"ds_read_b64_tr_b16 v[38:39], v232 offset:11264\n" \
"v_mfma_f32_16x16x32_bf16 v[76:79], v[28:31], v[196:199], v[76:79]\n" \
"s_cmp_eq_u32 s63, 2\n" \
"s_cselect_b64 s[40:41], s[24:25], s[40:41]\n" \
"s_cselect_b32 s80, 4, s80\n" \
"v_mfma_f32_16x16x32_bf16 v[108:111], v[24:27], v[200:203], v[108:111]\n" \
"s_add_u32 s81, s81, s62\n" \
"s_lshl_b32 s81, s81, 2\n" \
"ds_read_b64_tr_b16 v[40:41], v233 offset:1024\n" \
"v_mfma_f32_16x16x32_bf16 v[108:111], v[28:31], v[204:207], v[108:111]\n" \
"s_add_u32 s81, s81, s61\n" \
"s_lshl_b32 s81, s81, 12\n" \
"ds_read_b64_tr_b16 v[42:43], v233 offset:3072\n" \
"v_mfma_f32_16x16x32_bf16 v[104:107], v[16:19], v[200:203], v[104:107]\n" \
"s_add_u32 s40, s40, s81\n" \
"s_addc_u32 s41, s41, 0\n" \
"ds_read_b64_tr_b16 v[44:45], v233 offset:9216\n" \
"v_mfma_f32_16x16x32_bf16 v[104:107], v[20:23], v[204:207], v[104:107]\n" \
"s_min_u32 s81, s80, 1\n" \
"s_lshl_b32 s81, s81, 2\n" \
"v_mul_u32_u24 v252, s81, v248\n" \
"v_mfma_f32_16x16x32_bf16 v[100:103], v[8:11], v[200:203], v[100:103]\n" \
"ds_read_b128 a[68:71], v254\n" \
"ds_read_b64_tr_b16 v[46:47], v233 offset:11264\n" \
"v_mfma_f32_16x16x32_bf16 v[100:103], v[12:15], v[204:207], v[100:103]\n" \
"ds_read_b64_tr_b16 v[48:49], v232 offset:1536\n" \
"v_mfma_f32_16x16x32_bf16 v[96:99], v[0:3], v[200:203], v[96:99]\n" \
"s_add_u32 m0, s95, 53248\n" \
"ds_read_b64_tr_b16 v[50:51], v232 offset:3584\n" \
"v_mfma_f32_16x16x32_bf16 v[96:99], v[4:7], v[204:207], v[96:99]\n" \
"global_load_lds_dwordx4 v228, s[44:45] offset:-4096\n" \
"v_mfma_f32_16x16x32_bf16 v[128:131], v[0:3], v[208:211], v[128:131]\n" \
"ds_read_b64_tr_b16 v[52:53], v232 offset:9728\n" \
"v_mfma_f32_16x16x32_bf16 v[128:131], v[4:7], v[212:215], v[128:131]\n" \
"ds_read_b64_tr_b16 v[54:55], v232 offset:11776\n" \
"v_mfma_f32_16x16x32_bf16 v[132:135], v[8:11], v[208:211], v[132:135]\n" \
"ds_read_b64_tr_b16 v[56:57], v233 offset:1536\n" \
"v_mfma_f32_16x16x32_bf16 v[132:135], v[12:15], v[212:215], v[132:135]\n" \
"global_load_lds_dwordx4 v229, s[44:45] offset:-3072\n" \
"v_mfma_f32_16x16x32_bf16 v[136:139], v[16:19], v[208:211], v[136:139]\n" \
"ds_read_b64_tr_b16 v[58:59], v233 offset:3584\n" \
"v_mfma_f32_16x16x32_bf16 v[136:139], v[20:23], v[212:215], v[136:139]\n" \
"ds_read_b64_tr_b16 v[60:61], v233 offset:9728\n" \
"v_mfma_f32_16x16x32_bf16 v[140:143], v[24:27], v[208:211], v[140:143]\n" \
"ds_read_b64_tr_b16 v[62:63], v233 offset:11776\n" \
"v_mfma_f32_16x16x32_bf16 v[140:143], v[28:31], v[212:215], v[140:143]\n" \
"s_waitcnt lgkmcnt(3)\n" \
"global_load_lds_dwordx4 v228, s[42:43] offset:-2048\n" \
"v_mfma_f32_16x16x32_bf16 v[172:175], v[24:27], v[216:219], v[172:175]\n" \
"v_add_u32 v230, 0xffff0000, v230\n" \
"v_add_u32 v231, 0xffff0000, v231\n" \
"v_mfma_f32_16x16x32_bf16 v[172:175], v[28:31], v[220:223], v[172:175]\n" \
"v_add_u32 v232, 0x4000, v232\n" \
"v_add_u32 v233, 0x4000, v233\n" \
"v_mfma_f32_16x16x32_bf16 v[168:171], v[16:19], v[216:219], v[168:171]\n" \
"v_mfma_f32_16x16x32_bf16 v[168:171], v[20:23], v[220:223], v[168:171]\n" \
"global_load_lds_dwordx4 v229, s[42:43] offset:-1024\n" \
"v_mfma_f32_16x16x32_bf16 v[164:167], v[8:11], v[216:219], v[164:167]\n" \
"v_mfma_f32_16x16x32_bf16 v[164:167], v[12:15], v[220:223], v[164:167]\n" \
"s_waitcnt vmcnt(20) lgkmcnt(0)\n" \
"v_mfma_f32_16x16x32_bf16 v[160:163], v[0:3], v[216:219], v[160:163]\n" \
"v_mfma_f32_16x16x32_bf16 v[160:163], v[4:7], v[220:223], v[160:163]\n" \
"s_barrier\n" \
"v_mfma_f32_16x16x32_bf16 v[80:83], v[32:35], v[192:195], v[80:83]\n" \
"s_waitcnt vmcnt(17)\n" \
"ds_read_b64_tr_b16 v[0:1], v232 offset:0\n" \
"v_cmp_ne_u32 vcc, s60, v244\n" \
"v_mfma_f32_16x16x32_bf16 v[80:83], v[36:39], v[196:199], v[80:83]\n" \
"ds_read_b64_tr_b16 v[2:3], v232 offset:2048\n" \
"v_mfma_f32_16x16x32_bf16 v[84:87], v[40:43], v[192:195], v[84:87]\n" \
"s_add_u32 m0, s87, 135168\n" \
"ds_read_b64_tr_b16 v[4:5], v232 offset:8192\n" \
"v_mfma_f32_16x16x32_bf16 v[84:87], v[44:47], v[196:199], v[84:87]\n" \
"s_and_b32 vcc_hi, vcc_lo, 255\n" \
"s_cbranch_scc1 Lpl1_%=\n" \
"Lpr1_%=:\n" \
"global_load_lds_dwordx4 v224, s[48:49] offset:-4096\n" \
"v_mfma_f32_16x16x32_bf16 v[88:91], v[48:51], v[192:195], v[88:91]\n" \
"s_add_u32 s79, s91, 12288\n" \
"ds_read_b64_tr_b16 v[6:7], v232 offset:10240\n" \
"v_mfma_f32_16x16x32_bf16 v[88:91], v[52:55], v[196:199], v[88:91]\n" \
"ds_read_b64_tr_b16 v[8:9], v233 offset:0\n" \
"v_mfma_f32_16x16x32_bf16 v[92:95], v[56:59], v[192:195], v[92:95]\n" \
"ds_read_b64_tr_b16 v[10:11], v233 offset:2048\n" \
"v_mfma_f32_16x16x32_bf16 v[92:95], v[60:63], v[196:199], v[92:95]\n" \
"global_load_lds_dwordx4 v225, s[48:49] offset:-3072\n" \
"v_mfma_f32_16x16x32_bf16 v[124:127], v[56:59], v[200:203], v[124:127]\n" \
"ds_read_b128 v[192:195], v230 offset:0\n" \
"v_mfma_f32_16x16x32_bf16 v[124:127], v[60:63], v[204:207], v[124:127]\n" \
"ds_read_b128 v[196:199], v231 offset:0\n" \
"v_mfma_f32_16x16x32_bf16 v[120:123], v[48:51], v[200:203], v[120:123]\n" \
"ds_read_b64_tr_b16 v[12:13], v233 offset:8192\n" \
"v_mfma_f32_16x16x32_bf16 v[120:123], v[52:55], v[204:207], v[120:123]\n" \
"global_load_lds_dwordx4 v226, s[48:49] offset:-2048\n" \
"v_mfma_f32_16x16x32_bf16 v[116:119], v[40:43], v[200:203], v[116:119]\n" \
"ds_read_b64_tr_b16 v[14:15], v233 offset:10240\n" \
"v_mfma_f32_16x16x32_bf16 v[116:119], v[44:47], v[204:207], v[116:119]\n" \
"ds_read_b64_tr_b16 v[16:17], v232 offset:512\n" \
"v_mfma_f32_16x16x32_bf16 v[112:115], v[32:35], v[200:203], v[112:115]\n" \
"ds_read_b64_tr_b16 v[18:19], v232 offset:2560\n" \
"v_mfma_f32_16x16x32_bf16 v[112:115], v[36:39], v[204:207], v[112:115]\n" \
"global_load_lds_dwordx4 v227, s[48:49] offset:-1024\n" \
"v_mfma_f32_16x16x32_bf16 v[144:147], v[32:35], v[208:211], v[144:147]\n" \
"ds_read_b128 v[200:203], v230 offset:2048\n" \
"v_mfma_f32_16x16x32_bf16 v[144:147], v[36:39], v[212:215], v[144:147]\n" \
"ds_read_b128 v[204:207], v231 offset:2048\n" \
"v_mfma_f32_16x16x32_bf16 v[148:151], v[40:43], v[208:211], v[148:151]\n" \
"ds_read_b64_tr_b16 v[20:21], v232 offset:8704\n" \
"v_mfma_f32_16x16x32_bf16 v[148:151], v[44:47], v[212:215], v[148:151]\n" \
"global_load_lds_dwordx4 v224, s[46:47] offset:0\n" \
"v_mfma_f32_16x16x32_bf16 v[152:155], v[48:51], v[208:211], v[152:155]\n" \
"ds_read_b64_tr_b16 v[22:23], v232 offset:10752\n" \
"v_mfma_f32_16x16x32_bf16 v[152:155], v[52:55], v[212:215], v[152:155]\n" \
"ds_read_b64_tr_b16 v[24:25], v233 offset:512\n" \
"v_mfma_f32_16x16x32_bf16 v[156:159], v[56:59], v[208:211], v[156:159]\n" \
"ds_read_b64_tr_b16 v[26:27], v233 offset:2560\n" \
"v_mfma_f32_16x16x32_bf16 v[156:159], v[60:63], v[212:215], v[156:159]\n" \
"global_load_lds_dwordx4 v225, s[46:47] offset:1024\n" \
"v_mfma_f32_16x16x32_bf16 v[188:191], v[56:59], v[216:219], v[188:191]\n" \
"ds_read_b128 v[208:211], v230 offset:4096\n" \
"v_mfma_f32_16x16x32_bf16 v[188:191], v[60:63], v[220:223], v[188:191]\n" \
"ds_read_b128 v[212:215], v231 offset:4096\n" \
"v_mfma_f32_16x16x32_bf16 v[184:187], v[48:51], v[216:219], v[184:187]\n" \
"ds_read_b64_tr_b16 v[28:29], v233 offset:8704\n" \
"v_mfma_f32_16x16x32_bf16 v[184:187], v[52:55], v[220:223], v[184:187]\n" \
"global_load_lds_dwordx4 v226, s[46:47] offset:2048\n" \
"v_mfma_f32_16x16x32_bf16 v[180:183], v[40:43], v[216:219], v[180:183]\n" \
"ds_read_b64_tr_b16 v[30:31], v233 offset:10752\n" \
"v_mfma_f32_16x16x32_bf16 v[180:183], v[44:47], v[220:223], v[180:183]\n" \
"v_mfma_f32_16x16x32_bf16 v[176:179], v[32:35], v[216:219], v[176:179]\n" \
"v_mfma_f32_16x16x32_bf16 v[176:179], v[36:39], v[220:223], v[176:179]\n" \
"s_waitcnt lgkmcnt(1)\n" \
"global_load_lds_dwordx4 v227, s[46:47] offset:3072\n" \
"v_mfma_f32_16x16x32_bf16 v[64:67], v[0:3], v[192:195], v[64:67]\n" \
"ds_read_b128 v[216:219], v230 offset:6144\n" \
"v_mfma_f32_16x16x32_bf16 v[64:67], v[4:7], v[196:199], v[64:67]\n" \
"ds_read_b128 v[220:223], v231 offset:6144\n" \
"v_mfma_f32_16x16x32_bf16 v[68:71], v[8:11], v[192:195], v[68:71]\n" \
"ds_read_b128 v[240:243], v254\n" \
"ds_read_b64_tr_b16 v[32:33], v232 offset:1024\n" \
"v_mfma_f32_16x16x32_bf16 v[68:71], v[12:15], v[196:199], v[68:71]\n" \
"buffer_store_dwordx4 a[96:99], v249, s[56:59], s90 offen offset:256 sc1\n" \
"v_and_b32 v238, 0xf0, v248\n" \
"v_mov_b32 v239, 0x3f803f80\n" \
"v_cmp_eq_u32 vcc, 32, v238\n" \
"v_cmp_eq_u32 s[64:65], 48, v238\n" \
"v_mfma_f32_16x16x32_bf16 v[72:75], v[16:19], v[192:195], v[72:75]\n" \
"ds_read_b64_tr_b16 v[34:35], v232 offset:3072\n" \
"v_mov_b32 v244, 0\n" \
"v_mfma_f32_16x16x32_bf16 v[72:75], v[20:23], v[196:199], v[72:75]\n" \
"s_waitcnt lgkmcnt(4)\n" \
"ds_read_b64_tr_b16 v[36:37], v232 offset:9216\n" \
"v_mov_b32 v245, 0\n" \
"v_mfma_f32_16x16x32_bf16 v[76:79], v[24:27], v[192:195], v[76:79]\n" \
"ds_read_b128 a[96:99], v254\n" \
"ds_read_b64_tr_b16 v[38:39], v232 offset:11264\n" \
"v_mfma_f32_16x16x32_bf16 v[76:79], v[28:31], v[196:199], v[76:79]\n" \
"buffer_store_dwordx4 a[72:75], v249, s[56:59], s90 offen offset:2048 sc1\n" \
"ds_read_b128 a[100:103], v254\n" \
"v_cndmask_b32 v238, 0, v239, vcc\n" \
"v_cndmask_b32 v239, 0, v239, s[64:65]\n" \
"v_mfma_f32_16x16x32_bf16 v[108:111], v[24:27], v[200:203], v[108:111]\n" \
"ds_read_b64_tr_b16 v[40:41], v233 offset:1024\n" \
"v_mfma_f32_16x16x32_bf16 v[108:111], v[28:31], v[204:207], v[108:111]\n" \
"ds_read_b64_tr_b16 v[42:43], v233 offset:3072\n" \
"v_mfma_f32_16x16x32_bf16 v[104:107], v[16:19], v[200:203], v[104:107]\n" \
"ds_read_b128 a[72:75], v254\n" \
"ds_read_b64_tr_b16 v[44:45], v233 offset:9216\n" \
"v_mfma_f32_16x16x32_bf16 v[104:107], v[20:23], v[204:207], v[104:107]\n" \
"ds_read_b128 a[76:79], v254\n" \
"v_mfma_f32_16x16x32_bf16 v[100:103], v[8:11], v[200:203], v[100:103]\n" \
"ds_read_b64_tr_b16 v[46:47], v233 offset:11264\n" \
"v_mfma_f32_16x16x32_bf16 v[100:103], v[12:15], v[204:207], v[100:103]\n" \
"ds_read_b64_tr_b16 v[48:49], v232 offset:1536\n" \
"v_mfma_f32_16x16x32_bf16 v[96:99], v[0:3], v[200:203], v[96:99]\n" \
"s_add_u32 m0, s95, 0\n" \
"ds_read_b64_tr_b16 v[50:51], v232 offset:3584\n" \
"v_mfma_f32_16x16x32_bf16 v[96:99], v[4:7], v[204:207], v[96:99]\n" \
"global_load_lds_dwordx4 v250, s[36:37] offset:0 sc1\n" \
"v_mfma_f32_16x16x32_bf16 v[128:131], v[0:3], v[208:211], v[128:131]\n" \
"ds_read_b64_tr_b16 v[52:53], v232 offset:9728\n" \
"v_mfma_f32_16x16x32_bf16 v[128:131], v[4:7], v[212:215], v[128:131]\n" \
"ds_read_b64_tr_b16 v[54:55], v232 offset:11776\n" \
"v_mfma_f32_16x16x32_bf16 v[132:135], v[8:11], v[208:211], v[132:135]\n" \
"ds_read_b64_tr_b16 v[56:57], v233 offset:1536\n" \
"v_mfma_f32_16x16x32_bf16 v[132:135], v[12:15], v[212:215], v[132:135]\n" \
"global_load_lds_dwordx4 v250, s[36:37] offset:1024 sc1\n" \
"v_mfma_f32_16x16x32_bf16 v[136:139], v[16:19], v[208:211], v[136:139]\n" \
"ds_read_b64_tr_b16 v[58:59], v233 offset:3584\n" \
"v_mfma_f32_16x16x32_bf16 v[136:139], v[20:23], v[212:215], v[136:139]\n" \
"ds_read_b64_tr_b16 v[60:61], v233 offset:9728\n" \
"v_mfma_f32_16x16x32_bf16 v[140:143], v[24:27], v[208:211], v[140:143]\n" \
"ds_read_b64_tr_b16 v[62:63], v233 offset:11776\n" \
"v_mfma_f32_16x16x32_bf16 v[140:143], v[28:31], v[212:215], v[140:143]\n" \
"s_waitcnt lgkmcnt(3)\n" \
"global_load_lds_dwordx4 v250, s[36:37] offset:2048 sc1\n" \
"v_mfma_f32_16x16x32_bf16 v[172:175], v[24:27], v[216:219], v[172:175]\n" \
"v_add_u32 v230, 0x8000, v230\n" \
"v_add_u32 v231, 0x8000, v231\n" \
"v_mfma_f32_16x16x32_bf16 v[172:175], v[28:31], v[220:223], v[172:175]\n" \
"v_add_u32 v232, 0x4000, v232\n" \
"v_add_u32 v233, 0x4000, v233\n" \
"v_mfma_f32_16x16x32_bf16 v[168:171], v[16:19], v[216:219], v[168:171]\n" \
"v_mfma_f32_16x16x32_bf16 v[168:171], v[20:23], v[220:223], v[168:171]\n" \
"global_load_lds_dwordx4 v250, s[36:37] offset:3072 sc1\n" \
"v_mfma_f32_16x16x32_bf16 v[164:167], v[8:11], v[216:219], v[164:167]\n" \
"s_add_u32 s36, s36, 17408\n" \
"v_mfma_f32_16x16x32_bf16 v[164:167], v[12:15], v[220:223], v[164:167]\n" \
"s_waitcnt vmcnt(19) lgkmcnt(0)\n" \
"v_mfma_f32_16x16x32_bf16 v[160:163], v[0:3], v[216:219], v[160:163]\n" \
"v_mfma_f32_16x16x32_bf16 v[160:163], v[4:7], v[220:223], v[160:163]\n" \
"s_barrier\n" \
"v_mfma_f32_16x16x32_bf16 v[80:83], v[32:35], v[192:195], v[80:83]\n" \
"ds_read_b64_tr_b16 v[0:1], v232 offset:0\n" \
"v_mfma_f32_16x16x32_bf16 v[80:83], v[36:39], v[196:199], v[80:83]\n" \
"ds_read_b64_tr_b16 v[2:3], v232 offset:2048\n" \
"v_mfma_f32_16x16x32_bf16 v[84:87], v[40:43], v[192:195], v[84:87]\n" \
"s_add_u32 m0, s95, 16384\n" \
"ds_read_b64_tr_b16 v[4:5], v232 offset:8192\n" \
"v_mfma_f32_16x16x32_bf16 v[84:87], v[44:47], v[196:199], v[84:87]\n" \
"global_load_lds_dwordx4 v250, s[36:37] offset:0 sc1\n" \
"v_mfma_f32_16x16x32_bf16 v[88:91], v[48:51], v[192:195], v[88:91]\n" \
"ds_read_b64_tr_b16 v[6:7], v232 offset:10240\n" \
"v_mfma_f32_16x16x32_bf16 v[88:91], v[52:55], v[196:199], v[88:91]\n" \
"ds_read_b64_tr_b16 v[8:9], v233 offset:0\n" \
"v_mfma_f32_16x16x32_bf16 v[92:95], v[56:59], v[192:195], v[92:95]\n" \
"ds_read_b64_tr_b16 v[10:11], v233 offset:2048\n" \
"v_mfma_f32_16x16x32_bf16 v[92:95], v[60:63], v[196:199], v[92:95]\n" \
"global_load_lds_dwordx4 v250, s[36:37] offset:1024 sc1\n" \
"v_mfma_f32_16x16x32_bf16 v[124:127], v[56:59], v[200:203], v[124:127]\n" \
"ds_read_b128 v[192:195], v230 offset:0\n" \
"v_mfma_f32_16x16x32_bf16 v[124:127], v[60:63], v[204:207], v[124:127]\n" \
"ds_read_b128 v[196:199], v231 offset:0\n" \
"v_mfma_f32_16x16x32_bf16 v[120:123], v[48:51], v[200:203], v[120:123]\n" \
"ds_read_b64_tr_b16 v[12:13], v233 offset:8192\n" \
"v_mfma_f32_16x16x32_bf16 v[120:123], v[52:55], v[204:207], v[120:123]\n" \
"global_load_lds_dwordx4 v250, s[36:37] offset:2048 sc1\n" \
"v_mfma_f32_16x16x32_bf16 v[116:119], v[40:43], v[200:203], v[116:119]\n" \
"ds_read_b64_tr_b16 v[14:15], v233 offset:10240\n" \
"v_mfma_f32_16x16x32_bf16 v[116:119], v[44:47], v[204:207], v[116:119]\n" \
"ds_read_b64_tr_b16 v[16:17], v232 offset:512\n" \
"v_mfma_f32_16x16x32_bf16 v[112:115], v[32:35], v[200:203], v[112:115]\n" \
"ds_read_b64_tr_b16 v[18:19], v232 offset:2560\n" \
"v_mfma_f32_16x16x32_bf16 v[112:115], v[36:39], v[204:207], v[112:115]\n" \
"global_load_lds_dwordx4 v250, s[36:37] offset:3072 sc1\n" \
"v_mfma_f32_16x16x32_bf16 v[144:147], v[32:35], v[208:211], v[144:147]\n" \
"ds_read_b128 v[200:203], v230 offset:2048\n" \
"s_add_u32 s36, s36, 17408\n" \
"v_mfma_f32_16x16x32_bf16 v[144:147], v[36:39], v[212:215], v[144:147]\n" \
"ds_read_b128 v[204:207], v231 offset:2048\n" \
"v_mfma_f32_16x16x32_bf16 v[148:151], v[40:43], v[208:211], v[148:151]\n" \
"ds_read_b64_tr_b16 v[20:21], v232 offset:8704\n" \
"v_mfma_f32_16x16x32_bf16 v[148:151], v[44:47], v[212:215], v[148:151]\n" \
"buffer_store_dwordx4 a[104:107], v249, s[56:59], s90 offen offset:2304 sc1\n" \
"v_mfma_f32_16x16x32_bf16 v[152:155], v[48:51], v[208:211], v[152:155]\n" \
"s_add_u32 s90, s89, 278528\n" \
"ds_read_b64_tr_b16 v[22:23], v232 offset:10752\n" \
"v_mfma_f32_16x16x32_bf16 v[152:155], v[52:55], v[212:215], v[152:155]\n" \
"ds_read_b64_tr_b16 v[24:25], v233 offset:512\n" \
"v_mfma_f32_16x16x32_bf16 v[156:159], v[56:59], v[208:211], v[156:159]\n" \
"ds_read_b128 a[104:107], v254\n" \
"ds_read_b64_tr_b16 v[26:27], v233 offset:2560\n" \
"v_mfma_f32_16x16x32_bf16 v[156:159], v[60:63], v[212:215], v[156:159]\n" \
"buffer_store_dwordx4 a[80:83], v248, s[56:59], s90 offen offset:0 sc1\n" \
"ds_read_b128 a[108:111], v254\n" \
"v_mfma_f32_16x16x32_bf16 v[188:191], v[56:59], v[216:219], v[188:191]\n" \
"ds_read_b128 v[208:211], v230 offset:4096\n" \
"v_mfma_f32_16x16x32_bf16 v[188:191], v[60:63], v[220:223], v[188:191]\n" \
"ds_read_b128 v[212:215], v231 offset:4096\n" \
"v_mfma_f32_16x16x32_bf16 v[184:187], v[48:51], v[216:219], v[184:187]\n" \
"ds_read_b128 a[80:83], v254\n" \
"ds_read_b64_tr_b16 v[28:29], v233 offset:8704\n" \
"v_mfma_f32_16x16x32_bf16 v[184:187], v[52:55], v[220:223], v[184:187]\n" \
"buffer_store_dwordx4 a[84:87], v248, s[56:59], s90 offen offset:1024 sc1\n" \
"ds_read_b128 a[112:115], v254\n" \
"v_mfma_f32_16x16x32_bf16 v[180:183], v[40:43], v[216:219], v[180:183]\n" \
"ds_read_b64_tr_b16 v[30:31], v233 offset:10752\n" \
"v_mfma_f32_16x16x32_bf16 v[180:183], v[44:47], v[220:223], v[180:183]\n" \
"v_mfma_f32_16x16x32_bf16 v[176:179], v[32:35], v[216:219], v[176:179]\n" \
"ds_read_b128 a[84:87], v254\n" \
"v_mfma_f32_16x16x32_bf16 v[176:179], v[36:39], v[220:223], v[176:179]\n" \
"s_waitcnt lgkmcnt(3)\n" \
"ds_read_b128 a[116:119], v254\n" \
"v_mfma_f32_16x16x32_bf16 v[64:67], v[0:3], v[192:195], v[64:67]\n" \
"ds_read_b128 v[216:219], v230 offset:6144\n" \
"v_mfma_f32_16x16x32_bf16 v[64:67], v[4:7], v[196:199], v[64:67]\n" \
"ds_read_b128 v[220:223], v231 offset:6144\n" \
"v_mfma_f32_16x16x32_bf16 v[68:71], v[8:11], v[192:195], v[68:71]\n" \
"ds_read_b64_tr_b16 v[32:33], v232 offset:1024\n" \
"v_mfma_f32_16x16x32_bf16 v[68:71], v[12:15], v[196:199], v[68:71]\n" \
"v_mfma_f32_16x16x32_bf16 v[72:75], v[16:19], v[192:195], v[72:75]\n" \
"ds_read_b64_tr_b16 v[34:35], v232 offset:3072\n" \
"v_mfma_f32_16x16x32_bf16 v[72:75], v[20:23], v[196:199], v[72:75]\n" \
"s_waitcnt lgkmcnt(3)\n" \
"ds_read_b64_tr_b16 v[36:37], v232 offset:9216\n" \
"v_mfma_f32_16x16x32_bf16 v[76:79], v[24:27], v[192:195], v[76:79]\n" \
"ds_read_b64_tr_b16 v[38:39], v232 offset:11264\n" \
"v_mfma_f32_16x16x32_bf16 v[76:79], v[28:31], v[196:199], v[76:79]\n" \
"v_mfma_f32_16x16x32_bf16 v[108:111], v[24:27], v[200:203], v[108:111]\n" \
"ds_read_b64_tr_b16 v[40:41], v233 offset:1024\n" \
"v_mfma_f32_16x16x32_bf16 v[108:111], v[28:31], v[204:207], v[108:111]\n" \
"ds_read_b64_tr_b16 v[42:43], v233 offset:3072\n" \
"v_mfma_f32_16x16x32_bf16 v[104:107], v[16:19], v[200:203], v[104:107]\n" \
"ds_read_b64_tr_b16 v[44:45], v233 offset:9216\n" \
"v_mfma_f32_16x16x32_bf16 v[104:107], v[20:23], v[204:207], v[104:107]\n" \
"v_mfma_f32_16x16x32_bf16 v[100:103], v[8:11], v[200:203], v[100:103]\n" \
"ds_read_b64_tr_b16 v[46:47], v233 offset:11264\n" \
"v_mfma_f32_16x16x32_bf16 v[100:103], v[12:15], v[204:207], v[100:103]\n" \
"ds_read_b64_tr_b16 v[48:49], v232 offset:1536\n" \
"v_mfma_f32_16x16x32_bf16 v[96:99], v[0:3], v[200:203], v[96:99]\n" \
"ds_read_b64_tr_b16 v[50:51], v232 offset:3584\n" \
"v_mfma_f32_16x16x32_bf16 v[96:99], v[4:7], v[204:207], v[96:99]\n" \
"v_mfma_f32_16x16x32_bf16 v[128:131], v[0:3], v[208:211], v[128:131]\n" \
"ds_read_b64_tr_b16 v[52:53], v232 offset:9728\n" \
"v_mfma_f32_16x16x32_bf16 v[128:131], v[4:7], v[212:215], v[128:131]\n" \
"ds_read_b64_tr_b16 v[54:55], v232 offset:11776\n" \
"v_mfma_f32_16x16x32_bf16 v[132:135], v[8:11], v[208:211], v[132:135]\n" \
"ds_read_b64_tr_b16 v[56:57], v233 offset:1536\n" \
"v_mfma_f32_16x16x32_bf16 v[132:135], v[12:15], v[212:215], v[132:135]\n" \
"v_mfma_f32_16x16x32_bf16 v[136:139], v[16:19], v[208:211], v[136:139]\n" \
"ds_read_b64_tr_b16 v[58:59], v233 offset:3584\n" \
"v_mfma_f32_16x16x32_bf16 v[136:139], v[20:23], v[212:215], v[136:139]\n" \
"ds_read_b64_tr_b16 v[60:61], v233 offset:9728\n" \
"v_mfma_f32_16x16x32_bf16 v[140:143], v[24:27], v[208:211], v[140:143]\n" \
"ds_read_b64_tr_b16 v[62:63], v233 offset:11776\n" \
"v_mfma_f32_16x16x32_bf16 v[140:143], v[28:31], v[212:215], v[140:143]\n" \
"s_waitcnt lgkmcnt(3)\n" \
"v_mfma_f32_16x16x32_bf16 v[172:175], v[24:27], v[216:219], v[172:175]\n" \
"v_add_u32 v230, 0x8000, v230\n" \
"v_add_u32 v231, 0x8000, v231\n" \
"v_mfma_f32_16x16x32_bf16 v[172:175], v[28:31], v[220:223], v[172:175]\n" \
"v_add_u32 v232, 0x4000, v232\n" \
"v_add_u32 v233, 0x4000, v233\n" \
"v_mfma_f32_16x16x32_bf16 v[168:171], v[16:19], v[216:219], v[168:171]\n" \
"v_mfma_f32_16x16x32_bf16 v[168:171], v[20:23], v[220:223], v[168:171]\n" \
"v_mfma_f32_16x16x32_bf16 v[164:167], v[8:11], v[216:219], v[164:167]\n" \
"v_mfma_f32_16x16x32_bf16 v[164:167], v[12:15], v[220:223], v[164:167]\n" \
"s_waitcnt vmcnt(13) lgkmcnt(0)\n" \
"v_mfma_f32_16x16x32_bf16 v[160:163], v[0:3], v[216:219], v[160:163]\n" \
"v_mfma_f32_16x16x32_bf16 v[160:163], v[4:7], v[220:223], v[160:163]\n" \
"s_barrier\n" \
"v_mfma_f32_16x16x32_bf16 v[80:83], v[32:35], v[192:195], v[80:83]\n" \
"ds_read_b64_tr_b16 v[0:1], v232 offset:0\n" \
"v_mfma_f32_16x16x32_bf16 v[80:83], v[36:39], v[196:199], v[80:83]\n" \
"ds_read_b64_tr_b16 v[2:3], v232 offset:2048\n" \
"v_mfma_f32_16x16x32_bf16 v[84:87], v[40:43], v[192:195], v[84:87]\n" \
"s_add_u32 m0, s95, 32768\n" \
"ds_read_b64_tr_b16 v[4:5], v232 offset:8192\n" \
"v_mfma_f32_16x16x32_bf16 v[84:87], v[44:47], v[196:199], v[84:87]\n" \
"global_load_lds_dwordx4 v250, s[36:37] offset:0 sc1\n" \
"v_mfma_f32_16x16x32_bf16 v[88:91], v[48:51], v[192:195], v[88:91]\n" \
"ds_read_b64_tr_b16 v[6:7], v232 offset:10240\n" \
"v_mfma_f32_16x16x32_bf16 v[88:91], v[52:55], v[196:199], v[88:91]\n" \
"ds_read_b64_tr_b16 v[8:9], v233 offset:0\n" \
"v_mfma_f32_16x16x32_bf16 v[92:95], v[56:59], v[192:195], v[92:95]\n" \
"ds_read_b64_tr_b16 v[10:11], v233 offset:2048\n" \
"v_mfma_f32_16x16x32_bf16 v[92:95], v[60:63], v[196:199], v[92:95]\n" \
"global_load_lds_dwordx4 v250, s[36:37] offset:1024 sc1\n" \
"v_mfma_f32_16x16x32_bf16 v[124:127], v[56:59], v[200:203], v[124:127]\n" \
"ds_read_b128 v[192:195], v230 offset:0\n" \
"v_mfma_f32_16x16x32_bf16 v[124:127], v[60:63], v[204:207], v[124:127]\n" \
"ds_read_b128 v[196:199], v231 offset:0\n" \
"v_mfma_f32_16x16x32_bf16 v[120:123], v[48:51], v[200:203], v[120:123]\n" \
"ds_read_b64_tr_b16 v[12:13], v233 offset:8192\n" \
"v_mfma_f32_16x16x32_bf16 v[120:123], v[52:55], v[204:207], v[120:123]\n" \
"global_load_lds_dwordx4 v250, s[36:37] offset:2048 sc1\n" \
"v_mfma_f32_16x16x32_bf16 v[116:119], v[40:43], v[200:203], v[116:119]\n" \
"ds_read_b64_tr_b16 v[14:15], v233 offset:10240\n" \
"v_mfma_f32_16x16x32_bf16 v[116:119], v[44:47], v[204:207], v[116:119]\n" \
"ds_read_b64_tr_b16 v[16:17], v232 offset:512\n" \
"v_mfma_f32_16x16x32_bf16 v[112:115], v[32:35], v[200:203], v[112:115]\n" \
"ds_read_b64_tr_b16 v[18:19], v232 offset:2560\n" \
"v_mfma_f32_16x16x32_bf16 v[112:115], v[36:39], v[204:207], v[112:115]\n" \
"global_load_lds_dwordx4 v250, s[36:37] offset:3072 sc1\n" \
"v_mfma_f32_16x16x32_bf16 v[144:147], v[32:35], v[208:211], v[144:147]\n" \
"ds_read_b128 v[200:203], v230 offset:2048\n" \
"s_add_u32 s36, s36, 17408\n" \
"v_mfma_f32_16x16x32_bf16 v[144:147], v[36:39], v[212:215], v[144:147]\n" \
"ds_read_b128 v[204:207], v231 offset:2048\n" \
"v_mfma_f32_16x16x32_bf16 v[148:151], v[40:43], v[208:211], v[148:151]\n" \
"ds_read_b64_tr_b16 v[20:21], v232 offset:8704\n" \
"v_mfma_f32_16x16x32_bf16 v[148:151], v[44:47], v[212:215], v[148:151]\n" \
"buffer_store_dwordx4 a[88:91], v248, s[56:59], s90 offen offset:2048 sc1\n" \
"v_mfma_f32_16x16x32_bf16 v[152:155], v[48:51], v[208:211], v[152:155]\n" \
"ds_read_b64_tr_b16 v[22:23], v232 offset:10752\n" \
"v_mfma_f32_16x16x32_bf16 v[152:155], v[52:55], v[212:215], v[152:155]\n" \
"ds_read_b64_tr_b16 v[24:25], v233 offset:512\n" \
"v_mfma_f32_16x16x32_bf16 v[156:159], v[56:59], v[208:211], v[156:159]\n" \
"ds_read_b128 a[88:91], v254\n" \
"ds_read_b64_tr_b16 v[26:27], v233 offset:2560\n" \
"v_mfma_f32_16x16x32_bf16 v[156:159], v[60:63], v[212:215], v[156:159]\n" \
"buffer_store_dwordx4 a[92:95], v248, s[56:59], s90 offen offset:3072 sc1\n" \
"v_mfma_f32_16x16x32_bf16 v[188:191], v[56:59], v[216:219], v[188:191]\n" \
"ds_read_b128 v[208:211], v230 offset:4096\n" \
"s_add_u32 s90, s88, 417792\n" \
"v_mfma_f32_16x16x32_bf16 v[188:191], v[60:63], v[220:223], v[188:191]\n" \
"ds_read_b128 v[212:215], v231 offset:4096\n" \
"v_mfma_f32_16x16x32_bf16 v[184:187], v[48:51], v[216:219], v[184:187]\n" \
"ds_read_b128 a[92:95], v254\n" \
"ds_read_b64_tr_b16 v[28:29], v233 offset:8704\n" \
"v_mfma_f32_16x16x32_bf16 v[184:187], v[52:55], v[220:223], v[184:187]\n" \
"buffer_store_dwordx4 a[0:3], v249, s[56:59], s90 offen offset:0 sc1\n" \
"v_accvgpr_write_b32 a120, v238\n" \
"v_accvgpr_write_b32 a121, v238\n" \
"v_accvgpr_write_b32 a122, v238\n" \
"v_accvgpr_write_b32 a123, v238\n" \
"v_mfma_f32_16x16x32_bf16 v[180:183], v[40:43], v[216:219], v[180:183]\n" \
"ds_read_b64_tr_b16 v[30:31], v233 offset:10752\n" \
"v_accvgpr_write_b32 a124, v239\n" \
"v_mfma_f32_16x16x32_bf16 v[180:183], v[44:47], v[220:223], v[180:183]\n" \
"v_accvgpr_write_b32 a125, v239\n" \
"v_accvgpr_write_b32 a126, v239\n" \
"v_mfma_f32_16x16x32_bf16 v[176:179], v[32:35], v[216:219], v[176:179]\n" \
"ds_read_b128 a[0:3], v254\n" \
"v_accvgpr_write_b32 a127, v239\n" \
"v_mfma_f32_16x16x32_bf16 v[176:179], v[36:39], v[220:223], v[176:179]\n" \
"s_waitcnt lgkmcnt(2)\n" \
"ds_read_b128 a[4:7], v254\n" \
"v_mfma_f32_16x16x32_bf16 v[64:67], v[0:3], v[192:195], v[64:67]\n" \
"ds_read_b128 v[216:219], v230 offset:6144\n" \
"v_mfma_f32_16x16x32_bf16 v[64:67], v[4:7], v[196:199], v[64:67]\n" \
"ds_read_b128 v[220:223], v231 offset:6144\n" \
"v_mfma_f32_16x16x32_bf16 v[68:71], v[8:11], v[192:195], v[68:71]\n" \
"ds_read_b64_tr_b16 v[32:33], v232 offset:1024\n" \
"v_mfma_f32_16x16x32_bf16 v[68:71], v[12:15], v[196:199], v[68:71]\n" \
"v_mfma_f32_16x16x32_bf16 v[72:75], v[16:19], v[192:195], v[72:75]\n" \
"ds_read_b64_tr_b16 v[34:35], v232 offset:3072\n" \
"v_mfma_f32_16x16x32_bf16 v[72:75], v[20:23], v[196:199], v[72:75]\n" \
"s_waitcnt lgkmcnt(3)\n" \
"ds_read_b64_tr_b16 v[36:37], v232 offset:9216\n" \
"v_mfma_f32_16x16x32_bf16 v[76:79], v[24:27], v[192:195], v[76:79]\n" \
"ds_read_b64_tr_b16 v[38:39], v232 offset:11264\n" \
"v_cvt_pk_bf16_f32 v64, v64, v65\n" \
"v_mfma_f32_16x16x32_bf16 v[76:79], v[28:31], v[196:199], v[76:79]\n" \
"v_cvt_pk_bf16_f32 v65, v66, v67\n" \
"v_cvt_pk_bf16_f32 v66, v68, v69\n" \
"v_cvt_pk_bf16_f32 v67, v70, v71\n" \
"v_accvgpr_write_b32 a128, v64\n" \
"v_accvgpr_write_b32 a129, v65\n" \
"v_mfma_f32_16x16x32_bf16 v[96:99], v[0:3], v[200:203], v[96:99]\n" \
"ds_read_b64_tr_b16 v[40:41], v233 offset:1024\n" \
"v_accvgpr_write_b32 a130, v66\n" \
"v_mfma_f32_16x16x32_bf16 v[96:99], v[4:7], v[204:207], v[96:99]\n" \
"ds_read_b64_tr_b16 v[42:43], v233 offset:3072\n" \
"v_accvgpr_write_b32 a131, v67\n" \
"v_mfma_f32_16x16x32_bf16 v[100:103], v[8:11], v[200:203], v[100:103]\n" \
"ds_read_b64_tr_b16 v[44:45], v233 offset:9216\n" \
"v_cvt_pk_bf16_f32 v72, v72, v73\n" \
"v_mfma_f32_16x16x32_bf16 v[100:103], v[12:15], v[204:207], v[100:103]\n" \
"v_cvt_pk_bf16_f32 v73, v74, v75\n" \
"v_cvt_pk_bf16_f32 v74, v76, v77\n" \
"v_cvt_pk_bf16_f32 v75, v78, v79\n" \
"v_accvgpr_write_b32 a132, v72\n" \
"v_accvgpr_write_b32 a133, v73\n" \
"v_mfma_f32_16x16x32_bf16 v[104:107], v[16:19], v[200:203], v[104:107]\n" \
"ds_read_b64_tr_b16 v[46:47], v233 offset:11264\n" \
"v_accvgpr_write_b32 a134, v74\n" \
"v_mfma_f32_16x16x32_bf16 v[104:107], v[20:23], v[204:207], v[104:107]\n" \
"ds_read_b64_tr_b16 v[48:49], v232 offset:1536\n" \
"v_accvgpr_write_b32 a135, v75\n" \
"v_mfma_f32_16x16x32_bf16 v[108:111], v[24:27], v[200:203], v[108:111]\n" \
"ds_read_b64_tr_b16 v[50:51], v232 offset:3584\n" \
"v_cvt_pk_bf16_f32 v96, v96, v97\n" \
"v_mfma_f32_16x16x32_bf16 v[108:111], v[28:31], v[204:207], v[108:111]\n" \
"v_cvt_pk_bf16_f32 v97, v98, v99\n" \
"v_cvt_pk_bf16_f32 v98, v100, v101\n" \
"v_cvt_pk_bf16_f32 v99, v102, v103\n" \
"v_accvgpr_write_b32 a144, v96\n" \
"v_accvgpr_write_b32 a145, v97\n" \
"v_mfma_f32_16x16x32_bf16 v[128:131], v[0:3], v[208:211], v[128:131]\n" \
"ds_read_b64_tr_b16 v[52:53], v232 offset:9728\n" \
"v_accvgpr_write_b32 a146, v98\n" \
"v_mfma_f32_16x16x32_bf16 v[128:131], v[4:7], v[212:215], v[128:131]\n" \
"ds_read_b64_tr_b16 v[54:55], v232 offset:11776\n" \
"v_accvgpr_write_b32 a147, v99\n" \
"v_mfma_f32_16x16x32_bf16 v[132:135], v[8:11], v[208:211], v[132:135]\n" \
"ds_read_b64_tr_b16 v[56:57], v233 offset:1536\n" \
"v_cvt_pk_bf16_f32 v104, v104, v105\n" \
"v_mfma_f32_16x16x32_bf16 v[132:135], v[12:15], v[212:215], v[132:135]\n" \
"v_cvt_pk_bf16_f32 v105, v106, v107\n" \
"v_cvt_pk_bf16_f32 v106, v108, v109\n" \
"v_cvt_pk_bf16_f32 v107, v110, v111\n" \
"v_accvgpr_write_b32 a148, v104\n" \
"v_accvgpr_write_b32 a149, v105\n" \
"v_mfma_f32_16x16x32_bf16 v[136:139], v[16:19], v[208:211], v[136:139]\n" \
"ds_read_b64_tr_b16 v[58:59], v233 offset:3584\n" \
"v_accvgpr_write_b32 a150, v106\n" \
"v_mfma_f32_16x16x32_bf16 v[136:139], v[20:23], v[212:215], v[136:139]\n" \
"ds_read_b64_tr_b16 v[60:61], v233 offset:9728\n" \
"v_accvgpr_write_b32 a151, v107\n" \
"v_mfma_f32_16x16x32_bf16 v[140:143], v[24:27], v[208:211], v[140:143]\n" \
"ds_read_b64_tr_b16 v[62:63], v233 offset:11776\n" \
"v_cvt_pk_bf16_f32 v128, v128, v129\n" \
"v_mfma_f32_16x16x32_bf16 v[140:143], v[28:31], v[212:215], v[140:143]\n" \
"s_waitcnt lgkmcnt(3)\n" \
"v_cvt_pk_bf16_f32 v129, v130, v131\n" \
"v_cvt_pk_bf16_f32 v130, v132, v133\n" \
"v_cvt_pk_bf16_f32 v131, v134, v135\n" \
"v_mfma_f32_16x16x32_bf16 v[160:163], v[0:3], v[216:219], v[160:163]\n" \
"v_accvgpr_write_b32 a160, v128\n" \
"v_accvgpr_write_b32 a161, v129\n" \
"Lpg4934_%=:\n" \
"s_cbranch_execz Lpg6171_%=\n" \
"v_mfma_f32_16x16x32_bf16 v[160:163], v[4:7], v[220:223], v[160:163]\n" \
"v_accvgpr_write_b32 a162, v130\n" \
"v_accvgpr_write_b32 a163, v131\n" \
"v_mfma_f32_16x16x32_bf16 v[164:167], v[8:11], v[216:219], v[164:167]\n" \
"v_cvt_pk_bf16_f32 v136, v136, v137\n" \
"v_cvt_pk_bf16_f32 v137, v138, v139\n" \
"v_mfma_f32_16x16x32_bf16 v[164:167], v[12:15], v[220:223], v[164:167]\n" \
"v_cvt_pk_bf16_f32 v138, v140, v141\n" \
"v_cvt_pk_bf16_f32 v139, v142, v143\n" \
"v_mfma_f32_16x16x32_bf16 v[168:171], v[16:19], v[216:219], v[168:171]\n" \
"v_accvgpr_write_b32 a164, v136\n" \
"v_accvgpr_write_b32 a165, v137\n" \
"v_mfma_f32_16x16x32_bf16 v[168:171], v[20:23], v[220:223], v[168:171]\n" \
"s_waitcnt vmcnt(10) lgkmcnt(0)\n" \
"v_accvgpr_write_b32 a166, v138\n" \
"v_accvgpr_write_b32 a167, v139\n" \
"v_mfma_f32_16x16x32_bf16 v[172:175], v[24:27], v[216:219], v[172:175]\n" \
"v_cvt_pk_bf16_f32 v160, v160, v161\n" \
"v_cvt_pk_bf16_f32 v161, v162, v163\n" \
"v_mfma_f32_16x16x32_bf16 v[172:175], v[28:31], v[220:223], v[172:175]\n" \
"s_barrier\n" \
"v_cvt_pk_bf16_f32 v162, v164, v165\n" \
"v_cvt_pk_bf16_f32 v163, v166, v167\n" \
"v_accvgpr_write_b32 a176, v160\n" \
"v_accvgpr_write_b32 a177, v161\n" \
"v_mfma_f32_16x16x32_bf16 v[80:83], v[32:35], v[192:195], v[80:83]\n" \
"s_mov_b32 m0, s74\n" \
"v_accvgpr_write_b32 a178, v162\n" \
"v_accvgpr_write_b32 a179, v163\n" \
"v_mfma_f32_16x16x32_bf16 v[80:83], v[36:39], v[196:199], v[80:83]\n" \
"ds_read_b128 a[192:195], v249 offset:0\n" \
"global_load_lds_dwordx4 v250, s[38:39] offset:0 sc1\n" \
"v_mfma_f32_16x16x32_bf16 v[84:87], v[40:43], v[192:195], v[84:87]\n" \
"ds_read_b128 v[0:3], v254\n" \
"v_cvt_pk_bf16_f32 v168, v168, v169\n" \
"v_mfma_f32_16x16x32_bf16 v[84:87], v[44:47], v[196:199], v[84:87]\n" \
"ds_read_b128 a[196:199], v249 offset:2048\n" \
"global_load_lds_dwordx4 v250, s[38:39] offset:1024 sc1\n" \
"v_cvt_pk_bf16_f32 v169, v170, v171\n" \
"v_cvt_pk_bf16_f32 v170, v172, v173\n" \
"v_cvt_pk_bf16_f32 v171, v174, v175\n" \
"v_accvgpr_write_b32 a180, v168\n" \
"v_accvgpr_write_b32 a181, v169\n" \
"v_accvgpr_write_b32 a182, v170\n" \
"v_mfma_f32_16x16x32_bf16 v[88:91], v[48:51], v[192:195], v[88:91]\n" \
"ds_read_b128 v[4:7], v254\n" \
"v_accvgpr_write_b32 a183, v171\n" \
"v_mfma_f32_16x16x32_bf16 v[88:91], v[52:55], v[196:199], v[88:91]\n" \
"ds_read_b128 a[200:203], v249 offset:4096\n" \
"global_load_lds_dwordx4 v250, s[38:39] offset:2048 sc1\n" \
"v_mfma_f32_16x16x32_bf16 v[92:95], v[56:59], v[192:195], v[92:95]\n" \
"v_cvt_pk_bf16_f32 v80, v80, v81\n" \
"v_cvt_pk_bf16_f32 v81, v82, v83\n" \
"v_mfma_f32_16x16x32_bf16 v[92:95], v[60:63], v[196:199], v[92:95]\n" \
"ds_read_b128 a[204:207], v249 offset:6144\n" \
"global_load_lds_dwordx4 v250, s[38:39] offset:3072 sc1\n" \
"v_cvt_pk_bf16_f32 v82, v84, v85\n" \
"v_cvt_pk_bf16_f32 v83, v86, v87\n" \
"v_accvgpr_write_b32 a136, v80\n" \
"v_accvgpr_write_b32 a137, v81\n" \
"v_accvgpr_write_b32 a138, v82\n" \
"v_mfma_f32_16x16x32_bf16 v[112:115], v[32:35], v[200:203], v[112:115]\n" \
"s_add_u32 s38, s38, 17408\n" \
"v_accvgpr_write_b32 a139, v83\n" \
"s_add_u32 m0, m0, 0x4000\n" \
"v_mfma_f32_16x16x32_bf16 v[112:115], v[36:39], v[204:207], v[112:115]\n" \
"ds_read_b128 a[208:211], v249 offset:256\n" \
"global_load_lds_dwordx4 v250, s[38:39] offset:0 sc1\n" \
"v_mfma_f32_16x16x32_bf16 v[116:119], v[40:43], v[200:203], v[116:119]\n" \
"v_cvt_pk_bf16_f32 v88, v88, v89\n" \
"v_cvt_pk_bf16_f32 v89, v90, v91\n" \
"v_mfma_f32_16x16x32_bf16 v[116:119], v[44:47], v[204:207], v[116:119]\n" \
"ds_read_b128 a[212:215], v249 offset:2304\n" \
"global_load_lds_dwordx4 v250, s[38:39] offset:1024 sc1\n" \
"v_cvt_pk_bf16_f32 v90, v92, v93\n" \
"v_cvt_pk_bf16_f32 v91, v94, v95\n" \
"v_accvgpr_write_b32 a140, v88\n" \
"v_accvgpr_write_b32 a141, v89\n" \
"v_mfma_f32_16x16x32_bf16 v[120:123], v[48:51], v[200:203], v[120:123]\n" \
"v_accvgpr_write_b32 a142, v90\n" \
"v_accvgpr_write_b32 a143, v91\n" \
"v_mfma_f32_16x16x32_bf16 v[120:123], v[52:55], v[204:207], v[120:123]\n" \
"ds_read_b128 a[216:219], v249 offset:4352\n" \
"global_load_lds_dwordx4 v250, s[38:39] offset:2048 sc1\n" \
"v_mfma_f32_16x16x32_bf16 v[124:127], v[56:59], v[200:203], v[124:127]\n" \
"v_cvt_pk_bf16_f32 v112, v112, v113\n" \
"v_cvt_pk_bf16_f32 v113, v114, v115\n" \
"v_mfma_f32_16x16x32_bf16 v[124:127], v[60:63], v[204:207], v[124:127]\n" \
"ds_read_b128 a[220:223], v249 offset:6400\n" \
"global_load_lds_dwordx4 v250, s[38:39] offset:3072 sc1\n" \
"v_cvt_pk_bf16_f32 v114, v116, v117\n" \
"v_cvt_pk_bf16_f32 v115, v118, v119\n" \
"v_mfma_f32_16x16x32_bf16 v[144:147], v[32:35], v[208:211], v[144:147]\n" \
"s_add_u32 s38, s38, 17408\n" \
"v_accvgpr_write_b32 a152, v112\n" \
"s_xor_b32 s74, s74, 0x8000\n" \
"v_mfma_f32_16x16x32_bf16 v[144:147], v[36:39], v[212:215], v[144:147]\n" \
"ds_read_b128 a[224:227], v249 offset:8192\n" \
"v_accvgpr_write_b32 a153, v113\n" \
"v_mfma_f32_16x16x32_bf16 v[148:151], v[40:43], v[208:211], v[148:151]\n" \
"v_accvgpr_write_b32 a154, v114\n" \
"v_accvgpr_write_b32 a155, v115\n" \
"v_mfma_f32_16x16x32_bf16 v[148:151], v[44:47], v[212:215], v[148:151]\n" \
"buffer_store_dwordx4 a[32:35], v249, s[56:59], s90 offen offset:256 sc1\n" \
"ds_read_b128 a[228:231], v249 offset:10240\n" \
"v_cvt_pk_bf16_f32 v120, v120, v121\n" \
"v_cvt_pk_bf16_f32 v121, v122, v123\n" \
"v_cvt_pk_bf16_f32 v122, v124, v125\n" \
"v_mfma_f32_16x16x32_bf16 v[152:155], v[48:51], v[208:211], v[152:155]\n" \
"v_cvt_pk_bf16_f32 v123, v126, v127\n" \
"v_accvgpr_write_b32 a156, v120\n" \
"v_mfma_f32_16x16x32_bf16 v[152:155], v[52:55], v[212:215], v[152:155]\n" \
"ds_read_b128 a[232:235], v249 offset:12288\n" \
"v_accvgpr_write_b32 a157, v121\n" \
"v_mfma_f32_16x16x32_bf16 v[156:159], v[56:59], v[208:211], v[156:159]\n" \
"ds_read_b128 a[32:35], v254\n" \
"v_accvgpr_write_b32 a158, v122\n" \
"v_mfma_f32_16x16x32_bf16 v[156:159], v[60:63], v[212:215], v[156:159]\n" \
"ds_read_b128 a[236:239], v249 offset:14336\n" \
"v_accvgpr_write_b32 a159, v123\n" \
"v_mfma_f32_16x16x32_bf16 v[176:179], v[32:35], v[216:219], v[176:179]\n" \
"ds_read_b128 a[240:243], v249 offset:8448\n" \
"v_cvt_pk_bf16_f32 v144, v144, v145\n" \
"v_mfma_f32_16x16x32_bf16 v[176:179], v[36:39], v[220:223], v[176:179]\n" \
"ds_read_b128 a[244:247], v249 offset:10496\n" \
"v_cvt_pk_bf16_f32 v145, v146, v147\n" \
"v_mfma_f32_16x16x32_bf16 v[180:183], v[40:43], v[216:219], v[180:183]\n" \
"ds_read_b128 a[248:251], v249 offset:12544\n" \
"v_cvt_pk_bf16_f32 v146, v148, v149\n" \
"v_mfma_f32_16x16x32_bf16 v[180:183], v[44:47], v[220:223], v[180:183]\n" \
"ds_read_b128 a[252:255], v249 offset:14592\n" \
"v_cvt_pk_bf16_f32 v147, v150, v151\n" \
"v_mfma_f32_16x16x32_bf16 v[184:187], v[48:51], v[216:219], v[184:187]\n" \
"ds_read_b128 a[36:39], v254\n" \
"v_accvgpr_write_b32 a168, v144\n" \
"v_mfma_f32_16x16x32_bf16 v[184:187], v[52:55], v[220:223], v[184:187]\n" \
"ds_read_b128 v[32:35], v254\n" \
"v_accvgpr_write_b32 a169, v145\n" \
"v_mfma_f32_16x16x32_bf16 v[188:191], v[56:59], v[216:219], v[188:191]\n" \
"ds_read_b128 v[36:39], v254\n" \
"v_accvgpr_write_b32 a170, v146\n" \
"v_mfma_f32_16x16x32_bf16 v[188:191], v[60:63], v[220:223], v[188:191]\n" \
"s_waitcnt lgkmcnt(4)\n" \
"buffer_store_dwordx4 a[8:11], v249, s[56:59], s90 offen offset:2048 sc1\n" \
"v_accvgpr_write_b32 a171, v147\n" \
"s_cmp_eq_u32 s63, 0\n" \
"s_cbranch_scc1 Lmask0_%=\n" \
"Lmask0r_%=:\n" \
"v_mfma_f32_16x16x32_bf16 v[0:3], a[192:195], a[128:131], v[0:3]\n" \
"s_lshl_b32 s70, s95, 1\n" \
"v_cvt_pk_bf16_f32 v152, v152, v153\n" \
"v_cvt_pk_bf16_f32 v153, v154, v155\n" \
"v_mfma_f32_16x16x32_bf16 v[0:3], a[196:199], a[132:135], v[0:3]\n" \
"s_add_u32 s70, s70, 131072\n" \
"v_cvt_pk_bf16_f32 v154, v156, v157\n" \
"v_cvt_pk_bf16_f32 v155, v158, v159\n" \
"v_mfma_f32_16x16x32_bf16 v[0:3], a[200:203], a[136:139], v[0:3]\n" \
"ds_read_b128 a[8:11], v254\n" \
"v_accvgpr_write_b32 a172, v152\n" \
"v_mfma_f32_16x16x32_bf16 v[0:3], a[204:207], a[140:143], v[0:3]\n" \
"ds_read_b128 a[12:15], v254\n" \
"v_accvgpr_write_b32 a173, v153\n" \
"v_mov_b32 v201, 0xf149f2ca\n" \
"v_mfma_f32_16x16x32_bf16 v[8:11], a[192:195], a[144:147], 0\n" \
"v_accvgpr_write_b32 a174, v154\n" \
"v_accvgpr_write_b32 a175, v155\n" \
"v_mfma_f32_16x16x32_bf16 v[8:11], a[196:199], a[148:151], v[8:11]\n" \
"v_cvt_pk_bf16_f32 v176, v176, v177\n" \
"v_cvt_pk_bf16_f32 v177, v178, v179\n" \
"v_mfma_f32_16x16x32_bf16 v[8:11], a[200:203], a[152:155], v[8:11]\n" \
"v_cvt_pk_bf16_f32 v178, v180, v181\n" \
"v_cvt_pk_bf16_f32 v179, v182, v183\n" \
"v_mfma_f32_16x16x32_bf16 v[8:11], a[204:207], a[156:159], v[8:11]\n" \
"v_accvgpr_write_b32 a184, v176\n" \
"v_accvgpr_write_b32 a185, v177\n" \
"v_max3_f32 v192, v0, v1, v2\n" \
"v_max3_f32 v192, v192, v3, v201\n" \
"v_mfma_f32_16x16x32_bf16 v[16:19], a[192:195], a[160:163], 0\n" \
"v_accvgpr_write_b32 a186, v178\n" \
"v_accvgpr_write_b32 a187, v179\n" \
"v_mfma_f32_16x16x32_bf16 v[16:19], a[196:199], a[164:167], v[16:19]\n" \
"v_cvt_pk_bf16_f32 v184, v184, v185\n" \
"v_cvt_pk_bf16_f32 v185, v186, v187\n" \
"v_mfma_f32_16x16x32_bf16 v[16:19], a[200:203], a[168:171], v[16:19]\n" \
"v_cvt_pk_bf16_f32 v186, v188, v189\n" \
"v_cvt_pk_bf16_f32 v187, v190, v191\n" \
"v_mfma_f32_16x16x32_bf16 v[16:19], a[204:207], a[172:175], v[16:19]\n" \
"v_max3_f32 v193, v8, v9, v10\n" \
"v_accvgpr_write_b32 a188, v184\n" \
"v_accvgpr_write_b32 a189, v185\n" \
"v_mfma_f32_16x16x32_bf16 v[24:27], a[192:195], a[176:179], 0\n" \
"v_accvgpr_write_b32 a190, v186\n" \
"v_accvgpr_write_b32 a191, v187\n" \
"v_mfma_f32_16x16x32_bf16 v[24:27], a[196:199], a[180:183], v[24:27]\n" \
"v_add_u32 v200, s70, v248\n" \
"v_max3_f32 v193, v193, v11, v201\n" \
"v_mfma_f32_16x16x32_bf16 v[24:27], a[200:203], a[184:187], v[24:27]\n" \
"ds_read_b128 v[128:131], v254\n" \
"ds_read_b128 v[132:135], v254\n" \
"v_max3_f32 v194, v16, v17, v18\n" \
"v_max3_f32 v194, v194, v19, v201\n" \
"v_mfma_f32_16x16x32_bf16 v[24:27], a[204:207], a[188:191], v[24:27]\n" \
"s_waitcnt lgkmcnt(2)\n" \
"ds_read_b128 v[136:139], v254\n" \
"ds_read_b128 v[140:143], v254\n" \
"v_mfma_f32_16x16x32_bf16 v[4:7], a[208:211], a[128:131], v[4:7]\n" \
"ds_read_b128 v[144:147], v254\n" \
"ds_read_b128 v[148:151], v254\n" \
"v_mfma_f32_16x16x32_bf16 v[4:7], a[212:215], a[132:135], v[4:7]\n" \
"ds_read_b128 a[192:195], v249 offset:16384\n" \
"ds_read_b128 v[152:155], v254\n" \
"v_mfma_f32_16x16x32_bf16 v[4:7], a[216:219], a[136:139], v[4:7]\n" \
"ds_read_b128 v[156:159], v254\n" \
"ds_read_b128 v[160:163], v254\n" \
"v_max3_f32 v195, v24, v25, v26\n" \
"v_max3_f32 v195, v195, v27, v201\n" \
"v_mov_b32 v196, v192\n" \
"v_mov_b32 v197, v193\n" \
"v_mov_b32 v198, v194\n" \
"v_mov_b32 v199, v195\n" \
"v_permlane16_swap_b32 v196, v192\n" \
"v_permlane16_swap_b32 v197, v193\n" \
"v_permlane16_swap_b32 v198, v194\n" \
"v_permlane16_swap_b32 v199, v195\n" \
"v_max_f32 v192, v192, v196\n" \
"v_max_f32 v193, v193, v197\n" \
"v_mfma_f32_16x16x32_bf16 v[4:7], a[220:223], a[140:143], v[4:7]\n" \
"ds_read_b128 a[196:199], v249 offset:18432\n" \
"ds_read_b128 v[164:167], v254\n" \
"v_max_f32 v194, v194, v198\n" \
"v_max_f32 v195, v195, v199\n" \
"v_mov_b32 v196, v192\n" \
"v_mov_b32 v197, v193\n" \
"v_mov_b32 v198, v194\n" \
"v_mov_b32 v199, v195\n" \
"v_permlane32_swap_b32 v196, v192\n" \
"v_permlane32_swap_b32 v197, v193\n" \
"v_permlane32_swap_b32 v198, v194\n" \
"v_permlane32_swap_b32 v199, v195\n" \
"v_max_f32 v192, v192, v196\n" \
"v_max_f32 v193, v193, v197\n" \
"v_mfma_f32_16x16x32_bf16 v[12:15], a[216:219], a[152:155], 0\n" \
"ds_read_b128 v[168:171], v254\n" \
"ds_read_b128 v[172:175], v254\n" \
"v_max_f32 v194, v194, v198\n" \
"v_max_f32 v195, v195, v199\n" \
"v_sub_f32 v224, 0, v192\n" \
"v_sub_f32 v225, 0, v192\n" \
"v_sub_f32 v226, 0, v192\n" \
"v_sub_f32 v227, 0, v192\n" \
"v_add_f32 v32, v32, v224\n" \
"v_mfma_f32_16x16x32_bf16 v[12:15], a[220:223], a[156:159], v[12:15]\n" \
"ds_read_b128 a[200:203], v249 offset:20480\n" \
"ds_read_b128 v[176:179], v254\n" \
"v_mfma_f32_16x16x32_bf16 v[12:15], a[208:211], a[144:147], v[12:15]\n" \
"ds_read_b128 v[180:183], v254\n" \
"ds_read_b128 v[184:187], v254\n" \
"v_mfma_f32_16x16x32_bf16 v[12:15], a[212:215], a[148:151], v[12:15]\n" \
"ds_read_b128 a[204:207], v249 offset:22528\n" \
"buffer_store_dwordx4 a[40:43], v249, s[56:59], s90 offen offset:2304 sc1\n" \
"ds_write_b128 v200, a[160:163] offset:0\n" \
"s_add_u32 s90, s89, 417792\n" \
"v_mfma_f32_16x16x32_bf16 v[20:23], a[208:211], a[160:163], 0\n" \
"v_add_f32 v33, v33, v224\n" \
"v_add_f32 v34, v34, v224\n" \
"v_mfma_f32_16x16x32_bf16 v[20:23], a[212:215], a[164:167], v[20:23]\n" \
"v_add_f32 v35, v35, v224\n" \
"v_sub_f32 v228, 0, v193\n" \
"v_mfma_f32_16x16x32_bf16 v[20:23], a[216:219], a[168:171], v[20:23]\n" \
"ds_read_b128 a[40:43], v254\n" \
"ds_read_b128 v[188:191], v254\n" \
"v_mfma_f32_16x16x32_bf16 v[20:23], a[220:223], a[172:175], v[20:23]\n" \
"buffer_store_dwordx4 a[16:19], v248, s[56:59], s90 offen offset:0 sc1\n" \
"ds_write_b128 v200, a[164:167] offset:1024\n" \
"v_sub_f32 v229, 0, v193\n" \
"v_mfma_f32_16x16x32_bf16 v[28:31], a[216:219], a[184:187], 0\n" \
"v_sub_f32 v230, 0, v193\n" \
"v_sub_f32 v231, 0, v193\n" \
"v_mfma_f32_16x16x32_bf16 v[28:31], a[220:223], a[188:191], v[28:31]\n" \
"v_sub_f32 v232, 0, v194\n" \
"v_sub_f32 v233, 0, v194\n" \
"v_mfma_f32_16x16x32_bf16 v[28:31], a[208:211], a[176:179], v[28:31]\n" \
"buffer_store_dwordx4 a[20:23], v248, s[56:59], s90 offen offset:1024 sc1\n" \
"ds_write_b128 v200, a[168:171] offset:2048\n" \
"v_sub_f32 v234, 0, v194\n" \
"v_mfma_f32_16x16x32_bf16 v[28:31], a[212:215], a[180:183], v[28:31]\n" \
"v_sub_f32 v235, 0, v194\n" \
"v_sub_f32 v236, 0, v195\n" \
"v_mfma_f32_16x16x32_bf16 v[32:35], a[224:227], a[128:131], v[32:35]\n" \
"v_sub_f32 v237, 0, v195\n" \
"v_sub_f32 v238, 0, v195\n" \
"v_mfma_f32_16x16x32_bf16 v[32:35], a[228:231], a[132:135], v[32:35]\n" \
"ds_read_b128 a[208:211], v249 offset:16640\n" \
"v_sub_f32 v239, 0, v195\n" \
"v_mfma_f32_16x16x32_bf16 v[32:35], a[232:235], a[136:139], v[32:35]\n" \
"ds_read_b128 a[44:47], v254\n" \
"v_add_f32 v36, v36, v224\n" \
"v_mfma_f32_16x16x32_bf16 v[32:35], a[236:239], a[140:143], v[32:35]\n" \
"ds_read_b128 a[212:215], v249 offset:18688\n" \
"ds_read_b128 a[16:19], v254\n" \
"v_mfma_f32_16x16x32_bf16 v[40:43], a[232:235], a[152:155], v[228:231]\n" \
"ds_read_b128 a[48:51], v254\n" \
"v_add_f32 v37, v37, v224\n" \
"v_mfma_f32_16x16x32_bf16 v[40:43], a[236:239], a[156:159], v[40:43]\n" \
"ds_read_b128 a[216:219], v249 offset:20736\n" \
"v_add_f32 v38, v38, v224\n" \
"v_mfma_f32_16x16x32_bf16 v[40:43], a[224:227], a[144:147], v[40:43]\n" \
"ds_read_b128 a[20:23], v254\n" \
"v_add_f32 v39, v39, v224\n" \
"v_mfma_f32_16x16x32_bf16 v[40:43], a[228:231], a[148:151], v[40:43]\n" \
"buffer_store_dwordx4 a[24:27], v248, s[56:59], s90 offen offset:2048 sc1\n" \
"ds_read_b128 a[220:223], v249 offset:22784\n" \
"ds_write_b128 v200, a[172:175] offset:3072\n" \
"v_pk_add_f32 v[0:1], v[0:1], v[224:225]\n" \
"v_pk_add_f32 v[2:3], v[2:3], v[224:225]\n" \
"v_pk_add_f32 v[4:5], v[4:5], v[224:225]\n" \
"v_pk_add_f32 v[6:7], v[6:7], v[224:225]\n" \
"v_exp_f32 v0, v0\n" \
"v_exp_f32 v1, v1\n" \
"v_exp_f32 v2, v2\n" \
"v_cvt_pk_bf16_f32 v0, v0, v1\n" \
"v_exp_f32 v3, v3\n" \
"v_exp_f32 v4, v4\n" \
"v_cvt_pk_bf16_f32 v1, v2, v3\n" \
"v_mfma_f32_16x16x32_bf16 v[48:51], a[224:227], a[160:163], v[232:235]\n" \
"v_exp_f32 v5, v5\n" \
"v_mfma_f32_16x16x32_bf16 v[48:51], a[228:231], a[164:167], v[48:51]\n" \
"v_exp_f32 v6, v6\n" \
"v_mfma_f32_16x16x32_bf16 v[48:51], a[232:235], a[168:171], v[48:51]\n" \
"ds_read_b128 a[52:55], v254\n" \
"v_cvt_pk_bf16_f32 v2, v4, v5\n" \
"v_mfma_f32_16x16x32_bf16 v[48:51], a[236:239], a[172:175], v[48:51]\n" \
"buffer_store_dwordx4 a[28:31], v248, s[56:59], s90 offen offset:3072 sc1\n" \
"ds_write_b128 v200, a[176:179] offset:4096\n" \
"v_exp_f32 v7, v7\n" \
"v_pk_add_f32 v[8:9], v[8:9], v[228:229]\n" \
"v_pk_add_f32 v[10:11], v[10:11], v[228:229]\n" \
"v_pk_add_f32 v[12:13], v[12:13], v[228:229]\n" \
"v_pk_add_f32 v[14:15], v[14:15], v[228:229]\n" \
"v_exp_f32 v8, v8\n" \
"v_cvt_pk_bf16_f32 v3, v6, v7\n" \
"v_exp_f32 v9, v9\n" \
"v_exp_f32 v10, v10\n" \
"v_cvt_pk_bf16_f32 v8, v8, v9\n" \
"v_exp_f32 v11, v11\n" \
"v_mfma_f32_16x16x32_bf16 v[56:59], a[232:235], a[184:187], v[236:239]\n" \
"global_load_dword v253, v252, s[40:41]\n" \
"v_mfma_f32_16x16x32_bf16 v[56:59], a[236:239], a[188:191], v[56:59]\n" \
"s_sub_u32 s80, s80, 1\n" \
"v_exp_f32 v12, v12\n" \
"v_mfma_f32_16x16x32_bf16 v[56:59], a[224:227], a[176:179], v[56:59]\n" \
"s_cmp_gt_i32 s80, 0\n" \
"ds_write_b128 v200, a[180:183] offset:5120\n" \
"v_cvt_pk_bf16_f32 v9, v10, v11\n" \
"v_exp_f32 v13, v13\n" \
"v_exp_f32 v14, v14\n" \
"v_cvt_pk_bf16_f32 v10, v12, v13\n" \
"v_exp_f32 v15, v15\n" \
"v_pk_add_f32 v[16:17], v[16:17], v[232:233]\n" \
"v_pk_add_f32 v[18:19], v[18:19], v[232:233]\n" \
"v_pk_add_f32 v[20:21], v[20:21], v[232:233]\n" \
"v_pk_add_f32 v[22:23], v[22:23], v[232:233]\n" \
"v_exp_f32 v16, v16\n" \
"v_cvt_pk_bf16_f32 v11, v14, v15\n" \
"v_mfma_f32_16x16x32_bf16 v[56:59], a[228:231], a[180:183], v[56:59]\n" \
"s_cselect_b32 s81, 0x80000, 0\n" \
"v_exp_f32 v17, v17\n" \
"v_mfma_f32_16x16x32_bf16 v[36:39], a[240:243], a[128:131], v[36:39]\n" \
"s_add_u32 s40, s40, s81\n" \
"v_exp_f32 v18, v18\n" \
"v_mfma_f32_16x16x32_bf16 v[36:39], a[244:247], a[132:135], v[36:39]\n" \
"ds_read_b128 a[224:227], v249 offset:24576\n" \
"v_cvt_pk_bf16_f32 v16, v16, v17\n" \
"v_mfma_f32_16x16x32_bf16 v[240:243], a[120:123], v[0:3], v[240:243]\n" \
"v_exp_f32 v19, v19\n" \
"v_mfma_f32_16x16x32_bf16 v[240:243], a[124:127], v[8:11], v[240:243]\n" \
"s_addc_u32 s41, s41, 0\n" \
"v_exp_f32 v20, v20\n" \
"v_mfma_f32_16x16x32_bf16 v[36:39], a[248:251], a[136:139], v[36:39]\n" \
"s_add_u32 m0, s95, 49152\n" \
"ds_read_b128 a[24:27], v254\n" \
"v_cvt_pk_bf16_f32 v17, v18, v19\n" \
"v_mfma_f32_16x16x32_bf16 v[36:39], a[252:255], a[140:143], v[36:39]\n" \
"ds_read_b128 a[228:231], v249 offset:26624\n" \
"global_load_lds_dwordx4 v250, s[36:37] offset:0 sc1\n" \
"ds_read_b128 a[56:59], v254\n" \
"v_exp_f32 v21, v21\n" \
"v_exp_f32 v22, v22\n" \
"v_mfma_f32_16x16x32_bf16 v[44:47], a[248:251], a[152:155], v[228:231]\n" \
"ds_read_b128 a[28:31], v254\n" \
"v_cvt_pk_bf16_f32 v18, v20, v21\n" \
"v_mfma_f32_16x16x32_bf16 v[44:47], a[252:255], a[156:159], v[44:47]\n" \
"ds_read_b128 a[232:235], v249 offset:28672\n" \
"global_load_lds_dwordx4 v250, s[36:37] offset:1024 sc1\n" \
"v_mfma_f32_16x16x32_bf16 v[44:47], a[240:243], a[144:147], v[44:47]\n" \
"ds_read_b128 a[60:63], v254\n" \
"global_load_lds_dwordx4 v250, s[36:37] offset:2048 sc1\n" \
"v_mfma_f32_16x16x32_bf16 v[44:47], a[244:247], a[148:151], v[44:47]\n" \
"ds_read_b128 a[236:239], v249 offset:30720\n" \
"global_load_lds_dwordx4 v250, s[36:37] offset:3072 sc1\n" \
"ds_write_b128 v200, a[184:187] offset:6144\n" \
"v_exp_f32 v23, v23\n" \
"v_pk_add_f32 v[24:25], v[24:25], v[236:237]\n" \
"v_pk_add_f32 v[26:27], v[26:27], v[236:237]\n" \
"v_pk_add_f32 v[28:29], v[28:29], v[236:237]\n" \
"v_pk_add_f32 v[30:31], v[30:31], v[236:237]\n" \
"v_exp_f32 v24, v24\n" \
"v_cvt_pk_bf16_f32 v19, v22, v23\n" \
"v_exp_f32 v25, v25\n" \
"v_exp_f32 v26, v26\n" \
"v_cvt_pk_bf16_f32 v24, v24, v25\n" \
"v_mfma_f32_16x16x32_bf16 v[242:245], a[120:123], v[16:19], v[242:245]\n" \
"v_exp_f32 v27, v27\n" \
"v_mfma_f32_16x16x32_bf16 v[52:55], a[240:243], a[160:163], v[232:235]\n" \
"s_add_u32 s36, s36, 17408\n" \
"v_mov_b32 v202, s60\n" \
"v_lshlrev_b32 v203, 28, s61\n" \
"v_mfma_f32_16x16x32_bf16 v[52:55], a[244:247], a[164:167], v[52:55]\n" \
"s_mov_b32 s42, s60\n" \
"v_mov_b32 v246, v249\n" \
"v_mov_b32 v254, 0xff800000\n" \
"v_mfma_f32_16x16x32_bf16 v[52:55], a[248:251], a[168:171], v[52:55]\n" \
"s_mov_b32 s43, s60\n" \
"v_exp_f32 v28, v28\n" \
"v_mfma_f32_16x16x32_bf16 v[52:55], a[252:255], a[172:175], v[52:55]\n" \
"s_mov_b64 s[8:9], 0\n" \
"ds_write_b128 v200, a[188:191] offset:7168\n" \
"v_cvt_pk_bf16_f32 v25, v26, v27\n" \
"v_exp_f32 v29, v29\n" \
"v_mfma_f32_16x16x32_bf16 v[60:63], a[248:251], a[184:187], v[236:239]\n" \
"s_mov_b64 s[10:11], -1\n" \
"v_exp_f32 v30, v30\n" \
"v_cvt_pk_bf16_f32 v26, v28, v29\n" \
"v_mfma_f32_16x16x32_bf16 v[60:63], a[252:255], a[188:191], v[60:63]\n" \
"s_mov_b64 s[92:93], 0\n" \
"v_exp_f32 v31, v31\n" \
"v_mfma_f32_16x16x32_bf16 v[60:63], a[240:243], a[176:179], v[60:63]\n" \
"v_cvt_pk_bf16_f32 v27, v30, v31\n" \
"s_lshl_b32 s84, s63, 2\n" \
"v_mfma_f32_16x16x32_bf16 v[60:63], a[244:247], a[180:183], v[60:63]\n" \
"v_mfma_f32_16x16x32_bf16 v[242:245], a[124:127], v[24:27], v[242:245]\n" \
"s_add_u32 s84, s84, s79\n" \
"ds_read_b128 a[240:243], v249 offset:24832\n" \
"ds_read_b128 a[244:247], v249 offset:26880\n" \
"ds_read_b128 a[248:251], v249 offset:28928\n" \
"s_cmp_eq_u32 s63, 0\n" \
"s_waitcnt vmcnt(16) lgkmcnt(3)\n" \
"s_barrier\n" \
"s_memrealtime s[12:13]\n" \
"s_cbranch_scc1 LA1l_%=\n" \
"s_branch LA1_%=\n" \
"Lpg5386_%=:\n" \
"s_cbranch_execz Lpg6288_%=\n" \
"Lloop_%=:\n" \
"v_mfma_f32_16x16x32_bf16 a[0:3], a[192:195], v[64:67], a[0:3]\n" \
"v_exp_f32 v0, v0\n" \
"v_mfma_f32_16x16x32_bf16 a[0:3], a[196:199], v[96:99], a[0:3]\n" \
"v_cvt_pk_bf16_f32 v123, v126, v127\n" \
"v_add_f32 v242, v242, v0\n" \
"v_exp_f32 v1, v1\n" \
"v_mfma_f32_16x16x32_bf16 a[32:35], a[192:195], v[72:75], a[32:35]\n" \
"s_add_u32 m0, s73, 0x4000\n" \
"ds_read_b128 a[244:247], v247 offset:30720\n" \
"v_add_f32 v242, v242, v1\n" \
"v_mfma_f32_16x16x32_bf16 a[32:35], a[196:199], v[104:107], a[32:35]\n" \
"ds_read_b128 a[248:251], v247 offset:23552\n" \
"global_load_lds_dwordx4 v250, s[36:37] offset:0 sc1\n" \
"v_mfma_f32_16x16x32_bf16 v[128:131], a[192:195], v[80:83], v[128:131]\n" \
"ds_read_b128 a[252:255], v247 offset:31744\n" \
"v_exp_f32 v2, v2\n" \
"v_mfma_f32_16x16x32_bf16 v[128:131], a[196:199], v[112:115], v[128:131]\n" \
"v_add_f32 v242, v242, v2\n" \
"v_cvt_pk_bf16_f32 v0, v0, v1\n" \
"v_mfma_f32_16x16x32_bf16 v[160:163], a[192:195], v[88:91], v[160:163]\n" \
"v_exp_f32 v3, v3\n" \
"v_mfma_f32_16x16x32_bf16 v[160:163], a[196:199], v[120:123], v[160:163]\n" \
"ds_read_b128 a[192:195], v246 offset:16384\n" \
"v_xor_b32 v247, 0x8000, v247\n" \
"v_add_f32 v242, v242, v3\n" \
"v_mfma_f32_16x16x32_bf16 v[164:167], a[200:203], v[88:91], v[164:167]\n" \
"v_exp_f32 v4, v4\n" \
"v_mfma_f32_16x16x32_bf16 v[164:167], a[204:207], v[120:123], v[164:167]\n" \
"v_add_f32 v242, v242, v4\n" \
"v_cvt_pk_bf16_f32 v1, v2, v3\n" \
"v_mfma_f32_16x16x32_bf16 v[132:135], a[200:203], v[80:83], v[132:135]\n" \
"v_exp_f32 v5, v5\n" \
"v_mfma_f32_16x16x32_bf16 v[132:135], a[204:207], v[112:115], v[132:135]\n" \
"ds_read_b128 a[196:199], v246 offset:18432\n" \
"v_add_f32 v242, v242, v5\n" \
"v_mfma_f32_16x16x32_bf16 a[36:39], a[200:203], v[72:75], a[36:39]\n" \
"v_exp_f32 v6, v6\n" \
"v_mfma_f32_16x16x32_bf16 a[36:39], a[204:207], v[104:107], a[36:39]\n" \
"v_add_f32 v242, v242, v6\n" \
"v_cvt_pk_bf16_f32 v2, v4, v5\n" \
"v_mfma_f32_16x16x32_bf16 a[4:7], a[200:203], v[64:67], a[4:7]\n" \
"v_exp_f32 v7, v7\n" \
"v_mfma_f32_16x16x32_bf16 a[4:7], a[204:207], v[96:99], a[4:7]\n" \
"ds_read_b128 a[200:203], v246 offset:20480\n" \
"global_load_lds_dwordx4 v250, s[36:37] offset:1024 sc1\n" \
"v_add_f32 v242, v242, v7\n" \
"v_mfma_f32_16x16x32_bf16 a[8:11], a[208:211], v[64:67], a[8:11]\n" \
"v_exp_f32 v8, v8\n" \
"v_mfma_f32_16x16x32_bf16 a[8:11], a[212:215], v[96:99], a[8:11]\n" \
"v_add_f32 v243, v243, v8\n" \
"v_cvt_pk_bf16_f32 v3, v6, v7\n" \
"v_mfma_f32_16x16x32_bf16 a[40:43], a[208:211], v[72:75], a[40:43]\n" \
"v_exp_f32 v9, v9\n" \
"v_mfma_f32_16x16x32_bf16 a[40:43], a[212:215], v[104:107], a[40:43]\n" \
"ds_read_b128 a[204:207], v246 offset:22528\n" \
"v_add_f32 v243, v243, v9\n" \
"v_mfma_f32_16x16x32_bf16 v[136:139], a[208:211], v[80:83], v[136:139]\n" \
"v_exp_f32 v10, v10\n" \
"v_mfma_f32_16x16x32_bf16 v[136:139], a[212:215], v[112:115], v[136:139]\n" \
"v_add_f32 v243, v243, v10\n" \
"v_cvt_pk_bf16_f32 v8, v8, v9\n" \
"v_mfma_f32_16x16x32_bf16 v[168:171], a[208:211], v[88:91], v[168:171]\n" \
"v_exp_f32 v11, v11\n" \
"v_mfma_f32_16x16x32_bf16 v[168:171], a[212:215], v[120:123], v[168:171]\n" \
"ds_read_b128 a[208:211], v246 offset:16640\n" \
"v_add_f32 v243, v243, v11\n" \
"v_mfma_f32_16x16x32_bf16 v[172:175], a[216:219], v[88:91], v[172:175]\n" \
"v_exp_f32 v12, v12\n" \
"v_mfma_f32_16x16x32_bf16 v[172:175], a[220:223], v[120:123], v[172:175]\n" \
"v_add_f32 v243, v243, v12\n" \
"v_cvt_pk_bf16_f32 v9, v10, v11\n" \
"v_mfma_f32_16x16x32_bf16 v[140:143], a[216:219], v[80:83], v[140:143]\n" \
"v_exp_f32 v13, v13\n" \
"v_mfma_f32_16x16x32_bf16 v[140:143], a[220:223], v[112:115], v[140:143]\n" \
"ds_read_b128 a[212:215], v246 offset:18688\n" \
"global_load_lds_dwordx4 v250, s[36:37] offset:2048 sc1\n" \
"v_add_f32 v243, v243, v13\n" \
"v_mfma_f32_16x16x32_bf16 a[44:47], a[216:219], v[72:75], a[44:47]\n" \
"v_exp_f32 v14, v14\n" \
"v_mfma_f32_16x16x32_bf16 a[44:47], a[220:223], v[104:107], a[44:47]\n" \
"v_add_f32 v243, v243, v14\n" \
"v_cvt_pk_bf16_f32 v10, v12, v13\n" \
"v_mfma_f32_16x16x32_bf16 a[12:15], a[216:219], v[64:67], a[12:15]\n" \
"s_waitcnt lgkmcnt(6)\n" \
"v_exp_f32 v15, v15\n" \
"v_mfma_f32_16x16x32_bf16 a[12:15], a[220:223], v[96:99], a[12:15]\n" \
"ds_read_b128 a[216:219], v246 offset:20736\n" \
"v_add_f32 v243, v243, v15\n" \
"v_mfma_f32_16x16x32_bf16 a[16:19], a[224:227], v[64:67], a[16:19]\n" \
"v_exp_f32 v16, v16\n" \
"v_mfma_f32_16x16x32_bf16 a[16:19], a[228:231], v[96:99], a[16:19]\n" \
"v_add_f32 v244, v244, v16\n" \
"v_cvt_pk_bf16_f32 v11, v14, v15\n" \
"v_mfma_f32_16x16x32_bf16 a[48:51], a[224:227], v[72:75], a[48:51]\n" \
"v_exp_f32 v17, v17\n" \
"v_mfma_f32_16x16x32_bf16 a[48:51], a[228:231], v[104:107], a[48:51]\n" \
"ds_read_b128 a[220:223], v246 offset:22784\n" \
"global_load_lds_dwordx4 v250, s[36:37] offset:3072 sc1\n" \
"v_add_f32 v244, v244, v17\n" \
"v_mfma_f32_16x16x32_bf16 v[144:147], a[224:227], v[80:83], v[144:147]\n" \
"s_add_u32 s36, s36, 17408\n" \
"v_exp_f32 v18, v18\n" \
"v_mfma_f32_16x16x32_bf16 v[144:147], a[228:231], v[112:115], v[144:147]\n" \
"s_xor_b32 s73, s73, 0x8000\n" \
"v_add_f32 v244, v244, v18\n" \
"v_cvt_pk_bf16_f32 v16, v16, v17\n" \
"v_mfma_f32_16x16x32_bf16 v[176:179], a[224:227], v[88:91], v[176:179]\n" \
"s_xor_b64 s[52:53], s[44:45], s[42:43]\n" \
"v_exp_f32 v19, v19\n" \
"v_mfma_f32_16x16x32_bf16 v[176:179], a[228:231], v[120:123], v[176:179]\n" \
"s_xor_b64 s[54:55], s[46:47], s[42:43]\n" \
"ds_read_b128 a[224:227], v246 offset:24576\n" \
"v_add_f32 v244, v244, v19\n" \
"v_mfma_f32_16x16x32_bf16 v[180:183], a[232:235], v[88:91], v[180:183]\n" \
"s_or_b64 s[52:53], s[52:53], s[54:55]\n" \
"v_exp_f32 v20, v20\n" \
"v_mfma_f32_16x16x32_bf16 v[180:183], a[236:239], v[120:123], v[180:183]\n" \
"s_xor_b64 s[54:55], s[48:49], s[42:43]\n" \
"v_add_f32 v244, v244, v20\n" \
"v_cvt_pk_bf16_f32 v17, v18, v19\n" \
"v_mfma_f32_16x16x32_bf16 v[148:151], a[232:235], v[80:83], v[148:151]\n" \
"s_or_b64 s[52:53], s[52:53], s[54:55]\n" \
"v_exp_f32 v21, v21\n" \
"v_mfma_f32_16x16x32_bf16 v[148:151], a[236:239], v[112:115], v[148:151]\n" \
"s_xor_b64 s[54:55], s[50:51], s[42:43]\n" \
"ds_read_b128 a[228:231], v246 offset:26624\n" \
"v_add_f32 v244, v244, v21\n" \
"v_exp_f32 v22, v22\n" \
"v_mfma_f32_16x16x32_bf16 a[52:55], a[232:235], v[72:75], a[52:55]\n" \
"s_or_b64 s[52:53], s[52:53], s[54:55]\n" \
"v_add_f32 v244, v244, v22\n" \
"v_cvt_pk_bf16_f32 v18, v20, v21\n" \
"v_mfma_f32_16x16x32_bf16 a[52:55], a[236:239], v[104:107], a[52:55]\n" \
"s_and_b64 s[52:53], s[52:53], s[8:9]\n" \
"s_cbranch_scc1 Lrs0_%=\n" \
"Lrr0_%=:\n" \
"v_mfma_f32_16x16x32_bf16 a[20:23], a[232:235], v[64:67], a[20:23]\n" \
"s_mov_b64 s[8:9], s[10:11]\n" \
"v_exp_f32 v23, v23\n" \
"v_mfma_f32_16x16x32_bf16 a[20:23], a[236:239], v[96:99], a[20:23]\n" \
"ds_read_b128 a[232:235], v246 offset:28672\n" \
"v_add_f32 v244, v244, v23\n" \
"v_mfma_f32_16x16x32_bf16 a[24:27], a[240:243], v[64:67], a[24:27]\n" \
"v_exp_f32 v24, v24\n" \
"v_mfma_f32_16x16x32_bf16 a[24:27], a[244:247], v[96:99], a[24:27]\n" \
"v_add_f32 v245, v245, v24\n" \
"Lpg5534_%=:\n" \
"s_cbranch_execz Lpg6770_%=\n" \
"v_cvt_pk_bf16_f32 v19, v22, v23\n" \
"v_mfma_f32_16x16x32_bf16 a[56:59], a[240:243], v[72:75], a[56:59]\n" \
"v_exp_f32 v25, v25\n" \
"v_mfma_f32_16x16x32_bf16 a[56:59], a[244:247], v[104:107], a[56:59]\n" \
"ds_read_b128 a[236:239], v246 offset:30720\n" \
"v_add_f32 v245, v245, v25\n" \
"v_mfma_f32_16x16x32_bf16 v[152:155], a[240:243], v[80:83], v[152:155]\n" \
"v_exp_f32 v26, v26\n" \
"v_mfma_f32_16x16x32_bf16 v[152:155], a[244:247], v[112:115], v[152:155]\n" \
"v_add_f32 v245, v245, v26\n" \
"v_cvt_pk_bf16_f32 v24, v24, v25\n" \
"v_mfma_f32_16x16x32_bf16 v[184:187], a[240:243], v[88:91], v[184:187]\n" \
"v_exp_f32 v27, v27\n" \
"v_mfma_f32_16x16x32_bf16 v[184:187], a[244:247], v[120:123], v[184:187]\n" \
"ds_read_b128 a[240:243], v246 offset:24832\n" \
"v_add_f32 v245, v245, v27\n" \
"v_mfma_f32_16x16x32_bf16 v[188:191], a[248:251], v[88:91], v[188:191]\n" \
"v_exp_f32 v28, v28\n" \
"v_mfma_f32_16x16x32_bf16 v[188:191], a[252:255], v[120:123], v[188:191]\n" \
"v_add_f32 v245, v245, v28\n" \
"v_cvt_pk_bf16_f32 v25, v26, v27\n" \
"v_mfma_f32_16x16x32_bf16 v[156:159], a[248:251], v[80:83], v[156:159]\n" \
"v_exp_f32 v29, v29\n" \
"v_mfma_f32_16x16x32_bf16 v[156:159], a[252:255], v[112:115], v[156:159]\n" \
"ds_read_b128 a[244:247], v246 offset:26880\n" \
"v_add_f32 v245, v245, v29\n" \
"v_mfma_f32_16x16x32_bf16 a[60:63], a[248:251], v[72:75], a[60:63]\n" \
"v_exp_f32 v30, v30\n" \
"v_mfma_f32_16x16x32_bf16 a[60:63], a[252:255], v[104:107], a[60:63]\n" \
"v_add_f32 v245, v245, v30\n" \
"v_cvt_pk_bf16_f32 v26, v28, v29\n" \
"v_mfma_f32_16x16x32_bf16 a[28:31], a[248:251], v[64:67], a[28:31]\n" \
"s_waitcnt lgkmcnt(6)\n" \
"v_exp_f32 v31, v31\n" \
"v_mfma_f32_16x16x32_bf16 a[28:31], a[252:255], v[96:99], a[28:31]\n" \
"ds_read_b128 a[248:251], v246 offset:28928\n" \
"v_add_f32 v245, v245, v31\n" \
"LA1_%=:\n" \
"Lpg5573_%=:\n" \
"s_cbranch_execz Lpg7025_%=\n" \
"v_mfma_f32_16x16x32_bf16 v[64:67], a[192:195], a[128:131], v[224:227]\n" \
"v_exp_f32 v32, v32\n" \
"v_mfma_f32_16x16x32_bf16 v[64:67], a[196:199], a[132:135], v[64:67]\n" \
"v_cvt_pk_bf16_f32 v27, v30, v31\n" \
"v_add_f32 v242, v242, v32\n" \
"v_mfma_f32_16x16x32_bf16 v[64:67], a[200:203], a[136:139], v[64:67]\n" \
"s_mov_b32 m0, s74\n" \
"v_exp_f32 v33, v33\n" \
"v_mfma_f32_16x16x32_bf16 v[64:67], a[204:207], a[140:143], v[64:67]\n" \
"ds_read_b128 a[252:255], v246 offset:30976\n" \
"global_load_lds_dwordx4 v250, s[38:39] offset:0 sc1\n" \
"v_add_f32 v242, v242, v33\n" \
"v_mfma_f32_16x16x32_bf16 v[72:75], a[200:203], a[152:155], v[228:231]\n" \
"v_exp_f32 v34, v34\n" \
"v_mfma_f32_16x16x32_bf16 v[72:75], a[204:207], a[156:159], v[72:75]\n" \
"v_add_f32 v242, v242, v34\n" \
"v_cvt_pk_bf16_f32 v32, v32, v33\n" \
"v_mfma_f32_16x16x32_bf16 v[72:75], a[192:195], a[144:147], v[72:75]\n" \
"v_exp_f32 v35, v35\n" \
"v_mfma_f32_16x16x32_bf16 v[72:75], a[196:199], a[148:151], v[72:75]\n" \
"v_add_f32 v242, v242, v35\n" \
"v_exp_f32 v36, v36\n" \
"v_mfma_f32_16x16x32_bf16 v[80:83], a[192:195], a[160:163], v[232:235]\n" \
"v_add_f32 v242, v242, v36\n" \
"v_cvt_pk_bf16_f32 v33, v34, v35\n" \
"v_mfma_f32_16x16x32_bf16 v[80:83], a[196:199], a[164:167], v[80:83]\n" \
"v_exp_f32 v37, v37\n" \
"v_mfma_f32_16x16x32_bf16 v[80:83], a[200:203], a[168:171], v[80:83]\n" \
"v_add_f32 v242, v242, v37\n" \
"v_mfma_f32_16x16x32_bf16 v[80:83], a[204:207], a[172:175], v[80:83]\n" \
"v_exp_f32 v38, v38\n" \
"v_mfma_f32_16x16x32_bf16 v[88:91], a[200:203], a[184:187], v[236:239]\n" \
"v_add_f32 v242, v242, v38\n" \
"v_cvt_pk_bf16_f32 v34, v36, v37\n" \
"v_mfma_f32_16x16x32_bf16 v[88:91], a[204:207], a[188:191], v[88:91]\n" \
"v_exp_f32 v39, v39\n" \
"v_mfma_f32_16x16x32_bf16 v[88:91], a[192:195], a[176:179], v[88:91]\n" \
"v_add_f32 v242, v242, v39\n" \
"v_mfma_f32_16x16x32_bf16 v[88:91], a[196:199], a[180:183], v[88:91]\n" \
"ds_read_b128 a[192:195], v247 offset:0\n" \
"v_xor_b32 v246, 0x8000, v246\n" \
"v_mfma_f32_16x16x32_bf16 v[92:95], a[208:211], a[176:179], v[236:239]\n" \
"v_exp_f32 v40, v40\n" \
"v_mfma_f32_16x16x32_bf16 v[92:95], a[212:215], a[180:183], v[92:95]\n" \
"v_add_f32 v243, v243, v40\n" \
"v_cvt_pk_bf16_f32 v35, v38, v39\n" \
"v_mfma_f32_16x16x32_bf16 v[92:95], a[216:219], a[184:187], v[92:95]\n" \
"v_exp_f32 v41, v41\n" \
"v_mfma_f32_16x16x32_bf16 v[92:95], a[220:223], a[188:191], v[92:95]\n" \
"ds_read_b128 a[196:199], v247 offset:8192\n" \
"global_load_lds_dwordx4 v250, s[38:39] offset:1024 sc1\n" \
"v_add_f32 v243, v243, v41\n" \
"v_mfma_f32_16x16x32_bf16 v[84:87], a[216:219], a[168:171], v[232:235]\n" \
"v_exp_f32 v42, v42\n" \
"v_mfma_f32_16x16x32_bf16 v[84:87], a[220:223], a[172:175], v[84:87]\n" \
"v_add_f32 v243, v243, v42\n" \
"v_cvt_pk_bf16_f32 v40, v40, v41\n" \
"v_mfma_f32_16x16x32_bf16 v[84:87], a[208:211], a[160:163], v[84:87]\n" \
"v_exp_f32 v43, v43\n" \
"v_mfma_f32_16x16x32_bf16 v[84:87], a[212:215], a[164:167], v[84:87]\n" \
"ds_read_b128 a[200:203], v247 offset:1024\n" \
"v_add_f32 v243, v243, v43\n" \
"v_mfma_f32_16x16x32_bf16 v[76:79], a[208:211], a[144:147], v[228:231]\n" \
"v_exp_f32 v44, v44\n" \
"v_mfma_f32_16x16x32_bf16 v[76:79], a[212:215], a[148:151], v[76:79]\n" \
"v_add_f32 v243, v243, v44\n" \
"v_cvt_pk_bf16_f32 v41, v42, v43\n" \
"v_mfma_f32_16x16x32_bf16 v[76:79], a[216:219], a[152:155], v[76:79]\n" \
"v_exp_f32 v45, v45\n" \
"v_mfma_f32_16x16x32_bf16 v[76:79], a[220:223], a[156:159], v[76:79]\n" \
"ds_read_b128 a[204:207], v247 offset:9216\n" \
"global_load_lds_dwordx4 v250, s[38:39] offset:2048 sc1\n" \
"v_add_f32 v243, v243, v45\n" \
"v_mfma_f32_16x16x32_bf16 v[68:71], a[216:219], a[136:139], v[224:227]\n" \
"v_exp_f32 v46, v46\n" \
"v_mfma_f32_16x16x32_bf16 v[68:71], a[220:223], a[140:143], v[68:71]\n" \
"v_add_f32 v243, v243, v46\n" \
"v_cvt_pk_bf16_f32 v42, v44, v45\n" \
"v_mfma_f32_16x16x32_bf16 v[68:71], a[208:211], a[128:131], v[68:71]\n" \
"s_waitcnt lgkmcnt(4)\n" \
"v_exp_f32 v47, v47\n" \
"v_mfma_f32_16x16x32_bf16 v[68:71], a[212:215], a[132:135], v[68:71]\n" \
"ds_read_b128 a[208:211], v247 offset:2048\n" \
"v_add_f32 v243, v243, v47\n" \
"v_mfma_f32_16x16x32_bf16 v[96:99], a[224:227], a[128:131], v[224:227]\n" \
"v_exp_f32 v48, v48\n" \
"v_mfma_f32_16x16x32_bf16 v[96:99], a[228:231], a[132:135], v[96:99]\n" \
"v_add_f32 v244, v244, v48\n" \
"v_cvt_pk_bf16_f32 v43, v46, v47\n" \
"v_mfma_f32_16x16x32_bf16 v[96:99], a[232:235], a[136:139], v[96:99]\n" \
"v_exp_f32 v49, v49\n" \
"v_mfma_f32_16x16x32_bf16 v[96:99], a[236:239], a[140:143], v[96:99]\n" \
"ds_read_b128 a[212:215], v247 offset:10240\n" \
"v_add_f32 v244, v244, v49\n" \
"v_mfma_f32_16x16x32_bf16 v[104:107], a[232:235], a[152:155], v[228:231]\n" \
"v_exp_f32 v50, v50\n" \
"v_mfma_f32_16x16x32_bf16 v[104:107], a[236:239], a[156:159], v[104:107]\n" \
"v_add_f32 v244, v244, v50\n" \
"v_cvt_pk_bf16_f32 v48, v48, v49\n" \
"v_mfma_f32_16x16x32_bf16 v[104:107], a[224:227], a[144:147], v[104:107]\n" \
"v_exp_f32 v51, v51\n" \
"v_mfma_f32_16x16x32_bf16 v[104:107], a[228:231], a[148:151], v[104:107]\n" \
"ds_read_b128 a[216:219], v247 offset:3072\n" \
"global_load_lds_dwordx4 v250, s[38:39] offset:3072 sc1\n" \
"v_add_f32 v244, v244, v51\n" \
"v_mfma_f32_16x16x32_bf16 v[112:115], a[224:227], a[160:163], v[232:235]\n" \
"s_add_u32 s38, s38, 17408\n" \
"v_exp_f32 v52, v52\n" \
"v_mfma_f32_16x16x32_bf16 v[112:115], a[228:231], a[164:167], v[112:115]\n" \
"v_add_f32 v244, v244, v52\n" \
"v_cvt_pk_bf16_f32 v49, v50, v51\n" \
"v_mfma_f32_16x16x32_bf16 v[112:115], a[232:235], a[168:171], v[112:115]\n" \
"v_exp_f32 v53, v53\n" \
"v_mfma_f32_16x16x32_bf16 v[112:115], a[236:239], a[172:175], v[112:115]\n" \
"ds_read_b128 a[220:223], v247 offset:11264\n" \
"v_add_f32 v244, v244, v53\n" \
"v_exp_f32 v54, v54\n" \
"v_mfma_f32_16x16x32_bf16 v[120:123], a[232:235], a[184:187], v[236:239]\n" \
"global_load_dword v253, v252, s[40:41]\n" \
"v_mfma_f32_16x16x32_bf16 v[120:123], a[236:239], a[188:191], v[120:123]\n" \
"s_sub_u32 s80, s80, 1\n" \
"v_add_f32 v244, v244, v54\n" \
"v_cvt_pk_bf16_f32 v50, v52, v53\n" \
"v_mfma_f32_16x16x32_bf16 v[120:123], a[224:227], a[176:179], v[120:123]\n" \
"s_cmp_gt_i32 s80, 0\n" \
"v_exp_f32 v55, v55\n" \
"v_mfma_f32_16x16x32_bf16 v[120:123], a[228:231], a[180:183], v[120:123]\n" \
"s_cselect_b32 s81, 0x80000, 0\n" \
"ds_read_b128 a[224:227], v247 offset:4096\n" \
"v_add_f32 v244, v244, v55\n" \
"v_mfma_f32_16x16x32_bf16 v[124:127], a[240:243], a[176:179], v[236:239]\n" \
"s_add_u32 s40, s40, s81\n" \
"v_exp_f32 v56, v56\n" \
"v_mfma_f32_16x16x32_bf16 v[124:127], a[244:247], a[180:183], v[124:127]\n" \
"s_addc_u32 s41, s41, 0\n" \
"v_add_f32 v245, v245, v56\n" \
"v_cvt_pk_bf16_f32 v51, v54, v55\n" \
"v_mfma_f32_16x16x32_bf16 v[124:127], a[248:251], a[184:187], v[124:127]\n" \
"v_exp_f32 v57, v57\n" \
"v_mfma_f32_16x16x32_bf16 v[124:127], a[252:255], a[188:191], v[124:127]\n" \
"ds_read_b128 a[228:231], v247 offset:12288\n" \
"v_add_f32 v245, v245, v57\n" \
"v_mfma_f32_16x16x32_bf16 v[116:119], a[248:251], a[168:171], v[232:235]\n" \
"v_exp_f32 v58, v58\n" \
"v_mfma_f32_16x16x32_bf16 v[116:119], a[252:255], a[172:175], v[116:119]\n" \
"v_add_f32 v245, v245, v58\n" \
"v_cvt_pk_bf16_f32 v56, v56, v57\n" \
"v_mfma_f32_16x16x32_bf16 v[116:119], a[240:243], a[160:163], v[116:119]\n" \
"v_exp_f32 v59, v59\n" \
"v_mfma_f32_16x16x32_bf16 v[116:119], a[244:247], a[164:167], v[116:119]\n" \
"ds_read_b128 a[232:235], v247 offset:5120\n" \
"v_add_f32 v245, v245, v59\n" \
"v_mfma_f32_16x16x32_bf16 v[108:111], a[240:243], a[144:147], v[228:231]\n" \
"v_exp_f32 v60, v60\n" \
"v_mfma_f32_16x16x32_bf16 v[108:111], a[244:247], a[148:151], v[108:111]\n" \
"v_add_f32 v245, v245, v60\n" \
"v_cvt_pk_bf16_f32 v57, v58, v59\n" \
"v_mfma_f32_16x16x32_bf16 v[108:111], a[248:251], a[152:155], v[108:111]\n" \
"v_exp_f32 v61, v61\n" \
"v_mfma_f32_16x16x32_bf16 v[108:111], a[252:255], a[156:159], v[108:111]\n" \
"ds_read_b128 a[236:239], v247 offset:13312\n" \
"v_add_f32 v245, v245, v61\n" \
"v_mfma_f32_16x16x32_bf16 v[100:103], a[248:251], a[136:139], v[224:227]\n" \
"v_exp_f32 v62, v62\n" \
"v_mfma_f32_16x16x32_bf16 v[100:103], a[252:255], a[140:143], v[100:103]\n" \
"v_add_f32 v245, v245, v62\n" \
"v_cvt_pk_bf16_f32 v58, v60, v61\n" \
"v_mfma_f32_16x16x32_bf16 v[100:103], a[240:243], a[128:131], v[100:103]\n" \
"s_waitcnt vmcnt(10) lgkmcnt(4)\n" \
"v_exp_f32 v63, v63\n" \
"v_mfma_f32_16x16x32_bf16 v[100:103], a[244:247], a[132:135], v[100:103]\n" \
"s_barrier\n" \
"ds_read_b128 a[240:243], v247 offset:6144\n" \
"v_add_f32 v245, v245, v63\n" \
"v_mfma_f32_16x16x32_bf16 a[0:3], a[192:195], v[0:3], a[0:3]\n" \
"buffer_store_dword v202, v203, s[56:59], s84 offen sc1\n" \
"v_mfma_f32_16x16x32_bf16 a[0:3], a[196:199], v[32:35], a[0:3]\n" \
"s_mov_b32 s84, 0x20000000\n" \
"v_cvt_pk_bf16_f32 v59, v62, v63\n" \
"v_exp_f32 v64, v64\n" \
"v_mfma_f32_16x16x32_bf16 a[32:35], a[192:195], v[8:11], a[32:35]\n" \
"s_mov_b32 m0, s73\n" \
"ds_read_b128 a[244:247], v247 offset:14336\n" \
"v_add_f32 v242, v242, v64\n" \
"v_mfma_f32_16x16x32_bf16 a[32:35], a[196:199], v[40:43], a[32:35]\n" \
"ds_read_b128 a[248:251], v247 offset:7168\n" \
"global_load_lds_dwordx4 v250, s[36:37] offset:0 sc1\n" \
"v_exp_f32 v65, v65\n" \
"v_mfma_f32_16x16x32_bf16 v[128:131], a[192:195], v[16:19], v[128:131]\n" \
"ds_read_b128 a[252:255], v247 offset:15360\n" \
"v_add_f32 v242, v242, v65\n" \
"v_exp_f32 v66, v66\n" \
"v_mfma_f32_16x16x32_bf16 v[128:131], a[196:199], v[48:51], v[128:131]\n" \
"v_add_f32 v242, v242, v66\n" \
"v_cvt_pk_bf16_f32 v64, v64, v65\n" \
"v_mfma_f32_16x16x32_bf16 v[160:163], a[192:195], v[24:27], v[160:163]\n" \
"v_exp_f32 v67, v67\n" \
"v_mfma_f32_16x16x32_bf16 v[160:163], a[196:199], v[56:59], v[160:163]\n" \
"ds_read_b128 a[192:195], v246 offset:0\n" \
"v_add_f32 v242, v242, v67\n" \
"v_mfma_f32_16x16x32_bf16 v[164:167], a[200:203], v[24:27], v[164:167]\n" \
"v_exp_f32 v68, v68\n" \
"v_mfma_f32_16x16x32_bf16 v[164:167], a[204:207], v[56:59], v[164:167]\n" \
"v_add_f32 v242, v242, v68\n" \
"v_cvt_pk_bf16_f32 v65, v66, v67\n" \
"v_mfma_f32_16x16x32_bf16 v[132:135], a[200:203], v[16:19], v[132:135]\n" \
"v_exp_f32 v69, v69\n" \
"v_mfma_f32_16x16x32_bf16 v[132:135], a[204:207], v[48:51], v[132:135]\n" \
"ds_read_b128 a[196:199], v246 offset:2048\n" \
"v_add_f32 v242, v242, v69\n" \
"v_mfma_f32_16x16x32_bf16 a[36:39], a[200:203], v[8:11], a[36:39]\n" \
"v_exp_f32 v70, v70\n" \
"v_mfma_f32_16x16x32_bf16 a[36:39], a[204:207], v[40:43], a[36:39]\n" \
"v_add_f32 v242, v242, v70\n" \
"v_cvt_pk_bf16_f32 v66, v68, v69\n" \
"v_mfma_f32_16x16x32_bf16 a[4:7], a[200:203], v[0:3], a[4:7]\n" \
"v_exp_f32 v71, v71\n" \
"v_mfma_f32_16x16x32_bf16 a[4:7], a[204:207], v[32:35], a[4:7]\n" \
"ds_read_b128 a[200:203], v246 offset:4096\n" \
"global_load_lds_dwordx4 v250, s[36:37] offset:1024 sc1\n" \
"v_add_f32 v242, v242, v71\n" \
"v_mfma_f32_16x16x32_bf16 a[8:11], a[208:211], v[0:3], a[8:11]\n" \
"v_exp_f32 v72, v72\n" \
"v_mfma_f32_16x16x32_bf16 a[8:11], a[212:215], v[32:35], a[8:11]\n" \
"v_add_f32 v243, v243, v72\n" \
"v_cvt_pk_bf16_f32 v67, v70, v71\n" \
"v_mfma_f32_16x16x32_bf16 a[40:43], a[208:211], v[8:11], a[40:43]\n" \
"v_exp_f32 v73, v73\n" \
"v_mfma_f32_16x16x32_bf16 a[40:43], a[212:215], v[40:43], a[40:43]\n" \
"ds_read_b128 a[204:207], v246 offset:6144\n" \
"v_add_f32 v243, v243, v73\n" \
"v_mfma_f32_16x16x32_bf16 v[136:139], a[208:211], v[16:19], v[136:139]\n" \
"v_exp_f32 v74, v74\n" \
"v_mfma_f32_16x16x32_bf16 v[136:139], a[212:215], v[48:51], v[136:139]\n" \
"v_add_f32 v243, v243, v74\n" \
"v_cvt_pk_bf16_f32 v72, v72, v73\n" \
"v_mfma_f32_16x16x32_bf16 v[168:171], a[208:211], v[24:27], v[168:171]\n" \
"v_exp_f32 v75, v75\n" \
"v_mfma_f32_16x16x32_bf16 v[168:171], a[212:215], v[56:59], v[168:171]\n" \
"ds_read_b128 a[208:211], v246 offset:256\n" \
"v_add_f32 v243, v243, v75\n" \
"v_mfma_f32_16x16x32_bf16 v[172:175], a[216:219], v[24:27], v[172:175]\n" \
"v_exp_f32 v76, v76\n" \
"v_mfma_f32_16x16x32_bf16 v[172:175], a[220:223], v[56:59], v[172:175]\n" \
"v_add_f32 v243, v243, v76\n" \
"v_cvt_pk_bf16_f32 v73, v74, v75\n" \
"v_mfma_f32_16x16x32_bf16 v[140:143], a[216:219], v[16:19], v[140:143]\n" \
"v_exp_f32 v77, v77\n" \
"v_mfma_f32_16x16x32_bf16 v[140:143], a[220:223], v[48:51], v[140:143]\n" \
"ds_read_b128 a[212:215], v246 offset:2304\n" \
"global_load_lds_dwordx4 v250, s[36:37] offset:2048 sc1\n" \
"v_add_f32 v243, v243, v77\n" \
"v_mfma_f32_16x16x32_bf16 a[44:47], a[216:219], v[8:11], a[44:47]\n" \
"v_exp_f32 v78, v78\n" \
"v_mfma_f32_16x16x32_bf16 a[44:47], a[220:223], v[40:43], a[44:47]\n" \
"v_add_f32 v243, v243, v78\n" \
"v_cvt_pk_bf16_f32 v74, v76, v77\n" \
"v_mfma_f32_16x16x32_bf16 a[12:15], a[216:219], v[0:3], a[12:15]\n" \
"s_waitcnt lgkmcnt(6)\n" \
"v_exp_f32 v79, v79\n" \
"v_mfma_f32_16x16x32_bf16 a[12:15], a[220:223], v[32:35], a[12:15]\n" \
"ds_read_b128 a[216:219], v246 offset:4352\n" \
"v_add_f32 v243, v243, v79\n" \
"v_mfma_f32_16x16x32_bf16 a[16:19], a[224:227], v[0:3], a[16:19]\n" \
"v_exp_f32 v80, v80\n" \
"v_mfma_f32_16x16x32_bf16 a[16:19], a[228:231], v[32:35], a[16:19]\n" \
"v_add_f32 v244, v244, v80\n" \
"v_cvt_pk_bf16_f32 v75, v78, v79\n" \
"v_mfma_f32_16x16x32_bf16 a[48:51], a[224:227], v[8:11], a[48:51]\n" \
"v_exp_f32 v81, v81\n" \
"v_mfma_f32_16x16x32_bf16 a[48:51], a[228:231], v[40:43], a[48:51]\n" \
"ds_read_b128 a[220:223], v246 offset:6400\n" \
"v_add_f32 v244, v244, v81\n" \
"v_mfma_f32_16x16x32_bf16 v[144:147], a[224:227], v[16:19], v[144:147]\n" \
"v_exp_f32 v82, v82\n" \
"v_mfma_f32_16x16x32_bf16 v[144:147], a[228:231], v[48:51], v[144:147]\n" \
"v_add_f32 v244, v244, v82\n" \
"v_cvt_pk_bf16_f32 v80, v80, v81\n" \
"v_mfma_f32_16x16x32_bf16 v[176:179], a[224:227], v[24:27], v[176:179]\n" \
"v_exp_f32 v83, v83\n" \
"v_mfma_f32_16x16x32_bf16 v[176:179], a[228:231], v[56:59], v[176:179]\n" \
"ds_read_b128 a[224:227], v246 offset:8192\n" \
"global_load_lds_dwordx4 v250, s[36:37] offset:3072 sc1\n" \
"v_add_f32 v244, v244, v83\n" \
"v_mfma_f32_16x16x32_bf16 v[180:183], a[232:235], v[24:27], v[180:183]\n" \
"s_add_u32 s36, s36, 17408\n" \
"v_exp_f32 v84, v84\n" \
"v_mfma_f32_16x16x32_bf16 v[180:183], a[236:239], v[56:59], v[180:183]\n" \
"v_add_f32 v244, v244, v84\n" \
"v_cvt_pk_bf16_f32 v81, v82, v83\n" \
"v_mfma_f32_16x16x32_bf16 v[148:151], a[232:235], v[16:19], v[148:151]\n" \
"v_exp_f32 v85, v85\n" \
"v_mfma_f32_16x16x32_bf16 v[148:151], a[236:239], v[48:51], v[148:151]\n" \
"ds_read_b128 a[228:231], v246 offset:10240\n" \
"v_add_f32 v244, v244, v85\n" \
"v_mfma_f32_16x16x32_bf16 a[52:55], a[232:235], v[8:11], a[52:55]\n" \
"v_exp_f32 v86, v86\n" \
"v_mfma_f32_16x16x32_bf16 a[52:55], a[236:239], v[40:43], a[52:55]\n" \
"v_add_f32 v244, v244, v86\n" \
"v_cvt_pk_bf16_f32 v82, v84, v85\n" \
"v_mfma_f32_16x16x32_bf16 a[20:23], a[232:235], v[0:3], a[20:23]\n" \
"v_exp_f32 v87, v87\n" \
"v_mfma_f32_16x16x32_bf16 a[20:23], a[236:239], v[32:35], a[20:23]\n" \
"ds_read_b128 a[232:235], v246 offset:12288\n" \
"v_add_f32 v244, v244, v87\n" \
"v_mfma_f32_16x16x32_bf16 a[24:27], a[240:243], v[0:3], a[24:27]\n" \
"v_exp_f32 v88, v88\n" \
"v_mfma_f32_16x16x32_bf16 a[24:27], a[244:247], v[32:35], a[24:27]\n" \
"v_add_f32 v245, v245, v88\n" \
"v_cvt_pk_bf16_f32 v83, v86, v87\n" \
"v_mfma_f32_16x16x32_bf16 a[56:59], a[240:243], v[8:11], a[56:59]\n" \
"v_exp_f32 v89, v89\n" \
"v_mfma_f32_16x16x32_bf16 a[56:59], a[244:247], v[40:43], a[56:59]\n" \
"ds_read_b128 a[236:239], v246 offset:14336\n" \
"v_add_f32 v245, v245, v89\n" \
"v_mfma_f32_16x16x32_bf16 v[152:155], a[240:243], v[16:19], v[152:155]\n" \
"v_exp_f32 v90, v90\n" \
"v_mfma_f32_16x16x32_bf16 v[152:155], a[244:247], v[48:51], v[152:155]\n" \
"v_add_f32 v245, v245, v90\n" \
"v_cvt_pk_bf16_f32 v88, v88, v89\n" \
"v_mfma_f32_16x16x32_bf16 v[184:187], a[240:243], v[24:27], v[184:187]\n" \
"v_exp_f32 v91, v91\n" \
"v_mfma_f32_16x16x32_bf16 v[184:187], a[244:247], v[56:59], v[184:187]\n" \
"ds_read_b128 a[240:243], v246 offset:8448\n" \
"v_add_f32 v245, v245, v91\n" \
"v_mfma_f32_16x16x32_bf16 v[188:191], a[248:251], v[24:27], v[188:191]\n" \
"v_exp_f32 v92, v92\n" \
"v_mfma_f32_16x16x32_bf16 v[188:191], a[252:255], v[56:59], v[188:191]\n" \
"v_add_f32 v245, v245, v92\n" \
"v_cvt_pk_bf16_f32 v89, v90, v91\n" \
"v_mfma_f32_16x16x32_bf16 v[156:159], a[248:251], v[16:19], v[156:159]\n" \
"v_exp_f32 v93, v93\n" \
"v_mfma_f32_16x16x32_bf16 v[156:159], a[252:255], v[48:51], v[156:159]\n" \
"ds_read_b128 a[244:247], v246 offset:10496\n" \
"v_add_f32 v245, v245, v93\n" \
"v_mfma_f32_16x16x32_bf16 a[60:63], a[248:251], v[8:11], a[60:63]\n" \
"v_exp_f32 v94, v94\n" \
"v_mfma_f32_16x16x32_bf16 a[60:63], a[252:255], v[40:43], a[60:63]\n" \
"v_add_f32 v245, v245, v94\n" \
"v_cvt_pk_bf16_f32 v90, v92, v93\n" \
"v_mfma_f32_16x16x32_bf16 a[28:31], a[248:251], v[0:3], a[28:31]\n" \
"s_waitcnt lgkmcnt(6)\n" \
"v_exp_f32 v95, v95\n" \
"v_mfma_f32_16x16x32_bf16 a[28:31], a[252:255], v[32:35], a[28:31]\n" \
"ds_read_b128 a[248:251], v246 offset:12544\n" \
"v_add_f32 v245, v245, v95\n" \
"v_mfma_f32_16x16x32_bf16 v[0:3], a[192:195], a[128:131], v[224:227]\n" \
"v_exp_f32 v96, v96\n" \
"v_mfma_f32_16x16x32_bf16 v[0:3], a[196:199], a[132:135], v[0:3]\n" \
"v_cvt_pk_bf16_f32 v91, v94, v95\n" \
"v_add_f32 v242, v242, v96\n" \
"v_mfma_f32_16x16x32_bf16 v[0:3], a[200:203], a[136:139], v[0:3]\n" \
"s_add_u32 m0, s74, 0x4000\n" \
"v_exp_f32 v97, v97\n" \
"v_mfma_f32_16x16x32_bf16 v[0:3], a[204:207], a[140:143], v[0:3]\n" \
"ds_read_b128 a[252:255], v246 offset:14592\n" \
"global_load_lds_dwordx4 v250, s[38:39] offset:0 sc1\n" \
"v_add_f32 v242, v242, v97\n" \
"v_mfma_f32_16x16x32_bf16 v[8:11], a[200:203], a[152:155], v[228:231]\n" \
"v_exp_f32 v98, v98\n" \
"v_mfma_f32_16x16x32_bf16 v[8:11], a[204:207], a[156:159], v[8:11]\n" \
"v_add_f32 v242, v242, v98\n" \
"v_cvt_pk_bf16_f32 v96, v96, v97\n" \
"v_mfma_f32_16x16x32_bf16 v[8:11], a[192:195], a[144:147], v[8:11]\n" \
"v_exp_f32 v99, v99\n" \
"v_mfma_f32_16x16x32_bf16 v[8:11], a[196:199], a[148:151], v[8:11]\n" \
"v_add_f32 v242, v242, v99\n" \
"v_exp_f32 v100, v100\n" \
"v_mfma_f32_16x16x32_bf16 v[16:19], a[192:195], a[160:163], v[232:235]\n" \
"v_add_f32 v242, v242, v100\n" \
"v_cvt_pk_bf16_f32 v97, v98, v99\n" \
"v_mfma_f32_16x16x32_bf16 v[16:19], a[196:199], a[164:167], v[16:19]\n" \
"v_exp_f32 v101, v101\n" \
"v_mfma_f32_16x16x32_bf16 v[16:19], a[200:203], a[168:171], v[16:19]\n" \
"v_add_f32 v242, v242, v101\n" \
"v_mfma_f32_16x16x32_bf16 v[16:19], a[204:207], a[172:175], v[16:19]\n" \
"v_exp_f32 v102, v102\n" \
"v_mfma_f32_16x16x32_bf16 v[24:27], a[200:203], a[184:187], v[236:239]\n" \
"v_add_f32 v242, v242, v102\n" \
"v_cvt_pk_bf16_f32 v98, v100, v101\n" \
"v_mfma_f32_16x16x32_bf16 v[24:27], a[204:207], a[188:191], v[24:27]\n" \
"v_exp_f32 v103, v103\n" \
"v_mfma_f32_16x16x32_bf16 v[24:27], a[192:195], a[176:179], v[24:27]\n" \
"v_add_f32 v242, v242, v103\n" \
"v_mfma_f32_16x16x32_bf16 v[24:27], a[196:199], a[180:183], v[24:27]\n" \
"ds_read_b128 a[192:195], v247 offset:16384\n" \
"global_load_lds_dwordx4 v250, s[38:39] offset:1024 sc1\n" \
"v_mfma_f32_16x16x32_bf16 v[28:31], a[208:211], a[176:179], v[236:239]\n" \
"v_exp_f32 v104, v104\n" \
"v_mfma_f32_16x16x32_bf16 v[28:31], a[212:215], a[180:183], v[28:31]\n" \
"v_add_f32 v243, v243, v104\n" \
"v_cvt_pk_bf16_f32 v99, v102, v103\n" \
"v_mfma_f32_16x16x32_bf16 v[28:31], a[216:219], a[184:187], v[28:31]\n" \
"v_exp_f32 v105, v105\n" \
"v_mfma_f32_16x16x32_bf16 v[28:31], a[220:223], a[188:191], v[28:31]\n" \
"ds_read_b128 a[196:199], v247 offset:24576\n" \
"v_add_f32 v243, v243, v105\n" \
"v_mfma_f32_16x16x32_bf16 v[20:23], a[216:219], a[168:171], v[232:235]\n" \
"v_exp_f32 v106, v106\n" \
"v_mfma_f32_16x16x32_bf16 v[20:23], a[220:223], a[172:175], v[20:23]\n" \
"v_add_f32 v243, v243, v106\n" \
"v_cvt_pk_bf16_f32 v104, v104, v105\n" \
"v_mfma_f32_16x16x32_bf16 v[20:23], a[208:211], a[160:163], v[20:23]\n" \
"v_exp_f32 v107, v107\n" \
"v_mfma_f32_16x16x32_bf16 v[20:23], a[212:215], a[164:167], v[20:23]\n" \
"ds_read_b128 a[200:203], v247 offset:17408\n" \
"v_add_f32 v243, v243, v107\n" \
"v_mfma_f32_16x16x32_bf16 v[12:15], a[208:211], a[144:147], v[228:231]\n" \
"v_exp_f32 v108, v108\n" \
"v_mfma_f32_16x16x32_bf16 v[12:15], a[212:215], a[148:151], v[12:15]\n" \
"v_add_f32 v243, v243, v108\n" \
"v_cvt_pk_bf16_f32 v105, v106, v107\n" \
"v_mfma_f32_16x16x32_bf16 v[12:15], a[216:219], a[152:155], v[12:15]\n" \
"v_exp_f32 v109, v109\n" \
"v_mfma_f32_16x16x32_bf16 v[12:15], a[220:223], a[156:159], v[12:15]\n" \
"ds_read_b128 a[204:207], v247 offset:25600\n" \
"global_load_lds_dwordx4 v250, s[38:39] offset:2048 sc1\n" \
"v_add_f32 v243, v243, v109\n" \
"v_mfma_f32_16x16x32_bf16 v[4:7], a[216:219], a[136:139], v[224:227]\n" \
"v_exp_f32 v110, v110\n" \
"v_mfma_f32_16x16x32_bf16 v[4:7], a[220:223], a[140:143], v[4:7]\n" \
"v_add_f32 v243, v243, v110\n" \
"v_cvt_pk_bf16_f32 v106, v108, v109\n" \
"v_mfma_f32_16x16x32_bf16 v[4:7], a[208:211], a[128:131], v[4:7]\n" \
"s_waitcnt lgkmcnt(4)\n" \
"v_exp_f32 v111, v111\n" \
"v_mfma_f32_16x16x32_bf16 v[4:7], a[212:215], a[132:135], v[4:7]\n" \
"ds_read_b128 a[208:211], v247 offset:18432\n" \
"v_add_f32 v243, v243, v111\n" \
"v_mfma_f32_16x16x32_bf16 v[32:35], a[224:227], a[128:131], v[224:227]\n" \
"v_exp_f32 v112, v112\n" \
"v_mfma_f32_16x16x32_bf16 v[32:35], a[228:231], a[132:135], v[32:35]\n" \
"v_add_f32 v244, v244, v112\n" \
"v_cvt_pk_bf16_f32 v107, v110, v111\n" \
"v_mfma_f32_16x16x32_bf16 v[32:35], a[232:235], a[136:139], v[32:35]\n" \
"v_exp_f32 v113, v113\n" \
"v_mfma_f32_16x16x32_bf16 v[32:35], a[236:239], a[140:143], v[32:35]\n" \
"ds_read_b128 a[212:215], v247 offset:26624\n" \
"v_add_f32 v244, v244, v113\n" \
"v_mfma_f32_16x16x32_bf16 v[40:43], a[232:235], a[152:155], v[228:231]\n" \
"v_exp_f32 v114, v114\n" \
"v_mfma_f32_16x16x32_bf16 v[40:43], a[236:239], a[156:159], v[40:43]\n" \
"v_add_f32 v244, v244, v114\n" \
"v_cvt_pk_bf16_f32 v112, v112, v113\n" \
"v_mfma_f32_16x16x32_bf16 v[40:43], a[224:227], a[144:147], v[40:43]\n" \
"v_exp_f32 v115, v115\n" \
"v_mfma_f32_16x16x32_bf16 v[40:43], a[228:231], a[148:151], v[40:43]\n" \
"ds_read_b128 a[216:219], v247 offset:19456\n" \
"global_load_lds_dwordx4 v250, s[38:39] offset:3072 sc1\n" \
"v_add_f32 v244, v244, v115\n" \
"v_exp_f32 v116, v116\n" \
"v_mfma_f32_16x16x32_bf16 v[48:51], a[224:227], a[160:163], v[232:235]\n" \
"s_add_u32 s38, s38, 17408\n" \
"v_add_f32 v244, v244, v116\n" \
"v_cvt_pk_bf16_f32 v113, v114, v115\n" \
"v_mfma_f32_16x16x32_bf16 v[48:51], a[228:231], a[164:167], v[48:51]\n" \
"global_load_dword v253, v252, s[40:41]\n" \
"v_mfma_f32_16x16x32_bf16 v[48:51], a[232:235], a[168:171], v[48:51]\n" \
"s_sub_u32 s80, s80, 1\n" \
"v_exp_f32 v117, v117\n" \
"v_mfma_f32_16x16x32_bf16 v[48:51], a[236:239], a[172:175], v[48:51]\n" \
"s_cmp_gt_i32 s80, 0\n" \
"ds_read_b128 a[220:223], v247 offset:27648\n" \
"v_add_f32 v244, v244, v117\n" \
"v_mfma_f32_16x16x32_bf16 v[56:59], a[232:235], a[184:187], v[236:239]\n" \
"s_cselect_b32 s81, 0x80000, 0\n" \
"v_exp_f32 v118, v118\n" \
"v_mfma_f32_16x16x32_bf16 v[56:59], a[236:239], a[188:191], v[56:59]\n" \
"s_add_u32 s40, s40, s81\n" \
"v_add_f32 v244, v244, v118\n" \
"v_cvt_pk_bf16_f32 v114, v116, v117\n" \
"v_mfma_f32_16x16x32_bf16 v[56:59], a[224:227], a[176:179], v[56:59]\n" \
"s_addc_u32 s41, s41, 0\n" \
"v_exp_f32 v119, v119\n" \
"v_mfma_f32_16x16x32_bf16 v[56:59], a[228:231], a[180:183], v[56:59]\n" \
"s_load_dwordx8 s[44:51], s[56:57], s79 glc\n" \
"ds_read_b128 a[224:227], v247 offset:20480\n" \
"v_add_f32 v244, v244, v119\n" \
"v_mfma_f32_16x16x32_bf16 v[60:63], a[240:243], a[176:179], v[236:239]\n" \
"s_xor_b32 s74, s74, 0x8000\n" \
"v_exp_f32 v120, v120\n" \
"v_mfma_f32_16x16x32_bf16 v[60:63], a[244:247], a[180:183], v[60:63]\n" \
"s_sub_u32 s75, s75, 1\n" \
"v_add_f32 v245, v245, v120\n" \
"v_cvt_pk_bf16_f32 v115, v118, v119\n" \
"v_mfma_f32_16x16x32_bf16 v[60:63], a[248:251], a[184:187], v[60:63]\n" \
"s_cmp_lg_u32 s75, 0\n" \
"v_exp_f32 v121, v121\n" \
"v_mfma_f32_16x16x32_bf16 v[60:63], a[252:255], a[188:191], v[60:63]\n" \
"ds_read_b128 a[228:231], v247 offset:28672\n" \
"v_add_f32 v245, v245, v121\n" \
"v_mfma_f32_16x16x32_bf16 v[52:55], a[248:251], a[168:171], v[232:235]\n" \
"v_exp_f32 v122, v122\n" \
"v_mfma_f32_16x16x32_bf16 v[52:55], a[252:255], a[172:175], v[52:55]\n" \
"v_add_f32 v245, v245, v122\n" \
"v_cvt_pk_bf16_f32 v120, v120, v121\n" \
"v_mfma_f32_16x16x32_bf16 v[52:55], a[240:243], a[160:163], v[52:55]\n" \
"v_exp_f32 v123, v123\n" \
"v_mfma_f32_16x16x32_bf16 v[52:55], a[244:247], a[164:167], v[52:55]\n" \
"ds_read_b128 a[232:235], v247 offset:21504\n" \
"v_add_f32 v245, v245, v123\n" \
"v_mfma_f32_16x16x32_bf16 v[44:47], a[240:243], a[144:147], v[228:231]\n" \
"v_exp_f32 v124, v124\n" \
"v_mfma_f32_16x16x32_bf16 v[44:47], a[244:247], a[148:151], v[44:47]\n" \
"v_add_f32 v245, v245, v124\n" \
"v_cvt_pk_bf16_f32 v121, v122, v123\n" \
"v_mfma_f32_16x16x32_bf16 v[44:47], a[248:251], a[152:155], v[44:47]\n" \
"v_exp_f32 v125, v125\n" \
"v_mfma_f32_16x16x32_bf16 v[44:47], a[252:255], a[156:159], v[44:47]\n" \
"ds_read_b128 a[236:239], v247 offset:29696\n" \
"v_add_f32 v245, v245, v125\n" \
"v_mfma_f32_16x16x32_bf16 v[36:39], a[248:251], a[136:139], v[224:227]\n" \
"v_exp_f32 v126, v126\n" \
"v_mfma_f32_16x16x32_bf16 v[36:39], a[252:255], a[140:143], v[36:39]\n" \
"v_add_f32 v245, v245, v126\n" \
"v_cvt_pk_bf16_f32 v122, v124, v125\n" \
"v_mfma_f32_16x16x32_bf16 v[36:39], a[240:243], a[128:131], v[36:39]\n" \
"s_waitcnt vmcnt(11) lgkmcnt(4)\n" \
"v_exp_f32 v127, v127\n" \
"v_mfma_f32_16x16x32_bf16 v[36:39], a[244:247], a[132:135], v[36:39]\n" \
"s_barrier\n" \
"ds_read_b128 a[240:243], v247 offset:22528\n" \
"v_add_f32 v245, v245, v127\n" \
"s_cbranch_scc1 Lloop_%=\n" \
"v_mfma_f32_16x16x32_bf16 a[0:3], a[192:195], v[64:67], a[0:3]\n" \
"v_exp_f32 v8, v8\n" \
"v_mfma_f32_16x16x32_bf16 a[0:3], a[196:199], v[96:99], a[0:3]\n" \
"v_cvt_pk_bf16_f32 v123, v126, v127\n" \
"v_exp_f32 v9, v9\n" \
"v_mfma_f32_16x16x32_bf16 a[32:35], a[192:195], v[72:75], a[32:35]\n" \
"s_add_u32 m0, s73, 0x4000\n" \
"ds_read_b128 a[244:247], v247 offset:30720\n" \
"v_exp_f32 v10, v10\n" \
"v_mfma_f32_16x16x32_bf16 a[32:35], a[196:199], v[104:107], a[32:35]\n" \
"ds_read_b128 a[248:251], v247 offset:23552\n" \
"global_load_lds_dwordx4 v250, s[36:37] offset:0 sc1\n" \
"v_mfma_f32_16x16x32_bf16 v[128:131], a[192:195], v[80:83], v[128:131]\n" \
"ds_read_b128 a[252:255], v247 offset:31744\n" \
"v_cvt_pk_bf16_f32 v8, v8, v9\n" \
"v_mfma_f32_16x16x32_bf16 v[128:131], a[196:199], v[112:115], v[128:131]\n" \
"v_exp_f32 v11, v11\n" \
"v_mfma_f32_16x16x32_bf16 v[160:163], a[192:195], v[88:91], v[160:163]\n" \
"v_exp_f32 v12, v12\n" \
"v_mfma_f32_16x16x32_bf16 v[160:163], a[196:199], v[120:123], v[160:163]\n" \
"ds_read_b128 a[192:195], v246 offset:16384\n" \
"v_xor_b32 v247, 0x8000, v247\n" \
"v_cvt_pk_bf16_f32 v9, v10, v11\n" \
"v_mfma_f32_16x16x32_bf16 v[164:167], a[200:203], v[88:91], v[164:167]\n" \
"v_exp_f32 v13, v13\n" \
"v_mfma_f32_16x16x32_bf16 v[164:167], a[204:207], v[120:123], v[164:167]\n" \
"v_exp_f32 v14, v14\n" \
"v_cvt_pk_bf16_f32 v10, v12, v13\n" \
"v_mfma_f32_16x16x32_bf16 v[132:135], a[200:203], v[80:83], v[132:135]\n" \
"v_exp_f32 v15, v15\n" \
"v_mfma_f32_16x16x32_bf16 v[132:135], a[204:207], v[112:115], v[132:135]\n" \
"ds_read_b128 a[196:199], v246 offset:18432\n" \
"v_cmp_gt_i32 vcc, 0, v251\n" \
"v_cmp_gt_i32 s[64:65], 1, v251\n" \
"v_mfma_f32_16x16x32_bf16 a[36:39], a[200:203], v[72:75], a[36:39]\n" \
"v_cmp_gt_i32 s[66:67], 2, v251\n" \
"v_cndmask_b32 v0, v0, v254, vcc\n" \
"v_mfma_f32_16x16x32_bf16 a[36:39], a[204:207], v[104:107], a[36:39]\n" \
"v_cmp_gt_i32 vcc, 3, v251\n" \
"v_cndmask_b32 v1, v1, v254, s[64:65]\n" \
"v_mfma_f32_16x16x32_bf16 a[4:7], a[200:203], v[64:67], a[4:7]\n" \
"v_cmp_gt_i32 s[64:65], 16, v251\n" \
"v_cndmask_b32 v2, v2, v254, s[66:67]\n" \
"v_mfma_f32_16x16x32_bf16 a[4:7], a[204:207], v[96:99], a[4:7]\n" \
"ds_read_b128 a[200:203], v246 offset:20480\n" \
"global_load_lds_dwordx4 v250, s[36:37] offset:1024 sc1\n" \
"v_mfma_f32_16x16x32_bf16 a[8:11], a[208:211], v[64:67], a[8:11]\n" \
"v_cmp_gt_i32 s[66:67], 17, v251\n" \
"v_cndmask_b32 v3, v3, v254, vcc\n" \
"v_mfma_f32_16x16x32_bf16 a[8:11], a[212:215], v[96:99], a[8:11]\n" \
"v_cmp_gt_i32 vcc, 18, v251\n" \
"v_cndmask_b32 v4, v4, v254, s[64:65]\n" \
"v_mfma_f32_16x16x32_bf16 a[40:43], a[208:211], v[72:75], a[40:43]\n" \
"v_cmp_gt_i32 s[64:65], 19, v251\n" \
"v_cndmask_b32 v5, v5, v254, s[66:67]\n" \
"v_mfma_f32_16x16x32_bf16 a[40:43], a[212:215], v[104:107], a[40:43]\n" \
"ds_read_b128 a[204:207], v246 offset:22528\n" \
"v_cmp_gt_i32 s[66:67], 32, v251\n" \
"v_mfma_f32_16x16x32_bf16 v[136:139], a[208:211], v[80:83], v[136:139]\n" \
"v_cndmask_b32 v6, v6, v254, vcc\n" \
"v_cmp_gt_i32 vcc, 33, v251\n" \
"v_mfma_f32_16x16x32_bf16 v[136:139], a[212:215], v[112:115], v[136:139]\n" \
"v_cndmask_b32 v7, v7, v254, s[64:65]\n" \
"v_cmp_gt_i32 s[64:65], 34, v251\n" \
"v_mfma_f32_16x16x32_bf16 v[168:171], a[208:211], v[88:91], v[168:171]\n" \
"v_cndmask_b32 v32, v32, v254, s[66:67]\n" \
"v_cmp_gt_i32 s[66:67], 35, v251\n" \
"v_mfma_f32_16x16x32_bf16 v[168:171], a[212:215], v[120:123], v[168:171]\n" \
"ds_read_b128 a[208:211], v246 offset:16640\n" \
"v_cndmask_b32 v33, v33, v254, vcc\n" \
"v_mfma_f32_16x16x32_bf16 v[172:175], a[216:219], v[88:91], v[172:175]\n" \
"v_cmp_gt_i32 vcc, 48, v251\n" \
"v_cndmask_b32 v34, v34, v254, s[64:65]\n" \
"Lpg6171_%=:\n" \
"s_cbranch_execz Lpg7550_%=\n" \
"v_mfma_f32_16x16x32_bf16 v[172:175], a[220:223], v[120:123], v[172:175]\n" \
"v_cmp_gt_i32 s[64:65], 49, v251\n" \
"v_cndmask_b32 v35, v35, v254, s[66:67]\n" \
"v_mfma_f32_16x16x32_bf16 v[140:143], a[216:219], v[80:83], v[140:143]\n" \
"v_cmp_gt_i32 s[66:67], 50, v251\n" \
"v_cndmask_b32 v36, v36, v254, vcc\n" \
"v_mfma_f32_16x16x32_bf16 v[140:143], a[220:223], v[112:115], v[140:143]\n" \
"ds_read_b128 a[212:215], v246 offset:18688\n" \
"global_load_lds_dwordx4 v250, s[36:37] offset:2048 sc1\n" \
"v_mfma_f32_16x16x32_bf16 a[44:47], a[216:219], v[72:75], a[44:47]\n" \
"v_cmp_gt_i32 vcc, 51, v251\n" \
"v_cndmask_b32 v37, v37, v254, s[64:65]\n" \
"v_mfma_f32_16x16x32_bf16 a[44:47], a[220:223], v[104:107], a[44:47]\n" \
"v_cndmask_b32 v38, v38, v254, s[66:67]\n" \
"v_cndmask_b32 v39, v39, v254, vcc\n" \
"v_mfma_f32_16x16x32_bf16 a[12:15], a[216:219], v[64:67], a[12:15]\n" \
"s_waitcnt lgkmcnt(6)\n" \
"v_exp_f32 v0, v0\n" \
"v_mfma_f32_16x16x32_bf16 a[12:15], a[220:223], v[96:99], a[12:15]\n" \
"ds_read_b128 a[216:219], v246 offset:20736\n" \
"v_cvt_pk_bf16_f32 v11, v14, v15\n" \
"v_mfma_f32_16x16x32_bf16 a[16:19], a[224:227], v[64:67], a[16:19]\n" \
"v_exp_f32 v1, v1\n" \
"v_mfma_f32_16x16x32_bf16 a[16:19], a[228:231], v[96:99], a[16:19]\n" \
"v_exp_f32 v2, v2\n" \
"v_cvt_pk_bf16_f32 v0, v0, v1\n" \
"v_mfma_f32_16x16x32_bf16 a[48:51], a[224:227], v[72:75], a[48:51]\n" \
"v_exp_f32 v3, v3\n" \
"v_mfma_f32_16x16x32_bf16 a[48:51], a[228:231], v[104:107], a[48:51]\n" \
"ds_read_b128 a[220:223], v246 offset:22784\n" \
"global_load_lds_dwordx4 v250, s[36:37] offset:3072 sc1\n" \
"v_mfma_f32_16x16x32_bf16 v[144:147], a[224:227], v[80:83], v[144:147]\n" \
"s_add_u32 s36, s36, 17408\n" \
"v_exp_f32 v4, v4\n" \
"v_mfma_f32_16x16x32_bf16 v[144:147], a[228:231], v[112:115], v[144:147]\n" \
"s_xor_b32 s73, s73, 0x8000\n" \
"v_cvt_pk_bf16_f32 v1, v2, v3\n" \
"v_exp_f32 v5, v5\n" \
"v_mfma_f32_16x16x32_bf16 v[176:179], a[224:227], v[88:91], v[176:179]\n" \
"s_xor_b64 s[52:53], s[44:45], s[42:43]\n" \
"v_exp_f32 v6, v6\n" \
"v_mfma_f32_16x16x32_bf16 v[176:179], a[228:231], v[120:123], v[176:179]\n" \
"s_xor_b64 s[54:55], s[46:47], s[42:43]\n" \
"ds_read_b128 a[224:227], v246 offset:24576\n" \
"v_cvt_pk_bf16_f32 v2, v4, v5\n" \
"v_mfma_f32_16x16x32_bf16 v[180:183], a[232:235], v[88:91], v[180:183]\n" \
"s_or_b64 s[52:53], s[52:53], s[54:55]\n" \
"v_exp_f32 v7, v7\n" \
"v_mfma_f32_16x16x32_bf16 v[180:183], a[236:239], v[120:123], v[180:183]\n" \
"s_xor_b64 s[54:55], s[48:49], s[42:43]\n" \
"v_exp_f32 v16, v16\n" \
"v_cvt_pk_bf16_f32 v3, v6, v7\n" \
"v_mfma_f32_16x16x32_bf16 v[148:151], a[232:235], v[80:83], v[148:151]\n" \
"s_or_b64 s[52:53], s[52:53], s[54:55]\n" \
"v_exp_f32 v17, v17\n" \
"v_mfma_f32_16x16x32_bf16 v[148:151], a[236:239], v[112:115], v[148:151]\n" \
"ds_read_b128 a[228:231], v246 offset:26624\n" \
"s_xor_b64 s[54:55], s[50:51], s[42:43]\n" \
"v_mfma_f32_16x16x32_bf16 a[52:55], a[232:235], v[72:75], a[52:55]\n" \
"s_or_b64 s[52:53], s[52:53], s[54:55]\n" \
"v_exp_f32 v18, v18\n" \
"v_mfma_f32_16x16x32_bf16 a[52:55], a[236:239], v[104:107], a[52:55]\n" \
"v_cvt_pk_bf16_f32 v16, v16, v17\n" \
"v_mfma_f32_16x16x32_bf16 v[240:243], a[124:127], v[8:11], v[240:243]\n" \
"v_exp_f32 v19, v19\n" \
"v_mfma_f32_16x16x32_bf16 v[240:243], a[120:123], v[0:3], v[240:243]\n" \
"s_and_b64 s[52:53], s[52:53], s[8:9]\n" \
"s_cbranch_scc1 Lrs1_%=\n" \
"Lrr1_%=:\n" \
"v_mfma_f32_16x16x32_bf16 a[20:23], a[232:235], v[64:67], a[20:23]\n" \
"s_mov_b64 s[8:9], s[10:11]\n" \
"v_exp_f32 v20, v20\n" \
"v_mfma_f32_16x16x32_bf16 a[20:23], a[236:239], v[96:99], a[20:23]\n" \
"ds_read_b128 a[232:235], v246 offset:28672\n" \
"v_cvt_pk_bf16_f32 v17, v18, v19\n" \
"v_mfma_f32_16x16x32_bf16 a[24:27], a[240:243], v[64:67], a[24:27]\n" \
"v_exp_f32 v21, v21\n" \
"v_mfma_f32_16x16x32_bf16 a[24:27], a[244:247], v[96:99], a[24:27]\n" \
"v_exp_f32 v22, v22\n" \
"v_cvt_pk_bf16_f32 v18, v20, v21\n" \
"v_mfma_f32_16x16x32_bf16 a[56:59], a[240:243], v[72:75], a[56:59]\n" \
"v_exp_f32 v23, v23\n" \
"v_mfma_f32_16x16x32_bf16 a[56:59], a[244:247], v[104:107], a[56:59]\n" \
"ds_read_b128 a[236:239], v246 offset:30720\n" \
"v_exp_f32 v24, v24\n" \
"v_mfma_f32_16x16x32_bf16 v[152:155], a[240:243], v[80:83], v[152:155]\n" \
"v_cvt_pk_bf16_f32 v19, v22, v23\n" \
"v_mfma_f32_16x16x32_bf16 v[152:155], a[244:247], v[112:115], v[152:155]\n" \
"v_exp_f32 v25, v25\n" \
"v_mfma_f32_16x16x32_bf16 v[184:187], a[240:243], v[88:91], v[184:187]\n" \
"v_exp_f32 v26, v26\n" \
"v_mfma_f32_16x16x32_bf16 v[184:187], a[244:247], v[120:123], v[184:187]\n" \
"ds_read_b128 a[240:243], v246 offset:24832\n" \
"v_cvt_pk_bf16_f32 v24, v24, v25\n" \
"v_mfma_f32_16x16x32_bf16 v[242:245], a[120:123], v[16:19], v[242:245]\n" \
"v_exp_f32 v27, v27\n" \
"v_mfma_f32_16x16x32_bf16 v[188:191], a[248:251], v[88:91], v[188:191]\n" \
"v_exp_f32 v28, v28\n" \
"v_mfma_f32_16x16x32_bf16 v[188:191], a[252:255], v[120:123], v[188:191]\n" \
"v_cvt_pk_bf16_f32 v25, v26, v27\n" \
"v_exp_f32 v29, v29\n" \
"v_mfma_f32_16x16x32_bf16 v[156:159], a[248:251], v[80:83], v[156:159]\n" \
"v_exp_f32 v30, v30\n" \
"v_mfma_f32_16x16x32_bf16 v[156:159], a[252:255], v[112:115], v[156:159]\n" \
"ds_read_b128 a[244:247], v246 offset:26880\n" \
"v_cvt_pk_bf16_f32 v26, v28, v29\n" \
"v_mfma_f32_16x16x32_bf16 a[60:63], a[248:251], v[72:75], a[60:63]\n" \
"v_exp_f32 v31, v31\n" \
"v_mfma_f32_16x16x32_bf16 a[60:63], a[252:255], v[104:107], a[60:63]\n" \
"v_cvt_pk_bf16_f32 v27, v30, v31\n" \
"v_mfma_f32_16x16x32_bf16 a[28:31], a[248:251], v[64:67], a[28:31]\n" \
"s_waitcnt lgkmcnt(6)\n" \
"v_mfma_f32_16x16x32_bf16 v[242:245], a[124:127], v[24:27], v[242:245]\n" \
"v_mfma_f32_16x16x32_bf16 a[28:31], a[252:255], v[96:99], a[28:31]\n" \
"ds_read_b128 a[248:251], v246 offset:28928\n" \
"LA1l_%=:\n" \
"Lpg6288_%=:\n" \
"s_cbranch_execz Lpg13272_%=\n" \
"s_getpc_b64 s[40:41]\n" \
"Lpd_%=:\n" \
"s_lshl_b32 s80, s62, 2\n" \
"s_add_u32 s80, s80, s61\n" \
"s_and_b32 s80, s80, 15\n" \
"s_lshl_b32 s80, s80, 12\n" \
"s_mov_b32 s81, Lcode_end_%=-Lpd_%=-4\n" \
"v_lshl_add_u32 v252, v248, 2, s80\n" \
"v_min_u32 v252, s81, v252\n" \
"s_waitcnt lgkmcnt(0)\n" \
"v_mfma_f32_16x16x32_bf16 v[72:75], a[192:195], a[144:147], v[228:231]\n" \
"v_exp_f32 v32, v32\n" \
"v_mfma_f32_16x16x32_bf16 v[72:75], a[196:199], a[148:151], v[72:75]\n" \
"v_cvt_pk_bf16_f32 v27, v30, v31\n" \
"v_mfma_f32_16x16x32_bf16 v[72:75], a[200:203], a[152:155], v[72:75]\n" \
"s_mov_b32 m0, s74\n" \
"v_exp_f32 v33, v33\n" \
"v_mfma_f32_16x16x32_bf16 v[72:75], a[204:207], a[156:159], v[72:75]\n" \
"ds_read_b128 a[252:255], v246 offset:30976\n" \
"global_load_lds_dwordx4 v250, s[38:39] offset:0 sc1\n" \
"v_mfma_f32_16x16x32_bf16 v[80:83], a[200:203], a[168:171], v[232:235]\n" \
"s_lshl_b32 s70, s95, 1\n" \
"v_exp_f32 v34, v34\n" \
"v_mfma_f32_16x16x32_bf16 v[80:83], a[204:207], a[172:175], v[80:83]\n" \
"s_xor_b32 s70, s70, 139264\n" \
"v_cvt_pk_bf16_f32 v32, v32, v33\n" \
"v_add_u32 v64, s70, v248\n" \
"v_mfma_f32_16x16x32_bf16 v[80:83], a[192:195], a[160:163], v[80:83]\n" \
"ds_read_b128 v[192:195], v64 offset:0\n" \
"v_exp_f32 v35, v35\n" \
"v_mfma_f32_16x16x32_bf16 v[80:83], a[196:199], a[164:167], v[80:83]\n" \
"ds_read_b128 v[196:199], v64 offset:1024\n" \
"v_exp_f32 v36, v36\n" \
"v_mfma_f32_16x16x32_bf16 v[88:91], a[192:195], a[176:179], v[236:239]\n" \
"ds_read_b128 v[200:203], v64 offset:2048\n" \
"v_cvt_pk_bf16_f32 v33, v34, v35\n" \
"v_mfma_f32_16x16x32_bf16 v[88:91], a[196:199], a[180:183], v[88:91]\n" \
"ds_read_b128 v[204:207], v64 offset:3072\n" \
"v_exp_f32 v37, v37\n" \
"v_exp_f32 v38, v38\n" \
"v_mfma_f32_16x16x32_bf16 v[88:91], a[200:203], a[184:187], v[88:91]\n" \
"ds_read_b128 v[208:211], v64 offset:4096\n" \
"v_cvt_pk_bf16_f32 v34, v36, v37\n" \
"v_mfma_f32_16x16x32_bf16 v[88:91], a[204:207], a[188:191], v[88:91]\n" \
"ds_read_b128 a[192:195], v247 offset:0\n" \
"v_exp_f32 v39, v39\n" \
"v_mfma_f32_16x16x32_bf16 v[92:95], a[216:219], a[184:187], v[236:239]\n" \
"ds_read_b128 v[212:215], v64 offset:5120\n" \
"global_load_lds_dwordx4 v250, s[38:39] offset:1024 sc1\n" \
"v_mfma_f32_16x16x32_bf16 v[92:95], a[220:223], a[188:191], v[92:95]\n" \
"ds_read_b128 v[216:219], v64 offset:6144\n" \
"v_exp_f32 v40, v40\n" \
"v_mfma_f32_16x16x32_bf16 v[92:95], a[208:211], a[176:179], v[92:95]\n" \
"ds_read_b128 a[196:199], v247 offset:8192\n" \
"v_cvt_pk_bf16_f32 v35, v38, v39\n" \
"v_mfma_f32_16x16x32_bf16 v[92:95], a[212:215], a[180:183], v[92:95]\n" \
"ds_read_b128 v[220:223], v64 offset:7168\n" \
"v_exp_f32 v41, v41\n" \
"v_mfma_f32_16x16x32_bf16 v[84:87], a[208:211], a[160:163], v[232:235]\n" \
"ds_write_b128 v64, v[232:235]\n" \
"v_exp_f32 v42, v42\n" \
"v_mfma_f32_16x16x32_bf16 v[84:87], a[212:215], a[164:167], v[84:87]\n" \
"ds_read_b128 a[200:203], v247 offset:1024\n" \
"v_cvt_pk_bf16_f32 v40, v40, v41\n" \
"v_mfma_f32_16x16x32_bf16 v[84:87], a[216:219], a[168:171], v[84:87]\n" \
"v_exp_f32 v43, v43\n" \
"v_mfma_f32_16x16x32_bf16 v[84:87], a[220:223], a[172:175], v[84:87]\n" \
"global_load_lds_dwordx4 v250, s[38:39] offset:2048 sc1\n" \
"ds_write_b128 v64, v[236:239] offset:1024\n" \
"v_exp_f32 v44, v44\n" \
"v_mfma_f32_16x16x32_bf16 v[76:79], a[216:219], a[152:155], v[228:231]\n" \
"ds_read_b128 a[204:207], v247 offset:9216\n" \
"v_xor_b32 v65, 0x2000, v64\n" \
"v_mfma_f32_16x16x32_bf16 v[76:79], a[220:223], a[156:159], v[76:79]\n" \
"v_xor_b32 v246, 0x8000, v246\n" \
"v_add_u32 v246, s82, v246\n" \
"v_mfma_f32_16x16x32_bf16 v[76:79], a[208:211], a[144:147], v[76:79]\n" \
"s_waitcnt lgkmcnt(4)\n" \
"v_mov_b32 v66, s60\n" \
"v_lshlrev_b32 v67, 28, s61\n" \
"v_mfma_f32_16x16x32_bf16 v[76:79], a[212:215], a[148:151], v[76:79]\n" \
"ds_read_b128 a[208:211], v247 offset:2048\n" \
"v_cvt_pk_bf16_f32 v41, v42, v43\n" \
"v_mfma_f32_16x16x32_bf16 v[104:107], a[224:227], a[144:147], v[228:231]\n" \
"v_exp_f32 v45, v45\n" \
"v_mfma_f32_16x16x32_bf16 v[104:107], a[228:231], a[148:151], v[104:107]\n" \
"v_exp_f32 v46, v46\n" \
"v_mfma_f32_16x16x32_bf16 v[104:107], a[232:235], a[152:155], v[104:107]\n" \
"ds_read_b128 a[212:215], v247 offset:10240\n" \
"v_cvt_pk_bf16_f32 v42, v44, v45\n" \
"v_mfma_f32_16x16x32_bf16 v[104:107], a[236:239], a[156:159], v[104:107]\n" \
"v_exp_f32 v47, v47\n" \
"v_mfma_f32_16x16x32_bf16 v[112:115], a[232:235], a[168:171], v[232:235]\n" \
"v_exp_f32 v48, v48\n" \
"v_cvt_pk_bf16_f32 v43, v46, v47\n" \
"v_mfma_f32_16x16x32_bf16 v[112:115], a[236:239], a[172:175], v[112:115]\n" \
"ds_read_b128 a[216:219], v247 offset:3072\n" \
"global_load_lds_dwordx4 v250, s[38:39] offset:3072 sc1\n" \
"v_mfma_f32_16x16x32_bf16 v[112:115], a[224:227], a[160:163], v[112:115]\n" \
"v_exp_f32 v49, v49\n" \
"v_mfma_f32_16x16x32_bf16 v[112:115], a[228:231], a[164:167], v[112:115]\n" \
"v_exp_f32 v50, v50\n" \
"v_cvt_pk_bf16_f32 v48, v48, v49\n" \
"v_mfma_f32_16x16x32_bf16 v[240:243], a[120:123], v[32:35], v[240:243]\n" \
"v_exp_f32 v51, v51\n" \
"v_mfma_f32_16x16x32_bf16 v[240:243], a[124:127], v[40:43], v[240:243]\n" \
"s_add_u32 s38, s38, 17408\n" \
"v_exp_f32 v52, v52\n" \
"v_mfma_f32_16x16x32_bf16 v[120:123], a[224:227], a[176:179], v[236:239]\n" \
"ds_read_b128 a[220:223], v247 offset:11264\n" \
"v_cvt_pk_bf16_f32 v49, v50, v51\n" \
"v_mfma_f32_16x16x32_bf16 v[120:123], a[228:231], a[180:183], v[120:123]\n" \
"v_exp_f32 v53, v53\n" \
"v_mfma_f32_16x16x32_bf16 v[120:123], a[232:235], a[184:187], v[120:123]\n" \
"v_exp_f32 v54, v54\n" \
"v_mfma_f32_16x16x32_bf16 v[120:123], a[236:239], a[188:191], v[120:123]\n" \
"ds_read_b128 a[224:227], v247 offset:4096\n" \
"v_cvt_pk_bf16_f32 v50, v52, v53\n" \
"v_exp_f32 v55, v55\n" \
"v_mfma_f32_16x16x32_bf16 v[124:127], a[248:251], a[184:187], v[236:239]\n" \
"global_load_dword v253, v252, s[40:41]\n" \
"v_mfma_f32_16x16x32_bf16 v[124:127], a[252:255], a[188:191], v[124:127]\n" \
"v_exp_f32 v56, v56\n" \
"v_mfma_f32_16x16x32_bf16 v[124:127], a[240:243], a[176:179], v[124:127]\n" \
"ds_read_b128 a[228:231], v247 offset:12288\n" \
"v_cvt_pk_bf16_f32 v51, v54, v55\n" \
"v_mfma_f32_16x16x32_bf16 v[124:127], a[244:247], a[180:183], v[124:127]\n" \
"v_exp_f32 v57, v57\n" \
"v_exp_f32 v58, v58\n" \
"v_cvt_pk_bf16_f32 v56, v56, v57\n" \
"v_mfma_f32_16x16x32_bf16 v[242:245], a[120:123], v[48:51], v[242:245]\n" \
"v_exp_f32 v59, v59\n" \
"v_mfma_f32_16x16x32_bf16 v[116:119], a[240:243], a[160:163], v[232:235]\n" \
"v_exp_f32 v60, v60\n" \
"v_mfma_f32_16x16x32_bf16 v[116:119], a[244:247], a[164:167], v[116:119]\n" \
"ds_read_b128 a[232:235], v247 offset:5120\n" \
"v_cvt_pk_bf16_f32 v57, v58, v59\n" \
"v_mfma_f32_16x16x32_bf16 v[116:119], a[248:251], a[168:171], v[116:119]\n" \
"v_exp_f32 v61, v61\n" \
"v_mfma_f32_16x16x32_bf16 v[116:119], a[252:255], a[172:175], v[116:119]\n" \
"v_exp_f32 v62, v62\n" \
"v_cvt_pk_bf16_f32 v58, v60, v61\n" \
"v_mfma_f32_16x16x32_bf16 v[108:111], a[248:251], a[152:155], v[228:231]\n" \
"ds_read_b128 a[236:239], v247 offset:13312\n" \
"v_exp_f32 v63, v63\n" \
"v_mfma_f32_16x16x32_bf16 v[108:111], a[252:255], a[156:159], v[108:111]\n" \
"v_cvt_pk_bf16_f32 v59, v62, v63\n" \
"v_mfma_f32_16x16x32_bf16 v[108:111], a[240:243], a[144:147], v[108:111]\n" \
"s_waitcnt vmcnt(5) lgkmcnt(4)\n" \
"v_mfma_f32_16x16x32_bf16 v[242:245], a[124:127], v[56:59], v[242:245]\n" \
"v_mfma_f32_16x16x32_bf16 v[108:111], a[244:247], a[148:151], v[108:111]\n" \
"s_barrier\n" \
"ds_read_b128 a[240:243], v247 offset:6144\n" \
"v_mfma_f32_16x16x32_bf16 a[0:3], a[192:195], v[0:3], a[0:3]\n" \
"buffer_store_dword v66, v67, s[56:59], s84 offen sc1\n" \
"v_mfma_f32_16x16x32_bf16 a[0:3], a[196:199], v[32:35], a[0:3]\n" \
"s_add_u32 s73, s73, s95\n" \
"v_cvt_pk_bf16_f32 v59, v62, v63\n" \
"v_exp_f32 v80, v80\n" \
"v_mfma_f32_16x16x32_bf16 a[32:35], a[192:195], v[8:11], a[32:35]\n" \
"s_add_u32 s36, s36, s83\n" \
"ds_read_b128 a[244:247], v247 offset:14336\n" \
"v_exp_f32 v81, v81\n" \
"v_mfma_f32_16x16x32_bf16 a[32:35], a[196:199], v[40:43], a[32:35]\n" \
"s_add_u32 s73, s73, 4096\n" \
"ds_read_b128 a[248:251], v247 offset:7168\n" \
"v_exp_f32 v82, v82\n" \
"v_mfma_f32_16x16x32_bf16 v[128:131], a[192:195], v[16:19], v[128:131]\n" \
"ds_read_b128 a[252:255], v247 offset:15360\n" \
"v_cvt_pk_bf16_f32 v80, v80, v81\n" \
"v_mfma_f32_16x16x32_bf16 v[128:131], a[196:199], v[48:51], v[128:131]\n" \
"ds_read_b128 v[224:227], v65\n" \
"v_exp_f32 v83, v83\n" \
"v_mfma_f32_16x16x32_bf16 v[160:163], a[192:195], v[24:27], v[160:163]\n" \
"s_mov_b32 m0, s73\n" \
"ds_read_b128 v[228:231], v65 offset:1024\n" \
"v_xor_b32 v252, 0x8000, v246\n" \
"v_mfma_f32_16x16x32_bf16 v[160:163], a[196:199], v[56:59], v[160:163]\n" \
"ds_read_b128 a[192:195], v246 offset:0\n" \
"global_load_lds_dwordx4 v250, s[36:37] offset:-4096 sc1\n" \
"v_mfma_f32_16x16x32_bf16 v[164:167], a[200:203], v[24:27], v[164:167]\n" \
"v_exp_f32 v84, v84\n" \
"v_mfma_f32_16x16x32_bf16 v[164:167], a[204:207], v[56:59], v[164:167]\n" \
"v_cvt_pk_bf16_f32 v81, v82, v83\n" \
"v_exp_f32 v85, v85\n" \
"v_mfma_f32_16x16x32_bf16 v[132:135], a[200:203], v[16:19], v[132:135]\n" \
"v_exp_f32 v86, v86\n" \
"v_mfma_f32_16x16x32_bf16 v[132:135], a[204:207], v[48:51], v[132:135]\n" \
"ds_read_b128 a[196:199], v246 offset:2048\n" \
"global_load_lds_dwordx4 v250, s[36:37] offset:-3072 sc1\n" \
"v_mfma_f32_16x16x32_bf16 a[36:39], a[200:203], v[8:11], a[36:39]\n" \
"v_cvt_pk_bf16_f32 v82, v84, v85\n" \
"v_mfma_f32_16x16x32_bf16 a[36:39], a[204:207], v[40:43], a[36:39]\n" \
"v_exp_f32 v87, v87\n" \
"v_mfma_f32_16x16x32_bf16 a[4:7], a[200:203], v[0:3], a[4:7]\n" \
"v_cmp_gt_i32 vcc, 0, v251\n" \
"v_cmp_gt_i32 s[64:65], 1, v251\n" \
"v_mfma_f32_16x16x32_bf16 a[4:7], a[204:207], v[32:35], a[4:7]\n" \
"ds_read_b128 a[200:203], v246 offset:4096\n" \
"global_load_lds_dwordx4 v250, s[36:37] offset:-2048 sc1\n" \
"v_mfma_f32_16x16x32_bf16 a[8:11], a[208:211], v[0:3], a[8:11]\n" \
"v_cmp_gt_i32 s[66:67], 2, v251\n" \
"v_cndmask_b32 v72, v72, v254, vcc\n" \
"v_mfma_f32_16x16x32_bf16 a[8:11], a[212:215], v[32:35], a[8:11]\n" \
"v_cmp_gt_i32 vcc, 3, v251\n" \
"v_cndmask_b32 v73, v73, v254, s[64:65]\n" \
"v_mfma_f32_16x16x32_bf16 a[40:43], a[208:211], v[8:11], a[40:43]\n" \
"v_cmp_gt_i32 s[64:65], 16, v251\n" \
"v_cndmask_b32 v74, v74, v254, s[66:67]\n" \
"v_mfma_f32_16x16x32_bf16 a[40:43], a[212:215], v[40:43], a[40:43]\n" \
"ds_read_b128 a[204:207], v246 offset:6144\n" \
"global_load_lds_dwordx4 v250, s[36:37] offset:-1024 sc1\n" \
"v_mfma_f32_16x16x32_bf16 v[136:139], a[208:211], v[16:19], v[136:139]\n" \
"v_cmp_gt_i32 s[66:67], 17, v251\n" \
"v_cndmask_b32 v75, v75, v254, vcc\n" \
"v_mfma_f32_16x16x32_bf16 v[136:139], a[212:215], v[48:51], v[136:139]\n" \
"v_cmp_gt_i32 vcc, 18, v251\n" \
"v_cndmask_b32 v76, v76, v254, s[64:65]\n" \
"v_mfma_f32_16x16x32_bf16 v[168:171], a[208:211], v[24:27], v[168:171]\n" \
"v_cmp_gt_i32 s[64:65], 19, v251\n" \
"v_cndmask_b32 v77, v77, v254, s[66:67]\n" \
"v_mfma_f32_16x16x32_bf16 v[168:171], a[212:215], v[56:59], v[168:171]\n" \
"ds_read_b128 a[208:211], v246 offset:256\n" \
"global_load_lds_dwordx4 v250, s[36:37] offset:0 sc1\n" \
"v_mfma_f32_16x16x32_bf16 v[172:175], a[216:219], v[24:27], v[172:175]\n" \
"v_cmp_gt_i32 s[66:67], 32, v251\n" \
"v_cndmask_b32 v78, v78, v254, vcc\n" \
"v_mfma_f32_16x16x32_bf16 v[172:175], a[220:223], v[56:59], v[172:175]\n" \
"v_cmp_gt_i32 vcc, 33, v251\n" \
"v_cndmask_b32 v79, v79, v254, s[64:65]\n" \
"v_mfma_f32_16x16x32_bf16 v[140:143], a[216:219], v[16:19], v[140:143]\n" \
"v_cmp_gt_i32 s[64:65], 34, v251\n" \
"v_cndmask_b32 v104, v104, v254, s[66:67]\n" \
"v_mfma_f32_16x16x32_bf16 v[140:143], a[220:223], v[48:51], v[140:143]\n" \
"ds_read_b128 a[212:215], v246 offset:2304\n" \
"global_load_lds_dwordx4 v250, s[36:37] offset:1024 sc1\n" \
"v_mfma_f32_16x16x32_bf16 a[44:47], a[216:219], v[8:11], a[44:47]\n" \
"v_cmp_gt_i32 s[66:67], 35, v251\n" \
"v_cndmask_b32 v105, v105, v254, vcc\n" \
"v_mfma_f32_16x16x32_bf16 a[44:47], a[220:223], v[40:43], a[44:47]\n" \
"v_cmp_gt_i32 vcc, 48, v251\n" \
"v_cndmask_b32 v106, v106, v254, s[64:65]\n" \
"v_mfma_f32_16x16x32_bf16 a[12:15], a[216:219], v[0:3], a[12:15]\n" \
"s_waitcnt lgkmcnt(6)\n" \
"v_cmp_gt_i32 s[64:65], 49, v251\n" \
"v_cndmask_b32 v107, v107, v254, s[66:67]\n" \
"v_mfma_f32_16x16x32_bf16 a[12:15], a[220:223], v[32:35], a[12:15]\n" \
"ds_read_b128 a[216:219], v246 offset:4352\n" \
"global_load_lds_dwordx4 v250, s[36:37] offset:2048 sc1\n" \
"v_mfma_f32_16x16x32_bf16 a[16:19], a[224:227], v[0:3], a[16:19]\n" \
"v_cmp_gt_i32 s[66:67], 50, v251\n" \
"v_cndmask_b32 v108, v108, v254, vcc\n" \
"v_mfma_f32_16x16x32_bf16 a[16:19], a[228:231], v[32:35], a[16:19]\n" \
"v_cmp_gt_i32 vcc, 51, v251\n" \
"v_cndmask_b32 v109, v109, v254, s[64:65]\n" \
"v_mfma_f32_16x16x32_bf16 a[48:51], a[224:227], v[8:11], a[48:51]\n" \
"v_cndmask_b32 v110, v110, v254, s[66:67]\n" \
"v_cndmask_b32 v111, v111, v254, vcc\n" \
"v_mfma_f32_16x16x32_bf16 a[48:51], a[228:231], v[40:43], a[48:51]\n" \
"ds_read_b128 a[220:223], v246 offset:6400\n" \
"global_load_lds_dwordx4 v250, s[36:37] offset:3072 sc1\n" \
"v_mfma_f32_16x16x32_bf16 v[144:147], a[224:227], v[16:19], v[144:147]\n" \
"s_add_u32 s36, s36, 34816\n" \
"v_exp_f32 v72, v72\n" \
"v_mfma_f32_16x16x32_bf16 v[144:147], a[228:231], v[48:51], v[144:147]\n" \
"v_cvt_pk_bf16_f32 v83, v86, v87\n" \
"v_mfma_f32_16x16x32_bf16 v[176:179], a[224:227], v[24:27], v[176:179]\n" \
"s_add_u32 m0, s74, 0x4000\n" \
"v_exp_f32 v73, v73\n" \
"v_mfma_f32_16x16x32_bf16 v[176:179], a[228:231], v[56:59], v[176:179]\n" \
"ds_read_b128 a[224:227], v246 offset:8192\n" \
"global_load_lds_dwordx4 v250, s[38:39] offset:0 sc1\n" \
"v_mfma_f32_16x16x32_bf16 v[180:183], a[232:235], v[24:27], v[180:183]\n" \
"v_exp_f32 v74, v74\n" \
"v_mfma_f32_16x16x32_bf16 v[180:183], a[236:239], v[56:59], v[180:183]\n" \
"v_cvt_pk_bf16_f32 v72, v72, v73\n" \
"v_mfma_f32_16x16x32_bf16 v[242:245], a[120:123], v[80:83], v[242:245]\n" \
"v_exp_f32 v75, v75\n" \
"v_mfma_f32_16x16x32_bf16 v[148:151], a[232:235], v[16:19], v[148:151]\n" \
"v_exp_f32 v76, v76\n" \
"v_mfma_f32_16x16x32_bf16 v[148:151], a[236:239], v[48:51], v[148:151]\n" \
"ds_read_b128 a[228:231], v246 offset:10240\n" \
"global_load_lds_dwordx4 v250, s[38:39] offset:1024 sc1\n" \
"v_mfma_f32_16x16x32_bf16 a[52:55], a[232:235], v[8:11], a[52:55]\n" \
"v_cvt_pk_bf16_f32 v73, v74, v75\n" \
"v_mfma_f32_16x16x32_bf16 a[52:55], a[236:239], v[40:43], a[52:55]\n" \
"v_exp_f32 v77, v77\n" \
"v_mfma_f32_16x16x32_bf16 a[20:23], a[232:235], v[0:3], a[20:23]\n" \
"v_exp_f32 v78, v78\n" \
"v_mfma_f32_16x16x32_bf16 a[20:23], a[236:239], v[32:35], a[20:23]\n" \
"ds_read_b128 a[232:235], v246 offset:12288\n" \
"v_cvt_pk_bf16_f32 v74, v76, v77\n" \
"v_mfma_f32_16x16x32_bf16 a[24:27], a[240:243], v[0:3], a[24:27]\n" \
"global_load_lds_dwordx4 v250, s[38:39] offset:2048 sc1\n" \
"global_load_lds_dwordx4 v250, s[38:39] offset:3072 sc1\n" \
"v_mfma_f32_16x16x32_bf16 a[24:27], a[244:247], v[32:35], a[24:27]\n" \
"v_exp_f32 v79, v79\n" \
"v_mfma_f32_16x16x32_bf16 a[56:59], a[240:243], v[8:11], a[56:59]\n" \
"s_add_u32 s38, s38, 17408\n" \
"v_exp_f32 v88, v88\n" \
"v_mfma_f32_16x16x32_bf16 a[56:59], a[244:247], v[40:43], a[56:59]\n" \
"s_add_u32 s38, s38, s83\n" \
"ds_read_b128 a[236:239], v246 offset:14336\n" \
"v_cvt_pk_bf16_f32 v75, v78, v79\n" \
"v_mfma_f32_16x16x32_bf16 v[152:155], a[240:243], v[16:19], v[152:155]\n" \
"s_add_u32 s74, s74, s95\n" \
"v_exp_f32 v89, v89\n" \
"v_mfma_f32_16x16x32_bf16 v[152:155], a[244:247], v[48:51], v[152:155]\n" \
"v_exp_f32 v90, v90\n" \
"v_cvt_pk_bf16_f32 v88, v88, v89\n" \
"v_mfma_f32_16x16x32_bf16 v[240:243], a[124:127], v[72:75], v[240:243]\n" \
"s_add_u32 s74, s74, 4096\n" \
"v_exp_f32 v91, v91\n" \
"v_mfma_f32_16x16x32_bf16 v[184:187], a[240:243], v[24:27], v[184:187]\n" \
"s_cmp_eq_u32 s63, 7\n" \
"v_exp_f32 v92, v92\n" \
"v_mfma_f32_16x16x32_bf16 v[184:187], a[244:247], v[56:59], v[184:187]\n" \
"ds_read_b128 a[240:243], v246 offset:8448\n" \
"v_cvt_pk_bf16_f32 v89, v90, v91\n" \
"v_mfma_f32_16x16x32_bf16 v[188:191], a[248:251], v[24:27], v[188:191]\n" \
"v_exp_f32 v93, v93\n" \
"v_mfma_f32_16x16x32_bf16 v[188:191], a[252:255], v[56:59], v[188:191]\n" \
"v_exp_f32 v94, v94\n" \
"v_mfma_f32_16x16x32_bf16 v[156:159], a[248:251], v[16:19], v[156:159]\n" \
"v_cvt_pk_bf16_f32 v90, v92, v93\n" \
"v_mfma_f32_16x16x32_bf16 v[156:159], a[252:255], v[48:51], v[156:159]\n" \
"ds_read_b128 a[244:247], v246 offset:10496\n" \
"v_mfma_f32_16x16x32_bf16 a[60:63], a[248:251], v[8:11], a[60:63]\n" \
"v_exp_f32 v95, v95\n" \
"v_mfma_f32_16x16x32_bf16 a[60:63], a[252:255], v[40:43], a[60:63]\n" \
"v_cvt_pk_bf16_f32 v91, v94, v95\n" \
"v_mfma_f32_16x16x32_bf16 a[28:31], a[248:251], v[0:3], a[28:31]\n" \
"s_waitcnt lgkmcnt(6)\n" \
"v_mfma_f32_16x16x32_bf16 v[242:245], a[124:127], v[88:91], v[242:245]\n" \
"v_mfma_f32_16x16x32_bf16 a[28:31], a[252:255], v[32:35], a[28:31]\n" \
"ds_read_b128 a[248:251], v246 offset:12544\n" \
"s_cbranch_scc1 LF7_%=\n" \
"v_mfma_f32_16x16x32_bf16 v[0:3], a[192:195], v[192:195], v[224:227]\n" \
"s_load_dwordx8 s[44:51], s[56:57], s79 glc\n" \
"v_exp_f32 v104, v104\n" \
"v_mfma_f32_16x16x32_bf16 v[0:3], a[196:199], v[196:199], v[0:3]\n" \
"v_cvt_pk_bf16_f32 v91, v94, v95\n" \
"v_mfma_f32_16x16x32_bf16 v[0:3], a[200:203], v[200:203], v[0:3]\n" \
"v_exp_f32 v105, v105\n" \
"v_mfma_f32_16x16x32_bf16 v[0:3], a[204:207], v[204:207], v[0:3]\n" \
"ds_read_b128 a[252:255], v246 offset:14592\n" \
"v_mov_b32 v78, v242\n" \
"v_mfma_f32_16x16x32_bf16 v[8:11], a[200:203], v[216:219], v[228:231]\n" \
"v_exp_f32 v106, v106\n" \
"v_mfma_f32_16x16x32_bf16 v[8:11], a[204:207], v[220:223], v[8:11]\n" \
"v_cvt_pk_bf16_f32 v104, v104, v105\n" \
"v_mov_b32 v76, v78\n" \
"v_mfma_f32_16x16x32_bf16 v[8:11], a[192:195], v[208:211], v[8:11]\n" \
"v_exp_f32 v107, v107\n" \
"v_mfma_f32_16x16x32_bf16 v[8:11], a[196:199], v[212:215], v[8:11]\n" \
"v_exp_f32 v108, v108\n" \
"v_mfma_f32_16x16x32_bf16 v[16:19], a[192:195], a[160:163], v[232:235]\n" \
"v_cvt_pk_bf16_f32 v105, v106, v107\n" \
"v_mfma_f32_16x16x32_bf16 v[16:19], a[196:199], a[164:167], v[16:19]\n" \
"v_exp_f32 v109, v109\n" \
"v_mfma_f32_16x16x32_bf16 v[16:19], a[200:203], a[168:171], v[16:19]\n" \
"v_exp_f32 v110, v110\n" \
"v_mfma_f32_16x16x32_bf16 v[16:19], a[204:207], a[172:175], v[16:19]\n" \
"v_cvt_pk_bf16_f32 v106, v108, v109\n" \
"v_permlane16_swap_b32 v76, v78\n" \
"v_mfma_f32_16x16x32_bf16 v[24:27], a[200:203], a[184:187], v[236:239]\n" \
"v_exp_f32 v111, v111\n" \
"v_mfma_f32_16x16x32_bf16 v[24:27], a[204:207], a[188:191], v[24:27]\n" \
"v_exp_f32 v112, v112\n" \
"v_mfma_f32_16x16x32_bf16 v[24:27], a[192:195], a[176:179], v[24:27]\n" \
"v_cvt_pk_bf16_f32 v107, v110, v111\n" \
"v_add_f32 v78, v78, v76\n" \
"v_mfma_f32_16x16x32_bf16 v[24:27], a[196:199], a[180:183], v[24:27]\n" \
"ds_read_b128 a[192:195], v247 offset:16384\n" \
"v_mov_b32 v76, v78\n" \
"v_mfma_f32_16x16x32_bf16 v[28:31], a[208:211], a[176:179], v[236:239]\n" \
"v_exp_f32 v113, v113\n" \
"v_mfma_f32_16x16x32_bf16 v[28:31], a[212:215], a[180:183], v[28:31]\n" \
"v_exp_f32 v114, v114\n" \
"v_mfma_f32_16x16x32_bf16 v[28:31], a[216:219], a[184:187], v[28:31]\n" \
"v_cvt_pk_bf16_f32 v112, v112, v113\n" \
"v_mfma_f32_16x16x32_bf16 v[28:31], a[220:223], a[188:191], v[28:31]\n" \
"v_permlane32_swap_b32 v76, v78\n" \
"v_mfma_f32_16x16x32_bf16 v[240:243], a[124:127], v[104:107], v[240:243]\n" \
"ds_read_b128 a[196:199], v247 offset:24576\n" \
"v_exp_f32 v115, v115\n" \
"v_mfma_f32_16x16x32_bf16 v[20:23], a[216:219], a[168:171], v[232:235]\n" \
"v_exp_f32 v116, v116\n" \
"v_mfma_f32_16x16x32_bf16 v[20:23], a[220:223], a[172:175], v[20:23]\n" \
"v_cvt_pk_bf16_f32 v113, v114, v115\n" \
"v_add_f32 v78, v78, v76\n" \
"v_mfma_f32_16x16x32_bf16 v[20:23], a[208:211], a[160:163], v[20:23]\n" \
"v_exp_f32 v117, v117\n" \
"v_mfma_f32_16x16x32_bf16 v[20:23], a[212:215], a[164:167], v[20:23]\n" \
"ds_read_b128 a[200:203], v247 offset:17408\n" \
"v_rcp_f32 v76, v78\n" \
"v_mfma_f32_16x16x32_bf16 v[12:15], a[208:211], v[208:211], v[228:231]\n" \
"v_exp_f32 v118, v118\n" \
"v_mfma_f32_16x16x32_bf16 v[12:15], a[212:215], v[212:215], v[12:15]\n" \
"v_cvt_pk_bf16_f32 v114, v116, v117\n" \
"v_mul_f32 v78, 0x4d800000, v78\n" \
"v_mfma_f32_16x16x32_bf16 v[12:15], a[216:219], v[216:219], v[12:15]\n" \
"v_exp_f32 v119, v119\n" \
"v_mfma_f32_16x16x32_bf16 v[12:15], a[220:223], v[220:223], v[12:15]\n" \
"ds_read_b128 a[204:207], v247 offset:25600\n" \
"v_add_u32 v77, 0x1c90080, v249\n" \
"v_mfma_f32_16x16x32_bf16 v[4:7], a[216:219], v[200:203], v[224:227]\n" \
"v_exp_f32 v120, v120\n" \
"v_mfma_f32_16x16x32_bf16 v[4:7], a[220:223], v[204:207], v[4:7]\n" \
"v_cvt_pk_bf16_f32 v115, v118, v119\n" \
"v_cndmask_b32 v77, v249, v77, s[16:17]\n" \
"v_mfma_f32_16x16x32_bf16 v[4:7], a[208:211], v[192:195], v[4:7]\n" \
"s_waitcnt lgkmcnt(4)\n" \
"v_exp_f32 v121, v121\n" \
"v_mfma_f32_16x16x32_bf16 v[4:7], a[212:215], v[196:199], v[4:7]\n" \
"ds_read_b128 a[208:211], v247 offset:18432\n" \
"v_accvgpr_read_b32 v64, a0\n" \
"v_mfma_f32_16x16x32_bf16 v[32:35], a[224:227], v[192:195], v[224:227]\n" \
"v_exp_f32 v122, v122\n" \
"v_mfma_f32_16x16x32_bf16 v[32:35], a[228:231], v[196:199], v[32:35]\n" \
"v_cvt_pk_bf16_f32 v120, v120, v121\n" \
"v_accvgpr_read_b32 v65, a1\n" \
"v_mfma_f32_16x16x32_bf16 v[32:35], a[232:235], v[200:203], v[32:35]\n" \
"v_accvgpr_read_b32 v66, a2\n" \
"v_accvgpr_read_b32 v67, a3\n" \
"v_mfma_f32_16x16x32_bf16 v[32:35], a[236:239], v[204:207], v[32:35]\n" \
"ds_read_b128 a[212:215], v247 offset:26624\n" \
"v_accvgpr_read_b32 v68, a4\n" \
"v_mfma_f32_16x16x32_bf16 v[242:245], a[120:123], v[112:115], v[242:245]\n" \
"v_exp_f32 v123, v123\n" \
"v_mfma_f32_16x16x32_bf16 v[40:43], a[232:235], v[216:219], v[228:231]\n" \
"v_accvgpr_read_b32 v69, a5\n" \
"v_accvgpr_read_b32 v70, a6\n" \
"v_accvgpr_read_b32 v71, a7\n" \
"v_exp_f32 v124, v124\n" \
"v_pk_mul_f32 v[64:65], v[64:65], v[76:77] op_sel_hi:[1,0]\n" \
"v_pk_mul_f32 v[66:67], v[66:67], v[76:77] op_sel_hi:[1,0]\n" \
"v_pk_mul_f32 v[68:69], v[68:69], v[76:77] op_sel_hi:[1,0]\n" \
"v_pk_mul_f32 v[70:71], v[70:71], v[76:77] op_sel_hi:[1,0]\n" \
"v_mfma_f32_16x16x32_bf16 v[40:43], a[236:239], v[220:223], v[40:43]\n" \
"v_cvt_pk_bf16_f32 v121, v122, v123\n" \
"v_cvt_pk_bf16_f32 v96, v64, v65\n" \
"v_mfma_f32_16x16x32_bf16 v[40:43], a[224:227], v[208:211], v[40:43]\n" \
"v_exp_f32 v125, v125\n" \
"v_mfma_f32_16x16x32_bf16 v[40:43], a[228:231], v[212:215], v[40:43]\n" \
"ds_read_b128 a[216:219], v247 offset:19456\n" \
"v_cvt_pk_bf16_f32 v97, v66, v67\n" \
"v_cvt_pk_bf16_f32 v98, v68, v69\n" \
"v_cvt_pk_bf16_f32 v99, v70, v71\n" \
"v_pk_mul_f32 v[64:65], v[64:65], v[66:67]\n" \
"v_pk_fma_f32 v[64:65], v[68:69], v[70:71], v[64:65]\n" \
"v_mfma_f32_16x16x32_bf16 v[48:51], a[224:227], a[160:163], v[232:235]\n" \
"v_exp_f32 v126, v126\n" \
"v_mfma_f32_16x16x32_bf16 v[48:51], a[228:231], a[164:167], v[48:51]\n" \
"v_cvt_pk_bf16_f32 v122, v124, v125\n" \
"v_fma_f32 v78, v64, v65, v78\n" \
"v_mfma_f32_16x16x32_bf16 v[48:51], a[232:235], a[168:171], v[48:51]\n" \
"v_exp_f32 v127, v127\n" \
"v_mfma_f32_16x16x32_bf16 v[48:51], a[236:239], a[172:175], v[48:51]\n" \
"buffer_store_dwordx4 v[96:99], v77, s[56:59], s77 offen offset:0 sc1\n" \
"ds_read_b128 a[220:223], v247 offset:27648\n" \
"v_cvt_pk_bf16_f32 v123, v126, v127\n" \
"v_accvgpr_read_b32 v64, a8\n" \
"v_accvgpr_read_b32 v65, a9\n" \
"v_mfma_f32_16x16x32_bf16 v[56:59], a[232:235], a[184:187], v[236:239]\n" \
"s_xor_b64 s[52:53], s[44:45], s[42:43]\n" \
"v_accvgpr_read_b32 v66, a10\n" \
"v_accvgpr_read_b32 v67, a11\n" \
"v_mfma_f32_16x16x32_bf16 v[56:59], a[236:239], a[188:191], v[56:59]\n" \
"s_xor_b64 s[54:55], s[46:47], s[42:43]\n" \
"v_accvgpr_read_b32 v68, a12\n" \
"v_accvgpr_read_b32 v69, a13\n" \
"v_accvgpr_read_b32 v70, a14\n" \
"v_accvgpr_read_b32 v71, a15\n" \
"v_pk_mul_f32 v[64:65], v[64:65], v[76:77] op_sel_hi:[1,0]\n" \
"v_pk_mul_f32 v[66:67], v[66:67], v[76:77] op_sel_hi:[1,0]\n" \
"v_pk_mul_f32 v[68:69], v[68:69], v[76:77] op_sel_hi:[1,0]\n" \
"v_pk_mul_f32 v[70:71], v[70:71], v[76:77] op_sel_hi:[1,0]\n" \
"v_cvt_pk_bf16_f32 v100, v64, v65\n" \
"v_cvt_pk_bf16_f32 v101, v66, v67\n" \
"v_cvt_pk_bf16_f32 v102, v68, v69\n" \
"Lpg6770_%=:\n" \
"s_cbranch_execz Lcb0_%=\n" \
"v_cvt_pk_bf16_f32 v103, v70, v71\n" \
"v_pk_mul_f32 v[64:65], v[64:65], v[66:67]\n" \
"v_pk_fma_f32 v[64:65], v[68:69], v[70:71], v[64:65]\n" \
"v_mfma_f32_16x16x32_bf16 v[56:59], a[224:227], a[176:179], v[56:59]\n" \
"v_fma_f32 v78, v64, v65, v78\n" \
"s_or_b64 s[52:53], s[52:53], s[54:55]\n" \
"v_mfma_f32_16x16x32_bf16 v[56:59], a[228:231], a[180:183], v[56:59]\n" \
"ds_read_b128 a[224:227], v247 offset:20480\n" \
"v_mfma_f32_16x16x32_bf16 v[242:245], a[124:127], v[120:123], v[242:245]\n" \
"buffer_store_dwordx4 v[100:103], v77, s[56:59], s77 offen offset:2048 sc1\n" \
"s_xor_b64 s[54:55], s[48:49], s[42:43]\n" \
"s_add_u32 s77, s77, 0x1000\n" \
"v_accvgpr_read_b32 v64, a16\n" \
"v_accvgpr_read_b32 v65, a17\n" \
"v_mfma_f32_16x16x32_bf16 v[60:63], a[240:243], a[176:179], v[236:239]\n" \
"s_or_b64 s[52:53], s[52:53], s[54:55]\n" \
"v_accvgpr_read_b32 v66, a18\n" \
"v_accvgpr_read_b32 v67, a19\n" \
"v_mfma_f32_16x16x32_bf16 v[60:63], a[244:247], a[180:183], v[60:63]\n" \
"s_xor_b64 s[54:55], s[50:51], s[42:43]\n" \
"v_accvgpr_read_b32 v68, a20\n" \
"v_accvgpr_read_b32 v69, a21\n" \
"v_accvgpr_read_b32 v70, a22\n" \
"v_accvgpr_read_b32 v71, a23\n" \
"v_pk_mul_f32 v[64:65], v[64:65], v[76:77] op_sel_hi:[1,0]\n" \
"v_pk_mul_f32 v[66:67], v[66:67], v[76:77] op_sel_hi:[1,0]\n" \
"v_pk_mul_f32 v[68:69], v[68:69], v[76:77] op_sel_hi:[1,0]\n" \
"v_pk_mul_f32 v[70:71], v[70:71], v[76:77] op_sel_hi:[1,0]\n" \
"v_mfma_f32_16x16x32_bf16 v[60:63], a[248:251], a[184:187], v[60:63]\n" \
"v_cvt_pk_bf16_f32 v96, v64, v65\n" \
"v_cvt_pk_bf16_f32 v97, v66, v67\n" \
"v_cvt_pk_bf16_f32 v98, v68, v69\n" \
"v_cvt_pk_bf16_f32 v99, v70, v71\n" \
"s_or_b64 s[52:53], s[52:53], s[54:55]\n" \
"v_pk_mul_f32 v[64:65], v[64:65], v[66:67]\n" \
"v_pk_fma_f32 v[64:65], v[68:69], v[70:71], v[64:65]\n" \
"v_mfma_f32_16x16x32_bf16 v[60:63], a[252:255], a[188:191], v[60:63]\n" \
"ds_read_b128 a[228:231], v247 offset:28672\n" \
"v_fma_f32 v78, v64, v65, v78\n" \
"v_mfma_f32_16x16x32_bf16 v[52:55], a[248:251], a[168:171], v[232:235]\n" \
"buffer_store_dwordx4 v[96:99], v77, s[56:59], s77 offen offset:0 sc1\n" \
"s_and_b64 s[52:53], s[52:53], s[8:9]\n" \
"s_cbranch_scc1 Lrs2_%=\n" \
"Lrr2_%=:\n" \
"v_mfma_f32_16x16x32_bf16 v[52:55], a[252:255], a[172:175], v[52:55]\n" \
"s_mov_b64 s[8:9], s[10:11]\n" \
"v_accvgpr_read_b32 v64, a24\n" \
"v_accvgpr_read_b32 v65, a25\n" \
"v_mfma_f32_16x16x32_bf16 v[52:55], a[240:243], a[160:163], v[52:55]\n" \
"v_accvgpr_read_b32 v66, a26\n" \
"v_accvgpr_read_b32 v67, a27\n" \
"v_mfma_f32_16x16x32_bf16 v[52:55], a[244:247], a[164:167], v[52:55]\n" \
"ds_read_b128 a[232:235], v247 offset:21504\n" \
"v_accvgpr_read_b32 v68, a28\n" \
"v_accvgpr_read_b32 v69, a29\n" \
"v_accvgpr_read_b32 v70, a30\n" \
"v_accvgpr_read_b32 v71, a31\n" \
"v_pk_mul_f32 v[64:65], v[64:65], v[76:77] op_sel_hi:[1,0]\n" \
"v_pk_mul_f32 v[66:67], v[66:67], v[76:77] op_sel_hi:[1,0]\n" \
"v_pk_mul_f32 v[68:69], v[68:69], v[76:77] op_sel_hi:[1,0]\n" \
"v_pk_mul_f32 v[70:71], v[70:71], v[76:77] op_sel_hi:[1,0]\n" \
"v_mfma_f32_16x16x32_bf16 v[44:47], a[240:243], v[208:211], v[228:231]\n" \
"v_cvt_pk_bf16_f32 v100, v64, v65\n" \
"v_cvt_pk_bf16_f32 v101, v66, v67\n" \
"v_cvt_pk_bf16_f32 v102, v68, v69\n" \
"v_cvt_pk_bf16_f32 v103, v70, v71\n" \
"v_pk_mul_f32 v[64:65], v[64:65], v[66:67]\n" \
"v_pk_fma_f32 v[64:65], v[68:69], v[70:71], v[64:65]\n" \
"v_mfma_f32_16x16x32_bf16 v[44:47], a[244:247], v[212:215], v[44:47]\n" \
"v_fma_f32 v78, v64, v65, v78\n" \
"v_mfma_f32_16x16x32_bf16 v[44:47], a[248:251], v[216:219], v[44:47]\n" \
"v_mfma_f32_16x16x32_bf16 v[44:47], a[252:255], v[220:223], v[44:47]\n" \
"buffer_store_dwordx4 v[100:103], v77, s[56:59], s77 offen offset:2048 sc1\n" \
"ds_read_b128 a[236:239], v247 offset:29696\n" \
"v_cmp_class_f32 vcc, v78, s94\n" \
"s_sub_u32 s77, s77, 4096\n" \
"v_mfma_f32_16x16x32_bf16 v[36:39], a[248:251], v[200:203], v[224:227]\n" \
"s_or_b64 s[92:93], s[92:93], vcc\n" \
"v_mfma_f32_16x16x32_bf16 v[36:39], a[252:255], v[204:207], v[36:39]\n" \
"v_mfma_f32_16x16x32_bf16 v[36:39], a[240:243], v[192:195], v[36:39]\n" \
"s_waitcnt vmcnt(8) lgkmcnt(4)\n" \
"v_mfma_f32_16x16x32_bf16 v[36:39], a[244:247], v[196:199], v[36:39]\n" \
"s_barrier\n" \
"ds_read_b128 a[240:243], v247 offset:22528\n" \
"v_mfma_f32_16x16x32_bf16 a[32:35], a[192:195], v[72:75], a[32:35]\n" \
"v_exp_f32 v0, v0\n" \
"v_mfma_f32_16x16x32_bf16 a[32:35], a[196:199], v[104:107], a[32:35]\n" \
"v_cvt_pk_bf16_f32 v123, v126, v127\n" \
"v_exp_f32 v1, v1\n" \
"v_exp_f32 v2, v2\n" \
"v_cvt_pk_bf16_f32 v0, v0, v1\n" \
"v_exp_f32 v3, v3\n" \
"v_exp_f32 v4, v4\n" \
"v_mfma_f32_16x16x32_bf16 v[128:131], a[192:195], v[80:83], v[128:131]\n" \
"s_xor_b32 m0, s73, 0x8000\n" \
"ds_read_b128 a[244:247], v247 offset:30720\n" \
"v_cvt_pk_bf16_f32 v1, v2, v3\n" \
"v_mfma_f32_16x16x32_bf16 v[128:131], a[196:199], v[112:115], v[128:131]\n" \
"ds_read_b128 a[248:251], v247 offset:23552\n" \
"global_load_lds_dwordx4 v250, s[36:37] offset:-4096 sc1\n" \
"v_mfma_f32_16x16x32_bf16 v[160:163], a[192:195], v[88:91], v[160:163]\n" \
"ds_read_b128 a[252:255], v247 offset:31744\n" \
"global_load_lds_dwordx4 v250, s[36:37] offset:-3072 sc1\n" \
"v_mfma_f32_16x16x32_bf16 v[160:163], a[196:199], v[120:123], v[160:163]\n" \
"s_add_u32 s70, s82, 0x10000\n" \
"ds_read_b128 a[192:195], v252 offset:0\n" \
"v_add_u32 v247, s70, v248\n" \
"v_mfma_f32_16x16x32_bf16 v[164:167], a[200:203], v[88:91], v[164:167]\n" \
"v_accvgpr_mov_b32 a0, a120\n" \
"v_accvgpr_mov_b32 a1, a121\n" \
"v_mfma_f32_16x16x32_bf16 v[164:167], a[204:207], v[120:123], v[164:167]\n" \
"v_accvgpr_mov_b32 a2, a122\n" \
"v_accvgpr_mov_b32 a3, a123\n" \
"v_mfma_f32_16x16x32_bf16 v[132:135], a[200:203], v[80:83], v[132:135]\n" \
"ds_read_b128 a[196:199], v252 offset:2048\n" \
"v_accvgpr_mov_b32 a4, a124\n" \
"v_mfma_f32_16x16x32_bf16 v[132:135], a[204:207], v[112:115], v[132:135]\n" \
"v_accvgpr_mov_b32 a5, a125\n" \
"v_accvgpr_mov_b32 a6, a126\n" \
"v_mfma_f32_16x16x32_bf16 a[36:39], a[200:203], v[72:75], a[36:39]\n" \
"v_accvgpr_mov_b32 a7, a127\n" \
"v_mov_b32 v64, 0x30000\n" \
"v_mfma_f32_16x16x32_bf16 a[36:39], a[204:207], v[104:107], a[36:39]\n" \
"ds_read_b128 a[200:203], v252 offset:4096\n" \
"global_load_lds_dwordx4 v250, s[36:37] offset:-2048 sc1\n" \
"v_mfma_f32_16x16x32_bf16 a[40:43], a[208:211], v[72:75], a[40:43]\n" \
"v_exp_f32 v5, v5\n" \
"v_mfma_f32_16x16x32_bf16 a[40:43], a[212:215], v[104:107], a[40:43]\n" \
"v_exp_f32 v6, v6\n" \
"v_mfma_f32_16x16x32_bf16 v[136:139], a[208:211], v[80:83], v[136:139]\n" \
"ds_read_b128 a[204:207], v252 offset:6144\n" \
"v_cvt_pk_bf16_f32 v2, v4, v5\n" \
"v_mfma_f32_16x16x32_bf16 v[136:139], a[212:215], v[112:115], v[136:139]\n" \
"v_exp_f32 v7, v7\n" \
"v_mfma_f32_16x16x32_bf16 v[168:171], a[208:211], v[88:91], v[168:171]\n" \
"v_exp_f32 v8, v8\n" \
"v_mfma_f32_16x16x32_bf16 v[168:171], a[212:215], v[120:123], v[168:171]\n" \
"ds_read_b128 a[208:211], v252 offset:256\n" \
"global_load_lds_dwordx4 v250, s[36:37] offset:-1024 sc1\n" \
"v_cvt_pk_bf16_f32 v3, v6, v7\n" \
"v_mfma_f32_16x16x32_bf16 v[172:175], a[216:219], v[88:91], v[172:175]\n" \
"v_exp_f32 v9, v9\n" \
"v_mfma_f32_16x16x32_bf16 v[172:175], a[220:223], v[120:123], v[172:175]\n" \
"v_exp_f32 v10, v10\n" \
"v_cvt_pk_bf16_f32 v8, v8, v9\n" \
"v_mfma_f32_16x16x32_bf16 v[140:143], a[216:219], v[80:83], v[140:143]\n" \
"ds_read_b128 a[212:215], v252 offset:2304\n" \
"global_load_lds_dwordx4 v250, s[36:37] offset:0 sc1\n" \
"v_mfma_f32_16x16x32_bf16 v[140:143], a[220:223], v[112:115], v[140:143]\n" \
"v_exp_f32 v11, v11\n" \
"v_mfma_f32_16x16x32_bf16 a[44:47], a[216:219], v[72:75], a[44:47]\n" \
"s_waitcnt lgkmcnt(6)\n" \
"v_exp_f32 v12, v12\n" \
"v_mfma_f32_16x16x32_bf16 a[44:47], a[220:223], v[104:107], a[44:47]\n" \
"ds_read_b128 a[216:219], v252 offset:4352\n" \
"v_cvt_pk_bf16_f32 v9, v10, v11\n" \
"v_mfma_f32_16x16x32_bf16 a[48:51], a[224:227], v[72:75], a[48:51]\n" \
"v_exp_f32 v13, v13\n" \
"v_mfma_f32_16x16x32_bf16 a[48:51], a[228:231], v[104:107], a[48:51]\n" \
"v_exp_f32 v14, v14\n" \
"v_cvt_pk_bf16_f32 v10, v12, v13\n" \
"v_mfma_f32_16x16x32_bf16 v[144:147], a[224:227], v[80:83], v[144:147]\n" \
"ds_read_b128 a[220:223], v252 offset:6400\n" \
"global_load_lds_dwordx4 v250, s[36:37] offset:1024 sc1\n" \
"v_mfma_f32_16x16x32_bf16 v[144:147], a[228:231], v[112:115], v[144:147]\n" \
"v_exp_f32 v15, v15\n" \
"v_mfma_f32_16x16x32_bf16 v[176:179], a[224:227], v[88:91], v[176:179]\n" \
"v_exp_f32 v16, v16\n" \
"v_mfma_f32_16x16x32_bf16 v[176:179], a[228:231], v[120:123], v[176:179]\n" \
"ds_read_b128 a[224:227], v252 offset:8192\n" \
"global_load_lds_dwordx4 v250, s[36:37] offset:2048 sc1\n" \
"v_cvt_pk_bf16_f32 v11, v14, v15\n" \
"v_mfma_f32_16x16x32_bf16 v[180:183], a[232:235], v[88:91], v[180:183]\n" \
"v_exp_f32 v17, v17\n" \
"v_mfma_f32_16x16x32_bf16 v[180:183], a[236:239], v[120:123], v[180:183]\n" \
"v_exp_f32 v18, v18\n" \
"v_cvt_pk_bf16_f32 v16, v16, v17\n" \
"v_mfma_f32_16x16x32_bf16 v[238:241], a[120:123], v[0:3], v[238:241]\n" \
"v_exp_f32 v19, v19\n" \
"v_mfma_f32_16x16x32_bf16 v[238:241], a[124:127], v[8:11], v[238:241]\n" \
"v_exp_f32 v20, v20\n" \
"v_cvt_pk_bf16_f32 v17, v18, v19\n" \
"v_mfma_f32_16x16x32_bf16 v[148:151], a[232:235], v[80:83], v[148:151]\n" \
"ds_read_b128 a[228:231], v252 offset:10240\n" \
"global_load_lds_dwordx4 v250, s[36:37] offset:3072 sc1\n" \
"v_mfma_f32_16x16x32_bf16 v[148:151], a[236:239], v[112:115], v[148:151]\n" \
"v_exp_f32 v21, v21\n" \
"v_mfma_f32_16x16x32_bf16 a[52:55], a[232:235], v[72:75], a[52:55]\n" \
"v_exp_f32 v22, v22\n" \
"v_mfma_f32_16x16x32_bf16 a[52:55], a[236:239], v[104:107], a[52:55]\n" \
"s_add_u32 s36, s36, 34816\n" \
"ds_read_b128 a[232:235], v252 offset:12288\n" \
"v_cvt_pk_bf16_f32 v18, v20, v21\n" \
"v_mfma_f32_16x16x32_bf16 a[56:59], a[240:243], v[72:75], a[56:59]\n" \
"v_exp_f32 v23, v23\n" \
"v_mfma_f32_16x16x32_bf16 a[56:59], a[244:247], v[104:107], a[56:59]\n" \
"v_exp_f32 v24, v24\n" \
"v_mfma_f32_16x16x32_bf16 v[152:155], a[240:243], v[80:83], v[152:155]\n" \
"ds_read_b128 a[236:239], v252 offset:14336\n" \
"v_cvt_pk_bf16_f32 v19, v22, v23\n" \
"v_mfma_f32_16x16x32_bf16 v[152:155], a[244:247], v[112:115], v[152:155]\n" \
"v_exp_f32 v25, v25\n" \
"v_exp_f32 v26, v26\n" \
"v_cvt_pk_bf16_f32 v24, v24, v25\n" \
"v_mfma_f32_16x16x32_bf16 v[242:245], a[120:123], v[16:19], v[242:245]\n" \
"v_exp_f32 v27, v27\n" \
"v_mfma_f32_16x16x32_bf16 v[184:187], a[240:243], v[88:91], v[184:187]\n" \
"s_load_dwordx8 s[44:51], s[56:57], s79 glc\n" \
"v_exp_f32 v28, v28\n" \
"v_mfma_f32_16x16x32_bf16 v[184:187], a[244:247], v[120:123], v[184:187]\n" \
"ds_read_b128 a[240:243], v252 offset:8448\n" \
"v_cvt_pk_bf16_f32 v25, v26, v27\n" \
"v_exp_f32 v29, v29\n" \
"v_mfma_f32_16x16x32_bf16 v[188:191], a[248:251], v[88:91], v[188:191]\n" \
"v_exp_f32 v30, v30\n" \
"v_mfma_f32_16x16x32_bf16 v[188:191], a[252:255], v[120:123], v[188:191]\n" \
"v_cvt_pk_bf16_f32 v26, v28, v29\n" \
"v_exp_f32 v31, v31\n" \
"v_mfma_f32_16x16x32_bf16 v[156:159], a[248:251], v[80:83], v[156:159]\n" \
"ds_read_b128 a[244:247], v252 offset:10496\n" \
"v_cvt_pk_bf16_f32 v27, v30, v31\n" \
"v_mfma_f32_16x16x32_bf16 v[156:159], a[252:255], v[112:115], v[156:159]\n" \
"v_mfma_f32_16x16x32_bf16 v[242:245], a[124:127], v[24:27], v[242:245]\n" \
"v_mfma_f32_16x16x32_bf16 a[60:63], a[248:251], v[72:75], a[60:63]\n" \
"s_waitcnt vmcnt(12) lgkmcnt(6)\n" \
"v_mfma_f32_16x16x32_bf16 a[60:63], a[252:255], v[104:107], a[60:63]\n" \
"s_barrier\n" \
"ds_read_b128 a[248:251], v252 offset:12544\n" \
"ds_read_b128 a[120:123], v64\n" \
"ds_read_b128 a[124:127], v64\n" \
"s_mov_b64 exec, 0\n" \
"s_cmp_eq_u32 s61, 0\n" \
"s_cbranch_scc1 Lst1x0_%=\n" \
"s_cmp_eq_u32 s61, 1\n" \
"s_cbranch_scc1 Lst1x1_%=\n" \
"s_cmp_eq_u32 s61, 2\n" \
"s_cbranch_scc1 Lst1x2_%=\n" \
"s_branch Lst1x3_%=\n" \
"Lcb1_%=:\n" \
"s_mov_b64 exec, -1\n" \
"s_waitcnt lgkmcnt(0)\n" \
"s_cmp_lg_u32 s63, 0\n" \
"s_cbranch_scc1 LA1g_%=\n" \
"s_xor_b64 s[52:53], s[44:45], s[42:43]\n" \
"s_xor_b64 s[54:55], s[46:47], s[42:43]\n" \
"s_or_b64 s[52:53], s[52:53], s[54:55]\n" \
"s_xor_b64 s[54:55], s[48:49], s[42:43]\n" \
"s_or_b64 s[52:53], s[52:53], s[54:55]\n" \
"s_xor_b64 s[54:55], s[50:51], s[42:43]\n" \
"s_or_b64 s[52:53], s[52:53], s[54:55]\n" \
"s_and_b64 s[52:53], s[52:53], s[8:9]\n" \
"s_cbranch_scc1 Lrs3_%=\n" \
"Lrr3_%=:\n" \
"s_branch LA1g_%=\n" \
"Lpg7025_%=:\n" \
"s_cbranch_execz Lcb0_%=\n" \
"Lloop2_%=:\n" \
"v_mfma_f32_16x16x32_bf16 a[64:67], a[192:195], v[0:3], a[64:67]\n" \
"v_exp_f32 v64, v64\n" \
"v_mfma_f32_16x16x32_bf16 a[64:67], a[196:199], v[32:35], a[64:67]\n" \
"v_cvt_pk_bf16_f32 v59, v62, v63\n" \
"v_add_f32 v240, v240, v64\n" \
"v_exp_f32 v65, v65\n" \
"v_mfma_f32_16x16x32_bf16 a[96:99], a[192:195], v[8:11], a[96:99]\n" \
"s_mov_b32 m0, s73\n" \
"ds_read_b128 a[244:247], v247 offset:14336\n" \
"v_add_f32 v240, v240, v65\n" \
"v_mfma_f32_16x16x32_bf16 a[96:99], a[196:199], v[40:43], a[96:99]\n" \
"ds_read_b128 a[248:251], v247 offset:7168\n" \
"global_load_lds_dwordx4 v250, s[36:37] offset:-4096 sc1\n" \
"v_mfma_f32_16x16x32_bf16 v[128:131], a[192:195], v[16:19], v[128:131]\n" \
"ds_read_b128 a[252:255], v247 offset:15360\n" \
"v_exp_f32 v66, v66\n" \
"v_mfma_f32_16x16x32_bf16 v[128:131], a[196:199], v[48:51], v[128:131]\n" \
"v_add_f32 v240, v240, v66\n" \
"v_cvt_pk_bf16_f32 v64, v64, v65\n" \
"v_mfma_f32_16x16x32_bf16 v[160:163], a[192:195], v[24:27], v[160:163]\n" \
"v_exp_f32 v67, v67\n" \
"v_mfma_f32_16x16x32_bf16 v[160:163], a[196:199], v[56:59], v[160:163]\n" \
"ds_read_b128 a[192:195], v246 offset:0\n" \
"global_load_lds_dwordx4 v250, s[36:37] offset:-3072 sc1\n" \
"v_add_f32 v240, v240, v67\n" \
"v_mfma_f32_16x16x32_bf16 v[164:167], a[200:203], v[24:27], v[164:167]\n" \
"v_exp_f32 v68, v68\n" \
"v_mfma_f32_16x16x32_bf16 v[164:167], a[204:207], v[56:59], v[164:167]\n" \
"v_add_f32 v240, v240, v68\n" \
"v_cvt_pk_bf16_f32 v65, v66, v67\n" \
"v_mfma_f32_16x16x32_bf16 v[132:135], a[200:203], v[16:19], v[132:135]\n" \
"v_exp_f32 v69, v69\n" \
"v_mfma_f32_16x16x32_bf16 v[132:135], a[204:207], v[48:51], v[132:135]\n" \
"ds_read_b128 a[196:199], v246 offset:2048\n" \
"v_add_f32 v240, v240, v69\n" \
"v_mfma_f32_16x16x32_bf16 a[100:103], a[200:203], v[8:11], a[100:103]\n" \
"v_exp_f32 v70, v70\n" \
"v_mfma_f32_16x16x32_bf16 a[100:103], a[204:207], v[40:43], a[100:103]\n" \
"v_add_f32 v240, v240, v70\n" \
"v_cvt_pk_bf16_f32 v66, v68, v69\n" \
"v_mfma_f32_16x16x32_bf16 a[68:71], a[200:203], v[0:3], a[68:71]\n" \
"v_exp_f32 v71, v71\n" \
"v_mfma_f32_16x16x32_bf16 a[68:71], a[204:207], v[32:35], a[68:71]\n" \
"ds_read_b128 a[200:203], v246 offset:4096\n" \
"global_load_lds_dwordx4 v250, s[36:37] offset:-2048 sc1\n" \
"v_add_f32 v240, v240, v71\n" \
"v_mfma_f32_16x16x32_bf16 a[72:75], a[208:211], v[0:3], a[72:75]\n" \
"v_exp_f32 v72, v72\n" \
"v_mfma_f32_16x16x32_bf16 a[72:75], a[212:215], v[32:35], a[72:75]\n" \
"v_add_f32 v241, v241, v72\n" \
"v_cvt_pk_bf16_f32 v67, v70, v71\n" \
"v_mfma_f32_16x16x32_bf16 a[104:107], a[208:211], v[8:11], a[104:107]\n" \
"v_exp_f32 v73, v73\n" \
"v_mfma_f32_16x16x32_bf16 a[104:107], a[212:215], v[40:43], a[104:107]\n" \
"ds_read_b128 a[204:207], v246 offset:6144\n" \
"global_load_lds_dwordx4 v250, s[36:37] offset:-1024 sc1\n" \
"v_add_f32 v241, v241, v73\n" \
"v_mfma_f32_16x16x32_bf16 v[136:139], a[208:211], v[16:19], v[136:139]\n" \
"v_exp_f32 v74, v74\n" \
"v_mfma_f32_16x16x32_bf16 v[136:139], a[212:215], v[48:51], v[136:139]\n" \
"v_add_f32 v241, v241, v74\n" \
"v_cvt_pk_bf16_f32 v72, v72, v73\n" \
"v_mfma_f32_16x16x32_bf16 v[168:171], a[208:211], v[24:27], v[168:171]\n" \
"v_exp_f32 v75, v75\n" \
"v_mfma_f32_16x16x32_bf16 v[168:171], a[212:215], v[56:59], v[168:171]\n" \
"ds_read_b128 a[208:211], v246 offset:256\n" \
"v_add_f32 v241, v241, v75\n" \
"v_mfma_f32_16x16x32_bf16 v[172:175], a[216:219], v[24:27], v[172:175]\n" \
"v_exp_f32 v76, v76\n" \
"v_mfma_f32_16x16x32_bf16 v[172:175], a[220:223], v[56:59], v[172:175]\n" \
"v_add_f32 v241, v241, v76\n" \
"v_cvt_pk_bf16_f32 v73, v74, v75\n" \
"v_mfma_f32_16x16x32_bf16 v[140:143], a[216:219], v[16:19], v[140:143]\n" \
"v_exp_f32 v77, v77\n" \
"v_mfma_f32_16x16x32_bf16 v[140:143], a[220:223], v[48:51], v[140:143]\n" \
"ds_read_b128 a[212:215], v246 offset:2304\n" \
"global_load_lds_dwordx4 v250, s[36:37] offset:0 sc1\n" \
"v_add_f32 v241, v241, v77\n" \
"v_mfma_f32_16x16x32_bf16 a[108:111], a[216:219], v[8:11], a[108:111]\n" \
"v_exp_f32 v78, v78\n" \
"v_mfma_f32_16x16x32_bf16 a[108:111], a[220:223], v[40:43], a[108:111]\n" \
"v_add_f32 v241, v241, v78\n" \
"v_cvt_pk_bf16_f32 v74, v76, v77\n" \
"v_mfma_f32_16x16x32_bf16 a[76:79], a[216:219], v[0:3], a[76:79]\n" \
"s_waitcnt lgkmcnt(6)\n" \
"v_exp_f32 v79, v79\n" \
"v_mfma_f32_16x16x32_bf16 a[76:79], a[220:223], v[32:35], a[76:79]\n" \
"ds_read_b128 a[216:219], v246 offset:4352\n" \
"global_load_lds_dwordx4 v250, s[36:37] offset:1024 sc1\n" \
"v_add_f32 v241, v241, v79\n" \
"v_mfma_f32_16x16x32_bf16 a[80:83], a[224:227], v[0:3], a[80:83]\n" \
"v_exp_f32 v80, v80\n" \
"v_mfma_f32_16x16x32_bf16 a[80:83], a[228:231], v[32:35], a[80:83]\n" \
"v_add_f32 v244, v244, v80\n" \
"v_cvt_pk_bf16_f32 v75, v78, v79\n" \
"v_mfma_f32_16x16x32_bf16 a[112:115], a[224:227], v[8:11], a[112:115]\n" \
"v_exp_f32 v81, v81\n" \
"v_mfma_f32_16x16x32_bf16 a[112:115], a[228:231], v[40:43], a[112:115]\n" \
"ds_read_b128 a[220:223], v246 offset:6400\n" \
"v_add_f32 v244, v244, v81\n" \
"v_mfma_f32_16x16x32_bf16 v[144:147], a[224:227], v[16:19], v[144:147]\n" \
"v_exp_f32 v82, v82\n" \
"v_mfma_f32_16x16x32_bf16 v[144:147], a[228:231], v[48:51], v[144:147]\n" \
"v_add_f32 v244, v244, v82\n" \
"v_cvt_pk_bf16_f32 v80, v80, v81\n" \
"v_mfma_f32_16x16x32_bf16 v[176:179], a[224:227], v[24:27], v[176:179]\n" \
"v_exp_f32 v83, v83\n" \
"v_mfma_f32_16x16x32_bf16 v[176:179], a[228:231], v[56:59], v[176:179]\n" \
"ds_read_b128 a[224:227], v246 offset:8192\n" \
"global_load_lds_dwordx4 v250, s[36:37] offset:2048 sc1\n" \
"v_add_f32 v244, v244, v83\n" \
"v_mfma_f32_16x16x32_bf16 v[180:183], a[232:235], v[24:27], v[180:183]\n" \
"v_exp_f32 v84, v84\n" \
"v_mfma_f32_16x16x32_bf16 v[180:183], a[236:239], v[56:59], v[180:183]\n" \
"v_add_f32 v244, v244, v84\n" \
"v_cvt_pk_bf16_f32 v81, v82, v83\n" \
"v_mfma_f32_16x16x32_bf16 v[148:151], a[232:235], v[16:19], v[148:151]\n" \
"v_exp_f32 v85, v85\n" \
"v_mfma_f32_16x16x32_bf16 v[148:151], a[236:239], v[48:51], v[148:151]\n" \
"ds_read_b128 a[228:231], v246 offset:10240\n" \
"global_load_lds_dwordx4 v250, s[36:37] offset:3072 sc1\n" \
"v_add_f32 v244, v244, v85\n" \
"v_mfma_f32_16x16x32_bf16 a[116:119], a[232:235], v[8:11], a[116:119]\n" \
"s_add_u32 s36, s36, 34816\n" \
"v_exp_f32 v86, v86\n" \
"v_mfma_f32_16x16x32_bf16 a[116:119], a[236:239], v[40:43], a[116:119]\n" \
"v_add_f32 v244, v244, v86\n" \
"v_cvt_pk_bf16_f32 v82, v84, v85\n" \
"v_mfma_f32_16x16x32_bf16 a[84:87], a[232:235], v[0:3], a[84:87]\n" \
"v_exp_f32 v87, v87\n" \
"v_mfma_f32_16x16x32_bf16 a[84:87], a[236:239], v[32:35], a[84:87]\n" \
"ds_read_b128 a[232:235], v246 offset:12288\n" \
"v_add_f32 v244, v244, v87\n" \
"v_mfma_f32_16x16x32_bf16 a[88:91], a[240:243], v[0:3], a[88:91]\n" \
"v_exp_f32 v88, v88\n" \
"v_mfma_f32_16x16x32_bf16 a[88:91], a[244:247], v[32:35], a[88:91]\n" \
"v_add_f32 v245, v245, v88\n" \
"v_cvt_pk_bf16_f32 v83, v86, v87\n" \
"v_mfma_f32_16x16x32_bf16 a[120:123], a[240:243], v[8:11], a[120:123]\n" \
"v_exp_f32 v89, v89\n" \
"v_mfma_f32_16x16x32_bf16 a[120:123], a[244:247], v[40:43], a[120:123]\n" \
"ds_read_b128 a[236:239], v246 offset:14336\n" \
"v_add_f32 v245, v245, v89\n" \
"v_mfma_f32_16x16x32_bf16 v[152:155], a[240:243], v[16:19], v[152:155]\n" \
"v_exp_f32 v90, v90\n" \
"v_mfma_f32_16x16x32_bf16 v[152:155], a[244:247], v[48:51], v[152:155]\n" \
"v_add_f32 v245, v245, v90\n" \
"v_cvt_pk_bf16_f32 v88, v88, v89\n" \
"v_mfma_f32_16x16x32_bf16 v[184:187], a[240:243], v[24:27], v[184:187]\n" \
"v_exp_f32 v91, v91\n" \
"v_mfma_f32_16x16x32_bf16 v[184:187], a[244:247], v[56:59], v[184:187]\n" \
"ds_read_b128 a[240:243], v246 offset:8448\n" \
"v_add_f32 v245, v245, v91\n" \
"v_mfma_f32_16x16x32_bf16 v[188:191], a[248:251], v[24:27], v[188:191]\n" \
"v_exp_f32 v92, v92\n" \
"v_mfma_f32_16x16x32_bf16 v[188:191], a[252:255], v[56:59], v[188:191]\n" \
"v_add_f32 v245, v245, v92\n" \
"v_cvt_pk_bf16_f32 v89, v90, v91\n" \
"v_mfma_f32_16x16x32_bf16 v[156:159], a[248:251], v[16:19], v[156:159]\n" \
"v_exp_f32 v93, v93\n" \
"v_mfma_f32_16x16x32_bf16 v[156:159], a[252:255], v[48:51], v[156:159]\n" \
"ds_read_b128 a[244:247], v246 offset:10496\n" \
"v_add_f32 v245, v245, v93\n" \
"v_mfma_f32_16x16x32_bf16 a[124:127], a[248:251], v[8:11], a[124:127]\n" \
"v_exp_f32 v94, v94\n" \
"v_mfma_f32_16x16x32_bf16 a[124:127], a[252:255], v[40:43], a[124:127]\n" \
"v_add_f32 v245, v245, v94\n" \
"v_cvt_pk_bf16_f32 v90, v92, v93\n" \
"v_mfma_f32_16x16x32_bf16 a[92:95], a[248:251], v[0:3], a[92:95]\n" \
"s_waitcnt vmcnt(8) lgkmcnt(6)\n" \
"v_exp_f32 v95, v95\n" \
"v_mfma_f32_16x16x32_bf16 a[92:95], a[252:255], v[32:35], a[92:95]\n" \
"s_barrier\n" \
"ds_read_b128 a[248:251], v246 offset:12544\n" \
"v_add_f32 v245, v245, v95\n" \
"v_mfma_f32_16x16x32_bf16 v[0:3], a[192:195], v[192:195], v[224:227]\n" \
"v_exp_f32 v96, v96\n" \
"v_mfma_f32_16x16x32_bf16 v[0:3], a[196:199], v[196:199], v[0:3]\n" \
"v_cvt_pk_bf16_f32 v91, v94, v95\n" \
"v_add_f32 v240, v240, v96\n" \
"v_mfma_f32_16x16x32_bf16 v[0:3], a[200:203], v[200:203], v[0:3]\n" \
"s_mov_b32 m0, s74\n" \
"v_exp_f32 v97, v97\n" \
"v_mfma_f32_16x16x32_bf16 v[0:3], a[204:207], v[204:207], v[0:3]\n" \
"ds_read_b128 a[252:255], v246 offset:14592\n" \
"v_add_f32 v240, v240, v97\n" \
"v_mfma_f32_16x16x32_bf16 v[8:11], a[200:203], v[216:219], v[228:231]\n" \
"global_load_lds_dwordx4 v250, s[38:39] offset:-4096 sc1\n" \
"global_load_lds_dwordx4 v250, s[38:39] offset:-3072 sc1\n" \
"v_mfma_f32_16x16x32_bf16 v[8:11], a[204:207], v[220:223], v[8:11]\n" \
"v_exp_f32 v98, v98\n" \
"v_mfma_f32_16x16x32_bf16 v[8:11], a[192:195], v[208:211], v[8:11]\n" \
"v_add_f32 v240, v240, v98\n" \
"v_cvt_pk_bf16_f32 v96, v96, v97\n" \
"v_mfma_f32_16x16x32_bf16 v[8:11], a[196:199], v[212:215], v[8:11]\n" \
"v_exp_f32 v99, v99\n" \
"v_mfma_f32_16x16x32_bf16 v[16:19], a[192:195], a[160:163], v[232:235]\n" \
"v_add_f32 v240, v240, v99\n" \
"v_exp_f32 v100, v100\n" \
"v_mfma_f32_16x16x32_bf16 v[16:19], a[196:199], a[164:167], v[16:19]\n" \
"v_add_f32 v240, v240, v100\n" \
"v_cvt_pk_bf16_f32 v97, v98, v99\n" \
"v_mfma_f32_16x16x32_bf16 v[16:19], a[200:203], a[168:171], v[16:19]\n" \
"v_exp_f32 v101, v101\n" \
"v_mfma_f32_16x16x32_bf16 v[16:19], a[204:207], a[172:175], v[16:19]\n" \
"v_add_f32 v240, v240, v101\n" \
"v_exp_f32 v102, v102\n" \
"v_mfma_f32_16x16x32_bf16 v[24:27], a[200:203], a[184:187], v[236:239]\n" \
"v_add_f32 v240, v240, v102\n" \
"v_cvt_pk_bf16_f32 v98, v100, v101\n" \
"v_mfma_f32_16x16x32_bf16 v[24:27], a[204:207], a[188:191], v[24:27]\n" \
"v_exp_f32 v103, v103\n" \
"v_mfma_f32_16x16x32_bf16 v[24:27], a[192:195], a[176:179], v[24:27]\n" \
"v_add_f32 v240, v240, v103\n" \
"v_mfma_f32_16x16x32_bf16 v[24:27], a[196:199], a[180:183], v[24:27]\n" \
"ds_read_b128 a[192:195], v247 offset:32768\n" \
"global_load_lds_dwordx4 v250, s[38:39] offset:-2048 sc1\n" \
"v_mfma_f32_16x16x32_bf16 v[28:31], a[208:211], a[176:179], v[236:239]\n" \
"v_exp_f32 v104, v104\n" \
"v_mfma_f32_16x16x32_bf16 v[28:31], a[212:215], a[180:183], v[28:31]\n" \
"v_add_f32 v241, v241, v104\n" \
"v_cvt_pk_bf16_f32 v99, v102, v103\n" \
"v_mfma_f32_16x16x32_bf16 v[28:31], a[216:219], a[184:187], v[28:31]\n" \
"v_exp_f32 v105, v105\n" \
"v_mfma_f32_16x16x32_bf16 v[28:31], a[220:223], a[188:191], v[28:31]\n" \
"ds_read_b128 a[196:199], v247 offset:40960\n" \
"global_load_lds_dwordx4 v250, s[38:39] offset:-1024 sc1\n" \
"v_add_f32 v241, v241, v105\n" \
"v_mfma_f32_16x16x32_bf16 v[20:23], a[216:219], a[168:171], v[232:235]\n" \
"v_exp_f32 v106, v106\n" \
"v_mfma_f32_16x16x32_bf16 v[20:23], a[220:223], a[172:175], v[20:23]\n" \
"v_add_f32 v241, v241, v106\n" \
"v_cvt_pk_bf16_f32 v104, v104, v105\n" \
"v_mfma_f32_16x16x32_bf16 v[20:23], a[208:211], a[160:163], v[20:23]\n" \
"v_exp_f32 v107, v107\n" \
"v_mfma_f32_16x16x32_bf16 v[20:23], a[212:215], a[164:167], v[20:23]\n" \
"ds_read_b128 a[200:203], v247 offset:33792\n" \
"v_add_f32 v241, v241, v107\n" \
"v_mfma_f32_16x16x32_bf16 v[12:15], a[208:211], v[208:211], v[228:231]\n" \
"v_exp_f32 v108, v108\n" \
"v_mfma_f32_16x16x32_bf16 v[12:15], a[212:215], v[212:215], v[12:15]\n" \
"v_add_f32 v241, v241, v108\n" \
"v_cvt_pk_bf16_f32 v105, v106, v107\n" \
"v_mfma_f32_16x16x32_bf16 v[12:15], a[216:219], v[216:219], v[12:15]\n" \
"v_exp_f32 v109, v109\n" \
"v_mfma_f32_16x16x32_bf16 v[12:15], a[220:223], v[220:223], v[12:15]\n" \
"ds_read_b128 a[204:207], v247 offset:41984\n" \
"global_load_lds_dwordx4 v250, s[38:39] offset:0 sc1\n" \
"v_add_f32 v241, v241, v109\n" \
"v_mfma_f32_16x16x32_bf16 v[4:7], a[216:219], v[200:203], v[224:227]\n" \
"v_exp_f32 v110, v110\n" \
"v_mfma_f32_16x16x32_bf16 v[4:7], a[220:223], v[204:207], v[4:7]\n" \
"v_add_f32 v241, v241, v110\n" \
"v_cvt_pk_bf16_f32 v106, v108, v109\n" \
"v_mfma_f32_16x16x32_bf16 v[4:7], a[208:211], v[192:195], v[4:7]\n" \
"s_waitcnt lgkmcnt(4)\n" \
"v_exp_f32 v111, v111\n" \
"v_mfma_f32_16x16x32_bf16 v[4:7], a[212:215], v[196:199], v[4:7]\n" \
"ds_read_b128 a[208:211], v247 offset:34816\n" \
"global_load_lds_dwordx4 v250, s[38:39] offset:1024 sc1\n" \
"v_add_f32 v241, v241, v111\n" \
"v_mfma_f32_16x16x32_bf16 v[32:35], a[224:227], v[192:195], v[224:227]\n" \
"v_exp_f32 v112, v112\n" \
"v_mfma_f32_16x16x32_bf16 v[32:35], a[228:231], v[196:199], v[32:35]\n" \
"v_add_f32 v244, v244, v112\n" \
"v_cvt_pk_bf16_f32 v107, v110, v111\n" \
"v_mfma_f32_16x16x32_bf16 v[32:35], a[232:235], v[200:203], v[32:35]\n" \
"v_exp_f32 v113, v113\n" \
"v_mfma_f32_16x16x32_bf16 v[32:35], a[236:239], v[204:207], v[32:35]\n" \
"ds_read_b128 a[212:215], v247 offset:43008\n" \
"v_add_f32 v244, v244, v113\n" \
"v_mfma_f32_16x16x32_bf16 v[40:43], a[232:235], v[216:219], v[228:231]\n" \
"v_exp_f32 v114, v114\n" \
"v_mfma_f32_16x16x32_bf16 v[40:43], a[236:239], v[220:223], v[40:43]\n" \
"v_add_f32 v244, v244, v114\n" \
"v_cvt_pk_bf16_f32 v112, v112, v113\n" \
"v_mfma_f32_16x16x32_bf16 v[40:43], a[224:227], v[208:211], v[40:43]\n" \
"v_exp_f32 v115, v115\n" \
"v_mfma_f32_16x16x32_bf16 v[40:43], a[228:231], v[212:215], v[40:43]\n" \
"ds_read_b128 a[216:219], v247 offset:35840\n" \
"global_load_lds_dwordx4 v250, s[38:39] offset:2048 sc1\n" \
"v_add_f32 v244, v244, v115\n" \
"v_mfma_f32_16x16x32_bf16 v[48:51], a[224:227], a[160:163], v[232:235]\n" \
"v_exp_f32 v116, v116\n" \
"v_mfma_f32_16x16x32_bf16 v[48:51], a[228:231], a[164:167], v[48:51]\n" \
"v_add_f32 v244, v244, v116\n" \
"v_cvt_pk_bf16_f32 v113, v114, v115\n" \
"v_mfma_f32_16x16x32_bf16 v[48:51], a[232:235], a[168:171], v[48:51]\n" \
"v_exp_f32 v117, v117\n" \
"v_mfma_f32_16x16x32_bf16 v[48:51], a[236:239], a[172:175], v[48:51]\n" \
"ds_read_b128 a[220:223], v247 offset:44032\n" \
"global_load_lds_dwordx4 v250, s[38:39] offset:3072 sc1\n" \
"v_add_f32 v244, v244, v117\n" \
"v_mfma_f32_16x16x32_bf16 v[56:59], a[232:235], a[184:187], v[236:239]\n" \
"s_add_u32 s38, s38, 34816\n" \
"v_exp_f32 v118, v118\n" \
"v_mfma_f32_16x16x32_bf16 v[56:59], a[236:239], a[188:191], v[56:59]\n" \
"v_add_f32 v244, v244, v118\n" \
"v_cvt_pk_bf16_f32 v114, v116, v117\n" \
"v_mfma_f32_16x16x32_bf16 v[56:59], a[224:227], a[176:179], v[56:59]\n" \
"v_exp_f32 v119, v119\n" \
"v_mfma_f32_16x16x32_bf16 v[56:59], a[228:231], a[180:183], v[56:59]\n" \
"ds_read_b128 a[224:227], v247 offset:36864\n" \
"v_add_f32 v244, v244, v119\n" \
"v_mfma_f32_16x16x32_bf16 v[60:63], a[240:243], a[176:179], v[236:239]\n" \
"v_exp_f32 v120, v120\n" \
"v_mfma_f32_16x16x32_bf16 v[60:63], a[244:247], a[180:183], v[60:63]\n" \
"v_add_f32 v245, v245, v120\n" \
"v_cvt_pk_bf16_f32 v115, v118, v119\n" \
"v_mfma_f32_16x16x32_bf16 v[60:63], a[248:251], a[184:187], v[60:63]\n" \
"v_exp_f32 v121, v121\n" \
"v_mfma_f32_16x16x32_bf16 v[60:63], a[252:255], a[188:191], v[60:63]\n" \
"ds_read_b128 a[228:231], v247 offset:45056\n" \
"v_add_f32 v245, v245, v121\n" \
"v_mfma_f32_16x16x32_bf16 v[52:55], a[248:251], a[168:171], v[232:235]\n" \
"v_exp_f32 v122, v122\n" \
"v_mfma_f32_16x16x32_bf16 v[52:55], a[252:255], a[172:175], v[52:55]\n" \
"v_add_f32 v245, v245, v122\n" \
"v_cvt_pk_bf16_f32 v120, v120, v121\n" \
"v_mfma_f32_16x16x32_bf16 v[52:55], a[240:243], a[160:163], v[52:55]\n" \
"v_exp_f32 v123, v123\n" \
"v_mfma_f32_16x16x32_bf16 v[52:55], a[244:247], a[164:167], v[52:55]\n" \
"ds_read_b128 a[232:235], v247 offset:37888\n" \
"v_add_f32 v245, v245, v123\n" \
"v_mfma_f32_16x16x32_bf16 v[44:47], a[240:243], v[208:211], v[228:231]\n" \
"v_exp_f32 v124, v124\n" \
"v_mfma_f32_16x16x32_bf16 v[44:47], a[244:247], v[212:215], v[44:47]\n" \
"v_add_f32 v245, v245, v124\n" \
"v_cvt_pk_bf16_f32 v121, v122, v123\n" \
"v_mfma_f32_16x16x32_bf16 v[44:47], a[248:251], v[216:219], v[44:47]\n" \
"v_exp_f32 v125, v125\n" \
"v_mfma_f32_16x16x32_bf16 v[44:47], a[252:255], v[220:223], v[44:47]\n" \
"ds_read_b128 a[236:239], v247 offset:46080\n" \
"v_add_f32 v245, v245, v125\n" \
"v_mfma_f32_16x16x32_bf16 v[36:39], a[248:251], v[200:203], v[224:227]\n" \
"v_exp_f32 v126, v126\n" \
"v_mfma_f32_16x16x32_bf16 v[36:39], a[252:255], v[204:207], v[36:39]\n" \
"v_add_f32 v245, v245, v126\n" \
"v_cvt_pk_bf16_f32 v122, v124, v125\n" \
"v_mfma_f32_16x16x32_bf16 v[36:39], a[240:243], v[192:195], v[36:39]\n" \
"s_waitcnt vmcnt(8) lgkmcnt(4)\n" \
"v_exp_f32 v127, v127\n" \
"v_mfma_f32_16x16x32_bf16 v[36:39], a[244:247], v[196:199], v[36:39]\n" \
"s_barrier\n" \
"ds_read_b128 a[240:243], v247 offset:38912\n" \
"v_add_f32 v245, v245, v127\n" \
"v_mfma_f32_16x16x32_bf16 a[64:67], a[192:195], v[64:67], a[64:67]\n" \
"v_exp_f32 v0, v0\n" \
"v_mfma_f32_16x16x32_bf16 a[64:67], a[196:199], v[96:99], a[64:67]\n" \
"v_cvt_pk_bf16_f32 v123, v126, v127\n" \
"v_add_f32 v240, v240, v0\n" \
"v_exp_f32 v1, v1\n" \
"v_mfma_f32_16x16x32_bf16 a[96:99], a[192:195], v[72:75], a[96:99]\n" \
"s_xor_b32 m0, s73, 0x8000\n" \
"ds_read_b128 a[244:247], v247 offset:47104\n" \
"v_add_f32 v240, v240, v1\n" \
"v_mfma_f32_16x16x32_bf16 a[96:99], a[196:199], v[104:107], a[96:99]\n" \
"ds_read_b128 a[248:251], v247 offset:39936\n" \
"global_load_lds_dwordx4 v250, s[36:37] offset:-4096 sc1\n" \
"v_mfma_f32_16x16x32_bf16 v[128:131], a[192:195], v[80:83], v[128:131]\n" \
"ds_read_b128 a[252:255], v247 offset:48128\n" \
"v_exp_f32 v2, v2\n" \
"v_mfma_f32_16x16x32_bf16 v[128:131], a[196:199], v[112:115], v[128:131]\n" \
"v_add_f32 v240, v240, v2\n" \
"v_cvt_pk_bf16_f32 v0, v0, v1\n" \
"v_mfma_f32_16x16x32_bf16 v[160:163], a[192:195], v[88:91], v[160:163]\n" \
"v_exp_f32 v3, v3\n" \
"v_mfma_f32_16x16x32_bf16 v[160:163], a[196:199], v[120:123], v[160:163]\n" \
"ds_read_b128 a[192:195], v252 offset:0\n" \
"global_load_lds_dwordx4 v250, s[36:37] offset:-3072 sc1\n" \
"Lpg7397_%=:\n" \
"Lst1x0_%=:\n" \
"s_cbranch_execz Lpg9725_%=\n" \
"v_add_f32 v240, v240, v3\n" \
"v_mfma_f32_16x16x32_bf16 v[164:167], a[200:203], v[88:91], v[164:167]\n" \
"v_exp_f32 v4, v4\n" \
"v_mfma_f32_16x16x32_bf16 v[164:167], a[204:207], v[120:123], v[164:167]\n" \
"v_add_f32 v240, v240, v4\n" \
"v_cvt_pk_bf16_f32 v1, v2, v3\n" \
"v_mfma_f32_16x16x32_bf16 v[132:135], a[200:203], v[80:83], v[132:135]\n" \
"v_exp_f32 v5, v5\n" \
"v_mfma_f32_16x16x32_bf16 v[132:135], a[204:207], v[112:115], v[132:135]\n" \
"ds_read_b128 a[196:199], v252 offset:2048\n" \
"v_add_f32 v240, v240, v5\n" \
"v_mfma_f32_16x16x32_bf16 a[100:103], a[200:203], v[72:75], a[100:103]\n" \
"v_exp_f32 v6, v6\n" \
"v_mfma_f32_16x16x32_bf16 a[100:103], a[204:207], v[104:107], a[100:103]\n" \
"v_add_f32 v240, v240, v6\n" \
"v_cvt_pk_bf16_f32 v2, v4, v5\n" \
"v_mfma_f32_16x16x32_bf16 a[68:71], a[200:203], v[64:67], a[68:71]\n" \
"v_exp_f32 v7, v7\n" \
"v_mfma_f32_16x16x32_bf16 a[68:71], a[204:207], v[96:99], a[68:71]\n" \
"ds_read_b128 a[200:203], v252 offset:4096\n" \
"global_load_lds_dwordx4 v250, s[36:37] offset:-2048 sc1\n" \
"v_add_f32 v240, v240, v7\n" \
"v_mfma_f32_16x16x32_bf16 a[72:75], a[208:211], v[64:67], a[72:75]\n" \
"v_exp_f32 v8, v8\n" \
"v_mfma_f32_16x16x32_bf16 a[72:75], a[212:215], v[96:99], a[72:75]\n" \
"v_add_f32 v241, v241, v8\n" \
"v_cvt_pk_bf16_f32 v3, v6, v7\n" \
"v_mfma_f32_16x16x32_bf16 a[104:107], a[208:211], v[72:75], a[104:107]\n" \
"v_exp_f32 v9, v9\n" \
"v_mfma_f32_16x16x32_bf16 a[104:107], a[212:215], v[104:107], a[104:107]\n" \
"ds_read_b128 a[204:207], v252 offset:6144\n" \
"global_load_lds_dwordx4 v250, s[36:37] offset:-1024 sc1\n" \
"v_add_f32 v241, v241, v9\n" \
"v_mfma_f32_16x16x32_bf16 v[136:139], a[208:211], v[80:83], v[136:139]\n" \
"v_exp_f32 v10, v10\n" \
"v_mfma_f32_16x16x32_bf16 v[136:139], a[212:215], v[112:115], v[136:139]\n" \
"v_add_f32 v241, v241, v10\n" \
"v_cvt_pk_bf16_f32 v8, v8, v9\n" \
"v_mfma_f32_16x16x32_bf16 v[168:171], a[208:211], v[88:91], v[168:171]\n" \
"v_exp_f32 v11, v11\n" \
"v_mfma_f32_16x16x32_bf16 v[168:171], a[212:215], v[120:123], v[168:171]\n" \
"ds_read_b128 a[208:211], v252 offset:256\n" \
"v_add_f32 v241, v241, v11\n" \
"v_mfma_f32_16x16x32_bf16 v[172:175], a[216:219], v[88:91], v[172:175]\n" \
"v_exp_f32 v12, v12\n" \
"v_mfma_f32_16x16x32_bf16 v[172:175], a[220:223], v[120:123], v[172:175]\n" \
"v_add_f32 v241, v241, v12\n" \
"v_cvt_pk_bf16_f32 v9, v10, v11\n" \
"v_mfma_f32_16x16x32_bf16 v[140:143], a[216:219], v[80:83], v[140:143]\n" \
"v_exp_f32 v13, v13\n" \
"v_mfma_f32_16x16x32_bf16 v[140:143], a[220:223], v[112:115], v[140:143]\n" \
"ds_read_b128 a[212:215], v252 offset:2304\n" \
"global_load_lds_dwordx4 v250, s[36:37] offset:0 sc1\n" \
"v_add_f32 v241, v241, v13\n" \
"v_mfma_f32_16x16x32_bf16 a[108:111], a[216:219], v[72:75], a[108:111]\n" \
"v_exp_f32 v14, v14\n" \
"v_mfma_f32_16x16x32_bf16 a[108:111], a[220:223], v[104:107], a[108:111]\n" \
"v_add_f32 v241, v241, v14\n" \
"v_cvt_pk_bf16_f32 v10, v12, v13\n" \
"v_mfma_f32_16x16x32_bf16 a[76:79], a[216:219], v[64:67], a[76:79]\n" \
"s_waitcnt lgkmcnt(6)\n" \
"v_exp_f32 v15, v15\n" \
"v_mfma_f32_16x16x32_bf16 a[76:79], a[220:223], v[96:99], a[76:79]\n" \
"ds_read_b128 a[216:219], v252 offset:4352\n" \
"global_load_lds_dwordx4 v250, s[36:37] offset:1024 sc1\n" \
"v_add_f32 v241, v241, v15\n" \
"v_mfma_f32_16x16x32_bf16 a[80:83], a[224:227], v[64:67], a[80:83]\n" \
"v_exp_f32 v16, v16\n" \
"v_mfma_f32_16x16x32_bf16 a[80:83], a[228:231], v[96:99], a[80:83]\n" \
"v_add_f32 v244, v244, v16\n" \
"v_cvt_pk_bf16_f32 v11, v14, v15\n" \
"v_mfma_f32_16x16x32_bf16 a[112:115], a[224:227], v[72:75], a[112:115]\n" \
"v_exp_f32 v17, v17\n" \
"v_mfma_f32_16x16x32_bf16 a[112:115], a[228:231], v[104:107], a[112:115]\n" \
"ds_read_b128 a[220:223], v252 offset:6400\n" \
"v_add_f32 v244, v244, v17\n" \
"v_mfma_f32_16x16x32_bf16 v[144:147], a[224:227], v[80:83], v[144:147]\n" \
"v_exp_f32 v18, v18\n" \
"v_mfma_f32_16x16x32_bf16 v[144:147], a[228:231], v[112:115], v[144:147]\n" \
"v_add_f32 v244, v244, v18\n" \
"v_cvt_pk_bf16_f32 v16, v16, v17\n" \
"v_mfma_f32_16x16x32_bf16 v[176:179], a[224:227], v[88:91], v[176:179]\n" \
"v_exp_f32 v19, v19\n" \
"v_mfma_f32_16x16x32_bf16 v[176:179], a[228:231], v[120:123], v[176:179]\n" \
"ds_read_b128 a[224:227], v252 offset:8192\n" \
"global_load_lds_dwordx4 v250, s[36:37] offset:2048 sc1\n" \
"v_add_f32 v244, v244, v19\n" \
"v_mfma_f32_16x16x32_bf16 v[180:183], a[232:235], v[88:91], v[180:183]\n" \
"v_exp_f32 v20, v20\n" \
"v_mfma_f32_16x16x32_bf16 v[180:183], a[236:239], v[120:123], v[180:183]\n" \
"v_add_f32 v244, v244, v20\n" \
"v_cvt_pk_bf16_f32 v17, v18, v19\n" \
"v_mfma_f32_16x16x32_bf16 v[148:151], a[232:235], v[80:83], v[148:151]\n" \
"v_exp_f32 v21, v21\n" \
"v_mfma_f32_16x16x32_bf16 v[148:151], a[236:239], v[112:115], v[148:151]\n" \
"ds_read_b128 a[228:231], v252 offset:10240\n" \
"global_load_lds_dwordx4 v250, s[36:37] offset:3072 sc1\n" \
"v_add_f32 v244, v244, v21\n" \
"v_mfma_f32_16x16x32_bf16 a[116:119], a[232:235], v[72:75], a[116:119]\n" \
"s_add_u32 s36, s36, 34816\n" \
"v_exp_f32 v22, v22\n" \
"v_mfma_f32_16x16x32_bf16 a[116:119], a[236:239], v[104:107], a[116:119]\n" \
"v_add_f32 v244, v244, v22\n" \
"v_cvt_pk_bf16_f32 v18, v20, v21\n" \
"v_mfma_f32_16x16x32_bf16 a[84:87], a[232:235], v[64:67], a[84:87]\n" \
"v_exp_f32 v23, v23\n" \
"v_mfma_f32_16x16x32_bf16 a[84:87], a[236:239], v[96:99], a[84:87]\n" \
"ds_read_b128 a[232:235], v252 offset:12288\n" \
"v_add_f32 v244, v244, v23\n" \
"v_mfma_f32_16x16x32_bf16 a[88:91], a[240:243], v[64:67], a[88:91]\n" \
"v_exp_f32 v24, v24\n" \
"v_mfma_f32_16x16x32_bf16 a[88:91], a[244:247], v[96:99], a[88:91]\n" \
"v_add_f32 v245, v245, v24\n" \
"v_cvt_pk_bf16_f32 v19, v22, v23\n" \
"v_mfma_f32_16x16x32_bf16 a[120:123], a[240:243], v[72:75], a[120:123]\n" \
"v_exp_f32 v25, v25\n" \
"v_mfma_f32_16x16x32_bf16 a[120:123], a[244:247], v[104:107], a[120:123]\n" \
"ds_read_b128 a[236:239], v252 offset:14336\n" \
"v_add_f32 v245, v245, v25\n" \
"v_mfma_f32_16x16x32_bf16 v[152:155], a[240:243], v[80:83], v[152:155]\n" \
"v_exp_f32 v26, v26\n" \
"v_mfma_f32_16x16x32_bf16 v[152:155], a[244:247], v[112:115], v[152:155]\n" \
"v_add_f32 v245, v245, v26\n" \
"v_cvt_pk_bf16_f32 v24, v24, v25\n" \
"v_mfma_f32_16x16x32_bf16 v[184:187], a[240:243], v[88:91], v[184:187]\n" \
"v_exp_f32 v27, v27\n" \
"v_mfma_f32_16x16x32_bf16 v[184:187], a[244:247], v[120:123], v[184:187]\n" \
"ds_read_b128 a[240:243], v252 offset:8448\n" \
"v_add_f32 v245, v245, v27\n" \
"v_mfma_f32_16x16x32_bf16 v[188:191], a[248:251], v[88:91], v[188:191]\n" \
"v_exp_f32 v28, v28\n" \
"v_mfma_f32_16x16x32_bf16 v[188:191], a[252:255], v[120:123], v[188:191]\n" \
"v_add_f32 v245, v245, v28\n" \
"v_cvt_pk_bf16_f32 v25, v26, v27\n" \
"v_mfma_f32_16x16x32_bf16 v[156:159], a[248:251], v[80:83], v[156:159]\n" \
"v_exp_f32 v29, v29\n" \
"v_mfma_f32_16x16x32_bf16 v[156:159], a[252:255], v[112:115], v[156:159]\n" \
"ds_read_b128 a[244:247], v252 offset:10496\n" \
"v_add_f32 v245, v245, v29\n" \
"v_mfma_f32_16x16x32_bf16 a[124:127], a[248:251], v[72:75], a[124:127]\n" \
"v_exp_f32 v30, v30\n" \
"v_mfma_f32_16x16x32_bf16 a[124:127], a[252:255], v[104:107], a[124:127]\n" \
"v_add_f32 v245, v245, v30\n" \
"v_cvt_pk_bf16_f32 v26, v28, v29\n" \
"v_mfma_f32_16x16x32_bf16 a[92:95], a[248:251], v[64:67], a[92:95]\n" \
"s_waitcnt vmcnt(8) lgkmcnt(6)\n" \
"v_exp_f32 v31, v31\n" \
"v_mfma_f32_16x16x32_bf16 a[92:95], a[252:255], v[96:99], a[92:95]\n" \
"s_barrier\n" \
"ds_read_b128 a[248:251], v252 offset:12544\n" \
"v_add_f32 v245, v245, v31\n" \
"LA1g_%=:\n" \
"Lpg7550_%=:\n" \
"s_cbranch_execz Lcb0_%=\n" \
"v_mfma_f32_16x16x32_bf16 v[64:67], a[192:195], v[192:195], v[224:227]\n" \
"v_exp_f32 v32, v32\n" \
"v_mfma_f32_16x16x32_bf16 v[64:67], a[196:199], v[196:199], v[64:67]\n" \
"v_cvt_pk_bf16_f32 v27, v30, v31\n" \
"v_add_f32 v240, v240, v32\n" \
"v_mfma_f32_16x16x32_bf16 v[64:67], a[200:203], v[200:203], v[64:67]\n" \
"s_add_u32 m0, s74, 0x8000\n" \
"v_exp_f32 v33, v33\n" \
"v_mfma_f32_16x16x32_bf16 v[64:67], a[204:207], v[204:207], v[64:67]\n" \
"ds_read_b128 a[252:255], v252 offset:14592\n" \
"v_add_f32 v240, v240, v33\n" \
"v_mfma_f32_16x16x32_bf16 v[72:75], a[200:203], v[216:219], v[228:231]\n" \
"global_load_lds_dwordx4 v250, s[38:39] offset:-4096 sc1\n" \
"global_load_lds_dwordx4 v250, s[38:39] offset:-3072 sc1\n" \
"v_mfma_f32_16x16x32_bf16 v[72:75], a[204:207], v[220:223], v[72:75]\n" \
"v_exp_f32 v34, v34\n" \
"v_mfma_f32_16x16x32_bf16 v[72:75], a[192:195], v[208:211], v[72:75]\n" \
"v_add_f32 v240, v240, v34\n" \
"v_cvt_pk_bf16_f32 v32, v32, v33\n" \
"v_mfma_f32_16x16x32_bf16 v[72:75], a[196:199], v[212:215], v[72:75]\n" \
"v_exp_f32 v35, v35\n" \
"v_mfma_f32_16x16x32_bf16 v[80:83], a[192:195], a[160:163], v[232:235]\n" \
"v_add_f32 v240, v240, v35\n" \
"v_exp_f32 v36, v36\n" \
"v_mfma_f32_16x16x32_bf16 v[80:83], a[196:199], a[164:167], v[80:83]\n" \
"v_add_f32 v240, v240, v36\n" \
"v_cvt_pk_bf16_f32 v33, v34, v35\n" \
"v_mfma_f32_16x16x32_bf16 v[80:83], a[200:203], a[168:171], v[80:83]\n" \
"v_exp_f32 v37, v37\n" \
"v_mfma_f32_16x16x32_bf16 v[80:83], a[204:207], a[172:175], v[80:83]\n" \
"v_add_f32 v240, v240, v37\n" \
"v_exp_f32 v38, v38\n" \
"v_mfma_f32_16x16x32_bf16 v[88:91], a[200:203], a[184:187], v[236:239]\n" \
"v_add_f32 v240, v240, v38\n" \
"v_cvt_pk_bf16_f32 v34, v36, v37\n" \
"v_mfma_f32_16x16x32_bf16 v[88:91], a[204:207], a[188:191], v[88:91]\n" \
"v_exp_f32 v39, v39\n" \
"v_mfma_f32_16x16x32_bf16 v[88:91], a[192:195], a[176:179], v[88:91]\n" \
"v_add_f32 v240, v240, v39\n" \
"v_mfma_f32_16x16x32_bf16 v[88:91], a[196:199], a[180:183], v[88:91]\n" \
"ds_read_b128 a[192:195], v247 offset:0\n" \
"global_load_lds_dwordx4 v250, s[38:39] offset:-2048 sc1\n" \
"v_mfma_f32_16x16x32_bf16 v[92:95], a[208:211], a[176:179], v[236:239]\n" \
"v_exp_f32 v40, v40\n" \
"v_mfma_f32_16x16x32_bf16 v[92:95], a[212:215], a[180:183], v[92:95]\n" \
"v_add_f32 v241, v241, v40\n" \
"v_cvt_pk_bf16_f32 v35, v38, v39\n" \
"v_mfma_f32_16x16x32_bf16 v[92:95], a[216:219], a[184:187], v[92:95]\n" \
"v_exp_f32 v41, v41\n" \
"v_mfma_f32_16x16x32_bf16 v[92:95], a[220:223], a[188:191], v[92:95]\n" \
"ds_read_b128 a[196:199], v247 offset:8192\n" \
"global_load_lds_dwordx4 v250, s[38:39] offset:-1024 sc1\n" \
"v_add_f32 v241, v241, v41\n" \
"v_mfma_f32_16x16x32_bf16 v[84:87], a[216:219], a[168:171], v[232:235]\n" \
"v_exp_f32 v42, v42\n" \
"v_mfma_f32_16x16x32_bf16 v[84:87], a[220:223], a[172:175], v[84:87]\n" \
"v_add_f32 v241, v241, v42\n" \
"v_cvt_pk_bf16_f32 v40, v40, v41\n" \
"v_mfma_f32_16x16x32_bf16 v[84:87], a[208:211], a[160:163], v[84:87]\n" \
"v_exp_f32 v43, v43\n" \
"v_mfma_f32_16x16x32_bf16 v[84:87], a[212:215], a[164:167], v[84:87]\n" \
"ds_read_b128 a[200:203], v247 offset:1024\n" \
"v_add_f32 v241, v241, v43\n" \
"v_mfma_f32_16x16x32_bf16 v[76:79], a[208:211], v[208:211], v[228:231]\n" \
"v_exp_f32 v44, v44\n" \
"v_mfma_f32_16x16x32_bf16 v[76:79], a[212:215], v[212:215], v[76:79]\n" \
"v_add_f32 v241, v241, v44\n" \
"v_cvt_pk_bf16_f32 v41, v42, v43\n" \
"v_mfma_f32_16x16x32_bf16 v[76:79], a[216:219], v[216:219], v[76:79]\n" \
"v_exp_f32 v45, v45\n" \
"v_mfma_f32_16x16x32_bf16 v[76:79], a[220:223], v[220:223], v[76:79]\n" \
"ds_read_b128 a[204:207], v247 offset:9216\n" \
"global_load_lds_dwordx4 v250, s[38:39] offset:0 sc1\n" \
"v_add_f32 v241, v241, v45\n" \
"v_mfma_f32_16x16x32_bf16 v[68:71], a[216:219], v[200:203], v[224:227]\n" \
"v_exp_f32 v46, v46\n" \
"v_mfma_f32_16x16x32_bf16 v[68:71], a[220:223], v[204:207], v[68:71]\n" \
"v_add_f32 v241, v241, v46\n" \
"v_cvt_pk_bf16_f32 v42, v44, v45\n" \
"v_mfma_f32_16x16x32_bf16 v[68:71], a[208:211], v[192:195], v[68:71]\n" \
"s_waitcnt lgkmcnt(4)\n" \
"v_exp_f32 v47, v47\n" \
"v_mfma_f32_16x16x32_bf16 v[68:71], a[212:215], v[196:199], v[68:71]\n" \
"ds_read_b128 a[208:211], v247 offset:2048\n" \
"global_load_lds_dwordx4 v250, s[38:39] offset:1024 sc1\n" \
"v_add_f32 v241, v241, v47\n" \
"v_mfma_f32_16x16x32_bf16 v[96:99], a[224:227], v[192:195], v[224:227]\n" \
"v_exp_f32 v48, v48\n" \
"v_mfma_f32_16x16x32_bf16 v[96:99], a[228:231], v[196:199], v[96:99]\n" \
"v_add_f32 v244, v244, v48\n" \
"v_cvt_pk_bf16_f32 v43, v46, v47\n" \
"v_mfma_f32_16x16x32_bf16 v[96:99], a[232:235], v[200:203], v[96:99]\n" \
"v_exp_f32 v49, v49\n" \
"v_mfma_f32_16x16x32_bf16 v[96:99], a[236:239], v[204:207], v[96:99]\n" \
"ds_read_b128 a[212:215], v247 offset:10240\n" \
"v_add_f32 v244, v244, v49\n" \
"v_mfma_f32_16x16x32_bf16 v[104:107], a[232:235], v[216:219], v[228:231]\n" \
"v_exp_f32 v50, v50\n" \
"v_mfma_f32_16x16x32_bf16 v[104:107], a[236:239], v[220:223], v[104:107]\n" \
"v_add_f32 v244, v244, v50\n" \
"v_cvt_pk_bf16_f32 v48, v48, v49\n" \
"v_mfma_f32_16x16x32_bf16 v[104:107], a[224:227], v[208:211], v[104:107]\n" \
"v_exp_f32 v51, v51\n" \
"v_mfma_f32_16x16x32_bf16 v[104:107], a[228:231], v[212:215], v[104:107]\n" \
"ds_read_b128 a[216:219], v247 offset:3072\n" \
"global_load_lds_dwordx4 v250, s[38:39] offset:2048 sc1\n" \
"v_add_f32 v244, v244, v51\n" \
"v_mfma_f32_16x16x32_bf16 v[112:115], a[224:227], a[160:163], v[232:235]\n" \
"v_exp_f32 v52, v52\n" \
"v_mfma_f32_16x16x32_bf16 v[112:115], a[228:231], a[164:167], v[112:115]\n" \
"v_add_f32 v244, v244, v52\n" \
"v_cvt_pk_bf16_f32 v49, v50, v51\n" \
"v_mfma_f32_16x16x32_bf16 v[112:115], a[232:235], a[168:171], v[112:115]\n" \
"v_exp_f32 v53, v53\n" \
"v_mfma_f32_16x16x32_bf16 v[112:115], a[236:239], a[172:175], v[112:115]\n" \
"ds_read_b128 a[220:223], v247 offset:11264\n" \
"global_load_lds_dwordx4 v250, s[38:39] offset:3072 sc1\n" \
"v_add_f32 v244, v244, v53\n" \
"v_mfma_f32_16x16x32_bf16 v[120:123], a[232:235], a[184:187], v[236:239]\n" \
"s_add_u32 s38, s38, 34816\n" \
"v_exp_f32 v54, v54\n" \
"v_mfma_f32_16x16x32_bf16 v[120:123], a[236:239], a[188:191], v[120:123]\n" \
"s_sub_u32 s76, s76, 1\n" \
"v_add_f32 v244, v244, v54\n" \
"v_cvt_pk_bf16_f32 v50, v52, v53\n" \
"v_mfma_f32_16x16x32_bf16 v[120:123], a[224:227], a[176:179], v[120:123]\n" \
"s_cmp_lg_u32 s76, 0\n" \
"v_exp_f32 v55, v55\n" \
"v_mfma_f32_16x16x32_bf16 v[120:123], a[228:231], a[180:183], v[120:123]\n" \
"ds_read_b128 a[224:227], v247 offset:4096\n" \
"v_add_f32 v244, v244, v55\n" \
"v_mfma_f32_16x16x32_bf16 v[124:127], a[240:243], a[176:179], v[236:239]\n" \
"v_exp_f32 v56, v56\n" \
"v_mfma_f32_16x16x32_bf16 v[124:127], a[244:247], a[180:183], v[124:127]\n" \
"v_add_f32 v245, v245, v56\n" \
"v_cvt_pk_bf16_f32 v51, v54, v55\n" \
"v_mfma_f32_16x16x32_bf16 v[124:127], a[248:251], a[184:187], v[124:127]\n" \
"v_exp_f32 v57, v57\n" \
"v_mfma_f32_16x16x32_bf16 v[124:127], a[252:255], a[188:191], v[124:127]\n" \
"ds_read_b128 a[228:231], v247 offset:12288\n" \
"v_add_f32 v245, v245, v57\n" \
"v_mfma_f32_16x16x32_bf16 v[116:119], a[248:251], a[168:171], v[232:235]\n" \
"v_exp_f32 v58, v58\n" \
"v_mfma_f32_16x16x32_bf16 v[116:119], a[252:255], a[172:175], v[116:119]\n" \
"v_add_f32 v245, v245, v58\n" \
"v_cvt_pk_bf16_f32 v56, v56, v57\n" \
"v_mfma_f32_16x16x32_bf16 v[116:119], a[240:243], a[160:163], v[116:119]\n" \
"v_exp_f32 v59, v59\n" \
"v_mfma_f32_16x16x32_bf16 v[116:119], a[244:247], a[164:167], v[116:119]\n" \
"ds_read_b128 a[232:235], v247 offset:5120\n" \
"v_add_f32 v245, v245, v59\n" \
"v_mfma_f32_16x16x32_bf16 v[108:111], a[240:243], v[208:211], v[228:231]\n" \
"v_exp_f32 v60, v60\n" \
"v_mfma_f32_16x16x32_bf16 v[108:111], a[244:247], v[212:215], v[108:111]\n" \
"v_add_f32 v245, v245, v60\n" \
"v_cvt_pk_bf16_f32 v57, v58, v59\n" \
"v_mfma_f32_16x16x32_bf16 v[108:111], a[248:251], v[216:219], v[108:111]\n" \
"v_exp_f32 v61, v61\n" \
"v_mfma_f32_16x16x32_bf16 v[108:111], a[252:255], v[220:223], v[108:111]\n" \
"ds_read_b128 a[236:239], v247 offset:13312\n" \
"v_add_f32 v245, v245, v61\n" \
"v_mfma_f32_16x16x32_bf16 v[100:103], a[248:251], v[200:203], v[224:227]\n" \
"v_exp_f32 v62, v62\n" \
"v_mfma_f32_16x16x32_bf16 v[100:103], a[252:255], v[204:207], v[100:103]\n" \
"v_add_f32 v245, v245, v62\n" \
"v_cvt_pk_bf16_f32 v58, v60, v61\n" \
"v_mfma_f32_16x16x32_bf16 v[100:103], a[240:243], v[192:195], v[100:103]\n" \
"s_waitcnt vmcnt(8) lgkmcnt(4)\n" \
"v_exp_f32 v63, v63\n" \
"v_mfma_f32_16x16x32_bf16 v[100:103], a[244:247], v[196:199], v[100:103]\n" \
"s_barrier\n" \
"ds_read_b128 a[240:243], v247 offset:6144\n" \
"v_add_f32 v245, v245, v63\n" \
"s_cbranch_scc1 Lloop2_%=\n" \
"v_mfma_f32_16x16x32_bf16 a[64:67], a[192:195], v[0:3], a[64:67]\n" \
"v_subrev_u32 v246, s82, v246\n" \
"s_bfe_u32 s88, %1, 0x40003\n" \
"v_mfma_f32_16x16x32_bf16 a[64:67], a[196:199], v[32:35], a[64:67]\n" \
"s_memrealtime s[52:53]\n" \
"v_cvt_pk_bf16_f32 v59, v62, v63\n" \
"v_exp_f32 v64, v64\n" \
"v_mfma_f32_16x16x32_bf16 a[96:99], a[192:195], v[8:11], a[96:99]\n" \
"s_and_b32 s85, %1, 7\n" \
"ds_read_b128 a[244:247], v247 offset:14336\n" \
"v_mov_b32 v216, v243\n" \
"v_mfma_f32_16x16x32_bf16 a[96:99], a[196:199], v[40:43], a[96:99]\n" \
"s_lshl_b32 s85, s85, 1\n" \
"ds_read_b128 a[248:251], v247 offset:7168\n" \
"v_mov_b32 v214, v216\n" \
"v_mfma_f32_16x16x32_bf16 v[128:131], a[192:195], v[16:19], v[128:131]\n" \
"ds_read_b128 a[252:255], v247 offset:15360\n" \
"s_lshr_b32 s71, %1, 7\n" \
"v_mfma_f32_16x16x32_bf16 v[128:131], a[196:199], v[48:51], v[128:131]\n" \
"s_add_u32 s85, s85, s71\n" \
"v_exp_f32 v65, v65\n" \
"v_permlane16_swap_b32 v214, v216\n" \
"v_add_f32 v216, v216, v214\n" \
"v_mov_b32 v214, v216\n" \
"v_mfma_f32_16x16x32_bf16 v[160:163], a[192:195], v[24:27], v[160:163]\n" \
"s_lshl_b32 s70, s85, 7\n" \
"v_exp_f32 v66, v66\n" \
"v_mfma_f32_16x16x32_bf16 v[160:163], a[196:199], v[56:59], v[160:163]\n" \
"s_mul_i32 s84, s61, 6144\n" \
"ds_read_b128 a[192:195], v246 offset:0\n" \
"v_cvt_pk_bf16_f32 v64, v64, v65\n" \
"v_permlane32_swap_b32 v214, v216\n" \
"v_add_f32 v216, v216, v214\n" \
"v_rcp_f32 v214, v216\n" \
"v_mfma_f32_16x16x32_bf16 v[164:167], a[200:203], v[24:27], v[164:167]\n" \
"s_lshl_b32 s71, s84, 4\n" \
"v_exp_f32 v67, v67\n" \
"v_mfma_f32_16x16x32_bf16 v[164:167], a[204:207], v[56:59], v[164:167]\n" \
"s_waitcnt lgkmcnt(0)\n" \
"v_exp_f32 v68, v68\n" \
"v_mfma_f32_16x16x32_bf16 v[132:135], a[200:203], v[16:19], v[132:135]\n" \
"s_barrier\n" \
"v_mfma_f32_16x16x32_bf16 v[132:135], a[204:207], v[48:51], v[132:135]\n" \
"s_and_b32 s54, %1, 7\n" \
"ds_read_b128 a[196:199], v246 offset:2048\n" \
"v_cvt_pk_bf16_f32 v65, v66, v67\n" \
"v_mfma_f32_16x16x32_bf16 a[100:103], a[200:203], v[8:11], a[100:103]\n" \
"s_lshl_b32 s54, s54, 2\n" \
"v_exp_f32 v69, v69\n" \
"v_mfma_f32_16x16x32_bf16 a[100:103], a[204:207], v[40:43], a[100:103]\n" \
"s_lshr_b32 s55, %1, 6\n" \
"v_exp_f32 v70, v70\n" \
"v_cvt_pk_bf16_f32 v66, v68, v69\n" \
"v_mfma_f32_16x16x32_bf16 a[68:71], a[200:203], v[0:3], a[68:71]\n" \
"s_add_u32 s54, s54, s55\n" \
"v_exp_f32 v71, v71\n" \
"v_mfma_f32_16x16x32_bf16 a[68:71], a[204:207], v[32:35], a[68:71]\n" \
"s_lshl_b32 s54, s54, 3\n" \
"ds_read_b128 a[200:203], v246 offset:4096\n" \
"v_exp_f32 v72, v72\n" \
"v_mfma_f32_16x16x32_bf16 a[72:75], a[208:211], v[0:3], a[72:75]\n" \
"s_add_u32 s54, s54, 65593856\n" \
"v_cvt_pk_bf16_f32 v67, v70, v71\n" \
"v_mul_f32 v216, 0x4d800000, v216\n" \
"v_mfma_f32_16x16x32_bf16 a[72:75], a[212:215], v[32:35], a[72:75]\n" \
"s_sub_u32 s53, s52, s12\n" \
"v_exp_f32 v73, v73\n" \
"v_mfma_f32_16x16x32_bf16 a[104:107], a[208:211], v[8:11], a[104:107]\n" \
"s_mul_i32 s53, s53, 3\n" \
"v_exp_f32 v74, v74\n" \
"v_mfma_f32_16x16x32_bf16 a[104:107], a[212:215], v[40:43], a[104:107]\n" \
"s_lshl_b32 s52, s52, 1\n" \
"ds_read_b128 a[204:207], v246 offset:6144\n" \
"v_cvt_pk_bf16_f32 v72, v72, v73\n" \
"v_mfma_f32_16x16x32_bf16 v[136:139], a[208:211], v[16:19], v[136:139]\n" \
"s_add_u32 s52, s52, s53\n" \
"v_mov_b32 v211, s52\n" \
"v_xor_b32 v210, s60, v211\n" \
"v_mfma_f32_16x16x32_bf16 v[136:139], a[212:215], v[48:51], v[136:139]\n" \
"s_add_u32 s70, s70, s71\n" \
"v_exp_f32 v75, v75\n" \
"v_mfma_f32_16x16x32_bf16 v[168:171], a[208:211], v[24:27], v[168:171]\n" \
"s_add_u32 s70, s70, 4096\n" \
"v_exp_f32 v76, v76\n" \
"v_mfma_f32_16x16x32_bf16 v[168:171], a[212:215], v[56:59], v[168:171]\n" \
"s_mov_b32 m0, s74\n" \
"ds_read_b128 a[208:211], v246 offset:256\n" \
"v_lshlrev_b32 v212, 28, s61\n" \
"v_mfma_f32_16x16x32_bf16 v[172:175], a[216:219], v[24:27], v[172:175]\n" \
"s_cmp_eq_u32 s63, 3\n" \
"s_cselect_b32 s54, s54, 0x20000000\n" \
"v_cvt_pk_bf16_f32 v73, v74, v75\n" \
"v_mfma_f32_16x16x32_bf16 v[172:175], a[220:223], v[56:59], v[172:175]\n" \
"s_add_u32 s48, s22, s70\n" \
"s_addc_u32 s49, s23, 0\n" \
"v_add_u32 v215, 0x1c90080, v249\n" \
"v_mfma_f32_16x16x32_bf16 v[140:143], a[216:219], v[16:19], v[140:143]\n" \
"s_add_u32 s50, s48, 23552\n" \
"s_addc_u32 s51, s49, 0\n" \
"v_cndmask_b32 v215, v249, v215, s[16:17]\n" \
"v_mfma_f32_16x16x32_bf16 v[140:143], a[220:223], v[48:51], v[140:143]\n" \
"s_lshl_b32 s70, s88, 18\n" \
"ds_read_b128 a[212:215], v246 offset:2304\n" \
"global_load_lds_dwordx4 v250, s[38:39] offset:-4096 sc1\n" \
"v_mfma_f32_16x16x32_bf16 a[108:111], a[216:219], v[8:11], a[108:111]\n" \
"s_lshl_b32 s71, s61, 16\n" \
"v_exp_f32 v77, v77\n" \
"v_mfma_f32_16x16x32_bf16 a[108:111], a[220:223], v[40:43], a[108:111]\n" \
"s_add_u32 s71, s71, s70\n" \
"v_exp_f32 v78, v78\n" \
"v_mfma_f32_16x16x32_bf16 a[76:79], a[216:219], v[0:3], a[76:79]\n" \
"s_waitcnt lgkmcnt(6)\n" \
"global_load_lds_dwordx4 v250, s[38:39] offset:-3072 sc1\n" \
"global_load_lds_dwordx4 v250, s[38:39] offset:-2048 sc1\n" \
"v_mfma_f32_16x16x32_bf16 a[76:79], a[220:223], v[32:35], a[76:79]\n" \
"s_add_u32 s42, s20, s71\n" \
"s_addc_u32 s43, s21, 0\n" \
"ds_read_b128 a[216:219], v246 offset:4352\n" \
"v_mfma_f32_16x16x32_bf16 a[80:83], a[224:227], v[0:3], a[80:83]\n" \
"s_add_u32 s44, s42, 31744\n" \
"s_addc_u32 s45, s43, 0\n" \
"v_cvt_pk_bf16_f32 v74, v76, v77\n" \
"v_mfma_f32_16x16x32_bf16 a[80:83], a[228:231], v[32:35], a[80:83]\n" \
"global_load_lds_dwordx4 v250, s[38:39] offset:-1024 sc1\n" \
"global_load_lds_dwordx4 v250, s[38:39] offset:0 sc1\n" \
"v_mfma_f32_16x16x32_bf16 a[112:115], a[224:227], v[8:11], a[112:115]\n" \
"global_load_lds_dwordx4 v250, s[38:39] offset:1024 sc1\n" \
"global_load_lds_dwordx4 v250, s[38:39] offset:2048 sc1\n" \
"v_mfma_f32_16x16x32_bf16 a[112:115], a[228:231], v[40:43], a[112:115]\n" \
"s_add_u32 s77, s77, 532480\n" \
"ds_read_b128 a[220:223], v246 offset:6400\n" \
"global_load_lds_dwordx4 v250, s[38:39] offset:3072 sc1\n" \
"v_accvgpr_read_b32 v192, a32\n" \
"v_accvgpr_read_b32 v193, a33\n" \
"v_accvgpr_read_b32 v194, a34\n" \
"v_mfma_f32_16x16x32_bf16 v[144:147], a[224:227], v[16:19], v[144:147]\n" \
"v_exp_f32 v79, v79\n" \
"v_mfma_f32_16x16x32_bf16 v[144:147], a[228:231], v[48:51], v[144:147]\n" \
"v_exp_f32 v80, v80\n" \
"v_mfma_f32_16x16x32_bf16 v[176:179], a[224:227], v[24:27], v[176:179]\n" \
"v_cvt_pk_bf16_f32 v75, v78, v79\n" \
"v_accvgpr_read_b32 v195, a35\n" \
"v_mfma_f32_16x16x32_bf16 v[176:179], a[228:231], v[56:59], v[176:179]\n" \
"ds_read_b128 a[224:227], v246 offset:8192\n" \
"v_accvgpr_read_b32 v196, a36\n" \
"v_accvgpr_read_b32 v197, a37\n" \
"v_accvgpr_read_b32 v198, a38\n" \
"v_accvgpr_read_b32 v199, a39\n" \
"v_pk_mul_f32 v[192:193], v[192:193], v[214:215] op_sel_hi:[1,0]\n" \
"v_pk_mul_f32 v[194:195], v[194:195], v[214:215] op_sel_hi:[1,0]\n" \
"v_pk_mul_f32 v[196:197], v[196:197], v[214:215] op_sel_hi:[1,0]\n" \
"v_pk_mul_f32 v[198:199], v[198:199], v[214:215] op_sel_hi:[1,0]\n" \
"v_cvt_pk_bf16_f32 v200, v192, v193\n" \
"v_cvt_pk_bf16_f32 v201, v194, v195\n" \
"v_cvt_pk_bf16_f32 v202, v196, v197\n" \
"v_cvt_pk_bf16_f32 v203, v198, v199\n" \
"v_pk_mul_f32 v[192:193], v[192:193], v[194:195]\n" \
"v_pk_fma_f32 v[192:193], v[196:197], v[198:199], v[192:193]\n" \
"v_fma_f32 v216, v192, v193, v216\n" \
"v_mfma_f32_16x16x32_bf16 v[180:183], a[232:235], v[24:27], v[180:183]\n" \
"v_exp_f32 v81, v81\n" \
"v_mfma_f32_16x16x32_bf16 v[180:183], a[236:239], v[56:59], v[180:183]\n" \
"buffer_store_dwordx4 v[200:203], v215, s[56:59], s77 offen offset:0 sc1\n" \
"buffer_store_dwordx2 v[210:211], v212, s[56:59], s54 offen sc1\n" \
"v_accvgpr_read_b32 v192, a40\n" \
"v_accvgpr_read_b32 v193, a41\n" \
"v_accvgpr_read_b32 v194, a42\n" \
"v_accvgpr_read_b32 v195, a43\n" \
"v_accvgpr_read_b32 v196, a44\n" \
"v_accvgpr_read_b32 v197, a45\n" \
"v_accvgpr_read_b32 v198, a46\n" \
"v_accvgpr_read_b32 v199, a47\n" \
"v_pk_mul_f32 v[192:193], v[192:193], v[214:215] op_sel_hi:[1,0]\n" \
"v_pk_mul_f32 v[194:195], v[194:195], v[214:215] op_sel_hi:[1,0]\n" \
"v_pk_mul_f32 v[196:197], v[196:197], v[214:215] op_sel_hi:[1,0]\n" \
"v_pk_mul_f32 v[198:199], v[198:199], v[214:215] op_sel_hi:[1,0]\n" \
"v_cvt_pk_bf16_f32 v204, v192, v193\n" \
"v_cvt_pk_bf16_f32 v205, v194, v195\n" \
"v_cvt_pk_bf16_f32 v206, v196, v197\n" \
"v_cvt_pk_bf16_f32 v207, v198, v199\n" \
"v_pk_mul_f32 v[192:193], v[192:193], v[194:195]\n" \
"v_pk_fma_f32 v[192:193], v[196:197], v[198:199], v[192:193]\n" \
"v_mfma_f32_16x16x32_bf16 v[148:151], a[232:235], v[16:19], v[148:151]\n" \
"v_exp_f32 v82, v82\n" \
"v_mfma_f32_16x16x32_bf16 v[148:151], a[236:239], v[48:51], v[148:151]\n" \
"ds_read_b128 a[228:231], v246 offset:10240\n" \
"v_cvt_pk_bf16_f32 v80, v80, v81\n" \
"v_mfma_f32_16x16x32_bf16 v[238:241], a[0:3], v[64:67], v[238:241]\n" \
"v_exp_f32 v83, v83\n" \
"v_mfma_f32_16x16x32_bf16 v[238:241], a[4:7], v[72:75], v[238:241]\n" \
"v_exp_f32 v84, v84\n" \
"v_mfma_f32_16x16x32_bf16 a[116:119], a[232:235], v[8:11], a[116:119]\n" \
"v_cvt_pk_bf16_f32 v81, v82, v83\n" \
"v_fma_f32 v216, v192, v193, v216\n" \
"v_mfma_f32_16x16x32_bf16 a[116:119], a[236:239], v[40:43], a[116:119]\n" \
"buffer_store_dwordx4 v[204:207], v215, s[56:59], s77 offen offset:2048 sc1\n" \
"s_add_u32 s77, s77, 0x1000\n" \
"v_accvgpr_read_b32 v192, a48\n" \
"v_accvgpr_read_b32 v193, a49\n" \
"v_accvgpr_read_b32 v194, a50\n" \
"v_accvgpr_read_b32 v195, a51\n" \
"v_accvgpr_read_b32 v196, a52\n" \
"v_accvgpr_read_b32 v197, a53\n" \
"v_accvgpr_read_b32 v198, a54\n" \
"v_accvgpr_read_b32 v199, a55\n" \
"v_exp_f32 v85, v85\n" \
"v_pk_mul_f32 v[192:193], v[192:193], v[214:215] op_sel_hi:[1,0]\n" \
"v_pk_mul_f32 v[194:195], v[194:195], v[214:215] op_sel_hi:[1,0]\n" \
"v_pk_mul_f32 v[196:197], v[196:197], v[214:215] op_sel_hi:[1,0]\n" \
"v_pk_mul_f32 v[198:199], v[198:199], v[214:215] op_sel_hi:[1,0]\n" \
"v_cvt_pk_bf16_f32 v200, v192, v193\n" \
"v_cvt_pk_bf16_f32 v201, v194, v195\n" \
"v_cvt_pk_bf16_f32 v202, v196, v197\n" \
"v_cvt_pk_bf16_f32 v203, v198, v199\n" \
"v_pk_mul_f32 v[192:193], v[192:193], v[194:195]\n" \
"v_pk_fma_f32 v[192:193], v[196:197], v[198:199], v[192:193]\n" \
"v_mfma_f32_16x16x32_bf16 a[84:87], a[232:235], v[0:3], a[84:87]\n" \
"v_exp_f32 v86, v86\n" \
"v_mfma_f32_16x16x32_bf16 a[84:87], a[236:239], v[32:35], a[84:87]\n" \
"ds_read_b128 a[232:235], v246 offset:12288\n" \
"v_cvt_pk_bf16_f32 v82, v84, v85\n" \
"v_fma_f32 v216, v192, v193, v216\n" \
"v_mfma_f32_16x16x32_bf16 a[88:91], a[240:243], v[0:3], a[88:91]\n" \
"v_exp_f32 v87, v87\n" \
"v_mfma_f32_16x16x32_bf16 a[88:91], a[244:247], v[32:35], a[88:91]\n" \
"buffer_store_dwordx4 v[200:203], v215, s[56:59], s77 offen offset:0 sc1\n" \
"v_exp_f32 v88, v88\n" \
"v_cvt_pk_bf16_f32 v83, v86, v87\n" \
"v_accvgpr_read_b32 v192, a56\n" \
"v_mfma_f32_16x16x32_bf16 a[120:123], a[240:243], v[8:11], a[120:123]\n" \
"v_exp_f32 v89, v89\n" \
"v_mfma_f32_16x16x32_bf16 a[120:123], a[244:247], v[40:43], a[120:123]\n" \
"ds_read_b128 a[236:239], v246 offset:14336\n" \
"v_accvgpr_read_b32 v193, a57\n" \
"v_mfma_f32_16x16x32_bf16 v[152:155], a[240:243], v[16:19], v[152:155]\n" \
"v_exp_f32 v90, v90\n" \
"v_mfma_f32_16x16x32_bf16 v[152:155], a[244:247], v[48:51], v[152:155]\n" \
"v_accvgpr_read_b32 v194, a58\n" \
"v_cvt_pk_bf16_f32 v88, v88, v89\n" \
"v_mfma_f32_16x16x32_bf16 v[242:245], a[0:3], v[80:83], v[242:245]\n" \
"v_exp_f32 v91, v91\n" \
"v_mfma_f32_16x16x32_bf16 v[184:187], a[240:243], v[24:27], v[184:187]\n" \
"v_exp_f32 v92, v92\n" \
"v_mfma_f32_16x16x32_bf16 v[184:187], a[244:247], v[56:59], v[184:187]\n" \
"ds_read_b128 a[240:243], v246 offset:8448\n" \
"v_cvt_pk_bf16_f32 v89, v90, v91\n" \
"v_mfma_f32_16x16x32_bf16 v[188:191], a[248:251], v[24:27], v[188:191]\n" \
"v_exp_f32 v93, v93\n" \
"v_mfma_f32_16x16x32_bf16 v[188:191], a[252:255], v[56:59], v[188:191]\n" \
"v_exp_f32 v94, v94\n" \
"v_mfma_f32_16x16x32_bf16 v[156:159], a[248:251], v[16:19], v[156:159]\n" \
"v_cvt_pk_bf16_f32 v90, v92, v93\n" \
"v_accvgpr_read_b32 v195, a59\n" \
"v_mfma_f32_16x16x32_bf16 v[156:159], a[252:255], v[48:51], v[156:159]\n" \
"ds_read_b128 a[244:247], v246 offset:10496\n" \
"v_accvgpr_read_b32 v196, a60\n" \
"v_accvgpr_read_b32 v197, a61\n" \
"v_accvgpr_read_b32 v198, a62\n" \
"v_accvgpr_read_b32 v199, a63\n" \
"v_pk_mul_f32 v[192:193], v[192:193], v[214:215] op_sel_hi:[1,0]\n" \
"v_pk_mul_f32 v[194:195], v[194:195], v[214:215] op_sel_hi:[1,0]\n" \
"v_pk_mul_f32 v[196:197], v[196:197], v[214:215] op_sel_hi:[1,0]\n" \
"v_pk_mul_f32 v[198:199], v[198:199], v[214:215] op_sel_hi:[1,0]\n" \
"v_cvt_pk_bf16_f32 v204, v192, v193\n" \
"v_cvt_pk_bf16_f32 v205, v194, v195\n" \
"v_cvt_pk_bf16_f32 v206, v196, v197\n" \
"v_cvt_pk_bf16_f32 v207, v198, v199\n" \
"v_pk_mul_f32 v[192:193], v[192:193], v[194:195]\n" \
"v_pk_fma_f32 v[192:193], v[196:197], v[198:199], v[192:193]\n" \
"v_fma_f32 v216, v192, v193, v216\n" \
"v_mfma_f32_16x16x32_bf16 a[124:127], a[248:251], v[8:11], a[124:127]\n" \
"buffer_store_dwordx4 v[204:207], v215, s[56:59], s77 offen offset:2048 sc1\n" \
"v_exp_f32 v95, v95\n" \
"v_cmp_class_f32 vcc, v216, s94\n" \
"s_sub_u32 s77, s77, 536576\n" \
"v_mfma_f32_16x16x32_bf16 a[124:127], a[252:255], v[40:43], a[124:127]\n" \
"v_cvt_pk_bf16_f32 v91, v94, v95\n" \
"s_or_b64 s[92:93], s[92:93], vcc\n" \
"v_mfma_f32_16x16x32_bf16 a[92:95], a[248:251], v[0:3], a[92:95]\n" \
"s_waitcnt vmcnt(13) lgkmcnt(6)\n" \
"v_mfma_f32_16x16x32_bf16 v[242:245], a[4:7], v[88:91], v[242:245]\n" \
"v_mfma_f32_16x16x32_bf16 a[92:95], a[252:255], v[32:35], a[92:95]\n" \
"s_barrier\n" \
"ds_read_b128 a[248:251], v246 offset:12544\n" \
"v_mfma_f32_16x16x32_bf16 v[24:27], a[192:195], a[176:179], v[236:239]\n" \
"ds_read_b128 v[192:195], v246 offset:16384\n" \
"v_bfe_u32 v69, v248, 8, 1\n" \
"v_mfma_f32_16x16x32_bf16 v[24:27], a[196:199], a[180:183], v[24:27]\n" \
"ds_read_b128 v[196:199], v246 offset:18432\n" \
"Lpg8014_%=:\n" \
"Lst1x1_%=:\n" \
"s_cbranch_execz Lpg10309_%=\n" \
"v_cvt_pk_bf16_f32 v91, v94, v95\n" \
"v_mfma_f32_16x16x32_bf16 v[24:27], a[200:203], a[184:187], v[24:27]\n" \
"ds_read_b128 v[200:203], v246 offset:20480\n" \
"v_bfe_u32 v70, v248, 6, 2\n" \
"v_mfma_f32_16x16x32_bf16 v[24:27], a[204:207], a[188:191], v[24:27]\n" \
"ds_read_b128 a[252:255], v246 offset:14592\n" \
"v_lshl_add_u32 v70, v69, 3, v70\n" \
"v_exp_f32 v96, v96\n" \
"v_exp_f32 v97, v97\n" \
"v_exp_f32 v98, v98\n" \
"v_cvt_pk_bf16_f32 v96, v96, v97\n" \
"v_exp_f32 v99, v99\n" \
"v_exp_f32 v100, v100\n" \
"v_mfma_f32_16x16x32_bf16 v[16:19], a[200:203], a[168:171], v[232:235]\n" \
"ds_read_b128 v[204:207], v246 offset:22528\n" \
"v_mul_u32_u24 v70, 6144, v70\n" \
"v_mfma_f32_16x16x32_bf16 v[16:19], a[204:207], a[172:175], v[16:19]\n" \
"ds_read_b128 v[208:211], v246 offset:16640\n" \
"v_lshlrev_b32 v69, 5, v69\n" \
"v_mfma_f32_16x16x32_bf16 v[16:19], a[192:195], a[160:163], v[16:19]\n" \
"ds_read_b128 v[212:215], v246 offset:18688\n" \
"v_and_b32 v71, 0x30, v248\n" \
"v_mfma_f32_16x16x32_bf16 v[16:19], a[196:199], a[164:167], v[16:19]\n" \
"ds_read_b128 v[216:219], v246 offset:20736\n" \
"v_xor_b32 v69, v69, v71\n" \
"v_mfma_f32_16x16x32_bf16 v[20:23], a[208:211], a[160:163], v[232:235]\n" \
"ds_read_b128 v[220:223], v246 offset:22784\n" \
"v_lshrrev_b32 v71, 9, v248\n" \
"v_mfma_f32_16x16x32_bf16 v[20:23], a[212:215], a[164:167], v[20:23]\n" \
"ds_read_b128 a[192:195], v247 offset:32768\n" \
"v_lshl_add_u32 v70, v71, 6, v70\n" \
"v_mfma_f32_16x16x32_bf16 v[20:23], a[216:219], a[168:171], v[20:23]\n" \
"ds_read_b128 a[196:199], v247 offset:40960\n" \
"v_add_u32 v253, v69, v70\n" \
"v_mfma_f32_16x16x32_bf16 v[20:23], a[220:223], a[172:175], v[20:23]\n" \
"s_add_u32 m0, s84, 143360\n" \
"ds_read_b128 a[200:203], v247 offset:33792\n" \
"v_cvt_pk_bf16_f32 v97, v98, v99\n" \
"v_mfma_f32_16x16x32_bf16 v[28:31], a[216:219], a[184:187], v[236:239]\n" \
"ds_read_b128 a[204:207], v247 offset:41984\n" \
"global_load_lds_dwordx4 v253, s[48:49] offset:-4096\n" \
"v_mfma_f32_16x16x32_bf16 v[28:31], a[220:223], a[188:191], v[28:31]\n" \
"v_exp_f32 v101, v101\n" \
"v_mfma_f32_16x16x32_bf16 v[28:31], a[208:211], a[176:179], v[28:31]\n" \
"v_exp_f32 v102, v102\n" \
"v_mfma_f32_16x16x32_bf16 v[28:31], a[212:215], a[180:183], v[28:31]\n" \
"s_waitcnt lgkmcnt(2)\n" \
"ds_read_b128 a[208:211], v247 offset:34816\n" \
"global_load_lds_dwordx4 v253, s[48:49] offset:-2048\n" \
"v_mfma_f32_16x16x32_bf16 v[8:11], v[192:195], a[176:179], v[236:239]\n" \
"ds_read_b128 a[212:215], v247 offset:43008\n" \
"v_cvt_pk_bf16_f32 v98, v100, v101\n" \
"v_mfma_f32_16x16x32_bf16 v[8:11], v[196:199], a[180:183], v[8:11]\n" \
"ds_read_b128 a[216:219], v247 offset:35840\n" \
"v_exp_f32 v103, v103\n" \
"v_exp_f32 v104, v104\n" \
"v_mfma_f32_16x16x32_bf16 v[8:11], v[200:203], a[184:187], v[8:11]\n" \
"ds_read_b128 a[220:223], v247 offset:44032\n" \
"global_load_lds_dwordx4 v253, s[48:49] offset:0\n" \
"v_mfma_f32_16x16x32_bf16 v[8:11], v[204:207], a[188:191], v[8:11]\n" \
"s_add_u32 s48, s48, 0x120000\n" \
"s_addc_u32 s49, s49, 0\n" \
"ds_read_b128 v[192:195], v246 offset:24576\n" \
"v_mfma_f32_16x16x32_bf16 v[56:59], a[232:235], a[184:187], v[236:239]\n" \
"ds_read_b128 v[196:199], v246 offset:26624\n" \
"v_cvt_pk_bf16_f32 v99, v102, v103\n" \
"v_mfma_f32_16x16x32_bf16 v[56:59], a[236:239], a[188:191], v[56:59]\n" \
"ds_read_b128 v[200:203], v246 offset:28672\n" \
"global_load_lds_dwordx4 v253, s[50:51] offset:-3072\n" \
"v_mfma_f32_16x16x32_bf16 v[56:59], a[224:227], a[176:179], v[56:59]\n" \
"ds_read_b128 v[204:207], v246 offset:30720\n" \
"global_load_lds_dwordx4 v253, s[50:51] offset:-1024\n" \
"v_mfma_f32_16x16x32_bf16 v[56:59], a[228:231], a[180:183], v[56:59]\n" \
"v_exp_f32 v105, v105\n" \
"v_mfma_f32_16x16x32_bf16 v[48:51], a[224:227], a[160:163], v[232:235]\n" \
"v_exp_f32 v106, v106\n" \
"v_cvt_pk_bf16_f32 v104, v104, v105\n" \
"v_mfma_f32_16x16x32_bf16 v[48:51], a[228:231], a[164:167], v[48:51]\n" \
"v_exp_f32 v107, v107\n" \
"v_mfma_f32_16x16x32_bf16 v[48:51], a[232:235], a[168:171], v[48:51]\n" \
"v_exp_f32 v108, v108\n" \
"v_mfma_f32_16x16x32_bf16 v[48:51], a[236:239], a[172:175], v[48:51]\n" \
"ds_read_b128 a[224:227], v247 offset:36864\n" \
"global_load_lds_dwordx4 v253, s[50:51] offset:1024\n" \
"v_mfma_f32_16x16x32_bf16 v[12:15], v[216:219], a[184:187], v[236:239]\n" \
"s_add_u32 s50, s50, 0x120000\n" \
"s_addc_u32 s51, s51, 0\n" \
"ds_read_b128 a[228:231], v247 offset:45056\n" \
"v_mfma_f32_16x16x32_bf16 v[12:15], v[220:223], a[188:191], v[12:15]\n" \
"ds_read_b128 a[232:235], v247 offset:37888\n" \
"v_cvt_pk_bf16_f32 v105, v106, v107\n" \
"v_mfma_f32_16x16x32_bf16 v[12:15], v[208:211], a[176:179], v[12:15]\n" \
"ds_read_b128 a[236:239], v247 offset:46080\n" \
"v_exp_f32 v109, v109\n" \
"v_mfma_f32_16x16x32_bf16 v[12:15], v[212:215], a[180:183], v[12:15]\n" \
"ds_read_b128 v[208:211], v246 offset:24832\n" \
"v_exp_f32 v110, v110\n" \
"v_mfma_f32_16x16x32_bf16 v[60:63], a[240:243], a[176:179], v[236:239]\n" \
"ds_read_b128 v[212:215], v246 offset:26880\n" \
"v_cvt_pk_bf16_f32 v106, v108, v109\n" \
"v_mfma_f32_16x16x32_bf16 v[60:63], a[244:247], a[180:183], v[60:63]\n" \
"ds_read_b128 v[216:219], v246 offset:28928\n" \
"v_exp_f32 v111, v111\n" \
"v_exp_f32 v112, v112\n" \
"v_mfma_f32_16x16x32_bf16 v[60:63], a[248:251], a[184:187], v[60:63]\n" \
"ds_read_b128 v[220:223], v246 offset:30976\n" \
"v_cvt_pk_bf16_f32 v107, v110, v111\n" \
"v_mfma_f32_16x16x32_bf16 v[60:63], a[252:255], a[188:191], v[60:63]\n" \
"v_exp_f32 v113, v113\n" \
"v_exp_f32 v114, v114\n" \
"v_cvt_pk_bf16_f32 v112, v112, v113\n" \
"v_mfma_f32_16x16x32_bf16 v[52:55], a[248:251], a[168:171], v[232:235]\n" \
"v_exp_f32 v115, v115\n" \
"v_mfma_f32_16x16x32_bf16 v[52:55], a[252:255], a[172:175], v[52:55]\n" \
"v_exp_f32 v116, v116\n" \
"v_cvt_pk_bf16_f32 v113, v114, v115\n" \
"v_mfma_f32_16x16x32_bf16 v[52:55], a[240:243], a[160:163], v[52:55]\n" \
"v_exp_f32 v117, v117\n" \
"v_mfma_f32_16x16x32_bf16 v[52:55], a[244:247], a[164:167], v[52:55]\n" \
"s_waitcnt lgkmcnt(0)\n" \
"ds_read_b128 a[240:243], v247 offset:38912\n" \
"v_exp_f32 v118, v118\n" \
"v_mfma_f32_16x16x32_bf16 v[40:43], v[192:195], a[176:179], v[236:239]\n" \
"ds_read_b128 a[244:247], v247 offset:47104\n" \
"v_cvt_pk_bf16_f32 v114, v116, v117\n" \
"v_mfma_f32_16x16x32_bf16 v[40:43], v[196:199], a[180:183], v[40:43]\n" \
"ds_read_b128 a[248:251], v247 offset:39936\n" \
"v_exp_f32 v119, v119\n" \
"v_exp_f32 v120, v120\n" \
"v_mfma_f32_16x16x32_bf16 v[40:43], v[200:203], a[184:187], v[40:43]\n" \
"ds_read_b128 a[252:255], v247 offset:48128\n" \
"v_cvt_pk_bf16_f32 v115, v118, v119\n" \
"v_mfma_f32_16x16x32_bf16 v[40:43], v[204:207], a[188:191], v[40:43]\n" \
"v_exp_f32 v121, v121\n" \
"v_exp_f32 v122, v122\n" \
"v_cvt_pk_bf16_f32 v120, v120, v121\n" \
"v_mfma_f32_16x16x32_bf16 v[44:47], v[216:219], a[184:187], v[236:239]\n" \
"v_exp_f32 v123, v123\n" \
"v_mfma_f32_16x16x32_bf16 v[44:47], v[220:223], a[188:191], v[44:47]\n" \
"s_waitcnt vmcnt(11) lgkmcnt(0)\n" \
"v_exp_f32 v124, v124\n" \
"v_mfma_f32_16x16x32_bf16 v[44:47], v[208:211], a[176:179], v[44:47]\n" \
"v_add_u32 v247, 0x10000, v248\n" \
"v_cvt_pk_bf16_f32 v121, v122, v123\n" \
"v_mfma_f32_16x16x32_bf16 v[44:47], v[212:215], a[180:183], v[44:47]\n" \
"s_barrier\n" \
"v_exp_f32 v125, v125\n" \
"v_exp_f32 v126, v126\n" \
"v_cvt_pk_bf16_f32 v122, v124, v125\n" \
"v_mfma_f32_16x16x32_bf16 a[64:67], a[192:195], v[64:67], a[64:67]\n" \
"ds_read_b128 v[192:195], v247 offset:16384\n" \
"v_exp_f32 v127, v127\n" \
"v_mfma_f32_16x16x32_bf16 a[64:67], a[196:199], v[96:99], a[64:67]\n" \
"v_cvt_pk_bf16_f32 v123, v126, v127\n" \
"v_mfma_f32_16x16x32_bf16 a[96:99], a[192:195], v[72:75], a[96:99]\n" \
"s_lshl_b32 s70, s61, 14\n" \
"v_add_u32 v124, s70, v248\n" \
"v_xor_b32 v125, 0x4000, v124\n" \
"v_mfma_f32_16x16x32_bf16 a[96:99], a[196:199], v[104:107], a[96:99]\n" \
"s_lshl_b32 s70, s61, 9\n" \
"v_cmp_gt_i32 vcc, 0, v251\n" \
"v_cmp_gt_i32 s[64:65], 1, v251\n" \
"v_cmp_gt_i32 s[66:67], 2, v251\n" \
"v_cndmask_b32 v8, v8, v254, vcc\n" \
"v_mfma_f32_16x16x32_bf16 v[238:241], a[0:3], v[96:99], v[238:241]\n" \
"v_cndmask_b32 v16, v16, v254, vcc\n" \
"v_cmp_gt_i32 vcc, 3, v251\n" \
"v_mfma_f32_16x16x32_bf16 v[238:241], a[4:7], v[104:107], v[238:241]\n" \
"v_cndmask_b32 v9, v9, v254, s[64:65]\n" \
"v_cndmask_b32 v17, v17, v254, s[64:65]\n" \
"v_mfma_f32_16x16x32_bf16 v[128:131], a[192:195], v[80:83], v[128:131]\n" \
"s_add_u32 s70, s70, 110592\n" \
"ds_read_b128 v[196:199], v247 offset:24576\n" \
"v_lshrrev_b32 v126, 1, v248\n" \
"v_mfma_f32_16x16x32_bf16 v[128:131], a[196:199], v[112:115], v[128:131]\n" \
"v_add_u32 v126, s70, v126\n" \
"v_xor_b32 v127, 0x200, v126\n" \
"v_cmp_gt_i32 s[64:65], 16, v251\n" \
"v_cndmask_b32 v10, v10, v254, s[66:67]\n" \
"v_cndmask_b32 v18, v18, v254, s[66:67]\n" \
"v_cmp_gt_i32 s[66:67], 17, v251\n" \
"v_cndmask_b32 v11, v11, v254, vcc\n" \
"v_cndmask_b32 v19, v19, v254, vcc\n" \
"v_mfma_f32_16x16x32_bf16 v[160:163], a[192:195], v[88:91], v[160:163]\n" \
"v_lshrrev_b32 v71, 7, v248\n" \
"v_and_b32 v76, 0x70, v248\n" \
"v_mfma_f32_16x16x32_bf16 v[160:163], a[196:199], v[120:123], v[160:163]\n" \
"ds_read_b128 a[192:195], v247 offset:0\n" \
"v_lshlrev_b32 v77, 4, v71\n" \
"v_cmp_gt_i32 vcc, 18, v251\n" \
"v_cndmask_b32 v12, v12, v254, s[64:65]\n" \
"v_mfma_f32_16x16x32_bf16 v[242:245], a[0:3], v[112:115], v[242:245]\n" \
"v_cndmask_b32 v20, v20, v254, s[64:65]\n" \
"v_cmp_gt_i32 s[64:65], 19, v251\n" \
"v_mfma_f32_16x16x32_bf16 v[242:245], a[4:7], v[120:123], v[242:245]\n" \
"v_cndmask_b32 v13, v13, v254, s[66:67]\n" \
"v_cndmask_b32 v21, v21, v254, s[66:67]\n" \
"v_cmp_gt_i32 s[66:67], 32, v251\n" \
"v_cndmask_b32 v14, v14, v254, vcc\n" \
"v_mfma_f32_16x16x32_bf16 v[164:167], a[200:203], v[88:91], v[164:167]\n" \
"ds_read_b128 v[200:203], v247 offset:17408\n" \
"v_xor_b32 v77, v77, v76\n" \
"v_mfma_f32_16x16x32_bf16 v[164:167], a[204:207], v[120:123], v[164:167]\n" \
"ds_write_b128 v124, a[64:67] offset:0\n" \
"v_lshl_add_u32 v252, v71, 12, v77\n" \
"v_cndmask_b32 v22, v22, v254, vcc\n" \
"v_cmp_gt_i32 vcc, 33, v251\n" \
"v_cndmask_b32 v15, v15, v254, s[64:65]\n" \
"v_cndmask_b32 v23, v23, v254, s[64:65]\n" \
"v_mfma_f32_16x16x32_bf16 v[132:135], a[200:203], v[80:83], v[132:135]\n" \
"s_add_u32 m0, s95, 118784\n" \
"v_cmp_gt_i32 s[64:65], 34, v251\n" \
"v_cndmask_b32 v40, v40, v254, s[66:67]\n" \
"v_mfma_f32_16x16x32_bf16 v[132:135], a[204:207], v[112:115], v[132:135]\n" \
"ds_read_b128 a[196:199], v247 offset:8192\n" \
"global_load_lds_dwordx4 v252, s[42:43] offset:0\n" \
"v_mfma_f32_16x16x32_bf16 a[100:103], a[200:203], v[72:75], a[100:103]\n" \
"ds_read_b128 v[204:207], v247 offset:25600\n" \
"v_cndmask_b32 v48, v48, v254, s[66:67]\n" \
"v_mfma_f32_16x16x32_bf16 a[100:103], a[204:207], v[104:107], a[100:103]\n" \
"ds_write_b128 v124, a[96:99] offset:8192\n" \
"global_load_lds_dwordx4 v252, s[42:43] offset:2048\n" \
"v_mfma_f32_16x16x32_bf16 a[68:71], a[200:203], v[64:67], a[68:71]\n" \
"v_cmp_gt_i32 s[66:67], 35, v251\n" \
"v_cndmask_b32 v41, v41, v254, vcc\n" \
"v_mfma_f32_16x16x32_bf16 a[68:71], a[204:207], v[96:99], a[68:71]\n" \
"s_add_u32 s42, s42, 0x180\n" \
"s_addc_u32 s43, s43, 0\n" \
"ds_read_b128 a[200:203], v247 offset:1024\n" \
"v_mfma_f32_16x16x32_bf16 a[72:75], a[208:211], v[64:67], a[72:75]\n" \
"ds_read_b128 v[208:211], v247 offset:18432\n" \
"v_cndmask_b32 v49, v49, v254, vcc\n" \
"v_mfma_f32_16x16x32_bf16 a[72:75], a[212:215], v[96:99], a[72:75]\n" \
"ds_write_b128 v124, a[100:103] offset:9216\n" \
"global_load_lds_dwordx4 v252, s[44:45] offset:1024\n" \
"v_mfma_f32_16x16x32_bf16 a[104:107], a[208:211], v[72:75], a[104:107]\n" \
"v_cmp_gt_i32 vcc, 48, v251\n" \
"v_cndmask_b32 v42, v42, v254, s[64:65]\n" \
"v_mfma_f32_16x16x32_bf16 a[104:107], a[212:215], v[104:107], a[104:107]\n" \
"ds_read_b128 a[204:207], v247 offset:9216\n" \
"ds_write_b64 v126, v[240:241]\n" \
"v_mfma_f32_16x16x32_bf16 v[136:139], a[208:211], v[80:83], v[136:139]\n" \
"ds_read_b128 v[212:215], v247 offset:26624\n" \
"global_load_lds_dwordx4 v252, s[44:45] offset:3072\n" \
"v_mfma_f32_16x16x32_bf16 v[136:139], a[212:215], v[112:115], v[136:139]\n" \
"ds_write_b128 v124, a[68:71] offset:1024\n" \
"v_cndmask_b32 v50, v50, v254, s[64:65]\n" \
"v_mfma_f32_16x16x32_bf16 v[168:171], a[208:211], v[88:91], v[168:171]\n" \
"s_add_u32 s44, s44, 0x180\n" \
"s_addc_u32 s45, s45, 0\n" \
"v_cmp_gt_i32 s[64:65], 49, v251\n" \
"v_mfma_f32_16x16x32_bf16 v[168:171], a[212:215], v[120:123], v[168:171]\n" \
"ds_read_b128 a[208:211], v247 offset:2048\n" \
"v_cndmask_b32 v43, v43, v254, s[66:67]\n" \
"v_mfma_f32_16x16x32_bf16 v[172:175], a[216:219], v[88:91], v[172:175]\n" \
"ds_read_b128 v[216:219], v247 offset:19456\n" \
"v_cndmask_b32 v51, v51, v254, s[66:67]\n" \
"v_mfma_f32_16x16x32_bf16 v[172:175], a[220:223], v[120:123], v[172:175]\n" \
"ds_write_b128 v124, a[72:75] offset:2048\n" \
"v_cmp_gt_i32 s[66:67], 50, v251\n" \
"v_mfma_f32_16x16x32_bf16 v[140:143], a[216:219], v[80:83], v[140:143]\n" \
"v_cndmask_b32 v44, v44, v254, vcc\n" \
"v_cndmask_b32 v52, v52, v254, vcc\n" \
"v_mfma_f32_16x16x32_bf16 v[140:143], a[220:223], v[112:115], v[140:143]\n" \
"ds_read_b128 a[212:215], v247 offset:10240\n" \
"v_cmp_gt_i32 vcc, 51, v251\n" \
"v_mfma_f32_16x16x32_bf16 a[108:111], a[216:219], v[72:75], a[108:111]\n" \
"ds_read_b128 v[220:223], v247 offset:27648\n" \
"v_cndmask_b32 v45, v45, v254, s[64:65]\n" \
"v_mfma_f32_16x16x32_bf16 a[108:111], a[220:223], v[104:107], a[108:111]\n" \
"ds_write_b128 v124, a[104:107] offset:10240\n" \
"v_cndmask_b32 v53, v53, v254, s[64:65]\n" \
"v_mfma_f32_16x16x32_bf16 a[76:79], a[216:219], v[64:67], a[76:79]\n" \
"v_cndmask_b32 v46, v46, v254, s[66:67]\n" \
"v_cndmask_b32 v54, v54, v254, s[66:67]\n" \
"v_mfma_f32_16x16x32_bf16 a[76:79], a[220:223], v[96:99], a[76:79]\n" \
"ds_read_b128 a[216:219], v247 offset:3072\n" \
"v_cndmask_b32 v47, v47, v254, vcc\n" \
"v_mfma_f32_16x16x32_bf16 a[80:83], a[224:227], v[64:67], a[80:83]\n" \
"ds_read_b128 v[224:227], v247 offset:20480\n" \
"v_cndmask_b32 v55, v55, v254, vcc\n" \
"v_mfma_f32_16x16x32_bf16 a[80:83], a[228:231], v[96:99], a[80:83]\n" \
"ds_write_b128 v124, a[108:111] offset:11264\n" \
"v_exp_f32 v8, v8\n" \
"v_exp_f32 v9, v9\n" \
"v_exp_f32 v10, v10\n" \
"v_cvt_pk_bf16_f32 v8, v8, v9\n" \
"v_mfma_f32_16x16x32_bf16 a[112:115], a[224:227], v[72:75], a[112:115]\n" \
"v_exp_f32 v11, v11\n" \
"v_mfma_f32_16x16x32_bf16 a[112:115], a[228:231], v[104:107], a[112:115]\n" \
"ds_read_b128 a[220:223], v247 offset:11264\n" \
"v_exp_f32 v12, v12\n" \
"v_mfma_f32_16x16x32_bf16 v[144:147], a[224:227], v[80:83], v[144:147]\n" \
"ds_read_b128 v[228:231], v247 offset:28672\n" \
"v_cvt_pk_bf16_f32 v9, v10, v11\n" \
"v_mfma_f32_16x16x32_bf16 v[144:147], a[228:231], v[112:115], v[144:147]\n" \
"ds_write_b128 v124, a[76:79] offset:3072\n" \
"v_exp_f32 v13, v13\n" \
"v_exp_f32 v14, v14\n" \
"v_cvt_pk_bf16_f32 v10, v12, v13\n" \
"v_mfma_f32_16x16x32_bf16 v[176:179], a[224:227], v[88:91], v[176:179]\n" \
"v_exp_f32 v15, v15\n" \
"v_mfma_f32_16x16x32_bf16 v[176:179], a[228:231], v[120:123], v[176:179]\n" \
"ds_read_b128 a[224:227], v247 offset:4096\n" \
"v_exp_f32 v40, v40\n" \
"v_mfma_f32_16x16x32_bf16 v[180:183], a[232:235], v[88:91], v[180:183]\n" \
"ds_read_b128 v[232:235], v247 offset:21504\n" \
"v_cvt_pk_bf16_f32 v11, v14, v15\n" \
"v_mfma_f32_16x16x32_bf16 v[180:183], a[236:239], v[120:123], v[180:183]\n" \
"ds_write_b128 v124, a[80:83] offset:4096\n" \
"v_exp_f32 v41, v41\n" \
"v_exp_f32 v42, v42\n" \
"v_cvt_pk_bf16_f32 v40, v40, v41\n" \
"v_mfma_f32_16x16x32_bf16 v[148:151], a[232:235], v[80:83], v[148:151]\n" \
"v_exp_f32 v43, v43\n" \
"v_mfma_f32_16x16x32_bf16 v[148:151], a[236:239], v[112:115], v[148:151]\n" \
"ds_read_b128 a[228:231], v247 offset:12288\n" \
"v_exp_f32 v44, v44\n" \
"v_mfma_f32_16x16x32_bf16 a[116:119], a[232:235], v[72:75], a[116:119]\n" \
"ds_read_b128 v[236:239], v247 offset:29696\n" \
"v_cvt_pk_bf16_f32 v41, v42, v43\n" \
"v_mfma_f32_16x16x32_bf16 a[116:119], a[236:239], v[104:107], a[116:119]\n" \
"ds_write_b128 v124, a[112:115] offset:12288\n" \
"v_exp_f32 v45, v45\n" \
"v_exp_f32 v46, v46\n" \
"v_cvt_pk_bf16_f32 v42, v44, v45\n" \
"v_mfma_f32_16x16x32_bf16 a[84:87], a[232:235], v[64:67], a[84:87]\n" \
"v_exp_f32 v47, v47\n" \
"v_mfma_f32_16x16x32_bf16 a[84:87], a[236:239], v[96:99], a[84:87]\n" \
"ds_read_b128 a[232:235], v247 offset:5120\n" \
"v_exp_f32 v24, v24\n" \
"v_mfma_f32_16x16x32_bf16 a[88:91], a[240:243], v[64:67], a[88:91]\n" \
"ds_read_b128 v[0:3], v247 offset:22528\n" \
"v_cvt_pk_bf16_f32 v43, v46, v47\n" \
"v_mfma_f32_16x16x32_bf16 a[88:91], a[244:247], v[96:99], a[88:91]\n" \
"ds_write_b128 v124, a[116:119] offset:13312\n" \
"v_exp_f32 v25, v25\n" \
"v_exp_f32 v26, v26\n" \
"v_cvt_pk_bf16_f32 v24, v24, v25\n" \
"v_mfma_f32_16x16x32_bf16 v[242:245], a[4:7], v[8:11], v[242:245]\n" \
"v_exp_f32 v27, v27\n" \
"v_mfma_f32_16x16x32_bf16 v[242:245], a[4:7], v[40:43], v[242:245]\n" \
"v_exp_f32 v28, v28\n" \
"v_cvt_pk_bf16_f32 v25, v26, v27\n" \
"v_mfma_f32_16x16x32_bf16 a[120:123], a[240:243], v[72:75], a[120:123]\n" \
"v_exp_f32 v29, v29\n" \
"v_mfma_f32_16x16x32_bf16 a[120:123], a[244:247], v[104:107], a[120:123]\n" \
"ds_read_b128 a[236:239], v247 offset:13312\n" \
"v_exp_f32 v30, v30\n" \
"v_mfma_f32_16x16x32_bf16 v[152:155], a[240:243], v[80:83], v[152:155]\n" \
"ds_read_b128 v[4:7], v247 offset:30720\n" \
"v_cvt_pk_bf16_f32 v26, v28, v29\n" \
"v_mfma_f32_16x16x32_bf16 v[152:155], a[244:247], v[112:115], v[152:155]\n" \
"ds_write_b128 v124, a[84:87] offset:5120\n" \
"v_exp_f32 v31, v31\n" \
"v_exp_f32 v56, v56\n" \
"v_cvt_pk_bf16_f32 v27, v30, v31\n" \
"v_mfma_f32_16x16x32_bf16 v[184:187], a[240:243], v[88:91], v[184:187]\n" \
"v_exp_f32 v57, v57\n" \
"v_mfma_f32_16x16x32_bf16 v[184:187], a[244:247], v[120:123], v[184:187]\n" \
"ds_read_b128 a[240:243], v247 offset:6144\n" \
"v_exp_f32 v58, v58\n" \
"v_mfma_f32_16x16x32_bf16 a[92:95], a[248:251], v[64:67], a[92:95]\n" \
"ds_read_b128 v[32:35], v247 offset:23552\n" \
"v_cvt_pk_bf16_f32 v56, v56, v57\n" \
"v_mfma_f32_16x16x32_bf16 a[92:95], a[252:255], v[96:99], a[92:95]\n" \
"ds_write_b128 v124, a[88:91] offset:6144\n" \
"v_exp_f32 v59, v59\n" \
"v_exp_f32 v60, v60\n" \
"v_cvt_pk_bf16_f32 v57, v58, v59\n" \
"v_mfma_f32_16x16x32_bf16 a[124:127], a[248:251], v[72:75], a[124:127]\n" \
"v_exp_f32 v61, v61\n" \
"v_mfma_f32_16x16x32_bf16 a[124:127], a[252:255], v[104:107], a[124:127]\n" \
"ds_read_b128 a[244:247], v247 offset:14336\n" \
"v_exp_f32 v62, v62\n" \
"v_mfma_f32_16x16x32_bf16 v[156:159], a[248:251], v[80:83], v[156:159]\n" \
"ds_read_b128 v[36:39], v247 offset:31744\n" \
"v_cvt_pk_bf16_f32 v58, v60, v61\n" \
"v_mfma_f32_16x16x32_bf16 v[156:159], a[252:255], v[112:115], v[156:159]\n" \
"ds_write_b128 v124, a[120:123] offset:14336\n" \
"v_exp_f32 v63, v63\n" \
"v_mfma_f32_16x16x32_bf16 v[188:191], a[248:251], v[88:91], v[188:191]\n" \
"v_exp_f32 v16, v16\n" \
"v_mfma_f32_16x16x32_bf16 v[188:191], a[252:255], v[120:123], v[188:191]\n" \
"s_waitcnt lgkmcnt(3)\n" \
"ds_read_b128 a[248:251], v247 offset:7168\n" \
"ds_write_b128 v124, a[92:95] offset:7168\n" \
"v_cvt_pk_bf16_f32 v59, v62, v63\n" \
"v_exp_f32 v17, v17\n" \
"v_mfma_f32_16x16x32_bf16 v[160:163], v[192:195], v[8:11], v[160:163]\n" \
"v_exp_f32 v18, v18\n" \
"v_mfma_f32_16x16x32_bf16 v[160:163], v[196:199], v[40:43], v[160:163]\n" \
"ds_read_b128 a[252:255], v247 offset:15360\n" \
"v_cvt_pk_bf16_f32 v16, v16, v17\n" \
"v_mfma_f32_16x16x32_bf16 v[242:245], a[4:7], v[24:27], v[242:245]\n" \
"v_exp_f32 v19, v19\n" \
"v_mfma_f32_16x16x32_bf16 v[242:245], a[4:7], v[56:59], v[242:245]\n" \
"v_exp_f32 v20, v20\n" \
"v_cvt_pk_bf16_f32 v17, v18, v19\n" \
"v_exp_f32 v21, v21\n" \
"v_mfma_f32_16x16x32_bf16 v[164:167], v[200:203], v[8:11], v[164:167]\n" \
"v_exp_f32 v22, v22\n" \
"v_mfma_f32_16x16x32_bf16 v[164:167], v[204:207], v[40:43], v[164:167]\n" \
"ds_write_b128 v124, a[124:127] offset:15360\n" \
"v_cvt_pk_bf16_f32 v18, v20, v21\n" \
"v_exp_f32 v23, v23\n" \
"v_mfma_f32_16x16x32_bf16 v[168:171], v[208:211], v[8:11], v[168:171]\n" \
"v_exp_f32 v48, v48\n" \
"v_mfma_f32_16x16x32_bf16 v[168:171], v[212:215], v[40:43], v[168:171]\n" \
"s_waitcnt vmcnt(0) lgkmcnt(0)\n" \
"v_cvt_pk_bf16_f32 v19, v22, v23\n" \
"v_exp_f32 v49, v49\n" \
"v_mfma_f32_16x16x32_bf16 v[172:175], v[216:219], v[8:11], v[172:175]\n" \
"v_exp_f32 v50, v50\n" \
"v_mfma_f32_16x16x32_bf16 v[172:175], v[220:223], v[40:43], v[172:175]\n" \
"s_barrier\n" \
"v_cvt_pk_bf16_f32 v48, v48, v49\n" \
"v_mfma_f32_16x16x32_bf16 v[242:245], a[0:3], v[16:19], v[242:245]\n" \
"v_exp_f32 v51, v51\n" \
"v_exp_f32 v52, v52\n" \
"v_mfma_f32_16x16x32_bf16 v[176:179], v[224:227], v[8:11], v[176:179]\n" \
"s_add_u32 m0, s84, 90112\n" \
"ds_read_b64 v[240:241], v127\n" \
"v_cvt_pk_bf16_f32 v49, v50, v51\n" \
"v_mfma_f32_16x16x32_bf16 v[176:179], v[228:231], v[40:43], v[176:179]\n" \
"ds_read_b128 v[64:67], v125 offset:0\n" \
"global_load_lds_dwordx4 v253, s[48:49] offset:-4096\n" \
"v_exp_f32 v53, v53\n" \
"v_exp_f32 v54, v54\n" \
"v_mfma_f32_16x16x32_bf16 v[180:183], v[232:235], v[8:11], v[180:183]\n" \
"ds_read_b128 v[68:71], v125 offset:1024\n" \
"global_load_lds_dwordx4 v253, s[48:49] offset:-2048\n" \
"v_mfma_f32_16x16x32_bf16 v[180:183], v[236:239], v[40:43], v[180:183]\n" \
"ds_read_b128 v[72:75], v125 offset:2048\n" \
"v_cvt_pk_bf16_f32 v50, v52, v53\n" \
"v_mfma_f32_16x16x32_bf16 v[184:187], v[0:3], v[8:11], v[184:187]\n" \
"ds_read_b128 v[76:79], v125 offset:3072\n" \
"global_load_lds_dwordx4 v253, s[48:49] offset:0\n" \
"v_mfma_f32_16x16x32_bf16 v[184:187], v[4:7], v[40:43], v[184:187]\n" \
"s_add_u32 s48, s48, 0x300000\n" \
"s_addc_u32 s49, s49, 0\n" \
"ds_read_b128 v[80:83], v125 offset:4096\n" \
"v_mfma_f32_16x16x32_bf16 v[188:191], v[32:35], v[8:11], v[188:191]\n" \
"ds_read_b128 v[84:87], v125 offset:5120\n" \
"global_load_lds_dwordx4 v253, s[50:51] offset:-3072\n" \
"v_mfma_f32_16x16x32_bf16 v[188:191], v[36:39], v[40:43], v[188:191]\n" \
"s_waitcnt lgkmcnt(5)\n" \
"ds_read_b128 v[88:91], v125 offset:6144\n" \
"v_exp_f32 v55, v55\n" \
"v_mfma_f32_16x16x32_bf16 v[160:163], a[192:195], v[24:27], v[160:163]\n" \
"ds_read_b128 v[92:95], v125 offset:7168\n" \
"global_load_lds_dwordx4 v253, s[50:51] offset:-1024\n" \
"v_mfma_f32_16x16x32_bf16 v[160:163], a[196:199], v[56:59], v[160:163]\n" \
"s_movk_i32 s89, 0xc0\n" \
"ds_read_b128 v[96:99], v125 offset:8192\n" \
"global_load_lds_dwordx4 v253, s[50:51] offset:1024\n" \
"v_mfma_f32_16x16x32_bf16 v[164:167], a[200:203], v[24:27], v[164:167]\n" \
"s_add_u32 s50, s50, 0x300000\n" \
"s_addc_u32 s51, s51, 0\n" \
"ds_read_b128 v[100:103], v125 offset:9216\n" \
"v_mfma_f32_16x16x32_bf16 v[164:167], a[204:207], v[56:59], v[164:167]\n" \
"s_add_u32 m0, s95, 65536\n" \
"ds_read_b128 v[104:107], v125 offset:10240\n" \
"v_cvt_pk_bf16_f32 v51, v54, v55\n" \
"v_mfma_f32_16x16x32_bf16 v[168:171], a[208:211], v[24:27], v[168:171]\n" \
"s_movk_i32 s90, 288\n" \
"ds_read_b128 v[108:111], v125 offset:11264\n" \
"global_load_lds_dwordx4 v252, s[42:43] offset:0\n" \
"v_mfma_f32_16x16x32_bf16 v[242:245], a[0:3], v[48:51], v[242:245]\n" \
"v_lshrrev_b32 v211, 4, v248\n" \
"v_lshrrev_b32 v214, 8, v248\n" \
"v_and_b32 v215, 7, v211\n" \
"v_xor_b32 v214, v214, v215\n" \
"v_pk_add_f32 v[128:129], v[128:129], v[64:65]\n" \
"v_pk_add_f32 v[130:131], v[130:131], v[66:67]\n" \
"s_waitcnt lgkmcnt(10)\n" \
"v_pk_add_f32 v[132:133], v[132:133], v[68:69]\n" \
"v_pk_add_f32 v[134:135], v[134:135], v[70:71]\n" \
"s_waitcnt lgkmcnt(9)\n" \
"v_pk_add_f32 v[136:137], v[136:137], v[72:73]\n" \
"v_pk_add_f32 v[138:139], v[138:139], v[74:75]\n" \
"s_waitcnt lgkmcnt(8)\n" \
"v_pk_add_f32 v[140:141], v[140:141], v[76:77]\n" \
"v_pk_add_f32 v[142:143], v[142:143], v[78:79]\n" \
"s_waitcnt lgkmcnt(7)\n" \
"v_pk_add_f32 v[144:145], v[144:145], v[80:81]\n" \
"v_pk_add_f32 v[146:147], v[146:147], v[82:83]\n" \
"s_waitcnt lgkmcnt(6)\n" \
"v_pk_add_f32 v[148:149], v[148:149], v[84:85]\n" \
"v_pk_add_f32 v[150:151], v[150:151], v[86:87]\n" \
"s_waitcnt lgkmcnt(5)\n" \
"v_pk_add_f32 v[152:153], v[152:153], v[88:89]\n" \
"v_pk_add_f32 v[154:155], v[154:155], v[90:91]\n" \
"s_waitcnt lgkmcnt(4)\n" \
"v_pk_add_f32 v[156:157], v[156:157], v[92:93]\n" \
"v_pk_add_f32 v[158:159], v[158:159], v[94:95]\n" \
"v_mfma_f32_16x16x32_bf16 v[168:171], a[212:215], v[56:59], v[168:171]\n" \
"ds_read_b128 v[112:115], v125 offset:12288\n" \
"s_lshr_b32 s71, s61, 1\n" \
"v_mfma_f32_16x16x32_bf16 v[172:175], a[216:219], v[24:27], v[172:175]\n" \
"s_lshl_b32 s70, s71, 11\n" \
"ds_read_b128 v[116:119], v125 offset:13312\n" \
"global_load_lds_dwordx4 v252, s[42:43] offset:2048\n" \
"v_mfma_f32_16x16x32_bf16 v[172:175], a[220:223], v[56:59], v[172:175]\n" \
"s_add_u32 s42, s42, 0x400\n" \
"s_addc_u32 s43, s43, 0\n" \
"ds_read_b128 v[120:123], v125 offset:14336\n" \
"v_mfma_f32_16x16x32_bf16 v[176:179], a[224:227], v[24:27], v[176:179]\n" \
"s_add_u32 s70, s70, 118784\n" \
"ds_read_b128 v[124:127], v125 offset:15360\n" \
"v_and_b32 v216, 15, v211\n" \
"v_mfma_f32_16x16x32_bf16 v[176:179], a[228:231], v[56:59], v[176:179]\n" \
"s_waitcnt lgkmcnt(7)\n" \
"global_load_lds_dwordx4 v252, s[44:45] offset:1024\n" \
"global_load_lds_dwordx4 v252, s[44:45] offset:3072\n" \
"v_mfma_f32_16x16x32_bf16 v[180:183], a[232:235], v[24:27], v[180:183]\n" \
"s_add_u32 s44, s44, 0x400\n" \
"s_addc_u32 s45, s45, 0\n" \
"v_lshlrev_b32 v213, 7, v216\n" \
"v_mfma_f32_16x16x32_bf16 v[180:183], a[236:239], v[56:59], v[180:183]\n" \
"s_waitcnt lgkmcnt(0)\n" \
"v_lshl_add_u32 v213, v214, 4, v213\n" \
"v_add_u32 v205, s70, v213\n" \
"v_xor_b32 v206, 64, v205\n" \
"v_mfma_f32_16x16x32_bf16 v[184:187], a[240:243], v[24:27], v[184:187]\n" \
"s_and_b32 s70, s61, 1\n" \
"s_lshl_b32 s70, s70, 9\n" \
"s_add_u32 s70, s70, 139264\n" \
"v_mfma_f32_16x16x32_bf16 v[184:187], a[244:247], v[56:59], v[184:187]\n" \
"s_barrier\n" \
"v_and_b32 v213, 3, v211\n" \
"v_lshlrev_b32 v213, 3, v213\n" \
"v_mfma_f32_16x16x32_bf16 v[188:191], a[248:251], v[24:27], v[188:191]\n" \
"s_add_u32 m0, s95, 0\n" \
"v_and_or_b32 v213, v248, s89, v213\n" \
"v_bfe_u32 v214, v211, 4, 1\n" \
"v_mfma_f32_16x16x32_bf16 v[188:191], a[252:255], v[56:59], v[188:191]\n" \
"v_mad_u32_u24 v213, v214, s90, v213\n" \
"v_lshrrev_b32 v214, 5, v211\n" \
"v_mul_u32_u24 v214, 6144, v214\n" \
"v_mfma_f32_16x16x32_bf16 v[128:131], a[192:195], v[16:19], v[128:131]\n" \
"v_add3_u32 v209, v213, v214, s70\n" \
"v_xor_b32 v210, 32, v209\n" \
"v_mfma_f32_16x16x32_bf16 v[128:131], a[196:199], v[48:51], v[128:131]\n" \
"ds_read_b128 a[72:75], v205 offset:0\n" \
"ds_read_b64_tr_b16 a[96:97], v209 offset:2048\n" \
"v_mfma_f32_16x16x32_bf16 v[132:135], a[200:203], v[16:19], v[132:135]\n" \
"ds_read_b64_tr_b16 a[98:99], v209 offset:3072\n" \
"global_load_lds_dwordx4 v252, s[42:43] offset:0\n" \
"v_mfma_f32_16x16x32_bf16 v[132:135], a[204:207], v[48:51], v[132:135]\n" \
"ds_read_b128 a[76:79], v206 offset:0\n" \
"v_mfma_f32_16x16x32_bf16 v[136:139], a[208:211], v[16:19], v[136:139]\n" \
"ds_read_b64_tr_b16 a[100:101], v209 offset:14336\n" \
"ds_read_b64_tr_b16 a[102:103], v209 offset:15360\n" \
"global_load_lds_dwordx4 v252, s[42:43] offset:2048\n" \
"v_pk_add_f32 v[160:161], v[160:161], v[96:97]\n" \
"v_pk_add_f32 v[162:163], v[162:163], v[98:99]\n" \
"v_pk_add_f32 v[164:165], v[164:165], v[100:101]\n" \
"v_pk_add_f32 v[166:167], v[166:167], v[102:103]\n" \
"v_pk_add_f32 v[168:169], v[168:169], v[104:105]\n" \
"v_pk_add_f32 v[170:171], v[170:171], v[106:107]\n" \
"v_pk_add_f32 v[172:173], v[172:173], v[108:109]\n" \
"v_pk_add_f32 v[174:175], v[174:175], v[110:111]\n" \
"v_pk_add_f32 v[176:177], v[176:177], v[112:113]\n" \
"v_pk_add_f32 v[178:179], v[178:179], v[114:115]\n" \
"v_pk_add_f32 v[180:181], v[180:181], v[116:117]\n" \
"v_pk_add_f32 v[182:183], v[182:183], v[118:119]\n" \
"v_pk_add_f32 v[184:185], v[184:185], v[120:121]\n" \
"v_pk_add_f32 v[186:187], v[186:187], v[122:123]\n" \
"v_pk_add_f32 v[188:189], v[188:189], v[124:125]\n" \
"v_pk_add_f32 v[190:191], v[190:191], v[126:127]\n" \
"v_mfma_f32_16x16x32_bf16 v[136:139], a[212:215], v[48:51], v[136:139]\n" \
"s_add_u32 s42, s42, 0xfffffb00\n" \
"s_addc_u32 s43, s43, -1\n" \
"ds_read_b64_tr_b16 a[104:105], v209 offset:0\n" \
"v_mfma_f32_16x16x32_bf16 v[140:143], a[216:219], v[16:19], v[140:143]\n" \
"ds_read_b128 a[80:83], v205 offset:4096\n" \
"v_mfma_f32_16x16x32_bf16 v[140:143], a[220:223], v[48:51], v[140:143]\n" \
"ds_read_b64_tr_b16 a[106:107], v209 offset:1024\n" \
"global_load_lds_dwordx4 v252, s[44:45] offset:1024\n" \
"ds_read_b64_tr_b16 a[108:109], v209 offset:12288\n" \
"v_mfma_f32_16x16x32_bf16 v[144:147], a[224:227], v[16:19], v[144:147]\n" \
"ds_read_b64_tr_b16 a[110:111], v209 offset:13312\n" \
"v_mfma_f32_16x16x32_bf16 v[144:147], a[228:231], v[48:51], v[144:147]\n" \
"ds_read_b128 a[84:87], v206 offset:4096\n" \
"v_mfma_f32_16x16x32_bf16 v[148:151], a[232:235], v[16:19], v[148:151]\n" \
"ds_read_b64_tr_b16 a[112:113], v210 offset:0\n" \
"global_load_lds_dwordx4 v252, s[44:45] offset:3072\n" \
"ds_read_b64_tr_b16 a[114:115], v210 offset:1024\n" \
"v_mfma_f32_16x16x32_bf16 v[148:151], a[236:239], v[48:51], v[148:151]\n" \
"Lpg8605_%=:\n" \
"Lst1x2_%=:\n" \
"s_cbranch_execz Lpg10869_%=\n" \
"ds_read_b64_tr_b16 a[116:117], v210 offset:12288\n" \
"v_add_f32 v244, v244, v240\n" \
"v_mfma_f32_16x16x32_bf16 v[152:155], a[240:243], v[16:19], v[152:155]\n" \
"ds_read_b128 a[88:91], v205 offset:8192\n" \
"v_add_f32 v245, v245, v241\n" \
"v_mfma_f32_16x16x32_bf16 v[152:155], a[244:247], v[48:51], v[152:155]\n" \
"ds_read_b128 a[92:95], v206 offset:8192\n" \
"ds_read_b64_tr_b16 a[118:119], v210 offset:13312\n" \
"v_mfma_f32_16x16x32_bf16 v[156:159], a[248:251], v[16:19], v[156:159]\n" \
"v_mfma_f32_16x16x32_bf16 v[156:159], a[252:255], v[48:51], v[156:159]\n" \
"s_waitcnt lgkmcnt(4)\n" \
"Ljoin_%=:\n" \
"v_mfma_f32_16x16x32_bf16 v[40:43], a[96:99], a[72:75], 0\n" \
"s_add_u32 s44, s44, 0xfffffb00\n" \
"s_addc_u32 s45, s45, -1\n" \
"ds_read_b128 a[192:195], v205 offset:12288\n" \
"v_mfma_f32_16x16x32_bf16 v[40:43], a[100:103], a[76:79], v[40:43]\n" \
"ds_read_b128 a[196:199], v206 offset:12288\n" \
"s_add_u32 m0, s84, 24576\n" \
"v_mfma_f32_16x16x32_bf16 v[0:3], a[104:107], a[72:75], 0\n" \
"ds_read_b64_tr_b16 a[208:209], v209 offset:4096\n" \
"global_load_lds_dwordx4 v253, s[48:49] offset:-4096\n" \
"v_mfma_f32_16x16x32_bf16 v[0:3], a[108:111], a[76:79], v[0:3]\n" \
"s_waitcnt lgkmcnt(3)\n" \
"v_mov_b32 v232, v244\n" \
"v_mfma_f32_16x16x32_bf16 v[4:7], a[112:115], a[72:75], 0\n" \
"ds_read_b64_tr_b16 a[210:211], v209 offset:5120\n" \
"global_load_lds_dwordx4 v253, s[48:49] offset:-2048\n" \
"v_mfma_f32_16x16x32_bf16 v[4:7], a[116:119], a[76:79], v[4:7]\n" \
"ds_read_b64_tr_b16 a[212:213], v209 offset:16384\n" \
"v_permlane16_swap_b32 v232, v244\n" \
"v_mfma_f32_16x16x32_bf16 v[12:15], a[112:115], a[80:83], 0\n" \
"ds_read_b64_tr_b16 a[214:215], v209 offset:17408\n" \
"v_add_f32 v244, v244, v232\n" \
"v_mfma_f32_16x16x32_bf16 v[12:15], a[116:119], a[84:87], v[12:15]\n" \
"global_load_lds_dwordx4 v253, s[48:49] offset:0\n" \
"v_mov_b32 v232, v244\n" \
"v_mfma_f32_16x16x32_bf16 v[8:11], a[104:107], a[80:83], 0\n" \
"s_add_u32 s48, s48, 0xffc40000\n" \
"s_addc_u32 s49, s49, -1\n" \
"ds_read_b64_tr_b16 a[216:217], v210 offset:4096\n" \
"v_mfma_f32_16x16x32_bf16 v[8:11], a[108:111], a[84:87], v[8:11]\n" \
"ds_read_b64_tr_b16 a[218:219], v210 offset:5120\n" \
"v_permlane32_swap_b32 v232, v244\n" \
"v_add_f32 v244, v244, v232\n" \
"v_mfma_f32_16x16x32_bf16 v[48:51], a[96:99], a[80:83], 0\n" \
"ds_read_b64_tr_b16 a[220:221], v210 offset:16384\n" \
"global_load_lds_dwordx4 v253, s[50:51] offset:-3072\n" \
"v_mfma_f32_16x16x32_bf16 v[48:51], a[100:103], a[84:87], v[48:51]\n" \
"v_rcp_f32 v232, v244\n" \
"v_mfma_f32_16x16x32_bf16 v[56:59], a[96:99], a[88:91], 0\n" \
"ds_read_b64_tr_b16 a[222:223], v210 offset:17408\n" \
"v_mul_f32 v244, 0x4d800000, v244\n" \
"v_mfma_f32_16x16x32_bf16 v[56:59], a[100:103], a[92:95], v[56:59]\n" \
"ds_read_b64_tr_b16 a[200:201], v210 offset:2048\n" \
"global_load_lds_dwordx4 v253, s[50:51] offset:-1024\n" \
"v_mfma_f32_16x16x32_bf16 v[16:19], a[104:107], a[88:91], 0\n" \
"ds_read_b64_tr_b16 a[202:203], v210 offset:3072\n" \
"v_add_u32 v233, 0x1c90080, v249\n" \
"v_mfma_f32_16x16x32_bf16 v[16:19], a[108:111], a[92:95], v[16:19]\n" \
"v_cndmask_b32 v233, v249, v233, s[16:17]\n" \
"v_mul_f32 v128, v128, v232\n" \
"v_mfma_f32_16x16x32_bf16 v[20:23], a[112:115], a[88:91], 0\n" \
"ds_read_b64_tr_b16 a[204:205], v210 offset:14336\n" \
"global_load_lds_dwordx4 v253, s[50:51] offset:1024\n" \
"v_mfma_f32_16x16x32_bf16 v[20:23], a[116:119], a[92:95], v[20:23]\n" \
"s_waitcnt lgkmcnt(3)\n" \
"ds_read_b64_tr_b16 a[206:207], v210 offset:15360\n" \
"v_mul_f32 v129, v129, v232\n" \
"v_mfma_f32_16x16x32_bf16 v[28:31], a[112:115], a[192:195], 0\n" \
"s_add_u32 s50, s50, 0xffc40000\n" \
"s_addc_u32 s51, s51, -1\n" \
"v_add_u32 v205, 0xffff3000, v205\n" \
"v_mfma_f32_16x16x32_bf16 v[28:31], a[116:119], a[196:199], v[28:31]\n" \
"v_add_u32 v206, 0xffff3000, v206\n" \
"v_add_u32 v209, 0xffff3000, v209\n" \
"v_mfma_f32_16x16x32_bf16 v[24:27], a[104:107], a[192:195], 0\n" \
"v_add_u32 v210, 0xffff3000, v210\n" \
"v_mul_f32 v130, v130, v232\n" \
"v_mfma_f32_16x16x32_bf16 v[24:27], a[108:111], a[196:199], v[24:27]\n" \
"s_waitcnt vmcnt(10) lgkmcnt(0)\n" \
"v_mul_f32 v131, v131, v232\n" \
"v_mul_f32 v132, v132, v232\n" \
"v_mfma_f32_16x16x32_bf16 v[64:67], a[96:99], a[192:195], 0\n" \
"v_mul_f32 v133, v133, v232\n" \
"v_mul_f32 v134, v134, v232\n" \
"v_mfma_f32_16x16x32_bf16 v[64:67], a[100:103], a[196:199], v[64:67]\n" \
"s_barrier\n" \
"v_mfma_f32_16x16x32_bf16 v[80:83], a[72:75], a[208:211], 0\n" \
"ds_read_b64_tr_b16 a[96:97], v209 offset:2048\n" \
"v_mul_f32 v135, v135, v232\n" \
"v_mfma_f32_16x16x32_bf16 v[80:83], a[76:79], a[212:215], v[80:83]\n" \
"ds_read_b64_tr_b16 a[98:99], v209 offset:3072\n" \
"v_fma_f32 v244, v128, v129, v244\n" \
"v_mfma_f32_16x16x32_bf16 v[84:87], a[72:75], a[216:219], 0\n" \
"s_add_u32 m0, s95, 118784\n" \
"ds_read_b64_tr_b16 a[100:101], v209 offset:14336\n" \
"v_cvt_pk_bf16_f32 v128, v128, v129\n" \
"v_mfma_f32_16x16x32_bf16 v[84:87], a[76:79], a[220:223], v[84:87]\n" \
"global_load_lds_dwordx4 v252, s[42:43] offset:0\n" \
"v_fma_f32 v244, v130, v131, v244\n" \
"v_mfma_f32_16x16x32_bf16 v[44:47], a[200:203], a[72:75], 0\n" \
"ds_read_b64_tr_b16 a[102:103], v209 offset:15360\n" \
"v_cvt_pk_bf16_f32 v129, v130, v131\n" \
"v_mfma_f32_16x16x32_bf16 v[44:47], a[204:207], a[76:79], v[44:47]\n" \
"ds_read_b128 a[72:75], v205 offset:0\n" \
"ds_read_b64_tr_b16 a[104:105], v209 offset:0\n" \
"v_mfma_f32_16x16x32_bf16 v[52:55], a[200:203], a[80:83], 0\n" \
"ds_read_b128 a[76:79], v206 offset:0\n" \
"v_fma_f32 v244, v132, v133, v244\n" \
"v_mfma_f32_16x16x32_bf16 v[52:55], a[204:207], a[84:87], v[52:55]\n" \
"global_load_lds_dwordx4 v252, s[42:43] offset:2048\n" \
"v_cvt_pk_bf16_f32 v130, v132, v133\n" \
"v_mfma_f32_16x16x32_bf16 v[92:95], a[80:83], a[216:219], 0\n" \
"s_add_u32 s42, s42, 0x80\n" \
"s_addc_u32 s43, s43, 0\n" \
"ds_read_b64_tr_b16 a[106:107], v209 offset:1024\n" \
"v_mfma_f32_16x16x32_bf16 v[92:95], a[84:87], a[220:223], v[92:95]\n" \
"ds_read_b64_tr_b16 a[108:109], v209 offset:12288\n" \
"v_fma_f32 v244, v134, v135, v244\n" \
"v_mfma_f32_16x16x32_bf16 v[88:91], a[80:83], a[208:211], 0\n" \
"ds_read_b64_tr_b16 a[110:111], v209 offset:13312\n" \
"v_cvt_pk_bf16_f32 v131, v134, v135\n" \
"v_mfma_f32_16x16x32_bf16 v[88:91], a[84:87], a[212:215], v[88:91]\n" \
"buffer_store_dwordx4 v[128:131], v233, s[56:59], s78 offen offset:0 sc1\n" \
"global_load_lds_dwordx4 v252, s[44:45] offset:1024\n" \
"v_mul_f32 v136, v136, v232\n" \
"v_mul_f32 v137, v137, v232\n" \
"v_mul_f32 v138, v138, v232\n" \
"v_mfma_f32_16x16x32_bf16 v[96:99], a[88:91], a[208:211], 0\n" \
"ds_read_b128 a[80:83], v205 offset:4096\n" \
"v_mul_f32 v139, v139, v232\n" \
"v_mfma_f32_16x16x32_bf16 v[96:99], a[92:95], a[212:215], v[96:99]\n" \
"ds_read_b128 a[84:87], v206 offset:4096\n" \
"v_mul_f32 v140, v140, v232\n" \
"v_mfma_f32_16x16x32_bf16 v[100:103], a[88:91], a[216:219], 0\n" \
"ds_read_b64_tr_b16 a[112:113], v210 offset:0\n" \
"v_mul_f32 v141, v141, v232\n" \
"v_mfma_f32_16x16x32_bf16 v[100:103], a[92:95], a[220:223], v[100:103]\n" \
"global_load_lds_dwordx4 v252, s[44:45] offset:3072\n" \
"v_mul_f32 v142, v142, v232\n" \
"v_mfma_f32_16x16x32_bf16 v[60:63], a[200:203], a[88:91], 0\n" \
"s_add_u32 s44, s44, 0x80\n" \
"s_addc_u32 s45, s45, 0\n" \
"ds_read_b64_tr_b16 a[114:115], v210 offset:1024\n" \
"v_mfma_f32_16x16x32_bf16 v[60:63], a[204:207], a[92:95], v[60:63]\n" \
"ds_read_b64_tr_b16 a[116:117], v210 offset:12288\n" \
"v_mul_f32 v143, v143, v232\n" \
"v_mfma_f32_16x16x32_bf16 v[68:71], a[200:203], a[192:195], 0\n" \
"s_add_u32 m0, s84, 143360\n" \
"ds_read_b128 a[88:91], v205 offset:8192\n" \
"v_fma_f32 v244, v136, v137, v244\n" \
"v_mfma_f32_16x16x32_bf16 v[68:71], a[204:207], a[196:199], v[68:71]\n" \
"global_load_lds_dwordx4 v253, s[48:49] offset:-4096\n" \
"v_cvt_pk_bf16_f32 v136, v136, v137\n" \
"v_mfma_f32_16x16x32_bf16 v[108:111], a[192:195], a[216:219], 0\n" \
"ds_read_b128 a[92:95], v206 offset:8192\n" \
"v_fma_f32 v244, v138, v139, v244\n" \
"v_mfma_f32_16x16x32_bf16 v[108:111], a[196:199], a[220:223], v[108:111]\n" \
"ds_read_b64_tr_b16 a[118:119], v210 offset:13312\n" \
"v_cvt_pk_bf16_f32 v137, v138, v139\n" \
"v_mfma_f32_16x16x32_bf16 v[104:107], a[192:195], a[208:211], 0\n" \
"v_fma_f32 v244, v140, v141, v244\n" \
"v_cvt_pk_bf16_f32 v138, v140, v141\n" \
"v_mfma_f32_16x16x32_bf16 v[104:107], a[196:199], a[212:215], v[104:107]\n" \
"s_waitcnt lgkmcnt(2)\n" \
"global_load_lds_dwordx4 v253, s[48:49] offset:-2048\n" \
"v_fma_f32 v244, v142, v143, v244\n" \
"v_mfma_f32_16x16x32_bf16 v[40:43], a[96:99], a[72:75], v[40:43]\n" \
"ds_read_b128 a[192:195], v205 offset:12288\n" \
"v_cvt_pk_bf16_f32 v139, v142, v143\n" \
"v_mfma_f32_16x16x32_bf16 v[40:43], a[100:103], a[76:79], v[40:43]\n" \
"buffer_store_dwordx4 v[136:139], v233, s[56:59], s78 offen offset:2048 sc1\n" \
"ds_read_b128 a[196:199], v206 offset:12288\n" \
"s_add_u32 s78, s78, 0x1000\n" \
"v_mul_f32 v144, v144, v232\n" \
"v_mul_f32 v145, v145, v232\n" \
"v_mfma_f32_16x16x32_bf16 v[0:3], a[104:107], a[72:75], v[0:3]\n" \
"ds_read_b64_tr_b16 a[208:209], v209 offset:4096\n" \
"v_mul_f32 v146, v146, v232\n" \
"v_mfma_f32_16x16x32_bf16 v[0:3], a[108:111], a[76:79], v[0:3]\n" \
"s_waitcnt lgkmcnt(3)\n" \
"global_load_lds_dwordx4 v253, s[48:49] offset:0\n" \
"v_mul_f32 v147, v147, v232\n" \
"v_mfma_f32_16x16x32_bf16 v[4:7], a[112:115], a[72:75], v[4:7]\n" \
"s_add_u32 s48, s48, 0x60000\n" \
"s_addc_u32 s49, s49, 0\n" \
"ds_read_b64_tr_b16 a[210:211], v209 offset:5120\n" \
"v_mfma_f32_16x16x32_bf16 v[4:7], a[116:119], a[76:79], v[4:7]\n" \
"ds_read_b64_tr_b16 a[212:213], v209 offset:16384\n" \
"v_mul_f32 v148, v148, v232\n" \
"v_mfma_f32_16x16x32_bf16 v[12:15], a[112:115], a[80:83], v[12:15]\n" \
"ds_read_b64_tr_b16 a[214:215], v209 offset:17408\n" \
"v_mul_f32 v149, v149, v232\n" \
"v_mfma_f32_16x16x32_bf16 v[12:15], a[116:119], a[84:87], v[12:15]\n" \
"global_load_lds_dwordx4 v253, s[50:51] offset:-3072\n" \
"v_mul_f32 v150, v150, v232\n" \
"v_mfma_f32_16x16x32_bf16 v[8:11], a[104:107], a[80:83], v[8:11]\n" \
"ds_read_b64_tr_b16 a[216:217], v210 offset:4096\n" \
"v_mul_f32 v151, v151, v232\n" \
"v_mfma_f32_16x16x32_bf16 v[8:11], a[108:111], a[84:87], v[8:11]\n" \
"ds_read_b64_tr_b16 a[218:219], v210 offset:5120\n" \
"v_fma_f32 v244, v144, v145, v244\n" \
"v_mfma_f32_16x16x32_bf16 v[48:51], a[96:99], a[80:83], v[48:51]\n" \
"ds_read_b64_tr_b16 a[220:221], v210 offset:16384\n" \
"v_cvt_pk_bf16_f32 v144, v144, v145\n" \
"v_mfma_f32_16x16x32_bf16 v[48:51], a[100:103], a[84:87], v[48:51]\n" \
"global_load_lds_dwordx4 v253, s[50:51] offset:-1024\n" \
"v_fma_f32 v244, v146, v147, v244\n" \
"v_mfma_f32_16x16x32_bf16 v[56:59], a[96:99], a[88:91], v[56:59]\n" \
"ds_read_b64_tr_b16 a[222:223], v210 offset:17408\n" \
"v_cvt_pk_bf16_f32 v145, v146, v147\n" \
"v_mfma_f32_16x16x32_bf16 v[56:59], a[100:103], a[92:95], v[56:59]\n" \
"ds_read_b64_tr_b16 a[200:201], v210 offset:2048\n" \
"v_fma_f32 v244, v148, v149, v244\n" \
"v_mfma_f32_16x16x32_bf16 v[16:19], a[104:107], a[88:91], v[16:19]\n" \
"ds_read_b64_tr_b16 a[202:203], v210 offset:3072\n" \
"v_cvt_pk_bf16_f32 v146, v148, v149\n" \
"v_mfma_f32_16x16x32_bf16 v[16:19], a[108:111], a[92:95], v[16:19]\n" \
"global_load_lds_dwordx4 v253, s[50:51] offset:1024\n" \
"v_fma_f32 v244, v150, v151, v244\n" \
"v_mfma_f32_16x16x32_bf16 v[20:23], a[112:115], a[88:91], v[20:23]\n" \
"s_add_u32 s50, s50, 0x60000\n" \
"s_addc_u32 s51, s51, 0\n" \
"ds_read_b64_tr_b16 a[204:205], v210 offset:14336\n" \
"v_mfma_f32_16x16x32_bf16 v[20:23], a[116:119], a[92:95], v[20:23]\n" \
"s_waitcnt lgkmcnt(3)\n" \
"ds_read_b64_tr_b16 a[206:207], v210 offset:15360\n" \
"v_cvt_pk_bf16_f32 v147, v150, v151\n" \
"v_mfma_f32_16x16x32_bf16 v[28:31], a[112:115], a[192:195], v[28:31]\n" \
"v_add_u32 v205, 0xffff0000, v205\n" \
"v_add_u32 v206, 0xffff0000, v206\n" \
"v_mfma_f32_16x16x32_bf16 v[28:31], a[116:119], a[196:199], v[28:31]\n" \
"buffer_store_dwordx4 v[144:147], v233, s[56:59], s78 offen offset:0 sc1\n" \
"v_add_u32 v209, 0xffff0000, v209\n" \
"v_add_u32 v210, 0xffff0000, v210\n" \
"v_mul_f32 v152, v152, v232\n" \
"v_mul_f32 v153, v153, v232\n" \
"v_mfma_f32_16x16x32_bf16 v[24:27], a[104:107], a[192:195], v[24:27]\n" \
"v_mul_f32 v154, v154, v232\n" \
"v_mul_f32 v155, v155, v232\n" \
"v_mfma_f32_16x16x32_bf16 v[24:27], a[108:111], a[196:199], v[24:27]\n" \
"s_waitcnt vmcnt(13) lgkmcnt(0)\n" \
"v_mul_f32 v156, v156, v232\n" \
"v_mul_f32 v157, v157, v232\n" \
"v_mfma_f32_16x16x32_bf16 v[64:67], a[96:99], a[192:195], v[64:67]\n" \
"v_mul_f32 v158, v158, v232\n" \
"v_mul_f32 v159, v159, v232\n" \
"v_mfma_f32_16x16x32_bf16 v[64:67], a[100:103], a[196:199], v[64:67]\n" \
"s_barrier\n" \
"v_fma_f32 v244, v152, v153, v244\n" \
"v_mfma_f32_16x16x32_bf16 v[80:83], a[72:75], a[208:211], v[80:83]\n" \
"ds_read_b64_tr_b16 a[96:97], v209 offset:2048\n" \
"v_cvt_pk_bf16_f32 v152, v152, v153\n" \
"v_mfma_f32_16x16x32_bf16 v[80:83], a[76:79], a[212:215], v[80:83]\n" \
"ds_read_b64_tr_b16 a[98:99], v209 offset:3072\n" \
"v_fma_f32 v244, v154, v155, v244\n" \
"v_mfma_f32_16x16x32_bf16 v[84:87], a[72:75], a[216:219], v[84:87]\n" \
"s_add_u32 m0, s95, 65536\n" \
"ds_read_b64_tr_b16 a[100:101], v209 offset:14336\n" \
"v_cvt_pk_bf16_f32 v153, v154, v155\n" \
"v_mfma_f32_16x16x32_bf16 v[84:87], a[76:79], a[220:223], v[84:87]\n" \
"global_load_lds_dwordx4 v252, s[42:43] offset:0\n" \
"v_fma_f32 v244, v156, v157, v244\n" \
"v_mfma_f32_16x16x32_bf16 v[44:47], a[200:203], a[72:75], v[44:47]\n" \
"ds_read_b64_tr_b16 a[102:103], v209 offset:15360\n" \
"v_cvt_pk_bf16_f32 v154, v156, v157\n" \
"v_mfma_f32_16x16x32_bf16 v[44:47], a[204:207], a[76:79], v[44:47]\n" \
"ds_read_b128 a[72:75], v205 offset:0\n" \
"ds_read_b64_tr_b16 a[104:105], v209 offset:0\n" \
"v_mfma_f32_16x16x32_bf16 v[52:55], a[200:203], a[80:83], v[52:55]\n" \
"ds_read_b128 a[76:79], v206 offset:0\n" \
"v_fma_f32 v244, v158, v159, v244\n" \
"v_mfma_f32_16x16x32_bf16 v[52:55], a[204:207], a[84:87], v[52:55]\n" \
"global_load_lds_dwordx4 v252, s[42:43] offset:2048\n" \
"v_cvt_pk_bf16_f32 v155, v158, v159\n" \
"v_mfma_f32_16x16x32_bf16 v[92:95], a[80:83], a[216:219], v[92:95]\n" \
"s_add_u32 s42, s42, 0x100\n" \
"s_addc_u32 s43, s43, 0\n" \
"ds_read_b64_tr_b16 a[106:107], v209 offset:1024\n" \
"v_mfma_f32_16x16x32_bf16 v[92:95], a[84:87], a[220:223], v[92:95]\n" \
"buffer_store_dwordx4 v[152:155], v233, s[56:59], s78 offen offset:2048 sc1\n" \
"ds_read_b64_tr_b16 a[108:109], v209 offset:12288\n" \
"v_cmp_class_f32 vcc, v244, s94\n" \
"s_sub_u32 s78, s78, 4096\n" \
"v_mfma_f32_16x16x32_bf16 v[88:91], a[80:83], a[208:211], v[88:91]\n" \
"s_or_b64 s[92:93], s[92:93], vcc\n" \
"ds_read_b64_tr_b16 a[110:111], v209 offset:13312\n" \
"v_mov_b32 v234, v245\n" \
"v_mfma_f32_16x16x32_bf16 v[88:91], a[84:87], a[212:215], v[88:91]\n" \
"global_load_lds_dwordx4 v252, s[44:45] offset:1024\n" \
"v_mfma_f32_16x16x32_bf16 v[96:99], a[88:91], a[208:211], v[96:99]\n" \
"ds_read_b128 a[80:83], v205 offset:4096\n" \
"v_mfma_f32_16x16x32_bf16 v[96:99], a[92:95], a[212:215], v[96:99]\n" \
"ds_read_b128 a[84:87], v206 offset:4096\n" \
"v_permlane16_swap_b32 v234, v245\n" \
"v_mfma_f32_16x16x32_bf16 v[100:103], a[88:91], a[216:219], v[100:103]\n" \
"ds_read_b64_tr_b16 a[112:113], v210 offset:0\n" \
"v_add_f32 v245, v245, v234\n" \
"v_mfma_f32_16x16x32_bf16 v[100:103], a[92:95], a[220:223], v[100:103]\n" \
"global_load_lds_dwordx4 v252, s[44:45] offset:3072\n" \
"v_mov_b32 v234, v245\n" \
"v_mfma_f32_16x16x32_bf16 v[60:63], a[200:203], a[88:91], v[60:63]\n" \
"s_add_u32 s44, s44, 0x100\n" \
"s_addc_u32 s45, s45, 0\n" \
"ds_read_b64_tr_b16 a[114:115], v210 offset:1024\n" \
"v_mfma_f32_16x16x32_bf16 v[60:63], a[204:207], a[92:95], v[60:63]\n" \
"ds_read_b64_tr_b16 a[116:117], v210 offset:12288\n" \
"v_permlane32_swap_b32 v234, v245\n" \
"v_mfma_f32_16x16x32_bf16 v[68:71], a[200:203], a[192:195], v[68:71]\n" \
"s_add_u32 m0, s84, 90112\n" \
"ds_read_b128 a[88:91], v205 offset:8192\n" \
"v_add_f32 v245, v245, v234\n" \
"v_mfma_f32_16x16x32_bf16 v[68:71], a[204:207], a[196:199], v[68:71]\n" \
"global_load_lds_dwordx4 v253, s[48:49] offset:-4096\n" \
"v_mfma_f32_16x16x32_bf16 v[108:111], a[192:195], a[216:219], v[108:111]\n" \
"ds_read_b128 a[92:95], v206 offset:8192\n" \
"v_mfma_f32_16x16x32_bf16 v[108:111], a[196:199], a[220:223], v[108:111]\n" \
"ds_read_b64_tr_b16 a[118:119], v210 offset:13312\n" \
"v_mfma_f32_16x16x32_bf16 v[104:107], a[192:195], a[208:211], v[104:107]\n" \
"v_rcp_f32 v234, v245\n" \
"v_mfma_f32_16x16x32_bf16 v[104:107], a[196:199], a[212:215], v[104:107]\n" \
"s_waitcnt lgkmcnt(2)\n" \
"global_load_lds_dwordx4 v253, s[48:49] offset:-2048\n" \
"v_mul_f32 v245, 0x4d800000, v245\n" \
"v_mfma_f32_16x16x32_bf16 v[40:43], a[96:99], a[72:75], v[40:43]\n" \
"ds_read_b128 a[192:195], v205 offset:12288\n" \
"v_add_u32 v235, 0x1c90080, v249\n" \
"v_mfma_f32_16x16x32_bf16 v[40:43], a[100:103], a[76:79], v[40:43]\n" \
"ds_read_b128 a[196:199], v206 offset:12288\n" \
"v_cndmask_b32 v235, v249, v235, s[16:17]\n" \
"v_mfma_f32_16x16x32_bf16 v[0:3], a[104:107], a[72:75], v[0:3]\n" \
"s_add_u32 s78, s78, 532480\n" \
"ds_read_b64_tr_b16 a[208:209], v209 offset:4096\n" \
"v_mul_f32 v160, v160, v234\n" \
"v_mfma_f32_16x16x32_bf16 v[0:3], a[108:111], a[76:79], v[0:3]\n" \
"s_waitcnt lgkmcnt(3)\n" \
"global_load_lds_dwordx4 v253, s[48:49] offset:0\n" \
"v_mul_f32 v161, v161, v234\n" \
"v_mfma_f32_16x16x32_bf16 v[4:7], a[112:115], a[72:75], v[4:7]\n" \
"s_add_u32 s48, s48, 0xc0000\n" \
"s_addc_u32 s49, s49, 0\n" \
"ds_read_b64_tr_b16 a[210:211], v209 offset:5120\n" \
"v_mfma_f32_16x16x32_bf16 v[4:7], a[116:119], a[76:79], v[4:7]\n" \
"ds_read_b64_tr_b16 a[212:213], v209 offset:16384\n" \
"v_mul_f32 v162, v162, v234\n" \
"v_mfma_f32_16x16x32_bf16 v[12:15], a[112:115], a[80:83], v[12:15]\n" \
"ds_read_b64_tr_b16 a[214:215], v209 offset:17408\n" \
"v_mul_f32 v163, v163, v234\n" \
"v_mfma_f32_16x16x32_bf16 v[12:15], a[116:119], a[84:87], v[12:15]\n" \
"global_load_lds_dwordx4 v253, s[50:51] offset:-3072\n" \
"v_mul_f32 v164, v164, v234\n" \
"v_mfma_f32_16x16x32_bf16 v[8:11], a[104:107], a[80:83], v[8:11]\n" \
"ds_read_b64_tr_b16 a[216:217], v210 offset:4096\n" \
"v_mul_f32 v165, v165, v234\n" \
"v_mfma_f32_16x16x32_bf16 v[8:11], a[108:111], a[84:87], v[8:11]\n" \
"ds_read_b64_tr_b16 a[218:219], v210 offset:5120\n" \
"v_mul_f32 v166, v166, v234\n" \
"v_mfma_f32_16x16x32_bf16 v[48:51], a[96:99], a[80:83], v[48:51]\n" \
"ds_read_b64_tr_b16 a[220:221], v210 offset:16384\n" \
"v_mul_f32 v167, v167, v234\n" \
"v_mfma_f32_16x16x32_bf16 v[48:51], a[100:103], a[84:87], v[48:51]\n" \
"global_load_lds_dwordx4 v253, s[50:51] offset:-1024\n" \
"v_fma_f32 v245, v160, v161, v245\n" \
"v_mfma_f32_16x16x32_bf16 v[56:59], a[96:99], a[88:91], v[56:59]\n" \
"ds_read_b64_tr_b16 a[222:223], v210 offset:17408\n" \
"v_cvt_pk_bf16_f32 v160, v160, v161\n" \
"v_mfma_f32_16x16x32_bf16 v[56:59], a[100:103], a[92:95], v[56:59]\n" \
"ds_read_b64_tr_b16 a[200:201], v210 offset:2048\n" \
"v_fma_f32 v245, v162, v163, v245\n" \
"v_mfma_f32_16x16x32_bf16 v[16:19], a[104:107], a[88:91], v[16:19]\n" \
"ds_read_b64_tr_b16 a[202:203], v210 offset:3072\n" \
"v_cvt_pk_bf16_f32 v161, v162, v163\n" \
"v_mfma_f32_16x16x32_bf16 v[16:19], a[108:111], a[92:95], v[16:19]\n" \
"global_load_lds_dwordx4 v253, s[50:51] offset:1024\n" \
"v_fma_f32 v245, v164, v165, v245\n" \
"v_mfma_f32_16x16x32_bf16 v[20:23], a[112:115], a[88:91], v[20:23]\n" \
"s_add_u32 s50, s50, 0xc0000\n" \
"s_addc_u32 s51, s51, 0\n" \
"ds_read_b64_tr_b16 a[204:205], v210 offset:14336\n" \
"v_mfma_f32_16x16x32_bf16 v[20:23], a[116:119], a[92:95], v[20:23]\n" \
"s_waitcnt lgkmcnt(3)\n" \
"ds_read_b64_tr_b16 a[206:207], v210 offset:15360\n" \
"v_cvt_pk_bf16_f32 v162, v164, v165\n" \
"v_mfma_f32_16x16x32_bf16 v[28:31], a[112:115], a[192:195], v[28:31]\n" \
"v_add_u32 v205, 0x1d000, v205\n" \
"v_add_u32 v206, 0x1d000, v206\n" \
"v_mfma_f32_16x16x32_bf16 v[28:31], a[116:119], a[196:199], v[28:31]\n" \
"v_add_u32 v209, 0x1d000, v209\n" \
"v_add_u32 v210, 0x1d000, v210\n" \
"v_mfma_f32_16x16x32_bf16 v[24:27], a[104:107], a[192:195], v[24:27]\n" \
"v_fma_f32 v245, v166, v167, v245\n" \
"v_cvt_pk_bf16_f32 v163, v166, v167\n" \
"v_mfma_f32_16x16x32_bf16 v[24:27], a[108:111], a[196:199], v[24:27]\n" \
"s_waitcnt vmcnt(12) lgkmcnt(0)\n" \
"buffer_store_dwordx4 v[160:163], v235, s[56:59], s78 offen offset:0 sc1\n" \
"v_mul_f32 v168, v168, v234\n" \
"v_mul_f32 v169, v169, v234\n" \
"v_mul_f32 v170, v170, v234\n" \
"v_mul_f32 v171, v171, v234\n" \
"v_mfma_f32_16x16x32_bf16 v[64:67], a[96:99], a[192:195], v[64:67]\n" \
"v_mul_f32 v172, v172, v234\n" \
"v_mul_f32 v173, v173, v234\n" \
"v_mfma_f32_16x16x32_bf16 v[64:67], a[100:103], a[196:199], v[64:67]\n" \
"s_barrier\n" \
"v_mfma_f32_16x16x32_bf16 v[80:83], a[72:75], a[208:211], v[80:83]\n" \
"ds_read_b64_tr_b16 a[96:97], v209 offset:2048\n" \
"v_mul_f32 v174, v174, v234\n" \
"v_mfma_f32_16x16x32_bf16 v[80:83], a[76:79], a[212:215], v[80:83]\n" \
"ds_read_b64_tr_b16 a[98:99], v209 offset:3072\n" \
"v_mul_f32 v175, v175, v234\n" \
"v_mfma_f32_16x16x32_bf16 v[84:87], a[72:75], a[216:219], v[84:87]\n" \
"s_add_u32 m0, s95, 0\n" \
"ds_read_b64_tr_b16 a[100:101], v209 offset:14336\n" \
"v_fma_f32 v245, v168, v169, v245\n" \
"v_mfma_f32_16x16x32_bf16 v[84:87], a[76:79], a[220:223], v[84:87]\n" \
"global_load_lds_dwordx4 v252, s[42:43] offset:0\n" \
"v_cvt_pk_bf16_f32 v168, v168, v169\n" \
"v_mfma_f32_16x16x32_bf16 v[44:47], a[200:203], a[72:75], v[44:47]\n" \
"ds_read_b64_tr_b16 a[102:103], v209 offset:15360\n" \
"v_fma_f32 v245, v170, v171, v245\n" \
"v_mfma_f32_16x16x32_bf16 v[44:47], a[204:207], a[76:79], v[44:47]\n" \
"ds_read_b128 a[72:75], v205 offset:0\n" \
"ds_read_b64_tr_b16 a[104:105], v209 offset:0\n" \
"v_mfma_f32_16x16x32_bf16 v[52:55], a[200:203], a[80:83], v[52:55]\n" \
"ds_read_b128 a[76:79], v206 offset:0\n" \
"v_cvt_pk_bf16_f32 v169, v170, v171\n" \
"v_mfma_f32_16x16x32_bf16 v[52:55], a[204:207], a[84:87], v[52:55]\n" \
"global_load_lds_dwordx4 v252, s[42:43] offset:2048\n" \
"v_fma_f32 v245, v172, v173, v245\n" \
"v_mfma_f32_16x16x32_bf16 v[92:95], a[80:83], a[216:219], v[92:95]\n" \
"s_add_u32 s42, s42, 0x80\n" \
"s_addc_u32 s43, s43, 0\n" \
"ds_read_b64_tr_b16 a[106:107], v209 offset:1024\n" \
"v_mfma_f32_16x16x32_bf16 v[92:95], a[84:87], a[220:223], v[92:95]\n" \
"ds_read_b64_tr_b16 a[108:109], v209 offset:12288\n" \
"v_cvt_pk_bf16_f32 v170, v172, v173\n" \
"v_mfma_f32_16x16x32_bf16 v[88:91], a[80:83], a[208:211], v[88:91]\n" \
"ds_read_b64_tr_b16 a[110:111], v209 offset:13312\n" \
"v_fma_f32 v245, v174, v175, v245\n" \
"v_mfma_f32_16x16x32_bf16 v[88:91], a[84:87], a[212:215], v[88:91]\n" \
"global_load_lds_dwordx4 v252, s[44:45] offset:1024\n" \
"v_cvt_pk_bf16_f32 v171, v174, v175\n" \
"v_mfma_f32_16x16x32_bf16 v[96:99], a[88:91], a[208:211], v[96:99]\n" \
"ds_read_b128 a[80:83], v205 offset:4096\n" \
"v_mfma_f32_16x16x32_bf16 v[96:99], a[92:95], a[212:215], v[96:99]\n" \
"buffer_store_dwordx4 v[168:171], v235, s[56:59], s78 offen offset:2048 sc1\n" \
"ds_read_b128 a[84:87], v206 offset:4096\n" \
"s_add_u32 s78, s78, 0x1000\n" \
"v_mul_f32 v176, v176, v234\n" \
"v_mul_f32 v177, v177, v234\n" \
"v_mfma_f32_16x16x32_bf16 v[100:103], a[88:91], a[216:219], v[100:103]\n" \
"ds_read_b64_tr_b16 a[112:113], v210 offset:0\n" \
"v_mul_f32 v178, v178, v234\n" \
"v_mfma_f32_16x16x32_bf16 v[100:103], a[92:95], a[220:223], v[100:103]\n" \
"global_load_lds_dwordx4 v252, s[44:45] offset:3072\n" \
"v_mul_f32 v179, v179, v234\n" \
"v_mfma_f32_16x16x32_bf16 v[60:63], a[200:203], a[88:91], v[60:63]\n" \
"s_add_u32 s44, s44, 0x80\n" \
"s_addc_u32 s45, s45, 0\n" \
"ds_read_b64_tr_b16 a[114:115], v210 offset:1024\n" \
"v_mfma_f32_16x16x32_bf16 v[60:63], a[204:207], a[92:95], v[60:63]\n" \
"ds_read_b64_tr_b16 a[116:117], v210 offset:12288\n" \
"v_mul_f32 v180, v180, v234\n" \
"v_mfma_f32_16x16x32_bf16 v[68:71], a[200:203], a[192:195], v[68:71]\n" \
"s_add_u32 m0, s84, 24576\n" \
"ds_read_b128 a[88:91], v205 offset:8192\n" \
"v_mul_f32 v181, v181, v234\n" \
"v_mfma_f32_16x16x32_bf16 v[68:71], a[204:207], a[196:199], v[68:71]\n" \
"global_load_lds_dwordx4 v253, s[48:49] offset:-4096\n" \
"v_mul_f32 v182, v182, v234\n" \
"v_mfma_f32_16x16x32_bf16 v[108:111], a[192:195], a[216:219], v[108:111]\n" \
"ds_read_b128 a[92:95], v206 offset:8192\n" \
"v_mul_f32 v183, v183, v234\n" \
"v_mfma_f32_16x16x32_bf16 v[108:111], a[196:199], a[220:223], v[108:111]\n" \
"ds_read_b64_tr_b16 a[118:119], v210 offset:13312\n" \
"v_fma_f32 v245, v176, v177, v245\n" \
"v_mfma_f32_16x16x32_bf16 v[104:107], a[192:195], a[208:211], v[104:107]\n" \
"v_cvt_pk_bf16_f32 v176, v176, v177\n" \
"v_fma_f32 v245, v178, v179, v245\n" \
"v_mfma_f32_16x16x32_bf16 v[104:107], a[196:199], a[212:215], v[104:107]\n" \
"s_waitcnt lgkmcnt(2)\n" \
"global_load_lds_dwordx4 v253, s[48:49] offset:-2048\n" \
"v_cvt_pk_bf16_f32 v177, v178, v179\n" \
"v_mfma_f32_16x16x32_bf16 v[40:43], a[96:99], a[72:75], v[40:43]\n" \
"ds_read_b128 a[192:195], v205 offset:12288\n" \
"v_fma_f32 v245, v180, v181, v245\n" \
"v_mfma_f32_16x16x32_bf16 v[40:43], a[100:103], a[76:79], v[40:43]\n" \
"ds_read_b128 a[196:199], v206 offset:12288\n" \
"v_cvt_pk_bf16_f32 v178, v180, v181\n" \
"v_mfma_f32_16x16x32_bf16 v[0:3], a[104:107], a[72:75], v[0:3]\n" \
"ds_read_b64_tr_b16 a[208:209], v209 offset:4096\n" \
"v_fma_f32 v245, v182, v183, v245\n" \
"v_mfma_f32_16x16x32_bf16 v[0:3], a[108:111], a[76:79], v[0:3]\n" \
"s_waitcnt lgkmcnt(3)\n" \
"global_load_lds_dwordx4 v253, s[48:49] offset:0\n" \
"v_cvt_pk_bf16_f32 v179, v182, v183\n" \
"v_mfma_f32_16x16x32_bf16 v[4:7], a[112:115], a[72:75], v[4:7]\n" \
"s_add_u32 s48, s48, 0x60000\n" \
"s_addc_u32 s49, s49, 0\n" \
"ds_read_b64_tr_b16 a[210:211], v209 offset:5120\n" \
"v_mfma_f32_16x16x32_bf16 v[4:7], a[116:119], a[76:79], v[4:7]\n" \
"buffer_store_dwordx4 v[176:179], v235, s[56:59], s78 offen offset:0 sc1\n" \
"ds_read_b64_tr_b16 a[212:213], v209 offset:16384\n" \
"v_mul_f32 v184, v184, v234\n" \
"v_mul_f32 v185, v185, v234\n" \
"v_mul_f32 v186, v186, v234\n" \
"v_mfma_f32_16x16x32_bf16 v[12:15], a[112:115], a[80:83], v[12:15]\n" \
"ds_read_b64_tr_b16 a[214:215], v209 offset:17408\n" \
"v_mul_f32 v187, v187, v234\n" \
"v_mfma_f32_16x16x32_bf16 v[12:15], a[116:119], a[84:87], v[12:15]\n" \
"global_load_lds_dwordx4 v253, s[50:51] offset:-3072\n" \
"v_mul_f32 v188, v188, v234\n" \
"v_mfma_f32_16x16x32_bf16 v[8:11], a[104:107], a[80:83], v[8:11]\n" \
"ds_read_b64_tr_b16 a[216:217], v210 offset:4096\n" \
"v_mul_f32 v189, v189, v234\n" \
"v_mfma_f32_16x16x32_bf16 v[8:11], a[108:111], a[84:87], v[8:11]\n" \
"ds_read_b64_tr_b16 a[218:219], v210 offset:5120\n" \
"v_mul_f32 v190, v190, v234\n" \
"v_mfma_f32_16x16x32_bf16 v[48:51], a[96:99], a[80:83], v[48:51]\n" \
"ds_read_b64_tr_b16 a[220:221], v210 offset:16384\n" \
"v_mul_f32 v191, v191, v234\n" \
"v_mfma_f32_16x16x32_bf16 v[48:51], a[100:103], a[84:87], v[48:51]\n" \
"global_load_lds_dwordx4 v253, s[50:51] offset:-1024\n" \
"v_fma_f32 v245, v184, v185, v245\n" \
"v_mfma_f32_16x16x32_bf16 v[56:59], a[96:99], a[88:91], v[56:59]\n" \
"ds_read_b64_tr_b16 a[222:223], v210 offset:17408\n" \
"v_cvt_pk_bf16_f32 v184, v184, v185\n" \
"v_mfma_f32_16x16x32_bf16 v[56:59], a[100:103], a[92:95], v[56:59]\n" \
"ds_read_b64_tr_b16 a[200:201], v210 offset:2048\n" \
"v_fma_f32 v245, v186, v187, v245\n" \
"v_mfma_f32_16x16x32_bf16 v[16:19], a[104:107], a[88:91], v[16:19]\n" \
"ds_read_b64_tr_b16 a[202:203], v210 offset:3072\n" \
"v_cvt_pk_bf16_f32 v185, v186, v187\n" \
"v_mfma_f32_16x16x32_bf16 v[16:19], a[108:111], a[92:95], v[16:19]\n" \
"global_load_lds_dwordx4 v253, s[50:51] offset:1024\n" \
"v_fma_f32 v245, v188, v189, v245\n" \
"v_mfma_f32_16x16x32_bf16 v[20:23], a[112:115], a[88:91], v[20:23]\n" \
"s_add_u32 s50, s50, 0x60000\n" \
"s_addc_u32 s51, s51, 0\n" \
"ds_read_b64_tr_b16 a[204:205], v210 offset:14336\n" \
"v_mfma_f32_16x16x32_bf16 v[20:23], a[116:119], a[92:95], v[20:23]\n" \
"s_waitcnt lgkmcnt(3)\n" \
"ds_read_b64_tr_b16 a[206:207], v210 offset:15360\n" \
"v_cvt_pk_bf16_f32 v186, v188, v189\n" \
"v_mfma_f32_16x16x32_bf16 v[28:31], a[112:115], a[192:195], v[28:31]\n" \
"v_add_u32 v205, 0xffff3000, v205\n" \
"v_add_u32 v206, 0xffff3000, v206\n" \
"v_mfma_f32_16x16x32_bf16 v[28:31], a[116:119], a[196:199], v[28:31]\n" \
"v_add_u32 v209, 0xffff3000, v209\n" \
"v_add_u32 v210, 0xffff3000, v210\n" \
"v_mfma_f32_16x16x32_bf16 v[24:27], a[104:107], a[192:195], v[24:27]\n" \
"v_fma_f32 v245, v190, v191, v245\n" \
"v_cvt_pk_bf16_f32 v187, v190, v191\n" \
"v_mfma_f32_16x16x32_bf16 v[24:27], a[108:111], a[196:199], v[24:27]\n" \
"s_waitcnt vmcnt(13) lgkmcnt(0)\n" \
"buffer_store_dwordx4 v[184:187], v235, s[56:59], s78 offen offset:2048 sc1\n" \
"v_cmp_class_f32 vcc, v245, s94\n" \
"s_sub_u32 s78, s78, 536576\n" \
"v_mfma_f32_16x16x32_bf16 v[64:67], a[96:99], a[192:195], v[64:67]\n" \
"s_or_b64 s[92:93], s[92:93], vcc\n" \
"s_cbranch_scc1 Lexact_%=\n" \
"Lexr_%=:\n" \
"v_mfma_f32_16x16x32_bf16 v[64:67], a[100:103], a[196:199], v[64:67]\n" \
"s_barrier\n" \
"v_mfma_f32_16x16x32_bf16 v[80:83], a[72:75], a[208:211], v[80:83]\n" \
"ds_read_b64_tr_b16 a[96:97], v209 offset:2048\n" \
"v_mfma_f32_16x16x32_bf16 v[80:83], a[76:79], a[212:215], v[80:83]\n" \
"ds_read_b64_tr_b16 a[98:99], v209 offset:3072\n" \
"v_mfma_f32_16x16x32_bf16 v[84:87], a[72:75], a[216:219], v[84:87]\n" \
"s_add_u32 m0, s95, 118784\n" \
"ds_read_b64_tr_b16 a[100:101], v209 offset:14336\n" \
"v_mfma_f32_16x16x32_bf16 v[84:87], a[76:79], a[220:223], v[84:87]\n" \
"Lpg9179_%=:\n" \
"Lst1x3_%=:\n" \
"s_cbranch_execz Lpg11443_%=\n" \
"global_load_lds_dwordx4 v252, s[42:43] offset:0\n" \
"v_mfma_f32_16x16x32_bf16 v[44:47], a[200:203], a[72:75], v[44:47]\n" \
"ds_read_b64_tr_b16 a[102:103], v209 offset:15360\n" \
"v_mfma_f32_16x16x32_bf16 v[44:47], a[204:207], a[76:79], v[44:47]\n" \
"ds_read_b128 a[72:75], v205 offset:0\n" \
"ds_read_b64_tr_b16 a[104:105], v209 offset:0\n" \
"v_mfma_f32_16x16x32_bf16 v[52:55], a[200:203], a[80:83], v[52:55]\n" \
"ds_read_b128 a[76:79], v206 offset:0\n" \
"v_mfma_f32_16x16x32_bf16 v[52:55], a[204:207], a[84:87], v[52:55]\n" \
"global_load_lds_dwordx4 v252, s[42:43] offset:2048\n" \
"v_mfma_f32_16x16x32_bf16 v[92:95], a[80:83], a[216:219], v[92:95]\n" \
"s_add_u32 s42, s42, 0x80\n" \
"s_addc_u32 s43, s43, 0\n" \
"ds_read_b64_tr_b16 a[106:107], v209 offset:1024\n" \
"v_mfma_f32_16x16x32_bf16 v[92:95], a[84:87], a[220:223], v[92:95]\n" \
"ds_read_b64_tr_b16 a[108:109], v209 offset:12288\n" \
"v_mfma_f32_16x16x32_bf16 v[88:91], a[80:83], a[208:211], v[88:91]\n" \
"ds_read_b64_tr_b16 a[110:111], v209 offset:13312\n" \
"v_mfma_f32_16x16x32_bf16 v[88:91], a[84:87], a[212:215], v[88:91]\n" \
"global_load_lds_dwordx4 v252, s[44:45] offset:1024\n" \
"v_mfma_f32_16x16x32_bf16 v[96:99], a[88:91], a[208:211], v[96:99]\n" \
"ds_read_b128 a[80:83], v205 offset:4096\n" \
"v_mfma_f32_16x16x32_bf16 v[96:99], a[92:95], a[212:215], v[96:99]\n" \
"ds_read_b128 a[84:87], v206 offset:4096\n" \
"v_mfma_f32_16x16x32_bf16 v[100:103], a[88:91], a[216:219], v[100:103]\n" \
"ds_read_b64_tr_b16 a[112:113], v210 offset:0\n" \
"v_mfma_f32_16x16x32_bf16 v[100:103], a[92:95], a[220:223], v[100:103]\n" \
"global_load_lds_dwordx4 v252, s[44:45] offset:3072\n" \
"v_mfma_f32_16x16x32_bf16 v[60:63], a[200:203], a[88:91], v[60:63]\n" \
"s_add_u32 s44, s44, 0x80\n" \
"s_addc_u32 s45, s45, 0\n" \
"ds_read_b64_tr_b16 a[114:115], v210 offset:1024\n" \
"v_mfma_f32_16x16x32_bf16 v[60:63], a[204:207], a[92:95], v[60:63]\n" \
"ds_read_b64_tr_b16 a[116:117], v210 offset:12288\n" \
"v_mfma_f32_16x16x32_bf16 v[68:71], a[200:203], a[192:195], v[68:71]\n" \
"ds_read_b128 a[88:91], v205 offset:8192\n" \
"s_add_u32 m0, s84, 143360\n" \
"v_mfma_f32_16x16x32_bf16 v[68:71], a[204:207], a[196:199], v[68:71]\n" \
"global_load_lds_dwordx4 v253, s[48:49] offset:-4096\n" \
"v_mfma_f32_16x16x32_bf16 v[108:111], a[192:195], a[216:219], v[108:111]\n" \
"ds_read_b128 a[92:95], v206 offset:8192\n" \
"v_mfma_f32_16x16x32_bf16 v[108:111], a[196:199], a[220:223], v[108:111]\n" \
"ds_read_b64_tr_b16 a[118:119], v210 offset:13312\n" \
"v_mfma_f32_16x16x32_bf16 v[104:107], a[192:195], a[208:211], v[104:107]\n" \
"v_mfma_f32_16x16x32_bf16 v[104:107], a[196:199], a[212:215], v[104:107]\n" \
"s_waitcnt lgkmcnt(2)\n" \
"global_load_lds_dwordx4 v253, s[48:49] offset:-2048\n" \
"v_mfma_f32_16x16x32_bf16 v[40:43], a[96:99], a[72:75], v[40:43]\n" \
"ds_read_b128 a[192:195], v205 offset:12288\n" \
"v_mfma_f32_16x16x32_bf16 v[40:43], a[100:103], a[76:79], v[40:43]\n" \
"ds_read_b128 a[196:199], v206 offset:12288\n" \
"v_mfma_f32_16x16x32_bf16 v[0:3], a[104:107], a[72:75], v[0:3]\n" \
"ds_read_b64_tr_b16 a[208:209], v209 offset:4096\n" \
"v_mfma_f32_16x16x32_bf16 v[0:3], a[108:111], a[76:79], v[0:3]\n" \
"s_waitcnt lgkmcnt(3)\n" \
"global_load_lds_dwordx4 v253, s[48:49] offset:0\n" \
"v_mfma_f32_16x16x32_bf16 v[4:7], a[112:115], a[72:75], v[4:7]\n" \
"s_add_u32 s48, s48, 0x60000\n" \
"s_addc_u32 s49, s49, 0\n" \
"ds_read_b64_tr_b16 a[210:211], v209 offset:5120\n" \
"v_mfma_f32_16x16x32_bf16 v[4:7], a[116:119], a[76:79], v[4:7]\n" \
"ds_read_b64_tr_b16 a[212:213], v209 offset:16384\n" \
"v_mfma_f32_16x16x32_bf16 v[12:15], a[112:115], a[80:83], v[12:15]\n" \
"ds_read_b64_tr_b16 a[214:215], v209 offset:17408\n" \
"v_mfma_f32_16x16x32_bf16 v[12:15], a[116:119], a[84:87], v[12:15]\n" \
"global_load_lds_dwordx4 v253, s[50:51] offset:-3072\n" \
"v_mfma_f32_16x16x32_bf16 v[8:11], a[104:107], a[80:83], v[8:11]\n" \
"ds_read_b64_tr_b16 a[216:217], v210 offset:4096\n" \
"v_mfma_f32_16x16x32_bf16 v[8:11], a[108:111], a[84:87], v[8:11]\n" \
"ds_read_b64_tr_b16 a[218:219], v210 offset:5120\n" \
"v_mfma_f32_16x16x32_bf16 v[48:51], a[96:99], a[80:83], v[48:51]\n" \
"ds_read_b64_tr_b16 a[220:221], v210 offset:16384\n" \
"v_mfma_f32_16x16x32_bf16 v[48:51], a[100:103], a[84:87], v[48:51]\n" \
"global_load_lds_dwordx4 v253, s[50:51] offset:-1024\n" \
"v_mfma_f32_16x16x32_bf16 v[56:59], a[96:99], a[88:91], v[56:59]\n" \
"ds_read_b64_tr_b16 a[222:223], v210 offset:17408\n" \
"v_mfma_f32_16x16x32_bf16 v[56:59], a[100:103], a[92:95], v[56:59]\n" \
"ds_read_b64_tr_b16 a[200:201], v210 offset:2048\n" \
"v_mfma_f32_16x16x32_bf16 v[16:19], a[104:107], a[88:91], v[16:19]\n" \
"ds_read_b64_tr_b16 a[202:203], v210 offset:3072\n" \
"v_mfma_f32_16x16x32_bf16 v[16:19], a[108:111], a[92:95], v[16:19]\n" \
"global_load_lds_dwordx4 v253, s[50:51] offset:1024\n" \
"v_mfma_f32_16x16x32_bf16 v[20:23], a[112:115], a[88:91], v[20:23]\n" \
"s_add_u32 s50, s50, 0x60000\n" \
"s_addc_u32 s51, s51, 0\n" \
"ds_read_b64_tr_b16 a[204:205], v210 offset:14336\n" \
"v_mfma_f32_16x16x32_bf16 v[20:23], a[116:119], a[92:95], v[20:23]\n" \
"s_waitcnt lgkmcnt(3)\n" \
"ds_read_b64_tr_b16 a[206:207], v210 offset:15360\n" \
"v_mfma_f32_16x16x32_bf16 v[28:31], a[112:115], a[192:195], v[28:31]\n" \
"v_add_u32 v205, 0xffff0000, v205\n" \
"v_add_u32 v206, 0xffff0000, v206\n" \
"v_mfma_f32_16x16x32_bf16 v[28:31], a[116:119], a[196:199], v[28:31]\n" \
"v_add_u32 v209, 0xffff0000, v209\n" \
"v_add_u32 v210, 0xffff0000, v210\n" \
"v_mfma_f32_16x16x32_bf16 v[24:27], a[104:107], a[192:195], v[24:27]\n" \
"s_lshl_b32 s70, s63, 7\n" \
"s_lshl_b32 s71, s62, 2\n" \
"v_mfma_f32_16x16x32_bf16 v[24:27], a[108:111], a[196:199], v[24:27]\n" \
"s_waitcnt vmcnt(10) lgkmcnt(0)\n" \
"s_add_u32 s71, s71, 65589248\n" \
"s_add_u32 s72, s71, s70\n" \
"v_mfma_f32_16x16x32_bf16 v[64:67], a[96:99], a[192:195], v[64:67]\n" \
"s_sub_u32 s71, s71, s70\n" \
"s_add_u32 s71, s71, 1920\n" \
"v_mov_b32 v236, s60\n" \
"v_mfma_f32_16x16x32_bf16 v[64:67], a[100:103], a[196:199], v[64:67]\n" \
"s_barrier\n" \
"v_mfma_f32_16x16x32_bf16 v[80:83], a[72:75], a[208:211], v[80:83]\n" \
"ds_read_b64_tr_b16 a[96:97], v209 offset:2048\n" \
"v_lshlrev_b32 v237, 28, s61\n" \
"v_mfma_f32_16x16x32_bf16 v[80:83], a[76:79], a[212:215], v[80:83]\n" \
"buffer_store_dword v236, v237, s[56:59], s72 offen sc1\n" \
"ds_read_b64_tr_b16 a[98:99], v209 offset:3072\n" \
"v_mfma_f32_16x16x32_bf16 v[84:87], a[72:75], a[216:219], v[84:87]\n" \
"s_add_u32 m0, s95, 65536\n" \
"ds_read_b64_tr_b16 a[100:101], v209 offset:14336\n" \
"v_mfma_f32_16x16x32_bf16 v[84:87], a[76:79], a[220:223], v[84:87]\n" \
"buffer_store_dword v236, v237, s[56:59], s71 offen sc1\n" \
"global_load_lds_dwordx4 v252, s[42:43] offset:0\n" \
"v_mfma_f32_16x16x32_bf16 v[44:47], a[200:203], a[72:75], v[44:47]\n" \
"s_lshl_b32 s87, s85, 12\n" \
"ds_read_b64_tr_b16 a[102:103], v209 offset:15360\n" \
"v_mfma_f32_16x16x32_bf16 v[44:47], a[204:207], a[76:79], v[44:47]\n" \
"s_and_b32 s89, s61, 1\n" \
"ds_read_b128 a[72:75], v205 offset:0\n" \
"ds_read_b64_tr_b16 a[104:105], v209 offset:0\n" \
"v_mfma_f32_16x16x32_bf16 v[52:55], a[200:203], a[80:83], v[52:55]\n" \
"ds_read_b128 a[76:79], v206 offset:0\n" \
"s_lshl_b32 s89, s89, 11\n" \
"v_mfma_f32_16x16x32_bf16 v[52:55], a[204:207], a[84:87], v[52:55]\n" \
"global_load_lds_dwordx4 v252, s[42:43] offset:2048\n" \
"v_mfma_f32_16x16x32_bf16 v[92:95], a[80:83], a[216:219], v[92:95]\n" \
"s_add_u32 s42, s42, 0x80\n" \
"s_addc_u32 s43, s43, 0\n" \
"ds_read_b64_tr_b16 a[106:107], v209 offset:1024\n" \
"v_mfma_f32_16x16x32_bf16 v[92:95], a[84:87], a[220:223], v[92:95]\n" \
"s_add_u32 s87, s87, s89\n" \
"ds_read_b64_tr_b16 a[108:109], v209 offset:12288\n" \
"v_mfma_f32_16x16x32_bf16 v[88:91], a[80:83], a[208:211], v[88:91]\n" \
"s_add_u32 s87, s87, 52740096\n" \
"ds_read_b64_tr_b16 a[110:111], v209 offset:13312\n" \
"v_mfma_f32_16x16x32_bf16 v[88:91], a[84:87], a[212:215], v[88:91]\n" \
"global_load_lds_dwordx4 v252, s[44:45] offset:1024\n" \
"v_mfma_f32_16x16x32_bf16 v[96:99], a[88:91], a[208:211], v[96:99]\n" \
"ds_read_b128 a[80:83], v205 offset:4096\n" \
"s_mul_i32 s89, s88, 278528\n" \
"v_mfma_f32_16x16x32_bf16 v[96:99], a[92:95], a[212:215], v[96:99]\n" \
"ds_read_b128 a[84:87], v206 offset:4096\n" \
"s_add_u32 s87, s87, s89\n" \
"v_mfma_f32_16x16x32_bf16 v[100:103], a[88:91], a[216:219], v[100:103]\n" \
"s_lshr_b32 s89, s61, 1\n" \
"ds_read_b64_tr_b16 a[112:113], v210 offset:0\n" \
"v_mfma_f32_16x16x32_bf16 v[100:103], a[92:95], a[220:223], v[100:103]\n" \
"global_load_lds_dwordx4 v252, s[44:45] offset:3072\n" \
"v_mfma_f32_16x16x32_bf16 v[60:63], a[200:203], a[88:91], v[60:63]\n" \
"s_add_u32 s44, s44, 0x80\n" \
"s_addc_u32 s45, s45, 0\n" \
"ds_read_b64_tr_b16 a[114:115], v210 offset:1024\n" \
"v_mfma_f32_16x16x32_bf16 v[60:63], a[204:207], a[92:95], v[60:63]\n" \
"s_lshl_b32 s90, s89, 8\n" \
"ds_read_b64_tr_b16 a[116:117], v210 offset:12288\n" \
"v_mfma_f32_16x16x32_bf16 v[68:71], a[200:203], a[192:195], v[68:71]\n" \
"ds_read_b128 a[88:91], v205 offset:8192\n" \
"s_add_u32 m0, s84, 90112\n" \
"v_mfma_f32_16x16x32_bf16 v[68:71], a[204:207], a[196:199], v[68:71]\n" \
"global_load_lds_dwordx4 v253, s[48:49] offset:-4096\n" \
"v_mfma_f32_16x16x32_bf16 v[108:111], a[192:195], a[216:219], v[108:111]\n" \
"ds_read_b128 a[92:95], v206 offset:8192\n" \
"s_add_u32 s87, s87, s90\n" \
"v_mfma_f32_16x16x32_bf16 v[108:111], a[196:199], a[220:223], v[108:111]\n" \
"s_lshl_b32 s46, s88, 4\n" \
"ds_read_b64_tr_b16 a[118:119], v210 offset:13312\n" \
"v_mfma_f32_16x16x32_bf16 v[104:107], a[192:195], a[208:211], v[104:107]\n" \
"s_add_u32 s46, s46, s85\n" \
"v_mfma_f32_16x16x32_bf16 v[104:107], a[196:199], a[212:215], v[104:107]\n" \
"s_waitcnt lgkmcnt(2)\n" \
"global_load_lds_dwordx4 v253, s[48:49] offset:-2048\n" \
"v_mfma_f32_16x16x32_bf16 v[40:43], a[96:99], a[72:75], v[40:43]\n" \
"ds_read_b128 a[192:195], v205 offset:12288\n" \
"s_lshl_b32 s46, s46, 1\n" \
"v_mfma_f32_16x16x32_bf16 v[40:43], a[100:103], a[76:79], v[40:43]\n" \
"ds_read_b128 a[196:199], v206 offset:12288\n" \
"s_add_u32 s46, s46, s89\n" \
"v_mfma_f32_16x16x32_bf16 v[0:3], a[104:107], a[72:75], v[0:3]\n" \
"s_lshl_b32 s46, s46, 14\n" \
"ds_read_b64_tr_b16 a[208:209], v209 offset:4096\n" \
"v_mfma_f32_16x16x32_bf16 v[0:3], a[108:111], a[76:79], v[0:3]\n" \
"s_waitcnt lgkmcnt(3)\n" \
"global_load_lds_dwordx4 v253, s[48:49] offset:0\n" \
"v_mfma_f32_16x16x32_bf16 v[4:7], a[112:115], a[72:75], v[4:7]\n" \
"s_add_u32 s48, s48, 0x60000\n" \
"s_addc_u32 s49, s49, 0\n" \
"ds_read_b64_tr_b16 a[210:211], v209 offset:5120\n" \
"v_mfma_f32_16x16x32_bf16 v[4:7], a[116:119], a[76:79], v[4:7]\n" \
"s_and_b32 s90, s95, 0x1000\n" \
"ds_read_b64_tr_b16 a[212:213], v209 offset:16384\n" \
"v_mfma_f32_16x16x32_bf16 v[12:15], a[112:115], a[80:83], v[12:15]\n" \
"s_add_u32 s46, s46, s90\n" \
"ds_read_b64_tr_b16 a[214:215], v209 offset:17408\n" \
"v_mfma_f32_16x16x32_bf16 v[12:15], a[116:119], a[84:87], v[12:15]\n" \
"global_load_lds_dwordx4 v253, s[50:51] offset:-3072\n" \
"v_mfma_f32_16x16x32_bf16 v[8:11], a[104:107], a[80:83], v[8:11]\n" \
"s_add_u32 s46, s46, 57204736\n" \
"ds_read_b64_tr_b16 a[216:217], v210 offset:4096\n" \
"v_mfma_f32_16x16x32_bf16 v[8:11], a[108:111], a[84:87], v[8:11]\n" \
"s_sub_u32 s47, s46, 524288\n" \
"ds_read_b64_tr_b16 a[218:219], v210 offset:5120\n" \
"v_mfma_f32_16x16x32_bf16 v[48:51], a[96:99], a[80:83], v[48:51]\n" \
"ds_read_b64_tr_b16 a[220:221], v210 offset:16384\n" \
"v_mfma_f32_16x16x32_bf16 v[48:51], a[100:103], a[84:87], v[48:51]\n" \
"global_load_lds_dwordx4 v253, s[50:51] offset:-1024\n" \
"v_mfma_f32_16x16x32_bf16 v[56:59], a[96:99], a[88:91], v[56:59]\n" \
"ds_read_b64_tr_b16 a[222:223], v210 offset:17408\n" \
"v_mfma_f32_16x16x32_bf16 v[56:59], a[100:103], a[92:95], v[56:59]\n" \
"ds_read_b64_tr_b16 a[200:201], v210 offset:2048\n" \
"v_mfma_f32_16x16x32_bf16 v[16:19], a[104:107], a[88:91], v[16:19]\n" \
"ds_read_b64_tr_b16 a[202:203], v210 offset:3072\n" \
"v_mfma_f32_16x16x32_bf16 v[16:19], a[108:111], a[92:95], v[16:19]\n" \
"global_load_lds_dwordx4 v253, s[50:51] offset:1024\n" \
"v_mfma_f32_16x16x32_bf16 v[20:23], a[112:115], a[88:91], v[20:23]\n" \
"s_add_u32 s50, s50, 0x60000\n" \
"s_addc_u32 s51, s51, 0\n" \
"ds_read_b64_tr_b16 a[204:205], v210 offset:14336\n" \
"v_mfma_f32_16x16x32_bf16 v[20:23], a[116:119], a[92:95], v[20:23]\n" \
"s_waitcnt lgkmcnt(3)\n" \
"ds_read_b64_tr_b16 a[206:207], v210 offset:15360\n" \
"v_mfma_f32_16x16x32_bf16 v[28:31], a[112:115], a[192:195], v[28:31]\n" \
"v_add_u32 v205, 0x1d000, v205\n" \
"v_add_u32 v206, 0x1d000, v206\n" \
"v_mfma_f32_16x16x32_bf16 v[28:31], a[116:119], a[196:199], v[28:31]\n" \
"v_add_u32 v209, 0x1d000, v209\n" \
"v_add_u32 v210, 0x1d000, v210\n" \
"v_mfma_f32_16x16x32_bf16 v[24:27], a[104:107], a[192:195], v[24:27]\n" \
"s_cmp_eq_u32 s88, 0\n" \
"s_cselect_b32 s86, -1, 0\n" \
"s_cselect_b32 s47, 0x20000000, s47\n" \
"v_mfma_f32_16x16x32_bf16 v[24:27], a[108:111], a[196:199], v[24:27]\n" \
"s_waitcnt vmcnt(12) lgkmcnt(0)\n" \
"v_mfma_f32_16x16x32_bf16 v[64:67], a[96:99], a[192:195], v[64:67]\n" \
"v_mfma_f32_16x16x32_bf16 v[64:67], a[100:103], a[196:199], v[64:67]\n" \
"s_barrier\n" \
"v_mfma_f32_16x16x32_bf16 v[80:83], a[72:75], a[208:211], v[80:83]\n" \
"ds_read_b64_tr_b16 a[96:97], v209 offset:2048\n" \
"v_add_u32 v208, 0x20000000, v248\n" \
"v_mfma_f32_16x16x32_bf16 v[80:83], a[76:79], a[212:215], v[80:83]\n" \
"s_mov_b32 s12, 0xff00ff00\n" \
"ds_read_b64_tr_b16 a[98:99], v209 offset:3072\n" \
"v_mfma_f32_16x16x32_bf16 v[84:87], a[72:75], a[216:219], v[84:87]\n" \
"s_add_u32 m0, s95, 0\n" \
"ds_read_b64_tr_b16 a[100:101], v209 offset:14336\n" \
"v_mfma_f32_16x16x32_bf16 v[84:87], a[76:79], a[220:223], v[84:87]\n" \
"global_load_lds_dwordx4 v252, s[42:43] offset:0\n" \
"v_mfma_f32_16x16x32_bf16 v[44:47], a[200:203], a[72:75], v[44:47]\n" \
"s_mov_b32 s13, s12\n" \
"ds_read_b64_tr_b16 a[102:103], v209 offset:15360\n" \
"v_mfma_f32_16x16x32_bf16 v[44:47], a[204:207], a[76:79], v[44:47]\n" \
"s_mov_b32 s14, 0\n" \
"ds_read_b128 a[72:75], v205 offset:0\n" \
"ds_read_b64_tr_b16 a[104:105], v209 offset:0\n" \
"v_mfma_f32_16x16x32_bf16 v[52:55], a[200:203], a[80:83], v[52:55]\n" \
"s_mov_b32 s15, -1\n" \
"ds_read_b128 a[76:79], v206 offset:0\n" \
"v_cndmask_b32 v207, v208, v248, s[12:13]\n" \
"v_mfma_f32_16x16x32_bf16 v[52:55], a[204:207], a[84:87], v[52:55]\n" \
"global_load_lds_dwordx4 v252, s[42:43] offset:2048\n" \
"v_cndmask_b32 v223, v208, v248, s[14:15]\n" \
"v_mfma_f32_16x16x32_bf16 v[92:95], a[80:83], a[216:219], v[92:95]\n" \
"s_add_u32 s42, s42, 0x80\n" \
"s_addc_u32 s43, s43, 0\n" \
"ds_read_b64_tr_b16 a[106:107], v209 offset:1024\n" \
"v_mfma_f32_16x16x32_bf16 v[92:95], a[84:87], a[220:223], v[92:95]\n" \
"ds_read_b64_tr_b16 a[108:109], v209 offset:12288\n" \
"v_min_u32 v208, v207, v223\n" \
"v_mfma_f32_16x16x32_bf16 v[88:91], a[80:83], a[208:211], v[88:91]\n" \
"ds_read_b64_tr_b16 a[110:111], v209 offset:13312\n" \
"v_mfma_f32_16x16x32_bf16 v[88:91], a[84:87], a[212:215], v[88:91]\n" \
"global_load_lds_dwordx4 v252, s[44:45] offset:1024\n" \
"v_mfma_f32_16x16x32_bf16 v[96:99], a[88:91], a[208:211], v[96:99]\n" \
"ds_read_b128 a[80:83], v205 offset:4096\n" \
"v_mfma_f32_16x16x32_bf16 v[96:99], a[92:95], a[212:215], v[96:99]\n" \
"ds_read_b128 a[84:87], v206 offset:4096\n" \
"v_mfma_f32_16x16x32_bf16 v[100:103], a[88:91], a[216:219], v[100:103]\n" \
"ds_read_b64_tr_b16 a[112:113], v210 offset:0\n" \
"v_mfma_f32_16x16x32_bf16 v[100:103], a[92:95], a[220:223], v[100:103]\n" \
"global_load_lds_dwordx4 v252, s[44:45] offset:3072\n" \
"v_mfma_f32_16x16x32_bf16 v[60:63], a[200:203], a[88:91], v[60:63]\n" \
"s_add_u32 s44, s44, 0x80\n" \
"s_addc_u32 s45, s45, 0\n" \
"ds_read_b64_tr_b16 a[114:115], v210 offset:1024\n" \
"v_mfma_f32_16x16x32_bf16 v[60:63], a[204:207], a[92:95], v[60:63]\n" \
"ds_read_b64_tr_b16 a[116:117], v210 offset:12288\n" \
"v_mfma_f32_16x16x32_bf16 v[68:71], a[200:203], a[192:195], v[68:71]\n" \
"ds_read_b128 a[88:91], v205 offset:8192\n" \
"s_add_u32 m0, s84, 24576\n" \
"v_mfma_f32_16x16x32_bf16 v[68:71], a[204:207], a[196:199], v[68:71]\n" \
"global_load_lds_dwordx4 v253, s[48:49] offset:-4096\n" \
"v_mfma_f32_16x16x32_bf16 v[108:111], a[192:195], a[216:219], v[108:111]\n" \
"ds_read_b128 a[92:95], v206 offset:8192\n" \
"v_mfma_f32_16x16x32_bf16 v[108:111], a[196:199], a[220:223], v[108:111]\n" \
"ds_read_b64_tr_b16 a[118:119], v210 offset:13312\n" \
"v_mfma_f32_16x16x32_bf16 v[104:107], a[192:195], a[208:211], v[104:107]\n" \
"v_mfma_f32_16x16x32_bf16 v[104:107], a[196:199], a[212:215], v[104:107]\n" \
"s_waitcnt lgkmcnt(2)\n" \
"global_load_lds_dwordx4 v253, s[48:49] offset:-2048\n" \
"v_mfma_f32_16x16x32_bf16 v[40:43], a[96:99], a[72:75], v[40:43]\n" \
"s_mov_b32 s30, 65593856\n" \
"ds_read_b128 a[192:195], v205 offset:12288\n" \
"v_lshrrev_b32 v228, 1, v248\n" \
"v_mfma_f32_16x16x32_bf16 v[40:43], a[100:103], a[76:79], v[40:43]\n" \
"ds_read_b128 a[196:199], v206 offset:12288\n" \
"v_mfma_f32_16x16x32_bf16 v[0:3], a[104:107], a[72:75], v[0:3]\n" \
"ds_read_b64_tr_b16 a[208:209], v209 offset:4096\n" \
"v_mfma_f32_16x16x32_bf16 v[0:3], a[108:111], a[76:79], v[0:3]\n" \
"s_waitcnt lgkmcnt(3)\n" \
"global_load_lds_dwordx4 v253, s[48:49] offset:0\n" \
"v_mfma_f32_16x16x32_bf16 v[4:7], a[112:115], a[72:75], v[4:7]\n" \
"s_add_u32 s48, s48, 0x60000\n" \
"s_addc_u32 s49, s49, 0\n" \
"ds_read_b64_tr_b16 a[210:211], v209 offset:5120\n" \
"v_mfma_f32_16x16x32_bf16 v[4:7], a[116:119], a[76:79], v[4:7]\n" \
"ds_read_b64_tr_b16 a[212:213], v209 offset:16384\n" \
"v_mfma_f32_16x16x32_bf16 v[12:15], a[112:115], a[80:83], v[12:15]\n" \
"ds_read_b64_tr_b16 a[214:215], v209 offset:17408\n" \
"v_mfma_f32_16x16x32_bf16 v[12:15], a[116:119], a[84:87], v[12:15]\n" \
"global_load_lds_dwordx4 v253, s[50:51] offset:-3072\n" \
"v_mfma_f32_16x16x32_bf16 v[8:11], a[104:107], a[80:83], v[8:11]\n" \
"ds_read_b64_tr_b16 a[216:217], v210 offset:4096\n" \
"v_mfma_f32_16x16x32_bf16 v[8:11], a[108:111], a[84:87], v[8:11]\n" \
"ds_read_b64_tr_b16 a[218:219], v210 offset:5120\n" \
"v_mfma_f32_16x16x32_bf16 v[48:51], a[96:99], a[80:83], v[48:51]\n" \
"ds_read_b64_tr_b16 a[220:221], v210 offset:16384\n" \
"v_mfma_f32_16x16x32_bf16 v[48:51], a[100:103], a[84:87], v[48:51]\n" \
"global_load_lds_dwordx4 v253, s[50:51] offset:-1024\n" \
"v_mfma_f32_16x16x32_bf16 v[56:59], a[96:99], a[88:91], v[56:59]\n" \
"ds_read_b64_tr_b16 a[222:223], v210 offset:17408\n" \
"v_mfma_f32_16x16x32_bf16 v[56:59], a[100:103], a[92:95], v[56:59]\n" \
"ds_read_b64_tr_b16 a[200:201], v210 offset:2048\n" \
"v_mfma_f32_16x16x32_bf16 v[16:19], a[104:107], a[88:91], v[16:19]\n" \
"ds_read_b64_tr_b16 a[202:203], v210 offset:3072\n" \
"v_mfma_f32_16x16x32_bf16 v[16:19], a[108:111], a[92:95], v[16:19]\n" \
"global_load_lds_dwordx4 v253, s[50:51] offset:1024\n" \
"v_mfma_f32_16x16x32_bf16 v[20:23], a[112:115], a[88:91], v[20:23]\n" \
"s_add_u32 s50, s50, 0x60000\n" \
"s_addc_u32 s51, s51, 0\n" \
"ds_read_b64_tr_b16 a[204:205], v210 offset:14336\n" \
"v_mfma_f32_16x16x32_bf16 v[20:23], a[116:119], a[92:95], v[20:23]\n" \
"s_waitcnt lgkmcnt(3)\n" \
"ds_read_b64_tr_b16 a[206:207], v210 offset:15360\n" \
"v_mfma_f32_16x16x32_bf16 v[28:31], a[112:115], a[192:195], v[28:31]\n" \
"v_add_u32 v205, 0xffff3000, v205\n" \
"v_add_u32 v206, 0xffff3000, v206\n" \
"v_mfma_f32_16x16x32_bf16 v[28:31], a[116:119], a[196:199], v[28:31]\n" \
"v_add_u32 v209, 0xffff3000, v209\n" \
"v_add_u32 v210, 0xffff3000, v210\n" \
"v_mfma_f32_16x16x32_bf16 v[24:27], a[104:107], a[192:195], v[24:27]\n" \
"v_mfma_f32_16x16x32_bf16 v[24:27], a[108:111], a[196:199], v[24:27]\n" \
"s_waitcnt vmcnt(10) lgkmcnt(0)\n" \
"v_mfma_f32_16x16x32_bf16 v[64:67], a[96:99], a[192:195], v[64:67]\n" \
"v_mfma_f32_16x16x32_bf16 v[64:67], a[100:103], a[196:199], v[64:67]\n" \
"s_barrier\n" \
"v_mfma_f32_16x16x32_bf16 v[80:83], a[72:75], a[208:211], v[80:83]\n" \
"ds_read_b64_tr_b16 a[96:97], v209 offset:2048\n" \
"v_mfma_f32_16x16x32_bf16 v[80:83], a[76:79], a[212:215], v[80:83]\n" \
"ds_read_b64_tr_b16 a[98:99], v209 offset:3072\n" \
"v_mfma_f32_16x16x32_bf16 v[84:87], a[72:75], a[216:219], v[84:87]\n" \
"s_add_u32 m0, s95, 118784\n" \
"ds_read_b64_tr_b16 a[100:101], v209 offset:14336\n" \
"v_mfma_f32_16x16x32_bf16 v[84:87], a[76:79], a[220:223], v[84:87]\n" \
"global_load_lds_dwordx4 v252, s[42:43] offset:0\n" \
"v_mfma_f32_16x16x32_bf16 v[44:47], a[200:203], a[72:75], v[44:47]\n" \
"ds_read_b64_tr_b16 a[102:103], v209 offset:15360\n" \
"v_mfma_f32_16x16x32_bf16 v[44:47], a[204:207], a[76:79], v[44:47]\n" \
"ds_read_b128 a[72:75], v205 offset:0\n" \
"ds_read_b64_tr_b16 a[104:105], v209 offset:0\n" \
"v_mfma_f32_16x16x32_bf16 v[52:55], a[200:203], a[80:83], v[52:55]\n" \
"ds_read_b128 a[76:79], v206 offset:0\n" \
"v_mfma_f32_16x16x32_bf16 v[52:55], a[204:207], a[84:87], v[52:55]\n" \
"global_load_lds_dwordx4 v252, s[42:43] offset:2048\n" \
"v_mfma_f32_16x16x32_bf16 v[92:95], a[80:83], a[216:219], v[92:95]\n" \
"s_add_u32 s42, s42, 0x80\n" \
"s_addc_u32 s43, s43, 0\n" \
"ds_read_b64_tr_b16 a[106:107], v209 offset:1024\n" \
"v_mfma_f32_16x16x32_bf16 v[92:95], a[84:87], a[220:223], v[92:95]\n" \
"ds_read_b64_tr_b16 a[108:109], v209 offset:12288\n" \
"v_mfma_f32_16x16x32_bf16 v[88:91], a[80:83], a[208:211], v[88:91]\n" \
"ds_read_b64_tr_b16 a[110:111], v209 offset:13312\n" \
"v_mfma_f32_16x16x32_bf16 v[88:91], a[84:87], a[212:215], v[88:91]\n" \
"global_load_lds_dwordx4 v252, s[44:45] offset:1024\n" \
"v_mfma_f32_16x16x32_bf16 v[96:99], a[88:91], a[208:211], v[96:99]\n" \
"ds_read_b128 a[80:83], v205 offset:4096\n" \
"v_mfma_f32_16x16x32_bf16 v[96:99], a[92:95], a[212:215], v[96:99]\n" \
"ds_read_b128 a[84:87], v206 offset:4096\n" \
"v_mfma_f32_16x16x32_bf16 v[100:103], a[88:91], a[216:219], v[100:103]\n" \
"ds_read_b64_tr_b16 a[112:113], v210 offset:0\n" \
"v_mfma_f32_16x16x32_bf16 v[100:103], a[92:95], a[220:223], v[100:103]\n" \
"global_load_lds_dwordx4 v252, s[44:45] offset:3072\n" \
"v_mfma_f32_16x16x32_bf16 v[60:63], a[200:203], a[88:91], v[60:63]\n" \
"s_add_u32 s44, s44, 0x80\n" \
"s_addc_u32 s45, s45, 0\n" \
"ds_read_b64_tr_b16 a[114:115], v210 offset:1024\n" \
"v_mfma_f32_16x16x32_bf16 v[60:63], a[204:207], a[92:95], v[60:63]\n" \
"ds_read_b64_tr_b16 a[116:117], v210 offset:12288\n" \
"v_mfma_f32_16x16x32_bf16 v[68:71], a[200:203], a[192:195], v[68:71]\n" \
"ds_read_b128 a[88:91], v205 offset:8192\n" \
"s_add_u32 m0, s84, 143360\n" \
"v_mfma_f32_16x16x32_bf16 v[68:71], a[204:207], a[196:199], v[68:71]\n" \
"global_load_lds_dwordx4 v253, s[48:49] offset:-4096\n" \
"v_mfma_f32_16x16x32_bf16 v[108:111], a[192:195], a[216:219], v[108:111]\n" \
"ds_read_b128 a[92:95], v206 offset:8192\n" \
"v_mfma_f32_16x16x32_bf16 v[108:111], a[196:199], a[220:223], v[108:111]\n" \
"ds_read_b64_tr_b16 a[118:119], v210 offset:13312\n" \
"v_mfma_f32_16x16x32_bf16 v[104:107], a[192:195], a[208:211], v[104:107]\n" \
"v_mfma_f32_16x16x32_bf16 v[104:107], a[196:199], a[212:215], v[104:107]\n" \
"s_waitcnt lgkmcnt(2)\n" \
"global_load_lds_dwordx4 v253, s[48:49] offset:-2048\n" \
"v_mfma_f32_16x16x32_bf16 v[40:43], a[96:99], a[72:75], v[40:43]\n" \
"ds_read_b128 a[192:195], v205 offset:12288\n" \
"v_mfma_f32_16x16x32_bf16 v[40:43], a[100:103], a[76:79], v[40:43]\n" \
"ds_read_b128 a[196:199], v206 offset:12288\n" \
"v_mfma_f32_16x16x32_bf16 v[0:3], a[104:107], a[72:75], v[0:3]\n" \
"ds_read_b64_tr_b16 a[208:209], v209 offset:4096\n" \
"v_mfma_f32_16x16x32_bf16 v[0:3], a[108:111], a[76:79], v[0:3]\n" \
"s_waitcnt lgkmcnt(3)\n" \
"global_load_lds_dwordx4 v253, s[48:49] offset:0\n" \
"v_mfma_f32_16x16x32_bf16 v[4:7], a[112:115], a[72:75], v[4:7]\n" \
"s_add_u32 s48, s48, 0x60000\n" \
"s_addc_u32 s49, s49, 0\n" \
"ds_read_b64_tr_b16 a[210:211], v209 offset:5120\n" \
"v_mfma_f32_16x16x32_bf16 v[4:7], a[116:119], a[76:79], v[4:7]\n" \
"ds_read_b64_tr_b16 a[212:213], v209 offset:16384\n" \
"v_mfma_f32_16x16x32_bf16 v[12:15], a[112:115], a[80:83], v[12:15]\n" \
"ds_read_b64_tr_b16 a[214:215], v209 offset:17408\n" \
"v_mfma_f32_16x16x32_bf16 v[12:15], a[116:119], a[84:87], v[12:15]\n" \
"global_load_lds_dwordx4 v253, s[50:51] offset:-3072\n" \
"v_mfma_f32_16x16x32_bf16 v[8:11], a[104:107], a[80:83], v[8:11]\n" \
"ds_read_b64_tr_b16 a[216:217], v210 offset:4096\n" \
"v_mfma_f32_16x16x32_bf16 v[8:11], a[108:111], a[84:87], v[8:11]\n" \
"ds_read_b64_tr_b16 a[218:219], v210 offset:5120\n" \
"v_mfma_f32_16x16x32_bf16 v[48:51], a[96:99], a[80:83], v[48:51]\n" \
"ds_read_b64_tr_b16 a[220:221], v210 offset:16384\n" \
"v_mfma_f32_16x16x32_bf16 v[48:51], a[100:103], a[84:87], v[48:51]\n" \
"global_load_lds_dwordx4 v253, s[50:51] offset:-1024\n" \
"v_mfma_f32_16x16x32_bf16 v[56:59], a[96:99], a[88:91], v[56:59]\n" \
"ds_read_b64_tr_b16 a[222:223], v210 offset:17408\n" \
"v_mfma_f32_16x16x32_bf16 v[56:59], a[100:103], a[92:95], v[56:59]\n" \
"ds_read_b64_tr_b16 a[200:201], v210 offset:2048\n" \
"v_mfma_f32_16x16x32_bf16 v[16:19], a[104:107], a[88:91], v[16:19]\n" \
"ds_read_b64_tr_b16 a[202:203], v210 offset:3072\n" \
"v_mfma_f32_16x16x32_bf16 v[16:19], a[108:111], a[92:95], v[16:19]\n" \
"global_load_lds_dwordx4 v253, s[50:51] offset:1024\n" \
"v_mfma_f32_16x16x32_bf16 v[20:23], a[112:115], a[88:91], v[20:23]\n" \
"s_add_u32 s50, s50, 0x60000\n" \
"s_addc_u32 s51, s51, 0\n" \
"ds_read_b64_tr_b16 a[204:205], v210 offset:14336\n" \
"v_mfma_f32_16x16x32_bf16 v[20:23], a[116:119], a[92:95], v[20:23]\n" \
"s_waitcnt lgkmcnt(3)\n" \
"ds_read_b64_tr_b16 a[206:207], v210 offset:15360\n" \
"v_mfma_f32_16x16x32_bf16 v[28:31], a[112:115], a[192:195], v[28:31]\n" \
"v_add_u32 v205, 0xffff0000, v205\n" \
"v_add_u32 v206, 0xffff0000, v206\n" \
"v_mfma_f32_16x16x32_bf16 v[28:31], a[116:119], a[196:199], v[28:31]\n" \
"buffer_load_dwordx2 v[226:227], v228, s[56:59], s30 offen sc1\n" \
"v_mfma_f32_16x16x32_bf16 v[24:27], a[104:107], a[192:195], v[24:27]\n" \
"v_add_u32 v209, 0xffff0000, v209\n" \
"v_add_u32 v210, 0xffff0000, v210\n" \
"v_mfma_f32_16x16x32_bf16 v[24:27], a[108:111], a[196:199], v[24:27]\n" \
"s_waitcnt vmcnt(11) lgkmcnt(0)\n" \
"v_mfma_f32_16x16x32_bf16 v[64:67], a[96:99], a[192:195], v[64:67]\n" \
"v_mfma_f32_16x16x32_bf16 v[64:67], a[100:103], a[196:199], v[64:67]\n" \
"s_barrier\n" \
"v_mfma_f32_16x16x32_bf16 v[80:83], a[72:75], a[208:211], v[80:83]\n" \
"ds_read_b64_tr_b16 a[96:97], v209 offset:2048\n" \
"v_mfma_f32_16x16x32_bf16 v[80:83], a[76:79], a[212:215], v[80:83]\n" \
"ds_read_b64_tr_b16 a[98:99], v209 offset:3072\n" \
"v_mfma_f32_16x16x32_bf16 v[84:87], a[72:75], a[216:219], v[84:87]\n" \
"s_add_u32 m0, s95, 65536\n" \
"ds_read_b64_tr_b16 a[100:101], v209 offset:14336\n" \
"v_mfma_f32_16x16x32_bf16 v[84:87], a[76:79], a[220:223], v[84:87]\n" \
"global_load_lds_dwordx4 v252, s[42:43] offset:0\n" \
"v_mfma_f32_16x16x32_bf16 v[44:47], a[200:203], a[72:75], v[44:47]\n" \
"ds_read_b64_tr_b16 a[102:103], v209 offset:15360\n" \
"v_mfma_f32_16x16x32_bf16 v[44:47], a[204:207], a[76:79], v[44:47]\n" \
"ds_read_b128 a[72:75], v205 offset:0\n" \
"ds_read_b64_tr_b16 a[104:105], v209 offset:0\n" \
"v_mfma_f32_16x16x32_bf16 v[52:55], a[200:203], a[80:83], v[52:55]\n" \
"ds_read_b128 a[76:79], v206 offset:0\n" \
"v_mfma_f32_16x16x32_bf16 v[52:55], a[204:207], a[84:87], v[52:55]\n" \
"global_load_lds_dwordx4 v252, s[42:43] offset:2048\n" \
"v_mfma_f32_16x16x32_bf16 v[92:95], a[80:83], a[216:219], v[92:95]\n" \
"s_add_u32 s42, s42, 0x80\n" \
"s_addc_u32 s43, s43, 0\n" \
"ds_read_b64_tr_b16 a[106:107], v209 offset:1024\n" \
"v_mfma_f32_16x16x32_bf16 v[92:95], a[84:87], a[220:223], v[92:95]\n" \
"ds_read_b64_tr_b16 a[108:109], v209 offset:12288\n" \
"v_mfma_f32_16x16x32_bf16 v[88:91], a[80:83], a[208:211], v[88:91]\n" \
"ds_read_b64_tr_b16 a[110:111], v209 offset:13312\n" \
"v_mfma_f32_16x16x32_bf16 v[88:91], a[84:87], a[212:215], v[88:91]\n" \
"global_load_lds_dwordx4 v252, s[44:45] offset:1024\n" \
"v_mfma_f32_16x16x32_bf16 v[96:99], a[88:91], a[208:211], v[96:99]\n" \
"ds_read_b128 a[80:83], v205 offset:4096\n" \
"v_mfma_f32_16x16x32_bf16 v[96:99], a[92:95], a[212:215], v[96:99]\n" \
"ds_read_b128 a[84:87], v206 offset:4096\n" \
"v_mfma_f32_16x16x32_bf16 v[100:103], a[88:91], a[216:219], v[100:103]\n" \
"ds_read_b64_tr_b16 a[112:113], v210 offset:0\n" \
"v_mfma_f32_16x16x32_bf16 v[100:103], a[92:95], a[220:223], v[100:103]\n" \
"global_load_lds_dwordx4 v252, s[44:45] offset:3072\n" \
"v_mfma_f32_16x16x32_bf16 v[60:63], a[200:203], a[88:91], v[60:63]\n" \
"s_add_u32 s44, s44, 0x80\n" \
"s_addc_u32 s45, s45, 0\n" \
"ds_read_b64_tr_b16 a[114:115], v210 offset:1024\n" \
"v_mfma_f32_16x16x32_bf16 v[60:63], a[204:207], a[92:95], v[60:63]\n" \
"ds_read_b64_tr_b16 a[116:117], v210 offset:12288\n" \
"v_mfma_f32_16x16x32_bf16 v[68:71], a[200:203], a[192:195], v[68:71]\n" \
"ds_read_b128 a[88:91], v205 offset:8192\n" \
"s_add_u32 m0, s84, 90112\n" \
"v_mfma_f32_16x16x32_bf16 v[68:71], a[204:207], a[196:199], v[68:71]\n" \
"global_load_lds_dwordx4 v253, s[48:49] offset:-4096\n" \
"v_mfma_f32_16x16x32_bf16 v[108:111], a[192:195], a[216:219], v[108:111]\n" \
"ds_read_b128 a[92:95], v206 offset:8192\n" \
"v_mfma_f32_16x16x32_bf16 v[108:111], a[196:199], a[220:223], v[108:111]\n" \
"ds_read_b64_tr_b16 a[118:119], v210 offset:13312\n" \
"v_mfma_f32_16x16x32_bf16 v[104:107], a[192:195], a[208:211], v[104:107]\n" \
"v_mfma_f32_16x16x32_bf16 v[104:107], a[196:199], a[212:215], v[104:107]\n" \
"s_waitcnt lgkmcnt(2)\n" \
"global_load_lds_dwordx4 v253, s[48:49] offset:-2048\n" \
"v_mfma_f32_16x16x32_bf16 v[40:43], a[96:99], a[72:75], v[40:43]\n" \
"ds_read_b128 a[192:195], v205 offset:12288\n" \
"v_mfma_f32_16x16x32_bf16 v[40:43], a[100:103], a[76:79], v[40:43]\n" \
"ds_read_b128 a[196:199], v206 offset:12288\n" \
"v_mfma_f32_16x16x32_bf16 v[0:3], a[104:107], a[72:75], v[0:3]\n" \
"ds_read_b64_tr_b16 a[208:209], v209 offset:4096\n" \
"v_mfma_f32_16x16x32_bf16 v[0:3], a[108:111], a[76:79], v[0:3]\n" \
"s_waitcnt lgkmcnt(3)\n" \
"global_load_lds_dwordx4 v253, s[48:49] offset:0\n" \
"v_mfma_f32_16x16x32_bf16 v[4:7], a[112:115], a[72:75], v[4:7]\n" \
"s_add_u32 s48, s48, 0x60000\n" \
"s_addc_u32 s49, s49, 0\n" \
"ds_read_b64_tr_b16 a[210:211], v209 offset:5120\n" \
"v_mfma_f32_16x16x32_bf16 v[4:7], a[116:119], a[76:79], v[4:7]\n" \
"ds_read_b64_tr_b16 a[212:213], v209 offset:16384\n" \
"v_mfma_f32_16x16x32_bf16 v[12:15], a[112:115], a[80:83], v[12:15]\n" \
"ds_read_b64_tr_b16 a[214:215], v209 offset:17408\n" \
"v_mfma_f32_16x16x32_bf16 v[12:15], a[116:119], a[84:87], v[12:15]\n" \
"global_load_lds_dwordx4 v253, s[50:51] offset:-3072\n" \
"Lpg9725_%=:\n" \
"s_cbranch_execz Lpg12022_%=\n" \
"v_mfma_f32_16x16x32_bf16 v[8:11], a[104:107], a[80:83], v[8:11]\n" \
"ds_read_b64_tr_b16 a[216:217], v210 offset:4096\n" \
"v_mfma_f32_16x16x32_bf16 v[8:11], a[108:111], a[84:87], v[8:11]\n" \
"ds_read_b64_tr_b16 a[218:219], v210 offset:5120\n" \
"v_mfma_f32_16x16x32_bf16 v[48:51], a[96:99], a[80:83], v[48:51]\n" \
"ds_read_b64_tr_b16 a[220:221], v210 offset:16384\n" \
"v_mfma_f32_16x16x32_bf16 v[48:51], a[100:103], a[84:87], v[48:51]\n" \
"global_load_lds_dwordx4 v253, s[50:51] offset:-1024\n" \
"v_mfma_f32_16x16x32_bf16 v[56:59], a[96:99], a[88:91], v[56:59]\n" \
"ds_read_b64_tr_b16 a[222:223], v210 offset:17408\n" \
"v_mfma_f32_16x16x32_bf16 v[56:59], a[100:103], a[92:95], v[56:59]\n" \
"ds_read_b64_tr_b16 a[200:201], v210 offset:2048\n" \
"v_mfma_f32_16x16x32_bf16 v[16:19], a[104:107], a[88:91], v[16:19]\n" \
"ds_read_b64_tr_b16 a[202:203], v210 offset:3072\n" \
"v_mfma_f32_16x16x32_bf16 v[16:19], a[108:111], a[92:95], v[16:19]\n" \
"global_load_lds_dwordx4 v253, s[50:51] offset:1024\n" \
"v_mfma_f32_16x16x32_bf16 v[20:23], a[112:115], a[88:91], v[20:23]\n" \
"s_add_u32 s50, s50, 0x60000\n" \
"s_addc_u32 s51, s51, 0\n" \
"ds_read_b64_tr_b16 a[204:205], v210 offset:14336\n" \
"v_mfma_f32_16x16x32_bf16 v[20:23], a[116:119], a[92:95], v[20:23]\n" \
"s_waitcnt lgkmcnt(3)\n" \
"ds_read_b64_tr_b16 a[206:207], v210 offset:15360\n" \
"v_mfma_f32_16x16x32_bf16 v[28:31], a[112:115], a[192:195], v[28:31]\n" \
"v_add_u32 v205, 0x1d000, v205\n" \
"v_add_u32 v206, 0x1d000, v206\n" \
"v_mfma_f32_16x16x32_bf16 v[28:31], a[116:119], a[196:199], v[28:31]\n" \
"v_add_u32 v209, 0x1d000, v209\n" \
"v_add_u32 v210, 0x1d000, v210\n" \
"v_mfma_f32_16x16x32_bf16 v[24:27], a[104:107], a[192:195], v[24:27]\n" \
"v_mfma_f32_16x16x32_bf16 v[24:27], a[108:111], a[196:199], v[24:27]\n" \
"s_waitcnt vmcnt(11) lgkmcnt(0)\n" \
"v_mfma_f32_16x16x32_bf16 v[64:67], a[96:99], a[192:195], v[64:67]\n" \
"v_mfma_f32_16x16x32_bf16 v[64:67], a[100:103], a[196:199], v[64:67]\n" \
"s_barrier\n" \
"v_mfma_f32_16x16x32_bf16 v[80:83], a[72:75], a[208:211], v[80:83]\n" \
"ds_read_b64_tr_b16 a[96:97], v209 offset:2048\n" \
"s_waitcnt vmcnt(10)\n" \
"v_mfma_f32_16x16x32_bf16 v[80:83], a[76:79], a[212:215], v[80:83]\n" \
"ds_read_b64_tr_b16 a[98:99], v209 offset:3072\n" \
"v_xor_b32 v226, v226, v227\n" \
"v_cmp_ne_u32 vcc, s60, v226\n" \
"v_mfma_f32_16x16x32_bf16 v[84:87], a[72:75], a[216:219], v[84:87]\n" \
"s_add_u32 m0, s95, 0\n" \
"ds_read_b64_tr_b16 a[100:101], v209 offset:14336\n" \
"v_mfma_f32_16x16x32_bf16 v[84:87], a[76:79], a[220:223], v[84:87]\n" \
"s_and_b32 vcc_hi, vcc_lo, -1\n" \
"s_cbranch_scc1 Lpl3_%=\n" \
"Lpr3_%=:\n" \
"global_load_lds_dwordx4 v252, s[42:43] offset:0\n" \
"v_mfma_f32_16x16x32_bf16 v[44:47], a[200:203], a[72:75], v[44:47]\n" \
"ds_read_b64_tr_b16 a[102:103], v209 offset:15360\n" \
"v_readfirstlane_b32 s71, v227\n" \
"v_mfma_f32_16x16x32_bf16 v[44:47], a[204:207], a[76:79], v[44:47]\n" \
"s_and_b32 s70, %1, 7\n" \
"ds_read_b128 a[72:75], v205 offset:0\n" \
"ds_read_b64_tr_b16 a[104:105], v209 offset:0\n" \
"v_mfma_f32_16x16x32_bf16 v[52:55], a[200:203], a[80:83], v[52:55]\n" \
"ds_read_b128 a[76:79], v206 offset:0\n" \
"s_lshl_b32 s78, s70, 2\n" \
"v_mfma_f32_16x16x32_bf16 v[52:55], a[204:207], a[84:87], v[52:55]\n" \
"global_load_lds_dwordx4 v252, s[42:43] offset:2048\n" \
"v_subrev_u32 v227, s71, v227\n" \
"v_mfma_f32_16x16x32_bf16 v[92:95], a[80:83], a[216:219], v[92:95]\n" \
"s_add_u32 s42, s42, 0x100\n" \
"s_addc_u32 s43, s43, 0\n" \
"ds_read_b64_tr_b16 a[106:107], v209 offset:1024\n" \
"v_mfma_f32_16x16x32_bf16 v[92:95], a[84:87], a[220:223], v[92:95]\n" \
"ds_read_b64_tr_b16 a[108:109], v209 offset:12288\n" \
"v_add_u32_dpp v227, v227, v227 quad_perm:[1,0,3,2] row_mask:0xf bank_mask:0xf\n" \
"v_mfma_f32_16x16x32_bf16 v[88:91], a[80:83], a[208:211], v[88:91]\n" \
"ds_read_b64_tr_b16 a[110:111], v209 offset:13312\n" \
"v_mfma_f32_16x16x32_bf16 v[88:91], a[84:87], a[212:215], v[88:91]\n" \
"global_load_lds_dwordx4 v252, s[44:45] offset:1024\n" \
"v_add_u32_dpp v227, v227, v227 quad_perm:[2,3,0,1] row_mask:0xf bank_mask:0xf\n" \
"v_mfma_f32_16x16x32_bf16 v[96:99], a[88:91], a[208:211], v[96:99]\n" \
"ds_read_b128 a[80:83], v205 offset:4096\n" \
"v_readlane_b32 s71, v227, s78\n" \
"v_mfma_f32_16x16x32_bf16 v[96:99], a[92:95], a[212:215], v[96:99]\n" \
"ds_read_b128 a[84:87], v206 offset:4096\n" \
"v_lshrrev_b32 v226, 6, v248\n" \
"v_mfma_f32_16x16x32_bf16 v[100:103], a[88:91], a[216:219], v[100:103]\n" \
"ds_read_b64_tr_b16 a[112:113], v210 offset:0\n" \
"v_subrev_u32 v227, s71, v227\n" \
"v_mfma_f32_16x16x32_bf16 v[100:103], a[92:95], a[220:223], v[100:103]\n" \
"global_load_lds_dwordx4 v252, s[44:45] offset:3072\n" \
"v_lshl_add_u32 v227, v227, 3, v226\n" \
"v_mfma_f32_16x16x32_bf16 v[60:63], a[200:203], a[88:91], v[60:63]\n" \
"s_add_u32 s44, s44, 0x100\n" \
"s_addc_u32 s45, s45, 0\n" \
"ds_read_b64_tr_b16 a[114:115], v210 offset:1024\n" \
"v_mfma_f32_16x16x32_bf16 v[60:63], a[204:207], a[92:95], v[60:63]\n" \
"ds_read_b64_tr_b16 a[116:117], v210 offset:12288\n" \
"v_cmp_gt_i32 vcc, s70, v227\n" \
"v_mfma_f32_16x16x32_bf16 v[68:71], a[200:203], a[192:195], v[68:71]\n" \
"ds_read_b128 a[88:91], v205 offset:8192\n" \
"s_add_u32 m0, s84, 24576\n" \
"v_mfma_f32_16x16x32_bf16 v[68:71], a[204:207], a[196:199], v[68:71]\n" \
"global_load_lds_dwordx4 v253, s[48:49] offset:-4096\n" \
"v_mfma_f32_16x16x32_bf16 v[108:111], a[192:195], a[216:219], v[108:111]\n" \
"ds_read_b128 a[92:95], v206 offset:8192\n" \
"s_bcnt1_i32_b32 s70, vcc_lo\n" \
"v_mfma_f32_16x16x32_bf16 v[108:111], a[196:199], a[220:223], v[108:111]\n" \
"s_lshr_b32 s70, s70, 2\n" \
"ds_read_b64_tr_b16 a[118:119], v210 offset:13312\n" \
"v_mfma_f32_16x16x32_bf16 v[104:107], a[192:195], a[208:211], v[104:107]\n" \
"s_lshr_b32 s78, s70, 2\n" \
"v_mfma_f32_16x16x32_bf16 v[104:107], a[196:199], a[212:215], v[104:107]\n" \
"s_waitcnt lgkmcnt(2)\n" \
"global_load_lds_dwordx4 v253, s[48:49] offset:-2048\n" \
"v_mfma_f32_16x16x32_bf16 v[40:43], a[96:99], a[72:75], v[40:43]\n" \
"ds_read_b128 a[192:195], v205 offset:12288\n" \
"s_mul_i32 s71, s78, 7\n" \
"v_mfma_f32_16x16x32_bf16 v[40:43], a[100:103], a[76:79], v[40:43]\n" \
"ds_read_b128 a[196:199], v206 offset:12288\n" \
"s_xor_b32 s70, s70, s71\n" \
"v_mfma_f32_16x16x32_bf16 v[0:3], a[104:107], a[72:75], v[0:3]\n" \
"s_xor_b32 s78, s78, 1\n" \
"ds_read_b64_tr_b16 a[208:209], v209 offset:4096\n" \
"v_mfma_f32_16x16x32_bf16 v[0:3], a[108:111], a[76:79], v[0:3]\n" \
"s_waitcnt lgkmcnt(3)\n" \
"global_load_lds_dwordx4 v253, s[48:49] offset:0\n" \
"v_mfma_f32_16x16x32_bf16 v[4:7], a[112:115], a[72:75], v[4:7]\n" \
"s_add_u32 s48, s48, 0xc0000\n" \
"s_addc_u32 s49, s49, 0\n" \
"ds_read_b64_tr_b16 a[210:211], v209 offset:5120\n" \
"v_mfma_f32_16x16x32_bf16 v[4:7], a[116:119], a[76:79], v[4:7]\n" \
"s_lshl_b32 s65, s70, 2\n" \
"ds_read_b64_tr_b16 a[212:213], v209 offset:16384\n" \
"v_mfma_f32_16x16x32_bf16 v[12:15], a[112:115], a[80:83], v[12:15]\n" \
"s_lshr_b32 s64, 0x9764, s65\n" \
"ds_read_b64_tr_b16 a[214:215], v209 offset:17408\n" \
"v_mfma_f32_16x16x32_bf16 v[12:15], a[116:119], a[84:87], v[12:15]\n" \
"global_load_lds_dwordx4 v253, s[50:51] offset:-3072\n" \
"v_mfma_f32_16x16x32_bf16 v[8:11], a[104:107], a[80:83], v[8:11]\n" \
"s_and_b32 s64, s64, 15\n" \
"ds_read_b64_tr_b16 a[216:217], v210 offset:4096\n" \
"v_mfma_f32_16x16x32_bf16 v[8:11], a[108:111], a[84:87], v[8:11]\n" \
"s_add_u32 s64, s64, 32\n" \
"ds_read_b64_tr_b16 a[218:219], v210 offset:5120\n" \
"v_mfma_f32_16x16x32_bf16 v[48:51], a[96:99], a[80:83], v[48:51]\n" \
"s_lshr_b32 s70, %1, 6\n" \
"ds_read_b64_tr_b16 a[220:221], v210 offset:16384\n" \
"v_mfma_f32_16x16x32_bf16 v[48:51], a[100:103], a[84:87], v[48:51]\n" \
"global_load_lds_dwordx4 v253, s[50:51] offset:-1024\n" \
"v_mfma_f32_16x16x32_bf16 v[56:59], a[96:99], a[88:91], v[56:59]\n" \
"s_add_u32 s65, s65, s70\n" \
"ds_read_b64_tr_b16 a[222:223], v210 offset:17408\n" \
"v_mfma_f32_16x16x32_bf16 v[56:59], a[100:103], a[92:95], v[56:59]\n" \
"s_lshl_b32 s30, s85, 2\n" \
"ds_read_b64_tr_b16 a[200:201], v210 offset:2048\n" \
"v_mfma_f32_16x16x32_bf16 v[16:19], a[104:107], a[88:91], v[16:19]\n" \
"s_lshl_b32 s70, s88, 7\n" \
"ds_read_b64_tr_b16 a[202:203], v210 offset:3072\n" \
"v_mfma_f32_16x16x32_bf16 v[16:19], a[108:111], a[92:95], v[16:19]\n" \
"global_load_lds_dwordx4 v253, s[50:51] offset:1024\n" \
"v_mfma_f32_16x16x32_bf16 v[20:23], a[112:115], a[88:91], v[20:23]\n" \
"s_add_u32 s50, s50, 0xc0000\n" \
"s_addc_u32 s51, s51, 0\n" \
"ds_read_b64_tr_b16 a[204:205], v210 offset:14336\n" \
"v_mfma_f32_16x16x32_bf16 v[20:23], a[116:119], a[92:95], v[20:23]\n" \
"s_waitcnt lgkmcnt(3)\n" \
"ds_read_b64_tr_b16 a[206:207], v210 offset:15360\n" \
"v_mfma_f32_16x16x32_bf16 v[28:31], a[112:115], a[192:195], v[28:31]\n" \
"s_add_u32 s30, s30, s70\n" \
"v_add_u32 v205, 0xffff3000, v205\n" \
"v_add_u32 v206, 0xffff3000, v206\n" \
"v_mfma_f32_16x16x32_bf16 v[28:31], a[116:119], a[196:199], v[28:31]\n" \
"s_add_u32 s30, s30, 65591296\n" \
"v_add_u32 v209, 0xffff3000, v209\n" \
"v_add_u32 v210, 0xffff3000, v210\n" \
"v_mfma_f32_16x16x32_bf16 v[24:27], a[104:107], a[192:195], v[24:27]\n" \
"v_lshrrev_b32 v221, 2, v248\n" \
"v_mfma_f32_16x16x32_bf16 v[24:27], a[108:111], a[196:199], v[24:27]\n" \
"s_waitcnt vmcnt(10) lgkmcnt(0)\n" \
"v_mfma_f32_16x16x32_bf16 v[64:67], a[96:99], a[192:195], v[64:67]\n" \
"s_lshl_b32 s81, s65, 7\n" \
"v_mfma_f32_16x16x32_bf16 v[64:67], a[100:103], a[196:199], v[64:67]\n" \
"s_barrier\n" \
"v_mfma_f32_16x16x32_bf16 v[80:83], a[72:75], a[208:211], v[80:83]\n" \
"s_add_u32 s82, s81, 65591296\n" \
"ds_read_b64_tr_b16 a[96:97], v209 offset:2048\n" \
"v_mfma_f32_16x16x32_bf16 v[80:83], a[76:79], a[212:215], v[80:83]\n" \
"s_add_u32 s81, s81, 65589248\n" \
"ds_read_b64_tr_b16 a[98:99], v209 offset:3072\n" \
"v_mfma_f32_16x16x32_bf16 v[84:87], a[72:75], a[216:219], v[84:87]\n" \
"s_add_u32 m0, s95, 118784\n" \
"ds_read_b64_tr_b16 a[100:101], v209 offset:14336\n" \
"v_mfma_f32_16x16x32_bf16 v[84:87], a[76:79], a[220:223], v[84:87]\n" \
"global_load_lds_dwordx4 v252, s[42:43] offset:0\n" \
"v_mfma_f32_16x16x32_bf16 v[44:47], a[200:203], a[72:75], v[44:47]\n" \
"s_mov_b64 s[52:53], s[26:27]\n" \
"ds_read_b64_tr_b16 a[102:103], v209 offset:15360\n" \
"v_mfma_f32_16x16x32_bf16 v[44:47], a[204:207], a[76:79], v[44:47]\n" \
"s_and_b32 s53, s53, 0xffff\n" \
"ds_read_b128 a[72:75], v205 offset:0\n" \
"ds_read_b64_tr_b16 a[104:105], v209 offset:0\n" \
"v_mfma_f32_16x16x32_bf16 v[52:55], a[200:203], a[80:83], v[52:55]\n" \
"ds_read_b128 a[76:79], v206 offset:0\n" \
"s_mov_b32 s54, 8388608\n" \
"v_mfma_f32_16x16x32_bf16 v[52:55], a[204:207], a[84:87], v[52:55]\n" \
"global_load_lds_dwordx4 v252, s[42:43] offset:2048\n" \
"v_mfma_f32_16x16x32_bf16 v[92:95], a[80:83], a[216:219], v[92:95]\n" \
"s_add_u32 s42, s42, 0x80\n" \
"s_addc_u32 s43, s43, 0\n" \
"ds_read_b64_tr_b16 a[106:107], v209 offset:1024\n" \
"v_mfma_f32_16x16x32_bf16 v[92:95], a[84:87], a[220:223], v[92:95]\n" \
"s_mov_b32 s55, 0x20000\n" \
"ds_read_b64_tr_b16 a[108:109], v209 offset:12288\n" \
"v_mfma_f32_16x16x32_bf16 v[88:91], a[80:83], a[208:211], v[88:91]\n" \
"s_lshr_b32 s83, s63, 1\n" \
"ds_read_b64_tr_b16 a[110:111], v209 offset:13312\n" \
"v_mfma_f32_16x16x32_bf16 v[88:91], a[84:87], a[212:215], v[88:91]\n" \
"global_load_lds_dwordx4 v252, s[44:45] offset:1024\n" \
"v_mfma_f32_16x16x32_bf16 v[96:99], a[88:91], a[208:211], v[96:99]\n" \
"ds_read_b128 a[80:83], v205 offset:4096\n" \
"s_add_u32 s83, s83, 2\n" \
"v_mfma_f32_16x16x32_bf16 v[96:99], a[92:95], a[212:215], v[96:99]\n" \
"ds_read_b128 a[84:87], v206 offset:4096\n" \
"s_lshr_b32 s70, s18, 8\n" \
"v_mfma_f32_16x16x32_bf16 v[100:103], a[88:91], a[216:219], v[100:103]\n" \
"s_sub_u32 s83, s83, s70\n" \
"ds_read_b64_tr_b16 a[112:113], v210 offset:0\n" \
"v_mfma_f32_16x16x32_bf16 v[100:103], a[92:95], a[220:223], v[100:103]\n" \
"global_load_lds_dwordx4 v252, s[44:45] offset:3072\n" \
"v_mfma_f32_16x16x32_bf16 v[60:63], a[200:203], a[88:91], v[60:63]\n" \
"s_add_u32 s44, s44, 0x80\n" \
"s_addc_u32 s45, s45, 0\n" \
"ds_read_b64_tr_b16 a[114:115], v210 offset:1024\n" \
"v_mfma_f32_16x16x32_bf16 v[60:63], a[204:207], a[92:95], v[60:63]\n" \
"s_and_b32 s83, s83, 3\n" \
"ds_read_b64_tr_b16 a[116:117], v210 offset:12288\n" \
"v_mfma_f32_16x16x32_bf16 v[68:71], a[200:203], a[192:195], v[68:71]\n" \
"ds_read_b128 a[88:91], v205 offset:8192\n" \
"s_add_u32 m0, s84, 143360\n" \
"v_mfma_f32_16x16x32_bf16 v[68:71], a[204:207], a[196:199], v[68:71]\n" \
"global_load_lds_dwordx4 v253, s[48:49] offset:-4096\n" \
"v_mfma_f32_16x16x32_bf16 v[108:111], a[192:195], a[216:219], v[108:111]\n" \
"ds_read_b128 a[92:95], v206 offset:8192\n" \
"s_and_b32 s70, s63, 1\n" \
"v_mfma_f32_16x16x32_bf16 v[108:111], a[196:199], a[220:223], v[108:111]\n" \
"s_lshl_b32 s70, s70, 2\n" \
"ds_read_b64_tr_b16 a[118:119], v210 offset:13312\n" \
"v_mfma_f32_16x16x32_bf16 v[104:107], a[192:195], a[208:211], v[104:107]\n" \
"s_or_b32 s83, s83, s70\n" \
"v_mfma_f32_16x16x32_bf16 v[104:107], a[196:199], a[212:215], v[104:107]\n" \
"s_waitcnt lgkmcnt(2)\n" \
"global_load_lds_dwordx4 v253, s[48:49] offset:-2048\n" \
"v_mfma_f32_16x16x32_bf16 v[40:43], a[96:99], a[72:75], v[40:43]\n" \
"s_mul_i32 s75, s78, s64\n" \
"ds_read_b128 a[192:195], v205 offset:12288\n" \
"v_lshrrev_b32 v228, 4, v248\n" \
"v_mfma_f32_16x16x32_bf16 v[40:43], a[100:103], a[76:79], v[40:43]\n" \
"ds_read_b128 a[196:199], v206 offset:12288\n" \
"s_lshl_b32 s70, s65, 2\n" \
"v_mfma_f32_16x16x32_bf16 v[0:3], a[104:107], a[72:75], v[0:3]\n" \
"s_add_u32 s70, s70, s61\n" \
"ds_read_b64_tr_b16 a[208:209], v209 offset:4096\n" \
"v_mfma_f32_16x16x32_bf16 v[0:3], a[108:111], a[76:79], v[0:3]\n" \
"s_waitcnt lgkmcnt(3)\n" \
"global_load_lds_dwordx4 v253, s[48:49] offset:0\n" \
"v_mfma_f32_16x16x32_bf16 v[4:7], a[112:115], a[72:75], v[4:7]\n" \
"s_add_u32 s48, s48, 0x60000\n" \
"s_addc_u32 s49, s49, 0\n" \
"ds_read_b64_tr_b16 a[210:211], v209 offset:5120\n" \
"v_mfma_f32_16x16x32_bf16 v[4:7], a[116:119], a[76:79], v[4:7]\n" \
"s_mul_i32 s73, s70, 69632\n" \
"ds_read_b64_tr_b16 a[212:213], v209 offset:16384\n" \
"v_mfma_f32_16x16x32_bf16 v[12:15], a[112:115], a[80:83], v[12:15]\n" \
"s_add_u32 s73, s73, 52740096\n" \
"ds_read_b64_tr_b16 a[214:215], v209 offset:17408\n" \
"v_mfma_f32_16x16x32_bf16 v[12:15], a[116:119], a[84:87], v[12:15]\n" \
"global_load_lds_dwordx4 v253, s[50:51] offset:-3072\n" \
"v_mfma_f32_16x16x32_bf16 v[8:11], a[104:107], a[80:83], v[8:11]\n" \
"s_add_u32 s73, s73, s28\n" \
"ds_read_b64_tr_b16 a[216:217], v210 offset:4096\n" \
"v_mfma_f32_16x16x32_bf16 v[8:11], a[108:111], a[84:87], v[8:11]\n" \
"s_mul_i32 s70, s70, 266240\n" \
"ds_read_b64_tr_b16 a[218:219], v210 offset:5120\n" \
"v_mfma_f32_16x16x32_bf16 v[48:51], a[96:99], a[80:83], v[48:51]\n" \
"s_lshl_b32 s71, s75, 12\n" \
"ds_read_b64_tr_b16 a[220:221], v210 offset:16384\n" \
"v_mfma_f32_16x16x32_bf16 v[48:51], a[100:103], a[84:87], v[48:51]\n" \
"global_load_lds_dwordx4 v253, s[50:51] offset:-1024\n" \
"v_mfma_f32_16x16x32_bf16 v[56:59], a[96:99], a[88:91], v[56:59]\n" \
"s_add_u32 s70, s70, s71\n" \
"ds_read_b64_tr_b16 a[222:223], v210 offset:17408\n" \
"v_mfma_f32_16x16x32_bf16 v[56:59], a[100:103], a[92:95], v[56:59]\n" \
"s_add_u32 s70, s70, 35700736\n" \
"ds_read_b64_tr_b16 a[200:201], v210 offset:2048\n" \
"v_mfma_f32_16x16x32_bf16 v[16:19], a[104:107], a[88:91], v[16:19]\n" \
"s_add_u32 s36, s28, s70\n" \
"ds_read_b64_tr_b16 a[202:203], v210 offset:3072\n" \
"v_mfma_f32_16x16x32_bf16 v[16:19], a[108:111], a[92:95], v[16:19]\n" \
"global_load_lds_dwordx4 v253, s[50:51] offset:1024\n" \
"v_mfma_f32_16x16x32_bf16 v[20:23], a[112:115], a[88:91], v[20:23]\n" \
"s_add_u32 s50, s50, 0x60000\n" \
"s_addc_u32 s51, s51, 0\n" \
"ds_read_b64_tr_b16 a[204:205], v210 offset:14336\n" \
"v_mfma_f32_16x16x32_bf16 v[20:23], a[116:119], a[92:95], v[20:23]\n" \
"s_waitcnt lgkmcnt(3)\n" \
"ds_read_b64_tr_b16 a[206:207], v210 offset:15360\n" \
"v_mfma_f32_16x16x32_bf16 v[28:31], a[112:115], a[192:195], v[28:31]\n" \
"s_mov_b32 s37, s29\n" \
"v_add_u32 v205, 0xffff0000, v205\n" \
"v_add_u32 v206, 0xffff0000, v206\n" \
"v_mfma_f32_16x16x32_bf16 v[28:31], a[116:119], a[196:199], v[28:31]\n" \
"s_lshr_b32 s74, s61, 1\n" \
"v_add_u32 v209, 0xffff0000, v209\n" \
"v_add_u32 v210, 0xffff0000, v210\n" \
"v_mfma_f32_16x16x32_bf16 v[24:27], a[104:107], a[192:195], v[24:27]\n" \
"s_lshl_b32 s74, s74, 16\n" \
"v_mfma_f32_16x16x32_bf16 v[24:27], a[108:111], a[196:199], v[24:27]\n" \
"s_waitcnt vmcnt(10) lgkmcnt(0)\n" \
"v_mfma_f32_16x16x32_bf16 v[64:67], a[96:99], a[192:195], v[64:67]\n" \
"s_and_b32 s70, s61, 1\n" \
"v_mfma_f32_16x16x32_bf16 v[64:67], a[100:103], a[196:199], v[64:67]\n" \
"s_barrier\n" \
"v_mfma_f32_16x16x32_bf16 v[80:83], a[72:75], a[208:211], v[80:83]\n" \
"s_lshl_b32 s70, s70, 14\n" \
"ds_read_b64_tr_b16 a[96:97], v209 offset:2048\n" \
"v_mfma_f32_16x16x32_bf16 v[80:83], a[76:79], a[212:215], v[80:83]\n" \
"s_add_u32 s74, s74, s70\n" \
"ds_read_b64_tr_b16 a[98:99], v209 offset:3072\n" \
"v_mfma_f32_16x16x32_bf16 v[84:87], a[72:75], a[216:219], v[84:87]\n" \
"s_add_u32 m0, s95, 65536\n" \
"ds_read_b64_tr_b16 a[100:101], v209 offset:14336\n" \
"v_mfma_f32_16x16x32_bf16 v[84:87], a[76:79], a[220:223], v[84:87]\n" \
"global_load_lds_dwordx4 v252, s[42:43] offset:0\n" \
"v_mfma_f32_16x16x32_bf16 v[44:47], a[200:203], a[72:75], v[44:47]\n" \
"s_lshl_b32 s71, s83, 8\n" \
"ds_read_b64_tr_b16 a[102:103], v209 offset:15360\n" \
"v_mfma_f32_16x16x32_bf16 v[44:47], a[204:207], a[76:79], v[44:47]\n" \
"s_add_u32 s74, s74, s71\n" \
"ds_read_b128 a[72:75], v205 offset:0\n" \
"ds_read_b64_tr_b16 a[104:105], v209 offset:0\n" \
"v_mfma_f32_16x16x32_bf16 v[52:55], a[200:203], a[80:83], v[52:55]\n" \
"s_add_u32 s40, s24, s74\n" \
"s_addc_u32 s41, s25, 0\n" \
"ds_read_b128 a[76:79], v206 offset:0\n" \
"v_mfma_f32_16x16x32_bf16 v[52:55], a[204:207], a[84:87], v[52:55]\n" \
"global_load_lds_dwordx4 v252, s[42:43] offset:2048\n" \
"v_mfma_f32_16x16x32_bf16 v[92:95], a[80:83], a[216:219], v[92:95]\n" \
"s_add_u32 s42, s42, 0x80\n" \
"s_addc_u32 s43, s43, 0\n" \
"ds_read_b64_tr_b16 a[106:107], v209 offset:1024\n" \
"v_mfma_f32_16x16x32_bf16 v[92:95], a[84:87], a[220:223], v[92:95]\n" \
"s_lshl_b32 s70, s75, 17\n" \
"ds_read_b64_tr_b16 a[108:109], v209 offset:12288\n" \
"v_mfma_f32_16x16x32_bf16 v[88:91], a[80:83], a[208:211], v[88:91]\n" \
"s_add_u32 s70, s70, s74\n" \
"ds_read_b64_tr_b16 a[110:111], v209 offset:13312\n" \
"v_mfma_f32_16x16x32_bf16 v[88:91], a[84:87], a[212:215], v[88:91]\n" \
"global_load_lds_dwordx4 v252, s[44:45] offset:1024\n" \
"v_mfma_f32_16x16x32_bf16 v[96:99], a[88:91], a[208:211], v[96:99]\n" \
"s_add_u32 s38, s18, s70\n" \
"s_addc_u32 s39, s19, 0\n" \
"ds_read_b128 a[80:83], v205 offset:4096\n" \
"v_mfma_f32_16x16x32_bf16 v[96:99], a[92:95], a[212:215], v[96:99]\n" \
"ds_read_b128 a[84:87], v206 offset:4096\n" \
"v_bfe_u32 v229, v228, 2, 3\n" \
"v_lshlrev_b32 v229, 11, v229\n" \
"v_mfma_f32_16x16x32_bf16 v[100:103], a[88:91], a[216:219], v[100:103]\n" \
"ds_read_b64_tr_b16 a[112:113], v210 offset:0\n" \
"v_lshrrev_b32 v230, 5, v228\n" \
"v_mfma_f32_16x16x32_bf16 v[100:103], a[92:95], a[220:223], v[100:103]\n" \
"global_load_lds_dwordx4 v252, s[44:45] offset:3072\n" \
"v_lshl_add_u32 v229, v230, 6, v229\n" \
"v_mfma_f32_16x16x32_bf16 v[60:63], a[200:203], a[88:91], v[60:63]\n" \
"s_add_u32 s44, s44, 0x80\n" \
"s_addc_u32 s45, s45, 0\n" \
"ds_read_b64_tr_b16 a[114:115], v210 offset:1024\n" \
"v_mfma_f32_16x16x32_bf16 v[60:63], a[204:207], a[92:95], v[60:63]\n" \
"ds_read_b64_tr_b16 a[116:117], v210 offset:12288\n" \
"v_and_b32 v230, 3, v228\n" \
"v_mfma_f32_16x16x32_bf16 v[68:71], a[200:203], a[192:195], v[68:71]\n" \
"s_add_u32 m0, s84, 90112\n" \
"ds_read_b128 a[88:91], v205 offset:8192\n" \
"v_bfe_u32 v231, v228, 4, 1\n" \
"v_mfma_f32_16x16x32_bf16 v[68:71], a[204:207], a[196:199], v[68:71]\n" \
"global_load_lds_dwordx4 v253, s[48:49] offset:-4096\n" \
"v_lshlrev_b32 v231, 1, v231\n" \
"v_mfma_f32_16x16x32_bf16 v[108:111], a[192:195], a[216:219], v[108:111]\n" \
"ds_read_b128 a[92:95], v206 offset:8192\n" \
"v_xor_b32 v230, v230, v231\n" \
"v_mfma_f32_16x16x32_bf16 v[108:111], a[196:199], a[220:223], v[108:111]\n" \
"ds_read_b64_tr_b16 a[118:119], v210 offset:13312\n" \
"v_lshl_add_u32 v229, v230, 4, v229\n" \
"v_mfma_f32_16x16x32_bf16 v[104:107], a[192:195], a[208:211], v[104:107]\n" \
"v_add_u32 v216, 4096, v229\n" \
"v_add_u32 v217, 3200, v229\n" \
"v_mfma_f32_16x16x32_bf16 v[104:107], a[196:199], a[212:215], v[104:107]\n" \
"s_waitcnt lgkmcnt(2)\n" \
"global_load_lds_dwordx4 v253, s[48:49] offset:-2048\n" \
"v_add_u32 v218, 34816, v229\n" \
"v_mfma_f32_16x16x32_bf16 v[40:43], a[96:99], a[72:75], v[40:43]\n" \
"ds_read_b128 a[192:195], v205 offset:12288\n" \
"v_add_u32 v219, 33920, v229\n" \
"v_mfma_f32_16x16x32_bf16 v[40:43], a[100:103], a[76:79], v[40:43]\n" \
"ds_read_b128 a[196:199], v206 offset:12288\n" \
"s_mov_b32 s75, 65536\n" \
"v_mfma_f32_16x16x32_bf16 v[0:3], a[104:107], a[72:75], v[0:3]\n" \
"s_lshr_b32 s70, s61, 1\n" \
"ds_read_b64_tr_b16 a[208:209], v209 offset:4096\n" \
"v_mfma_f32_16x16x32_bf16 v[0:3], a[108:111], a[76:79], v[0:3]\n" \
"s_waitcnt lgkmcnt(3)\n" \
"global_load_lds_dwordx4 v253, s[48:49] offset:0\n" \
"v_mfma_f32_16x16x32_bf16 v[4:7], a[112:115], a[72:75], v[4:7]\n" \
"s_add_u32 s48, s48, 0x60000\n" \
"s_addc_u32 s49, s49, 0\n" \
"ds_read_b64_tr_b16 a[210:211], v209 offset:5120\n" \
"v_mfma_f32_16x16x32_bf16 v[4:7], a[116:119], a[76:79], v[4:7]\n" \
"s_lshl_b32 s71, s70, 13\n" \
"ds_read_b64_tr_b16 a[212:213], v209 offset:16384\n" \
"v_add_u32 v211, s71, v249\n" \
"v_mfma_f32_16x16x32_bf16 v[12:15], a[112:115], a[80:83], v[12:15]\n" \
"s_lshl_b32 s71, s65, 1\n" \
"ds_read_b64_tr_b16 a[214:215], v209 offset:17408\n" \
"v_mfma_f32_16x16x32_bf16 v[12:15], a[116:119], a[84:87], v[12:15]\n" \
"global_load_lds_dwordx4 v253, s[50:51] offset:-3072\n" \
"v_mfma_f32_16x16x32_bf16 v[8:11], a[104:107], a[80:83], v[8:11]\n" \
"s_add_u32 s70, s70, s71\n" \
"ds_read_b64_tr_b16 a[216:217], v210 offset:4096\n" \
"v_mfma_f32_16x16x32_bf16 v[8:11], a[108:111], a[84:87], v[8:11]\n" \
"s_lshl_b32 s77, s70, 18\n" \
"ds_read_b64_tr_b16 a[218:219], v210 offset:5120\n" \
"v_mfma_f32_16x16x32_bf16 v[48:51], a[96:99], a[80:83], v[48:51]\n" \
"s_lshl_b32 s71, s83, 8\n" \
"ds_read_b64_tr_b16 a[220:221], v210 offset:16384\n" \
"v_mfma_f32_16x16x32_bf16 v[48:51], a[100:103], a[84:87], v[48:51]\n" \
"global_load_lds_dwordx4 v253, s[50:51] offset:-1024\n" \
"v_mfma_f32_16x16x32_bf16 v[56:59], a[96:99], a[88:91], v[56:59]\n" \
"s_add_u32 s77, s77, s71\n" \
"ds_read_b64_tr_b16 a[222:223], v210 offset:17408\n" \
"v_mfma_f32_16x16x32_bf16 v[56:59], a[100:103], a[92:95], v[56:59]\n" \
"s_lshl_b32 s71, s78, 11\n" \
"ds_read_b64_tr_b16 a[200:201], v210 offset:2048\n" \
"v_mfma_f32_16x16x32_bf16 v[16:19], a[104:107], a[88:91], v[16:19]\n" \
"s_add_u32 s77, s77, s71\n" \
"ds_read_b64_tr_b16 a[202:203], v210 offset:3072\n" \
"v_mfma_f32_16x16x32_bf16 v[16:19], a[108:111], a[92:95], v[16:19]\n" \
"global_load_lds_dwordx4 v253, s[50:51] offset:1024\n" \
"v_mfma_f32_16x16x32_bf16 v[20:23], a[112:115], a[88:91], v[20:23]\n" \
"s_add_u32 s50, s50, 0x60000\n" \
"s_addc_u32 s51, s51, 0\n" \
"ds_read_b64_tr_b16 a[204:205], v210 offset:14336\n" \
"v_mfma_f32_16x16x32_bf16 v[20:23], a[116:119], a[92:95], v[20:23]\n" \
"s_waitcnt lgkmcnt(3)\n" \
"ds_read_b64_tr_b16 a[206:207], v210 offset:15360\n" \
"v_mfma_f32_16x16x32_bf16 v[28:31], a[112:115], a[192:195], v[28:31]\n" \
"s_and_b32 s70, s61, 1\n" \
"v_add_u32 v205, 0x1d000, v205\n" \
"v_add_u32 v206, 0x1d000, v206\n" \
"v_mfma_f32_16x16x32_bf16 v[28:31], a[116:119], a[196:199], v[28:31]\n" \
"s_lshl_b32 s71, s70, 7\n" \
"v_add_u32 v209, 0x1d000, v209\n" \
"v_add_u32 v210, 0x1d000, v210\n" \
"v_mfma_f32_16x16x32_bf16 v[24:27], a[104:107], a[192:195], v[24:27]\n" \
"s_add_u32 s77, s77, s71\n" \
"v_and_b32 v229, 3, v228\n" \
"v_lshlrev_b32 v229, 3, v229\n" \
"v_mfma_f32_16x16x32_bf16 v[24:27], a[108:111], a[196:199], v[24:27]\n" \
"s_waitcnt vmcnt(10) lgkmcnt(0)\n" \
"v_bfe_u32 v230, v228, 2, 2\n" \
"v_lshl_add_u32 v229, v230, 6, v229\n" \
"v_bfe_u32 v230, v228, 4, 1\n" \
"v_mfma_f32_16x16x32_bf16 v[64:67], a[96:99], a[192:195], v[64:67]\n" \
"v_lshl_add_u32 v229, v230, 5, v229\n" \
"v_lshl_add_u32 v229, v230, 8, v229\n" \
"v_mfma_f32_16x16x32_bf16 v[64:67], a[100:103], a[196:199], v[64:67]\n" \
"s_barrier\n" \
"v_lshrrev_b32 v230, 5, v228\n" \
"v_lshl_add_u32 v229, v230, 12, v229\n" \
"v_mfma_f32_16x16x32_bf16 v[80:83], a[72:75], a[208:211], v[80:83]\n" \
"s_lshl_b32 s71, s70, 10\n" \
"ds_read_b64_tr_b16 a[96:97], v209 offset:2048\n" \
"v_add_u32 v212, s71, v229\n" \
"v_mfma_f32_16x16x32_bf16 v[80:83], a[76:79], a[212:215], v[80:83]\n" \
"s_lshl_b32 s70, s65, 3\n" \
"s_add_u32 s70, s70, s83\n" \
"ds_read_b64_tr_b16 a[98:99], v209 offset:3072\n" \
"v_mfma_f32_16x16x32_bf16 v[84:87], a[72:75], a[216:219], v[84:87]\n" \
"s_add_u32 m0, s95, 0\n" \
"s_lshl_b32 s76, s70, 16\n" \
"ds_read_b64_tr_b16 a[100:101], v209 offset:14336\n" \
"v_mfma_f32_16x16x32_bf16 v[84:87], a[76:79], a[220:223], v[84:87]\n" \
"s_lshl_b32 s71, s61, 14\n" \
"global_load_lds_dwordx4 v252, s[42:43] offset:0\n" \
"v_mfma_f32_16x16x32_bf16 v[44:47], a[200:203], a[72:75], v[44:47]\n" \
"s_add_u32 s76, s76, s71\n" \
"ds_read_b64_tr_b16 a[102:103], v209 offset:15360\n" \
"v_mfma_f32_16x16x32_bf16 v[44:47], a[204:207], a[76:79], v[44:47]\n" \
"s_add_u32 s76, s76, 57196544\n" \
"ds_read_b128 a[72:75], v205 offset:0\n" \
"ds_read_b64_tr_b16 a[104:105], v209 offset:0\n" \
"v_mfma_f32_16x16x32_bf16 v[52:55], a[200:203], a[80:83], v[52:55]\n" \
"ds_read_b128 a[76:79], v206 offset:0\n" \
"s_lshl_b32 s79, s70, 7\n" \
"v_mfma_f32_16x16x32_bf16 v[52:55], a[204:207], a[84:87], v[52:55]\n" \
"global_load_lds_dwordx4 v252, s[42:43] offset:2048\n" \
"v_mfma_f32_16x16x32_bf16 v[92:95], a[80:83], a[216:219], v[92:95]\n" \
"s_add_u32 s42, s42, 0x80\n" \
"s_addc_u32 s43, s43, 0\n" \
"ds_read_b64_tr_b16 a[106:107], v209 offset:1024\n" \
"v_mfma_f32_16x16x32_bf16 v[92:95], a[84:87], a[220:223], v[92:95]\n" \
"s_add_u32 s79, s79, 65601536\n" \
"ds_read_b64_tr_b16 a[108:109], v209 offset:12288\n" \
"v_mfma_f32_16x16x32_bf16 v[88:91], a[80:83], a[208:211], v[88:91]\n" \
"s_lshr_b32 s70, s64, 1\n" \
"ds_read_b64_tr_b16 a[110:111], v209 offset:13312\n" \
"v_mfma_f32_16x16x32_bf16 v[88:91], a[84:87], a[212:215], v[88:91]\n" \
"global_load_lds_dwordx4 v252, s[44:45] offset:1024\n" \
"v_mfma_f32_16x16x32_bf16 v[96:99], a[88:91], a[208:211], v[96:99]\n" \
"ds_read_b128 a[80:83], v205 offset:4096\n" \
"s_lshl_b32 s70, -1, s70\n" \
"v_mfma_f32_16x16x32_bf16 v[96:99], a[92:95], a[212:215], v[96:99]\n" \
"ds_read_b128 a[84:87], v206 offset:4096\n" \
"s_add_u32 s71, s64, 1\n" \
"v_mfma_f32_16x16x32_bf16 v[100:103], a[88:91], a[216:219], v[100:103]\n" \
"s_lshr_b32 s71, s71, 1\n" \
"ds_read_b64_tr_b16 a[112:113], v210 offset:0\n" \
"v_mfma_f32_16x16x32_bf16 v[100:103], a[92:95], a[220:223], v[100:103]\n" \
"global_load_lds_dwordx4 v252, s[44:45] offset:3072\n" \
"v_mfma_f32_16x16x32_bf16 v[60:63], a[200:203], a[88:91], v[60:63]\n" \
"s_add_u32 s44, s44, 0x80\n" \
"s_addc_u32 s45, s45, 0\n" \
"ds_read_b64_tr_b16 a[114:115], v210 offset:1024\n" \
"v_mfma_f32_16x16x32_bf16 v[60:63], a[204:207], a[92:95], v[60:63]\n" \
"s_bfm_b32 s71, s71, 0\n" \
"ds_read_b64_tr_b16 a[116:117], v210 offset:12288\n" \
"v_mfma_f32_16x16x32_bf16 v[68:71], a[200:203], a[192:195], v[68:71]\n" \
"ds_read_b128 a[88:91], v205 offset:8192\n" \
"s_add_u32 m0, s84, 24576\n" \
"v_mfma_f32_16x16x32_bf16 v[68:71], a[204:207], a[196:199], v[68:71]\n" \
"global_load_lds_dwordx4 v253, s[48:49] offset:-4096\n" \
"v_mfma_f32_16x16x32_bf16 v[108:111], a[192:195], a[216:219], v[108:111]\n" \
"s_cmp_eq_u32 s78, 0\n" \
"s_cselect_b32 s80, s71, s70\n" \
"ds_read_b128 a[92:95], v206 offset:8192\n" \
"v_mfma_f32_16x16x32_bf16 v[108:111], a[196:199], a[220:223], v[108:111]\n" \
"s_mov_b32 s16, 0xff000000\n" \
"ds_read_b64_tr_b16 a[118:119], v210 offset:13312\n" \
"v_mfma_f32_16x16x32_bf16 v[104:107], a[192:195], a[208:211], v[104:107]\n" \
"s_mov_b32 s17, 0\n" \
"v_add_u32 v222, 0x1c90080, v250\n" \
"v_cndmask_b32 v222, v250, v222, s[16:17]\n" \
"v_mfma_f32_16x16x32_bf16 v[104:107], a[196:199], a[212:215], v[104:107]\n" \
"s_waitcnt lgkmcnt(2)\n" \
"global_load_lds_dwordx4 v253, s[48:49] offset:-2048\n" \
"v_mfma_f32_16x16x32_bf16 v[40:43], a[96:99], a[72:75], v[40:43]\n" \
"ds_read_b128 a[192:195], v205 offset:12288\n" \
"s_mov_b32 s74, 98304\n" \
"v_mfma_f32_16x16x32_bf16 v[40:43], a[100:103], a[76:79], v[40:43]\n" \
"s_sub_u32 s74, s74, 32768\n" \
"s_cselect_b32 s74, 131072, s74\n" \
"ds_read_b128 a[196:199], v206 offset:12288\n" \
"v_mfma_f32_16x16x32_bf16 v[0:3], a[104:107], a[72:75], v[0:3]\n" \
"ds_read_b64_tr_b16 a[208:209], v209 offset:4096\n" \
"v_add_u32 v213, s74, v211\n" \
"v_mfma_f32_16x16x32_bf16 v[0:3], a[108:111], a[76:79], v[0:3]\n" \
"s_waitcnt lgkmcnt(3)\n" \
"global_load_lds_dwordx4 v253, s[48:49] offset:0\n" \
"v_add_u32 v214, s74, v212\n" \
"v_mfma_f32_16x16x32_bf16 v[4:7], a[112:115], a[72:75], v[4:7]\n" \
"s_add_u32 s48, s48, 0x60000\n" \
"s_addc_u32 s49, s49, 0\n" \
"ds_read_b64_tr_b16 a[210:211], v209 offset:5120\n" \
"v_mfma_f32_16x16x32_bf16 v[4:7], a[116:119], a[76:79], v[4:7]\n" \
"ds_read_b64_tr_b16 a[212:213], v209 offset:16384\n" \
"v_xor_b32 v215, 32, v214\n" \
"v_mfma_f32_16x16x32_bf16 v[12:15], a[112:115], a[80:83], v[12:15]\n" \
"s_lshl_b32 s89, s61, 4\n" \
"ds_read_b64_tr_b16 a[214:215], v209 offset:17408\n" \
"v_subrev_u32 v147, s89, v251\n" \
"v_mfma_f32_16x16x32_bf16 v[12:15], a[116:119], a[84:87], v[12:15]\n" \
"global_load_lds_dwordx4 v253, s[50:51] offset:-3072\n" \
"v_mfma_f32_16x16x32_bf16 v[8:11], a[104:107], a[80:83], v[8:11]\n" \
"s_and_b32 s90, s86, 0xff800000\n" \
"ds_read_b64_tr_b16 a[216:217], v210 offset:4096\n" \
"v_subrev_u32 v152, 0, v147\n" \
"v_mfma_f32_16x16x32_bf16 v[8:11], a[108:111], a[84:87], v[8:11]\n" \
"ds_read_b64_tr_b16 a[218:219], v210 offset:5120\n" \
"v_sub_u32 v144, 7, v147\n" \
"Lpg10309_%=:\n" \
"s_cbranch_execz Lpg12210_%=\n" \
"v_mfma_f32_16x16x32_bf16 v[48:51], a[96:99], a[80:83], v[48:51]\n" \
"ds_read_b64_tr_b16 a[220:221], v210 offset:16384\n" \
"v_or_b32 v152, v152, v144\n" \
"v_mfma_f32_16x16x32_bf16 v[48:51], a[100:103], a[84:87], v[48:51]\n" \
"global_load_lds_dwordx4 v253, s[50:51] offset:-1024\n" \
"v_ashrrev_i32 v152, 31, v152\n" \
"v_mfma_f32_16x16x32_bf16 v[56:59], a[96:99], a[88:91], v[56:59]\n" \
"ds_read_b64_tr_b16 a[222:223], v210 offset:17408\n" \
"v_and_b32 v152, 0xff800000, v152\n" \
"v_mfma_f32_16x16x32_bf16 v[56:59], a[100:103], a[92:95], v[56:59]\n" \
"ds_read_b64_tr_b16 a[200:201], v210 offset:2048\n" \
"v_mov_b32 v160, v152\n" \
"v_mfma_f32_16x16x32_bf16 v[16:19], a[104:107], a[88:91], v[16:19]\n" \
"ds_read_b64_tr_b16 a[202:203], v210 offset:3072\n" \
"v_mov_b32 v168, v152\n" \
"v_mfma_f32_16x16x32_bf16 v[16:19], a[108:111], a[92:95], v[16:19]\n" \
"global_load_lds_dwordx4 v253, s[50:51] offset:1024\n" \
"v_mov_b32 v176, v152\n" \
"v_mfma_f32_16x16x32_bf16 v[20:23], a[112:115], a[88:91], v[20:23]\n" \
"s_add_u32 s50, s50, 0x60000\n" \
"s_addc_u32 s51, s51, 0\n" \
"ds_read_b64_tr_b16 a[204:205], v210 offset:14336\n" \
"v_mfma_f32_16x16x32_bf16 v[20:23], a[116:119], a[92:95], v[20:23]\n" \
"s_waitcnt lgkmcnt(3)\n" \
"ds_read_b64_tr_b16 a[206:207], v210 offset:15360\n" \
"v_sub_u32 v156, -9, v147\n" \
"v_mfma_f32_16x16x32_bf16 v[28:31], a[112:115], a[192:195], v[28:31]\n" \
"v_add_u32 v205, 0xffff3000, v205\n" \
"v_add_u32 v206, 0xffff3000, v206\n" \
"v_mfma_f32_16x16x32_bf16 v[28:31], a[116:119], a[196:199], v[28:31]\n" \
"buffer_load_dword v220, v221, s[56:59], s81 offen sc1\n" \
"v_mfma_f32_16x16x32_bf16 v[24:27], a[104:107], a[192:195], v[24:27]\n" \
"v_add_u32 v209, 0xffff3000, v209\n" \
"v_add_u32 v210, 0xffff3000, v210\n" \
"v_mfma_f32_16x16x32_bf16 v[24:27], a[108:111], a[196:199], v[24:27]\n" \
"s_waitcnt vmcnt(11) lgkmcnt(0)\n" \
"v_ashrrev_i32 v156, 31, v156\n" \
"v_and_b32 v156, 0xff800000, v156\n" \
"v_mfma_f32_16x16x32_bf16 v[64:67], a[96:99], a[192:195], v[64:67]\n" \
"v_mov_b32 v164, v156\n" \
"v_mov_b32 v172, v156\n" \
"v_mfma_f32_16x16x32_bf16 v[64:67], a[100:103], a[196:199], v[64:67]\n" \
"s_barrier\n" \
"v_mfma_f32_16x16x32_bf16 v[80:83], a[72:75], a[208:211], v[80:83]\n" \
"ds_read_b64_tr_b16 a[96:97], v209 offset:2048\n" \
"v_or_b32 v148, s90, v156\n" \
"v_mfma_f32_16x16x32_bf16 v[80:83], a[76:79], a[212:215], v[80:83]\n" \
"ds_read_b64_tr_b16 a[98:99], v209 offset:3072\n" \
"v_mov_b32 v188, 0x3f803f80\n" \
"v_mfma_f32_16x16x32_bf16 v[84:87], a[72:75], a[216:219], v[84:87]\n" \
"s_add_u32 m0, s95, 118784\n" \
"ds_read_b64_tr_b16 a[100:101], v209 offset:14336\n" \
"v_subrev_u32 v153, 1, v147\n" \
"v_mfma_f32_16x16x32_bf16 v[84:87], a[76:79], a[220:223], v[84:87]\n" \
"global_load_lds_dwordx4 v252, s[42:43] offset:0\n" \
"v_sub_u32 v144, 8, v147\n" \
"v_mfma_f32_16x16x32_bf16 v[44:47], a[200:203], a[72:75], v[44:47]\n" \
"ds_read_b64_tr_b16 a[102:103], v209 offset:15360\n" \
"v_or_b32 v153, v153, v144\n" \
"v_mfma_f32_16x16x32_bf16 v[44:47], a[204:207], a[76:79], v[44:47]\n" \
"ds_read_b128 a[72:75], v205 offset:0\n" \
"ds_read_b64_tr_b16 a[104:105], v209 offset:0\n" \
"v_mfma_f32_16x16x32_bf16 v[52:55], a[200:203], a[80:83], v[52:55]\n" \
"ds_read_b128 a[76:79], v206 offset:0\n" \
"v_ashrrev_i32 v153, 31, v153\n" \
"v_mfma_f32_16x16x32_bf16 v[52:55], a[204:207], a[84:87], v[52:55]\n" \
"global_load_lds_dwordx4 v252, s[42:43] offset:2048\n" \
"v_and_b32 v153, 0xff800000, v153\n" \
"v_mfma_f32_16x16x32_bf16 v[92:95], a[80:83], a[216:219], v[92:95]\n" \
"ds_read_b64_tr_b16 a[106:107], v209 offset:1024\n" \
"v_mov_b32 v161, v153\n" \
"v_mfma_f32_16x16x32_bf16 v[92:95], a[84:87], a[220:223], v[92:95]\n" \
"ds_read_b64_tr_b16 a[108:109], v209 offset:12288\n" \
"v_mov_b32 v169, v153\n" \
"v_mfma_f32_16x16x32_bf16 v[88:91], a[80:83], a[208:211], v[88:91]\n" \
"ds_read_b64_tr_b16 a[110:111], v209 offset:13312\n" \
"v_mov_b32 v177, v153\n" \
"v_mfma_f32_16x16x32_bf16 v[88:91], a[84:87], a[212:215], v[88:91]\n" \
"global_load_lds_dwordx4 v252, s[44:45] offset:1024\n" \
"v_sub_u32 v157, -8, v147\n" \
"v_mfma_f32_16x16x32_bf16 v[96:99], a[88:91], a[208:211], v[96:99]\n" \
"ds_read_b128 a[80:83], v205 offset:4096\n" \
"v_ashrrev_i32 v157, 31, v157\n" \
"v_mfma_f32_16x16x32_bf16 v[96:99], a[92:95], a[212:215], v[96:99]\n" \
"ds_read_b128 a[84:87], v206 offset:4096\n" \
"v_and_b32 v157, 0xff800000, v157\n" \
"v_mfma_f32_16x16x32_bf16 v[100:103], a[88:91], a[216:219], v[100:103]\n" \
"ds_read_b64_tr_b16 a[112:113], v210 offset:0\n" \
"v_mov_b32 v165, v157\n" \
"v_mfma_f32_16x16x32_bf16 v[100:103], a[92:95], a[220:223], v[100:103]\n" \
"global_load_lds_dwordx4 v252, s[44:45] offset:3072\n" \
"v_mov_b32 v173, v157\n" \
"v_mfma_f32_16x16x32_bf16 v[60:63], a[200:203], a[88:91], v[60:63]\n" \
"ds_read_b64_tr_b16 a[114:115], v210 offset:1024\n" \
"v_or_b32 v149, s90, v157\n" \
"v_mfma_f32_16x16x32_bf16 v[60:63], a[204:207], a[92:95], v[60:63]\n" \
"ds_read_b64_tr_b16 a[116:117], v210 offset:12288\n" \
"v_mov_b32 v189, 0x3f803f80\n" \
"v_mfma_f32_16x16x32_bf16 v[68:71], a[200:203], a[192:195], v[68:71]\n" \
"s_add_u32 m0, s84, 143360\n" \
"ds_read_b128 a[88:91], v205 offset:8192\n" \
"v_subrev_u32 v154, 2, v147\n" \
"v_mfma_f32_16x16x32_bf16 v[68:71], a[204:207], a[196:199], v[68:71]\n" \
"global_load_lds_dwordx4 v253, s[48:49] offset:-4096\n" \
"v_sub_u32 v144, 9, v147\n" \
"v_mfma_f32_16x16x32_bf16 v[108:111], a[192:195], a[216:219], v[108:111]\n" \
"ds_read_b128 a[92:95], v206 offset:8192\n" \
"v_or_b32 v154, v154, v144\n" \
"v_mfma_f32_16x16x32_bf16 v[108:111], a[196:199], a[220:223], v[108:111]\n" \
"ds_read_b64_tr_b16 a[118:119], v210 offset:13312\n" \
"v_ashrrev_i32 v154, 31, v154\n" \
"v_mfma_f32_16x16x32_bf16 v[104:107], a[192:195], a[208:211], v[104:107]\n" \
"v_and_b32 v154, 0xff800000, v154\n" \
"v_mov_b32 v162, v154\n" \
"v_mfma_f32_16x16x32_bf16 v[104:107], a[196:199], a[212:215], v[104:107]\n" \
"s_waitcnt lgkmcnt(2)\n" \
"global_load_lds_dwordx4 v253, s[48:49] offset:-2048\n" \
"v_mov_b32 v170, v154\n" \
"v_mfma_f32_16x16x32_bf16 v[40:43], a[96:99], a[72:75], v[40:43]\n" \
"ds_read_b128 a[192:195], v205 offset:12288\n" \
"v_mov_b32 v178, v154\n" \
"v_mfma_f32_16x16x32_bf16 v[40:43], a[100:103], a[76:79], v[40:43]\n" \
"ds_read_b128 a[196:199], v206 offset:12288\n" \
"v_sub_u32 v158, -7, v147\n" \
"v_mfma_f32_16x16x32_bf16 v[0:3], a[104:107], a[72:75], v[0:3]\n" \
"ds_read_b64_tr_b16 a[208:209], v209 offset:4096\n" \
"v_ashrrev_i32 v158, 31, v158\n" \
"v_mfma_f32_16x16x32_bf16 v[0:3], a[108:111], a[76:79], v[0:3]\n" \
"s_waitcnt lgkmcnt(3)\n" \
"global_load_lds_dwordx4 v253, s[48:49] offset:0\n" \
"v_and_b32 v158, 0xff800000, v158\n" \
"v_mfma_f32_16x16x32_bf16 v[4:7], a[112:115], a[72:75], v[4:7]\n" \
"ds_read_b64_tr_b16 a[210:211], v209 offset:5120\n" \
"v_mov_b32 v166, v158\n" \
"v_mfma_f32_16x16x32_bf16 v[4:7], a[116:119], a[76:79], v[4:7]\n" \
"ds_read_b64_tr_b16 a[212:213], v209 offset:16384\n" \
"v_mov_b32 v174, v158\n" \
"v_mfma_f32_16x16x32_bf16 v[12:15], a[112:115], a[80:83], v[12:15]\n" \
"ds_read_b64_tr_b16 a[214:215], v209 offset:17408\n" \
"v_or_b32 v150, s90, v158\n" \
"v_mfma_f32_16x16x32_bf16 v[12:15], a[116:119], a[84:87], v[12:15]\n" \
"global_load_lds_dwordx4 v253, s[50:51] offset:-3072\n" \
"v_mov_b32 v190, 0x3f803f80\n" \
"v_mfma_f32_16x16x32_bf16 v[8:11], a[104:107], a[80:83], v[8:11]\n" \
"ds_read_b64_tr_b16 a[216:217], v210 offset:4096\n" \
"v_subrev_u32 v155, 3, v147\n" \
"v_mfma_f32_16x16x32_bf16 v[8:11], a[108:111], a[84:87], v[8:11]\n" \
"ds_read_b64_tr_b16 a[218:219], v210 offset:5120\n" \
"v_sub_u32 v144, 10, v147\n" \
"v_mfma_f32_16x16x32_bf16 v[48:51], a[96:99], a[80:83], v[48:51]\n" \
"ds_read_b64_tr_b16 a[220:221], v210 offset:16384\n" \
"v_or_b32 v155, v155, v144\n" \
"v_mfma_f32_16x16x32_bf16 v[48:51], a[100:103], a[84:87], v[48:51]\n" \
"global_load_lds_dwordx4 v253, s[50:51] offset:-1024\n" \
"v_ashrrev_i32 v155, 31, v155\n" \
"v_mfma_f32_16x16x32_bf16 v[56:59], a[96:99], a[88:91], v[56:59]\n" \
"ds_read_b64_tr_b16 a[222:223], v210 offset:17408\n" \
"v_and_b32 v155, 0xff800000, v155\n" \
"v_mfma_f32_16x16x32_bf16 v[56:59], a[100:103], a[92:95], v[56:59]\n" \
"ds_read_b64_tr_b16 a[200:201], v210 offset:2048\n" \
"v_mov_b32 v163, v155\n" \
"v_mfma_f32_16x16x32_bf16 v[16:19], a[104:107], a[88:91], v[16:19]\n" \
"ds_read_b64_tr_b16 a[202:203], v210 offset:3072\n" \
"v_mov_b32 v171, v155\n" \
"v_mfma_f32_16x16x32_bf16 v[16:19], a[108:111], a[92:95], v[16:19]\n" \
"global_load_lds_dwordx4 v253, s[50:51] offset:1024\n" \
"v_mov_b32 v179, v155\n" \
"v_mfma_f32_16x16x32_bf16 v[20:23], a[112:115], a[88:91], v[20:23]\n" \
"ds_read_b64_tr_b16 a[204:205], v210 offset:14336\n" \
"v_sub_u32 v159, -6, v147\n" \
"v_mfma_f32_16x16x32_bf16 v[20:23], a[116:119], a[92:95], v[20:23]\n" \
"s_waitcnt lgkmcnt(3)\n" \
"ds_read_b64_tr_b16 a[206:207], v210 offset:15360\n" \
"v_ashrrev_i32 v159, 31, v159\n" \
"v_mfma_f32_16x16x32_bf16 v[28:31], a[112:115], a[192:195], v[28:31]\n" \
"v_add_u32 v205, 0xffff0000, v205\n" \
"v_add_u32 v206, 0xffff0000, v206\n" \
"v_mfma_f32_16x16x32_bf16 v[28:31], a[116:119], a[196:199], v[28:31]\n" \
"v_add_u32 v209, 0xffff0000, v209\n" \
"v_add_u32 v210, 0xffff0000, v210\n" \
"v_mfma_f32_16x16x32_bf16 v[24:27], a[104:107], a[192:195], v[24:27]\n" \
"v_and_b32 v159, 0xff800000, v159\n" \
"v_mov_b32 v167, v159\n" \
"v_mfma_f32_16x16x32_bf16 v[24:27], a[108:111], a[196:199], v[24:27]\n" \
"s_waitcnt vmcnt(11) lgkmcnt(0)\n" \
"v_mov_b32 v175, v159\n" \
"v_or_b32 v151, s90, v159\n" \
"v_mfma_f32_16x16x32_bf16 v[64:67], a[96:99], a[192:195], v[64:67]\n" \
"v_mov_b32 v191, 0x3f803f80\n" \
"v_mov_b32 v144, 0x3e8293ee\n" \
"v_mfma_f32_16x16x32_bf16 v[64:67], a[100:103], a[196:199], v[64:67]\n" \
"s_barrier\n" \
"v_mfma_f32_16x16x32_bf16 v[80:83], a[72:75], a[208:211], v[80:83]\n" \
"s_waitcnt vmcnt(10)\n" \
"ds_read_b64_tr_b16 a[96:97], v209 offset:2048\n" \
"v_cmp_ne_u32 vcc, s60, v220\n" \
"v_mfma_f32_16x16x32_bf16 v[80:83], a[76:79], a[212:215], v[80:83]\n" \
"s_lshr_b32 s89, s61, 1\n" \
"ds_read_b64_tr_b16 a[98:99], v209 offset:3072\n" \
"v_mfma_f32_16x16x32_bf16 v[84:87], a[72:75], a[216:219], v[84:87]\n" \
"s_cmp_eq_u32 s89, 0\n" \
"s_cselect_b32 s16, 0, 0xff00\n" \
"ds_read_b64_tr_b16 a[100:101], v209 offset:14336\n" \
"v_mfma_f32_16x16x32_bf16 v[84:87], a[76:79], a[220:223], v[84:87]\n" \
"s_and_b32 vcc_hi, vcc_lo, s80\n" \
"s_cbranch_scc1 Lpl2_%=\n" \
"Lpr2_%=:\n" \
"v_mfma_f32_16x16x32_bf16 v[44:47], a[200:203], a[72:75], v[44:47]\n" \
"s_add_u32 m0, s75, s95\n" \
"ds_read_b64_tr_b16 a[102:103], v209 offset:15360\n" \
"v_mfma_f32_16x16x32_bf16 v[44:47], a[204:207], a[76:79], v[44:47]\n" \
"s_mov_b32 s17, s16\n" \
"ds_read_b128 a[72:75], v205 offset:0\n" \
"ds_read_b64_tr_b16 a[104:105], v209 offset:0\n" \
"v_mfma_f32_16x16x32_bf16 v[52:55], a[200:203], a[80:83], v[52:55]\n" \
"ds_read_b128 a[76:79], v206 offset:0\n" \
"global_load_lds_dwordx4 v222, s[36:37] offset:0 sc1\n" \
"v_mfma_f32_16x16x32_bf16 v[52:55], a[204:207], a[84:87], v[52:55]\n" \
"v_add_u32 v146, 0x1c90080, v249\n" \
"v_cndmask_b32 v146, v249, v146, s[16:17]\n" \
"v_mfma_f32_16x16x32_bf16 v[92:95], a[80:83], a[216:219], v[92:95]\n" \
"s_andn2_b32 s12, s12, s86\n" \
"ds_read_b64_tr_b16 a[106:107], v209 offset:1024\n" \
"v_mfma_f32_16x16x32_bf16 v[92:95], a[84:87], a[220:223], v[92:95]\n" \
"s_mov_b32 s13, s12\n" \
"ds_read_b64_tr_b16 a[108:109], v209 offset:12288\n" \
"global_load_lds_dwordx4 v222, s[36:37] offset:1024 sc1\n" \
"v_mfma_f32_16x16x32_bf16 v[88:91], a[80:83], a[208:211], v[88:91]\n" \
"s_not_b32 s15, s86\n" \
"ds_read_b64_tr_b16 a[110:111], v209 offset:13312\n" \
"v_mfma_f32_16x16x32_bf16 v[88:91], a[84:87], a[212:215], v[88:91]\n" \
"s_or_b64 s[92:93], s[12:13], s[14:15]\n" \
"v_mfma_f32_16x16x32_bf16 v[96:99], a[88:91], a[208:211], v[96:99]\n" \
"ds_read_b128 a[80:83], v205 offset:4096\n" \
"v_mfma_f32_16x16x32_bf16 v[96:99], a[92:95], a[212:215], v[96:99]\n" \
"ds_read_b128 a[84:87], v206 offset:4096\n" \
"global_load_lds_dwordx4 v222, s[36:37] offset:2048 sc1\n" \
"v_mfma_f32_16x16x32_bf16 v[100:103], a[88:91], a[216:219], v[100:103]\n" \
"ds_read_b64_tr_b16 a[112:113], v210 offset:0\n" \
"v_mfma_f32_16x16x32_bf16 v[100:103], a[92:95], a[220:223], v[100:103]\n" \
"v_mfma_f32_16x16x32_bf16 v[60:63], a[200:203], a[88:91], v[60:63]\n" \
"ds_read_b64_tr_b16 a[114:115], v210 offset:1024\n" \
"v_mfma_f32_16x16x32_bf16 v[60:63], a[204:207], a[92:95], v[60:63]\n" \
"ds_read_b64_tr_b16 a[116:117], v210 offset:12288\n" \
"global_load_lds_dwordx4 v222, s[36:37] offset:3072 sc1\n" \
"v_mfma_f32_16x16x32_bf16 v[68:71], a[200:203], a[192:195], v[68:71]\n" \
"ds_read_b128 a[88:91], v205 offset:8192\n" \
"s_add_u32 s36, s36, 0x1000\n" \
"v_mfma_f32_16x16x32_bf16 v[68:71], a[204:207], a[196:199], v[68:71]\n" \
"v_mfma_f32_16x16x32_bf16 v[108:111], a[192:195], a[216:219], v[108:111]\n" \
"ds_read_b128 a[92:95], v206 offset:8192\n" \
"s_add_u32 m0, m0, 0x5000\n" \
"v_mfma_f32_16x16x32_bf16 v[108:111], a[196:199], a[220:223], v[108:111]\n" \
"ds_read_b64_tr_b16 a[118:119], v210 offset:13312\n" \
"global_load_lds_dwordx4 v216, s[38:39] offset:-4096\n" \
"v_mfma_f32_16x16x32_bf16 v[104:107], a[192:195], a[208:211], v[104:107]\n" \
"v_mfma_f32_16x16x32_bf16 v[104:107], a[196:199], a[212:215], v[104:107]\n" \
"s_waitcnt lgkmcnt(2)\n" \
"v_mfma_f32_16x16x32_bf16 v[40:43], a[96:99], a[72:75], v[40:43]\n" \
"ds_read_b128 a[192:195], v205 offset:12288\n" \
"v_mfma_f32_16x16x32_bf16 v[40:43], a[100:103], a[76:79], v[40:43]\n" \
"ds_read_b128 a[196:199], v206 offset:12288\n" \
"global_load_lds_dwordx4 v217, s[38:39] offset:-3072\n" \
"v_mfma_f32_16x16x32_bf16 v[0:3], a[104:107], a[72:75], v[0:3]\n" \
"ds_read_b64_tr_b16 a[208:209], v209 offset:4096\n" \
"v_mfma_f32_16x16x32_bf16 v[0:3], a[108:111], a[76:79], v[0:3]\n" \
"s_waitcnt lgkmcnt(3)\n" \
"v_mfma_f32_16x16x32_bf16 v[4:7], a[112:115], a[72:75], v[4:7]\n" \
"ds_read_b64_tr_b16 a[210:211], v209 offset:5120\n" \
"v_mfma_f32_16x16x32_bf16 v[4:7], a[116:119], a[76:79], v[4:7]\n" \
"ds_read_b64_tr_b16 a[212:213], v209 offset:16384\n" \
"global_load_lds_dwordx4 v218, s[38:39] offset:-2048\n" \
"v_mfma_f32_16x16x32_bf16 v[12:15], a[112:115], a[80:83], v[12:15]\n" \
"ds_read_b64_tr_b16 a[214:215], v209 offset:17408\n" \
"v_mfma_f32_16x16x32_bf16 v[12:15], a[116:119], a[84:87], v[12:15]\n" \
"v_mfma_f32_16x16x32_bf16 v[8:11], a[104:107], a[80:83], v[8:11]\n" \
"ds_read_b64_tr_b16 a[216:217], v210 offset:4096\n" \
"v_mfma_f32_16x16x32_bf16 v[8:11], a[108:111], a[84:87], v[8:11]\n" \
"ds_read_b64_tr_b16 a[218:219], v210 offset:5120\n" \
"global_load_lds_dwordx4 v219, s[38:39] offset:-1024\n" \
"v_mfma_f32_16x16x32_bf16 v[48:51], a[96:99], a[80:83], v[48:51]\n" \
"s_add_u32 s38, s38, 0x20000\n" \
"s_addc_u32 s39, s39, 0\n" \
"ds_read_b64_tr_b16 a[220:221], v210 offset:16384\n" \
"v_mfma_f32_16x16x32_bf16 v[48:51], a[100:103], a[84:87], v[48:51]\n" \
"s_sub_u32 s75, s75, 32768\n" \
"s_cselect_b32 s75, 131072, s75\n" \
"v_mfma_f32_16x16x32_bf16 v[56:59], a[96:99], a[88:91], v[56:59]\n" \
"ds_read_b64_tr_b16 a[222:223], v210 offset:17408\n" \
"v_mfma_f32_16x16x32_bf16 v[56:59], a[100:103], a[92:95], v[56:59]\n" \
"ds_read_b64_tr_b16 a[200:201], v210 offset:2048\n" \
"v_mfma_f32_16x16x32_bf16 v[16:19], a[104:107], a[88:91], v[16:19]\n" \
"ds_read_b64_tr_b16 a[202:203], v210 offset:3072\n" \
"v_mfma_f32_16x16x32_bf16 v[16:19], a[108:111], a[92:95], v[16:19]\n" \
"v_mfma_f32_16x16x32_bf16 v[20:23], a[112:115], a[88:91], v[20:23]\n" \
"ds_read_b64_tr_b16 a[204:205], v210 offset:14336\n" \
"v_mfma_f32_16x16x32_bf16 v[20:23], a[116:119], a[92:95], v[20:23]\n" \
"s_waitcnt lgkmcnt(3)\n" \
"ds_read_b64_tr_b16 a[206:207], v210 offset:15360\n" \
"v_mfma_f32_16x16x32_bf16 v[28:31], a[112:115], a[192:195], v[28:31]\n" \
"v_add_u32 v205, 0x1d000, v205\n" \
"v_add_u32 v206, 0x1d000, v206\n" \
"v_mfma_f32_16x16x32_bf16 v[28:31], a[116:119], a[196:199], v[28:31]\n" \
"v_add_u32 v209, 0x1d000, v209\n" \
"v_add_u32 v210, 0x1d000, v210\n" \
"v_mfma_f32_16x16x32_bf16 v[24:27], a[104:107], a[192:195], v[24:27]\n" \
"v_mfma_f32_16x16x32_bf16 v[24:27], a[108:111], a[196:199], v[24:27]\n" \
"s_waitcnt vmcnt(8) lgkmcnt(0)\n" \
"v_mfma_f32_16x16x32_bf16 v[64:67], a[96:99], a[192:195], v[64:67]\n" \
"v_mfma_f32_16x16x32_bf16 v[64:67], a[100:103], a[196:199], v[64:67]\n" \
"s_barrier\n" \
"v_mfma_f32_16x16x32_bf16 v[80:83], a[72:75], a[208:211], v[80:83]\n" \
"ds_read_b64_tr_b16 a[96:97], v209 offset:2048\n" \
"v_mfma_f32_16x16x32_bf16 v[80:83], a[76:79], a[212:215], v[80:83]\n" \
"ds_read_b64_tr_b16 a[98:99], v209 offset:3072\n" \
"v_mfma_f32_16x16x32_bf16 v[84:87], a[72:75], a[216:219], v[84:87]\n" \
"s_add_u32 m0, s75, s95\n" \
"ds_read_b64_tr_b16 a[100:101], v209 offset:14336\n" \
"v_mfma_f32_16x16x32_bf16 v[84:87], a[76:79], a[220:223], v[84:87]\n" \
"global_load_lds_dwordx4 v222, s[36:37] offset:0 sc1\n" \
"v_mfma_f32_16x16x32_bf16 v[44:47], a[200:203], a[72:75], v[44:47]\n" \
"ds_read_b64_tr_b16 a[102:103], v209 offset:15360\n" \
"v_mfma_f32_16x16x32_bf16 v[44:47], a[204:207], a[76:79], v[44:47]\n" \
"ds_read_b128 a[72:75], v205 offset:0\n" \
"ds_read_b64_tr_b16 a[104:105], v209 offset:0\n" \
"v_mfma_f32_16x16x32_bf16 v[52:55], a[200:203], a[80:83], v[52:55]\n" \
"ds_read_b128 a[76:79], v206 offset:0\n" \
"v_mfma_f32_16x16x32_bf16 v[52:55], a[204:207], a[84:87], v[52:55]\n" \
"global_load_lds_dwordx4 v222, s[36:37] offset:1024 sc1\n" \
"v_mfma_f32_16x16x32_bf16 v[92:95], a[80:83], a[216:219], v[92:95]\n" \
"ds_read_b64_tr_b16 a[106:107], v209 offset:1024\n" \
"v_mfma_f32_16x16x32_bf16 v[92:95], a[84:87], a[220:223], v[92:95]\n" \
"ds_read_b64_tr_b16 a[108:109], v209 offset:12288\n" \
"v_mfma_f32_16x16x32_bf16 v[88:91], a[80:83], a[208:211], v[88:91]\n" \
"ds_read_b64_tr_b16 a[110:111], v209 offset:13312\n" \
"v_mfma_f32_16x16x32_bf16 v[88:91], a[84:87], a[212:215], v[88:91]\n" \
"global_load_lds_dwordx4 v222, s[36:37] offset:2048 sc1\n" \
"v_mfma_f32_16x16x32_bf16 v[96:99], a[88:91], a[208:211], v[96:99]\n" \
"ds_read_b128 a[80:83], v205 offset:4096\n" \
"v_mfma_f32_16x16x32_bf16 v[96:99], a[92:95], a[212:215], v[96:99]\n" \
"ds_read_b128 a[84:87], v206 offset:4096\n" \
"v_mfma_f32_16x16x32_bf16 v[100:103], a[88:91], a[216:219], v[100:103]\n" \
"ds_read_b64_tr_b16 a[112:113], v210 offset:0\n" \
"v_mfma_f32_16x16x32_bf16 v[100:103], a[92:95], a[220:223], v[100:103]\n" \
"global_load_lds_dwordx4 v222, s[36:37] offset:3072 sc1\n" \
"v_mfma_f32_16x16x32_bf16 v[60:63], a[200:203], a[88:91], v[60:63]\n" \
"s_add_u32 s36, s36, 0x1000\n" \
"ds_read_b64_tr_b16 a[114:115], v210 offset:1024\n" \
"v_mfma_f32_16x16x32_bf16 v[60:63], a[204:207], a[92:95], v[60:63]\n" \
"ds_read_b64_tr_b16 a[116:117], v210 offset:12288\n" \
"v_mfma_f32_16x16x32_bf16 v[68:71], a[200:203], a[192:195], v[68:71]\n" \
"ds_read_b128 a[88:91], v205 offset:8192\n" \
"s_add_u32 m0, m0, 0x5000\n" \
"v_mfma_f32_16x16x32_bf16 v[68:71], a[204:207], a[196:199], v[68:71]\n" \
"global_load_lds_dwordx4 v216, s[38:39] offset:-4096\n" \
"v_mfma_f32_16x16x32_bf16 v[108:111], a[192:195], a[216:219], v[108:111]\n" \
"ds_read_b128 a[92:95], v206 offset:8192\n" \
"v_mfma_f32_16x16x32_bf16 v[108:111], a[196:199], a[220:223], v[108:111]\n" \
"ds_read_b64_tr_b16 a[118:119], v210 offset:13312\n" \
"v_mfma_f32_16x16x32_bf16 v[104:107], a[192:195], a[208:211], v[104:107]\n" \
"v_mfma_f32_16x16x32_bf16 v[104:107], a[196:199], a[212:215], v[104:107]\n" \
"s_waitcnt lgkmcnt(2)\n" \
"global_load_lds_dwordx4 v217, s[38:39] offset:-3072\n" \
"v_mfma_f32_16x16x32_bf16 v[40:43], a[96:99], a[72:75], v[40:43]\n" \
"ds_read_b128 a[192:195], v205 offset:12288\n" \
"v_mfma_f32_16x16x32_bf16 v[40:43], a[100:103], a[76:79], v[40:43]\n" \
"ds_read_b128 a[196:199], v206 offset:12288\n" \
"v_mfma_f32_16x16x32_bf16 v[0:3], a[104:107], a[72:75], v[0:3]\n" \
"ds_read_b64_tr_b16 a[208:209], v209 offset:4096\n" \
"v_mfma_f32_16x16x32_bf16 v[0:3], a[108:111], a[76:79], v[0:3]\n" \
"s_waitcnt lgkmcnt(3)\n" \
"global_load_lds_dwordx4 v218, s[38:39] offset:-2048\n" \
"v_mfma_f32_16x16x32_bf16 v[4:7], a[112:115], a[72:75], v[4:7]\n" \
"ds_read_b64_tr_b16 a[210:211], v209 offset:5120\n" \
"v_mfma_f32_16x16x32_bf16 v[4:7], a[116:119], a[76:79], v[4:7]\n" \
"ds_read_b64_tr_b16 a[212:213], v209 offset:16384\n" \
"v_mfma_f32_16x16x32_bf16 v[48:51], a[96:99], a[80:83], v[48:51]\n" \
"ds_read_b64_tr_b16 a[214:215], v209 offset:17408\n" \
"v_mfma_f32_16x16x32_bf16 v[48:51], a[100:103], a[84:87], v[48:51]\n" \
"global_load_lds_dwordx4 v219, s[38:39] offset:-1024\n" \
"v_mfma_f32_16x16x32_bf16 v[8:11], a[104:107], a[80:83], v[8:11]\n" \
"s_add_u32 s38, s38, 0x20000\n" \
"s_addc_u32 s39, s39, 0\n" \
"ds_read_b64_tr_b16 a[216:217], v210 offset:4096\n" \
"v_mfma_f32_16x16x32_bf16 v[8:11], a[108:111], a[84:87], v[8:11]\n" \
"s_sub_u32 s75, s75, 32768\n" \
"s_cselect_b32 s75, 131072, s75\n" \
"ds_read_b64_tr_b16 a[218:219], v210 offset:5120\n" \
"v_mfma_f32_16x16x32_bf16 v[12:15], a[112:115], a[80:83], v[12:15]\n" \
"s_add_u32 m0, s75, s95\n" \
"ds_read_b64_tr_b16 a[220:221], v210 offset:16384\n" \
"v_cvt_pk_bf16_f32 v112, v0, v1\n" \
"v_mfma_f32_16x16x32_bf16 v[12:15], a[116:119], a[84:87], v[12:15]\n" \
"global_load_lds_dwordx4 v222, s[36:37] offset:0 sc1\n" \
"v_cvt_pk_bf16_f32 v113, v2, v3\n" \
"v_mfma_f32_16x16x32_bf16 v[56:59], a[96:99], a[88:91], v[56:59]\n" \
"ds_read_b64_tr_b16 a[222:223], v210 offset:17408\n" \
"v_cvt_pk_bf16_f32 v114, v4, v5\n" \
"v_mfma_f32_16x16x32_bf16 v[56:59], a[100:103], a[92:95], v[56:59]\n" \
"ds_read_b64_tr_b16 a[200:201], v210 offset:2048\n" \
"v_cvt_pk_bf16_f32 v115, v6, v7\n" \
"v_mfma_f32_16x16x32_bf16 v[16:19], a[104:107], a[88:91], v[16:19]\n" \
"ds_read_b64_tr_b16 a[202:203], v210 offset:3072\n" \
"v_cvt_pk_bf16_f32 v116, v8, v9\n" \
"v_mfma_f32_16x16x32_bf16 v[16:19], a[108:111], a[92:95], v[16:19]\n" \
"global_load_lds_dwordx4 v222, s[36:37] offset:1024 sc1\n" \
"v_cvt_pk_bf16_f32 v117, v10, v11\n" \
"v_mfma_f32_16x16x32_bf16 v[20:23], a[112:115], a[88:91], v[20:23]\n" \
"ds_read_b64_tr_b16 a[204:205], v210 offset:14336\n" \
"v_cvt_pk_bf16_f32 v118, v12, v13\n" \
"v_mfma_f32_16x16x32_bf16 v[20:23], a[116:119], a[92:95], v[20:23]\n" \
"s_waitcnt lgkmcnt(3)\n" \
"ds_read_b64_tr_b16 a[206:207], v210 offset:15360\n" \
"v_cvt_pk_bf16_f32 v119, v14, v15\n" \
"v_mfma_f32_16x16x32_bf16 v[64:67], a[96:99], a[192:195], v[64:67]\n" \
"v_mfma_f32_16x16x32_bf16 v[64:67], a[100:103], a[196:199], v[64:67]\n" \
"global_load_lds_dwordx4 v222, s[36:37] offset:2048 sc1\n" \
"v_mfma_f32_16x16x32_bf16 v[24:27], a[104:107], a[192:195], v[24:27]\n" \
"v_cvt_pk_bf16_f32 v120, v16, v17\n" \
"v_cvt_pk_bf16_f32 v121, v18, v19\n" \
"v_mfma_f32_16x16x32_bf16 v[24:27], a[108:111], a[196:199], v[24:27]\n" \
"s_waitcnt vmcnt(11) lgkmcnt(0)\n" \
"v_cvt_pk_bf16_f32 v122, v20, v21\n" \
"v_cvt_pk_bf16_f32 v123, v22, v23\n" \
"v_mfma_f32_16x16x32_bf16 v[28:31], a[112:115], a[192:195], v[28:31]\n" \
"v_mfma_f32_16x16x32_bf16 v[28:31], a[116:119], a[196:199], v[28:31]\n" \
"s_barrier\n" \
"v_mfma_f32_16x16x32_bf16 v[68:71], a[200:203], a[192:195], v[68:71]\n" \
"v_mfma_f32_16x16x32_bf16 v[68:71], a[204:207], a[196:199], v[68:71]\n" \
"ds_read_b64_tr_b16 a[32:33], v214 offset:16384\n" \
"v_mfma_f32_16x16x32_bf16 v[104:107], a[192:195], a[208:211], v[104:107]\n" \
"ds_read_b64_tr_b16 a[34:35], v214 offset:18432\n" \
"v_cvt_pk_bf16_f32 v124, v24, v25\n" \
"v_mfma_f32_16x16x32_bf16 v[104:107], a[196:199], a[212:215], v[104:107]\n" \
"global_load_lds_dwordx4 v222, s[36:37] offset:3072 sc1\n" \
"v_cvt_pk_bf16_f32 v125, v26, v27\n" \
"v_cvt_pk_bf16_f32 v126, v28, v29\n" \
"v_mfma_f32_16x16x32_bf16 v[108:111], a[192:195], a[216:219], v[108:111]\n" \
"s_add_u32 s36, s36, 0x1000\n" \
"ds_read_b64_tr_b16 a[36:37], v214 offset:24576\n" \
"v_cvt_pk_bf16_f32 v32, v64, v65\n" \
"v_mfma_f32_16x16x32_bf16 v[108:111], a[196:199], a[220:223], v[108:111]\n" \
"ds_read_b64_tr_b16 a[38:39], v214 offset:26624\n" \
"v_cvt_pk_bf16_f32 v33, v66, v67\n" \
"v_mfma_f32_16x16x32_bf16 v[44:47], a[200:203], a[72:75], v[44:47]\n" \
"s_add_u32 m0, m0, 0x5000\n" \
"v_cvt_pk_bf16_f32 v34, v68, v69\n" \
"v_cvt_pk_bf16_f32 v37, v70, v71\n" \
"v_mfma_f32_16x16x32_bf16 v[44:47], a[204:207], a[76:79], v[44:47]\n" \
"ds_read_b128 a[0:3], v213 offset:0\n" \
"global_load_lds_dwordx4 v216, s[38:39] offset:-4096\n" \
"v_mfma_f32_16x16x32_bf16 v[80:83], a[72:75], a[208:211], v[80:83]\n" \
"ds_read_b128 a[4:7], v213 offset:2048\n" \
"v_bitop3_b32 v35, v32, v33, v34 bitop3:0x96\n" \
"v_mfma_f32_16x16x32_bf16 v[80:83], a[76:79], a[212:215], v[80:83]\n" \
"v_mov_b32 v73, 0\n" \
"v_xor_b32 v35, s60, v35\n" \
"v_mfma_f32_16x16x32_bf16 v[84:87], a[72:75], a[216:219], v[84:87]\n" \
"ds_read_b64_tr_b16 a[40:41], v215 offset:16384\n" \
"v_cvt_pk_bf16_f32 v127, v30, v31\n" \
"v_mfma_f32_16x16x32_bf16 v[84:87], a[76:79], a[220:223], v[84:87]\n" \
"buffer_store_dwordx4 v[32:35], v207, s[56:59], s46 offen offset:0 sc1\n" \
"ds_read_b64_tr_b16 a[42:43], v215 offset:18432\n" \
"global_load_lds_dwordx4 v217, s[38:39] offset:-3072\n" \
"v_cvt_pk_bf16_f32 v38, v104, v105\n" \
"v_cvt_pk_bf16_f32 v39, v106, v107\n" \
"v_mfma_f32_16x16x32_bf16 v[52:55], a[200:203], a[80:83], v[52:55]\n" \
"v_cvt_pk_bf16_f32 v74, v108, v109\n" \
"v_cvt_pk_bf16_f32 v75, v110, v111\n" \
"v_mfma_f32_16x16x32_bf16 v[52:55], a[204:207], a[84:87], v[52:55]\n" \
"ds_read_b64_tr_b16 a[44:45], v215 offset:24576\n" \
"v_bitop3_b32 v36, v37, v38, v39 bitop3:0x96\n" \
"v_mfma_f32_16x16x32_bf16 v[88:91], a[80:83], a[208:211], v[88:91]\n" \
"ds_read_b64_tr_b16 a[46:47], v215 offset:26624\n" \
"v_bitop3_b32 v72, s60, v74, v75 bitop3:0x96\n" \
"v_mfma_f32_16x16x32_bf16 v[88:91], a[84:87], a[212:215], v[88:91]\n" \
"global_load_lds_dwordx4 v218, s[38:39] offset:-2048\n" \
"v_xor_b32 v36, s60, v36\n" \
"v_mfma_f32_16x16x32_bf16 v[92:95], a[80:83], a[216:219], v[92:95]\n" \
"ds_read_b128 a[8:11], v213 offset:256\n" \
"v_cvt_pk_bf16_f32 v132, v40, v41\n" \
"v_mfma_f32_16x16x32_bf16 v[92:95], a[84:87], a[220:223], v[92:95]\n" \
"buffer_store_dwordx4 v[36:39], v208, s[56:59], s46 offen offset:1024 sc1\n" \
"ds_read_b128 a[12:15], v213 offset:2304\n" \
"v_cvt_pk_bf16_f32 v133, v42, v43\n" \
"v_cvt_pk_bf16_f32 v134, v44, v45\n" \
"v_cvt_pk_bf16_f32 v135, v46, v47\n" \
"v_mfma_f32_16x16x32_bf16 v[60:63], a[200:203], a[88:91], v[60:63]\n" \
"v_cvt_pk_bf16_f32 v136, v48, v49\n" \
"v_cvt_pk_bf16_f32 v137, v50, v51\n" \
"v_mfma_f32_16x16x32_bf16 v[60:63], a[204:207], a[92:95], v[60:63]\n" \
"ds_read_b128 a[16:19], v213 offset:4096\n" \
"global_load_lds_dwordx4 v219, s[38:39] offset:-1024\n" \
"v_mfma_f32_16x16x32_bf16 v[96:99], a[88:91], a[208:211], v[96:99]\n" \
"s_add_u32 s38, s38, 0x20000\n" \
"s_addc_u32 s39, s39, 0\n" \
"ds_read_b128 a[20:23], v213 offset:6144\n" \
"v_mfma_f32_16x16x32_bf16 v[96:99], a[92:95], a[212:215], v[96:99]\n" \
"buffer_store_dwordx4 v[72:75], v223, s[56:59], s46 offen offset:2048 sc1\n" \
"v_cvt_pk_bf16_f32 v138, v52, v53\n" \
"v_cvt_pk_bf16_f32 v139, v54, v55\n" \
"s_sub_u32 s75, s75, 32768\n" \
"s_cselect_b32 s75, 131072, s75\n" \
"v_mfma_f32_16x16x32_bf16 v[100:103], a[88:91], a[216:219], v[100:103]\n" \
"s_add_u32 m0, s75, s95\n" \
"v_cvt_pk_bf16_f32 v140, v56, v57\n" \
"v_cvt_pk_bf16_f32 v141, v58, v59\n" \
"v_mfma_f32_16x16x32_bf16 v[100:103], a[92:95], a[220:223], v[100:103]\n" \
"s_waitcnt lgkmcnt(2)\n" \
"global_load_lds_dwordx4 v222, s[36:37] offset:0 sc1\n" \
"v_cvt_pk_bf16_f32 v142, v60, v61\n" \
"v_cvt_pk_bf16_f32 v143, v62, v63\n" \
"v_mfma_f32_16x16x32_bf16 v[0:3], a[32:35], a[0:3], 0\n" \
"ds_read_b128 a[24:27], v213 offset:4352\n" \
"v_mfma_f32_16x16x32_bf16 v[0:3], a[36:39], a[4:7], v[0:3]\n" \
"ds_read_b128 a[28:31], v213 offset:6400\n" \
"global_load_lds_dwordx4 v222, s[36:37] offset:1024 sc1\n" \
"v_mfma_f32_16x16x32_bf16 v[4:7], a[40:43], a[0:3], 0\n" \
"s_sub_u32 s74, s74, 32768\n" \
"s_cselect_b32 s74, 131072, s74\n" \
"ds_read_b64_tr_b16 a[48:49], v214 offset:16896\n" \
"v_mfma_f32_16x16x32_bf16 v[4:7], a[44:47], a[4:7], v[4:7]\n" \
"ds_read_b64_tr_b16 a[50:51], v214 offset:18944\n" \
"v_cvt_pk_bf16_f32 v64, v64, v65\n" \
"v_cvt_pk_bf16_f32 v65, v66, v67\n" \
"v_cvt_pk_bf16_f32 v66, v68, v69\n" \
"v_mfma_f32_16x16x32_bf16 v[20:23], a[40:43], a[8:11], 0\n" \
"ds_read_b64_tr_b16 a[52:53], v214 offset:25088\n" \
"global_load_lds_dwordx4 v222, s[36:37] offset:2048 sc1\n" \
"v_mfma_f32_16x16x32_bf16 v[20:23], a[44:47], a[12:15], v[20:23]\n" \
"ds_read_b64_tr_b16 a[54:55], v214 offset:27136\n" \
"v_cvt_pk_bf16_f32 v67, v70, v71\n" \
"v_mfma_f32_16x16x32_bf16 v[16:19], a[32:35], a[8:11], 0\n" \
"ds_read_b64_tr_b16 a[56:57], v215 offset:16896\n" \
"v_cvt_pk_bf16_f32 v196, v80, v81\n" \
"v_mfma_f32_16x16x32_bf16 v[16:19], a[36:39], a[12:15], v[16:19]\n" \
"s_waitcnt lgkmcnt(4)\n" \
"ds_read_b64_tr_b16 a[58:59], v215 offset:18944\n" \
"global_load_lds_dwordx4 v222, s[36:37] offset:3072 sc1\n" \
"v_mfma_f32_16x16x32_bf16 v[32:35], a[32:35], a[16:19], 0\n" \
"s_add_u32 s36, s36, 0x1000\n" \
"ds_read_b64_tr_b16 a[60:61], v215 offset:25088\n" \
"v_cvt_pk_bf16_f32 v197, v82, v83\n" \
"v_mfma_f32_16x16x32_bf16 v[32:35], a[36:39], a[20:23], v[32:35]\n" \
"s_add_u32 m0, m0, 0x5000\n" \
"ds_read_b64_tr_b16 a[62:63], v215 offset:27136\n" \
"v_cvt_pk_bf16_f32 v72, v84, v85\n" \
"v_mfma_f32_16x16x32_bf16 v[36:39], a[40:43], a[16:19], 0\n" \
"global_load_lds_dwordx4 v216, s[38:39] offset:-4096\n" \
"v_add_u32 v213, s74, v211\n" \
"v_mfma_f32_16x16x32_bf16 v[36:39], a[44:47], a[20:23], v[36:39]\n" \
"v_add_u32 v214, s74, v212\n" \
"v_xor_b32 v215, 32, v214\n" \
"v_mfma_f32_16x16x32_bf16 v[52:55], a[40:43], a[24:27], 0\n" \
"v_cvt_pk_bf16_f32 v73, v86, v87\n" \
"v_cvt_pk_bf16_f32 v198, v88, v89\n" \
"v_mfma_f32_16x16x32_bf16 v[52:55], a[44:47], a[28:31], v[52:55]\n" \
"s_waitcnt vmcnt(16) lgkmcnt(0)\n" \
"global_load_lds_dwordx4 v217, s[38:39] offset:-3072\n" \
"Lpg10869_%=:\n" \
"s_cbranch_execz Lpg12298_%=\n" \
"v_cvt_pk_bf16_f32 v199, v90, v91\n" \
"v_mfma_f32_16x16x32_bf16 v[48:51], a[32:35], a[24:27], 0\n" \
"v_cvt_pk_bf16_f32 v74, v92, v93\n" \
"v_cvt_pk_bf16_f32 v75, v94, v95\n" \
"v_mfma_f32_16x16x32_bf16 v[48:51], a[36:39], a[28:31], v[48:51]\n" \
"s_barrier\n" \
"v_cvt_pk_bf16_f32 v200, v96, v97\n" \
"v_mfma_f32_16x16x32_bf16 v[8:11], a[48:51], a[0:3], 0\n" \
"ds_read_b64_tr_b16 a[32:33], v214 offset:16384\n" \
"v_cvt_pk_bf16_f32 v201, v98, v99\n" \
"v_mfma_f32_16x16x32_bf16 v[8:11], a[52:55], a[4:7], v[8:11]\n" \
"ds_read_b64_tr_b16 a[34:35], v214 offset:18432\n" \
"global_load_lds_dwordx4 v218, s[38:39] offset:-2048\n" \
"v_mfma_f32_16x16x32_bf16 v[12:15], a[56:59], a[0:3], 0\n" \
"ds_read_b64_tr_b16 a[36:37], v214 offset:24576\n" \
"v_cvt_pk_bf16_f32 v76, v100, v101\n" \
"v_mfma_f32_16x16x32_bf16 v[12:15], a[60:63], a[4:7], v[12:15]\n" \
"ds_read_b128 a[0:3], v213 offset:0\n" \
"global_load_lds_dwordx4 v219, s[38:39] offset:-1024\n" \
"v_mfma_f32_16x16x32_bf16 v[28:31], a[56:59], a[8:11], 0\n" \
"s_add_u32 s38, s38, 0x20000\n" \
"s_addc_u32 s39, s39, 0\n" \
"ds_read_b128 a[4:7], v213 offset:2048\n" \
"v_mfma_f32_16x16x32_bf16 v[28:31], a[60:63], a[12:15], v[28:31]\n" \
"s_sub_u32 s75, s75, 32768\n" \
"s_cselect_b32 s75, 131072, s75\n" \
"ds_read_b64_tr_b16 a[38:39], v214 offset:26624\n" \
"v_mfma_f32_16x16x32_bf16 v[24:27], a[48:51], a[8:11], 0\n" \
"s_add_u32 m0, s75, s95\n" \
"ds_read_b64_tr_b16 a[40:41], v215 offset:16384\n" \
"v_cvt_pk_bf16_f32 v77, v102, v103\n" \
"v_mfma_f32_16x16x32_bf16 v[24:27], a[52:55], a[12:15], v[24:27]\n" \
"ds_read_b128 a[8:11], v213 offset:256\n" \
"global_load_lds_dwordx4 v222, s[36:37] offset:0 sc1\n" \
"v_mfma_f32_16x16x32_bf16 v[40:43], a[48:51], a[16:19], 0\n" \
"ds_read_b128 a[12:15], v213 offset:2304\n" \
"v_cvt_pk_bf16_f32 v202, v104, v105\n" \
"v_mfma_f32_16x16x32_bf16 v[40:43], a[52:55], a[20:23], v[40:43]\n" \
"ds_read_b64_tr_b16 a[42:43], v215 offset:18432\n" \
"v_cvt_pk_bf16_f32 v203, v106, v107\n" \
"v_mfma_f32_16x16x32_bf16 v[44:47], a[56:59], a[16:19], 0\n" \
"v_cvt_pk_bf16_f32 v78, v108, v109\n" \
"v_cvt_pk_bf16_f32 v79, v110, v111\n" \
"v_mfma_f32_16x16x32_bf16 v[176:179], v[64:67], v[124:127], v[176:179]\n" \
"v_mfma_f32_16x16x32_bf16 v[168:171], v[140:143], v[120:123], v[168:171]\n" \
"v_mfma_f32_16x16x32_bf16 v[160:163], v[136:139], v[116:119], v[160:163]\n" \
"ds_read_b64_tr_b16 a[44:45], v215 offset:24576\n" \
"global_load_lds_dwordx4 v222, s[36:37] offset:1024 sc1\n" \
"v_mfma_f32_16x16x32_bf16 v[44:47], a[60:63], a[20:23], v[44:47]\n" \
"ds_read_b128 a[16:19], v213 offset:4096\n" \
"v_mfma_f32_16x16x32_bf16 v[152:155], v[132:135], v[112:115], v[152:155]\n" \
"v_mfma_f32_16x16x32_bf16 v[172:175], v[140:143], v[124:127], v[172:175]\n" \
"v_mfma_f32_16x16x32_bf16 v[164:167], v[136:139], v[120:123], v[164:167]\n" \
"v_mfma_f32_16x16x32_bf16 v[156:159], v[132:135], v[116:119], v[156:159]\n" \
"v_mfma_f32_16x16x32_bf16 v[60:63], a[56:59], a[24:27], 0\n" \
"ds_read_b128 a[20:23], v213 offset:6144\n" \
"global_load_lds_dwordx4 v222, s[36:37] offset:2048 sc1\n" \
"v_mfma_f32_16x16x32_bf16 v[60:63], a[60:63], a[28:31], v[60:63]\n" \
"ds_read_b64_tr_b16 a[46:47], v215 offset:26624\n" \
"v_mfma_f32_16x16x32_bf16 v[56:59], a[48:51], a[24:27], 0\n" \
"v_max3_f32 v183, v172, v173, v174\n" \
"v_max3_f32 v182, v164, v165, v166\n" \
"v_mfma_f32_16x16x32_bf16 v[56:59], a[52:55], a[28:31], v[56:59]\n" \
"s_waitcnt lgkmcnt(3)\n" \
"v_max3_f32 v181, v156, v157, v158\n" \
"v_max3_f32 v183, v183, v175, v176\n" \
"v_mfma_f32_16x16x32_bf16 v[0:3], a[32:35], a[0:3], v[0:3]\n" \
"ds_read_b128 a[24:27], v213 offset:4352\n" \
"v_max3_f32 v182, v182, v167, v168\n" \
"v_mfma_f32_16x16x32_bf16 v[0:3], a[36:39], a[4:7], v[0:3]\n" \
"s_waitcnt lgkmcnt(1)\n" \
"ds_read_b128 a[28:31], v213 offset:6400\n" \
"global_load_lds_dwordx4 v222, s[36:37] offset:3072 sc1\n" \
"v_mfma_f32_16x16x32_bf16 v[4:7], a[40:43], a[0:3], v[4:7]\n" \
"s_add_u32 s36, s36, 0x1000\n" \
"ds_read_b64_tr_b16 a[48:49], v214 offset:16896\n" \
"v_max3_f32 v181, v181, v159, v160\n" \
"v_mfma_f32_16x16x32_bf16 v[4:7], a[44:47], a[4:7], v[4:7]\n" \
"s_add_u32 m0, m0, 0x5000\n" \
"ds_read_b64_tr_b16 a[50:51], v214 offset:18944\n" \
"v_max3_f32 v183, v183, v177, v178\n" \
"v_mfma_f32_16x16x32_bf16 v[20:23], a[40:43], a[8:11], v[20:23]\n" \
"ds_read_b64_tr_b16 a[52:53], v214 offset:25088\n" \
"global_load_lds_dwordx4 v216, s[38:39] offset:-4096\n" \
"v_mfma_f32_16x16x32_bf16 v[20:23], a[44:47], a[12:15], v[20:23]\n" \
"s_sub_u32 s74, s74, 32768\n" \
"s_cselect_b32 s74, 131072, s74\n" \
"ds_read_b64_tr_b16 a[54:55], v214 offset:27136\n" \
"v_mfma_f32_16x16x32_bf16 v[16:19], a[32:35], a[8:11], v[16:19]\n" \
"ds_read_b64_tr_b16 a[56:57], v215 offset:16896\n" \
"v_max3_f32 v182, v182, v169, v170\n" \
"v_mfma_f32_16x16x32_bf16 v[16:19], a[36:39], a[12:15], v[16:19]\n" \
"ds_read_b64_tr_b16 a[58:59], v215 offset:18944\n" \
"global_load_lds_dwordx4 v217, s[38:39] offset:-3072\n" \
"v_mfma_f32_16x16x32_bf16 v[32:35], a[32:35], a[16:19], v[32:35]\n" \
"ds_read_b64_tr_b16 a[60:61], v215 offset:25088\n" \
"v_max3_f32 v181, v181, v161, v162\n" \
"v_mfma_f32_16x16x32_bf16 v[32:35], a[36:39], a[20:23], v[32:35]\n" \
"ds_read_b64_tr_b16 a[62:63], v215 offset:27136\n" \
"v_max_f32 v183, v183, v179\n" \
"v_mfma_f32_16x16x32_bf16 v[36:39], a[40:43], a[16:19], v[36:39]\n" \
"global_load_lds_dwordx4 v218, s[38:39] offset:-2048\n" \
"v_add_u32 v213, s74, v211\n" \
"v_mfma_f32_16x16x32_bf16 v[36:39], a[44:47], a[20:23], v[36:39]\n" \
"s_waitcnt lgkmcnt(3)\n" \
"v_add_u32 v214, s74, v212\n" \
"v_xor_b32 v215, 32, v214\n" \
"v_mfma_f32_16x16x32_bf16 v[52:55], a[40:43], a[24:27], v[52:55]\n" \
"global_load_lds_dwordx4 v219, s[38:39] offset:-1024\n" \
"v_max_f32 v182, v182, v171\n" \
"v_mfma_f32_16x16x32_bf16 v[52:55], a[44:47], a[28:31], v[52:55]\n" \
"s_waitcnt vmcnt(17) lgkmcnt(0)\n" \
"s_add_u32 s38, s38, 0x20000\n" \
"s_addc_u32 s39, s39, 0\n" \
"v_mfma_f32_16x16x32_bf16 v[48:51], a[32:35], a[24:27], v[48:51]\n" \
"s_sub_u32 s75, s75, 32768\n" \
"s_cselect_b32 s75, 131072, s75\n" \
"v_max_f32 v181, v181, v163\n" \
"v_mfma_f32_16x16x32_bf16 v[48:51], a[36:39], a[28:31], v[48:51]\n" \
"s_barrier\n" \
"v_mov_b32 v187, v183\n" \
"v_mfma_f32_16x16x32_bf16 v[8:11], a[48:51], a[0:3], v[8:11]\n" \
"s_add_u32 m0, s75, s95\n" \
"ds_read_b64_tr_b16 a[32:33], v214 offset:16384\n" \
"v_mov_b32 v186, v182\n" \
"v_mfma_f32_16x16x32_bf16 v[8:11], a[52:55], a[4:7], v[8:11]\n" \
"ds_read_b64_tr_b16 a[34:35], v214 offset:18432\n" \
"global_load_lds_dwordx4 v222, s[36:37] offset:0 sc1\n" \
"v_mfma_f32_16x16x32_bf16 v[12:15], a[56:59], a[0:3], v[12:15]\n" \
"ds_read_b64_tr_b16 a[36:37], v214 offset:24576\n" \
"v_mov_b32 v185, v181\n" \
"v_mfma_f32_16x16x32_bf16 v[12:15], a[60:63], a[4:7], v[12:15]\n" \
"ds_read_b128 a[0:3], v213 offset:0\n" \
"v_permlane16_swap_b32 v187, v183\n" \
"v_permlane16_swap_b32 v186, v182\n" \
"v_mfma_f32_16x16x32_bf16 v[28:31], a[56:59], a[8:11], v[28:31]\n" \
"ds_read_b128 a[4:7], v213 offset:2048\n" \
"global_load_lds_dwordx4 v222, s[36:37] offset:1024 sc1\n" \
"v_mfma_f32_16x16x32_bf16 v[28:31], a[60:63], a[12:15], v[28:31]\n" \
"ds_read_b64_tr_b16 a[38:39], v214 offset:26624\n" \
"v_permlane16_swap_b32 v185, v181\n" \
"v_mfma_f32_16x16x32_bf16 v[24:27], a[48:51], a[8:11], v[24:27]\n" \
"ds_read_b64_tr_b16 a[40:41], v215 offset:16384\n" \
"v_max_f32 v183, v183, v187\n" \
"v_mfma_f32_16x16x32_bf16 v[24:27], a[52:55], a[12:15], v[24:27]\n" \
"ds_read_b128 a[8:11], v213 offset:256\n" \
"global_load_lds_dwordx4 v222, s[36:37] offset:2048 sc1\n" \
"v_mfma_f32_16x16x32_bf16 v[40:43], a[48:51], a[16:19], v[40:43]\n" \
"ds_read_b128 a[12:15], v213 offset:2304\n" \
"v_max_f32 v182, v182, v186\n" \
"v_mfma_f32_16x16x32_bf16 v[40:43], a[52:55], a[20:23], v[40:43]\n" \
"ds_read_b64_tr_b16 a[42:43], v215 offset:18432\n" \
"v_max_f32 v181, v181, v185\n" \
"v_mfma_f32_16x16x32_bf16 v[44:47], a[56:59], a[16:19], v[44:47]\n" \
"ds_read_b64_tr_b16 a[44:45], v215 offset:24576\n" \
"global_load_lds_dwordx4 v222, s[36:37] offset:3072 sc1\n" \
"v_mfma_f32_16x16x32_bf16 v[44:47], a[60:63], a[20:23], v[44:47]\n" \
"s_add_u32 s36, s36, 0x1000\n" \
"ds_read_b128 a[16:19], v213 offset:4096\n" \
"s_add_u32 m0, m0, 0x5000\n" \
"v_mfma_f32_16x16x32_bf16 v[60:63], a[56:59], a[24:27], v[60:63]\n" \
"ds_read_b128 a[20:23], v213 offset:6144\n" \
"global_load_lds_dwordx4 v216, s[38:39] offset:-4096\n" \
"v_mfma_f32_16x16x32_bf16 v[60:63], a[60:63], a[28:31], v[60:63]\n" \
"ds_read_b64_tr_b16 a[46:47], v215 offset:26624\n" \
"v_mov_b32 v187, v183\n" \
"v_mfma_f32_16x16x32_bf16 v[56:59], a[48:51], a[24:27], v[56:59]\n" \
"v_mov_b32 v186, v182\n" \
"v_mov_b32 v185, v181\n" \
"v_mfma_f32_16x16x32_bf16 v[56:59], a[52:55], a[28:31], v[56:59]\n" \
"s_waitcnt lgkmcnt(3)\n" \
"v_permlane32_swap_b32 v187, v183\n" \
"v_mfma_f32_16x16x32_bf16 v[0:3], a[32:35], a[0:3], v[0:3]\n" \
"ds_read_b128 a[24:27], v213 offset:4352\n" \
"v_mfma_f32_16x16x32_bf16 v[0:3], a[36:39], a[4:7], v[0:3]\n" \
"s_waitcnt lgkmcnt(1)\n" \
"ds_read_b128 a[28:31], v213 offset:6400\n" \
"global_load_lds_dwordx4 v217, s[38:39] offset:-3072\n" \
"v_mfma_f32_16x16x32_bf16 v[4:7], a[40:43], a[0:3], v[4:7]\n" \
"s_sub_u32 s74, s74, 32768\n" \
"s_cselect_b32 s74, 131072, s74\n" \
"ds_read_b64_tr_b16 a[48:49], v214 offset:16896\n" \
"v_mfma_f32_16x16x32_bf16 v[4:7], a[44:47], a[4:7], v[4:7]\n" \
"ds_read_b64_tr_b16 a[50:51], v214 offset:18944\n" \
"v_permlane32_swap_b32 v186, v182\n" \
"v_mfma_f32_16x16x32_bf16 v[20:23], a[40:43], a[8:11], v[20:23]\n" \
"ds_read_b64_tr_b16 a[52:53], v214 offset:25088\n" \
"global_load_lds_dwordx4 v218, s[38:39] offset:-2048\n" \
"v_mfma_f32_16x16x32_bf16 v[20:23], a[44:47], a[12:15], v[20:23]\n" \
"ds_read_b64_tr_b16 a[54:55], v214 offset:27136\n" \
"v_permlane32_swap_b32 v185, v181\n" \
"v_mfma_f32_16x16x32_bf16 v[16:19], a[32:35], a[8:11], v[16:19]\n" \
"ds_read_b64_tr_b16 a[56:57], v215 offset:16896\n" \
"global_load_lds_dwordx4 v219, s[38:39] offset:-1024\n" \
"v_mfma_f32_16x16x32_bf16 v[16:19], a[36:39], a[12:15], v[16:19]\n" \
"s_add_u32 s38, s38, 0x20000\n" \
"s_addc_u32 s39, s39, 0\n" \
"ds_read_b64_tr_b16 a[58:59], v215 offset:18944\n" \
"v_mfma_f32_16x16x32_bf16 v[32:35], a[32:35], a[16:19], v[32:35]\n" \
"s_sub_u32 s75, s75, 32768\n" \
"s_cselect_b32 s75, 131072, s75\n" \
"ds_read_b64_tr_b16 a[60:61], v215 offset:25088\n" \
"v_mfma_f32_16x16x32_bf16 v[32:35], a[36:39], a[20:23], v[32:35]\n" \
"s_add_u32 m0, s75, s95\n" \
"ds_read_b64_tr_b16 a[62:63], v215 offset:27136\n" \
"v_max_f32 v183, v183, v187\n" \
"v_mfma_f32_16x16x32_bf16 v[36:39], a[40:43], a[16:19], v[36:39]\n" \
"global_load_lds_dwordx4 v222, s[36:37] offset:0 sc1\n" \
"v_add_u32 v213, s74, v211\n" \
"v_mfma_f32_16x16x32_bf16 v[36:39], a[44:47], a[20:23], v[36:39]\n" \
"s_waitcnt lgkmcnt(3)\n" \
"v_add_u32 v214, s74, v212\n" \
"v_xor_b32 v215, 32, v214\n" \
"v_mfma_f32_16x16x32_bf16 v[52:55], a[40:43], a[24:27], v[52:55]\n" \
"v_max_f32 v182, v182, v186\n" \
"v_max_f32 v181, v181, v185\n" \
"v_mfma_f32_16x16x32_bf16 v[52:55], a[44:47], a[28:31], v[52:55]\n" \
"s_waitcnt vmcnt(17) lgkmcnt(0)\n" \
"global_load_lds_dwordx4 v222, s[36:37] offset:1024 sc1\n" \
"v_mul_f32 v183, v183, v144\n" \
"v_mfma_f32_16x16x32_bf16 v[48:51], a[32:35], a[24:27], v[48:51]\n" \
"v_mul_f32 v182, v182, v144\n" \
"v_mul_f32 v181, v181, v144\n" \
"v_mfma_f32_16x16x32_bf16 v[48:51], a[36:39], a[28:31], v[48:51]\n" \
"s_barrier\n" \
"v_fma_f32 v172, v172, v144, -v183\n" \
"v_mfma_f32_16x16x32_bf16 v[8:11], a[48:51], a[0:3], v[8:11]\n" \
"ds_read_b64_tr_b16 a[32:33], v214 offset:16384\n" \
"v_fma_f32 v164, v164, v144, -v182\n" \
"v_mfma_f32_16x16x32_bf16 v[8:11], a[52:55], a[4:7], v[8:11]\n" \
"ds_read_b64_tr_b16 a[34:35], v214 offset:18432\n" \
"global_load_lds_dwordx4 v222, s[36:37] offset:2048 sc1\n" \
"v_mfma_f32_16x16x32_bf16 v[12:15], a[56:59], a[0:3], v[12:15]\n" \
"ds_read_b64_tr_b16 a[36:37], v214 offset:24576\n" \
"v_fma_f32 v156, v156, v144, -v181\n" \
"v_mfma_f32_16x16x32_bf16 v[12:15], a[60:63], a[4:7], v[12:15]\n" \
"ds_read_b128 a[0:3], v213 offset:0\n" \
"v_exp_f32 v172, v172\n" \
"v_fma_f32 v173, v173, v144, -v183\n" \
"v_mfma_f32_16x16x32_bf16 v[28:31], a[56:59], a[8:11], v[28:31]\n" \
"ds_read_b128 a[4:7], v213 offset:2048\n" \
"global_load_lds_dwordx4 v222, s[36:37] offset:3072 sc1\n" \
"v_mfma_f32_16x16x32_bf16 v[28:31], a[60:63], a[12:15], v[28:31]\n" \
"s_add_u32 s36, s36, 0x1000\n" \
"ds_read_b64_tr_b16 a[38:39], v214 offset:26624\n" \
"v_exp_f32 v164, v164\n" \
"v_mfma_f32_16x16x32_bf16 v[24:27], a[48:51], a[8:11], v[24:27]\n" \
"s_add_u32 m0, m0, 0x5000\n" \
"ds_read_b64_tr_b16 a[40:41], v215 offset:16384\n" \
"v_fma_f32 v165, v165, v144, -v182\n" \
"v_mfma_f32_16x16x32_bf16 v[24:27], a[52:55], a[12:15], v[24:27]\n" \
"ds_read_b128 a[8:11], v213 offset:256\n" \
"global_load_lds_dwordx4 v216, s[38:39] offset:-4096\n" \
"v_exp_f32 v156, v156\n" \
"v_mfma_f32_16x16x32_bf16 v[40:43], a[48:51], a[16:19], v[40:43]\n" \
"ds_read_b128 a[12:15], v213 offset:2304\n" \
"v_fma_f32 v157, v157, v144, -v181\n" \
"v_mfma_f32_16x16x32_bf16 v[40:43], a[52:55], a[20:23], v[40:43]\n" \
"ds_read_b64_tr_b16 a[42:43], v215 offset:18432\n" \
"v_exp_f32 v173, v173\n" \
"v_fma_f32 v174, v174, v144, -v183\n" \
"v_mfma_f32_16x16x32_bf16 v[44:47], a[56:59], a[16:19], v[44:47]\n" \
"ds_read_b64_tr_b16 a[44:45], v215 offset:24576\n" \
"global_load_lds_dwordx4 v217, s[38:39] offset:-3072\n" \
"v_mfma_f32_16x16x32_bf16 v[44:47], a[60:63], a[20:23], v[44:47]\n" \
"buffer_load_dwordx4 v[128:131], v207, s[56:59], s47 offen offset:0 sc1\n" \
"ds_read_b128 a[16:19], v213 offset:4096\n" \
"v_mfma_f32_16x16x32_bf16 v[60:63], a[56:59], a[24:27], v[60:63]\n" \
"ds_read_b128 a[20:23], v213 offset:6144\n" \
"global_load_lds_dwordx4 v218, s[38:39] offset:-2048\n" \
"v_mfma_f32_16x16x32_bf16 v[60:63], a[60:63], a[28:31], v[60:63]\n" \
"ds_read_b64_tr_b16 a[46:47], v215 offset:26624\n" \
"v_exp_f32 v165, v165\n" \
"v_mfma_f32_16x16x32_bf16 v[56:59], a[48:51], a[24:27], v[56:59]\n" \
"buffer_load_dwordx4 v[192:195], v208, s[56:59], s47 offen offset:1024 sc1\n" \
"v_mfma_f32_16x16x32_bf16 v[56:59], a[52:55], a[28:31], v[56:59]\n" \
"s_waitcnt lgkmcnt(3)\n" \
"buffer_load_dwordx4 v[68:71], v223, s[56:59], s47 offen offset:2048 sc1\n" \
"v_mfma_f32_16x16x32_bf16 v[0:3], a[32:35], a[0:3], v[0:3]\n" \
"ds_read_b128 a[24:27], v213 offset:4352\n" \
"v_fma_f32 v166, v166, v144, -v182\n" \
"v_mfma_f32_16x16x32_bf16 v[0:3], a[36:39], a[4:7], v[0:3]\n" \
"s_waitcnt lgkmcnt(1)\n" \
"ds_read_b128 a[28:31], v213 offset:6400\n" \
"global_load_lds_dwordx4 v219, s[38:39] offset:-1024\n" \
"v_exp_f32 v157, v157\n" \
"v_mfma_f32_16x16x32_bf16 v[4:7], a[40:43], a[0:3], v[4:7]\n" \
"s_add_u32 s38, s38, 0x20000\n" \
"s_addc_u32 s39, s39, 0\n" \
"ds_read_b64_tr_b16 a[48:49], v214 offset:16896\n" \
"v_mfma_f32_16x16x32_bf16 v[4:7], a[44:47], a[4:7], v[4:7]\n" \
"s_sub_u32 s75, s75, 32768\n" \
"s_cselect_b32 s75, 131072, s75\n" \
"ds_read_b64_tr_b16 a[50:51], v214 offset:18944\n" \
"v_mfma_f32_16x16x32_bf16 v[20:23], a[40:43], a[8:11], v[20:23]\n" \
"s_add_u32 m0, s75, s95\n" \
"ds_read_b64_tr_b16 a[52:53], v214 offset:25088\n" \
"v_fma_f32 v158, v158, v144, -v181\n" \
"v_mfma_f32_16x16x32_bf16 v[20:23], a[44:47], a[12:15], v[20:23]\n" \
"ds_read_b64_tr_b16 a[54:55], v214 offset:27136\n" \
"global_load_lds_dwordx4 v222, s[36:37] offset:0 sc1\n" \
"v_exp_f32 v174, v174\n" \
"v_mfma_f32_16x16x32_bf16 v[16:19], a[32:35], a[8:11], v[16:19]\n" \
"s_sub_u32 s74, s74, 32768\n" \
"s_cselect_b32 s74, 131072, s74\n" \
"ds_read_b64_tr_b16 a[56:57], v215 offset:16896\n" \
"v_mfma_f32_16x16x32_bf16 v[16:19], a[36:39], a[12:15], v[16:19]\n" \
"ds_read_b64_tr_b16 a[58:59], v215 offset:18944\n" \
"global_load_lds_dwordx4 v222, s[36:37] offset:1024 sc1\n" \
"v_mfma_f32_16x16x32_bf16 v[32:35], a[32:35], a[16:19], v[32:35]\n" \
"ds_read_b64_tr_b16 a[60:61], v215 offset:25088\n" \
"v_fma_f32 v175, v175, v144, -v183\n" \
"v_mfma_f32_16x16x32_bf16 v[32:35], a[36:39], a[20:23], v[32:35]\n" \
"ds_read_b64_tr_b16 a[62:63], v215 offset:27136\n" \
"v_exp_f32 v166, v166\n" \
"v_fma_f32 v167, v167, v144, -v182\n" \
"v_mfma_f32_16x16x32_bf16 v[36:39], a[40:43], a[16:19], v[36:39]\n" \
"global_load_lds_dwordx4 v222, s[36:37] offset:2048 sc1\n" \
"v_add_u32 v213, s74, v211\n" \
"v_mfma_f32_16x16x32_bf16 v[36:39], a[44:47], a[20:23], v[36:39]\n" \
"s_waitcnt lgkmcnt(3)\n" \
"v_add_u32 v214, s74, v212\n" \
"v_xor_b32 v215, 32, v214\n" \
"v_mfma_f32_16x16x32_bf16 v[52:55], a[40:43], a[24:27], v[52:55]\n" \
"v_exp_f32 v158, v158\n" \
"v_mfma_f32_16x16x32_bf16 v[52:55], a[44:47], a[28:31], v[52:55]\n" \
"s_waitcnt vmcnt(22) lgkmcnt(0)\n" \
"global_load_lds_dwordx4 v222, s[36:37] offset:3072 sc1\n" \
"v_fma_f32 v159, v159, v144, -v181\n" \
"v_mfma_f32_16x16x32_bf16 v[48:51], a[32:35], a[24:27], v[48:51]\n" \
"s_add_u32 s36, s36, 0x1000\n" \
"v_exp_f32 v175, v175\n" \
"v_mfma_f32_16x16x32_bf16 v[48:51], a[36:39], a[28:31], v[48:51]\n" \
"s_barrier\n" \
"v_fma_f32 v176, v176, v144, -v183\n" \
"v_exp_f32 v167, v167\n" \
"v_mfma_f32_16x16x32_bf16 v[8:11], a[48:51], a[0:3], v[8:11]\n" \
"s_add_u32 m0, m0, 0x5000\n" \
"ds_read_b64_tr_b16 a[32:33], v214 offset:16384\n" \
"v_fma_f32 v168, v168, v144, -v182\n" \
"v_mfma_f32_16x16x32_bf16 v[8:11], a[52:55], a[4:7], v[8:11]\n" \
"ds_read_b64_tr_b16 a[34:35], v214 offset:18432\n" \
"global_load_lds_dwordx4 v216, s[38:39] offset:-4096\n" \
"v_exp_f32 v159, v159\n" \
"v_mfma_f32_16x16x32_bf16 v[12:15], a[56:59], a[0:3], v[12:15]\n" \
"ds_read_b64_tr_b16 a[36:37], v214 offset:24576\n" \
"v_fma_f32 v160, v160, v144, -v181\n" \
"v_mfma_f32_16x16x32_bf16 v[12:15], a[60:63], a[4:7], v[12:15]\n" \
"ds_read_b128 a[0:3], v213 offset:0\n" \
"v_exp_f32 v176, v176\n" \
"v_mfma_f32_16x16x32_bf16 v[28:31], a[56:59], a[8:11], v[28:31]\n" \
"ds_read_b128 a[4:7], v213 offset:2048\n" \
"global_load_lds_dwordx4 v217, s[38:39] offset:-3072\n" \
"v_mfma_f32_16x16x32_bf16 v[28:31], a[60:63], a[12:15], v[28:31]\n" \
"ds_read_b64_tr_b16 a[38:39], v214 offset:26624\n" \
"v_fma_f32 v177, v177, v144, -v183\n" \
"v_mfma_f32_16x16x32_bf16 v[24:27], a[48:51], a[8:11], v[24:27]\n" \
"ds_read_b64_tr_b16 a[40:41], v215 offset:16384\n" \
"v_exp_f32 v168, v168\n" \
"v_mfma_f32_16x16x32_bf16 v[24:27], a[52:55], a[12:15], v[24:27]\n" \
"ds_read_b128 a[8:11], v213 offset:256\n" \
"global_load_lds_dwordx4 v218, s[38:39] offset:-2048\n" \
"v_mfma_f32_16x16x32_bf16 v[40:43], a[48:51], a[16:19], v[40:43]\n" \
"ds_read_b128 a[12:15], v213 offset:2304\n" \
"v_fma_f32 v169, v169, v144, -v182\n" \
"v_mfma_f32_16x16x32_bf16 v[40:43], a[52:55], a[20:23], v[40:43]\n" \
"ds_read_b64_tr_b16 a[42:43], v215 offset:18432\n" \
"v_exp_f32 v160, v160\n" \
"v_mfma_f32_16x16x32_bf16 v[44:47], a[56:59], a[16:19], v[44:47]\n" \
"ds_read_b64_tr_b16 a[44:45], v215 offset:24576\n" \
"global_load_lds_dwordx4 v219, s[38:39] offset:-1024\n" \
"v_mfma_f32_16x16x32_bf16 v[44:47], a[60:63], a[20:23], v[44:47]\n" \
"s_add_u32 s38, s38, 0x20000\n" \
"s_addc_u32 s39, s39, 0\n" \
"ds_read_b128 a[16:19], v213 offset:4096\n" \
"v_mfma_f32_16x16x32_bf16 v[60:63], a[56:59], a[24:27], v[60:63]\n" \
"s_sub_u32 s75, s75, 32768\n" \
"s_cselect_b32 s75, 131072, s75\n" \
"ds_read_b128 a[20:23], v213 offset:6144\n" \
"v_mfma_f32_16x16x32_bf16 v[60:63], a[60:63], a[28:31], v[60:63]\n" \
"ds_read_b64_tr_b16 a[46:47], v215 offset:26624\n" \
"v_fma_f32 v161, v161, v144, -v181\n" \
"v_mfma_f32_16x16x32_bf16 v[56:59], a[48:51], a[24:27], v[56:59]\n" \
"v_exp_f32 v177, v177\n" \
"v_mfma_f32_16x16x32_bf16 v[56:59], a[52:55], a[28:31], v[56:59]\n" \
"s_waitcnt lgkmcnt(3)\n" \
"v_fma_f32 v178, v178, v144, -v183\n" \
"v_exp_f32 v169, v169\n" \
"v_mfma_f32_16x16x32_bf16 v[0:3], a[32:35], a[0:3], v[0:3]\n" \
"s_add_u32 m0, s75, s95\n" \
"ds_read_b128 a[24:27], v213 offset:4352\n" \
"v_fma_f32 v170, v170, v144, -v182\n" \
"v_mfma_f32_16x16x32_bf16 v[0:3], a[36:39], a[4:7], v[0:3]\n" \
"s_waitcnt lgkmcnt(1)\n" \
"ds_read_b128 a[28:31], v213 offset:6400\n" \
"global_load_lds_dwordx4 v222, s[36:37] offset:0 sc1\n" \
"v_mfma_f32_16x16x32_bf16 v[4:7], a[40:43], a[0:3], v[4:7]\n" \
"s_sub_u32 s74, s74, 32768\n" \
"s_cselect_b32 s74, 131072, s74\n" \
"ds_read_b64_tr_b16 a[48:49], v214 offset:16896\n" \
"v_mfma_f32_16x16x32_bf16 v[4:7], a[44:47], a[4:7], v[4:7]\n" \
"ds_read_b64_tr_b16 a[50:51], v214 offset:18944\n" \
"v_exp_f32 v161, v161\n" \
"v_mfma_f32_16x16x32_bf16 v[20:23], a[40:43], a[8:11], v[20:23]\n" \
"ds_read_b64_tr_b16 a[52:53], v214 offset:25088\n" \
"v_fma_f32 v162, v162, v144, -v181\n" \
"v_mfma_f32_16x16x32_bf16 v[20:23], a[44:47], a[12:15], v[20:23]\n" \
"ds_read_b64_tr_b16 a[54:55], v214 offset:27136\n" \
"global_load_lds_dwordx4 v222, s[36:37] offset:1024 sc1\n" \
"v_exp_f32 v178, v178\n" \
"v_mfma_f32_16x16x32_bf16 v[16:19], a[32:35], a[8:11], v[16:19]\n" \
"ds_read_b64_tr_b16 a[56:57], v215 offset:16896\n" \
"v_fma_f32 v179, v179, v144, -v183\n" \
"v_mfma_f32_16x16x32_bf16 v[16:19], a[36:39], a[12:15], v[16:19]\n" \
"ds_read_b64_tr_b16 a[58:59], v215 offset:18944\n" \
"v_exp_f32 v170, v170\n" \
"v_fma_f32 v171, v171, v144, -v182\n" \
"v_mfma_f32_16x16x32_bf16 v[32:35], a[32:35], a[16:19], v[32:35]\n" \
"s_waitcnt vmcnt(11)\n" \
"ds_read_b64_tr_b16 a[60:61], v215 offset:25088\n" \
"v_bitop3_b32 v131, v131, v128, v129 bitop3:0x96\n" \
"v_mfma_f32_16x16x32_bf16 v[32:35], a[36:39], a[20:23], v[32:35]\n" \
"ds_read_b64_tr_b16 a[62:63], v215 offset:27136\n" \
"global_load_lds_dwordx4 v222, s[36:37] offset:2048 sc1\n" \
"v_mfma_f32_16x16x32_bf16 v[36:39], a[40:43], a[16:19], v[36:39]\n" \
"v_add_u32 v213, s74, v211\n" \
"v_add_u32 v214, s74, v212\n" \
"v_mfma_f32_16x16x32_bf16 v[36:39], a[44:47], a[20:23], v[36:39]\n" \
"s_waitcnt lgkmcnt(3)\n" \
"v_xor_b32 v215, 32, v214\n" \
"v_bitop3_b32 v192, v192, v193, v194 bitop3:0x96\n" \
"v_exp_f32 v162, v162\n" \
"v_fma_f32 v163, v163, v144, -v181\n" \
"v_mfma_f32_16x16x32_bf16 v[52:55], a[40:43], a[24:27], v[52:55]\n" \
"v_bitop3_b32 v68, v68, v69, v70 bitop3:0x96\n" \
"v_xor_b32 v131, v131, v130\n" \
"v_mfma_f32_16x16x32_bf16 v[52:55], a[44:47], a[28:31], v[52:55]\n" \
"s_waitcnt vmcnt(22) lgkmcnt(0)\n" \
"global_load_lds_dwordx4 v222, s[36:37] offset:3072 sc1\n" \
"v_xor_b32 v192, v192, v195\n" \
"v_exp_f32 v179, v179\n" \
"v_mfma_f32_16x16x32_bf16 v[48:51], a[32:35], a[24:27], v[48:51]\n" \
"s_add_u32 s36, s36, 0x1000\n" \
"v_xor_b32 v68, v68, v71\n" \
"v_cmp_ne_u32 s[42:43], s60, v131\n" \
"v_mfma_f32_16x16x32_bf16 v[48:51], a[36:39], a[28:31], v[48:51]\n" \
"s_barrier\n" \
"v_exp_f32 v171, v171\n" \
"v_mfma_f32_16x16x32_bf16 v[8:11], a[48:51], a[0:3], v[8:11]\n" \
"s_add_u32 m0, m0, 0x5000\n" \
"ds_read_b64_tr_b16 a[32:33], v214 offset:16384\n" \
"v_cmp_ne_u32 s[44:45], s60, v192\n" \
"v_mfma_f32_16x16x32_bf16 v[8:11], a[52:55], a[4:7], v[8:11]\n" \
"ds_read_b64_tr_b16 a[34:35], v214 offset:18432\n" \
"global_load_lds_dwordx4 v216, s[38:39] offset:-4096\n" \
"v_exp_f32 v163, v163\n" \
"v_mfma_f32_16x16x32_bf16 v[12:15], a[56:59], a[0:3], v[12:15]\n" \
"ds_read_b64_tr_b16 a[36:37], v214 offset:24576\n" \
"v_cmp_ne_u32 vcc, s60, v68\n" \
"v_mfma_f32_16x16x32_bf16 v[12:15], a[60:63], a[4:7], v[12:15]\n" \
"ds_read_b128 a[0:3], v213 offset:0\n" \
"v_cvt_pk_bf16_f32 v172, v172, v173\n" \
"v_mfma_f32_16x16x32_bf16 v[28:31], a[56:59], a[8:11], v[28:31]\n" \
"s_and_b64 s[42:43], s[42:43], s[12:13]\n" \
"ds_read_b128 a[4:7], v213 offset:2048\n" \
"v_cvt_pk_bf16_f32 v173, v174, v175\n" \
"v_mfma_f32_16x16x32_bf16 v[28:31], a[60:63], a[12:15], v[28:31]\n" \
"s_and_b64 s[44:45], s[44:45], s[92:93]\n" \
"ds_read_b64_tr_b16 a[38:39], v214 offset:26624\n" \
"global_load_lds_dwordx4 v217, s[38:39] offset:-3072\n" \
"v_cvt_pk_bf16_f32 v174, v176, v177\n" \
"v_mfma_f32_16x16x32_bf16 v[24:27], a[48:51], a[8:11], v[24:27]\n" \
"s_and_b64 vcc, vcc, s[14:15]\n" \
"s_or_b64 s[42:43], s[42:43], s[44:45]\n" \
"ds_read_b64_tr_b16 a[40:41], v215 offset:16384\n" \
"v_mfma_f32_16x16x32_bf16 v[24:27], a[52:55], a[12:15], v[24:27]\n" \
"s_or_b64 vcc, vcc, s[42:43]\n" \
"s_cbranch_scc1 Lplh_%=\n" \
"Lprh_%=:\n" \
"ds_read_b128 a[8:11], v213 offset:256\n" \
"v_cvt_pk_bf16_f32 v175, v178, v179\n" \
"v_cvt_pk_bf16_f32 v164, v164, v165\n" \
"v_mfma_f32_16x16x32_bf16 v[40:43], a[48:51], a[16:19], v[40:43]\n" \
"ds_read_b128 a[12:15], v213 offset:2304\n" \
"v_cvt_pk_bf16_f32 v165, v166, v167\n" \
"v_mfma_f32_16x16x32_bf16 v[40:43], a[52:55], a[20:23], v[40:43]\n" \
"ds_read_b64_tr_b16 a[42:43], v215 offset:18432\n" \
"global_load_lds_dwordx4 v218, s[38:39] offset:-2048\n" \
"v_mfma_f32_16x16x32_bf16 v[44:47], a[56:59], a[16:19], v[44:47]\n" \
"ds_read_b64_tr_b16 a[44:45], v215 offset:24576\n" \
"v_mov_b32 v131, v193\n" \
"v_mfma_f32_16x16x32_bf16 v[44:47], a[60:63], a[20:23], v[44:47]\n" \
"v_cvt_pk_bf16_f32 v166, v168, v169\n" \
"v_cvt_pk_bf16_f32 v167, v170, v171\n" \
"v_mfma_f32_16x16x32_bf16 v[148:151], v[128:131], v[112:115], v[148:151]\n" \
"ds_read_b128 a[16:19], v213 offset:4096\n" \
"v_cvt_pk_bf16_f32 v156, v156, v157\n" \
"v_mfma_f32_16x16x32_bf16 v[60:63], a[56:59], a[24:27], v[60:63]\n" \
"ds_read_b128 a[20:23], v213 offset:6144\n" \
"global_load_lds_dwordx4 v219, s[38:39] offset:-1024\n" \
"v_mfma_f32_16x16x32_bf16 v[60:63], a[60:63], a[28:31], v[60:63]\n" \
"s_add_u32 s38, s38, 0x20000\n" \
"s_addc_u32 s39, s39, 0\n" \
"ds_read_b64_tr_b16 a[46:47], v215 offset:26624\n" \
"v_mfma_f32_16x16x32_bf16 v[56:59], a[48:51], a[24:27], v[56:59]\n" \
"s_sub_u32 s75, s75, 32768\n" \
"s_cselect_b32 s75, 131072, s75\n" \
"v_cvt_pk_bf16_f32 v157, v158, v159\n" \
"v_mfma_f32_16x16x32_bf16 v[56:59], a[52:55], a[28:31], v[56:59]\n" \
"s_waitcnt lgkmcnt(3)\n" \
"v_max3_f32 v180, v148, v149, v150\n" \
"v_max3_f32 v180, v180, v151, v152\n" \
"v_mfma_f32_16x16x32_bf16 v[0:3], a[32:35], a[0:3], v[0:3]\n" \
"s_add_u32 m0, s75, s95\n" \
"ds_read_b128 a[24:27], v213 offset:4352\n" \
"v_max3_f32 v180, v180, v153, v154\n" \
"v_mfma_f32_16x16x32_bf16 v[0:3], a[36:39], a[4:7], v[0:3]\n" \
"s_waitcnt lgkmcnt(1)\n" \
"ds_read_b128 a[28:31], v213 offset:6400\n" \
"global_load_lds_dwordx4 v222, s[36:37] offset:0 sc1\n" \
"v_mfma_f32_16x16x32_bf16 v[4:7], a[40:43], a[0:3], v[4:7]\n" \
"s_sub_u32 s74, s74, 32768\n" \
"s_cselect_b32 s74, 131072, s74\n" \
"ds_read_b64_tr_b16 a[48:49], v214 offset:16896\n" \
"v_mfma_f32_16x16x32_bf16 v[4:7], a[44:47], a[4:7], v[4:7]\n" \
"ds_read_b64_tr_b16 a[50:51], v214 offset:18944\n" \
"v_max_f32 v180, v180, v155\n" \
"v_mfma_f32_16x16x32_bf16 v[20:23], a[40:43], a[8:11], v[20:23]\n" \
"ds_read_b64_tr_b16 a[52:53], v214 offset:25088\n" \
"v_mov_b32 v184, v180\n" \
"v_mfma_f32_16x16x32_bf16 v[20:23], a[44:47], a[12:15], v[20:23]\n" \
"ds_read_b64_tr_b16 a[54:55], v214 offset:27136\n" \
"global_load_lds_dwordx4 v222, s[36:37] offset:1024 sc1\n" \
"v_mfma_f32_16x16x32_bf16 v[16:19], a[32:35], a[8:11], v[16:19]\n" \
"ds_read_b64_tr_b16 a[56:57], v215 offset:16896\n" \
"v_permlane16_swap_b32 v184, v180\n" \
"v_mfma_f32_16x16x32_bf16 v[16:19], a[36:39], a[12:15], v[16:19]\n" \
"ds_read_b64_tr_b16 a[58:59], v215 offset:18944\n" \
"v_max_f32 v180, v180, v184\n" \
"v_mfma_f32_16x16x32_bf16 v[32:35], a[32:35], a[16:19], v[32:35]\n" \
"ds_read_b64_tr_b16 a[60:61], v215 offset:25088\n" \
"v_mov_b32 v184, v180\n" \
"v_mfma_f32_16x16x32_bf16 v[32:35], a[36:39], a[20:23], v[32:35]\n" \
"ds_read_b64_tr_b16 a[62:63], v215 offset:27136\n" \
"global_load_lds_dwordx4 v222, s[36:37] offset:2048 sc1\n" \
"v_mfma_f32_16x16x32_bf16 v[36:39], a[40:43], a[16:19], v[36:39]\n" \
"v_add_u32 v213, s74, v211\n" \
"v_add_u32 v214, s74, v212\n" \
"v_mfma_f32_16x16x32_bf16 v[36:39], a[44:47], a[20:23], v[36:39]\n" \
"s_waitcnt lgkmcnt(3)\n" \
"v_xor_b32 v215, 32, v214\n" \
"v_cvt_pk_bf16_f32 v158, v160, v161\n" \
"v_mfma_f32_16x16x32_bf16 v[52:55], a[40:43], a[24:27], v[52:55]\n" \
"v_permlane32_swap_b32 v184, v180\n" \
"v_mfma_f32_16x16x32_bf16 v[52:55], a[44:47], a[28:31], v[52:55]\n" \
"s_waitcnt vmcnt(19) lgkmcnt(0)\n" \
"global_load_lds_dwordx4 v222, s[36:37] offset:3072 sc1\n" \
"v_max_f32 v180, v180, v184\n" \
"v_mfma_f32_16x16x32_bf16 v[48:51], a[32:35], a[24:27], v[48:51]\n" \
"s_add_u32 s36, s36, 0x1000\n" \
"v_mul_f32 v180, v180, v144\n" \
"v_fma_f32 v148, v148, v144, -v180\n" \
"v_mfma_f32_16x16x32_bf16 v[48:51], a[36:39], a[28:31], v[48:51]\n" \
"s_barrier\n" \
"v_cvt_pk_bf16_f32 v159, v162, v163\n" \
"v_mfma_f32_16x16x32_bf16 v[8:11], a[48:51], a[0:3], v[8:11]\n" \
"s_add_u32 m0, m0, 0x5000\n" \
"ds_read_b64_tr_b16 a[32:33], v214 offset:16384\n" \
"v_fma_f32 v149, v149, v144, -v180\n" \
"v_mfma_f32_16x16x32_bf16 v[8:11], a[52:55], a[4:7], v[8:11]\n" \
"ds_read_b64_tr_b16 a[34:35], v214 offset:18432\n" \
"global_load_lds_dwordx4 v216, s[38:39] offset:-4096\n" \
"v_mfma_f32_16x16x32_bf16 v[176:179], v[188:191], v[172:175], 0\n" \
"Lpg11443_%=:\n" \
"s_cbranch_execz Lpg12573_%=\n" \
"v_mfma_f32_16x16x32_bf16 v[104:107], v[200:203], v[172:175], 0\n" \
"v_mfma_f32_16x16x32_bf16 v[108:111], v[76:79], v[172:175], 0\n" \
"v_mfma_f32_16x16x32_bf16 v[168:171], v[188:191], v[164:167], 0\n" \
"v_mfma_f32_16x16x32_bf16 v[96:99], v[198:201], v[164:167], 0\n" \
"v_mfma_f32_16x16x32_bf16 v[100:103], v[74:77], v[164:167], 0\n" \
"v_mfma_f32_16x16x32_bf16 v[12:15], a[56:59], a[0:3], v[12:15]\n" \
"ds_read_b64_tr_b16 a[36:37], v214 offset:24576\n" \
"v_fma_f32 v150, v150, v144, -v180\n" \
"v_mfma_f32_16x16x32_bf16 v[12:15], a[60:63], a[4:7], v[12:15]\n" \
"v_exp_f32 v148, v148\n" \
"v_mfma_f32_16x16x32_bf16 v[160:163], v[188:191], v[156:159], 0\n" \
"ds_read_b128 a[0:3], v213 offset:0\n" \
"v_mfma_f32_16x16x32_bf16 v[88:91], v[196:199], v[156:159], 0\n" \
"v_mfma_f32_16x16x32_bf16 v[92:95], v[72:75], v[156:159], 0\n" \
"v_mfma_f32_16x16x32_bf16 v[28:31], a[56:59], a[8:11], v[28:31]\n" \
"ds_read_b128 a[4:7], v213 offset:2048\n" \
"v_fma_f32 v151, v151, v144, -v180\n" \
"v_mfma_f32_16x16x32_bf16 v[28:31], a[60:63], a[12:15], v[28:31]\n" \
"ds_read_b64_tr_b16 a[38:39], v214 offset:26624\n" \
"global_load_lds_dwordx4 v217, s[38:39] offset:-3072\n" \
"v_exp_f32 v149, v149\n" \
"v_rcp_f32 v187, v176\n" \
"v_mfma_f32_16x16x32_bf16 v[24:27], a[48:51], a[8:11], v[24:27]\n" \
"ds_read_b64_tr_b16 a[40:41], v215 offset:16384\n" \
"v_fma_f32 v152, v152, v144, -v180\n" \
"v_mfma_f32_16x16x32_bf16 v[24:27], a[52:55], a[12:15], v[24:27]\n" \
"ds_read_b128 a[8:11], v213 offset:256\n" \
"v_exp_f32 v150, v150\n" \
"v_mfma_f32_16x16x32_bf16 v[40:43], a[48:51], a[16:19], v[40:43]\n" \
"ds_read_b128 a[12:15], v213 offset:2304\n" \
"v_fma_f32 v153, v153, v144, -v180\n" \
"v_mfma_f32_16x16x32_bf16 v[40:43], a[52:55], a[20:23], v[40:43]\n" \
"ds_read_b64_tr_b16 a[42:43], v215 offset:18432\n" \
"global_load_lds_dwordx4 v218, s[38:39] offset:-2048\n" \
"v_exp_f32 v151, v151\n" \
"v_mfma_f32_16x16x32_bf16 v[44:47], a[56:59], a[16:19], v[44:47]\n" \
"ds_read_b64_tr_b16 a[44:45], v215 offset:24576\n" \
"v_fma_f32 v154, v154, v144, -v180\n" \
"v_mfma_f32_16x16x32_bf16 v[44:47], a[60:63], a[20:23], v[44:47]\n" \
"ds_read_b128 a[16:19], v213 offset:4096\n" \
"v_exp_f32 v152, v152\n" \
"v_mfma_f32_16x16x32_bf16 v[60:63], a[56:59], a[24:27], v[60:63]\n" \
"ds_read_b128 a[20:23], v213 offset:6144\n" \
"global_load_lds_dwordx4 v219, s[38:39] offset:-1024\n" \
"v_mfma_f32_16x16x32_bf16 v[60:63], a[60:63], a[28:31], v[60:63]\n" \
"s_add_u32 s38, s38, 0x20000\n" \
"s_addc_u32 s39, s39, 0\n" \
"ds_read_b64_tr_b16 a[46:47], v215 offset:26624\n" \
"v_mfma_f32_16x16x32_bf16 v[56:59], a[48:51], a[24:27], v[56:59]\n" \
"s_sub_u32 s75, s75, 32768\n" \
"s_cselect_b32 s75, 131072, s75\n" \
"v_fma_f32 v155, v155, v144, -v180\n" \
"v_mfma_f32_16x16x32_bf16 v[56:59], a[52:55], a[28:31], v[56:59]\n" \
"s_waitcnt lgkmcnt(3)\n" \
"v_exp_f32 v153, v153\n" \
"v_mfma_f32_16x16x32_bf16 v[0:3], a[32:35], a[0:3], v[0:3]\n" \
"s_add_u32 m0, s75, s95\n" \
"ds_read_b128 a[24:27], v213 offset:4352\n" \
"v_exp_f32 v154, v154\n" \
"v_mfma_f32_16x16x32_bf16 v[0:3], a[36:39], a[4:7], v[0:3]\n" \
"s_waitcnt lgkmcnt(1)\n" \
"ds_read_b128 a[28:31], v213 offset:6400\n" \
"global_load_lds_dwordx4 v222, s[36:37] offset:0 sc1\n" \
"v_mfma_f32_16x16x32_bf16 v[4:7], a[40:43], a[0:3], v[4:7]\n" \
"s_sub_u32 s74, s74, 32768\n" \
"s_cselect_b32 s74, 131072, s74\n" \
"ds_read_b64_tr_b16 a[48:49], v214 offset:16896\n" \
"v_mfma_f32_16x16x32_bf16 v[4:7], a[44:47], a[4:7], v[4:7]\n" \
"ds_read_b64_tr_b16 a[50:51], v214 offset:18944\n" \
"v_exp_f32 v155, v155\n" \
"v_mfma_f32_16x16x32_bf16 v[20:23], a[40:43], a[8:11], v[20:23]\n" \
"ds_read_b64_tr_b16 a[52:53], v214 offset:25088\n" \
"v_cvt_pk_bf16_f32 v148, v148, v149\n" \
"v_mfma_f32_16x16x32_bf16 v[20:23], a[44:47], a[12:15], v[20:23]\n" \
"ds_read_b64_tr_b16 a[54:55], v214 offset:27136\n" \
"global_load_lds_dwordx4 v222, s[36:37] offset:1024 sc1\n" \
"v_mfma_f32_16x16x32_bf16 v[16:19], a[32:35], a[8:11], v[16:19]\n" \
"ds_read_b64_tr_b16 a[56:57], v215 offset:16896\n" \
"v_cvt_pk_bf16_f32 v149, v150, v151\n" \
"v_mfma_f32_16x16x32_bf16 v[16:19], a[36:39], a[12:15], v[16:19]\n" \
"ds_read_b64_tr_b16 a[58:59], v215 offset:18944\n" \
"v_cvt_pk_bf16_f32 v150, v152, v153\n" \
"v_mfma_f32_16x16x32_bf16 v[32:35], a[32:35], a[16:19], v[32:35]\n" \
"ds_read_b64_tr_b16 a[60:61], v215 offset:25088\n" \
"v_cvt_pk_bf16_f32 v151, v154, v155\n" \
"v_mfma_f32_16x16x32_bf16 v[32:35], a[36:39], a[20:23], v[32:35]\n" \
"v_rcp_f32 v186, v168\n" \
"v_mfma_f32_16x16x32_bf16 v[152:155], v[188:191], v[148:151], 0\n" \
"v_rcp_f32 v185, v160\n" \
"v_mfma_f32_16x16x32_bf16 v[80:83], v[194:197], v[148:151], 0\n" \
"v_mfma_f32_16x16x32_bf16 v[84:87], v[70:73], v[148:151], 0\n" \
"ds_read_b64_tr_b16 a[62:63], v215 offset:27136\n" \
"global_load_lds_dwordx4 v222, s[36:37] offset:2048 sc1\n" \
"v_mfma_f32_16x16x32_bf16 v[36:39], a[40:43], a[16:19], v[36:39]\n" \
"v_add_u32 v213, s74, v211\n" \
"v_add_u32 v214, s74, v212\n" \
"v_mul_f32 v104, v104, v187\n" \
"v_mul_f32 v105, v105, v187\n" \
"v_mul_f32 v106, v106, v187\n" \
"v_mul_f32 v107, v107, v187\n" \
"v_mul_f32 v108, v108, v187\n" \
"v_mul_f32 v109, v109, v187\n" \
"v_mfma_f32_16x16x32_bf16 v[36:39], a[44:47], a[20:23], v[36:39]\n" \
"s_waitcnt lgkmcnt(3)\n" \
"v_xor_b32 v215, 32, v214\n" \
"v_mul_f32 v110, v110, v187\n" \
"v_mul_f32 v111, v111, v187\n" \
"v_cvt_pk_bf16_f32 v104, v104, v105\n" \
"v_cvt_pk_bf16_f32 v105, v106, v107\n" \
"v_cvt_pk_bf16_f32 v106, v108, v109\n" \
"v_cvt_pk_bf16_f32 v107, v110, v111\n" \
"v_mfma_f32_16x16x32_bf16 v[52:55], a[40:43], a[24:27], v[52:55]\n" \
"s_add_u32 s71, s87, 208896\n" \
"v_rcp_f32 v184, v152\n" \
"v_mfma_f32_16x16x32_bf16 v[52:55], a[44:47], a[28:31], v[52:55]\n" \
"s_waitcnt vmcnt(19) lgkmcnt(0)\n" \
"buffer_store_dwordx4 v[104:107], v146, s[56:59], s71 offen sc1\n" \
"global_load_lds_dwordx4 v222, s[36:37] offset:3072 sc1\n" \
"v_mul_f32 v80, v80, v184\n" \
"v_mul_f32 v96, v96, v186\n" \
"v_mul_f32 v97, v97, v186\n" \
"v_mul_f32 v98, v98, v186\n" \
"v_mul_f32 v99, v99, v186\n" \
"v_mul_f32 v100, v100, v186\n" \
"v_mfma_f32_16x16x32_bf16 v[48:51], a[32:35], a[24:27], v[48:51]\n" \
"s_add_u32 s36, s36, 0x1000\n" \
"v_mul_f32 v81, v81, v184\n" \
"v_mul_f32 v82, v82, v184\n" \
"v_mfma_f32_16x16x32_bf16 v[48:51], a[36:39], a[28:31], v[48:51]\n" \
"s_barrier\n" \
"v_mul_f32 v101, v101, v186\n" \
"v_mul_f32 v102, v102, v186\n" \
"v_mul_f32 v103, v103, v186\n" \
"v_cvt_pk_bf16_f32 v96, v96, v97\n" \
"v_cvt_pk_bf16_f32 v97, v98, v99\n" \
"v_cvt_pk_bf16_f32 v98, v100, v101\n" \
"v_mfma_f32_16x16x32_bf16 v[8:11], a[48:51], a[0:3], v[8:11]\n" \
"s_add_u32 m0, m0, 0x5000\n" \
"ds_read_b64_tr_b16 a[32:33], v214 offset:16384\n" \
"v_mul_f32 v83, v83, v184\n" \
"v_mfma_f32_16x16x32_bf16 v[8:11], a[52:55], a[4:7], v[8:11]\n" \
"ds_read_b64_tr_b16 a[34:35], v214 offset:18432\n" \
"global_load_lds_dwordx4 v216, s[38:39] offset:-4096\n" \
"v_cvt_pk_bf16_f32 v99, v102, v103\n" \
"v_mfma_f32_16x16x32_bf16 v[12:15], a[56:59], a[0:3], v[12:15]\n" \
"s_add_u32 s71, s87, 139264\n" \
"ds_read_b64_tr_b16 a[36:37], v214 offset:24576\n" \
"v_mul_f32 v84, v84, v184\n" \
"v_mfma_f32_16x16x32_bf16 v[12:15], a[60:63], a[4:7], v[12:15]\n" \
"buffer_store_dwordx4 v[96:99], v146, s[56:59], s71 offen sc1\n" \
"ds_read_b128 a[0:3], v213 offset:0\n" \
"v_mul_f32 v85, v85, v184\n" \
"v_mul_f32 v88, v88, v185\n" \
"v_mul_f32 v89, v89, v185\n" \
"v_mul_f32 v90, v90, v185\n" \
"v_mul_f32 v91, v91, v185\n" \
"v_mul_f32 v92, v92, v185\n" \
"v_mfma_f32_16x16x32_bf16 v[28:31], a[56:59], a[8:11], v[28:31]\n" \
"ds_read_b128 a[4:7], v213 offset:2048\n" \
"v_mul_f32 v86, v86, v184\n" \
"v_mfma_f32_16x16x32_bf16 v[28:31], a[60:63], a[12:15], v[28:31]\n" \
"ds_read_b64_tr_b16 a[38:39], v214 offset:26624\n" \
"global_load_lds_dwordx4 v217, s[38:39] offset:-3072\n" \
"v_mul_f32 v93, v93, v185\n" \
"v_mul_f32 v94, v94, v185\n" \
"v_mul_f32 v95, v95, v185\n" \
"v_cvt_pk_bf16_f32 v88, v88, v89\n" \
"v_cvt_pk_bf16_f32 v89, v90, v91\n" \
"v_cvt_pk_bf16_f32 v90, v92, v93\n" \
"v_mfma_f32_16x16x32_bf16 v[24:27], a[48:51], a[8:11], v[24:27]\n" \
"ds_read_b64_tr_b16 a[40:41], v215 offset:16384\n" \
"v_mul_f32 v87, v87, v184\n" \
"v_mfma_f32_16x16x32_bf16 v[24:27], a[52:55], a[12:15], v[24:27]\n" \
"ds_read_b128 a[8:11], v213 offset:256\n" \
"v_cvt_pk_bf16_f32 v80, v80, v81\n" \
"v_cvt_pk_bf16_f32 v91, v94, v95\n" \
"v_mfma_f32_16x16x32_bf16 v[40:43], a[48:51], a[16:19], v[40:43]\n" \
"s_add_u32 s71, s87, 69632\n" \
"ds_read_b128 a[12:15], v213 offset:2304\n" \
"v_cvt_pk_bf16_f32 v81, v82, v83\n" \
"v_mfma_f32_16x16x32_bf16 v[40:43], a[52:55], a[20:23], v[40:43]\n" \
"buffer_store_dwordx4 v[88:91], v146, s[56:59], s71 offen sc1\n" \
"ds_read_b64_tr_b16 a[42:43], v215 offset:18432\n" \
"global_load_lds_dwordx4 v218, s[38:39] offset:-2048\n" \
"v_mfma_f32_16x16x32_bf16 v[44:47], a[56:59], a[16:19], v[44:47]\n" \
"ds_read_b64_tr_b16 a[44:45], v215 offset:24576\n" \
"v_cvt_pk_bf16_f32 v82, v84, v85\n" \
"v_mfma_f32_16x16x32_bf16 v[44:47], a[60:63], a[20:23], v[44:47]\n" \
"ds_read_b128 a[16:19], v213 offset:4096\n" \
"v_cvt_pk_bf16_f32 v83, v86, v87\n" \
"v_mfma_f32_16x16x32_bf16 v[60:63], a[56:59], a[24:27], v[60:63]\n" \
"ds_read_b128 a[20:23], v213 offset:6144\n" \
"global_load_lds_dwordx4 v219, s[38:39] offset:-1024\n" \
"v_mfma_f32_16x16x32_bf16 v[60:63], a[60:63], a[28:31], v[60:63]\n" \
"s_add_u32 s38, s38, 0x20000\n" \
"s_addc_u32 s39, s39, 0\n" \
"ds_read_b64_tr_b16 a[46:47], v215 offset:26624\n" \
"v_mfma_f32_16x16x32_bf16 v[56:59], a[48:51], a[24:27], v[56:59]\n" \
"s_add_u32 s70, s87, 0\n" \
"s_sub_u32 s75, s75, 32768\n" \
"s_cselect_b32 s75, 131072, s75\n" \
"v_mfma_f32_16x16x32_bf16 v[56:59], a[52:55], a[28:31], v[56:59]\n" \
"s_waitcnt lgkmcnt(3)\n" \
"buffer_store_dwordx4 v[80:83], v146, s[56:59], s70 offen sc1\n" \
"v_mfma_f32_16x16x32_bf16 v[0:3], a[32:35], a[0:3], v[0:3]\n" \
"s_add_u32 m0, s75, s95\n" \
"ds_read_b128 a[24:27], v213 offset:4352\n" \
"v_mov_b32 v224, s60\n" \
"v_mfma_f32_16x16x32_bf16 v[0:3], a[36:39], a[4:7], v[0:3]\n" \
"s_waitcnt lgkmcnt(1)\n" \
"ds_read_b128 a[28:31], v213 offset:6400\n" \
"global_load_lds_dwordx4 v222, s[36:37] offset:0 sc1\n" \
"v_mfma_f32_16x16x32_bf16 v[4:7], a[40:43], a[0:3], v[4:7]\n" \
"s_sub_u32 s74, s74, 32768\n" \
"s_cselect_b32 s74, 131072, s74\n" \
"ds_read_b64_tr_b16 a[48:49], v214 offset:16896\n" \
"v_mfma_f32_16x16x32_bf16 v[4:7], a[44:47], a[4:7], v[4:7]\n" \
"ds_read_b64_tr_b16 a[50:51], v214 offset:18944\n" \
"v_lshlrev_b32 v225, 28, s61\n" \
"v_mfma_f32_16x16x32_bf16 v[20:23], a[40:43], a[8:11], v[20:23]\n" \
"ds_read_b64_tr_b16 a[52:53], v214 offset:25088\n" \
"v_mfma_f32_16x16x32_bf16 v[20:23], a[44:47], a[12:15], v[20:23]\n" \
"ds_read_b64_tr_b16 a[54:55], v214 offset:27136\n" \
"global_load_lds_dwordx4 v222, s[36:37] offset:1024 sc1\n" \
"v_mfma_f32_16x16x32_bf16 v[16:19], a[32:35], a[8:11], v[16:19]\n" \
"ds_read_b64_tr_b16 a[56:57], v215 offset:16896\n" \
"v_mfma_f32_16x16x32_bf16 v[16:19], a[36:39], a[12:15], v[16:19]\n" \
"ds_read_b64_tr_b16 a[58:59], v215 offset:18944\n" \
"v_mfma_f32_16x16x32_bf16 v[32:35], a[32:35], a[16:19], v[32:35]\n" \
"ds_read_b64_tr_b16 a[60:61], v215 offset:25088\n" \
"v_mfma_f32_16x16x32_bf16 v[32:35], a[36:39], a[20:23], v[32:35]\n" \
"ds_read_b64_tr_b16 a[62:63], v215 offset:27136\n" \
"global_load_lds_dwordx4 v222, s[36:37] offset:2048 sc1\n" \
"v_mfma_f32_16x16x32_bf16 v[36:39], a[40:43], a[16:19], v[36:39]\n" \
"v_add_u32 v213, s74, v211\n" \
"v_add_u32 v214, s74, v212\n" \
"v_mfma_f32_16x16x32_bf16 v[36:39], a[44:47], a[20:23], v[36:39]\n" \
"s_waitcnt lgkmcnt(3)\n" \
"v_xor_b32 v215, 32, v214\n" \
"v_mfma_f32_16x16x32_bf16 v[52:55], a[40:43], a[24:27], v[52:55]\n" \
"v_mfma_f32_16x16x32_bf16 v[52:55], a[44:47], a[28:31], v[52:55]\n" \
"s_waitcnt vmcnt(23) lgkmcnt(0)\n" \
"global_load_lds_dwordx4 v222, s[36:37] offset:3072 sc1\n" \
"v_mfma_f32_16x16x32_bf16 v[48:51], a[32:35], a[24:27], v[48:51]\n" \
"s_add_u32 s36, s36, 0x1000\n" \
"v_mfma_f32_16x16x32_bf16 v[48:51], a[36:39], a[28:31], v[48:51]\n" \
"s_barrier\n" \
"v_mfma_f32_16x16x32_bf16 v[8:11], a[48:51], a[0:3], v[8:11]\n" \
"s_add_u32 m0, m0, 0x5000\n" \
"ds_read_b64_tr_b16 a[32:33], v214 offset:16384\n" \
"v_mfma_f32_16x16x32_bf16 v[8:11], a[52:55], a[4:7], v[8:11]\n" \
"ds_read_b64_tr_b16 a[34:35], v214 offset:18432\n" \
"global_load_lds_dwordx4 v216, s[38:39] offset:-4096\n" \
"v_mfma_f32_16x16x32_bf16 v[12:15], a[56:59], a[0:3], v[12:15]\n" \
"ds_read_b64_tr_b16 a[36:37], v214 offset:24576\n" \
"v_mfma_f32_16x16x32_bf16 v[12:15], a[60:63], a[4:7], v[12:15]\n" \
"ds_read_b128 a[0:3], v213 offset:0\n" \
"v_mfma_f32_16x16x32_bf16 v[28:31], a[56:59], a[8:11], v[28:31]\n" \
"ds_read_b128 a[4:7], v213 offset:2048\n" \
"v_mfma_f32_16x16x32_bf16 v[28:31], a[60:63], a[12:15], v[28:31]\n" \
"ds_read_b64_tr_b16 a[38:39], v214 offset:26624\n" \
"global_load_lds_dwordx4 v217, s[38:39] offset:-3072\n" \
"v_mfma_f32_16x16x32_bf16 v[24:27], a[48:51], a[8:11], v[24:27]\n" \
"ds_read_b64_tr_b16 a[40:41], v215 offset:16384\n" \
"v_mfma_f32_16x16x32_bf16 v[24:27], a[52:55], a[12:15], v[24:27]\n" \
"ds_read_b128 a[8:11], v213 offset:256\n" \
"v_mfma_f32_16x16x32_bf16 v[40:43], a[48:51], a[16:19], v[40:43]\n" \
"ds_read_b128 a[12:15], v213 offset:2304\n" \
"v_mfma_f32_16x16x32_bf16 v[40:43], a[52:55], a[20:23], v[40:43]\n" \
"ds_read_b64_tr_b16 a[42:43], v215 offset:18432\n" \
"global_load_lds_dwordx4 v218, s[38:39] offset:-2048\n" \
"v_mfma_f32_16x16x32_bf16 v[44:47], a[56:59], a[16:19], v[44:47]\n" \
"ds_read_b64_tr_b16 a[44:45], v215 offset:24576\n" \
"v_mfma_f32_16x16x32_bf16 v[44:47], a[60:63], a[20:23], v[44:47]\n" \
"ds_read_b128 a[16:19], v213 offset:4096\n" \
"v_mfma_f32_16x16x32_bf16 v[60:63], a[56:59], a[24:27], v[60:63]\n" \
"ds_read_b128 a[20:23], v213 offset:6144\n" \
"global_load_lds_dwordx4 v219, s[38:39] offset:-1024\n" \
"v_mfma_f32_16x16x32_bf16 v[60:63], a[60:63], a[28:31], v[60:63]\n" \
"s_add_u32 s38, s38, 0x20000\n" \
"s_addc_u32 s39, s39, 0\n" \
"ds_read_b64_tr_b16 a[46:47], v215 offset:26624\n" \
"v_mfma_f32_16x16x32_bf16 v[56:59], a[48:51], a[24:27], v[56:59]\n" \
"s_sub_u32 s75, s75, 32768\n" \
"s_cselect_b32 s75, 131072, s75\n" \
"v_mfma_f32_16x16x32_bf16 v[56:59], a[52:55], a[28:31], v[56:59]\n" \
"s_waitcnt lgkmcnt(3)\n" \
"v_mfma_f32_16x16x32_bf16 v[0:3], a[32:35], a[0:3], v[0:3]\n" \
"ds_read_b128 a[24:27], v213 offset:4352\n" \
"s_add_u32 m0, s75, s95\n" \
"v_mfma_f32_16x16x32_bf16 v[0:3], a[36:39], a[4:7], v[0:3]\n" \
"s_waitcnt lgkmcnt(1)\n" \
"ds_read_b128 a[28:31], v213 offset:6400\n" \
"global_load_lds_dwordx4 v222, s[36:37] offset:0 sc1\n" \
"v_mfma_f32_16x16x32_bf16 v[4:7], a[40:43], a[0:3], v[4:7]\n" \
"s_sub_u32 s74, s74, 32768\n" \
"s_cselect_b32 s74, 131072, s74\n" \
"ds_read_b64_tr_b16 a[48:49], v214 offset:16896\n" \
"v_mfma_f32_16x16x32_bf16 v[4:7], a[44:47], a[4:7], v[4:7]\n" \
"ds_read_b64_tr_b16 a[50:51], v214 offset:18944\n" \
"v_mfma_f32_16x16x32_bf16 v[20:23], a[40:43], a[8:11], v[20:23]\n" \
"ds_read_b64_tr_b16 a[52:53], v214 offset:25088\n" \
"v_mfma_f32_16x16x32_bf16 v[20:23], a[44:47], a[12:15], v[20:23]\n" \
"ds_read_b64_tr_b16 a[54:55], v214 offset:27136\n" \
"global_load_lds_dwordx4 v222, s[36:37] offset:1024 sc1\n" \
"v_mfma_f32_16x16x32_bf16 v[16:19], a[32:35], a[8:11], v[16:19]\n" \
"ds_read_b64_tr_b16 a[56:57], v215 offset:16896\n" \
"v_mfma_f32_16x16x32_bf16 v[16:19], a[36:39], a[12:15], v[16:19]\n" \
"ds_read_b64_tr_b16 a[58:59], v215 offset:18944\n" \
"v_mfma_f32_16x16x32_bf16 v[32:35], a[32:35], a[16:19], v[32:35]\n" \
"ds_read_b64_tr_b16 a[60:61], v215 offset:25088\n" \
"v_mfma_f32_16x16x32_bf16 v[32:35], a[36:39], a[20:23], v[32:35]\n" \
"ds_read_b64_tr_b16 a[62:63], v215 offset:27136\n" \
"global_load_lds_dwordx4 v222, s[36:37] offset:2048 sc1\n" \
"v_mfma_f32_16x16x32_bf16 v[36:39], a[40:43], a[16:19], v[36:39]\n" \
"v_add_u32 v213, s74, v211\n" \
"v_add_u32 v214, s74, v212\n" \
"v_mfma_f32_16x16x32_bf16 v[36:39], a[44:47], a[20:23], v[36:39]\n" \
"s_waitcnt lgkmcnt(3)\n" \
"v_xor_b32 v215, 32, v214\n" \
"v_mfma_f32_16x16x32_bf16 v[52:55], a[40:43], a[24:27], v[52:55]\n" \
"v_mfma_f32_16x16x32_bf16 v[52:55], a[44:47], a[28:31], v[52:55]\n" \
"s_waitcnt vmcnt(11) lgkmcnt(0)\n" \
"global_load_lds_dwordx4 v222, s[36:37] offset:3072 sc1\n" \
"v_mfma_f32_16x16x32_bf16 v[48:51], a[32:35], a[24:27], v[48:51]\n" \
"s_add_u32 s36, s36, 0x1000\n" \
"v_mfma_f32_16x16x32_bf16 v[48:51], a[36:39], a[28:31], v[48:51]\n" \
"s_barrier\n" \
"v_mfma_f32_16x16x32_bf16 v[8:11], a[48:51], a[0:3], v[8:11]\n" \
"s_add_u32 m0, m0, 0x5000\n" \
"ds_read_b64_tr_b16 a[32:33], v214 offset:16384\n" \
"v_mfma_f32_16x16x32_bf16 v[8:11], a[52:55], a[4:7], v[8:11]\n" \
"ds_read_b64_tr_b16 a[34:35], v214 offset:18432\n" \
"global_load_lds_dwordx4 v216, s[38:39] offset:-4096\n" \
"v_mfma_f32_16x16x32_bf16 v[12:15], a[56:59], a[0:3], v[12:15]\n" \
"ds_read_b64_tr_b16 a[36:37], v214 offset:24576\n" \
"v_mfma_f32_16x16x32_bf16 v[12:15], a[60:63], a[4:7], v[12:15]\n" \
"buffer_store_dword v224, v225, s[56:59], s30 offen sc1\n" \
"ds_read_b128 a[0:3], v213 offset:0\n" \
"v_mfma_f32_16x16x32_bf16 v[28:31], a[56:59], a[8:11], v[28:31]\n" \
"ds_read_b128 a[4:7], v213 offset:2048\n" \
"v_mfma_f32_16x16x32_bf16 v[28:31], a[60:63], a[12:15], v[28:31]\n" \
"ds_read_b64_tr_b16 a[38:39], v214 offset:26624\n" \
"global_load_lds_dwordx4 v217, s[38:39] offset:-3072\n" \
"v_mfma_f32_16x16x32_bf16 v[24:27], a[48:51], a[8:11], v[24:27]\n" \
"ds_read_b64_tr_b16 a[40:41], v215 offset:16384\n" \
"v_mfma_f32_16x16x32_bf16 v[24:27], a[52:55], a[12:15], v[24:27]\n" \
"ds_read_b128 a[8:11], v213 offset:256\n" \
"v_mfma_f32_16x16x32_bf16 v[40:43], a[48:51], a[16:19], v[40:43]\n" \
"ds_read_b128 a[12:15], v213 offset:2304\n" \
"v_mfma_f32_16x16x32_bf16 v[40:43], a[52:55], a[20:23], v[40:43]\n" \
"ds_read_b64_tr_b16 a[42:43], v215 offset:18432\n" \
"global_load_lds_dwordx4 v218, s[38:39] offset:-2048\n" \
"v_mfma_f32_16x16x32_bf16 v[44:47], a[56:59], a[16:19], v[44:47]\n" \
"ds_read_b64_tr_b16 a[44:45], v215 offset:24576\n" \
"v_mfma_f32_16x16x32_bf16 v[44:47], a[60:63], a[20:23], v[44:47]\n" \
"ds_read_b128 a[16:19], v213 offset:4096\n" \
"v_mfma_f32_16x16x32_bf16 v[60:63], a[56:59], a[24:27], v[60:63]\n" \
"ds_read_b128 a[20:23], v213 offset:6144\n" \
"global_load_lds_dwordx4 v219, s[38:39] offset:-1024\n" \
"v_mfma_f32_16x16x32_bf16 v[60:63], a[60:63], a[28:31], v[60:63]\n" \
"s_add_u32 s38, s38, 0x20000\n" \
"s_addc_u32 s39, s39, 0\n" \
"ds_read_b64_tr_b16 a[46:47], v215 offset:26624\n" \
"v_mfma_f32_16x16x32_bf16 v[56:59], a[48:51], a[24:27], v[56:59]\n" \
"s_sub_u32 s75, s75, 32768\n" \
"s_cselect_b32 s75, 131072, s75\n" \
"v_mfma_f32_16x16x32_bf16 v[56:59], a[52:55], a[28:31], v[56:59]\n" \
"s_waitcnt lgkmcnt(3)\n" \
"s_cmp_eq_u32 s78, 0\n" \
"s_cbranch_scc0 Lrole1_%=\n" \
"s_sub_u32 s72, s64, 15\n" \
"s_call_b64 s[68:69], Lo0_%=\n" \
"buffer_load_dword v220, off, s[56:59], s79 sc1\n" \
"s_mov_b32 s72, 2\n" \
"s_call_b64 s[68:69], Lo0_%=\n" \
"v_mfma_f32_16x16x32_bf16 v[0:3], a[32:35], a[0:3], v[0:3]\n" \
"s_lshl_b32 s71, s61, 4\n" \
"ds_read_b128 a[24:27], v213 offset:4352\n" \
"v_subrev_u32 v72, s71, v251\n" \
"v_mfma_f32_16x16x32_bf16 v[0:3], a[36:39], a[4:7], v[0:3]\n" \
"s_waitcnt lgkmcnt(1)\n" \
"ds_read_b128 a[28:31], v213 offset:6400\n" \
"v_mov_b32 v73, 0x3f80\n" \
"v_mfma_f32_16x16x32_bf16 v[4:7], a[40:43], a[0:3], v[4:7]\n" \
"s_sub_u32 s74, s74, 32768\n" \
"s_cselect_b32 s74, 131072, s74\n" \
"ds_read_b64_tr_b16 a[48:49], v214 offset:16896\n" \
"v_mfma_f32_16x16x32_bf16 v[4:7], a[44:47], a[4:7], v[4:7]\n" \
"ds_read_b64_tr_b16 a[50:51], v214 offset:18944\n" \
"v_mov_b32 v74, 0x3f800000\n" \
"v_mfma_f32_16x16x32_bf16 v[20:23], a[40:43], a[8:11], v[20:23]\n" \
"ds_read_b64_tr_b16 a[52:53], v214 offset:25088\n" \
"v_cmp_eq_u32 vcc, 0, v72\n" \
"v_mfma_f32_16x16x32_bf16 v[20:23], a[44:47], a[12:15], v[20:23]\n" \
"ds_read_b64_tr_b16 a[54:55], v214 offset:27136\n" \
"v_cmp_eq_u32 s[66:67], 1, v72\n" \
"v_mfma_f32_16x16x32_bf16 v[16:19], a[32:35], a[8:11], v[16:19]\n" \
"ds_read_b64_tr_b16 a[56:57], v215 offset:16896\n" \
"v_mov_b32 v66, 0\n" \
"v_mfma_f32_16x16x32_bf16 v[16:19], a[36:39], a[12:15], v[16:19]\n" \
"ds_read_b64_tr_b16 a[58:59], v215 offset:18944\n" \
"v_mov_b32 v68, 0\n" \
"v_mfma_f32_16x16x32_bf16 v[32:35], a[32:35], a[16:19], v[32:35]\n" \
"ds_read_b64_tr_b16 a[60:61], v215 offset:25088\n" \
"v_cndmask_b32 v64, 0, v73, vcc\n" \
"v_mfma_f32_16x16x32_bf16 v[32:35], a[36:39], a[20:23], v[32:35]\n" \
"ds_read_b64_tr_b16 a[62:63], v215 offset:27136\n" \
"v_cndmask_b32 v64, v64, v74, s[66:67]\n" \
"v_mfma_f32_16x16x32_bf16 v[36:39], a[40:43], a[16:19], v[36:39]\n" \
"v_add_u32 v213, s74, v211\n" \
"v_add_u32 v214, s74, v212\n" \
"v_mfma_f32_16x16x32_bf16 v[36:39], a[44:47], a[20:23], v[36:39]\n" \
"s_waitcnt lgkmcnt(3)\n" \
"v_xor_b32 v215, 32, v214\n" \
"v_mov_b32 v70, v64\n" \
"v_mfma_f32_16x16x32_bf16 v[52:55], a[40:43], a[24:27], v[52:55]\n" \
"v_cmp_eq_u32 vcc, 2, v72\n" \
"v_cmp_eq_u32 s[66:67], 3, v72\n" \
"v_mfma_f32_16x16x32_bf16 v[52:55], a[44:47], a[28:31], v[52:55]\n" \
"s_waitcnt vmcnt(17) lgkmcnt(0)\n" \
"v_mov_b32 v67, 0\n" \
"v_mov_b32 v69, 0\n" \
"v_mfma_f32_16x16x32_bf16 v[48:51], a[32:35], a[24:27], v[48:51]\n" \
"v_cndmask_b32 v65, 0, v73, vcc\n" \
"v_cndmask_b32 v65, v65, v74, s[66:67]\n" \
"v_mfma_f32_16x16x32_bf16 v[48:51], a[36:39], a[28:31], v[48:51]\n" \
"s_barrier\n" \
"v_mov_b32 v71, v65\n" \
"v_mfma_f32_16x16x32_bf16 v[8:11], a[48:51], a[0:3], v[8:11]\n" \
"s_lshl_b32 s83, s95, 1\n" \
"s_add_u32 s83, s83, s74\n" \
"ds_read_b64_tr_b16 a[32:33], v214 offset:16384\n" \
"v_mfma_f32_16x16x32_bf16 v[8:11], a[52:55], a[4:7], v[8:11]\n" \
"s_waitcnt vmcnt(16)\n" \
"ds_read_b64_tr_b16 a[34:35], v214 offset:18432\n" \
"v_cmp_ne_u32 vcc, s60, v220\n" \
"v_mfma_f32_16x16x32_bf16 v[12:15], a[56:59], a[0:3], v[12:15]\n" \
"ds_read_b64_tr_b16 a[36:37], v214 offset:24576\n" \
"v_mfma_f32_16x16x32_bf16 v[12:15], a[60:63], a[4:7], v[12:15]\n" \
"s_and_b32 vcc_hi, vcc_lo, 1\n" \
"s_cbranch_scc1 Lpl4_%=\n" \
"Lpr4_%=:\n" \
"ds_read_b128 a[0:3], v213 offset:0\n" \
"v_mfma_f32_16x16x32_bf16 v[28:31], a[56:59], a[8:11], v[28:31]\n" \
"ds_read_b128 a[4:7], v213 offset:2048\n" \
"v_mfma_f32_16x16x32_bf16 v[28:31], a[60:63], a[12:15], v[28:31]\n" \
"s_add_u32 s71, s76, 0\n" \
"ds_read_b64_tr_b16 a[38:39], v214 offset:26624\n" \
"buffer_load_dwordx4 v[176:179], v248, s[56:59], s71 offen offset:0 sc1\n" \
"v_mfma_f32_16x16x32_bf16 v[24:27], a[48:51], a[8:11], v[24:27]\n" \
"ds_read_b64_tr_b16 a[40:41], v215 offset:16384\n" \
"v_mfma_f32_16x16x32_bf16 v[24:27], a[52:55], a[12:15], v[24:27]\n" \
"buffer_load_dwordx4 v[180:183], v248, s[56:59], s71 offen offset:1024 sc1\n" \
"ds_read_b128 a[8:11], v213 offset:256\n" \
"v_mfma_f32_16x16x32_bf16 v[40:43], a[48:51], a[16:19], v[40:43]\n" \
"ds_read_b128 a[12:15], v213 offset:2304\n" \
"v_mfma_f32_16x16x32_bf16 v[40:43], a[52:55], a[20:23], v[40:43]\n" \
"ds_read_b64_tr_b16 a[42:43], v215 offset:18432\n" \
"v_mfma_f32_16x16x32_bf16 v[44:47], a[56:59], a[16:19], v[44:47]\n" \
"ds_read_b64_tr_b16 a[44:45], v215 offset:24576\n" \
"v_mfma_f32_16x16x32_bf16 v[44:47], a[60:63], a[20:23], v[44:47]\n" \
"ds_read_b128 a[16:19], v213 offset:4096\n" \
"v_mfma_f32_16x16x32_bf16 v[60:63], a[56:59], a[24:27], v[60:63]\n" \
"ds_read_b128 a[20:23], v213 offset:6144\n" \
"v_mfma_f32_16x16x32_bf16 v[60:63], a[60:63], a[28:31], v[60:63]\n" \
"ds_read_b64_tr_b16 a[46:47], v215 offset:26624\n" \
"v_mfma_f32_16x16x32_bf16 v[56:59], a[48:51], a[24:27], v[56:59]\n" \
"buffer_load_dwordx4 v[184:187], v248, s[56:59], s71 offen offset:2048 sc1\n" \
"v_mfma_f32_16x16x32_bf16 v[56:59], a[52:55], a[28:31], v[56:59]\n" \
"s_waitcnt lgkmcnt(3)\n" \
"buffer_load_dwordx4 v[188:191], v248, s[56:59], s71 offen offset:3072 sc1\n" \
"v_mfma_f32_16x16x32_bf16 v[0:3], a[32:35], a[0:3], v[0:3]\n" \
"ds_read_b128 a[24:27], v213 offset:4352\n" \
"s_add_u32 s71, s76, 4096\n" \
"v_mfma_f32_16x16x32_bf16 v[0:3], a[36:39], a[4:7], v[0:3]\n" \
"s_waitcnt lgkmcnt(1)\n" \
"buffer_load_dwordx4 v[192:195], v248, s[56:59], s71 offen offset:0 sc1\n" \
"ds_read_b128 a[28:31], v213 offset:6400\n" \
"v_mfma_f32_16x16x32_bf16 v[4:7], a[40:43], a[0:3], v[4:7]\n" \
"s_sub_u32 s74, s74, 32768\n" \
"s_cselect_b32 s74, 131072, s74\n" \
"ds_read_b64_tr_b16 a[48:49], v214 offset:16896\n" \
"v_mfma_f32_16x16x32_bf16 v[4:7], a[44:47], a[4:7], v[4:7]\n" \
"ds_read_b64_tr_b16 a[50:51], v214 offset:18944\n" \
"v_lshrrev_b32 v132, 4, v248\n" \
"v_mfma_f32_16x16x32_bf16 v[20:23], a[40:43], a[8:11], v[20:23]\n" \
"ds_read_b64_tr_b16 a[52:53], v214 offset:25088\n" \
"v_and_b32 v133, 15, v132\n" \
"v_mfma_f32_16x16x32_bf16 v[20:23], a[44:47], a[12:15], v[20:23]\n" \
"ds_read_b64_tr_b16 a[54:55], v214 offset:27136\n" \
"v_mul_u32_u24 v128, 144, v133\n" \
"v_mfma_f32_16x16x32_bf16 v[16:19], a[32:35], a[8:11], v[16:19]\n" \
"ds_read_b64_tr_b16 a[56:57], v215 offset:16896\n" \
"v_lshrrev_b32 v133, 4, v132\n" \
"v_mfma_f32_16x16x32_bf16 v[16:19], a[36:39], a[12:15], v[16:19]\n" \
"ds_read_b64_tr_b16 a[58:59], v215 offset:18944\n" \
"v_lshl_add_u32 v128, v133, 3, v128\n" \
"v_mfma_f32_16x16x32_bf16 v[32:35], a[32:35], a[16:19], v[32:35]\n" \
"ds_read_b64_tr_b16 a[60:61], v215 offset:25088\n" \
"v_add_u32 v128, s83, v128\n" \
"v_mfma_f32_16x16x32_bf16 v[32:35], a[36:39], a[20:23], v[32:35]\n" \
"ds_read_b64_tr_b16 a[62:63], v215 offset:27136\n" \
"v_lshrrev_b32 v133, 3, v132\n" \
"v_mfma_f32_16x16x32_bf16 v[36:39], a[40:43], a[16:19], v[36:39]\n" \
"v_add_u32 v213, s74, v211\n" \
"v_add_u32 v214, s74, v212\n" \
"v_mfma_f32_16x16x32_bf16 v[36:39], a[44:47], a[20:23], v[36:39]\n" \
"s_waitcnt lgkmcnt(3)\n" \
"v_xor_b32 v215, 32, v214\n" \
"v_and_b32 v134, 7, v132\n" \
"v_mfma_f32_16x16x32_bf16 v[52:55], a[40:43], a[24:27], v[52:55]\n" \
"buffer_load_dwordx4 v[196:199], v248, s[56:59], s71 offen offset:1024 sc1\n" \
"v_mfma_f32_16x16x32_bf16 v[52:55], a[44:47], a[28:31], v[52:55]\n" \
"s_waitcnt vmcnt(6) lgkmcnt(0)\n" \
"buffer_load_dwordx4 v[200:203], v248, s[56:59], s71 offen offset:2048 sc1\n" \
"v_mfma_f32_16x16x32_bf16 v[48:51], a[32:35], a[24:27], v[48:51]\n" \
"buffer_load_dwordx4 v[204:207], v248, s[56:59], s71 offen offset:3072 sc1\n" \
"v_mfma_f32_16x16x32_bf16 v[48:51], a[36:39], a[28:31], v[48:51]\n" \
"s_barrier\n" \
"v_mfma_f32_16x16x32_bf16 v[8:11], a[48:51], a[0:3], v[8:11]\n" \
"s_mov_b32 s70, 0x20000000\n" \
"ds_read_b128 a[192:195], v213 offset:0\n" \
"ds_read_b64_tr_b16 a[128:129], v214 offset:16384\n" \
"v_mfma_f32_16x16x32_bf16 v[8:11], a[52:55], a[4:7], v[8:11]\n" \
"s_sub_u32 s74, s74, 32768\n" \
"s_cselect_b32 s74, 131072, s74\n" \
"ds_read_b64_tr_b16 a[130:131], v214 offset:18432\n" \
"v_mfma_f32_16x16x32_bf16 v[12:15], a[56:59], a[0:3], v[12:15]\n" \
"ds_read_b128 a[196:199], v213 offset:2048\n" \
"ds_read_b64_tr_b16 a[132:133], v214 offset:24576\n" \
"v_mfma_f32_16x16x32_bf16 v[12:15], a[60:63], a[4:7], v[12:15]\n" \
"ds_read_b64_tr_b16 a[134:135], v214 offset:26624\n" \
"v_add_u32 v226, s74, v211\n" \
"v_mfma_f32_16x16x32_bf16 v[28:31], a[56:59], a[8:11], v[28:31]\n" \
"ds_read_b128 a[200:203], v213 offset:256\n" \
"ds_read_b64_tr_b16 a[136:137], v215 offset:16384\n" \
"v_mfma_f32_16x16x32_bf16 v[28:31], a[60:63], a[12:15], v[28:31]\n" \
"ds_read_b64_tr_b16 a[138:139], v215 offset:18432\n" \
"v_add_u32 v227, s74, v212\n" \
"v_mfma_f32_16x16x32_bf16 v[24:27], a[48:51], a[8:11], v[24:27]\n" \
"ds_read_b128 a[204:207], v213 offset:2304\n" \
"ds_read_b64_tr_b16 a[140:141], v215 offset:24576\n" \
"v_mfma_f32_16x16x32_bf16 v[24:27], a[52:55], a[12:15], v[24:27]\n" \
"ds_read_b64_tr_b16 a[142:143], v215 offset:26624\n" \
"v_xor_b32 v228, 32, v227\n" \
"v_mfma_f32_16x16x32_bf16 v[40:43], a[48:51], a[16:19], v[40:43]\n" \
"s_cmp_eq_u32 s79, 65601536\n" \
"s_cselect_b32 s70, 65593344, s70\n" \
"ds_read_b64_tr_b16 a[144:145], v214 offset:16896\n" \
"v_mfma_f32_16x16x32_bf16 v[40:43], a[52:55], a[20:23], v[40:43]\n" \
"buffer_store_dword v224, v225, s[56:59], s70 offen sc1\n" \
"ds_read_b64_tr_b16 a[146:147], v214 offset:18944\n" \
"v_mfma_f32_16x16x32_bf16 v[44:47], a[56:59], a[16:19], v[44:47]\n" \
"ds_read_b64_tr_b16 a[148:149], v214 offset:25088\n" \
"v_mul_u32_u24 v129, 144, v133\n" \
"v_mfma_f32_16x16x32_bf16 v[44:47], a[60:63], a[20:23], v[44:47]\n" \
"ds_read_b64_tr_b16 a[150:151], v214 offset:27136\n" \
"v_lshl_add_u32 v129, v134, 4, v129\n" \
"v_mfma_f32_16x16x32_bf16 v[60:63], a[56:59], a[24:27], v[60:63]\n" \
"ds_read_b64_tr_b16 a[152:153], v215 offset:16896\n" \
"v_add_u32 v129, s83, v129\n" \
"v_mfma_f32_16x16x32_bf16 v[60:63], a[60:63], a[28:31], v[60:63]\n" \
"ds_read_b64_tr_b16 a[154:155], v215 offset:18944\n" \
"v_lshlrev_b32 v134, 4, v134\n" \
"v_mfma_f32_16x16x32_bf16 v[56:59], a[48:51], a[24:27], v[56:59]\n" \
"ds_read_b64_tr_b16 a[156:157], v215 offset:25088\n" \
"v_lshl_add_u32 v130, v133, 12, v134\n" \
"v_mfma_f32_16x16x32_bf16 v[56:59], a[52:55], a[28:31], v[56:59]\n" \
"s_waitcnt lgkmcnt(4)\n" \
"ds_read_b64_tr_b16 a[158:159], v215 offset:27136\n" \
"v_mfma_f32_16x16x32_bf16 v[0:3], a[128:131], a[192:195], v[0:3]\n" \
"ds_read_b128 a[224:227], v226 offset:0\n" \
"ds_read_b64_tr_b16 a[160:161], v227 offset:16384\n" \
"v_mfma_f32_16x16x32_bf16 v[0:3], a[132:135], a[196:199], v[0:3]\n" \
"ds_read_b64_tr_b16 a[162:163], v227 offset:18432\n" \
"v_mfma_f32_16x16x32_bf16 v[16:19], a[128:131], a[200:203], v[16:19]\n" \
"ds_read_b64_tr_b16 a[164:165], v227 offset:24576\n" \
"Lpg12022_%=:\n" \
"s_cbranch_execz Lpg13120_%=\n" \
"v_mfma_f32_16x16x32_bf16 v[16:19], a[132:135], a[204:207], v[16:19]\n" \
"ds_read_b128 a[228:231], v226 offset:2048\n" \
"ds_read_b64_tr_b16 a[166:167], v227 offset:26624\n" \
"v_mfma_f32_16x16x32_bf16 v[20:23], a[136:139], a[200:203], v[20:23]\n" \
"ds_read_b64_tr_b16 a[168:169], v228 offset:16384\n" \
"v_mfma_f32_16x16x32_bf16 v[20:23], a[140:143], a[204:207], v[20:23]\n" \
"ds_read_b64_tr_b16 a[170:171], v228 offset:18432\n" \
"v_mfma_f32_16x16x32_bf16 v[4:7], a[136:139], a[192:195], v[4:7]\n" \
"ds_read_b128 a[232:235], v226 offset:256\n" \
"ds_read_b64_tr_b16 a[172:173], v228 offset:24576\n" \
"v_mfma_f32_16x16x32_bf16 v[4:7], a[140:143], a[196:199], v[4:7]\n" \
"s_waitcnt lgkmcnt(6)\n" \
"ds_read_b64_tr_b16 a[174:175], v228 offset:26624\n" \
"v_mfma_f32_16x16x32_bf16 v[8:11], a[144:147], a[192:195], v[8:11]\n" \
"ds_read_b64_tr_b16 a[176:177], v227 offset:16896\n" \
"v_mfma_f32_16x16x32_bf16 v[8:11], a[148:151], a[196:199], v[8:11]\n" \
"ds_read_b128 a[236:239], v226 offset:2304\n" \
"ds_read_b64_tr_b16 a[178:179], v227 offset:18944\n" \
"v_mfma_f32_16x16x32_bf16 v[24:27], a[144:147], a[200:203], v[24:27]\n" \
"ds_read_b64_tr_b16 a[180:181], v227 offset:25088\n" \
"v_mfma_f32_16x16x32_bf16 v[24:27], a[148:151], a[204:207], v[24:27]\n" \
"ds_read_b64_tr_b16 a[182:183], v227 offset:27136\n" \
"v_mfma_f32_16x16x32_bf16 v[28:31], a[152:155], a[200:203], v[28:31]\n" \
"ds_read_b64_tr_b16 a[184:185], v228 offset:16896\n" \
"v_mfma_f32_16x16x32_bf16 v[28:31], a[156:159], a[204:207], v[28:31]\n" \
"ds_read_b64_tr_b16 a[186:187], v228 offset:18944\n" \
"v_mfma_f32_16x16x32_bf16 v[12:15], a[152:155], a[192:195], v[12:15]\n" \
"ds_read_b64_tr_b16 a[188:189], v228 offset:25088\n" \
"v_mfma_f32_16x16x32_bf16 v[12:15], a[156:159], a[196:199], v[12:15]\n" \
"s_waitcnt lgkmcnt(4)\n" \
"ds_read_b64_tr_b16 a[190:191], v228 offset:27136\n" \
"v_mfma_f32_16x16x32_bf16 v[0:3], a[160:163], a[224:227], v[0:3]\n" \
"s_waitcnt vmcnt(1)\n" \
"ds_read_b128 a[208:211], v213 offset:4096\n" \
"v_mfma_f32_16x16x32_bf16 v[0:3], a[164:167], a[228:231], v[0:3]\n" \
"ds_read_b128 a[212:215], v213 offset:6144\n" \
"v_mfma_f32_16x16x32_bf16 v[0:3], v[64:67], v[176:179], v[0:3]\n" \
"ds_read_b128 a[240:243], v226 offset:4096\n" \
"v_mfma_f32_16x16x32_bf16 v[16:19], a[160:163], a[232:235], v[16:19]\n" \
"ds_read_b128 a[244:247], v226 offset:6144\n" \
"v_mfma_f32_16x16x32_bf16 v[16:19], a[164:167], a[236:239], v[16:19]\n" \
"ds_read_b128 a[216:219], v213 offset:4352\n" \
"v_mfma_f32_16x16x32_bf16 v[16:19], v[64:67], v[184:187], v[16:19]\n" \
"ds_read_b128 a[220:223], v213 offset:6400\n" \
"v_cvt_pk_bf16_f32 v0, v0, v1\n" \
"v_mfma_f32_16x16x32_bf16 v[20:23], a[168:171], a[232:235], v[20:23]\n" \
"ds_read_b128 a[248:251], v226 offset:4352\n" \
"v_cvt_pk_bf16_f32 v1, v2, v3\n" \
"v_mfma_f32_16x16x32_bf16 v[20:23], a[172:175], a[236:239], v[20:23]\n" \
"ds_read_b128 a[252:255], v226 offset:6400\n" \
"v_mfma_f32_16x16x32_bf16 v[20:23], v[68:71], v[184:187], v[20:23]\n" \
"ds_write_b64 v128, v[0:1] offset:0\n" \
"v_mfma_f32_16x16x32_bf16 v[4:7], a[168:171], a[224:227], v[4:7]\n" \
"v_cvt_pk_bf16_f32 v16, v16, v17\n" \
"v_cvt_pk_bf16_f32 v17, v18, v19\n" \
"v_mfma_f32_16x16x32_bf16 v[4:7], a[172:175], a[228:231], v[4:7]\n" \
"ds_write_b64 v128, v[16:17] offset:2304\n" \
"v_mfma_f32_16x16x32_bf16 v[4:7], v[68:71], v[176:179], v[4:7]\n" \
"s_waitcnt lgkmcnt(3)\n" \
"v_cvt_pk_bf16_f32 v20, v20, v21\n" \
"v_cvt_pk_bf16_f32 v21, v22, v23\n" \
"v_mfma_f32_16x16x32_bf16 v[8:11], a[176:179], a[224:227], v[8:11]\n" \
"ds_write_b64 v128, v[20:21] offset:2336\n" \
"v_mfma_f32_16x16x32_bf16 v[8:11], a[180:183], a[228:231], v[8:11]\n" \
"v_mfma_f32_16x16x32_bf16 v[8:11], v[64:67], v[180:183], v[8:11]\n" \
"v_cvt_pk_bf16_f32 v4, v4, v5\n" \
"v_cvt_pk_bf16_f32 v5, v6, v7\n" \
"v_mfma_f32_16x16x32_bf16 v[24:27], a[176:179], a[232:235], v[24:27]\n" \
"ds_write_b64 v128, v[4:5] offset:32\n" \
"v_mfma_f32_16x16x32_bf16 v[24:27], a[180:183], a[236:239], v[24:27]\n" \
"v_mfma_f32_16x16x32_bf16 v[24:27], v[64:67], v[188:191], v[24:27]\n" \
"v_cvt_pk_bf16_f32 v8, v8, v9\n" \
"v_cvt_pk_bf16_f32 v9, v10, v11\n" \
"v_mfma_f32_16x16x32_bf16 v[28:31], a[184:187], a[232:235], v[28:31]\n" \
"ds_write_b64 v128, v[8:9] offset:64\n" \
"v_mfma_f32_16x16x32_bf16 v[28:31], a[188:191], a[236:239], v[28:31]\n" \
"v_mfma_f32_16x16x32_bf16 v[28:31], v[68:71], v[188:191], v[28:31]\n" \
"v_cvt_pk_bf16_f32 v24, v24, v25\n" \
"v_cvt_pk_bf16_f32 v25, v26, v27\n" \
"v_mfma_f32_16x16x32_bf16 v[12:15], a[184:187], a[224:227], v[12:15]\n" \
"ds_write_b64 v128, v[24:25] offset:2368\n" \
"v_mfma_f32_16x16x32_bf16 v[12:15], a[188:191], a[228:231], v[12:15]\n" \
"v_mfma_f32_16x16x32_bf16 v[12:15], v[68:71], v[180:183], v[12:15]\n" \
"v_cvt_pk_bf16_f32 v28, v28, v29\n" \
"v_cvt_pk_bf16_f32 v29, v30, v31\n" \
"v_mfma_f32_16x16x32_bf16 v[32:35], a[128:131], a[208:211], v[32:35]\n" \
"ds_write_b64 v128, v[28:29] offset:2400\n" \
"v_mfma_f32_16x16x32_bf16 v[32:35], a[132:135], a[212:215], v[32:35]\n" \
"v_mfma_f32_16x16x32_bf16 v[32:35], a[160:163], a[240:243], v[32:35]\n" \
"v_cvt_pk_bf16_f32 v12, v12, v13\n" \
"v_cvt_pk_bf16_f32 v13, v14, v15\n" \
"v_mfma_f32_16x16x32_bf16 v[32:35], a[164:167], a[244:247], v[32:35]\n" \
"ds_write_b64 v128, v[12:13] offset:96\n" \
"v_mfma_f32_16x16x32_bf16 v[32:35], v[64:67], v[192:195], v[32:35]\n" \
"ds_read_b128 v[140:143], v129 offset:0\n" \
"v_mfma_f32_16x16x32_bf16 v[36:39], a[168:171], a[240:243], v[36:39]\n" \
"ds_read_b128 v[144:147], v129 offset:1152\n" \
"v_mfma_f32_16x16x32_bf16 v[36:39], a[172:175], a[244:247], v[36:39]\n" \
"ds_read_b128 v[148:151], v129 offset:2304\n" \
"v_mfma_f32_16x16x32_bf16 v[36:39], a[136:139], a[208:211], v[36:39]\n" \
"ds_read_b128 v[152:155], v129 offset:3456\n" \
"v_mfma_f32_16x16x32_bf16 v[36:39], a[140:143], a[212:215], v[36:39]\n" \
"v_cvt_pk_bf16_f32 v32, v32, v33\n" \
"v_cvt_pk_bf16_f32 v33, v34, v35\n" \
"v_mfma_f32_16x16x32_bf16 v[36:39], v[68:71], v[192:195], v[36:39]\n" \
"ds_write_b64 v128, v[32:33] offset:0\n" \
"v_mfma_f32_16x16x32_bf16 v[40:43], a[144:147], a[208:211], v[40:43]\n" \
"v_mfma_f32_16x16x32_bf16 v[40:43], a[148:151], a[212:215], v[40:43]\n" \
"s_add_u32 s70, s77, 0\n" \
"s_waitcnt lgkmcnt(4)\n" \
"v_mfma_f32_16x16x32_bf16 v[40:43], a[176:179], a[240:243], v[40:43]\n" \
"v_cvt_pk_bf16_f32 v36, v36, v37\n" \
"v_cvt_pk_bf16_f32 v37, v38, v39\n" \
"v_mfma_f32_16x16x32_bf16 v[40:43], a[180:183], a[244:247], v[40:43]\n" \
"buffer_store_dwordx4 v[140:143], v130, s[52:55], s70 offen sc1\n" \
"ds_write_b64 v128, v[36:37] offset:32\n" \
"v_mfma_f32_16x16x32_bf16 v[40:43], v[64:67], v[196:199], v[40:43]\n" \
"v_mfma_f32_16x16x32_bf16 v[44:47], a[184:187], a[240:243], v[44:47]\n" \
"v_mfma_f32_16x16x32_bf16 v[44:47], a[188:191], a[244:247], v[44:47]\n" \
"v_mfma_f32_16x16x32_bf16 v[44:47], a[152:155], a[208:211], v[44:47]\n" \
"s_add_u32 s70, s77, 32768\n" \
"v_cvt_pk_bf16_f32 v40, v40, v41\n" \
"v_cvt_pk_bf16_f32 v41, v42, v43\n" \
"v_mfma_f32_16x16x32_bf16 v[44:47], a[156:159], a[212:215], v[44:47]\n" \
"s_waitcnt lgkmcnt(4)\n" \
"buffer_store_dwordx4 v[144:147], v130, s[52:55], s70 offen sc1\n" \
"ds_write_b64 v128, v[40:41] offset:64\n" \
"v_mfma_f32_16x16x32_bf16 v[44:47], v[68:71], v[196:199], v[44:47]\n" \
"v_mfma_f32_16x16x32_bf16 v[48:51], a[128:131], a[216:219], v[48:51]\n" \
"v_mfma_f32_16x16x32_bf16 v[48:51], a[132:135], a[220:223], v[48:51]\n" \
"v_mfma_f32_16x16x32_bf16 v[48:51], a[160:163], a[248:251], v[48:51]\n" \
"s_add_u32 s70, s77, 65536\n" \
"v_cvt_pk_bf16_f32 v44, v44, v45\n" \
"v_cvt_pk_bf16_f32 v45, v46, v47\n" \
"v_mfma_f32_16x16x32_bf16 v[48:51], a[164:167], a[252:255], v[48:51]\n" \
"s_waitcnt lgkmcnt(4)\n" \
"buffer_store_dwordx4 v[148:151], v130, s[52:55], s70 offen sc1\n" \
"ds_write_b64 v128, v[44:45] offset:96\n" \
"v_mfma_f32_16x16x32_bf16 v[48:51], v[64:67], v[200:203], v[48:51]\n" \
"ds_read_b128 v[156:159], v129 offset:0\n" \
"v_mfma_f32_16x16x32_bf16 v[52:55], a[168:171], a[248:251], v[52:55]\n" \
"ds_read_b128 v[160:163], v129 offset:1152\n" \
"v_mfma_f32_16x16x32_bf16 v[52:55], a[172:175], a[252:255], v[52:55]\n" \
"v_mfma_f32_16x16x32_bf16 v[52:55], a[136:139], a[216:219], v[52:55]\n" \
"s_add_u32 s70, s77, 98304\n" \
"v_cvt_pk_bf16_f32 v48, v48, v49\n" \
"v_cvt_pk_bf16_f32 v49, v50, v51\n" \
"v_mfma_f32_16x16x32_bf16 v[52:55], a[140:143], a[220:223], v[52:55]\n" \
"s_waitcnt lgkmcnt(6)\n" \
"buffer_store_dwordx4 v[152:155], v130, s[52:55], s70 offen sc1\n" \
"ds_write_b64 v128, v[48:49] offset:2304\n" \
"v_mfma_f32_16x16x32_bf16 v[52:55], v[68:71], v[200:203], v[52:55]\n" \
"v_mfma_f32_16x16x32_bf16 v[56:59], a[144:147], a[216:219], v[56:59]\n" \
"v_mfma_f32_16x16x32_bf16 v[56:59], a[148:151], a[220:223], v[56:59]\n" \
"v_mfma_f32_16x16x32_bf16 v[56:59], a[176:179], a[248:251], v[56:59]\n" \
"s_add_u32 s70, s77, 131072\n" \
"v_cvt_pk_bf16_f32 v52, v52, v53\n" \
"v_cvt_pk_bf16_f32 v53, v54, v55\n" \
"v_mfma_f32_16x16x32_bf16 v[56:59], a[180:183], a[252:255], v[56:59]\n" \
"s_waitcnt lgkmcnt(2)\n" \
"buffer_store_dwordx4 v[156:159], v130, s[52:55], s70 offen sc1\n" \
"ds_write_b64 v128, v[52:53] offset:2336\n" \
"v_mfma_f32_16x16x32_bf16 v[56:59], v[64:67], v[204:207], v[56:59]\n" \
"v_mfma_f32_16x16x32_bf16 v[60:63], a[184:187], a[248:251], v[60:63]\n" \
"v_mfma_f32_16x16x32_bf16 v[60:63], a[188:191], a[252:255], v[60:63]\n" \
"v_mfma_f32_16x16x32_bf16 v[60:63], a[152:155], a[216:219], v[60:63]\n" \
"s_add_u32 s70, s77, 163840\n" \
"v_cvt_pk_bf16_f32 v56, v56, v57\n" \
"v_cvt_pk_bf16_f32 v57, v58, v59\n" \
"v_mfma_f32_16x16x32_bf16 v[60:63], a[156:159], a[220:223], v[60:63]\n" \
"s_waitcnt lgkmcnt(2)\n" \
"buffer_store_dwordx4 v[160:163], v130, s[52:55], s70 offen sc1\n" \
"ds_write_b64 v128, v[56:57] offset:2368\n" \
"v_mfma_f32_16x16x32_bf16 v[60:63], v[68:71], v[204:207], v[60:63]\n" \
"s_nop 7\n" \
"v_cvt_pk_bf16_f32 v60, v60, v61\n" \
"v_cvt_pk_bf16_f32 v61, v62, v63\n" \
"ds_write_b64 v128, v[60:61] offset:2400\n" \
"ds_read_b128 v[164:167], v129 offset:2304\n" \
"ds_read_b128 v[168:171], v129 offset:3456\n" \
"s_add_u32 s70, s77, 196608\n" \
"s_waitcnt lgkmcnt(1)\n" \
"buffer_store_dwordx4 v[164:167], v130, s[52:55], s70 offen sc1\n" \
"s_add_u32 s70, s77, 229376\n" \
"s_waitcnt lgkmcnt(0)\n" \
"buffer_store_dwordx4 v[168:171], v130, s[52:55], s70 offen sc1\n" \
"s_endpgm\n" \
"Lpg12210_%=:\n" \
"s_cbranch_execz Lpg13183_%=\n" \
"Lo0_%=:\n" \
"v_mfma_f32_16x16x32_bf16 v[0:3], a[32:35], a[0:3], v[0:3]\n" \
"ds_read_b128 a[24:27], v213 offset:4352\n" \
"s_add_u32 m0, s75, s95\n" \
"v_mfma_f32_16x16x32_bf16 v[0:3], a[36:39], a[4:7], v[0:3]\n" \
"s_waitcnt lgkmcnt(1)\n" \
"ds_read_b128 a[28:31], v213 offset:6400\n" \
"global_load_lds_dwordx4 v222, s[36:37] offset:0 sc1\n" \
"v_mfma_f32_16x16x32_bf16 v[4:7], a[40:43], a[0:3], v[4:7]\n" \
"s_sub_u32 s74, s74, 32768\n" \
"s_cselect_b32 s74, 131072, s74\n" \
"ds_read_b64_tr_b16 a[48:49], v214 offset:16896\n" \
"v_mfma_f32_16x16x32_bf16 v[4:7], a[44:47], a[4:7], v[4:7]\n" \
"ds_read_b64_tr_b16 a[50:51], v214 offset:18944\n" \
"v_mfma_f32_16x16x32_bf16 v[20:23], a[40:43], a[8:11], v[20:23]\n" \
"ds_read_b64_tr_b16 a[52:53], v214 offset:25088\n" \
"v_mfma_f32_16x16x32_bf16 v[20:23], a[44:47], a[12:15], v[20:23]\n" \
"ds_read_b64_tr_b16 a[54:55], v214 offset:27136\n" \
"global_load_lds_dwordx4 v222, s[36:37] offset:1024 sc1\n" \
"v_mfma_f32_16x16x32_bf16 v[16:19], a[32:35], a[8:11], v[16:19]\n" \
"ds_read_b64_tr_b16 a[56:57], v215 offset:16896\n" \
"v_mfma_f32_16x16x32_bf16 v[16:19], a[36:39], a[12:15], v[16:19]\n" \
"ds_read_b64_tr_b16 a[58:59], v215 offset:18944\n" \
"v_mfma_f32_16x16x32_bf16 v[32:35], a[32:35], a[16:19], v[32:35]\n" \
"ds_read_b64_tr_b16 a[60:61], v215 offset:25088\n" \
"v_mfma_f32_16x16x32_bf16 v[32:35], a[36:39], a[20:23], v[32:35]\n" \
"ds_read_b64_tr_b16 a[62:63], v215 offset:27136\n" \
"global_load_lds_dwordx4 v222, s[36:37] offset:2048 sc1\n" \
"v_mfma_f32_16x16x32_bf16 v[36:39], a[40:43], a[16:19], v[36:39]\n" \
"v_add_u32 v213, s74, v211\n" \
"v_add_u32 v214, s74, v212\n" \
"v_mfma_f32_16x16x32_bf16 v[36:39], a[44:47], a[20:23], v[36:39]\n" \
"s_waitcnt lgkmcnt(3)\n" \
"v_xor_b32 v215, 32, v214\n" \
"v_mfma_f32_16x16x32_bf16 v[52:55], a[40:43], a[24:27], v[52:55]\n" \
"v_mfma_f32_16x16x32_bf16 v[52:55], a[44:47], a[28:31], v[52:55]\n" \
"s_waitcnt vmcnt(19) lgkmcnt(0)\n" \
"global_load_lds_dwordx4 v222, s[36:37] offset:3072 sc1\n" \
"v_mfma_f32_16x16x32_bf16 v[48:51], a[32:35], a[24:27], v[48:51]\n" \
"s_add_u32 s36, s36, 0x1000\n" \
"v_mfma_f32_16x16x32_bf16 v[48:51], a[36:39], a[28:31], v[48:51]\n" \
"s_barrier\n" \
"v_mfma_f32_16x16x32_bf16 v[8:11], a[48:51], a[0:3], v[8:11]\n" \
"s_add_u32 m0, m0, 0x5000\n" \
"ds_read_b64_tr_b16 a[32:33], v214 offset:16384\n" \
"v_mfma_f32_16x16x32_bf16 v[8:11], a[52:55], a[4:7], v[8:11]\n" \
"ds_read_b64_tr_b16 a[34:35], v214 offset:18432\n" \
"global_load_lds_dwordx4 v216, s[38:39] offset:-4096\n" \
"v_mfma_f32_16x16x32_bf16 v[12:15], a[56:59], a[0:3], v[12:15]\n" \
"ds_read_b64_tr_b16 a[36:37], v214 offset:24576\n" \
"v_mfma_f32_16x16x32_bf16 v[12:15], a[60:63], a[4:7], v[12:15]\n" \
"ds_read_b128 a[0:3], v213 offset:0\n" \
"v_mfma_f32_16x16x32_bf16 v[28:31], a[56:59], a[8:11], v[28:31]\n" \
"ds_read_b128 a[4:7], v213 offset:2048\n" \
"v_mfma_f32_16x16x32_bf16 v[28:31], a[60:63], a[12:15], v[28:31]\n" \
"ds_read_b64_tr_b16 a[38:39], v214 offset:26624\n" \
"global_load_lds_dwordx4 v217, s[38:39] offset:-3072\n" \
"v_mfma_f32_16x16x32_bf16 v[24:27], a[48:51], a[8:11], v[24:27]\n" \
"ds_read_b64_tr_b16 a[40:41], v215 offset:16384\n" \
"v_mfma_f32_16x16x32_bf16 v[24:27], a[52:55], a[12:15], v[24:27]\n" \
"ds_read_b128 a[8:11], v213 offset:256\n" \
"v_mfma_f32_16x16x32_bf16 v[40:43], a[48:51], a[16:19], v[40:43]\n" \
"ds_read_b128 a[12:15], v213 offset:2304\n" \
"v_mfma_f32_16x16x32_bf16 v[40:43], a[52:55], a[20:23], v[40:43]\n" \
"ds_read_b64_tr_b16 a[42:43], v215 offset:18432\n" \
"global_load_lds_dwordx4 v218, s[38:39] offset:-2048\n" \
"v_mfma_f32_16x16x32_bf16 v[44:47], a[56:59], a[16:19], v[44:47]\n" \
"ds_read_b64_tr_b16 a[44:45], v215 offset:24576\n" \
"v_mfma_f32_16x16x32_bf16 v[44:47], a[60:63], a[20:23], v[44:47]\n" \
"ds_read_b128 a[16:19], v213 offset:4096\n" \
"global_load_lds_dwordx4 v219, s[38:39] offset:-1024\n" \
"v_mfma_f32_16x16x32_bf16 v[60:63], a[56:59], a[24:27], v[60:63]\n" \
"s_add_u32 s38, s38, 0x20000\n" \
"s_addc_u32 s39, s39, 0\n" \
"ds_read_b128 a[20:23], v213 offset:6144\n" \
"v_mfma_f32_16x16x32_bf16 v[60:63], a[60:63], a[28:31], v[60:63]\n" \
"s_sub_u32 s75, s75, 32768\n" \
"s_cselect_b32 s75, 131072, s75\n" \
"ds_read_b64_tr_b16 a[46:47], v215 offset:26624\n" \
"v_mfma_f32_16x16x32_bf16 v[56:59], a[48:51], a[24:27], v[56:59]\n" \
"s_sub_u32 s72, s72, 1\n" \
"s_cmp_lg_u32 s72, 0\n" \
"v_mfma_f32_16x16x32_bf16 v[56:59], a[52:55], a[28:31], v[56:59]\n" \
"s_waitcnt lgkmcnt(3)\n" \
"s_cbranch_scc1 Lo0_%=\n" \
"s_setpc_b64 s[68:69]\n" \
"Lrole1_%=:\n" \
"Lpg12298_%=:\n" \
"s_cbranch_execz Lcb1_%=\n" \
"s_sub_u32 s72, 49, s64\n" \
"s_call_b64 s[68:69], Lo0_%=\n" \
"buffer_load_dword v220, v221, s[56:59], s82 offen sc1\n" \
"s_mov_b32 s72, 2\n" \
"s_call_b64 s[68:69], Lo0_%=\n" \
"s_waitcnt vmcnt(16)\n" \
"v_cmp_ne_u32 vcc, s60, v220\n" \
"s_nop 1\n" \
"s_and_b32 vcc_hi, vcc_lo, 65535\n" \
"s_cbranch_scc1 Lpl5_%=\n" \
"Lpr5_%=:\n" \
"s_mov_b32 s36, s73\n" \
"s_mov_b64 s[38:39], s[40:41]\n" \
"s_mov_b32 s72, 4\n" \
"s_call_b64 s[68:69], Lo0_%=\n" \
"v_mfma_f32_16x16x32_bf16 v[64:67], a[32:35], a[0:3], 0\n" \
"ds_read_b128 a[24:27], v213 offset:4352\n" \
"s_add_u32 m0, s75, s95\n" \
"v_mfma_f32_16x16x32_bf16 v[64:67], a[36:39], a[4:7], v[64:67]\n" \
"s_waitcnt lgkmcnt(1)\n" \
"ds_read_b128 a[28:31], v213 offset:6400\n" \
"global_load_lds_dwordx4 v222, s[36:37] offset:0 sc1\n" \
"v_mfma_f32_16x16x32_bf16 v[68:71], a[40:43], a[0:3], 0\n" \
"s_sub_u32 s74, s74, 32768\n" \
"s_cselect_b32 s74, 131072, s74\n" \
"ds_read_b64_tr_b16 a[48:49], v214 offset:16896\n" \
"v_mfma_f32_16x16x32_bf16 v[68:71], a[44:47], a[4:7], v[68:71]\n" \
"ds_read_b64_tr_b16 a[50:51], v214 offset:18944\n" \
"v_cvt_pk_bf16_f32 v0, v0, v1\n" \
"v_mfma_f32_16x16x32_bf16 v[84:87], a[40:43], a[8:11], 0\n" \
"ds_read_b64_tr_b16 a[52:53], v214 offset:25088\n" \
"v_cvt_pk_bf16_f32 v1, v2, v3\n" \
"v_mfma_f32_16x16x32_bf16 v[84:87], a[44:47], a[12:15], v[84:87]\n" \
"ds_read_b64_tr_b16 a[54:55], v214 offset:27136\n" \
"global_load_lds_dwordx4 v222, s[36:37] offset:1024 sc1\n" \
"v_mfma_f32_16x16x32_bf16 v[80:83], a[32:35], a[8:11], 0\n" \
"ds_read_b64_tr_b16 a[56:57], v215 offset:16896\n" \
"v_cvt_pk_bf16_f32 v2, v4, v5\n" \
"v_mfma_f32_16x16x32_bf16 v[80:83], a[36:39], a[12:15], v[80:83]\n" \
"ds_read_b64_tr_b16 a[58:59], v215 offset:18944\n" \
"v_cvt_pk_bf16_f32 v3, v6, v7\n" \
"v_mfma_f32_16x16x32_bf16 v[96:99], a[32:35], a[16:19], 0\n" \
"s_add_u32 s71, s76, 0\n" \
"ds_read_b64_tr_b16 a[60:61], v215 offset:25088\n" \
"v_mfma_f32_16x16x32_bf16 v[96:99], a[36:39], a[20:23], v[96:99]\n" \
"ds_read_b64_tr_b16 a[62:63], v215 offset:27136\n" \
"global_load_lds_dwordx4 v222, s[36:37] offset:2048 sc1\n" \
"v_mfma_f32_16x16x32_bf16 v[100:103], a[40:43], a[16:19], 0\n" \
"v_add_u32 v213, s74, v211\n" \
"v_add_u32 v214, s74, v212\n" \
"v_mfma_f32_16x16x32_bf16 v[100:103], a[44:47], a[20:23], v[100:103]\n" \
"s_waitcnt lgkmcnt(3)\n" \
"v_xor_b32 v215, 32, v214\n" \
"v_mfma_f32_16x16x32_bf16 v[116:119], a[40:43], a[24:27], 0\n" \
"v_mfma_f32_16x16x32_bf16 v[116:119], a[44:47], a[28:31], v[116:119]\n" \
"s_waitcnt vmcnt(19) lgkmcnt(0)\n" \
"global_load_lds_dwordx4 v222, s[36:37] offset:3072 sc1\n" \
"v_mfma_f32_16x16x32_bf16 v[112:115], a[32:35], a[24:27], 0\n" \
"s_add_u32 s36, s36, 0x1000\n" \
"v_mfma_f32_16x16x32_bf16 v[112:115], a[36:39], a[28:31], v[112:115]\n" \
"s_barrier\n" \
"v_mfma_f32_16x16x32_bf16 v[72:75], a[48:51], a[0:3], 0\n" \
"s_add_u32 m0, m0, 0x5000\n" \
"ds_read_b64_tr_b16 a[32:33], v214 offset:16384\n" \
"v_mfma_f32_16x16x32_bf16 v[72:75], a[52:55], a[4:7], v[72:75]\n" \
"ds_read_b64_tr_b16 a[34:35], v214 offset:18432\n" \
"global_load_lds_dwordx4 v216, s[38:39] offset:-4096\n" \
"v_mfma_f32_16x16x32_bf16 v[76:79], a[56:59], a[0:3], 0\n" \
"ds_read_b64_tr_b16 a[36:37], v214 offset:24576\n" \
"v_mfma_f32_16x16x32_bf16 v[76:79], a[60:63], a[4:7], v[76:79]\n" \
"ds_read_b128 a[0:3], v213 offset:0\n" \
"v_mfma_f32_16x16x32_bf16 v[92:95], a[56:59], a[8:11], 0\n" \
"ds_read_b128 a[4:7], v213 offset:2048\n" \
"v_mfma_f32_16x16x32_bf16 v[92:95], a[60:63], a[12:15], v[92:95]\n" \
"ds_read_b64_tr_b16 a[38:39], v214 offset:26624\n" \
"global_load_lds_dwordx4 v217, s[38:39] offset:-3072\n" \
"v_mfma_f32_16x16x32_bf16 v[88:91], a[48:51], a[8:11], 0\n" \
"ds_read_b64_tr_b16 a[40:41], v215 offset:16384\n" \
"v_mfma_f32_16x16x32_bf16 v[88:91], a[52:55], a[12:15], v[88:91]\n" \
"buffer_store_dwordx4 v[0:3], v248, s[56:59], s71 offen offset:0 sc1\n" \
"ds_read_b128 a[8:11], v213 offset:256\n" \
"v_cvt_pk_bf16_f32 v4, v8, v9\n" \
"v_cvt_pk_bf16_f32 v5, v10, v11\n" \
"v_cvt_pk_bf16_f32 v6, v12, v13\n" \
"v_mfma_f32_16x16x32_bf16 v[104:107], a[48:51], a[16:19], 0\n" \
"ds_read_b128 a[12:15], v213 offset:2304\n" \
"v_cvt_pk_bf16_f32 v7, v14, v15\n" \
"v_mfma_f32_16x16x32_bf16 v[104:107], a[52:55], a[20:23], v[104:107]\n" \
"ds_read_b64_tr_b16 a[42:43], v215 offset:18432\n" \
"global_load_lds_dwordx4 v218, s[38:39] offset:-2048\n" \
"v_mfma_f32_16x16x32_bf16 v[108:111], a[56:59], a[16:19], 0\n" \
"ds_read_b64_tr_b16 a[44:45], v215 offset:24576\n" \
"v_mfma_f32_16x16x32_bf16 v[108:111], a[60:63], a[20:23], v[108:111]\n" \
"buffer_store_dwordx4 v[4:7], v248, s[56:59], s71 offen offset:1024 sc1\n" \
"ds_read_b128 a[16:19], v213 offset:4096\n" \
"v_cvt_pk_bf16_f32 v8, v16, v17\n" \
"v_cvt_pk_bf16_f32 v9, v18, v19\n" \
"v_cvt_pk_bf16_f32 v10, v20, v21\n" \
"v_mfma_f32_16x16x32_bf16 v[124:127], a[56:59], a[24:27], 0\n" \
"ds_read_b128 a[20:23], v213 offset:6144\n" \
"global_load_lds_dwordx4 v219, s[38:39] offset:-1024\n" \
"v_mfma_f32_16x16x32_bf16 v[124:127], a[60:63], a[28:31], v[124:127]\n" \
"s_add_u32 s38, s38, 0x20000\n" \
"s_addc_u32 s39, s39, 0\n" \
"ds_read_b64_tr_b16 a[46:47], v215 offset:26624\n" \
"v_mfma_f32_16x16x32_bf16 v[120:123], a[48:51], a[24:27], 0\n" \
"s_sub_u32 s75, s75, 32768\n" \
"s_cselect_b32 s75, 131072, s75\n" \
"v_cvt_pk_bf16_f32 v11, v22, v23\n" \
"v_mfma_f32_16x16x32_bf16 v[120:123], a[52:55], a[28:31], v[120:123]\n" \
"s_waitcnt lgkmcnt(3)\n" \
"buffer_store_dwordx4 v[8:11], v248, s[56:59], s71 offen offset:2048 sc1\n" \
"v_cvt_pk_bf16_f32 v12, v24, v25\n" \
"v_cvt_pk_bf16_f32 v13, v26, v27\n" \
"v_cvt_pk_bf16_f32 v14, v28, v29\n" \
"v_cvt_pk_bf16_f32 v15, v30, v31\n" \
"v_mfma_f32_16x16x32_bf16 v[64:67], a[32:35], a[0:3], v[64:67]\n" \
"ds_read_b128 a[24:27], v213 offset:4352\n" \
"s_add_u32 m0, s75, s95\n" \
"v_mfma_f32_16x16x32_bf16 v[64:67], a[36:39], a[4:7], v[64:67]\n" \
"s_waitcnt lgkmcnt(1)\n" \
"ds_read_b128 a[28:31], v213 offset:6400\n" \
"global_load_lds_dwordx4 v222, s[36:37] offset:0 sc1\n" \
"v_mfma_f32_16x16x32_bf16 v[68:71], a[40:43], a[0:3], v[68:71]\n" \
"s_sub_u32 s74, s74, 32768\n" \
"s_cselect_b32 s74, 131072, s74\n" \
"ds_read_b64_tr_b16 a[48:49], v214 offset:16896\n" \
"v_mfma_f32_16x16x32_bf16 v[68:71], a[44:47], a[4:7], v[68:71]\n" \
"ds_read_b64_tr_b16 a[50:51], v214 offset:18944\n" \
"v_mfma_f32_16x16x32_bf16 v[84:87], a[40:43], a[8:11], v[84:87]\n" \
"ds_read_b64_tr_b16 a[52:53], v214 offset:25088\n" \
"v_mfma_f32_16x16x32_bf16 v[84:87], a[44:47], a[12:15], v[84:87]\n" \
"ds_read_b64_tr_b16 a[54:55], v214 offset:27136\n" \
"global_load_lds_dwordx4 v222, s[36:37] offset:1024 sc1\n" \
"v_mfma_f32_16x16x32_bf16 v[80:83], a[32:35], a[8:11], v[80:83]\n" \
"ds_read_b64_tr_b16 a[56:57], v215 offset:16896\n" \
"v_mfma_f32_16x16x32_bf16 v[80:83], a[36:39], a[12:15], v[80:83]\n" \
"ds_read_b64_tr_b16 a[58:59], v215 offset:18944\n" \
"v_mfma_f32_16x16x32_bf16 v[96:99], a[32:35], a[16:19], v[96:99]\n" \
"ds_read_b64_tr_b16 a[60:61], v215 offset:25088\n" \
"v_mfma_f32_16x16x32_bf16 v[96:99], a[36:39], a[20:23], v[96:99]\n" \
"ds_read_b64_tr_b16 a[62:63], v215 offset:27136\n" \
"global_load_lds_dwordx4 v222, s[36:37] offset:2048 sc1\n" \
"v_mfma_f32_16x16x32_bf16 v[100:103], a[40:43], a[16:19], v[100:103]\n" \
"v_add_u32 v213, s74, v211\n" \
"v_add_u32 v214, s74, v212\n" \
"v_mfma_f32_16x16x32_bf16 v[100:103], a[44:47], a[20:23], v[100:103]\n" \
"s_waitcnt lgkmcnt(3)\n" \
"v_xor_b32 v215, 32, v214\n" \
"v_mfma_f32_16x16x32_bf16 v[116:119], a[40:43], a[24:27], v[116:119]\n" \
"v_mfma_f32_16x16x32_bf16 v[116:119], a[44:47], a[28:31], v[116:119]\n" \
"s_waitcnt vmcnt(22) lgkmcnt(0)\n" \
"global_load_lds_dwordx4 v222, s[36:37] offset:3072 sc1\n" \
"v_mfma_f32_16x16x32_bf16 v[112:115], a[32:35], a[24:27], v[112:115]\n" \
"s_add_u32 s36, s36, 0x1000\n" \
"v_mfma_f32_16x16x32_bf16 v[112:115], a[36:39], a[28:31], v[112:115]\n" \
"s_barrier\n" \
"v_mfma_f32_16x16x32_bf16 v[72:75], a[48:51], a[0:3], v[72:75]\n" \
"s_add_u32 m0, m0, 0x5000\n" \
"ds_read_b64_tr_b16 a[32:33], v214 offset:16384\n" \
"v_mfma_f32_16x16x32_bf16 v[72:75], a[52:55], a[4:7], v[72:75]\n" \
"ds_read_b64_tr_b16 a[34:35], v214 offset:18432\n" \
"global_load_lds_dwordx4 v216, s[38:39] offset:-4096\n" \
"v_mfma_f32_16x16x32_bf16 v[76:79], a[56:59], a[0:3], v[76:79]\n" \
"ds_read_b64_tr_b16 a[36:37], v214 offset:24576\n" \
"v_mfma_f32_16x16x32_bf16 v[76:79], a[60:63], a[4:7], v[76:79]\n" \
"ds_read_b128 a[0:3], v213 offset:0\n" \
"v_mfma_f32_16x16x32_bf16 v[92:95], a[56:59], a[8:11], v[92:95]\n" \
"ds_read_b128 a[4:7], v213 offset:2048\n" \
"v_mfma_f32_16x16x32_bf16 v[92:95], a[60:63], a[12:15], v[92:95]\n" \
"ds_read_b64_tr_b16 a[38:39], v214 offset:26624\n" \
"global_load_lds_dwordx4 v217, s[38:39] offset:-3072\n" \
"v_mfma_f32_16x16x32_bf16 v[88:91], a[48:51], a[8:11], v[88:91]\n" \
"ds_read_b64_tr_b16 a[40:41], v215 offset:16384\n" \
"v_mfma_f32_16x16x32_bf16 v[88:91], a[52:55], a[12:15], v[88:91]\n" \
"buffer_store_dwordx4 v[12:15], v248, s[56:59], s71 offen offset:3072 sc1\n" \
"ds_read_b128 a[8:11], v213 offset:256\n" \
"v_cvt_pk_bf16_f32 v16, v32, v33\n" \
"v_cvt_pk_bf16_f32 v17, v34, v35\n" \
"v_cvt_pk_bf16_f32 v18, v36, v37\n" \
"v_mfma_f32_16x16x32_bf16 v[104:107], a[48:51], a[16:19], v[104:107]\n" \
"ds_read_b128 a[12:15], v213 offset:2304\n" \
"v_cvt_pk_bf16_f32 v19, v38, v39\n" \
"v_mfma_f32_16x16x32_bf16 v[104:107], a[52:55], a[20:23], v[104:107]\n" \
"s_add_u32 s71, s76, 4096\n" \
"ds_read_b64_tr_b16 a[42:43], v215 offset:18432\n" \
"global_load_lds_dwordx4 v218, s[38:39] offset:-2048\n" \
"v_mfma_f32_16x16x32_bf16 v[108:111], a[56:59], a[16:19], v[108:111]\n" \
"ds_read_b64_tr_b16 a[44:45], v215 offset:24576\n" \
"v_mfma_f32_16x16x32_bf16 v[108:111], a[60:63], a[20:23], v[108:111]\n" \
"buffer_store_dwordx4 v[16:19], v248, s[56:59], s71 offen offset:0 sc1\n" \
"ds_read_b128 a[16:19], v213 offset:4096\n" \
"v_cvt_pk_bf16_f32 v20, v40, v41\n" \
"v_cvt_pk_bf16_f32 v21, v42, v43\n" \
"v_cvt_pk_bf16_f32 v22, v44, v45\n" \
"v_mfma_f32_16x16x32_bf16 v[124:127], a[56:59], a[24:27], v[124:127]\n" \
"ds_read_b128 a[20:23], v213 offset:6144\n" \
"global_load_lds_dwordx4 v219, s[38:39] offset:-1024\n" \
"v_mfma_f32_16x16x32_bf16 v[124:127], a[60:63], a[28:31], v[124:127]\n" \
"s_add_u32 s38, s38, 0x20000\n" \
"s_addc_u32 s39, s39, 0\n" \
"ds_read_b64_tr_b16 a[46:47], v215 offset:26624\n" \
"v_mfma_f32_16x16x32_bf16 v[120:123], a[48:51], a[24:27], v[120:123]\n" \
"s_sub_u32 s75, s75, 32768\n" \
"s_cselect_b32 s75, 131072, s75\n" \
"v_cvt_pk_bf16_f32 v23, v46, v47\n" \
"v_mfma_f32_16x16x32_bf16 v[120:123], a[52:55], a[28:31], v[120:123]\n" \
"s_waitcnt lgkmcnt(3)\n" \
"buffer_store_dwordx4 v[20:23], v248, s[56:59], s71 offen offset:1024 sc1\n" \
"v_cvt_pk_bf16_f32 v24, v48, v49\n" \
"v_cvt_pk_bf16_f32 v25, v50, v51\n" \
"v_cvt_pk_bf16_f32 v26, v52, v53\n" \
"v_cvt_pk_bf16_f32 v27, v54, v55\n" \
"v_mfma_f32_16x16x32_bf16 v[64:67], a[32:35], a[0:3], v[64:67]\n" \
"ds_read_b128 a[24:27], v213 offset:4352\n" \
"s_add_u32 m0, s75, s95\n" \
"v_mfma_f32_16x16x32_bf16 v[64:67], a[36:39], a[4:7], v[64:67]\n" \
"s_waitcnt lgkmcnt(1)\n" \
"ds_read_b128 a[28:31], v213 offset:6400\n" \
"global_load_lds_dwordx4 v222, s[36:37] offset:0 sc1\n" \
"v_mfma_f32_16x16x32_bf16 v[68:71], a[40:43], a[0:3], v[68:71]\n" \
"s_sub_u32 s74, s74, 32768\n" \
"s_cselect_b32 s74, 131072, s74\n" \
"ds_read_b64_tr_b16 a[48:49], v214 offset:16896\n" \
"v_mfma_f32_16x16x32_bf16 v[68:71], a[44:47], a[4:7], v[68:71]\n" \
"ds_read_b64_tr_b16 a[50:51], v214 offset:18944\n" \
"v_mfma_f32_16x16x32_bf16 v[84:87], a[40:43], a[8:11], v[84:87]\n" \
"ds_read_b64_tr_b16 a[52:53], v214 offset:25088\n" \
"v_mfma_f32_16x16x32_bf16 v[84:87], a[44:47], a[12:15], v[84:87]\n" \
"ds_read_b64_tr_b16 a[54:55], v214 offset:27136\n" \
"global_load_lds_dwordx4 v222, s[36:37] offset:1024 sc1\n" \
"v_mfma_f32_16x16x32_bf16 v[80:83], a[32:35], a[8:11], v[80:83]\n" \
"ds_read_b64_tr_b16 a[56:57], v215 offset:16896\n" \
"v_mfma_f32_16x16x32_bf16 v[80:83], a[36:39], a[12:15], v[80:83]\n" \
"ds_read_b64_tr_b16 a[58:59], v215 offset:18944\n" \
"v_mfma_f32_16x16x32_bf16 v[96:99], a[32:35], a[16:19], v[96:99]\n" \
"ds_read_b64_tr_b16 a[60:61], v215 offset:25088\n" \
"v_mfma_f32_16x16x32_bf16 v[96:99], a[36:39], a[20:23], v[96:99]\n" \
"ds_read_b64_tr_b16 a[62:63], v215 offset:27136\n" \
"global_load_lds_dwordx4 v222, s[36:37] offset:2048 sc1\n" \
"v_mfma_f32_16x16x32_bf16 v[100:103], a[40:43], a[16:19], v[100:103]\n" \
"v_add_u32 v213, s74, v211\n" \
"v_add_u32 v214, s74, v212\n" \
"v_mfma_f32_16x16x32_bf16 v[100:103], a[44:47], a[20:23], v[100:103]\n" \
"s_waitcnt lgkmcnt(3)\n" \
"v_xor_b32 v215, 32, v214\n" \
"v_mfma_f32_16x16x32_bf16 v[116:119], a[40:43], a[24:27], v[116:119]\n" \
"v_mfma_f32_16x16x32_bf16 v[116:119], a[44:47], a[28:31], v[116:119]\n" \
"s_waitcnt vmcnt(25) lgkmcnt(0)\n" \
"global_load_lds_dwordx4 v222, s[36:37] offset:3072 sc1\n" \
"v_mfma_f32_16x16x32_bf16 v[112:115], a[32:35], a[24:27], v[112:115]\n" \
"s_add_u32 s36, s36, 0x1000\n" \
"v_mfma_f32_16x16x32_bf16 v[112:115], a[36:39], a[28:31], v[112:115]\n" \
"s_barrier\n" \
"v_mfma_f32_16x16x32_bf16 v[72:75], a[48:51], a[0:3], v[72:75]\n" \
"s_add_u32 m0, m0, 0x5000\n" \
"ds_read_b64_tr_b16 a[32:33], v214 offset:16384\n" \
"v_mfma_f32_16x16x32_bf16 v[72:75], a[52:55], a[4:7], v[72:75]\n" \
"ds_read_b64_tr_b16 a[34:35], v214 offset:18432\n" \
"global_load_lds_dwordx4 v216, s[38:39] offset:-4096\n" \
"v_mfma_f32_16x16x32_bf16 v[76:79], a[56:59], a[0:3], v[76:79]\n" \
"ds_read_b64_tr_b16 a[36:37], v214 offset:24576\n" \
"v_mfma_f32_16x16x32_bf16 v[76:79], a[60:63], a[4:7], v[76:79]\n" \
"ds_read_b128 a[0:3], v213 offset:0\n" \
"v_mfma_f32_16x16x32_bf16 v[92:95], a[56:59], a[8:11], v[92:95]\n" \
"ds_read_b128 a[4:7], v213 offset:2048\n" \
"v_mfma_f32_16x16x32_bf16 v[92:95], a[60:63], a[12:15], v[92:95]\n" \
"ds_read_b64_tr_b16 a[38:39], v214 offset:26624\n" \
"global_load_lds_dwordx4 v217, s[38:39] offset:-3072\n" \
"v_mfma_f32_16x16x32_bf16 v[88:91], a[48:51], a[8:11], v[88:91]\n" \
"ds_read_b64_tr_b16 a[40:41], v215 offset:16384\n" \
"v_mfma_f32_16x16x32_bf16 v[88:91], a[52:55], a[12:15], v[88:91]\n" \
"buffer_store_dwordx4 v[24:27], v248, s[56:59], s71 offen offset:2048 sc1\n" \
"ds_read_b128 a[8:11], v213 offset:256\n" \
"Lpg12573_%=:\n" \
"s_cbranch_execz Lcb1_%=\n" \
"v_cvt_pk_bf16_f32 v28, v56, v57\n" \
"v_cvt_pk_bf16_f32 v29, v58, v59\n" \
"v_cvt_pk_bf16_f32 v30, v60, v61\n" \
"v_mfma_f32_16x16x32_bf16 v[104:107], a[48:51], a[16:19], v[104:107]\n" \
"ds_read_b128 a[12:15], v213 offset:2304\n" \
"v_cvt_pk_bf16_f32 v31, v62, v63\n" \
"v_mfma_f32_16x16x32_bf16 v[104:107], a[52:55], a[20:23], v[104:107]\n" \
"ds_read_b64_tr_b16 a[42:43], v215 offset:18432\n" \
"global_load_lds_dwordx4 v218, s[38:39] offset:-2048\n" \
"v_mfma_f32_16x16x32_bf16 v[108:111], a[56:59], a[16:19], v[108:111]\n" \
"ds_read_b64_tr_b16 a[44:45], v215 offset:24576\n" \
"v_mfma_f32_16x16x32_bf16 v[108:111], a[60:63], a[20:23], v[108:111]\n" \
"buffer_store_dwordx4 v[28:31], v248, s[56:59], s71 offen offset:3072 sc1\n" \
"ds_read_b128 a[16:19], v213 offset:4096\n" \
"v_mfma_f32_16x16x32_bf16 v[124:127], a[56:59], a[24:27], v[124:127]\n" \
"ds_read_b128 a[20:23], v213 offset:6144\n" \
"global_load_lds_dwordx4 v219, s[38:39] offset:-1024\n" \
"v_mfma_f32_16x16x32_bf16 v[124:127], a[60:63], a[28:31], v[124:127]\n" \
"s_add_u32 s38, s38, 0x20000\n" \
"s_addc_u32 s39, s39, 0\n" \
"ds_read_b64_tr_b16 a[46:47], v215 offset:26624\n" \
"v_mfma_f32_16x16x32_bf16 v[120:123], a[48:51], a[24:27], v[120:123]\n" \
"s_sub_u32 s75, s75, 32768\n" \
"s_cselect_b32 s75, 131072, s75\n" \
"v_mfma_f32_16x16x32_bf16 v[120:123], a[52:55], a[28:31], v[120:123]\n" \
"s_waitcnt lgkmcnt(3)\n" \
"v_mfma_f32_16x16x32_bf16 v[64:67], a[32:35], a[0:3], v[64:67]\n" \
"ds_read_b128 a[24:27], v213 offset:4352\n" \
"s_add_u32 m0, s75, s95\n" \
"v_mfma_f32_16x16x32_bf16 v[64:67], a[36:39], a[4:7], v[64:67]\n" \
"s_waitcnt lgkmcnt(1)\n" \
"ds_read_b128 a[28:31], v213 offset:6400\n" \
"global_load_lds_dwordx4 v222, s[36:37] offset:0 sc1\n" \
"v_mfma_f32_16x16x32_bf16 v[68:71], a[40:43], a[0:3], v[68:71]\n" \
"s_sub_u32 s74, s74, 32768\n" \
"s_cselect_b32 s74, 131072, s74\n" \
"ds_read_b64_tr_b16 a[48:49], v214 offset:16896\n" \
"v_mfma_f32_16x16x32_bf16 v[68:71], a[44:47], a[4:7], v[68:71]\n" \
"ds_read_b64_tr_b16 a[50:51], v214 offset:18944\n" \
"v_mfma_f32_16x16x32_bf16 v[84:87], a[40:43], a[8:11], v[84:87]\n" \
"ds_read_b64_tr_b16 a[52:53], v214 offset:25088\n" \
"v_mfma_f32_16x16x32_bf16 v[84:87], a[44:47], a[12:15], v[84:87]\n" \
"ds_read_b64_tr_b16 a[54:55], v214 offset:27136\n" \
"global_load_lds_dwordx4 v222, s[36:37] offset:1024 sc1\n" \
"v_mfma_f32_16x16x32_bf16 v[80:83], a[32:35], a[8:11], v[80:83]\n" \
"ds_read_b64_tr_b16 a[56:57], v215 offset:16896\n" \
"v_mfma_f32_16x16x32_bf16 v[80:83], a[36:39], a[12:15], v[80:83]\n" \
"ds_read_b64_tr_b16 a[58:59], v215 offset:18944\n" \
"v_mfma_f32_16x16x32_bf16 v[96:99], a[32:35], a[16:19], v[96:99]\n" \
"ds_read_b64_tr_b16 a[60:61], v215 offset:25088\n" \
"v_mfma_f32_16x16x32_bf16 v[96:99], a[36:39], a[20:23], v[96:99]\n" \
"ds_read_b64_tr_b16 a[62:63], v215 offset:27136\n" \
"global_load_lds_dwordx4 v222, s[36:37] offset:2048 sc1\n" \
"v_mfma_f32_16x16x32_bf16 v[100:103], a[40:43], a[16:19], v[100:103]\n" \
"v_add_u32 v213, s74, v211\n" \
"v_add_u32 v214, s74, v212\n" \
"v_mfma_f32_16x16x32_bf16 v[100:103], a[44:47], a[20:23], v[100:103]\n" \
"s_waitcnt lgkmcnt(3)\n" \
"v_xor_b32 v215, 32, v214\n" \
"v_mfma_f32_16x16x32_bf16 v[116:119], a[40:43], a[24:27], v[116:119]\n" \
"v_mfma_f32_16x16x32_bf16 v[116:119], a[44:47], a[28:31], v[116:119]\n" \
"s_waitcnt vmcnt(25) lgkmcnt(0)\n" \
"global_load_lds_dwordx4 v222, s[36:37] offset:3072 sc1\n" \
"v_mfma_f32_16x16x32_bf16 v[112:115], a[32:35], a[24:27], v[112:115]\n" \
"s_add_u32 s36, s36, 0x1000\n" \
"v_mfma_f32_16x16x32_bf16 v[112:115], a[36:39], a[28:31], v[112:115]\n" \
"s_barrier\n" \
"v_mfma_f32_16x16x32_bf16 v[72:75], a[48:51], a[0:3], v[72:75]\n" \
"s_add_u32 m0, m0, 0x5000\n" \
"ds_read_b64_tr_b16 a[32:33], v214 offset:16384\n" \
"v_mfma_f32_16x16x32_bf16 v[72:75], a[52:55], a[4:7], v[72:75]\n" \
"ds_read_b64_tr_b16 a[34:35], v214 offset:18432\n" \
"global_load_lds_dwordx4 v216, s[38:39] offset:-4096\n" \
"v_mfma_f32_16x16x32_bf16 v[76:79], a[56:59], a[0:3], v[76:79]\n" \
"ds_read_b64_tr_b16 a[36:37], v214 offset:24576\n" \
"v_mfma_f32_16x16x32_bf16 v[76:79], a[60:63], a[4:7], v[76:79]\n" \
"ds_read_b128 a[0:3], v213 offset:0\n" \
"v_mfma_f32_16x16x32_bf16 v[92:95], a[56:59], a[8:11], v[92:95]\n" \
"ds_read_b128 a[4:7], v213 offset:2048\n" \
"v_mfma_f32_16x16x32_bf16 v[92:95], a[60:63], a[12:15], v[92:95]\n" \
"ds_read_b64_tr_b16 a[38:39], v214 offset:26624\n" \
"global_load_lds_dwordx4 v217, s[38:39] offset:-3072\n" \
"v_mfma_f32_16x16x32_bf16 v[88:91], a[48:51], a[8:11], v[88:91]\n" \
"ds_read_b64_tr_b16 a[40:41], v215 offset:16384\n" \
"v_mfma_f32_16x16x32_bf16 v[88:91], a[52:55], a[12:15], v[88:91]\n" \
"ds_read_b128 a[8:11], v213 offset:256\n" \
"v_mfma_f32_16x16x32_bf16 v[104:107], a[48:51], a[16:19], v[104:107]\n" \
"ds_read_b128 a[12:15], v213 offset:2304\n" \
"v_mfma_f32_16x16x32_bf16 v[104:107], a[52:55], a[20:23], v[104:107]\n" \
"ds_read_b64_tr_b16 a[42:43], v215 offset:18432\n" \
"global_load_lds_dwordx4 v218, s[38:39] offset:-2048\n" \
"v_mfma_f32_16x16x32_bf16 v[108:111], a[56:59], a[16:19], v[108:111]\n" \
"ds_read_b64_tr_b16 a[44:45], v215 offset:24576\n" \
"v_mfma_f32_16x16x32_bf16 v[108:111], a[60:63], a[20:23], v[108:111]\n" \
"ds_read_b128 a[16:19], v213 offset:4096\n" \
"v_mfma_f32_16x16x32_bf16 v[124:127], a[56:59], a[24:27], v[124:127]\n" \
"ds_read_b128 a[20:23], v213 offset:6144\n" \
"global_load_lds_dwordx4 v219, s[38:39] offset:-1024\n" \
"v_mfma_f32_16x16x32_bf16 v[124:127], a[60:63], a[28:31], v[124:127]\n" \
"s_add_u32 s38, s38, 0x20000\n" \
"s_addc_u32 s39, s39, 0\n" \
"ds_read_b64_tr_b16 a[46:47], v215 offset:26624\n" \
"v_mfma_f32_16x16x32_bf16 v[120:123], a[48:51], a[24:27], v[120:123]\n" \
"s_sub_u32 s75, s75, 32768\n" \
"s_cselect_b32 s75, 131072, s75\n" \
"v_mfma_f32_16x16x32_bf16 v[120:123], a[52:55], a[28:31], v[120:123]\n" \
"s_waitcnt lgkmcnt(3)\n" \
"v_mfma_f32_16x16x32_bf16 v[64:67], a[32:35], a[0:3], v[64:67]\n" \
"ds_read_b128 a[24:27], v213 offset:4352\n" \
"s_add_u32 m0, s75, s95\n" \
"v_mfma_f32_16x16x32_bf16 v[64:67], a[36:39], a[4:7], v[64:67]\n" \
"s_waitcnt lgkmcnt(1)\n" \
"ds_read_b128 a[28:31], v213 offset:6400\n" \
"global_load_lds_dwordx4 v222, s[36:37] offset:0 sc1\n" \
"v_mfma_f32_16x16x32_bf16 v[68:71], a[40:43], a[0:3], v[68:71]\n" \
"s_sub_u32 s74, s74, 32768\n" \
"s_cselect_b32 s74, 131072, s74\n" \
"ds_read_b64_tr_b16 a[48:49], v214 offset:16896\n" \
"v_mfma_f32_16x16x32_bf16 v[68:71], a[44:47], a[4:7], v[68:71]\n" \
"ds_read_b64_tr_b16 a[50:51], v214 offset:18944\n" \
"v_mfma_f32_16x16x32_bf16 v[84:87], a[40:43], a[8:11], v[84:87]\n" \
"ds_read_b64_tr_b16 a[52:53], v214 offset:25088\n" \
"v_mfma_f32_16x16x32_bf16 v[84:87], a[44:47], a[12:15], v[84:87]\n" \
"ds_read_b64_tr_b16 a[54:55], v214 offset:27136\n" \
"global_load_lds_dwordx4 v222, s[36:37] offset:1024 sc1\n" \
"v_mfma_f32_16x16x32_bf16 v[80:83], a[32:35], a[8:11], v[80:83]\n" \
"ds_read_b64_tr_b16 a[56:57], v215 offset:16896\n" \
"v_mfma_f32_16x16x32_bf16 v[80:83], a[36:39], a[12:15], v[80:83]\n" \
"ds_read_b64_tr_b16 a[58:59], v215 offset:18944\n" \
"v_mfma_f32_16x16x32_bf16 v[96:99], a[32:35], a[16:19], v[96:99]\n" \
"ds_read_b64_tr_b16 a[60:61], v215 offset:25088\n" \
"v_mfma_f32_16x16x32_bf16 v[96:99], a[36:39], a[20:23], v[96:99]\n" \
"ds_read_b64_tr_b16 a[62:63], v215 offset:27136\n" \
"global_load_lds_dwordx4 v222, s[36:37] offset:2048 sc1\n" \
"v_mfma_f32_16x16x32_bf16 v[100:103], a[40:43], a[16:19], v[100:103]\n" \
"v_add_u32 v213, s74, v211\n" \
"v_add_u32 v214, s74, v212\n" \
"v_mfma_f32_16x16x32_bf16 v[100:103], a[44:47], a[20:23], v[100:103]\n" \
"s_waitcnt lgkmcnt(3)\n" \
"v_xor_b32 v215, 32, v214\n" \
"v_mfma_f32_16x16x32_bf16 v[116:119], a[40:43], a[24:27], v[116:119]\n" \
"v_mfma_f32_16x16x32_bf16 v[116:119], a[44:47], a[28:31], v[116:119]\n" \
"s_waitcnt vmcnt(22) lgkmcnt(0)\n" \
"global_load_lds_dwordx4 v222, s[36:37] offset:3072 sc1\n" \
"v_mfma_f32_16x16x32_bf16 v[112:115], a[32:35], a[24:27], v[112:115]\n" \
"s_add_u32 s36, s36, 0x1000\n" \
"v_mfma_f32_16x16x32_bf16 v[112:115], a[36:39], a[28:31], v[112:115]\n" \
"s_barrier\n" \
"v_mfma_f32_16x16x32_bf16 v[72:75], a[48:51], a[0:3], v[72:75]\n" \
"s_add_u32 m0, m0, 0x5000\n" \
"ds_read_b64_tr_b16 a[32:33], v214 offset:16384\n" \
"v_mfma_f32_16x16x32_bf16 v[72:75], a[52:55], a[4:7], v[72:75]\n" \
"ds_read_b64_tr_b16 a[34:35], v214 offset:18432\n" \
"global_load_lds_dwordx4 v216, s[38:39] offset:-4096\n" \
"v_mfma_f32_16x16x32_bf16 v[76:79], a[56:59], a[0:3], v[76:79]\n" \
"ds_read_b64_tr_b16 a[36:37], v214 offset:24576\n" \
"v_mfma_f32_16x16x32_bf16 v[76:79], a[60:63], a[4:7], v[76:79]\n" \
"ds_read_b128 a[0:3], v213 offset:0\n" \
"v_mfma_f32_16x16x32_bf16 v[92:95], a[56:59], a[8:11], v[92:95]\n" \
"ds_read_b128 a[4:7], v213 offset:2048\n" \
"v_mfma_f32_16x16x32_bf16 v[92:95], a[60:63], a[12:15], v[92:95]\n" \
"ds_read_b64_tr_b16 a[38:39], v214 offset:26624\n" \
"global_load_lds_dwordx4 v217, s[38:39] offset:-3072\n" \
"v_mfma_f32_16x16x32_bf16 v[88:91], a[48:51], a[8:11], v[88:91]\n" \
"ds_read_b64_tr_b16 a[40:41], v215 offset:16384\n" \
"v_mfma_f32_16x16x32_bf16 v[88:91], a[52:55], a[12:15], v[88:91]\n" \
"ds_read_b128 a[8:11], v213 offset:256\n" \
"v_mfma_f32_16x16x32_bf16 v[104:107], a[48:51], a[16:19], v[104:107]\n" \
"ds_read_b128 a[12:15], v213 offset:2304\n" \
"v_mfma_f32_16x16x32_bf16 v[104:107], a[52:55], a[20:23], v[104:107]\n" \
"ds_read_b64_tr_b16 a[42:43], v215 offset:18432\n" \
"global_load_lds_dwordx4 v218, s[38:39] offset:-2048\n" \
"v_mfma_f32_16x16x32_bf16 v[108:111], a[56:59], a[16:19], v[108:111]\n" \
"ds_read_b64_tr_b16 a[44:45], v215 offset:24576\n" \
"v_mfma_f32_16x16x32_bf16 v[108:111], a[60:63], a[20:23], v[108:111]\n" \
"ds_read_b128 a[16:19], v213 offset:4096\n" \
"v_mfma_f32_16x16x32_bf16 v[124:127], a[56:59], a[24:27], v[124:127]\n" \
"ds_read_b128 a[20:23], v213 offset:6144\n" \
"global_load_lds_dwordx4 v219, s[38:39] offset:-1024\n" \
"v_mfma_f32_16x16x32_bf16 v[124:127], a[60:63], a[28:31], v[124:127]\n" \
"s_add_u32 s38, s38, 0x20000\n" \
"s_addc_u32 s39, s39, 0\n" \
"ds_read_b64_tr_b16 a[46:47], v215 offset:26624\n" \
"v_mfma_f32_16x16x32_bf16 v[120:123], a[48:51], a[24:27], v[120:123]\n" \
"s_sub_u32 s75, s75, 32768\n" \
"s_cselect_b32 s75, 131072, s75\n" \
"v_mfma_f32_16x16x32_bf16 v[120:123], a[52:55], a[28:31], v[120:123]\n" \
"s_waitcnt lgkmcnt(3)\n" \
"v_mfma_f32_16x16x32_bf16 v[64:67], a[32:35], a[0:3], v[64:67]\n" \
"ds_read_b128 a[24:27], v213 offset:4352\n" \
"s_add_u32 m0, s75, s95\n" \
"v_mfma_f32_16x16x32_bf16 v[64:67], a[36:39], a[4:7], v[64:67]\n" \
"s_waitcnt lgkmcnt(1)\n" \
"ds_read_b128 a[28:31], v213 offset:6400\n" \
"global_load_lds_dwordx4 v222, s[36:37] offset:0 sc1\n" \
"v_mfma_f32_16x16x32_bf16 v[68:71], a[40:43], a[0:3], v[68:71]\n" \
"s_sub_u32 s74, s74, 32768\n" \
"s_cselect_b32 s74, 131072, s74\n" \
"ds_read_b64_tr_b16 a[48:49], v214 offset:16896\n" \
"v_mfma_f32_16x16x32_bf16 v[68:71], a[44:47], a[4:7], v[68:71]\n" \
"ds_read_b64_tr_b16 a[50:51], v214 offset:18944\n" \
"v_mfma_f32_16x16x32_bf16 v[84:87], a[40:43], a[8:11], v[84:87]\n" \
"ds_read_b64_tr_b16 a[52:53], v214 offset:25088\n" \
"v_mfma_f32_16x16x32_bf16 v[84:87], a[44:47], a[12:15], v[84:87]\n" \
"ds_read_b64_tr_b16 a[54:55], v214 offset:27136\n" \
"global_load_lds_dwordx4 v222, s[36:37] offset:1024 sc1\n" \
"v_mfma_f32_16x16x32_bf16 v[80:83], a[32:35], a[8:11], v[80:83]\n" \
"ds_read_b64_tr_b16 a[56:57], v215 offset:16896\n" \
"v_mfma_f32_16x16x32_bf16 v[80:83], a[36:39], a[12:15], v[80:83]\n" \
"ds_read_b64_tr_b16 a[58:59], v215 offset:18944\n" \
"v_mfma_f32_16x16x32_bf16 v[96:99], a[32:35], a[16:19], v[96:99]\n" \
"ds_read_b64_tr_b16 a[60:61], v215 offset:25088\n" \
"v_mfma_f32_16x16x32_bf16 v[96:99], a[36:39], a[20:23], v[96:99]\n" \
"ds_read_b64_tr_b16 a[62:63], v215 offset:27136\n" \
"global_load_lds_dwordx4 v222, s[36:37] offset:2048 sc1\n" \
"v_mfma_f32_16x16x32_bf16 v[100:103], a[40:43], a[16:19], v[100:103]\n" \
"v_add_u32 v213, s74, v211\n" \
"v_add_u32 v214, s74, v212\n" \
"v_mfma_f32_16x16x32_bf16 v[100:103], a[44:47], a[20:23], v[100:103]\n" \
"s_waitcnt lgkmcnt(3)\n" \
"v_xor_b32 v215, 32, v214\n" \
"v_mfma_f32_16x16x32_bf16 v[116:119], a[40:43], a[24:27], v[116:119]\n" \
"v_mfma_f32_16x16x32_bf16 v[116:119], a[44:47], a[28:31], v[116:119]\n" \
"s_waitcnt vmcnt(19) lgkmcnt(0)\n" \
"global_load_lds_dwordx4 v222, s[36:37] offset:3072 sc1\n" \
"v_mfma_f32_16x16x32_bf16 v[112:115], a[32:35], a[24:27], v[112:115]\n" \
"s_add_u32 s36, s36, 0x1000\n" \
"v_mfma_f32_16x16x32_bf16 v[112:115], a[36:39], a[28:31], v[112:115]\n" \
"s_barrier\n" \
"v_mfma_f32_16x16x32_bf16 v[72:75], a[48:51], a[0:3], v[72:75]\n" \
"s_add_u32 m0, m0, 0x5000\n" \
"ds_read_b64_tr_b16 a[32:33], v214 offset:16384\n" \
"v_mfma_f32_16x16x32_bf16 v[72:75], a[52:55], a[4:7], v[72:75]\n" \
"ds_read_b64_tr_b16 a[34:35], v214 offset:18432\n" \
"global_load_lds_dwordx4 v216, s[38:39] offset:-4096\n" \
"v_mfma_f32_16x16x32_bf16 v[76:79], a[56:59], a[0:3], v[76:79]\n" \
"ds_read_b64_tr_b16 a[36:37], v214 offset:24576\n" \
"v_mfma_f32_16x16x32_bf16 v[76:79], a[60:63], a[4:7], v[76:79]\n" \
"buffer_store_dword v224, v225, s[56:59], s79 offen sc1\n" \
"ds_read_b128 a[0:3], v213 offset:0\n" \
"v_mfma_f32_16x16x32_bf16 v[92:95], a[56:59], a[8:11], v[92:95]\n" \
"ds_read_b128 a[4:7], v213 offset:2048\n" \
"v_mfma_f32_16x16x32_bf16 v[92:95], a[60:63], a[12:15], v[92:95]\n" \
"ds_read_b64_tr_b16 a[38:39], v214 offset:26624\n" \
"global_load_lds_dwordx4 v217, s[38:39] offset:-3072\n" \
"v_mfma_f32_16x16x32_bf16 v[88:91], a[48:51], a[8:11], v[88:91]\n" \
"ds_read_b64_tr_b16 a[40:41], v215 offset:16384\n" \
"v_mfma_f32_16x16x32_bf16 v[88:91], a[52:55], a[12:15], v[88:91]\n" \
"ds_read_b128 a[8:11], v213 offset:256\n" \
"v_mfma_f32_16x16x32_bf16 v[104:107], a[48:51], a[16:19], v[104:107]\n" \
"ds_read_b128 a[12:15], v213 offset:2304\n" \
"v_mfma_f32_16x16x32_bf16 v[104:107], a[52:55], a[20:23], v[104:107]\n" \
"ds_read_b64_tr_b16 a[42:43], v215 offset:18432\n" \
"global_load_lds_dwordx4 v218, s[38:39] offset:-2048\n" \
"v_mfma_f32_16x16x32_bf16 v[108:111], a[56:59], a[16:19], v[108:111]\n" \
"ds_read_b64_tr_b16 a[44:45], v215 offset:24576\n" \
"v_mfma_f32_16x16x32_bf16 v[108:111], a[60:63], a[20:23], v[108:111]\n" \
"ds_read_b128 a[16:19], v213 offset:4096\n" \
"v_mfma_f32_16x16x32_bf16 v[124:127], a[56:59], a[24:27], v[124:127]\n" \
"ds_read_b128 a[20:23], v213 offset:6144\n" \
"global_load_lds_dwordx4 v219, s[38:39] offset:-1024\n" \
"v_mfma_f32_16x16x32_bf16 v[124:127], a[60:63], a[28:31], v[124:127]\n" \
"s_add_u32 s38, s38, 0x20000\n" \
"s_addc_u32 s39, s39, 0\n" \
"ds_read_b64_tr_b16 a[46:47], v215 offset:26624\n" \
"v_mfma_f32_16x16x32_bf16 v[120:123], a[48:51], a[24:27], v[120:123]\n" \
"s_sub_u32 s75, s75, 32768\n" \
"s_cselect_b32 s75, 131072, s75\n" \
"v_mfma_f32_16x16x32_bf16 v[120:123], a[52:55], a[28:31], v[120:123]\n" \
"s_waitcnt lgkmcnt(3)\n" \
"s_mov_b32 s72, 6\n" \
"s_call_b64 s[68:69], Lo64_%=\n" \
"v_mfma_f32_16x16x32_bf16 v[64:67], a[32:35], a[0:3], v[64:67]\n" \
"ds_read_b128 a[24:27], v213 offset:4352\n" \
"v_mfma_f32_16x16x32_bf16 v[64:67], a[36:39], a[4:7], v[64:67]\n" \
"s_waitcnt lgkmcnt(1)\n" \
"ds_read_b128 a[28:31], v213 offset:6400\n" \
"v_mfma_f32_16x16x32_bf16 v[68:71], a[40:43], a[0:3], v[68:71]\n" \
"s_sub_u32 s74, s74, 32768\n" \
"s_cselect_b32 s74, 131072, s74\n" \
"ds_read_b64_tr_b16 a[48:49], v214 offset:16896\n" \
"v_mfma_f32_16x16x32_bf16 v[68:71], a[44:47], a[4:7], v[68:71]\n" \
"ds_read_b64_tr_b16 a[50:51], v214 offset:18944\n" \
"v_mfma_f32_16x16x32_bf16 v[84:87], a[40:43], a[8:11], v[84:87]\n" \
"ds_read_b64_tr_b16 a[52:53], v214 offset:25088\n" \
"v_mfma_f32_16x16x32_bf16 v[84:87], a[44:47], a[12:15], v[84:87]\n" \
"ds_read_b64_tr_b16 a[54:55], v214 offset:27136\n" \
"v_mfma_f32_16x16x32_bf16 v[80:83], a[32:35], a[8:11], v[80:83]\n" \
"ds_read_b64_tr_b16 a[56:57], v215 offset:16896\n" \
"v_mfma_f32_16x16x32_bf16 v[80:83], a[36:39], a[12:15], v[80:83]\n" \
"ds_read_b64_tr_b16 a[58:59], v215 offset:18944\n" \
"v_mfma_f32_16x16x32_bf16 v[96:99], a[32:35], a[16:19], v[96:99]\n" \
"ds_read_b64_tr_b16 a[60:61], v215 offset:25088\n" \
"v_mfma_f32_16x16x32_bf16 v[96:99], a[36:39], a[20:23], v[96:99]\n" \
"ds_read_b64_tr_b16 a[62:63], v215 offset:27136\n" \
"v_mfma_f32_16x16x32_bf16 v[100:103], a[40:43], a[16:19], v[100:103]\n" \
"v_add_u32 v213, s74, v211\n" \
"v_add_u32 v214, s74, v212\n" \
"v_mfma_f32_16x16x32_bf16 v[100:103], a[44:47], a[20:23], v[100:103]\n" \
"s_waitcnt lgkmcnt(3)\n" \
"v_xor_b32 v215, 32, v214\n" \
"v_mfma_f32_16x16x32_bf16 v[116:119], a[40:43], a[24:27], v[116:119]\n" \
"v_mfma_f32_16x16x32_bf16 v[116:119], a[44:47], a[28:31], v[116:119]\n" \
"s_waitcnt vmcnt(16) lgkmcnt(0)\n" \
"v_mfma_f32_16x16x32_bf16 v[112:115], a[32:35], a[24:27], v[112:115]\n" \
"v_mfma_f32_16x16x32_bf16 v[112:115], a[36:39], a[28:31], v[112:115]\n" \
"s_barrier\n" \
"v_mfma_f32_16x16x32_bf16 v[72:75], a[48:51], a[0:3], v[72:75]\n" \
"s_lshl_b32 s83, s95, 1\n" \
"s_add_u32 s83, s83, s74\n" \
"ds_read_b64_tr_b16 a[32:33], v214 offset:16384\n" \
"v_mfma_f32_16x16x32_bf16 v[72:75], a[52:55], a[4:7], v[72:75]\n" \
"ds_read_b64_tr_b16 a[34:35], v214 offset:18432\n" \
"v_mfma_f32_16x16x32_bf16 v[76:79], a[56:59], a[0:3], v[76:79]\n" \
"ds_read_b64_tr_b16 a[36:37], v214 offset:24576\n" \
"v_mfma_f32_16x16x32_bf16 v[76:79], a[60:63], a[4:7], v[76:79]\n" \
"ds_read_b128 a[0:3], v213 offset:0\n" \
"v_mfma_f32_16x16x32_bf16 v[92:95], a[56:59], a[8:11], v[92:95]\n" \
"ds_read_b128 a[4:7], v213 offset:2048\n" \
"v_mfma_f32_16x16x32_bf16 v[92:95], a[60:63], a[12:15], v[92:95]\n" \
"ds_read_b64_tr_b16 a[38:39], v214 offset:26624\n" \
"v_mfma_f32_16x16x32_bf16 v[88:91], a[48:51], a[8:11], v[88:91]\n" \
"ds_read_b64_tr_b16 a[40:41], v215 offset:16384\n" \
"v_mfma_f32_16x16x32_bf16 v[88:91], a[52:55], a[12:15], v[88:91]\n" \
"ds_read_b128 a[8:11], v213 offset:256\n" \
"v_mfma_f32_16x16x32_bf16 v[104:107], a[48:51], a[16:19], v[104:107]\n" \
"ds_read_b128 a[12:15], v213 offset:2304\n" \
"v_mfma_f32_16x16x32_bf16 v[104:107], a[52:55], a[20:23], v[104:107]\n" \
"ds_read_b64_tr_b16 a[42:43], v215 offset:18432\n" \
"v_mfma_f32_16x16x32_bf16 v[108:111], a[56:59], a[16:19], v[108:111]\n" \
"ds_read_b64_tr_b16 a[44:45], v215 offset:24576\n" \
"v_mfma_f32_16x16x32_bf16 v[108:111], a[60:63], a[20:23], v[108:111]\n" \
"ds_read_b128 a[16:19], v213 offset:4096\n" \
"v_mfma_f32_16x16x32_bf16 v[124:127], a[56:59], a[24:27], v[124:127]\n" \
"ds_read_b128 a[20:23], v213 offset:6144\n" \
"v_mfma_f32_16x16x32_bf16 v[124:127], a[60:63], a[28:31], v[124:127]\n" \
"ds_read_b64_tr_b16 a[46:47], v215 offset:26624\n" \
"v_mfma_f32_16x16x32_bf16 v[120:123], a[48:51], a[24:27], v[120:123]\n" \
"v_mfma_f32_16x16x32_bf16 v[120:123], a[52:55], a[28:31], v[120:123]\n" \
"s_waitcnt lgkmcnt(3)\n" \
"v_mfma_f32_16x16x32_bf16 v[64:67], a[32:35], a[0:3], v[64:67]\n" \
"ds_read_b128 a[24:27], v213 offset:4352\n" \
"v_mfma_f32_16x16x32_bf16 v[64:67], a[36:39], a[4:7], v[64:67]\n" \
"s_waitcnt lgkmcnt(1)\n" \
"ds_read_b128 a[28:31], v213 offset:6400\n" \
"v_lshrrev_b32 v132, 4, v248\n" \
"v_mfma_f32_16x16x32_bf16 v[68:71], a[40:43], a[0:3], v[68:71]\n" \
"s_sub_u32 s74, s74, 32768\n" \
"s_cselect_b32 s74, 131072, s74\n" \
"ds_read_b64_tr_b16 a[48:49], v214 offset:16896\n" \
"v_mfma_f32_16x16x32_bf16 v[68:71], a[44:47], a[4:7], v[68:71]\n" \
"ds_read_b64_tr_b16 a[50:51], v214 offset:18944\n" \
"v_and_b32 v133, 15, v132\n" \
"v_mfma_f32_16x16x32_bf16 v[84:87], a[40:43], a[8:11], v[84:87]\n" \
"ds_read_b64_tr_b16 a[52:53], v214 offset:25088\n" \
"v_mul_u32_u24 v128, 144, v133\n" \
"v_mfma_f32_16x16x32_bf16 v[84:87], a[44:47], a[12:15], v[84:87]\n" \
"ds_read_b64_tr_b16 a[54:55], v214 offset:27136\n" \
"v_lshrrev_b32 v133, 4, v132\n" \
"v_mfma_f32_16x16x32_bf16 v[80:83], a[32:35], a[8:11], v[80:83]\n" \
"ds_read_b64_tr_b16 a[56:57], v215 offset:16896\n" \
"v_lshl_add_u32 v128, v133, 3, v128\n" \
"v_mfma_f32_16x16x32_bf16 v[80:83], a[36:39], a[12:15], v[80:83]\n" \
"ds_read_b64_tr_b16 a[58:59], v215 offset:18944\n" \
"v_add_u32 v128, s83, v128\n" \
"v_mfma_f32_16x16x32_bf16 v[96:99], a[32:35], a[16:19], v[96:99]\n" \
"ds_read_b64_tr_b16 a[60:61], v215 offset:25088\n" \
"v_lshrrev_b32 v133, 3, v132\n" \
"v_mfma_f32_16x16x32_bf16 v[96:99], a[36:39], a[20:23], v[96:99]\n" \
"ds_read_b64_tr_b16 a[62:63], v215 offset:27136\n" \
"v_and_b32 v134, 7, v132\n" \
"v_mfma_f32_16x16x32_bf16 v[100:103], a[40:43], a[16:19], v[100:103]\n" \
"v_add_u32 v213, s74, v211\n" \
"v_add_u32 v214, s74, v212\n" \
"v_mfma_f32_16x16x32_bf16 v[100:103], a[44:47], a[20:23], v[100:103]\n" \
"s_waitcnt lgkmcnt(3)\n" \
"v_xor_b32 v215, 32, v214\n" \
"v_mul_u32_u24 v129, 144, v133\n" \
"v_mfma_f32_16x16x32_bf16 v[116:119], a[40:43], a[24:27], v[116:119]\n" \
"v_lshl_add_u32 v129, v134, 4, v129\n" \
"v_add_u32 v129, s83, v129\n" \
"v_mfma_f32_16x16x32_bf16 v[116:119], a[44:47], a[28:31], v[116:119]\n" \
"s_waitcnt vmcnt(0) lgkmcnt(0)\n" \
"v_lshlrev_b32 v134, 4, v134\n" \
"v_lshl_add_u32 v130, v133, 13, v134\n" \
"v_mfma_f32_16x16x32_bf16 v[112:115], a[32:35], a[24:27], v[112:115]\n" \
"v_mfma_f32_16x16x32_bf16 v[112:115], a[36:39], a[28:31], v[112:115]\n" \
"s_barrier\n" \
"v_mfma_f32_16x16x32_bf16 v[72:75], a[48:51], a[0:3], v[72:75]\n" \
"ds_read_b128 a[192:195], v213 offset:0\n" \
"ds_read_b64_tr_b16 a[128:129], v214 offset:16384\n" \
"v_mfma_f32_16x16x32_bf16 v[72:75], a[52:55], a[4:7], v[72:75]\n" \
"s_sub_u32 s74, s74, 32768\n" \
"s_cselect_b32 s74, 131072, s74\n" \
"ds_read_b64_tr_b16 a[130:131], v214 offset:18432\n" \
"v_mfma_f32_16x16x32_bf16 v[76:79], a[56:59], a[0:3], v[76:79]\n" \
"ds_read_b128 a[196:199], v213 offset:2048\n" \
"ds_read_b64_tr_b16 a[132:133], v214 offset:24576\n" \
"v_mfma_f32_16x16x32_bf16 v[76:79], a[60:63], a[4:7], v[76:79]\n" \
"ds_read_b64_tr_b16 a[134:135], v214 offset:26624\n" \
"v_add_u32 v226, s74, v211\n" \
"v_mfma_f32_16x16x32_bf16 v[92:95], a[56:59], a[8:11], v[92:95]\n" \
"ds_read_b128 a[200:203], v213 offset:256\n" \
"ds_read_b64_tr_b16 a[136:137], v215 offset:16384\n" \
"v_mfma_f32_16x16x32_bf16 v[92:95], a[60:63], a[12:15], v[92:95]\n" \
"ds_read_b64_tr_b16 a[138:139], v215 offset:18432\n" \
"v_add_u32 v227, s74, v212\n" \
"v_mfma_f32_16x16x32_bf16 v[88:91], a[48:51], a[8:11], v[88:91]\n" \
"ds_read_b128 a[204:207], v213 offset:2304\n" \
"ds_read_b64_tr_b16 a[140:141], v215 offset:24576\n" \
"v_mfma_f32_16x16x32_bf16 v[88:91], a[52:55], a[12:15], v[88:91]\n" \
"ds_read_b64_tr_b16 a[142:143], v215 offset:26624\n" \
"v_xor_b32 v228, 32, v227\n" \
"v_mfma_f32_16x16x32_bf16 v[104:107], a[48:51], a[16:19], v[104:107]\n" \
"ds_read_b64_tr_b16 a[144:145], v214 offset:16896\n" \
"v_mfma_f32_16x16x32_bf16 v[104:107], a[52:55], a[20:23], v[104:107]\n" \
"ds_read_b64_tr_b16 a[146:147], v214 offset:18944\n" \
"v_mfma_f32_16x16x32_bf16 v[108:111], a[56:59], a[16:19], v[108:111]\n" \
"ds_read_b64_tr_b16 a[148:149], v214 offset:25088\n" \
"v_mfma_f32_16x16x32_bf16 v[108:111], a[60:63], a[20:23], v[108:111]\n" \
"ds_read_b64_tr_b16 a[150:151], v214 offset:27136\n" \
"v_mfma_f32_16x16x32_bf16 v[124:127], a[56:59], a[24:27], v[124:127]\n" \
"ds_read_b64_tr_b16 a[152:153], v215 offset:16896\n" \
"v_mfma_f32_16x16x32_bf16 v[124:127], a[60:63], a[28:31], v[124:127]\n" \
"ds_read_b64_tr_b16 a[154:155], v215 offset:18944\n" \
"v_mfma_f32_16x16x32_bf16 v[120:123], a[48:51], a[24:27], v[120:123]\n" \
"ds_read_b64_tr_b16 a[156:157], v215 offset:25088\n" \
"v_mfma_f32_16x16x32_bf16 v[120:123], a[52:55], a[28:31], v[120:123]\n" \
"s_waitcnt lgkmcnt(4)\n" \
"ds_read_b64_tr_b16 a[158:159], v215 offset:27136\n" \
"v_mfma_f32_16x16x32_bf16 v[64:67], a[128:131], a[192:195], v[64:67]\n" \
"ds_read_b128 a[224:227], v226 offset:0\n" \
"ds_read_b64_tr_b16 a[160:161], v227 offset:16384\n" \
"v_mfma_f32_16x16x32_bf16 v[64:67], a[132:135], a[196:199], v[64:67]\n" \
"ds_read_b64_tr_b16 a[162:163], v227 offset:18432\n" \
"v_mfma_f32_16x16x32_bf16 v[80:83], a[128:131], a[200:203], v[80:83]\n" \
"ds_read_b64_tr_b16 a[164:165], v227 offset:24576\n" \
"v_mfma_f32_16x16x32_bf16 v[80:83], a[132:135], a[204:207], v[80:83]\n" \
"ds_read_b128 a[228:231], v226 offset:2048\n" \
"ds_read_b64_tr_b16 a[166:167], v227 offset:26624\n" \
"v_mfma_f32_16x16x32_bf16 v[84:87], a[136:139], a[200:203], v[84:87]\n" \
"ds_read_b64_tr_b16 a[168:169], v228 offset:16384\n" \
"v_mfma_f32_16x16x32_bf16 v[84:87], a[140:143], a[204:207], v[84:87]\n" \
"ds_read_b64_tr_b16 a[170:171], v228 offset:18432\n" \
"v_mfma_f32_16x16x32_bf16 v[68:71], a[136:139], a[192:195], v[68:71]\n" \
"ds_read_b128 a[232:235], v226 offset:256\n" \
"ds_read_b64_tr_b16 a[172:173], v228 offset:24576\n" \
"v_mfma_f32_16x16x32_bf16 v[68:71], a[140:143], a[196:199], v[68:71]\n" \
"s_waitcnt lgkmcnt(6)\n" \
"ds_read_b64_tr_b16 a[174:175], v228 offset:26624\n" \
"v_mfma_f32_16x16x32_bf16 v[72:75], a[144:147], a[192:195], v[72:75]\n" \
"ds_read_b64_tr_b16 a[176:177], v227 offset:16896\n" \
"v_mfma_f32_16x16x32_bf16 v[72:75], a[148:151], a[196:199], v[72:75]\n" \
"ds_read_b128 a[236:239], v226 offset:2304\n" \
"ds_read_b64_tr_b16 a[178:179], v227 offset:18944\n" \
"v_mfma_f32_16x16x32_bf16 v[88:91], a[144:147], a[200:203], v[88:91]\n" \
"ds_read_b64_tr_b16 a[180:181], v227 offset:25088\n" \
"v_mfma_f32_16x16x32_bf16 v[88:91], a[148:151], a[204:207], v[88:91]\n" \
"ds_read_b64_tr_b16 a[182:183], v227 offset:27136\n" \
"v_mfma_f32_16x16x32_bf16 v[92:95], a[152:155], a[200:203], v[92:95]\n" \
"ds_read_b64_tr_b16 a[184:185], v228 offset:16896\n" \
"v_mfma_f32_16x16x32_bf16 v[92:95], a[156:159], a[204:207], v[92:95]\n" \
"ds_read_b64_tr_b16 a[186:187], v228 offset:18944\n" \
"v_mfma_f32_16x16x32_bf16 v[76:79], a[152:155], a[192:195], v[76:79]\n" \
"ds_read_b64_tr_b16 a[188:189], v228 offset:25088\n" \
"v_mfma_f32_16x16x32_bf16 v[76:79], a[156:159], a[196:199], v[76:79]\n" \
"s_waitcnt lgkmcnt(4)\n" \
"ds_read_b64_tr_b16 a[190:191], v228 offset:27136\n" \
"v_mfma_f32_16x16x32_bf16 v[64:67], a[160:163], a[224:227], v[64:67]\n" \
"ds_read_b128 a[208:211], v213 offset:4096\n" \
"v_mfma_f32_16x16x32_bf16 v[64:67], a[164:167], a[228:231], v[64:67]\n" \
"ds_read_b128 a[212:215], v213 offset:6144\n" \
"v_mfma_f32_16x16x32_bf16 v[80:83], a[160:163], a[232:235], v[80:83]\n" \
"ds_read_b128 a[240:243], v226 offset:4096\n" \
"v_mfma_f32_16x16x32_bf16 v[80:83], a[164:167], a[236:239], v[80:83]\n" \
"ds_read_b128 a[244:247], v226 offset:6144\n" \
"v_mfma_f32_16x16x32_bf16 v[84:87], a[168:171], a[232:235], v[84:87]\n" \
"ds_read_b128 a[216:219], v213 offset:4352\n" \
"v_cvt_pk_bf16_f32 v64, v64, v65\n" \
"v_mfma_f32_16x16x32_bf16 v[84:87], a[172:175], a[236:239], v[84:87]\n" \
"ds_read_b128 a[220:223], v213 offset:6400\n" \
"v_cvt_pk_bf16_f32 v65, v66, v67\n" \
"v_mfma_f32_16x16x32_bf16 v[68:71], a[168:171], a[224:227], v[68:71]\n" \
"ds_read_b128 a[248:251], v226 offset:4352\n" \
"v_mfma_f32_16x16x32_bf16 v[68:71], a[172:175], a[228:231], v[68:71]\n" \
"s_waitcnt lgkmcnt(4)\n" \
"ds_read_b128 a[252:255], v226 offset:6400\n" \
"v_mfma_f32_16x16x32_bf16 v[72:75], a[176:179], a[224:227], v[72:75]\n" \
"ds_write_b64 v128, v[64:65] offset:0\n" \
"v_mfma_f32_16x16x32_bf16 v[72:75], a[180:183], a[228:231], v[72:75]\n" \
"v_cvt_pk_bf16_f32 v80, v80, v81\n" \
"v_cvt_pk_bf16_f32 v81, v82, v83\n" \
"v_mfma_f32_16x16x32_bf16 v[88:91], a[176:179], a[232:235], v[88:91]\n" \
"ds_write_b64 v128, v[80:81] offset:2304\n" \
"v_mfma_f32_16x16x32_bf16 v[88:91], a[180:183], a[236:239], v[88:91]\n" \
"v_cvt_pk_bf16_f32 v84, v84, v85\n" \
"v_cvt_pk_bf16_f32 v85, v86, v87\n" \
"v_mfma_f32_16x16x32_bf16 v[92:95], a[184:187], a[232:235], v[92:95]\n" \
"ds_write_b64 v128, v[84:85] offset:2336\n" \
"v_mfma_f32_16x16x32_bf16 v[92:95], a[188:191], a[236:239], v[92:95]\n" \
"v_cvt_pk_bf16_f32 v68, v68, v69\n" \
"v_cvt_pk_bf16_f32 v69, v70, v71\n" \
"v_mfma_f32_16x16x32_bf16 v[76:79], a[184:187], a[224:227], v[76:79]\n" \
"ds_write_b64 v128, v[68:69] offset:32\n" \
"v_mfma_f32_16x16x32_bf16 v[76:79], a[188:191], a[228:231], v[76:79]\n" \
"v_cvt_pk_bf16_f32 v72, v72, v73\n" \
"v_cvt_pk_bf16_f32 v73, v74, v75\n" \
"v_mfma_f32_16x16x32_bf16 v[96:99], a[128:131], a[208:211], v[96:99]\n" \
"ds_write_b64 v128, v[72:73] offset:64\n" \
"v_mfma_f32_16x16x32_bf16 v[96:99], a[132:135], a[212:215], v[96:99]\n" \
"s_waitcnt lgkmcnt(2)\n" \
"v_cvt_pk_bf16_f32 v88, v88, v89\n" \
"v_cvt_pk_bf16_f32 v89, v90, v91\n" \
"v_mfma_f32_16x16x32_bf16 v[96:99], a[160:163], a[240:243], v[96:99]\n" \
"ds_write_b64 v128, v[88:89] offset:2368\n" \
"v_mfma_f32_16x16x32_bf16 v[96:99], a[164:167], a[244:247], v[96:99]\n" \
"v_cvt_pk_bf16_f32 v92, v92, v93\n" \
"v_cvt_pk_bf16_f32 v93, v94, v95\n" \
"v_mfma_f32_16x16x32_bf16 v[100:103], a[168:171], a[240:243], v[100:103]\n" \
"ds_write_b64 v128, v[92:93] offset:2400\n" \
"v_mfma_f32_16x16x32_bf16 v[100:103], a[172:175], a[244:247], v[100:103]\n" \
"v_cvt_pk_bf16_f32 v76, v76, v77\n" \
"v_cvt_pk_bf16_f32 v77, v78, v79\n" \
"v_mfma_f32_16x16x32_bf16 v[100:103], a[136:139], a[208:211], v[100:103]\n" \
"ds_write_b64 v128, v[76:77] offset:96\n" \
"v_mfma_f32_16x16x32_bf16 v[100:103], a[140:143], a[212:215], v[100:103]\n" \
"ds_read_b128 v[140:143], v129 offset:0\n" \
"v_mfma_f32_16x16x32_bf16 v[104:107], a[144:147], a[208:211], v[104:107]\n" \
"ds_read_b128 v[144:147], v129 offset:1152\n" \
"v_mfma_f32_16x16x32_bf16 v[104:107], a[148:151], a[212:215], v[104:107]\n" \
"ds_read_b128 v[148:151], v129 offset:2304\n" \
"v_mfma_f32_16x16x32_bf16 v[104:107], a[176:179], a[240:243], v[104:107]\n" \
"ds_read_b128 v[152:155], v129 offset:3456\n" \
"v_mfma_f32_16x16x32_bf16 v[104:107], a[180:183], a[244:247], v[104:107]\n" \
"v_cvt_pk_bf16_f32 v96, v96, v97\n" \
"v_cvt_pk_bf16_f32 v97, v98, v99\n" \
"v_mfma_f32_16x16x32_bf16 v[108:111], a[184:187], a[240:243], v[108:111]\n" \
"ds_write_b64 v128, v[96:97] offset:0\n" \
"v_mfma_f32_16x16x32_bf16 v[108:111], a[188:191], a[244:247], v[108:111]\n" \
"v_cvt_pk_bf16_f32 v100, v100, v101\n" \
"v_cvt_pk_bf16_f32 v101, v102, v103\n" \
"v_mfma_f32_16x16x32_bf16 v[108:111], a[152:155], a[208:211], v[108:111]\n" \
"s_add_u32 s70, s77, 0\n" \
"ds_write_b64 v128, v[100:101] offset:32\n" \
"v_mfma_f32_16x16x32_bf16 v[108:111], a[156:159], a[212:215], v[108:111]\n" \
"s_waitcnt lgkmcnt(5)\n" \
"Lpg13120_%=:\n" \
"s_cbranch_execz Lcb1_%=\n" \
"v_cvt_pk_bf16_f32 v104, v104, v105\n" \
"v_cvt_pk_bf16_f32 v105, v106, v107\n" \
"v_mfma_f32_16x16x32_bf16 v[112:115], a[128:131], a[216:219], v[112:115]\n" \
"buffer_store_dwordx4 v[140:143], v130, s[52:55], s70 offen sc1\n" \
"ds_write_b64 v128, v[104:105] offset:64\n" \
"v_mfma_f32_16x16x32_bf16 v[112:115], a[132:135], a[220:223], v[112:115]\n" \
"v_mfma_f32_16x16x32_bf16 v[112:115], a[160:163], a[248:251], v[112:115]\n" \
"v_cvt_pk_bf16_f32 v108, v108, v109\n" \
"v_cvt_pk_bf16_f32 v109, v110, v111\n" \
"v_mfma_f32_16x16x32_bf16 v[112:115], a[164:167], a[252:255], v[112:115]\n" \
"ds_write_b64 v128, v[108:109] offset:96\n" \
"v_mfma_f32_16x16x32_bf16 v[116:119], a[168:171], a[248:251], v[116:119]\n" \
"s_add_u32 s70, s77, 65536\n" \
"ds_read_b128 v[156:159], v129 offset:0\n" \
"s_waitcnt lgkmcnt(7)\n" \
"v_mfma_f32_16x16x32_bf16 v[116:119], a[172:175], a[252:255], v[116:119]\n" \
"buffer_store_dwordx4 v[144:147], v130, s[52:55], s70 offen sc1\n" \
"ds_read_b128 v[160:163], v129 offset:1152\n" \
"v_mfma_f32_16x16x32_bf16 v[116:119], a[136:139], a[216:219], v[116:119]\n" \
"v_cvt_pk_bf16_f32 v112, v112, v113\n" \
"v_cvt_pk_bf16_f32 v113, v114, v115\n" \
"v_mfma_f32_16x16x32_bf16 v[116:119], a[140:143], a[220:223], v[116:119]\n" \
"ds_write_b64 v128, v[112:113] offset:2304\n" \
"v_mfma_f32_16x16x32_bf16 v[120:123], a[144:147], a[216:219], v[120:123]\n" \
"v_mfma_f32_16x16x32_bf16 v[120:123], a[148:151], a[220:223], v[120:123]\n" \
"s_add_u32 s70, s77, 4096\n" \
"s_waitcnt lgkmcnt(8)\n" \
"v_mfma_f32_16x16x32_bf16 v[120:123], a[176:179], a[248:251], v[120:123]\n" \
"buffer_store_dwordx4 v[148:151], v130, s[52:55], s70 offen sc1\n" \
"v_cvt_pk_bf16_f32 v116, v116, v117\n" \
"v_cvt_pk_bf16_f32 v117, v118, v119\n" \
"v_mfma_f32_16x16x32_bf16 v[120:123], a[180:183], a[252:255], v[120:123]\n" \
"ds_write_b64 v128, v[116:117] offset:2336\n" \
"v_mfma_f32_16x16x32_bf16 v[124:127], a[184:187], a[248:251], v[124:127]\n" \
"v_mfma_f32_16x16x32_bf16 v[124:127], a[188:191], a[252:255], v[124:127]\n" \
"v_mfma_f32_16x16x32_bf16 v[124:127], a[152:155], a[216:219], v[124:127]\n" \
"s_add_u32 s70, s77, 69632\n" \
"v_cvt_pk_bf16_f32 v120, v120, v121\n" \
"v_cvt_pk_bf16_f32 v121, v122, v123\n" \
"v_mfma_f32_16x16x32_bf16 v[124:127], a[156:159], a[220:223], v[124:127]\n" \
"s_waitcnt lgkmcnt(8)\n" \
"ds_write_b64 v128, v[120:121] offset:2368\n" \
"buffer_store_dwordx4 v[152:155], v130, s[52:55], s70 offen sc1\n" \
"s_add_u32 s70, s77, 131072\n" \
"s_waitcnt lgkmcnt(4)\n" \
"s_nop 2\n" \
"v_cvt_pk_bf16_f32 v124, v124, v125\n" \
"v_cvt_pk_bf16_f32 v125, v126, v127\n" \
"ds_write_b64 v128, v[124:125] offset:2400\n" \
"ds_read_b128 v[164:167], v129 offset:2304\n" \
"ds_read_b128 v[168:171], v129 offset:3456\n" \
"buffer_store_dwordx4 v[156:159], v130, s[52:55], s70 offen sc1\n" \
"s_add_u32 s70, s77, 196608\n" \
"s_waitcnt lgkmcnt(6)\n" \
"buffer_store_dwordx4 v[160:163], v130, s[52:55], s70 offen sc1\n" \
"s_add_u32 s70, s77, 135168\n" \
"s_waitcnt lgkmcnt(1)\n" \
"buffer_store_dwordx4 v[164:167], v130, s[52:55], s70 offen sc1\n" \
"s_add_u32 s70, s77, 200704\n" \
"s_waitcnt lgkmcnt(0)\n" \
"buffer_store_dwordx4 v[168:171], v130, s[52:55], s70 offen sc1\n" \
"s_endpgm\n" \
"Lpg13183_%=:\n" \
"s_cbranch_execz Lcb1_%=\n" \
"Lo64_%=:\n" \
"v_mfma_f32_16x16x32_bf16 v[64:67], a[32:35], a[0:3], v[64:67]\n" \
"ds_read_b128 a[24:27], v213 offset:4352\n" \
"s_add_u32 m0, s75, s95\n" \
"v_mfma_f32_16x16x32_bf16 v[64:67], a[36:39], a[4:7], v[64:67]\n" \
"s_waitcnt lgkmcnt(1)\n" \
"ds_read_b128 a[28:31], v213 offset:6400\n" \
"global_load_lds_dwordx4 v222, s[36:37] offset:0 sc1\n" \
"v_mfma_f32_16x16x32_bf16 v[68:71], a[40:43], a[0:3], v[68:71]\n" \
"s_sub_u32 s74, s74, 32768\n" \
"s_cselect_b32 s74, 131072, s74\n" \
"ds_read_b64_tr_b16 a[48:49], v214 offset:16896\n" \
"v_mfma_f32_16x16x32_bf16 v[68:71], a[44:47], a[4:7], v[68:71]\n" \
"ds_read_b64_tr_b16 a[50:51], v214 offset:18944\n" \
"v_mfma_f32_16x16x32_bf16 v[84:87], a[40:43], a[8:11], v[84:87]\n" \
"ds_read_b64_tr_b16 a[52:53], v214 offset:25088\n" \
"v_mfma_f32_16x16x32_bf16 v[84:87], a[44:47], a[12:15], v[84:87]\n" \
"ds_read_b64_tr_b16 a[54:55], v214 offset:27136\n" \
"global_load_lds_dwordx4 v222, s[36:37] offset:1024 sc1\n" \
"v_mfma_f32_16x16x32_bf16 v[80:83], a[32:35], a[8:11], v[80:83]\n" \
"ds_read_b64_tr_b16 a[56:57], v215 offset:16896\n" \
"v_mfma_f32_16x16x32_bf16 v[80:83], a[36:39], a[12:15], v[80:83]\n" \
"ds_read_b64_tr_b16 a[58:59], v215 offset:18944\n" \
"v_mfma_f32_16x16x32_bf16 v[96:99], a[32:35], a[16:19], v[96:99]\n" \
"ds_read_b64_tr_b16 a[60:61], v215 offset:25088\n" \
"v_mfma_f32_16x16x32_bf16 v[96:99], a[36:39], a[20:23], v[96:99]\n" \
"ds_read_b64_tr_b16 a[62:63], v215 offset:27136\n" \
"global_load_lds_dwordx4 v222, s[36:37] offset:2048 sc1\n" \
"v_mfma_f32_16x16x32_bf16 v[100:103], a[40:43], a[16:19], v[100:103]\n" \
"v_add_u32 v213, s74, v211\n" \
"v_add_u32 v214, s74, v212\n" \
"v_mfma_f32_16x16x32_bf16 v[100:103], a[44:47], a[20:23], v[100:103]\n" \
"s_waitcnt lgkmcnt(3)\n" \
"v_xor_b32 v215, 32, v214\n" \
"v_mfma_f32_16x16x32_bf16 v[116:119], a[40:43], a[24:27], v[116:119]\n" \
"v_mfma_f32_16x16x32_bf16 v[116:119], a[44:47], a[28:31], v[116:119]\n" \
"s_waitcnt vmcnt(19) lgkmcnt(0)\n" \
"global_load_lds_dwordx4 v222, s[36:37] offset:3072 sc1\n" \
"v_mfma_f32_16x16x32_bf16 v[112:115], a[32:35], a[24:27], v[112:115]\n" \
"s_add_u32 s36, s36, 0x1000\n" \
"v_mfma_f32_16x16x32_bf16 v[112:115], a[36:39], a[28:31], v[112:115]\n" \
"s_barrier\n" \
"v_mfma_f32_16x16x32_bf16 v[72:75], a[48:51], a[0:3], v[72:75]\n" \
"s_add_u32 m0, m0, 0x5000\n" \
"ds_read_b64_tr_b16 a[32:33], v214 offset:16384\n" \
"v_mfma_f32_16x16x32_bf16 v[72:75], a[52:55], a[4:7], v[72:75]\n" \
"ds_read_b64_tr_b16 a[34:35], v214 offset:18432\n" \
"global_load_lds_dwordx4 v216, s[38:39] offset:-4096\n" \
"v_mfma_f32_16x16x32_bf16 v[76:79], a[56:59], a[0:3], v[76:79]\n" \
"ds_read_b64_tr_b16 a[36:37], v214 offset:24576\n" \
"v_mfma_f32_16x16x32_bf16 v[76:79], a[60:63], a[4:7], v[76:79]\n" \
"ds_read_b128 a[0:3], v213 offset:0\n" \
"v_mfma_f32_16x16x32_bf16 v[92:95], a[56:59], a[8:11], v[92:95]\n" \
"ds_read_b128 a[4:7], v213 offset:2048\n" \
"v_mfma_f32_16x16x32_bf16 v[92:95], a[60:63], a[12:15], v[92:95]\n" \
"ds_read_b64_tr_b16 a[38:39], v214 offset:26624\n" \
"global_load_lds_dwordx4 v217, s[38:39] offset:-3072\n" \
"v_mfma_f32_16x16x32_bf16 v[88:91], a[48:51], a[8:11], v[88:91]\n" \
"ds_read_b64_tr_b16 a[40:41], v215 offset:16384\n" \
"v_mfma_f32_16x16x32_bf16 v[88:91], a[52:55], a[12:15], v[88:91]\n" \
"ds_read_b128 a[8:11], v213 offset:256\n" \
"v_mfma_f32_16x16x32_bf16 v[104:107], a[48:51], a[16:19], v[104:107]\n" \
"ds_read_b128 a[12:15], v213 offset:2304\n" \
"v_mfma_f32_16x16x32_bf16 v[104:107], a[52:55], a[20:23], v[104:107]\n" \
"ds_read_b64_tr_b16 a[42:43], v215 offset:18432\n" \
"global_load_lds_dwordx4 v218, s[38:39] offset:-2048\n" \
"v_mfma_f32_16x16x32_bf16 v[108:111], a[56:59], a[16:19], v[108:111]\n" \
"ds_read_b64_tr_b16 a[44:45], v215 offset:24576\n" \
"v_mfma_f32_16x16x32_bf16 v[108:111], a[60:63], a[20:23], v[108:111]\n" \
"ds_read_b128 a[16:19], v213 offset:4096\n" \
"global_load_lds_dwordx4 v219, s[38:39] offset:-1024\n" \
"v_mfma_f32_16x16x32_bf16 v[124:127], a[56:59], a[24:27], v[124:127]\n" \
"s_add_u32 s38, s38, 0x20000\n" \
"s_addc_u32 s39, s39, 0\n" \
"ds_read_b128 a[20:23], v213 offset:6144\n" \
"v_mfma_f32_16x16x32_bf16 v[124:127], a[60:63], a[28:31], v[124:127]\n" \
"s_sub_u32 s75, s75, 32768\n" \
"s_cselect_b32 s75, 131072, s75\n" \
"ds_read_b64_tr_b16 a[46:47], v215 offset:26624\n" \
"v_mfma_f32_16x16x32_bf16 v[120:123], a[48:51], a[24:27], v[120:123]\n" \
"s_sub_u32 s72, s72, 1\n" \
"s_cmp_lg_u32 s72, 0\n" \
"v_mfma_f32_16x16x32_bf16 v[120:123], a[52:55], a[28:31], v[120:123]\n" \
"s_waitcnt lgkmcnt(3)\n" \
"s_cbranch_scc1 Lo64_%=\n" \
"s_setpc_b64 s[68:69]\n" \
"Lhot_end_%=:\n" \
"Lmask0_%=:\n" \
"Lpg13272_%=:\n" \
"s_cbranch_execz Lcb0_%=\n" \
"s_waitcnt lgkmcnt(0)\n" \
"v_subrev_u32 v0, 0, v251\n" \
"v_subrev_u32 v1, 1, v251\n" \
"v_subrev_u32 v2, 2, v251\n" \
"v_subrev_u32 v3, 3, v251\n" \
"v_subrev_u32 v4, 16, v251\n" \
"v_subrev_u32 v5, 17, v251\n" \
"v_subrev_u32 v6, 18, v251\n" \
"v_subrev_u32 v7, 19, v251\n" \
"v_subrev_u32 v32, 32, v251\n" \
"v_subrev_u32 v33, 33, v251\n" \
"v_subrev_u32 v34, 34, v251\n" \
"v_subrev_u32 v35, 35, v251\n" \
"v_subrev_u32 v36, 48, v251\n" \
"v_subrev_u32 v37, 49, v251\n" \
"v_subrev_u32 v38, 50, v251\n" \
"v_subrev_u32 v39, 51, v251\n" \
"v_ashrrev_i32 v0, 31, v0\n" \
"v_ashrrev_i32 v1, 31, v1\n" \
"v_ashrrev_i32 v2, 31, v2\n" \
"v_ashrrev_i32 v3, 31, v3\n" \
"v_ashrrev_i32 v4, 31, v4\n" \
"v_ashrrev_i32 v5, 31, v5\n" \
"v_ashrrev_i32 v6, 31, v6\n" \
"v_ashrrev_i32 v7, 31, v7\n" \
"v_ashrrev_i32 v32, 31, v32\n" \
"v_ashrrev_i32 v33, 31, v33\n" \
"v_ashrrev_i32 v34, 31, v34\n" \
"v_ashrrev_i32 v35, 31, v35\n" \
"v_ashrrev_i32 v36, 31, v36\n" \
"v_ashrrev_i32 v37, 31, v37\n" \
"v_ashrrev_i32 v38, 31, v38\n" \
"v_ashrrev_i32 v39, 31, v39\n" \
"v_and_b32 v0, 0xff800000, v0\n" \
"v_and_b32 v1, 0xff800000, v1\n" \
"v_and_b32 v2, 0xff800000, v2\n" \
"v_and_b32 v3, 0xff800000, v3\n" \
"v_and_b32 v4, 0xff800000, v4\n" \
"v_and_b32 v5, 0xff800000, v5\n" \
"v_and_b32 v6, 0xff800000, v6\n" \
"v_and_b32 v7, 0xff800000, v7\n" \
"v_and_b32 v32, 0xff800000, v32\n" \
"v_and_b32 v33, 0xff800000, v33\n" \
"v_and_b32 v34, 0xff800000, v34\n" \
"v_and_b32 v35, 0xff800000, v35\n" \
"v_and_b32 v36, 0xff800000, v36\n" \
"v_and_b32 v37, 0xff800000, v37\n" \
"v_and_b32 v38, 0xff800000, v38\n" \
"v_and_b32 v39, 0xff800000, v39\n" \
"s_branch Lmask0r_%=\n" \
"Lpl1_%=:\n" \
"s_mov_b32 s31, 0\n" \
"Lpk1_%=:\n" \
"s_sleep 2\n" \
"buffer_load_dword v244, v245, s[56:59], s91 offen sc1\n" \
"s_waitcnt vmcnt(0)\n" \
"v_cmp_ne_u32 vcc, s60, v244\n" \
"s_nop 1\n" \
"s_and_b32 vcc_hi, vcc_lo, 255\n" \
"s_cbranch_scc0 Lpr1_%=\n" \
"s_add_u32 s31, s31, 1\n" \
"s_cmp_lt_u32 s31, 0x40000\n" \
"s_cbranch_scc1 Lpk1_%=\n" \
"s_branch Lpr1_%=\n" \
"Lrs0_%=:\n" \
"s_call_b64 s[68:69], Lrest_%=\n" \
"s_branch Lrr0_%=\n" \
"Lrest_%=:\n" \
"s_mov_b32 s31, 0\n" \
"s_waitcnt vmcnt(0)\n" \
"Lrk_%=:\n" \
"v_lshrrev_b32 v253, 2, v248\n" \
"s_sleep 2\n" \
"buffer_load_dword v253, v253, s[56:59], s79 offen sc1\n" \
"s_waitcnt vmcnt(0)\n" \
"v_cmp_ne_u32 s[52:53], s60, v253\n" \
"s_add_u32 s31, s31, 1\n" \
"s_cmp_lt_u32 s31, 0x40000\n" \
"s_cselect_b32 s54, 0xff, 0\n" \
"s_and_b32 s52, s52, s54\n" \
"s_cmp_lg_u32 s52, 0\n" \
"s_cbranch_scc1 Lrk_%=\n" \
"s_mov_b64 s[10:11], 0\n" \
"s_setpc_b64 s[68:69]\n" \
"Lrs1_%=:\n" \
"s_call_b64 s[68:69], Lrest_%=\n" \
"s_branch Lrr1_%=\n" \
"Lrs2_%=:\n" \
"s_call_b64 s[68:69], Lrest_%=\n" \
"s_branch Lrr2_%=\n" \
"Lrs3_%=:\n" \
"s_call_b64 s[68:69], Lrest_%=\n" \
"s_branch Lrr3_%=\n" \
"LF7_%=:\n" \
"v_mfma_f32_16x16x32_bf16 v[24:27], a[192:195], a[176:179], v[236:239]\n" \
"s_waitcnt vmcnt(13)\n" \
"ds_read_b128 v[192:195], v246 offset:16384\n" \
"v_exp_f32 v104, v104\n" \
"v_exp_f32 v105, v105\n" \
"v_mfma_f32_16x16x32_bf16 v[24:27], a[196:199], a[180:183], v[24:27]\n" \
"s_bfe_u32 s88, %1, 0x40003\n" \
"ds_read_b128 v[196:199], v246 offset:18432\n" \
"v_cvt_pk_bf16_f32 v91, v94, v95\n" \
"v_mfma_f32_16x16x32_bf16 v[24:27], a[200:203], a[184:187], v[24:27]\n" \
"ds_read_b128 v[200:203], v246 offset:20480\n" \
"v_bfe_u32 v69, v248, 8, 1\n" \
"v_mfma_f32_16x16x32_bf16 v[24:27], a[204:207], a[188:191], v[24:27]\n" \
"ds_read_b128 a[252:255], v246 offset:14592\n" \
"v_bfe_u32 v70, v248, 6, 2\n" \
"v_mov_b32 v66, v242\n" \
"v_mov_b32 v64, v66\n" \
"v_mfma_f32_16x16x32_bf16 v[16:19], a[200:203], a[168:171], v[232:235]\n" \
"s_and_b32 s85, %1, 7\n" \
"ds_read_b128 v[204:207], v246 offset:22528\n" \
"v_exp_f32 v106, v106\n" \
"v_mfma_f32_16x16x32_bf16 v[16:19], a[204:207], a[172:175], v[16:19]\n" \
"s_lshl_b32 s85, s85, 1\n" \
"ds_read_b128 v[208:211], v246 offset:16640\n" \
"v_cvt_pk_bf16_f32 v104, v104, v105\n" \
"v_mfma_f32_16x16x32_bf16 v[16:19], a[192:195], a[160:163], v[16:19]\n" \
"s_lshr_b32 s71, %1, 7\n" \
"ds_read_b128 v[212:215], v246 offset:18688\n" \
"v_exp_f32 v107, v107\n" \
"v_permlane16_swap_b32 v64, v66\n" \
"v_add_f32 v66, v66, v64\n" \
"v_mov_b32 v64, v66\n" \
"v_mfma_f32_16x16x32_bf16 v[16:19], a[196:199], a[164:167], v[16:19]\n" \
"s_add_u32 s85, s85, s71\n" \
"ds_read_b128 v[216:219], v246 offset:20736\n" \
"v_lshl_add_u32 v70, v69, 3, v70\n" \
"v_mfma_f32_16x16x32_bf16 v[20:23], a[208:211], a[160:163], v[232:235]\n" \
"ds_read_b128 v[220:223], v246 offset:22784\n" \
"v_mul_u32_u24 v70, 6144, v70\n" \
"v_mfma_f32_16x16x32_bf16 v[20:23], a[212:215], a[164:167], v[20:23]\n" \
"ds_read_b128 a[192:195], v247 offset:16384\n" \
"v_lshlrev_b32 v69, 5, v69\n" \
"v_mfma_f32_16x16x32_bf16 v[20:23], a[216:219], a[168:171], v[20:23]\n" \
"ds_read_b128 a[196:199], v247 offset:24576\n" \
"v_and_b32 v71, 0x30, v248\n" \
"v_mfma_f32_16x16x32_bf16 v[20:23], a[220:223], a[172:175], v[20:23]\n" \
"s_lshl_b32 s70, s85, 7\n" \
"ds_read_b128 a[200:203], v247 offset:17408\n" \
"v_exp_f32 v108, v108\n" \
"v_permlane32_swap_b32 v64, v66\n" \
"v_add_f32 v66, v66, v64\n" \
"v_rcp_f32 v64, v66\n" \
"v_mul_f32 v66, 0x4d800000, v66\n" \
"v_add_u32 v65, 0x1c90080, v249\n" \
"v_cndmask_b32 v65, v249, v65, s[16:17]\n" \
"v_accvgpr_read_b32 v96, a0\n" \
"v_accvgpr_read_b32 v97, a1\n" \
"v_accvgpr_read_b32 v98, a2\n" \
"v_accvgpr_read_b32 v99, a3\n" \
"v_accvgpr_read_b32 v100, a4\n" \
"v_accvgpr_read_b32 v101, a5\n" \
"v_accvgpr_read_b32 v102, a6\n" \
"v_accvgpr_read_b32 v103, a7\n" \
"v_pk_mul_f32 v[96:97], v[96:97], v[64:65] op_sel_hi:[1,0]\n" \
"v_pk_mul_f32 v[98:99], v[98:99], v[64:65] op_sel_hi:[1,0]\n" \
"v_pk_mul_f32 v[100:101], v[100:101], v[64:65] op_sel_hi:[1,0]\n" \
"v_pk_mul_f32 v[102:103], v[102:103], v[64:65] op_sel_hi:[1,0]\n" \
"v_cvt_pk_bf16_f32 v224, v96, v97\n" \
"v_cvt_pk_bf16_f32 v225, v98, v99\n" \
"v_mfma_f32_16x16x32_bf16 v[28:31], a[216:219], a[184:187], v[236:239]\n" \
"s_mul_i32 s84, s61, 6144\n" \
"ds_read_b128 a[204:207], v247 offset:25600\n" \
"v_cvt_pk_bf16_f32 v105, v106, v107\n" \
"v_mfma_f32_16x16x32_bf16 v[28:31], a[220:223], a[188:191], v[28:31]\n" \
"s_lshl_b32 s71, s84, 4\n" \
"v_xor_b32 v69, v69, v71\n" \
"v_lshrrev_b32 v71, 9, v248\n" \
"v_mfma_f32_16x16x32_bf16 v[28:31], a[208:211], a[176:179], v[28:31]\n" \
"s_add_u32 s70, s70, s71\n" \
"v_exp_f32 v109, v109\n" \
"v_mfma_f32_16x16x32_bf16 v[28:31], a[212:215], a[180:183], v[28:31]\n" \
"s_waitcnt lgkmcnt(2)\n" \
"ds_read_b128 a[208:211], v247 offset:18432\n" \
"v_cvt_pk_bf16_f32 v226, v100, v101\n" \
"v_cvt_pk_bf16_f32 v227, v102, v103\n" \
"v_exp_f32 v110, v110\n" \
"v_pk_mul_f32 v[96:97], v[96:97], v[98:99]\n" \
"v_pk_fma_f32 v[96:97], v[100:101], v[102:103], v[96:97]\n" \
"v_fma_f32 v66, v96, v97, v66\n" \
"v_mfma_f32_16x16x32_bf16 v[8:11], v[192:195], a[176:179], v[236:239]\n" \
"s_add_u32 s70, s70, 4096\n" \
"ds_read_b128 a[212:215], v247 offset:26624\n" \
"v_lshl_add_u32 v70, v71, 6, v70\n" \
"v_mfma_f32_16x16x32_bf16 v[8:11], v[196:199], a[180:183], v[8:11]\n" \
"ds_read_b128 a[216:219], v247 offset:19456\n" \
"v_add_u32 v253, v69, v70\n" \
"v_mfma_f32_16x16x32_bf16 v[8:11], v[200:203], a[184:187], v[8:11]\n" \
"s_add_u32 s48, s22, s70\n" \
"s_addc_u32 s49, s23, 0\n" \
"ds_read_b128 a[220:223], v247 offset:27648\n" \
"v_mfma_f32_16x16x32_bf16 v[8:11], v[204:207], a[188:191], v[8:11]\n" \
"s_add_u32 s50, s48, 23552\n" \
"s_addc_u32 s51, s49, 0\n" \
"ds_read_b128 v[192:195], v246 offset:24576\n" \
"buffer_store_dwordx4 v[224:227], v65, s[56:59], s77 offen offset:0 sc1\n" \
"v_accvgpr_read_b32 v96, a8\n" \
"v_accvgpr_read_b32 v97, a9\n" \
"v_accvgpr_read_b32 v98, a10\n" \
"v_accvgpr_read_b32 v99, a11\n" \
"v_mfma_f32_16x16x32_bf16 v[56:59], a[232:235], a[184:187], v[236:239]\n" \
"s_add_u32 m0, s84, 143360\n" \
"ds_read_b128 v[196:199], v246 offset:26624\n" \
"v_cvt_pk_bf16_f32 v106, v108, v109\n" \
"v_mfma_f32_16x16x32_bf16 v[56:59], a[236:239], a[188:191], v[56:59]\n" \
"ds_read_b128 v[200:203], v246 offset:28672\n" \
"global_load_lds_dwordx4 v253, s[48:49] offset:-4096\n" \
"v_mfma_f32_16x16x32_bf16 v[56:59], a[224:227], a[176:179], v[56:59]\n" \
"ds_read_b128 v[204:207], v246 offset:30720\n" \
"global_load_lds_dwordx4 v253, s[48:49] offset:-2048\n" \
"v_mfma_f32_16x16x32_bf16 v[56:59], a[228:231], a[180:183], v[56:59]\n" \
"v_accvgpr_read_b32 v100, a12\n" \
"v_accvgpr_read_b32 v101, a13\n" \
"v_accvgpr_read_b32 v102, a14\n" \
"v_accvgpr_read_b32 v103, a15\n" \
"v_exp_f32 v111, v111\n" \
"v_pk_mul_f32 v[96:97], v[96:97], v[64:65] op_sel_hi:[1,0]\n" \
"v_pk_mul_f32 v[98:99], v[98:99], v[64:65] op_sel_hi:[1,0]\n" \
"v_pk_mul_f32 v[100:101], v[100:101], v[64:65] op_sel_hi:[1,0]\n" \
"v_pk_mul_f32 v[102:103], v[102:103], v[64:65] op_sel_hi:[1,0]\n" \
"v_cvt_pk_bf16_f32 v228, v96, v97\n" \
"v_cvt_pk_bf16_f32 v229, v98, v99\n" \
"v_cvt_pk_bf16_f32 v230, v100, v101\n" \
"v_cvt_pk_bf16_f32 v231, v102, v103\n" \
"v_pk_mul_f32 v[96:97], v[96:97], v[98:99]\n" \
"v_pk_fma_f32 v[96:97], v[100:101], v[102:103], v[96:97]\n" \
"v_fma_f32 v66, v96, v97, v66\n" \
"v_mfma_f32_16x16x32_bf16 v[48:51], a[224:227], a[160:163], v[232:235]\n" \
"v_exp_f32 v112, v112\n" \
"v_cvt_pk_bf16_f32 v107, v110, v111\n" \
"v_mfma_f32_16x16x32_bf16 v[48:51], a[228:231], a[164:167], v[48:51]\n" \
"v_exp_f32 v113, v113\n" \
"v_mfma_f32_16x16x32_bf16 v[48:51], a[232:235], a[168:171], v[48:51]\n" \
"v_exp_f32 v114, v114\n" \
"v_mfma_f32_16x16x32_bf16 v[48:51], a[236:239], a[172:175], v[48:51]\n" \
"ds_read_b128 a[224:227], v247 offset:20480\n" \
"global_load_lds_dwordx4 v253, s[48:49] offset:0\n" \
"v_mfma_f32_16x16x32_bf16 v[12:15], v[216:219], a[184:187], v[236:239]\n" \
"s_add_u32 s48, s48, 0x120000\n" \
"s_addc_u32 s49, s49, 0\n" \
"ds_read_b128 a[228:231], v247 offset:28672\n" \
"v_mfma_f32_16x16x32_bf16 v[12:15], v[220:223], a[188:191], v[12:15]\n" \
"ds_read_b128 a[232:235], v247 offset:21504\n" \
"v_cvt_pk_bf16_f32 v112, v112, v113\n" \
"v_mfma_f32_16x16x32_bf16 v[12:15], v[208:211], a[176:179], v[12:15]\n" \
"ds_read_b128 a[236:239], v247 offset:29696\n" \
"global_load_lds_dwordx4 v253, s[50:51] offset:-3072\n" \
"v_mfma_f32_16x16x32_bf16 v[12:15], v[212:215], a[180:183], v[12:15]\n" \
"ds_read_b128 v[208:211], v246 offset:24832\n" \
"buffer_store_dwordx4 v[228:231], v65, s[56:59], s77 offen offset:2048 sc1\n" \
"v_exp_f32 v115, v115\n" \
"v_exp_f32 v116, v116\n" \
"v_mfma_f32_16x16x32_bf16 v[60:63], a[240:243], a[176:179], v[236:239]\n" \
"s_add_u32 s77, s77, 0x1000\n" \
"ds_read_b128 v[212:215], v246 offset:26880\n" \
"v_cvt_pk_bf16_f32 v113, v114, v115\n" \
"v_mfma_f32_16x16x32_bf16 v[60:63], a[244:247], a[180:183], v[60:63]\n" \
"ds_read_b128 v[216:219], v246 offset:28928\n" \
"global_load_lds_dwordx4 v253, s[50:51] offset:-1024\n" \
"v_mfma_f32_16x16x32_bf16 v[60:63], a[248:251], a[184:187], v[60:63]\n" \
"ds_read_b128 v[220:223], v246 offset:30976\n" \
"global_load_lds_dwordx4 v253, s[50:51] offset:1024\n" \
"v_mfma_f32_16x16x32_bf16 v[60:63], a[252:255], a[188:191], v[60:63]\n" \
"v_accvgpr_read_b32 v96, a16\n" \
"v_accvgpr_read_b32 v97, a17\n" \
"v_accvgpr_read_b32 v98, a18\n" \
"v_accvgpr_read_b32 v99, a19\n" \
"v_accvgpr_read_b32 v100, a20\n" \
"v_accvgpr_read_b32 v101, a21\n" \
"v_accvgpr_read_b32 v102, a22\n" \
"v_accvgpr_read_b32 v103, a23\n" \
"v_exp_f32 v117, v117\n" \
"v_pk_mul_f32 v[96:97], v[96:97], v[64:65] op_sel_hi:[1,0]\n" \
"v_pk_mul_f32 v[98:99], v[98:99], v[64:65] op_sel_hi:[1,0]\n" \
"v_pk_mul_f32 v[100:101], v[100:101], v[64:65] op_sel_hi:[1,0]\n" \
"v_pk_mul_f32 v[102:103], v[102:103], v[64:65] op_sel_hi:[1,0]\n" \
"v_cvt_pk_bf16_f32 v224, v96, v97\n" \
"v_cvt_pk_bf16_f32 v225, v98, v99\n" \
"v_cvt_pk_bf16_f32 v226, v100, v101\n" \
"v_cvt_pk_bf16_f32 v227, v102, v103\n" \
"v_pk_mul_f32 v[96:97], v[96:97], v[98:99]\n" \
"v_pk_fma_f32 v[96:97], v[100:101], v[102:103], v[96:97]\n" \
"v_fma_f32 v66, v96, v97, v66\n" \
"v_mfma_f32_16x16x32_bf16 v[52:55], a[248:251], a[168:171], v[232:235]\n" \
"v_exp_f32 v118, v118\n" \
"v_mfma_f32_16x16x32_bf16 v[52:55], a[252:255], a[172:175], v[52:55]\n" \
"s_add_u32 s50, s50, 0x120000\n" \
"s_addc_u32 s51, s51, 0\n" \
"v_cvt_pk_bf16_f32 v114, v116, v117\n" \
"v_mfma_f32_16x16x32_bf16 v[52:55], a[240:243], a[160:163], v[52:55]\n" \
"v_exp_f32 v119, v119\n" \
"v_mfma_f32_16x16x32_bf16 v[52:55], a[244:247], a[164:167], v[52:55]\n" \
"s_waitcnt lgkmcnt(0)\n" \
"buffer_store_dwordx4 v[224:227], v65, s[56:59], s77 offen offset:0 sc1\n" \
"ds_read_b128 a[240:243], v247 offset:22528\n" \
"v_accvgpr_read_b32 v96, a24\n" \
"v_accvgpr_read_b32 v97, a25\n" \
"v_accvgpr_read_b32 v98, a26\n" \
"v_accvgpr_read_b32 v99, a27\n" \
"v_accvgpr_read_b32 v100, a28\n" \
"v_accvgpr_read_b32 v101, a29\n" \
"v_accvgpr_read_b32 v102, a30\n" \
"v_accvgpr_read_b32 v103, a31\n" \
"v_exp_f32 v120, v120\n" \
"v_pk_mul_f32 v[96:97], v[96:97], v[64:65] op_sel_hi:[1,0]\n" \
"v_pk_mul_f32 v[98:99], v[98:99], v[64:65] op_sel_hi:[1,0]\n" \
"v_pk_mul_f32 v[100:101], v[100:101], v[64:65] op_sel_hi:[1,0]\n" \
"v_pk_mul_f32 v[102:103], v[102:103], v[64:65] op_sel_hi:[1,0]\n" \
"v_mfma_f32_16x16x32_bf16 v[40:43], v[192:195], a[176:179], v[236:239]\n" \
"ds_read_b128 a[244:247], v247 offset:30720\n" \
"v_cvt_pk_bf16_f32 v115, v118, v119\n" \
"v_mfma_f32_16x16x32_bf16 v[40:43], v[196:199], a[180:183], v[40:43]\n" \
"ds_read_b128 a[248:251], v247 offset:23552\n" \
"v_exp_f32 v121, v121\n" \
"v_exp_f32 v122, v122\n" \
"v_mfma_f32_16x16x32_bf16 v[40:43], v[200:203], a[184:187], v[40:43]\n" \
"ds_read_b128 a[252:255], v247 offset:31744\n" \
"v_cvt_pk_bf16_f32 v120, v120, v121\n" \
"v_mfma_f32_16x16x32_bf16 v[40:43], v[204:207], a[188:191], v[40:43]\n" \
"v_exp_f32 v123, v123\n" \
"v_exp_f32 v124, v124\n" \
"v_cvt_pk_bf16_f32 v121, v122, v123\n" \
"v_mfma_f32_16x16x32_bf16 v[44:47], v[216:219], a[184:187], v[236:239]\n" \
"v_exp_f32 v125, v125\n" \
"v_mfma_f32_16x16x32_bf16 v[44:47], v[220:223], a[188:191], v[44:47]\n" \
"s_waitcnt vmcnt(9) lgkmcnt(0)\n" \
"v_exp_f32 v126, v126\n" \
"v_mfma_f32_16x16x32_bf16 v[44:47], v[208:211], a[176:179], v[44:47]\n" \
"v_add_u32 v247, 0x10000, v248\n" \
"v_cvt_pk_bf16_f32 v122, v124, v125\n" \
"v_mfma_f32_16x16x32_bf16 v[44:47], v[212:215], a[180:183], v[44:47]\n" \
"s_barrier\n" \
"v_cvt_pk_bf16_f32 v228, v96, v97\n" \
"v_cvt_pk_bf16_f32 v229, v98, v99\n" \
"v_cvt_pk_bf16_f32 v230, v100, v101\n" \
"v_cvt_pk_bf16_f32 v231, v102, v103\n" \
"v_pk_mul_f32 v[96:97], v[96:97], v[98:99]\n" \
"v_pk_fma_f32 v[96:97], v[100:101], v[102:103], v[96:97]\n" \
"v_mfma_f32_16x16x32_bf16 a[32:35], a[192:195], v[72:75], a[32:35]\n" \
"v_exp_f32 v127, v127\n" \
"v_mfma_f32_16x16x32_bf16 a[32:35], a[196:199], v[104:107], a[32:35]\n" \
"v_cvt_pk_bf16_f32 v123, v126, v127\n" \
"v_fma_f32 v66, v96, v97, v66\n" \
"v_mfma_f32_16x16x32_bf16 v[128:131], a[192:195], v[80:83], v[128:131]\n" \
"ds_read_b128 v[192:195], v247 offset:16384\n" \
"v_cmp_gt_i32 vcc, 0, v251\n" \
"v_mfma_f32_16x16x32_bf16 v[128:131], a[196:199], v[112:115], v[128:131]\n" \
"v_lshrrev_b32 v71, 7, v248\n" \
"v_and_b32 v76, 0x70, v248\n" \
"v_mfma_f32_16x16x32_bf16 v[160:163], a[192:195], v[88:91], v[160:163]\n" \
"s_lshl_b32 s70, s88, 18\n" \
"v_cmp_gt_i32 s[64:65], 1, v251\n" \
"v_cmp_gt_i32 s[66:67], 2, v251\n" \
"v_mfma_f32_16x16x32_bf16 v[160:163], a[196:199], v[120:123], v[160:163]\n" \
"s_lshl_b32 s71, s61, 16\n" \
"ds_read_b128 a[192:195], v247 offset:0\n" \
"v_cndmask_b32 v8, v8, v254, vcc\n" \
"v_mfma_f32_16x16x32_bf16 v[240:243], a[124:127], v[104:107], v[240:243]\n" \
"s_add_u32 s71, s71, s70\n" \
"ds_read_b128 v[196:199], v247 offset:24576\n" \
"v_cndmask_b32 v16, v16, v254, vcc\n" \
"v_mfma_f32_16x16x32_bf16 v[164:167], a[200:203], v[88:91], v[164:167]\n" \
"v_lshlrev_b32 v77, 4, v71\n" \
"v_xor_b32 v77, v77, v76\n" \
"v_mfma_f32_16x16x32_bf16 v[164:167], a[204:207], v[120:123], v[164:167]\n" \
"s_add_u32 s42, s20, s71\n" \
"s_addc_u32 s43, s21, 0\n" \
"ds_read_b128 a[196:199], v247 offset:8192\n" \
"v_cmp_gt_i32 vcc, 3, v251\n" \
"v_cndmask_b32 v9, v9, v254, s[64:65]\n" \
"v_cndmask_b32 v17, v17, v254, s[64:65]\n" \
"v_cmp_gt_i32 s[64:65], 16, v251\n" \
"v_cndmask_b32 v10, v10, v254, s[66:67]\n" \
"v_cndmask_b32 v18, v18, v254, s[66:67]\n" \
"v_cmp_gt_i32 s[66:67], 17, v251\n" \
"v_cndmask_b32 v11, v11, v254, vcc\n" \
"v_cndmask_b32 v19, v19, v254, vcc\n" \
"v_mfma_f32_16x16x32_bf16 v[132:135], a[200:203], v[80:83], v[132:135]\n" \
"ds_read_b128 v[200:203], v247 offset:17408\n" \
"v_lshl_add_u32 v252, v71, 12, v77\n" \
"v_mfma_f32_16x16x32_bf16 v[132:135], a[204:207], v[112:115], v[132:135]\n" \
"s_add_u32 s44, s42, 31744\n" \
"s_addc_u32 s45, s43, 0\n" \
"v_cmp_gt_i32 vcc, 18, v251\n" \
"v_cndmask_b32 v12, v12, v254, s[64:65]\n" \
"v_mfma_f32_16x16x32_bf16 v[242:245], a[120:123], v[112:115], v[242:245]\n" \
"v_cndmask_b32 v20, v20, v254, s[64:65]\n" \
"v_cmp_gt_i32 s[64:65], 19, v251\n" \
"v_mfma_f32_16x16x32_bf16 v[242:245], a[124:127], v[120:123], v[242:245]\n" \
"v_cndmask_b32 v13, v13, v254, s[66:67]\n" \
"v_cndmask_b32 v21, v21, v254, s[66:67]\n" \
"v_cmp_gt_i32 s[66:67], 32, v251\n" \
"v_cndmask_b32 v14, v14, v254, vcc\n" \
"v_mfma_f32_16x16x32_bf16 a[36:39], a[200:203], v[72:75], a[36:39]\n" \
"s_add_u32 m0, s95, 118784\n" \
"v_cndmask_b32 v22, v22, v254, vcc\n" \
"v_cmp_gt_i32 vcc, 33, v251\n" \
"v_mfma_f32_16x16x32_bf16 a[36:39], a[204:207], v[104:107], a[36:39]\n" \
"ds_read_b128 a[200:203], v247 offset:1024\n" \
"global_load_lds_dwordx4 v252, s[42:43] offset:0\n" \
"ds_read_b128 v[204:207], v247 offset:25600\n" \
"v_cndmask_b32 v15, v15, v254, s[64:65]\n" \
"v_cndmask_b32 v23, v23, v254, s[64:65]\n" \
"v_cmp_gt_i32 s[64:65], 34, v251\n" \
"v_cndmask_b32 v40, v40, v254, s[66:67]\n" \
"v_mfma_f32_16x16x32_bf16 a[40:43], a[208:211], v[72:75], a[40:43]\n" \
"v_cndmask_b32 v48, v48, v254, s[66:67]\n" \
"v_cmp_gt_i32 s[66:67], 35, v251\n" \
"v_mfma_f32_16x16x32_bf16 a[40:43], a[212:215], v[104:107], a[40:43]\n" \
"buffer_store_dwordx4 v[228:231], v65, s[56:59], s77 offen offset:2048 sc1\n" \
"ds_read_b128 a[204:207], v247 offset:9216\n" \
"v_cndmask_b32 v41, v41, v254, vcc\n" \
"v_cndmask_b32 v49, v49, v254, vcc\n" \
"v_cmp_gt_i32 vcc, 48, v251\n" \
"v_cndmask_b32 v42, v42, v254, s[64:65]\n" \
"v_cndmask_b32 v50, v50, v254, s[64:65]\n" \
"v_cmp_gt_i32 s[64:65], 49, v251\n" \
"v_cndmask_b32 v43, v43, v254, s[66:67]\n" \
"v_cndmask_b32 v51, v51, v254, s[66:67]\n" \
"v_cmp_gt_i32 s[66:67], 50, v251\n" \
"v_cndmask_b32 v44, v44, v254, vcc\n" \
"v_cmp_class_f32 s[54:55], v66, s94\n" \
"v_mfma_f32_16x16x32_bf16 v[136:139], a[208:211], v[80:83], v[136:139]\n" \
"s_sub_u32 s77, s77, 4096\n" \
"ds_read_b128 v[208:211], v247 offset:18432\n" \
"v_cndmask_b32 v52, v52, v254, vcc\n" \
"v_mfma_f32_16x16x32_bf16 v[136:139], a[212:215], v[112:115], v[136:139]\n" \
"s_or_b64 s[92:93], s[92:93], s[54:55]\n" \
"v_cmp_gt_i32 vcc, 51, v251\n" \
"v_cndmask_b32 v45, v45, v254, s[64:65]\n" \
"v_cndmask_b32 v53, v53, v254, s[64:65]\n" \
"v_cndmask_b32 v46, v46, v254, s[66:67]\n" \
"v_cndmask_b32 v54, v54, v254, s[66:67]\n" \
"v_cndmask_b32 v47, v47, v254, vcc\n" \
"v_cndmask_b32 v55, v55, v254, vcc\n" \
"v_exp_f32 v8, v8\n" \
"v_exp_f32 v9, v9\n" \
"v_mfma_f32_16x16x32_bf16 v[168:171], a[208:211], v[88:91], v[168:171]\n" \
"v_mov_b32 v66, v243\n" \
"v_exp_f32 v10, v10\n" \
"v_mfma_f32_16x16x32_bf16 v[168:171], a[212:215], v[120:123], v[168:171]\n" \
"ds_read_b128 a[208:211], v247 offset:2048\n" \
"global_load_lds_dwordx4 v252, s[42:43] offset:2048\n" \
"ds_read_b128 v[212:215], v247 offset:26624\n" \
"v_mfma_f32_16x16x32_bf16 v[172:175], a[216:219], v[88:91], v[172:175]\n" \
"s_add_u32 s42, s42, 0x180\n" \
"s_addc_u32 s43, s43, 0\n" \
"v_cvt_pk_bf16_f32 v8, v8, v9\n" \
"v_mfma_f32_16x16x32_bf16 v[172:175], a[220:223], v[120:123], v[172:175]\n" \
"ds_read_b128 a[212:215], v247 offset:10240\n" \
"v_exp_f32 v11, v11\n" \
"v_exp_f32 v12, v12\n" \
"v_mfma_f32_16x16x32_bf16 v[140:143], a[216:219], v[80:83], v[140:143]\n" \
"ds_read_b128 v[216:219], v247 offset:19456\n" \
"v_cvt_pk_bf16_f32 v9, v10, v11\n" \
"v_mfma_f32_16x16x32_bf16 v[140:143], a[220:223], v[112:115], v[140:143]\n" \
"v_exp_f32 v13, v13\n" \
"v_mfma_f32_16x16x32_bf16 a[44:47], a[216:219], v[72:75], a[44:47]\n" \
"v_exp_f32 v14, v14\n" \
"v_mfma_f32_16x16x32_bf16 a[44:47], a[220:223], v[104:107], a[44:47]\n" \
"ds_read_b128 a[216:219], v247 offset:3072\n" \
"global_load_lds_dwordx4 v252, s[44:45] offset:1024\n" \
"ds_read_b128 v[220:223], v247 offset:27648\n" \
"v_cvt_pk_bf16_f32 v10, v12, v13\n" \
"v_mfma_f32_16x16x32_bf16 a[48:51], a[224:227], v[72:75], a[48:51]\n" \
"v_exp_f32 v15, v15\n" \
"v_mfma_f32_16x16x32_bf16 a[48:51], a[228:231], v[104:107], a[48:51]\n" \
"ds_read_b128 a[220:223], v247 offset:11264\n" \
"v_exp_f32 v40, v40\n" \
"v_mfma_f32_16x16x32_bf16 v[144:147], a[224:227], v[80:83], v[144:147]\n" \
"ds_read_b128 v[224:227], v247 offset:20480\n" \
"v_cvt_pk_bf16_f32 v11, v14, v15\n" \
"v_mfma_f32_16x16x32_bf16 v[144:147], a[228:231], v[112:115], v[144:147]\n" \
"v_exp_f32 v41, v41\n" \
"v_mfma_f32_16x16x32_bf16 v[176:179], a[224:227], v[88:91], v[176:179]\n" \
"v_exp_f32 v42, v42\n" \
"v_mfma_f32_16x16x32_bf16 v[176:179], a[228:231], v[120:123], v[176:179]\n" \
"ds_read_b128 a[224:227], v247 offset:4096\n" \
"global_load_lds_dwordx4 v252, s[44:45] offset:3072\n" \
"ds_read_b128 v[228:231], v247 offset:28672\n" \
"v_mfma_f32_16x16x32_bf16 v[180:183], a[232:235], v[88:91], v[180:183]\n" \
"s_add_u32 s44, s44, 0x180\n" \
"s_addc_u32 s45, s45, 0\n" \
"v_cvt_pk_bf16_f32 v40, v40, v41\n" \
"v_mfma_f32_16x16x32_bf16 v[180:183], a[236:239], v[120:123], v[180:183]\n" \
"ds_read_b128 a[228:231], v247 offset:12288\n" \
"v_exp_f32 v43, v43\n" \
"v_exp_f32 v44, v44\n" \
"v_mfma_f32_16x16x32_bf16 v[148:151], a[232:235], v[80:83], v[148:151]\n" \
"ds_read_b128 v[232:235], v247 offset:21504\n" \
"v_cvt_pk_bf16_f32 v41, v42, v43\n" \
"v_mfma_f32_16x16x32_bf16 v[148:151], a[236:239], v[112:115], v[148:151]\n" \
"v_exp_f32 v45, v45\n" \
"v_mfma_f32_16x16x32_bf16 a[52:55], a[232:235], v[72:75], a[52:55]\n" \
"v_exp_f32 v46, v46\n" \
"v_mfma_f32_16x16x32_bf16 a[52:55], a[236:239], v[104:107], a[52:55]\n" \
"ds_read_b128 a[232:235], v247 offset:5120\n" \
"ds_read_b128 v[236:239], v247 offset:29696\n" \
"v_cvt_pk_bf16_f32 v42, v44, v45\n" \
"v_mfma_f32_16x16x32_bf16 a[56:59], a[240:243], v[72:75], a[56:59]\n" \
"v_exp_f32 v47, v47\n" \
"v_mfma_f32_16x16x32_bf16 a[56:59], a[244:247], v[104:107], a[56:59]\n" \
"ds_read_b128 a[236:239], v247 offset:13312\n" \
"v_exp_f32 v24, v24\n" \
"v_mfma_f32_16x16x32_bf16 v[152:155], a[240:243], v[80:83], v[152:155]\n" \
"ds_read_b128 v[0:3], v247 offset:22528\n" \
"v_cvt_pk_bf16_f32 v43, v46, v47\n" \
"v_mfma_f32_16x16x32_bf16 v[152:155], a[244:247], v[112:115], v[152:155]\n" \
"v_exp_f32 v25, v25\n" \
"v_mfma_f32_16x16x32_bf16 v[184:187], a[240:243], v[88:91], v[184:187]\n" \
"v_exp_f32 v26, v26\n" \
"v_mfma_f32_16x16x32_bf16 v[184:187], a[244:247], v[120:123], v[184:187]\n" \
"ds_read_b128 a[240:243], v247 offset:6144\n" \
"v_cvt_pk_bf16_f32 v24, v24, v25\n" \
"v_mfma_f32_16x16x32_bf16 v[242:245], a[124:127], v[8:11], v[242:245]\n" \
"v_exp_f32 v27, v27\n" \
"v_mfma_f32_16x16x32_bf16 v[242:245], a[124:127], v[40:43], v[242:245]\n" \
"ds_read_b128 v[4:7], v247 offset:30720\n" \
"v_exp_f32 v28, v28\n" \
"v_mfma_f32_16x16x32_bf16 a[60:63], a[248:251], v[72:75], a[60:63]\n" \
"v_cvt_pk_bf16_f32 v25, v26, v27\n" \
"v_exp_f32 v29, v29\n" \
"v_mfma_f32_16x16x32_bf16 a[60:63], a[252:255], v[104:107], a[60:63]\n" \
"ds_read_b128 a[244:247], v247 offset:14336\n" \
"v_exp_f32 v30, v30\n" \
"v_mfma_f32_16x16x32_bf16 v[156:159], a[248:251], v[80:83], v[156:159]\n" \
"ds_read_b128 v[32:35], v247 offset:23552\n" \
"v_cvt_pk_bf16_f32 v26, v28, v29\n" \
"v_mfma_f32_16x16x32_bf16 v[156:159], a[252:255], v[112:115], v[156:159]\n" \
"v_exp_f32 v31, v31\n" \
"v_exp_f32 v56, v56\n" \
"v_cvt_pk_bf16_f32 v27, v30, v31\n" \
"v_mfma_f32_16x16x32_bf16 v[188:191], a[248:251], v[88:91], v[188:191]\n" \
"v_exp_f32 v57, v57\n" \
"v_mfma_f32_16x16x32_bf16 v[188:191], a[252:255], v[120:123], v[188:191]\n" \
"s_waitcnt lgkmcnt(2)\n" \
"ds_read_b128 a[248:251], v247 offset:7168\n" \
"ds_read_b128 v[36:39], v247 offset:31744\n" \
"v_mfma_f32_16x16x32_bf16 v[160:163], v[192:195], v[8:11], v[160:163]\n" \
"v_exp_f32 v58, v58\n" \
"v_mfma_f32_16x16x32_bf16 v[160:163], v[196:199], v[40:43], v[160:163]\n" \
"ds_read_b128 a[252:255], v247 offset:15360\n" \
"v_cvt_pk_bf16_f32 v56, v56, v57\n" \
"v_exp_f32 v59, v59\n" \
"v_mfma_f32_16x16x32_bf16 v[164:167], v[200:203], v[8:11], v[164:167]\n" \
"v_exp_f32 v60, v60\n" \
"v_mfma_f32_16x16x32_bf16 v[164:167], v[204:207], v[40:43], v[164:167]\n" \
"v_mov_b32 v64, v66\n" \
"v_cvt_pk_bf16_f32 v57, v58, v59\n" \
"v_exp_f32 v61, v61\n" \
"v_mfma_f32_16x16x32_bf16 v[168:171], v[208:211], v[8:11], v[168:171]\n" \
"v_exp_f32 v62, v62\n" \
"v_mfma_f32_16x16x32_bf16 v[168:171], v[212:215], v[40:43], v[168:171]\n" \
"s_waitcnt vmcnt(0) lgkmcnt(0)\n" \
"v_cvt_pk_bf16_f32 v58, v60, v61\n" \
"v_exp_f32 v63, v63\n" \
"v_permlane16_swap_b32 v64, v66\n" \
"v_add_f32 v66, v66, v64\n" \
"v_mov_b32 v64, v66\n" \
"v_mfma_f32_16x16x32_bf16 v[172:175], v[216:219], v[8:11], v[172:175]\n" \
"v_exp_f32 v16, v16\n" \
"v_mfma_f32_16x16x32_bf16 v[172:175], v[220:223], v[40:43], v[172:175]\n" \
"s_barrier\n" \
"v_cvt_pk_bf16_f32 v59, v62, v63\n" \
"v_exp_f32 v17, v17\n" \
"v_permlane32_swap_b32 v64, v66\n" \
"v_add_f32 v66, v66, v64\n" \
"v_rcp_f32 v64, v66\n" \
"v_mul_f32 v66, 0x4d800000, v66\n" \
"v_add_u32 v65, 0x1c90080, v249\n" \
"v_cndmask_b32 v65, v249, v65, s[16:17]\n" \
"v_mfma_f32_16x16x32_bf16 v[176:179], v[224:227], v[8:11], v[176:179]\n" \
"s_add_u32 m0, s84, 90112\n" \
"v_exp_f32 v18, v18\n" \
"v_mfma_f32_16x16x32_bf16 v[176:179], v[228:231], v[40:43], v[176:179]\n" \
"global_load_lds_dwordx4 v253, s[48:49] offset:-4096\n" \
"global_load_lds_dwordx4 v253, s[48:49] offset:-2048\n" \
"v_cvt_pk_bf16_f32 v16, v16, v17\n" \
"v_mfma_f32_16x16x32_bf16 v[242:245], a[124:127], v[24:27], v[242:245]\n" \
"v_exp_f32 v19, v19\n" \
"v_mfma_f32_16x16x32_bf16 v[242:245], a[124:127], v[56:59], v[242:245]\n" \
"v_exp_f32 v20, v20\n" \
"v_cvt_pk_bf16_f32 v17, v18, v19\n" \
"v_mfma_f32_16x16x32_bf16 v[180:183], v[232:235], v[8:11], v[180:183]\n" \
"s_add_u32 s77, s77, 532480\n" \
"v_exp_f32 v21, v21\n" \
"v_mfma_f32_16x16x32_bf16 v[180:183], v[236:239], v[40:43], v[180:183]\n" \
"v_accvgpr_read_b32 v96, a32\n" \
"v_accvgpr_read_b32 v97, a33\n" \
"v_accvgpr_read_b32 v98, a34\n" \
"v_accvgpr_read_b32 v99, a35\n" \
"v_accvgpr_read_b32 v100, a36\n" \
"v_accvgpr_read_b32 v101, a37\n" \
"v_accvgpr_read_b32 v102, a38\n" \
"v_accvgpr_read_b32 v103, a39\n" \
"v_exp_f32 v22, v22\n" \
"v_pk_mul_f32 v[96:97], v[96:97], v[64:65] op_sel_hi:[1,0]\n" \
"v_pk_mul_f32 v[98:99], v[98:99], v[64:65] op_sel_hi:[1,0]\n" \
"v_pk_mul_f32 v[100:101], v[100:101], v[64:65] op_sel_hi:[1,0]\n" \
"v_pk_mul_f32 v[102:103], v[102:103], v[64:65] op_sel_hi:[1,0]\n" \
"v_cvt_pk_bf16_f32 v104, v96, v97\n" \
"v_cvt_pk_bf16_f32 v105, v98, v99\n" \
"v_cvt_pk_bf16_f32 v106, v100, v101\n" \
"v_cvt_pk_bf16_f32 v107, v102, v103\n" \
"v_pk_mul_f32 v[96:97], v[96:97], v[98:99]\n" \
"v_pk_fma_f32 v[96:97], v[100:101], v[102:103], v[96:97]\n" \
"v_fma_f32 v66, v96, v97, v66\n" \
"v_mfma_f32_16x16x32_bf16 v[184:187], v[0:3], v[8:11], v[184:187]\n" \
"global_load_lds_dwordx4 v253, s[48:49] offset:0\n" \
"v_cvt_pk_bf16_f32 v18, v20, v21\n" \
"v_mfma_f32_16x16x32_bf16 v[184:187], v[4:7], v[40:43], v[184:187]\n" \
"s_add_u32 s48, s48, 0x300000\n" \
"s_addc_u32 s49, s49, 0\n" \
"v_exp_f32 v23, v23\n" \
"v_exp_f32 v48, v48\n" \
"v_mfma_f32_16x16x32_bf16 v[188:191], v[32:35], v[8:11], v[188:191]\n" \
"global_load_lds_dwordx4 v253, s[50:51] offset:-3072\n" \
"v_cvt_pk_bf16_f32 v19, v22, v23\n" \
"v_mfma_f32_16x16x32_bf16 v[188:191], v[36:39], v[40:43], v[188:191]\n" \
"buffer_store_dwordx4 v[104:107], v65, s[56:59], s77 offen offset:0 sc1\n" \
"v_accvgpr_read_b32 v96, a40\n" \
"v_accvgpr_read_b32 v97, a41\n" \
"v_accvgpr_read_b32 v98, a42\n" \
"v_accvgpr_read_b32 v99, a43\n" \
"v_accvgpr_read_b32 v100, a44\n" \
"v_accvgpr_read_b32 v101, a45\n" \
"v_accvgpr_read_b32 v102, a46\n" \
"v_accvgpr_read_b32 v103, a47\n" \
"v_exp_f32 v49, v49\n" \
"v_pk_mul_f32 v[96:97], v[96:97], v[64:65] op_sel_hi:[1,0]\n" \
"v_pk_mul_f32 v[98:99], v[98:99], v[64:65] op_sel_hi:[1,0]\n" \
"v_pk_mul_f32 v[100:101], v[100:101], v[64:65] op_sel_hi:[1,0]\n" \
"v_pk_mul_f32 v[102:103], v[102:103], v[64:65] op_sel_hi:[1,0]\n" \
"v_cvt_pk_bf16_f32 v108, v96, v97\n" \
"v_cvt_pk_bf16_f32 v109, v98, v99\n" \
"v_cvt_pk_bf16_f32 v110, v100, v101\n" \
"v_cvt_pk_bf16_f32 v111, v102, v103\n" \
"v_pk_mul_f32 v[96:97], v[96:97], v[98:99]\n" \
"v_pk_fma_f32 v[96:97], v[100:101], v[102:103], v[96:97]\n" \
"v_fma_f32 v66, v96, v97, v66\n" \
"v_mfma_f32_16x16x32_bf16 v[160:163], a[192:195], v[24:27], v[160:163]\n" \
"global_load_lds_dwordx4 v253, s[50:51] offset:-1024\n" \
"global_load_lds_dwordx4 v253, s[50:51] offset:1024\n" \
"v_mfma_f32_16x16x32_bf16 v[160:163], a[196:199], v[56:59], v[160:163]\n" \
"s_movk_i32 s89, 0xc0\n" \
"v_exp_f32 v50, v50\n" \
"v_cvt_pk_bf16_f32 v48, v48, v49\n" \
"v_mfma_f32_16x16x32_bf16 v[242:245], a[120:123], v[16:19], v[242:245]\n" \
"s_movk_i32 s90, 288\n" \
"v_exp_f32 v51, v51\n" \
"v_lshrrev_b32 v211, 4, v248\n" \
"v_mfma_f32_16x16x32_bf16 v[164:167], a[200:203], v[24:27], v[164:167]\n" \
"s_add_u32 s50, s50, 0x300000\n" \
"s_addc_u32 s51, s51, 0\n" \
"v_lshrrev_b32 v214, 8, v248\n" \
"v_mfma_f32_16x16x32_bf16 v[164:167], a[204:207], v[56:59], v[164:167]\n" \
"s_add_u32 m0, s95, 65536\n" \
"buffer_store_dwordx4 v[108:111], v65, s[56:59], s77 offen offset:2048 sc1\n" \
"v_and_b32 v215, 7, v211\n" \
"v_xor_b32 v214, v214, v215\n" \
"v_exp_f32 v52, v52\n" \
"v_mfma_f32_16x16x32_bf16 v[168:171], a[208:211], v[24:27], v[168:171]\n" \
"global_load_lds_dwordx4 v252, s[42:43] offset:0\n" \
"global_load_lds_dwordx4 v252, s[42:43] offset:2048\n" \
"v_mfma_f32_16x16x32_bf16 v[168:171], a[212:215], v[56:59], v[168:171]\n" \
"s_lshr_b32 s71, s61, 1\n" \
"v_cvt_pk_bf16_f32 v49, v50, v51\n" \
"s_lshl_b32 s70, s71, 11\n" \
"v_mfma_f32_16x16x32_bf16 v[172:175], a[216:219], v[24:27], v[172:175]\n" \
"s_add_u32 s70, s70, 118784\n" \
"v_and_b32 v216, 15, v211\n" \
"v_lshlrev_b32 v213, 7, v216\n" \
"v_mfma_f32_16x16x32_bf16 v[172:175], a[220:223], v[56:59], v[172:175]\n" \
"s_add_u32 s42, s42, 0x400\n" \
"s_addc_u32 s43, s43, 0\n" \
"v_lshl_add_u32 v213, v214, 4, v213\n" \
"v_add_u32 v205, s70, v213\n" \
"v_xor_b32 v206, 64, v205\n" \
"v_mfma_f32_16x16x32_bf16 v[176:179], a[224:227], v[24:27], v[176:179]\n" \
"global_load_lds_dwordx4 v252, s[44:45] offset:1024\n" \
"global_load_lds_dwordx4 v252, s[44:45] offset:3072\n" \
"v_mfma_f32_16x16x32_bf16 v[176:179], a[228:231], v[56:59], v[176:179]\n" \
"s_and_b32 s70, s61, 1\n" \
"v_exp_f32 v53, v53\n" \
"v_mfma_f32_16x16x32_bf16 v[180:183], a[232:235], v[24:27], v[180:183]\n" \
"s_lshl_b32 s70, s70, 9\n" \
"v_exp_f32 v54, v54\n" \
"v_mfma_f32_16x16x32_bf16 v[180:183], a[236:239], v[56:59], v[180:183]\n" \
"s_add_u32 s44, s44, 0x400\n" \
"s_addc_u32 s45, s45, 0\n" \
"v_cvt_pk_bf16_f32 v50, v52, v53\n" \
"v_exp_f32 v55, v55\n" \
"v_mfma_f32_16x16x32_bf16 v[184:187], a[240:243], v[24:27], v[184:187]\n" \
"s_add_u32 s70, s70, 139264\n" \
"v_cvt_pk_bf16_f32 v51, v54, v55\n" \
"v_and_b32 v213, 3, v211\n" \
"v_mfma_f32_16x16x32_bf16 v[184:187], a[244:247], v[56:59], v[184:187]\n" \
"s_add_u32 m0, s95, 0\n" \
"v_lshlrev_b32 v213, 3, v213\n" \
"v_and_or_b32 v213, v248, s89, v213\n" \
"v_mfma_f32_16x16x32_bf16 v[242:245], a[120:123], v[48:51], v[242:245]\n" \
"s_add_u32 s77, s77, 0x1000\n" \
"v_accvgpr_read_b32 v96, a48\n" \
"v_accvgpr_read_b32 v97, a49\n" \
"v_mfma_f32_16x16x32_bf16 v[188:191], a[248:251], v[24:27], v[188:191]\n" \
"v_bfe_u32 v214, v211, 4, 1\n" \
"v_mad_u32_u24 v213, v214, s90, v213\n" \
"v_mfma_f32_16x16x32_bf16 v[188:191], a[252:255], v[56:59], v[188:191]\n" \
"v_lshrrev_b32 v214, 5, v211\n" \
"v_mul_u32_u24 v214, 6144, v214\n" \
"v_accvgpr_read_b32 v98, a50\n" \
"v_accvgpr_read_b32 v99, a51\n" \
"v_accvgpr_read_b32 v100, a52\n" \
"v_accvgpr_read_b32 v101, a53\n" \
"v_accvgpr_read_b32 v102, a54\n" \
"v_accvgpr_read_b32 v103, a55\n" \
"v_pk_mul_f32 v[96:97], v[96:97], v[64:65] op_sel_hi:[1,0]\n" \
"v_pk_mul_f32 v[98:99], v[98:99], v[64:65] op_sel_hi:[1,0]\n" \
"v_pk_mul_f32 v[100:101], v[100:101], v[64:65] op_sel_hi:[1,0]\n" \
"v_pk_mul_f32 v[102:103], v[102:103], v[64:65] op_sel_hi:[1,0]\n" \
"v_cvt_pk_bf16_f32 v104, v96, v97\n" \
"v_cvt_pk_bf16_f32 v105, v98, v99\n" \
"v_cvt_pk_bf16_f32 v106, v100, v101\n" \
"v_cvt_pk_bf16_f32 v107, v102, v103\n" \
"v_pk_mul_f32 v[96:97], v[96:97], v[98:99]\n" \
"v_pk_fma_f32 v[96:97], v[100:101], v[102:103], v[96:97]\n" \
"v_mfma_f32_16x16x32_bf16 v[128:131], a[192:195], v[16:19], v[128:131]\n" \
"v_add3_u32 v209, v213, v214, s70\n" \
"v_xor_b32 v210, 32, v209\n" \
"v_mfma_f32_16x16x32_bf16 v[128:131], a[196:199], v[48:51], v[128:131]\n" \
"ds_read_b128 a[72:75], v205 offset:0\n" \
"ds_read_b64_tr_b16 a[96:97], v209 offset:2048\n" \
"v_mfma_f32_16x16x32_bf16 v[132:135], a[200:203], v[16:19], v[132:135]\n" \
"ds_read_b64_tr_b16 a[98:99], v209 offset:3072\n" \
"global_load_lds_dwordx4 v252, s[42:43] offset:0\n" \
"v_mfma_f32_16x16x32_bf16 v[132:135], a[204:207], v[48:51], v[132:135]\n" \
"ds_read_b128 a[76:79], v206 offset:0\n" \
"v_fma_f32 v66, v96, v97, v66\n" \
"v_mfma_f32_16x16x32_bf16 v[136:139], a[208:211], v[16:19], v[136:139]\n" \
"ds_read_b64_tr_b16 a[100:101], v209 offset:14336\n" \
"buffer_store_dwordx4 v[104:107], v65, s[56:59], s77 offen offset:0 sc1\n" \
"ds_read_b64_tr_b16 a[102:103], v209 offset:15360\n" \
"global_load_lds_dwordx4 v252, s[42:43] offset:2048\n" \
"v_accvgpr_read_b32 v96, a56\n" \
"v_accvgpr_read_b32 v97, a57\n" \
"v_mfma_f32_16x16x32_bf16 v[136:139], a[212:215], v[48:51], v[136:139]\n" \
"s_add_u32 s42, s42, 0xfffffb00\n" \
"s_addc_u32 s43, s43, -1\n" \
"ds_read_b64_tr_b16 a[104:105], v209 offset:0\n" \
"v_mfma_f32_16x16x32_bf16 v[140:143], a[216:219], v[16:19], v[140:143]\n" \
"ds_read_b128 a[80:83], v205 offset:4096\n" \
"v_accvgpr_read_b32 v98, a58\n" \
"v_mfma_f32_16x16x32_bf16 v[140:143], a[220:223], v[48:51], v[140:143]\n" \
"ds_read_b64_tr_b16 a[106:107], v209 offset:1024\n" \
"global_load_lds_dwordx4 v252, s[44:45] offset:1024\n" \
"ds_read_b64_tr_b16 a[108:109], v209 offset:12288\n" \
"v_mfma_f32_16x16x32_bf16 v[144:147], a[224:227], v[16:19], v[144:147]\n" \
"ds_read_b64_tr_b16 a[110:111], v209 offset:13312\n" \
"v_accvgpr_read_b32 v99, a59\n" \
"v_mfma_f32_16x16x32_bf16 v[144:147], a[228:231], v[48:51], v[144:147]\n" \
"ds_read_b128 a[84:87], v206 offset:4096\n" \
"v_accvgpr_read_b32 v100, a60\n" \
"v_accvgpr_read_b32 v101, a61\n" \
"v_accvgpr_read_b32 v102, a62\n" \
"v_accvgpr_read_b32 v103, a63\n" \
"v_pk_mul_f32 v[96:97], v[96:97], v[64:65] op_sel_hi:[1,0]\n" \
"v_pk_mul_f32 v[98:99], v[98:99], v[64:65] op_sel_hi:[1,0]\n" \
"v_pk_mul_f32 v[100:101], v[100:101], v[64:65] op_sel_hi:[1,0]\n" \
"v_pk_mul_f32 v[102:103], v[102:103], v[64:65] op_sel_hi:[1,0]\n" \
"v_mfma_f32_16x16x32_bf16 v[148:151], a[232:235], v[16:19], v[148:151]\n" \
"ds_read_b64_tr_b16 a[112:113], v210 offset:0\n" \
"global_load_lds_dwordx4 v252, s[44:45] offset:3072\n" \
"ds_read_b64_tr_b16 a[114:115], v210 offset:1024\n" \
"v_mfma_f32_16x16x32_bf16 v[148:151], a[236:239], v[48:51], v[148:151]\n" \
"ds_read_b64_tr_b16 a[116:117], v210 offset:12288\n" \
"v_cvt_pk_bf16_f32 v108, v96, v97\n" \
"v_cvt_pk_bf16_f32 v109, v98, v99\n" \
"v_cvt_pk_bf16_f32 v110, v100, v101\n" \
"v_cvt_pk_bf16_f32 v111, v102, v103\n" \
"v_pk_mul_f32 v[96:97], v[96:97], v[98:99]\n" \
"v_pk_fma_f32 v[96:97], v[100:101], v[102:103], v[96:97]\n" \
"v_mfma_f32_16x16x32_bf16 v[152:155], a[240:243], v[16:19], v[152:155]\n" \
"ds_read_b128 a[88:91], v205 offset:8192\n" \
"v_fma_f32 v66, v96, v97, v66\n" \
"v_mfma_f32_16x16x32_bf16 v[152:155], a[244:247], v[48:51], v[152:155]\n" \
"buffer_store_dwordx4 v[108:111], v65, s[56:59], s77 offen offset:2048 sc1\n" \
"ds_read_b128 a[92:95], v206 offset:8192\n" \
"ds_read_b64_tr_b16 a[118:119], v210 offset:13312\n" \
"v_cmp_class_f32 s[54:55], v66, s94\n" \
"s_sub_u32 s77, s77, 536576\n" \
"v_mfma_f32_16x16x32_bf16 v[156:159], a[248:251], v[16:19], v[156:159]\n" \
"s_or_b64 s[92:93], s[92:93], s[54:55]\n" \
"v_mfma_f32_16x16x32_bf16 v[156:159], a[252:255], v[48:51], v[156:159]\n" \
"s_waitcnt lgkmcnt(4)\n" \
"s_branch Ljoin_%=\n" \
"Lexact_%=:\n" \
"v_mov_b32 v190, 0xff800000\n" \
"v_add_u32 v191, 0x1c90080, v249\n" \
"v_cndmask_b32 v191, v249, v191, s[16:17]\n" \
"s_and_b32 s80, s61, 2\n" \
"s_lshl_b32 s80, s80, 4\n" \
"v_subrev_u32 v186, s80, v251\n" \
"v_accvgpr_read_b32 v160, a128\n" \
"v_accvgpr_read_b32 v161, a129\n" \
"v_accvgpr_read_b32 v162, a130\n" \
"v_accvgpr_read_b32 v163, a131\n" \
"v_accvgpr_read_b32 v164, a132\n" \
"v_accvgpr_read_b32 v165, a133\n" \
"v_accvgpr_read_b32 v166, a134\n" \
"v_accvgpr_read_b32 v167, a135\n" \
"v_accvgpr_read_b32 v168, a136\n" \
"v_accvgpr_read_b32 v169, a137\n" \
"v_accvgpr_read_b32 v170, a138\n" \
"v_accvgpr_read_b32 v171, a139\n" \
"v_accvgpr_read_b32 v172, a140\n" \
"v_accvgpr_read_b32 v173, a141\n" \
"v_accvgpr_read_b32 v174, a142\n" \
"v_accvgpr_read_b32 v175, a143\n" \
"s_lshl_b32 s75, s63, 2\n" \
"s_lshr_b32 s80, s61, 1\n" \
"s_add_u32 s75, s75, s80\n" \
"s_add_u32 s75, s75, 0\n" \
"s_add_u32 s76, s77, 0\n" \
"s_call_b64 s[68:69], Lex1_%=\n" \
"v_accvgpr_read_b32 v160, a144\n" \
"v_accvgpr_read_b32 v161, a145\n" \
"v_accvgpr_read_b32 v162, a146\n" \
"v_accvgpr_read_b32 v163, a147\n" \
"v_accvgpr_read_b32 v164, a148\n" \
"v_accvgpr_read_b32 v165, a149\n" \
"v_accvgpr_read_b32 v166, a150\n" \
"v_accvgpr_read_b32 v167, a151\n" \
"v_accvgpr_read_b32 v168, a152\n" \
"v_accvgpr_read_b32 v169, a153\n" \
"v_accvgpr_read_b32 v170, a154\n" \
"v_accvgpr_read_b32 v171, a155\n" \
"v_accvgpr_read_b32 v172, a156\n" \
"v_accvgpr_read_b32 v173, a157\n" \
"v_accvgpr_read_b32 v174, a158\n" \
"v_accvgpr_read_b32 v175, a159\n" \
"s_lshl_b32 s75, s63, 2\n" \
"s_lshr_b32 s80, s61, 1\n" \
"s_add_u32 s75, s75, s80\n" \
"s_add_u32 s75, s75, 2\n" \
"s_add_u32 s76, s77, 532480\n" \
"s_call_b64 s[68:69], Lex1_%=\n" \
"v_accvgpr_read_b32 v160, a160\n" \
"v_accvgpr_read_b32 v161, a161\n" \
"v_accvgpr_read_b32 v162, a162\n" \
"v_accvgpr_read_b32 v163, a163\n" \
"v_accvgpr_read_b32 v164, a164\n" \
"v_accvgpr_read_b32 v165, a165\n" \
"v_accvgpr_read_b32 v166, a166\n" \
"v_accvgpr_read_b32 v167, a167\n" \
"v_accvgpr_read_b32 v168, a168\n" \
"v_accvgpr_read_b32 v169, a169\n" \
"v_accvgpr_read_b32 v170, a170\n" \
"v_accvgpr_read_b32 v171, a171\n" \
"v_accvgpr_read_b32 v172, a172\n" \
"v_accvgpr_read_b32 v173, a173\n" \
"v_accvgpr_read_b32 v174, a174\n" \
"v_accvgpr_read_b32 v175, a175\n" \
"s_lshl_b32 s75, s63, 2\n" \
"s_sub_u32 s75, 60, s75\n" \
"s_lshr_b32 s80, s61, 1\n" \
"s_add_u32 s75, s75, s80\n" \
"s_add_u32 s75, s75, 0\n" \
"s_add_u32 s76, s78, 0\n" \
"s_call_b64 s[68:69], Lex1_%=\n" \
"v_accvgpr_read_b32 v160, a176\n" \
"v_accvgpr_read_b32 v161, a177\n" \
"v_accvgpr_read_b32 v162, a178\n" \
"v_accvgpr_read_b32 v163, a179\n" \
"v_accvgpr_read_b32 v164, a180\n" \
"v_accvgpr_read_b32 v165, a181\n" \
"v_accvgpr_read_b32 v166, a182\n" \
"v_accvgpr_read_b32 v167, a183\n" \
"v_accvgpr_read_b32 v168, a184\n" \
"v_accvgpr_read_b32 v169, a185\n" \
"v_accvgpr_read_b32 v170, a186\n" \
"v_accvgpr_read_b32 v171, a187\n" \
"v_accvgpr_read_b32 v172, a188\n" \
"v_accvgpr_read_b32 v173, a189\n" \
"v_accvgpr_read_b32 v174, a190\n" \
"v_accvgpr_read_b32 v175, a191\n" \
"s_lshl_b32 s75, s63, 2\n" \
"s_sub_u32 s75, 60, s75\n" \
"s_lshr_b32 s80, s61, 1\n" \
"s_add_u32 s75, s75, s80\n" \
"s_add_u32 s75, s75, 2\n" \
"s_add_u32 s76, s78, 532480\n" \
"s_call_b64 s[68:69], Lex1_%=\n" \
"s_branch Lexr_%=\n" \
"Lex1_%=:\n" \
"v_mov_b32 v128, 0\n" \
"v_mov_b32 v129, 0\n" \
"v_mov_b32 v130, 0\n" \
"v_mov_b32 v131, 0\n" \
"v_mov_b32 v132, 0\n" \
"v_mov_b32 v133, 0\n" \
"v_mov_b32 v134, 0\n" \
"v_mov_b32 v135, 0\n" \
"v_mov_b32 v136, 0\n" \
"v_mov_b32 v137, 0\n" \
"v_mov_b32 v138, 0\n" \
"v_mov_b32 v139, 0\n" \
"v_mov_b32 v140, 0\n" \
"v_mov_b32 v141, 0\n" \
"v_mov_b32 v142, 0\n" \
"v_mov_b32 v143, 0\n" \
"v_mov_b32 v144, 0\n" \
"v_mov_b32 v145, 0\n" \
"v_mov_b32 v146, 0\n" \
"v_mov_b32 v147, 0\n" \
"v_mov_b32 v148, 0\n" \
"v_mov_b32 v149, 0\n" \
"v_mov_b32 v150, 0\n" \
"v_mov_b32 v151, 0\n" \
"v_mov_b32 v152, 0\n" \
"v_mov_b32 v153, 0\n" \
"v_mov_b32 v154, 0\n" \
"v_mov_b32 v155, 0\n" \
"v_mov_b32 v156, 0\n" \
"v_mov_b32 v157, 0\n" \
"v_mov_b32 v158, 0\n" \
"v_mov_b32 v159, 0\n" \
"v_mov_b32 v184, 0xf149f2ca\n" \
"v_mov_b32 v185, 0\n" \
"s_mov_b32 s73, 0\n" \
"s_mul_i32 s74, s62, 557824\n" \
"Lex2_%=:\n" \
"s_add_u32 s80, s74, 4096\n" \
"buffer_load_dwordx4 a[0:3], v249, s[56:59], s74 offen offset:0 sc1\n" \
"buffer_load_dwordx4 a[4:7], v249, s[56:59], s74 offen offset:2048 sc1\n" \
"buffer_load_dwordx4 a[8:11], v249, s[56:59], s80 offen offset:0 sc1\n" \
"buffer_load_dwordx4 a[12:15], v249, s[56:59], s80 offen offset:2048 sc1\n" \
"buffer_load_dwordx4 a[16:19], v249, s[56:59], s74 offen offset:256 sc1\n" \
"buffer_load_dwordx4 a[20:23], v249, s[56:59], s74 offen offset:2304 sc1\n" \
"buffer_load_dwordx4 a[24:27], v249, s[56:59], s80 offen offset:256 sc1\n" \
"buffer_load_dwordx4 a[28:31], v249, s[56:59], s80 offen offset:2304 sc1\n" \
"s_add_u32 s80, s74, 17850368\n" \
"s_add_u32 s81, s80, 4096\n" \
"buffer_load_dwordx4 a[32:35], v248, s[56:59], s80 offen offset:0 sc1\n" \
"buffer_load_dwordx4 a[36:39], v248, s[56:59], s80 offen offset:1024 sc1\n" \
"buffer_load_dwordx4 a[40:43], v248, s[56:59], s80 offen offset:2048 sc1\n" \
"buffer_load_dwordx4 a[44:47], v248, s[56:59], s80 offen offset:3072 sc1\n" \
"buffer_load_dwordx4 a[48:51], v248, s[56:59], s81 offen offset:0 sc1\n" \
"buffer_load_dwordx4 a[52:55], v248, s[56:59], s81 offen offset:1024 sc1\n" \
"buffer_load_dwordx4 a[56:59], v248, s[56:59], s81 offen offset:2048 sc1\n" \
"buffer_load_dwordx4 a[60:63], v248, s[56:59], s81 offen offset:3072 sc1\n" \
"s_waitcnt vmcnt(0)\n" \
"v_mfma_f32_16x16x32_bf16 v[176:179], a[0:3], v[160:163], 0\n" \
"v_mfma_f32_16x16x32_bf16 v[176:179], a[4:7], v[164:167], v[176:179]\n" \
"v_mfma_f32_16x16x32_bf16 v[176:179], a[8:11], v[168:171], v[176:179]\n" \
"v_mfma_f32_16x16x32_bf16 v[176:179], a[12:15], v[172:175], v[176:179]\n" \
"v_mfma_f32_16x16x32_bf16 v[180:183], a[16:19], v[160:163], 0\n" \
"v_mfma_f32_16x16x32_bf16 v[180:183], a[20:23], v[164:167], v[180:183]\n" \
"v_mfma_f32_16x16x32_bf16 v[180:183], a[24:27], v[168:171], v[180:183]\n" \
"v_mfma_f32_16x16x32_bf16 v[180:183], a[28:31], v[172:175], v[180:183]\n" \
"s_nop 7\n" \
"s_nop 1\n" \
"s_cmp_eq_u32 s73, s75\n" \
"s_cbranch_scc0 Lex3_%=\n" \
"v_cmp_gt_i32 vcc, 0, v186\n" \
"s_nop 1\n" \
"v_cndmask_b32 v176, v176, v190, vcc\n" \
"v_cmp_gt_i32 vcc, 1, v186\n" \
"s_nop 1\n" \
"v_cndmask_b32 v177, v177, v190, vcc\n" \
"v_cmp_gt_i32 vcc, 2, v186\n" \
"s_nop 1\n" \
"v_cndmask_b32 v178, v178, v190, vcc\n" \
"v_cmp_gt_i32 vcc, 3, v186\n" \
"s_nop 1\n" \
"v_cndmask_b32 v179, v179, v190, vcc\n" \
"v_cmp_gt_i32 vcc, 16, v186\n" \
"s_nop 1\n" \
"v_cndmask_b32 v180, v180, v190, vcc\n" \
"v_cmp_gt_i32 vcc, 17, v186\n" \
"s_nop 1\n" \
"v_cndmask_b32 v181, v181, v190, vcc\n" \
"v_cmp_gt_i32 vcc, 18, v186\n" \
"s_nop 1\n" \
"v_cndmask_b32 v182, v182, v190, vcc\n" \
"v_cmp_gt_i32 vcc, 19, v186\n" \
"s_nop 1\n" \
"v_cndmask_b32 v183, v183, v190, vcc\n" \
"Lex3_%=:\n" \
"v_max3_f32 v187, v176, v177, v178\n" \
"v_max3_f32 v187, v187, v179, v180\n" \
"v_max3_f32 v187, v187, v181, v182\n" \
"v_max3_f32 v187, v187, v183, v184\n" \
"v_mov_b32 v188, v187\n" \
"s_nop 1\n" \
"v_permlane16_swap_b32 v188, v187\n" \
"v_max_f32 v187, v187, v188\n" \
"v_mov_b32 v188, v187\n" \
"s_nop 1\n" \
"v_permlane32_swap_b32 v188, v187\n" \
"v_max_f32 v187, v187, v188\n" \
"v_sub_f32 v189, v184, v187\n" \
"v_exp_f32 v189, v189\n" \
"v_mov_b32 v184, v187\n" \
"v_sub_f32 v176, v176, v187\n" \
"v_sub_f32 v177, v177, v187\n" \
"v_sub_f32 v178, v178, v187\n" \
"v_sub_f32 v179, v179, v187\n" \
"v_sub_f32 v180, v180, v187\n" \
"v_sub_f32 v181, v181, v187\n" \
"v_sub_f32 v182, v182, v187\n" \
"v_sub_f32 v183, v183, v187\n" \
"v_exp_f32 v176, v176\n" \
"v_exp_f32 v177, v177\n" \
"v_exp_f32 v178, v178\n" \
"v_exp_f32 v179, v179\n" \
"v_exp_f32 v180, v180\n" \
"v_exp_f32 v181, v181\n" \
"v_exp_f32 v182, v182\n" \
"v_exp_f32 v183, v183\n" \
"v_mul_f32 v185, v185, v189\n" \
"v_add_f32 v185, v185, v176\n" \
"v_add_f32 v185, v185, v177\n" \
"v_add_f32 v185, v185, v178\n" \
"v_add_f32 v185, v185, v179\n" \
"v_add_f32 v185, v185, v180\n" \
"v_add_f32 v185, v185, v181\n" \
"v_add_f32 v185, v185, v182\n" \
"v_add_f32 v185, v185, v183\n" \
"v_cvt_pk_bf16_f32 v112, v176, v177\n" \
"v_cvt_pk_bf16_f32 v113, v178, v179\n" \
"v_cvt_pk_bf16_f32 v114, v180, v181\n" \
"v_cvt_pk_bf16_f32 v115, v182, v183\n" \
"v_mul_f32 v128, v128, v189\n" \
"v_mul_f32 v129, v129, v189\n" \
"v_mul_f32 v130, v130, v189\n" \
"v_mul_f32 v131, v131, v189\n" \
"v_mul_f32 v132, v132, v189\n" \
"v_mul_f32 v133, v133, v189\n" \
"v_mul_f32 v134, v134, v189\n" \
"v_mul_f32 v135, v135, v189\n" \
"v_mul_f32 v136, v136, v189\n" \
"v_mul_f32 v137, v137, v189\n" \
"v_mul_f32 v138, v138, v189\n" \
"v_mul_f32 v139, v139, v189\n" \
"v_mul_f32 v140, v140, v189\n" \
"v_mul_f32 v141, v141, v189\n" \
"v_mul_f32 v142, v142, v189\n" \
"v_mul_f32 v143, v143, v189\n" \
"v_mul_f32 v144, v144, v189\n" \
"v_mul_f32 v145, v145, v189\n" \
"v_mul_f32 v146, v146, v189\n" \
"v_mul_f32 v147, v147, v189\n" \
"v_mul_f32 v148, v148, v189\n" \
"v_mul_f32 v149, v149, v189\n" \
"v_mul_f32 v150, v150, v189\n" \
"v_mul_f32 v151, v151, v189\n" \
"v_mul_f32 v152, v152, v189\n" \
"v_mul_f32 v153, v153, v189\n" \
"v_mul_f32 v154, v154, v189\n" \
"v_mul_f32 v155, v155, v189\n" \
"v_mul_f32 v156, v156, v189\n" \
"v_mul_f32 v157, v157, v189\n" \
"v_mul_f32 v158, v158, v189\n" \
"v_mul_f32 v159, v159, v189\n" \
"s_nop 1\n" \
"v_mfma_f32_16x16x32_bf16 v[128:131], a[32:35], v[112:115], v[128:131]\n" \
"v_mfma_f32_16x16x32_bf16 v[132:135], a[36:39], v[112:115], v[132:135]\n" \
"v_mfma_f32_16x16x32_bf16 v[136:139], a[40:43], v[112:115], v[136:139]\n" \
"v_mfma_f32_16x16x32_bf16 v[140:143], a[44:47], v[112:115], v[140:143]\n" \
"v_mfma_f32_16x16x32_bf16 v[144:147], a[48:51], v[112:115], v[144:147]\n" \
"v_mfma_f32_16x16x32_bf16 v[148:151], a[52:55], v[112:115], v[148:151]\n" \
"v_mfma_f32_16x16x32_bf16 v[152:155], a[56:59], v[112:115], v[152:155]\n" \
"v_mfma_f32_16x16x32_bf16 v[156:159], a[60:63], v[112:115], v[156:159]\n" \
"s_nop 7\n" \
"s_nop 1\n" \
"s_and_b32 s80, s73, 1\n" \
"s_mul_i32 s80, s80, 1024\n" \
"s_add_u32 s74, s74, s80\n" \
"s_add_u32 s74, s74, 8192\n" \
"s_add_u32 s73, s73, 1\n" \
"s_cmp_le_u32 s73, s75\n" \
"s_cbranch_scc1 Lex2_%=\n" \
"v_mov_b32 v187, v185\n" \
"s_nop 1\n" \
"v_permlane16_swap_b32 v187, v185\n" \
"v_add_f32 v185, v185, v187\n" \
"v_mov_b32 v187, v185\n" \
"s_nop 1\n" \
"v_permlane32_swap_b32 v187, v185\n" \
"v_add_f32 v185, v185, v187\n" \
"v_rcp_f32 v185, v185\n" \
"s_nop 0\n" \
"v_mul_f32 v128, v128, v185\n" \
"v_mul_f32 v129, v129, v185\n" \
"v_mul_f32 v130, v130, v185\n" \
"v_mul_f32 v131, v131, v185\n" \
"v_mul_f32 v132, v132, v185\n" \
"v_mul_f32 v133, v133, v185\n" \
"v_mul_f32 v134, v134, v185\n" \
"v_mul_f32 v135, v135, v185\n" \
"v_mul_f32 v136, v136, v185\n" \
"v_mul_f32 v137, v137, v185\n" \
"v_mul_f32 v138, v138, v185\n" \
"v_mul_f32 v139, v139, v185\n" \
"v_mul_f32 v140, v140, v185\n" \
"v_mul_f32 v141, v141, v185\n" \
"v_mul_f32 v142, v142, v185\n" \
"v_mul_f32 v143, v143, v185\n" \
"v_mul_f32 v144, v144, v185\n" \
"v_mul_f32 v145, v145, v185\n" \
"v_mul_f32 v146, v146, v185\n" \
"v_mul_f32 v147, v147, v185\n" \
"v_mul_f32 v148, v148, v185\n" \
"v_mul_f32 v149, v149, v185\n" \
"v_mul_f32 v150, v150, v185\n" \
"v_mul_f32 v151, v151, v185\n" \
"v_mul_f32 v152, v152, v185\n" \
"v_mul_f32 v153, v153, v185\n" \
"v_mul_f32 v154, v154, v185\n" \
"v_mul_f32 v155, v155, v185\n" \
"v_mul_f32 v156, v156, v185\n" \
"v_mul_f32 v157, v157, v185\n" \
"v_mul_f32 v158, v158, v185\n" \
"v_mul_f32 v159, v159, v185\n" \
"s_add_u32 s80, s76, 4096\n" \
"v_cvt_pk_bf16_f32 v112, v128, v129\n" \
"v_cvt_pk_bf16_f32 v113, v130, v131\n" \
"v_cvt_pk_bf16_f32 v114, v132, v133\n" \
"v_cvt_pk_bf16_f32 v115, v134, v135\n" \
"buffer_store_dwordx4 v[112:115], v191, s[56:59], s76 offen offset:0 sc1\n" \
"v_cvt_pk_bf16_f32 v116, v136, v137\n" \
"v_cvt_pk_bf16_f32 v117, v138, v139\n" \
"v_cvt_pk_bf16_f32 v118, v140, v141\n" \
"v_cvt_pk_bf16_f32 v119, v142, v143\n" \
"buffer_store_dwordx4 v[116:119], v191, s[56:59], s76 offen offset:2048 sc1\n" \
"v_cvt_pk_bf16_f32 v120, v144, v145\n" \
"v_cvt_pk_bf16_f32 v121, v146, v147\n" \
"v_cvt_pk_bf16_f32 v122, v148, v149\n" \
"v_cvt_pk_bf16_f32 v123, v150, v151\n" \
"buffer_store_dwordx4 v[120:123], v191, s[56:59], s80 offen offset:0 sc1\n" \
"v_cvt_pk_bf16_f32 v124, v152, v153\n" \
"v_cvt_pk_bf16_f32 v125, v154, v155\n" \
"v_cvt_pk_bf16_f32 v126, v156, v157\n" \
"v_cvt_pk_bf16_f32 v127, v158, v159\n" \
"buffer_store_dwordx4 v[124:127], v191, s[56:59], s80 offen offset:2048 sc1\n" \
"s_waitcnt vmcnt(0)\n" \
"s_setpc_b64 s[68:69]\n" \
"Lpl2_%=:\n" \
"s_mov_b32 s31, 0\n" \
"Lpk2_%=:\n" \
"s_sleep 2\n" \
"buffer_load_dword v220, v221, s[56:59], s81 offen sc1\n" \
"s_waitcnt vmcnt(0)\n" \
"v_cmp_ne_u32 vcc, s60, v220\n" \
"s_nop 1\n" \
"s_and_b32 vcc_hi, vcc_lo, s80\n" \
"s_cbranch_scc0 Lpr2_%=\n" \
"s_add_u32 s31, s31, 1\n" \
"s_cmp_lt_u32 s31, 0x40000\n" \
"s_cbranch_scc1 Lpk2_%=\n" \
"s_branch Lpr2_%=\n" \
"Lpl3_%=:\n" \
"s_mov_b32 s31, 0\n" \
"Lpk3_%=:\n" \
"s_sleep 2\n" \
"buffer_load_dwordx2 v[226:227], v228, s[56:59], s30 offen sc1\n" \
"s_waitcnt vmcnt(0)\n" \
"v_xor_b32 v226, v226, v227\n" \
"v_cmp_ne_u32 vcc, s60, v226\n" \
"s_nop 1\n" \
"s_and_b32 vcc_hi, vcc_lo, -1\n" \
"s_cbranch_scc0 Lpr3_%=\n" \
"s_add_u32 s31, s31, 1\n" \
"s_cmp_lt_u32 s31, 0x40000\n" \
"s_cbranch_scc1 Lpk3_%=\n" \
"s_branch Lpr3_%=\n" \
"Lplh_%=:\n" \
"s_mov_b32 s31, 0\n" \
"Lpkh_%=:\n" \
"s_sleep 2\n" \
"buffer_load_dwordx4 v[128:131], v207, s[56:59], s47 offen offset:0 sc1\n" \
"buffer_load_dwordx4 v[192:195], v208, s[56:59], s47 offen offset:1024 sc1\n" \
"buffer_load_dwordx4 v[68:71], v223, s[56:59], s47 offen offset:2048 sc1\n" \
"s_waitcnt vmcnt(0)\n" \
"v_bitop3_b32 v131, v131, v128, v129 bitop3:0x96\n" \
"v_bitop3_b32 v192, v192, v193, v194 bitop3:0x96\n" \
"v_bitop3_b32 v68, v68, v69, v70 bitop3:0x96\n" \
"v_xor_b32 v131, v131, v130\n" \
"v_xor_b32 v192, v192, v195\n" \
"v_xor_b32 v68, v68, v71\n" \
"v_cmp_ne_u32 s[42:43], s60, v131\n" \
"v_cmp_ne_u32 s[44:45], s60, v192\n" \
"v_cmp_ne_u32 vcc, s60, v68\n" \
"s_nop 1\n" \
"s_and_b64 s[42:43], s[42:43], s[12:13]\n" \
"s_and_b64 s[44:45], s[44:45], s[92:93]\n" \
"s_and_b64 vcc, vcc, s[14:15]\n" \
"s_or_b64 s[42:43], s[42:43], s[44:45]\n" \
"s_or_b64 vcc, vcc, s[42:43]\n" \
"s_cbranch_scc0 Lprh_%=\n" \
"s_add_u32 s31, s31, 1\n" \
"s_cmp_lt_u32 s31, 0x40000\n" \
"s_cbranch_scc1 Lpkh_%=\n" \
"s_branch Lprh_%=\n" \
"Lpl4_%=:\n" \
"s_mov_b32 s31, 0\n" \
"Lpk4_%=:\n" \
"s_sleep 2\n" \
"buffer_load_dword v220, off, s[56:59], s79 sc1\n" \
"s_waitcnt vmcnt(0)\n" \
"v_cmp_ne_u32 vcc, s60, v220\n" \
"s_nop 1\n" \
"s_and_b32 vcc_hi, vcc_lo, 1\n" \
"s_cbranch_scc0 Lpr4_%=\n" \
"s_add_u32 s31, s31, 1\n" \
"s_cmp_lt_u32 s31, 0x40000\n" \
"s_cbranch_scc1 Lpk4_%=\n" \
"s_branch Lpr4_%=\n" \
"Lpl5_%=:\n" \
"s_mov_b32 s31, 0\n" \
"Lpk5_%=:\n" \
"s_sleep 2\n" \
"buffer_load_dword v220, v221, s[56:59], s82 offen sc1\n" \
"s_waitcnt vmcnt(0)\n" \
"v_cmp_ne_u32 vcc, s60, v220\n" \
"s_nop 1\n" \
"s_and_b32 vcc_hi, vcc_lo, 65535\n" \
"s_cbranch_scc0 Lpr5_%=\n" \
"s_add_u32 s31, s31, 1\n" \
"s_cmp_lt_u32 s31, 0x40000\n" \
"s_cbranch_scc1 Lpk5_%=\n" \
"s_branch Lpr5_%=\n" \
"Lcode_end_%=:\n" \
".p2align 12\n" \
""
#define MK_ASM_CLOBBERS "v1", "v2", "v3", "v4", "v5", "v6", "v7", "v8", "v9", "v10", "v11", "v12", "v13", "v14", "v15", "v16", "v17", "v18", "v19", "v20", "v21", "v22", "v23", "v24", "v25", "v26", "v27", "v28", "v29", "v30", "v31", "v32", "v33", "v34", "v35", "v36", "v37", "v38", "v39", "v40", "v41", "v42", "v43", "v44", "v45", "v46", "v47", "v48", "v49", "v50", "v51", "v52", "v53", "v54", "v55", "v56", "v57", "v58", "v59", "v60", "v61", "v62", "v63", "v64", "v65", "v66", "v67", "v68", "v69", "v70", "v71", "v72", "v73", "v74", "v75", "v76", "v77", "v78", "v79", "v80", "v81", "v82", "v83", "v84", "v85", "v86", "v87", "v88", "v89", "v90", "v91", "v92", "v93", "v94", "v95", "v96", "v97", "v98", "v99", "v100", "v101", "v102", "v103", "v104", "v105", "v106", "v107", "v108", "v109", "v110", "v111", "v112", "v113", "v114", "v115", "v116", "v117", "v118", "v119", "v120", "v121", "v122", "v123", "v124", "v125", "v126", "v127", "v128", "v129", "v130", "v131", "v132", "v133", "v134", "v135", "v136", "v137", "v138", "v139", "v140", "v141", "v142", "v143", "v144", "v145", "v146", "v147", "v148", "v149", "v150", "v151", "v152", "v153", "v154", "v155", "v156", "v157", "v158", "v159", "v160", "v161", "v162", "v163", "v164", "v165", "v166", "v167", "v168", "v169", "v170", "v171", "v172", "v173", "v174", "v175", "v176", "v177", "v178", "v179", "v180", "v181", "v182", "v183", "v184", "v185", "v186", "v187", "v188", "v189", "v190", "v191", "v192", "v193", "v194", "v195", "v196", "v197", "v198", "v199", "v200", "v201", "v202", "v203", "v204", "v205", "v206", "v207", "v208", "v209", "v210", "v211", "v212", "v213", "v214", "v215", "v216", "v217", "v218", "v219", "v220", "v221", "v222", "v223", "v224", "v225", "v226", "v227", "v228", "v229", "v230", "v231", "v232", "v233", "v234", "v235", "v236", "v237", "v238", "v239", "v240", "v241", "v242", "v243", "v244", "v245", "v246", "v247", "v248", "v249", "v250", "v251", "v252", "v253", "v254", "v255", "a0", "a1", "a2", "a3", "a4", "a5", "a6", "a7", "a8", "a9", "a10", "a11", "a12", "a13", "a14", "a15", "a16", "a17", "a18", "a19", "a20", "a21", "a22", "a23", "a24", "a25", "a26", "a27", "a28", "a29", "a30", "a31", "a32", "a33", "a34", "a35", "a36", "a37", "a38", "a39", "a40", "a41", "a42", "a43", "a44", "a45", "a46", "a47", "a48", "a49", "a50", "a51", "a52", "a53", "a54", "a55", "a56", "a57", "a58", "a59", "a60", "a61", "a62", "a63", "a64", "a65", "a66", "a67", "a68", "a69", "a70", "a71", "a72", "a73", "a74", "a75", "a76", "a77", "a78", "a79", "a80", "a81", "a82", "a83", "a84", "a85", "a86", "a87", "a88", "a89", "a90", "a91", "a92", "a93", "a94", "a95", "a96", "a97", "a98", "a99", "a100", "a101", "a102", "a103", "a104", "a105", "a106", "a107", "a108", "a109", "a110", "a111", "a112", "a113", "a114", "a115", "a116", "a117", "a118", "a119", "a120", "a121", "a122", "a123", "a124", "a125", "a126", "a127", "a128", "a129", "a130", "a131", "a132", "a133", "a134", "a135", "a136", "a137", "a138", "a139", "a140", "a141", "a142", "a143", "a144", "a145", "a146", "a147", "a148", "a149", "a150", "a151", "a152", "a153", "a154", "a155", "a156", "a157", "a158", "a159", "a160", "a161", "a162", "a163", "a164", "a165", "a166", "a167", "a168", "a169", "a170", "a171", "a172", "a173", "a174", "a175", "a176", "a177", "a178", "a179", "a180", "a181", "a182", "a183", "a184", "a185", "a186", "a187", "a188", "a189", "a190", "a191", "a192", "a193", "a194", "a195", "a196", "a197", "a198", "a199", "a200", "a201", "a202", "a203", "a204", "a205", "a206", "a207", "a208", "a209", "a210", "a211", "a212", "a213", "a214", "a215", "a216", "a217", "a218", "a219", "a220", "a221", "a222", "a223", "a224", "a225", "a226", "a227", "a228", "a229", "a230", "a231", "a232", "a233", "a234", "a235", "a236", "a237", "a238", "a239", "a240", "a241", "a242", "a243", "a244", "a245", "a246", "a247", "a248", "a249", "a250", "a251", "a252", "a253", "a254", "a255", "s8", "s9", "s10", "s11", "s12", "s13", "s14", "s15", "s16", "s17", "s18", "s19", "s20", "s21", "s22", "s23", "s24", "s25", "s26", "s27", "s28", "s29", "s30", "s31", "s36", "s37", "s38", "s39", "s40", "s41", "s42", "s43", "s44", "s45", "s46", "s47", "s48", "s49", "s50", "s51", "s52", "s53", "s54", "s55", "s56", "s57", "s58", "s59", "s60", "s61", "s62", "s63", "s64", "s65", "s66", "s67", "s68", "s69", "s70", "s71", "s72", "s73", "s74", "s75", "s76", "s77", "s78", "s79", "s80", "s81", "s82", "s83", "s84", "s85", "s86", "s87", "s88", "s89", "s90", "s91", "s92", "s93", "s94", "s95", "vcc", "scc", "memory"
namespace mk {
constexpr int LDS_BYTES = 163840;
struct Args { const void* in[9]; void* out; void* ws; };
__global__ __launch_bounds__(256, 1) void mega_k(const Args a) {
    extern __shared__ __attribute__((aligned(16))) unsigned char shm[];
    const unsigned ldsb = (unsigned)(size_t)(LAS unsigned char*)shm;
    asm volatile(MK_ASM_BODY
                 :
                 : "s"(__builtin_amdgcn_kernarg_segment_ptr()), "s"(blockIdx.x), "v"(threadIdx.x), "s"(ldsb)
                 : MK_ASM_CLOBBERS);
}
}
extern "C" void kernel_launch(void* const* d_in, const int* in_sizes, int n_in, void* d_out, int out_size, void* d_ws, size_t ws_size, hipStream_t stream) {
    (void)hipFuncSetAttribute((const void*)mk::mega_k, hipFuncAttributeMaxDynamicSharedMemorySize, mk::LDS_BYTES);
    mk::Args a;
    for (int i = 0; i < 9; ++i) a.in[i] = d_in[i];
    a.out = d_out; a.ws = (char*)d_ws + ((ws_size - WS_END) & ~(size_t)0x1fffff);
    mk::mega_k<<<256, 256, mk::LDS_BYTES, stream>>>(a);
}
```

```cpp
#include <hip/hip_runtime.h>
#include <stdint.h>
#define LAS __attribute__((address_space(3)))
constexpr size_t LAY_TS = 17408, LAY_HS = 557824, LAY_CRS = 266240, LAY_ORS = 69632, WS_K = 0, WS_V = 17850368, WS_CTX = 35700736, WS_OB = 52740096, WS_FL = 65585152, LAY_EXD = 29950080, WS_END = 87147648;
#define MK_ASM_BODY \
"s_load_dwordx16 s[8:23], %0, 0x0\n" \
"s_load_dwordx4 s[24:27], %0, 0x40\n" \
"s_load_dwordx2 s[28:29], %0, 0x50\n" \
"v_readfirstlane_b32 s61, %2\n" \
"s_lshr_b32 s61, s61, 6\n" \
"v_mbcnt_lo_u32_b32 v52, -1, 0\n" \
"v_mbcnt_hi_u32_b32 v52, -1, v52\n" \
"v_lshlrev_b32 v248, 4, v52\n" \
"s_getpc_b64 s[64:65]\n" \
"Lpc_%=:\n" \
"s_lshr_b32 s70, %1, 3\n" \
"s_lshl_b32 s70, s70, 2\n" \
"s_add_u32 s70, s70, s61\n" \
"s_and_b32 s71, s70, 127\n" \
"s_lshl_b32 s71, s71, 10\n" \
"v_lshl_add_u32 v53, v248, 0, s71\n" \
"s_mov_b32 s71, Lcode_end_%=-Lpc_%=-4\n" \
"v_min_u32 v53, s71, v53\n" \
"global_load_dword v253, v53, s[64:65]\n" \
"s_and_b32 s62, %1, 7\n" \
"s_lshl_b32 s62, s62, 2\n" \
"s_lshr_b32 s70, %1, 6\n" \
"s_add_u32 s62, s62, s70\n" \
"s_bfe_u32 s63, %1, 0x30003\n" \
"s_lshl_b32 s87, s61, 13\n" \
"v_bfe_u32 v53, v52, 4, 1\n" \
"v_bfe_u32 v54, v52, 2, 2\n" \
"v_lshl_add_u32 v54, v53, 3, v54\n" \
"s_lshl_b32 s71, s61, 4\n" \
"v_add_u32 v54, s71, v54\n" \
"v_lshlrev_b32 v54, 13, v54\n" \
"v_lshlrev_b32 v53, 1, v53\n" \
"v_and_b32 v55, 3, v52\n" \
"v_xor_b32 v53, v53, v55\n" \
"v_lshl_add_u32 v54, v53, 4, v54\n" \
"v_lshrrev_b32 v53, 5, v52\n" \
"v_lshl_add_u32 v54, v53, 6, v54\n" \
"v_add_u32 v40, 4096, v54\n" \
"v_add_u32 v41, 3072, v54\n" \
"v_add_u32 v42, 2176, v54\n" \
"v_add_u32 v43, 1152, v54\n" \
"v_add_u32 v44, 32768, v54\n" \
"v_add_u32 v45, 31744, v54\n" \
"v_add_u32 v46, 30848, v54\n" \
"v_add_u32 v47, 29824, v54\n" \
"v_lshrrev_b32 v55, 3, v52\n" \
"v_and_b32 v56, 7, v52\n" \
"v_xor_b32 v56, v56, v55\n" \
"v_lshlrev_b32 v56, 4, v56\n" \
"v_lshl_add_u32 v57, v55, 10, v56\n" \
"v_add_u32 v32, 528384, v57\n" \
"v_add_u32 v33, 535552, v57\n" \
"v_add_u32 v34, 542720, v57\n" \
"v_add_u32 v35, 549888, v57\n" \
"v_add_u32 v36, 0, v57\n" \
"v_add_u32 v37, 7168, v57\n" \
"v_add_u32 v38, 14336, v57\n" \
"v_add_u32 v39, 21504, v57\n" \
"s_lshl_b32 s88, s62, 8\n" \
"s_lshl_b32 s70, s63, 16\n" \
"s_and_b32 s71, s87, 0x4000\n" \
"s_lshl_b32 s71, s71, 1\n" \
"s_add_u32 s70, s70, s71\n" \
"s_andn2_b32 s71, 1, s61\n" \
"s_lshl_b32 s71, s71, 20\n" \
"s_add_u32 s70, s70, s71\n" \
"s_add_u32 s73, s63, s62\n" \
"s_and_b32 s73, s73, 1\n" \
"s_mov_b32 s74, 128\n" \
"s_cmp_lg_u32 s73, 0\n" \
"s_cselect_b64 exec, -1, 0\n" \
"v_swap_b32 v32, v40\n" \
"v_swap_b32 v33, v41\n" \
"v_swap_b32 v34, v42\n" \
"v_swap_b32 v35, v43\n" \
"v_swap_b32 v36, v44\n" \
"v_swap_b32 v37, v45\n" \
"v_swap_b32 v38, v46\n" \
"v_swap_b32 v39, v47\n" \
"s_mov_b64 exec, -1\n" \
"s_cselect_b32 s76, s70, s88\n" \
"s_cselect_b32 s77, s88, s70\n" \
"s_cselect_b32 s75, 524288, s74\n" \
"s_cselect_b32 s74, s74, 524288\n" \
"s_waitcnt lgkmcnt(0)\n" \
"s_cselect_b64 s[52:53], s[10:11], s[14:15]\n" \
"s_cselect_b64 s[54:55], s[14:15], s[10:11]\n" \
"s_cselect_b64 s[50:51], s[10:11], s[16:17]\n" \
"s_cselect_b64 s[40:41], s[16:17], s[10:11]\n" \
"s_add_u32 s52, s52, s76\n" \
"s_addc_u32 s53, s53, 0\n" \
"s_add_u32 s50, s50, s76\n" \
"s_addc_u32 s51, s51, 0\n" \
"s_add_u32 m0, s87, 36864\n" \
"s_nop 0\n" \
"global_load_lds_dwordx4 v40, s[52:53] offset:-4096\n" \
"s_add_u32 s54, s54, s77\n" \
"s_addc_u32 s55, s55, 0\n" \
"s_add_u32 s40, s40, s77\n" \
"s_addc_u32 s41, s41, 0\n" \
"global_load_lds_dwordx4 v41, s[50:51] offset:-3072\n" \
"global_load_lds_dwordx4 v42, s[52:53] offset:-2048\n" \
"global_load_lds_dwordx4 v43, s[50:51] offset:-1024\n" \
"global_load_lds_dwordx4 v44, s[52:53] offset:0\n" \
"global_load_lds_dwordx4 v45, s[50:51] offset:1024\n" \
"global_load_lds_dwordx4 v46, s[52:53] offset:2048\n" \
"global_load_lds_dwordx4 v47, s[50:51] offset:3072\n" \
"s_add_u32 s52, s52, s74\n" \
"s_addc_u32 s53, s53, 0\n" \
"s_add_u32 s50, s50, s74\n" \
"s_addc_u32 s51, s51, 0\n" \
"s_add_u32 m0, s87, 4096\n" \
"s_nop 0\n" \
"global_load_lds_dwordx4 v32, s[54:55] offset:-4096\n" \
"global_load_lds_dwordx4 v33, s[40:41] offset:-3072\n" \
"global_load_lds_dwordx4 v34, s[54:55] offset:-2048\n" \
"global_load_lds_dwordx4 v35, s[40:41] offset:-1024\n" \
"global_load_lds_dwordx4 v36, s[54:55] offset:0\n" \
"global_load_lds_dwordx4 v37, s[40:41] offset:1024\n" \
"global_load_lds_dwordx4 v38, s[54:55] offset:2048\n" \
"global_load_lds_dwordx4 v39, s[40:41] offset:3072\n" \
"s_add_u32 s54, s54, s75\n" \
"s_addc_u32 s55, s55, 0\n" \
"s_add_u32 s40, s40, s75\n" \
"s_addc_u32 s41, s41, 0\n" \
"s_mov_b64 s[56:57], s[28:29]\n" \
"s_and_b32 s57, s57, 0xffff\n" \
"s_mov_b32 s58, 87147648\n" \
"s_mov_b32 s59, 0x20000\n" \
"s_lshl_b32 s66, %1, 2\n" \
"s_add_u32 s66, s66, s61\n" \
"s_lshl_b32 s66, s66, 10\n" \
"s_lshr_b32 s67, %1, 3\n" \
"s_lshl_b32 s67, s67, 2\n" \
"s_add_u32 s67, s67, s61\n" \
"s_lshl_b32 s67, s67, 13\n" \
"s_and_b32 s64, %1, 7\n" \
"s_lshl_b32 s64, s64, 10\n" \
"s_add_u32 s67, s67, s64\n" \
"s_lshl_b32 s89, s63, 18\n" \
"s_lshl_b32 s95, s61, 12\n" \
"v_add_u32 v228, 4096, v54\n" \
"v_add_u32 v229, 3200, v54\n" \
"v_lshl_add_u32 v57, v55, 11, v56\n" \
"v_add_u32 v224, 0, v57\n" \
"v_add_u32 v225, 15360, v57\n" \
"v_add_u32 v226, 129024, v57\n" \
"v_add_u32 v227, 144384, v57\n" \
"s_lshl_b32 s70, s61, 15\n" \
"s_add_u32 s70, s70, 0\n" \
"s_add_u32 s71, s70, s89\n" \
"s_add_u32 s71, s71, 4096\n" \
"s_add_u32 s48, s8, s71\n" \
"s_addc_u32 s49, s9, 0\n" \
"s_sub_u32 s71, s70, s89\n" \
"s_add_u32 s71, s71, 3932160\n" \
"s_add_u32 s46, s8, s71\n" \
"s_addc_u32 s47, s9, 0\n" \
"s_add_u32 s70, s88, 0\n" \
"s_add_u32 s44, s12, s70\n" \
"s_addc_u32 s45, s13, 0\n" \
"s_add_u32 s42, s44, 30720\n" \
"s_addc_u32 s43, s45, 0\n" \
"s_lshr_b32 s70, s28, 31\n" \
"s_lshl_b32 s70, s70, 30\n" \
"s_sub_u32 s28, s28, s70\n" \
"v_add_u32 v250, s70, v248\n" \
"v_lshlrev_b32 v245, 2, v52\n" \
"v_mov_b32 v254, 0x30000\n" \
"v_lshrrev_b32 v55, 4, v52\n" \
"v_and_b32 v249, 0xf0, v248\n" \
"v_lshl_add_u32 v249, v55, 9, v249\n" \
"v_and_b32 v53, 15, v52\n" \
"s_lshl_b32 s70, s61, 4\n" \
"v_lshlrev_b32 v54, 2, v55\n" \
"v_sub_u32 v251, v53, v54\n" \
"v_add_u32 v251, s70, v251\n" \
"v_and_b32 v54, 7, v52\n" \
"v_xor_b32 v54, v54, v55\n" \
"v_lshlrev_b32 v53, 7, v53\n" \
"s_add_u32 s71, s87, 131072\n" \
"s_lshr_b32 s70, s61, 1\n" \
"s_lshl_b32 s70, s70, 14\n" \
"s_lshl_b32 s76, s73, 15\n" \
"s_add_u32 s70, s70, s76\n" \
"v_xor_b32 v56, 0, v54\n" \
"v_lshl_add_u32 v56, v56, 4, v53\n" \
"v_add_u32 v230, s71, v56\n" \
"v_add_u32 v48, s70, v56\n" \
"v_xor_b32 v56, 4, v54\n" \
"v_lshl_add_u32 v56, v56, 4, v53\n" \
"v_add_u32 v231, s71, v56\n" \
"v_add_u32 v49, s70, v56\n" \
"v_and_b32 v53, 3, v52\n" \
"v_lshlrev_b32 v53, 3, v53\n" \
"v_bfe_u32 v54, v52, 2, 2\n" \
"v_lshl_add_u32 v53, v54, 6, v53\n" \
"v_bfe_u32 v54, v52, 4, 1\n" \
"v_lshl_add_u32 v53, v54, 8, v53\n" \
"v_lshl_add_u32 v53, v54, 5, v53\n" \
"v_lshrrev_b32 v54, 5, v52\n" \
"s_and_b32 s70, s61, 1\n" \
"s_lshl_b32 s70, s70, 11\n" \
"s_xor_b32 s76, s76, 0x8000\n" \
"s_add_u32 s70, s70, s76\n" \
"v_lshl_add_u32 v56, v54, 13, v53\n" \
"v_add_u32 v50, s70, v56\n" \
"v_xor_b32 v51, 32, v50\n" \
"v_lshl_add_u32 v232, v54, 12, v53\n" \
"v_xor_b32 v233, 32, v232\n" \
"s_mov_b64 exec, 0\n" \
"s_cmp_eq_u32 s61, 0\n" \
"s_cbranch_scc1 Lst0x0_%=\n" \
"s_cmp_eq_u32 s61, 1\n" \
"s_cbranch_scc1 Lst0x1_%=\n" \
"s_cmp_eq_u32 s61, 2\n" \
"s_cbranch_scc1 Lst0x2_%=\n" \
"s_branch Lst0x3_%=\n" \
"Lcb0_%=:\n" \
"s_mov_b64 exec, -1\n" \
"s_add_u32 m0, s87, 135168\n" \
"s_nop 0\n" \
"global_load_lds_dwordx4 v40, s[52:53] offset:-4096\n" \
"global_load_lds_dwordx4 v41, s[50:51] offset:-3072\n" \
"global_load_lds_dwordx4 v42, s[52:53] offset:-2048\n" \
"global_load_lds_dwordx4 v43, s[50:51] offset:-1024\n" \
"global_load_lds_dwordx4 v44, s[52:53] offset:0\n" \
"global_load_lds_dwordx4 v45, s[50:51] offset:1024\n" \
"global_load_lds_dwordx4 v46, s[52:53] offset:2048\n" \
"global_load_lds_dwordx4 v47, s[50:51] offset:3072\n" \
"s_add_u32 s52, s52, s74\n" \
"s_addc_u32 s53, s53, 0\n" \
"s_add_u32 s50, s50, s74\n" \
"s_addc_u32 s51, s51, 0\n" \
"s_waitcnt vmcnt(8)\n" \
"s_barrier\n" \
"ds_read_b128 v[64:67], v48 offset:0\n" \
"ds_read_b64_tr_b16 v[128:129], v50 offset:0\n" \
"ds_read_b64_tr_b16 v[130:131], v50 offset:4096\n" \
"ds_read_b128 v[68:71], v49 offset:0\n" \
"ds_read_b64_tr_b16 v[132:133], v50 offset:16384\n" \
"ds_read_b64_tr_b16 v[134:135], v50 offset:20480\n" \
"ds_read_b128 v[72:75], v48 offset:2048\n" \
"ds_read_b64_tr_b16 v[136:137], v51 offset:0\n" \
"ds_read_b64_tr_b16 v[138:139], v51 offset:4096\n" \
"ds_read_b128 v[76:79], v49 offset:2048\n" \
"ds_read_b64_tr_b16 v[140:141], v51 offset:16384\n" \
"ds_read_b64_tr_b16 v[142:143], v51 offset:20480\n" \
"ds_read_b64_tr_b16 v[144:145], v50 offset:512\n" \
"ds_read_b64_tr_b16 v[146:147], v50 offset:4608\n" \
"ds_read_b64_tr_b16 v[148:149], v50 offset:16896\n" \
"ds_read_b64_tr_b16 v[150:151], v50 offset:20992\n" \
"ds_read_b64_tr_b16 v[152:153], v51 offset:512\n" \
"ds_read_b64_tr_b16 v[154:155], v51 offset:4608\n" \
"ds_read_b64_tr_b16 v[156:157], v51 offset:16896\n" \
"ds_read_b64_tr_b16 v[158:159], v51 offset:20992\n" \
"s_waitcnt lgkmcnt(14)\n" \
"v_mfma_f32_16x16x32_bf16 a[0:3], v[128:131], v[64:67], 0\n" \
"ds_read_b128 v[120:123], v48 offset:14336\n" \
"s_add_u32 m0, s87, 69632\n" \
"v_mfma_f32_16x16x32_bf16 a[0:3], v[132:135], v[68:71], a[0:3]\n" \
"s_waitcnt lgkmcnt(9)\n" \
"ds_read_b128 v[80:83], v48 offset:4096\n" \
"global_load_lds_dwordx4 v32, s[54:55] offset:-4096\n" \
"v_mfma_f32_16x16x32_bf16 a[4:7], v[136:139], v[64:67], 0\n" \
"ds_read_b128 v[124:127], v49 offset:14336\n" \
"v_mfma_f32_16x16x32_bf16 a[4:7], v[140:143], v[68:71], a[4:7]\n" \
"s_waitcnt lgkmcnt(7)\n" \
"ds_read_b128 v[84:87], v49 offset:4096\n" \
"v_mfma_f32_16x16x32_bf16 a[8:11], v[144:147], v[64:67], 0\n" \
"ds_read_b64_tr_b16 v[160:161], v50 offset:1024\n" \
"global_load_lds_dwordx4 v33, s[40:41] offset:-3072\n" \
"v_mfma_f32_16x16x32_bf16 a[8:11], v[148:151], v[68:71], a[8:11]\n" \
"s_waitcnt lgkmcnt(4)\n" \
"ds_read_b128 v[88:91], v48 offset:6144\n" \
"v_mfma_f32_16x16x32_bf16 a[12:15], v[152:155], v[64:67], 0\n" \
"ds_read_b64_tr_b16 v[162:163], v50 offset:5120\n" \
"v_mfma_f32_16x16x32_bf16 a[12:15], v[156:159], v[68:71], a[12:15]\n" \
"ds_read_b128 v[92:95], v49 offset:6144\n" \
"global_load_lds_dwordx4 v34, s[54:55] offset:-2048\n" \
"v_mfma_f32_16x16x32_bf16 a[44:47], v[152:155], v[72:75], 0\n" \
"ds_read_b64_tr_b16 v[164:165], v50 offset:17408\n" \
"v_mfma_f32_16x16x32_bf16 a[44:47], v[156:159], v[76:79], a[44:47]\n" \
"ds_read_b128 v[96:99], v48 offset:8192\n" \
"v_mfma_f32_16x16x32_bf16 a[40:43], v[144:147], v[72:75], 0\n" \
"ds_read_b64_tr_b16 v[166:167], v50 offset:21504\n" \
"global_load_lds_dwordx4 v35, s[40:41] offset:-1024\n" \
"v_mfma_f32_16x16x32_bf16 a[40:43], v[148:151], v[76:79], a[40:43]\n" \
"ds_read_b128 v[100:103], v49 offset:8192\n" \
"v_mfma_f32_16x16x32_bf16 a[36:39], v[136:139], v[72:75], 0\n" \
"ds_read_b64_tr_b16 v[168:169], v51 offset:1024\n" \
"v_mfma_f32_16x16x32_bf16 a[36:39], v[140:143], v[76:79], a[36:39]\n" \
"ds_read_b128 v[104:107], v48 offset:10240\n" \
"global_load_lds_dwordx4 v36, s[54:55] offset:0\n" \
"v_mfma_f32_16x16x32_bf16 a[32:35], v[128:131], v[72:75], 0\n" \
"ds_read_b64_tr_b16 v[170:171], v51 offset:5120\n" \
"v_mfma_f32_16x16x32_bf16 a[32:35], v[132:135], v[76:79], a[32:35]\n" \
"s_waitcnt lgkmcnt(4)\n" \
"ds_read_b128 v[108:111], v49 offset:10240\n" \
"v_mfma_f32_16x16x32_bf16 a[64:67], v[128:131], v[80:83], 0\n" \
"ds_read_b64_tr_b16 v[172:173], v51 offset:17408\n" \
"global_load_lds_dwordx4 v37, s[40:41] offset:1024\n" \
"v_mfma_f32_16x16x32_bf16 a[64:67], v[132:135], v[84:87], a[64:67]\n" \
"ds_read_b128 v[112:115], v48 offset:12288\n" \
"v_mfma_f32_16x16x32_bf16 a[68:71], v[136:139], v[80:83], 0\n" \
"ds_read_b64_tr_b16 v[174:175], v51 offset:21504\n" \
"v_mfma_f32_16x16x32_bf16 a[68:71], v[140:143], v[84:87], a[68:71]\n" \
"ds_read_b128 v[116:119], v49 offset:12288\n" \
"global_load_lds_dwordx4 v38, s[54:55] offset:2048\n" \
"v_mfma_f32_16x16x32_bf16 a[72:75], v[144:147], v[80:83], 0\n" \
"ds_read_b64_tr_b16 v[176:177], v50 offset:1536\n" \
"v_mfma_f32_16x16x32_bf16 a[72:75], v[148:151], v[84:87], a[72:75]\n" \
"global_load_lds_dwordx4 v39, s[40:41] offset:3072\n" \
"v_mfma_f32_16x16x32_bf16 a[76:79], v[152:155], v[80:83], 0\n" \
"s_add_u32 s54, s54, s75\n" \
"s_addc_u32 s55, s55, 0\n" \
"ds_read_b64_tr_b16 v[178:179], v50 offset:5632\n" \
"v_mfma_f32_16x16x32_bf16 a[76:79], v[156:159], v[84:87], a[76:79]\n" \
"s_add_u32 s40, s40, s75\n" \
"s_addc_u32 s41, s41, 0\n" \
"s_mov_b32 s90, 65593344\n" \
"v_mfma_f32_16x16x32_bf16 a[108:111], v[152:155], v[88:91], 0\n" \
"ds_read_b64_tr_b16 v[180:181], v50 offset:17920\n" \
"v_mfma_f32_16x16x32_bf16 a[108:111], v[156:159], v[92:95], a[108:111]\n" \
"buffer_load_dword v244, off, s[56:59], s90 sc1\n" \
"v_mfma_f32_16x16x32_bf16 a[104:107], v[144:147], v[88:91], 0\n" \
"ds_read_b64_tr_b16 v[182:183], v50 offset:22016\n" \
"v_mfma_f32_16x16x32_bf16 a[104:107], v[148:151], v[92:95], a[104:107]\n" \
"s_add_u32 m0, s87, 102400\n" \
"v_mfma_f32_16x16x32_bf16 a[100:103], v[136:139], v[88:91], 0\n" \
"ds_read_b64_tr_b16 v[184:185], v51 offset:1536\n" \
"global_load_lds_dwordx4 v40, s[52:53] offset:-4096\n" \
"v_mfma_f32_16x16x32_bf16 a[100:103], v[140:143], v[92:95], a[100:103]\n" \
"v_mfma_f32_16x16x32_bf16 a[96:99], v[128:131], v[88:91], 0\n" \
"ds_read_b64_tr_b16 v[186:187], v51 offset:5632\n" \
"v_mfma_f32_16x16x32_bf16 a[96:99], v[132:135], v[92:95], a[96:99]\n" \
"s_waitcnt lgkmcnt(2)\n" \
"global_load_lds_dwordx4 v41, s[50:51] offset:-3072\n" \
"v_mfma_f32_16x16x32_bf16 a[128:131], v[128:131], v[96:99], 0\n" \
"ds_read_b64_tr_b16 v[188:189], v51 offset:17920\n" \
"v_mfma_f32_16x16x32_bf16 a[128:131], v[132:135], v[100:103], a[128:131]\n" \
"v_mfma_f32_16x16x32_bf16 a[132:135], v[136:139], v[96:99], 0\n" \
"ds_read_b64_tr_b16 v[190:191], v51 offset:22016\n" \
"global_load_lds_dwordx4 v42, s[52:53] offset:-2048\n" \
"v_mfma_f32_16x16x32_bf16 a[132:135], v[140:143], v[100:103], a[132:135]\n" \
"v_mfma_f32_16x16x32_bf16 a[136:139], v[144:147], v[96:99], 0\n" \
"v_mfma_f32_16x16x32_bf16 a[136:139], v[148:151], v[100:103], a[136:139]\n" \
"global_load_lds_dwordx4 v43, s[50:51] offset:-1024\n" \
"v_mfma_f32_16x16x32_bf16 a[140:143], v[152:155], v[96:99], 0\n" \
"v_mfma_f32_16x16x32_bf16 a[140:143], v[156:159], v[100:103], a[140:143]\n" \
"global_load_lds_dwordx4 v44, s[52:53] offset:0\n" \
"v_mfma_f32_16x16x32_bf16 a[172:175], v[152:155], v[104:107], 0\n" \
"s_mul_i32 s76, s73, 0x8000\n" \
"s_add_u32 s77, s76, 0x10000\n" \
"s_sub_u32 s78, 0x18000, s76\n" \
"v_mfma_f32_16x16x32_bf16 a[172:175], v[156:159], v[108:111], a[172:175]\n" \
"v_add_u32 v48, s77, v48\n" \
"v_add_u32 v49, s77, v49\n" \
"v_mfma_f32_16x16x32_bf16 a[168:171], v[144:147], v[104:107], 0\n" \
"v_add_u32 v50, s78, v50\n" \
"v_add_u32 v51, s78, v51\n" \
"v_mfma_f32_16x16x32_bf16 a[168:171], v[148:151], v[108:111], a[168:171]\n" \
"global_load_lds_dwordx4 v45, s[50:51] offset:1024\n" \
"v_mfma_f32_16x16x32_bf16 a[164:167], v[136:139], v[104:107], 0\n" \
"v_mfma_f32_16x16x32_bf16 a[164:167], v[140:143], v[108:111], a[164:167]\n" \
"v_mfma_f32_16x16x32_bf16 a[160:163], v[128:131], v[104:107], 0\n" \
"global_load_lds_dwordx4 v46, s[52:53] offset:2048\n" \
"v_mfma_f32_16x16x32_bf16 a[160:163], v[132:135], v[108:111], a[160:163]\n" \
"v_mfma_f32_16x16x32_bf16 a[192:195], v[128:131], v[112:115], 0\n" \
"v_mfma_f32_16x16x32_bf16 a[192:195], v[132:135], v[116:119], a[192:195]\n" \
"global_load_lds_dwordx4 v47, s[50:51] offset:3072\n" \
"v_mfma_f32_16x16x32_bf16 a[196:199], v[136:139], v[112:115], 0\n" \
"s_add_u32 s52, s52, s74\n" \
"s_addc_u32 s53, s53, 0\n" \
"v_mfma_f32_16x16x32_bf16 a[196:199], v[140:143], v[116:119], a[196:199]\n" \
"s_add_u32 s50, s50, s74\n" \
"s_addc_u32 s51, s51, 0\n" \
"v_mfma_f32_16x16x32_bf16 a[200:203], v[144:147], v[112:115], 0\n" \
"v_mfma_f32_16x16x32_bf16 a[200:203], v[148:151], v[116:119], a[200:203]\n" \
"v_mfma_f32_16x16x32_bf16 a[204:207], v[152:155], v[112:115], 0\n" \
"v_mfma_f32_16x16x32_bf16 a[204:207], v[156:159], v[116:119], a[204:207]\n" \
"v_mfma_f32_16x16x32_bf16 a[236:239], v[152:155], v[120:123], 0\n" \
"v_mfma_f32_16x16x32_bf16 a[236:239], v[156:159], v[124:127], a[236:239]\n" \
"v_mfma_f32_16x16x32_bf16 a[232:235], v[144:147], v[120:123], 0\n" \
"v_mfma_f32_16x16x32_bf16 a[232:235], v[148:151], v[124:127], a[232:235]\n" \
"v_mfma_f32_16x16x32_bf16 a[228:231], v[136:139], v[120:123], 0\n" \
"v_mfma_f32_16x16x32_bf16 a[228:231], v[140:143], v[124:127], a[228:231]\n" \
"s_waitcnt vmcnt(9) lgkmcnt(0)\n" \
"v_mfma_f32_16x16x32_bf16 a[224:227], v[128:131], v[120:123], 0\n" \
"v_mfma_f32_16x16x32_bf16 a[224:227], v[132:135], v[124:127], a[224:227]\n" \
"s_barrier\n" \
"v_mfma_f32_16x16x32_bf16 a[16:19], v[64:67], v[160:163], 0\n" \
"s_add_u32 m0, s87, 36864\n" \
"ds_read_b64_tr_b16 v[128:129], v50 offset:0\n" \
"v_mfma_f32_16x16x32_bf16 a[16:19], v[68:71], v[164:167], a[16:19]\n" \
"global_load_lds_dwordx4 v32, s[54:55] offset:-4096\n" \
"v_mfma_f32_16x16x32_bf16 a[20:23], v[64:67], v[168:171], 0\n" \
"ds_read_b64_tr_b16 v[130:131], v50 offset:4096\n" \
"v_mfma_f32_16x16x32_bf16 a[20:23], v[68:71], v[172:175], a[20:23]\n" \
"v_mfma_f32_16x16x32_bf16 a[24:27], v[64:67], v[176:179], 0\n" \
"ds_read_b64_tr_b16 v[132:133], v50 offset:16384\n" \
"v_mfma_f32_16x16x32_bf16 a[24:27], v[68:71], v[180:183], a[24:27]\n" \
"global_load_lds_dwordx4 v33, s[40:41] offset:-3072\n" \
"v_mfma_f32_16x16x32_bf16 a[28:31], v[64:67], v[184:187], 0\n" \
"ds_read_b64_tr_b16 v[134:135], v50 offset:20480\n" \
"v_mfma_f32_16x16x32_bf16 a[28:31], v[68:71], v[188:191], a[28:31]\n" \
"v_mfma_f32_16x16x32_bf16 a[60:63], v[72:75], v[184:187], 0\n" \
"ds_read_b128 v[64:67], v48 offset:0\n" \
"v_mfma_f32_16x16x32_bf16 a[60:63], v[76:79], v[188:191], a[60:63]\n" \
"global_load_lds_dwordx4 v34, s[54:55] offset:-2048\n" \
"v_mfma_f32_16x16x32_bf16 a[56:59], v[72:75], v[176:179], 0\n" \
"ds_read_b64_tr_b16 v[136:137], v51 offset:0\n" \
"v_mfma_f32_16x16x32_bf16 a[56:59], v[76:79], v[180:183], a[56:59]\n" \
"v_mfma_f32_16x16x32_bf16 a[52:55], v[72:75], v[168:171], 0\n" \
"ds_read_b128 v[68:71], v49 offset:0\n" \
"v_mfma_f32_16x16x32_bf16 a[52:55], v[76:79], v[172:175], a[52:55]\n" \
"global_load_lds_dwordx4 v35, s[40:41] offset:-1024\n" \
"v_mfma_f32_16x16x32_bf16 a[48:51], v[72:75], v[160:163], 0\n" \
"ds_read_b64_tr_b16 v[138:139], v51 offset:4096\n" \
"v_mfma_f32_16x16x32_bf16 a[48:51], v[76:79], v[164:167], a[48:51]\n" \
"v_mfma_f32_16x16x32_bf16 a[80:83], v[80:83], v[160:163], 0\n" \
"ds_read_b128 v[72:75], v48 offset:2048\n" \
"v_mfma_f32_16x16x32_bf16 a[80:83], v[84:87], v[164:167], a[80:83]\n" \
"global_load_lds_dwordx4 v36, s[54:55] offset:0\n" \
"v_mfma_f32_16x16x32_bf16 a[84:87], v[80:83], v[168:171], 0\n" \
"ds_read_b64_tr_b16 v[140:141], v51 offset:16384\n" \
"v_mfma_f32_16x16x32_bf16 a[84:87], v[84:87], v[172:175], a[84:87]\n" \
"v_mfma_f32_16x16x32_bf16 a[88:91], v[80:83], v[176:179], 0\n" \
"ds_read_b128 v[76:79], v49 offset:2048\n" \
"v_mfma_f32_16x16x32_bf16 a[88:91], v[84:87], v[180:183], a[88:91]\n" \
"global_load_lds_dwordx4 v37, s[40:41] offset:1024\n" \
"v_mfma_f32_16x16x32_bf16 a[92:95], v[80:83], v[184:187], 0\n" \
"ds_read_b64_tr_b16 v[142:143], v51 offset:20480\n" \
"v_mfma_f32_16x16x32_bf16 a[92:95], v[84:87], v[188:191], a[92:95]\n" \
"v_mfma_f32_16x16x32_bf16 a[124:127], v[88:91], v[184:187], 0\n" \
"ds_read_b128 v[80:83], v48 offset:4096\n" \
"v_mfma_f32_16x16x32_bf16 a[124:127], v[92:95], v[188:191], a[124:127]\n" \
"global_load_lds_dwordx4 v38, s[54:55] offset:2048\n" \
"v_mfma_f32_16x16x32_bf16 a[120:123], v[88:91], v[176:179], 0\n" \
"ds_read_b64_tr_b16 v[144:145], v50 offset:512\n" \
"v_mfma_f32_16x16x32_bf16 a[120:123], v[92:95], v[180:183], a[120:123]\n" \
"v_mfma_f32_16x16x32_bf16 a[116:119], v[88:91], v[168:171], 0\n" \
"ds_read_b128 v[84:87], v49 offset:4096\n" \
"global_load_lds_dwordx4 v39, s[40:41] offset:3072\n" \
"v_mfma_f32_16x16x32_bf16 a[116:119], v[92:95], v[172:175], a[116:119]\n" \
"s_add_u32 s54, s54, s75\n" \
"s_addc_u32 s55, s55, 0\n" \
"v_mfma_f32_16x16x32_bf16 a[112:115], v[88:91], v[160:163], 0\n" \
"s_add_u32 s40, s40, s75\n" \
"s_addc_u32 s41, s41, 0\n" \
"ds_read_b64_tr_b16 v[146:147], v50 offset:4608\n" \
"v_mfma_f32_16x16x32_bf16 a[112:115], v[92:95], v[164:167], a[112:115]\n" \
"s_add_u32 s68, s66, 0x0\n" \
"s_add_u32 s64, s10, s68\n" \
"s_addc_u32 s65, s11, 0\n" \
"v_mfma_f32_16x16x32_bf16 a[144:147], v[96:99], v[160:163], 0\n" \
"ds_read_b128 v[88:91], v48 offset:6144\n" \
"v_mfma_f32_16x16x32_bf16 a[144:147], v[100:103], v[164:167], a[144:147]\n" \
"global_load_dword v253, v248, s[64:65]\n" \
"v_mfma_f32_16x16x32_bf16 a[148:151], v[96:99], v[168:171], 0\n" \
"ds_read_b64_tr_b16 v[148:149], v50 offset:16896\n" \
"v_mfma_f32_16x16x32_bf16 a[148:151], v[100:103], v[172:175], a[148:151]\n" \
"v_mfma_f32_16x16x32_bf16 a[152:155], v[96:99], v[176:179], 0\n" \
"ds_read_b128 v[92:95], v49 offset:6144\n" \
"v_mfma_f32_16x16x32_bf16 a[152:155], v[100:103], v[180:183], a[152:155]\n" \
"v_mfma_f32_16x16x32_bf16 a[156:159], v[96:99], v[184:187], 0\n" \
"ds_read_b64_tr_b16 v[150:151], v50 offset:20992\n" \
"v_mfma_f32_16x16x32_bf16 a[156:159], v[100:103], v[188:191], a[156:159]\n" \
"v_mfma_f32_16x16x32_bf16 a[188:191], v[104:107], v[184:187], 0\n" \
"ds_read_b128 v[96:99], v48 offset:8192\n" \
"v_mfma_f32_16x16x32_bf16 a[188:191], v[108:111], v[188:191], a[188:191]\n" \
"v_mfma_f32_16x16x32_bf16 a[184:187], v[104:107], v[176:179], 0\n" \
"ds_read_b64_tr_b16 v[152:153], v51 offset:512\n" \
"v_mfma_f32_16x16x32_bf16 a[184:187], v[108:111], v[180:183], a[184:187]\n" \
"v_mfma_f32_16x16x32_bf16 a[180:183], v[104:107], v[168:171], 0\n" \
"ds_read_b128 v[100:103], v49 offset:8192\n" \
"v_mfma_f32_16x16x32_bf16 a[180:183], v[108:111], v[172:175], a[180:183]\n" \
"v_mfma_f32_16x16x32_bf16 a[176:179], v[104:107], v[160:163], 0\n" \
"ds_read_b64_tr_b16 v[154:155], v51 offset:4608\n" \
"v_mfma_f32_16x16x32_bf16 a[176:179], v[108:111], v[164:167], a[176:179]\n" \
"v_mfma_f32_16x16x32_bf16 a[208:211], v[112:115], v[160:163], 0\n" \
"ds_read_b128 v[104:107], v48 offset:10240\n" \
"v_mfma_f32_16x16x32_bf16 a[208:211], v[116:119], v[164:167], a[208:211]\n" \
"v_mfma_f32_16x16x32_bf16 a[212:215], v[112:115], v[168:171], 0\n" \
"ds_read_b64_tr_b16 v[156:157], v51 offset:16896\n" \
"v_mfma_f32_16x16x32_bf16 a[212:215], v[116:119], v[172:175], a[212:215]\n" \
"v_mfma_f32_16x16x32_bf16 a[216:219], v[112:115], v[176:179], 0\n" \
"ds_read_b128 v[108:111], v49 offset:10240\n" \
"v_mfma_f32_16x16x32_bf16 a[216:219], v[116:119], v[180:183], a[216:219]\n" \
"v_mfma_f32_16x16x32_bf16 a[220:223], v[112:115], v[184:187], 0\n" \
"ds_read_b64_tr_b16 v[158:159], v51 offset:20992\n" \
"v_mfma_f32_16x16x32_bf16 a[220:223], v[116:119], v[188:191], a[220:223]\n" \
"v_mfma_f32_16x16x32_bf16 a[252:255], v[120:123], v[184:187], 0\n" \
"ds_read_b128 v[112:115], v48 offset:12288\n" \
"v_mfma_f32_16x16x32_bf16 a[252:255], v[124:127], v[188:191], a[252:255]\n" \
"v_mfma_f32_16x16x32_bf16 a[248:251], v[120:123], v[176:179], 0\n" \
"v_mfma_f32_16x16x32_bf16 a[248:251], v[124:127], v[180:183], a[248:251]\n" \
"v_mfma_f32_16x16x32_bf16 a[244:247], v[120:123], v[168:171], 0\n" \
"ds_read_b128 v[116:119], v49 offset:12288\n" \
"v_mfma_f32_16x16x32_bf16 a[244:247], v[124:127], v[172:175], a[244:247]\n" \
"v_mfma_f32_16x16x32_bf16 a[240:243], v[120:123], v[160:163], 0\n" \
"v_mfma_f32_16x16x32_bf16 a[240:243], v[124:127], v[164:167], a[240:243]\n" \
"s_waitcnt lgkmcnt(1)\n" \
"v_mfma_f32_16x16x32_bf16 a[0:3], v[128:131], v[64:67], a[0:3]\n" \
"ds_read_b128 v[120:123], v48 offset:14336\n" \
"v_mfma_f32_16x16x32_bf16 a[0:3], v[132:135], v[68:71], a[0:3]\n" \
"v_mfma_f32_16x16x32_bf16 a[4:7], v[136:139], v[64:67], a[4:7]\n" \
"ds_read_b128 v[124:127], v49 offset:14336\n" \
"v_mfma_f32_16x16x32_bf16 a[4:7], v[140:143], v[68:71], a[4:7]\n" \
"s_add_u32 s68, s66, 0x100000\n" \
"v_mfma_f32_16x16x32_bf16 a[8:11], v[144:147], v[64:67], a[8:11]\n" \
"s_add_u32 s64, s10, s68\n" \
"s_addc_u32 s65, s11, 0\n" \
"ds_read_b64_tr_b16 v[160:161], v50 offset:1024\n" \
"v_mfma_f32_16x16x32_bf16 a[8:11], v[148:151], v[68:71], a[8:11]\n" \
"global_load_dword v253, v248, s[64:65]\n" \
"v_mfma_f32_16x16x32_bf16 a[12:15], v[152:155], v[64:67], a[12:15]\n" \
"s_add_u32 m0, s87, 4096\n" \
"ds_read_b64_tr_b16 v[162:163], v50 offset:5120\n" \
"v_mfma_f32_16x16x32_bf16 a[12:15], v[156:159], v[68:71], a[12:15]\n" \
"global_load_lds_dwordx4 v40, s[52:53] offset:-4096\n" \
"v_mfma_f32_16x16x32_bf16 a[44:47], v[152:155], v[72:75], a[44:47]\n" \
"ds_read_b64_tr_b16 v[164:165], v50 offset:17408\n" \
"v_mfma_f32_16x16x32_bf16 a[44:47], v[156:159], v[76:79], a[44:47]\n" \
"v_mfma_f32_16x16x32_bf16 a[40:43], v[144:147], v[72:75], a[40:43]\n" \
"ds_read_b64_tr_b16 v[166:167], v50 offset:21504\n" \
"v_mfma_f32_16x16x32_bf16 a[40:43], v[148:151], v[76:79], a[40:43]\n" \
"global_load_lds_dwordx4 v41, s[50:51] offset:-3072\n" \
"v_mfma_f32_16x16x32_bf16 a[36:39], v[136:139], v[72:75], a[36:39]\n" \
"ds_read_b64_tr_b16 v[168:169], v51 offset:1024\n" \
"v_mfma_f32_16x16x32_bf16 a[36:39], v[140:143], v[76:79], a[36:39]\n" \
"v_mfma_f32_16x16x32_bf16 a[32:35], v[128:131], v[72:75], a[32:35]\n" \
"ds_read_b64_tr_b16 v[170:171], v51 offset:5120\n" \
"v_mfma_f32_16x16x32_bf16 a[32:35], v[132:135], v[76:79], a[32:35]\n" \
"global_load_lds_dwordx4 v42, s[52:53] offset:-2048\n" \
"v_mfma_f32_16x16x32_bf16 a[64:67], v[128:131], v[80:83], a[64:67]\n" \
"ds_read_b64_tr_b16 v[172:173], v51 offset:17408\n" \
"v_mfma_f32_16x16x32_bf16 a[64:67], v[132:135], v[84:87], a[64:67]\n" \
"v_mfma_f32_16x16x32_bf16 a[68:71], v[136:139], v[80:83], a[68:71]\n" \
"ds_read_b64_tr_b16 v[174:175], v51 offset:21504\n" \
"v_mfma_f32_16x16x32_bf16 a[68:71], v[140:143], v[84:87], a[68:71]\n" \
"global_load_lds_dwordx4 v43, s[50:51] offset:-1024\n" \
"v_mfma_f32_16x16x32_bf16 a[72:75], v[144:147], v[80:83], a[72:75]\n" \
"ds_read_b64_tr_b16 v[176:177], v50 offset:1536\n" \
"v_mfma_f32_16x16x32_bf16 a[72:75], v[148:151], v[84:87], a[72:75]\n" \
"v_mfma_f32_16x16x32_bf16 a[76:79], v[152:155], v[80:83], a[76:79]\n" \
"ds_read_b64_tr_b16 v[178:179], v50 offset:5632\n" \
"v_mfma_f32_16x16x32_bf16 a[76:79], v[156:159], v[84:87], a[76:79]\n" \
"global_load_lds_dwordx4 v44, s[52:53] offset:0\n" \
"v_mfma_f32_16x16x32_bf16 a[108:111], v[152:155], v[88:91], a[108:111]\n" \
"ds_read_b64_tr_b16 v[180:181], v50 offset:17920\n" \
"v_mfma_f32_16x16x32_bf16 a[108:111], v[156:159], v[92:95], a[108:111]\n" \
"v_mfma_f32_16x16x32_bf16 a[104:107], v[144:147], v[88:91], a[104:107]\n" \
"ds_read_b64_tr_b16 v[182:183], v50 offset:22016\n" \
"v_mfma_f32_16x16x32_bf16 a[104:107], v[148:151], v[92:95], a[104:107]\n" \
"global_load_lds_dwordx4 v45, s[50:51] offset:1024\n" \
"v_mfma_f32_16x16x32_bf16 a[100:103], v[136:139], v[88:91], a[100:103]\n" \
"ds_read_b64_tr_b16 v[184:185], v51 offset:1536\n" \
"v_mfma_f32_16x16x32_bf16 a[100:103], v[140:143], v[92:95], a[100:103]\n" \
"v_mfma_f32_16x16x32_bf16 a[96:99], v[128:131], v[88:91], a[96:99]\n" \
"ds_read_b64_tr_b16 v[186:187], v51 offset:5632\n" \
"v_mfma_f32_16x16x32_bf16 a[96:99], v[132:135], v[92:95], a[96:99]\n" \
"global_load_lds_dwordx4 v46, s[52:53] offset:2048\n" \
"v_mfma_f32_16x16x32_bf16 a[128:131], v[128:131], v[96:99], a[128:131]\n" \
"ds_read_b64_tr_b16 v[188:189], v51 offset:17920\n" \
"v_mfma_f32_16x16x32_bf16 a[128:131], v[132:135], v[100:103], a[128:131]\n" \
"v_mfma_f32_16x16x32_bf16 a[132:135], v[136:139], v[96:99], a[132:135]\n" \
"ds_read_b64_tr_b16 v[190:191], v51 offset:22016\n" \
"v_mfma_f32_16x16x32_bf16 a[132:135], v[140:143], v[100:103], a[132:135]\n" \
"global_load_lds_dwordx4 v47, s[50:51] offset:3072\n" \
"v_mfma_f32_16x16x32_bf16 a[136:139], v[144:147], v[96:99], a[136:139]\n" \
"s_add_u32 s52, s52, s74\n" \
"s_addc_u32 s53, s53, 0\n" \
"v_mfma_f32_16x16x32_bf16 a[136:139], v[148:151], v[100:103], a[136:139]\n" \
"s_add_u32 s50, s50, s74\n" \
"s_addc_u32 s51, s51, 0\n" \
"v_mfma_f32_16x16x32_bf16 a[140:143], v[152:155], v[96:99], a[140:143]\n" \
"v_mfma_f32_16x16x32_bf16 a[140:143], v[156:159], v[100:103], a[140:143]\n" \
"v_mfma_f32_16x16x32_bf16 a[172:175], v[152:155], v[104:107], a[172:175]\n" \
"s_mul_i32 s76, s73, 0x0\n" \
"s_add_u32 s77, s76, 0xffff8000\n" \
"s_sub_u32 s78, 0xffff8000, s76\n" \
"v_mfma_f32_16x16x32_bf16 a[172:175], v[156:159], v[108:111], a[172:175]\n" \
"v_add_u32 v48, s77, v48\n" \
"v_add_u32 v49, s77, v49\n" \
"v_mfma_f32_16x16x32_bf16 a[168:171], v[144:147], v[104:107], a[168:171]\n" \
"v_add_u32 v50, s78, v50\n" \
"v_add_u32 v51, s78, v51\n" \
"v_mfma_f32_16x16x32_bf16 a[168:171], v[148:151], v[108:111], a[168:171]\n" \
"v_mfma_f32_16x16x32_bf16 a[164:167], v[136:139], v[104:107], a[164:167]\n" \
"v_mfma_f32_16x16x32_bf16 a[164:167], v[140:143], v[108:111], a[164:167]\n" \
"v_mfma_f32_16x16x32_bf16 a[160:163], v[128:131], v[104:107], a[160:163]\n" \
"v_mfma_f32_16x16x32_bf16 a[160:163], v[132:135], v[108:111], a[160:163]\n" \
"s_waitcnt lgkmcnt(0)\n" \
"v_mfma_f32_16x16x32_bf16 a[192:195], v[128:131], v[112:115], a[192:195]\n" \
"v_mfma_f32_16x16x32_bf16 a[192:195], v[132:135], v[116:119], a[192:195]\n" \
"v_mfma_f32_16x16x32_bf16 a[196:199], v[136:139], v[112:115], a[196:199]\n" \
"v_mfma_f32_16x16x32_bf16 a[196:199], v[140:143], v[116:119], a[196:199]\n" \
"v_mfma_f32_16x16x32_bf16 a[200:203], v[144:147], v[112:115], a[200:203]\n" \
"v_mfma_f32_16x16x32_bf16 a[200:203], v[148:151], v[116:119], a[200:203]\n" \
"v_mfma_f32_16x16x32_bf16 a[204:207], v[152:155], v[112:115], a[204:207]\n" \
"v_mfma_f32_16x16x32_bf16 a[204:207], v[156:159], v[116:119], a[204:207]\n" \
"v_mfma_f32_16x16x32_bf16 a[236:239], v[152:155], v[120:123], a[236:239]\n" \
"v_mfma_f32_16x16x32_bf16 a[236:239], v[156:159], v[124:127], a[236:239]\n" \
"v_mfma_f32_16x16x32_bf16 a[232:235], v[144:147], v[120:123], a[232:235]\n" \
"v_mfma_f32_16x16x32_bf16 a[232:235], v[148:151], v[124:127], a[232:235]\n" \
"v_mfma_f32_16x16x32_bf16 a[228:231], v[136:139], v[120:123], a[228:231]\n" \
"v_mfma_f32_16x16x32_bf16 a[228:231], v[140:143], v[124:127], a[228:231]\n" \
"Lpg608_%=:\n" \
"Lst0x0_%=:\n" \
"s_cbranch_execz Lpg2766_%=\n" \
"s_waitcnt vmcnt(10) lgkmcnt(0)\n" \
"v_mfma_f32_16x16x32_bf16 a[224:227], v[128:131], v[120:123], a[224:227]\n" \
"v_mfma_f32_16x16x32_bf16 a[224:227], v[132:135], v[124:127], a[224:227]\n" \
"s_barrier\n" \
"v_mfma_f32_16x16x32_bf16 a[16:19], v[64:67], v[160:163], a[16:19]\n" \
"s_add_u32 m0, s87, 135168\n" \
"ds_read_b64_tr_b16 v[128:129], v50 offset:0\n" \
"v_readfirstlane_b32 s60, v244\n" \
"v_mfma_f32_16x16x32_bf16 a[16:19], v[68:71], v[164:167], a[16:19]\n" \
"s_add_u32 s60, s60, 0x9e3779b9\n" \
"global_load_lds_dwordx4 v32, s[54:55] offset:-4096\n" \
"v_mfma_f32_16x16x32_bf16 a[20:23], v[64:67], v[168:171], a[20:23]\n" \
"s_mul_i32 s88, s62, 557824\n" \
"s_mul_i32 s89, s63, 17408\n" \
"ds_read_b64_tr_b16 v[130:131], v50 offset:4096\n" \
"v_mfma_f32_16x16x32_bf16 a[20:23], v[68:71], v[172:175], a[20:23]\n" \
"s_add_u32 s88, s88, s89\n" \
"s_add_u32 s88, s88, s95\n" \
"s_add_u32 s89, s88, 17850368\n" \
"v_mfma_f32_16x16x32_bf16 a[24:27], v[64:67], v[176:179], a[24:27]\n" \
"s_lshl_b32 s91, s62, 7\n" \
"s_add_u32 s91, s91, 65585152\n" \
"ds_read_b64_tr_b16 v[132:133], v50 offset:16384\n" \
"v_mfma_f32_16x16x32_bf16 a[24:27], v[68:71], v[180:183], a[24:27]\n" \
"s_mov_b32 s92, 0x3e0293ee\n" \
"global_load_lds_dwordx4 v33, s[40:41] offset:-3072\n" \
"v_mfma_f32_16x16x32_bf16 a[28:31], v[64:67], v[184:187], a[28:31]\n" \
"ds_read_b64_tr_b16 v[134:135], v50 offset:20480\n" \
"v_mfma_f32_16x16x32_bf16 a[28:31], v[68:71], v[188:191], a[28:31]\n" \
"v_mfma_f32_16x16x32_bf16 a[60:63], v[72:75], v[184:187], a[60:63]\n" \
"ds_read_b128 v[64:67], v48 offset:0\n" \
"v_mfma_f32_16x16x32_bf16 a[60:63], v[76:79], v[188:191], a[60:63]\n" \
"global_load_lds_dwordx4 v34, s[54:55] offset:-2048\n" \
"v_mfma_f32_16x16x32_bf16 a[56:59], v[72:75], v[176:179], a[56:59]\n" \
"ds_read_b64_tr_b16 v[136:137], v51 offset:0\n" \
"v_mfma_f32_16x16x32_bf16 a[56:59], v[76:79], v[180:183], a[56:59]\n" \
"v_mfma_f32_16x16x32_bf16 a[52:55], v[72:75], v[168:171], a[52:55]\n" \
"ds_read_b128 v[68:71], v49 offset:0\n" \
"v_mfma_f32_16x16x32_bf16 a[52:55], v[76:79], v[172:175], a[52:55]\n" \
"global_load_lds_dwordx4 v35, s[40:41] offset:-1024\n" \
"v_mfma_f32_16x16x32_bf16 a[48:51], v[72:75], v[160:163], a[48:51]\n" \
"ds_read_b64_tr_b16 v[138:139], v51 offset:4096\n" \
"v_mfma_f32_16x16x32_bf16 a[48:51], v[76:79], v[164:167], a[48:51]\n" \
"v_mfma_f32_16x16x32_bf16 a[80:83], v[80:83], v[160:163], a[80:83]\n" \
"ds_read_b128 v[72:75], v48 offset:2048\n" \
"v_mfma_f32_16x16x32_bf16 a[80:83], v[84:87], v[164:167], a[80:83]\n" \
"global_load_lds_dwordx4 v36, s[54:55] offset:0\n" \
"v_mfma_f32_16x16x32_bf16 a[84:87], v[80:83], v[168:171], a[84:87]\n" \
"ds_read_b64_tr_b16 v[140:141], v51 offset:16384\n" \
"v_mfma_f32_16x16x32_bf16 a[84:87], v[84:87], v[172:175], a[84:87]\n" \
"v_mfma_f32_16x16x32_bf16 a[88:91], v[80:83], v[176:179], a[88:91]\n" \
"ds_read_b128 v[76:79], v49 offset:2048\n" \
"v_mfma_f32_16x16x32_bf16 a[88:91], v[84:87], v[180:183], a[88:91]\n" \
"global_load_lds_dwordx4 v37, s[40:41] offset:1024\n" \
"v_mfma_f32_16x16x32_bf16 a[92:95], v[80:83], v[184:187], a[92:95]\n" \
"ds_read_b64_tr_b16 v[142:143], v51 offset:20480\n" \
"v_mfma_f32_16x16x32_bf16 a[92:95], v[84:87], v[188:191], a[92:95]\n" \
"v_mfma_f32_16x16x32_bf16 a[124:127], v[88:91], v[184:187], a[124:127]\n" \
"ds_read_b128 v[80:83], v48 offset:4096\n" \
"v_mfma_f32_16x16x32_bf16 a[124:127], v[92:95], v[188:191], a[124:127]\n" \
"global_load_lds_dwordx4 v38, s[54:55] offset:2048\n" \
"v_mfma_f32_16x16x32_bf16 a[120:123], v[88:91], v[176:179], a[120:123]\n" \
"ds_read_b64_tr_b16 v[144:145], v50 offset:512\n" \
"v_mfma_f32_16x16x32_bf16 a[120:123], v[92:95], v[180:183], a[120:123]\n" \
"v_mfma_f32_16x16x32_bf16 a[116:119], v[88:91], v[168:171], a[116:119]\n" \
"ds_read_b128 v[84:87], v49 offset:4096\n" \
"global_load_lds_dwordx4 v39, s[40:41] offset:3072\n" \
"v_mfma_f32_16x16x32_bf16 a[116:119], v[92:95], v[172:175], a[116:119]\n" \
"s_add_u32 s54, s54, s75\n" \
"s_addc_u32 s55, s55, 0\n" \
"v_mfma_f32_16x16x32_bf16 a[112:115], v[88:91], v[160:163], a[112:115]\n" \
"s_add_u32 s40, s40, s75\n" \
"s_addc_u32 s41, s41, 0\n" \
"ds_read_b64_tr_b16 v[146:147], v50 offset:4608\n" \
"v_mfma_f32_16x16x32_bf16 a[112:115], v[92:95], v[164:167], a[112:115]\n" \
"s_add_u32 s68, s67, 0x200000\n" \
"s_add_u32 s64, s14, s68\n" \
"s_addc_u32 s65, s15, 0\n" \
"v_mfma_f32_16x16x32_bf16 a[144:147], v[96:99], v[160:163], a[144:147]\n" \
"ds_read_b128 v[88:91], v48 offset:6144\n" \
"v_mfma_f32_16x16x32_bf16 a[144:147], v[100:103], v[164:167], a[144:147]\n" \
"global_load_dword v253, v248, s[64:65]\n" \
"v_mfma_f32_16x16x32_bf16 a[148:151], v[96:99], v[168:171], a[148:151]\n" \
"s_add_u32 s68, s67, 0x200000\n" \
"ds_read_b64_tr_b16 v[148:149], v50 offset:16896\n" \
"v_mfma_f32_16x16x32_bf16 a[148:151], v[100:103], v[172:175], a[148:151]\n" \
"s_add_u32 s64, s16, s68\n" \
"s_addc_u32 s65, s17, 0\n" \
"v_mfma_f32_16x16x32_bf16 a[152:155], v[96:99], v[176:179], a[152:155]\n" \
"ds_read_b128 v[92:95], v49 offset:6144\n" \
"v_mfma_f32_16x16x32_bf16 a[152:155], v[100:103], v[180:183], a[152:155]\n" \
"global_load_dword v253, v248, s[64:65]\n" \
"v_mfma_f32_16x16x32_bf16 a[156:159], v[96:99], v[184:187], a[156:159]\n" \
"ds_read_b64_tr_b16 v[150:151], v50 offset:20992\n" \
"v_mfma_f32_16x16x32_bf16 a[156:159], v[100:103], v[188:191], a[156:159]\n" \
"v_mfma_f32_16x16x32_bf16 a[188:191], v[104:107], v[184:187], a[188:191]\n" \
"ds_read_b128 v[96:99], v48 offset:8192\n" \
"v_mfma_f32_16x16x32_bf16 a[188:191], v[108:111], v[188:191], a[188:191]\n" \
"v_mfma_f32_16x16x32_bf16 a[184:187], v[104:107], v[176:179], a[184:187]\n" \
"ds_read_b64_tr_b16 v[152:153], v51 offset:512\n" \
"v_mfma_f32_16x16x32_bf16 a[184:187], v[108:111], v[180:183], a[184:187]\n" \
"v_mfma_f32_16x16x32_bf16 a[180:183], v[104:107], v[168:171], a[180:183]\n" \
"ds_read_b128 v[100:103], v49 offset:8192\n" \
"v_mfma_f32_16x16x32_bf16 a[180:183], v[108:111], v[172:175], a[180:183]\n" \
"v_mfma_f32_16x16x32_bf16 a[176:179], v[104:107], v[160:163], a[176:179]\n" \
"ds_read_b64_tr_b16 v[154:155], v51 offset:4608\n" \
"v_mfma_f32_16x16x32_bf16 a[176:179], v[108:111], v[164:167], a[176:179]\n" \
"v_mfma_f32_16x16x32_bf16 a[208:211], v[112:115], v[160:163], a[208:211]\n" \
"ds_read_b128 v[104:107], v48 offset:10240\n" \
"v_mfma_f32_16x16x32_bf16 a[208:211], v[116:119], v[164:167], a[208:211]\n" \
"v_mfma_f32_16x16x32_bf16 a[212:215], v[112:115], v[168:171], a[212:215]\n" \
"ds_read_b64_tr_b16 v[156:157], v51 offset:16896\n" \
"v_mfma_f32_16x16x32_bf16 a[212:215], v[116:119], v[172:175], a[212:215]\n" \
"v_mfma_f32_16x16x32_bf16 a[216:219], v[112:115], v[176:179], a[216:219]\n" \
"ds_read_b128 v[108:111], v49 offset:10240\n" \
"v_mfma_f32_16x16x32_bf16 a[216:219], v[116:119], v[180:183], a[216:219]\n" \
"v_mfma_f32_16x16x32_bf16 a[220:223], v[112:115], v[184:187], a[220:223]\n" \
"ds_read_b64_tr_b16 v[158:159], v51 offset:20992\n" \
"v_mfma_f32_16x16x32_bf16 a[220:223], v[116:119], v[188:191], a[220:223]\n" \
"v_mfma_f32_16x16x32_bf16 a[252:255], v[120:123], v[184:187], a[252:255]\n" \
"ds_read_b128 v[112:115], v48 offset:12288\n" \
"v_mfma_f32_16x16x32_bf16 a[252:255], v[124:127], v[188:191], a[252:255]\n" \
"v_mfma_f32_16x16x32_bf16 a[248:251], v[120:123], v[176:179], a[248:251]\n" \
"v_mfma_f32_16x16x32_bf16 a[248:251], v[124:127], v[180:183], a[248:251]\n" \
"v_mfma_f32_16x16x32_bf16 a[244:247], v[120:123], v[168:171], a[244:247]\n" \
"ds_read_b128 v[116:119], v49 offset:12288\n" \
"v_mfma_f32_16x16x32_bf16 a[244:247], v[124:127], v[172:175], a[244:247]\n" \
"v_mfma_f32_16x16x32_bf16 a[240:243], v[120:123], v[160:163], a[240:243]\n" \
"v_mfma_f32_16x16x32_bf16 a[240:243], v[124:127], v[164:167], a[240:243]\n" \
"s_waitcnt lgkmcnt(1)\n" \
"v_mfma_f32_16x16x32_bf16 a[0:3], v[128:131], v[64:67], a[0:3]\n" \
"ds_read_b128 v[120:123], v48 offset:14336\n" \
"v_mfma_f32_16x16x32_bf16 a[0:3], v[132:135], v[68:71], a[0:3]\n" \
"v_mfma_f32_16x16x32_bf16 a[4:7], v[136:139], v[64:67], a[4:7]\n" \
"ds_read_b128 v[124:127], v49 offset:14336\n" \
"v_mfma_f32_16x16x32_bf16 a[4:7], v[140:143], v[68:71], a[4:7]\n" \
"s_add_u32 s68, s67, 0x300000\n" \
"v_mfma_f32_16x16x32_bf16 a[8:11], v[144:147], v[64:67], a[8:11]\n" \
"s_add_u32 s64, s14, s68\n" \
"s_addc_u32 s65, s15, 0\n" \
"ds_read_b64_tr_b16 v[160:161], v50 offset:1024\n" \
"v_mfma_f32_16x16x32_bf16 a[8:11], v[148:151], v[68:71], a[8:11]\n" \
"global_load_dword v253, v248, s[64:65]\n" \
"v_mfma_f32_16x16x32_bf16 a[12:15], v[152:155], v[64:67], a[12:15]\n" \
"s_add_u32 m0, s87, 69632\n" \
"ds_read_b64_tr_b16 v[162:163], v50 offset:5120\n" \
"v_mfma_f32_16x16x32_bf16 a[12:15], v[156:159], v[68:71], a[12:15]\n" \
"global_load_lds_dwordx4 v40, s[52:53] offset:-4096\n" \
"v_mfma_f32_16x16x32_bf16 a[44:47], v[152:155], v[72:75], a[44:47]\n" \
"ds_read_b64_tr_b16 v[164:165], v50 offset:17408\n" \
"v_mfma_f32_16x16x32_bf16 a[44:47], v[156:159], v[76:79], a[44:47]\n" \
"v_mfma_f32_16x16x32_bf16 a[40:43], v[144:147], v[72:75], a[40:43]\n" \
"ds_read_b64_tr_b16 v[166:167], v50 offset:21504\n" \
"v_mfma_f32_16x16x32_bf16 a[40:43], v[148:151], v[76:79], a[40:43]\n" \
"global_load_lds_dwordx4 v41, s[50:51] offset:-3072\n" \
"v_mfma_f32_16x16x32_bf16 a[36:39], v[136:139], v[72:75], a[36:39]\n" \
"ds_read_b64_tr_b16 v[168:169], v51 offset:1024\n" \
"v_mfma_f32_16x16x32_bf16 a[36:39], v[140:143], v[76:79], a[36:39]\n" \
"v_mfma_f32_16x16x32_bf16 a[32:35], v[128:131], v[72:75], a[32:35]\n" \
"ds_read_b64_tr_b16 v[170:171], v51 offset:5120\n" \
"v_mfma_f32_16x16x32_bf16 a[32:35], v[132:135], v[76:79], a[32:35]\n" \
"global_load_lds_dwordx4 v42, s[52:53] offset:-2048\n" \
"v_mfma_f32_16x16x32_bf16 a[64:67], v[128:131], v[80:83], a[64:67]\n" \
"ds_read_b64_tr_b16 v[172:173], v51 offset:17408\n" \
"v_mfma_f32_16x16x32_bf16 a[64:67], v[132:135], v[84:87], a[64:67]\n" \
"v_mfma_f32_16x16x32_bf16 a[68:71], v[136:139], v[80:83], a[68:71]\n" \
"ds_read_b64_tr_b16 v[174:175], v51 offset:21504\n" \
"v_mfma_f32_16x16x32_bf16 a[68:71], v[140:143], v[84:87], a[68:71]\n" \
"global_load_lds_dwordx4 v43, s[50:51] offset:-1024\n" \
"v_mfma_f32_16x16x32_bf16 a[72:75], v[144:147], v[80:83], a[72:75]\n" \
"ds_read_b64_tr_b16 v[176:177], v50 offset:1536\n" \
"v_mfma_f32_16x16x32_bf16 a[72:75], v[148:151], v[84:87], a[72:75]\n" \
"v_mfma_f32_16x16x32_bf16 a[76:79], v[152:155], v[80:83], a[76:79]\n" \
"ds_read_b64_tr_b16 v[178:179], v50 offset:5632\n" \
"v_mfma_f32_16x16x32_bf16 a[76:79], v[156:159], v[84:87], a[76:79]\n" \
"global_load_lds_dwordx4 v44, s[52:53] offset:0\n" \
"v_mfma_f32_16x16x32_bf16 a[108:111], v[152:155], v[88:91], a[108:111]\n" \
"ds_read_b64_tr_b16 v[180:181], v50 offset:17920\n" \
"v_mfma_f32_16x16x32_bf16 a[108:111], v[156:159], v[92:95], a[108:111]\n" \
"v_mfma_f32_16x16x32_bf16 a[104:107], v[144:147], v[88:91], a[104:107]\n" \
"ds_read_b64_tr_b16 v[182:183], v50 offset:22016\n" \
"v_mfma_f32_16x16x32_bf16 a[104:107], v[148:151], v[92:95], a[104:107]\n" \
"global_load_lds_dwordx4 v45, s[50:51] offset:1024\n" \
"v_mfma_f32_16x16x32_bf16 a[100:103], v[136:139], v[88:91], a[100:103]\n" \
"ds_read_b64_tr_b16 v[184:185], v51 offset:1536\n" \
"v_mfma_f32_16x16x32_bf16 a[100:103], v[140:143], v[92:95], a[100:103]\n" \
"v_mfma_f32_16x16x32_bf16 a[96:99], v[128:131], v[88:91], a[96:99]\n" \
"ds_read_b64_tr_b16 v[186:187], v51 offset:5632\n" \
"v_mfma_f32_16x16x32_bf16 a[96:99], v[132:135], v[92:95], a[96:99]\n" \
"global_load_lds_dwordx4 v46, s[52:53] offset:2048\n" \
"v_mfma_f32_16x16x32_bf16 a[128:131], v[128:131], v[96:99], a[128:131]\n" \
"ds_read_b64_tr_b16 v[188:189], v51 offset:17920\n" \
"v_mfma_f32_16x16x32_bf16 a[128:131], v[132:135], v[100:103], a[128:131]\n" \
"v_mfma_f32_16x16x32_bf16 a[132:135], v[136:139], v[96:99], a[132:135]\n" \
"ds_read_b64_tr_b16 v[190:191], v51 offset:22016\n" \
"v_mfma_f32_16x16x32_bf16 a[132:135], v[140:143], v[100:103], a[132:135]\n" \
"global_load_lds_dwordx4 v47, s[50:51] offset:3072\n" \
"v_mfma_f32_16x16x32_bf16 a[136:139], v[144:147], v[96:99], a[136:139]\n" \
"s_add_u32 s52, s52, s74\n" \
"s_addc_u32 s53, s53, 0\n" \
"v_mfma_f32_16x16x32_bf16 a[136:139], v[148:151], v[100:103], a[136:139]\n" \
"s_add_u32 s50, s50, s74\n" \
"s_addc_u32 s51, s51, 0\n" \
"v_mfma_f32_16x16x32_bf16 a[140:143], v[152:155], v[96:99], a[140:143]\n" \
"v_mfma_f32_16x16x32_bf16 a[140:143], v[156:159], v[100:103], a[140:143]\n" \
"v_mfma_f32_16x16x32_bf16 a[172:175], v[152:155], v[104:107], a[172:175]\n" \
"s_mul_i32 s76, s73, 0xfffd0000\n" \
"s_add_u32 s77, s76, 0x18000\n" \
"s_sub_u32 s78, 0xfffe8000, s76\n" \
"v_mfma_f32_16x16x32_bf16 a[172:175], v[156:159], v[108:111], a[172:175]\n" \
"v_add_u32 v48, s77, v48\n" \
"v_add_u32 v49, s77, v49\n" \
"v_mfma_f32_16x16x32_bf16 a[168:171], v[144:147], v[104:107], a[168:171]\n" \
"v_add_u32 v50, s78, v50\n" \
"v_add_u32 v51, s78, v51\n" \
"v_mfma_f32_16x16x32_bf16 a[168:171], v[148:151], v[108:111], a[168:171]\n" \
"v_mfma_f32_16x16x32_bf16 a[164:167], v[136:139], v[104:107], a[164:167]\n" \
"v_mfma_f32_16x16x32_bf16 a[164:167], v[140:143], v[108:111], a[164:167]\n" \
"v_mfma_f32_16x16x32_bf16 a[160:163], v[128:131], v[104:107], a[160:163]\n" \
"v_mfma_f32_16x16x32_bf16 a[160:163], v[132:135], v[108:111], a[160:163]\n" \
"s_waitcnt lgkmcnt(0)\n" \
"v_mfma_f32_16x16x32_bf16 a[192:195], v[128:131], v[112:115], a[192:195]\n" \
"v_mfma_f32_16x16x32_bf16 a[192:195], v[132:135], v[116:119], a[192:195]\n" \
"v_mfma_f32_16x16x32_bf16 a[196:199], v[136:139], v[112:115], a[196:199]\n" \
"v_mfma_f32_16x16x32_bf16 a[196:199], v[140:143], v[116:119], a[196:199]\n" \
"v_mfma_f32_16x16x32_bf16 a[200:203], v[144:147], v[112:115], a[200:203]\n" \
"v_mfma_f32_16x16x32_bf16 a[200:203], v[148:151], v[116:119], a[200:203]\n" \
"v_mfma_f32_16x16x32_bf16 a[204:207], v[152:155], v[112:115], a[204:207]\n" \
"v_mfma_f32_16x16x32_bf16 a[204:207], v[156:159], v[116:119], a[204:207]\n" \
"v_mfma_f32_16x16x32_bf16 a[236:239], v[152:155], v[120:123], a[236:239]\n" \
"v_mfma_f32_16x16x32_bf16 a[236:239], v[156:159], v[124:127], a[236:239]\n" \
"v_mfma_f32_16x16x32_bf16 a[232:235], v[144:147], v[120:123], a[232:235]\n" \
"v_mfma_f32_16x16x32_bf16 a[232:235], v[148:151], v[124:127], a[232:235]\n" \
"v_mfma_f32_16x16x32_bf16 a[228:231], v[136:139], v[120:123], a[228:231]\n" \
"v_mfma_f32_16x16x32_bf16 a[228:231], v[140:143], v[124:127], a[228:231]\n" \
"s_waitcnt vmcnt(11) lgkmcnt(0)\n" \
"v_mfma_f32_16x16x32_bf16 a[224:227], v[128:131], v[120:123], a[224:227]\n" \
"v_mfma_f32_16x16x32_bf16 a[224:227], v[132:135], v[124:127], a[224:227]\n" \
"s_barrier\n" \
"v_mfma_f32_16x16x32_bf16 a[16:19], v[64:67], v[160:163], a[16:19]\n" \
"s_add_u32 m0, s87, 102400\n" \
"ds_read_b64_tr_b16 v[128:129], v50 offset:0\n" \
"v_mfma_f32_16x16x32_bf16 a[16:19], v[68:71], v[164:167], a[16:19]\n" \
"global_load_lds_dwordx4 v32, s[54:55] offset:-4096\n" \
"v_mfma_f32_16x16x32_bf16 a[20:23], v[64:67], v[168:171], a[20:23]\n" \
"ds_read_b64_tr_b16 v[130:131], v50 offset:4096\n" \
"v_mfma_f32_16x16x32_bf16 a[20:23], v[68:71], v[172:175], a[20:23]\n" \
"v_mfma_f32_16x16x32_bf16 a[24:27], v[64:67], v[176:179], a[24:27]\n" \
"ds_read_b64_tr_b16 v[132:133], v50 offset:16384\n" \
"v_mfma_f32_16x16x32_bf16 a[24:27], v[68:71], v[180:183], a[24:27]\n" \
"global_load_lds_dwordx4 v33, s[40:41] offset:-3072\n" \
"v_mfma_f32_16x16x32_bf16 a[28:31], v[64:67], v[184:187], a[28:31]\n" \
"ds_read_b64_tr_b16 v[134:135], v50 offset:20480\n" \
"v_mfma_f32_16x16x32_bf16 a[28:31], v[68:71], v[188:191], a[28:31]\n" \
"v_mfma_f32_16x16x32_bf16 a[60:63], v[72:75], v[184:187], a[60:63]\n" \
"ds_read_b128 v[64:67], v48 offset:0\n" \
"v_mfma_f32_16x16x32_bf16 a[60:63], v[76:79], v[188:191], a[60:63]\n" \
"global_load_lds_dwordx4 v34, s[54:55] offset:-2048\n" \
"v_mfma_f32_16x16x32_bf16 a[56:59], v[72:75], v[176:179], a[56:59]\n" \
"ds_read_b64_tr_b16 v[136:137], v51 offset:0\n" \
"v_mfma_f32_16x16x32_bf16 a[56:59], v[76:79], v[180:183], a[56:59]\n" \
"v_mfma_f32_16x16x32_bf16 a[52:55], v[72:75], v[168:171], a[52:55]\n" \
"ds_read_b128 v[68:71], v49 offset:0\n" \
"v_mfma_f32_16x16x32_bf16 a[52:55], v[76:79], v[172:175], a[52:55]\n" \
"global_load_lds_dwordx4 v35, s[40:41] offset:-1024\n" \
"v_mfma_f32_16x16x32_bf16 a[48:51], v[72:75], v[160:163], a[48:51]\n" \
"ds_read_b64_tr_b16 v[138:139], v51 offset:4096\n" \
"v_mfma_f32_16x16x32_bf16 a[48:51], v[76:79], v[164:167], a[48:51]\n" \
"v_mfma_f32_16x16x32_bf16 a[80:83], v[80:83], v[160:163], a[80:83]\n" \
"ds_read_b128 v[72:75], v48 offset:2048\n" \
"v_mfma_f32_16x16x32_bf16 a[80:83], v[84:87], v[164:167], a[80:83]\n" \
"global_load_lds_dwordx4 v36, s[54:55] offset:0\n" \
"v_mfma_f32_16x16x32_bf16 a[84:87], v[80:83], v[168:171], a[84:87]\n" \
"ds_read_b64_tr_b16 v[140:141], v51 offset:16384\n" \
"v_mfma_f32_16x16x32_bf16 a[84:87], v[84:87], v[172:175], a[84:87]\n" \
"v_mfma_f32_16x16x32_bf16 a[88:91], v[80:83], v[176:179], a[88:91]\n" \
"ds_read_b128 v[76:79], v49 offset:2048\n" \
"v_mfma_f32_16x16x32_bf16 a[88:91], v[84:87], v[180:183], a[88:91]\n" \
"global_load_lds_dwordx4 v37, s[40:41] offset:1024\n" \
"v_mfma_f32_16x16x32_bf16 a[92:95], v[80:83], v[184:187], a[92:95]\n" \
"ds_read_b64_tr_b16 v[142:143], v51 offset:20480\n" \
"v_mfma_f32_16x16x32_bf16 a[92:95], v[84:87], v[188:191], a[92:95]\n" \
"v_mfma_f32_16x16x32_bf16 a[124:127], v[88:91], v[184:187], a[124:127]\n" \
"ds_read_b128 v[80:83], v48 offset:4096\n" \
"v_mfma_f32_16x16x32_bf16 a[124:127], v[92:95], v[188:191], a[124:127]\n" \
"global_load_lds_dwordx4 v38, s[54:55] offset:2048\n" \
"v_mfma_f32_16x16x32_bf16 a[120:123], v[88:91], v[176:179], a[120:123]\n" \
"ds_read_b64_tr_b16 v[144:145], v50 offset:512\n" \
"v_mfma_f32_16x16x32_bf16 a[120:123], v[92:95], v[180:183], a[120:123]\n" \
"v_mfma_f32_16x16x32_bf16 a[116:119], v[88:91], v[168:171], a[116:119]\n" \
"ds_read_b128 v[84:87], v49 offset:4096\n" \
"global_load_lds_dwordx4 v39, s[40:41] offset:3072\n" \
"v_mfma_f32_16x16x32_bf16 a[116:119], v[92:95], v[172:175], a[116:119]\n" \
"s_add_u32 s54, s54, s75\n" \
"s_addc_u32 s55, s55, 0\n" \
"v_mfma_f32_16x16x32_bf16 a[112:115], v[88:91], v[160:163], a[112:115]\n" \
"s_add_u32 s40, s40, s75\n" \
"s_addc_u32 s41, s41, 0\n" \
"ds_read_b64_tr_b16 v[146:147], v50 offset:4608\n" \
"v_mfma_f32_16x16x32_bf16 a[112:115], v[92:95], v[164:167], a[112:115]\n" \
"s_add_u32 s68, s67, 0x300000\n" \
"s_add_u32 s64, s16, s68\n" \
"s_addc_u32 s65, s17, 0\n" \
"v_mfma_f32_16x16x32_bf16 a[144:147], v[96:99], v[160:163], a[144:147]\n" \
"ds_read_b128 v[88:91], v48 offset:6144\n" \
"v_mfma_f32_16x16x32_bf16 a[144:147], v[100:103], v[164:167], a[144:147]\n" \
"global_load_dword v253, v248, s[64:65]\n" \
"v_mfma_f32_16x16x32_bf16 a[148:151], v[96:99], v[168:171], a[148:151]\n" \
"s_add_u32 s68, s66, 0x0\n" \
"ds_read_b64_tr_b16 v[148:149], v50 offset:16896\n" \
"v_mfma_f32_16x16x32_bf16 a[148:151], v[100:103], v[172:175], a[148:151]\n" \
"s_add_u32 s64, s8, s68\n" \
"s_addc_u32 s65, s9, 0\n" \
"v_mfma_f32_16x16x32_bf16 a[152:155], v[96:99], v[176:179], a[152:155]\n" \
"ds_read_b128 v[92:95], v49 offset:6144\n" \
"v_mfma_f32_16x16x32_bf16 a[152:155], v[100:103], v[180:183], a[152:155]\n" \
"global_load_dword v253, v248, s[64:65]\n" \
"v_mfma_f32_16x16x32_bf16 a[156:159], v[96:99], v[184:187], a[156:159]\n" \
"ds_read_b64_tr_b16 v[150:151], v50 offset:20992\n" \
"v_mfma_f32_16x16x32_bf16 a[156:159], v[100:103], v[188:191], a[156:159]\n" \
"v_mfma_f32_16x16x32_bf16 a[188:191], v[104:107], v[184:187], a[188:191]\n" \
"ds_read_b128 v[96:99], v48 offset:8192\n" \
"v_mfma_f32_16x16x32_bf16 a[188:191], v[108:111], v[188:191], a[188:191]\n" \
"v_mfma_f32_16x16x32_bf16 a[184:187], v[104:107], v[176:179], a[184:187]\n" \
"ds_read_b64_tr_b16 v[152:153], v51 offset:512\n" \
"v_mfma_f32_16x16x32_bf16 a[184:187], v[108:111], v[180:183], a[184:187]\n" \
"v_mfma_f32_16x16x32_bf16 a[180:183], v[104:107], v[168:171], a[180:183]\n" \
"ds_read_b128 v[100:103], v49 offset:8192\n" \
"v_mfma_f32_16x16x32_bf16 a[180:183], v[108:111], v[172:175], a[180:183]\n" \
"v_mfma_f32_16x16x32_bf16 a[176:179], v[104:107], v[160:163], a[176:179]\n" \
"ds_read_b64_tr_b16 v[154:155], v51 offset:4608\n" \
"v_mfma_f32_16x16x32_bf16 a[176:179], v[108:111], v[164:167], a[176:179]\n" \
"v_mfma_f32_16x16x32_bf16 a[208:211], v[112:115], v[160:163], a[208:211]\n" \
"ds_read_b128 v[104:107], v48 offset:10240\n" \
"v_mfma_f32_16x16x32_bf16 a[208:211], v[116:119], v[164:167], a[208:211]\n" \
"v_mfma_f32_16x16x32_bf16 a[212:215], v[112:115], v[168:171], a[212:215]\n" \
"ds_read_b64_tr_b16 v[156:157], v51 offset:16896\n" \
"v_mfma_f32_16x16x32_bf16 a[212:215], v[116:119], v[172:175], a[212:215]\n" \
"v_mfma_f32_16x16x32_bf16 a[216:219], v[112:115], v[176:179], a[216:219]\n" \
"ds_read_b128 v[108:111], v49 offset:10240\n" \
"v_mfma_f32_16x16x32_bf16 a[216:219], v[116:119], v[180:183], a[216:219]\n" \
"v_mfma_f32_16x16x32_bf16 a[220:223], v[112:115], v[184:187], a[220:223]\n" \
"ds_read_b64_tr_b16 v[158:159], v51 offset:20992\n" \
"v_mfma_f32_16x16x32_bf16 a[220:223], v[116:119], v[188:191], a[220:223]\n" \
"v_mfma_f32_16x16x32_bf16 a[252:255], v[120:123], v[184:187], a[252:255]\n" \
"ds_read_b128 v[112:115], v48 offset:12288\n" \
"v_mfma_f32_16x16x32_bf16 a[252:255], v[124:127], v[188:191], a[252:255]\n" \
"v_mfma_f32_16x16x32_bf16 a[248:251], v[120:123], v[176:179], a[248:251]\n" \
"v_mfma_f32_16x16x32_bf16 a[248:251], v[124:127], v[180:183], a[248:251]\n" \
"v_mfma_f32_16x16x32_bf16 a[244:247], v[120:123], v[168:171], a[244:247]\n" \
"ds_read_b128 v[116:119], v49 offset:12288\n" \
"v_mfma_f32_16x16x32_bf16 a[244:247], v[124:127], v[172:175], a[244:247]\n" \
"v_mfma_f32_16x16x32_bf16 a[240:243], v[120:123], v[160:163], a[240:243]\n" \
"v_mfma_f32_16x16x32_bf16 a[240:243], v[124:127], v[164:167], a[240:243]\n" \
"s_waitcnt lgkmcnt(1)\n" \
"v_mfma_f32_16x16x32_bf16 a[0:3], v[128:131], v[64:67], a[0:3]\n" \
"ds_read_b128 v[120:123], v48 offset:14336\n" \
"v_mfma_f32_16x16x32_bf16 a[0:3], v[132:135], v[68:71], a[0:3]\n" \
"v_mfma_f32_16x16x32_bf16 a[4:7], v[136:139], v[64:67], a[4:7]\n" \
"ds_read_b128 v[124:127], v49 offset:14336\n" \
"v_mfma_f32_16x16x32_bf16 a[4:7], v[140:143], v[68:71], a[4:7]\n" \
"s_add_u32 s68, s66, 0x100000\n" \
"v_mfma_f32_16x16x32_bf16 a[8:11], v[144:147], v[64:67], a[8:11]\n" \
"s_add_u32 s64, s8, s68\n" \
"s_addc_u32 s65, s9, 0\n" \
"ds_read_b64_tr_b16 v[160:161], v50 offset:1024\n" \
"v_mfma_f32_16x16x32_bf16 a[8:11], v[148:151], v[68:71], a[8:11]\n" \
"global_load_dword v253, v248, s[64:65]\n" \
"v_mfma_f32_16x16x32_bf16 a[12:15], v[152:155], v[64:67], a[12:15]\n" \
"s_add_u32 m0, s87, 36864\n" \
"ds_read_b64_tr_b16 v[162:163], v50 offset:5120\n" \
"v_mfma_f32_16x16x32_bf16 a[12:15], v[156:159], v[68:71], a[12:15]\n" \
"global_load_lds_dwordx4 v40, s[52:53] offset:-4096\n" \
"v_mfma_f32_16x16x32_bf16 a[44:47], v[152:155], v[72:75], a[44:47]\n" \
"ds_read_b64_tr_b16 v[164:165], v50 offset:17408\n" \
"v_mfma_f32_16x16x32_bf16 a[44:47], v[156:159], v[76:79], a[44:47]\n" \
"v_mfma_f32_16x16x32_bf16 a[40:43], v[144:147], v[72:75], a[40:43]\n" \
"ds_read_b64_tr_b16 v[166:167], v50 offset:21504\n" \
"v_mfma_f32_16x16x32_bf16 a[40:43], v[148:151], v[76:79], a[40:43]\n" \
"global_load_lds_dwordx4 v41, s[50:51] offset:-3072\n" \
"v_mfma_f32_16x16x32_bf16 a[36:39], v[136:139], v[72:75], a[36:39]\n" \
"ds_read_b64_tr_b16 v[168:169], v51 offset:1024\n" \
"v_mfma_f32_16x16x32_bf16 a[36:39], v[140:143], v[76:79], a[36:39]\n" \
"v_mfma_f32_16x16x32_bf16 a[32:35], v[128:131], v[72:75], a[32:35]\n" \
"ds_read_b64_tr_b16 v[170:171], v51 offset:5120\n" \
"v_mfma_f32_16x16x32_bf16 a[32:35], v[132:135], v[76:79], a[32:35]\n" \
"global_load_lds_dwordx4 v42, s[52:53] offset:-2048\n" \
"v_mfma_f32_16x16x32_bf16 a[64:67], v[128:131], v[80:83], a[64:67]\n" \
"ds_read_b64_tr_b16 v[172:173], v51 offset:17408\n" \
"v_mfma_f32_16x16x32_bf16 a[64:67], v[132:135], v[84:87], a[64:67]\n" \
"v_mfma_f32_16x16x32_bf16 a[68:71], v[136:139], v[80:83], a[68:71]\n" \
"ds_read_b64_tr_b16 v[174:175], v51 offset:21504\n" \
"v_mfma_f32_16x16x32_bf16 a[68:71], v[140:143], v[84:87], a[68:71]\n" \
"global_load_lds_dwordx4 v43, s[50:51] offset:-1024\n" \
"v_mfma_f32_16x16x32_bf16 a[72:75], v[144:147], v[80:83], a[72:75]\n" \
"ds_read_b64_tr_b16 v[176:177], v50 offset:1536\n" \
"v_mfma_f32_16x16x32_bf16 a[72:75], v[148:151], v[84:87], a[72:75]\n" \
"v_mfma_f32_16x16x32_bf16 a[76:79], v[152:155], v[80:83], a[76:79]\n" \
"ds_read_b64_tr_b16 v[178:179], v50 offset:5632\n" \
"v_mfma_f32_16x16x32_bf16 a[76:79], v[156:159], v[84:87], a[76:79]\n" \
"global_load_lds_dwordx4 v44, s[52:53] offset:0\n" \
"v_mfma_f32_16x16x32_bf16 a[108:111], v[152:155], v[88:91], a[108:111]\n" \
"ds_read_b64_tr_b16 v[180:181], v50 offset:17920\n" \
"v_mfma_f32_16x16x32_bf16 a[108:111], v[156:159], v[92:95], a[108:111]\n" \
"v_mfma_f32_16x16x32_bf16 a[104:107], v[144:147], v[88:91], a[104:107]\n" \
"ds_read_b64_tr_b16 v[182:183], v50 offset:22016\n" \
"v_mfma_f32_16x16x32_bf16 a[104:107], v[148:151], v[92:95], a[104:107]\n" \
"global_load_lds_dwordx4 v45, s[50:51] offset:1024\n" \
"v_mfma_f32_16x16x32_bf16 a[100:103], v[136:139], v[88:91], a[100:103]\n" \
"ds_read_b64_tr_b16 v[184:185], v51 offset:1536\n" \
"v_mfma_f32_16x16x32_bf16 a[100:103], v[140:143], v[92:95], a[100:103]\n" \
"v_mfma_f32_16x16x32_bf16 a[96:99], v[128:131], v[88:91], a[96:99]\n" \
"ds_read_b64_tr_b16 v[186:187], v51 offset:5632\n" \
"v_mfma_f32_16x16x32_bf16 a[96:99], v[132:135], v[92:95], a[96:99]\n" \
"global_load_lds_dwordx4 v46, s[52:53] offset:2048\n" \
"v_mfma_f32_16x16x32_bf16 a[128:131], v[128:131], v[96:99], a[128:131]\n" \
"ds_read_b64_tr_b16 v[188:189], v51 offset:17920\n" \
"v_mfma_f32_16x16x32_bf16 a[128:131], v[132:135], v[100:103], a[128:131]\n" \
"v_mfma_f32_16x16x32_bf16 a[132:135], v[136:139], v[96:99], a[132:135]\n" \
"ds_read_b64_tr_b16 v[190:191], v51 offset:22016\n" \
"v_mfma_f32_16x16x32_bf16 a[132:135], v[140:143], v[100:103], a[132:135]\n" \
"global_load_lds_dwordx4 v47, s[50:51] offset:3072\n" \
"v_mfma_f32_16x16x32_bf16 a[136:139], v[144:147], v[96:99], a[136:139]\n" \
"s_add_u32 s52, s52, s74\n" \
"s_addc_u32 s53, s53, 0\n" \
"v_mfma_f32_16x16x32_bf16 a[136:139], v[148:151], v[100:103], a[136:139]\n" \
"s_add_u32 s50, s50, s74\n" \
"s_addc_u32 s51, s51, 0\n" \
"v_mfma_f32_16x16x32_bf16 a[140:143], v[152:155], v[96:99], a[140:143]\n" \
"v_mfma_f32_16x16x32_bf16 a[140:143], v[156:159], v[100:103], a[140:143]\n" \
"v_mfma_f32_16x16x32_bf16 a[172:175], v[152:155], v[104:107], a[172:175]\n" \
"s_mul_i32 s76, s73, 0x18000\n" \
"s_add_u32 s77, s76, 0xffff8000\n" \
"s_sub_u32 s78, 0x10000, s76\n" \
"v_mfma_f32_16x16x32_bf16 a[172:175], v[156:159], v[108:111], a[172:175]\n" \
"v_add_u32 v48, s77, v48\n" \
"v_add_u32 v49, s77, v49\n" \
"v_mfma_f32_16x16x32_bf16 a[168:171], v[144:147], v[104:107], a[168:171]\n" \
"v_add_u32 v50, s78, v50\n" \
"v_add_u32 v51, s78, v51\n" \
"v_mfma_f32_16x16x32_bf16 a[168:171], v[148:151], v[108:111], a[168:171]\n" \
"v_mfma_f32_16x16x32_bf16 a[164:167], v[136:139], v[104:107], a[164:167]\n" \
"v_mfma_f32_16x16x32_bf16 a[164:167], v[140:143], v[108:111], a[164:167]\n" \
"v_mfma_f32_16x16x32_bf16 a[160:163], v[128:131], v[104:107], a[160:163]\n" \
"v_mfma_f32_16x16x32_bf16 a[160:163], v[132:135], v[108:111], a[160:163]\n" \
"s_waitcnt lgkmcnt(0)\n" \
"v_mfma_f32_16x16x32_bf16 a[192:195], v[128:131], v[112:115], a[192:195]\n" \
"v_mfma_f32_16x16x32_bf16 a[192:195], v[132:135], v[116:119], a[192:195]\n" \
"v_mfma_f32_16x16x32_bf16 a[196:199], v[136:139], v[112:115], a[196:199]\n" \
"v_mfma_f32_16x16x32_bf16 a[196:199], v[140:143], v[116:119], a[196:199]\n" \
"v_mfma_f32_16x16x32_bf16 a[200:203], v[144:147], v[112:115], a[200:203]\n" \
"v_mfma_f32_16x16x32_bf16 a[200:203], v[148:151], v[116:119], a[200:203]\n" \
"v_mfma_f32_16x16x32_bf16 a[204:207], v[152:155], v[112:115], a[204:207]\n" \
"v_mfma_f32_16x16x32_bf16 a[204:207], v[156:159], v[116:119], a[204:207]\n" \
"v_mfma_f32_16x16x32_bf16 a[236:239], v[152:155], v[120:123], a[236:239]\n" \
"v_mfma_f32_16x16x32_bf16 a[236:239], v[156:159], v[124:127], a[236:239]\n" \
"v_mfma_f32_16x16x32_bf16 a[232:235], v[144:147], v[120:123], a[232:235]\n" \
"v_mfma_f32_16x16x32_bf16 a[232:235], v[148:151], v[124:127], a[232:235]\n" \
"v_mfma_f32_16x16x32_bf16 a[228:231], v[136:139], v[120:123], a[228:231]\n" \
"v_mfma_f32_16x16x32_bf16 a[228:231], v[140:143], v[124:127], a[228:231]\n" \
"s_waitcnt vmcnt(11) lgkmcnt(0)\n" \
"v_mfma_f32_16x16x32_bf16 a[224:227], v[128:131], v[120:123], a[224:227]\n" \
"v_mfma_f32_16x16x32_bf16 a[224:227], v[132:135], v[124:127], a[224:227]\n" \
"s_barrier\n" \
"v_mfma_f32_16x16x32_bf16 a[16:19], v[64:67], v[160:163], a[16:19]\n" \
"s_add_u32 m0, s87, 4096\n" \
"ds_read_b64_tr_b16 v[128:129], v50 offset:0\n" \
"v_mfma_f32_16x16x32_bf16 a[16:19], v[68:71], v[164:167], a[16:19]\n" \
"global_load_lds_dwordx4 v32, s[54:55] offset:-4096\n" \
"v_mfma_f32_16x16x32_bf16 a[20:23], v[64:67], v[168:171], a[20:23]\n" \
"ds_read_b64_tr_b16 v[130:131], v50 offset:4096\n" \
"v_mfma_f32_16x16x32_bf16 a[20:23], v[68:71], v[172:175], a[20:23]\n" \
"v_mfma_f32_16x16x32_bf16 a[24:27], v[64:67], v[176:179], a[24:27]\n" \
"ds_read_b64_tr_b16 v[132:133], v50 offset:16384\n" \
"v_mfma_f32_16x16x32_bf16 a[24:27], v[68:71], v[180:183], a[24:27]\n" \
"global_load_lds_dwordx4 v33, s[40:41] offset:-3072\n" \
"v_mfma_f32_16x16x32_bf16 a[28:31], v[64:67], v[184:187], a[28:31]\n" \
"ds_read_b64_tr_b16 v[134:135], v50 offset:20480\n" \
"v_mfma_f32_16x16x32_bf16 a[28:31], v[68:71], v[188:191], a[28:31]\n" \
"v_mfma_f32_16x16x32_bf16 a[60:63], v[72:75], v[184:187], a[60:63]\n" \
"ds_read_b128 v[64:67], v48 offset:0\n" \
"v_mfma_f32_16x16x32_bf16 a[60:63], v[76:79], v[188:191], a[60:63]\n" \
"global_load_lds_dwordx4 v34, s[54:55] offset:-2048\n" \
"v_mfma_f32_16x16x32_bf16 a[56:59], v[72:75], v[176:179], a[56:59]\n" \
"ds_read_b64_tr_b16 v[136:137], v51 offset:0\n" \
"v_mfma_f32_16x16x32_bf16 a[56:59], v[76:79], v[180:183], a[56:59]\n" \
"v_mfma_f32_16x16x32_bf16 a[52:55], v[72:75], v[168:171], a[52:55]\n" \
"ds_read_b128 v[68:71], v49 offset:0\n" \
"v_mfma_f32_16x16x32_bf16 a[52:55], v[76:79], v[172:175], a[52:55]\n" \
"global_load_lds_dwordx4 v35, s[40:41] offset:-1024\n" \
"v_mfma_f32_16x16x32_bf16 a[48:51], v[72:75], v[160:163], a[48:51]\n" \
"ds_read_b64_tr_b16 v[138:139], v51 offset:4096\n" \
"v_mfma_f32_16x16x32_bf16 a[48:51], v[76:79], v[164:167], a[48:51]\n" \
"v_mfma_f32_16x16x32_bf16 a[80:83], v[80:83], v[160:163], a[80:83]\n" \
"ds_read_b128 v[72:75], v48 offset:2048\n" \
"v_mfma_f32_16x16x32_bf16 a[80:83], v[84:87], v[164:167], a[80:83]\n" \
"global_load_lds_dwordx4 v36, s[54:55] offset:0\n" \
"v_mfma_f32_16x16x32_bf16 a[84:87], v[80:83], v[168:171], a[84:87]\n" \
"ds_read_b64_tr_b16 v[140:141], v51 offset:16384\n" \
"v_mfma_f32_16x16x32_bf16 a[84:87], v[84:87], v[172:175], a[84:87]\n" \
"v_mfma_f32_16x16x32_bf16 a[88:91], v[80:83], v[176:179], a[88:91]\n" \
"ds_read_b128 v[76:79], v49 offset:2048\n" \
"v_mfma_f32_16x16x32_bf16 a[88:91], v[84:87], v[180:183], a[88:91]\n" \
"global_load_lds_dwordx4 v37, s[40:41] offset:1024\n" \
"v_mfma_f32_16x16x32_bf16 a[92:95], v[80:83], v[184:187], a[92:95]\n" \
"ds_read_b64_tr_b16 v[142:143], v51 offset:20480\n" \
"v_mfma_f32_16x16x32_bf16 a[92:95], v[84:87], v[188:191], a[92:95]\n" \
"v_mfma_f32_16x16x32_bf16 a[124:127], v[88:91], v[184:187], a[124:127]\n" \
"ds_read_b128 v[80:83], v48 offset:4096\n" \
"v_mfma_f32_16x16x32_bf16 a[124:127], v[92:95], v[188:191], a[124:127]\n" \
"global_load_lds_dwordx4 v38, s[54:55] offset:2048\n" \
"v_mfma_f32_16x16x32_bf16 a[120:123], v[88:91], v[176:179], a[120:123]\n" \
"ds_read_b64_tr_b16 v[144:145], v50 offset:512\n" \
"v_mfma_f32_16x16x32_bf16 a[120:123], v[92:95], v[180:183], a[120:123]\n" \
"v_mfma_f32_16x16x32_bf16 a[116:119], v[88:91], v[168:171], a[116:119]\n" \
"ds_read_b128 v[84:87], v49 offset:4096\n" \
"global_load_lds_dwordx4 v39, s[40:41] offset:3072\n" \
"v_mfma_f32_16x16x32_bf16 a[116:119], v[92:95], v[172:175], a[116:119]\n" \
"s_add_u32 s54, s54, s75\n" \
"s_addc_u32 s55, s55, 0\n" \
"v_mfma_f32_16x16x32_bf16 a[112:115], v[88:91], v[160:163], a[112:115]\n" \
"s_add_u32 s40, s40, s75\n" \
"s_addc_u32 s41, s41, 0\n" \
"ds_read_b64_tr_b16 v[146:147], v50 offset:4608\n" \
"v_mfma_f32_16x16x32_bf16 a[112:115], v[92:95], v[164:167], a[112:115]\n" \
"s_add_u32 s68, s66, 0x200000\n" \
"s_add_u32 s64, s8, s68\n" \
"s_addc_u32 s65, s9, 0\n" \
"v_mfma_f32_16x16x32_bf16 a[144:147], v[96:99], v[160:163], a[144:147]\n" \
"ds_read_b128 v[88:91], v48 offset:6144\n" \
"v_mfma_f32_16x16x32_bf16 a[144:147], v[100:103], v[164:167], a[144:147]\n" \
"global_load_dword v253, v248, s[64:65]\n" \
"v_mfma_f32_16x16x32_bf16 a[148:151], v[96:99], v[168:171], a[148:151]\n" \
"s_add_u32 s68, s67, 0x0\n" \
"ds_read_b64_tr_b16 v[148:149], v50 offset:16896\n" \
"v_mfma_f32_16x16x32_bf16 a[148:151], v[100:103], v[172:175], a[148:151]\n" \
"s_add_u32 s64, s12, s68\n" \
"s_addc_u32 s65, s13, 0\n" \
"v_mfma_f32_16x16x32_bf16 a[152:155], v[96:99], v[176:179], a[152:155]\n" \
"ds_read_b128 v[92:95], v49 offset:6144\n" \
"v_mfma_f32_16x16x32_bf16 a[152:155], v[100:103], v[180:183], a[152:155]\n" \
"Lpg1150_%=:\n" \
"Lst0x1_%=:\n" \
"s_cbranch_execz Lpg3308_%=\n" \
"global_load_dword v253, v248, s[64:65]\n" \
"v_mfma_f32_16x16x32_bf16 a[156:159], v[96:99], v[184:187], a[156:159]\n" \
"ds_read_b64_tr_b16 v[150:151], v50 offset:20992\n" \
"v_mfma_f32_16x16x32_bf16 a[156:159], v[100:103], v[188:191], a[156:159]\n" \
"v_mfma_f32_16x16x32_bf16 a[188:191], v[104:107], v[184:187], a[188:191]\n" \
"ds_read_b128 v[96:99], v48 offset:8192\n" \
"v_mfma_f32_16x16x32_bf16 a[188:191], v[108:111], v[188:191], a[188:191]\n" \
"v_mfma_f32_16x16x32_bf16 a[184:187], v[104:107], v[176:179], a[184:187]\n" \
"ds_read_b64_tr_b16 v[152:153], v51 offset:512\n" \
"v_mfma_f32_16x16x32_bf16 a[184:187], v[108:111], v[180:183], a[184:187]\n" \
"v_mfma_f32_16x16x32_bf16 a[180:183], v[104:107], v[168:171], a[180:183]\n" \
"ds_read_b128 v[100:103], v49 offset:8192\n" \
"v_mfma_f32_16x16x32_bf16 a[180:183], v[108:111], v[172:175], a[180:183]\n" \
"v_mfma_f32_16x16x32_bf16 a[176:179], v[104:107], v[160:163], a[176:179]\n" \
"ds_read_b64_tr_b16 v[154:155], v51 offset:4608\n" \
"v_mfma_f32_16x16x32_bf16 a[176:179], v[108:111], v[164:167], a[176:179]\n" \
"v_mfma_f32_16x16x32_bf16 a[208:211], v[112:115], v[160:163], a[208:211]\n" \
"ds_read_b128 v[104:107], v48 offset:10240\n" \
"v_mfma_f32_16x16x32_bf16 a[208:211], v[116:119], v[164:167], a[208:211]\n" \
"v_mfma_f32_16x16x32_bf16 a[212:215], v[112:115], v[168:171], a[212:215]\n" \
"ds_read_b64_tr_b16 v[156:157], v51 offset:16896\n" \
"v_mfma_f32_16x16x32_bf16 a[212:215], v[116:119], v[172:175], a[212:215]\n" \
"v_mfma_f32_16x16x32_bf16 a[216:219], v[112:115], v[176:179], a[216:219]\n" \
"ds_read_b128 v[108:111], v49 offset:10240\n" \
"v_mfma_f32_16x16x32_bf16 a[216:219], v[116:119], v[180:183], a[216:219]\n" \
"v_mfma_f32_16x16x32_bf16 a[220:223], v[112:115], v[184:187], a[220:223]\n" \
"ds_read_b64_tr_b16 v[158:159], v51 offset:20992\n" \
"v_mfma_f32_16x16x32_bf16 a[220:223], v[116:119], v[188:191], a[220:223]\n" \
"v_mfma_f32_16x16x32_bf16 a[252:255], v[120:123], v[184:187], a[252:255]\n" \
"ds_read_b128 v[112:115], v48 offset:12288\n" \
"v_mfma_f32_16x16x32_bf16 a[252:255], v[124:127], v[188:191], a[252:255]\n" \
"v_mfma_f32_16x16x32_bf16 a[248:251], v[120:123], v[176:179], a[248:251]\n" \
"v_mfma_f32_16x16x32_bf16 a[248:251], v[124:127], v[180:183], a[248:251]\n" \
"v_mfma_f32_16x16x32_bf16 a[244:247], v[120:123], v[168:171], a[244:247]\n" \
"ds_read_b128 v[116:119], v49 offset:12288\n" \
"v_mfma_f32_16x16x32_bf16 a[244:247], v[124:127], v[172:175], a[244:247]\n" \
"v_mfma_f32_16x16x32_bf16 a[240:243], v[120:123], v[160:163], a[240:243]\n" \
"v_mfma_f32_16x16x32_bf16 a[240:243], v[124:127], v[164:167], a[240:243]\n" \
"s_waitcnt lgkmcnt(1)\n" \
"v_mfma_f32_16x16x32_bf16 a[0:3], v[128:131], v[64:67], a[0:3]\n" \
"ds_read_b128 v[120:123], v48 offset:14336\n" \
"v_mfma_f32_16x16x32_bf16 a[0:3], v[132:135], v[68:71], a[0:3]\n" \
"v_mfma_f32_16x16x32_bf16 a[4:7], v[136:139], v[64:67], a[4:7]\n" \
"ds_read_b128 v[124:127], v49 offset:14336\n" \
"v_mfma_f32_16x16x32_bf16 a[4:7], v[140:143], v[68:71], a[4:7]\n" \
"s_add_u32 s68, s66, 0x300000\n" \
"v_mfma_f32_16x16x32_bf16 a[8:11], v[144:147], v[64:67], a[8:11]\n" \
"s_add_u32 s64, s8, s68\n" \
"s_addc_u32 s65, s9, 0\n" \
"ds_read_b64_tr_b16 v[160:161], v50 offset:1024\n" \
"v_mfma_f32_16x16x32_bf16 a[8:11], v[148:151], v[68:71], a[8:11]\n" \
"global_load_dword v253, v248, s[64:65]\n" \
"v_mfma_f32_16x16x32_bf16 a[12:15], v[152:155], v[64:67], a[12:15]\n" \
"s_add_u32 m0, s87, 135168\n" \
"ds_read_b64_tr_b16 v[162:163], v50 offset:5120\n" \
"v_mfma_f32_16x16x32_bf16 a[12:15], v[156:159], v[68:71], a[12:15]\n" \
"global_load_lds_dwordx4 v40, s[52:53] offset:-4096\n" \
"v_mfma_f32_16x16x32_bf16 a[44:47], v[152:155], v[72:75], a[44:47]\n" \
"ds_read_b64_tr_b16 v[164:165], v50 offset:17408\n" \
"v_mfma_f32_16x16x32_bf16 a[44:47], v[156:159], v[76:79], a[44:47]\n" \
"v_mfma_f32_16x16x32_bf16 a[40:43], v[144:147], v[72:75], a[40:43]\n" \
"ds_read_b64_tr_b16 v[166:167], v50 offset:21504\n" \
"v_mfma_f32_16x16x32_bf16 a[40:43], v[148:151], v[76:79], a[40:43]\n" \
"global_load_lds_dwordx4 v41, s[50:51] offset:-3072\n" \
"v_mfma_f32_16x16x32_bf16 a[36:39], v[136:139], v[72:75], a[36:39]\n" \
"ds_read_b64_tr_b16 v[168:169], v51 offset:1024\n" \
"v_mfma_f32_16x16x32_bf16 a[36:39], v[140:143], v[76:79], a[36:39]\n" \
"v_mfma_f32_16x16x32_bf16 a[32:35], v[128:131], v[72:75], a[32:35]\n" \
"ds_read_b64_tr_b16 v[170:171], v51 offset:5120\n" \
"v_mfma_f32_16x16x32_bf16 a[32:35], v[132:135], v[76:79], a[32:35]\n" \
"global_load_lds_dwordx4 v42, s[52:53] offset:-2048\n" \
"v_mfma_f32_16x16x32_bf16 a[64:67], v[128:131], v[80:83], a[64:67]\n" \
"ds_read_b64_tr_b16 v[172:173], v51 offset:17408\n" \
"v_mfma_f32_16x16x32_bf16 a[64:67], v[132:135], v[84:87], a[64:67]\n" \
"v_mfma_f32_16x16x32_bf16 a[68:71], v[136:139], v[80:83], a[68:71]\n" \
"ds_read_b64_tr_b16 v[174:175], v51 offset:21504\n" \
"v_mfma_f32_16x16x32_bf16 a[68:71], v[140:143], v[84:87], a[68:71]\n" \
"global_load_lds_dwordx4 v43, s[50:51] offset:-1024\n" \
"v_mfma_f32_16x16x32_bf16 a[72:75], v[144:147], v[80:83], a[72:75]\n" \
"ds_read_b64_tr_b16 v[176:177], v50 offset:1536\n" \
"v_mfma_f32_16x16x32_bf16 a[72:75], v[148:151], v[84:87], a[72:75]\n" \
"v_mfma_f32_16x16x32_bf16 a[76:79], v[152:155], v[80:83], a[76:79]\n" \
"ds_read_b64_tr_b16 v[178:179], v50 offset:5632\n" \
"v_mfma_f32_16x16x32_bf16 a[76:79], v[156:159], v[84:87], a[76:79]\n" \
"global_load_lds_dwordx4 v44, s[52:53] offset:0\n" \
"v_mfma_f32_16x16x32_bf16 a[108:111], v[152:155], v[88:91], a[108:111]\n" \
"ds_read_b64_tr_b16 v[180:181], v50 offset:17920\n" \
"v_mfma_f32_16x16x32_bf16 a[108:111], v[156:159], v[92:95], a[108:111]\n" \
"v_mfma_f32_16x16x32_bf16 a[104:107], v[144:147], v[88:91], a[104:107]\n" \
"ds_read_b64_tr_b16 v[182:183], v50 offset:22016\n" \
"v_mfma_f32_16x16x32_bf16 a[104:107], v[148:151], v[92:95], a[104:107]\n" \
"global_load_lds_dwordx4 v45, s[50:51] offset:1024\n" \
"v_mfma_f32_16x16x32_bf16 a[100:103], v[136:139], v[88:91], a[100:103]\n" \
"ds_read_b64_tr_b16 v[184:185], v51 offset:1536\n" \
"v_mfma_f32_16x16x32_bf16 a[100:103], v[140:143], v[92:95], a[100:103]\n" \
"v_mfma_f32_16x16x32_bf16 a[96:99], v[128:131], v[88:91], a[96:99]\n" \
"ds_read_b64_tr_b16 v[186:187], v51 offset:5632\n" \
"v_mfma_f32_16x16x32_bf16 a[96:99], v[132:135], v[92:95], a[96:99]\n" \
"global_load_lds_dwordx4 v46, s[52:53] offset:2048\n" \
"v_mfma_f32_16x16x32_bf16 a[128:131], v[128:131], v[96:99], a[128:131]\n" \
"ds_read_b64_tr_b16 v[188:189], v51 offset:17920\n" \
"v_mfma_f32_16x16x32_bf16 a[128:131], v[132:135], v[100:103], a[128:131]\n" \
"v_mfma_f32_16x16x32_bf16 a[132:135], v[136:139], v[96:99], a[132:135]\n" \
"ds_read_b64_tr_b16 v[190:191], v51 offset:22016\n" \
"v_mfma_f32_16x16x32_bf16 a[132:135], v[140:143], v[100:103], a[132:135]\n" \
"global_load_lds_dwordx4 v47, s[50:51] offset:3072\n" \
"v_mfma_f32_16x16x32_bf16 a[136:139], v[144:147], v[96:99], a[136:139]\n" \
"s_add_u32 s52, s52, s74\n" \
"s_addc_u32 s53, s53, 0\n" \
"v_mfma_f32_16x16x32_bf16 a[136:139], v[148:151], v[100:103], a[136:139]\n" \
"s_add_u32 s50, s50, s74\n" \
"s_addc_u32 s51, s51, 0\n" \
"v_mfma_f32_16x16x32_bf16 a[140:143], v[152:155], v[96:99], a[140:143]\n" \
"v_mfma_f32_16x16x32_bf16 a[140:143], v[156:159], v[100:103], a[140:143]\n" \
"v_mfma_f32_16x16x32_bf16 a[172:175], v[152:155], v[104:107], a[172:175]\n" \
"s_mul_i32 s76, s73, 0x10000\n" \
"s_add_u32 s77, s76, 0xfffe8000\n" \
"s_sub_u32 s78, 0xffff8000, s76\n" \
"v_mfma_f32_16x16x32_bf16 a[172:175], v[156:159], v[108:111], a[172:175]\n" \
"v_add_u32 v48, s77, v48\n" \
"v_add_u32 v49, s77, v49\n" \
"v_mfma_f32_16x16x32_bf16 a[168:171], v[144:147], v[104:107], a[168:171]\n" \
"v_add_u32 v50, s78, v50\n" \
"v_add_u32 v51, s78, v51\n" \
"v_mfma_f32_16x16x32_bf16 a[168:171], v[148:151], v[108:111], a[168:171]\n" \
"v_mfma_f32_16x16x32_bf16 a[164:167], v[136:139], v[104:107], a[164:167]\n" \
"v_mfma_f32_16x16x32_bf16 a[164:167], v[140:143], v[108:111], a[164:167]\n" \
"v_mfma_f32_16x16x32_bf16 a[160:163], v[128:131], v[104:107], a[160:163]\n" \
"v_mfma_f32_16x16x32_bf16 a[160:163], v[132:135], v[108:111], a[160:163]\n" \
"s_waitcnt lgkmcnt(0)\n" \
"v_mfma_f32_16x16x32_bf16 a[192:195], v[128:131], v[112:115], a[192:195]\n" \
"v_mfma_f32_16x16x32_bf16 a[192:195], v[132:135], v[116:119], a[192:195]\n" \
"v_mfma_f32_16x16x32_bf16 a[196:199], v[136:139], v[112:115], a[196:199]\n" \
"v_mfma_f32_16x16x32_bf16 a[196:199], v[140:143], v[116:119], a[196:199]\n" \
"v_mfma_f32_16x16x32_bf16 a[200:203], v[144:147], v[112:115], a[200:203]\n" \
"v_mfma_f32_16x16x32_bf16 a[200:203], v[148:151], v[116:119], a[200:203]\n" \
"v_mfma_f32_16x16x32_bf16 a[204:207], v[152:155], v[112:115], a[204:207]\n" \
"v_mfma_f32_16x16x32_bf16 a[204:207], v[156:159], v[116:119], a[204:207]\n" \
"v_mfma_f32_16x16x32_bf16 a[236:239], v[152:155], v[120:123], a[236:239]\n" \
"v_mfma_f32_16x16x32_bf16 a[236:239], v[156:159], v[124:127], a[236:239]\n" \
"v_mfma_f32_16x16x32_bf16 a[232:235], v[144:147], v[120:123], a[232:235]\n" \
"v_mfma_f32_16x16x32_bf16 a[232:235], v[148:151], v[124:127], a[232:235]\n" \
"v_mfma_f32_16x16x32_bf16 a[228:231], v[136:139], v[120:123], a[228:231]\n" \
"v_mfma_f32_16x16x32_bf16 a[228:231], v[140:143], v[124:127], a[228:231]\n" \
"s_waitcnt vmcnt(11) lgkmcnt(0)\n" \
"v_mfma_f32_16x16x32_bf16 a[224:227], v[128:131], v[120:123], a[224:227]\n" \
"v_mfma_f32_16x16x32_bf16 a[224:227], v[132:135], v[124:127], a[224:227]\n" \
"s_barrier\n" \
"v_mfma_f32_16x16x32_bf16 a[16:19], v[64:67], v[160:163], a[16:19]\n" \
"s_add_u32 m0, s87, 69632\n" \
"ds_read_b64_tr_b16 v[128:129], v50 offset:0\n" \
"v_mfma_f32_16x16x32_bf16 a[16:19], v[68:71], v[164:167], a[16:19]\n" \
"global_load_lds_dwordx4 v32, s[54:55] offset:-4096\n" \
"v_mfma_f32_16x16x32_bf16 a[20:23], v[64:67], v[168:171], a[20:23]\n" \
"ds_read_b64_tr_b16 v[130:131], v50 offset:4096\n" \
"v_mfma_f32_16x16x32_bf16 a[20:23], v[68:71], v[172:175], a[20:23]\n" \
"v_mfma_f32_16x16x32_bf16 a[24:27], v[64:67], v[176:179], a[24:27]\n" \
"ds_read_b64_tr_b16 v[132:133], v50 offset:16384\n" \
"v_mfma_f32_16x16x32_bf16 a[24:27], v[68:71], v[180:183], a[24:27]\n" \
"global_load_lds_dwordx4 v33, s[40:41] offset:-3072\n" \
"v_mfma_f32_16x16x32_bf16 a[28:31], v[64:67], v[184:187], a[28:31]\n" \
"ds_read_b64_tr_b16 v[134:135], v50 offset:20480\n" \
"v_mfma_f32_16x16x32_bf16 a[28:31], v[68:71], v[188:191], a[28:31]\n" \
"v_mfma_f32_16x16x32_bf16 a[60:63], v[72:75], v[184:187], a[60:63]\n" \
"ds_read_b128 v[64:67], v48 offset:0\n" \
"v_mfma_f32_16x16x32_bf16 a[60:63], v[76:79], v[188:191], a[60:63]\n" \
"global_load_lds_dwordx4 v34, s[54:55] offset:-2048\n" \
"v_mfma_f32_16x16x32_bf16 a[56:59], v[72:75], v[176:179], a[56:59]\n" \
"ds_read_b64_tr_b16 v[136:137], v51 offset:0\n" \
"v_mfma_f32_16x16x32_bf16 a[56:59], v[76:79], v[180:183], a[56:59]\n" \
"v_mfma_f32_16x16x32_bf16 a[52:55], v[72:75], v[168:171], a[52:55]\n" \
"ds_read_b128 v[68:71], v49 offset:0\n" \
"v_mfma_f32_16x16x32_bf16 a[52:55], v[76:79], v[172:175], a[52:55]\n" \
"global_load_lds_dwordx4 v35, s[40:41] offset:-1024\n" \
"v_mfma_f32_16x16x32_bf16 a[48:51], v[72:75], v[160:163], a[48:51]\n" \
"ds_read_b64_tr_b16 v[138:139], v51 offset:4096\n" \
"v_mfma_f32_16x16x32_bf16 a[48:51], v[76:79], v[164:167], a[48:51]\n" \
"v_mfma_f32_16x16x32_bf16 a[80:83], v[80:83], v[160:163], a[80:83]\n" \
"ds_read_b128 v[72:75], v48 offset:2048\n" \
"v_mfma_f32_16x16x32_bf16 a[80:83], v[84:87], v[164:167], a[80:83]\n" \
"global_load_lds_dwordx4 v36, s[54:55] offset:0\n" \
"v_mfma_f32_16x16x32_bf16 a[84:87], v[80:83], v[168:171], a[84:87]\n" \
"ds_read_b64_tr_b16 v[140:141], v51 offset:16384\n" \
"v_mfma_f32_16x16x32_bf16 a[84:87], v[84:87], v[172:175], a[84:87]\n" \
"v_mfma_f32_16x16x32_bf16 a[88:91], v[80:83], v[176:179], a[88:91]\n" \
"ds_read_b128 v[76:79], v49 offset:2048\n" \
"v_mfma_f32_16x16x32_bf16 a[88:91], v[84:87], v[180:183], a[88:91]\n" \
"global_load_lds_dwordx4 v37, s[40:41] offset:1024\n" \
"v_mfma_f32_16x16x32_bf16 a[92:95], v[80:83], v[184:187], a[92:95]\n" \
"ds_read_b64_tr_b16 v[142:143], v51 offset:20480\n" \
"v_mfma_f32_16x16x32_bf16 a[92:95], v[84:87], v[188:191], a[92:95]\n" \
"v_mfma_f32_16x16x32_bf16 a[124:127], v[88:91], v[184:187], a[124:127]\n" \
"ds_read_b128 v[80:83], v48 offset:4096\n" \
"v_mfma_f32_16x16x32_bf16 a[124:127], v[92:95], v[188:191], a[124:127]\n" \
"global_load_lds_dwordx4 v38, s[54:55] offset:2048\n" \
"v_mfma_f32_16x16x32_bf16 a[120:123], v[88:91], v[176:179], a[120:123]\n" \
"ds_read_b64_tr_b16 v[144:145], v50 offset:512\n" \
"v_mfma_f32_16x16x32_bf16 a[120:123], v[92:95], v[180:183], a[120:123]\n" \
"v_mfma_f32_16x16x32_bf16 a[116:119], v[88:91], v[168:171], a[116:119]\n" \
"ds_read_b128 v[84:87], v49 offset:4096\n" \
"global_load_lds_dwordx4 v39, s[40:41] offset:3072\n" \
"v_mfma_f32_16x16x32_bf16 a[116:119], v[92:95], v[172:175], a[116:119]\n" \
"s_add_u32 s54, s54, s75\n" \
"s_addc_u32 s55, s55, 0\n" \
"v_mfma_f32_16x16x32_bf16 a[112:115], v[88:91], v[160:163], a[112:115]\n" \
"s_add_u32 s40, s40, s75\n" \
"s_addc_u32 s41, s41, 0\n" \
"ds_read_b64_tr_b16 v[146:147], v50 offset:4608\n" \
"v_mfma_f32_16x16x32_bf16 a[112:115], v[92:95], v[164:167], a[112:115]\n" \
"s_add_u32 s68, s67, 0x100000\n" \
"s_add_u32 s64, s12, s68\n" \
"s_addc_u32 s65, s13, 0\n" \
"v_mfma_f32_16x16x32_bf16 a[144:147], v[96:99], v[160:163], a[144:147]\n" \
"ds_read_b128 v[88:91], v48 offset:6144\n" \
"v_mfma_f32_16x16x32_bf16 a[144:147], v[100:103], v[164:167], a[144:147]\n" \
"global_load_dword v253, v248, s[64:65]\n" \
"v_mfma_f32_16x16x32_bf16 a[148:151], v[96:99], v[168:171], a[148:151]\n" \
"s_add_u32 s68, s67, 0x500000\n" \
"ds_read_b64_tr_b16 v[148:149], v50 offset:16896\n" \
"v_mfma_f32_16x16x32_bf16 a[148:151], v[100:103], v[172:175], a[148:151]\n" \
"s_add_u32 s64, s12, s68\n" \
"s_addc_u32 s65, s13, 0\n" \
"v_mfma_f32_16x16x32_bf16 a[152:155], v[96:99], v[176:179], a[152:155]\n" \
"ds_read_b128 v[92:95], v49 offset:6144\n" \
"v_mfma_f32_16x16x32_bf16 a[152:155], v[100:103], v[180:183], a[152:155]\n" \
"global_load_dword v253, v248, s[64:65]\n" \
"v_mfma_f32_16x16x32_bf16 a[156:159], v[96:99], v[184:187], a[156:159]\n" \
"ds_read_b64_tr_b16 v[150:151], v50 offset:20992\n" \
"v_mfma_f32_16x16x32_bf16 a[156:159], v[100:103], v[188:191], a[156:159]\n" \
"v_mfma_f32_16x16x32_bf16 a[188:191], v[104:107], v[184:187], a[188:191]\n" \
"ds_read_b128 v[96:99], v48 offset:8192\n" \
"v_mfma_f32_16x16x32_bf16 a[188:191], v[108:111], v[188:191], a[188:191]\n" \
"v_mfma_f32_16x16x32_bf16 a[184:187], v[104:107], v[176:179], a[184:187]\n" \
"ds_read_b64_tr_b16 v[152:153], v51 offset:512\n" \
"v_mfma_f32_16x16x32_bf16 a[184:187], v[108:111], v[180:183], a[184:187]\n" \
"v_mfma_f32_16x16x32_bf16 a[180:183], v[104:107], v[168:171], a[180:183]\n" \
"ds_read_b128 v[100:103], v49 offset:8192\n" \
"v_mfma_f32_16x16x32_bf16 a[180:183], v[108:111], v[172:175], a[180:183]\n" \
"v_mfma_f32_16x16x32_bf16 a[176:179], v[104:107], v[160:163], a[176:179]\n" \
"ds_read_b64_tr_b16 v[154:155], v51 offset:4608\n" \
"v_mfma_f32_16x16x32_bf16 a[176:179], v[108:111], v[164:167], a[176:179]\n" \
"v_mfma_f32_16x16x32_bf16 a[208:211], v[112:115], v[160:163], a[208:211]\n" \
"ds_read_b128 v[104:107], v48 offset:10240\n" \
"v_mfma_f32_16x16x32_bf16 a[208:211], v[116:119], v[164:167], a[208:211]\n" \
"v_mfma_f32_16x16x32_bf16 a[212:215], v[112:115], v[168:171], a[212:215]\n" \
"ds_read_b64_tr_b16 v[156:157], v51 offset:16896\n" \
"v_mfma_f32_16x16x32_bf16 a[212:215], v[116:119], v[172:175], a[212:215]\n" \
"v_mfma_f32_16x16x32_bf16 a[216:219], v[112:115], v[176:179], a[216:219]\n" \
"ds_read_b128 v[108:111], v49 offset:10240\n" \
"v_mfma_f32_16x16x32_bf16 a[216:219], v[116:119], v[180:183], a[216:219]\n" \
"v_mfma_f32_16x16x32_bf16 a[220:223], v[112:115], v[184:187], a[220:223]\n" \
"ds_read_b64_tr_b16 v[158:159], v51 offset:20992\n" \
"v_mfma_f32_16x16x32_bf16 a[220:223], v[116:119], v[188:191], a[220:223]\n" \
"v_mfma_f32_16x16x32_bf16 a[252:255], v[120:123], v[184:187], a[252:255]\n" \
"ds_read_b128 v[112:115], v48 offset:12288\n" \
"v_mfma_f32_16x16x32_bf16 a[252:255], v[124:127], v[188:191], a[252:255]\n" \
"v_mfma_f32_16x16x32_bf16 a[248:251], v[120:123], v[176:179], a[248:251]\n" \
"v_mfma_f32_16x16x32_bf16 a[248:251], v[124:127], v[180:183], a[248:251]\n" \
"v_mfma_f32_16x16x32_bf16 a[244:247], v[120:123], v[168:171], a[244:247]\n" \
"ds_read_b128 v[116:119], v49 offset:12288\n" \
"v_mfma_f32_16x16x32_bf16 a[244:247], v[124:127], v[172:175], a[244:247]\n" \
"v_mfma_f32_16x16x32_bf16 a[240:243], v[120:123], v[160:163], a[240:243]\n" \
"v_mfma_f32_16x16x32_bf16 a[240:243], v[124:127], v[164:167], a[240:243]\n" \
"s_waitcnt lgkmcnt(1)\n" \
"v_mfma_f32_16x16x32_bf16 a[0:3], v[128:131], v[64:67], a[0:3]\n" \
"ds_read_b128 v[120:123], v48 offset:14336\n" \
"v_mfma_f32_16x16x32_bf16 a[0:3], v[132:135], v[68:71], a[0:3]\n" \
"v_mfma_f32_16x16x32_bf16 a[4:7], v[136:139], v[64:67], a[4:7]\n" \
"ds_read_b128 v[124:127], v49 offset:14336\n" \
"v_mfma_f32_16x16x32_bf16 a[4:7], v[140:143], v[68:71], a[4:7]\n" \
"s_add_u32 s68, s67, 0x200000\n" \
"v_mfma_f32_16x16x32_bf16 a[8:11], v[144:147], v[64:67], a[8:11]\n" \
"s_add_u32 s64, s12, s68\n" \
"s_addc_u32 s65, s13, 0\n" \
"ds_read_b64_tr_b16 v[160:161], v50 offset:1024\n" \
"v_mfma_f32_16x16x32_bf16 a[8:11], v[148:151], v[68:71], a[8:11]\n" \
"global_load_dword v253, v248, s[64:65]\n" \
"v_mfma_f32_16x16x32_bf16 a[12:15], v[152:155], v[64:67], a[12:15]\n" \
"s_add_u32 m0, s87, 102400\n" \
"ds_read_b64_tr_b16 v[162:163], v50 offset:5120\n" \
"v_mfma_f32_16x16x32_bf16 a[12:15], v[156:159], v[68:71], a[12:15]\n" \
"global_load_lds_dwordx4 v40, s[52:53] offset:-4096\n" \
"v_mfma_f32_16x16x32_bf16 a[44:47], v[152:155], v[72:75], a[44:47]\n" \
"ds_read_b64_tr_b16 v[164:165], v50 offset:17408\n" \
"v_mfma_f32_16x16x32_bf16 a[44:47], v[156:159], v[76:79], a[44:47]\n" \
"v_mfma_f32_16x16x32_bf16 a[40:43], v[144:147], v[72:75], a[40:43]\n" \
"ds_read_b64_tr_b16 v[166:167], v50 offset:21504\n" \
"v_mfma_f32_16x16x32_bf16 a[40:43], v[148:151], v[76:79], a[40:43]\n" \
"global_load_lds_dwordx4 v41, s[50:51] offset:-3072\n" \
"v_mfma_f32_16x16x32_bf16 a[36:39], v[136:139], v[72:75], a[36:39]\n" \
"ds_read_b64_tr_b16 v[168:169], v51 offset:1024\n" \
"v_mfma_f32_16x16x32_bf16 a[36:39], v[140:143], v[76:79], a[36:39]\n" \
"v_mfma_f32_16x16x32_bf16 a[32:35], v[128:131], v[72:75], a[32:35]\n" \
"ds_read_b64_tr_b16 v[170:171], v51 offset:5120\n" \
"v_mfma_f32_16x16x32_bf16 a[32:35], v[132:135], v[76:79], a[32:35]\n" \
"global_load_lds_dwordx4 v42, s[52:53] offset:-2048\n" \
"v_mfma_f32_16x16x32_bf16 a[64:67], v[128:131], v[80:83], a[64:67]\n" \
"ds_read_b64_tr_b16 v[172:173], v51 offset:17408\n" \
"v_mfma_f32_16x16x32_bf16 a[64:67], v[132:135], v[84:87], a[64:67]\n" \
"v_mfma_f32_16x16x32_bf16 a[68:71], v[136:139], v[80:83], a[68:71]\n" \
"ds_read_b64_tr_b16 v[174:175], v51 offset:21504\n" \
"v_mfma_f32_16x16x32_bf16 a[68:71], v[140:143], v[84:87], a[68:71]\n" \
"global_load_lds_dwordx4 v43, s[50:51] offset:-1024\n" \
"v_mfma_f32_16x16x32_bf16 a[72:75], v[144:147], v[80:83], a[72:75]\n" \
"ds_read_b64_tr_b16 v[176:177], v50 offset:1536\n" \
"v_mfma_f32_16x16x32_bf16 a[72:75], v[148:151], v[84:87], a[72:75]\n" \
"v_mfma_f32_16x16x32_bf16 a[76:79], v[152:155], v[80:83], a[76:79]\n" \
"ds_read_b64_tr_b16 v[178:179], v50 offset:5632\n" \
"v_mfma_f32_16x16x32_bf16 a[76:79], v[156:159], v[84:87], a[76:79]\n" \
"global_load_lds_dwordx4 v44, s[52:53] offset:0\n" \
"v_mfma_f32_16x16x32_bf16 a[108:111], v[152:155], v[88:91], a[108:111]\n" \
"ds_read_b64_tr_b16 v[180:181], v50 offset:17920\n" \
"v_mfma_f32_16x16x32_bf16 a[108:111], v[156:159], v[92:95], a[108:111]\n" \
"v_mfma_f32_16x16x32_bf16 a[104:107], v[144:147], v[88:91], a[104:107]\n" \
"ds_read_b64_tr_b16 v[182:183], v50 offset:22016\n" \
"v_mfma_f32_16x16x32_bf16 a[104:107], v[148:151], v[92:95], a[104:107]\n" \
"global_load_lds_dwordx4 v45, s[50:51] offset:1024\n" \
"v_mfma_f32_16x16x32_bf16 a[100:103], v[136:139], v[88:91], a[100:103]\n" \
"ds_read_b64_tr_b16 v[184:185], v51 offset:1536\n" \
"v_mfma_f32_16x16x32_bf16 a[100:103], v[140:143], v[92:95], a[100:103]\n" \
"v_mfma_f32_16x16x32_bf16 a[96:99], v[128:131], v[88:91], a[96:99]\n" \
"ds_read_b64_tr_b16 v[186:187], v51 offset:5632\n" \
"v_mfma_f32_16x16x32_bf16 a[96:99], v[132:135], v[92:95], a[96:99]\n" \
"global_load_lds_dwordx4 v46, s[52:53] offset:2048\n" \
"v_mfma_f32_16x16x32_bf16 a[128:131], v[128:131], v[96:99], a[128:131]\n" \
"ds_read_b64_tr_b16 v[188:189], v51 offset:17920\n" \
"v_mfma_f32_16x16x32_bf16 a[128:131], v[132:135], v[100:103], a[128:131]\n" \
"v_mfma_f32_16x16x32_bf16 a[132:135], v[136:139], v[96:99], a[132:135]\n" \
"ds_read_b64_tr_b16 v[190:191], v51 offset:22016\n" \
"v_mfma_f32_16x16x32_bf16 a[132:135], v[140:143], v[100:103], a[132:135]\n" \
"global_load_lds_dwordx4 v47, s[50:51] offset:3072\n" \
"v_mfma_f32_16x16x32_bf16 a[136:139], v[144:147], v[96:99], a[136:139]\n" \
"v_mfma_f32_16x16x32_bf16 a[136:139], v[148:151], v[100:103], a[136:139]\n" \
"v_mfma_f32_16x16x32_bf16 a[140:143], v[152:155], v[96:99], a[140:143]\n" \
"v_mfma_f32_16x16x32_bf16 a[140:143], v[156:159], v[100:103], a[140:143]\n" \
"v_mfma_f32_16x16x32_bf16 a[172:175], v[152:155], v[104:107], a[172:175]\n" \
"s_mul_i32 s76, s73, 0x8000\n" \
"s_add_u32 s77, s76, 0x10000\n" \
"s_sub_u32 s78, 0x18000, s76\n" \
"v_mfma_f32_16x16x32_bf16 a[172:175], v[156:159], v[108:111], a[172:175]\n" \
"v_add_u32 v48, s77, v48\n" \
"v_add_u32 v49, s77, v49\n" \
"v_mfma_f32_16x16x32_bf16 a[168:171], v[144:147], v[104:107], a[168:171]\n" \
"v_add_u32 v50, s78, v50\n" \
"v_add_u32 v51, s78, v51\n" \
"v_mfma_f32_16x16x32_bf16 a[168:171], v[148:151], v[108:111], a[168:171]\n" \
"v_mfma_f32_16x16x32_bf16 a[164:167], v[136:139], v[104:107], a[164:167]\n" \
"v_mfma_f32_16x16x32_bf16 a[164:167], v[140:143], v[108:111], a[164:167]\n" \
"v_mfma_f32_16x16x32_bf16 a[160:163], v[128:131], v[104:107], a[160:163]\n" \
"v_mfma_f32_16x16x32_bf16 a[160:163], v[132:135], v[108:111], a[160:163]\n" \
"s_waitcnt lgkmcnt(0)\n" \
"v_mfma_f32_16x16x32_bf16 a[192:195], v[128:131], v[112:115], a[192:195]\n" \
"v_mfma_f32_16x16x32_bf16 a[192:195], v[132:135], v[116:119], a[192:195]\n" \
"v_mfma_f32_16x16x32_bf16 a[196:199], v[136:139], v[112:115], a[196:199]\n" \
"v_mfma_f32_16x16x32_bf16 a[196:199], v[140:143], v[116:119], a[196:199]\n" \
"v_mfma_f32_16x16x32_bf16 a[200:203], v[144:147], v[112:115], a[200:203]\n" \
"v_mfma_f32_16x16x32_bf16 a[200:203], v[148:151], v[116:119], a[200:203]\n" \
"v_mfma_f32_16x16x32_bf16 a[204:207], v[152:155], v[112:115], a[204:207]\n" \
"v_mfma_f32_16x16x32_bf16 a[204:207], v[156:159], v[116:119], a[204:207]\n" \
"v_mfma_f32_16x16x32_bf16 a[236:239], v[152:155], v[120:123], a[236:239]\n" \
"v_mfma_f32_16x16x32_bf16 a[236:239], v[156:159], v[124:127], a[236:239]\n" \
"v_mfma_f32_16x16x32_bf16 a[232:235], v[144:147], v[120:123], a[232:235]\n" \
"v_mfma_f32_16x16x32_bf16 a[232:235], v[148:151], v[124:127], a[232:235]\n" \
"v_mfma_f32_16x16x32_bf16 a[228:231], v[136:139], v[120:123], a[228:231]\n" \
"v_mfma_f32_16x16x32_bf16 a[228:231], v[140:143], v[124:127], a[228:231]\n" \
"s_waitcnt vmcnt(11) lgkmcnt(0)\n" \
"v_mfma_f32_16x16x32_bf16 a[224:227], v[128:131], v[120:123], a[224:227]\n" \
"v_mfma_f32_16x16x32_bf16 a[224:227], v[132:135], v[124:127], a[224:227]\n" \
"s_barrier\n" \
"v_mfma_f32_16x16x32_bf16 a[16:19], v[64:67], v[160:163], a[16:19]\n" \
"s_add_u32 m0, s87, 36864\n" \
"ds_read_b64_tr_b16 v[128:129], v50 offset:0\n" \
"v_mfma_f32_16x16x32_bf16 a[16:19], v[68:71], v[164:167], a[16:19]\n" \
"global_load_lds_dwordx4 v32, s[54:55] offset:-4096\n" \
"v_mfma_f32_16x16x32_bf16 a[20:23], v[64:67], v[168:171], a[20:23]\n" \
"ds_read_b64_tr_b16 v[130:131], v50 offset:4096\n" \
"v_mfma_f32_16x16x32_bf16 a[20:23], v[68:71], v[172:175], a[20:23]\n" \
"v_mfma_f32_16x16x32_bf16 a[24:27], v[64:67], v[176:179], a[24:27]\n" \
"ds_read_b64_tr_b16 v[132:133], v50 offset:16384\n" \
"v_mfma_f32_16x16x32_bf16 a[24:27], v[68:71], v[180:183], a[24:27]\n" \
"global_load_lds_dwordx4 v33, s[40:41] offset:-3072\n" \
"v_mfma_f32_16x16x32_bf16 a[28:31], v[64:67], v[184:187], a[28:31]\n" \
"ds_read_b64_tr_b16 v[134:135], v50 offset:20480\n" \
"v_mfma_f32_16x16x32_bf16 a[28:31], v[68:71], v[188:191], a[28:31]\n" \
"v_mfma_f32_16x16x32_bf16 a[60:63], v[72:75], v[184:187], a[60:63]\n" \
"ds_read_b128 v[64:67], v48 offset:0\n" \
"v_mfma_f32_16x16x32_bf16 a[60:63], v[76:79], v[188:191], a[60:63]\n" \
"global_load_lds_dwordx4 v34, s[54:55] offset:-2048\n" \
"v_mfma_f32_16x16x32_bf16 a[56:59], v[72:75], v[176:179], a[56:59]\n" \
"ds_read_b64_tr_b16 v[136:137], v51 offset:0\n" \
"v_mfma_f32_16x16x32_bf16 a[56:59], v[76:79], v[180:183], a[56:59]\n" \
"v_mfma_f32_16x16x32_bf16 a[52:55], v[72:75], v[168:171], a[52:55]\n" \
"ds_read_b128 v[68:71], v49 offset:0\n" \
"v_mfma_f32_16x16x32_bf16 a[52:55], v[76:79], v[172:175], a[52:55]\n" \
"global_load_lds_dwordx4 v35, s[40:41] offset:-1024\n" \
"v_mfma_f32_16x16x32_bf16 a[48:51], v[72:75], v[160:163], a[48:51]\n" \
"ds_read_b64_tr_b16 v[138:139], v51 offset:4096\n" \
"v_mfma_f32_16x16x32_bf16 a[48:51], v[76:79], v[164:167], a[48:51]\n" \
"v_mfma_f32_16x16x32_bf16 a[80:83], v[80:83], v[160:163], a[80:83]\n" \
"ds_read_b128 v[72:75], v48 offset:2048\n" \
"v_mfma_f32_16x16x32_bf16 a[80:83], v[84:87], v[164:167], a[80:83]\n" \
"global_load_lds_dwordx4 v36, s[54:55] offset:0\n" \
"v_mfma_f32_16x16x32_bf16 a[84:87], v[80:83], v[168:171], a[84:87]\n" \
"ds_read_b64_tr_b16 v[140:141], v51 offset:16384\n" \
"v_mfma_f32_16x16x32_bf16 a[84:87], v[84:87], v[172:175], a[84:87]\n" \
"v_mfma_f32_16x16x32_bf16 a[88:91], v[80:83], v[176:179], a[88:91]\n" \
"ds_read_b128 v[76:79], v49 offset:2048\n" \
"v_mfma_f32_16x16x32_bf16 a[88:91], v[84:87], v[180:183], a[88:91]\n" \
"global_load_lds_dwordx4 v37, s[40:41] offset:1024\n" \
"v_mfma_f32_16x16x32_bf16 a[92:95], v[80:83], v[184:187], a[92:95]\n" \
"ds_read_b64_tr_b16 v[142:143], v51 offset:20480\n" \
"v_mfma_f32_16x16x32_bf16 a[92:95], v[84:87], v[188:191], a[92:95]\n" \
"v_mfma_f32_16x16x32_bf16 a[124:127], v[88:91], v[184:187], a[124:127]\n" \
"ds_read_b128 v[80:83], v48 offset:4096\n" \
"v_mfma_f32_16x16x32_bf16 a[124:127], v[92:95], v[188:191], a[124:127]\n" \
"global_load_lds_dwordx4 v38, s[54:55] offset:2048\n" \
"v_mfma_f32_16x16x32_bf16 a[120:123], v[88:91], v[176:179], a[120:123]\n" \
"ds_read_b64_tr_b16 v[144:145], v50 offset:512\n" \
"v_mfma_f32_16x16x32_bf16 a[120:123], v[92:95], v[180:183], a[120:123]\n" \
"v_mfma_f32_16x16x32_bf16 a[116:119], v[88:91], v[168:171], a[116:119]\n" \
"ds_read_b128 v[84:87], v49 offset:4096\n" \
"v_mfma_f32_16x16x32_bf16 a[116:119], v[92:95], v[172:175], a[116:119]\n" \
"global_load_lds_dwordx4 v39, s[40:41] offset:3072\n" \
"s_add_u32 s68, s67, 0x300000\n" \
"v_mfma_f32_16x16x32_bf16 a[112:115], v[88:91], v[160:163], a[112:115]\n" \
"s_add_u32 s64, s12, s68\n" \
"s_addc_u32 s65, s13, 0\n" \
"ds_read_b64_tr_b16 v[146:147], v50 offset:4608\n" \
"v_mfma_f32_16x16x32_bf16 a[112:115], v[92:95], v[164:167], a[112:115]\n" \
"v_mfma_f32_16x16x32_bf16 a[144:147], v[96:99], v[160:163], a[144:147]\n" \
"ds_read_b128 v[88:91], v48 offset:6144\n" \
"v_mfma_f32_16x16x32_bf16 a[144:147], v[100:103], v[164:167], a[144:147]\n" \
"global_load_dword v253, v248, s[64:65]\n" \
"v_mfma_f32_16x16x32_bf16 a[148:151], v[96:99], v[168:171], a[148:151]\n" \
"s_add_u32 s68, s67, 0x400000\n" \
"ds_read_b64_tr_b16 v[148:149], v50 offset:16896\n" \
"v_mfma_f32_16x16x32_bf16 a[148:151], v[100:103], v[172:175], a[148:151]\n" \
"s_add_u32 s64, s12, s68\n" \
"s_addc_u32 s65, s13, 0\n" \
"v_mfma_f32_16x16x32_bf16 a[152:155], v[96:99], v[176:179], a[152:155]\n" \
"ds_read_b128 v[92:95], v49 offset:6144\n" \
"v_mfma_f32_16x16x32_bf16 a[152:155], v[100:103], v[180:183], a[152:155]\n" \
"global_load_dword v253, v248, s[64:65]\n" \
"v_mfma_f32_16x16x32_bf16 a[156:159], v[96:99], v[184:187], a[156:159]\n" \
"ds_read_b64_tr_b16 v[150:151], v50 offset:20992\n" \
"v_mfma_f32_16x16x32_bf16 a[156:159], v[100:103], v[188:191], a[156:159]\n" \
"v_mfma_f32_16x16x32_bf16 a[188:191], v[104:107], v[184:187], a[188:191]\n" \
"ds_read_b128 v[96:99], v48 offset:8192\n" \
"v_mfma_f32_16x16x32_bf16 a[188:191], v[108:111], v[188:191], a[188:191]\n" \
"v_mfma_f32_16x16x32_bf16 a[184:187], v[104:107], v[176:179], a[184:187]\n" \
"ds_read_b64_tr_b16 v[152:153], v51 offset:512\n" \
"v_mfma_f32_16x16x32_bf16 a[184:187], v[108:111], v[180:183], a[184:187]\n" \
"v_mfma_f32_16x16x32_bf16 a[180:183], v[104:107], v[168:171], a[180:183]\n" \
"ds_read_b128 v[100:103], v49 offset:8192\n" \
"v_mfma_f32_16x16x32_bf16 a[180:183], v[108:111], v[172:175], a[180:183]\n" \
"v_mfma_f32_16x16x32_bf16 a[176:179], v[104:107], v[160:163], a[176:179]\n" \
"ds_read_b64_tr_b16 v[154:155], v51 offset:4608\n" \
"v_mfma_f32_16x16x32_bf16 a[176:179], v[108:111], v[164:167], a[176:179]\n" \
"v_mfma_f32_16x16x32_bf16 a[208:211], v[112:115], v[160:163], a[208:211]\n" \
"ds_read_b128 v[104:107], v48 offset:10240\n" \
"v_mfma_f32_16x16x32_bf16 a[208:211], v[116:119], v[164:167], a[208:211]\n" \
"v_mfma_f32_16x16x32_bf16 a[212:215], v[112:115], v[168:171], a[212:215]\n" \
"ds_read_b64_tr_b16 v[156:157], v51 offset:16896\n" \
"v_mfma_f32_16x16x32_bf16 a[212:215], v[116:119], v[172:175], a[212:215]\n" \
"v_mfma_f32_16x16x32_bf16 a[216:219], v[112:115], v[176:179], a[216:219]\n" \
"ds_read_b128 v[108:111], v49 offset:10240\n" \
"v_mfma_f32_16x16x32_bf16 a[216:219], v[116:119], v[180:183], a[216:219]\n" \
"v_mfma_f32_16x16x32_bf16 a[220:223], v[112:115], v[184:187], a[220:223]\n" \
"ds_read_b64_tr_b16 v[158:159], v51 offset:20992\n" \
"v_mfma_f32_16x16x32_bf16 a[220:223], v[116:119], v[188:191], a[220:223]\n" \
"v_mfma_f32_16x16x32_bf16 a[252:255], v[120:123], v[184:187], a[252:255]\n" \
"ds_read_b128 v[112:115], v48 offset:12288\n" \
"v_mfma_f32_16x16x32_bf16 a[252:255], v[124:127], v[188:191], a[252:255]\n" \
"v_mfma_f32_16x16x32_bf16 a[248:251], v[120:123], v[176:179], a[248:251]\n" \
"v_mfma_f32_16x16x32_bf16 a[248:251], v[124:127], v[180:183], a[248:251]\n" \
"v_mfma_f32_16x16x32_bf16 a[244:247], v[120:123], v[168:171], a[244:247]\n" \
"ds_read_b128 v[116:119], v49 offset:12288\n" \
"v_mfma_f32_16x16x32_bf16 a[244:247], v[124:127], v[172:175], a[244:247]\n" \
"v_mfma_f32_16x16x32_bf16 a[240:243], v[120:123], v[160:163], a[240:243]\n" \
"v_mfma_f32_16x16x32_bf16 a[240:243], v[124:127], v[164:167], a[240:243]\n" \
"s_waitcnt lgkmcnt(1)\n" \
"v_mfma_f32_16x16x32_bf16 a[0:3], v[128:131], v[64:67], a[0:3]\n" \
"ds_read_b128 v[120:123], v48 offset:14336\n" \
"v_mfma_f32_16x16x32_bf16 a[0:3], v[132:135], v[68:71], a[0:3]\n" \
"v_mfma_f32_16x16x32_bf16 a[4:7], v[136:139], v[64:67], a[4:7]\n" \
"ds_read_b128 v[124:127], v49 offset:14336\n" \
"s_add_u32 m0, s95, 4096\n" \
"v_mfma_f32_16x16x32_bf16 a[4:7], v[140:143], v[68:71], a[4:7]\n" \
"global_load_lds_dwordx4 v228, s[44:45] offset:-4096\n" \
"v_mfma_f32_16x16x32_bf16 a[8:11], v[144:147], v[64:67], a[8:11]\n" \
"ds_read_b64_tr_b16 v[160:161], v50 offset:1024\n" \
"v_mfma_f32_16x16x32_bf16 a[8:11], v[148:151], v[68:71], a[8:11]\n" \
"v_mfma_f32_16x16x32_bf16 a[12:15], v[152:155], v[64:67], a[12:15]\n" \
"ds_read_b64_tr_b16 v[162:163], v50 offset:5120\n" \
"v_mfma_f32_16x16x32_bf16 a[12:15], v[156:159], v[68:71], a[12:15]\n" \
"global_load_lds_dwordx4 v229, s[44:45] offset:-3072\n" \
"v_mfma_f32_16x16x32_bf16 a[44:47], v[152:155], v[72:75], a[44:47]\n" \
"s_add_u32 s44, s44, 0x180000\n" \
"s_addc_u32 s45, s45, 0\n" \
"ds_read_b64_tr_b16 v[164:165], v50 offset:17408\n" \
"v_mfma_f32_16x16x32_bf16 a[44:47], v[156:159], v[76:79], a[44:47]\n" \
"v_mfma_f32_16x16x32_bf16 a[40:43], v[144:147], v[72:75], a[40:43]\n" \
"ds_read_b64_tr_b16 v[166:167], v50 offset:21504\n" \
"v_mfma_f32_16x16x32_bf16 a[40:43], v[148:151], v[76:79], a[40:43]\n" \
"global_load_lds_dwordx4 v228, s[42:43] offset:-2048\n" \
"v_mfma_f32_16x16x32_bf16 a[36:39], v[136:139], v[72:75], a[36:39]\n" \
"ds_read_b64_tr_b16 v[168:169], v51 offset:1024\n" \
"v_mfma_f32_16x16x32_bf16 a[36:39], v[140:143], v[76:79], a[36:39]\n" \
"v_mfma_f32_16x16x32_bf16 a[32:35], v[128:131], v[72:75], a[32:35]\n" \
"ds_read_b64_tr_b16 v[170:171], v51 offset:5120\n" \
"v_mfma_f32_16x16x32_bf16 a[32:35], v[132:135], v[76:79], a[32:35]\n" \
"global_load_lds_dwordx4 v229, s[42:43] offset:-1024\n" \
"v_mfma_f32_16x16x32_bf16 a[64:67], v[128:131], v[80:83], a[64:67]\n" \
"s_add_u32 s42, s42, 0x180000\n" \
"s_addc_u32 s43, s43, 0\n" \
"ds_read_b64_tr_b16 v[172:173], v51 offset:17408\n" \
"v_mfma_f32_16x16x32_bf16 a[64:67], v[132:135], v[84:87], a[64:67]\n" \
"v_mfma_f32_16x16x32_bf16 a[68:71], v[136:139], v[80:83], a[68:71]\n" \
"s_add_u32 m0, s95, 20480\n" \
"ds_read_b64_tr_b16 v[174:175], v51 offset:21504\n" \
"v_mfma_f32_16x16x32_bf16 a[68:71], v[140:143], v[84:87], a[68:71]\n" \
"global_load_lds_dwordx4 v228, s[44:45] offset:-4096\n" \
"v_mfma_f32_16x16x32_bf16 a[72:75], v[144:147], v[80:83], a[72:75]\n" \
"ds_read_b64_tr_b16 v[176:177], v50 offset:1536\n" \
"v_mfma_f32_16x16x32_bf16 a[72:75], v[148:151], v[84:87], a[72:75]\n" \
"v_mfma_f32_16x16x32_bf16 a[76:79], v[152:155], v[80:83], a[76:79]\n" \
"ds_read_b64_tr_b16 v[178:179], v50 offset:5632\n" \
"v_mfma_f32_16x16x32_bf16 a[76:79], v[156:159], v[84:87], a[76:79]\n" \
"global_load_lds_dwordx4 v229, s[44:45] offset:-3072\n" \
"v_mfma_f32_16x16x32_bf16 a[108:111], v[152:155], v[88:91], a[108:111]\n" \
"s_add_u32 s44, s44, 0x400000\n" \
"Lpg1682_%=:\n" \
"Lst0x2_%=:\n" \
"s_cbranch_execz Lpg3849_%=\n" \
"s_addc_u32 s45, s45, 0\n" \
"ds_read_b64_tr_b16 v[180:181], v50 offset:17920\n" \
"v_mfma_f32_16x16x32_bf16 a[108:111], v[156:159], v[92:95], a[108:111]\n" \
"v_mfma_f32_16x16x32_bf16 a[104:107], v[144:147], v[88:91], a[104:107]\n" \
"ds_read_b64_tr_b16 v[182:183], v50 offset:22016\n" \
"v_mfma_f32_16x16x32_bf16 a[104:107], v[148:151], v[92:95], a[104:107]\n" \
"global_load_lds_dwordx4 v228, s[42:43] offset:-2048\n" \
"v_mfma_f32_16x16x32_bf16 a[100:103], v[136:139], v[88:91], a[100:103]\n" \
"ds_read_b64_tr_b16 v[184:185], v51 offset:1536\n" \
"v_mfma_f32_16x16x32_bf16 a[100:103], v[140:143], v[92:95], a[100:103]\n" \
"v_mfma_f32_16x16x32_bf16 a[96:99], v[128:131], v[88:91], a[96:99]\n" \
"ds_read_b64_tr_b16 v[186:187], v51 offset:5632\n" \
"v_mfma_f32_16x16x32_bf16 a[96:99], v[132:135], v[92:95], a[96:99]\n" \
"global_load_lds_dwordx4 v229, s[42:43] offset:-1024\n" \
"v_mfma_f32_16x16x32_bf16 a[128:131], v[128:131], v[96:99], a[128:131]\n" \
"s_add_u32 s42, s42, 0x400000\n" \
"s_addc_u32 s43, s43, 0\n" \
"ds_read_b64_tr_b16 v[188:189], v51 offset:17920\n" \
"v_mfma_f32_16x16x32_bf16 a[128:131], v[132:135], v[100:103], a[128:131]\n" \
"v_mfma_f32_16x16x32_bf16 a[132:135], v[136:139], v[96:99], a[132:135]\n" \
"ds_read_b64_tr_b16 v[190:191], v51 offset:22016\n" \
"v_mfma_f32_16x16x32_bf16 a[132:135], v[140:143], v[100:103], a[132:135]\n" \
"v_mfma_f32_16x16x32_bf16 a[136:139], v[144:147], v[96:99], a[136:139]\n" \
"v_mfma_f32_16x16x32_bf16 a[136:139], v[148:151], v[100:103], a[136:139]\n" \
"v_mfma_f32_16x16x32_bf16 a[140:143], v[152:155], v[96:99], a[140:143]\n" \
"v_mfma_f32_16x16x32_bf16 a[140:143], v[156:159], v[100:103], a[140:143]\n" \
"v_mfma_f32_16x16x32_bf16 a[172:175], v[152:155], v[104:107], a[172:175]\n" \
"s_mul_i32 s76, s73, 0x0\n" \
"s_add_u32 s77, s76, 0xffff8000\n" \
"s_sub_u32 s78, 0xffff8000, s76\n" \
"v_mfma_f32_16x16x32_bf16 a[172:175], v[156:159], v[108:111], a[172:175]\n" \
"v_add_u32 v48, s77, v48\n" \
"v_add_u32 v49, s77, v49\n" \
"v_mfma_f32_16x16x32_bf16 a[168:171], v[144:147], v[104:107], a[168:171]\n" \
"v_add_u32 v50, s78, v50\n" \
"v_add_u32 v51, s78, v51\n" \
"v_mfma_f32_16x16x32_bf16 a[168:171], v[148:151], v[108:111], a[168:171]\n" \
"v_mfma_f32_16x16x32_bf16 a[164:167], v[136:139], v[104:107], a[164:167]\n" \
"v_mfma_f32_16x16x32_bf16 a[164:167], v[140:143], v[108:111], a[164:167]\n" \
"v_mfma_f32_16x16x32_bf16 a[160:163], v[128:131], v[104:107], a[160:163]\n" \
"v_mfma_f32_16x16x32_bf16 a[160:163], v[132:135], v[108:111], a[160:163]\n" \
"s_waitcnt lgkmcnt(0)\n" \
"v_mfma_f32_16x16x32_bf16 a[192:195], v[128:131], v[112:115], a[192:195]\n" \
"v_mfma_f32_16x16x32_bf16 a[192:195], v[132:135], v[116:119], a[192:195]\n" \
"v_mfma_f32_16x16x32_bf16 a[196:199], v[136:139], v[112:115], a[196:199]\n" \
"v_mfma_f32_16x16x32_bf16 a[196:199], v[140:143], v[116:119], a[196:199]\n" \
"v_mfma_f32_16x16x32_bf16 a[200:203], v[144:147], v[112:115], a[200:203]\n" \
"v_mfma_f32_16x16x32_bf16 a[200:203], v[148:151], v[116:119], a[200:203]\n" \
"v_mfma_f32_16x16x32_bf16 a[204:207], v[152:155], v[112:115], a[204:207]\n" \
"v_mfma_f32_16x16x32_bf16 a[204:207], v[156:159], v[116:119], a[204:207]\n" \
"v_mfma_f32_16x16x32_bf16 a[236:239], v[152:155], v[120:123], a[236:239]\n" \
"v_mfma_f32_16x16x32_bf16 a[236:239], v[156:159], v[124:127], a[236:239]\n" \
"v_mfma_f32_16x16x32_bf16 a[232:235], v[144:147], v[120:123], a[232:235]\n" \
"v_mfma_f32_16x16x32_bf16 a[232:235], v[148:151], v[124:127], a[232:235]\n" \
"v_mfma_f32_16x16x32_bf16 a[228:231], v[136:139], v[120:123], a[228:231]\n" \
"v_mfma_f32_16x16x32_bf16 a[228:231], v[140:143], v[124:127], a[228:231]\n" \
"s_waitcnt vmcnt(10) lgkmcnt(0)\n" \
"v_mfma_f32_16x16x32_bf16 a[224:227], v[128:131], v[120:123], a[224:227]\n" \
"v_mfma_f32_16x16x32_bf16 a[224:227], v[132:135], v[124:127], a[224:227]\n" \
"s_barrier\n" \
"v_mfma_f32_16x16x32_bf16 a[16:19], v[64:67], v[160:163], a[16:19]\n" \
"s_add_u32 m0, s87, 135168\n" \
"ds_read_b64_tr_b16 v[128:129], v50 offset:0\n" \
"v_mfma_f32_16x16x32_bf16 a[16:19], v[68:71], v[164:167], a[16:19]\n" \
"global_load_lds_dwordx4 v224, s[48:49] offset:-4096\n" \
"v_mfma_f32_16x16x32_bf16 a[20:23], v[64:67], v[168:171], a[20:23]\n" \
"ds_read_b64_tr_b16 v[130:131], v50 offset:4096\n" \
"v_mfma_f32_16x16x32_bf16 a[20:23], v[68:71], v[172:175], a[20:23]\n" \
"v_mfma_f32_16x16x32_bf16 a[24:27], v[64:67], v[176:179], a[24:27]\n" \
"ds_read_b64_tr_b16 v[132:133], v50 offset:16384\n" \
"v_mfma_f32_16x16x32_bf16 a[24:27], v[68:71], v[180:183], a[24:27]\n" \
"global_load_lds_dwordx4 v225, s[48:49] offset:-3072\n" \
"v_mfma_f32_16x16x32_bf16 a[28:31], v[64:67], v[184:187], a[28:31]\n" \
"ds_read_b64_tr_b16 v[134:135], v50 offset:20480\n" \
"v_mfma_f32_16x16x32_bf16 a[28:31], v[68:71], v[188:191], a[28:31]\n" \
"v_mfma_f32_16x16x32_bf16 a[60:63], v[72:75], v[184:187], a[60:63]\n" \
"ds_read_b128 v[64:67], v48 offset:0\n" \
"v_mfma_f32_16x16x32_bf16 a[60:63], v[76:79], v[188:191], a[60:63]\n" \
"global_load_lds_dwordx4 v226, s[48:49] offset:-2048\n" \
"v_mfma_f32_16x16x32_bf16 a[56:59], v[72:75], v[176:179], a[56:59]\n" \
"ds_read_b64_tr_b16 v[136:137], v51 offset:0\n" \
"v_mfma_f32_16x16x32_bf16 a[56:59], v[76:79], v[180:183], a[56:59]\n" \
"v_mfma_f32_16x16x32_bf16 a[52:55], v[72:75], v[168:171], a[52:55]\n" \
"ds_read_b128 v[68:71], v49 offset:0\n" \
"v_mfma_f32_16x16x32_bf16 a[52:55], v[76:79], v[172:175], a[52:55]\n" \
"global_load_lds_dwordx4 v227, s[48:49] offset:-1024\n" \
"v_mfma_f32_16x16x32_bf16 a[48:51], v[72:75], v[160:163], a[48:51]\n" \
"s_add_u32 s48, s48, 0x180\n" \
"s_addc_u32 s49, s49, 0\n" \
"ds_read_b64_tr_b16 v[138:139], v51 offset:4096\n" \
"v_mfma_f32_16x16x32_bf16 a[48:51], v[76:79], v[164:167], a[48:51]\n" \
"v_mfma_f32_16x16x32_bf16 a[80:83], v[80:83], v[160:163], a[80:83]\n" \
"ds_read_b128 v[72:75], v48 offset:2048\n" \
"v_mfma_f32_16x16x32_bf16 a[80:83], v[84:87], v[164:167], a[80:83]\n" \
"global_load_lds_dwordx4 v224, s[46:47] offset:0\n" \
"v_mfma_f32_16x16x32_bf16 a[84:87], v[80:83], v[168:171], a[84:87]\n" \
"ds_read_b64_tr_b16 v[140:141], v51 offset:16384\n" \
"v_mfma_f32_16x16x32_bf16 a[84:87], v[84:87], v[172:175], a[84:87]\n" \
"v_mfma_f32_16x16x32_bf16 a[88:91], v[80:83], v[176:179], a[88:91]\n" \
"ds_read_b128 v[76:79], v49 offset:2048\n" \
"v_mfma_f32_16x16x32_bf16 a[88:91], v[84:87], v[180:183], a[88:91]\n" \
"global_load_lds_dwordx4 v225, s[46:47] offset:1024\n" \
"v_mfma_f32_16x16x32_bf16 a[92:95], v[80:83], v[184:187], a[92:95]\n" \
"ds_read_b64_tr_b16 v[142:143], v51 offset:20480\n" \
"v_mfma_f32_16x16x32_bf16 a[92:95], v[84:87], v[188:191], a[92:95]\n" \
"v_mfma_f32_16x16x32_bf16 a[124:127], v[88:91], v[184:187], a[124:127]\n" \
"ds_read_b128 v[80:83], v48 offset:4096\n" \
"v_mfma_f32_16x16x32_bf16 a[124:127], v[92:95], v[188:191], a[124:127]\n" \
"global_load_lds_dwordx4 v226, s[46:47] offset:2048\n" \
"v_mfma_f32_16x16x32_bf16 a[120:123], v[88:91], v[176:179], a[120:123]\n" \
"ds_read_b64_tr_b16 v[144:145], v50 offset:512\n" \
"v_mfma_f32_16x16x32_bf16 a[120:123], v[92:95], v[180:183], a[120:123]\n" \
"v_mfma_f32_16x16x32_bf16 a[116:119], v[88:91], v[168:171], a[116:119]\n" \
"ds_read_b128 v[84:87], v49 offset:4096\n" \
"v_mfma_f32_16x16x32_bf16 a[116:119], v[92:95], v[172:175], a[116:119]\n" \
"global_load_lds_dwordx4 v227, s[46:47] offset:3072\n" \
"v_mfma_f32_16x16x32_bf16 a[112:115], v[88:91], v[160:163], a[112:115]\n" \
"s_add_u32 s46, s46, 0x180\n" \
"s_addc_u32 s47, s47, 0\n" \
"ds_read_b64_tr_b16 v[146:147], v50 offset:4608\n" \
"v_mfma_f32_16x16x32_bf16 a[112:115], v[92:95], v[164:167], a[112:115]\n" \
"s_add_u32 s68, s67, 0x600000\n" \
"s_add_u32 s64, s12, s68\n" \
"s_addc_u32 s65, s13, 0\n" \
"v_mfma_f32_16x16x32_bf16 a[144:147], v[96:99], v[160:163], a[144:147]\n" \
"ds_read_b128 v[88:91], v48 offset:6144\n" \
"v_mfma_f32_16x16x32_bf16 a[144:147], v[100:103], v[164:167], a[144:147]\n" \
"global_load_dword v253, v248, s[64:65]\n" \
"v_mfma_f32_16x16x32_bf16 a[148:151], v[96:99], v[168:171], a[148:151]\n" \
"s_add_u32 s68, s67, 0x700000\n" \
"ds_read_b64_tr_b16 v[148:149], v50 offset:16896\n" \
"v_mfma_f32_16x16x32_bf16 a[148:151], v[100:103], v[172:175], a[148:151]\n" \
"s_add_u32 s64, s12, s68\n" \
"s_addc_u32 s65, s13, 0\n" \
"v_mfma_f32_16x16x32_bf16 a[152:155], v[96:99], v[176:179], a[152:155]\n" \
"ds_read_b128 v[92:95], v49 offset:6144\n" \
"v_mfma_f32_16x16x32_bf16 a[152:155], v[100:103], v[180:183], a[152:155]\n" \
"global_load_dword v253, v248, s[64:65]\n" \
"v_mfma_f32_16x16x32_bf16 a[156:159], v[96:99], v[184:187], a[156:159]\n" \
"s_lshr_b32 s68, s66, 2\n" \
"s_and_b32 s68, s68, 0xffff\n" \
"ds_read_b64_tr_b16 v[150:151], v50 offset:20992\n" \
"v_mfma_f32_16x16x32_bf16 a[156:159], v[100:103], v[188:191], a[156:159]\n" \
"s_add_u32 s68, s68, 65585152\n" \
"v_mfma_f32_16x16x32_bf16 a[188:191], v[104:107], v[184:187], a[188:191]\n" \
"ds_read_b128 v[96:99], v48 offset:8192\n" \
"v_mfma_f32_16x16x32_bf16 a[188:191], v[108:111], v[188:191], a[188:191]\n" \
"buffer_load_dword v253, v245, s[56:59], s68 offen sc1\n" \
"v_mfma_f32_16x16x32_bf16 a[184:187], v[104:107], v[176:179], a[184:187]\n" \
"ds_read_b64_tr_b16 v[152:153], v51 offset:512\n" \
"v_mfma_f32_16x16x32_bf16 a[184:187], v[108:111], v[180:183], a[184:187]\n" \
"v_mfma_f32_16x16x32_bf16 a[180:183], v[104:107], v[168:171], a[180:183]\n" \
"ds_read_b128 v[100:103], v49 offset:8192\n" \
"v_mfma_f32_16x16x32_bf16 a[180:183], v[108:111], v[172:175], a[180:183]\n" \
"v_mfma_f32_16x16x32_bf16 a[176:179], v[104:107], v[160:163], a[176:179]\n" \
"ds_read_b64_tr_b16 v[154:155], v51 offset:4608\n" \
"v_mfma_f32_16x16x32_bf16 a[176:179], v[108:111], v[164:167], a[176:179]\n" \
"v_mfma_f32_16x16x32_bf16 a[208:211], v[112:115], v[160:163], a[208:211]\n" \
"ds_read_b128 v[104:107], v48 offset:10240\n" \
"v_mfma_f32_16x16x32_bf16 a[208:211], v[116:119], v[164:167], a[208:211]\n" \
"v_mfma_f32_16x16x32_bf16 a[212:215], v[112:115], v[168:171], a[212:215]\n" \
"ds_read_b64_tr_b16 v[156:157], v51 offset:16896\n" \
"v_mfma_f32_16x16x32_bf16 a[212:215], v[116:119], v[172:175], a[212:215]\n" \
"v_mfma_f32_16x16x32_bf16 a[216:219], v[112:115], v[176:179], a[216:219]\n" \
"ds_read_b128 v[108:111], v49 offset:10240\n" \
"v_mfma_f32_16x16x32_bf16 a[216:219], v[116:119], v[180:183], a[216:219]\n" \
"v_mfma_f32_16x16x32_bf16 a[220:223], v[112:115], v[184:187], a[220:223]\n" \
"ds_read_b64_tr_b16 v[158:159], v51 offset:20992\n" \
"v_mfma_f32_16x16x32_bf16 a[220:223], v[116:119], v[188:191], a[220:223]\n" \
"v_mfma_f32_16x16x32_bf16 a[252:255], v[120:123], v[184:187], a[252:255]\n" \
"ds_read_b128 v[112:115], v48 offset:12288\n" \
"v_mfma_f32_16x16x32_bf16 a[252:255], v[124:127], v[188:191], a[252:255]\n" \
"v_mfma_f32_16x16x32_bf16 a[248:251], v[120:123], v[176:179], a[248:251]\n" \
"v_mfma_f32_16x16x32_bf16 a[248:251], v[124:127], v[180:183], a[248:251]\n" \
"v_mfma_f32_16x16x32_bf16 a[244:247], v[120:123], v[168:171], a[244:247]\n" \
"ds_read_b128 v[116:119], v49 offset:12288\n" \
"v_mfma_f32_16x16x32_bf16 a[244:247], v[124:127], v[172:175], a[244:247]\n" \
"v_mfma_f32_16x16x32_bf16 a[240:243], v[120:123], v[160:163], a[240:243]\n" \
"v_mfma_f32_16x16x32_bf16 a[240:243], v[124:127], v[164:167], a[240:243]\n" \
"s_waitcnt lgkmcnt(1)\n" \
"v_mfma_f32_16x16x32_bf16 a[0:3], v[128:131], v[64:67], a[0:3]\n" \
"ds_read_b128 v[120:123], v48 offset:14336\n" \
"v_mfma_f32_16x16x32_bf16 a[0:3], v[132:135], v[68:71], a[0:3]\n" \
"v_mfma_f32_16x16x32_bf16 a[4:7], v[136:139], v[64:67], a[4:7]\n" \
"ds_read_b128 v[124:127], v49 offset:14336\n" \
"s_add_u32 m0, s87, 69632\n" \
"v_mfma_f32_16x16x32_bf16 a[4:7], v[140:143], v[68:71], a[4:7]\n" \
"global_load_lds_dwordx4 v224, s[48:49] offset:-4096\n" \
"v_mfma_f32_16x16x32_bf16 a[8:11], v[144:147], v[64:67], a[8:11]\n" \
"ds_read_b64_tr_b16 v[160:161], v50 offset:1024\n" \
"v_mfma_f32_16x16x32_bf16 a[8:11], v[148:151], v[68:71], a[8:11]\n" \
"v_mfma_f32_16x16x32_bf16 a[12:15], v[152:155], v[64:67], a[12:15]\n" \
"ds_read_b64_tr_b16 v[162:163], v50 offset:5120\n" \
"v_mfma_f32_16x16x32_bf16 a[12:15], v[156:159], v[68:71], a[12:15]\n" \
"global_load_lds_dwordx4 v225, s[48:49] offset:-3072\n" \
"v_mfma_f32_16x16x32_bf16 a[44:47], v[152:155], v[72:75], a[44:47]\n" \
"ds_read_b64_tr_b16 v[164:165], v50 offset:17408\n" \
"v_mfma_f32_16x16x32_bf16 a[44:47], v[156:159], v[76:79], a[44:47]\n" \
"v_mfma_f32_16x16x32_bf16 a[40:43], v[144:147], v[72:75], a[40:43]\n" \
"ds_read_b64_tr_b16 v[166:167], v50 offset:21504\n" \
"v_mfma_f32_16x16x32_bf16 a[40:43], v[148:151], v[76:79], a[40:43]\n" \
"global_load_lds_dwordx4 v226, s[48:49] offset:-2048\n" \
"v_mfma_f32_16x16x32_bf16 a[36:39], v[136:139], v[72:75], a[36:39]\n" \
"ds_read_b64_tr_b16 v[168:169], v51 offset:1024\n" \
"v_mfma_f32_16x16x32_bf16 a[36:39], v[140:143], v[76:79], a[36:39]\n" \
"v_mfma_f32_16x16x32_bf16 a[32:35], v[128:131], v[72:75], a[32:35]\n" \
"ds_read_b64_tr_b16 v[170:171], v51 offset:5120\n" \
"v_mfma_f32_16x16x32_bf16 a[32:35], v[132:135], v[76:79], a[32:35]\n" \
"global_load_lds_dwordx4 v227, s[48:49] offset:-1024\n" \
"v_mfma_f32_16x16x32_bf16 a[64:67], v[128:131], v[80:83], a[64:67]\n" \
"s_add_u32 s48, s48, 0x400\n" \
"s_addc_u32 s49, s49, 0\n" \
"ds_read_b64_tr_b16 v[172:173], v51 offset:17408\n" \
"v_mfma_f32_16x16x32_bf16 a[64:67], v[132:135], v[84:87], a[64:67]\n" \
"v_mfma_f32_16x16x32_bf16 a[68:71], v[136:139], v[80:83], a[68:71]\n" \
"ds_read_b64_tr_b16 v[174:175], v51 offset:21504\n" \
"v_mfma_f32_16x16x32_bf16 a[68:71], v[140:143], v[84:87], a[68:71]\n" \
"global_load_lds_dwordx4 v224, s[46:47] offset:0\n" \
"v_mfma_f32_16x16x32_bf16 a[72:75], v[144:147], v[80:83], a[72:75]\n" \
"ds_read_b64_tr_b16 v[176:177], v50 offset:1536\n" \
"v_mfma_f32_16x16x32_bf16 a[72:75], v[148:151], v[84:87], a[72:75]\n" \
"v_mfma_f32_16x16x32_bf16 a[76:79], v[152:155], v[80:83], a[76:79]\n" \
"ds_read_b64_tr_b16 v[178:179], v50 offset:5632\n" \
"v_mfma_f32_16x16x32_bf16 a[76:79], v[156:159], v[84:87], a[76:79]\n" \
"global_load_lds_dwordx4 v225, s[46:47] offset:1024\n" \
"v_mfma_f32_16x16x32_bf16 a[108:111], v[152:155], v[88:91], a[108:111]\n" \
"ds_read_b64_tr_b16 v[180:181], v50 offset:17920\n" \
"v_mfma_f32_16x16x32_bf16 a[108:111], v[156:159], v[92:95], a[108:111]\n" \
"v_mfma_f32_16x16x32_bf16 a[104:107], v[144:147], v[88:91], a[104:107]\n" \
"ds_read_b64_tr_b16 v[182:183], v50 offset:22016\n" \
"v_mfma_f32_16x16x32_bf16 a[104:107], v[148:151], v[92:95], a[104:107]\n" \
"global_load_lds_dwordx4 v226, s[46:47] offset:2048\n" \
"v_mfma_f32_16x16x32_bf16 a[100:103], v[136:139], v[88:91], a[100:103]\n" \
"ds_read_b64_tr_b16 v[184:185], v51 offset:1536\n" \
"v_mfma_f32_16x16x32_bf16 a[100:103], v[140:143], v[92:95], a[100:103]\n" \
"v_mfma_f32_16x16x32_bf16 a[96:99], v[128:131], v[88:91], a[96:99]\n" \
"ds_read_b64_tr_b16 v[186:187], v51 offset:5632\n" \
"v_mfma_f32_16x16x32_bf16 a[96:99], v[132:135], v[92:95], a[96:99]\n" \
"global_load_lds_dwordx4 v227, s[46:47] offset:3072\n" \
"v_mfma_f32_16x16x32_bf16 a[128:131], v[128:131], v[96:99], a[128:131]\n" \
"s_add_u32 s46, s46, 0x400\n" \
"s_addc_u32 s47, s47, 0\n" \
"ds_read_b64_tr_b16 v[188:189], v51 offset:17920\n" \
"v_mfma_f32_16x16x32_bf16 a[128:131], v[132:135], v[100:103], a[128:131]\n" \
"v_mfma_f32_16x16x32_bf16 a[132:135], v[136:139], v[96:99], a[132:135]\n" \
"ds_read_b64_tr_b16 v[190:191], v51 offset:22016\n" \
"v_mfma_f32_16x16x32_bf16 a[132:135], v[140:143], v[100:103], a[132:135]\n" \
"v_mfma_f32_16x16x32_bf16 a[136:139], v[144:147], v[96:99], a[136:139]\n" \
"v_mfma_f32_16x16x32_bf16 a[136:139], v[148:151], v[100:103], a[136:139]\n" \
"v_mfma_f32_16x16x32_bf16 a[140:143], v[152:155], v[96:99], a[140:143]\n" \
"v_mfma_f32_16x16x32_bf16 a[140:143], v[156:159], v[100:103], a[140:143]\n" \
"v_mfma_f32_16x16x32_bf16 a[172:175], v[152:155], v[104:107], a[172:175]\n" \
"v_mfma_f32_16x16x32_bf16 a[172:175], v[156:159], v[108:111], a[172:175]\n" \
"v_mfma_f32_16x16x32_bf16 a[168:171], v[144:147], v[104:107], a[168:171]\n" \
"v_mfma_f32_16x16x32_bf16 a[168:171], v[148:151], v[108:111], a[168:171]\n" \
"v_mfma_f32_16x16x32_bf16 a[164:167], v[136:139], v[104:107], a[164:167]\n" \
"v_mfma_f32_16x16x32_bf16 a[164:167], v[140:143], v[108:111], a[164:167]\n" \
"v_mfma_f32_16x16x32_bf16 a[160:163], v[128:131], v[104:107], a[160:163]\n" \
"v_mfma_f32_16x16x32_bf16 a[160:163], v[132:135], v[108:111], a[160:163]\n" \
"s_waitcnt lgkmcnt(0)\n" \
"v_mfma_f32_16x16x32_bf16 a[192:195], v[128:131], v[112:115], a[192:195]\n" \
"v_mfma_f32_16x16x32_bf16 a[192:195], v[132:135], v[116:119], a[192:195]\n" \
"v_mfma_f32_16x16x32_bf16 a[196:199], v[136:139], v[112:115], a[196:199]\n" \
"v_mfma_f32_16x16x32_bf16 a[196:199], v[140:143], v[116:119], a[196:199]\n" \
"v_mfma_f32_16x16x32_bf16 a[200:203], v[144:147], v[112:115], a[200:203]\n" \
"v_mfma_f32_16x16x32_bf16 a[200:203], v[148:151], v[116:119], a[200:203]\n" \
"v_mfma_f32_16x16x32_bf16 a[204:207], v[152:155], v[112:115], a[204:207]\n" \
"v_mfma_f32_16x16x32_bf16 a[204:207], v[156:159], v[116:119], a[204:207]\n" \
"v_mfma_f32_16x16x32_bf16 a[236:239], v[152:155], v[120:123], a[236:239]\n" \
"v_mfma_f32_16x16x32_bf16 a[236:239], v[156:159], v[124:127], a[236:239]\n" \
"v_mfma_f32_16x16x32_bf16 a[232:235], v[144:147], v[120:123], a[232:235]\n" \
"v_mfma_f32_16x16x32_bf16 a[232:235], v[148:151], v[124:127], a[232:235]\n" \
"v_mfma_f32_16x16x32_bf16 a[228:231], v[136:139], v[120:123], a[228:231]\n" \
"v_mfma_f32_16x16x32_bf16 a[228:231], v[140:143], v[124:127], a[228:231]\n" \
"s_waitcnt vmcnt(11) lgkmcnt(0)\n" \
"v_mfma_f32_16x16x32_bf16 a[224:227], v[128:131], v[120:123], a[224:227]\n" \
"v_mfma_f32_16x16x32_bf16 a[224:227], v[132:135], v[124:127], a[224:227]\n" \
"s_barrier\n" \
"v_mfma_f32_16x16x32_bf16 a[16:19], v[64:67], v[160:163], a[16:19]\n" \
"s_add_u32 m0, s87, 102400\n" \
"v_mfma_f32_16x16x32_bf16 a[16:19], v[68:71], v[164:167], a[16:19]\n" \
"global_load_lds_dwordx4 v224, s[48:49] offset:-4096\n" \
"v_mfma_f32_16x16x32_bf16 a[20:23], v[64:67], v[168:171], a[20:23]\n" \
"ds_read_b64_tr_b16 v[0:1], v232 offset:0\n" \
"v_mfma_f32_16x16x32_bf16 a[20:23], v[68:71], v[172:175], a[20:23]\n" \
"v_accvgpr_read_b32 v234, a192\n" \
"v_accvgpr_read_b32 v235, a193\n" \
"v_mfma_f32_16x16x32_bf16 a[24:27], v[64:67], v[176:179], a[24:27]\n" \
"ds_read_b64_tr_b16 v[2:3], v232 offset:2048\n" \
"v_accvgpr_read_b32 v236, a194\n" \
"v_mfma_f32_16x16x32_bf16 a[24:27], v[68:71], v[180:183], a[24:27]\n" \
"global_load_lds_dwordx4 v225, s[48:49] offset:-3072\n" \
"v_accvgpr_read_b32 v237, a195\n" \
"v_mfma_f32_16x16x32_bf16 a[28:31], v[64:67], v[184:187], a[28:31]\n" \
"ds_read_b64_tr_b16 v[4:5], v232 offset:8192\n" \
"v_mul_f32 v234, s92, v234\n" \
"v_mfma_f32_16x16x32_bf16 a[28:31], v[68:71], v[188:191], a[28:31]\n" \
"v_mul_f32 v235, s92, v235\n" \
"v_mul_f32 v236, s92, v236\n" \
"v_mfma_f32_16x16x32_bf16 a[60:63], v[72:75], v[184:187], a[60:63]\n" \
"ds_read_b64_tr_b16 v[6:7], v232 offset:10240\n" \
"v_mul_f32 v237, s92, v237\n" \
"v_mfma_f32_16x16x32_bf16 a[60:63], v[76:79], v[188:191], a[60:63]\n" \
"global_load_lds_dwordx4 v226, s[48:49] offset:-2048\n" \
"v_cvt_pk_bf16_f32 v234, v234, v235\n" \
"v_mfma_f32_16x16x32_bf16 a[56:59], v[72:75], v[176:179], a[56:59]\n" \
"ds_read_b64_tr_b16 v[8:9], v233 offset:0\n" \
"v_cvt_pk_bf16_f32 v236, v236, v237\n" \
"v_mfma_f32_16x16x32_bf16 a[56:59], v[76:79], v[180:183], a[56:59]\n" \
"v_accvgpr_write_b32 a192, v234\n" \
"v_accvgpr_write_b32 a193, v236\n" \
"v_mfma_f32_16x16x32_bf16 a[52:55], v[72:75], v[168:171], a[52:55]\n" \
"ds_read_b64_tr_b16 v[10:11], v233 offset:2048\n" \
"v_accvgpr_read_b32 v234, a196\n" \
"v_mfma_f32_16x16x32_bf16 a[52:55], v[76:79], v[172:175], a[52:55]\n" \
"global_load_lds_dwordx4 v227, s[48:49] offset:-1024\n" \
"v_accvgpr_read_b32 v235, a197\n" \
"v_mfma_f32_16x16x32_bf16 a[48:51], v[72:75], v[160:163], a[48:51]\n" \
"s_add_u32 s48, s48, 0xfffffb00\n" \
"s_addc_u32 s49, s49, -1\n" \
"ds_read_b64_tr_b16 v[12:13], v233 offset:8192\n" \
"v_mfma_f32_16x16x32_bf16 a[48:51], v[76:79], v[164:167], a[48:51]\n" \
"v_accvgpr_read_b32 v236, a198\n" \
"v_accvgpr_read_b32 v237, a199\n" \
"v_mfma_f32_16x16x32_bf16 a[80:83], v[80:83], v[160:163], a[80:83]\n" \
"ds_read_b64_tr_b16 v[14:15], v233 offset:10240\n" \
"v_mul_f32 v234, s92, v234\n" \
"v_mfma_f32_16x16x32_bf16 a[80:83], v[84:87], v[164:167], a[80:83]\n" \
"global_load_lds_dwordx4 v224, s[46:47] offset:0\n" \
"v_mul_f32 v235, s92, v235\n" \
"v_mfma_f32_16x16x32_bf16 a[84:87], v[80:83], v[168:171], a[84:87]\n" \
"ds_read_b64_tr_b16 v[16:17], v232 offset:512\n" \
"v_mul_f32 v236, s92, v236\n" \
"v_mfma_f32_16x16x32_bf16 a[84:87], v[84:87], v[172:175], a[84:87]\n" \
"v_mul_f32 v237, s92, v237\n" \
"v_cvt_pk_bf16_f32 v234, v234, v235\n" \
"v_mfma_f32_16x16x32_bf16 a[88:91], v[80:83], v[176:179], a[88:91]\n" \
"ds_read_b64_tr_b16 v[18:19], v232 offset:2560\n" \
"v_cvt_pk_bf16_f32 v236, v236, v237\n" \
"v_mfma_f32_16x16x32_bf16 a[88:91], v[84:87], v[180:183], a[88:91]\n" \
"global_load_lds_dwordx4 v225, s[46:47] offset:1024\n" \
"v_accvgpr_write_b32 a194, v234\n" \
"v_mfma_f32_16x16x32_bf16 a[92:95], v[80:83], v[184:187], a[92:95]\n" \
"ds_read_b64_tr_b16 v[20:21], v232 offset:8704\n" \
"v_accvgpr_write_b32 a195, v236\n" \
"v_mfma_f32_16x16x32_bf16 a[92:95], v[84:87], v[188:191], a[92:95]\n" \
"v_accvgpr_read_b32 v234, a224\n" \
"v_accvgpr_read_b32 v235, a225\n" \
"v_mfma_f32_16x16x32_bf16 a[124:127], v[88:91], v[184:187], a[124:127]\n" \
"ds_read_b64_tr_b16 v[22:23], v232 offset:10752\n" \
"v_accvgpr_read_b32 v236, a226\n" \
"v_mfma_f32_16x16x32_bf16 a[124:127], v[92:95], v[188:191], a[124:127]\n" \
"global_load_lds_dwordx4 v226, s[46:47] offset:2048\n" \
"v_accvgpr_read_b32 v237, a227\n" \
"v_mfma_f32_16x16x32_bf16 a[120:123], v[88:91], v[176:179], a[120:123]\n" \
"ds_read_b64_tr_b16 v[24:25], v233 offset:512\n" \
"v_mul_f32 v234, s92, v234\n" \
"v_mfma_f32_16x16x32_bf16 a[120:123], v[92:95], v[180:183], a[120:123]\n" \
"v_mul_f32 v235, s92, v235\n" \
"v_mul_f32 v236, s92, v236\n" \
"v_mfma_f32_16x16x32_bf16 a[116:119], v[88:91], v[168:171], a[116:119]\n" \
"ds_read_b64_tr_b16 v[26:27], v233 offset:2560\n" \
"v_mul_f32 v237, s92, v237\n" \
"v_mfma_f32_16x16x32_bf16 a[116:119], v[92:95], v[172:175], a[116:119]\n" \
"global_load_lds_dwordx4 v227, s[46:47] offset:3072\n" \
"v_cvt_pk_bf16_f32 v234, v234, v235\n" \
"v_mfma_f32_16x16x32_bf16 a[112:115], v[88:91], v[160:163], a[112:115]\n" \
"s_add_u32 s46, s46, 0xfffffb00\n" \
"s_addc_u32 s47, s47, -1\n" \
"ds_read_b64_tr_b16 v[28:29], v233 offset:8704\n" \
"v_mfma_f32_16x16x32_bf16 a[112:115], v[92:95], v[164:167], a[112:115]\n" \
"v_cvt_pk_bf16_f32 v236, v236, v237\n" \
"v_accvgpr_write_b32 a224, v234\n" \
"v_mfma_f32_16x16x32_bf16 a[144:147], v[96:99], v[160:163], a[144:147]\n" \
"s_add_u32 m0, s95, 36864\n" \
"ds_read_b64_tr_b16 v[30:31], v233 offset:10752\n" \
"v_accvgpr_write_b32 a225, v236\n" \
"v_mfma_f32_16x16x32_bf16 a[144:147], v[100:103], v[164:167], a[144:147]\n" \
"global_load_lds_dwordx4 v228, s[44:45] offset:-4096\n" \
"v_accvgpr_read_b32 v234, a228\n" \
"v_mfma_f32_16x16x32_bf16 a[148:151], v[96:99], v[168:171], a[148:151]\n" \
"ds_read_b128 v[192:195], v230 offset:0\n" \
"v_accvgpr_read_b32 v235, a229\n" \
"v_mfma_f32_16x16x32_bf16 a[148:151], v[100:103], v[172:175], a[148:151]\n" \
"v_accvgpr_read_b32 v236, a230\n" \
"v_accvgpr_read_b32 v237, a231\n" \
"v_mfma_f32_16x16x32_bf16 a[152:155], v[96:99], v[176:179], a[152:155]\n" \
"ds_read_b128 v[196:199], v231 offset:0\n" \
"v_mul_f32 v234, s92, v234\n" \
"v_mfma_f32_16x16x32_bf16 a[152:155], v[100:103], v[180:183], a[152:155]\n" \
"global_load_lds_dwordx4 v229, s[44:45] offset:-3072\n" \
"v_mul_f32 v235, s92, v235\n" \
"v_mfma_f32_16x16x32_bf16 a[156:159], v[96:99], v[184:187], a[156:159]\n" \
"s_add_u32 s44, s44, 0xffb00000\n" \
"s_addc_u32 s45, s45, -1\n" \
"ds_read_b128 v[200:203], v230 offset:2048\n" \
"v_mfma_f32_16x16x32_bf16 a[156:159], v[100:103], v[188:191], a[156:159]\n" \
"v_mul_f32 v236, s92, v236\n" \
"v_mul_f32 v237, s92, v237\n" \
"v_mfma_f32_16x16x32_bf16 a[188:191], v[104:107], v[184:187], a[188:191]\n" \
"ds_read_b128 v[204:207], v231 offset:2048\n" \
"v_cvt_pk_bf16_f32 v234, v234, v235\n" \
"v_mfma_f32_16x16x32_bf16 a[188:191], v[108:111], v[188:191], a[188:191]\n" \
"global_load_lds_dwordx4 v228, s[42:43] offset:-2048\n" \
"v_cvt_pk_bf16_f32 v236, v236, v237\n" \
"v_mfma_f32_16x16x32_bf16 a[184:187], v[104:107], v[176:179], a[184:187]\n" \
"ds_read_b128 v[208:211], v230 offset:4096\n" \
"v_accvgpr_write_b32 a226, v234\n" \
"v_mfma_f32_16x16x32_bf16 a[184:187], v[108:111], v[180:183], a[184:187]\n" \
"v_accvgpr_write_b32 a227, v236\n" \
"v_accvgpr_read_b32 v234, a200\n" \
"v_mfma_f32_16x16x32_bf16 a[180:183], v[104:107], v[168:171], a[180:183]\n" \
"ds_read_b128 v[212:215], v231 offset:4096\n" \
"v_accvgpr_read_b32 v235, a201\n" \
"v_mfma_f32_16x16x32_bf16 a[180:183], v[108:111], v[172:175], a[180:183]\n" \
"global_load_lds_dwordx4 v229, s[42:43] offset:-1024\n" \
"v_accvgpr_read_b32 v236, a202\n" \
"v_mfma_f32_16x16x32_bf16 a[176:179], v[104:107], v[160:163], a[176:179]\n" \
"s_add_u32 s42, s42, 0xffb00000\n" \
"s_addc_u32 s43, s43, -1\n" \
"v_accvgpr_read_b32 v237, a203\n" \
"v_mfma_f32_16x16x32_bf16 a[176:179], v[108:111], v[164:167], a[176:179]\n" \
"v_mul_f32 v234, s92, v234\n" \
"v_mul_f32 v235, s92, v235\n" \
"v_mfma_f32_16x16x32_bf16 a[208:211], v[112:115], v[160:163], a[208:211]\n" \
"s_add_u32 m0, s95, 53248\n" \
"v_mul_f32 v236, s92, v236\n" \
"v_mul_f32 v237, s92, v237\n" \
"v_mfma_f32_16x16x32_bf16 a[208:211], v[116:119], v[164:167], a[208:211]\n" \
"global_load_lds_dwordx4 v228, s[44:45] offset:-4096\n" \
"v_cvt_pk_bf16_f32 v234, v234, v235\n" \
"v_mfma_f32_16x16x32_bf16 a[212:215], v[112:115], v[168:171], a[212:215]\n" \
"v_cvt_pk_bf16_f32 v236, v236, v237\n" \
"v_accvgpr_write_b32 a200, v234\n" \
"v_mfma_f32_16x16x32_bf16 a[212:215], v[116:119], v[172:175], a[212:215]\n" \
"v_accvgpr_write_b32 a201, v236\n" \
"v_accvgpr_read_b32 v234, a204\n" \
"v_mfma_f32_16x16x32_bf16 a[216:219], v[112:115], v[176:179], a[216:219]\n" \
"v_accvgpr_read_b32 v235, a205\n" \
"v_accvgpr_read_b32 v236, a206\n" \
"v_mfma_f32_16x16x32_bf16 a[216:219], v[116:119], v[180:183], a[216:219]\n" \
"global_load_lds_dwordx4 v229, s[44:45] offset:-3072\n" \
"v_accvgpr_read_b32 v237, a207\n" \
"v_mfma_f32_16x16x32_bf16 a[220:223], v[112:115], v[184:187], a[220:223]\n" \
"s_add_u32 s44, s44, 0x80000\n" \
"s_addc_u32 s45, s45, 0\n" \
"v_mul_f32 v234, s92, v234\n" \
"v_mfma_f32_16x16x32_bf16 a[220:223], v[116:119], v[188:191], a[220:223]\n" \
"v_mul_f32 v235, s92, v235\n" \
"v_mul_f32 v236, s92, v236\n" \
"v_mfma_f32_16x16x32_bf16 a[252:255], v[120:123], v[184:187], a[252:255]\n" \
"v_mul_f32 v237, s92, v237\n" \
"v_cvt_pk_bf16_f32 v234, v234, v235\n" \
"v_mfma_f32_16x16x32_bf16 a[252:255], v[124:127], v[188:191], a[252:255]\n" \
"global_load_lds_dwordx4 v228, s[42:43] offset:-2048\n" \
"v_cvt_pk_bf16_f32 v236, v236, v237\n" \
"v_mfma_f32_16x16x32_bf16 a[248:251], v[120:123], v[176:179], a[248:251]\n" \
"v_accvgpr_write_b32 a202, v234\n" \
"v_accvgpr_write_b32 a203, v236\n" \
"v_mfma_f32_16x16x32_bf16 a[248:251], v[124:127], v[180:183], a[248:251]\n" \
"v_accvgpr_read_b32 v234, a232\n" \
"v_accvgpr_read_b32 v235, a233\n" \
"v_mfma_f32_16x16x32_bf16 a[244:247], v[120:123], v[168:171], a[244:247]\n" \
"v_accvgpr_read_b32 v236, a234\n" \
"v_accvgpr_read_b32 v237, a235\n" \
"v_mfma_f32_16x16x32_bf16 a[244:247], v[124:127], v[172:175], a[244:247]\n" \
"global_load_lds_dwordx4 v229, s[42:43] offset:-1024\n" \
"v_mul_f32 v234, s92, v234\n" \
"v_mfma_f32_16x16x32_bf16 a[240:243], v[120:123], v[160:163], a[240:243]\n" \
"s_add_u32 s42, s42, 0x80000\n" \
"s_addc_u32 s43, s43, 0\n" \
"v_mul_f32 v235, s92, v235\n" \
"v_mfma_f32_16x16x32_bf16 a[240:243], v[124:127], v[164:167], a[240:243]\n" \
"s_waitcnt lgkmcnt(0)\n" \
"v_mul_f32 v236, s92, v236\n" \
"v_mul_f32 v237, s92, v237\n" \
"v_mfma_f32_16x16x32_bf16 v[64:67], v[0:3], v[192:195], 0\n" \
"ds_read_b128 v[216:219], v230 offset:6144\n" \
"v_cvt_pk_bf16_f32 v234, v234, v235\n" \
"v_mfma_f32_16x16x32_bf16 v[64:67], v[4:7], v[196:199], v[64:67]\n" \
"ds_read_b128 v[220:223], v231 offset:6144\n" \
"v_cvt_pk_bf16_f32 v236, v236, v237\n" \
"v_mfma_f32_16x16x32_bf16 v[68:71], v[8:11], v[192:195], 0\n" \
"s_add_u32 s90, s88, 0\n" \
"ds_read_b64_tr_b16 v[32:33], v232 offset:1024\n" \
"v_accvgpr_write_b32 a232, v234\n" \
"v_mfma_f32_16x16x32_bf16 v[68:71], v[12:15], v[196:199], v[68:71]\n" \
"buffer_store_dwordx4 a[192:195], v249, s[56:59], s90 offen offset:0 sc1\n" \
"v_accvgpr_write_b32 a233, v236\n" \
"v_accvgpr_read_b32 v234, a236\n" \
"v_accvgpr_read_b32 v235, a237\n" \
"v_accvgpr_read_b32 v236, a238\n" \
"v_mfma_f32_16x16x32_bf16 v[72:75], v[16:19], v[192:195], 0\n" \
"ds_read_b64_tr_b16 v[34:35], v232 offset:3072\n" \
"v_accvgpr_read_b32 v237, a239\n" \
"v_mfma_f32_16x16x32_bf16 v[72:75], v[20:23], v[196:199], v[72:75]\n" \
"ds_read_b64_tr_b16 v[36:37], v232 offset:9216\n" \
"v_mul_f32 v234, s92, v234\n" \
"v_mfma_f32_16x16x32_bf16 v[76:79], v[24:27], v[192:195], 0\n" \
"ds_read_b64_tr_b16 v[38:39], v232 offset:11264\n" \
"v_mul_f32 v235, s92, v235\n" \
"v_mfma_f32_16x16x32_bf16 v[76:79], v[28:31], v[196:199], v[76:79]\n" \
"buffer_store_dwordx4 a[224:227], v249, s[56:59], s90 offen offset:256 sc1\n" \
"v_mul_f32 v236, s92, v236\n" \
"v_mul_f32 v237, s92, v237\n" \
"v_cvt_pk_bf16_f32 v234, v234, v235\n" \
"v_cvt_pk_bf16_f32 v236, v236, v237\n" \
"v_mfma_f32_16x16x32_bf16 v[108:111], v[24:27], v[200:203], 0\n" \
"ds_read_b64_tr_b16 v[40:41], v233 offset:1024\n" \
"v_accvgpr_write_b32 a234, v234\n" \
"v_mfma_f32_16x16x32_bf16 v[108:111], v[28:31], v[204:207], v[108:111]\n" \
"ds_read_b64_tr_b16 v[42:43], v233 offset:3072\n" \
"v_accvgpr_write_b32 a235, v236\n" \
"v_mfma_f32_16x16x32_bf16 v[104:107], v[16:19], v[200:203], 0\n" \
"ds_read_b64_tr_b16 v[44:45], v233 offset:9216\n" \
"v_accvgpr_read_b32 v234, a208\n" \
"v_mfma_f32_16x16x32_bf16 v[104:107], v[20:23], v[204:207], v[104:107]\n" \
"v_accvgpr_read_b32 v235, a209\n" \
"v_accvgpr_read_b32 v236, a210\n" \
"v_mfma_f32_16x16x32_bf16 v[100:103], v[8:11], v[200:203], 0\n" \
"ds_read_b64_tr_b16 v[46:47], v233 offset:11264\n" \
"v_accvgpr_read_b32 v237, a211\n" \
"v_mfma_f32_16x16x32_bf16 v[100:103], v[12:15], v[204:207], v[100:103]\n" \
"ds_read_b64_tr_b16 v[48:49], v232 offset:1536\n" \
"v_cvt_pk_bf16_f32 v234, v234, v235\n" \
"v_mfma_f32_16x16x32_bf16 v[96:99], v[0:3], v[200:203], 0\n" \
"ds_read_b64_tr_b16 v[50:51], v232 offset:3584\n" \
"v_cvt_pk_bf16_f32 v236, v236, v237\n" \
"v_mfma_f32_16x16x32_bf16 v[96:99], v[4:7], v[204:207], v[96:99]\n" \
"v_accvgpr_write_b32 a208, v234\n" \
"v_accvgpr_write_b32 a209, v236\n" \
"v_mfma_f32_16x16x32_bf16 v[128:131], v[0:3], v[208:211], 0\n" \
"ds_read_b64_tr_b16 v[52:53], v232 offset:9728\n" \
"v_accvgpr_read_b32 v234, a240\n" \
"v_mfma_f32_16x16x32_bf16 v[128:131], v[4:7], v[212:215], v[128:131]\n" \
"ds_read_b64_tr_b16 v[54:55], v232 offset:11776\n" \
"v_accvgpr_read_b32 v235, a241\n" \
"v_mfma_f32_16x16x32_bf16 v[132:135], v[8:11], v[208:211], 0\n" \
"ds_read_b64_tr_b16 v[56:57], v233 offset:1536\n" \
"v_accvgpr_read_b32 v236, a242\n" \
"v_mfma_f32_16x16x32_bf16 v[132:135], v[12:15], v[212:215], v[132:135]\n" \
"v_accvgpr_read_b32 v237, a243\n" \
"v_cvt_pk_bf16_f32 v234, v234, v235\n" \
"Lpg2226_%=:\n" \
"Lst0x3_%=:\n" \
"s_cbranch_execz Lpg4398_%=\n" \
"v_mfma_f32_16x16x32_bf16 v[136:139], v[16:19], v[208:211], 0\n" \
"ds_read_b64_tr_b16 v[58:59], v233 offset:3584\n" \
"v_cvt_pk_bf16_f32 v236, v236, v237\n" \
"v_mfma_f32_16x16x32_bf16 v[136:139], v[20:23], v[212:215], v[136:139]\n" \
"ds_read_b64_tr_b16 v[60:61], v233 offset:9728\n" \
"v_accvgpr_write_b32 a210, v234\n" \
"v_mfma_f32_16x16x32_bf16 v[140:143], v[24:27], v[208:211], 0\n" \
"ds_read_b64_tr_b16 v[62:63], v233 offset:11776\n" \
"v_accvgpr_write_b32 a211, v236\n" \
"v_mfma_f32_16x16x32_bf16 v[140:143], v[28:31], v[212:215], v[140:143]\n" \
"s_waitcnt lgkmcnt(3)\n" \
"v_accvgpr_read_b32 v234, a212\n" \
"v_accvgpr_read_b32 v235, a213\n" \
"v_mfma_f32_16x16x32_bf16 v[172:175], v[24:27], v[216:219], 0\n" \
"v_add_u32 v230, 0xffff0000, v230\n" \
"v_add_u32 v231, 0xffff0000, v231\n" \
"v_mfma_f32_16x16x32_bf16 v[172:175], v[28:31], v[220:223], v[172:175]\n" \
"v_add_u32 v232, 0x4000, v232\n" \
"v_add_u32 v233, 0x4000, v233\n" \
"v_mfma_f32_16x16x32_bf16 v[168:171], v[16:19], v[216:219], 0\n" \
"v_accvgpr_read_b32 v236, a214\n" \
"v_accvgpr_read_b32 v237, a215\n" \
"v_mfma_f32_16x16x32_bf16 v[168:171], v[20:23], v[220:223], v[168:171]\n" \
"v_cvt_pk_bf16_f32 v234, v234, v235\n" \
"v_cvt_pk_bf16_f32 v236, v236, v237\n" \
"v_mfma_f32_16x16x32_bf16 v[164:167], v[8:11], v[216:219], 0\n" \
"v_accvgpr_write_b32 a212, v234\n" \
"v_accvgpr_write_b32 a213, v236\n" \
"v_mfma_f32_16x16x32_bf16 v[164:167], v[12:15], v[220:223], v[164:167]\n" \
"s_waitcnt vmcnt(18) lgkmcnt(0)\n" \
"v_accvgpr_read_b32 v234, a244\n" \
"v_accvgpr_read_b32 v235, a245\n" \
"v_mfma_f32_16x16x32_bf16 v[160:163], v[0:3], v[216:219], 0\n" \
"v_accvgpr_read_b32 v236, a246\n" \
"v_accvgpr_read_b32 v237, a247\n" \
"v_mfma_f32_16x16x32_bf16 v[160:163], v[4:7], v[220:223], v[160:163]\n" \
"s_barrier\n" \
"v_mfma_f32_16x16x32_bf16 v[80:83], v[32:35], v[192:195], 0\n" \
"ds_read_b64_tr_b16 v[0:1], v232 offset:0\n" \
"v_cvt_pk_bf16_f32 v234, v234, v235\n" \
"v_mfma_f32_16x16x32_bf16 v[80:83], v[36:39], v[196:199], v[80:83]\n" \
"ds_read_b64_tr_b16 v[2:3], v232 offset:2048\n" \
"v_cvt_pk_bf16_f32 v236, v236, v237\n" \
"v_mfma_f32_16x16x32_bf16 v[84:87], v[40:43], v[192:195], 0\n" \
"s_add_u32 m0, s87, 135168\n" \
"ds_read_b64_tr_b16 v[4:5], v232 offset:8192\n" \
"v_accvgpr_write_b32 a214, v234\n" \
"v_mfma_f32_16x16x32_bf16 v[84:87], v[44:47], v[196:199], v[84:87]\n" \
"global_load_lds_dwordx4 v224, s[48:49] offset:-4096\n" \
"v_accvgpr_write_b32 a215, v236\n" \
"v_mfma_f32_16x16x32_bf16 v[88:91], v[48:51], v[192:195], 0\n" \
"ds_read_b64_tr_b16 v[6:7], v232 offset:10240\n" \
"v_accvgpr_read_b32 v234, a216\n" \
"v_mfma_f32_16x16x32_bf16 v[88:91], v[52:55], v[196:199], v[88:91]\n" \
"ds_read_b64_tr_b16 v[8:9], v233 offset:0\n" \
"v_accvgpr_read_b32 v235, a217\n" \
"v_mfma_f32_16x16x32_bf16 v[92:95], v[56:59], v[192:195], 0\n" \
"ds_read_b64_tr_b16 v[10:11], v233 offset:2048\n" \
"v_accvgpr_read_b32 v236, a218\n" \
"v_mfma_f32_16x16x32_bf16 v[92:95], v[60:63], v[196:199], v[92:95]\n" \
"global_load_lds_dwordx4 v225, s[48:49] offset:-3072\n" \
"v_accvgpr_read_b32 v237, a219\n" \
"v_mfma_f32_16x16x32_bf16 v[124:127], v[56:59], v[200:203], 0\n" \
"ds_read_b128 v[192:195], v230 offset:0\n" \
"v_cvt_pk_bf16_f32 v234, v234, v235\n" \
"v_mfma_f32_16x16x32_bf16 v[124:127], v[60:63], v[204:207], v[124:127]\n" \
"ds_read_b128 v[196:199], v231 offset:0\n" \
"v_cvt_pk_bf16_f32 v236, v236, v237\n" \
"v_mfma_f32_16x16x32_bf16 v[120:123], v[48:51], v[200:203], 0\n" \
"ds_read_b64_tr_b16 v[12:13], v233 offset:8192\n" \
"v_accvgpr_write_b32 a216, v234\n" \
"v_mfma_f32_16x16x32_bf16 v[120:123], v[52:55], v[204:207], v[120:123]\n" \
"global_load_lds_dwordx4 v226, s[48:49] offset:-2048\n" \
"v_accvgpr_write_b32 a217, v236\n" \
"v_mfma_f32_16x16x32_bf16 v[116:119], v[40:43], v[200:203], 0\n" \
"ds_read_b64_tr_b16 v[14:15], v233 offset:10240\n" \
"v_accvgpr_read_b32 v234, a248\n" \
"v_mfma_f32_16x16x32_bf16 v[116:119], v[44:47], v[204:207], v[116:119]\n" \
"ds_read_b64_tr_b16 v[16:17], v232 offset:512\n" \
"v_accvgpr_read_b32 v235, a249\n" \
"v_mfma_f32_16x16x32_bf16 v[112:115], v[32:35], v[200:203], 0\n" \
"ds_read_b64_tr_b16 v[18:19], v232 offset:2560\n" \
"v_accvgpr_read_b32 v236, a250\n" \
"v_mfma_f32_16x16x32_bf16 v[112:115], v[36:39], v[204:207], v[112:115]\n" \
"global_load_lds_dwordx4 v227, s[48:49] offset:-1024\n" \
"v_accvgpr_read_b32 v237, a251\n" \
"v_mfma_f32_16x16x32_bf16 v[144:147], v[32:35], v[208:211], 0\n" \
"s_add_u32 s48, s48, 0x80\n" \
"s_addc_u32 s49, s49, 0\n" \
"ds_read_b128 v[200:203], v230 offset:2048\n" \
"v_mfma_f32_16x16x32_bf16 v[144:147], v[36:39], v[212:215], v[144:147]\n" \
"ds_read_b128 v[204:207], v231 offset:2048\n" \
"v_cvt_pk_bf16_f32 v234, v234, v235\n" \
"v_mfma_f32_16x16x32_bf16 v[148:151], v[40:43], v[208:211], 0\n" \
"ds_read_b64_tr_b16 v[20:21], v232 offset:8704\n" \
"v_cvt_pk_bf16_f32 v236, v236, v237\n" \
"v_mfma_f32_16x16x32_bf16 v[148:151], v[44:47], v[212:215], v[148:151]\n" \
"global_load_lds_dwordx4 v224, s[46:47] offset:0\n" \
"v_accvgpr_write_b32 a218, v234\n" \
"v_mfma_f32_16x16x32_bf16 v[152:155], v[48:51], v[208:211], 0\n" \
"ds_read_b64_tr_b16 v[22:23], v232 offset:10752\n" \
"v_accvgpr_write_b32 a219, v236\n" \
"v_mfma_f32_16x16x32_bf16 v[152:155], v[52:55], v[212:215], v[152:155]\n" \
"ds_read_b64_tr_b16 v[24:25], v233 offset:512\n" \
"v_accvgpr_read_b32 v234, a220\n" \
"v_mfma_f32_16x16x32_bf16 v[156:159], v[56:59], v[208:211], 0\n" \
"ds_read_b64_tr_b16 v[26:27], v233 offset:2560\n" \
"v_accvgpr_read_b32 v235, a221\n" \
"v_mfma_f32_16x16x32_bf16 v[156:159], v[60:63], v[212:215], v[156:159]\n" \
"global_load_lds_dwordx4 v225, s[46:47] offset:1024\n" \
"v_accvgpr_read_b32 v236, a222\n" \
"v_mfma_f32_16x16x32_bf16 v[188:191], v[56:59], v[216:219], 0\n" \
"ds_read_b128 v[208:211], v230 offset:4096\n" \
"v_accvgpr_read_b32 v237, a223\n" \
"v_mfma_f32_16x16x32_bf16 v[188:191], v[60:63], v[220:223], v[188:191]\n" \
"ds_read_b128 v[212:215], v231 offset:4096\n" \
"v_cvt_pk_bf16_f32 v234, v234, v235\n" \
"v_mfma_f32_16x16x32_bf16 v[184:187], v[48:51], v[216:219], 0\n" \
"ds_read_b64_tr_b16 v[28:29], v233 offset:8704\n" \
"v_cvt_pk_bf16_f32 v236, v236, v237\n" \
"v_mfma_f32_16x16x32_bf16 v[184:187], v[52:55], v[220:223], v[184:187]\n" \
"global_load_lds_dwordx4 v226, s[46:47] offset:2048\n" \
"v_accvgpr_write_b32 a220, v234\n" \
"v_mfma_f32_16x16x32_bf16 v[180:183], v[40:43], v[216:219], 0\n" \
"ds_read_b64_tr_b16 v[30:31], v233 offset:10752\n" \
"v_accvgpr_write_b32 a221, v236\n" \
"v_mfma_f32_16x16x32_bf16 v[180:183], v[44:47], v[220:223], v[180:183]\n" \
"v_accvgpr_read_b32 v234, a252\n" \
"v_accvgpr_read_b32 v235, a253\n" \
"v_mfma_f32_16x16x32_bf16 v[176:179], v[32:35], v[216:219], 0\n" \
"v_accvgpr_read_b32 v236, a254\n" \
"v_accvgpr_read_b32 v237, a255\n" \
"v_mfma_f32_16x16x32_bf16 v[176:179], v[36:39], v[220:223], v[176:179]\n" \
"s_waitcnt lgkmcnt(1)\n" \
"global_load_lds_dwordx4 v227, s[46:47] offset:3072\n" \
"v_cvt_pk_bf16_f32 v234, v234, v235\n" \
"v_mfma_f32_16x16x32_bf16 v[64:67], v[0:3], v[192:195], v[64:67]\n" \
"s_add_u32 s46, s46, 0x80\n" \
"s_addc_u32 s47, s47, 0\n" \
"ds_read_b128 v[216:219], v230 offset:6144\n" \
"v_mfma_f32_16x16x32_bf16 v[64:67], v[4:7], v[196:199], v[64:67]\n" \
"ds_read_b128 v[220:223], v231 offset:6144\n" \
"v_cvt_pk_bf16_f32 v236, v236, v237\n" \
"v_mfma_f32_16x16x32_bf16 v[68:71], v[8:11], v[192:195], v[68:71]\n" \
"ds_read_b64_tr_b16 v[32:33], v232 offset:1024\n" \
"v_accvgpr_write_b32 a222, v234\n" \
"v_mfma_f32_16x16x32_bf16 v[68:71], v[12:15], v[196:199], v[68:71]\n" \
"buffer_store_dwordx4 a[200:203], v249, s[56:59], s90 offen offset:2048 sc1\n" \
"v_accvgpr_write_b32 a223, v236\n" \
"v_accvgpr_read_b32 v234, a128\n" \
"v_accvgpr_read_b32 v235, a129\n" \
"v_accvgpr_read_b32 v236, a130\n" \
"v_mfma_f32_16x16x32_bf16 v[72:75], v[16:19], v[192:195], v[72:75]\n" \
"ds_read_b64_tr_b16 v[34:35], v232 offset:3072\n" \
"v_accvgpr_read_b32 v237, a131\n" \
"v_mfma_f32_16x16x32_bf16 v[72:75], v[20:23], v[196:199], v[72:75]\n" \
"s_waitcnt lgkmcnt(3)\n" \
"ds_read_b64_tr_b16 v[36:37], v232 offset:9216\n" \
"v_mul_f32 v234, s92, v234\n" \
"v_mfma_f32_16x16x32_bf16 v[76:79], v[24:27], v[192:195], v[76:79]\n" \
"ds_read_b64_tr_b16 v[38:39], v232 offset:11264\n" \
"v_mul_f32 v235, s92, v235\n" \
"v_mfma_f32_16x16x32_bf16 v[76:79], v[28:31], v[196:199], v[76:79]\n" \
"v_mul_f32 v236, s92, v236\n" \
"v_mul_f32 v237, s92, v237\n" \
"v_mfma_f32_16x16x32_bf16 v[108:111], v[24:27], v[200:203], v[108:111]\n" \
"ds_read_b64_tr_b16 v[40:41], v233 offset:1024\n" \
"v_cvt_pk_bf16_f32 v234, v234, v235\n" \
"v_mfma_f32_16x16x32_bf16 v[108:111], v[28:31], v[204:207], v[108:111]\n" \
"ds_read_b64_tr_b16 v[42:43], v233 offset:3072\n" \
"v_cvt_pk_bf16_f32 v236, v236, v237\n" \
"v_mfma_f32_16x16x32_bf16 v[104:107], v[16:19], v[200:203], v[104:107]\n" \
"ds_read_b64_tr_b16 v[44:45], v233 offset:9216\n" \
"v_accvgpr_write_b32 a128, v234\n" \
"v_mfma_f32_16x16x32_bf16 v[104:107], v[20:23], v[204:207], v[104:107]\n" \
"v_accvgpr_write_b32 a129, v236\n" \
"v_accvgpr_read_b32 v234, a132\n" \
"v_mfma_f32_16x16x32_bf16 v[100:103], v[8:11], v[200:203], v[100:103]\n" \
"ds_read_b64_tr_b16 v[46:47], v233 offset:11264\n" \
"v_accvgpr_read_b32 v235, a133\n" \
"v_mfma_f32_16x16x32_bf16 v[100:103], v[12:15], v[204:207], v[100:103]\n" \
"ds_read_b64_tr_b16 v[48:49], v232 offset:1536\n" \
"v_accvgpr_read_b32 v236, a134\n" \
"v_mfma_f32_16x16x32_bf16 v[96:99], v[0:3], v[200:203], v[96:99]\n" \
"s_add_u32 m0, s95, 4096\n" \
"ds_read_b64_tr_b16 v[50:51], v232 offset:3584\n" \
"v_accvgpr_read_b32 v237, a135\n" \
"v_mfma_f32_16x16x32_bf16 v[96:99], v[4:7], v[204:207], v[96:99]\n" \
"global_load_lds_dwordx4 v228, s[44:45] offset:-4096\n" \
"v_mul_f32 v234, s92, v234\n" \
"v_mfma_f32_16x16x32_bf16 v[128:131], v[0:3], v[208:211], v[128:131]\n" \
"ds_read_b64_tr_b16 v[52:53], v232 offset:9728\n" \
"v_mul_f32 v235, s92, v235\n" \
"v_mfma_f32_16x16x32_bf16 v[128:131], v[4:7], v[212:215], v[128:131]\n" \
"ds_read_b64_tr_b16 v[54:55], v232 offset:11776\n" \
"v_mul_f32 v236, s92, v236\n" \
"v_mfma_f32_16x16x32_bf16 v[132:135], v[8:11], v[208:211], v[132:135]\n" \
"ds_read_b64_tr_b16 v[56:57], v233 offset:1536\n" \
"v_mul_f32 v237, s92, v237\n" \
"v_mfma_f32_16x16x32_bf16 v[132:135], v[12:15], v[212:215], v[132:135]\n" \
"global_load_lds_dwordx4 v229, s[44:45] offset:-3072\n" \
"v_cvt_pk_bf16_f32 v234, v234, v235\n" \
"v_mfma_f32_16x16x32_bf16 v[136:139], v[16:19], v[208:211], v[136:139]\n" \
"s_add_u32 s44, s44, 0x100000\n" \
"s_addc_u32 s45, s45, 0\n" \
"ds_read_b64_tr_b16 v[58:59], v233 offset:3584\n" \
"v_mfma_f32_16x16x32_bf16 v[136:139], v[20:23], v[212:215], v[136:139]\n" \
"ds_read_b64_tr_b16 v[60:61], v233 offset:9728\n" \
"v_cvt_pk_bf16_f32 v236, v236, v237\n" \
"v_mfma_f32_16x16x32_bf16 v[140:143], v[24:27], v[208:211], v[140:143]\n" \
"ds_read_b64_tr_b16 v[62:63], v233 offset:11776\n" \
"v_accvgpr_write_b32 a130, v234\n" \
"v_mfma_f32_16x16x32_bf16 v[140:143], v[28:31], v[212:215], v[140:143]\n" \
"s_waitcnt lgkmcnt(3)\n" \
"global_load_lds_dwordx4 v228, s[42:43] offset:-2048\n" \
"v_accvgpr_write_b32 a131, v236\n" \
"v_mfma_f32_16x16x32_bf16 v[172:175], v[24:27], v[216:219], v[172:175]\n" \
"v_add_u32 v230, 0x8000, v230\n" \
"v_add_u32 v231, 0x8000, v231\n" \
"v_mfma_f32_16x16x32_bf16 v[172:175], v[28:31], v[220:223], v[172:175]\n" \
"v_add_u32 v232, 0x4000, v232\n" \
"v_add_u32 v233, 0x4000, v233\n" \
"v_mfma_f32_16x16x32_bf16 v[168:171], v[16:19], v[216:219], v[168:171]\n" \
"v_accvgpr_read_b32 v234, a160\n" \
"v_accvgpr_read_b32 v235, a161\n" \
"v_mfma_f32_16x16x32_bf16 v[168:171], v[20:23], v[220:223], v[168:171]\n" \
"global_load_lds_dwordx4 v229, s[42:43] offset:-1024\n" \
"v_accvgpr_read_b32 v236, a162\n" \
"v_mfma_f32_16x16x32_bf16 v[164:167], v[8:11], v[216:219], v[164:167]\n" \
"s_add_u32 s42, s42, 0x100000\n" \
"s_addc_u32 s43, s43, 0\n" \
"v_accvgpr_read_b32 v237, a163\n" \
"v_mfma_f32_16x16x32_bf16 v[164:167], v[12:15], v[220:223], v[164:167]\n" \
"s_waitcnt vmcnt(19) lgkmcnt(0)\n" \
"v_mul_f32 v234, s92, v234\n" \
"v_mul_f32 v235, s92, v235\n" \
"v_mfma_f32_16x16x32_bf16 v[160:163], v[0:3], v[216:219], v[160:163]\n" \
"v_mul_f32 v236, s92, v236\n" \
"v_mul_f32 v237, s92, v237\n" \
"v_mfma_f32_16x16x32_bf16 v[160:163], v[4:7], v[220:223], v[160:163]\n" \
"s_barrier\n" \
"v_mfma_f32_16x16x32_bf16 v[80:83], v[32:35], v[192:195], v[80:83]\n" \
"ds_read_b64_tr_b16 v[0:1], v232 offset:0\n" \
"v_cvt_pk_bf16_f32 v234, v234, v235\n" \
"v_mfma_f32_16x16x32_bf16 v[80:83], v[36:39], v[196:199], v[80:83]\n" \
"ds_read_b64_tr_b16 v[2:3], v232 offset:2048\n" \
"v_cvt_pk_bf16_f32 v236, v236, v237\n" \
"v_mfma_f32_16x16x32_bf16 v[84:87], v[40:43], v[192:195], v[84:87]\n" \
"s_add_u32 m0, s87, 69632\n" \
"ds_read_b64_tr_b16 v[4:5], v232 offset:8192\n" \
"v_accvgpr_write_b32 a160, v234\n" \
"v_mfma_f32_16x16x32_bf16 v[84:87], v[44:47], v[196:199], v[84:87]\n" \
"global_load_lds_dwordx4 v224, s[48:49] offset:-4096\n" \
"v_accvgpr_write_b32 a161, v236\n" \
"v_mfma_f32_16x16x32_bf16 v[88:91], v[48:51], v[192:195], v[88:91]\n" \
"ds_read_b64_tr_b16 v[6:7], v232 offset:10240\n" \
"v_accvgpr_read_b32 v234, a164\n" \
"v_mfma_f32_16x16x32_bf16 v[88:91], v[52:55], v[196:199], v[88:91]\n" \
"ds_read_b64_tr_b16 v[8:9], v233 offset:0\n" \
"v_accvgpr_read_b32 v235, a165\n" \
"v_mfma_f32_16x16x32_bf16 v[92:95], v[56:59], v[192:195], v[92:95]\n" \
"ds_read_b64_tr_b16 v[10:11], v233 offset:2048\n" \
"v_accvgpr_read_b32 v236, a166\n" \
"v_mfma_f32_16x16x32_bf16 v[92:95], v[60:63], v[196:199], v[92:95]\n" \
"global_load_lds_dwordx4 v225, s[48:49] offset:-3072\n" \
"v_accvgpr_read_b32 v237, a167\n" \
"v_mfma_f32_16x16x32_bf16 v[124:127], v[56:59], v[200:203], v[124:127]\n" \
"ds_read_b128 v[192:195], v230 offset:0\n" \
"v_mul_f32 v234, s92, v234\n" \
"v_mfma_f32_16x16x32_bf16 v[124:127], v[60:63], v[204:207], v[124:127]\n" \
"ds_read_b128 v[196:199], v231 offset:0\n" \
"v_mul_f32 v235, s92, v235\n" \
"v_mfma_f32_16x16x32_bf16 v[120:123], v[48:51], v[200:203], v[120:123]\n" \
"ds_read_b64_tr_b16 v[12:13], v233 offset:8192\n" \
"v_mul_f32 v236, s92, v236\n" \
"v_mfma_f32_16x16x32_bf16 v[120:123], v[52:55], v[204:207], v[120:123]\n" \
"global_load_lds_dwordx4 v226, s[48:49] offset:-2048\n" \
"v_mul_f32 v237, s92, v237\n" \
"v_mfma_f32_16x16x32_bf16 v[116:119], v[40:43], v[200:203], v[116:119]\n" \
"ds_read_b64_tr_b16 v[14:15], v233 offset:10240\n" \
"v_cvt_pk_bf16_f32 v234, v234, v235\n" \
"v_mfma_f32_16x16x32_bf16 v[116:119], v[44:47], v[204:207], v[116:119]\n" \
"ds_read_b64_tr_b16 v[16:17], v232 offset:512\n" \
"v_cvt_pk_bf16_f32 v236, v236, v237\n" \
"v_mfma_f32_16x16x32_bf16 v[112:115], v[32:35], v[200:203], v[112:115]\n" \
"ds_read_b64_tr_b16 v[18:19], v232 offset:2560\n" \
"v_accvgpr_write_b32 a162, v234\n" \
"v_mfma_f32_16x16x32_bf16 v[112:115], v[36:39], v[204:207], v[112:115]\n" \
"global_load_lds_dwordx4 v227, s[48:49] offset:-1024\n" \
"v_accvgpr_write_b32 a163, v236\n" \
"v_mfma_f32_16x16x32_bf16 v[144:147], v[32:35], v[208:211], v[144:147]\n" \
"s_add_u32 s48, s48, 0x100\n" \
"s_addc_u32 s49, s49, 0\n" \
"ds_read_b128 v[200:203], v230 offset:2048\n" \
"v_mfma_f32_16x16x32_bf16 v[144:147], v[36:39], v[212:215], v[144:147]\n" \
"ds_read_b128 v[204:207], v231 offset:2048\n" \
"v_accvgpr_read_b32 v234, a136\n" \
"v_mfma_f32_16x16x32_bf16 v[148:151], v[40:43], v[208:211], v[148:151]\n" \
"ds_read_b64_tr_b16 v[20:21], v232 offset:8704\n" \
"v_accvgpr_read_b32 v235, a137\n" \
"v_mfma_f32_16x16x32_bf16 v[148:151], v[44:47], v[212:215], v[148:151]\n" \
"global_load_lds_dwordx4 v224, s[46:47] offset:0\n" \
"v_accvgpr_read_b32 v236, a138\n" \
"v_mfma_f32_16x16x32_bf16 v[152:155], v[48:51], v[208:211], v[152:155]\n" \
"ds_read_b64_tr_b16 v[22:23], v232 offset:10752\n" \
"v_accvgpr_read_b32 v237, a139\n" \
"v_mfma_f32_16x16x32_bf16 v[152:155], v[52:55], v[212:215], v[152:155]\n" \
"ds_read_b64_tr_b16 v[24:25], v233 offset:512\n" \
"v_mul_f32 v234, s92, v234\n" \
"v_mfma_f32_16x16x32_bf16 v[156:159], v[56:59], v[208:211], v[156:159]\n" \
"ds_read_b64_tr_b16 v[26:27], v233 offset:2560\n" \
"v_mul_f32 v235, s92, v235\n" \
"v_mfma_f32_16x16x32_bf16 v[156:159], v[60:63], v[212:215], v[156:159]\n" \
"global_load_lds_dwordx4 v225, s[46:47] offset:1024\n" \
"v_mul_f32 v236, s92, v236\n" \
"v_mfma_f32_16x16x32_bf16 v[188:191], v[56:59], v[216:219], v[188:191]\n" \
"ds_read_b128 v[208:211], v230 offset:4096\n" \
"v_mul_f32 v237, s92, v237\n" \
"v_mfma_f32_16x16x32_bf16 v[188:191], v[60:63], v[220:223], v[188:191]\n" \
"ds_read_b128 v[212:215], v231 offset:4096\n" \
"v_cvt_pk_bf16_f32 v234, v234, v235\n" \
"v_mfma_f32_16x16x32_bf16 v[184:187], v[48:51], v[216:219], v[184:187]\n" \
"ds_read_b64_tr_b16 v[28:29], v233 offset:8704\n" \
"v_cvt_pk_bf16_f32 v236, v236, v237\n" \
"v_mfma_f32_16x16x32_bf16 v[184:187], v[52:55], v[220:223], v[184:187]\n" \
"global_load_lds_dwordx4 v226, s[46:47] offset:2048\n" \
"v_accvgpr_write_b32 a136, v234\n" \
"v_mfma_f32_16x16x32_bf16 v[180:183], v[40:43], v[216:219], v[180:183]\n" \
"ds_read_b64_tr_b16 v[30:31], v233 offset:10752\n" \
"v_accvgpr_write_b32 a137, v236\n" \
"v_mfma_f32_16x16x32_bf16 v[180:183], v[44:47], v[220:223], v[180:183]\n" \
"v_accvgpr_read_b32 v234, a140\n" \
"v_accvgpr_read_b32 v235, a141\n" \
"v_mfma_f32_16x16x32_bf16 v[176:179], v[32:35], v[216:219], v[176:179]\n" \
"v_accvgpr_read_b32 v236, a142\n" \
"v_accvgpr_read_b32 v237, a143\n" \
"v_mfma_f32_16x16x32_bf16 v[176:179], v[36:39], v[220:223], v[176:179]\n" \
"s_waitcnt lgkmcnt(1)\n" \
"global_load_lds_dwordx4 v227, s[46:47] offset:3072\n" \
"v_mul_f32 v234, s92, v234\n" \
"v_mfma_f32_16x16x32_bf16 v[64:67], v[0:3], v[192:195], v[64:67]\n" \
"s_add_u32 s46, s46, 0x100\n" \
"s_addc_u32 s47, s47, 0\n" \
"ds_read_b128 v[216:219], v230 offset:6144\n" \
"v_mfma_f32_16x16x32_bf16 v[64:67], v[4:7], v[196:199], v[64:67]\n" \
"ds_read_b128 v[220:223], v231 offset:6144\n" \
"v_mul_f32 v235, s92, v235\n" \
"v_mfma_f32_16x16x32_bf16 v[68:71], v[8:11], v[192:195], v[68:71]\n" \
"ds_read_b64_tr_b16 v[32:33], v232 offset:1024\n" \
"v_mul_f32 v236, s92, v236\n" \
"v_mfma_f32_16x16x32_bf16 v[68:71], v[12:15], v[196:199], v[68:71]\n" \
"buffer_store_dwordx4 a[232:235], v249, s[56:59], s90 offen offset:2304 sc1\n" \
"v_mul_f32 v237, s92, v237\n" \
"v_cvt_pk_bf16_f32 v234, v234, v235\n" \
"v_cvt_pk_bf16_f32 v236, v236, v237\n" \
"v_accvgpr_write_b32 a138, v234\n" \
"v_mfma_f32_16x16x32_bf16 v[72:75], v[16:19], v[192:195], v[72:75]\n" \
"ds_read_b64_tr_b16 v[34:35], v232 offset:3072\n" \
"v_accvgpr_write_b32 a139, v236\n" \
"v_mfma_f32_16x16x32_bf16 v[72:75], v[20:23], v[196:199], v[72:75]\n" \
"s_waitcnt lgkmcnt(3)\n" \
"ds_read_b64_tr_b16 v[36:37], v232 offset:9216\n" \
"v_accvgpr_read_b32 v234, a168\n" \
"v_mfma_f32_16x16x32_bf16 v[76:79], v[24:27], v[192:195], v[76:79]\n" \
"ds_read_b64_tr_b16 v[38:39], v232 offset:11264\n" \
"v_accvgpr_read_b32 v235, a169\n" \
"v_mfma_f32_16x16x32_bf16 v[76:79], v[28:31], v[196:199], v[76:79]\n" \
"v_accvgpr_read_b32 v236, a170\n" \
"v_accvgpr_read_b32 v237, a171\n" \
"v_mfma_f32_16x16x32_bf16 v[108:111], v[24:27], v[200:203], v[108:111]\n" \
"ds_read_b64_tr_b16 v[40:41], v233 offset:1024\n" \
"v_mul_f32 v234, s92, v234\n" \
"v_mfma_f32_16x16x32_bf16 v[108:111], v[28:31], v[204:207], v[108:111]\n" \
"ds_read_b64_tr_b16 v[42:43], v233 offset:3072\n" \
"v_mul_f32 v235, s92, v235\n" \
"v_mfma_f32_16x16x32_bf16 v[104:107], v[16:19], v[200:203], v[104:107]\n" \
"ds_read_b64_tr_b16 v[44:45], v233 offset:9216\n" \
"v_mul_f32 v236, s92, v236\n" \
"v_mfma_f32_16x16x32_bf16 v[104:107], v[20:23], v[204:207], v[104:107]\n" \
"v_mul_f32 v237, s92, v237\n" \
"v_cvt_pk_bf16_f32 v234, v234, v235\n" \
"v_mfma_f32_16x16x32_bf16 v[100:103], v[8:11], v[200:203], v[100:103]\n" \
"ds_read_b64_tr_b16 v[46:47], v233 offset:11264\n" \
"v_cvt_pk_bf16_f32 v236, v236, v237\n" \
"v_mfma_f32_16x16x32_bf16 v[100:103], v[12:15], v[204:207], v[100:103]\n" \
"ds_read_b64_tr_b16 v[48:49], v232 offset:1536\n" \
"v_accvgpr_write_b32 a168, v234\n" \
"v_mfma_f32_16x16x32_bf16 v[96:99], v[0:3], v[200:203], v[96:99]\n" \
"s_add_u32 m0, s95, 20480\n" \
"ds_read_b64_tr_b16 v[50:51], v232 offset:3584\n" \
"v_accvgpr_write_b32 a169, v236\n" \
"v_mfma_f32_16x16x32_bf16 v[96:99], v[4:7], v[204:207], v[96:99]\n" \
"global_load_lds_dwordx4 v228, s[44:45] offset:-4096\n" \
"v_accvgpr_read_b32 v234, a172\n" \
"v_mfma_f32_16x16x32_bf16 v[128:131], v[0:3], v[208:211], v[128:131]\n" \
"ds_read_b64_tr_b16 v[52:53], v232 offset:9728\n" \
"v_accvgpr_read_b32 v235, a173\n" \
"v_mfma_f32_16x16x32_bf16 v[128:131], v[4:7], v[212:215], v[128:131]\n" \
"ds_read_b64_tr_b16 v[54:55], v232 offset:11776\n" \
"v_accvgpr_read_b32 v236, a174\n" \
"v_mfma_f32_16x16x32_bf16 v[132:135], v[8:11], v[208:211], v[132:135]\n" \
"ds_read_b64_tr_b16 v[56:57], v233 offset:1536\n" \
"v_accvgpr_read_b32 v237, a175\n" \
"v_mfma_f32_16x16x32_bf16 v[132:135], v[12:15], v[212:215], v[132:135]\n" \
"global_load_lds_dwordx4 v229, s[44:45] offset:-3072\n" \
"v_mul_f32 v234, s92, v234\n" \
"v_mfma_f32_16x16x32_bf16 v[136:139], v[16:19], v[208:211], v[136:139]\n" \
"s_add_u32 s44, s44, 0x80000\n" \
"s_addc_u32 s45, s45, 0\n" \
"ds_read_b64_tr_b16 v[58:59], v233 offset:3584\n" \
"v_mfma_f32_16x16x32_bf16 v[136:139], v[20:23], v[212:215], v[136:139]\n" \
"ds_read_b64_tr_b16 v[60:61], v233 offset:9728\n" \
"v_mul_f32 v235, s92, v235\n" \
"v_mfma_f32_16x16x32_bf16 v[140:143], v[24:27], v[208:211], v[140:143]\n" \
"ds_read_b64_tr_b16 v[62:63], v233 offset:11776\n" \
"v_mul_f32 v236, s92, v236\n" \
"v_mfma_f32_16x16x32_bf16 v[140:143], v[28:31], v[212:215], v[140:143]\n" \
"s_waitcnt lgkmcnt(3)\n" \
"global_load_lds_dwordx4 v228, s[42:43] offset:-2048\n" \
"v_mul_f32 v237, s92, v237\n" \
"v_mfma_f32_16x16x32_bf16 v[172:175], v[24:27], v[216:219], v[172:175]\n" \
"v_add_u32 v230, 0x8000, v230\n" \
"v_add_u32 v231, 0x8000, v231\n" \
"v_mfma_f32_16x16x32_bf16 v[172:175], v[28:31], v[220:223], v[172:175]\n" \
"v_add_u32 v232, 0x4000, v232\n" \
"v_add_u32 v233, 0x4000, v233\n" \
"v_mfma_f32_16x16x32_bf16 v[168:171], v[16:19], v[216:219], v[168:171]\n" \
"v_cvt_pk_bf16_f32 v234, v234, v235\n" \
"v_cvt_pk_bf16_f32 v236, v236, v237\n" \
"v_mfma_f32_16x16x32_bf16 v[168:171], v[20:23], v[220:223], v[168:171]\n" \
"global_load_lds_dwordx4 v229, s[42:43] offset:-1024\n" \
"v_accvgpr_write_b32 a170, v234\n" \
"v_mfma_f32_16x16x32_bf16 v[164:167], v[8:11], v[216:219], v[164:167]\n" \
"s_add_u32 s42, s42, 0x80000\n" \
"s_addc_u32 s43, s43, 0\n" \
"v_accvgpr_write_b32 a171, v236\n" \
"v_mfma_f32_16x16x32_bf16 v[164:167], v[12:15], v[220:223], v[164:167]\n" \
"s_waitcnt vmcnt(18) lgkmcnt(0)\n" \
"v_accvgpr_read_b32 v234, a144\n" \
"v_accvgpr_read_b32 v235, a145\n" \
"v_mfma_f32_16x16x32_bf16 v[160:163], v[0:3], v[216:219], v[160:163]\n" \
"v_accvgpr_read_b32 v236, a146\n" \
"v_accvgpr_read_b32 v237, a147\n" \
"v_mfma_f32_16x16x32_bf16 v[160:163], v[4:7], v[220:223], v[160:163]\n" \
"s_barrier\n" \
"v_mfma_f32_16x16x32_bf16 v[80:83], v[32:35], v[192:195], v[80:83]\n" \
"ds_read_b64_tr_b16 v[0:1], v232 offset:0\n" \
"v_cvt_pk_bf16_f32 v234, v234, v235\n" \
"v_mfma_f32_16x16x32_bf16 v[80:83], v[36:39], v[196:199], v[80:83]\n" \
"ds_read_b64_tr_b16 v[2:3], v232 offset:2048\n" \
"v_cvt_pk_bf16_f32 v236, v236, v237\n" \
"v_mfma_f32_16x16x32_bf16 v[84:87], v[40:43], v[192:195], v[84:87]\n" \
"s_add_u32 m0, s87, 102400\n" \
"ds_read_b64_tr_b16 v[4:5], v232 offset:8192\n" \
"v_accvgpr_write_b32 a144, v234\n" \
"v_mfma_f32_16x16x32_bf16 v[84:87], v[44:47], v[196:199], v[84:87]\n" \
"global_load_lds_dwordx4 v224, s[48:49] offset:-4096\n" \
"v_accvgpr_write_b32 a145, v236\n" \
"v_mfma_f32_16x16x32_bf16 v[88:91], v[48:51], v[192:195], v[88:91]\n" \
"ds_read_b64_tr_b16 v[6:7], v232 offset:10240\n" \
"v_accvgpr_read_b32 v234, a176\n" \
"v_mfma_f32_16x16x32_bf16 v[88:91], v[52:55], v[196:199], v[88:91]\n" \
"ds_read_b64_tr_b16 v[8:9], v233 offset:0\n" \
"v_accvgpr_read_b32 v235, a177\n" \
"v_mfma_f32_16x16x32_bf16 v[92:95], v[56:59], v[192:195], v[92:95]\n" \
"ds_read_b64_tr_b16 v[10:11], v233 offset:2048\n" \
"v_accvgpr_read_b32 v236, a178\n" \
"v_mfma_f32_16x16x32_bf16 v[92:95], v[60:63], v[196:199], v[92:95]\n" \
"global_load_lds_dwordx4 v225, s[48:49] offset:-3072\n" \
"v_accvgpr_read_b32 v237, a179\n" \
"v_mfma_f32_16x16x32_bf16 v[124:127], v[56:59], v[200:203], v[124:127]\n" \
"ds_read_b128 v[192:195], v230 offset:0\n" \
"v_cvt_pk_bf16_f32 v234, v234, v235\n" \
"v_mfma_f32_16x16x32_bf16 v[124:127], v[60:63], v[204:207], v[124:127]\n" \
"ds_read_b128 v[196:199], v231 offset:0\n" \
"v_cvt_pk_bf16_f32 v236, v236, v237\n" \
"v_mfma_f32_16x16x32_bf16 v[120:123], v[48:51], v[200:203], v[120:123]\n" \
"ds_read_b64_tr_b16 v[12:13], v233 offset:8192\n" \
"v_accvgpr_write_b32 a146, v234\n" \
"v_mfma_f32_16x16x32_bf16 v[120:123], v[52:55], v[204:207], v[120:123]\n" \
"global_load_lds_dwordx4 v226, s[48:49] offset:-2048\n" \
"v_accvgpr_write_b32 a147, v236\n" \
"v_mfma_f32_16x16x32_bf16 v[116:119], v[40:43], v[200:203], v[116:119]\n" \
"ds_read_b64_tr_b16 v[14:15], v233 offset:10240\n" \
"v_accvgpr_read_b32 v234, a148\n" \
"v_mfma_f32_16x16x32_bf16 v[116:119], v[44:47], v[204:207], v[116:119]\n" \
"ds_read_b64_tr_b16 v[16:17], v232 offset:512\n" \
"v_accvgpr_read_b32 v235, a149\n" \
"v_mfma_f32_16x16x32_bf16 v[112:115], v[32:35], v[200:203], v[112:115]\n" \
"ds_read_b64_tr_b16 v[18:19], v232 offset:2560\n" \
"v_accvgpr_read_b32 v236, a150\n" \
"v_mfma_f32_16x16x32_bf16 v[112:115], v[36:39], v[204:207], v[112:115]\n" \
"global_load_lds_dwordx4 v227, s[48:49] offset:-1024\n" \
"v_accvgpr_read_b32 v237, a151\n" \
"v_mfma_f32_16x16x32_bf16 v[144:147], v[32:35], v[208:211], v[144:147]\n" \
"s_add_u32 s48, s48, 0x80\n" \
"s_addc_u32 s49, s49, 0\n" \
"ds_read_b128 v[200:203], v230 offset:2048\n" \
"v_mfma_f32_16x16x32_bf16 v[144:147], v[36:39], v[212:215], v[144:147]\n" \
"ds_read_b128 v[204:207], v231 offset:2048\n" \
"v_cvt_pk_bf16_f32 v234, v234, v235\n" \
"v_mfma_f32_16x16x32_bf16 v[148:151], v[40:43], v[208:211], v[148:151]\n" \
"ds_read_b64_tr_b16 v[20:21], v232 offset:8704\n" \
"v_cvt_pk_bf16_f32 v236, v236, v237\n" \
"v_mfma_f32_16x16x32_bf16 v[148:151], v[44:47], v[212:215], v[148:151]\n" \
"global_load_lds_dwordx4 v224, s[46:47] offset:0\n" \
"v_accvgpr_write_b32 a148, v234\n" \
"v_mfma_f32_16x16x32_bf16 v[152:155], v[48:51], v[208:211], v[152:155]\n" \
"ds_read_b64_tr_b16 v[22:23], v232 offset:10752\n" \
"v_accvgpr_write_b32 a149, v236\n" \
"v_mfma_f32_16x16x32_bf16 v[152:155], v[52:55], v[212:215], v[152:155]\n" \
"ds_read_b64_tr_b16 v[24:25], v233 offset:512\n" \
"v_accvgpr_read_b32 v234, a180\n" \
"v_mfma_f32_16x16x32_bf16 v[156:159], v[56:59], v[208:211], v[156:159]\n" \
"ds_read_b64_tr_b16 v[26:27], v233 offset:2560\n" \
"v_accvgpr_read_b32 v235, a181\n" \
"v_mfma_f32_16x16x32_bf16 v[156:159], v[60:63], v[212:215], v[156:159]\n" \
"global_load_lds_dwordx4 v225, s[46:47] offset:1024\n" \
"v_accvgpr_read_b32 v236, a182\n" \
"v_mfma_f32_16x16x32_bf16 v[188:191], v[56:59], v[216:219], v[188:191]\n" \
"ds_read_b128 v[208:211], v230 offset:4096\n" \
"v_accvgpr_read_b32 v237, a183\n" \
"v_mfma_f32_16x16x32_bf16 v[188:191], v[60:63], v[220:223], v[188:191]\n" \
"ds_read_b128 v[212:215], v231 offset:4096\n" \
"v_cvt_pk_bf16_f32 v234, v234, v235\n" \
"v_mfma_f32_16x16x32_bf16 v[184:187], v[48:51], v[216:219], v[184:187]\n" \
"ds_read_b64_tr_b16 v[28:29], v233 offset:8704\n" \
"v_cvt_pk_bf16_f32 v236, v236, v237\n" \
"v_mfma_f32_16x16x32_bf16 v[184:187], v[52:55], v[220:223], v[184:187]\n" \
"global_load_lds_dwordx4 v226, s[46:47] offset:2048\n" \
"v_accvgpr_write_b32 a150, v234\n" \
"v_mfma_f32_16x16x32_bf16 v[180:183], v[40:43], v[216:219], v[180:183]\n" \
"ds_read_b64_tr_b16 v[30:31], v233 offset:10752\n" \
"v_accvgpr_write_b32 a151, v236\n" \
"v_mfma_f32_16x16x32_bf16 v[180:183], v[44:47], v[220:223], v[180:183]\n" \
"v_accvgpr_read_b32 v234, a152\n" \
"v_accvgpr_read_b32 v235, a153\n" \
"v_mfma_f32_16x16x32_bf16 v[176:179], v[32:35], v[216:219], v[176:179]\n" \
"v_accvgpr_read_b32 v236, a154\n" \
"Lpg2766_%=:\n" \
"s_cbranch_execz Lpg4934_%=\n" \
"v_accvgpr_read_b32 v237, a155\n" \
"v_mfma_f32_16x16x32_bf16 v[176:179], v[36:39], v[220:223], v[176:179]\n" \
"s_waitcnt lgkmcnt(1)\n" \
"global_load_lds_dwordx4 v227, s[46:47] offset:3072\n" \
"v_cvt_pk_bf16_f32 v234, v234, v235\n" \
"v_mfma_f32_16x16x32_bf16 v[64:67], v[0:3], v[192:195], v[64:67]\n" \
"s_add_u32 s46, s46, 0x80\n" \
"s_addc_u32 s47, s47, 0\n" \
"ds_read_b128 v[216:219], v230 offset:6144\n" \
"v_mfma_f32_16x16x32_bf16 v[64:67], v[4:7], v[196:199], v[64:67]\n" \
"s_add_u32 s90, s89, 0\n" \
"ds_read_b128 v[220:223], v231 offset:6144\n" \
"v_cvt_pk_bf16_f32 v236, v236, v237\n" \
"v_mfma_f32_16x16x32_bf16 v[68:71], v[8:11], v[192:195], v[68:71]\n" \
"ds_read_b64_tr_b16 v[32:33], v232 offset:1024\n" \
"v_accvgpr_write_b32 a152, v234\n" \
"v_mfma_f32_16x16x32_bf16 v[68:71], v[12:15], v[196:199], v[68:71]\n" \
"buffer_store_dwordx4 a[208:211], v248, s[56:59], s90 offen offset:0 sc1\n" \
"v_accvgpr_write_b32 a153, v236\n" \
"v_accvgpr_read_b32 v234, a184\n" \
"v_accvgpr_read_b32 v235, a185\n" \
"v_accvgpr_read_b32 v236, a186\n" \
"v_mfma_f32_16x16x32_bf16 v[72:75], v[16:19], v[192:195], v[72:75]\n" \
"ds_read_b64_tr_b16 v[34:35], v232 offset:3072\n" \
"v_accvgpr_read_b32 v237, a187\n" \
"v_mfma_f32_16x16x32_bf16 v[72:75], v[20:23], v[196:199], v[72:75]\n" \
"s_waitcnt lgkmcnt(3)\n" \
"ds_read_b64_tr_b16 v[36:37], v232 offset:9216\n" \
"v_cvt_pk_bf16_f32 v234, v234, v235\n" \
"v_mfma_f32_16x16x32_bf16 v[76:79], v[24:27], v[192:195], v[76:79]\n" \
"ds_read_b64_tr_b16 v[38:39], v232 offset:11264\n" \
"v_cvt_pk_bf16_f32 v236, v236, v237\n" \
"v_mfma_f32_16x16x32_bf16 v[76:79], v[28:31], v[196:199], v[76:79]\n" \
"v_accvgpr_write_b32 a154, v234\n" \
"v_accvgpr_write_b32 a155, v236\n" \
"v_mfma_f32_16x16x32_bf16 v[108:111], v[24:27], v[200:203], v[108:111]\n" \
"ds_read_b64_tr_b16 v[40:41], v233 offset:1024\n" \
"v_accvgpr_read_b32 v234, a156\n" \
"v_mfma_f32_16x16x32_bf16 v[108:111], v[28:31], v[204:207], v[108:111]\n" \
"ds_read_b64_tr_b16 v[42:43], v233 offset:3072\n" \
"v_accvgpr_read_b32 v235, a157\n" \
"v_mfma_f32_16x16x32_bf16 v[104:107], v[16:19], v[200:203], v[104:107]\n" \
"ds_read_b64_tr_b16 v[44:45], v233 offset:9216\n" \
"v_accvgpr_read_b32 v236, a158\n" \
"v_mfma_f32_16x16x32_bf16 v[104:107], v[20:23], v[204:207], v[104:107]\n" \
"v_accvgpr_read_b32 v237, a159\n" \
"v_cvt_pk_bf16_f32 v234, v234, v235\n" \
"v_mfma_f32_16x16x32_bf16 v[100:103], v[8:11], v[200:203], v[100:103]\n" \
"ds_read_b64_tr_b16 v[46:47], v233 offset:11264\n" \
"v_cvt_pk_bf16_f32 v236, v236, v237\n" \
"v_mfma_f32_16x16x32_bf16 v[100:103], v[12:15], v[204:207], v[100:103]\n" \
"ds_read_b64_tr_b16 v[48:49], v232 offset:1536\n" \
"v_accvgpr_write_b32 a156, v234\n" \
"v_mfma_f32_16x16x32_bf16 v[96:99], v[0:3], v[200:203], v[96:99]\n" \
"s_add_u32 m0, s95, 36864\n" \
"ds_read_b64_tr_b16 v[50:51], v232 offset:3584\n" \
"v_accvgpr_write_b32 a157, v236\n" \
"v_mfma_f32_16x16x32_bf16 v[96:99], v[4:7], v[204:207], v[96:99]\n" \
"global_load_lds_dwordx4 v228, s[44:45] offset:-4096\n" \
"v_accvgpr_read_b32 v234, a188\n" \
"v_mfma_f32_16x16x32_bf16 v[128:131], v[0:3], v[208:211], v[128:131]\n" \
"ds_read_b64_tr_b16 v[52:53], v232 offset:9728\n" \
"v_accvgpr_read_b32 v235, a189\n" \
"v_mfma_f32_16x16x32_bf16 v[128:131], v[4:7], v[212:215], v[128:131]\n" \
"ds_read_b64_tr_b16 v[54:55], v232 offset:11776\n" \
"v_accvgpr_read_b32 v236, a190\n" \
"v_mfma_f32_16x16x32_bf16 v[132:135], v[8:11], v[208:211], v[132:135]\n" \
"ds_read_b64_tr_b16 v[56:57], v233 offset:1536\n" \
"v_accvgpr_read_b32 v237, a191\n" \
"v_mfma_f32_16x16x32_bf16 v[132:135], v[12:15], v[212:215], v[132:135]\n" \
"global_load_lds_dwordx4 v229, s[44:45] offset:-3072\n" \
"v_cvt_pk_bf16_f32 v234, v234, v235\n" \
"v_mfma_f32_16x16x32_bf16 v[136:139], v[16:19], v[208:211], v[136:139]\n" \
"s_add_u32 s44, s44, 0x80000\n" \
"s_addc_u32 s45, s45, 0\n" \
"ds_read_b64_tr_b16 v[58:59], v233 offset:3584\n" \
"v_mfma_f32_16x16x32_bf16 v[136:139], v[20:23], v[212:215], v[136:139]\n" \
"ds_read_b64_tr_b16 v[60:61], v233 offset:9728\n" \
"v_cvt_pk_bf16_f32 v236, v236, v237\n" \
"v_mfma_f32_16x16x32_bf16 v[140:143], v[24:27], v[208:211], v[140:143]\n" \
"ds_read_b64_tr_b16 v[62:63], v233 offset:11776\n" \
"v_accvgpr_write_b32 a158, v234\n" \
"v_mfma_f32_16x16x32_bf16 v[140:143], v[28:31], v[212:215], v[140:143]\n" \
"s_waitcnt lgkmcnt(3)\n" \
"global_load_lds_dwordx4 v228, s[42:43] offset:-2048\n" \
"v_accvgpr_write_b32 a159, v236\n" \
"v_mfma_f32_16x16x32_bf16 v[172:175], v[24:27], v[216:219], v[172:175]\n" \
"v_add_u32 v230, 0xffff0000, v230\n" \
"v_add_u32 v231, 0xffff0000, v231\n" \
"v_mfma_f32_16x16x32_bf16 v[172:175], v[28:31], v[220:223], v[172:175]\n" \
"v_add_u32 v232, 0xffff4000, v232\n" \
"v_add_u32 v233, 0xffff4000, v233\n" \
"v_mfma_f32_16x16x32_bf16 v[168:171], v[16:19], v[216:219], v[168:171]\n" \
"v_accvgpr_read_b32 v234, a64\n" \
"v_accvgpr_read_b32 v235, a65\n" \
"v_mfma_f32_16x16x32_bf16 v[168:171], v[20:23], v[220:223], v[168:171]\n" \
"global_load_lds_dwordx4 v229, s[42:43] offset:-1024\n" \
"v_accvgpr_read_b32 v236, a66\n" \
"v_mfma_f32_16x16x32_bf16 v[164:167], v[8:11], v[216:219], v[164:167]\n" \
"s_add_u32 s42, s42, 0x80000\n" \
"s_addc_u32 s43, s43, 0\n" \
"v_accvgpr_read_b32 v237, a67\n" \
"v_mfma_f32_16x16x32_bf16 v[164:167], v[12:15], v[220:223], v[164:167]\n" \
"s_waitcnt vmcnt(18) lgkmcnt(0)\n" \
"v_mul_f32 v234, s92, v234\n" \
"v_mul_f32 v235, s92, v235\n" \
"v_mfma_f32_16x16x32_bf16 v[160:163], v[0:3], v[216:219], v[160:163]\n" \
"v_mul_f32 v236, s92, v236\n" \
"v_mul_f32 v237, s92, v237\n" \
"v_mfma_f32_16x16x32_bf16 v[160:163], v[4:7], v[220:223], v[160:163]\n" \
"s_barrier\n" \
"v_mfma_f32_16x16x32_bf16 v[80:83], v[32:35], v[192:195], v[80:83]\n" \
"ds_read_b64_tr_b16 v[0:1], v232 offset:0\n" \
"v_cvt_pk_bf16_f32 v234, v234, v235\n" \
"v_mfma_f32_16x16x32_bf16 v[80:83], v[36:39], v[196:199], v[80:83]\n" \
"ds_read_b64_tr_b16 v[2:3], v232 offset:2048\n" \
"v_cvt_pk_bf16_f32 v236, v236, v237\n" \
"v_mfma_f32_16x16x32_bf16 v[84:87], v[40:43], v[192:195], v[84:87]\n" \
"s_add_u32 m0, s87, 135168\n" \
"ds_read_b64_tr_b16 v[4:5], v232 offset:8192\n" \
"v_accvgpr_write_b32 a64, v234\n" \
"v_mfma_f32_16x16x32_bf16 v[84:87], v[44:47], v[196:199], v[84:87]\n" \
"global_load_lds_dwordx4 v224, s[48:49] offset:-4096\n" \
"v_accvgpr_write_b32 a65, v236\n" \
"v_mfma_f32_16x16x32_bf16 v[88:91], v[48:51], v[192:195], v[88:91]\n" \
"ds_read_b64_tr_b16 v[6:7], v232 offset:10240\n" \
"v_accvgpr_read_b32 v234, a68\n" \
"v_mfma_f32_16x16x32_bf16 v[88:91], v[52:55], v[196:199], v[88:91]\n" \
"ds_read_b64_tr_b16 v[8:9], v233 offset:0\n" \
"v_accvgpr_read_b32 v235, a69\n" \
"v_mfma_f32_16x16x32_bf16 v[92:95], v[56:59], v[192:195], v[92:95]\n" \
"ds_read_b64_tr_b16 v[10:11], v233 offset:2048\n" \
"v_accvgpr_read_b32 v236, a70\n" \
"v_mfma_f32_16x16x32_bf16 v[92:95], v[60:63], v[196:199], v[92:95]\n" \
"global_load_lds_dwordx4 v225, s[48:49] offset:-3072\n" \
"v_accvgpr_read_b32 v237, a71\n" \
"v_mfma_f32_16x16x32_bf16 v[124:127], v[56:59], v[200:203], v[124:127]\n" \
"ds_read_b128 v[192:195], v230 offset:0\n" \
"v_mul_f32 v234, s92, v234\n" \
"v_mfma_f32_16x16x32_bf16 v[124:127], v[60:63], v[204:207], v[124:127]\n" \
"ds_read_b128 v[196:199], v231 offset:0\n" \
"v_mul_f32 v235, s92, v235\n" \
"v_mfma_f32_16x16x32_bf16 v[120:123], v[48:51], v[200:203], v[120:123]\n" \
"ds_read_b64_tr_b16 v[12:13], v233 offset:8192\n" \
"v_mul_f32 v236, s92, v236\n" \
"v_mfma_f32_16x16x32_bf16 v[120:123], v[52:55], v[204:207], v[120:123]\n" \
"global_load_lds_dwordx4 v226, s[48:49] offset:-2048\n" \
"v_mul_f32 v237, s92, v237\n" \
"v_mfma_f32_16x16x32_bf16 v[116:119], v[40:43], v[200:203], v[116:119]\n" \
"ds_read_b64_tr_b16 v[14:15], v233 offset:10240\n" \
"v_cvt_pk_bf16_f32 v234, v234, v235\n" \
"v_mfma_f32_16x16x32_bf16 v[116:119], v[44:47], v[204:207], v[116:119]\n" \
"ds_read_b64_tr_b16 v[16:17], v232 offset:512\n" \
"v_cvt_pk_bf16_f32 v236, v236, v237\n" \
"v_mfma_f32_16x16x32_bf16 v[112:115], v[32:35], v[200:203], v[112:115]\n" \
"ds_read_b64_tr_b16 v[18:19], v232 offset:2560\n" \
"v_accvgpr_write_b32 a66, v234\n" \
"v_mfma_f32_16x16x32_bf16 v[112:115], v[36:39], v[204:207], v[112:115]\n" \
"global_load_lds_dwordx4 v227, s[48:49] offset:-1024\n" \
"v_accvgpr_write_b32 a67, v236\n" \
"v_mfma_f32_16x16x32_bf16 v[144:147], v[32:35], v[208:211], v[144:147]\n" \
"s_add_u32 s48, s48, 0x80\n" \
"s_addc_u32 s49, s49, 0\n" \
"ds_read_b128 v[200:203], v230 offset:2048\n" \
"v_mfma_f32_16x16x32_bf16 v[144:147], v[36:39], v[212:215], v[144:147]\n" \
"ds_read_b128 v[204:207], v231 offset:2048\n" \
"v_accvgpr_read_b32 v234, a96\n" \
"v_mfma_f32_16x16x32_bf16 v[148:151], v[40:43], v[208:211], v[148:151]\n" \
"ds_read_b64_tr_b16 v[20:21], v232 offset:8704\n" \
"v_accvgpr_read_b32 v235, a97\n" \
"v_mfma_f32_16x16x32_bf16 v[148:151], v[44:47], v[212:215], v[148:151]\n" \
"global_load_lds_dwordx4 v224, s[46:47] offset:0\n" \
"v_accvgpr_read_b32 v236, a98\n" \
"v_mfma_f32_16x16x32_bf16 v[152:155], v[48:51], v[208:211], v[152:155]\n" \
"ds_read_b64_tr_b16 v[22:23], v232 offset:10752\n" \
"v_accvgpr_read_b32 v237, a99\n" \
"v_mfma_f32_16x16x32_bf16 v[152:155], v[52:55], v[212:215], v[152:155]\n" \
"ds_read_b64_tr_b16 v[24:25], v233 offset:512\n" \
"v_mul_f32 v234, s92, v234\n" \
"v_mfma_f32_16x16x32_bf16 v[156:159], v[56:59], v[208:211], v[156:159]\n" \
"ds_read_b64_tr_b16 v[26:27], v233 offset:2560\n" \
"v_mul_f32 v235, s92, v235\n" \
"v_mfma_f32_16x16x32_bf16 v[156:159], v[60:63], v[212:215], v[156:159]\n" \
"global_load_lds_dwordx4 v225, s[46:47] offset:1024\n" \
"v_mul_f32 v236, s92, v236\n" \
"v_mfma_f32_16x16x32_bf16 v[188:191], v[56:59], v[216:219], v[188:191]\n" \
"ds_read_b128 v[208:211], v230 offset:4096\n" \
"v_mul_f32 v237, s92, v237\n" \
"v_mfma_f32_16x16x32_bf16 v[188:191], v[60:63], v[220:223], v[188:191]\n" \
"ds_read_b128 v[212:215], v231 offset:4096\n" \
"v_cvt_pk_bf16_f32 v234, v234, v235\n" \
"v_mfma_f32_16x16x32_bf16 v[184:187], v[48:51], v[216:219], v[184:187]\n" \
"ds_read_b64_tr_b16 v[28:29], v233 offset:8704\n" \
"v_cvt_pk_bf16_f32 v236, v236, v237\n" \
"v_mfma_f32_16x16x32_bf16 v[184:187], v[52:55], v[220:223], v[184:187]\n" \
"global_load_lds_dwordx4 v226, s[46:47] offset:2048\n" \
"v_accvgpr_write_b32 a96, v234\n" \
"v_mfma_f32_16x16x32_bf16 v[180:183], v[40:43], v[216:219], v[180:183]\n" \
"ds_read_b64_tr_b16 v[30:31], v233 offset:10752\n" \
"v_accvgpr_write_b32 a97, v236\n" \
"v_mfma_f32_16x16x32_bf16 v[180:183], v[44:47], v[220:223], v[180:183]\n" \
"v_accvgpr_read_b32 v234, a100\n" \
"v_accvgpr_read_b32 v235, a101\n" \
"v_mfma_f32_16x16x32_bf16 v[176:179], v[32:35], v[216:219], v[176:179]\n" \
"v_accvgpr_read_b32 v236, a102\n" \
"v_accvgpr_read_b32 v237, a103\n" \
"v_mfma_f32_16x16x32_bf16 v[176:179], v[36:39], v[220:223], v[176:179]\n" \
"s_waitcnt lgkmcnt(1)\n" \
"global_load_lds_dwordx4 v227, s[46:47] offset:3072\n" \
"v_mul_f32 v234, s92, v234\n" \
"v_mfma_f32_16x16x32_bf16 v[64:67], v[0:3], v[192:195], v[64:67]\n" \
"s_add_u32 s46, s46, 0x80\n" \
"s_addc_u32 s47, s47, 0\n" \
"ds_read_b128 v[216:219], v230 offset:6144\n" \
"v_mfma_f32_16x16x32_bf16 v[64:67], v[4:7], v[196:199], v[64:67]\n" \
"ds_read_b128 v[220:223], v231 offset:6144\n" \
"v_mul_f32 v235, s92, v235\n" \
"v_mfma_f32_16x16x32_bf16 v[68:71], v[8:11], v[192:195], v[68:71]\n" \
"ds_read_b64_tr_b16 v[32:33], v232 offset:1024\n" \
"v_mul_f32 v236, s92, v236\n" \
"v_mfma_f32_16x16x32_bf16 v[68:71], v[12:15], v[196:199], v[68:71]\n" \
"buffer_store_dwordx4 a[212:215], v248, s[56:59], s90 offen offset:1024 sc1\n" \
"v_mul_f32 v237, s92, v237\n" \
"v_cvt_pk_bf16_f32 v234, v234, v235\n" \
"v_cvt_pk_bf16_f32 v236, v236, v237\n" \
"v_accvgpr_write_b32 a98, v234\n" \
"v_mfma_f32_16x16x32_bf16 v[72:75], v[16:19], v[192:195], v[72:75]\n" \
"ds_read_b64_tr_b16 v[34:35], v232 offset:3072\n" \
"v_accvgpr_write_b32 a99, v236\n" \
"v_mfma_f32_16x16x32_bf16 v[72:75], v[20:23], v[196:199], v[72:75]\n" \
"s_waitcnt lgkmcnt(3)\n" \
"ds_read_b64_tr_b16 v[36:37], v232 offset:9216\n" \
"v_accvgpr_read_b32 v234, a72\n" \
"v_mfma_f32_16x16x32_bf16 v[76:79], v[24:27], v[192:195], v[76:79]\n" \
"ds_read_b64_tr_b16 v[38:39], v232 offset:11264\n" \
"v_accvgpr_read_b32 v235, a73\n" \
"v_mfma_f32_16x16x32_bf16 v[76:79], v[28:31], v[196:199], v[76:79]\n" \
"buffer_store_dwordx4 a[216:219], v248, s[56:59], s90 offen offset:2048 sc1\n" \
"v_accvgpr_read_b32 v236, a74\n" \
"v_accvgpr_read_b32 v237, a75\n" \
"v_mul_f32 v234, s92, v234\n" \
"v_mul_f32 v235, s92, v235\n" \
"v_mfma_f32_16x16x32_bf16 v[108:111], v[24:27], v[200:203], v[108:111]\n" \
"ds_read_b64_tr_b16 v[40:41], v233 offset:1024\n" \
"v_mul_f32 v236, s92, v236\n" \
"v_mfma_f32_16x16x32_bf16 v[108:111], v[28:31], v[204:207], v[108:111]\n" \
"ds_read_b64_tr_b16 v[42:43], v233 offset:3072\n" \
"v_mul_f32 v237, s92, v237\n" \
"v_mfma_f32_16x16x32_bf16 v[104:107], v[16:19], v[200:203], v[104:107]\n" \
"ds_read_b64_tr_b16 v[44:45], v233 offset:9216\n" \
"v_cvt_pk_bf16_f32 v234, v234, v235\n" \
"v_mfma_f32_16x16x32_bf16 v[104:107], v[20:23], v[204:207], v[104:107]\n" \
"v_cvt_pk_bf16_f32 v236, v236, v237\n" \
"v_accvgpr_write_b32 a72, v234\n" \
"v_mfma_f32_16x16x32_bf16 v[100:103], v[8:11], v[200:203], v[100:103]\n" \
"ds_read_b64_tr_b16 v[46:47], v233 offset:11264\n" \
"v_accvgpr_write_b32 a73, v236\n" \
"v_mfma_f32_16x16x32_bf16 v[100:103], v[12:15], v[204:207], v[100:103]\n" \
"ds_read_b64_tr_b16 v[48:49], v232 offset:1536\n" \
"v_accvgpr_read_b32 v234, a76\n" \
"v_mfma_f32_16x16x32_bf16 v[96:99], v[0:3], v[200:203], v[96:99]\n" \
"s_add_u32 m0, s95, 53248\n" \
"ds_read_b64_tr_b16 v[50:51], v232 offset:3584\n" \
"v_accvgpr_read_b32 v235, a77\n" \
"v_mfma_f32_16x16x32_bf16 v[96:99], v[4:7], v[204:207], v[96:99]\n" \
"global_load_lds_dwordx4 v228, s[44:45] offset:-4096\n" \
"v_accvgpr_read_b32 v236, a78\n" \
"v_mfma_f32_16x16x32_bf16 v[128:131], v[0:3], v[208:211], v[128:131]\n" \
"ds_read_b64_tr_b16 v[52:53], v232 offset:9728\n" \
"v_accvgpr_read_b32 v237, a79\n" \
"v_mfma_f32_16x16x32_bf16 v[128:131], v[4:7], v[212:215], v[128:131]\n" \
"ds_read_b64_tr_b16 v[54:55], v232 offset:11776\n" \
"v_mul_f32 v234, s92, v234\n" \
"v_mfma_f32_16x16x32_bf16 v[132:135], v[8:11], v[208:211], v[132:135]\n" \
"ds_read_b64_tr_b16 v[56:57], v233 offset:1536\n" \
"v_mul_f32 v235, s92, v235\n" \
"v_mfma_f32_16x16x32_bf16 v[132:135], v[12:15], v[212:215], v[132:135]\n" \
"global_load_lds_dwordx4 v229, s[44:45] offset:-3072\n" \
"v_mul_f32 v236, s92, v236\n" \
"v_mfma_f32_16x16x32_bf16 v[136:139], v[16:19], v[208:211], v[136:139]\n" \
"s_add_u32 s44, s44, 0x80000\n" \
"s_addc_u32 s45, s45, 0\n" \
"ds_read_b64_tr_b16 v[58:59], v233 offset:3584\n" \
"v_mfma_f32_16x16x32_bf16 v[136:139], v[20:23], v[212:215], v[136:139]\n" \
"ds_read_b64_tr_b16 v[60:61], v233 offset:9728\n" \
"v_mul_f32 v237, s92, v237\n" \
"v_mfma_f32_16x16x32_bf16 v[140:143], v[24:27], v[208:211], v[140:143]\n" \
"ds_read_b64_tr_b16 v[62:63], v233 offset:11776\n" \
"v_cvt_pk_bf16_f32 v234, v234, v235\n" \
"v_mfma_f32_16x16x32_bf16 v[140:143], v[28:31], v[212:215], v[140:143]\n" \
"s_waitcnt lgkmcnt(3)\n" \
"global_load_lds_dwordx4 v228, s[42:43] offset:-2048\n" \
"v_cvt_pk_bf16_f32 v236, v236, v237\n" \
"v_mfma_f32_16x16x32_bf16 v[172:175], v[24:27], v[216:219], v[172:175]\n" \
"v_add_u32 v230, 0x8000, v230\n" \
"v_add_u32 v231, 0x8000, v231\n" \
"v_mfma_f32_16x16x32_bf16 v[172:175], v[28:31], v[220:223], v[172:175]\n" \
"v_add_u32 v232, 0x4000, v232\n" \
"v_add_u32 v233, 0x4000, v233\n" \
"v_mfma_f32_16x16x32_bf16 v[168:171], v[16:19], v[216:219], v[168:171]\n" \
"v_accvgpr_write_b32 a74, v234\n" \
"v_accvgpr_write_b32 a75, v236\n" \
"v_mfma_f32_16x16x32_bf16 v[168:171], v[20:23], v[220:223], v[168:171]\n" \
"global_load_lds_dwordx4 v229, s[42:43] offset:-1024\n" \
"v_accvgpr_read_b32 v234, a104\n" \
"v_mfma_f32_16x16x32_bf16 v[164:167], v[8:11], v[216:219], v[164:167]\n" \
"s_add_u32 s42, s42, 0x80000\n" \
"s_addc_u32 s43, s43, 0\n" \
"v_accvgpr_read_b32 v235, a105\n" \
"v_mfma_f32_16x16x32_bf16 v[164:167], v[12:15], v[220:223], v[164:167]\n" \
"s_waitcnt vmcnt(19) lgkmcnt(0)\n" \
"v_accvgpr_read_b32 v236, a106\n" \
"v_accvgpr_read_b32 v237, a107\n" \
"v_mfma_f32_16x16x32_bf16 v[160:163], v[0:3], v[216:219], v[160:163]\n" \
"v_mul_f32 v234, s92, v234\n" \
"v_mul_f32 v235, s92, v235\n" \
"v_mfma_f32_16x16x32_bf16 v[160:163], v[4:7], v[220:223], v[160:163]\n" \
"s_barrier\n" \
"v_mfma_f32_16x16x32_bf16 v[80:83], v[32:35], v[192:195], v[80:83]\n" \
"ds_read_b64_tr_b16 v[0:1], v232 offset:0\n" \
"v_mul_f32 v236, s92, v236\n" \
"v_mfma_f32_16x16x32_bf16 v[80:83], v[36:39], v[196:199], v[80:83]\n" \
"ds_read_b64_tr_b16 v[2:3], v232 offset:2048\n" \
"v_mul_f32 v237, s92, v237\n" \
"v_mfma_f32_16x16x32_bf16 v[84:87], v[40:43], v[192:195], v[84:87]\n" \
"s_add_u32 m0, s87, 69632\n" \
"ds_read_b64_tr_b16 v[4:5], v232 offset:8192\n" \
"v_cvt_pk_bf16_f32 v234, v234, v235\n" \
"v_mfma_f32_16x16x32_bf16 v[84:87], v[44:47], v[196:199], v[84:87]\n" \
"global_load_lds_dwordx4 v224, s[48:49] offset:-4096\n" \
"v_cvt_pk_bf16_f32 v236, v236, v237\n" \
"v_mfma_f32_16x16x32_bf16 v[88:91], v[48:51], v[192:195], v[88:91]\n" \
"ds_read_b64_tr_b16 v[6:7], v232 offset:10240\n" \
"v_accvgpr_write_b32 a104, v234\n" \
"v_mfma_f32_16x16x32_bf16 v[88:91], v[52:55], v[196:199], v[88:91]\n" \
"ds_read_b64_tr_b16 v[8:9], v233 offset:0\n" \
"v_accvgpr_write_b32 a105, v236\n" \
"v_mfma_f32_16x16x32_bf16 v[92:95], v[56:59], v[192:195], v[92:95]\n" \
"ds_read_b64_tr_b16 v[10:11], v233 offset:2048\n" \
"v_accvgpr_read_b32 v234, a108\n" \
"v_mfma_f32_16x16x32_bf16 v[92:95], v[60:63], v[196:199], v[92:95]\n" \
"global_load_lds_dwordx4 v225, s[48:49] offset:-3072\n" \
"v_accvgpr_read_b32 v235, a109\n" \
"v_mfma_f32_16x16x32_bf16 v[124:127], v[56:59], v[200:203], v[124:127]\n" \
"ds_read_b128 v[192:195], v230 offset:0\n" \
"v_accvgpr_read_b32 v236, a110\n" \
"v_mfma_f32_16x16x32_bf16 v[124:127], v[60:63], v[204:207], v[124:127]\n" \
"ds_read_b128 v[196:199], v231 offset:0\n" \
"v_accvgpr_read_b32 v237, a111\n" \
"v_mfma_f32_16x16x32_bf16 v[120:123], v[48:51], v[200:203], v[120:123]\n" \
"ds_read_b64_tr_b16 v[12:13], v233 offset:8192\n" \
"v_mul_f32 v234, s92, v234\n" \
"v_mfma_f32_16x16x32_bf16 v[120:123], v[52:55], v[204:207], v[120:123]\n" \
"global_load_lds_dwordx4 v226, s[48:49] offset:-2048\n" \
"v_mul_f32 v235, s92, v235\n" \
"v_mfma_f32_16x16x32_bf16 v[116:119], v[40:43], v[200:203], v[116:119]\n" \
"ds_read_b64_tr_b16 v[14:15], v233 offset:10240\n" \
"v_mul_f32 v236, s92, v236\n" \
"v_mfma_f32_16x16x32_bf16 v[116:119], v[44:47], v[204:207], v[116:119]\n" \
"ds_read_b64_tr_b16 v[16:17], v232 offset:512\n" \
"v_mul_f32 v237, s92, v237\n" \
"v_mfma_f32_16x16x32_bf16 v[112:115], v[32:35], v[200:203], v[112:115]\n" \
"ds_read_b64_tr_b16 v[18:19], v232 offset:2560\n" \
"v_cvt_pk_bf16_f32 v234, v234, v235\n" \
"v_mfma_f32_16x16x32_bf16 v[112:115], v[36:39], v[204:207], v[112:115]\n" \
"global_load_lds_dwordx4 v227, s[48:49] offset:-1024\n" \
"v_cvt_pk_bf16_f32 v236, v236, v237\n" \
"v_mfma_f32_16x16x32_bf16 v[144:147], v[32:35], v[208:211], v[144:147]\n" \
"s_add_u32 s48, s48, 0x80\n" \
"s_addc_u32 s49, s49, 0\n" \
"ds_read_b128 v[200:203], v230 offset:2048\n" \
"v_mfma_f32_16x16x32_bf16 v[144:147], v[36:39], v[212:215], v[144:147]\n" \
"ds_read_b128 v[204:207], v231 offset:2048\n" \
"v_accvgpr_write_b32 a106, v234\n" \
"v_mfma_f32_16x16x32_bf16 v[148:151], v[40:43], v[208:211], v[148:151]\n" \
"ds_read_b64_tr_b16 v[20:21], v232 offset:8704\n" \
"v_accvgpr_write_b32 a107, v236\n" \
"v_mfma_f32_16x16x32_bf16 v[148:151], v[44:47], v[212:215], v[148:151]\n" \
"global_load_lds_dwordx4 v224, s[46:47] offset:0\n" \
"v_accvgpr_read_b32 v234, a80\n" \
"v_mfma_f32_16x16x32_bf16 v[152:155], v[48:51], v[208:211], v[152:155]\n" \
"ds_read_b64_tr_b16 v[22:23], v232 offset:10752\n" \
"v_accvgpr_read_b32 v235, a81\n" \
"v_mfma_f32_16x16x32_bf16 v[152:155], v[52:55], v[212:215], v[152:155]\n" \
"ds_read_b64_tr_b16 v[24:25], v233 offset:512\n" \
"v_accvgpr_read_b32 v236, a82\n" \
"v_mfma_f32_16x16x32_bf16 v[156:159], v[56:59], v[208:211], v[156:159]\n" \
"ds_read_b64_tr_b16 v[26:27], v233 offset:2560\n" \
"v_accvgpr_read_b32 v237, a83\n" \
"v_mfma_f32_16x16x32_bf16 v[156:159], v[60:63], v[212:215], v[156:159]\n" \
"global_load_lds_dwordx4 v225, s[46:47] offset:1024\n" \
"v_cvt_pk_bf16_f32 v234, v234, v235\n" \
"v_mfma_f32_16x16x32_bf16 v[188:191], v[56:59], v[216:219], v[188:191]\n" \
"ds_read_b128 v[208:211], v230 offset:4096\n" \
"v_cvt_pk_bf16_f32 v236, v236, v237\n" \
"v_mfma_f32_16x16x32_bf16 v[188:191], v[60:63], v[220:223], v[188:191]\n" \
"ds_read_b128 v[212:215], v231 offset:4096\n" \
"v_accvgpr_write_b32 a80, v234\n" \
"v_mfma_f32_16x16x32_bf16 v[184:187], v[48:51], v[216:219], v[184:187]\n" \
"ds_read_b64_tr_b16 v[28:29], v233 offset:8704\n" \
"v_accvgpr_write_b32 a81, v236\n" \
"v_mfma_f32_16x16x32_bf16 v[184:187], v[52:55], v[220:223], v[184:187]\n" \
"global_load_lds_dwordx4 v226, s[46:47] offset:2048\n" \
"v_accvgpr_read_b32 v234, a112\n" \
"v_mfma_f32_16x16x32_bf16 v[180:183], v[40:43], v[216:219], v[180:183]\n" \
"ds_read_b64_tr_b16 v[30:31], v233 offset:10752\n" \
"v_accvgpr_read_b32 v235, a113\n" \
"v_mfma_f32_16x16x32_bf16 v[180:183], v[44:47], v[220:223], v[180:183]\n" \
"v_accvgpr_read_b32 v236, a114\n" \
"v_accvgpr_read_b32 v237, a115\n" \
"v_mfma_f32_16x16x32_bf16 v[176:179], v[32:35], v[216:219], v[176:179]\n" \
"v_cvt_pk_bf16_f32 v234, v234, v235\n" \
"v_cvt_pk_bf16_f32 v236, v236, v237\n" \
"v_mfma_f32_16x16x32_bf16 v[176:179], v[36:39], v[220:223], v[176:179]\n" \
"s_waitcnt lgkmcnt(1)\n" \
"global_load_lds_dwordx4 v227, s[46:47] offset:3072\n" \
"v_accvgpr_write_b32 a82, v234\n" \
"v_mfma_f32_16x16x32_bf16 v[64:67], v[0:3], v[192:195], v[64:67]\n" \
"s_add_u32 s46, s46, 0x80\n" \
"s_addc_u32 s47, s47, 0\n" \
"ds_read_b128 v[216:219], v230 offset:6144\n" \
"v_mfma_f32_16x16x32_bf16 v[64:67], v[4:7], v[196:199], v[64:67]\n" \
"ds_read_b128 v[220:223], v231 offset:6144\n" \
"v_accvgpr_write_b32 a83, v236\n" \
"v_mfma_f32_16x16x32_bf16 v[68:71], v[8:11], v[192:195], v[68:71]\n" \
"ds_read_b64_tr_b16 v[32:33], v232 offset:1024\n" \
"v_accvgpr_read_b32 v234, a84\n" \
"v_mfma_f32_16x16x32_bf16 v[68:71], v[12:15], v[196:199], v[68:71]\n" \
"buffer_store_dwordx4 a[220:223], v248, s[56:59], s90 offen offset:3072 sc1\n" \
"v_accvgpr_read_b32 v235, a85\n" \
"v_accvgpr_read_b32 v236, a86\n" \
"v_accvgpr_read_b32 v237, a87\n" \
"v_cvt_pk_bf16_f32 v234, v234, v235\n" \
"v_mfma_f32_16x16x32_bf16 v[72:75], v[16:19], v[192:195], v[72:75]\n" \
"ds_read_b64_tr_b16 v[34:35], v232 offset:3072\n" \
"v_cvt_pk_bf16_f32 v236, v236, v237\n" \
"v_mfma_f32_16x16x32_bf16 v[72:75], v[20:23], v[196:199], v[72:75]\n" \
"s_waitcnt lgkmcnt(3)\n" \
"ds_read_b64_tr_b16 v[36:37], v232 offset:9216\n" \
"v_accvgpr_write_b32 a84, v234\n" \
"v_mfma_f32_16x16x32_bf16 v[76:79], v[24:27], v[192:195], v[76:79]\n" \
"ds_read_b64_tr_b16 v[38:39], v232 offset:11264\n" \
"v_accvgpr_write_b32 a85, v236\n" \
"v_mfma_f32_16x16x32_bf16 v[76:79], v[28:31], v[196:199], v[76:79]\n" \
"v_accvgpr_read_b32 v234, a116\n" \
"v_accvgpr_read_b32 v235, a117\n" \
"v_mfma_f32_16x16x32_bf16 v[108:111], v[24:27], v[200:203], v[108:111]\n" \
"ds_read_b64_tr_b16 v[40:41], v233 offset:1024\n" \
"v_accvgpr_read_b32 v236, a118\n" \
"v_mfma_f32_16x16x32_bf16 v[108:111], v[28:31], v[204:207], v[108:111]\n" \
"ds_read_b64_tr_b16 v[42:43], v233 offset:3072\n" \
"v_accvgpr_read_b32 v237, a119\n" \
"v_mfma_f32_16x16x32_bf16 v[104:107], v[16:19], v[200:203], v[104:107]\n" \
"ds_read_b64_tr_b16 v[44:45], v233 offset:9216\n" \
"v_cvt_pk_bf16_f32 v234, v234, v235\n" \
"v_mfma_f32_16x16x32_bf16 v[104:107], v[20:23], v[204:207], v[104:107]\n" \
"v_cvt_pk_bf16_f32 v236, v236, v237\n" \
"v_accvgpr_write_b32 a86, v234\n" \
"v_mfma_f32_16x16x32_bf16 v[100:103], v[8:11], v[200:203], v[100:103]\n" \
"ds_read_b64_tr_b16 v[46:47], v233 offset:11264\n" \
"v_accvgpr_write_b32 a87, v236\n" \
"v_mfma_f32_16x16x32_bf16 v[100:103], v[12:15], v[204:207], v[100:103]\n" \
"ds_read_b64_tr_b16 v[48:49], v232 offset:1536\n" \
"v_accvgpr_read_b32 v234, a88\n" \
"v_mfma_f32_16x16x32_bf16 v[96:99], v[0:3], v[200:203], v[96:99]\n" \
"s_add_u32 m0, s95, 4096\n" \
"ds_read_b64_tr_b16 v[50:51], v232 offset:3584\n" \
"v_accvgpr_read_b32 v235, a89\n" \
"v_mfma_f32_16x16x32_bf16 v[96:99], v[4:7], v[204:207], v[96:99]\n" \
"global_load_lds_dwordx4 v228, s[44:45] offset:-4096\n" \
"v_accvgpr_read_b32 v236, a90\n" \
"v_mfma_f32_16x16x32_bf16 v[128:131], v[0:3], v[208:211], v[128:131]\n" \
"ds_read_b64_tr_b16 v[52:53], v232 offset:9728\n" \
"v_accvgpr_read_b32 v237, a91\n" \
"v_mfma_f32_16x16x32_bf16 v[128:131], v[4:7], v[212:215], v[128:131]\n" \
"ds_read_b64_tr_b16 v[54:55], v232 offset:11776\n" \
"v_cvt_pk_bf16_f32 v234, v234, v235\n" \
"v_mfma_f32_16x16x32_bf16 v[132:135], v[8:11], v[208:211], v[132:135]\n" \
"ds_read_b64_tr_b16 v[56:57], v233 offset:1536\n" \
"v_cvt_pk_bf16_f32 v236, v236, v237\n" \
"v_mfma_f32_16x16x32_bf16 v[132:135], v[12:15], v[212:215], v[132:135]\n" \
"global_load_lds_dwordx4 v229, s[44:45] offset:-3072\n" \
"v_accvgpr_write_b32 a88, v234\n" \
"v_mfma_f32_16x16x32_bf16 v[136:139], v[16:19], v[208:211], v[136:139]\n" \
"s_add_u32 s44, s44, 0x80000\n" \
"s_addc_u32 s45, s45, 0\n" \
"ds_read_b64_tr_b16 v[58:59], v233 offset:3584\n" \
"v_mfma_f32_16x16x32_bf16 v[136:139], v[20:23], v[212:215], v[136:139]\n" \
"ds_read_b64_tr_b16 v[60:61], v233 offset:9728\n" \
"v_accvgpr_write_b32 a89, v236\n" \
"v_mfma_f32_16x16x32_bf16 v[140:143], v[24:27], v[208:211], v[140:143]\n" \
"ds_read_b64_tr_b16 v[62:63], v233 offset:11776\n" \
"v_accvgpr_read_b32 v234, a120\n" \
"v_mfma_f32_16x16x32_bf16 v[140:143], v[28:31], v[212:215], v[140:143]\n" \
"s_waitcnt lgkmcnt(3)\n" \
"global_load_lds_dwordx4 v228, s[42:43] offset:-2048\n" \
"v_accvgpr_read_b32 v235, a121\n" \
"v_mfma_f32_16x16x32_bf16 v[172:175], v[24:27], v[216:219], v[172:175]\n" \
"v_add_u32 v230, 0x8000, v230\n" \
"v_add_u32 v231, 0x8000, v231\n" \
"v_mfma_f32_16x16x32_bf16 v[172:175], v[28:31], v[220:223], v[172:175]\n" \
"v_add_u32 v232, 0x4000, v232\n" \
"v_add_u32 v233, 0x4000, v233\n" \
"v_mfma_f32_16x16x32_bf16 v[168:171], v[16:19], v[216:219], v[168:171]\n" \
"v_accvgpr_read_b32 v236, a122\n" \
"v_accvgpr_read_b32 v237, a123\n" \
"v_mfma_f32_16x16x32_bf16 v[168:171], v[20:23], v[220:223], v[168:171]\n" \
"global_load_lds_dwordx4 v229, s[42:43] offset:-1024\n" \
"v_cvt_pk_bf16_f32 v234, v234, v235\n" \
"v_mfma_f32_16x16x32_bf16 v[164:167], v[8:11], v[216:219], v[164:167]\n" \
"s_add_u32 s42, s42, 0x80000\n" \
"s_addc_u32 s43, s43, 0\n" \
"v_cvt_pk_bf16_f32 v236, v236, v237\n" \
"v_mfma_f32_16x16x32_bf16 v[164:167], v[12:15], v[220:223], v[164:167]\n" \
"s_waitcnt vmcnt(19) lgkmcnt(0)\n" \
"v_accvgpr_write_b32 a90, v234\n" \
"v_accvgpr_write_b32 a91, v236\n" \
"v_mfma_f32_16x16x32_bf16 v[160:163], v[0:3], v[216:219], v[160:163]\n" \
"v_accvgpr_read_b32 v234, a92\n" \
"v_accvgpr_read_b32 v235, a93\n" \
"v_mfma_f32_16x16x32_bf16 v[160:163], v[4:7], v[220:223], v[160:163]\n" \
"s_barrier\n" \
"v_mfma_f32_16x16x32_bf16 v[80:83], v[32:35], v[192:195], v[80:83]\n" \
"ds_read_b64_tr_b16 v[0:1], v232 offset:0\n" \
"v_accvgpr_read_b32 v236, a94\n" \
"v_mfma_f32_16x16x32_bf16 v[80:83], v[36:39], v[196:199], v[80:83]\n" \
"ds_read_b64_tr_b16 v[2:3], v232 offset:2048\n" \
"v_accvgpr_read_b32 v237, a95\n" \
"v_mfma_f32_16x16x32_bf16 v[84:87], v[40:43], v[192:195], v[84:87]\n" \
"s_add_u32 m0, s87, 102400\n" \
"ds_read_b64_tr_b16 v[4:5], v232 offset:8192\n" \
"v_cvt_pk_bf16_f32 v234, v234, v235\n" \
"v_mfma_f32_16x16x32_bf16 v[84:87], v[44:47], v[196:199], v[84:87]\n" \
"global_load_lds_dwordx4 v224, s[48:49] offset:-4096\n" \
"v_cvt_pk_bf16_f32 v236, v236, v237\n" \
"v_mfma_f32_16x16x32_bf16 v[88:91], v[48:51], v[192:195], v[88:91]\n" \
"ds_read_b64_tr_b16 v[6:7], v232 offset:10240\n" \
"v_accvgpr_write_b32 a92, v234\n" \
"v_mfma_f32_16x16x32_bf16 v[88:91], v[52:55], v[196:199], v[88:91]\n" \
"ds_read_b64_tr_b16 v[8:9], v233 offset:0\n" \
"v_accvgpr_write_b32 a93, v236\n" \
"Lpg3308_%=:\n" \
"s_cbranch_execz Lpg5386_%=\n" \
"v_mfma_f32_16x16x32_bf16 v[92:95], v[56:59], v[192:195], v[92:95]\n" \
"ds_read_b64_tr_b16 v[10:11], v233 offset:2048\n" \
"v_accvgpr_read_b32 v234, a124\n" \
"v_mfma_f32_16x16x32_bf16 v[92:95], v[60:63], v[196:199], v[92:95]\n" \
"global_load_lds_dwordx4 v225, s[48:49] offset:-3072\n" \
"v_accvgpr_read_b32 v235, a125\n" \
"v_mfma_f32_16x16x32_bf16 v[124:127], v[56:59], v[200:203], v[124:127]\n" \
"ds_read_b128 v[192:195], v230 offset:0\n" \
"v_accvgpr_read_b32 v236, a126\n" \
"v_mfma_f32_16x16x32_bf16 v[124:127], v[60:63], v[204:207], v[124:127]\n" \
"ds_read_b128 v[196:199], v231 offset:0\n" \
"v_accvgpr_read_b32 v237, a127\n" \
"v_mfma_f32_16x16x32_bf16 v[120:123], v[48:51], v[200:203], v[120:123]\n" \
"ds_read_b64_tr_b16 v[12:13], v233 offset:8192\n" \
"v_cvt_pk_bf16_f32 v234, v234, v235\n" \
"v_mfma_f32_16x16x32_bf16 v[120:123], v[52:55], v[204:207], v[120:123]\n" \
"global_load_lds_dwordx4 v226, s[48:49] offset:-2048\n" \
"v_cvt_pk_bf16_f32 v236, v236, v237\n" \
"v_mfma_f32_16x16x32_bf16 v[116:119], v[40:43], v[200:203], v[116:119]\n" \
"ds_read_b64_tr_b16 v[14:15], v233 offset:10240\n" \
"v_accvgpr_write_b32 a94, v234\n" \
"v_mfma_f32_16x16x32_bf16 v[116:119], v[44:47], v[204:207], v[116:119]\n" \
"ds_read_b64_tr_b16 v[16:17], v232 offset:512\n" \
"v_accvgpr_write_b32 a95, v236\n" \
"v_mfma_f32_16x16x32_bf16 v[112:115], v[32:35], v[200:203], v[112:115]\n" \
"ds_read_b64_tr_b16 v[18:19], v232 offset:2560\n" \
"v_accvgpr_read_b32 v234, a0\n" \
"v_mfma_f32_16x16x32_bf16 v[112:115], v[36:39], v[204:207], v[112:115]\n" \
"global_load_lds_dwordx4 v227, s[48:49] offset:-1024\n" \
"v_accvgpr_read_b32 v235, a1\n" \
"v_mfma_f32_16x16x32_bf16 v[144:147], v[32:35], v[208:211], v[144:147]\n" \
"s_add_u32 s48, s48, 0x80\n" \
"s_addc_u32 s49, s49, 0\n" \
"ds_read_b128 v[200:203], v230 offset:2048\n" \
"v_mfma_f32_16x16x32_bf16 v[144:147], v[36:39], v[212:215], v[144:147]\n" \
"ds_read_b128 v[204:207], v231 offset:2048\n" \
"v_accvgpr_read_b32 v236, a2\n" \
"v_mfma_f32_16x16x32_bf16 v[148:151], v[40:43], v[208:211], v[148:151]\n" \
"ds_read_b64_tr_b16 v[20:21], v232 offset:8704\n" \
"v_accvgpr_read_b32 v237, a3\n" \
"v_mfma_f32_16x16x32_bf16 v[148:151], v[44:47], v[212:215], v[148:151]\n" \
"global_load_lds_dwordx4 v224, s[46:47] offset:0\n" \
"v_mul_f32 v234, s92, v234\n" \
"v_mfma_f32_16x16x32_bf16 v[152:155], v[48:51], v[208:211], v[152:155]\n" \
"ds_read_b64_tr_b16 v[22:23], v232 offset:10752\n" \
"v_mul_f32 v235, s92, v235\n" \
"v_mfma_f32_16x16x32_bf16 v[152:155], v[52:55], v[212:215], v[152:155]\n" \
"ds_read_b64_tr_b16 v[24:25], v233 offset:512\n" \
"v_mul_f32 v236, s92, v236\n" \
"v_mfma_f32_16x16x32_bf16 v[156:159], v[56:59], v[208:211], v[156:159]\n" \
"ds_read_b64_tr_b16 v[26:27], v233 offset:2560\n" \
"v_mul_f32 v237, s92, v237\n" \
"v_mfma_f32_16x16x32_bf16 v[156:159], v[60:63], v[212:215], v[156:159]\n" \
"global_load_lds_dwordx4 v225, s[46:47] offset:1024\n" \
"v_cvt_pk_bf16_f32 v234, v234, v235\n" \
"v_mfma_f32_16x16x32_bf16 v[188:191], v[56:59], v[216:219], v[188:191]\n" \
"ds_read_b128 v[208:211], v230 offset:4096\n" \
"v_cvt_pk_bf16_f32 v236, v236, v237\n" \
"v_mfma_f32_16x16x32_bf16 v[188:191], v[60:63], v[220:223], v[188:191]\n" \
"ds_read_b128 v[212:215], v231 offset:4096\n" \
"v_accvgpr_write_b32 a0, v234\n" \
"v_mfma_f32_16x16x32_bf16 v[184:187], v[48:51], v[216:219], v[184:187]\n" \
"ds_read_b64_tr_b16 v[28:29], v233 offset:8704\n" \
"v_accvgpr_write_b32 a1, v236\n" \
"v_mfma_f32_16x16x32_bf16 v[184:187], v[52:55], v[220:223], v[184:187]\n" \
"global_load_lds_dwordx4 v226, s[46:47] offset:2048\n" \
"v_accvgpr_read_b32 v234, a4\n" \
"v_mfma_f32_16x16x32_bf16 v[180:183], v[40:43], v[216:219], v[180:183]\n" \
"ds_read_b64_tr_b16 v[30:31], v233 offset:10752\n" \
"v_accvgpr_read_b32 v235, a5\n" \
"v_mfma_f32_16x16x32_bf16 v[180:183], v[44:47], v[220:223], v[180:183]\n" \
"v_accvgpr_read_b32 v236, a6\n" \
"v_accvgpr_read_b32 v237, a7\n" \
"v_mfma_f32_16x16x32_bf16 v[176:179], v[32:35], v[216:219], v[176:179]\n" \
"v_mul_f32 v234, s92, v234\n" \
"v_mul_f32 v235, s92, v235\n" \
"v_mfma_f32_16x16x32_bf16 v[176:179], v[36:39], v[220:223], v[176:179]\n" \
"s_waitcnt lgkmcnt(1)\n" \
"global_load_lds_dwordx4 v227, s[46:47] offset:3072\n" \
"v_mul_f32 v236, s92, v236\n" \
"v_mfma_f32_16x16x32_bf16 v[64:67], v[0:3], v[192:195], v[64:67]\n" \
"s_add_u32 s46, s46, 0x80\n" \
"s_addc_u32 s47, s47, 0\n" \
"ds_read_b128 v[216:219], v230 offset:6144\n" \
"v_mfma_f32_16x16x32_bf16 v[64:67], v[4:7], v[196:199], v[64:67]\n" \
"s_add_u32 s90, s88, 139264\n" \
"ds_read_b128 v[220:223], v231 offset:6144\n" \
"v_mul_f32 v237, s92, v237\n" \
"v_mfma_f32_16x16x32_bf16 v[68:71], v[8:11], v[192:195], v[68:71]\n" \
"ds_read_b64_tr_b16 v[32:33], v232 offset:1024\n" \
"v_cvt_pk_bf16_f32 v234, v234, v235\n" \
"v_mfma_f32_16x16x32_bf16 v[68:71], v[12:15], v[196:199], v[68:71]\n" \
"buffer_store_dwordx4 a[128:131], v249, s[56:59], s90 offen offset:0 sc1\n" \
"v_cvt_pk_bf16_f32 v236, v236, v237\n" \
"v_accvgpr_write_b32 a2, v234\n" \
"v_accvgpr_write_b32 a3, v236\n" \
"v_accvgpr_read_b32 v234, a32\n" \
"v_mfma_f32_16x16x32_bf16 v[72:75], v[16:19], v[192:195], v[72:75]\n" \
"ds_read_b64_tr_b16 v[34:35], v232 offset:3072\n" \
"v_accvgpr_read_b32 v235, a33\n" \
"v_mfma_f32_16x16x32_bf16 v[72:75], v[20:23], v[196:199], v[72:75]\n" \
"s_waitcnt lgkmcnt(3)\n" \
"ds_read_b64_tr_b16 v[36:37], v232 offset:9216\n" \
"v_accvgpr_read_b32 v236, a34\n" \
"v_mfma_f32_16x16x32_bf16 v[76:79], v[24:27], v[192:195], v[76:79]\n" \
"ds_read_b64_tr_b16 v[38:39], v232 offset:11264\n" \
"v_accvgpr_read_b32 v237, a35\n" \
"v_mfma_f32_16x16x32_bf16 v[76:79], v[28:31], v[196:199], v[76:79]\n" \
"v_mul_f32 v234, s92, v234\n" \
"v_mul_f32 v235, s92, v235\n" \
"v_mfma_f32_16x16x32_bf16 v[108:111], v[24:27], v[200:203], v[108:111]\n" \
"ds_read_b64_tr_b16 v[40:41], v233 offset:1024\n" \
"v_mul_f32 v236, s92, v236\n" \
"v_mfma_f32_16x16x32_bf16 v[108:111], v[28:31], v[204:207], v[108:111]\n" \
"ds_read_b64_tr_b16 v[42:43], v233 offset:3072\n" \
"v_mul_f32 v237, s92, v237\n" \
"v_mfma_f32_16x16x32_bf16 v[104:107], v[16:19], v[200:203], v[104:107]\n" \
"ds_read_b64_tr_b16 v[44:45], v233 offset:9216\n" \
"v_cvt_pk_bf16_f32 v234, v234, v235\n" \
"v_mfma_f32_16x16x32_bf16 v[104:107], v[20:23], v[204:207], v[104:107]\n" \
"v_cvt_pk_bf16_f32 v236, v236, v237\n" \
"v_accvgpr_write_b32 a32, v234\n" \
"v_mfma_f32_16x16x32_bf16 v[100:103], v[8:11], v[200:203], v[100:103]\n" \
"ds_read_b64_tr_b16 v[46:47], v233 offset:11264\n" \
"v_accvgpr_write_b32 a33, v236\n" \
"v_mfma_f32_16x16x32_bf16 v[100:103], v[12:15], v[204:207], v[100:103]\n" \
"ds_read_b64_tr_b16 v[48:49], v232 offset:1536\n" \
"v_accvgpr_read_b32 v234, a36\n" \
"v_mfma_f32_16x16x32_bf16 v[96:99], v[0:3], v[200:203], v[96:99]\n" \
"s_add_u32 m0, s95, 20480\n" \
"ds_read_b64_tr_b16 v[50:51], v232 offset:3584\n" \
"v_accvgpr_read_b32 v235, a37\n" \
"v_mfma_f32_16x16x32_bf16 v[96:99], v[4:7], v[204:207], v[96:99]\n" \
"global_load_lds_dwordx4 v228, s[44:45] offset:-4096\n" \
"v_accvgpr_read_b32 v236, a38\n" \
"v_mfma_f32_16x16x32_bf16 v[128:131], v[0:3], v[208:211], v[128:131]\n" \
"ds_read_b64_tr_b16 v[52:53], v232 offset:9728\n" \
"v_accvgpr_read_b32 v237, a39\n" \
"v_mfma_f32_16x16x32_bf16 v[128:131], v[4:7], v[212:215], v[128:131]\n" \
"ds_read_b64_tr_b16 v[54:55], v232 offset:11776\n" \
"v_mul_f32 v234, s92, v234\n" \
"v_mfma_f32_16x16x32_bf16 v[132:135], v[8:11], v[208:211], v[132:135]\n" \
"ds_read_b64_tr_b16 v[56:57], v233 offset:1536\n" \
"v_mul_f32 v235, s92, v235\n" \
"v_mfma_f32_16x16x32_bf16 v[132:135], v[12:15], v[212:215], v[132:135]\n" \
"global_load_lds_dwordx4 v229, s[44:45] offset:-3072\n" \
"v_mul_f32 v236, s92, v236\n" \
"v_mfma_f32_16x16x32_bf16 v[136:139], v[16:19], v[208:211], v[136:139]\n" \
"s_add_u32 s44, s44, 0x80000\n" \
"s_addc_u32 s45, s45, 0\n" \
"ds_read_b64_tr_b16 v[58:59], v233 offset:3584\n" \
"v_mfma_f32_16x16x32_bf16 v[136:139], v[20:23], v[212:215], v[136:139]\n" \
"ds_read_b64_tr_b16 v[60:61], v233 offset:9728\n" \
"v_mul_f32 v237, s92, v237\n" \
"v_mfma_f32_16x16x32_bf16 v[140:143], v[24:27], v[208:211], v[140:143]\n" \
"ds_read_b64_tr_b16 v[62:63], v233 offset:11776\n" \
"v_cvt_pk_bf16_f32 v234, v234, v235\n" \
"v_mfma_f32_16x16x32_bf16 v[140:143], v[28:31], v[212:215], v[140:143]\n" \
"s_waitcnt lgkmcnt(3)\n" \
"global_load_lds_dwordx4 v228, s[42:43] offset:-2048\n" \
"v_cvt_pk_bf16_f32 v236, v236, v237\n" \
"v_mfma_f32_16x16x32_bf16 v[172:175], v[24:27], v[216:219], v[172:175]\n" \
"v_add_u32 v230, 0xffff0000, v230\n" \
"v_add_u32 v231, 0xffff0000, v231\n" \
"v_mfma_f32_16x16x32_bf16 v[172:175], v[28:31], v[220:223], v[172:175]\n" \
"v_add_u32 v232, 0x4000, v232\n" \
"v_add_u32 v233, 0x4000, v233\n" \
"v_mfma_f32_16x16x32_bf16 v[168:171], v[16:19], v[216:219], v[168:171]\n" \
"v_accvgpr_write_b32 a34, v234\n" \
"v_accvgpr_write_b32 a35, v236\n" \
"v_mfma_f32_16x16x32_bf16 v[168:171], v[20:23], v[220:223], v[168:171]\n" \
"global_load_lds_dwordx4 v229, s[42:43] offset:-1024\n" \
"v_accvgpr_read_b32 v234, a8\n" \
"v_mfma_f32_16x16x32_bf16 v[164:167], v[8:11], v[216:219], v[164:167]\n" \
"s_add_u32 s42, s42, 0x80000\n" \
"s_addc_u32 s43, s43, 0\n" \
"v_accvgpr_read_b32 v235, a9\n" \
"v_mfma_f32_16x16x32_bf16 v[164:167], v[12:15], v[220:223], v[164:167]\n" \
"s_waitcnt vmcnt(18) lgkmcnt(0)\n" \
"v_accvgpr_read_b32 v236, a10\n" \
"v_accvgpr_read_b32 v237, a11\n" \
"v_mfma_f32_16x16x32_bf16 v[160:163], v[0:3], v[216:219], v[160:163]\n" \
"v_mul_f32 v234, s92, v234\n" \
"v_mul_f32 v235, s92, v235\n" \
"v_mfma_f32_16x16x32_bf16 v[160:163], v[4:7], v[220:223], v[160:163]\n" \
"s_barrier\n" \
"v_mfma_f32_16x16x32_bf16 v[80:83], v[32:35], v[192:195], v[80:83]\n" \
"ds_read_b64_tr_b16 v[0:1], v232 offset:0\n" \
"v_mul_f32 v236, s92, v236\n" \
"v_mfma_f32_16x16x32_bf16 v[80:83], v[36:39], v[196:199], v[80:83]\n" \
"ds_read_b64_tr_b16 v[2:3], v232 offset:2048\n" \
"v_mul_f32 v237, s92, v237\n" \
"v_mfma_f32_16x16x32_bf16 v[84:87], v[40:43], v[192:195], v[84:87]\n" \
"s_add_u32 m0, s87, 135168\n" \
"ds_read_b64_tr_b16 v[4:5], v232 offset:8192\n" \
"v_cvt_pk_bf16_f32 v234, v234, v235\n" \
"v_mfma_f32_16x16x32_bf16 v[84:87], v[44:47], v[196:199], v[84:87]\n" \
"global_load_lds_dwordx4 v224, s[48:49] offset:-4096\n" \
"v_cvt_pk_bf16_f32 v236, v236, v237\n" \
"v_mfma_f32_16x16x32_bf16 v[88:91], v[48:51], v[192:195], v[88:91]\n" \
"ds_read_b64_tr_b16 v[6:7], v232 offset:10240\n" \
"v_accvgpr_write_b32 a8, v234\n" \
"v_mfma_f32_16x16x32_bf16 v[88:91], v[52:55], v[196:199], v[88:91]\n" \
"ds_read_b64_tr_b16 v[8:9], v233 offset:0\n" \
"v_accvgpr_write_b32 a9, v236\n" \
"v_mfma_f32_16x16x32_bf16 v[92:95], v[56:59], v[192:195], v[92:95]\n" \
"ds_read_b64_tr_b16 v[10:11], v233 offset:2048\n" \
"v_accvgpr_read_b32 v234, a12\n" \
"v_mfma_f32_16x16x32_bf16 v[92:95], v[60:63], v[196:199], v[92:95]\n" \
"global_load_lds_dwordx4 v225, s[48:49] offset:-3072\n" \
"v_accvgpr_read_b32 v235, a13\n" \
"v_mfma_f32_16x16x32_bf16 v[124:127], v[56:59], v[200:203], v[124:127]\n" \
"ds_read_b128 v[192:195], v230 offset:0\n" \
"v_accvgpr_read_b32 v236, a14\n" \
"v_mfma_f32_16x16x32_bf16 v[124:127], v[60:63], v[204:207], v[124:127]\n" \
"ds_read_b128 v[196:199], v231 offset:0\n" \
"v_accvgpr_read_b32 v237, a15\n" \
"v_mfma_f32_16x16x32_bf16 v[120:123], v[48:51], v[200:203], v[120:123]\n" \
"ds_read_b64_tr_b16 v[12:13], v233 offset:8192\n" \
"v_mul_f32 v234, s92, v234\n" \
"v_mfma_f32_16x16x32_bf16 v[120:123], v[52:55], v[204:207], v[120:123]\n" \
"global_load_lds_dwordx4 v226, s[48:49] offset:-2048\n" \
"v_mul_f32 v235, s92, v235\n" \
"v_mfma_f32_16x16x32_bf16 v[116:119], v[40:43], v[200:203], v[116:119]\n" \
"ds_read_b64_tr_b16 v[14:15], v233 offset:10240\n" \
"v_mul_f32 v236, s92, v236\n" \
"v_mfma_f32_16x16x32_bf16 v[116:119], v[44:47], v[204:207], v[116:119]\n" \
"ds_read_b64_tr_b16 v[16:17], v232 offset:512\n" \
"v_mul_f32 v237, s92, v237\n" \
"v_mfma_f32_16x16x32_bf16 v[112:115], v[32:35], v[200:203], v[112:115]\n" \
"ds_read_b64_tr_b16 v[18:19], v232 offset:2560\n" \
"v_cvt_pk_bf16_f32 v234, v234, v235\n" \
"v_mfma_f32_16x16x32_bf16 v[112:115], v[36:39], v[204:207], v[112:115]\n" \
"global_load_lds_dwordx4 v227, s[48:49] offset:-1024\n" \
"v_cvt_pk_bf16_f32 v236, v236, v237\n" \
"v_mfma_f32_16x16x32_bf16 v[144:147], v[32:35], v[208:211], v[144:147]\n" \
"s_add_u32 s48, s48, 0x80\n" \
"s_addc_u32 s49, s49, 0\n" \
"ds_read_b128 v[200:203], v230 offset:2048\n" \
"v_mfma_f32_16x16x32_bf16 v[144:147], v[36:39], v[212:215], v[144:147]\n" \
"ds_read_b128 v[204:207], v231 offset:2048\n" \
"v_accvgpr_write_b32 a10, v234\n" \
"v_mfma_f32_16x16x32_bf16 v[148:151], v[40:43], v[208:211], v[148:151]\n" \
"ds_read_b64_tr_b16 v[20:21], v232 offset:8704\n" \
"v_accvgpr_write_b32 a11, v236\n" \
"v_mfma_f32_16x16x32_bf16 v[148:151], v[44:47], v[212:215], v[148:151]\n" \
"global_load_lds_dwordx4 v224, s[46:47] offset:0\n" \
"v_accvgpr_read_b32 v234, a40\n" \
"v_mfma_f32_16x16x32_bf16 v[152:155], v[48:51], v[208:211], v[152:155]\n" \
"ds_read_b64_tr_b16 v[22:23], v232 offset:10752\n" \
"v_accvgpr_read_b32 v235, a41\n" \
"v_mfma_f32_16x16x32_bf16 v[152:155], v[52:55], v[212:215], v[152:155]\n" \
"ds_read_b64_tr_b16 v[24:25], v233 offset:512\n" \
"v_accvgpr_read_b32 v236, a42\n" \
"v_mfma_f32_16x16x32_bf16 v[156:159], v[56:59], v[208:211], v[156:159]\n" \
"ds_read_b64_tr_b16 v[26:27], v233 offset:2560\n" \
"v_accvgpr_read_b32 v237, a43\n" \
"v_mfma_f32_16x16x32_bf16 v[156:159], v[60:63], v[212:215], v[156:159]\n" \
"global_load_lds_dwordx4 v225, s[46:47] offset:1024\n" \
"v_mul_f32 v234, s92, v234\n" \
"v_mfma_f32_16x16x32_bf16 v[188:191], v[56:59], v[216:219], v[188:191]\n" \
"ds_read_b128 v[208:211], v230 offset:4096\n" \
"v_mul_f32 v235, s92, v235\n" \
"v_mfma_f32_16x16x32_bf16 v[188:191], v[60:63], v[220:223], v[188:191]\n" \
"ds_read_b128 v[212:215], v231 offset:4096\n" \
"v_mul_f32 v236, s92, v236\n" \
"v_mfma_f32_16x16x32_bf16 v[184:187], v[48:51], v[216:219], v[184:187]\n" \
"ds_read_b64_tr_b16 v[28:29], v233 offset:8704\n" \
"v_mul_f32 v237, s92, v237\n" \
"v_mfma_f32_16x16x32_bf16 v[184:187], v[52:55], v[220:223], v[184:187]\n" \
"global_load_lds_dwordx4 v226, s[46:47] offset:2048\n" \
"v_cvt_pk_bf16_f32 v234, v234, v235\n" \
"v_mfma_f32_16x16x32_bf16 v[180:183], v[40:43], v[216:219], v[180:183]\n" \
"ds_read_b64_tr_b16 v[30:31], v233 offset:10752\n" \
"v_cvt_pk_bf16_f32 v236, v236, v237\n" \
"v_mfma_f32_16x16x32_bf16 v[180:183], v[44:47], v[220:223], v[180:183]\n" \
"v_accvgpr_write_b32 a40, v234\n" \
"v_accvgpr_write_b32 a41, v236\n" \
"v_mfma_f32_16x16x32_bf16 v[176:179], v[32:35], v[216:219], v[176:179]\n" \
"v_accvgpr_read_b32 v234, a44\n" \
"v_accvgpr_read_b32 v235, a45\n" \
"v_mfma_f32_16x16x32_bf16 v[176:179], v[36:39], v[220:223], v[176:179]\n" \
"s_waitcnt lgkmcnt(1)\n" \
"global_load_lds_dwordx4 v227, s[46:47] offset:3072\n" \
"v_accvgpr_read_b32 v236, a46\n" \
"v_mfma_f32_16x16x32_bf16 v[64:67], v[0:3], v[192:195], v[64:67]\n" \
"s_add_u32 s46, s46, 0x80\n" \
"s_addc_u32 s47, s47, 0\n" \
"ds_read_b128 v[216:219], v230 offset:6144\n" \
"v_mfma_f32_16x16x32_bf16 v[64:67], v[4:7], v[196:199], v[64:67]\n" \
"ds_read_b128 v[220:223], v231 offset:6144\n" \
"v_accvgpr_read_b32 v237, a47\n" \
"v_mfma_f32_16x16x32_bf16 v[68:71], v[8:11], v[192:195], v[68:71]\n" \
"ds_read_b64_tr_b16 v[32:33], v232 offset:1024\n" \
"v_mul_f32 v234, s92, v234\n" \
"v_mfma_f32_16x16x32_bf16 v[68:71], v[12:15], v[196:199], v[68:71]\n" \
"buffer_store_dwordx4 a[160:163], v249, s[56:59], s90 offen offset:256 sc1\n" \
"v_mul_f32 v235, s92, v235\n" \
"v_mul_f32 v236, s92, v236\n" \
"v_mul_f32 v237, s92, v237\n" \
"v_cvt_pk_bf16_f32 v234, v234, v235\n" \
"v_mfma_f32_16x16x32_bf16 v[72:75], v[16:19], v[192:195], v[72:75]\n" \
"ds_read_b64_tr_b16 v[34:35], v232 offset:3072\n" \
"v_cvt_pk_bf16_f32 v236, v236, v237\n" \
"v_mfma_f32_16x16x32_bf16 v[72:75], v[20:23], v[196:199], v[72:75]\n" \
"s_waitcnt lgkmcnt(3)\n" \
"ds_read_b64_tr_b16 v[36:37], v232 offset:9216\n" \
"v_accvgpr_write_b32 a42, v234\n" \
"v_mfma_f32_16x16x32_bf16 v[76:79], v[24:27], v[192:195], v[76:79]\n" \
"ds_read_b64_tr_b16 v[38:39], v232 offset:11264\n" \
"v_accvgpr_write_b32 a43, v236\n" \
"v_mfma_f32_16x16x32_bf16 v[76:79], v[28:31], v[196:199], v[76:79]\n" \
"v_accvgpr_read_b32 v234, a16\n" \
"v_accvgpr_read_b32 v235, a17\n" \
"v_mfma_f32_16x16x32_bf16 v[108:111], v[24:27], v[200:203], v[108:111]\n" \
"ds_read_b64_tr_b16 v[40:41], v233 offset:1024\n" \
"v_accvgpr_read_b32 v236, a18\n" \
"v_mfma_f32_16x16x32_bf16 v[108:111], v[28:31], v[204:207], v[108:111]\n" \
"ds_read_b64_tr_b16 v[42:43], v233 offset:3072\n" \
"v_accvgpr_read_b32 v237, a19\n" \
"v_mfma_f32_16x16x32_bf16 v[104:107], v[16:19], v[200:203], v[104:107]\n" \
"ds_read_b64_tr_b16 v[44:45], v233 offset:9216\n" \
"v_cvt_pk_bf16_f32 v234, v234, v235\n" \
"v_mfma_f32_16x16x32_bf16 v[104:107], v[20:23], v[204:207], v[104:107]\n" \
"v_cvt_pk_bf16_f32 v236, v236, v237\n" \
"v_accvgpr_write_b32 a16, v234\n" \
"v_mfma_f32_16x16x32_bf16 v[100:103], v[8:11], v[200:203], v[100:103]\n" \
"ds_read_b64_tr_b16 v[46:47], v233 offset:11264\n" \
"v_accvgpr_write_b32 a17, v236\n" \
"v_mfma_f32_16x16x32_bf16 v[100:103], v[12:15], v[204:207], v[100:103]\n" \
"ds_read_b64_tr_b16 v[48:49], v232 offset:1536\n" \
"v_accvgpr_read_b32 v234, a48\n" \
"v_mfma_f32_16x16x32_bf16 v[96:99], v[0:3], v[200:203], v[96:99]\n" \
"s_add_u32 m0, s95, 36864\n" \
"ds_read_b64_tr_b16 v[50:51], v232 offset:3584\n" \
"v_accvgpr_read_b32 v235, a49\n" \
"v_mfma_f32_16x16x32_bf16 v[96:99], v[4:7], v[204:207], v[96:99]\n" \
"global_load_lds_dwordx4 v228, s[44:45] offset:-4096\n" \
"v_accvgpr_read_b32 v236, a50\n" \
"v_mfma_f32_16x16x32_bf16 v[128:131], v[0:3], v[208:211], v[128:131]\n" \
"ds_read_b64_tr_b16 v[52:53], v232 offset:9728\n" \
"v_accvgpr_read_b32 v237, a51\n" \
"v_mfma_f32_16x16x32_bf16 v[128:131], v[4:7], v[212:215], v[128:131]\n" \
"ds_read_b64_tr_b16 v[54:55], v232 offset:11776\n" \
"v_cvt_pk_bf16_f32 v234, v234, v235\n" \
"v_mfma_f32_16x16x32_bf16 v[132:135], v[8:11], v[208:211], v[132:135]\n" \
"ds_read_b64_tr_b16 v[56:57], v233 offset:1536\n" \
"v_cvt_pk_bf16_f32 v236, v236, v237\n" \
"v_mfma_f32_16x16x32_bf16 v[132:135], v[12:15], v[212:215], v[132:135]\n" \
"global_load_lds_dwordx4 v229, s[44:45] offset:-3072\n" \
"v_accvgpr_write_b32 a18, v234\n" \
"v_mfma_f32_16x16x32_bf16 v[136:139], v[16:19], v[208:211], v[136:139]\n" \
"s_add_u32 s44, s44, 0x80000\n" \
"s_addc_u32 s45, s45, 0\n" \
"ds_read_b64_tr_b16 v[58:59], v233 offset:3584\n" \
"v_mfma_f32_16x16x32_bf16 v[136:139], v[20:23], v[212:215], v[136:139]\n" \
"ds_read_b64_tr_b16 v[60:61], v233 offset:9728\n" \
"v_accvgpr_write_b32 a19, v236\n" \
"v_mfma_f32_16x16x32_bf16 v[140:143], v[24:27], v[208:211], v[140:143]\n" \
"ds_read_b64_tr_b16 v[62:63], v233 offset:11776\n" \
"v_accvgpr_read_b32 v234, a20\n" \
"v_mfma_f32_16x16x32_bf16 v[140:143], v[28:31], v[212:215], v[140:143]\n" \
"s_waitcnt lgkmcnt(3)\n" \
"global_load_lds_dwordx4 v228, s[42:43] offset:-2048\n" \
"v_accvgpr_read_b32 v235, a21\n" \
"v_mfma_f32_16x16x32_bf16 v[172:175], v[24:27], v[216:219], v[172:175]\n" \
"v_add_u32 v230, 0x8000, v230\n" \
"v_add_u32 v231, 0x8000, v231\n" \
"v_mfma_f32_16x16x32_bf16 v[172:175], v[28:31], v[220:223], v[172:175]\n" \
"v_add_u32 v232, 0xffff4000, v232\n" \
"v_add_u32 v233, 0xffff4000, v233\n" \
"v_mfma_f32_16x16x32_bf16 v[168:171], v[16:19], v[216:219], v[168:171]\n" \
"v_accvgpr_read_b32 v236, a22\n" \
"v_accvgpr_read_b32 v237, a23\n" \
"v_mfma_f32_16x16x32_bf16 v[168:171], v[20:23], v[220:223], v[168:171]\n" \
"global_load_lds_dwordx4 v229, s[42:43] offset:-1024\n" \
"v_cvt_pk_bf16_f32 v234, v234, v235\n" \
"v_mfma_f32_16x16x32_bf16 v[164:167], v[8:11], v[216:219], v[164:167]\n" \
"s_add_u32 s42, s42, 0x80000\n" \
"s_addc_u32 s43, s43, 0\n" \
"v_cvt_pk_bf16_f32 v236, v236, v237\n" \
"v_mfma_f32_16x16x32_bf16 v[164:167], v[12:15], v[220:223], v[164:167]\n" \
"s_waitcnt vmcnt(18) lgkmcnt(0)\n" \
"v_accvgpr_write_b32 a20, v234\n" \
"v_accvgpr_write_b32 a21, v236\n" \
"v_mfma_f32_16x16x32_bf16 v[160:163], v[0:3], v[216:219], v[160:163]\n" \
"v_accvgpr_read_b32 v234, a52\n" \
"v_accvgpr_read_b32 v235, a53\n" \
"v_mfma_f32_16x16x32_bf16 v[160:163], v[4:7], v[220:223], v[160:163]\n" \
"s_barrier\n" \
"v_mfma_f32_16x16x32_bf16 v[80:83], v[32:35], v[192:195], v[80:83]\n" \
"ds_read_b64_tr_b16 v[0:1], v232 offset:0\n" \
"v_accvgpr_read_b32 v236, a54\n" \
"v_mfma_f32_16x16x32_bf16 v[80:83], v[36:39], v[196:199], v[80:83]\n" \
"ds_read_b64_tr_b16 v[2:3], v232 offset:2048\n" \
"v_accvgpr_read_b32 v237, a55\n" \
"v_mfma_f32_16x16x32_bf16 v[84:87], v[40:43], v[192:195], v[84:87]\n" \
"s_add_u32 m0, s87, 69632\n" \
"ds_read_b64_tr_b16 v[4:5], v232 offset:8192\n" \
"v_cvt_pk_bf16_f32 v234, v234, v235\n" \
"v_mfma_f32_16x16x32_bf16 v[84:87], v[44:47], v[196:199], v[84:87]\n" \
"global_load_lds_dwordx4 v224, s[48:49] offset:-4096\n" \
"v_cvt_pk_bf16_f32 v236, v236, v237\n" \
"v_mfma_f32_16x16x32_bf16 v[88:91], v[48:51], v[192:195], v[88:91]\n" \
"ds_read_b64_tr_b16 v[6:7], v232 offset:10240\n" \
"v_accvgpr_write_b32 a22, v234\n" \
"v_mfma_f32_16x16x32_bf16 v[88:91], v[52:55], v[196:199], v[88:91]\n" \
"ds_read_b64_tr_b16 v[8:9], v233 offset:0\n" \
"v_accvgpr_write_b32 a23, v236\n" \
"v_mfma_f32_16x16x32_bf16 v[92:95], v[56:59], v[192:195], v[92:95]\n" \
"ds_read_b64_tr_b16 v[10:11], v233 offset:2048\n" \
"v_accvgpr_read_b32 v234, a24\n" \
"v_mfma_f32_16x16x32_bf16 v[92:95], v[60:63], v[196:199], v[92:95]\n" \
"global_load_lds_dwordx4 v225, s[48:49] offset:-3072\n" \
"v_accvgpr_read_b32 v235, a25\n" \
"v_mfma_f32_16x16x32_bf16 v[124:127], v[56:59], v[200:203], v[124:127]\n" \
"ds_read_b128 v[192:195], v230 offset:0\n" \
"v_accvgpr_read_b32 v236, a26\n" \
"v_mfma_f32_16x16x32_bf16 v[124:127], v[60:63], v[204:207], v[124:127]\n" \
"ds_read_b128 v[196:199], v231 offset:0\n" \
"v_accvgpr_read_b32 v237, a27\n" \
"v_mfma_f32_16x16x32_bf16 v[120:123], v[48:51], v[200:203], v[120:123]\n" \
"ds_read_b64_tr_b16 v[12:13], v233 offset:8192\n" \
"v_cvt_pk_bf16_f32 v234, v234, v235\n" \
"v_mfma_f32_16x16x32_bf16 v[120:123], v[52:55], v[204:207], v[120:123]\n" \
"global_load_lds_dwordx4 v226, s[48:49] offset:-2048\n" \
"v_cvt_pk_bf16_f32 v236, v236, v237\n" \
"v_mfma_f32_16x16x32_bf16 v[116:119], v[40:43], v[200:203], v[116:119]\n" \
"ds_read_b64_tr_b16 v[14:15], v233 offset:10240\n" \
"v_accvgpr_write_b32 a24, v234\n" \
"v_mfma_f32_16x16x32_bf16 v[116:119], v[44:47], v[204:207], v[116:119]\n" \
"ds_read_b64_tr_b16 v[16:17], v232 offset:512\n" \
"v_accvgpr_write_b32 a25, v236\n" \
"v_mfma_f32_16x16x32_bf16 v[112:115], v[32:35], v[200:203], v[112:115]\n" \
"ds_read_b64_tr_b16 v[18:19], v232 offset:2560\n" \
"v_accvgpr_read_b32 v234, a56\n" \
"v_mfma_f32_16x16x32_bf16 v[112:115], v[36:39], v[204:207], v[112:115]\n" \
"global_load_lds_dwordx4 v227, s[48:49] offset:-1024\n" \
"v_accvgpr_read_b32 v235, a57\n" \
"v_mfma_f32_16x16x32_bf16 v[144:147], v[32:35], v[208:211], v[144:147]\n" \
"s_add_u32 s48, s48, 0x80\n" \
"s_addc_u32 s49, s49, 0\n" \
"ds_read_b128 v[200:203], v230 offset:2048\n" \
"v_mfma_f32_16x16x32_bf16 v[144:147], v[36:39], v[212:215], v[144:147]\n" \
"ds_read_b128 v[204:207], v231 offset:2048\n" \
"v_accvgpr_read_b32 v236, a58\n" \
"v_mfma_f32_16x16x32_bf16 v[148:151], v[40:43], v[208:211], v[148:151]\n" \
"ds_read_b64_tr_b16 v[20:21], v232 offset:8704\n" \
"v_accvgpr_read_b32 v237, a59\n" \
"v_mfma_f32_16x16x32_bf16 v[148:151], v[44:47], v[212:215], v[148:151]\n" \
"global_load_lds_dwordx4 v224, s[46:47] offset:0\n" \
"v_cvt_pk_bf16_f32 v234, v234, v235\n" \
"v_mfma_f32_16x16x32_bf16 v[152:155], v[48:51], v[208:211], v[152:155]\n" \
"ds_read_b64_tr_b16 v[22:23], v232 offset:10752\n" \
"v_cvt_pk_bf16_f32 v236, v236, v237\n" \
"v_mfma_f32_16x16x32_bf16 v[152:155], v[52:55], v[212:215], v[152:155]\n" \
"ds_read_b64_tr_b16 v[24:25], v233 offset:512\n" \
"v_accvgpr_write_b32 a26, v234\n" \
"v_mfma_f32_16x16x32_bf16 v[156:159], v[56:59], v[208:211], v[156:159]\n" \
"ds_read_b64_tr_b16 v[26:27], v233 offset:2560\n" \
"v_accvgpr_write_b32 a27, v236\n" \
"v_mfma_f32_16x16x32_bf16 v[156:159], v[60:63], v[212:215], v[156:159]\n" \
"global_load_lds_dwordx4 v225, s[46:47] offset:1024\n" \
"v_accvgpr_read_b32 v234, a28\n" \
"v_mfma_f32_16x16x32_bf16 v[188:191], v[56:59], v[216:219], v[188:191]\n" \
"ds_read_b128 v[208:211], v230 offset:4096\n" \
"v_accvgpr_read_b32 v235, a29\n" \
"v_mfma_f32_16x16x32_bf16 v[188:191], v[60:63], v[220:223], v[188:191]\n" \
"ds_read_b128 v[212:215], v231 offset:4096\n" \
"v_accvgpr_read_b32 v236, a30\n" \
"v_mfma_f32_16x16x32_bf16 v[184:187], v[48:51], v[216:219], v[184:187]\n" \
"ds_read_b64_tr_b16 v[28:29], v233 offset:8704\n" \
"v_accvgpr_read_b32 v237, a31\n" \
"v_mfma_f32_16x16x32_bf16 v[184:187], v[52:55], v[220:223], v[184:187]\n" \
"global_load_lds_dwordx4 v226, s[46:47] offset:2048\n" \
"v_cvt_pk_bf16_f32 v234, v234, v235\n" \
"v_mfma_f32_16x16x32_bf16 v[180:183], v[40:43], v[216:219], v[180:183]\n" \
"ds_read_b64_tr_b16 v[30:31], v233 offset:10752\n" \
"v_cvt_pk_bf16_f32 v236, v236, v237\n" \
"v_mfma_f32_16x16x32_bf16 v[180:183], v[44:47], v[220:223], v[180:183]\n" \
"v_accvgpr_write_b32 a28, v234\n" \
"v_accvgpr_write_b32 a29, v236\n" \
"v_mfma_f32_16x16x32_bf16 v[176:179], v[32:35], v[216:219], v[176:179]\n" \
"v_accvgpr_read_b32 v234, a60\n" \
"v_accvgpr_read_b32 v235, a61\n" \
"v_mfma_f32_16x16x32_bf16 v[176:179], v[36:39], v[220:223], v[176:179]\n" \
"s_waitcnt lgkmcnt(1)\n" \
"global_load_lds_dwordx4 v227, s[46:47] offset:3072\n" \
"v_accvgpr_read_b32 v236, a62\n" \
"v_mfma_f32_16x16x32_bf16 v[64:67], v[0:3], v[192:195], v[64:67]\n" \
"s_add_u32 s46, s46, 0x80\n" \
"s_addc_u32 s47, s47, 0\n" \
"ds_read_b128 v[216:219], v230 offset:6144\n" \
"v_mfma_f32_16x16x32_bf16 v[64:67], v[4:7], v[196:199], v[64:67]\n" \
"ds_read_b128 v[220:223], v231 offset:6144\n" \
"v_accvgpr_read_b32 v237, a63\n" \
"v_mfma_f32_16x16x32_bf16 v[68:71], v[8:11], v[192:195], v[68:71]\n" \
"ds_read_b64_tr_b16 v[32:33], v232 offset:1024\n" \
"v_cvt_pk_bf16_f32 v234, v234, v235\n" \
"v_mfma_f32_16x16x32_bf16 v[68:71], v[12:15], v[196:199], v[68:71]\n" \
"buffer_store_dwordx4 a[136:139], v249, s[56:59], s90 offen offset:2048 sc1\n" \
"v_cvt_pk_bf16_f32 v236, v236, v237\n" \
"v_accvgpr_write_b32 a30, v234\n" \
"v_accvgpr_write_b32 a31, v236\n" \
"v_mfma_f32_16x16x32_bf16 v[72:75], v[16:19], v[192:195], v[72:75]\n" \
"ds_read_b64_tr_b16 v[34:35], v232 offset:3072\n" \
"v_mfma_f32_16x16x32_bf16 v[72:75], v[20:23], v[196:199], v[72:75]\n" \
"s_waitcnt lgkmcnt(3)\n" \
"ds_read_b64_tr_b16 v[36:37], v232 offset:9216\n" \
"v_mfma_f32_16x16x32_bf16 v[76:79], v[24:27], v[192:195], v[76:79]\n" \
"ds_read_b64_tr_b16 v[38:39], v232 offset:11264\n" \
"v_mfma_f32_16x16x32_bf16 v[76:79], v[28:31], v[196:199], v[76:79]\n" \
"buffer_store_dwordx4 a[168:171], v249, s[56:59], s90 offen offset:2304 sc1\n" \
"v_mfma_f32_16x16x32_bf16 v[108:111], v[24:27], v[200:203], v[108:111]\n" \
"s_lshl_b32 s70, s63, 2\n" \
"ds_read_b64_tr_b16 v[40:41], v233 offset:1024\n" \
"v_mfma_f32_16x16x32_bf16 v[108:111], v[28:31], v[204:207], v[108:111]\n" \
"s_add_u32 s70, s70, s91\n" \
"ds_read_b64_tr_b16 v[42:43], v233 offset:3072\n" \
"v_mov_b32 v238, s60\n" \
"v_mfma_f32_16x16x32_bf16 v[104:107], v[16:19], v[200:203], v[104:107]\n" \
"ds_read_b64_tr_b16 v[44:45], v233 offset:9216\n" \
"v_lshlrev_b32 v239, 28, s61\n" \
"v_mfma_f32_16x16x32_bf16 v[104:107], v[20:23], v[204:207], v[104:107]\n" \
"buffer_store_dword v238, v239, s[56:59], s70 offen sc1\n" \
"v_mfma_f32_16x16x32_bf16 v[100:103], v[8:11], v[200:203], v[100:103]\n" \
"ds_read_b64_tr_b16 v[46:47], v233 offset:11264\n" \
"v_mfma_f32_16x16x32_bf16 v[100:103], v[12:15], v[204:207], v[100:103]\n" \
"ds_read_b64_tr_b16 v[48:49], v232 offset:1536\n" \
"v_mfma_f32_16x16x32_bf16 v[96:99], v[0:3], v[200:203], v[96:99]\n" \
"s_add_u32 m0, s95, 53248\n" \
"ds_read_b64_tr_b16 v[50:51], v232 offset:3584\n" \
"v_mfma_f32_16x16x32_bf16 v[96:99], v[4:7], v[204:207], v[96:99]\n" \
"global_load_lds_dwordx4 v228, s[44:45] offset:-4096\n" \
"v_mfma_f32_16x16x32_bf16 v[128:131], v[0:3], v[208:211], v[128:131]\n" \
"ds_read_b64_tr_b16 v[52:53], v232 offset:9728\n" \
"v_mfma_f32_16x16x32_bf16 v[128:131], v[4:7], v[212:215], v[128:131]\n" \
"ds_read_b64_tr_b16 v[54:55], v232 offset:11776\n" \
"v_mfma_f32_16x16x32_bf16 v[132:135], v[8:11], v[208:211], v[132:135]\n" \
"ds_read_b64_tr_b16 v[56:57], v233 offset:1536\n" \
"Lpg3849_%=:\n" \
"s_cbranch_execz Lpg5534_%=\n" \
"v_mfma_f32_16x16x32_bf16 v[132:135], v[12:15], v[212:215], v[132:135]\n" \
"global_load_lds_dwordx4 v229, s[44:45] offset:-3072\n" \
"v_mfma_f32_16x16x32_bf16 v[136:139], v[16:19], v[208:211], v[136:139]\n" \
"s_add_u32 s44, s44, 0x100000\n" \
"s_addc_u32 s45, s45, 0\n" \
"ds_read_b64_tr_b16 v[58:59], v233 offset:3584\n" \
"v_mfma_f32_16x16x32_bf16 v[136:139], v[20:23], v[212:215], v[136:139]\n" \
"ds_read_b64_tr_b16 v[60:61], v233 offset:9728\n" \
"v_mfma_f32_16x16x32_bf16 v[140:143], v[24:27], v[208:211], v[140:143]\n" \
"ds_read_b64_tr_b16 v[62:63], v233 offset:11776\n" \
"v_mfma_f32_16x16x32_bf16 v[140:143], v[28:31], v[212:215], v[140:143]\n" \
"s_waitcnt lgkmcnt(3)\n" \
"global_load_lds_dwordx4 v228, s[42:43] offset:-2048\n" \
"v_mfma_f32_16x16x32_bf16 v[172:175], v[24:27], v[216:219], v[172:175]\n" \
"v_add_u32 v230, 0x8000, v230\n" \
"v_add_u32 v231, 0x8000, v231\n" \
"v_mfma_f32_16x16x32_bf16 v[172:175], v[28:31], v[220:223], v[172:175]\n" \
"v_add_u32 v232, 0x4000, v232\n" \
"v_add_u32 v233, 0x4000, v233\n" \
"v_mfma_f32_16x16x32_bf16 v[168:171], v[16:19], v[216:219], v[168:171]\n" \
"v_mfma_f32_16x16x32_bf16 v[168:171], v[20:23], v[220:223], v[168:171]\n" \
"global_load_lds_dwordx4 v229, s[42:43] offset:-1024\n" \
"v_mfma_f32_16x16x32_bf16 v[164:167], v[8:11], v[216:219], v[164:167]\n" \
"s_add_u32 s42, s42, 0x100000\n" \
"s_addc_u32 s43, s43, 0\n" \
"v_mfma_f32_16x16x32_bf16 v[164:167], v[12:15], v[220:223], v[164:167]\n" \
"s_waitcnt vmcnt(20) lgkmcnt(0)\n" \
"v_mfma_f32_16x16x32_bf16 v[160:163], v[0:3], v[216:219], v[160:163]\n" \
"v_mfma_f32_16x16x32_bf16 v[160:163], v[4:7], v[220:223], v[160:163]\n" \
"s_barrier\n" \
"v_mfma_f32_16x16x32_bf16 v[80:83], v[32:35], v[192:195], v[80:83]\n" \
"ds_read_b64_tr_b16 v[0:1], v232 offset:0\n" \
"v_mfma_f32_16x16x32_bf16 v[80:83], v[36:39], v[196:199], v[80:83]\n" \
"ds_read_b64_tr_b16 v[2:3], v232 offset:2048\n" \
"v_mfma_f32_16x16x32_bf16 v[84:87], v[40:43], v[192:195], v[84:87]\n" \
"s_add_u32 m0, s87, 102400\n" \
"ds_read_b64_tr_b16 v[4:5], v232 offset:8192\n" \
"v_mfma_f32_16x16x32_bf16 v[84:87], v[44:47], v[196:199], v[84:87]\n" \
"global_load_lds_dwordx4 v224, s[48:49] offset:-4096\n" \
"v_mfma_f32_16x16x32_bf16 v[88:91], v[48:51], v[192:195], v[88:91]\n" \
"ds_read_b64_tr_b16 v[6:7], v232 offset:10240\n" \
"v_mfma_f32_16x16x32_bf16 v[88:91], v[52:55], v[196:199], v[88:91]\n" \
"ds_read_b64_tr_b16 v[8:9], v233 offset:0\n" \
"v_mfma_f32_16x16x32_bf16 v[92:95], v[56:59], v[192:195], v[92:95]\n" \
"ds_read_b64_tr_b16 v[10:11], v233 offset:2048\n" \
"v_mfma_f32_16x16x32_bf16 v[92:95], v[60:63], v[196:199], v[92:95]\n" \
"global_load_lds_dwordx4 v225, s[48:49] offset:-3072\n" \
"v_mfma_f32_16x16x32_bf16 v[124:127], v[56:59], v[200:203], v[124:127]\n" \
"ds_read_b128 v[192:195], v230 offset:0\n" \
"v_mfma_f32_16x16x32_bf16 v[124:127], v[60:63], v[204:207], v[124:127]\n" \
"ds_read_b128 v[196:199], v231 offset:0\n" \
"v_mfma_f32_16x16x32_bf16 v[120:123], v[48:51], v[200:203], v[120:123]\n" \
"ds_read_b64_tr_b16 v[12:13], v233 offset:8192\n" \
"v_mfma_f32_16x16x32_bf16 v[120:123], v[52:55], v[204:207], v[120:123]\n" \
"global_load_lds_dwordx4 v226, s[48:49] offset:-2048\n" \
"v_mfma_f32_16x16x32_bf16 v[116:119], v[40:43], v[200:203], v[116:119]\n" \
"ds_read_b64_tr_b16 v[14:15], v233 offset:10240\n" \
"v_mfma_f32_16x16x32_bf16 v[116:119], v[44:47], v[204:207], v[116:119]\n" \
"ds_read_b64_tr_b16 v[16:17], v232 offset:512\n" \
"v_mfma_f32_16x16x32_bf16 v[112:115], v[32:35], v[200:203], v[112:115]\n" \
"ds_read_b64_tr_b16 v[18:19], v232 offset:2560\n" \
"v_mfma_f32_16x16x32_bf16 v[112:115], v[36:39], v[204:207], v[112:115]\n" \
"global_load_lds_dwordx4 v227, s[48:49] offset:-1024\n" \
"v_mfma_f32_16x16x32_bf16 v[144:147], v[32:35], v[208:211], v[144:147]\n" \
"s_add_u32 s48, s48, 0x100\n" \
"s_addc_u32 s49, s49, 0\n" \
"ds_read_b128 v[200:203], v230 offset:2048\n" \
"v_mfma_f32_16x16x32_bf16 v[144:147], v[36:39], v[212:215], v[144:147]\n" \
"ds_read_b128 v[204:207], v231 offset:2048\n" \
"v_mfma_f32_16x16x32_bf16 v[148:151], v[40:43], v[208:211], v[148:151]\n" \
"ds_read_b64_tr_b16 v[20:21], v232 offset:8704\n" \
"v_mfma_f32_16x16x32_bf16 v[148:151], v[44:47], v[212:215], v[148:151]\n" \
"global_load_lds_dwordx4 v224, s[46:47] offset:0\n" \
"v_mfma_f32_16x16x32_bf16 v[152:155], v[48:51], v[208:211], v[152:155]\n" \
"ds_read_b64_tr_b16 v[22:23], v232 offset:10752\n" \
"v_mfma_f32_16x16x32_bf16 v[152:155], v[52:55], v[212:215], v[152:155]\n" \
"ds_read_b64_tr_b16 v[24:25], v233 offset:512\n" \
"v_mfma_f32_16x16x32_bf16 v[156:159], v[56:59], v[208:211], v[156:159]\n" \
"ds_read_b64_tr_b16 v[26:27], v233 offset:2560\n" \
"v_mfma_f32_16x16x32_bf16 v[156:159], v[60:63], v[212:215], v[156:159]\n" \
"global_load_lds_dwordx4 v225, s[46:47] offset:1024\n" \
"v_mfma_f32_16x16x32_bf16 v[188:191], v[56:59], v[216:219], v[188:191]\n" \
"ds_read_b128 v[208:211], v230 offset:4096\n" \
"v_mfma_f32_16x16x32_bf16 v[188:191], v[60:63], v[220:223], v[188:191]\n" \
"ds_read_b128 v[212:215], v231 offset:4096\n" \
"v_mfma_f32_16x16x32_bf16 v[184:187], v[48:51], v[216:219], v[184:187]\n" \
"ds_read_b64_tr_b16 v[28:29], v233 offset:8704\n" \
"v_mfma_f32_16x16x32_bf16 v[184:187], v[52:55], v[220:223], v[184:187]\n" \
"global_load_lds_dwordx4 v226, s[46:47] offset:2048\n" \
"v_mfma_f32_16x16x32_bf16 v[180:183], v[40:43], v[216:219], v[180:183]\n" \
"ds_read_b64_tr_b16 v[30:31], v233 offset:10752\n" \
"v_mfma_f32_16x16x32_bf16 v[180:183], v[44:47], v[220:223], v[180:183]\n" \
"v_mfma_f32_16x16x32_bf16 v[176:179], v[32:35], v[216:219], v[176:179]\n" \
"v_mfma_f32_16x16x32_bf16 v[176:179], v[36:39], v[220:223], v[176:179]\n" \
"s_waitcnt lgkmcnt(1)\n" \
"global_load_lds_dwordx4 v227, s[46:47] offset:3072\n" \
"v_mfma_f32_16x16x32_bf16 v[64:67], v[0:3], v[192:195], v[64:67]\n" \
"s_add_u32 s46, s46, 0x100\n" \
"s_addc_u32 s47, s47, 0\n" \
"ds_read_b128 v[216:219], v230 offset:6144\n" \
"v_mfma_f32_16x16x32_bf16 v[64:67], v[4:7], v[196:199], v[64:67]\n" \
"ds_read_b128 v[220:223], v231 offset:6144\n" \
"s_add_u32 s90, s89, 139264\n" \
"v_mfma_f32_16x16x32_bf16 v[68:71], v[8:11], v[192:195], v[68:71]\n" \
"ds_read_b64_tr_b16 v[32:33], v232 offset:1024\n" \
"v_mfma_f32_16x16x32_bf16 v[68:71], v[12:15], v[196:199], v[68:71]\n" \
"buffer_store_dwordx4 a[144:147], v248, s[56:59], s90 offen offset:0 sc1\n" \
"v_mfma_f32_16x16x32_bf16 v[72:75], v[16:19], v[192:195], v[72:75]\n" \
"ds_read_b64_tr_b16 v[34:35], v232 offset:3072\n" \
"v_mfma_f32_16x16x32_bf16 v[72:75], v[20:23], v[196:199], v[72:75]\n" \
"s_waitcnt lgkmcnt(3)\n" \
"ds_read_b64_tr_b16 v[36:37], v232 offset:9216\n" \
"v_mfma_f32_16x16x32_bf16 v[76:79], v[24:27], v[192:195], v[76:79]\n" \
"ds_read_b64_tr_b16 v[38:39], v232 offset:11264\n" \
"v_mfma_f32_16x16x32_bf16 v[76:79], v[28:31], v[196:199], v[76:79]\n" \
"v_mfma_f32_16x16x32_bf16 v[108:111], v[24:27], v[200:203], v[108:111]\n" \
"ds_read_b64_tr_b16 v[40:41], v233 offset:1024\n" \
"v_mfma_f32_16x16x32_bf16 v[108:111], v[28:31], v[204:207], v[108:111]\n" \
"ds_read_b64_tr_b16 v[42:43], v233 offset:3072\n" \
"v_mfma_f32_16x16x32_bf16 v[104:107], v[16:19], v[200:203], v[104:107]\n" \
"ds_read_b64_tr_b16 v[44:45], v233 offset:9216\n" \
"v_mfma_f32_16x16x32_bf16 v[104:107], v[20:23], v[204:207], v[104:107]\n" \
"v_mfma_f32_16x16x32_bf16 v[100:103], v[8:11], v[200:203], v[100:103]\n" \
"ds_read_b64_tr_b16 v[46:47], v233 offset:11264\n" \
"v_mfma_f32_16x16x32_bf16 v[100:103], v[12:15], v[204:207], v[100:103]\n" \
"ds_read_b64_tr_b16 v[48:49], v232 offset:1536\n" \
"v_mfma_f32_16x16x32_bf16 v[96:99], v[0:3], v[200:203], v[96:99]\n" \
"s_add_u32 m0, s95, 4096\n" \
"ds_read_b64_tr_b16 v[50:51], v232 offset:3584\n" \
"v_mfma_f32_16x16x32_bf16 v[96:99], v[4:7], v[204:207], v[96:99]\n" \
"global_load_lds_dwordx4 v228, s[44:45] offset:-4096\n" \
"v_mfma_f32_16x16x32_bf16 v[128:131], v[0:3], v[208:211], v[128:131]\n" \
"ds_read_b64_tr_b16 v[52:53], v232 offset:9728\n" \
"v_mfma_f32_16x16x32_bf16 v[128:131], v[4:7], v[212:215], v[128:131]\n" \
"ds_read_b64_tr_b16 v[54:55], v232 offset:11776\n" \
"v_mfma_f32_16x16x32_bf16 v[132:135], v[8:11], v[208:211], v[132:135]\n" \
"ds_read_b64_tr_b16 v[56:57], v233 offset:1536\n" \
"v_mfma_f32_16x16x32_bf16 v[132:135], v[12:15], v[212:215], v[132:135]\n" \
"global_load_lds_dwordx4 v229, s[44:45] offset:-3072\n" \
"v_mfma_f32_16x16x32_bf16 v[136:139], v[16:19], v[208:211], v[136:139]\n" \
"s_add_u32 s44, s44, 0x80000\n" \
"s_addc_u32 s45, s45, 0\n" \
"ds_read_b64_tr_b16 v[58:59], v233 offset:3584\n" \
"v_mfma_f32_16x16x32_bf16 v[136:139], v[20:23], v[212:215], v[136:139]\n" \
"ds_read_b64_tr_b16 v[60:61], v233 offset:9728\n" \
"v_mfma_f32_16x16x32_bf16 v[140:143], v[24:27], v[208:211], v[140:143]\n" \
"ds_read_b64_tr_b16 v[62:63], v233 offset:11776\n" \
"v_mfma_f32_16x16x32_bf16 v[140:143], v[28:31], v[212:215], v[140:143]\n" \
"s_waitcnt lgkmcnt(3)\n" \
"global_load_lds_dwordx4 v228, s[42:43] offset:-2048\n" \
"v_mfma_f32_16x16x32_bf16 v[172:175], v[24:27], v[216:219], v[172:175]\n" \
"v_add_u32 v230, 0xffff0000, v230\n" \
"v_add_u32 v231, 0xffff0000, v231\n" \
"v_mfma_f32_16x16x32_bf16 v[172:175], v[28:31], v[220:223], v[172:175]\n" \
"v_add_u32 v232, 0x4000, v232\n" \
"v_add_u32 v233, 0x4000, v233\n" \
"v_mfma_f32_16x16x32_bf16 v[168:171], v[16:19], v[216:219], v[168:171]\n" \
"v_mfma_f32_16x16x32_bf16 v[168:171], v[20:23], v[220:223], v[168:171]\n" \
"global_load_lds_dwordx4 v229, s[42:43] offset:-1024\n" \
"v_mfma_f32_16x16x32_bf16 v[164:167], v[8:11], v[216:219], v[164:167]\n" \
"s_add_u32 s42, s42, 0x80000\n" \
"s_addc_u32 s43, s43, 0\n" \
"v_mfma_f32_16x16x32_bf16 v[164:167], v[12:15], v[220:223], v[164:167]\n" \
"s_waitcnt vmcnt(20) lgkmcnt(0)\n" \
"v_mfma_f32_16x16x32_bf16 v[160:163], v[0:3], v[216:219], v[160:163]\n" \
"v_mfma_f32_16x16x32_bf16 v[160:163], v[4:7], v[220:223], v[160:163]\n" \
"s_barrier\n" \
"v_mfma_f32_16x16x32_bf16 v[80:83], v[32:35], v[192:195], v[80:83]\n" \
"ds_read_b64_tr_b16 v[0:1], v232 offset:0\n" \
"v_mfma_f32_16x16x32_bf16 v[80:83], v[36:39], v[196:199], v[80:83]\n" \
"ds_read_b64_tr_b16 v[2:3], v232 offset:2048\n" \
"v_mfma_f32_16x16x32_bf16 v[84:87], v[40:43], v[192:195], v[84:87]\n" \
"s_add_u32 m0, s87, 135168\n" \
"ds_read_b64_tr_b16 v[4:5], v232 offset:8192\n" \
"v_mfma_f32_16x16x32_bf16 v[84:87], v[44:47], v[196:199], v[84:87]\n" \
"global_load_lds_dwordx4 v224, s[48:49] offset:-4096\n" \
"v_mfma_f32_16x16x32_bf16 v[88:91], v[48:51], v[192:195], v[88:91]\n" \
"ds_read_b64_tr_b16 v[6:7], v232 offset:10240\n" \
"v_mfma_f32_16x16x32_bf16 v[88:91], v[52:55], v[196:199], v[88:91]\n" \
"ds_read_b64_tr_b16 v[8:9], v233 offset:0\n" \
"v_mfma_f32_16x16x32_bf16 v[92:95], v[56:59], v[192:195], v[92:95]\n" \
"ds_read_b64_tr_b16 v[10:11], v233 offset:2048\n" \
"v_mfma_f32_16x16x32_bf16 v[92:95], v[60:63], v[196:199], v[92:95]\n" \
"global_load_lds_dwordx4 v225, s[48:49] offset:-3072\n" \
"v_mfma_f32_16x16x32_bf16 v[124:127], v[56:59], v[200:203], v[124:127]\n" \
"ds_read_b128 v[192:195], v230 offset:0\n" \
"v_mfma_f32_16x16x32_bf16 v[124:127], v[60:63], v[204:207], v[124:127]\n" \
"ds_read_b128 v[196:199], v231 offset:0\n" \
"v_mfma_f32_16x16x32_bf16 v[120:123], v[48:51], v[200:203], v[120:123]\n" \
"ds_read_b64_tr_b16 v[12:13], v233 offset:8192\n" \
"v_mfma_f32_16x16x32_bf16 v[120:123], v[52:55], v[204:207], v[120:123]\n" \
"global_load_lds_dwordx4 v226, s[48:49] offset:-2048\n" \
"v_mfma_f32_16x16x32_bf16 v[116:119], v[40:43], v[200:203], v[116:119]\n" \
"ds_read_b64_tr_b16 v[14:15], v233 offset:10240\n" \
"v_mfma_f32_16x16x32_bf16 v[116:119], v[44:47], v[204:207], v[116:119]\n" \
"ds_read_b64_tr_b16 v[16:17], v232 offset:512\n" \
"v_mfma_f32_16x16x32_bf16 v[112:115], v[32:35], v[200:203], v[112:115]\n" \
"ds_read_b64_tr_b16 v[18:19], v232 offset:2560\n" \
"v_mfma_f32_16x16x32_bf16 v[112:115], v[36:39], v[204:207], v[112:115]\n" \
"global_load_lds_dwordx4 v227, s[48:49] offset:-1024\n" \
"v_mfma_f32_16x16x32_bf16 v[144:147], v[32:35], v[208:211], v[144:147]\n" \
"s_add_u32 s48, s48, 0x80\n" \
"s_addc_u32 s49, s49, 0\n" \
"ds_read_b128 v[200:203], v230 offset:2048\n" \
"v_mfma_f32_16x16x32_bf16 v[144:147], v[36:39], v[212:215], v[144:147]\n" \
"ds_read_b128 v[204:207], v231 offset:2048\n" \
"v_mfma_f32_16x16x32_bf16 v[148:151], v[40:43], v[208:211], v[148:151]\n" \
"ds_read_b64_tr_b16 v[20:21], v232 offset:8704\n" \
"v_mfma_f32_16x16x32_bf16 v[148:151], v[44:47], v[212:215], v[148:151]\n" \
"global_load_lds_dwordx4 v224, s[46:47] offset:0\n" \
"v_mfma_f32_16x16x32_bf16 v[152:155], v[48:51], v[208:211], v[152:155]\n" \
"ds_read_b64_tr_b16 v[22:23], v232 offset:10752\n" \
"v_mfma_f32_16x16x32_bf16 v[152:155], v[52:55], v[212:215], v[152:155]\n" \
"ds_read_b64_tr_b16 v[24:25], v233 offset:512\n" \
"v_mfma_f32_16x16x32_bf16 v[156:159], v[56:59], v[208:211], v[156:159]\n" \
"ds_read_b64_tr_b16 v[26:27], v233 offset:2560\n" \
"v_mfma_f32_16x16x32_bf16 v[156:159], v[60:63], v[212:215], v[156:159]\n" \
"global_load_lds_dwordx4 v225, s[46:47] offset:1024\n" \
"v_mfma_f32_16x16x32_bf16 v[188:191], v[56:59], v[216:219], v[188:191]\n" \
"ds_read_b128 v[208:211], v230 offset:4096\n" \
"v_mfma_f32_16x16x32_bf16 v[188:191], v[60:63], v[220:223], v[188:191]\n" \
"ds_read_b128 v[212:215], v231 offset:4096\n" \
"v_mfma_f32_16x16x32_bf16 v[184:187], v[48:51], v[216:219], v[184:187]\n" \
"ds_read_b64_tr_b16 v[28:29], v233 offset:8704\n" \
"v_mfma_f32_16x16x32_bf16 v[184:187], v[52:55], v[220:223], v[184:187]\n" \
"global_load_lds_dwordx4 v226, s[46:47] offset:2048\n" \
"v_mfma_f32_16x16x32_bf16 v[180:183], v[40:43], v[216:219], v[180:183]\n" \
"ds_read_b64_tr_b16 v[30:31], v233 offset:10752\n" \
"v_mfma_f32_16x16x32_bf16 v[180:183], v[44:47], v[220:223], v[180:183]\n" \
"v_mfma_f32_16x16x32_bf16 v[176:179], v[32:35], v[216:219], v[176:179]\n" \
"v_mfma_f32_16x16x32_bf16 v[176:179], v[36:39], v[220:223], v[176:179]\n" \
"s_waitcnt lgkmcnt(1)\n" \
"global_load_lds_dwordx4 v227, s[46:47] offset:3072\n" \
"v_mfma_f32_16x16x32_bf16 v[64:67], v[0:3], v[192:195], v[64:67]\n" \
"s_add_u32 s46, s46, 0x80\n" \
"s_addc_u32 s47, s47, 0\n" \
"ds_read_b128 v[216:219], v230 offset:6144\n" \
"v_mfma_f32_16x16x32_bf16 v[64:67], v[4:7], v[196:199], v[64:67]\n" \
"ds_read_b128 v[220:223], v231 offset:6144\n" \
"v_mfma_f32_16x16x32_bf16 v[68:71], v[8:11], v[192:195], v[68:71]\n" \
"ds_read_b64_tr_b16 v[32:33], v232 offset:1024\n" \
"v_mfma_f32_16x16x32_bf16 v[68:71], v[12:15], v[196:199], v[68:71]\n" \
"buffer_store_dwordx4 a[148:151], v248, s[56:59], s90 offen offset:1024 sc1\n" \
"v_mfma_f32_16x16x32_bf16 v[72:75], v[16:19], v[192:195], v[72:75]\n" \
"ds_read_b64_tr_b16 v[34:35], v232 offset:3072\n" \
"v_mfma_f32_16x16x32_bf16 v[72:75], v[20:23], v[196:199], v[72:75]\n" \
"s_waitcnt lgkmcnt(3)\n" \
"ds_read_b64_tr_b16 v[36:37], v232 offset:9216\n" \
"v_mfma_f32_16x16x32_bf16 v[76:79], v[24:27], v[192:195], v[76:79]\n" \
"ds_read_b64_tr_b16 v[38:39], v232 offset:11264\n" \
"v_mfma_f32_16x16x32_bf16 v[76:79], v[28:31], v[196:199], v[76:79]\n" \
"v_mfma_f32_16x16x32_bf16 v[108:111], v[24:27], v[200:203], v[108:111]\n" \
"ds_read_b64_tr_b16 v[40:41], v233 offset:1024\n" \
"v_mfma_f32_16x16x32_bf16 v[108:111], v[28:31], v[204:207], v[108:111]\n" \
"ds_read_b64_tr_b16 v[42:43], v233 offset:3072\n" \
"v_mfma_f32_16x16x32_bf16 v[104:107], v[16:19], v[200:203], v[104:107]\n" \
"ds_read_b64_tr_b16 v[44:45], v233 offset:9216\n" \
"v_mfma_f32_16x16x32_bf16 v[104:107], v[20:23], v[204:207], v[104:107]\n" \
"v_mfma_f32_16x16x32_bf16 v[100:103], v[8:11], v[200:203], v[100:103]\n" \
"ds_read_b64_tr_b16 v[46:47], v233 offset:11264\n" \
"v_mfma_f32_16x16x32_bf16 v[100:103], v[12:15], v[204:207], v[100:103]\n" \
"ds_read_b64_tr_b16 v[48:49], v232 offset:1536\n" \
"v_mfma_f32_16x16x32_bf16 v[96:99], v[0:3], v[200:203], v[96:99]\n" \
"s_add_u32 m0, s95, 20480\n" \
"ds_read_b64_tr_b16 v[50:51], v232 offset:3584\n" \
"v_mfma_f32_16x16x32_bf16 v[96:99], v[4:7], v[204:207], v[96:99]\n" \
"global_load_lds_dwordx4 v228, s[44:45] offset:-4096\n" \
"v_mfma_f32_16x16x32_bf16 v[128:131], v[0:3], v[208:211], v[128:131]\n" \
"ds_read_b64_tr_b16 v[52:53], v232 offset:9728\n" \
"v_mfma_f32_16x16x32_bf16 v[128:131], v[4:7], v[212:215], v[128:131]\n" \
"ds_read_b64_tr_b16 v[54:55], v232 offset:11776\n" \
"v_mfma_f32_16x16x32_bf16 v[132:135], v[8:11], v[208:211], v[132:135]\n" \
"ds_read_b64_tr_b16 v[56:57], v233 offset:1536\n" \
"v_mfma_f32_16x16x32_bf16 v[132:135], v[12:15], v[212:215], v[132:135]\n" \
"global_load_lds_dwordx4 v229, s[44:45] offset:-3072\n" \
"v_mfma_f32_16x16x32_bf16 v[136:139], v[16:19], v[208:211], v[136:139]\n" \
"s_add_u32 s44, s44, 0x80000\n" \
"s_addc_u32 s45, s45, 0\n" \
"ds_read_b64_tr_b16 v[58:59], v233 offset:3584\n" \
"v_mfma_f32_16x16x32_bf16 v[136:139], v[20:23], v[212:215], v[136:139]\n" \
"ds_read_b64_tr_b16 v[60:61], v233 offset:9728\n" \
"v_mfma_f32_16x16x32_bf16 v[140:143], v[24:27], v[208:211], v[140:143]\n" \
"ds_read_b64_tr_b16 v[62:63], v233 offset:11776\n" \
"v_mfma_f32_16x16x32_bf16 v[140:143], v[28:31], v[212:215], v[140:143]\n" \
"s_waitcnt lgkmcnt(3)\n" \
"global_load_lds_dwordx4 v228, s[42:43] offset:-2048\n" \
"v_mfma_f32_16x16x32_bf16 v[172:175], v[24:27], v[216:219], v[172:175]\n" \
"v_add_u32 v230, 0x8000, v230\n" \
"v_add_u32 v231, 0x8000, v231\n" \
"v_mfma_f32_16x16x32_bf16 v[172:175], v[28:31], v[220:223], v[172:175]\n" \
"v_add_u32 v232, 0x4000, v232\n" \
"v_add_u32 v233, 0x4000, v233\n" \
"v_mfma_f32_16x16x32_bf16 v[168:171], v[16:19], v[216:219], v[168:171]\n" \
"v_mfma_f32_16x16x32_bf16 v[168:171], v[20:23], v[220:223], v[168:171]\n" \
"global_load_lds_dwordx4 v229, s[42:43] offset:-1024\n" \
"v_mfma_f32_16x16x32_bf16 v[164:167], v[8:11], v[216:219], v[164:167]\n" \
"s_add_u32 s42, s42, 0x80000\n" \
"s_addc_u32 s43, s43, 0\n" \
"v_mfma_f32_16x16x32_bf16 v[164:167], v[12:15], v[220:223], v[164:167]\n" \
"s_waitcnt vmcnt(18) lgkmcnt(0)\n" \
"v_mfma_f32_16x16x32_bf16 v[160:163], v[0:3], v[216:219], v[160:163]\n" \
"v_mfma_f32_16x16x32_bf16 v[160:163], v[4:7], v[220:223], v[160:163]\n" \
"s_barrier\n" \
"v_mfma_f32_16x16x32_bf16 v[80:83], v[32:35], v[192:195], v[80:83]\n" \
"ds_read_b64_tr_b16 v[0:1], v232 offset:0\n" \
"v_mfma_f32_16x16x32_bf16 v[80:83], v[36:39], v[196:199], v[80:83]\n" \
"ds_read_b64_tr_b16 v[2:3], v232 offset:2048\n" \
"v_mfma_f32_16x16x32_bf16 v[84:87], v[40:43], v[192:195], v[84:87]\n" \
"s_add_u32 m0, s87, 69632\n" \
"ds_read_b64_tr_b16 v[4:5], v232 offset:8192\n" \
"v_mfma_f32_16x16x32_bf16 v[84:87], v[44:47], v[196:199], v[84:87]\n" \
"global_load_lds_dwordx4 v224, s[48:49] offset:-4096\n" \
"v_mfma_f32_16x16x32_bf16 v[88:91], v[48:51], v[192:195], v[88:91]\n" \
"ds_read_b64_tr_b16 v[6:7], v232 offset:10240\n" \
"v_mfma_f32_16x16x32_bf16 v[88:91], v[52:55], v[196:199], v[88:91]\n" \
"ds_read_b64_tr_b16 v[8:9], v233 offset:0\n" \
"v_mfma_f32_16x16x32_bf16 v[92:95], v[56:59], v[192:195], v[92:95]\n" \
"ds_read_b64_tr_b16 v[10:11], v233 offset:2048\n" \
"v_mfma_f32_16x16x32_bf16 v[92:95], v[60:63], v[196:199], v[92:95]\n" \
"global_load_lds_dwordx4 v225, s[48:49] offset:-3072\n" \
"v_mfma_f32_16x16x32_bf16 v[124:127], v[56:59], v[200:203], v[124:127]\n" \
"ds_read_b128 v[192:195], v230 offset:0\n" \
"v_mfma_f32_16x16x32_bf16 v[124:127], v[60:63], v[204:207], v[124:127]\n" \
"ds_read_b128 v[196:199], v231 offset:0\n" \
"v_mfma_f32_16x16x32_bf16 v[120:123], v[48:51], v[200:203], v[120:123]\n" \
"ds_read_b64_tr_b16 v[12:13], v233 offset:8192\n" \
"v_mfma_f32_16x16x32_bf16 v[120:123], v[52:55], v[204:207], v[120:123]\n" \
"global_load_lds_dwordx4 v226, s[48:49] offset:-2048\n" \
"v_mfma_f32_16x16x32_bf16 v[116:119], v[40:43], v[200:203], v[116:119]\n" \
"ds_read_b64_tr_b16 v[14:15], v233 offset:10240\n" \
"v_mfma_f32_16x16x32_bf16 v[116:119], v[44:47], v[204:207], v[116:119]\n" \
"ds_read_b64_tr_b16 v[16:17], v232 offset:512\n" \
"v_mfma_f32_16x16x32_bf16 v[112:115], v[32:35], v[200:203], v[112:115]\n" \
"ds_read_b64_tr_b16 v[18:19], v232 offset:2560\n" \
"v_mfma_f32_16x16x32_bf16 v[112:115], v[36:39], v[204:207], v[112:115]\n" \
"global_load_lds_dwordx4 v227, s[48:49] offset:-1024\n" \
"v_mfma_f32_16x16x32_bf16 v[144:147], v[32:35], v[208:211], v[144:147]\n" \
"s_add_u32 s48, s48, 0x80\n" \
"s_addc_u32 s49, s49, 0\n" \
"ds_read_b128 v[200:203], v230 offset:2048\n" \
"v_mfma_f32_16x16x32_bf16 v[144:147], v[36:39], v[212:215], v[144:147]\n" \
"ds_read_b128 v[204:207], v231 offset:2048\n" \
"v_mfma_f32_16x16x32_bf16 v[148:151], v[40:43], v[208:211], v[148:151]\n" \
"ds_read_b64_tr_b16 v[20:21], v232 offset:8704\n" \
"v_mfma_f32_16x16x32_bf16 v[148:151], v[44:47], v[212:215], v[148:151]\n" \
"global_load_lds_dwordx4 v224, s[46:47] offset:0\n" \
"v_mfma_f32_16x16x32_bf16 v[152:155], v[48:51], v[208:211], v[152:155]\n" \
"ds_read_b64_tr_b16 v[22:23], v232 offset:10752\n" \
"v_mfma_f32_16x16x32_bf16 v[152:155], v[52:55], v[212:215], v[152:155]\n" \
"ds_read_b64_tr_b16 v[24:25], v233 offset:512\n" \
"v_mfma_f32_16x16x32_bf16 v[156:159], v[56:59], v[208:211], v[156:159]\n" \
"ds_read_b64_tr_b16 v[26:27], v233 offset:2560\n" \
"v_mfma_f32_16x16x32_bf16 v[156:159], v[60:63], v[212:215], v[156:159]\n" \
"global_load_lds_dwordx4 v225, s[46:47] offset:1024\n" \
"v_mfma_f32_16x16x32_bf16 v[188:191], v[56:59], v[216:219], v[188:191]\n" \
"ds_read_b128 v[208:211], v230 offset:4096\n" \
"v_mfma_f32_16x16x32_bf16 v[188:191], v[60:63], v[220:223], v[188:191]\n" \
"ds_read_b128 v[212:215], v231 offset:4096\n" \
"v_mfma_f32_16x16x32_bf16 v[184:187], v[48:51], v[216:219], v[184:187]\n" \
"ds_read_b64_tr_b16 v[28:29], v233 offset:8704\n" \
"v_mfma_f32_16x16x32_bf16 v[184:187], v[52:55], v[220:223], v[184:187]\n" \
"global_load_lds_dwordx4 v226, s[46:47] offset:2048\n" \
"v_mfma_f32_16x16x32_bf16 v[180:183], v[40:43], v[216:219], v[180:183]\n" \
"ds_read_b64_tr_b16 v[30:31], v233 offset:10752\n" \
"v_mfma_f32_16x16x32_bf16 v[180:183], v[44:47], v[220:223], v[180:183]\n" \
"v_mfma_f32_16x16x32_bf16 v[176:179], v[32:35], v[216:219], v[176:179]\n" \
"v_mfma_f32_16x16x32_bf16 v[176:179], v[36:39], v[220:223], v[176:179]\n" \
"s_waitcnt lgkmcnt(1)\n" \
"global_load_lds_dwordx4 v227, s[46:47] offset:3072\n" \
"v_mfma_f32_16x16x32_bf16 v[64:67], v[0:3], v[192:195], v[64:67]\n" \
"s_add_u32 s46, s46, 0x80\n" \
"s_addc_u32 s47, s47, 0\n" \
"ds_read_b128 v[216:219], v230 offset:6144\n" \
"v_mfma_f32_16x16x32_bf16 v[64:67], v[4:7], v[196:199], v[64:67]\n" \
"s_mul_i32 s77, s62, 557824\n" \
"ds_read_b128 v[220:223], v231 offset:6144\n" \
"s_add_u32 s77, s77, s95\n" \
"v_mfma_f32_16x16x32_bf16 v[68:71], v[8:11], v[192:195], v[68:71]\n" \
"s_add_u32 s36, s28, s77\n" \
"s_mov_b32 s37, s29\n" \
"ds_read_b64_tr_b16 v[32:33], v232 offset:1024\n" \
"v_mfma_f32_16x16x32_bf16 v[68:71], v[12:15], v[196:199], v[68:71]\n" \
"buffer_store_dwordx4 a[152:155], v248, s[56:59], s90 offen offset:2048 sc1\n" \
"s_add_u32 s77, s77, 17850368\n" \
"s_add_u32 s38, s28, s77\n" \
"s_mov_b32 s39, s29\n" \
"s_and_b32 s74, s63, 1\n" \
"v_mfma_f32_16x16x32_bf16 v[72:75], v[16:19], v[192:195], v[72:75]\n" \
"s_lshl_b32 s74, s74, 15\n" \
"s_xor_b32 s74, s74, 0x18000\n" \
"ds_read_b64_tr_b16 v[34:35], v232 offset:3072\n" \
"v_mfma_f32_16x16x32_bf16 v[72:75], v[20:23], v[196:199], v[72:75]\n" \
"s_waitcnt lgkmcnt(3)\n" \
"ds_read_b64_tr_b16 v[36:37], v232 offset:9216\n" \
"v_add_u32 v247, s74, v248\n" \
"v_mfma_f32_16x16x32_bf16 v[76:79], v[24:27], v[192:195], v[76:79]\n" \
"s_add_u32 s74, s74, s95\n" \
"s_lshr_b32 s83, s61, 1\n" \
"ds_read_b64_tr_b16 v[38:39], v232 offset:11264\n" \
"v_mfma_f32_16x16x32_bf16 v[76:79], v[28:31], v[196:199], v[76:79]\n" \
"buffer_store_dwordx4 a[156:159], v248, s[56:59], s90 offen offset:3072 sc1\n" \
"s_lshl_b32 s75, s63, 2\n" \
"s_add_u32 s77, s83, s75\n" \
"s_mul_i32 s77, s77, 266240\n" \
"s_sub_u32 s78, s83, s75\n" \
"v_mfma_f32_16x16x32_bf16 v[108:111], v[24:27], v[200:203], v[108:111]\n" \
"s_add_u32 s78, s78, 60\n" \
"s_mul_i32 s78, s78, 266240\n" \
"ds_read_b64_tr_b16 v[40:41], v233 offset:1024\n" \
"v_mfma_f32_16x16x32_bf16 v[108:111], v[28:31], v[204:207], v[108:111]\n" \
"s_and_b32 s82, s61, 1\n" \
"ds_read_b64_tr_b16 v[42:43], v233 offset:3072\n" \
"v_mfma_f32_16x16x32_bf16 v[104:107], v[16:19], v[200:203], v[104:107]\n" \
"s_cmp_eq_u32 s82, 0\n" \
"s_cselect_b32 s16, 0, 0xff00\n" \
"ds_read_b64_tr_b16 v[44:45], v233 offset:9216\n" \
"v_mfma_f32_16x16x32_bf16 v[104:107], v[20:23], v[204:207], v[104:107]\n" \
"buffer_load_dword v244, v245, s[56:59], s91 offen sc1\n" \
"v_mfma_f32_16x16x32_bf16 v[100:103], v[8:11], v[200:203], v[100:103]\n" \
"s_mov_b32 s17, s16\n" \
"s_lshl_b32 s75, s82, 8\n" \
"ds_read_b64_tr_b16 v[46:47], v233 offset:11264\n" \
"v_mfma_f32_16x16x32_bf16 v[100:103], v[12:15], v[204:207], v[100:103]\n" \
"s_lshl_b32 s76, s62, 13\n" \
"s_add_u32 s75, s75, s76\n" \
"ds_read_b64_tr_b16 v[48:49], v232 offset:1536\n" \
"v_mfma_f32_16x16x32_bf16 v[96:99], v[0:3], v[200:203], v[96:99]\n" \
"s_add_u32 m0, s95, 36864\n" \
"s_add_u32 s75, s75, 35700736\n" \
"ds_read_b64_tr_b16 v[50:51], v232 offset:3584\n" \
"v_mfma_f32_16x16x32_bf16 v[96:99], v[4:7], v[204:207], v[96:99]\n" \
"s_add_u32 s77, s77, s75\n" \
"global_load_lds_dwordx4 v228, s[44:45] offset:-4096\n" \
"v_mfma_f32_16x16x32_bf16 v[128:131], v[0:3], v[208:211], v[128:131]\n" \
"s_add_u32 s78, s78, s75\n" \
"s_mul_i32 s83, s83, 9216\n" \
"ds_read_b64_tr_b16 v[52:53], v232 offset:9728\n" \
"v_mfma_f32_16x16x32_bf16 v[128:131], v[4:7], v[212:215], v[128:131]\n" \
"s_lshl_b32 s75, s82, 12\n" \
"s_add_u32 s83, s83, s75\n" \
"ds_read_b64_tr_b16 v[54:55], v232 offset:11776\n" \
"v_mfma_f32_16x16x32_bf16 v[132:135], v[8:11], v[208:211], v[132:135]\n" \
"s_add_u32 s83, s83, 4096\n" \
"s_lshl_b32 s82, s82, 14\n" \
"ds_read_b64_tr_b16 v[56:57], v233 offset:1536\n" \
"v_mfma_f32_16x16x32_bf16 v[132:135], v[12:15], v[212:215], v[132:135]\n" \
"global_load_lds_dwordx4 v229, s[44:45] offset:-3072\n" \
"v_mfma_f32_16x16x32_bf16 v[136:139], v[16:19], v[208:211], v[136:139]\n" \
"s_add_u32 s44, s44, 0x80000\n" \
"s_addc_u32 s45, s45, 0\n" \
"ds_read_b64_tr_b16 v[58:59], v233 offset:3584\n" \
"v_mfma_f32_16x16x32_bf16 v[136:139], v[20:23], v[212:215], v[136:139]\n" \
"s_cmp_eq_u32 s63, 7\n" \
"s_cselect_b32 s82, 0, s82\n" \
"ds_read_b64_tr_b16 v[60:61], v233 offset:9728\n" \
"v_mfma_f32_16x16x32_bf16 v[140:143], v[24:27], v[208:211], v[140:143]\n" \
"s_mov_b32 s75, s63\n" \
"s_sub_u32 s76, 7, s63\n" \
"ds_read_b64_tr_b16 v[62:63], v233 offset:11776\n" \
"v_mfma_f32_16x16x32_bf16 v[140:143], v[28:31], v[212:215], v[140:143]\n" \
"s_waitcnt lgkmcnt(3)\n" \
"global_load_lds_dwordx4 v228, s[42:43] offset:-2048\n" \
"s_mov_b32 s73, s95\n" \
"v_mfma_f32_16x16x32_bf16 v[172:175], v[24:27], v[216:219], v[172:175]\n" \
"s_movk_i32 s94, 0x207\n" \
"s_mov_b64 s[40:41], s[14:15]\n" \
"s_mov_b32 s80, 0\n" \
"v_mfma_f32_16x16x32_bf16 v[172:175], v[28:31], v[220:223], v[172:175]\n" \
"s_mov_b32 s81, 0\n" \
"v_add_u32 v230, 0x8000, v230\n" \
"v_add_u32 v231, 0x8000, v231\n" \
"v_mfma_f32_16x16x32_bf16 v[168:171], v[16:19], v[216:219], v[168:171]\n" \
"s_cmp_eq_u32 s63, 7\n" \
"s_cselect_b64 s[40:41], s[18:19], s[40:41]\n" \
"s_cselect_b32 s80, 12, s80\n" \
"v_mfma_f32_16x16x32_bf16 v[168:171], v[20:23], v[220:223], v[168:171]\n" \
"global_load_lds_dwordx4 v229, s[42:43] offset:-1024\n" \
"v_add_u32 v232, 0xffff4000, v232\n" \
"v_mfma_f32_16x16x32_bf16 v[164:167], v[8:11], v[216:219], v[164:167]\n" \
"s_add_u32 s42, s42, 0x80000\n" \
"s_addc_u32 s43, s43, 0\n" \
"v_add_u32 v233, 0xffff4000, v233\n" \
"v_mfma_f32_16x16x32_bf16 v[164:167], v[12:15], v[220:223], v[164:167]\n" \
"s_waitcnt vmcnt(20) lgkmcnt(0)\n" \
"v_mfma_f32_16x16x32_bf16 v[160:163], v[0:3], v[216:219], v[160:163]\n" \
"s_cmp_eq_u32 s63, 6\n" \
"s_cselect_b64 s[40:41], s[22:23], s[40:41]\n" \
"s_cselect_b32 s80, 12, s80\n" \
"v_mfma_f32_16x16x32_bf16 v[160:163], v[4:7], v[220:223], v[160:163]\n" \
"s_barrier\n" \
"v_mfma_f32_16x16x32_bf16 v[80:83], v[32:35], v[192:195], v[80:83]\n" \
"ds_read_b64_tr_b16 v[0:1], v232 offset:0\n" \
"v_mfma_f32_16x16x32_bf16 v[80:83], v[36:39], v[196:199], v[80:83]\n" \
"ds_read_b64_tr_b16 v[2:3], v232 offset:2048\n" \
"v_mfma_f32_16x16x32_bf16 v[84:87], v[40:43], v[192:195], v[84:87]\n" \
"s_add_u32 m0, s87, 102400\n" \
"ds_read_b64_tr_b16 v[4:5], v232 offset:8192\n" \
"v_mfma_f32_16x16x32_bf16 v[84:87], v[44:47], v[196:199], v[84:87]\n" \
"global_load_lds_dwordx4 v224, s[48:49] offset:-4096\n" \
"v_mfma_f32_16x16x32_bf16 v[88:91], v[48:51], v[192:195], v[88:91]\n" \
"ds_read_b64_tr_b16 v[6:7], v232 offset:10240\n" \
"v_mfma_f32_16x16x32_bf16 v[88:91], v[52:55], v[196:199], v[88:91]\n" \
"ds_read_b64_tr_b16 v[8:9], v233 offset:0\n" \
"v_mfma_f32_16x16x32_bf16 v[92:95], v[56:59], v[192:195], v[92:95]\n" \
"ds_read_b64_tr_b16 v[10:11], v233 offset:2048\n" \
"v_mfma_f32_16x16x32_bf16 v[92:95], v[60:63], v[196:199], v[92:95]\n" \
"global_load_lds_dwordx4 v225, s[48:49] offset:-3072\n" \
"v_mfma_f32_16x16x32_bf16 v[124:127], v[56:59], v[200:203], v[124:127]\n" \
"ds_read_b128 v[192:195], v230 offset:0\n" \
"v_mfma_f32_16x16x32_bf16 v[124:127], v[60:63], v[204:207], v[124:127]\n" \
"ds_read_b128 v[196:199], v231 offset:0\n" \
"v_mfma_f32_16x16x32_bf16 v[120:123], v[48:51], v[200:203], v[120:123]\n" \
"ds_read_b64_tr_b16 v[12:13], v233 offset:8192\n" \
"v_mfma_f32_16x16x32_bf16 v[120:123], v[52:55], v[204:207], v[120:123]\n" \
"global_load_lds_dwordx4 v226, s[48:49] offset:-2048\n" \
"v_mfma_f32_16x16x32_bf16 v[116:119], v[40:43], v[200:203], v[116:119]\n" \
"ds_read_b64_tr_b16 v[14:15], v233 offset:10240\n" \
"v_mfma_f32_16x16x32_bf16 v[116:119], v[44:47], v[204:207], v[116:119]\n" \
"ds_read_b64_tr_b16 v[16:17], v232 offset:512\n" \
"v_mfma_f32_16x16x32_bf16 v[112:115], v[32:35], v[200:203], v[112:115]\n" \
"ds_read_b64_tr_b16 v[18:19], v232 offset:2560\n" \
"v_mfma_f32_16x16x32_bf16 v[112:115], v[36:39], v[204:207], v[112:115]\n" \
"global_load_lds_dwordx4 v227, s[48:49] offset:-1024\n" \
"v_mfma_f32_16x16x32_bf16 v[144:147], v[32:35], v[208:211], v[144:147]\n" \
"s_add_u32 s48, s48, 0x80\n" \
"s_addc_u32 s49, s49, 0\n" \
"ds_read_b128 v[200:203], v230 offset:2048\n" \
"v_mfma_f32_16x16x32_bf16 v[144:147], v[36:39], v[212:215], v[144:147]\n" \
"ds_read_b128 v[204:207], v231 offset:2048\n" \
"v_mfma_f32_16x16x32_bf16 v[148:151], v[40:43], v[208:211], v[148:151]\n" \
"ds_read_b64_tr_b16 v[20:21], v232 offset:8704\n" \
"v_mfma_f32_16x16x32_bf16 v[148:151], v[44:47], v[212:215], v[148:151]\n" \
"global_load_lds_dwordx4 v224, s[46:47] offset:0\n" \
"v_mfma_f32_16x16x32_bf16 v[152:155], v[48:51], v[208:211], v[152:155]\n" \
"ds_read_b64_tr_b16 v[22:23], v232 offset:10752\n" \
"v_mfma_f32_16x16x32_bf16 v[152:155], v[52:55], v[212:215], v[152:155]\n" \
"ds_read_b64_tr_b16 v[24:25], v233 offset:512\n" \
"v_mfma_f32_16x16x32_bf16 v[156:159], v[56:59], v[208:211], v[156:159]\n" \
"ds_read_b64_tr_b16 v[26:27], v233 offset:2560\n" \
"v_mfma_f32_16x16x32_bf16 v[156:159], v[60:63], v[212:215], v[156:159]\n" \
"global_load_lds_dwordx4 v225, s[46:47] offset:1024\n" \
"v_mfma_f32_16x16x32_bf16 v[188:191], v[56:59], v[216:219], v[188:191]\n" \
"ds_read_b128 v[208:211], v230 offset:4096\n" \
"v_mfma_f32_16x16x32_bf16 v[188:191], v[60:63], v[220:223], v[188:191]\n" \
"ds_read_b128 v[212:215], v231 offset:4096\n" \
"v_mfma_f32_16x16x32_bf16 v[184:187], v[48:51], v[216:219], v[184:187]\n" \
"ds_read_b64_tr_b16 v[28:29], v233 offset:8704\n" \
"v_mfma_f32_16x16x32_bf16 v[184:187], v[52:55], v[220:223], v[184:187]\n" \
"Lpg4398_%=:\n" \
"s_cbranch_execz Lpg5573_%=\n" \
"global_load_lds_dwordx4 v226, s[46:47] offset:2048\n" \
"v_mfma_f32_16x16x32_bf16 v[180:183], v[40:43], v[216:219], v[180:183]\n" \
"ds_read_b64_tr_b16 v[30:31], v233 offset:10752\n" \
"v_mfma_f32_16x16x32_bf16 v[180:183], v[44:47], v[220:223], v[180:183]\n" \
"s_cmp_eq_u32 s63, 5\n" \
"s_cselect_b64 s[40:41], s[18:19], s[40:41]\n" \
"s_cselect_b32 s80, 4, s80\n" \
"v_mfma_f32_16x16x32_bf16 v[176:179], v[32:35], v[216:219], v[176:179]\n" \
"s_cmp_eq_u32 s63, 5\n" \
"s_cselect_b32 s81, 384, s81\n" \
"v_mfma_f32_16x16x32_bf16 v[176:179], v[36:39], v[220:223], v[176:179]\n" \
"s_waitcnt lgkmcnt(1)\n" \
"global_load_lds_dwordx4 v227, s[46:47] offset:3072\n" \
"v_mfma_f32_16x16x32_bf16 v[64:67], v[0:3], v[192:195], v[64:67]\n" \
"s_add_u32 s46, s46, 0x80\n" \
"s_addc_u32 s47, s47, 0\n" \
"ds_read_b128 v[216:219], v230 offset:6144\n" \
"v_mfma_f32_16x16x32_bf16 v[64:67], v[4:7], v[196:199], v[64:67]\n" \
"ds_read_b128 v[220:223], v231 offset:6144\n" \
"s_add_u32 s90, s88, 278528\n" \
"v_mfma_f32_16x16x32_bf16 v[68:71], v[8:11], v[192:195], v[68:71]\n" \
"ds_read_b64_tr_b16 v[32:33], v232 offset:1024\n" \
"v_mfma_f32_16x16x32_bf16 v[68:71], v[12:15], v[196:199], v[68:71]\n" \
"buffer_store_dwordx4 a[64:67], v249, s[56:59], s90 offen offset:0 sc1\n" \
"s_cmp_eq_u32 s63, 4\n" \
"s_cselect_b64 s[40:41], s[20:21], s[40:41]\n" \
"s_cselect_b32 s80, 8, s80\n" \
"v_mfma_f32_16x16x32_bf16 v[72:75], v[16:19], v[192:195], v[72:75]\n" \
"ds_read_b64_tr_b16 v[34:35], v232 offset:3072\n" \
"v_mfma_f32_16x16x32_bf16 v[72:75], v[20:23], v[196:199], v[72:75]\n" \
"s_waitcnt lgkmcnt(3)\n" \
"ds_read_b64_tr_b16 v[36:37], v232 offset:9216\n" \
"v_mfma_f32_16x16x32_bf16 v[76:79], v[24:27], v[192:195], v[76:79]\n" \
"ds_read_b128 a[64:67], v254\n" \
"ds_read_b64_tr_b16 v[38:39], v232 offset:11264\n" \
"v_mfma_f32_16x16x32_bf16 v[76:79], v[28:31], v[196:199], v[76:79]\n" \
"s_cmp_eq_u32 s63, 2\n" \
"s_cselect_b64 s[40:41], s[24:25], s[40:41]\n" \
"s_cselect_b32 s80, 4, s80\n" \
"v_mfma_f32_16x16x32_bf16 v[108:111], v[24:27], v[200:203], v[108:111]\n" \
"s_add_u32 s81, s81, s62\n" \
"s_lshl_b32 s81, s81, 2\n" \
"ds_read_b64_tr_b16 v[40:41], v233 offset:1024\n" \
"v_mfma_f32_16x16x32_bf16 v[108:111], v[28:31], v[204:207], v[108:111]\n" \
"s_add_u32 s81, s81, s61\n" \
"s_lshl_b32 s81, s81, 12\n" \
"ds_read_b64_tr_b16 v[42:43], v233 offset:3072\n" \
"v_mfma_f32_16x16x32_bf16 v[104:107], v[16:19], v[200:203], v[104:107]\n" \
"s_add_u32 s40, s40, s81\n" \
"s_addc_u32 s41, s41, 0\n" \
"ds_read_b64_tr_b16 v[44:45], v233 offset:9216\n" \
"v_mfma_f32_16x16x32_bf16 v[104:107], v[20:23], v[204:207], v[104:107]\n" \
"s_min_u32 s81, s80, 1\n" \
"s_lshl_b32 s81, s81, 2\n" \
"v_mul_u32_u24 v252, s81, v248\n" \
"v_mfma_f32_16x16x32_bf16 v[100:103], v[8:11], v[200:203], v[100:103]\n" \
"ds_read_b128 a[68:71], v254\n" \
"ds_read_b64_tr_b16 v[46:47], v233 offset:11264\n" \
"v_mfma_f32_16x16x32_bf16 v[100:103], v[12:15], v[204:207], v[100:103]\n" \
"ds_read_b64_tr_b16 v[48:49], v232 offset:1536\n" \
"v_mfma_f32_16x16x32_bf16 v[96:99], v[0:3], v[200:203], v[96:99]\n" \
"s_add_u32 m0, s95, 53248\n" \
"ds_read_b64_tr_b16 v[50:51], v232 offset:3584\n" \
"v_mfma_f32_16x16x32_bf16 v[96:99], v[4:7], v[204:207], v[96:99]\n" \
"global_load_lds_dwordx4 v228, s[44:45] offset:-4096\n" \
"v_mfma_f32_16x16x32_bf16 v[128:131], v[0:3], v[208:211], v[128:131]\n" \
"ds_read_b64_tr_b16 v[52:53], v232 offset:9728\n" \
"v_mfma_f32_16x16x32_bf16 v[128:131], v[4:7], v[212:215], v[128:131]\n" \
"ds_read_b64_tr_b16 v[54:55], v232 offset:11776\n" \
"v_mfma_f32_16x16x32_bf16 v[132:135], v[8:11], v[208:211], v[132:135]\n" \
"ds_read_b64_tr_b16 v[56:57], v233 offset:1536\n" \
"v_mfma_f32_16x16x32_bf16 v[132:135], v[12:15], v[212:215], v[132:135]\n" \
"global_load_lds_dwordx4 v229, s[44:45] offset:-3072\n" \
"v_mfma_f32_16x16x32_bf16 v[136:139], v[16:19], v[208:211], v[136:139]\n" \
"ds_read_b64_tr_b16 v[58:59], v233 offset:3584\n" \
"v_mfma_f32_16x16x32_bf16 v[136:139], v[20:23], v[212:215], v[136:139]\n" \
"ds_read_b64_tr_b16 v[60:61], v233 offset:9728\n" \
"v_mfma_f32_16x16x32_bf16 v[140:143], v[24:27], v[208:211], v[140:143]\n" \
"ds_read_b64_tr_b16 v[62:63], v233 offset:11776\n" \
"v_mfma_f32_16x16x32_bf16 v[140:143], v[28:31], v[212:215], v[140:143]\n" \
"s_waitcnt lgkmcnt(3)\n" \
"global_load_lds_dwordx4 v228, s[42:43] offset:-2048\n" \
"v_mfma_f32_16x16x32_bf16 v[172:175], v[24:27], v[216:219], v[172:175]\n" \
"v_add_u32 v230, 0xffff0000, v230\n" \
"v_add_u32 v231, 0xffff0000, v231\n" \
"v_mfma_f32_16x16x32_bf16 v[172:175], v[28:31], v[220:223], v[172:175]\n" \
"v_add_u32 v232, 0x4000, v232\n" \
"v_add_u32 v233, 0x4000, v233\n" \
"v_mfma_f32_16x16x32_bf16 v[168:171], v[16:19], v[216:219], v[168:171]\n" \
"v_mfma_f32_16x16x32_bf16 v[168:171], v[20:23], v[220:223], v[168:171]\n" \
"global_load_lds_dwordx4 v229, s[42:43] offset:-1024\n" \
"v_mfma_f32_16x16x32_bf16 v[164:167], v[8:11], v[216:219], v[164:167]\n" \
"v_mfma_f32_16x16x32_bf16 v[164:167], v[12:15], v[220:223], v[164:167]\n" \
"s_waitcnt vmcnt(20) lgkmcnt(0)\n" \
"v_mfma_f32_16x16x32_bf16 v[160:163], v[0:3], v[216:219], v[160:163]\n" \
"v_mfma_f32_16x16x32_bf16 v[160:163], v[4:7], v[220:223], v[160:163]\n" \
"s_barrier\n" \
"v_mfma_f32_16x16x32_bf16 v[80:83], v[32:35], v[192:195], v[80:83]\n" \
"s_waitcnt vmcnt(17)\n" \
"ds_read_b64_tr_b16 v[0:1], v232 offset:0\n" \
"v_cmp_ne_u32 vcc, s60, v244\n" \
"v_mfma_f32_16x16x32_bf16 v[80:83], v[36:39], v[196:199], v[80:83]\n" \
"ds_read_b64_tr_b16 v[2:3], v232 offset:2048\n" \
"v_mfma_f32_16x16x32_bf16 v[84:87], v[40:43], v[192:195], v[84:87]\n" \
"s_add_u32 m0, s87, 135168\n" \
"ds_read_b64_tr_b16 v[4:5], v232 offset:8192\n" \
"v_mfma_f32_16x16x32_bf16 v[84:87], v[44:47], v[196:199], v[84:87]\n" \
"s_and_b32 vcc_hi, vcc_lo, 255\n" \
"s_cbranch_scc1 Lpl1_%=\n" \
"Lpr1_%=:\n" \
"global_load_lds_dwordx4 v224, s[48:49] offset:-4096\n" \
"v_mfma_f32_16x16x32_bf16 v[88:91], v[48:51], v[192:195], v[88:91]\n" \
"s_add_u32 s79, s91, 12288\n" \
"ds_read_b64_tr_b16 v[6:7], v232 offset:10240\n" \
"v_mfma_f32_16x16x32_bf16 v[88:91], v[52:55], v[196:199], v[88:91]\n" \
"ds_read_b64_tr_b16 v[8:9], v233 offset:0\n" \
"v_mfma_f32_16x16x32_bf16 v[92:95], v[56:59], v[192:195], v[92:95]\n" \
"ds_read_b64_tr_b16 v[10:11], v233 offset:2048\n" \
"v_mfma_f32_16x16x32_bf16 v[92:95], v[60:63], v[196:199], v[92:95]\n" \
"global_load_lds_dwordx4 v225, s[48:49] offset:-3072\n" \
"v_mfma_f32_16x16x32_bf16 v[124:127], v[56:59], v[200:203], v[124:127]\n" \
"ds_read_b128 v[192:195], v230 offset:0\n" \
"v_mfma_f32_16x16x32_bf16 v[124:127], v[60:63], v[204:207], v[124:127]\n" \
"ds_read_b128 v[196:199], v231 offset:0\n" \
"v_mfma_f32_16x16x32_bf16 v[120:123], v[48:51], v[200:203], v[120:123]\n" \
"ds_read_b64_tr_b16 v[12:13], v233 offset:8192\n" \
"v_mfma_f32_16x16x32_bf16 v[120:123], v[52:55], v[204:207], v[120:123]\n" \
"global_load_lds_dwordx4 v226, s[48:49] offset:-2048\n" \
"v_mfma_f32_16x16x32_bf16 v[116:119], v[40:43], v[200:203], v[116:119]\n" \
"ds_read_b64_tr_b16 v[14:15], v233 offset:10240\n" \
"v_mfma_f32_16x16x32_bf16 v[116:119], v[44:47], v[204:207], v[116:119]\n" \
"ds_read_b64_tr_b16 v[16:17], v232 offset:512\n" \
"v_mfma_f32_16x16x32_bf16 v[112:115], v[32:35], v[200:203], v[112:115]\n" \
"ds_read_b64_tr_b16 v[18:19], v232 offset:2560\n" \
"v_mfma_f32_16x16x32_bf16 v[112:115], v[36:39], v[204:207], v[112:115]\n" \
"global_load_lds_dwordx4 v227, s[48:49] offset:-1024\n" \
"v_mfma_f32_16x16x32_bf16 v[144:147], v[32:35], v[208:211], v[144:147]\n" \
"ds_read_b128 v[200:203], v230 offset:2048\n" \
"v_mfma_f32_16x16x32_bf16 v[144:147], v[36:39], v[212:215], v[144:147]\n" \
"ds_read_b128 v[204:207], v231 offset:2048\n" \
"v_mfma_f32_16x16x32_bf16 v[148:151], v[40:43], v[208:211], v[148:151]\n" \
"ds_read_b64_tr_b16 v[20:21], v232 offset:8704\n" \
"v_mfma_f32_16x16x32_bf16 v[148:151], v[44:47], v[212:215], v[148:151]\n" \
"global_load_lds_dwordx4 v224, s[46:47] offset:0\n" \
"v_mfma_f32_16x16x32_bf16 v[152:155], v[48:51], v[208:211], v[152:155]\n" \
"ds_read_b64_tr_b16 v[22:23], v232 offset:10752\n" \
"v_mfma_f32_16x16x32_bf16 v[152:155], v[52:55], v[212:215], v[152:155]\n" \
"ds_read_b64_tr_b16 v[24:25], v233 offset:512\n" \
"v_mfma_f32_16x16x32_bf16 v[156:159], v[56:59], v[208:211], v[156:159]\n" \
"ds_read_b64_tr_b16 v[26:27], v233 offset:2560\n" \
"v_mfma_f32_16x16x32_bf16 v[156:159], v[60:63], v[212:215], v[156:159]\n" \
"global_load_lds_dwordx4 v225, s[46:47] offset:1024\n" \
"v_mfma_f32_16x16x32_bf16 v[188:191], v[56:59], v[216:219], v[188:191]\n" \
"ds_read_b128 v[208:211], v230 offset:4096\n" \
"v_mfma_f32_16x16x32_bf16 v[188:191], v[60:63], v[220:223], v[188:191]\n" \
"ds_read_b128 v[212:215], v231 offset:4096\n" \
"v_mfma_f32_16x16x32_bf16 v[184:187], v[48:51], v[216:219], v[184:187]\n" \
"ds_read_b64_tr_b16 v[28:29], v233 offset:8704\n" \
"v_mfma_f32_16x16x32_bf16 v[184:187], v[52:55], v[220:223], v[184:187]\n" \
"global_load_lds_dwordx4 v226, s[46:47] offset:2048\n" \
"v_mfma_f32_16x16x32_bf16 v[180:183], v[40:43], v[216:219], v[180:183]\n" \
"ds_read_b64_tr_b16 v[30:31], v233 offset:10752\n" \
"v_mfma_f32_16x16x32_bf16 v[180:183], v[44:47], v[220:223], v[180:183]\n" \
"v_mfma_f32_16x16x32_bf16 v[176:179], v[32:35], v[216:219], v[176:179]\n" \
"v_mfma_f32_16x16x32_bf16 v[176:179], v[36:39], v[220:223], v[176:179]\n" \
"s_waitcnt lgkmcnt(1)\n" \
"global_load_lds_dwordx4 v227, s[46:47] offset:3072\n" \
"v_mfma_f32_16x16x32_bf16 v[64:67], v[0:3], v[192:195], v[64:67]\n" \
"ds_read_b128 v[216:219], v230 offset:6144\n" \
"v_mfma_f32_16x16x32_bf16 v[64:67], v[4:7], v[196:199], v[64:67]\n" \
"ds_read_b128 v[220:223], v231 offset:6144\n" \
"v_mfma_f32_16x16x32_bf16 v[68:71], v[8:11], v[192:195], v[68:71]\n" \
"ds_read_b128 v[240:243], v254\n" \
"ds_read_b64_tr_b16 v[32:33], v232 offset:1024\n" \
"v_mfma_f32_16x16x32_bf16 v[68:71], v[12:15], v[196:199], v[68:71]\n" \
"buffer_store_dwordx4 a[96:99], v249, s[56:59], s90 offen offset:256 sc1\n" \
"v_and_b32 v238, 0xf0, v248\n" \
"v_mov_b32 v239, 0x3f803f80\n" \
"v_cmp_eq_u32 vcc, 32, v238\n" \
"v_cmp_eq_u32 s[64:65], 48, v238\n" \
"v_mfma_f32_16x16x32_bf16 v[72:75], v[16:19], v[192:195], v[72:75]\n" \
"ds_read_b64_tr_b16 v[34:35], v232 offset:3072\n" \
"v_mov_b32 v244, 0\n" \
"v_mfma_f32_16x16x32_bf16 v[72:75], v[20:23], v[196:199], v[72:75]\n" \
"s_waitcnt lgkmcnt(4)\n" \
"ds_read_b64_tr_b16 v[36:37], v232 offset:9216\n" \
"v_mov_b32 v245, 0\n" \
"v_mfma_f32_16x16x32_bf16 v[76:79], v[24:27], v[192:195], v[76:79]\n" \
"ds_read_b128 a[96:99], v254\n" \
"ds_read_b64_tr_b16 v[38:39], v232 offset:11264\n" \
"v_mfma_f32_16x16x32_bf16 v[76:79], v[28:31], v[196:199], v[76:79]\n" \
"buffer_store_dwordx4 a[72:75], v249, s[56:59], s90 offen offset:2048 sc1\n" \
"ds_read_b128 a[100:103], v254\n" \
"v_cndmask_b32 v238, 0, v239, vcc\n" \
"v_cndmask_b32 v239, 0, v239, s[64:65]\n" \
"v_mfma_f32_16x16x32_bf16 v[108:111], v[24:27], v[200:203], v[108:111]\n" \
"ds_read_b64_tr_b16 v[40:41], v233 offset:1024\n" \
"v_mfma_f32_16x16x32_bf16 v[108:111], v[28:31], v[204:207], v[108:111]\n" \
"ds_read_b64_tr_b16 v[42:43], v233 offset:3072\n" \
"v_mfma_f32_16x16x32_bf16 v[104:107], v[16:19], v[200:203], v[104:107]\n" \
"ds_read_b128 a[72:75], v254\n" \
"ds_read_b64_tr_b16 v[44:45], v233 offset:9216\n" \
"v_mfma_f32_16x16x32_bf16 v[104:107], v[20:23], v[204:207], v[104:107]\n" \
"ds_read_b128 a[76:79], v254\n" \
"v_mfma_f32_16x16x32_bf16 v[100:103], v[8:11], v[200:203], v[100:103]\n" \
"ds_read_b64_tr_b16 v[46:47], v233 offset:11264\n" \
"v_mfma_f32_16x16x32_bf16 v[100:103], v[12:15], v[204:207], v[100:103]\n" \
"ds_read_b64_tr_b16 v[48:49], v232 offset:1536\n" \
"v_mfma_f32_16x16x32_bf16 v[96:99], v[0:3], v[200:203], v[96:99]\n" \
"s_add_u32 m0, s95, 0\n" \
"ds_read_b64_tr_b16 v[50:51], v232 offset:3584\n" \
"v_mfma_f32_16x16x32_bf16 v[96:99], v[4:7], v[204:207], v[96:99]\n" \
"global_load_lds_dwordx4 v250, s[36:37] offset:0 sc1\n" \
"v_mfma_f32_16x16x32_bf16 v[128:131], v[0:3], v[208:211], v[128:131]\n" \
"ds_read_b64_tr_b16 v[52:53], v232 offset:9728\n" \
"v_mfma_f32_16x16x32_bf16 v[128:131], v[4:7], v[212:215], v[128:131]\n" \
"ds_read_b64_tr_b16 v[54:55], v232 offset:11776\n" \
"v_mfma_f32_16x16x32_bf16 v[132:135], v[8:11], v[208:211], v[132:135]\n" \
"ds_read_b64_tr_b16 v[56:57], v233 offset:1536\n" \
"v_mfma_f32_16x16x32_bf16 v[132:135], v[12:15], v[212:215], v[132:135]\n" \
"global_load_lds_dwordx4 v250, s[36:37] offset:1024 sc1\n" \
"v_mfma_f32_16x16x32_bf16 v[136:139], v[16:19], v[208:211], v[136:139]\n" \
"ds_read_b64_tr_b16 v[58:59], v233 offset:3584\n" \
"v_mfma_f32_16x16x32_bf16 v[136:139], v[20:23], v[212:215], v[136:139]\n" \
"ds_read_b64_tr_b16 v[60:61], v233 offset:9728\n" \
"v_mfma_f32_16x16x32_bf16 v[140:143], v[24:27], v[208:211], v[140:143]\n" \
"ds_read_b64_tr_b16 v[62:63], v233 offset:11776\n" \
"v_mfma_f32_16x16x32_bf16 v[140:143], v[28:31], v[212:215], v[140:143]\n" \
"s_waitcnt lgkmcnt(3)\n" \
"global_load_lds_dwordx4 v250, s[36:37] offset:2048 sc1\n" \
"v_mfma_f32_16x16x32_bf16 v[172:175], v[24:27], v[216:219], v[172:175]\n" \
"v_add_u32 v230, 0x8000, v230\n" \
"v_add_u32 v231, 0x8000, v231\n" \
"v_mfma_f32_16x16x32_bf16 v[172:175], v[28:31], v[220:223], v[172:175]\n" \
"v_add_u32 v232, 0x4000, v232\n" \
"v_add_u32 v233, 0x4000, v233\n" \
"v_mfma_f32_16x16x32_bf16 v[168:171], v[16:19], v[216:219], v[168:171]\n" \
"v_mfma_f32_16x16x32_bf16 v[168:171], v[20:23], v[220:223], v[168:171]\n" \
"global_load_lds_dwordx4 v250, s[36:37] offset:3072 sc1\n" \
"v_mfma_f32_16x16x32_bf16 v[164:167], v[8:11], v[216:219], v[164:167]\n" \
"s_add_u32 s36, s36, 17408\n" \
"v_mfma_f32_16x16x32_bf16 v[164:167], v[12:15], v[220:223], v[164:167]\n" \
"s_waitcnt vmcnt(19) lgkmcnt(0)\n" \
"v_mfma_f32_16x16x32_bf16 v[160:163], v[0:3], v[216:219], v[160:163]\n" \
"v_mfma_f32_16x16x32_bf16 v[160:163], v[4:7], v[220:223], v[160:163]\n" \
"s_barrier\n" \
"v_mfma_f32_16x16x32_bf16 v[80:83], v[32:35], v[192:195], v[80:83]\n" \
"ds_read_b64_tr_b16 v[0:1], v232 offset:0\n" \
"v_mfma_f32_16x16x32_bf16 v[80:83], v[36:39], v[196:199], v[80:83]\n" \
"ds_read_b64_tr_b16 v[2:3], v232 offset:2048\n" \
"v_mfma_f32_16x16x32_bf16 v[84:87], v[40:43], v[192:195], v[84:87]\n" \
"s_add_u32 m0, s95, 16384\n" \
"ds_read_b64_tr_b16 v[4:5], v232 offset:8192\n" \
"v_mfma_f32_16x16x32_bf16 v[84:87], v[44:47], v[196:199], v[84:87]\n" \
"global_load_lds_dwordx4 v250, s[36:37] offset:0 sc1\n" \
"v_mfma_f32_16x16x32_bf16 v[88:91], v[48:51], v[192:195], v[88:91]\n" \
"ds_read_b64_tr_b16 v[6:7], v232 offset:10240\n" \
"v_mfma_f32_16x16x32_bf16 v[88:91], v[52:55], v[196:199], v[88:91]\n" \
"ds_read_b64_tr_b16 v[8:9], v233 offset:0\n" \
"v_mfma_f32_16x16x32_bf16 v[92:95], v[56:59], v[192:195], v[92:95]\n" \
"ds_read_b64_tr_b16 v[10:11], v233 offset:2048\n" \
"v_mfma_f32_16x16x32_bf16 v[92:95], v[60:63], v[196:199], v[92:95]\n" \
"global_load_lds_dwordx4 v250, s[36:37] offset:1024 sc1\n" \
"v_mfma_f32_16x16x32_bf16 v[124:127], v[56:59], v[200:203], v[124:127]\n" \
"ds_read_b128 v[192:195], v230 offset:0\n" \
"v_mfma_f32_16x16x32_bf16 v[124:127], v[60:63], v[204:207], v[124:127]\n" \
"ds_read_b128 v[196:199], v231 offset:0\n" \
"v_mfma_f32_16x16x32_bf16 v[120:123], v[48:51], v[200:203], v[120:123]\n" \
"ds_read_b64_tr_b16 v[12:13], v233 offset:8192\n" \
"v_mfma_f32_16x16x32_bf16 v[120:123], v[52:55], v[204:207], v[120:123]\n" \
"global_load_lds_dwordx4 v250, s[36:37] offset:2048 sc1\n" \
"v_mfma_f32_16x16x32_bf16 v[116:119], v[40:43], v[200:203], v[116:119]\n" \
"ds_read_b64_tr_b16 v[14:15], v233 offset:10240\n" \
"v_mfma_f32_16x16x32_bf16 v[116:119], v[44:47], v[204:207], v[116:119]\n" \
"ds_read_b64_tr_b16 v[16:17], v232 offset:512\n" \
"v_mfma_f32_16x16x32_bf16 v[112:115], v[32:35], v[200:203], v[112:115]\n" \
"ds_read_b64_tr_b16 v[18:19], v232 offset:2560\n" \
"v_mfma_f32_16x16x32_bf16 v[112:115], v[36:39], v[204:207], v[112:115]\n" \
"global_load_lds_dwordx4 v250, s[36:37] offset:3072 sc1\n" \
"v_mfma_f32_16x16x32_bf16 v[144:147], v[32:35], v[208:211], v[144:147]\n" \
"ds_read_b128 v[200:203], v230 offset:2048\n" \
"s_add_u32 s36, s36, 17408\n" \
"v_mfma_f32_16x16x32_bf16 v[144:147], v[36:39], v[212:215], v[144:147]\n" \
"ds_read_b128 v[204:207], v231 offset:2048\n" \
"v_mfma_f32_16x16x32_bf16 v[148:151], v[40:43], v[208:211], v[148:151]\n" \
"ds_read_b64_tr_b16 v[20:21], v232 offset:8704\n" \
"v_mfma_f32_16x16x32_bf16 v[148:151], v[44:47], v[212:215], v[148:151]\n" \
"buffer_store_dwordx4 a[104:107], v249, s[56:59], s90 offen offset:2304 sc1\n" \
"v_mfma_f32_16x16x32_bf16 v[152:155], v[48:51], v[208:211], v[152:155]\n" \
"s_add_u32 s90, s89, 278528\n" \
"ds_read_b64_tr_b16 v[22:23], v232 offset:10752\n" \
"v_mfma_f32_16x16x32_bf16 v[152:155], v[52:55], v[212:215], v[152:155]\n" \
"ds_read_b64_tr_b16 v[24:25], v233 offset:512\n" \
"v_mfma_f32_16x16x32_bf16 v[156:159], v[56:59], v[208:211], v[156:159]\n" \
"ds_read_b128 a[104:107], v254\n" \
"ds_read_b64_tr_b16 v[26:27], v233 offset:2560\n" \
"v_mfma_f32_16x16x32_bf16 v[156:159], v[60:63], v[212:215], v[156:159]\n" \
"buffer_store_dwordx4 a[80:83], v248, s[56:59], s90 offen offset:0 sc1\n" \
"ds_read_b128 a[108:111], v254\n" \
"v_mfma_f32_16x16x32_bf16 v[188:191], v[56:59], v[216:219], v[188:191]\n" \
"ds_read_b128 v[208:211], v230 offset:4096\n" \
"v_mfma_f32_16x16x32_bf16 v[188:191], v[60:63], v[220:223], v[188:191]\n" \
"ds_read_b128 v[212:215], v231 offset:4096\n" \
"v_mfma_f32_16x16x32_bf16 v[184:187], v[48:51], v[216:219], v[184:187]\n" \
"ds_read_b128 a[80:83], v254\n" \
"ds_read_b64_tr_b16 v[28:29], v233 offset:8704\n" \
"v_mfma_f32_16x16x32_bf16 v[184:187], v[52:55], v[220:223], v[184:187]\n" \
"buffer_store_dwordx4 a[84:87], v248, s[56:59], s90 offen offset:1024 sc1\n" \
"ds_read_b128 a[112:115], v254\n" \
"v_mfma_f32_16x16x32_bf16 v[180:183], v[40:43], v[216:219], v[180:183]\n" \
"ds_read_b64_tr_b16 v[30:31], v233 offset:10752\n" \
"v_mfma_f32_16x16x32_bf16 v[180:183], v[44:47], v[220:223], v[180:183]\n" \
"v_mfma_f32_16x16x32_bf16 v[176:179], v[32:35], v[216:219], v[176:179]\n" \
"ds_read_b128 a[84:87], v254\n" \
"v_mfma_f32_16x16x32_bf16 v[176:179], v[36:39], v[220:223], v[176:179]\n" \
"s_waitcnt lgkmcnt(3)\n" \
"ds_read_b128 a[116:119], v254\n" \
"v_mfma_f32_16x16x32_bf16 v[64:67], v[0:3], v[192:195], v[64:67]\n" \
"ds_read_b128 v[216:219], v230 offset:6144\n" \
"v_mfma_f32_16x16x32_bf16 v[64:67], v[4:7], v[196:199], v[64:67]\n" \
"ds_read_b128 v[220:223], v231 offset:6144\n" \
"v_mfma_f32_16x16x32_bf16 v[68:71], v[8:11], v[192:195], v[68:71]\n" \
"ds_read_b64_tr_b16 v[32:33], v232 offset:1024\n" \
"v_mfma_f32_16x16x32_bf16 v[68:71], v[12:15], v[196:199], v[68:71]\n" \
"v_mfma_f32_16x16x32_bf16 v[72:75], v[16:19], v[192:195], v[72:75]\n" \
"ds_read_b64_tr_b16 v[34:35], v232 offset:3072\n" \
"v_mfma_f32_16x16x32_bf16 v[72:75], v[20:23], v[196:199], v[72:75]\n" \
"s_waitcnt lgkmcnt(3)\n" \
"ds_read_b64_tr_b16 v[36:37], v232 offset:9216\n" \
"v_mfma_f32_16x16x32_bf16 v[76:79], v[24:27], v[192:195], v[76:79]\n" \
"ds_read_b64_tr_b16 v[38:39], v232 offset:11264\n" \
"v_mfma_f32_16x16x32_bf16 v[76:79], v[28:31], v[196:199], v[76:79]\n" \
"v_mfma_f32_16x16x32_bf16 v[108:111], v[24:27], v[200:203], v[108:111]\n" \
"ds_read_b64_tr_b16 v[40:41], v233 offset:1024\n" \
"v_mfma_f32_16x16x32_bf16 v[108:111], v[28:31], v[204:207], v[108:111]\n" \
"ds_read_b64_tr_b16 v[42:43], v233 offset:3072\n" \
"v_mfma_f32_16x16x32_bf16 v[104:107], v[16:19], v[200:203], v[104:107]\n" \
"ds_read_b64_tr_b16 v[44:45], v233 offset:9216\n" \
"v_mfma_f32_16x16x32_bf16 v[104:107], v[20:23], v[204:207], v[104:107]\n" \
"v_mfma_f32_16x16x32_bf16 v[100:103], v[8:11], v[200:203], v[100:103]\n" \
"ds_read_b64_tr_b16 v[46:47], v233 offset:11264\n" \
"v_mfma_f32_16x16x32_bf16 v[100:103], v[12:15], v[204:207], v[100:103]\n" \
"ds_read_b64_tr_b16 v[48:49], v232 offset:1536\n" \
"v_mfma_f32_16x16x32_bf16 v[96:99], v[0:3], v[200:203], v[96:99]\n" \
"ds_read_b64_tr_b16 v[50:51], v232 offset:3584\n" \
"v_mfma_f32_16x16x32_bf16 v[96:99], v[4:7], v[204:207], v[96:99]\n" \
"v_mfma_f32_16x16x32_bf16 v[128:131], v[0:3], v[208:211], v[128:131]\n" \
"ds_read_b64_tr_b16 v[52:53], v232 offset:9728\n" \
"v_mfma_f32_16x16x32_bf16 v[128:131], v[4:7], v[212:215], v[128:131]\n" \
"ds_read_b64_tr_b16 v[54:55], v232 offset:11776\n" \
"v_mfma_f32_16x16x32_bf16 v[132:135], v[8:11], v[208:211], v[132:135]\n" \
"ds_read_b64_tr_b16 v[56:57], v233 offset:1536\n" \
"v_mfma_f32_16x16x32_bf16 v[132:135], v[12:15], v[212:215], v[132:135]\n" \
"v_mfma_f32_16x16x32_bf16 v[136:139], v[16:19], v[208:211], v[136:139]\n" \
"ds_read_b64_tr_b16 v[58:59], v233 offset:3584\n" \
"v_mfma_f32_16x16x32_bf16 v[136:139], v[20:23], v[212:215], v[136:139]\n" \
"ds_read_b64_tr_b16 v[60:61], v233 offset:9728\n" \
"v_mfma_f32_16x16x32_bf16 v[140:143], v[24:27], v[208:211], v[140:143]\n" \
"ds_read_b64_tr_b16 v[62:63], v233 offset:11776\n" \
"v_mfma_f32_16x16x32_bf16 v[140:143], v[28:31], v[212:215], v[140:143]\n" \
"s_waitcnt lgkmcnt(3)\n" \
"v_mfma_f32_16x16x32_bf16 v[172:175], v[24:27], v[216:219], v[172:175]\n" \
"v_add_u32 v230, 0x8000, v230\n" \
"v_add_u32 v231, 0x8000, v231\n" \
"v_mfma_f32_16x16x32_bf16 v[172:175], v[28:31], v[220:223], v[172:175]\n" \
"v_add_u32 v232, 0x4000, v232\n" \
"v_add_u32 v233, 0x4000, v233\n" \
"v_mfma_f32_16x16x32_bf16 v[168:171], v[16:19], v[216:219], v[168:171]\n" \
"v_mfma_f32_16x16x32_bf16 v[168:171], v[20:23], v[220:223], v[168:171]\n" \
"v_mfma_f32_16x16x32_bf16 v[164:167], v[8:11], v[216:219], v[164:167]\n" \
"v_mfma_f32_16x16x32_bf16 v[164:167], v[12:15], v[220:223], v[164:167]\n" \
"s_waitcnt vmcnt(13) lgkmcnt(0)\n" \
"v_mfma_f32_16x16x32_bf16 v[160:163], v[0:3], v[216:219], v[160:163]\n" \
"v_mfma_f32_16x16x32_bf16 v[160:163], v[4:7], v[220:223], v[160:163]\n" \
"s_barrier\n" \
"v_mfma_f32_16x16x32_bf16 v[80:83], v[32:35], v[192:195], v[80:83]\n" \
"ds_read_b64_tr_b16 v[0:1], v232 offset:0\n" \
"v_mfma_f32_16x16x32_bf16 v[80:83], v[36:39], v[196:199], v[80:83]\n" \
"ds_read_b64_tr_b16 v[2:3], v232 offset:2048\n" \
"v_mfma_f32_16x16x32_bf16 v[84:87], v[40:43], v[192:195], v[84:87]\n" \
"s_add_u32 m0, s95, 32768\n" \
"ds_read_b64_tr_b16 v[4:5], v232 offset:8192\n" \
"v_mfma_f32_16x16x32_bf16 v[84:87], v[44:47], v[196:199], v[84:87]\n" \
"global_load_lds_dwordx4 v250, s[36:37] offset:0 sc1\n" \
"v_mfma_f32_16x16x32_bf16 v[88:91], v[48:51], v[192:195], v[88:91]\n" \
"ds_read_b64_tr_b16 v[6:7], v232 offset:10240\n" \
"v_mfma_f32_16x16x32_bf16 v[88:91], v[52:55], v[196:199], v[88:91]\n" \
"ds_read_b64_tr_b16 v[8:9], v233 offset:0\n" \
"v_mfma_f32_16x16x32_bf16 v[92:95], v[56:59], v[192:195], v[92:95]\n" \
"ds_read_b64_tr_b16 v[10:11], v233 offset:2048\n" \
"v_mfma_f32_16x16x32_bf16 v[92:95], v[60:63], v[196:199], v[92:95]\n" \
"global_load_lds_dwordx4 v250, s[36:37] offset:1024 sc1\n" \
"v_mfma_f32_16x16x32_bf16 v[124:127], v[56:59], v[200:203], v[124:127]\n" \
"ds_read_b128 v[192:195], v230 offset:0\n" \
"v_mfma_f32_16x16x32_bf16 v[124:127], v[60:63], v[204:207], v[124:127]\n" \
"ds_read_b128 v[196:199], v231 offset:0\n" \
"v_mfma_f32_16x16x32_bf16 v[120:123], v[48:51], v[200:203], v[120:123]\n" \
"ds_read_b64_tr_b16 v[12:13], v233 offset:8192\n" \
"v_mfma_f32_16x16x32_bf16 v[120:123], v[52:55], v[204:207], v[120:123]\n" \
"global_load_lds_dwordx4 v250, s[36:37] offset:2048 sc1\n" \
"v_mfma_f32_16x16x32_bf16 v[116:119], v[40:43], v[200:203], v[116:119]\n" \
"ds_read_b64_tr_b16 v[14:15], v233 offset:10240\n" \
"v_mfma_f32_16x16x32_bf16 v[116:119], v[44:47], v[204:207], v[116:119]\n" \
"ds_read_b64_tr_b16 v[16:17], v232 offset:512\n" \
"v_mfma_f32_16x16x32_bf16 v[112:115], v[32:35], v[200:203], v[112:115]\n" \
"ds_read_b64_tr_b16 v[18:19], v232 offset:2560\n" \
"v_mfma_f32_16x16x32_bf16 v[112:115], v[36:39], v[204:207], v[112:115]\n" \
"global_load_lds_dwordx4 v250, s[36:37] offset:3072 sc1\n" \
"v_mfma_f32_16x16x32_bf16 v[144:147], v[32:35], v[208:211], v[144:147]\n" \
"ds_read_b128 v[200:203], v230 offset:2048\n" \
"s_add_u32 s36, s36, 17408\n" \
"v_mfma_f32_16x16x32_bf16 v[144:147], v[36:39], v[212:215], v[144:147]\n" \
"ds_read_b128 v[204:207], v231 offset:2048\n" \
"v_mfma_f32_16x16x32_bf16 v[148:151], v[40:43], v[208:211], v[148:151]\n" \
"ds_read_b64_tr_b16 v[20:21], v232 offset:8704\n" \
"v_mfma_f32_16x16x32_bf16 v[148:151], v[44:47], v[212:215], v[148:151]\n" \
"buffer_store_dwordx4 a[88:91], v248, s[56:59], s90 offen offset:2048 sc1\n" \
"v_mfma_f32_16x16x32_bf16 v[152:155], v[48:51], v[208:211], v[152:155]\n" \
"ds_read_b64_tr_b16 v[22:23], v232 offset:10752\n" \
"v_mfma_f32_16x16x32_bf16 v[152:155], v[52:55], v[212:215], v[152:155]\n" \
"ds_read_b64_tr_b16 v[24:25], v233 offset:512\n" \
"v_mfma_f32_16x16x32_bf16 v[156:159], v[56:59], v[208:211], v[156:159]\n" \
"ds_read_b128 a[88:91], v254\n" \
"ds_read_b64_tr_b16 v[26:27], v233 offset:2560\n" \
"v_mfma_f32_16x16x32_bf16 v[156:159], v[60:63], v[212:215], v[156:159]\n" \
"buffer_store_dwordx4 a[92:95], v248, s[56:59], s90 offen offset:3072 sc1\n" \
"v_mfma_f32_16x16x32_bf16 v[188:191], v[56:59], v[216:219], v[188:191]\n" \
"ds_read_b128 v[208:211], v230 offset:4096\n" \
"s_add_u32 s90, s88, 417792\n" \
"v_mfma_f32_16x16x32_bf16 v[188:191], v[60:63], v[220:223], v[188:191]\n" \
"ds_read_b128 v[212:215], v231 offset:4096\n" \
"v_mfma_f32_16x16x32_bf16 v[184:187], v[48:51], v[216:219], v[184:187]\n" \
"ds_read_b128 a[92:95], v254\n" \
"ds_read_b64_tr_b16 v[28:29], v233 offset:8704\n" \
"v_mfma_f32_16x16x32_bf16 v[184:187], v[52:55], v[220:223], v[184:187]\n" \
"buffer_store_dwordx4 a[0:3], v249, s[56:59], s90 offen offset:0 sc1\n" \
"v_accvgpr_write_b32 a120, v238\n" \
"v_accvgpr_write_b32 a121, v238\n" \
"v_accvgpr_write_b32 a122, v238\n" \
"v_accvgpr_write_b32 a123, v238\n" \
"v_mfma_f32_16x16x32_bf16 v[180:183], v[40:43], v[216:219], v[180:183]\n" \
"ds_read_b64_tr_b16 v[30:31], v233 offset:10752\n" \
"v_accvgpr_write_b32 a124, v239\n" \
"v_mfma_f32_16x16x32_bf16 v[180:183], v[44:47], v[220:223], v[180:183]\n" \
"v_accvgpr_write_b32 a125, v239\n" \
"v_accvgpr_write_b32 a126, v239\n" \
"v_mfma_f32_16x16x32_bf16 v[176:179], v[32:35], v[216:219], v[176:179]\n" \
"ds_read_b128 a[0:3], v254\n" \
"v_accvgpr_write_b32 a127, v239\n" \
"v_mfma_f32_16x16x32_bf16 v[176:179], v[36:39], v[220:223], v[176:179]\n" \
"s_waitcnt lgkmcnt(2)\n" \
"ds_read_b128 a[4:7], v254\n" \
"v_mfma_f32_16x16x32_bf16 v[64:67], v[0:3], v[192:195], v[64:67]\n" \
"ds_read_b128 v[216:219], v230 offset:6144\n" \
"v_mfma_f32_16x16x32_bf16 v[64:67], v[4:7], v[196:199], v[64:67]\n" \
"ds_read_b128 v[220:223], v231 offset:6144\n" \
"v_mfma_f32_16x16x32_bf16 v[68:71], v[8:11], v[192:195], v[68:71]\n" \
"ds_read_b64_tr_b16 v[32:33], v232 offset:1024\n" \
"v_mfma_f32_16x16x32_bf16 v[68:71], v[12:15], v[196:199], v[68:71]\n" \
"v_mfma_f32_16x16x32_bf16 v[72:75], v[16:19], v[192:195], v[72:75]\n" \
"ds_read_b64_tr_b16 v[34:35], v232 offset:3072\n" \
"v_mfma_f32_16x16x32_bf16 v[72:75], v[20:23], v[196:199], v[72:75]\n" \
"s_waitcnt lgkmcnt(3)\n" \
"ds_read_b64_tr_b16 v[36:37], v232 offset:9216\n" \
"v_mfma_f32_16x16x32_bf16 v[76:79], v[24:27], v[192:195], v[76:79]\n" \
"ds_read_b64_tr_b16 v[38:39], v232 offset:11264\n" \
"v_cvt_pk_bf16_f32 v64, v64, v65\n" \
"v_mfma_f32_16x16x32_bf16 v[76:79], v[28:31], v[196:199], v[76:79]\n" \
"v_cvt_pk_bf16_f32 v65, v66, v67\n" \
"v_cvt_pk_bf16_f32 v66, v68, v69\n" \
"v_cvt_pk_bf16_f32 v67, v70, v71\n" \
"v_accvgpr_write_b32 a128, v64\n" \
"v_accvgpr_write_b32 a129, v65\n" \
"v_mfma_f32_16x16x32_bf16 v[96:99], v[0:3], v[200:203], v[96:99]\n" \
"ds_read_b64_tr_b16 v[40:41], v233 offset:1024\n" \
"v_accvgpr_write_b32 a130, v66\n" \
"v_mfma_f32_16x16x32_bf16 v[96:99], v[4:7], v[204:207], v[96:99]\n" \
"ds_read_b64_tr_b16 v[42:43], v233 offset:3072\n" \
"v_accvgpr_write_b32 a131, v67\n" \
"v_mfma_f32_16x16x32_bf16 v[100:103], v[8:11], v[200:203], v[100:103]\n" \
"ds_read_b64_tr_b16 v[44:45], v233 offset:9216\n" \
"v_cvt_pk_bf16_f32 v72, v72, v73\n" \
"v_mfma_f32_16x16x32_bf16 v[100:103], v[12:15], v[204:207], v[100:103]\n" \
"v_cvt_pk_bf16_f32 v73, v74, v75\n" \
"v_cvt_pk_bf16_f32 v74, v76, v77\n" \
"v_cvt_pk_bf16_f32 v75, v78, v79\n" \
"v_accvgpr_write_b32 a132, v72\n" \
"v_accvgpr_write_b32 a133, v73\n" \
"v_mfma_f32_16x16x32_bf16 v[104:107], v[16:19], v[200:203], v[104:107]\n" \
"ds_read_b64_tr_b16 v[46:47], v233 offset:11264\n" \
"v_accvgpr_write_b32 a134, v74\n" \
"v_mfma_f32_16x16x32_bf16 v[104:107], v[20:23], v[204:207], v[104:107]\n" \
"ds_read_b64_tr_b16 v[48:49], v232 offset:1536\n" \
"v_accvgpr_write_b32 a135, v75\n" \
"v_mfma_f32_16x16x32_bf16 v[108:111], v[24:27], v[200:203], v[108:111]\n" \
"ds_read_b64_tr_b16 v[50:51], v232 offset:3584\n" \
"v_cvt_pk_bf16_f32 v96, v96, v97\n" \
"v_mfma_f32_16x16x32_bf16 v[108:111], v[28:31], v[204:207], v[108:111]\n" \
"v_cvt_pk_bf16_f32 v97, v98, v99\n" \
"v_cvt_pk_bf16_f32 v98, v100, v101\n" \
"v_cvt_pk_bf16_f32 v99, v102, v103\n" \
"v_accvgpr_write_b32 a144, v96\n" \
"v_accvgpr_write_b32 a145, v97\n" \
"v_mfma_f32_16x16x32_bf16 v[128:131], v[0:3], v[208:211], v[128:131]\n" \
"ds_read_b64_tr_b16 v[52:53], v232 offset:9728\n" \
"v_accvgpr_write_b32 a146, v98\n" \
"v_mfma_f32_16x16x32_bf16 v[128:131], v[4:7], v[212:215], v[128:131]\n" \
"ds_read_b64_tr_b16 v[54:55], v232 offset:11776\n" \
"v_accvgpr_write_b32 a147, v99\n" \
"v_mfma_f32_16x16x32_bf16 v[132:135], v[8:11], v[208:211], v[132:135]\n" \
"ds_read_b64_tr_b16 v[56:57], v233 offset:1536\n" \
"v_cvt_pk_bf16_f32 v104, v104, v105\n" \
"v_mfma_f32_16x16x32_bf16 v[132:135], v[12:15], v[212:215], v[132:135]\n" \
"v_cvt_pk_bf16_f32 v105, v106, v107\n" \
"v_cvt_pk_bf16_f32 v106, v108, v109\n" \
"v_cvt_pk_bf16_f32 v107, v110, v111\n" \
"v_accvgpr_write_b32 a148, v104\n" \
"v_accvgpr_write_b32 a149, v105\n" \
"v_mfma_f32_16x16x32_bf16 v[136:139], v[16:19], v[208:211], v[136:139]\n" \
"ds_read_b64_tr_b16 v[58:59], v233 offset:3584\n" \
"v_accvgpr_write_b32 a150, v106\n" \
"v_mfma_f32_16x16x32_bf16 v[136:139], v[20:23], v[212:215], v[136:139]\n" \
"ds_read_b64_tr_b16 v[60:61], v233 offset:9728\n" \
"v_accvgpr_write_b32 a151, v107\n" \
"v_mfma_f32_16x16x32_bf16 v[140:143], v[24:27], v[208:211], v[140:143]\n" \
"ds_read_b64_tr_b16 v[62:63], v233 offset:11776\n" \
"v_cvt_pk_bf16_f32 v128, v128, v129\n" \
"v_mfma_f32_16x16x32_bf16 v[140:143], v[28:31], v[212:215], v[140:143]\n" \
"s_waitcnt lgkmcnt(3)\n" \
"v_cvt_pk_bf16_f32 v129, v130, v131\n" \
"v_cvt_pk_bf16_f32 v130, v132, v133\n" \
"v_cvt_pk_bf16_f32 v131, v134, v135\n" \
"v_mfma_f32_16x16x32_bf16 v[160:163], v[0:3], v[216:219], v[160:163]\n" \
"v_accvgpr_write_b32 a160, v128\n" \
"v_accvgpr_write_b32 a161, v129\n" \
"Lpg4934_%=:\n" \
"s_cbranch_execz Lpg6171_%=\n" \
"v_mfma_f32_16x16x32_bf16 v[160:163], v[4:7], v[220:223], v[160:163]\n" \
"v_accvgpr_write_b32 a162, v130\n" \
"v_accvgpr_write_b32 a163, v131\n" \
"v_mfma_f32_16x16x32_bf16 v[164:167], v[8:11], v[216:219], v[164:167]\n" \
"v_cvt_pk_bf16_f32 v136, v136, v137\n" \
"v_cvt_pk_bf16_f32 v137, v138, v139\n" \
"v_mfma_f32_16x16x32_bf16 v[164:167], v[12:15], v[220:223], v[164:167]\n" \
"v_cvt_pk_bf16_f32 v138, v140, v141\n" \
"v_cvt_pk_bf16_f32 v139, v142, v143\n" \
"v_mfma_f32_16x16x32_bf16 v[168:171], v[16:19], v[216:219], v[168:171]\n" \
"v_accvgpr_write_b32 a164, v136\n" \
"v_accvgpr_write_b32 a165, v137\n" \
"v_mfma_f32_16x16x32_bf16 v[168:171], v[20:23], v[220:223], v[168:171]\n" \
"s_waitcnt vmcnt(10) lgkmcnt(0)\n" \
"v_accvgpr_write_b32 a166, v138\n" \
"v_accvgpr_write_b32 a167, v139\n" \
"v_mfma_f32_16x16x32_bf16 v[172:175], v[24:27], v[216:219], v[172:175]\n" \
"v_cvt_pk_bf16_f32 v160, v160, v161\n" \
"v_cvt_pk_bf16_f32 v161, v162, v163\n" \
"v_mfma_f32_16x16x32_bf16 v[172:175], v[28:31], v[220:223], v[172:175]\n" \
"s_barrier\n" \
"v_cvt_pk_bf16_f32 v162, v164, v165\n" \
"v_cvt_pk_bf16_f32 v163, v166, v167\n" \
"v_accvgpr_write_b32 a176, v160\n" \
"v_accvgpr_write_b32 a177, v161\n" \
"v_mfma_f32_16x16x32_bf16 v[80:83], v[32:35], v[192:195], v[80:83]\n" \
"s_mov_b32 m0, s74\n" \
"v_accvgpr_write_b32 a178, v162\n" \
"v_accvgpr_write_b32 a179, v163\n" \
"v_mfma_f32_16x16x32_bf16 v[80:83], v[36:39], v[196:199], v[80:83]\n" \
"ds_read_b128 a[192:195], v249 offset:0\n" \
"global_load_lds_dwordx4 v250, s[38:39] offset:0 sc1\n" \
"v_mfma_f32_16x16x32_bf16 v[84:87], v[40:43], v[192:195], v[84:87]\n" \
"ds_read_b128 v[0:3], v254\n" \
"v_cvt_pk_bf16_f32 v168, v168, v169\n" \
"v_mfma_f32_16x16x32_bf16 v[84:87], v[44:47], v[196:199], v[84:87]\n" \
"ds_read_b128 a[196:199], v249 offset:2048\n" \
"global_load_lds_dwordx4 v250, s[38:39] offset:1024 sc1\n" \
"v_cvt_pk_bf16_f32 v169, v170, v171\n" \
"v_cvt_pk_bf16_f32 v170, v172, v173\n" \
"v_cvt_pk_bf16_f32 v171, v174, v175\n" \
"v_accvgpr_write_b32 a180, v168\n" \
"v_accvgpr_write_b32 a181, v169\n" \
"v_accvgpr_write_b32 a182, v170\n" \
"v_mfma_f32_16x16x32_bf16 v[88:91], v[48:51], v[192:195], v[88:91]\n" \
"ds_read_b128 v[4:7], v254\n" \
"v_accvgpr_write_b32 a183, v171\n" \
"v_mfma_f32_16x16x32_bf16 v[88:91], v[52:55], v[196:199], v[88:91]\n" \
"ds_read_b128 a[200:203], v249 offset:4096\n" \
"global_load_lds_dwordx4 v250, s[38:39] offset:2048 sc1\n" \
"v_mfma_f32_16x16x32_bf16 v[92:95], v[56:59], v[192:195], v[92:95]\n" \
"v_cvt_pk_bf16_f32 v80, v80, v81\n" \
"v_cvt_pk_bf16_f32 v81, v82, v83\n" \
"v_mfma_f32_16x16x32_bf16 v[92:95], v[60:63], v[196:199], v[92:95]\n" \
"ds_read_b128 a[204:207], v249 offset:6144\n" \
"global_load_lds_dwordx4 v250, s[38:39] offset:3072 sc1\n" \
"v_cvt_pk_bf16_f32 v82, v84, v85\n" \
"v_cvt_pk_bf16_f32 v83, v86, v87\n" \
"v_accvgpr_write_b32 a136, v80\n" \
"v_accvgpr_write_b32 a137, v81\n" \
"v_accvgpr_write_b32 a138, v82\n" \
"v_mfma_f32_16x16x32_bf16 v[112:115], v[32:35], v[200:203], v[112:115]\n" \
"s_add_u32 s38, s38, 17408\n" \
"v_accvgpr_write_b32 a139, v83\n" \
"s_add_u32 m0, m0, 0x4000\n" \
"v_mfma_f32_16x16x32_bf16 v[112:115], v[36:39], v[204:207], v[112:115]\n" \
"ds_read_b128 a[208:211], v249 offset:256\n" \
"global_load_lds_dwordx4 v250, s[38:39] offset:0 sc1\n" \
"v_mfma_f32_16x16x32_bf16 v[116:119], v[40:43], v[200:203], v[116:119]\n" \
"v_cvt_pk_bf16_f32 v88, v88, v89\n" \
"v_cvt_pk_bf16_f32 v89, v90, v91\n" \
"v_mfma_f32_16x16x32_bf16 v[116:119], v[44:47], v[204:207], v[116:119]\n" \
"ds_read_b128 a[212:215], v249 offset:2304\n" \
"global_load_lds_dwordx4 v250, s[38:39] offset:1024 sc1\n" \
"v_cvt_pk_bf16_f32 v90, v92, v93\n" \
"v_cvt_pk_bf16_f32 v91, v94, v95\n" \
"v_accvgpr_write_b32 a140, v88\n" \
"v_accvgpr_write_b32 a141, v89\n" \
"v_mfma_f32_16x16x32_bf16 v[120:123], v[48:51], v[200:203], v[120:123]\n" \
"v_accvgpr_write_b32 a142, v90\n" \
"v_accvgpr_write_b32 a143, v91\n" \
"v_mfma_f32_16x16x32_bf16 v[120:123], v[52:55], v[204:207], v[120:123]\n" \
"ds_read_b128 a[216:219], v249 offset:4352\n" \
"global_load_lds_dwordx4 v250, s[38:39] offset:2048 sc1\n" \
"v_mfma_f32_16x16x32_bf16 v[124:127], v[56:59], v[200:203], v[124:127]\n" \
"v_cvt_pk_bf16_f32 v112, v112, v113\n" \
"v_cvt_pk_bf16_f32 v113, v114, v115\n" \
"v_mfma_f32_16x16x32_bf16 v[124:127], v[60:63], v[204:207], v[124:127]\n" \
"ds_read_b128 a[220:223], v249 offset:6400\n" \
"global_load_lds_dwordx4 v250, s[38:39] offset:3072 sc1\n" \
"v_cvt_pk_bf16_f32 v114, v116, v117\n" \
"v_cvt_pk_bf16_f32 v115, v118, v119\n" \
"v_mfma_f32_16x16x32_bf16 v[144:147], v[32:35], v[208:211], v[144:147]\n" \
"s_add_u32 s38, s38, 17408\n" \
"v_accvgpr_write_b32 a152, v112\n" \
"s_xor_b32 s74, s74, 0x8000\n" \
"v_mfma_f32_16x16x32_bf16 v[144:147], v[36:39], v[212:215], v[144:147]\n" \
"ds_read_b128 a[224:227], v249 offset:8192\n" \
"v_accvgpr_write_b32 a153, v113\n" \
"v_mfma_f32_16x16x32_bf16 v[148:151], v[40:43], v[208:211], v[148:151]\n" \
"v_accvgpr_write_b32 a154, v114\n" \
"v_accvgpr_write_b32 a155, v115\n" \
"v_mfma_f32_16x16x32_bf16 v[148:151], v[44:47], v[212:215], v[148:151]\n" \
"buffer_store_dwordx4 a[32:35], v249, s[56:59], s90 offen offset:256 sc1\n" \
"ds_read_b128 a[228:231], v249 offset:10240\n" \
"v_cvt_pk_bf16_f32 v120, v120, v121\n" \
"v_cvt_pk_bf16_f32 v121, v122, v123\n" \
"v_cvt_pk_bf16_f32 v122, v124, v125\n" \
"v_mfma_f32_16x16x32_bf16 v[152:155], v[48:51], v[208:211], v[152:155]\n" \
"v_cvt_pk_bf16_f32 v123, v126, v127\n" \
"v_accvgpr_write_b32 a156, v120\n" \
"v_mfma_f32_16x16x32_bf16 v[152:155], v[52:55], v[212:215], v[152:155]\n" \
"ds_read_b128 a[232:235], v249 offset:12288\n" \
"v_accvgpr_write_b32 a157, v121\n" \
"v_mfma_f32_16x16x32_bf16 v[156:159], v[56:59], v[208:211], v[156:159]\n" \
"ds_read_b128 a[32:35], v254\n" \
"v_accvgpr_write_b32 a158, v122\n" \
"v_mfma_f32_16x16x32_bf16 v[156:159], v[60:63], v[212:215], v[156:159]\n" \
"ds_read_b128 a[236:239], v249 offset:14336\n" \
"v_accvgpr_write_b32 a159, v123\n" \
"v_mfma_f32_16x16x32_bf16 v[176:179], v[32:35], v[216:219], v[176:179]\n" \
"ds_read_b128 a[240:243], v249 offset:8448\n" \
"v_cvt_pk_bf16_f32 v144, v144, v145\n" \
"v_mfma_f32_16x16x32_bf16 v[176:179], v[36:39], v[220:223], v[176:179]\n" \
"ds_read_b128 a[244:247], v249 offset:10496\n" \
"v_cvt_pk_bf16_f32 v145, v146, v147\n" \
"v_mfma_f32_16x16x32_bf16 v[180:183], v[40:43], v[216:219], v[180:183]\n" \
"ds_read_b128 a[248:251], v249 offset:12544\n" \
"v_cvt_pk_bf16_f32 v146, v148, v149\n" \
"v_mfma_f32_16x16x32_bf16 v[180:183], v[44:47], v[220:223], v[180:183]\n" \
"ds_read_b128 a[252:255], v249 offset:14592\n" \
"v_cvt_pk_bf16_f32 v147, v150, v151\n" \
"v_mfma_f32_16x16x32_bf16 v[184:187], v[48:51], v[216:219], v[184:187]\n" \
"ds_read_b128 a[36:39], v254\n" \
"v_accvgpr_write_b32 a168, v144\n" \
"v_mfma_f32_16x16x32_bf16 v[184:187], v[52:55], v[220:223], v[184:187]\n" \
"ds_read_b128 v[32:35], v254\n" \
"v_accvgpr_write_b32 a169, v145\n" \
"v_mfma_f32_16x16x32_bf16 v[188:191], v[56:59], v[216:219], v[188:191]\n" \
"ds_read_b128 v[36:39], v254\n" \
"v_accvgpr_write_b32 a170, v146\n" \
"v_mfma_f32_16x16x32_bf16 v[188:191], v[60:63], v[220:223], v[188:191]\n" \
"s_waitcnt lgkmcnt(4)\n" \
"buffer_store_dwordx4 a[8:11], v249, s[56:59], s90 offen offset:2048 sc1\n" \
"v_accvgpr_write_b32 a171, v147\n" \
"s_cmp_eq_u32 s63, 0\n" \
"s_cbranch_scc1 Lmask0_%=\n" \
"Lmask0r_%=:\n" \
"v_mfma_f32_16x16x32_bf16 v[0:3], a[192:195], a[128:131], v[0:3]\n" \
"s_lshl_b32 s70, s95, 1\n" \
"v_cvt_pk_bf16_f32 v152, v152, v153\n" \
"v_cvt_pk_bf16_f32 v153, v154, v155\n" \
"v_mfma_f32_16x16x32_bf16 v[0:3], a[196:199], a[132:135], v[0:3]\n" \
"s_add_u32 s70, s70, 131072\n" \
"v_cvt_pk_bf16_f32 v154, v156, v157\n" \
"v_cvt_pk_bf16_f32 v155, v158, v159\n" \
"v_mfma_f32_16x16x32_bf16 v[0:3], a[200:203], a[136:139], v[0:3]\n" \
"ds_read_b128 a[8:11], v254\n" \
"v_accvgpr_write_b32 a172, v152\n" \
"v_mfma_f32_16x16x32_bf16 v[0:3], a[204:207], a[140:143], v[0:3]\n" \
"ds_read_b128 a[12:15], v254\n" \
"v_accvgpr_write_b32 a173, v153\n" \
"v_mov_b32 v201, 0xf149f2ca\n" \
"v_mfma_f32_16x16x32_bf16 v[8:11], a[192:195], a[144:147], 0\n" \
"v_accvgpr_write_b32 a174, v154\n" \
"v_accvgpr_write_b32 a175, v155\n" \
"v_mfma_f32_16x16x32_bf16 v[8:11], a[196:199], a[148:151], v[8:11]\n" \
"v_cvt_pk_bf16_f32 v176, v176, v177\n" \
"v_cvt_pk_bf16_f32 v177, v178, v179\n" \
"v_mfma_f32_16x16x32_bf16 v[8:11], a[200:203], a[152:155], v[8:11]\n" \
"v_cvt_pk_bf16_f32 v178, v180, v181\n" \
"v_cvt_pk_bf16_f32 v179, v182, v183\n" \
"v_mfma_f32_16x16x32_bf16 v[8:11], a[204:207], a[156:159], v[8:11]\n" \
"v_accvgpr_write_b32 a184, v176\n" \
"v_accvgpr_write_b32 a185, v177\n" \
"v_max3_f32 v192, v0, v1, v2\n" \
"v_max3_f32 v192, v192, v3, v201\n" \
"v_mfma_f32_16x16x32_bf16 v[16:19], a[192:195], a[160:163], 0\n" \
"v_accvgpr_write_b32 a186, v178\n" \
"v_accvgpr_write_b32 a187, v179\n" \
"v_mfma_f32_16x16x32_bf16 v[16:19], a[196:199], a[164:167], v[16:19]\n" \
"v_cvt_pk_bf16_f32 v184, v184, v185\n" \
"v_cvt_pk_bf16_f32 v185, v186, v187\n" \
"v_mfma_f32_16x16x32_bf16 v[16:19], a[200:203], a[168:171], v[16:19]\n" \
"v_cvt_pk_bf16_f32 v186, v188, v189\n" \
"v_cvt_pk_bf16_f32 v187, v190, v191\n" \
"v_mfma_f32_16x16x32_bf16 v[16:19], a[204:207], a[172:175], v[16:19]\n" \
"v_max3_f32 v193, v8, v9, v10\n" \
"v_accvgpr_write_b32 a188, v184\n" \
"v_accvgpr_write_b32 a189, v185\n" \
"v_mfma_f32_16x16x32_bf16 v[24:27], a[192:195], a[176:179], 0\n" \
"v_accvgpr_write_b32 a190, v186\n" \
"v_accvgpr_write_b32 a191, v187\n" \
"v_mfma_f32_16x16x32_bf16 v[24:27], a[196:199], a[180:183], v[24:27]\n" \
"v_add_u32 v200, s70, v248\n" \
"v_max3_f32 v193, v193, v11, v201\n" \
"v_mfma_f32_16x16x32_bf16 v[24:27], a[200:203], a[184:187], v[24:27]\n" \
"ds_read_b128 v[128:131], v254\n" \
"ds_read_b128 v[132:135], v254\n" \
"v_max3_f32 v194, v16, v17, v18\n" \
"v_max3_f32 v194, v194, v19, v201\n" \
"v_mfma_f32_16x16x32_bf16 v[24:27], a[204:207], a[188:191], v[24:27]\n" \
"s_waitcnt lgkmcnt(2)\n" \
"ds_read_b128 v[136:139], v254\n" \
"ds_read_b128 v[140:143], v254\n" \
"v_mfma_f32_16x16x32_bf16 v[4:7], a[208:211], a[128:131], v[4:7]\n" \
"ds_read_b128 v[144:147], v254\n" \
"ds_read_b128 v[148:151], v254\n" \
"v_mfma_f32_16x16x32_bf16 v[4:7], a[212:215], a[132:135], v[4:7]\n" \
"ds_read_b128 a[192:195], v249 offset:16384\n" \
"ds_read_b128 v[152:155], v254\n" \
"v_mfma_f32_16x16x32_bf16 v[4:7], a[216:219], a[136:139], v[4:7]\n" \
"ds_read_b128 v[156:159], v254\n" \
"ds_read_b128 v[160:163], v254\n" \
"v_max3_f32 v195, v24, v25, v26\n" \
"v_max3_f32 v195, v195, v27, v201\n" \
"v_mov_b32 v196, v192\n" \
"v_mov_b32 v197, v193\n" \
"v_mov_b32 v198, v194\n" \
"v_mov_b32 v199, v195\n" \
"v_permlane16_swap_b32 v196, v192\n" \
"v_permlane16_swap_b32 v197, v193\n" \
"v_permlane16_swap_b32 v198, v194\n" \
"v_permlane16_swap_b32 v199, v195\n" \
"v_max_f32 v192, v192, v196\n" \
"v_max_f32 v193, v193, v197\n" \
"v_mfma_f32_16x16x32_bf16 v[4:7], a[220:223], a[140:143], v[4:7]\n" \
"ds_read_b128 a[196:199], v249 offset:18432\n" \
"ds_read_b128 v[164:167], v254\n" \
"v_max_f32 v194, v194, v198\n" \
"v_max_f32 v195, v195, v199\n" \
"v_mov_b32 v196, v192\n" \
"v_mov_b32 v197, v193\n" \
"v_mov_b32 v198, v194\n" \
"v_mov_b32 v199, v195\n" \
"v_permlane32_swap_b32 v196, v192\n" \
"v_permlane32_swap_b32 v197, v193\n" \
"v_permlane32_swap_b32 v198, v194\n" \
"v_permlane32_swap_b32 v199, v195\n" \
"v_max_f32 v192, v192, v196\n" \
"v_max_f32 v193, v193, v197\n" \
"v_mfma_f32_16x16x32_bf16 v[12:15], a[216:219], a[152:155], 0\n" \
"ds_read_b128 v[168:171], v254\n" \
"ds_read_b128 v[172:175], v254\n" \
"v_max_f32 v194, v194, v198\n" \
"v_max_f32 v195, v195, v199\n" \
"v_sub_f32 v224, 0, v192\n" \
"v_sub_f32 v225, 0, v192\n" \
"v_sub_f32 v226, 0, v192\n" \
"v_sub_f32 v227, 0, v192\n" \
"v_add_f32 v32, v32, v224\n" \
"v_mfma_f32_16x16x32_bf16 v[12:15], a[220:223], a[156:159], v[12:15]\n" \
"ds_read_b128 a[200:203], v249 offset:20480\n" \
"ds_read_b128 v[176:179], v254\n" \
"v_mfma_f32_16x16x32_bf16 v[12:15], a[208:211], a[144:147], v[12:15]\n" \
"ds_read_b128 v[180:183], v254\n" \
"ds_read_b128 v[184:187], v254\n" \
"v_mfma_f32_16x16x32_bf16 v[12:15], a[212:215], a[148:151], v[12:15]\n" \
"ds_read_b128 a[204:207], v249 offset:22528\n" \
"buffer_store_dwordx4 a[40:43], v249, s[56:59], s90 offen offset:2304 sc1\n" \
"ds_write_b128 v200, a[160:163] offset:0\n" \
"s_add_u32 s90, s89, 417792\n" \
"v_mfma_f32_16x16x32_bf16 v[20:23], a[208:211], a[160:163], 0\n" \
"v_add_f32 v33, v33, v224\n" \
"v_add_f32 v34, v34, v224\n" \
"v_mfma_f32_16x16x32_bf16 v[20:23], a[212:215], a[164:167], v[20:23]\n" \
"v_add_f32 v35, v35, v224\n" \
"v_sub_f32 v228, 0, v193\n" \
"v_mfma_f32_16x16x32_bf16 v[20:23], a[216:219], a[168:171], v[20:23]\n" \
"ds_read_b128 a[40:43], v254\n" \
"ds_read_b128 v[188:191], v254\n" \
"v_mfma_f32_16x16x32_bf16 v[20:23], a[220:223], a[172:175], v[20:23]\n" \
"buffer_store_dwordx4 a[16:19], v248, s[56:59], s90 offen offset:0 sc1\n" \
"ds_write_b128 v200, a[164:167] offset:1024\n" \
"v_sub_f32 v229, 0, v193\n" \
"v_mfma_f32_16x16x32_bf16 v[28:31], a[216:219], a[184:187], 0\n" \
"v_sub_f32 v230, 0, v193\n" \
"v_sub_f32 v231, 0, v193\n" \
"v_mfma_f32_16x16x32_bf16 v[28:31], a[220:223], a[188:191], v[28:31]\n" \
"v_sub_f32 v232, 0, v194\n" \
"v_sub_f32 v233, 0, v194\n" \
"v_mfma_f32_16x16x32_bf16 v[28:31], a[208:211], a[176:179], v[28:31]\n" \
"buffer_store_dwordx4 a[20:23], v248, s[56:59], s90 offen offset:1024 sc1\n" \
"ds_write_b128 v200, a[168:171] offset:2048\n" \
"v_sub_f32 v234, 0, v194\n" \
"v_mfma_f32_16x16x32_bf16 v[28:31], a[212:215], a[180:183], v[28:31]\n" \
"v_sub_f32 v235, 0, v194\n" \
"v_sub_f32 v236, 0, v195\n" \
"v_mfma_f32_16x16x32_bf16 v[32:35], a[224:227], a[128:131], v[32:35]\n" \
"v_sub_f32 v237, 0, v195\n" \
"v_sub_f32 v238, 0, v195\n" \
"v_mfma_f32_16x16x32_bf16 v[32:35], a[228:231], a[132:135], v[32:35]\n" \
"ds_read_b128 a[208:211], v249 offset:16640\n" \
"v_sub_f32 v239, 0, v195\n" \
"v_mfma_f32_16x16x32_bf16 v[32:35], a[232:235], a[136:139], v[32:35]\n" \
"ds_read_b128 a[44:47], v254\n" \
"v_add_f32 v36, v36, v224\n" \
"v_mfma_f32_16x16x32_bf16 v[32:35], a[236:239], a[140:143], v[32:35]\n" \
"ds_read_b128 a[212:215], v249 offset:18688\n" \
"ds_read_b128 a[16:19], v254\n" \
"v_mfma_f32_16x16x32_bf16 v[40:43], a[232:235], a[152:155], v[228:231]\n" \
"ds_read_b128 a[48:51], v254\n" \
"v_add_f32 v37, v37, v224\n" \
"v_mfma_f32_16x16x32_bf16 v[40:43], a[236:239], a[156:159], v[40:43]\n" \
"ds_read_b128 a[216:219], v249 offset:20736\n" \
"v_add_f32 v38, v38, v224\n" \
"v_mfma_f32_16x16x32_bf16 v[40:43], a[224:227], a[144:147], v[40:43]\n" \
"ds_read_b128 a[20:23], v254\n" \
"v_add_f32 v39, v39, v224\n" \
"v_mfma_f32_16x16x32_bf16 v[40:43], a[228:231], a[148:151], v[40:43]\n" \
"buffer_store_dwordx4 a[24:27], v248, s[56:59], s90 offen offset:2048 sc1\n" \
"ds_read_b128 a[220:223], v249 offset:22784\n" \
"ds_write_b128 v200, a[172:175] offset:3072\n" \
"v_pk_add_f32 v[0:1], v[0:1], v[224:225]\n" \
"v_pk_add_f32 v[2:3], v[2:3], v[224:225]\n" \
"v_pk_add_f32 v[4:5], v[4:5], v[224:225]\n" \
"v_pk_add_f32 v[6:7], v[6:7], v[224:225]\n" \
"v_exp_f32 v0, v0\n" \
"v_exp_f32 v1, v1\n" \
"v_exp_f32 v2, v2\n" \
"v_cvt_pk_bf16_f32 v0, v0, v1\n" \
"v_exp_f32 v3, v3\n" \
"v_exp_f32 v4, v4\n" \
"v_cvt_pk_bf16_f32 v1, v2, v3\n" \
"v_mfma_f32_16x16x32_bf16 v[48:51], a[224:227], a[160:163], v[232:235]\n" \
"v_exp_f32 v5, v5\n" \
"v_mfma_f32_16x16x32_bf16 v[48:51], a[228:231], a[164:167], v[48:51]\n" \
"v_exp_f32 v6, v6\n" \
"v_mfma_f32_16x16x32_bf16 v[48:51], a[232:235], a[168:171], v[48:51]\n" \
"ds_read_b128 a[52:55], v254\n" \
"v_cvt_pk_bf16_f32 v2, v4, v5\n" \
"v_mfma_f32_16x16x32_bf16 v[48:51], a[236:239], a[172:175], v[48:51]\n" \
"buffer_store_dwordx4 a[28:31], v248, s[56:59], s90 offen offset:3072 sc1\n" \
"ds_write_b128 v200, a[176:179] offset:4096\n" \
"v_exp_f32 v7, v7\n" \
"v_pk_add_f32 v[8:9], v[8:9], v[228:229]\n" \
"v_pk_add_f32 v[10:11], v[10:11], v[228:229]\n" \
"v_pk_add_f32 v[12:13], v[12:13], v[228:229]\n" \
"v_pk_add_f32 v[14:15], v[14:15], v[228:229]\n" \
"v_exp_f32 v8, v8\n" \
"v_cvt_pk_bf16_f32 v3, v6, v7\n" \
"v_exp_f32 v9, v9\n" \
"v_exp_f32 v10, v10\n" \
"v_cvt_pk_bf16_f32 v8, v8, v9\n" \
"v_exp_f32 v11, v11\n" \
"v_mfma_f32_16x16x32_bf16 v[56:59], a[232:235], a[184:187], v[236:239]\n" \
"global_load_dword v253, v252, s[40:41]\n" \
"v_mfma_f32_16x16x32_bf16 v[56:59], a[236:239], a[188:191], v[56:59]\n" \
"s_sub_u32 s80, s80, 1\n" \
"v_exp_f32 v12, v12\n" \
"v_mfma_f32_16x16x32_bf16 v[56:59], a[224:227], a[176:179], v[56:59]\n" \
"s_cmp_gt_i32 s80, 0\n" \
"ds_write_b128 v200, a[180:183] offset:5120\n" \
"v_cvt_pk_bf16_f32 v9, v10, v11\n" \
"v_exp_f32 v13, v13\n" \
"v_exp_f32 v14, v14\n" \
"v_cvt_pk_bf16_f32 v10, v12, v13\n" \
"v_exp_f32 v15, v15\n" \
"v_pk_add_f32 v[16:17], v[16:17], v[232:233]\n" \
"v_pk_add_f32 v[18:19], v[18:19], v[232:233]\n" \
"v_pk_add_f32 v[20:21], v[20:21], v[232:233]\n" \
"v_pk_add_f32 v[22:23], v[22:23], v[232:233]\n" \
"v_exp_f32 v16, v16\n" \
"v_cvt_pk_bf16_f32 v11, v14, v15\n" \
"v_mfma_f32_16x16x32_bf16 v[56:59], a[228:231], a[180:183], v[56:59]\n" \
"s_cselect_b32 s81, 0x80000, 0\n" \
"v_exp_f32 v17, v17\n" \
"v_mfma_f32_16x16x32_bf16 v[36:39], a[240:243], a[128:131], v[36:39]\n" \
"s_add_u32 s40, s40, s81\n" \
"v_exp_f32 v18, v18\n" \
"v_mfma_f32_16x16x32_bf16 v[36:39], a[244:247], a[132:135], v[36:39]\n" \
"ds_read_b128 a[224:227], v249 offset:24576\n" \
"v_cvt_pk_bf16_f32 v16, v16, v17\n" \
"v_mfma_f32_16x16x32_bf16 v[240:243], a[120:123], v[0:3], v[240:243]\n" \
"v_exp_f32 v19, v19\n" \
"v_mfma_f32_16x16x32_bf16 v[240:243], a[124:127], v[8:11], v[240:243]\n" \
"s_addc_u32 s41, s41, 0\n" \
"v_exp_f32 v20, v20\n" \
"v_mfma_f32_16x16x32_bf16 v[36:39], a[248:251], a[136:139], v[36:39]\n" \
"s_add_u32 m0, s95, 49152\n" \
"ds_read_b128 a[24:27], v254\n" \
"v_cvt_pk_bf16_f32 v17, v18, v19\n" \
"v_mfma_f32_16x16x32_bf16 v[36:39], a[252:255], a[140:143], v[36:39]\n" \
"ds_read_b128 a[228:231], v249 offset:26624\n" \
"global_load_lds_dwordx4 v250, s[36:37] offset:0 sc1\n" \
"ds_read_b128 a[56:59], v254\n" \
"v_exp_f32 v21, v21\n" \
"v_exp_f32 v22, v22\n" \
"v_mfma_f32_16x16x32_bf16 v[44:47], a[248:251], a[152:155], v[228:231]\n" \
"ds_read_b128 a[28:31], v254\n" \
"v_cvt_pk_bf16_f32 v18, v20, v21\n" \
"v_mfma_f32_16x16x32_bf16 v[44:47], a[252:255], a[156:159], v[44:47]\n" \
"ds_read_b128 a[232:235], v249 offset:28672\n" \
"global_load_lds_dwordx4 v250, s[36:37] offset:1024 sc1\n" \
"v_mfma_f32_16x16x32_bf16 v[44:47], a[240:243], a[144:147], v[44:47]\n" \
"ds_read_b128 a[60:63], v254\n" \
"global_load_lds_dwordx4 v250, s[36:37] offset:2048 sc1\n" \
"v_mfma_f32_16x16x32_bf16 v[44:47], a[244:247], a[148:151], v[44:47]\n" \
"ds_read_b128 a[236:239], v249 offset:30720\n" \
"global_load_lds_dwordx4 v250, s[36:37] offset:3072 sc1\n" \
"ds_write_b128 v200, a[184:187] offset:6144\n" \
"v_exp_f32 v23, v23\n" \
"v_pk_add_f32 v[24:25], v[24:25], v[236:237]\n" \
"v_pk_add_f32 v[26:27], v[26:27], v[236:237]\n" \
"v_pk_add_f32 v[28:29], v[28:29], v[236:237]\n" \
"v_pk_add_f32 v[30:31], v[30:31], v[236:237]\n" \
"v_exp_f32 v24, v24\n" \
"v_cvt_pk_bf16_f32 v19, v22, v23\n" \
"v_exp_f32 v25, v25\n" \
"v_exp_f32 v26, v26\n" \
"v_cvt_pk_bf16_f32 v24, v24, v25\n" \
"v_mfma_f32_16x16x32_bf16 v[242:245], a[120:123], v[16:19], v[242:245]\n" \
"v_exp_f32 v27, v27\n" \
"v_mfma_f32_16x16x32_bf16 v[52:55], a[240:243], a[160:163], v[232:235]\n" \
"s_add_u32 s36, s36, 17408\n" \
"v_mov_b32 v202, s60\n" \
"v_lshlrev_b32 v203, 28, s61\n" \
"v_mfma_f32_16x16x32_bf16 v[52:55], a[244:247], a[164:167], v[52:55]\n" \
"s_mov_b32 s42, s60\n" \
"v_mov_b32 v246, v249\n" \
"v_mov_b32 v254, 0xff800000\n" \
"v_mfma_f32_16x16x32_bf16 v[52:55], a[248:251], a[168:171], v[52:55]\n" \
"s_mov_b32 s43, s60\n" \
"v_exp_f32 v28, v28\n" \
"v_mfma_f32_16x16x32_bf16 v[52:55], a[252:255], a[172:175], v[52:55]\n" \
"s_mov_b64 s[8:9], 0\n" \
"ds_write_b128 v200, a[188:191] offset:7168\n" \
"v_cvt_pk_bf16_f32 v25, v26, v27\n" \
"v_exp_f32 v29, v29\n" \
"v_mfma_f32_16x16x32_bf16 v[60:63], a[248:251], a[184:187], v[236:239]\n" \
"s_mov_b64 s[10:11], -1\n" \
"v_exp_f32 v30, v30\n" \
"v_cvt_pk_bf16_f32 v26, v28, v29\n" \
"v_mfma_f32_16x16x32_bf16 v[60:63], a[252:255], a[188:191], v[60:63]\n" \
"s_mov_b64 s[92:93], 0\n" \
"v_exp_f32 v31, v31\n" \
"v_mfma_f32_16x16x32_bf16 v[60:63], a[240:243], a[176:179], v[60:63]\n" \
"v_cvt_pk_bf16_f32 v27, v30, v31\n" \
"s_lshl_b32 s84, s63, 2\n" \
"v_mfma_f32_16x16x32_bf16 v[60:63], a[244:247], a[180:183], v[60:63]\n" \
"v_mfma_f32_16x16x32_bf16 v[242:245], a[124:127], v[24:27], v[242:245]\n" \
"s_add_u32 s84, s84, s79\n" \
"ds_read_b128 a[240:243], v249 offset:24832\n" \
"ds_read_b128 a[244:247], v249 offset:26880\n" \
"ds_read_b128 a[248:251], v249 offset:28928\n" \
"s_cmp_eq_u32 s63, 0\n" \
"s_waitcnt vmcnt(16) lgkmcnt(3)\n" \
"s_barrier\n" \
"s_memrealtime s[12:13]\n" \
"s_cbranch_scc1 LA1l_%=\n" \
"s_branch LA1_%=\n" \
"Lpg5386_%=:\n" \
"s_cbranch_execz Lpg6288_%=\n" \
"Lloop_%=:\n" \
"v_mfma_f32_16x16x32_bf16 a[0:3], a[192:195], v[64:67], a[0:3]\n" \
"v_exp_f32 v0, v0\n" \
"v_mfma_f32_16x16x32_bf16 a[0:3], a[196:199], v[96:99], a[0:3]\n" \
"v_cvt_pk_bf16_f32 v123, v126, v127\n" \
"v_add_f32 v242, v242, v0\n" \
"v_exp_f32 v1, v1\n" \
"v_mfma_f32_16x16x32_bf16 a[32:35], a[192:195], v[72:75], a[32:35]\n" \
"s_add_u32 m0, s73, 0x4000\n" \
"ds_read_b128 a[244:247], v247 offset:30720\n" \
"v_add_f32 v242, v242, v1\n" \
"v_mfma_f32_16x16x32_bf16 a[32:35], a[196:199], v[104:107], a[32:35]\n" \
"ds_read_b128 a[248:251], v247 offset:23552\n" \
"global_load_lds_dwordx4 v250, s[36:37] offset:0 sc1\n" \
"v_mfma_f32_16x16x32_bf16 v[128:131], a[192:195], v[80:83], v[128:131]\n" \
"ds_read_b128 a[252:255], v247 offset:31744\n" \
"v_exp_f32 v2, v2\n" \
"v_mfma_f32_16x16x32_bf16 v[128:131], a[196:199], v[112:115], v[128:131]\n" \
"v_add_f32 v242, v242, v2\n" \
"v_cvt_pk_bf16_f32 v0, v0, v1\n" \
"v_mfma_f32_16x16x32_bf16 v[160:163], a[192:195], v[88:91], v[160:163]\n" \
"v_exp_f32 v3, v3\n" \
"v_mfma_f32_16x16x32_bf16 v[160:163], a[196:199], v[120:123], v[160:163]\n" \
"ds_read_b128 a[192:195], v246 offset:16384\n" \
"v_xor_b32 v247, 0x8000, v247\n" \
"v_add_f32 v242, v242, v3\n" \
"v_mfma_f32_16x16x32_bf16 v[164:167], a[200:203], v[88:91], v[164:167]\n" \
"v_exp_f32 v4, v4\n" \
"v_mfma_f32_16x16x32_bf16 v[164:167], a[204:207], v[120:123], v[164:167]\n" \
"v_add_f32 v242, v242, v4\n" \
"v_cvt_pk_bf16_f32 v1, v2, v3\n" \
"v_mfma_f32_16x16x32_bf16 v[132:135], a[200:203], v[80:83], v[132:135]\n" \
"v_exp_f32 v5, v5\n" \
"v_mfma_f32_16x16x32_bf16 v[132:135], a[204:207], v[112:115], v[132:135]\n" \
"ds_read_b128 a[196:199], v246 offset:18432\n" \
"v_add_f32 v242, v242, v5\n" \
"v_mfma_f32_16x16x32_bf16 a[36:39], a[200:203], v[72:75], a[36:39]\n" \
"v_exp_f32 v6, v6\n" \
"v_mfma_f32_16x16x32_bf16 a[36:39], a[204:207], v[104:107], a[36:39]\n" \
"v_add_f32 v242, v242, v6\n" \
"v_cvt_pk_bf16_f32 v2, v4, v5\n" \
"v_mfma_f32_16x16x32_bf16 a[4:7], a[200:203], v[64:67], a[4:7]\n" \
"v_exp_f32 v7, v7\n" \
"v_mfma_f32_16x16x32_bf16 a[4:7], a[204:207], v[96:99], a[4:7]\n" \
"ds_read_b128 a[200:203], v246 offset:20480\n" \
"global_load_lds_dwordx4 v250, s[36:37] offset:1024 sc1\n" \
"v_add_f32 v242, v242, v7\n" \
"v_mfma_f32_16x16x32_bf16 a[8:11], a[208:211], v[64:67], a[8:11]\n" \
"v_exp_f32 v8, v8\n" \
"v_mfma_f32_16x16x32_bf16 a[8:11], a[212:215], v[96:99], a[8:11]\n" \
"v_add_f32 v243, v243, v8\n" \
"v_cvt_pk_bf16_f32 v3, v6, v7\n" \
"v_mfma_f32_16x16x32_bf16 a[40:43], a[208:211], v[72:75], a[40:43]\n" \
"v_exp_f32 v9, v9\n" \
"v_mfma_f32_16x16x32_bf16 a[40:43], a[212:215], v[104:107], a[40:43]\n" \
"ds_read_b128 a[204:207], v246 offset:22528\n" \
"v_add_f32 v243, v243, v9\n" \
"v_mfma_f32_16x16x32_bf16 v[136:139], a[208:211], v[80:83], v[136:139]\n" \
"v_exp_f32 v10, v10\n" \
"v_mfma_f32_16x16x32_bf16 v[136:139], a[212:215], v[112:115], v[136:139]\n" \
"v_add_f32 v243, v243, v10\n" \
"v_cvt_pk_bf16_f32 v8, v8, v9\n" \
"v_mfma_f32_16x16x32_bf16 v[168:171], a[208:211], v[88:91], v[168:171]\n" \
"v_exp_f32 v11, v11\n" \
"v_mfma_f32_16x16x32_bf16 v[168:171], a[212:215], v[120:123], v[168:171]\n" \
"ds_read_b128 a[208:211], v246 offset:16640\n" \
"v_add_f32 v243, v243, v11\n" \
"v_mfma_f32_16x16x32_bf16 v[172:175], a[216:219], v[88:91], v[172:175]\n" \
"v_exp_f32 v12, v12\n" \
"v_mfma_f32_16x16x32_bf16 v[172:175], a[220:223], v[120:123], v[172:175]\n" \
"v_add_f32 v243, v243, v12\n" \
"v_cvt_pk_bf16_f32 v9, v10, v11\n" \
"v_mfma_f32_16x16x32_bf16 v[140:143], a[216:219], v[80:83], v[140:143]\n" \
"v_exp_f32 v13, v13\n" \
"v_mfma_f32_16x16x32_bf16 v[140:143], a[220:223], v[112:115], v[140:143]\n" \
"ds_read_b128 a[212:215], v246 offset:18688\n" \
"global_load_lds_dwordx4 v250, s[36:37] offset:2048 sc1\n" \
"v_add_f32 v243, v243, v13\n" \
"v_mfma_f32_16x16x32_bf16 a[44:47], a[216:219], v[72:75], a[44:47]\n" \
"v_exp_f32 v14, v14\n" \
"v_mfma_f32_16x16x32_bf16 a[44:47], a[220:223], v[104:107], a[44:47]\n" \
"v_add_f32 v243, v243, v14\n" \
"v_cvt_pk_bf16_f32 v10, v12, v13\n" \
"v_mfma_f32_16x16x32_bf16 a[12:15], a[216:219], v[64:67], a[12:15]\n" \
"s_waitcnt lgkmcnt(6)\n" \
"v_exp_f32 v15, v15\n" \
"v_mfma_f32_16x16x32_bf16 a[12:15], a[220:223], v[96:99], a[12:15]\n" \
"ds_read_b128 a[216:219], v246 offset:20736\n" \
"v_add_f32 v243, v243, v15\n" \
"v_mfma_f32_16x16x32_bf16 a[16:19], a[224:227], v[64:67], a[16:19]\n" \
"v_exp_f32 v16, v16\n" \
"v_mfma_f32_16x16x32_bf16 a[16:19], a[228:231], v[96:99], a[16:19]\n" \
"v_add_f32 v244, v244, v16\n" \
"v_cvt_pk_bf16_f32 v11, v14, v15\n" \
"v_mfma_f32_16x16x32_bf16 a[48:51], a[224:227], v[72:75], a[48:51]\n" \
"v_exp_f32 v17, v17\n" \
"v_mfma_f32_16x16x32_bf16 a[48:51], a[228:231], v[104:107], a[48:51]\n" \
"ds_read_b128 a[220:223], v246 offset:22784\n" \
"global_load_lds_dwordx4 v250, s[36:37] offset:3072 sc1\n" \
"v_add_f32 v244, v244, v17\n" \
"v_mfma_f32_16x16x32_bf16 v[144:147], a[224:227], v[80:83], v[144:147]\n" \
"s_add_u32 s36, s36, 17408\n" \
"v_exp_f32 v18, v18\n" \
"v_mfma_f32_16x16x32_bf16 v[144:147], a[228:231], v[112:115], v[144:147]\n" \
"s_xor_b32 s73, s73, 0x8000\n" \
"v_add_f32 v244, v244, v18\n" \
"v_cvt_pk_bf16_f32 v16, v16, v17\n" \
"v_mfma_f32_16x16x32_bf16 v[176:179], a[224:227], v[88:91], v[176:179]\n" \
"s_xor_b64 s[52:53], s[44:45], s[42:43]\n" \
"v_exp_f32 v19, v19\n" \
"v_mfma_f32_16x16x32_bf16 v[176:179], a[228:231], v[120:123], v[176:179]\n" \
"s_xor_b64 s[54:55], s[46:47], s[42:43]\n" \
"ds_read_b128 a[224:227], v246 offset:24576\n" \
"v_add_f32 v244, v244, v19\n" \
"v_mfma_f32_16x16x32_bf16 v[180:183], a[232:235], v[88:91], v[180:183]\n" \
"s_or_b64 s[52:53], s[52:53], s[54:55]\n" \
"v_exp_f32 v20, v20\n" \
"v_mfma_f32_16x16x32_bf16 v[180:183], a[236:239], v[120:123], v[180:183]\n" \
"s_xor_b64 s[54:55], s[48:49], s[42:43]\n" \
"v_add_f32 v244, v244, v20\n" \
"v_cvt_pk_bf16_f32 v17, v18, v19\n" \
"v_mfma_f32_16x16x32_bf16 v[148:151], a[232:235], v[80:83], v[148:151]\n" \
"s_or_b64 s[52:53], s[52:53], s[54:55]\n" \
"v_exp_f32 v21, v21\n" \
"v_mfma_f32_16x16x32_bf16 v[148:151], a[236:239], v[112:115], v[148:151]\n" \
"s_xor_b64 s[54:55], s[50:51], s[42:43]\n" \
"ds_read_b128 a[228:231], v246 offset:26624\n" \
"v_add_f32 v244, v244, v21\n" \
"v_exp_f32 v22, v22\n" \
"v_mfma_f32_16x16x32_bf16 a[52:55], a[232:235], v[72:75], a[52:55]\n" \
"s_or_b64 s[52:53], s[52:53], s[54:55]\n" \
"v_add_f32 v244, v244, v22\n" \
"v_cvt_pk_bf16_f32 v18, v20, v21\n" \
"v_mfma_f32_16x16x32_bf16 a[52:55], a[236:239], v[104:107], a[52:55]\n" \
"s_and_b64 s[52:53], s[52:53], s[8:9]\n" \
"s_cbranch_scc1 Lrs0_%=\n" \
"Lrr0_%=:\n" \
"v_mfma_f32_16x16x32_bf16 a[20:23], a[232:235], v[64:67], a[20:23]\n" \
"s_mov_b64 s[8:9], s[10:11]\n" \
"v_exp_f32 v23, v23\n" \
"v_mfma_f32_16x16x32_bf16 a[20:23], a[236:239], v[96:99], a[20:23]\n" \
"ds_read_b128 a[232:235], v246 offset:28672\n" \
"v_add_f32 v244, v244, v23\n" \
"v_mfma_f32_16x16x32_bf16 a[24:27], a[240:243], v[64:67], a[24:27]\n" \
"v_exp_f32 v24, v24\n" \
"v_mfma_f32_16x16x32_bf16 a[24:27], a[244:247], v[96:99], a[24:27]\n" \
"v_add_f32 v245, v245, v24\n" \
"Lpg5534_%=:\n" \
"s_cbranch_execz Lpg6770_%=\n" \
"v_cvt_pk_bf16_f32 v19, v22, v23\n" \
"v_mfma_f32_16x16x32_bf16 a[56:59], a[240:243], v[72:75], a[56:59]\n" \
"v_exp_f32 v25, v25\n" \
"v_mfma_f32_16x16x32_bf16 a[56:59], a[244:247], v[104:107], a[56:59]\n" \
"ds_read_b128 a[236:239], v246 offset:30720\n" \
"v_add_f32 v245, v245, v25\n" \
"v_mfma_f32_16x16x32_bf16 v[152:155], a[240:243], v[80:83], v[152:155]\n" \
"v_exp_f32 v26, v26\n" \
"v_mfma_f32_16x16x32_bf16 v[152:155], a[244:247], v[112:115], v[152:155]\n" \
"v_add_f32 v245, v245, v26\n" \
"v_cvt_pk_bf16_f32 v24, v24, v25\n" \
"v_mfma_f32_16x16x32_bf16 v[184:187], a[240:243], v[88:91], v[184:187]\n" \
"v_exp_f32 v27, v27\n" \
"v_mfma_f32_16x16x32_bf16 v[184:187], a[244:247], v[120:123], v[184:187]\n" \
"ds_read_b128 a[240:243], v246 offset:24832\n" \
"v_add_f32 v245, v245, v27\n" \
"v_mfma_f32_16x16x32_bf16 v[188:191], a[248:251], v[88:91], v[188:191]\n" \
"v_exp_f32 v28, v28\n" \
"v_mfma_f32_16x16x32_bf16 v[188:191], a[252:255], v[120:123], v[188:191]\n" \
"v_add_f32 v245, v245, v28\n" \
"v_cvt_pk_bf16_f32 v25, v26, v27\n" \
"v_mfma_f32_16x16x32_bf16 v[156:159], a[248:251], v[80:83], v[156:159]\n" \
"v_exp_f32 v29, v29\n" \
"v_mfma_f32_16x16x32_bf16 v[156:159], a[252:255], v[112:115], v[156:159]\n" \
"ds_read_b128 a[244:247], v246 offset:26880\n" \
"v_add_f32 v245, v245, v29\n" \
"v_mfma_f32_16x16x32_bf16 a[60:63], a[248:251], v[72:75], a[60:63]\n" \
"v_exp_f32 v30, v30\n" \
"v_mfma_f32_16x16x32_bf16 a[60:63], a[252:255], v[104:107], a[60:63]\n" \
"v_add_f32 v245, v245, v30\n" \
"v_cvt_pk_bf16_f32 v26, v28, v29\n" \
"v_mfma_f32_16x16x32_bf16 a[28:31], a[248:251], v[64:67], a[28:31]\n" \
"s_waitcnt lgkmcnt(6)\n" \
"v_exp_f32 v31, v31\n" \
"v_mfma_f32_16x16x32_bf16 a[28:31], a[252:255], v[96:99], a[28:31]\n" \
"ds_read_b128 a[248:251], v246 offset:28928\n" \
"v_add_f32 v245, v245, v31\n" \
"LA1_%=:\n" \
"Lpg5573_%=:\n" \
"s_cbranch_execz Lpg7025_%=\n" \
"v_mfma_f32_16x16x32_bf16 v[64:67], a[192:195], a[128:131], v[224:227]\n" \
"v_exp_f32 v32, v32\n" \
"v_mfma_f32_16x16x32_bf16 v[64:67], a[196:199], a[132:135], v[64:67]\n" \
"v_cvt_pk_bf16_f32 v27, v30, v31\n" \
"v_add_f32 v242, v242, v32\n" \
"v_mfma_f32_16x16x32_bf16 v[64:67], a[200:203], a[136:139], v[64:67]\n" \
"s_mov_b32 m0, s74\n" \
"v_exp_f32 v33, v33\n" \
"v_mfma_f32_16x16x32_bf16 v[64:67], a[204:207], a[140:143], v[64:67]\n" \
"ds_read_b128 a[252:255], v246 offset:30976\n" \
"global_load_lds_dwordx4 v250, s[38:39] offset:0 sc1\n" \
"v_add_f32 v242, v242, v33\n" \
"v_mfma_f32_16x16x32_bf16 v[72:75], a[200:203], a[152:155], v[228:231]\n" \
"v_exp_f32 v34, v34\n" \
"v_mfma_f32_16x16x32_bf16 v[72:75], a[204:207], a[156:159], v[72:75]\n" \
"v_add_f32 v242, v242, v34\n" \
"v_cvt_pk_bf16_f32 v32, v32, v33\n" \
"v_mfma_f32_16x16x32_bf16 v[72:75], a[192:195], a[144:147], v[72:75]\n" \
"v_exp_f32 v35, v35\n" \
"v_mfma_f32_16x16x32_bf16 v[72:75], a[196:199], a[148:151], v[72:75]\n" \
"v_add_f32 v242, v242, v35\n" \
"v_exp_f32 v36, v36\n" \
"v_mfma_f32_16x16x32_bf16 v[80:83], a[192:195], a[160:163], v[232:235]\n" \
"v_add_f32 v242, v242, v36\n" \
"v_cvt_pk_bf16_f32 v33, v34, v35\n" \
"v_mfma_f32_16x16x32_bf16 v[80:83], a[196:199], a[164:167], v[80:83]\n" \
"v_exp_f32 v37, v37\n" \
"v_mfma_f32_16x16x32_bf16 v[80:83], a[200:203], a[168:171], v[80:83]\n" \
"v_add_f32 v242, v242, v37\n" \
"v_mfma_f32_16x16x32_bf16 v[80:83], a[204:207], a[172:175], v[80:83]\n" \
"v_exp_f32 v38, v38\n" \
"v_mfma_f32_16x16x32_bf16 v[88:91], a[200:203], a[184:187], v[236:239]\n" \
"v_add_f32 v242, v242, v38\n" \
"v_cvt_pk_bf16_f32 v34, v36, v37\n" \
"v_mfma_f32_16x16x32_bf16 v[88:91], a[204:207], a[188:191], v[88:91]\n" \
"v_exp_f32 v39, v39\n" \
"v_mfma_f32_16x16x32_bf16 v[88:91], a[192:195], a[176:179], v[88:91]\n" \
"v_add_f32 v242, v242, v39\n" \
"v_mfma_f32_16x16x32_bf16 v[88:91], a[196:199], a[180:183], v[88:91]\n" \
"ds_read_b128 a[192:195], v247 offset:0\n" \
"v_xor_b32 v246, 0x8000, v246\n" \
"v_mfma_f32_16x16x32_bf16 v[92:95], a[208:211], a[176:179], v[236:239]\n" \
"v_exp_f32 v40, v40\n" \
"v_mfma_f32_16x16x32_bf16 v[92:95], a[212:215], a[180:183], v[92:95]\n" \
"v_add_f32 v243, v243, v40\n" \
"v_cvt_pk_bf16_f32 v35, v38, v39\n" \
"v_mfma_f32_16x16x32_bf16 v[92:95], a[216:219], a[184:187], v[92:95]\n" \
"v_exp_f32 v41, v41\n" \
"v_mfma_f32_16x16x32_bf16 v[92:95], a[220:223], a[188:191], v[92:95]\n" \
"ds_read_b128 a[196:199], v247 offset:8192\n" \
"global_load_lds_dwordx4 v250, s[38:39] offset:1024 sc1\n" \
"v_add_f32 v243, v243, v41\n" \
"v_mfma_f32_16x16x32_bf16 v[84:87], a[216:219], a[168:171], v[232:235]\n" \
"v_exp_f32 v42, v42\n" \
"v_mfma_f32_16x16x32_bf16 v[84:87], a[220:223], a[172:175], v[84:87]\n" \
"v_add_f32 v243, v243, v42\n" \
"v_cvt_pk_bf16_f32 v40, v40, v41\n" \
"v_mfma_f32_16x16x32_bf16 v[84:87], a[208:211], a[160:163], v[84:87]\n" \
"v_exp_f32 v43, v43\n" \
"v_mfma_f32_16x16x32_bf16 v[84:87], a[212:215], a[164:167], v[84:87]\n" \
"ds_read_b128 a[200:203], v247 offset:1024\n" \
"v_add_f32 v243, v243, v43\n" \
"v_mfma_f32_16x16x32_bf16 v[76:79], a[208:211], a[144:147], v[228:231]\n" \
"v_exp_f32 v44, v44\n" \
"v_mfma_f32_16x16x32_bf16 v[76:79], a[212:215], a[148:151], v[76:79]\n" \
"v_add_f32 v243, v243, v44\n" \
"v_cvt_pk_bf16_f32 v41, v42, v43\n" \
"v_mfma_f32_16x16x32_bf16 v[76:79], a[216:219], a[152:155], v[76:79]\n" \
"v_exp_f32 v45, v45\n" \
"v_mfma_f32_16x16x32_bf16 v[76:79], a[220:223], a[156:159], v[76:79]\n" \
"ds_read_b128 a[204:207], v247 offset:9216\n" \
"global_load_lds_dwordx4 v250, s[38:39] offset:2048 sc1\n" \
"v_add_f32 v243, v243, v45\n" \
"v_mfma_f32_16x16x32_bf16 v[68:71], a[216:219], a[136:139], v[224:227]\n" \
"v_exp_f32 v46, v46\n" \
"v_mfma_f32_16x16x32_bf16 v[68:71], a[220:223], a[140:143], v[68:71]\n" \
"v_add_f32 v243, v243, v46\n" \
"v_cvt_pk_bf16_f32 v42, v44, v45\n" \
"v_mfma_f32_16x16x32_bf16 v[68:71], a[208:211], a[128:131], v[68:71]\n" \
"s_waitcnt lgkmcnt(4)\n" \
"v_exp_f32 v47, v47\n" \
"v_mfma_f32_16x16x32_bf16 v[68:71], a[212:215], a[132:135], v[68:71]\n" \
"ds_read_b128 a[208:211], v247 offset:2048\n" \
"v_add_f32 v243, v243, v47\n" \
"v_mfma_f32_16x16x32_bf16 v[96:99], a[224:227], a[128:131], v[224:227]\n" \
"v_exp_f32 v48, v48\n" \
"v_mfma_f32_16x16x32_bf16 v[96:99], a[228:231], a[132:135], v[96:99]\n" \
"v_add_f32 v244, v244, v48\n" \
"v_cvt_pk_bf16_f32 v43, v46, v47\n" \
"v_mfma_f32_16x16x32_bf16 v[96:99], a[232:235], a[136:139], v[96:99]\n" \
"v_exp_f32 v49, v49\n" \
"v_mfma_f32_16x16x32_bf16 v[96:99], a[236:239], a[140:143], v[96:99]\n" \
"ds_read_b128 a[212:215], v247 offset:10240\n" \
"v_add_f32 v244, v244, v49\n" \
"v_mfma_f32_16x16x32_bf16 v[104:107], a[232:235], a[152:155], v[228:231]\n" \
"v_exp_f32 v50, v50\n" \
"v_mfma_f32_16x16x32_bf16 v[104:107], a[236:239], a[156:159], v[104:107]\n" \
"v_add_f32 v244, v244, v50\n" \
"v_cvt_pk_bf16_f32 v48, v48, v49\n" \
"v_mfma_f32_16x16x32_bf16 v[104:107], a[224:227], a[144:147], v[104:107]\n" \
"v_exp_f32 v51, v51\n" \
"v_mfma_f32_16x16x32_bf16 v[104:107], a[228:231], a[148:151], v[104:107]\n" \
"ds_read_b128 a[216:219], v247 offset:3072\n" \
"global_load_lds_dwordx4 v250, s[38:39] offset:3072 sc1\n" \
"v_add_f32 v244, v244, v51\n" \
"v_mfma_f32_16x16x32_bf16 v[112:115], a[224:227], a[160:163], v[232:235]\n" \
"s_add_u32 s38, s38, 17408\n" \
"v_exp_f32 v52, v52\n" \
"v_mfma_f32_16x16x32_bf16 v[112:115], a[228:231], a[164:167], v[112:115]\n" \
"v_add_f32 v244, v244, v52\n" \
"v_cvt_pk_bf16_f32 v49, v50, v51\n" \
"v_mfma_f32_16x16x32_bf16 v[112:115], a[232:235], a[168:171], v[112:115]\n" \
"v_exp_f32 v53, v53\n" \
"v_mfma_f32_16x16x32_bf16 v[112:115], a[236:239], a[172:175], v[112:115]\n" \
"ds_read_b128 a[220:223], v247 offset:11264\n" \
"v_add_f32 v244, v244, v53\n" \
"v_exp_f32 v54, v54\n" \
"v_mfma_f32_16x16x32_bf16 v[120:123], a[232:235], a[184:187], v[236:239]\n" \
"global_load_dword v253, v252, s[40:41]\n" \
"v_mfma_f32_16x16x32_bf16 v[120:123], a[236:239], a[188:191], v[120:123]\n" \
"s_sub_u32 s80, s80, 1\n" \
"v_add_f32 v244, v244, v54\n" \
"v_cvt_pk_bf16_f32 v50, v52, v53\n" \
"v_mfma_f32_16x16x32_bf16 v[120:123], a[224:227], a[176:179], v[120:123]\n" \
"s_cmp_gt_i32 s80, 0\n" \
"v_exp_f32 v55, v55\n" \
"v_mfma_f32_16x16x32_bf16 v[120:123], a[228:231], a[180:183], v[120:123]\n" \
"s_cselect_b32 s81, 0x80000, 0\n" \
"ds_read_b128 a[224:227], v247 offset:4096\n" \
"v_add_f32 v244, v244, v55\n" \
"v_mfma_f32_16x16x32_bf16 v[124:127], a[240:243], a[176:179], v[236:239]\n" \
"s_add_u32 s40, s40, s81\n" \
"v_exp_f32 v56, v56\n" \
"v_mfma_f32_16x16x32_bf16 v[124:127], a[244:247], a[180:183], v[124:127]\n" \
"s_addc_u32 s41, s41, 0\n" \
"v_add_f32 v245, v245, v56\n" \
"v_cvt_pk_bf16_f32 v51, v54, v55\n" \
"v_mfma_f32_16x16x32_bf16 v[124:127], a[248:251], a[184:187], v[124:127]\n" \
"v_exp_f32 v57, v57\n" \
"v_mfma_f32_16x16x32_bf16 v[124:127], a[252:255], a[188:191], v[124:127]\n" \
"ds_read_b128 a[228:231], v247 offset:12288\n" \
"v_add_f32 v245, v245, v57\n" \
"v_mfma_f32_16x16x32_bf16 v[116:119], a[248:251], a[168:171], v[232:235]\n" \
"v_exp_f32 v58, v58\n" \
"v_mfma_f32_16x16x32_bf16 v[116:119], a[252:255], a[172:175], v[116:119]\n" \
"v_add_f32 v245, v245, v58\n" \
"v_cvt_pk_bf16_f32 v56, v56, v57\n" \
"v_mfma_f32_16x16x32_bf16 v[116:119], a[240:243], a[160:163], v[116:119]\n" \
"v_exp_f32 v59, v59\n" \
"v_mfma_f32_16x16x32_bf16 v[116:119], a[244:247], a[164:167], v[116:119]\n" \
"ds_read_b128 a[232:235], v247 offset:5120\n" \
"v_add_f32 v245, v245, v59\n" \
"v_mfma_f32_16x16x32_bf16 v[108:111], a[240:243], a[144:147], v[228:231]\n" \
"v_exp_f32 v60, v60\n" \
"v_mfma_f32_16x16x32_bf16 v[108:111], a[244:247], a[148:151], v[108:111]\n" \
"v_add_f32 v245, v245, v60\n" \
"v_cvt_pk_bf16_f32 v57, v58, v59\n" \
"v_mfma_f32_16x16x32_bf16 v[108:111], a[248:251], a[152:155], v[108:111]\n" \
"v_exp_f32 v61, v61\n" \
"v_mfma_f32_16x16x32_bf16 v[108:111], a[252:255], a[156:159], v[108:111]\n" \
"ds_read_b128 a[236:239], v247 offset:13312\n" \
"v_add_f32 v245, v245, v61\n" \
"v_mfma_f32_16x16x32_bf16 v[100:103], a[248:251], a[136:139], v[224:227]\n" \
"v_exp_f32 v62, v62\n" \
"v_mfma_f32_16x16x32_bf16 v[100:103], a[252:255], a[140:143], v[100:103]\n" \
"v_add_f32 v245, v245, v62\n" \
"v_cvt_pk_bf16_f32 v58, v60, v61\n" \
"v_mfma_f32_16x16x32_bf16 v[100:103], a[240:243], a[128:131], v[100:103]\n" \
"s_waitcnt vmcnt(10) lgkmcnt(4)\n" \
"v_exp_f32 v63, v63\n" \
"v_mfma_f32_16x16x32_bf16 v[100:103], a[244:247], a[132:135], v[100:103]\n" \
"s_barrier\n" \
"ds_read_b128 a[240:243], v247 offset:6144\n" \
"v_add_f32 v245, v245, v63\n" \
"v_mfma_f32_16x16x32_bf16 a[0:3], a[192:195], v[0:3], a[0:3]\n" \
"buffer_store_dword v202, v203, s[56:59], s84 offen sc1\n" \
"v_mfma_f32_16x16x32_bf16 a[0:3], a[196:199], v[32:35], a[0:3]\n" \
"s_mov_b32 s84, 0x20000000\n" \
"v_cvt_pk_bf16_f32 v59, v62, v63\n" \
"v_exp_f32 v64, v64\n" \
"v_mfma_f32_16x16x32_bf16 a[32:35], a[192:195], v[8:11], a[32:35]\n" \
"s_mov_b32 m0, s73\n" \
"ds_read_b128 a[244:247], v247 offset:14336\n" \
"v_add_f32 v242, v242, v64\n" \
"v_mfma_f32_16x16x32_bf16 a[32:35], a[196:199], v[40:43], a[32:35]\n" \
"ds_read_b128 a[248:251], v247 offset:7168\n" \
"global_load_lds_dwordx4 v250, s[36:37] offset:0 sc1\n" \
"v_exp_f32 v65, v65\n" \
"v_mfma_f32_16x16x32_bf16 v[128:131], a[192:195], v[16:19], v[128:131]\n" \
"ds_read_b128 a[252:255], v247 offset:15360\n" \
"v_add_f32 v242, v242, v65\n" \
"v_exp_f32 v66, v66\n" \
"v_mfma_f32_16x16x32_bf16 v[128:131], a[196:199], v[48:51], v[128:131]\n" \
"v_add_f32 v242, v242, v66\n" \
"v_cvt_pk_bf16_f32 v64, v64, v65\n" \
"v_mfma_f32_16x16x32_bf16 v[160:163], a[192:195], v[24:27], v[160:163]\n" \
"v_exp_f32 v67, v67\n" \
"v_mfma_f32_16x16x32_bf16 v[160:163], a[196:199], v[56:59], v[160:163]\n" \
"ds_read_b128 a[192:195], v246 offset:0\n" \
"v_add_f32 v242, v242, v67\n" \
"v_mfma_f32_16x16x32_bf16 v[164:167], a[200:203], v[24:27], v[164:167]\n" \
"v_exp_f32 v68, v68\n" \
"v_mfma_f32_16x16x32_bf16 v[164:167], a[204:207], v[56:59], v[164:167]\n" \
"v_add_f32 v242, v242, v68\n" \
"v_cvt_pk_bf16_f32 v65, v66, v67\n" \
"v_mfma_f32_16x16x32_bf16 v[132:135], a[200:203], v[16:19], v[132:135]\n" \
"v_exp_f32 v69, v69\n" \
"v_mfma_f32_16x16x32_bf16 v[132:135], a[204:207], v[48:51], v[132:135]\n" \
"ds_read_b128 a[196:199], v246 offset:2048\n" \
"v_add_f32 v242, v242, v69\n" \
"v_mfma_f32_16x16x32_bf16 a[36:39], a[200:203], v[8:11], a[36:39]\n" \
"v_exp_f32 v70, v70\n" \
"v_mfma_f32_16x16x32_bf16 a[36:39], a[204:207], v[40:43], a[36:39]\n" \
"v_add_f32 v242, v242, v70\n" \
"v_cvt_pk_bf16_f32 v66, v68, v69\n" \
"v_mfma_f32_16x16x32_bf16 a[4:7], a[200:203], v[0:3], a[4:7]\n" \
"v_exp_f32 v71, v71\n" \
"v_mfma_f32_16x16x32_bf16 a[4:7], a[204:207], v[32:35], a[4:7]\n" \
"ds_read_b128 a[200:203], v246 offset:4096\n" \
"global_load_lds_dwordx4 v250, s[36:37] offset:1024 sc1\n" \
"v_add_f32 v242, v242, v71\n" \
"v_mfma_f32_16x16x32_bf16 a[8:11], a[208:211], v[0:3], a[8:11]\n" \
"v_exp_f32 v72, v72\n" \
"v_mfma_f32_16x16x32_bf16 a[8:11], a[212:215], v[32:35], a[8:11]\n" \
"v_add_f32 v243, v243, v72\n" \
"v_cvt_pk_bf16_f32 v67, v70, v71\n" \
"v_mfma_f32_16x16x32_bf16 a[40:43], a[208:211], v[8:11], a[40:43]\n" \
"v_exp_f32 v73, v73\n" \
"v_mfma_f32_16x16x32_bf16 a[40:43], a[212:215], v[40:43], a[40:43]\n" \
"ds_read_b128 a[204:207], v246 offset:6144\n" \
"v_add_f32 v243, v243, v73\n" \
"v_mfma_f32_16x16x32_bf16 v[136:139], a[208:211], v[16:19], v[136:139]\n" \
"v_exp_f32 v74, v74\n" \
"v_mfma_f32_16x16x32_bf16 v[136:139], a[212:215], v[48:51], v[136:139]\n" \
"v_add_f32 v243, v243, v74\n" \
"v_cvt_pk_bf16_f32 v72, v72, v73\n" \
"v_mfma_f32_16x16x32_bf16 v[168:171], a[208:211], v[24:27], v[168:171]\n" \
"v_exp_f32 v75, v75\n" \
"v_mfma_f32_16x16x32_bf16 v[168:171], a[212:215], v[56:59], v[168:171]\n" \
"ds_read_b128 a[208:211], v246 offset:256\n" \
"v_add_f32 v243, v243, v75\n" \
"v_mfma_f32_16x16x32_bf16 v[172:175], a[216:219], v[24:27], v[172:175]\n" \
"v_exp_f32 v76, v76\n" \
"v_mfma_f32_16x16x32_bf16 v[172:175], a[220:223], v[56:59], v[172:175]\n" \
"v_add_f32 v243, v243, v76\n" \
"v_cvt_pk_bf16_f32 v73, v74, v75\n" \
"v_mfma_f32_16x16x32_bf16 v[140:143], a[216:219], v[16:19], v[140:143]\n" \
"v_exp_f32 v77, v77\n" \
"v_mfma_f32_16x16x32_bf16 v[140:143], a[220:223], v[48:51], v[140:143]\n" \
"ds_read_b128 a[212:215], v246 offset:2304\n" \
"global_load_lds_dwordx4 v250, s[36:37] offset:2048 sc1\n" \
"v_add_f32 v243, v243, v77\n" \
"v_mfma_f32_16x16x32_bf16 a[44:47], a[216:219], v[8:11], a[44:47]\n" \
"v_exp_f32 v78, v78\n" \
"v_mfma_f32_16x16x32_bf16 a[44:47], a[220:223], v[40:43], a[44:47]\n" \
"v_add_f32 v243, v243, v78\n" \
"v_cvt_pk_bf16_f32 v74, v76, v77\n" \
"v_mfma_f32_16x16x32_bf16 a[12:15], a[216:219], v[0:3], a[12:15]\n" \
"s_waitcnt lgkmcnt(6)\n" \
"v_exp_f32 v79, v79\n" \
"v_mfma_f32_16x16x32_bf16 a[12:15], a[220:223], v[32:35], a[12:15]\n" \
"ds_read_b128 a[216:219], v246 offset:4352\n" \
"v_add_f32 v243, v243, v79\n" \
"v_mfma_f32_16x16x32_bf16 a[16:19], a[224:227], v[0:3], a[16:19]\n" \
"v_exp_f32 v80, v80\n" \
"v_mfma_f32_16x16x32_bf16 a[16:19], a[228:231], v[32:35], a[16:19]\n" \
"v_add_f32 v244, v244, v80\n" \
"v_cvt_pk_bf16_f32 v75, v78, v79\n" \
"v_mfma_f32_16x16x32_bf16 a[48:51], a[224:227], v[8:11], a[48:51]\n" \
"v_exp_f32 v81, v81\n" \
"v_mfma_f32_16x16x32_bf16 a[48:51], a[228:231], v[40:43], a[48:51]\n" \
"ds_read_b128 a[220:223], v246 offset:6400\n" \
"v_add_f32 v244, v244, v81\n" \
"v_mfma_f32_16x16x32_bf16 v[144:147], a[224:227], v[16:19], v[144:147]\n" \
"v_exp_f32 v82, v82\n" \
"v_mfma_f32_16x16x32_bf16 v[144:147], a[228:231], v[48:51], v[144:147]\n" \
"v_add_f32 v244, v244, v82\n" \
"v_cvt_pk_bf16_f32 v80, v80, v81\n" \
"v_mfma_f32_16x16x32_bf16 v[176:179], a[224:227], v[24:27], v[176:179]\n" \
"v_exp_f32 v83, v83\n" \
"v_mfma_f32_16x16x32_bf16 v[176:179], a[228:231], v[56:59], v[176:179]\n" \
"ds_read_b128 a[224:227], v246 offset:8192\n" \
"global_load_lds_dwordx4 v250, s[36:37] offset:3072 sc1\n" \
"v_add_f32 v244, v244, v83\n" \
"v_mfma_f32_16x16x32_bf16 v[180:183], a[232:235], v[24:27], v[180:183]\n" \
"s_add_u32 s36, s36, 17408\n" \
"v_exp_f32 v84, v84\n" \
"v_mfma_f32_16x16x32_bf16 v[180:183], a[236:239], v[56:59], v[180:183]\n" \
"v_add_f32 v244, v244, v84\n" \
"v_cvt_pk_bf16_f32 v81, v82, v83\n" \
"v_mfma_f32_16x16x32_bf16 v[148:151], a[232:235], v[16:19], v[148:151]\n" \
"v_exp_f32 v85, v85\n" \
"v_mfma_f32_16x16x32_bf16 v[148:151], a[236:239], v[48:51], v[148:151]\n" \
"ds_read_b128 a[228:231], v246 offset:10240\n" \
"v_add_f32 v244, v244, v85\n" \
"v_mfma_f32_16x16x32_bf16 a[52:55], a[232:235], v[8:11], a[52:55]\n" \
"v_exp_f32 v86, v86\n" \
"v_mfma_f32_16x16x32_bf16 a[52:55], a[236:239], v[40:43], a[52:55]\n" \
"v_add_f32 v244, v244, v86\n" \
"v_cvt_pk_bf16_f32 v82, v84, v85\n" \
"v_mfma_f32_16x16x32_bf16 a[20:23], a[232:235], v[0:3], a[20:23]\n" \
"v_exp_f32 v87, v87\n" \
"v_mfma_f32_16x16x32_bf16 a[20:23], a[236:239], v[32:35], a[20:23]\n" \
"ds_read_b128 a[232:235], v246 offset:12288\n" \
"v_add_f32 v244, v244, v87\n" \
"v_mfma_f32_16x16x32_bf16 a[24:27], a[240:243], v[0:3], a[24:27]\n" \
"v_exp_f32 v88, v88\n" \
"v_mfma_f32_16x16x32_bf16 a[24:27], a[244:247], v[32:35], a[24:27]\n" \
"v_add_f32 v245, v245, v88\n" \
"v_cvt_pk_bf16_f32 v83, v86, v87\n" \
"v_mfma_f32_16x16x32_bf16 a[56:59], a[240:243], v[8:11], a[56:59]\n" \
"v_exp_f32 v89, v89\n" \
"v_mfma_f32_16x16x32_bf16 a[56:59], a[244:247], v[40:43], a[56:59]\n" \
"ds_read_b128 a[236:239], v246 offset:14336\n" \
"v_add_f32 v245, v245, v89\n" \
"v_mfma_f32_16x16x32_bf16 v[152:155], a[240:243], v[16:19], v[152:155]\n" \
"v_exp_f32 v90, v90\n" \
"v_mfma_f32_16x16x32_bf16 v[152:155], a[244:247], v[48:51], v[152:155]\n" \
"v_add_f32 v245, v245, v90\n" \
"v_cvt_pk_bf16_f32 v88, v88, v89\n" \
"v_mfma_f32_16x16x32_bf16 v[184:187], a[240:243], v[24:27], v[184:187]\n" \
"v_exp_f32 v91, v91\n" \
"v_mfma_f32_16x16x32_bf16 v[184:187], a[244:247], v[56:59], v[184:187]\n" \
"ds_read_b128 a[240:243], v246 offset:8448\n" \
"v_add_f32 v245, v245, v91\n" \
"v_mfma_f32_16x16x32_bf16 v[188:191], a[248:251], v[24:27], v[188:191]\n" \
"v_exp_f32 v92, v92\n" \
"v_mfma_f32_16x16x32_bf16 v[188:191], a[252:255], v[56:59], v[188:191]\n" \
"v_add_f32 v245, v245, v92\n" \
"v_cvt_pk_bf16_f32 v89, v90, v91\n" \
"v_mfma_f32_16x16x32_bf16 v[156:159], a[248:251], v[16:19], v[156:159]\n" \
"v_exp_f32 v93, v93\n" \
"v_mfma_f32_16x16x32_bf16 v[156:159], a[252:255], v[48:51], v[156:159]\n" \
"ds_read_b128 a[244:247], v246 offset:10496\n" \
"v_add_f32 v245, v245, v93\n" \
"v_mfma_f32_16x16x32_bf16 a[60:63], a[248:251], v[8:11], a[60:63]\n" \
"v_exp_f32 v94, v94\n" \
"v_mfma_f32_16x16x32_bf16 a[60:63], a[252:255], v[40:43], a[60:63]\n" \
"v_add_f32 v245, v245, v94\n" \
"v_cvt_pk_bf16_f32 v90, v92, v93\n" \
"v_mfma_f32_16x16x32_bf16 a[28:31], a[248:251], v[0:3], a[28:31]\n" \
"s_waitcnt lgkmcnt(6)\n" \
"v_exp_f32 v95, v95\n" \
"v_mfma_f32_16x16x32_bf16 a[28:31], a[252:255], v[32:35], a[28:31]\n" \
"ds_read_b128 a[248:251], v246 offset:12544\n" \
"v_add_f32 v245, v245, v95\n" \
"v_mfma_f32_16x16x32_bf16 v[0:3], a[192:195], a[128:131], v[224:227]\n" \
"v_exp_f32 v96, v96\n" \
"v_mfma_f32_16x16x32_bf16 v[0:3], a[196:199], a[132:135], v[0:3]\n" \
"v_cvt_pk_bf16_f32 v91, v94, v95\n" \
"v_add_f32 v242, v242, v96\n" \
"v_mfma_f32_16x16x32_bf16 v[0:3], a[200:203], a[136:139], v[0:3]\n" \
"s_add_u32 m0, s74, 0x4000\n" \
"v_exp_f32 v97, v97\n" \
"v_mfma_f32_16x16x32_bf16 v[0:3], a[204:207], a[140:143], v[0:3]\n" \
"ds_read_b128 a[252:255], v246 offset:14592\n" \
"global_load_lds_dwordx4 v250, s[38:39] offset:0 sc1\n" \
"v_add_f32 v242, v242, v97\n" \
"v_mfma_f32_16x16x32_bf16 v[8:11], a[200:203], a[152:155], v[228:231]\n" \
"v_exp_f32 v98, v98\n" \
"v_mfma_f32_16x16x32_bf16 v[8:11], a[204:207], a[156:159], v[8:11]\n" \
"v_add_f32 v242, v242, v98\n" \
"v_cvt_pk_bf16_f32 v96, v96, v97\n" \
"v_mfma_f32_16x16x32_bf16 v[8:11], a[192:195], a[144:147], v[8:11]\n" \
"v_exp_f32 v99, v99\n" \
"v_mfma_f32_16x16x32_bf16 v[8:11], a[196:199], a[148:151], v[8:11]\n" \
"v_add_f32 v242, v242, v99\n" \
"v_exp_f32 v100, v100\n" \
"v_mfma_f32_16x16x32_bf16 v[16:19], a[192:195], a[160:163], v[232:235]\n" \
"v_add_f32 v242, v242, v100\n" \
"v_cvt_pk_bf16_f32 v97, v98, v99\n" \
"v_mfma_f32_16x16x32_bf16 v[16:19], a[196:199], a[164:167], v[16:19]\n" \
"v_exp_f32 v101, v101\n" \
"v_mfma_f32_16x16x32_bf16 v[16:19], a[200:203], a[168:171], v[16:19]\n" \
"v_add_f32 v242, v242, v101\n" \
"v_mfma_f32_16x16x32_bf16 v[16:19], a[204:207], a[172:175], v[16:19]\n" \
"v_exp_f32 v102, v102\n" \
"v_mfma_f32_16x16x32_bf16 v[24:27], a[200:203], a[184:187], v[236:239]\n" \
"v_add_f32 v242, v242, v102\n" \
"v_cvt_pk_bf16_f32 v98, v100, v101\n" \
"v_mfma_f32_16x16x32_bf16 v[24:27], a[204:207], a[188:191], v[24:27]\n" \
"v_exp_f32 v103, v103\n" \
"v_mfma_f32_16x16x32_bf16 v[24:27], a[192:195], a[176:179], v[24:27]\n" \
"v_add_f32 v242, v242, v103\n" \
"v_mfma_f32_16x16x32_bf16 v[24:27], a[196:199], a[180:183], v[24:27]\n" \
"ds_read_b128 a[192:195], v247 offset:16384\n" \
"global_load_lds_dwordx4 v250, s[38:39] offset:1024 sc1\n" \
"v_mfma_f32_16x16x32_bf16 v[28:31], a[208:211], a[176:179], v[236:239]\n" \
"v_exp_f32 v104, v104\n" \
"v_mfma_f32_16x16x32_bf16 v[28:31], a[212:215], a[180:183], v[28:31]\n" \
"v_add_f32 v243, v243, v104\n" \
"v_cvt_pk_bf16_f32 v99, v102, v103\n" \
"v_mfma_f32_16x16x32_bf16 v[28:31], a[216:219], a[184:187], v[28:31]\n" \
"v_exp_f32 v105, v105\n" \
"v_mfma_f32_16x16x32_bf16 v[28:31], a[220:223], a[188:191], v[28:31]\n" \
"ds_read_b128 a[196:199], v247 offset:24576\n" \
"v_add_f32 v243, v243, v105\n" \
"v_mfma_f32_16x16x32_bf16 v[20:23], a[216:219], a[168:171], v[232:235]\n" \
"v_exp_f32 v106, v106\n" \
"v_mfma_f32_16x16x32_bf16 v[20:23], a[220:223], a[172:175], v[20:23]\n" \
"v_add_f32 v243, v243, v106\n" \
"v_cvt_pk_bf16_f32 v104, v104, v105\n" \
"v_mfma_f32_16x16x32_bf16 v[20:23], a[208:211], a[160:163], v[20:23]\n" \
"v_exp_f32 v107, v107\n" \
"v_mfma_f32_16x16x32_bf16 v[20:23], a[212:215], a[164:167], v[20:23]\n" \
"ds_read_b128 a[200:203], v247 offset:17408\n" \
"v_add_f32 v243, v243, v107\n" \
"v_mfma_f32_16x16x32_bf16 v[12:15], a[208:211], a[144:147], v[228:231]\n" \
"v_exp_f32 v108, v108\n" \
"v_mfma_f32_16x16x32_bf16 v[12:15], a[212:215], a[148:151], v[12:15]\n" \
"v_add_f32 v243, v243, v108\n" \
"v_cvt_pk_bf16_f32 v105, v106, v107\n" \
"v_mfma_f32_16x16x32_bf16 v[12:15], a[216:219], a[152:155], v[12:15]\n" \
"v_exp_f32 v109, v109\n" \
"v_mfma_f32_16x16x32_bf16 v[12:15], a[220:223], a[156:159], v[12:15]\n" \
"ds_read_b128 a[204:207], v247 offset:25600\n" \
"global_load_lds_dwordx4 v250, s[38:39] offset:2048 sc1\n" \
"v_add_f32 v243, v243, v109\n" \
"v_mfma_f32_16x16x32_bf16 v[4:7], a[216:219], a[136:139], v[224:227]\n" \
"v_exp_f32 v110, v110\n" \
"v_mfma_f32_16x16x32_bf16 v[4:7], a[220:223], a[140:143], v[4:7]\n" \
"v_add_f32 v243, v243, v110\n" \
"v_cvt_pk_bf16_f32 v106, v108, v109\n" \
"v_mfma_f32_16x16x32_bf16 v[4:7], a[208:211], a[128:131], v[4:7]\n" \
"s_waitcnt lgkmcnt(4)\n" \
"v_exp_f32 v111, v111\n" \
"v_mfma_f32_16x16x32_bf16 v[4:7], a[212:215], a[132:135], v[4:7]\n" \
"ds_read_b128 a[208:211], v247 offset:18432\n" \
"v_add_f32 v243, v243, v111\n" \
"v_mfma_f32_16x16x32_bf16 v[32:35], a[224:227], a[128:131], v[224:227]\n" \
"v_exp_f32 v112, v112\n" \
"v_mfma_f32_16x16x32_bf16 v[32:35], a[228:231], a[132:135], v[32:35]\n" \
"v_add_f32 v244, v244, v112\n" \
"v_cvt_pk_bf16_f32 v107, v110, v111\n" \
"v_mfma_f32_16x16x32_bf16 v[32:35], a[232:235], a[136:139], v[32:35]\n" \
"v_exp_f32 v113, v113\n" \
"v_mfma_f32_16x16x32_bf16 v[32:35], a[236:239], a[140:143], v[32:35]\n" \
"ds_read_b128 a[212:215], v247 offset:26624\n" \
"v_add_f32 v244, v244, v113\n" \
"v_mfma_f32_16x16x32_bf16 v[40:43], a[232:235], a[152:155], v[228:231]\n" \
"v_exp_f32 v114, v114\n" \
"v_mfma_f32_16x16x32_bf16 v[40:43], a[236:239], a[156:159], v[40:43]\n" \
"v_add_f32 v244, v244, v114\n" \
"v_cvt_pk_bf16_f32 v112, v112, v113\n" \
"v_mfma_f32_16x16x32_bf16 v[40:43], a[224:227], a[144:147], v[40:43]\n" \
"v_exp_f32 v115, v115\n" \
"v_mfma_f32_16x16x32_bf16 v[40:43], a[228:231], a[148:151], v[40:43]\n" \
"ds_read_b128 a[216:219], v247 offset:19456\n" \
"global_load_lds_dwordx4 v250, s[38:39] offset:3072 sc1\n" \
"v_add_f32 v244, v244, v115\n" \
"v_exp_f32 v116, v116\n" \
"v_mfma_f32_16x16x32_bf16 v[48:51], a[224:227], a[160:163], v[232:235]\n" \
"s_add_u32 s38, s38, 17408\n" \
"v_add_f32 v244, v244, v116\n" \
"v_cvt_pk_bf16_f32 v113, v114, v115\n" \
"v_mfma_f32_16x16x32_bf16 v[48:51], a[228:231], a[164:167], v[48:51]\n" \
"global_load_dword v253, v252, s[40:41]\n" \
"v_mfma_f32_16x16x32_bf16 v[48:51], a[232:235], a[168:171], v[48:51]\n" \
"s_sub_u32 s80, s80, 1\n" \
"v_exp_f32 v117, v117\n" \
"v_mfma_f32_16x16x32_bf16 v[48:51], a[236:239], a[172:175], v[48:51]\n" \
"s_cmp_gt_i32 s80, 0\n" \
"ds_read_b128 a[220:223], v247 offset:27648\n" \
"v_add_f32 v244, v244, v117\n" \
"v_mfma_f32_16x16x32_bf16 v[56:59], a[232:235], a[184:187], v[236:239]\n" \
"s_cselect_b32 s81, 0x80000, 0\n" \
"v_exp_f32 v118, v118\n" \
"v_mfma_f32_16x16x32_bf16 v[56:59], a[236:239], a[188:191], v[56:59]\n" \
"s_add_u32 s40, s40, s81\n" \
"v_add_f32 v244, v244, v118\n" \
"v_cvt_pk_bf16_f32 v114, v116, v117\n" \
"v_mfma_f32_16x16x32_bf16 v[56:59], a[224:227], a[176:179], v[56:59]\n" \
"s_addc_u32 s41, s41, 0\n" \
"v_exp_f32 v119, v119\n" \
"v_mfma_f32_16x16x32_bf16 v[56:59], a[228:231], a[180:183], v[56:59]\n" \
"s_load_dwordx8 s[44:51], s[56:57], s79 glc\n" \
"ds_read_b128 a[224:227], v247 offset:20480\n" \
"v_add_f32 v244, v244, v119\n" \
"v_mfma_f32_16x16x32_bf16 v[60:63], a[240:243], a[176:179], v[236:239]\n" \
"s_xor_b32 s74, s74, 0x8000\n" \
"v_exp_f32 v120, v120\n" \
"v_mfma_f32_16x16x32_bf16 v[60:63], a[244:247], a[180:183], v[60:63]\n" \
"s_sub_u32 s75, s75, 1\n" \
"v_add_f32 v245, v245, v120\n" \
"v_cvt_pk_bf16_f32 v115, v118, v119\n" \
"v_mfma_f32_16x16x32_bf16 v[60:63], a[248:251], a[184:187], v[60:63]\n" \
"s_cmp_lg_u32 s75, 0\n" \
"v_exp_f32 v121, v121\n" \
"v_mfma_f32_16x16x32_bf16 v[60:63], a[252:255], a[188:191], v[60:63]\n" \
"ds_read_b128 a[228:231], v247 offset:28672\n" \
"v_add_f32 v245, v245, v121\n" \
"v_mfma_f32_16x16x32_bf16 v[52:55], a[248:251], a[168:171], v[232:235]\n" \
"v_exp_f32 v122, v122\n" \
"v_mfma_f32_16x16x32_bf16 v[52:55], a[252:255], a[172:175], v[52:55]\n" \
"v_add_f32 v245, v245, v122\n" \
"v_cvt_pk_bf16_f32 v120, v120, v121\n" \
"v_mfma_f32_16x16x32_bf16 v[52:55], a[240:243], a[160:163], v[52:55]\n" \
"v_exp_f32 v123, v123\n" \
"v_mfma_f32_16x16x32_bf16 v[52:55], a[244:247], a[164:167], v[52:55]\n" \
"ds_read_b128 a[232:235], v247 offset:21504\n" \
"v_add_f32 v245, v245, v123\n" \
"v_mfma_f32_16x16x32_bf16 v[44:47], a[240:243], a[144:147], v[228:231]\n" \
"v_exp_f32 v124, v124\n" \
"v_mfma_f32_16x16x32_bf16 v[44:47], a[244:247], a[148:151], v[44:47]\n" \
"v_add_f32 v245, v245, v124\n" \
"v_cvt_pk_bf16_f32 v121, v122, v123\n" \
"v_mfma_f32_16x16x32_bf16 v[44:47], a[248:251], a[152:155], v[44:47]\n" \
"v_exp_f32 v125, v125\n" \
"v_mfma_f32_16x16x32_bf16 v[44:47], a[252:255], a[156:159], v[44:47]\n" \
"ds_read_b128 a[236:239], v247 offset:29696\n" \
"v_add_f32 v245, v245, v125\n" \
"v_mfma_f32_16x16x32_bf16 v[36:39], a[248:251], a[136:139], v[224:227]\n" \
"v_exp_f32 v126, v126\n" \
"v_mfma_f32_16x16x32_bf16 v[36:39], a[252:255], a[140:143], v[36:39]\n" \
"v_add_f32 v245, v245, v126\n" \
"v_cvt_pk_bf16_f32 v122, v124, v125\n" \
"v_mfma_f32_16x16x32_bf16 v[36:39], a[240:243], a[128:131], v[36:39]\n" \
"s_waitcnt vmcnt(11) lgkmcnt(4)\n" \
"v_exp_f32 v127, v127\n" \
"v_mfma_f32_16x16x32_bf16 v[36:39], a[244:247], a[132:135], v[36:39]\n" \
"s_barrier\n" \
"ds_read_b128 a[240:243], v247 offset:22528\n" \
"v_add_f32 v245, v245, v127\n" \
"s_cbranch_scc1 Lloop_%=\n" \
"v_mfma_f32_16x16x32_bf16 a[0:3], a[192:195], v[64:67], a[0:3]\n" \
"v_exp_f32 v8, v8\n" \
"v_mfma_f32_16x16x32_bf16 a[0:3], a[196:199], v[96:99], a[0:3]\n" \
"v_cvt_pk_bf16_f32 v123, v126, v127\n" \
"v_exp_f32 v9, v9\n" \
"v_mfma_f32_16x16x32_bf16 a[32:35], a[192:195], v[72:75], a[32:35]\n" \
"s_add_u32 m0, s73, 0x4000\n" \
"ds_read_b128 a[244:247], v247 offset:30720\n" \
"v_exp_f32 v10, v10\n" \
"v_mfma_f32_16x16x32_bf16 a[32:35], a[196:199], v[104:107], a[32:35]\n" \
"ds_read_b128 a[248:251], v247 offset:23552\n" \
"global_load_lds_dwordx4 v250, s[36:37] offset:0 sc1\n" \
"v_mfma_f32_16x16x32_bf16 v[128:131], a[192:195], v[80:83], v[128:131]\n" \
"ds_read_b128 a[252:255], v247 offset:31744\n" \
"v_cvt_pk_bf16_f32 v8, v8, v9\n" \
"v_mfma_f32_16x16x32_bf16 v[128:131], a[196:199], v[112:115], v[128:131]\n" \
"v_exp_f32 v11, v11\n" \
"v_mfma_f32_16x16x32_bf16 v[160:163], a[192:195], v[88:91], v[160:163]\n" \
"v_exp_f32 v12, v12\n" \
"v_mfma_f32_16x16x32_bf16 v[160:163], a[196:199], v[120:123], v[160:163]\n" \
"ds_read_b128 a[192:195], v246 offset:16384\n" \
"v_xor_b32 v247, 0x8000, v247\n" \
"v_cvt_pk_bf16_f32 v9, v10, v11\n" \
"v_mfma_f32_16x16x32_bf16 v[164:167], a[200:203], v[88:91], v[164:167]\n" \
"v_exp_f32 v13, v13\n" \
"v_mfma_f32_16x16x32_bf16 v[164:167], a[204:207], v[120:123], v[164:167]\n" \
"v_exp_f32 v14, v14\n" \
"v_cvt_pk_bf16_f32 v10, v12, v13\n" \
"v_mfma_f32_16x16x32_bf16 v[132:135], a[200:203], v[80:83], v[132:135]\n" \
"v_exp_f32 v15, v15\n" \
"v_mfma_f32_16x16x32_bf16 v[132:135], a[204:207], v[112:115], v[132:135]\n" \
"ds_read_b128 a[196:199], v246 offset:18432\n" \
"v_cmp_gt_i32 vcc, 0, v251\n" \
"v_cmp_gt_i32 s[64:65], 1, v251\n" \
"v_mfma_f32_16x16x32_bf16 a[36:39], a[200:203], v[72:75], a[36:39]\n" \
"v_cmp_gt_i32 s[66:67], 2, v251\n" \
"v_cndmask_b32 v0, v0, v254, vcc\n" \
"v_mfma_f32_16x16x32_bf16 a[36:39], a[204:207], v[104:107], a[36:39]\n" \
"v_cmp_gt_i32 vcc, 3, v251\n" \
"v_cndmask_b32 v1, v1, v254, s[64:65]\n" \
"v_mfma_f32_16x16x32_bf16 a[4:7], a[200:203], v[64:67], a[4:7]\n" \
"v_cmp_gt_i32 s[64:65], 16, v251\n" \
"v_cndmask_b32 v2, v2, v254, s[66:67]\n" \
"v_mfma_f32_16x16x32_bf16 a[4:7], a[204:207], v[96:99], a[4:7]\n" \
"ds_read_b128 a[200:203], v246 offset:20480\n" \
"global_load_lds_dwordx4 v250, s[36:37] offset:1024 sc1\n" \
"v_mfma_f32_16x16x32_bf16 a[8:11], a[208:211], v[64:67], a[8:11]\n" \
"v_cmp_gt_i32 s[66:67], 17, v251\n" \
"v_cndmask_b32 v3, v3, v254, vcc\n" \
"v_mfma_f32_16x16x32_bf16 a[8:11], a[212:215], v[96:99], a[8:11]\n" \
"v_cmp_gt_i32 vcc, 18, v251\n" \
"v_cndmask_b32 v4, v4, v254, s[64:65]\n" \
"v_mfma_f32_16x16x32_bf16 a[40:43], a[208:211], v[72:75], a[40:43]\n" \
"v_cmp_gt_i32 s[64:65], 19, v251\n" \
"v_cndmask_b32 v5, v5, v254, s[66:67]\n" \
"v_mfma_f32_16x16x32_bf16 a[40:43], a[212:215], v[104:107], a[40:43]\n" \
"ds_read_b128 a[204:207], v246 offset:22528\n" \
"v_cmp_gt_i32 s[66:67], 32, v251\n" \
"v_mfma_f32_16x16x32_bf16 v[136:139], a[208:211], v[80:83], v[136:139]\n" \
"v_cndmask_b32 v6, v6, v254, vcc\n" \
"v_cmp_gt_i32 vcc, 33, v251\n" \
"v_mfma_f32_16x16x32_bf16 v[136:139], a[212:215], v[112:115], v[136:139]\n" \
"v_cndmask_b32 v7, v7, v254, s[64:65]\n" \
"v_cmp_gt_i32 s[64:65], 34, v251\n" \
"v_mfma_f32_16x16x32_bf16 v[168:171], a[208:211], v[88:91], v[168:171]\n" \
"v_cndmask_b32 v32, v32, v254, s[66:67]\n" \
"v_cmp_gt_i32 s[66:67], 35, v251\n" \
"v_mfma_f32_16x16x32_bf16 v[168:171], a[212:215], v[120:123], v[168:171]\n" \
"ds_read_b128 a[208:211], v246 offset:16640\n" \
"v_cndmask_b32 v33, v33, v254, vcc\n" \
"v_mfma_f32_16x16x32_bf16 v[172:175], a[216:219], v[88:91], v[172:175]\n" \
"v_cmp_gt_i32 vcc, 48, v251\n" \
"v_cndmask_b32 v34, v34, v254, s[64:65]\n" \
"Lpg6171_%=:\n" \
"s_cbranch_execz Lpg7550_%=\n" \
"v_mfma_f32_16x16x32_bf16 v[172:175], a[220:223], v[120:123], v[172:175]\n" \
"v_cmp_gt_i32 s[64:65], 49, v251\n" \
"v_cndmask_b32 v35, v35, v254, s[66:67]\n" \
"v_mfma_f32_16x16x32_bf16 v[140:143], a[216:219], v[80:83], v[140:143]\n" \
"v_cmp_gt_i32 s[66:67], 50, v251\n" \
"v_cndmask_b32 v36, v36, v254, vcc\n" \
"v_mfma_f32_16x16x32_bf16 v[140:143], a[220:223], v[112:115], v[140:143]\n" \
"ds_read_b128 a[212:215], v246 offset:18688\n" \
"global_load_lds_dwordx4 v250, s[36:37] offset:2048 sc1\n" \
"v_mfma_f32_16x16x32_bf16 a[44:47], a[216:219], v[72:75], a[44:47]\n" \
"v_cmp_gt_i32 vcc, 51, v251\n" \
"v_cndmask_b32 v37, v37, v254, s[64:65]\n" \
"v_mfma_f32_16x16x32_bf16 a[44:47], a[220:223], v[104:107], a[44:47]\n" \
"v_cndmask_b32 v38, v38, v254, s[66:67]\n" \
"v_cndmask_b32 v39, v39, v254, vcc\n" \
"v_mfma_f32_16x16x32_bf16 a[12:15], a[216:219], v[64:67], a[12:15]\n" \
"s_waitcnt lgkmcnt(6)\n" \
"v_exp_f32 v0, v0\n" \
"v_mfma_f32_16x16x32_bf16 a[12:15], a[220:223], v[96:99], a[12:15]\n" \
"ds_read_b128 a[216:219], v246 offset:20736\n" \
"v_cvt_pk_bf16_f32 v11, v14, v15\n" \
"v_mfma_f32_16x16x32_bf16 a[16:19], a[224:227], v[64:67], a[16:19]\n" \
"v_exp_f32 v1, v1\n" \
"v_mfma_f32_16x16x32_bf16 a[16:19], a[228:231], v[96:99], a[16:19]\n" \
"v_exp_f32 v2, v2\n" \
"v_cvt_pk_bf16_f32 v0, v0, v1\n" \
"v_mfma_f32_16x16x32_bf16 a[48:51], a[224:227], v[72:75], a[48:51]\n" \
"v_exp_f32 v3, v3\n" \
"v_mfma_f32_16x16x32_bf16 a[48:51], a[228:231], v[104:107], a[48:51]\n" \
"ds_read_b128 a[220:223], v246 offset:22784\n" \
"global_load_lds_dwordx4 v250, s[36:37] offset:3072 sc1\n" \
"v_mfma_f32_16x16x32_bf16 v[144:147], a[224:227], v[80:83], v[144:147]\n" \
"s_add_u32 s36, s36, 17408\n" \
"v_exp_f32 v4, v4\n" \
"v_mfma_f32_16x16x32_bf16 v[144:147], a[228:231], v[112:115], v[144:147]\n" \
"s_xor_b32 s73, s73, 0x8000\n" \
"v_cvt_pk_bf16_f32 v1, v2, v3\n" \
"v_exp_f32 v5, v5\n" \
"v_mfma_f32_16x16x32_bf16 v[176:179], a[224:227], v[88:91], v[176:179]\n" \
"s_xor_b64 s[52:53], s[44:45], s[42:43]\n" \
"v_exp_f32 v6, v6\n" \
"v_mfma_f32_16x16x32_bf16 v[176:179], a[228:231], v[120:123], v[176:179]\n" \
"s_xor_b64 s[54:55], s[46:47], s[42:43]\n" \
"ds_read_b128 a[224:227], v246 offset:24576\n" \
"v_cvt_pk_bf16_f32 v2, v4, v5\n" \
"v_mfma_f32_16x16x32_bf16 v[180:183], a[232:235], v[88:91], v[180:183]\n" \
"s_or_b64 s[52:53], s[52:53], s[54:55]\n" \
"v_exp_f32 v7, v7\n" \
"v_mfma_f32_16x16x32_bf16 v[180:183], a[236:239], v[120:123], v[180:183]\n" \
"s_xor_b64 s[54:55], s[48:49], s[42:43]\n" \
"v_exp_f32 v16, v16\n" \
"v_cvt_pk_bf16_f32 v3, v6, v7\n" \
"v_mfma_f32_16x16x32_bf16 v[148:151], a[232:235], v[80:83], v[148:151]\n" \
"s_or_b64 s[52:53], s[52:53], s[54:55]\n" \
"v_exp_f32 v17, v17\n" \
"v_mfma_f32_16x16x32_bf16 v[148:151], a[236:239], v[112:115], v[148:151]\n" \
"ds_read_b128 a[228:231], v246 offset:26624\n" \
"s_xor_b64 s[54:55], s[50:51], s[42:43]\n" \
"v_mfma_f32_16x16x32_bf16 a[52:55], a[232:235], v[72:75], a[52:55]\n" \
"s_or_b64 s[52:53], s[52:53], s[54:55]\n" \
"v_exp_f32 v18, v18\n" \
"v_mfma_f32_16x16x32_bf16 a[52:55], a[236:239], v[104:107], a[52:55]\n" \
"v_cvt_pk_bf16_f32 v16, v16, v17\n" \
"v_mfma_f32_16x16x32_bf16 v[240:243], a[124:127], v[8:11], v[240:243]\n" \
"v_exp_f32 v19, v19\n" \
"v_mfma_f32_16x16x32_bf16 v[240:243], a[120:123], v[0:3], v[240:243]\n" \
"s_and_b64 s[52:53], s[52:53], s[8:9]\n" \
"s_cbranch_scc1 Lrs1_%=\n" \
"Lrr1_%=:\n" \
"v_mfma_f32_16x16x32_bf16 a[20:23], a[232:235], v[64:67], a[20:23]\n" \
"s_mov_b64 s[8:9], s[10:11]\n" \
"v_exp_f32 v20, v20\n" \
"v_mfma_f32_16x16x32_bf16 a[20:23], a[236:239], v[96:99], a[20:23]\n" \
"ds_read_b128 a[232:235], v246 offset:28672\n" \
"v_cvt_pk_bf16_f32 v17, v18, v19\n" \
"v_mfma_f32_16x16x32_bf16 a[24:27], a[240:243], v[64:67], a[24:27]\n" \
"v_exp_f32 v21, v21\n" \
"v_mfma_f32_16x16x32_bf16 a[24:27], a[244:247], v[96:99], a[24:27]\n" \
"v_exp_f32 v22, v22\n" \
"v_cvt_pk_bf16_f32 v18, v20, v21\n" \
"v_mfma_f32_16x16x32_bf16 a[56:59], a[240:243], v[72:75], a[56:59]\n" \
"v_exp_f32 v23, v23\n" \
"v_mfma_f32_16x16x32_bf16 a[56:59], a[244:247], v[104:107], a[56:59]\n" \
"ds_read_b128 a[236:239], v246 offset:30720\n" \
"v_exp_f32 v24, v24\n" \
"v_mfma_f32_16x16x32_bf16 v[152:155], a[240:243], v[80:83], v[152:155]\n" \
"v_cvt_pk_bf16_f32 v19, v22, v23\n" \
"v_mfma_f32_16x16x32_bf16 v[152:155], a[244:247], v[112:115], v[152:155]\n" \
"v_exp_f32 v25, v25\n" \
"v_mfma_f32_16x16x32_bf16 v[184:187], a[240:243], v[88:91], v[184:187]\n" \
"v_exp_f32 v26, v26\n" \
"v_mfma_f32_16x16x32_bf16 v[184:187], a[244:247], v[120:123], v[184:187]\n" \
"ds_read_b128 a[240:243], v246 offset:24832\n" \
"v_cvt_pk_bf16_f32 v24, v24, v25\n" \
"v_mfma_f32_16x16x32_bf16 v[242:245], a[120:123], v[16:19], v[242:245]\n" \
"v_exp_f32 v27, v27\n" \
"v_mfma_f32_16x16x32_bf16 v[188:191], a[248:251], v[88:91], v[188:191]\n" \
"v_exp_f32 v28, v28\n" \
"v_mfma_f32_16x16x32_bf16 v[188:191], a[252:255], v[120:123], v[188:191]\n" \
"v_cvt_pk_bf16_f32 v25, v26, v27\n" \
"v_exp_f32 v29, v29\n" \
"v_mfma_f32_16x16x32_bf16 v[156:159], a[248:251], v[80:83], v[156:159]\n" \
"v_exp_f32 v30, v30\n" \
"v_mfma_f32_16x16x32_bf16 v[156:159], a[252:255], v[112:115], v[156:159]\n" \
"ds_read_b128 a[244:247], v246 offset:26880\n" \
"v_cvt_pk_bf16_f32 v26, v28, v29\n" \
"v_mfma_f32_16x16x32_bf16 a[60:63], a[248:251], v[72:75], a[60:63]\n" \
"v_exp_f32 v31, v31\n" \
"v_mfma_f32_16x16x32_bf16 a[60:63], a[252:255], v[104:107], a[60:63]\n" \
"v_cvt_pk_bf16_f32 v27, v30, v31\n" \
"v_mfma_f32_16x16x32_bf16 a[28:31], a[248:251], v[64:67], a[28:31]\n" \
"s_waitcnt lgkmcnt(6)\n" \
"v_mfma_f32_16x16x32_bf16 v[242:245], a[124:127], v[24:27], v[242:245]\n" \
"v_mfma_f32_16x16x32_bf16 a[28:31], a[252:255], v[96:99], a[28:31]\n" \
"ds_read_b128 a[248:251], v246 offset:28928\n" \
"LA1l_%=:\n" \
"Lpg6288_%=:\n" \
"s_cbranch_execz Lpg13279_%=\n" \
"s_getpc_b64 s[40:41]\n" \
"Lpd_%=:\n" \
"s_lshl_b32 s80, s62, 2\n" \
"s_add_u32 s80, s80, s61\n" \
"s_and_b32 s80, s80, 15\n" \
"s_lshl_b32 s80, s80, 12\n" \
"s_mov_b32 s81, Lcode_end_%=-Lpd_%=-4\n" \
"v_lshl_add_u32 v252, v248, 2, s80\n" \
"v_min_u32 v252, s81, v252\n" \
"s_waitcnt lgkmcnt(0)\n" \
"v_mfma_f32_16x16x32_bf16 v[72:75], a[192:195], a[144:147], v[228:231]\n" \
"v_exp_f32 v32, v32\n" \
"v_mfma_f32_16x16x32_bf16 v[72:75], a[196:199], a[148:151], v[72:75]\n" \
"v_cvt_pk_bf16_f32 v27, v30, v31\n" \
"v_mfma_f32_16x16x32_bf16 v[72:75], a[200:203], a[152:155], v[72:75]\n" \
"s_mov_b32 m0, s74\n" \
"v_exp_f32 v33, v33\n" \
"v_mfma_f32_16x16x32_bf16 v[72:75], a[204:207], a[156:159], v[72:75]\n" \
"ds_read_b128 a[252:255], v246 offset:30976\n" \
"global_load_lds_dwordx4 v250, s[38:39] offset:0 sc1\n" \
"v_mfma_f32_16x16x32_bf16 v[80:83], a[200:203], a[168:171], v[232:235]\n" \
"s_lshl_b32 s70, s95, 1\n" \
"v_exp_f32 v34, v34\n" \
"v_mfma_f32_16x16x32_bf16 v[80:83], a[204:207], a[172:175], v[80:83]\n" \
"s_xor_b32 s70, s70, 139264\n" \
"v_cvt_pk_bf16_f32 v32, v32, v33\n" \
"v_add_u32 v64, s70, v248\n" \
"v_mfma_f32_16x16x32_bf16 v[80:83], a[192:195], a[160:163], v[80:83]\n" \
"ds_read_b128 v[192:195], v64 offset:0\n" \
"v_exp_f32 v35, v35\n" \
"v_mfma_f32_16x16x32_bf16 v[80:83], a[196:199], a[164:167], v[80:83]\n" \
"ds_read_b128 v[196:199], v64 offset:1024\n" \
"v_exp_f32 v36, v36\n" \
"v_mfma_f32_16x16x32_bf16 v[88:91], a[192:195], a[176:179], v[236:239]\n" \
"ds_read_b128 v[200:203], v64 offset:2048\n" \
"v_cvt_pk_bf16_f32 v33, v34, v35\n" \
"v_mfma_f32_16x16x32_bf16 v[88:91], a[196:199], a[180:183], v[88:91]\n" \
"ds_read_b128 v[204:207], v64 offset:3072\n" \
"v_exp_f32 v37, v37\n" \
"v_exp_f32 v38, v38\n" \
"v_mfma_f32_16x16x32_bf16 v[88:91], a[200:203], a[184:187], v[88:91]\n" \
"ds_read_b128 v[208:211], v64 offset:4096\n" \
"v_cvt_pk_bf16_f32 v34, v36, v37\n" \
"v_mfma_f32_16x16x32_bf16 v[88:91], a[204:207], a[188:191], v[88:91]\n" \
"ds_read_b128 a[192:195], v247 offset:0\n" \
"v_exp_f32 v39, v39\n" \
"v_mfma_f32_16x16x32_bf16 v[92:95], a[216:219], a[184:187], v[236:239]\n" \
"ds_read_b128 v[212:215], v64 offset:5120\n" \
"global_load_lds_dwordx4 v250, s[38:39] offset:1024 sc1\n" \
"v_mfma_f32_16x16x32_bf16 v[92:95], a[220:223], a[188:191], v[92:95]\n" \
"ds_read_b128 v[216:219], v64 offset:6144\n" \
"v_exp_f32 v40, v40\n" \
"v_mfma_f32_16x16x32_bf16 v[92:95], a[208:211], a[176:179], v[92:95]\n" \
"ds_read_b128 a[196:199], v247 offset:8192\n" \
"v_cvt_pk_bf16_f32 v35, v38, v39\n" \
"v_mfma_f32_16x16x32_bf16 v[92:95], a[212:215], a[180:183], v[92:95]\n" \
"ds_read_b128 v[220:223], v64 offset:7168\n" \
"v_exp_f32 v41, v41\n" \
"v_mfma_f32_16x16x32_bf16 v[84:87], a[208:211], a[160:163], v[232:235]\n" \
"ds_write_b128 v64, v[232:235]\n" \
"v_exp_f32 v42, v42\n" \
"v_mfma_f32_16x16x32_bf16 v[84:87], a[212:215], a[164:167], v[84:87]\n" \
"ds_read_b128 a[200:203], v247 offset:1024\n" \
"v_cvt_pk_bf16_f32 v40, v40, v41\n" \
"v_mfma_f32_16x16x32_bf16 v[84:87], a[216:219], a[168:171], v[84:87]\n" \
"v_exp_f32 v43, v43\n" \
"v_mfma_f32_16x16x32_bf16 v[84:87], a[220:223], a[172:175], v[84:87]\n" \
"global_load_lds_dwordx4 v250, s[38:39] offset:2048 sc1\n" \
"ds_write_b128 v64, v[236:239] offset:1024\n" \
"v_exp_f32 v44, v44\n" \
"v_mfma_f32_16x16x32_bf16 v[76:79], a[216:219], a[152:155], v[228:231]\n" \
"ds_read_b128 a[204:207], v247 offset:9216\n" \
"v_xor_b32 v65, 0x2000, v64\n" \
"v_mfma_f32_16x16x32_bf16 v[76:79], a[220:223], a[156:159], v[76:79]\n" \
"v_xor_b32 v246, 0x8000, v246\n" \
"v_add_u32 v246, s82, v246\n" \
"v_mfma_f32_16x16x32_bf16 v[76:79], a[208:211], a[144:147], v[76:79]\n" \
"s_waitcnt lgkmcnt(4)\n" \
"v_mov_b32 v66, s60\n" \
"v_lshlrev_b32 v67, 28, s61\n" \
"v_mfma_f32_16x16x32_bf16 v[76:79], a[212:215], a[148:151], v[76:79]\n" \
"ds_read_b128 a[208:211], v247 offset:2048\n" \
"v_cvt_pk_bf16_f32 v41, v42, v43\n" \
"v_mfma_f32_16x16x32_bf16 v[104:107], a[224:227], a[144:147], v[228:231]\n" \
"v_exp_f32 v45, v45\n" \
"v_mfma_f32_16x16x32_bf16 v[104:107], a[228:231], a[148:151], v[104:107]\n" \
"v_exp_f32 v46, v46\n" \
"v_mfma_f32_16x16x32_bf16 v[104:107], a[232:235], a[152:155], v[104:107]\n" \
"ds_read_b128 a[212:215], v247 offset:10240\n" \
"v_cvt_pk_bf16_f32 v42, v44, v45\n" \
"v_mfma_f32_16x16x32_bf16 v[104:107], a[236:239], a[156:159], v[104:107]\n" \
"v_exp_f32 v47, v47\n" \
"v_mfma_f32_16x16x32_bf16 v[112:115], a[232:235], a[168:171], v[232:235]\n" \
"v_exp_f32 v48, v48\n" \
"v_cvt_pk_bf16_f32 v43, v46, v47\n" \
"v_mfma_f32_16x16x32_bf16 v[112:115], a[236:239], a[172:175], v[112:115]\n" \
"ds_read_b128 a[216:219], v247 offset:3072\n" \
"global_load_lds_dwordx4 v250, s[38:39] offset:3072 sc1\n" \
"v_mfma_f32_16x16x32_bf16 v[112:115], a[224:227], a[160:163], v[112:115]\n" \
"v_exp_f32 v49, v49\n" \
"v_mfma_f32_16x16x32_bf16 v[112:115], a[228:231], a[164:167], v[112:115]\n" \
"v_exp_f32 v50, v50\n" \
"v_cvt_pk_bf16_f32 v48, v48, v49\n" \
"v_mfma_f32_16x16x32_bf16 v[240:243], a[120:123], v[32:35], v[240:243]\n" \
"v_exp_f32 v51, v51\n" \
"v_mfma_f32_16x16x32_bf16 v[240:243], a[124:127], v[40:43], v[240:243]\n" \
"s_add_u32 s38, s38, 17408\n" \
"v_exp_f32 v52, v52\n" \
"v_mfma_f32_16x16x32_bf16 v[120:123], a[224:227], a[176:179], v[236:239]\n" \
"ds_read_b128 a[220:223], v247 offset:11264\n" \
"v_cvt_pk_bf16_f32 v49, v50, v51\n" \
"v_mfma_f32_16x16x32_bf16 v[120:123], a[228:231], a[180:183], v[120:123]\n" \
"v_exp_f32 v53, v53\n" \
"v_mfma_f32_16x16x32_bf16 v[120:123], a[232:235], a[184:187], v[120:123]\n" \
"v_exp_f32 v54, v54\n" \
"v_mfma_f32_16x16x32_bf16 v[120:123], a[236:239], a[188:191], v[120:123]\n" \
"ds_read_b128 a[224:227], v247 offset:4096\n" \
"v_cvt_pk_bf16_f32 v50, v52, v53\n" \
"v_exp_f32 v55, v55\n" \
"v_mfma_f32_16x16x32_bf16 v[124:127], a[248:251], a[184:187], v[236:239]\n" \
"global_load_dword v253, v252, s[40:41]\n" \
"v_mfma_f32_16x16x32_bf16 v[124:127], a[252:255], a[188:191], v[124:127]\n" \
"v_exp_f32 v56, v56\n" \
"v_mfma_f32_16x16x32_bf16 v[124:127], a[240:243], a[176:179], v[124:127]\n" \
"ds_read_b128 a[228:231], v247 offset:12288\n" \
"v_cvt_pk_bf16_f32 v51, v54, v55\n" \
"v_mfma_f32_16x16x32_bf16 v[124:127], a[244:247], a[180:183], v[124:127]\n" \
"v_exp_f32 v57, v57\n" \
"v_exp_f32 v58, v58\n" \
"v_cvt_pk_bf16_f32 v56, v56, v57\n" \
"v_mfma_f32_16x16x32_bf16 v[242:245], a[120:123], v[48:51], v[242:245]\n" \
"v_exp_f32 v59, v59\n" \
"v_mfma_f32_16x16x32_bf16 v[116:119], a[240:243], a[160:163], v[232:235]\n" \
"v_exp_f32 v60, v60\n" \
"v_mfma_f32_16x16x32_bf16 v[116:119], a[244:247], a[164:167], v[116:119]\n" \
"ds_read_b128 a[232:235], v247 offset:5120\n" \
"v_cvt_pk_bf16_f32 v57, v58, v59\n" \
"v_mfma_f32_16x16x32_bf16 v[116:119], a[248:251], a[168:171], v[116:119]\n" \
"v_exp_f32 v61, v61\n" \
"v_mfma_f32_16x16x32_bf16 v[116:119], a[252:255], a[172:175], v[116:119]\n" \
"v_exp_f32 v62, v62\n" \
"v_cvt_pk_bf16_f32 v58, v60, v61\n" \
"v_mfma_f32_16x16x32_bf16 v[108:111], a[248:251], a[152:155], v[228:231]\n" \
"ds_read_b128 a[236:239], v247 offset:13312\n" \
"v_exp_f32 v63, v63\n" \
"v_mfma_f32_16x16x32_bf16 v[108:111], a[252:255], a[156:159], v[108:111]\n" \
"v_cvt_pk_bf16_f32 v59, v62, v63\n" \
"v_mfma_f32_16x16x32_bf16 v[108:111], a[240:243], a[144:147], v[108:111]\n" \
"s_waitcnt vmcnt(5) lgkmcnt(4)\n" \
"v_mfma_f32_16x16x32_bf16 v[242:245], a[124:127], v[56:59], v[242:245]\n" \
"v_mfma_f32_16x16x32_bf16 v[108:111], a[244:247], a[148:151], v[108:111]\n" \
"s_barrier\n" \
"ds_read_b128 a[240:243], v247 offset:6144\n" \
"v_mfma_f32_16x16x32_bf16 a[0:3], a[192:195], v[0:3], a[0:3]\n" \
"buffer_store_dword v66, v67, s[56:59], s84 offen sc1\n" \
"v_mfma_f32_16x16x32_bf16 a[0:3], a[196:199], v[32:35], a[0:3]\n" \
"s_add_u32 s73, s73, s95\n" \
"v_cvt_pk_bf16_f32 v59, v62, v63\n" \
"v_exp_f32 v80, v80\n" \
"v_mfma_f32_16x16x32_bf16 a[32:35], a[192:195], v[8:11], a[32:35]\n" \
"s_add_u32 s36, s36, s83\n" \
"ds_read_b128 a[244:247], v247 offset:14336\n" \
"v_exp_f32 v81, v81\n" \
"v_mfma_f32_16x16x32_bf16 a[32:35], a[196:199], v[40:43], a[32:35]\n" \
"s_add_u32 s73, s73, 4096\n" \
"ds_read_b128 a[248:251], v247 offset:7168\n" \
"v_exp_f32 v82, v82\n" \
"v_mfma_f32_16x16x32_bf16 v[128:131], a[192:195], v[16:19], v[128:131]\n" \
"ds_read_b128 a[252:255], v247 offset:15360\n" \
"v_cvt_pk_bf16_f32 v80, v80, v81\n" \
"v_mfma_f32_16x16x32_bf16 v[128:131], a[196:199], v[48:51], v[128:131]\n" \
"ds_read_b128 v[224:227], v65\n" \
"v_exp_f32 v83, v83\n" \
"v_mfma_f32_16x16x32_bf16 v[160:163], a[192:195], v[24:27], v[160:163]\n" \
"s_mov_b32 m0, s73\n" \
"ds_read_b128 v[228:231], v65 offset:1024\n" \
"v_xor_b32 v252, 0x8000, v246\n" \
"v_mfma_f32_16x16x32_bf16 v[160:163], a[196:199], v[56:59], v[160:163]\n" \
"ds_read_b128 a[192:195], v246 offset:0\n" \
"global_load_lds_dwordx4 v250, s[36:37] offset:-4096 sc1\n" \
"v_mfma_f32_16x16x32_bf16 v[164:167], a[200:203], v[24:27], v[164:167]\n" \
"v_exp_f32 v84, v84\n" \
"v_mfma_f32_16x16x32_bf16 v[164:167], a[204:207], v[56:59], v[164:167]\n" \
"v_cvt_pk_bf16_f32 v81, v82, v83\n" \
"v_exp_f32 v85, v85\n" \
"v_mfma_f32_16x16x32_bf16 v[132:135], a[200:203], v[16:19], v[132:135]\n" \
"v_exp_f32 v86, v86\n" \
"v_mfma_f32_16x16x32_bf16 v[132:135], a[204:207], v[48:51], v[132:135]\n" \
"ds_read_b128 a[196:199], v246 offset:2048\n" \
"global_load_lds_dwordx4 v250, s[36:37] offset:-3072 sc1\n" \
"v_mfma_f32_16x16x32_bf16 a[36:39], a[200:203], v[8:11], a[36:39]\n" \
"v_cvt_pk_bf16_f32 v82, v84, v85\n" \
"v_mfma_f32_16x16x32_bf16 a[36:39], a[204:207], v[40:43], a[36:39]\n" \
"v_exp_f32 v87, v87\n" \
"v_mfma_f32_16x16x32_bf16 a[4:7], a[200:203], v[0:3], a[4:7]\n" \
"v_cmp_gt_i32 vcc, 0, v251\n" \
"v_cmp_gt_i32 s[64:65], 1, v251\n" \
"v_mfma_f32_16x16x32_bf16 a[4:7], a[204:207], v[32:35], a[4:7]\n" \
"ds_read_b128 a[200:203], v246 offset:4096\n" \
"global_load_lds_dwordx4 v250, s[36:37] offset:-2048 sc1\n" \
"v_mfma_f32_16x16x32_bf16 a[8:11], a[208:211], v[0:3], a[8:11]\n" \
"v_cmp_gt_i32 s[66:67], 2, v251\n" \
"v_cndmask_b32 v72, v72, v254, vcc\n" \
"v_mfma_f32_16x16x32_bf16 a[8:11], a[212:215], v[32:35], a[8:11]\n" \
"v_cmp_gt_i32 vcc, 3, v251\n" \
"v_cndmask_b32 v73, v73, v254, s[64:65]\n" \
"v_mfma_f32_16x16x32_bf16 a[40:43], a[208:211], v[8:11], a[40:43]\n" \
"v_cmp_gt_i32 s[64:65], 16, v251\n" \
"v_cndmask_b32 v74, v74, v254, s[66:67]\n" \
"v_mfma_f32_16x16x32_bf16 a[40:43], a[212:215], v[40:43], a[40:43]\n" \
"ds_read_b128 a[204:207], v246 offset:6144\n" \
"global_load_lds_dwordx4 v250, s[36:37] offset:-1024 sc1\n" \
"v_mfma_f32_16x16x32_bf16 v[136:139], a[208:211], v[16:19], v[136:139]\n" \
"v_cmp_gt_i32 s[66:67], 17, v251\n" \
"v_cndmask_b32 v75, v75, v254, vcc\n" \
"v_mfma_f32_16x16x32_bf16 v[136:139], a[212:215], v[48:51], v[136:139]\n" \
"v_cmp_gt_i32 vcc, 18, v251\n" \
"v_cndmask_b32 v76, v76, v254, s[64:65]\n" \
"v_mfma_f32_16x16x32_bf16 v[168:171], a[208:211], v[24:27], v[168:171]\n" \
"v_cmp_gt_i32 s[64:65], 19, v251\n" \
"v_cndmask_b32 v77, v77, v254, s[66:67]\n" \
"v_mfma_f32_16x16x32_bf16 v[168:171], a[212:215], v[56:59], v[168:171]\n" \
"ds_read_b128 a[208:211], v246 offset:256\n" \
"global_load_lds_dwordx4 v250, s[36:37] offset:0 sc1\n" \
"v_mfma_f32_16x16x32_bf16 v[172:175], a[216:219], v[24:27], v[172:175]\n" \
"v_cmp_gt_i32 s[66:67], 32, v251\n" \
"v_cndmask_b32 v78, v78, v254, vcc\n" \
"v_mfma_f32_16x16x32_bf16 v[172:175], a[220:223], v[56:59], v[172:175]\n" \
"v_cmp_gt_i32 vcc, 33, v251\n" \
"v_cndmask_b32 v79, v79, v254, s[64:65]\n" \
"v_mfma_f32_16x16x32_bf16 v[140:143], a[216:219], v[16:19], v[140:143]\n" \
"v_cmp_gt_i32 s[64:65], 34, v251\n" \
"v_cndmask_b32 v104, v104, v254, s[66:67]\n" \
"v_mfma_f32_16x16x32_bf16 v[140:143], a[220:223], v[48:51], v[140:143]\n" \
"ds_read_b128 a[212:215], v246 offset:2304\n" \
"global_load_lds_dwordx4 v250, s[36:37] offset:1024 sc1\n" \
"v_mfma_f32_16x16x32_bf16 a[44:47], a[216:219], v[8:11], a[44:47]\n" \
"v_cmp_gt_i32 s[66:67], 35, v251\n" \
"v_cndmask_b32 v105, v105, v254, vcc\n" \
"v_mfma_f32_16x16x32_bf16 a[44:47], a[220:223], v[40:43], a[44:47]\n" \
"v_cmp_gt_i32 vcc, 48, v251\n" \
"v_cndmask_b32 v106, v106, v254, s[64:65]\n" \
"v_mfma_f32_16x16x32_bf16 a[12:15], a[216:219], v[0:3], a[12:15]\n" \
"s_waitcnt lgkmcnt(6)\n" \
"v_cmp_gt_i32 s[64:65], 49, v251\n" \
"v_cndmask_b32 v107, v107, v254, s[66:67]\n" \
"v_mfma_f32_16x16x32_bf16 a[12:15], a[220:223], v[32:35], a[12:15]\n" \
"ds_read_b128 a[216:219], v246 offset:4352\n" \
"global_load_lds_dwordx4 v250, s[36:37] offset:2048 sc1\n" \
"v_mfma_f32_16x16x32_bf16 a[16:19], a[224:227], v[0:3], a[16:19]\n" \
"v_cmp_gt_i32 s[66:67], 50, v251\n" \
"v_cndmask_b32 v108, v108, v254, vcc\n" \
"v_mfma_f32_16x16x32_bf16 a[16:19], a[228:231], v[32:35], a[16:19]\n" \
"v_cmp_gt_i32 vcc, 51, v251\n" \
"v_cndmask_b32 v109, v109, v254, s[64:65]\n" \
"v_mfma_f32_16x16x32_bf16 a[48:51], a[224:227], v[8:11], a[48:51]\n" \
"v_cndmask_b32 v110, v110, v254, s[66:67]\n" \
"v_cndmask_b32 v111, v111, v254, vcc\n" \
"v_mfma_f32_16x16x32_bf16 a[48:51], a[228:231], v[40:43], a[48:51]\n" \
"ds_read_b128 a[220:223], v246 offset:6400\n" \
"global_load_lds_dwordx4 v250, s[36:37] offset:3072 sc1\n" \
"v_mfma_f32_16x16x32_bf16 v[144:147], a[224:227], v[16:19], v[144:147]\n" \
"s_add_u32 s36, s36, 34816\n" \
"v_exp_f32 v72, v72\n" \
"v_mfma_f32_16x16x32_bf16 v[144:147], a[228:231], v[48:51], v[144:147]\n" \
"v_cvt_pk_bf16_f32 v83, v86, v87\n" \
"v_mfma_f32_16x16x32_bf16 v[176:179], a[224:227], v[24:27], v[176:179]\n" \
"s_add_u32 m0, s74, 0x4000\n" \
"v_exp_f32 v73, v73\n" \
"v_mfma_f32_16x16x32_bf16 v[176:179], a[228:231], v[56:59], v[176:179]\n" \
"ds_read_b128 a[224:227], v246 offset:8192\n" \
"global_load_lds_dwordx4 v250, s[38:39] offset:0 sc1\n" \
"v_mfma_f32_16x16x32_bf16 v[180:183], a[232:235], v[24:27], v[180:183]\n" \
"v_exp_f32 v74, v74\n" \
"v_mfma_f32_16x16x32_bf16 v[180:183], a[236:239], v[56:59], v[180:183]\n" \
"v_cvt_pk_bf16_f32 v72, v72, v73\n" \
"v_mfma_f32_16x16x32_bf16 v[242:245], a[120:123], v[80:83], v[242:245]\n" \
"v_exp_f32 v75, v75\n" \
"v_mfma_f32_16x16x32_bf16 v[148:151], a[232:235], v[16:19], v[148:151]\n" \
"v_exp_f32 v76, v76\n" \
"v_mfma_f32_16x16x32_bf16 v[148:151], a[236:239], v[48:51], v[148:151]\n" \
"ds_read_b128 a[228:231], v246 offset:10240\n" \
"global_load_lds_dwordx4 v250, s[38:39] offset:1024 sc1\n" \
"v_mfma_f32_16x16x32_bf16 a[52:55], a[232:235], v[8:11], a[52:55]\n" \
"v_cvt_pk_bf16_f32 v73, v74, v75\n" \
"v_mfma_f32_16x16x32_bf16 a[52:55], a[236:239], v[40:43], a[52:55]\n" \
"v_exp_f32 v77, v77\n" \
"v_mfma_f32_16x16x32_bf16 a[20:23], a[232:235], v[0:3], a[20:23]\n" \
"v_exp_f32 v78, v78\n" \
"v_mfma_f32_16x16x32_bf16 a[20:23], a[236:239], v[32:35], a[20:23]\n" \
"ds_read_b128 a[232:235], v246 offset:12288\n" \
"v_cvt_pk_bf16_f32 v74, v76, v77\n" \
"v_mfma_f32_16x16x32_bf16 a[24:27], a[240:243], v[0:3], a[24:27]\n" \
"global_load_lds_dwordx4 v250, s[38:39] offset:2048 sc1\n" \
"global_load_lds_dwordx4 v250, s[38:39] offset:3072 sc1\n" \
"v_mfma_f32_16x16x32_bf16 a[24:27], a[244:247], v[32:35], a[24:27]\n" \
"v_exp_f32 v79, v79\n" \
"v_mfma_f32_16x16x32_bf16 a[56:59], a[240:243], v[8:11], a[56:59]\n" \
"s_add_u32 s38, s38, 17408\n" \
"v_exp_f32 v88, v88\n" \
"v_mfma_f32_16x16x32_bf16 a[56:59], a[244:247], v[40:43], a[56:59]\n" \
"s_add_u32 s38, s38, s83\n" \
"ds_read_b128 a[236:239], v246 offset:14336\n" \
"v_cvt_pk_bf16_f32 v75, v78, v79\n" \
"v_mfma_f32_16x16x32_bf16 v[152:155], a[240:243], v[16:19], v[152:155]\n" \
"s_add_u32 s74, s74, s95\n" \
"v_exp_f32 v89, v89\n" \
"v_mfma_f32_16x16x32_bf16 v[152:155], a[244:247], v[48:51], v[152:155]\n" \
"v_exp_f32 v90, v90\n" \
"v_cvt_pk_bf16_f32 v88, v88, v89\n" \
"v_mfma_f32_16x16x32_bf16 v[240:243], a[124:127], v[72:75], v[240:243]\n" \
"s_add_u32 s74, s74, 4096\n" \
"v_exp_f32 v91, v91\n" \
"v_mfma_f32_16x16x32_bf16 v[184:187], a[240:243], v[24:27], v[184:187]\n" \
"s_cmp_eq_u32 s63, 7\n" \
"v_exp_f32 v92, v92\n" \
"v_mfma_f32_16x16x32_bf16 v[184:187], a[244:247], v[56:59], v[184:187]\n" \
"ds_read_b128 a[240:243], v246 offset:8448\n" \
"v_cvt_pk_bf16_f32 v89, v90, v91\n" \
"v_mfma_f32_16x16x32_bf16 v[188:191], a[248:251], v[24:27], v[188:191]\n" \
"v_exp_f32 v93, v93\n" \
"v_mfma_f32_16x16x32_bf16 v[188:191], a[252:255], v[56:59], v[188:191]\n" \
"v_exp_f32 v94, v94\n" \
"v_mfma_f32_16x16x32_bf16 v[156:159], a[248:251], v[16:19], v[156:159]\n" \
"v_cvt_pk_bf16_f32 v90, v92, v93\n" \
"v_mfma_f32_16x16x32_bf16 v[156:159], a[252:255], v[48:51], v[156:159]\n" \
"ds_read_b128 a[244:247], v246 offset:10496\n" \
"v_mfma_f32_16x16x32_bf16 a[60:63], a[248:251], v[8:11], a[60:63]\n" \
"v_exp_f32 v95, v95\n" \
"v_mfma_f32_16x16x32_bf16 a[60:63], a[252:255], v[40:43], a[60:63]\n" \
"v_cvt_pk_bf16_f32 v91, v94, v95\n" \
"v_mfma_f32_16x16x32_bf16 a[28:31], a[248:251], v[0:3], a[28:31]\n" \
"s_waitcnt lgkmcnt(6)\n" \
"v_mfma_f32_16x16x32_bf16 v[242:245], a[124:127], v[88:91], v[242:245]\n" \
"v_mfma_f32_16x16x32_bf16 a[28:31], a[252:255], v[32:35], a[28:31]\n" \
"ds_read_b128 a[248:251], v246 offset:12544\n" \
"s_cbranch_scc1 LF7_%=\n" \
"v_mfma_f32_16x16x32_bf16 v[0:3], a[192:195], v[192:195], v[224:227]\n" \
"s_load_dwordx8 s[44:51], s[56:57], s79 glc\n" \
"v_exp_f32 v104, v104\n" \
"v_mfma_f32_16x16x32_bf16 v[0:3], a[196:199], v[196:199], v[0:3]\n" \
"v_cvt_pk_bf16_f32 v91, v94, v95\n" \
"v_mfma_f32_16x16x32_bf16 v[0:3], a[200:203], v[200:203], v[0:3]\n" \
"v_exp_f32 v105, v105\n" \
"v_mfma_f32_16x16x32_bf16 v[0:3], a[204:207], v[204:207], v[0:3]\n" \
"ds_read_b128 a[252:255], v246 offset:14592\n" \
"v_mov_b32 v78, v242\n" \
"v_mfma_f32_16x16x32_bf16 v[8:11], a[200:203], v[216:219], v[228:231]\n" \
"v_exp_f32 v106, v106\n" \
"v_mfma_f32_16x16x32_bf16 v[8:11], a[204:207], v[220:223], v[8:11]\n" \
"v_cvt_pk_bf16_f32 v104, v104, v105\n" \
"v_mov_b32 v76, v78\n" \
"v_mfma_f32_16x16x32_bf16 v[8:11], a[192:195], v[208:211], v[8:11]\n" \
"v_exp_f32 v107, v107\n" \
"v_mfma_f32_16x16x32_bf16 v[8:11], a[196:199], v[212:215], v[8:11]\n" \
"v_exp_f32 v108, v108\n" \
"v_mfma_f32_16x16x32_bf16 v[16:19], a[192:195], a[160:163], v[232:235]\n" \
"v_cvt_pk_bf16_f32 v105, v106, v107\n" \
"v_mfma_f32_16x16x32_bf16 v[16:19], a[196:199], a[164:167], v[16:19]\n" \
"v_exp_f32 v109, v109\n" \
"v_mfma_f32_16x16x32_bf16 v[16:19], a[200:203], a[168:171], v[16:19]\n" \
"v_exp_f32 v110, v110\n" \
"v_mfma_f32_16x16x32_bf16 v[16:19], a[204:207], a[172:175], v[16:19]\n" \
"v_cvt_pk_bf16_f32 v106, v108, v109\n" \
"v_permlane16_swap_b32 v76, v78\n" \
"v_mfma_f32_16x16x32_bf16 v[24:27], a[200:203], a[184:187], v[236:239]\n" \
"v_exp_f32 v111, v111\n" \
"v_mfma_f32_16x16x32_bf16 v[24:27], a[204:207], a[188:191], v[24:27]\n" \
"v_exp_f32 v112, v112\n" \
"v_mfma_f32_16x16x32_bf16 v[24:27], a[192:195], a[176:179], v[24:27]\n" \
"v_cvt_pk_bf16_f32 v107, v110, v111\n" \
"v_add_f32 v78, v78, v76\n" \
"v_mfma_f32_16x16x32_bf16 v[24:27], a[196:199], a[180:183], v[24:27]\n" \
"ds_read_b128 a[192:195], v247 offset:16384\n" \
"v_mov_b32 v76, v78\n" \
"v_mfma_f32_16x16x32_bf16 v[28:31], a[208:211], a[176:179], v[236:239]\n" \
"v_exp_f32 v113, v113\n" \
"v_mfma_f32_16x16x32_bf16 v[28:31], a[212:215], a[180:183], v[28:31]\n" \
"v_exp_f32 v114, v114\n" \
"v_mfma_f32_16x16x32_bf16 v[28:31], a[216:219], a[184:187], v[28:31]\n" \
"v_cvt_pk_bf16_f32 v112, v112, v113\n" \
"v_mfma_f32_16x16x32_bf16 v[28:31], a[220:223], a[188:191], v[28:31]\n" \
"v_permlane32_swap_b32 v76, v78\n" \
"v_mfma_f32_16x16x32_bf16 v[240:243], a[124:127], v[104:107], v[240:243]\n" \
"ds_read_b128 a[196:199], v247 offset:24576\n" \
"v_exp_f32 v115, v115\n" \
"v_mfma_f32_16x16x32_bf16 v[20:23], a[216:219], a[168:171], v[232:235]\n" \
"v_exp_f32 v116, v116\n" \
"v_mfma_f32_16x16x32_bf16 v[20:23], a[220:223], a[172:175], v[20:23]\n" \
"v_cvt_pk_bf16_f32 v113, v114, v115\n" \
"v_add_f32 v78, v78, v76\n" \
"v_mfma_f32_16x16x32_bf16 v[20:23], a[208:211], a[160:163], v[20:23]\n" \
"v_exp_f32 v117, v117\n" \
"v_mfma_f32_16x16x32_bf16 v[20:23], a[212:215], a[164:167], v[20:23]\n" \
"ds_read_b128 a[200:203], v247 offset:17408\n" \
"v_rcp_f32 v76, v78\n" \
"v_mfma_f32_16x16x32_bf16 v[12:15], a[208:211], v[208:211], v[228:231]\n" \
"v_exp_f32 v118, v118\n" \
"v_mfma_f32_16x16x32_bf16 v[12:15], a[212:215], v[212:215], v[12:15]\n" \
"v_cvt_pk_bf16_f32 v114, v116, v117\n" \
"v_mul_f32 v78, 0x4d800000, v78\n" \
"v_mfma_f32_16x16x32_bf16 v[12:15], a[216:219], v[216:219], v[12:15]\n" \
"v_exp_f32 v119, v119\n" \
"v_mfma_f32_16x16x32_bf16 v[12:15], a[220:223], v[220:223], v[12:15]\n" \
"ds_read_b128 a[204:207], v247 offset:25600\n" \
"v_add_u32 v77, 0x1c90080, v249\n" \
"v_mfma_f32_16x16x32_bf16 v[4:7], a[216:219], v[200:203], v[224:227]\n" \
"v_exp_f32 v120, v120\n" \
"v_mfma_f32_16x16x32_bf16 v[4:7], a[220:223], v[204:207], v[4:7]\n" \
"v_cvt_pk_bf16_f32 v115, v118, v119\n" \
"v_cndmask_b32 v77, v249, v77, s[16:17]\n" \
"v_mfma_f32_16x16x32_bf16 v[4:7], a[208:211], v[192:195], v[4:7]\n" \
"s_waitcnt lgkmcnt(4)\n" \
"v_exp_f32 v121, v121\n" \
"v_mfma_f32_16x16x32_bf16 v[4:7], a[212:215], v[196:199], v[4:7]\n" \
"ds_read_b128 a[208:211], v247 offset:18432\n" \
"v_accvgpr_read_b32 v64, a0\n" \
"v_mfma_f32_16x16x32_bf16 v[32:35], a[224:227], v[192:195], v[224:227]\n" \
"v_exp_f32 v122, v122\n" \
"v_mfma_f32_16x16x32_bf16 v[32:35], a[228:231], v[196:199], v[32:35]\n" \
"v_cvt_pk_bf16_f32 v120, v120, v121\n" \
"v_accvgpr_read_b32 v65, a1\n" \
"v_mfma_f32_16x16x32_bf16 v[32:35], a[232:235], v[200:203], v[32:35]\n" \
"v_accvgpr_read_b32 v66, a2\n" \
"v_accvgpr_read_b32 v67, a3\n" \
"v_mfma_f32_16x16x32_bf16 v[32:35], a[236:239], v[204:207], v[32:35]\n" \
"ds_read_b128 a[212:215], v247 offset:26624\n" \
"v_accvgpr_read_b32 v68, a4\n" \
"v_mfma_f32_16x16x32_bf16 v[242:245], a[120:123], v[112:115], v[242:245]\n" \
"v_exp_f32 v123, v123\n" \
"v_mfma_f32_16x16x32_bf16 v[40:43], a[232:235], v[216:219], v[228:231]\n" \
"v_accvgpr_read_b32 v69, a5\n" \
"v_accvgpr_read_b32 v70, a6\n" \
"v_accvgpr_read_b32 v71, a7\n" \
"v_exp_f32 v124, v124\n" \
"v_pk_mul_f32 v[64:65], v[64:65], v[76:77] op_sel_hi:[1,0]\n" \
"v_pk_mul_f32 v[66:67], v[66:67], v[76:77] op_sel_hi:[1,0]\n" \
"v_pk_mul_f32 v[68:69], v[68:69], v[76:77] op_sel_hi:[1,0]\n" \
"v_pk_mul_f32 v[70:71], v[70:71], v[76:77] op_sel_hi:[1,0]\n" \
"v_mfma_f32_16x16x32_bf16 v[40:43], a[236:239], v[220:223], v[40:43]\n" \
"v_cvt_pk_bf16_f32 v121, v122, v123\n" \
"v_cvt_pk_bf16_f32 v96, v64, v65\n" \
"v_mfma_f32_16x16x32_bf16 v[40:43], a[224:227], v[208:211], v[40:43]\n" \
"v_exp_f32 v125, v125\n" \
"v_mfma_f32_16x16x32_bf16 v[40:43], a[228:231], v[212:215], v[40:43]\n" \
"ds_read_b128 a[216:219], v247 offset:19456\n" \
"v_cvt_pk_bf16_f32 v97, v66, v67\n" \
"v_cvt_pk_bf16_f32 v98, v68, v69\n" \
"v_cvt_pk_bf16_f32 v99, v70, v71\n" \
"v_pk_mul_f32 v[64:65], v[64:65], v[66:67]\n" \
"v_pk_fma_f32 v[64:65], v[68:69], v[70:71], v[64:65]\n" \
"v_mfma_f32_16x16x32_bf16 v[48:51], a[224:227], a[160:163], v[232:235]\n" \
"v_exp_f32 v126, v126\n" \
"v_mfma_f32_16x16x32_bf16 v[48:51], a[228:231], a[164:167], v[48:51]\n" \
"v_cvt_pk_bf16_f32 v122, v124, v125\n" \
"v_fma_f32 v78, v64, v65, v78\n" \
"v_mfma_f32_16x16x32_bf16 v[48:51], a[232:235], a[168:171], v[48:51]\n" \
"v_exp_f32 v127, v127\n" \
"v_mfma_f32_16x16x32_bf16 v[48:51], a[236:239], a[172:175], v[48:51]\n" \
"buffer_store_dwordx4 v[96:99], v77, s[56:59], s77 offen offset:0 sc1\n" \
"ds_read_b128 a[220:223], v247 offset:27648\n" \
"v_cvt_pk_bf16_f32 v123, v126, v127\n" \
"v_accvgpr_read_b32 v64, a8\n" \
"v_accvgpr_read_b32 v65, a9\n" \
"v_mfma_f32_16x16x32_bf16 v[56:59], a[232:235], a[184:187], v[236:239]\n" \
"s_xor_b64 s[52:53], s[44:45], s[42:43]\n" \
"v_accvgpr_read_b32 v66, a10\n" \
"v_accvgpr_read_b32 v67, a11\n" \
"v_mfma_f32_16x16x32_bf16 v[56:59], a[236:239], a[188:191], v[56:59]\n" \
"s_xor_b64 s[54:55], s[46:47], s[42:43]\n" \
"v_accvgpr_read_b32 v68, a12\n" \
"v_accvgpr_read_b32 v69, a13\n" \
"v_accvgpr_read_b32 v70, a14\n" \
"v_accvgpr_read_b32 v71, a15\n" \
"v_pk_mul_f32 v[64:65], v[64:65], v[76:77] op_sel_hi:[1,0]\n" \
"v_pk_mul_f32 v[66:67], v[66:67], v[76:77] op_sel_hi:[1,0]\n" \
"v_pk_mul_f32 v[68:69], v[68:69], v[76:77] op_sel_hi:[1,0]\n" \
"v_pk_mul_f32 v[70:71], v[70:71], v[76:77] op_sel_hi:[1,0]\n" \
"v_cvt_pk_bf16_f32 v100, v64, v65\n" \
"v_cvt_pk_bf16_f32 v101, v66, v67\n" \
"v_cvt_pk_bf16_f32 v102, v68, v69\n" \
"Lpg6770_%=:\n" \
"s_cbranch_execz Lcb0_%=\n" \
"v_cvt_pk_bf16_f32 v103, v70, v71\n" \
"v_pk_mul_f32 v[64:65], v[64:65], v[66:67]\n" \
"v_pk_fma_f32 v[64:65], v[68:69], v[70:71], v[64:65]\n" \
"v_mfma_f32_16x16x32_bf16 v[56:59], a[224:227], a[176:179], v[56:59]\n" \
"v_fma_f32 v78, v64, v65, v78\n" \
"s_or_b64 s[52:53], s[52:53], s[54:55]\n" \
"v_mfma_f32_16x16x32_bf16 v[56:59], a[228:231], a[180:183], v[56:59]\n" \
"ds_read_b128 a[224:227], v247 offset:20480\n" \
"v_mfma_f32_16x16x32_bf16 v[242:245], a[124:127], v[120:123], v[242:245]\n" \
"buffer_store_dwordx4 v[100:103], v77, s[56:59], s77 offen offset:2048 sc1\n" \
"s_xor_b64 s[54:55], s[48:49], s[42:43]\n" \
"s_add_u32 s77, s77, 0x1000\n" \
"v_accvgpr_read_b32 v64, a16\n" \
"v_accvgpr_read_b32 v65, a17\n" \
"v_mfma_f32_16x16x32_bf16 v[60:63], a[240:243], a[176:179], v[236:239]\n" \
"s_or_b64 s[52:53], s[52:53], s[54:55]\n" \
"v_accvgpr_read_b32 v66, a18\n" \
"v_accvgpr_read_b32 v67, a19\n" \
"v_mfma_f32_16x16x32_bf16 v[60:63], a[244:247], a[180:183], v[60:63]\n" \
"s_xor_b64 s[54:55], s[50:51], s[42:43]\n" \
"v_accvgpr_read_b32 v68, a20\n" \
"v_accvgpr_read_b32 v69, a21\n" \
"v_accvgpr_read_b32 v70, a22\n" \
"v_accvgpr_read_b32 v71, a23\n" \
"v_pk_mul_f32 v[64:65], v[64:65], v[76:77] op_sel_hi:[1,0]\n" \
"v_pk_mul_f32 v[66:67], v[66:67], v[76:77] op_sel_hi:[1,0]\n" \
"v_pk_mul_f32 v[68:69], v[68:69], v[76:77] op_sel_hi:[1,0]\n" \
"v_pk_mul_f32 v[70:71], v[70:71], v[76:77] op_sel_hi:[1,0]\n" \
"v_mfma_f32_16x16x32_bf16 v[60:63], a[248:251], a[184:187], v[60:63]\n" \
"v_cvt_pk_bf16_f32 v96, v64, v65\n" \
"v_cvt_pk_bf16_f32 v97, v66, v67\n" \
"v_cvt_pk_bf16_f32 v98, v68, v69\n" \
"v_cvt_pk_bf16_f32 v99, v70, v71\n" \
"s_or_b64 s[52:53], s[52:53], s[54:55]\n" \
"v_pk_mul_f32 v[64:65], v[64:65], v[66:67]\n" \
"v_pk_fma_f32 v[64:65], v[68:69], v[70:71], v[64:65]\n" \
"v_mfma_f32_16x16x32_bf16 v[60:63], a[252:255], a[188:191], v[60:63]\n" \
"ds_read_b128 a[228:231], v247 offset:28672\n" \
"v_fma_f32 v78, v64, v65, v78\n" \
"v_mfma_f32_16x16x32_bf16 v[52:55], a[248:251], a[168:171], v[232:235]\n" \
"buffer_store_dwordx4 v[96:99], v77, s[56:59], s77 offen offset:0 sc1\n" \
"s_and_b64 s[52:53], s[52:53], s[8:9]\n" \
"s_cbranch_scc1 Lrs2_%=\n" \
"Lrr2_%=:\n" \
"v_mfma_f32_16x16x32_bf16 v[52:55], a[252:255], a[172:175], v[52:55]\n" \
"s_mov_b64 s[8:9], s[10:11]\n" \
"v_accvgpr_read_b32 v64, a24\n" \
"v_accvgpr_read_b32 v65, a25\n" \
"v_mfma_f32_16x16x32_bf16 v[52:55], a[240:243], a[160:163], v[52:55]\n" \
"v_accvgpr_read_b32 v66, a26\n" \
"v_accvgpr_read_b32 v67, a27\n" \
"v_mfma_f32_16x16x32_bf16 v[52:55], a[244:247], a[164:167], v[52:55]\n" \
"ds_read_b128 a[232:235], v247 offset:21504\n" \
"v_accvgpr_read_b32 v68, a28\n" \
"v_accvgpr_read_b32 v69, a29\n" \
"v_accvgpr_read_b32 v70, a30\n" \
"v_accvgpr_read_b32 v71, a31\n" \
"v_pk_mul_f32 v[64:65], v[64:65], v[76:77] op_sel_hi:[1,0]\n" \
"v_pk_mul_f32 v[66:67], v[66:67], v[76:77] op_sel_hi:[1,0]\n" \
"v_pk_mul_f32 v[68:69], v[68:69], v[76:77] op_sel_hi:[1,0]\n" \
"v_pk_mul_f32 v[70:71], v[70:71], v[76:77] op_sel_hi:[1,0]\n" \
"v_mfma_f32_16x16x32_bf16 v[44:47], a[240:243], v[208:211], v[228:231]\n" \
"v_cvt_pk_bf16_f32 v100, v64, v65\n" \
"v_cvt_pk_bf16_f32 v101, v66, v67\n" \
"v_cvt_pk_bf16_f32 v102, v68, v69\n" \
"v_cvt_pk_bf16_f32 v103, v70, v71\n" \
"v_pk_mul_f32 v[64:65], v[64:65], v[66:67]\n" \
"v_pk_fma_f32 v[64:65], v[68:69], v[70:71], v[64:65]\n" \
"v_mfma_f32_16x16x32_bf16 v[44:47], a[244:247], v[212:215], v[44:47]\n" \
"v_fma_f32 v78, v64, v65, v78\n" \
"v_mfma_f32_16x16x32_bf16 v[44:47], a[248:251], v[216:219], v[44:47]\n" \
"v_mfma_f32_16x16x32_bf16 v[44:47], a[252:255], v[220:223], v[44:47]\n" \
"buffer_store_dwordx4 v[100:103], v77, s[56:59], s77 offen offset:2048 sc1\n" \
"ds_read_b128 a[236:239], v247 offset:29696\n" \
"v_cmp_class_f32 vcc, v78, s94\n" \
"s_sub_u32 s77, s77, 4096\n" \
"v_mfma_f32_16x16x32_bf16 v[36:39], a[248:251], v[200:203], v[224:227]\n" \
"s_or_b64 s[92:93], s[92:93], vcc\n" \
"v_mfma_f32_16x16x32_bf16 v[36:39], a[252:255], v[204:207], v[36:39]\n" \
"v_mfma_f32_16x16x32_bf16 v[36:39], a[240:243], v[192:195], v[36:39]\n" \
"s_waitcnt vmcnt(8) lgkmcnt(4)\n" \
"v_mfma_f32_16x16x32_bf16 v[36:39], a[244:247], v[196:199], v[36:39]\n" \
"s_barrier\n" \
"ds_read_b128 a[240:243], v247 offset:22528\n" \
"v_mfma_f32_16x16x32_bf16 a[32:35], a[192:195], v[72:75], a[32:35]\n" \
"v_exp_f32 v0, v0\n" \
"v_mfma_f32_16x16x32_bf16 a[32:35], a[196:199], v[104:107], a[32:35]\n" \
"v_cvt_pk_bf16_f32 v123, v126, v127\n" \
"v_exp_f32 v1, v1\n" \
"v_exp_f32 v2, v2\n" \
"v_cvt_pk_bf16_f32 v0, v0, v1\n" \
"v_exp_f32 v3, v3\n" \
"v_exp_f32 v4, v4\n" \
"v_mfma_f32_16x16x32_bf16 v[128:131], a[192:195], v[80:83], v[128:131]\n" \
"s_xor_b32 m0, s73, 0x8000\n" \
"ds_read_b128 a[244:247], v247 offset:30720\n" \
"v_cvt_pk_bf16_f32 v1, v2, v3\n" \
"v_mfma_f32_16x16x32_bf16 v[128:131], a[196:199], v[112:115], v[128:131]\n" \
"ds_read_b128 a[248:251], v247 offset:23552\n" \
"global_load_lds_dwordx4 v250, s[36:37] offset:-4096 sc1\n" \
"v_mfma_f32_16x16x32_bf16 v[160:163], a[192:195], v[88:91], v[160:163]\n" \
"ds_read_b128 a[252:255], v247 offset:31744\n" \
"global_load_lds_dwordx4 v250, s[36:37] offset:-3072 sc1\n" \
"v_mfma_f32_16x16x32_bf16 v[160:163], a[196:199], v[120:123], v[160:163]\n" \
"s_add_u32 s70, s82, 0x10000\n" \
"ds_read_b128 a[192:195], v252 offset:0\n" \
"v_add_u32 v247, s70, v248\n" \
"v_mfma_f32_16x16x32_bf16 v[164:167], a[200:203], v[88:91], v[164:167]\n" \
"v_accvgpr_mov_b32 a0, a120\n" \
"v_accvgpr_mov_b32 a1, a121\n" \
"v_mfma_f32_16x16x32_bf16 v[164:167], a[204:207], v[120:123], v[164:167]\n" \
"v_accvgpr_mov_b32 a2, a122\n" \
"v_accvgpr_mov_b32 a3, a123\n" \
"v_mfma_f32_16x16x32_bf16 v[132:135], a[200:203], v[80:83], v[132:135]\n" \
"ds_read_b128 a[196:199], v252 offset:2048\n" \
"v_accvgpr_mov_b32 a4, a124\n" \
"v_mfma_f32_16x16x32_bf16 v[132:135], a[204:207], v[112:115], v[132:135]\n" \
"v_accvgpr_mov_b32 a5, a125\n" \
"v_accvgpr_mov_b32 a6, a126\n" \
"v_mfma_f32_16x16x32_bf16 a[36:39], a[200:203], v[72:75], a[36:39]\n" \
"v_accvgpr_mov_b32 a7, a127\n" \
"v_mov_b32 v64, 0x30000\n" \
"v_mfma_f32_16x16x32_bf16 a[36:39], a[204:207], v[104:107], a[36:39]\n" \
"ds_read_b128 a[200:203], v252 offset:4096\n" \
"global_load_lds_dwordx4 v250, s[36:37] offset:-2048 sc1\n" \
"v_mfma_f32_16x16x32_bf16 a[40:43], a[208:211], v[72:75], a[40:43]\n" \
"v_exp_f32 v5, v5\n" \
"v_mfma_f32_16x16x32_bf16 a[40:43], a[212:215], v[104:107], a[40:43]\n" \
"v_exp_f32 v6, v6\n" \
"v_mfma_f32_16x16x32_bf16 v[136:139], a[208:211], v[80:83], v[136:139]\n" \
"ds_read_b128 a[204:207], v252 offset:6144\n" \
"v_cvt_pk_bf16_f32 v2, v4, v5\n" \
"v_mfma_f32_16x16x32_bf16 v[136:139], a[212:215], v[112:115], v[136:139]\n" \
"v_exp_f32 v7, v7\n" \
"v_mfma_f32_16x16x32_bf16 v[168:171], a[208:211], v[88:91], v[168:171]\n" \
"v_exp_f32 v8, v8\n" \
"v_mfma_f32_16x16x32_bf16 v[168:171], a[212:215], v[120:123], v[168:171]\n" \
"ds_read_b128 a[208:211], v252 offset:256\n" \
"global_load_lds_dwordx4 v250, s[36:37] offset:-1024 sc1\n" \
"v_cvt_pk_bf16_f32 v3, v6, v7\n" \
"v_mfma_f32_16x16x32_bf16 v[172:175], a[216:219], v[88:91], v[172:175]\n" \
"v_exp_f32 v9, v9\n" \
"v_mfma_f32_16x16x32_bf16 v[172:175], a[220:223], v[120:123], v[172:175]\n" \
"v_exp_f32 v10, v10\n" \
"v_cvt_pk_bf16_f32 v8, v8, v9\n" \
"v_mfma_f32_16x16x32_bf16 v[140:143], a[216:219], v[80:83], v[140:143]\n" \
"ds_read_b128 a[212:215], v252 offset:2304\n" \
"global_load_lds_dwordx4 v250, s[36:37] offset:0 sc1\n" \
"v_mfma_f32_16x16x32_bf16 v[140:143], a[220:223], v[112:115], v[140:143]\n" \
"v_exp_f32 v11, v11\n" \
"v_mfma_f32_16x16x32_bf16 a[44:47], a[216:219], v[72:75], a[44:47]\n" \
"s_waitcnt lgkmcnt(6)\n" \
"v_exp_f32 v12, v12\n" \
"v_mfma_f32_16x16x32_bf16 a[44:47], a[220:223], v[104:107], a[44:47]\n" \
"ds_read_b128 a[216:219], v252 offset:4352\n" \
"v_cvt_pk_bf16_f32 v9, v10, v11\n" \
"v_mfma_f32_16x16x32_bf16 a[48:51], a[224:227], v[72:75], a[48:51]\n" \
"v_exp_f32 v13, v13\n" \
"v_mfma_f32_16x16x32_bf16 a[48:51], a[228:231], v[104:107], a[48:51]\n" \
"v_exp_f32 v14, v14\n" \
"v_cvt_pk_bf16_f32 v10, v12, v13\n" \
"v_mfma_f32_16x16x32_bf16 v[144:147], a[224:227], v[80:83], v[144:147]\n" \
"ds_read_b128 a[220:223], v252 offset:6400\n" \
"global_load_lds_dwordx4 v250, s[36:37] offset:1024 sc1\n" \
"v_mfma_f32_16x16x32_bf16 v[144:147], a[228:231], v[112:115], v[144:147]\n" \
"v_exp_f32 v15, v15\n" \
"v_mfma_f32_16x16x32_bf16 v[176:179], a[224:227], v[88:91], v[176:179]\n" \
"v_exp_f32 v16, v16\n" \
"v_mfma_f32_16x16x32_bf16 v[176:179], a[228:231], v[120:123], v[176:179]\n" \
"ds_read_b128 a[224:227], v252 offset:8192\n" \
"global_load_lds_dwordx4 v250, s[36:37] offset:2048 sc1\n" \
"v_cvt_pk_bf16_f32 v11, v14, v15\n" \
"v_mfma_f32_16x16x32_bf16 v[180:183], a[232:235], v[88:91], v[180:183]\n" \
"v_exp_f32 v17, v17\n" \
"v_mfma_f32_16x16x32_bf16 v[180:183], a[236:239], v[120:123], v[180:183]\n" \
"v_exp_f32 v18, v18\n" \
"v_cvt_pk_bf16_f32 v16, v16, v17\n" \
"v_mfma_f32_16x16x32_bf16 v[238:241], a[120:123], v[0:3], v[238:241]\n" \
"v_exp_f32 v19, v19\n" \
"v_mfma_f32_16x16x32_bf16 v[238:241], a[124:127], v[8:11], v[238:241]\n" \
"v_exp_f32 v20, v20\n" \
"v_cvt_pk_bf16_f32 v17, v18, v19\n" \
"v_mfma_f32_16x16x32_bf16 v[148:151], a[232:235], v[80:83], v[148:151]\n" \
"ds_read_b128 a[228:231], v252 offset:10240\n" \
"global_load_lds_dwordx4 v250, s[36:37] offset:3072 sc1\n" \
"v_mfma_f32_16x16x32_bf16 v[148:151], a[236:239], v[112:115], v[148:151]\n" \
"v_exp_f32 v21, v21\n" \
"v_mfma_f32_16x16x32_bf16 a[52:55], a[232:235], v[72:75], a[52:55]\n" \
"v_exp_f32 v22, v22\n" \
"v_mfma_f32_16x16x32_bf16 a[52:55], a[236:239], v[104:107], a[52:55]\n" \
"s_add_u32 s36, s36, 34816\n" \
"ds_read_b128 a[232:235], v252 offset:12288\n" \
"v_cvt_pk_bf16_f32 v18, v20, v21\n" \
"v_mfma_f32_16x16x32_bf16 a[56:59], a[240:243], v[72:75], a[56:59]\n" \
"v_exp_f32 v23, v23\n" \
"v_mfma_f32_16x16x32_bf16 a[56:59], a[244:247], v[104:107], a[56:59]\n" \
"v_exp_f32 v24, v24\n" \
"v_mfma_f32_16x16x32_bf16 v[152:155], a[240:243], v[80:83], v[152:155]\n" \
"ds_read_b128 a[236:239], v252 offset:14336\n" \
"v_cvt_pk_bf16_f32 v19, v22, v23\n" \
"v_mfma_f32_16x16x32_bf16 v[152:155], a[244:247], v[112:115], v[152:155]\n" \
"v_exp_f32 v25, v25\n" \
"v_exp_f32 v26, v26\n" \
"v_cvt_pk_bf16_f32 v24, v24, v25\n" \
"v_mfma_f32_16x16x32_bf16 v[242:245], a[120:123], v[16:19], v[242:245]\n" \
"v_exp_f32 v27, v27\n" \
"v_mfma_f32_16x16x32_bf16 v[184:187], a[240:243], v[88:91], v[184:187]\n" \
"s_load_dwordx8 s[44:51], s[56:57], s79 glc\n" \
"v_exp_f32 v28, v28\n" \
"v_mfma_f32_16x16x32_bf16 v[184:187], a[244:247], v[120:123], v[184:187]\n" \
"ds_read_b128 a[240:243], v252 offset:8448\n" \
"v_cvt_pk_bf16_f32 v25, v26, v27\n" \
"v_exp_f32 v29, v29\n" \
"v_mfma_f32_16x16x32_bf16 v[188:191], a[248:251], v[88:91], v[188:191]\n" \
"v_exp_f32 v30, v30\n" \
"v_mfma_f32_16x16x32_bf16 v[188:191], a[252:255], v[120:123], v[188:191]\n" \
"v_cvt_pk_bf16_f32 v26, v28, v29\n" \
"v_exp_f32 v31, v31\n" \
"v_mfma_f32_16x16x32_bf16 v[156:159], a[248:251], v[80:83], v[156:159]\n" \
"ds_read_b128 a[244:247], v252 offset:10496\n" \
"v_cvt_pk_bf16_f32 v27, v30, v31\n" \
"v_mfma_f32_16x16x32_bf16 v[156:159], a[252:255], v[112:115], v[156:159]\n" \
"v_mfma_f32_16x16x32_bf16 v[242:245], a[124:127], v[24:27], v[242:245]\n" \
"v_mfma_f32_16x16x32_bf16 a[60:63], a[248:251], v[72:75], a[60:63]\n" \
"s_waitcnt vmcnt(12) lgkmcnt(6)\n" \
"v_mfma_f32_16x16x32_bf16 a[60:63], a[252:255], v[104:107], a[60:63]\n" \
"s_barrier\n" \
"ds_read_b128 a[248:251], v252 offset:12544\n" \
"ds_read_b128 a[120:123], v64\n" \
"ds_read_b128 a[124:127], v64\n" \
"s_mov_b64 exec, 0\n" \
"s_cmp_eq_u32 s61, 0\n" \
"s_cbranch_scc1 Lst1x0_%=\n" \
"s_cmp_eq_u32 s61, 1\n" \
"s_cbranch_scc1 Lst1x1_%=\n" \
"s_cmp_eq_u32 s61, 2\n" \
"s_cbranch_scc1 Lst1x2_%=\n" \
"s_branch Lst1x3_%=\n" \
"Lcb1_%=:\n" \
"s_mov_b64 exec, -1\n" \
"s_waitcnt lgkmcnt(0)\n" \
"s_cmp_lg_u32 s63, 0\n" \
"s_cbranch_scc1 LA1g_%=\n" \
"s_xor_b64 s[52:53], s[44:45], s[42:43]\n" \
"s_xor_b64 s[54:55], s[46:47], s[42:43]\n" \
"s_or_b64 s[52:53], s[52:53], s[54:55]\n" \
"s_xor_b64 s[54:55], s[48:49], s[42:43]\n" \
"s_or_b64 s[52:53], s[52:53], s[54:55]\n" \
"s_xor_b64 s[54:55], s[50:51], s[42:43]\n" \
"s_or_b64 s[52:53], s[52:53], s[54:55]\n" \
"s_and_b64 s[52:53], s[52:53], s[8:9]\n" \
"s_cbranch_scc1 Lrs3_%=\n" \
"Lrr3_%=:\n" \
"s_branch LA1g_%=\n" \
"Lpg7025_%=:\n" \
"s_cbranch_execz Lcb0_%=\n" \
"Lloop2_%=:\n" \
"v_mfma_f32_16x16x32_bf16 a[64:67], a[192:195], v[0:3], a[64:67]\n" \
"v_exp_f32 v64, v64\n" \
"v_mfma_f32_16x16x32_bf16 a[64:67], a[196:199], v[32:35], a[64:67]\n" \
"v_cvt_pk_bf16_f32 v59, v62, v63\n" \
"v_add_f32 v240, v240, v64\n" \
"v_exp_f32 v65, v65\n" \
"v_mfma_f32_16x16x32_bf16 a[96:99], a[192:195], v[8:11], a[96:99]\n" \
"s_mov_b32 m0, s73\n" \
"ds_read_b128 a[244:247], v247 offset:14336\n" \
"v_add_f32 v240, v240, v65\n" \
"v_mfma_f32_16x16x32_bf16 a[96:99], a[196:199], v[40:43], a[96:99]\n" \
"ds_read_b128 a[248:251], v247 offset:7168\n" \
"global_load_lds_dwordx4 v250, s[36:37] offset:-4096 sc1\n" \
"v_mfma_f32_16x16x32_bf16 v[128:131], a[192:195], v[16:19], v[128:131]\n" \
"ds_read_b128 a[252:255], v247 offset:15360\n" \
"v_exp_f32 v66, v66\n" \
"v_mfma_f32_16x16x32_bf16 v[128:131], a[196:199], v[48:51], v[128:131]\n" \
"v_add_f32 v240, v240, v66\n" \
"v_cvt_pk_bf16_f32 v64, v64, v65\n" \
"v_mfma_f32_16x16x32_bf16 v[160:163], a[192:195], v[24:27], v[160:163]\n" \
"v_exp_f32 v67, v67\n" \
"v_mfma_f32_16x16x32_bf16 v[160:163], a[196:199], v[56:59], v[160:163]\n" \
"ds_read_b128 a[192:195], v246 offset:0\n" \
"global_load_lds_dwordx4 v250, s[36:37] offset:-3072 sc1\n" \
"v_add_f32 v240, v240, v67\n" \
"v_mfma_f32_16x16x32_bf16 v[164:167], a[200:203], v[24:27], v[164:167]\n" \
"v_exp_f32 v68, v68\n" \
"v_mfma_f32_16x16x32_bf16 v[164:167], a[204:207], v[56:59], v[164:167]\n" \
"v_add_f32 v240, v240, v68\n" \
"v_cvt_pk_bf16_f32 v65, v66, v67\n" \
"v_mfma_f32_16x16x32_bf16 v[132:135], a[200:203], v[16:19], v[132:135]\n" \
"v_exp_f32 v69, v69\n" \
"v_mfma_f32_16x16x32_bf16 v[132:135], a[204:207], v[48:51], v[132:135]\n" \
"ds_read_b128 a[196:199], v246 offset:2048\n" \
"v_add_f32 v240, v240, v69\n" \
"v_mfma_f32_16x16x32_bf16 a[100:103], a[200:203], v[8:11], a[100:103]\n" \
"v_exp_f32 v70, v70\n" \
"v_mfma_f32_16x16x32_bf16 a[100:103], a[204:207], v[40:43], a[100:103]\n" \
"v_add_f32 v240, v240, v70\n" \
"v_cvt_pk_bf16_f32 v66, v68, v69\n" \
"v_mfma_f32_16x16x32_bf16 a[68:71], a[200:203], v[0:3], a[68:71]\n" \
"v_exp_f32 v71, v71\n" \
"v_mfma_f32_16x16x32_bf16 a[68:71], a[204:207], v[32:35], a[68:71]\n" \
"ds_read_b128 a[200:203], v246 offset:4096\n" \
"global_load_lds_dwordx4 v250, s[36:37] offset:-2048 sc1\n" \
"v_add_f32 v240, v240, v71\n" \
"v_mfma_f32_16x16x32_bf16 a[72:75], a[208:211], v[0:3], a[72:75]\n" \
"v_exp_f32 v72, v72\n" \
"v_mfma_f32_16x16x32_bf16 a[72:75], a[212:215], v[32:35], a[72:75]\n" \
"v_add_f32 v241, v241, v72\n" \
"v_cvt_pk_bf16_f32 v67, v70, v71\n" \
"v_mfma_f32_16x16x32_bf16 a[104:107], a[208:211], v[8:11], a[104:107]\n" \
"v_exp_f32 v73, v73\n" \
"v_mfma_f32_16x16x32_bf16 a[104:107], a[212:215], v[40:43], a[104:107]\n" \
"ds_read_b128 a[204:207], v246 offset:6144\n" \
"global_load_lds_dwordx4 v250, s[36:37] offset:-1024 sc1\n" \
"v_add_f32 v241, v241, v73\n" \
"v_mfma_f32_16x16x32_bf16 v[136:139], a[208:211], v[16:19], v[136:139]\n" \
"v_exp_f32 v74, v74\n" \
"v_mfma_f32_16x16x32_bf16 v[136:139], a[212:215], v[48:51], v[136:139]\n" \
"v_add_f32 v241, v241, v74\n" \
"v_cvt_pk_bf16_f32 v72, v72, v73\n" \
"v_mfma_f32_16x16x32_bf16 v[168:171], a[208:211], v[24:27], v[168:171]\n" \
"v_exp_f32 v75, v75\n" \
"v_mfma_f32_16x16x32_bf16 v[168:171], a[212:215], v[56:59], v[168:171]\n" \
"ds_read_b128 a[208:211], v246 offset:256\n" \
"v_add_f32 v241, v241, v75\n" \
"v_mfma_f32_16x16x32_bf16 v[172:175], a[216:219], v[24:27], v[172:175]\n" \
"v_exp_f32 v76, v76\n" \
"v_mfma_f32_16x16x32_bf16 v[172:175], a[220:223], v[56:59], v[172:175]\n" \
"v_add_f32 v241, v241, v76\n" \
"v_cvt_pk_bf16_f32 v73, v74, v75\n" \
"v_mfma_f32_16x16x32_bf16 v[140:143], a[216:219], v[16:19], v[140:143]\n" \
"v_exp_f32 v77, v77\n" \
"v_mfma_f32_16x16x32_bf16 v[140:143], a[220:223], v[48:51], v[140:143]\n" \
"ds_read_b128 a[212:215], v246 offset:2304\n" \
"global_load_lds_dwordx4 v250, s[36:37] offset:0 sc1\n" \
"v_add_f32 v241, v241, v77\n" \
"v_mfma_f32_16x16x32_bf16 a[108:111], a[216:219], v[8:11], a[108:111]\n" \
"v_exp_f32 v78, v78\n" \
"v_mfma_f32_16x16x32_bf16 a[108:111], a[220:223], v[40:43], a[108:111]\n" \
"v_add_f32 v241, v241, v78\n" \
"v_cvt_pk_bf16_f32 v74, v76, v77\n" \
"v_mfma_f32_16x16x32_bf16 a[76:79], a[216:219], v[0:3], a[76:79]\n" \
"s_waitcnt lgkmcnt(6)\n" \
"v_exp_f32 v79, v79\n" \
"v_mfma_f32_16x16x32_bf16 a[76:79], a[220:223], v[32:35], a[76:79]\n" \
"ds_read_b128 a[216:219], v246 offset:4352\n" \
"global_load_lds_dwordx4 v250, s[36:37] offset:1024 sc1\n" \
"v_add_f32 v241, v241, v79\n" \
"v_mfma_f32_16x16x32_bf16 a[80:83], a[224:227], v[0:3], a[80:83]\n" \
"v_exp_f32 v80, v80\n" \
"v_mfma_f32_16x16x32_bf16 a[80:83], a[228:231], v[32:35], a[80:83]\n" \
"v_add_f32 v244, v244, v80\n" \
"v_cvt_pk_bf16_f32 v75, v78, v79\n" \
"v_mfma_f32_16x16x32_bf16 a[112:115], a[224:227], v[8:11], a[112:115]\n" \
"v_exp_f32 v81, v81\n" \
"v_mfma_f32_16x16x32_bf16 a[112:115], a[228:231], v[40:43], a[112:115]\n" \
"ds_read_b128 a[220:223], v246 offset:6400\n" \
"v_add_f32 v244, v244, v81\n" \
"v_mfma_f32_16x16x32_bf16 v[144:147], a[224:227], v[16:19], v[144:147]\n" \
"v_exp_f32 v82, v82\n" \
"v_mfma_f32_16x16x32_bf16 v[144:147], a[228:231], v[48:51], v[144:147]\n" \
"v_add_f32 v244, v244, v82\n" \
"v_cvt_pk_bf16_f32 v80, v80, v81\n" \
"v_mfma_f32_16x16x32_bf16 v[176:179], a[224:227], v[24:27], v[176:179]\n" \
"v_exp_f32 v83, v83\n" \
"v_mfma_f32_16x16x32_bf16 v[176:179], a[228:231], v[56:59], v[176:179]\n" \
"ds_read_b128 a[224:227], v246 offset:8192\n" \
"global_load_lds_dwordx4 v250, s[36:37] offset:2048 sc1\n" \
"v_add_f32 v244, v244, v83\n" \
"v_mfma_f32_16x16x32_bf16 v[180:183], a[232:235], v[24:27], v[180:183]\n" \
"v_exp_f32 v84, v84\n" \
"v_mfma_f32_16x16x32_bf16 v[180:183], a[236:239], v[56:59], v[180:183]\n" \
"v_add_f32 v244, v244, v84\n" \
"v_cvt_pk_bf16_f32 v81, v82, v83\n" \
"v_mfma_f32_16x16x32_bf16 v[148:151], a[232:235], v[16:19], v[148:151]\n" \
"v_exp_f32 v85, v85\n" \
"v_mfma_f32_16x16x32_bf16 v[148:151], a[236:239], v[48:51], v[148:151]\n" \
"ds_read_b128 a[228:231], v246 offset:10240\n" \
"global_load_lds_dwordx4 v250, s[36:37] offset:3072 sc1\n" \
"v_add_f32 v244, v244, v85\n" \
"v_mfma_f32_16x16x32_bf16 a[116:119], a[232:235], v[8:11], a[116:119]\n" \
"s_add_u32 s36, s36, 34816\n" \
"v_exp_f32 v86, v86\n" \
"v_mfma_f32_16x16x32_bf16 a[116:119], a[236:239], v[40:43], a[116:119]\n" \
"v_add_f32 v244, v244, v86\n" \
"v_cvt_pk_bf16_f32 v82, v84, v85\n" \
"v_mfma_f32_16x16x32_bf16 a[84:87], a[232:235], v[0:3], a[84:87]\n" \
"v_exp_f32 v87, v87\n" \
"v_mfma_f32_16x16x32_bf16 a[84:87], a[236:239], v[32:35], a[84:87]\n" \
"ds_read_b128 a[232:235], v246 offset:12288\n" \
"v_add_f32 v244, v244, v87\n" \
"v_mfma_f32_16x16x32_bf16 a[88:91], a[240:243], v[0:3], a[88:91]\n" \
"v_exp_f32 v88, v88\n" \
"v_mfma_f32_16x16x32_bf16 a[88:91], a[244:247], v[32:35], a[88:91]\n" \
"v_add_f32 v245, v245, v88\n" \
"v_cvt_pk_bf16_f32 v83, v86, v87\n" \
"v_mfma_f32_16x16x32_bf16 a[120:123], a[240:243], v[8:11], a[120:123]\n" \
"v_exp_f32 v89, v89\n" \
"v_mfma_f32_16x16x32_bf16 a[120:123], a[244:247], v[40:43], a[120:123]\n" \
"ds_read_b128 a[236:239], v246 offset:14336\n" \
"v_add_f32 v245, v245, v89\n" \
"v_mfma_f32_16x16x32_bf16 v[152:155], a[240:243], v[16:19], v[152:155]\n" \
"v_exp_f32 v90, v90\n" \
"v_mfma_f32_16x16x32_bf16 v[152:155], a[244:247], v[48:51], v[152:155]\n" \
"v_add_f32 v245, v245, v90\n" \
"v_cvt_pk_bf16_f32 v88, v88, v89\n" \
"v_mfma_f32_16x16x32_bf16 v[184:187], a[240:243], v[24:27], v[184:187]\n" \
"v_exp_f32 v91, v91\n" \
"v_mfma_f32_16x16x32_bf16 v[184:187], a[244:247], v[56:59], v[184:187]\n" \
"ds_read_b128 a[240:243], v246 offset:8448\n" \
"v_add_f32 v245, v245, v91\n" \
"v_mfma_f32_16x16x32_bf16 v[188:191], a[248:251], v[24:27], v[188:191]\n" \
"v_exp_f32 v92, v92\n" \
"v_mfma_f32_16x16x32_bf16 v[188:191], a[252:255], v[56:59], v[188:191]\n" \
"v_add_f32 v245, v245, v92\n" \
"v_cvt_pk_bf16_f32 v89, v90, v91\n" \
"v_mfma_f32_16x16x32_bf16 v[156:159], a[248:251], v[16:19], v[156:159]\n" \
"v_exp_f32 v93, v93\n" \
"v_mfma_f32_16x16x32_bf16 v[156:159], a[252:255], v[48:51], v[156:159]\n" \
"ds_read_b128 a[244:247], v246 offset:10496\n" \
"v_add_f32 v245, v245, v93\n" \
"v_mfma_f32_16x16x32_bf16 a[124:127], a[248:251], v[8:11], a[124:127]\n" \
"v_exp_f32 v94, v94\n" \
"v_mfma_f32_16x16x32_bf16 a[124:127], a[252:255], v[40:43], a[124:127]\n" \
"v_add_f32 v245, v245, v94\n" \
"v_cvt_pk_bf16_f32 v90, v92, v93\n" \
"v_mfma_f32_16x16x32_bf16 a[92:95], a[248:251], v[0:3], a[92:95]\n" \
"s_waitcnt vmcnt(8) lgkmcnt(6)\n" \
"v_exp_f32 v95, v95\n" \
"v_mfma_f32_16x16x32_bf16 a[92:95], a[252:255], v[32:35], a[92:95]\n" \
"s_barrier\n" \
"ds_read_b128 a[248:251], v246 offset:12544\n" \
"v_add_f32 v245, v245, v95\n" \
"v_mfma_f32_16x16x32_bf16 v[0:3], a[192:195], v[192:195], v[224:227]\n" \
"v_exp_f32 v96, v96\n" \
"v_mfma_f32_16x16x32_bf16 v[0:3], a[196:199], v[196:199], v[0:3]\n" \
"v_cvt_pk_bf16_f32 v91, v94, v95\n" \
"v_add_f32 v240, v240, v96\n" \
"v_mfma_f32_16x16x32_bf16 v[0:3], a[200:203], v[200:203], v[0:3]\n" \
"s_mov_b32 m0, s74\n" \
"v_exp_f32 v97, v97\n" \
"v_mfma_f32_16x16x32_bf16 v[0:3], a[204:207], v[204:207], v[0:3]\n" \
"ds_read_b128 a[252:255], v246 offset:14592\n" \
"v_add_f32 v240, v240, v97\n" \
"v_mfma_f32_16x16x32_bf16 v[8:11], a[200:203], v[216:219], v[228:231]\n" \
"global_load_lds_dwordx4 v250, s[38:39] offset:-4096 sc1\n" \
"global_load_lds_dwordx4 v250, s[38:39] offset:-3072 sc1\n" \
"v_mfma_f32_16x16x32_bf16 v[8:11], a[204:207], v[220:223], v[8:11]\n" \
"v_exp_f32 v98, v98\n" \
"v_mfma_f32_16x16x32_bf16 v[8:11], a[192:195], v[208:211], v[8:11]\n" \
"v_add_f32 v240, v240, v98\n" \
"v_cvt_pk_bf16_f32 v96, v96, v97\n" \
"v_mfma_f32_16x16x32_bf16 v[8:11], a[196:199], v[212:215], v[8:11]\n" \
"v_exp_f32 v99, v99\n" \
"v_mfma_f32_16x16x32_bf16 v[16:19], a[192:195], a[160:163], v[232:235]\n" \
"v_add_f32 v240, v240, v99\n" \
"v_exp_f32 v100, v100\n" \
"v_mfma_f32_16x16x32_bf16 v[16:19], a[196:199], a[164:167], v[16:19]\n" \
"v_add_f32 v240, v240, v100\n" \
"v_cvt_pk_bf16_f32 v97, v98, v99\n" \
"v_mfma_f32_16x16x32_bf16 v[16:19], a[200:203], a[168:171], v[16:19]\n" \
"v_exp_f32 v101, v101\n" \
"v_mfma_f32_16x16x32_bf16 v[16:19], a[204:207], a[172:175], v[16:19]\n" \
"v_add_f32 v240, v240, v101\n" \
"v_exp_f32 v102, v102\n" \
"v_mfma_f32_16x16x32_bf16 v[24:27], a[200:203], a[184:187], v[236:239]\n" \
"v_add_f32 v240, v240, v102\n" \
"v_cvt_pk_bf16_f32 v98, v100, v101\n" \
"v_mfma_f32_16x16x32_bf16 v[24:27], a[204:207], a[188:191], v[24:27]\n" \
"v_exp_f32 v103, v103\n" \
"v_mfma_f32_16x16x32_bf16 v[24:27], a[192:195], a[176:179], v[24:27]\n" \
"v_add_f32 v240, v240, v103\n" \
"v_mfma_f32_16x16x32_bf16 v[24:27], a[196:199], a[180:183], v[24:27]\n" \
"ds_read_b128 a[192:195], v247 offset:32768\n" \
"global_load_lds_dwordx4 v250, s[38:39] offset:-2048 sc1\n" \
"v_mfma_f32_16x16x32_bf16 v[28:31], a[208:211], a[176:179], v[236:239]\n" \
"v_exp_f32 v104, v104\n" \
"v_mfma_f32_16x16x32_bf16 v[28:31], a[212:215], a[180:183], v[28:31]\n" \
"v_add_f32 v241, v241, v104\n" \
"v_cvt_pk_bf16_f32 v99, v102, v103\n" \
"v_mfma_f32_16x16x32_bf16 v[28:31], a[216:219], a[184:187], v[28:31]\n" \
"v_exp_f32 v105, v105\n" \
"v_mfma_f32_16x16x32_bf16 v[28:31], a[220:223], a[188:191], v[28:31]\n" \
"ds_read_b128 a[196:199], v247 offset:40960\n" \
"global_load_lds_dwordx4 v250, s[38:39] offset:-1024 sc1\n" \
"v_add_f32 v241, v241, v105\n" \
"v_mfma_f32_16x16x32_bf16 v[20:23], a[216:219], a[168:171], v[232:235]\n" \
"v_exp_f32 v106, v106\n" \
"v_mfma_f32_16x16x32_bf16 v[20:23], a[220:223], a[172:175], v[20:23]\n" \
"v_add_f32 v241, v241, v106\n" \
"v_cvt_pk_bf16_f32 v104, v104, v105\n" \
"v_mfma_f32_16x16x32_bf16 v[20:23], a[208:211], a[160:163], v[20:23]\n" \
"v_exp_f32 v107, v107\n" \
"v_mfma_f32_16x16x32_bf16 v[20:23], a[212:215], a[164:167], v[20:23]\n" \
"ds_read_b128 a[200:203], v247 offset:33792\n" \
"v_add_f32 v241, v241, v107\n" \
"v_mfma_f32_16x16x32_bf16 v[12:15], a[208:211], v[208:211], v[228:231]\n" \
"v_exp_f32 v108, v108\n" \
"v_mfma_f32_16x16x32_bf16 v[12:15], a[212:215], v[212:215], v[12:15]\n" \
"v_add_f32 v241, v241, v108\n" \
"v_cvt_pk_bf16_f32 v105, v106, v107\n" \
"v_mfma_f32_16x16x32_bf16 v[12:15], a[216:219], v[216:219], v[12:15]\n" \
"v_exp_f32 v109, v109\n" \
"v_mfma_f32_16x16x32_bf16 v[12:15], a[220:223], v[220:223], v[12:15]\n" \
"ds_read_b128 a[204:207], v247 offset:41984\n" \
"global_load_lds_dwordx4 v250, s[38:39] offset:0 sc1\n" \
"v_add_f32 v241, v241, v109\n" \
"v_mfma_f32_16x16x32_bf16 v[4:7], a[216:219], v[200:203], v[224:227]\n" \
"v_exp_f32 v110, v110\n" \
"v_mfma_f32_16x16x32_bf16 v[4:7], a[220:223], v[204:207], v[4:7]\n" \
"v_add_f32 v241, v241, v110\n" \
"v_cvt_pk_bf16_f32 v106, v108, v109\n" \
"v_mfma_f32_16x16x32_bf16 v[4:7], a[208:211], v[192:195], v[4:7]\n" \
"s_waitcnt lgkmcnt(4)\n" \
"v_exp_f32 v111, v111\n" \
"v_mfma_f32_16x16x32_bf16 v[4:7], a[212:215], v[196:199], v[4:7]\n" \
"ds_read_b128 a[208:211], v247 offset:34816\n" \
"global_load_lds_dwordx4 v250, s[38:39] offset:1024 sc1\n" \
"v_add_f32 v241, v241, v111\n" \
"v_mfma_f32_16x16x32_bf16 v[32:35], a[224:227], v[192:195], v[224:227]\n" \
"v_exp_f32 v112, v112\n" \
"v_mfma_f32_16x16x32_bf16 v[32:35], a[228:231], v[196:199], v[32:35]\n" \
"v_add_f32 v244, v244, v112\n" \
"v_cvt_pk_bf16_f32 v107, v110, v111\n" \
"v_mfma_f32_16x16x32_bf16 v[32:35], a[232:235], v[200:203], v[32:35]\n" \
"v_exp_f32 v113, v113\n" \
"v_mfma_f32_16x16x32_bf16 v[32:35], a[236:239], v[204:207], v[32:35]\n" \
"ds_read_b128 a[212:215], v247 offset:43008\n" \
"v_add_f32 v244, v244, v113\n" \
"v_mfma_f32_16x16x32_bf16 v[40:43], a[232:235], v[216:219], v[228:231]\n" \
"v_exp_f32 v114, v114\n" \
"v_mfma_f32_16x16x32_bf16 v[40:43], a[236:239], v[220:223], v[40:43]\n" \
"v_add_f32 v244, v244, v114\n" \
"v_cvt_pk_bf16_f32 v112, v112, v113\n" \
"v_mfma_f32_16x16x32_bf16 v[40:43], a[224:227], v[208:211], v[40:43]\n" \
"v_exp_f32 v115, v115\n" \
"v_mfma_f32_16x16x32_bf16 v[40:43], a[228:231], v[212:215], v[40:43]\n" \
"ds_read_b128 a[216:219], v247 offset:35840\n" \
"global_load_lds_dwordx4 v250, s[38:39] offset:2048 sc1\n" \
"v_add_f32 v244, v244, v115\n" \
"v_mfma_f32_16x16x32_bf16 v[48:51], a[224:227], a[160:163], v[232:235]\n" \
"v_exp_f32 v116, v116\n" \
"v_mfma_f32_16x16x32_bf16 v[48:51], a[228:231], a[164:167], v[48:51]\n" \
"v_add_f32 v244, v244, v116\n" \
"v_cvt_pk_bf16_f32 v113, v114, v115\n" \
"v_mfma_f32_16x16x32_bf16 v[48:51], a[232:235], a[168:171], v[48:51]\n" \
"v_exp_f32 v117, v117\n" \
"v_mfma_f32_16x16x32_bf16 v[48:51], a[236:239], a[172:175], v[48:51]\n" \
"ds_read_b128 a[220:223], v247 offset:44032\n" \
"global_load_lds_dwordx4 v250, s[38:39] offset:3072 sc1\n" \
"v_add_f32 v244, v244, v117\n" \
"v_mfma_f32_16x16x32_bf16 v[56:59], a[232:235], a[184:187], v[236:239]\n" \
"s_add_u32 s38, s38, 34816\n" \
"v_exp_f32 v118, v118\n" \
"v_mfma_f32_16x16x32_bf16 v[56:59], a[236:239], a[188:191], v[56:59]\n" \
"v_add_f32 v244, v244, v118\n" \
"v_cvt_pk_bf16_f32 v114, v116, v117\n" \
"v_mfma_f32_16x16x32_bf16 v[56:59], a[224:227], a[176:179], v[56:59]\n" \
"v_exp_f32 v119, v119\n" \
"v_mfma_f32_16x16x32_bf16 v[56:59], a[228:231], a[180:183], v[56:59]\n" \
"ds_read_b128 a[224:227], v247 offset:36864\n" \
"v_add_f32 v244, v244, v119\n" \
"v_mfma_f32_16x16x32_bf16 v[60:63], a[240:243], a[176:179], v[236:239]\n" \
"v_exp_f32 v120, v120\n" \
"v_mfma_f32_16x16x32_bf16 v[60:63], a[244:247], a[180:183], v[60:63]\n" \
"v_add_f32 v245, v245, v120\n" \
"v_cvt_pk_bf16_f32 v115, v118, v119\n" \
"v_mfma_f32_16x16x32_bf16 v[60:63], a[248:251], a[184:187], v[60:63]\n" \
"v_exp_f32 v121, v121\n" \
"v_mfma_f32_16x16x32_bf16 v[60:63], a[252:255], a[188:191], v[60:63]\n" \
"ds_read_b128 a[228:231], v247 offset:45056\n" \
"v_add_f32 v245, v245, v121\n" \
"v_mfma_f32_16x16x32_bf16 v[52:55], a[248:251], a[168:171], v[232:235]\n" \
"v_exp_f32 v122, v122\n" \
"v_mfma_f32_16x16x32_bf16 v[52:55], a[252:255], a[172:175], v[52:55]\n" \
"v_add_f32 v245, v245, v122\n" \
"v_cvt_pk_bf16_f32 v120, v120, v121\n" \
"v_mfma_f32_16x16x32_bf16 v[52:55], a[240:243], a[160:163], v[52:55]\n" \
"v_exp_f32 v123, v123\n" \
"v_mfma_f32_16x16x32_bf16 v[52:55], a[244:247], a[164:167], v[52:55]\n" \
"ds_read_b128 a[232:235], v247 offset:37888\n" \
"v_add_f32 v245, v245, v123\n" \
"v_mfma_f32_16x16x32_bf16 v[44:47], a[240:243], v[208:211], v[228:231]\n" \
"v_exp_f32 v124, v124\n" \
"v_mfma_f32_16x16x32_bf16 v[44:47], a[244:247], v[212:215], v[44:47]\n" \
"v_add_f32 v245, v245, v124\n" \
"v_cvt_pk_bf16_f32 v121, v122, v123\n" \
"v_mfma_f32_16x16x32_bf16 v[44:47], a[248:251], v[216:219], v[44:47]\n" \
"v_exp_f32 v125, v125\n" \
"v_mfma_f32_16x16x32_bf16 v[44:47], a[252:255], v[220:223], v[44:47]\n" \
"ds_read_b128 a[236:239], v247 offset:46080\n" \
"v_add_f32 v245, v245, v125\n" \
"v_mfma_f32_16x16x32_bf16 v[36:39], a[248:251], v[200:203], v[224:227]\n" \
"v_exp_f32 v126, v126\n" \
"v_mfma_f32_16x16x32_bf16 v[36:39], a[252:255], v[204:207], v[36:39]\n" \
"v_add_f32 v245, v245, v126\n" \
"v_cvt_pk_bf16_f32 v122, v124, v125\n" \
"v_mfma_f32_16x16x32_bf16 v[36:39], a[240:243], v[192:195], v[36:39]\n" \
"s_waitcnt vmcnt(8) lgkmcnt(4)\n" \
"v_exp_f32 v127, v127\n" \
"v_mfma_f32_16x16x32_bf16 v[36:39], a[244:247], v[196:199], v[36:39]\n" \
"s_barrier\n" \
"ds_read_b128 a[240:243], v247 offset:38912\n" \
"v_add_f32 v245, v245, v127\n" \
"v_mfma_f32_16x16x32_bf16 a[64:67], a[192:195], v[64:67], a[64:67]\n" \
"v_exp_f32 v0, v0\n" \
"v_mfma_f32_16x16x32_bf16 a[64:67], a[196:199], v[96:99], a[64:67]\n" \
"v_cvt_pk_bf16_f32 v123, v126, v127\n" \
"v_add_f32 v240, v240, v0\n" \
"v_exp_f32 v1, v1\n" \
"v_mfma_f32_16x16x32_bf16 a[96:99], a[192:195], v[72:75], a[96:99]\n" \
"s_xor_b32 m0, s73, 0x8000\n" \
"ds_read_b128 a[244:247], v247 offset:47104\n" \
"v_add_f32 v240, v240, v1\n" \
"v_mfma_f32_16x16x32_bf16 a[96:99], a[196:199], v[104:107], a[96:99]\n" \
"ds_read_b128 a[248:251], v247 offset:39936\n" \
"global_load_lds_dwordx4 v250, s[36:37] offset:-4096 sc1\n" \
"v_mfma_f32_16x16x32_bf16 v[128:131], a[192:195], v[80:83], v[128:131]\n" \
"ds_read_b128 a[252:255], v247 offset:48128\n" \
"v_exp_f32 v2, v2\n" \
"v_mfma_f32_16x16x32_bf16 v[128:131], a[196:199], v[112:115], v[128:131]\n" \
"v_add_f32 v240, v240, v2\n" \
"v_cvt_pk_bf16_f32 v0, v0, v1\n" \
"v_mfma_f32_16x16x32_bf16 v[160:163], a[192:195], v[88:91], v[160:163]\n" \
"v_exp_f32 v3, v3\n" \
"v_mfma_f32_16x16x32_bf16 v[160:163], a[196:199], v[120:123], v[160:163]\n" \
"ds_read_b128 a[192:195], v252 offset:0\n" \
"global_load_lds_dwordx4 v250, s[36:37] offset:-3072 sc1\n" \
"Lpg7397_%=:\n" \
"Lst1x0_%=:\n" \
"s_cbranch_execz Lpg9729_%=\n" \
"v_add_f32 v240, v240, v3\n" \
"v_mfma_f32_16x16x32_bf16 v[164:167], a[200:203], v[88:91], v[164:167]\n" \
"v_exp_f32 v4, v4\n" \
"v_mfma_f32_16x16x32_bf16 v[164:167], a[204:207], v[120:123], v[164:167]\n" \
"v_add_f32 v240, v240, v4\n" \
"v_cvt_pk_bf16_f32 v1, v2, v3\n" \
"v_mfma_f32_16x16x32_bf16 v[132:135], a[200:203], v[80:83], v[132:135]\n" \
"v_exp_f32 v5, v5\n" \
"v_mfma_f32_16x16x32_bf16 v[132:135], a[204:207], v[112:115], v[132:135]\n" \
"ds_read_b128 a[196:199], v252 offset:2048\n" \
"v_add_f32 v240, v240, v5\n" \
"v_mfma_f32_16x16x32_bf16 a[100:103], a[200:203], v[72:75], a[100:103]\n" \
"v_exp_f32 v6, v6\n" \
"v_mfma_f32_16x16x32_bf16 a[100:103], a[204:207], v[104:107], a[100:103]\n" \
"v_add_f32 v240, v240, v6\n" \
"v_cvt_pk_bf16_f32 v2, v4, v5\n" \
"v_mfma_f32_16x16x32_bf16 a[68:71], a[200:203], v[64:67], a[68:71]\n" \
"v_exp_f32 v7, v7\n" \
"v_mfma_f32_16x16x32_bf16 a[68:71], a[204:207], v[96:99], a[68:71]\n" \
"ds_read_b128 a[200:203], v252 offset:4096\n" \
"global_load_lds_dwordx4 v250, s[36:37] offset:-2048 sc1\n" \
"v_add_f32 v240, v240, v7\n" \
"v_mfma_f32_16x16x32_bf16 a[72:75], a[208:211], v[64:67], a[72:75]\n" \
"v_exp_f32 v8, v8\n" \
"v_mfma_f32_16x16x32_bf16 a[72:75], a[212:215], v[96:99], a[72:75]\n" \
"v_add_f32 v241, v241, v8\n" \
"v_cvt_pk_bf16_f32 v3, v6, v7\n" \
"v_mfma_f32_16x16x32_bf16 a[104:107], a[208:211], v[72:75], a[104:107]\n" \
"v_exp_f32 v9, v9\n" \
"v_mfma_f32_16x16x32_bf16 a[104:107], a[212:215], v[104:107], a[104:107]\n" \
"ds_read_b128 a[204:207], v252 offset:6144\n" \
"global_load_lds_dwordx4 v250, s[36:37] offset:-1024 sc1\n" \
"v_add_f32 v241, v241, v9\n" \
"v_mfma_f32_16x16x32_bf16 v[136:139], a[208:211], v[80:83], v[136:139]\n" \
"v_exp_f32 v10, v10\n" \
"v_mfma_f32_16x16x32_bf16 v[136:139], a[212:215], v[112:115], v[136:139]\n" \
"v_add_f32 v241, v241, v10\n" \
"v_cvt_pk_bf16_f32 v8, v8, v9\n" \
"v_mfma_f32_16x16x32_bf16 v[168:171], a[208:211], v[88:91], v[168:171]\n" \
"v_exp_f32 v11, v11\n" \
"v_mfma_f32_16x16x32_bf16 v[168:171], a[212:215], v[120:123], v[168:171]\n" \
"ds_read_b128 a[208:211], v252 offset:256\n" \
"v_add_f32 v241, v241, v11\n" \
"v_mfma_f32_16x16x32_bf16 v[172:175], a[216:219], v[88:91], v[172:175]\n" \
"v_exp_f32 v12, v12\n" \
"v_mfma_f32_16x16x32_bf16 v[172:175], a[220:223], v[120:123], v[172:175]\n" \
"v_add_f32 v241, v241, v12\n" \
"v_cvt_pk_bf16_f32 v9, v10, v11\n" \
"v_mfma_f32_16x16x32_bf16 v[140:143], a[216:219], v[80:83], v[140:143]\n" \
"v_exp_f32 v13, v13\n" \
"v_mfma_f32_16x16x32_bf16 v[140:143], a[220:223], v[112:115], v[140:143]\n" \
"ds_read_b128 a[212:215], v252 offset:2304\n" \
"global_load_lds_dwordx4 v250, s[36:37] offset:0 sc1\n" \
"v_add_f32 v241, v241, v13\n" \
"v_mfma_f32_16x16x32_bf16 a[108:111], a[216:219], v[72:75], a[108:111]\n" \
"v_exp_f32 v14, v14\n" \
"v_mfma_f32_16x16x32_bf16 a[108:111], a[220:223], v[104:107], a[108:111]\n" \
"v_add_f32 v241, v241, v14\n" \
"v_cvt_pk_bf16_f32 v10, v12, v13\n" \
"v_mfma_f32_16x16x32_bf16 a[76:79], a[216:219], v[64:67], a[76:79]\n" \
"s_waitcnt lgkmcnt(6)\n" \
"v_exp_f32 v15, v15\n" \
"v_mfma_f32_16x16x32_bf16 a[76:79], a[220:223], v[96:99], a[76:79]\n" \
"ds_read_b128 a[216:219], v252 offset:4352\n" \
"global_load_lds_dwordx4 v250, s[36:37] offset:1024 sc1\n" \
"v_add_f32 v241, v241, v15\n" \
"v_mfma_f32_16x16x32_bf16 a[80:83], a[224:227], v[64:67], a[80:83]\n" \
"v_exp_f32 v16, v16\n" \
"v_mfma_f32_16x16x32_bf16 a[80:83], a[228:231], v[96:99], a[80:83]\n" \
"v_add_f32 v244, v244, v16\n" \
"v_cvt_pk_bf16_f32 v11, v14, v15\n" \
"v_mfma_f32_16x16x32_bf16 a[112:115], a[224:227], v[72:75], a[112:115]\n" \
"v_exp_f32 v17, v17\n" \
"v_mfma_f32_16x16x32_bf16 a[112:115], a[228:231], v[104:107], a[112:115]\n" \
"ds_read_b128 a[220:223], v252 offset:6400\n" \
"v_add_f32 v244, v244, v17\n" \
"v_mfma_f32_16x16x32_bf16 v[144:147], a[224:227], v[80:83], v[144:147]\n" \
"v_exp_f32 v18, v18\n" \
"v_mfma_f32_16x16x32_bf16 v[144:147], a[228:231], v[112:115], v[144:147]\n" \
"v_add_f32 v244, v244, v18\n" \
"v_cvt_pk_bf16_f32 v16, v16, v17\n" \
"v_mfma_f32_16x16x32_bf16 v[176:179], a[224:227], v[88:91], v[176:179]\n" \
"v_exp_f32 v19, v19\n" \
"v_mfma_f32_16x16x32_bf16 v[176:179], a[228:231], v[120:123], v[176:179]\n" \
"ds_read_b128 a[224:227], v252 offset:8192\n" \
"global_load_lds_dwordx4 v250, s[36:37] offset:2048 sc1\n" \
"v_add_f32 v244, v244, v19\n" \
"v_mfma_f32_16x16x32_bf16 v[180:183], a[232:235], v[88:91], v[180:183]\n" \
"v_exp_f32 v20, v20\n" \
"v_mfma_f32_16x16x32_bf16 v[180:183], a[236:239], v[120:123], v[180:183]\n" \
"v_add_f32 v244, v244, v20\n" \
"v_cvt_pk_bf16_f32 v17, v18, v19\n" \
"v_mfma_f32_16x16x32_bf16 v[148:151], a[232:235], v[80:83], v[148:151]\n" \
"v_exp_f32 v21, v21\n" \
"v_mfma_f32_16x16x32_bf16 v[148:151], a[236:239], v[112:115], v[148:151]\n" \
"ds_read_b128 a[228:231], v252 offset:10240\n" \
"global_load_lds_dwordx4 v250, s[36:37] offset:3072 sc1\n" \
"v_add_f32 v244, v244, v21\n" \
"v_mfma_f32_16x16x32_bf16 a[116:119], a[232:235], v[72:75], a[116:119]\n" \
"s_add_u32 s36, s36, 34816\n" \
"v_exp_f32 v22, v22\n" \
"v_mfma_f32_16x16x32_bf16 a[116:119], a[236:239], v[104:107], a[116:119]\n" \
"v_add_f32 v244, v244, v22\n" \
"v_cvt_pk_bf16_f32 v18, v20, v21\n" \
"v_mfma_f32_16x16x32_bf16 a[84:87], a[232:235], v[64:67], a[84:87]\n" \
"v_exp_f32 v23, v23\n" \
"v_mfma_f32_16x16x32_bf16 a[84:87], a[236:239], v[96:99], a[84:87]\n" \
"ds_read_b128 a[232:235], v252 offset:12288\n" \
"v_add_f32 v244, v244, v23\n" \
"v_mfma_f32_16x16x32_bf16 a[88:91], a[240:243], v[64:67], a[88:91]\n" \
"v_exp_f32 v24, v24\n" \
"v_mfma_f32_16x16x32_bf16 a[88:91], a[244:247], v[96:99], a[88:91]\n" \
"v_add_f32 v245, v245, v24\n" \
"v_cvt_pk_bf16_f32 v19, v22, v23\n" \
"v_mfma_f32_16x16x32_bf16 a[120:123], a[240:243], v[72:75], a[120:123]\n" \
"v_exp_f32 v25, v25\n" \
"v_mfma_f32_16x16x32_bf16 a[120:123], a[244:247], v[104:107], a[120:123]\n" \
"ds_read_b128 a[236:239], v252 offset:14336\n" \
"v_add_f32 v245, v245, v25\n" \
"v_mfma_f32_16x16x32_bf16 v[152:155], a[240:243], v[80:83], v[152:155]\n" \
"v_exp_f32 v26, v26\n" \
"v_mfma_f32_16x16x32_bf16 v[152:155], a[244:247], v[112:115], v[152:155]\n" \
"v_add_f32 v245, v245, v26\n" \
"v_cvt_pk_bf16_f32 v24, v24, v25\n" \
"v_mfma_f32_16x16x32_bf16 v[184:187], a[240:243], v[88:91], v[184:187]\n" \
"v_exp_f32 v27, v27\n" \
"v_mfma_f32_16x16x32_bf16 v[184:187], a[244:247], v[120:123], v[184:187]\n" \
"ds_read_b128 a[240:243], v252 offset:8448\n" \
"v_add_f32 v245, v245, v27\n" \
"v_mfma_f32_16x16x32_bf16 v[188:191], a[248:251], v[88:91], v[188:191]\n" \
"v_exp_f32 v28, v28\n" \
"v_mfma_f32_16x16x32_bf16 v[188:191], a[252:255], v[120:123], v[188:191]\n" \
"v_add_f32 v245, v245, v28\n" \
"v_cvt_pk_bf16_f32 v25, v26, v27\n" \
"v_mfma_f32_16x16x32_bf16 v[156:159], a[248:251], v[80:83], v[156:159]\n" \
"v_exp_f32 v29, v29\n" \
"v_mfma_f32_16x16x32_bf16 v[156:159], a[252:255], v[112:115], v[156:159]\n" \
"ds_read_b128 a[244:247], v252 offset:10496\n" \
"v_add_f32 v245, v245, v29\n" \
"v_mfma_f32_16x16x32_bf16 a[124:127], a[248:251], v[72:75], a[124:127]\n" \
"v_exp_f32 v30, v30\n" \
"v_mfma_f32_16x16x32_bf16 a[124:127], a[252:255], v[104:107], a[124:127]\n" \
"v_add_f32 v245, v245, v30\n" \
"v_cvt_pk_bf16_f32 v26, v28, v29\n" \
"v_mfma_f32_16x16x32_bf16 a[92:95], a[248:251], v[64:67], a[92:95]\n" \
"s_waitcnt vmcnt(8) lgkmcnt(6)\n" \
"v_exp_f32 v31, v31\n" \
"v_mfma_f32_16x16x32_bf16 a[92:95], a[252:255], v[96:99], a[92:95]\n" \
"s_barrier\n" \
"ds_read_b128 a[248:251], v252 offset:12544\n" \
"v_add_f32 v245, v245, v31\n" \
"LA1g_%=:\n" \
"Lpg7550_%=:\n" \
"s_cbranch_execz Lcb0_%=\n" \
"v_mfma_f32_16x16x32_bf16 v[64:67], a[192:195], v[192:195], v[224:227]\n" \
"v_exp_f32 v32, v32\n" \
"v_mfma_f32_16x16x32_bf16 v[64:67], a[196:199], v[196:199], v[64:67]\n" \
"v_cvt_pk_bf16_f32 v27, v30, v31\n" \
"v_add_f32 v240, v240, v32\n" \
"v_mfma_f32_16x16x32_bf16 v[64:67], a[200:203], v[200:203], v[64:67]\n" \
"s_add_u32 m0, s74, 0x8000\n" \
"v_exp_f32 v33, v33\n" \
"v_mfma_f32_16x16x32_bf16 v[64:67], a[204:207], v[204:207], v[64:67]\n" \
"ds_read_b128 a[252:255], v252 offset:14592\n" \
"v_add_f32 v240, v240, v33\n" \
"v_mfma_f32_16x16x32_bf16 v[72:75], a[200:203], v[216:219], v[228:231]\n" \
"global_load_lds_dwordx4 v250, s[38:39] offset:-4096 sc1\n" \
"global_load_lds_dwordx4 v250, s[38:39] offset:-3072 sc1\n" \
"v_mfma_f32_16x16x32_bf16 v[72:75], a[204:207], v[220:223], v[72:75]\n" \
"v_exp_f32 v34, v34\n" \
"v_mfma_f32_16x16x32_bf16 v[72:75], a[192:195], v[208:211], v[72:75]\n" \
"v_add_f32 v240, v240, v34\n" \
"v_cvt_pk_bf16_f32 v32, v32, v33\n" \
"v_mfma_f32_16x16x32_bf16 v[72:75], a[196:199], v[212:215], v[72:75]\n" \
"v_exp_f32 v35, v35\n" \
"v_mfma_f32_16x16x32_bf16 v[80:83], a[192:195], a[160:163], v[232:235]\n" \
"v_add_f32 v240, v240, v35\n" \
"v_exp_f32 v36, v36\n" \
"v_mfma_f32_16x16x32_bf16 v[80:83], a[196:199], a[164:167], v[80:83]\n" \
"v_add_f32 v240, v240, v36\n" \
"v_cvt_pk_bf16_f32 v33, v34, v35\n" \
"v_mfma_f32_16x16x32_bf16 v[80:83], a[200:203], a[168:171], v[80:83]\n" \
"v_exp_f32 v37, v37\n" \
"v_mfma_f32_16x16x32_bf16 v[80:83], a[204:207], a[172:175], v[80:83]\n" \
"v_add_f32 v240, v240, v37\n" \
"v_exp_f32 v38, v38\n" \
"v_mfma_f32_16x16x32_bf16 v[88:91], a[200:203], a[184:187], v[236:239]\n" \
"v_add_f32 v240, v240, v38\n" \
"v_cvt_pk_bf16_f32 v34, v36, v37\n" \
"v_mfma_f32_16x16x32_bf16 v[88:91], a[204:207], a[188:191], v[88:91]\n" \
"v_exp_f32 v39, v39\n" \
"v_mfma_f32_16x16x32_bf16 v[88:91], a[192:195], a[176:179], v[88:91]\n" \
"v_add_f32 v240, v240, v39\n" \
"v_mfma_f32_16x16x32_bf16 v[88:91], a[196:199], a[180:183], v[88:91]\n" \
"ds_read_b128 a[192:195], v247 offset:0\n" \
"global_load_lds_dwordx4 v250, s[38:39] offset:-2048 sc1\n" \
"v_mfma_f32_16x16x32_bf16 v[92:95], a[208:211], a[176:179], v[236:239]\n" \
"v_exp_f32 v40, v40\n" \
"v_mfma_f32_16x16x32_bf16 v[92:95], a[212:215], a[180:183], v[92:95]\n" \
"v_add_f32 v241, v241, v40\n" \
"v_cvt_pk_bf16_f32 v35, v38, v39\n" \
"v_mfma_f32_16x16x32_bf16 v[92:95], a[216:219], a[184:187], v[92:95]\n" \
"v_exp_f32 v41, v41\n" \
"v_mfma_f32_16x16x32_bf16 v[92:95], a[220:223], a[188:191], v[92:95]\n" \
"ds_read_b128 a[196:199], v247 offset:8192\n" \
"global_load_lds_dwordx4 v250, s[38:39] offset:-1024 sc1\n" \
"v_add_f32 v241, v241, v41\n" \
"v_mfma_f32_16x16x32_bf16 v[84:87], a[216:219], a[168:171], v[232:235]\n" \
"v_exp_f32 v42, v42\n" \
"v_mfma_f32_16x16x32_bf16 v[84:87], a[220:223], a[172:175], v[84:87]\n" \
"v_add_f32 v241, v241, v42\n" \
"v_cvt_pk_bf16_f32 v40, v40, v41\n" \
"v_mfma_f32_16x16x32_bf16 v[84:87], a[208:211], a[160:163], v[84:87]\n" \
"v_exp_f32 v43, v43\n" \
"v_mfma_f32_16x16x32_bf16 v[84:87], a[212:215], a[164:167], v[84:87]\n" \
"ds_read_b128 a[200:203], v247 offset:1024\n" \
"v_add_f32 v241, v241, v43\n" \
"v_mfma_f32_16x16x32_bf16 v[76:79], a[208:211], v[208:211], v[228:231]\n" \
"v_exp_f32 v44, v44\n" \
"v_mfma_f32_16x16x32_bf16 v[76:79], a[212:215], v[212:215], v[76:79]\n" \
"v_add_f32 v241, v241, v44\n" \
"v_cvt_pk_bf16_f32 v41, v42, v43\n" \
"v_mfma_f32_16x16x32_bf16 v[76:79], a[216:219], v[216:219], v[76:79]\n" \
"v_exp_f32 v45, v45\n" \
"v_mfma_f32_16x16x32_bf16 v[76:79], a[220:223], v[220:223], v[76:79]\n" \
"ds_read_b128 a[204:207], v247 offset:9216\n" \
"global_load_lds_dwordx4 v250, s[38:39] offset:0 sc1\n" \
"v_add_f32 v241, v241, v45\n" \
"v_mfma_f32_16x16x32_bf16 v[68:71], a[216:219], v[200:203], v[224:227]\n" \
"v_exp_f32 v46, v46\n" \
"v_mfma_f32_16x16x32_bf16 v[68:71], a[220:223], v[204:207], v[68:71]\n" \
"v_add_f32 v241, v241, v46\n" \
"v_cvt_pk_bf16_f32 v42, v44, v45\n" \
"v_mfma_f32_16x16x32_bf16 v[68:71], a[208:211], v[192:195], v[68:71]\n" \
"s_waitcnt lgkmcnt(4)\n" \
"v_exp_f32 v47, v47\n" \
"v_mfma_f32_16x16x32_bf16 v[68:71], a[212:215], v[196:199], v[68:71]\n" \
"ds_read_b128 a[208:211], v247 offset:2048\n" \
"global_load_lds_dwordx4 v250, s[38:39] offset:1024 sc1\n" \
"v_add_f32 v241, v241, v47\n" \
"v_mfma_f32_16x16x32_bf16 v[96:99], a[224:227], v[192:195], v[224:227]\n" \
"v_exp_f32 v48, v48\n" \
"v_mfma_f32_16x16x32_bf16 v[96:99], a[228:231], v[196:199], v[96:99]\n" \
"v_add_f32 v244, v244, v48\n" \
"v_cvt_pk_bf16_f32 v43, v46, v47\n" \
"v_mfma_f32_16x16x32_bf16 v[96:99], a[232:235], v[200:203], v[96:99]\n" \
"v_exp_f32 v49, v49\n" \
"v_mfma_f32_16x16x32_bf16 v[96:99], a[236:239], v[204:207], v[96:99]\n" \
"ds_read_b128 a[212:215], v247 offset:10240\n" \
"v_add_f32 v244, v244, v49\n" \
"v_mfma_f32_16x16x32_bf16 v[104:107], a[232:235], v[216:219], v[228:231]\n" \
"v_exp_f32 v50, v50\n" \
"v_mfma_f32_16x16x32_bf16 v[104:107], a[236:239], v[220:223], v[104:107]\n" \
"v_add_f32 v244, v244, v50\n" \
"v_cvt_pk_bf16_f32 v48, v48, v49\n" \
"v_mfma_f32_16x16x32_bf16 v[104:107], a[224:227], v[208:211], v[104:107]\n" \
"v_exp_f32 v51, v51\n" \
"v_mfma_f32_16x16x32_bf16 v[104:107], a[228:231], v[212:215], v[104:107]\n" \
"ds_read_b128 a[216:219], v247 offset:3072\n" \
"global_load_lds_dwordx4 v250, s[38:39] offset:2048 sc1\n" \
"v_add_f32 v244, v244, v51\n" \
"v_mfma_f32_16x16x32_bf16 v[112:115], a[224:227], a[160:163], v[232:235]\n" \
"v_exp_f32 v52, v52\n" \
"v_mfma_f32_16x16x32_bf16 v[112:115], a[228:231], a[164:167], v[112:115]\n" \
"v_add_f32 v244, v244, v52\n" \
"v_cvt_pk_bf16_f32 v49, v50, v51\n" \
"v_mfma_f32_16x16x32_bf16 v[112:115], a[232:235], a[168:171], v[112:115]\n" \
"v_exp_f32 v53, v53\n" \
"v_mfma_f32_16x16x32_bf16 v[112:115], a[236:239], a[172:175], v[112:115]\n" \
"ds_read_b128 a[220:223], v247 offset:11264\n" \
"global_load_lds_dwordx4 v250, s[38:39] offset:3072 sc1\n" \
"v_add_f32 v244, v244, v53\n" \
"v_mfma_f32_16x16x32_bf16 v[120:123], a[232:235], a[184:187], v[236:239]\n" \
"s_add_u32 s38, s38, 34816\n" \
"v_exp_f32 v54, v54\n" \
"v_mfma_f32_16x16x32_bf16 v[120:123], a[236:239], a[188:191], v[120:123]\n" \
"s_sub_u32 s76, s76, 1\n" \
"v_add_f32 v244, v244, v54\n" \
"v_cvt_pk_bf16_f32 v50, v52, v53\n" \
"v_mfma_f32_16x16x32_bf16 v[120:123], a[224:227], a[176:179], v[120:123]\n" \
"s_cmp_lg_u32 s76, 0\n" \
"v_exp_f32 v55, v55\n" \
"v_mfma_f32_16x16x32_bf16 v[120:123], a[228:231], a[180:183], v[120:123]\n" \
"ds_read_b128 a[224:227], v247 offset:4096\n" \
"v_add_f32 v244, v244, v55\n" \
"v_mfma_f32_16x16x32_bf16 v[124:127], a[240:243], a[176:179], v[236:239]\n" \
"v_exp_f32 v56, v56\n" \
"v_mfma_f32_16x16x32_bf16 v[124:127], a[244:247], a[180:183], v[124:127]\n" \
"v_add_f32 v245, v245, v56\n" \
"v_cvt_pk_bf16_f32 v51, v54, v55\n" \
"v_mfma_f32_16x16x32_bf16 v[124:127], a[248:251], a[184:187], v[124:127]\n" \
"v_exp_f32 v57, v57\n" \
"v_mfma_f32_16x16x32_bf16 v[124:127], a[252:255], a[188:191], v[124:127]\n" \
"ds_read_b128 a[228:231], v247 offset:12288\n" \
"v_add_f32 v245, v245, v57\n" \
"v_mfma_f32_16x16x32_bf16 v[116:119], a[248:251], a[168:171], v[232:235]\n" \
"v_exp_f32 v58, v58\n" \
"v_mfma_f32_16x16x32_bf16 v[116:119], a[252:255], a[172:175], v[116:119]\n" \
"v_add_f32 v245, v245, v58\n" \
"v_cvt_pk_bf16_f32 v56, v56, v57\n" \
"v_mfma_f32_16x16x32_bf16 v[116:119], a[240:243], a[160:163], v[116:119]\n" \
"v_exp_f32 v59, v59\n" \
"v_mfma_f32_16x16x32_bf16 v[116:119], a[244:247], a[164:167], v[116:119]\n" \
"ds_read_b128 a[232:235], v247 offset:5120\n" \
"v_add_f32 v245, v245, v59\n" \
"v_mfma_f32_16x16x32_bf16 v[108:111], a[240:243], v[208:211], v[228:231]\n" \
"v_exp_f32 v60, v60\n" \
"v_mfma_f32_16x16x32_bf16 v[108:111], a[244:247], v[212:215], v[108:111]\n" \
"v_add_f32 v245, v245, v60\n" \
"v_cvt_pk_bf16_f32 v57, v58, v59\n" \
"v_mfma_f32_16x16x32_bf16 v[108:111], a[248:251], v[216:219], v[108:111]\n" \
"v_exp_f32 v61, v61\n" \
"v_mfma_f32_16x16x32_bf16 v[108:111], a[252:255], v[220:223], v[108:111]\n" \
"ds_read_b128 a[236:239], v247 offset:13312\n" \
"v_add_f32 v245, v245, v61\n" \
"v_mfma_f32_16x16x32_bf16 v[100:103], a[248:251], v[200:203], v[224:227]\n" \
"v_exp_f32 v62, v62\n" \
"v_mfma_f32_16x16x32_bf16 v[100:103], a[252:255], v[204:207], v[100:103]\n" \
"v_add_f32 v245, v245, v62\n" \
"v_cvt_pk_bf16_f32 v58, v60, v61\n" \
"v_mfma_f32_16x16x32_bf16 v[100:103], a[240:243], v[192:195], v[100:103]\n" \
"s_waitcnt vmcnt(8) lgkmcnt(4)\n" \
"v_exp_f32 v63, v63\n" \
"v_mfma_f32_16x16x32_bf16 v[100:103], a[244:247], v[196:199], v[100:103]\n" \
"s_barrier\n" \
"ds_read_b128 a[240:243], v247 offset:6144\n" \
"v_add_f32 v245, v245, v63\n" \
"s_cbranch_scc1 Lloop2_%=\n" \
"v_mfma_f32_16x16x32_bf16 a[64:67], a[192:195], v[0:3], a[64:67]\n" \
"v_subrev_u32 v246, s82, v246\n" \
"s_add_u32 s70, s63, 2\n" \
"v_mfma_f32_16x16x32_bf16 a[64:67], a[196:199], v[32:35], a[64:67]\n" \
"s_memrealtime s[52:53]\n" \
"v_cvt_pk_bf16_f32 v59, v62, v63\n" \
"v_exp_f32 v64, v64\n" \
"v_mfma_f32_16x16x32_bf16 a[96:99], a[192:195], v[8:11], a[96:99]\n" \
"s_and_b32 s88, s70, 3\n" \
"ds_read_b128 a[244:247], v247 offset:14336\n" \
"v_mov_b32 v216, v243\n" \
"v_mfma_f32_16x16x32_bf16 a[96:99], a[196:199], v[40:43], a[96:99]\n" \
"s_lshr_b32 s71, %1, 6\n" \
"ds_read_b128 a[248:251], v247 offset:7168\n" \
"v_mov_b32 v214, v216\n" \
"v_mfma_f32_16x16x32_bf16 v[128:131], a[192:195], v[16:19], v[128:131]\n" \
"ds_read_b128 a[252:255], v247 offset:15360\n" \
"s_lshl_b32 s71, s71, 2\n" \
"v_mfma_f32_16x16x32_bf16 v[128:131], a[196:199], v[48:51], v[128:131]\n" \
"s_add_u32 s88, s88, s71\n" \
"v_exp_f32 v65, v65\n" \
"v_permlane16_swap_b32 v214, v216\n" \
"v_add_f32 v216, v216, v214\n" \
"v_mov_b32 v214, v216\n" \
"v_mfma_f32_16x16x32_bf16 v[160:163], a[192:195], v[24:27], v[160:163]\n" \
"s_lshr_b32 s70, s70, 2\n" \
"v_exp_f32 v66, v66\n" \
"v_mfma_f32_16x16x32_bf16 v[160:163], a[196:199], v[56:59], v[160:163]\n" \
"s_lshr_b32 s71, s22, 7\n" \
"ds_read_b128 a[192:195], v246 offset:0\n" \
"v_cvt_pk_bf16_f32 v64, v64, v65\n" \
"v_permlane32_swap_b32 v214, v216\n" \
"v_mfma_f32_16x16x32_bf16 v[164:167], a[200:203], v[24:27], v[164:167]\n" \
"s_xor_b32 s71, s71, s70\n" \
"v_exp_f32 v67, v67\n" \
"v_mfma_f32_16x16x32_bf16 v[164:167], a[204:207], v[56:59], v[164:167]\n" \
"s_waitcnt lgkmcnt(0)\n" \
"v_exp_f32 v68, v68\n" \
"v_cvt_pk_bf16_f32 v65, v66, v67\n" \
"v_mfma_f32_16x16x32_bf16 v[132:135], a[200:203], v[16:19], v[132:135]\n" \
"s_barrier\n" \
"v_mfma_f32_16x16x32_bf16 v[132:135], a[204:207], v[48:51], v[132:135]\n" \
"s_and_b32 s54, %1, 7\n" \
"ds_read_b128 a[196:199], v246 offset:2048\n" \
"s_mov_b32 m0, s74\n" \
"v_mfma_f32_16x16x32_bf16 a[100:103], a[200:203], v[8:11], a[100:103]\n" \
"s_lshl_b32 s54, s54, 2\n" \
"v_exp_f32 v69, v69\n" \
"v_mfma_f32_16x16x32_bf16 a[100:103], a[204:207], v[40:43], a[100:103]\n" \
"s_lshr_b32 s55, %1, 6\n" \
"v_exp_f32 v70, v70\n" \
"v_mfma_f32_16x16x32_bf16 a[68:71], a[200:203], v[0:3], a[68:71]\n" \
"s_add_u32 s54, s54, s55\n" \
"v_cvt_pk_bf16_f32 v66, v68, v69\n" \
"v_add_f32 v216, v216, v214\n" \
"v_mfma_f32_16x16x32_bf16 a[68:71], a[204:207], v[32:35], a[68:71]\n" \
"s_lshl_b32 s54, s54, 3\n" \
"ds_read_b128 a[200:203], v246 offset:4096\n" \
"global_load_lds_dwordx4 v250, s[38:39] offset:-4096 sc1\n" \
"v_rcp_f32 v214, v216\n" \
"v_mfma_f32_16x16x32_bf16 a[72:75], a[208:211], v[0:3], a[72:75]\n" \
"s_add_u32 s54, s54, 65593856\n" \
"v_exp_f32 v71, v71\n" \
"v_mfma_f32_16x16x32_bf16 a[72:75], a[212:215], v[32:35], a[72:75]\n" \
"s_sub_u32 s53, s52, s12\n" \
"v_exp_f32 v72, v72\n" \
"v_mfma_f32_16x16x32_bf16 a[104:107], a[208:211], v[8:11], a[104:107]\n" \
"s_mul_i32 s53, s53, 3\n" \
"v_cvt_pk_bf16_f32 v67, v70, v71\n" \
"v_mul_f32 v216, 0x4d800000, v216\n" \
"v_mfma_f32_16x16x32_bf16 a[104:107], a[212:215], v[40:43], a[104:107]\n" \
"s_lshl_b32 s52, s52, 1\n" \
"ds_read_b128 a[204:207], v246 offset:6144\n" \
"global_load_lds_dwordx4 v250, s[38:39] offset:-3072 sc1\n" \
"v_mfma_f32_16x16x32_bf16 v[136:139], a[208:211], v[16:19], v[136:139]\n" \
"s_add_u32 s52, s52, s53\n" \
"v_mov_b32 v211, s52\n" \
"v_xor_b32 v210, s60, v211\n" \
"v_mfma_f32_16x16x32_bf16 v[136:139], a[212:215], v[48:51], v[136:139]\n" \
"s_and_b32 s71, s71, 1\n" \
"v_exp_f32 v73, v73\n" \
"v_mfma_f32_16x16x32_bf16 v[168:171], a[208:211], v[24:27], v[168:171]\n" \
"s_and_b32 s85, %1, 7\n" \
"v_exp_f32 v74, v74\n" \
"v_mfma_f32_16x16x32_bf16 v[168:171], a[212:215], v[56:59], v[168:171]\n" \
"s_lshl_b32 s85, s85, 1\n" \
"ds_read_b128 a[208:211], v246 offset:256\n" \
"v_lshlrev_b32 v212, 28, s61\n" \
"v_mfma_f32_16x16x32_bf16 v[172:175], a[216:219], v[24:27], v[172:175]\n" \
"s_cmp_eq_u32 s63, 3\n" \
"s_cselect_b32 s54, s54, 0x20000000\n" \
"v_cvt_pk_bf16_f32 v72, v72, v73\n" \
"v_mfma_f32_16x16x32_bf16 v[172:175], a[220:223], v[56:59], v[172:175]\n" \
"s_add_u32 s85, s85, s71\n" \
"v_exp_f32 v75, v75\n" \
"v_mfma_f32_16x16x32_bf16 v[140:143], a[216:219], v[16:19], v[140:143]\n" \
"s_lshl_b32 s70, s85, 7\n" \
"v_exp_f32 v76, v76\n" \
"v_mfma_f32_16x16x32_bf16 v[140:143], a[220:223], v[48:51], v[140:143]\n" \
"s_mul_i32 s84, s61, 6144\n" \
"ds_read_b128 a[212:215], v246 offset:2304\n" \
"global_load_lds_dwordx4 v250, s[38:39] offset:-2048 sc1\n" \
"v_cvt_pk_bf16_f32 v73, v74, v75\n" \
"v_mfma_f32_16x16x32_bf16 a[108:111], a[216:219], v[8:11], a[108:111]\n" \
"s_lshl_b32 s71, s84, 4\n" \
"v_exp_f32 v77, v77\n" \
"v_mfma_f32_16x16x32_bf16 a[108:111], a[220:223], v[40:43], a[108:111]\n" \
"s_add_u32 s70, s70, s71\n" \
"v_exp_f32 v78, v78\n" \
"v_mfma_f32_16x16x32_bf16 a[76:79], a[216:219], v[0:3], a[76:79]\n" \
"s_waitcnt lgkmcnt(6)\n" \
"global_load_lds_dwordx4 v250, s[38:39] offset:-1024 sc1\n" \
"global_load_lds_dwordx4 v250, s[38:39] offset:0 sc1\n" \
"v_mfma_f32_16x16x32_bf16 a[76:79], a[220:223], v[32:35], a[76:79]\n" \
"s_add_u32 s70, s70, 4096\n" \
"ds_read_b128 a[216:219], v246 offset:4352\n" \
"global_load_lds_dwordx4 v250, s[38:39] offset:1024 sc1\n" \
"v_mfma_f32_16x16x32_bf16 a[80:83], a[224:227], v[0:3], a[80:83]\n" \
"s_add_u32 s48, s22, s70\n" \
"s_addc_u32 s49, s23, 0\n" \
"v_cvt_pk_bf16_f32 v74, v76, v77\n" \
"v_mfma_f32_16x16x32_bf16 a[80:83], a[228:231], v[32:35], a[80:83]\n" \
"s_add_u32 s50, s48, 23552\n" \
"s_addc_u32 s51, s49, 0\n" \
"v_add_u32 v215, 0x1c90080, v249\n" \
"v_mfma_f32_16x16x32_bf16 a[112:115], a[224:227], v[8:11], a[112:115]\n" \
"s_lshl_b32 s70, s88, 18\n" \
"v_exp_f32 v79, v79\n" \
"v_mfma_f32_16x16x32_bf16 a[112:115], a[228:231], v[40:43], a[112:115]\n" \
"s_lshl_b32 s71, s61, 16\n" \
"ds_read_b128 a[220:223], v246 offset:6400\n" \
"global_load_lds_dwordx4 v250, s[38:39] offset:2048 sc1\n" \
"v_mfma_f32_16x16x32_bf16 v[144:147], a[224:227], v[16:19], v[144:147]\n" \
"s_add_u32 s71, s71, s70\n" \
"v_exp_f32 v80, v80\n" \
"v_mfma_f32_16x16x32_bf16 v[144:147], a[228:231], v[48:51], v[144:147]\n" \
"s_add_u32 s42, s20, s71\n" \
"s_addc_u32 s43, s21, 0\n" \
"v_cvt_pk_bf16_f32 v75, v78, v79\n" \
"v_mfma_f32_16x16x32_bf16 v[176:179], a[224:227], v[24:27], v[176:179]\n" \
"s_add_u32 s44, s42, 31744\n" \
"s_addc_u32 s45, s43, 0\n" \
"v_cndmask_b32 v215, v249, v215, s[16:17]\n" \
"v_mfma_f32_16x16x32_bf16 v[176:179], a[228:231], v[56:59], v[176:179]\n" \
"s_add_u32 s77, s77, 532480\n" \
"ds_read_b128 a[224:227], v246 offset:8192\n" \
"global_load_lds_dwordx4 v250, s[38:39] offset:3072 sc1\n" \
"v_accvgpr_read_b32 v192, a32\n" \
"v_accvgpr_read_b32 v193, a33\n" \
"v_accvgpr_read_b32 v194, a34\n" \
"v_accvgpr_read_b32 v195, a35\n" \
"v_accvgpr_read_b32 v196, a36\n" \
"v_accvgpr_read_b32 v197, a37\n" \
"v_accvgpr_read_b32 v198, a38\n" \
"v_accvgpr_read_b32 v199, a39\n" \
"v_pk_mul_f32 v[192:193], v[192:193], v[214:215] op_sel_hi:[1,0]\n" \
"v_pk_mul_f32 v[194:195], v[194:195], v[214:215] op_sel_hi:[1,0]\n" \
"v_pk_mul_f32 v[196:197], v[196:197], v[214:215] op_sel_hi:[1,0]\n" \
"v_pk_mul_f32 v[198:199], v[198:199], v[214:215] op_sel_hi:[1,0]\n" \
"v_cvt_pk_bf16_f32 v200, v192, v193\n" \
"v_cvt_pk_bf16_f32 v201, v194, v195\n" \
"v_cvt_pk_bf16_f32 v202, v196, v197\n" \
"v_cvt_pk_bf16_f32 v203, v198, v199\n" \
"v_pk_mul_f32 v[192:193], v[192:193], v[194:195]\n" \
"v_pk_fma_f32 v[192:193], v[196:197], v[198:199], v[192:193]\n" \
"v_fma_f32 v216, v192, v193, v216\n" \
"v_mfma_f32_16x16x32_bf16 v[180:183], a[232:235], v[24:27], v[180:183]\n" \
"v_exp_f32 v81, v81\n" \
"v_mfma_f32_16x16x32_bf16 v[180:183], a[236:239], v[56:59], v[180:183]\n" \
"buffer_store_dwordx4 v[200:203], v215, s[56:59], s77 offen offset:0 sc1\n" \
"buffer_store_dwordx2 v[210:211], v212, s[56:59], s54 offen sc1\n" \
"v_accvgpr_read_b32 v192, a40\n" \
"v_accvgpr_read_b32 v193, a41\n" \
"v_accvgpr_read_b32 v194, a42\n" \
"v_accvgpr_read_b32 v195, a43\n" \
"v_accvgpr_read_b32 v196, a44\n" \
"v_accvgpr_read_b32 v197, a45\n" \
"v_accvgpr_read_b32 v198, a46\n" \
"v_accvgpr_read_b32 v199, a47\n" \
"v_pk_mul_f32 v[192:193], v[192:193], v[214:215] op_sel_hi:[1,0]\n" \
"v_pk_mul_f32 v[194:195], v[194:195], v[214:215] op_sel_hi:[1,0]\n" \
"v_pk_mul_f32 v[196:197], v[196:197], v[214:215] op_sel_hi:[1,0]\n" \
"v_pk_mul_f32 v[198:199], v[198:199], v[214:215] op_sel_hi:[1,0]\n" \
"v_cvt_pk_bf16_f32 v204, v192, v193\n" \
"v_cvt_pk_bf16_f32 v205, v194, v195\n" \
"v_cvt_pk_bf16_f32 v206, v196, v197\n" \
"v_cvt_pk_bf16_f32 v207, v198, v199\n" \
"v_pk_mul_f32 v[192:193], v[192:193], v[194:195]\n" \
"v_pk_fma_f32 v[192:193], v[196:197], v[198:199], v[192:193]\n" \
"v_mfma_f32_16x16x32_bf16 v[148:151], a[232:235], v[16:19], v[148:151]\n" \
"v_exp_f32 v82, v82\n" \
"v_mfma_f32_16x16x32_bf16 v[148:151], a[236:239], v[48:51], v[148:151]\n" \
"ds_read_b128 a[228:231], v246 offset:10240\n" \
"v_cvt_pk_bf16_f32 v80, v80, v81\n" \
"v_mfma_f32_16x16x32_bf16 v[238:241], a[0:3], v[64:67], v[238:241]\n" \
"v_exp_f32 v83, v83\n" \
"v_mfma_f32_16x16x32_bf16 v[238:241], a[4:7], v[72:75], v[238:241]\n" \
"v_exp_f32 v84, v84\n" \
"v_mfma_f32_16x16x32_bf16 a[116:119], a[232:235], v[8:11], a[116:119]\n" \
"v_cvt_pk_bf16_f32 v81, v82, v83\n" \
"v_fma_f32 v216, v192, v193, v216\n" \
"v_mfma_f32_16x16x32_bf16 a[116:119], a[236:239], v[40:43], a[116:119]\n" \
"buffer_store_dwordx4 v[204:207], v215, s[56:59], s77 offen offset:2048 sc1\n" \
"s_add_u32 s77, s77, 0x1000\n" \
"v_accvgpr_read_b32 v192, a48\n" \
"v_accvgpr_read_b32 v193, a49\n" \
"v_accvgpr_read_b32 v194, a50\n" \
"v_accvgpr_read_b32 v195, a51\n" \
"v_accvgpr_read_b32 v196, a52\n" \
"v_accvgpr_read_b32 v197, a53\n" \
"v_accvgpr_read_b32 v198, a54\n" \
"v_accvgpr_read_b32 v199, a55\n" \
"v_exp_f32 v85, v85\n" \
"v_pk_mul_f32 v[192:193], v[192:193], v[214:215] op_sel_hi:[1,0]\n" \
"v_pk_mul_f32 v[194:195], v[194:195], v[214:215] op_sel_hi:[1,0]\n" \
"v_pk_mul_f32 v[196:197], v[196:197], v[214:215] op_sel_hi:[1,0]\n" \
"v_pk_mul_f32 v[198:199], v[198:199], v[214:215] op_sel_hi:[1,0]\n" \
"v_cvt_pk_bf16_f32 v200, v192, v193\n" \
"v_cvt_pk_bf16_f32 v201, v194, v195\n" \
"v_cvt_pk_bf16_f32 v202, v196, v197\n" \
"v_cvt_pk_bf16_f32 v203, v198, v199\n" \
"v_pk_mul_f32 v[192:193], v[192:193], v[194:195]\n" \
"v_pk_fma_f32 v[192:193], v[196:197], v[198:199], v[192:193]\n" \
"v_mfma_f32_16x16x32_bf16 a[84:87], a[232:235], v[0:3], a[84:87]\n" \
"v_exp_f32 v86, v86\n" \
"v_mfma_f32_16x16x32_bf16 a[84:87], a[236:239], v[32:35], a[84:87]\n" \
"ds_read_b128 a[232:235], v246 offset:12288\n" \
"v_cvt_pk_bf16_f32 v82, v84, v85\n" \
"v_fma_f32 v216, v192, v193, v216\n" \
"v_mfma_f32_16x16x32_bf16 a[88:91], a[240:243], v[0:3], a[88:91]\n" \
"v_exp_f32 v87, v87\n" \
"v_mfma_f32_16x16x32_bf16 a[88:91], a[244:247], v[32:35], a[88:91]\n" \
"buffer_store_dwordx4 v[200:203], v215, s[56:59], s77 offen offset:0 sc1\n" \
"v_exp_f32 v88, v88\n" \
"v_cvt_pk_bf16_f32 v83, v86, v87\n" \
"v_accvgpr_read_b32 v192, a56\n" \
"v_mfma_f32_16x16x32_bf16 a[120:123], a[240:243], v[8:11], a[120:123]\n" \
"v_exp_f32 v89, v89\n" \
"v_mfma_f32_16x16x32_bf16 a[120:123], a[244:247], v[40:43], a[120:123]\n" \
"ds_read_b128 a[236:239], v246 offset:14336\n" \
"v_accvgpr_read_b32 v193, a57\n" \
"v_mfma_f32_16x16x32_bf16 v[152:155], a[240:243], v[16:19], v[152:155]\n" \
"v_exp_f32 v90, v90\n" \
"v_mfma_f32_16x16x32_bf16 v[152:155], a[244:247], v[48:51], v[152:155]\n" \
"v_accvgpr_read_b32 v194, a58\n" \
"v_cvt_pk_bf16_f32 v88, v88, v89\n" \
"v_mfma_f32_16x16x32_bf16 v[242:245], a[0:3], v[80:83], v[242:245]\n" \
"v_exp_f32 v91, v91\n" \
"v_mfma_f32_16x16x32_bf16 v[184:187], a[240:243], v[24:27], v[184:187]\n" \
"v_exp_f32 v92, v92\n" \
"v_mfma_f32_16x16x32_bf16 v[184:187], a[244:247], v[56:59], v[184:187]\n" \
"ds_read_b128 a[240:243], v246 offset:8448\n" \
"v_cvt_pk_bf16_f32 v89, v90, v91\n" \
"v_mfma_f32_16x16x32_bf16 v[188:191], a[248:251], v[24:27], v[188:191]\n" \
"v_exp_f32 v93, v93\n" \
"v_mfma_f32_16x16x32_bf16 v[188:191], a[252:255], v[56:59], v[188:191]\n" \
"v_exp_f32 v94, v94\n" \
"v_mfma_f32_16x16x32_bf16 v[156:159], a[248:251], v[16:19], v[156:159]\n" \
"v_cvt_pk_bf16_f32 v90, v92, v93\n" \
"v_accvgpr_read_b32 v195, a59\n" \
"v_mfma_f32_16x16x32_bf16 v[156:159], a[252:255], v[48:51], v[156:159]\n" \
"ds_read_b128 a[244:247], v246 offset:10496\n" \
"v_accvgpr_read_b32 v196, a60\n" \
"v_accvgpr_read_b32 v197, a61\n" \
"v_accvgpr_read_b32 v198, a62\n" \
"v_accvgpr_read_b32 v199, a63\n" \
"v_pk_mul_f32 v[192:193], v[192:193], v[214:215] op_sel_hi:[1,0]\n" \
"v_pk_mul_f32 v[194:195], v[194:195], v[214:215] op_sel_hi:[1,0]\n" \
"v_pk_mul_f32 v[196:197], v[196:197], v[214:215] op_sel_hi:[1,0]\n" \
"v_pk_mul_f32 v[198:199], v[198:199], v[214:215] op_sel_hi:[1,0]\n" \
"v_cvt_pk_bf16_f32 v204, v192, v193\n" \
"v_cvt_pk_bf16_f32 v205, v194, v195\n" \
"v_cvt_pk_bf16_f32 v206, v196, v197\n" \
"v_cvt_pk_bf16_f32 v207, v198, v199\n" \
"v_pk_mul_f32 v[192:193], v[192:193], v[194:195]\n" \
"v_pk_fma_f32 v[192:193], v[196:197], v[198:199], v[192:193]\n" \
"v_fma_f32 v216, v192, v193, v216\n" \
"v_mfma_f32_16x16x32_bf16 a[124:127], a[248:251], v[8:11], a[124:127]\n" \
"buffer_store_dwordx4 v[204:207], v215, s[56:59], s77 offen offset:2048 sc1\n" \
"v_exp_f32 v95, v95\n" \
"v_cmp_class_f32 vcc, v216, s94\n" \
"s_sub_u32 s77, s77, 536576\n" \
"v_mfma_f32_16x16x32_bf16 a[124:127], a[252:255], v[40:43], a[124:127]\n" \
"v_cvt_pk_bf16_f32 v91, v94, v95\n" \
"s_or_b64 s[92:93], s[92:93], vcc\n" \
"v_mfma_f32_16x16x32_bf16 a[92:95], a[248:251], v[0:3], a[92:95]\n" \
"s_waitcnt vmcnt(13) lgkmcnt(6)\n" \
"v_mfma_f32_16x16x32_bf16 v[242:245], a[4:7], v[88:91], v[242:245]\n" \
"v_mfma_f32_16x16x32_bf16 a[92:95], a[252:255], v[32:35], a[92:95]\n" \
"s_barrier\n" \
"ds_read_b128 a[248:251], v246 offset:12544\n" \
"v_mfma_f32_16x16x32_bf16 v[24:27], a[192:195], a[176:179], v[236:239]\n" \
"ds_read_b128 v[192:195], v246 offset:16384\n" \
"Lpg8018_%=:\n" \
"Lst1x1_%=:\n" \
"s_cbranch_execz Lpg10312_%=\n" \
"v_bfe_u32 v69, v248, 8, 1\n" \
"v_mfma_f32_16x16x32_bf16 v[24:27], a[196:199], a[180:183], v[24:27]\n" \
"ds_read_b128 v[196:199], v246 offset:18432\n" \
"v_cvt_pk_bf16_f32 v91, v94, v95\n" \
"v_mfma_f32_16x16x32_bf16 v[24:27], a[200:203], a[184:187], v[24:27]\n" \
"ds_read_b128 v[200:203], v246 offset:20480\n" \
"v_bfe_u32 v70, v248, 6, 2\n" \
"v_mfma_f32_16x16x32_bf16 v[24:27], a[204:207], a[188:191], v[24:27]\n" \
"ds_read_b128 a[252:255], v246 offset:14592\n" \
"v_lshl_add_u32 v70, v69, 3, v70\n" \
"v_exp_f32 v96, v96\n" \
"v_exp_f32 v97, v97\n" \
"v_exp_f32 v98, v98\n" \
"v_cvt_pk_bf16_f32 v96, v96, v97\n" \
"v_exp_f32 v99, v99\n" \
"v_exp_f32 v100, v100\n" \
"v_mfma_f32_16x16x32_bf16 v[16:19], a[200:203], a[168:171], v[232:235]\n" \
"ds_read_b128 v[204:207], v246 offset:22528\n" \
"v_mul_u32_u24 v70, 6144, v70\n" \
"v_mfma_f32_16x16x32_bf16 v[16:19], a[204:207], a[172:175], v[16:19]\n" \
"ds_read_b128 v[208:211], v246 offset:16640\n" \
"v_lshlrev_b32 v69, 5, v69\n" \
"v_mfma_f32_16x16x32_bf16 v[16:19], a[192:195], a[160:163], v[16:19]\n" \
"ds_read_b128 v[212:215], v246 offset:18688\n" \
"v_and_b32 v71, 0x30, v248\n" \
"v_mfma_f32_16x16x32_bf16 v[16:19], a[196:199], a[164:167], v[16:19]\n" \
"ds_read_b128 v[216:219], v246 offset:20736\n" \
"v_xor_b32 v69, v69, v71\n" \
"v_mfma_f32_16x16x32_bf16 v[20:23], a[208:211], a[160:163], v[232:235]\n" \
"ds_read_b128 v[220:223], v246 offset:22784\n" \
"v_lshrrev_b32 v71, 9, v248\n" \
"v_mfma_f32_16x16x32_bf16 v[20:23], a[212:215], a[164:167], v[20:23]\n" \
"ds_read_b128 a[192:195], v247 offset:32768\n" \
"v_lshl_add_u32 v70, v71, 6, v70\n" \
"v_mfma_f32_16x16x32_bf16 v[20:23], a[216:219], a[168:171], v[20:23]\n" \
"ds_read_b128 a[196:199], v247 offset:40960\n" \
"v_add_u32 v253, v69, v70\n" \
"v_mfma_f32_16x16x32_bf16 v[20:23], a[220:223], a[172:175], v[20:23]\n" \
"s_add_u32 m0, s84, 143360\n" \
"ds_read_b128 a[200:203], v247 offset:33792\n" \
"v_cvt_pk_bf16_f32 v97, v98, v99\n" \
"v_mfma_f32_16x16x32_bf16 v[28:31], a[216:219], a[184:187], v[236:239]\n" \
"ds_read_b128 a[204:207], v247 offset:41984\n" \
"global_load_lds_dwordx4 v253, s[48:49] offset:-4096\n" \
"v_mfma_f32_16x16x32_bf16 v[28:31], a[220:223], a[188:191], v[28:31]\n" \
"v_exp_f32 v101, v101\n" \
"v_mfma_f32_16x16x32_bf16 v[28:31], a[208:211], a[176:179], v[28:31]\n" \
"v_exp_f32 v102, v102\n" \
"v_mfma_f32_16x16x32_bf16 v[28:31], a[212:215], a[180:183], v[28:31]\n" \
"s_waitcnt lgkmcnt(2)\n" \
"ds_read_b128 a[208:211], v247 offset:34816\n" \
"global_load_lds_dwordx4 v253, s[48:49] offset:-2048\n" \
"v_mfma_f32_16x16x32_bf16 v[8:11], v[192:195], a[176:179], v[236:239]\n" \
"ds_read_b128 a[212:215], v247 offset:43008\n" \
"v_cvt_pk_bf16_f32 v98, v100, v101\n" \
"v_mfma_f32_16x16x32_bf16 v[8:11], v[196:199], a[180:183], v[8:11]\n" \
"ds_read_b128 a[216:219], v247 offset:35840\n" \
"v_exp_f32 v103, v103\n" \
"v_exp_f32 v104, v104\n" \
"v_mfma_f32_16x16x32_bf16 v[8:11], v[200:203], a[184:187], v[8:11]\n" \
"ds_read_b128 a[220:223], v247 offset:44032\n" \
"global_load_lds_dwordx4 v253, s[48:49] offset:0\n" \
"v_mfma_f32_16x16x32_bf16 v[8:11], v[204:207], a[188:191], v[8:11]\n" \
"s_add_u32 s48, s48, 0x120000\n" \
"s_addc_u32 s49, s49, 0\n" \
"ds_read_b128 v[192:195], v246 offset:24576\n" \
"v_mfma_f32_16x16x32_bf16 v[56:59], a[232:235], a[184:187], v[236:239]\n" \
"ds_read_b128 v[196:199], v246 offset:26624\n" \
"v_cvt_pk_bf16_f32 v99, v102, v103\n" \
"v_mfma_f32_16x16x32_bf16 v[56:59], a[236:239], a[188:191], v[56:59]\n" \
"ds_read_b128 v[200:203], v246 offset:28672\n" \
"global_load_lds_dwordx4 v253, s[50:51] offset:-3072\n" \
"v_mfma_f32_16x16x32_bf16 v[56:59], a[224:227], a[176:179], v[56:59]\n" \
"ds_read_b128 v[204:207], v246 offset:30720\n" \
"global_load_lds_dwordx4 v253, s[50:51] offset:-1024\n" \
"v_mfma_f32_16x16x32_bf16 v[56:59], a[228:231], a[180:183], v[56:59]\n" \
"v_exp_f32 v105, v105\n" \
"v_mfma_f32_16x16x32_bf16 v[48:51], a[224:227], a[160:163], v[232:235]\n" \
"v_exp_f32 v106, v106\n" \
"v_cvt_pk_bf16_f32 v104, v104, v105\n" \
"v_mfma_f32_16x16x32_bf16 v[48:51], a[228:231], a[164:167], v[48:51]\n" \
"v_exp_f32 v107, v107\n" \
"v_mfma_f32_16x16x32_bf16 v[48:51], a[232:235], a[168:171], v[48:51]\n" \
"v_exp_f32 v108, v108\n" \
"v_mfma_f32_16x16x32_bf16 v[48:51], a[236:239], a[172:175], v[48:51]\n" \
"ds_read_b128 a[224:227], v247 offset:36864\n" \
"global_load_lds_dwordx4 v253, s[50:51] offset:1024\n" \
"v_mfma_f32_16x16x32_bf16 v[12:15], v[216:219], a[184:187], v[236:239]\n" \
"s_add_u32 s50, s50, 0x120000\n" \
"s_addc_u32 s51, s51, 0\n" \
"ds_read_b128 a[228:231], v247 offset:45056\n" \
"v_mfma_f32_16x16x32_bf16 v[12:15], v[220:223], a[188:191], v[12:15]\n" \
"ds_read_b128 a[232:235], v247 offset:37888\n" \
"v_cvt_pk_bf16_f32 v105, v106, v107\n" \
"v_mfma_f32_16x16x32_bf16 v[12:15], v[208:211], a[176:179], v[12:15]\n" \
"ds_read_b128 a[236:239], v247 offset:46080\n" \
"v_exp_f32 v109, v109\n" \
"v_mfma_f32_16x16x32_bf16 v[12:15], v[212:215], a[180:183], v[12:15]\n" \
"ds_read_b128 v[208:211], v246 offset:24832\n" \
"v_exp_f32 v110, v110\n" \
"v_mfma_f32_16x16x32_bf16 v[60:63], a[240:243], a[176:179], v[236:239]\n" \
"ds_read_b128 v[212:215], v246 offset:26880\n" \
"v_cvt_pk_bf16_f32 v106, v108, v109\n" \
"v_mfma_f32_16x16x32_bf16 v[60:63], a[244:247], a[180:183], v[60:63]\n" \
"ds_read_b128 v[216:219], v246 offset:28928\n" \
"v_exp_f32 v111, v111\n" \
"v_exp_f32 v112, v112\n" \
"v_mfma_f32_16x16x32_bf16 v[60:63], a[248:251], a[184:187], v[60:63]\n" \
"ds_read_b128 v[220:223], v246 offset:30976\n" \
"v_cvt_pk_bf16_f32 v107, v110, v111\n" \
"v_mfma_f32_16x16x32_bf16 v[60:63], a[252:255], a[188:191], v[60:63]\n" \
"v_exp_f32 v113, v113\n" \
"v_exp_f32 v114, v114\n" \
"v_cvt_pk_bf16_f32 v112, v112, v113\n" \
"v_mfma_f32_16x16x32_bf16 v[52:55], a[248:251], a[168:171], v[232:235]\n" \
"v_exp_f32 v115, v115\n" \
"v_mfma_f32_16x16x32_bf16 v[52:55], a[252:255], a[172:175], v[52:55]\n" \
"v_exp_f32 v116, v116\n" \
"v_cvt_pk_bf16_f32 v113, v114, v115\n" \
"v_mfma_f32_16x16x32_bf16 v[52:55], a[240:243], a[160:163], v[52:55]\n" \
"v_exp_f32 v117, v117\n" \
"v_mfma_f32_16x16x32_bf16 v[52:55], a[244:247], a[164:167], v[52:55]\n" \
"s_waitcnt lgkmcnt(0)\n" \
"ds_read_b128 a[240:243], v247 offset:38912\n" \
"v_exp_f32 v118, v118\n" \
"v_mfma_f32_16x16x32_bf16 v[40:43], v[192:195], a[176:179], v[236:239]\n" \
"ds_read_b128 a[244:247], v247 offset:47104\n" \
"v_cvt_pk_bf16_f32 v114, v116, v117\n" \
"v_mfma_f32_16x16x32_bf16 v[40:43], v[196:199], a[180:183], v[40:43]\n" \
"ds_read_b128 a[248:251], v247 offset:39936\n" \
"v_exp_f32 v119, v119\n" \
"v_exp_f32 v120, v120\n" \
"v_mfma_f32_16x16x32_bf16 v[40:43], v[200:203], a[184:187], v[40:43]\n" \
"ds_read_b128 a[252:255], v247 offset:48128\n" \
"v_cvt_pk_bf16_f32 v115, v118, v119\n" \
"v_mfma_f32_16x16x32_bf16 v[40:43], v[204:207], a[188:191], v[40:43]\n" \
"v_exp_f32 v121, v121\n" \
"v_exp_f32 v122, v122\n" \
"v_cvt_pk_bf16_f32 v120, v120, v121\n" \
"v_mfma_f32_16x16x32_bf16 v[44:47], v[216:219], a[184:187], v[236:239]\n" \
"v_exp_f32 v123, v123\n" \
"v_mfma_f32_16x16x32_bf16 v[44:47], v[220:223], a[188:191], v[44:47]\n" \
"s_waitcnt vmcnt(11) lgkmcnt(0)\n" \
"v_exp_f32 v124, v124\n" \
"v_mfma_f32_16x16x32_bf16 v[44:47], v[208:211], a[176:179], v[44:47]\n" \
"v_add_u32 v247, 0x10000, v248\n" \
"v_cvt_pk_bf16_f32 v121, v122, v123\n" \
"v_mfma_f32_16x16x32_bf16 v[44:47], v[212:215], a[180:183], v[44:47]\n" \
"s_barrier\n" \
"v_exp_f32 v125, v125\n" \
"v_exp_f32 v126, v126\n" \
"v_cvt_pk_bf16_f32 v122, v124, v125\n" \
"v_mfma_f32_16x16x32_bf16 a[64:67], a[192:195], v[64:67], a[64:67]\n" \
"ds_read_b128 v[192:195], v247 offset:16384\n" \
"v_exp_f32 v127, v127\n" \
"v_mfma_f32_16x16x32_bf16 a[64:67], a[196:199], v[96:99], a[64:67]\n" \
"v_cvt_pk_bf16_f32 v123, v126, v127\n" \
"v_mfma_f32_16x16x32_bf16 a[96:99], a[192:195], v[72:75], a[96:99]\n" \
"s_lshl_b32 s70, s61, 14\n" \
"v_add_u32 v124, s70, v248\n" \
"v_xor_b32 v125, 0x4000, v124\n" \
"v_mfma_f32_16x16x32_bf16 a[96:99], a[196:199], v[104:107], a[96:99]\n" \
"s_lshl_b32 s70, s61, 9\n" \
"v_cmp_gt_i32 vcc, 0, v251\n" \
"v_cmp_gt_i32 s[64:65], 1, v251\n" \
"v_cmp_gt_i32 s[66:67], 2, v251\n" \
"v_cndmask_b32 v8, v8, v254, vcc\n" \
"v_mfma_f32_16x16x32_bf16 v[238:241], a[0:3], v[96:99], v[238:241]\n" \
"v_cndmask_b32 v16, v16, v254, vcc\n" \
"v_cmp_gt_i32 vcc, 3, v251\n" \
"v_mfma_f32_16x16x32_bf16 v[238:241], a[4:7], v[104:107], v[238:241]\n" \
"v_cndmask_b32 v9, v9, v254, s[64:65]\n" \
"v_cndmask_b32 v17, v17, v254, s[64:65]\n" \
"v_mfma_f32_16x16x32_bf16 v[128:131], a[192:195], v[80:83], v[128:131]\n" \
"s_add_u32 s70, s70, 110592\n" \
"ds_read_b128 v[196:199], v247 offset:24576\n" \
"v_lshrrev_b32 v126, 1, v248\n" \
"v_mfma_f32_16x16x32_bf16 v[128:131], a[196:199], v[112:115], v[128:131]\n" \
"v_add_u32 v126, s70, v126\n" \
"v_xor_b32 v127, 0x200, v126\n" \
"v_cmp_gt_i32 s[64:65], 16, v251\n" \
"v_cndmask_b32 v10, v10, v254, s[66:67]\n" \
"v_cndmask_b32 v18, v18, v254, s[66:67]\n" \
"v_cmp_gt_i32 s[66:67], 17, v251\n" \
"v_cndmask_b32 v11, v11, v254, vcc\n" \
"v_cndmask_b32 v19, v19, v254, vcc\n" \
"v_mfma_f32_16x16x32_bf16 v[160:163], a[192:195], v[88:91], v[160:163]\n" \
"v_lshrrev_b32 v71, 7, v248\n" \
"v_and_b32 v76, 0x70, v248\n" \
"v_mfma_f32_16x16x32_bf16 v[160:163], a[196:199], v[120:123], v[160:163]\n" \
"ds_read_b128 a[192:195], v247 offset:0\n" \
"v_lshlrev_b32 v77, 4, v71\n" \
"v_cmp_gt_i32 vcc, 18, v251\n" \
"v_cndmask_b32 v12, v12, v254, s[64:65]\n" \
"v_mfma_f32_16x16x32_bf16 v[242:245], a[0:3], v[112:115], v[242:245]\n" \
"v_cndmask_b32 v20, v20, v254, s[64:65]\n" \
"v_cmp_gt_i32 s[64:65], 19, v251\n" \
"v_mfma_f32_16x16x32_bf16 v[242:245], a[4:7], v[120:123], v[242:245]\n" \
"v_cndmask_b32 v13, v13, v254, s[66:67]\n" \
"v_cndmask_b32 v21, v21, v254, s[66:67]\n" \
"v_cmp_gt_i32 s[66:67], 32, v251\n" \
"v_cndmask_b32 v14, v14, v254, vcc\n" \
"v_mfma_f32_16x16x32_bf16 v[164:167], a[200:203], v[88:91], v[164:167]\n" \
"ds_read_b128 v[200:203], v247 offset:17408\n" \
"v_xor_b32 v77, v77, v76\n" \
"v_mfma_f32_16x16x32_bf16 v[164:167], a[204:207], v[120:123], v[164:167]\n" \
"ds_write_b128 v124, a[64:67] offset:0\n" \
"v_lshl_add_u32 v252, v71, 12, v77\n" \
"v_cndmask_b32 v22, v22, v254, vcc\n" \
"v_cmp_gt_i32 vcc, 33, v251\n" \
"v_cndmask_b32 v15, v15, v254, s[64:65]\n" \
"v_cndmask_b32 v23, v23, v254, s[64:65]\n" \
"v_mfma_f32_16x16x32_bf16 v[132:135], a[200:203], v[80:83], v[132:135]\n" \
"s_add_u32 m0, s95, 118784\n" \
"v_cmp_gt_i32 s[64:65], 34, v251\n" \
"v_cndmask_b32 v40, v40, v254, s[66:67]\n" \
"v_mfma_f32_16x16x32_bf16 v[132:135], a[204:207], v[112:115], v[132:135]\n" \
"ds_read_b128 a[196:199], v247 offset:8192\n" \
"global_load_lds_dwordx4 v252, s[42:43] offset:0\n" \
"v_mfma_f32_16x16x32_bf16 a[100:103], a[200:203], v[72:75], a[100:103]\n" \
"ds_read_b128 v[204:207], v247 offset:25600\n" \
"v_cndmask_b32 v48, v48, v254, s[66:67]\n" \
"v_mfma_f32_16x16x32_bf16 a[100:103], a[204:207], v[104:107], a[100:103]\n" \
"ds_write_b128 v124, a[96:99] offset:8192\n" \
"global_load_lds_dwordx4 v252, s[42:43] offset:2048\n" \
"v_mfma_f32_16x16x32_bf16 a[68:71], a[200:203], v[64:67], a[68:71]\n" \
"v_cmp_gt_i32 s[66:67], 35, v251\n" \
"v_cndmask_b32 v41, v41, v254, vcc\n" \
"v_mfma_f32_16x16x32_bf16 a[68:71], a[204:207], v[96:99], a[68:71]\n" \
"s_add_u32 s42, s42, 0x180\n" \
"s_addc_u32 s43, s43, 0\n" \
"ds_read_b128 a[200:203], v247 offset:1024\n" \
"v_mfma_f32_16x16x32_bf16 a[72:75], a[208:211], v[64:67], a[72:75]\n" \
"ds_read_b128 v[208:211], v247 offset:18432\n" \
"v_cndmask_b32 v49, v49, v254, vcc\n" \
"v_mfma_f32_16x16x32_bf16 a[72:75], a[212:215], v[96:99], a[72:75]\n" \
"ds_write_b128 v124, a[100:103] offset:9216\n" \
"global_load_lds_dwordx4 v252, s[44:45] offset:1024\n" \
"v_mfma_f32_16x16x32_bf16 a[104:107], a[208:211], v[72:75], a[104:107]\n" \
"v_cmp_gt_i32 vcc, 48, v251\n" \
"v_cndmask_b32 v42, v42, v254, s[64:65]\n" \
"v_mfma_f32_16x16x32_bf16 a[104:107], a[212:215], v[104:107], a[104:107]\n" \
"ds_read_b128 a[204:207], v247 offset:9216\n" \
"ds_write_b64 v126, v[240:241]\n" \
"v_mfma_f32_16x16x32_bf16 v[136:139], a[208:211], v[80:83], v[136:139]\n" \
"ds_read_b128 v[212:215], v247 offset:26624\n" \
"global_load_lds_dwordx4 v252, s[44:45] offset:3072\n" \
"v_mfma_f32_16x16x32_bf16 v[136:139], a[212:215], v[112:115], v[136:139]\n" \
"ds_write_b128 v124, a[68:71] offset:1024\n" \
"v_cndmask_b32 v50, v50, v254, s[64:65]\n" \
"v_mfma_f32_16x16x32_bf16 v[168:171], a[208:211], v[88:91], v[168:171]\n" \
"s_add_u32 s44, s44, 0x180\n" \
"s_addc_u32 s45, s45, 0\n" \
"v_cmp_gt_i32 s[64:65], 49, v251\n" \
"v_mfma_f32_16x16x32_bf16 v[168:171], a[212:215], v[120:123], v[168:171]\n" \
"ds_read_b128 a[208:211], v247 offset:2048\n" \
"v_cndmask_b32 v43, v43, v254, s[66:67]\n" \
"v_mfma_f32_16x16x32_bf16 v[172:175], a[216:219], v[88:91], v[172:175]\n" \
"ds_read_b128 v[216:219], v247 offset:19456\n" \
"v_cndmask_b32 v51, v51, v254, s[66:67]\n" \
"v_mfma_f32_16x16x32_bf16 v[172:175], a[220:223], v[120:123], v[172:175]\n" \
"ds_write_b128 v124, a[72:75] offset:2048\n" \
"v_cmp_gt_i32 s[66:67], 50, v251\n" \
"v_mfma_f32_16x16x32_bf16 v[140:143], a[216:219], v[80:83], v[140:143]\n" \
"v_cndmask_b32 v44, v44, v254, vcc\n" \
"v_cndmask_b32 v52, v52, v254, vcc\n" \
"v_mfma_f32_16x16x32_bf16 v[140:143], a[220:223], v[112:115], v[140:143]\n" \
"ds_read_b128 a[212:215], v247 offset:10240\n" \
"v_cmp_gt_i32 vcc, 51, v251\n" \
"v_mfma_f32_16x16x32_bf16 a[108:111], a[216:219], v[72:75], a[108:111]\n" \
"ds_read_b128 v[220:223], v247 offset:27648\n" \
"v_cndmask_b32 v45, v45, v254, s[64:65]\n" \
"v_mfma_f32_16x16x32_bf16 a[108:111], a[220:223], v[104:107], a[108:111]\n" \
"ds_write_b128 v124, a[104:107] offset:10240\n" \
"v_cndmask_b32 v53, v53, v254, s[64:65]\n" \
"v_mfma_f32_16x16x32_bf16 a[76:79], a[216:219], v[64:67], a[76:79]\n" \
"v_cndmask_b32 v46, v46, v254, s[66:67]\n" \
"v_cndmask_b32 v54, v54, v254, s[66:67]\n" \
"v_mfma_f32_16x16x32_bf16 a[76:79], a[220:223], v[96:99], a[76:79]\n" \
"ds_read_b128 a[216:219], v247 offset:3072\n" \
"v_cndmask_b32 v47, v47, v254, vcc\n" \
"v_mfma_f32_16x16x32_bf16 a[80:83], a[224:227], v[64:67], a[80:83]\n" \
"ds_read_b128 v[224:227], v247 offset:20480\n" \
"v_cndmask_b32 v55, v55, v254, vcc\n" \
"v_mfma_f32_16x16x32_bf16 a[80:83], a[228:231], v[96:99], a[80:83]\n" \
"ds_write_b128 v124, a[108:111] offset:11264\n" \
"v_exp_f32 v8, v8\n" \
"v_exp_f32 v9, v9\n" \
"v_exp_f32 v10, v10\n" \
"v_cvt_pk_bf16_f32 v8, v8, v9\n" \
"v_mfma_f32_16x16x32_bf16 a[112:115], a[224:227], v[72:75], a[112:115]\n" \
"v_exp_f32 v11, v11\n" \
"v_mfma_f32_16x16x32_bf16 a[112:115], a[228:231], v[104:107], a[112:115]\n" \
"ds_read_b128 a[220:223], v247 offset:11264\n" \
"v_exp_f32 v12, v12\n" \
"v_mfma_f32_16x16x32_bf16 v[144:147], a[224:227], v[80:83], v[144:147]\n" \
"ds_read_b128 v[228:231], v247 offset:28672\n" \
"v_cvt_pk_bf16_f32 v9, v10, v11\n" \
"v_mfma_f32_16x16x32_bf16 v[144:147], a[228:231], v[112:115], v[144:147]\n" \
"ds_write_b128 v124, a[76:79] offset:3072\n" \
"v_exp_f32 v13, v13\n" \
"v_exp_f32 v14, v14\n" \
"v_cvt_pk_bf16_f32 v10, v12, v13\n" \
"v_mfma_f32_16x16x32_bf16 v[176:179], a[224:227], v[88:91], v[176:179]\n" \
"v_exp_f32 v15, v15\n" \
"v_mfma_f32_16x16x32_bf16 v[176:179], a[228:231], v[120:123], v[176:179]\n" \
"ds_read_b128 a[224:227], v247 offset:4096\n" \
"v_exp_f32 v40, v40\n" \
"v_mfma_f32_16x16x32_bf16 v[180:183], a[232:235], v[88:91], v[180:183]\n" \
"ds_read_b128 v[232:235], v247 offset:21504\n" \
"v_cvt_pk_bf16_f32 v11, v14, v15\n" \
"v_mfma_f32_16x16x32_bf16 v[180:183], a[236:239], v[120:123], v[180:183]\n" \
"ds_write_b128 v124, a[80:83] offset:4096\n" \
"v_exp_f32 v41, v41\n" \
"v_exp_f32 v42, v42\n" \
"v_cvt_pk_bf16_f32 v40, v40, v41\n" \
"v_mfma_f32_16x16x32_bf16 v[148:151], a[232:235], v[80:83], v[148:151]\n" \
"v_exp_f32 v43, v43\n" \
"v_mfma_f32_16x16x32_bf16 v[148:151], a[236:239], v[112:115], v[148:151]\n" \
"ds_read_b128 a[228:231], v247 offset:12288\n" \
"v_exp_f32 v44, v44\n" \
"v_mfma_f32_16x16x32_bf16 a[116:119], a[232:235], v[72:75], a[116:119]\n" \
"ds_read_b128 v[236:239], v247 offset:29696\n" \
"v_cvt_pk_bf16_f32 v41, v42, v43\n" \
"v_mfma_f32_16x16x32_bf16 a[116:119], a[236:239], v[104:107], a[116:119]\n" \
"ds_write_b128 v124, a[112:115] offset:12288\n" \
"v_exp_f32 v45, v45\n" \
"v_exp_f32 v46, v46\n" \
"v_cvt_pk_bf16_f32 v42, v44, v45\n" \
"v_mfma_f32_16x16x32_bf16 a[84:87], a[232:235], v[64:67], a[84:87]\n" \
"v_exp_f32 v47, v47\n" \
"v_mfma_f32_16x16x32_bf16 a[84:87], a[236:239], v[96:99], a[84:87]\n" \
"ds_read_b128 a[232:235], v247 offset:5120\n" \
"v_exp_f32 v24, v24\n" \
"v_mfma_f32_16x16x32_bf16 a[88:91], a[240:243], v[64:67], a[88:91]\n" \
"ds_read_b128 v[0:3], v247 offset:22528\n" \
"v_cvt_pk_bf16_f32 v43, v46, v47\n" \
"v_mfma_f32_16x16x32_bf16 a[88:91], a[244:247], v[96:99], a[88:91]\n" \
"ds_write_b128 v124, a[116:119] offset:13312\n" \
"v_exp_f32 v25, v25\n" \
"v_exp_f32 v26, v26\n" \
"v_cvt_pk_bf16_f32 v24, v24, v25\n" \
"v_mfma_f32_16x16x32_bf16 v[242:245], a[4:7], v[8:11], v[242:245]\n" \
"v_exp_f32 v27, v27\n" \
"v_mfma_f32_16x16x32_bf16 v[242:245], a[4:7], v[40:43], v[242:245]\n" \
"v_exp_f32 v28, v28\n" \
"v_cvt_pk_bf16_f32 v25, v26, v27\n" \
"v_mfma_f32_16x16x32_bf16 a[120:123], a[240:243], v[72:75], a[120:123]\n" \
"v_exp_f32 v29, v29\n" \
"v_mfma_f32_16x16x32_bf16 a[120:123], a[244:247], v[104:107], a[120:123]\n" \
"ds_read_b128 a[236:239], v247 offset:13312\n" \
"v_exp_f32 v30, v30\n" \
"v_mfma_f32_16x16x32_bf16 v[152:155], a[240:243], v[80:83], v[152:155]\n" \
"ds_read_b128 v[4:7], v247 offset:30720\n" \
"v_cvt_pk_bf16_f32 v26, v28, v29\n" \
"v_mfma_f32_16x16x32_bf16 v[152:155], a[244:247], v[112:115], v[152:155]\n" \
"ds_write_b128 v124, a[84:87] offset:5120\n" \
"v_exp_f32 v31, v31\n" \
"v_exp_f32 v56, v56\n" \
"v_cvt_pk_bf16_f32 v27, v30, v31\n" \
"v_mfma_f32_16x16x32_bf16 v[184:187], a[240:243], v[88:91], v[184:187]\n" \
"v_exp_f32 v57, v57\n" \
"v_mfma_f32_16x16x32_bf16 v[184:187], a[244:247], v[120:123], v[184:187]\n" \
"ds_read_b128 a[240:243], v247 offset:6144\n" \
"v_exp_f32 v58, v58\n" \
"v_mfma_f32_16x16x32_bf16 a[92:95], a[248:251], v[64:67], a[92:95]\n" \
"ds_read_b128 v[32:35], v247 offset:23552\n" \
"v_cvt_pk_bf16_f32 v56, v56, v57\n" \
"v_mfma_f32_16x16x32_bf16 a[92:95], a[252:255], v[96:99], a[92:95]\n" \
"ds_write_b128 v124, a[88:91] offset:6144\n" \
"v_exp_f32 v59, v59\n" \
"v_exp_f32 v60, v60\n" \
"v_cvt_pk_bf16_f32 v57, v58, v59\n" \
"v_mfma_f32_16x16x32_bf16 a[124:127], a[248:251], v[72:75], a[124:127]\n" \
"v_exp_f32 v61, v61\n" \
"v_mfma_f32_16x16x32_bf16 a[124:127], a[252:255], v[104:107], a[124:127]\n" \
"ds_read_b128 a[244:247], v247 offset:14336\n" \
"v_exp_f32 v62, v62\n" \
"v_mfma_f32_16x16x32_bf16 v[156:159], a[248:251], v[80:83], v[156:159]\n" \
"ds_read_b128 v[36:39], v247 offset:31744\n" \
"v_cvt_pk_bf16_f32 v58, v60, v61\n" \
"v_mfma_f32_16x16x32_bf16 v[156:159], a[252:255], v[112:115], v[156:159]\n" \
"ds_write_b128 v124, a[120:123] offset:14336\n" \
"v_exp_f32 v63, v63\n" \
"v_mfma_f32_16x16x32_bf16 v[188:191], a[248:251], v[88:91], v[188:191]\n" \
"v_exp_f32 v16, v16\n" \
"v_mfma_f32_16x16x32_bf16 v[188:191], a[252:255], v[120:123], v[188:191]\n" \
"s_waitcnt lgkmcnt(3)\n" \
"ds_read_b128 a[248:251], v247 offset:7168\n" \
"ds_write_b128 v124, a[92:95] offset:7168\n" \
"v_cvt_pk_bf16_f32 v59, v62, v63\n" \
"v_exp_f32 v17, v17\n" \
"v_mfma_f32_16x16x32_bf16 v[160:163], v[192:195], v[8:11], v[160:163]\n" \
"v_exp_f32 v18, v18\n" \
"v_mfma_f32_16x16x32_bf16 v[160:163], v[196:199], v[40:43], v[160:163]\n" \
"ds_read_b128 a[252:255], v247 offset:15360\n" \
"v_cvt_pk_bf16_f32 v16, v16, v17\n" \
"v_mfma_f32_16x16x32_bf16 v[242:245], a[4:7], v[24:27], v[242:245]\n" \
"v_exp_f32 v19, v19\n" \
"v_mfma_f32_16x16x32_bf16 v[242:245], a[4:7], v[56:59], v[242:245]\n" \
"v_exp_f32 v20, v20\n" \
"v_cvt_pk_bf16_f32 v17, v18, v19\n" \
"v_exp_f32 v21, v21\n" \
"v_mfma_f32_16x16x32_bf16 v[164:167], v[200:203], v[8:11], v[164:167]\n" \
"v_exp_f32 v22, v22\n" \
"v_mfma_f32_16x16x32_bf16 v[164:167], v[204:207], v[40:43], v[164:167]\n" \
"ds_write_b128 v124, a[124:127] offset:15360\n" \
"v_cvt_pk_bf16_f32 v18, v20, v21\n" \
"v_exp_f32 v23, v23\n" \
"v_mfma_f32_16x16x32_bf16 v[168:171], v[208:211], v[8:11], v[168:171]\n" \
"v_exp_f32 v48, v48\n" \
"v_mfma_f32_16x16x32_bf16 v[168:171], v[212:215], v[40:43], v[168:171]\n" \
"s_waitcnt vmcnt(0) lgkmcnt(0)\n" \
"v_cvt_pk_bf16_f32 v19, v22, v23\n" \
"v_exp_f32 v49, v49\n" \
"v_mfma_f32_16x16x32_bf16 v[172:175], v[216:219], v[8:11], v[172:175]\n" \
"v_exp_f32 v50, v50\n" \
"v_mfma_f32_16x16x32_bf16 v[172:175], v[220:223], v[40:43], v[172:175]\n" \
"s_barrier\n" \
"v_cvt_pk_bf16_f32 v48, v48, v49\n" \
"v_mfma_f32_16x16x32_bf16 v[242:245], a[0:3], v[16:19], v[242:245]\n" \
"v_exp_f32 v51, v51\n" \
"v_exp_f32 v52, v52\n" \
"v_mfma_f32_16x16x32_bf16 v[176:179], v[224:227], v[8:11], v[176:179]\n" \
"s_add_u32 m0, s84, 90112\n" \
"ds_read_b64 v[240:241], v127\n" \
"v_cvt_pk_bf16_f32 v49, v50, v51\n" \
"v_mfma_f32_16x16x32_bf16 v[176:179], v[228:231], v[40:43], v[176:179]\n" \
"ds_read_b128 v[64:67], v125 offset:0\n" \
"global_load_lds_dwordx4 v253, s[48:49] offset:-4096\n" \
"v_exp_f32 v53, v53\n" \
"v_exp_f32 v54, v54\n" \
"v_mfma_f32_16x16x32_bf16 v[180:183], v[232:235], v[8:11], v[180:183]\n" \
"ds_read_b128 v[68:71], v125 offset:1024\n" \
"global_load_lds_dwordx4 v253, s[48:49] offset:-2048\n" \
"v_mfma_f32_16x16x32_bf16 v[180:183], v[236:239], v[40:43], v[180:183]\n" \
"ds_read_b128 v[72:75], v125 offset:2048\n" \
"v_cvt_pk_bf16_f32 v50, v52, v53\n" \
"v_mfma_f32_16x16x32_bf16 v[184:187], v[0:3], v[8:11], v[184:187]\n" \
"ds_read_b128 v[76:79], v125 offset:3072\n" \
"global_load_lds_dwordx4 v253, s[48:49] offset:0\n" \
"v_mfma_f32_16x16x32_bf16 v[184:187], v[4:7], v[40:43], v[184:187]\n" \
"s_add_u32 s48, s48, 0x300000\n" \
"s_addc_u32 s49, s49, 0\n" \
"ds_read_b128 v[80:83], v125 offset:4096\n" \
"v_mfma_f32_16x16x32_bf16 v[188:191], v[32:35], v[8:11], v[188:191]\n" \
"ds_read_b128 v[84:87], v125 offset:5120\n" \
"global_load_lds_dwordx4 v253, s[50:51] offset:-3072\n" \
"v_mfma_f32_16x16x32_bf16 v[188:191], v[36:39], v[40:43], v[188:191]\n" \
"s_waitcnt lgkmcnt(5)\n" \
"ds_read_b128 v[88:91], v125 offset:6144\n" \
"v_exp_f32 v55, v55\n" \
"v_mfma_f32_16x16x32_bf16 v[160:163], a[192:195], v[24:27], v[160:163]\n" \
"ds_read_b128 v[92:95], v125 offset:7168\n" \
"global_load_lds_dwordx4 v253, s[50:51] offset:-1024\n" \
"v_mfma_f32_16x16x32_bf16 v[160:163], a[196:199], v[56:59], v[160:163]\n" \
"s_movk_i32 s89, 0xc0\n" \
"ds_read_b128 v[96:99], v125 offset:8192\n" \
"global_load_lds_dwordx4 v253, s[50:51] offset:1024\n" \
"v_mfma_f32_16x16x32_bf16 v[164:167], a[200:203], v[24:27], v[164:167]\n" \
"s_add_u32 s50, s50, 0x300000\n" \
"s_addc_u32 s51, s51, 0\n" \
"ds_read_b128 v[100:103], v125 offset:9216\n" \
"v_mfma_f32_16x16x32_bf16 v[164:167], a[204:207], v[56:59], v[164:167]\n" \
"s_add_u32 m0, s95, 65536\n" \
"ds_read_b128 v[104:107], v125 offset:10240\n" \
"v_cvt_pk_bf16_f32 v51, v54, v55\n" \
"v_mfma_f32_16x16x32_bf16 v[168:171], a[208:211], v[24:27], v[168:171]\n" \
"s_movk_i32 s90, 288\n" \
"ds_read_b128 v[108:111], v125 offset:11264\n" \
"global_load_lds_dwordx4 v252, s[42:43] offset:0\n" \
"v_mfma_f32_16x16x32_bf16 v[242:245], a[0:3], v[48:51], v[242:245]\n" \
"v_lshrrev_b32 v211, 4, v248\n" \
"v_lshrrev_b32 v214, 8, v248\n" \
"v_and_b32 v215, 7, v211\n" \
"v_xor_b32 v214, v214, v215\n" \
"v_pk_add_f32 v[128:129], v[128:129], v[64:65]\n" \
"v_pk_add_f32 v[130:131], v[130:131], v[66:67]\n" \
"s_waitcnt lgkmcnt(10)\n" \
"v_pk_add_f32 v[132:133], v[132:133], v[68:69]\n" \
"v_pk_add_f32 v[134:135], v[134:135], v[70:71]\n" \
"s_waitcnt lgkmcnt(9)\n" \
"v_pk_add_f32 v[136:137], v[136:137], v[72:73]\n" \
"v_pk_add_f32 v[138:139], v[138:139], v[74:75]\n" \
"s_waitcnt lgkmcnt(8)\n" \
"v_pk_add_f32 v[140:141], v[140:141], v[76:77]\n" \
"v_pk_add_f32 v[142:143], v[142:143], v[78:79]\n" \
"s_waitcnt lgkmcnt(7)\n" \
"v_pk_add_f32 v[144:145], v[144:145], v[80:81]\n" \
"v_pk_add_f32 v[146:147], v[146:147], v[82:83]\n" \
"s_waitcnt lgkmcnt(6)\n" \
"v_pk_add_f32 v[148:149], v[148:149], v[84:85]\n" \
"v_pk_add_f32 v[150:151], v[150:151], v[86:87]\n" \
"s_waitcnt lgkmcnt(5)\n" \
"v_pk_add_f32 v[152:153], v[152:153], v[88:89]\n" \
"v_pk_add_f32 v[154:155], v[154:155], v[90:91]\n" \
"s_waitcnt lgkmcnt(4)\n" \
"v_pk_add_f32 v[156:157], v[156:157], v[92:93]\n" \
"v_pk_add_f32 v[158:159], v[158:159], v[94:95]\n" \
"v_mfma_f32_16x16x32_bf16 v[168:171], a[212:215], v[56:59], v[168:171]\n" \
"ds_read_b128 v[112:115], v125 offset:12288\n" \
"s_lshr_b32 s71, s61, 1\n" \
"v_mfma_f32_16x16x32_bf16 v[172:175], a[216:219], v[24:27], v[172:175]\n" \
"s_lshl_b32 s70, s71, 11\n" \
"ds_read_b128 v[116:119], v125 offset:13312\n" \
"global_load_lds_dwordx4 v252, s[42:43] offset:2048\n" \
"v_mfma_f32_16x16x32_bf16 v[172:175], a[220:223], v[56:59], v[172:175]\n" \
"s_add_u32 s42, s42, 0x400\n" \
"s_addc_u32 s43, s43, 0\n" \
"ds_read_b128 v[120:123], v125 offset:14336\n" \
"v_mfma_f32_16x16x32_bf16 v[176:179], a[224:227], v[24:27], v[176:179]\n" \
"s_add_u32 s70, s70, 118784\n" \
"ds_read_b128 v[124:127], v125 offset:15360\n" \
"v_and_b32 v216, 15, v211\n" \
"v_mfma_f32_16x16x32_bf16 v[176:179], a[228:231], v[56:59], v[176:179]\n" \
"s_waitcnt lgkmcnt(7)\n" \
"global_load_lds_dwordx4 v252, s[44:45] offset:1024\n" \
"global_load_lds_dwordx4 v252, s[44:45] offset:3072\n" \
"v_mfma_f32_16x16x32_bf16 v[180:183], a[232:235], v[24:27], v[180:183]\n" \
"s_add_u32 s44, s44, 0x400\n" \
"s_addc_u32 s45, s45, 0\n" \
"v_lshlrev_b32 v213, 7, v216\n" \
"v_mfma_f32_16x16x32_bf16 v[180:183], a[236:239], v[56:59], v[180:183]\n" \
"s_waitcnt lgkmcnt(0)\n" \
"v_lshl_add_u32 v213, v214, 4, v213\n" \
"v_add_u32 v205, s70, v213\n" \
"v_xor_b32 v206, 64, v205\n" \
"v_mfma_f32_16x16x32_bf16 v[184:187], a[240:243], v[24:27], v[184:187]\n" \
"s_and_b32 s70, s61, 1\n" \
"s_lshl_b32 s70, s70, 9\n" \
"s_add_u32 s70, s70, 139264\n" \
"v_mfma_f32_16x16x32_bf16 v[184:187], a[244:247], v[56:59], v[184:187]\n" \
"s_barrier\n" \
"v_and_b32 v213, 3, v211\n" \
"v_lshlrev_b32 v213, 3, v213\n" \
"v_mfma_f32_16x16x32_bf16 v[188:191], a[248:251], v[24:27], v[188:191]\n" \
"s_add_u32 m0, s95, 0\n" \
"v_and_or_b32 v213, v248, s89, v213\n" \
"v_bfe_u32 v214, v211, 4, 1\n" \
"v_mfma_f32_16x16x32_bf16 v[188:191], a[252:255], v[56:59], v[188:191]\n" \
"v_mad_u32_u24 v213, v214, s90, v213\n" \
"v_lshrrev_b32 v214, 5, v211\n" \
"v_mul_u32_u24 v214, 6144, v214\n" \
"v_mfma_f32_16x16x32_bf16 v[128:131], a[192:195], v[16:19], v[128:131]\n" \
"v_add3_u32 v209, v213, v214, s70\n" \
"v_xor_b32 v210, 32, v209\n" \
"v_mfma_f32_16x16x32_bf16 v[128:131], a[196:199], v[48:51], v[128:131]\n" \
"ds_read_b128 a[72:75], v205 offset:0\n" \
"ds_read_b64_tr_b16 a[96:97], v209 offset:2048\n" \
"v_mfma_f32_16x16x32_bf16 v[132:135], a[200:203], v[16:19], v[132:135]\n" \
"ds_read_b64_tr_b16 a[98:99], v209 offset:3072\n" \
"global_load_lds_dwordx4 v252, s[42:43] offset:0\n" \
"v_mfma_f32_16x16x32_bf16 v[132:135], a[204:207], v[48:51], v[132:135]\n" \
"ds_read_b128 a[76:79], v206 offset:0\n" \
"v_mfma_f32_16x16x32_bf16 v[136:139], a[208:211], v[16:19], v[136:139]\n" \
"ds_read_b64_tr_b16 a[100:101], v209 offset:14336\n" \
"ds_read_b64_tr_b16 a[102:103], v209 offset:15360\n" \
"global_load_lds_dwordx4 v252, s[42:43] offset:2048\n" \
"v_pk_add_f32 v[160:161], v[160:161], v[96:97]\n" \
"v_pk_add_f32 v[162:163], v[162:163], v[98:99]\n" \
"v_pk_add_f32 v[164:165], v[164:165], v[100:101]\n" \
"v_pk_add_f32 v[166:167], v[166:167], v[102:103]\n" \
"v_pk_add_f32 v[168:169], v[168:169], v[104:105]\n" \
"v_pk_add_f32 v[170:171], v[170:171], v[106:107]\n" \
"v_pk_add_f32 v[172:173], v[172:173], v[108:109]\n" \
"v_pk_add_f32 v[174:175], v[174:175], v[110:111]\n" \
"v_pk_add_f32 v[176:177], v[176:177], v[112:113]\n" \
"v_pk_add_f32 v[178:179], v[178:179], v[114:115]\n" \
"v_pk_add_f32 v[180:181], v[180:181], v[116:117]\n" \
"v_pk_add_f32 v[182:183], v[182:183], v[118:119]\n" \
"v_pk_add_f32 v[184:185], v[184:185], v[120:121]\n" \
"v_pk_add_f32 v[186:187], v[186:187], v[122:123]\n" \
"v_pk_add_f32 v[188:189], v[188:189], v[124:125]\n" \
"v_pk_add_f32 v[190:191], v[190:191], v[126:127]\n" \
"v_mfma_f32_16x16x32_bf16 v[136:139], a[212:215], v[48:51], v[136:139]\n" \
"s_add_u32 s42, s42, 0xfffffb00\n" \
"s_addc_u32 s43, s43, -1\n" \
"ds_read_b64_tr_b16 a[104:105], v209 offset:0\n" \
"v_mfma_f32_16x16x32_bf16 v[140:143], a[216:219], v[16:19], v[140:143]\n" \
"ds_read_b128 a[80:83], v205 offset:4096\n" \
"v_mfma_f32_16x16x32_bf16 v[140:143], a[220:223], v[48:51], v[140:143]\n" \
"ds_read_b64_tr_b16 a[106:107], v209 offset:1024\n" \
"global_load_lds_dwordx4 v252, s[44:45] offset:1024\n" \
"ds_read_b64_tr_b16 a[108:109], v209 offset:12288\n" \
"v_mfma_f32_16x16x32_bf16 v[144:147], a[224:227], v[16:19], v[144:147]\n" \
"ds_read_b64_tr_b16 a[110:111], v209 offset:13312\n" \
"v_mfma_f32_16x16x32_bf16 v[144:147], a[228:231], v[48:51], v[144:147]\n" \
"ds_read_b128 a[84:87], v206 offset:4096\n" \
"v_mfma_f32_16x16x32_bf16 v[148:151], a[232:235], v[16:19], v[148:151]\n" \
"ds_read_b64_tr_b16 a[112:113], v210 offset:0\n" \
"Lpg8609_%=:\n" \
"Lst1x2_%=:\n" \
"s_cbranch_execz Lpg10872_%=\n" \
"global_load_lds_dwordx4 v252, s[44:45] offset:3072\n" \
"ds_read_b64_tr_b16 a[114:115], v210 offset:1024\n" \
"v_mfma_f32_16x16x32_bf16 v[148:151], a[236:239], v[48:51], v[148:151]\n" \
"ds_read_b64_tr_b16 a[116:117], v210 offset:12288\n" \
"v_add_f32 v244, v244, v240\n" \
"v_mfma_f32_16x16x32_bf16 v[152:155], a[240:243], v[16:19], v[152:155]\n" \
"ds_read_b128 a[88:91], v205 offset:8192\n" \
"v_add_f32 v245, v245, v241\n" \
"v_mfma_f32_16x16x32_bf16 v[152:155], a[244:247], v[48:51], v[152:155]\n" \
"ds_read_b128 a[92:95], v206 offset:8192\n" \
"ds_read_b64_tr_b16 a[118:119], v210 offset:13312\n" \
"v_mfma_f32_16x16x32_bf16 v[156:159], a[248:251], v[16:19], v[156:159]\n" \
"v_mfma_f32_16x16x32_bf16 v[156:159], a[252:255], v[48:51], v[156:159]\n" \
"s_waitcnt lgkmcnt(4)\n" \
"Ljoin_%=:\n" \
"v_mfma_f32_16x16x32_bf16 v[40:43], a[96:99], a[72:75], 0\n" \
"s_add_u32 s44, s44, 0xfffffb00\n" \
"s_addc_u32 s45, s45, -1\n" \
"ds_read_b128 a[192:195], v205 offset:12288\n" \
"v_mfma_f32_16x16x32_bf16 v[40:43], a[100:103], a[76:79], v[40:43]\n" \
"ds_read_b128 a[196:199], v206 offset:12288\n" \
"s_add_u32 m0, s84, 24576\n" \
"v_mfma_f32_16x16x32_bf16 v[0:3], a[104:107], a[72:75], 0\n" \
"ds_read_b64_tr_b16 a[208:209], v209 offset:4096\n" \
"global_load_lds_dwordx4 v253, s[48:49] offset:-4096\n" \
"v_mfma_f32_16x16x32_bf16 v[0:3], a[108:111], a[76:79], v[0:3]\n" \
"s_waitcnt lgkmcnt(3)\n" \
"v_mov_b32 v232, v244\n" \
"v_mfma_f32_16x16x32_bf16 v[4:7], a[112:115], a[72:75], 0\n" \
"ds_read_b64_tr_b16 a[210:211], v209 offset:5120\n" \
"global_load_lds_dwordx4 v253, s[48:49] offset:-2048\n" \
"v_mfma_f32_16x16x32_bf16 v[4:7], a[116:119], a[76:79], v[4:7]\n" \
"ds_read_b64_tr_b16 a[212:213], v209 offset:16384\n" \
"v_permlane16_swap_b32 v232, v244\n" \
"v_mfma_f32_16x16x32_bf16 v[12:15], a[112:115], a[80:83], 0\n" \
"ds_read_b64_tr_b16 a[214:215], v209 offset:17408\n" \
"v_add_f32 v244, v244, v232\n" \
"v_mfma_f32_16x16x32_bf16 v[12:15], a[116:119], a[84:87], v[12:15]\n" \
"global_load_lds_dwordx4 v253, s[48:49] offset:0\n" \
"v_mov_b32 v232, v244\n" \
"v_mfma_f32_16x16x32_bf16 v[8:11], a[104:107], a[80:83], 0\n" \
"s_add_u32 s48, s48, 0xffc40000\n" \
"s_addc_u32 s49, s49, -1\n" \
"ds_read_b64_tr_b16 a[216:217], v210 offset:4096\n" \
"v_mfma_f32_16x16x32_bf16 v[8:11], a[108:111], a[84:87], v[8:11]\n" \
"ds_read_b64_tr_b16 a[218:219], v210 offset:5120\n" \
"v_permlane32_swap_b32 v232, v244\n" \
"v_add_f32 v244, v244, v232\n" \
"v_mfma_f32_16x16x32_bf16 v[48:51], a[96:99], a[80:83], 0\n" \
"ds_read_b64_tr_b16 a[220:221], v210 offset:16384\n" \
"global_load_lds_dwordx4 v253, s[50:51] offset:-3072\n" \
"v_mfma_f32_16x16x32_bf16 v[48:51], a[100:103], a[84:87], v[48:51]\n" \
"v_rcp_f32 v232, v244\n" \
"v_mfma_f32_16x16x32_bf16 v[56:59], a[96:99], a[88:91], 0\n" \
"ds_read_b64_tr_b16 a[222:223], v210 offset:17408\n" \
"v_mul_f32 v244, 0x4d800000, v244\n" \
"v_mfma_f32_16x16x32_bf16 v[56:59], a[100:103], a[92:95], v[56:59]\n" \
"ds_read_b64_tr_b16 a[200:201], v210 offset:2048\n" \
"global_load_lds_dwordx4 v253, s[50:51] offset:-1024\n" \
"v_mfma_f32_16x16x32_bf16 v[16:19], a[104:107], a[88:91], 0\n" \
"ds_read_b64_tr_b16 a[202:203], v210 offset:3072\n" \
"v_add_u32 v233, 0x1c90080, v249\n" \
"v_mfma_f32_16x16x32_bf16 v[16:19], a[108:111], a[92:95], v[16:19]\n" \
"v_cndmask_b32 v233, v249, v233, s[16:17]\n" \
"v_mul_f32 v128, v128, v232\n" \
"v_mfma_f32_16x16x32_bf16 v[20:23], a[112:115], a[88:91], 0\n" \
"ds_read_b64_tr_b16 a[204:205], v210 offset:14336\n" \
"global_load_lds_dwordx4 v253, s[50:51] offset:1024\n" \
"v_mfma_f32_16x16x32_bf16 v[20:23], a[116:119], a[92:95], v[20:23]\n" \
"s_waitcnt lgkmcnt(3)\n" \
"ds_read_b64_tr_b16 a[206:207], v210 offset:15360\n" \
"v_mul_f32 v129, v129, v232\n" \
"v_mfma_f32_16x16x32_bf16 v[28:31], a[112:115], a[192:195], 0\n" \
"s_add_u32 s50, s50, 0xffc40000\n" \
"s_addc_u32 s51, s51, -1\n" \
"v_add_u32 v205, 0xffff3000, v205\n" \
"v_mfma_f32_16x16x32_bf16 v[28:31], a[116:119], a[196:199], v[28:31]\n" \
"v_add_u32 v206, 0xffff3000, v206\n" \
"v_add_u32 v209, 0xffff3000, v209\n" \
"v_mfma_f32_16x16x32_bf16 v[24:27], a[104:107], a[192:195], 0\n" \
"v_add_u32 v210, 0xffff3000, v210\n" \
"v_mul_f32 v130, v130, v232\n" \
"v_mfma_f32_16x16x32_bf16 v[24:27], a[108:111], a[196:199], v[24:27]\n" \
"s_waitcnt vmcnt(10) lgkmcnt(0)\n" \
"v_mul_f32 v131, v131, v232\n" \
"v_mul_f32 v132, v132, v232\n" \
"v_mfma_f32_16x16x32_bf16 v[64:67], a[96:99], a[192:195], 0\n" \
"v_mul_f32 v133, v133, v232\n" \
"v_mul_f32 v134, v134, v232\n" \
"v_mfma_f32_16x16x32_bf16 v[64:67], a[100:103], a[196:199], v[64:67]\n" \
"s_barrier\n" \
"v_mfma_f32_16x16x32_bf16 v[80:83], a[72:75], a[208:211], 0\n" \
"ds_read_b64_tr_b16 a[96:97], v209 offset:2048\n" \
"v_mul_f32 v135, v135, v232\n" \
"v_mfma_f32_16x16x32_bf16 v[80:83], a[76:79], a[212:215], v[80:83]\n" \
"ds_read_b64_tr_b16 a[98:99], v209 offset:3072\n" \
"v_fma_f32 v244, v128, v129, v244\n" \
"v_mfma_f32_16x16x32_bf16 v[84:87], a[72:75], a[216:219], 0\n" \
"s_add_u32 m0, s95, 118784\n" \
"ds_read_b64_tr_b16 a[100:101], v209 offset:14336\n" \
"v_cvt_pk_bf16_f32 v128, v128, v129\n" \
"v_mfma_f32_16x16x32_bf16 v[84:87], a[76:79], a[220:223], v[84:87]\n" \
"global_load_lds_dwordx4 v252, s[42:43] offset:0\n" \
"v_fma_f32 v244, v130, v131, v244\n" \
"v_mfma_f32_16x16x32_bf16 v[44:47], a[200:203], a[72:75], 0\n" \
"ds_read_b64_tr_b16 a[102:103], v209 offset:15360\n" \
"v_cvt_pk_bf16_f32 v129, v130, v131\n" \
"v_mfma_f32_16x16x32_bf16 v[44:47], a[204:207], a[76:79], v[44:47]\n" \
"ds_read_b128 a[72:75], v205 offset:0\n" \
"ds_read_b64_tr_b16 a[104:105], v209 offset:0\n" \
"v_mfma_f32_16x16x32_bf16 v[52:55], a[200:203], a[80:83], 0\n" \
"ds_read_b128 a[76:79], v206 offset:0\n" \
"v_fma_f32 v244, v132, v133, v244\n" \
"v_mfma_f32_16x16x32_bf16 v[52:55], a[204:207], a[84:87], v[52:55]\n" \
"global_load_lds_dwordx4 v252, s[42:43] offset:2048\n" \
"v_cvt_pk_bf16_f32 v130, v132, v133\n" \
"v_mfma_f32_16x16x32_bf16 v[92:95], a[80:83], a[216:219], 0\n" \
"s_add_u32 s42, s42, 0x80\n" \
"s_addc_u32 s43, s43, 0\n" \
"ds_read_b64_tr_b16 a[106:107], v209 offset:1024\n" \
"v_mfma_f32_16x16x32_bf16 v[92:95], a[84:87], a[220:223], v[92:95]\n" \
"ds_read_b64_tr_b16 a[108:109], v209 offset:12288\n" \
"v_fma_f32 v244, v134, v135, v244\n" \
"v_mfma_f32_16x16x32_bf16 v[88:91], a[80:83], a[208:211], 0\n" \
"ds_read_b64_tr_b16 a[110:111], v209 offset:13312\n" \
"v_cvt_pk_bf16_f32 v131, v134, v135\n" \
"v_mfma_f32_16x16x32_bf16 v[88:91], a[84:87], a[212:215], v[88:91]\n" \
"buffer_store_dwordx4 v[128:131], v233, s[56:59], s78 offen offset:0 sc1\n" \
"global_load_lds_dwordx4 v252, s[44:45] offset:1024\n" \
"v_mul_f32 v136, v136, v232\n" \
"v_mul_f32 v137, v137, v232\n" \
"v_mul_f32 v138, v138, v232\n" \
"v_mfma_f32_16x16x32_bf16 v[96:99], a[88:91], a[208:211], 0\n" \
"ds_read_b128 a[80:83], v205 offset:4096\n" \
"v_mul_f32 v139, v139, v232\n" \
"v_mfma_f32_16x16x32_bf16 v[96:99], a[92:95], a[212:215], v[96:99]\n" \
"ds_read_b128 a[84:87], v206 offset:4096\n" \
"v_mul_f32 v140, v140, v232\n" \
"v_mfma_f32_16x16x32_bf16 v[100:103], a[88:91], a[216:219], 0\n" \
"ds_read_b64_tr_b16 a[112:113], v210 offset:0\n" \
"v_mul_f32 v141, v141, v232\n" \
"v_mfma_f32_16x16x32_bf16 v[100:103], a[92:95], a[220:223], v[100:103]\n" \
"global_load_lds_dwordx4 v252, s[44:45] offset:3072\n" \
"v_mul_f32 v142, v142, v232\n" \
"v_mfma_f32_16x16x32_bf16 v[60:63], a[200:203], a[88:91], 0\n" \
"s_add_u32 s44, s44, 0x80\n" \
"s_addc_u32 s45, s45, 0\n" \
"ds_read_b64_tr_b16 a[114:115], v210 offset:1024\n" \
"v_mfma_f32_16x16x32_bf16 v[60:63], a[204:207], a[92:95], v[60:63]\n" \
"ds_read_b64_tr_b16 a[116:117], v210 offset:12288\n" \
"v_mul_f32 v143, v143, v232\n" \
"v_mfma_f32_16x16x32_bf16 v[68:71], a[200:203], a[192:195], 0\n" \
"s_add_u32 m0, s84, 143360\n" \
"ds_read_b128 a[88:91], v205 offset:8192\n" \
"v_fma_f32 v244, v136, v137, v244\n" \
"v_mfma_f32_16x16x32_bf16 v[68:71], a[204:207], a[196:199], v[68:71]\n" \
"global_load_lds_dwordx4 v253, s[48:49] offset:-4096\n" \
"v_cvt_pk_bf16_f32 v136, v136, v137\n" \
"v_mfma_f32_16x16x32_bf16 v[108:111], a[192:195], a[216:219], 0\n" \
"ds_read_b128 a[92:95], v206 offset:8192\n" \
"v_fma_f32 v244, v138, v139, v244\n" \
"v_mfma_f32_16x16x32_bf16 v[108:111], a[196:199], a[220:223], v[108:111]\n" \
"ds_read_b64_tr_b16 a[118:119], v210 offset:13312\n" \
"v_cvt_pk_bf16_f32 v137, v138, v139\n" \
"v_mfma_f32_16x16x32_bf16 v[104:107], a[192:195], a[208:211], 0\n" \
"v_fma_f32 v244, v140, v141, v244\n" \
"v_cvt_pk_bf16_f32 v138, v140, v141\n" \
"v_mfma_f32_16x16x32_bf16 v[104:107], a[196:199], a[212:215], v[104:107]\n" \
"s_waitcnt lgkmcnt(2)\n" \
"global_load_lds_dwordx4 v253, s[48:49] offset:-2048\n" \
"v_fma_f32 v244, v142, v143, v244\n" \
"v_mfma_f32_16x16x32_bf16 v[40:43], a[96:99], a[72:75], v[40:43]\n" \
"ds_read_b128 a[192:195], v205 offset:12288\n" \
"v_cvt_pk_bf16_f32 v139, v142, v143\n" \
"v_mfma_f32_16x16x32_bf16 v[40:43], a[100:103], a[76:79], v[40:43]\n" \
"buffer_store_dwordx4 v[136:139], v233, s[56:59], s78 offen offset:2048 sc1\n" \
"ds_read_b128 a[196:199], v206 offset:12288\n" \
"s_add_u32 s78, s78, 0x1000\n" \
"v_mul_f32 v144, v144, v232\n" \
"v_mul_f32 v145, v145, v232\n" \
"v_mfma_f32_16x16x32_bf16 v[0:3], a[104:107], a[72:75], v[0:3]\n" \
"ds_read_b64_tr_b16 a[208:209], v209 offset:4096\n" \
"v_mul_f32 v146, v146, v232\n" \
"v_mfma_f32_16x16x32_bf16 v[0:3], a[108:111], a[76:79], v[0:3]\n" \
"s_waitcnt lgkmcnt(3)\n" \
"global_load_lds_dwordx4 v253, s[48:49] offset:0\n" \
"v_mul_f32 v147, v147, v232\n" \
"v_mfma_f32_16x16x32_bf16 v[4:7], a[112:115], a[72:75], v[4:7]\n" \
"s_add_u32 s48, s48, 0x60000\n" \
"s_addc_u32 s49, s49, 0\n" \
"ds_read_b64_tr_b16 a[210:211], v209 offset:5120\n" \
"v_mfma_f32_16x16x32_bf16 v[4:7], a[116:119], a[76:79], v[4:7]\n" \
"ds_read_b64_tr_b16 a[212:213], v209 offset:16384\n" \
"v_mul_f32 v148, v148, v232\n" \
"v_mfma_f32_16x16x32_bf16 v[12:15], a[112:115], a[80:83], v[12:15]\n" \
"ds_read_b64_tr_b16 a[214:215], v209 offset:17408\n" \
"v_mul_f32 v149, v149, v232\n" \
"v_mfma_f32_16x16x32_bf16 v[12:15], a[116:119], a[84:87], v[12:15]\n" \
"global_load_lds_dwordx4 v253, s[50:51] offset:-3072\n" \
"v_mul_f32 v150, v150, v232\n" \
"v_mfma_f32_16x16x32_bf16 v[8:11], a[104:107], a[80:83], v[8:11]\n" \
"ds_read_b64_tr_b16 a[216:217], v210 offset:4096\n" \
"v_mul_f32 v151, v151, v232\n" \
"v_mfma_f32_16x16x32_bf16 v[8:11], a[108:111], a[84:87], v[8:11]\n" \
"ds_read_b64_tr_b16 a[218:219], v210 offset:5120\n" \
"v_fma_f32 v244, v144, v145, v244\n" \
"v_mfma_f32_16x16x32_bf16 v[48:51], a[96:99], a[80:83], v[48:51]\n" \
"ds_read_b64_tr_b16 a[220:221], v210 offset:16384\n" \
"v_cvt_pk_bf16_f32 v144, v144, v145\n" \
"v_mfma_f32_16x16x32_bf16 v[48:51], a[100:103], a[84:87], v[48:51]\n" \
"global_load_lds_dwordx4 v253, s[50:51] offset:-1024\n" \
"v_fma_f32 v244, v146, v147, v244\n" \
"v_mfma_f32_16x16x32_bf16 v[56:59], a[96:99], a[88:91], v[56:59]\n" \
"ds_read_b64_tr_b16 a[222:223], v210 offset:17408\n" \
"v_cvt_pk_bf16_f32 v145, v146, v147\n" \
"v_mfma_f32_16x16x32_bf16 v[56:59], a[100:103], a[92:95], v[56:59]\n" \
"ds_read_b64_tr_b16 a[200:201], v210 offset:2048\n" \
"v_fma_f32 v244, v148, v149, v244\n" \
"v_mfma_f32_16x16x32_bf16 v[16:19], a[104:107], a[88:91], v[16:19]\n" \
"ds_read_b64_tr_b16 a[202:203], v210 offset:3072\n" \
"v_cvt_pk_bf16_f32 v146, v148, v149\n" \
"v_mfma_f32_16x16x32_bf16 v[16:19], a[108:111], a[92:95], v[16:19]\n" \
"global_load_lds_dwordx4 v253, s[50:51] offset:1024\n" \
"v_fma_f32 v244, v150, v151, v244\n" \
"v_mfma_f32_16x16x32_bf16 v[20:23], a[112:115], a[88:91], v[20:23]\n" \
"s_add_u32 s50, s50, 0x60000\n" \
"s_addc_u32 s51, s51, 0\n" \
"ds_read_b64_tr_b16 a[204:205], v210 offset:14336\n" \
"v_mfma_f32_16x16x32_bf16 v[20:23], a[116:119], a[92:95], v[20:23]\n" \
"s_waitcnt lgkmcnt(3)\n" \
"ds_read_b64_tr_b16 a[206:207], v210 offset:15360\n" \
"v_cvt_pk_bf16_f32 v147, v150, v151\n" \
"v_mfma_f32_16x16x32_bf16 v[28:31], a[112:115], a[192:195], v[28:31]\n" \
"v_add_u32 v205, 0xffff0000, v205\n" \
"v_add_u32 v206, 0xffff0000, v206\n" \
"v_mfma_f32_16x16x32_bf16 v[28:31], a[116:119], a[196:199], v[28:31]\n" \
"buffer_store_dwordx4 v[144:147], v233, s[56:59], s78 offen offset:0 sc1\n" \
"v_add_u32 v209, 0xffff0000, v209\n" \
"v_add_u32 v210, 0xffff0000, v210\n" \
"v_mul_f32 v152, v152, v232\n" \
"v_mul_f32 v153, v153, v232\n" \
"v_mfma_f32_16x16x32_bf16 v[24:27], a[104:107], a[192:195], v[24:27]\n" \
"v_mul_f32 v154, v154, v232\n" \
"v_mul_f32 v155, v155, v232\n" \
"v_mfma_f32_16x16x32_bf16 v[24:27], a[108:111], a[196:199], v[24:27]\n" \
"s_waitcnt vmcnt(13) lgkmcnt(0)\n" \
"v_mul_f32 v156, v156, v232\n" \
"v_mul_f32 v157, v157, v232\n" \
"v_mfma_f32_16x16x32_bf16 v[64:67], a[96:99], a[192:195], v[64:67]\n" \
"v_mul_f32 v158, v158, v232\n" \
"v_mul_f32 v159, v159, v232\n" \
"v_mfma_f32_16x16x32_bf16 v[64:67], a[100:103], a[196:199], v[64:67]\n" \
"s_barrier\n" \
"v_fma_f32 v244, v152, v153, v244\n" \
"v_mfma_f32_16x16x32_bf16 v[80:83], a[72:75], a[208:211], v[80:83]\n" \
"ds_read_b64_tr_b16 a[96:97], v209 offset:2048\n" \
"v_cvt_pk_bf16_f32 v152, v152, v153\n" \
"v_mfma_f32_16x16x32_bf16 v[80:83], a[76:79], a[212:215], v[80:83]\n" \
"ds_read_b64_tr_b16 a[98:99], v209 offset:3072\n" \
"v_fma_f32 v244, v154, v155, v244\n" \
"v_mfma_f32_16x16x32_bf16 v[84:87], a[72:75], a[216:219], v[84:87]\n" \
"s_add_u32 m0, s95, 65536\n" \
"ds_read_b64_tr_b16 a[100:101], v209 offset:14336\n" \
"v_cvt_pk_bf16_f32 v153, v154, v155\n" \
"v_mfma_f32_16x16x32_bf16 v[84:87], a[76:79], a[220:223], v[84:87]\n" \
"global_load_lds_dwordx4 v252, s[42:43] offset:0\n" \
"v_fma_f32 v244, v156, v157, v244\n" \
"v_mfma_f32_16x16x32_bf16 v[44:47], a[200:203], a[72:75], v[44:47]\n" \
"ds_read_b64_tr_b16 a[102:103], v209 offset:15360\n" \
"v_cvt_pk_bf16_f32 v154, v156, v157\n" \
"v_mfma_f32_16x16x32_bf16 v[44:47], a[204:207], a[76:79], v[44:47]\n" \
"ds_read_b128 a[72:75], v205 offset:0\n" \
"ds_read_b64_tr_b16 a[104:105], v209 offset:0\n" \
"v_mfma_f32_16x16x32_bf16 v[52:55], a[200:203], a[80:83], v[52:55]\n" \
"ds_read_b128 a[76:79], v206 offset:0\n" \
"v_fma_f32 v244, v158, v159, v244\n" \
"v_mfma_f32_16x16x32_bf16 v[52:55], a[204:207], a[84:87], v[52:55]\n" \
"global_load_lds_dwordx4 v252, s[42:43] offset:2048\n" \
"v_cvt_pk_bf16_f32 v155, v158, v159\n" \
"v_mfma_f32_16x16x32_bf16 v[92:95], a[80:83], a[216:219], v[92:95]\n" \
"s_add_u32 s42, s42, 0x100\n" \
"s_addc_u32 s43, s43, 0\n" \
"ds_read_b64_tr_b16 a[106:107], v209 offset:1024\n" \
"v_mfma_f32_16x16x32_bf16 v[92:95], a[84:87], a[220:223], v[92:95]\n" \
"buffer_store_dwordx4 v[152:155], v233, s[56:59], s78 offen offset:2048 sc1\n" \
"ds_read_b64_tr_b16 a[108:109], v209 offset:12288\n" \
"v_cmp_class_f32 vcc, v244, s94\n" \
"s_sub_u32 s78, s78, 4096\n" \
"v_mfma_f32_16x16x32_bf16 v[88:91], a[80:83], a[208:211], v[88:91]\n" \
"s_or_b64 s[92:93], s[92:93], vcc\n" \
"ds_read_b64_tr_b16 a[110:111], v209 offset:13312\n" \
"v_mov_b32 v234, v245\n" \
"v_mfma_f32_16x16x32_bf16 v[88:91], a[84:87], a[212:215], v[88:91]\n" \
"global_load_lds_dwordx4 v252, s[44:45] offset:1024\n" \
"v_mfma_f32_16x16x32_bf16 v[96:99], a[88:91], a[208:211], v[96:99]\n" \
"ds_read_b128 a[80:83], v205 offset:4096\n" \
"v_mfma_f32_16x16x32_bf16 v[96:99], a[92:95], a[212:215], v[96:99]\n" \
"ds_read_b128 a[84:87], v206 offset:4096\n" \
"v_permlane16_swap_b32 v234, v245\n" \
"v_mfma_f32_16x16x32_bf16 v[100:103], a[88:91], a[216:219], v[100:103]\n" \
"ds_read_b64_tr_b16 a[112:113], v210 offset:0\n" \
"v_add_f32 v245, v245, v234\n" \
"v_mfma_f32_16x16x32_bf16 v[100:103], a[92:95], a[220:223], v[100:103]\n" \
"global_load_lds_dwordx4 v252, s[44:45] offset:3072\n" \
"v_mov_b32 v234, v245\n" \
"v_mfma_f32_16x16x32_bf16 v[60:63], a[200:203], a[88:91], v[60:63]\n" \
"s_add_u32 s44, s44, 0x100\n" \
"s_addc_u32 s45, s45, 0\n" \
"ds_read_b64_tr_b16 a[114:115], v210 offset:1024\n" \
"v_mfma_f32_16x16x32_bf16 v[60:63], a[204:207], a[92:95], v[60:63]\n" \
"ds_read_b64_tr_b16 a[116:117], v210 offset:12288\n" \
"v_permlane32_swap_b32 v234, v245\n" \
"v_mfma_f32_16x16x32_bf16 v[68:71], a[200:203], a[192:195], v[68:71]\n" \
"s_add_u32 m0, s84, 90112\n" \
"ds_read_b128 a[88:91], v205 offset:8192\n" \
"v_add_f32 v245, v245, v234\n" \
"v_mfma_f32_16x16x32_bf16 v[68:71], a[204:207], a[196:199], v[68:71]\n" \
"global_load_lds_dwordx4 v253, s[48:49] offset:-4096\n" \
"v_mfma_f32_16x16x32_bf16 v[108:111], a[192:195], a[216:219], v[108:111]\n" \
"ds_read_b128 a[92:95], v206 offset:8192\n" \
"v_mfma_f32_16x16x32_bf16 v[108:111], a[196:199], a[220:223], v[108:111]\n" \
"ds_read_b64_tr_b16 a[118:119], v210 offset:13312\n" \
"v_mfma_f32_16x16x32_bf16 v[104:107], a[192:195], a[208:211], v[104:107]\n" \
"v_rcp_f32 v234, v245\n" \
"v_mfma_f32_16x16x32_bf16 v[104:107], a[196:199], a[212:215], v[104:107]\n" \
"s_waitcnt lgkmcnt(2)\n" \
"global_load_lds_dwordx4 v253, s[48:49] offset:-2048\n" \
"v_mul_f32 v245, 0x4d800000, v245\n" \
"v_mfma_f32_16x16x32_bf16 v[40:43], a[96:99], a[72:75], v[40:43]\n" \
"ds_read_b128 a[192:195], v205 offset:12288\n" \
"v_add_u32 v235, 0x1c90080, v249\n" \
"v_mfma_f32_16x16x32_bf16 v[40:43], a[100:103], a[76:79], v[40:43]\n" \
"ds_read_b128 a[196:199], v206 offset:12288\n" \
"v_cndmask_b32 v235, v249, v235, s[16:17]\n" \
"v_mfma_f32_16x16x32_bf16 v[0:3], a[104:107], a[72:75], v[0:3]\n" \
"s_add_u32 s78, s78, 532480\n" \
"ds_read_b64_tr_b16 a[208:209], v209 offset:4096\n" \
"v_mul_f32 v160, v160, v234\n" \
"v_mfma_f32_16x16x32_bf16 v[0:3], a[108:111], a[76:79], v[0:3]\n" \
"s_waitcnt lgkmcnt(3)\n" \
"global_load_lds_dwordx4 v253, s[48:49] offset:0\n" \
"v_mul_f32 v161, v161, v234\n" \
"v_mfma_f32_16x16x32_bf16 v[4:7], a[112:115], a[72:75], v[4:7]\n" \
"s_add_u32 s48, s48, 0xc0000\n" \
"s_addc_u32 s49, s49, 0\n" \
"ds_read_b64_tr_b16 a[210:211], v209 offset:5120\n" \
"v_mfma_f32_16x16x32_bf16 v[4:7], a[116:119], a[76:79], v[4:7]\n" \
"ds_read_b64_tr_b16 a[212:213], v209 offset:16384\n" \
"v_mul_f32 v162, v162, v234\n" \
"v_mfma_f32_16x16x32_bf16 v[12:15], a[112:115], a[80:83], v[12:15]\n" \
"ds_read_b64_tr_b16 a[214:215], v209 offset:17408\n" \
"v_mul_f32 v163, v163, v234\n" \
"v_mfma_f32_16x16x32_bf16 v[12:15], a[116:119], a[84:87], v[12:15]\n" \
"global_load_lds_dwordx4 v253, s[50:51] offset:-3072\n" \
"v_mul_f32 v164, v164, v234\n" \
"v_mfma_f32_16x16x32_bf16 v[8:11], a[104:107], a[80:83], v[8:11]\n" \
"ds_read_b64_tr_b16 a[216:217], v210 offset:4096\n" \
"v_mul_f32 v165, v165, v234\n" \
"v_mfma_f32_16x16x32_bf16 v[8:11], a[108:111], a[84:87], v[8:11]\n" \
"ds_read_b64_tr_b16 a[218:219], v210 offset:5120\n" \
"v_mul_f32 v166, v166, v234\n" \
"v_mfma_f32_16x16x32_bf16 v[48:51], a[96:99], a[80:83], v[48:51]\n" \
"ds_read_b64_tr_b16 a[220:221], v210 offset:16384\n" \
"v_mul_f32 v167, v167, v234\n" \
"v_mfma_f32_16x16x32_bf16 v[48:51], a[100:103], a[84:87], v[48:51]\n" \
"global_load_lds_dwordx4 v253, s[50:51] offset:-1024\n" \
"v_fma_f32 v245, v160, v161, v245\n" \
"v_mfma_f32_16x16x32_bf16 v[56:59], a[96:99], a[88:91], v[56:59]\n" \
"ds_read_b64_tr_b16 a[222:223], v210 offset:17408\n" \
"v_cvt_pk_bf16_f32 v160, v160, v161\n" \
"v_mfma_f32_16x16x32_bf16 v[56:59], a[100:103], a[92:95], v[56:59]\n" \
"ds_read_b64_tr_b16 a[200:201], v210 offset:2048\n" \
"v_fma_f32 v245, v162, v163, v245\n" \
"v_mfma_f32_16x16x32_bf16 v[16:19], a[104:107], a[88:91], v[16:19]\n" \
"ds_read_b64_tr_b16 a[202:203], v210 offset:3072\n" \
"v_cvt_pk_bf16_f32 v161, v162, v163\n" \
"v_mfma_f32_16x16x32_bf16 v[16:19], a[108:111], a[92:95], v[16:19]\n" \
"global_load_lds_dwordx4 v253, s[50:51] offset:1024\n" \
"v_fma_f32 v245, v164, v165, v245\n" \
"v_mfma_f32_16x16x32_bf16 v[20:23], a[112:115], a[88:91], v[20:23]\n" \
"s_add_u32 s50, s50, 0xc0000\n" \
"s_addc_u32 s51, s51, 0\n" \
"ds_read_b64_tr_b16 a[204:205], v210 offset:14336\n" \
"v_mfma_f32_16x16x32_bf16 v[20:23], a[116:119], a[92:95], v[20:23]\n" \
"s_waitcnt lgkmcnt(3)\n" \
"ds_read_b64_tr_b16 a[206:207], v210 offset:15360\n" \
"v_cvt_pk_bf16_f32 v162, v164, v165\n" \
"v_mfma_f32_16x16x32_bf16 v[28:31], a[112:115], a[192:195], v[28:31]\n" \
"v_add_u32 v205, 0x1d000, v205\n" \
"v_add_u32 v206, 0x1d000, v206\n" \
"v_mfma_f32_16x16x32_bf16 v[28:31], a[116:119], a[196:199], v[28:31]\n" \
"v_add_u32 v209, 0x1d000, v209\n" \
"v_add_u32 v210, 0x1d000, v210\n" \
"v_mfma_f32_16x16x32_bf16 v[24:27], a[104:107], a[192:195], v[24:27]\n" \
"v_fma_f32 v245, v166, v167, v245\n" \
"v_cvt_pk_bf16_f32 v163, v166, v167\n" \
"v_mfma_f32_16x16x32_bf16 v[24:27], a[108:111], a[196:199], v[24:27]\n" \
"s_waitcnt vmcnt(12) lgkmcnt(0)\n" \
"buffer_store_dwordx4 v[160:163], v235, s[56:59], s78 offen offset:0 sc1\n" \
"v_mul_f32 v168, v168, v234\n" \
"v_mul_f32 v169, v169, v234\n" \
"v_mul_f32 v170, v170, v234\n" \
"v_mul_f32 v171, v171, v234\n" \
"v_mfma_f32_16x16x32_bf16 v[64:67], a[96:99], a[192:195], v[64:67]\n" \
"v_mul_f32 v172, v172, v234\n" \
"v_mul_f32 v173, v173, v234\n" \
"v_mfma_f32_16x16x32_bf16 v[64:67], a[100:103], a[196:199], v[64:67]\n" \
"s_barrier\n" \
"v_mfma_f32_16x16x32_bf16 v[80:83], a[72:75], a[208:211], v[80:83]\n" \
"ds_read_b64_tr_b16 a[96:97], v209 offset:2048\n" \
"v_mul_f32 v174, v174, v234\n" \
"v_mfma_f32_16x16x32_bf16 v[80:83], a[76:79], a[212:215], v[80:83]\n" \
"ds_read_b64_tr_b16 a[98:99], v209 offset:3072\n" \
"v_mul_f32 v175, v175, v234\n" \
"v_mfma_f32_16x16x32_bf16 v[84:87], a[72:75], a[216:219], v[84:87]\n" \
"s_add_u32 m0, s95, 0\n" \
"ds_read_b64_tr_b16 a[100:101], v209 offset:14336\n" \
"v_fma_f32 v245, v168, v169, v245\n" \
"v_mfma_f32_16x16x32_bf16 v[84:87], a[76:79], a[220:223], v[84:87]\n" \
"global_load_lds_dwordx4 v252, s[42:43] offset:0\n" \
"v_cvt_pk_bf16_f32 v168, v168, v169\n" \
"v_mfma_f32_16x16x32_bf16 v[44:47], a[200:203], a[72:75], v[44:47]\n" \
"ds_read_b64_tr_b16 a[102:103], v209 offset:15360\n" \
"v_fma_f32 v245, v170, v171, v245\n" \
"v_mfma_f32_16x16x32_bf16 v[44:47], a[204:207], a[76:79], v[44:47]\n" \
"ds_read_b128 a[72:75], v205 offset:0\n" \
"ds_read_b64_tr_b16 a[104:105], v209 offset:0\n" \
"v_mfma_f32_16x16x32_bf16 v[52:55], a[200:203], a[80:83], v[52:55]\n" \
"ds_read_b128 a[76:79], v206 offset:0\n" \
"v_cvt_pk_bf16_f32 v169, v170, v171\n" \
"v_mfma_f32_16x16x32_bf16 v[52:55], a[204:207], a[84:87], v[52:55]\n" \
"global_load_lds_dwordx4 v252, s[42:43] offset:2048\n" \
"v_fma_f32 v245, v172, v173, v245\n" \
"v_mfma_f32_16x16x32_bf16 v[92:95], a[80:83], a[216:219], v[92:95]\n" \
"s_add_u32 s42, s42, 0x80\n" \
"s_addc_u32 s43, s43, 0\n" \
"ds_read_b64_tr_b16 a[106:107], v209 offset:1024\n" \
"v_mfma_f32_16x16x32_bf16 v[92:95], a[84:87], a[220:223], v[92:95]\n" \
"ds_read_b64_tr_b16 a[108:109], v209 offset:12288\n" \
"v_cvt_pk_bf16_f32 v170, v172, v173\n" \
"v_mfma_f32_16x16x32_bf16 v[88:91], a[80:83], a[208:211], v[88:91]\n" \
"ds_read_b64_tr_b16 a[110:111], v209 offset:13312\n" \
"v_fma_f32 v245, v174, v175, v245\n" \
"v_mfma_f32_16x16x32_bf16 v[88:91], a[84:87], a[212:215], v[88:91]\n" \
"global_load_lds_dwordx4 v252, s[44:45] offset:1024\n" \
"v_cvt_pk_bf16_f32 v171, v174, v175\n" \
"v_mfma_f32_16x16x32_bf16 v[96:99], a[88:91], a[208:211], v[96:99]\n" \
"ds_read_b128 a[80:83], v205 offset:4096\n" \
"v_mfma_f32_16x16x32_bf16 v[96:99], a[92:95], a[212:215], v[96:99]\n" \
"buffer_store_dwordx4 v[168:171], v235, s[56:59], s78 offen offset:2048 sc1\n" \
"ds_read_b128 a[84:87], v206 offset:4096\n" \
"s_add_u32 s78, s78, 0x1000\n" \
"v_mul_f32 v176, v176, v234\n" \
"v_mul_f32 v177, v177, v234\n" \
"v_mfma_f32_16x16x32_bf16 v[100:103], a[88:91], a[216:219], v[100:103]\n" \
"ds_read_b64_tr_b16 a[112:113], v210 offset:0\n" \
"v_mul_f32 v178, v178, v234\n" \
"v_mfma_f32_16x16x32_bf16 v[100:103], a[92:95], a[220:223], v[100:103]\n" \
"global_load_lds_dwordx4 v252, s[44:45] offset:3072\n" \
"v_mul_f32 v179, v179, v234\n" \
"v_mfma_f32_16x16x32_bf16 v[60:63], a[200:203], a[88:91], v[60:63]\n" \
"s_add_u32 s44, s44, 0x80\n" \
"s_addc_u32 s45, s45, 0\n" \
"ds_read_b64_tr_b16 a[114:115], v210 offset:1024\n" \
"v_mfma_f32_16x16x32_bf16 v[60:63], a[204:207], a[92:95], v[60:63]\n" \
"ds_read_b64_tr_b16 a[116:117], v210 offset:12288\n" \
"v_mul_f32 v180, v180, v234\n" \
"v_mfma_f32_16x16x32_bf16 v[68:71], a[200:203], a[192:195], v[68:71]\n" \
"s_add_u32 m0, s84, 24576\n" \
"ds_read_b128 a[88:91], v205 offset:8192\n" \
"v_mul_f32 v181, v181, v234\n" \
"v_mfma_f32_16x16x32_bf16 v[68:71], a[204:207], a[196:199], v[68:71]\n" \
"global_load_lds_dwordx4 v253, s[48:49] offset:-4096\n" \
"v_mul_f32 v182, v182, v234\n" \
"v_mfma_f32_16x16x32_bf16 v[108:111], a[192:195], a[216:219], v[108:111]\n" \
"ds_read_b128 a[92:95], v206 offset:8192\n" \
"v_mul_f32 v183, v183, v234\n" \
"v_mfma_f32_16x16x32_bf16 v[108:111], a[196:199], a[220:223], v[108:111]\n" \
"ds_read_b64_tr_b16 a[118:119], v210 offset:13312\n" \
"v_fma_f32 v245, v176, v177, v245\n" \
"v_mfma_f32_16x16x32_bf16 v[104:107], a[192:195], a[208:211], v[104:107]\n" \
"v_cvt_pk_bf16_f32 v176, v176, v177\n" \
"v_fma_f32 v245, v178, v179, v245\n" \
"v_mfma_f32_16x16x32_bf16 v[104:107], a[196:199], a[212:215], v[104:107]\n" \
"s_waitcnt lgkmcnt(2)\n" \
"global_load_lds_dwordx4 v253, s[48:49] offset:-2048\n" \
"v_cvt_pk_bf16_f32 v177, v178, v179\n" \
"v_mfma_f32_16x16x32_bf16 v[40:43], a[96:99], a[72:75], v[40:43]\n" \
"ds_read_b128 a[192:195], v205 offset:12288\n" \
"v_fma_f32 v245, v180, v181, v245\n" \
"v_mfma_f32_16x16x32_bf16 v[40:43], a[100:103], a[76:79], v[40:43]\n" \
"ds_read_b128 a[196:199], v206 offset:12288\n" \
"v_cvt_pk_bf16_f32 v178, v180, v181\n" \
"v_mfma_f32_16x16x32_bf16 v[0:3], a[104:107], a[72:75], v[0:3]\n" \
"ds_read_b64_tr_b16 a[208:209], v209 offset:4096\n" \
"v_fma_f32 v245, v182, v183, v245\n" \
"v_mfma_f32_16x16x32_bf16 v[0:3], a[108:111], a[76:79], v[0:3]\n" \
"s_waitcnt lgkmcnt(3)\n" \
"global_load_lds_dwordx4 v253, s[48:49] offset:0\n" \
"v_cvt_pk_bf16_f32 v179, v182, v183\n" \
"v_mfma_f32_16x16x32_bf16 v[4:7], a[112:115], a[72:75], v[4:7]\n" \
"s_add_u32 s48, s48, 0x60000\n" \
"s_addc_u32 s49, s49, 0\n" \
"ds_read_b64_tr_b16 a[210:211], v209 offset:5120\n" \
"v_mfma_f32_16x16x32_bf16 v[4:7], a[116:119], a[76:79], v[4:7]\n" \
"buffer_store_dwordx4 v[176:179], v235, s[56:59], s78 offen offset:0 sc1\n" \
"ds_read_b64_tr_b16 a[212:213], v209 offset:16384\n" \
"v_mul_f32 v184, v184, v234\n" \
"v_mul_f32 v185, v185, v234\n" \
"v_mul_f32 v186, v186, v234\n" \
"v_mfma_f32_16x16x32_bf16 v[12:15], a[112:115], a[80:83], v[12:15]\n" \
"ds_read_b64_tr_b16 a[214:215], v209 offset:17408\n" \
"v_mul_f32 v187, v187, v234\n" \
"v_mfma_f32_16x16x32_bf16 v[12:15], a[116:119], a[84:87], v[12:15]\n" \
"global_load_lds_dwordx4 v253, s[50:51] offset:-3072\n" \
"v_mul_f32 v188, v188, v234\n" \
"v_mfma_f32_16x16x32_bf16 v[8:11], a[104:107], a[80:83], v[8:11]\n" \
"ds_read_b64_tr_b16 a[216:217], v210 offset:4096\n" \
"v_mul_f32 v189, v189, v234\n" \
"v_mfma_f32_16x16x32_bf16 v[8:11], a[108:111], a[84:87], v[8:11]\n" \
"ds_read_b64_tr_b16 a[218:219], v210 offset:5120\n" \
"v_mul_f32 v190, v190, v234\n" \
"v_mfma_f32_16x16x32_bf16 v[48:51], a[96:99], a[80:83], v[48:51]\n" \
"ds_read_b64_tr_b16 a[220:221], v210 offset:16384\n" \
"v_mul_f32 v191, v191, v234\n" \
"v_mfma_f32_16x16x32_bf16 v[48:51], a[100:103], a[84:87], v[48:51]\n" \
"global_load_lds_dwordx4 v253, s[50:51] offset:-1024\n" \
"v_fma_f32 v245, v184, v185, v245\n" \
"v_mfma_f32_16x16x32_bf16 v[56:59], a[96:99], a[88:91], v[56:59]\n" \
"ds_read_b64_tr_b16 a[222:223], v210 offset:17408\n" \
"v_cvt_pk_bf16_f32 v184, v184, v185\n" \
"v_mfma_f32_16x16x32_bf16 v[56:59], a[100:103], a[92:95], v[56:59]\n" \
"ds_read_b64_tr_b16 a[200:201], v210 offset:2048\n" \
"v_fma_f32 v245, v186, v187, v245\n" \
"v_mfma_f32_16x16x32_bf16 v[16:19], a[104:107], a[88:91], v[16:19]\n" \
"ds_read_b64_tr_b16 a[202:203], v210 offset:3072\n" \
"v_cvt_pk_bf16_f32 v185, v186, v187\n" \
"v_mfma_f32_16x16x32_bf16 v[16:19], a[108:111], a[92:95], v[16:19]\n" \
"global_load_lds_dwordx4 v253, s[50:51] offset:1024\n" \
"v_fma_f32 v245, v188, v189, v245\n" \
"v_mfma_f32_16x16x32_bf16 v[20:23], a[112:115], a[88:91], v[20:23]\n" \
"s_add_u32 s50, s50, 0x60000\n" \
"s_addc_u32 s51, s51, 0\n" \
"ds_read_b64_tr_b16 a[204:205], v210 offset:14336\n" \
"v_mfma_f32_16x16x32_bf16 v[20:23], a[116:119], a[92:95], v[20:23]\n" \
"s_waitcnt lgkmcnt(3)\n" \
"ds_read_b64_tr_b16 a[206:207], v210 offset:15360\n" \
"v_cvt_pk_bf16_f32 v186, v188, v189\n" \
"v_mfma_f32_16x16x32_bf16 v[28:31], a[112:115], a[192:195], v[28:31]\n" \
"v_add_u32 v205, 0xffff3000, v205\n" \
"v_add_u32 v206, 0xffff3000, v206\n" \
"v_mfma_f32_16x16x32_bf16 v[28:31], a[116:119], a[196:199], v[28:31]\n" \
"v_add_u32 v209, 0xffff3000, v209\n" \
"v_add_u32 v210, 0xffff3000, v210\n" \
"v_mfma_f32_16x16x32_bf16 v[24:27], a[104:107], a[192:195], v[24:27]\n" \
"v_fma_f32 v245, v190, v191, v245\n" \
"v_cvt_pk_bf16_f32 v187, v190, v191\n" \
"v_mfma_f32_16x16x32_bf16 v[24:27], a[108:111], a[196:199], v[24:27]\n" \
"s_waitcnt vmcnt(13) lgkmcnt(0)\n" \
"buffer_store_dwordx4 v[184:187], v235, s[56:59], s78 offen offset:2048 sc1\n" \
"v_cmp_class_f32 vcc, v245, s94\n" \
"s_sub_u32 s78, s78, 536576\n" \
"v_mfma_f32_16x16x32_bf16 v[64:67], a[96:99], a[192:195], v[64:67]\n" \
"s_or_b64 s[92:93], s[92:93], vcc\n" \
"s_cbranch_scc1 Lexact_%=\n" \
"Lexr_%=:\n" \
"v_mfma_f32_16x16x32_bf16 v[64:67], a[100:103], a[196:199], v[64:67]\n" \
"s_barrier\n" \
"v_mfma_f32_16x16x32_bf16 v[80:83], a[72:75], a[208:211], v[80:83]\n" \
"ds_read_b64_tr_b16 a[96:97], v209 offset:2048\n" \
"v_mfma_f32_16x16x32_bf16 v[80:83], a[76:79], a[212:215], v[80:83]\n" \
"ds_read_b64_tr_b16 a[98:99], v209 offset:3072\n" \
"v_mfma_f32_16x16x32_bf16 v[84:87], a[72:75], a[216:219], v[84:87]\n" \
"Lpg9183_%=:\n" \
"Lst1x3_%=:\n" \
"s_cbranch_execz Lpg11447_%=\n" \
"s_add_u32 m0, s95, 118784\n" \
"ds_read_b64_tr_b16 a[100:101], v209 offset:14336\n" \
"v_mfma_f32_16x16x32_bf16 v[84:87], a[76:79], a[220:223], v[84:87]\n" \
"global_load_lds_dwordx4 v252, s[42:43] offset:0\n" \
"v_mfma_f32_16x16x32_bf16 v[44:47], a[200:203], a[72:75], v[44:47]\n" \
"ds_read_b64_tr_b16 a[102:103], v209 offset:15360\n" \
"v_mfma_f32_16x16x32_bf16 v[44:47], a[204:207], a[76:79], v[44:47]\n" \
"ds_read_b128 a[72:75], v205 offset:0\n" \
"ds_read_b64_tr_b16 a[104:105], v209 offset:0\n" \
"v_mfma_f32_16x16x32_bf16 v[52:55], a[200:203], a[80:83], v[52:55]\n" \
"ds_read_b128 a[76:79], v206 offset:0\n" \
"v_mfma_f32_16x16x32_bf16 v[52:55], a[204:207], a[84:87], v[52:55]\n" \
"global_load_lds_dwordx4 v252, s[42:43] offset:2048\n" \
"v_mfma_f32_16x16x32_bf16 v[92:95], a[80:83], a[216:219], v[92:95]\n" \
"s_add_u32 s42, s42, 0x80\n" \
"s_addc_u32 s43, s43, 0\n" \
"ds_read_b64_tr_b16 a[106:107], v209 offset:1024\n" \
"v_mfma_f32_16x16x32_bf16 v[92:95], a[84:87], a[220:223], v[92:95]\n" \
"ds_read_b64_tr_b16 a[108:109], v209 offset:12288\n" \
"v_mfma_f32_16x16x32_bf16 v[88:91], a[80:83], a[208:211], v[88:91]\n" \
"ds_read_b64_tr_b16 a[110:111], v209 offset:13312\n" \
"v_mfma_f32_16x16x32_bf16 v[88:91], a[84:87], a[212:215], v[88:91]\n" \
"global_load_lds_dwordx4 v252, s[44:45] offset:1024\n" \
"v_mfma_f32_16x16x32_bf16 v[96:99], a[88:91], a[208:211], v[96:99]\n" \
"ds_read_b128 a[80:83], v205 offset:4096\n" \
"v_mfma_f32_16x16x32_bf16 v[96:99], a[92:95], a[212:215], v[96:99]\n" \
"ds_read_b128 a[84:87], v206 offset:4096\n" \
"v_mfma_f32_16x16x32_bf16 v[100:103], a[88:91], a[216:219], v[100:103]\n" \
"ds_read_b64_tr_b16 a[112:113], v210 offset:0\n" \
"v_mfma_f32_16x16x32_bf16 v[100:103], a[92:95], a[220:223], v[100:103]\n" \
"global_load_lds_dwordx4 v252, s[44:45] offset:3072\n" \
"v_mfma_f32_16x16x32_bf16 v[60:63], a[200:203], a[88:91], v[60:63]\n" \
"s_add_u32 s44, s44, 0x80\n" \
"s_addc_u32 s45, s45, 0\n" \
"ds_read_b64_tr_b16 a[114:115], v210 offset:1024\n" \
"v_mfma_f32_16x16x32_bf16 v[60:63], a[204:207], a[92:95], v[60:63]\n" \
"ds_read_b64_tr_b16 a[116:117], v210 offset:12288\n" \
"v_mfma_f32_16x16x32_bf16 v[68:71], a[200:203], a[192:195], v[68:71]\n" \
"ds_read_b128 a[88:91], v205 offset:8192\n" \
"s_add_u32 m0, s84, 143360\n" \
"v_mfma_f32_16x16x32_bf16 v[68:71], a[204:207], a[196:199], v[68:71]\n" \
"global_load_lds_dwordx4 v253, s[48:49] offset:-4096\n" \
"v_mfma_f32_16x16x32_bf16 v[108:111], a[192:195], a[216:219], v[108:111]\n" \
"ds_read_b128 a[92:95], v206 offset:8192\n" \
"v_mfma_f32_16x16x32_bf16 v[108:111], a[196:199], a[220:223], v[108:111]\n" \
"ds_read_b64_tr_b16 a[118:119], v210 offset:13312\n" \
"v_mfma_f32_16x16x32_bf16 v[104:107], a[192:195], a[208:211], v[104:107]\n" \
"v_mfma_f32_16x16x32_bf16 v[104:107], a[196:199], a[212:215], v[104:107]\n" \
"s_waitcnt lgkmcnt(2)\n" \
"global_load_lds_dwordx4 v253, s[48:49] offset:-2048\n" \
"v_mfma_f32_16x16x32_bf16 v[40:43], a[96:99], a[72:75], v[40:43]\n" \
"ds_read_b128 a[192:195], v205 offset:12288\n" \
"v_mfma_f32_16x16x32_bf16 v[40:43], a[100:103], a[76:79], v[40:43]\n" \
"ds_read_b128 a[196:199], v206 offset:12288\n" \
"v_mfma_f32_16x16x32_bf16 v[0:3], a[104:107], a[72:75], v[0:3]\n" \
"ds_read_b64_tr_b16 a[208:209], v209 offset:4096\n" \
"v_mfma_f32_16x16x32_bf16 v[0:3], a[108:111], a[76:79], v[0:3]\n" \
"s_waitcnt lgkmcnt(3)\n" \
"global_load_lds_dwordx4 v253, s[48:49] offset:0\n" \
"v_mfma_f32_16x16x32_bf16 v[4:7], a[112:115], a[72:75], v[4:7]\n" \
"s_add_u32 s48, s48, 0x60000\n" \
"s_addc_u32 s49, s49, 0\n" \
"ds_read_b64_tr_b16 a[210:211], v209 offset:5120\n" \
"v_mfma_f32_16x16x32_bf16 v[4:7], a[116:119], a[76:79], v[4:7]\n" \
"ds_read_b64_tr_b16 a[212:213], v209 offset:16384\n" \
"v_mfma_f32_16x16x32_bf16 v[12:15], a[112:115], a[80:83], v[12:15]\n" \
"ds_read_b64_tr_b16 a[214:215], v209 offset:17408\n" \
"v_mfma_f32_16x16x32_bf16 v[12:15], a[116:119], a[84:87], v[12:15]\n" \
"global_load_lds_dwordx4 v253, s[50:51] offset:-3072\n" \
"v_mfma_f32_16x16x32_bf16 v[8:11], a[104:107], a[80:83], v[8:11]\n" \
"ds_read_b64_tr_b16 a[216:217], v210 offset:4096\n" \
"v_mfma_f32_16x16x32_bf16 v[8:11], a[108:111], a[84:87], v[8:11]\n" \
"ds_read_b64_tr_b16 a[218:219], v210 offset:5120\n" \
"v_mfma_f32_16x16x32_bf16 v[48:51], a[96:99], a[80:83], v[48:51]\n" \
"ds_read_b64_tr_b16 a[220:221], v210 offset:16384\n" \
"v_mfma_f32_16x16x32_bf16 v[48:51], a[100:103], a[84:87], v[48:51]\n" \
"global_load_lds_dwordx4 v253, s[50:51] offset:-1024\n" \
"v_mfma_f32_16x16x32_bf16 v[56:59], a[96:99], a[88:91], v[56:59]\n" \
"ds_read_b64_tr_b16 a[222:223], v210 offset:17408\n" \
"v_mfma_f32_16x16x32_bf16 v[56:59], a[100:103], a[92:95], v[56:59]\n" \
"ds_read_b64_tr_b16 a[200:201], v210 offset:2048\n" \
"v_mfma_f32_16x16x32_bf16 v[16:19], a[104:107], a[88:91], v[16:19]\n" \
"ds_read_b64_tr_b16 a[202:203], v210 offset:3072\n" \
"v_mfma_f32_16x16x32_bf16 v[16:19], a[108:111], a[92:95], v[16:19]\n" \
"global_load_lds_dwordx4 v253, s[50:51] offset:1024\n" \
"v_mfma_f32_16x16x32_bf16 v[20:23], a[112:115], a[88:91], v[20:23]\n" \
"s_add_u32 s50, s50, 0x60000\n" \
"s_addc_u32 s51, s51, 0\n" \
"ds_read_b64_tr_b16 a[204:205], v210 offset:14336\n" \
"v_mfma_f32_16x16x32_bf16 v[20:23], a[116:119], a[92:95], v[20:23]\n" \
"s_waitcnt lgkmcnt(3)\n" \
"ds_read_b64_tr_b16 a[206:207], v210 offset:15360\n" \
"v_mfma_f32_16x16x32_bf16 v[28:31], a[112:115], a[192:195], v[28:31]\n" \
"v_add_u32 v205, 0xffff0000, v205\n" \
"v_add_u32 v206, 0xffff0000, v206\n" \
"v_mfma_f32_16x16x32_bf16 v[28:31], a[116:119], a[196:199], v[28:31]\n" \
"v_add_u32 v209, 0xffff0000, v209\n" \
"v_add_u32 v210, 0xffff0000, v210\n" \
"v_mfma_f32_16x16x32_bf16 v[24:27], a[104:107], a[192:195], v[24:27]\n" \
"s_lshl_b32 s70, s63, 7\n" \
"s_lshl_b32 s71, s62, 2\n" \
"v_mfma_f32_16x16x32_bf16 v[24:27], a[108:111], a[196:199], v[24:27]\n" \
"s_waitcnt vmcnt(10) lgkmcnt(0)\n" \
"s_add_u32 s71, s71, 65589248\n" \
"s_add_u32 s72, s71, s70\n" \
"v_mfma_f32_16x16x32_bf16 v[64:67], a[96:99], a[192:195], v[64:67]\n" \
"s_sub_u32 s71, s71, s70\n" \
"s_add_u32 s71, s71, 1920\n" \
"v_mov_b32 v236, s60\n" \
"v_mfma_f32_16x16x32_bf16 v[64:67], a[100:103], a[196:199], v[64:67]\n" \
"s_barrier\n" \
"v_mfma_f32_16x16x32_bf16 v[80:83], a[72:75], a[208:211], v[80:83]\n" \
"ds_read_b64_tr_b16 a[96:97], v209 offset:2048\n" \
"v_lshlrev_b32 v237, 28, s61\n" \
"v_mfma_f32_16x16x32_bf16 v[80:83], a[76:79], a[212:215], v[80:83]\n" \
"buffer_store_dword v236, v237, s[56:59], s72 offen sc1\n" \
"ds_read_b64_tr_b16 a[98:99], v209 offset:3072\n" \
"v_mfma_f32_16x16x32_bf16 v[84:87], a[72:75], a[216:219], v[84:87]\n" \
"s_add_u32 m0, s95, 65536\n" \
"ds_read_b64_tr_b16 a[100:101], v209 offset:14336\n" \
"v_mfma_f32_16x16x32_bf16 v[84:87], a[76:79], a[220:223], v[84:87]\n" \
"buffer_store_dword v236, v237, s[56:59], s71 offen sc1\n" \
"global_load_lds_dwordx4 v252, s[42:43] offset:0\n" \
"v_mfma_f32_16x16x32_bf16 v[44:47], a[200:203], a[72:75], v[44:47]\n" \
"s_lshl_b32 s87, s85, 12\n" \
"ds_read_b64_tr_b16 a[102:103], v209 offset:15360\n" \
"v_mfma_f32_16x16x32_bf16 v[44:47], a[204:207], a[76:79], v[44:47]\n" \
"s_and_b32 s89, s61, 1\n" \
"ds_read_b128 a[72:75], v205 offset:0\n" \
"ds_read_b64_tr_b16 a[104:105], v209 offset:0\n" \
"v_mfma_f32_16x16x32_bf16 v[52:55], a[200:203], a[80:83], v[52:55]\n" \
"ds_read_b128 a[76:79], v206 offset:0\n" \
"s_lshl_b32 s89, s89, 11\n" \
"v_mfma_f32_16x16x32_bf16 v[52:55], a[204:207], a[84:87], v[52:55]\n" \
"global_load_lds_dwordx4 v252, s[42:43] offset:2048\n" \
"v_mfma_f32_16x16x32_bf16 v[92:95], a[80:83], a[216:219], v[92:95]\n" \
"s_add_u32 s42, s42, 0x80\n" \
"s_addc_u32 s43, s43, 0\n" \
"ds_read_b64_tr_b16 a[106:107], v209 offset:1024\n" \
"v_mfma_f32_16x16x32_bf16 v[92:95], a[84:87], a[220:223], v[92:95]\n" \
"s_add_u32 s87, s87, s89\n" \
"ds_read_b64_tr_b16 a[108:109], v209 offset:12288\n" \
"v_mfma_f32_16x16x32_bf16 v[88:91], a[80:83], a[208:211], v[88:91]\n" \
"s_add_u32 s87, s87, 52740096\n" \
"ds_read_b64_tr_b16 a[110:111], v209 offset:13312\n" \
"v_mfma_f32_16x16x32_bf16 v[88:91], a[84:87], a[212:215], v[88:91]\n" \
"global_load_lds_dwordx4 v252, s[44:45] offset:1024\n" \
"v_mfma_f32_16x16x32_bf16 v[96:99], a[88:91], a[208:211], v[96:99]\n" \
"ds_read_b128 a[80:83], v205 offset:4096\n" \
"s_mul_i32 s89, s88, 278528\n" \
"v_mfma_f32_16x16x32_bf16 v[96:99], a[92:95], a[212:215], v[96:99]\n" \
"ds_read_b128 a[84:87], v206 offset:4096\n" \
"s_add_u32 s87, s87, s89\n" \
"v_mfma_f32_16x16x32_bf16 v[100:103], a[88:91], a[216:219], v[100:103]\n" \
"s_lshr_b32 s89, s61, 1\n" \
"ds_read_b64_tr_b16 a[112:113], v210 offset:0\n" \
"v_mfma_f32_16x16x32_bf16 v[100:103], a[92:95], a[220:223], v[100:103]\n" \
"global_load_lds_dwordx4 v252, s[44:45] offset:3072\n" \
"v_mfma_f32_16x16x32_bf16 v[60:63], a[200:203], a[88:91], v[60:63]\n" \
"s_add_u32 s44, s44, 0x80\n" \
"s_addc_u32 s45, s45, 0\n" \
"ds_read_b64_tr_b16 a[114:115], v210 offset:1024\n" \
"v_mfma_f32_16x16x32_bf16 v[60:63], a[204:207], a[92:95], v[60:63]\n" \
"s_lshl_b32 s90, s89, 8\n" \
"ds_read_b64_tr_b16 a[116:117], v210 offset:12288\n" \
"v_mfma_f32_16x16x32_bf16 v[68:71], a[200:203], a[192:195], v[68:71]\n" \
"ds_read_b128 a[88:91], v205 offset:8192\n" \
"s_add_u32 m0, s84, 90112\n" \
"v_mfma_f32_16x16x32_bf16 v[68:71], a[204:207], a[196:199], v[68:71]\n" \
"global_load_lds_dwordx4 v253, s[48:49] offset:-4096\n" \
"v_mfma_f32_16x16x32_bf16 v[108:111], a[192:195], a[216:219], v[108:111]\n" \
"ds_read_b128 a[92:95], v206 offset:8192\n" \
"s_add_u32 s87, s87, s90\n" \
"v_mfma_f32_16x16x32_bf16 v[108:111], a[196:199], a[220:223], v[108:111]\n" \
"s_lshl_b32 s46, s88, 4\n" \
"ds_read_b64_tr_b16 a[118:119], v210 offset:13312\n" \
"v_mfma_f32_16x16x32_bf16 v[104:107], a[192:195], a[208:211], v[104:107]\n" \
"s_add_u32 s46, s46, s85\n" \
"v_mfma_f32_16x16x32_bf16 v[104:107], a[196:199], a[212:215], v[104:107]\n" \
"s_waitcnt lgkmcnt(2)\n" \
"global_load_lds_dwordx4 v253, s[48:49] offset:-2048\n" \
"v_mfma_f32_16x16x32_bf16 v[40:43], a[96:99], a[72:75], v[40:43]\n" \
"ds_read_b128 a[192:195], v205 offset:12288\n" \
"s_lshl_b32 s46, s46, 1\n" \
"v_mfma_f32_16x16x32_bf16 v[40:43], a[100:103], a[76:79], v[40:43]\n" \
"ds_read_b128 a[196:199], v206 offset:12288\n" \
"s_add_u32 s46, s46, s89\n" \
"v_mfma_f32_16x16x32_bf16 v[0:3], a[104:107], a[72:75], v[0:3]\n" \
"s_lshl_b32 s46, s46, 14\n" \
"ds_read_b64_tr_b16 a[208:209], v209 offset:4096\n" \
"v_mfma_f32_16x16x32_bf16 v[0:3], a[108:111], a[76:79], v[0:3]\n" \
"s_waitcnt lgkmcnt(3)\n" \
"global_load_lds_dwordx4 v253, s[48:49] offset:0\n" \
"v_mfma_f32_16x16x32_bf16 v[4:7], a[112:115], a[72:75], v[4:7]\n" \
"s_add_u32 s48, s48, 0x60000\n" \
"s_addc_u32 s49, s49, 0\n" \
"ds_read_b64_tr_b16 a[210:211], v209 offset:5120\n" \
"v_mfma_f32_16x16x32_bf16 v[4:7], a[116:119], a[76:79], v[4:7]\n" \
"s_and_b32 s90, s95, 0x1000\n" \
"ds_read_b64_tr_b16 a[212:213], v209 offset:16384\n" \
"v_mfma_f32_16x16x32_bf16 v[12:15], a[112:115], a[80:83], v[12:15]\n" \
"s_add_u32 s46, s46, s90\n" \
"ds_read_b64_tr_b16 a[214:215], v209 offset:17408\n" \
"v_mfma_f32_16x16x32_bf16 v[12:15], a[116:119], a[84:87], v[12:15]\n" \
"global_load_lds_dwordx4 v253, s[50:51] offset:-3072\n" \
"v_mfma_f32_16x16x32_bf16 v[8:11], a[104:107], a[80:83], v[8:11]\n" \
"s_add_u32 s46, s46, 57204736\n" \
"ds_read_b64_tr_b16 a[216:217], v210 offset:4096\n" \
"v_mfma_f32_16x16x32_bf16 v[8:11], a[108:111], a[84:87], v[8:11]\n" \
"s_sub_u32 s47, s46, 524288\n" \
"ds_read_b64_tr_b16 a[218:219], v210 offset:5120\n" \
"v_mfma_f32_16x16x32_bf16 v[48:51], a[96:99], a[80:83], v[48:51]\n" \
"ds_read_b64_tr_b16 a[220:221], v210 offset:16384\n" \
"v_mfma_f32_16x16x32_bf16 v[48:51], a[100:103], a[84:87], v[48:51]\n" \
"global_load_lds_dwordx4 v253, s[50:51] offset:-1024\n" \
"v_mfma_f32_16x16x32_bf16 v[56:59], a[96:99], a[88:91], v[56:59]\n" \
"ds_read_b64_tr_b16 a[222:223], v210 offset:17408\n" \
"v_mfma_f32_16x16x32_bf16 v[56:59], a[100:103], a[92:95], v[56:59]\n" \
"ds_read_b64_tr_b16 a[200:201], v210 offset:2048\n" \
"v_mfma_f32_16x16x32_bf16 v[16:19], a[104:107], a[88:91], v[16:19]\n" \
"ds_read_b64_tr_b16 a[202:203], v210 offset:3072\n" \
"v_mfma_f32_16x16x32_bf16 v[16:19], a[108:111], a[92:95], v[16:19]\n" \
"global_load_lds_dwordx4 v253, s[50:51] offset:1024\n" \
"v_mfma_f32_16x16x32_bf16 v[20:23], a[112:115], a[88:91], v[20:23]\n" \
"s_add_u32 s50, s50, 0x60000\n" \
"s_addc_u32 s51, s51, 0\n" \
"ds_read_b64_tr_b16 a[204:205], v210 offset:14336\n" \
"v_mfma_f32_16x16x32_bf16 v[20:23], a[116:119], a[92:95], v[20:23]\n" \
"s_waitcnt lgkmcnt(3)\n" \
"ds_read_b64_tr_b16 a[206:207], v210 offset:15360\n" \
"v_mfma_f32_16x16x32_bf16 v[28:31], a[112:115], a[192:195], v[28:31]\n" \
"v_add_u32 v205, 0x1d000, v205\n" \
"v_add_u32 v206, 0x1d000, v206\n" \
"v_mfma_f32_16x16x32_bf16 v[28:31], a[116:119], a[196:199], v[28:31]\n" \
"v_add_u32 v209, 0x1d000, v209\n" \
"v_add_u32 v210, 0x1d000, v210\n" \
"v_mfma_f32_16x16x32_bf16 v[24:27], a[104:107], a[192:195], v[24:27]\n" \
"s_cmp_eq_u32 s88, 0\n" \
"s_cselect_b32 s86, -1, 0\n" \
"s_cselect_b32 s47, 0x20000000, s47\n" \
"v_mfma_f32_16x16x32_bf16 v[24:27], a[108:111], a[196:199], v[24:27]\n" \
"s_waitcnt vmcnt(12) lgkmcnt(0)\n" \
"v_mfma_f32_16x16x32_bf16 v[64:67], a[96:99], a[192:195], v[64:67]\n" \
"v_mfma_f32_16x16x32_bf16 v[64:67], a[100:103], a[196:199], v[64:67]\n" \
"s_barrier\n" \
"v_mfma_f32_16x16x32_bf16 v[80:83], a[72:75], a[208:211], v[80:83]\n" \
"ds_read_b64_tr_b16 a[96:97], v209 offset:2048\n" \
"v_add_u32 v208, 0x20000000, v248\n" \
"v_mfma_f32_16x16x32_bf16 v[80:83], a[76:79], a[212:215], v[80:83]\n" \
"s_mov_b32 s12, 0xff00ff00\n" \
"ds_read_b64_tr_b16 a[98:99], v209 offset:3072\n" \
"v_mfma_f32_16x16x32_bf16 v[84:87], a[72:75], a[216:219], v[84:87]\n" \
"s_add_u32 m0, s95, 0\n" \
"ds_read_b64_tr_b16 a[100:101], v209 offset:14336\n" \
"v_mfma_f32_16x16x32_bf16 v[84:87], a[76:79], a[220:223], v[84:87]\n" \
"global_load_lds_dwordx4 v252, s[42:43] offset:0\n" \
"v_mfma_f32_16x16x32_bf16 v[44:47], a[200:203], a[72:75], v[44:47]\n" \
"s_mov_b32 s13, s12\n" \
"ds_read_b64_tr_b16 a[102:103], v209 offset:15360\n" \
"v_mfma_f32_16x16x32_bf16 v[44:47], a[204:207], a[76:79], v[44:47]\n" \
"s_mov_b32 s14, 0\n" \
"ds_read_b128 a[72:75], v205 offset:0\n" \
"ds_read_b64_tr_b16 a[104:105], v209 offset:0\n" \
"v_mfma_f32_16x16x32_bf16 v[52:55], a[200:203], a[80:83], v[52:55]\n" \
"s_mov_b32 s15, -1\n" \
"ds_read_b128 a[76:79], v206 offset:0\n" \
"v_cndmask_b32 v207, v208, v248, s[12:13]\n" \
"v_mfma_f32_16x16x32_bf16 v[52:55], a[204:207], a[84:87], v[52:55]\n" \
"global_load_lds_dwordx4 v252, s[42:43] offset:2048\n" \
"v_cndmask_b32 v223, v208, v248, s[14:15]\n" \
"v_mfma_f32_16x16x32_bf16 v[92:95], a[80:83], a[216:219], v[92:95]\n" \
"s_add_u32 s42, s42, 0x80\n" \
"s_addc_u32 s43, s43, 0\n" \
"ds_read_b64_tr_b16 a[106:107], v209 offset:1024\n" \
"v_mfma_f32_16x16x32_bf16 v[92:95], a[84:87], a[220:223], v[92:95]\n" \
"ds_read_b64_tr_b16 a[108:109], v209 offset:12288\n" \
"v_min_u32 v208, v207, v223\n" \
"v_mfma_f32_16x16x32_bf16 v[88:91], a[80:83], a[208:211], v[88:91]\n" \
"ds_read_b64_tr_b16 a[110:111], v209 offset:13312\n" \
"v_mfma_f32_16x16x32_bf16 v[88:91], a[84:87], a[212:215], v[88:91]\n" \
"global_load_lds_dwordx4 v252, s[44:45] offset:1024\n" \
"v_mfma_f32_16x16x32_bf16 v[96:99], a[88:91], a[208:211], v[96:99]\n" \
"ds_read_b128 a[80:83], v205 offset:4096\n" \
"v_mfma_f32_16x16x32_bf16 v[96:99], a[92:95], a[212:215], v[96:99]\n" \
"ds_read_b128 a[84:87], v206 offset:4096\n" \
"v_mfma_f32_16x16x32_bf16 v[100:103], a[88:91], a[216:219], v[100:103]\n" \
"ds_read_b64_tr_b16 a[112:113], v210 offset:0\n" \
"v_mfma_f32_16x16x32_bf16 v[100:103], a[92:95], a[220:223], v[100:103]\n" \
"global_load_lds_dwordx4 v252, s[44:45] offset:3072\n" \
"v_mfma_f32_16x16x32_bf16 v[60:63], a[200:203], a[88:91], v[60:63]\n" \
"s_add_u32 s44, s44, 0x80\n" \
"s_addc_u32 s45, s45, 0\n" \
"ds_read_b64_tr_b16 a[114:115], v210 offset:1024\n" \
"v_mfma_f32_16x16x32_bf16 v[60:63], a[204:207], a[92:95], v[60:63]\n" \
"ds_read_b64_tr_b16 a[116:117], v210 offset:12288\n" \
"v_mfma_f32_16x16x32_bf16 v[68:71], a[200:203], a[192:195], v[68:71]\n" \
"ds_read_b128 a[88:91], v205 offset:8192\n" \
"s_add_u32 m0, s84, 24576\n" \
"v_mfma_f32_16x16x32_bf16 v[68:71], a[204:207], a[196:199], v[68:71]\n" \
"global_load_lds_dwordx4 v253, s[48:49] offset:-4096\n" \
"v_mfma_f32_16x16x32_bf16 v[108:111], a[192:195], a[216:219], v[108:111]\n" \
"ds_read_b128 a[92:95], v206 offset:8192\n" \
"v_mfma_f32_16x16x32_bf16 v[108:111], a[196:199], a[220:223], v[108:111]\n" \
"ds_read_b64_tr_b16 a[118:119], v210 offset:13312\n" \
"v_mfma_f32_16x16x32_bf16 v[104:107], a[192:195], a[208:211], v[104:107]\n" \
"v_mfma_f32_16x16x32_bf16 v[104:107], a[196:199], a[212:215], v[104:107]\n" \
"s_waitcnt lgkmcnt(2)\n" \
"global_load_lds_dwordx4 v253, s[48:49] offset:-2048\n" \
"v_mfma_f32_16x16x32_bf16 v[40:43], a[96:99], a[72:75], v[40:43]\n" \
"s_mov_b32 s30, 65593856\n" \
"ds_read_b128 a[192:195], v205 offset:12288\n" \
"v_lshrrev_b32 v228, 1, v248\n" \
"v_mfma_f32_16x16x32_bf16 v[40:43], a[100:103], a[76:79], v[40:43]\n" \
"ds_read_b128 a[196:199], v206 offset:12288\n" \
"v_mfma_f32_16x16x32_bf16 v[0:3], a[104:107], a[72:75], v[0:3]\n" \
"ds_read_b64_tr_b16 a[208:209], v209 offset:4096\n" \
"v_mfma_f32_16x16x32_bf16 v[0:3], a[108:111], a[76:79], v[0:3]\n" \
"s_waitcnt lgkmcnt(3)\n" \
"global_load_lds_dwordx4 v253, s[48:49] offset:0\n" \
"v_mfma_f32_16x16x32_bf16 v[4:7], a[112:115], a[72:75], v[4:7]\n" \
"s_add_u32 s48, s48, 0x60000\n" \
"s_addc_u32 s49, s49, 0\n" \
"ds_read_b64_tr_b16 a[210:211], v209 offset:5120\n" \
"v_mfma_f32_16x16x32_bf16 v[4:7], a[116:119], a[76:79], v[4:7]\n" \
"ds_read_b64_tr_b16 a[212:213], v209 offset:16384\n" \
"v_mfma_f32_16x16x32_bf16 v[12:15], a[112:115], a[80:83], v[12:15]\n" \
"ds_read_b64_tr_b16 a[214:215], v209 offset:17408\n" \
"v_mfma_f32_16x16x32_bf16 v[12:15], a[116:119], a[84:87], v[12:15]\n" \
"global_load_lds_dwordx4 v253, s[50:51] offset:-3072\n" \
"v_mfma_f32_16x16x32_bf16 v[8:11], a[104:107], a[80:83], v[8:11]\n" \
"ds_read_b64_tr_b16 a[216:217], v210 offset:4096\n" \
"v_mfma_f32_16x16x32_bf16 v[8:11], a[108:111], a[84:87], v[8:11]\n" \
"ds_read_b64_tr_b16 a[218:219], v210 offset:5120\n" \
"v_mfma_f32_16x16x32_bf16 v[48:51], a[96:99], a[80:83], v[48:51]\n" \
"ds_read_b64_tr_b16 a[220:221], v210 offset:16384\n" \
"v_mfma_f32_16x16x32_bf16 v[48:51], a[100:103], a[84:87], v[48:51]\n" \
"global_load_lds_dwordx4 v253, s[50:51] offset:-1024\n" \
"v_mfma_f32_16x16x32_bf16 v[56:59], a[96:99], a[88:91], v[56:59]\n" \
"ds_read_b64_tr_b16 a[222:223], v210 offset:17408\n" \
"v_mfma_f32_16x16x32_bf16 v[56:59], a[100:103], a[92:95], v[56:59]\n" \
"ds_read_b64_tr_b16 a[200:201], v210 offset:2048\n" \
"v_mfma_f32_16x16x32_bf16 v[16:19], a[104:107], a[88:91], v[16:19]\n" \
"ds_read_b64_tr_b16 a[202:203], v210 offset:3072\n" \
"v_mfma_f32_16x16x32_bf16 v[16:19], a[108:111], a[92:95], v[16:19]\n" \
"global_load_lds_dwordx4 v253, s[50:51] offset:1024\n" \
"v_mfma_f32_16x16x32_bf16 v[20:23], a[112:115], a[88:91], v[20:23]\n" \
"s_add_u32 s50, s50, 0x60000\n" \
"s_addc_u32 s51, s51, 0\n" \
"ds_read_b64_tr_b16 a[204:205], v210 offset:14336\n" \
"v_mfma_f32_16x16x32_bf16 v[20:23], a[116:119], a[92:95], v[20:23]\n" \
"s_waitcnt lgkmcnt(3)\n" \
"ds_read_b64_tr_b16 a[206:207], v210 offset:15360\n" \
"v_mfma_f32_16x16x32_bf16 v[28:31], a[112:115], a[192:195], v[28:31]\n" \
"v_add_u32 v205, 0xffff3000, v205\n" \
"v_add_u32 v206, 0xffff3000, v206\n" \
"v_mfma_f32_16x16x32_bf16 v[28:31], a[116:119], a[196:199], v[28:31]\n" \
"v_add_u32 v209, 0xffff3000, v209\n" \
"v_add_u32 v210, 0xffff3000, v210\n" \
"v_mfma_f32_16x16x32_bf16 v[24:27], a[104:107], a[192:195], v[24:27]\n" \
"v_mfma_f32_16x16x32_bf16 v[24:27], a[108:111], a[196:199], v[24:27]\n" \
"s_waitcnt vmcnt(10) lgkmcnt(0)\n" \
"v_mfma_f32_16x16x32_bf16 v[64:67], a[96:99], a[192:195], v[64:67]\n" \
"v_mfma_f32_16x16x32_bf16 v[64:67], a[100:103], a[196:199], v[64:67]\n" \
"s_barrier\n" \
"v_mfma_f32_16x16x32_bf16 v[80:83], a[72:75], a[208:211], v[80:83]\n" \
"ds_read_b64_tr_b16 a[96:97], v209 offset:2048\n" \
"v_mfma_f32_16x16x32_bf16 v[80:83], a[76:79], a[212:215], v[80:83]\n" \
"ds_read_b64_tr_b16 a[98:99], v209 offset:3072\n" \
"v_mfma_f32_16x16x32_bf16 v[84:87], a[72:75], a[216:219], v[84:87]\n" \
"s_add_u32 m0, s95, 118784\n" \
"ds_read_b64_tr_b16 a[100:101], v209 offset:14336\n" \
"v_mfma_f32_16x16x32_bf16 v[84:87], a[76:79], a[220:223], v[84:87]\n" \
"global_load_lds_dwordx4 v252, s[42:43] offset:0\n" \
"v_mfma_f32_16x16x32_bf16 v[44:47], a[200:203], a[72:75], v[44:47]\n" \
"ds_read_b64_tr_b16 a[102:103], v209 offset:15360\n" \
"v_mfma_f32_16x16x32_bf16 v[44:47], a[204:207], a[76:79], v[44:47]\n" \
"ds_read_b128 a[72:75], v205 offset:0\n" \
"ds_read_b64_tr_b16 a[104:105], v209 offset:0\n" \
"v_mfma_f32_16x16x32_bf16 v[52:55], a[200:203], a[80:83], v[52:55]\n" \
"ds_read_b128 a[76:79], v206 offset:0\n" \
"v_mfma_f32_16x16x32_bf16 v[52:55], a[204:207], a[84:87], v[52:55]\n" \
"global_load_lds_dwordx4 v252, s[42:43] offset:2048\n" \
"v_mfma_f32_16x16x32_bf16 v[92:95], a[80:83], a[216:219], v[92:95]\n" \
"s_add_u32 s42, s42, 0x80\n" \
"s_addc_u32 s43, s43, 0\n" \
"ds_read_b64_tr_b16 a[106:107], v209 offset:1024\n" \
"v_mfma_f32_16x16x32_bf16 v[92:95], a[84:87], a[220:223], v[92:95]\n" \
"ds_read_b64_tr_b16 a[108:109], v209 offset:12288\n" \
"v_mfma_f32_16x16x32_bf16 v[88:91], a[80:83], a[208:211], v[88:91]\n" \
"ds_read_b64_tr_b16 a[110:111], v209 offset:13312\n" \
"v_mfma_f32_16x16x32_bf16 v[88:91], a[84:87], a[212:215], v[88:91]\n" \
"global_load_lds_dwordx4 v252, s[44:45] offset:1024\n" \
"v_mfma_f32_16x16x32_bf16 v[96:99], a[88:91], a[208:211], v[96:99]\n" \
"ds_read_b128 a[80:83], v205 offset:4096\n" \
"v_mfma_f32_16x16x32_bf16 v[96:99], a[92:95], a[212:215], v[96:99]\n" \
"ds_read_b128 a[84:87], v206 offset:4096\n" \
"v_mfma_f32_16x16x32_bf16 v[100:103], a[88:91], a[216:219], v[100:103]\n" \
"ds_read_b64_tr_b16 a[112:113], v210 offset:0\n" \
"v_mfma_f32_16x16x32_bf16 v[100:103], a[92:95], a[220:223], v[100:103]\n" \
"global_load_lds_dwordx4 v252, s[44:45] offset:3072\n" \
"v_mfma_f32_16x16x32_bf16 v[60:63], a[200:203], a[88:91], v[60:63]\n" \
"s_add_u32 s44, s44, 0x80\n" \
"s_addc_u32 s45, s45, 0\n" \
"ds_read_b64_tr_b16 a[114:115], v210 offset:1024\n" \
"v_mfma_f32_16x16x32_bf16 v[60:63], a[204:207], a[92:95], v[60:63]\n" \
"ds_read_b64_tr_b16 a[116:117], v210 offset:12288\n" \
"v_mfma_f32_16x16x32_bf16 v[68:71], a[200:203], a[192:195], v[68:71]\n" \
"ds_read_b128 a[88:91], v205 offset:8192\n" \
"s_add_u32 m0, s84, 143360\n" \
"v_mfma_f32_16x16x32_bf16 v[68:71], a[204:207], a[196:199], v[68:71]\n" \
"global_load_lds_dwordx4 v253, s[48:49] offset:-4096\n" \
"v_mfma_f32_16x16x32_bf16 v[108:111], a[192:195], a[216:219], v[108:111]\n" \
"ds_read_b128 a[92:95], v206 offset:8192\n" \
"v_mfma_f32_16x16x32_bf16 v[108:111], a[196:199], a[220:223], v[108:111]\n" \
"ds_read_b64_tr_b16 a[118:119], v210 offset:13312\n" \
"v_mfma_f32_16x16x32_bf16 v[104:107], a[192:195], a[208:211], v[104:107]\n" \
"v_mfma_f32_16x16x32_bf16 v[104:107], a[196:199], a[212:215], v[104:107]\n" \
"s_waitcnt lgkmcnt(2)\n" \
"global_load_lds_dwordx4 v253, s[48:49] offset:-2048\n" \
"v_mfma_f32_16x16x32_bf16 v[40:43], a[96:99], a[72:75], v[40:43]\n" \
"ds_read_b128 a[192:195], v205 offset:12288\n" \
"v_mfma_f32_16x16x32_bf16 v[40:43], a[100:103], a[76:79], v[40:43]\n" \
"ds_read_b128 a[196:199], v206 offset:12288\n" \
"v_mfma_f32_16x16x32_bf16 v[0:3], a[104:107], a[72:75], v[0:3]\n" \
"ds_read_b64_tr_b16 a[208:209], v209 offset:4096\n" \
"v_mfma_f32_16x16x32_bf16 v[0:3], a[108:111], a[76:79], v[0:3]\n" \
"s_waitcnt lgkmcnt(3)\n" \
"global_load_lds_dwordx4 v253, s[48:49] offset:0\n" \
"v_mfma_f32_16x16x32_bf16 v[4:7], a[112:115], a[72:75], v[4:7]\n" \
"s_add_u32 s48, s48, 0x60000\n" \
"s_addc_u32 s49, s49, 0\n" \
"ds_read_b64_tr_b16 a[210:211], v209 offset:5120\n" \
"v_mfma_f32_16x16x32_bf16 v[4:7], a[116:119], a[76:79], v[4:7]\n" \
"ds_read_b64_tr_b16 a[212:213], v209 offset:16384\n" \
"v_mfma_f32_16x16x32_bf16 v[12:15], a[112:115], a[80:83], v[12:15]\n" \
"ds_read_b64_tr_b16 a[214:215], v209 offset:17408\n" \
"v_mfma_f32_16x16x32_bf16 v[12:15], a[116:119], a[84:87], v[12:15]\n" \
"global_load_lds_dwordx4 v253, s[50:51] offset:-3072\n" \
"v_mfma_f32_16x16x32_bf16 v[8:11], a[104:107], a[80:83], v[8:11]\n" \
"ds_read_b64_tr_b16 a[216:217], v210 offset:4096\n" \
"v_mfma_f32_16x16x32_bf16 v[8:11], a[108:111], a[84:87], v[8:11]\n" \
"ds_read_b64_tr_b16 a[218:219], v210 offset:5120\n" \
"v_mfma_f32_16x16x32_bf16 v[48:51], a[96:99], a[80:83], v[48:51]\n" \
"ds_read_b64_tr_b16 a[220:221], v210 offset:16384\n" \
"v_mfma_f32_16x16x32_bf16 v[48:51], a[100:103], a[84:87], v[48:51]\n" \
"global_load_lds_dwordx4 v253, s[50:51] offset:-1024\n" \
"v_mfma_f32_16x16x32_bf16 v[56:59], a[96:99], a[88:91], v[56:59]\n" \
"ds_read_b64_tr_b16 a[222:223], v210 offset:17408\n" \
"v_mfma_f32_16x16x32_bf16 v[56:59], a[100:103], a[92:95], v[56:59]\n" \
"ds_read_b64_tr_b16 a[200:201], v210 offset:2048\n" \
"v_mfma_f32_16x16x32_bf16 v[16:19], a[104:107], a[88:91], v[16:19]\n" \
"ds_read_b64_tr_b16 a[202:203], v210 offset:3072\n" \
"v_mfma_f32_16x16x32_bf16 v[16:19], a[108:111], a[92:95], v[16:19]\n" \
"global_load_lds_dwordx4 v253, s[50:51] offset:1024\n" \
"v_mfma_f32_16x16x32_bf16 v[20:23], a[112:115], a[88:91], v[20:23]\n" \
"s_add_u32 s50, s50, 0x60000\n" \
"s_addc_u32 s51, s51, 0\n" \
"ds_read_b64_tr_b16 a[204:205], v210 offset:14336\n" \
"v_mfma_f32_16x16x32_bf16 v[20:23], a[116:119], a[92:95], v[20:23]\n" \
"s_waitcnt lgkmcnt(3)\n" \
"ds_read_b64_tr_b16 a[206:207], v210 offset:15360\n" \
"v_mfma_f32_16x16x32_bf16 v[28:31], a[112:115], a[192:195], v[28:31]\n" \
"v_add_u32 v205, 0xffff0000, v205\n" \
"v_add_u32 v206, 0xffff0000, v206\n" \
"v_mfma_f32_16x16x32_bf16 v[28:31], a[116:119], a[196:199], v[28:31]\n" \
"buffer_load_dwordx2 v[226:227], v228, s[56:59], s30 offen sc1\n" \
"v_mfma_f32_16x16x32_bf16 v[24:27], a[104:107], a[192:195], v[24:27]\n" \
"v_add_u32 v209, 0xffff0000, v209\n" \
"v_add_u32 v210, 0xffff0000, v210\n" \
"v_mfma_f32_16x16x32_bf16 v[24:27], a[108:111], a[196:199], v[24:27]\n" \
"s_waitcnt vmcnt(11) lgkmcnt(0)\n" \
"v_mfma_f32_16x16x32_bf16 v[64:67], a[96:99], a[192:195], v[64:67]\n" \
"v_mfma_f32_16x16x32_bf16 v[64:67], a[100:103], a[196:199], v[64:67]\n" \
"s_barrier\n" \
"v_mfma_f32_16x16x32_bf16 v[80:83], a[72:75], a[208:211], v[80:83]\n" \
"ds_read_b64_tr_b16 a[96:97], v209 offset:2048\n" \
"v_mfma_f32_16x16x32_bf16 v[80:83], a[76:79], a[212:215], v[80:83]\n" \
"ds_read_b64_tr_b16 a[98:99], v209 offset:3072\n" \
"v_mfma_f32_16x16x32_bf16 v[84:87], a[72:75], a[216:219], v[84:87]\n" \
"s_add_u32 m0, s95, 65536\n" \
"ds_read_b64_tr_b16 a[100:101], v209 offset:14336\n" \
"v_mfma_f32_16x16x32_bf16 v[84:87], a[76:79], a[220:223], v[84:87]\n" \
"global_load_lds_dwordx4 v252, s[42:43] offset:0\n" \
"v_mfma_f32_16x16x32_bf16 v[44:47], a[200:203], a[72:75], v[44:47]\n" \
"ds_read_b64_tr_b16 a[102:103], v209 offset:15360\n" \
"v_mfma_f32_16x16x32_bf16 v[44:47], a[204:207], a[76:79], v[44:47]\n" \
"ds_read_b128 a[72:75], v205 offset:0\n" \
"ds_read_b64_tr_b16 a[104:105], v209 offset:0\n" \
"v_mfma_f32_16x16x32_bf16 v[52:55], a[200:203], a[80:83], v[52:55]\n" \
"ds_read_b128 a[76:79], v206 offset:0\n" \
"v_mfma_f32_16x16x32_bf16 v[52:55], a[204:207], a[84:87], v[52:55]\n" \
"global_load_lds_dwordx4 v252, s[42:43] offset:2048\n" \
"v_mfma_f32_16x16x32_bf16 v[92:95], a[80:83], a[216:219], v[92:95]\n" \
"s_add_u32 s42, s42, 0x80\n" \
"s_addc_u32 s43, s43, 0\n" \
"ds_read_b64_tr_b16 a[106:107], v209 offset:1024\n" \
"v_mfma_f32_16x16x32_bf16 v[92:95], a[84:87], a[220:223], v[92:95]\n" \
"ds_read_b64_tr_b16 a[108:109], v209 offset:12288\n" \
"v_mfma_f32_16x16x32_bf16 v[88:91], a[80:83], a[208:211], v[88:91]\n" \
"ds_read_b64_tr_b16 a[110:111], v209 offset:13312\n" \
"v_mfma_f32_16x16x32_bf16 v[88:91], a[84:87], a[212:215], v[88:91]\n" \
"global_load_lds_dwordx4 v252, s[44:45] offset:1024\n" \
"v_mfma_f32_16x16x32_bf16 v[96:99], a[88:91], a[208:211], v[96:99]\n" \
"ds_read_b128 a[80:83], v205 offset:4096\n" \
"v_mfma_f32_16x16x32_bf16 v[96:99], a[92:95], a[212:215], v[96:99]\n" \
"ds_read_b128 a[84:87], v206 offset:4096\n" \
"v_mfma_f32_16x16x32_bf16 v[100:103], a[88:91], a[216:219], v[100:103]\n" \
"ds_read_b64_tr_b16 a[112:113], v210 offset:0\n" \
"v_mfma_f32_16x16x32_bf16 v[100:103], a[92:95], a[220:223], v[100:103]\n" \
"global_load_lds_dwordx4 v252, s[44:45] offset:3072\n" \
"v_mfma_f32_16x16x32_bf16 v[60:63], a[200:203], a[88:91], v[60:63]\n" \
"s_add_u32 s44, s44, 0x80\n" \
"s_addc_u32 s45, s45, 0\n" \
"ds_read_b64_tr_b16 a[114:115], v210 offset:1024\n" \
"v_mfma_f32_16x16x32_bf16 v[60:63], a[204:207], a[92:95], v[60:63]\n" \
"ds_read_b64_tr_b16 a[116:117], v210 offset:12288\n" \
"v_mfma_f32_16x16x32_bf16 v[68:71], a[200:203], a[192:195], v[68:71]\n" \
"ds_read_b128 a[88:91], v205 offset:8192\n" \
"s_add_u32 m0, s84, 90112\n" \
"v_mfma_f32_16x16x32_bf16 v[68:71], a[204:207], a[196:199], v[68:71]\n" \
"global_load_lds_dwordx4 v253, s[48:49] offset:-4096\n" \
"v_mfma_f32_16x16x32_bf16 v[108:111], a[192:195], a[216:219], v[108:111]\n" \
"ds_read_b128 a[92:95], v206 offset:8192\n" \
"v_mfma_f32_16x16x32_bf16 v[108:111], a[196:199], a[220:223], v[108:111]\n" \
"ds_read_b64_tr_b16 a[118:119], v210 offset:13312\n" \
"v_mfma_f32_16x16x32_bf16 v[104:107], a[192:195], a[208:211], v[104:107]\n" \
"v_mfma_f32_16x16x32_bf16 v[104:107], a[196:199], a[212:215], v[104:107]\n" \
"s_waitcnt lgkmcnt(2)\n" \
"global_load_lds_dwordx4 v253, s[48:49] offset:-2048\n" \
"v_mfma_f32_16x16x32_bf16 v[40:43], a[96:99], a[72:75], v[40:43]\n" \
"ds_read_b128 a[192:195], v205 offset:12288\n" \
"v_mfma_f32_16x16x32_bf16 v[40:43], a[100:103], a[76:79], v[40:43]\n" \
"ds_read_b128 a[196:199], v206 offset:12288\n" \
"v_mfma_f32_16x16x32_bf16 v[0:3], a[104:107], a[72:75], v[0:3]\n" \
"ds_read_b64_tr_b16 a[208:209], v209 offset:4096\n" \
"v_mfma_f32_16x16x32_bf16 v[0:3], a[108:111], a[76:79], v[0:3]\n" \
"s_waitcnt lgkmcnt(3)\n" \
"global_load_lds_dwordx4 v253, s[48:49] offset:0\n" \
"v_mfma_f32_16x16x32_bf16 v[4:7], a[112:115], a[72:75], v[4:7]\n" \
"s_add_u32 s48, s48, 0x60000\n" \
"s_addc_u32 s49, s49, 0\n" \
"ds_read_b64_tr_b16 a[210:211], v209 offset:5120\n" \
"v_mfma_f32_16x16x32_bf16 v[4:7], a[116:119], a[76:79], v[4:7]\n" \
"ds_read_b64_tr_b16 a[212:213], v209 offset:16384\n" \
"v_mfma_f32_16x16x32_bf16 v[12:15], a[112:115], a[80:83], v[12:15]\n" \
"Lpg9729_%=:\n" \
"s_cbranch_execz Lpg12026_%=\n" \
"ds_read_b64_tr_b16 a[214:215], v209 offset:17408\n" \
"v_mfma_f32_16x16x32_bf16 v[12:15], a[116:119], a[84:87], v[12:15]\n" \
"global_load_lds_dwordx4 v253, s[50:51] offset:-3072\n" \
"v_mfma_f32_16x16x32_bf16 v[8:11], a[104:107], a[80:83], v[8:11]\n" \
"ds_read_b64_tr_b16 a[216:217], v210 offset:4096\n" \
"v_mfma_f32_16x16x32_bf16 v[8:11], a[108:111], a[84:87], v[8:11]\n" \
"ds_read_b64_tr_b16 a[218:219], v210 offset:5120\n" \
"v_mfma_f32_16x16x32_bf16 v[48:51], a[96:99], a[80:83], v[48:51]\n" \
"ds_read_b64_tr_b16 a[220:221], v210 offset:16384\n" \
"v_mfma_f32_16x16x32_bf16 v[48:51], a[100:103], a[84:87], v[48:51]\n" \
"global_load_lds_dwordx4 v253, s[50:51] offset:-1024\n" \
"v_mfma_f32_16x16x32_bf16 v[56:59], a[96:99], a[88:91], v[56:59]\n" \
"ds_read_b64_tr_b16 a[222:223], v210 offset:17408\n" \
"v_mfma_f32_16x16x32_bf16 v[56:59], a[100:103], a[92:95], v[56:59]\n" \
"ds_read_b64_tr_b16 a[200:201], v210 offset:2048\n" \
"v_mfma_f32_16x16x32_bf16 v[16:19], a[104:107], a[88:91], v[16:19]\n" \
"ds_read_b64_tr_b16 a[202:203], v210 offset:3072\n" \
"v_mfma_f32_16x16x32_bf16 v[16:19], a[108:111], a[92:95], v[16:19]\n" \
"global_load_lds_dwordx4 v253, s[50:51] offset:1024\n" \
"v_mfma_f32_16x16x32_bf16 v[20:23], a[112:115], a[88:91], v[20:23]\n" \
"s_add_u32 s50, s50, 0x60000\n" \
"s_addc_u32 s51, s51, 0\n" \
"ds_read_b64_tr_b16 a[204:205], v210 offset:14336\n" \
"v_mfma_f32_16x16x32_bf16 v[20:23], a[116:119], a[92:95], v[20:23]\n" \
"s_waitcnt lgkmcnt(3)\n" \
"ds_read_b64_tr_b16 a[206:207], v210 offset:15360\n" \
"v_mfma_f32_16x16x32_bf16 v[28:31], a[112:115], a[192:195], v[28:31]\n" \
"v_add_u32 v205, 0x1d000, v205\n" \
"v_add_u32 v206, 0x1d000, v206\n" \
"v_mfma_f32_16x16x32_bf16 v[28:31], a[116:119], a[196:199], v[28:31]\n" \
"v_add_u32 v209, 0x1d000, v209\n" \
"v_add_u32 v210, 0x1d000, v210\n" \
"v_mfma_f32_16x16x32_bf16 v[24:27], a[104:107], a[192:195], v[24:27]\n" \
"v_mfma_f32_16x16x32_bf16 v[24:27], a[108:111], a[196:199], v[24:27]\n" \
"s_waitcnt vmcnt(11) lgkmcnt(0)\n" \
"v_mfma_f32_16x16x32_bf16 v[64:67], a[96:99], a[192:195], v[64:67]\n" \
"v_mfma_f32_16x16x32_bf16 v[64:67], a[100:103], a[196:199], v[64:67]\n" \
"s_barrier\n" \
"v_mfma_f32_16x16x32_bf16 v[80:83], a[72:75], a[208:211], v[80:83]\n" \
"ds_read_b64_tr_b16 a[96:97], v209 offset:2048\n" \
"s_waitcnt vmcnt(10)\n" \
"v_mfma_f32_16x16x32_bf16 v[80:83], a[76:79], a[212:215], v[80:83]\n" \
"ds_read_b64_tr_b16 a[98:99], v209 offset:3072\n" \
"v_xor_b32 v226, v226, v227\n" \
"v_cmp_ne_u32 vcc, s60, v226\n" \
"v_mfma_f32_16x16x32_bf16 v[84:87], a[72:75], a[216:219], v[84:87]\n" \
"s_add_u32 m0, s95, 0\n" \
"ds_read_b64_tr_b16 a[100:101], v209 offset:14336\n" \
"v_mfma_f32_16x16x32_bf16 v[84:87], a[76:79], a[220:223], v[84:87]\n" \
"s_and_b32 vcc_hi, vcc_lo, -1\n" \
"s_cbranch_scc1 Lpl3_%=\n" \
"Lpr3_%=:\n" \
"global_load_lds_dwordx4 v252, s[42:43] offset:0\n" \
"v_mfma_f32_16x16x32_bf16 v[44:47], a[200:203], a[72:75], v[44:47]\n" \
"ds_read_b64_tr_b16 a[102:103], v209 offset:15360\n" \
"v_readfirstlane_b32 s71, v227\n" \
"v_mfma_f32_16x16x32_bf16 v[44:47], a[204:207], a[76:79], v[44:47]\n" \
"s_and_b32 s70, %1, 7\n" \
"ds_read_b128 a[72:75], v205 offset:0\n" \
"ds_read_b64_tr_b16 a[104:105], v209 offset:0\n" \
"v_mfma_f32_16x16x32_bf16 v[52:55], a[200:203], a[80:83], v[52:55]\n" \
"ds_read_b128 a[76:79], v206 offset:0\n" \
"s_lshl_b32 s78, s70, 2\n" \
"v_mfma_f32_16x16x32_bf16 v[52:55], a[204:207], a[84:87], v[52:55]\n" \
"global_load_lds_dwordx4 v252, s[42:43] offset:2048\n" \
"v_subrev_u32 v227, s71, v227\n" \
"v_mfma_f32_16x16x32_bf16 v[92:95], a[80:83], a[216:219], v[92:95]\n" \
"s_add_u32 s42, s42, 0x100\n" \
"s_addc_u32 s43, s43, 0\n" \
"ds_read_b64_tr_b16 a[106:107], v209 offset:1024\n" \
"v_mfma_f32_16x16x32_bf16 v[92:95], a[84:87], a[220:223], v[92:95]\n" \
"ds_read_b64_tr_b16 a[108:109], v209 offset:12288\n" \
"v_add_u32_dpp v227, v227, v227 quad_perm:[1,0,3,2] row_mask:0xf bank_mask:0xf\n" \
"v_mfma_f32_16x16x32_bf16 v[88:91], a[80:83], a[208:211], v[88:91]\n" \
"ds_read_b64_tr_b16 a[110:111], v209 offset:13312\n" \
"v_mfma_f32_16x16x32_bf16 v[88:91], a[84:87], a[212:215], v[88:91]\n" \
"global_load_lds_dwordx4 v252, s[44:45] offset:1024\n" \
"v_add_u32_dpp v227, v227, v227 quad_perm:[2,3,0,1] row_mask:0xf bank_mask:0xf\n" \
"v_mfma_f32_16x16x32_bf16 v[96:99], a[88:91], a[208:211], v[96:99]\n" \
"ds_read_b128 a[80:83], v205 offset:4096\n" \
"v_readlane_b32 s71, v227, s78\n" \
"v_mfma_f32_16x16x32_bf16 v[96:99], a[92:95], a[212:215], v[96:99]\n" \
"ds_read_b128 a[84:87], v206 offset:4096\n" \
"v_lshrrev_b32 v226, 6, v248\n" \
"v_mfma_f32_16x16x32_bf16 v[100:103], a[88:91], a[216:219], v[100:103]\n" \
"ds_read_b64_tr_b16 a[112:113], v210 offset:0\n" \
"v_subrev_u32 v227, s71, v227\n" \
"v_mfma_f32_16x16x32_bf16 v[100:103], a[92:95], a[220:223], v[100:103]\n" \
"global_load_lds_dwordx4 v252, s[44:45] offset:3072\n" \
"v_lshl_add_u32 v227, v227, 3, v226\n" \
"v_mfma_f32_16x16x32_bf16 v[60:63], a[200:203], a[88:91], v[60:63]\n" \
"s_add_u32 s44, s44, 0x100\n" \
"s_addc_u32 s45, s45, 0\n" \
"ds_read_b64_tr_b16 a[114:115], v210 offset:1024\n" \
"v_mfma_f32_16x16x32_bf16 v[60:63], a[204:207], a[92:95], v[60:63]\n" \
"ds_read_b64_tr_b16 a[116:117], v210 offset:12288\n" \
"v_cmp_gt_i32 vcc, s70, v227\n" \
"v_mfma_f32_16x16x32_bf16 v[68:71], a[200:203], a[192:195], v[68:71]\n" \
"ds_read_b128 a[88:91], v205 offset:8192\n" \
"s_add_u32 m0, s84, 24576\n" \
"v_mfma_f32_16x16x32_bf16 v[68:71], a[204:207], a[196:199], v[68:71]\n" \
"global_load_lds_dwordx4 v253, s[48:49] offset:-4096\n" \
"v_mfma_f32_16x16x32_bf16 v[108:111], a[192:195], a[216:219], v[108:111]\n" \
"ds_read_b128 a[92:95], v206 offset:8192\n" \
"s_bcnt1_i32_b32 s70, vcc_lo\n" \
"v_mfma_f32_16x16x32_bf16 v[108:111], a[196:199], a[220:223], v[108:111]\n" \
"s_lshr_b32 s70, s70, 2\n" \
"ds_read_b64_tr_b16 a[118:119], v210 offset:13312\n" \
"v_mfma_f32_16x16x32_bf16 v[104:107], a[192:195], a[208:211], v[104:107]\n" \
"s_lshr_b32 s78, s70, 2\n" \
"v_mfma_f32_16x16x32_bf16 v[104:107], a[196:199], a[212:215], v[104:107]\n" \
"s_waitcnt lgkmcnt(2)\n" \
"global_load_lds_dwordx4 v253, s[48:49] offset:-2048\n" \
"v_mfma_f32_16x16x32_bf16 v[40:43], a[96:99], a[72:75], v[40:43]\n" \
"ds_read_b128 a[192:195], v205 offset:12288\n" \
"s_mul_i32 s71, s78, 7\n" \
"v_mfma_f32_16x16x32_bf16 v[40:43], a[100:103], a[76:79], v[40:43]\n" \
"ds_read_b128 a[196:199], v206 offset:12288\n" \
"s_xor_b32 s70, s70, s71\n" \
"v_mfma_f32_16x16x32_bf16 v[0:3], a[104:107], a[72:75], v[0:3]\n" \
"s_xor_b32 s78, s78, 1\n" \
"ds_read_b64_tr_b16 a[208:209], v209 offset:4096\n" \
"v_mfma_f32_16x16x32_bf16 v[0:3], a[108:111], a[76:79], v[0:3]\n" \
"s_waitcnt lgkmcnt(3)\n" \
"global_load_lds_dwordx4 v253, s[48:49] offset:0\n" \
"v_mfma_f32_16x16x32_bf16 v[4:7], a[112:115], a[72:75], v[4:7]\n" \
"s_add_u32 s48, s48, 0xc0000\n" \
"s_addc_u32 s49, s49, 0\n" \
"ds_read_b64_tr_b16 a[210:211], v209 offset:5120\n" \
"v_mfma_f32_16x16x32_bf16 v[4:7], a[116:119], a[76:79], v[4:7]\n" \
"s_lshl_b32 s65, s70, 2\n" \
"ds_read_b64_tr_b16 a[212:213], v209 offset:16384\n" \
"v_mfma_f32_16x16x32_bf16 v[12:15], a[112:115], a[80:83], v[12:15]\n" \
"s_lshr_b32 s64, 0x9764, s65\n" \
"ds_read_b64_tr_b16 a[214:215], v209 offset:17408\n" \
"v_mfma_f32_16x16x32_bf16 v[12:15], a[116:119], a[84:87], v[12:15]\n" \
"global_load_lds_dwordx4 v253, s[50:51] offset:-3072\n" \
"v_mfma_f32_16x16x32_bf16 v[8:11], a[104:107], a[80:83], v[8:11]\n" \
"s_and_b32 s64, s64, 15\n" \
"ds_read_b64_tr_b16 a[216:217], v210 offset:4096\n" \
"v_mfma_f32_16x16x32_bf16 v[8:11], a[108:111], a[84:87], v[8:11]\n" \
"s_add_u32 s64, s64, 32\n" \
"ds_read_b64_tr_b16 a[218:219], v210 offset:5120\n" \
"v_mfma_f32_16x16x32_bf16 v[48:51], a[96:99], a[80:83], v[48:51]\n" \
"s_lshr_b32 s70, %1, 6\n" \
"ds_read_b64_tr_b16 a[220:221], v210 offset:16384\n" \
"v_mfma_f32_16x16x32_bf16 v[48:51], a[100:103], a[84:87], v[48:51]\n" \
"global_load_lds_dwordx4 v253, s[50:51] offset:-1024\n" \
"v_mfma_f32_16x16x32_bf16 v[56:59], a[96:99], a[88:91], v[56:59]\n" \
"s_add_u32 s65, s65, s70\n" \
"ds_read_b64_tr_b16 a[222:223], v210 offset:17408\n" \
"v_mfma_f32_16x16x32_bf16 v[56:59], a[100:103], a[92:95], v[56:59]\n" \
"s_lshl_b32 s30, s85, 2\n" \
"ds_read_b64_tr_b16 a[200:201], v210 offset:2048\n" \
"v_mfma_f32_16x16x32_bf16 v[16:19], a[104:107], a[88:91], v[16:19]\n" \
"s_lshl_b32 s70, s88, 7\n" \
"ds_read_b64_tr_b16 a[202:203], v210 offset:3072\n" \
"v_mfma_f32_16x16x32_bf16 v[16:19], a[108:111], a[92:95], v[16:19]\n" \
"global_load_lds_dwordx4 v253, s[50:51] offset:1024\n" \
"v_mfma_f32_16x16x32_bf16 v[20:23], a[112:115], a[88:91], v[20:23]\n" \
"s_add_u32 s50, s50, 0xc0000\n" \
"s_addc_u32 s51, s51, 0\n" \
"ds_read_b64_tr_b16 a[204:205], v210 offset:14336\n" \
"v_mfma_f32_16x16x32_bf16 v[20:23], a[116:119], a[92:95], v[20:23]\n" \
"s_waitcnt lgkmcnt(3)\n" \
"ds_read_b64_tr_b16 a[206:207], v210 offset:15360\n" \
"v_mfma_f32_16x16x32_bf16 v[28:31], a[112:115], a[192:195], v[28:31]\n" \
"s_add_u32 s30, s30, s70\n" \
"v_add_u32 v205, 0xffff3000, v205\n" \
"v_add_u32 v206, 0xffff3000, v206\n" \
"v_mfma_f32_16x16x32_bf16 v[28:31], a[116:119], a[196:199], v[28:31]\n" \
"s_add_u32 s30, s30, 65591296\n" \
"v_add_u32 v209, 0xffff3000, v209\n" \
"v_add_u32 v210, 0xffff3000, v210\n" \
"v_mfma_f32_16x16x32_bf16 v[24:27], a[104:107], a[192:195], v[24:27]\n" \
"v_lshrrev_b32 v221, 2, v248\n" \
"v_mfma_f32_16x16x32_bf16 v[24:27], a[108:111], a[196:199], v[24:27]\n" \
"s_waitcnt vmcnt(10) lgkmcnt(0)\n" \
"v_mfma_f32_16x16x32_bf16 v[64:67], a[96:99], a[192:195], v[64:67]\n" \
"s_lshl_b32 s81, s65, 7\n" \
"v_mfma_f32_16x16x32_bf16 v[64:67], a[100:103], a[196:199], v[64:67]\n" \
"s_barrier\n" \
"v_mfma_f32_16x16x32_bf16 v[80:83], a[72:75], a[208:211], v[80:83]\n" \
"s_add_u32 s82, s81, 65591296\n" \
"ds_read_b64_tr_b16 a[96:97], v209 offset:2048\n" \
"v_mfma_f32_16x16x32_bf16 v[80:83], a[76:79], a[212:215], v[80:83]\n" \
"s_add_u32 s81, s81, 65589248\n" \
"ds_read_b64_tr_b16 a[98:99], v209 offset:3072\n" \
"v_mfma_f32_16x16x32_bf16 v[84:87], a[72:75], a[216:219], v[84:87]\n" \
"s_add_u32 m0, s95, 118784\n" \
"ds_read_b64_tr_b16 a[100:101], v209 offset:14336\n" \
"v_mfma_f32_16x16x32_bf16 v[84:87], a[76:79], a[220:223], v[84:87]\n" \
"global_load_lds_dwordx4 v252, s[42:43] offset:0\n" \
"v_mfma_f32_16x16x32_bf16 v[44:47], a[200:203], a[72:75], v[44:47]\n" \
"s_mov_b64 s[52:53], s[26:27]\n" \
"ds_read_b64_tr_b16 a[102:103], v209 offset:15360\n" \
"v_mfma_f32_16x16x32_bf16 v[44:47], a[204:207], a[76:79], v[44:47]\n" \
"s_and_b32 s53, s53, 0xffff\n" \
"ds_read_b128 a[72:75], v205 offset:0\n" \
"ds_read_b64_tr_b16 a[104:105], v209 offset:0\n" \
"v_mfma_f32_16x16x32_bf16 v[52:55], a[200:203], a[80:83], v[52:55]\n" \
"ds_read_b128 a[76:79], v206 offset:0\n" \
"s_mov_b32 s54, 8388608\n" \
"v_mfma_f32_16x16x32_bf16 v[52:55], a[204:207], a[84:87], v[52:55]\n" \
"global_load_lds_dwordx4 v252, s[42:43] offset:2048\n" \
"v_mfma_f32_16x16x32_bf16 v[92:95], a[80:83], a[216:219], v[92:95]\n" \
"s_add_u32 s42, s42, 0x80\n" \
"s_addc_u32 s43, s43, 0\n" \
"ds_read_b64_tr_b16 a[106:107], v209 offset:1024\n" \
"v_mfma_f32_16x16x32_bf16 v[92:95], a[84:87], a[220:223], v[92:95]\n" \
"s_mov_b32 s55, 0x20000\n" \
"ds_read_b64_tr_b16 a[108:109], v209 offset:12288\n" \
"v_mfma_f32_16x16x32_bf16 v[88:91], a[80:83], a[208:211], v[88:91]\n" \
"s_lshr_b32 s83, s63, 1\n" \
"ds_read_b64_tr_b16 a[110:111], v209 offset:13312\n" \
"v_mfma_f32_16x16x32_bf16 v[88:91], a[84:87], a[212:215], v[88:91]\n" \
"global_load_lds_dwordx4 v252, s[44:45] offset:1024\n" \
"v_mfma_f32_16x16x32_bf16 v[96:99], a[88:91], a[208:211], v[96:99]\n" \
"ds_read_b128 a[80:83], v205 offset:4096\n" \
"s_add_u32 s83, s83, 2\n" \
"v_mfma_f32_16x16x32_bf16 v[96:99], a[92:95], a[212:215], v[96:99]\n" \
"ds_read_b128 a[84:87], v206 offset:4096\n" \
"s_lshr_b32 s70, s18, 8\n" \
"v_mfma_f32_16x16x32_bf16 v[100:103], a[88:91], a[216:219], v[100:103]\n" \
"s_sub_u32 s83, s83, s70\n" \
"ds_read_b64_tr_b16 a[112:113], v210 offset:0\n" \
"v_mfma_f32_16x16x32_bf16 v[100:103], a[92:95], a[220:223], v[100:103]\n" \
"global_load_lds_dwordx4 v252, s[44:45] offset:3072\n" \
"v_mfma_f32_16x16x32_bf16 v[60:63], a[200:203], a[88:91], v[60:63]\n" \
"s_add_u32 s44, s44, 0x80\n" \
"s_addc_u32 s45, s45, 0\n" \
"ds_read_b64_tr_b16 a[114:115], v210 offset:1024\n" \
"v_mfma_f32_16x16x32_bf16 v[60:63], a[204:207], a[92:95], v[60:63]\n" \
"s_and_b32 s83, s83, 3\n" \
"ds_read_b64_tr_b16 a[116:117], v210 offset:12288\n" \
"v_mfma_f32_16x16x32_bf16 v[68:71], a[200:203], a[192:195], v[68:71]\n" \
"ds_read_b128 a[88:91], v205 offset:8192\n" \
"s_add_u32 m0, s84, 143360\n" \
"v_mfma_f32_16x16x32_bf16 v[68:71], a[204:207], a[196:199], v[68:71]\n" \
"global_load_lds_dwordx4 v253, s[48:49] offset:-4096\n" \
"v_mfma_f32_16x16x32_bf16 v[108:111], a[192:195], a[216:219], v[108:111]\n" \
"ds_read_b128 a[92:95], v206 offset:8192\n" \
"s_and_b32 s70, s63, 1\n" \
"v_mfma_f32_16x16x32_bf16 v[108:111], a[196:199], a[220:223], v[108:111]\n" \
"s_lshl_b32 s70, s70, 2\n" \
"ds_read_b64_tr_b16 a[118:119], v210 offset:13312\n" \
"v_mfma_f32_16x16x32_bf16 v[104:107], a[192:195], a[208:211], v[104:107]\n" \
"s_or_b32 s83, s83, s70\n" \
"v_mfma_f32_16x16x32_bf16 v[104:107], a[196:199], a[212:215], v[104:107]\n" \
"s_waitcnt lgkmcnt(2)\n" \
"global_load_lds_dwordx4 v253, s[48:49] offset:-2048\n" \
"v_mfma_f32_16x16x32_bf16 v[40:43], a[96:99], a[72:75], v[40:43]\n" \
"s_mul_i32 s75, s78, s64\n" \
"ds_read_b128 a[192:195], v205 offset:12288\n" \
"v_lshrrev_b32 v228, 4, v248\n" \
"v_mfma_f32_16x16x32_bf16 v[40:43], a[100:103], a[76:79], v[40:43]\n" \
"ds_read_b128 a[196:199], v206 offset:12288\n" \
"s_lshl_b32 s70, s65, 2\n" \
"v_mfma_f32_16x16x32_bf16 v[0:3], a[104:107], a[72:75], v[0:3]\n" \
"s_add_u32 s70, s70, s61\n" \
"ds_read_b64_tr_b16 a[208:209], v209 offset:4096\n" \
"v_mfma_f32_16x16x32_bf16 v[0:3], a[108:111], a[76:79], v[0:3]\n" \
"s_waitcnt lgkmcnt(3)\n" \
"global_load_lds_dwordx4 v253, s[48:49] offset:0\n" \
"v_mfma_f32_16x16x32_bf16 v[4:7], a[112:115], a[72:75], v[4:7]\n" \
"s_add_u32 s48, s48, 0x60000\n" \
"s_addc_u32 s49, s49, 0\n" \
"ds_read_b64_tr_b16 a[210:211], v209 offset:5120\n" \
"v_mfma_f32_16x16x32_bf16 v[4:7], a[116:119], a[76:79], v[4:7]\n" \
"s_mul_i32 s73, s70, 69632\n" \
"ds_read_b64_tr_b16 a[212:213], v209 offset:16384\n" \
"v_mfma_f32_16x16x32_bf16 v[12:15], a[112:115], a[80:83], v[12:15]\n" \
"s_add_u32 s73, s73, 52740096\n" \
"ds_read_b64_tr_b16 a[214:215], v209 offset:17408\n" \
"v_mfma_f32_16x16x32_bf16 v[12:15], a[116:119], a[84:87], v[12:15]\n" \
"global_load_lds_dwordx4 v253, s[50:51] offset:-3072\n" \
"v_mfma_f32_16x16x32_bf16 v[8:11], a[104:107], a[80:83], v[8:11]\n" \
"s_add_u32 s73, s73, s28\n" \
"ds_read_b64_tr_b16 a[216:217], v210 offset:4096\n" \
"v_mfma_f32_16x16x32_bf16 v[8:11], a[108:111], a[84:87], v[8:11]\n" \
"s_mul_i32 s70, s70, 266240\n" \
"ds_read_b64_tr_b16 a[218:219], v210 offset:5120\n" \
"v_mfma_f32_16x16x32_bf16 v[48:51], a[96:99], a[80:83], v[48:51]\n" \
"s_lshl_b32 s71, s75, 12\n" \
"ds_read_b64_tr_b16 a[220:221], v210 offset:16384\n" \
"v_mfma_f32_16x16x32_bf16 v[48:51], a[100:103], a[84:87], v[48:51]\n" \
"global_load_lds_dwordx4 v253, s[50:51] offset:-1024\n" \
"v_mfma_f32_16x16x32_bf16 v[56:59], a[96:99], a[88:91], v[56:59]\n" \
"s_add_u32 s70, s70, s71\n" \
"ds_read_b64_tr_b16 a[222:223], v210 offset:17408\n" \
"v_mfma_f32_16x16x32_bf16 v[56:59], a[100:103], a[92:95], v[56:59]\n" \
"s_add_u32 s70, s70, 35700736\n" \
"ds_read_b64_tr_b16 a[200:201], v210 offset:2048\n" \
"v_mfma_f32_16x16x32_bf16 v[16:19], a[104:107], a[88:91], v[16:19]\n" \
"s_add_u32 s36, s28, s70\n" \
"ds_read_b64_tr_b16 a[202:203], v210 offset:3072\n" \
"v_mfma_f32_16x16x32_bf16 v[16:19], a[108:111], a[92:95], v[16:19]\n" \
"global_load_lds_dwordx4 v253, s[50:51] offset:1024\n" \
"v_mfma_f32_16x16x32_bf16 v[20:23], a[112:115], a[88:91], v[20:23]\n" \
"s_add_u32 s50, s50, 0x60000\n" \
"s_addc_u32 s51, s51, 0\n" \
"ds_read_b64_tr_b16 a[204:205], v210 offset:14336\n" \
"v_mfma_f32_16x16x32_bf16 v[20:23], a[116:119], a[92:95], v[20:23]\n" \
"s_waitcnt lgkmcnt(3)\n" \
"ds_read_b64_tr_b16 a[206:207], v210 offset:15360\n" \
"v_mfma_f32_16x16x32_bf16 v[28:31], a[112:115], a[192:195], v[28:31]\n" \
"s_mov_b32 s37, s29\n" \
"v_add_u32 v205, 0xffff0000, v205\n" \
"v_add_u32 v206, 0xffff0000, v206\n" \
"v_mfma_f32_16x16x32_bf16 v[28:31], a[116:119], a[196:199], v[28:31]\n" \
"s_lshr_b32 s74, s61, 1\n" \
"v_add_u32 v209, 0xffff0000, v209\n" \
"v_add_u32 v210, 0xffff0000, v210\n" \
"v_mfma_f32_16x16x32_bf16 v[24:27], a[104:107], a[192:195], v[24:27]\n" \
"s_lshl_b32 s74, s74, 16\n" \
"v_mfma_f32_16x16x32_bf16 v[24:27], a[108:111], a[196:199], v[24:27]\n" \
"s_waitcnt vmcnt(10) lgkmcnt(0)\n" \
"v_mfma_f32_16x16x32_bf16 v[64:67], a[96:99], a[192:195], v[64:67]\n" \
"s_and_b32 s70, s61, 1\n" \
"v_mfma_f32_16x16x32_bf16 v[64:67], a[100:103], a[196:199], v[64:67]\n" \
"s_barrier\n" \
"v_mfma_f32_16x16x32_bf16 v[80:83], a[72:75], a[208:211], v[80:83]\n" \
"s_lshl_b32 s70, s70, 14\n" \
"ds_read_b64_tr_b16 a[96:97], v209 offset:2048\n" \
"v_mfma_f32_16x16x32_bf16 v[80:83], a[76:79], a[212:215], v[80:83]\n" \
"s_add_u32 s74, s74, s70\n" \
"ds_read_b64_tr_b16 a[98:99], v209 offset:3072\n" \
"v_mfma_f32_16x16x32_bf16 v[84:87], a[72:75], a[216:219], v[84:87]\n" \
"s_add_u32 m0, s95, 65536\n" \
"ds_read_b64_tr_b16 a[100:101], v209 offset:14336\n" \
"v_mfma_f32_16x16x32_bf16 v[84:87], a[76:79], a[220:223], v[84:87]\n" \
"global_load_lds_dwordx4 v252, s[42:43] offset:0\n" \
"v_mfma_f32_16x16x32_bf16 v[44:47], a[200:203], a[72:75], v[44:47]\n" \
"s_lshl_b32 s71, s83, 8\n" \
"ds_read_b64_tr_b16 a[102:103], v209 offset:15360\n" \
"v_mfma_f32_16x16x32_bf16 v[44:47], a[204:207], a[76:79], v[44:47]\n" \
"s_add_u32 s74, s74, s71\n" \
"ds_read_b128 a[72:75], v205 offset:0\n" \
"ds_read_b64_tr_b16 a[104:105], v209 offset:0\n" \
"v_mfma_f32_16x16x32_bf16 v[52:55], a[200:203], a[80:83], v[52:55]\n" \
"s_add_u32 s40, s24, s74\n" \
"s_addc_u32 s41, s25, 0\n" \
"ds_read_b128 a[76:79], v206 offset:0\n" \
"v_mfma_f32_16x16x32_bf16 v[52:55], a[204:207], a[84:87], v[52:55]\n" \
"global_load_lds_dwordx4 v252, s[42:43] offset:2048\n" \
"v_mfma_f32_16x16x32_bf16 v[92:95], a[80:83], a[216:219], v[92:95]\n" \
"s_add_u32 s42, s42, 0x80\n" \
"s_addc_u32 s43, s43, 0\n" \
"ds_read_b64_tr_b16 a[106:107], v209 offset:1024\n" \
"v_mfma_f32_16x16x32_bf16 v[92:95], a[84:87], a[220:223], v[92:95]\n" \
"s_lshl_b32 s70, s75, 17\n" \
"ds_read_b64_tr_b16 a[108:109], v209 offset:12288\n" \
"v_mfma_f32_16x16x32_bf16 v[88:91], a[80:83], a[208:211], v[88:91]\n" \
"s_add_u32 s70, s70, s74\n" \
"ds_read_b64_tr_b16 a[110:111], v209 offset:13312\n" \
"v_mfma_f32_16x16x32_bf16 v[88:91], a[84:87], a[212:215], v[88:91]\n" \
"global_load_lds_dwordx4 v252, s[44:45] offset:1024\n" \
"v_mfma_f32_16x16x32_bf16 v[96:99], a[88:91], a[208:211], v[96:99]\n" \
"s_add_u32 s38, s18, s70\n" \
"s_addc_u32 s39, s19, 0\n" \
"ds_read_b128 a[80:83], v205 offset:4096\n" \
"v_mfma_f32_16x16x32_bf16 v[96:99], a[92:95], a[212:215], v[96:99]\n" \
"ds_read_b128 a[84:87], v206 offset:4096\n" \
"v_bfe_u32 v229, v228, 2, 3\n" \
"v_lshlrev_b32 v229, 11, v229\n" \
"v_mfma_f32_16x16x32_bf16 v[100:103], a[88:91], a[216:219], v[100:103]\n" \
"ds_read_b64_tr_b16 a[112:113], v210 offset:0\n" \
"v_lshrrev_b32 v230, 5, v228\n" \
"v_mfma_f32_16x16x32_bf16 v[100:103], a[92:95], a[220:223], v[100:103]\n" \
"global_load_lds_dwordx4 v252, s[44:45] offset:3072\n" \
"v_lshl_add_u32 v229, v230, 6, v229\n" \
"v_mfma_f32_16x16x32_bf16 v[60:63], a[200:203], a[88:91], v[60:63]\n" \
"s_add_u32 s44, s44, 0x80\n" \
"s_addc_u32 s45, s45, 0\n" \
"ds_read_b64_tr_b16 a[114:115], v210 offset:1024\n" \
"v_mfma_f32_16x16x32_bf16 v[60:63], a[204:207], a[92:95], v[60:63]\n" \
"ds_read_b64_tr_b16 a[116:117], v210 offset:12288\n" \
"v_and_b32 v230, 3, v228\n" \
"v_mfma_f32_16x16x32_bf16 v[68:71], a[200:203], a[192:195], v[68:71]\n" \
"s_add_u32 m0, s84, 90112\n" \
"ds_read_b128 a[88:91], v205 offset:8192\n" \
"v_bfe_u32 v231, v228, 4, 1\n" \
"v_mfma_f32_16x16x32_bf16 v[68:71], a[204:207], a[196:199], v[68:71]\n" \
"global_load_lds_dwordx4 v253, s[48:49] offset:-4096\n" \
"v_lshlrev_b32 v231, 1, v231\n" \
"v_mfma_f32_16x16x32_bf16 v[108:111], a[192:195], a[216:219], v[108:111]\n" \
"ds_read_b128 a[92:95], v206 offset:8192\n" \
"v_xor_b32 v230, v230, v231\n" \
"v_mfma_f32_16x16x32_bf16 v[108:111], a[196:199], a[220:223], v[108:111]\n" \
"ds_read_b64_tr_b16 a[118:119], v210 offset:13312\n" \
"v_lshl_add_u32 v229, v230, 4, v229\n" \
"v_mfma_f32_16x16x32_bf16 v[104:107], a[192:195], a[208:211], v[104:107]\n" \
"v_add_u32 v216, 4096, v229\n" \
"v_add_u32 v217, 3200, v229\n" \
"v_mfma_f32_16x16x32_bf16 v[104:107], a[196:199], a[212:215], v[104:107]\n" \
"s_waitcnt lgkmcnt(2)\n" \
"global_load_lds_dwordx4 v253, s[48:49] offset:-2048\n" \
"v_add_u32 v218, 34816, v229\n" \
"v_mfma_f32_16x16x32_bf16 v[40:43], a[96:99], a[72:75], v[40:43]\n" \
"ds_read_b128 a[192:195], v205 offset:12288\n" \
"v_add_u32 v219, 33920, v229\n" \
"v_mfma_f32_16x16x32_bf16 v[40:43], a[100:103], a[76:79], v[40:43]\n" \
"ds_read_b128 a[196:199], v206 offset:12288\n" \
"s_mov_b32 s75, 65536\n" \
"v_mfma_f32_16x16x32_bf16 v[0:3], a[104:107], a[72:75], v[0:3]\n" \
"s_lshr_b32 s70, s61, 1\n" \
"ds_read_b64_tr_b16 a[208:209], v209 offset:4096\n" \
"v_mfma_f32_16x16x32_bf16 v[0:3], a[108:111], a[76:79], v[0:3]\n" \
"s_waitcnt lgkmcnt(3)\n" \
"global_load_lds_dwordx4 v253, s[48:49] offset:0\n" \
"v_mfma_f32_16x16x32_bf16 v[4:7], a[112:115], a[72:75], v[4:7]\n" \
"s_add_u32 s48, s48, 0x60000\n" \
"s_addc_u32 s49, s49, 0\n" \
"ds_read_b64_tr_b16 a[210:211], v209 offset:5120\n" \
"v_mfma_f32_16x16x32_bf16 v[4:7], a[116:119], a[76:79], v[4:7]\n" \
"s_lshl_b32 s71, s70, 13\n" \
"ds_read_b64_tr_b16 a[212:213], v209 offset:16384\n" \
"v_add_u32 v211, s71, v249\n" \
"v_mfma_f32_16x16x32_bf16 v[12:15], a[112:115], a[80:83], v[12:15]\n" \
"s_lshl_b32 s71, s65, 1\n" \
"ds_read_b64_tr_b16 a[214:215], v209 offset:17408\n" \
"v_mfma_f32_16x16x32_bf16 v[12:15], a[116:119], a[84:87], v[12:15]\n" \
"global_load_lds_dwordx4 v253, s[50:51] offset:-3072\n" \
"v_mfma_f32_16x16x32_bf16 v[8:11], a[104:107], a[80:83], v[8:11]\n" \
"s_add_u32 s70, s70, s71\n" \
"ds_read_b64_tr_b16 a[216:217], v210 offset:4096\n" \
"v_mfma_f32_16x16x32_bf16 v[8:11], a[108:111], a[84:87], v[8:11]\n" \
"s_lshl_b32 s77, s70, 18\n" \
"ds_read_b64_tr_b16 a[218:219], v210 offset:5120\n" \
"v_mfma_f32_16x16x32_bf16 v[48:51], a[96:99], a[80:83], v[48:51]\n" \
"s_lshl_b32 s71, s83, 8\n" \
"ds_read_b64_tr_b16 a[220:221], v210 offset:16384\n" \
"v_mfma_f32_16x16x32_bf16 v[48:51], a[100:103], a[84:87], v[48:51]\n" \
"global_load_lds_dwordx4 v253, s[50:51] offset:-1024\n" \
"v_mfma_f32_16x16x32_bf16 v[56:59], a[96:99], a[88:91], v[56:59]\n" \
"s_add_u32 s77, s77, s71\n" \
"ds_read_b64_tr_b16 a[222:223], v210 offset:17408\n" \
"v_mfma_f32_16x16x32_bf16 v[56:59], a[100:103], a[92:95], v[56:59]\n" \
"s_lshl_b32 s71, s78, 11\n" \
"ds_read_b64_tr_b16 a[200:201], v210 offset:2048\n" \
"v_mfma_f32_16x16x32_bf16 v[16:19], a[104:107], a[88:91], v[16:19]\n" \
"s_add_u32 s77, s77, s71\n" \
"ds_read_b64_tr_b16 a[202:203], v210 offset:3072\n" \
"v_mfma_f32_16x16x32_bf16 v[16:19], a[108:111], a[92:95], v[16:19]\n" \
"global_load_lds_dwordx4 v253, s[50:51] offset:1024\n" \
"v_mfma_f32_16x16x32_bf16 v[20:23], a[112:115], a[88:91], v[20:23]\n" \
"s_add_u32 s50, s50, 0x60000\n" \
"s_addc_u32 s51, s51, 0\n" \
"ds_read_b64_tr_b16 a[204:205], v210 offset:14336\n" \
"v_mfma_f32_16x16x32_bf16 v[20:23], a[116:119], a[92:95], v[20:23]\n" \
"s_waitcnt lgkmcnt(3)\n" \
"ds_read_b64_tr_b16 a[206:207], v210 offset:15360\n" \
"v_mfma_f32_16x16x32_bf16 v[28:31], a[112:115], a[192:195], v[28:31]\n" \
"s_and_b32 s70, s61, 1\n" \
"v_add_u32 v205, 0x1d000, v205\n" \
"v_add_u32 v206, 0x1d000, v206\n" \
"v_mfma_f32_16x16x32_bf16 v[28:31], a[116:119], a[196:199], v[28:31]\n" \
"s_lshl_b32 s71, s70, 7\n" \
"v_add_u32 v209, 0x1d000, v209\n" \
"v_add_u32 v210, 0x1d000, v210\n" \
"v_mfma_f32_16x16x32_bf16 v[24:27], a[104:107], a[192:195], v[24:27]\n" \
"s_add_u32 s77, s77, s71\n" \
"v_and_b32 v229, 3, v228\n" \
"v_lshlrev_b32 v229, 3, v229\n" \
"v_mfma_f32_16x16x32_bf16 v[24:27], a[108:111], a[196:199], v[24:27]\n" \
"s_waitcnt vmcnt(10) lgkmcnt(0)\n" \
"v_bfe_u32 v230, v228, 2, 2\n" \
"v_lshl_add_u32 v229, v230, 6, v229\n" \
"v_bfe_u32 v230, v228, 4, 1\n" \
"v_mfma_f32_16x16x32_bf16 v[64:67], a[96:99], a[192:195], v[64:67]\n" \
"v_lshl_add_u32 v229, v230, 5, v229\n" \
"v_lshl_add_u32 v229, v230, 8, v229\n" \
"v_mfma_f32_16x16x32_bf16 v[64:67], a[100:103], a[196:199], v[64:67]\n" \
"s_barrier\n" \
"v_lshrrev_b32 v230, 5, v228\n" \
"v_lshl_add_u32 v229, v230, 12, v229\n" \
"v_mfma_f32_16x16x32_bf16 v[80:83], a[72:75], a[208:211], v[80:83]\n" \
"s_lshl_b32 s71, s70, 10\n" \
"ds_read_b64_tr_b16 a[96:97], v209 offset:2048\n" \
"v_add_u32 v212, s71, v229\n" \
"v_mfma_f32_16x16x32_bf16 v[80:83], a[76:79], a[212:215], v[80:83]\n" \
"s_lshl_b32 s70, s65, 3\n" \
"s_add_u32 s70, s70, s83\n" \
"ds_read_b64_tr_b16 a[98:99], v209 offset:3072\n" \
"v_mfma_f32_16x16x32_bf16 v[84:87], a[72:75], a[216:219], v[84:87]\n" \
"s_add_u32 m0, s95, 0\n" \
"s_lshl_b32 s76, s70, 16\n" \
"ds_read_b64_tr_b16 a[100:101], v209 offset:14336\n" \
"v_mfma_f32_16x16x32_bf16 v[84:87], a[76:79], a[220:223], v[84:87]\n" \
"s_lshl_b32 s71, s61, 14\n" \
"global_load_lds_dwordx4 v252, s[42:43] offset:0\n" \
"v_mfma_f32_16x16x32_bf16 v[44:47], a[200:203], a[72:75], v[44:47]\n" \
"s_add_u32 s76, s76, s71\n" \
"ds_read_b64_tr_b16 a[102:103], v209 offset:15360\n" \
"v_mfma_f32_16x16x32_bf16 v[44:47], a[204:207], a[76:79], v[44:47]\n" \
"s_add_u32 s76, s76, 57196544\n" \
"ds_read_b128 a[72:75], v205 offset:0\n" \
"ds_read_b64_tr_b16 a[104:105], v209 offset:0\n" \
"v_mfma_f32_16x16x32_bf16 v[52:55], a[200:203], a[80:83], v[52:55]\n" \
"ds_read_b128 a[76:79], v206 offset:0\n" \
"s_lshl_b32 s79, s70, 7\n" \
"v_mfma_f32_16x16x32_bf16 v[52:55], a[204:207], a[84:87], v[52:55]\n" \
"global_load_lds_dwordx4 v252, s[42:43] offset:2048\n" \
"v_mfma_f32_16x16x32_bf16 v[92:95], a[80:83], a[216:219], v[92:95]\n" \
"s_add_u32 s42, s42, 0x80\n" \
"s_addc_u32 s43, s43, 0\n" \
"ds_read_b64_tr_b16 a[106:107], v209 offset:1024\n" \
"v_mfma_f32_16x16x32_bf16 v[92:95], a[84:87], a[220:223], v[92:95]\n" \
"s_add_u32 s79, s79, 65601536\n" \
"ds_read_b64_tr_b16 a[108:109], v209 offset:12288\n" \
"v_mfma_f32_16x16x32_bf16 v[88:91], a[80:83], a[208:211], v[88:91]\n" \
"s_lshr_b32 s70, s64, 1\n" \
"ds_read_b64_tr_b16 a[110:111], v209 offset:13312\n" \
"v_mfma_f32_16x16x32_bf16 v[88:91], a[84:87], a[212:215], v[88:91]\n" \
"global_load_lds_dwordx4 v252, s[44:45] offset:1024\n" \
"v_mfma_f32_16x16x32_bf16 v[96:99], a[88:91], a[208:211], v[96:99]\n" \
"ds_read_b128 a[80:83], v205 offset:4096\n" \
"s_lshl_b32 s70, -1, s70\n" \
"v_mfma_f32_16x16x32_bf16 v[96:99], a[92:95], a[212:215], v[96:99]\n" \
"ds_read_b128 a[84:87], v206 offset:4096\n" \
"s_add_u32 s71, s64, 1\n" \
"v_mfma_f32_16x16x32_bf16 v[100:103], a[88:91], a[216:219], v[100:103]\n" \
"s_lshr_b32 s71, s71, 1\n" \
"ds_read_b64_tr_b16 a[112:113], v210 offset:0\n" \
"v_mfma_f32_16x16x32_bf16 v[100:103], a[92:95], a[220:223], v[100:103]\n" \
"global_load_lds_dwordx4 v252, s[44:45] offset:3072\n" \
"v_mfma_f32_16x16x32_bf16 v[60:63], a[200:203], a[88:91], v[60:63]\n" \
"s_add_u32 s44, s44, 0x80\n" \
"s_addc_u32 s45, s45, 0\n" \
"ds_read_b64_tr_b16 a[114:115], v210 offset:1024\n" \
"v_mfma_f32_16x16x32_bf16 v[60:63], a[204:207], a[92:95], v[60:63]\n" \
"s_bfm_b32 s71, s71, 0\n" \
"ds_read_b64_tr_b16 a[116:117], v210 offset:12288\n" \
"v_mfma_f32_16x16x32_bf16 v[68:71], a[200:203], a[192:195], v[68:71]\n" \
"ds_read_b128 a[88:91], v205 offset:8192\n" \
"s_add_u32 m0, s84, 24576\n" \
"v_mfma_f32_16x16x32_bf16 v[68:71], a[204:207], a[196:199], v[68:71]\n" \
"global_load_lds_dwordx4 v253, s[48:49] offset:-4096\n" \
"v_mfma_f32_16x16x32_bf16 v[108:111], a[192:195], a[216:219], v[108:111]\n" \
"s_cmp_eq_u32 s78, 0\n" \
"s_cselect_b32 s80, s71, s70\n" \
"ds_read_b128 a[92:95], v206 offset:8192\n" \
"v_mfma_f32_16x16x32_bf16 v[108:111], a[196:199], a[220:223], v[108:111]\n" \
"s_mov_b32 s16, 0xff000000\n" \
"ds_read_b64_tr_b16 a[118:119], v210 offset:13312\n" \
"v_mfma_f32_16x16x32_bf16 v[104:107], a[192:195], a[208:211], v[104:107]\n" \
"s_mov_b32 s17, 0\n" \
"v_add_u32 v222, 0x1c90080, v250\n" \
"v_cndmask_b32 v222, v250, v222, s[16:17]\n" \
"v_mfma_f32_16x16x32_bf16 v[104:107], a[196:199], a[212:215], v[104:107]\n" \
"s_waitcnt lgkmcnt(2)\n" \
"global_load_lds_dwordx4 v253, s[48:49] offset:-2048\n" \
"v_mfma_f32_16x16x32_bf16 v[40:43], a[96:99], a[72:75], v[40:43]\n" \
"ds_read_b128 a[192:195], v205 offset:12288\n" \
"s_mov_b32 s74, 98304\n" \
"v_mfma_f32_16x16x32_bf16 v[40:43], a[100:103], a[76:79], v[40:43]\n" \
"s_sub_u32 s74, s74, 32768\n" \
"s_cselect_b32 s74, 131072, s74\n" \
"ds_read_b128 a[196:199], v206 offset:12288\n" \
"v_mfma_f32_16x16x32_bf16 v[0:3], a[104:107], a[72:75], v[0:3]\n" \
"ds_read_b64_tr_b16 a[208:209], v209 offset:4096\n" \
"v_add_u32 v213, s74, v211\n" \
"v_mfma_f32_16x16x32_bf16 v[0:3], a[108:111], a[76:79], v[0:3]\n" \
"s_waitcnt lgkmcnt(3)\n" \
"global_load_lds_dwordx4 v253, s[48:49] offset:0\n" \
"v_add_u32 v214, s74, v212\n" \
"v_mfma_f32_16x16x32_bf16 v[4:7], a[112:115], a[72:75], v[4:7]\n" \
"s_add_u32 s48, s48, 0x60000\n" \
"s_addc_u32 s49, s49, 0\n" \
"ds_read_b64_tr_b16 a[210:211], v209 offset:5120\n" \
"v_mfma_f32_16x16x32_bf16 v[4:7], a[116:119], a[76:79], v[4:7]\n" \
"ds_read_b64_tr_b16 a[212:213], v209 offset:16384\n" \
"v_xor_b32 v215, 32, v214\n" \
"v_mfma_f32_16x16x32_bf16 v[12:15], a[112:115], a[80:83], v[12:15]\n" \
"s_lshl_b32 s89, s61, 4\n" \
"ds_read_b64_tr_b16 a[214:215], v209 offset:17408\n" \
"v_subrev_u32 v147, s89, v251\n" \
"v_mfma_f32_16x16x32_bf16 v[12:15], a[116:119], a[84:87], v[12:15]\n" \
"global_load_lds_dwordx4 v253, s[50:51] offset:-3072\n" \
"v_mfma_f32_16x16x32_bf16 v[8:11], a[104:107], a[80:83], v[8:11]\n" \
"s_and_b32 s90, s86, 0xff800000\n" \
"ds_read_b64_tr_b16 a[216:217], v210 offset:4096\n" \
"Lpg10312_%=:\n" \
"s_cbranch_execz Lpg12217_%=\n" \
"v_subrev_u32 v152, 0, v147\n" \
"v_mfma_f32_16x16x32_bf16 v[8:11], a[108:111], a[84:87], v[8:11]\n" \
"ds_read_b64_tr_b16 a[218:219], v210 offset:5120\n" \
"v_sub_u32 v144, 7, v147\n" \
"v_mfma_f32_16x16x32_bf16 v[48:51], a[96:99], a[80:83], v[48:51]\n" \
"ds_read_b64_tr_b16 a[220:221], v210 offset:16384\n" \
"v_or_b32 v152, v152, v144\n" \
"v_mfma_f32_16x16x32_bf16 v[48:51], a[100:103], a[84:87], v[48:51]\n" \
"global_load_lds_dwordx4 v253, s[50:51] offset:-1024\n" \
"v_ashrrev_i32 v152, 31, v152\n" \
"v_mfma_f32_16x16x32_bf16 v[56:59], a[96:99], a[88:91], v[56:59]\n" \
"ds_read_b64_tr_b16 a[222:223], v210 offset:17408\n" \
"v_and_b32 v152, 0xff800000, v152\n" \
"v_mfma_f32_16x16x32_bf16 v[56:59], a[100:103], a[92:95], v[56:59]\n" \
"ds_read_b64_tr_b16 a[200:201], v210 offset:2048\n" \
"v_mov_b32 v160, v152\n" \
"v_mfma_f32_16x16x32_bf16 v[16:19], a[104:107], a[88:91], v[16:19]\n" \
"ds_read_b64_tr_b16 a[202:203], v210 offset:3072\n" \
"v_mov_b32 v168, v152\n" \
"v_mfma_f32_16x16x32_bf16 v[16:19], a[108:111], a[92:95], v[16:19]\n" \
"global_load_lds_dwordx4 v253, s[50:51] offset:1024\n" \
"v_mov_b32 v176, v152\n" \
"v_mfma_f32_16x16x32_bf16 v[20:23], a[112:115], a[88:91], v[20:23]\n" \
"s_add_u32 s50, s50, 0x60000\n" \
"s_addc_u32 s51, s51, 0\n" \
"ds_read_b64_tr_b16 a[204:205], v210 offset:14336\n" \
"v_mfma_f32_16x16x32_bf16 v[20:23], a[116:119], a[92:95], v[20:23]\n" \
"s_waitcnt lgkmcnt(3)\n" \
"ds_read_b64_tr_b16 a[206:207], v210 offset:15360\n" \
"v_sub_u32 v156, -9, v147\n" \
"v_mfma_f32_16x16x32_bf16 v[28:31], a[112:115], a[192:195], v[28:31]\n" \
"v_add_u32 v205, 0xffff3000, v205\n" \
"v_add_u32 v206, 0xffff3000, v206\n" \
"v_mfma_f32_16x16x32_bf16 v[28:31], a[116:119], a[196:199], v[28:31]\n" \
"buffer_load_dword v220, v221, s[56:59], s81 offen sc1\n" \
"v_mfma_f32_16x16x32_bf16 v[24:27], a[104:107], a[192:195], v[24:27]\n" \
"v_add_u32 v209, 0xffff3000, v209\n" \
"v_add_u32 v210, 0xffff3000, v210\n" \
"v_mfma_f32_16x16x32_bf16 v[24:27], a[108:111], a[196:199], v[24:27]\n" \
"s_waitcnt vmcnt(11) lgkmcnt(0)\n" \
"v_ashrrev_i32 v156, 31, v156\n" \
"v_and_b32 v156, 0xff800000, v156\n" \
"v_mfma_f32_16x16x32_bf16 v[64:67], a[96:99], a[192:195], v[64:67]\n" \
"v_mov_b32 v164, v156\n" \
"v_mov_b32 v172, v156\n" \
"v_mfma_f32_16x16x32_bf16 v[64:67], a[100:103], a[196:199], v[64:67]\n" \
"s_barrier\n" \
"v_mfma_f32_16x16x32_bf16 v[80:83], a[72:75], a[208:211], v[80:83]\n" \
"ds_read_b64_tr_b16 a[96:97], v209 offset:2048\n" \
"v_or_b32 v148, s90, v156\n" \
"v_mfma_f32_16x16x32_bf16 v[80:83], a[76:79], a[212:215], v[80:83]\n" \
"ds_read_b64_tr_b16 a[98:99], v209 offset:3072\n" \
"v_mov_b32 v188, 0x3f803f80\n" \
"v_mfma_f32_16x16x32_bf16 v[84:87], a[72:75], a[216:219], v[84:87]\n" \
"s_add_u32 m0, s95, 118784\n" \
"ds_read_b64_tr_b16 a[100:101], v209 offset:14336\n" \
"v_subrev_u32 v153, 1, v147\n" \
"v_mfma_f32_16x16x32_bf16 v[84:87], a[76:79], a[220:223], v[84:87]\n" \
"global_load_lds_dwordx4 v252, s[42:43] offset:0\n" \
"v_sub_u32 v144, 8, v147\n" \
"v_mfma_f32_16x16x32_bf16 v[44:47], a[200:203], a[72:75], v[44:47]\n" \
"ds_read_b64_tr_b16 a[102:103], v209 offset:15360\n" \
"v_or_b32 v153, v153, v144\n" \
"v_mfma_f32_16x16x32_bf16 v[44:47], a[204:207], a[76:79], v[44:47]\n" \
"ds_read_b128 a[72:75], v205 offset:0\n" \
"ds_read_b64_tr_b16 a[104:105], v209 offset:0\n" \
"v_mfma_f32_16x16x32_bf16 v[52:55], a[200:203], a[80:83], v[52:55]\n" \
"ds_read_b128 a[76:79], v206 offset:0\n" \
"v_ashrrev_i32 v153, 31, v153\n" \
"v_mfma_f32_16x16x32_bf16 v[52:55], a[204:207], a[84:87], v[52:55]\n" \
"global_load_lds_dwordx4 v252, s[42:43] offset:2048\n" \
"v_and_b32 v153, 0xff800000, v153\n" \
"v_mfma_f32_16x16x32_bf16 v[92:95], a[80:83], a[216:219], v[92:95]\n" \
"ds_read_b64_tr_b16 a[106:107], v209 offset:1024\n" \
"v_mov_b32 v161, v153\n" \
"v_mfma_f32_16x16x32_bf16 v[92:95], a[84:87], a[220:223], v[92:95]\n" \
"ds_read_b64_tr_b16 a[108:109], v209 offset:12288\n" \
"v_mov_b32 v169, v153\n" \
"v_mfma_f32_16x16x32_bf16 v[88:91], a[80:83], a[208:211], v[88:91]\n" \
"ds_read_b64_tr_b16 a[110:111], v209 offset:13312\n" \
"v_mov_b32 v177, v153\n" \
"v_mfma_f32_16x16x32_bf16 v[88:91], a[84:87], a[212:215], v[88:91]\n" \
"global_load_lds_dwordx4 v252, s[44:45] offset:1024\n" \
"v_sub_u32 v157, -8, v147\n" \
"v_mfma_f32_16x16x32_bf16 v[96:99], a[88:91], a[208:211], v[96:99]\n" \
"ds_read_b128 a[80:83], v205 offset:4096\n" \
"v_ashrrev_i32 v157, 31, v157\n" \
"v_mfma_f32_16x16x32_bf16 v[96:99], a[92:95], a[212:215], v[96:99]\n" \
"ds_read_b128 a[84:87], v206 offset:4096\n" \
"v_and_b32 v157, 0xff800000, v157\n" \
"v_mfma_f32_16x16x32_bf16 v[100:103], a[88:91], a[216:219], v[100:103]\n" \
"ds_read_b64_tr_b16 a[112:113], v210 offset:0\n" \
"v_mov_b32 v165, v157\n" \
"v_mfma_f32_16x16x32_bf16 v[100:103], a[92:95], a[220:223], v[100:103]\n" \
"global_load_lds_dwordx4 v252, s[44:45] offset:3072\n" \
"v_mov_b32 v173, v157\n" \
"v_mfma_f32_16x16x32_bf16 v[60:63], a[200:203], a[88:91], v[60:63]\n" \
"ds_read_b64_tr_b16 a[114:115], v210 offset:1024\n" \
"v_or_b32 v149, s90, v157\n" \
"v_mfma_f32_16x16x32_bf16 v[60:63], a[204:207], a[92:95], v[60:63]\n" \
"ds_read_b64_tr_b16 a[116:117], v210 offset:12288\n" \
"v_mov_b32 v189, 0x3f803f80\n" \
"v_mfma_f32_16x16x32_bf16 v[68:71], a[200:203], a[192:195], v[68:71]\n" \
"s_add_u32 m0, s84, 143360\n" \
"ds_read_b128 a[88:91], v205 offset:8192\n" \
"v_subrev_u32 v154, 2, v147\n" \
"v_mfma_f32_16x16x32_bf16 v[68:71], a[204:207], a[196:199], v[68:71]\n" \
"global_load_lds_dwordx4 v253, s[48:49] offset:-4096\n" \
"v_sub_u32 v144, 9, v147\n" \
"v_mfma_f32_16x16x32_bf16 v[108:111], a[192:195], a[216:219], v[108:111]\n" \
"ds_read_b128 a[92:95], v206 offset:8192\n" \
"v_or_b32 v154, v154, v144\n" \
"v_mfma_f32_16x16x32_bf16 v[108:111], a[196:199], a[220:223], v[108:111]\n" \
"ds_read_b64_tr_b16 a[118:119], v210 offset:13312\n" \
"v_ashrrev_i32 v154, 31, v154\n" \
"v_mfma_f32_16x16x32_bf16 v[104:107], a[192:195], a[208:211], v[104:107]\n" \
"v_and_b32 v154, 0xff800000, v154\n" \
"v_mov_b32 v162, v154\n" \
"v_mfma_f32_16x16x32_bf16 v[104:107], a[196:199], a[212:215], v[104:107]\n" \
"s_waitcnt lgkmcnt(2)\n" \
"global_load_lds_dwordx4 v253, s[48:49] offset:-2048\n" \
"v_mov_b32 v170, v154\n" \
"v_mfma_f32_16x16x32_bf16 v[40:43], a[96:99], a[72:75], v[40:43]\n" \
"ds_read_b128 a[192:195], v205 offset:12288\n" \
"v_mov_b32 v178, v154\n" \
"v_mfma_f32_16x16x32_bf16 v[40:43], a[100:103], a[76:79], v[40:43]\n" \
"ds_read_b128 a[196:199], v206 offset:12288\n" \
"v_sub_u32 v158, -7, v147\n" \
"v_mfma_f32_16x16x32_bf16 v[0:3], a[104:107], a[72:75], v[0:3]\n" \
"ds_read_b64_tr_b16 a[208:209], v209 offset:4096\n" \
"v_ashrrev_i32 v158, 31, v158\n" \
"v_mfma_f32_16x16x32_bf16 v[0:3], a[108:111], a[76:79], v[0:3]\n" \
"s_waitcnt lgkmcnt(3)\n" \
"global_load_lds_dwordx4 v253, s[48:49] offset:0\n" \
"v_and_b32 v158, 0xff800000, v158\n" \
"v_mfma_f32_16x16x32_bf16 v[4:7], a[112:115], a[72:75], v[4:7]\n" \
"ds_read_b64_tr_b16 a[210:211], v209 offset:5120\n" \
"v_mov_b32 v166, v158\n" \
"v_mfma_f32_16x16x32_bf16 v[4:7], a[116:119], a[76:79], v[4:7]\n" \
"ds_read_b64_tr_b16 a[212:213], v209 offset:16384\n" \
"v_mov_b32 v174, v158\n" \
"v_mfma_f32_16x16x32_bf16 v[12:15], a[112:115], a[80:83], v[12:15]\n" \
"ds_read_b64_tr_b16 a[214:215], v209 offset:17408\n" \
"v_or_b32 v150, s90, v158\n" \
"v_mfma_f32_16x16x32_bf16 v[12:15], a[116:119], a[84:87], v[12:15]\n" \
"global_load_lds_dwordx4 v253, s[50:51] offset:-3072\n" \
"v_mov_b32 v190, 0x3f803f80\n" \
"v_mfma_f32_16x16x32_bf16 v[8:11], a[104:107], a[80:83], v[8:11]\n" \
"ds_read_b64_tr_b16 a[216:217], v210 offset:4096\n" \
"v_subrev_u32 v155, 3, v147\n" \
"v_mfma_f32_16x16x32_bf16 v[8:11], a[108:111], a[84:87], v[8:11]\n" \
"ds_read_b64_tr_b16 a[218:219], v210 offset:5120\n" \
"v_sub_u32 v144, 10, v147\n" \
"v_mfma_f32_16x16x32_bf16 v[48:51], a[96:99], a[80:83], v[48:51]\n" \
"ds_read_b64_tr_b16 a[220:221], v210 offset:16384\n" \
"v_or_b32 v155, v155, v144\n" \
"v_mfma_f32_16x16x32_bf16 v[48:51], a[100:103], a[84:87], v[48:51]\n" \
"global_load_lds_dwordx4 v253, s[50:51] offset:-1024\n" \
"v_ashrrev_i32 v155, 31, v155\n" \
"v_mfma_f32_16x16x32_bf16 v[56:59], a[96:99], a[88:91], v[56:59]\n" \
"ds_read_b64_tr_b16 a[222:223], v210 offset:17408\n" \
"v_and_b32 v155, 0xff800000, v155\n" \
"v_mfma_f32_16x16x32_bf16 v[56:59], a[100:103], a[92:95], v[56:59]\n" \
"ds_read_b64_tr_b16 a[200:201], v210 offset:2048\n" \
"v_mov_b32 v163, v155\n" \
"v_mfma_f32_16x16x32_bf16 v[16:19], a[104:107], a[88:91], v[16:19]\n" \
"ds_read_b64_tr_b16 a[202:203], v210 offset:3072\n" \
"v_mov_b32 v171, v155\n" \
"v_mfma_f32_16x16x32_bf16 v[16:19], a[108:111], a[92:95], v[16:19]\n" \
"global_load_lds_dwordx4 v253, s[50:51] offset:1024\n" \
"v_mov_b32 v179, v155\n" \
"v_mfma_f32_16x16x32_bf16 v[20:23], a[112:115], a[88:91], v[20:23]\n" \
"ds_read_b64_tr_b16 a[204:205], v210 offset:14336\n" \
"v_sub_u32 v159, -6, v147\n" \
"v_mfma_f32_16x16x32_bf16 v[20:23], a[116:119], a[92:95], v[20:23]\n" \
"s_waitcnt lgkmcnt(3)\n" \
"ds_read_b64_tr_b16 a[206:207], v210 offset:15360\n" \
"v_ashrrev_i32 v159, 31, v159\n" \
"v_mfma_f32_16x16x32_bf16 v[28:31], a[112:115], a[192:195], v[28:31]\n" \
"v_add_u32 v205, 0xffff0000, v205\n" \
"v_add_u32 v206, 0xffff0000, v206\n" \
"v_mfma_f32_16x16x32_bf16 v[28:31], a[116:119], a[196:199], v[28:31]\n" \
"v_add_u32 v209, 0xffff0000, v209\n" \
"v_add_u32 v210, 0xffff0000, v210\n" \
"v_mfma_f32_16x16x32_bf16 v[24:27], a[104:107], a[192:195], v[24:27]\n" \
"v_and_b32 v159, 0xff800000, v159\n" \
"v_mov_b32 v167, v159\n" \
"v_mfma_f32_16x16x32_bf16 v[24:27], a[108:111], a[196:199], v[24:27]\n" \
"s_waitcnt vmcnt(11) lgkmcnt(0)\n" \
"v_mov_b32 v175, v159\n" \
"v_or_b32 v151, s90, v159\n" \
"v_mfma_f32_16x16x32_bf16 v[64:67], a[96:99], a[192:195], v[64:67]\n" \
"v_mov_b32 v191, 0x3f803f80\n" \
"v_mov_b32 v144, 0x3e8293ee\n" \
"v_mfma_f32_16x16x32_bf16 v[64:67], a[100:103], a[196:199], v[64:67]\n" \
"s_barrier\n" \
"v_mfma_f32_16x16x32_bf16 v[80:83], a[72:75], a[208:211], v[80:83]\n" \
"s_waitcnt vmcnt(10)\n" \
"ds_read_b64_tr_b16 a[96:97], v209 offset:2048\n" \
"v_cmp_ne_u32 vcc, s60, v220\n" \
"v_mfma_f32_16x16x32_bf16 v[80:83], a[76:79], a[212:215], v[80:83]\n" \
"s_lshr_b32 s89, s61, 1\n" \
"ds_read_b64_tr_b16 a[98:99], v209 offset:3072\n" \
"v_mfma_f32_16x16x32_bf16 v[84:87], a[72:75], a[216:219], v[84:87]\n" \
"s_cmp_eq_u32 s89, 0\n" \
"s_cselect_b32 s16, 0, 0xff00\n" \
"ds_read_b64_tr_b16 a[100:101], v209 offset:14336\n" \
"v_mfma_f32_16x16x32_bf16 v[84:87], a[76:79], a[220:223], v[84:87]\n" \
"s_and_b32 vcc_hi, vcc_lo, s80\n" \
"s_cbranch_scc1 Lpl2_%=\n" \
"Lpr2_%=:\n" \
"v_mfma_f32_16x16x32_bf16 v[44:47], a[200:203], a[72:75], v[44:47]\n" \
"s_add_u32 m0, s75, s95\n" \
"ds_read_b64_tr_b16 a[102:103], v209 offset:15360\n" \
"v_mfma_f32_16x16x32_bf16 v[44:47], a[204:207], a[76:79], v[44:47]\n" \
"s_mov_b32 s17, s16\n" \
"ds_read_b128 a[72:75], v205 offset:0\n" \
"ds_read_b64_tr_b16 a[104:105], v209 offset:0\n" \
"v_mfma_f32_16x16x32_bf16 v[52:55], a[200:203], a[80:83], v[52:55]\n" \
"ds_read_b128 a[76:79], v206 offset:0\n" \
"global_load_lds_dwordx4 v222, s[36:37] offset:0 sc1\n" \
"v_mfma_f32_16x16x32_bf16 v[52:55], a[204:207], a[84:87], v[52:55]\n" \
"v_add_u32 v146, 0x1c90080, v249\n" \
"v_cndmask_b32 v146, v249, v146, s[16:17]\n" \
"v_mfma_f32_16x16x32_bf16 v[92:95], a[80:83], a[216:219], v[92:95]\n" \
"s_andn2_b32 s12, s12, s86\n" \
"ds_read_b64_tr_b16 a[106:107], v209 offset:1024\n" \
"v_mfma_f32_16x16x32_bf16 v[92:95], a[84:87], a[220:223], v[92:95]\n" \
"s_mov_b32 s13, s12\n" \
"ds_read_b64_tr_b16 a[108:109], v209 offset:12288\n" \
"global_load_lds_dwordx4 v222, s[36:37] offset:1024 sc1\n" \
"v_mfma_f32_16x16x32_bf16 v[88:91], a[80:83], a[208:211], v[88:91]\n" \
"s_not_b32 s15, s86\n" \
"ds_read_b64_tr_b16 a[110:111], v209 offset:13312\n" \
"v_mfma_f32_16x16x32_bf16 v[88:91], a[84:87], a[212:215], v[88:91]\n" \
"s_or_b64 s[92:93], s[12:13], s[14:15]\n" \
"v_mfma_f32_16x16x32_bf16 v[96:99], a[88:91], a[208:211], v[96:99]\n" \
"ds_read_b128 a[80:83], v205 offset:4096\n" \
"v_mfma_f32_16x16x32_bf16 v[96:99], a[92:95], a[212:215], v[96:99]\n" \
"ds_read_b128 a[84:87], v206 offset:4096\n" \
"global_load_lds_dwordx4 v222, s[36:37] offset:2048 sc1\n" \
"v_mfma_f32_16x16x32_bf16 v[100:103], a[88:91], a[216:219], v[100:103]\n" \
"ds_read_b64_tr_b16 a[112:113], v210 offset:0\n" \
"v_mfma_f32_16x16x32_bf16 v[100:103], a[92:95], a[220:223], v[100:103]\n" \
"v_mfma_f32_16x16x32_bf16 v[60:63], a[200:203], a[88:91], v[60:63]\n" \
"ds_read_b64_tr_b16 a[114:115], v210 offset:1024\n" \
"v_mfma_f32_16x16x32_bf16 v[60:63], a[204:207], a[92:95], v[60:63]\n" \
"ds_read_b64_tr_b16 a[116:117], v210 offset:12288\n" \
"global_load_lds_dwordx4 v222, s[36:37] offset:3072 sc1\n" \
"v_mfma_f32_16x16x32_bf16 v[68:71], a[200:203], a[192:195], v[68:71]\n" \
"ds_read_b128 a[88:91], v205 offset:8192\n" \
"s_add_u32 s36, s36, 0x1000\n" \
"v_mfma_f32_16x16x32_bf16 v[68:71], a[204:207], a[196:199], v[68:71]\n" \
"v_mfma_f32_16x16x32_bf16 v[108:111], a[192:195], a[216:219], v[108:111]\n" \
"ds_read_b128 a[92:95], v206 offset:8192\n" \
"s_add_u32 m0, m0, 0x5000\n" \
"v_mfma_f32_16x16x32_bf16 v[108:111], a[196:199], a[220:223], v[108:111]\n" \
"ds_read_b64_tr_b16 a[118:119], v210 offset:13312\n" \
"global_load_lds_dwordx4 v216, s[38:39] offset:-4096\n" \
"v_mfma_f32_16x16x32_bf16 v[104:107], a[192:195], a[208:211], v[104:107]\n" \
"v_mfma_f32_16x16x32_bf16 v[104:107], a[196:199], a[212:215], v[104:107]\n" \
"s_waitcnt lgkmcnt(2)\n" \
"v_mfma_f32_16x16x32_bf16 v[40:43], a[96:99], a[72:75], v[40:43]\n" \
"ds_read_b128 a[192:195], v205 offset:12288\n" \
"v_mfma_f32_16x16x32_bf16 v[40:43], a[100:103], a[76:79], v[40:43]\n" \
"ds_read_b128 a[196:199], v206 offset:12288\n" \
"global_load_lds_dwordx4 v217, s[38:39] offset:-3072\n" \
"v_mfma_f32_16x16x32_bf16 v[0:3], a[104:107], a[72:75], v[0:3]\n" \
"ds_read_b64_tr_b16 a[208:209], v209 offset:4096\n" \
"v_mfma_f32_16x16x32_bf16 v[0:3], a[108:111], a[76:79], v[0:3]\n" \
"s_waitcnt lgkmcnt(3)\n" \
"v_mfma_f32_16x16x32_bf16 v[4:7], a[112:115], a[72:75], v[4:7]\n" \
"ds_read_b64_tr_b16 a[210:211], v209 offset:5120\n" \
"v_mfma_f32_16x16x32_bf16 v[4:7], a[116:119], a[76:79], v[4:7]\n" \
"ds_read_b64_tr_b16 a[212:213], v209 offset:16384\n" \
"global_load_lds_dwordx4 v218, s[38:39] offset:-2048\n" \
"v_mfma_f32_16x16x32_bf16 v[12:15], a[112:115], a[80:83], v[12:15]\n" \
"ds_read_b64_tr_b16 a[214:215], v209 offset:17408\n" \
"v_mfma_f32_16x16x32_bf16 v[12:15], a[116:119], a[84:87], v[12:15]\n" \
"v_mfma_f32_16x16x32_bf16 v[8:11], a[104:107], a[80:83], v[8:11]\n" \
"ds_read_b64_tr_b16 a[216:217], v210 offset:4096\n" \
"v_mfma_f32_16x16x32_bf16 v[8:11], a[108:111], a[84:87], v[8:11]\n" \
"ds_read_b64_tr_b16 a[218:219], v210 offset:5120\n" \
"global_load_lds_dwordx4 v219, s[38:39] offset:-1024\n" \
"v_mfma_f32_16x16x32_bf16 v[48:51], a[96:99], a[80:83], v[48:51]\n" \
"s_add_u32 s38, s38, 0x20000\n" \
"s_addc_u32 s39, s39, 0\n" \
"ds_read_b64_tr_b16 a[220:221], v210 offset:16384\n" \
"v_mfma_f32_16x16x32_bf16 v[48:51], a[100:103], a[84:87], v[48:51]\n" \
"s_sub_u32 s75, s75, 32768\n" \
"s_cselect_b32 s75, 131072, s75\n" \
"v_mfma_f32_16x16x32_bf16 v[56:59], a[96:99], a[88:91], v[56:59]\n" \
"ds_read_b64_tr_b16 a[222:223], v210 offset:17408\n" \
"v_mfma_f32_16x16x32_bf16 v[56:59], a[100:103], a[92:95], v[56:59]\n" \
"ds_read_b64_tr_b16 a[200:201], v210 offset:2048\n" \
"v_mfma_f32_16x16x32_bf16 v[16:19], a[104:107], a[88:91], v[16:19]\n" \
"ds_read_b64_tr_b16 a[202:203], v210 offset:3072\n" \
"v_mfma_f32_16x16x32_bf16 v[16:19], a[108:111], a[92:95], v[16:19]\n" \
"v_mfma_f32_16x16x32_bf16 v[20:23], a[112:115], a[88:91], v[20:23]\n" \
"ds_read_b64_tr_b16 a[204:205], v210 offset:14336\n" \
"v_mfma_f32_16x16x32_bf16 v[20:23], a[116:119], a[92:95], v[20:23]\n" \
"s_waitcnt lgkmcnt(3)\n" \
"ds_read_b64_tr_b16 a[206:207], v210 offset:15360\n" \
"v_mfma_f32_16x16x32_bf16 v[28:31], a[112:115], a[192:195], v[28:31]\n" \
"v_add_u32 v205, 0x1d000, v205\n" \
"v_add_u32 v206, 0x1d000, v206\n" \
"v_mfma_f32_16x16x32_bf16 v[28:31], a[116:119], a[196:199], v[28:31]\n" \
"v_add_u32 v209, 0x1d000, v209\n" \
"v_add_u32 v210, 0x1d000, v210\n" \
"v_mfma_f32_16x16x32_bf16 v[24:27], a[104:107], a[192:195], v[24:27]\n" \
"v_mfma_f32_16x16x32_bf16 v[24:27], a[108:111], a[196:199], v[24:27]\n" \
"s_waitcnt vmcnt(8) lgkmcnt(0)\n" \
"v_mfma_f32_16x16x32_bf16 v[64:67], a[96:99], a[192:195], v[64:67]\n" \
"v_mfma_f32_16x16x32_bf16 v[64:67], a[100:103], a[196:199], v[64:67]\n" \
"s_barrier\n" \
"v_mfma_f32_16x16x32_bf16 v[80:83], a[72:75], a[208:211], v[80:83]\n" \
"ds_read_b64_tr_b16 a[96:97], v209 offset:2048\n" \
"v_mfma_f32_16x16x32_bf16 v[80:83], a[76:79], a[212:215], v[80:83]\n" \
"ds_read_b64_tr_b16 a[98:99], v209 offset:3072\n" \
"v_mfma_f32_16x16x32_bf16 v[84:87], a[72:75], a[216:219], v[84:87]\n" \
"s_add_u32 m0, s75, s95\n" \
"ds_read_b64_tr_b16 a[100:101], v209 offset:14336\n" \
"v_mfma_f32_16x16x32_bf16 v[84:87], a[76:79], a[220:223], v[84:87]\n" \
"global_load_lds_dwordx4 v222, s[36:37] offset:0 sc1\n" \
"v_mfma_f32_16x16x32_bf16 v[44:47], a[200:203], a[72:75], v[44:47]\n" \
"ds_read_b64_tr_b16 a[102:103], v209 offset:15360\n" \
"v_mfma_f32_16x16x32_bf16 v[44:47], a[204:207], a[76:79], v[44:47]\n" \
"ds_read_b128 a[72:75], v205 offset:0\n" \
"ds_read_b64_tr_b16 a[104:105], v209 offset:0\n" \
"v_mfma_f32_16x16x32_bf16 v[52:55], a[200:203], a[80:83], v[52:55]\n" \
"ds_read_b128 a[76:79], v206 offset:0\n" \
"v_mfma_f32_16x16x32_bf16 v[52:55], a[204:207], a[84:87], v[52:55]\n" \
"global_load_lds_dwordx4 v222, s[36:37] offset:1024 sc1\n" \
"v_mfma_f32_16x16x32_bf16 v[92:95], a[80:83], a[216:219], v[92:95]\n" \
"ds_read_b64_tr_b16 a[106:107], v209 offset:1024\n" \
"v_mfma_f32_16x16x32_bf16 v[92:95], a[84:87], a[220:223], v[92:95]\n" \
"ds_read_b64_tr_b16 a[108:109], v209 offset:12288\n" \
"v_mfma_f32_16x16x32_bf16 v[88:91], a[80:83], a[208:211], v[88:91]\n" \
"ds_read_b64_tr_b16 a[110:111], v209 offset:13312\n" \
"v_mfma_f32_16x16x32_bf16 v[88:91], a[84:87], a[212:215], v[88:91]\n" \
"global_load_lds_dwordx4 v222, s[36:37] offset:2048 sc1\n" \
"v_mfma_f32_16x16x32_bf16 v[96:99], a[88:91], a[208:211], v[96:99]\n" \
"ds_read_b128 a[80:83], v205 offset:4096\n" \
"v_mfma_f32_16x16x32_bf16 v[96:99], a[92:95], a[212:215], v[96:99]\n" \
"ds_read_b128 a[84:87], v206 offset:4096\n" \
"v_mfma_f32_16x16x32_bf16 v[100:103], a[88:91], a[216:219], v[100:103]\n" \
"ds_read_b64_tr_b16 a[112:113], v210 offset:0\n" \
"v_mfma_f32_16x16x32_bf16 v[100:103], a[92:95], a[220:223], v[100:103]\n" \
"global_load_lds_dwordx4 v222, s[36:37] offset:3072 sc1\n" \
"v_mfma_f32_16x16x32_bf16 v[60:63], a[200:203], a[88:91], v[60:63]\n" \
"s_add_u32 s36, s36, 0x1000\n" \
"ds_read_b64_tr_b16 a[114:115], v210 offset:1024\n" \
"v_mfma_f32_16x16x32_bf16 v[60:63], a[204:207], a[92:95], v[60:63]\n" \
"ds_read_b64_tr_b16 a[116:117], v210 offset:12288\n" \
"v_mfma_f32_16x16x32_bf16 v[68:71], a[200:203], a[192:195], v[68:71]\n" \
"ds_read_b128 a[88:91], v205 offset:8192\n" \
"s_add_u32 m0, m0, 0x5000\n" \
"v_mfma_f32_16x16x32_bf16 v[68:71], a[204:207], a[196:199], v[68:71]\n" \
"global_load_lds_dwordx4 v216, s[38:39] offset:-4096\n" \
"v_mfma_f32_16x16x32_bf16 v[108:111], a[192:195], a[216:219], v[108:111]\n" \
"ds_read_b128 a[92:95], v206 offset:8192\n" \
"v_mfma_f32_16x16x32_bf16 v[108:111], a[196:199], a[220:223], v[108:111]\n" \
"ds_read_b64_tr_b16 a[118:119], v210 offset:13312\n" \
"v_mfma_f32_16x16x32_bf16 v[104:107], a[192:195], a[208:211], v[104:107]\n" \
"v_mfma_f32_16x16x32_bf16 v[104:107], a[196:199], a[212:215], v[104:107]\n" \
"s_waitcnt lgkmcnt(2)\n" \
"global_load_lds_dwordx4 v217, s[38:39] offset:-3072\n" \
"v_mfma_f32_16x16x32_bf16 v[40:43], a[96:99], a[72:75], v[40:43]\n" \
"ds_read_b128 a[192:195], v205 offset:12288\n" \
"v_mfma_f32_16x16x32_bf16 v[40:43], a[100:103], a[76:79], v[40:43]\n" \
"ds_read_b128 a[196:199], v206 offset:12288\n" \
"v_mfma_f32_16x16x32_bf16 v[0:3], a[104:107], a[72:75], v[0:3]\n" \
"ds_read_b64_tr_b16 a[208:209], v209 offset:4096\n" \
"v_mfma_f32_16x16x32_bf16 v[0:3], a[108:111], a[76:79], v[0:3]\n" \
"s_waitcnt lgkmcnt(3)\n" \
"global_load_lds_dwordx4 v218, s[38:39] offset:-2048\n" \
"v_mfma_f32_16x16x32_bf16 v[4:7], a[112:115], a[72:75], v[4:7]\n" \
"ds_read_b64_tr_b16 a[210:211], v209 offset:5120\n" \
"v_mfma_f32_16x16x32_bf16 v[4:7], a[116:119], a[76:79], v[4:7]\n" \
"ds_read_b64_tr_b16 a[212:213], v209 offset:16384\n" \
"v_mfma_f32_16x16x32_bf16 v[48:51], a[96:99], a[80:83], v[48:51]\n" \
"ds_read_b64_tr_b16 a[214:215], v209 offset:17408\n" \
"v_mfma_f32_16x16x32_bf16 v[48:51], a[100:103], a[84:87], v[48:51]\n" \
"global_load_lds_dwordx4 v219, s[38:39] offset:-1024\n" \
"v_mfma_f32_16x16x32_bf16 v[8:11], a[104:107], a[80:83], v[8:11]\n" \
"s_add_u32 s38, s38, 0x20000\n" \
"s_addc_u32 s39, s39, 0\n" \
"ds_read_b64_tr_b16 a[216:217], v210 offset:4096\n" \
"v_mfma_f32_16x16x32_bf16 v[8:11], a[108:111], a[84:87], v[8:11]\n" \
"s_sub_u32 s75, s75, 32768\n" \
"s_cselect_b32 s75, 131072, s75\n" \
"ds_read_b64_tr_b16 a[218:219], v210 offset:5120\n" \
"v_mfma_f32_16x16x32_bf16 v[12:15], a[112:115], a[80:83], v[12:15]\n" \
"s_add_u32 m0, s75, s95\n" \
"ds_read_b64_tr_b16 a[220:221], v210 offset:16384\n" \
"v_cvt_pk_bf16_f32 v112, v0, v1\n" \
"v_mfma_f32_16x16x32_bf16 v[12:15], a[116:119], a[84:87], v[12:15]\n" \
"global_load_lds_dwordx4 v222, s[36:37] offset:0 sc1\n" \
"v_cvt_pk_bf16_f32 v113, v2, v3\n" \
"v_mfma_f32_16x16x32_bf16 v[56:59], a[96:99], a[88:91], v[56:59]\n" \
"ds_read_b64_tr_b16 a[222:223], v210 offset:17408\n" \
"v_cvt_pk_bf16_f32 v114, v4, v5\n" \
"v_mfma_f32_16x16x32_bf16 v[56:59], a[100:103], a[92:95], v[56:59]\n" \
"ds_read_b64_tr_b16 a[200:201], v210 offset:2048\n" \
"v_cvt_pk_bf16_f32 v115, v6, v7\n" \
"v_mfma_f32_16x16x32_bf16 v[16:19], a[104:107], a[88:91], v[16:19]\n" \
"ds_read_b64_tr_b16 a[202:203], v210 offset:3072\n" \
"v_cvt_pk_bf16_f32 v116, v8, v9\n" \
"v_mfma_f32_16x16x32_bf16 v[16:19], a[108:111], a[92:95], v[16:19]\n" \
"global_load_lds_dwordx4 v222, s[36:37] offset:1024 sc1\n" \
"v_cvt_pk_bf16_f32 v117, v10, v11\n" \
"v_mfma_f32_16x16x32_bf16 v[20:23], a[112:115], a[88:91], v[20:23]\n" \
"ds_read_b64_tr_b16 a[204:205], v210 offset:14336\n" \
"v_cvt_pk_bf16_f32 v118, v12, v13\n" \
"v_mfma_f32_16x16x32_bf16 v[20:23], a[116:119], a[92:95], v[20:23]\n" \
"s_waitcnt lgkmcnt(3)\n" \
"ds_read_b64_tr_b16 a[206:207], v210 offset:15360\n" \
"v_cvt_pk_bf16_f32 v119, v14, v15\n" \
"v_mfma_f32_16x16x32_bf16 v[64:67], a[96:99], a[192:195], v[64:67]\n" \
"v_mfma_f32_16x16x32_bf16 v[64:67], a[100:103], a[196:199], v[64:67]\n" \
"global_load_lds_dwordx4 v222, s[36:37] offset:2048 sc1\n" \
"v_mfma_f32_16x16x32_bf16 v[24:27], a[104:107], a[192:195], v[24:27]\n" \
"v_cvt_pk_bf16_f32 v120, v16, v17\n" \
"v_cvt_pk_bf16_f32 v121, v18, v19\n" \
"v_mfma_f32_16x16x32_bf16 v[24:27], a[108:111], a[196:199], v[24:27]\n" \
"s_waitcnt vmcnt(11) lgkmcnt(0)\n" \
"v_cvt_pk_bf16_f32 v122, v20, v21\n" \
"v_cvt_pk_bf16_f32 v123, v22, v23\n" \
"v_mfma_f32_16x16x32_bf16 v[28:31], a[112:115], a[192:195], v[28:31]\n" \
"v_mfma_f32_16x16x32_bf16 v[28:31], a[116:119], a[196:199], v[28:31]\n" \
"s_barrier\n" \
"v_mfma_f32_16x16x32_bf16 v[68:71], a[200:203], a[192:195], v[68:71]\n" \
"v_mfma_f32_16x16x32_bf16 v[68:71], a[204:207], a[196:199], v[68:71]\n" \
"ds_read_b64_tr_b16 a[32:33], v214 offset:16384\n" \
"v_mfma_f32_16x16x32_bf16 v[104:107], a[192:195], a[208:211], v[104:107]\n" \
"ds_read_b64_tr_b16 a[34:35], v214 offset:18432\n" \
"v_cvt_pk_bf16_f32 v124, v24, v25\n" \
"v_mfma_f32_16x16x32_bf16 v[104:107], a[196:199], a[212:215], v[104:107]\n" \
"global_load_lds_dwordx4 v222, s[36:37] offset:3072 sc1\n" \
"v_cvt_pk_bf16_f32 v125, v26, v27\n" \
"v_cvt_pk_bf16_f32 v126, v28, v29\n" \
"v_mfma_f32_16x16x32_bf16 v[108:111], a[192:195], a[216:219], v[108:111]\n" \
"s_add_u32 s36, s36, 0x1000\n" \
"ds_read_b64_tr_b16 a[36:37], v214 offset:24576\n" \
"v_cvt_pk_bf16_f32 v32, v64, v65\n" \
"v_mfma_f32_16x16x32_bf16 v[108:111], a[196:199], a[220:223], v[108:111]\n" \
"ds_read_b64_tr_b16 a[38:39], v214 offset:26624\n" \
"v_cvt_pk_bf16_f32 v33, v66, v67\n" \
"v_mfma_f32_16x16x32_bf16 v[44:47], a[200:203], a[72:75], v[44:47]\n" \
"s_add_u32 m0, m0, 0x5000\n" \
"v_cvt_pk_bf16_f32 v34, v68, v69\n" \
"v_cvt_pk_bf16_f32 v37, v70, v71\n" \
"v_mfma_f32_16x16x32_bf16 v[44:47], a[204:207], a[76:79], v[44:47]\n" \
"ds_read_b128 a[0:3], v213 offset:0\n" \
"global_load_lds_dwordx4 v216, s[38:39] offset:-4096\n" \
"v_mfma_f32_16x16x32_bf16 v[80:83], a[72:75], a[208:211], v[80:83]\n" \
"ds_read_b128 a[4:7], v213 offset:2048\n" \
"v_bitop3_b32 v35, v32, v33, v34 bitop3:0x96\n" \
"v_mfma_f32_16x16x32_bf16 v[80:83], a[76:79], a[212:215], v[80:83]\n" \
"v_mov_b32 v73, 0\n" \
"v_xor_b32 v35, s60, v35\n" \
"v_mfma_f32_16x16x32_bf16 v[84:87], a[72:75], a[216:219], v[84:87]\n" \
"ds_read_b64_tr_b16 a[40:41], v215 offset:16384\n" \
"v_cvt_pk_bf16_f32 v127, v30, v31\n" \
"v_mfma_f32_16x16x32_bf16 v[84:87], a[76:79], a[220:223], v[84:87]\n" \
"buffer_store_dwordx4 v[32:35], v207, s[56:59], s46 offen offset:0 sc1\n" \
"ds_read_b64_tr_b16 a[42:43], v215 offset:18432\n" \
"global_load_lds_dwordx4 v217, s[38:39] offset:-3072\n" \
"v_cvt_pk_bf16_f32 v38, v104, v105\n" \
"v_cvt_pk_bf16_f32 v39, v106, v107\n" \
"v_mfma_f32_16x16x32_bf16 v[52:55], a[200:203], a[80:83], v[52:55]\n" \
"v_cvt_pk_bf16_f32 v74, v108, v109\n" \
"v_cvt_pk_bf16_f32 v75, v110, v111\n" \
"v_mfma_f32_16x16x32_bf16 v[52:55], a[204:207], a[84:87], v[52:55]\n" \
"ds_read_b64_tr_b16 a[44:45], v215 offset:24576\n" \
"v_bitop3_b32 v36, v37, v38, v39 bitop3:0x96\n" \
"v_mfma_f32_16x16x32_bf16 v[88:91], a[80:83], a[208:211], v[88:91]\n" \
"ds_read_b64_tr_b16 a[46:47], v215 offset:26624\n" \
"v_bitop3_b32 v72, s60, v74, v75 bitop3:0x96\n" \
"v_mfma_f32_16x16x32_bf16 v[88:91], a[84:87], a[212:215], v[88:91]\n" \
"global_load_lds_dwordx4 v218, s[38:39] offset:-2048\n" \
"v_xor_b32 v36, s60, v36\n" \
"v_mfma_f32_16x16x32_bf16 v[92:95], a[80:83], a[216:219], v[92:95]\n" \
"ds_read_b128 a[8:11], v213 offset:256\n" \
"v_cvt_pk_bf16_f32 v132, v40, v41\n" \
"v_mfma_f32_16x16x32_bf16 v[92:95], a[84:87], a[220:223], v[92:95]\n" \
"buffer_store_dwordx4 v[36:39], v208, s[56:59], s46 offen offset:1024 sc1\n" \
"ds_read_b128 a[12:15], v213 offset:2304\n" \
"v_cvt_pk_bf16_f32 v133, v42, v43\n" \
"v_cvt_pk_bf16_f32 v134, v44, v45\n" \
"v_cvt_pk_bf16_f32 v135, v46, v47\n" \
"v_mfma_f32_16x16x32_bf16 v[60:63], a[200:203], a[88:91], v[60:63]\n" \
"v_cvt_pk_bf16_f32 v136, v48, v49\n" \
"v_cvt_pk_bf16_f32 v137, v50, v51\n" \
"v_mfma_f32_16x16x32_bf16 v[60:63], a[204:207], a[92:95], v[60:63]\n" \
"ds_read_b128 a[16:19], v213 offset:4096\n" \
"global_load_lds_dwordx4 v219, s[38:39] offset:-1024\n" \
"v_mfma_f32_16x16x32_bf16 v[96:99], a[88:91], a[208:211], v[96:99]\n" \
"s_add_u32 s38, s38, 0x20000\n" \
"s_addc_u32 s39, s39, 0\n" \
"ds_read_b128 a[20:23], v213 offset:6144\n" \
"v_mfma_f32_16x16x32_bf16 v[96:99], a[92:95], a[212:215], v[96:99]\n" \
"buffer_store_dwordx4 v[72:75], v223, s[56:59], s46 offen offset:2048 sc1\n" \
"v_cvt_pk_bf16_f32 v138, v52, v53\n" \
"v_cvt_pk_bf16_f32 v139, v54, v55\n" \
"s_sub_u32 s75, s75, 32768\n" \
"s_cselect_b32 s75, 131072, s75\n" \
"v_mfma_f32_16x16x32_bf16 v[100:103], a[88:91], a[216:219], v[100:103]\n" \
"s_add_u32 m0, s75, s95\n" \
"v_cvt_pk_bf16_f32 v140, v56, v57\n" \
"v_cvt_pk_bf16_f32 v141, v58, v59\n" \
"v_mfma_f32_16x16x32_bf16 v[100:103], a[92:95], a[220:223], v[100:103]\n" \
"s_waitcnt lgkmcnt(2)\n" \
"global_load_lds_dwordx4 v222, s[36:37] offset:0 sc1\n" \
"v_cvt_pk_bf16_f32 v142, v60, v61\n" \
"v_cvt_pk_bf16_f32 v143, v62, v63\n" \
"v_mfma_f32_16x16x32_bf16 v[0:3], a[32:35], a[0:3], 0\n" \
"ds_read_b128 a[24:27], v213 offset:4352\n" \
"v_mfma_f32_16x16x32_bf16 v[0:3], a[36:39], a[4:7], v[0:3]\n" \
"ds_read_b128 a[28:31], v213 offset:6400\n" \
"global_load_lds_dwordx4 v222, s[36:37] offset:1024 sc1\n" \
"v_mfma_f32_16x16x32_bf16 v[4:7], a[40:43], a[0:3], 0\n" \
"s_sub_u32 s74, s74, 32768\n" \
"s_cselect_b32 s74, 131072, s74\n" \
"ds_read_b64_tr_b16 a[48:49], v214 offset:16896\n" \
"v_mfma_f32_16x16x32_bf16 v[4:7], a[44:47], a[4:7], v[4:7]\n" \
"ds_read_b64_tr_b16 a[50:51], v214 offset:18944\n" \
"v_cvt_pk_bf16_f32 v64, v64, v65\n" \
"v_cvt_pk_bf16_f32 v65, v66, v67\n" \
"v_cvt_pk_bf16_f32 v66, v68, v69\n" \
"v_mfma_f32_16x16x32_bf16 v[20:23], a[40:43], a[8:11], 0\n" \
"ds_read_b64_tr_b16 a[52:53], v214 offset:25088\n" \
"global_load_lds_dwordx4 v222, s[36:37] offset:2048 sc1\n" \
"v_mfma_f32_16x16x32_bf16 v[20:23], a[44:47], a[12:15], v[20:23]\n" \
"ds_read_b64_tr_b16 a[54:55], v214 offset:27136\n" \
"v_cvt_pk_bf16_f32 v67, v70, v71\n" \
"v_mfma_f32_16x16x32_bf16 v[16:19], a[32:35], a[8:11], 0\n" \
"ds_read_b64_tr_b16 a[56:57], v215 offset:16896\n" \
"v_cvt_pk_bf16_f32 v196, v80, v81\n" \
"v_mfma_f32_16x16x32_bf16 v[16:19], a[36:39], a[12:15], v[16:19]\n" \
"s_waitcnt lgkmcnt(4)\n" \
"ds_read_b64_tr_b16 a[58:59], v215 offset:18944\n" \
"global_load_lds_dwordx4 v222, s[36:37] offset:3072 sc1\n" \
"v_mfma_f32_16x16x32_bf16 v[32:35], a[32:35], a[16:19], 0\n" \
"s_add_u32 s36, s36, 0x1000\n" \
"ds_read_b64_tr_b16 a[60:61], v215 offset:25088\n" \
"v_cvt_pk_bf16_f32 v197, v82, v83\n" \
"v_mfma_f32_16x16x32_bf16 v[32:35], a[36:39], a[20:23], v[32:35]\n" \
"s_add_u32 m0, m0, 0x5000\n" \
"ds_read_b64_tr_b16 a[62:63], v215 offset:27136\n" \
"v_cvt_pk_bf16_f32 v72, v84, v85\n" \
"v_mfma_f32_16x16x32_bf16 v[36:39], a[40:43], a[16:19], 0\n" \
"global_load_lds_dwordx4 v216, s[38:39] offset:-4096\n" \
"v_add_u32 v213, s74, v211\n" \
"v_mfma_f32_16x16x32_bf16 v[36:39], a[44:47], a[20:23], v[36:39]\n" \
"v_add_u32 v214, s74, v212\n" \
"v_xor_b32 v215, 32, v214\n" \
"v_mfma_f32_16x16x32_bf16 v[52:55], a[40:43], a[24:27], 0\n" \
"v_cvt_pk_bf16_f32 v73, v86, v87\n" \
"Lpg10872_%=:\n" \
"s_cbranch_execz Lpg12305_%=\n" \
"v_cvt_pk_bf16_f32 v198, v88, v89\n" \
"v_mfma_f32_16x16x32_bf16 v[52:55], a[44:47], a[28:31], v[52:55]\n" \
"s_waitcnt vmcnt(16) lgkmcnt(0)\n" \
"global_load_lds_dwordx4 v217, s[38:39] offset:-3072\n" \
"v_cvt_pk_bf16_f32 v199, v90, v91\n" \
"v_mfma_f32_16x16x32_bf16 v[48:51], a[32:35], a[24:27], 0\n" \
"v_cvt_pk_bf16_f32 v74, v92, v93\n" \
"v_cvt_pk_bf16_f32 v75, v94, v95\n" \
"v_mfma_f32_16x16x32_bf16 v[48:51], a[36:39], a[28:31], v[48:51]\n" \
"s_barrier\n" \
"v_cvt_pk_bf16_f32 v200, v96, v97\n" \
"v_mfma_f32_16x16x32_bf16 v[8:11], a[48:51], a[0:3], 0\n" \
"ds_read_b64_tr_b16 a[32:33], v214 offset:16384\n" \
"v_cvt_pk_bf16_f32 v201, v98, v99\n" \
"v_mfma_f32_16x16x32_bf16 v[8:11], a[52:55], a[4:7], v[8:11]\n" \
"ds_read_b64_tr_b16 a[34:35], v214 offset:18432\n" \
"global_load_lds_dwordx4 v218, s[38:39] offset:-2048\n" \
"v_mfma_f32_16x16x32_bf16 v[12:15], a[56:59], a[0:3], 0\n" \
"ds_read_b64_tr_b16 a[36:37], v214 offset:24576\n" \
"v_cvt_pk_bf16_f32 v76, v100, v101\n" \
"v_mfma_f32_16x16x32_bf16 v[12:15], a[60:63], a[4:7], v[12:15]\n" \
"ds_read_b128 a[0:3], v213 offset:0\n" \
"global_load_lds_dwordx4 v219, s[38:39] offset:-1024\n" \
"v_mfma_f32_16x16x32_bf16 v[28:31], a[56:59], a[8:11], 0\n" \
"s_add_u32 s38, s38, 0x20000\n" \
"s_addc_u32 s39, s39, 0\n" \
"ds_read_b128 a[4:7], v213 offset:2048\n" \
"v_mfma_f32_16x16x32_bf16 v[28:31], a[60:63], a[12:15], v[28:31]\n" \
"s_sub_u32 s75, s75, 32768\n" \
"s_cselect_b32 s75, 131072, s75\n" \
"ds_read_b64_tr_b16 a[38:39], v214 offset:26624\n" \
"v_mfma_f32_16x16x32_bf16 v[24:27], a[48:51], a[8:11], 0\n" \
"s_add_u32 m0, s75, s95\n" \
"ds_read_b64_tr_b16 a[40:41], v215 offset:16384\n" \
"v_cvt_pk_bf16_f32 v77, v102, v103\n" \
"v_mfma_f32_16x16x32_bf16 v[24:27], a[52:55], a[12:15], v[24:27]\n" \
"ds_read_b128 a[8:11], v213 offset:256\n" \
"global_load_lds_dwordx4 v222, s[36:37] offset:0 sc1\n" \
"v_mfma_f32_16x16x32_bf16 v[40:43], a[48:51], a[16:19], 0\n" \
"ds_read_b128 a[12:15], v213 offset:2304\n" \
"v_cvt_pk_bf16_f32 v202, v104, v105\n" \
"v_mfma_f32_16x16x32_bf16 v[40:43], a[52:55], a[20:23], v[40:43]\n" \
"ds_read_b64_tr_b16 a[42:43], v215 offset:18432\n" \
"v_cvt_pk_bf16_f32 v203, v106, v107\n" \
"v_mfma_f32_16x16x32_bf16 v[44:47], a[56:59], a[16:19], 0\n" \
"v_cvt_pk_bf16_f32 v78, v108, v109\n" \
"v_cvt_pk_bf16_f32 v79, v110, v111\n" \
"v_mfma_f32_16x16x32_bf16 v[176:179], v[64:67], v[124:127], v[176:179]\n" \
"v_mfma_f32_16x16x32_bf16 v[168:171], v[140:143], v[120:123], v[168:171]\n" \
"v_mfma_f32_16x16x32_bf16 v[160:163], v[136:139], v[116:119], v[160:163]\n" \
"ds_read_b64_tr_b16 a[44:45], v215 offset:24576\n" \
"global_load_lds_dwordx4 v222, s[36:37] offset:1024 sc1\n" \
"v_mfma_f32_16x16x32_bf16 v[44:47], a[60:63], a[20:23], v[44:47]\n" \
"ds_read_b128 a[16:19], v213 offset:4096\n" \
"v_mfma_f32_16x16x32_bf16 v[152:155], v[132:135], v[112:115], v[152:155]\n" \
"v_mfma_f32_16x16x32_bf16 v[172:175], v[140:143], v[124:127], v[172:175]\n" \
"v_mfma_f32_16x16x32_bf16 v[164:167], v[136:139], v[120:123], v[164:167]\n" \
"v_mfma_f32_16x16x32_bf16 v[156:159], v[132:135], v[116:119], v[156:159]\n" \
"v_mfma_f32_16x16x32_bf16 v[60:63], a[56:59], a[24:27], 0\n" \
"ds_read_b128 a[20:23], v213 offset:6144\n" \
"global_load_lds_dwordx4 v222, s[36:37] offset:2048 sc1\n" \
"v_mfma_f32_16x16x32_bf16 v[60:63], a[60:63], a[28:31], v[60:63]\n" \
"ds_read_b64_tr_b16 a[46:47], v215 offset:26624\n" \
"v_mfma_f32_16x16x32_bf16 v[56:59], a[48:51], a[24:27], 0\n" \
"v_max3_f32 v183, v172, v173, v174\n" \
"v_max3_f32 v182, v164, v165, v166\n" \
"v_mfma_f32_16x16x32_bf16 v[56:59], a[52:55], a[28:31], v[56:59]\n" \
"s_waitcnt lgkmcnt(3)\n" \
"v_max3_f32 v181, v156, v157, v158\n" \
"v_max3_f32 v183, v183, v175, v176\n" \
"v_mfma_f32_16x16x32_bf16 v[0:3], a[32:35], a[0:3], v[0:3]\n" \
"ds_read_b128 a[24:27], v213 offset:4352\n" \
"v_max3_f32 v182, v182, v167, v168\n" \
"v_mfma_f32_16x16x32_bf16 v[0:3], a[36:39], a[4:7], v[0:3]\n" \
"s_waitcnt lgkmcnt(1)\n" \
"ds_read_b128 a[28:31], v213 offset:6400\n" \
"global_load_lds_dwordx4 v222, s[36:37] offset:3072 sc1\n" \
"v_mfma_f32_16x16x32_bf16 v[4:7], a[40:43], a[0:3], v[4:7]\n" \
"s_add_u32 s36, s36, 0x1000\n" \
"ds_read_b64_tr_b16 a[48:49], v214 offset:16896\n" \
"v_max3_f32 v181, v181, v159, v160\n" \
"v_mfma_f32_16x16x32_bf16 v[4:7], a[44:47], a[4:7], v[4:7]\n" \
"s_add_u32 m0, m0, 0x5000\n" \
"ds_read_b64_tr_b16 a[50:51], v214 offset:18944\n" \
"v_max3_f32 v183, v183, v177, v178\n" \
"v_mfma_f32_16x16x32_bf16 v[20:23], a[40:43], a[8:11], v[20:23]\n" \
"ds_read_b64_tr_b16 a[52:53], v214 offset:25088\n" \
"global_load_lds_dwordx4 v216, s[38:39] offset:-4096\n" \
"v_mfma_f32_16x16x32_bf16 v[20:23], a[44:47], a[12:15], v[20:23]\n" \
"s_sub_u32 s74, s74, 32768\n" \
"s_cselect_b32 s74, 131072, s74\n" \
"ds_read_b64_tr_b16 a[54:55], v214 offset:27136\n" \
"v_mfma_f32_16x16x32_bf16 v[16:19], a[32:35], a[8:11], v[16:19]\n" \
"ds_read_b64_tr_b16 a[56:57], v215 offset:16896\n" \
"v_max3_f32 v182, v182, v169, v170\n" \
"v_mfma_f32_16x16x32_bf16 v[16:19], a[36:39], a[12:15], v[16:19]\n" \
"ds_read_b64_tr_b16 a[58:59], v215 offset:18944\n" \
"global_load_lds_dwordx4 v217, s[38:39] offset:-3072\n" \
"v_mfma_f32_16x16x32_bf16 v[32:35], a[32:35], a[16:19], v[32:35]\n" \
"ds_read_b64_tr_b16 a[60:61], v215 offset:25088\n" \
"v_max3_f32 v181, v181, v161, v162\n" \
"v_mfma_f32_16x16x32_bf16 v[32:35], a[36:39], a[20:23], v[32:35]\n" \
"ds_read_b64_tr_b16 a[62:63], v215 offset:27136\n" \
"v_max_f32 v183, v183, v179\n" \
"v_mfma_f32_16x16x32_bf16 v[36:39], a[40:43], a[16:19], v[36:39]\n" \
"global_load_lds_dwordx4 v218, s[38:39] offset:-2048\n" \
"v_add_u32 v213, s74, v211\n" \
"v_mfma_f32_16x16x32_bf16 v[36:39], a[44:47], a[20:23], v[36:39]\n" \
"s_waitcnt lgkmcnt(3)\n" \
"v_add_u32 v214, s74, v212\n" \
"v_xor_b32 v215, 32, v214\n" \
"v_mfma_f32_16x16x32_bf16 v[52:55], a[40:43], a[24:27], v[52:55]\n" \
"global_load_lds_dwordx4 v219, s[38:39] offset:-1024\n" \
"v_max_f32 v182, v182, v171\n" \
"v_mfma_f32_16x16x32_bf16 v[52:55], a[44:47], a[28:31], v[52:55]\n" \
"s_waitcnt vmcnt(17) lgkmcnt(0)\n" \
"s_add_u32 s38, s38, 0x20000\n" \
"s_addc_u32 s39, s39, 0\n" \
"v_mfma_f32_16x16x32_bf16 v[48:51], a[32:35], a[24:27], v[48:51]\n" \
"s_sub_u32 s75, s75, 32768\n" \
"s_cselect_b32 s75, 131072, s75\n" \
"v_max_f32 v181, v181, v163\n" \
"v_mfma_f32_16x16x32_bf16 v[48:51], a[36:39], a[28:31], v[48:51]\n" \
"s_barrier\n" \
"v_mov_b32 v187, v183\n" \
"v_mfma_f32_16x16x32_bf16 v[8:11], a[48:51], a[0:3], v[8:11]\n" \
"s_add_u32 m0, s75, s95\n" \
"ds_read_b64_tr_b16 a[32:33], v214 offset:16384\n" \
"v_mov_b32 v186, v182\n" \
"v_mfma_f32_16x16x32_bf16 v[8:11], a[52:55], a[4:7], v[8:11]\n" \
"ds_read_b64_tr_b16 a[34:35], v214 offset:18432\n" \
"global_load_lds_dwordx4 v222, s[36:37] offset:0 sc1\n" \
"v_mfma_f32_16x16x32_bf16 v[12:15], a[56:59], a[0:3], v[12:15]\n" \
"ds_read_b64_tr_b16 a[36:37], v214 offset:24576\n" \
"v_mov_b32 v185, v181\n" \
"v_mfma_f32_16x16x32_bf16 v[12:15], a[60:63], a[4:7], v[12:15]\n" \
"ds_read_b128 a[0:3], v213 offset:0\n" \
"v_permlane16_swap_b32 v187, v183\n" \
"v_permlane16_swap_b32 v186, v182\n" \
"v_mfma_f32_16x16x32_bf16 v[28:31], a[56:59], a[8:11], v[28:31]\n" \
"ds_read_b128 a[4:7], v213 offset:2048\n" \
"global_load_lds_dwordx4 v222, s[36:37] offset:1024 sc1\n" \
"v_mfma_f32_16x16x32_bf16 v[28:31], a[60:63], a[12:15], v[28:31]\n" \
"ds_read_b64_tr_b16 a[38:39], v214 offset:26624\n" \
"v_permlane16_swap_b32 v185, v181\n" \
"v_mfma_f32_16x16x32_bf16 v[24:27], a[48:51], a[8:11], v[24:27]\n" \
"ds_read_b64_tr_b16 a[40:41], v215 offset:16384\n" \
"v_max_f32 v183, v183, v187\n" \
"v_mfma_f32_16x16x32_bf16 v[24:27], a[52:55], a[12:15], v[24:27]\n" \
"ds_read_b128 a[8:11], v213 offset:256\n" \
"global_load_lds_dwordx4 v222, s[36:37] offset:2048 sc1\n" \
"v_mfma_f32_16x16x32_bf16 v[40:43], a[48:51], a[16:19], v[40:43]\n" \
"ds_read_b128 a[12:15], v213 offset:2304\n" \
"v_max_f32 v182, v182, v186\n" \
"v_mfma_f32_16x16x32_bf16 v[40:43], a[52:55], a[20:23], v[40:43]\n" \
"ds_read_b64_tr_b16 a[42:43], v215 offset:18432\n" \
"v_max_f32 v181, v181, v185\n" \
"v_mfma_f32_16x16x32_bf16 v[44:47], a[56:59], a[16:19], v[44:47]\n" \
"ds_read_b64_tr_b16 a[44:45], v215 offset:24576\n" \
"global_load_lds_dwordx4 v222, s[36:37] offset:3072 sc1\n" \
"v_mfma_f32_16x16x32_bf16 v[44:47], a[60:63], a[20:23], v[44:47]\n" \
"s_add_u32 s36, s36, 0x1000\n" \
"ds_read_b128 a[16:19], v213 offset:4096\n" \
"s_add_u32 m0, m0, 0x5000\n" \
"v_mfma_f32_16x16x32_bf16 v[60:63], a[56:59], a[24:27], v[60:63]\n" \
"ds_read_b128 a[20:23], v213 offset:6144\n" \
"global_load_lds_dwordx4 v216, s[38:39] offset:-4096\n" \
"v_mfma_f32_16x16x32_bf16 v[60:63], a[60:63], a[28:31], v[60:63]\n" \
"ds_read_b64_tr_b16 a[46:47], v215 offset:26624\n" \
"v_mov_b32 v187, v183\n" \
"v_mfma_f32_16x16x32_bf16 v[56:59], a[48:51], a[24:27], v[56:59]\n" \
"v_mov_b32 v186, v182\n" \
"v_mov_b32 v185, v181\n" \
"v_mfma_f32_16x16x32_bf16 v[56:59], a[52:55], a[28:31], v[56:59]\n" \
"s_waitcnt lgkmcnt(3)\n" \
"v_permlane32_swap_b32 v187, v183\n" \
"v_mfma_f32_16x16x32_bf16 v[0:3], a[32:35], a[0:3], v[0:3]\n" \
"ds_read_b128 a[24:27], v213 offset:4352\n" \
"v_mfma_f32_16x16x32_bf16 v[0:3], a[36:39], a[4:7], v[0:3]\n" \
"s_waitcnt lgkmcnt(1)\n" \
"ds_read_b128 a[28:31], v213 offset:6400\n" \
"global_load_lds_dwordx4 v217, s[38:39] offset:-3072\n" \
"v_mfma_f32_16x16x32_bf16 v[4:7], a[40:43], a[0:3], v[4:7]\n" \
"s_sub_u32 s74, s74, 32768\n" \
"s_cselect_b32 s74, 131072, s74\n" \
"ds_read_b64_tr_b16 a[48:49], v214 offset:16896\n" \
"v_mfma_f32_16x16x32_bf16 v[4:7], a[44:47], a[4:7], v[4:7]\n" \
"ds_read_b64_tr_b16 a[50:51], v214 offset:18944\n" \
"v_permlane32_swap_b32 v186, v182\n" \
"v_mfma_f32_16x16x32_bf16 v[20:23], a[40:43], a[8:11], v[20:23]\n" \
"ds_read_b64_tr_b16 a[52:53], v214 offset:25088\n" \
"global_load_lds_dwordx4 v218, s[38:39] offset:-2048\n" \
"v_mfma_f32_16x16x32_bf16 v[20:23], a[44:47], a[12:15], v[20:23]\n" \
"ds_read_b64_tr_b16 a[54:55], v214 offset:27136\n" \
"v_permlane32_swap_b32 v185, v181\n" \
"v_mfma_f32_16x16x32_bf16 v[16:19], a[32:35], a[8:11], v[16:19]\n" \
"ds_read_b64_tr_b16 a[56:57], v215 offset:16896\n" \
"global_load_lds_dwordx4 v219, s[38:39] offset:-1024\n" \
"v_mfma_f32_16x16x32_bf16 v[16:19], a[36:39], a[12:15], v[16:19]\n" \
"s_add_u32 s38, s38, 0x20000\n" \
"s_addc_u32 s39, s39, 0\n" \
"ds_read_b64_tr_b16 a[58:59], v215 offset:18944\n" \
"v_mfma_f32_16x16x32_bf16 v[32:35], a[32:35], a[16:19], v[32:35]\n" \
"s_sub_u32 s75, s75, 32768\n" \
"s_cselect_b32 s75, 131072, s75\n" \
"ds_read_b64_tr_b16 a[60:61], v215 offset:25088\n" \
"v_mfma_f32_16x16x32_bf16 v[32:35], a[36:39], a[20:23], v[32:35]\n" \
"s_add_u32 m0, s75, s95\n" \
"ds_read_b64_tr_b16 a[62:63], v215 offset:27136\n" \
"v_max_f32 v183, v183, v187\n" \
"v_mfma_f32_16x16x32_bf16 v[36:39], a[40:43], a[16:19], v[36:39]\n" \
"global_load_lds_dwordx4 v222, s[36:37] offset:0 sc1\n" \
"v_add_u32 v213, s74, v211\n" \
"v_mfma_f32_16x16x32_bf16 v[36:39], a[44:47], a[20:23], v[36:39]\n" \
"s_waitcnt lgkmcnt(3)\n" \
"v_add_u32 v214, s74, v212\n" \
"v_xor_b32 v215, 32, v214\n" \
"v_mfma_f32_16x16x32_bf16 v[52:55], a[40:43], a[24:27], v[52:55]\n" \
"v_max_f32 v182, v182, v186\n" \
"v_max_f32 v181, v181, v185\n" \
"v_mfma_f32_16x16x32_bf16 v[52:55], a[44:47], a[28:31], v[52:55]\n" \
"s_waitcnt vmcnt(17) lgkmcnt(0)\n" \
"global_load_lds_dwordx4 v222, s[36:37] offset:1024 sc1\n" \
"v_mul_f32 v183, v183, v144\n" \
"v_mfma_f32_16x16x32_bf16 v[48:51], a[32:35], a[24:27], v[48:51]\n" \
"v_mul_f32 v182, v182, v144\n" \
"v_mul_f32 v181, v181, v144\n" \
"v_mfma_f32_16x16x32_bf16 v[48:51], a[36:39], a[28:31], v[48:51]\n" \
"s_barrier\n" \
"v_fma_f32 v172, v172, v144, -v183\n" \
"v_mfma_f32_16x16x32_bf16 v[8:11], a[48:51], a[0:3], v[8:11]\n" \
"ds_read_b64_tr_b16 a[32:33], v214 offset:16384\n" \
"v_fma_f32 v164, v164, v144, -v182\n" \
"v_mfma_f32_16x16x32_bf16 v[8:11], a[52:55], a[4:7], v[8:11]\n" \
"ds_read_b64_tr_b16 a[34:35], v214 offset:18432\n" \
"global_load_lds_dwordx4 v222, s[36:37] offset:2048 sc1\n" \
"v_mfma_f32_16x16x32_bf16 v[12:15], a[56:59], a[0:3], v[12:15]\n" \
"ds_read_b64_tr_b16 a[36:37], v214 offset:24576\n" \
"v_fma_f32 v156, v156, v144, -v181\n" \
"v_mfma_f32_16x16x32_bf16 v[12:15], a[60:63], a[4:7], v[12:15]\n" \
"ds_read_b128 a[0:3], v213 offset:0\n" \
"v_exp_f32 v172, v172\n" \
"v_fma_f32 v173, v173, v144, -v183\n" \
"v_mfma_f32_16x16x32_bf16 v[28:31], a[56:59], a[8:11], v[28:31]\n" \
"ds_read_b128 a[4:7], v213 offset:2048\n" \
"global_load_lds_dwordx4 v222, s[36:37] offset:3072 sc1\n" \
"v_mfma_f32_16x16x32_bf16 v[28:31], a[60:63], a[12:15], v[28:31]\n" \
"s_add_u32 s36, s36, 0x1000\n" \
"ds_read_b64_tr_b16 a[38:39], v214 offset:26624\n" \
"v_exp_f32 v164, v164\n" \
"v_mfma_f32_16x16x32_bf16 v[24:27], a[48:51], a[8:11], v[24:27]\n" \
"s_add_u32 m0, m0, 0x5000\n" \
"ds_read_b64_tr_b16 a[40:41], v215 offset:16384\n" \
"v_fma_f32 v165, v165, v144, -v182\n" \
"v_mfma_f32_16x16x32_bf16 v[24:27], a[52:55], a[12:15], v[24:27]\n" \
"ds_read_b128 a[8:11], v213 offset:256\n" \
"global_load_lds_dwordx4 v216, s[38:39] offset:-4096\n" \
"v_exp_f32 v156, v156\n" \
"v_mfma_f32_16x16x32_bf16 v[40:43], a[48:51], a[16:19], v[40:43]\n" \
"ds_read_b128 a[12:15], v213 offset:2304\n" \
"v_fma_f32 v157, v157, v144, -v181\n" \
"v_mfma_f32_16x16x32_bf16 v[40:43], a[52:55], a[20:23], v[40:43]\n" \
"ds_read_b64_tr_b16 a[42:43], v215 offset:18432\n" \
"v_exp_f32 v173, v173\n" \
"v_fma_f32 v174, v174, v144, -v183\n" \
"v_mfma_f32_16x16x32_bf16 v[44:47], a[56:59], a[16:19], v[44:47]\n" \
"ds_read_b64_tr_b16 a[44:45], v215 offset:24576\n" \
"global_load_lds_dwordx4 v217, s[38:39] offset:-3072\n" \
"v_mfma_f32_16x16x32_bf16 v[44:47], a[60:63], a[20:23], v[44:47]\n" \
"buffer_load_dwordx4 v[128:131], v207, s[56:59], s47 offen offset:0 sc1\n" \
"ds_read_b128 a[16:19], v213 offset:4096\n" \
"v_mfma_f32_16x16x32_bf16 v[60:63], a[56:59], a[24:27], v[60:63]\n" \
"ds_read_b128 a[20:23], v213 offset:6144\n" \
"global_load_lds_dwordx4 v218, s[38:39] offset:-2048\n" \
"v_mfma_f32_16x16x32_bf16 v[60:63], a[60:63], a[28:31], v[60:63]\n" \
"ds_read_b64_tr_b16 a[46:47], v215 offset:26624\n" \
"v_exp_f32 v165, v165\n" \
"v_mfma_f32_16x16x32_bf16 v[56:59], a[48:51], a[24:27], v[56:59]\n" \
"buffer_load_dwordx4 v[192:195], v208, s[56:59], s47 offen offset:1024 sc1\n" \
"v_mfma_f32_16x16x32_bf16 v[56:59], a[52:55], a[28:31], v[56:59]\n" \
"s_waitcnt lgkmcnt(3)\n" \
"buffer_load_dwordx4 v[68:71], v223, s[56:59], s47 offen offset:2048 sc1\n" \
"v_mfma_f32_16x16x32_bf16 v[0:3], a[32:35], a[0:3], v[0:3]\n" \
"ds_read_b128 a[24:27], v213 offset:4352\n" \
"v_fma_f32 v166, v166, v144, -v182\n" \
"v_mfma_f32_16x16x32_bf16 v[0:3], a[36:39], a[4:7], v[0:3]\n" \
"s_waitcnt lgkmcnt(1)\n" \
"ds_read_b128 a[28:31], v213 offset:6400\n" \
"global_load_lds_dwordx4 v219, s[38:39] offset:-1024\n" \
"v_exp_f32 v157, v157\n" \
"v_mfma_f32_16x16x32_bf16 v[4:7], a[40:43], a[0:3], v[4:7]\n" \
"s_add_u32 s38, s38, 0x20000\n" \
"s_addc_u32 s39, s39, 0\n" \
"ds_read_b64_tr_b16 a[48:49], v214 offset:16896\n" \
"v_mfma_f32_16x16x32_bf16 v[4:7], a[44:47], a[4:7], v[4:7]\n" \
"s_sub_u32 s75, s75, 32768\n" \
"s_cselect_b32 s75, 131072, s75\n" \
"ds_read_b64_tr_b16 a[50:51], v214 offset:18944\n" \
"v_mfma_f32_16x16x32_bf16 v[20:23], a[40:43], a[8:11], v[20:23]\n" \
"s_add_u32 m0, s75, s95\n" \
"ds_read_b64_tr_b16 a[52:53], v214 offset:25088\n" \
"v_fma_f32 v158, v158, v144, -v181\n" \
"v_mfma_f32_16x16x32_bf16 v[20:23], a[44:47], a[12:15], v[20:23]\n" \
"ds_read_b64_tr_b16 a[54:55], v214 offset:27136\n" \
"global_load_lds_dwordx4 v222, s[36:37] offset:0 sc1\n" \
"v_exp_f32 v174, v174\n" \
"v_mfma_f32_16x16x32_bf16 v[16:19], a[32:35], a[8:11], v[16:19]\n" \
"s_sub_u32 s74, s74, 32768\n" \
"s_cselect_b32 s74, 131072, s74\n" \
"ds_read_b64_tr_b16 a[56:57], v215 offset:16896\n" \
"v_mfma_f32_16x16x32_bf16 v[16:19], a[36:39], a[12:15], v[16:19]\n" \
"ds_read_b64_tr_b16 a[58:59], v215 offset:18944\n" \
"global_load_lds_dwordx4 v222, s[36:37] offset:1024 sc1\n" \
"v_mfma_f32_16x16x32_bf16 v[32:35], a[32:35], a[16:19], v[32:35]\n" \
"ds_read_b64_tr_b16 a[60:61], v215 offset:25088\n" \
"v_fma_f32 v175, v175, v144, -v183\n" \
"v_mfma_f32_16x16x32_bf16 v[32:35], a[36:39], a[20:23], v[32:35]\n" \
"ds_read_b64_tr_b16 a[62:63], v215 offset:27136\n" \
"v_exp_f32 v166, v166\n" \
"v_fma_f32 v167, v167, v144, -v182\n" \
"v_mfma_f32_16x16x32_bf16 v[36:39], a[40:43], a[16:19], v[36:39]\n" \
"global_load_lds_dwordx4 v222, s[36:37] offset:2048 sc1\n" \
"v_add_u32 v213, s74, v211\n" \
"v_mfma_f32_16x16x32_bf16 v[36:39], a[44:47], a[20:23], v[36:39]\n" \
"s_waitcnt lgkmcnt(3)\n" \
"v_add_u32 v214, s74, v212\n" \
"v_xor_b32 v215, 32, v214\n" \
"v_mfma_f32_16x16x32_bf16 v[52:55], a[40:43], a[24:27], v[52:55]\n" \
"v_exp_f32 v158, v158\n" \
"v_mfma_f32_16x16x32_bf16 v[52:55], a[44:47], a[28:31], v[52:55]\n" \
"s_waitcnt vmcnt(22) lgkmcnt(0)\n" \
"global_load_lds_dwordx4 v222, s[36:37] offset:3072 sc1\n" \
"v_fma_f32 v159, v159, v144, -v181\n" \
"v_mfma_f32_16x16x32_bf16 v[48:51], a[32:35], a[24:27], v[48:51]\n" \
"s_add_u32 s36, s36, 0x1000\n" \
"v_exp_f32 v175, v175\n" \
"v_mfma_f32_16x16x32_bf16 v[48:51], a[36:39], a[28:31], v[48:51]\n" \
"s_barrier\n" \
"v_fma_f32 v176, v176, v144, -v183\n" \
"v_exp_f32 v167, v167\n" \
"v_mfma_f32_16x16x32_bf16 v[8:11], a[48:51], a[0:3], v[8:11]\n" \
"s_add_u32 m0, m0, 0x5000\n" \
"ds_read_b64_tr_b16 a[32:33], v214 offset:16384\n" \
"v_fma_f32 v168, v168, v144, -v182\n" \
"v_mfma_f32_16x16x32_bf16 v[8:11], a[52:55], a[4:7], v[8:11]\n" \
"ds_read_b64_tr_b16 a[34:35], v214 offset:18432\n" \
"global_load_lds_dwordx4 v216, s[38:39] offset:-4096\n" \
"v_exp_f32 v159, v159\n" \
"v_mfma_f32_16x16x32_bf16 v[12:15], a[56:59], a[0:3], v[12:15]\n" \
"ds_read_b64_tr_b16 a[36:37], v214 offset:24576\n" \
"v_fma_f32 v160, v160, v144, -v181\n" \
"v_mfma_f32_16x16x32_bf16 v[12:15], a[60:63], a[4:7], v[12:15]\n" \
"ds_read_b128 a[0:3], v213 offset:0\n" \
"v_exp_f32 v176, v176\n" \
"v_mfma_f32_16x16x32_bf16 v[28:31], a[56:59], a[8:11], v[28:31]\n" \
"ds_read_b128 a[4:7], v213 offset:2048\n" \
"global_load_lds_dwordx4 v217, s[38:39] offset:-3072\n" \
"v_mfma_f32_16x16x32_bf16 v[28:31], a[60:63], a[12:15], v[28:31]\n" \
"ds_read_b64_tr_b16 a[38:39], v214 offset:26624\n" \
"v_fma_f32 v177, v177, v144, -v183\n" \
"v_mfma_f32_16x16x32_bf16 v[24:27], a[48:51], a[8:11], v[24:27]\n" \
"ds_read_b64_tr_b16 a[40:41], v215 offset:16384\n" \
"v_exp_f32 v168, v168\n" \
"v_mfma_f32_16x16x32_bf16 v[24:27], a[52:55], a[12:15], v[24:27]\n" \
"ds_read_b128 a[8:11], v213 offset:256\n" \
"global_load_lds_dwordx4 v218, s[38:39] offset:-2048\n" \
"v_mfma_f32_16x16x32_bf16 v[40:43], a[48:51], a[16:19], v[40:43]\n" \
"ds_read_b128 a[12:15], v213 offset:2304\n" \
"v_fma_f32 v169, v169, v144, -v182\n" \
"v_mfma_f32_16x16x32_bf16 v[40:43], a[52:55], a[20:23], v[40:43]\n" \
"ds_read_b64_tr_b16 a[42:43], v215 offset:18432\n" \
"v_exp_f32 v160, v160\n" \
"v_mfma_f32_16x16x32_bf16 v[44:47], a[56:59], a[16:19], v[44:47]\n" \
"ds_read_b64_tr_b16 a[44:45], v215 offset:24576\n" \
"global_load_lds_dwordx4 v219, s[38:39] offset:-1024\n" \
"v_mfma_f32_16x16x32_bf16 v[44:47], a[60:63], a[20:23], v[44:47]\n" \
"s_add_u32 s38, s38, 0x20000\n" \
"s_addc_u32 s39, s39, 0\n" \
"ds_read_b128 a[16:19], v213 offset:4096\n" \
"v_mfma_f32_16x16x32_bf16 v[60:63], a[56:59], a[24:27], v[60:63]\n" \
"s_sub_u32 s75, s75, 32768\n" \
"s_cselect_b32 s75, 131072, s75\n" \
"ds_read_b128 a[20:23], v213 offset:6144\n" \
"v_mfma_f32_16x16x32_bf16 v[60:63], a[60:63], a[28:31], v[60:63]\n" \
"ds_read_b64_tr_b16 a[46:47], v215 offset:26624\n" \
"v_fma_f32 v161, v161, v144, -v181\n" \
"v_mfma_f32_16x16x32_bf16 v[56:59], a[48:51], a[24:27], v[56:59]\n" \
"v_exp_f32 v177, v177\n" \
"v_mfma_f32_16x16x32_bf16 v[56:59], a[52:55], a[28:31], v[56:59]\n" \
"s_waitcnt lgkmcnt(3)\n" \
"v_fma_f32 v178, v178, v144, -v183\n" \
"v_exp_f32 v169, v169\n" \
"v_mfma_f32_16x16x32_bf16 v[0:3], a[32:35], a[0:3], v[0:3]\n" \
"s_add_u32 m0, s75, s95\n" \
"ds_read_b128 a[24:27], v213 offset:4352\n" \
"v_fma_f32 v170, v170, v144, -v182\n" \
"v_mfma_f32_16x16x32_bf16 v[0:3], a[36:39], a[4:7], v[0:3]\n" \
"s_waitcnt lgkmcnt(1)\n" \
"ds_read_b128 a[28:31], v213 offset:6400\n" \
"global_load_lds_dwordx4 v222, s[36:37] offset:0 sc1\n" \
"v_mfma_f32_16x16x32_bf16 v[4:7], a[40:43], a[0:3], v[4:7]\n" \
"s_sub_u32 s74, s74, 32768\n" \
"s_cselect_b32 s74, 131072, s74\n" \
"ds_read_b64_tr_b16 a[48:49], v214 offset:16896\n" \
"v_mfma_f32_16x16x32_bf16 v[4:7], a[44:47], a[4:7], v[4:7]\n" \
"ds_read_b64_tr_b16 a[50:51], v214 offset:18944\n" \
"v_exp_f32 v161, v161\n" \
"v_mfma_f32_16x16x32_bf16 v[20:23], a[40:43], a[8:11], v[20:23]\n" \
"ds_read_b64_tr_b16 a[52:53], v214 offset:25088\n" \
"v_fma_f32 v162, v162, v144, -v181\n" \
"v_mfma_f32_16x16x32_bf16 v[20:23], a[44:47], a[12:15], v[20:23]\n" \
"ds_read_b64_tr_b16 a[54:55], v214 offset:27136\n" \
"global_load_lds_dwordx4 v222, s[36:37] offset:1024 sc1\n" \
"v_exp_f32 v178, v178\n" \
"v_mfma_f32_16x16x32_bf16 v[16:19], a[32:35], a[8:11], v[16:19]\n" \
"ds_read_b64_tr_b16 a[56:57], v215 offset:16896\n" \
"v_fma_f32 v179, v179, v144, -v183\n" \
"v_mfma_f32_16x16x32_bf16 v[16:19], a[36:39], a[12:15], v[16:19]\n" \
"ds_read_b64_tr_b16 a[58:59], v215 offset:18944\n" \
"v_exp_f32 v170, v170\n" \
"v_fma_f32 v171, v171, v144, -v182\n" \
"v_mfma_f32_16x16x32_bf16 v[32:35], a[32:35], a[16:19], v[32:35]\n" \
"s_waitcnt vmcnt(11)\n" \
"ds_read_b64_tr_b16 a[60:61], v215 offset:25088\n" \
"v_bitop3_b32 v131, v131, v128, v129 bitop3:0x96\n" \
"v_mfma_f32_16x16x32_bf16 v[32:35], a[36:39], a[20:23], v[32:35]\n" \
"ds_read_b64_tr_b16 a[62:63], v215 offset:27136\n" \
"global_load_lds_dwordx4 v222, s[36:37] offset:2048 sc1\n" \
"v_mfma_f32_16x16x32_bf16 v[36:39], a[40:43], a[16:19], v[36:39]\n" \
"v_add_u32 v213, s74, v211\n" \
"v_add_u32 v214, s74, v212\n" \
"v_mfma_f32_16x16x32_bf16 v[36:39], a[44:47], a[20:23], v[36:39]\n" \
"s_waitcnt lgkmcnt(3)\n" \
"v_xor_b32 v215, 32, v214\n" \
"v_bitop3_b32 v192, v192, v193, v194 bitop3:0x96\n" \
"v_exp_f32 v162, v162\n" \
"v_fma_f32 v163, v163, v144, -v181\n" \
"v_mfma_f32_16x16x32_bf16 v[52:55], a[40:43], a[24:27], v[52:55]\n" \
"v_bitop3_b32 v68, v68, v69, v70 bitop3:0x96\n" \
"v_xor_b32 v131, v131, v130\n" \
"v_mfma_f32_16x16x32_bf16 v[52:55], a[44:47], a[28:31], v[52:55]\n" \
"s_waitcnt vmcnt(22) lgkmcnt(0)\n" \
"global_load_lds_dwordx4 v222, s[36:37] offset:3072 sc1\n" \
"v_xor_b32 v192, v192, v195\n" \
"v_exp_f32 v179, v179\n" \
"v_mfma_f32_16x16x32_bf16 v[48:51], a[32:35], a[24:27], v[48:51]\n" \
"s_add_u32 s36, s36, 0x1000\n" \
"v_xor_b32 v68, v68, v71\n" \
"v_cmp_ne_u32 s[42:43], s60, v131\n" \
"v_mfma_f32_16x16x32_bf16 v[48:51], a[36:39], a[28:31], v[48:51]\n" \
"s_barrier\n" \
"v_exp_f32 v171, v171\n" \
"v_mfma_f32_16x16x32_bf16 v[8:11], a[48:51], a[0:3], v[8:11]\n" \
"s_add_u32 m0, m0, 0x5000\n" \
"ds_read_b64_tr_b16 a[32:33], v214 offset:16384\n" \
"v_cmp_ne_u32 s[44:45], s60, v192\n" \
"v_mfma_f32_16x16x32_bf16 v[8:11], a[52:55], a[4:7], v[8:11]\n" \
"ds_read_b64_tr_b16 a[34:35], v214 offset:18432\n" \
"global_load_lds_dwordx4 v216, s[38:39] offset:-4096\n" \
"v_exp_f32 v163, v163\n" \
"v_mfma_f32_16x16x32_bf16 v[12:15], a[56:59], a[0:3], v[12:15]\n" \
"ds_read_b64_tr_b16 a[36:37], v214 offset:24576\n" \
"v_cmp_ne_u32 vcc, s60, v68\n" \
"v_mfma_f32_16x16x32_bf16 v[12:15], a[60:63], a[4:7], v[12:15]\n" \
"ds_read_b128 a[0:3], v213 offset:0\n" \
"v_cvt_pk_bf16_f32 v172, v172, v173\n" \
"v_mfma_f32_16x16x32_bf16 v[28:31], a[56:59], a[8:11], v[28:31]\n" \
"s_and_b64 s[42:43], s[42:43], s[12:13]\n" \
"ds_read_b128 a[4:7], v213 offset:2048\n" \
"v_cvt_pk_bf16_f32 v173, v174, v175\n" \
"v_mfma_f32_16x16x32_bf16 v[28:31], a[60:63], a[12:15], v[28:31]\n" \
"s_and_b64 s[44:45], s[44:45], s[92:93]\n" \
"ds_read_b64_tr_b16 a[38:39], v214 offset:26624\n" \
"global_load_lds_dwordx4 v217, s[38:39] offset:-3072\n" \
"v_cvt_pk_bf16_f32 v174, v176, v177\n" \
"v_mfma_f32_16x16x32_bf16 v[24:27], a[48:51], a[8:11], v[24:27]\n" \
"s_and_b64 vcc, vcc, s[14:15]\n" \
"s_or_b64 s[42:43], s[42:43], s[44:45]\n" \
"ds_read_b64_tr_b16 a[40:41], v215 offset:16384\n" \
"v_mfma_f32_16x16x32_bf16 v[24:27], a[52:55], a[12:15], v[24:27]\n" \
"s_or_b64 vcc, vcc, s[42:43]\n" \
"s_cbranch_scc1 Lplh_%=\n" \
"Lprh_%=:\n" \
"ds_read_b128 a[8:11], v213 offset:256\n" \
"v_cvt_pk_bf16_f32 v175, v178, v179\n" \
"v_cvt_pk_bf16_f32 v164, v164, v165\n" \
"v_mfma_f32_16x16x32_bf16 v[40:43], a[48:51], a[16:19], v[40:43]\n" \
"ds_read_b128 a[12:15], v213 offset:2304\n" \
"v_cvt_pk_bf16_f32 v165, v166, v167\n" \
"v_mfma_f32_16x16x32_bf16 v[40:43], a[52:55], a[20:23], v[40:43]\n" \
"ds_read_b64_tr_b16 a[42:43], v215 offset:18432\n" \
"global_load_lds_dwordx4 v218, s[38:39] offset:-2048\n" \
"v_mfma_f32_16x16x32_bf16 v[44:47], a[56:59], a[16:19], v[44:47]\n" \
"ds_read_b64_tr_b16 a[44:45], v215 offset:24576\n" \
"v_mov_b32 v131, v193\n" \
"v_mfma_f32_16x16x32_bf16 v[44:47], a[60:63], a[20:23], v[44:47]\n" \
"v_cvt_pk_bf16_f32 v166, v168, v169\n" \
"v_cvt_pk_bf16_f32 v167, v170, v171\n" \
"v_mfma_f32_16x16x32_bf16 v[148:151], v[128:131], v[112:115], v[148:151]\n" \
"ds_read_b128 a[16:19], v213 offset:4096\n" \
"v_cvt_pk_bf16_f32 v156, v156, v157\n" \
"v_mfma_f32_16x16x32_bf16 v[60:63], a[56:59], a[24:27], v[60:63]\n" \
"ds_read_b128 a[20:23], v213 offset:6144\n" \
"global_load_lds_dwordx4 v219, s[38:39] offset:-1024\n" \
"v_mfma_f32_16x16x32_bf16 v[60:63], a[60:63], a[28:31], v[60:63]\n" \
"s_add_u32 s38, s38, 0x20000\n" \
"s_addc_u32 s39, s39, 0\n" \
"ds_read_b64_tr_b16 a[46:47], v215 offset:26624\n" \
"v_mfma_f32_16x16x32_bf16 v[56:59], a[48:51], a[24:27], v[56:59]\n" \
"s_sub_u32 s75, s75, 32768\n" \
"s_cselect_b32 s75, 131072, s75\n" \
"v_cvt_pk_bf16_f32 v157, v158, v159\n" \
"v_mfma_f32_16x16x32_bf16 v[56:59], a[52:55], a[28:31], v[56:59]\n" \
"s_waitcnt lgkmcnt(3)\n" \
"v_max3_f32 v180, v148, v149, v150\n" \
"v_max3_f32 v180, v180, v151, v152\n" \
"v_mfma_f32_16x16x32_bf16 v[0:3], a[32:35], a[0:3], v[0:3]\n" \
"s_add_u32 m0, s75, s95\n" \
"ds_read_b128 a[24:27], v213 offset:4352\n" \
"v_max3_f32 v180, v180, v153, v154\n" \
"v_mfma_f32_16x16x32_bf16 v[0:3], a[36:39], a[4:7], v[0:3]\n" \
"s_waitcnt lgkmcnt(1)\n" \
"ds_read_b128 a[28:31], v213 offset:6400\n" \
"global_load_lds_dwordx4 v222, s[36:37] offset:0 sc1\n" \
"v_mfma_f32_16x16x32_bf16 v[4:7], a[40:43], a[0:3], v[4:7]\n" \
"s_sub_u32 s74, s74, 32768\n" \
"s_cselect_b32 s74, 131072, s74\n" \
"ds_read_b64_tr_b16 a[48:49], v214 offset:16896\n" \
"v_mfma_f32_16x16x32_bf16 v[4:7], a[44:47], a[4:7], v[4:7]\n" \
"ds_read_b64_tr_b16 a[50:51], v214 offset:18944\n" \
"v_max_f32 v180, v180, v155\n" \
"v_mfma_f32_16x16x32_bf16 v[20:23], a[40:43], a[8:11], v[20:23]\n" \
"ds_read_b64_tr_b16 a[52:53], v214 offset:25088\n" \
"v_mov_b32 v184, v180\n" \
"v_mfma_f32_16x16x32_bf16 v[20:23], a[44:47], a[12:15], v[20:23]\n" \
"ds_read_b64_tr_b16 a[54:55], v214 offset:27136\n" \
"global_load_lds_dwordx4 v222, s[36:37] offset:1024 sc1\n" \
"v_mfma_f32_16x16x32_bf16 v[16:19], a[32:35], a[8:11], v[16:19]\n" \
"ds_read_b64_tr_b16 a[56:57], v215 offset:16896\n" \
"v_permlane16_swap_b32 v184, v180\n" \
"v_mfma_f32_16x16x32_bf16 v[16:19], a[36:39], a[12:15], v[16:19]\n" \
"ds_read_b64_tr_b16 a[58:59], v215 offset:18944\n" \
"v_max_f32 v180, v180, v184\n" \
"v_mfma_f32_16x16x32_bf16 v[32:35], a[32:35], a[16:19], v[32:35]\n" \
"ds_read_b64_tr_b16 a[60:61], v215 offset:25088\n" \
"v_mov_b32 v184, v180\n" \
"v_mfma_f32_16x16x32_bf16 v[32:35], a[36:39], a[20:23], v[32:35]\n" \
"ds_read_b64_tr_b16 a[62:63], v215 offset:27136\n" \
"global_load_lds_dwordx4 v222, s[36:37] offset:2048 sc1\n" \
"v_mfma_f32_16x16x32_bf16 v[36:39], a[40:43], a[16:19], v[36:39]\n" \
"v_add_u32 v213, s74, v211\n" \
"v_add_u32 v214, s74, v212\n" \
"v_mfma_f32_16x16x32_bf16 v[36:39], a[44:47], a[20:23], v[36:39]\n" \
"s_waitcnt lgkmcnt(3)\n" \
"v_xor_b32 v215, 32, v214\n" \
"v_cvt_pk_bf16_f32 v158, v160, v161\n" \
"v_mfma_f32_16x16x32_bf16 v[52:55], a[40:43], a[24:27], v[52:55]\n" \
"v_permlane32_swap_b32 v184, v180\n" \
"v_mfma_f32_16x16x32_bf16 v[52:55], a[44:47], a[28:31], v[52:55]\n" \
"s_waitcnt vmcnt(19) lgkmcnt(0)\n" \
"global_load_lds_dwordx4 v222, s[36:37] offset:3072 sc1\n" \
"v_max_f32 v180, v180, v184\n" \
"v_mfma_f32_16x16x32_bf16 v[48:51], a[32:35], a[24:27], v[48:51]\n" \
"s_add_u32 s36, s36, 0x1000\n" \
"v_mul_f32 v180, v180, v144\n" \
"v_fma_f32 v148, v148, v144, -v180\n" \
"v_mfma_f32_16x16x32_bf16 v[48:51], a[36:39], a[28:31], v[48:51]\n" \
"s_barrier\n" \
"v_cvt_pk_bf16_f32 v159, v162, v163\n" \
"v_mfma_f32_16x16x32_bf16 v[8:11], a[48:51], a[0:3], v[8:11]\n" \
"s_add_u32 m0, m0, 0x5000\n" \
"ds_read_b64_tr_b16 a[32:33], v214 offset:16384\n" \
"v_fma_f32 v149, v149, v144, -v180\n" \
"v_mfma_f32_16x16x32_bf16 v[8:11], a[52:55], a[4:7], v[8:11]\n" \
"Lpg11447_%=:\n" \
"s_cbranch_execz Lpg12577_%=\n" \
"ds_read_b64_tr_b16 a[34:35], v214 offset:18432\n" \
"global_load_lds_dwordx4 v216, s[38:39] offset:-4096\n" \
"v_mfma_f32_16x16x32_bf16 v[176:179], v[188:191], v[172:175], 0\n" \
"v_mfma_f32_16x16x32_bf16 v[104:107], v[200:203], v[172:175], 0\n" \
"v_mfma_f32_16x16x32_bf16 v[108:111], v[76:79], v[172:175], 0\n" \
"v_mfma_f32_16x16x32_bf16 v[168:171], v[188:191], v[164:167], 0\n" \
"v_mfma_f32_16x16x32_bf16 v[96:99], v[198:201], v[164:167], 0\n" \
"v_mfma_f32_16x16x32_bf16 v[100:103], v[74:77], v[164:167], 0\n" \
"v_mfma_f32_16x16x32_bf16 v[12:15], a[56:59], a[0:3], v[12:15]\n" \
"ds_read_b64_tr_b16 a[36:37], v214 offset:24576\n" \
"v_fma_f32 v150, v150, v144, -v180\n" \
"v_mfma_f32_16x16x32_bf16 v[12:15], a[60:63], a[4:7], v[12:15]\n" \
"v_exp_f32 v148, v148\n" \
"v_mfma_f32_16x16x32_bf16 v[160:163], v[188:191], v[156:159], 0\n" \
"ds_read_b128 a[0:3], v213 offset:0\n" \
"v_mfma_f32_16x16x32_bf16 v[88:91], v[196:199], v[156:159], 0\n" \
"v_mfma_f32_16x16x32_bf16 v[92:95], v[72:75], v[156:159], 0\n" \
"v_mfma_f32_16x16x32_bf16 v[28:31], a[56:59], a[8:11], v[28:31]\n" \
"ds_read_b128 a[4:7], v213 offset:2048\n" \
"v_fma_f32 v151, v151, v144, -v180\n" \
"v_mfma_f32_16x16x32_bf16 v[28:31], a[60:63], a[12:15], v[28:31]\n" \
"ds_read_b64_tr_b16 a[38:39], v214 offset:26624\n" \
"global_load_lds_dwordx4 v217, s[38:39] offset:-3072\n" \
"v_exp_f32 v149, v149\n" \
"v_rcp_f32 v187, v176\n" \
"v_mfma_f32_16x16x32_bf16 v[24:27], a[48:51], a[8:11], v[24:27]\n" \
"ds_read_b64_tr_b16 a[40:41], v215 offset:16384\n" \
"v_fma_f32 v152, v152, v144, -v180\n" \
"v_mfma_f32_16x16x32_bf16 v[24:27], a[52:55], a[12:15], v[24:27]\n" \
"ds_read_b128 a[8:11], v213 offset:256\n" \
"v_exp_f32 v150, v150\n" \
"v_mfma_f32_16x16x32_bf16 v[40:43], a[48:51], a[16:19], v[40:43]\n" \
"ds_read_b128 a[12:15], v213 offset:2304\n" \
"v_fma_f32 v153, v153, v144, -v180\n" \
"v_mfma_f32_16x16x32_bf16 v[40:43], a[52:55], a[20:23], v[40:43]\n" \
"ds_read_b64_tr_b16 a[42:43], v215 offset:18432\n" \
"global_load_lds_dwordx4 v218, s[38:39] offset:-2048\n" \
"v_exp_f32 v151, v151\n" \
"v_mfma_f32_16x16x32_bf16 v[44:47], a[56:59], a[16:19], v[44:47]\n" \
"ds_read_b64_tr_b16 a[44:45], v215 offset:24576\n" \
"v_fma_f32 v154, v154, v144, -v180\n" \
"v_mfma_f32_16x16x32_bf16 v[44:47], a[60:63], a[20:23], v[44:47]\n" \
"ds_read_b128 a[16:19], v213 offset:4096\n" \
"v_exp_f32 v152, v152\n" \
"v_mfma_f32_16x16x32_bf16 v[60:63], a[56:59], a[24:27], v[60:63]\n" \
"ds_read_b128 a[20:23], v213 offset:6144\n" \
"global_load_lds_dwordx4 v219, s[38:39] offset:-1024\n" \
"v_mfma_f32_16x16x32_bf16 v[60:63], a[60:63], a[28:31], v[60:63]\n" \
"s_add_u32 s38, s38, 0x20000\n" \
"s_addc_u32 s39, s39, 0\n" \
"ds_read_b64_tr_b16 a[46:47], v215 offset:26624\n" \
"v_mfma_f32_16x16x32_bf16 v[56:59], a[48:51], a[24:27], v[56:59]\n" \
"s_sub_u32 s75, s75, 32768\n" \
"s_cselect_b32 s75, 131072, s75\n" \
"v_fma_f32 v155, v155, v144, -v180\n" \
"v_mfma_f32_16x16x32_bf16 v[56:59], a[52:55], a[28:31], v[56:59]\n" \
"s_waitcnt lgkmcnt(3)\n" \
"v_exp_f32 v153, v153\n" \
"v_mfma_f32_16x16x32_bf16 v[0:3], a[32:35], a[0:3], v[0:3]\n" \
"s_add_u32 m0, s75, s95\n" \
"ds_read_b128 a[24:27], v213 offset:4352\n" \
"v_exp_f32 v154, v154\n" \
"v_mfma_f32_16x16x32_bf16 v[0:3], a[36:39], a[4:7], v[0:3]\n" \
"s_waitcnt lgkmcnt(1)\n" \
"ds_read_b128 a[28:31], v213 offset:6400\n" \
"global_load_lds_dwordx4 v222, s[36:37] offset:0 sc1\n" \
"v_mfma_f32_16x16x32_bf16 v[4:7], a[40:43], a[0:3], v[4:7]\n" \
"s_sub_u32 s74, s74, 32768\n" \
"s_cselect_b32 s74, 131072, s74\n" \
"ds_read_b64_tr_b16 a[48:49], v214 offset:16896\n" \
"v_mfma_f32_16x16x32_bf16 v[4:7], a[44:47], a[4:7], v[4:7]\n" \
"ds_read_b64_tr_b16 a[50:51], v214 offset:18944\n" \
"v_exp_f32 v155, v155\n" \
"v_mfma_f32_16x16x32_bf16 v[20:23], a[40:43], a[8:11], v[20:23]\n" \
"ds_read_b64_tr_b16 a[52:53], v214 offset:25088\n" \
"v_cvt_pk_bf16_f32 v148, v148, v149\n" \
"v_mfma_f32_16x16x32_bf16 v[20:23], a[44:47], a[12:15], v[20:23]\n" \
"ds_read_b64_tr_b16 a[54:55], v214 offset:27136\n" \
"global_load_lds_dwordx4 v222, s[36:37] offset:1024 sc1\n" \
"v_mfma_f32_16x16x32_bf16 v[16:19], a[32:35], a[8:11], v[16:19]\n" \
"ds_read_b64_tr_b16 a[56:57], v215 offset:16896\n" \
"v_cvt_pk_bf16_f32 v149, v150, v151\n" \
"v_mfma_f32_16x16x32_bf16 v[16:19], a[36:39], a[12:15], v[16:19]\n" \
"ds_read_b64_tr_b16 a[58:59], v215 offset:18944\n" \
"v_cvt_pk_bf16_f32 v150, v152, v153\n" \
"v_mfma_f32_16x16x32_bf16 v[32:35], a[32:35], a[16:19], v[32:35]\n" \
"ds_read_b64_tr_b16 a[60:61], v215 offset:25088\n" \
"v_cvt_pk_bf16_f32 v151, v154, v155\n" \
"v_mfma_f32_16x16x32_bf16 v[32:35], a[36:39], a[20:23], v[32:35]\n" \
"v_rcp_f32 v186, v168\n" \
"v_mfma_f32_16x16x32_bf16 v[152:155], v[188:191], v[148:151], 0\n" \
"v_rcp_f32 v185, v160\n" \
"v_mfma_f32_16x16x32_bf16 v[80:83], v[194:197], v[148:151], 0\n" \
"v_mfma_f32_16x16x32_bf16 v[84:87], v[70:73], v[148:151], 0\n" \
"ds_read_b64_tr_b16 a[62:63], v215 offset:27136\n" \
"global_load_lds_dwordx4 v222, s[36:37] offset:2048 sc1\n" \
"v_mfma_f32_16x16x32_bf16 v[36:39], a[40:43], a[16:19], v[36:39]\n" \
"v_add_u32 v213, s74, v211\n" \
"v_add_u32 v214, s74, v212\n" \
"v_mul_f32 v104, v104, v187\n" \
"v_mul_f32 v105, v105, v187\n" \
"v_mul_f32 v106, v106, v187\n" \
"v_mul_f32 v107, v107, v187\n" \
"v_mul_f32 v108, v108, v187\n" \
"v_mul_f32 v109, v109, v187\n" \
"v_mfma_f32_16x16x32_bf16 v[36:39], a[44:47], a[20:23], v[36:39]\n" \
"s_waitcnt lgkmcnt(3)\n" \
"v_xor_b32 v215, 32, v214\n" \
"v_mul_f32 v110, v110, v187\n" \
"v_mul_f32 v111, v111, v187\n" \
"v_cvt_pk_bf16_f32 v104, v104, v105\n" \
"v_cvt_pk_bf16_f32 v105, v106, v107\n" \
"v_cvt_pk_bf16_f32 v106, v108, v109\n" \
"v_cvt_pk_bf16_f32 v107, v110, v111\n" \
"v_mfma_f32_16x16x32_bf16 v[52:55], a[40:43], a[24:27], v[52:55]\n" \
"s_add_u32 s71, s87, 208896\n" \
"v_rcp_f32 v184, v152\n" \
"v_mfma_f32_16x16x32_bf16 v[52:55], a[44:47], a[28:31], v[52:55]\n" \
"s_waitcnt vmcnt(19) lgkmcnt(0)\n" \
"buffer_store_dwordx4 v[104:107], v146, s[56:59], s71 offen sc1\n" \
"global_load_lds_dwordx4 v222, s[36:37] offset:3072 sc1\n" \
"v_mul_f32 v80, v80, v184\n" \
"v_mul_f32 v96, v96, v186\n" \
"v_mul_f32 v97, v97, v186\n" \
"v_mul_f32 v98, v98, v186\n" \
"v_mul_f32 v99, v99, v186\n" \
"v_mul_f32 v100, v100, v186\n" \
"v_mfma_f32_16x16x32_bf16 v[48:51], a[32:35], a[24:27], v[48:51]\n" \
"s_add_u32 s36, s36, 0x1000\n" \
"v_mul_f32 v81, v81, v184\n" \
"v_mul_f32 v82, v82, v184\n" \
"v_mfma_f32_16x16x32_bf16 v[48:51], a[36:39], a[28:31], v[48:51]\n" \
"s_barrier\n" \
"v_mul_f32 v101, v101, v186\n" \
"v_mul_f32 v102, v102, v186\n" \
"v_mul_f32 v103, v103, v186\n" \
"v_cvt_pk_bf16_f32 v96, v96, v97\n" \
"v_cvt_pk_bf16_f32 v97, v98, v99\n" \
"v_cvt_pk_bf16_f32 v98, v100, v101\n" \
"v_mfma_f32_16x16x32_bf16 v[8:11], a[48:51], a[0:3], v[8:11]\n" \
"s_add_u32 m0, m0, 0x5000\n" \
"ds_read_b64_tr_b16 a[32:33], v214 offset:16384\n" \
"v_mul_f32 v83, v83, v184\n" \
"v_mfma_f32_16x16x32_bf16 v[8:11], a[52:55], a[4:7], v[8:11]\n" \
"ds_read_b64_tr_b16 a[34:35], v214 offset:18432\n" \
"global_load_lds_dwordx4 v216, s[38:39] offset:-4096\n" \
"v_cvt_pk_bf16_f32 v99, v102, v103\n" \
"v_mfma_f32_16x16x32_bf16 v[12:15], a[56:59], a[0:3], v[12:15]\n" \
"s_add_u32 s71, s87, 139264\n" \
"ds_read_b64_tr_b16 a[36:37], v214 offset:24576\n" \
"v_mul_f32 v84, v84, v184\n" \
"v_mfma_f32_16x16x32_bf16 v[12:15], a[60:63], a[4:7], v[12:15]\n" \
"buffer_store_dwordx4 v[96:99], v146, s[56:59], s71 offen sc1\n" \
"ds_read_b128 a[0:3], v213 offset:0\n" \
"v_mul_f32 v85, v85, v184\n" \
"v_mul_f32 v88, v88, v185\n" \
"v_mul_f32 v89, v89, v185\n" \
"v_mul_f32 v90, v90, v185\n" \
"v_mul_f32 v91, v91, v185\n" \
"v_mul_f32 v92, v92, v185\n" \
"v_mfma_f32_16x16x32_bf16 v[28:31], a[56:59], a[8:11], v[28:31]\n" \
"ds_read_b128 a[4:7], v213 offset:2048\n" \
"v_mul_f32 v86, v86, v184\n" \
"v_mfma_f32_16x16x32_bf16 v[28:31], a[60:63], a[12:15], v[28:31]\n" \
"ds_read_b64_tr_b16 a[38:39], v214 offset:26624\n" \
"global_load_lds_dwordx4 v217, s[38:39] offset:-3072\n" \
"v_mul_f32 v93, v93, v185\n" \
"v_mul_f32 v94, v94, v185\n" \
"v_mul_f32 v95, v95, v185\n" \
"v_cvt_pk_bf16_f32 v88, v88, v89\n" \
"v_cvt_pk_bf16_f32 v89, v90, v91\n" \
"v_cvt_pk_bf16_f32 v90, v92, v93\n" \
"v_mfma_f32_16x16x32_bf16 v[24:27], a[48:51], a[8:11], v[24:27]\n" \
"ds_read_b64_tr_b16 a[40:41], v215 offset:16384\n" \
"v_mul_f32 v87, v87, v184\n" \
"v_mfma_f32_16x16x32_bf16 v[24:27], a[52:55], a[12:15], v[24:27]\n" \
"ds_read_b128 a[8:11], v213 offset:256\n" \
"v_cvt_pk_bf16_f32 v80, v80, v81\n" \
"v_cvt_pk_bf16_f32 v91, v94, v95\n" \
"v_mfma_f32_16x16x32_bf16 v[40:43], a[48:51], a[16:19], v[40:43]\n" \
"s_add_u32 s71, s87, 69632\n" \
"ds_read_b128 a[12:15], v213 offset:2304\n" \
"v_cvt_pk_bf16_f32 v81, v82, v83\n" \
"v_mfma_f32_16x16x32_bf16 v[40:43], a[52:55], a[20:23], v[40:43]\n" \
"buffer_store_dwordx4 v[88:91], v146, s[56:59], s71 offen sc1\n" \
"ds_read_b64_tr_b16 a[42:43], v215 offset:18432\n" \
"global_load_lds_dwordx4 v218, s[38:39] offset:-2048\n" \
"v_mfma_f32_16x16x32_bf16 v[44:47], a[56:59], a[16:19], v[44:47]\n" \
"ds_read_b64_tr_b16 a[44:45], v215 offset:24576\n" \
"v_cvt_pk_bf16_f32 v82, v84, v85\n" \
"v_mfma_f32_16x16x32_bf16 v[44:47], a[60:63], a[20:23], v[44:47]\n" \
"ds_read_b128 a[16:19], v213 offset:4096\n" \
"v_cvt_pk_bf16_f32 v83, v86, v87\n" \
"v_mfma_f32_16x16x32_bf16 v[60:63], a[56:59], a[24:27], v[60:63]\n" \
"ds_read_b128 a[20:23], v213 offset:6144\n" \
"global_load_lds_dwordx4 v219, s[38:39] offset:-1024\n" \
"v_mfma_f32_16x16x32_bf16 v[60:63], a[60:63], a[28:31], v[60:63]\n" \
"s_add_u32 s38, s38, 0x20000\n" \
"s_addc_u32 s39, s39, 0\n" \
"ds_read_b64_tr_b16 a[46:47], v215 offset:26624\n" \
"v_mfma_f32_16x16x32_bf16 v[56:59], a[48:51], a[24:27], v[56:59]\n" \
"s_add_u32 s70, s87, 0\n" \
"s_sub_u32 s75, s75, 32768\n" \
"s_cselect_b32 s75, 131072, s75\n" \
"v_mfma_f32_16x16x32_bf16 v[56:59], a[52:55], a[28:31], v[56:59]\n" \
"s_waitcnt lgkmcnt(3)\n" \
"buffer_store_dwordx4 v[80:83], v146, s[56:59], s70 offen sc1\n" \
"v_mfma_f32_16x16x32_bf16 v[0:3], a[32:35], a[0:3], v[0:3]\n" \
"s_add_u32 m0, s75, s95\n" \
"ds_read_b128 a[24:27], v213 offset:4352\n" \
"v_mov_b32 v224, s60\n" \
"v_mfma_f32_16x16x32_bf16 v[0:3], a[36:39], a[4:7], v[0:3]\n" \
"s_waitcnt lgkmcnt(1)\n" \
"ds_read_b128 a[28:31], v213 offset:6400\n" \
"global_load_lds_dwordx4 v222, s[36:37] offset:0 sc1\n" \
"v_mfma_f32_16x16x32_bf16 v[4:7], a[40:43], a[0:3], v[4:7]\n" \
"s_sub_u32 s74, s74, 32768\n" \
"s_cselect_b32 s74, 131072, s74\n" \
"ds_read_b64_tr_b16 a[48:49], v214 offset:16896\n" \
"v_mfma_f32_16x16x32_bf16 v[4:7], a[44:47], a[4:7], v[4:7]\n" \
"ds_read_b64_tr_b16 a[50:51], v214 offset:18944\n" \
"v_lshlrev_b32 v225, 28, s61\n" \
"v_mfma_f32_16x16x32_bf16 v[20:23], a[40:43], a[8:11], v[20:23]\n" \
"ds_read_b64_tr_b16 a[52:53], v214 offset:25088\n" \
"v_mfma_f32_16x16x32_bf16 v[20:23], a[44:47], a[12:15], v[20:23]\n" \
"ds_read_b64_tr_b16 a[54:55], v214 offset:27136\n" \
"global_load_lds_dwordx4 v222, s[36:37] offset:1024 sc1\n" \
"v_mfma_f32_16x16x32_bf16 v[16:19], a[32:35], a[8:11], v[16:19]\n" \
"ds_read_b64_tr_b16 a[56:57], v215 offset:16896\n" \
"v_mfma_f32_16x16x32_bf16 v[16:19], a[36:39], a[12:15], v[16:19]\n" \
"ds_read_b64_tr_b16 a[58:59], v215 offset:18944\n" \
"v_mfma_f32_16x16x32_bf16 v[32:35], a[32:35], a[16:19], v[32:35]\n" \
"ds_read_b64_tr_b16 a[60:61], v215 offset:25088\n" \
"v_mfma_f32_16x16x32_bf16 v[32:35], a[36:39], a[20:23], v[32:35]\n" \
"ds_read_b64_tr_b16 a[62:63], v215 offset:27136\n" \
"global_load_lds_dwordx4 v222, s[36:37] offset:2048 sc1\n" \
"v_mfma_f32_16x16x32_bf16 v[36:39], a[40:43], a[16:19], v[36:39]\n" \
"v_add_u32 v213, s74, v211\n" \
"v_add_u32 v214, s74, v212\n" \
"v_mfma_f32_16x16x32_bf16 v[36:39], a[44:47], a[20:23], v[36:39]\n" \
"s_waitcnt lgkmcnt(3)\n" \
"v_xor_b32 v215, 32, v214\n" \
"v_mfma_f32_16x16x32_bf16 v[52:55], a[40:43], a[24:27], v[52:55]\n" \
"v_mfma_f32_16x16x32_bf16 v[52:55], a[44:47], a[28:31], v[52:55]\n" \
"s_waitcnt vmcnt(23) lgkmcnt(0)\n" \
"global_load_lds_dwordx4 v222, s[36:37] offset:3072 sc1\n" \
"v_mfma_f32_16x16x32_bf16 v[48:51], a[32:35], a[24:27], v[48:51]\n" \
"s_add_u32 s36, s36, 0x1000\n" \
"v_mfma_f32_16x16x32_bf16 v[48:51], a[36:39], a[28:31], v[48:51]\n" \
"s_barrier\n" \
"v_mfma_f32_16x16x32_bf16 v[8:11], a[48:51], a[0:3], v[8:11]\n" \
"s_add_u32 m0, m0, 0x5000\n" \
"ds_read_b64_tr_b16 a[32:33], v214 offset:16384\n" \
"v_mfma_f32_16x16x32_bf16 v[8:11], a[52:55], a[4:7], v[8:11]\n" \
"ds_read_b64_tr_b16 a[34:35], v214 offset:18432\n" \
"global_load_lds_dwordx4 v216, s[38:39] offset:-4096\n" \
"v_mfma_f32_16x16x32_bf16 v[12:15], a[56:59], a[0:3], v[12:15]\n" \
"ds_read_b64_tr_b16 a[36:37], v214 offset:24576\n" \
"v_mfma_f32_16x16x32_bf16 v[12:15], a[60:63], a[4:7], v[12:15]\n" \
"ds_read_b128 a[0:3], v213 offset:0\n" \
"v_mfma_f32_16x16x32_bf16 v[28:31], a[56:59], a[8:11], v[28:31]\n" \
"ds_read_b128 a[4:7], v213 offset:2048\n" \
"v_mfma_f32_16x16x32_bf16 v[28:31], a[60:63], a[12:15], v[28:31]\n" \
"ds_read_b64_tr_b16 a[38:39], v214 offset:26624\n" \
"global_load_lds_dwordx4 v217, s[38:39] offset:-3072\n" \
"v_mfma_f32_16x16x32_bf16 v[24:27], a[48:51], a[8:11], v[24:27]\n" \
"ds_read_b64_tr_b16 a[40:41], v215 offset:16384\n" \
"v_mfma_f32_16x16x32_bf16 v[24:27], a[52:55], a[12:15], v[24:27]\n" \
"ds_read_b128 a[8:11], v213 offset:256\n" \
"v_mfma_f32_16x16x32_bf16 v[40:43], a[48:51], a[16:19], v[40:43]\n" \
"ds_read_b128 a[12:15], v213 offset:2304\n" \
"v_mfma_f32_16x16x32_bf16 v[40:43], a[52:55], a[20:23], v[40:43]\n" \
"ds_read_b64_tr_b16 a[42:43], v215 offset:18432\n" \
"global_load_lds_dwordx4 v218, s[38:39] offset:-2048\n" \
"v_mfma_f32_16x16x32_bf16 v[44:47], a[56:59], a[16:19], v[44:47]\n" \
"ds_read_b64_tr_b16 a[44:45], v215 offset:24576\n" \
"v_mfma_f32_16x16x32_bf16 v[44:47], a[60:63], a[20:23], v[44:47]\n" \
"ds_read_b128 a[16:19], v213 offset:4096\n" \
"v_mfma_f32_16x16x32_bf16 v[60:63], a[56:59], a[24:27], v[60:63]\n" \
"ds_read_b128 a[20:23], v213 offset:6144\n" \
"global_load_lds_dwordx4 v219, s[38:39] offset:-1024\n" \
"v_mfma_f32_16x16x32_bf16 v[60:63], a[60:63], a[28:31], v[60:63]\n" \
"s_add_u32 s38, s38, 0x20000\n" \
"s_addc_u32 s39, s39, 0\n" \
"ds_read_b64_tr_b16 a[46:47], v215 offset:26624\n" \
"v_mfma_f32_16x16x32_bf16 v[56:59], a[48:51], a[24:27], v[56:59]\n" \
"s_sub_u32 s75, s75, 32768\n" \
"s_cselect_b32 s75, 131072, s75\n" \
"v_mfma_f32_16x16x32_bf16 v[56:59], a[52:55], a[28:31], v[56:59]\n" \
"s_waitcnt lgkmcnt(3)\n" \
"v_mfma_f32_16x16x32_bf16 v[0:3], a[32:35], a[0:3], v[0:3]\n" \
"ds_read_b128 a[24:27], v213 offset:4352\n" \
"s_add_u32 m0, s75, s95\n" \
"v_mfma_f32_16x16x32_bf16 v[0:3], a[36:39], a[4:7], v[0:3]\n" \
"s_waitcnt lgkmcnt(1)\n" \
"ds_read_b128 a[28:31], v213 offset:6400\n" \
"global_load_lds_dwordx4 v222, s[36:37] offset:0 sc1\n" \
"v_mfma_f32_16x16x32_bf16 v[4:7], a[40:43], a[0:3], v[4:7]\n" \
"s_sub_u32 s74, s74, 32768\n" \
"s_cselect_b32 s74, 131072, s74\n" \
"ds_read_b64_tr_b16 a[48:49], v214 offset:16896\n" \
"v_mfma_f32_16x16x32_bf16 v[4:7], a[44:47], a[4:7], v[4:7]\n" \
"ds_read_b64_tr_b16 a[50:51], v214 offset:18944\n" \
"v_mfma_f32_16x16x32_bf16 v[20:23], a[40:43], a[8:11], v[20:23]\n" \
"ds_read_b64_tr_b16 a[52:53], v214 offset:25088\n" \
"v_mfma_f32_16x16x32_bf16 v[20:23], a[44:47], a[12:15], v[20:23]\n" \
"ds_read_b64_tr_b16 a[54:55], v214 offset:27136\n" \
"global_load_lds_dwordx4 v222, s[36:37] offset:1024 sc1\n" \
"v_mfma_f32_16x16x32_bf16 v[16:19], a[32:35], a[8:11], v[16:19]\n" \
"ds_read_b64_tr_b16 a[56:57], v215 offset:16896\n" \
"v_mfma_f32_16x16x32_bf16 v[16:19], a[36:39], a[12:15], v[16:19]\n" \
"ds_read_b64_tr_b16 a[58:59], v215 offset:18944\n" \
"v_mfma_f32_16x16x32_bf16 v[32:35], a[32:35], a[16:19], v[32:35]\n" \
"ds_read_b64_tr_b16 a[60:61], v215 offset:25088\n" \
"v_mfma_f32_16x16x32_bf16 v[32:35], a[36:39], a[20:23], v[32:35]\n" \
"ds_read_b64_tr_b16 a[62:63], v215 offset:27136\n" \
"global_load_lds_dwordx4 v222, s[36:37] offset:2048 sc1\n" \
"v_mfma_f32_16x16x32_bf16 v[36:39], a[40:43], a[16:19], v[36:39]\n" \
"v_add_u32 v213, s74, v211\n" \
"v_add_u32 v214, s74, v212\n" \
"v_mfma_f32_16x16x32_bf16 v[36:39], a[44:47], a[20:23], v[36:39]\n" \
"s_waitcnt lgkmcnt(3)\n" \
"v_xor_b32 v215, 32, v214\n" \
"v_mfma_f32_16x16x32_bf16 v[52:55], a[40:43], a[24:27], v[52:55]\n" \
"v_mfma_f32_16x16x32_bf16 v[52:55], a[44:47], a[28:31], v[52:55]\n" \
"s_waitcnt vmcnt(11) lgkmcnt(0)\n" \
"global_load_lds_dwordx4 v222, s[36:37] offset:3072 sc1\n" \
"v_mfma_f32_16x16x32_bf16 v[48:51], a[32:35], a[24:27], v[48:51]\n" \
"s_add_u32 s36, s36, 0x1000\n" \
"v_mfma_f32_16x16x32_bf16 v[48:51], a[36:39], a[28:31], v[48:51]\n" \
"s_barrier\n" \
"v_mfma_f32_16x16x32_bf16 v[8:11], a[48:51], a[0:3], v[8:11]\n" \
"s_add_u32 m0, m0, 0x5000\n" \
"ds_read_b64_tr_b16 a[32:33], v214 offset:16384\n" \
"v_mfma_f32_16x16x32_bf16 v[8:11], a[52:55], a[4:7], v[8:11]\n" \
"ds_read_b64_tr_b16 a[34:35], v214 offset:18432\n" \
"global_load_lds_dwordx4 v216, s[38:39] offset:-4096\n" \
"v_mfma_f32_16x16x32_bf16 v[12:15], a[56:59], a[0:3], v[12:15]\n" \
"ds_read_b64_tr_b16 a[36:37], v214 offset:24576\n" \
"v_mfma_f32_16x16x32_bf16 v[12:15], a[60:63], a[4:7], v[12:15]\n" \
"buffer_store_dword v224, v225, s[56:59], s30 offen sc1\n" \
"ds_read_b128 a[0:3], v213 offset:0\n" \
"v_mfma_f32_16x16x32_bf16 v[28:31], a[56:59], a[8:11], v[28:31]\n" \
"ds_read_b128 a[4:7], v213 offset:2048\n" \
"v_mfma_f32_16x16x32_bf16 v[28:31], a[60:63], a[12:15], v[28:31]\n" \
"ds_read_b64_tr_b16 a[38:39], v214 offset:26624\n" \
"global_load_lds_dwordx4 v217, s[38:39] offset:-3072\n" \
"v_mfma_f32_16x16x32_bf16 v[24:27], a[48:51], a[8:11], v[24:27]\n" \
"ds_read_b64_tr_b16 a[40:41], v215 offset:16384\n" \
"v_mfma_f32_16x16x32_bf16 v[24:27], a[52:55], a[12:15], v[24:27]\n" \
"ds_read_b128 a[8:11], v213 offset:256\n" \
"v_mfma_f32_16x16x32_bf16 v[40:43], a[48:51], a[16:19], v[40:43]\n" \
"ds_read_b128 a[12:15], v213 offset:2304\n" \
"v_mfma_f32_16x16x32_bf16 v[40:43], a[52:55], a[20:23], v[40:43]\n" \
"ds_read_b64_tr_b16 a[42:43], v215 offset:18432\n" \
"global_load_lds_dwordx4 v218, s[38:39] offset:-2048\n" \
"v_mfma_f32_16x16x32_bf16 v[44:47], a[56:59], a[16:19], v[44:47]\n" \
"ds_read_b64_tr_b16 a[44:45], v215 offset:24576\n" \
"v_mfma_f32_16x16x32_bf16 v[44:47], a[60:63], a[20:23], v[44:47]\n" \
"ds_read_b128 a[16:19], v213 offset:4096\n" \
"v_mfma_f32_16x16x32_bf16 v[60:63], a[56:59], a[24:27], v[60:63]\n" \
"ds_read_b128 a[20:23], v213 offset:6144\n" \
"global_load_lds_dwordx4 v219, s[38:39] offset:-1024\n" \
"v_mfma_f32_16x16x32_bf16 v[60:63], a[60:63], a[28:31], v[60:63]\n" \
"s_add_u32 s38, s38, 0x20000\n" \
"s_addc_u32 s39, s39, 0\n" \
"ds_read_b64_tr_b16 a[46:47], v215 offset:26624\n" \
"v_mfma_f32_16x16x32_bf16 v[56:59], a[48:51], a[24:27], v[56:59]\n" \
"s_sub_u32 s75, s75, 32768\n" \
"s_cselect_b32 s75, 131072, s75\n" \
"v_mfma_f32_16x16x32_bf16 v[56:59], a[52:55], a[28:31], v[56:59]\n" \
"s_waitcnt lgkmcnt(3)\n" \
"s_cmp_eq_u32 s78, 0\n" \
"s_cbranch_scc0 Lrole1_%=\n" \
"s_sub_u32 s72, s64, 15\n" \
"s_call_b64 s[68:69], Lo0_%=\n" \
"buffer_load_dword v220, off, s[56:59], s79 sc1\n" \
"s_mov_b32 s72, 2\n" \
"s_call_b64 s[68:69], Lo0_%=\n" \
"v_mfma_f32_16x16x32_bf16 v[0:3], a[32:35], a[0:3], v[0:3]\n" \
"s_lshl_b32 s71, s61, 4\n" \
"ds_read_b128 a[24:27], v213 offset:4352\n" \
"v_subrev_u32 v72, s71, v251\n" \
"v_mfma_f32_16x16x32_bf16 v[0:3], a[36:39], a[4:7], v[0:3]\n" \
"s_waitcnt lgkmcnt(1)\n" \
"ds_read_b128 a[28:31], v213 offset:6400\n" \
"v_mov_b32 v73, 0x3f80\n" \
"v_mfma_f32_16x16x32_bf16 v[4:7], a[40:43], a[0:3], v[4:7]\n" \
"s_sub_u32 s74, s74, 32768\n" \
"s_cselect_b32 s74, 131072, s74\n" \
"ds_read_b64_tr_b16 a[48:49], v214 offset:16896\n" \
"v_mfma_f32_16x16x32_bf16 v[4:7], a[44:47], a[4:7], v[4:7]\n" \
"ds_read_b64_tr_b16 a[50:51], v214 offset:18944\n" \
"v_mov_b32 v74, 0x3f800000\n" \
"v_mfma_f32_16x16x32_bf16 v[20:23], a[40:43], a[8:11], v[20:23]\n" \
"ds_read_b64_tr_b16 a[52:53], v214 offset:25088\n" \
"v_cmp_eq_u32 vcc, 0, v72\n" \
"v_mfma_f32_16x16x32_bf16 v[20:23], a[44:47], a[12:15], v[20:23]\n" \
"ds_read_b64_tr_b16 a[54:55], v214 offset:27136\n" \
"v_cmp_eq_u32 s[66:67], 1, v72\n" \
"v_mfma_f32_16x16x32_bf16 v[16:19], a[32:35], a[8:11], v[16:19]\n" \
"ds_read_b64_tr_b16 a[56:57], v215 offset:16896\n" \
"v_mov_b32 v66, 0\n" \
"v_mfma_f32_16x16x32_bf16 v[16:19], a[36:39], a[12:15], v[16:19]\n" \
"ds_read_b64_tr_b16 a[58:59], v215 offset:18944\n" \
"v_mov_b32 v68, 0\n" \
"v_mfma_f32_16x16x32_bf16 v[32:35], a[32:35], a[16:19], v[32:35]\n" \
"ds_read_b64_tr_b16 a[60:61], v215 offset:25088\n" \
"v_cndmask_b32 v64, 0, v73, vcc\n" \
"v_mfma_f32_16x16x32_bf16 v[32:35], a[36:39], a[20:23], v[32:35]\n" \
"ds_read_b64_tr_b16 a[62:63], v215 offset:27136\n" \
"v_cndmask_b32 v64, v64, v74, s[66:67]\n" \
"v_mfma_f32_16x16x32_bf16 v[36:39], a[40:43], a[16:19], v[36:39]\n" \
"v_add_u32 v213, s74, v211\n" \
"v_add_u32 v214, s74, v212\n" \
"v_mfma_f32_16x16x32_bf16 v[36:39], a[44:47], a[20:23], v[36:39]\n" \
"s_waitcnt lgkmcnt(3)\n" \
"v_xor_b32 v215, 32, v214\n" \
"v_mov_b32 v70, v64\n" \
"v_mfma_f32_16x16x32_bf16 v[52:55], a[40:43], a[24:27], v[52:55]\n" \
"v_cmp_eq_u32 vcc, 2, v72\n" \
"v_cmp_eq_u32 s[66:67], 3, v72\n" \
"v_mfma_f32_16x16x32_bf16 v[52:55], a[44:47], a[28:31], v[52:55]\n" \
"s_waitcnt vmcnt(17) lgkmcnt(0)\n" \
"v_mov_b32 v67, 0\n" \
"v_mov_b32 v69, 0\n" \
"v_mfma_f32_16x16x32_bf16 v[48:51], a[32:35], a[24:27], v[48:51]\n" \
"v_cndmask_b32 v65, 0, v73, vcc\n" \
"v_cndmask_b32 v65, v65, v74, s[66:67]\n" \
"v_mfma_f32_16x16x32_bf16 v[48:51], a[36:39], a[28:31], v[48:51]\n" \
"s_barrier\n" \
"v_mov_b32 v71, v65\n" \
"v_mfma_f32_16x16x32_bf16 v[8:11], a[48:51], a[0:3], v[8:11]\n" \
"s_lshl_b32 s83, s95, 1\n" \
"s_add_u32 s83, s83, s74\n" \
"ds_read_b64_tr_b16 a[32:33], v214 offset:16384\n" \
"v_mfma_f32_16x16x32_bf16 v[8:11], a[52:55], a[4:7], v[8:11]\n" \
"s_waitcnt vmcnt(16)\n" \
"ds_read_b64_tr_b16 a[34:35], v214 offset:18432\n" \
"v_cmp_ne_u32 vcc, s60, v220\n" \
"v_mfma_f32_16x16x32_bf16 v[12:15], a[56:59], a[0:3], v[12:15]\n" \
"ds_read_b64_tr_b16 a[36:37], v214 offset:24576\n" \
"v_mfma_f32_16x16x32_bf16 v[12:15], a[60:63], a[4:7], v[12:15]\n" \
"s_and_b32 vcc_hi, vcc_lo, 1\n" \
"s_cbranch_scc1 Lpl4_%=\n" \
"Lpr4_%=:\n" \
"ds_read_b128 a[0:3], v213 offset:0\n" \
"v_mfma_f32_16x16x32_bf16 v[28:31], a[56:59], a[8:11], v[28:31]\n" \
"ds_read_b128 a[4:7], v213 offset:2048\n" \
"v_mfma_f32_16x16x32_bf16 v[28:31], a[60:63], a[12:15], v[28:31]\n" \
"s_add_u32 s71, s76, 0\n" \
"ds_read_b64_tr_b16 a[38:39], v214 offset:26624\n" \
"buffer_load_dwordx4 v[176:179], v248, s[56:59], s71 offen offset:0 sc1\n" \
"v_mfma_f32_16x16x32_bf16 v[24:27], a[48:51], a[8:11], v[24:27]\n" \
"ds_read_b64_tr_b16 a[40:41], v215 offset:16384\n" \
"v_mfma_f32_16x16x32_bf16 v[24:27], a[52:55], a[12:15], v[24:27]\n" \
"buffer_load_dwordx4 v[180:183], v248, s[56:59], s71 offen offset:1024 sc1\n" \
"ds_read_b128 a[8:11], v213 offset:256\n" \
"v_mfma_f32_16x16x32_bf16 v[40:43], a[48:51], a[16:19], v[40:43]\n" \
"ds_read_b128 a[12:15], v213 offset:2304\n" \
"v_mfma_f32_16x16x32_bf16 v[40:43], a[52:55], a[20:23], v[40:43]\n" \
"ds_read_b64_tr_b16 a[42:43], v215 offset:18432\n" \
"v_mfma_f32_16x16x32_bf16 v[44:47], a[56:59], a[16:19], v[44:47]\n" \
"ds_read_b64_tr_b16 a[44:45], v215 offset:24576\n" \
"v_mfma_f32_16x16x32_bf16 v[44:47], a[60:63], a[20:23], v[44:47]\n" \
"ds_read_b128 a[16:19], v213 offset:4096\n" \
"v_mfma_f32_16x16x32_bf16 v[60:63], a[56:59], a[24:27], v[60:63]\n" \
"ds_read_b128 a[20:23], v213 offset:6144\n" \
"v_mfma_f32_16x16x32_bf16 v[60:63], a[60:63], a[28:31], v[60:63]\n" \
"ds_read_b64_tr_b16 a[46:47], v215 offset:26624\n" \
"v_mfma_f32_16x16x32_bf16 v[56:59], a[48:51], a[24:27], v[56:59]\n" \
"buffer_load_dwordx4 v[184:187], v248, s[56:59], s71 offen offset:2048 sc1\n" \
"v_mfma_f32_16x16x32_bf16 v[56:59], a[52:55], a[28:31], v[56:59]\n" \
"s_waitcnt lgkmcnt(3)\n" \
"buffer_load_dwordx4 v[188:191], v248, s[56:59], s71 offen offset:3072 sc1\n" \
"v_mfma_f32_16x16x32_bf16 v[0:3], a[32:35], a[0:3], v[0:3]\n" \
"ds_read_b128 a[24:27], v213 offset:4352\n" \
"s_add_u32 s71, s76, 4096\n" \
"v_mfma_f32_16x16x32_bf16 v[0:3], a[36:39], a[4:7], v[0:3]\n" \
"s_waitcnt lgkmcnt(1)\n" \
"buffer_load_dwordx4 v[192:195], v248, s[56:59], s71 offen offset:0 sc1\n" \
"ds_read_b128 a[28:31], v213 offset:6400\n" \
"v_mfma_f32_16x16x32_bf16 v[4:7], a[40:43], a[0:3], v[4:7]\n" \
"s_sub_u32 s74, s74, 32768\n" \
"s_cselect_b32 s74, 131072, s74\n" \
"ds_read_b64_tr_b16 a[48:49], v214 offset:16896\n" \
"v_mfma_f32_16x16x32_bf16 v[4:7], a[44:47], a[4:7], v[4:7]\n" \
"ds_read_b64_tr_b16 a[50:51], v214 offset:18944\n" \
"v_lshrrev_b32 v132, 4, v248\n" \
"v_mfma_f32_16x16x32_bf16 v[20:23], a[40:43], a[8:11], v[20:23]\n" \
"ds_read_b64_tr_b16 a[52:53], v214 offset:25088\n" \
"v_and_b32 v133, 15, v132\n" \
"v_mfma_f32_16x16x32_bf16 v[20:23], a[44:47], a[12:15], v[20:23]\n" \
"ds_read_b64_tr_b16 a[54:55], v214 offset:27136\n" \
"v_mul_u32_u24 v128, 144, v133\n" \
"v_mfma_f32_16x16x32_bf16 v[16:19], a[32:35], a[8:11], v[16:19]\n" \
"ds_read_b64_tr_b16 a[56:57], v215 offset:16896\n" \
"v_lshrrev_b32 v133, 4, v132\n" \
"v_mfma_f32_16x16x32_bf16 v[16:19], a[36:39], a[12:15], v[16:19]\n" \
"ds_read_b64_tr_b16 a[58:59], v215 offset:18944\n" \
"v_lshl_add_u32 v128, v133, 3, v128\n" \
"v_mfma_f32_16x16x32_bf16 v[32:35], a[32:35], a[16:19], v[32:35]\n" \
"ds_read_b64_tr_b16 a[60:61], v215 offset:25088\n" \
"v_add_u32 v128, s83, v128\n" \
"v_mfma_f32_16x16x32_bf16 v[32:35], a[36:39], a[20:23], v[32:35]\n" \
"ds_read_b64_tr_b16 a[62:63], v215 offset:27136\n" \
"v_lshrrev_b32 v133, 3, v132\n" \
"v_mfma_f32_16x16x32_bf16 v[36:39], a[40:43], a[16:19], v[36:39]\n" \
"v_add_u32 v213, s74, v211\n" \
"v_add_u32 v214, s74, v212\n" \
"v_mfma_f32_16x16x32_bf16 v[36:39], a[44:47], a[20:23], v[36:39]\n" \
"s_waitcnt lgkmcnt(3)\n" \
"v_xor_b32 v215, 32, v214\n" \
"v_and_b32 v134, 7, v132\n" \
"v_mfma_f32_16x16x32_bf16 v[52:55], a[40:43], a[24:27], v[52:55]\n" \
"buffer_load_dwordx4 v[196:199], v248, s[56:59], s71 offen offset:1024 sc1\n" \
"v_mfma_f32_16x16x32_bf16 v[52:55], a[44:47], a[28:31], v[52:55]\n" \
"s_waitcnt vmcnt(6) lgkmcnt(0)\n" \
"buffer_load_dwordx4 v[200:203], v248, s[56:59], s71 offen offset:2048 sc1\n" \
"v_mfma_f32_16x16x32_bf16 v[48:51], a[32:35], a[24:27], v[48:51]\n" \
"buffer_load_dwordx4 v[204:207], v248, s[56:59], s71 offen offset:3072 sc1\n" \
"v_mfma_f32_16x16x32_bf16 v[48:51], a[36:39], a[28:31], v[48:51]\n" \
"s_barrier\n" \
"v_mfma_f32_16x16x32_bf16 v[8:11], a[48:51], a[0:3], v[8:11]\n" \
"s_mov_b32 s70, 0x20000000\n" \
"ds_read_b128 a[192:195], v213 offset:0\n" \
"ds_read_b64_tr_b16 a[128:129], v214 offset:16384\n" \
"v_mfma_f32_16x16x32_bf16 v[8:11], a[52:55], a[4:7], v[8:11]\n" \
"s_sub_u32 s74, s74, 32768\n" \
"s_cselect_b32 s74, 131072, s74\n" \
"ds_read_b64_tr_b16 a[130:131], v214 offset:18432\n" \
"v_mfma_f32_16x16x32_bf16 v[12:15], a[56:59], a[0:3], v[12:15]\n" \
"ds_read_b128 a[196:199], v213 offset:2048\n" \
"ds_read_b64_tr_b16 a[132:133], v214 offset:24576\n" \
"v_mfma_f32_16x16x32_bf16 v[12:15], a[60:63], a[4:7], v[12:15]\n" \
"ds_read_b64_tr_b16 a[134:135], v214 offset:26624\n" \
"v_add_u32 v226, s74, v211\n" \
"v_mfma_f32_16x16x32_bf16 v[28:31], a[56:59], a[8:11], v[28:31]\n" \
"ds_read_b128 a[200:203], v213 offset:256\n" \
"ds_read_b64_tr_b16 a[136:137], v215 offset:16384\n" \
"v_mfma_f32_16x16x32_bf16 v[28:31], a[60:63], a[12:15], v[28:31]\n" \
"ds_read_b64_tr_b16 a[138:139], v215 offset:18432\n" \
"v_add_u32 v227, s74, v212\n" \
"v_mfma_f32_16x16x32_bf16 v[24:27], a[48:51], a[8:11], v[24:27]\n" \
"ds_read_b128 a[204:207], v213 offset:2304\n" \
"ds_read_b64_tr_b16 a[140:141], v215 offset:24576\n" \
"v_mfma_f32_16x16x32_bf16 v[24:27], a[52:55], a[12:15], v[24:27]\n" \
"ds_read_b64_tr_b16 a[142:143], v215 offset:26624\n" \
"v_xor_b32 v228, 32, v227\n" \
"v_mfma_f32_16x16x32_bf16 v[40:43], a[48:51], a[16:19], v[40:43]\n" \
"s_cmp_eq_u32 s79, 65601536\n" \
"s_cselect_b32 s70, 65593344, s70\n" \
"ds_read_b64_tr_b16 a[144:145], v214 offset:16896\n" \
"v_mfma_f32_16x16x32_bf16 v[40:43], a[52:55], a[20:23], v[40:43]\n" \
"buffer_store_dword v224, v225, s[56:59], s70 offen sc1\n" \
"ds_read_b64_tr_b16 a[146:147], v214 offset:18944\n" \
"v_mfma_f32_16x16x32_bf16 v[44:47], a[56:59], a[16:19], v[44:47]\n" \
"ds_read_b64_tr_b16 a[148:149], v214 offset:25088\n" \
"v_mul_u32_u24 v129, 144, v133\n" \
"v_mfma_f32_16x16x32_bf16 v[44:47], a[60:63], a[20:23], v[44:47]\n" \
"ds_read_b64_tr_b16 a[150:151], v214 offset:27136\n" \
"v_lshl_add_u32 v129, v134, 4, v129\n" \
"v_mfma_f32_16x16x32_bf16 v[60:63], a[56:59], a[24:27], v[60:63]\n" \
"ds_read_b64_tr_b16 a[152:153], v215 offset:16896\n" \
"v_add_u32 v129, s83, v129\n" \
"v_mfma_f32_16x16x32_bf16 v[60:63], a[60:63], a[28:31], v[60:63]\n" \
"ds_read_b64_tr_b16 a[154:155], v215 offset:18944\n" \
"v_lshlrev_b32 v134, 4, v134\n" \
"v_mfma_f32_16x16x32_bf16 v[56:59], a[48:51], a[24:27], v[56:59]\n" \
"ds_read_b64_tr_b16 a[156:157], v215 offset:25088\n" \
"v_lshl_add_u32 v130, v133, 12, v134\n" \
"v_mfma_f32_16x16x32_bf16 v[56:59], a[52:55], a[28:31], v[56:59]\n" \
"s_waitcnt lgkmcnt(4)\n" \
"ds_read_b64_tr_b16 a[158:159], v215 offset:27136\n" \
"v_mfma_f32_16x16x32_bf16 v[0:3], a[128:131], a[192:195], v[0:3]\n" \
"ds_read_b128 a[224:227], v226 offset:0\n" \
"ds_read_b64_tr_b16 a[160:161], v227 offset:16384\n" \
"v_mfma_f32_16x16x32_bf16 v[0:3], a[132:135], a[196:199], v[0:3]\n" \
"Lpg12026_%=:\n" \
"s_cbranch_execz Lpg13123_%=\n" \
"ds_read_b64_tr_b16 a[162:163], v227 offset:18432\n" \
"v_mfma_f32_16x16x32_bf16 v[16:19], a[128:131], a[200:203], v[16:19]\n" \
"ds_read_b64_tr_b16 a[164:165], v227 offset:24576\n" \
"v_mfma_f32_16x16x32_bf16 v[16:19], a[132:135], a[204:207], v[16:19]\n" \
"ds_read_b128 a[228:231], v226 offset:2048\n" \
"ds_read_b64_tr_b16 a[166:167], v227 offset:26624\n" \
"v_mfma_f32_16x16x32_bf16 v[20:23], a[136:139], a[200:203], v[20:23]\n" \
"ds_read_b64_tr_b16 a[168:169], v228 offset:16384\n" \
"v_mfma_f32_16x16x32_bf16 v[20:23], a[140:143], a[204:207], v[20:23]\n" \
"ds_read_b64_tr_b16 a[170:171], v228 offset:18432\n" \
"v_mfma_f32_16x16x32_bf16 v[4:7], a[136:139], a[192:195], v[4:7]\n" \
"ds_read_b128 a[232:235], v226 offset:256\n" \
"ds_read_b64_tr_b16 a[172:173], v228 offset:24576\n" \
"v_mfma_f32_16x16x32_bf16 v[4:7], a[140:143], a[196:199], v[4:7]\n" \
"s_waitcnt lgkmcnt(6)\n" \
"ds_read_b64_tr_b16 a[174:175], v228 offset:26624\n" \
"v_mfma_f32_16x16x32_bf16 v[8:11], a[144:147], a[192:195], v[8:11]\n" \
"ds_read_b64_tr_b16 a[176:177], v227 offset:16896\n" \
"v_mfma_f32_16x16x32_bf16 v[8:11], a[148:151], a[196:199], v[8:11]\n" \
"ds_read_b128 a[236:239], v226 offset:2304\n" \
"ds_read_b64_tr_b16 a[178:179], v227 offset:18944\n" \
"v_mfma_f32_16x16x32_bf16 v[24:27], a[144:147], a[200:203], v[24:27]\n" \
"ds_read_b64_tr_b16 a[180:181], v227 offset:25088\n" \
"v_mfma_f32_16x16x32_bf16 v[24:27], a[148:151], a[204:207], v[24:27]\n" \
"ds_read_b64_tr_b16 a[182:183], v227 offset:27136\n" \
"v_mfma_f32_16x16x32_bf16 v[28:31], a[152:155], a[200:203], v[28:31]\n" \
"ds_read_b64_tr_b16 a[184:185], v228 offset:16896\n" \
"v_mfma_f32_16x16x32_bf16 v[28:31], a[156:159], a[204:207], v[28:31]\n" \
"ds_read_b64_tr_b16 a[186:187], v228 offset:18944\n" \
"v_mfma_f32_16x16x32_bf16 v[12:15], a[152:155], a[192:195], v[12:15]\n" \
"ds_read_b64_tr_b16 a[188:189], v228 offset:25088\n" \
"v_mfma_f32_16x16x32_bf16 v[12:15], a[156:159], a[196:199], v[12:15]\n" \
"s_waitcnt lgkmcnt(4)\n" \
"ds_read_b64_tr_b16 a[190:191], v228 offset:27136\n" \
"v_mfma_f32_16x16x32_bf16 v[0:3], a[160:163], a[224:227], v[0:3]\n" \
"s_waitcnt vmcnt(1)\n" \
"ds_read_b128 a[208:211], v213 offset:4096\n" \
"v_mfma_f32_16x16x32_bf16 v[0:3], a[164:167], a[228:231], v[0:3]\n" \
"ds_read_b128 a[212:215], v213 offset:6144\n" \
"v_mfma_f32_16x16x32_bf16 v[0:3], v[64:67], v[176:179], v[0:3]\n" \
"ds_read_b128 a[240:243], v226 offset:4096\n" \
"v_mfma_f32_16x16x32_bf16 v[16:19], a[160:163], a[232:235], v[16:19]\n" \
"ds_read_b128 a[244:247], v226 offset:6144\n" \
"v_mfma_f32_16x16x32_bf16 v[16:19], a[164:167], a[236:239], v[16:19]\n" \
"ds_read_b128 a[216:219], v213 offset:4352\n" \
"v_mfma_f32_16x16x32_bf16 v[16:19], v[64:67], v[184:187], v[16:19]\n" \
"ds_read_b128 a[220:223], v213 offset:6400\n" \
"v_cvt_pk_bf16_f32 v0, v0, v1\n" \
"v_mfma_f32_16x16x32_bf16 v[20:23], a[168:171], a[232:235], v[20:23]\n" \
"ds_read_b128 a[248:251], v226 offset:4352\n" \
"v_cvt_pk_bf16_f32 v1, v2, v3\n" \
"v_mfma_f32_16x16x32_bf16 v[20:23], a[172:175], a[236:239], v[20:23]\n" \
"ds_read_b128 a[252:255], v226 offset:6400\n" \
"v_mfma_f32_16x16x32_bf16 v[20:23], v[68:71], v[184:187], v[20:23]\n" \
"ds_write_b64 v128, v[0:1] offset:0\n" \
"v_mfma_f32_16x16x32_bf16 v[4:7], a[168:171], a[224:227], v[4:7]\n" \
"v_cvt_pk_bf16_f32 v16, v16, v17\n" \
"v_cvt_pk_bf16_f32 v17, v18, v19\n" \
"v_mfma_f32_16x16x32_bf16 v[4:7], a[172:175], a[228:231], v[4:7]\n" \
"ds_write_b64 v128, v[16:17] offset:2304\n" \
"v_mfma_f32_16x16x32_bf16 v[4:7], v[68:71], v[176:179], v[4:7]\n" \
"s_waitcnt lgkmcnt(3)\n" \
"v_cvt_pk_bf16_f32 v20, v20, v21\n" \
"v_cvt_pk_bf16_f32 v21, v22, v23\n" \
"v_mfma_f32_16x16x32_bf16 v[8:11], a[176:179], a[224:227], v[8:11]\n" \
"ds_write_b64 v128, v[20:21] offset:2336\n" \
"v_mfma_f32_16x16x32_bf16 v[8:11], a[180:183], a[228:231], v[8:11]\n" \
"v_mfma_f32_16x16x32_bf16 v[8:11], v[64:67], v[180:183], v[8:11]\n" \
"v_cvt_pk_bf16_f32 v4, v4, v5\n" \
"v_cvt_pk_bf16_f32 v5, v6, v7\n" \
"v_mfma_f32_16x16x32_bf16 v[24:27], a[176:179], a[232:235], v[24:27]\n" \
"ds_write_b64 v128, v[4:5] offset:32\n" \
"v_mfma_f32_16x16x32_bf16 v[24:27], a[180:183], a[236:239], v[24:27]\n" \
"v_mfma_f32_16x16x32_bf16 v[24:27], v[64:67], v[188:191], v[24:27]\n" \
"v_cvt_pk_bf16_f32 v8, v8, v9\n" \
"v_cvt_pk_bf16_f32 v9, v10, v11\n" \
"v_mfma_f32_16x16x32_bf16 v[28:31], a[184:187], a[232:235], v[28:31]\n" \
"ds_write_b64 v128, v[8:9] offset:64\n" \
"v_mfma_f32_16x16x32_bf16 v[28:31], a[188:191], a[236:239], v[28:31]\n" \
"v_mfma_f32_16x16x32_bf16 v[28:31], v[68:71], v[188:191], v[28:31]\n" \
"v_cvt_pk_bf16_f32 v24, v24, v25\n" \
"v_cvt_pk_bf16_f32 v25, v26, v27\n" \
"v_mfma_f32_16x16x32_bf16 v[12:15], a[184:187], a[224:227], v[12:15]\n" \
"ds_write_b64 v128, v[24:25] offset:2368\n" \
"v_mfma_f32_16x16x32_bf16 v[12:15], a[188:191], a[228:231], v[12:15]\n" \
"v_mfma_f32_16x16x32_bf16 v[12:15], v[68:71], v[180:183], v[12:15]\n" \
"v_cvt_pk_bf16_f32 v28, v28, v29\n" \
"v_cvt_pk_bf16_f32 v29, v30, v31\n" \
"v_mfma_f32_16x16x32_bf16 v[32:35], a[128:131], a[208:211], v[32:35]\n" \
"ds_write_b64 v128, v[28:29] offset:2400\n" \
"v_mfma_f32_16x16x32_bf16 v[32:35], a[132:135], a[212:215], v[32:35]\n" \
"v_mfma_f32_16x16x32_bf16 v[32:35], a[160:163], a[240:243], v[32:35]\n" \
"v_cvt_pk_bf16_f32 v12, v12, v13\n" \
"v_cvt_pk_bf16_f32 v13, v14, v15\n" \
"v_mfma_f32_16x16x32_bf16 v[32:35], a[164:167], a[244:247], v[32:35]\n" \
"ds_write_b64 v128, v[12:13] offset:96\n" \
"v_mfma_f32_16x16x32_bf16 v[32:35], v[64:67], v[192:195], v[32:35]\n" \
"ds_read_b128 v[140:143], v129 offset:0\n" \
"v_mfma_f32_16x16x32_bf16 v[36:39], a[168:171], a[240:243], v[36:39]\n" \
"ds_read_b128 v[144:147], v129 offset:1152\n" \
"v_mfma_f32_16x16x32_bf16 v[36:39], a[172:175], a[244:247], v[36:39]\n" \
"ds_read_b128 v[148:151], v129 offset:2304\n" \
"v_mfma_f32_16x16x32_bf16 v[36:39], a[136:139], a[208:211], v[36:39]\n" \
"ds_read_b128 v[152:155], v129 offset:3456\n" \
"v_mfma_f32_16x16x32_bf16 v[36:39], a[140:143], a[212:215], v[36:39]\n" \
"v_cvt_pk_bf16_f32 v32, v32, v33\n" \
"v_cvt_pk_bf16_f32 v33, v34, v35\n" \
"v_mfma_f32_16x16x32_bf16 v[36:39], v[68:71], v[192:195], v[36:39]\n" \
"ds_write_b64 v128, v[32:33] offset:0\n" \
"v_mfma_f32_16x16x32_bf16 v[40:43], a[144:147], a[208:211], v[40:43]\n" \
"v_mfma_f32_16x16x32_bf16 v[40:43], a[148:151], a[212:215], v[40:43]\n" \
"s_add_u32 s70, s77, 0\n" \
"s_waitcnt lgkmcnt(4)\n" \
"v_mfma_f32_16x16x32_bf16 v[40:43], a[176:179], a[240:243], v[40:43]\n" \
"v_cvt_pk_bf16_f32 v36, v36, v37\n" \
"v_cvt_pk_bf16_f32 v37, v38, v39\n" \
"v_mfma_f32_16x16x32_bf16 v[40:43], a[180:183], a[244:247], v[40:43]\n" \
"buffer_store_dwordx4 v[140:143], v130, s[52:55], s70 offen sc1\n" \
"ds_write_b64 v128, v[36:37] offset:32\n" \
"v_mfma_f32_16x16x32_bf16 v[40:43], v[64:67], v[196:199], v[40:43]\n" \
"v_mfma_f32_16x16x32_bf16 v[44:47], a[184:187], a[240:243], v[44:47]\n" \
"v_mfma_f32_16x16x32_bf16 v[44:47], a[188:191], a[244:247], v[44:47]\n" \
"v_mfma_f32_16x16x32_bf16 v[44:47], a[152:155], a[208:211], v[44:47]\n" \
"s_add_u32 s70, s77, 32768\n" \
"v_cvt_pk_bf16_f32 v40, v40, v41\n" \
"v_cvt_pk_bf16_f32 v41, v42, v43\n" \
"v_mfma_f32_16x16x32_bf16 v[44:47], a[156:159], a[212:215], v[44:47]\n" \
"s_waitcnt lgkmcnt(4)\n" \
"buffer_store_dwordx4 v[144:147], v130, s[52:55], s70 offen sc1\n" \
"ds_write_b64 v128, v[40:41] offset:64\n" \
"v_mfma_f32_16x16x32_bf16 v[44:47], v[68:71], v[196:199], v[44:47]\n" \
"v_mfma_f32_16x16x32_bf16 v[48:51], a[128:131], a[216:219], v[48:51]\n" \
"v_mfma_f32_16x16x32_bf16 v[48:51], a[132:135], a[220:223], v[48:51]\n" \
"v_mfma_f32_16x16x32_bf16 v[48:51], a[160:163], a[248:251], v[48:51]\n" \
"s_add_u32 s70, s77, 65536\n" \
"v_cvt_pk_bf16_f32 v44, v44, v45\n" \
"v_cvt_pk_bf16_f32 v45, v46, v47\n" \
"v_mfma_f32_16x16x32_bf16 v[48:51], a[164:167], a[252:255], v[48:51]\n" \
"s_waitcnt lgkmcnt(4)\n" \
"buffer_store_dwordx4 v[148:151], v130, s[52:55], s70 offen sc1\n" \
"ds_write_b64 v128, v[44:45] offset:96\n" \
"v_mfma_f32_16x16x32_bf16 v[48:51], v[64:67], v[200:203], v[48:51]\n" \
"ds_read_b128 v[156:159], v129 offset:0\n" \
"v_mfma_f32_16x16x32_bf16 v[52:55], a[168:171], a[248:251], v[52:55]\n" \
"ds_read_b128 v[160:163], v129 offset:1152\n" \
"v_mfma_f32_16x16x32_bf16 v[52:55], a[172:175], a[252:255], v[52:55]\n" \
"v_mfma_f32_16x16x32_bf16 v[52:55], a[136:139], a[216:219], v[52:55]\n" \
"s_add_u32 s70, s77, 98304\n" \
"v_cvt_pk_bf16_f32 v48, v48, v49\n" \
"v_cvt_pk_bf16_f32 v49, v50, v51\n" \
"v_mfma_f32_16x16x32_bf16 v[52:55], a[140:143], a[220:223], v[52:55]\n" \
"s_waitcnt lgkmcnt(6)\n" \
"buffer_store_dwordx4 v[152:155], v130, s[52:55], s70 offen sc1\n" \
"ds_write_b64 v128, v[48:49] offset:2304\n" \
"v_mfma_f32_16x16x32_bf16 v[52:55], v[68:71], v[200:203], v[52:55]\n" \
"v_mfma_f32_16x16x32_bf16 v[56:59], a[144:147], a[216:219], v[56:59]\n" \
"v_mfma_f32_16x16x32_bf16 v[56:59], a[148:151], a[220:223], v[56:59]\n" \
"v_mfma_f32_16x16x32_bf16 v[56:59], a[176:179], a[248:251], v[56:59]\n" \
"s_add_u32 s70, s77, 131072\n" \
"v_cvt_pk_bf16_f32 v52, v52, v53\n" \
"v_cvt_pk_bf16_f32 v53, v54, v55\n" \
"v_mfma_f32_16x16x32_bf16 v[56:59], a[180:183], a[252:255], v[56:59]\n" \
"s_waitcnt lgkmcnt(2)\n" \
"buffer_store_dwordx4 v[156:159], v130, s[52:55], s70 offen sc1\n" \
"ds_write_b64 v128, v[52:53] offset:2336\n" \
"v_mfma_f32_16x16x32_bf16 v[56:59], v[64:67], v[204:207], v[56:59]\n" \
"v_mfma_f32_16x16x32_bf16 v[60:63], a[184:187], a[248:251], v[60:63]\n" \
"v_mfma_f32_16x16x32_bf16 v[60:63], a[188:191], a[252:255], v[60:63]\n" \
"v_mfma_f32_16x16x32_bf16 v[60:63], a[152:155], a[216:219], v[60:63]\n" \
"s_add_u32 s70, s77, 163840\n" \
"v_cvt_pk_bf16_f32 v56, v56, v57\n" \
"v_cvt_pk_bf16_f32 v57, v58, v59\n" \
"v_mfma_f32_16x16x32_bf16 v[60:63], a[156:159], a[220:223], v[60:63]\n" \
"s_waitcnt lgkmcnt(2)\n" \
"buffer_store_dwordx4 v[160:163], v130, s[52:55], s70 offen sc1\n" \
"ds_write_b64 v128, v[56:57] offset:2368\n" \
"v_mfma_f32_16x16x32_bf16 v[60:63], v[68:71], v[204:207], v[60:63]\n" \
"s_nop 7\n" \
"v_cvt_pk_bf16_f32 v60, v60, v61\n" \
"v_cvt_pk_bf16_f32 v61, v62, v63\n" \
"ds_write_b64 v128, v[60:61] offset:2400\n" \
"ds_read_b128 v[164:167], v129 offset:2304\n" \
"ds_read_b128 v[168:171], v129 offset:3456\n" \
"s_add_u32 s70, s77, 196608\n" \
"s_waitcnt lgkmcnt(1)\n" \
"buffer_store_dwordx4 v[164:167], v130, s[52:55], s70 offen sc1\n" \
"s_add_u32 s70, s77, 229376\n" \
"s_waitcnt lgkmcnt(0)\n" \
"buffer_store_dwordx4 v[168:171], v130, s[52:55], s70 offen sc1\n" \
"s_endpgm\n" \
"Lpg12217_%=:\n" \
"s_cbranch_execz Lpg13190_%=\n" \
"Lo0_%=:\n" \
"v_mfma_f32_16x16x32_bf16 v[0:3], a[32:35], a[0:3], v[0:3]\n" \
"ds_read_b128 a[24:27], v213 offset:4352\n" \
"s_add_u32 m0, s75, s95\n" \
"v_mfma_f32_16x16x32_bf16 v[0:3], a[36:39], a[4:7], v[0:3]\n" \
"s_waitcnt lgkmcnt(1)\n" \
"ds_read_b128 a[28:31], v213 offset:6400\n" \
"global_load_lds_dwordx4 v222, s[36:37] offset:0 sc1\n" \
"v_mfma_f32_16x16x32_bf16 v[4:7], a[40:43], a[0:3], v[4:7]\n" \
"s_sub_u32 s74, s74, 32768\n" \
"s_cselect_b32 s74, 131072, s74\n" \
"ds_read_b64_tr_b16 a[48:49], v214 offset:16896\n" \
"v_mfma_f32_16x16x32_bf16 v[4:7], a[44:47], a[4:7], v[4:7]\n" \
"ds_read_b64_tr_b16 a[50:51], v214 offset:18944\n" \
"v_mfma_f32_16x16x32_bf16 v[20:23], a[40:43], a[8:11], v[20:23]\n" \
"ds_read_b64_tr_b16 a[52:53], v214 offset:25088\n" \
"v_mfma_f32_16x16x32_bf16 v[20:23], a[44:47], a[12:15], v[20:23]\n" \
"ds_read_b64_tr_b16 a[54:55], v214 offset:27136\n" \
"global_load_lds_dwordx4 v222, s[36:37] offset:1024 sc1\n" \
"v_mfma_f32_16x16x32_bf16 v[16:19], a[32:35], a[8:11], v[16:19]\n" \
"ds_read_b64_tr_b16 a[56:57], v215 offset:16896\n" \
"v_mfma_f32_16x16x32_bf16 v[16:19], a[36:39], a[12:15], v[16:19]\n" \
"ds_read_b64_tr_b16 a[58:59], v215 offset:18944\n" \
"v_mfma_f32_16x16x32_bf16 v[32:35], a[32:35], a[16:19], v[32:35]\n" \
"ds_read_b64_tr_b16 a[60:61], v215 offset:25088\n" \
"v_mfma_f32_16x16x32_bf16 v[32:35], a[36:39], a[20:23], v[32:35]\n" \
"ds_read_b64_tr_b16 a[62:63], v215 offset:27136\n" \
"global_load_lds_dwordx4 v222, s[36:37] offset:2048 sc1\n" \
"v_mfma_f32_16x16x32_bf16 v[36:39], a[40:43], a[16:19], v[36:39]\n" \
"v_add_u32 v213, s74, v211\n" \
"v_add_u32 v214, s74, v212\n" \
"v_mfma_f32_16x16x32_bf16 v[36:39], a[44:47], a[20:23], v[36:39]\n" \
"s_waitcnt lgkmcnt(3)\n" \
"v_xor_b32 v215, 32, v214\n" \
"v_mfma_f32_16x16x32_bf16 v[52:55], a[40:43], a[24:27], v[52:55]\n" \
"v_mfma_f32_16x16x32_bf16 v[52:55], a[44:47], a[28:31], v[52:55]\n" \
"s_waitcnt vmcnt(19) lgkmcnt(0)\n" \
"global_load_lds_dwordx4 v222, s[36:37] offset:3072 sc1\n" \
"v_mfma_f32_16x16x32_bf16 v[48:51], a[32:35], a[24:27], v[48:51]\n" \
"s_add_u32 s36, s36, 0x1000\n" \
"v_mfma_f32_16x16x32_bf16 v[48:51], a[36:39], a[28:31], v[48:51]\n" \
"s_barrier\n" \
"v_mfma_f32_16x16x32_bf16 v[8:11], a[48:51], a[0:3], v[8:11]\n" \
"s_add_u32 m0, m0, 0x5000\n" \
"ds_read_b64_tr_b16 a[32:33], v214 offset:16384\n" \
"v_mfma_f32_16x16x32_bf16 v[8:11], a[52:55], a[4:7], v[8:11]\n" \
"ds_read_b64_tr_b16 a[34:35], v214 offset:18432\n" \
"global_load_lds_dwordx4 v216, s[38:39] offset:-4096\n" \
"v_mfma_f32_16x16x32_bf16 v[12:15], a[56:59], a[0:3], v[12:15]\n" \
"ds_read_b64_tr_b16 a[36:37], v214 offset:24576\n" \
"v_mfma_f32_16x16x32_bf16 v[12:15], a[60:63], a[4:7], v[12:15]\n" \
"ds_read_b128 a[0:3], v213 offset:0\n" \
"v_mfma_f32_16x16x32_bf16 v[28:31], a[56:59], a[8:11], v[28:31]\n" \
"ds_read_b128 a[4:7], v213 offset:2048\n" \
"v_mfma_f32_16x16x32_bf16 v[28:31], a[60:63], a[12:15], v[28:31]\n" \
"ds_read_b64_tr_b16 a[38:39], v214 offset:26624\n" \
"global_load_lds_dwordx4 v217, s[38:39] offset:-3072\n" \
"v_mfma_f32_16x16x32_bf16 v[24:27], a[48:51], a[8:11], v[24:27]\n" \
"ds_read_b64_tr_b16 a[40:41], v215 offset:16384\n" \
"v_mfma_f32_16x16x32_bf16 v[24:27], a[52:55], a[12:15], v[24:27]\n" \
"ds_read_b128 a[8:11], v213 offset:256\n" \
"v_mfma_f32_16x16x32_bf16 v[40:43], a[48:51], a[16:19], v[40:43]\n" \
"ds_read_b128 a[12:15], v213 offset:2304\n" \
"v_mfma_f32_16x16x32_bf16 v[40:43], a[52:55], a[20:23], v[40:43]\n" \
"ds_read_b64_tr_b16 a[42:43], v215 offset:18432\n" \
"global_load_lds_dwordx4 v218, s[38:39] offset:-2048\n" \
"v_mfma_f32_16x16x32_bf16 v[44:47], a[56:59], a[16:19], v[44:47]\n" \
"ds_read_b64_tr_b16 a[44:45], v215 offset:24576\n" \
"v_mfma_f32_16x16x32_bf16 v[44:47], a[60:63], a[20:23], v[44:47]\n" \
"ds_read_b128 a[16:19], v213 offset:4096\n" \
"global_load_lds_dwordx4 v219, s[38:39] offset:-1024\n" \
"v_mfma_f32_16x16x32_bf16 v[60:63], a[56:59], a[24:27], v[60:63]\n" \
"s_add_u32 s38, s38, 0x20000\n" \
"s_addc_u32 s39, s39, 0\n" \
"ds_read_b128 a[20:23], v213 offset:6144\n" \
"v_mfma_f32_16x16x32_bf16 v[60:63], a[60:63], a[28:31], v[60:63]\n" \
"s_sub_u32 s75, s75, 32768\n" \
"s_cselect_b32 s75, 131072, s75\n" \
"ds_read_b64_tr_b16 a[46:47], v215 offset:26624\n" \
"v_mfma_f32_16x16x32_bf16 v[56:59], a[48:51], a[24:27], v[56:59]\n" \
"s_sub_u32 s72, s72, 1\n" \
"s_cmp_lg_u32 s72, 0\n" \
"v_mfma_f32_16x16x32_bf16 v[56:59], a[52:55], a[28:31], v[56:59]\n" \
"s_waitcnt lgkmcnt(3)\n" \
"s_cbranch_scc1 Lo0_%=\n" \
"s_setpc_b64 s[68:69]\n" \
"Lrole1_%=:\n" \
"Lpg12305_%=:\n" \
"s_cbranch_execz Lcb1_%=\n" \
"s_sub_u32 s72, 49, s64\n" \
"s_call_b64 s[68:69], Lo0_%=\n" \
"buffer_load_dword v220, v221, s[56:59], s82 offen sc1\n" \
"s_mov_b32 s72, 2\n" \
"s_call_b64 s[68:69], Lo0_%=\n" \
"s_waitcnt vmcnt(16)\n" \
"v_cmp_ne_u32 vcc, s60, v220\n" \
"s_nop 1\n" \
"s_and_b32 vcc_hi, vcc_lo, 65535\n" \
"s_cbranch_scc1 Lpl5_%=\n" \
"Lpr5_%=:\n" \
"s_mov_b32 s36, s73\n" \
"s_mov_b64 s[38:39], s[40:41]\n" \
"s_mov_b32 s72, 4\n" \
"s_call_b64 s[68:69], Lo0_%=\n" \
"v_mfma_f32_16x16x32_bf16 v[64:67], a[32:35], a[0:3], 0\n" \
"ds_read_b128 a[24:27], v213 offset:4352\n" \
"s_add_u32 m0, s75, s95\n" \
"v_mfma_f32_16x16x32_bf16 v[64:67], a[36:39], a[4:7], v[64:67]\n" \
"s_waitcnt lgkmcnt(1)\n" \
"ds_read_b128 a[28:31], v213 offset:6400\n" \
"global_load_lds_dwordx4 v222, s[36:37] offset:0 sc1\n" \
"v_mfma_f32_16x16x32_bf16 v[68:71], a[40:43], a[0:3], 0\n" \
"s_sub_u32 s74, s74, 32768\n" \
"s_cselect_b32 s74, 131072, s74\n" \
"ds_read_b64_tr_b16 a[48:49], v214 offset:16896\n" \
"v_mfma_f32_16x16x32_bf16 v[68:71], a[44:47], a[4:7], v[68:71]\n" \
"ds_read_b64_tr_b16 a[50:51], v214 offset:18944\n" \
"v_cvt_pk_bf16_f32 v0, v0, v1\n" \
"v_mfma_f32_16x16x32_bf16 v[84:87], a[40:43], a[8:11], 0\n" \
"ds_read_b64_tr_b16 a[52:53], v214 offset:25088\n" \
"v_cvt_pk_bf16_f32 v1, v2, v3\n" \
"v_mfma_f32_16x16x32_bf16 v[84:87], a[44:47], a[12:15], v[84:87]\n" \
"ds_read_b64_tr_b16 a[54:55], v214 offset:27136\n" \
"global_load_lds_dwordx4 v222, s[36:37] offset:1024 sc1\n" \
"v_mfma_f32_16x16x32_bf16 v[80:83], a[32:35], a[8:11], 0\n" \
"ds_read_b64_tr_b16 a[56:57], v215 offset:16896\n" \
"v_cvt_pk_bf16_f32 v2, v4, v5\n" \
"v_mfma_f32_16x16x32_bf16 v[80:83], a[36:39], a[12:15], v[80:83]\n" \
"ds_read_b64_tr_b16 a[58:59], v215 offset:18944\n" \
"v_cvt_pk_bf16_f32 v3, v6, v7\n" \
"v_mfma_f32_16x16x32_bf16 v[96:99], a[32:35], a[16:19], 0\n" \
"s_add_u32 s71, s76, 0\n" \
"ds_read_b64_tr_b16 a[60:61], v215 offset:25088\n" \
"v_mfma_f32_16x16x32_bf16 v[96:99], a[36:39], a[20:23], v[96:99]\n" \
"ds_read_b64_tr_b16 a[62:63], v215 offset:27136\n" \
"global_load_lds_dwordx4 v222, s[36:37] offset:2048 sc1\n" \
"v_mfma_f32_16x16x32_bf16 v[100:103], a[40:43], a[16:19], 0\n" \
"v_add_u32 v213, s74, v211\n" \
"v_add_u32 v214, s74, v212\n" \
"v_mfma_f32_16x16x32_bf16 v[100:103], a[44:47], a[20:23], v[100:103]\n" \
"s_waitcnt lgkmcnt(3)\n" \
"v_xor_b32 v215, 32, v214\n" \
"v_mfma_f32_16x16x32_bf16 v[116:119], a[40:43], a[24:27], 0\n" \
"v_mfma_f32_16x16x32_bf16 v[116:119], a[44:47], a[28:31], v[116:119]\n" \
"s_waitcnt vmcnt(19) lgkmcnt(0)\n" \
"global_load_lds_dwordx4 v222, s[36:37] offset:3072 sc1\n" \
"v_mfma_f32_16x16x32_bf16 v[112:115], a[32:35], a[24:27], 0\n" \
"s_add_u32 s36, s36, 0x1000\n" \
"v_mfma_f32_16x16x32_bf16 v[112:115], a[36:39], a[28:31], v[112:115]\n" \
"s_barrier\n" \
"v_mfma_f32_16x16x32_bf16 v[72:75], a[48:51], a[0:3], 0\n" \
"s_add_u32 m0, m0, 0x5000\n" \
"ds_read_b64_tr_b16 a[32:33], v214 offset:16384\n" \
"v_mfma_f32_16x16x32_bf16 v[72:75], a[52:55], a[4:7], v[72:75]\n" \
"ds_read_b64_tr_b16 a[34:35], v214 offset:18432\n" \
"global_load_lds_dwordx4 v216, s[38:39] offset:-4096\n" \
"v_mfma_f32_16x16x32_bf16 v[76:79], a[56:59], a[0:3], 0\n" \
"ds_read_b64_tr_b16 a[36:37], v214 offset:24576\n" \
"v_mfma_f32_16x16x32_bf16 v[76:79], a[60:63], a[4:7], v[76:79]\n" \
"ds_read_b128 a[0:3], v213 offset:0\n" \
"v_mfma_f32_16x16x32_bf16 v[92:95], a[56:59], a[8:11], 0\n" \
"ds_read_b128 a[4:7], v213 offset:2048\n" \
"v_mfma_f32_16x16x32_bf16 v[92:95], a[60:63], a[12:15], v[92:95]\n" \
"ds_read_b64_tr_b16 a[38:39], v214 offset:26624\n" \
"global_load_lds_dwordx4 v217, s[38:39] offset:-3072\n" \
"v_mfma_f32_16x16x32_bf16 v[88:91], a[48:51], a[8:11], 0\n" \
"ds_read_b64_tr_b16 a[40:41], v215 offset:16384\n" \
"v_mfma_f32_16x16x32_bf16 v[88:91], a[52:55], a[12:15], v[88:91]\n" \
"buffer_store_dwordx4 v[0:3], v248, s[56:59], s71 offen offset:0 sc1\n" \
"ds_read_b128 a[8:11], v213 offset:256\n" \
"v_cvt_pk_bf16_f32 v4, v8, v9\n" \
"v_cvt_pk_bf16_f32 v5, v10, v11\n" \
"v_cvt_pk_bf16_f32 v6, v12, v13\n" \
"v_mfma_f32_16x16x32_bf16 v[104:107], a[48:51], a[16:19], 0\n" \
"ds_read_b128 a[12:15], v213 offset:2304\n" \
"v_cvt_pk_bf16_f32 v7, v14, v15\n" \
"v_mfma_f32_16x16x32_bf16 v[104:107], a[52:55], a[20:23], v[104:107]\n" \
"ds_read_b64_tr_b16 a[42:43], v215 offset:18432\n" \
"global_load_lds_dwordx4 v218, s[38:39] offset:-2048\n" \
"v_mfma_f32_16x16x32_bf16 v[108:111], a[56:59], a[16:19], 0\n" \
"ds_read_b64_tr_b16 a[44:45], v215 offset:24576\n" \
"v_mfma_f32_16x16x32_bf16 v[108:111], a[60:63], a[20:23], v[108:111]\n" \
"buffer_store_dwordx4 v[4:7], v248, s[56:59], s71 offen offset:1024 sc1\n" \
"ds_read_b128 a[16:19], v213 offset:4096\n" \
"v_cvt_pk_bf16_f32 v8, v16, v17\n" \
"v_cvt_pk_bf16_f32 v9, v18, v19\n" \
"v_cvt_pk_bf16_f32 v10, v20, v21\n" \
"v_mfma_f32_16x16x32_bf16 v[124:127], a[56:59], a[24:27], 0\n" \
"ds_read_b128 a[20:23], v213 offset:6144\n" \
"global_load_lds_dwordx4 v219, s[38:39] offset:-1024\n" \
"v_mfma_f32_16x16x32_bf16 v[124:127], a[60:63], a[28:31], v[124:127]\n" \
"s_add_u32 s38, s38, 0x20000\n" \
"s_addc_u32 s39, s39, 0\n" \
"ds_read_b64_tr_b16 a[46:47], v215 offset:26624\n" \
"v_mfma_f32_16x16x32_bf16 v[120:123], a[48:51], a[24:27], 0\n" \
"s_sub_u32 s75, s75, 32768\n" \
"s_cselect_b32 s75, 131072, s75\n" \
"v_cvt_pk_bf16_f32 v11, v22, v23\n" \
"v_mfma_f32_16x16x32_bf16 v[120:123], a[52:55], a[28:31], v[120:123]\n" \
"s_waitcnt lgkmcnt(3)\n" \
"buffer_store_dwordx4 v[8:11], v248, s[56:59], s71 offen offset:2048 sc1\n" \
"v_cvt_pk_bf16_f32 v12, v24, v25\n" \
"v_cvt_pk_bf16_f32 v13, v26, v27\n" \
"v_cvt_pk_bf16_f32 v14, v28, v29\n" \
"v_cvt_pk_bf16_f32 v15, v30, v31\n" \
"v_mfma_f32_16x16x32_bf16 v[64:67], a[32:35], a[0:3], v[64:67]\n" \
"ds_read_b128 a[24:27], v213 offset:4352\n" \
"s_add_u32 m0, s75, s95\n" \
"v_mfma_f32_16x16x32_bf16 v[64:67], a[36:39], a[4:7], v[64:67]\n" \
"s_waitcnt lgkmcnt(1)\n" \
"ds_read_b128 a[28:31], v213 offset:6400\n" \
"global_load_lds_dwordx4 v222, s[36:37] offset:0 sc1\n" \
"v_mfma_f32_16x16x32_bf16 v[68:71], a[40:43], a[0:3], v[68:71]\n" \
"s_sub_u32 s74, s74, 32768\n" \
"s_cselect_b32 s74, 131072, s74\n" \
"ds_read_b64_tr_b16 a[48:49], v214 offset:16896\n" \
"v_mfma_f32_16x16x32_bf16 v[68:71], a[44:47], a[4:7], v[68:71]\n" \
"ds_read_b64_tr_b16 a[50:51], v214 offset:18944\n" \
"v_mfma_f32_16x16x32_bf16 v[84:87], a[40:43], a[8:11], v[84:87]\n" \
"ds_read_b64_tr_b16 a[52:53], v214 offset:25088\n" \
"v_mfma_f32_16x16x32_bf16 v[84:87], a[44:47], a[12:15], v[84:87]\n" \
"ds_read_b64_tr_b16 a[54:55], v214 offset:27136\n" \
"global_load_lds_dwordx4 v222, s[36:37] offset:1024 sc1\n" \
"v_mfma_f32_16x16x32_bf16 v[80:83], a[32:35], a[8:11], v[80:83]\n" \
"ds_read_b64_tr_b16 a[56:57], v215 offset:16896\n" \
"v_mfma_f32_16x16x32_bf16 v[80:83], a[36:39], a[12:15], v[80:83]\n" \
"ds_read_b64_tr_b16 a[58:59], v215 offset:18944\n" \
"v_mfma_f32_16x16x32_bf16 v[96:99], a[32:35], a[16:19], v[96:99]\n" \
"ds_read_b64_tr_b16 a[60:61], v215 offset:25088\n" \
"v_mfma_f32_16x16x32_bf16 v[96:99], a[36:39], a[20:23], v[96:99]\n" \
"ds_read_b64_tr_b16 a[62:63], v215 offset:27136\n" \
"global_load_lds_dwordx4 v222, s[36:37] offset:2048 sc1\n" \
"v_mfma_f32_16x16x32_bf16 v[100:103], a[40:43], a[16:19], v[100:103]\n" \
"v_add_u32 v213, s74, v211\n" \
"v_add_u32 v214, s74, v212\n" \
"v_mfma_f32_16x16x32_bf16 v[100:103], a[44:47], a[20:23], v[100:103]\n" \
"s_waitcnt lgkmcnt(3)\n" \
"v_xor_b32 v215, 32, v214\n" \
"v_mfma_f32_16x16x32_bf16 v[116:119], a[40:43], a[24:27], v[116:119]\n" \
"v_mfma_f32_16x16x32_bf16 v[116:119], a[44:47], a[28:31], v[116:119]\n" \
"s_waitcnt vmcnt(22) lgkmcnt(0)\n" \
"global_load_lds_dwordx4 v222, s[36:37] offset:3072 sc1\n" \
"v_mfma_f32_16x16x32_bf16 v[112:115], a[32:35], a[24:27], v[112:115]\n" \
"s_add_u32 s36, s36, 0x1000\n" \
"v_mfma_f32_16x16x32_bf16 v[112:115], a[36:39], a[28:31], v[112:115]\n" \
"s_barrier\n" \
"v_mfma_f32_16x16x32_bf16 v[72:75], a[48:51], a[0:3], v[72:75]\n" \
"s_add_u32 m0, m0, 0x5000\n" \
"ds_read_b64_tr_b16 a[32:33], v214 offset:16384\n" \
"v_mfma_f32_16x16x32_bf16 v[72:75], a[52:55], a[4:7], v[72:75]\n" \
"ds_read_b64_tr_b16 a[34:35], v214 offset:18432\n" \
"global_load_lds_dwordx4 v216, s[38:39] offset:-4096\n" \
"v_mfma_f32_16x16x32_bf16 v[76:79], a[56:59], a[0:3], v[76:79]\n" \
"ds_read_b64_tr_b16 a[36:37], v214 offset:24576\n" \
"v_mfma_f32_16x16x32_bf16 v[76:79], a[60:63], a[4:7], v[76:79]\n" \
"ds_read_b128 a[0:3], v213 offset:0\n" \
"v_mfma_f32_16x16x32_bf16 v[92:95], a[56:59], a[8:11], v[92:95]\n" \
"ds_read_b128 a[4:7], v213 offset:2048\n" \
"v_mfma_f32_16x16x32_bf16 v[92:95], a[60:63], a[12:15], v[92:95]\n" \
"ds_read_b64_tr_b16 a[38:39], v214 offset:26624\n" \
"global_load_lds_dwordx4 v217, s[38:39] offset:-3072\n" \
"v_mfma_f32_16x16x32_bf16 v[88:91], a[48:51], a[8:11], v[88:91]\n" \
"ds_read_b64_tr_b16 a[40:41], v215 offset:16384\n" \
"v_mfma_f32_16x16x32_bf16 v[88:91], a[52:55], a[12:15], v[88:91]\n" \
"buffer_store_dwordx4 v[12:15], v248, s[56:59], s71 offen offset:3072 sc1\n" \
"ds_read_b128 a[8:11], v213 offset:256\n" \
"v_cvt_pk_bf16_f32 v16, v32, v33\n" \
"v_cvt_pk_bf16_f32 v17, v34, v35\n" \
"v_cvt_pk_bf16_f32 v18, v36, v37\n" \
"v_mfma_f32_16x16x32_bf16 v[104:107], a[48:51], a[16:19], v[104:107]\n" \
"ds_read_b128 a[12:15], v213 offset:2304\n" \
"v_cvt_pk_bf16_f32 v19, v38, v39\n" \
"v_mfma_f32_16x16x32_bf16 v[104:107], a[52:55], a[20:23], v[104:107]\n" \
"s_add_u32 s71, s76, 4096\n" \
"ds_read_b64_tr_b16 a[42:43], v215 offset:18432\n" \
"global_load_lds_dwordx4 v218, s[38:39] offset:-2048\n" \
"v_mfma_f32_16x16x32_bf16 v[108:111], a[56:59], a[16:19], v[108:111]\n" \
"ds_read_b64_tr_b16 a[44:45], v215 offset:24576\n" \
"v_mfma_f32_16x16x32_bf16 v[108:111], a[60:63], a[20:23], v[108:111]\n" \
"buffer_store_dwordx4 v[16:19], v248, s[56:59], s71 offen offset:0 sc1\n" \
"ds_read_b128 a[16:19], v213 offset:4096\n" \
"v_cvt_pk_bf16_f32 v20, v40, v41\n" \
"v_cvt_pk_bf16_f32 v21, v42, v43\n" \
"v_cvt_pk_bf16_f32 v22, v44, v45\n" \
"v_mfma_f32_16x16x32_bf16 v[124:127], a[56:59], a[24:27], v[124:127]\n" \
"ds_read_b128 a[20:23], v213 offset:6144\n" \
"global_load_lds_dwordx4 v219, s[38:39] offset:-1024\n" \
"v_mfma_f32_16x16x32_bf16 v[124:127], a[60:63], a[28:31], v[124:127]\n" \
"s_add_u32 s38, s38, 0x20000\n" \
"s_addc_u32 s39, s39, 0\n" \
"ds_read_b64_tr_b16 a[46:47], v215 offset:26624\n" \
"v_mfma_f32_16x16x32_bf16 v[120:123], a[48:51], a[24:27], v[120:123]\n" \
"s_sub_u32 s75, s75, 32768\n" \
"s_cselect_b32 s75, 131072, s75\n" \
"v_cvt_pk_bf16_f32 v23, v46, v47\n" \
"v_mfma_f32_16x16x32_bf16 v[120:123], a[52:55], a[28:31], v[120:123]\n" \
"s_waitcnt lgkmcnt(3)\n" \
"buffer_store_dwordx4 v[20:23], v248, s[56:59], s71 offen offset:1024 sc1\n" \
"v_cvt_pk_bf16_f32 v24, v48, v49\n" \
"v_cvt_pk_bf16_f32 v25, v50, v51\n" \
"v_cvt_pk_bf16_f32 v26, v52, v53\n" \
"v_cvt_pk_bf16_f32 v27, v54, v55\n" \
"v_mfma_f32_16x16x32_bf16 v[64:67], a[32:35], a[0:3], v[64:67]\n" \
"ds_read_b128 a[24:27], v213 offset:4352\n" \
"s_add_u32 m0, s75, s95\n" \
"v_mfma_f32_16x16x32_bf16 v[64:67], a[36:39], a[4:7], v[64:67]\n" \
"s_waitcnt lgkmcnt(1)\n" \
"ds_read_b128 a[28:31], v213 offset:6400\n" \
"global_load_lds_dwordx4 v222, s[36:37] offset:0 sc1\n" \
"v_mfma_f32_16x16x32_bf16 v[68:71], a[40:43], a[0:3], v[68:71]\n" \
"s_sub_u32 s74, s74, 32768\n" \
"s_cselect_b32 s74, 131072, s74\n" \
"ds_read_b64_tr_b16 a[48:49], v214 offset:16896\n" \
"v_mfma_f32_16x16x32_bf16 v[68:71], a[44:47], a[4:7], v[68:71]\n" \
"ds_read_b64_tr_b16 a[50:51], v214 offset:18944\n" \
"v_mfma_f32_16x16x32_bf16 v[84:87], a[40:43], a[8:11], v[84:87]\n" \
"ds_read_b64_tr_b16 a[52:53], v214 offset:25088\n" \
"v_mfma_f32_16x16x32_bf16 v[84:87], a[44:47], a[12:15], v[84:87]\n" \
"ds_read_b64_tr_b16 a[54:55], v214 offset:27136\n" \
"global_load_lds_dwordx4 v222, s[36:37] offset:1024 sc1\n" \
"v_mfma_f32_16x16x32_bf16 v[80:83], a[32:35], a[8:11], v[80:83]\n" \
"ds_read_b64_tr_b16 a[56:57], v215 offset:16896\n" \
"v_mfma_f32_16x16x32_bf16 v[80:83], a[36:39], a[12:15], v[80:83]\n" \
"ds_read_b64_tr_b16 a[58:59], v215 offset:18944\n" \
"v_mfma_f32_16x16x32_bf16 v[96:99], a[32:35], a[16:19], v[96:99]\n" \
"ds_read_b64_tr_b16 a[60:61], v215 offset:25088\n" \
"v_mfma_f32_16x16x32_bf16 v[96:99], a[36:39], a[20:23], v[96:99]\n" \
"ds_read_b64_tr_b16 a[62:63], v215 offset:27136\n" \
"global_load_lds_dwordx4 v222, s[36:37] offset:2048 sc1\n" \
"v_mfma_f32_16x16x32_bf16 v[100:103], a[40:43], a[16:19], v[100:103]\n" \
"v_add_u32 v213, s74, v211\n" \
"v_add_u32 v214, s74, v212\n" \
"v_mfma_f32_16x16x32_bf16 v[100:103], a[44:47], a[20:23], v[100:103]\n" \
"s_waitcnt lgkmcnt(3)\n" \
"v_xor_b32 v215, 32, v214\n" \
"v_mfma_f32_16x16x32_bf16 v[116:119], a[40:43], a[24:27], v[116:119]\n" \
"v_mfma_f32_16x16x32_bf16 v[116:119], a[44:47], a[28:31], v[116:119]\n" \
"s_waitcnt vmcnt(25) lgkmcnt(0)\n" \
"global_load_lds_dwordx4 v222, s[36:37] offset:3072 sc1\n" \
"v_mfma_f32_16x16x32_bf16 v[112:115], a[32:35], a[24:27], v[112:115]\n" \
"s_add_u32 s36, s36, 0x1000\n" \
"v_mfma_f32_16x16x32_bf16 v[112:115], a[36:39], a[28:31], v[112:115]\n" \
"s_barrier\n" \
"v_mfma_f32_16x16x32_bf16 v[72:75], a[48:51], a[0:3], v[72:75]\n" \
"s_add_u32 m0, m0, 0x5000\n" \
"ds_read_b64_tr_b16 a[32:33], v214 offset:16384\n" \
"v_mfma_f32_16x16x32_bf16 v[72:75], a[52:55], a[4:7], v[72:75]\n" \
"ds_read_b64_tr_b16 a[34:35], v214 offset:18432\n" \
"global_load_lds_dwordx4 v216, s[38:39] offset:-4096\n" \
"v_mfma_f32_16x16x32_bf16 v[76:79], a[56:59], a[0:3], v[76:79]\n" \
"ds_read_b64_tr_b16 a[36:37], v214 offset:24576\n" \
"v_mfma_f32_16x16x32_bf16 v[76:79], a[60:63], a[4:7], v[76:79]\n" \
"ds_read_b128 a[0:3], v213 offset:0\n" \
"v_mfma_f32_16x16x32_bf16 v[92:95], a[56:59], a[8:11], v[92:95]\n" \
"ds_read_b128 a[4:7], v213 offset:2048\n" \
"v_mfma_f32_16x16x32_bf16 v[92:95], a[60:63], a[12:15], v[92:95]\n" \
"ds_read_b64_tr_b16 a[38:39], v214 offset:26624\n" \
"global_load_lds_dwordx4 v217, s[38:39] offset:-3072\n" \
"v_mfma_f32_16x16x32_bf16 v[88:91], a[48:51], a[8:11], v[88:91]\n" \
"ds_read_b64_tr_b16 a[40:41], v215 offset:16384\n" \
"Lpg12577_%=:\n" \
"s_cbranch_execz Lcb1_%=\n" \
"v_mfma_f32_16x16x32_bf16 v[88:91], a[52:55], a[12:15], v[88:91]\n" \
"buffer_store_dwordx4 v[24:27], v248, s[56:59], s71 offen offset:2048 sc1\n" \
"ds_read_b128 a[8:11], v213 offset:256\n" \
"v_cvt_pk_bf16_f32 v28, v56, v57\n" \
"v_cvt_pk_bf16_f32 v29, v58, v59\n" \
"v_cvt_pk_bf16_f32 v30, v60, v61\n" \
"v_mfma_f32_16x16x32_bf16 v[104:107], a[48:51], a[16:19], v[104:107]\n" \
"ds_read_b128 a[12:15], v213 offset:2304\n" \
"v_cvt_pk_bf16_f32 v31, v62, v63\n" \
"v_mfma_f32_16x16x32_bf16 v[104:107], a[52:55], a[20:23], v[104:107]\n" \
"ds_read_b64_tr_b16 a[42:43], v215 offset:18432\n" \
"global_load_lds_dwordx4 v218, s[38:39] offset:-2048\n" \
"v_mfma_f32_16x16x32_bf16 v[108:111], a[56:59], a[16:19], v[108:111]\n" \
"ds_read_b64_tr_b16 a[44:45], v215 offset:24576\n" \
"v_mfma_f32_16x16x32_bf16 v[108:111], a[60:63], a[20:23], v[108:111]\n" \
"buffer_store_dwordx4 v[28:31], v248, s[56:59], s71 offen offset:3072 sc1\n" \
"ds_read_b128 a[16:19], v213 offset:4096\n" \
"v_mfma_f32_16x16x32_bf16 v[124:127], a[56:59], a[24:27], v[124:127]\n" \
"ds_read_b128 a[20:23], v213 offset:6144\n" \
"global_load_lds_dwordx4 v219, s[38:39] offset:-1024\n" \
"v_mfma_f32_16x16x32_bf16 v[124:127], a[60:63], a[28:31], v[124:127]\n" \
"s_add_u32 s38, s38, 0x20000\n" \
"s_addc_u32 s39, s39, 0\n" \
"ds_read_b64_tr_b16 a[46:47], v215 offset:26624\n" \
"v_mfma_f32_16x16x32_bf16 v[120:123], a[48:51], a[24:27], v[120:123]\n" \
"s_sub_u32 s75, s75, 32768\n" \
"s_cselect_b32 s75, 131072, s75\n" \
"v_mfma_f32_16x16x32_bf16 v[120:123], a[52:55], a[28:31], v[120:123]\n" \
"s_waitcnt lgkmcnt(3)\n" \
"v_mfma_f32_16x16x32_bf16 v[64:67], a[32:35], a[0:3], v[64:67]\n" \
"ds_read_b128 a[24:27], v213 offset:4352\n" \
"s_add_u32 m0, s75, s95\n" \
"v_mfma_f32_16x16x32_bf16 v[64:67], a[36:39], a[4:7], v[64:67]\n" \
"s_waitcnt lgkmcnt(1)\n" \
"ds_read_b128 a[28:31], v213 offset:6400\n" \
"global_load_lds_dwordx4 v222, s[36:37] offset:0 sc1\n" \
"v_mfma_f32_16x16x32_bf16 v[68:71], a[40:43], a[0:3], v[68:71]\n" \
"s_sub_u32 s74, s74, 32768\n" \
"s_cselect_b32 s74, 131072, s74\n" \
"ds_read_b64_tr_b16 a[48:49], v214 offset:16896\n" \
"v_mfma_f32_16x16x32_bf16 v[68:71], a[44:47], a[4:7], v[68:71]\n" \
"ds_read_b64_tr_b16 a[50:51], v214 offset:18944\n" \
"v_mfma_f32_16x16x32_bf16 v[84:87], a[40:43], a[8:11], v[84:87]\n" \
"ds_read_b64_tr_b16 a[52:53], v214 offset:25088\n" \
"v_mfma_f32_16x16x32_bf16 v[84:87], a[44:47], a[12:15], v[84:87]\n" \
"ds_read_b64_tr_b16 a[54:55], v214 offset:27136\n" \
"global_load_lds_dwordx4 v222, s[36:37] offset:1024 sc1\n" \
"v_mfma_f32_16x16x32_bf16 v[80:83], a[32:35], a[8:11], v[80:83]\n" \
"ds_read_b64_tr_b16 a[56:57], v215 offset:16896\n" \
"v_mfma_f32_16x16x32_bf16 v[80:83], a[36:39], a[12:15], v[80:83]\n" \
"ds_read_b64_tr_b16 a[58:59], v215 offset:18944\n" \
"v_mfma_f32_16x16x32_bf16 v[96:99], a[32:35], a[16:19], v[96:99]\n" \
"ds_read_b64_tr_b16 a[60:61], v215 offset:25088\n" \
"v_mfma_f32_16x16x32_bf16 v[96:99], a[36:39], a[20:23], v[96:99]\n" \
"ds_read_b64_tr_b16 a[62:63], v215 offset:27136\n" \
"global_load_lds_dwordx4 v222, s[36:37] offset:2048 sc1\n" \
"v_mfma_f32_16x16x32_bf16 v[100:103], a[40:43], a[16:19], v[100:103]\n" \
"v_add_u32 v213, s74, v211\n" \
"v_add_u32 v214, s74, v212\n" \
"v_mfma_f32_16x16x32_bf16 v[100:103], a[44:47], a[20:23], v[100:103]\n" \
"s_waitcnt lgkmcnt(3)\n" \
"v_xor_b32 v215, 32, v214\n" \
"v_mfma_f32_16x16x32_bf16 v[116:119], a[40:43], a[24:27], v[116:119]\n" \
"v_mfma_f32_16x16x32_bf16 v[116:119], a[44:47], a[28:31], v[116:119]\n" \
"s_waitcnt vmcnt(25) lgkmcnt(0)\n" \
"global_load_lds_dwordx4 v222, s[36:37] offset:3072 sc1\n" \
"v_mfma_f32_16x16x32_bf16 v[112:115], a[32:35], a[24:27], v[112:115]\n" \
"s_add_u32 s36, s36, 0x1000\n" \
"v_mfma_f32_16x16x32_bf16 v[112:115], a[36:39], a[28:31], v[112:115]\n" \
"s_barrier\n" \
"v_mfma_f32_16x16x32_bf16 v[72:75], a[48:51], a[0:3], v[72:75]\n" \
"s_add_u32 m0, m0, 0x5000\n" \
"ds_read_b64_tr_b16 a[32:33], v214 offset:16384\n" \
"v_mfma_f32_16x16x32_bf16 v[72:75], a[52:55], a[4:7], v[72:75]\n" \
"ds_read_b64_tr_b16 a[34:35], v214 offset:18432\n" \
"global_load_lds_dwordx4 v216, s[38:39] offset:-4096\n" \
"v_mfma_f32_16x16x32_bf16 v[76:79], a[56:59], a[0:3], v[76:79]\n" \
"ds_read_b64_tr_b16 a[36:37], v214 offset:24576\n" \
"v_mfma_f32_16x16x32_bf16 v[76:79], a[60:63], a[4:7], v[76:79]\n" \
"ds_read_b128 a[0:3], v213 offset:0\n" \
"v_mfma_f32_16x16x32_bf16 v[92:95], a[56:59], a[8:11], v[92:95]\n" \
"ds_read_b128 a[4:7], v213 offset:2048\n" \
"v_mfma_f32_16x16x32_bf16 v[92:95], a[60:63], a[12:15], v[92:95]\n" \
"ds_read_b64_tr_b16 a[38:39], v214 offset:26624\n" \
"global_load_lds_dwordx4 v217, s[38:39] offset:-3072\n" \
"v_mfma_f32_16x16x32_bf16 v[88:91], a[48:51], a[8:11], v[88:91]\n" \
"ds_read_b64_tr_b16 a[40:41], v215 offset:16384\n" \
"v_mfma_f32_16x16x32_bf16 v[88:91], a[52:55], a[12:15], v[88:91]\n" \
"ds_read_b128 a[8:11], v213 offset:256\n" \
"v_mfma_f32_16x16x32_bf16 v[104:107], a[48:51], a[16:19], v[104:107]\n" \
"ds_read_b128 a[12:15], v213 offset:2304\n" \
"v_mfma_f32_16x16x32_bf16 v[104:107], a[52:55], a[20:23], v[104:107]\n" \
"ds_read_b64_tr_b16 a[42:43], v215 offset:18432\n" \
"global_load_lds_dwordx4 v218, s[38:39] offset:-2048\n" \
"v_mfma_f32_16x16x32_bf16 v[108:111], a[56:59], a[16:19], v[108:111]\n" \
"ds_read_b64_tr_b16 a[44:45], v215 offset:24576\n" \
"v_mfma_f32_16x16x32_bf16 v[108:111], a[60:63], a[20:23], v[108:111]\n" \
"ds_read_b128 a[16:19], v213 offset:4096\n" \
"v_mfma_f32_16x16x32_bf16 v[124:127], a[56:59], a[24:27], v[124:127]\n" \
"ds_read_b128 a[20:23], v213 offset:6144\n" \
"global_load_lds_dwordx4 v219, s[38:39] offset:-1024\n" \
"v_mfma_f32_16x16x32_bf16 v[124:127], a[60:63], a[28:31], v[124:127]\n" \
"s_add_u32 s38, s38, 0x20000\n" \
"s_addc_u32 s39, s39, 0\n" \
"ds_read_b64_tr_b16 a[46:47], v215 offset:26624\n" \
"v_mfma_f32_16x16x32_bf16 v[120:123], a[48:51], a[24:27], v[120:123]\n" \
"s_sub_u32 s75, s75, 32768\n" \
"s_cselect_b32 s75, 131072, s75\n" \
"v_mfma_f32_16x16x32_bf16 v[120:123], a[52:55], a[28:31], v[120:123]\n" \
"s_waitcnt lgkmcnt(3)\n" \
"v_mfma_f32_16x16x32_bf16 v[64:67], a[32:35], a[0:3], v[64:67]\n" \
"ds_read_b128 a[24:27], v213 offset:4352\n" \
"s_add_u32 m0, s75, s95\n" \
"v_mfma_f32_16x16x32_bf16 v[64:67], a[36:39], a[4:7], v[64:67]\n" \
"s_waitcnt lgkmcnt(1)\n" \
"ds_read_b128 a[28:31], v213 offset:6400\n" \
"global_load_lds_dwordx4 v222, s[36:37] offset:0 sc1\n" \
"v_mfma_f32_16x16x32_bf16 v[68:71], a[40:43], a[0:3], v[68:71]\n" \
"s_sub_u32 s74, s74, 32768\n" \
"s_cselect_b32 s74, 131072, s74\n" \
"ds_read_b64_tr_b16 a[48:49], v214 offset:16896\n" \
"v_mfma_f32_16x16x32_bf16 v[68:71], a[44:47], a[4:7], v[68:71]\n" \
"ds_read_b64_tr_b16 a[50:51], v214 offset:18944\n" \
"v_mfma_f32_16x16x32_bf16 v[84:87], a[40:43], a[8:11], v[84:87]\n" \
"ds_read_b64_tr_b16 a[52:53], v214 offset:25088\n" \
"v_mfma_f32_16x16x32_bf16 v[84:87], a[44:47], a[12:15], v[84:87]\n" \
"ds_read_b64_tr_b16 a[54:55], v214 offset:27136\n" \
"global_load_lds_dwordx4 v222, s[36:37] offset:1024 sc1\n" \
"v_mfma_f32_16x16x32_bf16 v[80:83], a[32:35], a[8:11], v[80:83]\n" \
"ds_read_b64_tr_b16 a[56:57], v215 offset:16896\n" \
"v_mfma_f32_16x16x32_bf16 v[80:83], a[36:39], a[12:15], v[80:83]\n" \
"ds_read_b64_tr_b16 a[58:59], v215 offset:18944\n" \
"v_mfma_f32_16x16x32_bf16 v[96:99], a[32:35], a[16:19], v[96:99]\n" \
"ds_read_b64_tr_b16 a[60:61], v215 offset:25088\n" \
"v_mfma_f32_16x16x32_bf16 v[96:99], a[36:39], a[20:23], v[96:99]\n" \
"ds_read_b64_tr_b16 a[62:63], v215 offset:27136\n" \
"global_load_lds_dwordx4 v222, s[36:37] offset:2048 sc1\n" \
"v_mfma_f32_16x16x32_bf16 v[100:103], a[40:43], a[16:19], v[100:103]\n" \
"v_add_u32 v213, s74, v211\n" \
"v_add_u32 v214, s74, v212\n" \
"v_mfma_f32_16x16x32_bf16 v[100:103], a[44:47], a[20:23], v[100:103]\n" \
"s_waitcnt lgkmcnt(3)\n" \
"v_xor_b32 v215, 32, v214\n" \
"v_mfma_f32_16x16x32_bf16 v[116:119], a[40:43], a[24:27], v[116:119]\n" \
"v_mfma_f32_16x16x32_bf16 v[116:119], a[44:47], a[28:31], v[116:119]\n" \
"s_waitcnt vmcnt(22) lgkmcnt(0)\n" \
"global_load_lds_dwordx4 v222, s[36:37] offset:3072 sc1\n" \
"v_mfma_f32_16x16x32_bf16 v[112:115], a[32:35], a[24:27], v[112:115]\n" \
"s_add_u32 s36, s36, 0x1000\n" \
"v_mfma_f32_16x16x32_bf16 v[112:115], a[36:39], a[28:31], v[112:115]\n" \
"s_barrier\n" \
"v_mfma_f32_16x16x32_bf16 v[72:75], a[48:51], a[0:3], v[72:75]\n" \
"s_add_u32 m0, m0, 0x5000\n" \
"ds_read_b64_tr_b16 a[32:33], v214 offset:16384\n" \
"v_mfma_f32_16x16x32_bf16 v[72:75], a[52:55], a[4:7], v[72:75]\n" \
"ds_read_b64_tr_b16 a[34:35], v214 offset:18432\n" \
"global_load_lds_dwordx4 v216, s[38:39] offset:-4096\n" \
"v_mfma_f32_16x16x32_bf16 v[76:79], a[56:59], a[0:3], v[76:79]\n" \
"ds_read_b64_tr_b16 a[36:37], v214 offset:24576\n" \
"v_mfma_f32_16x16x32_bf16 v[76:79], a[60:63], a[4:7], v[76:79]\n" \
"ds_read_b128 a[0:3], v213 offset:0\n" \
"v_mfma_f32_16x16x32_bf16 v[92:95], a[56:59], a[8:11], v[92:95]\n" \
"ds_read_b128 a[4:7], v213 offset:2048\n" \
"v_mfma_f32_16x16x32_bf16 v[92:95], a[60:63], a[12:15], v[92:95]\n" \
"ds_read_b64_tr_b16 a[38:39], v214 offset:26624\n" \
"global_load_lds_dwordx4 v217, s[38:39] offset:-3072\n" \
"v_mfma_f32_16x16x32_bf16 v[88:91], a[48:51], a[8:11], v[88:91]\n" \
"ds_read_b64_tr_b16 a[40:41], v215 offset:16384\n" \
"v_mfma_f32_16x16x32_bf16 v[88:91], a[52:55], a[12:15], v[88:91]\n" \
"ds_read_b128 a[8:11], v213 offset:256\n" \
"v_mfma_f32_16x16x32_bf16 v[104:107], a[48:51], a[16:19], v[104:107]\n" \
"ds_read_b128 a[12:15], v213 offset:2304\n" \
"v_mfma_f32_16x16x32_bf16 v[104:107], a[52:55], a[20:23], v[104:107]\n" \
"ds_read_b64_tr_b16 a[42:43], v215 offset:18432\n" \
"global_load_lds_dwordx4 v218, s[38:39] offset:-2048\n" \
"v_mfma_f32_16x16x32_bf16 v[108:111], a[56:59], a[16:19], v[108:111]\n" \
"ds_read_b64_tr_b16 a[44:45], v215 offset:24576\n" \
"v_mfma_f32_16x16x32_bf16 v[108:111], a[60:63], a[20:23], v[108:111]\n" \
"ds_read_b128 a[16:19], v213 offset:4096\n" \
"v_mfma_f32_16x16x32_bf16 v[124:127], a[56:59], a[24:27], v[124:127]\n" \
"ds_read_b128 a[20:23], v213 offset:6144\n" \
"global_load_lds_dwordx4 v219, s[38:39] offset:-1024\n" \
"v_mfma_f32_16x16x32_bf16 v[124:127], a[60:63], a[28:31], v[124:127]\n" \
"s_add_u32 s38, s38, 0x20000\n" \
"s_addc_u32 s39, s39, 0\n" \
"ds_read_b64_tr_b16 a[46:47], v215 offset:26624\n" \
"v_mfma_f32_16x16x32_bf16 v[120:123], a[48:51], a[24:27], v[120:123]\n" \
"s_sub_u32 s75, s75, 32768\n" \
"s_cselect_b32 s75, 131072, s75\n" \
"v_mfma_f32_16x16x32_bf16 v[120:123], a[52:55], a[28:31], v[120:123]\n" \
"s_waitcnt lgkmcnt(3)\n" \
"v_mfma_f32_16x16x32_bf16 v[64:67], a[32:35], a[0:3], v[64:67]\n" \
"ds_read_b128 a[24:27], v213 offset:4352\n" \
"s_add_u32 m0, s75, s95\n" \
"v_mfma_f32_16x16x32_bf16 v[64:67], a[36:39], a[4:7], v[64:67]\n" \
"s_waitcnt lgkmcnt(1)\n" \
"ds_read_b128 a[28:31], v213 offset:6400\n" \
"global_load_lds_dwordx4 v222, s[36:37] offset:0 sc1\n" \
"v_mfma_f32_16x16x32_bf16 v[68:71], a[40:43], a[0:3], v[68:71]\n" \
"s_sub_u32 s74, s74, 32768\n" \
"s_cselect_b32 s74, 131072, s74\n" \
"ds_read_b64_tr_b16 a[48:49], v214 offset:16896\n" \
"v_mfma_f32_16x16x32_bf16 v[68:71], a[44:47], a[4:7], v[68:71]\n" \
"ds_read_b64_tr_b16 a[50:51], v214 offset:18944\n" \
"v_mfma_f32_16x16x32_bf16 v[84:87], a[40:43], a[8:11], v[84:87]\n" \
"ds_read_b64_tr_b16 a[52:53], v214 offset:25088\n" \
"v_mfma_f32_16x16x32_bf16 v[84:87], a[44:47], a[12:15], v[84:87]\n" \
"ds_read_b64_tr_b16 a[54:55], v214 offset:27136\n" \
"global_load_lds_dwordx4 v222, s[36:37] offset:1024 sc1\n" \
"v_mfma_f32_16x16x32_bf16 v[80:83], a[32:35], a[8:11], v[80:83]\n" \
"ds_read_b64_tr_b16 a[56:57], v215 offset:16896\n" \
"v_mfma_f32_16x16x32_bf16 v[80:83], a[36:39], a[12:15], v[80:83]\n" \
"ds_read_b64_tr_b16 a[58:59], v215 offset:18944\n" \
"v_mfma_f32_16x16x32_bf16 v[96:99], a[32:35], a[16:19], v[96:99]\n" \
"ds_read_b64_tr_b16 a[60:61], v215 offset:25088\n" \
"v_mfma_f32_16x16x32_bf16 v[96:99], a[36:39], a[20:23], v[96:99]\n" \
"ds_read_b64_tr_b16 a[62:63], v215 offset:27136\n" \
"global_load_lds_dwordx4 v222, s[36:37] offset:2048 sc1\n" \
"v_mfma_f32_16x16x32_bf16 v[100:103], a[40:43], a[16:19], v[100:103]\n" \
"v_add_u32 v213, s74, v211\n" \
"v_add_u32 v214, s74, v212\n" \
"v_mfma_f32_16x16x32_bf16 v[100:103], a[44:47], a[20:23], v[100:103]\n" \
"s_waitcnt lgkmcnt(3)\n" \
"v_xor_b32 v215, 32, v214\n" \
"v_mfma_f32_16x16x32_bf16 v[116:119], a[40:43], a[24:27], v[116:119]\n" \
"v_mfma_f32_16x16x32_bf16 v[116:119], a[44:47], a[28:31], v[116:119]\n" \
"s_waitcnt vmcnt(19) lgkmcnt(0)\n" \
"global_load_lds_dwordx4 v222, s[36:37] offset:3072 sc1\n" \
"v_mfma_f32_16x16x32_bf16 v[112:115], a[32:35], a[24:27], v[112:115]\n" \
"s_add_u32 s36, s36, 0x1000\n" \
"v_mfma_f32_16x16x32_bf16 v[112:115], a[36:39], a[28:31], v[112:115]\n" \
"s_barrier\n" \
"v_mfma_f32_16x16x32_bf16 v[72:75], a[48:51], a[0:3], v[72:75]\n" \
"s_add_u32 m0, m0, 0x5000\n" \
"ds_read_b64_tr_b16 a[32:33], v214 offset:16384\n" \
"v_mfma_f32_16x16x32_bf16 v[72:75], a[52:55], a[4:7], v[72:75]\n" \
"ds_read_b64_tr_b16 a[34:35], v214 offset:18432\n" \
"global_load_lds_dwordx4 v216, s[38:39] offset:-4096\n" \
"v_mfma_f32_16x16x32_bf16 v[76:79], a[56:59], a[0:3], v[76:79]\n" \
"ds_read_b64_tr_b16 a[36:37], v214 offset:24576\n" \
"v_mfma_f32_16x16x32_bf16 v[76:79], a[60:63], a[4:7], v[76:79]\n" \
"buffer_store_dword v224, v225, s[56:59], s79 offen sc1\n" \
"ds_read_b128 a[0:3], v213 offset:0\n" \
"v_mfma_f32_16x16x32_bf16 v[92:95], a[56:59], a[8:11], v[92:95]\n" \
"ds_read_b128 a[4:7], v213 offset:2048\n" \
"v_mfma_f32_16x16x32_bf16 v[92:95], a[60:63], a[12:15], v[92:95]\n" \
"ds_read_b64_tr_b16 a[38:39], v214 offset:26624\n" \
"global_load_lds_dwordx4 v217, s[38:39] offset:-3072\n" \
"v_mfma_f32_16x16x32_bf16 v[88:91], a[48:51], a[8:11], v[88:91]\n" \
"ds_read_b64_tr_b16 a[40:41], v215 offset:16384\n" \
"v_mfma_f32_16x16x32_bf16 v[88:91], a[52:55], a[12:15], v[88:91]\n" \
"ds_read_b128 a[8:11], v213 offset:256\n" \
"v_mfma_f32_16x16x32_bf16 v[104:107], a[48:51], a[16:19], v[104:107]\n" \
"ds_read_b128 a[12:15], v213 offset:2304\n" \
"v_mfma_f32_16x16x32_bf16 v[104:107], a[52:55], a[20:23], v[104:107]\n" \
"ds_read_b64_tr_b16 a[42:43], v215 offset:18432\n" \
"global_load_lds_dwordx4 v218, s[38:39] offset:-2048\n" \
"v_mfma_f32_16x16x32_bf16 v[108:111], a[56:59], a[16:19], v[108:111]\n" \
"ds_read_b64_tr_b16 a[44:45], v215 offset:24576\n" \
"v_mfma_f32_16x16x32_bf16 v[108:111], a[60:63], a[20:23], v[108:111]\n" \
"ds_read_b128 a[16:19], v213 offset:4096\n" \
"v_mfma_f32_16x16x32_bf16 v[124:127], a[56:59], a[24:27], v[124:127]\n" \
"ds_read_b128 a[20:23], v213 offset:6144\n" \
"global_load_lds_dwordx4 v219, s[38:39] offset:-1024\n" \
"v_mfma_f32_16x16x32_bf16 v[124:127], a[60:63], a[28:31], v[124:127]\n" \
"s_add_u32 s38, s38, 0x20000\n" \
"s_addc_u32 s39, s39, 0\n" \
"ds_read_b64_tr_b16 a[46:47], v215 offset:26624\n" \
"v_mfma_f32_16x16x32_bf16 v[120:123], a[48:51], a[24:27], v[120:123]\n" \
"s_sub_u32 s75, s75, 32768\n" \
"s_cselect_b32 s75, 131072, s75\n" \
"v_mfma_f32_16x16x32_bf16 v[120:123], a[52:55], a[28:31], v[120:123]\n" \
"s_waitcnt lgkmcnt(3)\n" \
"s_mov_b32 s72, 6\n" \
"s_call_b64 s[68:69], Lo64_%=\n" \
"v_mfma_f32_16x16x32_bf16 v[64:67], a[32:35], a[0:3], v[64:67]\n" \
"ds_read_b128 a[24:27], v213 offset:4352\n" \
"v_mfma_f32_16x16x32_bf16 v[64:67], a[36:39], a[4:7], v[64:67]\n" \
"s_waitcnt lgkmcnt(1)\n" \
"ds_read_b128 a[28:31], v213 offset:6400\n" \
"v_mfma_f32_16x16x32_bf16 v[68:71], a[40:43], a[0:3], v[68:71]\n" \
"s_sub_u32 s74, s74, 32768\n" \
"s_cselect_b32 s74, 131072, s74\n" \
"ds_read_b64_tr_b16 a[48:49], v214 offset:16896\n" \
"v_mfma_f32_16x16x32_bf16 v[68:71], a[44:47], a[4:7], v[68:71]\n" \
"ds_read_b64_tr_b16 a[50:51], v214 offset:18944\n" \
"v_mfma_f32_16x16x32_bf16 v[84:87], a[40:43], a[8:11], v[84:87]\n" \
"ds_read_b64_tr_b16 a[52:53], v214 offset:25088\n" \
"v_mfma_f32_16x16x32_bf16 v[84:87], a[44:47], a[12:15], v[84:87]\n" \
"ds_read_b64_tr_b16 a[54:55], v214 offset:27136\n" \
"v_mfma_f32_16x16x32_bf16 v[80:83], a[32:35], a[8:11], v[80:83]\n" \
"ds_read_b64_tr_b16 a[56:57], v215 offset:16896\n" \
"v_mfma_f32_16x16x32_bf16 v[80:83], a[36:39], a[12:15], v[80:83]\n" \
"ds_read_b64_tr_b16 a[58:59], v215 offset:18944\n" \
"v_mfma_f32_16x16x32_bf16 v[96:99], a[32:35], a[16:19], v[96:99]\n" \
"ds_read_b64_tr_b16 a[60:61], v215 offset:25088\n" \
"v_mfma_f32_16x16x32_bf16 v[96:99], a[36:39], a[20:23], v[96:99]\n" \
"ds_read_b64_tr_b16 a[62:63], v215 offset:27136\n" \
"v_mfma_f32_16x16x32_bf16 v[100:103], a[40:43], a[16:19], v[100:103]\n" \
"v_add_u32 v213, s74, v211\n" \
"v_add_u32 v214, s74, v212\n" \
"v_mfma_f32_16x16x32_bf16 v[100:103], a[44:47], a[20:23], v[100:103]\n" \
"s_waitcnt lgkmcnt(3)\n" \
"v_xor_b32 v215, 32, v214\n" \
"v_mfma_f32_16x16x32_bf16 v[116:119], a[40:43], a[24:27], v[116:119]\n" \
"v_mfma_f32_16x16x32_bf16 v[116:119], a[44:47], a[28:31], v[116:119]\n" \
"s_waitcnt vmcnt(16) lgkmcnt(0)\n" \
"v_mfma_f32_16x16x32_bf16 v[112:115], a[32:35], a[24:27], v[112:115]\n" \
"v_mfma_f32_16x16x32_bf16 v[112:115], a[36:39], a[28:31], v[112:115]\n" \
"s_barrier\n" \
"v_mfma_f32_16x16x32_bf16 v[72:75], a[48:51], a[0:3], v[72:75]\n" \
"s_lshl_b32 s83, s95, 1\n" \
"s_add_u32 s83, s83, s74\n" \
"ds_read_b64_tr_b16 a[32:33], v214 offset:16384\n" \
"v_mfma_f32_16x16x32_bf16 v[72:75], a[52:55], a[4:7], v[72:75]\n" \
"ds_read_b64_tr_b16 a[34:35], v214 offset:18432\n" \
"v_mfma_f32_16x16x32_bf16 v[76:79], a[56:59], a[0:3], v[76:79]\n" \
"ds_read_b64_tr_b16 a[36:37], v214 offset:24576\n" \
"v_mfma_f32_16x16x32_bf16 v[76:79], a[60:63], a[4:7], v[76:79]\n" \
"ds_read_b128 a[0:3], v213 offset:0\n" \
"v_mfma_f32_16x16x32_bf16 v[92:95], a[56:59], a[8:11], v[92:95]\n" \
"ds_read_b128 a[4:7], v213 offset:2048\n" \
"v_mfma_f32_16x16x32_bf16 v[92:95], a[60:63], a[12:15], v[92:95]\n" \
"ds_read_b64_tr_b16 a[38:39], v214 offset:26624\n" \
"v_mfma_f32_16x16x32_bf16 v[88:91], a[48:51], a[8:11], v[88:91]\n" \
"ds_read_b64_tr_b16 a[40:41], v215 offset:16384\n" \
"v_mfma_f32_16x16x32_bf16 v[88:91], a[52:55], a[12:15], v[88:91]\n" \
"ds_read_b128 a[8:11], v213 offset:256\n" \
"v_mfma_f32_16x16x32_bf16 v[104:107], a[48:51], a[16:19], v[104:107]\n" \
"ds_read_b128 a[12:15], v213 offset:2304\n" \
"v_mfma_f32_16x16x32_bf16 v[104:107], a[52:55], a[20:23], v[104:107]\n" \
"ds_read_b64_tr_b16 a[42:43], v215 offset:18432\n" \
"v_mfma_f32_16x16x32_bf16 v[108:111], a[56:59], a[16:19], v[108:111]\n" \
"ds_read_b64_tr_b16 a[44:45], v215 offset:24576\n" \
"v_mfma_f32_16x16x32_bf16 v[108:111], a[60:63], a[20:23], v[108:111]\n" \
"ds_read_b128 a[16:19], v213 offset:4096\n" \
"v_mfma_f32_16x16x32_bf16 v[124:127], a[56:59], a[24:27], v[124:127]\n" \
"ds_read_b128 a[20:23], v213 offset:6144\n" \
"v_mfma_f32_16x16x32_bf16 v[124:127], a[60:63], a[28:31], v[124:127]\n" \
"ds_read_b64_tr_b16 a[46:47], v215 offset:26624\n" \
"v_mfma_f32_16x16x32_bf16 v[120:123], a[48:51], a[24:27], v[120:123]\n" \
"v_mfma_f32_16x16x32_bf16 v[120:123], a[52:55], a[28:31], v[120:123]\n" \
"s_waitcnt lgkmcnt(3)\n" \
"v_mfma_f32_16x16x32_bf16 v[64:67], a[32:35], a[0:3], v[64:67]\n" \
"ds_read_b128 a[24:27], v213 offset:4352\n" \
"v_mfma_f32_16x16x32_bf16 v[64:67], a[36:39], a[4:7], v[64:67]\n" \
"s_waitcnt lgkmcnt(1)\n" \
"ds_read_b128 a[28:31], v213 offset:6400\n" \
"v_lshrrev_b32 v132, 4, v248\n" \
"v_mfma_f32_16x16x32_bf16 v[68:71], a[40:43], a[0:3], v[68:71]\n" \
"s_sub_u32 s74, s74, 32768\n" \
"s_cselect_b32 s74, 131072, s74\n" \
"ds_read_b64_tr_b16 a[48:49], v214 offset:16896\n" \
"v_mfma_f32_16x16x32_bf16 v[68:71], a[44:47], a[4:7], v[68:71]\n" \
"ds_read_b64_tr_b16 a[50:51], v214 offset:18944\n" \
"v_and_b32 v133, 15, v132\n" \
"v_mfma_f32_16x16x32_bf16 v[84:87], a[40:43], a[8:11], v[84:87]\n" \
"ds_read_b64_tr_b16 a[52:53], v214 offset:25088\n" \
"v_mul_u32_u24 v128, 144, v133\n" \
"v_mfma_f32_16x16x32_bf16 v[84:87], a[44:47], a[12:15], v[84:87]\n" \
"ds_read_b64_tr_b16 a[54:55], v214 offset:27136\n" \
"v_lshrrev_b32 v133, 4, v132\n" \
"v_mfma_f32_16x16x32_bf16 v[80:83], a[32:35], a[8:11], v[80:83]\n" \
"ds_read_b64_tr_b16 a[56:57], v215 offset:16896\n" \
"v_lshl_add_u32 v128, v133, 3, v128\n" \
"v_mfma_f32_16x16x32_bf16 v[80:83], a[36:39], a[12:15], v[80:83]\n" \
"ds_read_b64_tr_b16 a[58:59], v215 offset:18944\n" \
"v_add_u32 v128, s83, v128\n" \
"v_mfma_f32_16x16x32_bf16 v[96:99], a[32:35], a[16:19], v[96:99]\n" \
"ds_read_b64_tr_b16 a[60:61], v215 offset:25088\n" \
"v_lshrrev_b32 v133, 3, v132\n" \
"v_mfma_f32_16x16x32_bf16 v[96:99], a[36:39], a[20:23], v[96:99]\n" \
"ds_read_b64_tr_b16 a[62:63], v215 offset:27136\n" \
"v_and_b32 v134, 7, v132\n" \
"v_mfma_f32_16x16x32_bf16 v[100:103], a[40:43], a[16:19], v[100:103]\n" \
"v_add_u32 v213, s74, v211\n" \
"v_add_u32 v214, s74, v212\n" \
"v_mfma_f32_16x16x32_bf16 v[100:103], a[44:47], a[20:23], v[100:103]\n" \
"s_waitcnt lgkmcnt(3)\n" \
"v_xor_b32 v215, 32, v214\n" \
"v_mul_u32_u24 v129, 144, v133\n" \
"v_mfma_f32_16x16x32_bf16 v[116:119], a[40:43], a[24:27], v[116:119]\n" \
"v_lshl_add_u32 v129, v134, 4, v129\n" \
"v_add_u32 v129, s83, v129\n" \
"v_mfma_f32_16x16x32_bf16 v[116:119], a[44:47], a[28:31], v[116:119]\n" \
"s_waitcnt vmcnt(0) lgkmcnt(0)\n" \
"v_lshlrev_b32 v134, 4, v134\n" \
"v_lshl_add_u32 v130, v133, 13, v134\n" \
"v_mfma_f32_16x16x32_bf16 v[112:115], a[32:35], a[24:27], v[112:115]\n" \
"v_mfma_f32_16x16x32_bf16 v[112:115], a[36:39], a[28:31], v[112:115]\n" \
"s_barrier\n" \
"v_mfma_f32_16x16x32_bf16 v[72:75], a[48:51], a[0:3], v[72:75]\n" \
"ds_read_b128 a[192:195], v213 offset:0\n" \
"ds_read_b64_tr_b16 a[128:129], v214 offset:16384\n" \
"v_mfma_f32_16x16x32_bf16 v[72:75], a[52:55], a[4:7], v[72:75]\n" \
"s_sub_u32 s74, s74, 32768\n" \
"s_cselect_b32 s74, 131072, s74\n" \
"ds_read_b64_tr_b16 a[130:131], v214 offset:18432\n" \
"v_mfma_f32_16x16x32_bf16 v[76:79], a[56:59], a[0:3], v[76:79]\n" \
"ds_read_b128 a[196:199], v213 offset:2048\n" \
"ds_read_b64_tr_b16 a[132:133], v214 offset:24576\n" \
"v_mfma_f32_16x16x32_bf16 v[76:79], a[60:63], a[4:7], v[76:79]\n" \
"ds_read_b64_tr_b16 a[134:135], v214 offset:26624\n" \
"v_add_u32 v226, s74, v211\n" \
"v_mfma_f32_16x16x32_bf16 v[92:95], a[56:59], a[8:11], v[92:95]\n" \
"ds_read_b128 a[200:203], v213 offset:256\n" \
"ds_read_b64_tr_b16 a[136:137], v215 offset:16384\n" \
"v_mfma_f32_16x16x32_bf16 v[92:95], a[60:63], a[12:15], v[92:95]\n" \
"ds_read_b64_tr_b16 a[138:139], v215 offset:18432\n" \
"v_add_u32 v227, s74, v212\n" \
"v_mfma_f32_16x16x32_bf16 v[88:91], a[48:51], a[8:11], v[88:91]\n" \
"ds_read_b128 a[204:207], v213 offset:2304\n" \
"ds_read_b64_tr_b16 a[140:141], v215 offset:24576\n" \
"v_mfma_f32_16x16x32_bf16 v[88:91], a[52:55], a[12:15], v[88:91]\n" \
"ds_read_b64_tr_b16 a[142:143], v215 offset:26624\n" \
"v_xor_b32 v228, 32, v227\n" \
"v_mfma_f32_16x16x32_bf16 v[104:107], a[48:51], a[16:19], v[104:107]\n" \
"ds_read_b64_tr_b16 a[144:145], v214 offset:16896\n" \
"v_mfma_f32_16x16x32_bf16 v[104:107], a[52:55], a[20:23], v[104:107]\n" \
"ds_read_b64_tr_b16 a[146:147], v214 offset:18944\n" \
"v_mfma_f32_16x16x32_bf16 v[108:111], a[56:59], a[16:19], v[108:111]\n" \
"ds_read_b64_tr_b16 a[148:149], v214 offset:25088\n" \
"v_mfma_f32_16x16x32_bf16 v[108:111], a[60:63], a[20:23], v[108:111]\n" \
"ds_read_b64_tr_b16 a[150:151], v214 offset:27136\n" \
"v_mfma_f32_16x16x32_bf16 v[124:127], a[56:59], a[24:27], v[124:127]\n" \
"ds_read_b64_tr_b16 a[152:153], v215 offset:16896\n" \
"v_mfma_f32_16x16x32_bf16 v[124:127], a[60:63], a[28:31], v[124:127]\n" \
"ds_read_b64_tr_b16 a[154:155], v215 offset:18944\n" \
"v_mfma_f32_16x16x32_bf16 v[120:123], a[48:51], a[24:27], v[120:123]\n" \
"ds_read_b64_tr_b16 a[156:157], v215 offset:25088\n" \
"v_mfma_f32_16x16x32_bf16 v[120:123], a[52:55], a[28:31], v[120:123]\n" \
"s_waitcnt lgkmcnt(4)\n" \
"ds_read_b64_tr_b16 a[158:159], v215 offset:27136\n" \
"v_mfma_f32_16x16x32_bf16 v[64:67], a[128:131], a[192:195], v[64:67]\n" \
"ds_read_b128 a[224:227], v226 offset:0\n" \
"ds_read_b64_tr_b16 a[160:161], v227 offset:16384\n" \
"v_mfma_f32_16x16x32_bf16 v[64:67], a[132:135], a[196:199], v[64:67]\n" \
"ds_read_b64_tr_b16 a[162:163], v227 offset:18432\n" \
"v_mfma_f32_16x16x32_bf16 v[80:83], a[128:131], a[200:203], v[80:83]\n" \
"ds_read_b64_tr_b16 a[164:165], v227 offset:24576\n" \
"v_mfma_f32_16x16x32_bf16 v[80:83], a[132:135], a[204:207], v[80:83]\n" \
"ds_read_b128 a[228:231], v226 offset:2048\n" \
"ds_read_b64_tr_b16 a[166:167], v227 offset:26624\n" \
"v_mfma_f32_16x16x32_bf16 v[84:87], a[136:139], a[200:203], v[84:87]\n" \
"ds_read_b64_tr_b16 a[168:169], v228 offset:16384\n" \
"v_mfma_f32_16x16x32_bf16 v[84:87], a[140:143], a[204:207], v[84:87]\n" \
"ds_read_b64_tr_b16 a[170:171], v228 offset:18432\n" \
"v_mfma_f32_16x16x32_bf16 v[68:71], a[136:139], a[192:195], v[68:71]\n" \
"ds_read_b128 a[232:235], v226 offset:256\n" \
"ds_read_b64_tr_b16 a[172:173], v228 offset:24576\n" \
"v_mfma_f32_16x16x32_bf16 v[68:71], a[140:143], a[196:199], v[68:71]\n" \
"s_waitcnt lgkmcnt(6)\n" \
"ds_read_b64_tr_b16 a[174:175], v228 offset:26624\n" \
"v_mfma_f32_16x16x32_bf16 v[72:75], a[144:147], a[192:195], v[72:75]\n" \
"ds_read_b64_tr_b16 a[176:177], v227 offset:16896\n" \
"v_mfma_f32_16x16x32_bf16 v[72:75], a[148:151], a[196:199], v[72:75]\n" \
"ds_read_b128 a[236:239], v226 offset:2304\n" \
"ds_read_b64_tr_b16 a[178:179], v227 offset:18944\n" \
"v_mfma_f32_16x16x32_bf16 v[88:91], a[144:147], a[200:203], v[88:91]\n" \
"ds_read_b64_tr_b16 a[180:181], v227 offset:25088\n" \
"v_mfma_f32_16x16x32_bf16 v[88:91], a[148:151], a[204:207], v[88:91]\n" \
"ds_read_b64_tr_b16 a[182:183], v227 offset:27136\n" \
"v_mfma_f32_16x16x32_bf16 v[92:95], a[152:155], a[200:203], v[92:95]\n" \
"ds_read_b64_tr_b16 a[184:185], v228 offset:16896\n" \
"v_mfma_f32_16x16x32_bf16 v[92:95], a[156:159], a[204:207], v[92:95]\n" \
"ds_read_b64_tr_b16 a[186:187], v228 offset:18944\n" \
"v_mfma_f32_16x16x32_bf16 v[76:79], a[152:155], a[192:195], v[76:79]\n" \
"ds_read_b64_tr_b16 a[188:189], v228 offset:25088\n" \
"v_mfma_f32_16x16x32_bf16 v[76:79], a[156:159], a[196:199], v[76:79]\n" \
"s_waitcnt lgkmcnt(4)\n" \
"ds_read_b64_tr_b16 a[190:191], v228 offset:27136\n" \
"v_mfma_f32_16x16x32_bf16 v[64:67], a[160:163], a[224:227], v[64:67]\n" \
"ds_read_b128 a[208:211], v213 offset:4096\n" \
"v_mfma_f32_16x16x32_bf16 v[64:67], a[164:167], a[228:231], v[64:67]\n" \
"ds_read_b128 a[212:215], v213 offset:6144\n" \
"v_mfma_f32_16x16x32_bf16 v[80:83], a[160:163], a[232:235], v[80:83]\n" \
"ds_read_b128 a[240:243], v226 offset:4096\n" \
"v_mfma_f32_16x16x32_bf16 v[80:83], a[164:167], a[236:239], v[80:83]\n" \
"ds_read_b128 a[244:247], v226 offset:6144\n" \
"v_mfma_f32_16x16x32_bf16 v[84:87], a[168:171], a[232:235], v[84:87]\n" \
"ds_read_b128 a[216:219], v213 offset:4352\n" \
"v_cvt_pk_bf16_f32 v64, v64, v65\n" \
"v_mfma_f32_16x16x32_bf16 v[84:87], a[172:175], a[236:239], v[84:87]\n" \
"ds_read_b128 a[220:223], v213 offset:6400\n" \
"v_cvt_pk_bf16_f32 v65, v66, v67\n" \
"v_mfma_f32_16x16x32_bf16 v[68:71], a[168:171], a[224:227], v[68:71]\n" \
"ds_read_b128 a[248:251], v226 offset:4352\n" \
"v_mfma_f32_16x16x32_bf16 v[68:71], a[172:175], a[228:231], v[68:71]\n" \
"s_waitcnt lgkmcnt(4)\n" \
"ds_read_b128 a[252:255], v226 offset:6400\n" \
"v_mfma_f32_16x16x32_bf16 v[72:75], a[176:179], a[224:227], v[72:75]\n" \
"ds_write_b64 v128, v[64:65] offset:0\n" \
"v_mfma_f32_16x16x32_bf16 v[72:75], a[180:183], a[228:231], v[72:75]\n" \
"v_cvt_pk_bf16_f32 v80, v80, v81\n" \
"v_cvt_pk_bf16_f32 v81, v82, v83\n" \
"v_mfma_f32_16x16x32_bf16 v[88:91], a[176:179], a[232:235], v[88:91]\n" \
"ds_write_b64 v128, v[80:81] offset:2304\n" \
"v_mfma_f32_16x16x32_bf16 v[88:91], a[180:183], a[236:239], v[88:91]\n" \
"v_cvt_pk_bf16_f32 v84, v84, v85\n" \
"v_cvt_pk_bf16_f32 v85, v86, v87\n" \
"v_mfma_f32_16x16x32_bf16 v[92:95], a[184:187], a[232:235], v[92:95]\n" \
"ds_write_b64 v128, v[84:85] offset:2336\n" \
"v_mfma_f32_16x16x32_bf16 v[92:95], a[188:191], a[236:239], v[92:95]\n" \
"v_cvt_pk_bf16_f32 v68, v68, v69\n" \
"v_cvt_pk_bf16_f32 v69, v70, v71\n" \
"v_mfma_f32_16x16x32_bf16 v[76:79], a[184:187], a[224:227], v[76:79]\n" \
"ds_write_b64 v128, v[68:69] offset:32\n" \
"v_mfma_f32_16x16x32_bf16 v[76:79], a[188:191], a[228:231], v[76:79]\n" \
"v_cvt_pk_bf16_f32 v72, v72, v73\n" \
"v_cvt_pk_bf16_f32 v73, v74, v75\n" \
"v_mfma_f32_16x16x32_bf16 v[96:99], a[128:131], a[208:211], v[96:99]\n" \
"ds_write_b64 v128, v[72:73] offset:64\n" \
"v_mfma_f32_16x16x32_bf16 v[96:99], a[132:135], a[212:215], v[96:99]\n" \
"s_waitcnt lgkmcnt(2)\n" \
"v_cvt_pk_bf16_f32 v88, v88, v89\n" \
"v_cvt_pk_bf16_f32 v89, v90, v91\n" \
"v_mfma_f32_16x16x32_bf16 v[96:99], a[160:163], a[240:243], v[96:99]\n" \
"ds_write_b64 v128, v[88:89] offset:2368\n" \
"v_mfma_f32_16x16x32_bf16 v[96:99], a[164:167], a[244:247], v[96:99]\n" \
"v_cvt_pk_bf16_f32 v92, v92, v93\n" \
"v_cvt_pk_bf16_f32 v93, v94, v95\n" \
"v_mfma_f32_16x16x32_bf16 v[100:103], a[168:171], a[240:243], v[100:103]\n" \
"ds_write_b64 v128, v[92:93] offset:2400\n" \
"v_mfma_f32_16x16x32_bf16 v[100:103], a[172:175], a[244:247], v[100:103]\n" \
"v_cvt_pk_bf16_f32 v76, v76, v77\n" \
"v_cvt_pk_bf16_f32 v77, v78, v79\n" \
"v_mfma_f32_16x16x32_bf16 v[100:103], a[136:139], a[208:211], v[100:103]\n" \
"ds_write_b64 v128, v[76:77] offset:96\n" \
"v_mfma_f32_16x16x32_bf16 v[100:103], a[140:143], a[212:215], v[100:103]\n" \
"ds_read_b128 v[140:143], v129 offset:0\n" \
"v_mfma_f32_16x16x32_bf16 v[104:107], a[144:147], a[208:211], v[104:107]\n" \
"ds_read_b128 v[144:147], v129 offset:1152\n" \
"v_mfma_f32_16x16x32_bf16 v[104:107], a[148:151], a[212:215], v[104:107]\n" \
"ds_read_b128 v[148:151], v129 offset:2304\n" \
"v_mfma_f32_16x16x32_bf16 v[104:107], a[176:179], a[240:243], v[104:107]\n" \
"ds_read_b128 v[152:155], v129 offset:3456\n" \
"v_mfma_f32_16x16x32_bf16 v[104:107], a[180:183], a[244:247], v[104:107]\n" \
"v_cvt_pk_bf16_f32 v96, v96, v97\n" \
"v_cvt_pk_bf16_f32 v97, v98, v99\n" \
"v_mfma_f32_16x16x32_bf16 v[108:111], a[184:187], a[240:243], v[108:111]\n" \
"ds_write_b64 v128, v[96:97] offset:0\n" \
"v_mfma_f32_16x16x32_bf16 v[108:111], a[188:191], a[244:247], v[108:111]\n" \
"v_cvt_pk_bf16_f32 v100, v100, v101\n" \
"v_cvt_pk_bf16_f32 v101, v102, v103\n" \
"v_mfma_f32_16x16x32_bf16 v[108:111], a[152:155], a[208:211], v[108:111]\n" \
"Lpg13123_%=:\n" \
"s_cbranch_execz Lcb1_%=\n" \
"s_add_u32 s70, s77, 0\n" \
"ds_write_b64 v128, v[100:101] offset:32\n" \
"v_mfma_f32_16x16x32_bf16 v[108:111], a[156:159], a[212:215], v[108:111]\n" \
"s_waitcnt lgkmcnt(5)\n" \
"v_cvt_pk_bf16_f32 v104, v104, v105\n" \
"v_cvt_pk_bf16_f32 v105, v106, v107\n" \
"v_mfma_f32_16x16x32_bf16 v[112:115], a[128:131], a[216:219], v[112:115]\n" \
"buffer_store_dwordx4 v[140:143], v130, s[52:55], s70 offen sc1\n" \
"ds_write_b64 v128, v[104:105] offset:64\n" \
"v_mfma_f32_16x16x32_bf16 v[112:115], a[132:135], a[220:223], v[112:115]\n" \
"v_mfma_f32_16x16x32_bf16 v[112:115], a[160:163], a[248:251], v[112:115]\n" \
"v_cvt_pk_bf16_f32 v108, v108, v109\n" \
"v_cvt_pk_bf16_f32 v109, v110, v111\n" \
"v_mfma_f32_16x16x32_bf16 v[112:115], a[164:167], a[252:255], v[112:115]\n" \
"ds_write_b64 v128, v[108:109] offset:96\n" \
"v_mfma_f32_16x16x32_bf16 v[116:119], a[168:171], a[248:251], v[116:119]\n" \
"s_add_u32 s70, s77, 65536\n" \
"ds_read_b128 v[156:159], v129 offset:0\n" \
"s_waitcnt lgkmcnt(7)\n" \
"v_mfma_f32_16x16x32_bf16 v[116:119], a[172:175], a[252:255], v[116:119]\n" \
"buffer_store_dwordx4 v[144:147], v130, s[52:55], s70 offen sc1\n" \
"ds_read_b128 v[160:163], v129 offset:1152\n" \
"v_mfma_f32_16x16x32_bf16 v[116:119], a[136:139], a[216:219], v[116:119]\n" \
"v_cvt_pk_bf16_f32 v112, v112, v113\n" \
"v_cvt_pk_bf16_f32 v113, v114, v115\n" \
"v_mfma_f32_16x16x32_bf16 v[116:119], a[140:143], a[220:223], v[116:119]\n" \
"ds_write_b64 v128, v[112:113] offset:2304\n" \
"v_mfma_f32_16x16x32_bf16 v[120:123], a[144:147], a[216:219], v[120:123]\n" \
"v_mfma_f32_16x16x32_bf16 v[120:123], a[148:151], a[220:223], v[120:123]\n" \
"s_add_u32 s70, s77, 4096\n" \
"s_waitcnt lgkmcnt(8)\n" \
"v_mfma_f32_16x16x32_bf16 v[120:123], a[176:179], a[248:251], v[120:123]\n" \
"buffer_store_dwordx4 v[148:151], v130, s[52:55], s70 offen sc1\n" \
"v_cvt_pk_bf16_f32 v116, v116, v117\n" \
"v_cvt_pk_bf16_f32 v117, v118, v119\n" \
"v_mfma_f32_16x16x32_bf16 v[120:123], a[180:183], a[252:255], v[120:123]\n" \
"ds_write_b64 v128, v[116:117] offset:2336\n" \
"v_mfma_f32_16x16x32_bf16 v[124:127], a[184:187], a[248:251], v[124:127]\n" \
"v_mfma_f32_16x16x32_bf16 v[124:127], a[188:191], a[252:255], v[124:127]\n" \
"v_mfma_f32_16x16x32_bf16 v[124:127], a[152:155], a[216:219], v[124:127]\n" \
"s_add_u32 s70, s77, 69632\n" \
"v_cvt_pk_bf16_f32 v120, v120, v121\n" \
"v_cvt_pk_bf16_f32 v121, v122, v123\n" \
"v_mfma_f32_16x16x32_bf16 v[124:127], a[156:159], a[220:223], v[124:127]\n" \
"s_waitcnt lgkmcnt(8)\n" \
"ds_write_b64 v128, v[120:121] offset:2368\n" \
"buffer_store_dwordx4 v[152:155], v130, s[52:55], s70 offen sc1\n" \
"s_add_u32 s70, s77, 131072\n" \
"s_waitcnt lgkmcnt(4)\n" \
"s_nop 2\n" \
"v_cvt_pk_bf16_f32 v124, v124, v125\n" \
"v_cvt_pk_bf16_f32 v125, v126, v127\n" \
"ds_write_b64 v128, v[124:125] offset:2400\n" \
"ds_read_b128 v[164:167], v129 offset:2304\n" \
"ds_read_b128 v[168:171], v129 offset:3456\n" \
"buffer_store_dwordx4 v[156:159], v130, s[52:55], s70 offen sc1\n" \
"s_add_u32 s70, s77, 196608\n" \
"s_waitcnt lgkmcnt(6)\n" \
"buffer_store_dwordx4 v[160:163], v130, s[52:55], s70 offen sc1\n" \
"s_add_u32 s70, s77, 135168\n" \
"s_waitcnt lgkmcnt(1)\n" \
"buffer_store_dwordx4 v[164:167], v130, s[52:55], s70 offen sc1\n" \
"s_add_u32 s70, s77, 200704\n" \
"s_waitcnt lgkmcnt(0)\n" \
"buffer_store_dwordx4 v[168:171], v130, s[52:55], s70 offen sc1\n" \
"s_endpgm\n" \
"Lpg13190_%=:\n" \
"s_cbranch_execz Lcb1_%=\n" \
"Lo64_%=:\n" \
"v_mfma_f32_16x16x32_bf16 v[64:67], a[32:35], a[0:3], v[64:67]\n" \
"ds_read_b128 a[24:27], v213 offset:4352\n" \
"s_add_u32 m0, s75, s95\n" \
"v_mfma_f32_16x16x32_bf16 v[64:67], a[36:39], a[4:7], v[64:67]\n" \
"s_waitcnt lgkmcnt(1)\n" \
"ds_read_b128 a[28:31], v213 offset:6400\n" \
"global_load_lds_dwordx4 v222, s[36:37] offset:0 sc1\n" \
"v_mfma_f32_16x16x32_bf16 v[68:71], a[40:43], a[0:3], v[68:71]\n" \
"s_sub_u32 s74, s74, 32768\n" \
"s_cselect_b32 s74, 131072, s74\n" \
"ds_read_b64_tr_b16 a[48:49], v214 offset:16896\n" \
"v_mfma_f32_16x16x32_bf16 v[68:71], a[44:47], a[4:7], v[68:71]\n" \
"ds_read_b64_tr_b16 a[50:51], v214 offset:18944\n" \
"v_mfma_f32_16x16x32_bf16 v[84:87], a[40:43], a[8:11], v[84:87]\n" \
"ds_read_b64_tr_b16 a[52:53], v214 offset:25088\n" \
"v_mfma_f32_16x16x32_bf16 v[84:87], a[44:47], a[12:15], v[84:87]\n" \
"ds_read_b64_tr_b16 a[54:55], v214 offset:27136\n" \
"global_load_lds_dwordx4 v222, s[36:37] offset:1024 sc1\n" \
"v_mfma_f32_16x16x32_bf16 v[80:83], a[32:35], a[8:11], v[80:83]\n" \
"ds_read_b64_tr_b16 a[56:57], v215 offset:16896\n" \
"v_mfma_f32_16x16x32_bf16 v[80:83], a[36:39], a[12:15], v[80:83]\n" \
"ds_read_b64_tr_b16 a[58:59], v215 offset:18944\n" \
"v_mfma_f32_16x16x32_bf16 v[96:99], a[32:35], a[16:19], v[96:99]\n" \
"ds_read_b64_tr_b16 a[60:61], v215 offset:25088\n" \
"v_mfma_f32_16x16x32_bf16 v[96:99], a[36:39], a[20:23], v[96:99]\n" \
"ds_read_b64_tr_b16 a[62:63], v215 offset:27136\n" \
"global_load_lds_dwordx4 v222, s[36:37] offset:2048 sc1\n" \
"v_mfma_f32_16x16x32_bf16 v[100:103], a[40:43], a[16:19], v[100:103]\n" \
"v_add_u32 v213, s74, v211\n" \
"v_add_u32 v214, s74, v212\n" \
"v_mfma_f32_16x16x32_bf16 v[100:103], a[44:47], a[20:23], v[100:103]\n" \
"s_waitcnt lgkmcnt(3)\n" \
"v_xor_b32 v215, 32, v214\n" \
"v_mfma_f32_16x16x32_bf16 v[116:119], a[40:43], a[24:27], v[116:119]\n" \
"v_mfma_f32_16x16x32_bf16 v[116:119], a[44:47], a[28:31], v[116:119]\n" \
"s_waitcnt vmcnt(19) lgkmcnt(0)\n" \
"global_load_lds_dwordx4 v222, s[36:37] offset:3072 sc1\n" \
"v_mfma_f32_16x16x32_bf16 v[112:115], a[32:35], a[24:27], v[112:115]\n" \
"s_add_u32 s36, s36, 0x1000\n" \
"v_mfma_f32_16x16x32_bf16 v[112:115], a[36:39], a[28:31], v[112:115]\n" \
"s_barrier\n" \
"v_mfma_f32_16x16x32_bf16 v[72:75], a[48:51], a[0:3], v[72:75]\n" \
"s_add_u32 m0, m0, 0x5000\n" \
"ds_read_b64_tr_b16 a[32:33], v214 offset:16384\n" \
"v_mfma_f32_16x16x32_bf16 v[72:75], a[52:55], a[4:7], v[72:75]\n" \
"ds_read_b64_tr_b16 a[34:35], v214 offset:18432\n" \
"global_load_lds_dwordx4 v216, s[38:39] offset:-4096\n" \
"v_mfma_f32_16x16x32_bf16 v[76:79], a[56:59], a[0:3], v[76:79]\n" \
"ds_read_b64_tr_b16 a[36:37], v214 offset:24576\n" \
"v_mfma_f32_16x16x32_bf16 v[76:79], a[60:63], a[4:7], v[76:79]\n" \
"ds_read_b128 a[0:3], v213 offset:0\n" \
"v_mfma_f32_16x16x32_bf16 v[92:95], a[56:59], a[8:11], v[92:95]\n" \
"ds_read_b128 a[4:7], v213 offset:2048\n" \
"v_mfma_f32_16x16x32_bf16 v[92:95], a[60:63], a[12:15], v[92:95]\n" \
"ds_read_b64_tr_b16 a[38:39], v214 offset:26624\n" \
"global_load_lds_dwordx4 v217, s[38:39] offset:-3072\n" \
"v_mfma_f32_16x16x32_bf16 v[88:91], a[48:51], a[8:11], v[88:91]\n" \
"ds_read_b64_tr_b16 a[40:41], v215 offset:16384\n" \
"v_mfma_f32_16x16x32_bf16 v[88:91], a[52:55], a[12:15], v[88:91]\n" \
"ds_read_b128 a[8:11], v213 offset:256\n" \
"v_mfma_f32_16x16x32_bf16 v[104:107], a[48:51], a[16:19], v[104:107]\n" \
"ds_read_b128 a[12:15], v213 offset:2304\n" \
"v_mfma_f32_16x16x32_bf16 v[104:107], a[52:55], a[20:23], v[104:107]\n" \
"ds_read_b64_tr_b16 a[42:43], v215 offset:18432\n" \
"global_load_lds_dwordx4 v218, s[38:39] offset:-2048\n" \
"v_mfma_f32_16x16x32_bf16 v[108:111], a[56:59], a[16:19], v[108:111]\n" \
"ds_read_b64_tr_b16 a[44:45], v215 offset:24576\n" \
"v_mfma_f32_16x16x32_bf16 v[108:111], a[60:63], a[20:23], v[108:111]\n" \
"ds_read_b128 a[16:19], v213 offset:4096\n" \
"global_load_lds_dwordx4 v219, s[38:39] offset:-1024\n" \
"v_mfma_f32_16x16x32_bf16 v[124:127], a[56:59], a[24:27], v[124:127]\n" \
"s_add_u32 s38, s38, 0x20000\n" \
"s_addc_u32 s39, s39, 0\n" \
"ds_read_b128 a[20:23], v213 offset:6144\n" \
"v_mfma_f32_16x16x32_bf16 v[124:127], a[60:63], a[28:31], v[124:127]\n" \
"s_sub_u32 s75, s75, 32768\n" \
"s_cselect_b32 s75, 131072, s75\n" \
"ds_read_b64_tr_b16 a[46:47], v215 offset:26624\n" \
"v_mfma_f32_16x16x32_bf16 v[120:123], a[48:51], a[24:27], v[120:123]\n" \
"s_sub_u32 s72, s72, 1\n" \
"s_cmp_lg_u32 s72, 0\n" \
"v_mfma_f32_16x16x32_bf16 v[120:123], a[52:55], a[28:31], v[120:123]\n" \
"s_waitcnt lgkmcnt(3)\n" \
"s_cbranch_scc1 Lo64_%=\n" \
"s_setpc_b64 s[68:69]\n" \
"Lhot_end_%=:\n" \
"Lmask0_%=:\n" \
"Lpg13279_%=:\n" \
"s_cbranch_execz Lcb0_%=\n" \
"s_waitcnt lgkmcnt(0)\n" \
"v_subrev_u32 v0, 0, v251\n" \
"v_subrev_u32 v1, 1, v251\n" \
"v_subrev_u32 v2, 2, v251\n" \
"v_subrev_u32 v3, 3, v251\n" \
"v_subrev_u32 v4, 16, v251\n" \
"v_subrev_u32 v5, 17, v251\n" \
"v_subrev_u32 v6, 18, v251\n" \
"v_subrev_u32 v7, 19, v251\n" \
"v_subrev_u32 v32, 32, v251\n" \
"v_subrev_u32 v33, 33, v251\n" \
"v_subrev_u32 v34, 34, v251\n" \
"v_subrev_u32 v35, 35, v251\n" \
"v_subrev_u32 v36, 48, v251\n" \
"v_subrev_u32 v37, 49, v251\n" \
"v_subrev_u32 v38, 50, v251\n" \
"v_subrev_u32 v39, 51, v251\n" \
"v_ashrrev_i32 v0, 31, v0\n" \
"v_ashrrev_i32 v1, 31, v1\n" \
"v_ashrrev_i32 v2, 31, v2\n" \
"v_ashrrev_i32 v3, 31, v3\n" \
"v_ashrrev_i32 v4, 31, v4\n" \
"v_ashrrev_i32 v5, 31, v5\n" \
"v_ashrrev_i32 v6, 31, v6\n" \
"v_ashrrev_i32 v7, 31, v7\n" \
"v_ashrrev_i32 v32, 31, v32\n" \
"v_ashrrev_i32 v33, 31, v33\n" \
"v_ashrrev_i32 v34, 31, v34\n" \
"v_ashrrev_i32 v35, 31, v35\n" \
"v_ashrrev_i32 v36, 31, v36\n" \
"v_ashrrev_i32 v37, 31, v37\n" \
"v_ashrrev_i32 v38, 31, v38\n" \
"v_ashrrev_i32 v39, 31, v39\n" \
"v_and_b32 v0, 0xff800000, v0\n" \
"v_and_b32 v1, 0xff800000, v1\n" \
"v_and_b32 v2, 0xff800000, v2\n" \
"v_and_b32 v3, 0xff800000, v3\n" \
"v_and_b32 v4, 0xff800000, v4\n" \
"v_and_b32 v5, 0xff800000, v5\n" \
"v_and_b32 v6, 0xff800000, v6\n" \
"v_and_b32 v7, 0xff800000, v7\n" \
"v_and_b32 v32, 0xff800000, v32\n" \
"v_and_b32 v33, 0xff800000, v33\n" \
"v_and_b32 v34, 0xff800000, v34\n" \
"v_and_b32 v35, 0xff800000, v35\n" \
"v_and_b32 v36, 0xff800000, v36\n" \
"v_and_b32 v37, 0xff800000, v37\n" \
"v_and_b32 v38, 0xff800000, v38\n" \
"v_and_b32 v39, 0xff800000, v39\n" \
"s_branch Lmask0r_%=\n" \
"Lpl1_%=:\n" \
"s_mov_b32 s31, 0\n" \
"Lpk1_%=:\n" \
"s_sleep 2\n" \
"buffer_load_dword v244, v245, s[56:59], s91 offen sc1\n" \
"s_waitcnt vmcnt(0)\n" \
"v_cmp_ne_u32 vcc, s60, v244\n" \
"s_nop 1\n" \
"s_and_b32 vcc_hi, vcc_lo, 255\n" \
"s_cbranch_scc0 Lpr1_%=\n" \
"s_add_u32 s31, s31, 1\n" \
"s_cmp_lt_u32 s31, 0x40000\n" \
"s_cbranch_scc1 Lpk1_%=\n" \
"s_branch Lpr1_%=\n" \
"Lrs0_%=:\n" \
"s_call_b64 s[68:69], Lrest_%=\n" \
"s_branch Lrr0_%=\n" \
"Lrest_%=:\n" \
"s_mov_b32 s31, 0\n" \
"s_waitcnt vmcnt(0)\n" \
"Lrk_%=:\n" \
"v_lshrrev_b32 v253, 2, v248\n" \
"s_sleep 2\n" \
"buffer_load_dword v253, v253, s[56:59], s79 offen sc1\n" \
"s_waitcnt vmcnt(0)\n" \
"v_cmp_ne_u32 s[52:53], s60, v253\n" \
"s_add_u32 s31, s31, 1\n" \
"s_cmp_lt_u32 s31, 0x40000\n" \
"s_cselect_b32 s54, 0xff, 0\n" \
"s_and_b32 s52, s52, s54\n" \
"s_cmp_lg_u32 s52, 0\n" \
"s_cbranch_scc1 Lrk_%=\n" \
"s_mov_b64 s[10:11], 0\n" \
"s_setpc_b64 s[68:69]\n" \
"Lrs1_%=:\n" \
"s_call_b64 s[68:69], Lrest_%=\n" \
"s_branch Lrr1_%=\n" \
"Lrs2_%=:\n" \
"s_call_b64 s[68:69], Lrest_%=\n" \
"s_branch Lrr2_%=\n" \
"Lrs3_%=:\n" \
"s_call_b64 s[68:69], Lrest_%=\n" \
"s_branch Lrr3_%=\n" \
"LF7_%=:\n" \
"v_mfma_f32_16x16x32_bf16 v[24:27], a[192:195], a[176:179], v[236:239]\n" \
"s_waitcnt vmcnt(13)\n" \
"ds_read_b128 v[192:195], v246 offset:16384\n" \
"v_exp_f32 v104, v104\n" \
"v_exp_f32 v105, v105\n" \
"v_exp_f32 v106, v106\n" \
"v_mfma_f32_16x16x32_bf16 v[24:27], a[196:199], a[180:183], v[24:27]\n" \
"s_add_u32 s70, s63, 2\n" \
"ds_read_b128 v[196:199], v246 offset:18432\n" \
"v_cvt_pk_bf16_f32 v91, v94, v95\n" \
"v_mfma_f32_16x16x32_bf16 v[24:27], a[200:203], a[184:187], v[24:27]\n" \
"s_and_b32 s88, s70, 3\n" \
"ds_read_b128 v[200:203], v246 offset:20480\n" \
"v_cvt_pk_bf16_f32 v104, v104, v105\n" \
"v_mfma_f32_16x16x32_bf16 v[24:27], a[204:207], a[188:191], v[24:27]\n" \
"s_lshr_b32 s71, %1, 6\n" \
"ds_read_b128 a[252:255], v246 offset:14592\n" \
"v_mov_b32 v66, v242\n" \
"v_mov_b32 v64, v66\n" \
"v_exp_f32 v107, v107\n" \
"v_mfma_f32_16x16x32_bf16 v[16:19], a[200:203], a[168:171], v[232:235]\n" \
"s_lshl_b32 s71, s71, 2\n" \
"ds_read_b128 v[204:207], v246 offset:22528\n" \
"v_exp_f32 v108, v108\n" \
"v_mfma_f32_16x16x32_bf16 v[16:19], a[204:207], a[172:175], v[16:19]\n" \
"s_add_u32 s88, s88, s71\n" \
"ds_read_b128 v[208:211], v246 offset:16640\n" \
"v_cvt_pk_bf16_f32 v105, v106, v107\n" \
"v_mfma_f32_16x16x32_bf16 v[16:19], a[192:195], a[160:163], v[16:19]\n" \
"s_lshr_b32 s70, s70, 2\n" \
"ds_read_b128 v[212:215], v246 offset:18688\n" \
"v_exp_f32 v109, v109\n" \
"v_permlane16_swap_b32 v64, v66\n" \
"v_add_f32 v66, v66, v64\n" \
"v_mov_b32 v64, v66\n" \
"v_mfma_f32_16x16x32_bf16 v[16:19], a[196:199], a[164:167], v[16:19]\n" \
"s_lshr_b32 s71, s22, 7\n" \
"ds_read_b128 v[216:219], v246 offset:20736\n" \
"v_exp_f32 v110, v110\n" \
"v_mfma_f32_16x16x32_bf16 v[20:23], a[208:211], a[160:163], v[232:235]\n" \
"s_xor_b32 s71, s71, s70\n" \
"ds_read_b128 v[220:223], v246 offset:22784\n" \
"v_cvt_pk_bf16_f32 v106, v108, v109\n" \
"v_mfma_f32_16x16x32_bf16 v[20:23], a[212:215], a[164:167], v[20:23]\n" \
"s_and_b32 s71, s71, 1\n" \
"ds_read_b128 a[192:195], v247 offset:16384\n" \
"v_bfe_u32 v69, v248, 8, 1\n" \
"v_mfma_f32_16x16x32_bf16 v[20:23], a[216:219], a[168:171], v[20:23]\n" \
"ds_read_b128 a[196:199], v247 offset:24576\n" \
"v_bfe_u32 v70, v248, 6, 2\n" \
"v_mfma_f32_16x16x32_bf16 v[20:23], a[220:223], a[172:175], v[20:23]\n" \
"s_and_b32 s85, %1, 7\n" \
"ds_read_b128 a[200:203], v247 offset:17408\n" \
"v_permlane32_swap_b32 v64, v66\n" \
"v_add_f32 v66, v66, v64\n" \
"v_rcp_f32 v64, v66\n" \
"v_mul_f32 v66, 0x4d800000, v66\n" \
"v_add_u32 v65, 0x1c90080, v249\n" \
"v_cndmask_b32 v65, v249, v65, s[16:17]\n" \
"v_accvgpr_read_b32 v96, a0\n" \
"v_accvgpr_read_b32 v97, a1\n" \
"v_accvgpr_read_b32 v98, a2\n" \
"v_accvgpr_read_b32 v99, a3\n" \
"v_accvgpr_read_b32 v100, a4\n" \
"v_accvgpr_read_b32 v101, a5\n" \
"v_accvgpr_read_b32 v102, a6\n" \
"v_accvgpr_read_b32 v103, a7\n" \
"v_exp_f32 v111, v111\n" \
"v_exp_f32 v112, v112\n" \
"v_pk_mul_f32 v[96:97], v[96:97], v[64:65] op_sel_hi:[1,0]\n" \
"v_pk_mul_f32 v[98:99], v[98:99], v[64:65] op_sel_hi:[1,0]\n" \
"v_pk_mul_f32 v[100:101], v[100:101], v[64:65] op_sel_hi:[1,0]\n" \
"v_pk_mul_f32 v[102:103], v[102:103], v[64:65] op_sel_hi:[1,0]\n" \
"v_cvt_pk_bf16_f32 v224, v96, v97\n" \
"v_cvt_pk_bf16_f32 v225, v98, v99\n" \
"v_mfma_f32_16x16x32_bf16 v[28:31], a[216:219], a[184:187], v[236:239]\n" \
"s_lshl_b32 s85, s85, 1\n" \
"ds_read_b128 a[204:207], v247 offset:25600\n" \
"v_cvt_pk_bf16_f32 v107, v110, v111\n" \
"v_mfma_f32_16x16x32_bf16 v[28:31], a[220:223], a[188:191], v[28:31]\n" \
"s_add_u32 s85, s85, s71\n" \
"v_lshl_add_u32 v70, v69, 3, v70\n" \
"v_mul_u32_u24 v70, 6144, v70\n" \
"v_mfma_f32_16x16x32_bf16 v[28:31], a[208:211], a[176:179], v[28:31]\n" \
"v_lshlrev_b32 v69, 5, v69\n" \
"v_and_b32 v71, 0x30, v248\n" \
"v_mfma_f32_16x16x32_bf16 v[28:31], a[212:215], a[180:183], v[28:31]\n" \
"s_waitcnt lgkmcnt(2)\n" \
"ds_read_b128 a[208:211], v247 offset:18432\n" \
"v_cvt_pk_bf16_f32 v226, v100, v101\n" \
"v_cvt_pk_bf16_f32 v227, v102, v103\n" \
"v_exp_f32 v113, v113\n" \
"v_pk_mul_f32 v[96:97], v[96:97], v[98:99]\n" \
"v_pk_fma_f32 v[96:97], v[100:101], v[102:103], v[96:97]\n" \
"v_fma_f32 v66, v96, v97, v66\n" \
"v_mfma_f32_16x16x32_bf16 v[8:11], v[192:195], a[176:179], v[236:239]\n" \
"s_lshl_b32 s70, s85, 7\n" \
"ds_read_b128 a[212:215], v247 offset:26624\n" \
"buffer_store_dwordx4 v[224:227], v65, s[56:59], s77 offen offset:0 sc1\n" \
"v_exp_f32 v114, v114\n" \
"v_accvgpr_read_b32 v96, a8\n" \
"v_accvgpr_read_b32 v97, a9\n" \
"v_accvgpr_read_b32 v98, a10\n" \
"v_mfma_f32_16x16x32_bf16 v[8:11], v[196:199], a[180:183], v[8:11]\n" \
"s_mul_i32 s84, s61, 6144\n" \
"ds_read_b128 a[216:219], v247 offset:19456\n" \
"v_cvt_pk_bf16_f32 v112, v112, v113\n" \
"v_mfma_f32_16x16x32_bf16 v[8:11], v[200:203], a[184:187], v[8:11]\n" \
"s_lshl_b32 s71, s84, 4\n" \
"ds_read_b128 a[220:223], v247 offset:27648\n" \
"v_xor_b32 v69, v69, v71\n" \
"v_mfma_f32_16x16x32_bf16 v[8:11], v[204:207], a[188:191], v[8:11]\n" \
"ds_read_b128 v[192:195], v246 offset:24576\n" \
"v_lshrrev_b32 v71, 9, v248\n" \
"v_accvgpr_read_b32 v99, a11\n" \
"v_accvgpr_read_b32 v100, a12\n" \
"v_accvgpr_read_b32 v101, a13\n" \
"v_accvgpr_read_b32 v102, a14\n" \
"v_accvgpr_read_b32 v103, a15\n" \
"v_pk_mul_f32 v[96:97], v[96:97], v[64:65] op_sel_hi:[1,0]\n" \
"v_pk_mul_f32 v[98:99], v[98:99], v[64:65] op_sel_hi:[1,0]\n" \
"v_pk_mul_f32 v[100:101], v[100:101], v[64:65] op_sel_hi:[1,0]\n" \
"v_pk_mul_f32 v[102:103], v[102:103], v[64:65] op_sel_hi:[1,0]\n" \
"v_cvt_pk_bf16_f32 v228, v96, v97\n" \
"v_mfma_f32_16x16x32_bf16 v[56:59], a[232:235], a[184:187], v[236:239]\n" \
"s_add_u32 s70, s70, s71\n" \
"ds_read_b128 v[196:199], v246 offset:26624\n" \
"v_exp_f32 v115, v115\n" \
"v_mfma_f32_16x16x32_bf16 v[56:59], a[236:239], a[188:191], v[56:59]\n" \
"s_add_u32 s70, s70, 4096\n" \
"ds_read_b128 v[200:203], v246 offset:28672\n" \
"v_lshl_add_u32 v70, v71, 6, v70\n" \
"v_mfma_f32_16x16x32_bf16 v[56:59], a[224:227], a[176:179], v[56:59]\n" \
"ds_read_b128 v[204:207], v246 offset:30720\n" \
"v_add_u32 v253, v69, v70\n" \
"v_mfma_f32_16x16x32_bf16 v[56:59], a[228:231], a[180:183], v[56:59]\n" \
"s_add_u32 s48, s22, s70\n" \
"s_addc_u32 s49, s23, 0\n" \
"v_exp_f32 v116, v116\n" \
"v_mfma_f32_16x16x32_bf16 v[48:51], a[224:227], a[160:163], v[232:235]\n" \
"s_add_u32 s50, s48, 23552\n" \
"s_addc_u32 s51, s49, 0\n" \
"v_cvt_pk_bf16_f32 v113, v114, v115\n" \
"v_mfma_f32_16x16x32_bf16 v[48:51], a[228:231], a[164:167], v[48:51]\n" \
"s_add_u32 m0, s84, 143360\n" \
"v_exp_f32 v117, v117\n" \
"v_mfma_f32_16x16x32_bf16 v[48:51], a[232:235], a[168:171], v[48:51]\n" \
"v_exp_f32 v118, v118\n" \
"v_mfma_f32_16x16x32_bf16 v[48:51], a[236:239], a[172:175], v[48:51]\n" \
"ds_read_b128 a[224:227], v247 offset:20480\n" \
"global_load_lds_dwordx4 v253, s[48:49] offset:-4096\n" \
"v_mfma_f32_16x16x32_bf16 v[12:15], v[216:219], a[184:187], v[236:239]\n" \
"ds_read_b128 a[228:231], v247 offset:28672\n" \
"v_cvt_pk_bf16_f32 v114, v116, v117\n" \
"v_mfma_f32_16x16x32_bf16 v[12:15], v[220:223], a[188:191], v[12:15]\n" \
"ds_read_b128 a[232:235], v247 offset:21504\n" \
"global_load_lds_dwordx4 v253, s[48:49] offset:-2048\n" \
"v_mfma_f32_16x16x32_bf16 v[12:15], v[208:211], a[176:179], v[12:15]\n" \
"ds_read_b128 a[236:239], v247 offset:29696\n" \
"v_cvt_pk_bf16_f32 v229, v98, v99\n" \
"v_mfma_f32_16x16x32_bf16 v[12:15], v[212:215], a[180:183], v[12:15]\n" \
"ds_read_b128 v[208:211], v246 offset:24832\n" \
"v_cvt_pk_bf16_f32 v230, v100, v101\n" \
"v_cvt_pk_bf16_f32 v231, v102, v103\n" \
"v_exp_f32 v119, v119\n" \
"v_pk_mul_f32 v[96:97], v[96:97], v[98:99]\n" \
"v_pk_fma_f32 v[96:97], v[100:101], v[102:103], v[96:97]\n" \
"v_mfma_f32_16x16x32_bf16 v[60:63], a[240:243], a[176:179], v[236:239]\n" \
"ds_read_b128 v[212:215], v246 offset:26880\n" \
"global_load_lds_dwordx4 v253, s[48:49] offset:0\n" \
"v_mfma_f32_16x16x32_bf16 v[60:63], a[244:247], a[180:183], v[60:63]\n" \
"s_add_u32 s48, s48, 0x120000\n" \
"s_addc_u32 s49, s49, 0\n" \
"ds_read_b128 v[216:219], v246 offset:28928\n" \
"v_mfma_f32_16x16x32_bf16 v[60:63], a[248:251], a[184:187], v[60:63]\n" \
"ds_read_b128 v[220:223], v246 offset:30976\n" \
"global_load_lds_dwordx4 v253, s[50:51] offset:-3072\n" \
"v_mfma_f32_16x16x32_bf16 v[60:63], a[252:255], a[188:191], v[60:63]\n" \
"v_exp_f32 v120, v120\n" \
"v_mfma_f32_16x16x32_bf16 v[52:55], a[248:251], a[168:171], v[232:235]\n" \
"v_cvt_pk_bf16_f32 v115, v118, v119\n" \
"v_fma_f32 v66, v96, v97, v66\n" \
"v_mfma_f32_16x16x32_bf16 v[52:55], a[252:255], a[172:175], v[52:55]\n" \
"v_exp_f32 v121, v121\n" \
"v_mfma_f32_16x16x32_bf16 v[52:55], a[240:243], a[160:163], v[52:55]\n" \
"v_exp_f32 v122, v122\n" \
"v_mfma_f32_16x16x32_bf16 v[52:55], a[244:247], a[164:167], v[52:55]\n" \
"s_waitcnt lgkmcnt(0)\n" \
"ds_read_b128 a[240:243], v247 offset:22528\n" \
"global_load_lds_dwordx4 v253, s[50:51] offset:-1024\n" \
"v_mfma_f32_16x16x32_bf16 v[40:43], v[192:195], a[176:179], v[236:239]\n" \
"ds_read_b128 a[244:247], v247 offset:30720\n" \
"v_cvt_pk_bf16_f32 v120, v120, v121\n" \
"v_mfma_f32_16x16x32_bf16 v[40:43], v[196:199], a[180:183], v[40:43]\n" \
"ds_read_b128 a[248:251], v247 offset:23552\n" \
"global_load_lds_dwordx4 v253, s[50:51] offset:1024\n" \
"v_mfma_f32_16x16x32_bf16 v[40:43], v[200:203], a[184:187], v[40:43]\n" \
"s_add_u32 s50, s50, 0x120000\n" \
"s_addc_u32 s51, s51, 0\n" \
"ds_read_b128 a[252:255], v247 offset:31744\n" \
"v_mfma_f32_16x16x32_bf16 v[40:43], v[204:207], a[188:191], v[40:43]\n" \
"buffer_store_dwordx4 v[228:231], v65, s[56:59], s77 offen offset:2048 sc1\n" \
"v_exp_f32 v123, v123\n" \
"v_exp_f32 v124, v124\n" \
"v_cvt_pk_bf16_f32 v121, v122, v123\n" \
"v_mfma_f32_16x16x32_bf16 v[44:47], v[216:219], a[184:187], v[236:239]\n" \
"s_add_u32 s77, s77, 0x1000\n" \
"v_exp_f32 v125, v125\n" \
"v_mfma_f32_16x16x32_bf16 v[44:47], v[220:223], a[188:191], v[44:47]\n" \
"s_waitcnt vmcnt(8) lgkmcnt(0)\n" \
"v_exp_f32 v126, v126\n" \
"v_mfma_f32_16x16x32_bf16 v[44:47], v[208:211], a[176:179], v[44:47]\n" \
"v_add_u32 v247, 0x10000, v248\n" \
"v_cvt_pk_bf16_f32 v122, v124, v125\n" \
"v_mfma_f32_16x16x32_bf16 v[44:47], v[212:215], a[180:183], v[44:47]\n" \
"s_barrier\n" \
"v_accvgpr_read_b32 v96, a16\n" \
"v_accvgpr_read_b32 v97, a17\n" \
"v_accvgpr_read_b32 v98, a18\n" \
"v_accvgpr_read_b32 v99, a19\n" \
"v_accvgpr_read_b32 v100, a20\n" \
"v_accvgpr_read_b32 v101, a21\n" \
"v_accvgpr_read_b32 v102, a22\n" \
"v_accvgpr_read_b32 v103, a23\n" \
"v_pk_mul_f32 v[96:97], v[96:97], v[64:65] op_sel_hi:[1,0]\n" \
"v_pk_mul_f32 v[98:99], v[98:99], v[64:65] op_sel_hi:[1,0]\n" \
"v_pk_mul_f32 v[100:101], v[100:101], v[64:65] op_sel_hi:[1,0]\n" \
"v_pk_mul_f32 v[102:103], v[102:103], v[64:65] op_sel_hi:[1,0]\n" \
"v_cvt_pk_bf16_f32 v224, v96, v97\n" \
"v_cvt_pk_bf16_f32 v225, v98, v99\n" \
"v_cvt_pk_bf16_f32 v226, v100, v101\n" \
"v_cvt_pk_bf16_f32 v227, v102, v103\n" \
"v_pk_mul_f32 v[96:97], v[96:97], v[98:99]\n" \
"v_pk_fma_f32 v[96:97], v[100:101], v[102:103], v[96:97]\n" \
"v_fma_f32 v66, v96, v97, v66\n" \
"v_mfma_f32_16x16x32_bf16 a[32:35], a[192:195], v[72:75], a[32:35]\n" \
"v_exp_f32 v127, v127\n" \
"v_mfma_f32_16x16x32_bf16 a[32:35], a[196:199], v[104:107], a[32:35]\n" \
"buffer_store_dwordx4 v[224:227], v65, s[56:59], s77 offen offset:0 sc1\n" \
"v_cvt_pk_bf16_f32 v123, v126, v127\n" \
"v_accvgpr_read_b32 v96, a24\n" \
"v_accvgpr_read_b32 v97, a25\n" \
"v_accvgpr_read_b32 v98, a26\n" \
"v_accvgpr_read_b32 v99, a27\n" \
"v_accvgpr_read_b32 v100, a28\n" \
"v_accvgpr_read_b32 v101, a29\n" \
"v_accvgpr_read_b32 v102, a30\n" \
"v_accvgpr_read_b32 v103, a31\n" \
"v_pk_mul_f32 v[96:97], v[96:97], v[64:65] op_sel_hi:[1,0]\n" \
"v_pk_mul_f32 v[98:99], v[98:99], v[64:65] op_sel_hi:[1,0]\n" \
"v_pk_mul_f32 v[100:101], v[100:101], v[64:65] op_sel_hi:[1,0]\n" \
"v_pk_mul_f32 v[102:103], v[102:103], v[64:65] op_sel_hi:[1,0]\n" \
"v_cvt_pk_bf16_f32 v228, v96, v97\n" \
"v_cvt_pk_bf16_f32 v229, v98, v99\n" \
"v_cvt_pk_bf16_f32 v230, v100, v101\n" \
"v_cvt_pk_bf16_f32 v231, v102, v103\n" \
"v_pk_mul_f32 v[96:97], v[96:97], v[98:99]\n" \
"v_pk_fma_f32 v[96:97], v[100:101], v[102:103], v[96:97]\n" \
"v_fma_f32 v66, v96, v97, v66\n" \
"v_mfma_f32_16x16x32_bf16 v[128:131], a[192:195], v[80:83], v[128:131]\n" \
"ds_read_b128 v[192:195], v247 offset:16384\n" \
"v_cmp_gt_i32 vcc, 0, v251\n" \
"v_mfma_f32_16x16x32_bf16 v[128:131], a[196:199], v[112:115], v[128:131]\n" \
"v_lshrrev_b32 v71, 7, v248\n" \
"v_and_b32 v76, 0x70, v248\n" \
"v_mfma_f32_16x16x32_bf16 v[160:163], a[192:195], v[88:91], v[160:163]\n" \
"s_lshl_b32 s70, s88, 18\n" \
"v_cmp_gt_i32 s[64:65], 1, v251\n" \
"v_cmp_gt_i32 s[66:67], 2, v251\n" \
"v_mfma_f32_16x16x32_bf16 v[160:163], a[196:199], v[120:123], v[160:163]\n" \
"s_lshl_b32 s71, s61, 16\n" \
"ds_read_b128 a[192:195], v247 offset:0\n" \
"v_cndmask_b32 v8, v8, v254, vcc\n" \
"v_mfma_f32_16x16x32_bf16 v[240:243], a[124:127], v[104:107], v[240:243]\n" \
"s_add_u32 s71, s71, s70\n" \
"ds_read_b128 v[196:199], v247 offset:24576\n" \
"v_cndmask_b32 v16, v16, v254, vcc\n" \
"v_mfma_f32_16x16x32_bf16 v[164:167], a[200:203], v[88:91], v[164:167]\n" \
"v_lshlrev_b32 v77, 4, v71\n" \
"v_xor_b32 v77, v77, v76\n" \
"v_mfma_f32_16x16x32_bf16 v[164:167], a[204:207], v[120:123], v[164:167]\n" \
"s_add_u32 s42, s20, s71\n" \
"s_addc_u32 s43, s21, 0\n" \
"ds_read_b128 a[196:199], v247 offset:8192\n" \
"v_cmp_gt_i32 vcc, 3, v251\n" \
"v_cndmask_b32 v9, v9, v254, s[64:65]\n" \
"v_cndmask_b32 v17, v17, v254, s[64:65]\n" \
"v_cmp_gt_i32 s[64:65], 16, v251\n" \
"v_cndmask_b32 v10, v10, v254, s[66:67]\n" \
"v_cndmask_b32 v18, v18, v254, s[66:67]\n" \
"v_cmp_gt_i32 s[66:67], 17, v251\n" \
"v_cndmask_b32 v11, v11, v254, vcc\n" \
"v_cndmask_b32 v19, v19, v254, vcc\n" \
"v_mfma_f32_16x16x32_bf16 v[132:135], a[200:203], v[80:83], v[132:135]\n" \
"ds_read_b128 v[200:203], v247 offset:17408\n" \
"v_lshl_add_u32 v252, v71, 12, v77\n" \
"v_mfma_f32_16x16x32_bf16 v[132:135], a[204:207], v[112:115], v[132:135]\n" \
"s_add_u32 s44, s42, 31744\n" \
"s_addc_u32 s45, s43, 0\n" \
"v_cmp_gt_i32 vcc, 18, v251\n" \
"v_cndmask_b32 v12, v12, v254, s[64:65]\n" \
"v_mfma_f32_16x16x32_bf16 v[242:245], a[120:123], v[112:115], v[242:245]\n" \
"v_cndmask_b32 v20, v20, v254, s[64:65]\n" \
"v_cmp_gt_i32 s[64:65], 19, v251\n" \
"v_mfma_f32_16x16x32_bf16 v[242:245], a[124:127], v[120:123], v[242:245]\n" \
"v_cndmask_b32 v13, v13, v254, s[66:67]\n" \
"v_cndmask_b32 v21, v21, v254, s[66:67]\n" \
"v_cmp_gt_i32 s[66:67], 32, v251\n" \
"v_cndmask_b32 v14, v14, v254, vcc\n" \
"v_mfma_f32_16x16x32_bf16 a[36:39], a[200:203], v[72:75], a[36:39]\n" \
"s_add_u32 m0, s95, 118784\n" \
"v_cndmask_b32 v22, v22, v254, vcc\n" \
"v_cmp_gt_i32 vcc, 33, v251\n" \
"v_mfma_f32_16x16x32_bf16 a[36:39], a[204:207], v[104:107], a[36:39]\n" \
"ds_read_b128 a[200:203], v247 offset:1024\n" \
"global_load_lds_dwordx4 v252, s[42:43] offset:0\n" \
"ds_read_b128 v[204:207], v247 offset:25600\n" \
"v_cndmask_b32 v15, v15, v254, s[64:65]\n" \
"v_cndmask_b32 v23, v23, v254, s[64:65]\n" \
"v_cmp_gt_i32 s[64:65], 34, v251\n" \
"v_cndmask_b32 v40, v40, v254, s[66:67]\n" \
"v_mfma_f32_16x16x32_bf16 a[40:43], a[208:211], v[72:75], a[40:43]\n" \
"v_cndmask_b32 v48, v48, v254, s[66:67]\n" \
"v_cmp_gt_i32 s[66:67], 35, v251\n" \
"v_mfma_f32_16x16x32_bf16 a[40:43], a[212:215], v[104:107], a[40:43]\n" \
"buffer_store_dwordx4 v[228:231], v65, s[56:59], s77 offen offset:2048 sc1\n" \
"ds_read_b128 a[204:207], v247 offset:9216\n" \
"v_cndmask_b32 v41, v41, v254, vcc\n" \
"v_cndmask_b32 v49, v49, v254, vcc\n" \
"v_cmp_gt_i32 vcc, 48, v251\n" \
"v_cndmask_b32 v42, v42, v254, s[64:65]\n" \
"v_cndmask_b32 v50, v50, v254, s[64:65]\n" \
"v_cmp_gt_i32 s[64:65], 49, v251\n" \
"v_cndmask_b32 v43, v43, v254, s[66:67]\n" \
"v_cndmask_b32 v51, v51, v254, s[66:67]\n" \
"v_cmp_gt_i32 s[66:67], 50, v251\n" \
"v_cndmask_b32 v44, v44, v254, vcc\n" \
"v_cmp_class_f32 s[54:55], v66, s94\n" \
"v_mfma_f32_16x16x32_bf16 v[136:139], a[208:211], v[80:83], v[136:139]\n" \
"s_sub_u32 s77, s77, 4096\n" \
"ds_read_b128 v[208:211], v247 offset:18432\n" \
"v_cndmask_b32 v52, v52, v254, vcc\n" \
"v_mfma_f32_16x16x32_bf16 v[136:139], a[212:215], v[112:115], v[136:139]\n" \
"s_or_b64 s[92:93], s[92:93], s[54:55]\n" \
"v_cmp_gt_i32 vcc, 51, v251\n" \
"v_cndmask_b32 v45, v45, v254, s[64:65]\n" \
"v_cndmask_b32 v53, v53, v254, s[64:65]\n" \
"v_cndmask_b32 v46, v46, v254, s[66:67]\n" \
"v_cndmask_b32 v54, v54, v254, s[66:67]\n" \
"v_cndmask_b32 v47, v47, v254, vcc\n" \
"v_cndmask_b32 v55, v55, v254, vcc\n" \
"v_exp_f32 v8, v8\n" \
"v_exp_f32 v9, v9\n" \
"v_mfma_f32_16x16x32_bf16 v[168:171], a[208:211], v[88:91], v[168:171]\n" \
"v_mov_b32 v66, v243\n" \
"v_exp_f32 v10, v10\n" \
"v_mfma_f32_16x16x32_bf16 v[168:171], a[212:215], v[120:123], v[168:171]\n" \
"ds_read_b128 a[208:211], v247 offset:2048\n" \
"global_load_lds_dwordx4 v252, s[42:43] offset:2048\n" \
"ds_read_b128 v[212:215], v247 offset:26624\n" \
"v_mfma_f32_16x16x32_bf16 v[172:175], a[216:219], v[88:91], v[172:175]\n" \
"s_add_u32 s42, s42, 0x180\n" \
"s_addc_u32 s43, s43, 0\n" \
"v_cvt_pk_bf16_f32 v8, v8, v9\n" \
"v_mfma_f32_16x16x32_bf16 v[172:175], a[220:223], v[120:123], v[172:175]\n" \
"ds_read_b128 a[212:215], v247 offset:10240\n" \
"v_exp_f32 v11, v11\n" \
"v_exp_f32 v12, v12\n" \
"v_mfma_f32_16x16x32_bf16 v[140:143], a[216:219], v[80:83], v[140:143]\n" \
"ds_read_b128 v[216:219], v247 offset:19456\n" \
"v_cvt_pk_bf16_f32 v9, v10, v11\n" \
"v_mfma_f32_16x16x32_bf16 v[140:143], a[220:223], v[112:115], v[140:143]\n" \
"v_exp_f32 v13, v13\n" \
"v_mfma_f32_16x16x32_bf16 a[44:47], a[216:219], v[72:75], a[44:47]\n" \
"v_exp_f32 v14, v14\n" \
"v_mfma_f32_16x16x32_bf16 a[44:47], a[220:223], v[104:107], a[44:47]\n" \
"ds_read_b128 a[216:219], v247 offset:3072\n" \
"global_load_lds_dwordx4 v252, s[44:45] offset:1024\n" \
"ds_read_b128 v[220:223], v247 offset:27648\n" \
"v_cvt_pk_bf16_f32 v10, v12, v13\n" \
"v_mfma_f32_16x16x32_bf16 a[48:51], a[224:227], v[72:75], a[48:51]\n" \
"v_exp_f32 v15, v15\n" \
"v_mfma_f32_16x16x32_bf16 a[48:51], a[228:231], v[104:107], a[48:51]\n" \
"ds_read_b128 a[220:223], v247 offset:11264\n" \
"v_exp_f32 v40, v40\n" \
"v_mfma_f32_16x16x32_bf16 v[144:147], a[224:227], v[80:83], v[144:147]\n" \
"ds_read_b128 v[224:227], v247 offset:20480\n" \
"v_cvt_pk_bf16_f32 v11, v14, v15\n" \
"v_mfma_f32_16x16x32_bf16 v[144:147], a[228:231], v[112:115], v[144:147]\n" \
"v_exp_f32 v41, v41\n" \
"v_mfma_f32_16x16x32_bf16 v[176:179], a[224:227], v[88:91], v[176:179]\n" \
"v_exp_f32 v42, v42\n" \
"v_mfma_f32_16x16x32_bf16 v[176:179], a[228:231], v[120:123], v[176:179]\n" \
"ds_read_b128 a[224:227], v247 offset:4096\n" \
"global_load_lds_dwordx4 v252, s[44:45] offset:3072\n" \
"ds_read_b128 v[228:231], v247 offset:28672\n" \
"v_mfma_f32_16x16x32_bf16 v[180:183], a[232:235], v[88:91], v[180:183]\n" \
"s_add_u32 s44, s44, 0x180\n" \
"s_addc_u32 s45, s45, 0\n" \
"v_cvt_pk_bf16_f32 v40, v40, v41\n" \
"v_mfma_f32_16x16x32_bf16 v[180:183], a[236:239], v[120:123], v[180:183]\n" \
"ds_read_b128 a[228:231], v247 offset:12288\n" \
"v_exp_f32 v43, v43\n" \
"v_exp_f32 v44, v44\n" \
"v_mfma_f32_16x16x32_bf16 v[148:151], a[232:235], v[80:83], v[148:151]\n" \
"ds_read_b128 v[232:235], v247 offset:21504\n" \
"v_cvt_pk_bf16_f32 v41, v42, v43\n" \
"v_mfma_f32_16x16x32_bf16 v[148:151], a[236:239], v[112:115], v[148:151]\n" \
"v_exp_f32 v45, v45\n" \
"v_mfma_f32_16x16x32_bf16 a[52:55], a[232:235], v[72:75], a[52:55]\n" \
"v_exp_f32 v46, v46\n" \
"v_mfma_f32_16x16x32_bf16 a[52:55], a[236:239], v[104:107], a[52:55]\n" \
"ds_read_b128 a[232:235], v247 offset:5120\n" \
"ds_read_b128 v[236:239], v247 offset:29696\n" \
"v_cvt_pk_bf16_f32 v42, v44, v45\n" \
"v_mfma_f32_16x16x32_bf16 a[56:59], a[240:243], v[72:75], a[56:59]\n" \
"v_exp_f32 v47, v47\n" \
"v_mfma_f32_16x16x32_bf16 a[56:59], a[244:247], v[104:107], a[56:59]\n" \
"ds_read_b128 a[236:239], v247 offset:13312\n" \
"v_exp_f32 v24, v24\n" \
"v_mfma_f32_16x16x32_bf16 v[152:155], a[240:243], v[80:83], v[152:155]\n" \
"ds_read_b128 v[0:3], v247 offset:22528\n" \
"v_cvt_pk_bf16_f32 v43, v46, v47\n" \
"v_mfma_f32_16x16x32_bf16 v[152:155], a[244:247], v[112:115], v[152:155]\n" \
"v_exp_f32 v25, v25\n" \
"v_mfma_f32_16x16x32_bf16 v[184:187], a[240:243], v[88:91], v[184:187]\n" \
"v_exp_f32 v26, v26\n" \
"v_mfma_f32_16x16x32_bf16 v[184:187], a[244:247], v[120:123], v[184:187]\n" \
"ds_read_b128 a[240:243], v247 offset:6144\n" \
"v_cvt_pk_bf16_f32 v24, v24, v25\n" \
"v_mfma_f32_16x16x32_bf16 v[242:245], a[124:127], v[8:11], v[242:245]\n" \
"v_exp_f32 v27, v27\n" \
"v_mfma_f32_16x16x32_bf16 v[242:245], a[124:127], v[40:43], v[242:245]\n" \
"ds_read_b128 v[4:7], v247 offset:30720\n" \
"v_exp_f32 v28, v28\n" \
"v_mfma_f32_16x16x32_bf16 a[60:63], a[248:251], v[72:75], a[60:63]\n" \
"v_cvt_pk_bf16_f32 v25, v26, v27\n" \
"v_exp_f32 v29, v29\n" \
"v_mfma_f32_16x16x32_bf16 a[60:63], a[252:255], v[104:107], a[60:63]\n" \
"ds_read_b128 a[244:247], v247 offset:14336\n" \
"v_exp_f32 v30, v30\n" \
"v_mfma_f32_16x16x32_bf16 v[156:159], a[248:251], v[80:83], v[156:159]\n" \
"ds_read_b128 v[32:35], v247 offset:23552\n" \
"v_cvt_pk_bf16_f32 v26, v28, v29\n" \
"v_mfma_f32_16x16x32_bf16 v[156:159], a[252:255], v[112:115], v[156:159]\n" \
"v_exp_f32 v31, v31\n" \
"v_exp_f32 v56, v56\n" \
"v_cvt_pk_bf16_f32 v27, v30, v31\n" \
"v_mfma_f32_16x16x32_bf16 v[188:191], a[248:251], v[88:91], v[188:191]\n" \
"v_exp_f32 v57, v57\n" \
"v_mfma_f32_16x16x32_bf16 v[188:191], a[252:255], v[120:123], v[188:191]\n" \
"s_waitcnt lgkmcnt(2)\n" \
"ds_read_b128 a[248:251], v247 offset:7168\n" \
"ds_read_b128 v[36:39], v247 offset:31744\n" \
"v_mfma_f32_16x16x32_bf16 v[160:163], v[192:195], v[8:11], v[160:163]\n" \
"v_exp_f32 v58, v58\n" \
"v_mfma_f32_16x16x32_bf16 v[160:163], v[196:199], v[40:43], v[160:163]\n" \
"ds_read_b128 a[252:255], v247 offset:15360\n" \
"v_cvt_pk_bf16_f32 v56, v56, v57\n" \
"v_exp_f32 v59, v59\n" \
"v_mfma_f32_16x16x32_bf16 v[164:167], v[200:203], v[8:11], v[164:167]\n" \
"v_exp_f32 v60, v60\n" \
"v_mfma_f32_16x16x32_bf16 v[164:167], v[204:207], v[40:43], v[164:167]\n" \
"v_mov_b32 v64, v66\n" \
"v_cvt_pk_bf16_f32 v57, v58, v59\n" \
"v_exp_f32 v61, v61\n" \
"v_mfma_f32_16x16x32_bf16 v[168:171], v[208:211], v[8:11], v[168:171]\n" \
"v_exp_f32 v62, v62\n" \
"v_mfma_f32_16x16x32_bf16 v[168:171], v[212:215], v[40:43], v[168:171]\n" \
"s_waitcnt vmcnt(0) lgkmcnt(0)\n" \
"v_cvt_pk_bf16_f32 v58, v60, v61\n" \
"v_exp_f32 v63, v63\n" \
"v_permlane16_swap_b32 v64, v66\n" \
"v_add_f32 v66, v66, v64\n" \
"v_mov_b32 v64, v66\n" \
"v_mfma_f32_16x16x32_bf16 v[172:175], v[216:219], v[8:11], v[172:175]\n" \
"v_exp_f32 v16, v16\n" \
"v_mfma_f32_16x16x32_bf16 v[172:175], v[220:223], v[40:43], v[172:175]\n" \
"s_barrier\n" \
"v_cvt_pk_bf16_f32 v59, v62, v63\n" \
"v_exp_f32 v17, v17\n" \
"v_permlane32_swap_b32 v64, v66\n" \
"v_add_f32 v66, v66, v64\n" \
"v_rcp_f32 v64, v66\n" \
"v_mul_f32 v66, 0x4d800000, v66\n" \
"v_add_u32 v65, 0x1c90080, v249\n" \
"v_cndmask_b32 v65, v249, v65, s[16:17]\n" \
"v_mfma_f32_16x16x32_bf16 v[176:179], v[224:227], v[8:11], v[176:179]\n" \
"s_add_u32 m0, s84, 90112\n" \
"v_exp_f32 v18, v18\n" \
"v_mfma_f32_16x16x32_bf16 v[176:179], v[228:231], v[40:43], v[176:179]\n" \
"global_load_lds_dwordx4 v253, s[48:49] offset:-4096\n" \
"global_load_lds_dwordx4 v253, s[48:49] offset:-2048\n" \
"v_cvt_pk_bf16_f32 v16, v16, v17\n" \
"v_mfma_f32_16x16x32_bf16 v[242:245], a[124:127], v[24:27], v[242:245]\n" \
"v_exp_f32 v19, v19\n" \
"v_mfma_f32_16x16x32_bf16 v[242:245], a[124:127], v[56:59], v[242:245]\n" \
"v_exp_f32 v20, v20\n" \
"v_cvt_pk_bf16_f32 v17, v18, v19\n" \
"v_mfma_f32_16x16x32_bf16 v[180:183], v[232:235], v[8:11], v[180:183]\n" \
"s_add_u32 s77, s77, 532480\n" \
"v_exp_f32 v21, v21\n" \
"v_mfma_f32_16x16x32_bf16 v[180:183], v[236:239], v[40:43], v[180:183]\n" \
"v_accvgpr_read_b32 v96, a32\n" \
"v_accvgpr_read_b32 v97, a33\n" \
"v_accvgpr_read_b32 v98, a34\n" \
"v_accvgpr_read_b32 v99, a35\n" \
"v_accvgpr_read_b32 v100, a36\n" \
"v_accvgpr_read_b32 v101, a37\n" \
"v_accvgpr_read_b32 v102, a38\n" \
"v_accvgpr_read_b32 v103, a39\n" \
"v_exp_f32 v22, v22\n" \
"v_pk_mul_f32 v[96:97], v[96:97], v[64:65] op_sel_hi:[1,0]\n" \
"v_pk_mul_f32 v[98:99], v[98:99], v[64:65] op_sel_hi:[1,0]\n" \
"v_pk_mul_f32 v[100:101], v[100:101], v[64:65] op_sel_hi:[1,0]\n" \
"v_pk_mul_f32 v[102:103], v[102:103], v[64:65] op_sel_hi:[1,0]\n" \
"v_cvt_pk_bf16_f32 v104, v96, v97\n" \
"v_cvt_pk_bf16_f32 v105, v98, v99\n" \
"v_cvt_pk_bf16_f32 v106, v100, v101\n" \
"v_cvt_pk_bf16_f32 v107, v102, v103\n" \
"v_pk_mul_f32 v[96:97], v[96:97], v[98:99]\n" \
"v_pk_fma_f32 v[96:97], v[100:101], v[102:103], v[96:97]\n" \
"v_fma_f32 v66, v96, v97, v66\n" \
"v_mfma_f32_16x16x32_bf16 v[184:187], v[0:3], v[8:11], v[184:187]\n" \
"global_load_lds_dwordx4 v253, s[48:49] offset:0\n" \
"v_cvt_pk_bf16_f32 v18, v20, v21\n" \
"v_mfma_f32_16x16x32_bf16 v[184:187], v[4:7], v[40:43], v[184:187]\n" \
"s_add_u32 s48, s48, 0x300000\n" \
"s_addc_u32 s49, s49, 0\n" \
"v_exp_f32 v23, v23\n" \
"v_exp_f32 v48, v48\n" \
"v_mfma_f32_16x16x32_bf16 v[188:191], v[32:35], v[8:11], v[188:191]\n" \
"global_load_lds_dwordx4 v253, s[50:51] offset:-3072\n" \
"v_cvt_pk_bf16_f32 v19, v22, v23\n" \
"v_mfma_f32_16x16x32_bf16 v[188:191], v[36:39], v[40:43], v[188:191]\n" \
"buffer_store_dwordx4 v[104:107], v65, s[56:59], s77 offen offset:0 sc1\n" \
"v_accvgpr_read_b32 v96, a40\n" \
"v_accvgpr_read_b32 v97, a41\n" \
"v_accvgpr_read_b32 v98, a42\n" \
"v_accvgpr_read_b32 v99, a43\n" \
"v_accvgpr_read_b32 v100, a44\n" \
"v_accvgpr_read_b32 v101, a45\n" \
"v_accvgpr_read_b32 v102, a46\n" \
"v_accvgpr_read_b32 v103, a47\n" \
"v_exp_f32 v49, v49\n" \
"v_pk_mul_f32 v[96:97], v[96:97], v[64:65] op_sel_hi:[1,0]\n" \
"v_pk_mul_f32 v[98:99], v[98:99], v[64:65] op_sel_hi:[1,0]\n" \
"v_pk_mul_f32 v[100:101], v[100:101], v[64:65] op_sel_hi:[1,0]\n" \
"v_pk_mul_f32 v[102:103], v[102:103], v[64:65] op_sel_hi:[1,0]\n" \
"v_cvt_pk_bf16_f32 v108, v96, v97\n" \
"v_cvt_pk_bf16_f32 v109, v98, v99\n" \
"v_cvt_pk_bf16_f32 v110, v100, v101\n" \
"v_cvt_pk_bf16_f32 v111, v102, v103\n" \
"v_pk_mul_f32 v[96:97], v[96:97], v[98:99]\n" \
"v_pk_fma_f32 v[96:97], v[100:101], v[102:103], v[96:97]\n" \
"v_fma_f32 v66, v96, v97, v66\n" \
"v_mfma_f32_16x16x32_bf16 v[160:163], a[192:195], v[24:27], v[160:163]\n" \
"global_load_lds_dwordx4 v253, s[50:51] offset:-1024\n" \
"global_load_lds_dwordx4 v253, s[50:51] offset:1024\n" \
"v_mfma_f32_16x16x32_bf16 v[160:163], a[196:199], v[56:59], v[160:163]\n" \
"s_movk_i32 s89, 0xc0\n" \
"v_exp_f32 v50, v50\n" \
"v_cvt_pk_bf16_f32 v48, v48, v49\n" \
"v_mfma_f32_16x16x32_bf16 v[242:245], a[120:123], v[16:19], v[242:245]\n" \
"s_movk_i32 s90, 288\n" \
"v_exp_f32 v51, v51\n" \
"v_lshrrev_b32 v211, 4, v248\n" \
"v_mfma_f32_16x16x32_bf16 v[164:167], a[200:203], v[24:27], v[164:167]\n" \
"s_add_u32 s50, s50, 0x300000\n" \
"s_addc_u32 s51, s51, 0\n" \
"v_lshrrev_b32 v214, 8, v248\n" \
"v_mfma_f32_16x16x32_bf16 v[164:167], a[204:207], v[56:59], v[164:167]\n" \
"s_add_u32 m0, s95, 65536\n" \
"buffer_store_dwordx4 v[108:111], v65, s[56:59], s77 offen offset:2048 sc1\n" \
"v_and_b32 v215, 7, v211\n" \
"v_xor_b32 v214, v214, v215\n" \
"v_exp_f32 v52, v52\n" \
"v_mfma_f32_16x16x32_bf16 v[168:171], a[208:211], v[24:27], v[168:171]\n" \
"global_load_lds_dwordx4 v252, s[42:43] offset:0\n" \
"global_load_lds_dwordx4 v252, s[42:43] offset:2048\n" \
"v_mfma_f32_16x16x32_bf16 v[168:171], a[212:215], v[56:59], v[168:171]\n" \
"s_lshr_b32 s71, s61, 1\n" \
"v_cvt_pk_bf16_f32 v49, v50, v51\n" \
"s_lshl_b32 s70, s71, 11\n" \
"v_mfma_f32_16x16x32_bf16 v[172:175], a[216:219], v[24:27], v[172:175]\n" \
"s_add_u32 s70, s70, 118784\n" \
"v_and_b32 v216, 15, v211\n" \
"v_lshlrev_b32 v213, 7, v216\n" \
"v_mfma_f32_16x16x32_bf16 v[172:175], a[220:223], v[56:59], v[172:175]\n" \
"s_add_u32 s42, s42, 0x400\n" \
"s_addc_u32 s43, s43, 0\n" \
"v_lshl_add_u32 v213, v214, 4, v213\n" \
"v_add_u32 v205, s70, v213\n" \
"v_xor_b32 v206, 64, v205\n" \
"v_mfma_f32_16x16x32_bf16 v[176:179], a[224:227], v[24:27], v[176:179]\n" \
"global_load_lds_dwordx4 v252, s[44:45] offset:1024\n" \
"global_load_lds_dwordx4 v252, s[44:45] offset:3072\n" \
"v_mfma_f32_16x16x32_bf16 v[176:179], a[228:231], v[56:59], v[176:179]\n" \
"s_and_b32 s70, s61, 1\n" \
"v_exp_f32 v53, v53\n" \
"v_mfma_f32_16x16x32_bf16 v[180:183], a[232:235], v[24:27], v[180:183]\n" \
"s_lshl_b32 s70, s70, 9\n" \
"v_exp_f32 v54, v54\n" \
"v_mfma_f32_16x16x32_bf16 v[180:183], a[236:239], v[56:59], v[180:183]\n" \
"s_add_u32 s44, s44, 0x400\n" \
"s_addc_u32 s45, s45, 0\n" \
"v_cvt_pk_bf16_f32 v50, v52, v53\n" \
"v_exp_f32 v55, v55\n" \
"v_mfma_f32_16x16x32_bf16 v[184:187], a[240:243], v[24:27], v[184:187]\n" \
"s_add_u32 s70, s70, 139264\n" \
"v_cvt_pk_bf16_f32 v51, v54, v55\n" \
"v_and_b32 v213, 3, v211\n" \
"v_mfma_f32_16x16x32_bf16 v[184:187], a[244:247], v[56:59], v[184:187]\n" \
"s_add_u32 m0, s95, 0\n" \
"v_lshlrev_b32 v213, 3, v213\n" \
"v_and_or_b32 v213, v248, s89, v213\n" \
"v_mfma_f32_16x16x32_bf16 v[242:245], a[120:123], v[48:51], v[242:245]\n" \
"s_add_u32 s77, s77, 0x1000\n" \
"v_accvgpr_read_b32 v96, a48\n" \
"v_accvgpr_read_b32 v97, a49\n" \
"v_mfma_f32_16x16x32_bf16 v[188:191], a[248:251], v[24:27], v[188:191]\n" \
"v_bfe_u32 v214, v211, 4, 1\n" \
"v_mad_u32_u24 v213, v214, s90, v213\n" \
"v_mfma_f32_16x16x32_bf16 v[188:191], a[252:255], v[56:59], v[188:191]\n" \
"v_lshrrev_b32 v214, 5, v211\n" \
"v_mul_u32_u24 v214, 6144, v214\n" \
"v_accvgpr_read_b32 v98, a50\n" \
"v_accvgpr_read_b32 v99, a51\n" \
"v_accvgpr_read_b32 v100, a52\n" \
"v_accvgpr_read_b32 v101, a53\n" \
"v_accvgpr_read_b32 v102, a54\n" \
"v_accvgpr_read_b32 v103, a55\n" \
"v_pk_mul_f32 v[96:97], v[96:97], v[64:65] op_sel_hi:[1,0]\n" \
"v_pk_mul_f32 v[98:99], v[98:99], v[64:65] op_sel_hi:[1,0]\n" \
"v_pk_mul_f32 v[100:101], v[100:101], v[64:65] op_sel_hi:[1,0]\n" \
"v_pk_mul_f32 v[102:103], v[102:103], v[64:65] op_sel_hi:[1,0]\n" \
"v_cvt_pk_bf16_f32 v104, v96, v97\n" \
"v_cvt_pk_bf16_f32 v105, v98, v99\n" \
"v_cvt_pk_bf16_f32 v106, v100, v101\n" \
"v_cvt_pk_bf16_f32 v107, v102, v103\n" \
"v_pk_mul_f32 v[96:97], v[96:97], v[98:99]\n" \
"v_pk_fma_f32 v[96:97], v[100:101], v[102:103], v[96:97]\n" \
"v_mfma_f32_16x16x32_bf16 v[128:131], a[192:195], v[16:19], v[128:131]\n" \
"v_add3_u32 v209, v213, v214, s70\n" \
"v_xor_b32 v210, 32, v209\n" \
"v_mfma_f32_16x16x32_bf16 v[128:131], a[196:199], v[48:51], v[128:131]\n" \
"ds_read_b128 a[72:75], v205 offset:0\n" \
"ds_read_b64_tr_b16 a[96:97], v209 offset:2048\n" \
"v_mfma_f32_16x16x32_bf16 v[132:135], a[200:203], v[16:19], v[132:135]\n" \
"ds_read_b64_tr_b16 a[98:99], v209 offset:3072\n" \
"global_load_lds_dwordx4 v252, s[42:43] offset:0\n" \
"v_mfma_f32_16x16x32_bf16 v[132:135], a[204:207], v[48:51], v[132:135]\n" \
"ds_read_b128 a[76:79], v206 offset:0\n" \
"v_fma_f32 v66, v96, v97, v66\n" \
"v_mfma_f32_16x16x32_bf16 v[136:139], a[208:211], v[16:19], v[136:139]\n" \
"ds_read_b64_tr_b16 a[100:101], v209 offset:14336\n" \
"buffer_store_dwordx4 v[104:107], v65, s[56:59], s77 offen offset:0 sc1\n" \
"ds_read_b64_tr_b16 a[102:103], v209 offset:15360\n" \
"global_load_lds_dwordx4 v252, s[42:43] offset:2048\n" \
"v_accvgpr_read_b32 v96, a56\n" \
"v_accvgpr_read_b32 v97, a57\n" \
"v_mfma_f32_16x16x32_bf16 v[136:139], a[212:215], v[48:51], v[136:139]\n" \
"s_add_u32 s42, s42, 0xfffffb00\n" \
"s_addc_u32 s43, s43, -1\n" \
"ds_read_b64_tr_b16 a[104:105], v209 offset:0\n" \
"v_mfma_f32_16x16x32_bf16 v[140:143], a[216:219], v[16:19], v[140:143]\n" \
"ds_read_b128 a[80:83], v205 offset:4096\n" \
"v_accvgpr_read_b32 v98, a58\n" \
"v_mfma_f32_16x16x32_bf16 v[140:143], a[220:223], v[48:51], v[140:143]\n" \
"ds_read_b64_tr_b16 a[106:107], v209 offset:1024\n" \
"global_load_lds_dwordx4 v252, s[44:45] offset:1024\n" \
"ds_read_b64_tr_b16 a[108:109], v209 offset:12288\n" \
"v_mfma_f32_16x16x32_bf16 v[144:147], a[224:227], v[16:19], v[144:147]\n" \
"ds_read_b64_tr_b16 a[110:111], v209 offset:13312\n" \
"v_accvgpr_read_b32 v99, a59\n" \
"v_mfma_f32_16x16x32_bf16 v[144:147], a[228:231], v[48:51], v[144:147]\n" \
"ds_read_b128 a[84:87], v206 offset:4096\n" \
"v_accvgpr_read_b32 v100, a60\n" \
"v_accvgpr_read_b32 v101, a61\n" \
"v_accvgpr_read_b32 v102, a62\n" \
"v_accvgpr_read_b32 v103, a63\n" \
"v_pk_mul_f32 v[96:97], v[96:97], v[64:65] op_sel_hi:[1,0]\n" \
"v_pk_mul_f32 v[98:99], v[98:99], v[64:65] op_sel_hi:[1,0]\n" \
"v_pk_mul_f32 v[100:101], v[100:101], v[64:65] op_sel_hi:[1,0]\n" \
"v_pk_mul_f32 v[102:103], v[102:103], v[64:65] op_sel_hi:[1,0]\n" \
"v_mfma_f32_16x16x32_bf16 v[148:151], a[232:235], v[16:19], v[148:151]\n" \
"ds_read_b64_tr_b16 a[112:113], v210 offset:0\n" \
"global_load_lds_dwordx4 v252, s[44:45] offset:3072\n" \
"ds_read_b64_tr_b16 a[114:115], v210 offset:1024\n" \
"v_mfma_f32_16x16x32_bf16 v[148:151], a[236:239], v[48:51], v[148:151]\n" \
"ds_read_b64_tr_b16 a[116:117], v210 offset:12288\n" \
"v_cvt_pk_bf16_f32 v108, v96, v97\n" \
"v_cvt_pk_bf16_f32 v109, v98, v99\n" \
"v_cvt_pk_bf16_f32 v110, v100, v101\n" \
"v_cvt_pk_bf16_f32 v111, v102, v103\n" \
"v_pk_mul_f32 v[96:97], v[96:97], v[98:99]\n" \
"v_pk_fma_f32 v[96:97], v[100:101], v[102:103], v[96:97]\n" \
"v_mfma_f32_16x16x32_bf16 v[152:155], a[240:243], v[16:19], v[152:155]\n" \
"ds_read_b128 a[88:91], v205 offset:8192\n" \
"v_fma_f32 v66, v96, v97, v66\n" \
"v_mfma_f32_16x16x32_bf16 v[152:155], a[244:247], v[48:51], v[152:155]\n" \
"buffer_store_dwordx4 v[108:111], v65, s[56:59], s77 offen offset:2048 sc1\n" \
"ds_read_b128 a[92:95], v206 offset:8192\n" \
"ds_read_b64_tr_b16 a[118:119], v210 offset:13312\n" \
"v_cmp_class_f32 s[54:55], v66, s94\n" \
"s_sub_u32 s77, s77, 536576\n" \
"v_mfma_f32_16x16x32_bf16 v[156:159], a[248:251], v[16:19], v[156:159]\n" \
"s_or_b64 s[92:93], s[92:93], s[54:55]\n" \
"v_mfma_f32_16x16x32_bf16 v[156:159], a[252:255], v[48:51], v[156:159]\n" \
"s_waitcnt lgkmcnt(4)\n" \
"s_branch Ljoin_%=\n" \
"Lexact_%=:\n" \
"v_mov_b32 v190, 0xff800000\n" \
"v_add_u32 v191, 0x1c90080, v249\n" \
"v_cndmask_b32 v191, v249, v191, s[16:17]\n" \
"s_and_b32 s80, s61, 2\n" \
"s_lshl_b32 s80, s80, 4\n" \
"v_subrev_u32 v186, s80, v251\n" \
"v_accvgpr_read_b32 v160, a128\n" \
"v_accvgpr_read_b32 v161, a129\n" \
"v_accvgpr_read_b32 v162, a130\n" \
"v_accvgpr_read_b32 v163, a131\n" \
"v_accvgpr_read_b32 v164, a132\n" \
"v_accvgpr_read_b32 v165, a133\n" \
"v_accvgpr_read_b32 v166, a134\n" \
"v_accvgpr_read_b32 v167, a135\n" \
"v_accvgpr_read_b32 v168, a136\n" \
"v_accvgpr_read_b32 v169, a137\n" \
"v_accvgpr_read_b32 v170, a138\n" \
"v_accvgpr_read_b32 v171, a139\n" \
"v_accvgpr_read_b32 v172, a140\n" \
"v_accvgpr_read_b32 v173, a141\n" \
"v_accvgpr_read_b32 v174, a142\n" \
"v_accvgpr_read_b32 v175, a143\n" \
"s_lshl_b32 s75, s63, 2\n" \
"s_lshr_b32 s80, s61, 1\n" \
"s_add_u32 s75, s75, s80\n" \
"s_add_u32 s75, s75, 0\n" \
"s_add_u32 s76, s77, 0\n" \
"s_call_b64 s[68:69], Lex1_%=\n" \
"v_accvgpr_read_b32 v160, a144\n" \
"v_accvgpr_read_b32 v161, a145\n" \
"v_accvgpr_read_b32 v162, a146\n" \
"v_accvgpr_read_b32 v163, a147\n" \
"v_accvgpr_read_b32 v164, a148\n" \
"v_accvgpr_read_b32 v165, a149\n" \
"v_accvgpr_read_b32 v166, a150\n" \
"v_accvgpr_read_b32 v167, a151\n" \
"v_accvgpr_read_b32 v168, a152\n" \
"v_accvgpr_read_b32 v169, a153\n" \
"v_accvgpr_read_b32 v170, a154\n" \
"v_accvgpr_read_b32 v171, a155\n" \
"v_accvgpr_read_b32 v172, a156\n" \
"v_accvgpr_read_b32 v173, a157\n" \
"v_accvgpr_read_b32 v174, a158\n" \
"v_accvgpr_read_b32 v175, a159\n" \
"s_lshl_b32 s75, s63, 2\n" \
"s_lshr_b32 s80, s61, 1\n" \
"s_add_u32 s75, s75, s80\n" \
"s_add_u32 s75, s75, 2\n" \
"s_add_u32 s76, s77, 532480\n" \
"s_call_b64 s[68:69], Lex1_%=\n" \
"v_accvgpr_read_b32 v160, a160\n" \
"v_accvgpr_read_b32 v161, a161\n" \
"v_accvgpr_read_b32 v162, a162\n" \
"v_accvgpr_read_b32 v163, a163\n" \
"v_accvgpr_read_b32 v164, a164\n" \
"v_accvgpr_read_b32 v165, a165\n" \
"v_accvgpr_read_b32 v166, a166\n" \
"v_accvgpr_read_b32 v167, a167\n" \
"v_accvgpr_read_b32 v168, a168\n" \
"v_accvgpr_read_b32 v169, a169\n" \
"v_accvgpr_read_b32 v170, a170\n" \
"v_accvgpr_read_b32 v171, a171\n" \
"v_accvgpr_read_b32 v172, a172\n" \
"v_accvgpr_read_b32 v173, a173\n" \
"v_accvgpr_read_b32 v174, a174\n" \
"v_accvgpr_read_b32 v175, a175\n" \
"s_lshl_b32 s75, s63, 2\n" \
"s_sub_u32 s75, 60, s75\n" \
"s_lshr_b32 s80, s61, 1\n" \
"s_add_u32 s75, s75, s80\n" \
"s_add_u32 s75, s75, 0\n" \
"s_add_u32 s76, s78, 0\n" \
"s_call_b64 s[68:69], Lex1_%=\n" \
"v_accvgpr_read_b32 v160, a176\n" \
"v_accvgpr_read_b32 v161, a177\n" \
"v_accvgpr_read_b32 v162, a178\n" \
"v_accvgpr_read_b32 v163, a179\n" \
"v_accvgpr_read_b32 v164, a180\n" \
"v_accvgpr_read_b32 v165, a181\n" \
"v_accvgpr_read_b32 v166, a182\n" \
"v_accvgpr_read_b32 v167, a183\n" \
"v_accvgpr_read_b32 v168, a184\n" \
"v_accvgpr_read_b32 v169, a185\n" \
"v_accvgpr_read_b32 v170, a186\n" \
"v_accvgpr_read_b32 v171, a187\n" \
"v_accvgpr_read_b32 v172, a188\n" \
"v_accvgpr_read_b32 v173, a189\n" \
"v_accvgpr_read_b32 v174, a190\n" \
"v_accvgpr_read_b32 v175, a191\n" \
"s_lshl_b32 s75, s63, 2\n" \
"s_sub_u32 s75, 60, s75\n" \
"s_lshr_b32 s80, s61, 1\n" \
"s_add_u32 s75, s75, s80\n" \
"s_add_u32 s75, s75, 2\n" \
"s_add_u32 s76, s78, 532480\n" \
"s_call_b64 s[68:69], Lex1_%=\n" \
"s_branch Lexr_%=\n" \
"Lex1_%=:\n" \
"v_mov_b32 v128, 0\n" \
"v_mov_b32 v129, 0\n" \
"v_mov_b32 v130, 0\n" \
"v_mov_b32 v131, 0\n" \
"v_mov_b32 v132, 0\n" \
"v_mov_b32 v133, 0\n" \
"v_mov_b32 v134, 0\n" \
"v_mov_b32 v135, 0\n" \
"v_mov_b32 v136, 0\n" \
"v_mov_b32 v137, 0\n" \
"v_mov_b32 v138, 0\n" \
"v_mov_b32 v139, 0\n" \
"v_mov_b32 v140, 0\n" \
"v_mov_b32 v141, 0\n" \
"v_mov_b32 v142, 0\n" \
"v_mov_b32 v143, 0\n" \
"v_mov_b32 v144, 0\n" \
"v_mov_b32 v145, 0\n" \
"v_mov_b32 v146, 0\n" \
"v_mov_b32 v147, 0\n" \
"v_mov_b32 v148, 0\n" \
"v_mov_b32 v149, 0\n" \
"v_mov_b32 v150, 0\n" \
"v_mov_b32 v151, 0\n" \
"v_mov_b32 v152, 0\n" \
"v_mov_b32 v153, 0\n" \
"v_mov_b32 v154, 0\n" \
"v_mov_b32 v155, 0\n" \
"v_mov_b32 v156, 0\n" \
"v_mov_b32 v157, 0\n" \
"v_mov_b32 v158, 0\n" \
"v_mov_b32 v159, 0\n" \
"v_mov_b32 v184, 0xf149f2ca\n" \
"v_mov_b32 v185, 0\n" \
"s_mov_b32 s73, 0\n" \
"s_mul_i32 s74, s62, 557824\n" \
"Lex2_%=:\n" \
"s_add_u32 s80, s74, 4096\n" \
"buffer_load_dwordx4 a[0:3], v249, s[56:59], s74 offen offset:0 sc1\n" \
"buffer_load_dwordx4 a[4:7], v249, s[56:59], s74 offen offset:2048 sc1\n" \
"buffer_load_dwordx4 a[8:11], v249, s[56:59], s80 offen offset:0 sc1\n" \
"buffer_load_dwordx4 a[12:15], v249, s[56:59], s80 offen offset:2048 sc1\n" \
"buffer_load_dwordx4 a[16:19], v249, s[56:59], s74 offen offset:256 sc1\n" \
"buffer_load_dwordx4 a[20:23], v249, s[56:59], s74 offen offset:2304 sc1\n" \
"buffer_load_dwordx4 a[24:27], v249, s[56:59], s80 offen offset:256 sc1\n" \
"buffer_load_dwordx4 a[28:31], v249, s[56:59], s80 offen offset:2304 sc1\n" \
"s_add_u32 s80, s74, 17850368\n" \
"s_add_u32 s81, s80, 4096\n" \
"buffer_load_dwordx4 a[32:35], v248, s[56:59], s80 offen offset:0 sc1\n" \
"buffer_load_dwordx4 a[36:39], v248, s[56:59], s80 offen offset:1024 sc1\n" \
"buffer_load_dwordx4 a[40:43], v248, s[56:59], s80 offen offset:2048 sc1\n" \
"buffer_load_dwordx4 a[44:47], v248, s[56:59], s80 offen offset:3072 sc1\n" \
"buffer_load_dwordx4 a[48:51], v248, s[56:59], s81 offen offset:0 sc1\n" \
"buffer_load_dwordx4 a[52:55], v248, s[56:59], s81 offen offset:1024 sc1\n" \
"buffer_load_dwordx4 a[56:59], v248, s[56:59], s81 offen offset:2048 sc1\n" \
"buffer_load_dwordx4 a[60:63], v248, s[56:59], s81 offen offset:3072 sc1\n" \
"s_waitcnt vmcnt(0)\n" \
"v_mfma_f32_16x16x32_bf16 v[176:179], a[0:3], v[160:163], 0\n" \
"v_mfma_f32_16x16x32_bf16 v[176:179], a[4:7], v[164:167], v[176:179]\n" \
"v_mfma_f32_16x16x32_bf16 v[176:179], a[8:11], v[168:171], v[176:179]\n" \
"v_mfma_f32_16x16x32_bf16 v[176:179], a[12:15], v[172:175], v[176:179]\n" \
"v_mfma_f32_16x16x32_bf16 v[180:183], a[16:19], v[160:163], 0\n" \
"v_mfma_f32_16x16x32_bf16 v[180:183], a[20:23], v[164:167], v[180:183]\n" \
"v_mfma_f32_16x16x32_bf16 v[180:183], a[24:27], v[168:171], v[180:183]\n" \
"v_mfma_f32_16x16x32_bf16 v[180:183], a[28:31], v[172:175], v[180:183]\n" \
"s_nop 7\n" \
"s_nop 1\n" \
"s_cmp_eq_u32 s73, s75\n" \
"s_cbranch_scc0 Lex3_%=\n" \
"v_cmp_gt_i32 vcc, 0, v186\n" \
"s_nop 1\n" \
"v_cndmask_b32 v176, v176, v190, vcc\n" \
"v_cmp_gt_i32 vcc, 1, v186\n" \
"s_nop 1\n" \
"v_cndmask_b32 v177, v177, v190, vcc\n" \
"v_cmp_gt_i32 vcc, 2, v186\n" \
"s_nop 1\n" \
"v_cndmask_b32 v178, v178, v190, vcc\n" \
"v_cmp_gt_i32 vcc, 3, v186\n" \
"s_nop 1\n" \
"v_cndmask_b32 v179, v179, v190, vcc\n" \
"v_cmp_gt_i32 vcc, 16, v186\n" \
"s_nop 1\n" \
"v_cndmask_b32 v180, v180, v190, vcc\n" \
"v_cmp_gt_i32 vcc, 17, v186\n" \
"s_nop 1\n" \
"v_cndmask_b32 v181, v181, v190, vcc\n" \
"v_cmp_gt_i32 vcc, 18, v186\n" \
"s_nop 1\n" \
"v_cndmask_b32 v182, v182, v190, vcc\n" \
"v_cmp_gt_i32 vcc, 19, v186\n" \
"s_nop 1\n" \
"v_cndmask_b32 v183, v183, v190, vcc\n" \
"Lex3_%=:\n" \
"v_max3_f32 v187, v176, v177, v178\n" \
"v_max3_f32 v187, v187, v179, v180\n" \
"v_max3_f32 v187, v187, v181, v182\n" \
"v_max3_f32 v187, v187, v183, v184\n" \
"v_mov_b32 v188, v187\n" \
"s_nop 1\n" \
"v_permlane16_swap_b32 v188, v187\n" \
"v_max_f32 v187, v187, v188\n" \
"v_mov_b32 v188, v187\n" \
"s_nop 1\n" \
"v_permlane32_swap_b32 v188, v187\n" \
"v_max_f32 v187, v187, v188\n" \
"v_sub_f32 v189, v184, v187\n" \
"v_exp_f32 v189, v189\n" \
"v_mov_b32 v184, v187\n" \
"v_sub_f32 v176, v176, v187\n" \
"v_sub_f32 v177, v177, v187\n" \
"v_sub_f32 v178, v178, v187\n" \
"v_sub_f32 v179, v179, v187\n" \
"v_sub_f32 v180, v180, v187\n" \
"v_sub_f32 v181, v181, v187\n" \
"v_sub_f32 v182, v182, v187\n" \
"v_sub_f32 v183, v183, v187\n" \
"v_exp_f32 v176, v176\n" \
"v_exp_f32 v177, v177\n" \
"v_exp_f32 v178, v178\n" \
"v_exp_f32 v179, v179\n" \
"v_exp_f32 v180, v180\n" \
"v_exp_f32 v181, v181\n" \
"v_exp_f32 v182, v182\n" \
"v_exp_f32 v183, v183\n" \
"v_mul_f32 v185, v185, v189\n" \
"v_add_f32 v185, v185, v176\n" \
"v_add_f32 v185, v185, v177\n" \
"v_add_f32 v185, v185, v178\n" \
"v_add_f32 v185, v185, v179\n" \
"v_add_f32 v185, v185, v180\n" \
"v_add_f32 v185, v185, v181\n" \
"v_add_f32 v185, v185, v182\n" \
"v_add_f32 v185, v185, v183\n" \
"v_cvt_pk_bf16_f32 v112, v176, v177\n" \
"v_cvt_pk_bf16_f32 v113, v178, v179\n" \
"v_cvt_pk_bf16_f32 v114, v180, v181\n" \
"v_cvt_pk_bf16_f32 v115, v182, v183\n" \
"v_mul_f32 v128, v128, v189\n" \
"v_mul_f32 v129, v129, v189\n" \
"v_mul_f32 v130, v130, v189\n" \
"v_mul_f32 v131, v131, v189\n" \
"v_mul_f32 v132, v132, v189\n" \
"v_mul_f32 v133, v133, v189\n" \
"v_mul_f32 v134, v134, v189\n" \
"v_mul_f32 v135, v135, v189\n" \
"v_mul_f32 v136, v136, v189\n" \
"v_mul_f32 v137, v137, v189\n" \
"v_mul_f32 v138, v138, v189\n" \
"v_mul_f32 v139, v139, v189\n" \
"v_mul_f32 v140, v140, v189\n" \
"v_mul_f32 v141, v141, v189\n" \
"v_mul_f32 v142, v142, v189\n" \
"v_mul_f32 v143, v143, v189\n" \
"v_mul_f32 v144, v144, v189\n" \
"v_mul_f32 v145, v145, v189\n" \
"v_mul_f32 v146, v146, v189\n" \
"v_mul_f32 v147, v147, v189\n" \
"v_mul_f32 v148, v148, v189\n" \
"v_mul_f32 v149, v149, v189\n" \
"v_mul_f32 v150, v150, v189\n" \
"v_mul_f32 v151, v151, v189\n" \
"v_mul_f32 v152, v152, v189\n" \
"v_mul_f32 v153, v153, v189\n" \
"v_mul_f32 v154, v154, v189\n" \
"v_mul_f32 v155, v155, v189\n" \
"v_mul_f32 v156, v156, v189\n" \
"v_mul_f32 v157, v157, v189\n" \
"v_mul_f32 v158, v158, v189\n" \
"v_mul_f32 v159, v159, v189\n" \
"s_nop 1\n" \
"v_mfma_f32_16x16x32_bf16 v[128:131], a[32:35], v[112:115], v[128:131]\n" \
"v_mfma_f32_16x16x32_bf16 v[132:135], a[36:39], v[112:115], v[132:135]\n" \
"v_mfma_f32_16x16x32_bf16 v[136:139], a[40:43], v[112:115], v[136:139]\n" \
"v_mfma_f32_16x16x32_bf16 v[140:143], a[44:47], v[112:115], v[140:143]\n" \
"v_mfma_f32_16x16x32_bf16 v[144:147], a[48:51], v[112:115], v[144:147]\n" \
"v_mfma_f32_16x16x32_bf16 v[148:151], a[52:55], v[112:115], v[148:151]\n" \
"v_mfma_f32_16x16x32_bf16 v[152:155], a[56:59], v[112:115], v[152:155]\n" \
"v_mfma_f32_16x16x32_bf16 v[156:159], a[60:63], v[112:115], v[156:159]\n" \
"s_nop 7\n" \
"s_nop 1\n" \
"s_and_b32 s80, s73, 1\n" \
"s_mul_i32 s80, s80, 1024\n" \
"s_add_u32 s74, s74, s80\n" \
"s_add_u32 s74, s74, 8192\n" \
"s_add_u32 s73, s73, 1\n" \
"s_cmp_le_u32 s73, s75\n" \
"s_cbranch_scc1 Lex2_%=\n" \
"v_mov_b32 v187, v185\n" \
"s_nop 1\n" \
"v_permlane16_swap_b32 v187, v185\n" \
"v_add_f32 v185, v185, v187\n" \
"v_mov_b32 v187, v185\n" \
"s_nop 1\n" \
"v_permlane32_swap_b32 v187, v185\n" \
"v_add_f32 v185, v185, v187\n" \
"v_rcp_f32 v185, v185\n" \
"s_nop 0\n" \
"v_mul_f32 v128, v128, v185\n" \
"v_mul_f32 v129, v129, v185\n" \
"v_mul_f32 v130, v130, v185\n" \
"v_mul_f32 v131, v131, v185\n" \
"v_mul_f32 v132, v132, v185\n" \
"v_mul_f32 v133, v133, v185\n" \
"v_mul_f32 v134, v134, v185\n" \
"v_mul_f32 v135, v135, v185\n" \
"v_mul_f32 v136, v136, v185\n" \
"v_mul_f32 v137, v137, v185\n" \
"v_mul_f32 v138, v138, v185\n" \
"v_mul_f32 v139, v139, v185\n" \
"v_mul_f32 v140, v140, v185\n" \
"v_mul_f32 v141, v141, v185\n" \
"v_mul_f32 v142, v142, v185\n" \
"v_mul_f32 v143, v143, v185\n" \
"v_mul_f32 v144, v144, v185\n" \
"v_mul_f32 v145, v145, v185\n" \
"v_mul_f32 v146, v146, v185\n" \
"v_mul_f32 v147, v147, v185\n" \
"v_mul_f32 v148, v148, v185\n" \
"v_mul_f32 v149, v149, v185\n" \
"v_mul_f32 v150, v150, v185\n" \
"v_mul_f32 v151, v151, v185\n" \
"v_mul_f32 v152, v152, v185\n" \
"v_mul_f32 v153, v153, v185\n" \
"v_mul_f32 v154, v154, v185\n" \
"v_mul_f32 v155, v155, v185\n" \
"v_mul_f32 v156, v156, v185\n" \
"v_mul_f32 v157, v157, v185\n" \
"v_mul_f32 v158, v158, v185\n" \
"v_mul_f32 v159, v159, v185\n" \
"s_add_u32 s80, s76, 4096\n" \
"v_cvt_pk_bf16_f32 v112, v128, v129\n" \
"v_cvt_pk_bf16_f32 v113, v130, v131\n" \
"v_cvt_pk_bf16_f32 v114, v132, v133\n" \
"v_cvt_pk_bf16_f32 v115, v134, v135\n" \
"buffer_store_dwordx4 v[112:115], v191, s[56:59], s76 offen offset:0 sc1\n" \
"v_cvt_pk_bf16_f32 v116, v136, v137\n" \
"v_cvt_pk_bf16_f32 v117, v138, v139\n" \
"v_cvt_pk_bf16_f32 v118, v140, v141\n" \
"v_cvt_pk_bf16_f32 v119, v142, v143\n" \
"buffer_store_dwordx4 v[116:119], v191, s[56:59], s76 offen offset:2048 sc1\n" \
"v_cvt_pk_bf16_f32 v120, v144, v145\n" \
"v_cvt_pk_bf16_f32 v121, v146, v147\n" \
"v_cvt_pk_bf16_f32 v122, v148, v149\n" \
"v_cvt_pk_bf16_f32 v123, v150, v151\n" \
"buffer_store_dwordx4 v[120:123], v191, s[56:59], s80 offen offset:0 sc1\n" \
"v_cvt_pk_bf16_f32 v124, v152, v153\n" \
"v_cvt_pk_bf16_f32 v125, v154, v155\n" \
"v_cvt_pk_bf16_f32 v126, v156, v157\n" \
"v_cvt_pk_bf16_f32 v127, v158, v159\n" \
"buffer_store_dwordx4 v[124:127], v191, s[56:59], s80 offen offset:2048 sc1\n" \
"s_waitcnt vmcnt(0)\n" \
"s_setpc_b64 s[68:69]\n" \
"Lpl2_%=:\n" \
"s_mov_b32 s31, 0\n" \
"Lpk2_%=:\n" \
"s_sleep 2\n" \
"buffer_load_dword v220, v221, s[56:59], s81 offen sc1\n" \
"s_waitcnt vmcnt(0)\n" \
"v_cmp_ne_u32 vcc, s60, v220\n" \
"s_nop 1\n" \
"s_and_b32 vcc_hi, vcc_lo, s80\n" \
"s_cbranch_scc0 Lpr2_%=\n" \
"s_add_u32 s31, s31, 1\n" \
"s_cmp_lt_u32 s31, 0x40000\n" \
"s_cbranch_scc1 Lpk2_%=\n" \
"s_branch Lpr2_%=\n" \
"Lpl3_%=:\n" \
"s_mov_b32 s31, 0\n" \
"Lpk3_%=:\n" \
"s_sleep 2\n" \
"buffer_load_dwordx2 v[226:227], v228, s[56:59], s30 offen sc1\n" \
"s_waitcnt vmcnt(0)\n" \
"v_xor_b32 v226, v226, v227\n" \
"v_cmp_ne_u32 vcc, s60, v226\n" \
"s_nop 1\n" \
"s_and_b32 vcc_hi, vcc_lo, -1\n" \
"s_cbranch_scc0 Lpr3_%=\n" \
"s_add_u32 s31, s31, 1\n" \
"s_cmp_lt_u32 s31, 0x40000\n" \
"s_cbranch_scc1 Lpk3_%=\n" \
"s_branch Lpr3_%=\n" \
"Lplh_%=:\n" \
"s_mov_b32 s31, 0\n" \
"Lpkh_%=:\n" \
"s_sleep 2\n" \
"buffer_load_dwordx4 v[128:131], v207, s[56:59], s47 offen offset:0 sc1\n" \
"buffer_load_dwordx4 v[192:195], v208, s[56:59], s47 offen offset:1024 sc1\n" \
"buffer_load_dwordx4 v[68:71], v223, s[56:59], s47 offen offset:2048 sc1\n" \
"s_waitcnt vmcnt(0)\n" \
"v_bitop3_b32 v131, v131, v128, v129 bitop3:0x96\n" \
"v_bitop3_b32 v192, v192, v193, v194 bitop3:0x96\n" \
"v_bitop3_b32 v68, v68, v69, v70 bitop3:0x96\n" \
"v_xor_b32 v131, v131, v130\n" \
"v_xor_b32 v192, v192, v195\n" \
"v_xor_b32 v68, v68, v71\n" \
"v_cmp_ne_u32 s[42:43], s60, v131\n" \
"v_cmp_ne_u32 s[44:45], s60, v192\n" \
"v_cmp_ne_u32 vcc, s60, v68\n" \
"s_nop 1\n" \
"s_and_b64 s[42:43], s[42:43], s[12:13]\n" \
"s_and_b64 s[44:45], s[44:45], s[92:93]\n" \
"s_and_b64 vcc, vcc, s[14:15]\n" \
"s_or_b64 s[42:43], s[42:43], s[44:45]\n" \
"s_or_b64 vcc, vcc, s[42:43]\n" \
"s_cbranch_scc0 Lprh_%=\n" \
"s_add_u32 s31, s31, 1\n" \
"s_cmp_lt_u32 s31, 0x40000\n" \
"s_cbranch_scc1 Lpkh_%=\n" \
"s_branch Lprh_%=\n" \
"Lpl4_%=:\n" \
"s_mov_b32 s31, 0\n" \
"Lpk4_%=:\n" \
"s_sleep 2\n" \
"buffer_load_dword v220, off, s[56:59], s79 sc1\n" \
"s_waitcnt vmcnt(0)\n" \
"v_cmp_ne_u32 vcc, s60, v220\n" \
"s_nop 1\n" \
"s_and_b32 vcc_hi, vcc_lo, 1\n" \
"s_cbranch_scc0 Lpr4_%=\n" \
"s_add_u32 s31, s31, 1\n" \
"s_cmp_lt_u32 s31, 0x40000\n" \
"s_cbranch_scc1 Lpk4_%=\n" \
"s_branch Lpr4_%=\n" \
"Lpl5_%=:\n" \
"s_mov_b32 s31, 0\n" \
"Lpk5_%=:\n" \
"s_sleep 2\n" \
"buffer_load_dword v220, v221, s[56:59], s82 offen sc1\n" \
"s_waitcnt vmcnt(0)\n" \
"v_cmp_ne_u32 vcc, s60, v220\n" \
"s_nop 1\n" \
"s_and_b32 vcc_hi, vcc_lo, 65535\n" \
"s_cbranch_scc0 Lpr5_%=\n" \
"s_add_u32 s31, s31, 1\n" \
"s_cmp_lt_u32 s31, 0x40000\n" \
"s_cbranch_scc1 Lpk5_%=\n" \
"s_branch Lpr5_%=\n" \
"Lcode_end_%=:\n" \
".p2align 12\n" \
""
#define MK_ASM_CLOBBERS "v1", "v2", "v3", "v4", "v5", "v6", "v7", "v8", "v9", "v10", "v11", "v12", "v13", "v14", "v15", "v16", "v17", "v18", "v19", "v20", "v21", "v22", "v23", "v24", "v25", "v26", "v27", "v28", "v29", "v30", "v31", "v32", "v33", "v34", "v35", "v36", "v37", "v38", "v39", "v40", "v41", "v42", "v43", "v44", "v45", "v46", "v47", "v48", "v49", "v50", "v51", "v52", "v53", "v54", "v55", "v56", "v57", "v58", "v59", "v60", "v61", "v62", "v63", "v64", "v65", "v66", "v67", "v68", "v69", "v70", "v71", "v72", "v73", "v74", "v75", "v76", "v77", "v78", "v79", "v80", "v81", "v82", "v83", "v84", "v85", "v86", "v87", "v88", "v89", "v90", "v91", "v92", "v93", "v94", "v95", "v96", "v97", "v98", "v99", "v100", "v101", "v102", "v103", "v104", "v105", "v106", "v107", "v108", "v109", "v110", "v111", "v112", "v113", "v114", "v115", "v116", "v117", "v118", "v119", "v120", "v121", "v122", "v123", "v124", "v125", "v126", "v127", "v128", "v129", "v130", "v131", "v132", "v133", "v134", "v135", "v136", "v137", "v138", "v139", "v140", "v141", "v142", "v143", "v144", "v145", "v146", "v147", "v148", "v149", "v150", "v151", "v152", "v153", "v154", "v155", "v156", "v157", "v158", "v159", "v160", "v161", "v162", "v163", "v164", "v165", "v166", "v167", "v168", "v169", "v170", "v171", "v172", "v173", "v174", "v175", "v176", "v177", "v178", "v179", "v180", "v181", "v182", "v183", "v184", "v185", "v186", "v187", "v188", "v189", "v190", "v191", "v192", "v193", "v194", "v195", "v196", "v197", "v198", "v199", "v200", "v201", "v202", "v203", "v204", "v205", "v206", "v207", "v208", "v209", "v210", "v211", "v212", "v213", "v214", "v215", "v216", "v217", "v218", "v219", "v220", "v221", "v222", "v223", "v224", "v225", "v226", "v227", "v228", "v229", "v230", "v231", "v232", "v233", "v234", "v235", "v236", "v237", "v238", "v239", "v240", "v241", "v242", "v243", "v244", "v245", "v246", "v247", "v248", "v249", "v250", "v251", "v252", "v253", "v254", "v255", "a0", "a1", "a2", "a3", "a4", "a5", "a6", "a7", "a8", "a9", "a10", "a11", "a12", "a13", "a14", "a15", "a16", "a17", "a18", "a19", "a20", "a21", "a22", "a23", "a24", "a25", "a26", "a27", "a28", "a29", "a30", "a31", "a32", "a33", "a34", "a35", "a36", "a37", "a38", "a39", "a40", "a41", "a42", "a43", "a44", "a45", "a46", "a47", "a48", "a49", "a50", "a51", "a52", "a53", "a54", "a55", "a56", "a57", "a58", "a59", "a60", "a61", "a62", "a63", "a64", "a65", "a66", "a67", "a68", "a69", "a70", "a71", "a72", "a73", "a74", "a75", "a76", "a77", "a78", "a79", "a80", "a81", "a82", "a83", "a84", "a85", "a86", "a87", "a88", "a89", "a90", "a91", "a92", "a93", "a94", "a95", "a96", "a97", "a98", "a99", "a100", "a101", "a102", "a103", "a104", "a105", "a106", "a107", "a108", "a109", "a110", "a111", "a112", "a113", "a114", "a115", "a116", "a117", "a118", "a119", "a120", "a121", "a122", "a123", "a124", "a125", "a126", "a127", "a128", "a129", "a130", "a131", "a132", "a133", "a134", "a135", "a136", "a137", "a138", "a139", "a140", "a141", "a142", "a143", "a144", "a145", "a146", "a147", "a148", "a149", "a150", "a151", "a152", "a153", "a154", "a155", "a156", "a157", "a158", "a159", "a160", "a161", "a162", "a163", "a164", "a165", "a166", "a167", "a168", "a169", "a170", "a171", "a172", "a173", "a174", "a175", "a176", "a177", "a178", "a179", "a180", "a181", "a182", "a183", "a184", "a185", "a186", "a187", "a188", "a189", "a190", "a191", "a192", "a193", "a194", "a195", "a196", "a197", "a198", "a199", "a200", "a201", "a202", "a203", "a204", "a205", "a206", "a207", "a208", "a209", "a210", "a211", "a212", "a213", "a214", "a215", "a216", "a217", "a218", "a219", "a220", "a221", "a222", "a223", "a224", "a225", "a226", "a227", "a228", "a229", "a230", "a231", "a232", "a233", "a234", "a235", "a236", "a237", "a238", "a239", "a240", "a241", "a242", "a243", "a244", "a245", "a246", "a247", "a248", "a249", "a250", "a251", "a252", "a253", "a254", "a255", "s8", "s9", "s10", "s11", "s12", "s13", "s14", "s15", "s16", "s17", "s18", "s19", "s20", "s21", "s22", "s23", "s24", "s25", "s26", "s27", "s28", "s29", "s30", "s31", "s36", "s37", "s38", "s39", "s40", "s41", "s42", "s43", "s44", "s45", "s46", "s47", "s48", "s49", "s50", "s51", "s52", "s53", "s54", "s55", "s56", "s57", "s58", "s59", "s60", "s61", "s62", "s63", "s64", "s65", "s66", "s67", "s68", "s69", "s70", "s71", "s72", "s73", "s74", "s75", "s76", "s77", "s78", "s79", "s80", "s81", "s82", "s83", "s84", "s85", "s86", "s87", "s88", "s89", "s90", "s91", "s92", "s93", "s94", "s95", "vcc", "scc", "memory"
namespace mk {
constexpr int LDS_BYTES = 163840;
struct Args { const void* in[9]; void* out; void* ws; };
__global__ __launch_bounds__(256, 1) void mega_k(const Args a) {
    extern __shared__ __attribute__((aligned(16))) unsigned char shm[];
    const unsigned ldsb = (unsigned)(size_t)(LAS unsigned char*)shm;
    asm volatile(MK_ASM_BODY
                 :
                 : "s"(__builtin_amdgcn_kernarg_segment_ptr()), "s"(blockIdx.x), "v"(threadIdx.x), "s"(ldsb)
                 : MK_ASM_CLOBBERS);
}
}
extern "C" void kernel_launch(void* const* d_in, const int* in_sizes, int n_in, void* d_out, int out_size, void* d_ws, size_t ws_size, hipStream_t stream) {
    (void)hipFuncSetAttribute((const void*)mk::mega_k, hipFuncAttributeMaxDynamicSharedMemorySize, mk::LDS_BYTES);
    mk::Args a;
    for (int i = 0; i < 9; ++i) a.in[i] = d_in[i];
    a.out = d_out; a.ws = (char*)d_ws + ((ws_size - WS_END) & ~(size_t)0x1fffff);
    mk::mega_k<<<256, 256, mk::LDS_BYTES, stream>>>(a);
}
```
